```python
import torch

def _softmax(x, axis=-1):
    return torch.softmax(x, dim=axis)
def _gelu(x):
    return torch.nn.functional.gelu(x, approximate="tanh")
def _silu(x):
    return torch.nn.functional.silu(x)
def _sigmoid(x):
    return torch.sigmoid(x)
def _layernorm(x, axis=-1, eps=1e-5):
    m = x.mean(dim=axis, keepdim=True)
    v = x.var(dim=axis, keepdim=True, unbiased=False)
    return (x - m) / torch.sqrt(v + eps)
def _scatter_add(tgt, idx, src, axis=0):
    return tgt.clone().index_add_(axis, idx, src)
def _segment_sum(src, seg, n):
    out = torch.zeros((n,) + src.shape[1:], dtype=src.dtype)
    return out.index_add_(0, seg, src)
def _pad(x, pw):
    flat = tuple(v for pair in reversed(pw) for v in pair)
    return torch.nn.functional.pad(x, flat)

kernel_name = 'interleave_layers__sliding_block__sliding_local_dilated__prompt'

def setup_inputs():

    _w = (lambda a: (lambda t: t.to(torch.bfloat16) if t.is_floating_point() else t)(torch.as_tensor(a)))
    def _A_setup():
        B, S, D = 1, 2048, 1024
        return {
            "x": _w((lambda *s: torch.randn(*s, dtype=torch.bfloat16) * 0.05)(B, S, D)),
            "wqkv": _w((lambda *s: torch.randn(*s, dtype=torch.bfloat16) * 0.05)(D, 3 * D)),
            "wo": _w((lambda *s: torch.randn(*s, dtype=torch.bfloat16) * 0.05)(D, D)),
        }
    _A_in = _A_setup()

    def _B_setup():
        B, S, D = 1, 2048, 1024
        return {
            "x": _w((lambda *s: torch.randn(*s, dtype=torch.bfloat16) * 0.05)(B, S, D)),
            "wqkv": _w((lambda *s: torch.randn(*s, dtype=torch.bfloat16) * 0.05)(D, 3 * D)),
            "wo": _w((lambda *s: torch.randn(*s, dtype=torch.bfloat16) * 0.05)(D, D)),
        }
    _B_in = _B_setup()

    return {
        "A_x": _A_in["x"],
        "A_wqkv": _A_in["wqkv"],
        "A_wo": _A_in["wo"],
        "B_x": _B_in["x"],
        "B_wqkv": _B_in["wqkv"],
        "B_wo": _B_in["wo"],
    }

def reference(A_x, A_wqkv, A_wo, B_x, B_wqkv, B_wo):
    def _A_ref(x, wqkv, wo):
        B, S, D = x.shape
        H = 32; Dh = D // H; W = 256
        qkv = (lambda x, *d: x.permute(*d))((x @ wqkv).reshape(B, S, 3, H, Dh), 2, 0, 3, 1, 4)
        q, k, v = qkv[0], qkv[1], qkv[2]
        scores = torch.einsum("bhqd,bhkd->bhqk", q, k) * (Dh ** -0.5)
        idx = torch.arange(S)
        mask = (idx[:, None] - idx[None, :] >= 0) & (idx[:, None] - idx[None, :] < W)
        scores = torch.where(mask[None, None], scores, -1e9)
        p = _softmax(scores, axis=-1)
        o = torch.einsum("bhqk,bhkd->bhqd", p, v)
        return (lambda x, *d: x.permute(*d))(o, 0, 2, 1, 3).reshape(B, S, D) @ wo

    def _B_ref(x, wqkv, wo):
        B, S, D = x.shape
        H = 32; Dh = D // H
        W, dil = 8, 2
        qkv = (lambda x, *d: x.permute(*d))((x @ wqkv).reshape(B, S, 3, H, Dh), 2, 0, 3, 1, 4)
        q, k, v = qkv[0], qkv[1], qkv[2]
        scale = Dh ** -0.5
        pos = torch.arange(S)
        off = torch.arange(W) * dil
        idx = pos[:, None] - off[None, :]
        mask = idx >= 0
        idx_c = torch.where(mask, idx, 0).reshape(-1)
        kg = k[:, :, idx_c, :].reshape(B, H, S, W, Dh)
        vg = v[:, :, idx_c, :].reshape(B, H, S, W, Dh)
        logits = torch.einsum("bhsd,bhswd->bhsw", q, kg) * scale
        logits = torch.where(mask[None, None, :, :], logits, -1e9)
        attn = _softmax(logits, axis=-1)
        o = torch.einsum("bhsw,bhswd->bhsd", attn, vg)
        return (lambda x, *d: x.permute(*d))(o, 0, 2, 1, 3).reshape(B, S, D) @ wo

    x = A_x
    x = x + _A_ref(_layernorm(x, axis=-1), A_wqkv, A_wo)
    x = x + _B_ref(_layernorm(x, axis=-1), B_wqkv, B_wo)
    x = x + _A_ref(_layernorm(x, axis=-1), A_wqkv, A_wo)
    x = x + _B_ref(_layernorm(x, axis=-1), B_wqkv, B_wo)
    return x
```

```cpp
#include <hip/hip_runtime.h>
#include <stdint.h>

namespace {

typedef unsigned short u16;
typedef unsigned long long u64;
typedef short s16x4 __attribute__((ext_vector_type(4)));
typedef float f32x4 __attribute__((ext_vector_type(4)));
typedef unsigned u32x4 __attribute__((ext_vector_type(4)));
typedef unsigned u32x2 __attribute__((ext_vector_type(2)));
typedef unsigned u32x8 __attribute__((ext_vector_type(8)));
typedef unsigned u32x16 __attribute__((ext_vector_type(16)));
typedef __amdgpu_buffer_rsrc_t rsrc_t;
#define LAS __attribute__((address_space(3)))
#define DI __device__ __forceinline__
#define LDSP(a) ((LAS char*)(size_t)(a))

constexpr unsigned MB = 1u << 20;
constexpr unsigned WS_XF = 0;
constexpr unsigned WS_OF = 16 * MB;
constexpr unsigned WS_KV = 32 * MB;
constexpr unsigned WS_WF = 64 * MB;
constexpr unsigned WS_WO = 76 * MB;
constexpr unsigned WS_ST = 80 * MB;
constexpr unsigned WS_CS = 81 * MB;
constexpr unsigned WS_FL = 82 * MB;
constexpr unsigned WS_EP = WS_FL + 2048 * 128;
constexpr unsigned WS_DBG = 96 * MB;
constexpr unsigned OOB = 0x80000000u;
constexpr int L_RES = 0, L_K = 16384, L_V = 49152, L_INB = 81920, L_CS = 155648, L_PM = L_CS + 1024, L_MB = L_PM + 2048, L_SCR = L_MB + 2048;
constexpr int L_WS = 57344;
constexpr int LDS_BYTES = 163840;

template <int N> DI void wait_vm() { asm volatile("s_waitcnt vmcnt(%0)" ::"n"(N) : "memory"); }
DI void lds_barrier() { asm volatile("s_waitcnt lgkmcnt(0)\n\ts_barrier" ::: "memory"); }
template <int N> DI void nops() { asm volatile("s_nop %0" ::"n"(N - 1)); }
template <int OFF> DI void ldv(u32x4& d, rsrc_t r, unsigned v, unsigned s) {
  asm volatile("buffer_load_dwordx4 %0, %1, %2, %3 offen offset:%4 sc1" : "=v"(d) : "v"(v), "s"(r), "s"(s), "i"(OFF) : "memory");
}
template <int OFF> DI void ld8(u32x2& d, rsrc_t r, unsigned v, unsigned s) {
  asm volatile("buffer_load_dwordx2 %0, %1, %2, %3 offen offset:%4 sc1" : "=v"(d) : "v"(v), "s"(r), "s"(s), "i"(OFF) : "memory");
}
template <int OFF> DI void st16(const u32x4& d, rsrc_t r, unsigned v, unsigned s) {
  asm volatile("buffer_store_dwordx4 %0, %1, %2, %3 offen offset:%4 sc1\n\ts_nop 1" :: "v"(d), "v"(v), "s"(r), "s"(s), "i"(OFF) : "memory");
}
template <int OFF> DI void st8(const u32x2& d, rsrc_t r, unsigned v, unsigned s) {
  asm volatile("buffer_store_dwordx2 %0, %1, %2, %3 offen offset:%4 sc1" :: "v"(d), "v"(v), "s"(r), "s"(s), "i"(OFF) : "memory");
}
DI void dma(rsrc_t r, unsigned v, unsigned s, unsigned lm) {
  asm volatile("s_mov_b32 m0, %3\n\ts_nop 0\n\tbuffer_load_dwordx4 %0, %1, %2 offen lds" :: "v"(v), "s"(r), "s"(s), "s"(lm) : "memory");
}
DI void touch(rsrc_t r, unsigned v) {
  asm volatile("s_mov_b32 m0, %2\n\ts_nop 0\n\tbuffer_load_dword %0, %1, 0 offen lds" :: "v"(v), "s"(r), "s"((unsigned)L_SCR) : "memory");
}
DI void mma_v(f32x4& c, const u32x4& a, const u32x4& b) { asm volatile("v_mfma_f32_16x16x32_bf16 %0, %1, %2, %0" : "+v"(c) : "v"(a), "v"(b)); }
DI void mm0_v(f32x4& c, const u32x4& a, const u32x4& b) { asm volatile("v_mfma_f32_16x16x32_bf16 %0, %1, %2, 0" : "=v"(c) : "v"(a), "v"(b)); }
DI unsigned uni(unsigned v) { v = __builtin_amdgcn_readfirstlane(v); asm volatile("" : "+s"(v)); return v; }
template <class T> DI void pin(T& v) { asm volatile("" : "+v"(v)); }
DI float sum_x16(float v) {
  auto r = __builtin_amdgcn_permlane16_swap(__float_as_uint(v), __float_as_uint(v), false, false);
  return __uint_as_float(r[0]) + __uint_as_float(r[1]);
}
DI float sum_x32(float v) {
  auto r = __builtin_amdgcn_permlane32_swap(__float_as_uint(v), __float_as_uint(v), false, false);
  return __uint_as_float(r[0]) + __uint_as_float(r[1]);
}
DI u32x4 swap16(const u32x4& x) {
  auto r0 = __builtin_amdgcn_permlane16_swap(x[0], x[2], false, false);
  auto r1 = __builtin_amdgcn_permlane16_swap(x[1], x[3], false, false);
  return (u32x4){(unsigned)r0[0], (unsigned)r1[0], (unsigned)r0[1], (unsigned)r1[1]};
}
DI u32x2 lds_tr(unsigned a) { return __builtin_bit_cast(u32x2, __builtin_amdgcn_ds_read_tr16_b64_v4i16((LAS s16x4*)LDSP(a))); }
DI u32x4 cat(const u32x2& a, const u32x2& b) { return __builtin_shufflevector(a, b, 0, 1, 2, 3); }
DI u32x2 lds_r2(unsigned a) { return *(LAS u32x2*)LDSP(a); }
DI void lds_w4(unsigned a, const u32x4& v) { *(LAS u32x4*)LDSP(a) = v; }
DI unsigned rot(unsigned g, unsigned sl) { return (g + (0x641u >> 4 * sl)) & 15; }
DI int swzK(int u) { return ((((u >> 1) ^ u) & 1) << 1) | (u >> 1); }

#define STAMP(i)

__global__ __launch_bounds__(256, 1) void mega_kernel(const u16* __restrict__ x0, const u16* __restrict__ WqA,
                                                      const u16* __restrict__ WoA, const u16* __restrict__ WqB,
                                                      const u16* __restrict__ WoB, u16* __restrict__ out, char* ws) {
  const unsigned bid = blockIdx.x, xcd = bid & 7, bix = bid >> 3;
  const unsigned w = __builtin_amdgcn_readfirstlane(threadIdx.x >> 6), lane = threadIdx.x & 63;
  const unsigned c = (xcd >> 2) * 4 + (bix >> 3), h = (xcd & 3) * 8 + (bix & 7), par = h & 1, g = h >> 1;
  const unsigned p2 = h >> 3 & 1, g2 = (h >> 4) * 8 + (h & 7);
  const unsigned fr = lane & 15, fq = lane >> 4;
  const unsigned dl = 0, wr = (w + dl) & 3;
  const rsrc_t wsr = __builtin_amdgcn_make_buffer_rsrc(ws, 0, 128 * MB, 0x00020000);
  unsigned stw = 0;
  STAMP(0);
  const unsigned l16 = lane * 16, lw = w * 2048 + l16;
  const unsigned fl0 = lane == 0 ? 0u : OOB;
  const unsigned myfl = WS_FL + (c * 64 + h) * 128 + w * 8;
  const unsigned xo = (fr * 1024 + (((fq & 1) << 4) | ((fq >> 1) << 3))) * 2;
  u64 pc_; unsigned len_;
  asm volatile("s_getpc_b64 %0\n.Lmk_here:\n\ts_mov_b32 %1, .Lmk_end-.Lmk_here" : "=s"(pc_), "=s"(len_));
  const rsrc_t cr = __builtin_amdgcn_make_buffer_rsrc((void*)(pc_ & ~15ull), 0, len_ & ~15u, 0x00020000);
  const rsrc_t xr = __builtin_amdgcn_make_buffer_rsrc((void*)x0, 0, 4 * MB, 0x00020000);
  const unsigned osrc0 = (lane >> 3) * 2048 + (lane & 7) * 16;
  auto get_x = [&](int s) __attribute__((always_inline)) {
#pragma unroll
    for (int i = 0; i < 2; ++i) dma(xr, osrc0, ((c * 16 + s * 8 + 2 * wr + p2) * 16 + i * 8) * 2048 + g2 * 128, L_RES + s * 8192 + w * 2048 + i * 1024);
  };
  u32x2 tag;
  const rsrc_t wa = __builtin_amdgcn_make_buffer_rsrc((void*)WqA, 0, 6 * MB, 0x00020000);
  const unsigned rho = 2 * (c & 3), wbase = w * 256 * 6144 + h * 64;
  const unsigned wv = (lane >> 2) * 6144 + ((lane & 3) ^ ((lane >> 3) & 2)) * 16;
  auto piece = [&](int q, int j) __attribute__((always_inline)) {
    const unsigned k = j ^ par, src = uni(wbase + (((rho + 2 * q) & 7) + k) * 196608), dst = uni(L_WS + ((q & 1) * 8 + k * 4 + w) * 6144);
#pragma unroll
    for (int i = 0; i < 6; ++i) dma(wa, wv, src + (i & 1) * 98304 + (i >> 1) * 2048, dst + i * 1024);
  };
  const unsigned kb = 2 * c + par;
  const rsrc_t oa = __builtin_amdgcn_make_buffer_rsrc((void*)WoA, 0, 2 * MB, 0x00020000);
  const rsrc_t wb = __builtin_amdgcn_make_buffer_rsrc((void*)WqB, 0, 6 * MB, 0x00020000);
  const rsrc_t ob = __builtin_amdgcn_make_buffer_rsrc((void*)WoB, 0, 2 * MB, 0x00020000);
  const unsigned wsrc = (w * 16 + (lane >> 3)) * 6144 + (lane & 7) * 16, osrc = w * 16 * 2048 + osrc0;
  auto get_o = [&](int t) __attribute__((always_inline)) {
#pragma unroll
    for (int i = 0; i < 2; ++i) dma(t ? ob : oa, osrc, (kb * 64 + i * 8) * 2048 + rot(g, 1 + t) * 128, L_K + 24576 + t * 8192 + w * 2048 + i * 1024);
  };
  auto get_q = [&]() __attribute__((always_inline)) {
#pragma unroll
    for (int i = 0; i < 6; ++i) dma(wb, wsrc, (kb * 64 + (i & 1) * 8) * 6144 + (i >> 1) * 2048 + rot(g, i >> 1) * 128, L_K + (i >> 1) * 8192 + w * 2048 + (i & 1) * 1024);
  };
  const unsigned fr8 = fq == fr >> 2 ? fr * 8 : OOB;
  u32x4 ones = {0x3F803F80u, 0x3F803F80u, 0x3F803F80u, 0x3F803F80u};
  u32x2 sta = {};
  auto publish = [&](int s, bool now) __attribute__((always_inline)) {
    const unsigned a = L_RES + s * 8192 + w * 2048 + fr * 128 + fq * 8, T = c * 16 + 8 * s + 2 * wr + p2;
    u32x4 y0 = cat(lds_r2(a), lds_r2(a + 32)), y1 = cat(lds_r2(a + 64), lds_r2(a + 96));
    f32x4 sm, gm;
    pin(y0); pin(y1); pin(ones);
    nops<2>();
    mm0_v(sm, ones, y0); mm0_v(gm, y0, y0); mma_v(sm, ones, y1); mma_v(gm, y1, y1);
    if (now) { st16<0>(y0, wsr, l16, WS_XF + T * 32768 + g2 * 2048); st16<1024>(y1, wsr, l16, WS_XF + T * 32768 + g2 * 2048); }
    lds_w4(lw + L_RES + s * 8192, y0); lds_w4(lw + L_RES + s * 8192 + 1024, y1);
    nops<8>();
    pin(sm); pin(gm);
    const u32x2 st = {__float_as_uint(sm[0]), __float_as_uint(fr & 2 ? (fr & 1 ? gm[3] : gm[2]) : (fr & 1 ? gm[1] : gm[0]))};
    if (now) st8<0>(st, wsr, fr8, WS_ST + T * 2048 + g2 * 128); else sta = st;
  };
  const unsigned cb = c * 16 * 32768, skv = WS_KV + (c * 32 + h) * 32768;
  const unsigned soo = WS_OF + cb + wr * 65536 + h * 1024, sxo = WS_XF + cb + (2 * wr + p2) * 32768 + g2 * 2048, sso = WS_ST + (c * 16 + 2 * wr + p2) * 2048 + g2 * 128;
  auto clear = [&](int from, int to) __attribute__((always_inline)) {
    u32x4 z = {~0u, ~0u, ~0u, ~0u};
    pin(z);
#pragma unroll
    for (int k = from; k < to; ++k) {
      if (k < 8) st16<0>(z, wsr, l16, soo + (k == 4 ? 9 : k == 5 ? 8 : k - 6) * 32768);
      else if (k < 10) st16<0>(z, wsr, l16, sxo + 4 * MB + 8 * 32768 + (k - 8) * 1024);
      else st8<0>((u32x2){z[0], z[1]}, wsr, fr8, sso + 136 * 2048);
    }
  };
  dma(cr, l16, (bix * 4 + w) * 1024, L_SCR + 1024);
  get_x(1);
  get_x(0);
  ld8<0>(tag, wsr, 0u, WS_EP);
  piece(0, 0);
  touch(wsr, lane < 2 ? WS_FL + (w * 512 + c * 64 + lane * 32 + h) * 128 : OOB);
  STAMP(29);

  unsigned ps[16];
  u32x8 pv = {};
#pragma unroll
  for (int j = 0; j < 3; ++j) {
    const unsigned ct = (w & 1) * 3 + j;
    pv[1 + j] = L_K + (ct >> 1) * 8192 + (4 * fq + (fr >> 2)) * 128 + (w >> 1) * 64 + (ct & 1) * 32 + (lane & 3) * 8;
    ps[j] = WS_WF + 6 * MB + ((2 * rot(g, ct >> 1) + (w >> 1)) * 192 + kb * 12 + ct) * 1024;
    ps[10 + j] = kb * 64 * 6144 + j * 2048 + rot(g, j) * 128;
  }
  { const unsigned ct = (w & 1) * 3 + (fr >> 1);
    pv[4] = ((2 * rot(g, ct >> 1) + (w >> 1)) * 16384 + ct * 128 + (fr & 1) * 64 + fq * 16) | (fr < 6 ? 0u : OOB); }
  ps[3] = WS_CS + 524288 + kb * 1024;
  ps[6] = (c >> 2) | par << 1 | (bix * 4 + w) << 10;                                            ps[7] = L_WS + w * 6144;
  ps[8] = wbase;                                             ps[9] = rho;
  ps[13] = kb * 64 * 2048 + rot(g, 1) * 128;                 ps[14] = kb * 64 * 2048 + rot(g, 2) * 128;
  ps[15] = WS_WF + (h * 4 + w) * 49152 + (rho + (c >> 2)) * 6144;
  pv[5] = wsrc;                                              pv[6] = osrc;
  u32x16 pw = {wv, L_WS + w * 6144 + (4 * fq + (fr >> 2)) * 64 + (fq & 1) * 32 + (lane & 3) * 8,
                    l16 + rho * 1024, l16 + ((rho + 2) & 7) * 1024, l16 + ((rho + 4) & 7) * 1024, l16 + ((rho + 6) & 7) * 1024,
                    L_WS + w * 6144 + (fr == 0 ? fq * 16 : 1024 + l16), L_WS + (w * 24 + (lane < 24 ? lane : 0)) * 4,
                    (w * 128 + (lane >> 3 & 1) * 64 + 2 * ((g2 - rot(0, 1 + (fq & 1))) & 15) + (lane >> 2 & 1)) * 128 + (lane & 3) * 8};
  if (w < 2) {
    u32x4 pm;
#pragma unroll
    for (int k = 0; k < 4; ++k)
      pm[k] = (8 * fq + 2 * k > 16 * w + fr ? 0xFFFFu : 0u) | (8 * fq + 2 * k + 1 > 16 * w + fr ? 0xFFFF0000u : 0u);
    lds_w4(L_PM + w * 1024 + l16, pm);
  } else {
    const unsigned want = (unsigned)((0x5555ull << 16) >> (fr + 25 - 8 * fq));
    u32x4 mb;
#pragma unroll
    for (int k = 0; k < 4; ++k) mb[k] = (want >> (15 - 4 * w - k)) & 1 ? 0u : 0xFF800000u;
    lds_w4(L_MB + (w - 2) * 1024 + l16, mb);
  }
  pv[0] = L_K + 24576 + (16 * (fq & 1) + 4 * (fq >> 1) + (fr >> 2)) * 128 + w * 32 + (lane & 3) * 8;
  pv[7] = lane < 2 ? lane * 32 * 128 : OOB;
  ps[4] = WS_WO + (rot(g, 1) * 128 + kb * 8 + w) * 1024;
  ps[5] = WS_WO + 2 * MB + (rot(g, 2) * 128 + kb * 8 + w) * 1024;
  const unsigned krow = (8 * (fr >> 2) + (fr & 3)) * 64 + wr * 2048 + L_K;
  const unsigned kv0 = 16384 + wr * 2048 + fr * 64 + ((fq ^ ((fr >> 3) << 1) ^ ((fr >> 1) & 3)) * 16);
  u32x16 va = {l16, l16 + 4096, lw, L_INB + w * 18432 + l16, L_INB + ((w + 3) & 3) * 18432 + l16, L_INB + ((w + 2) & 3) * 18432 + 6144 + l16,
                     L_INB + ((w + 1) & 3) * 18432 + 12288 + l16, kv0, kv0 ^ 48, L_CS + fq * 16, fq * 512 + fr * 8, fl0,
                     ((lane & 31) >> 2) * 128 + (lane & 3) * 8,
                     fq < 3 ? (w * 128 + (lane >> 3 & 1) * 64 + 2 * ((2 * g - rot(g, fq % 3)) & 15) + (lane >> 2 & 1)) * 128 + (lane & 3) * 8 : ((lane & 7) * 64 + h) * 128 + w * 8,
                     fr8, krow + (fq ^ swzK(fr >> 2) ^ ((fr >> 1) & 1)) * 16};
  u32x8 vb = {L_V + wr * 2048 + (8 * fq + (fr >> 2)) * 64 + 8 * (lane & 1) + (((lane >> 1) & 1) ^ ((fq & 1) << 1) ^ (fr >> 3)) * 16,
                    krow + (fq ^ swzK(((fr >> 2) + 2) & 3) ^ ((fr >> 1) & 1)) * 16, 0, 0, xo,
                    lane < 24 ? L_CS + w * 96 + lane * 4 : L_SCR, w == 0 ? bid * 1024 + lane * 4 : OOB, 0};
  u32x16 sa, sb;
#pragma unroll
  for (int i = 0; i < 4; ++i) {
    sa[i] = WS_XF + cb + w * 8192 + ((wr + 1 + i) & 3) * 65536;
    sa[4 + i] = sa[i] + WS_OF + p2 * 32768;
  }
  sa[8] = soo;                                              sa[9] = sxo;
  sa[10] = WS_ST + (c * 16 + 2 * wr) * 2048;                sa[11] = sso;
  sa[12] = skv - 16384;                                     sa[13] = c ? skv - 32 * 32768 : WS_XF + (h & 15) * 32768;
  sa[14] = c ? skv - 32 * 32768 + 8 * MB : sa[13];          sa[15] = WS_FL + (c * 64 + 8 * w) * 128;
  sb[0] = myfl;                                             sb[1] = WS_WF + (h * 4 + w) * 49152;
  sb[2] = WS_WO + (g2 * 4 + w) * 32768;                     sb[3] = c ? myfl - w * 8 + ((w - dl) & 3) * 8 - 32 * 128 + 64 : myfl + 4096;
  sb[4] = w * 96;                                           sb[5] = c ? myfl + 65536 - w * 8 + ((3 - dl) & 3) * 8 - 32 * 128 + 64 : myfl + 4096;
  sb[6] = ((c * 16 + 2 * wr + p2) * 16 * 1024 + g2 * 64) * 2;  sb[7] = WS_FL + 96;
  sb[8] = WS_CS + h * 16384;                                sb[9] = w * 2048;
  sb[10] = c ? -8 : 8 - wr;                                  sb[11] = c ? 16 * MB : 0;
  sb[12] = (2 * g2 + p2 - h) * 128;                                sb[13] = bid == 0 && w == 0 ? WS_EP : 0;
  sb[14] = c == 0 && wr == 0 ? ~0u : 0u;                     sb[15] = 0;
#pragma unroll
  for (int i = 0; i < 16; ++i) { sa[i] = uni(sa[i]); sb[i] = uni(sb[i]); }
  const rsrc_t wsk = __builtin_amdgcn_make_buffer_rsrc(ws, 0, wr == 3 ? 128 * MB : 0u, 0x00020000);
  const rsrc_t outr = __builtin_amdgcn_make_buffer_rsrc(out, 0, 4 * MB, 0x00020000);
  u32x8 pa, pc;
#pragma unroll
  for (int i = 0; i < 8; ++i) { pa[i] = uni(ps[i]); pc[i] = uni(ps[8 + i]); }
  u32x16 pz = {pw[2], pw[3], pw[4], pw[5], pv[1], pv[2], pv[3], pv[4], pv[0], pw[8], pw[0], pw[1], pw[6], pw[7], pv[5], pv[6]};
  unsigned pf = pv[7];
  pin(va); pin(vb); pin(pz); pin(pf);
  STAMP(17);
  wait_vm<7>();
  pin(tag);
  STAMP(18);
  publish(1, true);
  publish(0, true);
  { const u64 t = (((u64)tag[1] << 32) | tag[0]) + 1; vb[2] = (unsigned)t; vb[3] = (unsigned)(t >> 32); }
  STAMP(19);
  vb[7] = stw;
  asm volatile(
"; @entry\n"
"s_mov_b32 s0, 0\n"
"s_mov_b32 s3, 0\n"
"v_xor_b32_e32 v255, 32, v43\n"
"v_add_u32_e32 v254, 0xc000, v43\n"
"v_add_u32_e32 v253, 0xc000, v255\n"
"v_mov_b32_e32 v24, 0x3f803f80\n"
"v_mov_b32_e32 v25, 0x3f803f80\n"
"v_mov_b32_e32 v26, 0x3f803f80\n"
"v_mov_b32_e32 v27, 0x3f803f80\n"
"s_waitcnt lgkmcnt(0)\n"
"s_bfe_u32 s99, s26, 0x10001\n"
"s_xor_b32 s99, s99, 1\n"
"s_mul_i32 s98, s99, 0x6000\n"
"s_add_u32 s98, s98, s27\n"
"s_add_u32 s97, s45, 0\n"
"s_and_b32 s97, s97, 7\n"
"s_add_u32 s97, s97, s99\n"
"s_mul_i32 s97, s97, 0x30000\n"
"s_add_u32 s97, s97, s44\n"
"s_add_u32 m0, s98, 0\n"
"s_add_u32 s99, s97, 0\n"
"buffer_load_dwordx4 v42, s[28:31], s99 offen lds\n"
"s_add_u32 m0, s98, 0x400\n"
"s_add_u32 s99, s97, 0x18000\n"
"buffer_load_dwordx4 v42, s[28:31], s99 offen lds\n"
"s_add_u32 m0, s98, 0x800\n"
"s_add_u32 s99, s97, 0x800\n"
"buffer_load_dwordx4 v42, s[28:31], s99 offen lds\n"
"s_add_u32 m0, s98, 0xc00\n"
"s_add_u32 s99, s97, 0x18800\n"
"buffer_load_dwordx4 v42, s[28:31], s99 offen lds\n"
"s_add_u32 m0, s98, 0x1000\n"
"s_add_u32 s99, s97, 0x1000\n"
"buffer_load_dwordx4 v42, s[28:31], s99 offen lds\n"
"s_add_u32 m0, s98, 0x1400\n"
"s_add_u32 s99, s97, 0x19000\n"
"buffer_load_dwordx4 v42, s[28:31], s99 offen lds\n"
"s_add_u32 s99, s68, s80\n"
"s_waitcnt vmcnt(6)\n"
"buffer_store_dwordx2 v[18:19], v50, s[4:7], s99 offen sc1\n"
"s_bfe_u32 s99, s26, 0x10001\n"
"s_xor_b32 s99, s99, 0\n"
"s_mul_i32 s98, s99, 0x6000\n"
"s_add_u32 s98, s98, s27\n"
"s_add_u32 s97, s45, 2\n"
"s_and_b32 s97, s97, 7\n"
"s_add_u32 s97, s97, s99\n"
"s_mul_i32 s97, s97, 0x30000\n"
"s_add_u32 s97, s97, s44\n"
"s_add_u32 m0, s98, 0xc000\n"
"s_add_u32 s99, s97, 0\n"
"buffer_load_dwordx4 v42, s[28:31], s99 offen lds\n"
"s_add_u32 m0, s98, 0xc400\n"
"s_add_u32 s99, s97, 0x18000\n"
"buffer_load_dwordx4 v42, s[28:31], s99 offen lds\n"
"s_add_u32 m0, s98, 0xc800\n"
"s_add_u32 s99, s97, 0x800\n"
"buffer_load_dwordx4 v42, s[28:31], s99 offen lds\n"
"s_add_u32 m0, s98, 0xcc00\n"
"s_add_u32 s99, s97, 0x18800\n"
"buffer_load_dwordx4 v42, s[28:31], s99 offen lds\n"
"s_add_u32 m0, s98, 0xd000\n"
"s_add_u32 s99, s97, 0x1000\n"
"buffer_load_dwordx4 v42, s[28:31], s99 offen lds\n"
"s_add_u32 m0, s98, 0xd400\n"
"s_add_u32 s99, s97, 0x19000\n"
"buffer_load_dwordx4 v42, s[28:31], s99 offen lds\n"
"s_bfe_u32 s99, s26, 0x10001\n"
"s_xor_b32 s99, s99, 1\n"
"s_mul_i32 s98, s99, 0x6000\n"
"s_add_u32 s98, s98, s27\n"
"s_add_u32 s97, s45, 2\n"
"s_and_b32 s97, s97, 7\n"
"s_add_u32 s97, s97, s99\n"
"s_mul_i32 s97, s97, 0x30000\n"
"s_add_u32 s97, s97, s44\n"
"s_add_u32 m0, s98, 0xc000\n"
"s_add_u32 s99, s97, 0\n"
"buffer_load_dwordx4 v42, s[28:31], s99 offen lds\n"
"s_add_u32 m0, s98, 0xc400\n"
"s_add_u32 s99, s97, 0x18000\n"
"buffer_load_dwordx4 v42, s[28:31], s99 offen lds\n"
"s_add_u32 m0, s98, 0xc800\n"
"s_add_u32 s99, s97, 0x800\n"
"buffer_load_dwordx4 v42, s[28:31], s99 offen lds\n"
"s_add_u32 m0, s98, 0xcc00\n"
"s_add_u32 s99, s97, 0x18800\n"
"buffer_load_dwordx4 v42, s[28:31], s99 offen lds\n"
"s_add_u32 m0, s98, 0xd000\n"
"s_add_u32 s99, s97, 0x1000\n"
"buffer_load_dwordx4 v42, s[28:31], s99 offen lds\n"
"s_add_u32 m0, s98, 0xd400\n"
"s_add_u32 s99, s97, 0x19000\n"
"buffer_load_dwordx4 v42, s[28:31], s99 offen lds\n"
"s_waitcnt vmcnt(13)\n"
"ds_read_b64_tr_b16 a[0:1], v43\n"
"ds_read_b64_tr_b16 a[2:3], v43 offset:1024\n"
"ds_read_b64_tr_b16 a[4:5], v255\n"
"ds_read_b64_tr_b16 a[6:7], v255 offset:1024\n"
"ds_read_b64_tr_b16 a[8:9], v43 offset:2048\n"
"ds_read_b64_tr_b16 a[10:11], v43 offset:3072\n"
"ds_read_b64_tr_b16 a[12:13], v255 offset:2048\n"
"ds_read_b64_tr_b16 a[14:15], v255 offset:3072\n"
"ds_read_b64_tr_b16 a[16:17], v43 offset:4096\n"
"ds_read_b64_tr_b16 a[18:19], v43 offset:5120\n"
"ds_read_b64_tr_b16 a[20:21], v255 offset:4096\n"
"ds_read_b64_tr_b16 a[22:23], v255 offset:5120\n"
"ds_read_b64_tr_b16 a[24:25], v43 offset:24576\n"
"ds_read_b64_tr_b16 a[26:27], v43 offset:25600\n"
"ds_read_b64_tr_b16 a[28:29], v255 offset:24576\n"
"s_waitcnt lgkmcnt(13)\n"
"ds_read_b64_tr_b16 a[30:31], v255 offset:25600\n"
"ds_read_b64_tr_b16 a[32:33], v43 offset:26624\n"
"s_waitcnt lgkmcnt(13)\n"
"ds_read_b64_tr_b16 a[34:35], v43 offset:27648\n"
"ds_read_b64_tr_b16 a[36:37], v255 offset:26624\n"
"s_waitcnt lgkmcnt(13)\n"
"ds_read_b64_tr_b16 a[38:39], v255 offset:27648\n"
"ds_read_b64_tr_b16 a[40:41], v43 offset:28672\n"
"s_waitcnt lgkmcnt(13)\n"
"ds_read_b64_tr_b16 a[42:43], v43 offset:29696\n"
"ds_read_b64_tr_b16 a[44:45], v255 offset:28672\n"
"s_waitcnt lgkmcnt(13)\n"
"ds_read_b64_tr_b16 a[46:47], v255 offset:29696\n"
"s_waitcnt lgkmcnt(12)\n"
"v_mfma_f32_16x16x32_bf16 v[28:31], a[0:3], v[24:27], 0\n"
"v_mfma_f32_16x16x32_bf16 v[48:51], a[4:7], v[24:27], 0\n"
"v_mfma_f32_16x16x32_bf16 v[52:55], a[8:11], v[24:27], 0\n"
"v_mfma_f32_16x16x32_bf16 v[56:59], a[12:15], v[24:27], 0\n"
"v_mfma_f32_16x16x32_bf16 v[60:63], a[16:19], v[24:27], 0\n"
"v_mfma_f32_16x16x32_bf16 v[64:67], a[20:23], v[24:27], 0\n"
"s_waitcnt lgkmcnt(0)\n"
"v_mfma_f32_16x16x32_bf16 v[28:31], a[24:27], v[24:27], v[28:31]\n"
"v_mfma_f32_16x16x32_bf16 v[48:51], a[28:31], v[24:27], v[48:51]\n"
"v_mfma_f32_16x16x32_bf16 v[52:55], a[32:35], v[24:27], v[52:55]\n"
"v_mfma_f32_16x16x32_bf16 v[56:59], a[36:39], v[24:27], v[56:59]\n"
"v_mfma_f32_16x16x32_bf16 v[60:63], a[40:43], v[24:27], v[60:63]\n"
"v_mfma_f32_16x16x32_bf16 v[64:67], a[44:47], v[24:27], v[64:67]\n"
"s_bfe_u32 s99, s26, 0x10001\n"
"s_xor_b32 s99, s99, 0\n"
"s_mul_i32 s98, s99, 0x6000\n"
"s_add_u32 s98, s98, s27\n"
"s_add_u32 s97, s45, 4\n"
"s_and_b32 s97, s97, 7\n"
"s_add_u32 s97, s97, s99\n"
"s_mul_i32 s97, s97, 0x30000\n"
"s_add_u32 s97, s97, s44\n"
"s_add_u32 m0, s98, 0\n"
"s_add_u32 s99, s97, 0\n"
"buffer_load_dwordx4 v42, s[28:31], s99 offen lds\n"
"s_add_u32 m0, s98, 0x400\n"
"s_add_u32 s99, s97, 0x18000\n"
"buffer_load_dwordx4 v42, s[28:31], s99 offen lds\n"
"s_add_u32 m0, s98, 0x800\n"
"s_add_u32 s99, s97, 0x800\n"
"buffer_load_dwordx4 v42, s[28:31], s99 offen lds\n"
"s_add_u32 m0, s98, 0xc00\n"
"s_add_u32 s99, s97, 0x18800\n"
"buffer_load_dwordx4 v42, s[28:31], s99 offen lds\n"
"s_add_u32 m0, s98, 0x1000\n"
"s_add_u32 s99, s97, 0x1000\n"
"buffer_load_dwordx4 v42, s[28:31], s99 offen lds\n"
"s_add_u32 m0, s98, 0x1400\n"
"s_add_u32 s99, s97, 0x19000\n"
"buffer_load_dwordx4 v42, s[28:31], s99 offen lds\n"
"s_bfe_u32 s99, s26, 0x10001\n"
"s_xor_b32 s99, s99, 1\n"
"s_mul_i32 s98, s99, 0x6000\n"
"s_add_u32 s98, s98, s27\n"
"s_add_u32 s97, s45, 4\n"
"s_and_b32 s97, s97, 7\n"
"s_add_u32 s97, s97, s99\n"
"s_mul_i32 s97, s97, 0x30000\n"
"s_add_u32 s97, s97, s44\n"
"s_add_u32 m0, s98, 0\n"
"s_add_u32 s99, s97, 0\n"
"buffer_load_dwordx4 v42, s[28:31], s99 offen lds\n"
"s_add_u32 m0, s98, 0x400\n"
"s_add_u32 s99, s97, 0x18000\n"
"buffer_load_dwordx4 v42, s[28:31], s99 offen lds\n"
"s_add_u32 m0, s98, 0x800\n"
"s_add_u32 s99, s97, 0x800\n"
"buffer_load_dwordx4 v42, s[28:31], s99 offen lds\n"
"s_add_u32 m0, s98, 0xc00\n"
"s_add_u32 s99, s97, 0x18800\n"
"buffer_load_dwordx4 v42, s[28:31], s99 offen lds\n"
"s_add_u32 m0, s98, 0x1000\n"
"s_add_u32 s99, s97, 0x1000\n"
"buffer_load_dwordx4 v42, s[28:31], s99 offen lds\n"
"s_add_u32 m0, s98, 0x1400\n"
"s_add_u32 s99, s97, 0x19000\n"
"buffer_load_dwordx4 v42, s[28:31], s99 offen lds\n"
"s_waitcnt vmcnt(12)\n"
"ds_read_b64_tr_b16 a[48:49], v254\n"
"ds_read_b64_tr_b16 a[50:51], v254 offset:1024\n"
"ds_read_b64_tr_b16 a[52:53], v253\n"
"ds_read_b64_tr_b16 a[54:55], v253 offset:1024\n"
"ds_read_b64_tr_b16 a[56:57], v254 offset:2048\n"
"ds_read_b64_tr_b16 a[58:59], v254 offset:3072\n"
"ds_read_b64_tr_b16 a[60:61], v253 offset:2048\n"
"ds_read_b64_tr_b16 a[62:63], v253 offset:3072\n"
"ds_read_b64_tr_b16 a[64:65], v254 offset:4096\n"
"ds_read_b64_tr_b16 a[66:67], v254 offset:5120\n"
"ds_read_b64_tr_b16 a[68:69], v253 offset:4096\n"
"ds_read_b64_tr_b16 a[70:71], v253 offset:5120\n"
"ds_read_b64_tr_b16 a[72:73], v254 offset:24576\n"
"ds_read_b64_tr_b16 a[74:75], v254 offset:25600\n"
"ds_read_b64_tr_b16 a[76:77], v253 offset:24576\n"
"s_waitcnt lgkmcnt(13)\n"
"ds_read_b64_tr_b16 a[78:79], v253 offset:25600\n"
"ds_read_b64_tr_b16 a[80:81], v254 offset:26624\n"
"s_waitcnt lgkmcnt(13)\n"
"ds_read_b64_tr_b16 a[82:83], v254 offset:27648\n"
"ds_read_b64_tr_b16 a[84:85], v253 offset:26624\n"
"s_waitcnt lgkmcnt(13)\n"
"ds_read_b64_tr_b16 a[86:87], v253 offset:27648\n"
"ds_read_b64_tr_b16 a[88:89], v254 offset:28672\n"
"s_waitcnt lgkmcnt(13)\n"
"ds_read_b64_tr_b16 a[90:91], v254 offset:29696\n"
"ds_read_b64_tr_b16 a[92:93], v253 offset:28672\n"
"s_waitcnt lgkmcnt(13)\n"
"ds_read_b64_tr_b16 a[94:95], v253 offset:29696\n"
"s_waitcnt lgkmcnt(12)\n"
"v_mfma_f32_16x16x32_bf16 v[28:31], a[48:51], v[24:27], v[28:31]\n"
"v_mfma_f32_16x16x32_bf16 v[48:51], a[52:55], v[24:27], v[48:51]\n"
"v_mfma_f32_16x16x32_bf16 v[52:55], a[56:59], v[24:27], v[52:55]\n"
"v_mfma_f32_16x16x32_bf16 v[56:59], a[60:63], v[24:27], v[56:59]\n"
"v_mfma_f32_16x16x32_bf16 v[60:63], a[64:67], v[24:27], v[60:63]\n"
"v_mfma_f32_16x16x32_bf16 v[64:67], a[68:71], v[24:27], v[64:67]\n"
"s_waitcnt lgkmcnt(0)\n"
"v_mfma_f32_16x16x32_bf16 v[28:31], a[72:75], v[24:27], v[28:31]\n"
"v_mfma_f32_16x16x32_bf16 v[48:51], a[76:79], v[24:27], v[48:51]\n"
"v_mfma_f32_16x16x32_bf16 v[52:55], a[80:83], v[24:27], v[52:55]\n"
"v_mfma_f32_16x16x32_bf16 v[56:59], a[84:87], v[24:27], v[56:59]\n"
"v_mfma_f32_16x16x32_bf16 v[60:63], a[88:91], v[24:27], v[60:63]\n"
"v_mfma_f32_16x16x32_bf16 v[64:67], a[92:95], v[24:27], v[64:67]\n"
"s_bfe_u32 s99, s26, 0x10001\n"
"s_xor_b32 s99, s99, 0\n"
"s_mul_i32 s98, s99, 0x6000\n"
"s_add_u32 s98, s98, s27\n"
"s_add_u32 s97, s45, 6\n"
"s_and_b32 s97, s97, 7\n"
"s_add_u32 s97, s97, s99\n"
"s_mul_i32 s97, s97, 0x30000\n"
"s_add_u32 s97, s97, s44\n"
"s_add_u32 m0, s98, 0xc000\n"
"s_add_u32 s99, s97, 0\n"
"buffer_load_dwordx4 v42, s[28:31], s99 offen lds\n"
"s_add_u32 m0, s98, 0xc400\n"
"s_add_u32 s99, s97, 0x18000\n"
"buffer_load_dwordx4 v42, s[28:31], s99 offen lds\n"
"s_add_u32 m0, s98, 0xc800\n"
"s_add_u32 s99, s97, 0x800\n"
"buffer_load_dwordx4 v42, s[28:31], s99 offen lds\n"
"s_add_u32 m0, s98, 0xcc00\n"
"s_add_u32 s99, s97, 0x18800\n"
"buffer_load_dwordx4 v42, s[28:31], s99 offen lds\n"
"s_add_u32 m0, s98, 0xd000\n"
"s_add_u32 s99, s97, 0x1000\n"
"buffer_load_dwordx4 v42, s[28:31], s99 offen lds\n"
"s_add_u32 m0, s98, 0xd400\n"
"s_add_u32 s99, s97, 0x19000\n"
"buffer_load_dwordx4 v42, s[28:31], s99 offen lds\n"
"s_bfe_u32 s99, s26, 0x10001\n"
"s_xor_b32 s99, s99, 1\n"
"s_mul_i32 s98, s99, 0x6000\n"
"s_add_u32 s98, s98, s27\n"
"s_add_u32 s97, s45, 6\n"
"s_and_b32 s97, s97, 7\n"
"s_add_u32 s97, s97, s99\n"
"s_mul_i32 s97, s97, 0x30000\n"
"s_add_u32 s97, s97, s44\n"
"s_add_u32 m0, s98, 0xc000\n"
"s_add_u32 s99, s97, 0\n"
"buffer_load_dwordx4 v42, s[28:31], s99 offen lds\n"
"s_add_u32 m0, s98, 0xc400\n"
"s_add_u32 s99, s97, 0x18000\n"
"buffer_load_dwordx4 v42, s[28:31], s99 offen lds\n"
"s_add_u32 m0, s98, 0xc800\n"
"s_add_u32 s99, s97, 0x800\n"
"buffer_load_dwordx4 v42, s[28:31], s99 offen lds\n"
"s_add_u32 m0, s98, 0xcc00\n"
"s_add_u32 s99, s97, 0x18800\n"
"buffer_load_dwordx4 v42, s[28:31], s99 offen lds\n"
"s_add_u32 m0, s98, 0xd000\n"
"s_add_u32 s99, s97, 0x1000\n"
"buffer_load_dwordx4 v42, s[28:31], s99 offen lds\n"
"s_add_u32 m0, s98, 0xd400\n"
"s_add_u32 s99, s97, 0x19000\n"
"buffer_load_dwordx4 v42, s[28:31], s99 offen lds\n"
"s_add_u32 s99, s67, 0x1000\n"
"buffer_load_dwordx2 v[250:251], v12, s[4:7], s99 offen sc1\n"
"s_waitcnt vmcnt(13)\n"
"ds_read_b64_tr_b16 a[96:97], v43\n"
"ds_read_b64_tr_b16 a[98:99], v43 offset:1024\n"
"ds_read_b64_tr_b16 a[100:101], v255\n"
"ds_read_b64_tr_b16 a[102:103], v255 offset:1024\n"
"ds_read_b64_tr_b16 a[104:105], v43 offset:2048\n"
"ds_read_b64_tr_b16 a[106:107], v43 offset:3072\n"
"ds_read_b64_tr_b16 a[108:109], v255 offset:2048\n"
"ds_read_b64_tr_b16 a[110:111], v255 offset:3072\n"
"ds_read_b64_tr_b16 a[112:113], v43 offset:4096\n"
"ds_read_b64_tr_b16 a[114:115], v43 offset:5120\n"
"ds_read_b64_tr_b16 a[116:117], v255 offset:4096\n"
"ds_read_b64_tr_b16 a[118:119], v255 offset:5120\n"
"ds_read_b64_tr_b16 a[120:121], v43 offset:24576\n"
"ds_read_b64_tr_b16 a[122:123], v43 offset:25600\n"
"ds_read_b64_tr_b16 a[124:125], v255 offset:24576\n"
"s_waitcnt lgkmcnt(13)\n"
"ds_read_b64_tr_b16 a[126:127], v255 offset:25600\n"
"ds_read_b64_tr_b16 v[68:69], v43 offset:26624\n"
"s_waitcnt lgkmcnt(13)\n"
"ds_read_b64_tr_b16 v[70:71], v43 offset:27648\n"
"ds_read_b64_tr_b16 v[72:73], v255 offset:26624\n"
"s_waitcnt lgkmcnt(13)\n"
"ds_read_b64_tr_b16 v[74:75], v255 offset:27648\n"
"ds_read_b64_tr_b16 v[76:77], v43 offset:28672\n"
"s_waitcnt lgkmcnt(13)\n"
"ds_read_b64_tr_b16 v[78:79], v43 offset:29696\n"
"ds_read_b64_tr_b16 v[80:81], v255 offset:28672\n"
"s_waitcnt lgkmcnt(13)\n"
"ds_read_b64_tr_b16 v[82:83], v255 offset:29696\n"
"s_waitcnt lgkmcnt(12)\n"
"v_mfma_f32_16x16x32_bf16 v[28:31], a[96:99], v[24:27], v[28:31]\n"
"v_mfma_f32_16x16x32_bf16 v[48:51], a[100:103], v[24:27], v[48:51]\n"
"v_mfma_f32_16x16x32_bf16 v[52:55], a[104:107], v[24:27], v[52:55]\n"
"v_mfma_f32_16x16x32_bf16 v[56:59], a[108:111], v[24:27], v[56:59]\n"
"v_mfma_f32_16x16x32_bf16 v[60:63], a[112:115], v[24:27], v[60:63]\n"
"v_mfma_f32_16x16x32_bf16 v[64:67], a[116:119], v[24:27], v[64:67]\n"
"s_waitcnt lgkmcnt(0)\n"
"v_mfma_f32_16x16x32_bf16 v[28:31], a[120:123], v[24:27], v[28:31]\n"
"v_mfma_f32_16x16x32_bf16 v[48:51], a[124:127], v[24:27], v[48:51]\n"
"v_mfma_f32_16x16x32_bf16 v[52:55], v[68:71], v[24:27], v[52:55]\n"
"v_mfma_f32_16x16x32_bf16 v[56:59], v[72:75], v[24:27], v[56:59]\n"
"v_mfma_f32_16x16x32_bf16 v[60:63], v[76:79], v[24:27], v[60:63]\n"
"v_mfma_f32_16x16x32_bf16 v[64:67], v[80:83], v[24:27], v[64:67]\n"
"s_waitcnt vmcnt(0)\n"
"v_cmp_ne_u64_e32 vcc, v[250:251], v[18:19]\n"
"s_cbranch_vccnz .Lmk_f1s\n"
".Lmk_f1r:\n"
"s_add_u32 s99, s52, 0x40000\n"
"buffer_load_dwordx4 v[84:87], v32, s[4:7], s99 offen sc1\n"
"buffer_load_dwordx4 v[88:91], v32, s[4:7], s99 offen offset:1024 sc1\n"
"buffer_load_dwordx4 v[92:95], v33, s[4:7], s99 offen sc1\n"
"buffer_load_dwordx4 v[96:99], v33, s[4:7], s99 offen offset:1024 sc1\n"
"buffer_load_dwordx4 v[100:103], v34, s[4:7], s99 offen sc1\n"
"buffer_load_dwordx4 v[104:107], v34, s[4:7], s99 offen offset:1024 sc1\n"
"buffer_load_dwordx4 v[108:111], v35, s[4:7], s99 offen sc1\n"
"buffer_load_dwordx4 v[112:115], v35, s[4:7], s99 offen offset:1024 sc1\n"
"s_add_u32 s99, s53, 0x40000\n"
"buffer_load_dwordx4 v[116:119], v32, s[4:7], s99 offen sc1\n"
"buffer_load_dwordx4 v[120:123], v32, s[4:7], s99 offen offset:1024 sc1\n"
"buffer_load_dwordx4 v[124:127], v33, s[4:7], s99 offen sc1\n"
"buffer_load_dwordx4 v[128:131], v33, s[4:7], s99 offen offset:1024 sc1\n"
"buffer_load_dwordx4 v[132:135], v34, s[4:7], s99 offen sc1\n"
"buffer_load_dwordx4 v[136:139], v34, s[4:7], s99 offen offset:1024 sc1\n"
"buffer_load_dwordx4 v[140:143], v35, s[4:7], s99 offen sc1\n"
"buffer_load_dwordx4 v[144:147], v35, s[4:7], s99 offen offset:1024 sc1\n"
"s_getpc_b64 s[84:85]\n"
".Lmk_h2:\n"
"s_and_b32 s84, s84, -16\n"
"s_and_b32 s85, s85, 0xffff\n"
"s_mov_b32 s86, (.Lmk_end-.Lmk_h2)&0xFFFFFFF0\n"
"s_mov_b32 s87, 0x20000\n"
"s_and_b32 s99, s26, 0xfffffc00\n"
"s_mov_b32 m0, 0x27800\n"
"s_add_u32 s99, s99, 0x1e000\n"
"buffer_load_dwordx4 v0, s[84:87], s99 offen lds\n"
"ds_read_b64_tr_b16 v[148:149], v254\n"
"ds_read_b64_tr_b16 v[150:151], v254 offset:1024\n"
"ds_read_b64_tr_b16 v[152:153], v253\n"
"ds_read_b64_tr_b16 v[154:155], v253 offset:1024\n"
"ds_read_b64_tr_b16 v[156:157], v254 offset:2048\n"
"ds_read_b64_tr_b16 v[158:159], v254 offset:3072\n"
"ds_read_b64_tr_b16 v[160:161], v253 offset:2048\n"
"ds_read_b64_tr_b16 v[162:163], v253 offset:3072\n"
"ds_read_b64_tr_b16 v[164:165], v254 offset:4096\n"
"ds_read_b64_tr_b16 v[166:167], v254 offset:5120\n"
"ds_read_b64_tr_b16 v[168:169], v253 offset:4096\n"
"ds_read_b64_tr_b16 v[170:171], v253 offset:5120\n"
"ds_read_b64_tr_b16 v[172:173], v254 offset:24576\n"
"ds_read_b64_tr_b16 v[174:175], v254 offset:25600\n"
"ds_read_b64_tr_b16 v[176:177], v253 offset:24576\n"
"s_waitcnt lgkmcnt(13)\n"
"ds_read_b64_tr_b16 v[178:179], v253 offset:25600\n"
"ds_read_b64_tr_b16 v[180:181], v254 offset:26624\n"
"s_waitcnt lgkmcnt(13)\n"
"ds_read_b64_tr_b16 v[182:183], v254 offset:27648\n"
"ds_read_b64_tr_b16 v[184:185], v253 offset:26624\n"
"s_waitcnt lgkmcnt(13)\n"
"ds_read_b64_tr_b16 v[186:187], v253 offset:27648\n"
"ds_read_b64_tr_b16 v[188:189], v254 offset:28672\n"
"s_waitcnt lgkmcnt(13)\n"
"ds_read_b64_tr_b16 v[190:191], v254 offset:29696\n"
"ds_read_b64_tr_b16 v[192:193], v253 offset:28672\n"
"s_waitcnt lgkmcnt(13)\n"
"ds_read_b64_tr_b16 v[194:195], v253 offset:29696\n"
"s_waitcnt lgkmcnt(12)\n"
"v_mfma_f32_16x16x32_bf16 v[28:31], v[148:151], v[24:27], v[28:31]\n"
"v_mfma_f32_16x16x32_bf16 v[48:51], v[152:155], v[24:27], v[48:51]\n"
"v_mfma_f32_16x16x32_bf16 v[52:55], v[156:159], v[24:27], v[52:55]\n"
"v_mfma_f32_16x16x32_bf16 v[56:59], v[160:163], v[24:27], v[56:59]\n"
"v_mfma_f32_16x16x32_bf16 v[60:63], v[164:167], v[24:27], v[60:63]\n"
"v_mfma_f32_16x16x32_bf16 v[64:67], v[168:171], v[24:27], v[64:67]\n"
"s_waitcnt lgkmcnt(0)\n"
"v_mfma_f32_16x16x32_bf16 v[28:31], v[172:175], v[24:27], v[28:31]\n"
"v_mfma_f32_16x16x32_bf16 v[48:51], v[176:179], v[24:27], v[48:51]\n"
"v_mfma_f32_16x16x32_bf16 v[52:55], v[180:183], v[24:27], v[52:55]\n"
"v_mfma_f32_16x16x32_bf16 v[56:59], v[184:187], v[24:27], v[56:59]\n"
"v_mfma_f32_16x16x32_bf16 v[60:63], v[188:191], v[24:27], v[60:63]\n"
"v_mfma_f32_16x16x32_bf16 v[64:67], v[192:195], v[24:27], v[64:67]\n"
"s_nop 7\n"
"ds_write_b128 v44, v[28:31]\n"
"ds_write_b128 v44, v[48:51] offset:64\n"
"ds_write_b128 v44, v[52:55] offset:128\n"
"ds_write_b128 v44, v[56:59] offset:192\n"
"ds_write_b128 v44, v[60:63] offset:256\n"
"ds_write_b128 v44, v[64:67] offset:320\n"
"s_waitcnt lgkmcnt(0)\n"
"s_barrier\n"
"ds_read_b32 v255, v45\n"
"ds_read_b32 v254, v45 offset:6144\n"
"ds_read_b32 v253, v45 offset:12288\n"
"ds_read_b32 v252, v45 offset:18432\n"
"s_waitcnt lgkmcnt(0)\n"
"v_add_f32_e32 v255, v255, v254\n"
"v_add_f32_e32 v253, v253, v252\n"
"v_add_f32_e32 v255, v255, v253\n"
"ds_write_b32 v21, v255\n"
"s_waitcnt vmcnt(16)\n"
"; @A G1 0.0\n"
"v_mfma_f32_16x16x32_bf16 v[24:27], a[0:3], v[84:87], 0\n"
"s_add_u32 s99, s62, 0x4000\n"
"v_mfma_f32_16x16x32_bf16 v[28:31], a[4:7], v[84:87], 0\n"
"s_add_u32 m0, s77, 0xa000\n"
"s_add_u32 s98, s49, 0\n"
"buffer_load_dwordx4 v47, s[36:39], s98 offen lds\n"
"v_mfma_f32_16x16x32_bf16 v[48:51], a[8:11], v[84:87], 0\n"
"s_add_u32 m0, s77, 0xa400\n"
"s_add_u32 s98, s49, 0x4000\n"
"v_mfma_f32_16x16x32_bf16 v[52:55], a[12:15], v[84:87], 0\n"
"buffer_load_dwordx2 v[254:255], v10, s[4:7], s99 offen sc1\n"
"v_mfma_f32_16x16x32_bf16 v[56:59], a[16:19], v[84:87], 0\n"
"s_add_u32 s97, s54, 0x40000\n"
"buffer_load_dwordx4 v47, s[36:39], s98 offen lds\n"
"v_mfma_f32_16x16x32_bf16 v[60:63], a[20:23], v[84:87], 0\n"
"s_waitcnt vmcnt(18)\n"
"s_add_u32 m0, s77, 0xc000\n"
"s_add_u32 s98, s50, 0\n"
"v_mfma_f32_16x16x32_bf16 v[24:27], a[24:27], v[88:91], v[24:27]\n"
"buffer_load_dwordx4 v[64:67], v32, s[4:7], s97 offen sc1\n"
"v_mfma_f32_16x16x32_bf16 v[28:31], a[28:31], v[88:91], v[28:31]\n"
"buffer_load_dwordx4 v47, s[40:43], s98 offen lds\n"
"v_mfma_f32_16x16x32_bf16 v[48:51], a[32:35], v[88:91], v[48:51]\n"
"s_add_u32 m0, s77, 0xc400\n"
"s_add_u32 s98, s50, 0x4000\n"
"v_mfma_f32_16x16x32_bf16 v[52:55], a[36:39], v[88:91], v[52:55]\n"
"buffer_load_dwordx2 v[252:253], v10, s[4:7], s99 offen offset:128 sc1\n"
"v_mfma_f32_16x16x32_bf16 v[56:59], a[40:43], v[88:91], v[56:59]\n"
"buffer_load_dwordx4 v[84:87], v32, s[4:7], s97 offen offset:1024 sc1\n"
"buffer_load_dwordx4 v47, s[40:43], s98 offen lds\n"
"v_mfma_f32_16x16x32_bf16 v[60:63], a[44:47], v[88:91], v[60:63]\n"
"s_waitcnt vmcnt(22)\n"
"s_add_u32 m0, s77, 0x4000\n"
"s_add_u32 s98, s46, 0\n"
"v_mfma_f32_16x16x32_bf16 v[24:27], a[48:51], v[92:95], v[24:27]\n"
"buffer_load_dwordx2 v[250:251], v10, s[4:7], s99 offen offset:256 sc1\n"
"v_mfma_f32_16x16x32_bf16 v[28:31], a[52:55], v[92:95], v[28:31]\n"
"buffer_load_dwordx4 v46, s[32:35], s98 offen lds\n"
"v_mfma_f32_16x16x32_bf16 v[48:51], a[56:59], v[92:95], v[48:51]\n"
"s_add_u32 m0, s77, 0x4400\n"
"s_add_u32 s98, s46, 0xc000\n"
"v_mfma_f32_16x16x32_bf16 v[52:55], a[60:63], v[92:95], v[52:55]\n"
"buffer_load_dwordx2 v[248:249], v10, s[4:7], s99 offen offset:384 sc1\n"
"v_mfma_f32_16x16x32_bf16 v[56:59], a[64:67], v[92:95], v[56:59]\n"
"buffer_load_dwordx4 v[88:91], v33, s[4:7], s97 offen sc1\n"
"buffer_load_dwordx4 v46, s[32:35], s98 offen lds\n"
"v_mfma_f32_16x16x32_bf16 v[60:63], a[68:71], v[92:95], v[60:63]\n"
"s_waitcnt vmcnt(26)\n"
"s_add_u32 m0, s77, 0x6000\n"
"s_add_u32 s99, s47, 0\n"
"v_mfma_f32_16x16x32_bf16 v[24:27], a[72:75], v[96:99], v[24:27]\n"
"v_mfma_f32_16x16x32_bf16 v[28:31], a[76:79], v[96:99], v[28:31]\n"
"buffer_load_dwordx4 v46, s[32:35], s99 offen lds\n"
"v_mfma_f32_16x16x32_bf16 v[48:51], a[80:83], v[96:99], v[48:51]\n"
"s_add_u32 m0, s77, 0x6400\n"
"s_add_u32 s99, s47, 0xc000\n"
"v_mfma_f32_16x16x32_bf16 v[52:55], a[84:87], v[96:99], v[52:55]\n"
"v_mfma_f32_16x16x32_bf16 v[56:59], a[88:91], v[96:99], v[56:59]\n"
"buffer_load_dwordx4 v[92:95], v33, s[4:7], s97 offen offset:1024 sc1\n"
"buffer_load_dwordx4 v46, s[32:35], s99 offen lds\n"
"v_mfma_f32_16x16x32_bf16 v[60:63], a[92:95], v[96:99], v[60:63]\n"
"s_waitcnt vmcnt(28)\n"
"s_add_u32 m0, s77, 0x8000\n"
"s_add_u32 s99, s48, 0\n"
"v_mfma_f32_16x16x32_bf16 v[24:27], a[96:99], v[100:103], v[24:27]\n"
"v_mfma_f32_16x16x32_bf16 v[28:31], a[100:103], v[100:103], v[28:31]\n"
"buffer_load_dwordx4 v46, s[32:35], s99 offen lds\n"
"v_mfma_f32_16x16x32_bf16 v[48:51], a[104:107], v[100:103], v[48:51]\n"
"s_add_u32 m0, s77, 0x8400\n"
"s_add_u32 s99, s48, 0xc000\n"
"v_mfma_f32_16x16x32_bf16 v[52:55], a[108:111], v[100:103], v[52:55]\n"
"v_mfma_f32_16x16x32_bf16 v[56:59], a[112:115], v[100:103], v[56:59]\n"
"buffer_load_dwordx4 v[96:99], v34, s[4:7], s97 offen sc1\n"
"buffer_load_dwordx4 v46, s[32:35], s99 offen lds\n"
"v_mfma_f32_16x16x32_bf16 v[60:63], a[116:119], v[100:103], v[60:63]\n"
"s_waitcnt vmcnt(30)\n"
"v_mfma_f32_16x16x32_bf16 v[24:27], a[120:123], v[104:107], v[24:27]\n"
"v_mfma_f32_16x16x32_bf16 v[28:31], a[124:127], v[104:107], v[28:31]\n"
"v_mfma_f32_16x16x32_bf16 v[48:51], v[68:71], v[104:107], v[48:51]\n"
"v_mfma_f32_16x16x32_bf16 v[52:55], v[72:75], v[104:107], v[52:55]\n"
"v_mfma_f32_16x16x32_bf16 v[56:59], v[76:79], v[104:107], v[56:59]\n"
"buffer_load_dwordx4 v[44:47], v34, s[4:7], s97 offen offset:1024 sc1\n"
"v_mfma_f32_16x16x32_bf16 v[60:63], v[80:83], v[104:107], v[60:63]\n"
"s_waitcnt vmcnt(30)\n"
"v_mfma_f32_16x16x32_bf16 v[24:27], v[148:151], v[108:111], v[24:27]\n"
"v_mfma_f32_16x16x32_bf16 v[28:31], v[152:155], v[108:111], v[28:31]\n"
"v_mfma_f32_16x16x32_bf16 v[48:51], v[156:159], v[108:111], v[48:51]\n"
"v_mfma_f32_16x16x32_bf16 v[52:55], v[160:163], v[108:111], v[52:55]\n"
"v_mfma_f32_16x16x32_bf16 v[56:59], v[164:167], v[108:111], v[56:59]\n"
"buffer_load_dwordx4 v[100:103], v35, s[4:7], s97 offen sc1\n"
"v_mfma_f32_16x16x32_bf16 v[60:63], v[168:171], v[108:111], v[60:63]\n"
"s_waitcnt vmcnt(30)\n"
"v_mfma_f32_16x16x32_bf16 v[24:27], v[172:175], v[112:115], v[24:27]\n"
"v_mfma_f32_16x16x32_bf16 v[28:31], v[176:179], v[112:115], v[28:31]\n"
"v_mfma_f32_16x16x32_bf16 v[48:51], v[180:183], v[112:115], v[48:51]\n"
"v_mfma_f32_16x16x32_bf16 v[52:55], v[184:187], v[112:115], v[52:55]\n"
"v_mfma_f32_16x16x32_bf16 v[56:59], v[188:191], v[112:115], v[56:59]\n"
"buffer_load_dwordx4 v[104:107], v35, s[4:7], s97 offen offset:1024 sc1\n"
"v_mfma_f32_16x16x32_bf16 v[60:63], v[192:195], v[112:115], v[60:63]\n"
"s_waitcnt vmcnt(29)\n"
"; @A G1 0.1\n"
"v_mfma_f32_16x16x32_bf16 v[108:111], a[0:3], v[116:119], 0\n"
"v_mfma_f32_16x16x32_bf16 v[112:115], a[4:7], v[116:119], 0\n"
"v_mfma_f32_16x16x32_bf16 v[196:199], a[8:11], v[116:119], 0\n"
"v_mfma_f32_16x16x32_bf16 v[200:203], a[12:15], v[116:119], 0\n"
"ds_write_b128 v3, v[24:27]\n"
"v_mfma_f32_16x16x32_bf16 v[24:27], a[16:19], v[116:119], 0\n"
"s_add_u32 s99, s55, 0x40000\n"
"v_mfma_f32_16x16x32_bf16 v[204:207], a[20:23], v[116:119], 0\n"
"buffer_load_dwordx4 v[116:119], v32, s[4:7], s99 offen sc1\n"
"v_mfma_f32_16x16x32_bf16 v[108:111], a[24:27], v[120:123], v[108:111]\n"
"ds_write_b128 v3, v[28:31] offset:1024\n"
"v_mfma_f32_16x16x32_bf16 v[112:115], a[28:31], v[120:123], v[112:115]\n"
"v_mfma_f32_16x16x32_bf16 v[196:199], a[32:35], v[120:123], v[196:199]\n"
"v_mfma_f32_16x16x32_bf16 v[200:203], a[36:39], v[120:123], v[200:203]\n"
"ds_write_b128 v3, v[48:51] offset:2048\n"
"v_mfma_f32_16x16x32_bf16 v[24:27], a[40:43], v[120:123], v[24:27]\n"
"buffer_load_dwordx4 v[28:31], v32, s[4:7], s99 offen offset:1024 sc1\n"
"v_mfma_f32_16x16x32_bf16 v[204:207], a[44:47], v[120:123], v[204:207]\n"
"s_waitcnt vmcnt(29)\n"
"v_mfma_f32_16x16x32_bf16 v[108:111], a[48:51], v[124:127], v[108:111]\n"
"ds_write_b128 v3, v[52:55] offset:3072\n"
"v_mfma_f32_16x16x32_bf16 v[112:115], a[52:55], v[124:127], v[112:115]\n"
"v_mfma_f32_16x16x32_bf16 v[196:199], a[56:59], v[124:127], v[196:199]\n"
"v_mfma_f32_16x16x32_bf16 v[200:203], a[60:63], v[124:127], v[200:203]\n"
"ds_write_b128 v3, v[56:59] offset:4096\n"
"v_mfma_f32_16x16x32_bf16 v[24:27], a[64:67], v[124:127], v[24:27]\n"
"buffer_load_dwordx4 v[48:51], v33, s[4:7], s99 offen sc1\n"
"v_mfma_f32_16x16x32_bf16 v[204:207], a[68:71], v[124:127], v[204:207]\n"
"v_mfma_f32_16x16x32_bf16 v[108:111], a[72:75], v[128:131], v[108:111]\n"
"ds_write_b128 v3, v[60:63] offset:5120\n"
"v_mfma_f32_16x16x32_bf16 v[112:115], a[76:79], v[128:131], v[112:115]\n"
"s_waitcnt vmcnt(14)\n"
"v_add_f32_e32 v254, v254, v252\n"
"v_mfma_f32_16x16x32_bf16 v[196:199], a[80:83], v[128:131], v[196:199]\n"
"v_add_f32_e32 v255, v255, v253\n"
"v_add_f32_e32 v250, v250, v248\n"
"v_mfma_f32_16x16x32_bf16 v[200:203], a[84:87], v[128:131], v[200:203]\n"
"v_add_f32_e32 v251, v251, v249\n"
"v_add_f32_e32 v254, v254, v250\n"
"v_mfma_f32_16x16x32_bf16 v[24:27], a[88:91], v[128:131], v[24:27]\n"
"buffer_load_dwordx4 v[52:55], v33, s[4:7], s99 offen offset:1024 sc1\n"
"v_mfma_f32_16x16x32_bf16 v[204:207], a[92:95], v[128:131], v[204:207]\n"
"v_add_f32_e32 v255, v255, v251\n"
"v_mfma_f32_16x16x32_bf16 v[108:111], a[96:99], v[132:135], v[108:111]\n"
"s_nop 0\n"
"v_permlane16_swap_b32_e32 v254, v255\n"
"v_add_f32_e32 v254, v254, v255\n"
"v_mfma_f32_16x16x32_bf16 v[112:115], a[100:103], v[132:135], v[112:115]\n"
"v_mov_b32_e32 v255, v254\n"
"v_mfma_f32_16x16x32_bf16 v[196:199], a[104:107], v[132:135], v[196:199]\n"
"s_nop 0\n"
"v_permlane32_swap_b32_e32 v254, v255\n"
"v_add_f32_e32 v254, v254, v255\n"
"v_mfma_f32_16x16x32_bf16 v[200:203], a[108:111], v[132:135], v[200:203]\n"
"v_mul_f32_e32 v254, 0x3a800000, v254\n"
"v_mov_b32_e32 v255, v254\n"
"v_mfma_f32_16x16x32_bf16 v[24:27], a[112:115], v[132:135], v[24:27]\n"
"buffer_load_dwordx4 v[56:59], v34, s[4:7], s99 offen sc1\n"
"v_mfma_f32_16x16x32_bf16 v[204:207], a[116:119], v[132:135], v[204:207]\n"
"s_waitcnt vmcnt(8)\n"
"s_waitcnt lgkmcnt(0)\n"
"s_barrier\n"
"v_mfma_f32_16x16x32_bf16 v[108:111], a[120:123], v[136:139], v[108:111]\n"
"v_permlane16_swap_b32_e32 v254, v255\n"
"v_cvt_pk_bf16_f32 v253, -v254, 0\n"
"v_mfma_f32_16x16x32_bf16 v[112:115], a[124:127], v[136:139], v[112:115]\n"
"v_fma_f32 v255, -v254, v254, v255\n"
"v_lshlrev_b32_e32 v253, 16, v253\n"
"ds_read_b64_tr_b16 v[60:61], v40\n"
"v_mfma_f32_16x16x32_bf16 v[196:199], v[68:71], v[136:139], v[196:199]\n"
"ds_read_b128 v[120:123], v4\n"
"ds_read_b128 v[124:127], v9\n"
"ds_read_b64_tr_b16 v[62:63], v40 offset:1024\n"
"v_mfma_f32_16x16x32_bf16 v[200:203], v[72:75], v[136:139], v[200:203]\n"
"ds_read_b128 v[128:131], v4 offset:1024\n"
"ds_read_b128 v[132:135], v9 offset:64\n"
"v_mfma_f32_16x16x32_bf16 v[24:27], v[76:79], v[136:139], v[24:27]\n"
"buffer_load_dwordx4 v[208:211], v34, s[4:7], s99 offen offset:1024 sc1\n"
"v_mfma_f32_16x16x32_bf16 v[204:207], v[80:83], v[136:139], v[204:207]\n"
"ds_read_b128 v[136:139], v4 offset:2048\n"
"v_mfma_f32_16x16x32_bf16 v[108:111], v[148:151], v[140:143], v[108:111]\n"
"ds_read_b128 v[212:215], v9 offset:128\n"
"v_max_f32_e32 v255, 0, v255\n"
"v_mfma_f32_16x16x32_bf16 v[112:115], v[152:155], v[140:143], v[112:115]\n"
"v_cvt_pk_bf16_f32 v255, v255, 0\n"
"v_lshlrev_b32_e32 v255, 16, v255\n"
"s_waitcnt lgkmcnt(4)\n"
"buffer_store_dwordx4 v[60:63], v0, s[4:7], s24 offen sc1\n"
"v_mfma_f32_16x16x32_bf16 v[196:199], v[156:159], v[140:143], v[196:199]\n"
"v_add_f32_e32 v255, 0x3727c5ac, v255\n"
"v_cvt_pk_bf16_f32 v255, v255, 0\n"
"v_mfma_f32_16x16x32_bf16 v[200:203], v[160:163], v[140:143], v[200:203]\n"
"v_lshlrev_b32_e32 v255, 16, v255\n"
"v_mfma_f32_16x16x32_bf16 v[24:27], v[164:167], v[140:143], v[24:27]\n"
"buffer_load_dwordx4 v[60:63], v35, s[4:7], s99 offen sc1\n"
"v_mfma_f32_16x16x32_bf16 v[204:207], v[168:171], v[140:143], v[204:207]\n"
"s_waitcnt lgkmcnt(0)\n"
"v_fmac_f32_e32 v120, v253, v124\n"
"v_mfma_f32_16x16x32_bf16 v[108:111], v[172:175], v[144:147], v[108:111]\n"
"v_fmac_f32_e32 v121, v253, v125\n"
"v_fmac_f32_e32 v122, v253, v126\n"
"v_mfma_f32_16x16x32_bf16 v[112:115], v[176:179], v[144:147], v[112:115]\n"
"v_fmac_f32_e32 v123, v253, v127\n"
"v_fmac_f32_e32 v128, v253, v132\n"
"v_mfma_f32_16x16x32_bf16 v[196:199], v[180:183], v[144:147], v[196:199]\n"
"v_fmac_f32_e32 v129, v253, v133\n"
"v_fmac_f32_e32 v130, v253, v134\n"
"v_mfma_f32_16x16x32_bf16 v[200:203], v[184:187], v[144:147], v[200:203]\n"
"v_fmac_f32_e32 v131, v253, v135\n"
"v_fmac_f32_e32 v136, v253, v212\n"
"v_mfma_f32_16x16x32_bf16 v[24:27], v[188:191], v[144:147], v[24:27]\n"
"buffer_load_dwordx4 v[124:127], v35, s[4:7], s99 offen offset:1024 sc1\n"
"ds_read_b64_tr_b16 v[132:133], v40 offset:4096\n"
"v_mfma_f32_16x16x32_bf16 v[204:207], v[192:195], v[144:147], v[204:207]\n"
"v_fmac_f32_e32 v137, v253, v213\n"
"ds_read_b64_tr_b16 v[134:135], v40 offset:5120\n"
"; @A G1 0.2\n"
"v_mfma_f32_16x16x32_bf16 v[140:143], a[0:3], v[64:67], 0\n"
"v_fmac_f32_e32 v138, v253, v214\n"
"v_fmac_f32_e32 v139, v253, v215\n"
"v_mfma_f32_16x16x32_bf16 v[144:147], a[4:7], v[64:67], 0\n"
"ds_read_b128 v[212:215], v4 offset:3072\n"
"ds_read_b128 v[216:219], v9 offset:192\n"
"v_mfma_f32_16x16x32_bf16 v[220:223], a[8:11], v[64:67], 0\n"
"ds_read_b128 v[224:227], v4 offset:4096\n"
"ds_read_b128 v[228:231], v9 offset:256\n"
"v_mfma_f32_16x16x32_bf16 v[232:235], a[12:15], v[64:67], 0\n"
"ds_write_b128 v3, v[108:111] offset:6144\n"
"v_mfma_f32_16x16x32_bf16 v[108:111], a[16:19], v[64:67], 0\n"
"s_add_u32 s99, s52, 0x48000\n"
"ds_read_b128 v[236:239], v4 offset:5120\n"
"s_waitcnt lgkmcnt(6)\n"
"s_add_u32 s98, s24, 0x1000\n"
"v_mfma_f32_16x16x32_bf16 v[240:243], a[20:23], v[64:67], 0\n"
"buffer_load_dwordx4 v[64:67], v32, s[4:7], s99 offen sc1\n"
"buffer_store_dwordx4 v[132:135], v0, s[4:7], s98 offen sc1\n"
"v_mfma_f32_16x16x32_bf16 v[140:143], a[24:27], v[84:87], v[140:143]\n"
"ds_write_b128 v3, v[112:115] offset:7168\n"
"v_mfma_f32_16x16x32_bf16 v[144:147], a[28:31], v[84:87], v[144:147]\n"
"ds_read_b128 v[112:115], v9 offset:320\n"
"v_mfma_f32_16x16x32_bf16 v[220:223], a[32:35], v[84:87], v[220:223]\n"
"v_sqrt_f32_e32 v255, v255\n"
"v_mfma_f32_16x16x32_bf16 v[232:235], a[36:39], v[84:87], v[232:235]\n"
"ds_write_b128 v3, v[196:199] offset:8192\n"
"v_mfma_f32_16x16x32_bf16 v[108:111], a[40:43], v[84:87], v[108:111]\n"
"buffer_load_dwordx4 v[132:135], v32, s[4:7], s99 offen offset:1024 sc1\n"
"v_mfma_f32_16x16x32_bf16 v[240:243], a[44:47], v[84:87], v[240:243]\n"
"v_cvt_pk_bf16_f32 v255, v255, 0\n"
"v_mfma_f32_16x16x32_bf16 v[140:143], a[48:51], v[88:91], v[140:143]\n"
"ds_write_b128 v3, v[200:203] offset:9216\n"
"v_mfma_f32_16x16x32_bf16 v[144:147], a[52:55], v[88:91], v[144:147]\n"
"s_waitcnt lgkmcnt(2)\n"
"v_fmac_f32_e32 v212, v253, v216\n"
"v_mfma_f32_16x16x32_bf16 v[220:223], a[56:59], v[88:91], v[220:223]\n"
"v_fmac_f32_e32 v213, v253, v217\n"
"v_fmac_f32_e32 v214, v253, v218\n"
"v_mfma_f32_16x16x32_bf16 v[232:235], a[60:63], v[88:91], v[232:235]\n"
"ds_write_b128 v3, v[24:27] offset:10240\n"
"v_mfma_f32_16x16x32_bf16 v[108:111], a[64:67], v[88:91], v[108:111]\n"
"buffer_load_dwordx4 v[24:27], v33, s[4:7], s99 offen sc1\n"
"v_mfma_f32_16x16x32_bf16 v[240:243], a[68:71], v[88:91], v[240:243]\n"
"v_fmac_f32_e32 v215, v253, v219\n"
"v_fmac_f32_e32 v224, v253, v228\n"
"v_mfma_f32_16x16x32_bf16 v[140:143], a[72:75], v[92:95], v[140:143]\n"
"ds_write_b128 v3, v[204:207] offset:11264\n"
"v_mfma_f32_16x16x32_bf16 v[144:147], a[76:79], v[92:95], v[144:147]\n"
"v_fmac_f32_e32 v225, v253, v229\n"
"v_fmac_f32_e32 v226, v253, v230\n"
"v_mfma_f32_16x16x32_bf16 v[220:223], a[80:83], v[92:95], v[220:223]\n"
"v_fmac_f32_e32 v227, v253, v231\n"
"v_fmac_f32_e32 v236, v253, v112\n"
"v_mfma_f32_16x16x32_bf16 v[232:235], a[84:87], v[92:95], v[232:235]\n"
"v_fmac_f32_e32 v237, v253, v113\n"
"v_fmac_f32_e32 v238, v253, v114\n"
"v_mfma_f32_16x16x32_bf16 v[108:111], a[88:91], v[92:95], v[108:111]\n"
"buffer_load_dwordx4 v[84:87], v33, s[4:7], s99 offen offset:1024 sc1\n"
"ds_read_b64_tr_b16 v[88:89], v40 offset:8192\n"
"v_mfma_f32_16x16x32_bf16 v[240:243], a[92:95], v[92:95], v[240:243]\n"
"s_waitcnt vmcnt(16)\n"
"v_fmac_f32_e32 v239, v253, v115\n"
"ds_read_b64_tr_b16 v[90:91], v40 offset:9216\n"
"v_mfma_f32_16x16x32_bf16 v[140:143], a[96:99], v[96:99], v[140:143]\n"
"v_lshlrev_b32_e32 v255, 16, v255\n"
"v_mfma_f32_16x16x32_bf16 v[144:147], a[100:103], v[96:99], v[144:147]\n"
"v_rcp_f32_e32 v253, v255\n"
"v_mfma_f32_16x16x32_bf16 v[220:223], a[104:107], v[96:99], v[220:223]\n"
"v_mul_f32_e32 v255, 0x3e8293ee, v253\n"
"v_mfma_f32_16x16x32_bf16 v[232:235], a[108:111], v[96:99], v[232:235]\n"
"v_mfma_f32_16x16x32_bf16 v[108:111], a[112:115], v[96:99], v[108:111]\n"
"buffer_load_dwordx4 v[92:95], v34, s[4:7], s99 offen sc1\n"
"s_waitcnt lgkmcnt(0)\n"
"buffer_store_dwordx4 v[88:91], v0, s[4:7], s25 offen sc1\n"
"v_mfma_f32_16x16x32_bf16 v[240:243], a[116:119], v[96:99], v[240:243]\n"
"v_mfma_f32_16x16x32_bf16 v[140:143], a[120:123], v[44:47], v[140:143]\n"
"v_mfma_f32_16x16x32_bf16 v[144:147], a[124:127], v[44:47], v[144:147]\n"
"v_mfma_f32_16x16x32_bf16 v[220:223], v[68:71], v[44:47], v[220:223]\n"
"v_mfma_f32_16x16x32_bf16 v[232:235], v[72:75], v[44:47], v[232:235]\n"
"v_mfma_f32_16x16x32_bf16 v[108:111], v[76:79], v[44:47], v[108:111]\n"
"buffer_load_dwordx4 v[88:91], v34, s[4:7], s99 offen offset:1024 sc1\n"
"v_mfma_f32_16x16x32_bf16 v[240:243], v[80:83], v[44:47], v[240:243]\n"
"s_waitcnt vmcnt(17)\n"
"v_mfma_f32_16x16x32_bf16 v[140:143], v[148:151], v[100:103], v[140:143]\n"
"v_mfma_f32_16x16x32_bf16 v[144:147], v[152:155], v[100:103], v[144:147]\n"
"v_mfma_f32_16x16x32_bf16 v[220:223], v[156:159], v[100:103], v[220:223]\n"
"v_mfma_f32_16x16x32_bf16 v[232:235], v[160:163], v[100:103], v[232:235]\n"
"v_mfma_f32_16x16x32_bf16 v[108:111], v[164:167], v[100:103], v[108:111]\n"
"buffer_load_dwordx4 v[44:47], v35, s[4:7], s99 offen sc1\n"
"v_mfma_f32_16x16x32_bf16 v[240:243], v[168:171], v[100:103], v[240:243]\n"
"v_mfma_f32_16x16x32_bf16 v[140:143], v[172:175], v[104:107], v[140:143]\n"
"v_mfma_f32_16x16x32_bf16 v[144:147], v[176:179], v[104:107], v[144:147]\n"
"v_mfma_f32_16x16x32_bf16 v[220:223], v[180:183], v[104:107], v[220:223]\n"
"v_mfma_f32_16x16x32_bf16 v[232:235], v[184:187], v[104:107], v[232:235]\n"
"v_mfma_f32_16x16x32_bf16 v[108:111], v[188:191], v[104:107], v[108:111]\n"
"buffer_load_dwordx4 v[96:99], v35, s[4:7], s99 offen offset:1024 sc1\n"
"ds_read_b64_tr_b16 v[100:101], v40 offset:12288\n"
"v_mfma_f32_16x16x32_bf16 v[240:243], v[192:195], v[104:107], v[240:243]\n"
"s_waitcnt vmcnt(17)\n"
"ds_read_b64_tr_b16 v[102:103], v40 offset:13312\n"
"; @A G1 0.3\n"
"v_mfma_f32_16x16x32_bf16 v[120:123], a[0:3], v[116:119], v[120:123]\n"
"v_mfma_f32_16x16x32_bf16 v[128:131], a[4:7], v[116:119], v[128:131]\n"
"v_mfma_f32_16x16x32_bf16 v[136:139], a[8:11], v[116:119], v[136:139]\n"
"v_mfma_f32_16x16x32_bf16 v[212:215], a[12:15], v[116:119], v[212:215]\n"
"ds_write_b128 v3, v[140:143] offset:12288\n"
"v_mfma_f32_16x16x32_bf16 v[224:227], a[16:19], v[116:119], v[224:227]\n"
"s_add_u32 s99, s53, 0x48000\n"
"s_waitcnt lgkmcnt(1)\n"
"s_add_u32 s98, s25, 0x1000\n"
"buffer_store_dwordx4 v[100:103], v0, s[4:7], s98 offen sc1\n"
"v_mfma_f32_16x16x32_bf16 v[236:239], a[20:23], v[116:119], v[236:239]\n"
"buffer_load_dwordx4 v[100:103], v32, s[4:7], s99 offen sc1\n"
"v_mfma_f32_16x16x32_bf16 v[120:123], a[24:27], v[28:31], v[120:123]\n"
"ds_write_b128 v3, v[144:147] offset:13312\n"
"v_mfma_f32_16x16x32_bf16 v[128:131], a[28:31], v[28:31], v[128:131]\n"
"v_mfma_f32_16x16x32_bf16 v[136:139], a[32:35], v[28:31], v[136:139]\n"
"v_mfma_f32_16x16x32_bf16 v[212:215], a[36:39], v[28:31], v[212:215]\n"
"ds_write_b128 v3, v[220:223] offset:14336\n"
"v_mfma_f32_16x16x32_bf16 v[224:227], a[40:43], v[28:31], v[224:227]\n"
"buffer_load_dwordx4 v[104:107], v32, s[4:7], s99 offen offset:1024 sc1\n"
"v_mfma_f32_16x16x32_bf16 v[236:239], a[44:47], v[28:31], v[236:239]\n"
"s_waitcnt vmcnt(18)\n"
"v_mfma_f32_16x16x32_bf16 v[120:123], a[48:51], v[48:51], v[120:123]\n"
"ds_write_b128 v3, v[232:235] offset:15360\n"
"v_mfma_f32_16x16x32_bf16 v[128:131], a[52:55], v[48:51], v[128:131]\n"
"v_mfma_f32_16x16x32_bf16 v[136:139], a[56:59], v[48:51], v[136:139]\n"
"v_mfma_f32_16x16x32_bf16 v[212:215], a[60:63], v[48:51], v[212:215]\n"
"ds_write_b128 v3, v[108:111] offset:16384\n"
"v_mfma_f32_16x16x32_bf16 v[224:227], a[64:67], v[48:51], v[224:227]\n"
"buffer_load_dwordx4 v[28:31], v33, s[4:7], s99 offen sc1\n"
"v_mfma_f32_16x16x32_bf16 v[236:239], a[68:71], v[48:51], v[236:239]\n"
"v_mfma_f32_16x16x32_bf16 v[120:123], a[72:75], v[52:55], v[120:123]\n"
"ds_write_b128 v3, v[240:243] offset:17408\n"
"v_mfma_f32_16x16x32_bf16 v[128:131], a[76:79], v[52:55], v[128:131]\n"
"v_mfma_f32_16x16x32_bf16 v[136:139], a[80:83], v[52:55], v[136:139]\n"
"v_mfma_f32_16x16x32_bf16 v[212:215], a[84:87], v[52:55], v[212:215]\n"
"s_waitcnt lgkmcnt(0)\n"
"s_barrier\n"
"v_mfma_f32_16x16x32_bf16 v[224:227], a[88:91], v[52:55], v[224:227]\n"
"buffer_load_dwordx4 v[48:51], v33, s[4:7], s99 offen offset:1024 sc1\n"
"v_mfma_f32_16x16x32_bf16 v[236:239], a[92:95], v[52:55], v[236:239]\n"
"s_waitcnt vmcnt(18)\n"
"ds_read_b128 v[52:55], v5 offset:2048\n"
"v_mfma_f32_16x16x32_bf16 v[120:123], a[96:99], v[56:59], v[120:123]\n"
"ds_read_b128 v[108:111], v6 offset:2048\n"
"ds_read_b128 v[112:115], v5 offset:3072\n"
"v_mfma_f32_16x16x32_bf16 v[128:131], a[100:103], v[56:59], v[128:131]\n"
"ds_read_b128 v[116:119], v6 offset:3072\n"
"v_mfma_f32_16x16x32_bf16 v[136:139], a[104:107], v[56:59], v[136:139]\n"
"v_mfma_f32_16x16x32_bf16 v[212:215], a[108:111], v[56:59], v[212:215]\n"
"v_mfma_f32_16x16x32_bf16 v[224:227], a[112:115], v[56:59], v[224:227]\n"
"buffer_load_dwordx4 v[140:143], v34, s[4:7], s99 offen sc1\n"
"v_mfma_f32_16x16x32_bf16 v[236:239], a[116:119], v[56:59], v[236:239]\n"
"ds_read_b128 v[56:59], v5 offset:4096\n"
"ds_read_b128 v[144:147], v6 offset:4096\n"
"v_mfma_f32_16x16x32_bf16 v[120:123], a[120:123], v[208:211], v[120:123]\n"
"ds_read_b128 v[196:199], v5 offset:5120\n"
"ds_read_b128 v[200:203], v6 offset:5120\n"
"v_mfma_f32_16x16x32_bf16 v[128:131], a[124:127], v[208:211], v[128:131]\n"
"s_waitcnt lgkmcnt(4)\n"
"v_add_f32_e32 v52, v52, v108\n"
"v_mfma_f32_16x16x32_bf16 v[136:139], v[68:71], v[208:211], v[136:139]\n"
"v_add_f32_e32 v53, v53, v109\n"
"v_add_f32_e32 v54, v54, v110\n"
"v_mfma_f32_16x16x32_bf16 v[212:215], v[72:75], v[208:211], v[212:215]\n"
"ds_read_b128 v[204:207], v5\n"
"ds_read_b128 v[216:219], v6\n"
"v_mfma_f32_16x16x32_bf16 v[224:227], v[76:79], v[208:211], v[224:227]\n"
"buffer_load_dwordx4 v[220:223], v34, s[4:7], s99 offen offset:1024 sc1\n"
"v_mfma_f32_16x16x32_bf16 v[236:239], v[80:83], v[208:211], v[236:239]\n"
"s_waitcnt vmcnt(17)\n"
"ds_read_b128 v[208:211], v5 offset:1024\n"
"v_mfma_f32_16x16x32_bf16 v[120:123], v[148:151], v[60:63], v[120:123]\n"
"ds_read_b128 v[228:231], v6 offset:1024\n"
"v_add_f32_e32 v55, v55, v111\n"
"v_mfma_f32_16x16x32_bf16 v[128:131], v[152:155], v[60:63], v[128:131]\n"
"v_add_f32_e32 v112, v112, v116\n"
"v_add_f32_e32 v113, v113, v117\n"
"v_mfma_f32_16x16x32_bf16 v[136:139], v[156:159], v[60:63], v[136:139]\n"
"v_add_f32_e32 v114, v114, v118\n"
"v_add_f32_e32 v115, v115, v119\n"
"v_mfma_f32_16x16x32_bf16 v[212:215], v[160:163], v[60:63], v[212:215]\n"
"s_waitcnt lgkmcnt(4)\n"
"v_add_f32_e32 v56, v56, v144\n"
"v_mfma_f32_16x16x32_bf16 v[224:227], v[164:167], v[60:63], v[224:227]\n"
"buffer_load_dwordx4 v[108:111], v35, s[4:7], s99 offen sc1\n"
"v_mfma_f32_16x16x32_bf16 v[236:239], v[168:171], v[60:63], v[236:239]\n"
"v_add_f32_e32 v57, v57, v145\n"
"v_add_f32_e32 v58, v58, v146\n"
"v_mfma_f32_16x16x32_bf16 v[120:123], v[172:175], v[124:127], v[120:123]\n"
"v_add_f32_e32 v59, v59, v147\n"
"v_add_f32_e32 v196, v196, v200\n"
"v_mfma_f32_16x16x32_bf16 v[128:131], v[176:179], v[124:127], v[128:131]\n"
"v_add_f32_e32 v197, v197, v201\n"
"v_add_f32_e32 v198, v198, v202\n"
"v_mfma_f32_16x16x32_bf16 v[136:139], v[180:183], v[124:127], v[136:139]\n"
"v_add_f32_e32 v199, v199, v203\n"
"s_waitcnt lgkmcnt(0)\n"
"v_mfma_f32_16x16x32_bf16 v[212:215], v[184:187], v[124:127], v[212:215]\n"
"v_add_f32_e32 v204, v204, v216\n"
"v_add_f32_e32 v205, v205, v217\n"
"v_mfma_f32_16x16x32_bf16 v[224:227], v[188:191], v[124:127], v[224:227]\n"
"buffer_load_dwordx4 v[60:63], v35, s[4:7], s99 offen offset:1024 sc1\n"
"v_mfma_f32_16x16x32_bf16 v[236:239], v[192:195], v[124:127], v[236:239]\n"
"v_add_f32_e32 v206, v206, v218\n"
"v_add_f32_e32 v207, v207, v219\n"
"v_add_f32_e32 v208, v208, v228\n"
"v_add_f32_e32 v209, v209, v229\n"
"v_add_f32_e32 v210, v210, v230\n"
"v_add_f32_e32 v211, v211, v231\n"
"s_waitcnt vmcnt(16)\n"
"; @A G1 1.0\n"
"v_mfma_f32_16x16x32_bf16 v[116:119], a[0:3], v[64:67], 0\n"
"s_add_u32 s99, s62, 0x4800\n"
"v_mfma_f32_16x16x32_bf16 v[124:127], a[4:7], v[64:67], 0\n"
"buffer_load_dwordx2 v[250:251], v10, s[4:7], s99 offen sc1\n"
"v_mfma_f32_16x16x32_bf16 v[144:147], a[8:11], v[64:67], 0\n"
"buffer_load_dwordx2 v[248:249], v10, s[4:7], s99 offen offset:128 sc1\n"
"v_mfma_f32_16x16x32_bf16 v[200:203], a[12:15], v[64:67], 0\n"
"buffer_load_dwordx2 v[246:247], v10, s[4:7], s99 offen offset:256 sc1\n"
"v_mfma_f32_16x16x32_bf16 v[216:219], a[16:19], v[64:67], 0\n"
"s_add_u32 s98, s54, 0x48000\n"
"v_add_f32_e32 v136, v136, v52\n"
"v_mfma_f32_16x16x32_bf16 v[228:231], a[20:23], v[64:67], 0\n"
"buffer_load_dwordx4 v[64:67], v32, s[4:7], s98 offen sc1\n"
"v_mfma_f32_16x16x32_bf16 v[116:119], a[24:27], v[132:135], v[116:119]\n"
"buffer_load_dwordx2 v[244:245], v10, s[4:7], s99 offen offset:384 sc1\n"
"v_mfma_f32_16x16x32_bf16 v[124:127], a[28:31], v[132:135], v[124:127]\n"
"v_add_f32_e32 v137, v137, v53\n"
"v_add_f32_e32 v138, v138, v54\n"
"v_mfma_f32_16x16x32_bf16 v[144:147], a[32:35], v[132:135], v[144:147]\n"
"v_add_f32_e32 v139, v139, v55\n"
"v_add_f32_e32 v212, v212, v112\n"
"v_mfma_f32_16x16x32_bf16 v[200:203], a[36:39], v[132:135], v[200:203]\n"
"v_add_f32_e32 v213, v213, v113\n"
"v_add_f32_e32 v214, v214, v114\n"
"v_mfma_f32_16x16x32_bf16 v[216:219], a[40:43], v[132:135], v[216:219]\n"
"buffer_load_dwordx4 v[52:55], v32, s[4:7], s98 offen offset:1024 sc1\n"
"v_mfma_f32_16x16x32_bf16 v[228:231], a[44:47], v[132:135], v[228:231]\n"
"s_waitcnt vmcnt(20)\n"
"s_waitcnt vmcnt(14)\n"
"v_mfma_f32_16x16x32_bf16 v[116:119], a[48:51], v[24:27], v[116:119]\n"
"buffer_store_dwordx2 v[18:19], v11, s[4:7], s68 offen offset:96 sc1\n"
"v_mfma_f32_16x16x32_bf16 v[124:127], a[52:55], v[24:27], v[124:127]\n"
"v_add_f32_e32 v215, v215, v115\n"
"v_mul_f32_e32 v136, v136, v253\n"
"v_mfma_f32_16x16x32_bf16 v[144:147], a[56:59], v[24:27], v[144:147]\n"
"v_mul_f32_e32 v137, v137, v253\n"
"v_mul_f32_e32 v138, v138, v253\n"
"v_mfma_f32_16x16x32_bf16 v[200:203], a[60:63], v[24:27], v[200:203]\n"
"v_mul_f32_e32 v139, v139, v253\n"
"v_mul_f32_e32 v212, v212, v253\n"
"v_mfma_f32_16x16x32_bf16 v[216:219], a[64:67], v[24:27], v[216:219]\n"
"buffer_load_dwordx4 v[112:115], v33, s[4:7], s98 offen sc1\n"
"v_mfma_f32_16x16x32_bf16 v[228:231], a[68:71], v[24:27], v[228:231]\n"
"v_mul_f32_e32 v213, v213, v253\n"
"v_mul_f32_e32 v214, v214, v253\n"
"v_mfma_f32_16x16x32_bf16 v[116:119], a[72:75], v[84:87], v[116:119]\n"
"v_mul_f32_e32 v215, v215, v253\n"
"v_cvt_pk_bf16_f32 v24, v136, v137\n"
"v_mfma_f32_16x16x32_bf16 v[124:127], a[76:79], v[84:87], v[124:127]\n"
"v_cvt_pk_bf16_f32 v25, v138, v139\n"
"v_cvt_pk_bf16_f32 v26, v212, v213\n"
"v_mfma_f32_16x16x32_bf16 v[144:147], a[80:83], v[84:87], v[144:147]\n"
"v_cvt_pk_bf16_f32 v27, v214, v215\n"
"v_mfma_f32_16x16x32_bf16 v[200:203], a[84:87], v[84:87], v[200:203]\n"
"ds_write_b128 v7, v[24:27] offset:24576\n"
"v_mfma_f32_16x16x32_bf16 v[216:219], a[88:91], v[84:87], v[216:219]\n"
"buffer_load_dwordx4 v[132:135], v33, s[4:7], s98 offen offset:1024 sc1\n"
"v_mfma_f32_16x16x32_bf16 v[228:231], a[92:95], v[84:87], v[228:231]\n"
"v_add_f32_e32 v224, v224, v56\n"
"v_mfma_f32_16x16x32_bf16 v[116:119], a[96:99], v[92:95], v[116:119]\n"
"v_add_f32_e32 v225, v225, v57\n"
"v_add_f32_e32 v226, v226, v58\n"
"v_mfma_f32_16x16x32_bf16 v[124:127], a[100:103], v[92:95], v[124:127]\n"
"v_add_f32_e32 v227, v227, v59\n"
"v_add_f32_e32 v236, v236, v196\n"
"v_mfma_f32_16x16x32_bf16 v[144:147], a[104:107], v[92:95], v[144:147]\n"
"v_add_f32_e32 v237, v237, v197\n"
"v_add_f32_e32 v238, v238, v198\n"
"v_mfma_f32_16x16x32_bf16 v[200:203], a[108:111], v[92:95], v[200:203]\n"
"v_add_f32_e32 v239, v239, v199\n"
"v_mul_f32_e32 v224, v224, v253\n"
"v_mfma_f32_16x16x32_bf16 v[216:219], a[112:115], v[92:95], v[216:219]\n"
"buffer_load_dwordx4 v[56:59], v34, s[4:7], s98 offen sc1\n"
"v_mfma_f32_16x16x32_bf16 v[228:231], a[116:119], v[92:95], v[228:231]\n"
"v_mul_f32_e32 v225, v225, v253\n"
"v_mul_f32_e32 v226, v226, v253\n"
"v_mfma_f32_16x16x32_bf16 v[116:119], a[120:123], v[88:91], v[116:119]\n"
"v_mul_f32_e32 v227, v227, v253\n"
"v_mul_f32_e32 v236, v236, v253\n"
"v_mfma_f32_16x16x32_bf16 v[124:127], a[124:127], v[88:91], v[124:127]\n"
"v_mul_f32_e32 v237, v237, v253\n"
"v_mul_f32_e32 v238, v238, v253\n"
"v_mfma_f32_16x16x32_bf16 v[144:147], v[68:71], v[88:91], v[144:147]\n"
"v_mul_f32_e32 v239, v239, v253\n"
"v_cvt_pk_bf16_f32 v84, v224, v225\n"
"v_mfma_f32_16x16x32_bf16 v[200:203], v[72:75], v[88:91], v[200:203]\n"
"v_cvt_pk_bf16_f32 v85, v226, v227\n"
"v_cvt_pk_bf16_f32 v86, v236, v237\n"
"v_mfma_f32_16x16x32_bf16 v[216:219], v[76:79], v[88:91], v[216:219]\n"
"buffer_load_dwordx4 v[92:95], v34, s[4:7], s98 offen offset:1024 sc1\n"
"v_mfma_f32_16x16x32_bf16 v[228:231], v[80:83], v[88:91], v[228:231]\n"
"v_cvt_pk_bf16_f32 v87, v238, v239\n"
"v_mfma_f32_16x16x32_bf16 v[116:119], v[148:151], v[44:47], v[116:119]\n"
"ds_write_b128 v7, v[84:87] offset:57344\n"
"v_mfma_f32_16x16x32_bf16 v[124:127], v[152:155], v[44:47], v[124:127]\n"
"v_add_f32_e32 v120, v120, v204\n"
"v_add_f32_e32 v121, v121, v205\n"
"v_mfma_f32_16x16x32_bf16 v[144:147], v[156:159], v[44:47], v[144:147]\n"
"v_add_f32_e32 v122, v122, v206\n"
"v_add_f32_e32 v123, v123, v207\n"
"v_mfma_f32_16x16x32_bf16 v[200:203], v[160:163], v[44:47], v[200:203]\n"
"v_add_f32_e32 v128, v128, v208\n"
"v_add_f32_e32 v129, v129, v209\n"
"v_mfma_f32_16x16x32_bf16 v[216:219], v[164:167], v[44:47], v[216:219]\n"
"buffer_load_dwordx4 v[88:91], v35, s[4:7], s98 offen sc1\n"
"v_mfma_f32_16x16x32_bf16 v[228:231], v[168:171], v[44:47], v[228:231]\n"
"v_add_f32_e32 v130, v130, v210\n"
"v_add_f32_e32 v131, v131, v211\n"
"v_mfma_f32_16x16x32_bf16 v[116:119], v[172:175], v[96:99], v[116:119]\n"
"v_mul_f32_e32 v120, v120, v255\n"
"v_mul_f32_e32 v121, v121, v255\n"
"v_mfma_f32_16x16x32_bf16 v[124:127], v[176:179], v[96:99], v[124:127]\n"
"v_mul_f32_e32 v122, v122, v255\n"
"v_mul_f32_e32 v123, v123, v255\n"
"v_mfma_f32_16x16x32_bf16 v[144:147], v[180:183], v[96:99], v[144:147]\n"
"v_mul_f32_e32 v128, v128, v255\n"
"v_mul_f32_e32 v129, v129, v255\n"
"v_mfma_f32_16x16x32_bf16 v[200:203], v[184:187], v[96:99], v[200:203]\n"
"v_mul_f32_e32 v130, v130, v255\n"
"v_mul_f32_e32 v131, v131, v255\n"
"v_mfma_f32_16x16x32_bf16 v[216:219], v[188:191], v[96:99], v[216:219]\n"
"buffer_load_dwordx4 v[44:47], v35, s[4:7], s98 offen offset:1024 sc1\n"
"v_mfma_f32_16x16x32_bf16 v[228:231], v[192:195], v[96:99], v[228:231]\n"
"s_waitcnt vmcnt(19)\n"
"v_cvt_pk_bf16_f32 v96, v120, v121\n"
"; @A G1 1.1\n"
"v_mfma_f32_16x16x32_bf16 v[136:139], a[0:3], v[100:103], 0\n"
"v_cvt_pk_bf16_f32 v97, v122, v123\n"
"v_cvt_pk_bf16_f32 v98, v128, v129\n"
"v_mfma_f32_16x16x32_bf16 v[120:123], a[4:7], v[100:103], 0\n"
"v_cvt_pk_bf16_f32 v99, v130, v131\n"
"ds_read_b64_tr_b16 v[128:129], v36\n"
"ds_read_b64_tr_b16 v[130:131], v36 offset:2048\n"
"v_mfma_f32_16x16x32_bf16 v[196:199], a[8:11], v[100:103], 0\n"
"ds_write_b128 v2, v[96:99] offset:49152\n"
"v_mfma_f32_16x16x32_bf16 v[96:99], a[12:15], v[100:103], 0\n"
"ds_write_b128 v3, v[116:119]\n"
"v_mfma_f32_16x16x32_bf16 v[116:119], a[16:19], v[100:103], 0\n"
"s_add_u32 s99, s55, 0x48000\n"
"s_mov_b32 s96, 0x30003\n"
"s_mov_b32 s97, 0x30003\n"
"v_mfma_f32_16x16x32_bf16 v[204:207], a[20:23], v[100:103], 0\n"
"buffer_load_dwordx4 v[100:103], v32, s[4:7], s99 offen sc1\n"
"v_mov_b32_e32 v211, 0x3f803f80\n"
"v_mfma_f32_16x16x32_bf16 v[136:139], a[24:27], v[104:107], v[136:139]\n"
"ds_write_b128 v3, v[124:127] offset:1024\n"
"v_mfma_f32_16x16x32_bf16 v[120:123], a[28:31], v[104:107], v[120:123]\n"
"v_cndmask_b32_e64 v208, 0, v211, s[96:97]\n"
"v_cndmask_b32_e64 v209, 0, v211, s[96:97]\n"
"v_cndmask_b32_e64 v210, 0, v211, s[96:97]\n"
"v_mfma_f32_16x16x32_bf16 v[196:199], a[32:35], v[104:107], v[196:199]\n"
"v_cndmask_b32_e64 v211, 0, v211, s[96:97]\n"
"v_mfma_f32_16x16x32_bf16 v[96:99], a[36:39], v[104:107], v[96:99]\n"
"ds_write_b128 v3, v[144:147] offset:2048\n"
"v_mfma_f32_16x16x32_bf16 v[116:119], a[40:43], v[104:107], v[116:119]\n"
"buffer_load_dwordx4 v[124:127], v32, s[4:7], s99 offen offset:1024 sc1\n"
"s_waitcnt lgkmcnt(4)\n"
"v_mfma_f32_16x16x32_bf16 v[144:147], v[128:131], v[208:211], 0\n"
"buffer_store_dwordx4 v[128:131], v0, s[4:7], s20 offen sc1\n"
"v_mfma_f32_16x16x32_bf16 v[204:207], a[44:47], v[104:107], v[204:207]\n"
"s_waitcnt vmcnt(20)\n"
"v_mfma_f32_16x16x32_bf16 v[136:139], a[48:51], v[28:31], v[136:139]\n"
"ds_write_b128 v3, v[200:203] offset:3072\n"
"v_mfma_f32_16x16x32_bf16 v[120:123], a[52:55], v[28:31], v[120:123]\n"
"s_add_u32 s98, s67, 0\n"
"v_mfma_f32_16x16x32_bf16 v[196:199], a[56:59], v[28:31], v[196:199]\n"
"buffer_load_dwordx2 v[254:255], v12, s[4:7], s98 offen sc1\n"
"s_add_u32 s97, s64, 0x2000\n"
"v_mfma_f32_16x16x32_bf16 v[96:99], a[60:63], v[28:31], v[96:99]\n"
"ds_write_b128 v3, v[216:219] offset:4096\n"
"v_mfma_f32_16x16x32_bf16 v[116:119], a[64:67], v[28:31], v[116:119]\n"
"buffer_load_dwordx4 v[104:107], v33, s[4:7], s99 offen sc1\n"
"v_mfma_f32_16x16x32_bf16 v[204:207], a[68:71], v[28:31], v[204:207]\n"
"buffer_store_dwordx4 v[24:27], v7, s[4:7], s97 offen sc1\n"
"v_mfma_f32_16x16x32_bf16 v[136:139], a[72:75], v[48:51], v[136:139]\n"
"ds_write_b128 v3, v[228:231] offset:5120\n"
"v_mfma_f32_16x16x32_bf16 v[120:123], a[76:79], v[48:51], v[120:123]\n"
"v_mfma_f32_16x16x32_bf16 v[196:199], a[80:83], v[48:51], v[196:199]\n"
"v_mfma_f32_16x16x32_bf16 v[96:99], a[84:87], v[48:51], v[96:99]\n"
"s_add_u32 s97, s64, 0x6000\n"
"v_mfma_f32_16x16x32_bf16 v[116:119], a[88:91], v[48:51], v[116:119]\n"
"buffer_load_dwordx4 v[24:27], v33, s[4:7], s99 offen offset:1024 sc1\n"
"v_mfma_f32_16x16x32_bf16 v[204:207], a[92:95], v[48:51], v[204:207]\n"
"s_waitcnt vmcnt(22)\n"
"buffer_store_dwordx4 v[84:87], v7, s[4:7], s97 offen sc1\n"
"v_mfma_f32_16x16x32_bf16 v[136:139], a[96:99], v[140:143], v[136:139]\n"
"v_mfma_f32_16x16x32_bf16 v[120:123], a[100:103], v[140:143], v[120:123]\n"
"ds_read_b64_tr_b16 v[28:29], v36 offset:4096\n"
"ds_read_b64_tr_b16 v[30:31], v36 offset:6144\n"
"s_mov_b32 s96, 0x30003\n"
"v_mfma_f32_16x16x32_bf16 v[196:199], a[104:107], v[140:143], v[196:199]\n"
"s_mov_b32 s97, 0x30003\n"
"v_mov_b32_e32 v51, 0x3f803f80\n"
"v_cndmask_b32_e64 v48, 0, v51, s[96:97]\n"
"v_mfma_f32_16x16x32_bf16 v[96:99], a[108:111], v[140:143], v[96:99]\n"
"v_cndmask_b32_e64 v49, 0, v51, s[96:97]\n"
"v_cndmask_b32_e64 v50, 0, v51, s[96:97]\n"
"v_cndmask_b32_e64 v51, 0, v51, s[96:97]\n"
"v_mfma_f32_16x16x32_bf16 v[116:119], a[112:115], v[140:143], v[116:119]\n"
"buffer_load_dwordx4 v[84:87], v34, s[4:7], s99 offen sc1\n"
"s_waitcnt lgkmcnt(0)\n"
"v_mfma_f32_16x16x32_bf16 v[144:147], v[28:31], v[48:51], v[144:147]\n"
"s_add_u32 s97, s20, 0x1000\n"
"v_mfma_f32_16x16x32_bf16 v[204:207], a[116:119], v[140:143], v[204:207]\n"
"s_waitcnt lgkmcnt(0)\n"
"s_barrier\n"
"buffer_store_dwordx4 v[28:31], v0, s[4:7], s97 offen offset:2048 sc1\n"
"v_mfma_f32_16x16x32_bf16 v[136:139], a[120:123], v[220:223], v[136:139]\n"
"s_waitcnt vmcnt(18)\n"
"v_add_f32_e32 v250, v250, v248\n"
"v_mfma_f32_16x16x32_bf16 v[120:123], a[124:127], v[220:223], v[120:123]\n"
"v_add_f32_e32 v251, v251, v249\n"
"v_add_f32_e32 v246, v246, v244\n"
"v_mfma_f32_16x16x32_bf16 v[196:199], v[68:71], v[220:223], v[196:199]\n"
"v_add_f32_e32 v247, v247, v245\n"
"v_add_f32_e32 v250, v250, v246\n"
"v_mfma_f32_16x16x32_bf16 v[96:99], v[72:75], v[220:223], v[96:99]\n"
"v_add_f32_e32 v251, v251, v247\n"
"v_mfma_f32_16x16x32_bf16 v[116:119], v[76:79], v[220:223], v[116:119]\n"
"buffer_load_dwordx4 v[28:31], v34, s[4:7], s99 offen offset:1024 sc1\n"
"v_mfma_f32_16x16x32_bf16 v[204:207], v[80:83], v[220:223], v[204:207]\n"
"v_permlane16_swap_b32_e32 v250, v251\n"
"v_mfma_f32_16x16x32_bf16 v[136:139], v[148:151], v[108:111], v[136:139]\n"
"v_add_f32_e32 v250, v250, v251\n"
"v_mov_b32_e32 v251, v250\n"
"v_mfma_f32_16x16x32_bf16 v[120:123], v[152:155], v[108:111], v[120:123]\n"
"s_nop 0\n"
"v_permlane32_swap_b32_e32 v250, v251\n"
"v_add_f32_e32 v250, v250, v251\n"
"v_mfma_f32_16x16x32_bf16 v[196:199], v[156:159], v[108:111], v[196:199]\n"
"v_mul_f32_e32 v250, 0x3a800000, v250\n"
"v_mov_b32_e32 v251, v250\n"
"v_mfma_f32_16x16x32_bf16 v[96:99], v[160:163], v[108:111], v[96:99]\n"
"s_nop 0\n"
"v_permlane16_swap_b32_e32 v250, v251\n"
"v_cvt_pk_bf16_f32 v253, -v250, 0\n"
"v_mfma_f32_16x16x32_bf16 v[116:119], v[164:167], v[108:111], v[116:119]\n"
"buffer_load_dwordx4 v[48:51], v35, s[4:7], s99 offen sc1\n"
"v_mfma_f32_16x16x32_bf16 v[204:207], v[168:171], v[108:111], v[204:207]\n"
"v_fma_f32 v251, -v250, v250, v251\n"
"v_lshlrev_b32_e32 v253, 16, v253\n"
"v_mfma_f32_16x16x32_bf16 v[136:139], v[172:175], v[60:63], v[136:139]\n"
"ds_read_b128 v[108:111], v4\n"
"ds_read_b128 v[128:131], v9\n"
"v_mfma_f32_16x16x32_bf16 v[120:123], v[176:179], v[60:63], v[120:123]\n"
"ds_read_b128 v[140:143], v4 offset:1024\n"
"ds_read_b128 v[200:203], v9 offset:64\n"
"v_mfma_f32_16x16x32_bf16 v[196:199], v[180:183], v[60:63], v[196:199]\n"
"ds_read_b128 v[208:211], v4 offset:2048\n"
"ds_read_b128 v[212:215], v9 offset:128\n"
"v_mfma_f32_16x16x32_bf16 v[96:99], v[184:187], v[60:63], v[96:99]\n"
"v_max_f32_e32 v251, 0, v251\n"
"v_cvt_pk_bf16_f32 v251, v251, 0\n"
"v_mfma_f32_16x16x32_bf16 v[116:119], v[188:191], v[60:63], v[116:119]\n"
"buffer_load_dwordx4 v[216:219], v35, s[4:7], s99 offen offset:1024 sc1\n"
"v_mfma_f32_16x16x32_bf16 v[204:207], v[192:195], v[60:63], v[204:207]\n"
"s_waitcnt vmcnt(20)\n"
"v_lshlrev_b32_e32 v251, 16, v251\n"
"; @A G1 1.2\n"
"v_mfma_f32_16x16x32_bf16 v[60:63], a[0:3], v[64:67], 0\n"
"v_add_f32_e32 v251, 0x3727c5ac, v251\n"
"v_cvt_pk_bf16_f32 v251, v251, 0\n"
"v_mfma_f32_16x16x32_bf16 v[220:223], a[4:7], v[64:67], 0\n"
"s_waitcnt vmcnt(9)\n"
"v_cmp_ne_u64_e32 vcc, v[254:255], v[18:19]\n"
"v_mfma_f32_16x16x32_bf16 v[224:227], a[8:11], v[64:67], 0\n"
"s_cbranch_vccnz .Lmk_f2s\n"
".Lmk_f2r:\n"
"s_waitcnt lgkmcnt(0)\n"
"v_mfma_f32_16x16x32_bf16 v[228:231], a[12:15], v[64:67], 0\n"
"ds_write_b128 v3, v[136:139] offset:6144\n"
"v_mfma_f32_16x16x32_bf16 v[136:139], a[16:19], v[64:67], 0\n"
"s_add_u32 s99, s52, 0x8000\n"
"v_fmac_f32_e32 v108, v253, v128\n"
"v_mfma_f32_16x16x32_bf16 v[232:235], a[20:23], v[64:67], 0\n"
"buffer_load_dwordx4 v[64:67], v32, s[4:7], s99 offen sc1\n"
"v_mfma_f32_16x16x32_bf16 v[60:63], a[24:27], v[52:55], v[60:63]\n"
"ds_write_b128 v3, v[120:123] offset:7168\n"
"v_mfma_f32_16x16x32_bf16 v[220:223], a[28:31], v[52:55], v[220:223]\n"
"v_fmac_f32_e32 v109, v253, v129\n"
"v_fmac_f32_e32 v110, v253, v130\n"
"v_mfma_f32_16x16x32_bf16 v[224:227], a[32:35], v[52:55], v[224:227]\n"
"v_fmac_f32_e32 v111, v253, v131\n"
"v_fmac_f32_e32 v140, v253, v200\n"
"v_mfma_f32_16x16x32_bf16 v[228:231], a[36:39], v[52:55], v[228:231]\n"
"ds_write_b128 v3, v[196:199] offset:8192\n"
"v_mfma_f32_16x16x32_bf16 v[136:139], a[40:43], v[52:55], v[136:139]\n"
"buffer_load_dwordx4 v[120:123], v32, s[4:7], s99 offen offset:1024 sc1\n"
"ds_read_b64_tr_b16 v[128:129], v37\n"
"v_mfma_f32_16x16x32_bf16 v[232:235], a[44:47], v[52:55], v[232:235]\n"
"v_fmac_f32_e32 v141, v253, v201\n"
"ds_read_b64_tr_b16 v[130:131], v37 offset:2048\n"
"v_mfma_f32_16x16x32_bf16 v[60:63], a[48:51], v[112:115], v[60:63]\n"
"ds_write_b128 v3, v[96:99] offset:9216\n"
"v_mfma_f32_16x16x32_bf16 v[220:223], a[52:55], v[112:115], v[220:223]\n"
"v_fmac_f32_e32 v142, v253, v202\n"
"v_fmac_f32_e32 v143, v253, v203\n"
"s_mov_b32 s96, 0xc000c\n"
"v_mfma_f32_16x16x32_bf16 v[224:227], a[56:59], v[112:115], v[224:227]\n"
"v_fmac_f32_e32 v208, v253, v212\n"
"v_fmac_f32_e32 v209, v253, v213\n"
"s_mov_b32 s97, 0xc000c\n"
"v_mfma_f32_16x16x32_bf16 v[228:231], a[60:63], v[112:115], v[228:231]\n"
"ds_write_b128 v3, v[116:119] offset:10240\n"
"v_mfma_f32_16x16x32_bf16 v[136:139], a[64:67], v[112:115], v[136:139]\n"
"buffer_load_dwordx4 v[52:55], v33, s[4:7], s99 offen sc1\n"
"v_mov_b32_e32 v99, 0x3f803f80\n"
"v_mfma_f32_16x16x32_bf16 v[232:235], a[68:71], v[112:115], v[232:235]\n"
"v_fmac_f32_e32 v210, v253, v214\n"
"v_fmac_f32_e32 v211, v253, v215\n"
"v_cndmask_b32_e64 v96, 0, v99, s[96:97]\n"
"v_mfma_f32_16x16x32_bf16 v[60:63], a[72:75], v[132:135], v[60:63]\n"
"ds_write_b128 v3, v[204:207] offset:11264\n"
"v_mfma_f32_16x16x32_bf16 v[220:223], a[76:79], v[132:135], v[220:223]\n"
"ds_read_b128 v[112:115], v4 offset:3072\n"
"ds_read_b128 v[116:119], v9 offset:192\n"
"v_cndmask_b32_e64 v97, 0, v99, s[96:97]\n"
"v_mfma_f32_16x16x32_bf16 v[224:227], a[80:83], v[132:135], v[224:227]\n"
"ds_read_b128 v[196:199], v4 offset:4096\n"
"ds_read_b128 v[200:203], v9 offset:256\n"
"v_cndmask_b32_e64 v98, 0, v99, s[96:97]\n"
"v_mfma_f32_16x16x32_bf16 v[228:231], a[84:87], v[132:135], v[228:231]\n"
"ds_read_b128 v[204:207], v4 offset:5120\n"
"ds_read_b128 v[212:215], v9 offset:320\n"
"v_cndmask_b32_e64 v99, 0, v99, s[96:97]\n"
"v_mfma_f32_16x16x32_bf16 v[136:139], a[88:91], v[132:135], v[136:139]\n"
"buffer_load_dwordx4 v[236:239], v33, s[4:7], s99 offen offset:1024 sc1\n"
"s_waitcnt lgkmcnt(9)\n"
"v_mfma_f32_16x16x32_bf16 v[144:147], v[128:131], v[96:99], v[144:147]\n"
"buffer_store_dwordx4 v[128:131], v0, s[4:7], s21 offen sc1\n"
"v_mfma_f32_16x16x32_bf16 v[232:235], a[92:95], v[132:135], v[232:235]\n"
"v_lshlrev_b32_e32 v251, 16, v251\n"
"v_mfma_f32_16x16x32_bf16 v[60:63], a[96:99], v[56:59], v[60:63]\n"
"v_sqrt_f32_e32 v251, v251\n"
"v_mfma_f32_16x16x32_bf16 v[220:223], a[100:103], v[56:59], v[220:223]\n"
"v_cvt_pk_bf16_f32 v251, v251, 0\n"
"v_lshlrev_b32_e32 v251, 16, v251\n"
"v_mfma_f32_16x16x32_bf16 v[224:227], a[104:107], v[56:59], v[224:227]\n"
"s_waitcnt lgkmcnt(0)\n"
"v_fmac_f32_e32 v112, v253, v116\n"
"v_mfma_f32_16x16x32_bf16 v[228:231], a[108:111], v[56:59], v[228:231]\n"
"v_fmac_f32_e32 v113, v253, v117\n"
"v_fmac_f32_e32 v114, v253, v118\n"
"v_mfma_f32_16x16x32_bf16 v[136:139], a[112:115], v[56:59], v[136:139]\n"
"buffer_load_dwordx4 v[96:99], v34, s[4:7], s99 offen sc1\n"
"v_mfma_f32_16x16x32_bf16 v[232:235], a[116:119], v[56:59], v[232:235]\n"
"v_fmac_f32_e32 v115, v253, v119\n"
"v_fmac_f32_e32 v196, v253, v200\n"
"v_mfma_f32_16x16x32_bf16 v[60:63], a[120:123], v[92:95], v[60:63]\n"
"v_fmac_f32_e32 v197, v253, v201\n"
"v_fmac_f32_e32 v198, v253, v202\n"
"v_mfma_f32_16x16x32_bf16 v[220:223], a[124:127], v[92:95], v[220:223]\n"
"v_fmac_f32_e32 v199, v253, v203\n"
"v_fmac_f32_e32 v204, v253, v212\n"
"v_mfma_f32_16x16x32_bf16 v[224:227], v[68:71], v[92:95], v[224:227]\n"
"v_fmac_f32_e32 v205, v253, v213\n"
"v_fmac_f32_e32 v206, v253, v214\n"
"v_mfma_f32_16x16x32_bf16 v[228:231], v[72:75], v[92:95], v[228:231]\n"
"v_fmac_f32_e32 v207, v253, v215\n"
"v_mfma_f32_16x16x32_bf16 v[136:139], v[76:79], v[92:95], v[136:139]\n"
"buffer_load_dwordx4 v[56:59], v34, s[4:7], s99 offen offset:1024 sc1\n"
"v_mfma_f32_16x16x32_bf16 v[232:235], v[80:83], v[92:95], v[232:235]\n"
"v_mfma_f32_16x16x32_bf16 v[60:63], v[148:151], v[88:91], v[60:63]\n"
"v_rcp_f32_e32 v255, v251\n"
"v_mfma_f32_16x16x32_bf16 v[220:223], v[152:155], v[88:91], v[220:223]\n"
"v_mul_f32_e32 v254, 0x3e8293ee, v255\n"
"v_mfma_f32_16x16x32_bf16 v[224:227], v[156:159], v[88:91], v[224:227]\n"
"v_mfma_f32_16x16x32_bf16 v[228:231], v[160:163], v[88:91], v[228:231]\n"
"v_mfma_f32_16x16x32_bf16 v[136:139], v[164:167], v[88:91], v[136:139]\n"
"buffer_load_dwordx4 v[92:95], v35, s[4:7], s99 offen sc1\n"
"v_mfma_f32_16x16x32_bf16 v[232:235], v[168:171], v[88:91], v[232:235]\n"
"v_mfma_f32_16x16x32_bf16 v[60:63], v[172:175], v[44:47], v[60:63]\n"
"v_mfma_f32_16x16x32_bf16 v[220:223], v[176:179], v[44:47], v[220:223]\n"
"ds_read_b64_tr_b16 v[88:89], v37 offset:4096\n"
"ds_read_b64_tr_b16 v[90:91], v37 offset:6144\n"
"s_mov_b32 s96, 0xc000c\n"
"v_mfma_f32_16x16x32_bf16 v[224:227], v[180:183], v[44:47], v[224:227]\n"
"s_add_u32 s98, s75, 0\n"
"s_mov_b32 s97, 0xc000c\n"
"v_mov_b32_e32 v119, 0x3f803f80\n"
"v_mfma_f32_16x16x32_bf16 v[228:231], v[184:187], v[44:47], v[228:231]\n"
"buffer_load_dwordx2 v[252:253], v41, s[4:7], s98 offen sc1\n"
"v_cndmask_b32_e64 v116, 0, v119, s[96:97]\n"
"v_mfma_f32_16x16x32_bf16 v[136:139], v[188:191], v[44:47], v[136:139]\n"
"buffer_load_dwordx4 v[128:131], v35, s[4:7], s99 offen offset:1024 sc1\n"
"v_cndmask_b32_e64 v117, 0, v119, s[96:97]\n"
"v_mfma_f32_16x16x32_bf16 v[232:235], v[192:195], v[44:47], v[232:235]\n"
"v_cndmask_b32_e64 v118, 0, v119, s[96:97]\n"
"v_cndmask_b32_e64 v119, 0, v119, s[96:97]\n"
"; @A G1 1.3\n"
"v_mfma_f32_16x16x32_bf16 v[108:111], a[0:3], v[100:103], v[108:111]\n"
"s_waitcnt lgkmcnt(0)\n"
"v_mfma_f32_16x16x32_bf16 v[144:147], v[88:91], v[116:119], v[144:147]\n"
"s_add_u32 s99, s21, 0x1000\n"
"buffer_store_dwordx4 v[88:91], v0, s[4:7], s99 offen offset:2048 sc1\n"
"v_mfma_f32_16x16x32_bf16 v[140:143], a[4:7], v[100:103], v[140:143]\n"
"v_mfma_f32_16x16x32_bf16 v[208:211], a[8:11], v[100:103], v[208:211]\n"
"v_mfma_f32_16x16x32_bf16 v[112:115], a[12:15], v[100:103], v[112:115]\n"
"ds_write_b128 v3, v[60:63] offset:12288\n"
"v_mfma_f32_16x16x32_bf16 v[196:199], a[16:19], v[100:103], v[196:199]\n"
"s_add_u32 s99, s53, 0x8000\n"
"v_mfma_f32_16x16x32_bf16 v[204:207], a[20:23], v[100:103], v[204:207]\n"
"buffer_load_dwordx4 v[44:47], v32, s[4:7], s99 offen sc1\n"
"v_mfma_f32_16x16x32_bf16 v[108:111], a[24:27], v[124:127], v[108:111]\n"
"ds_write_b128 v3, v[220:223] offset:13312\n"
"v_mfma_f32_16x16x32_bf16 v[140:143], a[28:31], v[124:127], v[140:143]\n"
"v_mfma_f32_16x16x32_bf16 v[208:211], a[32:35], v[124:127], v[208:211]\n"
"v_mfma_f32_16x16x32_bf16 v[112:115], a[36:39], v[124:127], v[112:115]\n"
"ds_write_b128 v3, v[224:227] offset:14336\n"
"v_mfma_f32_16x16x32_bf16 v[196:199], a[40:43], v[124:127], v[196:199]\n"
"buffer_load_dwordx4 v[60:63], v32, s[4:7], s99 offen offset:1024 sc1\n"
"v_mfma_f32_16x16x32_bf16 v[204:207], a[44:47], v[124:127], v[204:207]\n"
"s_waitcnt vmcnt(19)\n"
"v_mfma_f32_16x16x32_bf16 v[108:111], a[48:51], v[104:107], v[108:111]\n"
"ds_write_b128 v3, v[228:231] offset:15360\n"
"v_mfma_f32_16x16x32_bf16 v[140:143], a[52:55], v[104:107], v[140:143]\n"
"v_mfma_f32_16x16x32_bf16 v[208:211], a[56:59], v[104:107], v[208:211]\n"
"v_mfma_f32_16x16x32_bf16 v[112:115], a[60:63], v[104:107], v[112:115]\n"
"ds_write_b128 v3, v[136:139] offset:16384\n"
"v_mfma_f32_16x16x32_bf16 v[196:199], a[64:67], v[104:107], v[196:199]\n"
"buffer_load_dwordx4 v[88:91], v33, s[4:7], s99 offen sc1\n"
"v_mfma_f32_16x16x32_bf16 v[204:207], a[68:71], v[104:107], v[204:207]\n"
"v_mfma_f32_16x16x32_bf16 v[108:111], a[72:75], v[24:27], v[108:111]\n"
"ds_write_b128 v3, v[232:235] offset:17408\n"
"v_mfma_f32_16x16x32_bf16 v[140:143], a[76:79], v[24:27], v[140:143]\n"
"v_mfma_f32_16x16x32_bf16 v[208:211], a[80:83], v[24:27], v[208:211]\n"
"v_mfma_f32_16x16x32_bf16 v[112:115], a[84:87], v[24:27], v[112:115]\n"
"s_waitcnt lgkmcnt(0)\n"
"s_barrier\n"
"v_mfma_f32_16x16x32_bf16 v[196:199], a[88:91], v[24:27], v[196:199]\n"
"buffer_load_dwordx4 v[100:103], v33, s[4:7], s99 offen offset:1024 sc1\n"
"v_mfma_f32_16x16x32_bf16 v[204:207], a[92:95], v[24:27], v[204:207]\n"
"s_waitcnt vmcnt(17)\n"
"ds_read_b128 v[24:27], v5 offset:2048\n"
"v_mfma_f32_16x16x32_bf16 v[108:111], a[96:99], v[84:87], v[108:111]\n"
"ds_read_b128 v[104:107], v6 offset:2048\n"
"ds_read_b128 v[116:119], v5 offset:3072\n"
"v_mfma_f32_16x16x32_bf16 v[140:143], a[100:103], v[84:87], v[140:143]\n"
"ds_read_b128 v[124:127], v6 offset:3072\n"
"v_mfma_f32_16x16x32_bf16 v[208:211], a[104:107], v[84:87], v[208:211]\n"
"v_mfma_f32_16x16x32_bf16 v[112:115], a[108:111], v[84:87], v[112:115]\n"
"v_mfma_f32_16x16x32_bf16 v[196:199], a[112:115], v[84:87], v[196:199]\n"
"buffer_load_dwordx4 v[132:135], v34, s[4:7], s99 offen sc1\n"
"ds_read_b64_tr_b16 v[136:137], v38\n"
"v_mfma_f32_16x16x32_bf16 v[204:207], a[116:119], v[84:87], v[204:207]\n"
"ds_read_b128 v[84:87], v5 offset:4096\n"
"ds_read_b128 v[200:203], v6 offset:4096\n"
"ds_read_b64_tr_b16 v[138:139], v38 offset:2048\n"
"v_mfma_f32_16x16x32_bf16 v[108:111], a[120:123], v[28:31], v[108:111]\n"
"ds_read_b128 v[212:215], v5 offset:5120\n"
"ds_read_b128 v[220:223], v6 offset:5120\n"
"s_mov_b32 s96, 0x300030\n"
"v_mfma_f32_16x16x32_bf16 v[140:143], a[124:127], v[28:31], v[140:143]\n"
"s_waitcnt lgkmcnt(6)\n"
"v_add_f32_e32 v24, v24, v104\n"
"s_mov_b32 s97, 0x300030\n"
"v_mfma_f32_16x16x32_bf16 v[208:211], v[68:71], v[28:31], v[208:211]\n"
"v_add_f32_e32 v25, v25, v105\n"
"v_add_f32_e32 v26, v26, v106\n"
"v_mov_b32_e32 v227, 0x3f803f80\n"
"v_mfma_f32_16x16x32_bf16 v[112:115], v[72:75], v[28:31], v[112:115]\n"
"ds_read_b128 v[228:231], v5\n"
"ds_read_b128 v[232:235], v6\n"
"v_cndmask_b32_e64 v224, 0, v227, s[96:97]\n"
"v_mfma_f32_16x16x32_bf16 v[196:199], v[76:79], v[28:31], v[196:199]\n"
"buffer_load_dwordx4 v[240:243], v34, s[4:7], s99 offen offset:1024 sc1\n"
"v_cndmask_b32_e64 v225, 0, v227, s[96:97]\n"
"v_mfma_f32_16x16x32_bf16 v[204:207], v[80:83], v[28:31], v[204:207]\n"
"s_waitcnt vmcnt(17)\n"
"ds_read_b128 v[28:31], v5 offset:1024\n"
"v_cndmask_b32_e64 v226, 0, v227, s[96:97]\n"
"v_mfma_f32_16x16x32_bf16 v[108:111], v[148:151], v[48:51], v[108:111]\n"
"ds_read_b128 v[244:247], v6 offset:1024\n"
"v_add_f32_e32 v27, v27, v107\n"
"v_cndmask_b32_e64 v227, 0, v227, s[96:97]\n"
"v_mfma_f32_16x16x32_bf16 v[140:143], v[152:155], v[48:51], v[140:143]\n"
"v_add_f32_e32 v116, v116, v124\n"
"v_add_f32_e32 v117, v117, v125\n"
"s_waitcnt lgkmcnt(6)\n"
"v_mfma_f32_16x16x32_bf16 v[144:147], v[136:139], v[224:227], v[144:147]\n"
"buffer_store_dwordx4 v[136:139], v0, s[4:7], s22 offen sc1\n"
"v_mfma_f32_16x16x32_bf16 v[208:211], v[156:159], v[48:51], v[208:211]\n"
"v_add_f32_e32 v118, v118, v126\n"
"v_add_f32_e32 v119, v119, v127\n"
"v_mfma_f32_16x16x32_bf16 v[112:115], v[160:163], v[48:51], v[112:115]\n"
"s_waitcnt lgkmcnt(4)\n"
"v_add_f32_e32 v84, v84, v200\n"
"v_mfma_f32_16x16x32_bf16 v[196:199], v[164:167], v[48:51], v[196:199]\n"
"buffer_load_dwordx4 v[104:107], v35, s[4:7], s99 offen sc1\n"
"v_mfma_f32_16x16x32_bf16 v[204:207], v[168:171], v[48:51], v[204:207]\n"
"s_waitcnt vmcnt(10)\n"
"v_cmp_ne_u64_e32 vcc, v[252:253], v[18:19]\n"
"v_mfma_f32_16x16x32_bf16 v[108:111], v[172:175], v[216:219], v[108:111]\n"
"s_cbranch_vccnz .Lmk_f5s\n"
".Lmk_f5r:\n"
"v_add_f32_e32 v85, v85, v201\n"
"v_mfma_f32_16x16x32_bf16 v[140:143], v[176:179], v[216:219], v[140:143]\n"
"v_add_f32_e32 v86, v86, v202\n"
"v_add_f32_e32 v87, v87, v203\n"
"v_mfma_f32_16x16x32_bf16 v[208:211], v[180:183], v[216:219], v[208:211]\n"
"v_add_f32_e32 v212, v212, v220\n"
"v_add_f32_e32 v213, v213, v221\n"
"v_mfma_f32_16x16x32_bf16 v[112:115], v[184:187], v[216:219], v[112:115]\n"
"v_add_f32_e32 v214, v214, v222\n"
"v_add_f32_e32 v215, v215, v223\n"
"v_mfma_f32_16x16x32_bf16 v[196:199], v[188:191], v[216:219], v[196:199]\n"
"buffer_load_dwordx4 v[40:43], v35, s[4:7], s99 offen offset:1024 sc1\n"
"v_mfma_f32_16x16x32_bf16 v[204:207], v[192:195], v[216:219], v[204:207]\n"
"s_waitcnt lgkmcnt(0)\n"
"v_add_f32_e32 v228, v228, v232\n"
"v_add_f32_e32 v229, v229, v233\n"
"v_add_f32_e32 v230, v230, v234\n"
"v_add_f32_e32 v231, v231, v235\n"
"v_add_f32_e32 v28, v28, v244\n"
"v_add_f32_e32 v29, v29, v245\n"
"v_add_f32_e32 v30, v30, v246\n"
"v_add_f32_e32 v31, v31, v247\n"
"; @A G1 2.0\n"
"v_mfma_f32_16x16x32_bf16 v[48:51], a[0:3], v[64:67], 0\n"
"s_add_u32 s99, s62, 0x800\n"
"v_mfma_f32_16x16x32_bf16 v[124:127], a[4:7], v[64:67], 0\n"
"buffer_load_dwordx2 v[252:253], v10, s[4:7], s99 offen sc1\n"
"v_mfma_f32_16x16x32_bf16 v[136:139], a[8:11], v[64:67], 0\n"
"buffer_load_dwordx2 v[250:251], v10, s[4:7], s99 offen offset:128 sc1\n"
"v_mfma_f32_16x16x32_bf16 v[200:203], a[12:15], v[64:67], 0\n"
"buffer_load_dwordx2 v[248:249], v10, s[4:7], s99 offen offset:256 sc1\n"
"v_mfma_f32_16x16x32_bf16 v[216:219], a[16:19], v[64:67], 0\n"
"s_add_u32 s98, s54, 0x8000\n"
"v_add_f32_e32 v208, v208, v24\n"
"v_mfma_f32_16x16x32_bf16 v[220:223], a[20:23], v[64:67], 0\n"
"buffer_load_dwordx4 v[64:67], v32, s[4:7], s98 offen sc1\n"
"v_mfma_f32_16x16x32_bf16 v[48:51], a[24:27], v[120:123], v[48:51]\n"
"buffer_load_dwordx2 v[246:247], v10, s[4:7], s99 offen offset:384 sc1\n"
"v_mfma_f32_16x16x32_bf16 v[124:127], a[28:31], v[120:123], v[124:127]\n"
"v_add_f32_e32 v209, v209, v25\n"
"v_add_f32_e32 v210, v210, v26\n"
"v_mfma_f32_16x16x32_bf16 v[136:139], a[32:35], v[120:123], v[136:139]\n"
"s_add_u32 s99, s70, 0\n"
"v_add_f32_e32 v211, v211, v27\n"
"v_mfma_f32_16x16x32_bf16 v[200:203], a[36:39], v[120:123], v[200:203]\n"
"buffer_load_dwordx4 a[128:131], v0, s[4:7], s99 offen sc1\n"
"v_mfma_f32_16x16x32_bf16 v[216:219], a[40:43], v[120:123], v[216:219]\n"
"buffer_load_dwordx4 v[24:27], v32, s[4:7], s98 offen offset:1024 sc1\n"
"v_mfma_f32_16x16x32_bf16 v[220:223], a[44:47], v[120:123], v[220:223]\n"
"v_add_f32_e32 v112, v112, v116\n"
"v_mfma_f32_16x16x32_bf16 v[48:51], a[48:51], v[52:55], v[48:51]\n"
"v_add_f32_e32 v113, v113, v117\n"
"v_add_f32_e32 v114, v114, v118\n"
"v_mfma_f32_16x16x32_bf16 v[124:127], a[52:55], v[52:55], v[124:127]\n"
"v_add_f32_e32 v115, v115, v119\n"
"v_mul_f32_e32 v208, v208, v255\n"
"v_mfma_f32_16x16x32_bf16 v[136:139], a[56:59], v[52:55], v[136:139]\n"
"v_mul_f32_e32 v209, v209, v255\n"
"v_mul_f32_e32 v210, v210, v255\n"
"v_mfma_f32_16x16x32_bf16 v[200:203], a[60:63], v[52:55], v[200:203]\n"
"v_mul_f32_e32 v211, v211, v255\n"
"v_mul_f32_e32 v112, v112, v255\n"
"v_mfma_f32_16x16x32_bf16 v[216:219], a[64:67], v[52:55], v[216:219]\n"
"buffer_load_dwordx4 v[116:119], v33, s[4:7], s98 offen sc1\n"
"v_mfma_f32_16x16x32_bf16 v[220:223], a[68:71], v[52:55], v[220:223]\n"
"v_mul_f32_e32 v113, v113, v255\n"
"v_mul_f32_e32 v114, v114, v255\n"
"v_mfma_f32_16x16x32_bf16 v[48:51], a[72:75], v[236:239], v[48:51]\n"
"v_mul_f32_e32 v115, v115, v255\n"
"v_cvt_pk_bf16_f32 v52, v208, v209\n"
"v_mfma_f32_16x16x32_bf16 v[124:127], a[76:79], v[236:239], v[124:127]\n"
"buffer_load_dwordx4 a[132:135], v0, s[4:7], s99 offen offset:1024 sc1\n"
"v_mfma_f32_16x16x32_bf16 v[136:139], a[80:83], v[236:239], v[136:139]\n"
"v_cvt_pk_bf16_f32 v53, v210, v211\n"
"v_cvt_pk_bf16_f32 v54, v112, v113\n"
"v_mfma_f32_16x16x32_bf16 v[200:203], a[84:87], v[236:239], v[200:203]\n"
"v_cvt_pk_bf16_f32 v55, v114, v115\n"
"v_mfma_f32_16x16x32_bf16 v[216:219], a[88:91], v[236:239], v[216:219]\n"
"buffer_load_dwordx4 v[112:115], v33, s[4:7], s98 offen offset:1024 sc1\n"
"v_mfma_f32_16x16x32_bf16 v[220:223], a[92:95], v[236:239], v[220:223]\n"
"v_mfma_f32_16x16x32_bf16 v[48:51], a[96:99], v[96:99], v[48:51]\n"
"ds_write_b128 v8, v[52:55] offset:25600\n"
"v_mfma_f32_16x16x32_bf16 v[124:127], a[100:103], v[96:99], v[124:127]\n"
"s_add_u32 s97, s64, 0x2000\n"
"v_mfma_f32_16x16x32_bf16 v[136:139], a[104:107], v[96:99], v[136:139]\n"
"buffer_store_dwordx4 v[52:55], v8, s[4:7], s97 offen offset:1024 sc1\n"
"v_mfma_f32_16x16x32_bf16 v[200:203], a[108:111], v[96:99], v[200:203]\n"
"v_add_f32_e32 v196, v196, v84\n"
"v_add_f32_e32 v197, v197, v85\n"
"v_mfma_f32_16x16x32_bf16 v[216:219], a[112:115], v[96:99], v[216:219]\n"
"buffer_load_dwordx4 v[52:55], v34, s[4:7], s98 offen sc1\n"
"v_mfma_f32_16x16x32_bf16 v[220:223], a[116:119], v[96:99], v[220:223]\n"
"buffer_load_dwordx4 a[136:139], v0, s[4:7], s99 offen offset:2048 sc1\n"
"v_mfma_f32_16x16x32_bf16 v[48:51], a[120:123], v[56:59], v[48:51]\n"
"v_add_f32_e32 v198, v198, v86\n"
"v_add_f32_e32 v199, v199, v87\n"
"v_mfma_f32_16x16x32_bf16 v[124:127], a[124:127], v[56:59], v[124:127]\n"
"v_add_f32_e32 v204, v204, v212\n"
"v_add_f32_e32 v205, v205, v213\n"
"v_mfma_f32_16x16x32_bf16 v[136:139], v[68:71], v[56:59], v[136:139]\n"
"v_add_f32_e32 v206, v206, v214\n"
"v_add_f32_e32 v207, v207, v215\n"
"v_mfma_f32_16x16x32_bf16 v[200:203], v[72:75], v[56:59], v[200:203]\n"
"v_mul_f32_e32 v196, v196, v255\n"
"v_mul_f32_e32 v197, v197, v255\n"
"v_mfma_f32_16x16x32_bf16 v[216:219], v[76:79], v[56:59], v[216:219]\n"
"buffer_load_dwordx4 v[84:87], v34, s[4:7], s98 offen offset:1024 sc1\n"
"v_mfma_f32_16x16x32_bf16 v[220:223], v[80:83], v[56:59], v[220:223]\n"
"s_waitcnt vmcnt(24)\n"
"v_mul_f32_e32 v198, v198, v255\n"
"v_mfma_f32_16x16x32_bf16 v[48:51], v[148:151], v[92:95], v[48:51]\n"
"v_mul_f32_e32 v199, v199, v255\n"
"v_mul_f32_e32 v204, v204, v255\n"
"v_mfma_f32_16x16x32_bf16 v[124:127], v[152:155], v[92:95], v[124:127]\n"
"v_mul_f32_e32 v205, v205, v255\n"
"v_mul_f32_e32 v206, v206, v255\n"
"v_mfma_f32_16x16x32_bf16 v[136:139], v[156:159], v[92:95], v[136:139]\n"
"v_mul_f32_e32 v207, v207, v255\n"
"v_cvt_pk_bf16_f32 v56, v196, v197\n"
"v_mfma_f32_16x16x32_bf16 v[200:203], v[160:163], v[92:95], v[200:203]\n"
"buffer_load_dwordx4 a[140:143], v0, s[4:7], s99 offen offset:3072 sc1\n"
"v_mfma_f32_16x16x32_bf16 v[216:219], v[164:167], v[92:95], v[216:219]\n"
"buffer_load_dwordx4 v[96:99], v35, s[4:7], s98 offen sc1\n"
"v_mfma_f32_16x16x32_bf16 v[220:223], v[168:171], v[92:95], v[220:223]\n"
"v_cvt_pk_bf16_f32 v57, v198, v199\n"
"v_cvt_pk_bf16_f32 v58, v204, v205\n"
"v_mfma_f32_16x16x32_bf16 v[48:51], v[172:175], v[128:131], v[48:51]\n"
"v_cvt_pk_bf16_f32 v59, v206, v207\n"
"v_mfma_f32_16x16x32_bf16 v[124:127], v[176:179], v[128:131], v[124:127]\n"
"ds_write_b128 v7, v[56:59] offset:58368\n"
"v_mfma_f32_16x16x32_bf16 v[136:139], v[180:183], v[128:131], v[136:139]\n"
"s_add_u32 s97, s64, 0x6000\n"
"v_mfma_f32_16x16x32_bf16 v[200:203], v[184:187], v[128:131], v[200:203]\n"
"buffer_store_dwordx4 v[56:59], v7, s[4:7], s97 offen offset:1024 sc1\n"
"v_mfma_f32_16x16x32_bf16 v[216:219], v[188:191], v[128:131], v[216:219]\n"
"buffer_load_dwordx4 v[56:59], v35, s[4:7], s98 offen offset:1024 sc1\n"
"v_mfma_f32_16x16x32_bf16 v[220:223], v[192:195], v[128:131], v[220:223]\n"
"s_waitcnt vmcnt(25)\n"
"v_add_f32_e32 v108, v108, v228\n"
"; @A G1 2.1\n"
"v_mfma_f32_16x16x32_bf16 v[92:95], a[0:3], v[44:47], 0\n"
"v_add_f32_e32 v109, v109, v229\n"
"v_add_f32_e32 v110, v110, v230\n"
"v_mfma_f32_16x16x32_bf16 v[120:123], a[4:7], v[44:47], 0\n"
"buffer_load_dwordx4 a[144:147], v1, s[4:7], s99 offen sc1\n"
"ds_read_b64_tr_b16 v[128:129], v38 offset:4096\n"
"v_mfma_f32_16x16x32_bf16 v[196:199], a[8:11], v[44:47], 0\n"
"v_add_f32_e32 v111, v111, v231\n"
"v_add_f32_e32 v140, v140, v28\n"
"ds_read_b64_tr_b16 v[130:131], v38 offset:6144\n"
"v_mfma_f32_16x16x32_bf16 v[204:207], a[12:15], v[44:47], 0\n"
"ds_write_b128 v3, v[48:51]\n"
"v_mfma_f32_16x16x32_bf16 v[48:51], a[16:19], v[44:47], 0\n"
"s_add_u32 s98, s55, 0x8000\n"
"v_add_f32_e32 v141, v141, v29\n"
"s_mov_b32 s96, 0x300030\n"
"v_mfma_f32_16x16x32_bf16 v[208:211], a[20:23], v[44:47], 0\n"
"buffer_load_dwordx4 v[44:47], v32, s[4:7], s98 offen sc1\n"
"s_mov_b32 s97, 0x300030\n"
"v_mfma_f32_16x16x32_bf16 v[92:95], a[24:27], v[60:63], v[92:95]\n"
"ds_write_b128 v3, v[124:127] offset:1024\n"
"v_mfma_f32_16x16x32_bf16 v[120:123], a[28:31], v[60:63], v[120:123]\n"
"v_add_f32_e32 v142, v142, v30\n"
"v_add_f32_e32 v143, v143, v31\n"
"v_mov_b32_e32 v31, 0x3f803f80\n"
"v_mfma_f32_16x16x32_bf16 v[196:199], a[32:35], v[60:63], v[196:199]\n"
"v_mul_f32_e32 v108, v108, v254\n"
"v_mul_f32_e32 v109, v109, v254\n"
"v_cndmask_b32_e64 v28, 0, v31, s[96:97]\n"
"v_mfma_f32_16x16x32_bf16 v[204:207], a[36:39], v[60:63], v[204:207]\n"
"ds_write_b128 v3, v[136:139] offset:2048\n"
"v_mfma_f32_16x16x32_bf16 v[48:51], a[40:43], v[60:63], v[48:51]\n"
"buffer_load_dwordx4 v[124:127], v32, s[4:7], s98 offen offset:1024 sc1\n"
"v_cndmask_b32_e64 v29, 0, v31, s[96:97]\n"
"v_mfma_f32_16x16x32_bf16 v[208:211], a[44:47], v[60:63], v[208:211]\n"
"s_waitcnt vmcnt(26)\n"
"v_mul_f32_e32 v110, v110, v254\n"
"v_cndmask_b32_e64 v30, 0, v31, s[96:97]\n"
"v_mfma_f32_16x16x32_bf16 v[92:95], a[48:51], v[88:91], v[92:95]\n"
"ds_write_b128 v3, v[200:203] offset:3072\n"
"v_mfma_f32_16x16x32_bf16 v[120:123], a[52:55], v[88:91], v[120:123]\n"
"buffer_load_dwordx4 a[148:151], v1, s[4:7], s99 offen offset:1024 sc1\n"
"v_cndmask_b32_e64 v31, 0, v31, s[96:97]\n"
"v_mfma_f32_16x16x32_bf16 v[196:199], a[56:59], v[88:91], v[196:199]\n"
"v_mul_f32_e32 v111, v111, v254\n"
"v_mul_f32_e32 v140, v140, v254\n"
"s_waitcnt lgkmcnt(4)\n"
"v_mfma_f32_16x16x32_bf16 v[144:147], v[128:131], v[28:31], v[144:147]\n"
"s_add_u32 s97, s22, 0x1000\n"
"v_mfma_f32_16x16x32_bf16 v[204:207], a[60:63], v[88:91], v[204:207]\n"
"ds_write_b128 v3, v[216:219] offset:4096\n"
"v_mfma_f32_16x16x32_bf16 v[48:51], a[64:67], v[88:91], v[48:51]\n"
"buffer_load_dwordx4 v[28:31], v33, s[4:7], s98 offen sc1\n"
"buffer_store_dwordx4 v[128:131], v0, s[4:7], s97 offen offset:2048 sc1\n"
"v_mfma_f32_16x16x32_bf16 v[208:211], a[68:71], v[88:91], v[208:211]\n"
"v_mul_f32_e32 v141, v141, v254\n"
"v_mul_f32_e32 v142, v142, v254\n"
"v_mfma_f32_16x16x32_bf16 v[92:95], a[72:75], v[100:103], v[92:95]\n"
"ds_write_b128 v3, v[220:223] offset:5120\n"
"v_mfma_f32_16x16x32_bf16 v[120:123], a[76:79], v[100:103], v[120:123]\n"
"v_mul_f32_e32 v143, v143, v254\n"
"v_cvt_pk_bf16_f32 v60, v108, v109\n"
"v_mfma_f32_16x16x32_bf16 v[196:199], a[80:83], v[100:103], v[196:199]\n"
"v_cvt_pk_bf16_f32 v61, v110, v111\n"
"v_cvt_pk_bf16_f32 v62, v140, v141\n"
"v_mfma_f32_16x16x32_bf16 v[204:207], a[84:87], v[100:103], v[204:207]\n"
"v_cvt_pk_bf16_f32 v63, v142, v143\n"
"v_mfma_f32_16x16x32_bf16 v[48:51], a[88:91], v[100:103], v[48:51]\n"
"buffer_load_dwordx4 v[88:91], v33, s[4:7], s98 offen offset:1024 sc1\n"
"v_mfma_f32_16x16x32_bf16 v[208:211], a[92:95], v[100:103], v[208:211]\n"
"s_waitcnt vmcnt(28)\n"
"v_mfma_f32_16x16x32_bf16 v[92:95], a[96:99], v[132:135], v[92:95]\n"
"buffer_load_dwordx4 a[152:155], v1, s[4:7], s99 offen offset:2048 sc1\n"
"buffer_store_dwordx4 v[144:147], v39, s[4:7], s23 offen sc1\n"
"v_mfma_f32_16x16x32_bf16 v[120:123], a[100:103], v[132:135], v[120:123]\n"
"ds_write_b128 v2, v[60:63] offset:50176\n"
"v_mfma_f32_16x16x32_bf16 v[196:199], a[104:107], v[132:135], v[196:199]\n"
"v_mfma_f32_16x16x32_bf16 v[204:207], a[108:111], v[132:135], v[204:207]\n"
"v_mfma_f32_16x16x32_bf16 v[48:51], a[112:115], v[132:135], v[48:51]\n"
"buffer_load_dwordx4 v[36:39], v34, s[4:7], s98 offen sc1\n"
"v_mfma_f32_16x16x32_bf16 v[208:211], a[116:119], v[132:135], v[208:211]\n"
"s_waitcnt lgkmcnt(0)\n"
"s_barrier\n"
"v_mfma_f32_16x16x32_bf16 v[92:95], a[120:123], v[240:243], v[92:95]\n"
"s_waitcnt vmcnt(23)\n"
"v_add_f32_e32 v252, v252, v250\n"
"v_mfma_f32_16x16x32_bf16 v[120:123], a[124:127], v[240:243], v[120:123]\n"
"v_add_f32_e32 v253, v253, v251\n"
"v_add_f32_e32 v248, v248, v246\n"
"v_mfma_f32_16x16x32_bf16 v[196:199], v[68:71], v[240:243], v[196:199]\n"
"v_add_f32_e32 v249, v249, v247\n"
"v_add_f32_e32 v252, v252, v248\n"
"v_mfma_f32_16x16x32_bf16 v[204:207], v[72:75], v[240:243], v[204:207]\n"
"v_add_f32_e32 v253, v253, v249\n"
"v_mfma_f32_16x16x32_bf16 v[48:51], v[76:79], v[240:243], v[48:51]\n"
"buffer_load_dwordx4 v[60:63], v34, s[4:7], s98 offen offset:1024 sc1\n"
"v_mfma_f32_16x16x32_bf16 v[208:211], v[80:83], v[240:243], v[208:211]\n"
"v_permlane16_swap_b32_e32 v252, v253\n"
"v_mfma_f32_16x16x32_bf16 v[92:95], v[148:151], v[104:107], v[92:95]\n"
"buffer_load_dwordx4 a[156:159], v1, s[4:7], s99 offen offset:3072 sc1\n"
"v_mfma_f32_16x16x32_bf16 v[120:123], v[152:155], v[104:107], v[120:123]\n"
"v_add_f32_e32 v252, v252, v253\n"
"v_mov_b32_e32 v253, v252\n"
"v_mfma_f32_16x16x32_bf16 v[196:199], v[156:159], v[104:107], v[196:199]\n"
"s_nop 0\n"
"v_permlane32_swap_b32_e32 v252, v253\n"
"v_add_f32_e32 v252, v252, v253\n"
"v_mfma_f32_16x16x32_bf16 v[204:207], v[160:163], v[104:107], v[204:207]\n"
"v_mul_f32_e32 v252, 0x3a800000, v252\n"
"v_mov_b32_e32 v253, v252\n"
"v_mfma_f32_16x16x32_bf16 v[48:51], v[164:167], v[104:107], v[48:51]\n"
"buffer_load_dwordx4 v[100:103], v35, s[4:7], s98 offen sc1\n"
"v_mfma_f32_16x16x32_bf16 v[208:211], v[168:171], v[104:107], v[208:211]\n"
"v_permlane16_swap_b32_e32 v252, v253\n"
"v_cvt_pk_bf16_f32 v255, -v252, 0\n"
"v_mfma_f32_16x16x32_bf16 v[92:95], v[172:175], v[40:43], v[92:95]\n"
"v_fma_f32 v253, -v252, v252, v253\n"
"v_lshlrev_b32_e32 v255, 16, v255\n"
"v_mfma_f32_16x16x32_bf16 v[120:123], v[176:179], v[40:43], v[120:123]\n"
"ds_read_b128 v[104:107], v4\n"
"ds_read_b128 v[108:111], v9\n"
"v_mfma_f32_16x16x32_bf16 v[196:199], v[180:183], v[40:43], v[196:199]\n"
"s_add_u32 s99, s70, 0x2000\n"
"ds_read_b128 v[128:131], v4 offset:1024\n"
"v_mfma_f32_16x16x32_bf16 v[204:207], v[184:187], v[40:43], v[204:207]\n"
"ds_read_b128 v[132:135], v9 offset:64\n"
"ds_read_b128 v[136:139], v4 offset:2048\n"
"v_mfma_f32_16x16x32_bf16 v[48:51], v[188:191], v[40:43], v[48:51]\n"
"buffer_load_dwordx4 v[140:143], v35, s[4:7], s98 offen offset:1024 sc1\n"
"v_mfma_f32_16x16x32_bf16 v[208:211], v[192:195], v[40:43], v[208:211]\n"
"s_waitcnt vmcnt(25)\n"
"ds_read_b128 v[40:43], v9 offset:128\n"
"; @A G1 2.2\n"
"v_mfma_f32_16x16x32_bf16 v[144:147], a[0:3], v[64:67], 0\n"
"buffer_load_dwordx4 a[160:163], v0, s[4:7], s99 offen sc1\n"
"s_add_u32 s98, s51, 0\n"
"v_mfma_f32_16x16x32_bf16 v[200:203], a[4:7], v[64:67], 0\n"
"v_max_f32_e32 v253, 0, v253\n"
"v_cvt_pk_bf16_f32 v253, v253, 0\n"
"s_bitcmp1_b32 s26, 0\n"
"s_cbranch_scc1 .Lmk_w7s\n"
"buffer_store_dwordx4 a[0:3], v0, s[4:7], s98 offen sc1\n"
".Lmk_w7r:\n"
"v_mfma_f32_16x16x32_bf16 v[212:215], a[8:11], v[64:67], 0\n"
"ds_read_b128 v[216:219], v4 offset:3072\n"
"ds_read_b128 v[220:223], v9 offset:192\n"
"v_mfma_f32_16x16x32_bf16 v[224:227], a[12:15], v[64:67], 0\n"
"ds_write_b128 v3, v[92:95] offset:6144\n"
"v_mfma_f32_16x16x32_bf16 v[92:95], a[16:19], v[64:67], 0\n"
"s_add_u32 s98, s52, 0\n"
"s_waitcnt lgkmcnt(3)\n"
"v_mfma_f32_16x16x32_bf16 v[228:231], a[20:23], v[64:67], 0\n"
"buffer_load_dwordx4 v[64:67], v32, s[4:7], s98 offen sc1\n"
"v_mfma_f32_16x16x32_bf16 v[144:147], a[24:27], v[24:27], v[144:147]\n"
"ds_write_b128 v3, v[120:123] offset:7168\n"
"v_mfma_f32_16x16x32_bf16 v[200:203], a[28:31], v[24:27], v[200:203]\n"
"v_fmac_f32_e32 v104, v255, v108\n"
"v_fmac_f32_e32 v105, v255, v109\n"
"v_mfma_f32_16x16x32_bf16 v[212:215], a[32:35], v[24:27], v[212:215]\n"
"v_fmac_f32_e32 v106, v255, v110\n"
"v_fmac_f32_e32 v107, v255, v111\n"
"v_mfma_f32_16x16x32_bf16 v[224:227], a[36:39], v[24:27], v[224:227]\n"
"ds_write_b128 v3, v[196:199] offset:8192\n"
"v_mfma_f32_16x16x32_bf16 v[92:95], a[40:43], v[24:27], v[92:95]\n"
"buffer_load_dwordx4 v[108:111], v32, s[4:7], s98 offen offset:1024 sc1\n"
"v_mfma_f32_16x16x32_bf16 v[228:231], a[44:47], v[24:27], v[228:231]\n"
"s_waitcnt vmcnt(26)\n"
"v_fmac_f32_e32 v128, v255, v132\n"
"v_mfma_f32_16x16x32_bf16 v[144:147], a[48:51], v[116:119], v[144:147]\n"
"ds_write_b128 v3, v[204:207] offset:9216\n"
"v_mfma_f32_16x16x32_bf16 v[200:203], a[52:55], v[116:119], v[200:203]\n"
"buffer_load_dwordx4 a[164:167], v0, s[4:7], s99 offen offset:1024 sc1\n"
"v_mfma_f32_16x16x32_bf16 v[212:215], a[56:59], v[116:119], v[212:215]\n"
"v_fmac_f32_e32 v129, v255, v133\n"
"v_fmac_f32_e32 v130, v255, v134\n"
"v_mfma_f32_16x16x32_bf16 v[224:227], a[60:63], v[116:119], v[224:227]\n"
"ds_write_b128 v3, v[48:51] offset:10240\n"
"v_mfma_f32_16x16x32_bf16 v[92:95], a[64:67], v[116:119], v[92:95]\n"
"buffer_load_dwordx4 v[24:27], v33, s[4:7], s98 offen sc1\n"
"v_mfma_f32_16x16x32_bf16 v[228:231], a[68:71], v[116:119], v[228:231]\n"
"v_fmac_f32_e32 v131, v255, v135\n"
"v_fmac_f32_e32 v136, v255, v40\n"
"v_mfma_f32_16x16x32_bf16 v[144:147], a[72:75], v[112:115], v[144:147]\n"
"ds_write_b128 v3, v[208:211] offset:11264\n"
"v_mfma_f32_16x16x32_bf16 v[200:203], a[76:79], v[112:115], v[200:203]\n"
"v_fmac_f32_e32 v137, v255, v41\n"
"v_fmac_f32_e32 v138, v255, v42\n"
"v_mfma_f32_16x16x32_bf16 v[212:215], a[80:83], v[112:115], v[212:215]\n"
"v_fmac_f32_e32 v139, v255, v43\n"
"ds_read_b128 v[40:43], v4 offset:4096\n"
"v_mfma_f32_16x16x32_bf16 v[224:227], a[84:87], v[112:115], v[224:227]\n"
"ds_read_b128 v[48:51], v9 offset:256\n"
"ds_read_b128 v[116:119], v4 offset:5120\n"
"v_mfma_f32_16x16x32_bf16 v[92:95], a[88:91], v[112:115], v[92:95]\n"
"buffer_load_dwordx4 v[120:123], v33, s[4:7], s98 offen offset:1024 sc1\n"
"v_mfma_f32_16x16x32_bf16 v[228:231], a[92:95], v[112:115], v[228:231]\n"
"s_waitcnt vmcnt(25)\n"
"ds_read_b128 v[112:115], v9 offset:320\n"
"v_mfma_f32_16x16x32_bf16 v[144:147], a[96:99], v[52:55], v[144:147]\n"
"buffer_load_dwordx4 a[168:171], v0, s[4:7], s99 offen offset:2048 sc1\n"
"s_add_u32 s97, s51, 0x400\n"
"v_mfma_f32_16x16x32_bf16 v[200:203], a[100:103], v[52:55], v[200:203]\n"
"v_lshlrev_b32_e32 v253, 16, v253\n"
"v_add_f32_e32 v253, 0x3727c5ac, v253\n"
"s_bitcmp1_b32 s26, 0\n"
"s_cbranch_scc1 .Lmk_w8s\n"
"buffer_store_dwordx4 a[4:7], v0, s[4:7], s97 offen sc1\n"
".Lmk_w8r:\n"
"v_mfma_f32_16x16x32_bf16 v[212:215], a[104:107], v[52:55], v[212:215]\n"
"v_cvt_pk_bf16_f32 v253, v253, 0\n"
"v_lshlrev_b32_e32 v253, 16, v253\n"
"v_mfma_f32_16x16x32_bf16 v[224:227], a[108:111], v[52:55], v[224:227]\n"
"v_sqrt_f32_e32 v253, v253\n"
"v_mfma_f32_16x16x32_bf16 v[92:95], a[112:115], v[52:55], v[92:95]\n"
"buffer_load_dwordx4 v[132:135], v34, s[4:7], s98 offen sc1\n"
"v_mfma_f32_16x16x32_bf16 v[228:231], a[116:119], v[52:55], v[228:231]\n"
"s_waitcnt lgkmcnt(0)\n"
"v_fmac_f32_e32 v216, v255, v220\n"
"v_mfma_f32_16x16x32_bf16 v[144:147], a[120:123], v[84:87], v[144:147]\n"
"v_fmac_f32_e32 v217, v255, v221\n"
"v_fmac_f32_e32 v218, v255, v222\n"
"v_mfma_f32_16x16x32_bf16 v[200:203], a[124:127], v[84:87], v[200:203]\n"
"v_fmac_f32_e32 v219, v255, v223\n"
"v_fmac_f32_e32 v40, v255, v48\n"
"v_mfma_f32_16x16x32_bf16 v[212:215], v[68:71], v[84:87], v[212:215]\n"
"v_fmac_f32_e32 v41, v255, v49\n"
"v_fmac_f32_e32 v42, v255, v50\n"
"v_mfma_f32_16x16x32_bf16 v[224:227], v[72:75], v[84:87], v[224:227]\n"
"v_fmac_f32_e32 v43, v255, v51\n"
"v_fmac_f32_e32 v116, v255, v112\n"
"v_mfma_f32_16x16x32_bf16 v[92:95], v[76:79], v[84:87], v[92:95]\n"
"buffer_load_dwordx4 v[48:51], v34, s[4:7], s98 offen offset:1024 sc1\n"
"v_mfma_f32_16x16x32_bf16 v[228:231], v[80:83], v[84:87], v[228:231]\n"
"s_waitcnt vmcnt(25)\n"
"v_mfma_f32_16x16x32_bf16 v[144:147], v[148:151], v[96:99], v[144:147]\n"
"s_add_u32 s97, s68, 0x1000\n"
"v_fmac_f32_e32 v117, v255, v113\n"
"v_mfma_f32_16x16x32_bf16 v[200:203], v[152:155], v[96:99], v[200:203]\n"
"buffer_store_dwordx2 v[18:19], v11, s[4:7], s97 offen offset:64 sc1\n"
"v_mfma_f32_16x16x32_bf16 v[212:215], v[156:159], v[96:99], v[212:215]\n"
"buffer_load_dwordx4 a[172:175], v0, s[4:7], s99 offen offset:3072 sc1\n"
"v_mfma_f32_16x16x32_bf16 v[224:227], v[160:163], v[96:99], v[224:227]\n"
"v_fmac_f32_e32 v118, v255, v114\n"
"v_fmac_f32_e32 v119, v255, v115\n"
"v_mfma_f32_16x16x32_bf16 v[92:95], v[164:167], v[96:99], v[92:95]\n"
"buffer_load_dwordx4 v[52:55], v35, s[4:7], s98 offen sc1\n"
"v_mfma_f32_16x16x32_bf16 v[228:231], v[168:171], v[96:99], v[228:231]\n"
"v_cvt_pk_bf16_f32 v253, v253, 0\n"
"v_lshlrev_b32_e32 v253, 16, v253\n"
"v_mfma_f32_16x16x32_bf16 v[144:147], v[172:175], v[56:59], v[144:147]\n"
"v_rcp_f32_e32 v255, v253\n"
"v_mfma_f32_16x16x32_bf16 v[200:203], v[176:179], v[56:59], v[200:203]\n"
"v_mul_f32_e32 v254, 0x3e8293ee, v255\n"
"v_mfma_f32_16x16x32_bf16 v[212:215], v[180:183], v[56:59], v[212:215]\n"
"v_mfma_f32_16x16x32_bf16 v[224:227], v[184:187], v[56:59], v[224:227]\n"
"v_mfma_f32_16x16x32_bf16 v[92:95], v[188:191], v[56:59], v[92:95]\n"
"buffer_load_dwordx4 v[84:87], v35, s[4:7], s98 offen offset:1024 sc1\n"
"v_mfma_f32_16x16x32_bf16 v[228:231], v[192:195], v[56:59], v[228:231]\n"
"s_waitcnt vmcnt(26)\n"
"; @A G1 2.3\n"
"v_mfma_f32_16x16x32_bf16 v[104:107], a[0:3], v[44:47], v[104:107]\n"
"buffer_load_dwordx4 a[176:179], v1, s[4:7], s99 offen sc1\n"
"v_mfma_f32_16x16x32_bf16 v[128:131], a[4:7], v[44:47], v[128:131]\n"
"v_mfma_f32_16x16x32_bf16 v[136:139], a[8:11], v[44:47], v[136:139]\n"
"v_mfma_f32_16x16x32_bf16 v[216:219], a[12:15], v[44:47], v[216:219]\n"
"ds_write_b128 v3, v[144:147] offset:12288\n"
"v_mfma_f32_16x16x32_bf16 v[40:43], a[16:19], v[44:47], v[40:43]\n"
"s_add_u32 s98, s53, 0\n"
"v_mfma_f32_16x16x32_bf16 v[116:119], a[20:23], v[44:47], v[116:119]\n"
"buffer_load_dwordx4 v[44:47], v32, s[4:7], s98 offen sc1\n"
"v_mfma_f32_16x16x32_bf16 v[104:107], a[24:27], v[124:127], v[104:107]\n"
"ds_write_b128 v3, v[200:203] offset:13312\n"
"v_mfma_f32_16x16x32_bf16 v[128:131], a[28:31], v[124:127], v[128:131]\n"
"v_mfma_f32_16x16x32_bf16 v[136:139], a[32:35], v[124:127], v[136:139]\n"
"v_mfma_f32_16x16x32_bf16 v[216:219], a[36:39], v[124:127], v[216:219]\n"
"ds_write_b128 v3, v[212:215] offset:14336\n"
"v_mfma_f32_16x16x32_bf16 v[40:43], a[40:43], v[124:127], v[40:43]\n"
"buffer_load_dwordx4 v[56:59], v32, s[4:7], s98 offen offset:1024 sc1\n"
"v_mfma_f32_16x16x32_bf16 v[116:119], a[44:47], v[124:127], v[116:119]\n"
"s_waitcnt vmcnt(25)\n"
"v_mfma_f32_16x16x32_bf16 v[104:107], a[48:51], v[28:31], v[104:107]\n"
"ds_write_b128 v3, v[224:227] offset:15360\n"
"v_mfma_f32_16x16x32_bf16 v[128:131], a[52:55], v[28:31], v[128:131]\n"
"buffer_load_dwordx4 a[180:183], v1, s[4:7], s99 offen offset:1024 sc1\n"
"v_mfma_f32_16x16x32_bf16 v[136:139], a[56:59], v[28:31], v[136:139]\n"
"v_mfma_f32_16x16x32_bf16 v[216:219], a[60:63], v[28:31], v[216:219]\n"
"ds_write_b128 v3, v[92:95] offset:16384\n"
"v_mfma_f32_16x16x32_bf16 v[40:43], a[64:67], v[28:31], v[40:43]\n"
"buffer_load_dwordx4 v[92:95], v33, s[4:7], s98 offen sc1\n"
"v_mfma_f32_16x16x32_bf16 v[116:119], a[68:71], v[28:31], v[116:119]\n"
"v_mfma_f32_16x16x32_bf16 v[104:107], a[72:75], v[88:91], v[104:107]\n"
"ds_write_b128 v3, v[228:231] offset:17408\n"
"v_mfma_f32_16x16x32_bf16 v[128:131], a[76:79], v[88:91], v[128:131]\n"
"v_mfma_f32_16x16x32_bf16 v[136:139], a[80:83], v[88:91], v[136:139]\n"
"v_mfma_f32_16x16x32_bf16 v[216:219], a[84:87], v[88:91], v[216:219]\n"
"s_waitcnt lgkmcnt(0)\n"
"s_barrier\n"
"v_mfma_f32_16x16x32_bf16 v[40:43], a[88:91], v[88:91], v[40:43]\n"
"buffer_load_dwordx4 v[28:31], v33, s[4:7], s98 offen offset:1024 sc1\n"
"v_mfma_f32_16x16x32_bf16 v[116:119], a[92:95], v[88:91], v[116:119]\n"
"s_waitcnt vmcnt(24)\n"
"ds_read_b128 v[88:91], v5 offset:2048\n"
"v_mfma_f32_16x16x32_bf16 v[104:107], a[96:99], v[36:39], v[104:107]\n"
"buffer_load_dwordx4 a[184:187], v1, s[4:7], s99 offen offset:2048 sc1\n"
"v_mfma_f32_16x16x32_bf16 v[128:131], a[100:103], v[36:39], v[128:131]\n"
"ds_read_b128 v[96:99], v6 offset:2048\n"
"ds_read_b128 v[112:115], v5 offset:3072\n"
"v_mfma_f32_16x16x32_bf16 v[136:139], a[104:107], v[36:39], v[136:139]\n"
"ds_read_b128 v[124:127], v6 offset:3072\n"
"s_waitcnt vmcnt(13)\n"
"s_add_u32 s97, s68, 0x1000\n"
"buffer_store_dwordx2 v[18:19], v11, s[4:7], s97 offen offset:96 sc1\n"
"v_mfma_f32_16x16x32_bf16 v[216:219], a[108:111], v[36:39], v[216:219]\n"
"v_mfma_f32_16x16x32_bf16 v[40:43], a[112:115], v[36:39], v[40:43]\n"
"buffer_load_dwordx4 v[144:147], v34, s[4:7], s98 offen sc1\n"
"v_mfma_f32_16x16x32_bf16 v[116:119], a[116:119], v[36:39], v[116:119]\n"
"ds_read_b128 v[36:39], v5 offset:4096\n"
"ds_read_b128 v[196:199], v6 offset:4096\n"
"v_mfma_f32_16x16x32_bf16 v[104:107], a[120:123], v[60:63], v[104:107]\n"
"ds_read_b128 v[200:203], v5 offset:5120\n"
"ds_read_b128 v[204:207], v6 offset:5120\n"
"v_mfma_f32_16x16x32_bf16 v[128:131], a[124:127], v[60:63], v[128:131]\n"
"s_waitcnt lgkmcnt(4)\n"
"v_add_f32_e32 v88, v88, v96\n"
"v_mfma_f32_16x16x32_bf16 v[136:139], v[68:71], v[60:63], v[136:139]\n"
"v_add_f32_e32 v89, v89, v97\n"
"v_add_f32_e32 v90, v90, v98\n"
"v_mfma_f32_16x16x32_bf16 v[216:219], v[72:75], v[60:63], v[216:219]\n"
"ds_read_b128 v[208:211], v5\n"
"ds_read_b128 v[212:215], v6\n"
"v_mfma_f32_16x16x32_bf16 v[40:43], v[76:79], v[60:63], v[40:43]\n"
"buffer_load_dwordx4 v[220:223], v34, s[4:7], s98 offen offset:1024 sc1\n"
"v_mfma_f32_16x16x32_bf16 v[116:119], v[80:83], v[60:63], v[116:119]\n"
"ds_read_b128 v[60:63], v5 offset:1024\n"
"v_mfma_f32_16x16x32_bf16 v[104:107], v[148:151], v[100:103], v[104:107]\n"
"buffer_load_dwordx4 a[188:191], v1, s[4:7], s99 offen offset:3072 sc1\n"
"v_mfma_f32_16x16x32_bf16 v[128:131], v[152:155], v[100:103], v[128:131]\n"
"ds_read_b128 v[224:227], v6 offset:1024\n"
"v_add_f32_e32 v91, v91, v99\n"
"v_mfma_f32_16x16x32_bf16 v[136:139], v[156:159], v[100:103], v[136:139]\n"
"v_add_f32_e32 v112, v112, v124\n"
"v_add_f32_e32 v113, v113, v125\n"
"v_mfma_f32_16x16x32_bf16 v[216:219], v[160:163], v[100:103], v[216:219]\n"
"v_add_f32_e32 v114, v114, v126\n"
"v_add_f32_e32 v115, v115, v127\n"
"v_mfma_f32_16x16x32_bf16 v[40:43], v[164:167], v[100:103], v[40:43]\n"
"buffer_load_dwordx4 v[96:99], v35, s[4:7], s98 offen sc1\n"
"v_mfma_f32_16x16x32_bf16 v[116:119], v[168:171], v[100:103], v[116:119]\n"
"s_waitcnt lgkmcnt(4)\n"
"v_add_f32_e32 v36, v36, v196\n"
"v_mfma_f32_16x16x32_bf16 v[104:107], v[172:175], v[140:143], v[104:107]\n"
"v_add_f32_e32 v37, v37, v197\n"
"v_add_f32_e32 v38, v38, v198\n"
"v_mfma_f32_16x16x32_bf16 v[128:131], v[176:179], v[140:143], v[128:131]\n"
"v_add_f32_e32 v39, v39, v199\n"
"v_add_f32_e32 v200, v200, v204\n"
"v_mfma_f32_16x16x32_bf16 v[136:139], v[180:183], v[140:143], v[136:139]\n"
"s_add_u32 s99, s70, 0x4000\n"
"v_add_f32_e32 v201, v201, v205\n"
"v_mfma_f32_16x16x32_bf16 v[216:219], v[184:187], v[140:143], v[216:219]\n"
"v_add_f32_e32 v202, v202, v206\n"
"v_add_f32_e32 v203, v203, v207\n"
"v_mfma_f32_16x16x32_bf16 v[40:43], v[188:191], v[140:143], v[40:43]\n"
"buffer_load_dwordx4 v[100:103], v35, s[4:7], s98 offen offset:1024 sc1\n"
"v_mfma_f32_16x16x32_bf16 v[116:119], v[192:195], v[140:143], v[116:119]\n"
"buffer_load_dwordx4 a[192:195], v0, s[4:7], s99 offen sc1\n"
"s_waitcnt lgkmcnt(0)\n"
"v_add_f32_e32 v208, v208, v212\n"
"v_add_f32_e32 v209, v209, v213\n"
"v_add_f32_e32 v210, v210, v214\n"
"v_add_f32_e32 v211, v211, v215\n"
"v_add_f32_e32 v60, v60, v224\n"
"v_add_f32_e32 v61, v61, v225\n"
"v_add_f32_e32 v62, v62, v226\n"
"v_add_f32_e32 v63, v63, v227\n"
"buffer_load_dwordx4 a[196:199], v0, s[4:7], s99 offen offset:1024 sc1\n"
"; @A G1 3.0\n"
"v_mfma_f32_16x16x32_bf16 v[124:127], a[0:3], v[64:67], 0\n"
"s_add_u32 s98, s62, 0\n"
"v_mfma_f32_16x16x32_bf16 v[140:143], a[4:7], v[64:67], 0\n"
"buffer_load_dwordx2 v[252:253], v10, s[4:7], s98 offen sc1\n"
"v_mfma_f32_16x16x32_bf16 v[196:199], a[8:11], v[64:67], 0\n"
"buffer_load_dwordx2 v[250:251], v10, s[4:7], s98 offen offset:128 sc1\n"
"v_mfma_f32_16x16x32_bf16 v[204:207], a[12:15], v[64:67], 0\n"
"buffer_load_dwordx2 v[248:249], v10, s[4:7], s98 offen offset:256 sc1\n"
"v_mfma_f32_16x16x32_bf16 v[212:215], a[16:19], v[64:67], 0\n"
"s_add_u32 s97, s54, 0\n"
"v_add_f32_e32 v136, v136, v88\n"
"v_mfma_f32_16x16x32_bf16 v[224:227], a[20:23], v[64:67], 0\n"
"buffer_load_dwordx4 v[64:67], v32, s[4:7], s97 offen sc1\n"
"v_mfma_f32_16x16x32_bf16 v[124:127], a[24:27], v[108:111], v[124:127]\n"
"buffer_load_dwordx2 v[246:247], v10, s[4:7], s98 offen offset:384 sc1\n"
"v_mfma_f32_16x16x32_bf16 v[140:143], a[28:31], v[108:111], v[140:143]\n"
"v_add_f32_e32 v137, v137, v89\n"
"v_add_f32_e32 v138, v138, v90\n"
"v_mfma_f32_16x16x32_bf16 v[196:199], a[32:35], v[108:111], v[196:199]\n"
"buffer_load_dwordx4 a[200:203], v0, s[4:7], s99 offen offset:2048 sc1\n"
"v_mfma_f32_16x16x32_bf16 v[204:207], a[36:39], v[108:111], v[204:207]\n"
"v_add_f32_e32 v139, v139, v91\n"
"v_add_f32_e32 v216, v216, v112\n"
"v_mfma_f32_16x16x32_bf16 v[212:215], a[40:43], v[108:111], v[212:215]\n"
"buffer_load_dwordx4 v[88:91], v32, s[4:7], s97 offen offset:1024 sc1\n"
"v_mfma_f32_16x16x32_bf16 v[224:227], a[44:47], v[108:111], v[224:227]\n"
"v_add_f32_e32 v217, v217, v113\n"
"v_mfma_f32_16x16x32_bf16 v[124:127], a[48:51], v[24:27], v[124:127]\n"
"v_add_f32_e32 v218, v218, v114\n"
"v_add_f32_e32 v219, v219, v115\n"
"v_mfma_f32_16x16x32_bf16 v[140:143], a[52:55], v[24:27], v[140:143]\n"
"v_mul_f32_e32 v136, v136, v255\n"
"v_mul_f32_e32 v137, v137, v255\n"
"v_mfma_f32_16x16x32_bf16 v[196:199], a[56:59], v[24:27], v[196:199]\n"
"v_mul_f32_e32 v138, v138, v255\n"
"v_mul_f32_e32 v139, v139, v255\n"
"v_mfma_f32_16x16x32_bf16 v[204:207], a[60:63], v[24:27], v[204:207]\n"
"v_mul_f32_e32 v216, v216, v255\n"
"v_mul_f32_e32 v217, v217, v255\n"
"v_mfma_f32_16x16x32_bf16 v[212:215], a[64:67], v[24:27], v[212:215]\n"
"buffer_load_dwordx4 v[108:111], v33, s[4:7], s97 offen sc1\n"
"v_mfma_f32_16x16x32_bf16 v[224:227], a[68:71], v[24:27], v[224:227]\n"
"v_mul_f32_e32 v218, v218, v255\n"
"v_mul_f32_e32 v219, v219, v255\n"
"v_mfma_f32_16x16x32_bf16 v[124:127], a[72:75], v[120:123], v[124:127]\n"
"v_cvt_pk_bf16_f32 v24, v136, v137\n"
"v_cvt_pk_bf16_f32 v25, v138, v139\n"
"v_mfma_f32_16x16x32_bf16 v[140:143], a[76:79], v[120:123], v[140:143]\n"
"v_cvt_pk_bf16_f32 v26, v216, v217\n"
"v_cvt_pk_bf16_f32 v27, v218, v219\n"
"v_mfma_f32_16x16x32_bf16 v[196:199], a[80:83], v[120:123], v[196:199]\n"
"ds_write_b128 v8, v[24:27] offset:17408\n"
"v_mfma_f32_16x16x32_bf16 v[204:207], a[84:87], v[120:123], v[204:207]\n"
"buffer_store_dwordx4 v[24:27], v8, s[4:7], s64 offen offset:1024 sc1\n"
"v_mfma_f32_16x16x32_bf16 v[212:215], a[88:91], v[120:123], v[212:215]\n"
"buffer_load_dwordx4 v[24:27], v33, s[4:7], s97 offen offset:1024 sc1\n"
"v_mfma_f32_16x16x32_bf16 v[224:227], a[92:95], v[120:123], v[224:227]\n"
"s_waitcnt vmcnt(29)\n"
"v_add_f32_e32 v40, v40, v36\n"
"v_mfma_f32_16x16x32_bf16 v[124:127], a[96:99], v[132:135], v[124:127]\n"
"v_add_f32_e32 v41, v41, v37\n"
"v_add_f32_e32 v42, v42, v38\n"
"v_mfma_f32_16x16x32_bf16 v[140:143], a[100:103], v[132:135], v[140:143]\n"
"v_add_f32_e32 v43, v43, v39\n"
"v_add_f32_e32 v116, v116, v200\n"
"v_mfma_f32_16x16x32_bf16 v[196:199], a[104:107], v[132:135], v[196:199]\n"
"buffer_load_dwordx4 a[204:207], v0, s[4:7], s99 offen offset:3072 sc1\n"
"v_mfma_f32_16x16x32_bf16 v[204:207], a[108:111], v[132:135], v[204:207]\n"
"v_add_f32_e32 v117, v117, v201\n"
"v_add_f32_e32 v118, v118, v202\n"
"v_mfma_f32_16x16x32_bf16 v[212:215], a[112:115], v[132:135], v[212:215]\n"
"buffer_load_dwordx4 v[36:39], v34, s[4:7], s97 offen sc1\n"
"v_mfma_f32_16x16x32_bf16 v[224:227], a[116:119], v[132:135], v[224:227]\n"
"v_add_f32_e32 v119, v119, v203\n"
"v_mul_f32_e32 v40, v40, v255\n"
"v_mfma_f32_16x16x32_bf16 v[124:127], a[120:123], v[48:51], v[124:127]\n"
"v_mul_f32_e32 v41, v41, v255\n"
"v_mul_f32_e32 v42, v42, v255\n"
"v_mfma_f32_16x16x32_bf16 v[140:143], a[124:127], v[48:51], v[140:143]\n"
"v_mul_f32_e32 v43, v43, v255\n"
"v_mul_f32_e32 v116, v116, v255\n"
"v_mfma_f32_16x16x32_bf16 v[196:199], v[68:71], v[48:51], v[196:199]\n"
"v_mul_f32_e32 v117, v117, v255\n"
"v_mul_f32_e32 v118, v118, v255\n"
"v_mfma_f32_16x16x32_bf16 v[204:207], v[72:75], v[48:51], v[204:207]\n"
"v_mul_f32_e32 v119, v119, v255\n"
"v_cvt_pk_bf16_f32 v112, v40, v41\n"
"v_mfma_f32_16x16x32_bf16 v[212:215], v[76:79], v[48:51], v[212:215]\n"
"buffer_load_dwordx4 v[120:123], v34, s[4:7], s97 offen offset:1024 sc1\n"
"v_mfma_f32_16x16x32_bf16 v[224:227], v[80:83], v[48:51], v[224:227]\n"
"s_waitcnt vmcnt(28)\n"
"v_cvt_pk_bf16_f32 v113, v42, v43\n"
"v_mfma_f32_16x16x32_bf16 v[124:127], v[148:151], v[52:55], v[124:127]\n"
"v_cvt_pk_bf16_f32 v114, v116, v117\n"
"v_cvt_pk_bf16_f32 v115, v118, v119\n"
"v_mfma_f32_16x16x32_bf16 v[140:143], v[152:155], v[52:55], v[140:143]\n"
"ds_write_b128 v7, v[112:115] offset:50176\n"
"v_mfma_f32_16x16x32_bf16 v[196:199], v[156:159], v[52:55], v[196:199]\n"
"s_add_u32 s98, s64, 0x4000\n"
"v_mfma_f32_16x16x32_bf16 v[204:207], v[160:163], v[52:55], v[204:207]\n"
"buffer_store_dwordx4 v[112:115], v7, s[4:7], s98 offen offset:1024 sc1\n"
"v_mfma_f32_16x16x32_bf16 v[212:215], v[164:167], v[52:55], v[212:215]\n"
"buffer_load_dwordx4 v[40:43], v35, s[4:7], s97 offen sc1\n"
"v_mfma_f32_16x16x32_bf16 v[224:227], v[168:171], v[52:55], v[224:227]\n"
"v_add_f32_e32 v104, v104, v208\n"
"v_add_f32_e32 v105, v105, v209\n"
"v_mfma_f32_16x16x32_bf16 v[124:127], v[172:175], v[84:87], v[124:127]\n"
"v_add_f32_e32 v106, v106, v210\n"
"v_add_f32_e32 v107, v107, v211\n"
"v_mfma_f32_16x16x32_bf16 v[140:143], v[176:179], v[84:87], v[140:143]\n"
"v_add_f32_e32 v128, v128, v60\n"
"v_add_f32_e32 v129, v129, v61\n"
"v_mfma_f32_16x16x32_bf16 v[196:199], v[180:183], v[84:87], v[196:199]\n"
"buffer_load_dwordx4 a[208:211], v1, s[4:7], s99 offen sc1\n"
"v_mfma_f32_16x16x32_bf16 v[204:207], v[184:187], v[84:87], v[204:207]\n"
"v_add_f32_e32 v130, v130, v62\n"
"v_add_f32_e32 v131, v131, v63\n"
"v_mfma_f32_16x16x32_bf16 v[212:215], v[188:191], v[84:87], v[212:215]\n"
"buffer_load_dwordx4 v[48:51], v35, s[4:7], s97 offen offset:1024 sc1\n"
"v_mfma_f32_16x16x32_bf16 v[224:227], v[192:195], v[84:87], v[224:227]\n"
"s_waitcnt vmcnt(29)\n"
"v_mul_f32_e32 v104, v104, v254\n"
"; @A G1 3.1\n"
"v_mfma_f32_16x16x32_bf16 v[52:55], a[0:3], v[44:47], 0\n"
"v_mul_f32_e32 v105, v105, v254\n"
"v_mul_f32_e32 v106, v106, v254\n"
"v_mfma_f32_16x16x32_bf16 v[60:63], a[4:7], v[44:47], 0\n"
"v_mul_f32_e32 v107, v107, v254\n"
"v_mul_f32_e32 v128, v128, v254\n"
"s_add_u32 s98, s51, 0x800\n"
"v_mfma_f32_16x16x32_bf16 v[84:87], a[8:11], v[44:47], 0\n"
"v_mul_f32_e32 v129, v129, v254\n"
"v_mul_f32_e32 v130, v130, v254\n"
"s_bitcmp1_b32 s26, 0\n"
"s_cbranch_scc1 .Lmk_w9s\n"
"buffer_store_dwordx4 a[8:11], v0, s[4:7], s98 offen sc1\n"
".Lmk_w9r:\n"
"v_mfma_f32_16x16x32_bf16 v[112:115], a[12:15], v[44:47], 0\n"
"ds_write_b128 v3, v[124:127]\n"
"v_mfma_f32_16x16x32_bf16 v[116:119], a[16:19], v[44:47], 0\n"
"s_add_u32 s98, s55, 0\n"
"v_mul_f32_e32 v131, v131, v254\n"
"v_mfma_f32_16x16x32_bf16 v[124:127], a[20:23], v[44:47], 0\n"
"buffer_load_dwordx4 v[44:47], v32, s[4:7], s98 offen sc1\n"
"v_mfma_f32_16x16x32_bf16 v[52:55], a[24:27], v[56:59], v[52:55]\n"
"ds_write_b128 v3, v[140:143] offset:1024\n"
"v_mfma_f32_16x16x32_bf16 v[60:63], a[28:31], v[56:59], v[60:63]\n"
"v_cvt_pk_bf16_f32 v132, v104, v105\n"
"v_cvt_pk_bf16_f32 v133, v106, v107\n"
"v_mfma_f32_16x16x32_bf16 v[84:87], a[32:35], v[56:59], v[84:87]\n"
"v_cvt_pk_bf16_f32 v134, v128, v129\n"
"v_cvt_pk_bf16_f32 v135, v130, v131\n"
"v_mfma_f32_16x16x32_bf16 v[112:115], a[36:39], v[56:59], v[112:115]\n"
"ds_write_b128 v3, v[196:199] offset:2048\n"
"v_mfma_f32_16x16x32_bf16 v[116:119], a[40:43], v[56:59], v[116:119]\n"
"buffer_load_dwordx4 v[104:107], v32, s[4:7], s98 offen offset:1024 sc1\n"
"v_mfma_f32_16x16x32_bf16 v[124:127], a[44:47], v[56:59], v[124:127]\n"
"s_waitcnt vmcnt(29)\n"
"v_mfma_f32_16x16x32_bf16 v[52:55], a[48:51], v[92:95], v[52:55]\n"
"ds_write_b128 v3, v[204:207] offset:3072\n"
"v_mfma_f32_16x16x32_bf16 v[60:63], a[52:55], v[92:95], v[60:63]\n"
"ds_write_b128 v2, v[132:135] offset:17408\n"
"v_mfma_f32_16x16x32_bf16 v[84:87], a[56:59], v[92:95], v[84:87]\n"
"buffer_load_dwordx4 a[212:215], v1, s[4:7], s99 offen offset:1024 sc1\n"
"v_mfma_f32_16x16x32_bf16 v[112:115], a[60:63], v[92:95], v[112:115]\n"
"ds_write_b128 v3, v[212:215] offset:4096\n"
"v_mfma_f32_16x16x32_bf16 v[116:119], a[64:67], v[92:95], v[116:119]\n"
"buffer_load_dwordx4 v[56:59], v33, s[4:7], s98 offen sc1\n"
"v_mfma_f32_16x16x32_bf16 v[124:127], a[68:71], v[92:95], v[124:127]\n"
"v_mfma_f32_16x16x32_bf16 v[52:55], a[72:75], v[28:31], v[52:55]\n"
"ds_write_b128 v3, v[224:227] offset:5120\n"
"v_mfma_f32_16x16x32_bf16 v[60:63], a[76:79], v[28:31], v[60:63]\n"
"v_mfma_f32_16x16x32_bf16 v[84:87], a[80:83], v[28:31], v[84:87]\n"
"v_mfma_f32_16x16x32_bf16 v[112:115], a[84:87], v[28:31], v[112:115]\n"
"v_mfma_f32_16x16x32_bf16 v[116:119], a[88:91], v[28:31], v[116:119]\n"
"buffer_load_dwordx4 v[92:95], v33, s[4:7], s98 offen offset:1024 sc1\n"
"v_mfma_f32_16x16x32_bf16 v[124:127], a[92:95], v[28:31], v[124:127]\n"
"s_waitcnt vmcnt(28)\n"
"v_mfma_f32_16x16x32_bf16 v[52:55], a[96:99], v[144:147], v[52:55]\n"
"s_add_u32 s97, s51, 0xc00\n"
"s_bitcmp1_b32 s26, 0\n"
"s_cbranch_scc1 .Lmk_w10s\n"
"buffer_store_dwordx4 a[12:15], v0, s[4:7], s97 offen sc1\n"
".Lmk_w10r:\n"
"v_mfma_f32_16x16x32_bf16 v[60:63], a[100:103], v[144:147], v[60:63]\n"
"v_mfma_f32_16x16x32_bf16 v[84:87], a[104:107], v[144:147], v[84:87]\n"
"v_mfma_f32_16x16x32_bf16 v[112:115], a[108:111], v[144:147], v[112:115]\n"
"v_mfma_f32_16x16x32_bf16 v[116:119], a[112:115], v[144:147], v[116:119]\n"
"buffer_load_dwordx4 v[28:31], v34, s[4:7], s98 offen sc1\n"
"v_mfma_f32_16x16x32_bf16 v[124:127], a[116:119], v[144:147], v[124:127]\n"
"s_waitcnt lgkmcnt(0)\n"
"s_barrier\n"
"v_mfma_f32_16x16x32_bf16 v[52:55], a[120:123], v[220:223], v[52:55]\n"
"s_waitcnt vmcnt(20)\n"
"v_add_f32_e32 v252, v252, v250\n"
"v_mfma_f32_16x16x32_bf16 v[60:63], a[124:127], v[220:223], v[60:63]\n"
"v_add_f32_e32 v253, v253, v251\n"
"v_add_f32_e32 v248, v248, v246\n"
"v_mfma_f32_16x16x32_bf16 v[84:87], v[68:71], v[220:223], v[84:87]\n"
"buffer_load_dwordx4 a[216:219], v1, s[4:7], s99 offen offset:2048 sc1\n"
"v_mfma_f32_16x16x32_bf16 v[112:115], v[72:75], v[220:223], v[112:115]\n"
"v_add_f32_e32 v249, v249, v247\n"
"v_add_f32_e32 v252, v252, v248\n"
"v_mfma_f32_16x16x32_bf16 v[116:119], v[76:79], v[220:223], v[116:119]\n"
"buffer_load_dwordx4 v[128:131], v34, s[4:7], s98 offen offset:1024 sc1\n"
"v_mfma_f32_16x16x32_bf16 v[124:127], v[80:83], v[220:223], v[124:127]\n"
"v_add_f32_e32 v253, v253, v249\n"
"v_mfma_f32_16x16x32_bf16 v[52:55], v[148:151], v[96:99], v[52:55]\n"
"s_nop 0\n"
"v_permlane16_swap_b32_e32 v252, v253\n"
"v_add_f32_e32 v252, v252, v253\n"
"v_mfma_f32_16x16x32_bf16 v[60:63], v[152:155], v[96:99], v[60:63]\n"
"v_mov_b32_e32 v253, v252\n"
"v_mfma_f32_16x16x32_bf16 v[84:87], v[156:159], v[96:99], v[84:87]\n"
"s_add_u32 s97, s75, 0x1000\n"
"v_permlane32_swap_b32_e32 v252, v253\n"
"v_mfma_f32_16x16x32_bf16 v[112:115], v[160:163], v[96:99], v[112:115]\n"
"buffer_load_dwordx2 v[254:255], v13, s[4:7], s97 offen sc1\n"
"v_mfma_f32_16x16x32_bf16 v[116:119], v[164:167], v[96:99], v[116:119]\n"
"buffer_load_dwordx4 v[132:135], v35, s[4:7], s98 offen sc1\n"
"v_mfma_f32_16x16x32_bf16 v[124:127], v[168:171], v[96:99], v[124:127]\n"
"v_add_f32_e32 v252, v252, v253\n"
"v_mul_f32_e32 v252, 0x3a800000, v252\n"
"v_mfma_f32_16x16x32_bf16 v[52:55], v[172:175], v[100:103], v[52:55]\n"
"s_add_u32 s96, s71, 0\n"
"v_mov_b32_e32 v253, v252\n"
"v_mfma_f32_16x16x32_bf16 v[60:63], v[176:179], v[100:103], v[60:63]\n"
"buffer_load_dwordx2 v[250:251], off, s[4:7], s96 sc1\n"
"v_mfma_f32_16x16x32_bf16 v[84:87], v[180:183], v[100:103], v[84:87]\n"
"v_permlane16_swap_b32_e32 v252, v253\n"
"v_cvt_pk_bf16_f32 v249, -v252, 0\n"
"v_mfma_f32_16x16x32_bf16 v[112:115], v[184:187], v[100:103], v[112:115]\n"
"v_fma_f32 v253, -v252, v252, v253\n"
"v_lshlrev_b32_e32 v249, 16, v249\n"
"v_mfma_f32_16x16x32_bf16 v[116:119], v[188:191], v[100:103], v[116:119]\n"
"buffer_load_dwordx4 v[96:99], v35, s[4:7], s98 offen offset:1024 sc1\n"
"v_mfma_f32_16x16x32_bf16 v[124:127], v[192:195], v[100:103], v[124:127]\n"
"s_waitcnt vmcnt(24)\n"
"ds_read_b128 v[32:35], v4\n"
"; @A G1 3.2\n"
"v_mfma_f32_16x16x32_bf16 v[100:103], a[0:3], v[64:67], 0\n"
"ds_read_b128 v[136:139], v9\n"
"ds_read_b128 v[140:143], v4 offset:1024\n"
"s_add_u32 s98, s51, 0x1000\n"
"v_mfma_f32_16x16x32_bf16 v[144:147], a[4:7], v[64:67], 0\n"
"ds_read_b128 v[196:199], v9 offset:64\n"
"ds_read_b128 v[200:203], v4 offset:2048\n"
"s_bitcmp1_b32 s26, 0\n"
"s_cbranch_scc1 .Lmk_w11s\n"
"buffer_store_dwordx4 a[16:19], v0, s[4:7], s98 offen sc1\n"
".Lmk_w11r:\n"
"v_mfma_f32_16x16x32_bf16 v[204:207], a[8:11], v[64:67], 0\n"
"buffer_load_dwordx4 a[220:223], v1, s[4:7], s99 offen offset:3072 sc1\n"
"v_mfma_f32_16x16x32_bf16 v[208:211], a[12:15], v[64:67], 0\n"
"ds_write_b128 v3, v[52:55] offset:6144\n"
"v_mfma_f32_16x16x32_bf16 v[52:55], a[16:19], v[64:67], 0\n"
"s_add_u32 s99, s70, 0x6000\n"
"ds_read_b128 v[212:215], v9 offset:128\n"
"v_mfma_f32_16x16x32_bf16 v[216:219], a[20:23], v[64:67], 0\n"
"ds_read_b128 v[64:67], v4 offset:3072\n"
"ds_read_b128 v[220:223], v9 offset:192\n"
"v_mfma_f32_16x16x32_bf16 v[100:103], a[24:27], v[88:91], v[100:103]\n"
"ds_write_b128 v3, v[60:63] offset:7168\n"
"v_mfma_f32_16x16x32_bf16 v[144:147], a[28:31], v[88:91], v[144:147]\n"
"buffer_load_dwordx4 a[224:227], v0, s[4:7], s99 offen sc1\n"
"v_mfma_f32_16x16x32_bf16 v[204:207], a[32:35], v[88:91], v[204:207]\n"
"ds_read_b128 v[60:63], v4 offset:4096\n"
"ds_read_b128 v[224:227], v9 offset:256\n"
"v_mfma_f32_16x16x32_bf16 v[208:211], a[36:39], v[88:91], v[208:211]\n"
"ds_write_b128 v3, v[84:87] offset:8192\n"
"v_mfma_f32_16x16x32_bf16 v[52:55], a[40:43], v[88:91], v[52:55]\n"
"s_waitcnt lgkmcnt(6)\n"
"v_fmac_f32_e32 v32, v249, v136\n"
"v_mfma_f32_16x16x32_bf16 v[216:219], a[44:47], v[88:91], v[216:219]\n"
"s_waitcnt vmcnt(24)\n"
"v_fmac_f32_e32 v33, v249, v137\n"
"v_mfma_f32_16x16x32_bf16 v[100:103], a[48:51], v[108:111], v[100:103]\n"
"ds_write_b128 v3, v[112:115] offset:9216\n"
"v_mfma_f32_16x16x32_bf16 v[144:147], a[52:55], v[108:111], v[144:147]\n"
"buffer_load_dwordx4 a[228:231], v0, s[4:7], s99 offen offset:1024 sc1\n"
"v_mfma_f32_16x16x32_bf16 v[204:207], a[56:59], v[108:111], v[204:207]\n"
"v_fmac_f32_e32 v34, v249, v138\n"
"v_fmac_f32_e32 v35, v249, v139\n"
"v_mfma_f32_16x16x32_bf16 v[208:211], a[60:63], v[108:111], v[208:211]\n"
"ds_write_b128 v3, v[116:119] offset:10240\n"
"v_mfma_f32_16x16x32_bf16 v[52:55], a[64:67], v[108:111], v[52:55]\n"
"v_fmac_f32_e32 v140, v249, v196\n"
"v_fmac_f32_e32 v141, v249, v197\n"
"v_mfma_f32_16x16x32_bf16 v[216:219], a[68:71], v[108:111], v[216:219]\n"
"buffer_load_dwordx4 a[232:235], v0, s[4:7], s99 offen offset:2048 sc1\n"
"v_mfma_f32_16x16x32_bf16 v[100:103], a[72:75], v[24:27], v[100:103]\n"
"ds_write_b128 v3, v[124:127] offset:11264\n"
"v_mfma_f32_16x16x32_bf16 v[144:147], a[76:79], v[24:27], v[144:147]\n"
"v_fmac_f32_e32 v142, v249, v198\n"
"v_fmac_f32_e32 v143, v249, v199\n"
"v_mfma_f32_16x16x32_bf16 v[204:207], a[80:83], v[24:27], v[204:207]\n"
"v_fmac_f32_e32 v200, v249, v212\n"
"v_fmac_f32_e32 v201, v249, v213\n"
"v_mfma_f32_16x16x32_bf16 v[208:211], a[84:87], v[24:27], v[208:211]\n"
"buffer_load_dwordx4 a[236:239], v0, s[4:7], s99 offen offset:3072 sc1\n"
"v_mfma_f32_16x16x32_bf16 v[52:55], a[88:91], v[24:27], v[52:55]\n"
"v_fmac_f32_e32 v202, v249, v214\n"
"v_fmac_f32_e32 v203, v249, v215\n"
"v_mfma_f32_16x16x32_bf16 v[216:219], a[92:95], v[24:27], v[216:219]\n"
"s_waitcnt vmcnt(24)\n"
"ds_read_b128 v[24:27], v4 offset:5120\n"
"v_mfma_f32_16x16x32_bf16 v[100:103], a[96:99], v[36:39], v[100:103]\n"
"ds_read_b128 v[84:87], v9 offset:320\n"
"v_max_f32_e32 v253, 0, v253\n"
"s_add_u32 s98, s51, 0x1400\n"
"v_mfma_f32_16x16x32_bf16 v[144:147], a[100:103], v[36:39], v[144:147]\n"
"buffer_load_dwordx4 a[240:243], v1, s[4:7], s99 offen sc1\n"
"s_bitcmp1_b32 s26, 0\n"
"s_cbranch_scc1 .Lmk_w12s\n"
"buffer_store_dwordx4 a[20:23], v0, s[4:7], s98 offen sc1\n"
".Lmk_w12r:\n"
"v_mfma_f32_16x16x32_bf16 v[204:207], a[104:107], v[36:39], v[204:207]\n"
"v_cvt_pk_bf16_f32 v253, v253, 0\n"
"v_lshlrev_b32_e32 v253, 16, v253\n"
"v_mfma_f32_16x16x32_bf16 v[208:211], a[108:111], v[36:39], v[208:211]\n"
"v_add_f32_e32 v253, 0x3727c5ac, v253\n"
"v_cvt_pk_bf16_f32 v253, v253, 0\n"
"v_mfma_f32_16x16x32_bf16 v[52:55], a[112:115], v[36:39], v[52:55]\n"
"v_lshlrev_b32_e32 v253, 16, v253\n"
"s_mov_b32 s20, 0xff0000\n"
"v_mfma_f32_16x16x32_bf16 v[216:219], a[116:119], v[36:39], v[216:219]\n"
"buffer_load_dwordx4 a[244:247], v1, s[4:7], s99 offen offset:1024 sc1\n"
"v_mfma_f32_16x16x32_bf16 v[100:103], a[120:123], v[120:123], v[100:103]\n"
"s_waitcnt lgkmcnt(0)\n"
"v_fmac_f32_e32 v64, v249, v220\n"
"v_mfma_f32_16x16x32_bf16 v[144:147], a[124:127], v[120:123], v[144:147]\n"
"v_fmac_f32_e32 v65, v249, v221\n"
"v_fmac_f32_e32 v66, v249, v222\n"
"v_mfma_f32_16x16x32_bf16 v[204:207], v[68:71], v[120:123], v[204:207]\n"
"v_fmac_f32_e32 v67, v249, v223\n"
"v_fmac_f32_e32 v60, v249, v224\n"
"v_mfma_f32_16x16x32_bf16 v[208:211], v[72:75], v[120:123], v[208:211]\n"
"buffer_load_dwordx4 a[248:251], v1, s[4:7], s99 offen offset:2048 sc1\n"
"v_mfma_f32_16x16x32_bf16 v[52:55], v[76:79], v[120:123], v[52:55]\n"
"v_fmac_f32_e32 v61, v249, v225\n"
"v_fmac_f32_e32 v62, v249, v226\n"
"v_mfma_f32_16x16x32_bf16 v[216:219], v[80:83], v[120:123], v[216:219]\n"
"s_waitcnt vmcnt(24)\n"
"v_fmac_f32_e32 v63, v249, v227\n"
"v_mfma_f32_16x16x32_bf16 v[100:103], v[148:151], v[40:43], v[100:103]\n"
"s_waitcnt vmcnt(13)\n"
"v_cmp_ne_u64_e32 vcc, v[254:255], v[18:19]\n"
"v_mfma_f32_16x16x32_bf16 v[144:147], v[152:155], v[40:43], v[144:147]\n"
"s_cbranch_vccnz .Lmk_f6s\n"
".Lmk_f6r:\n"
"v_fmac_f32_e32 v24, v249, v84\n"
"v_mfma_f32_16x16x32_bf16 v[204:207], v[156:159], v[40:43], v[204:207]\n"
"buffer_load_dwordx4 a[252:255], v1, s[4:7], s99 offen offset:3072 sc1\n"
"v_mfma_f32_16x16x32_bf16 v[208:211], v[160:163], v[40:43], v[208:211]\n"
"v_fmac_f32_e32 v25, v249, v85\n"
"v_fmac_f32_e32 v26, v249, v86\n"
"v_mfma_f32_16x16x32_bf16 v[52:55], v[164:167], v[40:43], v[52:55]\n"
"v_fmac_f32_e32 v27, v249, v87\n"
"s_mov_b32 s21, 0xffffffff\n"
"v_mfma_f32_16x16x32_bf16 v[216:219], v[168:171], v[40:43], v[216:219]\n"
"v_sqrt_f32_e32 v253, v253\n"
"v_mfma_f32_16x16x32_bf16 v[100:103], v[172:175], v[48:51], v[100:103]\n"
"v_cvt_pk_bf16_f32 v253, v253, 0\n"
"v_lshlrev_b32_e32 v253, 16, v253\n"
"v_mfma_f32_16x16x32_bf16 v[144:147], v[176:179], v[48:51], v[144:147]\n"
"v_rcp_f32_e32 v255, v253\n"
"v_mfma_f32_16x16x32_bf16 v[204:207], v[180:183], v[48:51], v[204:207]\n"
"s_waitcnt vmcnt(12)\n"
"v_cmp_ne_u64_e32 vcc, v[250:251], v[18:19]\n"
"v_mfma_f32_16x16x32_bf16 v[208:211], v[184:187], v[48:51], v[208:211]\n"
"s_cbranch_vccnz .Lmk_f3s\n"
".Lmk_f3r:\n"
"s_add_u32 m0, s77, 0x6000\n"
"v_mfma_f32_16x16x32_bf16 v[52:55], v[188:191], v[48:51], v[52:55]\n"
"s_add_u32 s99, s65, 0x2000\n"
"v_mul_f32_e32 v254, 0x3e8293ee, v255\n"
"v_mfma_f32_16x16x32_bf16 v[216:219], v[192:195], v[48:51], v[216:219]\n"
"s_mov_b32 s22, 0x1ff0001\n"
"; @A G1 3.3\n"
"v_mfma_f32_16x16x32_bf16 v[32:35], a[0:3], v[44:47], v[32:35]\n"
"buffer_load_dwordx4 v2, s[4:7], s99 offen sc1 lds\n"
"v_mfma_f32_16x16x32_bf16 v[140:143], a[4:7], v[44:47], v[140:143]\n"
"buffer_load_dwordx4 v2, s[4:7], s99 offen offset:1024 sc1 lds\n"
"v_mfma_f32_16x16x32_bf16 v[200:203], a[8:11], v[44:47], v[200:203]\n"
"s_mov_b32 s23, 0xffffffff\n"
"s_mov_b32 s24, 0x3ff0003\n"
"v_mfma_f32_16x16x32_bf16 v[64:67], a[12:15], v[44:47], v[64:67]\n"
"ds_write_b128 v3, v[100:103] offset:12288\n"
"v_mfma_f32_16x16x32_bf16 v[60:63], a[16:19], v[44:47], v[60:63]\n"
"s_mov_b32 s25, 0xffffffff\n"
"s_mov_b32 s26, 0x7ff0007\n"
"v_mfma_f32_16x16x32_bf16 v[24:27], a[20:23], v[44:47], v[24:27]\n"
"s_mov_b32 s27, 0xffffffff\n"
"s_mov_b32 s28, 0xfff000f\n"
"v_mfma_f32_16x16x32_bf16 v[32:35], a[24:27], v[104:107], v[32:35]\n"
"ds_write_b128 v3, v[144:147] offset:13312\n"
"v_mfma_f32_16x16x32_bf16 v[140:143], a[28:31], v[104:107], v[140:143]\n"
"s_add_u32 m0, s77, 0xe000\n"
"s_add_u32 s99, s65, 0x6000\n"
"v_mfma_f32_16x16x32_bf16 v[200:203], a[32:35], v[104:107], v[200:203]\n"
"buffer_load_dwordx4 v2, s[4:7], s99 offen sc1 lds\n"
"v_mfma_f32_16x16x32_bf16 v[64:67], a[36:39], v[104:107], v[64:67]\n"
"ds_write_b128 v3, v[204:207] offset:14336\n"
"v_mfma_f32_16x16x32_bf16 v[60:63], a[40:43], v[104:107], v[60:63]\n"
"buffer_load_dwordx4 v2, s[4:7], s99 offen offset:1024 sc1 lds\n"
"v_mfma_f32_16x16x32_bf16 v[24:27], a[44:47], v[104:107], v[24:27]\n"
"s_mov_b32 s29, 0xffffffff\n"
"v_mfma_f32_16x16x32_bf16 v[32:35], a[48:51], v[56:59], v[32:35]\n"
"ds_write_b128 v3, v[208:211] offset:15360\n"
"v_mfma_f32_16x16x32_bf16 v[140:143], a[52:55], v[56:59], v[140:143]\n"
"s_mov_b32 s30, 0x1fff001f\n"
"s_mov_b32 s31, 0xffffffff\n"
"v_mfma_f32_16x16x32_bf16 v[200:203], a[56:59], v[56:59], v[200:203]\n"
"s_mov_b32 s32, 0x3fff003f\n"
"s_mov_b32 s33, 0xffffffff\n"
"v_mfma_f32_16x16x32_bf16 v[64:67], a[60:63], v[56:59], v[64:67]\n"
"ds_write_b128 v3, v[52:55] offset:16384\n"
"v_mfma_f32_16x16x32_bf16 v[60:63], a[64:67], v[56:59], v[60:63]\n"
"s_add_u32 s99, s69, 0x600000\n"
"s_mov_b32 s34, 0x7fff007f\n"
"v_mfma_f32_16x16x32_bf16 v[24:27], a[68:71], v[56:59], v[24:27]\n"
"buffer_load_dwordx4 a[0:3], v0, s[4:7], s99 offen sc1\n"
"v_mfma_f32_16x16x32_bf16 v[32:35], a[72:75], v[92:95], v[32:35]\n"
"ds_write_b128 v3, v[216:219] offset:17408\n"
"v_mfma_f32_16x16x32_bf16 v[140:143], a[76:79], v[92:95], v[140:143]\n"
"s_mov_b32 s35, 0xffffffff\n"
"s_mov_b32 s36, 0\n"
"v_mfma_f32_16x16x32_bf16 v[200:203], a[80:83], v[92:95], v[200:203]\n"
"s_mov_b32 s37, 0xff0000\n"
"s_mov_b32 s38, 0\n"
"v_mfma_f32_16x16x32_bf16 v[64:67], a[84:87], v[92:95], v[64:67]\n"
"s_waitcnt lgkmcnt(0)\n"
"s_barrier\n"
"v_mfma_f32_16x16x32_bf16 v[60:63], a[88:91], v[92:95], v[60:63]\n"
"buffer_load_dwordx4 a[4:7], v0, s[4:7], s99 offen offset:1024 sc1\n"
"v_mfma_f32_16x16x32_bf16 v[24:27], a[92:95], v[92:95], v[24:27]\n"
"ds_read_b128 v[36:39], v5 offset:2048\n"
"v_mfma_f32_16x16x32_bf16 v[32:35], a[96:99], v[28:31], v[32:35]\n"
"ds_read_b128 v[40:43], v6 offset:2048\n"
"ds_read_b128 v[44:47], v5 offset:3072\n"
"v_mfma_f32_16x16x32_bf16 v[140:143], a[100:103], v[28:31], v[140:143]\n"
"ds_read_b128 v[48:51], v6 offset:3072\n"
"s_mov_b32 s39, 0x1ff0001\n"
"v_mfma_f32_16x16x32_bf16 v[200:203], a[104:107], v[28:31], v[200:203]\n"
"s_mov_b32 s40, 0\n"
"s_mov_b32 s41, 0x3ff0003\n"
"v_mfma_f32_16x16x32_bf16 v[64:67], a[108:111], v[28:31], v[64:67]\n"
"buffer_load_dwordx4 a[8:11], v0, s[4:7], s99 offen offset:2048 sc1\n"
"v_mfma_f32_16x16x32_bf16 v[60:63], a[112:115], v[28:31], v[60:63]\n"
"ds_read_b128 v[52:55], v5 offset:4096\n"
"ds_read_b128 v[56:59], v6 offset:4096\n"
"v_mfma_f32_16x16x32_bf16 v[24:27], a[116:119], v[28:31], v[24:27]\n"
"ds_read_b128 v[28:31], v5 offset:5120\n"
"ds_read_b128 v[84:87], v6 offset:5120\n"
"v_mfma_f32_16x16x32_bf16 v[32:35], a[120:123], v[128:131], v[32:35]\n"
"s_waitcnt lgkmcnt(4)\n"
"v_add_f32_e32 v36, v36, v40\n"
"v_mfma_f32_16x16x32_bf16 v[140:143], a[124:127], v[128:131], v[140:143]\n"
"v_add_f32_e32 v37, v37, v41\n"
"v_add_f32_e32 v38, v38, v42\n"
"v_mfma_f32_16x16x32_bf16 v[200:203], v[68:71], v[128:131], v[200:203]\n"
"buffer_load_dwordx4 a[12:15], v0, s[4:7], s99 offen offset:3072 sc1\n"
"v_mfma_f32_16x16x32_bf16 v[64:67], v[72:75], v[128:131], v[64:67]\n"
"ds_read_b128 v[68:71], v5\n"
"ds_read_b128 v[72:75], v6\n"
"v_mfma_f32_16x16x32_bf16 v[60:63], v[76:79], v[128:131], v[60:63]\n"
"ds_read_b128 v[76:79], v5 offset:1024\n"
"ds_read_b128 v[88:91], v6 offset:1024\n"
"v_mfma_f32_16x16x32_bf16 v[24:27], v[80:83], v[128:131], v[24:27]\n"
"s_waitcnt vmcnt(19)\n"
"v_add_f32_e32 v39, v39, v43\n"
"v_mfma_f32_16x16x32_bf16 v[32:35], v[148:151], v[132:135], v[32:35]\n"
"v_add_f32_e32 v44, v44, v48\n"
"v_add_f32_e32 v45, v45, v49\n"
"v_mfma_f32_16x16x32_bf16 v[140:143], v[152:155], v[132:135], v[140:143]\n"
"buffer_load_dwordx4 a[16:19], v1, s[4:7], s99 offen sc1\n"
"v_mfma_f32_16x16x32_bf16 v[200:203], v[156:159], v[132:135], v[200:203]\n"
"v_add_f32_e32 v46, v46, v50\n"
"v_add_f32_e32 v47, v47, v51\n"
"v_mfma_f32_16x16x32_bf16 v[64:67], v[160:163], v[132:135], v[64:67]\n"
"s_waitcnt lgkmcnt(4)\n"
"v_add_f32_e32 v52, v52, v56\n"
"v_mfma_f32_16x16x32_bf16 v[60:63], v[164:167], v[132:135], v[60:63]\n"
"v_add_f32_e32 v53, v53, v57\n"
"v_add_f32_e32 v54, v54, v58\n"
"v_mfma_f32_16x16x32_bf16 v[24:27], v[168:171], v[132:135], v[24:27]\n"
"v_add_f32_e32 v55, v55, v59\n"
"v_add_f32_e32 v28, v28, v84\n"
"v_mfma_f32_16x16x32_bf16 v[32:35], v[172:175], v[96:99], v[32:35]\n"
"buffer_load_dwordx4 a[20:23], v1, s[4:7], s99 offen offset:1024 sc1\n"
"v_mfma_f32_16x16x32_bf16 v[140:143], v[176:179], v[96:99], v[140:143]\n"
"v_add_f32_e32 v29, v29, v85\n"
"v_add_f32_e32 v30, v30, v86\n"
"v_mfma_f32_16x16x32_bf16 v[200:203], v[180:183], v[96:99], v[200:203]\n"
"v_add_f32_e32 v31, v31, v87\n"
"s_waitcnt lgkmcnt(0)\n"
"v_mfma_f32_16x16x32_bf16 v[64:67], v[184:187], v[96:99], v[64:67]\n"
"v_add_f32_e32 v68, v68, v72\n"
"v_add_f32_e32 v69, v69, v73\n"
"v_mfma_f32_16x16x32_bf16 v[60:63], v[188:191], v[96:99], v[60:63]\n"
"v_add_f32_e32 v70, v70, v74\n"
"v_add_f32_e32 v71, v71, v75\n"
"v_mfma_f32_16x16x32_bf16 v[24:27], v[192:195], v[96:99], v[24:27]\n"
"buffer_load_dwordx4 a[24:27], v1, s[4:7], s99 offen offset:2048 sc1\n"
"v_add_f32_e32 v76, v76, v88\n"
"v_add_f32_e32 v77, v77, v89\n"
"v_add_f32_e32 v78, v78, v90\n"
"v_add_f32_e32 v79, v79, v91\n"
"s_mov_b32 s42, 0\n"
"s_mov_b32 s43, 0x7ff0007\n"
"s_mov_b32 s44, 0\n"
"s_mov_b32 s45, 0xfff000f\n"
"s_mov_b32 s46, 0\n"
"s_mov_b32 s47, 0x1fff001f\n"
"s_mov_b32 s48, 0\n"
"s_mov_b32 s49, 0x3fff003f\n"
"s_mov_b32 s50, 0\n"
"s_mov_b32 s51, 0x7fff007f\n"
"v_xor_b32_e32 v253, 32, v16\n"
"v_xor_b32_e32 v252, 32, v15\n"
"v_mov_b32_e32 v88, 0x3f803f80\n"
"v_mov_b32_e32 v89, 0x3f803f80\n"
"v_mov_b32_e32 v90, 0x3f803f80\n"
"v_mov_b32_e32 v91, 0x3f803f80\n"
"v_mov_b32_e32 v251, 0x53800000\n"
"buffer_load_dwordx4 a[28:31], v1, s[4:7], s99 offen offset:3072 sc1\n"
"; @A tail\n"
"v_add_f32_e32 v200, v200, v36\n"
"v_add_f32_e32 v201, v201, v37\n"
"v_add_f32_e32 v202, v202, v38\n"
"v_add_f32_e32 v203, v203, v39\n"
"v_add_f32_e32 v64, v64, v44\n"
"v_add_f32_e32 v65, v65, v45\n"
"v_add_f32_e32 v66, v66, v46\n"
"v_add_f32_e32 v67, v67, v47\n"
"v_mul_f32_e32 v200, v200, v255\n"
"v_mul_f32_e32 v201, v201, v255\n"
"v_mul_f32_e32 v202, v202, v255\n"
"v_mul_f32_e32 v203, v203, v255\n"
"v_mul_f32_e32 v64, v64, v255\n"
"v_mul_f32_e32 v65, v65, v255\n"
"v_mul_f32_e32 v66, v66, v255\n"
"v_mul_f32_e32 v67, v67, v255\n"
"v_cvt_pk_bf16_f32 v36, v200, v201\n"
"v_cvt_pk_bf16_f32 v37, v202, v203\n"
"v_cvt_pk_bf16_f32 v38, v64, v65\n"
"v_cvt_pk_bf16_f32 v39, v66, v67\n"
"ds_write_b128 v7, v[36:39] offset:16384\n"
"buffer_store_dwordx4 v[36:39], v7, s[4:7], s64 offen sc1\n"
"v_add_f32_e32 v60, v60, v52\n"
"v_add_f32_e32 v61, v61, v53\n"
"v_add_f32_e32 v62, v62, v54\n"
"v_add_f32_e32 v63, v63, v55\n"
"v_add_f32_e32 v24, v24, v28\n"
"v_add_f32_e32 v25, v25, v29\n"
"v_add_f32_e32 v26, v26, v30\n"
"v_add_f32_e32 v27, v27, v31\n"
"v_mul_f32_e32 v60, v60, v255\n"
"v_mul_f32_e32 v61, v61, v255\n"
"v_mul_f32_e32 v62, v62, v255\n"
"v_mul_f32_e32 v63, v63, v255\n"
"v_mul_f32_e32 v24, v24, v255\n"
"v_mul_f32_e32 v25, v25, v255\n"
"v_mul_f32_e32 v26, v26, v255\n"
"v_mul_f32_e32 v27, v27, v255\n"
"v_cvt_pk_bf16_f32 v28, v60, v61\n"
"v_cvt_pk_bf16_f32 v29, v62, v63\n"
"v_cvt_pk_bf16_f32 v30, v24, v25\n"
"v_cvt_pk_bf16_f32 v31, v26, v27\n"
"ds_write_b128 v7, v[28:31] offset:49152\n"
"s_add_u32 s99, s69, 0x602000\n"
"buffer_load_dwordx4 a[32:35], v0, s[4:7], s99 offen sc1\n"
"s_add_u32 s98, s64, 0x4000\n"
"buffer_store_dwordx4 v[28:31], v7, s[4:7], s98 offen sc1\n"
"v_add_f32_e32 v32, v32, v68\n"
"v_add_f32_e32 v33, v33, v69\n"
"v_add_f32_e32 v34, v34, v70\n"
"v_add_f32_e32 v35, v35, v71\n"
"v_add_f32_e32 v140, v140, v76\n"
"buffer_load_dwordx4 a[36:39], v0, s[4:7], s99 offen offset:1024 sc1\n"
"v_add_f32_e32 v141, v141, v77\n"
"v_add_f32_e32 v142, v142, v78\n"
"v_add_f32_e32 v143, v143, v79\n"
"v_mul_f32_e32 v32, v32, v254\n"
"v_mul_f32_e32 v33, v33, v254\n"
"v_mul_f32_e32 v34, v34, v254\n"
"v_mul_f32_e32 v35, v35, v254\n"
"v_mul_f32_e32 v140, v140, v254\n"
"v_mul_f32_e32 v141, v141, v254\n"
"v_mul_f32_e32 v142, v142, v254\n"
"buffer_load_dwordx4 a[40:43], v0, s[4:7], s99 offen offset:2048 sc1\n"
"v_mul_f32_e32 v143, v143, v254\n"
"v_cvt_pk_bf16_f32 v24, v32, v33\n"
"v_cvt_pk_bf16_f32 v25, v34, v35\n"
"v_cvt_pk_bf16_f32 v26, v140, v141\n"
"v_cvt_pk_bf16_f32 v27, v142, v143\n"
"ds_write_b128 v2, v[24:27] offset:16384\n"
"s_waitcnt lgkmcnt(0)\n"
"s_barrier\n"
"ds_read_b128 v[24:27], v2 offset:49152\n"
"ds_read_b128 v[28:31], v2 offset:50176\n"
"ds_read_b128 v[92:95], v2 offset:16384\n"
"ds_read_b128 v[96:99], v2 offset:17408\n"
"; @A AT b\n"
"s_mov_b32 s2, 0\n"
"v_mov_b32_e32 v255, 0xff800000\n"
"v_mov_b32_e32 v254, 0xff800000\n"
"v_mov_b32_e32 v250, 0x7f800000\n"
"v_mov_b32_e32 v249, 0x7f800000\n"
".Lmk_a19:\n"
"ds_read_b128 v[32:35], v15 offset:24576\n"
"ds_read_b128 v[36:39], v252 offset:24832\n"
"ds_read_b128 v[40:43], v15 offset:22528\n"
"ds_read_b128 v[44:47], v252 offset:22784\n"
"s_cmp_gt_i32 s78, 11\n"
"s_cselect_b32 s98, 0x80000000, 0\n"
"s_or_b32 s98, s98, 0x7f800000\n"
"buffer_load_dwordx4 a[44:47], v0, s[4:7], s99 offen offset:3072 sc1\n"
"buffer_load_dwordx4 a[48:51], v1, s[4:7], s99 offen sc1\n"
"s_waitcnt lgkmcnt(2)\n"
"v_mfma_f32_16x16x32_bf16 v[48:51], v[32:35], v[24:27], 0\n"
"buffer_load_dwordx4 a[52:55], v1, s[4:7], s99 offen offset:1024 sc1\n"
"v_mfma_f32_16x16x32_bf16 v[52:55], v[32:35], v[28:31], 0\n"
"v_mfma_f32_16x16x32_bf16 v[32:35], v[36:39], v[24:27], 0\n"
"v_mfma_f32_16x16x32_bf16 v[56:59], v[36:39], v[28:31], 0\n"
"s_nop 3\n"
"v_max_f32_e32 v248, v48, v49\n"
"s_nop 0\n"
"v_max_f32_e32 v247, v52, v53\n"
"v_max3_f32 v248, v248, v50, v51\n"
"v_max3_f32 v247, v247, v54, v55\n"
"v_max3_f32 v248, v248, v32, v33\n"
"v_max3_f32 v247, v247, v56, v57\n"
"v_max3_f32 v248, v248, v34, v35\n"
"v_max3_f32 v247, v247, v58, v59\n"
"v_mov_b32_e32 v246, v248\n"
"v_mov_b32_e32 v245, v247\n"
"s_nop 0\n"
"v_permlane16_swap_b32_e32 v248, v246\n"
"v_permlane16_swap_b32_e32 v247, v245\n"
"v_max_f32_e32 v248, v248, v246\n"
"v_max_f32_e32 v247, v247, v245\n"
"v_mov_b32_e32 v246, v248\n"
"v_mov_b32_e32 v245, v247\n"
"s_nop 0\n"
"v_permlane32_swap_b32_e32 v248, v246\n"
"v_permlane32_swap_b32_e32 v247, v245\n"
"v_max_f32_e32 v248, v248, v246\n"
"v_max_f32_e32 v247, v247, v245\n"
"v_med3_f32 v248, v248, v255, v250\n"
"v_med3_f32 v247, v247, v254, v249\n"
"v_min_f32_e64 v36, s98, -v248\n"
"v_min_f32_e64 v37, s98, -v248\n"
"v_min_f32_e64 v38, s98, -v248\n"
"v_min_f32_e64 v39, s98, -v248\n"
"v_min_f32_e64 v60, s98, -v247\n"
"v_min_f32_e64 v61, s98, -v247\n"
"v_min_f32_e64 v62, s98, -v247\n"
"v_min_f32_e64 v63, s98, -v247\n"
"s_waitcnt lgkmcnt(0)\n"
"v_mfma_f32_16x16x32_bf16 v[64:67], v[40:43], v[24:27], v[36:39]\n"
"ds_read_b128 v[68:71], v15 offset:20480\n"
"v_sub_f32_e32 v48, v48, v248\n"
"v_sub_f32_e32 v49, v49, v248\n"
"v_sub_f32_e32 v50, v50, v248\n"
"buffer_load_dwordx4 a[56:59], v1, s[4:7], s99 offen offset:2048 sc1\n"
"ds_read_b128 v[72:75], v252 offset:20736\n"
"v_sub_f32_e32 v51, v51, v248\n"
"v_sub_f32_e32 v32, v32, v248\n"
"v_mfma_f32_16x16x32_bf16 v[76:79], v[40:43], v[28:31], v[60:63]\n"
"v_mov_b32_e32 v40, -1\n"
"v_sub_f32_e32 v33, v33, v248\n"
"v_sub_f32_e32 v34, v34, v248\n"
"v_mov_b32_e32 v41, -1\n"
"v_sub_f32_e32 v35, v35, v248\n"
"buffer_load_dwordx4 a[60:63], v1, s[4:7], s99 offen offset:3072 sc1\n"
"v_sub_f32_e32 v52, v52, v247\n"
"v_sub_f32_e32 v53, v53, v247\n"
"v_mfma_f32_16x16x32_bf16 v[80:83], v[44:47], v[24:27], v[36:39]\n"
"v_mov_b32_e32 v42, -1\n"
"v_sub_f32_e32 v54, v54, v247\n"
"v_sub_f32_e32 v55, v55, v247\n"
"v_mov_b32_e32 v43, -1\n"
"v_sub_f32_e32 v56, v56, v247\n"
"v_sub_f32_e32 v57, v57, v247\n"
"s_add_u32 s98, s69, 0x604000\n"
"buffer_load_dwordx4 a[64:67], v0, s[4:7], s98 offen sc1\n"
"v_mfma_f32_16x16x32_bf16 v[84:87], v[44:47], v[28:31], v[60:63]\n"
"s_add_u32 s97, s60, 0x48000\n"
"buffer_store_dwordx4 v[40:43], v0, s[4:7], s97 offen sc1\n"
"v_sub_f32_e32 v58, v58, v247\n"
"v_sub_f32_e32 v59, v59, v247\n"
"s_waitcnt lgkmcnt(0)\n"
"v_mfma_f32_16x16x32_bf16 v[44:47], v[68:71], v[24:27], v[36:39]\n"
"ds_read_b128 v[100:103], v15 offset:18432\n"
"v_exp_f32_e32 v64, v64\n"
"v_exp_f32_e32 v65, v65\n"
"v_exp_f32_e32 v66, v66\n"
"buffer_load_dwordx4 a[68:71], v0, s[4:7], s98 offen offset:1024 sc1\n"
"ds_read_b128 v[104:107], v252 offset:18688\n"
"v_exp_f32_e32 v67, v67\n"
"v_exp_f32_e32 v80, v80\n"
"ds_read_b64_tr_b16 v[108:109], v16 offset:22528\n"
"v_exp_f32_e32 v81, v81\n"
"v_exp_f32_e32 v82, v82\n"
"v_mfma_f32_16x16x32_bf16 v[112:115], v[68:71], v[28:31], v[60:63]\n"
"buffer_load_dwordx4 a[72:75], v0, s[4:7], s98 offen offset:2048 sc1\n"
"v_exp_f32_e32 v83, v83\n"
"ds_read_b64_tr_b16 v[110:111], v253 offset:22784\n"
"v_cvt_pk_bf16_f32 v68, v64, v65\n"
"v_cvt_pk_bf16_f32 v69, v66, v67\n"
"v_cvt_pk_bf16_f32 v70, v80, v81\n"
"v_cvt_pk_bf16_f32 v71, v82, v83\n"
"ds_read_b64_tr_b16 v[64:65], v253 offset:22528\n"
"v_exp_f32_e32 v76, v76\n"
"buffer_load_dwordx4 a[76:79], v0, s[4:7], s98 offen offset:3072 sc1\n"
"v_exp_f32_e32 v77, v77\n"
"v_exp_f32_e32 v78, v78\n"
"v_mfma_f32_16x16x32_bf16 v[80:83], v[72:75], v[24:27], v[36:39]\n"
"ds_read_b64_tr_b16 v[66:67], v16 offset:22784\n"
"v_exp_f32_e32 v79, v79\n"
"v_exp_f32_e32 v84, v84\n"
"s_waitcnt vmcnt(30)\n"
"s_add_u32 s97, s68, 0x10000\n"
"buffer_store_dwordx2 v[18:19], v11, s[4:7], s97 offen offset:96 sc1\n"
"v_bfe_u32 v248, v0, 2, 7\n"
"v_add_u32_e32 v248, s72, v248\n"
"v_and_b32_e32 v247, 0xffffffc0, v248\n"
"v_add_u32_e32 v248, v248, v247\n"
"v_and_b32_e32 v247, 0x200, v0\n"
"v_lshl_add_u32 v248, v247, 4, v248\n"
"s_add_u32 s97, s76, 0x80000\n"
"buffer_load_dword v247, v248, s[4:7], s97 offen sc1\n"
"v_mfma_f32_16x16x32_bf16 v[116:119], v[72:75], v[28:31], v[60:63]\n"
"v_exp_f32_e32 v85, v85\n"
"v_exp_f32_e32 v86, v86\n"
"v_exp_f32_e32 v87, v87\n"
"buffer_load_dword v246, v248, s[4:7], s97 offen offset:1024 sc1\n"
"s_add_u32 s96, s60, 0x40000\n"
"buffer_store_dwordx4 v[40:43], v0, s[4:7], s96 offen sc1\n"
"v_cvt_pk_bf16_f32 v72, v76, v77\n"
"v_cvt_pk_bf16_f32 v73, v78, v79\n"
"v_cvt_pk_bf16_f32 v74, v84, v85\n"
"v_cvt_pk_bf16_f32 v75, v86, v87\n"
"s_waitcnt lgkmcnt(4)\n"
"v_mfma_f32_16x16x32_bf16 v[76:79], v[100:103], v[24:27], v[36:39]\n"
"ds_read_b128 v[84:87], v15 offset:16384\n"
"v_exp_f32_e32 v44, v44\n"
"v_exp_f32_e32 v45, v45\n"
"v_exp_f32_e32 v46, v46\n"
"v_mfma_f32_16x16x32_bf16 v[120:123], v[100:103], v[28:31], v[60:63]\n"
"buffer_load_dwordx4 a[80:83], v1, s[4:7], s98 offen sc1\n"
"ds_read_b128 v[100:103], v252 offset:16640\n"
"v_exp_f32_e32 v47, v47\n"
"v_exp_f32_e32 v80, v80\n"
"v_mfma_f32_16x16x32_bf16 v[124:127], v[104:107], v[24:27], v[36:39]\n"
"ds_read_b64_tr_b16 v[128:129], v16 offset:20480\n"
"v_exp_f32_e32 v81, v81\n"
"v_exp_f32_e32 v82, v82\n"
"buffer_load_dwordx4 a[84:87], v1, s[4:7], s98 offen offset:1024 sc1\n"
"v_mfma_f32_16x16x32_bf16 v[132:135], v[104:107], v[28:31], v[60:63]\n"
"v_exp_f32_e32 v83, v83\n"
"ds_read_b64_tr_b16 v[130:131], v253 offset:20736\n"
"v_cvt_pk_bf16_f32 v104, v44, v45\n"
"v_cvt_pk_bf16_f32 v105, v46, v47\n"
"s_waitcnt lgkmcnt(4)\n"
"v_mfma_f32_16x16x32_bf16 v[44:47], v[88:91], v[68:71], 0\n"
"v_cvt_pk_bf16_f32 v106, v80, v81\n"
"v_cvt_pk_bf16_f32 v107, v82, v83\n"
"ds_read_b64_tr_b16 v[80:81], v253 offset:20480\n"
"v_exp_f32_e32 v112, v112\n"
"buffer_load_dwordx4 a[88:91], v1, s[4:7], s98 offen offset:2048 sc1\n"
"v_mfma_f32_16x16x32_bf16 v[136:139], v[108:111], v[68:71], 0\n"
"v_exp_f32_e32 v113, v113\n"
"v_exp_f32_e32 v114, v114\n"
"ds_read_b64_tr_b16 v[82:83], v16 offset:20736\n"
"v_exp_f32_e32 v115, v115\n"
"v_mfma_f32_16x16x32_bf16 v[140:143], v[64:67], v[68:71], 0\n"
"v_exp_f32_e32 v116, v116\n"
"buffer_load_dword v245, v248, s[4:7], s97 offen offset:2048 sc1\n"
"s_waitcnt vmcnt(20)\n"
"buffer_store_dwordx2 v[18:19], v11, s[4:7], s68 offen offset:64 sc1\n"
"v_mfma_f32_16x16x32_bf16 v[68:71], v[88:91], v[72:75], 0\n"
"v_exp_f32_e32 v117, v117\n"
"v_exp_f32_e32 v118, v118\n"
"v_mfma_f32_16x16x32_bf16 v[144:147], v[108:111], v[72:75], 0\n"
"v_exp_f32_e32 v119, v119\n"
"buffer_load_dword v244, v248, s[4:7], s97 offen offset:3072 sc1\n"
"buffer_store_dwordx4 v[40:43], v0, s[4:7], s60 offen sc1\n"
"v_mfma_f32_16x16x32_bf16 v[108:111], v[64:67], v[72:75], 0\n"
"v_cvt_pk_bf16_f32 v64, v112, v113\n"
"v_cvt_pk_bf16_f32 v65, v114, v115\n"
"v_cvt_pk_bf16_f32 v66, v116, v117\n"
"v_cvt_pk_bf16_f32 v67, v118, v119\n"
"s_cmp_lg_u32 s2, 0\n"
"s_cbranch_scc1 .Lmk_s21\n"
"s_barrier\n"
".Lmk_s21:\n"
"s_waitcnt lgkmcnt(4)\n"
"v_mfma_f32_16x16x32_bf16 v[72:75], v[84:87], v[24:27], v[36:39]\n"
"ds_read_b128 v[112:115], v15 offset:14336\n"
"v_exp_f32_e32 v76, v76\n"
"v_exp_f32_e32 v77, v77\n"
"v_exp_f32_e32 v78, v78\n"
"v_mfma_f32_16x16x32_bf16 v[116:119], v[84:87], v[28:31], v[60:63]\n"
"buffer_load_dwordx4 a[92:95], v1, s[4:7], s98 offen offset:3072 sc1\n"
"ds_read_b128 v[84:87], v252 offset:14592\n"
"v_exp_f32_e32 v79, v79\n"
"v_exp_f32_e32 v124, v124\n"
"v_mfma_f32_16x16x32_bf16 v[148:151], v[100:103], v[24:27], v[36:39]\n"
"v_exp_f32_e32 v125, v125\n"
"ds_read_b64_tr_b16 v[152:153], v16 offset:18432\n"
"v_exp_f32_e32 v126, v126\n"
"s_add_u32 s98, s69, 0x606000\n"
"buffer_load_dwordx4 a[96:99], v0, s[4:7], s98 offen sc1\n"
"v_mfma_f32_16x16x32_bf16 v[156:159], v[100:103], v[28:31], v[60:63]\n"
"v_exp_f32_e32 v127, v127\n"
"v_cvt_pk_bf16_f32 v100, v76, v77\n"
"v_cvt_pk_bf16_f32 v101, v78, v79\n"
"s_waitcnt lgkmcnt(3)\n"
"v_mfma_f32_16x16x32_bf16 v[44:47], v[88:91], v[104:107], v[44:47]\n"
"ds_read_b64_tr_b16 v[154:155], v253 offset:18688\n"
"v_cvt_pk_bf16_f32 v102, v124, v125\n"
"v_cvt_pk_bf16_f32 v103, v126, v127\n"
"v_exp_f32_e32 v120, v120\n"
"buffer_load_dwordx4 a[100:103], v0, s[4:7], s98 offen offset:1024 sc1\n"
"v_mfma_f32_16x16x32_bf16 v[136:139], v[128:131], v[104:107], v[136:139]\n"
"v_exp_f32_e32 v121, v121\n"
"ds_read_b64_tr_b16 v[76:77], v253 offset:18432\n"
"v_exp_f32_e32 v122, v122\n"
"v_exp_f32_e32 v123, v123\n"
"v_mfma_f32_16x16x32_bf16 v[140:143], v[80:83], v[104:107], v[140:143]\n"
"v_exp_f32_e32 v132, v132\n"
"s_add_u32 s97, s76, 0x81000\n"
"buffer_load_dword v243, v248, s[4:7], s97 offen sc1\n"
"v_mfma_f32_16x16x32_bf16 v[68:71], v[88:91], v[64:67], v[68:71]\n"
"ds_read_b64_tr_b16 v[78:79], v16 offset:18688\n"
"v_exp_f32_e32 v133, v133\n"
"v_exp_f32_e32 v134, v134\n"
"v_exp_f32_e32 v135, v135\n"
"v_mfma_f32_16x16x32_bf16 v[144:147], v[128:131], v[64:67], v[144:147]\n"
"s_add_u32 s96, s60, 0x8000\n"
"buffer_store_dwordx4 v[40:43], v0, s[4:7], s96 offen sc1\n"
"v_mfma_f32_16x16x32_bf16 v[108:111], v[80:83], v[64:67], v[108:111]\n"
"buffer_load_dword v242, v248, s[4:7], s97 offen offset:1024 sc1\n"
"v_cvt_pk_bf16_f32 v64, v120, v121\n"
"v_cvt_pk_bf16_f32 v65, v122, v123\n"
"v_cvt_pk_bf16_f32 v66, v132, v133\n"
"v_cvt_pk_bf16_f32 v67, v134, v135\n"
"s_waitcnt lgkmcnt(4)\n"
"s_cmp_eq_i32 s78, 8\n"
"s_cbranch_scc1 .Lmk_x22s\n"
".Lmk_x22r:\n"
"v_mfma_f32_16x16x32_bf16 v[80:83], v[112:115], v[24:27], v[36:39]\n"
"ds_read_b128 v[104:107], v15 offset:12288\n"
"v_exp_f32_e32 v72, v72\n"
"v_exp_f32_e32 v73, v73\n"
"v_exp_f32_e32 v74, v74\n"
"v_mfma_f32_16x16x32_bf16 v[120:123], v[112:115], v[28:31], v[60:63]\n"
"buffer_load_dwordx4 a[104:107], v0, s[4:7], s98 offen offset:2048 sc1\n"
"ds_read_b128 v[112:115], v252 offset:12544\n"
"v_exp_f32_e32 v75, v75\n"
"v_mfma_f32_16x16x32_bf16 v[124:127], v[84:87], v[24:27], v[36:39]\n"
"v_exp_f32_e32 v148, v148\n"
"v_exp_f32_e32 v149, v149\n"
"ds_read_b64_tr_b16 v[128:129], v16 offset:16384\n"
"v_exp_f32_e32 v150, v150\n"
"v_mfma_f32_16x16x32_bf16 v[132:135], v[84:87], v[28:31], v[60:63]\n"
"v_exp_f32_e32 v151, v151\n"
"buffer_load_dwordx4 a[108:111], v0, s[4:7], s98 offen offset:3072 sc1\n"
"v_cvt_pk_bf16_f32 v84, v72, v73\n"
"s_waitcnt lgkmcnt(3)\n"
"v_mfma_f32_16x16x32_bf16 v[44:47], v[88:91], v[100:103], v[44:47]\n"
"v_cvt_pk_bf16_f32 v85, v74, v75\n"
"ds_read_b64_tr_b16 v[130:131], v253 offset:16640\n"
"v_cvt_pk_bf16_f32 v86, v148, v149\n"
"v_cvt_pk_bf16_f32 v87, v150, v151\n"
"v_exp_f32_e32 v116, v116\n"
"v_mfma_f32_16x16x32_bf16 v[136:139], v[152:155], v[100:103], v[136:139]\n"
"v_exp_f32_e32 v117, v117\n"
"ds_read_b64_tr_b16 v[72:73], v253 offset:16384\n"
"v_exp_f32_e32 v118, v118\n"
"buffer_load_dword v241, v248, s[4:7], s97 offen offset:2048 sc1\n"
"v_mfma_f32_16x16x32_bf16 v[140:143], v[76:79], v[100:103], v[140:143]\n"
"v_exp_f32_e32 v119, v119\n"
"v_exp_f32_e32 v156, v156\n"
"ds_read_b64_tr_b16 v[74:75], v16 offset:16640\n"
"v_mfma_f32_16x16x32_bf16 v[68:71], v[88:91], v[64:67], v[68:71]\n"
"v_exp_f32_e32 v157, v157\n"
"v_exp_f32_e32 v158, v158\n"
"v_exp_f32_e32 v159, v159\n"
"v_mfma_f32_16x16x32_bf16 v[144:147], v[152:155], v[64:67], v[144:147]\n"
"buffer_load_dword v240, v248, s[4:7], s97 offen offset:3072 sc1\n"
"s_add_u32 s97, s61, 0x440000\n"
"buffer_store_dwordx4 v[40:43], v0, s[4:7], s97 offen sc1\n"
"v_mfma_f32_16x16x32_bf16 v[108:111], v[76:79], v[64:67], v[108:111]\n"
"v_cvt_pk_bf16_f32 v64, v116, v117\n"
"v_cvt_pk_bf16_f32 v65, v118, v119\n"
"v_cvt_pk_bf16_f32 v66, v156, v157\n"
"v_cvt_pk_bf16_f32 v67, v158, v159\n"
"s_waitcnt lgkmcnt(4)\n"
"s_cmp_eq_i32 s78, 7\n"
"s_cbranch_scc1 .Lmk_x23s\n"
".Lmk_x23r:\n"
"v_mfma_f32_16x16x32_bf16 v[76:79], v[104:107], v[24:27], v[36:39]\n"
"ds_read_b128 v[100:103], v15 offset:10240\n"
"v_exp_f32_e32 v80, v80\n"
"v_exp_f32_e32 v81, v81\n"
"v_exp_f32_e32 v82, v82\n"
"v_mfma_f32_16x16x32_bf16 v[116:119], v[104:107], v[28:31], v[60:63]\n"
"ds_read_b128 v[104:107], v252 offset:10496\n"
"v_exp_f32_e32 v83, v83\n"
"v_exp_f32_e32 v124, v124\n"
"v_mfma_f32_16x16x32_bf16 v[148:151], v[112:115], v[24:27], v[36:39]\n"
"v_exp_f32_e32 v125, v125\n"
"ds_read_b64_tr_b16 v[152:153], v16 offset:14336\n"
"v_exp_f32_e32 v126, v126\n"
"v_mfma_f32_16x16x32_bf16 v[156:159], v[112:115], v[28:31], v[60:63]\n"
"v_exp_f32_e32 v127, v127\n"
"v_cvt_pk_bf16_f32 v112, v80, v81\n"
"v_cvt_pk_bf16_f32 v113, v82, v83\n"
"s_waitcnt lgkmcnt(3)\n"
"v_mfma_f32_16x16x32_bf16 v[44:47], v[88:91], v[84:87], v[44:47]\n"
"ds_read_b64_tr_b16 v[154:155], v253 offset:14592\n"
"v_cvt_pk_bf16_f32 v114, v124, v125\n"
"v_cvt_pk_bf16_f32 v115, v126, v127\n"
"v_exp_f32_e32 v120, v120\n"
"v_mfma_f32_16x16x32_bf16 v[136:139], v[128:131], v[84:87], v[136:139]\n"
"v_exp_f32_e32 v121, v121\n"
"ds_read_b64_tr_b16 v[80:81], v253 offset:14336\n"
"v_exp_f32_e32 v122, v122\n"
"v_exp_f32_e32 v123, v123\n"
"v_mfma_f32_16x16x32_bf16 v[140:143], v[72:75], v[84:87], v[140:143]\n"
"v_exp_f32_e32 v132, v132\n"
"ds_read_b64_tr_b16 v[82:83], v16 offset:14592\n"
"v_exp_f32_e32 v133, v133\n"
"v_mfma_f32_16x16x32_bf16 v[68:71], v[88:91], v[64:67], v[68:71]\n"
"v_exp_f32_e32 v134, v134\n"
"v_exp_f32_e32 v135, v135\n"
"v_mfma_f32_16x16x32_bf16 v[144:147], v[128:131], v[64:67], v[144:147]\n"
"s_add_u32 s97, s61, 0x440000\n"
"buffer_store_dwordx4 v[40:43], v0, s[4:7], s97 offen offset:1024 sc1\n"
"v_mfma_f32_16x16x32_bf16 v[108:111], v[72:75], v[64:67], v[108:111]\n"
"v_cvt_pk_bf16_f32 v64, v120, v121\n"
"v_cvt_pk_bf16_f32 v65, v122, v123\n"
"v_cvt_pk_bf16_f32 v66, v132, v133\n"
"v_cvt_pk_bf16_f32 v67, v134, v135\n"
"s_waitcnt lgkmcnt(4)\n"
"s_cmp_eq_i32 s78, 6\n"
"s_cbranch_scc1 .Lmk_x24s\n"
".Lmk_x24r:\n"
"v_mfma_f32_16x16x32_bf16 v[72:75], v[100:103], v[24:27], v[36:39]\n"
"ds_read_b128 v[84:87], v15 offset:8192\n"
"v_exp_f32_e32 v76, v76\n"
"v_exp_f32_e32 v77, v77\n"
"v_mfma_f32_16x16x32_bf16 v[120:123], v[100:103], v[28:31], v[60:63]\n"
"v_exp_f32_e32 v78, v78\n"
"ds_read_b128 v[100:103], v252 offset:8448\n"
"v_exp_f32_e32 v79, v79\n"
"v_mfma_f32_16x16x32_bf16 v[124:127], v[104:107], v[24:27], v[36:39]\n"
"v_exp_f32_e32 v148, v148\n"
"v_exp_f32_e32 v149, v149\n"
"ds_read_b64_tr_b16 v[128:129], v16 offset:12288\n"
"v_mfma_f32_16x16x32_bf16 v[132:135], v[104:107], v[28:31], v[60:63]\n"
"v_exp_f32_e32 v150, v150\n"
"v_exp_f32_e32 v151, v151\n"
"v_cvt_pk_bf16_f32 v104, v76, v77\n"
"s_waitcnt lgkmcnt(3)\n"
"v_mfma_f32_16x16x32_bf16 v[44:47], v[88:91], v[112:115], v[44:47]\n"
"v_cvt_pk_bf16_f32 v105, v78, v79\n"
"ds_read_b64_tr_b16 v[130:131], v253 offset:12544\n"
"v_cvt_pk_bf16_f32 v106, v148, v149\n"
"v_cvt_pk_bf16_f32 v107, v150, v151\n"
"v_exp_f32_e32 v116, v116\n"
"v_mfma_f32_16x16x32_bf16 v[136:139], v[152:155], v[112:115], v[136:139]\n"
"v_exp_f32_e32 v117, v117\n"
"ds_read_b64_tr_b16 v[76:77], v253 offset:12288\n"
"v_exp_f32_e32 v118, v118\n"
"v_mfma_f32_16x16x32_bf16 v[140:143], v[80:83], v[112:115], v[140:143]\n"
"v_exp_f32_e32 v119, v119\n"
"v_exp_f32_e32 v156, v156\n"
"v_mfma_f32_16x16x32_bf16 v[68:71], v[88:91], v[64:67], v[68:71]\n"
"ds_read_b64_tr_b16 v[78:79], v16 offset:12544\n"
"v_exp_f32_e32 v157, v157\n"
"v_exp_f32_e32 v158, v158\n"
"v_mfma_f32_16x16x32_bf16 v[144:147], v[152:155], v[64:67], v[144:147]\n"
"v_exp_f32_e32 v159, v159\n"
"s_add_u32 s97, s63, 0x44000\n"
"buffer_store_dwordx2 v[40:41], v14, s[4:7], s97 offen sc1\n"
"v_mfma_f32_16x16x32_bf16 v[108:111], v[80:83], v[64:67], v[108:111]\n"
"v_cvt_pk_bf16_f32 v40, v116, v117\n"
"v_cvt_pk_bf16_f32 v41, v118, v119\n"
"v_cvt_pk_bf16_f32 v42, v156, v157\n"
"v_cvt_pk_bf16_f32 v43, v158, v159\n"
"s_waitcnt lgkmcnt(4)\n"
"s_cmp_eq_i32 s78, 5\n"
"s_cbranch_scc1 .Lmk_x25s\n"
".Lmk_x25r:\n"
"v_mfma_f32_16x16x32_bf16 v[64:67], v[84:87], v[24:27], v[36:39]\n"
"ds_read_b64_tr_b16 v[80:81], v16 offset:10240\n"
"v_exp_f32_e32 v72, v72\n"
"v_exp_f32_e32 v73, v73\n"
"ds_read_b64_tr_b16 v[82:83], v253 offset:10496\n"
"v_mfma_f32_16x16x32_bf16 v[112:115], v[84:87], v[28:31], v[60:63]\n"
"s_add_u32 s97, s71, 0xfffff000\n"
"buffer_load_dwordx2 v[238:239], off, s[4:7], s97 sc1\n"
"v_exp_f32_e32 v74, v74\n"
"v_mfma_f32_16x16x32_bf16 v[84:87], v[100:103], v[24:27], v[36:39]\n"
"v_exp_f32_e32 v75, v75\n"
"ds_read_b64_tr_b16 v[36:37], v253 offset:10240\n"
"v_exp_f32_e32 v124, v124\n"
"v_exp_f32_e32 v125, v125\n"
"v_mfma_f32_16x16x32_bf16 v[116:119], v[100:103], v[28:31], v[60:63]\n"
"ds_read_b64_tr_b16 v[38:39], v16 offset:10496\n"
"v_exp_f32_e32 v126, v126\n"
"v_exp_f32_e32 v127, v127\n"
"s_waitcnt lgkmcnt(4)\n"
"v_mfma_f32_16x16x32_bf16 v[44:47], v[88:91], v[104:107], v[44:47]\n"
"ds_read_b64_tr_b16 v[60:61], v16 offset:24576\n"
"v_cvt_pk_bf16_f32 v100, v72, v73\n"
"v_cvt_pk_bf16_f32 v101, v74, v75\n"
"v_cvt_pk_bf16_f32 v102, v124, v125\n"
"v_mfma_f32_16x16x32_bf16 v[136:139], v[128:131], v[104:107], v[136:139]\n"
"ds_read_b64_tr_b16 v[62:63], v253 offset:24832\n"
"v_cvt_pk_bf16_f32 v103, v126, v127\n"
"v_exp_f32_e32 v120, v120\n"
"ds_read_b64_tr_b16 v[72:73], v253 offset:24576\n"
"v_exp_f32_e32 v121, v121\n"
"v_mfma_f32_16x16x32_bf16 v[140:143], v[76:79], v[104:107], v[140:143]\n"
"v_exp_f32_e32 v122, v122\n"
"ds_read_b64_tr_b16 v[74:75], v16 offset:24832\n"
"v_exp_f32_e32 v123, v123\n"
"v_mfma_f32_16x16x32_bf16 v[68:71], v[88:91], v[40:43], v[68:71]\n"
"v_exp_f32_e32 v132, v132\n"
"v_add_u32_e32 v248, 0x26400, v0\n"
"v_exp_f32_e32 v133, v133\n"
"v_mfma_f32_16x16x32_bf16 v[144:147], v[128:131], v[40:43], v[144:147]\n"
"v_exp_f32_e32 v134, v134\n"
"ds_read_b128 v[104:107], v248\n"
"v_exp_f32_e32 v135, v135\n"
"v_mfma_f32_16x16x32_bf16 v[108:111], v[76:79], v[40:43], v[108:111]\n"
"v_cvt_pk_bf16_f32 v40, v120, v121\n"
"ds_read_b128 v[76:79], v248 offset:1024\n"
"v_cvt_pk_bf16_f32 v41, v122, v123\n"
"v_cvt_pk_bf16_f32 v42, v132, v133\n"
"v_cvt_pk_bf16_f32 v43, v134, v135\n"
"s_waitcnt lgkmcnt(6)\n"
"v_mfma_f32_16x16x32_bf16 v[44:47], v[88:91], v[100:103], v[44:47]\n"
"ds_read_b64_tr_b16 v[120:121], v16 offset:8192\n"
"v_cndmask_b32_e64 v64, v48, v64, s[20:21]\n"
"v_cndmask_b32_e64 v65, v49, v65, s[22:23]\n"
"v_cndmask_b32_e64 v66, v50, v66, s[24:25]\n"
"v_cndmask_b32_e64 v67, v51, v67, s[26:27]\n"
"ds_read_b64_tr_b16 v[122:123], v253 offset:8448\n"
"v_cndmask_b32_e64 v84, v32, v84, s[28:29]\n"
"v_cndmask_b32_e64 v85, v33, v85, s[30:31]\n"
"v_cndmask_b32_e64 v86, v34, v86, s[32:33]\n"
"ds_read_b64_tr_b16 v[48:49], v253 offset:8192\n"
"v_cndmask_b32_e64 v87, v35, v87, s[34:35]\n"
"v_exp_f32_e32 v64, v64\n"
"v_mfma_f32_16x16x32_bf16 v[136:139], v[80:83], v[100:103], v[136:139]\n"
"ds_read_b64_tr_b16 v[50:51], v16 offset:8448\n"
"v_exp_f32_e32 v65, v65\n"
"v_exp_f32_e32 v66, v66\n"
"s_waitcnt vmcnt(4)\n"
"v_exp_f32_e32 v67, v67\n"
"v_exp_f32_e32 v84, v84\n"
"v_add_f32_e32 v247, v247, v246\n"
"v_exp_f32_e32 v85, v85\n"
"v_mfma_f32_16x16x32_bf16 v[140:143], v[36:39], v[100:103], v[140:143]\n"
"v_add_f32_e32 v247, v247, v245\n"
"v_exp_f32_e32 v86, v86\n"
"v_exp_f32_e32 v87, v87\n"
"v_add_f32_e32 v247, v247, v244\n"
"v_cvt_pk_bf16_f32 v32, v64, v65\n"
"v_cvt_pk_bf16_f32 v33, v66, v67\n"
"v_cvt_pk_bf16_f32 v34, v84, v85\n"
"v_add_f32_e32 v247, v247, v243\n"
"v_cvt_pk_bf16_f32 v35, v86, v87\n"
"v_cndmask_b32_e64 v112, v52, v112, s[36:37]\n"
"v_cndmask_b32_e64 v113, v53, v113, s[38:39]\n"
"v_mfma_f32_16x16x32_bf16 v[68:71], v[88:91], v[40:43], v[68:71]\n"
"v_add_f32_e32 v247, v247, v242\n"
"v_cndmask_b32_e64 v114, v54, v114, s[40:41]\n"
"v_cndmask_b32_e64 v115, v55, v115, s[42:43]\n"
"v_cndmask_b32_e64 v116, v56, v116, s[44:45]\n"
"v_add_f32_e32 v247, v247, v241\n"
"v_cndmask_b32_e64 v117, v57, v117, s[46:47]\n"
"v_cndmask_b32_e64 v118, v58, v118, s[48:49]\n"
"v_cndmask_b32_e64 v119, v59, v119, s[50:51]\n"
"v_exp_f32_e32 v112, v112\n"
"v_add_f32_e32 v247, v247, v240\n"
"v_exp_f32_e32 v113, v113\n"
"v_mfma_f32_16x16x32_bf16 v[144:147], v[80:83], v[40:43], v[144:147]\n"
"v_mov_b32_e32 v246, v247\n"
"v_exp_f32_e32 v114, v114\n"
"v_exp_f32_e32 v115, v115\n"
"v_permlane32_swap_b32_e32 v247, v246\n"
"v_exp_f32_e32 v116, v116\n"
"v_exp_f32_e32 v117, v117\n"
"v_add_f32_e32 v247, v247, v246\n"
"v_exp_f32_e32 v118, v118\n"
"v_mfma_f32_16x16x32_bf16 v[108:111], v[36:39], v[40:43], v[108:111]\n"
"ds_write_b32 v21, v247 offset:512\n"
"v_exp_f32_e32 v119, v119\n"
"v_cvt_pk_bf16_f32 v36, v112, v113\n"
"s_waitcnt vmcnt(1)\n"
"buffer_store_dwordx2 v[18:19], v11, s[4:7], s68 offen offset:32 sc1\n"
"v_cvt_pk_bf16_f32 v37, v114, v115\n"
"v_cvt_pk_bf16_f32 v38, v116, v117\n"
"v_cvt_pk_bf16_f32 v39, v118, v119\n"
"s_waitcnt lgkmcnt(1)\n"
"v_mfma_f32_16x16x32_bf16 v[44:47], v[88:91], v[32:35], v[44:47]\n"
"v_and_b32_e32 v104, v32, v104\n"
"v_and_b32_e32 v105, v33, v105\n"
"v_and_b32_e32 v106, v34, v106\n"
"v_and_b32_e32 v107, v35, v107\n"
"v_mfma_f32_16x16x32_bf16 v[68:71], v[88:91], v[36:39], v[68:71]\n"
"v_xor_b32_e32 v40, v32, v104\n"
"v_xor_b32_e32 v41, v33, v105\n"
"v_xor_b32_e32 v42, v34, v106\n"
"v_xor_b32_e32 v43, v35, v107\n"
"v_mfma_f32_16x16x32_bf16 v[136:139], v[120:123], v[104:107], v[136:139]\n"
"v_and_b32_e32 v76, v36, v76\n"
"v_and_b32_e32 v77, v37, v77\n"
"v_mfma_f32_16x16x32_bf16 v[140:143], v[48:51], v[104:107], v[140:143]\n"
"v_and_b32_e32 v78, v38, v78\n"
"v_and_b32_e32 v79, v39, v79\n"
"v_mfma_f32_16x16x32_bf16 v[136:139], v[60:63], v[40:43], v[136:139]\n"
"v_xor_b32_e32 v32, v36, v76\n"
"v_xor_b32_e32 v33, v37, v77\n"
"v_mfma_f32_16x16x32_bf16 v[140:143], v[72:75], v[40:43], v[140:143]\n"
"v_xor_b32_e32 v34, v38, v78\n"
"v_xor_b32_e32 v35, v39, v79\n"
"v_mfma_f32_16x16x32_bf16 v[144:147], v[120:123], v[76:79], v[144:147]\n"
"s_waitcnt vmcnt(1)\n"
"v_cmp_ne_u64_e32 vcc, v[238:239], v[18:19]\n"
"s_nop 0\n"
"s_cbranch_vccnz .Lmk_f18s\n"
".Lmk_f18r:\n"
"v_mfma_f32_16x16x32_bf16 v[108:111], v[48:51], v[76:79], v[108:111]\n"
"s_add_u32 m0, s77, 0x4000\n"
"s_add_u32 s97, s65, 0\n"
"buffer_load_dwordx4 v2, s[4:7], s97 offen sc1 lds\n"
"buffer_load_dwordx4 v2, s[4:7], s97 offen offset:1024 sc1 lds\n"
"v_mfma_f32_16x16x32_bf16 v[144:147], v[60:63], v[32:35], v[144:147]\n"
"s_add_u32 m0, s77, 0xc000\n"
"s_add_u32 s97, s65, 0x4000\n"
"buffer_load_dwordx4 v2, s[4:7], s97 offen sc1 lds\n"
"buffer_load_dwordx4 v2, s[4:7], s97 offen offset:1024 sc1 lds\n"
"v_mfma_f32_16x16x32_bf16 v[108:111], v[72:75], v[32:35], v[108:111]\n"
"v_max_f32_e32 v248, v44, v68\n"
"v_rcp_f32_e32 v44, v44\n"
"v_rcp_f32_e32 v68, v68\n"
"s_nop 0\n"
"v_max3_f32 v248, v248, v44, v68\n"
"v_cmp_nlt_f32_e32 vcc, v248, v251\n"
"s_cbranch_vccnz .Lmk_b20\n"
".Lmk_k26:\n"
"v_mul_f32_e32 v136, v136, v44\n"
"v_mul_f32_e32 v137, v137, v44\n"
"v_mul_f32_e32 v138, v138, v44\n"
"v_mul_f32_e32 v139, v139, v44\n"
"v_mul_f32_e32 v140, v140, v44\n"
"v_mul_f32_e32 v141, v141, v44\n"
"v_mul_f32_e32 v142, v142, v44\n"
"v_mul_f32_e32 v143, v143, v44\n"
"v_cvt_pk_bf16_f32 v24, v136, v137\n"
"v_cvt_pk_bf16_f32 v25, v138, v139\n"
"v_cvt_pk_bf16_f32 v26, v140, v141\n"
"v_cvt_pk_bf16_f32 v27, v142, v143\n"
"s_add_u32 s99, s60, 0x40000\n"
"buffer_store_dwordx4 v[24:27], v0, s[4:7], s99 offen sc1\n"
"v_mul_f32_e32 v144, v144, v68\n"
"v_mul_f32_e32 v145, v145, v68\n"
"v_mul_f32_e32 v146, v146, v68\n"
"v_mul_f32_e32 v147, v147, v68\n"
"v_mul_f32_e32 v108, v108, v68\n"
"v_mul_f32_e32 v109, v109, v68\n"
"v_mul_f32_e32 v110, v110, v68\n"
"v_mul_f32_e32 v111, v111, v68\n"
"v_cvt_pk_bf16_f32 v24, v144, v145\n"
"v_cvt_pk_bf16_f32 v25, v146, v147\n"
"v_cvt_pk_bf16_f32 v26, v108, v109\n"
"v_cvt_pk_bf16_f32 v27, v110, v111\n"
"s_add_u32 s99, s60, 0x48000\n"
"buffer_store_dwordx4 v[24:27], v0, s[4:7], s99 offen sc1\n"
"; @A AT a\n"
"s_mov_b32 s2, 0\n"
"v_mov_b32_e32 v255, 0xff800000\n"
"v_mov_b32_e32 v254, 0xff800000\n"
"v_mov_b32_e32 v250, 0x7f800000\n"
"v_mov_b32_e32 v249, 0x7f800000\n"
".Lmk_a27:\n"
"ds_read_b128 v[24:27], v15 offset:16384\n"
"ds_read_b128 v[28:31], v252 offset:16640\n"
"ds_read_b128 v[32:35], v15 offset:14336\n"
"ds_read_b128 v[36:39], v252 offset:14592\n"
"s_cmp_gt_i32 s78, 7\n"
"s_cselect_b32 s99, 0x80000000, 0\n"
"s_or_b32 s99, s99, 0x7f800000\n"
"buffer_load_dwordx4 a[112:115], v1, s[4:7], s98 offen sc1\n"
"buffer_load_dwordx4 a[116:119], v1, s[4:7], s98 offen offset:1024 sc1\n"
"s_waitcnt lgkmcnt(2)\n"
"v_mfma_f32_16x16x32_bf16 v[100:103], v[24:27], v[92:95], 0\n"
"buffer_load_dwordx4 a[120:123], v1, s[4:7], s98 offen offset:2048 sc1\n"
"v_mfma_f32_16x16x32_bf16 v[104:107], v[24:27], v[96:99], 0\n"
"v_mfma_f32_16x16x32_bf16 v[108:111], v[28:31], v[92:95], 0\n"
"v_mfma_f32_16x16x32_bf16 v[112:115], v[28:31], v[96:99], 0\n"
"s_nop 3\n"
"v_max_f32_e32 v248, v100, v101\n"
"s_nop 0\n"
"v_max_f32_e32 v247, v104, v105\n"
"v_max3_f32 v248, v248, v102, v103\n"
"v_max3_f32 v247, v247, v106, v107\n"
"v_max3_f32 v248, v248, v108, v109\n"
"v_max3_f32 v247, v247, v112, v113\n"
"v_max3_f32 v248, v248, v110, v111\n"
"v_max3_f32 v247, v247, v114, v115\n"
"v_mov_b32_e32 v246, v248\n"
"v_mov_b32_e32 v245, v247\n"
"s_nop 0\n"
"v_permlane16_swap_b32_e32 v248, v246\n"
"v_permlane16_swap_b32_e32 v247, v245\n"
"v_max_f32_e32 v248, v248, v246\n"
"v_max_f32_e32 v247, v247, v245\n"
"v_mov_b32_e32 v246, v248\n"
"v_mov_b32_e32 v245, v247\n"
"s_nop 0\n"
"v_permlane32_swap_b32_e32 v248, v246\n"
"v_permlane32_swap_b32_e32 v247, v245\n"
"v_max_f32_e32 v248, v248, v246\n"
"v_max_f32_e32 v247, v247, v245\n"
"v_med3_f32 v248, v248, v255, v250\n"
"v_med3_f32 v247, v247, v254, v249\n"
"v_min_f32_e64 v116, s99, -v248\n"
"v_min_f32_e64 v117, s99, -v248\n"
"v_min_f32_e64 v118, s99, -v248\n"
"v_min_f32_e64 v119, s99, -v248\n"
"v_min_f32_e64 v120, s99, -v247\n"
"v_min_f32_e64 v121, s99, -v247\n"
"v_min_f32_e64 v122, s99, -v247\n"
"v_min_f32_e64 v123, s99, -v247\n"
"s_waitcnt lgkmcnt(0)\n"
"v_mfma_f32_16x16x32_bf16 v[40:43], v[32:35], v[92:95], v[116:119]\n"
"ds_read_b128 v[44:47], v15 offset:12288\n"
"v_sub_f32_e32 v100, v100, v248\n"
"v_sub_f32_e32 v101, v101, v248\n"
"v_sub_f32_e32 v102, v102, v248\n"
"buffer_load_dwordx4 a[124:127], v1, s[4:7], s98 offen offset:3072 sc1\n"
"v_sub_f32_e32 v103, v103, v248\n"
"v_mfma_f32_16x16x32_bf16 v[48:51], v[32:35], v[96:99], v[120:123]\n"
"v_sub_f32_e32 v108, v108, v248\n"
"v_sub_f32_e32 v109, v109, v248\n"
"v_sub_f32_e32 v110, v110, v248\n"
"v_sub_f32_e32 v111, v111, v248\n"
"s_add_u32 s99, s69, 0x608000\n"
"buffer_load_dwordx4 v[24:27], v0, s[4:7], s99 offen sc1\n"
"v_mfma_f32_16x16x32_bf16 v[52:55], v[36:39], v[92:95], v[116:119]\n"
"ds_read_b128 v[56:59], v252 offset:12544\n"
"v_sub_f32_e32 v104, v104, v247\n"
"v_sub_f32_e32 v105, v105, v247\n"
"v_sub_f32_e32 v106, v106, v247\n"
"v_sub_f32_e32 v107, v107, v247\n"
"v_mfma_f32_16x16x32_bf16 v[60:63], v[36:39], v[96:99], v[120:123]\n"
"buffer_load_dwordx4 v[28:31], v0, s[4:7], s99 offen offset:1024 sc1\n"
"v_sub_f32_e32 v112, v112, v247\n"
"v_sub_f32_e32 v113, v113, v247\n"
"v_sub_f32_e32 v114, v114, v247\n"
"v_sub_f32_e32 v115, v115, v247\n"
"s_waitcnt lgkmcnt(0)\n"
"s_cmp_eq_i32 s78, 7\n"
"s_cbranch_scc1 .Lmk_x29s\n"
".Lmk_x29r:\n"
"v_mfma_f32_16x16x32_bf16 v[64:67], v[44:47], v[92:95], v[116:119]\n"
"ds_read_b128 v[68:71], v15 offset:10240\n"
"v_exp_f32_e32 v40, v40\n"
"v_exp_f32_e32 v41, v41\n"
"v_exp_f32_e32 v42, v42\n"
"buffer_load_dwordx4 v[32:35], v0, s[4:7], s99 offen offset:2048 sc1\n"
"v_exp_f32_e32 v43, v43\n"
"ds_read_b128 v[72:75], v252 offset:10496\n"
"v_exp_f32_e32 v52, v52\n"
"v_mfma_f32_16x16x32_bf16 v[76:79], v[44:47], v[96:99], v[120:123]\n"
"v_exp_f32_e32 v53, v53\n"
"v_exp_f32_e32 v54, v54\n"
"ds_read_b64_tr_b16 v[80:81], v16 offset:14336\n"
"v_exp_f32_e32 v55, v55\n"
"v_cvt_pk_bf16_f32 v84, v40, v41\n"
"buffer_load_dwordx4 v[36:39], v0, s[4:7], s99 offen offset:3072 sc1\n"
"v_cvt_pk_bf16_f32 v85, v42, v43\n"
"v_cvt_pk_bf16_f32 v86, v52, v53\n"
"v_mfma_f32_16x16x32_bf16 v[124:127], v[56:59], v[92:95], v[116:119]\n"
"v_cvt_pk_bf16_f32 v87, v54, v55\n"
"ds_read_b64_tr_b16 v[82:83], v253 offset:14592\n"
"v_exp_f32_e32 v48, v48\n"
"v_exp_f32_e32 v49, v49\n"
"v_exp_f32_e32 v50, v50\n"
"v_exp_f32_e32 v51, v51\n"
"ds_read_b64_tr_b16 v[128:129], v253 offset:14336\n"
"buffer_load_dwordx4 v[40:43], v1, s[4:7], s99 offen sc1\n"
"v_mfma_f32_16x16x32_bf16 v[132:135], v[56:59], v[96:99], v[120:123]\n"
"v_exp_f32_e32 v60, v60\n"
"v_exp_f32_e32 v61, v61\n"
"v_exp_f32_e32 v62, v62\n"
"ds_read_b64_tr_b16 v[130:131], v16 offset:14592\n"
"v_exp_f32_e32 v63, v63\n"
"v_cvt_pk_bf16_f32 v56, v48, v49\n"
"v_cvt_pk_bf16_f32 v57, v50, v51\n"
"v_cvt_pk_bf16_f32 v58, v60, v61\n"
"v_cvt_pk_bf16_f32 v59, v62, v63\n"
"s_waitcnt lgkmcnt(4)\n"
"s_cmp_eq_i32 s78, 6\n"
"s_cbranch_scc1 .Lmk_x30s\n"
".Lmk_x30r:\n"
"v_mfma_f32_16x16x32_bf16 v[136:139], v[68:71], v[92:95], v[116:119]\n"
"ds_read_b128 v[60:63], v15 offset:8192\n"
"v_exp_f32_e32 v64, v64\n"
"v_exp_f32_e32 v65, v65\n"
"v_exp_f32_e32 v66, v66\n"
"v_mfma_f32_16x16x32_bf16 v[140:143], v[68:71], v[96:99], v[120:123]\n"
"s_add_u32 s97, s67, 32\n"
"buffer_load_dwordx2 v[246:247], v12, s[4:7], s97 offen sc1\n"
"v_exp_f32_e32 v67, v67\n"
"v_mfma_f32_16x16x32_bf16 v[68:71], v[72:75], v[92:95], v[116:119]\n"
"ds_read_b128 v[144:147], v252 offset:8448\n"
"v_exp_f32_e32 v124, v124\n"
"v_exp_f32_e32 v125, v125\n"
"v_mfma_f32_16x16x32_bf16 v[148:151], v[72:75], v[96:99], v[120:123]\n"
"v_exp_f32_e32 v126, v126\n"
"ds_read_b64_tr_b16 v[72:73], v16 offset:12288\n"
"buffer_load_dwordx4 v[44:47], v1, s[4:7], s99 offen offset:1024 sc1\n"
"s_waitcnt lgkmcnt(3)\n"
"v_mfma_f32_16x16x32_bf16 v[152:155], v[88:91], v[84:87], 0\n"
"v_exp_f32_e32 v127, v127\n"
"v_cvt_pk_bf16_f32 v156, v64, v65\n"
"v_cvt_pk_bf16_f32 v157, v66, v67\n"
"v_cvt_pk_bf16_f32 v158, v124, v125\n"
"v_cvt_pk_bf16_f32 v159, v126, v127\n"
"v_mfma_f32_16x16x32_bf16 v[124:127], v[80:83], v[84:87], 0\n"
"ds_read_b64_tr_b16 v[74:75], v253 offset:12544\n"
"v_exp_f32_e32 v76, v76\n"
"v_exp_f32_e32 v77, v77\n"
"v_mfma_f32_16x16x32_bf16 v[160:163], v[128:131], v[84:87], 0\n"
"buffer_load_dwordx4 v[48:51], v1, s[4:7], s99 offen offset:2048 sc1\n"
"v_exp_f32_e32 v78, v78\n"
"v_exp_f32_e32 v79, v79\n"
"v_mfma_f32_16x16x32_bf16 v[164:167], v[88:91], v[56:59], 0\n"
"ds_read_b64_tr_b16 v[84:85], v253 offset:12288\n"
"v_exp_f32_e32 v132, v132\n"
"v_exp_f32_e32 v133, v133\n"
"v_mfma_f32_16x16x32_bf16 v[168:171], v[80:83], v[56:59], 0\n"
"buffer_load_dwordx4 v[52:55], v1, s[4:7], s99 offen offset:3072 sc1\n"
"v_exp_f32_e32 v134, v134\n"
"ds_read_b64_tr_b16 v[86:87], v16 offset:12544\n"
"v_exp_f32_e32 v135, v135\n"
"v_mfma_f32_16x16x32_bf16 v[172:175], v[128:131], v[56:59], 0\n"
"v_cvt_pk_bf16_f32 v80, v76, v77\n"
"v_cvt_pk_bf16_f32 v81, v78, v79\n"
"v_cvt_pk_bf16_f32 v82, v132, v133\n"
"v_cvt_pk_bf16_f32 v83, v134, v135\n"
"s_waitcnt vmcnt(15)\n"
"s_cmp_lg_u32 s2, 0\n"
"s_cbranch_scc1 .Lmk_s31\n"
"s_barrier\n"
".Lmk_s31:\n"
"s_waitcnt lgkmcnt(4)\n"
"s_cmp_eq_i32 s78, 5\n"
"s_cbranch_scc1 .Lmk_x32s\n"
".Lmk_x32r:\n"
"v_mfma_f32_16x16x32_bf16 v[76:79], v[60:63], v[92:95], v[116:119]\n"
"ds_read_b128 v[128:131], v15 offset:6144\n"
"v_exp_f32_e32 v136, v136\n"
"v_exp_f32_e32 v137, v137\n"
"v_exp_f32_e32 v138, v138\n"
"v_mfma_f32_16x16x32_bf16 v[132:135], v[60:63], v[96:99], v[120:123]\n"
"s_add_u32 s99, s69, 0x60a000\n"
"buffer_load_dwordx4 v[56:59], v0, s[4:7], s99 offen sc1\n"
"v_exp_f32_e32 v139, v139\n"
"v_mfma_f32_16x16x32_bf16 v[176:179], v[144:147], v[92:95], v[116:119]\n"
"ds_read_b128 v[180:183], v252 offset:6400\n"
"v_exp_f32_e32 v68, v68\n"
"v_exp_f32_e32 v69, v69\n"
"v_mfma_f32_16x16x32_bf16 v[184:187], v[144:147], v[96:99], v[120:123]\n"
"v_exp_f32_e32 v70, v70\n"
"ds_read_b64_tr_b16 v[144:145], v16 offset:10240\n"
"v_exp_f32_e32 v71, v71\n"
"s_waitcnt lgkmcnt(3)\n"
"v_mfma_f32_16x16x32_bf16 v[152:155], v[88:91], v[156:159], v[152:155]\n"
"v_cvt_pk_bf16_f32 v188, v136, v137\n"
"buffer_load_dwordx4 v[60:63], v0, s[4:7], s99 offen offset:1024 sc1\n"
"v_cvt_pk_bf16_f32 v189, v138, v139\n"
"v_cvt_pk_bf16_f32 v190, v68, v69\n"
"v_mfma_f32_16x16x32_bf16 v[124:127], v[72:75], v[156:159], v[124:127]\n"
"v_cvt_pk_bf16_f32 v191, v70, v71\n"
"ds_read_b64_tr_b16 v[146:147], v253 offset:10496\n"
"v_exp_f32_e32 v140, v140\n"
"v_exp_f32_e32 v141, v141\n"
"v_mfma_f32_16x16x32_bf16 v[160:163], v[84:87], v[156:159], v[160:163]\n"
"v_exp_f32_e32 v142, v142\n"
"v_exp_f32_e32 v143, v143\n"
"ds_read_b64_tr_b16 v[136:137], v253 offset:10240\n"
"v_mfma_f32_16x16x32_bf16 v[164:167], v[88:91], v[80:83], v[164:167]\n"
"buffer_load_dwordx4 v[64:67], v0, s[4:7], s99 offen offset:2048 sc1\n"
"v_exp_f32_e32 v148, v148\n"
"v_exp_f32_e32 v149, v149\n"
"v_mfma_f32_16x16x32_bf16 v[168:171], v[72:75], v[80:83], v[168:171]\n"
"v_exp_f32_e32 v150, v150\n"
"ds_read_b64_tr_b16 v[138:139], v16 offset:10496\n"
"v_exp_f32_e32 v151, v151\n"
"v_mfma_f32_16x16x32_bf16 v[172:175], v[84:87], v[80:83], v[172:175]\n"
"v_cvt_pk_bf16_f32 v80, v140, v141\n"
"v_cvt_pk_bf16_f32 v81, v142, v143\n"
"v_cvt_pk_bf16_f32 v82, v148, v149\n"
"v_cvt_pk_bf16_f32 v83, v150, v151\n"
"s_waitcnt lgkmcnt(4)\n"
"v_mfma_f32_16x16x32_bf16 v[84:87], v[128:131], v[92:95], v[116:119]\n"
"ds_read_b128 v[140:143], v15 offset:4096\n"
"v_exp_f32_e32 v76, v76\n"
"v_exp_f32_e32 v77, v77\n"
"v_exp_f32_e32 v78, v78\n"
"v_mfma_f32_16x16x32_bf16 v[148:151], v[128:131], v[96:99], v[120:123]\n"
"buffer_load_dwordx4 v[68:71], v0, s[4:7], s99 offen offset:3072 sc1\n"
"v_exp_f32_e32 v79, v79\n"
"v_mfma_f32_16x16x32_bf16 v[128:131], v[180:183], v[92:95], v[116:119]\n"
"ds_read_b128 v[156:159], v252 offset:4352\n"
"v_exp_f32_e32 v176, v176\n"
"v_exp_f32_e32 v177, v177\n"
"v_mfma_f32_16x16x32_bf16 v[192:195], v[180:183], v[96:99], v[120:123]\n"
"v_exp_f32_e32 v178, v178\n"
"ds_read_b64_tr_b16 v[180:181], v16 offset:8192\n"
"v_exp_f32_e32 v179, v179\n"
"s_waitcnt lgkmcnt(3)\n"
"v_mfma_f32_16x16x32_bf16 v[152:155], v[88:91], v[188:191], v[152:155]\n"
"v_cvt_pk_bf16_f32 v196, v76, v77\n"
"buffer_load_dwordx4 v[72:75], v1, s[4:7], s99 offen sc1\n"
"v_cvt_pk_bf16_f32 v197, v78, v79\n"
"v_cvt_pk_bf16_f32 v198, v176, v177\n"
"v_mfma_f32_16x16x32_bf16 v[124:127], v[144:147], v[188:191], v[124:127]\n"
"v_cvt_pk_bf16_f32 v199, v178, v179\n"
"ds_read_b64_tr_b16 v[182:183], v253 offset:8448\n"
"v_exp_f32_e32 v132, v132\n"
"v_exp_f32_e32 v133, v133\n"
"v_mfma_f32_16x16x32_bf16 v[160:163], v[136:139], v[188:191], v[160:163]\n"
"v_exp_f32_e32 v134, v134\n"
"v_exp_f32_e32 v135, v135\n"
"ds_read_b64_tr_b16 v[176:177], v253 offset:8192\n"
"v_mfma_f32_16x16x32_bf16 v[164:167], v[88:91], v[80:83], v[164:167]\n"
"buffer_load_dwordx4 v[76:79], v1, s[4:7], s99 offen offset:1024 sc1\n"
"v_exp_f32_e32 v184, v184\n"
"v_exp_f32_e32 v185, v185\n"
"v_mfma_f32_16x16x32_bf16 v[168:171], v[144:147], v[80:83], v[168:171]\n"
"v_exp_f32_e32 v186, v186\n"
"ds_read_b64_tr_b16 v[178:179], v16 offset:8448\n"
"v_exp_f32_e32 v187, v187\n"
"v_mfma_f32_16x16x32_bf16 v[172:175], v[136:139], v[80:83], v[172:175]\n"
"v_cvt_pk_bf16_f32 v136, v132, v133\n"
"v_cvt_pk_bf16_f32 v137, v134, v135\n"
"v_cvt_pk_bf16_f32 v138, v184, v185\n"
"v_cvt_pk_bf16_f32 v139, v186, v187\n"
"s_waitcnt lgkmcnt(4)\n"
"v_mfma_f32_16x16x32_bf16 v[132:135], v[140:143], v[92:95], v[116:119]\n"
"ds_read_b128 v[144:147], v15 offset:2048\n"
"v_exp_f32_e32 v84, v84\n"
"v_exp_f32_e32 v85, v85\n"
"v_exp_f32_e32 v86, v86\n"
"v_mfma_f32_16x16x32_bf16 v[184:187], v[140:143], v[96:99], v[120:123]\n"
"buffer_load_dwordx4 v[80:83], v1, s[4:7], s99 offen offset:2048 sc1\n"
"ds_read_b128 v[140:143], v252 offset:2304\n"
"v_exp_f32_e32 v87, v87\n"
"v_mfma_f32_16x16x32_bf16 v[188:191], v[156:159], v[92:95], v[116:119]\n"
"v_exp_f32_e32 v128, v128\n"
"v_exp_f32_e32 v129, v129\n"
"ds_read_b64_tr_b16 v[200:201], v16 offset:6144\n"
"v_mfma_f32_16x16x32_bf16 v[204:207], v[156:159], v[96:99], v[120:123]\n"
"v_exp_f32_e32 v130, v130\n"
"v_exp_f32_e32 v131, v131\n"
"v_cvt_pk_bf16_f32 v156, v84, v85\n"
"s_waitcnt lgkmcnt(3)\n"
"v_mfma_f32_16x16x32_bf16 v[152:155], v[88:91], v[196:199], v[152:155]\n"
"v_cvt_pk_bf16_f32 v157, v86, v87\n"
"ds_read_b64_tr_b16 v[202:203], v253 offset:6400\n"
"v_cvt_pk_bf16_f32 v158, v128, v129\n"
"v_cvt_pk_bf16_f32 v159, v130, v131\n"
"v_exp_f32_e32 v148, v148\n"
"v_mfma_f32_16x16x32_bf16 v[124:127], v[180:183], v[196:199], v[124:127]\n"
"v_exp_f32_e32 v149, v149\n"
"ds_read_b64_tr_b16 v[128:129], v253 offset:6144\n"
"buffer_load_dwordx4 v[84:87], v1, s[4:7], s99 offen offset:3072 sc1\n"
"v_mfma_f32_16x16x32_bf16 v[160:163], v[176:179], v[196:199], v[160:163]\n"
"v_exp_f32_e32 v150, v150\n"
"v_exp_f32_e32 v151, v151\n"
"v_exp_f32_e32 v192, v192\n"
"v_mfma_f32_16x16x32_bf16 v[164:167], v[88:91], v[136:139], v[164:167]\n"
"ds_read_b64_tr_b16 v[130:131], v16 offset:6400\n"
"v_exp_f32_e32 v193, v193\n"
"v_exp_f32_e32 v194, v194\n"
"v_mfma_f32_16x16x32_bf16 v[168:171], v[180:183], v[136:139], v[168:171]\n"
"v_exp_f32_e32 v195, v195\n"
"s_waitcnt vmcnt(11)\n"
"v_cmp_ne_u64_e32 vcc, v[246:247], v[18:19]\n"
"s_nop 0\n"
"s_cbranch_vccnz .Lmk_f4s\n"
".Lmk_f4r:\n"
"v_mfma_f32_16x16x32_bf16 v[172:175], v[176:179], v[136:139], v[172:175]\n"
"v_cvt_pk_bf16_f32 v136, v148, v149\n"
"v_cvt_pk_bf16_f32 v137, v150, v151\n"
"v_cvt_pk_bf16_f32 v138, v192, v193\n"
"v_cvt_pk_bf16_f32 v139, v194, v195\n"
"s_waitcnt lgkmcnt(4)\n"
"v_mfma_f32_16x16x32_bf16 v[148:151], v[144:147], v[92:95], v[116:119]\n"
"ds_read_b128 v[176:179], v15\n"
"v_exp_f32_e32 v132, v132\n"
"v_exp_f32_e32 v133, v133\n"
"v_exp_f32_e32 v134, v134\n"
"v_mfma_f32_16x16x32_bf16 v[180:183], v[144:147], v[96:99], v[120:123]\n"
"s_add_u32 s99, s56, 0x40000\n"
"buffer_load_dwordx4 v[144:147], v0, s[4:7], s99 offen sc1\n"
"v_exp_f32_e32 v135, v135\n"
"v_mfma_f32_16x16x32_bf16 v[192:195], v[140:143], v[92:95], v[116:119]\n"
"ds_read_b128 v[196:199], v252 offset:256\n"
"v_exp_f32_e32 v188, v188\n"
"v_exp_f32_e32 v189, v189\n"
"v_exp_f32_e32 v190, v190\n"
"v_mfma_f32_16x16x32_bf16 v[208:211], v[140:143], v[96:99], v[120:123]\n"
"buffer_load_dwordx4 v[140:143], v0, s[4:7], s99 offen offset:1024 sc1\n"
"ds_read_b64_tr_b16 v[212:213], v16 offset:4096\n"
"v_exp_f32_e32 v191, v191\n"
"s_waitcnt lgkmcnt(3)\n"
"v_mfma_f32_16x16x32_bf16 v[152:155], v[88:91], v[156:159], v[152:155]\n"
"v_cvt_pk_bf16_f32 v216, v132, v133\n"
"v_cvt_pk_bf16_f32 v217, v134, v135\n"
"v_cvt_pk_bf16_f32 v218, v188, v189\n"
"v_cvt_pk_bf16_f32 v219, v190, v191\n"
"buffer_load_dwordx4 v[132:135], v0, s[4:7], s99 offen offset:2048 sc1\n"
"v_mfma_f32_16x16x32_bf16 v[124:127], v[200:203], v[156:159], v[124:127]\n"
"ds_read_b64_tr_b16 v[214:215], v253 offset:4352\n"
"v_exp_f32_e32 v184, v184\n"
"v_exp_f32_e32 v185, v185\n"
"v_mfma_f32_16x16x32_bf16 v[160:163], v[128:131], v[156:159], v[160:163]\n"
"v_exp_f32_e32 v186, v186\n"
"v_exp_f32_e32 v187, v187\n"
"buffer_load_dwordx4 v[156:159], v0, s[4:7], s99 offen offset:3072 sc1\n"
"v_mfma_f32_16x16x32_bf16 v[164:167], v[88:91], v[136:139], v[164:167]\n"
"ds_read_b64_tr_b16 v[188:189], v253 offset:4096\n"
"v_exp_f32_e32 v204, v204\n"
"v_exp_f32_e32 v205, v205\n"
"v_mfma_f32_16x16x32_bf16 v[168:171], v[200:203], v[136:139], v[168:171]\n"
"v_exp_f32_e32 v206, v206\n"
"buffer_load_dwordx4 v[200:203], v1, s[4:7], s99 offen sc1\n"
"ds_read_b64_tr_b16 v[190:191], v16 offset:4352\n"
"v_exp_f32_e32 v207, v207\n"
"v_mfma_f32_16x16x32_bf16 v[172:175], v[128:131], v[136:139], v[172:175]\n"
"v_cvt_pk_bf16_f32 v128, v184, v185\n"
"v_cvt_pk_bf16_f32 v129, v186, v187\n"
"v_cvt_pk_bf16_f32 v130, v204, v205\n"
"v_cvt_pk_bf16_f32 v131, v206, v207\n"
"s_waitcnt lgkmcnt(4)\n"
"v_mfma_f32_16x16x32_bf16 v[136:139], v[176:179], v[92:95], v[116:119]\n"
"ds_read_b64_tr_b16 v[184:185], v16 offset:2048\n"
"v_exp_f32_e32 v148, v148\n"
"v_exp_f32_e32 v149, v149\n"
"ds_read_b64_tr_b16 v[186:187], v253 offset:2304\n"
"buffer_load_dwordx4 v[204:207], v1, s[4:7], s99 offen offset:1024 sc1\n"
"v_mfma_f32_16x16x32_bf16 v[220:223], v[176:179], v[96:99], v[120:123]\n"
"v_exp_f32_e32 v150, v150\n"
"v_exp_f32_e32 v151, v151\n"
"ds_read_b64_tr_b16 v[176:177], v253 offset:2048\n"
"v_mfma_f32_16x16x32_bf16 v[224:227], v[196:199], v[92:95], v[116:119]\n"
"v_exp_f32_e32 v192, v192\n"
"v_exp_f32_e32 v193, v193\n"
"buffer_load_dwordx4 v[116:119], v1, s[4:7], s99 offen offset:2048 sc1\n"
"v_mfma_f32_16x16x32_bf16 v[228:231], v[196:199], v[96:99], v[120:123]\n"
"ds_read_b64_tr_b16 v[178:179], v16 offset:2304\n"
"v_exp_f32_e32 v194, v194\n"
"v_exp_f32_e32 v195, v195\n"
"ds_read_b64_tr_b16 v[120:121], v16 offset:16384\n"
"s_waitcnt lgkmcnt(5)\n"
"v_mfma_f32_16x16x32_bf16 v[152:155], v[88:91], v[216:219], v[152:155]\n"
"v_cvt_pk_bf16_f32 v196, v148, v149\n"
"v_cvt_pk_bf16_f32 v197, v150, v151\n"
"v_cvt_pk_bf16_f32 v198, v192, v193\n"
"buffer_load_dwordx4 v[148:151], v1, s[4:7], s99 offen offset:3072 sc1\n"
"ds_read_b64_tr_b16 v[122:123], v253 offset:16640\n"
"v_mfma_f32_16x16x32_bf16 v[124:127], v[212:215], v[216:219], v[124:127]\n"
"v_cvt_pk_bf16_f32 v199, v194, v195\n"
"v_exp_f32_e32 v180, v180\n"
"ds_read_b64_tr_b16 v[192:193], v253 offset:16384\n"
"v_exp_f32_e32 v181, v181\n"
"v_exp_f32_e32 v182, v182\n"
"v_mfma_f32_16x16x32_bf16 v[160:163], v[188:191], v[216:219], v[160:163]\n"
"s_add_u32 s99, s57, 0x40000\n"
"buffer_load_dwordx4 v[216:219], v0, s[4:7], s99 offen sc1\n"
"ds_read_b64_tr_b16 v[194:195], v16 offset:16640\n"
"v_exp_f32_e32 v183, v183\n"
"v_mfma_f32_16x16x32_bf16 v[164:167], v[88:91], v[128:131], v[164:167]\n"
"v_exp_f32_e32 v208, v208\n"
"v_add_u32_e32 v248, 0x26400, v0\n"
"v_exp_f32_e32 v209, v209\n"
"v_mfma_f32_16x16x32_bf16 v[168:171], v[212:215], v[128:131], v[168:171]\n"
"v_exp_f32_e32 v210, v210\n"
"buffer_load_dwordx4 v[212:215], v0, s[4:7], s99 offen offset:1024 sc1\n"
"ds_read_b128 v[232:235], v248\n"
"v_exp_f32_e32 v211, v211\n"
"v_mfma_f32_16x16x32_bf16 v[172:175], v[188:191], v[128:131], v[172:175]\n"
"v_cvt_pk_bf16_f32 v128, v180, v181\n"
"ds_read_b128 v[188:191], v248 offset:1024\n"
"v_cvt_pk_bf16_f32 v129, v182, v183\n"
"v_cvt_pk_bf16_f32 v130, v208, v209\n"
"v_cvt_pk_bf16_f32 v131, v210, v211\n"
"s_waitcnt lgkmcnt(6)\n"
"v_mfma_f32_16x16x32_bf16 v[152:155], v[88:91], v[196:199], v[152:155]\n"
"ds_read_b64_tr_b16 v[180:181], v16\n"
"v_cndmask_b32_e64 v136, v100, v136, s[20:21]\n"
"v_cndmask_b32_e64 v137, v101, v137, s[22:23]\n"
"v_cndmask_b32_e64 v138, v102, v138, s[24:25]\n"
"buffer_load_dwordx4 v[208:211], v0, s[4:7], s99 offen offset:2048 sc1\n"
"v_cndmask_b32_e64 v139, v103, v139, s[26:27]\n"
"v_cndmask_b32_e64 v224, v108, v224, s[28:29]\n"
"v_cndmask_b32_e64 v225, v109, v225, s[30:31]\n"
"v_cndmask_b32_e64 v226, v110, v226, s[32:33]\n"
"v_cndmask_b32_e64 v227, v111, v227, s[34:35]\n"
"v_exp_f32_e32 v136, v136\n"
"v_mfma_f32_16x16x32_bf16 v[124:127], v[184:187], v[196:199], v[124:127]\n"
"v_exp_f32_e32 v137, v137\n"
"v_exp_f32_e32 v138, v138\n"
"ds_read_b64_tr_b16 v[182:183], v253 offset:256\n"
"buffer_load_dwordx4 v[100:103], v0, s[4:7], s99 offen offset:3072 sc1\n"
"v_exp_f32_e32 v139, v139\n"
"v_exp_f32_e32 v224, v224\n"
"v_mfma_f32_16x16x32_bf16 v[160:163], v[176:179], v[196:199], v[160:163]\n"
"v_exp_f32_e32 v225, v225\n"
"v_exp_f32_e32 v226, v226\n"
"v_exp_f32_e32 v227, v227\n"
"v_cvt_pk_bf16_f32 v108, v136, v137\n"
"buffer_load_dwordx4 v[196:199], v1, s[4:7], s99 offen sc1\n"
"v_cvt_pk_bf16_f32 v109, v138, v139\n"
"v_cvt_pk_bf16_f32 v110, v224, v225\n"
"v_mfma_f32_16x16x32_bf16 v[164:167], v[88:91], v[128:131], v[164:167]\n"
"v_cvt_pk_bf16_f32 v111, v226, v227\n"
"ds_read_b64_tr_b16 v[136:137], v253\n"
"v_cndmask_b32_e64 v220, v104, v220, s[36:37]\n"
"v_cndmask_b32_e64 v221, v105, v221, s[38:39]\n"
"v_cndmask_b32_e64 v222, v106, v222, s[40:41]\n"
"v_cndmask_b32_e64 v223, v107, v223, s[42:43]\n"
"v_cndmask_b32_e64 v228, v112, v228, s[44:45]\n"
"v_cndmask_b32_e64 v229, v113, v229, s[46:47]\n"
"v_cndmask_b32_e64 v230, v114, v230, s[48:49]\n"
"buffer_load_dwordx4 v[104:107], v1, s[4:7], s99 offen offset:1024 sc1\n"
"v_cndmask_b32_e64 v231, v115, v231, s[50:51]\n"
"v_mfma_f32_16x16x32_bf16 v[168:171], v[184:187], v[128:131], v[168:171]\n"
"v_exp_f32_e32 v220, v220\n"
"v_exp_f32_e32 v221, v221\n"
"v_exp_f32_e32 v222, v222\n"
"ds_read_b64_tr_b16 v[138:139], v16 offset:256\n"
"v_exp_f32_e32 v223, v223\n"
"v_exp_f32_e32 v228, v228\n"
"buffer_load_dwordx4 v[112:115], v1, s[4:7], s99 offen offset:2048 sc1\n"
"v_mfma_f32_16x16x32_bf16 v[172:175], v[176:179], v[128:131], v[172:175]\n"
"v_exp_f32_e32 v229, v229\n"
"v_exp_f32_e32 v230, v230\n"
"v_exp_f32_e32 v231, v231\n"
"v_cvt_pk_bf16_f32 v128, v220, v221\n"
"v_cvt_pk_bf16_f32 v129, v222, v223\n"
"v_cvt_pk_bf16_f32 v130, v228, v229\n"
"v_cvt_pk_bf16_f32 v131, v230, v231\n"
"s_waitcnt lgkmcnt(0)\n"
"v_mfma_f32_16x16x32_bf16 v[152:155], v[88:91], v[108:111], v[152:155]\n"
"v_and_b32_e32 v232, v108, v232\n"
"v_and_b32_e32 v233, v109, v233\n"
"v_and_b32_e32 v234, v110, v234\n"
"v_and_b32_e32 v235, v111, v235\n"
"v_mfma_f32_16x16x32_bf16 v[164:167], v[88:91], v[128:131], v[164:167]\n"
"v_xor_b32_e32 v176, v108, v232\n"
"v_xor_b32_e32 v177, v109, v233\n"
"v_xor_b32_e32 v178, v110, v234\n"
"v_xor_b32_e32 v179, v111, v235\n"
"v_mfma_f32_16x16x32_bf16 v[124:127], v[180:183], v[232:235], v[124:127]\n"
"v_and_b32_e32 v188, v128, v188\n"
"v_and_b32_e32 v189, v129, v189\n"
"v_mfma_f32_16x16x32_bf16 v[160:163], v[136:139], v[232:235], v[160:163]\n"
"v_and_b32_e32 v190, v130, v190\n"
"v_and_b32_e32 v191, v131, v191\n"
"v_mfma_f32_16x16x32_bf16 v[124:127], v[120:123], v[176:179], v[124:127]\n"
"v_xor_b32_e32 v108, v128, v188\n"
"v_xor_b32_e32 v109, v129, v189\n"
"v_mfma_f32_16x16x32_bf16 v[160:163], v[192:195], v[176:179], v[160:163]\n"
"v_xor_b32_e32 v110, v130, v190\n"
"v_xor_b32_e32 v111, v131, v191\n"
"v_mfma_f32_16x16x32_bf16 v[168:171], v[180:183], v[188:191], v[168:171]\n"
"buffer_load_dwordx4 v[128:131], v1, s[4:7], s99 offen offset:3072 sc1\n"
"s_add_u32 s99, s58, 0x40000\n"
"buffer_load_dwordx4 v[176:179], v0, s[4:7], s99 offen sc1\n"
"v_mfma_f32_16x16x32_bf16 v[172:175], v[136:139], v[188:191], v[172:175]\n"
"buffer_load_dwordx4 v[136:139], v0, s[4:7], s99 offen offset:1024 sc1\n"
"v_mfma_f32_16x16x32_bf16 v[168:171], v[120:123], v[108:111], v[168:171]\n"
"buffer_load_dwordx4 v[120:123], v0, s[4:7], s99 offen offset:2048 sc1\n"
"v_mfma_f32_16x16x32_bf16 v[172:175], v[192:195], v[108:111], v[172:175]\n"
"buffer_load_dwordx4 v[108:111], v0, s[4:7], s99 offen offset:3072 sc1\n"
"v_max_f32_e32 v248, v152, v164\n"
"v_rcp_f32_e32 v152, v152\n"
"v_rcp_f32_e32 v164, v164\n"
"s_nop 0\n"
"v_max3_f32 v248, v248, v152, v164\n"
"v_cmp_nlt_f32_e32 vcc, v248, v251\n"
"s_cbranch_vccnz .Lmk_b28\n"
".Lmk_k33:\n"
"v_mul_f32_e32 v124, v124, v152\n"
"v_mul_f32_e32 v125, v125, v152\n"
"v_mul_f32_e32 v126, v126, v152\n"
"v_mul_f32_e32 v127, v127, v152\n"
"v_mul_f32_e32 v160, v160, v152\n"
"v_mul_f32_e32 v161, v161, v152\n"
"v_mul_f32_e32 v162, v162, v152\n"
"v_mul_f32_e32 v163, v163, v152\n"
"v_cvt_pk_bf16_f32 v88, v124, v125\n"
"v_cvt_pk_bf16_f32 v89, v126, v127\n"
"v_cvt_pk_bf16_f32 v90, v160, v161\n"
"v_cvt_pk_bf16_f32 v91, v162, v163\n"
"buffer_store_dwordx4 v[88:91], v0, s[4:7], s60 offen sc1\n"
"v_mul_f32_e32 v168, v168, v164\n"
"v_mul_f32_e32 v169, v169, v164\n"
"v_mul_f32_e32 v170, v170, v164\n"
"v_mul_f32_e32 v171, v171, v164\n"
"v_mul_f32_e32 v172, v172, v164\n"
"v_mul_f32_e32 v173, v173, v164\n"
"v_mul_f32_e32 v174, v174, v164\n"
"v_mul_f32_e32 v175, v175, v164\n"
"v_cvt_pk_bf16_f32 v88, v168, v169\n"
"v_cvt_pk_bf16_f32 v89, v170, v171\n"
"v_cvt_pk_bf16_f32 v90, v172, v173\n"
"v_cvt_pk_bf16_f32 v91, v174, v175\n"
"s_add_u32 s98, s60, 0x8000\n"
"buffer_store_dwordx4 v[88:91], v0, s[4:7], s98 offen sc1\n"
"s_waitcnt vmcnt(21)\n"
"; @A G2 b.0\n"
"s_nop 0\n"
"v_mfma_f32_16x16x32_bf16 v[88:91], a[128:131], v[144:147], 0\n"
"v_mfma_f32_16x16x32_bf16 v[92:95], a[132:135], v[144:147], 0\n"
"v_mfma_f32_16x16x32_bf16 v[96:99], a[136:139], v[144:147], 0\n"
"buffer_load_dwordx4 v[124:127], v1, s[4:7], s99 offen sc1\n"
"v_mfma_f32_16x16x32_bf16 v[152:155], a[140:143], v[144:147], 0\n"
"s_waitcnt vmcnt(21)\n"
"v_mfma_f32_16x16x32_bf16 v[88:91], a[144:147], v[140:143], v[88:91]\n"
"v_mfma_f32_16x16x32_bf16 v[92:95], a[148:151], v[140:143], v[92:95]\n"
"v_mfma_f32_16x16x32_bf16 v[96:99], a[152:155], v[140:143], v[96:99]\n"
"buffer_load_dwordx4 v[144:147], v1, s[4:7], s99 offen offset:1024 sc1\n"
"v_mfma_f32_16x16x32_bf16 v[152:155], a[156:159], v[140:143], v[152:155]\n"
"s_waitcnt vmcnt(21)\n"
"v_mfma_f32_16x16x32_bf16 v[88:91], a[160:163], v[132:135], v[88:91]\n"
"v_mfma_f32_16x16x32_bf16 v[92:95], a[164:167], v[132:135], v[92:95]\n"
"v_mfma_f32_16x16x32_bf16 v[96:99], a[168:171], v[132:135], v[96:99]\n"
"buffer_load_dwordx4 v[140:143], v1, s[4:7], s99 offen offset:2048 sc1\n"
"v_mfma_f32_16x16x32_bf16 v[152:155], a[172:175], v[132:135], v[152:155]\n"
"s_waitcnt vmcnt(21)\n"
"v_mfma_f32_16x16x32_bf16 v[88:91], a[176:179], v[156:159], v[88:91]\n"
"v_mfma_f32_16x16x32_bf16 v[92:95], a[180:183], v[156:159], v[92:95]\n"
"v_mfma_f32_16x16x32_bf16 v[96:99], a[184:187], v[156:159], v[96:99]\n"
"buffer_load_dwordx4 v[132:135], v1, s[4:7], s99 offen offset:3072 sc1\n"
"v_mfma_f32_16x16x32_bf16 v[152:155], a[188:191], v[156:159], v[152:155]\n"
"s_waitcnt vmcnt(21)\n"
"v_mfma_f32_16x16x32_bf16 v[88:91], a[192:195], v[200:203], v[88:91]\n"
"s_add_u32 s99, s59, 0x40000\n"
"v_mfma_f32_16x16x32_bf16 v[92:95], a[196:199], v[200:203], v[92:95]\n"
"v_mfma_f32_16x16x32_bf16 v[96:99], a[200:203], v[200:203], v[96:99]\n"
"buffer_load_dwordx4 v[156:159], v0, s[4:7], s99 offen sc1\n"
"v_mfma_f32_16x16x32_bf16 v[152:155], a[204:207], v[200:203], v[152:155]\n"
"s_waitcnt vmcnt(21)\n"
"v_mfma_f32_16x16x32_bf16 v[88:91], a[208:211], v[204:207], v[88:91]\n"
"v_mfma_f32_16x16x32_bf16 v[92:95], a[212:215], v[204:207], v[92:95]\n"
"v_mfma_f32_16x16x32_bf16 v[96:99], a[216:219], v[204:207], v[96:99]\n"
"buffer_load_dwordx4 v[160:163], v0, s[4:7], s99 offen offset:1024 sc1\n"
"v_mfma_f32_16x16x32_bf16 v[152:155], a[220:223], v[204:207], v[152:155]\n"
"s_waitcnt vmcnt(21)\n"
"v_mfma_f32_16x16x32_bf16 v[88:91], a[224:227], v[116:119], v[88:91]\n"
"v_mfma_f32_16x16x32_bf16 v[92:95], a[228:231], v[116:119], v[92:95]\n"
"v_mfma_f32_16x16x32_bf16 v[96:99], a[232:235], v[116:119], v[96:99]\n"
"buffer_load_dwordx4 v[164:167], v0, s[4:7], s99 offen offset:2048 sc1\n"
"v_mfma_f32_16x16x32_bf16 v[152:155], a[236:239], v[116:119], v[152:155]\n"
"s_waitcnt vmcnt(21)\n"
"v_mfma_f32_16x16x32_bf16 v[88:91], a[240:243], v[148:151], v[88:91]\n"
"v_mfma_f32_16x16x32_bf16 v[92:95], a[244:247], v[148:151], v[92:95]\n"
"v_mfma_f32_16x16x32_bf16 v[96:99], a[248:251], v[148:151], v[96:99]\n"
"buffer_load_dwordx4 v[116:119], v0, s[4:7], s99 offen offset:3072 sc1\n"
"v_mfma_f32_16x16x32_bf16 v[152:155], a[252:255], v[148:151], v[152:155]\n"
"s_waitcnt vmcnt(20)\n"
"; @A G2 b.1\n"
"v_mfma_f32_16x16x32_bf16 v[148:151], a[128:131], v[216:219], 0\n"
"s_nop 1\n"
"v_cmp_u_f32_e32 vcc, v88, v88\n"
"v_mfma_f32_16x16x32_bf16 v[168:171], a[132:135], v[216:219], 0\n"
"s_cbranch_vccnz .Lmk_p34s\n"
".Lmk_p34r:\n"
"v_mfma_f32_16x16x32_bf16 v[172:175], a[136:139], v[216:219], 0\n"
"buffer_load_dwordx4 v[180:183], v1, s[4:7], s99 offen sc1\n"
"v_mfma_f32_16x16x32_bf16 v[184:187], a[140:143], v[216:219], 0\n"
"v_mfma_f32_16x16x32_bf16 v[148:151], a[144:147], v[212:215], v[148:151]\n"
"ds_write_b128 v3, v[88:91]\n"
"v_mfma_f32_16x16x32_bf16 v[168:171], a[148:151], v[212:215], v[168:171]\n"
"v_mfma_f32_16x16x32_bf16 v[172:175], a[152:155], v[212:215], v[172:175]\n"
"ds_write_b128 v3, v[92:95] offset:1024\n"
"v_mfma_f32_16x16x32_bf16 v[184:187], a[156:159], v[212:215], v[184:187]\n"
"s_waitcnt vmcnt(19)\n"
"v_mfma_f32_16x16x32_bf16 v[148:151], a[160:163], v[208:211], v[148:151]\n"
"ds_write_b128 v3, v[96:99] offset:2048\n"
"v_mfma_f32_16x16x32_bf16 v[168:171], a[164:167], v[208:211], v[168:171]\n"
"buffer_load_dwordx4 v[88:91], v1, s[4:7], s99 offen offset:1024 sc1\n"
"v_mfma_f32_16x16x32_bf16 v[172:175], a[168:171], v[208:211], v[172:175]\n"
"ds_write_b128 v3, v[152:155] offset:3072\n"
"v_mfma_f32_16x16x32_bf16 v[184:187], a[172:175], v[208:211], v[184:187]\n"
"v_mfma_f32_16x16x32_bf16 v[148:151], a[176:179], v[100:103], v[148:151]\n"
"v_mfma_f32_16x16x32_bf16 v[168:171], a[180:183], v[100:103], v[168:171]\n"
"buffer_load_dwordx4 v[92:95], v1, s[4:7], s99 offen offset:2048 sc1\n"
"v_mfma_f32_16x16x32_bf16 v[172:175], a[184:187], v[100:103], v[172:175]\n"
"ds_read_b128 v[96:99], v2 offset:8192\n"
"ds_read_b128 v[152:155], v2 offset:9216\n"
"v_mfma_f32_16x16x32_bf16 v[184:187], a[188:191], v[100:103], v[184:187]\n"
"s_waitcnt vmcnt(19)\n"
"v_mfma_f32_16x16x32_bf16 v[148:151], a[192:195], v[196:199], v[148:151]\n"
"v_mfma_f32_16x16x32_bf16 v[168:171], a[196:199], v[196:199], v[168:171]\n"
"buffer_load_dwordx4 v[100:103], v1, s[4:7], s99 offen offset:3072 sc1\n"
"v_mfma_f32_16x16x32_bf16 v[172:175], a[200:203], v[196:199], v[172:175]\n"
"v_mfma_f32_16x16x32_bf16 v[184:187], a[204:207], v[196:199], v[184:187]\n"
"s_waitcnt lgkmcnt(0)\n"
"s_barrier\n"
"v_mfma_f32_16x16x32_bf16 v[148:151], a[208:211], v[104:107], v[148:151]\n"
"v_lshlrev_b32_e32 v255, 16, v96\n"
"v_mfma_f32_16x16x32_bf16 v[168:171], a[212:215], v[104:107], v[168:171]\n"
"ds_read_b128 v[188:191], v4\n"
"ds_read_b128 v[192:195], v4 offset:1024\n"
"v_mfma_f32_16x16x32_bf16 v[172:175], a[216:219], v[104:107], v[172:175]\n"
"ds_read_b128 v[196:199], v4 offset:2048\n"
"ds_read_b128 v[200:203], v4 offset:3072\n"
"v_mfma_f32_16x16x32_bf16 v[184:187], a[220:223], v[104:107], v[184:187]\n"
"s_waitcnt vmcnt(18)\n"
"v_and_b32_e32 v254, 0xffff0000, v96\n"
"v_mfma_f32_16x16x32_bf16 v[148:151], a[224:227], v[112:115], v[148:151]\n"
"v_lshlrev_b32_e32 v253, 16, v97\n"
"v_and_b32_e32 v252, 0xffff0000, v97\n"
"v_mfma_f32_16x16x32_bf16 v[168:171], a[228:231], v[112:115], v[168:171]\n"
"v_lshlrev_b32_e32 v251, 16, v98\n"
"v_and_b32_e32 v250, 0xffff0000, v98\n"
"v_mfma_f32_16x16x32_bf16 v[172:175], a[232:235], v[112:115], v[172:175]\n"
"v_lshlrev_b32_e32 v249, 16, v99\n"
"v_and_b32_e32 v248, 0xffff0000, v99\n"
"v_mfma_f32_16x16x32_bf16 v[184:187], a[236:239], v[112:115], v[184:187]\n"
"v_lshlrev_b32_e32 v247, 16, v152\n"
"v_and_b32_e32 v246, 0xffff0000, v152\n"
"v_mfma_f32_16x16x32_bf16 v[148:151], a[240:243], v[128:131], v[148:151]\n"
"v_lshlrev_b32_e32 v245, 16, v153\n"
"v_and_b32_e32 v244, 0xffff0000, v153\n"
"v_mfma_f32_16x16x32_bf16 v[168:171], a[244:247], v[128:131], v[168:171]\n"
"v_lshlrev_b32_e32 v243, 16, v154\n"
"v_and_b32_e32 v242, 0xffff0000, v154\n"
"v_mfma_f32_16x16x32_bf16 v[172:175], a[248:251], v[128:131], v[172:175]\n"
"v_lshlrev_b32_e32 v241, 16, v155\n"
"v_and_b32_e32 v240, 0xffff0000, v155\n"
"v_mfma_f32_16x16x32_bf16 v[184:187], a[252:255], v[128:131], v[184:187]\n"
"s_waitcnt vmcnt(16)\n"
"s_waitcnt lgkmcnt(0)\n"
"; @A G2 b.2\n"
"v_mfma_f32_16x16x32_bf16 v[96:99], a[128:131], v[176:179], 0\n"
"v_cmp_u_f32_e32 vcc, v148, v148\n"
"v_add_f32_e32 v188, v188, v255\n"
"v_mfma_f32_16x16x32_bf16 v[104:107], a[132:135], v[176:179], 0\n"
"s_cbranch_vccnz .Lmk_p35s\n"
".Lmk_p35r:\n"
"v_add_f32_e32 v189, v189, v254\n"
"v_mfma_f32_16x16x32_bf16 v[112:115], a[136:139], v[176:179], 0\n"
"v_add_f32_e32 v190, v190, v253\n"
"v_add_f32_e32 v191, v191, v252\n"
"v_mfma_f32_16x16x32_bf16 v[128:131], a[140:143], v[176:179], 0\n"
"v_add_f32_e32 v192, v192, v251\n"
"v_add_f32_e32 v193, v193, v250\n"
"v_mfma_f32_16x16x32_bf16 v[96:99], a[144:147], v[136:139], v[96:99]\n"
"ds_write_b128 v3, v[148:151] offset:6144\n"
"v_mfma_f32_16x16x32_bf16 v[104:107], a[148:151], v[136:139], v[104:107]\n"
"v_add_f32_e32 v194, v194, v249\n"
"v_add_f32_e32 v195, v195, v248\n"
"v_mfma_f32_16x16x32_bf16 v[112:115], a[152:155], v[136:139], v[112:115]\n"
"ds_write_b128 v3, v[168:171] offset:7168\n"
"v_mfma_f32_16x16x32_bf16 v[128:131], a[156:159], v[136:139], v[128:131]\n"
"s_waitcnt vmcnt(14)\n"
"v_add_f32_e32 v196, v196, v247\n"
"v_mfma_f32_16x16x32_bf16 v[96:99], a[160:163], v[120:123], v[96:99]\n"
"ds_write_b128 v3, v[172:175] offset:8192\n"
"v_mfma_f32_16x16x32_bf16 v[104:107], a[164:167], v[120:123], v[104:107]\n"
"v_add_f32_e32 v197, v197, v246\n"
"v_add_f32_e32 v198, v198, v245\n"
"v_mfma_f32_16x16x32_bf16 v[112:115], a[168:171], v[120:123], v[112:115]\n"
"ds_write_b128 v3, v[184:187] offset:9216\n"
"v_mfma_f32_16x16x32_bf16 v[128:131], a[172:175], v[120:123], v[128:131]\n"
"v_add_f32_e32 v199, v199, v244\n"
"v_add_f32_e32 v200, v200, v243\n"
"v_mfma_f32_16x16x32_bf16 v[96:99], a[176:179], v[108:111], v[96:99]\n"
"s_add_u32 s99, s56, 0\n"
"v_add_f32_e32 v201, v201, v242\n"
"v_mfma_f32_16x16x32_bf16 v[104:107], a[180:183], v[108:111], v[104:107]\n"
"buffer_load_dwordx4 v[120:123], v0, s[4:7], s99 offen sc1\n"
"v_mfma_f32_16x16x32_bf16 v[112:115], a[184:187], v[108:111], v[112:115]\n"
"v_add_f32_e32 v202, v202, v241\n"
"v_add_f32_e32 v203, v203, v240\n"
"v_mfma_f32_16x16x32_bf16 v[128:131], a[188:191], v[108:111], v[128:131]\n"
"s_waitcnt vmcnt(11)\n"
"v_mfma_f32_16x16x32_bf16 v[96:99], a[192:195], v[124:127], v[96:99]\n"
"buffer_load_dwordx4 v[108:111], v0, s[4:7], s99 offen offset:1024 sc1\n"
"v_mfma_f32_16x16x32_bf16 v[104:107], a[196:199], v[124:127], v[104:107]\n"
"v_mfma_f32_16x16x32_bf16 v[112:115], a[200:203], v[124:127], v[112:115]\n"
"v_mfma_f32_16x16x32_bf16 v[128:131], a[204:207], v[124:127], v[128:131]\n"
"buffer_load_dwordx4 v[124:127], v0, s[4:7], s99 offen offset:2048 sc1\n"
"v_mfma_f32_16x16x32_bf16 v[96:99], a[208:211], v[144:147], v[96:99]\n"
"v_mfma_f32_16x16x32_bf16 v[104:107], a[212:215], v[144:147], v[104:107]\n"
"v_mfma_f32_16x16x32_bf16 v[112:115], a[216:219], v[144:147], v[112:115]\n"
"buffer_load_dwordx4 v[136:139], v0, s[4:7], s99 offen offset:3072 sc1\n"
"v_mfma_f32_16x16x32_bf16 v[128:131], a[220:223], v[144:147], v[128:131]\n"
"s_waitcnt vmcnt(12)\n"
"v_mfma_f32_16x16x32_bf16 v[96:99], a[224:227], v[140:143], v[96:99]\n"
"s_waitcnt vmcnt(10)\n"
"v_max_u32_e32 v255, v156, v157\n"
"v_mfma_f32_16x16x32_bf16 v[104:107], a[228:231], v[140:143], v[104:107]\n"
"v_max3_u32 v255, v255, v158, v159\n"
"v_max3_u32 v255, v255, v160, v161\n"
"v_mfma_f32_16x16x32_bf16 v[112:115], a[232:235], v[140:143], v[112:115]\n"
"v_max3_u32 v255, v255, v162, v163\n"
"s_waitcnt vmcnt(8)\n"
"v_mfma_f32_16x16x32_bf16 v[128:131], a[236:239], v[140:143], v[128:131]\n"
"v_max3_u32 v255, v255, v164, v165\n"
"v_max3_u32 v255, v255, v166, v167\n"
"v_mfma_f32_16x16x32_bf16 v[96:99], a[240:243], v[132:135], v[96:99]\n"
"v_max3_u32 v255, v255, v116, v117\n"
"v_max3_u32 v255, v255, v118, v119\n"
"v_mfma_f32_16x16x32_bf16 v[104:107], a[244:247], v[132:135], v[104:107]\n"
"v_cmp_eq_u32_e32 vcc, -1, v255\n"
"v_mfma_f32_16x16x32_bf16 v[112:115], a[248:251], v[132:135], v[112:115]\n"
"s_cbranch_vccnz .Lmk_q37s\n"
".Lmk_q37r:\n"
"v_mfma_f32_16x16x32_bf16 v[128:131], a[252:255], v[132:135], v[128:131]\n"
"; @A G2 b.3\n"
"v_mfma_f32_16x16x32_bf16 v[188:191], a[128:131], v[156:159], v[188:191]\n"
"v_cmp_u_f32_e32 vcc, v96, v96\n"
"v_mfma_f32_16x16x32_bf16 v[192:195], a[132:135], v[156:159], v[192:195]\n"
"s_cbranch_vccnz .Lmk_p36s\n"
".Lmk_p36r:\n"
"v_mfma_f32_16x16x32_bf16 v[196:199], a[136:139], v[156:159], v[196:199]\n"
"buffer_load_dwordx4 v[132:135], v1, s[4:7], s99 offen sc1\n"
"v_mfma_f32_16x16x32_bf16 v[200:203], a[140:143], v[156:159], v[200:203]\n"
"v_mfma_f32_16x16x32_bf16 v[188:191], a[144:147], v[160:163], v[188:191]\n"
"ds_write_b128 v3, v[96:99] offset:12288\n"
"v_mfma_f32_16x16x32_bf16 v[192:195], a[148:151], v[160:163], v[192:195]\n"
"buffer_load_dwordx4 v[96:99], v1, s[4:7], s99 offen offset:1024 sc1\n"
"v_mfma_f32_16x16x32_bf16 v[196:199], a[152:155], v[160:163], v[196:199]\n"
"ds_write_b128 v3, v[104:107] offset:13312\n"
"v_mfma_f32_16x16x32_bf16 v[200:203], a[156:159], v[160:163], v[200:203]\n"
"v_mfma_f32_16x16x32_bf16 v[188:191], a[160:163], v[164:167], v[188:191]\n"
"s_waitcnt vmcnt(8)\n"
"v_max_u32_e32 v255, v180, v181\n"
"v_mfma_f32_16x16x32_bf16 v[192:195], a[164:167], v[164:167], v[192:195]\n"
"v_max3_u32 v255, v255, v182, v183\n"
"v_max3_u32 v255, v255, v88, v89\n"
"v_mfma_f32_16x16x32_bf16 v[196:199], a[168:171], v[164:167], v[196:199]\n"
"v_max3_u32 v255, v255, v90, v91\n"
"s_waitcnt vmcnt(6)\n"
"v_mfma_f32_16x16x32_bf16 v[200:203], a[172:175], v[164:167], v[200:203]\n"
"v_max3_u32 v255, v255, v92, v93\n"
"v_max3_u32 v255, v255, v94, v95\n"
"v_mfma_f32_16x16x32_bf16 v[188:191], a[176:179], v[116:119], v[188:191]\n"
"v_max3_u32 v255, v255, v100, v101\n"
"v_max3_u32 v255, v255, v102, v103\n"
"v_mfma_f32_16x16x32_bf16 v[192:195], a[180:183], v[116:119], v[192:195]\n"
"v_cmp_eq_u32_e32 vcc, -1, v255\n"
"v_mfma_f32_16x16x32_bf16 v[196:199], a[184:187], v[116:119], v[196:199]\n"
"s_cbranch_vccnz .Lmk_q38s\n"
".Lmk_q38r:\n"
"v_mfma_f32_16x16x32_bf16 v[200:203], a[188:191], v[116:119], v[200:203]\n"
"v_mfma_f32_16x16x32_bf16 v[188:191], a[192:195], v[180:183], v[188:191]\n"
"ds_write_b128 v3, v[112:115] offset:14336\n"
"v_mfma_f32_16x16x32_bf16 v[192:195], a[196:199], v[180:183], v[192:195]\n"
"buffer_load_dwordx4 v[104:107], v1, s[4:7], s99 offen offset:2048 sc1\n"
"v_mfma_f32_16x16x32_bf16 v[196:199], a[200:203], v[180:183], v[196:199]\n"
"ds_write_b128 v3, v[128:131] offset:15360\n"
"v_mfma_f32_16x16x32_bf16 v[200:203], a[204:207], v[180:183], v[200:203]\n"
"v_mfma_f32_16x16x32_bf16 v[188:191], a[208:211], v[88:91], v[188:191]\n"
"buffer_load_dwordx4 v[112:115], v1, s[4:7], s99 offen offset:3072 sc1\n"
"v_mfma_f32_16x16x32_bf16 v[192:195], a[212:215], v[88:91], v[192:195]\n"
"s_waitcnt lgkmcnt(0)\n"
"s_barrier\n"
"v_mfma_f32_16x16x32_bf16 v[196:199], a[216:219], v[88:91], v[196:199]\n"
"s_add_u32 s99, s57, 0\n"
"ds_read_b128 v[116:119], v5\n"
"v_mfma_f32_16x16x32_bf16 v[200:203], a[220:223], v[88:91], v[200:203]\n"
"ds_read_b128 v[88:91], v6\n"
"v_mfma_f32_16x16x32_bf16 v[188:191], a[224:227], v[92:95], v[188:191]\n"
"buffer_load_dwordx4 v[128:131], v0, s[4:7], s99 offen sc1\n"
"v_mfma_f32_16x16x32_bf16 v[192:195], a[228:231], v[92:95], v[192:195]\n"
"ds_read_b128 v[140:143], v5 offset:1024\n"
"ds_read_b128 v[144:147], v6 offset:1024\n"
"v_mfma_f32_16x16x32_bf16 v[196:199], a[232:235], v[92:95], v[196:199]\n"
"ds_read_b128 v[148:151], v5 offset:2048\n"
"ds_read_b128 v[152:155], v6 offset:2048\n"
"v_mfma_f32_16x16x32_bf16 v[200:203], a[236:239], v[92:95], v[200:203]\n"
"buffer_load_dwordx4 v[92:95], v0, s[4:7], s99 offen offset:1024 sc1\n"
"v_mfma_f32_16x16x32_bf16 v[188:191], a[240:243], v[100:103], v[188:191]\n"
"ds_read_b128 v[156:159], v5 offset:3072\n"
"ds_read_b128 v[160:163], v6 offset:3072\n"
"v_mfma_f32_16x16x32_bf16 v[192:195], a[244:247], v[100:103], v[192:195]\n"
"v_mfma_f32_16x16x32_bf16 v[196:199], a[248:251], v[100:103], v[196:199]\n"
"buffer_load_dwordx4 v[164:167], v0, s[4:7], s99 offen offset:2048 sc1\n"
"v_mfma_f32_16x16x32_bf16 v[200:203], a[252:255], v[100:103], v[200:203]\n"
"s_waitcnt lgkmcnt(4)\n"
"v_add_f32_e32 v116, v116, v88\n"
"v_add_f32_e32 v117, v117, v89\n"
"v_add_f32_e32 v118, v118, v90\n"
"v_add_f32_e32 v119, v119, v91\n"
"v_add_f32_e32 v140, v140, v144\n"
"v_add_f32_e32 v141, v141, v145\n"
"v_add_f32_e32 v142, v142, v146\n"
"v_add_f32_e32 v143, v143, v147\n"
"buffer_load_dwordx4 v[88:91], v0, s[4:7], s99 offen offset:3072 sc1\n"
"s_waitcnt lgkmcnt(0)\n"
"v_add_f32_e32 v148, v148, v152\n"
"v_add_f32_e32 v149, v149, v153\n"
"v_add_f32_e32 v150, v150, v154\n"
"v_add_f32_e32 v151, v151, v155\n"
"v_add_f32_e32 v156, v156, v160\n"
"v_add_f32_e32 v157, v157, v161\n"
"v_add_f32_e32 v158, v158, v162\n"
"v_add_f32_e32 v159, v159, v163\n"
"buffer_load_dwordx4 v[100:103], v1, s[4:7], s99 offen sc1\n"
"buffer_load_dwordx4 v[144:147], v1, s[4:7], s99 offen offset:1024 sc1\n"
"buffer_load_dwordx4 v[152:155], v1, s[4:7], s99 offen offset:2048 sc1\n"
"buffer_load_dwordx4 v[160:163], v1, s[4:7], s99 offen offset:3072 sc1\n"
"; @A fin b\n"
"v_mov_b32_e32 v168, 0x3f803f80\n"
"v_mov_b32_e32 v169, 0x3f803f80\n"
"v_mov_b32_e32 v170, 0x3f803f80\n"
"v_mov_b32_e32 v171, 0x3f803f80\n"
"v_add_f32_e32 v116, v188, v116\n"
"v_add_f32_e32 v117, v189, v117\n"
"v_add_f32_e32 v118, v190, v118\n"
"v_add_f32_e32 v119, v191, v119\n"
"v_add_f32_e32 v140, v192, v140\n"
"v_add_f32_e32 v141, v193, v141\n"
"v_add_f32_e32 v142, v194, v142\n"
"v_add_f32_e32 v143, v195, v143\n"
"v_cvt_pk_bf16_f32 v172, v116, v117\n"
"v_cvt_pk_bf16_f32 v173, v118, v119\n"
"v_cvt_pk_bf16_f32 v174, v140, v141\n"
"v_cvt_pk_bf16_f32 v175, v142, v143\n"
"s_add_u32 s99, s61, 0x440000\n"
"buffer_store_dwordx4 v[172:175], v0, s[4:7], s99 offen sc1\n"
"ds_write_b128 v2, v[172:175] offset:8192\n"
"v_add_f32_e32 v148, v196, v148\n"
"v_add_f32_e32 v149, v197, v149\n"
"v_add_f32_e32 v150, v198, v150\n"
"v_add_f32_e32 v151, v199, v151\n"
"v_add_f32_e32 v156, v200, v156\n"
"v_add_f32_e32 v157, v201, v157\n"
"v_add_f32_e32 v158, v202, v158\n"
"v_add_f32_e32 v159, v203, v159\n"
"v_cvt_pk_bf16_f32 v116, v148, v149\n"
"v_cvt_pk_bf16_f32 v117, v150, v151\n"
"v_cvt_pk_bf16_f32 v118, v156, v157\n"
"v_cvt_pk_bf16_f32 v119, v158, v159\n"
"s_add_u32 s99, s61, 0x440000\n"
"buffer_store_dwordx4 v[116:119], v0, s[4:7], s99 offen offset:1024 sc1\n"
"ds_write_b128 v2, v[116:119] offset:9216\n"
"v_mfma_f32_16x16x32_bf16 v[140:143], v[168:171], v[172:175], 0\n"
"v_mfma_f32_16x16x32_bf16 v[148:151], v[172:175], v[172:175], 0\n"
"v_mfma_f32_16x16x32_bf16 v[140:143], v[168:171], v[116:119], v[140:143]\n"
"v_mfma_f32_16x16x32_bf16 v[148:151], v[116:119], v[116:119], v[148:151]\n"
"s_mov_b32 s98, 0xaaaaaaaa\n"
"s_mov_b32 s99, 0xaaaaaaaa\n"
"s_nop 5\n"
"v_cndmask_b32_e64 v148, v148, v149, s[98:99]\n"
"v_cndmask_b32_e64 v150, v150, v151, s[98:99]\n"
"s_mov_b32 s98, 0xcccccccc\n"
"s_mov_b32 s99, 0xcccccccc\n"
"v_cndmask_b32_e64 v141, v148, v150, s[98:99]\n"
"s_add_u32 s99, s63, 0x44000\n"
"buffer_store_dwordx2 v[140:141], v14, s[4:7], s99 offen sc1\n"
"s_add_u32 s99, s58, 0\n"
"buffer_load_dwordx4 v[116:119], v0, s[4:7], s99 offen sc1\n"
"buffer_load_dwordx4 v[140:143], v0, s[4:7], s99 offen offset:1024 sc1\n"
"buffer_load_dwordx4 v[148:151], v0, s[4:7], s99 offen offset:2048 sc1\n"
"buffer_load_dwordx4 v[156:159], v0, s[4:7], s99 offen offset:3072 sc1\n"
"buffer_load_dwordx4 v[168:171], v1, s[4:7], s99 offen sc1\n"
"buffer_load_dwordx4 v[172:175], v1, s[4:7], s99 offen offset:1024 sc1\n"
"buffer_load_dwordx4 v[176:179], v1, s[4:7], s99 offen offset:2048 sc1\n"
"buffer_load_dwordx4 v[180:183], v1, s[4:7], s99 offen offset:3072 sc1\n"
"s_waitcnt vmcnt(26)\n"
"; @A G2 a.0\n"
"v_mfma_f32_16x16x32_bf16 v[184:187], a[128:131], v[120:123], 0\n"
"v_mfma_f32_16x16x32_bf16 v[188:191], a[132:135], v[120:123], 0\n"
"v_mfma_f32_16x16x32_bf16 v[192:195], a[136:139], v[120:123], 0\n"
"s_add_u32 s99, s59, 0\n"
"v_mfma_f32_16x16x32_bf16 v[196:199], a[140:143], v[120:123], 0\n"
"s_waitcnt vmcnt(25)\n"
"v_mfma_f32_16x16x32_bf16 v[184:187], a[144:147], v[108:111], v[184:187]\n"
"buffer_load_dwordx4 v[200:203], v0, s[4:7], s99 offen sc1\n"
"v_mfma_f32_16x16x32_bf16 v[188:191], a[148:151], v[108:111], v[188:191]\n"
"v_mfma_f32_16x16x32_bf16 v[192:195], a[152:155], v[108:111], v[192:195]\n"
"v_mfma_f32_16x16x32_bf16 v[196:199], a[156:159], v[108:111], v[196:199]\n"
"s_waitcnt vmcnt(25)\n"
"v_mfma_f32_16x16x32_bf16 v[184:187], a[160:163], v[124:127], v[184:187]\n"
"buffer_load_dwordx4 v[204:207], v0, s[4:7], s99 offen offset:1024 sc1\n"
"v_mfma_f32_16x16x32_bf16 v[188:191], a[164:167], v[124:127], v[188:191]\n"
"v_mfma_f32_16x16x32_bf16 v[192:195], a[168:171], v[124:127], v[192:195]\n"
"v_mfma_f32_16x16x32_bf16 v[196:199], a[172:175], v[124:127], v[196:199]\n"
"s_waitcnt vmcnt(25)\n"
"v_mfma_f32_16x16x32_bf16 v[184:187], a[176:179], v[136:139], v[184:187]\n"
"buffer_load_dwordx4 v[208:211], v0, s[4:7], s99 offen offset:2048 sc1\n"
"v_mfma_f32_16x16x32_bf16 v[188:191], a[180:183], v[136:139], v[188:191]\n"
"v_mfma_f32_16x16x32_bf16 v[192:195], a[184:187], v[136:139], v[192:195]\n"
"v_mfma_f32_16x16x32_bf16 v[196:199], a[188:191], v[136:139], v[196:199]\n"
"s_waitcnt vmcnt(25)\n"
"v_mfma_f32_16x16x32_bf16 v[184:187], a[192:195], v[132:135], v[184:187]\n"
"buffer_load_dwordx4 v[212:215], v0, s[4:7], s99 offen offset:3072 sc1\n"
"v_mfma_f32_16x16x32_bf16 v[188:191], a[196:199], v[132:135], v[188:191]\n"
"v_mfma_f32_16x16x32_bf16 v[192:195], a[200:203], v[132:135], v[192:195]\n"
"v_mfma_f32_16x16x32_bf16 v[196:199], a[204:207], v[132:135], v[196:199]\n"
"s_waitcnt vmcnt(25)\n"
"v_mfma_f32_16x16x32_bf16 v[184:187], a[208:211], v[96:99], v[184:187]\n"
"buffer_load_dwordx4 v[216:219], v1, s[4:7], s99 offen sc1\n"
"v_mfma_f32_16x16x32_bf16 v[188:191], a[212:215], v[96:99], v[188:191]\n"
"v_mfma_f32_16x16x32_bf16 v[192:195], a[216:219], v[96:99], v[192:195]\n"
"v_mfma_f32_16x16x32_bf16 v[196:199], a[220:223], v[96:99], v[196:199]\n"
"s_waitcnt vmcnt(25)\n"
"v_mfma_f32_16x16x32_bf16 v[184:187], a[224:227], v[104:107], v[184:187]\n"
"buffer_load_dwordx4 v[220:223], v1, s[4:7], s99 offen offset:1024 sc1\n"
"v_mfma_f32_16x16x32_bf16 v[188:191], a[228:231], v[104:107], v[188:191]\n"
"v_mfma_f32_16x16x32_bf16 v[192:195], a[232:235], v[104:107], v[192:195]\n"
"v_mfma_f32_16x16x32_bf16 v[196:199], a[236:239], v[104:107], v[196:199]\n"
"s_waitcnt vmcnt(25)\n"
"v_mfma_f32_16x16x32_bf16 v[184:187], a[240:243], v[112:115], v[184:187]\n"
"buffer_load_dwordx4 v[224:227], v1, s[4:7], s99 offen offset:2048 sc1\n"
"v_mfma_f32_16x16x32_bf16 v[188:191], a[244:247], v[112:115], v[188:191]\n"
"v_mfma_f32_16x16x32_bf16 v[192:195], a[248:251], v[112:115], v[192:195]\n"
"v_mfma_f32_16x16x32_bf16 v[196:199], a[252:255], v[112:115], v[196:199]\n"
"s_waitcnt vmcnt(24)\n"
"; @A G2 a.1\n"
"v_mfma_f32_16x16x32_bf16 v[108:111], a[128:131], v[128:131], 0\n"
"s_nop 1\n"
"v_cmp_u_f32_e32 vcc, v184, v184\n"
"v_mfma_f32_16x16x32_bf16 v[112:115], a[132:135], v[128:131], 0\n"
"s_cbranch_vccnz .Lmk_p39s\n"
".Lmk_p39r:\n"
"v_mfma_f32_16x16x32_bf16 v[120:123], a[136:139], v[128:131], 0\n"
"buffer_load_dwordx4 v[228:231], v1, s[4:7], s99 offen offset:3072 sc1\n"
"v_mfma_f32_16x16x32_bf16 v[124:127], a[140:143], v[128:131], 0\n"
"v_mfma_f32_16x16x32_bf16 v[108:111], a[144:147], v[92:95], v[108:111]\n"
"ds_write_b128 v3, v[184:187]\n"
"v_mfma_f32_16x16x32_bf16 v[112:115], a[148:151], v[92:95], v[112:115]\n"
"v_mfma_f32_16x16x32_bf16 v[120:123], a[152:155], v[92:95], v[120:123]\n"
"ds_write_b128 v3, v[188:191] offset:1024\n"
"v_mfma_f32_16x16x32_bf16 v[124:127], a[156:159], v[92:95], v[124:127]\n"
"s_waitcnt vmcnt(23)\n"
"v_mfma_f32_16x16x32_bf16 v[108:111], a[160:163], v[164:167], v[108:111]\n"
"ds_write_b128 v3, v[192:195] offset:2048\n"
"v_mfma_f32_16x16x32_bf16 v[112:115], a[164:167], v[164:167], v[112:115]\n"
"s_add_u32 s99, s75, 0x10000\n"
"v_mfma_f32_16x16x32_bf16 v[120:123], a[168:171], v[164:167], v[120:123]\n"
"ds_write_b128 v3, v[196:199] offset:3072\n"
"v_mfma_f32_16x16x32_bf16 v[124:127], a[172:175], v[164:167], v[124:127]\n"
"buffer_load_dwordx2 v[254:255], v13, s[4:7], s99 offen sc1\n"
"v_mfma_f32_16x16x32_bf16 v[108:111], a[176:179], v[88:91], v[108:111]\n"
"v_mfma_f32_16x16x32_bf16 v[112:115], a[180:183], v[88:91], v[112:115]\n"
"ds_read_b128 v[104:107], v2\n"
"ds_read_b128 v[128:131], v2 offset:1024\n"
"v_mfma_f32_16x16x32_bf16 v[120:123], a[184:187], v[88:91], v[120:123]\n"
"v_mfma_f32_16x16x32_bf16 v[124:127], a[188:191], v[88:91], v[124:127]\n"
"s_waitcnt vmcnt(22)\n"
"v_mfma_f32_16x16x32_bf16 v[108:111], a[192:195], v[100:103], v[108:111]\n"
"v_mfma_f32_16x16x32_bf16 v[112:115], a[196:199], v[100:103], v[112:115]\n"
"v_mfma_f32_16x16x32_bf16 v[120:123], a[200:203], v[100:103], v[120:123]\n"
"s_waitcnt lgkmcnt(0)\n"
"v_lshlrev_b32_e32 v253, 16, v104\n"
"v_mfma_f32_16x16x32_bf16 v[124:127], a[204:207], v[100:103], v[124:127]\n"
"s_waitcnt lgkmcnt(0)\n"
"s_barrier\n"
"v_mfma_f32_16x16x32_bf16 v[108:111], a[208:211], v[144:147], v[108:111]\n"
"v_and_b32_e32 v252, 0xffff0000, v104\n"
"v_lshlrev_b32_e32 v251, 16, v105\n"
"v_mfma_f32_16x16x32_bf16 v[112:115], a[212:215], v[144:147], v[112:115]\n"
"ds_read_b128 v[88:91], v4\n"
"ds_read_b128 v[92:95], v4 offset:1024\n"
"v_mfma_f32_16x16x32_bf16 v[120:123], a[216:219], v[144:147], v[120:123]\n"
"ds_read_b128 v[96:99], v4 offset:2048\n"
"ds_read_b128 v[100:103], v4 offset:3072\n"
"v_mfma_f32_16x16x32_bf16 v[124:127], a[220:223], v[144:147], v[124:127]\n"
"s_waitcnt vmcnt(20)\n"
"v_and_b32_e32 v250, 0xffff0000, v105\n"
"v_mfma_f32_16x16x32_bf16 v[108:111], a[224:227], v[152:155], v[108:111]\n"
"v_lshlrev_b32_e32 v249, 16, v106\n"
"v_and_b32_e32 v248, 0xffff0000, v106\n"
"v_mfma_f32_16x16x32_bf16 v[112:115], a[228:231], v[152:155], v[112:115]\n"
"v_lshlrev_b32_e32 v247, 16, v107\n"
"v_and_b32_e32 v246, 0xffff0000, v107\n"
"v_mfma_f32_16x16x32_bf16 v[120:123], a[232:235], v[152:155], v[120:123]\n"
"v_lshlrev_b32_e32 v245, 16, v128\n"
"v_and_b32_e32 v244, 0xffff0000, v128\n"
"v_mfma_f32_16x16x32_bf16 v[124:127], a[236:239], v[152:155], v[124:127]\n"
"v_lshlrev_b32_e32 v243, 16, v129\n"
"v_and_b32_e32 v242, 0xffff0000, v129\n"
"v_mfma_f32_16x16x32_bf16 v[108:111], a[240:243], v[160:163], v[108:111]\n"
"v_lshlrev_b32_e32 v241, 16, v130\n"
"v_and_b32_e32 v240, 0xffff0000, v130\n"
"v_mfma_f32_16x16x32_bf16 v[112:115], a[244:247], v[160:163], v[112:115]\n"
"v_lshlrev_b32_e32 v239, 16, v131\n"
"v_and_b32_e32 v238, 0xffff0000, v131\n"
"v_mfma_f32_16x16x32_bf16 v[120:123], a[248:251], v[160:163], v[120:123]\n"
"s_waitcnt lgkmcnt(0)\n"
"v_add_f32_e32 v88, v88, v253\n"
"v_mfma_f32_16x16x32_bf16 v[124:127], a[252:255], v[160:163], v[124:127]\n"
"s_waitcnt vmcnt(15)\n"
"v_add_f32_e32 v89, v89, v252\n"
"; @A G2 a.2\n"
"v_mfma_f32_16x16x32_bf16 v[144:147], a[128:131], v[116:119], 0\n"
"v_cmp_u_f32_e32 vcc, v108, v108\n"
"s_add_u32 s98, s52, 0x440000\n"
"v_mfma_f32_16x16x32_bf16 v[152:155], a[132:135], v[116:119], 0\n"
"s_cbranch_vccnz .Lmk_p40s\n"
".Lmk_p40r:\n"
"v_add_f32_e32 v90, v90, v251\n"
"v_mfma_f32_16x16x32_bf16 v[160:163], a[136:139], v[116:119], 0\n"
"buffer_load_dwordx4 v[104:107], v0, s[4:7], s98 offen sc1\n"
"v_mfma_f32_16x16x32_bf16 v[164:167], a[140:143], v[116:119], 0\n"
"v_add_f32_e32 v91, v91, v250\n"
"v_add_f32_e32 v92, v92, v249\n"
"v_mfma_f32_16x16x32_bf16 v[144:147], a[144:147], v[140:143], v[144:147]\n"
"ds_write_b128 v3, v[108:111] offset:6144\n"
"v_mfma_f32_16x16x32_bf16 v[152:155], a[148:151], v[140:143], v[152:155]\n"
"buffer_load_dwordx4 v[108:111], v0, s[4:7], s98 offen offset:1024 sc1\n"
"v_mfma_f32_16x16x32_bf16 v[160:163], a[152:155], v[140:143], v[160:163]\n"
"ds_write_b128 v3, v[112:115] offset:7168\n"
"v_mfma_f32_16x16x32_bf16 v[164:167], a[156:159], v[140:143], v[164:167]\n"
"s_waitcnt vmcnt(15)\n"
"v_add_f32_e32 v93, v93, v248\n"
"v_mfma_f32_16x16x32_bf16 v[144:147], a[160:163], v[148:151], v[144:147]\n"
"ds_write_b128 v3, v[120:123] offset:8192\n"
"v_mfma_f32_16x16x32_bf16 v[152:155], a[164:167], v[148:151], v[152:155]\n"
"buffer_load_dwordx4 v[112:115], v0, s[4:7], s98 offen offset:2048 sc1\n"
"v_mfma_f32_16x16x32_bf16 v[160:163], a[168:171], v[148:151], v[160:163]\n"
"ds_write_b128 v3, v[124:127] offset:9216\n"
"v_mfma_f32_16x16x32_bf16 v[164:167], a[172:175], v[148:151], v[164:167]\n"
"buffer_load_dwordx4 v[116:119], v0, s[4:7], s98 offen offset:3072 sc1\n"
"v_mfma_f32_16x16x32_bf16 v[144:147], a[176:179], v[156:159], v[144:147]\n"
"v_add_f32_e32 v94, v94, v247\n"
"v_add_f32_e32 v95, v95, v246\n"
"v_mfma_f32_16x16x32_bf16 v[152:155], a[180:183], v[156:159], v[152:155]\n"
"buffer_load_dwordx4 v[120:123], v1, s[4:7], s98 offen sc1\n"
"v_mfma_f32_16x16x32_bf16 v[160:163], a[184:187], v[156:159], v[160:163]\n"
"v_add_f32_e32 v96, v96, v245\n"
"v_add_f32_e32 v97, v97, v244\n"
"v_mfma_f32_16x16x32_bf16 v[164:167], a[188:191], v[156:159], v[164:167]\n"
"s_waitcnt vmcnt(16)\n"
"v_add_f32_e32 v98, v98, v243\n"
"v_mfma_f32_16x16x32_bf16 v[144:147], a[192:195], v[168:171], v[144:147]\n"
"buffer_load_dwordx4 v[124:127], v1, s[4:7], s98 offen offset:1024 sc1\n"
"v_mfma_f32_16x16x32_bf16 v[152:155], a[196:199], v[168:171], v[152:155]\n"
"v_add_f32_e32 v99, v99, v242\n"
"v_add_f32_e32 v100, v100, v241\n"
"v_mfma_f32_16x16x32_bf16 v[160:163], a[200:203], v[168:171], v[160:163]\n"
"buffer_load_dwordx4 v[128:131], v1, s[4:7], s98 offen offset:2048 sc1\n"
"v_mfma_f32_16x16x32_bf16 v[164:167], a[204:207], v[168:171], v[164:167]\n"
"v_add_f32_e32 v101, v101, v240\n"
"v_add_f32_e32 v102, v102, v239\n"
"v_mfma_f32_16x16x32_bf16 v[144:147], a[208:211], v[172:175], v[144:147]\n"
"buffer_load_dwordx4 v[132:135], v1, s[4:7], s98 offen offset:3072 sc1\n"
"v_mfma_f32_16x16x32_bf16 v[152:155], a[212:215], v[172:175], v[152:155]\n"
"s_add_u32 s98, s53, 0x440000\n"
"v_add_f32_e32 v103, v103, v238\n"
"v_mfma_f32_16x16x32_bf16 v[160:163], a[216:219], v[172:175], v[160:163]\n"
"buffer_load_dwordx4 v[136:139], v0, s[4:7], s98 offen sc1\n"
"v_mfma_f32_16x16x32_bf16 v[164:167], a[220:223], v[172:175], v[164:167]\n"
"s_waitcnt vmcnt(18)\n"
"v_mfma_f32_16x16x32_bf16 v[144:147], a[224:227], v[176:179], v[144:147]\n"
"s_waitcnt vmcnt(16)\n"
"v_max_u32_e32 v253, v200, v201\n"
"v_mfma_f32_16x16x32_bf16 v[152:155], a[228:231], v[176:179], v[152:155]\n"
"v_max3_u32 v253, v253, v202, v203\n"
"v_max3_u32 v253, v253, v204, v205\n"
"v_mfma_f32_16x16x32_bf16 v[160:163], a[232:235], v[176:179], v[160:163]\n"
"v_max3_u32 v253, v253, v206, v207\n"
"s_waitcnt vmcnt(14)\n"
"v_mfma_f32_16x16x32_bf16 v[164:167], a[236:239], v[176:179], v[164:167]\n"
"v_max3_u32 v253, v253, v208, v209\n"
"v_max3_u32 v253, v253, v210, v211\n"
"v_mfma_f32_16x16x32_bf16 v[144:147], a[240:243], v[180:183], v[144:147]\n"
"v_max3_u32 v253, v253, v212, v213\n"
"v_max3_u32 v253, v253, v214, v215\n"
"v_mfma_f32_16x16x32_bf16 v[152:155], a[244:247], v[180:183], v[152:155]\n"
"v_cmp_eq_u32_e32 vcc, -1, v253\n"
"v_mfma_f32_16x16x32_bf16 v[160:163], a[248:251], v[180:183], v[160:163]\n"
"s_cbranch_vccnz .Lmk_q42s\n"
".Lmk_q42r:\n"
"v_mfma_f32_16x16x32_bf16 v[164:167], a[252:255], v[180:183], v[164:167]\n"
"; @A G2 a.3\n"
"v_mfma_f32_16x16x32_bf16 v[88:91], a[128:131], v[200:203], v[88:91]\n"
"v_cmp_u_f32_e32 vcc, v144, v144\n"
"v_mfma_f32_16x16x32_bf16 v[92:95], a[132:135], v[200:203], v[92:95]\n"
"s_cbranch_vccnz .Lmk_p41s\n"
".Lmk_p41r:\n"
"v_mfma_f32_16x16x32_bf16 v[96:99], a[136:139], v[200:203], v[96:99]\n"
"buffer_load_dwordx4 v[140:143], v0, s[4:7], s98 offen offset:1024 sc1\n"
"v_mfma_f32_16x16x32_bf16 v[100:103], a[140:143], v[200:203], v[100:103]\n"
"v_mfma_f32_16x16x32_bf16 v[88:91], a[144:147], v[204:207], v[88:91]\n"
"ds_write_b128 v3, v[144:147] offset:12288\n"
"v_mfma_f32_16x16x32_bf16 v[92:95], a[148:151], v[204:207], v[92:95]\n"
"s_waitcnt vmcnt(10)\n"
"v_cmp_ne_u64_e32 vcc, v[254:255], v[18:19]\n"
"v_mfma_f32_16x16x32_bf16 v[96:99], a[152:155], v[204:207], v[96:99]\n"
"ds_write_b128 v3, v[152:155] offset:13312\n"
"v_mfma_f32_16x16x32_bf16 v[100:103], a[156:159], v[204:207], v[100:103]\n"
"s_cbranch_vccnz .Lmk_f13s\n"
".Lmk_f13r:\n"
"v_mfma_f32_16x16x32_bf16 v[88:91], a[160:163], v[208:211], v[88:91]\n"
"v_max_u32_e32 v255, v216, v217\n"
"v_mfma_f32_16x16x32_bf16 v[92:95], a[164:167], v[208:211], v[92:95]\n"
"v_max3_u32 v255, v255, v218, v219\n"
"v_max3_u32 v255, v255, v220, v221\n"
"v_mfma_f32_16x16x32_bf16 v[96:99], a[168:171], v[208:211], v[96:99]\n"
"v_max3_u32 v255, v255, v222, v223\n"
"v_mfma_f32_16x16x32_bf16 v[100:103], a[172:175], v[208:211], v[100:103]\n"
"v_max3_u32 v255, v255, v224, v225\n"
"v_max3_u32 v255, v255, v226, v227\n"
"v_mfma_f32_16x16x32_bf16 v[88:91], a[176:179], v[212:215], v[88:91]\n"
"v_max3_u32 v255, v255, v228, v229\n"
"v_max3_u32 v255, v255, v230, v231\n"
"v_mfma_f32_16x16x32_bf16 v[92:95], a[180:183], v[212:215], v[92:95]\n"
"v_cmp_eq_u32_e32 vcc, -1, v255\n"
"v_mfma_f32_16x16x32_bf16 v[96:99], a[184:187], v[212:215], v[96:99]\n"
"s_cbranch_vccnz .Lmk_q43s\n"
".Lmk_q43r:\n"
"v_mfma_f32_16x16x32_bf16 v[100:103], a[188:191], v[212:215], v[100:103]\n"
"v_mfma_f32_16x16x32_bf16 v[88:91], a[192:195], v[216:219], v[88:91]\n"
"ds_write_b128 v3, v[160:163] offset:14336\n"
"v_mfma_f32_16x16x32_bf16 v[92:95], a[196:199], v[216:219], v[92:95]\n"
"buffer_load_dwordx4 v[144:147], v0, s[4:7], s98 offen offset:2048 sc1\n"
"v_mfma_f32_16x16x32_bf16 v[96:99], a[200:203], v[216:219], v[96:99]\n"
"ds_write_b128 v3, v[164:167] offset:15360\n"
"v_mfma_f32_16x16x32_bf16 v[100:103], a[204:207], v[216:219], v[100:103]\n"
"buffer_load_dwordx4 v[148:151], v0, s[4:7], s98 offen offset:3072 sc1\n"
"v_mfma_f32_16x16x32_bf16 v[88:91], a[208:211], v[220:223], v[88:91]\n"
"v_mfma_f32_16x16x32_bf16 v[92:95], a[212:215], v[220:223], v[92:95]\n"
"s_waitcnt lgkmcnt(0)\n"
"s_barrier\n"
"v_mfma_f32_16x16x32_bf16 v[96:99], a[216:219], v[220:223], v[96:99]\n"
"buffer_load_dwordx4 v[152:155], v1, s[4:7], s98 offen sc1\n"
"v_mfma_f32_16x16x32_bf16 v[100:103], a[220:223], v[220:223], v[100:103]\n"
"ds_read_b128 v[156:159], v5\n"
"v_mfma_f32_16x16x32_bf16 v[88:91], a[224:227], v[224:227], v[88:91]\n"
"buffer_load_dwordx4 v[160:163], v1, s[4:7], s98 offen offset:1024 sc1\n"
"v_mfma_f32_16x16x32_bf16 v[92:95], a[228:231], v[224:227], v[92:95]\n"
"ds_read_b128 v[184:187], v6\n"
"ds_read_b128 v[164:167], v5 offset:1024\n"
"v_mfma_f32_16x16x32_bf16 v[96:99], a[232:235], v[224:227], v[96:99]\n"
"buffer_load_dwordx4 v[168:171], v1, s[4:7], s98 offen offset:2048 sc1\n"
"v_mfma_f32_16x16x32_bf16 v[100:103], a[236:239], v[224:227], v[100:103]\n"
"ds_read_b128 v[188:191], v6 offset:1024\n"
"v_mfma_f32_16x16x32_bf16 v[88:91], a[240:243], v[228:231], v[88:91]\n"
"buffer_load_dwordx4 v[172:175], v1, s[4:7], s98 offen offset:3072 sc1\n"
"v_mfma_f32_16x16x32_bf16 v[92:95], a[244:247], v[228:231], v[92:95]\n"
"ds_read_b128 v[176:179], v5 offset:2048\n"
"ds_read_b128 v[192:195], v6 offset:2048\n"
"v_mfma_f32_16x16x32_bf16 v[96:99], a[248:251], v[228:231], v[96:99]\n"
"ds_read_b128 v[180:183], v5 offset:3072\n"
"ds_read_b128 v[196:199], v6 offset:3072\n"
"v_mfma_f32_16x16x32_bf16 v[100:103], a[252:255], v[228:231], v[100:103]\n"
"s_waitcnt lgkmcnt(4)\n"
"v_add_f32_e32 v156, v156, v184\n"
"v_add_f32_e32 v157, v157, v185\n"
"v_add_f32_e32 v158, v158, v186\n"
"v_add_f32_e32 v159, v159, v187\n"
"v_add_f32_e32 v164, v164, v188\n"
"v_add_f32_e32 v165, v165, v189\n"
"v_add_f32_e32 v166, v166, v190\n"
"v_add_f32_e32 v167, v167, v191\n"
"s_waitcnt lgkmcnt(0)\n"
"v_add_f32_e32 v176, v176, v192\n"
"v_add_f32_e32 v177, v177, v193\n"
"v_add_f32_e32 v178, v178, v194\n"
"v_add_f32_e32 v179, v179, v195\n"
"v_add_f32_e32 v180, v180, v196\n"
"v_add_f32_e32 v181, v181, v197\n"
"v_add_f32_e32 v182, v182, v198\n"
"v_add_f32_e32 v183, v183, v199\n"
".Lmk_loop:\n"
"s_waitcnt vmcnt(15)\n"
"; @B G1 0.0\n"
"v_mfma_f32_16x16x32_bf16 v[184:187], a[0:3], v[104:107], 0\n"
"s_add_u32 s99, s62, 0x44000\n"
"v_mfma_f32_16x16x32_bf16 v[188:191], a[4:7], v[104:107], 0\n"
"buffer_load_dwordx2 v[254:255], v10, s[4:7], s99 offen sc1\n"
"v_mfma_f32_16x16x32_bf16 v[192:195], a[8:11], v[104:107], 0\n"
"buffer_load_dwordx2 v[252:253], v10, s[4:7], s99 offen offset:128 sc1\n"
"v_mfma_f32_16x16x32_bf16 v[196:199], a[12:15], v[104:107], 0\n"
"buffer_load_dwordx2 v[250:251], v10, s[4:7], s99 offen offset:256 sc1\n"
"v_mfma_f32_16x16x32_bf16 v[200:203], a[16:19], v[104:107], 0\n"
"s_add_u32 s98, s54, 0x440000\n"
"v_mov_b32_e32 v204, 0x3f803f80\n"
"v_mfma_f32_16x16x32_bf16 v[208:211], a[20:23], v[104:107], 0\n"
"s_waitcnt vmcnt(17)\n"
"v_mov_b32_e32 v205, 0x3f803f80\n"
"v_mfma_f32_16x16x32_bf16 v[184:187], a[24:27], v[108:111], v[184:187]\n"
"buffer_load_dwordx4 v[104:107], v0, s[4:7], s98 offen sc1\n"
"v_mfma_f32_16x16x32_bf16 v[188:191], a[28:31], v[108:111], v[188:191]\n"
"buffer_load_dwordx2 v[248:249], v10, s[4:7], s99 offen offset:384 sc1\n"
"v_mfma_f32_16x16x32_bf16 v[192:195], a[32:35], v[108:111], v[192:195]\n"
"v_mov_b32_e32 v206, 0x3f803f80\n"
"v_mov_b32_e32 v207, 0x3f803f80\n"
"v_mfma_f32_16x16x32_bf16 v[196:199], a[36:39], v[108:111], v[196:199]\n"
"v_add_f32_e32 v156, v88, v156\n"
"v_add_f32_e32 v157, v89, v157\n"
"v_mfma_f32_16x16x32_bf16 v[200:203], a[40:43], v[108:111], v[200:203]\n"
"buffer_load_dwordx4 v[212:215], v0, s[4:7], s98 offen offset:1024 sc1\n"
"v_mfma_f32_16x16x32_bf16 v[208:211], a[44:47], v[108:111], v[208:211]\n"
"s_waitcnt vmcnt(19)\n"
"v_add_f32_e32 v158, v90, v158\n"
"v_mfma_f32_16x16x32_bf16 v[184:187], a[48:51], v[112:115], v[184:187]\n"
"v_add_f32_e32 v159, v91, v159\n"
"v_add_f32_e32 v164, v92, v164\n"
"v_mfma_f32_16x16x32_bf16 v[188:191], a[52:55], v[112:115], v[188:191]\n"
"v_add_f32_e32 v165, v93, v165\n"
"v_add_f32_e32 v166, v94, v166\n"
"v_mfma_f32_16x16x32_bf16 v[192:195], a[56:59], v[112:115], v[192:195]\n"
"v_add_f32_e32 v167, v95, v167\n"
"v_cvt_pk_bf16_f32 v88, v156, v157\n"
"v_mfma_f32_16x16x32_bf16 v[196:199], a[60:63], v[112:115], v[196:199]\n"
"v_cvt_pk_bf16_f32 v89, v158, v159\n"
"v_cvt_pk_bf16_f32 v90, v164, v165\n"
"v_mfma_f32_16x16x32_bf16 v[200:203], a[64:67], v[112:115], v[200:203]\n"
"buffer_load_dwordx4 v[92:95], v0, s[4:7], s98 offen offset:2048 sc1\n"
"v_mfma_f32_16x16x32_bf16 v[208:211], a[68:71], v[112:115], v[208:211]\n"
"s_waitcnt vmcnt(19)\n"
"v_cvt_pk_bf16_f32 v91, v166, v167\n"
"v_mfma_f32_16x16x32_bf16 v[184:187], a[72:75], v[116:119], v[184:187]\n"
"s_add_u32 s99, s61, 0x400000\n"
"v_mfma_f32_16x16x32_bf16 v[188:191], a[76:79], v[116:119], v[188:191]\n"
"buffer_store_dwordx4 v[88:91], v0, s[4:7], s99 offen sc1\n"
"v_mfma_f32_16x16x32_bf16 v[192:195], a[80:83], v[116:119], v[192:195]\n"
"ds_write_b128 v2, v[88:91]\n"
"v_mfma_f32_16x16x32_bf16 v[196:199], a[84:87], v[116:119], v[196:199]\n"
"v_add_f32_e32 v176, v96, v176\n"
"v_add_f32_e32 v177, v97, v177\n"
"v_mfma_f32_16x16x32_bf16 v[200:203], a[88:91], v[116:119], v[200:203]\n"
"buffer_load_dwordx4 v[108:111], v0, s[4:7], s98 offen offset:3072 sc1\n"
"v_mfma_f32_16x16x32_bf16 v[208:211], a[92:95], v[116:119], v[208:211]\n"
"s_waitcnt vmcnt(20)\n"
"v_add_f32_e32 v178, v98, v178\n"
"v_mfma_f32_16x16x32_bf16 v[184:187], a[96:99], v[120:123], v[184:187]\n"
"v_add_f32_e32 v179, v99, v179\n"
"v_add_f32_e32 v180, v100, v180\n"
"v_mfma_f32_16x16x32_bf16 v[188:191], a[100:103], v[120:123], v[188:191]\n"
"v_add_f32_e32 v181, v101, v181\n"
"v_add_f32_e32 v182, v102, v182\n"
"v_mfma_f32_16x16x32_bf16 v[192:195], a[104:107], v[120:123], v[192:195]\n"
"v_add_f32_e32 v183, v103, v183\n"
"v_cvt_pk_bf16_f32 v96, v176, v177\n"
"v_mfma_f32_16x16x32_bf16 v[196:199], a[108:111], v[120:123], v[196:199]\n"
"v_cvt_pk_bf16_f32 v97, v178, v179\n"
"v_cvt_pk_bf16_f32 v98, v180, v181\n"
"v_mfma_f32_16x16x32_bf16 v[200:203], a[112:115], v[120:123], v[200:203]\n"
"buffer_load_dwordx4 v[100:103], v1, s[4:7], s98 offen sc1\n"
"v_mfma_f32_16x16x32_bf16 v[208:211], a[116:119], v[120:123], v[208:211]\n"
"s_waitcnt vmcnt(20)\n"
"v_cvt_pk_bf16_f32 v99, v182, v183\n"
"v_mfma_f32_16x16x32_bf16 v[184:187], a[120:123], v[124:127], v[184:187]\n"
"v_mov_b32_e32 v112, -1\n"
"v_mov_b32_e32 v113, -1\n"
"v_mfma_f32_16x16x32_bf16 v[188:191], a[124:127], v[124:127], v[188:191]\n"
"v_mov_b32_e32 v114, -1\n"
"v_mov_b32_e32 v115, -1\n"
"v_mfma_f32_16x16x32_bf16 v[192:195], v[24:27], v[124:127], v[192:195]\n"
"s_add_u32 s99, s60, 0x448000\n"
"s_add_u32 s97, s61, 0x400000\n"
"v_mfma_f32_16x16x32_bf16 v[196:199], v[28:31], v[124:127], v[196:199]\n"
"buffer_store_dwordx4 v[112:115], v0, s[4:7], s99 offen sc1\n"
"v_mfma_f32_16x16x32_bf16 v[200:203], v[32:35], v[124:127], v[200:203]\n"
"buffer_load_dwordx4 v[116:119], v1, s[4:7], s98 offen offset:1024 sc1\n"
"v_mfma_f32_16x16x32_bf16 v[208:211], v[36:39], v[124:127], v[208:211]\n"
"s_waitcnt vmcnt(21)\n"
"v_mfma_f32_16x16x32_bf16 v[184:187], v[40:43], v[128:131], v[184:187]\n"
"buffer_store_dwordx4 v[96:99], v0, s[4:7], s97 offen offset:1024 sc1\n"
"v_mfma_f32_16x16x32_bf16 v[188:191], v[44:47], v[128:131], v[188:191]\n"
"ds_write_b128 v2, v[96:99] offset:1024\n"
"v_mfma_f32_16x16x32_bf16 v[120:123], v[204:207], v[88:91], 0\n"
"v_mfma_f32_16x16x32_bf16 v[124:127], v[88:91], v[88:91], 0\n"
"v_mfma_f32_16x16x32_bf16 v[120:123], v[204:207], v[96:99], v[120:123]\n"
"v_mfma_f32_16x16x32_bf16 v[124:127], v[96:99], v[96:99], v[124:127]\n"
"v_mfma_f32_16x16x32_bf16 v[192:195], v[48:51], v[128:131], v[192:195]\n"
"s_mov_b32 s96, 0xaaaaaaaa\n"
"s_mov_b32 s97, 0xaaaaaaaa\n"
"v_mfma_f32_16x16x32_bf16 v[196:199], v[52:55], v[128:131], v[196:199]\n"
"v_mfma_f32_16x16x32_bf16 v[200:203], v[56:59], v[128:131], v[200:203]\n"
"buffer_load_dwordx4 v[88:91], v1, s[4:7], s98 offen offset:2048 sc1\n"
"v_mfma_f32_16x16x32_bf16 v[208:211], v[60:63], v[128:131], v[208:211]\n"
"s_waitcnt vmcnt(22)\n"
"v_cndmask_b32_e64 v124, v124, v125, s[96:97]\n"
"v_mfma_f32_16x16x32_bf16 v[184:187], v[64:67], v[132:135], v[184:187]\n"
"v_cndmask_b32_e64 v126, v126, v127, s[96:97]\n"
"v_mfma_f32_16x16x32_bf16 v[188:191], v[68:71], v[132:135], v[188:191]\n"
"s_mov_b32 s96, 0xcccccccc\n"
"s_mov_b32 s97, 0xcccccccc\n"
"v_mfma_f32_16x16x32_bf16 v[192:195], v[72:75], v[132:135], v[192:195]\n"
"s_add_u32 s99, s70, 0x200000\n"
"v_cndmask_b32_e64 v121, v124, v126, s[96:97]\n"
"v_mfma_f32_16x16x32_bf16 v[196:199], v[76:79], v[132:135], v[196:199]\n"
"buffer_load_dwordx4 a[128:131], v0, s[4:7], s99 offen sc1\n"
"v_mfma_f32_16x16x32_bf16 v[200:203], v[80:83], v[132:135], v[200:203]\n"
"buffer_load_dwordx4 v[96:99], v1, s[4:7], s98 offen offset:3072 sc1\n"
"v_mfma_f32_16x16x32_bf16 v[208:211], v[84:87], v[132:135], v[208:211]\n"
"v_cmp_u_f32_e32 vcc, v184, v184\n"
"s_waitcnt vmcnt(22)\n"
"; @B G1 0.1\n"
"v_mfma_f32_16x16x32_bf16 v[124:127], a[0:3], v[136:139], 0\n"
"s_cbranch_vccnz .Lmk_p47s\n"
".Lmk_p47r:\n"
"s_add_u32 s98, s63, 0x40000\n"
"v_mfma_f32_16x16x32_bf16 v[128:131], a[4:7], v[136:139], 0\n"
"buffer_store_dwordx2 v[120:121], v14, s[4:7], s98 offen sc1\n"
"v_mfma_f32_16x16x32_bf16 v[120:123], a[8:11], v[136:139], 0\n"
"v_mfma_f32_16x16x32_bf16 v[132:135], a[12:15], v[136:139], 0\n"
"ds_write_b128 v3, v[184:187]\n"
"v_mfma_f32_16x16x32_bf16 v[156:159], a[16:19], v[136:139], 0\n"
"s_add_u32 s98, s55, 0x440000\n"
"v_mfma_f32_16x16x32_bf16 v[164:167], a[20:23], v[136:139], 0\n"
"buffer_load_dwordx4 v[136:139], v0, s[4:7], s98 offen sc1\n"
"v_mfma_f32_16x16x32_bf16 v[124:127], a[24:27], v[140:143], v[124:127]\n"
"ds_write_b128 v3, v[188:191] offset:1024\n"
"v_mfma_f32_16x16x32_bf16 v[128:131], a[28:31], v[140:143], v[128:131]\n"
"v_mfma_f32_16x16x32_bf16 v[120:123], a[32:35], v[140:143], v[120:123]\n"
"v_mfma_f32_16x16x32_bf16 v[132:135], a[36:39], v[140:143], v[132:135]\n"
"ds_write_b128 v3, v[192:195] offset:2048\n"
"v_mfma_f32_16x16x32_bf16 v[156:159], a[40:43], v[140:143], v[156:159]\n"
"buffer_load_dwordx4 v[176:179], v0, s[4:7], s98 offen offset:1024 sc1\n"
"v_mfma_f32_16x16x32_bf16 v[164:167], a[44:47], v[140:143], v[164:167]\n"
"s_waitcnt vmcnt(23)\n"
"v_mfma_f32_16x16x32_bf16 v[124:127], a[48:51], v[144:147], v[124:127]\n"
"ds_write_b128 v3, v[196:199] offset:3072\n"
"v_mfma_f32_16x16x32_bf16 v[128:131], a[52:55], v[144:147], v[128:131]\n"
"v_mfma_f32_16x16x32_bf16 v[120:123], a[56:59], v[144:147], v[120:123]\n"
"v_mfma_f32_16x16x32_bf16 v[132:135], a[60:63], v[144:147], v[132:135]\n"
"ds_write_b128 v3, v[200:203] offset:4096\n"
"v_mfma_f32_16x16x32_bf16 v[156:159], a[64:67], v[144:147], v[156:159]\n"
"buffer_load_dwordx4 v[140:143], v0, s[4:7], s98 offen offset:2048 sc1\n"
"v_mfma_f32_16x16x32_bf16 v[164:167], a[68:71], v[144:147], v[164:167]\n"
"v_mfma_f32_16x16x32_bf16 v[124:127], a[72:75], v[148:151], v[124:127]\n"
"ds_write_b128 v3, v[208:211] offset:5120\n"
"v_mfma_f32_16x16x32_bf16 v[128:131], a[76:79], v[148:151], v[128:131]\n"
"s_waitcnt vmcnt(15)\n"
"v_add_f32_e32 v254, v254, v252\n"
"v_mfma_f32_16x16x32_bf16 v[120:123], a[80:83], v[148:151], v[120:123]\n"
"v_add_f32_e32 v255, v255, v253\n"
"v_add_f32_e32 v250, v250, v248\n"
"v_mfma_f32_16x16x32_bf16 v[132:135], a[84:87], v[148:151], v[132:135]\n"
"buffer_load_dwordx4 a[132:135], v0, s[4:7], s99 offen offset:1024 sc1\n"
"v_mfma_f32_16x16x32_bf16 v[156:159], a[88:91], v[148:151], v[156:159]\n"
"buffer_load_dwordx4 v[144:147], v0, s[4:7], s98 offen offset:3072 sc1\n"
"v_mfma_f32_16x16x32_bf16 v[164:167], a[92:95], v[148:151], v[164:167]\n"
"v_add_f32_e32 v251, v251, v249\n"
"v_mfma_f32_16x16x32_bf16 v[124:127], a[96:99], v[152:155], v[124:127]\n"
"v_add_f32_e32 v254, v254, v250\n"
"v_add_f32_e32 v255, v255, v251\n"
"v_mfma_f32_16x16x32_bf16 v[128:131], a[100:103], v[152:155], v[128:131]\n"
"v_cmp_u_f32_e32 vcc, v254, v255\n"
"v_mfma_f32_16x16x32_bf16 v[120:123], a[104:107], v[152:155], v[120:123]\n"
"s_cbranch_vccnz .Lmk_m52s\n"
".Lmk_m52r:\n"
"v_mfma_f32_16x16x32_bf16 v[132:135], a[108:111], v[152:155], v[132:135]\n"
"v_permlane16_swap_b32_e32 v254, v255\n"
"v_add_f32_e32 v254, v254, v255\n"
"v_mfma_f32_16x16x32_bf16 v[156:159], a[112:115], v[152:155], v[156:159]\n"
"buffer_load_dwordx4 v[148:151], v1, s[4:7], s98 offen sc1\n"
"v_mfma_f32_16x16x32_bf16 v[164:167], a[116:119], v[152:155], v[164:167]\n"
"s_waitcnt lgkmcnt(0)\n"
"s_barrier\n"
"v_mfma_f32_16x16x32_bf16 v[124:127], a[120:123], v[160:163], v[124:127]\n"
"v_mov_b32_e32 v255, v254\n"
"v_mfma_f32_16x16x32_bf16 v[128:131], a[124:127], v[160:163], v[128:131]\n"
"s_nop 0\n"
"v_permlane32_swap_b32_e32 v254, v255\n"
"v_add_f32_e32 v254, v254, v255\n"
"v_mfma_f32_16x16x32_bf16 v[120:123], v[24:27], v[160:163], v[120:123]\n"
"v_mul_f32_e32 v254, 0x3a800000, v254\n"
"v_mov_b32_e32 v255, v254\n"
"v_mfma_f32_16x16x32_bf16 v[132:135], v[28:31], v[160:163], v[132:135]\n"
"s_nop 0\n"
"v_permlane16_swap_b32_e32 v254, v255\n"
"v_cvt_pk_bf16_f32 v253, -v254, 0\n"
"v_mfma_f32_16x16x32_bf16 v[156:159], v[32:35], v[160:163], v[156:159]\n"
"buffer_load_dwordx4 v[152:155], v1, s[4:7], s98 offen offset:1024 sc1\n"
"v_mfma_f32_16x16x32_bf16 v[164:167], v[36:39], v[160:163], v[164:167]\n"
"v_fma_f32 v255, -v254, v254, v255\n"
"v_mfma_f32_16x16x32_bf16 v[124:127], v[40:43], v[168:171], v[124:127]\n"
"v_lshlrev_b32_e32 v253, 16, v253\n"
"ds_read_b128 v[160:163], v4\n"
"v_mfma_f32_16x16x32_bf16 v[128:131], v[44:47], v[168:171], v[128:131]\n"
"ds_read_b128 v[180:183], v9 offset:512\n"
"ds_read_b128 v[184:187], v4 offset:1024\n"
"v_mfma_f32_16x16x32_bf16 v[120:123], v[48:51], v[168:171], v[120:123]\n"
"ds_read_b128 v[188:191], v9 offset:576\n"
"ds_read_b128 v[192:195], v4 offset:2048\n"
"v_mfma_f32_16x16x32_bf16 v[132:135], v[52:55], v[168:171], v[132:135]\n"
"ds_read_b128 v[196:199], v9 offset:640\n"
"v_max_f32_e32 v255, 0, v255\n"
"v_mfma_f32_16x16x32_bf16 v[156:159], v[56:59], v[168:171], v[156:159]\n"
"buffer_load_dwordx4 v[200:203], v1, s[4:7], s98 offen offset:2048 sc1\n"
"v_mfma_f32_16x16x32_bf16 v[164:167], v[60:63], v[168:171], v[164:167]\n"
"v_cvt_pk_bf16_f32 v255, v255, 0\n"
"v_lshlrev_b32_e32 v255, 16, v255\n"
"v_mfma_f32_16x16x32_bf16 v[124:127], v[64:67], v[172:175], v[124:127]\n"
"v_add_f32_e32 v255, 0x3727c5ac, v255\n"
"v_cvt_pk_bf16_f32 v255, v255, 0\n"
"v_mfma_f32_16x16x32_bf16 v[128:131], v[68:71], v[172:175], v[128:131]\n"
"v_lshlrev_b32_e32 v255, 16, v255\n"
"v_mfma_f32_16x16x32_bf16 v[120:123], v[72:75], v[172:175], v[120:123]\n"
"s_waitcnt lgkmcnt(0)\n"
"v_fmac_f32_e32 v160, v253, v180\n"
"v_mfma_f32_16x16x32_bf16 v[132:135], v[76:79], v[172:175], v[132:135]\n"
"buffer_load_dwordx4 a[136:139], v0, s[4:7], s99 offen offset:2048 sc1\n"
"v_mfma_f32_16x16x32_bf16 v[156:159], v[80:83], v[172:175], v[156:159]\n"
"buffer_load_dwordx4 v[168:171], v1, s[4:7], s98 offen offset:3072 sc1\n"
"v_mfma_f32_16x16x32_bf16 v[164:167], v[84:87], v[172:175], v[164:167]\n"
"v_cmp_u_f32_e32 vcc, v124, v124\n"
"s_waitcnt vmcnt(21)\n"
"; @B G1 0.2\n"
"v_mfma_f32_16x16x32_bf16 v[172:175], a[0:3], v[104:107], 0\n"
"s_cbranch_vccnz .Lmk_p48s\n"
".Lmk_p48r:\n"
"v_fmac_f32_e32 v161, v253, v181\n"
"v_mfma_f32_16x16x32_bf16 v[204:207], a[4:7], v[104:107], 0\n"
"v_fmac_f32_e32 v162, v253, v182\n"
"v_fmac_f32_e32 v163, v253, v183\n"
"v_mfma_f32_16x16x32_bf16 v[180:183], a[8:11], v[104:107], 0\n"
"v_fmac_f32_e32 v184, v253, v188\n"
"v_fmac_f32_e32 v185, v253, v189\n"
"v_mfma_f32_16x16x32_bf16 v[208:211], a[12:15], v[104:107], 0\n"
"ds_write_b128 v3, v[124:127] offset:6144\n"
"v_mfma_f32_16x16x32_bf16 v[124:127], a[16:19], v[104:107], 0\n"
"s_add_u32 s98, s52, 0x448000\n"
"v_fmac_f32_e32 v186, v253, v190\n"
"v_mfma_f32_16x16x32_bf16 v[216:219], a[20:23], v[104:107], 0\n"
"buffer_load_dwordx4 v[104:107], v0, s[4:7], s98 offen sc1\n"
"v_mfma_f32_16x16x32_bf16 v[172:175], a[24:27], v[212:215], v[172:175]\n"
"ds_write_b128 v3, v[128:131] offset:7168\n"
"v_mfma_f32_16x16x32_bf16 v[204:207], a[28:31], v[212:215], v[204:207]\n"
"v_fmac_f32_e32 v187, v253, v191\n"
"v_fmac_f32_e32 v192, v253, v196\n"
"v_mfma_f32_16x16x32_bf16 v[180:183], a[32:35], v[212:215], v[180:183]\n"
"v_fmac_f32_e32 v193, v253, v197\n"
"v_fmac_f32_e32 v194, v253, v198\n"
"v_mfma_f32_16x16x32_bf16 v[208:211], a[36:39], v[212:215], v[208:211]\n"
"ds_write_b128 v3, v[120:123] offset:8192\n"
"v_mfma_f32_16x16x32_bf16 v[124:127], a[40:43], v[212:215], v[124:127]\n"
"buffer_load_dwordx4 v[120:123], v0, s[4:7], s98 offen offset:1024 sc1\n"
"v_mfma_f32_16x16x32_bf16 v[216:219], a[44:47], v[212:215], v[216:219]\n"
"s_waitcnt vmcnt(20)\n"
"s_add_u32 s97, s60, 0x440000\n"
"v_mfma_f32_16x16x32_bf16 v[172:175], a[48:51], v[92:95], v[172:175]\n"
"ds_write_b128 v3, v[132:135] offset:9216\n"
"v_mfma_f32_16x16x32_bf16 v[204:207], a[52:55], v[92:95], v[204:207]\n"
"buffer_store_dwordx4 v[112:115], v0, s[4:7], s97 offen sc1\n"
"v_mfma_f32_16x16x32_bf16 v[180:183], a[56:59], v[92:95], v[180:183]\n"
"v_fmac_f32_e32 v195, v253, v199\n"
"ds_read_b128 v[112:115], v4 offset:3072\n"
"v_mfma_f32_16x16x32_bf16 v[208:211], a[60:63], v[92:95], v[208:211]\n"
"ds_write_b128 v3, v[156:159] offset:10240\n"
"v_mfma_f32_16x16x32_bf16 v[124:127], a[64:67], v[92:95], v[124:127]\n"
"buffer_load_dwordx4 v[128:131], v0, s[4:7], s98 offen offset:2048 sc1\n"
"v_mfma_f32_16x16x32_bf16 v[216:219], a[68:71], v[92:95], v[216:219]\n"
"ds_read_b128 v[92:95], v9 offset:704\n"
"ds_read_b128 v[132:135], v4 offset:4096\n"
"v_mfma_f32_16x16x32_bf16 v[172:175], a[72:75], v[108:111], v[172:175]\n"
"ds_write_b128 v3, v[164:167] offset:11264\n"
"v_mfma_f32_16x16x32_bf16 v[204:207], a[76:79], v[108:111], v[204:207]\n"
"ds_read_b128 v[156:159], v9 offset:768\n"
"ds_read_b128 v[164:167], v4 offset:5120\n"
"v_mfma_f32_16x16x32_bf16 v[180:183], a[80:83], v[108:111], v[180:183]\n"
"ds_read_b128 v[188:191], v9 offset:832\n"
"v_mfma_f32_16x16x32_bf16 v[208:211], a[84:87], v[108:111], v[208:211]\n"
"buffer_load_dwordx4 a[140:143], v0, s[4:7], s99 offen offset:3072 sc1\n"
"v_mfma_f32_16x16x32_bf16 v[124:127], a[88:91], v[108:111], v[124:127]\n"
"buffer_load_dwordx4 v[196:199], v0, s[4:7], s98 offen offset:3072 sc1\n"
"v_mfma_f32_16x16x32_bf16 v[216:219], a[92:95], v[108:111], v[216:219]\n"
"s_waitcnt vmcnt(21)\n"
"v_mfma_f32_16x16x32_bf16 v[172:175], a[96:99], v[100:103], v[172:175]\n"
"v_sqrt_f32_e32 v255, v255\n"
"v_mfma_f32_16x16x32_bf16 v[204:207], a[100:103], v[100:103], v[204:207]\n"
"s_waitcnt lgkmcnt(0)\n"
"v_fmac_f32_e32 v112, v253, v92\n"
"v_mfma_f32_16x16x32_bf16 v[180:183], a[104:107], v[100:103], v[180:183]\n"
"v_fmac_f32_e32 v113, v253, v93\n"
"v_fmac_f32_e32 v114, v253, v94\n"
"v_mfma_f32_16x16x32_bf16 v[208:211], a[108:111], v[100:103], v[208:211]\n"
"v_fmac_f32_e32 v115, v253, v95\n"
"v_fmac_f32_e32 v132, v253, v156\n"
"v_mfma_f32_16x16x32_bf16 v[124:127], a[112:115], v[100:103], v[124:127]\n"
"buffer_load_dwordx4 v[92:95], v1, s[4:7], s98 offen sc1\n"
"v_mfma_f32_16x16x32_bf16 v[216:219], a[116:119], v[100:103], v[216:219]\n"
"v_fmac_f32_e32 v133, v253, v157\n"
"v_fmac_f32_e32 v134, v253, v158\n"
"v_mfma_f32_16x16x32_bf16 v[172:175], a[120:123], v[116:119], v[172:175]\n"
"v_fmac_f32_e32 v135, v253, v159\n"
"v_fmac_f32_e32 v164, v253, v188\n"
"v_mfma_f32_16x16x32_bf16 v[204:207], a[124:127], v[116:119], v[204:207]\n"
"v_fmac_f32_e32 v165, v253, v189\n"
"v_fmac_f32_e32 v166, v253, v190\n"
"v_mfma_f32_16x16x32_bf16 v[180:183], v[24:27], v[116:119], v[180:183]\n"
"v_fmac_f32_e32 v167, v253, v191\n"
"v_cvt_pk_bf16_f32 v255, v255, 0\n"
"v_mfma_f32_16x16x32_bf16 v[208:211], v[28:31], v[116:119], v[208:211]\n"
"v_lshlrev_b32_e32 v255, 16, v255\n"
"v_mfma_f32_16x16x32_bf16 v[124:127], v[32:35], v[116:119], v[124:127]\n"
"buffer_load_dwordx4 v[100:103], v1, s[4:7], s98 offen offset:1024 sc1\n"
"v_mfma_f32_16x16x32_bf16 v[216:219], v[36:39], v[116:119], v[216:219]\n"
"s_waitcnt vmcnt(19)\n"
"v_mfma_f32_16x16x32_bf16 v[172:175], v[40:43], v[88:91], v[172:175]\n"
"v_rcp_f32_e32 v253, v255\n"
"v_mfma_f32_16x16x32_bf16 v[204:207], v[44:47], v[88:91], v[204:207]\n"
"v_mul_f32_e32 v255, 0x3e8293ee, v253\n"
"v_mfma_f32_16x16x32_bf16 v[180:183], v[48:51], v[88:91], v[180:183]\n"
"v_mfma_f32_16x16x32_bf16 v[208:211], v[52:55], v[88:91], v[208:211]\n"
"v_mfma_f32_16x16x32_bf16 v[124:127], v[56:59], v[88:91], v[124:127]\n"
"s_waitcnt vmcnt(16)\n"
"v_max_u32_e32 v254, v136, v137\n"
"v_mfma_f32_16x16x32_bf16 v[216:219], v[60:63], v[88:91], v[216:219]\n"
"v_max3_u32 v254, v254, v138, v139\n"
"v_max3_u32 v254, v254, v176, v177\n"
"v_mfma_f32_16x16x32_bf16 v[172:175], v[64:67], v[96:99], v[172:175]\n"
"v_max3_u32 v254, v254, v178, v179\n"
"s_waitcnt vmcnt(13)\n"
"v_mfma_f32_16x16x32_bf16 v[204:207], v[68:71], v[96:99], v[204:207]\n"
"v_max3_u32 v254, v254, v140, v141\n"
"v_max3_u32 v254, v254, v142, v143\n"
"v_mfma_f32_16x16x32_bf16 v[180:183], v[72:75], v[96:99], v[180:183]\n"
"v_max3_u32 v254, v254, v144, v145\n"
"v_max3_u32 v254, v254, v146, v147\n"
"v_mfma_f32_16x16x32_bf16 v[208:211], v[76:79], v[96:99], v[208:211]\n"
"v_cmp_eq_u32_e32 vcc, -1, v254\n"
"s_add_u32 s97, s68, s80\n"
"v_mfma_f32_16x16x32_bf16 v[124:127], v[80:83], v[96:99], v[124:127]\n"
"s_cbranch_vccnz .Lmk_q50s\n"
".Lmk_q50r:\n"
"s_add_u32 s97, s97, 0x10000\n"
"v_mfma_f32_16x16x32_bf16 v[216:219], v[84:87], v[96:99], v[216:219]\n"
"v_cmp_u_f32_e32 vcc, v172, v172\n"
"; @B G1 0.3\n"
"v_mfma_f32_16x16x32_bf16 v[160:163], a[0:3], v[136:139], v[160:163]\n"
"s_cbranch_vccnz .Lmk_p49s\n"
".Lmk_p49r:\n"
"v_mfma_f32_16x16x32_bf16 v[184:187], a[4:7], v[136:139], v[184:187]\n"
"buffer_load_dwordx4 v[88:91], v1, s[4:7], s98 offen offset:2048 sc1\n"
"v_mfma_f32_16x16x32_bf16 v[192:195], a[8:11], v[136:139], v[192:195]\n"
"buffer_load_dwordx4 v[96:99], v1, s[4:7], s98 offen offset:3072 sc1\n"
"v_mfma_f32_16x16x32_bf16 v[112:115], a[12:15], v[136:139], v[112:115]\n"
"ds_write_b128 v3, v[172:175] offset:12288\n"
"v_mfma_f32_16x16x32_bf16 v[132:135], a[16:19], v[136:139], v[132:135]\n"
"s_add_u32 s98, s53, 0x448000\n"
"v_mfma_f32_16x16x32_bf16 v[164:167], a[20:23], v[136:139], v[164:167]\n"
"buffer_load_dwordx4 v[108:111], v0, s[4:7], s98 offen sc1\n"
"v_mfma_f32_16x16x32_bf16 v[160:163], a[24:27], v[176:179], v[160:163]\n"
"ds_write_b128 v3, v[204:207] offset:13312\n"
"v_mfma_f32_16x16x32_bf16 v[184:187], a[28:31], v[176:179], v[184:187]\n"
"buffer_store_dwordx2 v[18:19], v11, s[4:7], s97 offen sc1\n"
"v_mfma_f32_16x16x32_bf16 v[192:195], a[32:35], v[176:179], v[192:195]\n"
"buffer_load_dwordx4 a[144:147], v1, s[4:7], s99 offen sc1\n"
"v_mfma_f32_16x16x32_bf16 v[112:115], a[36:39], v[176:179], v[112:115]\n"
"ds_write_b128 v3, v[180:183] offset:14336\n"
"v_mfma_f32_16x16x32_bf16 v[132:135], a[40:43], v[176:179], v[132:135]\n"
"buffer_load_dwordx4 v[116:119], v0, s[4:7], s98 offen offset:1024 sc1\n"
"v_mfma_f32_16x16x32_bf16 v[164:167], a[44:47], v[176:179], v[164:167]\n"
"v_mfma_f32_16x16x32_bf16 v[160:163], a[48:51], v[140:143], v[160:163]\n"
"ds_write_b128 v3, v[208:211] offset:15360\n"
"v_mfma_f32_16x16x32_bf16 v[184:187], a[52:55], v[140:143], v[184:187]\n"
"v_mfma_f32_16x16x32_bf16 v[192:195], a[56:59], v[140:143], v[192:195]\n"
"v_mfma_f32_16x16x32_bf16 v[112:115], a[60:63], v[140:143], v[112:115]\n"
"ds_write_b128 v3, v[124:127] offset:16384\n"
"v_mfma_f32_16x16x32_bf16 v[132:135], a[64:67], v[140:143], v[132:135]\n"
"s_waitcnt vmcnt(17)\n"
"v_max_u32_e32 v254, v148, v149\n"
"v_mfma_f32_16x16x32_bf16 v[164:167], a[68:71], v[140:143], v[164:167]\n"
"v_max3_u32 v254, v254, v150, v151\n"
"v_max3_u32 v254, v254, v152, v153\n"
"v_mfma_f32_16x16x32_bf16 v[160:163], a[72:75], v[144:147], v[160:163]\n"
"v_max3_u32 v254, v254, v154, v155\n"
"s_waitcnt vmcnt(14)\n"
"v_mfma_f32_16x16x32_bf16 v[184:187], a[76:79], v[144:147], v[184:187]\n"
"v_max3_u32 v254, v254, v200, v201\n"
"v_max3_u32 v254, v254, v202, v203\n"
"v_mfma_f32_16x16x32_bf16 v[192:195], a[80:83], v[144:147], v[192:195]\n"
"v_max3_u32 v254, v254, v168, v169\n"
"v_max3_u32 v254, v254, v170, v171\n"
"v_mfma_f32_16x16x32_bf16 v[112:115], a[84:87], v[144:147], v[112:115]\n"
"v_cmp_eq_u32_e32 vcc, -1, v254\n"
"v_mfma_f32_16x16x32_bf16 v[132:135], a[88:91], v[144:147], v[132:135]\n"
"s_cbranch_vccnz .Lmk_q51s\n"
".Lmk_q51r:\n"
"v_mfma_f32_16x16x32_bf16 v[164:167], a[92:95], v[144:147], v[164:167]\n"
"v_mfma_f32_16x16x32_bf16 v[160:163], a[96:99], v[148:151], v[160:163]\n"
"buffer_load_dwordx4 v[124:127], v0, s[4:7], s98 offen offset:2048 sc1\n"
"v_mfma_f32_16x16x32_bf16 v[184:187], a[100:103], v[148:151], v[184:187]\n"
"buffer_load_dwordx4 v[136:139], v0, s[4:7], s98 offen offset:3072 sc1\n"
"v_mfma_f32_16x16x32_bf16 v[192:195], a[104:107], v[148:151], v[192:195]\n"
"ds_write_b128 v3, v[216:219] offset:17408\n"
"v_mfma_f32_16x16x32_bf16 v[112:115], a[108:111], v[148:151], v[112:115]\n"
"v_mfma_f32_16x16x32_bf16 v[132:135], a[112:115], v[148:151], v[132:135]\n"
"buffer_load_dwordx4 v[140:143], v1, s[4:7], s98 offen sc1\n"
"v_mfma_f32_16x16x32_bf16 v[164:167], a[116:119], v[148:151], v[164:167]\n"
"s_waitcnt lgkmcnt(0)\n"
"s_barrier\n"
"v_mfma_f32_16x16x32_bf16 v[160:163], a[120:123], v[152:155], v[160:163]\n"
"ds_read_b128 v[144:147], v5 offset:2048\n"
"ds_read_b128 v[148:151], v6 offset:2048\n"
"v_mfma_f32_16x16x32_bf16 v[184:187], a[124:127], v[152:155], v[184:187]\n"
"ds_read_b128 v[156:159], v5 offset:3072\n"
"ds_read_b128 v[172:175], v6 offset:3072\n"
"v_mfma_f32_16x16x32_bf16 v[192:195], v[24:27], v[152:155], v[192:195]\n"
"buffer_load_dwordx4 a[148:151], v1, s[4:7], s99 offen offset:1024 sc1\n"
"v_mfma_f32_16x16x32_bf16 v[112:115], v[28:31], v[152:155], v[112:115]\n"
"v_mfma_f32_16x16x32_bf16 v[132:135], v[32:35], v[152:155], v[132:135]\n"
"buffer_load_dwordx4 v[176:179], v1, s[4:7], s98 offen offset:1024 sc1\n"
"v_mfma_f32_16x16x32_bf16 v[164:167], v[36:39], v[152:155], v[164:167]\n"
"ds_read_b128 v[152:155], v5 offset:4096\n"
"v_mfma_f32_16x16x32_bf16 v[160:163], v[40:43], v[200:203], v[160:163]\n"
"ds_read_b128 v[180:183], v6 offset:4096\n"
"ds_read_b128 v[188:191], v5 offset:5120\n"
"v_mfma_f32_16x16x32_bf16 v[184:187], v[44:47], v[200:203], v[184:187]\n"
"ds_read_b128 v[204:207], v6 offset:5120\n"
"s_waitcnt lgkmcnt(4)\n"
"v_mfma_f32_16x16x32_bf16 v[192:195], v[48:51], v[200:203], v[192:195]\n"
"v_add_f32_e32 v144, v144, v148\n"
"v_add_f32_e32 v145, v145, v149\n"
"v_mfma_f32_16x16x32_bf16 v[112:115], v[52:55], v[200:203], v[112:115]\n"
"v_add_f32_e32 v146, v146, v150\n"
"v_add_f32_e32 v147, v147, v151\n"
"v_mfma_f32_16x16x32_bf16 v[132:135], v[56:59], v[200:203], v[132:135]\n"
"buffer_load_dwordx4 v[148:151], v1, s[4:7], s98 offen offset:2048 sc1\n"
"v_mfma_f32_16x16x32_bf16 v[164:167], v[60:63], v[200:203], v[164:167]\n"
"ds_read_b128 v[200:203], v5\n"
"ds_read_b128 v[208:211], v6\n"
"v_mfma_f32_16x16x32_bf16 v[160:163], v[64:67], v[168:171], v[160:163]\n"
"ds_read_b128 v[212:215], v5 offset:1024\n"
"ds_read_b128 v[216:219], v6 offset:1024\n"
"v_mfma_f32_16x16x32_bf16 v[184:187], v[68:71], v[168:171], v[184:187]\n"
"v_add_f32_e32 v156, v156, v172\n"
"v_add_f32_e32 v157, v157, v173\n"
"v_mfma_f32_16x16x32_bf16 v[192:195], v[72:75], v[168:171], v[192:195]\n"
"v_add_f32_e32 v158, v158, v174\n"
"v_add_f32_e32 v159, v159, v175\n"
"v_mfma_f32_16x16x32_bf16 v[112:115], v[76:79], v[168:171], v[112:115]\n"
"s_waitcnt lgkmcnt(4)\n"
"v_add_f32_e32 v152, v152, v180\n"
"v_mfma_f32_16x16x32_bf16 v[132:135], v[80:83], v[168:171], v[132:135]\n"
"buffer_load_dwordx4 v[172:175], v1, s[4:7], s98 offen offset:3072 sc1\n"
"v_mfma_f32_16x16x32_bf16 v[164:167], v[84:87], v[168:171], v[164:167]\n"
"v_add_f32_e32 v153, v153, v181\n"
"v_add_f32_e32 v154, v154, v182\n"
"v_add_f32_e32 v155, v155, v183\n"
"v_add_f32_e32 v188, v188, v204\n"
"v_add_f32_e32 v189, v189, v205\n"
"v_add_f32_e32 v190, v190, v206\n"
"v_add_f32_e32 v191, v191, v207\n"
"s_waitcnt lgkmcnt(0)\n"
"v_add_f32_e32 v200, v200, v208\n"
"v_add_f32_e32 v201, v201, v209\n"
"v_add_f32_e32 v202, v202, v210\n"
"v_add_f32_e32 v203, v203, v211\n"
"v_add_f32_e32 v212, v212, v216\n"
"v_add_f32_e32 v213, v213, v217\n"
"v_add_f32_e32 v214, v214, v218\n"
"v_add_f32_e32 v215, v215, v219\n"
"s_waitcnt vmcnt(19)\n"
"; @B G1 1.0\n"
"v_mfma_f32_16x16x32_bf16 v[168:171], a[0:3], v[104:107], 0\n"
"s_add_u32 s98, s62, 0x44800\n"
"v_mfma_f32_16x16x32_bf16 v[180:183], a[4:7], v[104:107], 0\n"
"buffer_load_dwordx2 v[250:251], v10, s[4:7], s98 offen sc1\n"
"v_mfma_f32_16x16x32_bf16 v[204:207], a[8:11], v[104:107], 0\n"
"buffer_load_dwordx2 v[248:249], v10, s[4:7], s98 offen offset:128 sc1\n"
"v_mfma_f32_16x16x32_bf16 v[208:211], a[12:15], v[104:107], 0\n"
"buffer_load_dwordx2 v[246:247], v10, s[4:7], s98 offen offset:256 sc1\n"
"v_mfma_f32_16x16x32_bf16 v[216:219], a[16:19], v[104:107], 0\n"
"s_add_u32 s97, s54, 0x448000\n"
"v_add_f32_e32 v192, v192, v144\n"
"v_mfma_f32_16x16x32_bf16 v[220:223], a[20:23], v[104:107], 0\n"
"buffer_load_dwordx4 v[104:107], v0, s[4:7], s97 offen sc1\n"
"v_mfma_f32_16x16x32_bf16 v[168:171], a[24:27], v[120:123], v[168:171]\n"
"buffer_load_dwordx2 v[244:245], v10, s[4:7], s98 offen offset:384 sc1\n"
"v_mfma_f32_16x16x32_bf16 v[180:183], a[28:31], v[120:123], v[180:183]\n"
"v_add_f32_e32 v193, v193, v145\n"
"v_add_f32_e32 v194, v194, v146\n"
"v_mfma_f32_16x16x32_bf16 v[204:207], a[32:35], v[120:123], v[204:207]\n"
"buffer_load_dwordx4 a[152:155], v1, s[4:7], s99 offen offset:2048 sc1\n"
"v_mfma_f32_16x16x32_bf16 v[208:211], a[36:39], v[120:123], v[208:211]\n"
"v_add_f32_e32 v195, v195, v147\n"
"v_add_f32_e32 v112, v112, v156\n"
"v_mfma_f32_16x16x32_bf16 v[216:219], a[40:43], v[120:123], v[216:219]\n"
"buffer_load_dwordx4 v[144:147], v0, s[4:7], s97 offen offset:1024 sc1\n"
"v_mfma_f32_16x16x32_bf16 v[220:223], a[44:47], v[120:123], v[220:223]\n"
"s_waitcnt vmcnt(22)\n"
"v_add_f32_e32 v113, v113, v157\n"
"v_mfma_f32_16x16x32_bf16 v[168:171], a[48:51], v[128:131], v[168:171]\n"
"v_add_f32_e32 v114, v114, v158\n"
"v_add_f32_e32 v115, v115, v159\n"
"v_mfma_f32_16x16x32_bf16 v[180:183], a[52:55], v[128:131], v[180:183]\n"
"v_mul_f32_e32 v192, v192, v253\n"
"v_mul_f32_e32 v193, v193, v253\n"
"v_mfma_f32_16x16x32_bf16 v[204:207], a[56:59], v[128:131], v[204:207]\n"
"v_mul_f32_e32 v194, v194, v253\n"
"v_mul_f32_e32 v195, v195, v253\n"
"v_mfma_f32_16x16x32_bf16 v[208:211], a[60:63], v[128:131], v[208:211]\n"
"v_mul_f32_e32 v112, v112, v253\n"
"v_mul_f32_e32 v113, v113, v253\n"
"v_mfma_f32_16x16x32_bf16 v[216:219], a[64:67], v[128:131], v[216:219]\n"
"buffer_load_dwordx4 v[120:123], v0, s[4:7], s97 offen offset:2048 sc1\n"
"v_mfma_f32_16x16x32_bf16 v[220:223], a[68:71], v[128:131], v[220:223]\n"
"v_mul_f32_e32 v114, v114, v253\n"
"v_mul_f32_e32 v115, v115, v253\n"
"v_mfma_f32_16x16x32_bf16 v[168:171], a[72:75], v[196:199], v[168:171]\n"
"v_cvt_pk_bf16_f32 v128, v192, v193\n"
"v_cvt_pk_bf16_f32 v129, v194, v195\n"
"v_mfma_f32_16x16x32_bf16 v[180:183], a[76:79], v[196:199], v[180:183]\n"
"v_cvt_pk_bf16_f32 v130, v112, v113\n"
"v_cvt_pk_bf16_f32 v131, v114, v115\n"
"v_mfma_f32_16x16x32_bf16 v[204:207], a[80:83], v[196:199], v[204:207]\n"
"ds_write_b128 v7, v[128:131] offset:24576\n"
"v_mfma_f32_16x16x32_bf16 v[208:211], a[84:87], v[196:199], v[208:211]\n"
"v_add_f32_e32 v132, v132, v152\n"
"v_add_f32_e32 v133, v133, v153\n"
"v_mfma_f32_16x16x32_bf16 v[216:219], a[88:91], v[196:199], v[216:219]\n"
"buffer_load_dwordx4 v[112:115], v0, s[4:7], s97 offen offset:3072 sc1\n"
"v_mfma_f32_16x16x32_bf16 v[220:223], a[92:95], v[196:199], v[220:223]\n"
"s_waitcnt vmcnt(22)\n"
"v_add_f32_e32 v134, v134, v154\n"
"v_mfma_f32_16x16x32_bf16 v[168:171], a[96:99], v[92:95], v[168:171]\n"
"v_add_f32_e32 v135, v135, v155\n"
"v_add_f32_e32 v164, v164, v188\n"
"v_mfma_f32_16x16x32_bf16 v[180:183], a[100:103], v[92:95], v[180:183]\n"
"v_add_f32_e32 v165, v165, v189\n"
"v_add_f32_e32 v166, v166, v190\n"
"v_mfma_f32_16x16x32_bf16 v[204:207], a[104:107], v[92:95], v[204:207]\n"
"v_add_f32_e32 v167, v167, v191\n"
"v_mul_f32_e32 v132, v132, v253\n"
"v_mfma_f32_16x16x32_bf16 v[208:211], a[108:111], v[92:95], v[208:211]\n"
"v_mul_f32_e32 v133, v133, v253\n"
"v_mul_f32_e32 v134, v134, v253\n"
"v_mfma_f32_16x16x32_bf16 v[216:219], a[112:115], v[92:95], v[216:219]\n"
"buffer_load_dwordx4 v[128:131], v1, s[4:7], s97 offen sc1\n"
"v_mfma_f32_16x16x32_bf16 v[220:223], a[116:119], v[92:95], v[220:223]\n"
"v_mul_f32_e32 v135, v135, v253\n"
"v_mul_f32_e32 v164, v164, v253\n"
"v_mfma_f32_16x16x32_bf16 v[168:171], a[120:123], v[100:103], v[168:171]\n"
"v_mul_f32_e32 v165, v165, v253\n"
"v_mul_f32_e32 v166, v166, v253\n"
"v_mfma_f32_16x16x32_bf16 v[180:183], a[124:127], v[100:103], v[180:183]\n"
"v_mul_f32_e32 v167, v167, v253\n"
"v_cvt_pk_bf16_f32 v92, v132, v133\n"
"v_mfma_f32_16x16x32_bf16 v[204:207], v[24:27], v[100:103], v[204:207]\n"
"buffer_load_dwordx4 a[156:159], v1, s[4:7], s99 offen offset:3072 sc1\n"
"v_mfma_f32_16x16x32_bf16 v[208:211], v[28:31], v[100:103], v[208:211]\n"
"v_cvt_pk_bf16_f32 v93, v134, v135\n"
"v_cvt_pk_bf16_f32 v94, v164, v165\n"
"v_mfma_f32_16x16x32_bf16 v[216:219], v[32:35], v[100:103], v[216:219]\n"
"buffer_load_dwordx4 v[132:135], v1, s[4:7], s97 offen offset:1024 sc1\n"
"v_mfma_f32_16x16x32_bf16 v[220:223], v[36:39], v[100:103], v[220:223]\n"
"s_waitcnt vmcnt(23)\n"
"v_cvt_pk_bf16_f32 v95, v166, v167\n"
"v_mfma_f32_16x16x32_bf16 v[168:171], v[40:43], v[88:91], v[168:171]\n"
"ds_write_b128 v7, v[92:95] offset:57344\n"
"v_mfma_f32_16x16x32_bf16 v[180:183], v[44:47], v[88:91], v[180:183]\n"
"v_add_f32_e32 v160, v160, v200\n"
"v_add_f32_e32 v161, v161, v201\n"
"v_mfma_f32_16x16x32_bf16 v[204:207], v[48:51], v[88:91], v[204:207]\n"
"v_add_f32_e32 v162, v162, v202\n"
"v_add_f32_e32 v163, v163, v203\n"
"v_mfma_f32_16x16x32_bf16 v[208:211], v[52:55], v[88:91], v[208:211]\n"
"v_add_f32_e32 v184, v184, v212\n"
"v_add_f32_e32 v185, v185, v213\n"
"v_mfma_f32_16x16x32_bf16 v[216:219], v[56:59], v[88:91], v[216:219]\n"
"buffer_load_dwordx4 v[92:95], v1, s[4:7], s97 offen offset:2048 sc1\n"
"v_mfma_f32_16x16x32_bf16 v[220:223], v[60:63], v[88:91], v[220:223]\n"
"v_add_f32_e32 v186, v186, v214\n"
"v_add_f32_e32 v187, v187, v215\n"
"v_mfma_f32_16x16x32_bf16 v[168:171], v[64:67], v[96:99], v[168:171]\n"
"v_mul_f32_e32 v160, v160, v255\n"
"v_mul_f32_e32 v161, v161, v255\n"
"v_mfma_f32_16x16x32_bf16 v[180:183], v[68:71], v[96:99], v[180:183]\n"
"v_mul_f32_e32 v162, v162, v255\n"
"v_mul_f32_e32 v163, v163, v255\n"
"v_mfma_f32_16x16x32_bf16 v[204:207], v[72:75], v[96:99], v[204:207]\n"
"v_mul_f32_e32 v184, v184, v255\n"
"v_mul_f32_e32 v185, v185, v255\n"
"v_mfma_f32_16x16x32_bf16 v[208:211], v[76:79], v[96:99], v[208:211]\n"
"v_mul_f32_e32 v186, v186, v255\n"
"v_mul_f32_e32 v187, v187, v255\n"
"v_mfma_f32_16x16x32_bf16 v[216:219], v[80:83], v[96:99], v[216:219]\n"
"buffer_load_dwordx4 v[88:91], v1, s[4:7], s97 offen offset:3072 sc1\n"
"v_mfma_f32_16x16x32_bf16 v[220:223], v[84:87], v[96:99], v[220:223]\n"
"v_cmp_u_f32_e32 vcc, v168, v168\n"
"s_waitcnt vmcnt(21)\n"
"; @B G1 1.1\n"
"v_mfma_f32_16x16x32_bf16 v[96:99], a[0:3], v[108:111], 0\n"
"s_cbranch_vccnz .Lmk_p53s\n"
".Lmk_p53r:\n"
"v_cvt_pk_bf16_f32 v100, v160, v161\n"
"v_mfma_f32_16x16x32_bf16 v[152:155], a[4:7], v[108:111], 0\n"
"v_cvt_pk_bf16_f32 v101, v162, v163\n"
"v_cvt_pk_bf16_f32 v102, v184, v185\n"
"v_mfma_f32_16x16x32_bf16 v[156:159], a[8:11], v[108:111], 0\n"
"v_cvt_pk_bf16_f32 v103, v186, v187\n"
"v_mfma_f32_16x16x32_bf16 v[160:163], a[12:15], v[108:111], 0\n"
"ds_write_b128 v3, v[168:171]\n"
"v_mfma_f32_16x16x32_bf16 v[164:167], a[16:19], v[108:111], 0\n"
"s_add_u32 s99, s55, 0x448000\n"
"v_mfma_f32_16x16x32_bf16 v[168:171], a[20:23], v[108:111], 0\n"
"buffer_load_dwordx4 v[108:111], v0, s[4:7], s99 offen sc1\n"
"v_mfma_f32_16x16x32_bf16 v[96:99], a[24:27], v[116:119], v[96:99]\n"
"ds_write_b128 v3, v[180:183] offset:1024\n"
"v_mfma_f32_16x16x32_bf16 v[152:155], a[28:31], v[116:119], v[152:155]\n"
"s_add_u32 s98, s70, 0x202000\n"
"v_mfma_f32_16x16x32_bf16 v[156:159], a[32:35], v[116:119], v[156:159]\n"
"buffer_load_dwordx4 a[160:163], v0, s[4:7], s98 offen sc1\n"
"v_mfma_f32_16x16x32_bf16 v[160:163], a[36:39], v[116:119], v[160:163]\n"
"ds_write_b128 v3, v[204:207] offset:2048\n"
"v_mfma_f32_16x16x32_bf16 v[164:167], a[40:43], v[116:119], v[164:167]\n"
"buffer_load_dwordx4 v[180:183], v0, s[4:7], s99 offen offset:1024 sc1\n"
"v_mfma_f32_16x16x32_bf16 v[168:171], a[44:47], v[116:119], v[168:171]\n"
"s_waitcnt vmcnt(22)\n"
"v_mfma_f32_16x16x32_bf16 v[96:99], a[48:51], v[124:127], v[96:99]\n"
"ds_write_b128 v3, v[208:211] offset:3072\n"
"v_mfma_f32_16x16x32_bf16 v[152:155], a[52:55], v[124:127], v[152:155]\n"
"s_add_u32 s97, s67, 0x10000\n"
"v_mfma_f32_16x16x32_bf16 v[156:159], a[56:59], v[124:127], v[156:159]\n"
"buffer_load_dwordx2 v[254:255], v12, s[4:7], s97 offen sc1\n"
"v_mfma_f32_16x16x32_bf16 v[160:163], a[60:63], v[124:127], v[160:163]\n"
"ds_write_b128 v3, v[216:219] offset:4096\n"
"v_mfma_f32_16x16x32_bf16 v[164:167], a[64:67], v[124:127], v[164:167]\n"
"buffer_load_dwordx4 v[116:119], v0, s[4:7], s99 offen offset:2048 sc1\n"
"v_mfma_f32_16x16x32_bf16 v[168:171], a[68:71], v[124:127], v[168:171]\n"
"ds_write_b128 v2, v[100:103] offset:49152\n"
"v_mfma_f32_16x16x32_bf16 v[96:99], a[72:75], v[136:139], v[96:99]\n"
"ds_write_b128 v3, v[220:223] offset:5120\n"
"v_mfma_f32_16x16x32_bf16 v[152:155], a[76:79], v[136:139], v[152:155]\n"
"v_mfma_f32_16x16x32_bf16 v[156:159], a[80:83], v[136:139], v[156:159]\n"
"v_mov_b32_e32 v100, -1\n"
"v_mov_b32_e32 v101, -1\n"
"v_mfma_f32_16x16x32_bf16 v[160:163], a[84:87], v[136:139], v[160:163]\n"
"v_mov_b32_e32 v102, -1\n"
"v_mov_b32_e32 v103, -1\n"
"v_mfma_f32_16x16x32_bf16 v[164:167], a[88:91], v[136:139], v[164:167]\n"
"buffer_load_dwordx4 v[124:127], v0, s[4:7], s99 offen offset:3072 sc1\n"
"v_mfma_f32_16x16x32_bf16 v[168:171], a[92:95], v[136:139], v[168:171]\n"
"s_waitcnt vmcnt(22)\n"
"s_add_u32 s96, s60, 0x400000\n"
"v_mfma_f32_16x16x32_bf16 v[96:99], a[96:99], v[140:143], v[96:99]\n"
"buffer_store_dwordx4 v[100:103], v0, s[4:7], s96 offen sc1\n"
"v_mfma_f32_16x16x32_bf16 v[152:155], a[100:103], v[140:143], v[152:155]\n"
"v_mfma_f32_16x16x32_bf16 v[156:159], a[104:107], v[140:143], v[156:159]\n"
"v_mfma_f32_16x16x32_bf16 v[160:163], a[108:111], v[140:143], v[160:163]\n"
"v_mfma_f32_16x16x32_bf16 v[164:167], a[112:115], v[140:143], v[164:167]\n"
"buffer_load_dwordx4 v[136:139], v1, s[4:7], s99 offen sc1\n"
"v_mfma_f32_16x16x32_bf16 v[168:171], a[116:119], v[140:143], v[168:171]\n"
"s_waitcnt lgkmcnt(0)\n"
"s_barrier\n"
"v_mfma_f32_16x16x32_bf16 v[96:99], a[120:123], v[176:179], v[96:99]\n"
"s_waitcnt vmcnt(17)\n"
"v_add_f32_e32 v250, v250, v248\n"
"v_mfma_f32_16x16x32_bf16 v[152:155], a[124:127], v[176:179], v[152:155]\n"
"v_add_f32_e32 v251, v251, v249\n"
"v_add_f32_e32 v246, v246, v244\n"
"v_mfma_f32_16x16x32_bf16 v[156:159], v[24:27], v[176:179], v[156:159]\n"
"buffer_load_dwordx4 a[164:167], v0, s[4:7], s98 offen offset:1024 sc1\n"
"v_mfma_f32_16x16x32_bf16 v[160:163], v[28:31], v[176:179], v[160:163]\n"
"v_add_f32_e32 v247, v247, v245\n"
"v_add_f32_e32 v250, v250, v246\n"
"v_mfma_f32_16x16x32_bf16 v[164:167], v[32:35], v[176:179], v[164:167]\n"
"buffer_load_dwordx4 v[140:143], v1, s[4:7], s99 offen offset:1024 sc1\n"
"v_mfma_f32_16x16x32_bf16 v[168:171], v[36:39], v[176:179], v[168:171]\n"
"v_add_f32_e32 v251, v251, v247\n"
"v_mfma_f32_16x16x32_bf16 v[96:99], v[40:43], v[148:151], v[96:99]\n"
"v_cmp_u_f32_e32 vcc, v250, v251\n"
"v_mfma_f32_16x16x32_bf16 v[152:155], v[44:47], v[148:151], v[152:155]\n"
"s_cbranch_vccnz .Lmk_m58s\n"
".Lmk_m58r:\n"
"v_mfma_f32_16x16x32_bf16 v[156:159], v[48:51], v[148:151], v[156:159]\n"
"v_permlane16_swap_b32_e32 v250, v251\n"
"v_add_f32_e32 v250, v250, v251\n"
"v_mfma_f32_16x16x32_bf16 v[160:163], v[52:55], v[148:151], v[160:163]\n"
"v_mov_b32_e32 v251, v250\n"
"ds_read_b128 v[176:179], v4\n"
"v_mfma_f32_16x16x32_bf16 v[164:167], v[56:59], v[148:151], v[164:167]\n"
"buffer_load_dwordx4 v[184:187], v1, s[4:7], s99 offen offset:2048 sc1\n"
"v_mfma_f32_16x16x32_bf16 v[168:171], v[60:63], v[148:151], v[168:171]\n"
"v_permlane32_swap_b32_e32 v250, v251\n"
"v_add_f32_e32 v250, v250, v251\n"
"v_mfma_f32_16x16x32_bf16 v[96:99], v[64:67], v[172:175], v[96:99]\n"
"v_mul_f32_e32 v250, 0x3a800000, v250\n"
"v_mov_b32_e32 v251, v250\n"
"v_mfma_f32_16x16x32_bf16 v[152:155], v[68:71], v[172:175], v[152:155]\n"
"s_nop 0\n"
"v_permlane16_swap_b32_e32 v250, v251\n"
"v_cvt_pk_bf16_f32 v253, -v250, 0\n"
"v_mfma_f32_16x16x32_bf16 v[156:159], v[72:75], v[172:175], v[156:159]\n"
"v_fma_f32 v251, -v250, v250, v251\n"
"v_lshlrev_b32_e32 v253, 16, v253\n"
"v_mfma_f32_16x16x32_bf16 v[160:163], v[76:79], v[172:175], v[160:163]\n"
"ds_read_b128 v[148:151], v9 offset:512\n"
"ds_read_b128 v[188:191], v4 offset:1024\n"
"v_mfma_f32_16x16x32_bf16 v[164:167], v[80:83], v[172:175], v[164:167]\n"
"buffer_load_dwordx4 v[192:195], v1, s[4:7], s99 offen offset:3072 sc1\n"
"v_mfma_f32_16x16x32_bf16 v[168:171], v[84:87], v[172:175], v[168:171]\n"
"v_cmp_u_f32_e32 vcc, v96, v96\n"
"s_waitcnt vmcnt(19)\n"
"; @B G1 1.2\n"
"v_mfma_f32_16x16x32_bf16 v[172:175], a[0:3], v[104:107], 0\n"
"s_cbranch_vccnz .Lmk_p54s\n"
".Lmk_p54r:\n"
"ds_read_b128 v[196:199], v9 offset:576\n"
"v_mfma_f32_16x16x32_bf16 v[200:203], a[4:7], v[104:107], 0\n"
"s_waitcnt vmcnt(8)\n"
"v_cmp_ne_u64_e32 vcc, v[254:255], v[18:19]\n"
"v_mfma_f32_16x16x32_bf16 v[204:207], a[8:11], v[104:107], 0\n"
"s_cbranch_vccnz .Lmk_f44s\n"
".Lmk_f44r:\n"
"ds_read_b128 v[208:211], v4 offset:2048\n"
"v_mfma_f32_16x16x32_bf16 v[212:215], a[12:15], v[104:107], 0\n"
"ds_write_b128 v3, v[96:99] offset:6144\n"
"v_mfma_f32_16x16x32_bf16 v[96:99], a[16:19], v[104:107], 0\n"
"s_add_u32 s99, s52, 0x408000\n"
"ds_read_b128 v[216:219], v9 offset:640\n"
"v_mfma_f32_16x16x32_bf16 v[220:223], a[20:23], v[104:107], 0\n"
"buffer_load_dwordx4 v[104:107], v0, s[4:7], s99 offen sc1\n"
"v_mfma_f32_16x16x32_bf16 v[172:175], a[24:27], v[144:147], v[172:175]\n"
"ds_write_b128 v3, v[152:155] offset:7168\n"
"v_mfma_f32_16x16x32_bf16 v[200:203], a[28:31], v[144:147], v[200:203]\n"
"ds_read_b128 v[152:155], v4 offset:3072\n"
"ds_read_b128 v[224:227], v9 offset:704\n"
"v_mfma_f32_16x16x32_bf16 v[204:207], a[32:35], v[144:147], v[204:207]\n"
"buffer_load_dwordx4 a[168:171], v0, s[4:7], s98 offen offset:2048 sc1\n"
"v_mfma_f32_16x16x32_bf16 v[212:215], a[36:39], v[144:147], v[212:215]\n"
"ds_write_b128 v3, v[156:159] offset:8192\n"
"v_mfma_f32_16x16x32_bf16 v[96:99], a[40:43], v[144:147], v[96:99]\n"
"buffer_load_dwordx4 v[156:159], v0, s[4:7], s99 offen offset:1024 sc1\n"
"v_mfma_f32_16x16x32_bf16 v[220:223], a[44:47], v[144:147], v[220:223]\n"
"s_add_u32 s97, s60, 0x408000\n"
"v_mfma_f32_16x16x32_bf16 v[172:175], a[48:51], v[120:123], v[172:175]\n"
"ds_write_b128 v3, v[160:163] offset:9216\n"
"v_mfma_f32_16x16x32_bf16 v[200:203], a[52:55], v[120:123], v[200:203]\n"
"buffer_store_dwordx4 v[100:103], v0, s[4:7], s97 offen sc1\n"
"v_mfma_f32_16x16x32_bf16 v[204:207], a[56:59], v[120:123], v[204:207]\n"
"s_waitcnt lgkmcnt(5)\n"
"v_fmac_f32_e32 v176, v253, v148\n"
"v_mfma_f32_16x16x32_bf16 v[212:215], a[60:63], v[120:123], v[212:215]\n"
"ds_write_b128 v3, v[164:167] offset:10240\n"
"v_mfma_f32_16x16x32_bf16 v[96:99], a[64:67], v[120:123], v[96:99]\n"
"buffer_load_dwordx4 v[144:147], v0, s[4:7], s99 offen offset:2048 sc1\n"
"v_mfma_f32_16x16x32_bf16 v[220:223], a[68:71], v[120:123], v[220:223]\n"
"v_fmac_f32_e32 v177, v253, v149\n"
"v_fmac_f32_e32 v178, v253, v150\n"
"v_mfma_f32_16x16x32_bf16 v[172:175], a[72:75], v[112:115], v[172:175]\n"
"ds_write_b128 v3, v[168:171] offset:11264\n"
"v_mfma_f32_16x16x32_bf16 v[200:203], a[76:79], v[112:115], v[200:203]\n"
"v_fmac_f32_e32 v179, v253, v151\n"
"v_fmac_f32_e32 v188, v253, v196\n"
"v_mfma_f32_16x16x32_bf16 v[204:207], a[80:83], v[112:115], v[204:207]\n"
"v_fmac_f32_e32 v189, v253, v197\n"
"v_fmac_f32_e32 v190, v253, v198\n"
"v_mfma_f32_16x16x32_bf16 v[212:215], a[84:87], v[112:115], v[212:215]\n"
"v_fmac_f32_e32 v191, v253, v199\n"
"v_fmac_f32_e32 v208, v253, v216\n"
"v_mfma_f32_16x16x32_bf16 v[96:99], a[88:91], v[112:115], v[96:99]\n"
"buffer_load_dwordx4 v[120:123], v0, s[4:7], s99 offen offset:3072 sc1\n"
"v_mfma_f32_16x16x32_bf16 v[220:223], a[92:95], v[112:115], v[220:223]\n"
"v_fmac_f32_e32 v209, v253, v217\n"
"v_mfma_f32_16x16x32_bf16 v[172:175], a[96:99], v[128:131], v[172:175]\n"
"v_fmac_f32_e32 v210, v253, v218\n"
"v_fmac_f32_e32 v211, v253, v219\n"
"v_mfma_f32_16x16x32_bf16 v[200:203], a[100:103], v[128:131], v[200:203]\n"
"ds_read_b128 v[112:115], v4 offset:4096\n"
"ds_read_b128 v[148:151], v9 offset:768\n"
"v_mfma_f32_16x16x32_bf16 v[204:207], a[104:107], v[128:131], v[204:207]\n"
"ds_read_b128 v[160:163], v4 offset:5120\n"
"ds_read_b128 v[164:167], v9 offset:832\n"
"v_mfma_f32_16x16x32_bf16 v[212:215], a[108:111], v[128:131], v[212:215]\n"
"v_max_f32_e32 v251, 0, v251\n"
"v_cvt_pk_bf16_f32 v251, v251, 0\n"
"v_mfma_f32_16x16x32_bf16 v[96:99], a[112:115], v[128:131], v[96:99]\n"
"buffer_load_dwordx4 v[168:171], v1, s[4:7], s99 offen sc1\n"
"v_mfma_f32_16x16x32_bf16 v[220:223], a[116:119], v[128:131], v[220:223]\n"
"v_lshlrev_b32_e32 v251, 16, v251\n"
"v_add_f32_e32 v251, 0x3727c5ac, v251\n"
"v_mfma_f32_16x16x32_bf16 v[172:175], a[120:123], v[132:135], v[172:175]\n"
"v_cvt_pk_bf16_f32 v251, v251, 0\n"
"v_lshlrev_b32_e32 v251, 16, v251\n"
"v_mfma_f32_16x16x32_bf16 v[200:203], a[124:127], v[132:135], v[200:203]\n"
"s_waitcnt lgkmcnt(0)\n"
"v_fmac_f32_e32 v152, v253, v224\n"
"v_mfma_f32_16x16x32_bf16 v[204:207], v[24:27], v[132:135], v[204:207]\n"
"buffer_load_dwordx4 a[172:175], v0, s[4:7], s98 offen offset:3072 sc1\n"
"v_mfma_f32_16x16x32_bf16 v[212:215], v[28:31], v[132:135], v[212:215]\n"
"v_fmac_f32_e32 v153, v253, v225\n"
"v_fmac_f32_e32 v154, v253, v226\n"
"v_mfma_f32_16x16x32_bf16 v[96:99], v[32:35], v[132:135], v[96:99]\n"
"buffer_load_dwordx4 v[128:131], v1, s[4:7], s99 offen offset:1024 sc1\n"
"v_mfma_f32_16x16x32_bf16 v[220:223], v[36:39], v[132:135], v[220:223]\n"
"v_fmac_f32_e32 v155, v253, v227\n"
"v_mfma_f32_16x16x32_bf16 v[172:175], v[40:43], v[92:95], v[172:175]\n"
"v_fmac_f32_e32 v112, v253, v148\n"
"v_fmac_f32_e32 v113, v253, v149\n"
"v_mfma_f32_16x16x32_bf16 v[200:203], v[44:47], v[92:95], v[200:203]\n"
"v_fmac_f32_e32 v114, v253, v150\n"
"v_fmac_f32_e32 v115, v253, v151\n"
"v_mfma_f32_16x16x32_bf16 v[204:207], v[48:51], v[92:95], v[204:207]\n"
"v_fmac_f32_e32 v160, v253, v164\n"
"v_fmac_f32_e32 v161, v253, v165\n"
"v_mfma_f32_16x16x32_bf16 v[212:215], v[52:55], v[92:95], v[212:215]\n"
"v_fmac_f32_e32 v162, v253, v166\n"
"v_fmac_f32_e32 v163, v253, v167\n"
"v_mfma_f32_16x16x32_bf16 v[96:99], v[56:59], v[92:95], v[96:99]\n"
"v_max_u32_e32 v255, v108, v109\n"
"v_mfma_f32_16x16x32_bf16 v[220:223], v[60:63], v[92:95], v[220:223]\n"
"v_max3_u32 v255, v255, v110, v111\n"
"v_max3_u32 v255, v255, v180, v181\n"
"v_mfma_f32_16x16x32_bf16 v[172:175], v[64:67], v[88:91], v[172:175]\n"
"v_max3_u32 v255, v255, v182, v183\n"
"s_waitcnt vmcnt(15)\n"
"v_mfma_f32_16x16x32_bf16 v[200:203], v[68:71], v[88:91], v[200:203]\n"
"v_max3_u32 v255, v255, v116, v117\n"
"v_max3_u32 v255, v255, v118, v119\n"
"v_mfma_f32_16x16x32_bf16 v[204:207], v[72:75], v[88:91], v[204:207]\n"
"v_max3_u32 v255, v255, v124, v125\n"
"v_max3_u32 v255, v255, v126, v127\n"
"v_mfma_f32_16x16x32_bf16 v[212:215], v[76:79], v[88:91], v[212:215]\n"
"v_cmp_eq_u32_e32 vcc, -1, v255\n"
"v_mfma_f32_16x16x32_bf16 v[96:99], v[80:83], v[88:91], v[96:99]\n"
"s_cbranch_vccnz .Lmk_q56s\n"
".Lmk_q56r:\n"
"v_mfma_f32_16x16x32_bf16 v[220:223], v[84:87], v[88:91], v[220:223]\n"
"v_cmp_u_f32_e32 vcc, v172, v172\n"
"; @B G1 1.3\n"
"v_mfma_f32_16x16x32_bf16 v[176:179], a[0:3], v[108:111], v[176:179]\n"
"s_cbranch_vccnz .Lmk_p55s\n"
".Lmk_p55r:\n"
"s_add_u32 s97, s61, 0x840000\n"
"v_mfma_f32_16x16x32_bf16 v[188:191], a[4:7], v[108:111], v[188:191]\n"
"buffer_load_dwordx4 v[88:91], v1, s[4:7], s99 offen offset:2048 sc1\n"
"v_mfma_f32_16x16x32_bf16 v[208:211], a[8:11], v[108:111], v[208:211]\n"
"buffer_load_dwordx4 v[92:95], v1, s[4:7], s99 offen offset:3072 sc1\n"
"v_mfma_f32_16x16x32_bf16 v[152:155], a[12:15], v[108:111], v[152:155]\n"
"ds_write_b128 v3, v[172:175] offset:12288\n"
"v_mfma_f32_16x16x32_bf16 v[112:115], a[16:19], v[108:111], v[112:115]\n"
"s_add_u32 s99, s53, 0x408000\n"
"v_mfma_f32_16x16x32_bf16 v[160:163], a[20:23], v[108:111], v[160:163]\n"
"buffer_load_dwordx4 v[108:111], v0, s[4:7], s99 offen sc1\n"
"v_mfma_f32_16x16x32_bf16 v[176:179], a[24:27], v[180:183], v[176:179]\n"
"ds_write_b128 v3, v[200:203] offset:13312\n"
"v_mfma_f32_16x16x32_bf16 v[188:191], a[28:31], v[180:183], v[188:191]\n"
"buffer_store_dwordx4 v[100:103], v0, s[8:11], s97 offen sc1\n"
"v_mfma_f32_16x16x32_bf16 v[208:211], a[32:35], v[180:183], v[208:211]\n"
"buffer_load_dwordx4 a[176:179], v1, s[4:7], s98 offen sc1\n"
"v_mfma_f32_16x16x32_bf16 v[152:155], a[36:39], v[180:183], v[152:155]\n"
"ds_write_b128 v3, v[204:207] offset:14336\n"
"v_mfma_f32_16x16x32_bf16 v[112:115], a[40:43], v[180:183], v[112:115]\n"
"buffer_load_dwordx4 v[132:135], v0, s[4:7], s99 offen offset:1024 sc1\n"
"v_mfma_f32_16x16x32_bf16 v[160:163], a[44:47], v[180:183], v[160:163]\n"
"v_mfma_f32_16x16x32_bf16 v[176:179], a[48:51], v[116:119], v[176:179]\n"
"ds_write_b128 v3, v[212:215] offset:15360\n"
"v_mfma_f32_16x16x32_bf16 v[188:191], a[52:55], v[116:119], v[188:191]\n"
"v_sqrt_f32_e32 v251, v251\n"
"v_mfma_f32_16x16x32_bf16 v[208:211], a[56:59], v[116:119], v[208:211]\n"
"v_cvt_pk_bf16_f32 v251, v251, 0\n"
"v_lshlrev_b32_e32 v251, 16, v251\n"
"v_mfma_f32_16x16x32_bf16 v[152:155], a[60:63], v[116:119], v[152:155]\n"
"ds_write_b128 v3, v[96:99] offset:16384\n"
"v_mfma_f32_16x16x32_bf16 v[112:115], a[64:67], v[116:119], v[112:115]\n"
"s_waitcnt vmcnt(17)\n"
"v_max_u32_e32 v255, v136, v137\n"
"v_mfma_f32_16x16x32_bf16 v[160:163], a[68:71], v[116:119], v[160:163]\n"
"v_max3_u32 v255, v255, v138, v139\n"
"v_max3_u32 v255, v255, v140, v141\n"
"v_mfma_f32_16x16x32_bf16 v[176:179], a[72:75], v[124:127], v[176:179]\n"
"v_max3_u32 v255, v255, v142, v143\n"
"s_waitcnt vmcnt(15)\n"
"v_mfma_f32_16x16x32_bf16 v[188:191], a[76:79], v[124:127], v[188:191]\n"
"v_max3_u32 v255, v255, v184, v185\n"
"v_max3_u32 v255, v255, v186, v187\n"
"v_mfma_f32_16x16x32_bf16 v[208:211], a[80:83], v[124:127], v[208:211]\n"
"v_max3_u32 v255, v255, v192, v193\n"
"v_max3_u32 v255, v255, v194, v195\n"
"v_mfma_f32_16x16x32_bf16 v[152:155], a[84:87], v[124:127], v[152:155]\n"
"v_cmp_eq_u32_e32 vcc, -1, v255\n"
"v_mfma_f32_16x16x32_bf16 v[112:115], a[88:91], v[124:127], v[112:115]\n"
"s_cbranch_vccnz .Lmk_q57s\n"
".Lmk_q57r:\n"
"v_mfma_f32_16x16x32_bf16 v[160:163], a[92:95], v[124:127], v[160:163]\n"
"v_mfma_f32_16x16x32_bf16 v[176:179], a[96:99], v[136:139], v[176:179]\n"
"buffer_load_dwordx4 v[96:99], v0, s[4:7], s99 offen offset:2048 sc1\n"
"v_mfma_f32_16x16x32_bf16 v[188:191], a[100:103], v[136:139], v[188:191]\n"
"buffer_load_dwordx4 v[116:119], v0, s[4:7], s99 offen offset:3072 sc1\n"
"v_mfma_f32_16x16x32_bf16 v[208:211], a[104:107], v[136:139], v[208:211]\n"
"ds_write_b128 v3, v[220:223] offset:17408\n"
"v_mfma_f32_16x16x32_bf16 v[152:155], a[108:111], v[136:139], v[152:155]\n"
"v_rcp_f32_e32 v255, v251\n"
"v_mfma_f32_16x16x32_bf16 v[112:115], a[112:115], v[136:139], v[112:115]\n"
"buffer_load_dwordx4 v[124:127], v1, s[4:7], s99 offen sc1\n"
"v_mfma_f32_16x16x32_bf16 v[160:163], a[116:119], v[136:139], v[160:163]\n"
"s_waitcnt lgkmcnt(0)\n"
"s_barrier\n"
"v_mfma_f32_16x16x32_bf16 v[176:179], a[120:123], v[140:143], v[176:179]\n"
"ds_read_b128 v[136:139], v5 offset:2048\n"
"ds_read_b128 v[148:151], v6 offset:2048\n"
"v_mfma_f32_16x16x32_bf16 v[188:191], a[124:127], v[140:143], v[188:191]\n"
"ds_read_b128 v[164:167], v5 offset:3072\n"
"ds_read_b128 v[172:175], v6 offset:3072\n"
"v_mfma_f32_16x16x32_bf16 v[208:211], v[24:27], v[140:143], v[208:211]\n"
"buffer_load_dwordx4 a[180:183], v1, s[4:7], s98 offen offset:1024 sc1\n"
"v_mfma_f32_16x16x32_bf16 v[152:155], v[28:31], v[140:143], v[152:155]\n"
"v_mul_f32_e32 v254, 0x3e8293ee, v255\n"
"v_mfma_f32_16x16x32_bf16 v[112:115], v[32:35], v[140:143], v[112:115]\n"
"buffer_load_dwordx4 v[180:183], v1, s[4:7], s99 offen offset:1024 sc1\n"
"v_mfma_f32_16x16x32_bf16 v[160:163], v[36:39], v[140:143], v[160:163]\n"
"ds_read_b128 v[140:143], v5 offset:4096\n"
"v_mfma_f32_16x16x32_bf16 v[176:179], v[40:43], v[184:187], v[176:179]\n"
"ds_read_b128 v[196:199], v6 offset:4096\n"
"ds_read_b128 v[200:203], v5 offset:5120\n"
"v_mfma_f32_16x16x32_bf16 v[188:191], v[44:47], v[184:187], v[188:191]\n"
"ds_read_b128 v[204:207], v6 offset:5120\n"
"s_waitcnt lgkmcnt(4)\n"
"v_mfma_f32_16x16x32_bf16 v[208:211], v[48:51], v[184:187], v[208:211]\n"
"v_add_f32_e32 v136, v136, v148\n"
"v_add_f32_e32 v137, v137, v149\n"
"v_mfma_f32_16x16x32_bf16 v[152:155], v[52:55], v[184:187], v[152:155]\n"
"v_add_f32_e32 v138, v138, v150\n"
"v_add_f32_e32 v139, v139, v151\n"
"v_mfma_f32_16x16x32_bf16 v[112:115], v[56:59], v[184:187], v[112:115]\n"
"buffer_load_dwordx4 v[148:151], v1, s[4:7], s99 offen offset:2048 sc1\n"
"v_mfma_f32_16x16x32_bf16 v[160:163], v[60:63], v[184:187], v[160:163]\n"
"ds_read_b128 v[184:187], v5\n"
"ds_read_b128 v[212:215], v6\n"
"v_mfma_f32_16x16x32_bf16 v[176:179], v[64:67], v[192:195], v[176:179]\n"
"s_add_u32 s97, s61, 0x840000\n"
"ds_read_b128 v[216:219], v5 offset:1024\n"
"v_mfma_f32_16x16x32_bf16 v[188:191], v[68:71], v[192:195], v[188:191]\n"
"ds_read_b128 v[220:223], v6 offset:1024\n"
"v_add_f32_e32 v164, v164, v172\n"
"v_mfma_f32_16x16x32_bf16 v[208:211], v[72:75], v[192:195], v[208:211]\n"
"buffer_store_dwordx4 v[100:103], v0, s[8:11], s97 offen offset:1024 sc1\n"
"v_mfma_f32_16x16x32_bf16 v[152:155], v[76:79], v[192:195], v[152:155]\n"
"v_add_f32_e32 v165, v165, v173\n"
"v_add_f32_e32 v166, v166, v174\n"
"v_mfma_f32_16x16x32_bf16 v[112:115], v[80:83], v[192:195], v[112:115]\n"
"buffer_load_dwordx4 v[100:103], v1, s[4:7], s99 offen offset:3072 sc1\n"
"v_mfma_f32_16x16x32_bf16 v[160:163], v[84:87], v[192:195], v[160:163]\n"
"v_add_f32_e32 v167, v167, v175\n"
"s_waitcnt lgkmcnt(4)\n"
"v_add_f32_e32 v140, v140, v196\n"
"v_add_f32_e32 v141, v141, v197\n"
"v_add_f32_e32 v142, v142, v198\n"
"v_add_f32_e32 v143, v143, v199\n"
"v_add_f32_e32 v200, v200, v204\n"
"v_add_f32_e32 v201, v201, v205\n"
"v_add_f32_e32 v202, v202, v206\n"
"v_add_f32_e32 v203, v203, v207\n"
"s_waitcnt lgkmcnt(0)\n"
"v_add_f32_e32 v184, v184, v212\n"
"v_add_f32_e32 v185, v185, v213\n"
"v_add_f32_e32 v186, v186, v214\n"
"v_add_f32_e32 v187, v187, v215\n"
"v_add_f32_e32 v216, v216, v220\n"
"v_add_f32_e32 v217, v217, v221\n"
"v_add_f32_e32 v218, v218, v222\n"
"v_add_f32_e32 v219, v219, v223\n"
"s_waitcnt vmcnt(20)\n"
"; @B G1 2.0\n"
"v_mfma_f32_16x16x32_bf16 v[172:175], a[0:3], v[104:107], 0\n"
"s_add_u32 s99, s62, 0x40800\n"
"v_mfma_f32_16x16x32_bf16 v[192:195], a[4:7], v[104:107], 0\n"
"buffer_load_dwordx2 v[252:253], v10, s[4:7], s99 offen sc1\n"
"v_mfma_f32_16x16x32_bf16 v[196:199], a[8:11], v[104:107], 0\n"
"buffer_load_dwordx2 v[250:251], v10, s[4:7], s99 offen offset:128 sc1\n"
"v_mfma_f32_16x16x32_bf16 v[204:207], a[12:15], v[104:107], 0\n"
"buffer_load_dwordx2 v[248:249], v10, s[4:7], s99 offen offset:256 sc1\n"
"v_mfma_f32_16x16x32_bf16 v[212:215], a[16:19], v[104:107], 0\n"
"s_add_u32 s97, s54, 0x408000\n"
"v_add_f32_e32 v208, v208, v136\n"
"v_mfma_f32_16x16x32_bf16 v[220:223], a[20:23], v[104:107], 0\n"
"buffer_load_dwordx4 v[104:107], v0, s[4:7], s97 offen sc1\n"
"v_mfma_f32_16x16x32_bf16 v[172:175], a[24:27], v[156:159], v[172:175]\n"
"buffer_load_dwordx2 v[246:247], v10, s[4:7], s99 offen offset:384 sc1\n"
"v_mfma_f32_16x16x32_bf16 v[192:195], a[28:31], v[156:159], v[192:195]\n"
"v_add_f32_e32 v209, v209, v137\n"
"v_add_f32_e32 v210, v210, v138\n"
"v_mfma_f32_16x16x32_bf16 v[196:199], a[32:35], v[156:159], v[196:199]\n"
"buffer_load_dwordx4 a[184:187], v1, s[4:7], s98 offen offset:2048 sc1\n"
"v_mfma_f32_16x16x32_bf16 v[204:207], a[36:39], v[156:159], v[204:207]\n"
"v_add_f32_e32 v211, v211, v139\n"
"v_add_f32_e32 v152, v152, v164\n"
"v_mfma_f32_16x16x32_bf16 v[212:215], a[40:43], v[156:159], v[212:215]\n"
"buffer_load_dwordx4 v[136:139], v0, s[4:7], s97 offen offset:1024 sc1\n"
"v_mfma_f32_16x16x32_bf16 v[220:223], a[44:47], v[156:159], v[220:223]\n"
"s_waitcnt vmcnt(24)\n"
"v_add_f32_e32 v153, v153, v165\n"
"v_mfma_f32_16x16x32_bf16 v[172:175], a[48:51], v[144:147], v[172:175]\n"
"v_add_f32_e32 v154, v154, v166\n"
"v_add_f32_e32 v155, v155, v167\n"
"v_mfma_f32_16x16x32_bf16 v[192:195], a[52:55], v[144:147], v[192:195]\n"
"v_mul_f32_e32 v208, v208, v255\n"
"v_mul_f32_e32 v209, v209, v255\n"
"v_mfma_f32_16x16x32_bf16 v[196:199], a[56:59], v[144:147], v[196:199]\n"
"v_mul_f32_e32 v210, v210, v255\n"
"v_mul_f32_e32 v211, v211, v255\n"
"v_mfma_f32_16x16x32_bf16 v[204:207], a[60:63], v[144:147], v[204:207]\n"
"v_mul_f32_e32 v152, v152, v255\n"
"v_mul_f32_e32 v153, v153, v255\n"
"v_mfma_f32_16x16x32_bf16 v[212:215], a[64:67], v[144:147], v[212:215]\n"
"buffer_load_dwordx4 v[156:159], v0, s[4:7], s97 offen offset:2048 sc1\n"
"v_mfma_f32_16x16x32_bf16 v[220:223], a[68:71], v[144:147], v[220:223]\n"
"v_mul_f32_e32 v154, v154, v255\n"
"v_mul_f32_e32 v155, v155, v255\n"
"v_mfma_f32_16x16x32_bf16 v[172:175], a[72:75], v[120:123], v[172:175]\n"
"v_cvt_pk_bf16_f32 v144, v208, v209\n"
"v_cvt_pk_bf16_f32 v145, v210, v211\n"
"v_mfma_f32_16x16x32_bf16 v[192:195], a[76:79], v[120:123], v[192:195]\n"
"v_cvt_pk_bf16_f32 v146, v152, v153\n"
"v_cvt_pk_bf16_f32 v147, v154, v155\n"
"v_mfma_f32_16x16x32_bf16 v[196:199], a[80:83], v[120:123], v[196:199]\n"
"ds_write_b128 v8, v[144:147] offset:25600\n"
"v_mfma_f32_16x16x32_bf16 v[204:207], a[84:87], v[120:123], v[204:207]\n"
"s_add_u32 s99, s64, 0x802000\n"
"v_mfma_f32_16x16x32_bf16 v[212:215], a[88:91], v[120:123], v[212:215]\n"
"buffer_load_dwordx4 v[152:155], v0, s[4:7], s97 offen offset:3072 sc1\n"
"v_mfma_f32_16x16x32_bf16 v[220:223], a[92:95], v[120:123], v[220:223]\n"
"s_waitcnt vmcnt(23)\n"
"v_mfma_f32_16x16x32_bf16 v[172:175], a[96:99], v[168:171], v[172:175]\n"
"buffer_store_dwordx4 v[144:147], v8, s[12:15], s99 offen offset:1024 sc1\n"
"v_mfma_f32_16x16x32_bf16 v[192:195], a[100:103], v[168:171], v[192:195]\n"
"v_add_f32_e32 v112, v112, v140\n"
"v_add_f32_e32 v113, v113, v141\n"
"v_mfma_f32_16x16x32_bf16 v[196:199], a[104:107], v[168:171], v[196:199]\n"
"v_add_f32_e32 v114, v114, v142\n"
"v_add_f32_e32 v115, v115, v143\n"
"v_mfma_f32_16x16x32_bf16 v[204:207], a[108:111], v[168:171], v[204:207]\n"
"v_add_f32_e32 v160, v160, v200\n"
"v_add_f32_e32 v161, v161, v201\n"
"v_mfma_f32_16x16x32_bf16 v[212:215], a[112:115], v[168:171], v[212:215]\n"
"buffer_load_dwordx4 v[120:123], v1, s[4:7], s97 offen sc1\n"
"v_mfma_f32_16x16x32_bf16 v[220:223], a[116:119], v[168:171], v[220:223]\n"
"v_add_f32_e32 v162, v162, v202\n"
"v_add_f32_e32 v163, v163, v203\n"
"v_mfma_f32_16x16x32_bf16 v[172:175], a[120:123], v[128:131], v[172:175]\n"
"v_mul_f32_e32 v112, v112, v255\n"
"v_mul_f32_e32 v113, v113, v255\n"
"v_mfma_f32_16x16x32_bf16 v[192:195], a[124:127], v[128:131], v[192:195]\n"
"v_mul_f32_e32 v114, v114, v255\n"
"v_mul_f32_e32 v115, v115, v255\n"
"v_mfma_f32_16x16x32_bf16 v[196:199], v[24:27], v[128:131], v[196:199]\n"
"buffer_load_dwordx4 a[188:191], v1, s[4:7], s98 offen offset:3072 sc1\n"
"v_mfma_f32_16x16x32_bf16 v[204:207], v[28:31], v[128:131], v[204:207]\n"
"v_mul_f32_e32 v160, v160, v255\n"
"v_mul_f32_e32 v161, v161, v255\n"
"v_mfma_f32_16x16x32_bf16 v[212:215], v[32:35], v[128:131], v[212:215]\n"
"buffer_load_dwordx4 v[140:143], v1, s[4:7], s97 offen offset:1024 sc1\n"
"v_mfma_f32_16x16x32_bf16 v[220:223], v[36:39], v[128:131], v[220:223]\n"
"s_waitcnt vmcnt(25)\n"
"v_mul_f32_e32 v162, v162, v255\n"
"v_mfma_f32_16x16x32_bf16 v[172:175], v[40:43], v[88:91], v[172:175]\n"
"v_mul_f32_e32 v163, v163, v255\n"
"v_cvt_pk_bf16_f32 v128, v112, v113\n"
"v_mfma_f32_16x16x32_bf16 v[192:195], v[44:47], v[88:91], v[192:195]\n"
"v_cvt_pk_bf16_f32 v129, v114, v115\n"
"v_cvt_pk_bf16_f32 v130, v160, v161\n"
"v_mfma_f32_16x16x32_bf16 v[196:199], v[48:51], v[88:91], v[196:199]\n"
"v_cvt_pk_bf16_f32 v131, v162, v163\n"
"v_mfma_f32_16x16x32_bf16 v[204:207], v[52:55], v[88:91], v[204:207]\n"
"ds_write_b128 v7, v[128:131] offset:58368\n"
"v_mfma_f32_16x16x32_bf16 v[212:215], v[56:59], v[88:91], v[212:215]\n"
"buffer_load_dwordx4 v[112:115], v1, s[4:7], s97 offen offset:2048 sc1\n"
"v_mfma_f32_16x16x32_bf16 v[220:223], v[60:63], v[88:91], v[220:223]\n"
"s_add_u32 s99, s64, 0x806000\n"
"v_mfma_f32_16x16x32_bf16 v[172:175], v[64:67], v[92:95], v[172:175]\n"
"buffer_store_dwordx4 v[128:131], v7, s[12:15], s99 offen offset:1024 sc1\n"
"v_mfma_f32_16x16x32_bf16 v[192:195], v[68:71], v[92:95], v[192:195]\n"
"v_add_f32_e32 v176, v176, v184\n"
"v_add_f32_e32 v177, v177, v185\n"
"v_mfma_f32_16x16x32_bf16 v[196:199], v[72:75], v[92:95], v[196:199]\n"
"v_add_f32_e32 v178, v178, v186\n"
"v_add_f32_e32 v179, v179, v187\n"
"v_mfma_f32_16x16x32_bf16 v[204:207], v[76:79], v[92:95], v[204:207]\n"
"v_add_f32_e32 v188, v188, v216\n"
"v_add_f32_e32 v189, v189, v217\n"
"v_mfma_f32_16x16x32_bf16 v[212:215], v[80:83], v[92:95], v[212:215]\n"
"buffer_load_dwordx4 v[88:91], v1, s[4:7], s97 offen offset:3072 sc1\n"
"v_mfma_f32_16x16x32_bf16 v[220:223], v[84:87], v[92:95], v[220:223]\n"
"s_waitcnt vmcnt(24)\n"
"v_add_f32_e32 v190, v190, v218\n"
"; @B G1 2.1\n"
"v_mfma_f32_16x16x32_bf16 v[92:95], a[0:3], v[108:111], 0\n"
"v_add_f32_e32 v191, v191, v219\n"
"v_mul_f32_e32 v176, v176, v254\n"
"v_mfma_f32_16x16x32_bf16 v[128:131], a[4:7], v[108:111], 0\n"
"v_mul_f32_e32 v177, v177, v254\n"
"v_mul_f32_e32 v178, v178, v254\n"
"v_mfma_f32_16x16x32_bf16 v[144:147], a[8:11], v[108:111], 0\n"
"v_mul_f32_e32 v179, v179, v254\n"
"v_mul_f32_e32 v188, v188, v254\n"
"v_mfma_f32_16x16x32_bf16 v[160:163], a[12:15], v[108:111], 0\n"
"ds_write_b128 v3, v[172:175]\n"
"v_mfma_f32_16x16x32_bf16 v[164:167], a[16:19], v[108:111], 0\n"
"s_add_u32 s99, s55, 0x408000\n"
"v_mul_f32_e32 v189, v189, v254\n"
"v_mfma_f32_16x16x32_bf16 v[168:171], a[20:23], v[108:111], 0\n"
"buffer_load_dwordx4 v[108:111], v0, s[4:7], s99 offen sc1\n"
"v_mfma_f32_16x16x32_bf16 v[92:95], a[24:27], v[132:135], v[92:95]\n"
"ds_write_b128 v3, v[192:195] offset:1024\n"
"v_mfma_f32_16x16x32_bf16 v[128:131], a[28:31], v[132:135], v[128:131]\n"
"s_add_u32 s98, s70, 0x204000\n"
"v_mul_f32_e32 v190, v190, v254\n"
"v_mfma_f32_16x16x32_bf16 v[144:147], a[32:35], v[132:135], v[144:147]\n"
"buffer_load_dwordx4 a[192:195], v0, s[4:7], s98 offen sc1\n"
"v_mfma_f32_16x16x32_bf16 v[160:163], a[36:39], v[132:135], v[160:163]\n"
"ds_write_b128 v3, v[196:199] offset:2048\n"
"v_mfma_f32_16x16x32_bf16 v[164:167], a[40:43], v[132:135], v[164:167]\n"
"buffer_load_dwordx4 v[172:175], v0, s[4:7], s99 offen offset:1024 sc1\n"
"v_mfma_f32_16x16x32_bf16 v[168:171], a[44:47], v[132:135], v[168:171]\n"
"s_waitcnt vmcnt(25)\n"
"v_mul_f32_e32 v191, v191, v254\n"
"v_mfma_f32_16x16x32_bf16 v[92:95], a[48:51], v[96:99], v[92:95]\n"
"ds_write_b128 v3, v[204:207] offset:3072\n"
"v_mfma_f32_16x16x32_bf16 v[128:131], a[52:55], v[96:99], v[128:131]\n"
"v_cvt_pk_bf16_f32 v132, v176, v177\n"
"v_cvt_pk_bf16_f32 v133, v178, v179\n"
"v_mfma_f32_16x16x32_bf16 v[144:147], a[56:59], v[96:99], v[144:147]\n"
"v_cvt_pk_bf16_f32 v134, v188, v189\n"
"v_cvt_pk_bf16_f32 v135, v190, v191\n"
"v_mfma_f32_16x16x32_bf16 v[160:163], a[60:63], v[96:99], v[160:163]\n"
"ds_write_b128 v3, v[212:215] offset:4096\n"
"v_mfma_f32_16x16x32_bf16 v[164:167], a[64:67], v[96:99], v[164:167]\n"
"buffer_load_dwordx4 v[176:179], v0, s[4:7], s99 offen offset:2048 sc1\n"
"v_mfma_f32_16x16x32_bf16 v[168:171], a[68:71], v[96:99], v[168:171]\n"
"ds_write_b128 v2, v[132:135] offset:50176\n"
"v_mfma_f32_16x16x32_bf16 v[92:95], a[72:75], v[116:119], v[92:95]\n"
"ds_write_b128 v3, v[220:223] offset:5120\n"
"v_mfma_f32_16x16x32_bf16 v[128:131], a[76:79], v[116:119], v[128:131]\n"
"v_mfma_f32_16x16x32_bf16 v[144:147], a[80:83], v[116:119], v[144:147]\n"
"v_mov_b32_e32 v96, -1\n"
"v_mov_b32_e32 v97, -1\n"
"v_mfma_f32_16x16x32_bf16 v[160:163], a[84:87], v[116:119], v[160:163]\n"
"v_mov_b32_e32 v98, -1\n"
"v_mov_b32_e32 v99, -1\n"
"v_mfma_f32_16x16x32_bf16 v[164:167], a[88:91], v[116:119], v[164:167]\n"
"buffer_load_dwordx4 v[132:135], v0, s[4:7], s99 offen offset:3072 sc1\n"
"v_mfma_f32_16x16x32_bf16 v[168:171], a[92:95], v[116:119], v[168:171]\n"
"s_waitcnt vmcnt(24)\n"
"s_add_u32 s97, s63, 0x84000\n"
"v_mfma_f32_16x16x32_bf16 v[92:95], a[96:99], v[124:127], v[92:95]\n"
"buffer_store_dwordx2 v[96:97], v14, s[8:11], s97 offen sc1\n"
"v_mfma_f32_16x16x32_bf16 v[128:131], a[100:103], v[124:127], v[128:131]\n"
"v_mfma_f32_16x16x32_bf16 v[144:147], a[104:107], v[124:127], v[144:147]\n"
"v_mfma_f32_16x16x32_bf16 v[160:163], a[108:111], v[124:127], v[160:163]\n"
"v_mfma_f32_16x16x32_bf16 v[164:167], a[112:115], v[124:127], v[164:167]\n"
"buffer_load_dwordx4 v[116:119], v1, s[4:7], s99 offen sc1\n"
"v_mfma_f32_16x16x32_bf16 v[168:171], a[116:119], v[124:127], v[168:171]\n"
"s_waitcnt lgkmcnt(0)\n"
"s_barrier\n"
"v_mfma_f32_16x16x32_bf16 v[92:95], a[120:123], v[180:183], v[92:95]\n"
"s_waitcnt vmcnt(18)\n"
"v_add_f32_e32 v252, v252, v250\n"
"v_mfma_f32_16x16x32_bf16 v[128:131], a[124:127], v[180:183], v[128:131]\n"
"v_add_f32_e32 v253, v253, v251\n"
"v_add_f32_e32 v248, v248, v246\n"
"v_mfma_f32_16x16x32_bf16 v[144:147], v[24:27], v[180:183], v[144:147]\n"
"buffer_load_dwordx4 a[196:199], v0, s[4:7], s98 offen offset:1024 sc1\n"
"v_mfma_f32_16x16x32_bf16 v[160:163], v[28:31], v[180:183], v[160:163]\n"
"v_add_f32_e32 v249, v249, v247\n"
"v_add_f32_e32 v252, v252, v248\n"
"v_mfma_f32_16x16x32_bf16 v[164:167], v[32:35], v[180:183], v[164:167]\n"
"buffer_load_dwordx4 v[124:127], v1, s[4:7], s99 offen offset:1024 sc1\n"
"v_mfma_f32_16x16x32_bf16 v[168:171], v[36:39], v[180:183], v[168:171]\n"
"v_add_f32_e32 v253, v253, v249\n"
"v_mfma_f32_16x16x32_bf16 v[92:95], v[40:43], v[148:151], v[92:95]\n"
"s_nop 0\n"
"v_permlane16_swap_b32_e32 v252, v253\n"
"v_add_f32_e32 v252, v252, v253\n"
"v_mfma_f32_16x16x32_bf16 v[128:131], v[44:47], v[148:151], v[128:131]\n"
"v_mov_b32_e32 v253, v252\n"
"v_mfma_f32_16x16x32_bf16 v[144:147], v[48:51], v[148:151], v[144:147]\n"
"s_nop 0\n"
"v_permlane32_swap_b32_e32 v252, v253\n"
"v_add_f32_e32 v252, v252, v253\n"
"v_mfma_f32_16x16x32_bf16 v[160:163], v[52:55], v[148:151], v[160:163]\n"
"v_mul_f32_e32 v252, 0x3a800000, v252\n"
"v_mov_b32_e32 v253, v252\n"
"v_mfma_f32_16x16x32_bf16 v[164:167], v[56:59], v[148:151], v[164:167]\n"
"buffer_load_dwordx4 v[180:183], v1, s[4:7], s99 offen offset:2048 sc1\n"
"v_mfma_f32_16x16x32_bf16 v[168:171], v[60:63], v[148:151], v[168:171]\n"
"v_permlane16_swap_b32_e32 v252, v253\n"
"v_cvt_pk_bf16_f32 v255, -v252, 0\n"
"v_mfma_f32_16x16x32_bf16 v[92:95], v[64:67], v[100:103], v[92:95]\n"
"v_fma_f32 v253, -v252, v252, v253\n"
"v_lshlrev_b32_e32 v255, 16, v255\n"
"v_mfma_f32_16x16x32_bf16 v[128:131], v[68:71], v[100:103], v[128:131]\n"
"ds_read_b128 v[148:151], v4\n"
"ds_read_b128 v[184:187], v9 offset:512\n"
"v_mfma_f32_16x16x32_bf16 v[144:147], v[72:75], v[100:103], v[144:147]\n"
"ds_read_b128 v[188:191], v4 offset:1024\n"
"ds_read_b128 v[192:195], v9 offset:576\n"
"v_mfma_f32_16x16x32_bf16 v[160:163], v[76:79], v[100:103], v[160:163]\n"
"ds_read_b128 v[196:199], v4 offset:2048\n"
"ds_read_b128 v[200:203], v9 offset:640\n"
"v_mfma_f32_16x16x32_bf16 v[164:167], v[80:83], v[100:103], v[164:167]\n"
"buffer_load_dwordx4 v[204:207], v1, s[4:7], s99 offen offset:3072 sc1\n"
"v_mfma_f32_16x16x32_bf16 v[168:171], v[84:87], v[100:103], v[168:171]\n"
"s_waitcnt vmcnt(20)\n"
"v_max_f32_e32 v253, 0, v253\n"
"; @B G1 2.2\n"
"v_mfma_f32_16x16x32_bf16 v[100:103], a[0:3], v[104:107], 0\n"
"v_cvt_pk_bf16_f32 v253, v253, 0\n"
"v_lshlrev_b32_e32 v253, 16, v253\n"
"v_mfma_f32_16x16x32_bf16 v[208:211], a[4:7], v[104:107], 0\n"
"v_add_f32_e32 v253, 0x3727c5ac, v253\n"
"v_cvt_pk_bf16_f32 v253, v253, 0\n"
"v_mfma_f32_16x16x32_bf16 v[212:215], a[8:11], v[104:107], 0\n"
"s_waitcnt lgkmcnt(0)\n"
"v_fmac_f32_e32 v148, v255, v184\n"
"v_mfma_f32_16x16x32_bf16 v[216:219], a[12:15], v[104:107], 0\n"
"ds_write_b128 v3, v[92:95] offset:6144\n"
"v_mfma_f32_16x16x32_bf16 v[92:95], a[16:19], v[104:107], 0\n"
"s_add_u32 s99, s52, 0x400000\n"
"v_fmac_f32_e32 v149, v255, v185\n"
"v_mfma_f32_16x16x32_bf16 v[220:223], a[20:23], v[104:107], 0\n"
"buffer_load_dwordx4 v[104:107], v0, s[4:7], s99 offen sc1\n"
"v_mfma_f32_16x16x32_bf16 v[100:103], a[24:27], v[136:139], v[100:103]\n"
"ds_write_b128 v3, v[128:131] offset:7168\n"
"v_mfma_f32_16x16x32_bf16 v[208:211], a[28:31], v[136:139], v[208:211]\n"
"v_fmac_f32_e32 v150, v255, v186\n"
"v_fmac_f32_e32 v151, v255, v187\n"
"v_mfma_f32_16x16x32_bf16 v[212:215], a[32:35], v[136:139], v[212:215]\n"
"buffer_load_dwordx4 a[200:203], v0, s[4:7], s98 offen offset:2048 sc1\n"
"v_mfma_f32_16x16x32_bf16 v[216:219], a[36:39], v[136:139], v[216:219]\n"
"ds_write_b128 v3, v[144:147] offset:8192\n"
"v_mfma_f32_16x16x32_bf16 v[92:95], a[40:43], v[136:139], v[92:95]\n"
"buffer_load_dwordx4 v[128:131], v0, s[4:7], s99 offen offset:1024 sc1\n"
"v_mfma_f32_16x16x32_bf16 v[220:223], a[44:47], v[136:139], v[220:223]\n"
"s_waitcnt vmcnt(21)\n"
"v_fmac_f32_e32 v188, v255, v192\n"
"v_mfma_f32_16x16x32_bf16 v[100:103], a[48:51], v[156:159], v[100:103]\n"
"ds_write_b128 v3, v[160:163] offset:9216\n"
"v_mfma_f32_16x16x32_bf16 v[208:211], a[52:55], v[156:159], v[208:211]\n"
"v_fmac_f32_e32 v189, v255, v193\n"
"v_fmac_f32_e32 v190, v255, v194\n"
"v_mfma_f32_16x16x32_bf16 v[212:215], a[56:59], v[156:159], v[212:215]\n"
"v_fmac_f32_e32 v191, v255, v195\n"
"v_fmac_f32_e32 v196, v255, v200\n"
"v_mfma_f32_16x16x32_bf16 v[216:219], a[60:63], v[156:159], v[216:219]\n"
"ds_write_b128 v3, v[164:167] offset:10240\n"
"v_mfma_f32_16x16x32_bf16 v[92:95], a[64:67], v[156:159], v[92:95]\n"
"buffer_load_dwordx4 v[136:139], v0, s[4:7], s99 offen offset:2048 sc1\n"
"v_mfma_f32_16x16x32_bf16 v[220:223], a[68:71], v[156:159], v[220:223]\n"
"v_fmac_f32_e32 v197, v255, v201\n"
"v_fmac_f32_e32 v198, v255, v202\n"
"v_mfma_f32_16x16x32_bf16 v[100:103], a[72:75], v[152:155], v[100:103]\n"
"ds_write_b128 v3, v[168:171] offset:11264\n"
"v_mfma_f32_16x16x32_bf16 v[208:211], a[76:79], v[152:155], v[208:211]\n"
"v_fmac_f32_e32 v199, v255, v203\n"
"ds_read_b128 v[144:147], v4 offset:3072\n"
"v_mfma_f32_16x16x32_bf16 v[212:215], a[80:83], v[152:155], v[212:215]\n"
"ds_read_b128 v[156:159], v9 offset:704\n"
"ds_read_b128 v[160:163], v4 offset:4096\n"
"v_mfma_f32_16x16x32_bf16 v[216:219], a[84:87], v[152:155], v[216:219]\n"
"ds_read_b128 v[164:167], v9 offset:768\n"
"ds_read_b128 v[168:171], v4 offset:5120\n"
"v_mfma_f32_16x16x32_bf16 v[92:95], a[88:91], v[152:155], v[92:95]\n"
"buffer_load_dwordx4 v[184:187], v0, s[4:7], s99 offen offset:3072 sc1\n"
"v_mfma_f32_16x16x32_bf16 v[220:223], a[92:95], v[152:155], v[220:223]\n"
"s_waitcnt vmcnt(19)\n"
"ds_read_b128 v[152:155], v9 offset:832\n"
"v_mfma_f32_16x16x32_bf16 v[100:103], a[96:99], v[120:123], v[100:103]\n"
"v_lshlrev_b32_e32 v253, 16, v253\n"
"v_mfma_f32_16x16x32_bf16 v[208:211], a[100:103], v[120:123], v[208:211]\n"
"v_sqrt_f32_e32 v253, v253\n"
"v_mfma_f32_16x16x32_bf16 v[212:215], a[104:107], v[120:123], v[212:215]\n"
"v_cvt_pk_bf16_f32 v253, v253, 0\n"
"v_lshlrev_b32_e32 v253, 16, v253\n"
"v_mfma_f32_16x16x32_bf16 v[216:219], a[108:111], v[120:123], v[216:219]\n"
"v_rcp_f32_e32 v254, v253\n"
"v_mfma_f32_16x16x32_bf16 v[92:95], a[112:115], v[120:123], v[92:95]\n"
"buffer_load_dwordx4 v[192:195], v1, s[4:7], s99 offen sc1\n"
"v_mfma_f32_16x16x32_bf16 v[220:223], a[116:119], v[120:123], v[220:223]\n"
"s_waitcnt lgkmcnt(0)\n"
"v_fmac_f32_e32 v144, v255, v156\n"
"v_mfma_f32_16x16x32_bf16 v[100:103], a[120:123], v[140:143], v[100:103]\n"
"s_add_u32 s97, s60, 0x848000\n"
"v_fmac_f32_e32 v145, v255, v157\n"
"v_add_u32_e32 v253, 0x26c00, v0\n"
"v_mfma_f32_16x16x32_bf16 v[208:211], a[124:127], v[140:143], v[208:211]\n"
"v_fmac_f32_e32 v146, v255, v158\n"
"v_fmac_f32_e32 v147, v255, v159\n"
"ds_read_b128 v[120:123], v253\n"
"v_mfma_f32_16x16x32_bf16 v[212:215], v[24:27], v[140:143], v[212:215]\n"
"buffer_store_dwordx4 v[96:99], v0, s[8:11], s97 offen sc1\n"
"v_mfma_f32_16x16x32_bf16 v[216:219], v[28:31], v[140:143], v[216:219]\n"
"buffer_load_dwordx4 a[204:207], v0, s[4:7], s98 offen offset:3072 sc1\n"
"ds_read_b128 v[156:159], v253 offset:1024\n"
"v_mfma_f32_16x16x32_bf16 v[92:95], v[32:35], v[140:143], v[92:95]\n"
"buffer_load_dwordx4 v[200:203], v1, s[4:7], s99 offen offset:1024 sc1\n"
"v_xor_b32_e32 v253, 32, v15\n"
"v_mfma_f32_16x16x32_bf16 v[220:223], v[36:39], v[140:143], v[220:223]\n"
"s_waitcnt vmcnt(20)\n"
"ds_read_b128 v[140:143], v2 offset:50176\n"
"v_mfma_f32_16x16x32_bf16 v[100:103], v[40:43], v[112:115], v[100:103]\n"
"s_add_u32 s97, s68, 0x11000\n"
"v_fmac_f32_e32 v160, v255, v164\n"
"ds_read_b128 v[224:227], v15 offset:24576\n"
"v_mfma_f32_16x16x32_bf16 v[208:211], v[44:47], v[112:115], v[208:211]\n"
"buffer_store_dwordx2 v[18:19], v11, s[12:15], s97 offen offset:64 sc1\n"
"v_mfma_f32_16x16x32_bf16 v[212:215], v[48:51], v[112:115], v[212:215]\n"
"v_fmac_f32_e32 v161, v255, v165\n"
"v_fmac_f32_e32 v162, v255, v166\n"
"ds_read_b128 v[228:231], v253 offset:24832\n"
"v_mfma_f32_16x16x32_bf16 v[216:219], v[52:55], v[112:115], v[216:219]\n"
"v_fmac_f32_e32 v163, v255, v167\n"
"v_fmac_f32_e32 v168, v255, v152\n"
"v_mfma_f32_16x16x32_bf16 v[92:95], v[56:59], v[112:115], v[92:95]\n"
"buffer_load_dwordx4 v[164:167], v1, s[4:7], s99 offen offset:2048 sc1\n"
"v_mfma_f32_16x16x32_bf16 v[220:223], v[60:63], v[112:115], v[220:223]\n"
"v_fmac_f32_e32 v169, v255, v153\n"
"v_fmac_f32_e32 v170, v255, v154\n"
"v_mfma_f32_16x16x32_bf16 v[100:103], v[64:67], v[88:91], v[100:103]\n"
"v_fmac_f32_e32 v171, v255, v155\n"
"v_mul_f32_e32 v255, 0x3e8293ee, v254\n"
"v_mfma_f32_16x16x32_bf16 v[208:211], v[68:71], v[88:91], v[208:211]\n"
"s_waitcnt lgkmcnt(0)\n"
"v_mfma_f32_16x16x32_bf16 v[120:123], v[224:227], v[140:143], v[120:123]\n"
"v_mfma_f32_16x16x32_bf16 v[156:159], v[228:231], v[140:143], v[156:159]\n"
"v_xor_b32_e32 v253, 32, v16\n"
"ds_read_b64_tr_b16 v[112:113], v16 offset:24576\n"
"v_mfma_f32_16x16x32_bf16 v[212:215], v[72:75], v[88:91], v[212:215]\n"
"ds_read_b64_tr_b16 v[114:115], v253 offset:24832\n"
"ds_read_b64_tr_b16 v[140:141], v253 offset:24576\n"
"ds_read_b64_tr_b16 v[142:143], v16 offset:24832\n"
"v_mfma_f32_16x16x32_bf16 v[216:219], v[76:79], v[88:91], v[216:219]\n"
"v_mov_b32_e32 v152, 0x3f803f80\n"
"v_mov_b32_e32 v153, 0x3f803f80\n"
"v_mov_b32_e32 v154, 0x3f803f80\n"
"v_mfma_f32_16x16x32_bf16 v[92:95], v[80:83], v[88:91], v[92:95]\n"
"buffer_load_dwordx4 v[224:227], v1, s[4:7], s99 offen offset:3072 sc1\n"
"v_mov_b32_e32 v155, 0x3f803f80\n"
"v_mfma_f32_16x16x32_bf16 v[220:223], v[84:87], v[88:91], v[220:223]\n"
"s_waitcnt vmcnt(20)\n"
"v_max_f32_e32 v253, v120, v121\n"
"v_max3_f32 v253, v253, v122, v123\n"
"; @B G1 2.3\n"
"v_mfma_f32_16x16x32_bf16 v[148:151], a[0:3], v[108:111], v[148:151]\n"
"v_max3_f32 v253, v253, v156, v157\n"
"v_max3_f32 v253, v253, v158, v159\n"
"v_mov_b32_e32 v252, v253\n"
"v_mfma_f32_16x16x32_bf16 v[188:191], a[4:7], v[108:111], v[188:191]\n"
"s_nop 0\n"
"v_permlane16_swap_b32_e32 v253, v252\n"
"v_max_f32_e32 v253, v253, v252\n"
"v_mov_b32_e32 v252, v253\n"
"v_mfma_f32_16x16x32_bf16 v[196:199], a[8:11], v[108:111], v[196:199]\n"
"s_nop 0\n"
"v_permlane32_swap_b32_e32 v253, v252\n"
"v_max_f32_e32 v253, v253, v252\n"
"v_sub_f32_e32 v120, v120, v253\n"
"v_mfma_f32_16x16x32_bf16 v[144:147], a[12:15], v[108:111], v[144:147]\n"
"ds_write_b128 v3, v[100:103] offset:12288\n"
"v_mfma_f32_16x16x32_bf16 v[160:163], a[16:19], v[108:111], v[160:163]\n"
"s_add_u32 s99, s53, 0x400000\n"
"v_sub_f32_e32 v121, v121, v253\n"
"v_sub_f32_e32 v122, v122, v253\n"
"v_mfma_f32_16x16x32_bf16 v[168:171], a[20:23], v[108:111], v[168:171]\n"
"buffer_load_dwordx4 v[88:91], v0, s[4:7], s99 offen sc1\n"
"v_sub_f32_e32 v123, v123, v253\n"
"v_mfma_f32_16x16x32_bf16 v[148:151], a[24:27], v[172:175], v[148:151]\n"
"ds_write_b128 v3, v[208:211] offset:13312\n"
"v_mfma_f32_16x16x32_bf16 v[188:191], a[28:31], v[172:175], v[188:191]\n"
"v_sub_f32_e32 v156, v156, v253\n"
"v_sub_f32_e32 v157, v157, v253\n"
"v_sub_f32_e32 v158, v158, v253\n"
"v_mfma_f32_16x16x32_bf16 v[196:199], a[32:35], v[172:175], v[196:199]\n"
"v_sub_f32_e32 v159, v159, v253\n"
"v_exp_f32_e32 v120, v120\n"
"v_mfma_f32_16x16x32_bf16 v[144:147], a[36:39], v[172:175], v[144:147]\n"
"ds_write_b128 v3, v[212:215] offset:14336\n"
"v_mfma_f32_16x16x32_bf16 v[160:163], a[40:43], v[172:175], v[160:163]\n"
"buffer_load_dwordx4 v[100:103], v0, s[4:7], s99 offen offset:1024 sc1\n"
"v_mfma_f32_16x16x32_bf16 v[168:171], a[44:47], v[172:175], v[168:171]\n"
"s_waitcnt vmcnt(20)\n"
"v_exp_f32_e32 v121, v121\n"
"v_mfma_f32_16x16x32_bf16 v[148:151], a[48:51], v[176:179], v[148:151]\n"
"ds_write_b128 v3, v[216:219] offset:15360\n"
"v_mfma_f32_16x16x32_bf16 v[188:191], a[52:55], v[176:179], v[188:191]\n"
"buffer_load_dwordx4 a[208:211], v1, s[4:7], s98 offen sc1\n"
"v_mfma_f32_16x16x32_bf16 v[196:199], a[56:59], v[176:179], v[196:199]\n"
"v_exp_f32_e32 v122, v122\n"
"v_mfma_f32_16x16x32_bf16 v[144:147], a[60:63], v[176:179], v[144:147]\n"
"ds_write_b128 v3, v[92:95] offset:16384\n"
"v_mfma_f32_16x16x32_bf16 v[160:163], a[64:67], v[176:179], v[160:163]\n"
"buffer_load_dwordx4 v[92:95], v0, s[4:7], s99 offen offset:2048 sc1\n"
"v_mfma_f32_16x16x32_bf16 v[168:171], a[68:71], v[176:179], v[168:171]\n"
"v_exp_f32_e32 v123, v123\n"
"v_mfma_f32_16x16x32_bf16 v[148:151], a[72:75], v[132:135], v[148:151]\n"
"ds_write_b128 v3, v[220:223] offset:17408\n"
"v_mfma_f32_16x16x32_bf16 v[188:191], a[76:79], v[132:135], v[188:191]\n"
"v_exp_f32_e32 v156, v156\n"
"v_mfma_f32_16x16x32_bf16 v[196:199], a[80:83], v[132:135], v[196:199]\n"
"v_exp_f32_e32 v157, v157\n"
"v_mfma_f32_16x16x32_bf16 v[144:147], a[84:87], v[132:135], v[144:147]\n"
"s_waitcnt lgkmcnt(0)\n"
"s_barrier\n"
"v_mfma_f32_16x16x32_bf16 v[160:163], a[88:91], v[132:135], v[160:163]\n"
"buffer_load_dwordx4 v[108:111], v0, s[4:7], s99 offen offset:3072 sc1\n"
"v_mfma_f32_16x16x32_bf16 v[168:171], a[92:95], v[132:135], v[168:171]\n"
"s_waitcnt vmcnt(19)\n"
"ds_read_b128 v[132:135], v5 offset:2048\n"
"v_mfma_f32_16x16x32_bf16 v[148:151], a[96:99], v[116:119], v[148:151]\n"
"ds_read_b128 v[172:175], v6 offset:2048\n"
"ds_read_b128 v[176:179], v5 offset:3072\n"
"v_mfma_f32_16x16x32_bf16 v[188:191], a[100:103], v[116:119], v[188:191]\n"
"ds_read_b128 v[208:211], v6 offset:3072\n"
"v_exp_f32_e32 v158, v158\n"
"v_mfma_f32_16x16x32_bf16 v[196:199], a[104:107], v[116:119], v[196:199]\n"
"v_exp_f32_e32 v159, v159\n"
"v_cvt_pk_bf16_f32 v212, v120, v121\n"
"v_mfma_f32_16x16x32_bf16 v[144:147], a[108:111], v[116:119], v[144:147]\n"
"v_cvt_pk_bf16_f32 v213, v122, v123\n"
"v_cvt_pk_bf16_f32 v214, v156, v157\n"
"v_cvt_pk_bf16_f32 v215, v158, v159\n"
"v_mfma_f32_16x16x32_bf16 v[160:163], a[112:115], v[116:119], v[160:163]\n"
"buffer_load_dwordx4 v[120:123], v1, s[4:7], s99 offen sc1\n"
"v_mfma_f32_16x16x32_bf16 v[156:159], v[152:155], v[212:215], 0\n"
"v_mfma_f32_16x16x32_bf16 v[152:155], v[112:115], v[212:215], 0\n"
"v_mfma_f32_16x16x32_bf16 v[112:115], v[140:143], v[212:215], 0\n"
"v_mfma_f32_16x16x32_bf16 v[168:171], a[116:119], v[116:119], v[168:171]\n"
"ds_read_b128 v[116:119], v5 offset:4096\n"
"ds_read_b128 v[140:143], v6 offset:4096\n"
"v_mfma_f32_16x16x32_bf16 v[148:151], a[120:123], v[124:127], v[148:151]\n"
"ds_read_b128 v[212:215], v5 offset:5120\n"
"ds_read_b128 v[216:219], v6 offset:5120\n"
"v_mfma_f32_16x16x32_bf16 v[188:191], a[124:127], v[124:127], v[188:191]\n"
"s_waitcnt lgkmcnt(4)\n"
"v_add_f32_e32 v132, v132, v172\n"
"v_mfma_f32_16x16x32_bf16 v[196:199], v[24:27], v[124:127], v[196:199]\n"
"v_add_f32_e32 v133, v133, v173\n"
"v_add_f32_e32 v134, v134, v174\n"
"v_mfma_f32_16x16x32_bf16 v[144:147], v[28:31], v[124:127], v[144:147]\n"
"ds_read_b128 v[220:223], v5\n"
"ds_read_b128 v[228:231], v6\n"
"v_mfma_f32_16x16x32_bf16 v[160:163], v[32:35], v[124:127], v[160:163]\n"
"buffer_load_dwordx4 v[232:235], v1, s[4:7], s99 offen offset:1024 sc1\n"
"v_mfma_f32_16x16x32_bf16 v[168:171], v[36:39], v[124:127], v[168:171]\n"
"s_waitcnt vmcnt(19)\n"
"ds_read_b128 v[124:127], v5 offset:1024\n"
"v_mfma_f32_16x16x32_bf16 v[148:151], v[40:43], v[180:183], v[148:151]\n"
"ds_read_b128 v[236:239], v6 offset:1024\n"
"v_add_f32_e32 v135, v135, v175\n"
"v_mfma_f32_16x16x32_bf16 v[188:191], v[44:47], v[180:183], v[188:191]\n"
"buffer_load_dwordx4 a[212:215], v1, s[4:7], s98 offen offset:1024 sc1\n"
"v_mfma_f32_16x16x32_bf16 v[196:199], v[48:51], v[180:183], v[196:199]\n"
"s_add_u32 s97, s60, 0x840000\n"
"v_add_f32_e32 v176, v176, v208\n"
"v_mfma_f32_16x16x32_bf16 v[144:147], v[52:55], v[180:183], v[144:147]\n"
"v_add_f32_e32 v177, v177, v209\n"
"v_add_f32_e32 v178, v178, v210\n"
"v_mfma_f32_16x16x32_bf16 v[160:163], v[56:59], v[180:183], v[160:163]\n"
"buffer_load_dwordx4 v[172:175], v1, s[4:7], s99 offen offset:2048 sc1\n"
"v_mfma_f32_16x16x32_bf16 v[168:171], v[60:63], v[180:183], v[168:171]\n"
"buffer_store_dwordx4 v[96:99], v0, s[8:11], s97 offen sc1\n"
"v_mfma_f32_16x16x32_bf16 v[148:151], v[64:67], v[204:207], v[148:151]\n"
"s_add_u32 s97, s68, 0x10000\n"
"v_mfma_f32_16x16x32_bf16 v[188:191], v[68:71], v[204:207], v[188:191]\n"
"buffer_store_dwordx2 v[18:19], v11, s[4:7], s97 offen offset:32 sc1\n"
"v_mfma_f32_16x16x32_bf16 v[196:199], v[72:75], v[204:207], v[196:199]\n"
"v_add_f32_e32 v179, v179, v211\n"
"s_waitcnt lgkmcnt(4)\n"
"v_mfma_f32_16x16x32_bf16 v[144:147], v[76:79], v[204:207], v[144:147]\n"
"v_add_f32_e32 v116, v116, v140\n"
"v_add_f32_e32 v117, v117, v141\n"
"v_mfma_f32_16x16x32_bf16 v[160:163], v[80:83], v[204:207], v[160:163]\n"
"buffer_load_dwordx4 v[96:99], v1, s[4:7], s99 offen offset:3072 sc1\n"
"v_mfma_f32_16x16x32_bf16 v[168:171], v[84:87], v[204:207], v[168:171]\n"
"v_add_f32_e32 v118, v118, v142\n"
"v_add_f32_e32 v119, v119, v143\n"
"v_add_f32_e32 v212, v212, v216\n"
"v_add_f32_e32 v213, v213, v217\n"
"v_add_f32_e32 v214, v214, v218\n"
"v_add_f32_e32 v215, v215, v219\n"
"s_waitcnt lgkmcnt(0)\n"
"v_add_f32_e32 v220, v220, v228\n"
"v_add_f32_e32 v221, v221, v229\n"
"v_add_f32_e32 v222, v222, v230\n"
"v_add_f32_e32 v223, v223, v231\n"
"v_add_f32_e32 v124, v124, v236\n"
"v_add_f32_e32 v125, v125, v237\n"
"v_add_f32_e32 v126, v126, v238\n"
"v_add_f32_e32 v127, v127, v239\n"
"v_rcp_f32_e32 v156, v156\n"
"s_nop 0\n"
"v_mul_f32_e32 v152, v152, v156\n"
"v_mul_f32_e32 v153, v153, v156\n"
"v_mul_f32_e32 v154, v154, v156\n"
"v_mul_f32_e32 v155, v155, v156\n"
"v_mul_f32_e32 v112, v112, v156\n"
"v_mul_f32_e32 v113, v113, v156\n"
"v_mul_f32_e32 v114, v114, v156\n"
"v_mul_f32_e32 v115, v115, v156\n"
"v_cvt_pk_bf16_f32 v140, v152, v153\n"
"v_cvt_pk_bf16_f32 v141, v154, v155\n"
"v_cvt_pk_bf16_f32 v142, v112, v113\n"
"v_cvt_pk_bf16_f32 v143, v114, v115\n"
"s_add_u32 s99, s60, 0x448000\n"
"buffer_store_dwordx4 v[140:143], v0, s[4:7], s99 offen sc1\n"
"s_waitcnt vmcnt(22)\n"
"; @B G1 3.0\n"
"v_mfma_f32_16x16x32_bf16 v[112:115], a[0:3], v[104:107], 0\n"
"s_add_u32 s99, s62, 0x40000\n"
"v_mfma_f32_16x16x32_bf16 v[140:143], a[4:7], v[104:107], 0\n"
"buffer_load_dwordx2 v[252:253], v10, s[4:7], s99 offen sc1\n"
"v_mfma_f32_16x16x32_bf16 v[152:155], a[8:11], v[104:107], 0\n"
"buffer_load_dwordx2 v[250:251], v10, s[4:7], s99 offen offset:128 sc1\n"
"v_mfma_f32_16x16x32_bf16 v[156:159], a[12:15], v[104:107], 0\n"
"buffer_load_dwordx2 v[248:249], v10, s[4:7], s99 offen offset:256 sc1\n"
"v_mfma_f32_16x16x32_bf16 v[180:183], a[16:19], v[104:107], 0\n"
"s_add_u32 s97, s54, 0x400000\n"
"v_add_f32_e32 v196, v196, v132\n"
"v_mfma_f32_16x16x32_bf16 v[204:207], a[20:23], v[104:107], 0\n"
"buffer_load_dwordx4 v[104:107], v0, s[4:7], s97 offen sc1\n"
"v_mfma_f32_16x16x32_bf16 v[112:115], a[24:27], v[128:131], v[112:115]\n"
"buffer_load_dwordx2 v[246:247], v10, s[4:7], s99 offen offset:384 sc1\n"
"v_mfma_f32_16x16x32_bf16 v[140:143], a[28:31], v[128:131], v[140:143]\n"
"v_add_f32_e32 v197, v197, v133\n"
"v_add_f32_e32 v198, v198, v134\n"
"v_mfma_f32_16x16x32_bf16 v[152:155], a[32:35], v[128:131], v[152:155]\n"
"buffer_load_dwordx4 a[216:219], v1, s[4:7], s98 offen offset:2048 sc1\n"
"v_mfma_f32_16x16x32_bf16 v[156:159], a[36:39], v[128:131], v[156:159]\n"
"v_add_f32_e32 v199, v199, v135\n"
"v_add_f32_e32 v144, v144, v176\n"
"v_mfma_f32_16x16x32_bf16 v[180:183], a[40:43], v[128:131], v[180:183]\n"
"buffer_load_dwordx4 v[132:135], v0, s[4:7], s97 offen offset:1024 sc1\n"
"v_mfma_f32_16x16x32_bf16 v[204:207], a[44:47], v[128:131], v[204:207]\n"
"s_waitcnt vmcnt(27)\n"
"v_add_f32_e32 v145, v145, v177\n"
"v_mfma_f32_16x16x32_bf16 v[112:115], a[48:51], v[136:139], v[112:115]\n"
"v_add_f32_e32 v146, v146, v178\n"
"v_add_f32_e32 v147, v147, v179\n"
"v_mfma_f32_16x16x32_bf16 v[140:143], a[52:55], v[136:139], v[140:143]\n"
"v_mul_f32_e32 v196, v196, v254\n"
"v_mul_f32_e32 v197, v197, v254\n"
"v_mfma_f32_16x16x32_bf16 v[152:155], a[56:59], v[136:139], v[152:155]\n"
"v_mul_f32_e32 v198, v198, v254\n"
"v_mul_f32_e32 v199, v199, v254\n"
"v_mfma_f32_16x16x32_bf16 v[156:159], a[60:63], v[136:139], v[156:159]\n"
"v_mul_f32_e32 v144, v144, v254\n"
"v_mul_f32_e32 v145, v145, v254\n"
"v_mfma_f32_16x16x32_bf16 v[180:183], a[64:67], v[136:139], v[180:183]\n"
"buffer_load_dwordx4 v[128:131], v0, s[4:7], s97 offen offset:2048 sc1\n"
"v_mfma_f32_16x16x32_bf16 v[204:207], a[68:71], v[136:139], v[204:207]\n"
"v_mul_f32_e32 v146, v146, v254\n"
"v_mul_f32_e32 v147, v147, v254\n"
"v_mfma_f32_16x16x32_bf16 v[112:115], a[72:75], v[184:187], v[112:115]\n"
"v_cvt_pk_bf16_f32 v136, v196, v197\n"
"v_cvt_pk_bf16_f32 v137, v198, v199\n"
"v_mfma_f32_16x16x32_bf16 v[140:143], a[76:79], v[184:187], v[140:143]\n"
"v_cvt_pk_bf16_f32 v138, v144, v145\n"
"v_cvt_pk_bf16_f32 v139, v146, v147\n"
"v_mfma_f32_16x16x32_bf16 v[152:155], a[80:83], v[184:187], v[152:155]\n"
"ds_write_b128 v8, v[136:139] offset:17408\n"
"v_mfma_f32_16x16x32_bf16 v[156:159], a[84:87], v[184:187], v[156:159]\n"
"v_add_f32_e32 v160, v160, v116\n"
"v_add_f32_e32 v161, v161, v117\n"
"v_mfma_f32_16x16x32_bf16 v[180:183], a[88:91], v[184:187], v[180:183]\n"
"buffer_load_dwordx4 v[136:139], v0, s[4:7], s97 offen offset:3072 sc1\n"
"v_mfma_f32_16x16x32_bf16 v[204:207], a[92:95], v[184:187], v[204:207]\n"
"s_waitcnt vmcnt(25)\n"
"v_add_f32_e32 v162, v162, v118\n"
"v_mfma_f32_16x16x32_bf16 v[112:115], a[96:99], v[192:195], v[112:115]\n"
"v_add_f32_e32 v163, v163, v119\n"
"v_add_f32_e32 v168, v168, v212\n"
"v_mfma_f32_16x16x32_bf16 v[140:143], a[100:103], v[192:195], v[140:143]\n"
"v_add_f32_e32 v169, v169, v213\n"
"v_add_f32_e32 v170, v170, v214\n"
"v_mfma_f32_16x16x32_bf16 v[152:155], a[104:107], v[192:195], v[152:155]\n"
"v_add_f32_e32 v171, v171, v215\n"
"v_mul_f32_e32 v160, v160, v254\n"
"v_mfma_f32_16x16x32_bf16 v[156:159], a[108:111], v[192:195], v[156:159]\n"
"v_mul_f32_e32 v161, v161, v254\n"
"v_mul_f32_e32 v162, v162, v254\n"
"v_mfma_f32_16x16x32_bf16 v[180:183], a[112:115], v[192:195], v[180:183]\n"
"buffer_load_dwordx4 v[116:119], v1, s[4:7], s97 offen sc1\n"
"v_mfma_f32_16x16x32_bf16 v[204:207], a[116:119], v[192:195], v[204:207]\n"
"v_mul_f32_e32 v163, v163, v254\n"
"v_mul_f32_e32 v168, v168, v254\n"
"v_mfma_f32_16x16x32_bf16 v[112:115], a[120:123], v[200:203], v[112:115]\n"
"v_mul_f32_e32 v169, v169, v254\n"
"v_mul_f32_e32 v170, v170, v254\n"
"v_mfma_f32_16x16x32_bf16 v[140:143], a[124:127], v[200:203], v[140:143]\n"
"v_mul_f32_e32 v171, v171, v254\n"
"v_cvt_pk_bf16_f32 v144, v160, v161\n"
"v_mfma_f32_16x16x32_bf16 v[152:155], v[24:27], v[200:203], v[152:155]\n"
"buffer_load_dwordx4 a[220:223], v1, s[4:7], s98 offen offset:3072 sc1\n"
"v_mfma_f32_16x16x32_bf16 v[156:159], v[28:31], v[200:203], v[156:159]\n"
"v_cvt_pk_bf16_f32 v145, v162, v163\n"
"v_cvt_pk_bf16_f32 v146, v168, v169\n"
"v_mfma_f32_16x16x32_bf16 v[180:183], v[32:35], v[200:203], v[180:183]\n"
"buffer_load_dwordx4 v[160:163], v1, s[4:7], s97 offen offset:1024 sc1\n"
"v_mfma_f32_16x16x32_bf16 v[204:207], v[36:39], v[200:203], v[204:207]\n"
"s_waitcnt vmcnt(25)\n"
"v_cvt_pk_bf16_f32 v147, v170, v171\n"
"v_mfma_f32_16x16x32_bf16 v[112:115], v[40:43], v[164:167], v[112:115]\n"
"ds_write_b128 v7, v[144:147] offset:50176\n"
"v_mfma_f32_16x16x32_bf16 v[140:143], v[44:47], v[164:167], v[140:143]\n"
"v_add_f32_e32 v148, v148, v220\n"
"v_add_f32_e32 v149, v149, v221\n"
"v_mfma_f32_16x16x32_bf16 v[152:155], v[48:51], v[164:167], v[152:155]\n"
"v_add_f32_e32 v150, v150, v222\n"
"v_add_f32_e32 v151, v151, v223\n"
"v_mfma_f32_16x16x32_bf16 v[156:159], v[52:55], v[164:167], v[156:159]\n"
"v_add_f32_e32 v188, v188, v124\n"
"v_add_f32_e32 v189, v189, v125\n"
"v_mfma_f32_16x16x32_bf16 v[180:183], v[56:59], v[164:167], v[180:183]\n"
"buffer_load_dwordx4 v[144:147], v1, s[4:7], s97 offen offset:2048 sc1\n"
"v_mfma_f32_16x16x32_bf16 v[204:207], v[60:63], v[164:167], v[204:207]\n"
"v_add_f32_e32 v190, v190, v126\n"
"v_add_f32_e32 v191, v191, v127\n"
"v_mfma_f32_16x16x32_bf16 v[112:115], v[64:67], v[224:227], v[112:115]\n"
"v_mul_f32_e32 v148, v148, v255\n"
"v_mul_f32_e32 v149, v149, v255\n"
"v_mfma_f32_16x16x32_bf16 v[140:143], v[68:71], v[224:227], v[140:143]\n"
"v_mul_f32_e32 v150, v150, v255\n"
"v_mul_f32_e32 v151, v151, v255\n"
"v_mfma_f32_16x16x32_bf16 v[152:155], v[72:75], v[224:227], v[152:155]\n"
"v_mul_f32_e32 v188, v188, v255\n"
"v_mul_f32_e32 v189, v189, v255\n"
"v_mfma_f32_16x16x32_bf16 v[156:159], v[76:79], v[224:227], v[156:159]\n"
"v_mul_f32_e32 v190, v190, v255\n"
"v_mul_f32_e32 v191, v191, v255\n"
"v_mfma_f32_16x16x32_bf16 v[180:183], v[80:83], v[224:227], v[180:183]\n"
"buffer_load_dwordx4 v[124:127], v1, s[4:7], s97 offen offset:3072 sc1\n"
"v_mfma_f32_16x16x32_bf16 v[204:207], v[84:87], v[224:227], v[204:207]\n"
"s_waitcnt vmcnt(25)\n"
"v_cvt_pk_bf16_f32 v164, v148, v149\n"
"; @B G1 3.1\n"
"v_mfma_f32_16x16x32_bf16 v[168:171], a[0:3], v[88:91], 0\n"
"v_cvt_pk_bf16_f32 v165, v150, v151\n"
"v_cvt_pk_bf16_f32 v166, v188, v189\n"
"v_mfma_f32_16x16x32_bf16 v[148:151], a[4:7], v[88:91], 0\n"
"v_cvt_pk_bf16_f32 v167, v190, v191\n"
"v_mfma_f32_16x16x32_bf16 v[176:179], a[8:11], v[88:91], 0\n"
"ds_write_b128 v2, v[164:167] offset:17408\n"
"v_mfma_f32_16x16x32_bf16 v[164:167], a[12:15], v[88:91], 0\n"
"ds_write_b128 v3, v[112:115]\n"
"v_mfma_f32_16x16x32_bf16 v[112:115], a[16:19], v[88:91], 0\n"
"s_add_u32 s99, s55, 0x400000\n"
"v_mfma_f32_16x16x32_bf16 v[184:187], a[20:23], v[88:91], 0\n"
"buffer_load_dwordx4 v[88:91], v0, s[4:7], s99 offen sc1\n"
"v_mfma_f32_16x16x32_bf16 v[168:171], a[24:27], v[100:103], v[168:171]\n"
"ds_write_b128 v3, v[140:143] offset:1024\n"
"v_mfma_f32_16x16x32_bf16 v[148:151], a[28:31], v[100:103], v[148:151]\n"
"s_add_u32 s98, s70, 0x206000\n"
"v_mfma_f32_16x16x32_bf16 v[176:179], a[32:35], v[100:103], v[176:179]\n"
"buffer_load_dwordx4 a[224:227], v0, s[4:7], s98 offen sc1\n"
"v_mfma_f32_16x16x32_bf16 v[164:167], a[36:39], v[100:103], v[164:167]\n"
"ds_write_b128 v3, v[152:155] offset:2048\n"
"v_mfma_f32_16x16x32_bf16 v[112:115], a[40:43], v[100:103], v[112:115]\n"
"buffer_load_dwordx4 v[140:143], v0, s[4:7], s99 offen offset:1024 sc1\n"
"v_mfma_f32_16x16x32_bf16 v[184:187], a[44:47], v[100:103], v[184:187]\n"
"s_waitcnt vmcnt(25)\n"
"v_mfma_f32_16x16x32_bf16 v[168:171], a[48:51], v[92:95], v[168:171]\n"
"ds_write_b128 v3, v[156:159] offset:3072\n"
"v_mfma_f32_16x16x32_bf16 v[148:151], a[52:55], v[92:95], v[148:151]\n"
"s_add_u32 s97, s73, 0\n"
"v_mfma_f32_16x16x32_bf16 v[176:179], a[56:59], v[92:95], v[176:179]\n"
"buffer_load_dwordx2 v[254:255], off, s[4:7], s97 sc1\n"
"v_mfma_f32_16x16x32_bf16 v[164:167], a[60:63], v[92:95], v[164:167]\n"
"ds_write_b128 v3, v[180:183] offset:4096\n"
"v_mfma_f32_16x16x32_bf16 v[112:115], a[64:67], v[92:95], v[112:115]\n"
"buffer_load_dwordx4 v[100:103], v0, s[4:7], s99 offen offset:2048 sc1\n"
"v_mfma_f32_16x16x32_bf16 v[184:187], a[68:71], v[92:95], v[184:187]\n"
"v_mfma_f32_16x16x32_bf16 v[168:171], a[72:75], v[108:111], v[168:171]\n"
"ds_write_b128 v3, v[204:207] offset:5120\n"
"v_mfma_f32_16x16x32_bf16 v[148:151], a[76:79], v[108:111], v[148:151]\n"
"v_mfma_f32_16x16x32_bf16 v[176:179], a[80:83], v[108:111], v[176:179]\n"
"v_mov_b32_e32 v92, -1\n"
"v_mov_b32_e32 v93, -1\n"
"v_mfma_f32_16x16x32_bf16 v[164:167], a[84:87], v[108:111], v[164:167]\n"
"v_mov_b32_e32 v94, -1\n"
"v_mov_b32_e32 v95, -1\n"
"v_mfma_f32_16x16x32_bf16 v[112:115], a[88:91], v[108:111], v[112:115]\n"
"buffer_load_dwordx4 v[152:155], v0, s[4:7], s99 offen offset:3072 sc1\n"
"v_mfma_f32_16x16x32_bf16 v[184:187], a[92:95], v[108:111], v[184:187]\n"
"s_waitcnt vmcnt(26)\n"
"s_add_u32 s96, s60, 0x800000\n"
"v_mfma_f32_16x16x32_bf16 v[168:171], a[96:99], v[120:123], v[168:171]\n"
"buffer_store_dwordx4 v[92:95], v0, s[8:11], s96 offen sc1\n"
"v_mfma_f32_16x16x32_bf16 v[148:151], a[100:103], v[120:123], v[148:151]\n"
"v_mfma_f32_16x16x32_bf16 v[176:179], a[104:107], v[120:123], v[176:179]\n"
"v_mfma_f32_16x16x32_bf16 v[164:167], a[108:111], v[120:123], v[164:167]\n"
"v_mfma_f32_16x16x32_bf16 v[112:115], a[112:115], v[120:123], v[112:115]\n"
"buffer_load_dwordx4 v[108:111], v1, s[4:7], s99 offen sc1\n"
"v_mfma_f32_16x16x32_bf16 v[184:187], a[116:119], v[120:123], v[184:187]\n"
"s_waitcnt lgkmcnt(0)\n"
"s_barrier\n"
"v_mfma_f32_16x16x32_bf16 v[168:171], a[120:123], v[232:235], v[168:171]\n"
"s_waitcnt vmcnt(17)\n"
"v_add_f32_e32 v252, v252, v250\n"
"v_mfma_f32_16x16x32_bf16 v[148:151], a[124:127], v[232:235], v[148:151]\n"
"v_add_f32_e32 v253, v253, v251\n"
"v_add_f32_e32 v248, v248, v246\n"
"v_mfma_f32_16x16x32_bf16 v[176:179], v[24:27], v[232:235], v[176:179]\n"
"buffer_load_dwordx4 a[228:231], v0, s[4:7], s98 offen offset:1024 sc1\n"
"v_mfma_f32_16x16x32_bf16 v[164:167], v[28:31], v[232:235], v[164:167]\n"
"v_add_f32_e32 v249, v249, v247\n"
"v_add_f32_e32 v252, v252, v248\n"
"v_mfma_f32_16x16x32_bf16 v[112:115], v[32:35], v[232:235], v[112:115]\n"
"buffer_load_dwordx4 v[120:123], v1, s[4:7], s99 offen offset:1024 sc1\n"
"v_mfma_f32_16x16x32_bf16 v[184:187], v[36:39], v[232:235], v[184:187]\n"
"v_add_f32_e32 v253, v253, v249\n"
"v_mfma_f32_16x16x32_bf16 v[168:171], v[40:43], v[172:175], v[168:171]\n"
"s_nop 0\n"
"v_permlane16_swap_b32_e32 v252, v253\n"
"v_add_f32_e32 v252, v252, v253\n"
"v_mfma_f32_16x16x32_bf16 v[148:151], v[44:47], v[172:175], v[148:151]\n"
"v_mov_b32_e32 v253, v252\n"
"v_add_u32_e32 v251, 0x26c00, v0\n"
"v_mfma_f32_16x16x32_bf16 v[176:179], v[48:51], v[172:175], v[176:179]\n"
"v_permlane32_swap_b32_e32 v252, v253\n"
"v_add_f32_e32 v252, v252, v253\n"
"v_mfma_f32_16x16x32_bf16 v[164:167], v[52:55], v[172:175], v[164:167]\n"
"v_mul_f32_e32 v252, 0x3a800000, v252\n"
"v_mov_b32_e32 v253, v252\n"
"v_mfma_f32_16x16x32_bf16 v[112:115], v[56:59], v[172:175], v[112:115]\n"
"buffer_load_dwordx4 v[156:159], v1, s[4:7], s99 offen offset:2048 sc1\n"
"v_mfma_f32_16x16x32_bf16 v[184:187], v[60:63], v[172:175], v[184:187]\n"
"v_permlane16_swap_b32_e32 v252, v253\n"
"v_cvt_pk_bf16_f32 v250, -v252, 0\n"
"v_mfma_f32_16x16x32_bf16 v[168:171], v[64:67], v[96:99], v[168:171]\n"
"v_fma_f32 v253, -v252, v252, v253\n"
"v_lshlrev_b32_e32 v250, 16, v250\n"
"v_mfma_f32_16x16x32_bf16 v[148:151], v[68:71], v[96:99], v[148:151]\n"
"ds_read_b128 v[172:175], v4\n"
"ds_read_b128 v[180:183], v9 offset:512\n"
"v_mfma_f32_16x16x32_bf16 v[176:179], v[72:75], v[96:99], v[176:179]\n"
"ds_read_b128 v[188:191], v4 offset:1024\n"
"ds_read_b128 v[192:195], v9 offset:576\n"
"v_mfma_f32_16x16x32_bf16 v[164:167], v[76:79], v[96:99], v[164:167]\n"
"ds_read_b128 v[196:199], v4 offset:2048\n"
"ds_read_b128 v[200:203], v9 offset:640\n"
"v_mfma_f32_16x16x32_bf16 v[112:115], v[80:83], v[96:99], v[112:115]\n"
"buffer_load_dwordx4 v[204:207], v1, s[4:7], s99 offen offset:3072 sc1\n"
"v_mfma_f32_16x16x32_bf16 v[184:187], v[84:87], v[96:99], v[184:187]\n"
"s_waitcnt vmcnt(19)\n"
"ds_read_b128 v[96:99], v251\n"
"; @B G1 3.2\n"
"v_mfma_f32_16x16x32_bf16 v[208:211], a[0:3], v[104:107], 0\n"
"ds_read_b128 v[212:215], v251 offset:1024\n"
"v_xor_b32_e32 v251, 32, v17\n"
"v_mfma_f32_16x16x32_bf16 v[216:219], a[4:7], v[104:107], 0\n"
"ds_read_b128 v[220:223], v2 offset:49152\n"
"ds_read_b128 v[224:227], v17 offset:23552\n"
"v_mfma_f32_16x16x32_bf16 v[228:231], a[8:11], v[104:107], 0\n"
"s_add_u32 s99, s60, 0x808000\n"
"s_waitcnt lgkmcnt(4)\n"
"v_mfma_f32_16x16x32_bf16 v[232:235], a[12:15], v[104:107], 0\n"
"ds_write_b128 v3, v[168:171] offset:6144\n"
"v_mfma_f32_16x16x32_bf16 v[168:171], a[16:19], v[104:107], 0\n"
"buffer_store_dwordx4 v[92:95], v0, s[8:11], s99 offen sc1\n"
"v_mfma_f32_16x16x32_bf16 v[236:239], a[20:23], v[104:107], 0\n"
"buffer_load_dwordx4 a[232:235], v0, s[4:7], s98 offen offset:2048 sc1\n"
"v_mfma_f32_16x16x32_bf16 v[208:211], a[24:27], v[132:135], v[208:211]\n"
"ds_write_b128 v3, v[148:151] offset:7168\n"
"v_mfma_f32_16x16x32_bf16 v[216:219], a[28:31], v[132:135], v[216:219]\n"
"v_fmac_f32_e32 v172, v250, v180\n"
"v_fmac_f32_e32 v173, v250, v181\n"
"v_mfma_f32_16x16x32_bf16 v[228:231], a[32:35], v[132:135], v[228:231]\n"
"v_fmac_f32_e32 v174, v250, v182\n"
"v_fmac_f32_e32 v175, v250, v183\n"
"v_mfma_f32_16x16x32_bf16 v[232:235], a[36:39], v[132:135], v[232:235]\n"
"ds_write_b128 v3, v[176:179] offset:8192\n"
"v_mfma_f32_16x16x32_bf16 v[168:171], a[40:43], v[132:135], v[168:171]\n"
"v_fmac_f32_e32 v188, v250, v192\n"
"v_fmac_f32_e32 v189, v250, v193\n"
"v_mfma_f32_16x16x32_bf16 v[236:239], a[44:47], v[132:135], v[236:239]\n"
"s_waitcnt vmcnt(19)\n"
"v_fmac_f32_e32 v190, v250, v194\n"
"v_mfma_f32_16x16x32_bf16 v[208:211], a[48:51], v[128:131], v[208:211]\n"
"ds_write_b128 v3, v[164:167] offset:9216\n"
"v_mfma_f32_16x16x32_bf16 v[216:219], a[52:55], v[128:131], v[216:219]\n"
"buffer_load_dwordx4 a[236:239], v0, s[4:7], s98 offen offset:3072 sc1\n"
"v_mfma_f32_16x16x32_bf16 v[228:231], a[56:59], v[128:131], v[228:231]\n"
"v_fmac_f32_e32 v191, v250, v195\n"
"v_fmac_f32_e32 v196, v250, v200\n"
"v_mfma_f32_16x16x32_bf16 v[232:235], a[60:63], v[128:131], v[232:235]\n"
"ds_write_b128 v3, v[112:115] offset:10240\n"
"v_mfma_f32_16x16x32_bf16 v[168:171], a[64:67], v[128:131], v[168:171]\n"
"v_fmac_f32_e32 v197, v250, v201\n"
"v_fmac_f32_e32 v198, v250, v202\n"
"v_mfma_f32_16x16x32_bf16 v[236:239], a[68:71], v[128:131], v[236:239]\n"
"v_fmac_f32_e32 v199, v250, v203\n"
"ds_read_b128 v[104:107], v4 offset:3072\n"
"v_mfma_f32_16x16x32_bf16 v[208:211], a[72:75], v[136:139], v[208:211]\n"
"ds_write_b128 v3, v[184:187] offset:11264\n"
"v_mfma_f32_16x16x32_bf16 v[216:219], a[76:79], v[136:139], v[216:219]\n"
"buffer_load_dwordx4 a[240:243], v1, s[4:7], s98 offen sc1\n"
"v_mfma_f32_16x16x32_bf16 v[228:231], a[80:83], v[136:139], v[228:231]\n"
"ds_read_b128 v[112:115], v9 offset:704\n"
"ds_read_b128 v[128:131], v4 offset:4096\n"
"v_mfma_f32_16x16x32_bf16 v[232:235], a[84:87], v[136:139], v[232:235]\n"
"ds_read_b128 v[132:135], v9 offset:768\n"
"ds_read_b128 v[148:151], v4 offset:5120\n"
"v_mfma_f32_16x16x32_bf16 v[168:171], a[88:91], v[136:139], v[168:171]\n"
"s_waitcnt lgkmcnt(13)\n"
"ds_read_b128 v[164:167], v9 offset:832\n"
"ds_read_b128 v[176:179], v251 offset:23808\n"
"v_mfma_f32_16x16x32_bf16 v[236:239], a[92:95], v[136:139], v[236:239]\n"
"s_waitcnt vmcnt(18)\n"
"v_max_f32_e32 v253, 0, v253\n"
"v_mfma_f32_16x16x32_bf16 v[208:211], a[96:99], v[116:119], v[208:211]\n"
"s_waitcnt vmcnt(12)\n"
"v_cmp_ne_u64_e32 vcc, v[254:255], v[18:19]\n"
"v_mfma_f32_16x16x32_bf16 v[216:219], a[100:103], v[116:119], v[216:219]\n"
"buffer_load_dwordx4 a[244:247], v1, s[4:7], s98 offen offset:1024 sc1\n"
"v_mfma_f32_16x16x32_bf16 v[228:231], a[104:107], v[116:119], v[228:231]\n"
"s_cbranch_vccnz .Lmk_f45s\n"
".Lmk_f45r:\n"
"s_mov_b32 m0, 0x7c00\n"
"v_mfma_f32_16x16x32_bf16 v[232:235], a[108:111], v[116:119], v[232:235]\n"
"s_add_u32 s99, s66, 0x3c00\n"
"s_waitcnt lgkmcnt(1)\n"
"v_mfma_f32_16x16x32_bf16 v[168:171], a[112:115], v[116:119], v[168:171]\n"
"buffer_load_dwordx4 v0, s[4:7], s99 offen sc1 lds\n"
"v_mfma_f32_16x16x32_bf16 v[236:239], a[116:119], v[116:119], v[236:239]\n"
"s_mov_b32 m0, 0xfc00\n"
"s_add_u32 s99, s66, 0x7c00\n"
"v_mfma_f32_16x16x32_bf16 v[208:211], a[120:123], v[160:163], v[208:211]\n"
"s_add_u32 s97, s61, 0xc40000\n"
"v_fmac_f32_e32 v104, v250, v112\n"
"v_mfma_f32_16x16x32_bf16 v[216:219], a[124:127], v[160:163], v[216:219]\n"
"buffer_load_dwordx4 a[248:251], v1, s[4:7], s98 offen offset:2048 sc1\n"
"v_mfma_f32_16x16x32_bf16 v[228:231], v[24:27], v[160:163], v[228:231]\n"
"buffer_store_dwordx4 v[92:95], v0, s[8:11], s97 offen sc1\n"
"v_mfma_f32_16x16x32_bf16 v[232:235], v[28:31], v[160:163], v[232:235]\n"
"buffer_load_dwordx4 v0, s[4:7], s99 offen sc1 lds\n"
"v_mfma_f32_16x16x32_bf16 v[168:171], v[32:35], v[160:163], v[168:171]\n"
"s_add_u32 s99, s67, 0x10020\n"
"v_fmac_f32_e32 v105, v250, v113\n"
"v_mfma_f32_16x16x32_bf16 v[236:239], v[36:39], v[160:163], v[236:239]\n"
"v_fmac_f32_e32 v106, v250, v114\n"
"v_mfma_f32_16x16x32_bf16 v[208:211], v[40:43], v[144:147], v[208:211]\n"
"buffer_load_dwordx2 v[254:255], v12, s[4:7], s99 offen sc1\n"
"v_mfma_f32_16x16x32_bf16 v[216:219], v[44:47], v[144:147], v[216:219]\n"
"buffer_load_dwordx4 a[252:255], v1, s[4:7], s98 offen offset:3072 sc1\n"
"v_mfma_f32_16x16x32_bf16 v[228:231], v[48:51], v[144:147], v[228:231]\n"
"v_fmac_f32_e32 v107, v250, v115\n"
"v_fmac_f32_e32 v128, v250, v132\n"
"v_mfma_f32_16x16x32_bf16 v[232:235], v[52:55], v[144:147], v[232:235]\n"
"v_fmac_f32_e32 v129, v250, v133\n"
"v_fmac_f32_e32 v130, v250, v134\n"
"v_mfma_f32_16x16x32_bf16 v[168:171], v[56:59], v[144:147], v[168:171]\n"
"v_fmac_f32_e32 v131, v250, v135\n"
"v_fmac_f32_e32 v148, v250, v164\n"
"v_mfma_f32_16x16x32_bf16 v[236:239], v[60:63], v[144:147], v[236:239]\n"
"v_fmac_f32_e32 v149, v250, v165\n"
"v_fmac_f32_e32 v150, v250, v166\n"
"v_mfma_f32_16x16x32_bf16 v[208:211], v[64:67], v[124:127], v[208:211]\n"
"v_fmac_f32_e32 v151, v250, v167\n"
"s_waitcnt lgkmcnt(0)\n"
"v_mfma_f32_16x16x32_bf16 v[96:99], v[224:227], v[220:223], v[96:99]\n"
"v_mfma_f32_16x16x32_bf16 v[212:215], v[176:179], v[220:223], v[212:215]\n"
"v_mfma_f32_16x16x32_bf16 v[216:219], v[68:71], v[124:127], v[216:219]\n"
"v_xor_b32_e32 v251, 32, v16\n"
"ds_read_b64_tr_b16 v[112:113], v16 offset:23552\n"
"v_mfma_f32_16x16x32_bf16 v[228:231], v[72:75], v[124:127], v[228:231]\n"
"ds_read_b64_tr_b16 v[114:115], v251 offset:23808\n"
"ds_read_b64_tr_b16 v[116:117], v251 offset:23552\n"
"v_mfma_f32_16x16x32_bf16 v[232:235], v[76:79], v[124:127], v[232:235]\n"
"ds_read_b64_tr_b16 v[118:119], v16 offset:23808\n"
"v_mov_b32_e32 v132, 0x3f803f80\n"
"v_mfma_f32_16x16x32_bf16 v[168:171], v[80:83], v[124:127], v[168:171]\n"
"v_mov_b32_e32 v133, 0x3f803f80\n"
"v_mov_b32_e32 v134, 0x3f803f80\n"
"v_mfma_f32_16x16x32_bf16 v[236:239], v[84:87], v[124:127], v[236:239]\n"
"v_mov_b32_e32 v135, 0x3f803f80\n"
"; @B G1 3.3\n"
"v_mfma_f32_16x16x32_bf16 v[172:175], a[0:3], v[88:91], v[172:175]\n"
"v_max_f32_e32 v251, v96, v97\n"
"v_max3_f32 v251, v251, v98, v99\n"
"v_mfma_f32_16x16x32_bf16 v[188:191], a[4:7], v[88:91], v[188:191]\n"
"v_max3_f32 v251, v251, v212, v213\n"
"v_max3_f32 v251, v251, v214, v215\n"
"v_mfma_f32_16x16x32_bf16 v[196:199], a[8:11], v[88:91], v[196:199]\n"
"v_mov_b32_e32 v250, v251\n"
"v_cvt_pk_bf16_f32 v253, v253, 0\n"
"v_mfma_f32_16x16x32_bf16 v[104:107], a[12:15], v[88:91], v[104:107]\n"
"ds_write_b128 v3, v[208:211] offset:12288\n"
"v_mfma_f32_16x16x32_bf16 v[128:131], a[16:19], v[88:91], v[128:131]\n"
"v_permlane16_swap_b32_e32 v251, v250\n"
"v_max_f32_e32 v251, v251, v250\n"
"v_mfma_f32_16x16x32_bf16 v[148:151], a[20:23], v[88:91], v[148:151]\n"
"v_mov_b32_e32 v250, v251\n"
"v_lshlrev_b32_e32 v253, 16, v253\n"
"v_mfma_f32_16x16x32_bf16 v[172:175], a[24:27], v[140:143], v[172:175]\n"
"ds_write_b128 v3, v[216:219] offset:13312\n"
"v_mfma_f32_16x16x32_bf16 v[188:191], a[28:31], v[140:143], v[188:191]\n"
"v_permlane32_swap_b32_e32 v251, v250\n"
"v_max_f32_e32 v251, v251, v250\n"
"v_mfma_f32_16x16x32_bf16 v[196:199], a[32:35], v[140:143], v[196:199]\n"
"v_sub_f32_e32 v96, v96, v251\n"
"v_sub_f32_e32 v97, v97, v251\n"
"v_mfma_f32_16x16x32_bf16 v[104:107], a[36:39], v[140:143], v[104:107]\n"
"ds_write_b128 v3, v[228:231] offset:14336\n"
"v_mfma_f32_16x16x32_bf16 v[128:131], a[40:43], v[140:143], v[128:131]\n"
"s_add_u32 s98, s61, 0xc40000\n"
"v_sub_f32_e32 v98, v98, v251\n"
"v_mfma_f32_16x16x32_bf16 v[148:151], a[44:47], v[140:143], v[148:151]\n"
"s_waitcnt vmcnt(17)\n"
"v_sub_f32_e32 v99, v99, v251\n"
"v_mfma_f32_16x16x32_bf16 v[172:175], a[48:51], v[100:103], v[172:175]\n"
"ds_write_b128 v3, v[232:235] offset:15360\n"
"v_mfma_f32_16x16x32_bf16 v[188:191], a[52:55], v[100:103], v[188:191]\n"
"buffer_store_dwordx4 v[92:95], v0, s[8:11], s98 offen offset:1024 sc1\n"
"v_mfma_f32_16x16x32_bf16 v[196:199], a[56:59], v[100:103], v[196:199]\n"
"v_sub_f32_e32 v212, v212, v251\n"
"v_sub_f32_e32 v213, v213, v251\n"
"v_mfma_f32_16x16x32_bf16 v[104:107], a[60:63], v[100:103], v[104:107]\n"
"ds_write_b128 v3, v[168:171] offset:16384\n"
"v_mfma_f32_16x16x32_bf16 v[128:131], a[64:67], v[100:103], v[128:131]\n"
"v_sub_f32_e32 v214, v214, v251\n"
"v_sub_f32_e32 v215, v215, v251\n"
"v_mfma_f32_16x16x32_bf16 v[148:151], a[68:71], v[100:103], v[148:151]\n"
"v_exp_f32_e32 v96, v96\n"
"v_mfma_f32_16x16x32_bf16 v[172:175], a[72:75], v[152:155], v[172:175]\n"
"ds_write_b128 v3, v[236:239] offset:17408\n"
"v_mfma_f32_16x16x32_bf16 v[188:191], a[76:79], v[152:155], v[188:191]\n"
"v_exp_f32_e32 v97, v97\n"
"v_mfma_f32_16x16x32_bf16 v[196:199], a[80:83], v[152:155], v[196:199]\n"
"v_exp_f32_e32 v98, v98\n"
"v_mfma_f32_16x16x32_bf16 v[104:107], a[84:87], v[152:155], v[104:107]\n"
"v_exp_f32_e32 v99, v99\n"
"v_mfma_f32_16x16x32_bf16 v[128:131], a[88:91], v[152:155], v[128:131]\n"
"v_exp_f32_e32 v212, v212\n"
"v_mfma_f32_16x16x32_bf16 v[148:151], a[92:95], v[152:155], v[148:151]\n"
"s_waitcnt vmcnt(14)\n"
"v_add_f32_e32 v253, 0x3727c5ac, v253\n"
"v_mfma_f32_16x16x32_bf16 v[172:175], a[96:99], v[108:111], v[172:175]\n"
"v_exp_f32_e32 v213, v213\n"
"v_mfma_f32_16x16x32_bf16 v[188:191], a[100:103], v[108:111], v[188:191]\n"
"v_exp_f32_e32 v214, v214\n"
"v_mfma_f32_16x16x32_bf16 v[196:199], a[104:107], v[108:111], v[196:199]\n"
"v_exp_f32_e32 v215, v215\n"
"v_mfma_f32_16x16x32_bf16 v[104:107], a[108:111], v[108:111], v[104:107]\n"
"v_cvt_pk_bf16_f32 v88, v96, v97\n"
"v_cvt_pk_bf16_f32 v89, v98, v99\n"
"v_mfma_f32_16x16x32_bf16 v[128:131], a[112:115], v[108:111], v[128:131]\n"
"v_cvt_pk_bf16_f32 v90, v212, v213\n"
"v_cvt_pk_bf16_f32 v91, v214, v215\n"
"v_mfma_f32_16x16x32_bf16 v[148:151], a[116:119], v[108:111], v[148:151]\n"
"s_waitcnt lgkmcnt(6)\n"
"v_mfma_f32_16x16x32_bf16 v[96:99], v[132:135], v[88:91], 0\n"
"v_mfma_f32_16x16x32_bf16 v[100:103], v[112:115], v[88:91], 0\n"
"v_mfma_f32_16x16x32_bf16 v[108:111], v[116:119], v[88:91], 0\n"
"v_cvt_pk_bf16_f32 v253, v253, 0\n"
"v_mfma_f32_16x16x32_bf16 v[172:175], a[120:123], v[120:123], v[172:175]\n"
"s_waitcnt lgkmcnt(0)\n"
"s_barrier\n"
"v_mfma_f32_16x16x32_bf16 v[188:191], a[124:127], v[120:123], v[188:191]\n"
"ds_read_b128 v[88:91], v5 offset:2048\n"
"ds_read_b128 v[112:115], v6 offset:2048\n"
"v_mfma_f32_16x16x32_bf16 v[196:199], v[24:27], v[120:123], v[196:199]\n"
"ds_read_b128 v[24:27], v5 offset:3072\n"
"ds_read_b128 v[116:119], v6 offset:3072\n"
"v_mfma_f32_16x16x32_bf16 v[104:107], v[28:31], v[120:123], v[104:107]\n"
"v_lshlrev_b32_e32 v253, 16, v253\n"
"v_add_u32_e32 v251, 0x26c00, v0\n"
"v_mfma_f32_16x16x32_bf16 v[128:131], v[32:35], v[120:123], v[128:131]\n"
"v_rcp_f32_e32 v96, v96\n"
"v_mfma_f32_16x16x32_bf16 v[148:151], v[36:39], v[120:123], v[148:151]\n"
"s_waitcnt vmcnt(12)\n"
"v_mul_f32_e32 v100, v100, v96\n"
"v_mfma_f32_16x16x32_bf16 v[172:175], v[40:43], v[156:159], v[172:175]\n"
"v_mul_f32_e32 v101, v101, v96\n"
"v_mul_f32_e32 v102, v102, v96\n"
"v_mfma_f32_16x16x32_bf16 v[188:191], v[44:47], v[156:159], v[188:191]\n"
"v_mul_f32_e32 v103, v103, v96\n"
"v_mul_f32_e32 v108, v108, v96\n"
"v_mfma_f32_16x16x32_bf16 v[196:199], v[48:51], v[156:159], v[196:199]\n"
"v_mul_f32_e32 v109, v109, v96\n"
"v_mul_f32_e32 v110, v110, v96\n"
"v_mfma_f32_16x16x32_bf16 v[104:107], v[52:55], v[156:159], v[104:107]\n"
"s_add_u32 s98, s63, 0xc4000\n"
"v_mul_f32_e32 v111, v111, v96\n"
"v_mfma_f32_16x16x32_bf16 v[128:131], v[56:59], v[156:159], v[128:131]\n"
"v_cvt_pk_bf16_f32 v28, v100, v101\n"
"v_cvt_pk_bf16_f32 v29, v102, v103\n"
"v_mfma_f32_16x16x32_bf16 v[148:151], v[60:63], v[156:159], v[148:151]\n"
"buffer_store_dwordx2 v[92:93], v14, s[8:11], s98 offen sc1\n"
"v_mfma_f32_16x16x32_bf16 v[172:175], v[64:67], v[204:207], v[172:175]\n"
"v_cvt_pk_bf16_f32 v30, v108, v109\n"
"v_cvt_pk_bf16_f32 v31, v110, v111\n"
"v_mfma_f32_16x16x32_bf16 v[188:191], v[68:71], v[204:207], v[188:191]\n"
"s_add_u32 s98, s60, 0x440000\n"
"ds_read_b128 v[32:35], v5 offset:4096\n"
"v_mfma_f32_16x16x32_bf16 v[196:199], v[72:75], v[204:207], v[196:199]\n"
"buffer_store_dwordx4 v[28:31], v0, s[4:7], s98 offen sc1\n"
"v_mfma_f32_16x16x32_bf16 v[104:107], v[76:79], v[204:207], v[104:107]\n"
"ds_read_b128 v[28:31], v6 offset:4096\n"
"ds_read_b128 v[36:39], v5 offset:5120\n"
"v_mfma_f32_16x16x32_bf16 v[128:131], v[80:83], v[204:207], v[128:131]\n"
"ds_read_b128 v[40:43], v6 offset:5120\n"
"s_waitcnt lgkmcnt(4)\n"
"v_mfma_f32_16x16x32_bf16 v[148:151], v[84:87], v[204:207], v[148:151]\n"
"v_add_f32_e32 v88, v88, v112\n"
"v_add_f32_e32 v89, v89, v113\n"
"v_add_f32_e32 v90, v90, v114\n"
"v_add_f32_e32 v91, v91, v115\n"
"v_add_f32_e32 v24, v24, v116\n"
"v_add_f32_e32 v25, v25, v117\n"
"v_add_f32_e32 v26, v26, v118\n"
"v_add_f32_e32 v27, v27, v119\n"
"v_sqrt_f32_e32 v253, v253\n"
"ds_read_b128 v[44:47], v251\n"
"ds_read_b128 v[48:51], v251 offset:1024\n"
"v_add_u32_e32 v251, 0x26c00, v0\n"
"ds_read_b128 v[52:55], v251\n"
"ds_read_b128 v[56:59], v251 offset:1024\n"
"v_mov_b32_e32 v251, 0xff800000\n"
"v_cvt_pk_bf16_f32 v253, v253, 0\n"
"v_lshlrev_b32_e32 v253, 16, v253\n"
"v_rcp_f32_e32 v250, v253\n"
"s_add_u32 s98, s69, 0\n"
"buffer_load_dwordx4 a[0:3], v0, s[8:11], s98 offen sc1\n"
"ds_read_b128 v[60:63], v5\n"
"ds_read_b128 v[64:67], v6\n"
"ds_read_b128 v[68:71], v5 offset:1024\n"
"ds_read_b128 v[72:75], v6 offset:1024\n"
"v_mul_f32_e32 v253, 0x3e8293ee, v250\n"
"s_waitcnt lgkmcnt(8)\n"
"v_add_f32_e32 v32, v32, v28\n"
"v_add_f32_e32 v33, v33, v29\n"
"v_add_f32_e32 v34, v34, v30\n"
"v_add_f32_e32 v35, v35, v31\n"
"v_add_f32_e32 v36, v36, v40\n"
"v_add_f32_e32 v37, v37, v41\n"
"v_add_f32_e32 v38, v38, v42\n"
"v_add_f32_e32 v39, v39, v43\n"
"s_waitcnt lgkmcnt(4)\n"
"v_cndmask_b32_e64 v52, v52, v251, s[82:83]\n"
"v_cndmask_b32_e64 v53, v53, v251, s[82:83]\n"
"v_cndmask_b32_e64 v54, v54, v251, s[82:83]\n"
"v_cndmask_b32_e64 v55, v55, v251, s[82:83]\n"
"v_cndmask_b32_e64 v56, v56, v251, s[82:83]\n"
"v_cndmask_b32_e64 v57, v57, v251, s[82:83]\n"
"v_cndmask_b32_e64 v58, v58, v251, s[82:83]\n"
"v_cndmask_b32_e64 v59, v59, v251, s[82:83]\n"
"buffer_load_dwordx4 a[4:7], v0, s[8:11], s98 offen offset:1024 sc1\n"
"s_waitcnt lgkmcnt(0)\n"
"v_add_f32_e32 v60, v60, v64\n"
"v_add_f32_e32 v61, v61, v65\n"
"v_add_f32_e32 v62, v62, v66\n"
"v_add_f32_e32 v63, v63, v67\n"
"v_add_f32_e32 v68, v68, v72\n"
"v_add_f32_e32 v69, v69, v73\n"
"v_add_f32_e32 v70, v70, v74\n"
"v_add_f32_e32 v71, v71, v75\n"
"buffer_load_dwordx4 a[8:11], v0, s[8:11], s98 offen offset:2048 sc1\n"
"buffer_load_dwordx4 a[12:15], v0, s[8:11], s98 offen offset:3072 sc1\n"
"buffer_load_dwordx4 a[16:19], v1, s[8:11], s98 offen sc1\n"
"buffer_load_dwordx4 a[20:23], v1, s[8:11], s98 offen offset:1024 sc1\n"
"buffer_load_dwordx4 a[24:27], v1, s[8:11], s98 offen offset:2048 sc1\n"
"buffer_load_dwordx4 a[28:31], v1, s[8:11], s98 offen offset:3072 sc1\n"
"; @B tail\n"
"s_add_u32 s98, s69, 0x2000\n"
"buffer_load_dwordx4 a[32:35], v0, s[8:11], s98 offen sc1\n"
"v_add_f32_e32 v196, v196, v88\n"
"v_add_f32_e32 v197, v197, v89\n"
"v_add_f32_e32 v198, v198, v90\n"
"v_add_f32_e32 v199, v199, v91\n"
"v_add_f32_e32 v104, v104, v24\n"
"v_add_f32_e32 v105, v105, v25\n"
"v_add_f32_e32 v106, v106, v26\n"
"v_add_f32_e32 v107, v107, v27\n"
"v_mul_f32_e32 v196, v196, v250\n"
"v_mul_f32_e32 v197, v197, v250\n"
"v_mul_f32_e32 v198, v198, v250\n"
"v_mul_f32_e32 v199, v199, v250\n"
"v_mul_f32_e32 v104, v104, v250\n"
"buffer_load_dwordx4 a[36:39], v0, s[8:11], s98 offen offset:1024 sc1\n"
"v_mul_f32_e32 v105, v105, v250\n"
"v_mul_f32_e32 v106, v106, v250\n"
"v_mul_f32_e32 v107, v107, v250\n"
"v_cvt_pk_bf16_f32 v24, v196, v197\n"
"v_cvt_pk_bf16_f32 v25, v198, v199\n"
"v_cvt_pk_bf16_f32 v26, v104, v105\n"
"v_cvt_pk_bf16_f32 v27, v106, v107\n"
"ds_write_b128 v7, v[24:27] offset:16384\n"
"buffer_load_dwordx4 a[40:43], v0, s[8:11], s98 offen offset:2048 sc1\n"
"v_add_f32_e32 v128, v128, v32\n"
"v_add_f32_e32 v129, v129, v33\n"
"v_add_f32_e32 v130, v130, v34\n"
"v_add_f32_e32 v131, v131, v35\n"
"v_add_f32_e32 v148, v148, v36\n"
"v_add_f32_e32 v149, v149, v37\n"
"v_add_f32_e32 v150, v150, v38\n"
"v_add_f32_e32 v151, v151, v39\n"
"v_mul_f32_e32 v128, v128, v250\n"
"v_mul_f32_e32 v129, v129, v250\n"
"v_mul_f32_e32 v130, v130, v250\n"
"v_mul_f32_e32 v131, v131, v250\n"
"v_mul_f32_e32 v148, v148, v250\n"
"buffer_load_dwordx4 a[44:47], v0, s[8:11], s98 offen offset:3072 sc1\n"
"v_mul_f32_e32 v149, v149, v250\n"
"v_mul_f32_e32 v150, v150, v250\n"
"v_mul_f32_e32 v151, v151, v250\n"
"v_cvt_pk_bf16_f32 v24, v128, v129\n"
"v_cvt_pk_bf16_f32 v25, v130, v131\n"
"v_cvt_pk_bf16_f32 v26, v148, v149\n"
"v_cvt_pk_bf16_f32 v27, v150, v151\n"
"ds_write_b128 v7, v[24:27] offset:49152\n"
"buffer_load_dwordx4 a[48:51], v1, s[8:11], s98 offen sc1\n"
"v_add_f32_e32 v172, v172, v60\n"
"v_add_f32_e32 v173, v173, v61\n"
"v_add_f32_e32 v174, v174, v62\n"
"v_add_f32_e32 v175, v175, v63\n"
"v_add_f32_e32 v188, v188, v68\n"
"v_add_f32_e32 v189, v189, v69\n"
"v_add_f32_e32 v190, v190, v70\n"
"v_add_f32_e32 v191, v191, v71\n"
"v_mul_f32_e32 v172, v172, v253\n"
"v_mul_f32_e32 v173, v173, v253\n"
"v_mul_f32_e32 v174, v174, v253\n"
"v_mul_f32_e32 v175, v175, v253\n"
"v_mul_f32_e32 v188, v188, v253\n"
"buffer_load_dwordx4 a[52:55], v1, s[8:11], s98 offen offset:1024 sc1\n"
"v_mul_f32_e32 v189, v189, v253\n"
"v_mul_f32_e32 v190, v190, v253\n"
"v_mul_f32_e32 v191, v191, v253\n"
"v_cvt_pk_bf16_f32 v24, v172, v173\n"
"v_cvt_pk_bf16_f32 v25, v174, v175\n"
"v_cvt_pk_bf16_f32 v26, v188, v189\n"
"v_cvt_pk_bf16_f32 v27, v190, v191\n"
"ds_write_b128 v2, v[24:27] offset:16384\n"
"s_waitcnt vmcnt(18)\n"
"v_cmp_ne_u64_e32 vcc, v[254:255], v[18:19]\n"
"s_cbranch_vccnz .Lmk_f46s\n"
".Lmk_f46r:\n"
"s_waitcnt lgkmcnt(0)\n"
"s_barrier\n"
"; @B AT a\n"
"s_cmp_lg_u32 s0, 0\n"
"s_cbranch_scc1 .Lmk_exit\n"
"v_xor_b32_e32 v255, 32, v15\n"
"ds_read_b128 v[24:27], v2 offset:17408\n"
"ds_read_b128 v[28:31], v15 offset:16384\n"
"ds_read_b128 v[32:35], v255 offset:16640\n"
"v_xor_b32_e32 v255, 32, v17\n"
"ds_read_b128 v[36:39], v2 offset:16384\n"
"ds_read_b128 v[40:43], v17 offset:15360\n"
"ds_read_b128 v[60:63], v255 offset:15616\n"
"v_xor_b32_e32 v255, 32, v16\n"
"ds_read_b64_tr_b16 v[64:65], v16 offset:16384\n"
"ds_read_b64_tr_b16 v[66:67], v255 offset:16640\n"
"ds_read_b64_tr_b16 v[68:69], v255 offset:16384\n"
"ds_read_b64_tr_b16 v[70:71], v16 offset:16640\n"
"v_mov_b32_e32 v72, 0x3f803f80\n"
"v_mov_b32_e32 v73, 0x3f803f80\n"
"v_mov_b32_e32 v74, 0x3f803f80\n"
"v_mov_b32_e32 v75, 0x3f803f80\n"
"v_xor_b32_e32 v255, 32, v16\n"
"ds_read_b64_tr_b16 v[76:77], v16 offset:15360\n"
"ds_read_b64_tr_b16 v[78:79], v255 offset:15616\n"
"ds_read_b64_tr_b16 v[80:81], v255 offset:15360\n"
"ds_read_b64_tr_b16 v[82:83], v16 offset:15616\n"
"v_mov_b32_e32 v84, 0x3f803f80\n"
"v_mov_b32_e32 v85, 0x3f803f80\n"
"v_mov_b32_e32 v86, 0x3f803f80\n"
"v_mov_b32_e32 v87, 0x3f803f80\n"
"s_waitcnt lgkmcnt(11)\n"
"v_mfma_f32_16x16x32_bf16 v[44:47], v[28:31], v[24:27], v[44:47]\n"
"v_mfma_f32_16x16x32_bf16 v[48:51], v[32:35], v[24:27], v[48:51]\n"
"s_waitcnt lgkmcnt(8)\n"
"v_mfma_f32_16x16x32_bf16 v[52:55], v[40:43], v[36:39], v[52:55]\n"
"v_mfma_f32_16x16x32_bf16 v[56:59], v[60:63], v[36:39], v[56:59]\n"
"buffer_load_dwordx4 a[56:59], v1, s[8:11], s98 offen offset:2048 sc1\n"
"buffer_load_dwordx4 a[60:63], v1, s[8:11], s98 offen offset:3072 sc1\n"
"s_add_u32 s99, s69, 0x4000\n"
"buffer_load_dwordx4 a[64:67], v0, s[8:11], s99 offen sc1\n"
"buffer_load_dwordx4 a[68:71], v0, s[8:11], s99 offen offset:1024 sc1\n"
"v_max_f32_e32 v255, v44, v45\n"
"s_nop 0\n"
"v_max_f32_e32 v254, v52, v53\n"
"v_max3_f32 v255, v255, v46, v47\n"
"v_max3_f32 v254, v254, v54, v55\n"
"v_max3_f32 v255, v255, v48, v49\n"
"v_max3_f32 v254, v254, v56, v57\n"
"v_max3_f32 v255, v255, v50, v51\n"
"v_max3_f32 v254, v254, v58, v59\n"
"v_mov_b32_e32 v253, v255\n"
"v_mov_b32_e32 v252, v254\n"
"s_nop 0\n"
"v_permlane16_swap_b32_e32 v255, v253\n"
"v_permlane16_swap_b32_e32 v254, v252\n"
"v_max_f32_e32 v255, v255, v253\n"
"v_max_f32_e32 v254, v254, v252\n"
"v_mov_b32_e32 v253, v255\n"
"v_mov_b32_e32 v252, v254\n"
"s_nop 0\n"
"v_permlane32_swap_b32_e32 v255, v253\n"
"v_permlane32_swap_b32_e32 v254, v252\n"
"v_max_f32_e32 v255, v255, v253\n"
"v_max_f32_e32 v254, v254, v252\n"
"s_add_u32 s98, s56, 0x440000\n"
"buffer_load_dwordx4 v[24:27], v0, s[4:7], s98 offen sc1\n"
"v_sub_f32_e32 v44, v44, v255\n"
"v_sub_f32_e32 v45, v45, v255\n"
"v_sub_f32_e32 v46, v46, v255\n"
"v_sub_f32_e32 v47, v47, v255\n"
"v_sub_f32_e32 v48, v48, v255\n"
"v_sub_f32_e32 v49, v49, v255\n"
"v_sub_f32_e32 v50, v50, v255\n"
"buffer_load_dwordx4 v[28:31], v0, s[4:7], s98 offen offset:1024 sc1\n"
"v_sub_f32_e32 v51, v51, v255\n"
"v_exp_f32_e32 v44, v44\n"
"v_exp_f32_e32 v45, v45\n"
"v_exp_f32_e32 v46, v46\n"
"buffer_load_dwordx4 v[32:35], v0, s[4:7], s98 offen offset:2048 sc1\n"
"v_exp_f32_e32 v47, v47\n"
"v_exp_f32_e32 v48, v48\n"
"v_exp_f32_e32 v49, v49\n"
"v_exp_f32_e32 v50, v50\n"
"buffer_load_dwordx4 v[36:39], v0, s[4:7], s98 offen offset:3072 sc1\n"
"v_exp_f32_e32 v51, v51\n"
"v_cvt_pk_bf16_f32 v40, v44, v45\n"
"v_cvt_pk_bf16_f32 v41, v46, v47\n"
"v_cvt_pk_bf16_f32 v42, v48, v49\n"
"v_cvt_pk_bf16_f32 v43, v50, v51\n"
"s_waitcnt lgkmcnt(4)\n"
"v_sub_f32_e32 v52, v52, v254\n"
"v_sub_f32_e32 v53, v53, v254\n"
"v_sub_f32_e32 v54, v54, v254\n"
"v_sub_f32_e32 v55, v55, v254\n"
"v_sub_f32_e32 v56, v56, v254\n"
"v_sub_f32_e32 v57, v57, v254\n"
"v_sub_f32_e32 v58, v58, v254\n"
"v_mfma_f32_16x16x32_bf16 v[44:47], v[72:75], v[40:43], 0\n"
"v_sub_f32_e32 v59, v59, v254\n"
"v_exp_f32_e32 v52, v52\n"
"v_exp_f32_e32 v53, v53\n"
"v_exp_f32_e32 v54, v54\n"
"v_mfma_f32_16x16x32_bf16 v[48:51], v[64:67], v[40:43], 0\n"
"v_exp_f32_e32 v55, v55\n"
"v_exp_f32_e32 v56, v56\n"
"v_exp_f32_e32 v57, v57\n"
"v_exp_f32_e32 v58, v58\n"
"v_mfma_f32_16x16x32_bf16 v[60:63], v[68:71], v[40:43], 0\n"
"v_exp_f32_e32 v59, v59\n"
"v_cvt_pk_bf16_f32 v40, v52, v53\n"
"v_cvt_pk_bf16_f32 v41, v54, v55\n"
"v_cvt_pk_bf16_f32 v42, v56, v57\n"
"v_cvt_pk_bf16_f32 v43, v58, v59\n"
"buffer_load_dwordx4 v[52:55], v1, s[4:7], s98 offen sc1\n"
"buffer_load_dwordx4 v[56:59], v1, s[4:7], s98 offen offset:1024 sc1\n"
"buffer_load_dwordx4 v[64:67], v1, s[4:7], s98 offen offset:2048 sc1\n"
"buffer_load_dwordx4 v[68:71], v1, s[4:7], s98 offen offset:3072 sc1\n"
"s_waitcnt lgkmcnt(0)\n"
"v_rcp_f32_e32 v44, v44\n"
"s_nop 0\n"
"v_mul_f32_e32 v48, v48, v44\n"
"v_mul_f32_e32 v49, v49, v44\n"
"v_mul_f32_e32 v50, v50, v44\n"
"v_mfma_f32_16x16x32_bf16 v[72:75], v[84:87], v[40:43], 0\n"
"v_mul_f32_e32 v51, v51, v44\n"
"v_mul_f32_e32 v60, v60, v44\n"
"v_mul_f32_e32 v61, v61, v44\n"
"v_mul_f32_e32 v62, v62, v44\n"
"v_mul_f32_e32 v63, v63, v44\n"
"v_mfma_f32_16x16x32_bf16 v[44:47], v[76:79], v[40:43], 0\n"
"v_cvt_pk_bf16_f32 v76, v48, v49\n"
"v_cvt_pk_bf16_f32 v77, v50, v51\n"
"v_cvt_pk_bf16_f32 v78, v60, v61\n"
"v_cvt_pk_bf16_f32 v79, v62, v63\n"
"s_add_u32 s98, s60, 0x408000\n"
"buffer_store_dwordx4 v[76:79], v0, s[4:7], s98 offen sc1\n"
"v_mfma_f32_16x16x32_bf16 v[48:51], v[80:83], v[40:43], 0\n"
"v_rcp_f32_e32 v72, v72\n"
"s_nop 0\n"
"v_mul_f32_e32 v44, v44, v72\n"
"v_mul_f32_e32 v45, v45, v72\n"
"v_mul_f32_e32 v46, v46, v72\n"
"v_mul_f32_e32 v47, v47, v72\n"
"s_nop 1\n"
"v_mul_f32_e32 v48, v48, v72\n"
"v_mul_f32_e32 v49, v49, v72\n"
"v_mul_f32_e32 v50, v50, v72\n"
"v_mul_f32_e32 v51, v51, v72\n"
"v_cvt_pk_bf16_f32 v40, v44, v45\n"
"v_cvt_pk_bf16_f32 v41, v46, v47\n"
"v_cvt_pk_bf16_f32 v42, v48, v49\n"
"v_cvt_pk_bf16_f32 v43, v50, v51\n"
"s_add_u32 s98, s60, 0x400000\n"
"buffer_store_dwordx4 v[40:43], v0, s[4:7], s98 offen sc1\n"
"s_add_u32 s98, s57, 0x440000\n"
"buffer_load_dwordx4 v[40:43], v0, s[4:7], s98 offen sc1\n"
"buffer_load_dwordx4 v[44:47], v0, s[4:7], s98 offen offset:1024 sc1\n"
"buffer_load_dwordx4 v[48:51], v0, s[4:7], s98 offen offset:2048 sc1\n"
"buffer_load_dwordx4 v[60:63], v0, s[4:7], s98 offen offset:3072 sc1\n"
"buffer_load_dwordx4 v[72:75], v1, s[4:7], s98 offen sc1\n"
"buffer_load_dwordx4 v[76:79], v1, s[4:7], s98 offen offset:1024 sc1\n"
"buffer_load_dwordx4 v[80:83], v1, s[4:7], s98 offen offset:2048 sc1\n"
"buffer_load_dwordx4 v[84:87], v1, s[4:7], s98 offen offset:3072 sc1\n"
"s_add_u32 s98, s58, 0x440000\n"
"buffer_load_dwordx4 v[88:91], v0, s[4:7], s98 offen sc1\n"
"buffer_load_dwordx4 v[92:95], v0, s[4:7], s98 offen offset:1024 sc1\n"
"buffer_load_dwordx4 v[96:99], v0, s[4:7], s98 offen offset:2048 sc1\n"
"buffer_load_dwordx4 v[100:103], v0, s[4:7], s98 offen offset:3072 sc1\n"
"buffer_load_dwordx4 v[104:107], v1, s[4:7], s98 offen sc1\n"
"buffer_load_dwordx4 v[108:111], v1, s[4:7], s98 offen offset:1024 sc1\n"
"buffer_load_dwordx4 v[112:115], v1, s[4:7], s98 offen offset:2048 sc1\n"
"buffer_load_dwordx4 v[116:119], v1, s[4:7], s98 offen offset:3072 sc1\n"
"buffer_load_dwordx4 a[72:75], v0, s[8:11], s99 offen offset:2048 sc1\n"
"buffer_load_dwordx4 a[76:79], v0, s[8:11], s99 offen offset:3072 sc1\n"
"buffer_load_dwordx4 a[80:83], v1, s[8:11], s99 offen sc1\n"
"buffer_load_dwordx4 a[84:87], v1, s[8:11], s99 offen offset:1024 sc1\n"
"buffer_load_dwordx4 a[88:91], v1, s[8:11], s99 offen offset:2048 sc1\n"
"buffer_load_dwordx4 a[92:95], v1, s[8:11], s99 offen offset:3072 sc1\n"
"s_add_u32 s99, s69, 0x6000\n"
"buffer_load_dwordx4 a[96:99], v0, s[8:11], s99 offen sc1\n"
"buffer_load_dwordx4 a[100:103], v0, s[8:11], s99 offen offset:1024 sc1\n"
"s_waitcnt vmcnt(33)\n"
"; @B G2 b.0\n"
"v_mfma_f32_16x16x32_bf16 v[120:123], a[128:131], v[24:27], 0\n"
"v_mfma_f32_16x16x32_bf16 v[124:127], a[132:135], v[24:27], 0\n"
"v_mfma_f32_16x16x32_bf16 v[128:131], a[136:139], v[24:27], 0\n"
"s_add_u32 s98, s59, 0x440000\n"
"v_mfma_f32_16x16x32_bf16 v[132:135], a[140:143], v[24:27], 0\n"
"s_waitcnt vmcnt(32)\n"
"v_mfma_f32_16x16x32_bf16 v[120:123], a[144:147], v[28:31], v[120:123]\n"
"buffer_load_dwordx4 v[24:27], v0, s[4:7], s98 offen sc1\n"
"v_mfma_f32_16x16x32_bf16 v[124:127], a[148:151], v[28:31], v[124:127]\n"
"v_mfma_f32_16x16x32_bf16 v[128:131], a[152:155], v[28:31], v[128:131]\n"
"v_mfma_f32_16x16x32_bf16 v[132:135], a[156:159], v[28:31], v[132:135]\n"
"s_waitcnt vmcnt(32)\n"
"v_mfma_f32_16x16x32_bf16 v[120:123], a[160:163], v[32:35], v[120:123]\n"
"buffer_load_dwordx4 v[28:31], v0, s[4:7], s98 offen offset:1024 sc1\n"
"v_mfma_f32_16x16x32_bf16 v[124:127], a[164:167], v[32:35], v[124:127]\n"
"v_mfma_f32_16x16x32_bf16 v[128:131], a[168:171], v[32:35], v[128:131]\n"
"v_mfma_f32_16x16x32_bf16 v[132:135], a[172:175], v[32:35], v[132:135]\n"
"s_waitcnt vmcnt(32)\n"
"v_mfma_f32_16x16x32_bf16 v[120:123], a[176:179], v[36:39], v[120:123]\n"
"buffer_load_dwordx4 v[32:35], v0, s[4:7], s98 offen offset:2048 sc1\n"
"v_mfma_f32_16x16x32_bf16 v[124:127], a[180:183], v[36:39], v[124:127]\n"
"v_mfma_f32_16x16x32_bf16 v[128:131], a[184:187], v[36:39], v[128:131]\n"
"v_mfma_f32_16x16x32_bf16 v[132:135], a[188:191], v[36:39], v[132:135]\n"
"s_waitcnt vmcnt(32)\n"
"v_mfma_f32_16x16x32_bf16 v[120:123], a[192:195], v[52:55], v[120:123]\n"
"buffer_load_dwordx4 v[36:39], v0, s[4:7], s98 offen offset:3072 sc1\n"
"v_mfma_f32_16x16x32_bf16 v[124:127], a[196:199], v[52:55], v[124:127]\n"
"v_mfma_f32_16x16x32_bf16 v[128:131], a[200:203], v[52:55], v[128:131]\n"
"v_mfma_f32_16x16x32_bf16 v[132:135], a[204:207], v[52:55], v[132:135]\n"
"s_waitcnt vmcnt(32)\n"
"v_mfma_f32_16x16x32_bf16 v[120:123], a[208:211], v[56:59], v[120:123]\n"
"buffer_load_dwordx4 v[52:55], v1, s[4:7], s98 offen sc1\n"
"v_mfma_f32_16x16x32_bf16 v[124:127], a[212:215], v[56:59], v[124:127]\n"
"v_mfma_f32_16x16x32_bf16 v[128:131], a[216:219], v[56:59], v[128:131]\n"
"v_mfma_f32_16x16x32_bf16 v[132:135], a[220:223], v[56:59], v[132:135]\n"
"s_waitcnt vmcnt(32)\n"
"v_mfma_f32_16x16x32_bf16 v[120:123], a[224:227], v[64:67], v[120:123]\n"
"buffer_load_dwordx4 v[56:59], v1, s[4:7], s98 offen offset:1024 sc1\n"
"v_mfma_f32_16x16x32_bf16 v[124:127], a[228:231], v[64:67], v[124:127]\n"
"v_mfma_f32_16x16x32_bf16 v[128:131], a[232:235], v[64:67], v[128:131]\n"
"v_mfma_f32_16x16x32_bf16 v[132:135], a[236:239], v[64:67], v[132:135]\n"
"s_waitcnt vmcnt(32)\n"
"v_mfma_f32_16x16x32_bf16 v[120:123], a[240:243], v[68:71], v[120:123]\n"
"buffer_load_dwordx4 v[64:67], v1, s[4:7], s98 offen offset:2048 sc1\n"
"v_mfma_f32_16x16x32_bf16 v[124:127], a[244:247], v[68:71], v[124:127]\n"
"v_mfma_f32_16x16x32_bf16 v[128:131], a[248:251], v[68:71], v[128:131]\n"
"v_mfma_f32_16x16x32_bf16 v[132:135], a[252:255], v[68:71], v[132:135]\n"
"s_waitcnt vmcnt(29)\n"
"; @B G2 b.1\n"
"v_mfma_f32_16x16x32_bf16 v[68:71], a[128:131], v[40:43], 0\n"
"s_nop 1\n"
"v_cmp_u_f32_e32 vcc, v120, v120\n"
"v_mfma_f32_16x16x32_bf16 v[136:139], a[132:135], v[40:43], 0\n"
"s_cbranch_vccnz .Lmk_p71s\n"
".Lmk_p71r:\n"
"v_mfma_f32_16x16x32_bf16 v[140:143], a[136:139], v[40:43], 0\n"
"buffer_load_dwordx4 v[144:147], v1, s[4:7], s98 offen offset:3072 sc1\n"
"v_mfma_f32_16x16x32_bf16 v[148:151], a[140:143], v[40:43], 0\n"
"v_mfma_f32_16x16x32_bf16 v[68:71], a[144:147], v[44:47], v[68:71]\n"
"ds_write_b128 v3, v[120:123]\n"
"v_mfma_f32_16x16x32_bf16 v[136:139], a[148:151], v[44:47], v[136:139]\n"
"v_mfma_f32_16x16x32_bf16 v[140:143], a[152:155], v[44:47], v[140:143]\n"
"ds_write_b128 v3, v[124:127] offset:1024\n"
"v_mfma_f32_16x16x32_bf16 v[148:151], a[156:159], v[44:47], v[148:151]\n"
"s_waitcnt vmcnt(28)\n"
"v_mfma_f32_16x16x32_bf16 v[68:71], a[160:163], v[48:51], v[68:71]\n"
"ds_write_b128 v3, v[128:131] offset:2048\n"
"v_mfma_f32_16x16x32_bf16 v[136:139], a[164:167], v[48:51], v[136:139]\n"
"s_add_u32 s98, s56, 0x400000\n"
"v_mfma_f32_16x16x32_bf16 v[140:143], a[168:171], v[48:51], v[140:143]\n"
"ds_write_b128 v3, v[132:135] offset:3072\n"
"v_mfma_f32_16x16x32_bf16 v[148:151], a[172:175], v[48:51], v[148:151]\n"
"buffer_load_dwordx4 v[40:43], v0, s[4:7], s98 offen sc1\n"
"v_mfma_f32_16x16x32_bf16 v[68:71], a[176:179], v[60:63], v[68:71]\n"
"v_mfma_f32_16x16x32_bf16 v[136:139], a[180:183], v[60:63], v[136:139]\n"
"ds_read_b128 v[44:47], v2 offset:8192\n"
"ds_read_b128 v[48:51], v2 offset:9216\n"
"v_mfma_f32_16x16x32_bf16 v[140:143], a[184:187], v[60:63], v[140:143]\n"
"buffer_load_dwordx4 v[120:123], v0, s[4:7], s98 offen offset:1024 sc1\n"
"v_mfma_f32_16x16x32_bf16 v[148:151], a[188:191], v[60:63], v[148:151]\n"
"s_waitcnt vmcnt(28)\n"
"v_mfma_f32_16x16x32_bf16 v[68:71], a[192:195], v[72:75], v[68:71]\n"
"v_mfma_f32_16x16x32_bf16 v[136:139], a[196:199], v[72:75], v[136:139]\n"
"buffer_load_dwordx4 v[60:63], v0, s[4:7], s98 offen offset:2048 sc1\n"
"v_mfma_f32_16x16x32_bf16 v[140:143], a[200:203], v[72:75], v[140:143]\n"
"s_waitcnt lgkmcnt(0)\n"
"v_lshlrev_b32_e32 v255, 16, v44\n"
"v_mfma_f32_16x16x32_bf16 v[148:151], a[204:207], v[72:75], v[148:151]\n"
"s_waitcnt lgkmcnt(0)\n"
"s_barrier\n"
"v_mfma_f32_16x16x32_bf16 v[68:71], a[208:211], v[76:79], v[68:71]\n"
"buffer_load_dwordx4 v[72:75], v0, s[4:7], s98 offen offset:3072 sc1\n"
"v_mfma_f32_16x16x32_bf16 v[136:139], a[212:215], v[76:79], v[136:139]\n"
"ds_read_b128 v[124:127], v4\n"
"ds_read_b128 v[128:131], v4 offset:1024\n"
"v_mfma_f32_16x16x32_bf16 v[140:143], a[216:219], v[76:79], v[140:143]\n"
"ds_read_b128 v[132:135], v4 offset:2048\n"
"ds_read_b128 v[152:155], v4 offset:3072\n"
"v_mfma_f32_16x16x32_bf16 v[148:151], a[220:223], v[76:79], v[148:151]\n"
"s_waitcnt vmcnt(28)\n"
"v_and_b32_e32 v254, 0xffff0000, v44\n"
"v_mfma_f32_16x16x32_bf16 v[68:71], a[224:227], v[80:83], v[68:71]\n"
"buffer_load_dwordx4 v[76:79], v1, s[4:7], s98 offen sc1\n"
"v_mfma_f32_16x16x32_bf16 v[136:139], a[228:231], v[80:83], v[136:139]\n"
"v_lshlrev_b32_e32 v253, 16, v45\n"
"v_and_b32_e32 v252, 0xffff0000, v45\n"
"v_mfma_f32_16x16x32_bf16 v[140:143], a[232:235], v[80:83], v[140:143]\n"
"v_lshlrev_b32_e32 v251, 16, v46\n"
"v_and_b32_e32 v250, 0xffff0000, v46\n"
"v_mfma_f32_16x16x32_bf16 v[148:151], a[236:239], v[80:83], v[148:151]\n"
"buffer_load_dwordx4 v[80:83], v1, s[4:7], s98 offen offset:1024 sc1\n"
"v_mfma_f32_16x16x32_bf16 v[68:71], a[240:243], v[84:87], v[68:71]\n"
"v_lshlrev_b32_e32 v249, 16, v47\n"
"v_and_b32_e32 v248, 0xffff0000, v47\n"
"v_mfma_f32_16x16x32_bf16 v[136:139], a[244:247], v[84:87], v[136:139]\n"
"v_lshlrev_b32_e32 v247, 16, v48\n"
"v_and_b32_e32 v246, 0xffff0000, v48\n"
"v_mfma_f32_16x16x32_bf16 v[140:143], a[248:251], v[84:87], v[140:143]\n"
"buffer_load_dwordx4 v[44:47], v1, s[4:7], s98 offen offset:2048 sc1\n"
"v_mfma_f32_16x16x32_bf16 v[148:151], a[252:255], v[84:87], v[148:151]\n"
"s_waitcnt vmcnt(29)\n"
"v_lshlrev_b32_e32 v245, 16, v49\n"
"; @B G2 b.2\n"
"v_mfma_f32_16x16x32_bf16 v[84:87], a[128:131], v[88:91], 0\n"
"v_cmp_u_f32_e32 vcc, v68, v68\n"
"v_and_b32_e32 v244, 0xffff0000, v49\n"
"v_mfma_f32_16x16x32_bf16 v[156:159], a[132:135], v[88:91], 0\n"
"s_cbranch_vccnz .Lmk_p72s\n"
".Lmk_p72r:\n"
"v_lshlrev_b32_e32 v243, 16, v50\n"
"v_mfma_f32_16x16x32_bf16 v[160:163], a[136:139], v[88:91], 0\n"
"buffer_load_dwordx4 v[164:167], v1, s[4:7], s98 offen offset:3072 sc1\n"
"v_mfma_f32_16x16x32_bf16 v[168:171], a[140:143], v[88:91], 0\n"
"s_add_u32 s98, s57, 0x400000\n"
"v_and_b32_e32 v242, 0xffff0000, v50\n"
"v_mfma_f32_16x16x32_bf16 v[84:87], a[144:147], v[92:95], v[84:87]\n"
"ds_write_b128 v3, v[68:71] offset:6144\n"
"v_mfma_f32_16x16x32_bf16 v[156:159], a[148:151], v[92:95], v[156:159]\n"
"buffer_load_dwordx4 v[68:71], v0, s[4:7], s98 offen sc1\n"
"v_mfma_f32_16x16x32_bf16 v[160:163], a[152:155], v[92:95], v[160:163]\n"
"ds_write_b128 v3, v[136:139] offset:7168\n"
"v_mfma_f32_16x16x32_bf16 v[168:171], a[156:159], v[92:95], v[168:171]\n"
"s_waitcnt vmcnt(29)\n"
"v_lshlrev_b32_e32 v241, 16, v51\n"
"v_mfma_f32_16x16x32_bf16 v[84:87], a[160:163], v[96:99], v[84:87]\n"
"ds_write_b128 v3, v[140:143] offset:8192\n"
"v_mfma_f32_16x16x32_bf16 v[156:159], a[164:167], v[96:99], v[156:159]\n"
"buffer_load_dwordx4 v[88:91], v0, s[4:7], s98 offen offset:1024 sc1\n"
"v_mfma_f32_16x16x32_bf16 v[160:163], a[168:171], v[96:99], v[160:163]\n"
"ds_write_b128 v3, v[148:151] offset:9216\n"
"v_mfma_f32_16x16x32_bf16 v[168:171], a[172:175], v[96:99], v[168:171]\n"
"v_and_b32_e32 v240, 0xffff0000, v51\n"
"s_waitcnt lgkmcnt(4)\n"
"v_mfma_f32_16x16x32_bf16 v[84:87], a[176:179], v[100:103], v[84:87]\n"
"buffer_load_dwordx4 v[48:51], v0, s[4:7], s98 offen offset:2048 sc1\n"
"v_mfma_f32_16x16x32_bf16 v[156:159], a[180:183], v[100:103], v[156:159]\n"
"v_add_f32_e32 v124, v124, v255\n"
"v_add_f32_e32 v125, v125, v254\n"
"v_mfma_f32_16x16x32_bf16 v[160:163], a[184:187], v[100:103], v[160:163]\n"
"v_add_f32_e32 v126, v126, v253\n"
"v_add_f32_e32 v127, v127, v252\n"
"v_mfma_f32_16x16x32_bf16 v[168:171], a[188:191], v[100:103], v[168:171]\n"
"s_waitcnt vmcnt(29)\n"
"v_add_f32_e32 v128, v128, v251\n"
"v_mfma_f32_16x16x32_bf16 v[84:87], a[192:195], v[104:107], v[84:87]\n"
"buffer_load_dwordx4 v[92:95], v0, s[4:7], s98 offen offset:3072 sc1\n"
"v_mfma_f32_16x16x32_bf16 v[156:159], a[196:199], v[104:107], v[156:159]\n"
"v_add_f32_e32 v129, v129, v250\n"
"v_add_f32_e32 v130, v130, v249\n"
"v_mfma_f32_16x16x32_bf16 v[160:163], a[200:203], v[104:107], v[160:163]\n"
"v_add_f32_e32 v131, v131, v248\n"
"v_add_f32_e32 v132, v132, v247\n"
"v_mfma_f32_16x16x32_bf16 v[168:171], a[204:207], v[104:107], v[168:171]\n"
"buffer_load_dwordx4 v[96:99], v1, s[4:7], s98 offen sc1\n"
"v_mfma_f32_16x16x32_bf16 v[84:87], a[208:211], v[108:111], v[84:87]\n"
"v_add_f32_e32 v133, v133, v246\n"
"v_add_f32_e32 v134, v134, v245\n"
"v_mfma_f32_16x16x32_bf16 v[156:159], a[212:215], v[108:111], v[156:159]\n"
"v_add_f32_e32 v135, v135, v244\n"
"v_add_f32_e32 v152, v152, v243\n"
"v_mfma_f32_16x16x32_bf16 v[160:163], a[216:219], v[108:111], v[160:163]\n"
"buffer_load_dwordx4 v[100:103], v1, s[4:7], s98 offen offset:1024 sc1\n"
"v_mfma_f32_16x16x32_bf16 v[168:171], a[220:223], v[108:111], v[168:171]\n"
"s_waitcnt vmcnt(30)\n"
"v_add_f32_e32 v153, v153, v242\n"
"v_mfma_f32_16x16x32_bf16 v[84:87], a[224:227], v[112:115], v[84:87]\n"
"s_waitcnt vmcnt(20)\n"
"v_max_u32_e32 v255, v24, v25\n"
"v_mfma_f32_16x16x32_bf16 v[156:159], a[228:231], v[112:115], v[156:159]\n"
"v_max3_u32 v255, v255, v26, v27\n"
"v_max3_u32 v255, v255, v28, v29\n"
"v_mfma_f32_16x16x32_bf16 v[160:163], a[232:235], v[112:115], v[160:163]\n"
"v_max3_u32 v255, v255, v30, v31\n"
"s_waitcnt vmcnt(18)\n"
"v_mfma_f32_16x16x32_bf16 v[168:171], a[236:239], v[112:115], v[168:171]\n"
"v_max3_u32 v255, v255, v32, v33\n"
"v_max3_u32 v255, v255, v34, v35\n"
"v_mfma_f32_16x16x32_bf16 v[84:87], a[240:243], v[116:119], v[84:87]\n"
"v_max3_u32 v255, v255, v36, v37\n"
"v_max3_u32 v255, v255, v38, v39\n"
"v_mfma_f32_16x16x32_bf16 v[156:159], a[244:247], v[116:119], v[156:159]\n"
"v_cmp_eq_u32_e32 vcc, -1, v255\n"
"v_add_f32_e32 v154, v154, v241\n"
"v_mfma_f32_16x16x32_bf16 v[160:163], a[248:251], v[116:119], v[160:163]\n"
"s_cbranch_vccnz .Lmk_q74s\n"
".Lmk_q74r:\n"
"v_add_f32_e32 v155, v155, v240\n"
"v_mfma_f32_16x16x32_bf16 v[168:171], a[252:255], v[116:119], v[168:171]\n"
"; @B G2 b.3\n"
"v_mfma_f32_16x16x32_bf16 v[124:127], a[128:131], v[24:27], v[124:127]\n"
"v_cmp_u_f32_e32 vcc, v84, v84\n"
"v_mfma_f32_16x16x32_bf16 v[128:131], a[132:135], v[24:27], v[128:131]\n"
"s_cbranch_vccnz .Lmk_p73s\n"
".Lmk_p73r:\n"
"v_mfma_f32_16x16x32_bf16 v[132:135], a[136:139], v[24:27], v[132:135]\n"
"buffer_load_dwordx4 v[104:107], v1, s[4:7], s98 offen offset:2048 sc1\n"
"v_mfma_f32_16x16x32_bf16 v[152:155], a[140:143], v[24:27], v[152:155]\n"
"v_mfma_f32_16x16x32_bf16 v[124:127], a[144:147], v[28:31], v[124:127]\n"
"ds_write_b128 v3, v[84:87] offset:12288\n"
"v_mfma_f32_16x16x32_bf16 v[128:131], a[148:151], v[28:31], v[128:131]\n"
"buffer_load_dwordx4 v[24:27], v1, s[4:7], s98 offen offset:3072 sc1\n"
"v_mfma_f32_16x16x32_bf16 v[132:135], a[152:155], v[28:31], v[132:135]\n"
"ds_write_b128 v3, v[156:159] offset:13312\n"
"v_mfma_f32_16x16x32_bf16 v[152:155], a[156:159], v[28:31], v[152:155]\n"
"s_add_u32 s98, s58, 0x400000\n"
"v_mfma_f32_16x16x32_bf16 v[124:127], a[160:163], v[32:35], v[124:127]\n"
"s_waitcnt vmcnt(18)\n"
"v_max_u32_e32 v255, v52, v53\n"
"v_mfma_f32_16x16x32_bf16 v[128:131], a[164:167], v[32:35], v[128:131]\n"
"v_max3_u32 v255, v255, v54, v55\n"
"v_max3_u32 v255, v255, v56, v57\n"
"v_mfma_f32_16x16x32_bf16 v[132:135], a[168:171], v[32:35], v[132:135]\n"
"v_max3_u32 v255, v255, v58, v59\n"
"s_waitcnt vmcnt(16)\n"
"v_mfma_f32_16x16x32_bf16 v[152:155], a[172:175], v[32:35], v[152:155]\n"
"v_max3_u32 v255, v255, v64, v65\n"
"v_max3_u32 v255, v255, v66, v67\n"
"v_mfma_f32_16x16x32_bf16 v[124:127], a[176:179], v[36:39], v[124:127]\n"
"v_max3_u32 v255, v255, v144, v145\n"
"v_max3_u32 v255, v255, v146, v147\n"
"v_mfma_f32_16x16x32_bf16 v[128:131], a[180:183], v[36:39], v[128:131]\n"
"v_cmp_eq_u32_e32 vcc, -1, v255\n"
"v_mfma_f32_16x16x32_bf16 v[132:135], a[184:187], v[36:39], v[132:135]\n"
"s_cbranch_vccnz .Lmk_q75s\n"
".Lmk_q75r:\n"
"v_mfma_f32_16x16x32_bf16 v[152:155], a[188:191], v[36:39], v[152:155]\n"
"v_mfma_f32_16x16x32_bf16 v[124:127], a[192:195], v[52:55], v[124:127]\n"
"ds_write_b128 v3, v[160:163] offset:14336\n"
"v_mfma_f32_16x16x32_bf16 v[128:131], a[196:199], v[52:55], v[128:131]\n"
"buffer_load_dwordx4 v[28:31], v0, s[4:7], s98 offen sc1\n"
"v_mfma_f32_16x16x32_bf16 v[132:135], a[200:203], v[52:55], v[132:135]\n"
"ds_write_b128 v3, v[168:171] offset:15360\n"
"v_mfma_f32_16x16x32_bf16 v[152:155], a[204:207], v[52:55], v[152:155]\n"
"v_mfma_f32_16x16x32_bf16 v[124:127], a[208:211], v[56:59], v[124:127]\n"
"buffer_load_dwordx4 v[32:35], v0, s[4:7], s98 offen offset:1024 sc1\n"
"v_mfma_f32_16x16x32_bf16 v[128:131], a[212:215], v[56:59], v[128:131]\n"
"s_waitcnt lgkmcnt(0)\n"
"s_barrier\n"
"v_mfma_f32_16x16x32_bf16 v[132:135], a[216:219], v[56:59], v[132:135]\n"
"ds_read_b128 v[36:39], v5\n"
"ds_read_b128 v[52:55], v6\n"
"v_mfma_f32_16x16x32_bf16 v[152:155], a[220:223], v[56:59], v[152:155]\n"
"ds_read_b128 v[56:59], v5 offset:1024\n"
"v_mfma_f32_16x16x32_bf16 v[124:127], a[224:227], v[64:67], v[124:127]\n"
"buffer_load_dwordx4 v[84:87], v0, s[4:7], s98 offen offset:2048 sc1\n"
"v_mfma_f32_16x16x32_bf16 v[128:131], a[228:231], v[64:67], v[128:131]\n"
"ds_read_b128 v[108:111], v6 offset:1024\n"
"v_mfma_f32_16x16x32_bf16 v[132:135], a[232:235], v[64:67], v[132:135]\n"
"ds_read_b128 v[112:115], v5 offset:2048\n"
"ds_read_b128 v[116:119], v6 offset:2048\n"
"v_mfma_f32_16x16x32_bf16 v[152:155], a[236:239], v[64:67], v[152:155]\n"
"buffer_load_dwordx4 v[64:67], v0, s[4:7], s98 offen offset:3072 sc1\n"
"v_mfma_f32_16x16x32_bf16 v[124:127], a[240:243], v[144:147], v[124:127]\n"
"ds_read_b128 v[136:139], v5 offset:3072\n"
"ds_read_b128 v[140:143], v6 offset:3072\n"
"v_mfma_f32_16x16x32_bf16 v[128:131], a[244:247], v[144:147], v[128:131]\n"
"v_mfma_f32_16x16x32_bf16 v[132:135], a[248:251], v[144:147], v[132:135]\n"
"buffer_load_dwordx4 v[148:151], v1, s[4:7], s98 offen sc1\n"
"v_mfma_f32_16x16x32_bf16 v[152:155], a[252:255], v[144:147], v[152:155]\n"
"s_waitcnt lgkmcnt(4)\n"
"v_add_f32_e32 v36, v36, v52\n"
"v_add_f32_e32 v37, v37, v53\n"
"v_add_f32_e32 v38, v38, v54\n"
"v_add_f32_e32 v39, v39, v55\n"
"v_add_f32_e32 v56, v56, v108\n"
"v_add_f32_e32 v57, v57, v109\n"
"v_add_f32_e32 v58, v58, v110\n"
"v_add_f32_e32 v59, v59, v111\n"
"buffer_load_dwordx4 v[52:55], v1, s[4:7], s98 offen offset:1024 sc1\n"
"s_waitcnt lgkmcnt(0)\n"
"v_add_f32_e32 v112, v112, v116\n"
"v_add_f32_e32 v113, v113, v117\n"
"v_add_f32_e32 v114, v114, v118\n"
"v_add_f32_e32 v115, v115, v119\n"
"v_add_f32_e32 v136, v136, v140\n"
"v_add_f32_e32 v137, v137, v141\n"
"v_add_f32_e32 v138, v138, v142\n"
"v_add_f32_e32 v139, v139, v143\n"
"buffer_load_dwordx4 v[108:111], v1, s[4:7], s98 offen offset:2048 sc1\n"
"buffer_load_dwordx4 v[116:119], v1, s[4:7], s98 offen offset:3072 sc1\n"
"; @B fin b\n"
"s_add_u32 s98, s59, 0x400000\n"
"buffer_load_dwordx4 v[140:143], v0, s[4:7], s98 offen sc1\n"
"v_mov_b32_e32 v144, 0x3f803f80\n"
"v_mov_b32_e32 v145, 0x3f803f80\n"
"v_mov_b32_e32 v146, 0x3f803f80\n"
"v_mov_b32_e32 v147, 0x3f803f80\n"
"v_add_f32_e32 v36, v124, v36\n"
"v_add_f32_e32 v37, v125, v37\n"
"v_add_f32_e32 v38, v126, v38\n"
"v_add_f32_e32 v39, v127, v39\n"
"v_add_f32_e32 v56, v128, v56\n"
"buffer_load_dwordx4 v[124:127], v0, s[4:7], s98 offen offset:1024 sc1\n"
"v_add_f32_e32 v57, v129, v57\n"
"v_add_f32_e32 v58, v130, v58\n"
"v_add_f32_e32 v59, v131, v59\n"
"v_cvt_pk_bf16_f32 v128, v36, v37\n"
"v_cvt_pk_bf16_f32 v129, v38, v39\n"
"v_cvt_pk_bf16_f32 v130, v56, v57\n"
"v_cvt_pk_bf16_f32 v131, v58, v59\n"
"s_add_u32 s97, s61, 0x840000\n"
"buffer_store_dwordx4 v[128:131], v0, s[4:7], s97 offen sc1\n"
"buffer_load_dwordx4 v[36:39], v0, s[4:7], s98 offen offset:2048 sc1\n"
"ds_write_b128 v2, v[128:131] offset:8192\n"
"buffer_load_dwordx4 v[56:59], v0, s[4:7], s98 offen offset:3072 sc1\n"
"v_add_f32_e32 v112, v132, v112\n"
"v_add_f32_e32 v113, v133, v113\n"
"v_add_f32_e32 v114, v134, v114\n"
"v_add_f32_e32 v115, v135, v115\n"
"v_add_f32_e32 v136, v152, v136\n"
"v_add_f32_e32 v137, v153, v137\n"
"v_add_f32_e32 v138, v154, v138\n"
"v_add_f32_e32 v139, v155, v139\n"
"buffer_load_dwordx4 v[132:135], v1, s[4:7], s98 offen sc1\n"
"v_cvt_pk_bf16_f32 v152, v112, v113\n"
"v_cvt_pk_bf16_f32 v153, v114, v115\n"
"v_cvt_pk_bf16_f32 v154, v136, v137\n"
"v_cvt_pk_bf16_f32 v155, v138, v139\n"
"s_add_u32 s97, s61, 0x840000\n"
"buffer_store_dwordx4 v[152:155], v0, s[4:7], s97 offen offset:1024 sc1\n"
"buffer_load_dwordx4 v[112:115], v1, s[4:7], s98 offen offset:1024 sc1\n"
"ds_write_b128 v2, v[152:155] offset:9216\n"
"v_mfma_f32_16x16x32_bf16 v[136:139], v[144:147], v[128:131], 0\n"
"buffer_load_dwordx4 v[156:159], v1, s[4:7], s98 offen offset:2048 sc1\n"
"v_mfma_f32_16x16x32_bf16 v[160:163], v[128:131], v[128:131], 0\n"
"v_mfma_f32_16x16x32_bf16 v[136:139], v[144:147], v[152:155], v[136:139]\n"
"v_mfma_f32_16x16x32_bf16 v[160:163], v[152:155], v[152:155], v[160:163]\n"
"s_mov_b32 s96, 0xaaaaaaaa\n"
"s_mov_b32 s97, 0xaaaaaaaa\n"
"s_nop 5\n"
"v_cndmask_b32_e64 v160, v160, v161, s[96:97]\n"
"buffer_load_dwordx4 v[128:131], v1, s[4:7], s98 offen offset:3072 sc1\n"
"v_cndmask_b32_e64 v162, v162, v163, s[96:97]\n"
"s_mov_b32 s96, 0xcccccccc\n"
"s_mov_b32 s97, 0xcccccccc\n"
"v_cndmask_b32_e64 v137, v160, v162, s[96:97]\n"
"s_add_u32 s98, s63, 0x84000\n"
"buffer_store_dwordx2 v[136:137], v14, s[4:7], s98 offen sc1\n"
"s_waitcnt vmcnt(34)\n"
"; @B G2 a.0\n"
"v_mfma_f32_16x16x32_bf16 v[136:139], a[128:131], v[40:43], 0\n"
"v_mfma_f32_16x16x32_bf16 v[144:147], a[132:135], v[40:43], 0\n"
"v_mfma_f32_16x16x32_bf16 v[152:155], a[136:139], v[40:43], 0\n"
"buffer_load_dwordx4 a[104:107], v0, s[8:11], s99 offen offset:2048 sc1\n"
"v_mfma_f32_16x16x32_bf16 v[160:163], a[140:143], v[40:43], 0\n"
"s_waitcnt vmcnt(34)\n"
"v_mfma_f32_16x16x32_bf16 v[136:139], a[144:147], v[120:123], v[136:139]\n"
"v_mfma_f32_16x16x32_bf16 v[144:147], a[148:151], v[120:123], v[144:147]\n"
"buffer_load_dwordx4 a[108:111], v0, s[8:11], s99 offen offset:3072 sc1\n"
"v_mfma_f32_16x16x32_bf16 v[152:155], a[152:155], v[120:123], v[152:155]\n"
"v_mfma_f32_16x16x32_bf16 v[160:163], a[156:159], v[120:123], v[160:163]\n"
"s_waitcnt vmcnt(34)\n"
"v_mfma_f32_16x16x32_bf16 v[136:139], a[160:163], v[60:63], v[136:139]\n"
"buffer_load_dwordx4 a[112:115], v1, s[8:11], s99 offen sc1\n"
"v_mfma_f32_16x16x32_bf16 v[144:147], a[164:167], v[60:63], v[144:147]\n"
"v_mfma_f32_16x16x32_bf16 v[152:155], a[168:171], v[60:63], v[152:155]\n"
"v_mfma_f32_16x16x32_bf16 v[160:163], a[172:175], v[60:63], v[160:163]\n"
"s_waitcnt vmcnt(34)\n"
"v_mfma_f32_16x16x32_bf16 v[136:139], a[176:179], v[72:75], v[136:139]\n"
"buffer_load_dwordx4 a[116:119], v1, s[8:11], s99 offen offset:1024 sc1\n"
"v_mfma_f32_16x16x32_bf16 v[144:147], a[180:183], v[72:75], v[144:147]\n"
"v_mfma_f32_16x16x32_bf16 v[152:155], a[184:187], v[72:75], v[152:155]\n"
"v_mfma_f32_16x16x32_bf16 v[160:163], a[188:191], v[72:75], v[160:163]\n"
"s_waitcnt vmcnt(34)\n"
"v_mfma_f32_16x16x32_bf16 v[136:139], a[192:195], v[76:79], v[136:139]\n"
"buffer_load_dwordx4 a[120:123], v1, s[8:11], s99 offen offset:2048 sc1\n"
"v_mfma_f32_16x16x32_bf16 v[144:147], a[196:199], v[76:79], v[144:147]\n"
"v_mfma_f32_16x16x32_bf16 v[152:155], a[200:203], v[76:79], v[152:155]\n"
"v_mfma_f32_16x16x32_bf16 v[160:163], a[204:207], v[76:79], v[160:163]\n"
"s_waitcnt vmcnt(34)\n"
"v_mfma_f32_16x16x32_bf16 v[136:139], a[208:211], v[80:83], v[136:139]\n"
"buffer_load_dwordx4 a[124:127], v1, s[8:11], s99 offen offset:3072 sc1\n"
"v_mfma_f32_16x16x32_bf16 v[144:147], a[212:215], v[80:83], v[144:147]\n"
"s_add_u32 s99, s69, 0x8000\n"
"v_mfma_f32_16x16x32_bf16 v[152:155], a[216:219], v[80:83], v[152:155]\n"
"v_mfma_f32_16x16x32_bf16 v[160:163], a[220:223], v[80:83], v[160:163]\n"
"s_waitcnt vmcnt(34)\n"
"v_mfma_f32_16x16x32_bf16 v[136:139], a[224:227], v[44:47], v[136:139]\n"
"buffer_load_dwordx4 v[40:43], v0, s[8:11], s99 offen sc1\n"
"v_mfma_f32_16x16x32_bf16 v[144:147], a[228:231], v[44:47], v[144:147]\n"
"v_mfma_f32_16x16x32_bf16 v[152:155], a[232:235], v[44:47], v[152:155]\n"
"v_mfma_f32_16x16x32_bf16 v[160:163], a[236:239], v[44:47], v[160:163]\n"
"s_waitcnt vmcnt(34)\n"
"v_mfma_f32_16x16x32_bf16 v[136:139], a[240:243], v[164:167], v[136:139]\n"
"buffer_load_dwordx4 v[44:47], v0, s[8:11], s99 offen offset:1024 sc1\n"
"v_mfma_f32_16x16x32_bf16 v[144:147], a[244:247], v[164:167], v[144:147]\n"
"v_mfma_f32_16x16x32_bf16 v[152:155], a[248:251], v[164:167], v[152:155]\n"
"v_mfma_f32_16x16x32_bf16 v[160:163], a[252:255], v[164:167], v[160:163]\n"
"s_waitcnt vmcnt(33)\n"
"; @B G2 a.1\n"
"v_mfma_f32_16x16x32_bf16 v[60:63], a[128:131], v[68:71], 0\n"
"s_nop 1\n"
"v_cmp_u_f32_e32 vcc, v136, v136\n"
"v_mfma_f32_16x16x32_bf16 v[72:75], a[132:135], v[68:71], 0\n"
"s_cbranch_vccnz .Lmk_p76s\n"
".Lmk_p76r:\n"
"v_mfma_f32_16x16x32_bf16 v[76:79], a[136:139], v[68:71], 0\n"
"buffer_load_dwordx4 v[80:83], v0, s[8:11], s99 offen offset:2048 sc1\n"
"v_mfma_f32_16x16x32_bf16 v[120:123], a[140:143], v[68:71], 0\n"
"v_mfma_f32_16x16x32_bf16 v[60:63], a[144:147], v[88:91], v[60:63]\n"
"ds_write_b128 v3, v[136:139]\n"
"v_mfma_f32_16x16x32_bf16 v[72:75], a[148:151], v[88:91], v[72:75]\n"
"buffer_load_dwordx4 v[68:71], v0, s[8:11], s99 offen offset:3072 sc1\n"
"v_mfma_f32_16x16x32_bf16 v[76:79], a[152:155], v[88:91], v[76:79]\n"
"ds_write_b128 v3, v[144:147] offset:1024\n"
"v_mfma_f32_16x16x32_bf16 v[120:123], a[156:159], v[88:91], v[120:123]\n"
"s_waitcnt vmcnt(33)\n"
"v_mfma_f32_16x16x32_bf16 v[60:63], a[160:163], v[48:51], v[60:63]\n"
"ds_write_b128 v3, v[152:155] offset:2048\n"
"v_mfma_f32_16x16x32_bf16 v[72:75], a[164:167], v[48:51], v[72:75]\n"
"buffer_load_dwordx4 v[88:91], v1, s[8:11], s99 offen sc1\n"
"v_mfma_f32_16x16x32_bf16 v[76:79], a[168:171], v[48:51], v[76:79]\n"
"ds_write_b128 v3, v[160:163] offset:3072\n"
"v_mfma_f32_16x16x32_bf16 v[120:123], a[172:175], v[48:51], v[120:123]\n"
"v_mfma_f32_16x16x32_bf16 v[60:63], a[176:179], v[92:95], v[60:63]\n"
"buffer_load_dwordx4 v[48:51], v1, s[8:11], s99 offen offset:1024 sc1\n"
"v_mfma_f32_16x16x32_bf16 v[72:75], a[180:183], v[92:95], v[72:75]\n"
"ds_read_b128 v[136:139], v2\n"
"ds_read_b128 v[144:147], v2 offset:1024\n"
"v_mfma_f32_16x16x32_bf16 v[76:79], a[184:187], v[92:95], v[76:79]\n"
"v_mfma_f32_16x16x32_bf16 v[120:123], a[188:191], v[92:95], v[120:123]\n"
"s_waitcnt vmcnt(33)\n"
"v_mfma_f32_16x16x32_bf16 v[60:63], a[192:195], v[96:99], v[60:63]\n"
"buffer_load_dwordx4 v[92:95], v1, s[8:11], s99 offen offset:2048 sc1\n"
"v_mfma_f32_16x16x32_bf16 v[72:75], a[196:199], v[96:99], v[72:75]\n"
"v_mfma_f32_16x16x32_bf16 v[76:79], a[200:203], v[96:99], v[76:79]\n"
"s_waitcnt lgkmcnt(0)\n"
"v_lshlrev_b32_e32 v255, 16, v136\n"
"v_mfma_f32_16x16x32_bf16 v[120:123], a[204:207], v[96:99], v[120:123]\n"
"s_waitcnt lgkmcnt(0)\n"
"s_barrier\n"
"v_mfma_f32_16x16x32_bf16 v[60:63], a[208:211], v[100:103], v[60:63]\n"
"buffer_load_dwordx4 v[96:99], v1, s[8:11], s99 offen offset:3072 sc1\n"
"v_mfma_f32_16x16x32_bf16 v[72:75], a[212:215], v[100:103], v[72:75]\n"
"s_add_u32 s99, s69, 0xa000\n"
"ds_read_b128 v[152:155], v4\n"
"v_mfma_f32_16x16x32_bf16 v[76:79], a[216:219], v[100:103], v[76:79]\n"
"ds_read_b128 v[160:163], v4 offset:1024\n"
"ds_read_b128 v[164:167], v4 offset:2048\n"
"v_mfma_f32_16x16x32_bf16 v[120:123], a[220:223], v[100:103], v[120:123]\n"
"s_waitcnt vmcnt(33)\n"
"ds_read_b128 v[100:103], v4 offset:3072\n"
"v_mfma_f32_16x16x32_bf16 v[60:63], a[224:227], v[104:107], v[60:63]\n"
"buffer_load_dwordx4 v[168:171], v0, s[8:11], s99 offen sc1\n"
"v_mfma_f32_16x16x32_bf16 v[72:75], a[228:231], v[104:107], v[72:75]\n"
"v_and_b32_e32 v254, 0xffff0000, v136\n"
"v_lshlrev_b32_e32 v253, 16, v137\n"
"v_mfma_f32_16x16x32_bf16 v[76:79], a[232:235], v[104:107], v[76:79]\n"
"v_and_b32_e32 v252, 0xffff0000, v137\n"
"v_lshlrev_b32_e32 v251, 16, v138\n"
"v_mfma_f32_16x16x32_bf16 v[120:123], a[236:239], v[104:107], v[120:123]\n"
"buffer_load_dwordx4 v[104:107], v0, s[8:11], s99 offen offset:1024 sc1\n"
"v_mfma_f32_16x16x32_bf16 v[60:63], a[240:243], v[24:27], v[60:63]\n"
"v_and_b32_e32 v250, 0xffff0000, v138\n"
"v_lshlrev_b32_e32 v249, 16, v139\n"
"v_mfma_f32_16x16x32_bf16 v[72:75], a[244:247], v[24:27], v[72:75]\n"
"v_and_b32_e32 v248, 0xffff0000, v139\n"
"v_lshlrev_b32_e32 v247, 16, v144\n"
"v_mfma_f32_16x16x32_bf16 v[76:79], a[248:251], v[24:27], v[76:79]\n"
"buffer_load_dwordx4 v[136:139], v0, s[8:11], s99 offen offset:2048 sc1\n"
"v_mfma_f32_16x16x32_bf16 v[120:123], a[252:255], v[24:27], v[120:123]\n"
"s_waitcnt vmcnt(34)\n"
"v_and_b32_e32 v246, 0xffff0000, v144\n"
"; @B G2 a.2\n"
"v_mfma_f32_16x16x32_bf16 v[24:27], a[128:131], v[28:31], 0\n"
"v_cmp_u_f32_e32 vcc, v60, v60\n"
"v_lshlrev_b32_e32 v245, 16, v145\n"
"v_mfma_f32_16x16x32_bf16 v[172:175], a[132:135], v[28:31], 0\n"
"s_cbranch_vccnz .Lmk_p77s\n"
".Lmk_p77r:\n"
"v_and_b32_e32 v244, 0xffff0000, v145\n"
"v_mfma_f32_16x16x32_bf16 v[176:179], a[136:139], v[28:31], 0\n"
"buffer_load_dwordx4 v[180:183], v0, s[8:11], s99 offen offset:3072 sc1\n"
"v_mfma_f32_16x16x32_bf16 v[184:187], a[140:143], v[28:31], 0\n"
"v_lshlrev_b32_e32 v243, 16, v146\n"
"v_and_b32_e32 v242, 0xffff0000, v146\n"
"v_mfma_f32_16x16x32_bf16 v[24:27], a[144:147], v[32:35], v[24:27]\n"
"ds_write_b128 v3, v[60:63] offset:6144\n"
"v_mfma_f32_16x16x32_bf16 v[172:175], a[148:151], v[32:35], v[172:175]\n"
"buffer_load_dwordx4 v[28:31], v1, s[8:11], s99 offen sc1\n"
"v_mfma_f32_16x16x32_bf16 v[176:179], a[152:155], v[32:35], v[176:179]\n"
"ds_write_b128 v3, v[72:75] offset:7168\n"
"v_mfma_f32_16x16x32_bf16 v[184:187], a[156:159], v[32:35], v[184:187]\n"
"s_waitcnt vmcnt(34)\n"
"v_lshlrev_b32_e32 v241, 16, v147\n"
"v_mfma_f32_16x16x32_bf16 v[24:27], a[160:163], v[84:87], v[24:27]\n"
"ds_write_b128 v3, v[76:79] offset:8192\n"
"v_mfma_f32_16x16x32_bf16 v[172:175], a[164:167], v[84:87], v[172:175]\n"
"buffer_load_dwordx4 v[32:35], v1, s[8:11], s99 offen offset:1024 sc1\n"
"v_mfma_f32_16x16x32_bf16 v[176:179], a[168:171], v[84:87], v[176:179]\n"
"ds_write_b128 v3, v[120:123] offset:9216\n"
"v_mfma_f32_16x16x32_bf16 v[184:187], a[172:175], v[84:87], v[184:187]\n"
"v_and_b32_e32 v240, 0xffff0000, v147\n"
"s_waitcnt lgkmcnt(4)\n"
"v_mfma_f32_16x16x32_bf16 v[24:27], a[176:179], v[64:67], v[24:27]\n"
"buffer_load_dwordx4 v[60:63], v1, s[8:11], s99 offen offset:2048 sc1\n"
"v_mfma_f32_16x16x32_bf16 v[172:175], a[180:183], v[64:67], v[172:175]\n"
"v_add_f32_e32 v152, v152, v255\n"
"v_add_f32_e32 v153, v153, v254\n"
"v_mfma_f32_16x16x32_bf16 v[176:179], a[184:187], v[64:67], v[176:179]\n"
"v_add_f32_e32 v154, v154, v253\n"
"v_add_f32_e32 v155, v155, v252\n"
"v_mfma_f32_16x16x32_bf16 v[184:187], a[188:191], v[64:67], v[184:187]\n"
"s_waitcnt vmcnt(34)\n"
"v_add_f32_e32 v160, v160, v251\n"
"v_mfma_f32_16x16x32_bf16 v[24:27], a[192:195], v[148:151], v[24:27]\n"
"buffer_load_dwordx4 v[64:67], v1, s[8:11], s99 offen offset:3072 sc1\n"
"v_mfma_f32_16x16x32_bf16 v[172:175], a[196:199], v[148:151], v[172:175]\n"
"v_add_f32_e32 v161, v161, v250\n"
"v_add_f32_e32 v162, v162, v249\n"
"v_mfma_f32_16x16x32_bf16 v[176:179], a[200:203], v[148:151], v[176:179]\n"
"v_add_f32_e32 v163, v163, v248\n"
"v_add_f32_e32 v164, v164, v247\n"
"v_mfma_f32_16x16x32_bf16 v[184:187], a[204:207], v[148:151], v[184:187]\n"
"v_add_f32_e32 v165, v165, v246\n"
"v_add_f32_e32 v166, v166, v245\n"
"v_mfma_f32_16x16x32_bf16 v[24:27], a[208:211], v[52:55], v[24:27]\n"
"s_add_u32 s99, s52, 0x840000\n"
"v_add_f32_e32 v167, v167, v244\n"
"v_mfma_f32_16x16x32_bf16 v[172:175], a[212:215], v[52:55], v[172:175]\n"
"buffer_load_dwordx4 v[72:75], v0, s[8:11], s99 offen sc1\n"
"v_mfma_f32_16x16x32_bf16 v[176:179], a[216:219], v[52:55], v[176:179]\n"
"v_add_f32_e32 v100, v100, v243\n"
"v_add_f32_e32 v101, v101, v242\n"
"v_mfma_f32_16x16x32_bf16 v[184:187], a[220:223], v[52:55], v[184:187]\n"
"s_waitcnt vmcnt(34)\n"
"v_add_f32_e32 v102, v102, v241\n"
"v_mfma_f32_16x16x32_bf16 v[24:27], a[224:227], v[108:111], v[24:27]\n"
"s_waitcnt vmcnt(32)\n"
"v_max_u32_e32 v255, v140, v141\n"
"v_mfma_f32_16x16x32_bf16 v[172:175], a[228:231], v[108:111], v[172:175]\n"
"v_max3_u32 v255, v255, v142, v143\n"
"v_max3_u32 v255, v255, v124, v125\n"
"v_mfma_f32_16x16x32_bf16 v[176:179], a[232:235], v[108:111], v[176:179]\n"
"v_max3_u32 v255, v255, v126, v127\n"
"s_waitcnt vmcnt(29)\n"
"v_mfma_f32_16x16x32_bf16 v[184:187], a[236:239], v[108:111], v[184:187]\n"
"v_max3_u32 v255, v255, v36, v37\n"
"v_max3_u32 v255, v255, v38, v39\n"
"v_mfma_f32_16x16x32_bf16 v[24:27], a[240:243], v[116:119], v[24:27]\n"
"v_max3_u32 v255, v255, v56, v57\n"
"v_max3_u32 v255, v255, v58, v59\n"
"v_mfma_f32_16x16x32_bf16 v[172:175], a[244:247], v[116:119], v[172:175]\n"
"v_cmp_eq_u32_e32 vcc, -1, v255\n"
"v_add_f32_e32 v103, v103, v240\n"
"v_mfma_f32_16x16x32_bf16 v[176:179], a[248:251], v[116:119], v[176:179]\n"
"s_cbranch_vccnz .Lmk_q79s\n"
".Lmk_q79r:\n"
"v_mfma_f32_16x16x32_bf16 v[184:187], a[252:255], v[116:119], v[184:187]\n"
"; @B G2 a.3\n"
"v_mfma_f32_16x16x32_bf16 v[152:155], a[128:131], v[140:143], v[152:155]\n"
"v_cmp_u_f32_e32 vcc, v24, v24\n"
"v_mfma_f32_16x16x32_bf16 v[160:163], a[132:135], v[140:143], v[160:163]\n"
"s_cbranch_vccnz .Lmk_p78s\n"
".Lmk_p78r:\n"
"v_mfma_f32_16x16x32_bf16 v[164:167], a[136:139], v[140:143], v[164:167]\n"
"buffer_load_dwordx4 v[52:55], v0, s[8:11], s99 offen offset:1024 sc1\n"
"v_mfma_f32_16x16x32_bf16 v[100:103], a[140:143], v[140:143], v[100:103]\n"
"v_mfma_f32_16x16x32_bf16 v[152:155], a[144:147], v[124:127], v[152:155]\n"
"ds_write_b128 v3, v[24:27] offset:12288\n"
"v_mfma_f32_16x16x32_bf16 v[160:163], a[148:151], v[124:127], v[160:163]\n"
"buffer_load_dwordx4 v[24:27], v0, s[8:11], s99 offen offset:2048 sc1\n"
"v_mfma_f32_16x16x32_bf16 v[164:167], a[152:155], v[124:127], v[164:167]\n"
"ds_write_b128 v3, v[172:175] offset:13312\n"
"v_mfma_f32_16x16x32_bf16 v[100:103], a[156:159], v[124:127], v[100:103]\n"
"v_mfma_f32_16x16x32_bf16 v[152:155], a[160:163], v[36:39], v[152:155]\n"
"s_waitcnt vmcnt(28)\n"
"v_max_u32_e32 v255, v132, v133\n"
"v_mfma_f32_16x16x32_bf16 v[160:163], a[164:167], v[36:39], v[160:163]\n"
"v_max3_u32 v255, v255, v134, v135\n"
"v_max3_u32 v255, v255, v112, v113\n"
"v_mfma_f32_16x16x32_bf16 v[164:167], a[168:171], v[36:39], v[164:167]\n"
"v_max3_u32 v255, v255, v114, v115\n"
"s_waitcnt vmcnt(26)\n"
"v_mfma_f32_16x16x32_bf16 v[100:103], a[172:175], v[36:39], v[100:103]\n"
"v_max3_u32 v255, v255, v156, v157\n"
"v_max3_u32 v255, v255, v158, v159\n"
"v_mfma_f32_16x16x32_bf16 v[152:155], a[176:179], v[56:59], v[152:155]\n"
"v_max3_u32 v255, v255, v128, v129\n"
"v_max3_u32 v255, v255, v130, v131\n"
"v_mfma_f32_16x16x32_bf16 v[160:163], a[180:183], v[56:59], v[160:163]\n"
"v_cmp_eq_u32_e32 vcc, -1, v255\n"
"v_mfma_f32_16x16x32_bf16 v[164:167], a[184:187], v[56:59], v[164:167]\n"
"s_cbranch_vccnz .Lmk_q80s\n"
".Lmk_q80r:\n"
"v_mfma_f32_16x16x32_bf16 v[100:103], a[188:191], v[56:59], v[100:103]\n"
"v_mfma_f32_16x16x32_bf16 v[152:155], a[192:195], v[132:135], v[152:155]\n"
"ds_write_b128 v3, v[176:179] offset:14336\n"
"v_mfma_f32_16x16x32_bf16 v[160:163], a[196:199], v[132:135], v[160:163]\n"
"buffer_load_dwordx4 v[36:39], v0, s[8:11], s99 offen offset:3072 sc1\n"
"v_mfma_f32_16x16x32_bf16 v[164:167], a[200:203], v[132:135], v[164:167]\n"
"ds_write_b128 v3, v[184:187] offset:15360\n"
"v_mfma_f32_16x16x32_bf16 v[100:103], a[204:207], v[132:135], v[100:103]\n"
"buffer_load_dwordx4 v[56:59], v1, s[8:11], s99 offen sc1\n"
"v_mfma_f32_16x16x32_bf16 v[152:155], a[208:211], v[112:115], v[152:155]\n"
"v_mfma_f32_16x16x32_bf16 v[160:163], a[212:215], v[112:115], v[160:163]\n"
"s_waitcnt lgkmcnt(0)\n"
"s_barrier\n"
"v_mfma_f32_16x16x32_bf16 v[164:167], a[216:219], v[112:115], v[164:167]\n"
"buffer_load_dwordx4 v[76:79], v1, s[8:11], s99 offen offset:1024 sc1\n"
"v_mfma_f32_16x16x32_bf16 v[100:103], a[220:223], v[112:115], v[100:103]\n"
"ds_read_b128 v[84:87], v5\n"
"v_mfma_f32_16x16x32_bf16 v[152:155], a[224:227], v[156:159], v[152:155]\n"
"buffer_load_dwordx4 v[108:111], v1, s[8:11], s99 offen offset:2048 sc1\n"
"v_mfma_f32_16x16x32_bf16 v[160:163], a[228:231], v[156:159], v[160:163]\n"
"ds_read_b128 v[112:115], v6\n"
"ds_read_b128 v[116:119], v5 offset:1024\n"
"v_mfma_f32_16x16x32_bf16 v[164:167], a[232:235], v[156:159], v[164:167]\n"
"buffer_load_dwordx4 v[120:123], v1, s[8:11], s99 offen offset:3072 sc1\n"
"v_mfma_f32_16x16x32_bf16 v[100:103], a[236:239], v[156:159], v[100:103]\n"
"s_add_u32 s99, s53, 0x840000\n"
"ds_read_b128 v[124:127], v6 offset:1024\n"
"v_mfma_f32_16x16x32_bf16 v[152:155], a[240:243], v[128:131], v[152:155]\n"
"buffer_load_dwordx4 v[132:135], v0, s[8:11], s99 offen sc1\n"
"v_mfma_f32_16x16x32_bf16 v[160:163], a[244:247], v[128:131], v[160:163]\n"
"ds_read_b128 v[140:143], v5 offset:2048\n"
"ds_read_b128 v[144:147], v6 offset:2048\n"
"v_mfma_f32_16x16x32_bf16 v[164:167], a[248:251], v[128:131], v[164:167]\n"
"buffer_load_dwordx4 v[148:151], v0, s[8:11], s99 offen offset:1024 sc1\n"
"v_mfma_f32_16x16x32_bf16 v[100:103], a[252:255], v[128:131], v[100:103]\n"
"ds_read_b128 v[128:131], v5 offset:3072\n"
"ds_read_b128 v[156:159], v6 offset:3072\n"
"buffer_load_dwordx4 v[172:175], v0, s[8:11], s99 offen offset:2048 sc1\n"
"s_waitcnt lgkmcnt(4)\n"
"v_add_f32_e32 v84, v84, v112\n"
"v_add_f32_e32 v85, v85, v113\n"
"v_add_f32_e32 v86, v86, v114\n"
"v_add_f32_e32 v87, v87, v115\n"
"v_add_f32_e32 v116, v116, v124\n"
"v_add_f32_e32 v117, v117, v125\n"
"v_add_f32_e32 v118, v118, v126\n"
"v_add_f32_e32 v119, v119, v127\n"
"buffer_load_dwordx4 v[112:115], v0, s[8:11], s99 offen offset:3072 sc1\n"
"buffer_load_dwordx4 v[124:127], v1, s[8:11], s99 offen sc1\n"
"s_waitcnt lgkmcnt(0)\n"
"v_add_f32_e32 v140, v140, v144\n"
"v_add_f32_e32 v141, v141, v145\n"
"v_add_f32_e32 v142, v142, v146\n"
"v_add_f32_e32 v143, v143, v147\n"
"v_add_f32_e32 v128, v128, v156\n"
"v_add_f32_e32 v129, v129, v157\n"
"v_add_f32_e32 v130, v130, v158\n"
"v_add_f32_e32 v131, v131, v159\n"
"buffer_load_dwordx4 v[144:147], v1, s[8:11], s99 offen offset:1024 sc1\n"
"buffer_load_dwordx4 v[156:159], v1, s[8:11], s99 offen offset:2048 sc1\n"
"buffer_load_dwordx4 v[176:179], v1, s[8:11], s99 offen offset:3072 sc1\n"
"s_lshr_b32 s99, s79, 7\n"
"s_add_u32 s52, s52, 0x800000\n"
"s_add_u32 s53, s53, 0x800000\n"
"s_add_u32 s54, s54, 0x800000\n"
"s_add_u32 s55, s55, 0x800000\n"
"s_add_u32 s56, s56, 0x800000\n"
"s_add_u32 s57, s57, 0x800000\n"
"s_add_u32 s58, s58, 0x800000\n"
"s_add_u32 s59, s59, 0x800000\n"
"s_add_u32 s60, s60, 0x800000\n"
"s_add_u32 s61, s61, 0x800000\n"
"s_add_u32 s62, s62, 0x80000\n"
"s_add_u32 s63, s63, 0x80000\n"
"s_add_u32 s64, s64, 0x1000000\n"
"s_add_u32 s65, s65, s79\n"
"s_add_u32 s66, s66, s79\n"
"s_add_u32 s67, s67, 0x20000\n"
"s_add_u32 s68, s68, 0x20000\n"
"s_add_u32 s71, s71, s99\n"
"s_add_u32 s73, s73, s99\n"
"s_mov_b32 s0, 1\n"
"s_mov_b32 s10, 0\n"
"s_waitcnt vmcnt(15)\n"
"; @A G1 0.0\n"
"v_mfma_f32_16x16x32_bf16 v[184:187], a[0:3], v[72:75], 0\n"
"s_add_u32 s99, s62, 0x4000\n"
"v_mfma_f32_16x16x32_bf16 v[188:191], a[4:7], v[72:75], 0\n"
"buffer_load_dwordx2 v[254:255], v10, s[4:7], s99 offen sc1\n"
"v_mfma_f32_16x16x32_bf16 v[192:195], a[8:11], v[72:75], 0\n"
"buffer_load_dwordx2 v[252:253], v10, s[4:7], s99 offen offset:128 sc1\n"
"v_mfma_f32_16x16x32_bf16 v[196:199], a[12:15], v[72:75], 0\n"
"buffer_load_dwordx2 v[250:251], v10, s[4:7], s99 offen offset:256 sc1\n"
"v_mfma_f32_16x16x32_bf16 v[200:203], a[16:19], v[72:75], 0\n"
"s_add_u32 s98, s54, 0x40000\n"
"v_mov_b32_e32 v204, 0x3f803f80\n"
"v_mfma_f32_16x16x32_bf16 v[208:211], a[20:23], v[72:75], 0\n"
"s_waitcnt vmcnt(17)\n"
"v_mov_b32_e32 v205, 0x3f803f80\n"
"v_mfma_f32_16x16x32_bf16 v[184:187], a[24:27], v[52:55], v[184:187]\n"
"buffer_load_dwordx4 v[72:75], v0, s[4:7], s98 offen sc1\n"
"v_mfma_f32_16x16x32_bf16 v[188:191], a[28:31], v[52:55], v[188:191]\n"
"buffer_load_dwordx2 v[248:249], v10, s[4:7], s99 offen offset:384 sc1\n"
"v_mfma_f32_16x16x32_bf16 v[192:195], a[32:35], v[52:55], v[192:195]\n"
"v_mov_b32_e32 v206, 0x3f803f80\n"
"v_mov_b32_e32 v207, 0x3f803f80\n"
"v_mfma_f32_16x16x32_bf16 v[196:199], a[36:39], v[52:55], v[196:199]\n"
"v_add_f32_e32 v84, v152, v84\n"
"v_add_f32_e32 v85, v153, v85\n"
"v_mfma_f32_16x16x32_bf16 v[200:203], a[40:43], v[52:55], v[200:203]\n"
"buffer_load_dwordx4 v[212:215], v0, s[4:7], s98 offen offset:1024 sc1\n"
"v_mfma_f32_16x16x32_bf16 v[208:211], a[44:47], v[52:55], v[208:211]\n"
"s_waitcnt vmcnt(19)\n"
"v_add_f32_e32 v86, v154, v86\n"
"v_mfma_f32_16x16x32_bf16 v[184:187], a[48:51], v[24:27], v[184:187]\n"
"v_add_f32_e32 v87, v155, v87\n"
"v_add_f32_e32 v116, v160, v116\n"
"v_mfma_f32_16x16x32_bf16 v[188:191], a[52:55], v[24:27], v[188:191]\n"
"v_add_f32_e32 v117, v161, v117\n"
"v_add_f32_e32 v118, v162, v118\n"
"v_mfma_f32_16x16x32_bf16 v[192:195], a[56:59], v[24:27], v[192:195]\n"
"v_add_f32_e32 v119, v163, v119\n"
"v_cvt_pk_bf16_f32 v52, v84, v85\n"
"v_mfma_f32_16x16x32_bf16 v[196:199], a[60:63], v[24:27], v[196:199]\n"
"v_cvt_pk_bf16_f32 v53, v86, v87\n"
"v_cvt_pk_bf16_f32 v54, v116, v117\n"
"v_mfma_f32_16x16x32_bf16 v[200:203], a[64:67], v[24:27], v[200:203]\n"
"buffer_load_dwordx4 v[84:87], v0, s[4:7], s98 offen offset:2048 sc1\n"
"v_mfma_f32_16x16x32_bf16 v[208:211], a[68:71], v[24:27], v[208:211]\n"
"s_waitcnt vmcnt(19)\n"
"v_cvt_pk_bf16_f32 v55, v118, v119\n"
"v_mfma_f32_16x16x32_bf16 v[184:187], a[72:75], v[36:39], v[184:187]\n"
"buffer_store_dwordx4 v[52:55], v0, s[4:7], s61 offen sc1\n"
"v_mfma_f32_16x16x32_bf16 v[188:191], a[76:79], v[36:39], v[188:191]\n"
"ds_write_b128 v2, v[52:55]\n"
"v_mfma_f32_16x16x32_bf16 v[192:195], a[80:83], v[36:39], v[192:195]\n"
"v_add_f32_e32 v140, v164, v140\n"
"v_add_f32_e32 v141, v165, v141\n"
"v_mfma_f32_16x16x32_bf16 v[196:199], a[84:87], v[36:39], v[196:199]\n"
"v_add_f32_e32 v142, v166, v142\n"
"v_add_f32_e32 v143, v167, v143\n"
"v_mfma_f32_16x16x32_bf16 v[200:203], a[88:91], v[36:39], v[200:203]\n"
"buffer_load_dwordx4 v[24:27], v0, s[4:7], s98 offen offset:3072 sc1\n"
"v_mfma_f32_16x16x32_bf16 v[208:211], a[92:95], v[36:39], v[208:211]\n"
"s_waitcnt vmcnt(20)\n"
"v_add_f32_e32 v128, v100, v128\n"
"v_mfma_f32_16x16x32_bf16 v[184:187], a[96:99], v[56:59], v[184:187]\n"
"v_add_f32_e32 v129, v101, v129\n"
"v_add_f32_e32 v130, v102, v130\n"
"v_mfma_f32_16x16x32_bf16 v[188:191], a[100:103], v[56:59], v[188:191]\n"
"v_add_f32_e32 v131, v103, v131\n"
"v_cvt_pk_bf16_f32 v36, v140, v141\n"
"v_mfma_f32_16x16x32_bf16 v[192:195], a[104:107], v[56:59], v[192:195]\n"
"v_cvt_pk_bf16_f32 v37, v142, v143\n"
"v_cvt_pk_bf16_f32 v38, v128, v129\n"
"v_mfma_f32_16x16x32_bf16 v[196:199], a[108:111], v[56:59], v[196:199]\n"
"v_cvt_pk_bf16_f32 v39, v130, v131\n"
"v_mfma_f32_16x16x32_bf16 v[200:203], a[112:115], v[56:59], v[200:203]\n"
"buffer_load_dwordx4 v[100:103], v1, s[4:7], s98 offen sc1\n"
"v_mfma_f32_16x16x32_bf16 v[208:211], a[116:119], v[56:59], v[208:211]\n"
"s_waitcnt vmcnt(20)\n"
"v_mfma_f32_16x16x32_bf16 v[184:187], a[120:123], v[76:79], v[184:187]\n"
"buffer_store_dwordx4 v[36:39], v0, s[4:7], s61 offen offset:1024 sc1\n"
"v_mfma_f32_16x16x32_bf16 v[188:191], a[124:127], v[76:79], v[188:191]\n"
"ds_write_b128 v2, v[36:39] offset:1024\n"
"v_mfma_f32_16x16x32_bf16 v[56:59], v[204:207], v[52:55], 0\n"
"v_mfma_f32_16x16x32_bf16 v[116:119], v[52:55], v[52:55], 0\n"
"v_mfma_f32_16x16x32_bf16 v[56:59], v[204:207], v[36:39], v[56:59]\n"
"v_mfma_f32_16x16x32_bf16 v[116:119], v[36:39], v[36:39], v[116:119]\n"
"v_mfma_f32_16x16x32_bf16 v[192:195], v[40:43], v[76:79], v[192:195]\n"
"s_mov_b32 s96, 0xaaaaaaaa\n"
"s_mov_b32 s97, 0xaaaaaaaa\n"
"v_mfma_f32_16x16x32_bf16 v[196:199], v[44:47], v[76:79], v[196:199]\n"
"v_mfma_f32_16x16x32_bf16 v[200:203], v[80:83], v[76:79], v[200:203]\n"
"buffer_load_dwordx4 v[36:39], v1, s[4:7], s98 offen offset:1024 sc1\n"
"v_mfma_f32_16x16x32_bf16 v[208:211], v[68:71], v[76:79], v[208:211]\n"
"s_waitcnt vmcnt(21)\n"
"v_cndmask_b32_e64 v116, v116, v117, s[96:97]\n"
"v_mfma_f32_16x16x32_bf16 v[184:187], v[88:91], v[108:111], v[184:187]\n"
"v_cndmask_b32_e64 v118, v118, v119, s[96:97]\n"
"v_mfma_f32_16x16x32_bf16 v[188:191], v[48:51], v[108:111], v[188:191]\n"
"s_mov_b32 s96, 0xcccccccc\n"
"s_mov_b32 s97, 0xcccccccc\n"
"v_mfma_f32_16x16x32_bf16 v[192:195], v[92:95], v[108:111], v[192:195]\n"
"v_cndmask_b32_e64 v57, v116, v118, s[96:97]\n"
"v_mfma_f32_16x16x32_bf16 v[196:199], v[96:99], v[108:111], v[196:199]\n"
"buffer_store_dwordx2 v[56:57], v14, s[4:7], s63 offen sc1\n"
"v_mfma_f32_16x16x32_bf16 v[200:203], v[168:171], v[108:111], v[200:203]\n"
"buffer_load_dwordx4 v[52:55], v1, s[4:7], s98 offen offset:2048 sc1\n"
"v_mfma_f32_16x16x32_bf16 v[208:211], v[104:107], v[108:111], v[208:211]\n"
"s_waitcnt vmcnt(22)\n"
"v_mfma_f32_16x16x32_bf16 v[184:187], v[136:139], v[120:123], v[184:187]\n"
"v_mfma_f32_16x16x32_bf16 v[188:191], v[180:183], v[120:123], v[188:191]\n"
"v_mfma_f32_16x16x32_bf16 v[192:195], v[28:31], v[120:123], v[192:195]\n"
"v_mfma_f32_16x16x32_bf16 v[196:199], v[32:35], v[120:123], v[196:199]\n"
"v_mfma_f32_16x16x32_bf16 v[200:203], v[60:63], v[120:123], v[200:203]\n"
"buffer_load_dwordx4 v[56:59], v1, s[4:7], s98 offen offset:3072 sc1\n"
"v_mfma_f32_16x16x32_bf16 v[208:211], v[64:67], v[120:123], v[208:211]\n"
"s_nop 1\n"
"v_cmp_u_f32_e32 vcc, v184, v184\n"
"s_waitcnt vmcnt(21)\n"
"; @A G1 0.1\n"
"v_mfma_f32_16x16x32_bf16 v[76:79], a[0:3], v[132:135], 0\n"
"s_cbranch_vccnz .Lmk_p84s\n"
".Lmk_p84r:\n"
"v_mfma_f32_16x16x32_bf16 v[108:111], a[4:7], v[132:135], 0\n"
"v_mfma_f32_16x16x32_bf16 v[116:119], a[8:11], v[132:135], 0\n"
"v_mfma_f32_16x16x32_bf16 v[120:123], a[12:15], v[132:135], 0\n"
"ds_write_b128 v3, v[184:187]\n"
"v_mfma_f32_16x16x32_bf16 v[128:131], a[16:19], v[132:135], 0\n"
"s_add_u32 s99, s55, 0x40000\n"
"v_mfma_f32_16x16x32_bf16 v[140:143], a[20:23], v[132:135], 0\n"
"buffer_load_dwordx4 v[132:135], v0, s[4:7], s99 offen sc1\n"
"v_mfma_f32_16x16x32_bf16 v[76:79], a[24:27], v[148:151], v[76:79]\n"
"ds_write_b128 v3, v[188:191] offset:1024\n"
"v_mfma_f32_16x16x32_bf16 v[108:111], a[28:31], v[148:151], v[108:111]\n"
"v_mfma_f32_16x16x32_bf16 v[116:119], a[32:35], v[148:151], v[116:119]\n"
"v_mfma_f32_16x16x32_bf16 v[120:123], a[36:39], v[148:151], v[120:123]\n"
"ds_write_b128 v3, v[192:195] offset:2048\n"
"v_mfma_f32_16x16x32_bf16 v[128:131], a[40:43], v[148:151], v[128:131]\n"
"buffer_load_dwordx4 v[152:155], v0, s[4:7], s99 offen offset:1024 sc1\n"
"v_mfma_f32_16x16x32_bf16 v[140:143], a[44:47], v[148:151], v[140:143]\n"
"s_waitcnt vmcnt(21)\n"
"v_mfma_f32_16x16x32_bf16 v[76:79], a[48:51], v[172:175], v[76:79]\n"
"ds_write_b128 v3, v[196:199] offset:3072\n"
"v_mfma_f32_16x16x32_bf16 v[108:111], a[52:55], v[172:175], v[108:111]\n"
"v_mfma_f32_16x16x32_bf16 v[116:119], a[56:59], v[172:175], v[116:119]\n"
"v_mfma_f32_16x16x32_bf16 v[120:123], a[60:63], v[172:175], v[120:123]\n"
"ds_write_b128 v3, v[200:203] offset:4096\n"
"v_mfma_f32_16x16x32_bf16 v[128:131], a[64:67], v[172:175], v[128:131]\n"
"buffer_load_dwordx4 v[148:151], v0, s[4:7], s99 offen offset:2048 sc1\n"
"v_mfma_f32_16x16x32_bf16 v[140:143], a[68:71], v[172:175], v[140:143]\n"
"v_mfma_f32_16x16x32_bf16 v[76:79], a[72:75], v[112:115], v[76:79]\n"
"ds_write_b128 v3, v[208:211] offset:5120\n"
"v_mfma_f32_16x16x32_bf16 v[108:111], a[76:79], v[112:115], v[108:111]\n"
"s_waitcnt vmcnt(13)\n"
"v_add_f32_e32 v254, v254, v252\n"
"v_mfma_f32_16x16x32_bf16 v[116:119], a[80:83], v[112:115], v[116:119]\n"
"v_add_f32_e32 v255, v255, v253\n"
"v_add_f32_e32 v250, v250, v248\n"
"v_mfma_f32_16x16x32_bf16 v[120:123], a[84:87], v[112:115], v[120:123]\n"
"v_add_f32_e32 v251, v251, v249\n"
"v_add_f32_e32 v254, v254, v250\n"
"v_mfma_f32_16x16x32_bf16 v[128:131], a[88:91], v[112:115], v[128:131]\n"
"buffer_load_dwordx4 v[160:163], v0, s[4:7], s99 offen offset:3072 sc1\n"
"v_mfma_f32_16x16x32_bf16 v[140:143], a[92:95], v[112:115], v[140:143]\n"
"v_add_f32_e32 v255, v255, v251\n"
"v_mfma_f32_16x16x32_bf16 v[76:79], a[96:99], v[124:127], v[76:79]\n"
"v_cmp_u_f32_e32 vcc, v254, v255\n"
"v_mfma_f32_16x16x32_bf16 v[108:111], a[100:103], v[124:127], v[108:111]\n"
"s_cbranch_vccnz .Lmk_m89s\n"
".Lmk_m89r:\n"
"v_mfma_f32_16x16x32_bf16 v[116:119], a[104:107], v[124:127], v[116:119]\n"
"v_permlane16_swap_b32_e32 v254, v255\n"
"v_add_f32_e32 v254, v254, v255\n"
"v_mfma_f32_16x16x32_bf16 v[120:123], a[108:111], v[124:127], v[120:123]\n"
"v_mov_b32_e32 v255, v254\n"
"v_mfma_f32_16x16x32_bf16 v[128:131], a[112:115], v[124:127], v[128:131]\n"
"buffer_load_dwordx4 v[112:115], v1, s[4:7], s99 offen sc1\n"
"v_mfma_f32_16x16x32_bf16 v[140:143], a[116:119], v[124:127], v[140:143]\n"
"s_waitcnt lgkmcnt(0)\n"
"s_barrier\n"
"v_mfma_f32_16x16x32_bf16 v[76:79], a[120:123], v[144:147], v[76:79]\n"
"v_permlane32_swap_b32_e32 v254, v255\n"
"v_add_f32_e32 v254, v254, v255\n"
"v_mfma_f32_16x16x32_bf16 v[108:111], a[124:127], v[144:147], v[108:111]\n"
"v_mul_f32_e32 v254, 0x3a800000, v254\n"
"v_mov_b32_e32 v255, v254\n"
"v_mfma_f32_16x16x32_bf16 v[116:119], v[40:43], v[144:147], v[116:119]\n"
"s_nop 0\n"
"v_permlane16_swap_b32_e32 v254, v255\n"
"v_cvt_pk_bf16_f32 v253, -v254, 0\n"
"v_mfma_f32_16x16x32_bf16 v[120:123], v[44:47], v[144:147], v[120:123]\n"
"v_fma_f32 v255, -v254, v254, v255\n"
"v_lshlrev_b32_e32 v253, 16, v253\n"
"v_mfma_f32_16x16x32_bf16 v[128:131], v[80:83], v[144:147], v[128:131]\n"
"buffer_load_dwordx4 v[124:127], v1, s[4:7], s99 offen offset:1024 sc1\n"
"v_mfma_f32_16x16x32_bf16 v[140:143], v[68:71], v[144:147], v[140:143]\n"
"ds_read_b128 v[144:147], v4\n"
"v_mfma_f32_16x16x32_bf16 v[76:79], v[88:91], v[156:159], v[76:79]\n"
"ds_read_b128 v[164:167], v9\n"
"ds_read_b128 v[172:175], v4 offset:1024\n"
"v_mfma_f32_16x16x32_bf16 v[108:111], v[48:51], v[156:159], v[108:111]\n"
"ds_read_b128 v[184:187], v9 offset:64\n"
"ds_read_b128 v[188:191], v4 offset:2048\n"
"v_mfma_f32_16x16x32_bf16 v[116:119], v[92:95], v[156:159], v[116:119]\n"
"ds_read_b128 v[192:195], v9 offset:128\n"
"v_max_f32_e32 v255, 0, v255\n"
"v_mfma_f32_16x16x32_bf16 v[120:123], v[96:99], v[156:159], v[120:123]\n"
"v_cvt_pk_bf16_f32 v255, v255, 0\n"
"v_lshlrev_b32_e32 v255, 16, v255\n"
"v_mfma_f32_16x16x32_bf16 v[128:131], v[168:171], v[156:159], v[128:131]\n"
"buffer_load_dwordx4 v[196:199], v1, s[4:7], s99 offen offset:2048 sc1\n"
"v_mfma_f32_16x16x32_bf16 v[140:143], v[104:107], v[156:159], v[140:143]\n"
"v_add_f32_e32 v255, 0x3727c5ac, v255\n"
"v_cvt_pk_bf16_f32 v255, v255, 0\n"
"v_mfma_f32_16x16x32_bf16 v[76:79], v[136:139], v[176:179], v[76:79]\n"
"v_lshlrev_b32_e32 v255, 16, v255\n"
"v_mfma_f32_16x16x32_bf16 v[108:111], v[180:183], v[176:179], v[108:111]\n"
"s_waitcnt lgkmcnt(0)\n"
"v_fmac_f32_e32 v144, v253, v164\n"
"v_mfma_f32_16x16x32_bf16 v[116:119], v[28:31], v[176:179], v[116:119]\n"
"v_fmac_f32_e32 v145, v253, v165\n"
"v_fmac_f32_e32 v146, v253, v166\n"
"v_mfma_f32_16x16x32_bf16 v[120:123], v[32:35], v[176:179], v[120:123]\n"
"v_fmac_f32_e32 v147, v253, v167\n"
"v_fmac_f32_e32 v172, v253, v184\n"
"v_mfma_f32_16x16x32_bf16 v[128:131], v[60:63], v[176:179], v[128:131]\n"
"buffer_load_dwordx4 v[156:159], v1, s[4:7], s99 offen offset:3072 sc1\n"
"v_mfma_f32_16x16x32_bf16 v[140:143], v[64:67], v[176:179], v[140:143]\n"
"v_cmp_u_f32_e32 vcc, v76, v76\n"
"s_waitcnt vmcnt(17)\n"
"; @A G1 0.2\n"
"v_mfma_f32_16x16x32_bf16 v[164:167], a[0:3], v[72:75], 0\n"
"s_cbranch_vccnz .Lmk_p85s\n"
".Lmk_p85r:\n"
"v_fmac_f32_e32 v173, v253, v185\n"
"v_mfma_f32_16x16x32_bf16 v[176:179], a[4:7], v[72:75], 0\n"
"v_fmac_f32_e32 v174, v253, v186\n"
"v_fmac_f32_e32 v175, v253, v187\n"
"v_mfma_f32_16x16x32_bf16 v[184:187], a[8:11], v[72:75], 0\n"
"v_fmac_f32_e32 v188, v253, v192\n"
"v_fmac_f32_e32 v189, v253, v193\n"
"v_mfma_f32_16x16x32_bf16 v[200:203], a[12:15], v[72:75], 0\n"
"ds_write_b128 v3, v[76:79] offset:6144\n"
"v_mfma_f32_16x16x32_bf16 v[76:79], a[16:19], v[72:75], 0\n"
"s_add_u32 s99, s52, 0x48000\n"
"v_fmac_f32_e32 v190, v253, v194\n"
"v_mfma_f32_16x16x32_bf16 v[204:207], a[20:23], v[72:75], 0\n"
"buffer_load_dwordx4 v[72:75], v0, s[4:7], s99 offen sc1\n"
"v_mfma_f32_16x16x32_bf16 v[164:167], a[24:27], v[212:215], v[164:167]\n"
"ds_write_b128 v3, v[108:111] offset:7168\n"
"v_mfma_f32_16x16x32_bf16 v[176:179], a[28:31], v[212:215], v[176:179]\n"
"v_fmac_f32_e32 v191, v253, v195\n"
"ds_read_b128 v[108:111], v4 offset:3072\n"
"v_mfma_f32_16x16x32_bf16 v[184:187], a[32:35], v[212:215], v[184:187]\n"
"ds_read_b128 v[192:195], v9 offset:192\n"
"ds_read_b128 v[208:211], v4 offset:4096\n"
"v_mfma_f32_16x16x32_bf16 v[200:203], a[36:39], v[212:215], v[200:203]\n"
"ds_write_b128 v3, v[116:119] offset:8192\n"
"v_mfma_f32_16x16x32_bf16 v[76:79], a[40:43], v[212:215], v[76:79]\n"
"buffer_load_dwordx4 v[116:119], v0, s[4:7], s99 offen offset:1024 sc1\n"
"v_mfma_f32_16x16x32_bf16 v[204:207], a[44:47], v[212:215], v[204:207]\n"
"s_waitcnt vmcnt(16)\n"
"ds_read_b128 v[212:215], v9 offset:256\n"
"v_mfma_f32_16x16x32_bf16 v[164:167], a[48:51], v[84:87], v[164:167]\n"
"ds_write_b128 v3, v[120:123] offset:9216\n"
"v_mfma_f32_16x16x32_bf16 v[176:179], a[52:55], v[84:87], v[176:179]\n"
"ds_read_b128 v[120:123], v4 offset:5120\n"
"ds_read_b128 v[216:219], v9 offset:320\n"
"v_mfma_f32_16x16x32_bf16 v[184:187], a[56:59], v[84:87], v[184:187]\n"
"v_sqrt_f32_e32 v255, v255\n"
"v_mfma_f32_16x16x32_bf16 v[200:203], a[60:63], v[84:87], v[200:203]\n"
"ds_write_b128 v3, v[128:131] offset:10240\n"
"v_mfma_f32_16x16x32_bf16 v[76:79], a[64:67], v[84:87], v[76:79]\n"
"buffer_load_dwordx4 v[128:131], v0, s[4:7], s99 offen offset:2048 sc1\n"
"v_mfma_f32_16x16x32_bf16 v[204:207], a[68:71], v[84:87], v[204:207]\n"
"v_cvt_pk_bf16_f32 v255, v255, 0\n"
"v_lshlrev_b32_e32 v255, 16, v255\n"
"v_mfma_f32_16x16x32_bf16 v[164:167], a[72:75], v[24:27], v[164:167]\n"
"ds_write_b128 v3, v[140:143] offset:11264\n"
"v_mfma_f32_16x16x32_bf16 v[176:179], a[76:79], v[24:27], v[176:179]\n"
"s_waitcnt lgkmcnt(2)\n"
"v_fmac_f32_e32 v108, v253, v192\n"
"v_mfma_f32_16x16x32_bf16 v[184:187], a[80:83], v[24:27], v[184:187]\n"
"v_fmac_f32_e32 v109, v253, v193\n"
"v_fmac_f32_e32 v110, v253, v194\n"
"v_mfma_f32_16x16x32_bf16 v[200:203], a[84:87], v[24:27], v[200:203]\n"
"v_fmac_f32_e32 v111, v253, v195\n"
"v_fmac_f32_e32 v208, v253, v212\n"
"v_mfma_f32_16x16x32_bf16 v[76:79], a[88:91], v[24:27], v[76:79]\n"
"buffer_load_dwordx4 v[84:87], v0, s[4:7], s99 offen offset:3072 sc1\n"
"v_mfma_f32_16x16x32_bf16 v[204:207], a[92:95], v[24:27], v[204:207]\n"
"s_waitcnt vmcnt(15)\n"
"v_fmac_f32_e32 v209, v253, v213\n"
"v_mfma_f32_16x16x32_bf16 v[164:167], a[96:99], v[100:103], v[164:167]\n"
"v_fmac_f32_e32 v210, v253, v214\n"
"v_fmac_f32_e32 v211, v253, v215\n"
"v_mfma_f32_16x16x32_bf16 v[176:179], a[100:103], v[100:103], v[176:179]\n"
"v_fmac_f32_e32 v120, v253, v216\n"
"v_fmac_f32_e32 v121, v253, v217\n"
"v_mfma_f32_16x16x32_bf16 v[184:187], a[104:107], v[100:103], v[184:187]\n"
"v_fmac_f32_e32 v122, v253, v218\n"
"v_fmac_f32_e32 v123, v253, v219\n"
"v_mfma_f32_16x16x32_bf16 v[200:203], a[108:111], v[100:103], v[200:203]\n"
"v_rcp_f32_e32 v253, v255\n"
"v_mfma_f32_16x16x32_bf16 v[76:79], a[112:115], v[100:103], v[76:79]\n"
"buffer_load_dwordx4 v[24:27], v1, s[4:7], s99 offen sc1\n"
"v_mfma_f32_16x16x32_bf16 v[204:207], a[116:119], v[100:103], v[204:207]\n"
"v_mul_f32_e32 v255, 0x3e8293ee, v253\n"
"v_mfma_f32_16x16x32_bf16 v[164:167], a[120:123], v[36:39], v[164:167]\n"
"v_mfma_f32_16x16x32_bf16 v[176:179], a[124:127], v[36:39], v[176:179]\n"
"v_mfma_f32_16x16x32_bf16 v[184:187], v[40:43], v[36:39], v[184:187]\n"
"v_mfma_f32_16x16x32_bf16 v[200:203], v[44:47], v[36:39], v[200:203]\n"
"v_mfma_f32_16x16x32_bf16 v[76:79], v[80:83], v[36:39], v[76:79]\n"
"buffer_load_dwordx4 v[100:103], v1, s[4:7], s99 offen offset:1024 sc1\n"
"v_mfma_f32_16x16x32_bf16 v[204:207], v[68:71], v[36:39], v[204:207]\n"
"s_waitcnt vmcnt(14)\n"
"v_mfma_f32_16x16x32_bf16 v[164:167], v[88:91], v[52:55], v[164:167]\n"
"v_mfma_f32_16x16x32_bf16 v[176:179], v[48:51], v[52:55], v[176:179]\n"
"v_mfma_f32_16x16x32_bf16 v[184:187], v[92:95], v[52:55], v[184:187]\n"
"v_mfma_f32_16x16x32_bf16 v[200:203], v[96:99], v[52:55], v[200:203]\n"
"v_mfma_f32_16x16x32_bf16 v[76:79], v[168:171], v[52:55], v[76:79]\n"
"s_waitcnt vmcnt(12)\n"
"v_max_u32_e32 v254, v132, v133\n"
"v_mfma_f32_16x16x32_bf16 v[204:207], v[104:107], v[52:55], v[204:207]\n"
"v_max3_u32 v254, v254, v134, v135\n"
"v_max3_u32 v254, v254, v152, v153\n"
"v_mfma_f32_16x16x32_bf16 v[164:167], v[136:139], v[56:59], v[164:167]\n"
"v_max3_u32 v254, v254, v154, v155\n"
"s_waitcnt vmcnt(10)\n"
"v_mfma_f32_16x16x32_bf16 v[176:179], v[180:183], v[56:59], v[176:179]\n"
"v_max3_u32 v254, v254, v148, v149\n"
"v_max3_u32 v254, v254, v150, v151\n"
"v_mfma_f32_16x16x32_bf16 v[184:187], v[28:31], v[56:59], v[184:187]\n"
"v_max3_u32 v254, v254, v160, v161\n"
"v_max3_u32 v254, v254, v162, v163\n"
"v_mfma_f32_16x16x32_bf16 v[200:203], v[32:35], v[56:59], v[200:203]\n"
"v_cmp_eq_u32_e32 vcc, -1, v254\n"
"s_add_u32 s98, s68, s80\n"
"v_mfma_f32_16x16x32_bf16 v[76:79], v[60:63], v[56:59], v[76:79]\n"
"s_cbranch_vccnz .Lmk_q87s\n"
".Lmk_q87r:\n"
"v_mfma_f32_16x16x32_bf16 v[204:207], v[64:67], v[56:59], v[204:207]\n"
"v_cmp_u_f32_e32 vcc, v164, v164\n"
"; @A G1 0.3\n"
"v_mfma_f32_16x16x32_bf16 v[144:147], a[0:3], v[132:135], v[144:147]\n"
"s_cbranch_vccnz .Lmk_p86s\n"
".Lmk_p86r:\n"
"v_mfma_f32_16x16x32_bf16 v[172:175], a[4:7], v[132:135], v[172:175]\n"
"buffer_load_dwordx4 v[36:39], v1, s[4:7], s99 offen offset:2048 sc1\n"
"v_mfma_f32_16x16x32_bf16 v[188:191], a[8:11], v[132:135], v[188:191]\n"
"buffer_load_dwordx4 v[52:55], v1, s[4:7], s99 offen offset:3072 sc1\n"
"v_mfma_f32_16x16x32_bf16 v[108:111], a[12:15], v[132:135], v[108:111]\n"
"ds_write_b128 v3, v[164:167] offset:12288\n"
"v_mfma_f32_16x16x32_bf16 v[208:211], a[16:19], v[132:135], v[208:211]\n"
"s_add_u32 s99, s53, 0x48000\n"
"v_mfma_f32_16x16x32_bf16 v[120:123], a[20:23], v[132:135], v[120:123]\n"
"buffer_load_dwordx4 v[56:59], v0, s[4:7], s99 offen sc1\n"
"v_mfma_f32_16x16x32_bf16 v[144:147], a[24:27], v[152:155], v[144:147]\n"
"ds_write_b128 v3, v[176:179] offset:13312\n"
"v_mfma_f32_16x16x32_bf16 v[172:175], a[28:31], v[152:155], v[172:175]\n"
"buffer_store_dwordx2 v[18:19], v11, s[4:7], s98 offen sc1\n"
"v_mfma_f32_16x16x32_bf16 v[188:191], a[32:35], v[152:155], v[188:191]\n"
"v_mfma_f32_16x16x32_bf16 v[108:111], a[36:39], v[152:155], v[108:111]\n"
"ds_write_b128 v3, v[184:187] offset:14336\n"
"v_mfma_f32_16x16x32_bf16 v[208:211], a[40:43], v[152:155], v[208:211]\n"
"buffer_load_dwordx4 v[132:135], v0, s[4:7], s99 offen offset:1024 sc1\n"
"v_mfma_f32_16x16x32_bf16 v[120:123], a[44:47], v[152:155], v[120:123]\n"
"v_mfma_f32_16x16x32_bf16 v[144:147], a[48:51], v[148:151], v[144:147]\n"
"ds_write_b128 v3, v[200:203] offset:15360\n"
"v_mfma_f32_16x16x32_bf16 v[172:175], a[52:55], v[148:151], v[172:175]\n"
"v_mfma_f32_16x16x32_bf16 v[188:191], a[56:59], v[148:151], v[188:191]\n"
"v_mfma_f32_16x16x32_bf16 v[108:111], a[60:63], v[148:151], v[108:111]\n"
"ds_write_b128 v3, v[76:79] offset:16384\n"
"v_mfma_f32_16x16x32_bf16 v[208:211], a[64:67], v[148:151], v[208:211]\n"
"s_waitcnt vmcnt(13)\n"
"v_max_u32_e32 v254, v112, v113\n"
"v_mfma_f32_16x16x32_bf16 v[120:123], a[68:71], v[148:151], v[120:123]\n"
"v_max3_u32 v254, v254, v114, v115\n"
"v_max3_u32 v254, v254, v124, v125\n"
"v_mfma_f32_16x16x32_bf16 v[144:147], a[72:75], v[160:163], v[144:147]\n"
"v_max3_u32 v254, v254, v126, v127\n"
"s_waitcnt vmcnt(11)\n"
"v_mfma_f32_16x16x32_bf16 v[172:175], a[76:79], v[160:163], v[172:175]\n"
"v_max3_u32 v254, v254, v196, v197\n"
"v_max3_u32 v254, v254, v198, v199\n"
"v_mfma_f32_16x16x32_bf16 v[188:191], a[80:83], v[160:163], v[188:191]\n"
"v_max3_u32 v254, v254, v156, v157\n"
"v_max3_u32 v254, v254, v158, v159\n"
"v_mfma_f32_16x16x32_bf16 v[108:111], a[84:87], v[160:163], v[108:111]\n"
"v_cmp_eq_u32_e32 vcc, -1, v254\n"
"v_mfma_f32_16x16x32_bf16 v[208:211], a[88:91], v[160:163], v[208:211]\n"
"s_cbranch_vccnz .Lmk_q88s\n"
".Lmk_q88r:\n"
"v_mfma_f32_16x16x32_bf16 v[120:123], a[92:95], v[160:163], v[120:123]\n"
"v_mfma_f32_16x16x32_bf16 v[144:147], a[96:99], v[112:115], v[144:147]\n"
"buffer_load_dwordx4 v[76:79], v0, s[4:7], s99 offen offset:2048 sc1\n"
"v_mfma_f32_16x16x32_bf16 v[172:175], a[100:103], v[112:115], v[172:175]\n"
"buffer_load_dwordx4 v[140:143], v0, s[4:7], s99 offen offset:3072 sc1\n"
"v_mfma_f32_16x16x32_bf16 v[188:191], a[104:107], v[112:115], v[188:191]\n"
"ds_write_b128 v3, v[204:207] offset:17408\n"
"v_mfma_f32_16x16x32_bf16 v[108:111], a[108:111], v[112:115], v[108:111]\n"
"v_mfma_f32_16x16x32_bf16 v[208:211], a[112:115], v[112:115], v[208:211]\n"
"buffer_load_dwordx4 v[148:151], v1, s[4:7], s99 offen sc1\n"
"v_mfma_f32_16x16x32_bf16 v[120:123], a[116:119], v[112:115], v[120:123]\n"
"s_waitcnt lgkmcnt(0)\n"
"s_barrier\n"
"v_mfma_f32_16x16x32_bf16 v[144:147], a[120:123], v[124:127], v[144:147]\n"
"ds_read_b128 v[112:115], v5 offset:2048\n"
"ds_read_b128 v[152:155], v6 offset:2048\n"
"v_mfma_f32_16x16x32_bf16 v[172:175], a[124:127], v[124:127], v[172:175]\n"
"ds_read_b128 v[160:163], v5 offset:3072\n"
"ds_read_b128 v[164:167], v6 offset:3072\n"
"v_mfma_f32_16x16x32_bf16 v[188:191], v[40:43], v[124:127], v[188:191]\n"
"v_mfma_f32_16x16x32_bf16 v[108:111], v[44:47], v[124:127], v[108:111]\n"
"v_mfma_f32_16x16x32_bf16 v[208:211], v[80:83], v[124:127], v[208:211]\n"
"buffer_load_dwordx4 v[176:179], v1, s[4:7], s99 offen offset:1024 sc1\n"
"v_mfma_f32_16x16x32_bf16 v[120:123], v[68:71], v[124:127], v[120:123]\n"
"ds_read_b128 v[124:127], v5 offset:4096\n"
"v_mfma_f32_16x16x32_bf16 v[144:147], v[88:91], v[196:199], v[144:147]\n"
"ds_read_b128 v[184:187], v6 offset:4096\n"
"ds_read_b128 v[192:195], v5 offset:5120\n"
"v_mfma_f32_16x16x32_bf16 v[172:175], v[48:51], v[196:199], v[172:175]\n"
"ds_read_b128 v[200:203], v6 offset:5120\n"
"s_waitcnt lgkmcnt(4)\n"
"v_mfma_f32_16x16x32_bf16 v[188:191], v[92:95], v[196:199], v[188:191]\n"
"v_add_f32_e32 v112, v112, v152\n"
"v_add_f32_e32 v113, v113, v153\n"
"v_mfma_f32_16x16x32_bf16 v[108:111], v[96:99], v[196:199], v[108:111]\n"
"v_add_f32_e32 v114, v114, v154\n"
"v_add_f32_e32 v115, v115, v155\n"
"v_mfma_f32_16x16x32_bf16 v[208:211], v[168:171], v[196:199], v[208:211]\n"
"buffer_load_dwordx4 v[152:155], v1, s[4:7], s99 offen offset:2048 sc1\n"
"v_mfma_f32_16x16x32_bf16 v[120:123], v[104:107], v[196:199], v[120:123]\n"
"ds_read_b128 v[196:199], v5\n"
"ds_read_b128 v[204:207], v6\n"
"v_mfma_f32_16x16x32_bf16 v[144:147], v[136:139], v[156:159], v[144:147]\n"
"ds_read_b128 v[212:215], v5 offset:1024\n"
"ds_read_b128 v[216:219], v6 offset:1024\n"
"v_mfma_f32_16x16x32_bf16 v[172:175], v[180:183], v[156:159], v[172:175]\n"
"v_add_f32_e32 v160, v160, v164\n"
"v_add_f32_e32 v161, v161, v165\n"
"v_mfma_f32_16x16x32_bf16 v[188:191], v[28:31], v[156:159], v[188:191]\n"
"v_add_f32_e32 v162, v162, v166\n"
"v_add_f32_e32 v163, v163, v167\n"
"v_mfma_f32_16x16x32_bf16 v[108:111], v[32:35], v[156:159], v[108:111]\n"
"s_waitcnt lgkmcnt(4)\n"
"v_add_f32_e32 v124, v124, v184\n"
"v_mfma_f32_16x16x32_bf16 v[208:211], v[60:63], v[156:159], v[208:211]\n"
"buffer_load_dwordx4 v[164:167], v1, s[4:7], s99 offen offset:3072 sc1\n"
"v_mfma_f32_16x16x32_bf16 v[120:123], v[64:67], v[156:159], v[120:123]\n"
"v_add_f32_e32 v125, v125, v185\n"
"v_add_f32_e32 v126, v126, v186\n"
"v_add_f32_e32 v127, v127, v187\n"
"v_add_f32_e32 v192, v192, v200\n"
"v_add_f32_e32 v193, v193, v201\n"
"v_add_f32_e32 v194, v194, v202\n"
"v_add_f32_e32 v195, v195, v203\n"
"s_waitcnt lgkmcnt(0)\n"
"v_add_f32_e32 v196, v196, v204\n"
"v_add_f32_e32 v197, v197, v205\n"
"v_add_f32_e32 v198, v198, v206\n"
"v_add_f32_e32 v199, v199, v207\n"
"v_add_f32_e32 v212, v212, v216\n"
"v_add_f32_e32 v213, v213, v217\n"
"v_add_f32_e32 v214, v214, v218\n"
"v_add_f32_e32 v215, v215, v219\n"
"s_waitcnt vmcnt(15)\n"
"; @A G1 1.0\n"
"v_mfma_f32_16x16x32_bf16 v[156:159], a[0:3], v[72:75], 0\n"
"s_add_u32 s99, s62, 0x4800\n"
"v_mfma_f32_16x16x32_bf16 v[184:187], a[4:7], v[72:75], 0\n"
"buffer_load_dwordx2 v[250:251], v10, s[4:7], s99 offen sc1\n"
"v_mfma_f32_16x16x32_bf16 v[200:203], a[8:11], v[72:75], 0\n"
"buffer_load_dwordx2 v[248:249], v10, s[4:7], s99 offen offset:128 sc1\n"
"v_mfma_f32_16x16x32_bf16 v[204:207], a[12:15], v[72:75], 0\n"
"buffer_load_dwordx2 v[246:247], v10, s[4:7], s99 offen offset:256 sc1\n"
"v_mfma_f32_16x16x32_bf16 v[216:219], a[16:19], v[72:75], 0\n"
"s_add_u32 s98, s54, 0x48000\n"
"v_add_f32_e32 v188, v188, v112\n"
"v_mfma_f32_16x16x32_bf16 v[220:223], a[20:23], v[72:75], 0\n"
"buffer_load_dwordx4 v[72:75], v0, s[4:7], s98 offen sc1\n"
"v_mfma_f32_16x16x32_bf16 v[156:159], a[24:27], v[116:119], v[156:159]\n"
"buffer_load_dwordx2 v[244:245], v10, s[4:7], s99 offen offset:384 sc1\n"
"v_mfma_f32_16x16x32_bf16 v[184:187], a[28:31], v[116:119], v[184:187]\n"
"v_add_f32_e32 v189, v189, v113\n"
"v_add_f32_e32 v190, v190, v114\n"
"v_mfma_f32_16x16x32_bf16 v[200:203], a[32:35], v[116:119], v[200:203]\n"
"s_add_u32 s99, s70, 0\n"
"v_add_f32_e32 v191, v191, v115\n"
"v_mfma_f32_16x16x32_bf16 v[204:207], a[36:39], v[116:119], v[204:207]\n"
"buffer_load_dwordx4 a[128:131], v0, s[4:7], s99 offen sc1\n"
"v_mfma_f32_16x16x32_bf16 v[216:219], a[40:43], v[116:119], v[216:219]\n"
"buffer_load_dwordx4 v[112:115], v0, s[4:7], s98 offen offset:1024 sc1\n"
"v_mfma_f32_16x16x32_bf16 v[220:223], a[44:47], v[116:119], v[220:223]\n"
"s_waitcnt vmcnt(20)\n"
"v_add_f32_e32 v108, v108, v160\n"
"v_mfma_f32_16x16x32_bf16 v[156:159], a[48:51], v[128:131], v[156:159]\n"
"v_add_f32_e32 v109, v109, v161\n"
"v_add_f32_e32 v110, v110, v162\n"
"v_mfma_f32_16x16x32_bf16 v[184:187], a[52:55], v[128:131], v[184:187]\n"
"v_add_f32_e32 v111, v111, v163\n"
"v_mul_f32_e32 v188, v188, v253\n"
"v_mfma_f32_16x16x32_bf16 v[200:203], a[56:59], v[128:131], v[200:203]\n"
"v_mul_f32_e32 v189, v189, v253\n"
"v_mul_f32_e32 v190, v190, v253\n"
"v_mfma_f32_16x16x32_bf16 v[204:207], a[60:63], v[128:131], v[204:207]\n"
"v_mul_f32_e32 v191, v191, v253\n"
"v_mul_f32_e32 v108, v108, v253\n"
"v_mfma_f32_16x16x32_bf16 v[216:219], a[64:67], v[128:131], v[216:219]\n"
"buffer_load_dwordx4 v[116:119], v0, s[4:7], s98 offen offset:2048 sc1\n"
"v_mfma_f32_16x16x32_bf16 v[220:223], a[68:71], v[128:131], v[220:223]\n"
"v_mul_f32_e32 v109, v109, v253\n"
"v_mul_f32_e32 v110, v110, v253\n"
"v_mfma_f32_16x16x32_bf16 v[156:159], a[72:75], v[84:87], v[156:159]\n"
"v_mul_f32_e32 v111, v111, v253\n"
"v_cvt_pk_bf16_f32 v128, v188, v189\n"
"v_mfma_f32_16x16x32_bf16 v[184:187], a[76:79], v[84:87], v[184:187]\n"
"v_cvt_pk_bf16_f32 v129, v190, v191\n"
"v_cvt_pk_bf16_f32 v130, v108, v109\n"
"v_mfma_f32_16x16x32_bf16 v[200:203], a[80:83], v[84:87], v[200:203]\n"
"v_cvt_pk_bf16_f32 v131, v110, v111\n"
"v_mfma_f32_16x16x32_bf16 v[204:207], a[84:87], v[84:87], v[204:207]\n"
"ds_write_b128 v7, v[128:131] offset:24576\n"
"v_mfma_f32_16x16x32_bf16 v[216:219], a[88:91], v[84:87], v[216:219]\n"
"buffer_load_dwordx4 v[108:111], v0, s[4:7], s98 offen offset:3072 sc1\n"
"v_mfma_f32_16x16x32_bf16 v[220:223], a[92:95], v[84:87], v[220:223]\n"
"s_waitcnt vmcnt(20)\n"
"v_add_f32_e32 v208, v208, v124\n"
"v_mfma_f32_16x16x32_bf16 v[156:159], a[96:99], v[24:27], v[156:159]\n"
"v_add_f32_e32 v209, v209, v125\n"
"v_add_f32_e32 v210, v210, v126\n"
"v_mfma_f32_16x16x32_bf16 v[184:187], a[100:103], v[24:27], v[184:187]\n"
"v_add_f32_e32 v211, v211, v127\n"
"v_add_f32_e32 v120, v120, v192\n"
"v_mfma_f32_16x16x32_bf16 v[200:203], a[104:107], v[24:27], v[200:203]\n"
"v_add_f32_e32 v121, v121, v193\n"
"v_add_f32_e32 v122, v122, v194\n"
"v_mfma_f32_16x16x32_bf16 v[204:207], a[108:111], v[24:27], v[204:207]\n"
"v_add_f32_e32 v123, v123, v195\n"
"v_mul_f32_e32 v208, v208, v253\n"
"v_mfma_f32_16x16x32_bf16 v[216:219], a[112:115], v[24:27], v[216:219]\n"
"buffer_load_dwordx4 v[84:87], v1, s[4:7], s98 offen sc1\n"
"v_mfma_f32_16x16x32_bf16 v[220:223], a[116:119], v[24:27], v[220:223]\n"
"buffer_load_dwordx4 a[132:135], v0, s[4:7], s99 offen offset:1024 sc1\n"
"v_mfma_f32_16x16x32_bf16 v[156:159], a[120:123], v[100:103], v[156:159]\n"
"v_mul_f32_e32 v209, v209, v253\n"
"v_mul_f32_e32 v210, v210, v253\n"
"v_mfma_f32_16x16x32_bf16 v[184:187], a[124:127], v[100:103], v[184:187]\n"
"v_mul_f32_e32 v211, v211, v253\n"
"v_mul_f32_e32 v120, v120, v253\n"
"v_mfma_f32_16x16x32_bf16 v[200:203], v[40:43], v[100:103], v[200:203]\n"
"v_mul_f32_e32 v121, v121, v253\n"
"v_mul_f32_e32 v122, v122, v253\n"
"v_mfma_f32_16x16x32_bf16 v[204:207], v[44:47], v[100:103], v[204:207]\n"
"v_mul_f32_e32 v123, v123, v253\n"
"v_cvt_pk_bf16_f32 v24, v208, v209\n"
"v_mfma_f32_16x16x32_bf16 v[216:219], v[80:83], v[100:103], v[216:219]\n"
"buffer_load_dwordx4 v[124:127], v1, s[4:7], s98 offen offset:1024 sc1\n"
"v_mfma_f32_16x16x32_bf16 v[220:223], v[68:71], v[100:103], v[220:223]\n"
"s_waitcnt vmcnt(21)\n"
"v_cvt_pk_bf16_f32 v25, v210, v211\n"
"v_mfma_f32_16x16x32_bf16 v[156:159], v[88:91], v[36:39], v[156:159]\n"
"v_cvt_pk_bf16_f32 v26, v120, v121\n"
"v_cvt_pk_bf16_f32 v27, v122, v123\n"
"v_mfma_f32_16x16x32_bf16 v[184:187], v[48:51], v[36:39], v[184:187]\n"
"ds_write_b128 v7, v[24:27] offset:57344\n"
"v_mfma_f32_16x16x32_bf16 v[200:203], v[92:95], v[36:39], v[200:203]\n"
"v_add_f32_e32 v144, v144, v196\n"
"v_add_f32_e32 v145, v145, v197\n"
"v_mfma_f32_16x16x32_bf16 v[204:207], v[96:99], v[36:39], v[204:207]\n"
"v_add_f32_e32 v146, v146, v198\n"
"v_add_f32_e32 v147, v147, v199\n"
"v_mfma_f32_16x16x32_bf16 v[216:219], v[168:171], v[36:39], v[216:219]\n"
"buffer_load_dwordx4 v[100:103], v1, s[4:7], s98 offen offset:2048 sc1\n"
"v_mfma_f32_16x16x32_bf16 v[220:223], v[104:107], v[36:39], v[220:223]\n"
"v_add_f32_e32 v172, v172, v212\n"
"v_add_f32_e32 v173, v173, v213\n"
"v_mfma_f32_16x16x32_bf16 v[156:159], v[136:139], v[52:55], v[156:159]\n"
"v_add_f32_e32 v174, v174, v214\n"
"v_add_f32_e32 v175, v175, v215\n"
"v_mfma_f32_16x16x32_bf16 v[184:187], v[180:183], v[52:55], v[184:187]\n"
"v_mul_f32_e32 v144, v144, v255\n"
"v_mul_f32_e32 v145, v145, v255\n"
"v_mfma_f32_16x16x32_bf16 v[200:203], v[28:31], v[52:55], v[200:203]\n"
"v_mul_f32_e32 v146, v146, v255\n"
"v_mul_f32_e32 v147, v147, v255\n"
"v_mfma_f32_16x16x32_bf16 v[204:207], v[32:35], v[52:55], v[204:207]\n"
"v_mul_f32_e32 v172, v172, v255\n"
"v_mul_f32_e32 v173, v173, v255\n"
"v_mfma_f32_16x16x32_bf16 v[216:219], v[60:63], v[52:55], v[216:219]\n"
"buffer_load_dwordx4 v[36:39], v1, s[4:7], s98 offen offset:3072 sc1\n"
"v_mfma_f32_16x16x32_bf16 v[220:223], v[64:67], v[52:55], v[220:223]\n"
"v_cmp_u_f32_e32 vcc, v156, v156\n"
"s_waitcnt vmcnt(20)\n"
"; @A G1 1.1\n"
"v_mfma_f32_16x16x32_bf16 v[52:55], a[0:3], v[56:59], 0\n"
"s_cbranch_vccnz .Lmk_p90s\n"
".Lmk_p90r:\n"
"v_mul_f32_e32 v174, v174, v255\n"
"v_mfma_f32_16x16x32_bf16 v[120:123], a[4:7], v[56:59], 0\n"
"buffer_load_dwordx4 a[136:139], v0, s[4:7], s99 offen offset:2048 sc1\n"
"v_mfma_f32_16x16x32_bf16 v[160:163], a[8:11], v[56:59], 0\n"
"v_mul_f32_e32 v175, v175, v255\n"
"v_cvt_pk_bf16_f32 v188, v144, v145\n"
"v_mfma_f32_16x16x32_bf16 v[192:195], a[12:15], v[56:59], 0\n"
"ds_write_b128 v3, v[156:159]\n"
"v_mfma_f32_16x16x32_bf16 v[156:159], a[16:19], v[56:59], 0\n"
"s_add_u32 s98, s55, 0x48000\n"
"v_cvt_pk_bf16_f32 v189, v146, v147\n"
"v_mfma_f32_16x16x32_bf16 v[144:147], a[20:23], v[56:59], 0\n"
"buffer_load_dwordx4 v[56:59], v0, s[4:7], s98 offen sc1\n"
"v_mfma_f32_16x16x32_bf16 v[52:55], a[24:27], v[132:135], v[52:55]\n"
"ds_write_b128 v3, v[184:187] offset:1024\n"
"v_mfma_f32_16x16x32_bf16 v[120:123], a[28:31], v[132:135], v[120:123]\n"
"v_cvt_pk_bf16_f32 v190, v172, v173\n"
"v_cvt_pk_bf16_f32 v191, v174, v175\n"
"v_mfma_f32_16x16x32_bf16 v[160:163], a[32:35], v[132:135], v[160:163]\n"
"ds_write_b128 v2, v[188:191] offset:49152\n"
"v_mfma_f32_16x16x32_bf16 v[192:195], a[36:39], v[132:135], v[192:195]\n"
"ds_write_b128 v3, v[200:203] offset:2048\n"
"v_mfma_f32_16x16x32_bf16 v[156:159], a[40:43], v[132:135], v[156:159]\n"
"buffer_load_dwordx4 v[172:175], v0, s[4:7], s98 offen offset:1024 sc1\n"
"v_mfma_f32_16x16x32_bf16 v[144:147], a[44:47], v[132:135], v[144:147]\n"
"s_waitcnt vmcnt(21)\n"
"v_mfma_f32_16x16x32_bf16 v[52:55], a[48:51], v[76:79], v[52:55]\n"
"ds_write_b128 v3, v[204:207] offset:3072\n"
"v_mfma_f32_16x16x32_bf16 v[120:123], a[52:55], v[76:79], v[120:123]\n"
"s_add_u32 s97, s67, 0\n"
"v_mfma_f32_16x16x32_bf16 v[160:163], a[56:59], v[76:79], v[160:163]\n"
"buffer_load_dwordx2 v[254:255], v12, s[4:7], s97 offen sc1\n"
"s_add_u32 s96, s64, 0x2000\n"
"v_mfma_f32_16x16x32_bf16 v[192:195], a[60:63], v[76:79], v[192:195]\n"
"ds_write_b128 v3, v[216:219] offset:4096\n"
"v_mfma_f32_16x16x32_bf16 v[156:159], a[64:67], v[76:79], v[156:159]\n"
"buffer_load_dwordx4 v[132:135], v0, s[4:7], s98 offen offset:2048 sc1\n"
"v_mfma_f32_16x16x32_bf16 v[144:147], a[68:71], v[76:79], v[144:147]\n"
"buffer_store_dwordx4 v[128:131], v7, s[4:7], s96 offen sc1\n"
"v_mfma_f32_16x16x32_bf16 v[52:55], a[72:75], v[140:143], v[52:55]\n"
"ds_write_b128 v3, v[220:223] offset:5120\n"
"v_mfma_f32_16x16x32_bf16 v[120:123], a[76:79], v[140:143], v[120:123]\n"
"v_mfma_f32_16x16x32_bf16 v[160:163], a[80:83], v[140:143], v[160:163]\n"
"v_mfma_f32_16x16x32_bf16 v[192:195], a[84:87], v[140:143], v[192:195]\n"
"buffer_load_dwordx4 a[140:143], v0, s[4:7], s99 offen offset:3072 sc1\n"
"s_add_u32 s96, s64, 0x6000\n"
"v_mfma_f32_16x16x32_bf16 v[156:159], a[88:91], v[140:143], v[156:159]\n"
"buffer_load_dwordx4 v[76:79], v0, s[4:7], s98 offen offset:3072 sc1\n"
"v_mfma_f32_16x16x32_bf16 v[144:147], a[92:95], v[140:143], v[144:147]\n"
"s_waitcnt vmcnt(24)\n"
"buffer_store_dwordx4 v[24:27], v7, s[4:7], s96 offen sc1\n"
"v_mfma_f32_16x16x32_bf16 v[52:55], a[96:99], v[148:151], v[52:55]\n"
"v_mfma_f32_16x16x32_bf16 v[120:123], a[100:103], v[148:151], v[120:123]\n"
"v_mfma_f32_16x16x32_bf16 v[160:163], a[104:107], v[148:151], v[160:163]\n"
"v_mfma_f32_16x16x32_bf16 v[192:195], a[108:111], v[148:151], v[192:195]\n"
"v_mfma_f32_16x16x32_bf16 v[156:159], a[112:115], v[148:151], v[156:159]\n"
"buffer_load_dwordx4 v[24:27], v1, s[4:7], s98 offen sc1\n"
"v_mfma_f32_16x16x32_bf16 v[144:147], a[116:119], v[148:151], v[144:147]\n"
"s_waitcnt lgkmcnt(0)\n"
"s_barrier\n"
"v_mfma_f32_16x16x32_bf16 v[52:55], a[120:123], v[176:179], v[52:55]\n"
"s_waitcnt vmcnt(19)\n"
"v_add_f32_e32 v250, v250, v248\n"
"v_mfma_f32_16x16x32_bf16 v[120:123], a[124:127], v[176:179], v[120:123]\n"
"v_add_f32_e32 v251, v251, v249\n"
"v_add_f32_e32 v246, v246, v244\n"
"v_mfma_f32_16x16x32_bf16 v[160:163], v[40:43], v[176:179], v[160:163]\n"
"v_add_f32_e32 v247, v247, v245\n"
"v_add_f32_e32 v250, v250, v246\n"
"v_mfma_f32_16x16x32_bf16 v[192:195], v[44:47], v[176:179], v[192:195]\n"
"v_add_f32_e32 v251, v251, v247\n"
"v_cmp_u_f32_e32 vcc, v250, v251\n"
"v_mfma_f32_16x16x32_bf16 v[156:159], v[80:83], v[176:179], v[156:159]\n"
"buffer_load_dwordx4 v[128:131], v1, s[4:7], s98 offen offset:1024 sc1\n"
"v_mfma_f32_16x16x32_bf16 v[144:147], v[68:71], v[176:179], v[144:147]\n"
"s_cbranch_vccnz .Lmk_m95s\n"
".Lmk_m95r:\n"
"v_mfma_f32_16x16x32_bf16 v[52:55], v[88:91], v[152:155], v[52:55]\n"
"v_permlane16_swap_b32_e32 v250, v251\n"
"v_add_f32_e32 v250, v250, v251\n"
"v_mfma_f32_16x16x32_bf16 v[120:123], v[48:51], v[152:155], v[120:123]\n"
"v_mov_b32_e32 v251, v250\n"
"v_mfma_f32_16x16x32_bf16 v[160:163], v[92:95], v[152:155], v[160:163]\n"
"s_nop 0\n"
"v_permlane32_swap_b32_e32 v250, v251\n"
"v_add_f32_e32 v250, v250, v251\n"
"v_mfma_f32_16x16x32_bf16 v[192:195], v[96:99], v[152:155], v[192:195]\n"
"v_mul_f32_e32 v250, 0x3a800000, v250\n"
"v_mov_b32_e32 v251, v250\n"
"v_mfma_f32_16x16x32_bf16 v[156:159], v[168:171], v[152:155], v[156:159]\n"
"buffer_load_dwordx4 v[140:143], v1, s[4:7], s98 offen offset:2048 sc1\n"
"v_mfma_f32_16x16x32_bf16 v[144:147], v[104:107], v[152:155], v[144:147]\n"
"buffer_load_dwordx4 a[144:147], v1, s[4:7], s99 offen sc1\n"
"v_mfma_f32_16x16x32_bf16 v[52:55], v[136:139], v[164:167], v[52:55]\n"
"v_permlane16_swap_b32_e32 v250, v251\n"
"v_cvt_pk_bf16_f32 v253, -v250, 0\n"
"v_mfma_f32_16x16x32_bf16 v[120:123], v[180:183], v[164:167], v[120:123]\n"
"v_fma_f32 v251, -v250, v250, v251\n"
"v_lshlrev_b32_e32 v253, 16, v253\n"
"v_mfma_f32_16x16x32_bf16 v[160:163], v[28:31], v[164:167], v[160:163]\n"
"ds_read_b128 v[148:151], v4\n"
"ds_read_b128 v[152:155], v9\n"
"v_mfma_f32_16x16x32_bf16 v[192:195], v[32:35], v[164:167], v[192:195]\n"
"ds_read_b128 v[176:179], v4 offset:1024\n"
"ds_read_b128 v[184:187], v9 offset:64\n"
"v_mfma_f32_16x16x32_bf16 v[156:159], v[60:63], v[164:167], v[156:159]\n"
"buffer_load_dwordx4 v[188:191], v1, s[4:7], s98 offen offset:3072 sc1\n"
"v_mfma_f32_16x16x32_bf16 v[144:147], v[64:67], v[164:167], v[144:147]\n"
"v_cmp_u_f32_e32 vcc, v52, v52\n"
"s_waitcnt vmcnt(21)\n"
"; @A G1 1.2\n"
"v_mfma_f32_16x16x32_bf16 v[164:167], a[0:3], v[72:75], 0\n"
"s_cbranch_vccnz .Lmk_p91s\n"
".Lmk_p91r:\n"
"ds_read_b128 v[196:199], v4 offset:2048\n"
"v_mfma_f32_16x16x32_bf16 v[200:203], a[4:7], v[72:75], 0\n"
"s_waitcnt vmcnt(10)\n"
"v_cmp_ne_u64_e32 vcc, v[254:255], v[18:19]\n"
"v_mfma_f32_16x16x32_bf16 v[204:207], a[8:11], v[72:75], 0\n"
"s_cbranch_vccnz .Lmk_f81s\n"
".Lmk_f81r:\n"
"ds_read_b128 v[208:211], v9 offset:128\n"
"v_mfma_f32_16x16x32_bf16 v[212:215], a[12:15], v[72:75], 0\n"
"ds_write_b128 v3, v[52:55] offset:6144\n"
"v_mfma_f32_16x16x32_bf16 v[52:55], a[16:19], v[72:75], 0\n"
"s_add_u32 s98, s52, 0x8000\n"
"ds_read_b128 v[216:219], v4 offset:3072\n"
"v_mfma_f32_16x16x32_bf16 v[220:223], a[20:23], v[72:75], 0\n"
"buffer_load_dwordx4 v[72:75], v0, s[4:7], s98 offen sc1\n"
"v_mfma_f32_16x16x32_bf16 v[164:167], a[24:27], v[112:115], v[164:167]\n"
"ds_write_b128 v3, v[120:123] offset:7168\n"
"v_mfma_f32_16x16x32_bf16 v[200:203], a[28:31], v[112:115], v[200:203]\n"
"s_waitcnt lgkmcnt(3)\n"
"v_fmac_f32_e32 v148, v253, v152\n"
"v_mfma_f32_16x16x32_bf16 v[204:207], a[32:35], v[112:115], v[204:207]\n"
"v_fmac_f32_e32 v149, v253, v153\n"
"v_fmac_f32_e32 v150, v253, v154\n"
"v_mfma_f32_16x16x32_bf16 v[212:215], a[36:39], v[112:115], v[212:215]\n"
"ds_write_b128 v3, v[160:163] offset:8192\n"
"v_mfma_f32_16x16x32_bf16 v[52:55], a[40:43], v[112:115], v[52:55]\n"
"buffer_load_dwordx4 v[120:123], v0, s[4:7], s98 offen offset:1024 sc1\n"
"v_mfma_f32_16x16x32_bf16 v[220:223], a[44:47], v[112:115], v[220:223]\n"
"v_fmac_f32_e32 v151, v253, v155\n"
"v_mfma_f32_16x16x32_bf16 v[164:167], a[48:51], v[116:119], v[164:167]\n"
"ds_write_b128 v3, v[192:195] offset:9216\n"
"v_mfma_f32_16x16x32_bf16 v[200:203], a[52:55], v[116:119], v[200:203]\n"
"buffer_load_dwordx4 a[148:151], v1, s[4:7], s99 offen offset:1024 sc1\n"
"v_mfma_f32_16x16x32_bf16 v[204:207], a[56:59], v[116:119], v[204:207]\n"
"v_fmac_f32_e32 v176, v253, v184\n"
"v_fmac_f32_e32 v177, v253, v185\n"
"v_mfma_f32_16x16x32_bf16 v[212:215], a[60:63], v[116:119], v[212:215]\n"
"ds_write_b128 v3, v[156:159] offset:10240\n"
"v_mfma_f32_16x16x32_bf16 v[52:55], a[64:67], v[116:119], v[52:55]\n"
"buffer_load_dwordx4 v[112:115], v0, s[4:7], s98 offen offset:2048 sc1\n"
"v_mfma_f32_16x16x32_bf16 v[220:223], a[68:71], v[116:119], v[220:223]\n"
"v_fmac_f32_e32 v178, v253, v186\n"
"v_fmac_f32_e32 v179, v253, v187\n"
"v_mfma_f32_16x16x32_bf16 v[164:167], a[72:75], v[108:111], v[164:167]\n"
"ds_write_b128 v3, v[144:147] offset:11264\n"
"v_mfma_f32_16x16x32_bf16 v[200:203], a[76:79], v[108:111], v[200:203]\n"
"v_fmac_f32_e32 v196, v253, v208\n"
"v_fmac_f32_e32 v197, v253, v209\n"
"v_mfma_f32_16x16x32_bf16 v[204:207], a[80:83], v[108:111], v[204:207]\n"
"v_fmac_f32_e32 v198, v253, v210\n"
"v_fmac_f32_e32 v199, v253, v211\n"
"v_mfma_f32_16x16x32_bf16 v[212:215], a[84:87], v[108:111], v[212:215]\n"
"ds_read_b128 v[116:119], v9 offset:192\n"
"ds_read_b128 v[144:147], v4 offset:4096\n"
"v_mfma_f32_16x16x32_bf16 v[52:55], a[88:91], v[108:111], v[52:55]\n"
"buffer_load_dwordx4 v[152:155], v0, s[4:7], s98 offen offset:3072 sc1\n"
"v_mfma_f32_16x16x32_bf16 v[220:223], a[92:95], v[108:111], v[220:223]\n"
"ds_read_b128 v[108:111], v9 offset:256\n"
"v_mfma_f32_16x16x32_bf16 v[164:167], a[96:99], v[84:87], v[164:167]\n"
"ds_read_b128 v[156:159], v4 offset:5120\n"
"ds_read_b128 v[160:163], v9 offset:320\n"
"v_mfma_f32_16x16x32_bf16 v[200:203], a[100:103], v[84:87], v[200:203]\n"
"v_max_f32_e32 v251, 0, v251\n"
"v_cvt_pk_bf16_f32 v251, v251, 0\n"
"v_mfma_f32_16x16x32_bf16 v[204:207], a[104:107], v[84:87], v[204:207]\n"
"v_lshlrev_b32_e32 v251, 16, v251\n"
"v_add_f32_e32 v251, 0x3727c5ac, v251\n"
"v_mfma_f32_16x16x32_bf16 v[212:215], a[108:111], v[84:87], v[212:215]\n"
"v_cvt_pk_bf16_f32 v251, v251, 0\n"
"v_lshlrev_b32_e32 v251, 16, v251\n"
"v_mfma_f32_16x16x32_bf16 v[52:55], a[112:115], v[84:87], v[52:55]\n"
"buffer_load_dwordx4 v[184:187], v1, s[4:7], s98 offen sc1\n"
"v_mfma_f32_16x16x32_bf16 v[220:223], a[116:119], v[84:87], v[220:223]\n"
"s_waitcnt lgkmcnt(0)\n"
"v_fmac_f32_e32 v216, v253, v116\n"
"v_mfma_f32_16x16x32_bf16 v[164:167], a[120:123], v[124:127], v[164:167]\n"
"v_fmac_f32_e32 v217, v253, v117\n"
"v_fmac_f32_e32 v218, v253, v118\n"
"v_mfma_f32_16x16x32_bf16 v[200:203], a[124:127], v[124:127], v[200:203]\n"
"v_fmac_f32_e32 v219, v253, v119\n"
"v_fmac_f32_e32 v144, v253, v108\n"
"v_mfma_f32_16x16x32_bf16 v[204:207], v[40:43], v[124:127], v[204:207]\n"
"v_fmac_f32_e32 v145, v253, v109\n"
"v_fmac_f32_e32 v146, v253, v110\n"
"v_mfma_f32_16x16x32_bf16 v[212:215], v[44:47], v[124:127], v[212:215]\n"
"buffer_load_dwordx4 a[152:155], v1, s[4:7], s99 offen offset:2048 sc1\n"
"v_mfma_f32_16x16x32_bf16 v[52:55], v[80:83], v[124:127], v[52:55]\n"
"buffer_load_dwordx4 v[84:87], v1, s[4:7], s98 offen offset:1024 sc1\n"
"v_mfma_f32_16x16x32_bf16 v[220:223], v[68:71], v[124:127], v[220:223]\n"
"v_fmac_f32_e32 v147, v253, v111\n"
"v_mfma_f32_16x16x32_bf16 v[164:167], v[88:91], v[100:103], v[164:167]\n"
"v_fmac_f32_e32 v156, v253, v160\n"
"v_fmac_f32_e32 v157, v253, v161\n"
"v_mfma_f32_16x16x32_bf16 v[200:203], v[48:51], v[100:103], v[200:203]\n"
"v_fmac_f32_e32 v158, v253, v162\n"
"v_fmac_f32_e32 v159, v253, v163\n"
"v_mfma_f32_16x16x32_bf16 v[204:207], v[92:95], v[100:103], v[204:207]\n"
"v_sqrt_f32_e32 v251, v251\n"
"v_mfma_f32_16x16x32_bf16 v[212:215], v[96:99], v[100:103], v[212:215]\n"
"v_cvt_pk_bf16_f32 v251, v251, 0\n"
"v_lshlrev_b32_e32 v251, 16, v251\n"
"v_mfma_f32_16x16x32_bf16 v[52:55], v[168:171], v[100:103], v[52:55]\n"
"v_max_u32_e32 v255, v56, v57\n"
"v_mfma_f32_16x16x32_bf16 v[220:223], v[104:107], v[100:103], v[220:223]\n"
"v_max3_u32 v255, v255, v58, v59\n"
"v_max3_u32 v255, v255, v172, v173\n"
"v_mfma_f32_16x16x32_bf16 v[164:167], v[136:139], v[36:39], v[164:167]\n"
"v_max3_u32 v255, v255, v174, v175\n"
"s_waitcnt vmcnt(14)\n"
"v_mfma_f32_16x16x32_bf16 v[200:203], v[180:183], v[36:39], v[200:203]\n"
"v_max3_u32 v255, v255, v132, v133\n"
"v_max3_u32 v255, v255, v134, v135\n"
"v_mfma_f32_16x16x32_bf16 v[204:207], v[28:31], v[36:39], v[204:207]\n"
"v_max3_u32 v255, v255, v76, v77\n"
"v_max3_u32 v255, v255, v78, v79\n"
"v_mfma_f32_16x16x32_bf16 v[212:215], v[32:35], v[36:39], v[212:215]\n"
"v_cmp_eq_u32_e32 vcc, -1, v255\n"
"v_mfma_f32_16x16x32_bf16 v[52:55], v[60:63], v[36:39], v[52:55]\n"
"s_cbranch_vccnz .Lmk_q93s\n"
".Lmk_q93r:\n"
"v_mfma_f32_16x16x32_bf16 v[220:223], v[64:67], v[36:39], v[220:223]\n"
"v_cmp_u_f32_e32 vcc, v164, v164\n"
"; @A G1 1.3\n"
"v_mfma_f32_16x16x32_bf16 v[148:151], a[0:3], v[56:59], v[148:151]\n"
"s_cbranch_vccnz .Lmk_p92s\n"
".Lmk_p92r:\n"
"v_mfma_f32_16x16x32_bf16 v[176:179], a[4:7], v[56:59], v[176:179]\n"
"buffer_load_dwordx4 v[36:39], v1, s[4:7], s98 offen offset:2048 sc1\n"
"v_mfma_f32_16x16x32_bf16 v[196:199], a[8:11], v[56:59], v[196:199]\n"
"buffer_load_dwordx4 v[100:103], v1, s[4:7], s98 offen offset:3072 sc1\n"
"v_mfma_f32_16x16x32_bf16 v[216:219], a[12:15], v[56:59], v[216:219]\n"
"ds_write_b128 v3, v[164:167] offset:12288\n"
"v_mfma_f32_16x16x32_bf16 v[144:147], a[16:19], v[56:59], v[144:147]\n"
"s_add_u32 s98, s53, 0x8000\n"
"v_mfma_f32_16x16x32_bf16 v[156:159], a[20:23], v[56:59], v[156:159]\n"
"buffer_load_dwordx4 v[56:59], v0, s[4:7], s98 offen sc1\n"
"v_mfma_f32_16x16x32_bf16 v[148:151], a[24:27], v[172:175], v[148:151]\n"
"ds_write_b128 v3, v[200:203] offset:13312\n"
"v_mfma_f32_16x16x32_bf16 v[176:179], a[28:31], v[172:175], v[176:179]\n"
"buffer_load_dwordx4 a[156:159], v1, s[4:7], s99 offen offset:3072 sc1\n"
"v_mfma_f32_16x16x32_bf16 v[196:199], a[32:35], v[172:175], v[196:199]\n"
"v_rcp_f32_e32 v255, v251\n"
"v_mfma_f32_16x16x32_bf16 v[216:219], a[36:39], v[172:175], v[216:219]\n"
"ds_write_b128 v3, v[204:207] offset:14336\n"
"v_mfma_f32_16x16x32_bf16 v[144:147], a[40:43], v[172:175], v[144:147]\n"
"buffer_load_dwordx4 v[108:111], v0, s[4:7], s98 offen offset:1024 sc1\n"
"v_mfma_f32_16x16x32_bf16 v[156:159], a[44:47], v[172:175], v[156:159]\n"
"v_mul_f32_e32 v254, 0x3e8293ee, v255\n"
"v_mfma_f32_16x16x32_bf16 v[148:151], a[48:51], v[132:135], v[148:151]\n"
"ds_write_b128 v3, v[212:215] offset:15360\n"
"v_mfma_f32_16x16x32_bf16 v[176:179], a[52:55], v[132:135], v[176:179]\n"
"v_mfma_f32_16x16x32_bf16 v[196:199], a[56:59], v[132:135], v[196:199]\n"
"v_mfma_f32_16x16x32_bf16 v[216:219], a[60:63], v[132:135], v[216:219]\n"
"ds_write_b128 v3, v[52:55] offset:16384\n"
"v_mfma_f32_16x16x32_bf16 v[144:147], a[64:67], v[132:135], v[144:147]\n"
"s_waitcnt vmcnt(16)\n"
"v_max_u32_e32 v253, v24, v25\n"
"v_mfma_f32_16x16x32_bf16 v[156:159], a[68:71], v[132:135], v[156:159]\n"
"v_max3_u32 v253, v253, v26, v27\n"
"v_max3_u32 v253, v253, v128, v129\n"
"v_mfma_f32_16x16x32_bf16 v[148:151], a[72:75], v[76:79], v[148:151]\n"
"v_max3_u32 v253, v253, v130, v131\n"
"s_waitcnt vmcnt(13)\n"
"v_mfma_f32_16x16x32_bf16 v[176:179], a[76:79], v[76:79], v[176:179]\n"
"v_max3_u32 v253, v253, v140, v141\n"
"v_max3_u32 v253, v253, v142, v143\n"
"v_mfma_f32_16x16x32_bf16 v[196:199], a[80:83], v[76:79], v[196:199]\n"
"v_max3_u32 v253, v253, v188, v189\n"
"v_max3_u32 v253, v253, v190, v191\n"
"v_mfma_f32_16x16x32_bf16 v[216:219], a[84:87], v[76:79], v[216:219]\n"
"v_cmp_eq_u32_e32 vcc, -1, v253\n"
"v_mfma_f32_16x16x32_bf16 v[144:147], a[88:91], v[76:79], v[144:147]\n"
"s_cbranch_vccnz .Lmk_q94s\n"
".Lmk_q94r:\n"
"v_mfma_f32_16x16x32_bf16 v[156:159], a[92:95], v[76:79], v[156:159]\n"
"v_mfma_f32_16x16x32_bf16 v[148:151], a[96:99], v[24:27], v[148:151]\n"
"buffer_load_dwordx4 v[52:55], v0, s[4:7], s98 offen offset:2048 sc1\n"
"v_mfma_f32_16x16x32_bf16 v[176:179], a[100:103], v[24:27], v[176:179]\n"
"buffer_load_dwordx4 v[76:79], v0, s[4:7], s98 offen offset:3072 sc1\n"
"v_mfma_f32_16x16x32_bf16 v[196:199], a[104:107], v[24:27], v[196:199]\n"
"ds_write_b128 v3, v[220:223] offset:17408\n"
"v_mfma_f32_16x16x32_bf16 v[216:219], a[108:111], v[24:27], v[216:219]\n"
"s_add_u32 s99, s70, 0x2000\n"
"v_mfma_f32_16x16x32_bf16 v[144:147], a[112:115], v[24:27], v[144:147]\n"
"buffer_load_dwordx4 v[116:119], v1, s[4:7], s98 offen sc1\n"
"v_mfma_f32_16x16x32_bf16 v[156:159], a[116:119], v[24:27], v[156:159]\n"
"buffer_load_dwordx4 a[160:163], v0, s[4:7], s99 offen sc1\n"
"v_mfma_f32_16x16x32_bf16 v[148:151], a[120:123], v[128:131], v[148:151]\n"
"s_waitcnt lgkmcnt(0)\n"
"s_barrier\n"
"v_mfma_f32_16x16x32_bf16 v[176:179], a[124:127], v[128:131], v[176:179]\n"
"ds_read_b128 v[24:27], v5 offset:2048\n"
"ds_read_b128 v[124:127], v6 offset:2048\n"
"v_mfma_f32_16x16x32_bf16 v[196:199], v[40:43], v[128:131], v[196:199]\n"
"ds_read_b128 v[132:135], v5 offset:3072\n"
"ds_read_b128 v[160:163], v6 offset:3072\n"
"v_mfma_f32_16x16x32_bf16 v[216:219], v[44:47], v[128:131], v[216:219]\n"
"v_mfma_f32_16x16x32_bf16 v[144:147], v[80:83], v[128:131], v[144:147]\n"
"buffer_load_dwordx4 v[164:167], v1, s[4:7], s98 offen offset:1024 sc1\n"
"v_mfma_f32_16x16x32_bf16 v[156:159], v[68:71], v[128:131], v[156:159]\n"
"ds_read_b128 v[128:131], v5 offset:4096\n"
"v_mfma_f32_16x16x32_bf16 v[148:151], v[88:91], v[140:143], v[148:151]\n"
"ds_read_b128 v[172:175], v6 offset:4096\n"
"ds_read_b128 v[192:195], v5 offset:5120\n"
"v_mfma_f32_16x16x32_bf16 v[176:179], v[48:51], v[140:143], v[176:179]\n"
"ds_read_b128 v[200:203], v6 offset:5120\n"
"s_waitcnt lgkmcnt(4)\n"
"v_mfma_f32_16x16x32_bf16 v[196:199], v[92:95], v[140:143], v[196:199]\n"
"v_add_f32_e32 v24, v24, v124\n"
"v_add_f32_e32 v25, v25, v125\n"
"v_mfma_f32_16x16x32_bf16 v[216:219], v[96:99], v[140:143], v[216:219]\n"
"v_add_f32_e32 v26, v26, v126\n"
"v_add_f32_e32 v27, v27, v127\n"
"v_mfma_f32_16x16x32_bf16 v[144:147], v[168:171], v[140:143], v[144:147]\n"
"buffer_load_dwordx4 v[124:127], v1, s[4:7], s98 offen offset:2048 sc1\n"
"v_mfma_f32_16x16x32_bf16 v[156:159], v[104:107], v[140:143], v[156:159]\n"
"ds_read_b128 v[140:143], v5\n"
"ds_read_b128 v[204:207], v6\n"
"v_mfma_f32_16x16x32_bf16 v[148:151], v[136:139], v[188:191], v[148:151]\n"
"ds_read_b128 v[208:211], v5 offset:1024\n"
"ds_read_b128 v[212:215], v6 offset:1024\n"
"v_mfma_f32_16x16x32_bf16 v[176:179], v[180:183], v[188:191], v[176:179]\n"
"v_add_f32_e32 v132, v132, v160\n"
"v_add_f32_e32 v133, v133, v161\n"
"v_mfma_f32_16x16x32_bf16 v[196:199], v[28:31], v[188:191], v[196:199]\n"
"v_add_f32_e32 v134, v134, v162\n"
"v_add_f32_e32 v135, v135, v163\n"
"v_mfma_f32_16x16x32_bf16 v[216:219], v[32:35], v[188:191], v[216:219]\n"
"s_waitcnt lgkmcnt(4)\n"
"v_add_f32_e32 v128, v128, v172\n"
"v_mfma_f32_16x16x32_bf16 v[144:147], v[60:63], v[188:191], v[144:147]\n"
"buffer_load_dwordx4 v[160:163], v1, s[4:7], s98 offen offset:3072 sc1\n"
"v_mfma_f32_16x16x32_bf16 v[156:159], v[64:67], v[188:191], v[156:159]\n"
"v_add_f32_e32 v129, v129, v173\n"
"v_add_f32_e32 v130, v130, v174\n"
"v_add_f32_e32 v131, v131, v175\n"
"v_add_f32_e32 v192, v192, v200\n"
"v_add_f32_e32 v193, v193, v201\n"
"v_add_f32_e32 v194, v194, v202\n"
"v_add_f32_e32 v195, v195, v203\n"
"s_waitcnt lgkmcnt(0)\n"
"v_add_f32_e32 v140, v140, v204\n"
"v_add_f32_e32 v141, v141, v205\n"
"v_add_f32_e32 v142, v142, v206\n"
"v_add_f32_e32 v143, v143, v207\n"
"v_add_f32_e32 v208, v208, v212\n"
"v_add_f32_e32 v209, v209, v213\n"
"v_add_f32_e32 v210, v210, v214\n"
"v_add_f32_e32 v211, v211, v215\n"
"s_waitcnt vmcnt(18)\n"
"; @A G1 2.0\n"
"v_mfma_f32_16x16x32_bf16 v[172:175], a[0:3], v[72:75], 0\n"
"s_add_u32 s98, s62, 0x800\n"
"v_mfma_f32_16x16x32_bf16 v[188:191], a[4:7], v[72:75], 0\n"
"buffer_load_dwordx2 v[252:253], v10, s[4:7], s98 offen sc1\n"
"v_mfma_f32_16x16x32_bf16 v[200:203], a[8:11], v[72:75], 0\n"
"buffer_load_dwordx2 v[250:251], v10, s[4:7], s98 offen offset:128 sc1\n"
"v_mfma_f32_16x16x32_bf16 v[204:207], a[12:15], v[72:75], 0\n"
"buffer_load_dwordx2 v[248:249], v10, s[4:7], s98 offen offset:256 sc1\n"
"v_mfma_f32_16x16x32_bf16 v[212:215], a[16:19], v[72:75], 0\n"
"s_add_u32 s97, s54, 0x8000\n"
"v_add_f32_e32 v196, v196, v24\n"
"v_mfma_f32_16x16x32_bf16 v[220:223], a[20:23], v[72:75], 0\n"
"buffer_load_dwordx4 v[72:75], v0, s[4:7], s97 offen sc1\n"
"v_mfma_f32_16x16x32_bf16 v[172:175], a[24:27], v[120:123], v[172:175]\n"
"buffer_load_dwordx2 v[246:247], v10, s[4:7], s98 offen offset:384 sc1\n"
"v_mfma_f32_16x16x32_bf16 v[188:191], a[28:31], v[120:123], v[188:191]\n"
"v_add_f32_e32 v197, v197, v25\n"
"v_add_f32_e32 v198, v198, v26\n"
"v_mfma_f32_16x16x32_bf16 v[200:203], a[32:35], v[120:123], v[200:203]\n"
"buffer_load_dwordx4 a[164:167], v0, s[4:7], s99 offen offset:1024 sc1\n"
"v_mfma_f32_16x16x32_bf16 v[204:207], a[36:39], v[120:123], v[204:207]\n"
"v_add_f32_e32 v199, v199, v27\n"
"v_add_f32_e32 v216, v216, v132\n"
"v_mfma_f32_16x16x32_bf16 v[212:215], a[40:43], v[120:123], v[212:215]\n"
"buffer_load_dwordx4 v[24:27], v0, s[4:7], s97 offen offset:1024 sc1\n"
"v_mfma_f32_16x16x32_bf16 v[220:223], a[44:47], v[120:123], v[220:223]\n"
"s_waitcnt vmcnt(22)\n"
"v_add_f32_e32 v217, v217, v133\n"
"v_mfma_f32_16x16x32_bf16 v[172:175], a[48:51], v[112:115], v[172:175]\n"
"v_add_f32_e32 v218, v218, v134\n"
"v_add_f32_e32 v219, v219, v135\n"
"v_mfma_f32_16x16x32_bf16 v[188:191], a[52:55], v[112:115], v[188:191]\n"
"v_mul_f32_e32 v196, v196, v255\n"
"v_mul_f32_e32 v197, v197, v255\n"
"v_mfma_f32_16x16x32_bf16 v[200:203], a[56:59], v[112:115], v[200:203]\n"
"v_mul_f32_e32 v198, v198, v255\n"
"v_mul_f32_e32 v199, v199, v255\n"
"v_mfma_f32_16x16x32_bf16 v[204:207], a[60:63], v[112:115], v[204:207]\n"
"v_mul_f32_e32 v216, v216, v255\n"
"v_mul_f32_e32 v217, v217, v255\n"
"v_mfma_f32_16x16x32_bf16 v[212:215], a[64:67], v[112:115], v[212:215]\n"
"buffer_load_dwordx4 v[120:123], v0, s[4:7], s97 offen offset:2048 sc1\n"
"v_mfma_f32_16x16x32_bf16 v[220:223], a[68:71], v[112:115], v[220:223]\n"
"v_mul_f32_e32 v218, v218, v255\n"
"v_mul_f32_e32 v219, v219, v255\n"
"v_mfma_f32_16x16x32_bf16 v[172:175], a[72:75], v[152:155], v[172:175]\n"
"v_cvt_pk_bf16_f32 v112, v196, v197\n"
"v_cvt_pk_bf16_f32 v113, v198, v199\n"
"v_mfma_f32_16x16x32_bf16 v[188:191], a[76:79], v[152:155], v[188:191]\n"
"v_cvt_pk_bf16_f32 v114, v216, v217\n"
"v_cvt_pk_bf16_f32 v115, v218, v219\n"
"v_mfma_f32_16x16x32_bf16 v[200:203], a[80:83], v[152:155], v[200:203]\n"
"ds_write_b128 v8, v[112:115] offset:25600\n"
"v_mfma_f32_16x16x32_bf16 v[204:207], a[84:87], v[152:155], v[204:207]\n"
"s_add_u32 s98, s64, 0x2000\n"
"v_mfma_f32_16x16x32_bf16 v[212:215], a[88:91], v[152:155], v[212:215]\n"
"buffer_load_dwordx4 v[132:135], v0, s[4:7], s97 offen offset:3072 sc1\n"
"v_mfma_f32_16x16x32_bf16 v[220:223], a[92:95], v[152:155], v[220:223]\n"
"s_waitcnt vmcnt(21)\n"
"v_mfma_f32_16x16x32_bf16 v[172:175], a[96:99], v[184:187], v[172:175]\n"
"buffer_store_dwordx4 v[112:115], v8, s[4:7], s98 offen offset:1024 sc1\n"
"v_mfma_f32_16x16x32_bf16 v[188:191], a[100:103], v[184:187], v[188:191]\n"
"v_add_f32_e32 v144, v144, v128\n"
"v_add_f32_e32 v145, v145, v129\n"
"v_mfma_f32_16x16x32_bf16 v[200:203], a[104:107], v[184:187], v[200:203]\n"
"v_add_f32_e32 v146, v146, v130\n"
"v_add_f32_e32 v147, v147, v131\n"
"v_mfma_f32_16x16x32_bf16 v[204:207], a[108:111], v[184:187], v[204:207]\n"
"v_add_f32_e32 v156, v156, v192\n"
"v_add_f32_e32 v157, v157, v193\n"
"v_mfma_f32_16x16x32_bf16 v[212:215], a[112:115], v[184:187], v[212:215]\n"
"buffer_load_dwordx4 v[112:115], v1, s[4:7], s97 offen sc1\n"
"v_mfma_f32_16x16x32_bf16 v[220:223], a[116:119], v[184:187], v[220:223]\n"
"buffer_load_dwordx4 a[168:171], v0, s[4:7], s99 offen offset:2048 sc1\n"
"v_mfma_f32_16x16x32_bf16 v[172:175], a[120:123], v[84:87], v[172:175]\n"
"v_add_f32_e32 v158, v158, v194\n"
"v_add_f32_e32 v159, v159, v195\n"
"v_mfma_f32_16x16x32_bf16 v[188:191], a[124:127], v[84:87], v[188:191]\n"
"v_mul_f32_e32 v144, v144, v255\n"
"v_mul_f32_e32 v145, v145, v255\n"
"v_mfma_f32_16x16x32_bf16 v[200:203], v[40:43], v[84:87], v[200:203]\n"
"v_mul_f32_e32 v146, v146, v255\n"
"v_mul_f32_e32 v147, v147, v255\n"
"v_mfma_f32_16x16x32_bf16 v[204:207], v[44:47], v[84:87], v[204:207]\n"
"v_mul_f32_e32 v156, v156, v255\n"
"v_mul_f32_e32 v157, v157, v255\n"
"v_mfma_f32_16x16x32_bf16 v[212:215], v[80:83], v[84:87], v[212:215]\n"
"buffer_load_dwordx4 v[128:131], v1, s[4:7], s97 offen offset:1024 sc1\n"
"v_mfma_f32_16x16x32_bf16 v[220:223], v[68:71], v[84:87], v[220:223]\n"
"s_waitcnt vmcnt(23)\n"
"v_mul_f32_e32 v158, v158, v255\n"
"v_mfma_f32_16x16x32_bf16 v[172:175], v[88:91], v[36:39], v[172:175]\n"
"v_mul_f32_e32 v159, v159, v255\n"
"v_cvt_pk_bf16_f32 v84, v144, v145\n"
"v_mfma_f32_16x16x32_bf16 v[188:191], v[48:51], v[36:39], v[188:191]\n"
"v_cvt_pk_bf16_f32 v85, v146, v147\n"
"v_cvt_pk_bf16_f32 v86, v156, v157\n"
"v_mfma_f32_16x16x32_bf16 v[200:203], v[92:95], v[36:39], v[200:203]\n"
"v_cvt_pk_bf16_f32 v87, v158, v159\n"
"v_mfma_f32_16x16x32_bf16 v[204:207], v[96:99], v[36:39], v[204:207]\n"
"ds_write_b128 v7, v[84:87] offset:58368\n"
"v_mfma_f32_16x16x32_bf16 v[212:215], v[168:171], v[36:39], v[212:215]\n"
"buffer_load_dwordx4 v[144:147], v1, s[4:7], s97 offen offset:2048 sc1\n"
"v_mfma_f32_16x16x32_bf16 v[220:223], v[104:107], v[36:39], v[220:223]\n"
"s_add_u32 s98, s64, 0x6000\n"
"v_mfma_f32_16x16x32_bf16 v[172:175], v[136:139], v[100:103], v[172:175]\n"
"buffer_store_dwordx4 v[84:87], v7, s[4:7], s98 offen offset:1024 sc1\n"
"v_mfma_f32_16x16x32_bf16 v[188:191], v[180:183], v[100:103], v[188:191]\n"
"v_add_f32_e32 v148, v148, v140\n"
"v_add_f32_e32 v149, v149, v141\n"
"v_mfma_f32_16x16x32_bf16 v[200:203], v[28:31], v[100:103], v[200:203]\n"
"v_add_f32_e32 v150, v150, v142\n"
"v_add_f32_e32 v151, v151, v143\n"
"v_mfma_f32_16x16x32_bf16 v[204:207], v[32:35], v[100:103], v[204:207]\n"
"v_add_f32_e32 v176, v176, v208\n"
"v_add_f32_e32 v177, v177, v209\n"
"v_mfma_f32_16x16x32_bf16 v[212:215], v[60:63], v[100:103], v[212:215]\n"
"buffer_load_dwordx4 v[36:39], v1, s[4:7], s97 offen offset:3072 sc1\n"
"v_mfma_f32_16x16x32_bf16 v[220:223], v[64:67], v[100:103], v[220:223]\n"
"s_waitcnt vmcnt(23)\n"
"v_add_f32_e32 v178, v178, v210\n"
"; @A G1 2.1\n"
"v_mfma_f32_16x16x32_bf16 v[84:87], a[0:3], v[56:59], 0\n"
"v_add_f32_e32 v179, v179, v211\n"
"v_mul_f32_e32 v148, v148, v254\n"
"v_mfma_f32_16x16x32_bf16 v[100:103], a[4:7], v[56:59], 0\n"
"buffer_load_dwordx4 a[172:175], v0, s[4:7], s99 offen offset:3072 sc1\n"
"v_mfma_f32_16x16x32_bf16 v[140:143], a[8:11], v[56:59], 0\n"
"v_mul_f32_e32 v149, v149, v254\n"
"v_mul_f32_e32 v150, v150, v254\n"
"v_mfma_f32_16x16x32_bf16 v[152:155], a[12:15], v[56:59], 0\n"
"ds_write_b128 v3, v[172:175]\n"
"v_mfma_f32_16x16x32_bf16 v[156:159], a[16:19], v[56:59], 0\n"
"s_add_u32 s98, s55, 0x8000\n"
"v_mul_f32_e32 v151, v151, v254\n"
"v_mfma_f32_16x16x32_bf16 v[172:175], a[20:23], v[56:59], 0\n"
"buffer_load_dwordx4 v[56:59], v0, s[4:7], s98 offen sc1\n"
"v_mfma_f32_16x16x32_bf16 v[84:87], a[24:27], v[108:111], v[84:87]\n"
"ds_write_b128 v3, v[188:191] offset:1024\n"
"v_mfma_f32_16x16x32_bf16 v[100:103], a[28:31], v[108:111], v[100:103]\n"
"v_mul_f32_e32 v176, v176, v254\n"
"v_mul_f32_e32 v177, v177, v254\n"
"v_mfma_f32_16x16x32_bf16 v[140:143], a[32:35], v[108:111], v[140:143]\n"
"v_mul_f32_e32 v178, v178, v254\n"
"v_mul_f32_e32 v179, v179, v254\n"
"v_mfma_f32_16x16x32_bf16 v[152:155], a[36:39], v[108:111], v[152:155]\n"
"ds_write_b128 v3, v[200:203] offset:2048\n"
"v_mfma_f32_16x16x32_bf16 v[156:159], a[40:43], v[108:111], v[156:159]\n"
"buffer_load_dwordx4 v[184:187], v0, s[4:7], s98 offen offset:1024 sc1\n"
"v_mfma_f32_16x16x32_bf16 v[172:175], a[44:47], v[108:111], v[172:175]\n"
"s_waitcnt vmcnt(24)\n"
"v_cvt_pk_bf16_f32 v108, v148, v149\n"
"v_mfma_f32_16x16x32_bf16 v[84:87], a[48:51], v[52:55], v[84:87]\n"
"ds_write_b128 v3, v[204:207] offset:3072\n"
"v_mfma_f32_16x16x32_bf16 v[100:103], a[52:55], v[52:55], v[100:103]\n"
"v_cvt_pk_bf16_f32 v109, v150, v151\n"
"v_cvt_pk_bf16_f32 v110, v176, v177\n"
"v_mfma_f32_16x16x32_bf16 v[140:143], a[56:59], v[52:55], v[140:143]\n"
"v_cvt_pk_bf16_f32 v111, v178, v179\n"
"v_mfma_f32_16x16x32_bf16 v[152:155], a[60:63], v[52:55], v[152:155]\n"
"ds_write_b128 v3, v[212:215] offset:4096\n"
"v_mfma_f32_16x16x32_bf16 v[156:159], a[64:67], v[52:55], v[156:159]\n"
"buffer_load_dwordx4 v[148:151], v0, s[4:7], s98 offen offset:2048 sc1\n"
"v_mfma_f32_16x16x32_bf16 v[172:175], a[68:71], v[52:55], v[172:175]\n"
"ds_write_b128 v2, v[108:111] offset:50176\n"
"v_mfma_f32_16x16x32_bf16 v[84:87], a[72:75], v[76:79], v[84:87]\n"
"ds_write_b128 v3, v[220:223] offset:5120\n"
"v_mfma_f32_16x16x32_bf16 v[100:103], a[76:79], v[76:79], v[100:103]\n"
"v_mfma_f32_16x16x32_bf16 v[140:143], a[80:83], v[76:79], v[140:143]\n"
"v_mfma_f32_16x16x32_bf16 v[152:155], a[84:87], v[76:79], v[152:155]\n"
"buffer_load_dwordx4 a[176:179], v1, s[4:7], s99 offen sc1\n"
"v_mfma_f32_16x16x32_bf16 v[156:159], a[88:91], v[76:79], v[156:159]\n"
"buffer_load_dwordx4 v[52:55], v0, s[4:7], s98 offen offset:3072 sc1\n"
"v_mfma_f32_16x16x32_bf16 v[172:175], a[92:95], v[76:79], v[172:175]\n"
"s_waitcnt vmcnt(24)\n"
"v_mfma_f32_16x16x32_bf16 v[84:87], a[96:99], v[116:119], v[84:87]\n"
"v_mfma_f32_16x16x32_bf16 v[100:103], a[100:103], v[116:119], v[100:103]\n"
"v_mfma_f32_16x16x32_bf16 v[140:143], a[104:107], v[116:119], v[140:143]\n"
"v_mfma_f32_16x16x32_bf16 v[152:155], a[108:111], v[116:119], v[152:155]\n"
"v_mfma_f32_16x16x32_bf16 v[156:159], a[112:115], v[116:119], v[156:159]\n"
"buffer_load_dwordx4 v[76:79], v1, s[4:7], s98 offen sc1\n"
"v_mfma_f32_16x16x32_bf16 v[172:175], a[116:119], v[116:119], v[172:175]\n"
"s_waitcnt lgkmcnt(0)\n"
"s_barrier\n"
"v_mfma_f32_16x16x32_bf16 v[84:87], a[120:123], v[164:167], v[84:87]\n"
"s_waitcnt vmcnt(18)\n"
"v_add_f32_e32 v252, v252, v250\n"
"v_mfma_f32_16x16x32_bf16 v[100:103], a[124:127], v[164:167], v[100:103]\n"
"v_add_f32_e32 v253, v253, v251\n"
"v_add_f32_e32 v248, v248, v246\n"
"v_mfma_f32_16x16x32_bf16 v[140:143], v[40:43], v[164:167], v[140:143]\n"
"v_add_f32_e32 v249, v249, v247\n"
"v_add_f32_e32 v252, v252, v248\n"
"v_mfma_f32_16x16x32_bf16 v[152:155], v[44:47], v[164:167], v[152:155]\n"
"v_add_f32_e32 v253, v253, v249\n"
"v_mfma_f32_16x16x32_bf16 v[156:159], v[80:83], v[164:167], v[156:159]\n"
"buffer_load_dwordx4 v[108:111], v1, s[4:7], s98 offen offset:1024 sc1\n"
"v_mfma_f32_16x16x32_bf16 v[172:175], v[68:71], v[164:167], v[172:175]\n"
"v_permlane16_swap_b32_e32 v252, v253\n"
"v_mfma_f32_16x16x32_bf16 v[84:87], v[88:91], v[124:127], v[84:87]\n"
"v_add_f32_e32 v252, v252, v253\n"
"v_mov_b32_e32 v253, v252\n"
"v_mfma_f32_16x16x32_bf16 v[100:103], v[48:51], v[124:127], v[100:103]\n"
"s_nop 0\n"
"v_permlane32_swap_b32_e32 v252, v253\n"
"v_add_f32_e32 v252, v252, v253\n"
"v_mfma_f32_16x16x32_bf16 v[140:143], v[92:95], v[124:127], v[140:143]\n"
"v_mul_f32_e32 v252, 0x3a800000, v252\n"
"v_mov_b32_e32 v253, v252\n"
"v_mfma_f32_16x16x32_bf16 v[152:155], v[96:99], v[124:127], v[152:155]\n"
"s_nop 0\n"
"v_permlane16_swap_b32_e32 v252, v253\n"
"v_cvt_pk_bf16_f32 v255, -v252, 0\n"
"v_mfma_f32_16x16x32_bf16 v[156:159], v[168:171], v[124:127], v[156:159]\n"
"buffer_load_dwordx4 v[116:119], v1, s[4:7], s98 offen offset:2048 sc1\n"
"v_mfma_f32_16x16x32_bf16 v[172:175], v[104:107], v[124:127], v[172:175]\n"
"buffer_load_dwordx4 a[180:183], v1, s[4:7], s99 offen offset:1024 sc1\n"
"v_mfma_f32_16x16x32_bf16 v[84:87], v[136:139], v[160:163], v[84:87]\n"
"v_fma_f32 v253, -v252, v252, v253\n"
"v_lshlrev_b32_e32 v255, 16, v255\n"
"v_mfma_f32_16x16x32_bf16 v[100:103], v[180:183], v[160:163], v[100:103]\n"
"ds_read_b128 v[124:127], v4\n"
"ds_read_b128 v[164:167], v9\n"
"v_mfma_f32_16x16x32_bf16 v[140:143], v[28:31], v[160:163], v[140:143]\n"
"ds_read_b128 v[176:179], v4 offset:1024\n"
"ds_read_b128 v[188:191], v9 offset:64\n"
"v_mfma_f32_16x16x32_bf16 v[152:155], v[32:35], v[160:163], v[152:155]\n"
"ds_read_b128 v[192:195], v4 offset:2048\n"
"ds_read_b128 v[196:199], v9 offset:128\n"
"v_mfma_f32_16x16x32_bf16 v[156:159], v[60:63], v[160:163], v[156:159]\n"
"buffer_load_dwordx4 v[200:203], v1, s[4:7], s98 offen offset:3072 sc1\n"
"v_mfma_f32_16x16x32_bf16 v[172:175], v[64:67], v[160:163], v[172:175]\n"
"s_waitcnt vmcnt(20)\n"
"v_max_f32_e32 v253, 0, v253\n"
"; @A G1 2.2\n"
"v_mfma_f32_16x16x32_bf16 v[160:163], a[0:3], v[72:75], 0\n"
"v_cvt_pk_bf16_f32 v253, v253, 0\n"
"v_lshlrev_b32_e32 v253, 16, v253\n"
"v_mfma_f32_16x16x32_bf16 v[204:207], a[4:7], v[72:75], 0\n"
"v_add_f32_e32 v253, 0x3727c5ac, v253\n"
"v_cvt_pk_bf16_f32 v253, v253, 0\n"
"v_mfma_f32_16x16x32_bf16 v[208:211], a[8:11], v[72:75], 0\n"
"s_waitcnt lgkmcnt(0)\n"
"v_fmac_f32_e32 v124, v255, v164\n"
"v_mfma_f32_16x16x32_bf16 v[212:215], a[12:15], v[72:75], 0\n"
"ds_write_b128 v3, v[84:87] offset:6144\n"
"v_mfma_f32_16x16x32_bf16 v[84:87], a[16:19], v[72:75], 0\n"
"s_add_u32 s98, s52, 0\n"
"v_fmac_f32_e32 v125, v255, v165\n"
"v_mfma_f32_16x16x32_bf16 v[216:219], a[20:23], v[72:75], 0\n"
"buffer_load_dwordx4 v[72:75], v0, s[4:7], s98 offen sc1\n"
"v_mfma_f32_16x16x32_bf16 v[160:163], a[24:27], v[24:27], v[160:163]\n"
"ds_write_b128 v3, v[100:103] offset:7168\n"
"v_mfma_f32_16x16x32_bf16 v[204:207], a[28:31], v[24:27], v[204:207]\n"
"v_fmac_f32_e32 v126, v255, v166\n"
"v_fmac_f32_e32 v127, v255, v167\n"
"v_mfma_f32_16x16x32_bf16 v[208:211], a[32:35], v[24:27], v[208:211]\n"
"v_fmac_f32_e32 v176, v255, v188\n"
"v_fmac_f32_e32 v177, v255, v189\n"
"v_mfma_f32_16x16x32_bf16 v[212:215], a[36:39], v[24:27], v[212:215]\n"
"ds_write_b128 v3, v[140:143] offset:8192\n"
"v_mfma_f32_16x16x32_bf16 v[84:87], a[40:43], v[24:27], v[84:87]\n"
"buffer_load_dwordx4 v[100:103], v0, s[4:7], s98 offen offset:1024 sc1\n"
"v_mfma_f32_16x16x32_bf16 v[216:219], a[44:47], v[24:27], v[216:219]\n"
"s_waitcnt vmcnt(20)\n"
"v_fmac_f32_e32 v178, v255, v190\n"
"v_mfma_f32_16x16x32_bf16 v[160:163], a[48:51], v[120:123], v[160:163]\n"
"ds_write_b128 v3, v[152:155] offset:9216\n"
"v_mfma_f32_16x16x32_bf16 v[204:207], a[52:55], v[120:123], v[204:207]\n"
"buffer_load_dwordx4 a[184:187], v1, s[4:7], s99 offen offset:2048 sc1\n"
"v_mfma_f32_16x16x32_bf16 v[208:211], a[56:59], v[120:123], v[208:211]\n"
"v_fmac_f32_e32 v179, v255, v191\n"
"v_fmac_f32_e32 v192, v255, v196\n"
"v_mfma_f32_16x16x32_bf16 v[212:215], a[60:63], v[120:123], v[212:215]\n"
"ds_write_b128 v3, v[156:159] offset:10240\n"
"v_mfma_f32_16x16x32_bf16 v[84:87], a[64:67], v[120:123], v[84:87]\n"
"buffer_load_dwordx4 v[24:27], v0, s[4:7], s98 offen offset:2048 sc1\n"
"v_mfma_f32_16x16x32_bf16 v[216:219], a[68:71], v[120:123], v[216:219]\n"
"v_fmac_f32_e32 v193, v255, v197\n"
"v_fmac_f32_e32 v194, v255, v198\n"
"v_mfma_f32_16x16x32_bf16 v[160:163], a[72:75], v[132:135], v[160:163]\n"
"ds_write_b128 v3, v[172:175] offset:11264\n"
"v_mfma_f32_16x16x32_bf16 v[204:207], a[76:79], v[132:135], v[204:207]\n"
"v_fmac_f32_e32 v195, v255, v199\n"
"ds_read_b128 v[120:123], v4 offset:3072\n"
"v_mfma_f32_16x16x32_bf16 v[208:211], a[80:83], v[132:135], v[208:211]\n"
"ds_read_b128 v[140:143], v9 offset:192\n"
"ds_read_b128 v[152:155], v4 offset:4096\n"
"v_mfma_f32_16x16x32_bf16 v[212:215], a[84:87], v[132:135], v[212:215]\n"
"ds_read_b128 v[156:159], v9 offset:256\n"
"ds_read_b128 v[164:167], v4 offset:5120\n"
"v_mfma_f32_16x16x32_bf16 v[84:87], a[88:91], v[132:135], v[84:87]\n"
"buffer_load_dwordx4 v[172:175], v0, s[4:7], s98 offen offset:3072 sc1\n"
"v_mfma_f32_16x16x32_bf16 v[216:219], a[92:95], v[132:135], v[216:219]\n"
"s_waitcnt vmcnt(19)\n"
"ds_read_b128 v[132:135], v9 offset:320\n"
"v_mfma_f32_16x16x32_bf16 v[160:163], a[96:99], v[112:115], v[160:163]\n"
"v_lshlrev_b32_e32 v253, 16, v253\n"
"v_mfma_f32_16x16x32_bf16 v[204:207], a[100:103], v[112:115], v[204:207]\n"
"v_sqrt_f32_e32 v253, v253\n"
"v_mfma_f32_16x16x32_bf16 v[208:211], a[104:107], v[112:115], v[208:211]\n"
"v_cvt_pk_bf16_f32 v253, v253, 0\n"
"v_lshlrev_b32_e32 v253, 16, v253\n"
"v_mfma_f32_16x16x32_bf16 v[212:215], a[108:111], v[112:115], v[212:215]\n"
"v_rcp_f32_e32 v254, v253\n"
"v_mfma_f32_16x16x32_bf16 v[84:87], a[112:115], v[112:115], v[84:87]\n"
"buffer_load_dwordx4 v[188:191], v1, s[4:7], s98 offen sc1\n"
"v_mfma_f32_16x16x32_bf16 v[216:219], a[116:119], v[112:115], v[216:219]\n"
"s_waitcnt lgkmcnt(0)\n"
"v_fmac_f32_e32 v120, v255, v140\n"
"v_mfma_f32_16x16x32_bf16 v[160:163], a[120:123], v[128:131], v[160:163]\n"
"v_fmac_f32_e32 v121, v255, v141\n"
"v_fmac_f32_e32 v122, v255, v142\n"
"v_mfma_f32_16x16x32_bf16 v[204:207], a[124:127], v[128:131], v[204:207]\n"
"v_fmac_f32_e32 v123, v255, v143\n"
"v_fmac_f32_e32 v152, v255, v156\n"
"v_mfma_f32_16x16x32_bf16 v[208:211], v[40:43], v[128:131], v[208:211]\n"
"v_fmac_f32_e32 v153, v255, v157\n"
"v_fmac_f32_e32 v154, v255, v158\n"
"v_mfma_f32_16x16x32_bf16 v[212:215], v[44:47], v[128:131], v[212:215]\n"
"buffer_load_dwordx4 a[188:191], v1, s[4:7], s99 offen offset:3072 sc1\n"
"v_mfma_f32_16x16x32_bf16 v[84:87], v[80:83], v[128:131], v[84:87]\n"
"buffer_load_dwordx4 v[112:115], v1, s[4:7], s98 offen offset:1024 sc1\n"
"v_mfma_f32_16x16x32_bf16 v[216:219], v[68:71], v[128:131], v[216:219]\n"
"s_waitcnt vmcnt(19)\n"
"v_mfma_f32_16x16x32_bf16 v[160:163], v[88:91], v[144:147], v[160:163]\n"
"s_add_u32 s99, s68, 0x1000\n"
"v_fmac_f32_e32 v155, v255, v159\n"
"v_mfma_f32_16x16x32_bf16 v[204:207], v[48:51], v[144:147], v[204:207]\n"
"buffer_store_dwordx2 v[18:19], v11, s[4:7], s99 offen offset:64 sc1\n"
"v_mfma_f32_16x16x32_bf16 v[208:211], v[92:95], v[144:147], v[208:211]\n"
"v_fmac_f32_e32 v164, v255, v132\n"
"v_fmac_f32_e32 v165, v255, v133\n"
"v_mfma_f32_16x16x32_bf16 v[212:215], v[96:99], v[144:147], v[212:215]\n"
"v_fmac_f32_e32 v166, v255, v134\n"
"v_fmac_f32_e32 v167, v255, v135\n"
"v_mfma_f32_16x16x32_bf16 v[84:87], v[168:171], v[144:147], v[84:87]\n"
"buffer_load_dwordx4 v[128:131], v1, s[4:7], s98 offen offset:2048 sc1\n"
"v_mfma_f32_16x16x32_bf16 v[216:219], v[104:107], v[144:147], v[216:219]\n"
"v_mul_f32_e32 v255, 0x3e8293ee, v254\n"
"v_mfma_f32_16x16x32_bf16 v[160:163], v[136:139], v[36:39], v[160:163]\n"
"v_mfma_f32_16x16x32_bf16 v[204:207], v[180:183], v[36:39], v[204:207]\n"
"v_mfma_f32_16x16x32_bf16 v[208:211], v[28:31], v[36:39], v[208:211]\n"
"v_mfma_f32_16x16x32_bf16 v[212:215], v[32:35], v[36:39], v[212:215]\n"
"v_mfma_f32_16x16x32_bf16 v[84:87], v[60:63], v[36:39], v[84:87]\n"
"buffer_load_dwordx4 v[132:135], v1, s[4:7], s98 offen offset:3072 sc1\n"
"v_mfma_f32_16x16x32_bf16 v[216:219], v[64:67], v[36:39], v[216:219]\n"
"s_waitcnt vmcnt(19)\n"
"; @A G1 2.3\n"
"v_mfma_f32_16x16x32_bf16 v[124:127], a[0:3], v[56:59], v[124:127]\n"
"v_mfma_f32_16x16x32_bf16 v[176:179], a[4:7], v[56:59], v[176:179]\n"
"v_mfma_f32_16x16x32_bf16 v[192:195], a[8:11], v[56:59], v[192:195]\n"
"v_mfma_f32_16x16x32_bf16 v[120:123], a[12:15], v[56:59], v[120:123]\n"
"ds_write_b128 v3, v[160:163] offset:12288\n"
"v_mfma_f32_16x16x32_bf16 v[152:155], a[16:19], v[56:59], v[152:155]\n"
"s_add_u32 s99, s53, 0\n"
"s_add_u32 s98, s70, 0x4000\n"
"v_mfma_f32_16x16x32_bf16 v[164:167], a[20:23], v[56:59], v[164:167]\n"
"buffer_load_dwordx4 v[36:39], v0, s[4:7], s99 offen sc1\n"
"v_mfma_f32_16x16x32_bf16 v[124:127], a[24:27], v[184:187], v[124:127]\n"
"ds_write_b128 v3, v[204:207] offset:13312\n"
"v_mfma_f32_16x16x32_bf16 v[176:179], a[28:31], v[184:187], v[176:179]\n"
"buffer_load_dwordx4 a[192:195], v0, s[4:7], s98 offen sc1\n"
"v_mfma_f32_16x16x32_bf16 v[192:195], a[32:35], v[184:187], v[192:195]\n"
"v_mfma_f32_16x16x32_bf16 v[120:123], a[36:39], v[184:187], v[120:123]\n"
"ds_write_b128 v3, v[208:211] offset:14336\n"
"v_mfma_f32_16x16x32_bf16 v[152:155], a[40:43], v[184:187], v[152:155]\n"
"buffer_load_dwordx4 v[56:59], v0, s[4:7], s99 offen offset:1024 sc1\n"
"v_mfma_f32_16x16x32_bf16 v[164:167], a[44:47], v[184:187], v[164:167]\n"
"s_waitcnt vmcnt(19)\n"
"v_mfma_f32_16x16x32_bf16 v[124:127], a[48:51], v[148:151], v[124:127]\n"
"ds_write_b128 v3, v[212:215] offset:15360\n"
"v_mfma_f32_16x16x32_bf16 v[176:179], a[52:55], v[148:151], v[176:179]\n"
"v_mfma_f32_16x16x32_bf16 v[192:195], a[56:59], v[148:151], v[192:195]\n"
"v_mfma_f32_16x16x32_bf16 v[120:123], a[60:63], v[148:151], v[120:123]\n"
"ds_write_b128 v3, v[84:87] offset:16384\n"
"v_mfma_f32_16x16x32_bf16 v[152:155], a[64:67], v[148:151], v[152:155]\n"
"buffer_load_dwordx4 v[84:87], v0, s[4:7], s99 offen offset:2048 sc1\n"
"v_mfma_f32_16x16x32_bf16 v[164:167], a[68:71], v[148:151], v[164:167]\n"
"v_mfma_f32_16x16x32_bf16 v[124:127], a[72:75], v[52:55], v[124:127]\n"
"ds_write_b128 v3, v[216:219] offset:17408\n"
"v_mfma_f32_16x16x32_bf16 v[176:179], a[76:79], v[52:55], v[176:179]\n"
"v_mfma_f32_16x16x32_bf16 v[192:195], a[80:83], v[52:55], v[192:195]\n"
"v_mfma_f32_16x16x32_bf16 v[120:123], a[84:87], v[52:55], v[120:123]\n"
"s_waitcnt lgkmcnt(0)\n"
"s_barrier\n"
"v_mfma_f32_16x16x32_bf16 v[152:155], a[88:91], v[52:55], v[152:155]\n"
"buffer_load_dwordx4 v[140:143], v0, s[4:7], s99 offen offset:3072 sc1\n"
"v_mfma_f32_16x16x32_bf16 v[164:167], a[92:95], v[52:55], v[164:167]\n"
"s_waitcnt vmcnt(19)\n"
"ds_read_b128 v[52:55], v5 offset:2048\n"
"v_mfma_f32_16x16x32_bf16 v[124:127], a[96:99], v[76:79], v[124:127]\n"
"ds_read_b128 v[144:147], v6 offset:2048\n"
"ds_read_b128 v[148:151], v5 offset:3072\n"
"v_mfma_f32_16x16x32_bf16 v[176:179], a[100:103], v[76:79], v[176:179]\n"
"ds_read_b128 v[156:159], v6 offset:3072\n"
"v_mfma_f32_16x16x32_bf16 v[192:195], a[104:107], v[76:79], v[192:195]\n"
"v_mfma_f32_16x16x32_bf16 v[120:123], a[108:111], v[76:79], v[120:123]\n"
"buffer_load_dwordx4 a[196:199], v0, s[4:7], s98 offen offset:1024 sc1\n"
"v_mfma_f32_16x16x32_bf16 v[152:155], a[112:115], v[76:79], v[152:155]\n"
"buffer_load_dwordx4 v[160:163], v1, s[4:7], s99 offen sc1\n"
"v_mfma_f32_16x16x32_bf16 v[164:167], a[116:119], v[76:79], v[164:167]\n"
"ds_read_b128 v[76:79], v5 offset:4096\n"
"ds_read_b128 v[184:187], v6 offset:4096\n"
"v_mfma_f32_16x16x32_bf16 v[124:127], a[120:123], v[108:111], v[124:127]\n"
"ds_read_b128 v[196:199], v5 offset:5120\n"
"ds_read_b128 v[204:207], v6 offset:5120\n"
"v_mfma_f32_16x16x32_bf16 v[176:179], a[124:127], v[108:111], v[176:179]\n"
"s_waitcnt lgkmcnt(4)\n"
"v_add_f32_e32 v52, v52, v144\n"
"v_mfma_f32_16x16x32_bf16 v[192:195], v[40:43], v[108:111], v[192:195]\n"
"v_add_f32_e32 v53, v53, v145\n"
"v_add_f32_e32 v54, v54, v146\n"
"v_mfma_f32_16x16x32_bf16 v[120:123], v[44:47], v[108:111], v[120:123]\n"
"ds_read_b128 v[208:211], v5\n"
"ds_read_b128 v[212:215], v6\n"
"v_mfma_f32_16x16x32_bf16 v[152:155], v[80:83], v[108:111], v[152:155]\n"
"buffer_load_dwordx4 v[216:219], v1, s[4:7], s99 offen offset:1024 sc1\n"
"v_mfma_f32_16x16x32_bf16 v[164:167], v[68:71], v[108:111], v[164:167]\n"
"s_waitcnt vmcnt(19)\n"
"ds_read_b128 v[108:111], v5 offset:1024\n"
"v_mfma_f32_16x16x32_bf16 v[124:127], v[88:91], v[116:119], v[124:127]\n"
"ds_read_b128 v[220:223], v6 offset:1024\n"
"v_add_f32_e32 v55, v55, v147\n"
"v_mfma_f32_16x16x32_bf16 v[176:179], v[48:51], v[116:119], v[176:179]\n"
"v_add_f32_e32 v148, v148, v156\n"
"v_add_f32_e32 v149, v149, v157\n"
"v_mfma_f32_16x16x32_bf16 v[192:195], v[92:95], v[116:119], v[192:195]\n"
"v_add_f32_e32 v150, v150, v158\n"
"v_add_f32_e32 v151, v151, v159\n"
"v_mfma_f32_16x16x32_bf16 v[120:123], v[96:99], v[116:119], v[120:123]\n"
"s_waitcnt lgkmcnt(4)\n"
"v_add_f32_e32 v76, v76, v184\n"
"v_mfma_f32_16x16x32_bf16 v[152:155], v[168:171], v[116:119], v[152:155]\n"
"buffer_load_dwordx4 v[144:147], v1, s[4:7], s99 offen offset:2048 sc1\n"
"v_mfma_f32_16x16x32_bf16 v[164:167], v[104:107], v[116:119], v[164:167]\n"
"v_add_f32_e32 v77, v77, v185\n"
"v_add_f32_e32 v78, v78, v186\n"
"v_mfma_f32_16x16x32_bf16 v[124:127], v[136:139], v[200:203], v[124:127]\n"
"v_add_f32_e32 v79, v79, v187\n"
"v_add_f32_e32 v196, v196, v204\n"
"v_mfma_f32_16x16x32_bf16 v[176:179], v[180:183], v[200:203], v[176:179]\n"
"v_add_f32_e32 v197, v197, v205\n"
"v_add_f32_e32 v198, v198, v206\n"
"v_mfma_f32_16x16x32_bf16 v[192:195], v[28:31], v[200:203], v[192:195]\n"
"v_add_f32_e32 v199, v199, v207\n"
"s_waitcnt lgkmcnt(0)\n"
"v_mfma_f32_16x16x32_bf16 v[120:123], v[32:35], v[200:203], v[120:123]\n"
"v_add_f32_e32 v208, v208, v212\n"
"v_add_f32_e32 v209, v209, v213\n"
"v_mfma_f32_16x16x32_bf16 v[152:155], v[60:63], v[200:203], v[152:155]\n"
"buffer_load_dwordx4 v[116:119], v1, s[4:7], s99 offen offset:3072 sc1\n"
"v_mfma_f32_16x16x32_bf16 v[164:167], v[64:67], v[200:203], v[164:167]\n"
"v_add_f32_e32 v210, v210, v214\n"
"v_add_f32_e32 v211, v211, v215\n"
"v_add_f32_e32 v108, v108, v220\n"
"v_add_f32_e32 v109, v109, v221\n"
"v_add_f32_e32 v110, v110, v222\n"
"v_add_f32_e32 v111, v111, v223\n"
"s_waitcnt vmcnt(19)\n"
"; @A G1 3.0\n"
"v_mfma_f32_16x16x32_bf16 v[156:159], a[0:3], v[72:75], 0\n"
"s_add_u32 s99, s62, 0\n"
"v_mfma_f32_16x16x32_bf16 v[184:187], a[4:7], v[72:75], 0\n"
"buffer_load_dwordx2 v[252:253], v10, s[4:7], s99 offen sc1\n"
"v_mfma_f32_16x16x32_bf16 v[200:203], a[8:11], v[72:75], 0\n"
"buffer_load_dwordx2 v[250:251], v10, s[4:7], s99 offen offset:128 sc1\n"
"v_mfma_f32_16x16x32_bf16 v[204:207], a[12:15], v[72:75], 0\n"
"buffer_load_dwordx2 v[248:249], v10, s[4:7], s99 offen offset:256 sc1\n"
"v_mfma_f32_16x16x32_bf16 v[212:215], a[16:19], v[72:75], 0\n"
"s_add_u32 s97, s54, 0\n"
"v_add_f32_e32 v192, v192, v52\n"
"v_mfma_f32_16x16x32_bf16 v[220:223], a[20:23], v[72:75], 0\n"
"buffer_load_dwordx4 v[72:75], v0, s[4:7], s97 offen sc1\n"
"v_mfma_f32_16x16x32_bf16 v[156:159], a[24:27], v[100:103], v[156:159]\n"
"buffer_load_dwordx2 v[246:247], v10, s[4:7], s99 offen offset:384 sc1\n"
"v_mfma_f32_16x16x32_bf16 v[184:187], a[28:31], v[100:103], v[184:187]\n"
"v_add_f32_e32 v193, v193, v53\n"
"v_add_f32_e32 v194, v194, v54\n"
"v_mfma_f32_16x16x32_bf16 v[200:203], a[32:35], v[100:103], v[200:203]\n"
"buffer_load_dwordx4 a[200:203], v0, s[4:7], s98 offen offset:2048 sc1\n"
"v_mfma_f32_16x16x32_bf16 v[204:207], a[36:39], v[100:103], v[204:207]\n"
"v_add_f32_e32 v195, v195, v55\n"
"v_add_f32_e32 v120, v120, v148\n"
"v_mfma_f32_16x16x32_bf16 v[212:215], a[40:43], v[100:103], v[212:215]\n"
"buffer_load_dwordx4 v[52:55], v0, s[4:7], s97 offen offset:1024 sc1\n"
"v_mfma_f32_16x16x32_bf16 v[220:223], a[44:47], v[100:103], v[220:223]\n"
"s_waitcnt vmcnt(23)\n"
"v_add_f32_e32 v121, v121, v149\n"
"v_mfma_f32_16x16x32_bf16 v[156:159], a[48:51], v[24:27], v[156:159]\n"
"v_add_f32_e32 v122, v122, v150\n"
"v_add_f32_e32 v123, v123, v151\n"
"v_mfma_f32_16x16x32_bf16 v[184:187], a[52:55], v[24:27], v[184:187]\n"
"v_mul_f32_e32 v192, v192, v254\n"
"v_mul_f32_e32 v193, v193, v254\n"
"v_mfma_f32_16x16x32_bf16 v[200:203], a[56:59], v[24:27], v[200:203]\n"
"v_mul_f32_e32 v194, v194, v254\n"
"v_mul_f32_e32 v195, v195, v254\n"
"v_mfma_f32_16x16x32_bf16 v[204:207], a[60:63], v[24:27], v[204:207]\n"
"v_mul_f32_e32 v120, v120, v254\n"
"v_mul_f32_e32 v121, v121, v254\n"
"v_mfma_f32_16x16x32_bf16 v[212:215], a[64:67], v[24:27], v[212:215]\n"
"buffer_load_dwordx4 v[100:103], v0, s[4:7], s97 offen offset:2048 sc1\n"
"v_mfma_f32_16x16x32_bf16 v[220:223], a[68:71], v[24:27], v[220:223]\n"
"v_mul_f32_e32 v122, v122, v254\n"
"v_mul_f32_e32 v123, v123, v254\n"
"v_mfma_f32_16x16x32_bf16 v[156:159], a[72:75], v[172:175], v[156:159]\n"
"v_cvt_pk_bf16_f32 v24, v192, v193\n"
"v_cvt_pk_bf16_f32 v25, v194, v195\n"
"v_mfma_f32_16x16x32_bf16 v[184:187], a[76:79], v[172:175], v[184:187]\n"
"v_cvt_pk_bf16_f32 v26, v120, v121\n"
"v_cvt_pk_bf16_f32 v27, v122, v123\n"
"v_mfma_f32_16x16x32_bf16 v[200:203], a[80:83], v[172:175], v[200:203]\n"
"ds_write_b128 v8, v[24:27] offset:17408\n"
"v_mfma_f32_16x16x32_bf16 v[204:207], a[84:87], v[172:175], v[204:207]\n"
"buffer_store_dwordx4 v[24:27], v8, s[4:7], s64 offen offset:1024 sc1\n"
"v_mfma_f32_16x16x32_bf16 v[212:215], a[88:91], v[172:175], v[212:215]\n"
"buffer_load_dwordx4 v[24:27], v0, s[4:7], s97 offen offset:3072 sc1\n"
"v_mfma_f32_16x16x32_bf16 v[220:223], a[92:95], v[172:175], v[220:223]\n"
"s_waitcnt vmcnt(23)\n"
"v_add_f32_e32 v152, v152, v76\n"
"v_mfma_f32_16x16x32_bf16 v[156:159], a[96:99], v[188:191], v[156:159]\n"
"v_add_f32_e32 v153, v153, v77\n"
"v_add_f32_e32 v154, v154, v78\n"
"v_mfma_f32_16x16x32_bf16 v[184:187], a[100:103], v[188:191], v[184:187]\n"
"v_add_f32_e32 v155, v155, v79\n"
"v_add_f32_e32 v164, v164, v196\n"
"v_mfma_f32_16x16x32_bf16 v[200:203], a[104:107], v[188:191], v[200:203]\n"
"buffer_load_dwordx4 a[204:207], v0, s[4:7], s98 offen offset:3072 sc1\n"
"v_mfma_f32_16x16x32_bf16 v[204:207], a[108:111], v[188:191], v[204:207]\n"
"v_add_f32_e32 v165, v165, v197\n"
"v_add_f32_e32 v166, v166, v198\n"
"v_mfma_f32_16x16x32_bf16 v[212:215], a[112:115], v[188:191], v[212:215]\n"
"buffer_load_dwordx4 v[76:79], v1, s[4:7], s97 offen sc1\n"
"v_mfma_f32_16x16x32_bf16 v[220:223], a[116:119], v[188:191], v[220:223]\n"
"v_add_f32_e32 v167, v167, v199\n"
"v_mul_f32_e32 v152, v152, v254\n"
"v_mfma_f32_16x16x32_bf16 v[156:159], a[120:123], v[112:115], v[156:159]\n"
"v_mul_f32_e32 v153, v153, v254\n"
"v_mul_f32_e32 v154, v154, v254\n"
"v_mfma_f32_16x16x32_bf16 v[184:187], a[124:127], v[112:115], v[184:187]\n"
"v_mul_f32_e32 v155, v155, v254\n"
"v_mul_f32_e32 v164, v164, v254\n"
"v_mfma_f32_16x16x32_bf16 v[200:203], v[40:43], v[112:115], v[200:203]\n"
"v_mul_f32_e32 v165, v165, v254\n"
"v_mul_f32_e32 v166, v166, v254\n"
"v_mfma_f32_16x16x32_bf16 v[204:207], v[44:47], v[112:115], v[204:207]\n"
"v_mul_f32_e32 v167, v167, v254\n"
"v_cvt_pk_bf16_f32 v120, v152, v153\n"
"v_mfma_f32_16x16x32_bf16 v[212:215], v[80:83], v[112:115], v[212:215]\n"
"buffer_load_dwordx4 v[148:151], v1, s[4:7], s97 offen offset:1024 sc1\n"
"v_mfma_f32_16x16x32_bf16 v[220:223], v[68:71], v[112:115], v[220:223]\n"
"s_waitcnt vmcnt(23)\n"
"v_cvt_pk_bf16_f32 v121, v154, v155\n"
"v_mfma_f32_16x16x32_bf16 v[156:159], v[88:91], v[128:131], v[156:159]\n"
"v_cvt_pk_bf16_f32 v122, v164, v165\n"
"v_cvt_pk_bf16_f32 v123, v166, v167\n"
"v_mfma_f32_16x16x32_bf16 v[184:187], v[48:51], v[128:131], v[184:187]\n"
"ds_write_b128 v7, v[120:123] offset:50176\n"
"v_mfma_f32_16x16x32_bf16 v[200:203], v[92:95], v[128:131], v[200:203]\n"
"s_add_u32 s99, s64, 0x4000\n"
"v_mfma_f32_16x16x32_bf16 v[204:207], v[96:99], v[128:131], v[204:207]\n"
"buffer_store_dwordx4 v[120:123], v7, s[4:7], s99 offen offset:1024 sc1\n"
"v_mfma_f32_16x16x32_bf16 v[212:215], v[168:171], v[128:131], v[212:215]\n"
"buffer_load_dwordx4 v[112:115], v1, s[4:7], s97 offen offset:2048 sc1\n"
"v_mfma_f32_16x16x32_bf16 v[220:223], v[104:107], v[128:131], v[220:223]\n"
"v_add_f32_e32 v124, v124, v208\n"
"v_add_f32_e32 v125, v125, v209\n"
"v_mfma_f32_16x16x32_bf16 v[156:159], v[136:139], v[132:135], v[156:159]\n"
"v_add_f32_e32 v126, v126, v210\n"
"v_add_f32_e32 v127, v127, v211\n"
"v_mfma_f32_16x16x32_bf16 v[184:187], v[180:183], v[132:135], v[184:187]\n"
"v_add_f32_e32 v176, v176, v108\n"
"v_add_f32_e32 v177, v177, v109\n"
"v_mfma_f32_16x16x32_bf16 v[200:203], v[28:31], v[132:135], v[200:203]\n"
"buffer_load_dwordx4 a[208:211], v1, s[4:7], s98 offen sc1\n"
"v_mfma_f32_16x16x32_bf16 v[204:207], v[32:35], v[132:135], v[204:207]\n"
"v_add_f32_e32 v178, v178, v110\n"
"v_add_f32_e32 v179, v179, v111\n"
"v_mfma_f32_16x16x32_bf16 v[212:215], v[60:63], v[132:135], v[212:215]\n"
"buffer_load_dwordx4 v[108:111], v1, s[4:7], s97 offen offset:3072 sc1\n"
"v_mfma_f32_16x16x32_bf16 v[220:223], v[64:67], v[132:135], v[220:223]\n"
"s_waitcnt vmcnt(24)\n"
"v_mul_f32_e32 v124, v124, v255\n"
"; @A G1 3.1\n"
"v_mfma_f32_16x16x32_bf16 v[120:123], a[0:3], v[36:39], 0\n"
"v_mul_f32_e32 v125, v125, v255\n"
"v_mul_f32_e32 v126, v126, v255\n"
"v_mfma_f32_16x16x32_bf16 v[128:131], a[4:7], v[36:39], 0\n"
"v_mul_f32_e32 v127, v127, v255\n"
"v_mul_f32_e32 v176, v176, v255\n"
"v_mfma_f32_16x16x32_bf16 v[132:135], a[8:11], v[36:39], 0\n"
"v_mul_f32_e32 v177, v177, v255\n"
"v_mul_f32_e32 v178, v178, v255\n"
"v_mfma_f32_16x16x32_bf16 v[152:155], a[12:15], v[36:39], 0\n"
"ds_write_b128 v3, v[156:159]\n"
"v_mfma_f32_16x16x32_bf16 v[156:159], a[16:19], v[36:39], 0\n"
"s_add_u32 s99, s55, 0\n"
"v_mul_f32_e32 v179, v179, v255\n"
"v_mfma_f32_16x16x32_bf16 v[164:167], a[20:23], v[36:39], 0\n"
"buffer_load_dwordx4 v[36:39], v0, s[4:7], s99 offen sc1\n"
"v_mfma_f32_16x16x32_bf16 v[120:123], a[24:27], v[56:59], v[120:123]\n"
"ds_write_b128 v3, v[184:187] offset:1024\n"
"v_mfma_f32_16x16x32_bf16 v[128:131], a[28:31], v[56:59], v[128:131]\n"
"v_cvt_pk_bf16_f32 v172, v124, v125\n"
"v_cvt_pk_bf16_f32 v173, v126, v127\n"
"v_mfma_f32_16x16x32_bf16 v[132:135], a[32:35], v[56:59], v[132:135]\n"
"v_cvt_pk_bf16_f32 v174, v176, v177\n"
"v_cvt_pk_bf16_f32 v175, v178, v179\n"
"v_mfma_f32_16x16x32_bf16 v[152:155], a[36:39], v[56:59], v[152:155]\n"
"ds_write_b128 v3, v[200:203] offset:2048\n"
"v_mfma_f32_16x16x32_bf16 v[156:159], a[40:43], v[56:59], v[156:159]\n"
"buffer_load_dwordx4 v[124:127], v0, s[4:7], s99 offen offset:1024 sc1\n"
"v_mfma_f32_16x16x32_bf16 v[164:167], a[44:47], v[56:59], v[164:167]\n"
"s_waitcnt vmcnt(24)\n"
"v_mfma_f32_16x16x32_bf16 v[120:123], a[48:51], v[84:87], v[120:123]\n"
"ds_write_b128 v3, v[204:207] offset:3072\n"
"v_mfma_f32_16x16x32_bf16 v[128:131], a[52:55], v[84:87], v[128:131]\n"
"ds_write_b128 v2, v[172:175] offset:17408\n"
"v_mfma_f32_16x16x32_bf16 v[132:135], a[56:59], v[84:87], v[132:135]\n"
"buffer_load_dwordx4 a[212:215], v1, s[4:7], s98 offen offset:1024 sc1\n"
"v_mfma_f32_16x16x32_bf16 v[152:155], a[60:63], v[84:87], v[152:155]\n"
"ds_write_b128 v3, v[212:215] offset:4096\n"
"v_mfma_f32_16x16x32_bf16 v[156:159], a[64:67], v[84:87], v[156:159]\n"
"buffer_load_dwordx4 v[56:59], v0, s[4:7], s99 offen offset:2048 sc1\n"
"v_mfma_f32_16x16x32_bf16 v[164:167], a[68:71], v[84:87], v[164:167]\n"
"v_mfma_f32_16x16x32_bf16 v[120:123], a[72:75], v[140:143], v[120:123]\n"
"ds_write_b128 v3, v[220:223] offset:5120\n"
"v_mfma_f32_16x16x32_bf16 v[128:131], a[76:79], v[140:143], v[128:131]\n"
"v_mfma_f32_16x16x32_bf16 v[132:135], a[80:83], v[140:143], v[132:135]\n"
"v_mfma_f32_16x16x32_bf16 v[152:155], a[84:87], v[140:143], v[152:155]\n"
"v_mfma_f32_16x16x32_bf16 v[156:159], a[88:91], v[140:143], v[156:159]\n"
"buffer_load_dwordx4 v[84:87], v0, s[4:7], s99 offen offset:3072 sc1\n"
"v_mfma_f32_16x16x32_bf16 v[164:167], a[92:95], v[140:143], v[164:167]\n"
"s_waitcnt vmcnt(24)\n"
"v_mfma_f32_16x16x32_bf16 v[120:123], a[96:99], v[160:163], v[120:123]\n"
"v_mfma_f32_16x16x32_bf16 v[128:131], a[100:103], v[160:163], v[128:131]\n"
"v_mfma_f32_16x16x32_bf16 v[132:135], a[104:107], v[160:163], v[132:135]\n"
"v_mfma_f32_16x16x32_bf16 v[152:155], a[108:111], v[160:163], v[152:155]\n"
"v_mfma_f32_16x16x32_bf16 v[156:159], a[112:115], v[160:163], v[156:159]\n"
"buffer_load_dwordx4 v[140:143], v1, s[4:7], s99 offen sc1\n"
"v_mfma_f32_16x16x32_bf16 v[164:167], a[116:119], v[160:163], v[164:167]\n"
"s_waitcnt lgkmcnt(0)\n"
"s_barrier\n"
"v_mfma_f32_16x16x32_bf16 v[120:123], a[120:123], v[216:219], v[120:123]\n"
"s_waitcnt vmcnt(18)\n"
"v_add_f32_e32 v252, v252, v250\n"
"v_mfma_f32_16x16x32_bf16 v[128:131], a[124:127], v[216:219], v[128:131]\n"
"v_add_f32_e32 v253, v253, v251\n"
"v_add_f32_e32 v248, v248, v246\n"
"v_mfma_f32_16x16x32_bf16 v[132:135], v[40:43], v[216:219], v[132:135]\n"
"buffer_load_dwordx4 a[216:219], v1, s[4:7], s98 offen offset:2048 sc1\n"
"v_mfma_f32_16x16x32_bf16 v[152:155], v[44:47], v[216:219], v[152:155]\n"
"v_add_f32_e32 v249, v249, v247\n"
"v_add_f32_e32 v252, v252, v248\n"
"v_mfma_f32_16x16x32_bf16 v[156:159], v[80:83], v[216:219], v[156:159]\n"
"buffer_load_dwordx4 v[160:163], v1, s[4:7], s99 offen offset:1024 sc1\n"
"v_mfma_f32_16x16x32_bf16 v[164:167], v[68:71], v[216:219], v[164:167]\n"
"v_add_f32_e32 v253, v253, v249\n"
"v_mfma_f32_16x16x32_bf16 v[120:123], v[88:91], v[144:147], v[120:123]\n"
"s_nop 0\n"
"v_permlane16_swap_b32_e32 v252, v253\n"
"v_add_f32_e32 v252, v252, v253\n"
"v_mfma_f32_16x16x32_bf16 v[128:131], v[48:51], v[144:147], v[128:131]\n"
"v_mov_b32_e32 v253, v252\n"
"v_mfma_f32_16x16x32_bf16 v[132:135], v[92:95], v[144:147], v[132:135]\n"
"s_nop 0\n"
"v_permlane32_swap_b32_e32 v252, v253\n"
"v_add_f32_e32 v252, v252, v253\n"
"v_mfma_f32_16x16x32_bf16 v[152:155], v[96:99], v[144:147], v[152:155]\n"
"v_mul_f32_e32 v252, 0x3a800000, v252\n"
"v_mov_b32_e32 v253, v252\n"
"v_mfma_f32_16x16x32_bf16 v[156:159], v[168:171], v[144:147], v[156:159]\n"
"buffer_load_dwordx4 v[172:175], v1, s[4:7], s99 offen offset:2048 sc1\n"
"v_mfma_f32_16x16x32_bf16 v[164:167], v[104:107], v[144:147], v[164:167]\n"
"v_permlane16_swap_b32_e32 v252, v253\n"
"v_cvt_pk_bf16_f32 v255, -v252, 0\n"
"v_mfma_f32_16x16x32_bf16 v[120:123], v[136:139], v[116:119], v[120:123]\n"
"s_add_u32 s97, s71, 0\n"
"v_fma_f32 v253, -v252, v252, v253\n"
"v_mfma_f32_16x16x32_bf16 v[128:131], v[180:183], v[116:119], v[128:131]\n"
"buffer_load_dwordx2 v[250:251], off, s[4:7], s97 sc1\n"
"v_mfma_f32_16x16x32_bf16 v[132:135], v[28:31], v[116:119], v[132:135]\n"
"v_lshlrev_b32_e32 v255, 16, v255\n"
"ds_read_b128 v[144:147], v4\n"
"v_mfma_f32_16x16x32_bf16 v[152:155], v[32:35], v[116:119], v[152:155]\n"
"ds_read_b128 v[176:179], v9\n"
"ds_read_b128 v[184:187], v4 offset:1024\n"
"v_mfma_f32_16x16x32_bf16 v[156:159], v[60:63], v[116:119], v[156:159]\n"
"buffer_load_dwordx4 v[188:191], v1, s[4:7], s99 offen offset:3072 sc1\n"
"v_mfma_f32_16x16x32_bf16 v[164:167], v[64:67], v[116:119], v[164:167]\n"
"s_waitcnt vmcnt(21)\n"
"; @A G1 3.2\n"
"v_mfma_f32_16x16x32_bf16 v[116:119], a[0:3], v[72:75], 0\n"
"buffer_store_dwordx2 v[18:19], v11, s[4:7], s68 offen offset:32 sc1\n"
"v_mfma_f32_16x16x32_bf16 v[192:195], a[4:7], v[72:75], 0\n"
"ds_read_b128 v[196:199], v9 offset:64\n"
"ds_read_b128 v[200:203], v4 offset:2048\n"
"v_mfma_f32_16x16x32_bf16 v[204:207], a[8:11], v[72:75], 0\n"
"buffer_load_dwordx4 a[220:223], v1, s[4:7], s98 offen offset:3072 sc1\n"
"v_mfma_f32_16x16x32_bf16 v[208:211], a[12:15], v[72:75], 0\n"
"ds_write_b128 v3, v[120:123] offset:6144\n"
"v_mfma_f32_16x16x32_bf16 v[120:123], a[16:19], v[72:75], 0\n"
"s_add_u32 s99, s70, 0x6000\n"
"ds_read_b128 v[212:215], v9 offset:128\n"
"v_mfma_f32_16x16x32_bf16 v[216:219], a[20:23], v[72:75], 0\n"
"ds_read_b128 v[72:75], v4 offset:3072\n"
"ds_read_b128 v[220:223], v9 offset:192\n"
"v_mfma_f32_16x16x32_bf16 v[116:119], a[24:27], v[52:55], v[116:119]\n"
"ds_write_b128 v3, v[128:131] offset:7168\n"
"v_mfma_f32_16x16x32_bf16 v[192:195], a[28:31], v[52:55], v[192:195]\n"
"buffer_load_dwordx4 a[224:227], v0, s[4:7], s99 offen sc1\n"
"v_mfma_f32_16x16x32_bf16 v[204:207], a[32:35], v[52:55], v[204:207]\n"
"ds_read_b128 v[128:131], v4 offset:4096\n"
"ds_read_b128 v[224:227], v9 offset:256\n"
"v_mfma_f32_16x16x32_bf16 v[208:211], a[36:39], v[52:55], v[208:211]\n"
"ds_write_b128 v3, v[132:135] offset:8192\n"
"v_mfma_f32_16x16x32_bf16 v[120:123], a[40:43], v[52:55], v[120:123]\n"
"s_waitcnt lgkmcnt(6)\n"
"v_fmac_f32_e32 v144, v255, v176\n"
"v_mfma_f32_16x16x32_bf16 v[216:219], a[44:47], v[52:55], v[216:219]\n"
"s_waitcnt vmcnt(21)\n"
"v_fmac_f32_e32 v145, v255, v177\n"
"v_mfma_f32_16x16x32_bf16 v[116:119], a[48:51], v[100:103], v[116:119]\n"
"ds_write_b128 v3, v[152:155] offset:9216\n"
"v_mfma_f32_16x16x32_bf16 v[192:195], a[52:55], v[100:103], v[192:195]\n"
"buffer_load_dwordx4 a[228:231], v0, s[4:7], s99 offen offset:1024 sc1\n"
"v_mfma_f32_16x16x32_bf16 v[204:207], a[56:59], v[100:103], v[204:207]\n"
"v_fmac_f32_e32 v146, v255, v178\n"
"v_fmac_f32_e32 v147, v255, v179\n"
"v_mfma_f32_16x16x32_bf16 v[208:211], a[60:63], v[100:103], v[208:211]\n"
"ds_write_b128 v3, v[156:159] offset:10240\n"
"v_mfma_f32_16x16x32_bf16 v[120:123], a[64:67], v[100:103], v[120:123]\n"
"v_fmac_f32_e32 v184, v255, v196\n"
"v_fmac_f32_e32 v185, v255, v197\n"
"v_mfma_f32_16x16x32_bf16 v[216:219], a[68:71], v[100:103], v[216:219]\n"
"buffer_load_dwordx4 a[232:235], v0, s[4:7], s99 offen offset:2048 sc1\n"
"v_mfma_f32_16x16x32_bf16 v[116:119], a[72:75], v[24:27], v[116:119]\n"
"ds_write_b128 v3, v[164:167] offset:11264\n"
"v_mfma_f32_16x16x32_bf16 v[192:195], a[76:79], v[24:27], v[192:195]\n"
"v_fmac_f32_e32 v186, v255, v198\n"
"v_fmac_f32_e32 v187, v255, v199\n"
"v_mfma_f32_16x16x32_bf16 v[204:207], a[80:83], v[24:27], v[204:207]\n"
"v_fmac_f32_e32 v200, v255, v212\n"
"v_fmac_f32_e32 v201, v255, v213\n"
"v_mfma_f32_16x16x32_bf16 v[208:211], a[84:87], v[24:27], v[208:211]\n"
"buffer_load_dwordx4 a[236:239], v0, s[4:7], s99 offen offset:3072 sc1\n"
"v_mfma_f32_16x16x32_bf16 v[120:123], a[88:91], v[24:27], v[120:123]\n"
"v_fmac_f32_e32 v202, v255, v214\n"
"v_fmac_f32_e32 v203, v255, v215\n"
"v_mfma_f32_16x16x32_bf16 v[216:219], a[92:95], v[24:27], v[216:219]\n"
"s_waitcnt vmcnt(21)\n"
"ds_read_b128 v[24:27], v4 offset:5120\n"
"v_mfma_f32_16x16x32_bf16 v[116:119], a[96:99], v[76:79], v[116:119]\n"
"ds_read_b128 v[52:55], v9 offset:320\n"
"v_max_f32_e32 v253, 0, v253\n"
"v_mfma_f32_16x16x32_bf16 v[192:195], a[100:103], v[76:79], v[192:195]\n"
"buffer_load_dwordx4 a[240:243], v1, s[4:7], s99 offen sc1\n"
"v_mfma_f32_16x16x32_bf16 v[204:207], a[104:107], v[76:79], v[204:207]\n"
"v_cvt_pk_bf16_f32 v253, v253, 0\n"
"v_lshlrev_b32_e32 v253, 16, v253\n"
"v_mfma_f32_16x16x32_bf16 v[208:211], a[108:111], v[76:79], v[208:211]\n"
"v_add_f32_e32 v253, 0x3727c5ac, v253\n"
"v_cvt_pk_bf16_f32 v253, v253, 0\n"
"v_mfma_f32_16x16x32_bf16 v[120:123], a[112:115], v[76:79], v[120:123]\n"
"v_lshlrev_b32_e32 v253, 16, v253\n"
"v_mfma_f32_16x16x32_bf16 v[216:219], a[116:119], v[76:79], v[216:219]\n"
"buffer_load_dwordx4 a[244:247], v1, s[4:7], s99 offen offset:1024 sc1\n"
"v_mfma_f32_16x16x32_bf16 v[116:119], a[120:123], v[148:151], v[116:119]\n"
"s_waitcnt lgkmcnt(0)\n"
"v_fmac_f32_e32 v72, v255, v220\n"
"v_mfma_f32_16x16x32_bf16 v[192:195], a[124:127], v[148:151], v[192:195]\n"
"v_fmac_f32_e32 v73, v255, v221\n"
"v_fmac_f32_e32 v74, v255, v222\n"
"v_mfma_f32_16x16x32_bf16 v[204:207], v[40:43], v[148:151], v[204:207]\n"
"v_fmac_f32_e32 v75, v255, v223\n"
"v_fmac_f32_e32 v128, v255, v224\n"
"v_mfma_f32_16x16x32_bf16 v[208:211], v[44:47], v[148:151], v[208:211]\n"
"buffer_load_dwordx4 a[248:251], v1, s[4:7], s99 offen offset:2048 sc1\n"
"v_mfma_f32_16x16x32_bf16 v[120:123], v[80:83], v[148:151], v[120:123]\n"
"v_fmac_f32_e32 v129, v255, v225\n"
"v_fmac_f32_e32 v130, v255, v226\n"
"v_mfma_f32_16x16x32_bf16 v[216:219], v[68:71], v[148:151], v[216:219]\n"
"s_waitcnt vmcnt(20)\n"
"v_fmac_f32_e32 v131, v255, v227\n"
"v_mfma_f32_16x16x32_bf16 v[116:119], v[88:91], v[112:115], v[116:119]\n"
"v_fmac_f32_e32 v24, v255, v52\n"
"v_fmac_f32_e32 v25, v255, v53\n"
"v_mfma_f32_16x16x32_bf16 v[192:195], v[48:51], v[112:115], v[192:195]\n"
"buffer_load_dwordx4 a[252:255], v1, s[4:7], s99 offen offset:3072 sc1\n"
"v_mfma_f32_16x16x32_bf16 v[204:207], v[92:95], v[112:115], v[204:207]\n"
"v_fmac_f32_e32 v26, v255, v54\n"
"v_fmac_f32_e32 v27, v255, v55\n"
"v_mfma_f32_16x16x32_bf16 v[208:211], v[96:99], v[112:115], v[208:211]\n"
"v_sqrt_f32_e32 v253, v253\n"
"v_mfma_f32_16x16x32_bf16 v[120:123], v[168:171], v[112:115], v[120:123]\n"
"v_cvt_pk_bf16_f32 v253, v253, 0\n"
"v_lshlrev_b32_e32 v253, 16, v253\n"
"v_mfma_f32_16x16x32_bf16 v[216:219], v[104:107], v[112:115], v[216:219]\n"
"v_rcp_f32_e32 v255, v253\n"
"v_mfma_f32_16x16x32_bf16 v[116:119], v[136:139], v[108:111], v[116:119]\n"
"v_mul_f32_e32 v254, 0x3e8293ee, v255\n"
"v_mfma_f32_16x16x32_bf16 v[192:195], v[180:183], v[108:111], v[192:195]\n"
"v_mfma_f32_16x16x32_bf16 v[204:207], v[28:31], v[108:111], v[204:207]\n"
"s_waitcnt vmcnt(11)\n"
"v_cmp_ne_u64_e32 vcc, v[250:251], v[18:19]\n"
"v_mfma_f32_16x16x32_bf16 v[208:211], v[32:35], v[108:111], v[208:211]\n"
"s_cbranch_vccnz .Lmk_f82s\n"
".Lmk_f82r:\n"
"s_add_u32 m0, s77, 0x6000\n"
"v_mfma_f32_16x16x32_bf16 v[120:123], v[60:63], v[108:111], v[120:123]\n"
"s_add_u32 s99, s65, 0x2000\n"
"v_mfma_f32_16x16x32_bf16 v[216:219], v[64:67], v[108:111], v[216:219]\n"
"; @A G1 3.3\n"
"v_mfma_f32_16x16x32_bf16 v[144:147], a[0:3], v[36:39], v[144:147]\n"
"buffer_load_dwordx4 v2, s[4:7], s99 offen sc1 lds\n"
"v_mfma_f32_16x16x32_bf16 v[184:187], a[4:7], v[36:39], v[184:187]\n"
"buffer_load_dwordx4 v2, s[4:7], s99 offen offset:1024 sc1 lds\n"
"v_mfma_f32_16x16x32_bf16 v[200:203], a[8:11], v[36:39], v[200:203]\n"
"v_xor_b32_e32 v253, 32, v16\n"
"v_xor_b32_e32 v252, 32, v15\n"
"v_mfma_f32_16x16x32_bf16 v[72:75], a[12:15], v[36:39], v[72:75]\n"
"ds_write_b128 v3, v[116:119] offset:12288\n"
"v_mfma_f32_16x16x32_bf16 v[128:131], a[16:19], v[36:39], v[128:131]\n"
"v_mov_b32_e32 v100, 0x3f803f80\n"
"v_mov_b32_e32 v101, 0x3f803f80\n"
"v_mfma_f32_16x16x32_bf16 v[24:27], a[20:23], v[36:39], v[24:27]\n"
"v_mov_b32_e32 v102, 0x3f803f80\n"
"v_mov_b32_e32 v103, 0x3f803f80\n"
"v_mfma_f32_16x16x32_bf16 v[144:147], a[24:27], v[124:127], v[144:147]\n"
"ds_write_b128 v3, v[192:195] offset:13312\n"
"v_mfma_f32_16x16x32_bf16 v[184:187], a[28:31], v[124:127], v[184:187]\n"
"s_add_u32 m0, s77, 0xe000\n"
"s_add_u32 s99, s65, 0x6000\n"
"v_mfma_f32_16x16x32_bf16 v[200:203], a[32:35], v[124:127], v[200:203]\n"
"buffer_load_dwordx4 v2, s[4:7], s99 offen sc1 lds\n"
"v_mfma_f32_16x16x32_bf16 v[72:75], a[36:39], v[124:127], v[72:75]\n"
"ds_write_b128 v3, v[204:207] offset:14336\n"
"v_mfma_f32_16x16x32_bf16 v[128:131], a[40:43], v[124:127], v[128:131]\n"
"buffer_load_dwordx4 v2, s[4:7], s99 offen offset:1024 sc1 lds\n"
"v_mfma_f32_16x16x32_bf16 v[24:27], a[44:47], v[124:127], v[24:27]\n"
"v_mov_b32_e32 v251, 0x53800000\n"
"v_mfma_f32_16x16x32_bf16 v[144:147], a[48:51], v[56:59], v[144:147]\n"
"ds_write_b128 v3, v[208:211] offset:15360\n"
"v_mfma_f32_16x16x32_bf16 v[184:187], a[52:55], v[56:59], v[184:187]\n"
"v_mfma_f32_16x16x32_bf16 v[200:203], a[56:59], v[56:59], v[200:203]\n"
"v_mfma_f32_16x16x32_bf16 v[72:75], a[60:63], v[56:59], v[72:75]\n"
"ds_write_b128 v3, v[120:123] offset:16384\n"
"v_mfma_f32_16x16x32_bf16 v[128:131], a[64:67], v[56:59], v[128:131]\n"
"s_add_u32 s99, s69, 0x600000\n"
"v_mfma_f32_16x16x32_bf16 v[24:27], a[68:71], v[56:59], v[24:27]\n"
"buffer_load_dwordx4 a[0:3], v0, s[4:7], s99 offen sc1\n"
"v_mfma_f32_16x16x32_bf16 v[144:147], a[72:75], v[84:87], v[144:147]\n"
"ds_write_b128 v3, v[216:219] offset:17408\n"
"v_mfma_f32_16x16x32_bf16 v[184:187], a[76:79], v[84:87], v[184:187]\n"
"v_mfma_f32_16x16x32_bf16 v[200:203], a[80:83], v[84:87], v[200:203]\n"
"v_mfma_f32_16x16x32_bf16 v[72:75], a[84:87], v[84:87], v[72:75]\n"
"s_waitcnt lgkmcnt(0)\n"
"s_barrier\n"
"v_mfma_f32_16x16x32_bf16 v[128:131], a[88:91], v[84:87], v[128:131]\n"
"buffer_load_dwordx4 a[4:7], v0, s[4:7], s99 offen offset:1024 sc1\n"
"v_mfma_f32_16x16x32_bf16 v[24:27], a[92:95], v[84:87], v[24:27]\n"
"ds_read_b128 v[36:39], v5 offset:2048\n"
"v_mfma_f32_16x16x32_bf16 v[144:147], a[96:99], v[140:143], v[144:147]\n"
"ds_read_b128 v[52:55], v6 offset:2048\n"
"ds_read_b128 v[56:59], v5 offset:3072\n"
"v_mfma_f32_16x16x32_bf16 v[184:187], a[100:103], v[140:143], v[184:187]\n"
"ds_read_b128 v[76:79], v6 offset:3072\n"
"v_mfma_f32_16x16x32_bf16 v[200:203], a[104:107], v[140:143], v[200:203]\n"
"v_mfma_f32_16x16x32_bf16 v[72:75], a[108:111], v[140:143], v[72:75]\n"
"buffer_load_dwordx4 a[8:11], v0, s[4:7], s99 offen offset:2048 sc1\n"
"v_mfma_f32_16x16x32_bf16 v[128:131], a[112:115], v[140:143], v[128:131]\n"
"ds_read_b128 v[84:87], v5 offset:4096\n"
"ds_read_b128 v[108:111], v6 offset:4096\n"
"v_mfma_f32_16x16x32_bf16 v[24:27], a[116:119], v[140:143], v[24:27]\n"
"ds_read_b128 v[112:115], v5 offset:5120\n"
"ds_read_b128 v[116:119], v6 offset:5120\n"
"v_mfma_f32_16x16x32_bf16 v[144:147], a[120:123], v[160:163], v[144:147]\n"
"s_waitcnt lgkmcnt(4)\n"
"v_add_f32_e32 v36, v36, v52\n"
"v_mfma_f32_16x16x32_bf16 v[184:187], a[124:127], v[160:163], v[184:187]\n"
"v_add_f32_e32 v37, v37, v53\n"
"v_add_f32_e32 v38, v38, v54\n"
"v_mfma_f32_16x16x32_bf16 v[200:203], v[40:43], v[160:163], v[200:203]\n"
"buffer_load_dwordx4 a[12:15], v0, s[4:7], s99 offen offset:3072 sc1\n"
"v_mfma_f32_16x16x32_bf16 v[72:75], v[44:47], v[160:163], v[72:75]\n"
"ds_read_b128 v[40:43], v5\n"
"ds_read_b128 v[44:47], v6\n"
"v_mfma_f32_16x16x32_bf16 v[128:131], v[80:83], v[160:163], v[128:131]\n"
"ds_read_b128 v[80:83], v5 offset:1024\n"
"ds_read_b128 v[120:123], v6 offset:1024\n"
"v_mfma_f32_16x16x32_bf16 v[24:27], v[68:71], v[160:163], v[24:27]\n"
"s_waitcnt vmcnt(18)\n"
"v_add_f32_e32 v39, v39, v55\n"
"v_mfma_f32_16x16x32_bf16 v[144:147], v[88:91], v[172:175], v[144:147]\n"
"v_add_f32_e32 v56, v56, v76\n"
"v_add_f32_e32 v57, v57, v77\n"
"v_mfma_f32_16x16x32_bf16 v[184:187], v[48:51], v[172:175], v[184:187]\n"
"buffer_load_dwordx4 a[16:19], v1, s[4:7], s99 offen sc1\n"
"v_mfma_f32_16x16x32_bf16 v[200:203], v[92:95], v[172:175], v[200:203]\n"
"v_add_f32_e32 v58, v58, v78\n"
"v_add_f32_e32 v59, v59, v79\n"
"v_mfma_f32_16x16x32_bf16 v[72:75], v[96:99], v[172:175], v[72:75]\n"
"s_waitcnt lgkmcnt(4)\n"
"v_add_f32_e32 v84, v84, v108\n"
"v_mfma_f32_16x16x32_bf16 v[128:131], v[168:171], v[172:175], v[128:131]\n"
"v_add_f32_e32 v85, v85, v109\n"
"v_add_f32_e32 v86, v86, v110\n"
"v_mfma_f32_16x16x32_bf16 v[24:27], v[104:107], v[172:175], v[24:27]\n"
"v_add_f32_e32 v87, v87, v111\n"
"v_add_f32_e32 v112, v112, v116\n"
"v_mfma_f32_16x16x32_bf16 v[144:147], v[136:139], v[188:191], v[144:147]\n"
"buffer_load_dwordx4 a[20:23], v1, s[4:7], s99 offen offset:1024 sc1\n"
"v_mfma_f32_16x16x32_bf16 v[184:187], v[180:183], v[188:191], v[184:187]\n"
"v_add_f32_e32 v113, v113, v117\n"
"v_add_f32_e32 v114, v114, v118\n"
"v_mfma_f32_16x16x32_bf16 v[200:203], v[28:31], v[188:191], v[200:203]\n"
"v_add_f32_e32 v115, v115, v119\n"
"s_waitcnt lgkmcnt(0)\n"
"v_mfma_f32_16x16x32_bf16 v[72:75], v[32:35], v[188:191], v[72:75]\n"
"v_add_f32_e32 v40, v40, v44\n"
"v_add_f32_e32 v41, v41, v45\n"
"v_mfma_f32_16x16x32_bf16 v[128:131], v[60:63], v[188:191], v[128:131]\n"
"v_add_f32_e32 v42, v42, v46\n"
"v_add_f32_e32 v43, v43, v47\n"
"v_mfma_f32_16x16x32_bf16 v[24:27], v[64:67], v[188:191], v[24:27]\n"
"buffer_load_dwordx4 a[24:27], v1, s[4:7], s99 offen offset:2048 sc1\n"
"v_add_f32_e32 v80, v80, v120\n"
"v_add_f32_e32 v81, v81, v121\n"
"v_add_f32_e32 v82, v82, v122\n"
"v_add_f32_e32 v83, v83, v123\n"
"buffer_load_dwordx4 a[28:31], v1, s[4:7], s99 offen offset:3072 sc1\n"
"; @A tail\n"
"v_add_f32_e32 v200, v200, v36\n"
"v_add_f32_e32 v201, v201, v37\n"
"v_add_f32_e32 v202, v202, v38\n"
"v_add_f32_e32 v203, v203, v39\n"
"v_add_f32_e32 v72, v72, v56\n"
"v_add_f32_e32 v73, v73, v57\n"
"v_add_f32_e32 v74, v74, v58\n"
"v_add_f32_e32 v75, v75, v59\n"
"v_mul_f32_e32 v200, v200, v255\n"
"v_mul_f32_e32 v201, v201, v255\n"
"v_mul_f32_e32 v202, v202, v255\n"
"v_mul_f32_e32 v203, v203, v255\n"
"v_mul_f32_e32 v72, v72, v255\n"
"v_mul_f32_e32 v73, v73, v255\n"
"v_mul_f32_e32 v74, v74, v255\n"
"v_mul_f32_e32 v75, v75, v255\n"
"v_cvt_pk_bf16_f32 v28, v200, v201\n"
"v_cvt_pk_bf16_f32 v29, v202, v203\n"
"v_cvt_pk_bf16_f32 v30, v72, v73\n"
"v_cvt_pk_bf16_f32 v31, v74, v75\n"
"ds_write_b128 v7, v[28:31] offset:16384\n"
"buffer_store_dwordx4 v[28:31], v7, s[4:7], s64 offen sc1\n"
"v_add_f32_e32 v128, v128, v84\n"
"v_add_f32_e32 v129, v129, v85\n"
"v_add_f32_e32 v130, v130, v86\n"
"v_add_f32_e32 v131, v131, v87\n"
"v_add_f32_e32 v24, v24, v112\n"
"v_add_f32_e32 v25, v25, v113\n"
"v_add_f32_e32 v26, v26, v114\n"
"v_add_f32_e32 v27, v27, v115\n"
"v_mul_f32_e32 v128, v128, v255\n"
"v_mul_f32_e32 v129, v129, v255\n"
"v_mul_f32_e32 v130, v130, v255\n"
"v_mul_f32_e32 v131, v131, v255\n"
"v_mul_f32_e32 v24, v24, v255\n"
"v_mul_f32_e32 v25, v25, v255\n"
"v_mul_f32_e32 v26, v26, v255\n"
"v_mul_f32_e32 v27, v27, v255\n"
"v_cvt_pk_bf16_f32 v28, v128, v129\n"
"v_cvt_pk_bf16_f32 v29, v130, v131\n"
"v_cvt_pk_bf16_f32 v30, v24, v25\n"
"v_cvt_pk_bf16_f32 v31, v26, v27\n"
"ds_write_b128 v7, v[28:31] offset:49152\n"
"s_add_u32 s99, s69, 0x602000\n"
"buffer_load_dwordx4 a[32:35], v0, s[4:7], s99 offen sc1\n"
"s_add_u32 s98, s64, 0x4000\n"
"buffer_store_dwordx4 v[28:31], v7, s[4:7], s98 offen sc1\n"
"v_add_f32_e32 v144, v144, v40\n"
"v_add_f32_e32 v145, v145, v41\n"
"v_add_f32_e32 v146, v146, v42\n"
"v_add_f32_e32 v147, v147, v43\n"
"v_add_f32_e32 v184, v184, v80\n"
"buffer_load_dwordx4 a[36:39], v0, s[4:7], s99 offen offset:1024 sc1\n"
"v_add_f32_e32 v185, v185, v81\n"
"v_add_f32_e32 v186, v186, v82\n"
"v_add_f32_e32 v187, v187, v83\n"
"v_mul_f32_e32 v144, v144, v254\n"
"v_mul_f32_e32 v145, v145, v254\n"
"v_mul_f32_e32 v146, v146, v254\n"
"v_mul_f32_e32 v147, v147, v254\n"
"v_mul_f32_e32 v184, v184, v254\n"
"v_mul_f32_e32 v185, v185, v254\n"
"v_mul_f32_e32 v186, v186, v254\n"
"buffer_load_dwordx4 a[40:43], v0, s[4:7], s99 offen offset:2048 sc1\n"
"v_mul_f32_e32 v187, v187, v254\n"
"v_cvt_pk_bf16_f32 v24, v144, v145\n"
"v_cvt_pk_bf16_f32 v25, v146, v147\n"
"v_cvt_pk_bf16_f32 v26, v184, v185\n"
"v_cvt_pk_bf16_f32 v27, v186, v187\n"
"ds_write_b128 v2, v[24:27] offset:16384\n"
"s_waitcnt lgkmcnt(0)\n"
"s_barrier\n"
"ds_read_b128 v[40:43], v2 offset:49152\n"
"ds_read_b128 v[44:47], v2 offset:50176\n"
"ds_read_b128 v[88:91], v2 offset:16384\n"
"ds_read_b128 v[92:95], v2 offset:17408\n"
"; @A AT b\n"
"s_mov_b32 s2, 0\n"
"v_mov_b32_e32 v255, 0xff800000\n"
"v_mov_b32_e32 v254, 0xff800000\n"
"v_mov_b32_e32 v250, 0x7f800000\n"
"v_mov_b32_e32 v249, 0x7f800000\n"
".Lmk_a99:\n"
"ds_read_b128 v[24:27], v15 offset:24576\n"
"ds_read_b128 v[28:31], v252 offset:24832\n"
"ds_read_b128 v[32:35], v15 offset:22528\n"
"ds_read_b128 v[36:39], v252 offset:22784\n"
"s_cmp_gt_i32 s78, 11\n"
"s_cselect_b32 s98, 0x80000000, 0\n"
"s_or_b32 s98, s98, 0x7f800000\n"
"buffer_load_dwordx4 a[44:47], v0, s[4:7], s99 offen offset:3072 sc1\n"
"buffer_load_dwordx4 a[48:51], v1, s[4:7], s99 offen sc1\n"
"s_waitcnt lgkmcnt(2)\n"
"v_mfma_f32_16x16x32_bf16 v[48:51], v[24:27], v[40:43], 0\n"
"buffer_load_dwordx4 a[52:55], v1, s[4:7], s99 offen offset:1024 sc1\n"
"v_mfma_f32_16x16x32_bf16 v[52:55], v[24:27], v[44:47], 0\n"
"v_mfma_f32_16x16x32_bf16 v[56:59], v[28:31], v[40:43], 0\n"
"v_mfma_f32_16x16x32_bf16 v[60:63], v[28:31], v[44:47], 0\n"
"s_nop 3\n"
"v_max_f32_e32 v248, v48, v49\n"
"s_nop 0\n"
"v_max_f32_e32 v247, v52, v53\n"
"v_max3_f32 v248, v248, v50, v51\n"
"v_max3_f32 v247, v247, v54, v55\n"
"v_max3_f32 v248, v248, v56, v57\n"
"v_max3_f32 v247, v247, v60, v61\n"
"v_max3_f32 v248, v248, v58, v59\n"
"v_max3_f32 v247, v247, v62, v63\n"
"v_mov_b32_e32 v246, v248\n"
"v_mov_b32_e32 v245, v247\n"
"s_nop 0\n"
"v_permlane16_swap_b32_e32 v248, v246\n"
"v_permlane16_swap_b32_e32 v247, v245\n"
"v_max_f32_e32 v248, v248, v246\n"
"v_max_f32_e32 v247, v247, v245\n"
"v_mov_b32_e32 v246, v248\n"
"v_mov_b32_e32 v245, v247\n"
"s_nop 0\n"
"v_permlane32_swap_b32_e32 v248, v246\n"
"v_permlane32_swap_b32_e32 v247, v245\n"
"v_max_f32_e32 v248, v248, v246\n"
"v_max_f32_e32 v247, v247, v245\n"
"v_med3_f32 v248, v248, v255, v250\n"
"v_med3_f32 v247, v247, v254, v249\n"
"v_min_f32_e64 v64, s98, -v248\n"
"v_min_f32_e64 v65, s98, -v248\n"
"v_min_f32_e64 v66, s98, -v248\n"
"v_min_f32_e64 v67, s98, -v248\n"
"v_min_f32_e64 v68, s98, -v247\n"
"v_min_f32_e64 v69, s98, -v247\n"
"v_min_f32_e64 v70, s98, -v247\n"
"v_min_f32_e64 v71, s98, -v247\n"
"s_waitcnt lgkmcnt(0)\n"
"v_mfma_f32_16x16x32_bf16 v[24:27], v[32:35], v[40:43], v[64:67]\n"
"ds_read_b128 v[28:31], v15 offset:20480\n"
"v_sub_f32_e32 v48, v48, v248\n"
"v_sub_f32_e32 v49, v49, v248\n"
"v_sub_f32_e32 v50, v50, v248\n"
"buffer_load_dwordx4 a[56:59], v1, s[4:7], s99 offen offset:2048 sc1\n"
"v_sub_f32_e32 v51, v51, v248\n"
"v_mfma_f32_16x16x32_bf16 v[72:75], v[32:35], v[44:47], v[68:71]\n"
"v_sub_f32_e32 v56, v56, v248\n"
"v_sub_f32_e32 v57, v57, v248\n"
"v_sub_f32_e32 v58, v58, v248\n"
"v_sub_f32_e32 v59, v59, v248\n"
"buffer_load_dwordx4 a[60:63], v1, s[4:7], s99 offen offset:3072 sc1\n"
"v_mfma_f32_16x16x32_bf16 v[32:35], v[36:39], v[40:43], v[64:67]\n"
"ds_read_b128 v[76:79], v252 offset:20736\n"
"v_sub_f32_e32 v52, v52, v247\n"
"v_sub_f32_e32 v53, v53, v247\n"
"v_sub_f32_e32 v54, v54, v247\n"
"v_sub_f32_e32 v55, v55, v247\n"
"s_add_u32 s98, s69, 0x604000\n"
"buffer_load_dwordx4 a[64:67], v0, s[4:7], s98 offen sc1\n"
"v_mfma_f32_16x16x32_bf16 v[80:83], v[36:39], v[44:47], v[68:71]\n"
"v_sub_f32_e32 v60, v60, v247\n"
"v_sub_f32_e32 v61, v61, v247\n"
"v_sub_f32_e32 v62, v62, v247\n"
"v_sub_f32_e32 v63, v63, v247\n"
"s_waitcnt lgkmcnt(0)\n"
"v_mfma_f32_16x16x32_bf16 v[36:39], v[28:31], v[40:43], v[64:67]\n"
"ds_read_b128 v[84:87], v15 offset:18432\n"
"v_exp_f32_e32 v24, v24\n"
"v_exp_f32_e32 v25, v25\n"
"v_exp_f32_e32 v26, v26\n"
"buffer_load_dwordx4 a[68:71], v0, s[4:7], s98 offen offset:1024 sc1\n"
"v_exp_f32_e32 v27, v27\n"
"ds_read_b128 v[96:99], v252 offset:18688\n"
"v_exp_f32_e32 v32, v32\n"
"v_mfma_f32_16x16x32_bf16 v[104:107], v[28:31], v[44:47], v[68:71]\n"
"v_exp_f32_e32 v33, v33\n"
"v_exp_f32_e32 v34, v34\n"
"ds_read_b64_tr_b16 v[28:29], v16 offset:22528\n"
"v_exp_f32_e32 v35, v35\n"
"v_cvt_pk_bf16_f32 v108, v24, v25\n"
"buffer_load_dwordx4 a[72:75], v0, s[4:7], s98 offen offset:2048 sc1\n"
"v_cvt_pk_bf16_f32 v109, v26, v27\n"
"v_cvt_pk_bf16_f32 v110, v32, v33\n"
"v_mfma_f32_16x16x32_bf16 v[24:27], v[76:79], v[40:43], v[64:67]\n"
"v_cvt_pk_bf16_f32 v111, v34, v35\n"
"ds_read_b64_tr_b16 v[30:31], v253 offset:22784\n"
"v_exp_f32_e32 v72, v72\n"
"v_exp_f32_e32 v73, v73\n"
"v_exp_f32_e32 v74, v74\n"
"v_exp_f32_e32 v75, v75\n"
"ds_read_b64_tr_b16 v[32:33], v253 offset:22528\n"
"buffer_load_dwordx4 a[76:79], v0, s[4:7], s98 offen offset:3072 sc1\n"
"v_mfma_f32_16x16x32_bf16 v[112:115], v[76:79], v[44:47], v[68:71]\n"
"v_exp_f32_e32 v80, v80\n"
"v_exp_f32_e32 v81, v81\n"
"v_exp_f32_e32 v82, v82\n"
"ds_read_b64_tr_b16 v[34:35], v16 offset:22784\n"
"v_exp_f32_e32 v83, v83\n"
"v_cvt_pk_bf16_f32 v76, v72, v73\n"
"v_cvt_pk_bf16_f32 v77, v74, v75\n"
"v_cvt_pk_bf16_f32 v78, v80, v81\n"
"v_cvt_pk_bf16_f32 v79, v82, v83\n"
"s_waitcnt lgkmcnt(4)\n"
"v_mfma_f32_16x16x32_bf16 v[72:75], v[84:87], v[40:43], v[64:67]\n"
"ds_read_b128 v[80:83], v15 offset:16384\n"
"v_exp_f32_e32 v36, v36\n"
"v_exp_f32_e32 v37, v37\n"
"v_exp_f32_e32 v38, v38\n"
"v_mfma_f32_16x16x32_bf16 v[116:119], v[84:87], v[44:47], v[68:71]\n"
"buffer_load_dwordx4 a[80:83], v1, s[4:7], s98 offen sc1\n"
"ds_read_b128 v[84:87], v252 offset:16640\n"
"v_exp_f32_e32 v39, v39\n"
"v_mfma_f32_16x16x32_bf16 v[120:123], v[96:99], v[40:43], v[64:67]\n"
"v_exp_f32_e32 v24, v24\n"
"v_exp_f32_e32 v25, v25\n"
"ds_read_b64_tr_b16 v[124:125], v16 offset:20480\n"
"v_mfma_f32_16x16x32_bf16 v[128:131], v[96:99], v[44:47], v[68:71]\n"
"v_exp_f32_e32 v26, v26\n"
"v_exp_f32_e32 v27, v27\n"
"v_cvt_pk_bf16_f32 v96, v36, v37\n"
"v_cvt_pk_bf16_f32 v97, v38, v39\n"
"s_waitcnt lgkmcnt(3)\n"
"v_mfma_f32_16x16x32_bf16 v[132:135], v[100:103], v[108:111], 0\n"
"ds_read_b64_tr_b16 v[126:127], v253 offset:20736\n"
"buffer_load_dwordx4 a[84:87], v1, s[4:7], s98 offen offset:1024 sc1\n"
"v_cvt_pk_bf16_f32 v98, v24, v25\n"
"v_cvt_pk_bf16_f32 v99, v26, v27\n"
"v_mfma_f32_16x16x32_bf16 v[136:139], v[28:31], v[108:111], 0\n"
"v_exp_f32_e32 v104, v104\n"
"v_exp_f32_e32 v105, v105\n"
"ds_read_b64_tr_b16 v[24:25], v253 offset:20480\n"
"v_exp_f32_e32 v106, v106\n"
"v_mfma_f32_16x16x32_bf16 v[140:143], v[32:35], v[108:111], 0\n"
"v_exp_f32_e32 v107, v107\n"
"v_exp_f32_e32 v112, v112\n"
"ds_read_b64_tr_b16 v[26:27], v16 offset:20736\n"
"v_mfma_f32_16x16x32_bf16 v[108:111], v[100:103], v[76:79], 0\n"
"buffer_load_dwordx4 a[88:91], v1, s[4:7], s98 offen offset:2048 sc1\n"
"v_exp_f32_e32 v113, v113\n"
"v_exp_f32_e32 v114, v114\n"
"v_mfma_f32_16x16x32_bf16 v[144:147], v[28:31], v[76:79], 0\n"
"v_exp_f32_e32 v115, v115\n"
"s_waitcnt vmcnt(14)\n"
"buffer_store_dwordx2 v[18:19], v11, s[4:7], s68 offen offset:64 sc1\n"
"v_mfma_f32_16x16x32_bf16 v[148:151], v[32:35], v[76:79], 0\n"
"v_cvt_pk_bf16_f32 v28, v104, v105\n"
"v_cvt_pk_bf16_f32 v29, v106, v107\n"
"v_cvt_pk_bf16_f32 v30, v112, v113\n"
"v_cvt_pk_bf16_f32 v31, v114, v115\n"
"s_cmp_lg_u32 s2, 0\n"
"s_cbranch_scc1 .Lmk_s101\n"
"s_barrier\n"
".Lmk_s101:\n"
"s_waitcnt lgkmcnt(4)\n"
"v_mfma_f32_16x16x32_bf16 v[32:35], v[80:83], v[40:43], v[64:67]\n"
"ds_read_b128 v[36:39], v15 offset:14336\n"
"v_exp_f32_e32 v72, v72\n"
"v_exp_f32_e32 v73, v73\n"
"v_exp_f32_e32 v74, v74\n"
"v_mfma_f32_16x16x32_bf16 v[76:79], v[80:83], v[44:47], v[68:71]\n"
"buffer_load_dwordx4 a[92:95], v1, s[4:7], s98 offen offset:3072 sc1\n"
"v_exp_f32_e32 v75, v75\n"
"v_mfma_f32_16x16x32_bf16 v[80:83], v[84:87], v[40:43], v[64:67]\n"
"ds_read_b128 v[104:107], v252 offset:14592\n"
"v_exp_f32_e32 v120, v120\n"
"v_exp_f32_e32 v121, v121\n"
"v_mfma_f32_16x16x32_bf16 v[112:115], v[84:87], v[44:47], v[68:71]\n"
"v_exp_f32_e32 v122, v122\n"
"ds_read_b64_tr_b16 v[84:85], v16 offset:18432\n"
"v_exp_f32_e32 v123, v123\n"
"v_cvt_pk_bf16_f32 v152, v72, v73\n"
"s_waitcnt lgkmcnt(3)\n"
"v_mfma_f32_16x16x32_bf16 v[132:135], v[100:103], v[96:99], v[132:135]\n"
"s_add_u32 s98, s69, 0x606000\n"
"buffer_load_dwordx4 a[96:99], v0, s[4:7], s98 offen sc1\n"
"v_cvt_pk_bf16_f32 v153, v74, v75\n"
"v_cvt_pk_bf16_f32 v154, v120, v121\n"
"v_mfma_f32_16x16x32_bf16 v[136:139], v[124:127], v[96:99], v[136:139]\n"
"v_cvt_pk_bf16_f32 v155, v122, v123\n"
"ds_read_b64_tr_b16 v[86:87], v253 offset:18688\n"
"v_exp_f32_e32 v116, v116\n"
"v_exp_f32_e32 v117, v117\n"
"v_mfma_f32_16x16x32_bf16 v[140:143], v[24:27], v[96:99], v[140:143]\n"
"v_exp_f32_e32 v118, v118\n"
"v_exp_f32_e32 v119, v119\n"
"ds_read_b64_tr_b16 v[72:73], v253 offset:18432\n"
"v_mfma_f32_16x16x32_bf16 v[108:111], v[100:103], v[28:31], v[108:111]\n"
"buffer_load_dwordx4 a[100:103], v0, s[4:7], s98 offen offset:1024 sc1\n"
"v_exp_f32_e32 v128, v128\n"
"v_exp_f32_e32 v129, v129\n"
"v_mfma_f32_16x16x32_bf16 v[144:147], v[124:127], v[28:31], v[144:147]\n"
"v_exp_f32_e32 v130, v130\n"
"ds_read_b64_tr_b16 v[74:75], v16 offset:18688\n"
"v_exp_f32_e32 v131, v131\n"
"v_mfma_f32_16x16x32_bf16 v[148:151], v[24:27], v[28:31], v[148:151]\n"
"v_cvt_pk_bf16_f32 v24, v116, v117\n"
"v_cvt_pk_bf16_f32 v25, v118, v119\n"
"v_cvt_pk_bf16_f32 v26, v128, v129\n"
"v_cvt_pk_bf16_f32 v27, v130, v131\n"
"s_waitcnt lgkmcnt(4)\n"
"s_cmp_eq_i32 s78, 8\n"
"s_cbranch_scc1 .Lmk_x102s\n"
".Lmk_x102r:\n"
"v_mfma_f32_16x16x32_bf16 v[28:31], v[36:39], v[40:43], v[64:67]\n"
"ds_read_b128 v[96:99], v15 offset:12288\n"
"v_exp_f32_e32 v32, v32\n"
"v_exp_f32_e32 v33, v33\n"
"v_exp_f32_e32 v34, v34\n"
"v_mfma_f32_16x16x32_bf16 v[116:119], v[36:39], v[44:47], v[68:71]\n"
"buffer_load_dwordx4 a[104:107], v0, s[4:7], s98 offen offset:2048 sc1\n"
"v_exp_f32_e32 v35, v35\n"
"v_mfma_f32_16x16x32_bf16 v[36:39], v[104:107], v[40:43], v[64:67]\n"
"ds_read_b128 v[120:123], v252 offset:12544\n"
"v_exp_f32_e32 v80, v80\n"
"v_exp_f32_e32 v81, v81\n"
"v_mfma_f32_16x16x32_bf16 v[124:127], v[104:107], v[44:47], v[68:71]\n"
"v_exp_f32_e32 v82, v82\n"
"ds_read_b64_tr_b16 v[104:105], v16 offset:16384\n"
"v_exp_f32_e32 v83, v83\n"
"s_waitcnt lgkmcnt(3)\n"
"v_mfma_f32_16x16x32_bf16 v[132:135], v[100:103], v[152:155], v[132:135]\n"
"v_cvt_pk_bf16_f32 v128, v32, v33\n"
"buffer_load_dwordx4 a[108:111], v0, s[4:7], s98 offen offset:3072 sc1\n"
"v_cvt_pk_bf16_f32 v129, v34, v35\n"
"v_cvt_pk_bf16_f32 v130, v80, v81\n"
"v_mfma_f32_16x16x32_bf16 v[136:139], v[84:87], v[152:155], v[136:139]\n"
"v_cvt_pk_bf16_f32 v131, v82, v83\n"
"ds_read_b64_tr_b16 v[106:107], v253 offset:16640\n"
"v_exp_f32_e32 v76, v76\n"
"v_exp_f32_e32 v77, v77\n"
"v_mfma_f32_16x16x32_bf16 v[140:143], v[72:75], v[152:155], v[140:143]\n"
"v_exp_f32_e32 v78, v78\n"
"v_exp_f32_e32 v79, v79\n"
"ds_read_b64_tr_b16 v[32:33], v253 offset:16384\n"
"v_mfma_f32_16x16x32_bf16 v[108:111], v[100:103], v[24:27], v[108:111]\n"
"buffer_load_dwordx4 a[112:115], v1, s[4:7], s98 offen sc1\n"
"v_exp_f32_e32 v112, v112\n"
"v_exp_f32_e32 v113, v113\n"
"v_mfma_f32_16x16x32_bf16 v[144:147], v[84:87], v[24:27], v[144:147]\n"
"v_exp_f32_e32 v114, v114\n"
"ds_read_b64_tr_b16 v[34:35], v16 offset:16640\n"
"v_exp_f32_e32 v115, v115\n"
"v_mfma_f32_16x16x32_bf16 v[148:151], v[72:75], v[24:27], v[148:151]\n"
"v_cvt_pk_bf16_f32 v24, v76, v77\n"
"v_cvt_pk_bf16_f32 v25, v78, v79\n"
"v_cvt_pk_bf16_f32 v26, v112, v113\n"
"v_cvt_pk_bf16_f32 v27, v114, v115\n"
"s_waitcnt lgkmcnt(4)\n"
"s_cmp_eq_i32 s78, 7\n"
"s_cbranch_scc1 .Lmk_x103s\n"
".Lmk_x103r:\n"
"v_mfma_f32_16x16x32_bf16 v[72:75], v[96:99], v[40:43], v[64:67]\n"
"ds_read_b128 v[76:79], v15 offset:10240\n"
"v_exp_f32_e32 v28, v28\n"
"v_exp_f32_e32 v29, v29\n"
"v_exp_f32_e32 v30, v30\n"
"v_mfma_f32_16x16x32_bf16 v[80:83], v[96:99], v[44:47], v[68:71]\n"
"buffer_load_dwordx4 a[116:119], v1, s[4:7], s98 offen offset:1024 sc1\n"
"v_exp_f32_e32 v31, v31\n"
"v_mfma_f32_16x16x32_bf16 v[84:87], v[120:123], v[40:43], v[64:67]\n"
"ds_read_b128 v[96:99], v252 offset:10496\n"
"v_exp_f32_e32 v36, v36\n"
"v_exp_f32_e32 v37, v37\n"
"v_mfma_f32_16x16x32_bf16 v[112:115], v[120:123], v[44:47], v[68:71]\n"
"v_exp_f32_e32 v38, v38\n"
"ds_read_b64_tr_b16 v[120:121], v16 offset:14336\n"
"v_exp_f32_e32 v39, v39\n"
"s_waitcnt lgkmcnt(3)\n"
"v_mfma_f32_16x16x32_bf16 v[132:135], v[100:103], v[128:131], v[132:135]\n"
"v_cvt_pk_bf16_f32 v152, v28, v29\n"
"buffer_load_dwordx4 a[120:123], v1, s[4:7], s98 offen offset:2048 sc1\n"
"v_cvt_pk_bf16_f32 v153, v30, v31\n"
"v_cvt_pk_bf16_f32 v154, v36, v37\n"
"v_mfma_f32_16x16x32_bf16 v[136:139], v[104:107], v[128:131], v[136:139]\n"
"v_cvt_pk_bf16_f32 v155, v38, v39\n"
"ds_read_b64_tr_b16 v[122:123], v253 offset:14592\n"
"v_exp_f32_e32 v116, v116\n"
"v_exp_f32_e32 v117, v117\n"
"v_mfma_f32_16x16x32_bf16 v[140:143], v[32:35], v[128:131], v[140:143]\n"
"v_exp_f32_e32 v118, v118\n"
"v_exp_f32_e32 v119, v119\n"
"ds_read_b64_tr_b16 v[36:37], v253 offset:14336\n"
"v_mfma_f32_16x16x32_bf16 v[108:111], v[100:103], v[24:27], v[108:111]\n"
"buffer_load_dwordx4 a[124:127], v1, s[4:7], s98 offen offset:3072 sc1\n"
"v_exp_f32_e32 v124, v124\n"
"v_exp_f32_e32 v125, v125\n"
"v_mfma_f32_16x16x32_bf16 v[144:147], v[104:107], v[24:27], v[144:147]\n"
"v_exp_f32_e32 v126, v126\n"
"ds_read_b64_tr_b16 v[38:39], v16 offset:14592\n"
"v_exp_f32_e32 v127, v127\n"
"v_mfma_f32_16x16x32_bf16 v[148:151], v[32:35], v[24:27], v[148:151]\n"
"v_cvt_pk_bf16_f32 v104, v116, v117\n"
"v_cvt_pk_bf16_f32 v105, v118, v119\n"
"v_cvt_pk_bf16_f32 v106, v124, v125\n"
"v_cvt_pk_bf16_f32 v107, v126, v127\n"
"s_waitcnt lgkmcnt(4)\n"
"s_cmp_eq_i32 s78, 6\n"
"s_cbranch_scc1 .Lmk_x104s\n"
".Lmk_x104r:\n"
"v_mfma_f32_16x16x32_bf16 v[116:119], v[76:79], v[40:43], v[64:67]\n"
"ds_read_b128 v[124:127], v15 offset:8192\n"
"v_exp_f32_e32 v72, v72\n"
"v_exp_f32_e32 v73, v73\n"
"v_exp_f32_e32 v74, v74\n"
"v_mfma_f32_16x16x32_bf16 v[128:131], v[76:79], v[44:47], v[68:71]\n"
"s_add_u32 s98, s69, 0x608000\n"
"buffer_load_dwordx4 v[24:27], v0, s[4:7], s98 offen sc1\n"
"v_exp_f32_e32 v75, v75\n"
"v_mfma_f32_16x16x32_bf16 v[76:79], v[96:99], v[40:43], v[64:67]\n"
"ds_read_b128 v[156:159], v252 offset:8448\n"
"v_exp_f32_e32 v84, v84\n"
"v_exp_f32_e32 v85, v85\n"
"v_mfma_f32_16x16x32_bf16 v[160:163], v[96:99], v[44:47], v[68:71]\n"
"v_exp_f32_e32 v86, v86\n"
"ds_read_b64_tr_b16 v[96:97], v16 offset:12288\n"
"v_exp_f32_e32 v87, v87\n"
"s_waitcnt lgkmcnt(3)\n"
"v_mfma_f32_16x16x32_bf16 v[132:135], v[100:103], v[152:155], v[132:135]\n"
"v_cvt_pk_bf16_f32 v164, v72, v73\n"
"buffer_load_dwordx4 v[28:31], v0, s[4:7], s98 offen offset:1024 sc1\n"
"v_cvt_pk_bf16_f32 v165, v74, v75\n"
"v_cvt_pk_bf16_f32 v166, v84, v85\n"
"v_mfma_f32_16x16x32_bf16 v[136:139], v[120:123], v[152:155], v[136:139]\n"
"v_cvt_pk_bf16_f32 v167, v86, v87\n"
"ds_read_b64_tr_b16 v[98:99], v253 offset:12544\n"
"v_exp_f32_e32 v80, v80\n"
"v_exp_f32_e32 v81, v81\n"
"v_mfma_f32_16x16x32_bf16 v[140:143], v[36:39], v[152:155], v[140:143]\n"
"v_exp_f32_e32 v82, v82\n"
"v_exp_f32_e32 v83, v83\n"
"ds_read_b64_tr_b16 v[72:73], v253 offset:12288\n"
"v_mfma_f32_16x16x32_bf16 v[108:111], v[100:103], v[104:107], v[108:111]\n"
"buffer_load_dwordx4 v[32:35], v0, s[4:7], s98 offen offset:2048 sc1\n"
"v_exp_f32_e32 v112, v112\n"
"v_exp_f32_e32 v113, v113\n"
"v_mfma_f32_16x16x32_bf16 v[144:147], v[120:123], v[104:107], v[144:147]\n"
"v_exp_f32_e32 v114, v114\n"
"ds_read_b64_tr_b16 v[74:75], v16 offset:12544\n"
"v_exp_f32_e32 v115, v115\n"
"v_mfma_f32_16x16x32_bf16 v[148:151], v[36:39], v[104:107], v[148:151]\n"
"v_cvt_pk_bf16_f32 v84, v80, v81\n"
"v_cvt_pk_bf16_f32 v85, v82, v83\n"
"v_cvt_pk_bf16_f32 v86, v112, v113\n"
"v_cvt_pk_bf16_f32 v87, v114, v115\n"
"s_waitcnt lgkmcnt(4)\n"
"s_cmp_eq_i32 s78, 5\n"
"s_cbranch_scc1 .Lmk_x105s\n"
".Lmk_x105r:\n"
"v_mfma_f32_16x16x32_bf16 v[80:83], v[124:127], v[40:43], v[64:67]\n"
"ds_read_b64_tr_b16 v[104:105], v16 offset:10240\n"
"v_exp_f32_e32 v116, v116\n"
"v_exp_f32_e32 v117, v117\n"
"ds_read_b64_tr_b16 v[106:107], v253 offset:10496\n"
"v_mfma_f32_16x16x32_bf16 v[112:115], v[124:127], v[44:47], v[68:71]\n"
"s_add_u32 s97, s71, 0xfffff000\n"
"buffer_load_dwordx2 v[246:247], off, s[4:7], s97 sc1\n"
"v_exp_f32_e32 v118, v118\n"
"v_exp_f32_e32 v119, v119\n"
"v_mfma_f32_16x16x32_bf16 v[120:123], v[156:159], v[40:43], v[64:67]\n"
"ds_read_b64_tr_b16 v[64:65], v253 offset:10240\n"
"v_exp_f32_e32 v76, v76\n"
"v_exp_f32_e32 v77, v77\n"
"v_mfma_f32_16x16x32_bf16 v[124:127], v[156:159], v[44:47], v[68:71]\n"
"ds_read_b64_tr_b16 v[66:67], v16 offset:10496\n"
"v_exp_f32_e32 v78, v78\n"
"v_exp_f32_e32 v79, v79\n"
"s_waitcnt lgkmcnt(4)\n"
"v_mfma_f32_16x16x32_bf16 v[132:135], v[100:103], v[164:167], v[132:135]\n"
"ds_read_b64_tr_b16 v[68:69], v16 offset:24576\n"
"v_cvt_pk_bf16_f32 v152, v116, v117\n"
"v_cvt_pk_bf16_f32 v153, v118, v119\n"
"v_cvt_pk_bf16_f32 v154, v76, v77\n"
"ds_read_b64_tr_b16 v[70:71], v253 offset:24832\n"
"v_mfma_f32_16x16x32_bf16 v[136:139], v[96:99], v[164:167], v[136:139]\n"
"v_cvt_pk_bf16_f32 v155, v78, v79\n"
"v_exp_f32_e32 v128, v128\n"
"ds_read_b64_tr_b16 v[76:77], v253 offset:24576\n"
"buffer_load_dwordx4 v[36:39], v0, s[4:7], s98 offen offset:3072 sc1\n"
"v_mfma_f32_16x16x32_bf16 v[140:143], v[72:75], v[164:167], v[140:143]\n"
"v_exp_f32_e32 v129, v129\n"
"v_exp_f32_e32 v130, v130\n"
"ds_read_b64_tr_b16 v[78:79], v16 offset:24832\n"
"v_exp_f32_e32 v131, v131\n"
"v_mfma_f32_16x16x32_bf16 v[108:111], v[100:103], v[84:87], v[108:111]\n"
"v_exp_f32_e32 v160, v160\n"
"v_add_u32_e32 v248, 0x26400, v0\n"
"v_exp_f32_e32 v161, v161\n"
"v_mfma_f32_16x16x32_bf16 v[144:147], v[96:99], v[84:87], v[144:147]\n"
"v_exp_f32_e32 v162, v162\n"
"ds_read_b128 v[96:99], v248\n"
"v_exp_f32_e32 v163, v163\n"
"v_mfma_f32_16x16x32_bf16 v[148:151], v[72:75], v[84:87], v[148:151]\n"
"v_cvt_pk_bf16_f32 v72, v128, v129\n"
"ds_read_b128 v[84:87], v248 offset:1024\n"
"v_cvt_pk_bf16_f32 v73, v130, v131\n"
"v_cvt_pk_bf16_f32 v74, v160, v161\n"
"v_cvt_pk_bf16_f32 v75, v162, v163\n"
"s_waitcnt lgkmcnt(6)\n"
"v_mfma_f32_16x16x32_bf16 v[132:135], v[100:103], v[152:155], v[132:135]\n"
"ds_read_b64_tr_b16 v[116:117], v16 offset:8192\n"
"v_cndmask_b32_e64 v80, v48, v80, s[20:21]\n"
"v_cndmask_b32_e64 v81, v49, v81, s[22:23]\n"
"v_cndmask_b32_e64 v82, v50, v82, s[24:25]\n"
"v_cndmask_b32_e64 v83, v51, v83, s[26:27]\n"
"v_cndmask_b32_e64 v120, v56, v120, s[28:29]\n"
"v_cndmask_b32_e64 v121, v57, v121, s[30:31]\n"
"v_cndmask_b32_e64 v122, v58, v122, s[32:33]\n"
"v_cndmask_b32_e64 v123, v59, v123, s[34:35]\n"
"v_exp_f32_e32 v80, v80\n"
"v_mfma_f32_16x16x32_bf16 v[136:139], v[104:107], v[152:155], v[136:139]\n"
"v_exp_f32_e32 v81, v81\n"
"v_exp_f32_e32 v82, v82\n"
"ds_read_b64_tr_b16 v[118:119], v253 offset:8448\n"
"v_exp_f32_e32 v83, v83\n"
"v_exp_f32_e32 v120, v120\n"
"v_mfma_f32_16x16x32_bf16 v[140:143], v[64:67], v[152:155], v[140:143]\n"
"v_exp_f32_e32 v121, v121\n"
"v_exp_f32_e32 v122, v122\n"
"v_exp_f32_e32 v123, v123\n"
"v_cvt_pk_bf16_f32 v48, v80, v81\n"
"v_cvt_pk_bf16_f32 v49, v82, v83\n"
"v_cvt_pk_bf16_f32 v50, v120, v121\n"
"v_cvt_pk_bf16_f32 v51, v122, v123\n"
"v_mfma_f32_16x16x32_bf16 v[108:111], v[100:103], v[72:75], v[108:111]\n"
"ds_read_b64_tr_b16 v[56:57], v253 offset:8192\n"
"v_cndmask_b32_e64 v112, v52, v112, s[36:37]\n"
"v_cndmask_b32_e64 v113, v53, v113, s[38:39]\n"
"v_cndmask_b32_e64 v114, v54, v114, s[40:41]\n"
"v_cndmask_b32_e64 v115, v55, v115, s[42:43]\n"
"v_cndmask_b32_e64 v124, v60, v124, s[44:45]\n"
"v_cndmask_b32_e64 v125, v61, v125, s[46:47]\n"
"v_cndmask_b32_e64 v126, v62, v126, s[48:49]\n"
"v_cndmask_b32_e64 v127, v63, v127, s[50:51]\n"
"v_exp_f32_e32 v112, v112\n"
"v_mfma_f32_16x16x32_bf16 v[144:147], v[104:107], v[72:75], v[144:147]\n"
"v_exp_f32_e32 v113, v113\n"
"v_exp_f32_e32 v114, v114\n"
"ds_read_b64_tr_b16 v[58:59], v16 offset:8448\n"
"v_exp_f32_e32 v115, v115\n"
"v_exp_f32_e32 v124, v124\n"
"v_mfma_f32_16x16x32_bf16 v[148:151], v[64:67], v[72:75], v[148:151]\n"
"v_exp_f32_e32 v125, v125\n"
"v_exp_f32_e32 v126, v126\n"
"v_exp_f32_e32 v127, v127\n"
"v_cvt_pk_bf16_f32 v52, v112, v113\n"
"v_cvt_pk_bf16_f32 v53, v114, v115\n"
"v_cvt_pk_bf16_f32 v54, v124, v125\n"
"v_cvt_pk_bf16_f32 v55, v126, v127\n"
"s_waitcnt lgkmcnt(0)\n"
"v_mfma_f32_16x16x32_bf16 v[132:135], v[100:103], v[48:51], v[132:135]\n"
"v_and_b32_e32 v96, v48, v96\n"
"v_and_b32_e32 v97, v49, v97\n"
"v_and_b32_e32 v98, v50, v98\n"
"v_and_b32_e32 v99, v51, v99\n"
"v_mfma_f32_16x16x32_bf16 v[108:111], v[100:103], v[52:55], v[108:111]\n"
"v_xor_b32_e32 v60, v48, v96\n"
"v_xor_b32_e32 v61, v49, v97\n"
"v_xor_b32_e32 v62, v50, v98\n"
"v_xor_b32_e32 v63, v51, v99\n"
"v_mfma_f32_16x16x32_bf16 v[136:139], v[116:119], v[96:99], v[136:139]\n"
"v_and_b32_e32 v84, v52, v84\n"
"v_and_b32_e32 v85, v53, v85\n"
"v_mfma_f32_16x16x32_bf16 v[140:143], v[56:59], v[96:99], v[140:143]\n"
"v_and_b32_e32 v86, v54, v86\n"
"v_and_b32_e32 v87, v55, v87\n"
"v_mfma_f32_16x16x32_bf16 v[136:139], v[68:71], v[60:63], v[136:139]\n"
"v_xor_b32_e32 v48, v52, v84\n"
"v_xor_b32_e32 v49, v53, v85\n"
"v_mfma_f32_16x16x32_bf16 v[140:143], v[76:79], v[60:63], v[140:143]\n"
"v_xor_b32_e32 v50, v54, v86\n"
"v_xor_b32_e32 v51, v55, v87\n"
"v_mfma_f32_16x16x32_bf16 v[144:147], v[116:119], v[84:87], v[144:147]\n"
"s_waitcnt vmcnt(1)\n"
"v_cmp_ne_u64_e32 vcc, v[246:247], v[18:19]\n"
"s_nop 0\n"
"s_cbranch_vccnz .Lmk_f98s\n"
".Lmk_f98r:\n"
"v_mfma_f32_16x16x32_bf16 v[148:151], v[56:59], v[84:87], v[148:151]\n"
"s_add_u32 m0, s77, 0x4000\n"
"s_add_u32 s97, s65, 0\n"
"buffer_load_dwordx4 v2, s[4:7], s97 offen sc1 lds\n"
"buffer_load_dwordx4 v2, s[4:7], s97 offen offset:1024 sc1 lds\n"
"v_mfma_f32_16x16x32_bf16 v[144:147], v[68:71], v[48:51], v[144:147]\n"
"s_add_u32 m0, s77, 0xc000\n"
"s_add_u32 s97, s65, 0x4000\n"
"buffer_load_dwordx4 v2, s[4:7], s97 offen sc1 lds\n"
"buffer_load_dwordx4 v2, s[4:7], s97 offen offset:1024 sc1 lds\n"
"v_mfma_f32_16x16x32_bf16 v[148:151], v[76:79], v[48:51], v[148:151]\n"
"v_max_f32_e32 v248, v132, v108\n"
"v_rcp_f32_e32 v132, v132\n"
"v_rcp_f32_e32 v108, v108\n"
"s_nop 0\n"
"v_max3_f32 v248, v248, v132, v108\n"
"v_cmp_nlt_f32_e32 vcc, v248, v251\n"
"s_cbranch_vccnz .Lmk_b100\n"
".Lmk_k106:\n"
"v_mul_f32_e32 v136, v136, v132\n"
"v_mul_f32_e32 v137, v137, v132\n"
"v_mul_f32_e32 v138, v138, v132\n"
"v_mul_f32_e32 v139, v139, v132\n"
"v_mul_f32_e32 v140, v140, v132\n"
"v_mul_f32_e32 v141, v141, v132\n"
"v_mul_f32_e32 v142, v142, v132\n"
"v_mul_f32_e32 v143, v143, v132\n"
"v_cvt_pk_bf16_f32 v40, v136, v137\n"
"v_cvt_pk_bf16_f32 v41, v138, v139\n"
"v_cvt_pk_bf16_f32 v42, v140, v141\n"
"v_cvt_pk_bf16_f32 v43, v142, v143\n"
"s_add_u32 s99, s60, 0x40000\n"
"buffer_store_dwordx4 v[40:43], v0, s[4:7], s99 offen sc1\n"
"v_mul_f32_e32 v144, v144, v108\n"
"v_mul_f32_e32 v145, v145, v108\n"
"v_mul_f32_e32 v146, v146, v108\n"
"v_mul_f32_e32 v147, v147, v108\n"
"v_mul_f32_e32 v148, v148, v108\n"
"v_mul_f32_e32 v149, v149, v108\n"
"v_mul_f32_e32 v150, v150, v108\n"
"v_mul_f32_e32 v151, v151, v108\n"
"v_cvt_pk_bf16_f32 v40, v144, v145\n"
"v_cvt_pk_bf16_f32 v41, v146, v147\n"
"v_cvt_pk_bf16_f32 v42, v148, v149\n"
"v_cvt_pk_bf16_f32 v43, v150, v151\n"
"s_add_u32 s99, s60, 0x48000\n"
"buffer_store_dwordx4 v[40:43], v0, s[4:7], s99 offen sc1\n"
"; @A AT a\n"
"s_mov_b32 s2, 0\n"
"v_mov_b32_e32 v255, 0xff800000\n"
"v_mov_b32_e32 v254, 0xff800000\n"
"v_mov_b32_e32 v250, 0x7f800000\n"
"v_mov_b32_e32 v249, 0x7f800000\n"
".Lmk_a107:\n"
"ds_read_b128 v[48:51], v15 offset:16384\n"
"ds_read_b128 v[52:55], v252 offset:16640\n"
"ds_read_b128 v[56:59], v15 offset:14336\n"
"ds_read_b128 v[60:63], v252 offset:14592\n"
"s_cmp_gt_i32 s78, 7\n"
"s_cselect_b32 s99, 0x80000000, 0\n"
"s_or_b32 s99, s99, 0x7f800000\n"
"s_add_u32 s97, s67, 32\n"
"buffer_load_dwordx2 v[246:247], v12, s[4:7], s97 offen sc1\n"
"buffer_load_dwordx4 v[40:43], v1, s[4:7], s98 offen sc1\n"
"s_waitcnt lgkmcnt(2)\n"
"v_mfma_f32_16x16x32_bf16 v[96:99], v[48:51], v[88:91], 0\n"
"buffer_load_dwordx4 v[44:47], v1, s[4:7], s98 offen offset:1024 sc1\n"
"v_mfma_f32_16x16x32_bf16 v[104:107], v[48:51], v[92:95], 0\n"
"v_mfma_f32_16x16x32_bf16 v[108:111], v[52:55], v[88:91], 0\n"
"v_mfma_f32_16x16x32_bf16 v[112:115], v[52:55], v[92:95], 0\n"
"s_nop 3\n"
"v_max_f32_e32 v248, v96, v97\n"
"s_nop 0\n"
"v_max_f32_e32 v245, v104, v105\n"
"v_max3_f32 v248, v248, v98, v99\n"
"v_max3_f32 v245, v245, v106, v107\n"
"v_max3_f32 v248, v248, v108, v109\n"
"v_max3_f32 v245, v245, v112, v113\n"
"v_max3_f32 v248, v248, v110, v111\n"
"v_max3_f32 v245, v245, v114, v115\n"
"v_mov_b32_e32 v244, v248\n"
"v_mov_b32_e32 v243, v245\n"
"s_nop 0\n"
"v_permlane16_swap_b32_e32 v248, v244\n"
"v_permlane16_swap_b32_e32 v245, v243\n"
"v_max_f32_e32 v248, v248, v244\n"
"v_max_f32_e32 v245, v245, v243\n"
"v_mov_b32_e32 v244, v248\n"
"v_mov_b32_e32 v243, v245\n"
"s_nop 0\n"
"v_permlane32_swap_b32_e32 v248, v244\n"
"v_permlane32_swap_b32_e32 v245, v243\n"
"v_max_f32_e32 v248, v248, v244\n"
"v_max_f32_e32 v245, v245, v243\n"
"v_med3_f32 v248, v248, v255, v250\n"
"v_med3_f32 v245, v245, v254, v249\n"
"v_min_f32_e64 v116, s99, -v248\n"
"v_min_f32_e64 v117, s99, -v248\n"
"v_min_f32_e64 v118, s99, -v248\n"
"v_min_f32_e64 v119, s99, -v248\n"
"v_min_f32_e64 v120, s99, -v245\n"
"v_min_f32_e64 v121, s99, -v245\n"
"v_min_f32_e64 v122, s99, -v245\n"
"v_min_f32_e64 v123, s99, -v245\n"
"s_waitcnt lgkmcnt(0)\n"
"v_mfma_f32_16x16x32_bf16 v[64:67], v[56:59], v[88:91], v[116:119]\n"
"ds_read_b128 v[68:71], v15 offset:12288\n"
"v_sub_f32_e32 v96, v96, v248\n"
"v_sub_f32_e32 v97, v97, v248\n"
"v_sub_f32_e32 v98, v98, v248\n"
"buffer_load_dwordx4 v[48:51], v1, s[4:7], s98 offen offset:2048 sc1\n"
"v_mfma_f32_16x16x32_bf16 v[72:75], v[56:59], v[92:95], v[120:123]\n"
"v_sub_f32_e32 v99, v99, v248\n"
"v_sub_f32_e32 v108, v108, v248\n"
"v_sub_f32_e32 v109, v109, v248\n"
"v_sub_f32_e32 v110, v110, v248\n"
"v_sub_f32_e32 v111, v111, v248\n"
"v_mfma_f32_16x16x32_bf16 v[76:79], v[60:63], v[88:91], v[116:119]\n"
"ds_read_b128 v[80:83], v252 offset:12544\n"
"v_sub_f32_e32 v104, v104, v245\n"
"buffer_load_dwordx4 v[52:55], v1, s[4:7], s98 offen offset:3072 sc1\n"
"v_sub_f32_e32 v105, v105, v245\n"
"v_sub_f32_e32 v106, v106, v245\n"
"v_mfma_f32_16x16x32_bf16 v[84:87], v[60:63], v[92:95], v[120:123]\n"
"v_sub_f32_e32 v107, v107, v245\n"
"v_sub_f32_e32 v112, v112, v245\n"
"v_sub_f32_e32 v113, v113, v245\n"
"v_sub_f32_e32 v114, v114, v245\n"
"v_sub_f32_e32 v115, v115, v245\n"
"s_waitcnt lgkmcnt(0)\n"
"s_cmp_eq_i32 s78, 7\n"
"s_cbranch_scc1 .Lmk_x109s\n"
".Lmk_x109r:\n"
"v_mfma_f32_16x16x32_bf16 v[124:127], v[68:71], v[88:91], v[116:119]\n"
"ds_read_b128 v[128:131], v15 offset:10240\n"
"v_exp_f32_e32 v64, v64\n"
"v_exp_f32_e32 v65, v65\n"
"v_exp_f32_e32 v66, v66\n"
"s_add_u32 s99, s69, 0x60a000\n"
"buffer_load_dwordx4 v[56:59], v0, s[4:7], s99 offen sc1\n"
"v_exp_f32_e32 v67, v67\n"
"ds_read_b128 v[132:135], v252 offset:10496\n"
"v_mfma_f32_16x16x32_bf16 v[136:139], v[68:71], v[92:95], v[120:123]\n"
"v_exp_f32_e32 v76, v76\n"
"v_exp_f32_e32 v77, v77\n"
"v_exp_f32_e32 v78, v78\n"
"ds_read_b64_tr_b16 v[140:141], v16 offset:14336\n"
"v_exp_f32_e32 v79, v79\n"
"v_cvt_pk_bf16_f32 v68, v64, v65\n"
"v_cvt_pk_bf16_f32 v69, v66, v67\n"
"v_cvt_pk_bf16_f32 v70, v76, v77\n"
"v_cvt_pk_bf16_f32 v71, v78, v79\n"
"v_mfma_f32_16x16x32_bf16 v[76:79], v[80:83], v[88:91], v[116:119]\n"
"ds_read_b64_tr_b16 v[142:143], v253 offset:14592\n"
"v_exp_f32_e32 v72, v72\n"
"v_exp_f32_e32 v73, v73\n"
"buffer_load_dwordx4 v[60:63], v0, s[4:7], s99 offen offset:1024 sc1\n"
"v_exp_f32_e32 v74, v74\n"
"v_exp_f32_e32 v75, v75\n"
"ds_read_b64_tr_b16 v[144:145], v253 offset:14336\n"
"v_exp_f32_e32 v84, v84\n"
"v_mfma_f32_16x16x32_bf16 v[148:151], v[80:83], v[92:95], v[120:123]\n"
"v_exp_f32_e32 v85, v85\n"
"v_exp_f32_e32 v86, v86\n"
"ds_read_b64_tr_b16 v[146:147], v16 offset:14592\n"
"v_exp_f32_e32 v87, v87\n"
"v_cvt_pk_bf16_f32 v80, v72, v73\n"
"v_cvt_pk_bf16_f32 v81, v74, v75\n"
"v_cvt_pk_bf16_f32 v82, v84, v85\n"
"v_cvt_pk_bf16_f32 v83, v86, v87\n"
"s_waitcnt lgkmcnt(4)\n"
"s_cmp_eq_i32 s78, 6\n"
"s_cbranch_scc1 .Lmk_x110s\n"
".Lmk_x110r:\n"
"v_mfma_f32_16x16x32_bf16 v[84:87], v[128:131], v[88:91], v[116:119]\n"
"ds_read_b128 v[72:75], v15 offset:8192\n"
"v_exp_f32_e32 v124, v124\n"
"v_exp_f32_e32 v125, v125\n"
"v_mfma_f32_16x16x32_bf16 v[152:155], v[128:131], v[92:95], v[120:123]\n"
"v_exp_f32_e32 v126, v126\n"
"buffer_load_dwordx4 v[64:67], v0, s[4:7], s99 offen offset:2048 sc1\n"
"v_exp_f32_e32 v127, v127\n"
"v_mfma_f32_16x16x32_bf16 v[128:131], v[132:135], v[88:91], v[116:119]\n"
"ds_read_b128 v[156:159], v252 offset:8448\n"
"v_exp_f32_e32 v76, v76\n"
"v_exp_f32_e32 v77, v77\n"
"v_mfma_f32_16x16x32_bf16 v[160:163], v[132:135], v[92:95], v[120:123]\n"
"v_exp_f32_e32 v78, v78\n"
"ds_read_b64_tr_b16 v[132:133], v16 offset:12288\n"
"v_exp_f32_e32 v79, v79\n"
"s_waitcnt lgkmcnt(3)\n"
"v_mfma_f32_16x16x32_bf16 v[164:167], v[100:103], v[68:71], 0\n"
"v_cvt_pk_bf16_f32 v168, v124, v125\n"
"v_cvt_pk_bf16_f32 v169, v126, v127\n"
"v_cvt_pk_bf16_f32 v170, v76, v77\n"
"v_cvt_pk_bf16_f32 v171, v78, v79\n"
"v_mfma_f32_16x16x32_bf16 v[124:127], v[140:143], v[68:71], 0\n"
"ds_read_b64_tr_b16 v[134:135], v253 offset:12544\n"
"v_exp_f32_e32 v136, v136\n"
"v_exp_f32_e32 v137, v137\n"
"v_mfma_f32_16x16x32_bf16 v[172:175], v[144:147], v[68:71], 0\n"
"buffer_load_dwordx4 v[68:71], v0, s[4:7], s99 offen offset:3072 sc1\n"
"v_exp_f32_e32 v138, v138\n"
"v_exp_f32_e32 v139, v139\n"
"v_mfma_f32_16x16x32_bf16 v[176:179], v[100:103], v[80:83], 0\n"
"ds_read_b64_tr_b16 v[180:181], v253 offset:12288\n"
"v_exp_f32_e32 v148, v148\n"
"v_exp_f32_e32 v149, v149\n"
"v_mfma_f32_16x16x32_bf16 v[184:187], v[140:143], v[80:83], 0\n"
"v_exp_f32_e32 v150, v150\n"
"ds_read_b64_tr_b16 v[182:183], v16 offset:12544\n"
"v_exp_f32_e32 v151, v151\n"
"v_mfma_f32_16x16x32_bf16 v[140:143], v[144:147], v[80:83], 0\n"
"v_cvt_pk_bf16_f32 v80, v136, v137\n"
"v_cvt_pk_bf16_f32 v81, v138, v139\n"
"v_cvt_pk_bf16_f32 v82, v148, v149\n"
"v_cvt_pk_bf16_f32 v83, v150, v151\n"
"s_waitcnt vmcnt(11)\n"
"s_cmp_lg_u32 s2, 0\n"
"s_cbranch_scc1 .Lmk_s111\n"
"s_barrier\n"
".Lmk_s111:\n"
"s_waitcnt lgkmcnt(4)\n"
"s_cmp_eq_i32 s78, 5\n"
"s_cbranch_scc1 .Lmk_x112s\n"
".Lmk_x112r:\n"
"v_mfma_f32_16x16x32_bf16 v[136:139], v[72:75], v[88:91], v[116:119]\n"
"ds_read_b128 v[144:147], v15 offset:6144\n"
"v_exp_f32_e32 v84, v84\n"
"v_exp_f32_e32 v85, v85\n"
"v_mfma_f32_16x16x32_bf16 v[148:151], v[72:75], v[92:95], v[120:123]\n"
"v_exp_f32_e32 v86, v86\n"
"buffer_load_dwordx4 v[72:75], v1, s[4:7], s99 offen sc1\n"
"v_exp_f32_e32 v87, v87\n"
"v_mfma_f32_16x16x32_bf16 v[188:191], v[156:159], v[88:91], v[116:119]\n"
"ds_read_b128 v[192:195], v252 offset:6400\n"
"v_exp_f32_e32 v128, v128\n"
"v_exp_f32_e32 v129, v129\n"
"v_mfma_f32_16x16x32_bf16 v[196:199], v[156:159], v[92:95], v[120:123]\n"
"v_exp_f32_e32 v130, v130\n"
"ds_read_b64_tr_b16 v[156:157], v16 offset:10240\n"
"v_exp_f32_e32 v131, v131\n"
"s_waitcnt lgkmcnt(3)\n"
"v_mfma_f32_16x16x32_bf16 v[164:167], v[100:103], v[168:171], v[164:167]\n"
"v_cvt_pk_bf16_f32 v200, v84, v85\n"
"v_cvt_pk_bf16_f32 v201, v86, v87\n"
"v_cvt_pk_bf16_f32 v202, v128, v129\n"
"v_cvt_pk_bf16_f32 v203, v130, v131\n"
"v_mfma_f32_16x16x32_bf16 v[124:127], v[132:135], v[168:171], v[124:127]\n"
"ds_read_b64_tr_b16 v[158:159], v253 offset:10496\n"
"v_exp_f32_e32 v152, v152\n"
"v_exp_f32_e32 v153, v153\n"
"v_mfma_f32_16x16x32_bf16 v[172:175], v[180:183], v[168:171], v[172:175]\n"
"buffer_load_dwordx4 v[76:79], v1, s[4:7], s99 offen offset:1024 sc1\n"
"v_exp_f32_e32 v154, v154\n"
"v_exp_f32_e32 v155, v155\n"
"v_mfma_f32_16x16x32_bf16 v[176:179], v[100:103], v[80:83], v[176:179]\n"
"ds_read_b64_tr_b16 v[128:129], v253 offset:10240\n"
"v_exp_f32_e32 v160, v160\n"
"v_exp_f32_e32 v161, v161\n"
"v_mfma_f32_16x16x32_bf16 v[184:187], v[132:135], v[80:83], v[184:187]\n"
"v_exp_f32_e32 v162, v162\n"
"ds_read_b64_tr_b16 v[130:131], v16 offset:10496\n"
"v_exp_f32_e32 v163, v163\n"
"v_mfma_f32_16x16x32_bf16 v[140:143], v[180:183], v[80:83], v[140:143]\n"
"v_cvt_pk_bf16_f32 v132, v152, v153\n"
"v_cvt_pk_bf16_f32 v133, v154, v155\n"
"v_cvt_pk_bf16_f32 v134, v160, v161\n"
"v_cvt_pk_bf16_f32 v135, v162, v163\n"
"s_waitcnt lgkmcnt(4)\n"
"v_mfma_f32_16x16x32_bf16 v[152:155], v[144:147], v[88:91], v[116:119]\n"
"ds_read_b128 v[160:163], v15 offset:4096\n"
"v_exp_f32_e32 v136, v136\n"
"v_exp_f32_e32 v137, v137\n"
"v_exp_f32_e32 v138, v138\n"
"v_mfma_f32_16x16x32_bf16 v[168:171], v[144:147], v[92:95], v[120:123]\n"
"buffer_load_dwordx4 v[80:83], v1, s[4:7], s99 offen offset:2048 sc1\n"
"ds_read_b128 v[144:147], v252 offset:4352\n"
"v_exp_f32_e32 v139, v139\n"
"v_mfma_f32_16x16x32_bf16 v[180:183], v[192:195], v[88:91], v[116:119]\n"
"v_exp_f32_e32 v188, v188\n"
"ds_read_b64_tr_b16 v[204:205], v16 offset:8192\n"
"v_exp_f32_e32 v189, v189\n"
"v_mfma_f32_16x16x32_bf16 v[208:211], v[192:195], v[92:95], v[120:123]\n"
"v_exp_f32_e32 v190, v190\n"
"v_exp_f32_e32 v191, v191\n"
"ds_read_b64_tr_b16 v[206:207], v253 offset:8448\n"
"s_waitcnt lgkmcnt(4)\n"
"v_mfma_f32_16x16x32_bf16 v[164:167], v[100:103], v[200:203], v[164:167]\n"
"v_cvt_pk_bf16_f32 v192, v136, v137\n"
"v_cvt_pk_bf16_f32 v193, v138, v139\n"
"v_cvt_pk_bf16_f32 v194, v188, v189\n"
"v_cvt_pk_bf16_f32 v195, v190, v191\n"
"ds_read_b64_tr_b16 v[136:137], v253 offset:8192\n"
"v_mfma_f32_16x16x32_bf16 v[124:127], v[156:159], v[200:203], v[124:127]\n"
"v_exp_f32_e32 v148, v148\n"
"v_exp_f32_e32 v149, v149\n"
"buffer_load_dwordx4 v[84:87], v1, s[4:7], s99 offen offset:3072 sc1\n"
"v_mfma_f32_16x16x32_bf16 v[172:175], v[128:131], v[200:203], v[172:175]\n"
"v_exp_f32_e32 v150, v150\n"
"ds_read_b64_tr_b16 v[138:139], v16 offset:8448\n"
"v_exp_f32_e32 v151, v151\n"
"v_mfma_f32_16x16x32_bf16 v[176:179], v[100:103], v[132:135], v[176:179]\n"
"v_exp_f32_e32 v196, v196\n"
"s_waitcnt vmcnt(12)\n"
"v_cmp_ne_u64_e32 vcc, v[246:247], v[18:19]\n"
"v_exp_f32_e32 v197, v197\n"
"v_mfma_f32_16x16x32_bf16 v[184:187], v[156:159], v[132:135], v[184:187]\n"
"v_exp_f32_e32 v198, v198\n"
"v_exp_f32_e32 v199, v199\n"
"v_mfma_f32_16x16x32_bf16 v[140:143], v[128:131], v[132:135], v[140:143]\n"
"s_cbranch_vccnz .Lmk_f83s\n"
".Lmk_f83r:\n"
"v_cvt_pk_bf16_f32 v128, v148, v149\n"
"v_cvt_pk_bf16_f32 v129, v150, v151\n"
"v_cvt_pk_bf16_f32 v130, v196, v197\n"
"v_cvt_pk_bf16_f32 v131, v198, v199\n"
"s_waitcnt lgkmcnt(4)\n"
"v_mfma_f32_16x16x32_bf16 v[132:135], v[160:163], v[88:91], v[116:119]\n"
"ds_read_b128 v[148:151], v15 offset:2048\n"
"v_exp_f32_e32 v152, v152\n"
"v_exp_f32_e32 v153, v153\n"
"v_mfma_f32_16x16x32_bf16 v[156:159], v[160:163], v[92:95], v[120:123]\n"
"v_exp_f32_e32 v154, v154\n"
"v_exp_f32_e32 v155, v155\n"
"ds_read_b128 v[160:163], v252 offset:2304\n"
"v_mfma_f32_16x16x32_bf16 v[188:191], v[144:147], v[88:91], v[116:119]\n"
"v_exp_f32_e32 v180, v180\n"
"v_exp_f32_e32 v181, v181\n"
"v_mfma_f32_16x16x32_bf16 v[196:199], v[144:147], v[92:95], v[120:123]\n"
"v_exp_f32_e32 v182, v182\n"
"ds_read_b64_tr_b16 v[144:145], v16 offset:6144\n"
"v_exp_f32_e32 v183, v183\n"
"s_waitcnt lgkmcnt(3)\n"
"v_mfma_f32_16x16x32_bf16 v[164:167], v[100:103], v[192:195], v[164:167]\n"
"v_cvt_pk_bf16_f32 v200, v152, v153\n"
"v_cvt_pk_bf16_f32 v201, v154, v155\n"
"v_cvt_pk_bf16_f32 v202, v180, v181\n"
"v_cvt_pk_bf16_f32 v203, v182, v183\n"
"v_mfma_f32_16x16x32_bf16 v[124:127], v[204:207], v[192:195], v[124:127]\n"
"ds_read_b64_tr_b16 v[146:147], v253 offset:6400\n"
"v_exp_f32_e32 v168, v168\n"
"v_exp_f32_e32 v169, v169\n"
"v_mfma_f32_16x16x32_bf16 v[172:175], v[136:139], v[192:195], v[172:175]\n"
"v_exp_f32_e32 v170, v170\n"
"v_exp_f32_e32 v171, v171\n"
"ds_read_b64_tr_b16 v[152:153], v253 offset:6144\n"
"v_mfma_f32_16x16x32_bf16 v[176:179], v[100:103], v[128:131], v[176:179]\n"
"v_exp_f32_e32 v208, v208\n"
"v_exp_f32_e32 v209, v209\n"
"v_mfma_f32_16x16x32_bf16 v[184:187], v[204:207], v[128:131], v[184:187]\n"
"v_exp_f32_e32 v210, v210\n"
"ds_read_b64_tr_b16 v[154:155], v16 offset:6400\n"
"v_exp_f32_e32 v211, v211\n"
"v_mfma_f32_16x16x32_bf16 v[140:143], v[136:139], v[128:131], v[140:143]\n"
"v_cvt_pk_bf16_f32 v128, v168, v169\n"
"v_cvt_pk_bf16_f32 v129, v170, v171\n"
"v_cvt_pk_bf16_f32 v130, v208, v209\n"
"v_cvt_pk_bf16_f32 v131, v210, v211\n"
"s_waitcnt lgkmcnt(4)\n"
"v_mfma_f32_16x16x32_bf16 v[136:139], v[148:151], v[88:91], v[116:119]\n"
"ds_read_b128 v[168:171], v15\n"
"v_exp_f32_e32 v132, v132\n"
"v_exp_f32_e32 v133, v133\n"
"v_exp_f32_e32 v134, v134\n"
"v_mfma_f32_16x16x32_bf16 v[180:183], v[148:151], v[92:95], v[120:123]\n"
"s_add_u32 s99, s56, 0x40000\n"
"buffer_load_dwordx4 v[148:151], v0, s[4:7], s99 offen sc1\n"
"v_exp_f32_e32 v135, v135\n"
"v_mfma_f32_16x16x32_bf16 v[192:195], v[160:163], v[88:91], v[116:119]\n"
"ds_read_b128 v[204:207], v252 offset:256\n"
"v_exp_f32_e32 v188, v188\n"
"v_exp_f32_e32 v189, v189\n"
"v_exp_f32_e32 v190, v190\n"
"v_mfma_f32_16x16x32_bf16 v[208:211], v[160:163], v[92:95], v[120:123]\n"
"buffer_load_dwordx4 v[160:163], v0, s[4:7], s99 offen offset:1024 sc1\n"
"ds_read_b64_tr_b16 v[212:213], v16 offset:4096\n"
"v_exp_f32_e32 v191, v191\n"
"s_waitcnt lgkmcnt(3)\n"
"v_mfma_f32_16x16x32_bf16 v[164:167], v[100:103], v[200:203], v[164:167]\n"
"v_cvt_pk_bf16_f32 v216, v132, v133\n"
"v_cvt_pk_bf16_f32 v217, v134, v135\n"
"v_cvt_pk_bf16_f32 v218, v188, v189\n"
"v_cvt_pk_bf16_f32 v219, v190, v191\n"
"buffer_load_dwordx4 v[132:135], v0, s[4:7], s99 offen offset:2048 sc1\n"
"v_mfma_f32_16x16x32_bf16 v[124:127], v[144:147], v[200:203], v[124:127]\n"
"ds_read_b64_tr_b16 v[214:215], v253 offset:4352\n"
"v_exp_f32_e32 v156, v156\n"
"v_exp_f32_e32 v157, v157\n"
"v_mfma_f32_16x16x32_bf16 v[172:175], v[152:155], v[200:203], v[172:175]\n"
"v_exp_f32_e32 v158, v158\n"
"v_exp_f32_e32 v159, v159\n"
"buffer_load_dwordx4 v[188:191], v0, s[4:7], s99 offen offset:3072 sc1\n"
"v_mfma_f32_16x16x32_bf16 v[176:179], v[100:103], v[128:131], v[176:179]\n"
"ds_read_b64_tr_b16 v[200:201], v253 offset:4096\n"
"v_exp_f32_e32 v196, v196\n"
"v_exp_f32_e32 v197, v197\n"
"v_mfma_f32_16x16x32_bf16 v[184:187], v[144:147], v[128:131], v[184:187]\n"
"v_exp_f32_e32 v198, v198\n"
"buffer_load_dwordx4 v[144:147], v1, s[4:7], s99 offen sc1\n"
"ds_read_b64_tr_b16 v[202:203], v16 offset:4352\n"
"v_exp_f32_e32 v199, v199\n"
"v_mfma_f32_16x16x32_bf16 v[140:143], v[152:155], v[128:131], v[140:143]\n"
"v_cvt_pk_bf16_f32 v128, v156, v157\n"
"v_cvt_pk_bf16_f32 v129, v158, v159\n"
"v_cvt_pk_bf16_f32 v130, v196, v197\n"
"v_cvt_pk_bf16_f32 v131, v198, v199\n"
"s_waitcnt lgkmcnt(4)\n"
"v_mfma_f32_16x16x32_bf16 v[152:155], v[168:171], v[88:91], v[116:119]\n"
"ds_read_b64_tr_b16 v[156:157], v16 offset:2048\n"
"v_exp_f32_e32 v136, v136\n"
"v_exp_f32_e32 v137, v137\n"
"ds_read_b64_tr_b16 v[158:159], v253 offset:2304\n"
"buffer_load_dwordx4 v[196:199], v1, s[4:7], s99 offen offset:1024 sc1\n"
"v_mfma_f32_16x16x32_bf16 v[220:223], v[168:171], v[92:95], v[120:123]\n"
"v_exp_f32_e32 v138, v138\n"
"v_exp_f32_e32 v139, v139\n"
"ds_read_b64_tr_b16 v[168:169], v253 offset:2048\n"
"v_mfma_f32_16x16x32_bf16 v[224:227], v[204:207], v[88:91], v[116:119]\n"
"v_exp_f32_e32 v192, v192\n"
"v_exp_f32_e32 v193, v193\n"
"buffer_load_dwordx4 v[116:119], v1, s[4:7], s99 offen offset:2048 sc1\n"
"v_mfma_f32_16x16x32_bf16 v[228:231], v[204:207], v[92:95], v[120:123]\n"
"ds_read_b64_tr_b16 v[170:171], v16 offset:2304\n"
"v_exp_f32_e32 v194, v194\n"
"v_exp_f32_e32 v195, v195\n"
"ds_read_b64_tr_b16 v[120:121], v16 offset:16384\n"
"s_waitcnt lgkmcnt(5)\n"
"v_mfma_f32_16x16x32_bf16 v[164:167], v[100:103], v[216:219], v[164:167]\n"
"v_cvt_pk_bf16_f32 v204, v136, v137\n"
"v_cvt_pk_bf16_f32 v205, v138, v139\n"
"v_cvt_pk_bf16_f32 v206, v192, v193\n"
"buffer_load_dwordx4 v[136:139], v1, s[4:7], s99 offen offset:3072 sc1\n"
"ds_read_b64_tr_b16 v[122:123], v253 offset:16640\n"
"v_mfma_f32_16x16x32_bf16 v[124:127], v[212:215], v[216:219], v[124:127]\n"
"v_cvt_pk_bf16_f32 v207, v194, v195\n"
"v_exp_f32_e32 v180, v180\n"
"ds_read_b64_tr_b16 v[192:193], v253 offset:16384\n"
"v_exp_f32_e32 v181, v181\n"
"v_exp_f32_e32 v182, v182\n"
"v_mfma_f32_16x16x32_bf16 v[172:175], v[200:203], v[216:219], v[172:175]\n"
"s_add_u32 s99, s57, 0x40000\n"
"buffer_load_dwordx4 v[216:219], v0, s[4:7], s99 offen sc1\n"
"ds_read_b64_tr_b16 v[194:195], v16 offset:16640\n"
"v_exp_f32_e32 v183, v183\n"
"v_mfma_f32_16x16x32_bf16 v[176:179], v[100:103], v[128:131], v[176:179]\n"
"v_exp_f32_e32 v208, v208\n"
"v_add_u32_e32 v248, 0x26400, v0\n"
"v_exp_f32_e32 v209, v209\n"
"v_mfma_f32_16x16x32_bf16 v[184:187], v[212:215], v[128:131], v[184:187]\n"
"v_exp_f32_e32 v210, v210\n"
"buffer_load_dwordx4 v[212:215], v0, s[4:7], s99 offen offset:1024 sc1\n"
"ds_read_b128 v[232:235], v248\n"
"v_exp_f32_e32 v211, v211\n"
"v_mfma_f32_16x16x32_bf16 v[140:143], v[200:203], v[128:131], v[140:143]\n"
"v_cvt_pk_bf16_f32 v128, v180, v181\n"
"ds_read_b128 v[200:203], v248 offset:1024\n"
"v_cvt_pk_bf16_f32 v129, v182, v183\n"
"v_cvt_pk_bf16_f32 v130, v208, v209\n"
"v_cvt_pk_bf16_f32 v131, v210, v211\n"
"s_waitcnt lgkmcnt(6)\n"
"v_mfma_f32_16x16x32_bf16 v[164:167], v[100:103], v[204:207], v[164:167]\n"
"ds_read_b64_tr_b16 v[180:181], v16\n"
"v_cndmask_b32_e64 v152, v96, v152, s[20:21]\n"
"v_cndmask_b32_e64 v153, v97, v153, s[22:23]\n"
"v_cndmask_b32_e64 v154, v98, v154, s[24:25]\n"
"buffer_load_dwordx4 v[208:211], v0, s[4:7], s99 offen offset:2048 sc1\n"
"v_cndmask_b32_e64 v155, v99, v155, s[26:27]\n"
"v_cndmask_b32_e64 v224, v108, v224, s[28:29]\n"
"v_cndmask_b32_e64 v225, v109, v225, s[30:31]\n"
"v_cndmask_b32_e64 v226, v110, v226, s[32:33]\n"
"v_cndmask_b32_e64 v227, v111, v227, s[34:35]\n"
"v_exp_f32_e32 v152, v152\n"
"v_mfma_f32_16x16x32_bf16 v[124:127], v[156:159], v[204:207], v[124:127]\n"
"v_exp_f32_e32 v153, v153\n"
"v_exp_f32_e32 v154, v154\n"
"ds_read_b64_tr_b16 v[182:183], v253 offset:256\n"
"buffer_load_dwordx4 v[96:99], v0, s[4:7], s99 offen offset:3072 sc1\n"
"v_exp_f32_e32 v155, v155\n"
"v_exp_f32_e32 v224, v224\n"
"v_mfma_f32_16x16x32_bf16 v[172:175], v[168:171], v[204:207], v[172:175]\n"
"v_exp_f32_e32 v225, v225\n"
"v_exp_f32_e32 v226, v226\n"
"v_exp_f32_e32 v227, v227\n"
"v_cvt_pk_bf16_f32 v108, v152, v153\n"
"buffer_load_dwordx4 v[204:207], v1, s[4:7], s99 offen sc1\n"
"v_cvt_pk_bf16_f32 v109, v154, v155\n"
"v_cvt_pk_bf16_f32 v110, v224, v225\n"
"v_mfma_f32_16x16x32_bf16 v[176:179], v[100:103], v[128:131], v[176:179]\n"
"v_cvt_pk_bf16_f32 v111, v226, v227\n"
"ds_read_b64_tr_b16 v[152:153], v253\n"
"v_cndmask_b32_e64 v220, v104, v220, s[36:37]\n"
"v_cndmask_b32_e64 v221, v105, v221, s[38:39]\n"
"v_cndmask_b32_e64 v222, v106, v222, s[40:41]\n"
"v_cndmask_b32_e64 v223, v107, v223, s[42:43]\n"
"v_cndmask_b32_e64 v228, v112, v228, s[44:45]\n"
"v_cndmask_b32_e64 v229, v113, v229, s[46:47]\n"
"v_cndmask_b32_e64 v230, v114, v230, s[48:49]\n"
"buffer_load_dwordx4 v[104:107], v1, s[4:7], s99 offen offset:1024 sc1\n"
"v_cndmask_b32_e64 v231, v115, v231, s[50:51]\n"
"v_mfma_f32_16x16x32_bf16 v[184:187], v[156:159], v[128:131], v[184:187]\n"
"v_exp_f32_e32 v220, v220\n"
"v_exp_f32_e32 v221, v221\n"
"v_exp_f32_e32 v222, v222\n"
"ds_read_b64_tr_b16 v[154:155], v16 offset:256\n"
"v_exp_f32_e32 v223, v223\n"
"v_exp_f32_e32 v228, v228\n"
"buffer_load_dwordx4 v[112:115], v1, s[4:7], s99 offen offset:2048 sc1\n"
"v_mfma_f32_16x16x32_bf16 v[140:143], v[168:171], v[128:131], v[140:143]\n"
"v_exp_f32_e32 v229, v229\n"
"v_exp_f32_e32 v230, v230\n"
"v_exp_f32_e32 v231, v231\n"
"v_cvt_pk_bf16_f32 v128, v220, v221\n"
"v_cvt_pk_bf16_f32 v129, v222, v223\n"
"v_cvt_pk_bf16_f32 v130, v228, v229\n"
"v_cvt_pk_bf16_f32 v131, v230, v231\n"
"s_waitcnt lgkmcnt(0)\n"
"v_mfma_f32_16x16x32_bf16 v[164:167], v[100:103], v[108:111], v[164:167]\n"
"v_and_b32_e32 v232, v108, v232\n"
"v_and_b32_e32 v233, v109, v233\n"
"v_and_b32_e32 v234, v110, v234\n"
"v_and_b32_e32 v235, v111, v235\n"
"v_mfma_f32_16x16x32_bf16 v[176:179], v[100:103], v[128:131], v[176:179]\n"
"v_xor_b32_e32 v156, v108, v232\n"
"v_xor_b32_e32 v157, v109, v233\n"
"v_xor_b32_e32 v158, v110, v234\n"
"v_xor_b32_e32 v159, v111, v235\n"
"v_mfma_f32_16x16x32_bf16 v[124:127], v[180:183], v[232:235], v[124:127]\n"
"v_and_b32_e32 v200, v128, v200\n"
"v_and_b32_e32 v201, v129, v201\n"
"v_mfma_f32_16x16x32_bf16 v[172:175], v[152:155], v[232:235], v[172:175]\n"
"v_and_b32_e32 v202, v130, v202\n"
"v_and_b32_e32 v203, v131, v203\n"
"v_mfma_f32_16x16x32_bf16 v[124:127], v[120:123], v[156:159], v[124:127]\n"
"v_xor_b32_e32 v108, v128, v200\n"
"v_xor_b32_e32 v109, v129, v201\n"
"v_mfma_f32_16x16x32_bf16 v[172:175], v[192:195], v[156:159], v[172:175]\n"
"v_xor_b32_e32 v110, v130, v202\n"
"v_xor_b32_e32 v111, v131, v203\n"
"v_mfma_f32_16x16x32_bf16 v[184:187], v[180:183], v[200:203], v[184:187]\n"
"buffer_load_dwordx4 v[128:131], v1, s[4:7], s99 offen offset:3072 sc1\n"
"s_add_u32 s99, s58, 0x40000\n"
"buffer_load_dwordx4 v[156:159], v0, s[4:7], s99 offen sc1\n"
"v_mfma_f32_16x16x32_bf16 v[140:143], v[152:155], v[200:203], v[140:143]\n"
"buffer_load_dwordx4 v[152:155], v0, s[4:7], s99 offen offset:1024 sc1\n"
"v_mfma_f32_16x16x32_bf16 v[184:187], v[120:123], v[108:111], v[184:187]\n"
"buffer_load_dwordx4 v[120:123], v0, s[4:7], s99 offen offset:2048 sc1\n"
"v_mfma_f32_16x16x32_bf16 v[140:143], v[192:195], v[108:111], v[140:143]\n"
"buffer_load_dwordx4 v[108:111], v0, s[4:7], s99 offen offset:3072 sc1\n"
"v_max_f32_e32 v248, v164, v176\n"
"v_rcp_f32_e32 v164, v164\n"
"v_rcp_f32_e32 v176, v176\n"
"s_nop 0\n"
"v_max3_f32 v248, v248, v164, v176\n"
"v_cmp_nlt_f32_e32 vcc, v248, v251\n"
"s_cbranch_vccnz .Lmk_b108\n"
".Lmk_k113:\n"
"v_mul_f32_e32 v124, v124, v164\n"
"v_mul_f32_e32 v125, v125, v164\n"
"v_mul_f32_e32 v126, v126, v164\n"
"v_mul_f32_e32 v127, v127, v164\n"
"v_mul_f32_e32 v172, v172, v164\n"
"v_mul_f32_e32 v173, v173, v164\n"
"v_mul_f32_e32 v174, v174, v164\n"
"v_mul_f32_e32 v175, v175, v164\n"
"v_cvt_pk_bf16_f32 v88, v124, v125\n"
"v_cvt_pk_bf16_f32 v89, v126, v127\n"
"v_cvt_pk_bf16_f32 v90, v172, v173\n"
"v_cvt_pk_bf16_f32 v91, v174, v175\n"
"buffer_store_dwordx4 v[88:91], v0, s[4:7], s60 offen sc1\n"
"v_mul_f32_e32 v184, v184, v176\n"
"v_mul_f32_e32 v185, v185, v176\n"
"v_mul_f32_e32 v186, v186, v176\n"
"v_mul_f32_e32 v187, v187, v176\n"
"v_mul_f32_e32 v140, v140, v176\n"
"v_mul_f32_e32 v141, v141, v176\n"
"v_mul_f32_e32 v142, v142, v176\n"
"v_mul_f32_e32 v143, v143, v176\n"
"v_cvt_pk_bf16_f32 v88, v184, v185\n"
"v_cvt_pk_bf16_f32 v89, v186, v187\n"
"v_cvt_pk_bf16_f32 v90, v140, v141\n"
"v_cvt_pk_bf16_f32 v91, v142, v143\n"
"s_add_u32 s98, s60, 0x8000\n"
"buffer_store_dwordx4 v[88:91], v0, s[4:7], s98 offen sc1\n"
"s_waitcnt vmcnt(21)\n"
"; @A G2 b.0\n"
"s_nop 0\n"
"v_mfma_f32_16x16x32_bf16 v[88:91], a[128:131], v[148:151], 0\n"
"v_mfma_f32_16x16x32_bf16 v[92:95], a[132:135], v[148:151], 0\n"
"v_mfma_f32_16x16x32_bf16 v[100:103], a[136:139], v[148:151], 0\n"
"buffer_load_dwordx4 v[124:127], v1, s[4:7], s99 offen sc1\n"
"v_mfma_f32_16x16x32_bf16 v[140:143], a[140:143], v[148:151], 0\n"
"s_waitcnt vmcnt(21)\n"
"v_mfma_f32_16x16x32_bf16 v[88:91], a[144:147], v[160:163], v[88:91]\n"
"v_mfma_f32_16x16x32_bf16 v[92:95], a[148:151], v[160:163], v[92:95]\n"
"v_mfma_f32_16x16x32_bf16 v[100:103], a[152:155], v[160:163], v[100:103]\n"
"buffer_load_dwordx4 v[148:151], v1, s[4:7], s99 offen offset:1024 sc1\n"
"v_mfma_f32_16x16x32_bf16 v[140:143], a[156:159], v[160:163], v[140:143]\n"
"s_waitcnt vmcnt(21)\n"
"v_mfma_f32_16x16x32_bf16 v[88:91], a[160:163], v[132:135], v[88:91]\n"
"v_mfma_f32_16x16x32_bf16 v[92:95], a[164:167], v[132:135], v[92:95]\n"
"v_mfma_f32_16x16x32_bf16 v[100:103], a[168:171], v[132:135], v[100:103]\n"
"buffer_load_dwordx4 v[160:163], v1, s[4:7], s99 offen offset:2048 sc1\n"
"v_mfma_f32_16x16x32_bf16 v[140:143], a[172:175], v[132:135], v[140:143]\n"
"s_waitcnt vmcnt(21)\n"
"v_mfma_f32_16x16x32_bf16 v[88:91], a[176:179], v[188:191], v[88:91]\n"
"v_mfma_f32_16x16x32_bf16 v[92:95], a[180:183], v[188:191], v[92:95]\n"
"v_mfma_f32_16x16x32_bf16 v[100:103], a[184:187], v[188:191], v[100:103]\n"
"buffer_load_dwordx4 v[132:135], v1, s[4:7], s99 offen offset:3072 sc1\n"
"v_mfma_f32_16x16x32_bf16 v[140:143], a[188:191], v[188:191], v[140:143]\n"
"s_waitcnt vmcnt(21)\n"
"v_mfma_f32_16x16x32_bf16 v[88:91], a[192:195], v[144:147], v[88:91]\n"
"s_add_u32 s99, s59, 0x40000\n"
"v_mfma_f32_16x16x32_bf16 v[92:95], a[196:199], v[144:147], v[92:95]\n"
"v_mfma_f32_16x16x32_bf16 v[100:103], a[200:203], v[144:147], v[100:103]\n"
"buffer_load_dwordx4 v[164:167], v0, s[4:7], s99 offen sc1\n"
"v_mfma_f32_16x16x32_bf16 v[140:143], a[204:207], v[144:147], v[140:143]\n"
"s_waitcnt vmcnt(21)\n"
"v_mfma_f32_16x16x32_bf16 v[88:91], a[208:211], v[196:199], v[88:91]\n"
"v_mfma_f32_16x16x32_bf16 v[92:95], a[212:215], v[196:199], v[92:95]\n"
"v_mfma_f32_16x16x32_bf16 v[100:103], a[216:219], v[196:199], v[100:103]\n"
"buffer_load_dwordx4 v[144:147], v0, s[4:7], s99 offen offset:1024 sc1\n"
"v_mfma_f32_16x16x32_bf16 v[140:143], a[220:223], v[196:199], v[140:143]\n"
"s_waitcnt vmcnt(21)\n"
"v_mfma_f32_16x16x32_bf16 v[88:91], a[224:227], v[116:119], v[88:91]\n"
"v_mfma_f32_16x16x32_bf16 v[92:95], a[228:231], v[116:119], v[92:95]\n"
"v_mfma_f32_16x16x32_bf16 v[100:103], a[232:235], v[116:119], v[100:103]\n"
"buffer_load_dwordx4 v[168:171], v0, s[4:7], s99 offen offset:2048 sc1\n"
"v_mfma_f32_16x16x32_bf16 v[140:143], a[236:239], v[116:119], v[140:143]\n"
"s_waitcnt vmcnt(21)\n"
"v_mfma_f32_16x16x32_bf16 v[88:91], a[240:243], v[136:139], v[88:91]\n"
"v_mfma_f32_16x16x32_bf16 v[92:95], a[244:247], v[136:139], v[92:95]\n"
"v_mfma_f32_16x16x32_bf16 v[100:103], a[248:251], v[136:139], v[100:103]\n"
"buffer_load_dwordx4 v[116:119], v0, s[4:7], s99 offen offset:3072 sc1\n"
"v_mfma_f32_16x16x32_bf16 v[140:143], a[252:255], v[136:139], v[140:143]\n"
"s_waitcnt vmcnt(20)\n"
"; @A G2 b.1\n"
"v_mfma_f32_16x16x32_bf16 v[136:139], a[128:131], v[216:219], 0\n"
"s_nop 1\n"
"v_cmp_u_f32_e32 vcc, v88, v88\n"
"v_mfma_f32_16x16x32_bf16 v[172:175], a[132:135], v[216:219], 0\n"
"s_cbranch_vccnz .Lmk_p114s\n"
".Lmk_p114r:\n"
"v_mfma_f32_16x16x32_bf16 v[176:179], a[136:139], v[216:219], 0\n"
"buffer_load_dwordx4 v[180:183], v1, s[4:7], s99 offen sc1\n"
"v_mfma_f32_16x16x32_bf16 v[184:187], a[140:143], v[216:219], 0\n"
"v_mfma_f32_16x16x32_bf16 v[136:139], a[144:147], v[212:215], v[136:139]\n"
"ds_write_b128 v3, v[88:91]\n"
"v_mfma_f32_16x16x32_bf16 v[172:175], a[148:151], v[212:215], v[172:175]\n"
"v_mfma_f32_16x16x32_bf16 v[176:179], a[152:155], v[212:215], v[176:179]\n"
"ds_write_b128 v3, v[92:95] offset:1024\n"
"v_mfma_f32_16x16x32_bf16 v[184:187], a[156:159], v[212:215], v[184:187]\n"
"s_waitcnt vmcnt(19)\n"
"v_mfma_f32_16x16x32_bf16 v[136:139], a[160:163], v[208:211], v[136:139]\n"
"ds_write_b128 v3, v[100:103] offset:2048\n"
"v_mfma_f32_16x16x32_bf16 v[172:175], a[164:167], v[208:211], v[172:175]\n"
"buffer_load_dwordx4 v[88:91], v1, s[4:7], s99 offen offset:1024 sc1\n"
"v_mfma_f32_16x16x32_bf16 v[176:179], a[168:171], v[208:211], v[176:179]\n"
"ds_write_b128 v3, v[140:143] offset:3072\n"
"v_mfma_f32_16x16x32_bf16 v[184:187], a[172:175], v[208:211], v[184:187]\n"
"v_mfma_f32_16x16x32_bf16 v[136:139], a[176:179], v[96:99], v[136:139]\n"
"v_mfma_f32_16x16x32_bf16 v[172:175], a[180:183], v[96:99], v[172:175]\n"
"buffer_load_dwordx4 v[92:95], v1, s[4:7], s99 offen offset:2048 sc1\n"
"v_mfma_f32_16x16x32_bf16 v[176:179], a[184:187], v[96:99], v[176:179]\n"
"ds_read_b128 v[100:103], v2 offset:8192\n"
"ds_read_b128 v[140:143], v2 offset:9216\n"
"v_mfma_f32_16x16x32_bf16 v[184:187], a[188:191], v[96:99], v[184:187]\n"
"s_waitcnt vmcnt(19)\n"
"v_mfma_f32_16x16x32_bf16 v[136:139], a[192:195], v[204:207], v[136:139]\n"
"v_mfma_f32_16x16x32_bf16 v[172:175], a[196:199], v[204:207], v[172:175]\n"
"buffer_load_dwordx4 v[96:99], v1, s[4:7], s99 offen offset:3072 sc1\n"
"v_mfma_f32_16x16x32_bf16 v[176:179], a[200:203], v[204:207], v[176:179]\n"
"v_mfma_f32_16x16x32_bf16 v[184:187], a[204:207], v[204:207], v[184:187]\n"
"s_waitcnt lgkmcnt(0)\n"
"s_barrier\n"
"v_mfma_f32_16x16x32_bf16 v[136:139], a[208:211], v[104:107], v[136:139]\n"
"v_lshlrev_b32_e32 v255, 16, v100\n"
"v_mfma_f32_16x16x32_bf16 v[172:175], a[212:215], v[104:107], v[172:175]\n"
"ds_read_b128 v[188:191], v4\n"
"ds_read_b128 v[192:195], v4 offset:1024\n"
"v_mfma_f32_16x16x32_bf16 v[176:179], a[216:219], v[104:107], v[176:179]\n"
"ds_read_b128 v[196:199], v4 offset:2048\n"
"ds_read_b128 v[200:203], v4 offset:3072\n"
"v_mfma_f32_16x16x32_bf16 v[184:187], a[220:223], v[104:107], v[184:187]\n"
"s_waitcnt vmcnt(18)\n"
"v_and_b32_e32 v254, 0xffff0000, v100\n"
"v_mfma_f32_16x16x32_bf16 v[136:139], a[224:227], v[112:115], v[136:139]\n"
"v_lshlrev_b32_e32 v253, 16, v101\n"
"v_and_b32_e32 v252, 0xffff0000, v101\n"
"v_mfma_f32_16x16x32_bf16 v[172:175], a[228:231], v[112:115], v[172:175]\n"
"v_lshlrev_b32_e32 v251, 16, v102\n"
"v_and_b32_e32 v250, 0xffff0000, v102\n"
"v_mfma_f32_16x16x32_bf16 v[176:179], a[232:235], v[112:115], v[176:179]\n"
"v_lshlrev_b32_e32 v249, 16, v103\n"
"v_and_b32_e32 v248, 0xffff0000, v103\n"
"v_mfma_f32_16x16x32_bf16 v[184:187], a[236:239], v[112:115], v[184:187]\n"
"v_lshlrev_b32_e32 v247, 16, v140\n"
"v_and_b32_e32 v246, 0xffff0000, v140\n"
"v_mfma_f32_16x16x32_bf16 v[136:139], a[240:243], v[128:131], v[136:139]\n"
"v_lshlrev_b32_e32 v245, 16, v141\n"
"v_and_b32_e32 v244, 0xffff0000, v141\n"
"v_mfma_f32_16x16x32_bf16 v[172:175], a[244:247], v[128:131], v[172:175]\n"
"v_lshlrev_b32_e32 v243, 16, v142\n"
"v_and_b32_e32 v242, 0xffff0000, v142\n"
"v_mfma_f32_16x16x32_bf16 v[176:179], a[248:251], v[128:131], v[176:179]\n"
"v_lshlrev_b32_e32 v241, 16, v143\n"
"v_and_b32_e32 v240, 0xffff0000, v143\n"
"v_mfma_f32_16x16x32_bf16 v[184:187], a[252:255], v[128:131], v[184:187]\n"
"s_waitcnt vmcnt(16)\n"
"s_waitcnt lgkmcnt(0)\n"
"; @A G2 b.2\n"
"v_mfma_f32_16x16x32_bf16 v[100:103], a[128:131], v[156:159], 0\n"
"v_cmp_u_f32_e32 vcc, v136, v136\n"
"v_add_f32_e32 v188, v188, v255\n"
"v_mfma_f32_16x16x32_bf16 v[104:107], a[132:135], v[156:159], 0\n"
"s_cbranch_vccnz .Lmk_p115s\n"
".Lmk_p115r:\n"
"v_add_f32_e32 v189, v189, v254\n"
"v_mfma_f32_16x16x32_bf16 v[112:115], a[136:139], v[156:159], 0\n"
"v_add_f32_e32 v190, v190, v253\n"
"v_add_f32_e32 v191, v191, v252\n"
"v_mfma_f32_16x16x32_bf16 v[128:131], a[140:143], v[156:159], 0\n"
"v_add_f32_e32 v192, v192, v251\n"
"v_add_f32_e32 v193, v193, v250\n"
"v_mfma_f32_16x16x32_bf16 v[100:103], a[144:147], v[152:155], v[100:103]\n"
"ds_write_b128 v3, v[136:139] offset:6144\n"
"v_mfma_f32_16x16x32_bf16 v[104:107], a[148:151], v[152:155], v[104:107]\n"
"v_add_f32_e32 v194, v194, v249\n"
"v_add_f32_e32 v195, v195, v248\n"
"v_mfma_f32_16x16x32_bf16 v[112:115], a[152:155], v[152:155], v[112:115]\n"
"ds_write_b128 v3, v[172:175] offset:7168\n"
"v_mfma_f32_16x16x32_bf16 v[128:131], a[156:159], v[152:155], v[128:131]\n"
"s_waitcnt vmcnt(14)\n"
"v_add_f32_e32 v196, v196, v247\n"
"v_mfma_f32_16x16x32_bf16 v[100:103], a[160:163], v[120:123], v[100:103]\n"
"ds_write_b128 v3, v[176:179] offset:8192\n"
"v_mfma_f32_16x16x32_bf16 v[104:107], a[164:167], v[120:123], v[104:107]\n"
"v_add_f32_e32 v197, v197, v246\n"
"v_add_f32_e32 v198, v198, v245\n"
"v_mfma_f32_16x16x32_bf16 v[112:115], a[168:171], v[120:123], v[112:115]\n"
"ds_write_b128 v3, v[184:187] offset:9216\n"
"v_mfma_f32_16x16x32_bf16 v[128:131], a[172:175], v[120:123], v[128:131]\n"
"v_add_f32_e32 v199, v199, v244\n"
"v_add_f32_e32 v200, v200, v243\n"
"v_mfma_f32_16x16x32_bf16 v[100:103], a[176:179], v[108:111], v[100:103]\n"
"s_add_u32 s99, s56, 0\n"
"v_add_f32_e32 v201, v201, v242\n"
"v_mfma_f32_16x16x32_bf16 v[104:107], a[180:183], v[108:111], v[104:107]\n"
"buffer_load_dwordx4 v[120:123], v0, s[4:7], s99 offen sc1\n"
"v_mfma_f32_16x16x32_bf16 v[112:115], a[184:187], v[108:111], v[112:115]\n"
"v_add_f32_e32 v202, v202, v241\n"
"v_add_f32_e32 v203, v203, v240\n"
"v_mfma_f32_16x16x32_bf16 v[128:131], a[188:191], v[108:111], v[128:131]\n"
"s_waitcnt vmcnt(11)\n"
"v_mfma_f32_16x16x32_bf16 v[100:103], a[192:195], v[124:127], v[100:103]\n"
"buffer_load_dwordx4 v[108:111], v0, s[4:7], s99 offen offset:1024 sc1\n"
"v_mfma_f32_16x16x32_bf16 v[104:107], a[196:199], v[124:127], v[104:107]\n"
"v_mfma_f32_16x16x32_bf16 v[112:115], a[200:203], v[124:127], v[112:115]\n"
"v_mfma_f32_16x16x32_bf16 v[128:131], a[204:207], v[124:127], v[128:131]\n"
"buffer_load_dwordx4 v[124:127], v0, s[4:7], s99 offen offset:2048 sc1\n"
"v_mfma_f32_16x16x32_bf16 v[100:103], a[208:211], v[148:151], v[100:103]\n"
"v_mfma_f32_16x16x32_bf16 v[104:107], a[212:215], v[148:151], v[104:107]\n"
"v_mfma_f32_16x16x32_bf16 v[112:115], a[216:219], v[148:151], v[112:115]\n"
"buffer_load_dwordx4 v[136:139], v0, s[4:7], s99 offen offset:3072 sc1\n"
"v_mfma_f32_16x16x32_bf16 v[128:131], a[220:223], v[148:151], v[128:131]\n"
"s_waitcnt vmcnt(12)\n"
"v_mfma_f32_16x16x32_bf16 v[100:103], a[224:227], v[160:163], v[100:103]\n"
"s_waitcnt vmcnt(10)\n"
"v_max_u32_e32 v255, v164, v165\n"
"v_mfma_f32_16x16x32_bf16 v[104:107], a[228:231], v[160:163], v[104:107]\n"
"v_max3_u32 v255, v255, v166, v167\n"
"v_max3_u32 v255, v255, v144, v145\n"
"v_mfma_f32_16x16x32_bf16 v[112:115], a[232:235], v[160:163], v[112:115]\n"
"v_max3_u32 v255, v255, v146, v147\n"
"s_waitcnt vmcnt(8)\n"
"v_mfma_f32_16x16x32_bf16 v[128:131], a[236:239], v[160:163], v[128:131]\n"
"v_max3_u32 v255, v255, v168, v169\n"
"v_max3_u32 v255, v255, v170, v171\n"
"v_mfma_f32_16x16x32_bf16 v[100:103], a[240:243], v[132:135], v[100:103]\n"
"v_max3_u32 v255, v255, v116, v117\n"
"v_max3_u32 v255, v255, v118, v119\n"
"v_mfma_f32_16x16x32_bf16 v[104:107], a[244:247], v[132:135], v[104:107]\n"
"v_cmp_eq_u32_e32 vcc, -1, v255\n"
"v_mfma_f32_16x16x32_bf16 v[112:115], a[248:251], v[132:135], v[112:115]\n"
"s_cbranch_vccnz .Lmk_q117s\n"
".Lmk_q117r:\n"
"v_mfma_f32_16x16x32_bf16 v[128:131], a[252:255], v[132:135], v[128:131]\n"
"; @A G2 b.3\n"
"v_mfma_f32_16x16x32_bf16 v[188:191], a[128:131], v[164:167], v[188:191]\n"
"v_cmp_u_f32_e32 vcc, v100, v100\n"
"v_mfma_f32_16x16x32_bf16 v[192:195], a[132:135], v[164:167], v[192:195]\n"
"s_cbranch_vccnz .Lmk_p116s\n"
".Lmk_p116r:\n"
"v_mfma_f32_16x16x32_bf16 v[196:199], a[136:139], v[164:167], v[196:199]\n"
"buffer_load_dwordx4 v[132:135], v1, s[4:7], s99 offen sc1\n"
"v_mfma_f32_16x16x32_bf16 v[200:203], a[140:143], v[164:167], v[200:203]\n"
"v_mfma_f32_16x16x32_bf16 v[188:191], a[144:147], v[144:147], v[188:191]\n"
"ds_write_b128 v3, v[100:103] offset:12288\n"
"v_mfma_f32_16x16x32_bf16 v[192:195], a[148:151], v[144:147], v[192:195]\n"
"buffer_load_dwordx4 v[100:103], v1, s[4:7], s99 offen offset:1024 sc1\n"
"v_mfma_f32_16x16x32_bf16 v[196:199], a[152:155], v[144:147], v[196:199]\n"
"ds_write_b128 v3, v[104:107] offset:13312\n"
"v_mfma_f32_16x16x32_bf16 v[200:203], a[156:159], v[144:147], v[200:203]\n"
"v_mfma_f32_16x16x32_bf16 v[188:191], a[160:163], v[168:171], v[188:191]\n"
"s_waitcnt vmcnt(8)\n"
"v_max_u32_e32 v255, v180, v181\n"
"v_mfma_f32_16x16x32_bf16 v[192:195], a[164:167], v[168:171], v[192:195]\n"
"v_max3_u32 v255, v255, v182, v183\n"
"v_max3_u32 v255, v255, v88, v89\n"
"v_mfma_f32_16x16x32_bf16 v[196:199], a[168:171], v[168:171], v[196:199]\n"
"v_max3_u32 v255, v255, v90, v91\n"
"s_waitcnt vmcnt(6)\n"
"v_mfma_f32_16x16x32_bf16 v[200:203], a[172:175], v[168:171], v[200:203]\n"
"v_max3_u32 v255, v255, v92, v93\n"
"v_max3_u32 v255, v255, v94, v95\n"
"v_mfma_f32_16x16x32_bf16 v[188:191], a[176:179], v[116:119], v[188:191]\n"
"v_max3_u32 v255, v255, v96, v97\n"
"v_max3_u32 v255, v255, v98, v99\n"
"v_mfma_f32_16x16x32_bf16 v[192:195], a[180:183], v[116:119], v[192:195]\n"
"v_cmp_eq_u32_e32 vcc, -1, v255\n"
"v_mfma_f32_16x16x32_bf16 v[196:199], a[184:187], v[116:119], v[196:199]\n"
"s_cbranch_vccnz .Lmk_q118s\n"
".Lmk_q118r:\n"
"v_mfma_f32_16x16x32_bf16 v[200:203], a[188:191], v[116:119], v[200:203]\n"
"v_mfma_f32_16x16x32_bf16 v[188:191], a[192:195], v[180:183], v[188:191]\n"
"ds_write_b128 v3, v[112:115] offset:14336\n"
"v_mfma_f32_16x16x32_bf16 v[192:195], a[196:199], v[180:183], v[192:195]\n"
"buffer_load_dwordx4 v[104:107], v1, s[4:7], s99 offen offset:2048 sc1\n"
"v_mfma_f32_16x16x32_bf16 v[196:199], a[200:203], v[180:183], v[196:199]\n"
"ds_write_b128 v3, v[128:131] offset:15360\n"
"v_mfma_f32_16x16x32_bf16 v[200:203], a[204:207], v[180:183], v[200:203]\n"
"v_mfma_f32_16x16x32_bf16 v[188:191], a[208:211], v[88:91], v[188:191]\n"
"buffer_load_dwordx4 v[112:115], v1, s[4:7], s99 offen offset:3072 sc1\n"
"v_mfma_f32_16x16x32_bf16 v[192:195], a[212:215], v[88:91], v[192:195]\n"
"s_waitcnt lgkmcnt(0)\n"
"s_barrier\n"
"v_mfma_f32_16x16x32_bf16 v[196:199], a[216:219], v[88:91], v[196:199]\n"
"s_add_u32 s99, s57, 0\n"
"ds_read_b128 v[116:119], v5\n"
"v_mfma_f32_16x16x32_bf16 v[200:203], a[220:223], v[88:91], v[200:203]\n"
"ds_read_b128 v[88:91], v6\n"
"v_mfma_f32_16x16x32_bf16 v[188:191], a[224:227], v[92:95], v[188:191]\n"
"buffer_load_dwordx4 v[128:131], v0, s[4:7], s99 offen sc1\n"
"v_mfma_f32_16x16x32_bf16 v[192:195], a[228:231], v[92:95], v[192:195]\n"
"ds_read_b128 v[140:143], v5 offset:1024\n"
"ds_read_b128 v[144:147], v6 offset:1024\n"
"v_mfma_f32_16x16x32_bf16 v[196:199], a[232:235], v[92:95], v[196:199]\n"
"ds_read_b128 v[148:151], v5 offset:2048\n"
"ds_read_b128 v[152:155], v6 offset:2048\n"
"v_mfma_f32_16x16x32_bf16 v[200:203], a[236:239], v[92:95], v[200:203]\n"
"buffer_load_dwordx4 v[92:95], v0, s[4:7], s99 offen offset:1024 sc1\n"
"v_mfma_f32_16x16x32_bf16 v[188:191], a[240:243], v[96:99], v[188:191]\n"
"ds_read_b128 v[156:159], v5 offset:3072\n"
"ds_read_b128 v[160:163], v6 offset:3072\n"
"v_mfma_f32_16x16x32_bf16 v[192:195], a[244:247], v[96:99], v[192:195]\n"
"v_mfma_f32_16x16x32_bf16 v[196:199], a[248:251], v[96:99], v[196:199]\n"
"buffer_load_dwordx4 v[164:167], v0, s[4:7], s99 offen offset:2048 sc1\n"
"v_mfma_f32_16x16x32_bf16 v[200:203], a[252:255], v[96:99], v[200:203]\n"
"s_waitcnt lgkmcnt(4)\n"
"v_add_f32_e32 v116, v116, v88\n"
"v_add_f32_e32 v117, v117, v89\n"
"v_add_f32_e32 v118, v118, v90\n"
"v_add_f32_e32 v119, v119, v91\n"
"v_add_f32_e32 v140, v140, v144\n"
"v_add_f32_e32 v141, v141, v145\n"
"v_add_f32_e32 v142, v142, v146\n"
"v_add_f32_e32 v143, v143, v147\n"
"buffer_load_dwordx4 v[88:91], v0, s[4:7], s99 offen offset:3072 sc1\n"
"s_waitcnt lgkmcnt(0)\n"
"v_add_f32_e32 v148, v148, v152\n"
"v_add_f32_e32 v149, v149, v153\n"
"v_add_f32_e32 v150, v150, v154\n"
"v_add_f32_e32 v151, v151, v155\n"
"v_add_f32_e32 v156, v156, v160\n"
"v_add_f32_e32 v157, v157, v161\n"
"v_add_f32_e32 v158, v158, v162\n"
"v_add_f32_e32 v159, v159, v163\n"
"buffer_load_dwordx4 v[96:99], v1, s[4:7], s99 offen sc1\n"
"buffer_load_dwordx4 v[144:147], v1, s[4:7], s99 offen offset:1024 sc1\n"
"buffer_load_dwordx4 v[152:155], v1, s[4:7], s99 offen offset:2048 sc1\n"
"buffer_load_dwordx4 v[160:163], v1, s[4:7], s99 offen offset:3072 sc1\n"
"; @A fin b\n"
"v_mov_b32_e32 v168, 0x3f803f80\n"
"v_mov_b32_e32 v169, 0x3f803f80\n"
"v_mov_b32_e32 v170, 0x3f803f80\n"
"v_mov_b32_e32 v171, 0x3f803f80\n"
"v_add_f32_e32 v116, v188, v116\n"
"v_add_f32_e32 v117, v189, v117\n"
"v_add_f32_e32 v118, v190, v118\n"
"v_add_f32_e32 v119, v191, v119\n"
"v_add_f32_e32 v140, v192, v140\n"
"v_add_f32_e32 v141, v193, v141\n"
"v_add_f32_e32 v142, v194, v142\n"
"v_add_f32_e32 v143, v195, v143\n"
"v_cvt_pk_bf16_f32 v172, v116, v117\n"
"v_cvt_pk_bf16_f32 v173, v118, v119\n"
"v_cvt_pk_bf16_f32 v174, v140, v141\n"
"v_cvt_pk_bf16_f32 v175, v142, v143\n"
"s_add_u32 s99, s61, 0x440000\n"
"buffer_store_dwordx4 v[172:175], v0, s[4:7], s99 offen sc1\n"
"ds_write_b128 v2, v[172:175] offset:8192\n"
"v_add_f32_e32 v148, v196, v148\n"
"v_add_f32_e32 v149, v197, v149\n"
"v_add_f32_e32 v150, v198, v150\n"
"v_add_f32_e32 v151, v199, v151\n"
"v_add_f32_e32 v156, v200, v156\n"
"v_add_f32_e32 v157, v201, v157\n"
"v_add_f32_e32 v158, v202, v158\n"
"v_add_f32_e32 v159, v203, v159\n"
"v_cvt_pk_bf16_f32 v116, v148, v149\n"
"v_cvt_pk_bf16_f32 v117, v150, v151\n"
"v_cvt_pk_bf16_f32 v118, v156, v157\n"
"v_cvt_pk_bf16_f32 v119, v158, v159\n"
"s_add_u32 s99, s61, 0x440000\n"
"buffer_store_dwordx4 v[116:119], v0, s[4:7], s99 offen offset:1024 sc1\n"
"ds_write_b128 v2, v[116:119] offset:9216\n"
"v_mfma_f32_16x16x32_bf16 v[140:143], v[168:171], v[172:175], 0\n"
"v_mfma_f32_16x16x32_bf16 v[148:151], v[172:175], v[172:175], 0\n"
"v_mfma_f32_16x16x32_bf16 v[140:143], v[168:171], v[116:119], v[140:143]\n"
"v_mfma_f32_16x16x32_bf16 v[148:151], v[116:119], v[116:119], v[148:151]\n"
"s_mov_b32 s98, 0xaaaaaaaa\n"
"s_mov_b32 s99, 0xaaaaaaaa\n"
"s_nop 5\n"
"v_cndmask_b32_e64 v148, v148, v149, s[98:99]\n"
"v_cndmask_b32_e64 v150, v150, v151, s[98:99]\n"
"s_mov_b32 s98, 0xcccccccc\n"
"s_mov_b32 s99, 0xcccccccc\n"
"v_cndmask_b32_e64 v141, v148, v150, s[98:99]\n"
"s_add_u32 s99, s63, 0x44000\n"
"buffer_store_dwordx2 v[140:141], v14, s[4:7], s99 offen sc1\n"
"s_add_u32 s99, s58, 0\n"
"buffer_load_dwordx4 v[116:119], v0, s[4:7], s99 offen sc1\n"
"buffer_load_dwordx4 v[140:143], v0, s[4:7], s99 offen offset:1024 sc1\n"
"buffer_load_dwordx4 v[148:151], v0, s[4:7], s99 offen offset:2048 sc1\n"
"buffer_load_dwordx4 v[156:159], v0, s[4:7], s99 offen offset:3072 sc1\n"
"buffer_load_dwordx4 v[168:171], v1, s[4:7], s99 offen sc1\n"
"buffer_load_dwordx4 v[172:175], v1, s[4:7], s99 offen offset:1024 sc1\n"
"buffer_load_dwordx4 v[176:179], v1, s[4:7], s99 offen offset:2048 sc1\n"
"buffer_load_dwordx4 v[180:183], v1, s[4:7], s99 offen offset:3072 sc1\n"
"s_waitcnt vmcnt(26)\n"
"; @A G2 a.0\n"
"v_mfma_f32_16x16x32_bf16 v[184:187], a[128:131], v[120:123], 0\n"
"v_mfma_f32_16x16x32_bf16 v[188:191], a[132:135], v[120:123], 0\n"
"v_mfma_f32_16x16x32_bf16 v[192:195], a[136:139], v[120:123], 0\n"
"s_add_u32 s99, s59, 0\n"
"v_mfma_f32_16x16x32_bf16 v[196:199], a[140:143], v[120:123], 0\n"
"s_waitcnt vmcnt(25)\n"
"v_mfma_f32_16x16x32_bf16 v[184:187], a[144:147], v[108:111], v[184:187]\n"
"buffer_load_dwordx4 v[200:203], v0, s[4:7], s99 offen sc1\n"
"v_mfma_f32_16x16x32_bf16 v[188:191], a[148:151], v[108:111], v[188:191]\n"
"v_mfma_f32_16x16x32_bf16 v[192:195], a[152:155], v[108:111], v[192:195]\n"
"v_mfma_f32_16x16x32_bf16 v[196:199], a[156:159], v[108:111], v[196:199]\n"
"s_waitcnt vmcnt(25)\n"
"v_mfma_f32_16x16x32_bf16 v[184:187], a[160:163], v[124:127], v[184:187]\n"
"buffer_load_dwordx4 v[204:207], v0, s[4:7], s99 offen offset:1024 sc1\n"
"v_mfma_f32_16x16x32_bf16 v[188:191], a[164:167], v[124:127], v[188:191]\n"
"v_mfma_f32_16x16x32_bf16 v[192:195], a[168:171], v[124:127], v[192:195]\n"
"v_mfma_f32_16x16x32_bf16 v[196:199], a[172:175], v[124:127], v[196:199]\n"
"s_waitcnt vmcnt(25)\n"
"v_mfma_f32_16x16x32_bf16 v[184:187], a[176:179], v[136:139], v[184:187]\n"
"buffer_load_dwordx4 v[208:211], v0, s[4:7], s99 offen offset:2048 sc1\n"
"v_mfma_f32_16x16x32_bf16 v[188:191], a[180:183], v[136:139], v[188:191]\n"
"v_mfma_f32_16x16x32_bf16 v[192:195], a[184:187], v[136:139], v[192:195]\n"
"v_mfma_f32_16x16x32_bf16 v[196:199], a[188:191], v[136:139], v[196:199]\n"
"s_waitcnt vmcnt(25)\n"
"v_mfma_f32_16x16x32_bf16 v[184:187], a[192:195], v[132:135], v[184:187]\n"
"buffer_load_dwordx4 v[212:215], v0, s[4:7], s99 offen offset:3072 sc1\n"
"v_mfma_f32_16x16x32_bf16 v[188:191], a[196:199], v[132:135], v[188:191]\n"
"v_mfma_f32_16x16x32_bf16 v[192:195], a[200:203], v[132:135], v[192:195]\n"
"v_mfma_f32_16x16x32_bf16 v[196:199], a[204:207], v[132:135], v[196:199]\n"
"s_waitcnt vmcnt(25)\n"
"v_mfma_f32_16x16x32_bf16 v[184:187], a[208:211], v[100:103], v[184:187]\n"
"buffer_load_dwordx4 v[216:219], v1, s[4:7], s99 offen sc1\n"
"v_mfma_f32_16x16x32_bf16 v[188:191], a[212:215], v[100:103], v[188:191]\n"
"v_mfma_f32_16x16x32_bf16 v[192:195], a[216:219], v[100:103], v[192:195]\n"
"v_mfma_f32_16x16x32_bf16 v[196:199], a[220:223], v[100:103], v[196:199]\n"
"s_waitcnt vmcnt(25)\n"
"v_mfma_f32_16x16x32_bf16 v[184:187], a[224:227], v[104:107], v[184:187]\n"
"buffer_load_dwordx4 v[220:223], v1, s[4:7], s99 offen offset:1024 sc1\n"
"v_mfma_f32_16x16x32_bf16 v[188:191], a[228:231], v[104:107], v[188:191]\n"
"v_mfma_f32_16x16x32_bf16 v[192:195], a[232:235], v[104:107], v[192:195]\n"
"v_mfma_f32_16x16x32_bf16 v[196:199], a[236:239], v[104:107], v[196:199]\n"
"s_waitcnt vmcnt(25)\n"
"v_mfma_f32_16x16x32_bf16 v[184:187], a[240:243], v[112:115], v[184:187]\n"
"buffer_load_dwordx4 v[224:227], v1, s[4:7], s99 offen offset:2048 sc1\n"
"v_mfma_f32_16x16x32_bf16 v[188:191], a[244:247], v[112:115], v[188:191]\n"
"v_mfma_f32_16x16x32_bf16 v[192:195], a[248:251], v[112:115], v[192:195]\n"
"v_mfma_f32_16x16x32_bf16 v[196:199], a[252:255], v[112:115], v[196:199]\n"
"s_waitcnt vmcnt(24)\n"
"; @A G2 a.1\n"
"v_mfma_f32_16x16x32_bf16 v[108:111], a[128:131], v[128:131], 0\n"
"s_nop 1\n"
"v_cmp_u_f32_e32 vcc, v184, v184\n"
"v_mfma_f32_16x16x32_bf16 v[112:115], a[132:135], v[128:131], 0\n"
"s_cbranch_vccnz .Lmk_p119s\n"
".Lmk_p119r:\n"
"v_mfma_f32_16x16x32_bf16 v[120:123], a[136:139], v[128:131], 0\n"
"buffer_load_dwordx4 v[228:231], v1, s[4:7], s99 offen offset:3072 sc1\n"
"v_mfma_f32_16x16x32_bf16 v[124:127], a[140:143], v[128:131], 0\n"
"v_mfma_f32_16x16x32_bf16 v[108:111], a[144:147], v[92:95], v[108:111]\n"
"ds_write_b128 v3, v[184:187]\n"
"v_mfma_f32_16x16x32_bf16 v[112:115], a[148:151], v[92:95], v[112:115]\n"
"v_mfma_f32_16x16x32_bf16 v[120:123], a[152:155], v[92:95], v[120:123]\n"
"ds_write_b128 v3, v[188:191] offset:1024\n"
"v_mfma_f32_16x16x32_bf16 v[124:127], a[156:159], v[92:95], v[124:127]\n"
"s_waitcnt vmcnt(23)\n"
"v_mfma_f32_16x16x32_bf16 v[108:111], a[160:163], v[164:167], v[108:111]\n"
"ds_write_b128 v3, v[192:195] offset:2048\n"
"v_mfma_f32_16x16x32_bf16 v[112:115], a[164:167], v[164:167], v[112:115]\n"
"v_mfma_f32_16x16x32_bf16 v[120:123], a[168:171], v[164:167], v[120:123]\n"
"ds_write_b128 v3, v[196:199] offset:3072\n"
"v_mfma_f32_16x16x32_bf16 v[124:127], a[172:175], v[164:167], v[124:127]\n"
"v_mfma_f32_16x16x32_bf16 v[108:111], a[176:179], v[88:91], v[108:111]\n"
"v_mfma_f32_16x16x32_bf16 v[112:115], a[180:183], v[88:91], v[112:115]\n"
"ds_read_b128 v[104:107], v2\n"
"ds_read_b128 v[128:131], v2 offset:1024\n"
"v_mfma_f32_16x16x32_bf16 v[120:123], a[184:187], v[88:91], v[120:123]\n"
"v_mfma_f32_16x16x32_bf16 v[124:127], a[188:191], v[88:91], v[124:127]\n"
"s_waitcnt vmcnt(21)\n"
"v_mfma_f32_16x16x32_bf16 v[108:111], a[192:195], v[96:99], v[108:111]\n"
"v_mfma_f32_16x16x32_bf16 v[112:115], a[196:199], v[96:99], v[112:115]\n"
"v_mfma_f32_16x16x32_bf16 v[120:123], a[200:203], v[96:99], v[120:123]\n"
"s_waitcnt lgkmcnt(0)\n"
"v_lshlrev_b32_e32 v255, 16, v104\n"
"v_mfma_f32_16x16x32_bf16 v[124:127], a[204:207], v[96:99], v[124:127]\n"
"s_waitcnt lgkmcnt(0)\n"
"s_barrier\n"
"v_mfma_f32_16x16x32_bf16 v[108:111], a[208:211], v[144:147], v[108:111]\n"
"v_and_b32_e32 v254, 0xffff0000, v104\n"
"v_lshlrev_b32_e32 v253, 16, v105\n"
"v_mfma_f32_16x16x32_bf16 v[112:115], a[212:215], v[144:147], v[112:115]\n"
"ds_read_b128 v[88:91], v4\n"
"ds_read_b128 v[92:95], v4 offset:1024\n"
"v_mfma_f32_16x16x32_bf16 v[120:123], a[216:219], v[144:147], v[120:123]\n"
"ds_read_b128 v[96:99], v4 offset:2048\n"
"ds_read_b128 v[100:103], v4 offset:3072\n"
"v_mfma_f32_16x16x32_bf16 v[124:127], a[220:223], v[144:147], v[124:127]\n"
"s_waitcnt vmcnt(19)\n"
"v_and_b32_e32 v252, 0xffff0000, v105\n"
"v_mfma_f32_16x16x32_bf16 v[108:111], a[224:227], v[152:155], v[108:111]\n"
"v_lshlrev_b32_e32 v251, 16, v106\n"
"v_and_b32_e32 v250, 0xffff0000, v106\n"
"v_mfma_f32_16x16x32_bf16 v[112:115], a[228:231], v[152:155], v[112:115]\n"
"v_lshlrev_b32_e32 v249, 16, v107\n"
"v_and_b32_e32 v248, 0xffff0000, v107\n"
"v_mfma_f32_16x16x32_bf16 v[120:123], a[232:235], v[152:155], v[120:123]\n"
"v_lshlrev_b32_e32 v247, 16, v128\n"
"v_and_b32_e32 v246, 0xffff0000, v128\n"
"v_mfma_f32_16x16x32_bf16 v[124:127], a[236:239], v[152:155], v[124:127]\n"
"v_lshlrev_b32_e32 v245, 16, v129\n"
"v_and_b32_e32 v244, 0xffff0000, v129\n"
"v_mfma_f32_16x16x32_bf16 v[108:111], a[240:243], v[160:163], v[108:111]\n"
"v_lshlrev_b32_e32 v243, 16, v130\n"
"v_and_b32_e32 v242, 0xffff0000, v130\n"
"v_mfma_f32_16x16x32_bf16 v[112:115], a[244:247], v[160:163], v[112:115]\n"
"v_lshlrev_b32_e32 v241, 16, v131\n"
"v_and_b32_e32 v240, 0xffff0000, v131\n"
"v_mfma_f32_16x16x32_bf16 v[120:123], a[248:251], v[160:163], v[120:123]\n"
"s_waitcnt lgkmcnt(0)\n"
"v_add_f32_e32 v88, v88, v255\n"
"v_mfma_f32_16x16x32_bf16 v[124:127], a[252:255], v[160:163], v[124:127]\n"
"s_waitcnt vmcnt(14)\n"
"v_add_f32_e32 v89, v89, v254\n"
"; @A G2 a.2\n"
"v_mfma_f32_16x16x32_bf16 v[144:147], a[128:131], v[116:119], 0\n"
"v_cmp_u_f32_e32 vcc, v108, v108\n"
"s_add_u32 s99, s52, 0x440000\n"
"v_mfma_f32_16x16x32_bf16 v[152:155], a[132:135], v[116:119], 0\n"
"s_cbranch_vccnz .Lmk_p120s\n"
".Lmk_p120r:\n"
"v_add_f32_e32 v90, v90, v253\n"
"v_mfma_f32_16x16x32_bf16 v[160:163], a[136:139], v[116:119], 0\n"
"buffer_load_dwordx4 v[104:107], v0, s[4:7], s99 offen sc1\n"
"v_mfma_f32_16x16x32_bf16 v[164:167], a[140:143], v[116:119], 0\n"
"v_add_f32_e32 v91, v91, v252\n"
"v_add_f32_e32 v92, v92, v251\n"
"v_mfma_f32_16x16x32_bf16 v[144:147], a[144:147], v[140:143], v[144:147]\n"
"ds_write_b128 v3, v[108:111] offset:6144\n"
"v_mfma_f32_16x16x32_bf16 v[152:155], a[148:151], v[140:143], v[152:155]\n"
"buffer_load_dwordx4 v[108:111], v0, s[4:7], s99 offen offset:1024 sc1\n"
"v_mfma_f32_16x16x32_bf16 v[160:163], a[152:155], v[140:143], v[160:163]\n"
"ds_write_b128 v3, v[112:115] offset:7168\n"
"v_mfma_f32_16x16x32_bf16 v[164:167], a[156:159], v[140:143], v[164:167]\n"
"s_waitcnt vmcnt(14)\n"
"v_add_f32_e32 v93, v93, v250\n"
"v_mfma_f32_16x16x32_bf16 v[144:147], a[160:163], v[148:151], v[144:147]\n"
"ds_write_b128 v3, v[120:123] offset:8192\n"
"v_mfma_f32_16x16x32_bf16 v[152:155], a[164:167], v[148:151], v[152:155]\n"
"buffer_load_dwordx4 v[112:115], v0, s[4:7], s99 offen offset:2048 sc1\n"
"v_mfma_f32_16x16x32_bf16 v[160:163], a[168:171], v[148:151], v[160:163]\n"
"ds_write_b128 v3, v[124:127] offset:9216\n"
"v_mfma_f32_16x16x32_bf16 v[164:167], a[172:175], v[148:151], v[164:167]\n"
"buffer_load_dwordx4 v[116:119], v0, s[4:7], s99 offen offset:3072 sc1\n"
"v_mfma_f32_16x16x32_bf16 v[144:147], a[176:179], v[156:159], v[144:147]\n"
"v_add_f32_e32 v94, v94, v249\n"
"v_add_f32_e32 v95, v95, v248\n"
"v_mfma_f32_16x16x32_bf16 v[152:155], a[180:183], v[156:159], v[152:155]\n"
"buffer_load_dwordx4 v[120:123], v1, s[4:7], s99 offen sc1\n"
"v_mfma_f32_16x16x32_bf16 v[160:163], a[184:187], v[156:159], v[160:163]\n"
"v_add_f32_e32 v96, v96, v247\n"
"v_add_f32_e32 v97, v97, v246\n"
"v_mfma_f32_16x16x32_bf16 v[164:167], a[188:191], v[156:159], v[164:167]\n"
"s_waitcnt vmcnt(15)\n"
"v_add_f32_e32 v98, v98, v245\n"
"v_mfma_f32_16x16x32_bf16 v[144:147], a[192:195], v[168:171], v[144:147]\n"
"buffer_load_dwordx4 v[124:127], v1, s[4:7], s99 offen offset:1024 sc1\n"
"v_mfma_f32_16x16x32_bf16 v[152:155], a[196:199], v[168:171], v[152:155]\n"
"v_add_f32_e32 v99, v99, v244\n"
"v_add_f32_e32 v100, v100, v243\n"
"v_mfma_f32_16x16x32_bf16 v[160:163], a[200:203], v[168:171], v[160:163]\n"
"buffer_load_dwordx4 v[128:131], v1, s[4:7], s99 offen offset:2048 sc1\n"
"v_mfma_f32_16x16x32_bf16 v[164:167], a[204:207], v[168:171], v[164:167]\n"
"v_add_f32_e32 v101, v101, v242\n"
"v_add_f32_e32 v102, v102, v241\n"
"v_mfma_f32_16x16x32_bf16 v[144:147], a[208:211], v[172:175], v[144:147]\n"
"buffer_load_dwordx4 v[132:135], v1, s[4:7], s99 offen offset:3072 sc1\n"
"v_mfma_f32_16x16x32_bf16 v[152:155], a[212:215], v[172:175], v[152:155]\n"
"s_add_u32 s99, s53, 0x440000\n"
"v_add_f32_e32 v103, v103, v240\n"
"v_mfma_f32_16x16x32_bf16 v[160:163], a[216:219], v[172:175], v[160:163]\n"
"buffer_load_dwordx4 v[136:139], v0, s[4:7], s99 offen sc1\n"
"v_mfma_f32_16x16x32_bf16 v[164:167], a[220:223], v[172:175], v[164:167]\n"
"s_waitcnt vmcnt(17)\n"
"v_mfma_f32_16x16x32_bf16 v[144:147], a[224:227], v[176:179], v[144:147]\n"
"s_waitcnt vmcnt(15)\n"
"v_max_u32_e32 v255, v200, v201\n"
"v_mfma_f32_16x16x32_bf16 v[152:155], a[228:231], v[176:179], v[152:155]\n"
"v_max3_u32 v255, v255, v202, v203\n"
"v_max3_u32 v255, v255, v204, v205\n"
"v_mfma_f32_16x16x32_bf16 v[160:163], a[232:235], v[176:179], v[160:163]\n"
"v_max3_u32 v255, v255, v206, v207\n"
"s_waitcnt vmcnt(13)\n"
"v_mfma_f32_16x16x32_bf16 v[164:167], a[236:239], v[176:179], v[164:167]\n"
"v_max3_u32 v255, v255, v208, v209\n"
"v_max3_u32 v255, v255, v210, v211\n"
"v_mfma_f32_16x16x32_bf16 v[144:147], a[240:243], v[180:183], v[144:147]\n"
"v_max3_u32 v255, v255, v212, v213\n"
"v_max3_u32 v255, v255, v214, v215\n"
"v_mfma_f32_16x16x32_bf16 v[152:155], a[244:247], v[180:183], v[152:155]\n"
"v_cmp_eq_u32_e32 vcc, -1, v255\n"
"v_mfma_f32_16x16x32_bf16 v[160:163], a[248:251], v[180:183], v[160:163]\n"
"s_cbranch_vccnz .Lmk_q122s\n"
".Lmk_q122r:\n"
"v_mfma_f32_16x16x32_bf16 v[164:167], a[252:255], v[180:183], v[164:167]\n"
"; @A G2 a.3\n"
"v_mfma_f32_16x16x32_bf16 v[88:91], a[128:131], v[200:203], v[88:91]\n"
"v_cmp_u_f32_e32 vcc, v144, v144\n"
"v_mfma_f32_16x16x32_bf16 v[92:95], a[132:135], v[200:203], v[92:95]\n"
"s_cbranch_vccnz .Lmk_p121s\n"
".Lmk_p121r:\n"
"v_mfma_f32_16x16x32_bf16 v[96:99], a[136:139], v[200:203], v[96:99]\n"
"buffer_load_dwordx4 v[140:143], v0, s[4:7], s99 offen offset:1024 sc1\n"
"v_mfma_f32_16x16x32_bf16 v[100:103], a[140:143], v[200:203], v[100:103]\n"
"v_mfma_f32_16x16x32_bf16 v[88:91], a[144:147], v[204:207], v[88:91]\n"
"ds_write_b128 v3, v[144:147] offset:12288\n"
"v_mfma_f32_16x16x32_bf16 v[92:95], a[148:151], v[204:207], v[92:95]\n"
"buffer_load_dwordx4 v[144:147], v0, s[4:7], s99 offen offset:2048 sc1\n"
"v_mfma_f32_16x16x32_bf16 v[96:99], a[152:155], v[204:207], v[96:99]\n"
"ds_write_b128 v3, v[152:155] offset:13312\n"
"v_mfma_f32_16x16x32_bf16 v[100:103], a[156:159], v[204:207], v[100:103]\n"
"v_mfma_f32_16x16x32_bf16 v[88:91], a[160:163], v[208:211], v[88:91]\n"
"s_waitcnt vmcnt(13)\n"
"v_max_u32_e32 v255, v216, v217\n"
"v_mfma_f32_16x16x32_bf16 v[92:95], a[164:167], v[208:211], v[92:95]\n"
"v_max3_u32 v255, v255, v218, v219\n"
"v_max3_u32 v255, v255, v220, v221\n"
"v_mfma_f32_16x16x32_bf16 v[96:99], a[168:171], v[208:211], v[96:99]\n"
"v_max3_u32 v255, v255, v222, v223\n"
"s_waitcnt vmcnt(11)\n"
"v_mfma_f32_16x16x32_bf16 v[100:103], a[172:175], v[208:211], v[100:103]\n"
"v_max3_u32 v255, v255, v224, v225\n"
"v_max3_u32 v255, v255, v226, v227\n"
"v_mfma_f32_16x16x32_bf16 v[88:91], a[176:179], v[212:215], v[88:91]\n"
"v_max3_u32 v255, v255, v228, v229\n"
"v_max3_u32 v255, v255, v230, v231\n"
"v_mfma_f32_16x16x32_bf16 v[92:95], a[180:183], v[212:215], v[92:95]\n"
"v_cmp_eq_u32_e32 vcc, -1, v255\n"
"v_mfma_f32_16x16x32_bf16 v[96:99], a[184:187], v[212:215], v[96:99]\n"
"s_cbranch_vccnz .Lmk_q123s\n"
".Lmk_q123r:\n"
"v_mfma_f32_16x16x32_bf16 v[100:103], a[188:191], v[212:215], v[100:103]\n"
"v_mfma_f32_16x16x32_bf16 v[88:91], a[192:195], v[216:219], v[88:91]\n"
"ds_write_b128 v3, v[160:163] offset:14336\n"
"v_mfma_f32_16x16x32_bf16 v[92:95], a[196:199], v[216:219], v[92:95]\n"
"buffer_load_dwordx4 v[148:151], v0, s[4:7], s99 offen offset:3072 sc1\n"
"v_mfma_f32_16x16x32_bf16 v[96:99], a[200:203], v[216:219], v[96:99]\n"
"ds_write_b128 v3, v[164:167] offset:15360\n"
"v_mfma_f32_16x16x32_bf16 v[100:103], a[204:207], v[216:219], v[100:103]\n"
"buffer_load_dwordx4 v[152:155], v1, s[4:7], s99 offen sc1\n"
"v_mfma_f32_16x16x32_bf16 v[88:91], a[208:211], v[220:223], v[88:91]\n"
"v_mfma_f32_16x16x32_bf16 v[92:95], a[212:215], v[220:223], v[92:95]\n"
"s_waitcnt lgkmcnt(0)\n"
"s_barrier\n"
"v_mfma_f32_16x16x32_bf16 v[96:99], a[216:219], v[220:223], v[96:99]\n"
"buffer_load_dwordx4 v[160:163], v1, s[4:7], s99 offen offset:1024 sc1\n"
"v_mfma_f32_16x16x32_bf16 v[100:103], a[220:223], v[220:223], v[100:103]\n"
"ds_read_b128 v[156:159], v5\n"
"v_mfma_f32_16x16x32_bf16 v[88:91], a[224:227], v[224:227], v[88:91]\n"
"buffer_load_dwordx4 v[168:171], v1, s[4:7], s99 offen offset:2048 sc1\n"
"v_mfma_f32_16x16x32_bf16 v[92:95], a[228:231], v[224:227], v[92:95]\n"
"ds_read_b128 v[184:187], v6\n"
"ds_read_b128 v[164:167], v5 offset:1024\n"
"v_mfma_f32_16x16x32_bf16 v[96:99], a[232:235], v[224:227], v[96:99]\n"
"buffer_load_dwordx4 v[172:175], v1, s[4:7], s99 offen offset:3072 sc1\n"
"v_mfma_f32_16x16x32_bf16 v[100:103], a[236:239], v[224:227], v[100:103]\n"
"ds_read_b128 v[188:191], v6 offset:1024\n"
"v_mfma_f32_16x16x32_bf16 v[88:91], a[240:243], v[228:231], v[88:91]\n"
"ds_read_b128 v[176:179], v5 offset:2048\n"
"ds_read_b128 v[192:195], v6 offset:2048\n"
"v_mfma_f32_16x16x32_bf16 v[92:95], a[244:247], v[228:231], v[92:95]\n"
"ds_read_b128 v[180:183], v5 offset:3072\n"
"ds_read_b128 v[196:199], v6 offset:3072\n"
"v_mfma_f32_16x16x32_bf16 v[96:99], a[248:251], v[228:231], v[96:99]\n"
"v_mfma_f32_16x16x32_bf16 v[100:103], a[252:255], v[228:231], v[100:103]\n"
"s_waitcnt lgkmcnt(4)\n"
"v_add_f32_e32 v156, v156, v184\n"
"v_add_f32_e32 v157, v157, v185\n"
"v_add_f32_e32 v158, v158, v186\n"
"v_add_f32_e32 v159, v159, v187\n"
"v_add_f32_e32 v164, v164, v188\n"
"v_add_f32_e32 v165, v165, v189\n"
"v_add_f32_e32 v166, v166, v190\n"
"v_add_f32_e32 v167, v167, v191\n"
"s_waitcnt lgkmcnt(0)\n"
"v_add_f32_e32 v176, v176, v192\n"
"v_add_f32_e32 v177, v177, v193\n"
"v_add_f32_e32 v178, v178, v194\n"
"v_add_f32_e32 v179, v179, v195\n"
"v_add_f32_e32 v180, v180, v196\n"
"v_add_f32_e32 v181, v181, v197\n"
"v_add_f32_e32 v182, v182, v198\n"
"v_add_f32_e32 v183, v183, v199\n"
"s_branch .Lmk_loop\n"
".Lmk_f1s:\n"
";\n"
"s_mov_b32 s1, 0x10000\n"
".Lmk_f1l:\n"
"buffer_load_dwordx2 v[250:251], v12, s[4:7], s99 offen sc1\n"
"s_waitcnt vmcnt(0)\n"
"v_cmp_ne_u64_e32 vcc, v[250:251], v[18:19]\n"
"s_cbranch_vccz .Lmk_f1r\n"
"s_sub_u32 s1, s1, 1\n"
"s_cbranch_scc0 .Lmk_f1l\n"
"s_branch .Lmk_f1r\n"
".Lmk_f2s:\n"
";\n"
"s_mov_b32 s1, 0x10000\n"
".Lmk_f2l:\n"
"buffer_load_dwordx2 v[254:255], v12, s[4:7], s98 offen sc1\n"
"s_waitcnt vmcnt(0)\n"
"v_cmp_ne_u64_e32 vcc, v[254:255], v[18:19]\n"
"s_cbranch_vccz .Lmk_f2r\n"
"s_sub_u32 s1, s1, 1\n"
"s_cbranch_scc0 .Lmk_f2l\n"
"s_branch .Lmk_f2r\n"
".Lmk_f5s:\n"
";\n"
"s_mov_b32 s1, 0x10000\n"
".Lmk_f5l:\n"
"buffer_load_dwordx2 v[252:253], v41, s[4:7], s98 offen sc1\n"
"s_waitcnt vmcnt(0)\n"
"v_cmp_ne_u64_e32 vcc, v[252:253], v[18:19]\n"
"s_cbranch_vccz .Lmk_f5r\n"
"s_sub_u32 s1, s1, 1\n"
"s_cbranch_scc0 .Lmk_f5l\n"
"s_branch .Lmk_f5r\n"
".Lmk_w7s:\n"
"buffer_store_dwordx4 a[24:27], v0, s[4:7], s98 offen sc1\n"
"s_branch .Lmk_w7r\n"
".Lmk_w8s:\n"
"buffer_store_dwordx4 a[28:31], v0, s[4:7], s97 offen sc1\n"
"s_branch .Lmk_w8r\n"
".Lmk_w9s:\n"
"buffer_store_dwordx4 a[32:35], v0, s[4:7], s98 offen sc1\n"
"s_branch .Lmk_w9r\n"
".Lmk_w10s:\n"
"buffer_store_dwordx4 a[36:39], v0, s[4:7], s97 offen sc1\n"
"s_branch .Lmk_w10r\n"
".Lmk_w11s:\n"
"buffer_store_dwordx4 a[40:43], v0, s[4:7], s98 offen sc1\n"
"s_branch .Lmk_w11r\n"
".Lmk_w12s:\n"
"buffer_store_dwordx4 a[44:47], v0, s[4:7], s98 offen sc1\n"
"s_branch .Lmk_w12r\n"
".Lmk_f6s:\n"
";\n"
"s_mov_b32 s1, 0x10000\n"
".Lmk_f6l:\n"
"buffer_load_dwordx2 v[254:255], v13, s[4:7], s97 offen sc1\n"
"s_waitcnt vmcnt(0)\n"
"v_cmp_ne_u64_e32 vcc, v[254:255], v[18:19]\n"
"s_cbranch_vccz .Lmk_f6r\n"
"s_sub_u32 s1, s1, 1\n"
"s_cbranch_scc0 .Lmk_f6l\n"
"s_branch .Lmk_f6r\n"
".Lmk_f3s:\n"
";\n"
"s_mov_b32 s1, 0x10000\n"
".Lmk_f3l:\n"
"buffer_load_dwordx2 v[250:251], off, s[4:7], s96 sc1\n"
"s_waitcnt vmcnt(0)\n"
"v_cmp_ne_u64_e32 vcc, v[250:251], v[18:19]\n"
"s_cbranch_vccz .Lmk_f3r\n"
"s_sub_u32 s1, s1, 1\n"
"s_cbranch_scc0 .Lmk_f3l\n"
"s_branch .Lmk_f3r\n"
".Lmk_x22s:\n"
"s_nop 3\n"
"v_mov_b32_e32 v36, 0xff800000\n"
"v_mov_b32_e32 v37, 0xff800000\n"
"v_mov_b32_e32 v38, 0xff800000\n"
"v_mov_b32_e32 v39, 0xff800000\n"
"v_mov_b32_e32 v60, 0xff800000\n"
"v_mov_b32_e32 v61, 0xff800000\n"
"v_mov_b32_e32 v62, 0xff800000\n"
"v_mov_b32_e32 v63, 0xff800000\n"
"s_nop 1\n"
"s_branch .Lmk_x22r\n"
".Lmk_x23s:\n"
"s_nop 3\n"
"v_mov_b32_e32 v36, 0xff800000\n"
"v_mov_b32_e32 v37, 0xff800000\n"
"v_mov_b32_e32 v38, 0xff800000\n"
"v_mov_b32_e32 v39, 0xff800000\n"
"v_mov_b32_e32 v60, 0xff800000\n"
"v_mov_b32_e32 v61, 0xff800000\n"
"v_mov_b32_e32 v62, 0xff800000\n"
"v_mov_b32_e32 v63, 0xff800000\n"
"s_nop 1\n"
"s_branch .Lmk_x23r\n"
".Lmk_x24s:\n"
"s_nop 3\n"
"v_mov_b32_e32 v36, 0xff800000\n"
"v_mov_b32_e32 v37, 0xff800000\n"
"v_mov_b32_e32 v38, 0xff800000\n"
"v_mov_b32_e32 v39, 0xff800000\n"
"v_mov_b32_e32 v60, 0xff800000\n"
"v_mov_b32_e32 v61, 0xff800000\n"
"v_mov_b32_e32 v62, 0xff800000\n"
"v_mov_b32_e32 v63, 0xff800000\n"
"s_nop 1\n"
"s_branch .Lmk_x24r\n"
".Lmk_x25s:\n"
"s_nop 3\n"
"v_mov_b32_e32 v36, 0xff800000\n"
"v_mov_b32_e32 v37, 0xff800000\n"
"v_mov_b32_e32 v38, 0xff800000\n"
"v_mov_b32_e32 v39, 0xff800000\n"
"v_mov_b32_e32 v60, 0xff800000\n"
"v_mov_b32_e32 v61, 0xff800000\n"
"v_mov_b32_e32 v62, 0xff800000\n"
"v_mov_b32_e32 v63, 0xff800000\n"
"s_nop 1\n"
"s_branch .Lmk_x25r\n"
".Lmk_f18s:\n"
";\n"
"s_mov_b32 s1, 0x10000\n"
".Lmk_f18l:\n"
"buffer_load_dwordx2 v[238:239], off, s[4:7], s97 sc1\n"
"s_waitcnt vmcnt(0)\n"
"v_cmp_ne_u64_e32 vcc, v[238:239], v[18:19]\n"
"s_cbranch_vccz .Lmk_f18r\n"
"s_sub_u32 s1, s1, 1\n"
"s_cbranch_scc0 .Lmk_f18l\n"
"s_branch .Lmk_f18r\n"
".Lmk_b20:\n"
"s_cmp_lg_u32 s2, 0\n"
"s_cbranch_scc1 .Lmk_k26\n"
"s_mov_b32 s2, 1\n"
"s_waitcnt vmcnt(0) lgkmcnt(0)\n"
"s_nop 7\n"
"s_mov_b32 s97, 11\n"
"v_add_u32_e32 v248, 0x3800, v15\n"
"v_add_u32_e32 v247, 0x3800, v252\n"
"ds_read_b128 v[32:35], v15 offset:24576\n"
"ds_read_b128 v[36:39], v252 offset:24832\n"
"s_waitcnt lgkmcnt(0)\n"
"v_mfma_f32_16x16x32_bf16 v[40:43], v[32:35], v[24:27], 0\n"
"v_mfma_f32_16x16x32_bf16 v[48:51], v[32:35], v[28:31], 0\n"
"v_mfma_f32_16x16x32_bf16 v[32:35], v[36:39], v[24:27], 0\n"
"v_mfma_f32_16x16x32_bf16 v[52:55], v[36:39], v[28:31], 0\n"
".Lmk_b20l:\n"
"ds_read_b128 v[36:39], v248 offset:8192\n"
"ds_read_b128 v[56:59], v247 offset:8448\n"
"s_cmp_gt_i32 s78, s97\n"
"s_cselect_b32 s96, 0xff800000, 0\n"
"v_mov_b32_e32 v60, s96\n"
"v_mov_b32_e32 v61, s96\n"
"v_mov_b32_e32 v62, s96\n"
"v_mov_b32_e32 v63, s96\n"
"v_subrev_u32_e32 v248, 0x800, v248\n"
"v_subrev_u32_e32 v247, 0x800, v247\n"
"s_waitcnt lgkmcnt(0)\n"
"v_mfma_f32_16x16x32_bf16 v[64:67], v[36:39], v[24:27], v[60:63]\n"
"v_mfma_f32_16x16x32_bf16 v[72:75], v[36:39], v[28:31], v[60:63]\n"
"v_mfma_f32_16x16x32_bf16 v[36:39], v[56:59], v[24:27], v[60:63]\n"
"v_mfma_f32_16x16x32_bf16 v[76:79], v[56:59], v[28:31], v[60:63]\n"
"s_nop 7\n"
"s_cmp_lg_u32 s97, 4\n"
"s_cbranch_scc1 .Lmk_b20m\n"
"v_cndmask_b32_e64 v64, v40, v64, s[20:21]\n"
"v_cndmask_b32_e64 v65, v41, v65, s[22:23]\n"
"v_cndmask_b32_e64 v66, v42, v66, s[24:25]\n"
"v_cndmask_b32_e64 v67, v43, v67, s[26:27]\n"
"v_cndmask_b32_e64 v36, v32, v36, s[28:29]\n"
"v_cndmask_b32_e64 v37, v33, v37, s[30:31]\n"
"v_cndmask_b32_e64 v38, v34, v38, s[32:33]\n"
"v_cndmask_b32_e64 v39, v35, v39, s[34:35]\n"
"v_cndmask_b32_e64 v72, v48, v72, s[36:37]\n"
"v_cndmask_b32_e64 v73, v49, v73, s[38:39]\n"
"v_cndmask_b32_e64 v74, v50, v74, s[40:41]\n"
"v_cndmask_b32_e64 v75, v51, v75, s[42:43]\n"
"v_cndmask_b32_e64 v76, v52, v76, s[44:45]\n"
"v_cndmask_b32_e64 v77, v53, v77, s[46:47]\n"
"v_cndmask_b32_e64 v78, v54, v78, s[48:49]\n"
"v_cndmask_b32_e64 v79, v55, v79, s[50:51]\n"
".Lmk_b20m:\n"
"v_max3_f32 v255, v255, v64, v65\n"
"v_max3_f32 v254, v254, v72, v73\n"
"v_max3_f32 v255, v255, v66, v67\n"
"v_max3_f32 v254, v254, v74, v75\n"
"v_max3_f32 v255, v255, v36, v37\n"
"v_max3_f32 v254, v254, v76, v77\n"
"v_max3_f32 v255, v255, v38, v39\n"
"v_max3_f32 v254, v254, v78, v79\n"
"s_sub_u32 s97, s97, 1\n"
"s_cmp_ge_i32 s97, 4\n"
"s_cbranch_scc1 .Lmk_b20l\n"
"v_mov_b32_e32 v248, v255\n"
"v_mov_b32_e32 v247, v254\n"
"s_nop 0\n"
"v_permlane16_swap_b32_e32 v255, v248\n"
"v_permlane16_swap_b32_e32 v254, v247\n"
"v_max_f32_e32 v255, v255, v248\n"
"v_max_f32_e32 v254, v254, v247\n"
"v_mov_b32_e32 v248, v255\n"
"v_mov_b32_e32 v247, v254\n"
"s_nop 0\n"
"v_permlane32_swap_b32_e32 v255, v248\n"
"v_permlane32_swap_b32_e32 v254, v247\n"
"v_max_f32_e32 v255, v255, v248\n"
"v_max_f32_e32 v254, v254, v247\n"
"v_mov_b32_e32 v250, v255\n"
"v_mov_b32_e32 v249, v254\n"
"s_branch .Lmk_a19\n"
".Lmk_x29s:\n"
"s_nop 3\n"
"v_mov_b32_e32 v116, 0xff800000\n"
"v_mov_b32_e32 v117, 0xff800000\n"
"v_mov_b32_e32 v118, 0xff800000\n"
"v_mov_b32_e32 v119, 0xff800000\n"
"v_mov_b32_e32 v120, 0xff800000\n"
"v_mov_b32_e32 v121, 0xff800000\n"
"v_mov_b32_e32 v122, 0xff800000\n"
"v_mov_b32_e32 v123, 0xff800000\n"
"s_nop 1\n"
"s_branch .Lmk_x29r\n"
".Lmk_x30s:\n"
"s_nop 3\n"
"v_mov_b32_e32 v116, 0xff800000\n"
"v_mov_b32_e32 v117, 0xff800000\n"
"v_mov_b32_e32 v118, 0xff800000\n"
"v_mov_b32_e32 v119, 0xff800000\n"
"v_mov_b32_e32 v120, 0xff800000\n"
"v_mov_b32_e32 v121, 0xff800000\n"
"v_mov_b32_e32 v122, 0xff800000\n"
"v_mov_b32_e32 v123, 0xff800000\n"
"s_nop 1\n"
"s_branch .Lmk_x30r\n"
".Lmk_x32s:\n"
"s_nop 3\n"
"v_mov_b32_e32 v116, 0xff800000\n"
"v_mov_b32_e32 v117, 0xff800000\n"
"v_mov_b32_e32 v118, 0xff800000\n"
"v_mov_b32_e32 v119, 0xff800000\n"
"v_mov_b32_e32 v120, 0xff800000\n"
"v_mov_b32_e32 v121, 0xff800000\n"
"v_mov_b32_e32 v122, 0xff800000\n"
"v_mov_b32_e32 v123, 0xff800000\n"
"s_nop 1\n"
"s_branch .Lmk_x32r\n"
".Lmk_f4s:\n"
";\n"
"s_mov_b32 s1, 0x10000\n"
".Lmk_f4l:\n"
"buffer_load_dwordx2 v[246:247], v12, s[4:7], s97 offen sc1\n"
"s_waitcnt vmcnt(0)\n"
"v_cmp_ne_u64_e32 vcc, v[246:247], v[18:19]\n"
"s_cbranch_vccz .Lmk_f4r\n"
"s_sub_u32 s1, s1, 1\n"
"s_cbranch_scc0 .Lmk_f4l\n"
"s_branch .Lmk_f4r\n"
".Lmk_b28:\n"
"s_cmp_lg_u32 s2, 0\n"
"s_cbranch_scc1 .Lmk_k33\n"
"s_mov_b32 s2, 1\n"
"s_waitcnt vmcnt(0) lgkmcnt(0)\n"
"s_nop 7\n"
"s_mov_b32 s97, 7\n"
"v_add_u32_e32 v248, 0x3800, v15\n"
"v_add_u32_e32 v247, 0x3800, v252\n"
"ds_read_b128 v[180:183], v15 offset:16384\n"
"ds_read_b128 v[184:187], v252 offset:16640\n"
"s_waitcnt lgkmcnt(0)\n"
"v_mfma_f32_16x16x32_bf16 v[188:191], v[180:183], v[92:95], 0\n"
"v_mfma_f32_16x16x32_bf16 v[192:195], v[180:183], v[96:99], 0\n"
"v_mfma_f32_16x16x32_bf16 v[180:183], v[184:187], v[92:95], 0\n"
"v_mfma_f32_16x16x32_bf16 v[220:223], v[184:187], v[96:99], 0\n"
".Lmk_b28l:\n"
"ds_read_b128 v[184:187], v248\n"
"ds_read_b128 v[224:227], v247 offset:256\n"
"s_cmp_gt_i32 s78, s97\n"
"s_cselect_b32 s96, 0xff800000, 0\n"
"v_mov_b32_e32 v228, s96\n"
"v_mov_b32_e32 v229, s96\n"
"v_mov_b32_e32 v230, s96\n"
"v_mov_b32_e32 v231, s96\n"
"v_subrev_u32_e32 v248, 0x800, v248\n"
"v_subrev_u32_e32 v247, 0x800, v247\n"
"s_waitcnt lgkmcnt(0)\n"
"v_mfma_f32_16x16x32_bf16 v[232:235], v[184:187], v[92:95], v[228:231]\n"
"v_mfma_f32_16x16x32_bf16 v[236:239], v[184:187], v[96:99], v[228:231]\n"
"v_mfma_f32_16x16x32_bf16 v[184:187], v[224:227], v[92:95], v[228:231]\n"
"v_mfma_f32_16x16x32_bf16 v[240:243], v[224:227], v[96:99], v[228:231]\n"
"s_nop 7\n"
"s_cmp_lg_u32 s97, 0\n"
"s_cbranch_scc1 .Lmk_b28m\n"
"v_cndmask_b32_e64 v232, v188, v232, s[20:21]\n"
"v_cndmask_b32_e64 v233, v189, v233, s[22:23]\n"
"v_cndmask_b32_e64 v234, v190, v234, s[24:25]\n"
"v_cndmask_b32_e64 v235, v191, v235, s[26:27]\n"
"v_cndmask_b32_e64 v184, v180, v184, s[28:29]\n"
"v_cndmask_b32_e64 v185, v181, v185, s[30:31]\n"
"v_cndmask_b32_e64 v186, v182, v186, s[32:33]\n"
"v_cndmask_b32_e64 v187, v183, v187, s[34:35]\n"
"v_cndmask_b32_e64 v236, v192, v236, s[36:37]\n"
"v_cndmask_b32_e64 v237, v193, v237, s[38:39]\n"
"v_cndmask_b32_e64 v238, v194, v238, s[40:41]\n"
"v_cndmask_b32_e64 v239, v195, v239, s[42:43]\n"
"v_cndmask_b32_e64 v240, v220, v240, s[44:45]\n"
"v_cndmask_b32_e64 v241, v221, v241, s[46:47]\n"
"v_cndmask_b32_e64 v242, v222, v242, s[48:49]\n"
"v_cndmask_b32_e64 v243, v223, v243, s[50:51]\n"
".Lmk_b28m:\n"
"v_max3_f32 v255, v255, v232, v233\n"
"v_max3_f32 v254, v254, v236, v237\n"
"v_max3_f32 v255, v255, v234, v235\n"
"v_max3_f32 v254, v254, v238, v239\n"
"v_max3_f32 v255, v255, v184, v185\n"
"v_max3_f32 v254, v254, v240, v241\n"
"v_max3_f32 v255, v255, v186, v187\n"
"v_max3_f32 v254, v254, v242, v243\n"
"s_sub_u32 s97, s97, 1\n"
"s_cmp_ge_i32 s97, 0\n"
"s_cbranch_scc1 .Lmk_b28l\n"
"v_mov_b32_e32 v248, v255\n"
"v_mov_b32_e32 v247, v254\n"
"s_nop 0\n"
"v_permlane16_swap_b32_e32 v255, v248\n"
"v_permlane16_swap_b32_e32 v254, v247\n"
"v_max_f32_e32 v255, v255, v248\n"
"v_max_f32_e32 v254, v254, v247\n"
"v_mov_b32_e32 v248, v255\n"
"v_mov_b32_e32 v247, v254\n"
"s_nop 0\n"
"v_permlane32_swap_b32_e32 v255, v248\n"
"v_permlane32_swap_b32_e32 v254, v247\n"
"v_max_f32_e32 v255, v255, v248\n"
"v_max_f32_e32 v254, v254, v247\n"
"v_mov_b32_e32 v250, v255\n"
"v_mov_b32_e32 v249, v254\n"
"s_branch .Lmk_a27\n"
".Lmk_p34s:\n"
";\n"
"s_mov_b32 s1, 0x10000\n"
".Lmk_p34l:\n"
"s_add_u32 s98, s56, 0x40000\n"
"buffer_load_dwordx4 v[176:179], v0, s[4:7], s98 offen sc1\n"
"buffer_load_dwordx4 v[136:139], v0, s[4:7], s98 offen offset:1024 sc1\n"
"buffer_load_dwordx4 v[120:123], v0, s[4:7], s98 offen offset:2048 sc1\n"
"buffer_load_dwordx4 v[108:111], v0, s[4:7], s98 offen offset:3072 sc1\n"
"buffer_load_dwordx4 v[124:127], v1, s[4:7], s98 offen sc1\n"
"buffer_load_dwordx4 v[144:147], v1, s[4:7], s98 offen offset:1024 sc1\n"
"buffer_load_dwordx4 v[140:143], v1, s[4:7], s98 offen offset:2048 sc1\n"
"buffer_load_dwordx4 v[132:135], v1, s[4:7], s98 offen offset:3072 sc1\n"
"s_add_u32 s98, s57, 0x40000\n"
"buffer_load_dwordx4 v[216:219], v0, s[4:7], s98 offen sc1\n"
"buffer_load_dwordx4 v[212:215], v0, s[4:7], s98 offen offset:1024 sc1\n"
"buffer_load_dwordx4 v[208:211], v0, s[4:7], s98 offen offset:2048 sc1\n"
"buffer_load_dwordx4 v[100:103], v0, s[4:7], s98 offen offset:3072 sc1\n"
"buffer_load_dwordx4 v[196:199], v1, s[4:7], s98 offen sc1\n"
"buffer_load_dwordx4 v[104:107], v1, s[4:7], s98 offen offset:1024 sc1\n"
"buffer_load_dwordx4 v[112:115], v1, s[4:7], s98 offen offset:2048 sc1\n"
"buffer_load_dwordx4 v[128:131], v1, s[4:7], s98 offen offset:3072 sc1\n"
"s_add_u32 s98, s59, 0x40000\n"
"buffer_load_dwordx4 v[156:159], v0, s[4:7], s98 offen sc1\n"
"buffer_load_dwordx4 v[160:163], v0, s[4:7], s98 offen offset:1024 sc1\n"
"buffer_load_dwordx4 v[164:167], v0, s[4:7], s98 offen offset:2048 sc1\n"
"buffer_load_dwordx4 v[116:119], v0, s[4:7], s98 offen offset:3072 sc1\n"
"s_waitcnt vmcnt(0)\n"
"v_max_u32_e32 v255, v176, v177\n"
"v_max3_u32 v255, v255, v178, v179\n"
"v_max3_u32 v255, v255, v136, v137\n"
"v_max3_u32 v255, v255, v138, v139\n"
"v_max3_u32 v255, v255, v120, v121\n"
"v_max3_u32 v255, v255, v122, v123\n"
"v_max3_u32 v255, v255, v108, v109\n"
"v_max3_u32 v255, v255, v110, v111\n"
"v_max3_u32 v255, v255, v124, v125\n"
"v_max3_u32 v255, v255, v126, v127\n"
"v_max3_u32 v255, v255, v144, v145\n"
"v_max3_u32 v255, v255, v146, v147\n"
"v_max3_u32 v255, v255, v140, v141\n"
"v_max3_u32 v255, v255, v142, v143\n"
"v_max3_u32 v255, v255, v132, v133\n"
"v_max3_u32 v255, v255, v134, v135\n"
"v_cmp_eq_u32_e32 vcc, -1, v255\n"
"s_cbranch_vccz .Lmk_p34k\n"
"s_sub_u32 s1, s1, 1\n"
"s_cbranch_scc0 .Lmk_p34l\n"
".Lmk_p34k:\n"
"v_mfma_f32_16x16x32_bf16 v[88:91], a[128:131], v[176:179], 0\n"
"v_mfma_f32_16x16x32_bf16 v[92:95], a[132:135], v[176:179], 0\n"
"v_mfma_f32_16x16x32_bf16 v[96:99], a[136:139], v[176:179], 0\n"
"v_mfma_f32_16x16x32_bf16 v[152:155], a[140:143], v[176:179], 0\n"
"v_mfma_f32_16x16x32_bf16 v[88:91], a[144:147], v[136:139], v[88:91]\n"
"v_mfma_f32_16x16x32_bf16 v[92:95], a[148:151], v[136:139], v[92:95]\n"
"v_mfma_f32_16x16x32_bf16 v[96:99], a[152:155], v[136:139], v[96:99]\n"
"v_mfma_f32_16x16x32_bf16 v[152:155], a[156:159], v[136:139], v[152:155]\n"
"v_mfma_f32_16x16x32_bf16 v[88:91], a[160:163], v[120:123], v[88:91]\n"
"v_mfma_f32_16x16x32_bf16 v[92:95], a[164:167], v[120:123], v[92:95]\n"
"v_mfma_f32_16x16x32_bf16 v[96:99], a[168:171], v[120:123], v[96:99]\n"
"v_mfma_f32_16x16x32_bf16 v[152:155], a[172:175], v[120:123], v[152:155]\n"
"v_mfma_f32_16x16x32_bf16 v[88:91], a[176:179], v[108:111], v[88:91]\n"
"v_mfma_f32_16x16x32_bf16 v[92:95], a[180:183], v[108:111], v[92:95]\n"
"v_mfma_f32_16x16x32_bf16 v[96:99], a[184:187], v[108:111], v[96:99]\n"
"v_mfma_f32_16x16x32_bf16 v[152:155], a[188:191], v[108:111], v[152:155]\n"
"v_mfma_f32_16x16x32_bf16 v[88:91], a[192:195], v[124:127], v[88:91]\n"
"v_mfma_f32_16x16x32_bf16 v[92:95], a[196:199], v[124:127], v[92:95]\n"
"v_mfma_f32_16x16x32_bf16 v[96:99], a[200:203], v[124:127], v[96:99]\n"
"v_mfma_f32_16x16x32_bf16 v[152:155], a[204:207], v[124:127], v[152:155]\n"
"v_mfma_f32_16x16x32_bf16 v[88:91], a[208:211], v[144:147], v[88:91]\n"
"v_mfma_f32_16x16x32_bf16 v[92:95], a[212:215], v[144:147], v[92:95]\n"
"v_mfma_f32_16x16x32_bf16 v[96:99], a[216:219], v[144:147], v[96:99]\n"
"v_mfma_f32_16x16x32_bf16 v[152:155], a[220:223], v[144:147], v[152:155]\n"
"v_mfma_f32_16x16x32_bf16 v[88:91], a[224:227], v[140:143], v[88:91]\n"
"v_mfma_f32_16x16x32_bf16 v[92:95], a[228:231], v[140:143], v[92:95]\n"
"v_mfma_f32_16x16x32_bf16 v[96:99], a[232:235], v[140:143], v[96:99]\n"
"v_mfma_f32_16x16x32_bf16 v[152:155], a[236:239], v[140:143], v[152:155]\n"
"v_mfma_f32_16x16x32_bf16 v[88:91], a[240:243], v[132:135], v[88:91]\n"
"v_mfma_f32_16x16x32_bf16 v[92:95], a[244:247], v[132:135], v[92:95]\n"
"v_mfma_f32_16x16x32_bf16 v[96:99], a[248:251], v[132:135], v[96:99]\n"
"v_mfma_f32_16x16x32_bf16 v[152:155], a[252:255], v[132:135], v[152:155]\n"
"s_add_u32 s98, s58, 0x40000\n"
"v_mov_b32_e32 v255, 0x80000000\n"
"buffer_load_dwordx4 v[176:179], v0, s[4:7], s98 offen sc1\n"
"buffer_load_dwordx4 v[136:139], v0, s[4:7], s98 offen offset:1024 sc1\n"
"buffer_load_dwordx4 v[120:123], v0, s[4:7], s98 offen offset:2048 sc1\n"
"buffer_load_dwordx4 v[108:111], v0, s[4:7], s98 offen offset:3072 sc1\n"
"buffer_store_dword v255, v255, s[4:7], 0 offen\n"
"buffer_store_dword v255, v255, s[4:7], 0 offen\n"
"buffer_load_dwordx4 v[124:127], v1, s[4:7], s98 offen sc1\n"
"buffer_load_dwordx4 v[144:147], v1, s[4:7], s98 offen offset:1024 sc1\n"
"buffer_load_dwordx4 v[140:143], v1, s[4:7], s98 offen offset:2048 sc1\n"
"buffer_load_dwordx4 v[132:135], v1, s[4:7], s98 offen offset:3072 sc1\n"
"buffer_store_dword v255, v255, s[4:7], 0 offen\n"
"buffer_store_dword v255, v255, s[4:7], 0 offen\n"
"buffer_store_dword v255, v255, s[4:7], 0 offen\n"
"buffer_store_dword v255, v255, s[4:7], 0 offen\n"
"s_nop 7\n"
"s_branch .Lmk_p34r\n"
".Lmk_p35s:\n"
";\n"
"s_mov_b32 s1, 0x10000\n"
".Lmk_p35l:\n"
"s_add_u32 s99, s57, 0x40000\n"
"buffer_load_dwordx4 v[156:159], v0, s[4:7], s99 offen sc1\n"
"buffer_load_dwordx4 v[160:163], v0, s[4:7], s99 offen offset:1024 sc1\n"
"buffer_load_dwordx4 v[164:167], v0, s[4:7], s99 offen offset:2048 sc1\n"
"buffer_load_dwordx4 v[116:119], v0, s[4:7], s99 offen offset:3072 sc1\n"
"buffer_load_dwordx4 v[180:183], v1, s[4:7], s99 offen sc1\n"
"buffer_load_dwordx4 v[88:91], v1, s[4:7], s99 offen offset:1024 sc1\n"
"buffer_load_dwordx4 v[92:95], v1, s[4:7], s99 offen offset:2048 sc1\n"
"buffer_load_dwordx4 v[100:103], v1, s[4:7], s99 offen offset:3072 sc1\n"
"s_add_u32 s99, s58, 0x40000\n"
"buffer_load_dwordx4 v[176:179], v0, s[4:7], s99 offen sc1\n"
"buffer_load_dwordx4 v[136:139], v0, s[4:7], s99 offen offset:1024 sc1\n"
"buffer_load_dwordx4 v[120:123], v0, s[4:7], s99 offen offset:2048 sc1\n"
"buffer_load_dwordx4 v[108:111], v0, s[4:7], s99 offen offset:3072 sc1\n"
"buffer_load_dwordx4 v[124:127], v1, s[4:7], s99 offen sc1\n"
"buffer_load_dwordx4 v[144:147], v1, s[4:7], s99 offen offset:1024 sc1\n"
"buffer_load_dwordx4 v[140:143], v1, s[4:7], s99 offen offset:2048 sc1\n"
"buffer_load_dwordx4 v[132:135], v1, s[4:7], s99 offen offset:3072 sc1\n"
"s_waitcnt vmcnt(0)\n"
"v_max_u32_e32 v255, v156, v157\n"
"v_max3_u32 v255, v255, v158, v159\n"
"v_max3_u32 v255, v255, v160, v161\n"
"v_max3_u32 v255, v255, v162, v163\n"
"v_max3_u32 v255, v255, v164, v165\n"
"v_max3_u32 v255, v255, v166, v167\n"
"v_max3_u32 v255, v255, v116, v117\n"
"v_max3_u32 v255, v255, v118, v119\n"
"v_max3_u32 v255, v255, v180, v181\n"
"v_max3_u32 v255, v255, v182, v183\n"
"v_max3_u32 v255, v255, v88, v89\n"
"v_max3_u32 v255, v255, v90, v91\n"
"v_max3_u32 v255, v255, v92, v93\n"
"v_max3_u32 v255, v255, v94, v95\n"
"v_max3_u32 v255, v255, v100, v101\n"
"v_max3_u32 v255, v255, v102, v103\n"
"v_cmp_eq_u32_e32 vcc, -1, v255\n"
"s_cbranch_vccz .Lmk_p35k\n"
"s_sub_u32 s1, s1, 1\n"
"s_cbranch_scc0 .Lmk_p35l\n"
".Lmk_p35k:\n"
"v_mfma_f32_16x16x32_bf16 v[148:151], a[128:131], v[156:159], 0\n"
"v_mfma_f32_16x16x32_bf16 v[168:171], a[132:135], v[156:159], 0\n"
"v_mfma_f32_16x16x32_bf16 v[172:175], a[136:139], v[156:159], 0\n"
"v_mfma_f32_16x16x32_bf16 v[184:187], a[140:143], v[156:159], 0\n"
"v_mfma_f32_16x16x32_bf16 v[148:151], a[144:147], v[160:163], v[148:151]\n"
"v_mfma_f32_16x16x32_bf16 v[168:171], a[148:151], v[160:163], v[168:171]\n"
"v_mfma_f32_16x16x32_bf16 v[172:175], a[152:155], v[160:163], v[172:175]\n"
"v_mfma_f32_16x16x32_bf16 v[184:187], a[156:159], v[160:163], v[184:187]\n"
"v_mfma_f32_16x16x32_bf16 v[148:151], a[160:163], v[164:167], v[148:151]\n"
"v_mfma_f32_16x16x32_bf16 v[168:171], a[164:167], v[164:167], v[168:171]\n"
"v_mfma_f32_16x16x32_bf16 v[172:175], a[168:171], v[164:167], v[172:175]\n"
"v_mfma_f32_16x16x32_bf16 v[184:187], a[172:175], v[164:167], v[184:187]\n"
"v_mfma_f32_16x16x32_bf16 v[148:151], a[176:179], v[116:119], v[148:151]\n"
"v_mfma_f32_16x16x32_bf16 v[168:171], a[180:183], v[116:119], v[168:171]\n"
"v_mfma_f32_16x16x32_bf16 v[172:175], a[184:187], v[116:119], v[172:175]\n"
"v_mfma_f32_16x16x32_bf16 v[184:187], a[188:191], v[116:119], v[184:187]\n"
"v_mfma_f32_16x16x32_bf16 v[148:151], a[192:195], v[180:183], v[148:151]\n"
"v_mfma_f32_16x16x32_bf16 v[168:171], a[196:199], v[180:183], v[168:171]\n"
"v_mfma_f32_16x16x32_bf16 v[172:175], a[200:203], v[180:183], v[172:175]\n"
"v_mfma_f32_16x16x32_bf16 v[184:187], a[204:207], v[180:183], v[184:187]\n"
"v_mfma_f32_16x16x32_bf16 v[148:151], a[208:211], v[88:91], v[148:151]\n"
"v_mfma_f32_16x16x32_bf16 v[168:171], a[212:215], v[88:91], v[168:171]\n"
"v_mfma_f32_16x16x32_bf16 v[172:175], a[216:219], v[88:91], v[172:175]\n"
"v_mfma_f32_16x16x32_bf16 v[184:187], a[220:223], v[88:91], v[184:187]\n"
"v_mfma_f32_16x16x32_bf16 v[148:151], a[224:227], v[92:95], v[148:151]\n"
"v_mfma_f32_16x16x32_bf16 v[168:171], a[228:231], v[92:95], v[168:171]\n"
"v_mfma_f32_16x16x32_bf16 v[172:175], a[232:235], v[92:95], v[172:175]\n"
"v_mfma_f32_16x16x32_bf16 v[184:187], a[236:239], v[92:95], v[184:187]\n"
"v_mfma_f32_16x16x32_bf16 v[148:151], a[240:243], v[100:103], v[148:151]\n"
"v_mfma_f32_16x16x32_bf16 v[168:171], a[244:247], v[100:103], v[168:171]\n"
"v_mfma_f32_16x16x32_bf16 v[172:175], a[248:251], v[100:103], v[172:175]\n"
"v_mfma_f32_16x16x32_bf16 v[184:187], a[252:255], v[100:103], v[184:187]\n"
"s_add_u32 s99, s59, 0x40000\n"
"v_mov_b32_e32 v255, 0x80000000\n"
"buffer_load_dwordx4 v[156:159], v0, s[4:7], s99 offen sc1\n"
"buffer_load_dwordx4 v[160:163], v0, s[4:7], s99 offen offset:1024 sc1\n"
"buffer_load_dwordx4 v[164:167], v0, s[4:7], s99 offen offset:2048 sc1\n"
"buffer_load_dwordx4 v[116:119], v0, s[4:7], s99 offen offset:3072 sc1\n"
"buffer_load_dwordx4 v[180:183], v1, s[4:7], s99 offen sc1\n"
"buffer_load_dwordx4 v[88:91], v1, s[4:7], s99 offen offset:1024 sc1\n"
"buffer_load_dwordx4 v[92:95], v1, s[4:7], s99 offen offset:2048 sc1\n"
"buffer_load_dwordx4 v[100:103], v1, s[4:7], s99 offen offset:3072 sc1\n"
"s_nop 7\n"
"s_branch .Lmk_p35r\n"
".Lmk_q37s:\n"
";\n"
"s_mov_b32 s1, 0x10000\n"
".Lmk_q37l:\n"
"s_add_u32 s98, s59, 0x40000\n"
"buffer_load_dwordx4 v[156:159], v0, s[4:7], s98 offen sc1\n"
"buffer_load_dwordx4 v[160:163], v0, s[4:7], s98 offen offset:1024 sc1\n"
"buffer_load_dwordx4 v[164:167], v0, s[4:7], s98 offen offset:2048 sc1\n"
"buffer_load_dwordx4 v[116:119], v0, s[4:7], s98 offen offset:3072 sc1\n"
"buffer_load_dwordx4 v[180:183], v1, s[4:7], s98 offen sc1\n"
"buffer_load_dwordx4 v[88:91], v1, s[4:7], s98 offen offset:1024 sc1\n"
"buffer_load_dwordx4 v[92:95], v1, s[4:7], s98 offen offset:2048 sc1\n"
"buffer_load_dwordx4 v[100:103], v1, s[4:7], s98 offen offset:3072 sc1\n"
"s_add_u32 s98, s56, 0\n"
"buffer_load_dwordx4 v[120:123], v0, s[4:7], s98 offen sc1\n"
"buffer_load_dwordx4 v[108:111], v0, s[4:7], s98 offen offset:1024 sc1\n"
"buffer_load_dwordx4 v[124:127], v0, s[4:7], s98 offen offset:2048 sc1\n"
"buffer_load_dwordx4 v[136:139], v0, s[4:7], s98 offen offset:3072 sc1\n"
"s_waitcnt vmcnt(0)\n"
"v_max_u32_e32 v255, v156, v157\n"
"v_max3_u32 v255, v255, v158, v159\n"
"v_max3_u32 v255, v255, v160, v161\n"
"v_max3_u32 v255, v255, v162, v163\n"
"v_max3_u32 v255, v255, v164, v165\n"
"v_max3_u32 v255, v255, v166, v167\n"
"v_max3_u32 v255, v255, v116, v117\n"
"v_max3_u32 v255, v255, v118, v119\n"
"v_cmp_eq_u32_e32 vcc, -1, v255\n"
"s_cbranch_vccz .Lmk_q37k\n"
"s_sub_u32 s1, s1, 1\n"
"s_cbranch_scc0 .Lmk_q37l\n"
".Lmk_q37k:\n"
"s_branch .Lmk_q37r\n"
".Lmk_p36s:\n"
";\n"
"s_mov_b32 s1, 0x10000\n"
".Lmk_p36l:\n"
"s_add_u32 s98, s58, 0x40000\n"
"buffer_load_dwordx4 v[132:135], v0, s[4:7], s98 offen sc1\n"
"buffer_load_dwordx4 v[140:143], v0, s[4:7], s98 offen offset:1024 sc1\n"
"buffer_load_dwordx4 v[144:147], v0, s[4:7], s98 offen offset:2048 sc1\n"
"buffer_load_dwordx4 v[148:151], v0, s[4:7], s98 offen offset:3072 sc1\n"
"buffer_load_dwordx4 v[152:155], v1, s[4:7], s98 offen sc1\n"
"buffer_load_dwordx4 v[168:171], v1, s[4:7], s98 offen offset:1024 sc1\n"
"buffer_load_dwordx4 v[172:175], v1, s[4:7], s98 offen offset:2048 sc1\n"
"buffer_load_dwordx4 v[176:179], v1, s[4:7], s98 offen offset:3072 sc1\n"
"s_add_u32 s98, s59, 0x40000\n"
"buffer_load_dwordx4 v[156:159], v0, s[4:7], s98 offen sc1\n"
"buffer_load_dwordx4 v[160:163], v0, s[4:7], s98 offen offset:1024 sc1\n"
"buffer_load_dwordx4 v[164:167], v0, s[4:7], s98 offen offset:2048 sc1\n"
"buffer_load_dwordx4 v[116:119], v0, s[4:7], s98 offen offset:3072 sc1\n"
"buffer_load_dwordx4 v[180:183], v1, s[4:7], s98 offen sc1\n"
"buffer_load_dwordx4 v[88:91], v1, s[4:7], s98 offen offset:1024 sc1\n"
"buffer_load_dwordx4 v[92:95], v1, s[4:7], s98 offen offset:2048 sc1\n"
"buffer_load_dwordx4 v[100:103], v1, s[4:7], s98 offen offset:3072 sc1\n"
"s_add_u32 s98, s56, 0\n"
"buffer_load_dwordx4 v[120:123], v0, s[4:7], s98 offen sc1\n"
"buffer_load_dwordx4 v[108:111], v0, s[4:7], s98 offen offset:1024 sc1\n"
"buffer_load_dwordx4 v[124:127], v0, s[4:7], s98 offen offset:2048 sc1\n"
"buffer_load_dwordx4 v[136:139], v0, s[4:7], s98 offen offset:3072 sc1\n"
"s_waitcnt vmcnt(0)\n"
"v_max_u32_e32 v255, v132, v133\n"
"v_max3_u32 v255, v255, v134, v135\n"
"v_max3_u32 v255, v255, v140, v141\n"
"v_max3_u32 v255, v255, v142, v143\n"
"v_max3_u32 v255, v255, v144, v145\n"
"v_max3_u32 v255, v255, v146, v147\n"
"v_max3_u32 v255, v255, v148, v149\n"
"v_max3_u32 v255, v255, v150, v151\n"
"v_max3_u32 v255, v255, v152, v153\n"
"v_max3_u32 v255, v255, v154, v155\n"
"v_max3_u32 v255, v255, v168, v169\n"
"v_max3_u32 v255, v255, v170, v171\n"
"v_max3_u32 v255, v255, v172, v173\n"
"v_max3_u32 v255, v255, v174, v175\n"
"v_max3_u32 v255, v255, v176, v177\n"
"v_max3_u32 v255, v255, v178, v179\n"
"v_cmp_eq_u32_e32 vcc, -1, v255\n"
"s_cbranch_vccz .Lmk_p36k\n"
"s_sub_u32 s1, s1, 1\n"
"s_cbranch_scc0 .Lmk_p36l\n"
".Lmk_p36k:\n"
"v_mfma_f32_16x16x32_bf16 v[96:99], a[128:131], v[132:135], 0\n"
"v_mfma_f32_16x16x32_bf16 v[104:107], a[132:135], v[132:135], 0\n"
"v_mfma_f32_16x16x32_bf16 v[112:115], a[136:139], v[132:135], 0\n"
"v_mfma_f32_16x16x32_bf16 v[128:131], a[140:143], v[132:135], 0\n"
"v_mfma_f32_16x16x32_bf16 v[96:99], a[144:147], v[140:143], v[96:99]\n"
"v_mfma_f32_16x16x32_bf16 v[104:107], a[148:151], v[140:143], v[104:107]\n"
"v_mfma_f32_16x16x32_bf16 v[112:115], a[152:155], v[140:143], v[112:115]\n"
"v_mfma_f32_16x16x32_bf16 v[128:131], a[156:159], v[140:143], v[128:131]\n"
"v_mfma_f32_16x16x32_bf16 v[96:99], a[160:163], v[144:147], v[96:99]\n"
"v_mfma_f32_16x16x32_bf16 v[104:107], a[164:167], v[144:147], v[104:107]\n"
"v_mfma_f32_16x16x32_bf16 v[112:115], a[168:171], v[144:147], v[112:115]\n"
"v_mfma_f32_16x16x32_bf16 v[128:131], a[172:175], v[144:147], v[128:131]\n"
"v_mfma_f32_16x16x32_bf16 v[96:99], a[176:179], v[148:151], v[96:99]\n"
"v_mfma_f32_16x16x32_bf16 v[104:107], a[180:183], v[148:151], v[104:107]\n"
"v_mfma_f32_16x16x32_bf16 v[112:115], a[184:187], v[148:151], v[112:115]\n"
"v_mfma_f32_16x16x32_bf16 v[128:131], a[188:191], v[148:151], v[128:131]\n"
"v_mfma_f32_16x16x32_bf16 v[96:99], a[192:195], v[152:155], v[96:99]\n"
"v_mfma_f32_16x16x32_bf16 v[104:107], a[196:199], v[152:155], v[104:107]\n"
"v_mfma_f32_16x16x32_bf16 v[112:115], a[200:203], v[152:155], v[112:115]\n"
"v_mfma_f32_16x16x32_bf16 v[128:131], a[204:207], v[152:155], v[128:131]\n"
"v_mfma_f32_16x16x32_bf16 v[96:99], a[208:211], v[168:171], v[96:99]\n"
"v_mfma_f32_16x16x32_bf16 v[104:107], a[212:215], v[168:171], v[104:107]\n"
"v_mfma_f32_16x16x32_bf16 v[112:115], a[216:219], v[168:171], v[112:115]\n"
"v_mfma_f32_16x16x32_bf16 v[128:131], a[220:223], v[168:171], v[128:131]\n"
"v_mfma_f32_16x16x32_bf16 v[96:99], a[224:227], v[172:175], v[96:99]\n"
"v_mfma_f32_16x16x32_bf16 v[104:107], a[228:231], v[172:175], v[104:107]\n"
"v_mfma_f32_16x16x32_bf16 v[112:115], a[232:235], v[172:175], v[112:115]\n"
"v_mfma_f32_16x16x32_bf16 v[128:131], a[236:239], v[172:175], v[128:131]\n"
"v_mfma_f32_16x16x32_bf16 v[96:99], a[240:243], v[176:179], v[96:99]\n"
"v_mfma_f32_16x16x32_bf16 v[104:107], a[244:247], v[176:179], v[104:107]\n"
"v_mfma_f32_16x16x32_bf16 v[112:115], a[248:251], v[176:179], v[112:115]\n"
"v_mfma_f32_16x16x32_bf16 v[128:131], a[252:255], v[176:179], v[128:131]\n"
"s_nop 7\n"
"s_branch .Lmk_p36r\n"
".Lmk_q38s:\n"
";\n"
"s_mov_b32 s1, 0x10000\n"
".Lmk_q38l:\n"
"s_add_u32 s98, s59, 0x40000\n"
"buffer_load_dwordx4 v[180:183], v1, s[4:7], s98 offen sc1\n"
"buffer_load_dwordx4 v[88:91], v1, s[4:7], s98 offen offset:1024 sc1\n"
"buffer_load_dwordx4 v[92:95], v1, s[4:7], s98 offen offset:2048 sc1\n"
"buffer_load_dwordx4 v[100:103], v1, s[4:7], s98 offen offset:3072 sc1\n"
"s_add_u32 s98, s56, 0\n"
"buffer_load_dwordx4 v[120:123], v0, s[4:7], s98 offen sc1\n"
"buffer_load_dwordx4 v[108:111], v0, s[4:7], s98 offen offset:1024 sc1\n"
"buffer_load_dwordx4 v[124:127], v0, s[4:7], s98 offen offset:2048 sc1\n"
"buffer_load_dwordx4 v[136:139], v0, s[4:7], s98 offen offset:3072 sc1\n"
"buffer_load_dwordx4 v[132:135], v1, s[4:7], s98 offen sc1\n"
"buffer_load_dwordx4 v[96:99], v1, s[4:7], s98 offen offset:1024 sc1\n"
"s_waitcnt vmcnt(0)\n"
"v_max_u32_e32 v255, v180, v181\n"
"v_max3_u32 v255, v255, v182, v183\n"
"v_max3_u32 v255, v255, v88, v89\n"
"v_max3_u32 v255, v255, v90, v91\n"
"v_max3_u32 v255, v255, v92, v93\n"
"v_max3_u32 v255, v255, v94, v95\n"
"v_max3_u32 v255, v255, v100, v101\n"
"v_max3_u32 v255, v255, v102, v103\n"
"v_cmp_eq_u32_e32 vcc, -1, v255\n"
"s_cbranch_vccz .Lmk_q38k\n"
"s_sub_u32 s1, s1, 1\n"
"s_cbranch_scc0 .Lmk_q38l\n"
".Lmk_q38k:\n"
"s_branch .Lmk_q38r\n"
".Lmk_p39s:\n"
";\n"
"s_mov_b32 s1, 0x10000\n"
".Lmk_p39l:\n"
"s_add_u32 s98, s56, 0\n"
"buffer_load_dwordx4 v[116:119], v0, s[4:7], s98 offen sc1\n"
"buffer_load_dwordx4 v[140:143], v0, s[4:7], s98 offen offset:1024 sc1\n"
"buffer_load_dwordx4 v[148:151], v0, s[4:7], s98 offen offset:2048 sc1\n"
"buffer_load_dwordx4 v[156:159], v0, s[4:7], s98 offen offset:3072 sc1\n"
"buffer_load_dwordx4 v[168:171], v1, s[4:7], s98 offen sc1\n"
"buffer_load_dwordx4 v[172:175], v1, s[4:7], s98 offen offset:1024 sc1\n"
"buffer_load_dwordx4 v[176:179], v1, s[4:7], s98 offen offset:2048 sc1\n"
"buffer_load_dwordx4 v[180:183], v1, s[4:7], s98 offen offset:3072 sc1\n"
"s_add_u32 s98, s57, 0\n"
"buffer_load_dwordx4 v[128:131], v0, s[4:7], s98 offen sc1\n"
"buffer_load_dwordx4 v[92:95], v0, s[4:7], s98 offen offset:1024 sc1\n"
"buffer_load_dwordx4 v[164:167], v0, s[4:7], s98 offen offset:2048 sc1\n"
"buffer_load_dwordx4 v[88:91], v0, s[4:7], s98 offen offset:3072 sc1\n"
"buffer_load_dwordx4 v[100:103], v1, s[4:7], s98 offen sc1\n"
"buffer_load_dwordx4 v[144:147], v1, s[4:7], s98 offen offset:1024 sc1\n"
"buffer_load_dwordx4 v[152:155], v1, s[4:7], s98 offen offset:2048 sc1\n"
"buffer_load_dwordx4 v[160:163], v1, s[4:7], s98 offen offset:3072 sc1\n"
"s_add_u32 s98, s59, 0\n"
"buffer_load_dwordx4 v[200:203], v0, s[4:7], s98 offen sc1\n"
"buffer_load_dwordx4 v[204:207], v0, s[4:7], s98 offen offset:1024 sc1\n"
"buffer_load_dwordx4 v[208:211], v0, s[4:7], s98 offen offset:2048 sc1\n"
"buffer_load_dwordx4 v[212:215], v0, s[4:7], s98 offen offset:3072 sc1\n"
"buffer_load_dwordx4 v[216:219], v1, s[4:7], s98 offen sc1\n"
"buffer_load_dwordx4 v[220:223], v1, s[4:7], s98 offen offset:1024 sc1\n"
"buffer_load_dwordx4 v[224:227], v1, s[4:7], s98 offen offset:2048 sc1\n"
"s_waitcnt vmcnt(0)\n"
"v_max_u32_e32 v255, v116, v117\n"
"v_max3_u32 v255, v255, v118, v119\n"
"v_max3_u32 v255, v255, v140, v141\n"
"v_max3_u32 v255, v255, v142, v143\n"
"v_max3_u32 v255, v255, v148, v149\n"
"v_max3_u32 v255, v255, v150, v151\n"
"v_max3_u32 v255, v255, v156, v157\n"
"v_max3_u32 v255, v255, v158, v159\n"
"v_max3_u32 v255, v255, v168, v169\n"
"v_max3_u32 v255, v255, v170, v171\n"
"v_max3_u32 v255, v255, v172, v173\n"
"v_max3_u32 v255, v255, v174, v175\n"
"v_max3_u32 v255, v255, v176, v177\n"
"v_max3_u32 v255, v255, v178, v179\n"
"v_max3_u32 v255, v255, v180, v181\n"
"v_max3_u32 v255, v255, v182, v183\n"
"v_cmp_eq_u32_e32 vcc, -1, v255\n"
"s_cbranch_vccz .Lmk_p39k\n"
"s_sub_u32 s1, s1, 1\n"
"s_cbranch_scc0 .Lmk_p39l\n"
".Lmk_p39k:\n"
"v_mfma_f32_16x16x32_bf16 v[184:187], a[128:131], v[116:119], 0\n"
"v_mfma_f32_16x16x32_bf16 v[188:191], a[132:135], v[116:119], 0\n"
"v_mfma_f32_16x16x32_bf16 v[192:195], a[136:139], v[116:119], 0\n"
"v_mfma_f32_16x16x32_bf16 v[196:199], a[140:143], v[116:119], 0\n"
"v_mfma_f32_16x16x32_bf16 v[184:187], a[144:147], v[140:143], v[184:187]\n"
"v_mfma_f32_16x16x32_bf16 v[188:191], a[148:151], v[140:143], v[188:191]\n"
"v_mfma_f32_16x16x32_bf16 v[192:195], a[152:155], v[140:143], v[192:195]\n"
"v_mfma_f32_16x16x32_bf16 v[196:199], a[156:159], v[140:143], v[196:199]\n"
"v_mfma_f32_16x16x32_bf16 v[184:187], a[160:163], v[148:151], v[184:187]\n"
"v_mfma_f32_16x16x32_bf16 v[188:191], a[164:167], v[148:151], v[188:191]\n"
"v_mfma_f32_16x16x32_bf16 v[192:195], a[168:171], v[148:151], v[192:195]\n"
"v_mfma_f32_16x16x32_bf16 v[196:199], a[172:175], v[148:151], v[196:199]\n"
"v_mfma_f32_16x16x32_bf16 v[184:187], a[176:179], v[156:159], v[184:187]\n"
"v_mfma_f32_16x16x32_bf16 v[188:191], a[180:183], v[156:159], v[188:191]\n"
"v_mfma_f32_16x16x32_bf16 v[192:195], a[184:187], v[156:159], v[192:195]\n"
"v_mfma_f32_16x16x32_bf16 v[196:199], a[188:191], v[156:159], v[196:199]\n"
"v_mfma_f32_16x16x32_bf16 v[184:187], a[192:195], v[168:171], v[184:187]\n"
"v_mfma_f32_16x16x32_bf16 v[188:191], a[196:199], v[168:171], v[188:191]\n"
"v_mfma_f32_16x16x32_bf16 v[192:195], a[200:203], v[168:171], v[192:195]\n"
"v_mfma_f32_16x16x32_bf16 v[196:199], a[204:207], v[168:171], v[196:199]\n"
"v_mfma_f32_16x16x32_bf16 v[184:187], a[208:211], v[172:175], v[184:187]\n"
"v_mfma_f32_16x16x32_bf16 v[188:191], a[212:215], v[172:175], v[188:191]\n"
"v_mfma_f32_16x16x32_bf16 v[192:195], a[216:219], v[172:175], v[192:195]\n"
"v_mfma_f32_16x16x32_bf16 v[196:199], a[220:223], v[172:175], v[196:199]\n"
"v_mfma_f32_16x16x32_bf16 v[184:187], a[224:227], v[176:179], v[184:187]\n"
"v_mfma_f32_16x16x32_bf16 v[188:191], a[228:231], v[176:179], v[188:191]\n"
"v_mfma_f32_16x16x32_bf16 v[192:195], a[232:235], v[176:179], v[192:195]\n"
"v_mfma_f32_16x16x32_bf16 v[196:199], a[236:239], v[176:179], v[196:199]\n"
"v_mfma_f32_16x16x32_bf16 v[184:187], a[240:243], v[180:183], v[184:187]\n"
"v_mfma_f32_16x16x32_bf16 v[188:191], a[244:247], v[180:183], v[188:191]\n"
"v_mfma_f32_16x16x32_bf16 v[192:195], a[248:251], v[180:183], v[192:195]\n"
"v_mfma_f32_16x16x32_bf16 v[196:199], a[252:255], v[180:183], v[196:199]\n"
"s_add_u32 s98, s58, 0\n"
"v_mov_b32_e32 v255, 0x80000000\n"
"buffer_load_dwordx4 v[116:119], v0, s[4:7], s98 offen sc1\n"
"buffer_load_dwordx4 v[140:143], v0, s[4:7], s98 offen offset:1024 sc1\n"
"buffer_load_dwordx4 v[148:151], v0, s[4:7], s98 offen offset:2048 sc1\n"
"buffer_load_dwordx4 v[156:159], v0, s[4:7], s98 offen offset:3072 sc1\n"
"buffer_load_dwordx4 v[168:171], v1, s[4:7], s98 offen sc1\n"
"buffer_load_dwordx4 v[172:175], v1, s[4:7], s98 offen offset:1024 sc1\n"
"buffer_load_dwordx4 v[176:179], v1, s[4:7], s98 offen offset:2048 sc1\n"
"buffer_load_dwordx4 v[180:183], v1, s[4:7], s98 offen offset:3072 sc1\n"
"buffer_store_dword v255, v255, s[4:7], 0 offen\n"
"buffer_store_dword v255, v255, s[4:7], 0 offen\n"
"buffer_store_dword v255, v255, s[4:7], 0 offen\n"
"buffer_store_dword v255, v255, s[4:7], 0 offen\n"
"buffer_store_dword v255, v255, s[4:7], 0 offen\n"
"buffer_store_dword v255, v255, s[4:7], 0 offen\n"
"buffer_store_dword v255, v255, s[4:7], 0 offen\n"
"s_nop 7\n"
"s_branch .Lmk_p39r\n"
".Lmk_p40s:\n"
";\n"
"s_mov_b32 s1, 0x10000\n"
".Lmk_p40l:\n"
"s_add_u32 s97, s57, 0\n"
"buffer_load_dwordx4 v[200:203], v0, s[4:7], s97 offen sc1\n"
"buffer_load_dwordx4 v[204:207], v0, s[4:7], s97 offen offset:1024 sc1\n"
"buffer_load_dwordx4 v[208:211], v0, s[4:7], s97 offen offset:2048 sc1\n"
"buffer_load_dwordx4 v[212:215], v0, s[4:7], s97 offen offset:3072 sc1\n"
"buffer_load_dwordx4 v[216:219], v1, s[4:7], s97 offen sc1\n"
"buffer_load_dwordx4 v[220:223], v1, s[4:7], s97 offen offset:1024 sc1\n"
"buffer_load_dwordx4 v[224:227], v1, s[4:7], s97 offen offset:2048 sc1\n"
"buffer_load_dwordx4 v[228:231], v1, s[4:7], s97 offen offset:3072 sc1\n"
"s_add_u32 s97, s58, 0\n"
"buffer_load_dwordx4 v[116:119], v0, s[4:7], s97 offen sc1\n"
"buffer_load_dwordx4 v[140:143], v0, s[4:7], s97 offen offset:1024 sc1\n"
"buffer_load_dwordx4 v[148:151], v0, s[4:7], s97 offen offset:2048 sc1\n"
"buffer_load_dwordx4 v[156:159], v0, s[4:7], s97 offen offset:3072 sc1\n"
"buffer_load_dwordx4 v[168:171], v1, s[4:7], s97 offen sc1\n"
"buffer_load_dwordx4 v[172:175], v1, s[4:7], s97 offen offset:1024 sc1\n"
"buffer_load_dwordx4 v[176:179], v1, s[4:7], s97 offen offset:2048 sc1\n"
"buffer_load_dwordx4 v[180:183], v1, s[4:7], s97 offen offset:3072 sc1\n"
"s_waitcnt vmcnt(0)\n"
"v_max_u32_e32 v253, v200, v201\n"
"v_max3_u32 v253, v253, v202, v203\n"
"v_max3_u32 v253, v253, v204, v205\n"
"v_max3_u32 v253, v253, v206, v207\n"
"v_max3_u32 v253, v253, v208, v209\n"
"v_max3_u32 v253, v253, v210, v211\n"
"v_max3_u32 v253, v253, v212, v213\n"
"v_max3_u32 v253, v253, v214, v215\n"
"v_max3_u32 v253, v253, v216, v217\n"
"v_max3_u32 v253, v253, v218, v219\n"
"v_max3_u32 v253, v253, v220, v221\n"
"v_max3_u32 v253, v253, v222, v223\n"
"v_max3_u32 v253, v253, v224, v225\n"
"v_max3_u32 v253, v253, v226, v227\n"
"v_max3_u32 v253, v253, v228, v229\n"
"v_max3_u32 v253, v253, v230, v231\n"
"v_cmp_eq_u32_e32 vcc, -1, v253\n"
"s_cbranch_vccz .Lmk_p40k\n"
"s_sub_u32 s1, s1, 1\n"
"s_cbranch_scc0 .Lmk_p40l\n"
".Lmk_p40k:\n"
"v_mfma_f32_16x16x32_bf16 v[108:111], a[128:131], v[200:203], 0\n"
"v_mfma_f32_16x16x32_bf16 v[112:115], a[132:135], v[200:203], 0\n"
"v_mfma_f32_16x16x32_bf16 v[120:123], a[136:139], v[200:203], 0\n"
"v_mfma_f32_16x16x32_bf16 v[124:127], a[140:143], v[200:203], 0\n"
"v_mfma_f32_16x16x32_bf16 v[108:111], a[144:147], v[204:207], v[108:111]\n"
"v_mfma_f32_16x16x32_bf16 v[112:115], a[148:151], v[204:207], v[112:115]\n"
"v_mfma_f32_16x16x32_bf16 v[120:123], a[152:155], v[204:207], v[120:123]\n"
"v_mfma_f32_16x16x32_bf16 v[124:127], a[156:159], v[204:207], v[124:127]\n"
"v_mfma_f32_16x16x32_bf16 v[108:111], a[160:163], v[208:211], v[108:111]\n"
"v_mfma_f32_16x16x32_bf16 v[112:115], a[164:167], v[208:211], v[112:115]\n"
"v_mfma_f32_16x16x32_bf16 v[120:123], a[168:171], v[208:211], v[120:123]\n"
"v_mfma_f32_16x16x32_bf16 v[124:127], a[172:175], v[208:211], v[124:127]\n"
"v_mfma_f32_16x16x32_bf16 v[108:111], a[176:179], v[212:215], v[108:111]\n"
"v_mfma_f32_16x16x32_bf16 v[112:115], a[180:183], v[212:215], v[112:115]\n"
"v_mfma_f32_16x16x32_bf16 v[120:123], a[184:187], v[212:215], v[120:123]\n"
"v_mfma_f32_16x16x32_bf16 v[124:127], a[188:191], v[212:215], v[124:127]\n"
"v_mfma_f32_16x16x32_bf16 v[108:111], a[192:195], v[216:219], v[108:111]\n"
"v_mfma_f32_16x16x32_bf16 v[112:115], a[196:199], v[216:219], v[112:115]\n"
"v_mfma_f32_16x16x32_bf16 v[120:123], a[200:203], v[216:219], v[120:123]\n"
"v_mfma_f32_16x16x32_bf16 v[124:127], a[204:207], v[216:219], v[124:127]\n"
"v_mfma_f32_16x16x32_bf16 v[108:111], a[208:211], v[220:223], v[108:111]\n"
"v_mfma_f32_16x16x32_bf16 v[112:115], a[212:215], v[220:223], v[112:115]\n"
"v_mfma_f32_16x16x32_bf16 v[120:123], a[216:219], v[220:223], v[120:123]\n"
"v_mfma_f32_16x16x32_bf16 v[124:127], a[220:223], v[220:223], v[124:127]\n"
"v_mfma_f32_16x16x32_bf16 v[108:111], a[224:227], v[224:227], v[108:111]\n"
"v_mfma_f32_16x16x32_bf16 v[112:115], a[228:231], v[224:227], v[112:115]\n"
"v_mfma_f32_16x16x32_bf16 v[120:123], a[232:235], v[224:227], v[120:123]\n"
"v_mfma_f32_16x16x32_bf16 v[124:127], a[236:239], v[224:227], v[124:127]\n"
"v_mfma_f32_16x16x32_bf16 v[108:111], a[240:243], v[228:231], v[108:111]\n"
"v_mfma_f32_16x16x32_bf16 v[112:115], a[244:247], v[228:231], v[112:115]\n"
"v_mfma_f32_16x16x32_bf16 v[120:123], a[248:251], v[228:231], v[120:123]\n"
"v_mfma_f32_16x16x32_bf16 v[124:127], a[252:255], v[228:231], v[124:127]\n"
"s_add_u32 s97, s59, 0\n"
"v_mov_b32_e32 v253, 0x80000000\n"
"buffer_load_dwordx4 v[200:203], v0, s[4:7], s97 offen sc1\n"
"buffer_load_dwordx4 v[204:207], v0, s[4:7], s97 offen offset:1024 sc1\n"
"buffer_load_dwordx4 v[208:211], v0, s[4:7], s97 offen offset:2048 sc1\n"
"buffer_load_dwordx4 v[212:215], v0, s[4:7], s97 offen offset:3072 sc1\n"
"buffer_load_dwordx4 v[216:219], v1, s[4:7], s97 offen sc1\n"
"buffer_load_dwordx4 v[220:223], v1, s[4:7], s97 offen offset:1024 sc1\n"
"buffer_load_dwordx4 v[224:227], v1, s[4:7], s97 offen offset:2048 sc1\n"
"buffer_load_dwordx4 v[228:231], v1, s[4:7], s97 offen offset:3072 sc1\n"
"buffer_store_dword v253, v253, s[4:7], 0 offen\n"
"s_nop 7\n"
"s_branch .Lmk_p40r\n"
".Lmk_q42s:\n"
";\n"
"s_mov_b32 s1, 0x10000\n"
".Lmk_q42l:\n"
"s_add_u32 s97, s59, 0\n"
"buffer_load_dwordx4 v[200:203], v0, s[4:7], s97 offen sc1\n"
"buffer_load_dwordx4 v[204:207], v0, s[4:7], s97 offen offset:1024 sc1\n"
"buffer_load_dwordx4 v[208:211], v0, s[4:7], s97 offen offset:2048 sc1\n"
"buffer_load_dwordx4 v[212:215], v0, s[4:7], s97 offen offset:3072 sc1\n"
"buffer_load_dwordx4 v[216:219], v1, s[4:7], s97 offen sc1\n"
"buffer_load_dwordx4 v[220:223], v1, s[4:7], s97 offen offset:1024 sc1\n"
"buffer_load_dwordx4 v[224:227], v1, s[4:7], s97 offen offset:2048 sc1\n"
"buffer_load_dwordx4 v[228:231], v1, s[4:7], s97 offen offset:3072 sc1\n"
"s_add_u32 s97, s52, 0x440000\n"
"buffer_load_dwordx4 v[104:107], v0, s[4:7], s97 offen sc1\n"
"buffer_load_dwordx4 v[108:111], v0, s[4:7], s97 offen offset:1024 sc1\n"
"buffer_load_dwordx4 v[112:115], v0, s[4:7], s97 offen offset:2048 sc1\n"
"buffer_load_dwordx4 v[116:119], v0, s[4:7], s97 offen offset:3072 sc1\n"
"buffer_load_dwordx4 v[120:123], v1, s[4:7], s97 offen sc1\n"
"buffer_load_dwordx4 v[124:127], v1, s[4:7], s97 offen offset:1024 sc1\n"
"buffer_load_dwordx4 v[128:131], v1, s[4:7], s97 offen offset:2048 sc1\n"
"buffer_load_dwordx4 v[132:135], v1, s[4:7], s97 offen offset:3072 sc1\n"
"s_add_u32 s97, s53, 0x440000\n"
"buffer_load_dwordx4 v[136:139], v0, s[4:7], s97 offen sc1\n"
"s_waitcnt vmcnt(0)\n"
"v_max_u32_e32 v253, v200, v201\n"
"v_max3_u32 v253, v253, v202, v203\n"
"v_max3_u32 v253, v253, v204, v205\n"
"v_max3_u32 v253, v253, v206, v207\n"
"v_max3_u32 v253, v253, v208, v209\n"
"v_max3_u32 v253, v253, v210, v211\n"
"v_max3_u32 v253, v253, v212, v213\n"
"v_max3_u32 v253, v253, v214, v215\n"
"v_cmp_eq_u32_e32 vcc, -1, v253\n"
"s_cbranch_vccz .Lmk_q42k\n"
"s_sub_u32 s1, s1, 1\n"
"s_cbranch_scc0 .Lmk_q42l\n"
".Lmk_q42k:\n"
"s_branch .Lmk_q42r\n"
".Lmk_p41s:\n"
";\n"
"s_mov_b32 s1, 0x10000\n"
".Lmk_p41l:\n"
"s_add_u32 s97, s58, 0\n"
"buffer_load_dwordx4 v[104:107], v0, s[4:7], s97 offen sc1\n"
"buffer_load_dwordx4 v[108:111], v0, s[4:7], s97 offen offset:1024 sc1\n"
"buffer_load_dwordx4 v[112:115], v0, s[4:7], s97 offen offset:2048 sc1\n"
"buffer_load_dwordx4 v[116:119], v0, s[4:7], s97 offen offset:3072 sc1\n"
"buffer_load_dwordx4 v[120:123], v1, s[4:7], s97 offen sc1\n"
"buffer_load_dwordx4 v[124:127], v1, s[4:7], s97 offen offset:1024 sc1\n"
"buffer_load_dwordx4 v[128:131], v1, s[4:7], s97 offen offset:2048 sc1\n"
"buffer_load_dwordx4 v[132:135], v1, s[4:7], s97 offen offset:3072 sc1\n"
"s_add_u32 s97, s59, 0\n"
"buffer_load_dwordx4 v[200:203], v0, s[4:7], s97 offen sc1\n"
"buffer_load_dwordx4 v[204:207], v0, s[4:7], s97 offen offset:1024 sc1\n"
"buffer_load_dwordx4 v[208:211], v0, s[4:7], s97 offen offset:2048 sc1\n"
"buffer_load_dwordx4 v[212:215], v0, s[4:7], s97 offen offset:3072 sc1\n"
"buffer_load_dwordx4 v[216:219], v1, s[4:7], s97 offen sc1\n"
"buffer_load_dwordx4 v[220:223], v1, s[4:7], s97 offen offset:1024 sc1\n"
"buffer_load_dwordx4 v[224:227], v1, s[4:7], s97 offen offset:2048 sc1\n"
"buffer_load_dwordx4 v[228:231], v1, s[4:7], s97 offen offset:3072 sc1\n"
"s_add_u32 s97, s53, 0x440000\n"
"buffer_load_dwordx4 v[136:139], v0, s[4:7], s97 offen sc1\n"
"s_waitcnt vmcnt(0)\n"
"v_max_u32_e32 v253, v104, v105\n"
"v_max3_u32 v253, v253, v106, v107\n"
"v_max3_u32 v253, v253, v108, v109\n"
"v_max3_u32 v253, v253, v110, v111\n"
"v_max3_u32 v253, v253, v112, v113\n"
"v_max3_u32 v253, v253, v114, v115\n"
"v_max3_u32 v253, v253, v116, v117\n"
"v_max3_u32 v253, v253, v118, v119\n"
"v_max3_u32 v253, v253, v120, v121\n"
"v_max3_u32 v253, v253, v122, v123\n"
"v_max3_u32 v253, v253, v124, v125\n"
"v_max3_u32 v253, v253, v126, v127\n"
"v_max3_u32 v253, v253, v128, v129\n"
"v_max3_u32 v253, v253, v130, v131\n"
"v_max3_u32 v253, v253, v132, v133\n"
"v_max3_u32 v253, v253, v134, v135\n"
"v_cmp_eq_u32_e32 vcc, -1, v253\n"
"s_cbranch_vccz .Lmk_p41k\n"
"s_sub_u32 s1, s1, 1\n"
"s_cbranch_scc0 .Lmk_p41l\n"
".Lmk_p41k:\n"
"v_mfma_f32_16x16x32_bf16 v[144:147], a[128:131], v[104:107], 0\n"
"v_mfma_f32_16x16x32_bf16 v[152:155], a[132:135], v[104:107], 0\n"
"v_mfma_f32_16x16x32_bf16 v[160:163], a[136:139], v[104:107], 0\n"
"v_mfma_f32_16x16x32_bf16 v[164:167], a[140:143], v[104:107], 0\n"
"v_mfma_f32_16x16x32_bf16 v[144:147], a[144:147], v[108:111], v[144:147]\n"
"v_mfma_f32_16x16x32_bf16 v[152:155], a[148:151], v[108:111], v[152:155]\n"
"v_mfma_f32_16x16x32_bf16 v[160:163], a[152:155], v[108:111], v[160:163]\n"
"v_mfma_f32_16x16x32_bf16 v[164:167], a[156:159], v[108:111], v[164:167]\n"
"v_mfma_f32_16x16x32_bf16 v[144:147], a[160:163], v[112:115], v[144:147]\n"
"v_mfma_f32_16x16x32_bf16 v[152:155], a[164:167], v[112:115], v[152:155]\n"
"v_mfma_f32_16x16x32_bf16 v[160:163], a[168:171], v[112:115], v[160:163]\n"
"v_mfma_f32_16x16x32_bf16 v[164:167], a[172:175], v[112:115], v[164:167]\n"
"v_mfma_f32_16x16x32_bf16 v[144:147], a[176:179], v[116:119], v[144:147]\n"
"v_mfma_f32_16x16x32_bf16 v[152:155], a[180:183], v[116:119], v[152:155]\n"
"v_mfma_f32_16x16x32_bf16 v[160:163], a[184:187], v[116:119], v[160:163]\n"
"v_mfma_f32_16x16x32_bf16 v[164:167], a[188:191], v[116:119], v[164:167]\n"
"v_mfma_f32_16x16x32_bf16 v[144:147], a[192:195], v[120:123], v[144:147]\n"
"v_mfma_f32_16x16x32_bf16 v[152:155], a[196:199], v[120:123], v[152:155]\n"
"v_mfma_f32_16x16x32_bf16 v[160:163], a[200:203], v[120:123], v[160:163]\n"
"v_mfma_f32_16x16x32_bf16 v[164:167], a[204:207], v[120:123], v[164:167]\n"
"v_mfma_f32_16x16x32_bf16 v[144:147], a[208:211], v[124:127], v[144:147]\n"
"v_mfma_f32_16x16x32_bf16 v[152:155], a[212:215], v[124:127], v[152:155]\n"
"v_mfma_f32_16x16x32_bf16 v[160:163], a[216:219], v[124:127], v[160:163]\n"
"v_mfma_f32_16x16x32_bf16 v[164:167], a[220:223], v[124:127], v[164:167]\n"
"v_mfma_f32_16x16x32_bf16 v[144:147], a[224:227], v[128:131], v[144:147]\n"
"v_mfma_f32_16x16x32_bf16 v[152:155], a[228:231], v[128:131], v[152:155]\n"
"v_mfma_f32_16x16x32_bf16 v[160:163], a[232:235], v[128:131], v[160:163]\n"
"v_mfma_f32_16x16x32_bf16 v[164:167], a[236:239], v[128:131], v[164:167]\n"
"v_mfma_f32_16x16x32_bf16 v[144:147], a[240:243], v[132:135], v[144:147]\n"
"v_mfma_f32_16x16x32_bf16 v[152:155], a[244:247], v[132:135], v[152:155]\n"
"v_mfma_f32_16x16x32_bf16 v[160:163], a[248:251], v[132:135], v[160:163]\n"
"v_mfma_f32_16x16x32_bf16 v[164:167], a[252:255], v[132:135], v[164:167]\n"
"s_add_u32 s97, s52, 0x440000\n"
"v_mov_b32_e32 v253, 0x80000000\n"
"buffer_load_dwordx4 v[104:107], v0, s[4:7], s97 offen sc1\n"
"buffer_load_dwordx4 v[108:111], v0, s[4:7], s97 offen offset:1024 sc1\n"
"buffer_load_dwordx4 v[112:115], v0, s[4:7], s97 offen offset:2048 sc1\n"
"buffer_load_dwordx4 v[116:119], v0, s[4:7], s97 offen offset:3072 sc1\n"
"buffer_load_dwordx4 v[120:123], v1, s[4:7], s97 offen sc1\n"
"buffer_load_dwordx4 v[124:127], v1, s[4:7], s97 offen offset:1024 sc1\n"
"buffer_load_dwordx4 v[128:131], v1, s[4:7], s97 offen offset:2048 sc1\n"
"buffer_load_dwordx4 v[132:135], v1, s[4:7], s97 offen offset:3072 sc1\n"
"buffer_store_dword v253, v253, s[4:7], 0 offen\n"
"s_nop 7\n"
"s_branch .Lmk_p41r\n"
".Lmk_f13s:\n"
";\n"
"s_mov_b32 s1, 0x10000\n"
".Lmk_f13l:\n"
"buffer_load_dwordx2 v[254:255], v13, s[4:7], s99 offen sc1\n"
"s_waitcnt vmcnt(0)\n"
"v_cmp_ne_u64_e32 vcc, v[254:255], v[18:19]\n"
"s_cbranch_vccz .Lmk_f13r\n"
"s_sub_u32 s1, s1, 1\n"
"s_cbranch_scc0 .Lmk_f13l\n"
"s_branch .Lmk_f13r\n"
".Lmk_q43s:\n"
";\n"
"s_mov_b32 s1, 0x10000\n"
".Lmk_q43l:\n"
"s_add_u32 s99, s59, 0\n"
"buffer_load_dwordx4 v[216:219], v1, s[4:7], s99 offen sc1\n"
"buffer_load_dwordx4 v[220:223], v1, s[4:7], s99 offen offset:1024 sc1\n"
"buffer_load_dwordx4 v[224:227], v1, s[4:7], s99 offen offset:2048 sc1\n"
"buffer_load_dwordx4 v[228:231], v1, s[4:7], s99 offen offset:3072 sc1\n"
"s_add_u32 s99, s52, 0x440000\n"
"buffer_load_dwordx4 v[104:107], v0, s[4:7], s99 offen sc1\n"
"buffer_load_dwordx4 v[108:111], v0, s[4:7], s99 offen offset:1024 sc1\n"
"buffer_load_dwordx4 v[112:115], v0, s[4:7], s99 offen offset:2048 sc1\n"
"buffer_load_dwordx4 v[116:119], v0, s[4:7], s99 offen offset:3072 sc1\n"
"buffer_load_dwordx4 v[120:123], v1, s[4:7], s99 offen sc1\n"
"buffer_load_dwordx4 v[124:127], v1, s[4:7], s99 offen offset:1024 sc1\n"
"buffer_load_dwordx4 v[128:131], v1, s[4:7], s99 offen offset:2048 sc1\n"
"buffer_load_dwordx4 v[132:135], v1, s[4:7], s99 offen offset:3072 sc1\n"
"s_add_u32 s99, s53, 0x440000\n"
"buffer_load_dwordx4 v[136:139], v0, s[4:7], s99 offen sc1\n"
"buffer_load_dwordx4 v[140:143], v0, s[4:7], s99 offen offset:1024 sc1\n"
"s_waitcnt vmcnt(0)\n"
"v_max_u32_e32 v255, v216, v217\n"
"v_max3_u32 v255, v255, v218, v219\n"
"v_max3_u32 v255, v255, v220, v221\n"
"v_max3_u32 v255, v255, v222, v223\n"
"v_max3_u32 v255, v255, v224, v225\n"
"v_max3_u32 v255, v255, v226, v227\n"
"v_max3_u32 v255, v255, v228, v229\n"
"v_max3_u32 v255, v255, v230, v231\n"
"v_cmp_eq_u32_e32 vcc, -1, v255\n"
"s_cbranch_vccz .Lmk_q43k\n"
"s_sub_u32 s1, s1, 1\n"
"s_cbranch_scc0 .Lmk_q43l\n"
".Lmk_q43k:\n"
"s_branch .Lmk_q43r\n"
".Lmk_p47s:\n"
";\n"
"s_mov_b32 s1, 0x10000\n"
".Lmk_p47l:\n"
"s_add_u32 s98, s52, 0x440000\n"
"buffer_load_dwordx4 v[104:107], v0, s[4:7], s98 offen sc1\n"
"buffer_load_dwordx4 v[212:215], v0, s[4:7], s98 offen offset:1024 sc1\n"
"buffer_load_dwordx4 v[92:95], v0, s[4:7], s98 offen offset:2048 sc1\n"
"buffer_load_dwordx4 v[108:111], v0, s[4:7], s98 offen offset:3072 sc1\n"
"buffer_load_dwordx4 v[100:103], v1, s[4:7], s98 offen sc1\n"
"buffer_load_dwordx4 v[116:119], v1, s[4:7], s98 offen offset:1024 sc1\n"
"buffer_load_dwordx4 v[88:91], v1, s[4:7], s98 offen offset:2048 sc1\n"
"buffer_load_dwordx4 v[96:99], v1, s[4:7], s98 offen offset:3072 sc1\n"
"s_add_u32 s98, s53, 0x440000\n"
"buffer_load_dwordx4 v[136:139], v0, s[4:7], s98 offen sc1\n"
"buffer_load_dwordx4 v[140:143], v0, s[4:7], s98 offen offset:1024 sc1\n"
"buffer_load_dwordx4 v[144:147], v0, s[4:7], s98 offen offset:2048 sc1\n"
"buffer_load_dwordx4 v[148:151], v0, s[4:7], s98 offen offset:3072 sc1\n"
"buffer_load_dwordx4 v[152:155], v1, s[4:7], s98 offen sc1\n"
"buffer_load_dwordx4 v[160:163], v1, s[4:7], s98 offen offset:1024 sc1\n"
"buffer_load_dwordx4 v[168:171], v1, s[4:7], s98 offen offset:2048 sc1\n"
"buffer_load_dwordx4 v[172:175], v1, s[4:7], s98 offen offset:3072 sc1\n"
"s_waitcnt vmcnt(0)\n"
"v_max_u32_e32 v247, v104, v105\n"
"v_max3_u32 v247, v247, v106, v107\n"
"v_max3_u32 v247, v247, v212, v213\n"
"v_max3_u32 v247, v247, v214, v215\n"
"v_max3_u32 v247, v247, v92, v93\n"
"v_max3_u32 v247, v247, v94, v95\n"
"v_max3_u32 v247, v247, v108, v109\n"
"v_max3_u32 v247, v247, v110, v111\n"
"v_max3_u32 v247, v247, v100, v101\n"
"v_max3_u32 v247, v247, v102, v103\n"
"v_max3_u32 v247, v247, v116, v117\n"
"v_max3_u32 v247, v247, v118, v119\n"
"v_max3_u32 v247, v247, v88, v89\n"
"v_max3_u32 v247, v247, v90, v91\n"
"v_max3_u32 v247, v247, v96, v97\n"
"v_max3_u32 v247, v247, v98, v99\n"
"v_cmp_eq_u32_e32 vcc, -1, v247\n"
"s_cbranch_vccz .Lmk_p47k\n"
"s_sub_u32 s1, s1, 1\n"
"s_cbranch_scc0 .Lmk_p47l\n"
".Lmk_p47k:\n"
"v_mfma_f32_16x16x32_bf16 v[184:187], a[0:3], v[104:107], 0\n"
"v_mfma_f32_16x16x32_bf16 v[188:191], a[4:7], v[104:107], 0\n"
"v_mfma_f32_16x16x32_bf16 v[192:195], a[8:11], v[104:107], 0\n"
"v_mfma_f32_16x16x32_bf16 v[196:199], a[12:15], v[104:107], 0\n"
"v_mfma_f32_16x16x32_bf16 v[200:203], a[16:19], v[104:107], 0\n"
"v_mfma_f32_16x16x32_bf16 v[208:211], a[20:23], v[104:107], 0\n"
"v_mfma_f32_16x16x32_bf16 v[184:187], a[24:27], v[212:215], v[184:187]\n"
"v_mfma_f32_16x16x32_bf16 v[188:191], a[28:31], v[212:215], v[188:191]\n"
"v_mfma_f32_16x16x32_bf16 v[192:195], a[32:35], v[212:215], v[192:195]\n"
"v_mfma_f32_16x16x32_bf16 v[196:199], a[36:39], v[212:215], v[196:199]\n"
"v_mfma_f32_16x16x32_bf16 v[200:203], a[40:43], v[212:215], v[200:203]\n"
"v_mfma_f32_16x16x32_bf16 v[208:211], a[44:47], v[212:215], v[208:211]\n"
"v_mfma_f32_16x16x32_bf16 v[184:187], a[48:51], v[92:95], v[184:187]\n"
"v_mfma_f32_16x16x32_bf16 v[188:191], a[52:55], v[92:95], v[188:191]\n"
"v_mfma_f32_16x16x32_bf16 v[192:195], a[56:59], v[92:95], v[192:195]\n"
"v_mfma_f32_16x16x32_bf16 v[196:199], a[60:63], v[92:95], v[196:199]\n"
"v_mfma_f32_16x16x32_bf16 v[200:203], a[64:67], v[92:95], v[200:203]\n"
"v_mfma_f32_16x16x32_bf16 v[208:211], a[68:71], v[92:95], v[208:211]\n"
"v_mfma_f32_16x16x32_bf16 v[184:187], a[72:75], v[108:111], v[184:187]\n"
"v_mfma_f32_16x16x32_bf16 v[188:191], a[76:79], v[108:111], v[188:191]\n"
"v_mfma_f32_16x16x32_bf16 v[192:195], a[80:83], v[108:111], v[192:195]\n"
"v_mfma_f32_16x16x32_bf16 v[196:199], a[84:87], v[108:111], v[196:199]\n"
"v_mfma_f32_16x16x32_bf16 v[200:203], a[88:91], v[108:111], v[200:203]\n"
"v_mfma_f32_16x16x32_bf16 v[208:211], a[92:95], v[108:111], v[208:211]\n"
"v_mfma_f32_16x16x32_bf16 v[184:187], a[96:99], v[100:103], v[184:187]\n"
"v_mfma_f32_16x16x32_bf16 v[188:191], a[100:103], v[100:103], v[188:191]\n"
"v_mfma_f32_16x16x32_bf16 v[192:195], a[104:107], v[100:103], v[192:195]\n"
"v_mfma_f32_16x16x32_bf16 v[196:199], a[108:111], v[100:103], v[196:199]\n"
"v_mfma_f32_16x16x32_bf16 v[200:203], a[112:115], v[100:103], v[200:203]\n"
"v_mfma_f32_16x16x32_bf16 v[208:211], a[116:119], v[100:103], v[208:211]\n"
"v_mfma_f32_16x16x32_bf16 v[184:187], a[120:123], v[116:119], v[184:187]\n"
"v_mfma_f32_16x16x32_bf16 v[188:191], a[124:127], v[116:119], v[188:191]\n"
"v_mfma_f32_16x16x32_bf16 v[192:195], v[24:27], v[116:119], v[192:195]\n"
"v_mfma_f32_16x16x32_bf16 v[196:199], v[28:31], v[116:119], v[196:199]\n"
"v_mfma_f32_16x16x32_bf16 v[200:203], v[32:35], v[116:119], v[200:203]\n"
"v_mfma_f32_16x16x32_bf16 v[208:211], v[36:39], v[116:119], v[208:211]\n"
"v_mfma_f32_16x16x32_bf16 v[184:187], v[40:43], v[88:91], v[184:187]\n"
"v_mfma_f32_16x16x32_bf16 v[188:191], v[44:47], v[88:91], v[188:191]\n"
"v_mfma_f32_16x16x32_bf16 v[192:195], v[48:51], v[88:91], v[192:195]\n"
"v_mfma_f32_16x16x32_bf16 v[196:199], v[52:55], v[88:91], v[196:199]\n"
"v_mfma_f32_16x16x32_bf16 v[200:203], v[56:59], v[88:91], v[200:203]\n"
"v_mfma_f32_16x16x32_bf16 v[208:211], v[60:63], v[88:91], v[208:211]\n"
"v_mfma_f32_16x16x32_bf16 v[184:187], v[64:67], v[96:99], v[184:187]\n"
"v_mfma_f32_16x16x32_bf16 v[188:191], v[68:71], v[96:99], v[188:191]\n"
"v_mfma_f32_16x16x32_bf16 v[192:195], v[72:75], v[96:99], v[192:195]\n"
"v_mfma_f32_16x16x32_bf16 v[196:199], v[76:79], v[96:99], v[196:199]\n"
"v_mfma_f32_16x16x32_bf16 v[200:203], v[80:83], v[96:99], v[200:203]\n"
"v_mfma_f32_16x16x32_bf16 v[208:211], v[84:87], v[96:99], v[208:211]\n"
"s_add_u32 s98, s54, 0x440000\n"
"v_mov_b32_e32 v247, 0x80000000\n"
"buffer_load_dwordx4 v[104:107], v0, s[4:7], s98 offen sc1\n"
"buffer_store_dword v247, v247, s[4:7], 0 offen\n"
"buffer_load_dwordx4 v[212:215], v0, s[4:7], s98 offen offset:1024 sc1\n"
"buffer_load_dwordx4 v[92:95], v0, s[4:7], s98 offen offset:2048 sc1\n"
"buffer_store_dword v247, v247, s[4:7], 0 offen\n"
"buffer_load_dwordx4 v[108:111], v0, s[4:7], s98 offen offset:3072 sc1\n"
"buffer_load_dwordx4 v[100:103], v1, s[4:7], s98 offen sc1\n"
"buffer_store_dword v247, v247, s[4:7], 0 offen\n"
"buffer_load_dwordx4 v[116:119], v1, s[4:7], s98 offen offset:1024 sc1\n"
"buffer_store_dword v247, v247, s[4:7], 0 offen\n"
"buffer_load_dwordx4 v[88:91], v1, s[4:7], s98 offen offset:2048 sc1\n"
"buffer_store_dword v247, v247, s[4:7], 0 offen\n"
"buffer_load_dwordx4 v[96:99], v1, s[4:7], s98 offen offset:3072 sc1\n"
"s_nop 7\n"
"s_branch .Lmk_p47r\n"
".Lmk_m52s:\n"
";\n"
"s_mov_b32 s1, 0x10000\n"
".Lmk_m52l:\n"
"s_add_u32 s97, s62, 0x44000\n"
"buffer_load_dwordx2 v[254:255], v10, s[4:7], s97 offen sc1\n"
"buffer_load_dwordx2 v[252:253], v10, s[4:7], s97 offen offset:128 sc1\n"
"buffer_load_dwordx2 v[250:251], v10, s[4:7], s97 offen offset:256 sc1\n"
"buffer_load_dwordx2 v[248:249], v10, s[4:7], s97 offen offset:384 sc1\n"
"s_waitcnt vmcnt(0)\n"
"v_max_u32_e32 v247, v254, v255\n"
"v_max3_u32 v247, v247, v252, v253\n"
"v_max3_u32 v247, v247, v250, v251\n"
"v_max3_u32 v247, v247, v248, v249\n"
"v_cmp_eq_u32_e32 vcc, -1, v247\n"
"s_cbranch_vccz .Lmk_m52k\n"
"s_sub_u32 s1, s1, 1\n"
"s_cbranch_scc0 .Lmk_m52l\n"
".Lmk_m52k:\n"
"v_add_f32_e32 v254, v254, v252\n"
"v_add_f32_e32 v255, v255, v253\n"
"v_add_f32_e32 v250, v250, v248\n"
"v_add_f32_e32 v251, v251, v249\n"
"v_add_f32_e32 v254, v254, v250\n"
"v_add_f32_e32 v255, v255, v251\n"
"s_nop 1\n"
"s_branch .Lmk_m52r\n"
".Lmk_p48s:\n"
";\n"
"s_mov_b32 s1, 0x10000\n"
".Lmk_p48l:\n"
"s_add_u32 s98, s53, 0x440000\n"
"buffer_load_dwordx4 v[136:139], v0, s[4:7], s98 offen sc1\n"
"buffer_load_dwordx4 v[176:179], v0, s[4:7], s98 offen offset:1024 sc1\n"
"buffer_load_dwordx4 v[140:143], v0, s[4:7], s98 offen offset:2048 sc1\n"
"buffer_load_dwordx4 v[144:147], v0, s[4:7], s98 offen offset:3072 sc1\n"
"buffer_load_dwordx4 v[148:151], v1, s[4:7], s98 offen sc1\n"
"buffer_load_dwordx4 v[152:155], v1, s[4:7], s98 offen offset:1024 sc1\n"
"buffer_load_dwordx4 v[200:203], v1, s[4:7], s98 offen offset:2048 sc1\n"
"buffer_load_dwordx4 v[168:171], v1, s[4:7], s98 offen offset:3072 sc1\n"
"s_add_u32 s98, s54, 0x440000\n"
"buffer_load_dwordx4 v[104:107], v0, s[4:7], s98 offen sc1\n"
"buffer_load_dwordx4 v[212:215], v0, s[4:7], s98 offen offset:1024 sc1\n"
"buffer_load_dwordx4 v[92:95], v0, s[4:7], s98 offen offset:2048 sc1\n"
"buffer_load_dwordx4 v[108:111], v0, s[4:7], s98 offen offset:3072 sc1\n"
"buffer_load_dwordx4 v[100:103], v1, s[4:7], s98 offen sc1\n"
"buffer_load_dwordx4 v[116:119], v1, s[4:7], s98 offen offset:1024 sc1\n"
"buffer_load_dwordx4 v[88:91], v1, s[4:7], s98 offen offset:2048 sc1\n"
"buffer_load_dwordx4 v[96:99], v1, s[4:7], s98 offen offset:3072 sc1\n"
"s_waitcnt vmcnt(0)\n"
"v_max_u32_e32 v252, v136, v137\n"
"v_max3_u32 v252, v252, v138, v139\n"
"v_max3_u32 v252, v252, v176, v177\n"
"v_max3_u32 v252, v252, v178, v179\n"
"v_max3_u32 v252, v252, v140, v141\n"
"v_max3_u32 v252, v252, v142, v143\n"
"v_max3_u32 v252, v252, v144, v145\n"
"v_max3_u32 v252, v252, v146, v147\n"
"v_max3_u32 v252, v252, v148, v149\n"
"v_max3_u32 v252, v252, v150, v151\n"
"v_max3_u32 v252, v252, v152, v153\n"
"v_max3_u32 v252, v252, v154, v155\n"
"v_max3_u32 v252, v252, v200, v201\n"
"v_max3_u32 v252, v252, v202, v203\n"
"v_max3_u32 v252, v252, v168, v169\n"
"v_max3_u32 v252, v252, v170, v171\n"
"v_cmp_eq_u32_e32 vcc, -1, v252\n"
"s_cbranch_vccz .Lmk_p48k\n"
"s_sub_u32 s1, s1, 1\n"
"s_cbranch_scc0 .Lmk_p48l\n"
".Lmk_p48k:\n"
"v_mfma_f32_16x16x32_bf16 v[124:127], a[0:3], v[136:139], 0\n"
"v_mfma_f32_16x16x32_bf16 v[128:131], a[4:7], v[136:139], 0\n"
"v_mfma_f32_16x16x32_bf16 v[120:123], a[8:11], v[136:139], 0\n"
"v_mfma_f32_16x16x32_bf16 v[132:135], a[12:15], v[136:139], 0\n"
"v_mfma_f32_16x16x32_bf16 v[156:159], a[16:19], v[136:139], 0\n"
"v_mfma_f32_16x16x32_bf16 v[164:167], a[20:23], v[136:139], 0\n"
"v_mfma_f32_16x16x32_bf16 v[124:127], a[24:27], v[176:179], v[124:127]\n"
"v_mfma_f32_16x16x32_bf16 v[128:131], a[28:31], v[176:179], v[128:131]\n"
"v_mfma_f32_16x16x32_bf16 v[120:123], a[32:35], v[176:179], v[120:123]\n"
"v_mfma_f32_16x16x32_bf16 v[132:135], a[36:39], v[176:179], v[132:135]\n"
"v_mfma_f32_16x16x32_bf16 v[156:159], a[40:43], v[176:179], v[156:159]\n"
"v_mfma_f32_16x16x32_bf16 v[164:167], a[44:47], v[176:179], v[164:167]\n"
"v_mfma_f32_16x16x32_bf16 v[124:127], a[48:51], v[140:143], v[124:127]\n"
"v_mfma_f32_16x16x32_bf16 v[128:131], a[52:55], v[140:143], v[128:131]\n"
"v_mfma_f32_16x16x32_bf16 v[120:123], a[56:59], v[140:143], v[120:123]\n"
"v_mfma_f32_16x16x32_bf16 v[132:135], a[60:63], v[140:143], v[132:135]\n"
"v_mfma_f32_16x16x32_bf16 v[156:159], a[64:67], v[140:143], v[156:159]\n"
"v_mfma_f32_16x16x32_bf16 v[164:167], a[68:71], v[140:143], v[164:167]\n"
"v_mfma_f32_16x16x32_bf16 v[124:127], a[72:75], v[144:147], v[124:127]\n"
"v_mfma_f32_16x16x32_bf16 v[128:131], a[76:79], v[144:147], v[128:131]\n"
"v_mfma_f32_16x16x32_bf16 v[120:123], a[80:83], v[144:147], v[120:123]\n"
"v_mfma_f32_16x16x32_bf16 v[132:135], a[84:87], v[144:147], v[132:135]\n"
"v_mfma_f32_16x16x32_bf16 v[156:159], a[88:91], v[144:147], v[156:159]\n"
"v_mfma_f32_16x16x32_bf16 v[164:167], a[92:95], v[144:147], v[164:167]\n"
"v_mfma_f32_16x16x32_bf16 v[124:127], a[96:99], v[148:151], v[124:127]\n"
"v_mfma_f32_16x16x32_bf16 v[128:131], a[100:103], v[148:151], v[128:131]\n"
"v_mfma_f32_16x16x32_bf16 v[120:123], a[104:107], v[148:151], v[120:123]\n"
"v_mfma_f32_16x16x32_bf16 v[132:135], a[108:111], v[148:151], v[132:135]\n"
"v_mfma_f32_16x16x32_bf16 v[156:159], a[112:115], v[148:151], v[156:159]\n"
"v_mfma_f32_16x16x32_bf16 v[164:167], a[116:119], v[148:151], v[164:167]\n"
"v_mfma_f32_16x16x32_bf16 v[124:127], a[120:123], v[152:155], v[124:127]\n"
"v_mfma_f32_16x16x32_bf16 v[128:131], a[124:127], v[152:155], v[128:131]\n"
"v_mfma_f32_16x16x32_bf16 v[120:123], v[24:27], v[152:155], v[120:123]\n"
"v_mfma_f32_16x16x32_bf16 v[132:135], v[28:31], v[152:155], v[132:135]\n"
"v_mfma_f32_16x16x32_bf16 v[156:159], v[32:35], v[152:155], v[156:159]\n"
"v_mfma_f32_16x16x32_bf16 v[164:167], v[36:39], v[152:155], v[164:167]\n"
"v_mfma_f32_16x16x32_bf16 v[124:127], v[40:43], v[200:203], v[124:127]\n"
"v_mfma_f32_16x16x32_bf16 v[128:131], v[44:47], v[200:203], v[128:131]\n"
"v_mfma_f32_16x16x32_bf16 v[120:123], v[48:51], v[200:203], v[120:123]\n"
"v_mfma_f32_16x16x32_bf16 v[132:135], v[52:55], v[200:203], v[132:135]\n"
"v_mfma_f32_16x16x32_bf16 v[156:159], v[56:59], v[200:203], v[156:159]\n"
"v_mfma_f32_16x16x32_bf16 v[164:167], v[60:63], v[200:203], v[164:167]\n"
"v_mfma_f32_16x16x32_bf16 v[124:127], v[64:67], v[168:171], v[124:127]\n"
"v_mfma_f32_16x16x32_bf16 v[128:131], v[68:71], v[168:171], v[128:131]\n"
"v_mfma_f32_16x16x32_bf16 v[120:123], v[72:75], v[168:171], v[120:123]\n"
"v_mfma_f32_16x16x32_bf16 v[132:135], v[76:79], v[168:171], v[132:135]\n"
"v_mfma_f32_16x16x32_bf16 v[156:159], v[80:83], v[168:171], v[156:159]\n"
"v_mfma_f32_16x16x32_bf16 v[164:167], v[84:87], v[168:171], v[164:167]\n"
"s_add_u32 s98, s55, 0x440000\n"
"v_mov_b32_e32 v252, 0x80000000\n"
"buffer_load_dwordx4 v[136:139], v0, s[4:7], s98 offen sc1\n"
"buffer_load_dwordx4 v[176:179], v0, s[4:7], s98 offen offset:1024 sc1\n"
"buffer_load_dwordx4 v[140:143], v0, s[4:7], s98 offen offset:2048 sc1\n"
"buffer_store_dword v252, v252, s[4:7], 0 offen\n"
"buffer_load_dwordx4 v[144:147], v0, s[4:7], s98 offen offset:3072 sc1\n"
"buffer_load_dwordx4 v[148:151], v1, s[4:7], s98 offen sc1\n"
"buffer_load_dwordx4 v[152:155], v1, s[4:7], s98 offen offset:1024 sc1\n"
"buffer_load_dwordx4 v[200:203], v1, s[4:7], s98 offen offset:2048 sc1\n"
"buffer_store_dword v252, v252, s[4:7], 0 offen\n"
"buffer_load_dwordx4 v[168:171], v1, s[4:7], s98 offen offset:3072 sc1\n"
"s_nop 7\n"
"s_branch .Lmk_p48r\n"
".Lmk_q50s:\n"
";\n"
"s_mov_b32 s1, 0x10000\n"
".Lmk_q50l:\n"
"s_add_u32 s96, s55, 0x440000\n"
"buffer_load_dwordx4 v[136:139], v0, s[4:7], s96 offen sc1\n"
"buffer_load_dwordx4 v[176:179], v0, s[4:7], s96 offen offset:1024 sc1\n"
"buffer_load_dwordx4 v[140:143], v0, s[4:7], s96 offen offset:2048 sc1\n"
"buffer_load_dwordx4 v[144:147], v0, s[4:7], s96 offen offset:3072 sc1\n"
"buffer_load_dwordx4 v[148:151], v1, s[4:7], s96 offen sc1\n"
"buffer_load_dwordx4 v[152:155], v1, s[4:7], s96 offen offset:1024 sc1\n"
"buffer_load_dwordx4 v[200:203], v1, s[4:7], s96 offen offset:2048 sc1\n"
"buffer_load_dwordx4 v[168:171], v1, s[4:7], s96 offen offset:3072 sc1\n"
"s_add_u32 s96, s52, 0x448000\n"
"buffer_load_dwordx4 v[104:107], v0, s[4:7], s96 offen sc1\n"
"buffer_load_dwordx4 v[120:123], v0, s[4:7], s96 offen offset:1024 sc1\n"
"buffer_load_dwordx4 v[128:131], v0, s[4:7], s96 offen offset:2048 sc1\n"
"buffer_load_dwordx4 v[196:199], v0, s[4:7], s96 offen offset:3072 sc1\n"
"buffer_load_dwordx4 v[92:95], v1, s[4:7], s96 offen sc1\n"
"buffer_load_dwordx4 v[100:103], v1, s[4:7], s96 offen offset:1024 sc1\n"
"s_waitcnt vmcnt(0)\n"
"v_max_u32_e32 v254, v136, v137\n"
"v_max3_u32 v254, v254, v138, v139\n"
"v_max3_u32 v254, v254, v176, v177\n"
"v_max3_u32 v254, v254, v178, v179\n"
"v_max3_u32 v254, v254, v140, v141\n"
"v_max3_u32 v254, v254, v142, v143\n"
"v_max3_u32 v254, v254, v144, v145\n"
"v_max3_u32 v254, v254, v146, v147\n"
"v_cmp_eq_u32_e32 vcc, -1, v254\n"
"s_cbranch_vccz .Lmk_q50k\n"
"s_sub_u32 s1, s1, 1\n"
"s_cbranch_scc0 .Lmk_q50l\n"
".Lmk_q50k:\n"
"s_branch .Lmk_q50r\n"
".Lmk_p49s:\n"
";\n"
"s_mov_b32 s1, 0x10000\n"
".Lmk_p49l:\n"
"s_add_u32 s96, s54, 0x440000\n"
"buffer_load_dwordx4 v[88:91], v0, s[4:7], s96 offen sc1\n"
"buffer_load_dwordx4 v[96:99], v0, s[4:7], s96 offen offset:1024 sc1\n"
"buffer_load_dwordx4 v[108:111], v0, s[4:7], s96 offen offset:2048 sc1\n"
"buffer_load_dwordx4 v[116:119], v0, s[4:7], s96 offen offset:3072 sc1\n"
"buffer_load_dwordx4 v[156:159], v1, s[4:7], s96 offen sc1\n"
"buffer_load_dwordx4 v[188:191], v1, s[4:7], s96 offen offset:1024 sc1\n"
"buffer_load_dwordx4 v[212:215], v1, s[4:7], s96 offen offset:2048 sc1\n"
"buffer_load_dwordx4 v[220:223], v1, s[4:7], s96 offen offset:3072 sc1\n"
"s_add_u32 s96, s55, 0x440000\n"
"buffer_load_dwordx4 v[136:139], v0, s[4:7], s96 offen sc1\n"
"buffer_load_dwordx4 v[176:179], v0, s[4:7], s96 offen offset:1024 sc1\n"
"buffer_load_dwordx4 v[140:143], v0, s[4:7], s96 offen offset:2048 sc1\n"
"buffer_load_dwordx4 v[144:147], v0, s[4:7], s96 offen offset:3072 sc1\n"
"buffer_load_dwordx4 v[148:151], v1, s[4:7], s96 offen sc1\n"
"buffer_load_dwordx4 v[152:155], v1, s[4:7], s96 offen offset:1024 sc1\n"
"buffer_load_dwordx4 v[200:203], v1, s[4:7], s96 offen offset:2048 sc1\n"
"buffer_load_dwordx4 v[168:171], v1, s[4:7], s96 offen offset:3072 sc1\n"
"s_add_u32 s96, s52, 0x448000\n"
"buffer_load_dwordx4 v[104:107], v0, s[4:7], s96 offen sc1\n"
"buffer_load_dwordx4 v[120:123], v0, s[4:7], s96 offen offset:1024 sc1\n"
"buffer_load_dwordx4 v[128:131], v0, s[4:7], s96 offen offset:2048 sc1\n"
"buffer_load_dwordx4 v[196:199], v0, s[4:7], s96 offen offset:3072 sc1\n"
"buffer_load_dwordx4 v[92:95], v1, s[4:7], s96 offen sc1\n"
"buffer_load_dwordx4 v[100:103], v1, s[4:7], s96 offen offset:1024 sc1\n"
"s_waitcnt vmcnt(0)\n"
"v_max_u32_e32 v254, v88, v89\n"
"v_max3_u32 v254, v254, v90, v91\n"
"v_max3_u32 v254, v254, v96, v97\n"
"v_max3_u32 v254, v254, v98, v99\n"
"v_max3_u32 v254, v254, v108, v109\n"
"v_max3_u32 v254, v254, v110, v111\n"
"v_max3_u32 v254, v254, v116, v117\n"
"v_max3_u32 v254, v254, v118, v119\n"
"v_max3_u32 v254, v254, v156, v157\n"
"v_max3_u32 v254, v254, v158, v159\n"
"v_max3_u32 v254, v254, v188, v189\n"
"v_max3_u32 v254, v254, v190, v191\n"
"v_max3_u32 v254, v254, v212, v213\n"
"v_max3_u32 v254, v254, v214, v215\n"
"v_max3_u32 v254, v254, v220, v221\n"
"v_max3_u32 v254, v254, v222, v223\n"
"v_cmp_eq_u32_e32 vcc, -1, v254\n"
"s_cbranch_vccz .Lmk_p49k\n"
"s_sub_u32 s1, s1, 1\n"
"s_cbranch_scc0 .Lmk_p49l\n"
".Lmk_p49k:\n"
"v_mfma_f32_16x16x32_bf16 v[172:175], a[0:3], v[88:91], 0\n"
"v_mfma_f32_16x16x32_bf16 v[204:207], a[4:7], v[88:91], 0\n"
"v_mfma_f32_16x16x32_bf16 v[180:183], a[8:11], v[88:91], 0\n"
"v_mfma_f32_16x16x32_bf16 v[208:211], a[12:15], v[88:91], 0\n"
"v_mfma_f32_16x16x32_bf16 v[124:127], a[16:19], v[88:91], 0\n"
"v_mfma_f32_16x16x32_bf16 v[216:219], a[20:23], v[88:91], 0\n"
"v_mfma_f32_16x16x32_bf16 v[172:175], a[24:27], v[96:99], v[172:175]\n"
"v_mfma_f32_16x16x32_bf16 v[204:207], a[28:31], v[96:99], v[204:207]\n"
"v_mfma_f32_16x16x32_bf16 v[180:183], a[32:35], v[96:99], v[180:183]\n"
"v_mfma_f32_16x16x32_bf16 v[208:211], a[36:39], v[96:99], v[208:211]\n"
"v_mfma_f32_16x16x32_bf16 v[124:127], a[40:43], v[96:99], v[124:127]\n"
"v_mfma_f32_16x16x32_bf16 v[216:219], a[44:47], v[96:99], v[216:219]\n"
"v_mfma_f32_16x16x32_bf16 v[172:175], a[48:51], v[108:111], v[172:175]\n"
"v_mfma_f32_16x16x32_bf16 v[204:207], a[52:55], v[108:111], v[204:207]\n"
"v_mfma_f32_16x16x32_bf16 v[180:183], a[56:59], v[108:111], v[180:183]\n"
"v_mfma_f32_16x16x32_bf16 v[208:211], a[60:63], v[108:111], v[208:211]\n"
"v_mfma_f32_16x16x32_bf16 v[124:127], a[64:67], v[108:111], v[124:127]\n"
"v_mfma_f32_16x16x32_bf16 v[216:219], a[68:71], v[108:111], v[216:219]\n"
"v_mfma_f32_16x16x32_bf16 v[172:175], a[72:75], v[116:119], v[172:175]\n"
"v_mfma_f32_16x16x32_bf16 v[204:207], a[76:79], v[116:119], v[204:207]\n"
"v_mfma_f32_16x16x32_bf16 v[180:183], a[80:83], v[116:119], v[180:183]\n"
"v_mfma_f32_16x16x32_bf16 v[208:211], a[84:87], v[116:119], v[208:211]\n"
"v_mfma_f32_16x16x32_bf16 v[124:127], a[88:91], v[116:119], v[124:127]\n"
"v_mfma_f32_16x16x32_bf16 v[216:219], a[92:95], v[116:119], v[216:219]\n"
"v_mfma_f32_16x16x32_bf16 v[172:175], a[96:99], v[156:159], v[172:175]\n"
"v_mfma_f32_16x16x32_bf16 v[204:207], a[100:103], v[156:159], v[204:207]\n"
"v_mfma_f32_16x16x32_bf16 v[180:183], a[104:107], v[156:159], v[180:183]\n"
"v_mfma_f32_16x16x32_bf16 v[208:211], a[108:111], v[156:159], v[208:211]\n"
"v_mfma_f32_16x16x32_bf16 v[124:127], a[112:115], v[156:159], v[124:127]\n"
"v_mfma_f32_16x16x32_bf16 v[216:219], a[116:119], v[156:159], v[216:219]\n"
"v_mfma_f32_16x16x32_bf16 v[172:175], a[120:123], v[188:191], v[172:175]\n"
"v_mfma_f32_16x16x32_bf16 v[204:207], a[124:127], v[188:191], v[204:207]\n"
"v_mfma_f32_16x16x32_bf16 v[180:183], v[24:27], v[188:191], v[180:183]\n"
"v_mfma_f32_16x16x32_bf16 v[208:211], v[28:31], v[188:191], v[208:211]\n"
"v_mfma_f32_16x16x32_bf16 v[124:127], v[32:35], v[188:191], v[124:127]\n"
"v_mfma_f32_16x16x32_bf16 v[216:219], v[36:39], v[188:191], v[216:219]\n"
"v_mfma_f32_16x16x32_bf16 v[172:175], v[40:43], v[212:215], v[172:175]\n"
"v_mfma_f32_16x16x32_bf16 v[204:207], v[44:47], v[212:215], v[204:207]\n"
"v_mfma_f32_16x16x32_bf16 v[180:183], v[48:51], v[212:215], v[180:183]\n"
"v_mfma_f32_16x16x32_bf16 v[208:211], v[52:55], v[212:215], v[208:211]\n"
"v_mfma_f32_16x16x32_bf16 v[124:127], v[56:59], v[212:215], v[124:127]\n"
"v_mfma_f32_16x16x32_bf16 v[216:219], v[60:63], v[212:215], v[216:219]\n"
"v_mfma_f32_16x16x32_bf16 v[172:175], v[64:67], v[220:223], v[172:175]\n"
"v_mfma_f32_16x16x32_bf16 v[204:207], v[68:71], v[220:223], v[204:207]\n"
"v_mfma_f32_16x16x32_bf16 v[180:183], v[72:75], v[220:223], v[180:183]\n"
"v_mfma_f32_16x16x32_bf16 v[208:211], v[76:79], v[220:223], v[208:211]\n"
"v_mfma_f32_16x16x32_bf16 v[124:127], v[80:83], v[220:223], v[124:127]\n"
"v_mfma_f32_16x16x32_bf16 v[216:219], v[84:87], v[220:223], v[216:219]\n"
"s_nop 7\n"
"s_branch .Lmk_p49r\n"
".Lmk_q51s:\n"
";\n"
"s_mov_b32 s1, 0x10000\n"
".Lmk_q51l:\n"
"s_add_u32 s97, s55, 0x440000\n"
"buffer_load_dwordx4 v[148:151], v1, s[4:7], s97 offen sc1\n"
"buffer_load_dwordx4 v[152:155], v1, s[4:7], s97 offen offset:1024 sc1\n"
"buffer_load_dwordx4 v[200:203], v1, s[4:7], s97 offen offset:2048 sc1\n"
"buffer_load_dwordx4 v[168:171], v1, s[4:7], s97 offen offset:3072 sc1\n"
"s_add_u32 s97, s52, 0x448000\n"
"buffer_load_dwordx4 v[104:107], v0, s[4:7], s97 offen sc1\n"
"buffer_load_dwordx4 v[120:123], v0, s[4:7], s97 offen offset:1024 sc1\n"
"buffer_load_dwordx4 v[128:131], v0, s[4:7], s97 offen offset:2048 sc1\n"
"buffer_load_dwordx4 v[196:199], v0, s[4:7], s97 offen offset:3072 sc1\n"
"buffer_load_dwordx4 v[92:95], v1, s[4:7], s97 offen sc1\n"
"buffer_load_dwordx4 v[100:103], v1, s[4:7], s97 offen offset:1024 sc1\n"
"buffer_load_dwordx4 v[88:91], v1, s[4:7], s97 offen offset:2048 sc1\n"
"buffer_load_dwordx4 v[96:99], v1, s[4:7], s97 offen offset:3072 sc1\n"
"s_add_u32 s97, s53, 0x448000\n"
"buffer_load_dwordx4 v[108:111], v0, s[4:7], s97 offen sc1\n"
"buffer_load_dwordx4 v[116:119], v0, s[4:7], s97 offen offset:1024 sc1\n"
"s_waitcnt vmcnt(0)\n"
"v_max_u32_e32 v254, v148, v149\n"
"v_max3_u32 v254, v254, v150, v151\n"
"v_max3_u32 v254, v254, v152, v153\n"
"v_max3_u32 v254, v254, v154, v155\n"
"v_max3_u32 v254, v254, v200, v201\n"
"v_max3_u32 v254, v254, v202, v203\n"
"v_max3_u32 v254, v254, v168, v169\n"
"v_max3_u32 v254, v254, v170, v171\n"
"v_cmp_eq_u32_e32 vcc, -1, v254\n"
"s_cbranch_vccz .Lmk_q51k\n"
"s_sub_u32 s1, s1, 1\n"
"s_cbranch_scc0 .Lmk_q51l\n"
".Lmk_q51k:\n"
"s_branch .Lmk_q51r\n"
".Lmk_p53s:\n"
";\n"
"s_mov_b32 s1, 0x10000\n"
".Lmk_p53l:\n"
"s_add_u32 s99, s52, 0x448000\n"
"buffer_load_dwordx4 v[104:107], v0, s[4:7], s99 offen sc1\n"
"buffer_load_dwordx4 v[144:147], v0, s[4:7], s99 offen offset:1024 sc1\n"
"buffer_load_dwordx4 v[120:123], v0, s[4:7], s99 offen offset:2048 sc1\n"
"buffer_load_dwordx4 v[112:115], v0, s[4:7], s99 offen offset:3072 sc1\n"
"buffer_load_dwordx4 v[128:131], v1, s[4:7], s99 offen sc1\n"
"buffer_load_dwordx4 v[132:135], v1, s[4:7], s99 offen offset:1024 sc1\n"
"buffer_load_dwordx4 v[92:95], v1, s[4:7], s99 offen offset:2048 sc1\n"
"buffer_load_dwordx4 v[88:91], v1, s[4:7], s99 offen offset:3072 sc1\n"
"s_add_u32 s99, s53, 0x448000\n"
"buffer_load_dwordx4 v[108:111], v0, s[4:7], s99 offen sc1\n"
"buffer_load_dwordx4 v[116:119], v0, s[4:7], s99 offen offset:1024 sc1\n"
"buffer_load_dwordx4 v[124:127], v0, s[4:7], s99 offen offset:2048 sc1\n"
"buffer_load_dwordx4 v[136:139], v0, s[4:7], s99 offen offset:3072 sc1\n"
"buffer_load_dwordx4 v[140:143], v1, s[4:7], s99 offen sc1\n"
"buffer_load_dwordx4 v[176:179], v1, s[4:7], s99 offen offset:1024 sc1\n"
"buffer_load_dwordx4 v[148:151], v1, s[4:7], s99 offen offset:2048 sc1\n"
"buffer_load_dwordx4 v[172:175], v1, s[4:7], s99 offen offset:3072 sc1\n"
"s_waitcnt vmcnt(0)\n"
"v_max_u32_e32 v255, v104, v105\n"
"v_max3_u32 v255, v255, v106, v107\n"
"v_max3_u32 v255, v255, v144, v145\n"
"v_max3_u32 v255, v255, v146, v147\n"
"v_max3_u32 v255, v255, v120, v121\n"
"v_max3_u32 v255, v255, v122, v123\n"
"v_max3_u32 v255, v255, v112, v113\n"
"v_max3_u32 v255, v255, v114, v115\n"
"v_max3_u32 v255, v255, v128, v129\n"
"v_max3_u32 v255, v255, v130, v131\n"
"v_max3_u32 v255, v255, v132, v133\n"
"v_max3_u32 v255, v255, v134, v135\n"
"v_max3_u32 v255, v255, v92, v93\n"
"v_max3_u32 v255, v255, v94, v95\n"
"v_max3_u32 v255, v255, v88, v89\n"
"v_max3_u32 v255, v255, v90, v91\n"
"v_cmp_eq_u32_e32 vcc, -1, v255\n"
"s_cbranch_vccz .Lmk_p53k\n"
"s_sub_u32 s1, s1, 1\n"
"s_cbranch_scc0 .Lmk_p53l\n"
".Lmk_p53k:\n"
"v_mfma_f32_16x16x32_bf16 v[168:171], a[0:3], v[104:107], 0\n"
"v_mfma_f32_16x16x32_bf16 v[180:183], a[4:7], v[104:107], 0\n"
"v_mfma_f32_16x16x32_bf16 v[204:207], a[8:11], v[104:107], 0\n"
"v_mfma_f32_16x16x32_bf16 v[208:211], a[12:15], v[104:107], 0\n"
"v_mfma_f32_16x16x32_bf16 v[216:219], a[16:19], v[104:107], 0\n"
"v_mfma_f32_16x16x32_bf16 v[220:223], a[20:23], v[104:107], 0\n"
"v_mfma_f32_16x16x32_bf16 v[168:171], a[24:27], v[144:147], v[168:171]\n"
"v_mfma_f32_16x16x32_bf16 v[180:183], a[28:31], v[144:147], v[180:183]\n"
"v_mfma_f32_16x16x32_bf16 v[204:207], a[32:35], v[144:147], v[204:207]\n"
"v_mfma_f32_16x16x32_bf16 v[208:211], a[36:39], v[144:147], v[208:211]\n"
"v_mfma_f32_16x16x32_bf16 v[216:219], a[40:43], v[144:147], v[216:219]\n"
"v_mfma_f32_16x16x32_bf16 v[220:223], a[44:47], v[144:147], v[220:223]\n"
"v_mfma_f32_16x16x32_bf16 v[168:171], a[48:51], v[120:123], v[168:171]\n"
"v_mfma_f32_16x16x32_bf16 v[180:183], a[52:55], v[120:123], v[180:183]\n"
"v_mfma_f32_16x16x32_bf16 v[204:207], a[56:59], v[120:123], v[204:207]\n"
"v_mfma_f32_16x16x32_bf16 v[208:211], a[60:63], v[120:123], v[208:211]\n"
"v_mfma_f32_16x16x32_bf16 v[216:219], a[64:67], v[120:123], v[216:219]\n"
"v_mfma_f32_16x16x32_bf16 v[220:223], a[68:71], v[120:123], v[220:223]\n"
"v_mfma_f32_16x16x32_bf16 v[168:171], a[72:75], v[112:115], v[168:171]\n"
"v_mfma_f32_16x16x32_bf16 v[180:183], a[76:79], v[112:115], v[180:183]\n"
"v_mfma_f32_16x16x32_bf16 v[204:207], a[80:83], v[112:115], v[204:207]\n"
"v_mfma_f32_16x16x32_bf16 v[208:211], a[84:87], v[112:115], v[208:211]\n"
"v_mfma_f32_16x16x32_bf16 v[216:219], a[88:91], v[112:115], v[216:219]\n"
"v_mfma_f32_16x16x32_bf16 v[220:223], a[92:95], v[112:115], v[220:223]\n"
"v_mfma_f32_16x16x32_bf16 v[168:171], a[96:99], v[128:131], v[168:171]\n"
"v_mfma_f32_16x16x32_bf16 v[180:183], a[100:103], v[128:131], v[180:183]\n"
"v_mfma_f32_16x16x32_bf16 v[204:207], a[104:107], v[128:131], v[204:207]\n"
"v_mfma_f32_16x16x32_bf16 v[208:211], a[108:111], v[128:131], v[208:211]\n"
"v_mfma_f32_16x16x32_bf16 v[216:219], a[112:115], v[128:131], v[216:219]\n"
"v_mfma_f32_16x16x32_bf16 v[220:223], a[116:119], v[128:131], v[220:223]\n"
"v_mfma_f32_16x16x32_bf16 v[168:171], a[120:123], v[132:135], v[168:171]\n"
"v_mfma_f32_16x16x32_bf16 v[180:183], a[124:127], v[132:135], v[180:183]\n"
"v_mfma_f32_16x16x32_bf16 v[204:207], v[24:27], v[132:135], v[204:207]\n"
"v_mfma_f32_16x16x32_bf16 v[208:211], v[28:31], v[132:135], v[208:211]\n"
"v_mfma_f32_16x16x32_bf16 v[216:219], v[32:35], v[132:135], v[216:219]\n"
"v_mfma_f32_16x16x32_bf16 v[220:223], v[36:39], v[132:135], v[220:223]\n"
"v_mfma_f32_16x16x32_bf16 v[168:171], v[40:43], v[92:95], v[168:171]\n"
"v_mfma_f32_16x16x32_bf16 v[180:183], v[44:47], v[92:95], v[180:183]\n"
"v_mfma_f32_16x16x32_bf16 v[204:207], v[48:51], v[92:95], v[204:207]\n"
"v_mfma_f32_16x16x32_bf16 v[208:211], v[52:55], v[92:95], v[208:211]\n"
"v_mfma_f32_16x16x32_bf16 v[216:219], v[56:59], v[92:95], v[216:219]\n"
"v_mfma_f32_16x16x32_bf16 v[220:223], v[60:63], v[92:95], v[220:223]\n"
"v_mfma_f32_16x16x32_bf16 v[168:171], v[64:67], v[88:91], v[168:171]\n"
"v_mfma_f32_16x16x32_bf16 v[180:183], v[68:71], v[88:91], v[180:183]\n"
"v_mfma_f32_16x16x32_bf16 v[204:207], v[72:75], v[88:91], v[204:207]\n"
"v_mfma_f32_16x16x32_bf16 v[208:211], v[76:79], v[88:91], v[208:211]\n"
"v_mfma_f32_16x16x32_bf16 v[216:219], v[80:83], v[88:91], v[216:219]\n"
"v_mfma_f32_16x16x32_bf16 v[220:223], v[84:87], v[88:91], v[220:223]\n"
"s_add_u32 s99, s54, 0x448000\n"
"v_mov_b32_e32 v255, 0x80000000\n"
"buffer_load_dwordx4 v[104:107], v0, s[4:7], s99 offen sc1\n"
"buffer_store_dword v255, v255, s[4:7], 0 offen\n"
"buffer_store_dword v255, v255, s[4:7], 0 offen\n"
"buffer_load_dwordx4 v[144:147], v0, s[4:7], s99 offen offset:1024 sc1\n"
"buffer_load_dwordx4 v[120:123], v0, s[4:7], s99 offen offset:2048 sc1\n"
"buffer_load_dwordx4 v[112:115], v0, s[4:7], s99 offen offset:3072 sc1\n"
"buffer_load_dwordx4 v[128:131], v1, s[4:7], s99 offen sc1\n"
"buffer_store_dword v255, v255, s[4:7], 0 offen\n"
"buffer_load_dwordx4 v[132:135], v1, s[4:7], s99 offen offset:1024 sc1\n"
"buffer_load_dwordx4 v[92:95], v1, s[4:7], s99 offen offset:2048 sc1\n"
"buffer_load_dwordx4 v[88:91], v1, s[4:7], s99 offen offset:3072 sc1\n"
"s_nop 7\n"
"s_branch .Lmk_p53r\n"
".Lmk_m58s:\n"
";\n"
"s_mov_b32 s1, 0x10000\n"
".Lmk_m58l:\n"
"s_add_u32 s96, s62, 0x44800\n"
"buffer_load_dwordx2 v[250:251], v10, s[4:7], s96 offen sc1\n"
"buffer_load_dwordx2 v[248:249], v10, s[4:7], s96 offen offset:128 sc1\n"
"buffer_load_dwordx2 v[246:247], v10, s[4:7], s96 offen offset:256 sc1\n"
"buffer_load_dwordx2 v[244:245], v10, s[4:7], s96 offen offset:384 sc1\n"
"s_waitcnt vmcnt(0)\n"
"v_max_u32_e32 v253, v250, v251\n"
"v_max3_u32 v253, v253, v248, v249\n"
"v_max3_u32 v253, v253, v246, v247\n"
"v_max3_u32 v253, v253, v244, v245\n"
"v_cmp_eq_u32_e32 vcc, -1, v253\n"
"s_cbranch_vccz .Lmk_m58k\n"
"s_sub_u32 s1, s1, 1\n"
"s_cbranch_scc0 .Lmk_m58l\n"
".Lmk_m58k:\n"
"v_add_f32_e32 v250, v250, v248\n"
"v_add_f32_e32 v251, v251, v249\n"
"v_add_f32_e32 v246, v246, v244\n"
"v_add_f32_e32 v247, v247, v245\n"
"v_add_f32_e32 v250, v250, v246\n"
"v_add_f32_e32 v251, v251, v247\n"
"s_nop 1\n"
"s_branch .Lmk_m58r\n"
".Lmk_p54s:\n"
";\n"
"s_mov_b32 s1, 0x10000\n"
".Lmk_p54l:\n"
"s_add_u32 s99, s53, 0x448000\n"
"buffer_load_dwordx4 v[108:111], v0, s[4:7], s99 offen sc1\n"
"buffer_load_dwordx4 v[180:183], v0, s[4:7], s99 offen offset:1024 sc1\n"
"buffer_load_dwordx4 v[116:119], v0, s[4:7], s99 offen offset:2048 sc1\n"
"buffer_load_dwordx4 v[124:127], v0, s[4:7], s99 offen offset:3072 sc1\n"
"buffer_load_dwordx4 v[136:139], v1, s[4:7], s99 offen sc1\n"
"buffer_load_dwordx4 v[140:143], v1, s[4:7], s99 offen offset:1024 sc1\n"
"buffer_load_dwordx4 v[184:187], v1, s[4:7], s99 offen offset:2048 sc1\n"
"buffer_load_dwordx4 v[192:195], v1, s[4:7], s99 offen offset:3072 sc1\n"
"s_add_u32 s99, s54, 0x448000\n"
"buffer_load_dwordx4 v[104:107], v0, s[4:7], s99 offen sc1\n"
"buffer_load_dwordx4 v[144:147], v0, s[4:7], s99 offen offset:1024 sc1\n"
"buffer_load_dwordx4 v[120:123], v0, s[4:7], s99 offen offset:2048 sc1\n"
"buffer_load_dwordx4 v[112:115], v0, s[4:7], s99 offen offset:3072 sc1\n"
"buffer_load_dwordx4 v[128:131], v1, s[4:7], s99 offen sc1\n"
"buffer_load_dwordx4 v[132:135], v1, s[4:7], s99 offen offset:1024 sc1\n"
"buffer_load_dwordx4 v[92:95], v1, s[4:7], s99 offen offset:2048 sc1\n"
"buffer_load_dwordx4 v[88:91], v1, s[4:7], s99 offen offset:3072 sc1\n"
"s_waitcnt vmcnt(0)\n"
"v_max_u32_e32 v252, v108, v109\n"
"v_max3_u32 v252, v252, v110, v111\n"
"v_max3_u32 v252, v252, v180, v181\n"
"v_max3_u32 v252, v252, v182, v183\n"
"v_max3_u32 v252, v252, v116, v117\n"
"v_max3_u32 v252, v252, v118, v119\n"
"v_max3_u32 v252, v252, v124, v125\n"
"v_max3_u32 v252, v252, v126, v127\n"
"v_max3_u32 v252, v252, v136, v137\n"
"v_max3_u32 v252, v252, v138, v139\n"
"v_max3_u32 v252, v252, v140, v141\n"
"v_max3_u32 v252, v252, v142, v143\n"
"v_max3_u32 v252, v252, v184, v185\n"
"v_max3_u32 v252, v252, v186, v187\n"
"v_max3_u32 v252, v252, v192, v193\n"
"v_max3_u32 v252, v252, v194, v195\n"
"v_cmp_eq_u32_e32 vcc, -1, v252\n"
"s_cbranch_vccz .Lmk_p54k\n"
"s_sub_u32 s1, s1, 1\n"
"s_cbranch_scc0 .Lmk_p54l\n"
".Lmk_p54k:\n"
"v_mfma_f32_16x16x32_bf16 v[96:99], a[0:3], v[108:111], 0\n"
"v_mfma_f32_16x16x32_bf16 v[152:155], a[4:7], v[108:111], 0\n"
"v_mfma_f32_16x16x32_bf16 v[156:159], a[8:11], v[108:111], 0\n"
"v_mfma_f32_16x16x32_bf16 v[160:163], a[12:15], v[108:111], 0\n"
"v_mfma_f32_16x16x32_bf16 v[164:167], a[16:19], v[108:111], 0\n"
"v_mfma_f32_16x16x32_bf16 v[168:171], a[20:23], v[108:111], 0\n"
"v_mfma_f32_16x16x32_bf16 v[96:99], a[24:27], v[180:183], v[96:99]\n"
"v_mfma_f32_16x16x32_bf16 v[152:155], a[28:31], v[180:183], v[152:155]\n"
"v_mfma_f32_16x16x32_bf16 v[156:159], a[32:35], v[180:183], v[156:159]\n"
"v_mfma_f32_16x16x32_bf16 v[160:163], a[36:39], v[180:183], v[160:163]\n"
"v_mfma_f32_16x16x32_bf16 v[164:167], a[40:43], v[180:183], v[164:167]\n"
"v_mfma_f32_16x16x32_bf16 v[168:171], a[44:47], v[180:183], v[168:171]\n"
"v_mfma_f32_16x16x32_bf16 v[96:99], a[48:51], v[116:119], v[96:99]\n"
"v_mfma_f32_16x16x32_bf16 v[152:155], a[52:55], v[116:119], v[152:155]\n"
"v_mfma_f32_16x16x32_bf16 v[156:159], a[56:59], v[116:119], v[156:159]\n"
"v_mfma_f32_16x16x32_bf16 v[160:163], a[60:63], v[116:119], v[160:163]\n"
"v_mfma_f32_16x16x32_bf16 v[164:167], a[64:67], v[116:119], v[164:167]\n"
"v_mfma_f32_16x16x32_bf16 v[168:171], a[68:71], v[116:119], v[168:171]\n"
"v_mfma_f32_16x16x32_bf16 v[96:99], a[72:75], v[124:127], v[96:99]\n"
"v_mfma_f32_16x16x32_bf16 v[152:155], a[76:79], v[124:127], v[152:155]\n"
"v_mfma_f32_16x16x32_bf16 v[156:159], a[80:83], v[124:127], v[156:159]\n"
"v_mfma_f32_16x16x32_bf16 v[160:163], a[84:87], v[124:127], v[160:163]\n"
"v_mfma_f32_16x16x32_bf16 v[164:167], a[88:91], v[124:127], v[164:167]\n"
"v_mfma_f32_16x16x32_bf16 v[168:171], a[92:95], v[124:127], v[168:171]\n"
"v_mfma_f32_16x16x32_bf16 v[96:99], a[96:99], v[136:139], v[96:99]\n"
"v_mfma_f32_16x16x32_bf16 v[152:155], a[100:103], v[136:139], v[152:155]\n"
"v_mfma_f32_16x16x32_bf16 v[156:159], a[104:107], v[136:139], v[156:159]\n"
"v_mfma_f32_16x16x32_bf16 v[160:163], a[108:111], v[136:139], v[160:163]\n"
"v_mfma_f32_16x16x32_bf16 v[164:167], a[112:115], v[136:139], v[164:167]\n"
"v_mfma_f32_16x16x32_bf16 v[168:171], a[116:119], v[136:139], v[168:171]\n"
"v_mfma_f32_16x16x32_bf16 v[96:99], a[120:123], v[140:143], v[96:99]\n"
"v_mfma_f32_16x16x32_bf16 v[152:155], a[124:127], v[140:143], v[152:155]\n"
"v_mfma_f32_16x16x32_bf16 v[156:159], v[24:27], v[140:143], v[156:159]\n"
"v_mfma_f32_16x16x32_bf16 v[160:163], v[28:31], v[140:143], v[160:163]\n"
"v_mfma_f32_16x16x32_bf16 v[164:167], v[32:35], v[140:143], v[164:167]\n"
"v_mfma_f32_16x16x32_bf16 v[168:171], v[36:39], v[140:143], v[168:171]\n"
"v_mfma_f32_16x16x32_bf16 v[96:99], v[40:43], v[184:187], v[96:99]\n"
"v_mfma_f32_16x16x32_bf16 v[152:155], v[44:47], v[184:187], v[152:155]\n"
"v_mfma_f32_16x16x32_bf16 v[156:159], v[48:51], v[184:187], v[156:159]\n"
"v_mfma_f32_16x16x32_bf16 v[160:163], v[52:55], v[184:187], v[160:163]\n"
"v_mfma_f32_16x16x32_bf16 v[164:167], v[56:59], v[184:187], v[164:167]\n"
"v_mfma_f32_16x16x32_bf16 v[168:171], v[60:63], v[184:187], v[168:171]\n"
"v_mfma_f32_16x16x32_bf16 v[96:99], v[64:67], v[192:195], v[96:99]\n"
"v_mfma_f32_16x16x32_bf16 v[152:155], v[68:71], v[192:195], v[152:155]\n"
"v_mfma_f32_16x16x32_bf16 v[156:159], v[72:75], v[192:195], v[156:159]\n"
"v_mfma_f32_16x16x32_bf16 v[160:163], v[76:79], v[192:195], v[160:163]\n"
"v_mfma_f32_16x16x32_bf16 v[164:167], v[80:83], v[192:195], v[164:167]\n"
"v_mfma_f32_16x16x32_bf16 v[168:171], v[84:87], v[192:195], v[168:171]\n"
"s_add_u32 s99, s55, 0x448000\n"
"v_mov_b32_e32 v252, 0x80000000\n"
"buffer_load_dwordx4 v[108:111], v0, s[4:7], s99 offen sc1\n"
"buffer_store_dword v252, v252, s[4:7], 0 offen\n"
"buffer_load_dwordx4 v[180:183], v0, s[4:7], s99 offen offset:1024 sc1\n"
"buffer_store_dword v252, v252, s[4:7], 0 offen\n"
"buffer_load_dwordx4 v[116:119], v0, s[4:7], s99 offen offset:2048 sc1\n"
"buffer_load_dwordx4 v[124:127], v0, s[4:7], s99 offen offset:3072 sc1\n"
"buffer_store_dword v252, v252, s[4:7], 0 offen\n"
"buffer_load_dwordx4 v[136:139], v1, s[4:7], s99 offen sc1\n"
"buffer_store_dword v252, v252, s[4:7], 0 offen\n"
"buffer_load_dwordx4 v[140:143], v1, s[4:7], s99 offen offset:1024 sc1\n"
"buffer_load_dwordx4 v[184:187], v1, s[4:7], s99 offen offset:2048 sc1\n"
"buffer_load_dwordx4 v[192:195], v1, s[4:7], s99 offen offset:3072 sc1\n"
"s_nop 7\n"
"s_branch .Lmk_p54r\n"
".Lmk_f44s:\n"
";\n"
"s_mov_b32 s1, 0x10000\n"
".Lmk_f44l:\n"
"buffer_load_dwordx2 v[254:255], v12, s[4:7], s97 offen sc1\n"
"s_waitcnt vmcnt(0)\n"
"v_cmp_ne_u64_e32 vcc, v[254:255], v[18:19]\n"
"s_cbranch_vccz .Lmk_f44r\n"
"s_sub_u32 s1, s1, 1\n"
"s_cbranch_scc0 .Lmk_f44l\n"
"s_branch .Lmk_f44r\n"
".Lmk_q56s:\n"
";\n"
"s_mov_b32 s1, 0x10000\n"
".Lmk_q56l:\n"
"s_add_u32 s97, s55, 0x448000\n"
"buffer_load_dwordx4 v[108:111], v0, s[4:7], s97 offen sc1\n"
"buffer_load_dwordx4 v[180:183], v0, s[4:7], s97 offen offset:1024 sc1\n"
"buffer_load_dwordx4 v[116:119], v0, s[4:7], s97 offen offset:2048 sc1\n"
"buffer_load_dwordx4 v[124:127], v0, s[4:7], s97 offen offset:3072 sc1\n"
"buffer_load_dwordx4 v[136:139], v1, s[4:7], s97 offen sc1\n"
"buffer_load_dwordx4 v[140:143], v1, s[4:7], s97 offen offset:1024 sc1\n"
"buffer_load_dwordx4 v[184:187], v1, s[4:7], s97 offen offset:2048 sc1\n"
"buffer_load_dwordx4 v[192:195], v1, s[4:7], s97 offen offset:3072 sc1\n"
"s_waitcnt vmcnt(0)\n"
"v_max_u32_e32 v255, v108, v109\n"
"v_max3_u32 v255, v255, v110, v111\n"
"v_max3_u32 v255, v255, v180, v181\n"
"v_max3_u32 v255, v255, v182, v183\n"
"v_max3_u32 v255, v255, v116, v117\n"
"v_max3_u32 v255, v255, v118, v119\n"
"v_max3_u32 v255, v255, v124, v125\n"
"v_max3_u32 v255, v255, v126, v127\n"
"v_cmp_eq_u32_e32 vcc, -1, v255\n"
"s_cbranch_vccz .Lmk_q56k\n"
"s_sub_u32 s1, s1, 1\n"
"s_cbranch_scc0 .Lmk_q56l\n"
".Lmk_q56k:\n"
"s_branch .Lmk_q56r\n"
".Lmk_p55s:\n"
";\n"
"s_mov_b32 s1, 0x10000\n"
".Lmk_p55l:\n"
"s_add_u32 s97, s54, 0x448000\n"
"buffer_load_dwordx4 v[88:91], v0, s[4:7], s97 offen sc1\n"
"buffer_load_dwordx4 v[92:95], v0, s[4:7], s97 offen offset:1024 sc1\n"
"buffer_load_dwordx4 v[132:135], v0, s[4:7], s97 offen offset:2048 sc1\n"
"buffer_load_dwordx4 v[148:151], v0, s[4:7], s97 offen offset:3072 sc1\n"
"buffer_load_dwordx4 v[164:167], v1, s[4:7], s97 offen sc1\n"
"buffer_load_dwordx4 v[196:199], v1, s[4:7], s97 offen offset:1024 sc1\n"
"buffer_load_dwordx4 v[216:219], v1, s[4:7], s97 offen offset:2048 sc1\n"
"buffer_load_dwordx4 v[224:227], v1, s[4:7], s97 offen offset:3072 sc1\n"
"s_add_u32 s97, s55, 0x448000\n"
"buffer_load_dwordx4 v[108:111], v0, s[4:7], s97 offen sc1\n"
"buffer_load_dwordx4 v[180:183], v0, s[4:7], s97 offen offset:1024 sc1\n"
"buffer_load_dwordx4 v[116:119], v0, s[4:7], s97 offen offset:2048 sc1\n"
"buffer_load_dwordx4 v[124:127], v0, s[4:7], s97 offen offset:3072 sc1\n"
"buffer_load_dwordx4 v[136:139], v1, s[4:7], s97 offen sc1\n"
"buffer_load_dwordx4 v[140:143], v1, s[4:7], s97 offen offset:1024 sc1\n"
"buffer_load_dwordx4 v[184:187], v1, s[4:7], s97 offen offset:2048 sc1\n"
"buffer_load_dwordx4 v[192:195], v1, s[4:7], s97 offen offset:3072 sc1\n"
"s_waitcnt vmcnt(0)\n"
"v_max_u32_e32 v255, v88, v89\n"
"v_max3_u32 v255, v255, v90, v91\n"
"v_max3_u32 v255, v255, v92, v93\n"
"v_max3_u32 v255, v255, v94, v95\n"
"v_max3_u32 v255, v255, v132, v133\n"
"v_max3_u32 v255, v255, v134, v135\n"
"v_max3_u32 v255, v255, v148, v149\n"
"v_max3_u32 v255, v255, v150, v151\n"
"v_max3_u32 v255, v255, v164, v165\n"
"v_max3_u32 v255, v255, v166, v167\n"
"v_max3_u32 v255, v255, v196, v197\n"
"v_max3_u32 v255, v255, v198, v199\n"
"v_max3_u32 v255, v255, v216, v217\n"
"v_max3_u32 v255, v255, v218, v219\n"
"v_max3_u32 v255, v255, v224, v225\n"
"v_max3_u32 v255, v255, v226, v227\n"
"v_cmp_eq_u32_e32 vcc, -1, v255\n"
"s_cbranch_vccz .Lmk_p55k\n"
"s_sub_u32 s1, s1, 1\n"
"s_cbranch_scc0 .Lmk_p55l\n"
".Lmk_p55k:\n"
"v_mfma_f32_16x16x32_bf16 v[172:175], a[0:3], v[88:91], 0\n"
"v_mfma_f32_16x16x32_bf16 v[200:203], a[4:7], v[88:91], 0\n"
"v_mfma_f32_16x16x32_bf16 v[204:207], a[8:11], v[88:91], 0\n"
"v_mfma_f32_16x16x32_bf16 v[212:215], a[12:15], v[88:91], 0\n"
"v_mfma_f32_16x16x32_bf16 v[96:99], a[16:19], v[88:91], 0\n"
"v_mfma_f32_16x16x32_bf16 v[220:223], a[20:23], v[88:91], 0\n"
"v_mfma_f32_16x16x32_bf16 v[172:175], a[24:27], v[92:95], v[172:175]\n"
"v_mfma_f32_16x16x32_bf16 v[200:203], a[28:31], v[92:95], v[200:203]\n"
"v_mfma_f32_16x16x32_bf16 v[204:207], a[32:35], v[92:95], v[204:207]\n"
"v_mfma_f32_16x16x32_bf16 v[212:215], a[36:39], v[92:95], v[212:215]\n"
"v_mfma_f32_16x16x32_bf16 v[96:99], a[40:43], v[92:95], v[96:99]\n"
"v_mfma_f32_16x16x32_bf16 v[220:223], a[44:47], v[92:95], v[220:223]\n"
"v_mfma_f32_16x16x32_bf16 v[172:175], a[48:51], v[132:135], v[172:175]\n"
"v_mfma_f32_16x16x32_bf16 v[200:203], a[52:55], v[132:135], v[200:203]\n"
"v_mfma_f32_16x16x32_bf16 v[204:207], a[56:59], v[132:135], v[204:207]\n"
"v_mfma_f32_16x16x32_bf16 v[212:215], a[60:63], v[132:135], v[212:215]\n"
"v_mfma_f32_16x16x32_bf16 v[96:99], a[64:67], v[132:135], v[96:99]\n"
"v_mfma_f32_16x16x32_bf16 v[220:223], a[68:71], v[132:135], v[220:223]\n"
"v_mfma_f32_16x16x32_bf16 v[172:175], a[72:75], v[148:151], v[172:175]\n"
"v_mfma_f32_16x16x32_bf16 v[200:203], a[76:79], v[148:151], v[200:203]\n"
"v_mfma_f32_16x16x32_bf16 v[204:207], a[80:83], v[148:151], v[204:207]\n"
"v_mfma_f32_16x16x32_bf16 v[212:215], a[84:87], v[148:151], v[212:215]\n"
"v_mfma_f32_16x16x32_bf16 v[96:99], a[88:91], v[148:151], v[96:99]\n"
"v_mfma_f32_16x16x32_bf16 v[220:223], a[92:95], v[148:151], v[220:223]\n"
"v_mfma_f32_16x16x32_bf16 v[172:175], a[96:99], v[164:167], v[172:175]\n"
"v_mfma_f32_16x16x32_bf16 v[200:203], a[100:103], v[164:167], v[200:203]\n"
"v_mfma_f32_16x16x32_bf16 v[204:207], a[104:107], v[164:167], v[204:207]\n"
"v_mfma_f32_16x16x32_bf16 v[212:215], a[108:111], v[164:167], v[212:215]\n"
"v_mfma_f32_16x16x32_bf16 v[96:99], a[112:115], v[164:167], v[96:99]\n"
"v_mfma_f32_16x16x32_bf16 v[220:223], a[116:119], v[164:167], v[220:223]\n"
"v_mfma_f32_16x16x32_bf16 v[172:175], a[120:123], v[196:199], v[172:175]\n"
"v_mfma_f32_16x16x32_bf16 v[200:203], a[124:127], v[196:199], v[200:203]\n"
"v_mfma_f32_16x16x32_bf16 v[204:207], v[24:27], v[196:199], v[204:207]\n"
"v_mfma_f32_16x16x32_bf16 v[212:215], v[28:31], v[196:199], v[212:215]\n"
"v_mfma_f32_16x16x32_bf16 v[96:99], v[32:35], v[196:199], v[96:99]\n"
"v_mfma_f32_16x16x32_bf16 v[220:223], v[36:39], v[196:199], v[220:223]\n"
"v_mfma_f32_16x16x32_bf16 v[172:175], v[40:43], v[216:219], v[172:175]\n"
"v_mfma_f32_16x16x32_bf16 v[200:203], v[44:47], v[216:219], v[200:203]\n"
"v_mfma_f32_16x16x32_bf16 v[204:207], v[48:51], v[216:219], v[204:207]\n"
"v_mfma_f32_16x16x32_bf16 v[212:215], v[52:55], v[216:219], v[212:215]\n"
"v_mfma_f32_16x16x32_bf16 v[96:99], v[56:59], v[216:219], v[96:99]\n"
"v_mfma_f32_16x16x32_bf16 v[220:223], v[60:63], v[216:219], v[220:223]\n"
"v_mfma_f32_16x16x32_bf16 v[172:175], v[64:67], v[224:227], v[172:175]\n"
"v_mfma_f32_16x16x32_bf16 v[200:203], v[68:71], v[224:227], v[200:203]\n"
"v_mfma_f32_16x16x32_bf16 v[204:207], v[72:75], v[224:227], v[204:207]\n"
"v_mfma_f32_16x16x32_bf16 v[212:215], v[76:79], v[224:227], v[212:215]\n"
"v_mfma_f32_16x16x32_bf16 v[96:99], v[80:83], v[224:227], v[96:99]\n"
"v_mfma_f32_16x16x32_bf16 v[220:223], v[84:87], v[224:227], v[220:223]\n"
"s_nop 7\n"
"s_branch .Lmk_p55r\n"
".Lmk_q57s:\n"
";\n"
"s_mov_b32 s1, 0x10000\n"
".Lmk_q57l:\n"
"s_add_u32 s97, s55, 0x448000\n"
"buffer_load_dwordx4 v[136:139], v1, s[4:7], s97 offen sc1\n"
"buffer_load_dwordx4 v[140:143], v1, s[4:7], s97 offen offset:1024 sc1\n"
"buffer_load_dwordx4 v[184:187], v1, s[4:7], s97 offen offset:2048 sc1\n"
"buffer_load_dwordx4 v[192:195], v1, s[4:7], s97 offen offset:3072 sc1\n"
"s_waitcnt vmcnt(0)\n"
"v_max_u32_e32 v255, v136, v137\n"
"v_max3_u32 v255, v255, v138, v139\n"
"v_max3_u32 v255, v255, v140, v141\n"
"v_max3_u32 v255, v255, v142, v143\n"
"v_max3_u32 v255, v255, v184, v185\n"
"v_max3_u32 v255, v255, v186, v187\n"
"v_max3_u32 v255, v255, v192, v193\n"
"v_max3_u32 v255, v255, v194, v195\n"
"v_cmp_eq_u32_e32 vcc, -1, v255\n"
"s_cbranch_vccz .Lmk_q57k\n"
"s_sub_u32 s1, s1, 1\n"
"s_cbranch_scc0 .Lmk_q57l\n"
".Lmk_q57k:\n"
"s_branch .Lmk_q57r\n"
".Lmk_f45s:\n"
";\n"
"s_mov_b32 s1, 0x10000\n"
".Lmk_f45l:\n"
"buffer_load_dwordx2 v[254:255], off, s[4:7], s97 sc1\n"
"s_waitcnt vmcnt(0)\n"
"v_cmp_ne_u64_e32 vcc, v[254:255], v[18:19]\n"
"s_cbranch_vccz .Lmk_f45r\n"
"s_sub_u32 s1, s1, 1\n"
"s_cbranch_scc0 .Lmk_f45l\n"
"s_branch .Lmk_f45r\n"
".Lmk_f46s:\n"
";\n"
"s_mov_b32 s1, 0x10000\n"
".Lmk_f46l:\n"
"buffer_load_dwordx2 v[254:255], v12, s[4:7], s99 offen sc1\n"
"s_waitcnt vmcnt(0)\n"
"v_cmp_ne_u64_e32 vcc, v[254:255], v[18:19]\n"
"s_cbranch_vccz .Lmk_f46r\n"
"s_sub_u32 s1, s1, 1\n"
"s_cbranch_scc0 .Lmk_f46l\n"
"s_branch .Lmk_f46r\n"
".Lmk_p61s:\n"
";\n"
"s_mov_b32 s1, 0x10000\n"
".Lmk_p61l:\n"
"s_add_u32 s97, s56, 0x440000\n"
"buffer_load_dwordx4 v[104:107], v0, s[4:7], s97 offen sc1\n"
"buffer_load_dwordx4 v[24:27], v0, s[4:7], s97 offen offset:1024 sc1\n"
"buffer_load_dwordx4 v[28:31], v0, s[4:7], s97 offen offset:2048 sc1\n"
"buffer_load_dwordx4 v[32:35], v0, s[4:7], s97 offen offset:3072 sc1\n"
"buffer_load_dwordx4 v[36:39], v1, s[4:7], s97 offen sc1\n"
"buffer_load_dwordx4 v[40:43], v1, s[4:7], s97 offen offset:1024 sc1\n"
"buffer_load_dwordx4 v[60:63], v1, s[4:7], s97 offen offset:2048 sc1\n"
"buffer_load_dwordx4 v[88:91], v1, s[4:7], s97 offen offset:3072 sc1\n"
"s_add_u32 s97, s57, 0x440000\n"
"buffer_load_dwordx4 v[100:103], v0, s[4:7], s97 offen sc1\n"
"buffer_load_dwordx4 v[72:75], v0, s[4:7], s97 offen offset:1024 sc1\n"
"buffer_load_dwordx4 v[64:67], v0, s[4:7], s97 offen offset:2048 sc1\n"
"buffer_load_dwordx4 v[112:115], v0, s[4:7], s97 offen offset:3072 sc1\n"
"buffer_load_dwordx4 v[96:99], v1, s[4:7], s97 offen sc1\n"
"buffer_load_dwordx4 v[84:87], v1, s[4:7], s97 offen offset:1024 sc1\n"
"buffer_load_dwordx4 v[76:79], v1, s[4:7], s97 offen offset:2048 sc1\n"
"buffer_load_dwordx4 v[128:131], v1, s[4:7], s97 offen offset:3072 sc1\n"
"s_waitcnt vmcnt(0)\n"
"v_max_u32_e32 v255, v104, v105\n"
"v_max3_u32 v255, v255, v106, v107\n"
"v_max3_u32 v255, v255, v24, v25\n"
"v_max3_u32 v255, v255, v26, v27\n"
"v_max3_u32 v255, v255, v28, v29\n"
"v_max3_u32 v255, v255, v30, v31\n"
"v_max3_u32 v255, v255, v32, v33\n"
"v_max3_u32 v255, v255, v34, v35\n"
"v_max3_u32 v255, v255, v36, v37\n"
"v_max3_u32 v255, v255, v38, v39\n"
"v_max3_u32 v255, v255, v40, v41\n"
"v_max3_u32 v255, v255, v42, v43\n"
"v_max3_u32 v255, v255, v60, v61\n"
"v_max3_u32 v255, v255, v62, v63\n"
"v_max3_u32 v255, v255, v88, v89\n"
"v_max3_u32 v255, v255, v90, v91\n"
"v_cmp_eq_u32_e32 vcc, -1, v255\n"
"s_cbranch_vccz .Lmk_p61k\n"
"s_sub_u32 s1, s1, 1\n"
"s_cbranch_scc0 .Lmk_p61l\n"
".Lmk_p61k:\n"
"v_mfma_f32_16x16x32_bf16 v[68:71], a[128:131], v[104:107], 0\n"
"v_mfma_f32_16x16x32_bf16 v[80:83], a[132:135], v[104:107], 0\n"
"v_mfma_f32_16x16x32_bf16 v[108:111], a[136:139], v[104:107], 0\n"
"v_mfma_f32_16x16x32_bf16 v[116:119], a[140:143], v[104:107], 0\n"
"v_mfma_f32_16x16x32_bf16 v[68:71], a[144:147], v[24:27], v[68:71]\n"
"v_mfma_f32_16x16x32_bf16 v[80:83], a[148:151], v[24:27], v[80:83]\n"
"v_mfma_f32_16x16x32_bf16 v[108:111], a[152:155], v[24:27], v[108:111]\n"
"v_mfma_f32_16x16x32_bf16 v[116:119], a[156:159], v[24:27], v[116:119]\n"
"v_mfma_f32_16x16x32_bf16 v[68:71], a[160:163], v[28:31], v[68:71]\n"
"v_mfma_f32_16x16x32_bf16 v[80:83], a[164:167], v[28:31], v[80:83]\n"
"v_mfma_f32_16x16x32_bf16 v[108:111], a[168:171], v[28:31], v[108:111]\n"
"v_mfma_f32_16x16x32_bf16 v[116:119], a[172:175], v[28:31], v[116:119]\n"
"v_mfma_f32_16x16x32_bf16 v[68:71], a[176:179], v[32:35], v[68:71]\n"
"v_mfma_f32_16x16x32_bf16 v[80:83], a[180:183], v[32:35], v[80:83]\n"
"v_mfma_f32_16x16x32_bf16 v[108:111], a[184:187], v[32:35], v[108:111]\n"
"v_mfma_f32_16x16x32_bf16 v[116:119], a[188:191], v[32:35], v[116:119]\n"
"v_mfma_f32_16x16x32_bf16 v[68:71], a[192:195], v[36:39], v[68:71]\n"
"v_mfma_f32_16x16x32_bf16 v[80:83], a[196:199], v[36:39], v[80:83]\n"
"v_mfma_f32_16x16x32_bf16 v[108:111], a[200:203], v[36:39], v[108:111]\n"
"v_mfma_f32_16x16x32_bf16 v[116:119], a[204:207], v[36:39], v[116:119]\n"
"v_mfma_f32_16x16x32_bf16 v[68:71], a[208:211], v[40:43], v[68:71]\n"
"v_mfma_f32_16x16x32_bf16 v[80:83], a[212:215], v[40:43], v[80:83]\n"
"v_mfma_f32_16x16x32_bf16 v[108:111], a[216:219], v[40:43], v[108:111]\n"
"v_mfma_f32_16x16x32_bf16 v[116:119], a[220:223], v[40:43], v[116:119]\n"
"v_mfma_f32_16x16x32_bf16 v[68:71], a[224:227], v[60:63], v[68:71]\n"
"v_mfma_f32_16x16x32_bf16 v[80:83], a[228:231], v[60:63], v[80:83]\n"
"v_mfma_f32_16x16x32_bf16 v[108:111], a[232:235], v[60:63], v[108:111]\n"
"v_mfma_f32_16x16x32_bf16 v[116:119], a[236:239], v[60:63], v[116:119]\n"
"v_mfma_f32_16x16x32_bf16 v[68:71], a[240:243], v[88:91], v[68:71]\n"
"v_mfma_f32_16x16x32_bf16 v[80:83], a[244:247], v[88:91], v[80:83]\n"
"v_mfma_f32_16x16x32_bf16 v[108:111], a[248:251], v[88:91], v[108:111]\n"
"v_mfma_f32_16x16x32_bf16 v[116:119], a[252:255], v[88:91], v[116:119]\n"
"s_add_u32 s97, s58, 0x440000\n"
"v_mov_b32_e32 v255, 0x80000000\n"
"buffer_load_dwordx4 v[104:107], v0, s[4:7], s97 offen sc1\n"
"buffer_load_dwordx4 v[24:27], v0, s[4:7], s97 offen offset:1024 sc1\n"
"buffer_load_dwordx4 v[28:31], v0, s[4:7], s97 offen offset:2048 sc1\n"
"buffer_load_dwordx4 v[32:35], v0, s[4:7], s97 offen offset:3072 sc1\n"
"buffer_load_dwordx4 v[36:39], v1, s[4:7], s97 offen sc1\n"
"buffer_load_dwordx4 v[40:43], v1, s[4:7], s97 offen offset:1024 sc1\n"
"buffer_load_dwordx4 v[60:63], v1, s[4:7], s97 offen offset:2048 sc1\n"
"buffer_load_dwordx4 v[88:91], v1, s[4:7], s97 offen offset:3072 sc1\n"
"s_nop 7\n"
"s_branch .Lmk_p61r\n"
".Lmk_p62s:\n"
";\n"
"s_mov_b32 s1, 0x10000\n"
".Lmk_p62l:\n"
"s_add_u32 s97, s57, 0x440000\n"
"buffer_load_dwordx4 v[144:147], v0, s[4:7], s97 offen sc1\n"
"buffer_load_dwordx4 v[148:151], v0, s[4:7], s97 offen offset:1024 sc1\n"
"buffer_load_dwordx4 v[152:155], v0, s[4:7], s97 offen offset:2048 sc1\n"
"buffer_load_dwordx4 v[156:159], v0, s[4:7], s97 offen offset:3072 sc1\n"
"buffer_load_dwordx4 v[160:163], v1, s[4:7], s97 offen sc1\n"
"buffer_load_dwordx4 v[164:167], v1, s[4:7], s97 offen offset:1024 sc1\n"
"buffer_load_dwordx4 v[168:171], v1, s[4:7], s97 offen offset:2048 sc1\n"
"buffer_load_dwordx4 v[172:175], v1, s[4:7], s97 offen offset:3072 sc1\n"
"s_add_u32 s97, s58, 0x440000\n"
"buffer_load_dwordx4 v[104:107], v0, s[4:7], s97 offen sc1\n"
"buffer_load_dwordx4 v[24:27], v0, s[4:7], s97 offen offset:1024 sc1\n"
"buffer_load_dwordx4 v[28:31], v0, s[4:7], s97 offen offset:2048 sc1\n"
"buffer_load_dwordx4 v[32:35], v0, s[4:7], s97 offen offset:3072 sc1\n"
"buffer_load_dwordx4 v[36:39], v1, s[4:7], s97 offen sc1\n"
"buffer_load_dwordx4 v[40:43], v1, s[4:7], s97 offen offset:1024 sc1\n"
"buffer_load_dwordx4 v[60:63], v1, s[4:7], s97 offen offset:2048 sc1\n"
"buffer_load_dwordx4 v[88:91], v1, s[4:7], s97 offen offset:3072 sc1\n"
"s_add_u32 s97, s59, 0x440000\n"
"buffer_load_dwordx4 v[132:135], v0, s[4:7], s97 offen sc1\n"
"buffer_load_dwordx4 v[68:71], v0, s[4:7], s97 offen offset:1024 sc1\n"
"buffer_load_dwordx4 v[64:67], v0, s[4:7], s97 offen offset:2048 sc1\n"
"buffer_load_dwordx4 v[100:103], v0, s[4:7], s97 offen offset:3072 sc1\n"
"buffer_load_dwordx4 v[96:99], v1, s[4:7], s97 offen sc1\n"
"buffer_load_dwordx4 v[140:143], v1, s[4:7], s97 offen offset:1024 sc1\n"
"buffer_load_dwordx4 v[72:75], v1, s[4:7], s97 offen offset:2048 sc1\n"
"s_waitcnt vmcnt(0)\n"
"v_max_u32_e32 v243, v144, v145\n"
"v_max3_u32 v243, v243, v146, v147\n"
"v_max3_u32 v243, v243, v148, v149\n"
"v_max3_u32 v243, v243, v150, v151\n"
"v_max3_u32 v243, v243, v152, v153\n"
"v_max3_u32 v243, v243, v154, v155\n"
"v_max3_u32 v243, v243, v156, v157\n"
"v_max3_u32 v243, v243, v158, v159\n"
"v_max3_u32 v243, v243, v160, v161\n"
"v_max3_u32 v243, v243, v162, v163\n"
"v_max3_u32 v243, v243, v164, v165\n"
"v_max3_u32 v243, v243, v166, v167\n"
"v_max3_u32 v243, v243, v168, v169\n"
"v_max3_u32 v243, v243, v170, v171\n"
"v_max3_u32 v243, v243, v172, v173\n"
"v_max3_u32 v243, v243, v174, v175\n"
"v_cmp_eq_u32_e32 vcc, -1, v243\n"
"s_cbranch_vccz .Lmk_p62k\n"
"s_sub_u32 s1, s1, 1\n"
"s_cbranch_scc0 .Lmk_p62l\n"
".Lmk_p62k:\n"
"v_mfma_f32_16x16x32_bf16 v[92:95], a[128:131], v[144:147], 0\n"
"v_mfma_f32_16x16x32_bf16 v[120:123], a[132:135], v[144:147], 0\n"
"v_mfma_f32_16x16x32_bf16 v[124:127], a[136:139], v[144:147], 0\n"
"v_mfma_f32_16x16x32_bf16 v[136:139], a[140:143], v[144:147], 0\n"
"v_mfma_f32_16x16x32_bf16 v[92:95], a[144:147], v[148:151], v[92:95]\n"
"v_mfma_f32_16x16x32_bf16 v[120:123], a[148:151], v[148:151], v[120:123]\n"
"v_mfma_f32_16x16x32_bf16 v[124:127], a[152:155], v[148:151], v[124:127]\n"
"v_mfma_f32_16x16x32_bf16 v[136:139], a[156:159], v[148:151], v[136:139]\n"
"v_mfma_f32_16x16x32_bf16 v[92:95], a[160:163], v[152:155], v[92:95]\n"
"v_mfma_f32_16x16x32_bf16 v[120:123], a[164:167], v[152:155], v[120:123]\n"
"v_mfma_f32_16x16x32_bf16 v[124:127], a[168:171], v[152:155], v[124:127]\n"
"v_mfma_f32_16x16x32_bf16 v[136:139], a[172:175], v[152:155], v[136:139]\n"
"v_mfma_f32_16x16x32_bf16 v[92:95], a[176:179], v[156:159], v[92:95]\n"
"v_mfma_f32_16x16x32_bf16 v[120:123], a[180:183], v[156:159], v[120:123]\n"
"v_mfma_f32_16x16x32_bf16 v[124:127], a[184:187], v[156:159], v[124:127]\n"
"v_mfma_f32_16x16x32_bf16 v[136:139], a[188:191], v[156:159], v[136:139]\n"
"v_mfma_f32_16x16x32_bf16 v[92:95], a[192:195], v[160:163], v[92:95]\n"
"v_mfma_f32_16x16x32_bf16 v[120:123], a[196:199], v[160:163], v[120:123]\n"
"v_mfma_f32_16x16x32_bf16 v[124:127], a[200:203], v[160:163], v[124:127]\n"
"v_mfma_f32_16x16x32_bf16 v[136:139], a[204:207], v[160:163], v[136:139]\n"
"v_mfma_f32_16x16x32_bf16 v[92:95], a[208:211], v[164:167], v[92:95]\n"
"v_mfma_f32_16x16x32_bf16 v[120:123], a[212:215], v[164:167], v[120:123]\n"
"v_mfma_f32_16x16x32_bf16 v[124:127], a[216:219], v[164:167], v[124:127]\n"
"v_mfma_f32_16x16x32_bf16 v[136:139], a[220:223], v[164:167], v[136:139]\n"
"v_mfma_f32_16x16x32_bf16 v[92:95], a[224:227], v[168:171], v[92:95]\n"
"v_mfma_f32_16x16x32_bf16 v[120:123], a[228:231], v[168:171], v[120:123]\n"
"v_mfma_f32_16x16x32_bf16 v[124:127], a[232:235], v[168:171], v[124:127]\n"
"v_mfma_f32_16x16x32_bf16 v[136:139], a[236:239], v[168:171], v[136:139]\n"
"v_mfma_f32_16x16x32_bf16 v[92:95], a[240:243], v[172:175], v[92:95]\n"
"v_mfma_f32_16x16x32_bf16 v[120:123], a[244:247], v[172:175], v[120:123]\n"
"v_mfma_f32_16x16x32_bf16 v[124:127], a[248:251], v[172:175], v[124:127]\n"
"v_mfma_f32_16x16x32_bf16 v[136:139], a[252:255], v[172:175], v[136:139]\n"
"s_nop 7\n"
"s_branch .Lmk_p62r\n"
".Lmk_q64s:\n"
";\n"
"s_mov_b32 s1, 0x10000\n"
".Lmk_q64l:\n"
"s_add_u32 s97, s59, 0x440000\n"
"buffer_load_dwordx4 v[132:135], v0, s[4:7], s97 offen sc1\n"
"buffer_load_dwordx4 v[68:71], v0, s[4:7], s97 offen offset:1024 sc1\n"
"buffer_load_dwordx4 v[64:67], v0, s[4:7], s97 offen offset:2048 sc1\n"
"buffer_load_dwordx4 v[100:103], v0, s[4:7], s97 offen offset:3072 sc1\n"
"buffer_load_dwordx4 v[96:99], v1, s[4:7], s97 offen sc1\n"
"buffer_load_dwordx4 v[140:143], v1, s[4:7], s97 offen offset:1024 sc1\n"
"buffer_load_dwordx4 v[72:75], v1, s[4:7], s97 offen offset:2048 sc1\n"
"buffer_load_dwordx4 v[148:151], v1, s[4:7], s97 offen offset:3072 sc1\n"
"s_add_u32 s97, s56, 0x400000\n"
"buffer_load_dwordx4 v[24:27], v0, s[4:7], s97 offen sc1\n"
"buffer_load_dwordx4 v[28:31], v0, s[4:7], s97 offen offset:1024 sc1\n"
"buffer_load_dwordx4 v[32:35], v0, s[4:7], s97 offen offset:2048 sc1\n"
"buffer_load_dwordx4 v[36:39], v0, s[4:7], s97 offen offset:3072 sc1\n"
"s_waitcnt vmcnt(0)\n"
"v_max_u32_e32 v255, v132, v133\n"
"v_max3_u32 v255, v255, v134, v135\n"
"v_max3_u32 v255, v255, v68, v69\n"
"v_max3_u32 v255, v255, v70, v71\n"
"v_max3_u32 v255, v255, v64, v65\n"
"v_max3_u32 v255, v255, v66, v67\n"
"v_max3_u32 v255, v255, v100, v101\n"
"v_max3_u32 v255, v255, v102, v103\n"
"v_cmp_eq_u32_e32 vcc, -1, v255\n"
"s_cbranch_vccz .Lmk_q64k\n"
"s_sub_u32 s1, s1, 1\n"
"s_cbranch_scc0 .Lmk_q64l\n"
".Lmk_q64k:\n"
"s_branch .Lmk_q64r\n"
".Lmk_p63s:\n"
";\n"
"s_mov_b32 s1, 0x10000\n"
".Lmk_p63l:\n"
"s_add_u32 s97, s58, 0x440000\n"
"buffer_load_dwordx4 v[40:43], v0, s[4:7], s97 offen sc1\n"
"buffer_load_dwordx4 v[60:63], v0, s[4:7], s97 offen offset:1024 sc1\n"
"buffer_load_dwordx4 v[80:83], v0, s[4:7], s97 offen offset:2048 sc1\n"
"buffer_load_dwordx4 v[88:91], v0, s[4:7], s97 offen offset:3072 sc1\n"
"buffer_load_dwordx4 v[92:95], v1, s[4:7], s97 offen sc1\n"
"buffer_load_dwordx4 v[104:107], v1, s[4:7], s97 offen offset:1024 sc1\n"
"buffer_load_dwordx4 v[120:123], v1, s[4:7], s97 offen offset:2048 sc1\n"
"buffer_load_dwordx4 v[124:127], v1, s[4:7], s97 offen offset:3072 sc1\n"
"s_add_u32 s97, s59, 0x440000\n"
"buffer_load_dwordx4 v[132:135], v0, s[4:7], s97 offen sc1\n"
"buffer_load_dwordx4 v[68:71], v0, s[4:7], s97 offen offset:1024 sc1\n"
"buffer_load_dwordx4 v[64:67], v0, s[4:7], s97 offen offset:2048 sc1\n"
"buffer_load_dwordx4 v[100:103], v0, s[4:7], s97 offen offset:3072 sc1\n"
"buffer_load_dwordx4 v[96:99], v1, s[4:7], s97 offen sc1\n"
"buffer_load_dwordx4 v[140:143], v1, s[4:7], s97 offen offset:1024 sc1\n"
"buffer_load_dwordx4 v[72:75], v1, s[4:7], s97 offen offset:2048 sc1\n"
"buffer_load_dwordx4 v[148:151], v1, s[4:7], s97 offen offset:3072 sc1\n"
"s_add_u32 s97, s56, 0x400000\n"
"buffer_load_dwordx4 v[24:27], v0, s[4:7], s97 offen sc1\n"
"buffer_load_dwordx4 v[28:31], v0, s[4:7], s97 offen offset:1024 sc1\n"
"buffer_load_dwordx4 v[32:35], v0, s[4:7], s97 offen offset:2048 sc1\n"
"buffer_load_dwordx4 v[36:39], v0, s[4:7], s97 offen offset:3072 sc1\n"
"s_waitcnt vmcnt(0)\n"
"v_max_u32_e32 v255, v40, v41\n"
"v_max3_u32 v255, v255, v42, v43\n"
"v_max3_u32 v255, v255, v60, v61\n"
"v_max3_u32 v255, v255, v62, v63\n"
"v_max3_u32 v255, v255, v80, v81\n"
"v_max3_u32 v255, v255, v82, v83\n"
"v_max3_u32 v255, v255, v88, v89\n"
"v_max3_u32 v255, v255, v90, v91\n"
"v_max3_u32 v255, v255, v92, v93\n"
"v_max3_u32 v255, v255, v94, v95\n"
"v_max3_u32 v255, v255, v104, v105\n"
"v_max3_u32 v255, v255, v106, v107\n"
"v_max3_u32 v255, v255, v120, v121\n"
"v_max3_u32 v255, v255, v122, v123\n"
"v_max3_u32 v255, v255, v124, v125\n"
"v_max3_u32 v255, v255, v126, v127\n"
"v_cmp_eq_u32_e32 vcc, -1, v255\n"
"s_cbranch_vccz .Lmk_p63k\n"
"s_sub_u32 s1, s1, 1\n"
"s_cbranch_scc0 .Lmk_p63l\n"
".Lmk_p63k:\n"
"v_mfma_f32_16x16x32_bf16 v[76:79], a[128:131], v[40:43], 0\n"
"v_mfma_f32_16x16x32_bf16 v[128:131], a[132:135], v[40:43], 0\n"
"v_mfma_f32_16x16x32_bf16 v[144:147], a[136:139], v[40:43], 0\n"
"v_mfma_f32_16x16x32_bf16 v[152:155], a[140:143], v[40:43], 0\n"
"v_mfma_f32_16x16x32_bf16 v[76:79], a[144:147], v[60:63], v[76:79]\n"
"v_mfma_f32_16x16x32_bf16 v[128:131], a[148:151], v[60:63], v[128:131]\n"
"v_mfma_f32_16x16x32_bf16 v[144:147], a[152:155], v[60:63], v[144:147]\n"
"v_mfma_f32_16x16x32_bf16 v[152:155], a[156:159], v[60:63], v[152:155]\n"
"v_mfma_f32_16x16x32_bf16 v[76:79], a[160:163], v[80:83], v[76:79]\n"
"v_mfma_f32_16x16x32_bf16 v[128:131], a[164:167], v[80:83], v[128:131]\n"
"v_mfma_f32_16x16x32_bf16 v[144:147], a[168:171], v[80:83], v[144:147]\n"
"v_mfma_f32_16x16x32_bf16 v[152:155], a[172:175], v[80:83], v[152:155]\n"
"v_mfma_f32_16x16x32_bf16 v[76:79], a[176:179], v[88:91], v[76:79]\n"
"v_mfma_f32_16x16x32_bf16 v[128:131], a[180:183], v[88:91], v[128:131]\n"
"v_mfma_f32_16x16x32_bf16 v[144:147], a[184:187], v[88:91], v[144:147]\n"
"v_mfma_f32_16x16x32_bf16 v[152:155], a[188:191], v[88:91], v[152:155]\n"
"v_mfma_f32_16x16x32_bf16 v[76:79], a[192:195], v[92:95], v[76:79]\n"
"v_mfma_f32_16x16x32_bf16 v[128:131], a[196:199], v[92:95], v[128:131]\n"
"v_mfma_f32_16x16x32_bf16 v[144:147], a[200:203], v[92:95], v[144:147]\n"
"v_mfma_f32_16x16x32_bf16 v[152:155], a[204:207], v[92:95], v[152:155]\n"
"v_mfma_f32_16x16x32_bf16 v[76:79], a[208:211], v[104:107], v[76:79]\n"
"v_mfma_f32_16x16x32_bf16 v[128:131], a[212:215], v[104:107], v[128:131]\n"
"v_mfma_f32_16x16x32_bf16 v[144:147], a[216:219], v[104:107], v[144:147]\n"
"v_mfma_f32_16x16x32_bf16 v[152:155], a[220:223], v[104:107], v[152:155]\n"
"v_mfma_f32_16x16x32_bf16 v[76:79], a[224:227], v[120:123], v[76:79]\n"
"v_mfma_f32_16x16x32_bf16 v[128:131], a[228:231], v[120:123], v[128:131]\n"
"v_mfma_f32_16x16x32_bf16 v[144:147], a[232:235], v[120:123], v[144:147]\n"
"v_mfma_f32_16x16x32_bf16 v[152:155], a[236:239], v[120:123], v[152:155]\n"
"v_mfma_f32_16x16x32_bf16 v[76:79], a[240:243], v[124:127], v[76:79]\n"
"v_mfma_f32_16x16x32_bf16 v[128:131], a[244:247], v[124:127], v[128:131]\n"
"v_mfma_f32_16x16x32_bf16 v[144:147], a[248:251], v[124:127], v[144:147]\n"
"v_mfma_f32_16x16x32_bf16 v[152:155], a[252:255], v[124:127], v[152:155]\n"
"s_nop 7\n"
"s_branch .Lmk_p63r\n"
".Lmk_q65s:\n"
";\n"
"s_mov_b32 s1, 0x10000\n"
".Lmk_q65l:\n"
"s_add_u32 s97, s59, 0x440000\n"
"buffer_load_dwordx4 v[96:99], v1, s[4:7], s97 offen sc1\n"
"buffer_load_dwordx4 v[140:143], v1, s[4:7], s97 offen offset:1024 sc1\n"
"buffer_load_dwordx4 v[72:75], v1, s[4:7], s97 offen offset:2048 sc1\n"
"buffer_load_dwordx4 v[148:151], v1, s[4:7], s97 offen offset:3072 sc1\n"
"s_add_u32 s97, s56, 0x400000\n"
"buffer_load_dwordx4 v[24:27], v0, s[4:7], s97 offen sc1\n"
"buffer_load_dwordx4 v[28:31], v0, s[4:7], s97 offen offset:1024 sc1\n"
"buffer_load_dwordx4 v[32:35], v0, s[4:7], s97 offen offset:2048 sc1\n"
"buffer_load_dwordx4 v[36:39], v0, s[4:7], s97 offen offset:3072 sc1\n"
"buffer_load_dwordx4 v[40:43], v1, s[4:7], s97 offen sc1\n"
"buffer_load_dwordx4 v[60:63], v1, s[4:7], s97 offen offset:1024 sc1\n"
"s_waitcnt vmcnt(0)\n"
"v_max_u32_e32 v255, v96, v97\n"
"v_max3_u32 v255, v255, v98, v99\n"
"v_max3_u32 v255, v255, v140, v141\n"
"v_max3_u32 v255, v255, v142, v143\n"
"v_max3_u32 v255, v255, v72, v73\n"
"v_max3_u32 v255, v255, v74, v75\n"
"v_max3_u32 v255, v255, v148, v149\n"
"v_max3_u32 v255, v255, v150, v151\n"
"v_cmp_eq_u32_e32 vcc, -1, v255\n"
"s_cbranch_vccz .Lmk_q65k\n"
"s_sub_u32 s1, s1, 1\n"
"s_cbranch_scc0 .Lmk_q65l\n"
".Lmk_q65k:\n"
"s_branch .Lmk_q65r\n"
".Lmk_p66s:\n"
";\n"
"s_mov_b32 s1, 0x10000\n"
".Lmk_p66l:\n"
"s_add_u32 s97, s56, 0x400000\n"
"buffer_load_dwordx4 v[144:147], v0, s[4:7], s97 offen sc1\n"
"buffer_load_dwordx4 v[24:27], v0, s[4:7], s97 offen offset:1024 sc1\n"
"buffer_load_dwordx4 v[28:31], v0, s[4:7], s97 offen offset:2048 sc1\n"
"buffer_load_dwordx4 v[32:35], v0, s[4:7], s97 offen offset:3072 sc1\n"
"buffer_load_dwordx4 v[36:39], v1, s[4:7], s97 offen sc1\n"
"buffer_load_dwordx4 v[40:43], v1, s[4:7], s97 offen offset:1024 sc1\n"
"buffer_load_dwordx4 v[60:63], v1, s[4:7], s97 offen offset:2048 sc1\n"
"buffer_load_dwordx4 v[64:67], v1, s[4:7], s97 offen offset:3072 sc1\n"
"s_add_u32 s97, s57, 0x400000\n"
"buffer_load_dwordx4 v[88:91], v0, s[4:7], s97 offen sc1\n"
"buffer_load_dwordx4 v[72:75], v0, s[4:7], s97 offen offset:1024 sc1\n"
"buffer_load_dwordx4 v[128:131], v0, s[4:7], s97 offen offset:2048 sc1\n"
"buffer_load_dwordx4 v[80:83], v0, s[4:7], s97 offen offset:3072 sc1\n"
"buffer_load_dwordx4 v[96:99], v1, s[4:7], s97 offen sc1\n"
"buffer_load_dwordx4 v[104:107], v1, s[4:7], s97 offen offset:1024 sc1\n"
"buffer_load_dwordx4 v[124:127], v1, s[4:7], s97 offen offset:2048 sc1\n"
"buffer_load_dwordx4 v[132:135], v1, s[4:7], s97 offen offset:3072 sc1\n"
"s_waitcnt vmcnt(0)\n"
"v_max_u32_e32 v255, v144, v145\n"
"v_max3_u32 v255, v255, v146, v147\n"
"v_max3_u32 v255, v255, v24, v25\n"
"v_max3_u32 v255, v255, v26, v27\n"
"v_max3_u32 v255, v255, v28, v29\n"
"v_max3_u32 v255, v255, v30, v31\n"
"v_max3_u32 v255, v255, v32, v33\n"
"v_max3_u32 v255, v255, v34, v35\n"
"v_max3_u32 v255, v255, v36, v37\n"
"v_max3_u32 v255, v255, v38, v39\n"
"v_max3_u32 v255, v255, v40, v41\n"
"v_max3_u32 v255, v255, v42, v43\n"
"v_max3_u32 v255, v255, v60, v61\n"
"v_max3_u32 v255, v255, v62, v63\n"
"v_max3_u32 v255, v255, v64, v65\n"
"v_max3_u32 v255, v255, v66, v67\n"
"v_cmp_eq_u32_e32 vcc, -1, v255\n"
"s_cbranch_vccz .Lmk_p66k\n"
"s_sub_u32 s1, s1, 1\n"
"s_cbranch_scc0 .Lmk_p66l\n"
".Lmk_p66k:\n"
"v_mfma_f32_16x16x32_bf16 v[136:139], a[128:131], v[144:147], 0\n"
"v_mfma_f32_16x16x32_bf16 v[140:143], a[132:135], v[144:147], 0\n"
"v_mfma_f32_16x16x32_bf16 v[148:151], a[136:139], v[144:147], 0\n"
"v_mfma_f32_16x16x32_bf16 v[152:155], a[140:143], v[144:147], 0\n"
"v_mfma_f32_16x16x32_bf16 v[136:139], a[144:147], v[24:27], v[136:139]\n"
"v_mfma_f32_16x16x32_bf16 v[140:143], a[148:151], v[24:27], v[140:143]\n"
"v_mfma_f32_16x16x32_bf16 v[148:151], a[152:155], v[24:27], v[148:151]\n"
"v_mfma_f32_16x16x32_bf16 v[152:155], a[156:159], v[24:27], v[152:155]\n"
"v_mfma_f32_16x16x32_bf16 v[136:139], a[160:163], v[28:31], v[136:139]\n"
"v_mfma_f32_16x16x32_bf16 v[140:143], a[164:167], v[28:31], v[140:143]\n"
"v_mfma_f32_16x16x32_bf16 v[148:151], a[168:171], v[28:31], v[148:151]\n"
"v_mfma_f32_16x16x32_bf16 v[152:155], a[172:175], v[28:31], v[152:155]\n"
"v_mfma_f32_16x16x32_bf16 v[136:139], a[176:179], v[32:35], v[136:139]\n"
"v_mfma_f32_16x16x32_bf16 v[140:143], a[180:183], v[32:35], v[140:143]\n"
"v_mfma_f32_16x16x32_bf16 v[148:151], a[184:187], v[32:35], v[148:151]\n"
"v_mfma_f32_16x16x32_bf16 v[152:155], a[188:191], v[32:35], v[152:155]\n"
"v_mfma_f32_16x16x32_bf16 v[136:139], a[192:195], v[36:39], v[136:139]\n"
"v_mfma_f32_16x16x32_bf16 v[140:143], a[196:199], v[36:39], v[140:143]\n"
"v_mfma_f32_16x16x32_bf16 v[148:151], a[200:203], v[36:39], v[148:151]\n"
"v_mfma_f32_16x16x32_bf16 v[152:155], a[204:207], v[36:39], v[152:155]\n"
"v_mfma_f32_16x16x32_bf16 v[136:139], a[208:211], v[40:43], v[136:139]\n"
"v_mfma_f32_16x16x32_bf16 v[140:143], a[212:215], v[40:43], v[140:143]\n"
"v_mfma_f32_16x16x32_bf16 v[148:151], a[216:219], v[40:43], v[148:151]\n"
"v_mfma_f32_16x16x32_bf16 v[152:155], a[220:223], v[40:43], v[152:155]\n"
"v_mfma_f32_16x16x32_bf16 v[136:139], a[224:227], v[60:63], v[136:139]\n"
"v_mfma_f32_16x16x32_bf16 v[140:143], a[228:231], v[60:63], v[140:143]\n"
"v_mfma_f32_16x16x32_bf16 v[148:151], a[232:235], v[60:63], v[148:151]\n"
"v_mfma_f32_16x16x32_bf16 v[152:155], a[236:239], v[60:63], v[152:155]\n"
"v_mfma_f32_16x16x32_bf16 v[136:139], a[240:243], v[64:67], v[136:139]\n"
"v_mfma_f32_16x16x32_bf16 v[140:143], a[244:247], v[64:67], v[140:143]\n"
"v_mfma_f32_16x16x32_bf16 v[148:151], a[248:251], v[64:67], v[148:151]\n"
"v_mfma_f32_16x16x32_bf16 v[152:155], a[252:255], v[64:67], v[152:155]\n"
"s_add_u32 s97, s58, 0x400000\n"
"v_mov_b32_e32 v255, 0x80000000\n"
"buffer_load_dwordx4 v[144:147], v0, s[4:7], s97 offen sc1\n"
"buffer_load_dwordx4 v[24:27], v0, s[4:7], s97 offen offset:1024 sc1\n"
"buffer_load_dwordx4 v[28:31], v0, s[4:7], s97 offen offset:2048 sc1\n"
"buffer_load_dwordx4 v[32:35], v0, s[4:7], s97 offen offset:3072 sc1\n"
"buffer_load_dwordx4 v[36:39], v1, s[4:7], s97 offen sc1\n"
"buffer_load_dwordx4 v[40:43], v1, s[4:7], s97 offen offset:1024 sc1\n"
"buffer_load_dwordx4 v[60:63], v1, s[4:7], s97 offen offset:2048 sc1\n"
"buffer_load_dwordx4 v[64:67], v1, s[4:7], s97 offen offset:3072 sc1\n"
"s_nop 7\n"
"s_branch .Lmk_p66r\n"
".Lmk_p67s:\n"
";\n"
"s_mov_b32 s1, 0x10000\n"
".Lmk_p67l:\n"
"s_add_u32 s99, s57, 0x400000\n"
"buffer_load_dwordx4 v[100:103], v0, s[4:7], s99 offen sc1\n"
"buffer_load_dwordx4 v[88:91], v0, s[4:7], s99 offen offset:1024 sc1\n"
"buffer_load_dwordx4 v[72:75], v0, s[4:7], s99 offen offset:2048 sc1\n"
"buffer_load_dwordx4 v[116:119], v0, s[4:7], s99 offen offset:3072 sc1\n"
"buffer_load_dwordx4 v[80:83], v1, s[4:7], s99 offen sc1\n"
"buffer_load_dwordx4 v[96:99], v1, s[4:7], s99 offen offset:1024 sc1\n"
"buffer_load_dwordx4 v[104:107], v1, s[4:7], s99 offen offset:2048 sc1\n"
"buffer_load_dwordx4 v[124:127], v1, s[4:7], s99 offen offset:3072 sc1\n"
"s_add_u32 s99, s58, 0x400000\n"
"buffer_load_dwordx4 v[144:147], v0, s[4:7], s99 offen sc1\n"
"buffer_load_dwordx4 v[24:27], v0, s[4:7], s99 offen offset:1024 sc1\n"
"buffer_load_dwordx4 v[28:31], v0, s[4:7], s99 offen offset:2048 sc1\n"
"buffer_load_dwordx4 v[32:35], v0, s[4:7], s99 offen offset:3072 sc1\n"
"buffer_load_dwordx4 v[36:39], v1, s[4:7], s99 offen sc1\n"
"buffer_load_dwordx4 v[40:43], v1, s[4:7], s99 offen offset:1024 sc1\n"
"buffer_load_dwordx4 v[60:63], v1, s[4:7], s99 offen offset:2048 sc1\n"
"buffer_load_dwordx4 v[64:67], v1, s[4:7], s99 offen offset:3072 sc1\n"
"s_waitcnt vmcnt(0)\n"
"v_max_u32_e32 v244, v100, v101\n"
"v_max3_u32 v244, v244, v102, v103\n"
"v_max3_u32 v244, v244, v88, v89\n"
"v_max3_u32 v244, v244, v90, v91\n"
"v_max3_u32 v244, v244, v72, v73\n"
"v_max3_u32 v244, v244, v74, v75\n"
"v_max3_u32 v244, v244, v116, v117\n"
"v_max3_u32 v244, v244, v118, v119\n"
"v_max3_u32 v244, v244, v80, v81\n"
"v_max3_u32 v244, v244, v82, v83\n"
"v_max3_u32 v244, v244, v96, v97\n"
"v_max3_u32 v244, v244, v98, v99\n"
"v_max3_u32 v244, v244, v104, v105\n"
"v_max3_u32 v244, v244, v106, v107\n"
"v_max3_u32 v244, v244, v124, v125\n"
"v_max3_u32 v244, v244, v126, v127\n"
"v_cmp_eq_u32_e32 vcc, -1, v244\n"
"s_cbranch_vccz .Lmk_p67k\n"
"s_sub_u32 s1, s1, 1\n"
"s_cbranch_scc0 .Lmk_p67l\n"
".Lmk_p67k:\n"
"v_mfma_f32_16x16x32_bf16 v[68:71], a[128:131], v[100:103], 0\n"
"v_mfma_f32_16x16x32_bf16 v[84:87], a[132:135], v[100:103], 0\n"
"v_mfma_f32_16x16x32_bf16 v[92:95], a[136:139], v[100:103], 0\n"
"v_mfma_f32_16x16x32_bf16 v[112:115], a[140:143], v[100:103], 0\n"
"v_mfma_f32_16x16x32_bf16 v[68:71], a[144:147], v[88:91], v[68:71]\n"
"v_mfma_f32_16x16x32_bf16 v[84:87], a[148:151], v[88:91], v[84:87]\n"
"v_mfma_f32_16x16x32_bf16 v[92:95], a[152:155], v[88:91], v[92:95]\n"
"v_mfma_f32_16x16x32_bf16 v[112:115], a[156:159], v[88:91], v[112:115]\n"
"v_mfma_f32_16x16x32_bf16 v[68:71], a[160:163], v[72:75], v[68:71]\n"
"v_mfma_f32_16x16x32_bf16 v[84:87], a[164:167], v[72:75], v[84:87]\n"
"v_mfma_f32_16x16x32_bf16 v[92:95], a[168:171], v[72:75], v[92:95]\n"
"v_mfma_f32_16x16x32_bf16 v[112:115], a[172:175], v[72:75], v[112:115]\n"
"v_mfma_f32_16x16x32_bf16 v[68:71], a[176:179], v[116:119], v[68:71]\n"
"v_mfma_f32_16x16x32_bf16 v[84:87], a[180:183], v[116:119], v[84:87]\n"
"v_mfma_f32_16x16x32_bf16 v[92:95], a[184:187], v[116:119], v[92:95]\n"
"v_mfma_f32_16x16x32_bf16 v[112:115], a[188:191], v[116:119], v[112:115]\n"
"v_mfma_f32_16x16x32_bf16 v[68:71], a[192:195], v[80:83], v[68:71]\n"
"v_mfma_f32_16x16x32_bf16 v[84:87], a[196:199], v[80:83], v[84:87]\n"
"v_mfma_f32_16x16x32_bf16 v[92:95], a[200:203], v[80:83], v[92:95]\n"
"v_mfma_f32_16x16x32_bf16 v[112:115], a[204:207], v[80:83], v[112:115]\n"
"v_mfma_f32_16x16x32_bf16 v[68:71], a[208:211], v[96:99], v[68:71]\n"
"v_mfma_f32_16x16x32_bf16 v[84:87], a[212:215], v[96:99], v[84:87]\n"
"v_mfma_f32_16x16x32_bf16 v[92:95], a[216:219], v[96:99], v[92:95]\n"
"v_mfma_f32_16x16x32_bf16 v[112:115], a[220:223], v[96:99], v[112:115]\n"
"v_mfma_f32_16x16x32_bf16 v[68:71], a[224:227], v[104:107], v[68:71]\n"
"v_mfma_f32_16x16x32_bf16 v[84:87], a[228:231], v[104:107], v[84:87]\n"
"v_mfma_f32_16x16x32_bf16 v[92:95], a[232:235], v[104:107], v[92:95]\n"
"v_mfma_f32_16x16x32_bf16 v[112:115], a[236:239], v[104:107], v[112:115]\n"
"v_mfma_f32_16x16x32_bf16 v[68:71], a[240:243], v[124:127], v[68:71]\n"
"v_mfma_f32_16x16x32_bf16 v[84:87], a[244:247], v[124:127], v[84:87]\n"
"v_mfma_f32_16x16x32_bf16 v[92:95], a[248:251], v[124:127], v[92:95]\n"
"v_mfma_f32_16x16x32_bf16 v[112:115], a[252:255], v[124:127], v[112:115]\n"
"s_add_u32 s99, s59, 0x400000\n"
"v_mov_b32_e32 v244, 0x80000000\n"
"buffer_load_dwordx4 v[100:103], v0, s[4:7], s99 offen sc1\n"
"buffer_load_dwordx4 v[88:91], v0, s[4:7], s99 offen offset:1024 sc1\n"
"buffer_load_dwordx4 v[72:75], v0, s[4:7], s99 offen offset:2048 sc1\n"
"buffer_load_dwordx4 v[116:119], v0, s[4:7], s99 offen offset:3072 sc1\n"
"buffer_load_dwordx4 v[80:83], v1, s[4:7], s99 offen sc1\n"
"buffer_load_dwordx4 v[96:99], v1, s[4:7], s99 offen offset:1024 sc1\n"
"buffer_load_dwordx4 v[104:107], v1, s[4:7], s99 offen offset:2048 sc1\n"
"buffer_load_dwordx4 v[124:127], v1, s[4:7], s99 offen offset:3072 sc1\n"
"buffer_store_dword v244, v244, s[4:7], 0 offen\n"
"s_nop 7\n"
"s_branch .Lmk_p67r\n"
".Lmk_q69s:\n"
";\n"
"s_mov_b32 s1, 0x10000\n"
".Lmk_q69l:\n"
"s_add_u32 s99, s59, 0x400000\n"
"buffer_load_dwordx4 v[100:103], v0, s[4:7], s99 offen sc1\n"
"buffer_load_dwordx4 v[88:91], v0, s[4:7], s99 offen offset:1024 sc1\n"
"buffer_load_dwordx4 v[72:75], v0, s[4:7], s99 offen offset:2048 sc1\n"
"buffer_load_dwordx4 v[116:119], v0, s[4:7], s99 offen offset:3072 sc1\n"
"buffer_load_dwordx4 v[80:83], v1, s[4:7], s99 offen sc1\n"
"buffer_load_dwordx4 v[96:99], v1, s[4:7], s99 offen offset:1024 sc1\n"
"buffer_load_dwordx4 v[104:107], v1, s[4:7], s99 offen offset:2048 sc1\n"
"buffer_load_dwordx4 v[124:127], v1, s[4:7], s99 offen offset:3072 sc1\n"
"s_waitcnt vmcnt(0)\n"
"v_max_u32_e32 v255, v100, v101\n"
"v_max3_u32 v255, v255, v102, v103\n"
"v_max3_u32 v255, v255, v88, v89\n"
"v_max3_u32 v255, v255, v90, v91\n"
"v_max3_u32 v255, v255, v72, v73\n"
"v_max3_u32 v255, v255, v74, v75\n"
"v_max3_u32 v255, v255, v116, v117\n"
"v_max3_u32 v255, v255, v118, v119\n"
"v_cmp_eq_u32_e32 vcc, -1, v255\n"
"s_cbranch_vccz .Lmk_q69k\n"
"s_sub_u32 s1, s1, 1\n"
"s_cbranch_scc0 .Lmk_q69l\n"
".Lmk_q69k:\n"
"s_branch .Lmk_q69r\n"
".Lmk_p68s:\n"
";\n"
"s_mov_b32 s1, 0x10000\n"
".Lmk_p68l:\n"
"s_add_u32 s99, s58, 0x400000\n"
"buffer_load_dwordx4 v[24:27], v0, s[4:7], s99 offen sc1\n"
"buffer_load_dwordx4 v[28:31], v0, s[4:7], s99 offen offset:1024 sc1\n"
"buffer_load_dwordx4 v[32:35], v0, s[4:7], s99 offen offset:2048 sc1\n"
"buffer_load_dwordx4 v[36:39], v0, s[4:7], s99 offen offset:3072 sc1\n"
"buffer_load_dwordx4 v[40:43], v1, s[4:7], s99 offen sc1\n"
"buffer_load_dwordx4 v[60:63], v1, s[4:7], s99 offen offset:1024 sc1\n"
"buffer_load_dwordx4 v[64:67], v1, s[4:7], s99 offen offset:2048 sc1\n"
"buffer_load_dwordx4 v[68:71], v1, s[4:7], s99 offen offset:3072 sc1\n"
"s_add_u32 s99, s59, 0x400000\n"
"buffer_load_dwordx4 v[100:103], v0, s[4:7], s99 offen sc1\n"
"buffer_load_dwordx4 v[88:91], v0, s[4:7], s99 offen offset:1024 sc1\n"
"buffer_load_dwordx4 v[72:75], v0, s[4:7], s99 offen offset:2048 sc1\n"
"buffer_load_dwordx4 v[116:119], v0, s[4:7], s99 offen offset:3072 sc1\n"
"buffer_load_dwordx4 v[80:83], v1, s[4:7], s99 offen sc1\n"
"buffer_load_dwordx4 v[96:99], v1, s[4:7], s99 offen offset:1024 sc1\n"
"buffer_load_dwordx4 v[104:107], v1, s[4:7], s99 offen offset:2048 sc1\n"
"buffer_load_dwordx4 v[124:127], v1, s[4:7], s99 offen offset:3072 sc1\n"
"s_waitcnt vmcnt(0)\n"
"v_max_u32_e32 v255, v24, v25\n"
"v_max3_u32 v255, v255, v26, v27\n"
"v_max3_u32 v255, v255, v28, v29\n"
"v_max3_u32 v255, v255, v30, v31\n"
"v_max3_u32 v255, v255, v32, v33\n"
"v_max3_u32 v255, v255, v34, v35\n"
"v_max3_u32 v255, v255, v36, v37\n"
"v_max3_u32 v255, v255, v38, v39\n"
"v_max3_u32 v255, v255, v40, v41\n"
"v_max3_u32 v255, v255, v42, v43\n"
"v_max3_u32 v255, v255, v60, v61\n"
"v_max3_u32 v255, v255, v62, v63\n"
"v_max3_u32 v255, v255, v64, v65\n"
"v_max3_u32 v255, v255, v66, v67\n"
"v_max3_u32 v255, v255, v68, v69\n"
"v_max3_u32 v255, v255, v70, v71\n"
"v_cmp_eq_u32_e32 vcc, -1, v255\n"
"s_cbranch_vccz .Lmk_p68k\n"
"s_sub_u32 s1, s1, 1\n"
"s_cbranch_scc0 .Lmk_p68l\n"
".Lmk_p68k:\n"
"v_mfma_f32_16x16x32_bf16 v[108:111], a[128:131], v[24:27], 0\n"
"v_mfma_f32_16x16x32_bf16 v[120:123], a[132:135], v[24:27], 0\n"
"v_mfma_f32_16x16x32_bf16 v[132:135], a[136:139], v[24:27], 0\n"
"v_mfma_f32_16x16x32_bf16 v[156:159], a[140:143], v[24:27], 0\n"
"v_mfma_f32_16x16x32_bf16 v[108:111], a[144:147], v[28:31], v[108:111]\n"
"v_mfma_f32_16x16x32_bf16 v[120:123], a[148:151], v[28:31], v[120:123]\n"
"v_mfma_f32_16x16x32_bf16 v[132:135], a[152:155], v[28:31], v[132:135]\n"
"v_mfma_f32_16x16x32_bf16 v[156:159], a[156:159], v[28:31], v[156:159]\n"
"v_mfma_f32_16x16x32_bf16 v[108:111], a[160:163], v[32:35], v[108:111]\n"
"v_mfma_f32_16x16x32_bf16 v[120:123], a[164:167], v[32:35], v[120:123]\n"
"v_mfma_f32_16x16x32_bf16 v[132:135], a[168:171], v[32:35], v[132:135]\n"
"v_mfma_f32_16x16x32_bf16 v[156:159], a[172:175], v[32:35], v[156:159]\n"
"v_mfma_f32_16x16x32_bf16 v[108:111], a[176:179], v[36:39], v[108:111]\n"
"v_mfma_f32_16x16x32_bf16 v[120:123], a[180:183], v[36:39], v[120:123]\n"
"v_mfma_f32_16x16x32_bf16 v[132:135], a[184:187], v[36:39], v[132:135]\n"
"v_mfma_f32_16x16x32_bf16 v[156:159], a[188:191], v[36:39], v[156:159]\n"
"v_mfma_f32_16x16x32_bf16 v[108:111], a[192:195], v[40:43], v[108:111]\n"
"v_mfma_f32_16x16x32_bf16 v[120:123], a[196:199], v[40:43], v[120:123]\n"
"v_mfma_f32_16x16x32_bf16 v[132:135], a[200:203], v[40:43], v[132:135]\n"
"v_mfma_f32_16x16x32_bf16 v[156:159], a[204:207], v[40:43], v[156:159]\n"
"v_mfma_f32_16x16x32_bf16 v[108:111], a[208:211], v[60:63], v[108:111]\n"
"v_mfma_f32_16x16x32_bf16 v[120:123], a[212:215], v[60:63], v[120:123]\n"
"v_mfma_f32_16x16x32_bf16 v[132:135], a[216:219], v[60:63], v[132:135]\n"
"v_mfma_f32_16x16x32_bf16 v[156:159], a[220:223], v[60:63], v[156:159]\n"
"v_mfma_f32_16x16x32_bf16 v[108:111], a[224:227], v[64:67], v[108:111]\n"
"v_mfma_f32_16x16x32_bf16 v[120:123], a[228:231], v[64:67], v[120:123]\n"
"v_mfma_f32_16x16x32_bf16 v[132:135], a[232:235], v[64:67], v[132:135]\n"
"v_mfma_f32_16x16x32_bf16 v[156:159], a[236:239], v[64:67], v[156:159]\n"
"v_mfma_f32_16x16x32_bf16 v[108:111], a[240:243], v[68:71], v[108:111]\n"
"v_mfma_f32_16x16x32_bf16 v[120:123], a[244:247], v[68:71], v[120:123]\n"
"v_mfma_f32_16x16x32_bf16 v[132:135], a[248:251], v[68:71], v[132:135]\n"
"v_mfma_f32_16x16x32_bf16 v[156:159], a[252:255], v[68:71], v[156:159]\n"
"s_nop 7\n"
"s_branch .Lmk_p68r\n"
".Lmk_q70s:\n"
";\n"
"s_mov_b32 s1, 0x10000\n"
".Lmk_q70l:\n"
"s_add_u32 s99, s59, 0x400000\n"
"buffer_load_dwordx4 v[80:83], v1, s[4:7], s99 offen sc1\n"
"buffer_load_dwordx4 v[96:99], v1, s[4:7], s99 offen offset:1024 sc1\n"
"buffer_load_dwordx4 v[104:107], v1, s[4:7], s99 offen offset:2048 sc1\n"
"buffer_load_dwordx4 v[124:127], v1, s[4:7], s99 offen offset:3072 sc1\n"
"s_waitcnt vmcnt(0)\n"
"v_max_u32_e32 v255, v80, v81\n"
"v_max3_u32 v255, v255, v82, v83\n"
"v_max3_u32 v255, v255, v96, v97\n"
"v_max3_u32 v255, v255, v98, v99\n"
"v_max3_u32 v255, v255, v104, v105\n"
"v_max3_u32 v255, v255, v106, v107\n"
"v_max3_u32 v255, v255, v124, v125\n"
"v_max3_u32 v255, v255, v126, v127\n"
"v_cmp_eq_u32_e32 vcc, -1, v255\n"
"s_cbranch_vccz .Lmk_q70k\n"
"s_sub_u32 s1, s1, 1\n"
"s_cbranch_scc0 .Lmk_q70l\n"
".Lmk_q70k:\n"
"s_branch .Lmk_q70r\n"
".Lmk_p71s:\n"
";\n"
"s_mov_b32 s1, 0x10000\n"
".Lmk_p71l:\n"
"s_add_u32 s97, s56, 0x440000\n"
"buffer_load_dwordx4 v[88:91], v0, s[4:7], s97 offen sc1\n"
"buffer_load_dwordx4 v[92:95], v0, s[4:7], s97 offen offset:1024 sc1\n"
"buffer_load_dwordx4 v[96:99], v0, s[4:7], s97 offen offset:2048 sc1\n"
"buffer_load_dwordx4 v[100:103], v0, s[4:7], s97 offen offset:3072 sc1\n"
"buffer_load_dwordx4 v[104:107], v1, s[4:7], s97 offen sc1\n"
"buffer_load_dwordx4 v[108:111], v1, s[4:7], s97 offen offset:1024 sc1\n"
"buffer_load_dwordx4 v[112:115], v1, s[4:7], s97 offen offset:2048 sc1\n"
"buffer_load_dwordx4 v[116:119], v1, s[4:7], s97 offen offset:3072 sc1\n"
"s_add_u32 s97, s57, 0x440000\n"
"buffer_load_dwordx4 v[40:43], v0, s[4:7], s97 offen sc1\n"
"buffer_load_dwordx4 v[44:47], v0, s[4:7], s97 offen offset:1024 sc1\n"
"buffer_load_dwordx4 v[48:51], v0, s[4:7], s97 offen offset:2048 sc1\n"
"buffer_load_dwordx4 v[60:63], v0, s[4:7], s97 offen offset:3072 sc1\n"
"buffer_load_dwordx4 v[72:75], v1, s[4:7], s97 offen sc1\n"
"buffer_load_dwordx4 v[76:79], v1, s[4:7], s97 offen offset:1024 sc1\n"
"buffer_load_dwordx4 v[80:83], v1, s[4:7], s97 offen offset:2048 sc1\n"
"buffer_load_dwordx4 v[84:87], v1, s[4:7], s97 offen offset:3072 sc1\n"
"s_add_u32 s97, s59, 0x440000\n"
"buffer_load_dwordx4 v[24:27], v0, s[4:7], s97 offen sc1\n"
"buffer_load_dwordx4 v[28:31], v0, s[4:7], s97 offen offset:1024 sc1\n"
"buffer_load_dwordx4 v[32:35], v0, s[4:7], s97 offen offset:2048 sc1\n"
"buffer_load_dwordx4 v[36:39], v0, s[4:7], s97 offen offset:3072 sc1\n"
"buffer_load_dwordx4 v[52:55], v1, s[4:7], s97 offen sc1\n"
"buffer_load_dwordx4 v[56:59], v1, s[4:7], s97 offen offset:1024 sc1\n"
"buffer_load_dwordx4 v[64:67], v1, s[4:7], s97 offen offset:2048 sc1\n"
"s_waitcnt vmcnt(0)\n"
"v_max_u32_e32 v255, v88, v89\n"
"v_max3_u32 v255, v255, v90, v91\n"
"v_max3_u32 v255, v255, v92, v93\n"
"v_max3_u32 v255, v255, v94, v95\n"
"v_max3_u32 v255, v255, v96, v97\n"
"v_max3_u32 v255, v255, v98, v99\n"
"v_max3_u32 v255, v255, v100, v101\n"
"v_max3_u32 v255, v255, v102, v103\n"
"v_max3_u32 v255, v255, v104, v105\n"
"v_max3_u32 v255, v255, v106, v107\n"
"v_max3_u32 v255, v255, v108, v109\n"
"v_max3_u32 v255, v255, v110, v111\n"
"v_max3_u32 v255, v255, v112, v113\n"
"v_max3_u32 v255, v255, v114, v115\n"
"v_max3_u32 v255, v255, v116, v117\n"
"v_max3_u32 v255, v255, v118, v119\n"
"v_cmp_eq_u32_e32 vcc, -1, v255\n"
"s_cbranch_vccz .Lmk_p71k\n"
"s_sub_u32 s1, s1, 1\n"
"s_cbranch_scc0 .Lmk_p71l\n"
".Lmk_p71k:\n"
"v_mfma_f32_16x16x32_bf16 v[120:123], a[128:131], v[88:91], 0\n"
"v_mfma_f32_16x16x32_bf16 v[124:127], a[132:135], v[88:91], 0\n"
"v_mfma_f32_16x16x32_bf16 v[128:131], a[136:139], v[88:91], 0\n"
"v_mfma_f32_16x16x32_bf16 v[132:135], a[140:143], v[88:91], 0\n"
"v_mfma_f32_16x16x32_bf16 v[120:123], a[144:147], v[92:95], v[120:123]\n"
"v_mfma_f32_16x16x32_bf16 v[124:127], a[148:151], v[92:95], v[124:127]\n"
"v_mfma_f32_16x16x32_bf16 v[128:131], a[152:155], v[92:95], v[128:131]\n"
"v_mfma_f32_16x16x32_bf16 v[132:135], a[156:159], v[92:95], v[132:135]\n"
"v_mfma_f32_16x16x32_bf16 v[120:123], a[160:163], v[96:99], v[120:123]\n"
"v_mfma_f32_16x16x32_bf16 v[124:127], a[164:167], v[96:99], v[124:127]\n"
"v_mfma_f32_16x16x32_bf16 v[128:131], a[168:171], v[96:99], v[128:131]\n"
"v_mfma_f32_16x16x32_bf16 v[132:135], a[172:175], v[96:99], v[132:135]\n"
"v_mfma_f32_16x16x32_bf16 v[120:123], a[176:179], v[100:103], v[120:123]\n"
"v_mfma_f32_16x16x32_bf16 v[124:127], a[180:183], v[100:103], v[124:127]\n"
"v_mfma_f32_16x16x32_bf16 v[128:131], a[184:187], v[100:103], v[128:131]\n"
"v_mfma_f32_16x16x32_bf16 v[132:135], a[188:191], v[100:103], v[132:135]\n"
"v_mfma_f32_16x16x32_bf16 v[120:123], a[192:195], v[104:107], v[120:123]\n"
"v_mfma_f32_16x16x32_bf16 v[124:127], a[196:199], v[104:107], v[124:127]\n"
"v_mfma_f32_16x16x32_bf16 v[128:131], a[200:203], v[104:107], v[128:131]\n"
"v_mfma_f32_16x16x32_bf16 v[132:135], a[204:207], v[104:107], v[132:135]\n"
"v_mfma_f32_16x16x32_bf16 v[120:123], a[208:211], v[108:111], v[120:123]\n"
"v_mfma_f32_16x16x32_bf16 v[124:127], a[212:215], v[108:111], v[124:127]\n"
"v_mfma_f32_16x16x32_bf16 v[128:131], a[216:219], v[108:111], v[128:131]\n"
"v_mfma_f32_16x16x32_bf16 v[132:135], a[220:223], v[108:111], v[132:135]\n"
"v_mfma_f32_16x16x32_bf16 v[120:123], a[224:227], v[112:115], v[120:123]\n"
"v_mfma_f32_16x16x32_bf16 v[124:127], a[228:231], v[112:115], v[124:127]\n"
"v_mfma_f32_16x16x32_bf16 v[128:131], a[232:235], v[112:115], v[128:131]\n"
"v_mfma_f32_16x16x32_bf16 v[132:135], a[236:239], v[112:115], v[132:135]\n"
"v_mfma_f32_16x16x32_bf16 v[120:123], a[240:243], v[116:119], v[120:123]\n"
"v_mfma_f32_16x16x32_bf16 v[124:127], a[244:247], v[116:119], v[124:127]\n"
"v_mfma_f32_16x16x32_bf16 v[128:131], a[248:251], v[116:119], v[128:131]\n"
"v_mfma_f32_16x16x32_bf16 v[132:135], a[252:255], v[116:119], v[132:135]\n"
"s_add_u32 s97, s58, 0x440000\n"
"v_mov_b32_e32 v255, 0x80000000\n"
"buffer_load_dwordx4 v[88:91], v0, s[4:7], s97 offen sc1\n"
"buffer_load_dwordx4 v[92:95], v0, s[4:7], s97 offen offset:1024 sc1\n"
"buffer_load_dwordx4 v[96:99], v0, s[4:7], s97 offen offset:2048 sc1\n"
"buffer_load_dwordx4 v[100:103], v0, s[4:7], s97 offen offset:3072 sc1\n"
"buffer_load_dwordx4 v[104:107], v1, s[4:7], s97 offen sc1\n"
"buffer_load_dwordx4 v[108:111], v1, s[4:7], s97 offen offset:1024 sc1\n"
"buffer_load_dwordx4 v[112:115], v1, s[4:7], s97 offen offset:2048 sc1\n"
"buffer_load_dwordx4 v[116:119], v1, s[4:7], s97 offen offset:3072 sc1\n"
"buffer_store_dword v255, v255, s[4:7], 0 offen\n"
"buffer_store_dword v255, v255, s[4:7], 0 offen\n"
"buffer_store_dword v255, v255, s[4:7], 0 offen\n"
"buffer_store_dword v255, v255, s[4:7], 0 offen\n"
"buffer_store_dword v255, v255, s[4:7], 0 offen\n"
"buffer_store_dword v255, v255, s[4:7], 0 offen\n"
"buffer_store_dword v255, v255, s[4:7], 0 offen\n"
"buffer_store_dword v255, v255, s[4:7], 0 offen\n"
"buffer_store_dword v255, v255, s[4:7], 0 offen\n"
"buffer_store_dword v255, v255, s[4:7], 0 offen\n"
"buffer_store_dword v255, v255, s[4:7], 0 offen\n"
"buffer_store_dword v255, v255, s[4:7], 0 offen\n"
"buffer_store_dword v255, v255, s[4:7], 0 offen\n"
"buffer_store_dword v255, v255, s[4:7], 0 offen\n"
"buffer_store_dword v255, v255, s[4:7], 0 offen\n"
"s_nop 7\n"
"s_branch .Lmk_p71r\n"
".Lmk_p72s:\n"
";\n"
"s_mov_b32 s1, 0x10000\n"
".Lmk_p72l:\n"
"s_add_u32 s97, s57, 0x440000\n"
"buffer_load_dwordx4 v[24:27], v0, s[4:7], s97 offen sc1\n"
"buffer_load_dwordx4 v[28:31], v0, s[4:7], s97 offen offset:1024 sc1\n"
"buffer_load_dwordx4 v[32:35], v0, s[4:7], s97 offen offset:2048 sc1\n"
"buffer_load_dwordx4 v[36:39], v0, s[4:7], s97 offen offset:3072 sc1\n"
"buffer_load_dwordx4 v[52:55], v1, s[4:7], s97 offen sc1\n"
"buffer_load_dwordx4 v[56:59], v1, s[4:7], s97 offen offset:1024 sc1\n"
"buffer_load_dwordx4 v[64:67], v1, s[4:7], s97 offen offset:2048 sc1\n"
"buffer_load_dwordx4 v[144:147], v1, s[4:7], s97 offen offset:3072 sc1\n"
"s_add_u32 s97, s58, 0x440000\n"
"buffer_load_dwordx4 v[88:91], v0, s[4:7], s97 offen sc1\n"
"buffer_load_dwordx4 v[92:95], v0, s[4:7], s97 offen offset:1024 sc1\n"
"buffer_load_dwordx4 v[96:99], v0, s[4:7], s97 offen offset:2048 sc1\n"
"buffer_load_dwordx4 v[100:103], v0, s[4:7], s97 offen offset:3072 sc1\n"
"buffer_load_dwordx4 v[104:107], v1, s[4:7], s97 offen sc1\n"
"buffer_load_dwordx4 v[108:111], v1, s[4:7], s97 offen offset:1024 sc1\n"
"buffer_load_dwordx4 v[112:115], v1, s[4:7], s97 offen offset:2048 sc1\n"
"buffer_load_dwordx4 v[116:119], v1, s[4:7], s97 offen offset:3072 sc1\n"
"s_add_u32 s97, s56, 0x400000\n"
"buffer_load_dwordx4 v[40:43], v0, s[4:7], s97 offen sc1\n"
"buffer_load_dwordx4 v[120:123], v0, s[4:7], s97 offen offset:1024 sc1\n"
"buffer_load_dwordx4 v[60:63], v0, s[4:7], s97 offen offset:2048 sc1\n"
"buffer_load_dwordx4 v[72:75], v0, s[4:7], s97 offen offset:3072 sc1\n"
"buffer_load_dwordx4 v[76:79], v1, s[4:7], s97 offen sc1\n"
"buffer_load_dwordx4 v[80:83], v1, s[4:7], s97 offen offset:1024 sc1\n"
"buffer_load_dwordx4 v[44:47], v1, s[4:7], s97 offen offset:2048 sc1\n"
"s_waitcnt vmcnt(0)\n"
"v_max_u32_e32 v243, v24, v25\n"
"v_max3_u32 v243, v243, v26, v27\n"
"v_max3_u32 v243, v243, v28, v29\n"
"v_max3_u32 v243, v243, v30, v31\n"
"v_max3_u32 v243, v243, v32, v33\n"
"v_max3_u32 v243, v243, v34, v35\n"
"v_max3_u32 v243, v243, v36, v37\n"
"v_max3_u32 v243, v243, v38, v39\n"
"v_max3_u32 v243, v243, v52, v53\n"
"v_max3_u32 v243, v243, v54, v55\n"
"v_max3_u32 v243, v243, v56, v57\n"
"v_max3_u32 v243, v243, v58, v59\n"
"v_max3_u32 v243, v243, v64, v65\n"
"v_max3_u32 v243, v243, v66, v67\n"
"v_max3_u32 v243, v243, v144, v145\n"
"v_max3_u32 v243, v243, v146, v147\n"
"v_cmp_eq_u32_e32 vcc, -1, v243\n"
"s_cbranch_vccz .Lmk_p72k\n"
"s_sub_u32 s1, s1, 1\n"
"s_cbranch_scc0 .Lmk_p72l\n"
".Lmk_p72k:\n"
"v_mfma_f32_16x16x32_bf16 v[68:71], a[128:131], v[24:27], 0\n"
"v_mfma_f32_16x16x32_bf16 v[136:139], a[132:135], v[24:27], 0\n"
"v_mfma_f32_16x16x32_bf16 v[140:143], a[136:139], v[24:27], 0\n"
"v_mfma_f32_16x16x32_bf16 v[148:151], a[140:143], v[24:27], 0\n"
"v_mfma_f32_16x16x32_bf16 v[68:71], a[144:147], v[28:31], v[68:71]\n"
"v_mfma_f32_16x16x32_bf16 v[136:139], a[148:151], v[28:31], v[136:139]\n"
"v_mfma_f32_16x16x32_bf16 v[140:143], a[152:155], v[28:31], v[140:143]\n"
"v_mfma_f32_16x16x32_bf16 v[148:151], a[156:159], v[28:31], v[148:151]\n"
"v_mfma_f32_16x16x32_bf16 v[68:71], a[160:163], v[32:35], v[68:71]\n"
"v_mfma_f32_16x16x32_bf16 v[136:139], a[164:167], v[32:35], v[136:139]\n"
"v_mfma_f32_16x16x32_bf16 v[140:143], a[168:171], v[32:35], v[140:143]\n"
"v_mfma_f32_16x16x32_bf16 v[148:151], a[172:175], v[32:35], v[148:151]\n"
"v_mfma_f32_16x16x32_bf16 v[68:71], a[176:179], v[36:39], v[68:71]\n"
"v_mfma_f32_16x16x32_bf16 v[136:139], a[180:183], v[36:39], v[136:139]\n"
"v_mfma_f32_16x16x32_bf16 v[140:143], a[184:187], v[36:39], v[140:143]\n"
"v_mfma_f32_16x16x32_bf16 v[148:151], a[188:191], v[36:39], v[148:151]\n"
"v_mfma_f32_16x16x32_bf16 v[68:71], a[192:195], v[52:55], v[68:71]\n"
"v_mfma_f32_16x16x32_bf16 v[136:139], a[196:199], v[52:55], v[136:139]\n"
"v_mfma_f32_16x16x32_bf16 v[140:143], a[200:203], v[52:55], v[140:143]\n"
"v_mfma_f32_16x16x32_bf16 v[148:151], a[204:207], v[52:55], v[148:151]\n"
"v_mfma_f32_16x16x32_bf16 v[68:71], a[208:211], v[56:59], v[68:71]\n"
"v_mfma_f32_16x16x32_bf16 v[136:139], a[212:215], v[56:59], v[136:139]\n"
"v_mfma_f32_16x16x32_bf16 v[140:143], a[216:219], v[56:59], v[140:143]\n"
"v_mfma_f32_16x16x32_bf16 v[148:151], a[220:223], v[56:59], v[148:151]\n"
"v_mfma_f32_16x16x32_bf16 v[68:71], a[224:227], v[64:67], v[68:71]\n"
"v_mfma_f32_16x16x32_bf16 v[136:139], a[228:231], v[64:67], v[136:139]\n"
"v_mfma_f32_16x16x32_bf16 v[140:143], a[232:235], v[64:67], v[140:143]\n"
"v_mfma_f32_16x16x32_bf16 v[148:151], a[236:239], v[64:67], v[148:151]\n"
"v_mfma_f32_16x16x32_bf16 v[68:71], a[240:243], v[144:147], v[68:71]\n"
"v_mfma_f32_16x16x32_bf16 v[136:139], a[244:247], v[144:147], v[136:139]\n"
"v_mfma_f32_16x16x32_bf16 v[140:143], a[248:251], v[144:147], v[140:143]\n"
"v_mfma_f32_16x16x32_bf16 v[148:151], a[252:255], v[144:147], v[148:151]\n"
"s_add_u32 s97, s59, 0x440000\n"
"v_mov_b32_e32 v243, 0x80000000\n"
"buffer_load_dwordx4 v[24:27], v0, s[4:7], s97 offen sc1\n"
"buffer_load_dwordx4 v[28:31], v0, s[4:7], s97 offen offset:1024 sc1\n"
"buffer_load_dwordx4 v[32:35], v0, s[4:7], s97 offen offset:2048 sc1\n"
"buffer_load_dwordx4 v[36:39], v0, s[4:7], s97 offen offset:3072 sc1\n"
"buffer_load_dwordx4 v[52:55], v1, s[4:7], s97 offen sc1\n"
"buffer_load_dwordx4 v[56:59], v1, s[4:7], s97 offen offset:1024 sc1\n"
"buffer_load_dwordx4 v[64:67], v1, s[4:7], s97 offen offset:2048 sc1\n"
"buffer_load_dwordx4 v[144:147], v1, s[4:7], s97 offen offset:3072 sc1\n"
"buffer_store_dword v243, v243, s[4:7], 0 offen\n"
"buffer_store_dword v243, v243, s[4:7], 0 offen\n"
"buffer_store_dword v243, v243, s[4:7], 0 offen\n"
"buffer_store_dword v243, v243, s[4:7], 0 offen\n"
"buffer_store_dword v243, v243, s[4:7], 0 offen\n"
"buffer_store_dword v243, v243, s[4:7], 0 offen\n"
"buffer_store_dword v243, v243, s[4:7], 0 offen\n"
"s_nop 7\n"
"s_branch .Lmk_p72r\n"
".Lmk_q74s:\n"
";\n"
"s_mov_b32 s1, 0x10000\n"
".Lmk_q74l:\n"
"s_add_u32 s97, s59, 0x440000\n"
"buffer_load_dwordx4 v[24:27], v0, s[4:7], s97 offen sc1\n"
"buffer_load_dwordx4 v[28:31], v0, s[4:7], s97 offen offset:1024 sc1\n"
"buffer_load_dwordx4 v[32:35], v0, s[4:7], s97 offen offset:2048 sc1\n"
"buffer_load_dwordx4 v[36:39], v0, s[4:7], s97 offen offset:3072 sc1\n"
"buffer_load_dwordx4 v[52:55], v1, s[4:7], s97 offen sc1\n"
"buffer_load_dwordx4 v[56:59], v1, s[4:7], s97 offen offset:1024 sc1\n"
"buffer_load_dwordx4 v[64:67], v1, s[4:7], s97 offen offset:2048 sc1\n"
"buffer_load_dwordx4 v[144:147], v1, s[4:7], s97 offen offset:3072 sc1\n"
"s_add_u32 s97, s56, 0x400000\n"
"buffer_load_dwordx4 v[40:43], v0, s[4:7], s97 offen sc1\n"
"buffer_load_dwordx4 v[120:123], v0, s[4:7], s97 offen offset:1024 sc1\n"
"buffer_load_dwordx4 v[60:63], v0, s[4:7], s97 offen offset:2048 sc1\n"
"buffer_load_dwordx4 v[72:75], v0, s[4:7], s97 offen offset:3072 sc1\n"
"buffer_load_dwordx4 v[76:79], v1, s[4:7], s97 offen sc1\n"
"buffer_load_dwordx4 v[80:83], v1, s[4:7], s97 offen offset:1024 sc1\n"
"buffer_load_dwordx4 v[44:47], v1, s[4:7], s97 offen offset:2048 sc1\n"
"buffer_load_dwordx4 v[164:167], v1, s[4:7], s97 offen offset:3072 sc1\n"
"s_add_u32 s97, s57, 0x400000\n"
"buffer_load_dwordx4 v[68:71], v0, s[4:7], s97 offen sc1\n"
"buffer_load_dwordx4 v[88:91], v0, s[4:7], s97 offen offset:1024 sc1\n"
"buffer_load_dwordx4 v[48:51], v0, s[4:7], s97 offen offset:2048 sc1\n"
"buffer_load_dwordx4 v[92:95], v0, s[4:7], s97 offen offset:3072 sc1\n"
"buffer_load_dwordx4 v[96:99], v1, s[4:7], s97 offen sc1\n"
"buffer_load_dwordx4 v[100:103], v1, s[4:7], s97 offen offset:1024 sc1\n"
"s_waitcnt vmcnt(0)\n"
"v_max_u32_e32 v255, v24, v25\n"
"v_max3_u32 v255, v255, v26, v27\n"
"v_max3_u32 v255, v255, v28, v29\n"
"v_max3_u32 v255, v255, v30, v31\n"
"v_max3_u32 v255, v255, v32, v33\n"
"v_max3_u32 v255, v255, v34, v35\n"
"v_max3_u32 v255, v255, v36, v37\n"
"v_max3_u32 v255, v255, v38, v39\n"
"v_cmp_eq_u32_e32 vcc, -1, v255\n"
"s_cbranch_vccz .Lmk_q74k\n"
"s_sub_u32 s1, s1, 1\n"
"s_cbranch_scc0 .Lmk_q74l\n"
".Lmk_q74k:\n"
"s_branch .Lmk_q74r\n"
".Lmk_p73s:\n"
";\n"
"s_mov_b32 s1, 0x10000\n"
".Lmk_p73l:\n"
"s_add_u32 s97, s58, 0x440000\n"
"buffer_load_dwordx4 v[40:43], v0, s[4:7], s97 offen sc1\n"
"buffer_load_dwordx4 v[120:123], v0, s[4:7], s97 offen offset:1024 sc1\n"
"buffer_load_dwordx4 v[60:63], v0, s[4:7], s97 offen offset:2048 sc1\n"
"buffer_load_dwordx4 v[72:75], v0, s[4:7], s97 offen offset:3072 sc1\n"
"buffer_load_dwordx4 v[76:79], v1, s[4:7], s97 offen sc1\n"
"buffer_load_dwordx4 v[80:83], v1, s[4:7], s97 offen offset:1024 sc1\n"
"buffer_load_dwordx4 v[44:47], v1, s[4:7], s97 offen offset:2048 sc1\n"
"buffer_load_dwordx4 v[164:167], v1, s[4:7], s97 offen offset:3072 sc1\n"
"s_add_u32 s97, s59, 0x440000\n"
"buffer_load_dwordx4 v[24:27], v0, s[4:7], s97 offen sc1\n"
"buffer_load_dwordx4 v[28:31], v0, s[4:7], s97 offen offset:1024 sc1\n"
"buffer_load_dwordx4 v[32:35], v0, s[4:7], s97 offen offset:2048 sc1\n"
"buffer_load_dwordx4 v[36:39], v0, s[4:7], s97 offen offset:3072 sc1\n"
"buffer_load_dwordx4 v[52:55], v1, s[4:7], s97 offen sc1\n"
"buffer_load_dwordx4 v[56:59], v1, s[4:7], s97 offen offset:1024 sc1\n"
"buffer_load_dwordx4 v[64:67], v1, s[4:7], s97 offen offset:2048 sc1\n"
"buffer_load_dwordx4 v[144:147], v1, s[4:7], s97 offen offset:3072 sc1\n"
"s_add_u32 s97, s57, 0x400000\n"
"buffer_load_dwordx4 v[68:71], v0, s[4:7], s97 offen sc1\n"
"buffer_load_dwordx4 v[88:91], v0, s[4:7], s97 offen offset:1024 sc1\n"
"buffer_load_dwordx4 v[48:51], v0, s[4:7], s97 offen offset:2048 sc1\n"
"buffer_load_dwordx4 v[92:95], v0, s[4:7], s97 offen offset:3072 sc1\n"
"buffer_load_dwordx4 v[96:99], v1, s[4:7], s97 offen sc1\n"
"buffer_load_dwordx4 v[100:103], v1, s[4:7], s97 offen offset:1024 sc1\n"
"s_waitcnt vmcnt(0)\n"
"v_max_u32_e32 v255, v40, v41\n"
"v_max3_u32 v255, v255, v42, v43\n"
"v_max3_u32 v255, v255, v120, v121\n"
"v_max3_u32 v255, v255, v122, v123\n"
"v_max3_u32 v255, v255, v60, v61\n"
"v_max3_u32 v255, v255, v62, v63\n"
"v_max3_u32 v255, v255, v72, v73\n"
"v_max3_u32 v255, v255, v74, v75\n"
"v_max3_u32 v255, v255, v76, v77\n"
"v_max3_u32 v255, v255, v78, v79\n"
"v_max3_u32 v255, v255, v80, v81\n"
"v_max3_u32 v255, v255, v82, v83\n"
"v_max3_u32 v255, v255, v44, v45\n"
"v_max3_u32 v255, v255, v46, v47\n"
"v_max3_u32 v255, v255, v164, v165\n"
"v_max3_u32 v255, v255, v166, v167\n"
"v_cmp_eq_u32_e32 vcc, -1, v255\n"
"s_cbranch_vccz .Lmk_p73k\n"
"s_sub_u32 s1, s1, 1\n"
"s_cbranch_scc0 .Lmk_p73l\n"
".Lmk_p73k:\n"
"v_mfma_f32_16x16x32_bf16 v[84:87], a[128:131], v[40:43], 0\n"
"v_mfma_f32_16x16x32_bf16 v[156:159], a[132:135], v[40:43], 0\n"
"v_mfma_f32_16x16x32_bf16 v[160:163], a[136:139], v[40:43], 0\n"
"v_mfma_f32_16x16x32_bf16 v[168:171], a[140:143], v[40:43], 0\n"
"v_mfma_f32_16x16x32_bf16 v[84:87], a[144:147], v[120:123], v[84:87]\n"
"v_mfma_f32_16x16x32_bf16 v[156:159], a[148:151], v[120:123], v[156:159]\n"
"v_mfma_f32_16x16x32_bf16 v[160:163], a[152:155], v[120:123], v[160:163]\n"
"v_mfma_f32_16x16x32_bf16 v[168:171], a[156:159], v[120:123], v[168:171]\n"
"v_mfma_f32_16x16x32_bf16 v[84:87], a[160:163], v[60:63], v[84:87]\n"
"v_mfma_f32_16x16x32_bf16 v[156:159], a[164:167], v[60:63], v[156:159]\n"
"v_mfma_f32_16x16x32_bf16 v[160:163], a[168:171], v[60:63], v[160:163]\n"
"v_mfma_f32_16x16x32_bf16 v[168:171], a[172:175], v[60:63], v[168:171]\n"
"v_mfma_f32_16x16x32_bf16 v[84:87], a[176:179], v[72:75], v[84:87]\n"
"v_mfma_f32_16x16x32_bf16 v[156:159], a[180:183], v[72:75], v[156:159]\n"
"v_mfma_f32_16x16x32_bf16 v[160:163], a[184:187], v[72:75], v[160:163]\n"
"v_mfma_f32_16x16x32_bf16 v[168:171], a[188:191], v[72:75], v[168:171]\n"
"v_mfma_f32_16x16x32_bf16 v[84:87], a[192:195], v[76:79], v[84:87]\n"
"v_mfma_f32_16x16x32_bf16 v[156:159], a[196:199], v[76:79], v[156:159]\n"
"v_mfma_f32_16x16x32_bf16 v[160:163], a[200:203], v[76:79], v[160:163]\n"
"v_mfma_f32_16x16x32_bf16 v[168:171], a[204:207], v[76:79], v[168:171]\n"
"v_mfma_f32_16x16x32_bf16 v[84:87], a[208:211], v[80:83], v[84:87]\n"
"v_mfma_f32_16x16x32_bf16 v[156:159], a[212:215], v[80:83], v[156:159]\n"
"v_mfma_f32_16x16x32_bf16 v[160:163], a[216:219], v[80:83], v[160:163]\n"
"v_mfma_f32_16x16x32_bf16 v[168:171], a[220:223], v[80:83], v[168:171]\n"
"v_mfma_f32_16x16x32_bf16 v[84:87], a[224:227], v[44:47], v[84:87]\n"
"v_mfma_f32_16x16x32_bf16 v[156:159], a[228:231], v[44:47], v[156:159]\n"
"v_mfma_f32_16x16x32_bf16 v[160:163], a[232:235], v[44:47], v[160:163]\n"
"v_mfma_f32_16x16x32_bf16 v[168:171], a[236:239], v[44:47], v[168:171]\n"
"v_mfma_f32_16x16x32_bf16 v[84:87], a[240:243], v[164:167], v[84:87]\n"
"v_mfma_f32_16x16x32_bf16 v[156:159], a[244:247], v[164:167], v[156:159]\n"
"v_mfma_f32_16x16x32_bf16 v[160:163], a[248:251], v[164:167], v[160:163]\n"
"v_mfma_f32_16x16x32_bf16 v[168:171], a[252:255], v[164:167], v[168:171]\n"
"s_add_u32 s97, s56, 0x400000\n"
"v_mov_b32_e32 v255, 0x80000000\n"
"buffer_load_dwordx4 v[40:43], v0, s[4:7], s97 offen sc1\n"
"buffer_load_dwordx4 v[120:123], v0, s[4:7], s97 offen offset:1024 sc1\n"
"buffer_load_dwordx4 v[60:63], v0, s[4:7], s97 offen offset:2048 sc1\n"
"buffer_load_dwordx4 v[72:75], v0, s[4:7], s97 offen offset:3072 sc1\n"
"buffer_load_dwordx4 v[76:79], v1, s[4:7], s97 offen sc1\n"
"buffer_load_dwordx4 v[80:83], v1, s[4:7], s97 offen offset:1024 sc1\n"
"buffer_load_dwordx4 v[44:47], v1, s[4:7], s97 offen offset:2048 sc1\n"
"buffer_load_dwordx4 v[164:167], v1, s[4:7], s97 offen offset:3072 sc1\n"
"buffer_store_dword v255, v255, s[4:7], 0 offen\n"
"buffer_store_dword v255, v255, s[4:7], 0 offen\n"
"buffer_store_dword v255, v255, s[4:7], 0 offen\n"
"buffer_store_dword v255, v255, s[4:7], 0 offen\n"
"buffer_store_dword v255, v255, s[4:7], 0 offen\n"
"buffer_store_dword v255, v255, s[4:7], 0 offen\n"
"s_nop 7\n"
"s_branch .Lmk_p73r\n"
".Lmk_q75s:\n"
";\n"
"s_mov_b32 s1, 0x10000\n"
".Lmk_q75l:\n"
"s_add_u32 s97, s59, 0x440000\n"
"buffer_load_dwordx4 v[52:55], v1, s[4:7], s97 offen sc1\n"
"buffer_load_dwordx4 v[56:59], v1, s[4:7], s97 offen offset:1024 sc1\n"
"buffer_load_dwordx4 v[64:67], v1, s[4:7], s97 offen offset:2048 sc1\n"
"buffer_load_dwordx4 v[144:147], v1, s[4:7], s97 offen offset:3072 sc1\n"
"s_add_u32 s97, s56, 0x400000\n"
"buffer_load_dwordx4 v[40:43], v0, s[4:7], s97 offen sc1\n"
"buffer_load_dwordx4 v[120:123], v0, s[4:7], s97 offen offset:1024 sc1\n"
"buffer_load_dwordx4 v[60:63], v0, s[4:7], s97 offen offset:2048 sc1\n"
"buffer_load_dwordx4 v[72:75], v0, s[4:7], s97 offen offset:3072 sc1\n"
"buffer_load_dwordx4 v[76:79], v1, s[4:7], s97 offen sc1\n"
"buffer_load_dwordx4 v[80:83], v1, s[4:7], s97 offen offset:1024 sc1\n"
"buffer_load_dwordx4 v[44:47], v1, s[4:7], s97 offen offset:2048 sc1\n"
"buffer_load_dwordx4 v[164:167], v1, s[4:7], s97 offen offset:3072 sc1\n"
"s_add_u32 s97, s57, 0x400000\n"
"buffer_load_dwordx4 v[68:71], v0, s[4:7], s97 offen sc1\n"
"buffer_load_dwordx4 v[88:91], v0, s[4:7], s97 offen offset:1024 sc1\n"
"buffer_load_dwordx4 v[48:51], v0, s[4:7], s97 offen offset:2048 sc1\n"
"buffer_load_dwordx4 v[92:95], v0, s[4:7], s97 offen offset:3072 sc1\n"
"buffer_load_dwordx4 v[96:99], v1, s[4:7], s97 offen sc1\n"
"buffer_load_dwordx4 v[100:103], v1, s[4:7], s97 offen offset:1024 sc1\n"
"buffer_load_dwordx4 v[104:107], v1, s[4:7], s97 offen offset:2048 sc1\n"
"buffer_load_dwordx4 v[24:27], v1, s[4:7], s97 offen offset:3072 sc1\n"
"s_waitcnt vmcnt(0)\n"
"v_max_u32_e32 v255, v52, v53\n"
"v_max3_u32 v255, v255, v54, v55\n"
"v_max3_u32 v255, v255, v56, v57\n"
"v_max3_u32 v255, v255, v58, v59\n"
"v_max3_u32 v255, v255, v64, v65\n"
"v_max3_u32 v255, v255, v66, v67\n"
"v_max3_u32 v255, v255, v144, v145\n"
"v_max3_u32 v255, v255, v146, v147\n"
"v_cmp_eq_u32_e32 vcc, -1, v255\n"
"s_cbranch_vccz .Lmk_q75k\n"
"s_sub_u32 s1, s1, 1\n"
"s_cbranch_scc0 .Lmk_q75l\n"
".Lmk_q75k:\n"
"s_branch .Lmk_q75r\n"
".Lmk_p76s:\n"
";\n"
"s_mov_b32 s1, 0x10000\n"
".Lmk_p76l:\n"
"s_add_u32 s98, s56, 0x400000\n"
"buffer_load_dwordx4 v[140:143], v0, s[4:7], s98 offen sc1\n"
"buffer_load_dwordx4 v[124:127], v0, s[4:7], s98 offen offset:1024 sc1\n"
"buffer_load_dwordx4 v[36:39], v0, s[4:7], s98 offen offset:2048 sc1\n"
"buffer_load_dwordx4 v[56:59], v0, s[4:7], s98 offen offset:3072 sc1\n"
"buffer_load_dwordx4 v[132:135], v1, s[4:7], s98 offen sc1\n"
"buffer_load_dwordx4 v[112:115], v1, s[4:7], s98 offen offset:1024 sc1\n"
"buffer_load_dwordx4 v[156:159], v1, s[4:7], s98 offen offset:2048 sc1\n"
"buffer_load_dwordx4 v[128:131], v1, s[4:7], s98 offen offset:3072 sc1\n"
"s_add_u32 s98, s57, 0x400000\n"
"buffer_load_dwordx4 v[68:71], v0, s[4:7], s98 offen sc1\n"
"buffer_load_dwordx4 v[88:91], v0, s[4:7], s98 offen offset:1024 sc1\n"
"buffer_load_dwordx4 v[48:51], v0, s[4:7], s98 offen offset:2048 sc1\n"
"buffer_load_dwordx4 v[92:95], v0, s[4:7], s98 offen offset:3072 sc1\n"
"buffer_load_dwordx4 v[96:99], v1, s[4:7], s98 offen sc1\n"
"buffer_load_dwordx4 v[100:103], v1, s[4:7], s98 offen offset:1024 sc1\n"
"buffer_load_dwordx4 v[104:107], v1, s[4:7], s98 offen offset:2048 sc1\n"
"buffer_load_dwordx4 v[24:27], v1, s[4:7], s98 offen offset:3072 sc1\n"
"s_add_u32 s98, s58, 0x400000\n"
"buffer_load_dwordx4 v[28:31], v0, s[4:7], s98 offen sc1\n"
"buffer_load_dwordx4 v[32:35], v0, s[4:7], s98 offen offset:1024 sc1\n"
"buffer_load_dwordx4 v[84:87], v0, s[4:7], s98 offen offset:2048 sc1\n"
"buffer_load_dwordx4 v[64:67], v0, s[4:7], s98 offen offset:3072 sc1\n"
"buffer_load_dwordx4 v[148:151], v1, s[4:7], s98 offen sc1\n"
"buffer_load_dwordx4 v[52:55], v1, s[4:7], s98 offen offset:1024 sc1\n"
"buffer_load_dwordx4 v[108:111], v1, s[4:7], s98 offen offset:2048 sc1\n"
"buffer_load_dwordx4 v[116:119], v1, s[4:7], s98 offen offset:3072 sc1\n"
"s_waitcnt vmcnt(0)\n"
"v_max_u32_e32 v255, v140, v141\n"
"v_max3_u32 v255, v255, v142, v143\n"
"v_max3_u32 v255, v255, v124, v125\n"
"v_max3_u32 v255, v255, v126, v127\n"
"v_max3_u32 v255, v255, v36, v37\n"
"v_max3_u32 v255, v255, v38, v39\n"
"v_max3_u32 v255, v255, v56, v57\n"
"v_max3_u32 v255, v255, v58, v59\n"
"v_max3_u32 v255, v255, v132, v133\n"
"v_max3_u32 v255, v255, v134, v135\n"
"v_max3_u32 v255, v255, v112, v113\n"
"v_max3_u32 v255, v255, v114, v115\n"
"v_max3_u32 v255, v255, v156, v157\n"
"v_max3_u32 v255, v255, v158, v159\n"
"v_max3_u32 v255, v255, v128, v129\n"
"v_max3_u32 v255, v255, v130, v131\n"
"v_cmp_eq_u32_e32 vcc, -1, v255\n"
"s_cbranch_vccz .Lmk_p76k\n"
"s_sub_u32 s1, s1, 1\n"
"s_cbranch_scc0 .Lmk_p76l\n"
".Lmk_p76k:\n"
"v_mfma_f32_16x16x32_bf16 v[136:139], a[128:131], v[140:143], 0\n"
"v_mfma_f32_16x16x32_bf16 v[144:147], a[132:135], v[140:143], 0\n"
"v_mfma_f32_16x16x32_bf16 v[152:155], a[136:139], v[140:143], 0\n"
"v_mfma_f32_16x16x32_bf16 v[160:163], a[140:143], v[140:143], 0\n"
"v_mfma_f32_16x16x32_bf16 v[136:139], a[144:147], v[124:127], v[136:139]\n"
"v_mfma_f32_16x16x32_bf16 v[144:147], a[148:151], v[124:127], v[144:147]\n"
"v_mfma_f32_16x16x32_bf16 v[152:155], a[152:155], v[124:127], v[152:155]\n"
"v_mfma_f32_16x16x32_bf16 v[160:163], a[156:159], v[124:127], v[160:163]\n"
"v_mfma_f32_16x16x32_bf16 v[136:139], a[160:163], v[36:39], v[136:139]\n"
"v_mfma_f32_16x16x32_bf16 v[144:147], a[164:167], v[36:39], v[144:147]\n"
"v_mfma_f32_16x16x32_bf16 v[152:155], a[168:171], v[36:39], v[152:155]\n"
"v_mfma_f32_16x16x32_bf16 v[160:163], a[172:175], v[36:39], v[160:163]\n"
"v_mfma_f32_16x16x32_bf16 v[136:139], a[176:179], v[56:59], v[136:139]\n"
"v_mfma_f32_16x16x32_bf16 v[144:147], a[180:183], v[56:59], v[144:147]\n"
"v_mfma_f32_16x16x32_bf16 v[152:155], a[184:187], v[56:59], v[152:155]\n"
"v_mfma_f32_16x16x32_bf16 v[160:163], a[188:191], v[56:59], v[160:163]\n"
"v_mfma_f32_16x16x32_bf16 v[136:139], a[192:195], v[132:135], v[136:139]\n"
"v_mfma_f32_16x16x32_bf16 v[144:147], a[196:199], v[132:135], v[144:147]\n"
"v_mfma_f32_16x16x32_bf16 v[152:155], a[200:203], v[132:135], v[152:155]\n"
"v_mfma_f32_16x16x32_bf16 v[160:163], a[204:207], v[132:135], v[160:163]\n"
"v_mfma_f32_16x16x32_bf16 v[136:139], a[208:211], v[112:115], v[136:139]\n"
"v_mfma_f32_16x16x32_bf16 v[144:147], a[212:215], v[112:115], v[144:147]\n"
"v_mfma_f32_16x16x32_bf16 v[152:155], a[216:219], v[112:115], v[152:155]\n"
"v_mfma_f32_16x16x32_bf16 v[160:163], a[220:223], v[112:115], v[160:163]\n"
"v_mfma_f32_16x16x32_bf16 v[136:139], a[224:227], v[156:159], v[136:139]\n"
"v_mfma_f32_16x16x32_bf16 v[144:147], a[228:231], v[156:159], v[144:147]\n"
"v_mfma_f32_16x16x32_bf16 v[152:155], a[232:235], v[156:159], v[152:155]\n"
"v_mfma_f32_16x16x32_bf16 v[160:163], a[236:239], v[156:159], v[160:163]\n"
"v_mfma_f32_16x16x32_bf16 v[136:139], a[240:243], v[128:131], v[136:139]\n"
"v_mfma_f32_16x16x32_bf16 v[144:147], a[244:247], v[128:131], v[144:147]\n"
"v_mfma_f32_16x16x32_bf16 v[152:155], a[248:251], v[128:131], v[152:155]\n"
"v_mfma_f32_16x16x32_bf16 v[160:163], a[252:255], v[128:131], v[160:163]\n"
"s_add_u32 s98, s59, 0x400000\n"
"v_mov_b32_e32 v255, 0x80000000\n"
"buffer_load_dwordx4 v[140:143], v0, s[4:7], s98 offen sc1\n"
"buffer_load_dwordx4 v[124:127], v0, s[4:7], s98 offen offset:1024 sc1\n"
"buffer_store_dword v255, v255, s[4:7], 0 offen\n"
"buffer_load_dwordx4 v[36:39], v0, s[4:7], s98 offen offset:2048 sc1\n"
"buffer_load_dwordx4 v[56:59], v0, s[4:7], s98 offen offset:3072 sc1\n"
"buffer_load_dwordx4 v[132:135], v1, s[4:7], s98 offen sc1\n"
"buffer_store_dword v255, v255, s[4:7], 0 offen\n"
"buffer_load_dwordx4 v[112:115], v1, s[4:7], s98 offen offset:1024 sc1\n"
"buffer_load_dwordx4 v[156:159], v1, s[4:7], s98 offen offset:2048 sc1\n"
"buffer_load_dwordx4 v[128:131], v1, s[4:7], s98 offen offset:3072 sc1\n"
"buffer_store_dword v255, v255, s[4:7], 0 offen\n"
"buffer_store_dword v255, v255, s[4:7], 0 offen\n"
"buffer_store_dword v255, v255, s[4:7], 0 offen\n"
"buffer_store_dword v255, v255, s[4:7], 0 offen\n"
"buffer_store_dword v255, v255, s[4:7], 0 offen\n"
"buffer_store_dword v255, v255, s[4:7], 0 offen\n"
"buffer_store_dword v255, v255, s[4:7], 0 offen\n"
"buffer_store_dword v255, v255, s[4:7], 0 offen\n"
"buffer_store_dword v255, v255, s[4:7], 0 offen\n"
"s_nop 7\n"
"s_branch .Lmk_p76r\n"
".Lmk_p77s:\n"
";\n"
"s_mov_b32 s1, 0x10000\n"
".Lmk_p77l:\n"
"s_add_u32 s98, s57, 0x400000\n"
"buffer_load_dwordx4 v[140:143], v0, s[4:7], s98 offen sc1\n"
"buffer_load_dwordx4 v[124:127], v0, s[4:7], s98 offen offset:1024 sc1\n"
"buffer_load_dwordx4 v[36:39], v0, s[4:7], s98 offen offset:2048 sc1\n"
"buffer_load_dwordx4 v[56:59], v0, s[4:7], s98 offen offset:3072 sc1\n"
"buffer_load_dwordx4 v[132:135], v1, s[4:7], s98 offen sc1\n"
"buffer_load_dwordx4 v[112:115], v1, s[4:7], s98 offen offset:1024 sc1\n"
"buffer_load_dwordx4 v[156:159], v1, s[4:7], s98 offen offset:2048 sc1\n"
"buffer_load_dwordx4 v[128:131], v1, s[4:7], s98 offen offset:3072 sc1\n"
"s_add_u32 s98, s58, 0x400000\n"
"buffer_load_dwordx4 v[28:31], v0, s[4:7], s98 offen sc1\n"
"buffer_load_dwordx4 v[32:35], v0, s[4:7], s98 offen offset:1024 sc1\n"
"buffer_load_dwordx4 v[84:87], v0, s[4:7], s98 offen offset:2048 sc1\n"
"buffer_load_dwordx4 v[64:67], v0, s[4:7], s98 offen offset:3072 sc1\n"
"buffer_load_dwordx4 v[148:151], v1, s[4:7], s98 offen sc1\n"
"buffer_load_dwordx4 v[52:55], v1, s[4:7], s98 offen offset:1024 sc1\n"
"buffer_load_dwordx4 v[108:111], v1, s[4:7], s98 offen offset:2048 sc1\n"
"buffer_load_dwordx4 v[116:119], v1, s[4:7], s98 offen offset:3072 sc1\n"
"s_waitcnt vmcnt(0)\n"
"v_max_u32_e32 v244, v140, v141\n"
"v_max3_u32 v244, v244, v142, v143\n"
"v_max3_u32 v244, v244, v124, v125\n"
"v_max3_u32 v244, v244, v126, v127\n"
"v_max3_u32 v244, v244, v36, v37\n"
"v_max3_u32 v244, v244, v38, v39\n"
"v_max3_u32 v244, v244, v56, v57\n"
"v_max3_u32 v244, v244, v58, v59\n"
"v_max3_u32 v244, v244, v132, v133\n"
"v_max3_u32 v244, v244, v134, v135\n"
"v_max3_u32 v244, v244, v112, v113\n"
"v_max3_u32 v244, v244, v114, v115\n"
"v_max3_u32 v244, v244, v156, v157\n"
"v_max3_u32 v244, v244, v158, v159\n"
"v_max3_u32 v244, v244, v128, v129\n"
"v_max3_u32 v244, v244, v130, v131\n"
"v_cmp_eq_u32_e32 vcc, -1, v244\n"
"s_cbranch_vccz .Lmk_p77k\n"
"s_sub_u32 s1, s1, 1\n"
"s_cbranch_scc0 .Lmk_p77l\n"
".Lmk_p77k:\n"
"v_mfma_f32_16x16x32_bf16 v[60:63], a[128:131], v[140:143], 0\n"
"v_mfma_f32_16x16x32_bf16 v[72:75], a[132:135], v[140:143], 0\n"
"v_mfma_f32_16x16x32_bf16 v[76:79], a[136:139], v[140:143], 0\n"
"v_mfma_f32_16x16x32_bf16 v[120:123], a[140:143], v[140:143], 0\n"
"v_mfma_f32_16x16x32_bf16 v[60:63], a[144:147], v[124:127], v[60:63]\n"
"v_mfma_f32_16x16x32_bf16 v[72:75], a[148:151], v[124:127], v[72:75]\n"
"v_mfma_f32_16x16x32_bf16 v[76:79], a[152:155], v[124:127], v[76:79]\n"
"v_mfma_f32_16x16x32_bf16 v[120:123], a[156:159], v[124:127], v[120:123]\n"
"v_mfma_f32_16x16x32_bf16 v[60:63], a[160:163], v[36:39], v[60:63]\n"
"v_mfma_f32_16x16x32_bf16 v[72:75], a[164:167], v[36:39], v[72:75]\n"
"v_mfma_f32_16x16x32_bf16 v[76:79], a[168:171], v[36:39], v[76:79]\n"
"v_mfma_f32_16x16x32_bf16 v[120:123], a[172:175], v[36:39], v[120:123]\n"
"v_mfma_f32_16x16x32_bf16 v[60:63], a[176:179], v[56:59], v[60:63]\n"
"v_mfma_f32_16x16x32_bf16 v[72:75], a[180:183], v[56:59], v[72:75]\n"
"v_mfma_f32_16x16x32_bf16 v[76:79], a[184:187], v[56:59], v[76:79]\n"
"v_mfma_f32_16x16x32_bf16 v[120:123], a[188:191], v[56:59], v[120:123]\n"
"v_mfma_f32_16x16x32_bf16 v[60:63], a[192:195], v[132:135], v[60:63]\n"
"v_mfma_f32_16x16x32_bf16 v[72:75], a[196:199], v[132:135], v[72:75]\n"
"v_mfma_f32_16x16x32_bf16 v[76:79], a[200:203], v[132:135], v[76:79]\n"
"v_mfma_f32_16x16x32_bf16 v[120:123], a[204:207], v[132:135], v[120:123]\n"
"v_mfma_f32_16x16x32_bf16 v[60:63], a[208:211], v[112:115], v[60:63]\n"
"v_mfma_f32_16x16x32_bf16 v[72:75], a[212:215], v[112:115], v[72:75]\n"
"v_mfma_f32_16x16x32_bf16 v[76:79], a[216:219], v[112:115], v[76:79]\n"
"v_mfma_f32_16x16x32_bf16 v[120:123], a[220:223], v[112:115], v[120:123]\n"
"v_mfma_f32_16x16x32_bf16 v[60:63], a[224:227], v[156:159], v[60:63]\n"
"v_mfma_f32_16x16x32_bf16 v[72:75], a[228:231], v[156:159], v[72:75]\n"
"v_mfma_f32_16x16x32_bf16 v[76:79], a[232:235], v[156:159], v[76:79]\n"
"v_mfma_f32_16x16x32_bf16 v[120:123], a[236:239], v[156:159], v[120:123]\n"
"v_mfma_f32_16x16x32_bf16 v[60:63], a[240:243], v[128:131], v[60:63]\n"
"v_mfma_f32_16x16x32_bf16 v[72:75], a[244:247], v[128:131], v[72:75]\n"
"v_mfma_f32_16x16x32_bf16 v[76:79], a[248:251], v[128:131], v[76:79]\n"
"v_mfma_f32_16x16x32_bf16 v[120:123], a[252:255], v[128:131], v[120:123]\n"
"s_add_u32 s98, s59, 0x400000\n"
"v_mov_b32_e32 v244, 0x80000000\n"
"buffer_load_dwordx4 v[140:143], v0, s[4:7], s98 offen sc1\n"
"buffer_load_dwordx4 v[124:127], v0, s[4:7], s98 offen offset:1024 sc1\n"
"buffer_store_dword v244, v244, s[4:7], 0 offen\n"
"buffer_load_dwordx4 v[36:39], v0, s[4:7], s98 offen offset:2048 sc1\n"
"buffer_load_dwordx4 v[56:59], v0, s[4:7], s98 offen offset:3072 sc1\n"
"buffer_load_dwordx4 v[132:135], v1, s[4:7], s98 offen sc1\n"
"buffer_store_dword v244, v244, s[4:7], 0 offen\n"
"buffer_load_dwordx4 v[112:115], v1, s[4:7], s98 offen offset:1024 sc1\n"
"buffer_load_dwordx4 v[156:159], v1, s[4:7], s98 offen offset:2048 sc1\n"
"buffer_load_dwordx4 v[128:131], v1, s[4:7], s98 offen offset:3072 sc1\n"
"buffer_store_dword v244, v244, s[4:7], 0 offen\n"
"buffer_store_dword v244, v244, s[4:7], 0 offen\n"
"buffer_store_dword v244, v244, s[4:7], 0 offen\n"
"buffer_store_dword v244, v244, s[4:7], 0 offen\n"
"buffer_store_dword v244, v244, s[4:7], 0 offen\n"
"buffer_store_dword v244, v244, s[4:7], 0 offen\n"
"buffer_store_dword v244, v244, s[4:7], 0 offen\n"
"buffer_store_dword v244, v244, s[4:7], 0 offen\n"
"buffer_store_dword v244, v244, s[4:7], 0 offen\n"
"buffer_store_dword v244, v244, s[4:7], 0 offen\n"
"buffer_store_dword v244, v244, s[4:7], 0 offen\n"
"buffer_store_dword v244, v244, s[4:7], 0 offen\n"
"buffer_store_dword v244, v244, s[4:7], 0 offen\n"
"buffer_store_dword v244, v244, s[4:7], 0 offen\n"
"buffer_store_dword v244, v244, s[4:7], 0 offen\n"
"buffer_store_dword v244, v244, s[4:7], 0 offen\n"
"buffer_store_dword v244, v244, s[4:7], 0 offen\n"
"buffer_store_dword v244, v244, s[4:7], 0 offen\n"
"s_nop 7\n"
"s_branch .Lmk_p77r\n"
".Lmk_q79s:\n"
";\n"
"s_mov_b32 s1, 0x10000\n"
".Lmk_q79l:\n"
"s_add_u32 s98, s59, 0x400000\n"
"buffer_load_dwordx4 v[140:143], v0, s[4:7], s98 offen sc1\n"
"buffer_load_dwordx4 v[124:127], v0, s[4:7], s98 offen offset:1024 sc1\n"
"buffer_load_dwordx4 v[36:39], v0, s[4:7], s98 offen offset:2048 sc1\n"
"buffer_load_dwordx4 v[56:59], v0, s[4:7], s98 offen offset:3072 sc1\n"
"buffer_load_dwordx4 v[132:135], v1, s[4:7], s98 offen sc1\n"
"buffer_load_dwordx4 v[112:115], v1, s[4:7], s98 offen offset:1024 sc1\n"
"buffer_load_dwordx4 v[156:159], v1, s[4:7], s98 offen offset:2048 sc1\n"
"buffer_load_dwordx4 v[128:131], v1, s[4:7], s98 offen offset:3072 sc1\n"
"s_add_u32 s98, s52, 0x840000\n"
"buffer_load_dwordx4 v[72:75], v0, s[8:11], s98 offen sc1\n"
"s_waitcnt vmcnt(0)\n"
"v_max_u32_e32 v255, v140, v141\n"
"v_max3_u32 v255, v255, v142, v143\n"
"v_max3_u32 v255, v255, v124, v125\n"
"v_max3_u32 v255, v255, v126, v127\n"
"v_max3_u32 v255, v255, v36, v37\n"
"v_max3_u32 v255, v255, v38, v39\n"
"v_max3_u32 v255, v255, v56, v57\n"
"v_max3_u32 v255, v255, v58, v59\n"
"v_cmp_eq_u32_e32 vcc, -1, v255\n"
"s_cbranch_vccz .Lmk_q79k\n"
"s_sub_u32 s1, s1, 1\n"
"s_cbranch_scc0 .Lmk_q79l\n"
".Lmk_q79k:\n"
"s_branch .Lmk_q79r\n"
".Lmk_p78s:\n"
";\n"
"s_mov_b32 s1, 0x10000\n"
".Lmk_p78l:\n"
"s_add_u32 s98, s58, 0x400000\n"
"buffer_load_dwordx4 v[52:55], v0, s[4:7], s98 offen sc1\n"
"buffer_load_dwordx4 v[76:79], v0, s[4:7], s98 offen offset:1024 sc1\n"
"buffer_load_dwordx4 v[84:87], v0, s[4:7], s98 offen offset:2048 sc1\n"
"buffer_load_dwordx4 v[108:111], v0, s[4:7], s98 offen offset:3072 sc1\n"
"buffer_load_dwordx4 v[116:119], v1, s[4:7], s98 offen sc1\n"
"buffer_load_dwordx4 v[120:123], v1, s[4:7], s98 offen offset:1024 sc1\n"
"buffer_load_dwordx4 v[144:147], v1, s[4:7], s98 offen offset:2048 sc1\n"
"buffer_load_dwordx4 v[148:151], v1, s[4:7], s98 offen offset:3072 sc1\n"
"s_add_u32 s98, s59, 0x400000\n"
"buffer_load_dwordx4 v[140:143], v0, s[4:7], s98 offen sc1\n"
"buffer_load_dwordx4 v[124:127], v0, s[4:7], s98 offen offset:1024 sc1\n"
"buffer_load_dwordx4 v[36:39], v0, s[4:7], s98 offen offset:2048 sc1\n"
"buffer_load_dwordx4 v[56:59], v0, s[4:7], s98 offen offset:3072 sc1\n"
"buffer_load_dwordx4 v[132:135], v1, s[4:7], s98 offen sc1\n"
"buffer_load_dwordx4 v[112:115], v1, s[4:7], s98 offen offset:1024 sc1\n"
"buffer_load_dwordx4 v[156:159], v1, s[4:7], s98 offen offset:2048 sc1\n"
"buffer_load_dwordx4 v[128:131], v1, s[4:7], s98 offen offset:3072 sc1\n"
"s_add_u32 s98, s52, 0x840000\n"
"buffer_load_dwordx4 v[72:75], v0, s[8:11], s98 offen sc1\n"
"s_waitcnt vmcnt(0)\n"
"v_max_u32_e32 v255, v52, v53\n"
"v_max3_u32 v255, v255, v54, v55\n"
"v_max3_u32 v255, v255, v76, v77\n"
"v_max3_u32 v255, v255, v78, v79\n"
"v_max3_u32 v255, v255, v84, v85\n"
"v_max3_u32 v255, v255, v86, v87\n"
"v_max3_u32 v255, v255, v108, v109\n"
"v_max3_u32 v255, v255, v110, v111\n"
"v_max3_u32 v255, v255, v116, v117\n"
"v_max3_u32 v255, v255, v118, v119\n"
"v_max3_u32 v255, v255, v120, v121\n"
"v_max3_u32 v255, v255, v122, v123\n"
"v_max3_u32 v255, v255, v144, v145\n"
"v_max3_u32 v255, v255, v146, v147\n"
"v_max3_u32 v255, v255, v148, v149\n"
"v_max3_u32 v255, v255, v150, v151\n"
"v_cmp_eq_u32_e32 vcc, -1, v255\n"
"s_cbranch_vccz .Lmk_p78k\n"
"s_sub_u32 s1, s1, 1\n"
"s_cbranch_scc0 .Lmk_p78l\n"
".Lmk_p78k:\n"
"v_mfma_f32_16x16x32_bf16 v[24:27], a[128:131], v[52:55], 0\n"
"v_mfma_f32_16x16x32_bf16 v[172:175], a[132:135], v[52:55], 0\n"
"v_mfma_f32_16x16x32_bf16 v[176:179], a[136:139], v[52:55], 0\n"
"v_mfma_f32_16x16x32_bf16 v[184:187], a[140:143], v[52:55], 0\n"
"v_mfma_f32_16x16x32_bf16 v[24:27], a[144:147], v[76:79], v[24:27]\n"
"v_mfma_f32_16x16x32_bf16 v[172:175], a[148:151], v[76:79], v[172:175]\n"
"v_mfma_f32_16x16x32_bf16 v[176:179], a[152:155], v[76:79], v[176:179]\n"
"v_mfma_f32_16x16x32_bf16 v[184:187], a[156:159], v[76:79], v[184:187]\n"
"v_mfma_f32_16x16x32_bf16 v[24:27], a[160:163], v[84:87], v[24:27]\n"
"v_mfma_f32_16x16x32_bf16 v[172:175], a[164:167], v[84:87], v[172:175]\n"
"v_mfma_f32_16x16x32_bf16 v[176:179], a[168:171], v[84:87], v[176:179]\n"
"v_mfma_f32_16x16x32_bf16 v[184:187], a[172:175], v[84:87], v[184:187]\n"
"v_mfma_f32_16x16x32_bf16 v[24:27], a[176:179], v[108:111], v[24:27]\n"
"v_mfma_f32_16x16x32_bf16 v[172:175], a[180:183], v[108:111], v[172:175]\n"
"v_mfma_f32_16x16x32_bf16 v[176:179], a[184:187], v[108:111], v[176:179]\n"
"v_mfma_f32_16x16x32_bf16 v[184:187], a[188:191], v[108:111], v[184:187]\n"
"v_mfma_f32_16x16x32_bf16 v[24:27], a[192:195], v[116:119], v[24:27]\n"
"v_mfma_f32_16x16x32_bf16 v[172:175], a[196:199], v[116:119], v[172:175]\n"
"v_mfma_f32_16x16x32_bf16 v[176:179], a[200:203], v[116:119], v[176:179]\n"
"v_mfma_f32_16x16x32_bf16 v[184:187], a[204:207], v[116:119], v[184:187]\n"
"v_mfma_f32_16x16x32_bf16 v[24:27], a[208:211], v[120:123], v[24:27]\n"
"v_mfma_f32_16x16x32_bf16 v[172:175], a[212:215], v[120:123], v[172:175]\n"
"v_mfma_f32_16x16x32_bf16 v[176:179], a[216:219], v[120:123], v[176:179]\n"
"v_mfma_f32_16x16x32_bf16 v[184:187], a[220:223], v[120:123], v[184:187]\n"
"v_mfma_f32_16x16x32_bf16 v[24:27], a[224:227], v[144:147], v[24:27]\n"
"v_mfma_f32_16x16x32_bf16 v[172:175], a[228:231], v[144:147], v[172:175]\n"
"v_mfma_f32_16x16x32_bf16 v[176:179], a[232:235], v[144:147], v[176:179]\n"
"v_mfma_f32_16x16x32_bf16 v[184:187], a[236:239], v[144:147], v[184:187]\n"
"v_mfma_f32_16x16x32_bf16 v[24:27], a[240:243], v[148:151], v[24:27]\n"
"v_mfma_f32_16x16x32_bf16 v[172:175], a[244:247], v[148:151], v[172:175]\n"
"v_mfma_f32_16x16x32_bf16 v[176:179], a[248:251], v[148:151], v[176:179]\n"
"v_mfma_f32_16x16x32_bf16 v[184:187], a[252:255], v[148:151], v[184:187]\n"
"s_nop 7\n"
"s_branch .Lmk_p78r\n"
".Lmk_q80s:\n"
";\n"
"s_mov_b32 s1, 0x10000\n"
".Lmk_q80l:\n"
"s_add_u32 s98, s59, 0x400000\n"
"buffer_load_dwordx4 v[132:135], v1, s[4:7], s98 offen sc1\n"
"buffer_load_dwordx4 v[112:115], v1, s[4:7], s98 offen offset:1024 sc1\n"
"buffer_load_dwordx4 v[156:159], v1, s[4:7], s98 offen offset:2048 sc1\n"
"buffer_load_dwordx4 v[128:131], v1, s[4:7], s98 offen offset:3072 sc1\n"
"s_add_u32 s98, s52, 0x840000\n"
"buffer_load_dwordx4 v[72:75], v0, s[8:11], s98 offen sc1\n"
"buffer_load_dwordx4 v[52:55], v0, s[8:11], s98 offen offset:1024 sc1\n"
"buffer_load_dwordx4 v[24:27], v0, s[8:11], s98 offen offset:2048 sc1\n"
"s_waitcnt vmcnt(0)\n"
"v_max_u32_e32 v255, v132, v133\n"
"v_max3_u32 v255, v255, v134, v135\n"
"v_max3_u32 v255, v255, v112, v113\n"
"v_max3_u32 v255, v255, v114, v115\n"
"v_max3_u32 v255, v255, v156, v157\n"
"v_max3_u32 v255, v255, v158, v159\n"
"v_max3_u32 v255, v255, v128, v129\n"
"v_max3_u32 v255, v255, v130, v131\n"
"v_cmp_eq_u32_e32 vcc, -1, v255\n"
"s_cbranch_vccz .Lmk_q80k\n"
"s_sub_u32 s1, s1, 1\n"
"s_cbranch_scc0 .Lmk_q80l\n"
".Lmk_q80k:\n"
"s_branch .Lmk_q80r\n"
".Lmk_p84s:\n"
";\n"
"s_mov_b32 s1, 0x10000\n"
".Lmk_p84l:\n"
"s_add_u32 s99, s52, 0x40000\n"
"buffer_load_dwordx4 v[72:75], v0, s[4:7], s99 offen sc1\n"
"buffer_load_dwordx4 v[212:215], v0, s[4:7], s99 offen offset:1024 sc1\n"
"buffer_load_dwordx4 v[84:87], v0, s[4:7], s99 offen offset:2048 sc1\n"
"buffer_load_dwordx4 v[24:27], v0, s[4:7], s99 offen offset:3072 sc1\n"
"buffer_load_dwordx4 v[100:103], v1, s[4:7], s99 offen sc1\n"
"buffer_load_dwordx4 v[36:39], v1, s[4:7], s99 offen offset:1024 sc1\n"
"buffer_load_dwordx4 v[52:55], v1, s[4:7], s99 offen offset:2048 sc1\n"
"buffer_load_dwordx4 v[56:59], v1, s[4:7], s99 offen offset:3072 sc1\n"
"s_add_u32 s99, s53, 0x40000\n"
"buffer_load_dwordx4 v[132:135], v0, s[4:7], s99 offen sc1\n"
"buffer_load_dwordx4 v[148:151], v0, s[4:7], s99 offen offset:1024 sc1\n"
"buffer_load_dwordx4 v[172:175], v0, s[4:7], s99 offen offset:2048 sc1\n"
"buffer_load_dwordx4 v[112:115], v0, s[4:7], s99 offen offset:3072 sc1\n"
"buffer_load_dwordx4 v[124:127], v1, s[4:7], s99 offen sc1\n"
"buffer_load_dwordx4 v[144:147], v1, s[4:7], s99 offen offset:1024 sc1\n"
"buffer_load_dwordx4 v[156:159], v1, s[4:7], s99 offen offset:2048 sc1\n"
"buffer_load_dwordx4 v[176:179], v1, s[4:7], s99 offen offset:3072 sc1\n"
"s_waitcnt vmcnt(0)\n"
"v_max_u32_e32 v247, v72, v73\n"
"v_max3_u32 v247, v247, v74, v75\n"
"v_max3_u32 v247, v247, v212, v213\n"
"v_max3_u32 v247, v247, v214, v215\n"
"v_max3_u32 v247, v247, v84, v85\n"
"v_max3_u32 v247, v247, v86, v87\n"
"v_max3_u32 v247, v247, v24, v25\n"
"v_max3_u32 v247, v247, v26, v27\n"
"v_max3_u32 v247, v247, v100, v101\n"
"v_max3_u32 v247, v247, v102, v103\n"
"v_max3_u32 v247, v247, v36, v37\n"
"v_max3_u32 v247, v247, v38, v39\n"
"v_max3_u32 v247, v247, v52, v53\n"
"v_max3_u32 v247, v247, v54, v55\n"
"v_max3_u32 v247, v247, v56, v57\n"
"v_max3_u32 v247, v247, v58, v59\n"
"v_cmp_eq_u32_e32 vcc, -1, v247\n"
"s_cbranch_vccz .Lmk_p84k\n"
"s_sub_u32 s1, s1, 1\n"
"s_cbranch_scc0 .Lmk_p84l\n"
".Lmk_p84k:\n"
"v_mfma_f32_16x16x32_bf16 v[184:187], a[0:3], v[72:75], 0\n"
"v_mfma_f32_16x16x32_bf16 v[188:191], a[4:7], v[72:75], 0\n"
"v_mfma_f32_16x16x32_bf16 v[192:195], a[8:11], v[72:75], 0\n"
"v_mfma_f32_16x16x32_bf16 v[196:199], a[12:15], v[72:75], 0\n"
"v_mfma_f32_16x16x32_bf16 v[200:203], a[16:19], v[72:75], 0\n"
"v_mfma_f32_16x16x32_bf16 v[208:211], a[20:23], v[72:75], 0\n"
"v_mfma_f32_16x16x32_bf16 v[184:187], a[24:27], v[212:215], v[184:187]\n"
"v_mfma_f32_16x16x32_bf16 v[188:191], a[28:31], v[212:215], v[188:191]\n"
"v_mfma_f32_16x16x32_bf16 v[192:195], a[32:35], v[212:215], v[192:195]\n"
"v_mfma_f32_16x16x32_bf16 v[196:199], a[36:39], v[212:215], v[196:199]\n"
"v_mfma_f32_16x16x32_bf16 v[200:203], a[40:43], v[212:215], v[200:203]\n"
"v_mfma_f32_16x16x32_bf16 v[208:211], a[44:47], v[212:215], v[208:211]\n"
"v_mfma_f32_16x16x32_bf16 v[184:187], a[48:51], v[84:87], v[184:187]\n"
"v_mfma_f32_16x16x32_bf16 v[188:191], a[52:55], v[84:87], v[188:191]\n"
"v_mfma_f32_16x16x32_bf16 v[192:195], a[56:59], v[84:87], v[192:195]\n"
"v_mfma_f32_16x16x32_bf16 v[196:199], a[60:63], v[84:87], v[196:199]\n"
"v_mfma_f32_16x16x32_bf16 v[200:203], a[64:67], v[84:87], v[200:203]\n"
"v_mfma_f32_16x16x32_bf16 v[208:211], a[68:71], v[84:87], v[208:211]\n"
"v_mfma_f32_16x16x32_bf16 v[184:187], a[72:75], v[24:27], v[184:187]\n"
"v_mfma_f32_16x16x32_bf16 v[188:191], a[76:79], v[24:27], v[188:191]\n"
"v_mfma_f32_16x16x32_bf16 v[192:195], a[80:83], v[24:27], v[192:195]\n"
"v_mfma_f32_16x16x32_bf16 v[196:199], a[84:87], v[24:27], v[196:199]\n"
"v_mfma_f32_16x16x32_bf16 v[200:203], a[88:91], v[24:27], v[200:203]\n"
"v_mfma_f32_16x16x32_bf16 v[208:211], a[92:95], v[24:27], v[208:211]\n"
"v_mfma_f32_16x16x32_bf16 v[184:187], a[96:99], v[100:103], v[184:187]\n"
"v_mfma_f32_16x16x32_bf16 v[188:191], a[100:103], v[100:103], v[188:191]\n"
"v_mfma_f32_16x16x32_bf16 v[192:195], a[104:107], v[100:103], v[192:195]\n"
"v_mfma_f32_16x16x32_bf16 v[196:199], a[108:111], v[100:103], v[196:199]\n"
"v_mfma_f32_16x16x32_bf16 v[200:203], a[112:115], v[100:103], v[200:203]\n"
"v_mfma_f32_16x16x32_bf16 v[208:211], a[116:119], v[100:103], v[208:211]\n"
"v_mfma_f32_16x16x32_bf16 v[184:187], a[120:123], v[36:39], v[184:187]\n"
"v_mfma_f32_16x16x32_bf16 v[188:191], a[124:127], v[36:39], v[188:191]\n"
"v_mfma_f32_16x16x32_bf16 v[192:195], v[40:43], v[36:39], v[192:195]\n"
"v_mfma_f32_16x16x32_bf16 v[196:199], v[44:47], v[36:39], v[196:199]\n"
"v_mfma_f32_16x16x32_bf16 v[200:203], v[80:83], v[36:39], v[200:203]\n"
"v_mfma_f32_16x16x32_bf16 v[208:211], v[68:71], v[36:39], v[208:211]\n"
"v_mfma_f32_16x16x32_bf16 v[184:187], v[88:91], v[52:55], v[184:187]\n"
"v_mfma_f32_16x16x32_bf16 v[188:191], v[48:51], v[52:55], v[188:191]\n"
"v_mfma_f32_16x16x32_bf16 v[192:195], v[92:95], v[52:55], v[192:195]\n"
"v_mfma_f32_16x16x32_bf16 v[196:199], v[96:99], v[52:55], v[196:199]\n"
"v_mfma_f32_16x16x32_bf16 v[200:203], v[168:171], v[52:55], v[200:203]\n"
"v_mfma_f32_16x16x32_bf16 v[208:211], v[104:107], v[52:55], v[208:211]\n"
"v_mfma_f32_16x16x32_bf16 v[184:187], v[136:139], v[56:59], v[184:187]\n"
"v_mfma_f32_16x16x32_bf16 v[188:191], v[180:183], v[56:59], v[188:191]\n"
"v_mfma_f32_16x16x32_bf16 v[192:195], v[28:31], v[56:59], v[192:195]\n"
"v_mfma_f32_16x16x32_bf16 v[196:199], v[32:35], v[56:59], v[196:199]\n"
"v_mfma_f32_16x16x32_bf16 v[200:203], v[60:63], v[56:59], v[200:203]\n"
"v_mfma_f32_16x16x32_bf16 v[208:211], v[64:67], v[56:59], v[208:211]\n"
"s_add_u32 s99, s54, 0x40000\n"
"v_mov_b32_e32 v247, 0x80000000\n"
"buffer_load_dwordx4 v[72:75], v0, s[4:7], s99 offen sc1\n"
"buffer_store_dword v247, v247, s[4:7], 0 offen\n"
"buffer_load_dwordx4 v[212:215], v0, s[4:7], s99 offen offset:1024 sc1\n"
"buffer_load_dwordx4 v[84:87], v0, s[4:7], s99 offen offset:2048 sc1\n"
"buffer_store_dword v247, v247, s[4:7], 0 offen\n"
"buffer_load_dwordx4 v[24:27], v0, s[4:7], s99 offen offset:3072 sc1\n"
"buffer_load_dwordx4 v[100:103], v1, s[4:7], s99 offen sc1\n"
"buffer_store_dword v247, v247, s[4:7], 0 offen\n"
"buffer_load_dwordx4 v[36:39], v1, s[4:7], s99 offen offset:1024 sc1\n"
"buffer_store_dword v247, v247, s[4:7], 0 offen\n"
"buffer_load_dwordx4 v[52:55], v1, s[4:7], s99 offen offset:2048 sc1\n"
"buffer_load_dwordx4 v[56:59], v1, s[4:7], s99 offen offset:3072 sc1\n"
"s_nop 7\n"
"s_branch .Lmk_p84r\n"
".Lmk_m89s:\n"
";\n"
"s_mov_b32 s1, 0x10000\n"
".Lmk_m89l:\n"
"s_add_u32 s98, s62, 0x4000\n"
"buffer_load_dwordx2 v[254:255], v10, s[4:7], s98 offen sc1\n"
"buffer_load_dwordx2 v[252:253], v10, s[4:7], s98 offen offset:128 sc1\n"
"buffer_load_dwordx2 v[250:251], v10, s[4:7], s98 offen offset:256 sc1\n"
"buffer_load_dwordx2 v[248:249], v10, s[4:7], s98 offen offset:384 sc1\n"
"s_waitcnt vmcnt(0)\n"
"v_max_u32_e32 v247, v254, v255\n"
"v_max3_u32 v247, v247, v252, v253\n"
"v_max3_u32 v247, v247, v250, v251\n"
"v_max3_u32 v247, v247, v248, v249\n"
"v_cmp_eq_u32_e32 vcc, -1, v247\n"
"s_cbranch_vccz .Lmk_m89k\n"
"s_sub_u32 s1, s1, 1\n"
"s_cbranch_scc0 .Lmk_m89l\n"
".Lmk_m89k:\n"
"v_add_f32_e32 v254, v254, v252\n"
"v_add_f32_e32 v255, v255, v253\n"
"v_add_f32_e32 v250, v250, v248\n"
"v_add_f32_e32 v251, v251, v249\n"
"v_add_f32_e32 v254, v254, v250\n"
"v_add_f32_e32 v255, v255, v251\n"
"s_nop 1\n"
"s_branch .Lmk_m89r\n"
".Lmk_p85s:\n"
";\n"
"s_mov_b32 s1, 0x10000\n"
".Lmk_p85l:\n"
"s_add_u32 s99, s53, 0x40000\n"
"buffer_load_dwordx4 v[132:135], v0, s[4:7], s99 offen sc1\n"
"buffer_load_dwordx4 v[152:155], v0, s[4:7], s99 offen offset:1024 sc1\n"
"buffer_load_dwordx4 v[148:151], v0, s[4:7], s99 offen offset:2048 sc1\n"
"buffer_load_dwordx4 v[160:163], v0, s[4:7], s99 offen offset:3072 sc1\n"
"buffer_load_dwordx4 v[112:115], v1, s[4:7], s99 offen sc1\n"
"buffer_load_dwordx4 v[124:127], v1, s[4:7], s99 offen offset:1024 sc1\n"
"buffer_load_dwordx4 v[196:199], v1, s[4:7], s99 offen offset:2048 sc1\n"
"buffer_load_dwordx4 v[156:159], v1, s[4:7], s99 offen offset:3072 sc1\n"
"s_add_u32 s99, s54, 0x40000\n"
"buffer_load_dwordx4 v[72:75], v0, s[4:7], s99 offen sc1\n"
"buffer_load_dwordx4 v[212:215], v0, s[4:7], s99 offen offset:1024 sc1\n"
"buffer_load_dwordx4 v[84:87], v0, s[4:7], s99 offen offset:2048 sc1\n"
"buffer_load_dwordx4 v[24:27], v0, s[4:7], s99 offen offset:3072 sc1\n"
"buffer_load_dwordx4 v[100:103], v1, s[4:7], s99 offen sc1\n"
"buffer_load_dwordx4 v[36:39], v1, s[4:7], s99 offen offset:1024 sc1\n"
"buffer_load_dwordx4 v[52:55], v1, s[4:7], s99 offen offset:2048 sc1\n"
"buffer_load_dwordx4 v[56:59], v1, s[4:7], s99 offen offset:3072 sc1\n"
"s_waitcnt vmcnt(0)\n"
"v_max_u32_e32 v252, v132, v133\n"
"v_max3_u32 v252, v252, v134, v135\n"
"v_max3_u32 v252, v252, v152, v153\n"
"v_max3_u32 v252, v252, v154, v155\n"
"v_max3_u32 v252, v252, v148, v149\n"
"v_max3_u32 v252, v252, v150, v151\n"
"v_max3_u32 v252, v252, v160, v161\n"
"v_max3_u32 v252, v252, v162, v163\n"
"v_max3_u32 v252, v252, v112, v113\n"
"v_max3_u32 v252, v252, v114, v115\n"
"v_max3_u32 v252, v252, v124, v125\n"
"v_max3_u32 v252, v252, v126, v127\n"
"v_max3_u32 v252, v252, v196, v197\n"
"v_max3_u32 v252, v252, v198, v199\n"
"v_max3_u32 v252, v252, v156, v157\n"
"v_max3_u32 v252, v252, v158, v159\n"
"v_cmp_eq_u32_e32 vcc, -1, v252\n"
"s_cbranch_vccz .Lmk_p85k\n"
"s_sub_u32 s1, s1, 1\n"
"s_cbranch_scc0 .Lmk_p85l\n"
".Lmk_p85k:\n"
"v_mfma_f32_16x16x32_bf16 v[76:79], a[0:3], v[132:135], 0\n"
"v_mfma_f32_16x16x32_bf16 v[108:111], a[4:7], v[132:135], 0\n"
"v_mfma_f32_16x16x32_bf16 v[116:119], a[8:11], v[132:135], 0\n"
"v_mfma_f32_16x16x32_bf16 v[120:123], a[12:15], v[132:135], 0\n"
"v_mfma_f32_16x16x32_bf16 v[128:131], a[16:19], v[132:135], 0\n"
"v_mfma_f32_16x16x32_bf16 v[140:143], a[20:23], v[132:135], 0\n"
"v_mfma_f32_16x16x32_bf16 v[76:79], a[24:27], v[152:155], v[76:79]\n"
"v_mfma_f32_16x16x32_bf16 v[108:111], a[28:31], v[152:155], v[108:111]\n"
"v_mfma_f32_16x16x32_bf16 v[116:119], a[32:35], v[152:155], v[116:119]\n"
"v_mfma_f32_16x16x32_bf16 v[120:123], a[36:39], v[152:155], v[120:123]\n"
"v_mfma_f32_16x16x32_bf16 v[128:131], a[40:43], v[152:155], v[128:131]\n"
"v_mfma_f32_16x16x32_bf16 v[140:143], a[44:47], v[152:155], v[140:143]\n"
"v_mfma_f32_16x16x32_bf16 v[76:79], a[48:51], v[148:151], v[76:79]\n"
"v_mfma_f32_16x16x32_bf16 v[108:111], a[52:55], v[148:151], v[108:111]\n"
"v_mfma_f32_16x16x32_bf16 v[116:119], a[56:59], v[148:151], v[116:119]\n"
"v_mfma_f32_16x16x32_bf16 v[120:123], a[60:63], v[148:151], v[120:123]\n"
"v_mfma_f32_16x16x32_bf16 v[128:131], a[64:67], v[148:151], v[128:131]\n"
"v_mfma_f32_16x16x32_bf16 v[140:143], a[68:71], v[148:151], v[140:143]\n"
"v_mfma_f32_16x16x32_bf16 v[76:79], a[72:75], v[160:163], v[76:79]\n"
"v_mfma_f32_16x16x32_bf16 v[108:111], a[76:79], v[160:163], v[108:111]\n"
"v_mfma_f32_16x16x32_bf16 v[116:119], a[80:83], v[160:163], v[116:119]\n"
"v_mfma_f32_16x16x32_bf16 v[120:123], a[84:87], v[160:163], v[120:123]\n"
"v_mfma_f32_16x16x32_bf16 v[128:131], a[88:91], v[160:163], v[128:131]\n"
"v_mfma_f32_16x16x32_bf16 v[140:143], a[92:95], v[160:163], v[140:143]\n"
"v_mfma_f32_16x16x32_bf16 v[76:79], a[96:99], v[112:115], v[76:79]\n"
"v_mfma_f32_16x16x32_bf16 v[108:111], a[100:103], v[112:115], v[108:111]\n"
"v_mfma_f32_16x16x32_bf16 v[116:119], a[104:107], v[112:115], v[116:119]\n"
"v_mfma_f32_16x16x32_bf16 v[120:123], a[108:111], v[112:115], v[120:123]\n"
"v_mfma_f32_16x16x32_bf16 v[128:131], a[112:115], v[112:115], v[128:131]\n"
"v_mfma_f32_16x16x32_bf16 v[140:143], a[116:119], v[112:115], v[140:143]\n"
"v_mfma_f32_16x16x32_bf16 v[76:79], a[120:123], v[124:127], v[76:79]\n"
"v_mfma_f32_16x16x32_bf16 v[108:111], a[124:127], v[124:127], v[108:111]\n"
"v_mfma_f32_16x16x32_bf16 v[116:119], v[40:43], v[124:127], v[116:119]\n"
"v_mfma_f32_16x16x32_bf16 v[120:123], v[44:47], v[124:127], v[120:123]\n"
"v_mfma_f32_16x16x32_bf16 v[128:131], v[80:83], v[124:127], v[128:131]\n"
"v_mfma_f32_16x16x32_bf16 v[140:143], v[68:71], v[124:127], v[140:143]\n"
"v_mfma_f32_16x16x32_bf16 v[76:79], v[88:91], v[196:199], v[76:79]\n"
"v_mfma_f32_16x16x32_bf16 v[108:111], v[48:51], v[196:199], v[108:111]\n"
"v_mfma_f32_16x16x32_bf16 v[116:119], v[92:95], v[196:199], v[116:119]\n"
"v_mfma_f32_16x16x32_bf16 v[120:123], v[96:99], v[196:199], v[120:123]\n"
"v_mfma_f32_16x16x32_bf16 v[128:131], v[168:171], v[196:199], v[128:131]\n"
"v_mfma_f32_16x16x32_bf16 v[140:143], v[104:107], v[196:199], v[140:143]\n"
"v_mfma_f32_16x16x32_bf16 v[76:79], v[136:139], v[156:159], v[76:79]\n"
"v_mfma_f32_16x16x32_bf16 v[108:111], v[180:183], v[156:159], v[108:111]\n"
"v_mfma_f32_16x16x32_bf16 v[116:119], v[28:31], v[156:159], v[116:119]\n"
"v_mfma_f32_16x16x32_bf16 v[120:123], v[32:35], v[156:159], v[120:123]\n"
"v_mfma_f32_16x16x32_bf16 v[128:131], v[60:63], v[156:159], v[128:131]\n"
"v_mfma_f32_16x16x32_bf16 v[140:143], v[64:67], v[156:159], v[140:143]\n"
"s_add_u32 s99, s55, 0x40000\n"
"v_mov_b32_e32 v252, 0x80000000\n"
"buffer_load_dwordx4 v[132:135], v0, s[4:7], s99 offen sc1\n"
"buffer_load_dwordx4 v[152:155], v0, s[4:7], s99 offen offset:1024 sc1\n"
"buffer_load_dwordx4 v[148:151], v0, s[4:7], s99 offen offset:2048 sc1\n"
"buffer_load_dwordx4 v[160:163], v0, s[4:7], s99 offen offset:3072 sc1\n"
"buffer_load_dwordx4 v[112:115], v1, s[4:7], s99 offen sc1\n"
"buffer_load_dwordx4 v[124:127], v1, s[4:7], s99 offen offset:1024 sc1\n"
"buffer_load_dwordx4 v[196:199], v1, s[4:7], s99 offen offset:2048 sc1\n"
"buffer_load_dwordx4 v[156:159], v1, s[4:7], s99 offen offset:3072 sc1\n"
"s_nop 7\n"
"s_branch .Lmk_p85r\n"
".Lmk_q87s:\n"
";\n"
"s_mov_b32 s1, 0x10000\n"
".Lmk_q87l:\n"
"s_add_u32 s97, s55, 0x40000\n"
"buffer_load_dwordx4 v[132:135], v0, s[4:7], s97 offen sc1\n"
"buffer_load_dwordx4 v[152:155], v0, s[4:7], s97 offen offset:1024 sc1\n"
"buffer_load_dwordx4 v[148:151], v0, s[4:7], s97 offen offset:2048 sc1\n"
"buffer_load_dwordx4 v[160:163], v0, s[4:7], s97 offen offset:3072 sc1\n"
"buffer_load_dwordx4 v[112:115], v1, s[4:7], s97 offen sc1\n"
"buffer_load_dwordx4 v[124:127], v1, s[4:7], s97 offen offset:1024 sc1\n"
"buffer_load_dwordx4 v[196:199], v1, s[4:7], s97 offen offset:2048 sc1\n"
"buffer_load_dwordx4 v[156:159], v1, s[4:7], s97 offen offset:3072 sc1\n"
"s_add_u32 s97, s52, 0x48000\n"
"buffer_load_dwordx4 v[72:75], v0, s[4:7], s97 offen sc1\n"
"buffer_load_dwordx4 v[116:119], v0, s[4:7], s97 offen offset:1024 sc1\n"
"buffer_load_dwordx4 v[128:131], v0, s[4:7], s97 offen offset:2048 sc1\n"
"buffer_load_dwordx4 v[84:87], v0, s[4:7], s97 offen offset:3072 sc1\n"
"buffer_load_dwordx4 v[24:27], v1, s[4:7], s97 offen sc1\n"
"buffer_load_dwordx4 v[100:103], v1, s[4:7], s97 offen offset:1024 sc1\n"
"s_waitcnt vmcnt(0)\n"
"v_max_u32_e32 v254, v132, v133\n"
"v_max3_u32 v254, v254, v134, v135\n"
"v_max3_u32 v254, v254, v152, v153\n"
"v_max3_u32 v254, v254, v154, v155\n"
"v_max3_u32 v254, v254, v148, v149\n"
"v_max3_u32 v254, v254, v150, v151\n"
"v_max3_u32 v254, v254, v160, v161\n"
"v_max3_u32 v254, v254, v162, v163\n"
"v_cmp_eq_u32_e32 vcc, -1, v254\n"
"s_cbranch_vccz .Lmk_q87k\n"
"s_sub_u32 s1, s1, 1\n"
"s_cbranch_scc0 .Lmk_q87l\n"
".Lmk_q87k:\n"
"s_branch .Lmk_q87r\n"
".Lmk_p86s:\n"
";\n"
"s_mov_b32 s1, 0x10000\n"
".Lmk_p86l:\n"
"s_add_u32 s97, s54, 0x40000\n"
"buffer_load_dwordx4 v[36:39], v0, s[4:7], s97 offen sc1\n"
"buffer_load_dwordx4 v[52:55], v0, s[4:7], s97 offen offset:1024 sc1\n"
"buffer_load_dwordx4 v[56:59], v0, s[4:7], s97 offen offset:2048 sc1\n"
"buffer_load_dwordx4 v[140:143], v0, s[4:7], s97 offen offset:3072 sc1\n"
"buffer_load_dwordx4 v[192:195], v1, s[4:7], s97 offen sc1\n"
"buffer_load_dwordx4 v[212:215], v1, s[4:7], s97 offen offset:1024 sc1\n"
"buffer_load_dwordx4 v[216:219], v1, s[4:7], s97 offen offset:2048 sc1\n"
"buffer_load_dwordx4 v[220:223], v1, s[4:7], s97 offen offset:3072 sc1\n"
"s_add_u32 s97, s55, 0x40000\n"
"buffer_load_dwordx4 v[132:135], v0, s[4:7], s97 offen sc1\n"
"buffer_load_dwordx4 v[152:155], v0, s[4:7], s97 offen offset:1024 sc1\n"
"buffer_load_dwordx4 v[148:151], v0, s[4:7], s97 offen offset:2048 sc1\n"
"buffer_load_dwordx4 v[160:163], v0, s[4:7], s97 offen offset:3072 sc1\n"
"buffer_load_dwordx4 v[112:115], v1, s[4:7], s97 offen sc1\n"
"buffer_load_dwordx4 v[124:127], v1, s[4:7], s97 offen offset:1024 sc1\n"
"buffer_load_dwordx4 v[196:199], v1, s[4:7], s97 offen offset:2048 sc1\n"
"buffer_load_dwordx4 v[156:159], v1, s[4:7], s97 offen offset:3072 sc1\n"
"s_add_u32 s97, s52, 0x48000\n"
"buffer_load_dwordx4 v[72:75], v0, s[4:7], s97 offen sc1\n"
"buffer_load_dwordx4 v[116:119], v0, s[4:7], s97 offen offset:1024 sc1\n"
"buffer_load_dwordx4 v[128:131], v0, s[4:7], s97 offen offset:2048 sc1\n"
"buffer_load_dwordx4 v[84:87], v0, s[4:7], s97 offen offset:3072 sc1\n"
"buffer_load_dwordx4 v[24:27], v1, s[4:7], s97 offen sc1\n"
"buffer_load_dwordx4 v[100:103], v1, s[4:7], s97 offen offset:1024 sc1\n"
"s_waitcnt vmcnt(0)\n"
"v_max_u32_e32 v254, v36, v37\n"
"v_max3_u32 v254, v254, v38, v39\n"
"v_max3_u32 v254, v254, v52, v53\n"
"v_max3_u32 v254, v254, v54, v55\n"
"v_max3_u32 v254, v254, v56, v57\n"
"v_max3_u32 v254, v254, v58, v59\n"
"v_max3_u32 v254, v254, v140, v141\n"
"v_max3_u32 v254, v254, v142, v143\n"
"v_max3_u32 v254, v254, v192, v193\n"
"v_max3_u32 v254, v254, v194, v195\n"
"v_max3_u32 v254, v254, v212, v213\n"
"v_max3_u32 v254, v254, v214, v215\n"
"v_max3_u32 v254, v254, v216, v217\n"
"v_max3_u32 v254, v254, v218, v219\n"
"v_max3_u32 v254, v254, v220, v221\n"
"v_max3_u32 v254, v254, v222, v223\n"
"v_cmp_eq_u32_e32 vcc, -1, v254\n"
"s_cbranch_vccz .Lmk_p86k\n"
"s_sub_u32 s1, s1, 1\n"
"s_cbranch_scc0 .Lmk_p86l\n"
".Lmk_p86k:\n"
"v_mfma_f32_16x16x32_bf16 v[164:167], a[0:3], v[36:39], 0\n"
"v_mfma_f32_16x16x32_bf16 v[176:179], a[4:7], v[36:39], 0\n"
"v_mfma_f32_16x16x32_bf16 v[184:187], a[8:11], v[36:39], 0\n"
"v_mfma_f32_16x16x32_bf16 v[200:203], a[12:15], v[36:39], 0\n"
"v_mfma_f32_16x16x32_bf16 v[76:79], a[16:19], v[36:39], 0\n"
"v_mfma_f32_16x16x32_bf16 v[204:207], a[20:23], v[36:39], 0\n"
"v_mfma_f32_16x16x32_bf16 v[164:167], a[24:27], v[52:55], v[164:167]\n"
"v_mfma_f32_16x16x32_bf16 v[176:179], a[28:31], v[52:55], v[176:179]\n"
"v_mfma_f32_16x16x32_bf16 v[184:187], a[32:35], v[52:55], v[184:187]\n"
"v_mfma_f32_16x16x32_bf16 v[200:203], a[36:39], v[52:55], v[200:203]\n"
"v_mfma_f32_16x16x32_bf16 v[76:79], a[40:43], v[52:55], v[76:79]\n"
"v_mfma_f32_16x16x32_bf16 v[204:207], a[44:47], v[52:55], v[204:207]\n"
"v_mfma_f32_16x16x32_bf16 v[164:167], a[48:51], v[56:59], v[164:167]\n"
"v_mfma_f32_16x16x32_bf16 v[176:179], a[52:55], v[56:59], v[176:179]\n"
"v_mfma_f32_16x16x32_bf16 v[184:187], a[56:59], v[56:59], v[184:187]\n"
"v_mfma_f32_16x16x32_bf16 v[200:203], a[60:63], v[56:59], v[200:203]\n"
"v_mfma_f32_16x16x32_bf16 v[76:79], a[64:67], v[56:59], v[76:79]\n"
"v_mfma_f32_16x16x32_bf16 v[204:207], a[68:71], v[56:59], v[204:207]\n"
"v_mfma_f32_16x16x32_bf16 v[164:167], a[72:75], v[140:143], v[164:167]\n"
"v_mfma_f32_16x16x32_bf16 v[176:179], a[76:79], v[140:143], v[176:179]\n"
"v_mfma_f32_16x16x32_bf16 v[184:187], a[80:83], v[140:143], v[184:187]\n"
"v_mfma_f32_16x16x32_bf16 v[200:203], a[84:87], v[140:143], v[200:203]\n"
"v_mfma_f32_16x16x32_bf16 v[76:79], a[88:91], v[140:143], v[76:79]\n"
"v_mfma_f32_16x16x32_bf16 v[204:207], a[92:95], v[140:143], v[204:207]\n"
"v_mfma_f32_16x16x32_bf16 v[164:167], a[96:99], v[192:195], v[164:167]\n"
"v_mfma_f32_16x16x32_bf16 v[176:179], a[100:103], v[192:195], v[176:179]\n"
"v_mfma_f32_16x16x32_bf16 v[184:187], a[104:107], v[192:195], v[184:187]\n"
"v_mfma_f32_16x16x32_bf16 v[200:203], a[108:111], v[192:195], v[200:203]\n"
"v_mfma_f32_16x16x32_bf16 v[76:79], a[112:115], v[192:195], v[76:79]\n"
"v_mfma_f32_16x16x32_bf16 v[204:207], a[116:119], v[192:195], v[204:207]\n"
"v_mfma_f32_16x16x32_bf16 v[164:167], a[120:123], v[212:215], v[164:167]\n"
"v_mfma_f32_16x16x32_bf16 v[176:179], a[124:127], v[212:215], v[176:179]\n"
"v_mfma_f32_16x16x32_bf16 v[184:187], v[40:43], v[212:215], v[184:187]\n"
"v_mfma_f32_16x16x32_bf16 v[200:203], v[44:47], v[212:215], v[200:203]\n"
"v_mfma_f32_16x16x32_bf16 v[76:79], v[80:83], v[212:215], v[76:79]\n"
"v_mfma_f32_16x16x32_bf16 v[204:207], v[68:71], v[212:215], v[204:207]\n"
"v_mfma_f32_16x16x32_bf16 v[164:167], v[88:91], v[216:219], v[164:167]\n"
"v_mfma_f32_16x16x32_bf16 v[176:179], v[48:51], v[216:219], v[176:179]\n"
"v_mfma_f32_16x16x32_bf16 v[184:187], v[92:95], v[216:219], v[184:187]\n"
"v_mfma_f32_16x16x32_bf16 v[200:203], v[96:99], v[216:219], v[200:203]\n"
"v_mfma_f32_16x16x32_bf16 v[76:79], v[168:171], v[216:219], v[76:79]\n"
"v_mfma_f32_16x16x32_bf16 v[204:207], v[104:107], v[216:219], v[204:207]\n"
"v_mfma_f32_16x16x32_bf16 v[164:167], v[136:139], v[220:223], v[164:167]\n"
"v_mfma_f32_16x16x32_bf16 v[176:179], v[180:183], v[220:223], v[176:179]\n"
"v_mfma_f32_16x16x32_bf16 v[184:187], v[28:31], v[220:223], v[184:187]\n"
"v_mfma_f32_16x16x32_bf16 v[200:203], v[32:35], v[220:223], v[200:203]\n"
"v_mfma_f32_16x16x32_bf16 v[76:79], v[60:63], v[220:223], v[76:79]\n"
"v_mfma_f32_16x16x32_bf16 v[204:207], v[64:67], v[220:223], v[204:207]\n"
"s_nop 7\n"
"s_branch .Lmk_p86r\n"
".Lmk_q88s:\n"
";\n"
"s_mov_b32 s1, 0x10000\n"
".Lmk_q88l:\n"
"s_add_u32 s98, s55, 0x40000\n"
"buffer_load_dwordx4 v[112:115], v1, s[4:7], s98 offen sc1\n"
"buffer_load_dwordx4 v[124:127], v1, s[4:7], s98 offen offset:1024 sc1\n"
"buffer_load_dwordx4 v[196:199], v1, s[4:7], s98 offen offset:2048 sc1\n"
"buffer_load_dwordx4 v[156:159], v1, s[4:7], s98 offen offset:3072 sc1\n"
"s_add_u32 s98, s52, 0x48000\n"
"buffer_load_dwordx4 v[72:75], v0, s[4:7], s98 offen sc1\n"
"buffer_load_dwordx4 v[116:119], v0, s[4:7], s98 offen offset:1024 sc1\n"
"buffer_load_dwordx4 v[128:131], v0, s[4:7], s98 offen offset:2048 sc1\n"
"buffer_load_dwordx4 v[84:87], v0, s[4:7], s98 offen offset:3072 sc1\n"
"buffer_load_dwordx4 v[24:27], v1, s[4:7], s98 offen sc1\n"
"buffer_load_dwordx4 v[100:103], v1, s[4:7], s98 offen offset:1024 sc1\n"
"buffer_load_dwordx4 v[36:39], v1, s[4:7], s98 offen offset:2048 sc1\n"
"buffer_load_dwordx4 v[52:55], v1, s[4:7], s98 offen offset:3072 sc1\n"
"s_add_u32 s98, s53, 0x48000\n"
"buffer_load_dwordx4 v[56:59], v0, s[4:7], s98 offen sc1\n"
"buffer_load_dwordx4 v[132:135], v0, s[4:7], s98 offen offset:1024 sc1\n"
"s_waitcnt vmcnt(0)\n"
"v_max_u32_e32 v254, v112, v113\n"
"v_max3_u32 v254, v254, v114, v115\n"
"v_max3_u32 v254, v254, v124, v125\n"
"v_max3_u32 v254, v254, v126, v127\n"
"v_max3_u32 v254, v254, v196, v197\n"
"v_max3_u32 v254, v254, v198, v199\n"
"v_max3_u32 v254, v254, v156, v157\n"
"v_max3_u32 v254, v254, v158, v159\n"
"v_cmp_eq_u32_e32 vcc, -1, v254\n"
"s_cbranch_vccz .Lmk_q88k\n"
"s_sub_u32 s1, s1, 1\n"
"s_cbranch_scc0 .Lmk_q88l\n"
".Lmk_q88k:\n"
"s_branch .Lmk_q88r\n"
".Lmk_p90s:\n"
";\n"
"s_mov_b32 s1, 0x10000\n"
".Lmk_p90l:\n"
"s_add_u32 s98, s52, 0x48000\n"
"buffer_load_dwordx4 v[72:75], v0, s[4:7], s98 offen sc1\n"
"buffer_load_dwordx4 v[112:115], v0, s[4:7], s98 offen offset:1024 sc1\n"
"buffer_load_dwordx4 v[116:119], v0, s[4:7], s98 offen offset:2048 sc1\n"
"buffer_load_dwordx4 v[108:111], v0, s[4:7], s98 offen offset:3072 sc1\n"
"buffer_load_dwordx4 v[84:87], v1, s[4:7], s98 offen sc1\n"
"buffer_load_dwordx4 v[124:127], v1, s[4:7], s98 offen offset:1024 sc1\n"
"buffer_load_dwordx4 v[100:103], v1, s[4:7], s98 offen offset:2048 sc1\n"
"buffer_load_dwordx4 v[36:39], v1, s[4:7], s98 offen offset:3072 sc1\n"
"s_add_u32 s98, s53, 0x48000\n"
"buffer_load_dwordx4 v[56:59], v0, s[4:7], s98 offen sc1\n"
"buffer_load_dwordx4 v[132:135], v0, s[4:7], s98 offen offset:1024 sc1\n"
"buffer_load_dwordx4 v[76:79], v0, s[4:7], s98 offen offset:2048 sc1\n"
"buffer_load_dwordx4 v[140:143], v0, s[4:7], s98 offen offset:3072 sc1\n"
"buffer_load_dwordx4 v[148:151], v1, s[4:7], s98 offen sc1\n"
"buffer_load_dwordx4 v[176:179], v1, s[4:7], s98 offen offset:1024 sc1\n"
"buffer_load_dwordx4 v[152:155], v1, s[4:7], s98 offen offset:2048 sc1\n"
"buffer_load_dwordx4 v[164:167], v1, s[4:7], s98 offen offset:3072 sc1\n"
"s_waitcnt vmcnt(0)\n"
"v_max_u32_e32 v254, v72, v73\n"
"v_max3_u32 v254, v254, v74, v75\n"
"v_max3_u32 v254, v254, v112, v113\n"
"v_max3_u32 v254, v254, v114, v115\n"
"v_max3_u32 v254, v254, v116, v117\n"
"v_max3_u32 v254, v254, v118, v119\n"
"v_max3_u32 v254, v254, v108, v109\n"
"v_max3_u32 v254, v254, v110, v111\n"
"v_max3_u32 v254, v254, v84, v85\n"
"v_max3_u32 v254, v254, v86, v87\n"
"v_max3_u32 v254, v254, v124, v125\n"
"v_max3_u32 v254, v254, v126, v127\n"
"v_max3_u32 v254, v254, v100, v101\n"
"v_max3_u32 v254, v254, v102, v103\n"
"v_max3_u32 v254, v254, v36, v37\n"
"v_max3_u32 v254, v254, v38, v39\n"
"v_cmp_eq_u32_e32 vcc, -1, v254\n"
"s_cbranch_vccz .Lmk_p90k\n"
"s_sub_u32 s1, s1, 1\n"
"s_cbranch_scc0 .Lmk_p90l\n"
".Lmk_p90k:\n"
"v_mfma_f32_16x16x32_bf16 v[156:159], a[0:3], v[72:75], 0\n"
"v_mfma_f32_16x16x32_bf16 v[184:187], a[4:7], v[72:75], 0\n"
"v_mfma_f32_16x16x32_bf16 v[200:203], a[8:11], v[72:75], 0\n"
"v_mfma_f32_16x16x32_bf16 v[204:207], a[12:15], v[72:75], 0\n"
"v_mfma_f32_16x16x32_bf16 v[216:219], a[16:19], v[72:75], 0\n"
"v_mfma_f32_16x16x32_bf16 v[220:223], a[20:23], v[72:75], 0\n"
"v_mfma_f32_16x16x32_bf16 v[156:159], a[24:27], v[112:115], v[156:159]\n"
"v_mfma_f32_16x16x32_bf16 v[184:187], a[28:31], v[112:115], v[184:187]\n"
"v_mfma_f32_16x16x32_bf16 v[200:203], a[32:35], v[112:115], v[200:203]\n"
"v_mfma_f32_16x16x32_bf16 v[204:207], a[36:39], v[112:115], v[204:207]\n"
"v_mfma_f32_16x16x32_bf16 v[216:219], a[40:43], v[112:115], v[216:219]\n"
"v_mfma_f32_16x16x32_bf16 v[220:223], a[44:47], v[112:115], v[220:223]\n"
"v_mfma_f32_16x16x32_bf16 v[156:159], a[48:51], v[116:119], v[156:159]\n"
"v_mfma_f32_16x16x32_bf16 v[184:187], a[52:55], v[116:119], v[184:187]\n"
"v_mfma_f32_16x16x32_bf16 v[200:203], a[56:59], v[116:119], v[200:203]\n"
"v_mfma_f32_16x16x32_bf16 v[204:207], a[60:63], v[116:119], v[204:207]\n"
"v_mfma_f32_16x16x32_bf16 v[216:219], a[64:67], v[116:119], v[216:219]\n"
"v_mfma_f32_16x16x32_bf16 v[220:223], a[68:71], v[116:119], v[220:223]\n"
"v_mfma_f32_16x16x32_bf16 v[156:159], a[72:75], v[108:111], v[156:159]\n"
"v_mfma_f32_16x16x32_bf16 v[184:187], a[76:79], v[108:111], v[184:187]\n"
"v_mfma_f32_16x16x32_bf16 v[200:203], a[80:83], v[108:111], v[200:203]\n"
"v_mfma_f32_16x16x32_bf16 v[204:207], a[84:87], v[108:111], v[204:207]\n"
"v_mfma_f32_16x16x32_bf16 v[216:219], a[88:91], v[108:111], v[216:219]\n"
"v_mfma_f32_16x16x32_bf16 v[220:223], a[92:95], v[108:111], v[220:223]\n"
"v_mfma_f32_16x16x32_bf16 v[156:159], a[96:99], v[84:87], v[156:159]\n"
"v_mfma_f32_16x16x32_bf16 v[184:187], a[100:103], v[84:87], v[184:187]\n"
"v_mfma_f32_16x16x32_bf16 v[200:203], a[104:107], v[84:87], v[200:203]\n"
"v_mfma_f32_16x16x32_bf16 v[204:207], a[108:111], v[84:87], v[204:207]\n"
"v_mfma_f32_16x16x32_bf16 v[216:219], a[112:115], v[84:87], v[216:219]\n"
"v_mfma_f32_16x16x32_bf16 v[220:223], a[116:119], v[84:87], v[220:223]\n"
"v_mfma_f32_16x16x32_bf16 v[156:159], a[120:123], v[124:127], v[156:159]\n"
"v_mfma_f32_16x16x32_bf16 v[184:187], a[124:127], v[124:127], v[184:187]\n"
"v_mfma_f32_16x16x32_bf16 v[200:203], v[40:43], v[124:127], v[200:203]\n"
"v_mfma_f32_16x16x32_bf16 v[204:207], v[44:47], v[124:127], v[204:207]\n"
"v_mfma_f32_16x16x32_bf16 v[216:219], v[80:83], v[124:127], v[216:219]\n"
"v_mfma_f32_16x16x32_bf16 v[220:223], v[68:71], v[124:127], v[220:223]\n"
"v_mfma_f32_16x16x32_bf16 v[156:159], v[88:91], v[100:103], v[156:159]\n"
"v_mfma_f32_16x16x32_bf16 v[184:187], v[48:51], v[100:103], v[184:187]\n"
"v_mfma_f32_16x16x32_bf16 v[200:203], v[92:95], v[100:103], v[200:203]\n"
"v_mfma_f32_16x16x32_bf16 v[204:207], v[96:99], v[100:103], v[204:207]\n"
"v_mfma_f32_16x16x32_bf16 v[216:219], v[168:171], v[100:103], v[216:219]\n"
"v_mfma_f32_16x16x32_bf16 v[220:223], v[104:107], v[100:103], v[220:223]\n"
"v_mfma_f32_16x16x32_bf16 v[156:159], v[136:139], v[36:39], v[156:159]\n"
"v_mfma_f32_16x16x32_bf16 v[184:187], v[180:183], v[36:39], v[184:187]\n"
"v_mfma_f32_16x16x32_bf16 v[200:203], v[28:31], v[36:39], v[200:203]\n"
"v_mfma_f32_16x16x32_bf16 v[204:207], v[32:35], v[36:39], v[204:207]\n"
"v_mfma_f32_16x16x32_bf16 v[216:219], v[60:63], v[36:39], v[216:219]\n"
"v_mfma_f32_16x16x32_bf16 v[220:223], v[64:67], v[36:39], v[220:223]\n"
"s_add_u32 s98, s54, 0x48000\n"
"v_mov_b32_e32 v254, 0x80000000\n"
"buffer_load_dwordx4 v[72:75], v0, s[4:7], s98 offen sc1\n"
"buffer_store_dword v254, v254, s[4:7], 0 offen\n"
"buffer_store_dword v254, v254, s[4:7], 0 offen\n"
"buffer_load_dwordx4 v[112:115], v0, s[4:7], s98 offen offset:1024 sc1\n"
"buffer_load_dwordx4 v[116:119], v0, s[4:7], s98 offen offset:2048 sc1\n"
"buffer_load_dwordx4 v[108:111], v0, s[4:7], s98 offen offset:3072 sc1\n"
"buffer_load_dwordx4 v[84:87], v1, s[4:7], s98 offen sc1\n"
"buffer_store_dword v254, v254, s[4:7], 0 offen\n"
"buffer_load_dwordx4 v[124:127], v1, s[4:7], s98 offen offset:1024 sc1\n"
"buffer_load_dwordx4 v[100:103], v1, s[4:7], s98 offen offset:2048 sc1\n"
"buffer_load_dwordx4 v[36:39], v1, s[4:7], s98 offen offset:3072 sc1\n"
"s_nop 7\n"
"s_branch .Lmk_p90r\n"
".Lmk_m95s:\n"
";\n"
"s_mov_b32 s1, 0x10000\n"
".Lmk_m95l:\n"
"s_add_u32 s96, s62, 0x4800\n"
"buffer_load_dwordx2 v[250:251], v10, s[4:7], s96 offen sc1\n"
"buffer_load_dwordx2 v[248:249], v10, s[4:7], s96 offen offset:128 sc1\n"
"buffer_load_dwordx2 v[246:247], v10, s[4:7], s96 offen offset:256 sc1\n"
"buffer_load_dwordx2 v[244:245], v10, s[4:7], s96 offen offset:384 sc1\n"
"s_waitcnt vmcnt(0)\n"
"v_max_u32_e32 v253, v250, v251\n"
"v_max3_u32 v253, v253, v248, v249\n"
"v_max3_u32 v253, v253, v246, v247\n"
"v_max3_u32 v253, v253, v244, v245\n"
"v_cmp_eq_u32_e32 vcc, -1, v253\n"
"s_cbranch_vccz .Lmk_m95k\n"
"s_sub_u32 s1, s1, 1\n"
"s_cbranch_scc0 .Lmk_m95l\n"
".Lmk_m95k:\n"
"v_add_f32_e32 v250, v250, v248\n"
"v_add_f32_e32 v251, v251, v249\n"
"v_add_f32_e32 v246, v246, v244\n"
"v_add_f32_e32 v247, v247, v245\n"
"v_add_f32_e32 v250, v250, v246\n"
"v_add_f32_e32 v251, v251, v247\n"
"s_nop 1\n"
"s_branch .Lmk_m95r\n"
".Lmk_p91s:\n"
";\n"
"s_mov_b32 s1, 0x10000\n"
".Lmk_p91l:\n"
"s_add_u32 s98, s53, 0x48000\n"
"buffer_load_dwordx4 v[56:59], v0, s[4:7], s98 offen sc1\n"
"buffer_load_dwordx4 v[172:175], v0, s[4:7], s98 offen offset:1024 sc1\n"
"buffer_load_dwordx4 v[132:135], v0, s[4:7], s98 offen offset:2048 sc1\n"
"buffer_load_dwordx4 v[76:79], v0, s[4:7], s98 offen offset:3072 sc1\n"
"buffer_load_dwordx4 v[24:27], v1, s[4:7], s98 offen sc1\n"
"buffer_load_dwordx4 v[128:131], v1, s[4:7], s98 offen offset:1024 sc1\n"
"buffer_load_dwordx4 v[140:143], v1, s[4:7], s98 offen offset:2048 sc1\n"
"buffer_load_dwordx4 v[188:191], v1, s[4:7], s98 offen offset:3072 sc1\n"
"s_add_u32 s98, s54, 0x48000\n"
"buffer_load_dwordx4 v[72:75], v0, s[4:7], s98 offen sc1\n"
"buffer_load_dwordx4 v[112:115], v0, s[4:7], s98 offen offset:1024 sc1\n"
"buffer_load_dwordx4 v[116:119], v0, s[4:7], s98 offen offset:2048 sc1\n"
"buffer_load_dwordx4 v[108:111], v0, s[4:7], s98 offen offset:3072 sc1\n"
"buffer_load_dwordx4 v[84:87], v1, s[4:7], s98 offen sc1\n"
"buffer_load_dwordx4 v[124:127], v1, s[4:7], s98 offen offset:1024 sc1\n"
"buffer_load_dwordx4 v[100:103], v1, s[4:7], s98 offen offset:2048 sc1\n"
"buffer_load_dwordx4 v[36:39], v1, s[4:7], s98 offen offset:3072 sc1\n"
"s_waitcnt vmcnt(0)\n"
"v_max_u32_e32 v252, v56, v57\n"
"v_max3_u32 v252, v252, v58, v59\n"
"v_max3_u32 v252, v252, v172, v173\n"
"v_max3_u32 v252, v252, v174, v175\n"
"v_max3_u32 v252, v252, v132, v133\n"
"v_max3_u32 v252, v252, v134, v135\n"
"v_max3_u32 v252, v252, v76, v77\n"
"v_max3_u32 v252, v252, v78, v79\n"
"v_max3_u32 v252, v252, v24, v25\n"
"v_max3_u32 v252, v252, v26, v27\n"
"v_max3_u32 v252, v252, v128, v129\n"
"v_max3_u32 v252, v252, v130, v131\n"
"v_max3_u32 v252, v252, v140, v141\n"
"v_max3_u32 v252, v252, v142, v143\n"
"v_max3_u32 v252, v252, v188, v189\n"
"v_max3_u32 v252, v252, v190, v191\n"
"v_cmp_eq_u32_e32 vcc, -1, v252\n"
"s_cbranch_vccz .Lmk_p91k\n"
"s_sub_u32 s1, s1, 1\n"
"s_cbranch_scc0 .Lmk_p91l\n"
".Lmk_p91k:\n"
"v_mfma_f32_16x16x32_bf16 v[52:55], a[0:3], v[56:59], 0\n"
"v_mfma_f32_16x16x32_bf16 v[120:123], a[4:7], v[56:59], 0\n"
"v_mfma_f32_16x16x32_bf16 v[160:163], a[8:11], v[56:59], 0\n"
"v_mfma_f32_16x16x32_bf16 v[192:195], a[12:15], v[56:59], 0\n"
"v_mfma_f32_16x16x32_bf16 v[156:159], a[16:19], v[56:59], 0\n"
"v_mfma_f32_16x16x32_bf16 v[144:147], a[20:23], v[56:59], 0\n"
"v_mfma_f32_16x16x32_bf16 v[52:55], a[24:27], v[172:175], v[52:55]\n"
"v_mfma_f32_16x16x32_bf16 v[120:123], a[28:31], v[172:175], v[120:123]\n"
"v_mfma_f32_16x16x32_bf16 v[160:163], a[32:35], v[172:175], v[160:163]\n"
"v_mfma_f32_16x16x32_bf16 v[192:195], a[36:39], v[172:175], v[192:195]\n"
"v_mfma_f32_16x16x32_bf16 v[156:159], a[40:43], v[172:175], v[156:159]\n"
"v_mfma_f32_16x16x32_bf16 v[144:147], a[44:47], v[172:175], v[144:147]\n"
"v_mfma_f32_16x16x32_bf16 v[52:55], a[48:51], v[132:135], v[52:55]\n"
"v_mfma_f32_16x16x32_bf16 v[120:123], a[52:55], v[132:135], v[120:123]\n"
"v_mfma_f32_16x16x32_bf16 v[160:163], a[56:59], v[132:135], v[160:163]\n"
"v_mfma_f32_16x16x32_bf16 v[192:195], a[60:63], v[132:135], v[192:195]\n"
"v_mfma_f32_16x16x32_bf16 v[156:159], a[64:67], v[132:135], v[156:159]\n"
"v_mfma_f32_16x16x32_bf16 v[144:147], a[68:71], v[132:135], v[144:147]\n"
"v_mfma_f32_16x16x32_bf16 v[52:55], a[72:75], v[76:79], v[52:55]\n"
"v_mfma_f32_16x16x32_bf16 v[120:123], a[76:79], v[76:79], v[120:123]\n"
"v_mfma_f32_16x16x32_bf16 v[160:163], a[80:83], v[76:79], v[160:163]\n"
"v_mfma_f32_16x16x32_bf16 v[192:195], a[84:87], v[76:79], v[192:195]\n"
"v_mfma_f32_16x16x32_bf16 v[156:159], a[88:91], v[76:79], v[156:159]\n"
"v_mfma_f32_16x16x32_bf16 v[144:147], a[92:95], v[76:79], v[144:147]\n"
"v_mfma_f32_16x16x32_bf16 v[52:55], a[96:99], v[24:27], v[52:55]\n"
"v_mfma_f32_16x16x32_bf16 v[120:123], a[100:103], v[24:27], v[120:123]\n"
"v_mfma_f32_16x16x32_bf16 v[160:163], a[104:107], v[24:27], v[160:163]\n"
"v_mfma_f32_16x16x32_bf16 v[192:195], a[108:111], v[24:27], v[192:195]\n"
"v_mfma_f32_16x16x32_bf16 v[156:159], a[112:115], v[24:27], v[156:159]\n"
"v_mfma_f32_16x16x32_bf16 v[144:147], a[116:119], v[24:27], v[144:147]\n"
"v_mfma_f32_16x16x32_bf16 v[52:55], a[120:123], v[128:131], v[52:55]\n"
"v_mfma_f32_16x16x32_bf16 v[120:123], a[124:127], v[128:131], v[120:123]\n"
"v_mfma_f32_16x16x32_bf16 v[160:163], v[40:43], v[128:131], v[160:163]\n"
"v_mfma_f32_16x16x32_bf16 v[192:195], v[44:47], v[128:131], v[192:195]\n"
"v_mfma_f32_16x16x32_bf16 v[156:159], v[80:83], v[128:131], v[156:159]\n"
"v_mfma_f32_16x16x32_bf16 v[144:147], v[68:71], v[128:131], v[144:147]\n"
"v_mfma_f32_16x16x32_bf16 v[52:55], v[88:91], v[140:143], v[52:55]\n"
"v_mfma_f32_16x16x32_bf16 v[120:123], v[48:51], v[140:143], v[120:123]\n"
"v_mfma_f32_16x16x32_bf16 v[160:163], v[92:95], v[140:143], v[160:163]\n"
"v_mfma_f32_16x16x32_bf16 v[192:195], v[96:99], v[140:143], v[192:195]\n"
"v_mfma_f32_16x16x32_bf16 v[156:159], v[168:171], v[140:143], v[156:159]\n"
"v_mfma_f32_16x16x32_bf16 v[144:147], v[104:107], v[140:143], v[144:147]\n"
"v_mfma_f32_16x16x32_bf16 v[52:55], v[136:139], v[188:191], v[52:55]\n"
"v_mfma_f32_16x16x32_bf16 v[120:123], v[180:183], v[188:191], v[120:123]\n"
"v_mfma_f32_16x16x32_bf16 v[160:163], v[28:31], v[188:191], v[160:163]\n"
"v_mfma_f32_16x16x32_bf16 v[192:195], v[32:35], v[188:191], v[192:195]\n"
"v_mfma_f32_16x16x32_bf16 v[156:159], v[60:63], v[188:191], v[156:159]\n"
"v_mfma_f32_16x16x32_bf16 v[144:147], v[64:67], v[188:191], v[144:147]\n"
"s_add_u32 s98, s55, 0x48000\n"
"v_mov_b32_e32 v252, 0x80000000\n"
"buffer_load_dwordx4 v[56:59], v0, s[4:7], s98 offen sc1\n"
"buffer_load_dwordx4 v[172:175], v0, s[4:7], s98 offen offset:1024 sc1\n"
"buffer_store_dword v252, v252, s[4:7], 0 offen\n"
"buffer_load_dwordx4 v[132:135], v0, s[4:7], s98 offen offset:2048 sc1\n"
"buffer_store_dword v252, v252, s[4:7], 0 offen\n"
"buffer_store_dword v252, v252, s[4:7], 0 offen\n"
"buffer_load_dwordx4 v[76:79], v0, s[4:7], s98 offen offset:3072 sc1\n"
"buffer_store_dword v252, v252, s[4:7], 0 offen\n"
"buffer_load_dwordx4 v[24:27], v1, s[4:7], s98 offen sc1\n"
"buffer_load_dwordx4 v[128:131], v1, s[4:7], s98 offen offset:1024 sc1\n"
"buffer_load_dwordx4 v[140:143], v1, s[4:7], s98 offen offset:2048 sc1\n"
"buffer_store_dword v252, v252, s[4:7], 0 offen\n"
"buffer_load_dwordx4 v[188:191], v1, s[4:7], s98 offen offset:3072 sc1\n"
"s_nop 7\n"
"s_branch .Lmk_p91r\n"
".Lmk_f81s:\n"
";\n"
"s_mov_b32 s1, 0x10000\n"
".Lmk_f81l:\n"
"buffer_load_dwordx2 v[254:255], v12, s[4:7], s97 offen sc1\n"
"s_waitcnt vmcnt(0)\n"
"v_cmp_ne_u64_e32 vcc, v[254:255], v[18:19]\n"
"s_cbranch_vccz .Lmk_f81r\n"
"s_sub_u32 s1, s1, 1\n"
"s_cbranch_scc0 .Lmk_f81l\n"
"s_branch .Lmk_f81r\n"
".Lmk_q93s:\n"
";\n"
"s_mov_b32 s1, 0x10000\n"
".Lmk_q93l:\n"
"s_add_u32 s97, s55, 0x48000\n"
"buffer_load_dwordx4 v[56:59], v0, s[4:7], s97 offen sc1\n"
"buffer_load_dwordx4 v[172:175], v0, s[4:7], s97 offen offset:1024 sc1\n"
"buffer_load_dwordx4 v[132:135], v0, s[4:7], s97 offen offset:2048 sc1\n"
"buffer_load_dwordx4 v[76:79], v0, s[4:7], s97 offen offset:3072 sc1\n"
"buffer_load_dwordx4 v[24:27], v1, s[4:7], s97 offen sc1\n"
"buffer_load_dwordx4 v[128:131], v1, s[4:7], s97 offen offset:1024 sc1\n"
"buffer_load_dwordx4 v[140:143], v1, s[4:7], s97 offen offset:2048 sc1\n"
"buffer_load_dwordx4 v[188:191], v1, s[4:7], s97 offen offset:3072 sc1\n"
"s_waitcnt vmcnt(0)\n"
"v_max_u32_e32 v255, v56, v57\n"
"v_max3_u32 v255, v255, v58, v59\n"
"v_max3_u32 v255, v255, v172, v173\n"
"v_max3_u32 v255, v255, v174, v175\n"
"v_max3_u32 v255, v255, v132, v133\n"
"v_max3_u32 v255, v255, v134, v135\n"
"v_max3_u32 v255, v255, v76, v77\n"
"v_max3_u32 v255, v255, v78, v79\n"
"v_cmp_eq_u32_e32 vcc, -1, v255\n"
"s_cbranch_vccz .Lmk_q93k\n"
"s_sub_u32 s1, s1, 1\n"
"s_cbranch_scc0 .Lmk_q93l\n"
".Lmk_q93k:\n"
"s_branch .Lmk_q93r\n"
".Lmk_p92s:\n"
";\n"
"s_mov_b32 s1, 0x10000\n"
".Lmk_p92l:\n"
"s_add_u32 s97, s54, 0x48000\n"
"buffer_load_dwordx4 v[36:39], v0, s[4:7], s97 offen sc1\n"
"buffer_load_dwordx4 v[100:103], v0, s[4:7], s97 offen offset:1024 sc1\n"
"buffer_load_dwordx4 v[108:111], v0, s[4:7], s97 offen offset:2048 sc1\n"
"buffer_load_dwordx4 v[116:119], v0, s[4:7], s97 offen offset:3072 sc1\n"
"buffer_load_dwordx4 v[124:127], v1, s[4:7], s97 offen sc1\n"
"buffer_load_dwordx4 v[160:163], v1, s[4:7], s97 offen offset:1024 sc1\n"
"buffer_load_dwordx4 v[192:195], v1, s[4:7], s97 offen offset:2048 sc1\n"
"buffer_load_dwordx4 v[208:211], v1, s[4:7], s97 offen offset:3072 sc1\n"
"s_add_u32 s97, s55, 0x48000\n"
"buffer_load_dwordx4 v[56:59], v0, s[4:7], s97 offen sc1\n"
"buffer_load_dwordx4 v[172:175], v0, s[4:7], s97 offen offset:1024 sc1\n"
"buffer_load_dwordx4 v[132:135], v0, s[4:7], s97 offen offset:2048 sc1\n"
"buffer_load_dwordx4 v[76:79], v0, s[4:7], s97 offen offset:3072 sc1\n"
"buffer_load_dwordx4 v[24:27], v1, s[4:7], s97 offen sc1\n"
"buffer_load_dwordx4 v[128:131], v1, s[4:7], s97 offen offset:1024 sc1\n"
"buffer_load_dwordx4 v[140:143], v1, s[4:7], s97 offen offset:2048 sc1\n"
"buffer_load_dwordx4 v[188:191], v1, s[4:7], s97 offen offset:3072 sc1\n"
"s_waitcnt vmcnt(0)\n"
"v_max_u32_e32 v255, v36, v37\n"
"v_max3_u32 v255, v255, v38, v39\n"
"v_max3_u32 v255, v255, v100, v101\n"
"v_max3_u32 v255, v255, v102, v103\n"
"v_max3_u32 v255, v255, v108, v109\n"
"v_max3_u32 v255, v255, v110, v111\n"
"v_max3_u32 v255, v255, v116, v117\n"
"v_max3_u32 v255, v255, v118, v119\n"
"v_max3_u32 v255, v255, v124, v125\n"
"v_max3_u32 v255, v255, v126, v127\n"
"v_max3_u32 v255, v255, v160, v161\n"
"v_max3_u32 v255, v255, v162, v163\n"
"v_max3_u32 v255, v255, v192, v193\n"
"v_max3_u32 v255, v255, v194, v195\n"
"v_max3_u32 v255, v255, v208, v209\n"
"v_max3_u32 v255, v255, v210, v211\n"
"v_cmp_eq_u32_e32 vcc, -1, v255\n"
"s_cbranch_vccz .Lmk_p92k\n"
"s_sub_u32 s1, s1, 1\n"
"s_cbranch_scc0 .Lmk_p92l\n"
".Lmk_p92k:\n"
"v_mfma_f32_16x16x32_bf16 v[164:167], a[0:3], v[36:39], 0\n"
"v_mfma_f32_16x16x32_bf16 v[200:203], a[4:7], v[36:39], 0\n"
"v_mfma_f32_16x16x32_bf16 v[204:207], a[8:11], v[36:39], 0\n"
"v_mfma_f32_16x16x32_bf16 v[212:215], a[12:15], v[36:39], 0\n"
"v_mfma_f32_16x16x32_bf16 v[52:55], a[16:19], v[36:39], 0\n"
"v_mfma_f32_16x16x32_bf16 v[220:223], a[20:23], v[36:39], 0\n"
"v_mfma_f32_16x16x32_bf16 v[164:167], a[24:27], v[100:103], v[164:167]\n"
"v_mfma_f32_16x16x32_bf16 v[200:203], a[28:31], v[100:103], v[200:203]\n"
"v_mfma_f32_16x16x32_bf16 v[204:207], a[32:35], v[100:103], v[204:207]\n"
"v_mfma_f32_16x16x32_bf16 v[212:215], a[36:39], v[100:103], v[212:215]\n"
"v_mfma_f32_16x16x32_bf16 v[52:55], a[40:43], v[100:103], v[52:55]\n"
"v_mfma_f32_16x16x32_bf16 v[220:223], a[44:47], v[100:103], v[220:223]\n"
"v_mfma_f32_16x16x32_bf16 v[164:167], a[48:51], v[108:111], v[164:167]\n"
"v_mfma_f32_16x16x32_bf16 v[200:203], a[52:55], v[108:111], v[200:203]\n"
"v_mfma_f32_16x16x32_bf16 v[204:207], a[56:59], v[108:111], v[204:207]\n"
"v_mfma_f32_16x16x32_bf16 v[212:215], a[60:63], v[108:111], v[212:215]\n"
"v_mfma_f32_16x16x32_bf16 v[52:55], a[64:67], v[108:111], v[52:55]\n"
"v_mfma_f32_16x16x32_bf16 v[220:223], a[68:71], v[108:111], v[220:223]\n"
"v_mfma_f32_16x16x32_bf16 v[164:167], a[72:75], v[116:119], v[164:167]\n"
"v_mfma_f32_16x16x32_bf16 v[200:203], a[76:79], v[116:119], v[200:203]\n"
"v_mfma_f32_16x16x32_bf16 v[204:207], a[80:83], v[116:119], v[204:207]\n"
"v_mfma_f32_16x16x32_bf16 v[212:215], a[84:87], v[116:119], v[212:215]\n"
"v_mfma_f32_16x16x32_bf16 v[52:55], a[88:91], v[116:119], v[52:55]\n"
"v_mfma_f32_16x16x32_bf16 v[220:223], a[92:95], v[116:119], v[220:223]\n"
"v_mfma_f32_16x16x32_bf16 v[164:167], a[96:99], v[124:127], v[164:167]\n"
"v_mfma_f32_16x16x32_bf16 v[200:203], a[100:103], v[124:127], v[200:203]\n"
"v_mfma_f32_16x16x32_bf16 v[204:207], a[104:107], v[124:127], v[204:207]\n"
"v_mfma_f32_16x16x32_bf16 v[212:215], a[108:111], v[124:127], v[212:215]\n"
"v_mfma_f32_16x16x32_bf16 v[52:55], a[112:115], v[124:127], v[52:55]\n"
"v_mfma_f32_16x16x32_bf16 v[220:223], a[116:119], v[124:127], v[220:223]\n"
"v_mfma_f32_16x16x32_bf16 v[164:167], a[120:123], v[160:163], v[164:167]\n"
"v_mfma_f32_16x16x32_bf16 v[200:203], a[124:127], v[160:163], v[200:203]\n"
"v_mfma_f32_16x16x32_bf16 v[204:207], v[40:43], v[160:163], v[204:207]\n"
"v_mfma_f32_16x16x32_bf16 v[212:215], v[44:47], v[160:163], v[212:215]\n"
"v_mfma_f32_16x16x32_bf16 v[52:55], v[80:83], v[160:163], v[52:55]\n"
"v_mfma_f32_16x16x32_bf16 v[220:223], v[68:71], v[160:163], v[220:223]\n"
"v_mfma_f32_16x16x32_bf16 v[164:167], v[88:91], v[192:195], v[164:167]\n"
"v_mfma_f32_16x16x32_bf16 v[200:203], v[48:51], v[192:195], v[200:203]\n"
"v_mfma_f32_16x16x32_bf16 v[204:207], v[92:95], v[192:195], v[204:207]\n"
"v_mfma_f32_16x16x32_bf16 v[212:215], v[96:99], v[192:195], v[212:215]\n"
"v_mfma_f32_16x16x32_bf16 v[52:55], v[168:171], v[192:195], v[52:55]\n"
"v_mfma_f32_16x16x32_bf16 v[220:223], v[104:107], v[192:195], v[220:223]\n"
"v_mfma_f32_16x16x32_bf16 v[164:167], v[136:139], v[208:211], v[164:167]\n"
"v_mfma_f32_16x16x32_bf16 v[200:203], v[180:183], v[208:211], v[200:203]\n"
"v_mfma_f32_16x16x32_bf16 v[204:207], v[28:31], v[208:211], v[204:207]\n"
"v_mfma_f32_16x16x32_bf16 v[212:215], v[32:35], v[208:211], v[212:215]\n"
"v_mfma_f32_16x16x32_bf16 v[52:55], v[60:63], v[208:211], v[52:55]\n"
"v_mfma_f32_16x16x32_bf16 v[220:223], v[64:67], v[208:211], v[220:223]\n"
"s_nop 7\n"
"s_branch .Lmk_p92r\n"
".Lmk_q94s:\n"
";\n"
"s_mov_b32 s1, 0x10000\n"
".Lmk_q94l:\n"
"s_add_u32 s99, s55, 0x48000\n"
"buffer_load_dwordx4 v[24:27], v1, s[4:7], s99 offen sc1\n"
"buffer_load_dwordx4 v[128:131], v1, s[4:7], s99 offen offset:1024 sc1\n"
"buffer_load_dwordx4 v[140:143], v1, s[4:7], s99 offen offset:2048 sc1\n"
"buffer_load_dwordx4 v[188:191], v1, s[4:7], s99 offen offset:3072 sc1\n"
"s_waitcnt vmcnt(0)\n"
"v_max_u32_e32 v253, v24, v25\n"
"v_max3_u32 v253, v253, v26, v27\n"
"v_max3_u32 v253, v253, v128, v129\n"
"v_max3_u32 v253, v253, v130, v131\n"
"v_max3_u32 v253, v253, v140, v141\n"
"v_max3_u32 v253, v253, v142, v143\n"
"v_max3_u32 v253, v253, v188, v189\n"
"v_max3_u32 v253, v253, v190, v191\n"
"v_cmp_eq_u32_e32 vcc, -1, v253\n"
"s_cbranch_vccz .Lmk_q94k\n"
"s_sub_u32 s1, s1, 1\n"
"s_cbranch_scc0 .Lmk_q94l\n"
".Lmk_q94k:\n"
"s_branch .Lmk_q94r\n"
".Lmk_f82s:\n"
";\n"
"s_mov_b32 s1, 0x10000\n"
".Lmk_f82l:\n"
"buffer_load_dwordx2 v[250:251], off, s[4:7], s97 sc1\n"
"s_waitcnt vmcnt(0)\n"
"v_cmp_ne_u64_e32 vcc, v[250:251], v[18:19]\n"
"s_cbranch_vccz .Lmk_f82r\n"
"s_sub_u32 s1, s1, 1\n"
"s_cbranch_scc0 .Lmk_f82l\n"
"s_branch .Lmk_f82r\n"
".Lmk_x102s:\n"
"s_nop 3\n"
"v_mov_b32_e32 v64, 0xff800000\n"
"v_mov_b32_e32 v65, 0xff800000\n"
"v_mov_b32_e32 v66, 0xff800000\n"
"v_mov_b32_e32 v67, 0xff800000\n"
"v_mov_b32_e32 v68, 0xff800000\n"
"v_mov_b32_e32 v69, 0xff800000\n"
"v_mov_b32_e32 v70, 0xff800000\n"
"v_mov_b32_e32 v71, 0xff800000\n"
"s_nop 1\n"
"s_branch .Lmk_x102r\n"
".Lmk_x103s:\n"
"s_nop 3\n"
"v_mov_b32_e32 v64, 0xff800000\n"
"v_mov_b32_e32 v65, 0xff800000\n"
"v_mov_b32_e32 v66, 0xff800000\n"
"v_mov_b32_e32 v67, 0xff800000\n"
"v_mov_b32_e32 v68, 0xff800000\n"
"v_mov_b32_e32 v69, 0xff800000\n"
"v_mov_b32_e32 v70, 0xff800000\n"
"v_mov_b32_e32 v71, 0xff800000\n"
"s_nop 1\n"
"s_branch .Lmk_x103r\n"
".Lmk_x104s:\n"
"s_nop 3\n"
"v_mov_b32_e32 v64, 0xff800000\n"
"v_mov_b32_e32 v65, 0xff800000\n"
"v_mov_b32_e32 v66, 0xff800000\n"
"v_mov_b32_e32 v67, 0xff800000\n"
"v_mov_b32_e32 v68, 0xff800000\n"
"v_mov_b32_e32 v69, 0xff800000\n"
"v_mov_b32_e32 v70, 0xff800000\n"
"v_mov_b32_e32 v71, 0xff800000\n"
"s_nop 1\n"
"s_branch .Lmk_x104r\n"
".Lmk_x105s:\n"
"s_nop 3\n"
"v_mov_b32_e32 v64, 0xff800000\n"
"v_mov_b32_e32 v65, 0xff800000\n"
"v_mov_b32_e32 v66, 0xff800000\n"
"v_mov_b32_e32 v67, 0xff800000\n"
"v_mov_b32_e32 v68, 0xff800000\n"
"v_mov_b32_e32 v69, 0xff800000\n"
"v_mov_b32_e32 v70, 0xff800000\n"
"v_mov_b32_e32 v71, 0xff800000\n"
"s_nop 1\n"
"s_branch .Lmk_x105r\n"
".Lmk_f98s:\n"
";\n"
"s_mov_b32 s1, 0x10000\n"
".Lmk_f98l:\n"
"buffer_load_dwordx2 v[246:247], off, s[4:7], s97 sc1\n"
"s_waitcnt vmcnt(0)\n"
"v_cmp_ne_u64_e32 vcc, v[246:247], v[18:19]\n"
"s_cbranch_vccz .Lmk_f98r\n"
"s_sub_u32 s1, s1, 1\n"
"s_cbranch_scc0 .Lmk_f98l\n"
"s_branch .Lmk_f98r\n"
".Lmk_b100:\n"
"s_cmp_lg_u32 s2, 0\n"
"s_cbranch_scc1 .Lmk_k106\n"
"s_mov_b32 s2, 1\n"
"s_waitcnt vmcnt(0) lgkmcnt(0)\n"
"s_nop 7\n"
"s_mov_b32 s97, 11\n"
"v_add_u32_e32 v248, 0x3800, v15\n"
"v_add_u32_e32 v247, 0x3800, v252\n"
"ds_read_b128 v[48:51], v15 offset:24576\n"
"ds_read_b128 v[52:55], v252 offset:24832\n"
"s_waitcnt lgkmcnt(0)\n"
"v_mfma_f32_16x16x32_bf16 v[56:59], v[48:51], v[40:43], 0\n"
"v_mfma_f32_16x16x32_bf16 v[60:63], v[48:51], v[44:47], 0\n"
"v_mfma_f32_16x16x32_bf16 v[48:51], v[52:55], v[40:43], 0\n"
"v_mfma_f32_16x16x32_bf16 v[64:67], v[52:55], v[44:47], 0\n"
".Lmk_b100l:\n"
"ds_read_b128 v[52:55], v248 offset:8192\n"
"ds_read_b128 v[68:71], v247 offset:8448\n"
"s_cmp_gt_i32 s78, s97\n"
"s_cselect_b32 s96, 0xff800000, 0\n"
"v_mov_b32_e32 v72, s96\n"
"v_mov_b32_e32 v73, s96\n"
"v_mov_b32_e32 v74, s96\n"
"v_mov_b32_e32 v75, s96\n"
"v_subrev_u32_e32 v248, 0x800, v248\n"
"v_subrev_u32_e32 v247, 0x800, v247\n"
"s_waitcnt lgkmcnt(0)\n"
"v_mfma_f32_16x16x32_bf16 v[76:79], v[52:55], v[40:43], v[72:75]\n"
"v_mfma_f32_16x16x32_bf16 v[80:83], v[52:55], v[44:47], v[72:75]\n"
"v_mfma_f32_16x16x32_bf16 v[52:55], v[68:71], v[40:43], v[72:75]\n"
"v_mfma_f32_16x16x32_bf16 v[84:87], v[68:71], v[44:47], v[72:75]\n"
"s_nop 7\n"
"s_cmp_lg_u32 s97, 4\n"
"s_cbranch_scc1 .Lmk_b100m\n"
"v_cndmask_b32_e64 v76, v56, v76, s[20:21]\n"
"v_cndmask_b32_e64 v77, v57, v77, s[22:23]\n"
"v_cndmask_b32_e64 v78, v58, v78, s[24:25]\n"
"v_cndmask_b32_e64 v79, v59, v79, s[26:27]\n"
"v_cndmask_b32_e64 v52, v48, v52, s[28:29]\n"
"v_cndmask_b32_e64 v53, v49, v53, s[30:31]\n"
"v_cndmask_b32_e64 v54, v50, v54, s[32:33]\n"
"v_cndmask_b32_e64 v55, v51, v55, s[34:35]\n"
"v_cndmask_b32_e64 v80, v60, v80, s[36:37]\n"
"v_cndmask_b32_e64 v81, v61, v81, s[38:39]\n"
"v_cndmask_b32_e64 v82, v62, v82, s[40:41]\n"
"v_cndmask_b32_e64 v83, v63, v83, s[42:43]\n"
"v_cndmask_b32_e64 v84, v64, v84, s[44:45]\n"
"v_cndmask_b32_e64 v85, v65, v85, s[46:47]\n"
"v_cndmask_b32_e64 v86, v66, v86, s[48:49]\n"
"v_cndmask_b32_e64 v87, v67, v87, s[50:51]\n"
".Lmk_b100m:\n"
"v_max3_f32 v255, v255, v76, v77\n"
"v_max3_f32 v254, v254, v80, v81\n"
"v_max3_f32 v255, v255, v78, v79\n"
"v_max3_f32 v254, v254, v82, v83\n"
"v_max3_f32 v255, v255, v52, v53\n"
"v_max3_f32 v254, v254, v84, v85\n"
"v_max3_f32 v255, v255, v54, v55\n"
"v_max3_f32 v254, v254, v86, v87\n"
"s_sub_u32 s97, s97, 1\n"
"s_cmp_ge_i32 s97, 4\n"
"s_cbranch_scc1 .Lmk_b100l\n"
"v_mov_b32_e32 v248, v255\n"
"v_mov_b32_e32 v247, v254\n"
"s_nop 0\n"
"v_permlane16_swap_b32_e32 v255, v248\n"
"v_permlane16_swap_b32_e32 v254, v247\n"
"v_max_f32_e32 v255, v255, v248\n"
"v_max_f32_e32 v254, v254, v247\n"
"v_mov_b32_e32 v248, v255\n"
"v_mov_b32_e32 v247, v254\n"
"s_nop 0\n"
"v_permlane32_swap_b32_e32 v255, v248\n"
"v_permlane32_swap_b32_e32 v254, v247\n"
"v_max_f32_e32 v255, v255, v248\n"
"v_max_f32_e32 v254, v254, v247\n"
"v_mov_b32_e32 v250, v255\n"
"v_mov_b32_e32 v249, v254\n"
"s_branch .Lmk_a99\n"
".Lmk_x109s:\n"
"s_nop 3\n"
"v_mov_b32_e32 v116, 0xff800000\n"
"v_mov_b32_e32 v117, 0xff800000\n"
"v_mov_b32_e32 v118, 0xff800000\n"
"v_mov_b32_e32 v119, 0xff800000\n"
"v_mov_b32_e32 v120, 0xff800000\n"
"v_mov_b32_e32 v121, 0xff800000\n"
"v_mov_b32_e32 v122, 0xff800000\n"
"v_mov_b32_e32 v123, 0xff800000\n"
"s_nop 1\n"
"s_branch .Lmk_x109r\n"
".Lmk_x110s:\n"
"s_nop 3\n"
"v_mov_b32_e32 v116, 0xff800000\n"
"v_mov_b32_e32 v117, 0xff800000\n"
"v_mov_b32_e32 v118, 0xff800000\n"
"v_mov_b32_e32 v119, 0xff800000\n"
"v_mov_b32_e32 v120, 0xff800000\n"
"v_mov_b32_e32 v121, 0xff800000\n"
"v_mov_b32_e32 v122, 0xff800000\n"
"v_mov_b32_e32 v123, 0xff800000\n"
"s_nop 1\n"
"s_branch .Lmk_x110r\n"
".Lmk_x112s:\n"
"s_nop 3\n"
"v_mov_b32_e32 v116, 0xff800000\n"
"v_mov_b32_e32 v117, 0xff800000\n"
"v_mov_b32_e32 v118, 0xff800000\n"
"v_mov_b32_e32 v119, 0xff800000\n"
"v_mov_b32_e32 v120, 0xff800000\n"
"v_mov_b32_e32 v121, 0xff800000\n"
"v_mov_b32_e32 v122, 0xff800000\n"
"v_mov_b32_e32 v123, 0xff800000\n"
"s_nop 1\n"
"s_branch .Lmk_x112r\n"
".Lmk_f83s:\n"
";\n"
"s_mov_b32 s1, 0x10000\n"
".Lmk_f83l:\n"
"buffer_load_dwordx2 v[246:247], v12, s[4:7], s97 offen sc1\n"
"s_waitcnt vmcnt(0)\n"
"v_cmp_ne_u64_e32 vcc, v[246:247], v[18:19]\n"
"s_cbranch_vccz .Lmk_f83r\n"
"s_sub_u32 s1, s1, 1\n"
"s_cbranch_scc0 .Lmk_f83l\n"
"s_branch .Lmk_f83r\n"
".Lmk_b108:\n"
"s_cmp_lg_u32 s2, 0\n"
"s_cbranch_scc1 .Lmk_k113\n"
"s_mov_b32 s2, 1\n"
"s_waitcnt vmcnt(0) lgkmcnt(0)\n"
"s_nop 7\n"
"s_mov_b32 s97, 7\n"
"v_add_u32_e32 v248, 0x3800, v15\n"
"v_add_u32_e32 v247, 0x3800, v252\n"
"ds_read_b128 v[168:171], v15 offset:16384\n"
"ds_read_b128 v[180:183], v252 offset:16640\n"
"s_waitcnt lgkmcnt(0)\n"
"v_mfma_f32_16x16x32_bf16 v[192:195], v[168:171], v[88:91], 0\n"
"v_mfma_f32_16x16x32_bf16 v[200:203], v[168:171], v[92:95], 0\n"
"v_mfma_f32_16x16x32_bf16 v[168:171], v[180:183], v[88:91], 0\n"
"v_mfma_f32_16x16x32_bf16 v[220:223], v[180:183], v[92:95], 0\n"
".Lmk_b108l:\n"
"ds_read_b128 v[180:183], v248\n"
"ds_read_b128 v[224:227], v247 offset:256\n"
"s_cmp_gt_i32 s78, s97\n"
"s_cselect_b32 s96, 0xff800000, 0\n"
"v_mov_b32_e32 v228, s96\n"
"v_mov_b32_e32 v229, s96\n"
"v_mov_b32_e32 v230, s96\n"
"v_mov_b32_e32 v231, s96\n"
"v_subrev_u32_e32 v248, 0x800, v248\n"
"v_subrev_u32_e32 v247, 0x800, v247\n"
"s_waitcnt lgkmcnt(0)\n"
"v_mfma_f32_16x16x32_bf16 v[232:235], v[180:183], v[88:91], v[228:231]\n"
"v_mfma_f32_16x16x32_bf16 v[236:239], v[180:183], v[92:95], v[228:231]\n"
"v_mfma_f32_16x16x32_bf16 v[180:183], v[224:227], v[88:91], v[228:231]\n"
"v_mfma_f32_16x16x32_bf16 v[240:243], v[224:227], v[92:95], v[228:231]\n"
"s_nop 7\n"
"s_cmp_lg_u32 s97, 0\n"
"s_cbranch_scc1 .Lmk_b108m\n"
"v_cndmask_b32_e64 v232, v192, v232, s[20:21]\n"
"v_cndmask_b32_e64 v233, v193, v233, s[22:23]\n"
"v_cndmask_b32_e64 v234, v194, v234, s[24:25]\n"
"v_cndmask_b32_e64 v235, v195, v235, s[26:27]\n"
"v_cndmask_b32_e64 v180, v168, v180, s[28:29]\n"
"v_cndmask_b32_e64 v181, v169, v181, s[30:31]\n"
"v_cndmask_b32_e64 v182, v170, v182, s[32:33]\n"
"v_cndmask_b32_e64 v183, v171, v183, s[34:35]\n"
"v_cndmask_b32_e64 v236, v200, v236, s[36:37]\n"
"v_cndmask_b32_e64 v237, v201, v237, s[38:39]\n"
"v_cndmask_b32_e64 v238, v202, v238, s[40:41]\n"
"v_cndmask_b32_e64 v239, v203, v239, s[42:43]\n"
"v_cndmask_b32_e64 v240, v220, v240, s[44:45]\n"
"v_cndmask_b32_e64 v241, v221, v241, s[46:47]\n"
"v_cndmask_b32_e64 v242, v222, v242, s[48:49]\n"
"v_cndmask_b32_e64 v243, v223, v243, s[50:51]\n"
".Lmk_b108m:\n"
"v_max3_f32 v255, v255, v232, v233\n"
"v_max3_f32 v254, v254, v236, v237\n"
"v_max3_f32 v255, v255, v234, v235\n"
"v_max3_f32 v254, v254, v238, v239\n"
"v_max3_f32 v255, v255, v180, v181\n"
"v_max3_f32 v254, v254, v240, v241\n"
"v_max3_f32 v255, v255, v182, v183\n"
"v_max3_f32 v254, v254, v242, v243\n"
"s_sub_u32 s97, s97, 1\n"
"s_cmp_ge_i32 s97, 0\n"
"s_cbranch_scc1 .Lmk_b108l\n"
"v_mov_b32_e32 v248, v255\n"
"v_mov_b32_e32 v247, v254\n"
"s_nop 0\n"
"v_permlane16_swap_b32_e32 v255, v248\n"
"v_permlane16_swap_b32_e32 v254, v247\n"
"v_max_f32_e32 v255, v255, v248\n"
"v_max_f32_e32 v254, v254, v247\n"
"v_mov_b32_e32 v248, v255\n"
"v_mov_b32_e32 v247, v254\n"
"s_nop 0\n"
"v_permlane32_swap_b32_e32 v255, v248\n"
"v_permlane32_swap_b32_e32 v254, v247\n"
"v_max_f32_e32 v255, v255, v248\n"
"v_max_f32_e32 v254, v254, v247\n"
"v_mov_b32_e32 v250, v255\n"
"v_mov_b32_e32 v249, v254\n"
"s_branch .Lmk_a107\n"
".Lmk_p114s:\n"
";\n"
"s_mov_b32 s1, 0x10000\n"
".Lmk_p114l:\n"
"s_add_u32 s98, s56, 0x40000\n"
"buffer_load_dwordx4 v[156:159], v0, s[4:7], s98 offen sc1\n"
"buffer_load_dwordx4 v[152:155], v0, s[4:7], s98 offen offset:1024 sc1\n"
"buffer_load_dwordx4 v[120:123], v0, s[4:7], s98 offen offset:2048 sc1\n"
"buffer_load_dwordx4 v[108:111], v0, s[4:7], s98 offen offset:3072 sc1\n"
"buffer_load_dwordx4 v[124:127], v1, s[4:7], s98 offen sc1\n"
"buffer_load_dwordx4 v[148:151], v1, s[4:7], s98 offen offset:1024 sc1\n"
"buffer_load_dwordx4 v[160:163], v1, s[4:7], s98 offen offset:2048 sc1\n"
"buffer_load_dwordx4 v[132:135], v1, s[4:7], s98 offen offset:3072 sc1\n"
"s_add_u32 s98, s57, 0x40000\n"
"buffer_load_dwordx4 v[216:219], v0, s[4:7], s98 offen sc1\n"
"buffer_load_dwordx4 v[212:215], v0, s[4:7], s98 offen offset:1024 sc1\n"
"buffer_load_dwordx4 v[208:211], v0, s[4:7], s98 offen offset:2048 sc1\n"
"buffer_load_dwordx4 v[96:99], v0, s[4:7], s98 offen offset:3072 sc1\n"
"buffer_load_dwordx4 v[204:207], v1, s[4:7], s98 offen sc1\n"
"buffer_load_dwordx4 v[104:107], v1, s[4:7], s98 offen offset:1024 sc1\n"
"buffer_load_dwordx4 v[112:115], v1, s[4:7], s98 offen offset:2048 sc1\n"
"buffer_load_dwordx4 v[128:131], v1, s[4:7], s98 offen offset:3072 sc1\n"
"s_add_u32 s98, s59, 0x40000\n"
"buffer_load_dwordx4 v[164:167], v0, s[4:7], s98 offen sc1\n"
"buffer_load_dwordx4 v[144:147], v0, s[4:7], s98 offen offset:1024 sc1\n"
"buffer_load_dwordx4 v[168:171], v0, s[4:7], s98 offen offset:2048 sc1\n"
"buffer_load_dwordx4 v[116:119], v0, s[4:7], s98 offen offset:3072 sc1\n"
"s_waitcnt vmcnt(0)\n"
"v_max_u32_e32 v255, v156, v157\n"
"v_max3_u32 v255, v255, v158, v159\n"
"v_max3_u32 v255, v255, v152, v153\n"
"v_max3_u32 v255, v255, v154, v155\n"
"v_max3_u32 v255, v255, v120, v121\n"
"v_max3_u32 v255, v255, v122, v123\n"
"v_max3_u32 v255, v255, v108, v109\n"
"v_max3_u32 v255, v255, v110, v111\n"
"v_max3_u32 v255, v255, v124, v125\n"
"v_max3_u32 v255, v255, v126, v127\n"
"v_max3_u32 v255, v255, v148, v149\n"
"v_max3_u32 v255, v255, v150, v151\n"
"v_max3_u32 v255, v255, v160, v161\n"
"v_max3_u32 v255, v255, v162, v163\n"
"v_max3_u32 v255, v255, v132, v133\n"
"v_max3_u32 v255, v255, v134, v135\n"
"v_cmp_eq_u32_e32 vcc, -1, v255\n"
"s_cbranch_vccz .Lmk_p114k\n"
"s_sub_u32 s1, s1, 1\n"
"s_cbranch_scc0 .Lmk_p114l\n"
".Lmk_p114k:\n"
"v_mfma_f32_16x16x32_bf16 v[88:91], a[128:131], v[156:159], 0\n"
"v_mfma_f32_16x16x32_bf16 v[92:95], a[132:135], v[156:159], 0\n"
"v_mfma_f32_16x16x32_bf16 v[100:103], a[136:139], v[156:159], 0\n"
"v_mfma_f32_16x16x32_bf16 v[140:143], a[140:143], v[156:159], 0\n"
"v_mfma_f32_16x16x32_bf16 v[88:91], a[144:147], v[152:155], v[88:91]\n"
"v_mfma_f32_16x16x32_bf16 v[92:95], a[148:151], v[152:155], v[92:95]\n"
"v_mfma_f32_16x16x32_bf16 v[100:103], a[152:155], v[152:155], v[100:103]\n"
"v_mfma_f32_16x16x32_bf16 v[140:143], a[156:159], v[152:155], v[140:143]\n"
"v_mfma_f32_16x16x32_bf16 v[88:91], a[160:163], v[120:123], v[88:91]\n"
"v_mfma_f32_16x16x32_bf16 v[92:95], a[164:167], v[120:123], v[92:95]\n"
"v_mfma_f32_16x16x32_bf16 v[100:103], a[168:171], v[120:123], v[100:103]\n"
"v_mfma_f32_16x16x32_bf16 v[140:143], a[172:175], v[120:123], v[140:143]\n"
"v_mfma_f32_16x16x32_bf16 v[88:91], a[176:179], v[108:111], v[88:91]\n"
"v_mfma_f32_16x16x32_bf16 v[92:95], a[180:183], v[108:111], v[92:95]\n"
"v_mfma_f32_16x16x32_bf16 v[100:103], a[184:187], v[108:111], v[100:103]\n"
"v_mfma_f32_16x16x32_bf16 v[140:143], a[188:191], v[108:111], v[140:143]\n"
"v_mfma_f32_16x16x32_bf16 v[88:91], a[192:195], v[124:127], v[88:91]\n"
"v_mfma_f32_16x16x32_bf16 v[92:95], a[196:199], v[124:127], v[92:95]\n"
"v_mfma_f32_16x16x32_bf16 v[100:103], a[200:203], v[124:127], v[100:103]\n"
"v_mfma_f32_16x16x32_bf16 v[140:143], a[204:207], v[124:127], v[140:143]\n"
"v_mfma_f32_16x16x32_bf16 v[88:91], a[208:211], v[148:151], v[88:91]\n"
"v_mfma_f32_16x16x32_bf16 v[92:95], a[212:215], v[148:151], v[92:95]\n"
"v_mfma_f32_16x16x32_bf16 v[100:103], a[216:219], v[148:151], v[100:103]\n"
"v_mfma_f32_16x16x32_bf16 v[140:143], a[220:223], v[148:151], v[140:143]\n"
"v_mfma_f32_16x16x32_bf16 v[88:91], a[224:227], v[160:163], v[88:91]\n"
"v_mfma_f32_16x16x32_bf16 v[92:95], a[228:231], v[160:163], v[92:95]\n"
"v_mfma_f32_16x16x32_bf16 v[100:103], a[232:235], v[160:163], v[100:103]\n"
"v_mfma_f32_16x16x32_bf16 v[140:143], a[236:239], v[160:163], v[140:143]\n"
"v_mfma_f32_16x16x32_bf16 v[88:91], a[240:243], v[132:135], v[88:91]\n"
"v_mfma_f32_16x16x32_bf16 v[92:95], a[244:247], v[132:135], v[92:95]\n"
"v_mfma_f32_16x16x32_bf16 v[100:103], a[248:251], v[132:135], v[100:103]\n"
"v_mfma_f32_16x16x32_bf16 v[140:143], a[252:255], v[132:135], v[140:143]\n"
"s_add_u32 s98, s58, 0x40000\n"
"v_mov_b32_e32 v255, 0x80000000\n"
"buffer_load_dwordx4 v[156:159], v0, s[4:7], s98 offen sc1\n"
"buffer_load_dwordx4 v[152:155], v0, s[4:7], s98 offen offset:1024 sc1\n"
"buffer_load_dwordx4 v[120:123], v0, s[4:7], s98 offen offset:2048 sc1\n"
"buffer_load_dwordx4 v[108:111], v0, s[4:7], s98 offen offset:3072 sc1\n"
"buffer_store_dword v255, v255, s[4:7], 0 offen\n"
"buffer_store_dword v255, v255, s[4:7], 0 offen\n"
"buffer_load_dwordx4 v[124:127], v1, s[4:7], s98 offen sc1\n"
"buffer_load_dwordx4 v[148:151], v1, s[4:7], s98 offen offset:1024 sc1\n"
"buffer_load_dwordx4 v[160:163], v1, s[4:7], s98 offen offset:2048 sc1\n"
"buffer_load_dwordx4 v[132:135], v1, s[4:7], s98 offen offset:3072 sc1\n"
"buffer_store_dword v255, v255, s[4:7], 0 offen\n"
"buffer_store_dword v255, v255, s[4:7], 0 offen\n"
"buffer_store_dword v255, v255, s[4:7], 0 offen\n"
"buffer_store_dword v255, v255, s[4:7], 0 offen\n"
"s_nop 7\n"
"s_branch .Lmk_p114r\n"
".Lmk_p115s:\n"
";\n"
"s_mov_b32 s1, 0x10000\n"
".Lmk_p115l:\n"
"s_add_u32 s99, s57, 0x40000\n"
"buffer_load_dwordx4 v[164:167], v0, s[4:7], s99 offen sc1\n"
"buffer_load_dwordx4 v[144:147], v0, s[4:7], s99 offen offset:1024 sc1\n"
"buffer_load_dwordx4 v[168:171], v0, s[4:7], s99 offen offset:2048 sc1\n"
"buffer_load_dwordx4 v[116:119], v0, s[4:7], s99 offen offset:3072 sc1\n"
"buffer_load_dwordx4 v[180:183], v1, s[4:7], s99 offen sc1\n"
"buffer_load_dwordx4 v[88:91], v1, s[4:7], s99 offen offset:1024 sc1\n"
"buffer_load_dwordx4 v[92:95], v1, s[4:7], s99 offen offset:2048 sc1\n"
"buffer_load_dwordx4 v[96:99], v1, s[4:7], s99 offen offset:3072 sc1\n"
"s_add_u32 s99, s58, 0x40000\n"
"buffer_load_dwordx4 v[156:159], v0, s[4:7], s99 offen sc1\n"
"buffer_load_dwordx4 v[152:155], v0, s[4:7], s99 offen offset:1024 sc1\n"
"buffer_load_dwordx4 v[120:123], v0, s[4:7], s99 offen offset:2048 sc1\n"
"buffer_load_dwordx4 v[108:111], v0, s[4:7], s99 offen offset:3072 sc1\n"
"buffer_load_dwordx4 v[124:127], v1, s[4:7], s99 offen sc1\n"
"buffer_load_dwordx4 v[148:151], v1, s[4:7], s99 offen offset:1024 sc1\n"
"buffer_load_dwordx4 v[160:163], v1, s[4:7], s99 offen offset:2048 sc1\n"
"buffer_load_dwordx4 v[132:135], v1, s[4:7], s99 offen offset:3072 sc1\n"
"s_waitcnt vmcnt(0)\n"
"v_max_u32_e32 v255, v164, v165\n"
"v_max3_u32 v255, v255, v166, v167\n"
"v_max3_u32 v255, v255, v144, v145\n"
"v_max3_u32 v255, v255, v146, v147\n"
"v_max3_u32 v255, v255, v168, v169\n"
"v_max3_u32 v255, v255, v170, v171\n"
"v_max3_u32 v255, v255, v116, v117\n"
"v_max3_u32 v255, v255, v118, v119\n"
"v_max3_u32 v255, v255, v180, v181\n"
"v_max3_u32 v255, v255, v182, v183\n"
"v_max3_u32 v255, v255, v88, v89\n"
"v_max3_u32 v255, v255, v90, v91\n"
"v_max3_u32 v255, v255, v92, v93\n"
"v_max3_u32 v255, v255, v94, v95\n"
"v_max3_u32 v255, v255, v96, v97\n"
"v_max3_u32 v255, v255, v98, v99\n"
"v_cmp_eq_u32_e32 vcc, -1, v255\n"
"s_cbranch_vccz .Lmk_p115k\n"
"s_sub_u32 s1, s1, 1\n"
"s_cbranch_scc0 .Lmk_p115l\n"
".Lmk_p115k:\n"
"v_mfma_f32_16x16x32_bf16 v[136:139], a[128:131], v[164:167], 0\n"
"v_mfma_f32_16x16x32_bf16 v[172:175], a[132:135], v[164:167], 0\n"
"v_mfma_f32_16x16x32_bf16 v[176:179], a[136:139], v[164:167], 0\n"
"v_mfma_f32_16x16x32_bf16 v[184:187], a[140:143], v[164:167], 0\n"
"v_mfma_f32_16x16x32_bf16 v[136:139], a[144:147], v[144:147], v[136:139]\n"
"v_mfma_f32_16x16x32_bf16 v[172:175], a[148:151], v[144:147], v[172:175]\n"
"v_mfma_f32_16x16x32_bf16 v[176:179], a[152:155], v[144:147], v[176:179]\n"
"v_mfma_f32_16x16x32_bf16 v[184:187], a[156:159], v[144:147], v[184:187]\n"
"v_mfma_f32_16x16x32_bf16 v[136:139], a[160:163], v[168:171], v[136:139]\n"
"v_mfma_f32_16x16x32_bf16 v[172:175], a[164:167], v[168:171], v[172:175]\n"
"v_mfma_f32_16x16x32_bf16 v[176:179], a[168:171], v[168:171], v[176:179]\n"
"v_mfma_f32_16x16x32_bf16 v[184:187], a[172:175], v[168:171], v[184:187]\n"
"v_mfma_f32_16x16x32_bf16 v[136:139], a[176:179], v[116:119], v[136:139]\n"
"v_mfma_f32_16x16x32_bf16 v[172:175], a[180:183], v[116:119], v[172:175]\n"
"v_mfma_f32_16x16x32_bf16 v[176:179], a[184:187], v[116:119], v[176:179]\n"
"v_mfma_f32_16x16x32_bf16 v[184:187], a[188:191], v[116:119], v[184:187]\n"
"v_mfma_f32_16x16x32_bf16 v[136:139], a[192:195], v[180:183], v[136:139]\n"
"v_mfma_f32_16x16x32_bf16 v[172:175], a[196:199], v[180:183], v[172:175]\n"
"v_mfma_f32_16x16x32_bf16 v[176:179], a[200:203], v[180:183], v[176:179]\n"
"v_mfma_f32_16x16x32_bf16 v[184:187], a[204:207], v[180:183], v[184:187]\n"
"v_mfma_f32_16x16x32_bf16 v[136:139], a[208:211], v[88:91], v[136:139]\n"
"v_mfma_f32_16x16x32_bf16 v[172:175], a[212:215], v[88:91], v[172:175]\n"
"v_mfma_f32_16x16x32_bf16 v[176:179], a[216:219], v[88:91], v[176:179]\n"
"v_mfma_f32_16x16x32_bf16 v[184:187], a[220:223], v[88:91], v[184:187]\n"
"v_mfma_f32_16x16x32_bf16 v[136:139], a[224:227], v[92:95], v[136:139]\n"
"v_mfma_f32_16x16x32_bf16 v[172:175], a[228:231], v[92:95], v[172:175]\n"
"v_mfma_f32_16x16x32_bf16 v[176:179], a[232:235], v[92:95], v[176:179]\n"
"v_mfma_f32_16x16x32_bf16 v[184:187], a[236:239], v[92:95], v[184:187]\n"
"v_mfma_f32_16x16x32_bf16 v[136:139], a[240:243], v[96:99], v[136:139]\n"
"v_mfma_f32_16x16x32_bf16 v[172:175], a[244:247], v[96:99], v[172:175]\n"
"v_mfma_f32_16x16x32_bf16 v[176:179], a[248:251], v[96:99], v[176:179]\n"
"v_mfma_f32_16x16x32_bf16 v[184:187], a[252:255], v[96:99], v[184:187]\n"
"s_add_u32 s99, s59, 0x40000\n"
"v_mov_b32_e32 v255, 0x80000000\n"
"buffer_load_dwordx4 v[164:167], v0, s[4:7], s99 offen sc1\n"
"buffer_load_dwordx4 v[144:147], v0, s[4:7], s99 offen offset:1024 sc1\n"
"buffer_load_dwordx4 v[168:171], v0, s[4:7], s99 offen offset:2048 sc1\n"
"buffer_load_dwordx4 v[116:119], v0, s[4:7], s99 offen offset:3072 sc1\n"
"buffer_load_dwordx4 v[180:183], v1, s[4:7], s99 offen sc1\n"
"buffer_load_dwordx4 v[88:91], v1, s[4:7], s99 offen offset:1024 sc1\n"
"buffer_load_dwordx4 v[92:95], v1, s[4:7], s99 offen offset:2048 sc1\n"
"buffer_load_dwordx4 v[96:99], v1, s[4:7], s99 offen offset:3072 sc1\n"
"s_nop 7\n"
"s_branch .Lmk_p115r\n"
".Lmk_q117s:\n"
";\n"
"s_mov_b32 s1, 0x10000\n"
".Lmk_q117l:\n"
"s_add_u32 s98, s59, 0x40000\n"
"buffer_load_dwordx4 v[164:167], v0, s[4:7], s98 offen sc1\n"
"buffer_load_dwordx4 v[144:147], v0, s[4:7], s98 offen offset:1024 sc1\n"
"buffer_load_dwordx4 v[168:171], v0, s[4:7], s98 offen offset:2048 sc1\n"
"buffer_load_dwordx4 v[116:119], v0, s[4:7], s98 offen offset:3072 sc1\n"
"buffer_load_dwordx4 v[180:183], v1, s[4:7], s98 offen sc1\n"
"buffer_load_dwordx4 v[88:91], v1, s[4:7], s98 offen offset:1024 sc1\n"
"buffer_load_dwordx4 v[92:95], v1, s[4:7], s98 offen offset:2048 sc1\n"
"buffer_load_dwordx4 v[96:99], v1, s[4:7], s98 offen offset:3072 sc1\n"
"s_add_u32 s98, s56, 0\n"
"buffer_load_dwordx4 v[120:123], v0, s[4:7], s98 offen sc1\n"
"buffer_load_dwordx4 v[108:111], v0, s[4:7], s98 offen offset:1024 sc1\n"
"buffer_load_dwordx4 v[124:127], v0, s[4:7], s98 offen offset:2048 sc1\n"
"buffer_load_dwordx4 v[136:139], v0, s[4:7], s98 offen offset:3072 sc1\n"
"s_waitcnt vmcnt(0)\n"
"v_max_u32_e32 v255, v164, v165\n"
"v_max3_u32 v255, v255, v166, v167\n"
"v_max3_u32 v255, v255, v144, v145\n"
"v_max3_u32 v255, v255, v146, v147\n"
"v_max3_u32 v255, v255, v168, v169\n"
"v_max3_u32 v255, v255, v170, v171\n"
"v_max3_u32 v255, v255, v116, v117\n"
"v_max3_u32 v255, v255, v118, v119\n"
"v_cmp_eq_u32_e32 vcc, -1, v255\n"
"s_cbranch_vccz .Lmk_q117k\n"
"s_sub_u32 s1, s1, 1\n"
"s_cbranch_scc0 .Lmk_q117l\n"
".Lmk_q117k:\n"
"s_branch .Lmk_q117r\n"
".Lmk_p116s:\n"
";\n"
"s_mov_b32 s1, 0x10000\n"
".Lmk_p116l:\n"
"s_add_u32 s98, s58, 0x40000\n"
"buffer_load_dwordx4 v[132:135], v0, s[4:7], s98 offen sc1\n"
"buffer_load_dwordx4 v[140:143], v0, s[4:7], s98 offen offset:1024 sc1\n"
"buffer_load_dwordx4 v[148:151], v0, s[4:7], s98 offen offset:2048 sc1\n"
"buffer_load_dwordx4 v[152:155], v0, s[4:7], s98 offen offset:3072 sc1\n"
"buffer_load_dwordx4 v[156:159], v1, s[4:7], s98 offen sc1\n"
"buffer_load_dwordx4 v[160:163], v1, s[4:7], s98 offen offset:1024 sc1\n"
"buffer_load_dwordx4 v[172:175], v1, s[4:7], s98 offen offset:2048 sc1\n"
"buffer_load_dwordx4 v[176:179], v1, s[4:7], s98 offen offset:3072 sc1\n"
"s_add_u32 s98, s59, 0x40000\n"
"buffer_load_dwordx4 v[164:167], v0, s[4:7], s98 offen sc1\n"
"buffer_load_dwordx4 v[144:147], v0, s[4:7], s98 offen offset:1024 sc1\n"
"buffer_load_dwordx4 v[168:171], v0, s[4:7], s98 offen offset:2048 sc1\n"
"buffer_load_dwordx4 v[116:119], v0, s[4:7], s98 offen offset:3072 sc1\n"
"buffer_load_dwordx4 v[180:183], v1, s[4:7], s98 offen sc1\n"
"buffer_load_dwordx4 v[88:91], v1, s[4:7], s98 offen offset:1024 sc1\n"
"buffer_load_dwordx4 v[92:95], v1, s[4:7], s98 offen offset:2048 sc1\n"
"buffer_load_dwordx4 v[96:99], v1, s[4:7], s98 offen offset:3072 sc1\n"
"s_add_u32 s98, s56, 0\n"
"buffer_load_dwordx4 v[120:123], v0, s[4:7], s98 offen sc1\n"
"buffer_load_dwordx4 v[108:111], v0, s[4:7], s98 offen offset:1024 sc1\n"
"buffer_load_dwordx4 v[124:127], v0, s[4:7], s98 offen offset:2048 sc1\n"
"buffer_load_dwordx4 v[136:139], v0, s[4:7], s98 offen offset:3072 sc1\n"
"s_waitcnt vmcnt(0)\n"
"v_max_u32_e32 v255, v132, v133\n"
"v_max3_u32 v255, v255, v134, v135\n"
"v_max3_u32 v255, v255, v140, v141\n"
"v_max3_u32 v255, v255, v142, v143\n"
"v_max3_u32 v255, v255, v148, v149\n"
"v_max3_u32 v255, v255, v150, v151\n"
"v_max3_u32 v255, v255, v152, v153\n"
"v_max3_u32 v255, v255, v154, v155\n"
"v_max3_u32 v255, v255, v156, v157\n"
"v_max3_u32 v255, v255, v158, v159\n"
"v_max3_u32 v255, v255, v160, v161\n"
"v_max3_u32 v255, v255, v162, v163\n"
"v_max3_u32 v255, v255, v172, v173\n"
"v_max3_u32 v255, v255, v174, v175\n"
"v_max3_u32 v255, v255, v176, v177\n"
"v_max3_u32 v255, v255, v178, v179\n"
"v_cmp_eq_u32_e32 vcc, -1, v255\n"
"s_cbranch_vccz .Lmk_p116k\n"
"s_sub_u32 s1, s1, 1\n"
"s_cbranch_scc0 .Lmk_p116l\n"
".Lmk_p116k:\n"
"v_mfma_f32_16x16x32_bf16 v[100:103], a[128:131], v[132:135], 0\n"
"v_mfma_f32_16x16x32_bf16 v[104:107], a[132:135], v[132:135], 0\n"
"v_mfma_f32_16x16x32_bf16 v[112:115], a[136:139], v[132:135], 0\n"
"v_mfma_f32_16x16x32_bf16 v[128:131], a[140:143], v[132:135], 0\n"
"v_mfma_f32_16x16x32_bf16 v[100:103], a[144:147], v[140:143], v[100:103]\n"
"v_mfma_f32_16x16x32_bf16 v[104:107], a[148:151], v[140:143], v[104:107]\n"
"v_mfma_f32_16x16x32_bf16 v[112:115], a[152:155], v[140:143], v[112:115]\n"
"v_mfma_f32_16x16x32_bf16 v[128:131], a[156:159], v[140:143], v[128:131]\n"
"v_mfma_f32_16x16x32_bf16 v[100:103], a[160:163], v[148:151], v[100:103]\n"
"v_mfma_f32_16x16x32_bf16 v[104:107], a[164:167], v[148:151], v[104:107]\n"
"v_mfma_f32_16x16x32_bf16 v[112:115], a[168:171], v[148:151], v[112:115]\n"
"v_mfma_f32_16x16x32_bf16 v[128:131], a[172:175], v[148:151], v[128:131]\n"
"v_mfma_f32_16x16x32_bf16 v[100:103], a[176:179], v[152:155], v[100:103]\n"
"v_mfma_f32_16x16x32_bf16 v[104:107], a[180:183], v[152:155], v[104:107]\n"
"v_mfma_f32_16x16x32_bf16 v[112:115], a[184:187], v[152:155], v[112:115]\n"
"v_mfma_f32_16x16x32_bf16 v[128:131], a[188:191], v[152:155], v[128:131]\n"
"v_mfma_f32_16x16x32_bf16 v[100:103], a[192:195], v[156:159], v[100:103]\n"
"v_mfma_f32_16x16x32_bf16 v[104:107], a[196:199], v[156:159], v[104:107]\n"
"v_mfma_f32_16x16x32_bf16 v[112:115], a[200:203], v[156:159], v[112:115]\n"
"v_mfma_f32_16x16x32_bf16 v[128:131], a[204:207], v[156:159], v[128:131]\n"
"v_mfma_f32_16x16x32_bf16 v[100:103], a[208:211], v[160:163], v[100:103]\n"
"v_mfma_f32_16x16x32_bf16 v[104:107], a[212:215], v[160:163], v[104:107]\n"
"v_mfma_f32_16x16x32_bf16 v[112:115], a[216:219], v[160:163], v[112:115]\n"
"v_mfma_f32_16x16x32_bf16 v[128:131], a[220:223], v[160:163], v[128:131]\n"
"v_mfma_f32_16x16x32_bf16 v[100:103], a[224:227], v[172:175], v[100:103]\n"
"v_mfma_f32_16x16x32_bf16 v[104:107], a[228:231], v[172:175], v[104:107]\n"
"v_mfma_f32_16x16x32_bf16 v[112:115], a[232:235], v[172:175], v[112:115]\n"
"v_mfma_f32_16x16x32_bf16 v[128:131], a[236:239], v[172:175], v[128:131]\n"
"v_mfma_f32_16x16x32_bf16 v[100:103], a[240:243], v[176:179], v[100:103]\n"
"v_mfma_f32_16x16x32_bf16 v[104:107], a[244:247], v[176:179], v[104:107]\n"
"v_mfma_f32_16x16x32_bf16 v[112:115], a[248:251], v[176:179], v[112:115]\n"
"v_mfma_f32_16x16x32_bf16 v[128:131], a[252:255], v[176:179], v[128:131]\n"
"s_nop 7\n"
"s_branch .Lmk_p116r\n"
".Lmk_q118s:\n"
";\n"
"s_mov_b32 s1, 0x10000\n"
".Lmk_q118l:\n"
"s_add_u32 s98, s59, 0x40000\n"
"buffer_load_dwordx4 v[180:183], v1, s[4:7], s98 offen sc1\n"
"buffer_load_dwordx4 v[88:91], v1, s[4:7], s98 offen offset:1024 sc1\n"
"buffer_load_dwordx4 v[92:95], v1, s[4:7], s98 offen offset:2048 sc1\n"
"buffer_load_dwordx4 v[96:99], v1, s[4:7], s98 offen offset:3072 sc1\n"
"s_add_u32 s98, s56, 0\n"
"buffer_load_dwordx4 v[120:123], v0, s[4:7], s98 offen sc1\n"
"buffer_load_dwordx4 v[108:111], v0, s[4:7], s98 offen offset:1024 sc1\n"
"buffer_load_dwordx4 v[124:127], v0, s[4:7], s98 offen offset:2048 sc1\n"
"buffer_load_dwordx4 v[136:139], v0, s[4:7], s98 offen offset:3072 sc1\n"
"buffer_load_dwordx4 v[132:135], v1, s[4:7], s98 offen sc1\n"
"buffer_load_dwordx4 v[100:103], v1, s[4:7], s98 offen offset:1024 sc1\n"
"s_waitcnt vmcnt(0)\n"
"v_max_u32_e32 v255, v180, v181\n"
"v_max3_u32 v255, v255, v182, v183\n"
"v_max3_u32 v255, v255, v88, v89\n"
"v_max3_u32 v255, v255, v90, v91\n"
"v_max3_u32 v255, v255, v92, v93\n"
"v_max3_u32 v255, v255, v94, v95\n"
"v_max3_u32 v255, v255, v96, v97\n"
"v_max3_u32 v255, v255, v98, v99\n"
"v_cmp_eq_u32_e32 vcc, -1, v255\n"
"s_cbranch_vccz .Lmk_q118k\n"
"s_sub_u32 s1, s1, 1\n"
"s_cbranch_scc0 .Lmk_q118l\n"
".Lmk_q118k:\n"
"s_branch .Lmk_q118r\n"
".Lmk_p119s:\n"
";\n"
"s_mov_b32 s1, 0x10000\n"
".Lmk_p119l:\n"
"s_add_u32 s98, s56, 0\n"
"buffer_load_dwordx4 v[116:119], v0, s[4:7], s98 offen sc1\n"
"buffer_load_dwordx4 v[140:143], v0, s[4:7], s98 offen offset:1024 sc1\n"
"buffer_load_dwordx4 v[148:151], v0, s[4:7], s98 offen offset:2048 sc1\n"
"buffer_load_dwordx4 v[156:159], v0, s[4:7], s98 offen offset:3072 sc1\n"
"buffer_load_dwordx4 v[168:171], v1, s[4:7], s98 offen sc1\n"
"buffer_load_dwordx4 v[172:175], v1, s[4:7], s98 offen offset:1024 sc1\n"
"buffer_load_dwordx4 v[176:179], v1, s[4:7], s98 offen offset:2048 sc1\n"
"buffer_load_dwordx4 v[180:183], v1, s[4:7], s98 offen offset:3072 sc1\n"
"s_add_u32 s98, s57, 0\n"
"buffer_load_dwordx4 v[128:131], v0, s[4:7], s98 offen sc1\n"
"buffer_load_dwordx4 v[92:95], v0, s[4:7], s98 offen offset:1024 sc1\n"
"buffer_load_dwordx4 v[164:167], v0, s[4:7], s98 offen offset:2048 sc1\n"
"buffer_load_dwordx4 v[88:91], v0, s[4:7], s98 offen offset:3072 sc1\n"
"buffer_load_dwordx4 v[96:99], v1, s[4:7], s98 offen sc1\n"
"buffer_load_dwordx4 v[144:147], v1, s[4:7], s98 offen offset:1024 sc1\n"
"buffer_load_dwordx4 v[152:155], v1, s[4:7], s98 offen offset:2048 sc1\n"
"buffer_load_dwordx4 v[160:163], v1, s[4:7], s98 offen offset:3072 sc1\n"
"s_add_u32 s98, s59, 0\n"
"buffer_load_dwordx4 v[200:203], v0, s[4:7], s98 offen sc1\n"
"buffer_load_dwordx4 v[204:207], v0, s[4:7], s98 offen offset:1024 sc1\n"
"buffer_load_dwordx4 v[208:211], v0, s[4:7], s98 offen offset:2048 sc1\n"
"buffer_load_dwordx4 v[212:215], v0, s[4:7], s98 offen offset:3072 sc1\n"
"buffer_load_dwordx4 v[216:219], v1, s[4:7], s98 offen sc1\n"
"buffer_load_dwordx4 v[220:223], v1, s[4:7], s98 offen offset:1024 sc1\n"
"buffer_load_dwordx4 v[224:227], v1, s[4:7], s98 offen offset:2048 sc1\n"
"s_waitcnt vmcnt(0)\n"
"v_max_u32_e32 v255, v116, v117\n"
"v_max3_u32 v255, v255, v118, v119\n"
"v_max3_u32 v255, v255, v140, v141\n"
"v_max3_u32 v255, v255, v142, v143\n"
"v_max3_u32 v255, v255, v148, v149\n"
"v_max3_u32 v255, v255, v150, v151\n"
"v_max3_u32 v255, v255, v156, v157\n"
"v_max3_u32 v255, v255, v158, v159\n"
"v_max3_u32 v255, v255, v168, v169\n"
"v_max3_u32 v255, v255, v170, v171\n"
"v_max3_u32 v255, v255, v172, v173\n"
"v_max3_u32 v255, v255, v174, v175\n"
"v_max3_u32 v255, v255, v176, v177\n"
"v_max3_u32 v255, v255, v178, v179\n"
"v_max3_u32 v255, v255, v180, v181\n"
"v_max3_u32 v255, v255, v182, v183\n"
"v_cmp_eq_u32_e32 vcc, -1, v255\n"
"s_cbranch_vccz .Lmk_p119k\n"
"s_sub_u32 s1, s1, 1\n"
"s_cbranch_scc0 .Lmk_p119l\n"
".Lmk_p119k:\n"
"v_mfma_f32_16x16x32_bf16 v[184:187], a[128:131], v[116:119], 0\n"
"v_mfma_f32_16x16x32_bf16 v[188:191], a[132:135], v[116:119], 0\n"
"v_mfma_f32_16x16x32_bf16 v[192:195], a[136:139], v[116:119], 0\n"
"v_mfma_f32_16x16x32_bf16 v[196:199], a[140:143], v[116:119], 0\n"
"v_mfma_f32_16x16x32_bf16 v[184:187], a[144:147], v[140:143], v[184:187]\n"
"v_mfma_f32_16x16x32_bf16 v[188:191], a[148:151], v[140:143], v[188:191]\n"
"v_mfma_f32_16x16x32_bf16 v[192:195], a[152:155], v[140:143], v[192:195]\n"
"v_mfma_f32_16x16x32_bf16 v[196:199], a[156:159], v[140:143], v[196:199]\n"
"v_mfma_f32_16x16x32_bf16 v[184:187], a[160:163], v[148:151], v[184:187]\n"
"v_mfma_f32_16x16x32_bf16 v[188:191], a[164:167], v[148:151], v[188:191]\n"
"v_mfma_f32_16x16x32_bf16 v[192:195], a[168:171], v[148:151], v[192:195]\n"
"v_mfma_f32_16x16x32_bf16 v[196:199], a[172:175], v[148:151], v[196:199]\n"
"v_mfma_f32_16x16x32_bf16 v[184:187], a[176:179], v[156:159], v[184:187]\n"
"v_mfma_f32_16x16x32_bf16 v[188:191], a[180:183], v[156:159], v[188:191]\n"
"v_mfma_f32_16x16x32_bf16 v[192:195], a[184:187], v[156:159], v[192:195]\n"
"v_mfma_f32_16x16x32_bf16 v[196:199], a[188:191], v[156:159], v[196:199]\n"
"v_mfma_f32_16x16x32_bf16 v[184:187], a[192:195], v[168:171], v[184:187]\n"
"v_mfma_f32_16x16x32_bf16 v[188:191], a[196:199], v[168:171], v[188:191]\n"
"v_mfma_f32_16x16x32_bf16 v[192:195], a[200:203], v[168:171], v[192:195]\n"
"v_mfma_f32_16x16x32_bf16 v[196:199], a[204:207], v[168:171], v[196:199]\n"
"v_mfma_f32_16x16x32_bf16 v[184:187], a[208:211], v[172:175], v[184:187]\n"
"v_mfma_f32_16x16x32_bf16 v[188:191], a[212:215], v[172:175], v[188:191]\n"
"v_mfma_f32_16x16x32_bf16 v[192:195], a[216:219], v[172:175], v[192:195]\n"
"v_mfma_f32_16x16x32_bf16 v[196:199], a[220:223], v[172:175], v[196:199]\n"
"v_mfma_f32_16x16x32_bf16 v[184:187], a[224:227], v[176:179], v[184:187]\n"
"v_mfma_f32_16x16x32_bf16 v[188:191], a[228:231], v[176:179], v[188:191]\n"
"v_mfma_f32_16x16x32_bf16 v[192:195], a[232:235], v[176:179], v[192:195]\n"
"v_mfma_f32_16x16x32_bf16 v[196:199], a[236:239], v[176:179], v[196:199]\n"
"v_mfma_f32_16x16x32_bf16 v[184:187], a[240:243], v[180:183], v[184:187]\n"
"v_mfma_f32_16x16x32_bf16 v[188:191], a[244:247], v[180:183], v[188:191]\n"
"v_mfma_f32_16x16x32_bf16 v[192:195], a[248:251], v[180:183], v[192:195]\n"
"v_mfma_f32_16x16x32_bf16 v[196:199], a[252:255], v[180:183], v[196:199]\n"
"s_add_u32 s98, s58, 0\n"
"v_mov_b32_e32 v255, 0x80000000\n"
"buffer_load_dwordx4 v[116:119], v0, s[4:7], s98 offen sc1\n"
"buffer_load_dwordx4 v[140:143], v0, s[4:7], s98 offen offset:1024 sc1\n"
"buffer_load_dwordx4 v[148:151], v0, s[4:7], s98 offen offset:2048 sc1\n"
"buffer_load_dwordx4 v[156:159], v0, s[4:7], s98 offen offset:3072 sc1\n"
"buffer_load_dwordx4 v[168:171], v1, s[4:7], s98 offen sc1\n"
"buffer_load_dwordx4 v[172:175], v1, s[4:7], s98 offen offset:1024 sc1\n"
"buffer_load_dwordx4 v[176:179], v1, s[4:7], s98 offen offset:2048 sc1\n"
"buffer_load_dwordx4 v[180:183], v1, s[4:7], s98 offen offset:3072 sc1\n"
"buffer_store_dword v255, v255, s[4:7], 0 offen\n"
"buffer_store_dword v255, v255, s[4:7], 0 offen\n"
"buffer_store_dword v255, v255, s[4:7], 0 offen\n"
"buffer_store_dword v255, v255, s[4:7], 0 offen\n"
"buffer_store_dword v255, v255, s[4:7], 0 offen\n"
"buffer_store_dword v255, v255, s[4:7], 0 offen\n"
"buffer_store_dword v255, v255, s[4:7], 0 offen\n"
"s_nop 7\n"
"s_branch .Lmk_p119r\n"
".Lmk_p120s:\n"
";\n"
"s_mov_b32 s1, 0x10000\n"
".Lmk_p120l:\n"
"s_add_u32 s98, s57, 0\n"
"buffer_load_dwordx4 v[200:203], v0, s[4:7], s98 offen sc1\n"
"buffer_load_dwordx4 v[204:207], v0, s[4:7], s98 offen offset:1024 sc1\n"
"buffer_load_dwordx4 v[208:211], v0, s[4:7], s98 offen offset:2048 sc1\n"
"buffer_load_dwordx4 v[212:215], v0, s[4:7], s98 offen offset:3072 sc1\n"
"buffer_load_dwordx4 v[216:219], v1, s[4:7], s98 offen sc1\n"
"buffer_load_dwordx4 v[220:223], v1, s[4:7], s98 offen offset:1024 sc1\n"
"buffer_load_dwordx4 v[224:227], v1, s[4:7], s98 offen offset:2048 sc1\n"
"buffer_load_dwordx4 v[228:231], v1, s[4:7], s98 offen offset:3072 sc1\n"
"s_add_u32 s98, s58, 0\n"
"buffer_load_dwordx4 v[116:119], v0, s[4:7], s98 offen sc1\n"
"buffer_load_dwordx4 v[140:143], v0, s[4:7], s98 offen offset:1024 sc1\n"
"buffer_load_dwordx4 v[148:151], v0, s[4:7], s98 offen offset:2048 sc1\n"
"buffer_load_dwordx4 v[156:159], v0, s[4:7], s98 offen offset:3072 sc1\n"
"buffer_load_dwordx4 v[168:171], v1, s[4:7], s98 offen sc1\n"
"buffer_load_dwordx4 v[172:175], v1, s[4:7], s98 offen offset:1024 sc1\n"
"buffer_load_dwordx4 v[176:179], v1, s[4:7], s98 offen offset:2048 sc1\n"
"buffer_load_dwordx4 v[180:183], v1, s[4:7], s98 offen offset:3072 sc1\n"
"s_waitcnt vmcnt(0)\n"
"v_max_u32_e32 v255, v200, v201\n"
"v_max3_u32 v255, v255, v202, v203\n"
"v_max3_u32 v255, v255, v204, v205\n"
"v_max3_u32 v255, v255, v206, v207\n"
"v_max3_u32 v255, v255, v208, v209\n"
"v_max3_u32 v255, v255, v210, v211\n"
"v_max3_u32 v255, v255, v212, v213\n"
"v_max3_u32 v255, v255, v214, v215\n"
"v_max3_u32 v255, v255, v216, v217\n"
"v_max3_u32 v255, v255, v218, v219\n"
"v_max3_u32 v255, v255, v220, v221\n"
"v_max3_u32 v255, v255, v222, v223\n"
"v_max3_u32 v255, v255, v224, v225\n"
"v_max3_u32 v255, v255, v226, v227\n"
"v_max3_u32 v255, v255, v228, v229\n"
"v_max3_u32 v255, v255, v230, v231\n"
"v_cmp_eq_u32_e32 vcc, -1, v255\n"
"s_cbranch_vccz .Lmk_p120k\n"
"s_sub_u32 s1, s1, 1\n"
"s_cbranch_scc0 .Lmk_p120l\n"
".Lmk_p120k:\n"
"v_mfma_f32_16x16x32_bf16 v[108:111], a[128:131], v[200:203], 0\n"
"v_mfma_f32_16x16x32_bf16 v[112:115], a[132:135], v[200:203], 0\n"
"v_mfma_f32_16x16x32_bf16 v[120:123], a[136:139], v[200:203], 0\n"
"v_mfma_f32_16x16x32_bf16 v[124:127], a[140:143], v[200:203], 0\n"
"v_mfma_f32_16x16x32_bf16 v[108:111], a[144:147], v[204:207], v[108:111]\n"
"v_mfma_f32_16x16x32_bf16 v[112:115], a[148:151], v[204:207], v[112:115]\n"
"v_mfma_f32_16x16x32_bf16 v[120:123], a[152:155], v[204:207], v[120:123]\n"
"v_mfma_f32_16x16x32_bf16 v[124:127], a[156:159], v[204:207], v[124:127]\n"
"v_mfma_f32_16x16x32_bf16 v[108:111], a[160:163], v[208:211], v[108:111]\n"
"v_mfma_f32_16x16x32_bf16 v[112:115], a[164:167], v[208:211], v[112:115]\n"
"v_mfma_f32_16x16x32_bf16 v[120:123], a[168:171], v[208:211], v[120:123]\n"
"v_mfma_f32_16x16x32_bf16 v[124:127], a[172:175], v[208:211], v[124:127]\n"
"v_mfma_f32_16x16x32_bf16 v[108:111], a[176:179], v[212:215], v[108:111]\n"
"v_mfma_f32_16x16x32_bf16 v[112:115], a[180:183], v[212:215], v[112:115]\n"
"v_mfma_f32_16x16x32_bf16 v[120:123], a[184:187], v[212:215], v[120:123]\n"
"v_mfma_f32_16x16x32_bf16 v[124:127], a[188:191], v[212:215], v[124:127]\n"
"v_mfma_f32_16x16x32_bf16 v[108:111], a[192:195], v[216:219], v[108:111]\n"
"v_mfma_f32_16x16x32_bf16 v[112:115], a[196:199], v[216:219], v[112:115]\n"
"v_mfma_f32_16x16x32_bf16 v[120:123], a[200:203], v[216:219], v[120:123]\n"
"v_mfma_f32_16x16x32_bf16 v[124:127], a[204:207], v[216:219], v[124:127]\n"
"v_mfma_f32_16x16x32_bf16 v[108:111], a[208:211], v[220:223], v[108:111]\n"
"v_mfma_f32_16x16x32_bf16 v[112:115], a[212:215], v[220:223], v[112:115]\n"
"v_mfma_f32_16x16x32_bf16 v[120:123], a[216:219], v[220:223], v[120:123]\n"
"v_mfma_f32_16x16x32_bf16 v[124:127], a[220:223], v[220:223], v[124:127]\n"
"v_mfma_f32_16x16x32_bf16 v[108:111], a[224:227], v[224:227], v[108:111]\n"
"v_mfma_f32_16x16x32_bf16 v[112:115], a[228:231], v[224:227], v[112:115]\n"
"v_mfma_f32_16x16x32_bf16 v[120:123], a[232:235], v[224:227], v[120:123]\n"
"v_mfma_f32_16x16x32_bf16 v[124:127], a[236:239], v[224:227], v[124:127]\n"
"v_mfma_f32_16x16x32_bf16 v[108:111], a[240:243], v[228:231], v[108:111]\n"
"v_mfma_f32_16x16x32_bf16 v[112:115], a[244:247], v[228:231], v[112:115]\n"
"v_mfma_f32_16x16x32_bf16 v[120:123], a[248:251], v[228:231], v[120:123]\n"
"v_mfma_f32_16x16x32_bf16 v[124:127], a[252:255], v[228:231], v[124:127]\n"
"s_add_u32 s98, s59, 0\n"
"v_mov_b32_e32 v255, 0x80000000\n"
"buffer_load_dwordx4 v[200:203], v0, s[4:7], s98 offen sc1\n"
"buffer_load_dwordx4 v[204:207], v0, s[4:7], s98 offen offset:1024 sc1\n"
"buffer_load_dwordx4 v[208:211], v0, s[4:7], s98 offen offset:2048 sc1\n"
"buffer_load_dwordx4 v[212:215], v0, s[4:7], s98 offen offset:3072 sc1\n"
"buffer_load_dwordx4 v[216:219], v1, s[4:7], s98 offen sc1\n"
"buffer_load_dwordx4 v[220:223], v1, s[4:7], s98 offen offset:1024 sc1\n"
"buffer_load_dwordx4 v[224:227], v1, s[4:7], s98 offen offset:2048 sc1\n"
"buffer_load_dwordx4 v[228:231], v1, s[4:7], s98 offen offset:3072 sc1\n"
"s_nop 7\n"
"s_branch .Lmk_p120r\n"
".Lmk_q122s:\n"
";\n"
"s_mov_b32 s1, 0x10000\n"
".Lmk_q122l:\n"
"s_add_u32 s98, s59, 0\n"
"buffer_load_dwordx4 v[200:203], v0, s[4:7], s98 offen sc1\n"
"buffer_load_dwordx4 v[204:207], v0, s[4:7], s98 offen offset:1024 sc1\n"
"buffer_load_dwordx4 v[208:211], v0, s[4:7], s98 offen offset:2048 sc1\n"
"buffer_load_dwordx4 v[212:215], v0, s[4:7], s98 offen offset:3072 sc1\n"
"buffer_load_dwordx4 v[216:219], v1, s[4:7], s98 offen sc1\n"
"buffer_load_dwordx4 v[220:223], v1, s[4:7], s98 offen offset:1024 sc1\n"
"buffer_load_dwordx4 v[224:227], v1, s[4:7], s98 offen offset:2048 sc1\n"
"buffer_load_dwordx4 v[228:231], v1, s[4:7], s98 offen offset:3072 sc1\n"
"s_add_u32 s98, s52, 0x440000\n"
"buffer_load_dwordx4 v[104:107], v0, s[4:7], s98 offen sc1\n"
"buffer_load_dwordx4 v[108:111], v0, s[4:7], s98 offen offset:1024 sc1\n"
"buffer_load_dwordx4 v[112:115], v0, s[4:7], s98 offen offset:2048 sc1\n"
"buffer_load_dwordx4 v[116:119], v0, s[4:7], s98 offen offset:3072 sc1\n"
"buffer_load_dwordx4 v[120:123], v1, s[4:7], s98 offen sc1\n"
"buffer_load_dwordx4 v[124:127], v1, s[4:7], s98 offen offset:1024 sc1\n"
"buffer_load_dwordx4 v[128:131], v1, s[4:7], s98 offen offset:2048 sc1\n"
"buffer_load_dwordx4 v[132:135], v1, s[4:7], s98 offen offset:3072 sc1\n"
"s_add_u32 s98, s53, 0x440000\n"
"buffer_load_dwordx4 v[136:139], v0, s[4:7], s98 offen sc1\n"
"s_waitcnt vmcnt(0)\n"
"v_max_u32_e32 v255, v200, v201\n"
"v_max3_u32 v255, v255, v202, v203\n"
"v_max3_u32 v255, v255, v204, v205\n"
"v_max3_u32 v255, v255, v206, v207\n"
"v_max3_u32 v255, v255, v208, v209\n"
"v_max3_u32 v255, v255, v210, v211\n"
"v_max3_u32 v255, v255, v212, v213\n"
"v_max3_u32 v255, v255, v214, v215\n"
"v_cmp_eq_u32_e32 vcc, -1, v255\n"
"s_cbranch_vccz .Lmk_q122k\n"
"s_sub_u32 s1, s1, 1\n"
"s_cbranch_scc0 .Lmk_q122l\n"
".Lmk_q122k:\n"
"s_branch .Lmk_q122r\n"
".Lmk_p121s:\n"
";\n"
"s_mov_b32 s1, 0x10000\n"
".Lmk_p121l:\n"
"s_add_u32 s98, s58, 0\n"
"buffer_load_dwordx4 v[104:107], v0, s[4:7], s98 offen sc1\n"
"buffer_load_dwordx4 v[108:111], v0, s[4:7], s98 offen offset:1024 sc1\n"
"buffer_load_dwordx4 v[112:115], v0, s[4:7], s98 offen offset:2048 sc1\n"
"buffer_load_dwordx4 v[116:119], v0, s[4:7], s98 offen offset:3072 sc1\n"
"buffer_load_dwordx4 v[120:123], v1, s[4:7], s98 offen sc1\n"
"buffer_load_dwordx4 v[124:127], v1, s[4:7], s98 offen offset:1024 sc1\n"
"buffer_load_dwordx4 v[128:131], v1, s[4:7], s98 offen offset:2048 sc1\n"
"buffer_load_dwordx4 v[132:135], v1, s[4:7], s98 offen offset:3072 sc1\n"
"s_add_u32 s98, s59, 0\n"
"buffer_load_dwordx4 v[200:203], v0, s[4:7], s98 offen sc1\n"
"buffer_load_dwordx4 v[204:207], v0, s[4:7], s98 offen offset:1024 sc1\n"
"buffer_load_dwordx4 v[208:211], v0, s[4:7], s98 offen offset:2048 sc1\n"
"buffer_load_dwordx4 v[212:215], v0, s[4:7], s98 offen offset:3072 sc1\n"
"buffer_load_dwordx4 v[216:219], v1, s[4:7], s98 offen sc1\n"
"buffer_load_dwordx4 v[220:223], v1, s[4:7], s98 offen offset:1024 sc1\n"
"buffer_load_dwordx4 v[224:227], v1, s[4:7], s98 offen offset:2048 sc1\n"
"buffer_load_dwordx4 v[228:231], v1, s[4:7], s98 offen offset:3072 sc1\n"
"s_add_u32 s98, s53, 0x440000\n"
"buffer_load_dwordx4 v[136:139], v0, s[4:7], s98 offen sc1\n"
"s_waitcnt vmcnt(0)\n"
"v_max_u32_e32 v255, v104, v105\n"
"v_max3_u32 v255, v255, v106, v107\n"
"v_max3_u32 v255, v255, v108, v109\n"
"v_max3_u32 v255, v255, v110, v111\n"
"v_max3_u32 v255, v255, v112, v113\n"
"v_max3_u32 v255, v255, v114, v115\n"
"v_max3_u32 v255, v255, v116, v117\n"
"v_max3_u32 v255, v255, v118, v119\n"
"v_max3_u32 v255, v255, v120, v121\n"
"v_max3_u32 v255, v255, v122, v123\n"
"v_max3_u32 v255, v255, v124, v125\n"
"v_max3_u32 v255, v255, v126, v127\n"
"v_max3_u32 v255, v255, v128, v129\n"
"v_max3_u32 v255, v255, v130, v131\n"
"v_max3_u32 v255, v255, v132, v133\n"
"v_max3_u32 v255, v255, v134, v135\n"
"v_cmp_eq_u32_e32 vcc, -1, v255\n"
"s_cbranch_vccz .Lmk_p121k\n"
"s_sub_u32 s1, s1, 1\n"
"s_cbranch_scc0 .Lmk_p121l\n"
".Lmk_p121k:\n"
"v_mfma_f32_16x16x32_bf16 v[144:147], a[128:131], v[104:107], 0\n"
"v_mfma_f32_16x16x32_bf16 v[152:155], a[132:135], v[104:107], 0\n"
"v_mfma_f32_16x16x32_bf16 v[160:163], a[136:139], v[104:107], 0\n"
"v_mfma_f32_16x16x32_bf16 v[164:167], a[140:143], v[104:107], 0\n"
"v_mfma_f32_16x16x32_bf16 v[144:147], a[144:147], v[108:111], v[144:147]\n"
"v_mfma_f32_16x16x32_bf16 v[152:155], a[148:151], v[108:111], v[152:155]\n"
"v_mfma_f32_16x16x32_bf16 v[160:163], a[152:155], v[108:111], v[160:163]\n"
"v_mfma_f32_16x16x32_bf16 v[164:167], a[156:159], v[108:111], v[164:167]\n"
"v_mfma_f32_16x16x32_bf16 v[144:147], a[160:163], v[112:115], v[144:147]\n"
"v_mfma_f32_16x16x32_bf16 v[152:155], a[164:167], v[112:115], v[152:155]\n"
"v_mfma_f32_16x16x32_bf16 v[160:163], a[168:171], v[112:115], v[160:163]\n"
"v_mfma_f32_16x16x32_bf16 v[164:167], a[172:175], v[112:115], v[164:167]\n"
"v_mfma_f32_16x16x32_bf16 v[144:147], a[176:179], v[116:119], v[144:147]\n"
"v_mfma_f32_16x16x32_bf16 v[152:155], a[180:183], v[116:119], v[152:155]\n"
"v_mfma_f32_16x16x32_bf16 v[160:163], a[184:187], v[116:119], v[160:163]\n"
"v_mfma_f32_16x16x32_bf16 v[164:167], a[188:191], v[116:119], v[164:167]\n"
"v_mfma_f32_16x16x32_bf16 v[144:147], a[192:195], v[120:123], v[144:147]\n"
"v_mfma_f32_16x16x32_bf16 v[152:155], a[196:199], v[120:123], v[152:155]\n"
"v_mfma_f32_16x16x32_bf16 v[160:163], a[200:203], v[120:123], v[160:163]\n"
"v_mfma_f32_16x16x32_bf16 v[164:167], a[204:207], v[120:123], v[164:167]\n"
"v_mfma_f32_16x16x32_bf16 v[144:147], a[208:211], v[124:127], v[144:147]\n"
"v_mfma_f32_16x16x32_bf16 v[152:155], a[212:215], v[124:127], v[152:155]\n"
"v_mfma_f32_16x16x32_bf16 v[160:163], a[216:219], v[124:127], v[160:163]\n"
"v_mfma_f32_16x16x32_bf16 v[164:167], a[220:223], v[124:127], v[164:167]\n"
"v_mfma_f32_16x16x32_bf16 v[144:147], a[224:227], v[128:131], v[144:147]\n"
"v_mfma_f32_16x16x32_bf16 v[152:155], a[228:231], v[128:131], v[152:155]\n"
"v_mfma_f32_16x16x32_bf16 v[160:163], a[232:235], v[128:131], v[160:163]\n"
"v_mfma_f32_16x16x32_bf16 v[164:167], a[236:239], v[128:131], v[164:167]\n"
"v_mfma_f32_16x16x32_bf16 v[144:147], a[240:243], v[132:135], v[144:147]\n"
"v_mfma_f32_16x16x32_bf16 v[152:155], a[244:247], v[132:135], v[152:155]\n"
"v_mfma_f32_16x16x32_bf16 v[160:163], a[248:251], v[132:135], v[160:163]\n"
"v_mfma_f32_16x16x32_bf16 v[164:167], a[252:255], v[132:135], v[164:167]\n"
"s_add_u32 s98, s52, 0x440000\n"
"v_mov_b32_e32 v255, 0x80000000\n"
"buffer_load_dwordx4 v[104:107], v0, s[4:7], s98 offen sc1\n"
"buffer_load_dwordx4 v[108:111], v0, s[4:7], s98 offen offset:1024 sc1\n"
"buffer_load_dwordx4 v[112:115], v0, s[4:7], s98 offen offset:2048 sc1\n"
"buffer_load_dwordx4 v[116:119], v0, s[4:7], s98 offen offset:3072 sc1\n"
"buffer_load_dwordx4 v[120:123], v1, s[4:7], s98 offen sc1\n"
"buffer_load_dwordx4 v[124:127], v1, s[4:7], s98 offen offset:1024 sc1\n"
"buffer_load_dwordx4 v[128:131], v1, s[4:7], s98 offen offset:2048 sc1\n"
"buffer_load_dwordx4 v[132:135], v1, s[4:7], s98 offen offset:3072 sc1\n"
"buffer_store_dword v255, v255, s[4:7], 0 offen\n"
"s_nop 7\n"
"s_branch .Lmk_p121r\n"
".Lmk_q123s:\n"
";\n"
"s_mov_b32 s1, 0x10000\n"
".Lmk_q123l:\n"
"s_add_u32 s98, s59, 0\n"
"buffer_load_dwordx4 v[216:219], v1, s[4:7], s98 offen sc1\n"
"buffer_load_dwordx4 v[220:223], v1, s[4:7], s98 offen offset:1024 sc1\n"
"buffer_load_dwordx4 v[224:227], v1, s[4:7], s98 offen offset:2048 sc1\n"
"buffer_load_dwordx4 v[228:231], v1, s[4:7], s98 offen offset:3072 sc1\n"
"s_add_u32 s98, s52, 0x440000\n"
"buffer_load_dwordx4 v[104:107], v0, s[4:7], s98 offen sc1\n"
"buffer_load_dwordx4 v[108:111], v0, s[4:7], s98 offen offset:1024 sc1\n"
"buffer_load_dwordx4 v[112:115], v0, s[4:7], s98 offen offset:2048 sc1\n"
"buffer_load_dwordx4 v[116:119], v0, s[4:7], s98 offen offset:3072 sc1\n"
"buffer_load_dwordx4 v[120:123], v1, s[4:7], s98 offen sc1\n"
"buffer_load_dwordx4 v[124:127], v1, s[4:7], s98 offen offset:1024 sc1\n"
"buffer_load_dwordx4 v[128:131], v1, s[4:7], s98 offen offset:2048 sc1\n"
"buffer_load_dwordx4 v[132:135], v1, s[4:7], s98 offen offset:3072 sc1\n"
"s_add_u32 s98, s53, 0x440000\n"
"buffer_load_dwordx4 v[136:139], v0, s[4:7], s98 offen sc1\n"
"buffer_load_dwordx4 v[140:143], v0, s[4:7], s98 offen offset:1024 sc1\n"
"buffer_load_dwordx4 v[144:147], v0, s[4:7], s98 offen offset:2048 sc1\n"
"s_waitcnt vmcnt(0)\n"
"v_max_u32_e32 v255, v216, v217\n"
"v_max3_u32 v255, v255, v218, v219\n"
"v_max3_u32 v255, v255, v220, v221\n"
"v_max3_u32 v255, v255, v222, v223\n"
"v_max3_u32 v255, v255, v224, v225\n"
"v_max3_u32 v255, v255, v226, v227\n"
"v_max3_u32 v255, v255, v228, v229\n"
"v_max3_u32 v255, v255, v230, v231\n"
"v_cmp_eq_u32_e32 vcc, -1, v255\n"
"s_cbranch_vccz .Lmk_q123k\n"
"s_sub_u32 s1, s1, 1\n"
"s_cbranch_scc0 .Lmk_q123l\n"
".Lmk_q123k:\n"
"s_branch .Lmk_q123r\n"
".Lmk_exit:\n"
"v_xor_b32_e32 v255, 32, v15\n"
"ds_read_b128 v[24:27], v2 offset:17408\n"
"ds_read_b128 v[28:31], v15 offset:16384\n"
"ds_read_b128 v[32:35], v255 offset:16640\n"
"v_xor_b32_e32 v255, 32, v17\n"
"ds_read_b128 v[36:39], v2 offset:16384\n"
"ds_read_b128 v[40:43], v17 offset:15360\n"
"ds_read_b128 v[60:63], v255 offset:15616\n"
"v_xor_b32_e32 v255, 32, v16\n"
"ds_read_b64_tr_b16 v[64:65], v16 offset:16384\n"
"ds_read_b64_tr_b16 v[66:67], v255 offset:16640\n"
"ds_read_b64_tr_b16 v[68:69], v255 offset:16384\n"
"ds_read_b64_tr_b16 v[70:71], v16 offset:16640\n"
"v_mov_b32_e32 v72, 0x3f803f80\n"
"v_mov_b32_e32 v73, 0x3f803f80\n"
"v_mov_b32_e32 v74, 0x3f803f80\n"
"v_mov_b32_e32 v75, 0x3f803f80\n"
"v_xor_b32_e32 v255, 32, v16\n"
"ds_read_b64_tr_b16 v[76:77], v16 offset:15360\n"
"ds_read_b64_tr_b16 v[78:79], v255 offset:15616\n"
"ds_read_b64_tr_b16 v[80:81], v255 offset:15360\n"
"ds_read_b64_tr_b16 v[82:83], v16 offset:15616\n"
"v_mov_b32_e32 v84, 0x3f803f80\n"
"v_mov_b32_e32 v85, 0x3f803f80\n"
"v_mov_b32_e32 v86, 0x3f803f80\n"
"v_mov_b32_e32 v87, 0x3f803f80\n"
"s_waitcnt lgkmcnt(11)\n"
"v_mfma_f32_16x16x32_bf16 v[44:47], v[28:31], v[24:27], v[44:47]\n"
"v_mfma_f32_16x16x32_bf16 v[48:51], v[32:35], v[24:27], v[48:51]\n"
"s_waitcnt lgkmcnt(8)\n"
"v_mfma_f32_16x16x32_bf16 v[52:55], v[40:43], v[36:39], v[52:55]\n"
"v_mfma_f32_16x16x32_bf16 v[56:59], v[60:63], v[36:39], v[56:59]\n"
"s_add_u32 s99, s56, 0x440000\n"
"buffer_load_dwordx4 v[24:27], v0, s[4:7], s99 offen sc1\n"
"buffer_load_dwordx4 v[28:31], v0, s[4:7], s99 offen offset:1024 sc1\n"
"buffer_load_dwordx4 v[32:35], v0, s[4:7], s99 offen offset:2048 sc1\n"
"buffer_load_dwordx4 v[36:39], v0, s[4:7], s99 offen offset:3072 sc1\n"
"v_max_f32_e32 v255, v44, v45\n"
"s_nop 0\n"
"v_max_f32_e32 v254, v52, v53\n"
"v_max3_f32 v255, v255, v46, v47\n"
"v_max3_f32 v254, v254, v54, v55\n"
"v_max3_f32 v255, v255, v48, v49\n"
"v_max3_f32 v254, v254, v56, v57\n"
"v_max3_f32 v255, v255, v50, v51\n"
"v_max3_f32 v254, v254, v58, v59\n"
"v_mov_b32_e32 v253, v255\n"
"v_mov_b32_e32 v252, v254\n"
"s_nop 0\n"
"v_permlane16_swap_b32_e32 v255, v253\n"
"v_permlane16_swap_b32_e32 v254, v252\n"
"v_max_f32_e32 v255, v255, v253\n"
"v_max_f32_e32 v254, v254, v252\n"
"v_mov_b32_e32 v253, v255\n"
"v_mov_b32_e32 v252, v254\n"
"s_nop 0\n"
"v_permlane32_swap_b32_e32 v255, v253\n"
"v_permlane32_swap_b32_e32 v254, v252\n"
"v_max_f32_e32 v255, v255, v253\n"
"v_max_f32_e32 v254, v254, v252\n"
"buffer_load_dwordx4 v[40:43], v1, s[4:7], s99 offen sc1\n"
"v_sub_f32_e32 v44, v44, v255\n"
"v_sub_f32_e32 v45, v45, v255\n"
"v_sub_f32_e32 v46, v46, v255\n"
"v_sub_f32_e32 v47, v47, v255\n"
"v_sub_f32_e32 v48, v48, v255\n"
"v_sub_f32_e32 v49, v49, v255\n"
"v_sub_f32_e32 v50, v50, v255\n"
"buffer_load_dwordx4 v[60:63], v1, s[4:7], s99 offen offset:1024 sc1\n"
"v_sub_f32_e32 v51, v51, v255\n"
"v_exp_f32_e32 v44, v44\n"
"v_exp_f32_e32 v45, v45\n"
"v_exp_f32_e32 v46, v46\n"
"buffer_load_dwordx4 v[88:91], v1, s[4:7], s99 offen offset:2048 sc1\n"
"v_exp_f32_e32 v47, v47\n"
"v_exp_f32_e32 v48, v48\n"
"v_exp_f32_e32 v49, v49\n"
"v_exp_f32_e32 v50, v50\n"
"buffer_load_dwordx4 v[92:95], v1, s[4:7], s99 offen offset:3072 sc1\n"
"v_exp_f32_e32 v51, v51\n"
"v_cvt_pk_bf16_f32 v96, v44, v45\n"
"v_cvt_pk_bf16_f32 v97, v46, v47\n"
"v_cvt_pk_bf16_f32 v98, v48, v49\n"
"v_cvt_pk_bf16_f32 v99, v50, v51\n"
"s_add_u32 s99, s57, 0x440000\n"
"buffer_load_dwordx4 v[100:103], v0, s[4:7], s99 offen sc1\n"
"s_waitcnt lgkmcnt(4)\n"
"v_sub_f32_e32 v52, v52, v254\n"
"v_sub_f32_e32 v53, v53, v254\n"
"v_sub_f32_e32 v54, v54, v254\n"
"v_sub_f32_e32 v55, v55, v254\n"
"v_sub_f32_e32 v56, v56, v254\n"
"v_sub_f32_e32 v57, v57, v254\n"
"v_sub_f32_e32 v58, v58, v254\n"
"v_mfma_f32_16x16x32_bf16 v[104:107], v[72:75], v[96:99], 0\n"
"buffer_load_dwordx4 v[72:75], v0, s[4:7], s99 offen offset:1024 sc1\n"
"v_sub_f32_e32 v59, v59, v254\n"
"v_exp_f32_e32 v52, v52\n"
"v_exp_f32_e32 v53, v53\n"
"v_exp_f32_e32 v54, v54\n"
"v_mfma_f32_16x16x32_bf16 v[108:111], v[64:67], v[96:99], 0\n"
"buffer_load_dwordx4 v[64:67], v0, s[4:7], s99 offen offset:2048 sc1\n"
"v_exp_f32_e32 v55, v55\n"
"v_exp_f32_e32 v56, v56\n"
"v_exp_f32_e32 v57, v57\n"
"v_exp_f32_e32 v58, v58\n"
"buffer_load_dwordx4 v[112:115], v0, s[4:7], s99 offen offset:3072 sc1\n"
"v_mfma_f32_16x16x32_bf16 v[116:119], v[68:71], v[96:99], 0\n"
"v_exp_f32_e32 v59, v59\n"
"v_cvt_pk_bf16_f32 v68, v52, v53\n"
"v_cvt_pk_bf16_f32 v69, v54, v55\n"
"v_cvt_pk_bf16_f32 v70, v56, v57\n"
"v_cvt_pk_bf16_f32 v71, v58, v59\n"
"buffer_load_dwordx4 v[96:99], v1, s[4:7], s99 offen sc1\n"
"s_waitcnt lgkmcnt(0)\n"
"v_rcp_f32_e32 v104, v104\n"
"s_nop 0\n"
"v_mul_f32_e32 v108, v108, v104\n"
"v_mul_f32_e32 v109, v109, v104\n"
"v_mfma_f32_16x16x32_bf16 v[120:123], v[84:87], v[68:71], 0\n"
"buffer_load_dwordx4 v[84:87], v1, s[4:7], s99 offen offset:1024 sc1\n"
"v_mul_f32_e32 v110, v110, v104\n"
"v_mul_f32_e32 v111, v111, v104\n"
"v_mul_f32_e32 v116, v116, v104\n"
"v_mfma_f32_16x16x32_bf16 v[124:127], v[76:79], v[68:71], 0\n"
"buffer_load_dwordx4 v[76:79], v1, s[4:7], s99 offen offset:2048 sc1\n"
"v_mul_f32_e32 v117, v117, v104\n"
"v_mul_f32_e32 v118, v118, v104\n"
"v_mul_f32_e32 v119, v119, v104\n"
"v_cvt_pk_bf16_f32 v104, v108, v109\n"
"buffer_load_dwordx4 v[128:131], v1, s[4:7], s99 offen offset:3072 sc1\n"
"v_mfma_f32_16x16x32_bf16 v[132:135], v[80:83], v[68:71], 0\n"
"v_cvt_pk_bf16_f32 v105, v110, v111\n"
"v_cvt_pk_bf16_f32 v106, v116, v117\n"
"v_cvt_pk_bf16_f32 v107, v118, v119\n"
"s_add_u32 s99, s60, 0x408000\n"
"buffer_store_dwordx4 v[104:107], v0, s[4:7], s99 offen sc1\n"
"v_rcp_f32_e32 v120, v120\n"
"s_nop 0\n"
"v_mul_f32_e32 v124, v124, v120\n"
"v_mul_f32_e32 v125, v125, v120\n"
"v_mul_f32_e32 v126, v126, v120\n"
"v_mul_f32_e32 v127, v127, v120\n"
"v_mul_f32_e32 v132, v132, v120\n"
"v_mul_f32_e32 v133, v133, v120\n"
"v_mul_f32_e32 v134, v134, v120\n"
"v_mul_f32_e32 v135, v135, v120\n"
"v_cvt_pk_bf16_f32 v68, v124, v125\n"
"v_cvt_pk_bf16_f32 v69, v126, v127\n"
"v_cvt_pk_bf16_f32 v70, v132, v133\n"
"v_cvt_pk_bf16_f32 v71, v134, v135\n"
"s_add_u32 s99, s60, 0x400000\n"
"buffer_store_dwordx4 v[68:71], v0, s[4:7], s99 offen sc1\n"
"s_waitcnt vmcnt(17)\n"
"; @B G2 b.0\n"
"s_nop 0\n"
"v_mfma_f32_16x16x32_bf16 v[68:71], a[128:131], v[24:27], 0\n"
"s_add_u32 s99, s58, 0x440000\n"
"v_mfma_f32_16x16x32_bf16 v[80:83], a[132:135], v[24:27], 0\n"
"buffer_load_dwordx4 v[104:107], v0, s[4:7], s99 offen sc1\n"
"v_mfma_f32_16x16x32_bf16 v[108:111], a[136:139], v[24:27], 0\n"
"v_mfma_f32_16x16x32_bf16 v[116:119], a[140:143], v[24:27], 0\n"
"s_waitcnt vmcnt(17)\n"
"v_mfma_f32_16x16x32_bf16 v[68:71], a[144:147], v[28:31], v[68:71]\n"
"v_mfma_f32_16x16x32_bf16 v[80:83], a[148:151], v[28:31], v[80:83]\n"
"buffer_load_dwordx4 v[24:27], v0, s[4:7], s99 offen offset:1024 sc1\n"
"v_mfma_f32_16x16x32_bf16 v[108:111], a[152:155], v[28:31], v[108:111]\n"
"v_mfma_f32_16x16x32_bf16 v[116:119], a[156:159], v[28:31], v[116:119]\n"
"s_waitcnt vmcnt(17)\n"
"v_mfma_f32_16x16x32_bf16 v[68:71], a[160:163], v[32:35], v[68:71]\n"
"v_mfma_f32_16x16x32_bf16 v[80:83], a[164:167], v[32:35], v[80:83]\n"
"buffer_load_dwordx4 v[28:31], v0, s[4:7], s99 offen offset:2048 sc1\n"
"v_mfma_f32_16x16x32_bf16 v[108:111], a[168:171], v[32:35], v[108:111]\n"
"v_mfma_f32_16x16x32_bf16 v[116:119], a[172:175], v[32:35], v[116:119]\n"
"s_waitcnt vmcnt(17)\n"
"v_mfma_f32_16x16x32_bf16 v[68:71], a[176:179], v[36:39], v[68:71]\n"
"v_mfma_f32_16x16x32_bf16 v[80:83], a[180:183], v[36:39], v[80:83]\n"
"buffer_load_dwordx4 v[32:35], v0, s[4:7], s99 offen offset:3072 sc1\n"
"v_mfma_f32_16x16x32_bf16 v[108:111], a[184:187], v[36:39], v[108:111]\n"
"v_mfma_f32_16x16x32_bf16 v[116:119], a[188:191], v[36:39], v[116:119]\n"
"s_waitcnt vmcnt(17)\n"
"v_mfma_f32_16x16x32_bf16 v[68:71], a[192:195], v[40:43], v[68:71]\n"
"v_mfma_f32_16x16x32_bf16 v[80:83], a[196:199], v[40:43], v[80:83]\n"
"buffer_load_dwordx4 v[36:39], v1, s[4:7], s99 offen sc1\n"
"v_mfma_f32_16x16x32_bf16 v[108:111], a[200:203], v[40:43], v[108:111]\n"
"v_mfma_f32_16x16x32_bf16 v[116:119], a[204:207], v[40:43], v[116:119]\n"
"s_waitcnt vmcnt(17)\n"
"v_mfma_f32_16x16x32_bf16 v[68:71], a[208:211], v[60:63], v[68:71]\n"
"v_mfma_f32_16x16x32_bf16 v[80:83], a[212:215], v[60:63], v[80:83]\n"
"buffer_load_dwordx4 v[40:43], v1, s[4:7], s99 offen offset:1024 sc1\n"
"v_mfma_f32_16x16x32_bf16 v[108:111], a[216:219], v[60:63], v[108:111]\n"
"v_mfma_f32_16x16x32_bf16 v[116:119], a[220:223], v[60:63], v[116:119]\n"
"s_waitcnt vmcnt(17)\n"
"v_mfma_f32_16x16x32_bf16 v[68:71], a[224:227], v[88:91], v[68:71]\n"
"v_mfma_f32_16x16x32_bf16 v[80:83], a[228:231], v[88:91], v[80:83]\n"
"buffer_load_dwordx4 v[60:63], v1, s[4:7], s99 offen offset:2048 sc1\n"
"v_mfma_f32_16x16x32_bf16 v[108:111], a[232:235], v[88:91], v[108:111]\n"
"v_mfma_f32_16x16x32_bf16 v[116:119], a[236:239], v[88:91], v[116:119]\n"
"s_waitcnt vmcnt(17)\n"
"v_mfma_f32_16x16x32_bf16 v[68:71], a[240:243], v[92:95], v[68:71]\n"
"v_mfma_f32_16x16x32_bf16 v[80:83], a[244:247], v[92:95], v[80:83]\n"
"buffer_load_dwordx4 v[88:91], v1, s[4:7], s99 offen offset:3072 sc1\n"
"v_mfma_f32_16x16x32_bf16 v[108:111], a[248:251], v[92:95], v[108:111]\n"
"v_mfma_f32_16x16x32_bf16 v[116:119], a[252:255], v[92:95], v[116:119]\n"
"s_waitcnt vmcnt(16)\n"
"s_add_u32 s99, s59, 0x440000\n"
"; @B G2 b.1\n"
"v_mfma_f32_16x16x32_bf16 v[92:95], a[128:131], v[100:103], 0\n"
"s_nop 0\n"
"v_cmp_u_f32_e32 vcc, v68, v68\n"
"v_mfma_f32_16x16x32_bf16 v[120:123], a[132:135], v[100:103], 0\n"
"s_cbranch_vccnz .Lmk_p61s\n"
".Lmk_p61r:\n"
"v_mfma_f32_16x16x32_bf16 v[124:127], a[136:139], v[100:103], 0\n"
"buffer_load_dwordx4 v[132:135], v0, s[4:7], s99 offen sc1\n"
"v_mfma_f32_16x16x32_bf16 v[136:139], a[140:143], v[100:103], 0\n"
"v_mfma_f32_16x16x32_bf16 v[92:95], a[144:147], v[72:75], v[92:95]\n"
"ds_write_b128 v3, v[68:71]\n"
"v_mfma_f32_16x16x32_bf16 v[120:123], a[148:151], v[72:75], v[120:123]\n"
"v_mfma_f32_16x16x32_bf16 v[124:127], a[152:155], v[72:75], v[124:127]\n"
"ds_write_b128 v3, v[80:83] offset:1024\n"
"v_mfma_f32_16x16x32_bf16 v[136:139], a[156:159], v[72:75], v[136:139]\n"
"s_waitcnt vmcnt(15)\n"
"v_mfma_f32_16x16x32_bf16 v[92:95], a[160:163], v[64:67], v[92:95]\n"
"ds_write_b128 v3, v[108:111] offset:2048\n"
"v_mfma_f32_16x16x32_bf16 v[120:123], a[164:167], v[64:67], v[120:123]\n"
"buffer_load_dwordx4 v[68:71], v0, s[4:7], s99 offen offset:1024 sc1\n"
"v_mfma_f32_16x16x32_bf16 v[124:127], a[168:171], v[64:67], v[124:127]\n"
"ds_write_b128 v3, v[116:119] offset:3072\n"
"v_mfma_f32_16x16x32_bf16 v[136:139], a[172:175], v[64:67], v[136:139]\n"
"v_mfma_f32_16x16x32_bf16 v[92:95], a[176:179], v[112:115], v[92:95]\n"
"v_mfma_f32_16x16x32_bf16 v[120:123], a[180:183], v[112:115], v[120:123]\n"
"buffer_load_dwordx4 v[64:67], v0, s[4:7], s99 offen offset:2048 sc1\n"
"v_mfma_f32_16x16x32_bf16 v[124:127], a[184:187], v[112:115], v[124:127]\n"
"ds_read_b128 v[72:75], v2 offset:8192\n"
"ds_read_b128 v[80:83], v2 offset:9216\n"
"v_mfma_f32_16x16x32_bf16 v[136:139], a[188:191], v[112:115], v[136:139]\n"
"s_waitcnt vmcnt(15)\n"
"v_mfma_f32_16x16x32_bf16 v[92:95], a[192:195], v[96:99], v[92:95]\n"
"v_mfma_f32_16x16x32_bf16 v[120:123], a[196:199], v[96:99], v[120:123]\n"
"buffer_load_dwordx4 v[100:103], v0, s[4:7], s99 offen offset:3072 sc1\n"
"v_mfma_f32_16x16x32_bf16 v[124:127], a[200:203], v[96:99], v[124:127]\n"
"v_mfma_f32_16x16x32_bf16 v[136:139], a[204:207], v[96:99], v[136:139]\n"
"s_waitcnt lgkmcnt(0)\n"
"s_barrier\n"
"v_mfma_f32_16x16x32_bf16 v[92:95], a[208:211], v[84:87], v[92:95]\n"
"v_lshlrev_b32_e32 v255, 16, v72\n"
"v_mfma_f32_16x16x32_bf16 v[120:123], a[212:215], v[84:87], v[120:123]\n"
"buffer_load_dwordx4 v[96:99], v1, s[4:7], s99 offen sc1\n"
"v_mfma_f32_16x16x32_bf16 v[124:127], a[216:219], v[84:87], v[124:127]\n"
"ds_read_b128 v[108:111], v4\n"
"ds_read_b128 v[112:115], v4 offset:1024\n"
"v_mfma_f32_16x16x32_bf16 v[136:139], a[220:223], v[84:87], v[136:139]\n"
"s_waitcnt vmcnt(15)\n"
"ds_read_b128 v[84:87], v4 offset:2048\n"
"v_mfma_f32_16x16x32_bf16 v[92:95], a[224:227], v[76:79], v[92:95]\n"
"ds_read_b128 v[116:119], v4 offset:3072\n"
"v_and_b32_e32 v254, 0xffff0000, v72\n"
"v_mfma_f32_16x16x32_bf16 v[120:123], a[228:231], v[76:79], v[120:123]\n"
"buffer_load_dwordx4 v[140:143], v1, s[4:7], s99 offen offset:1024 sc1\n"
"v_mfma_f32_16x16x32_bf16 v[124:127], a[232:235], v[76:79], v[124:127]\n"
"v_lshlrev_b32_e32 v253, 16, v73\n"
"v_and_b32_e32 v252, 0xffff0000, v73\n"
"v_mfma_f32_16x16x32_bf16 v[136:139], a[236:239], v[76:79], v[136:139]\n"
"v_lshlrev_b32_e32 v251, 16, v74\n"
"v_and_b32_e32 v250, 0xffff0000, v74\n"
"v_mfma_f32_16x16x32_bf16 v[92:95], a[240:243], v[128:131], v[92:95]\n"
"v_lshlrev_b32_e32 v249, 16, v75\n"
"v_and_b32_e32 v248, 0xffff0000, v75\n"
"v_mfma_f32_16x16x32_bf16 v[120:123], a[244:247], v[128:131], v[120:123]\n"
"buffer_load_dwordx4 v[72:75], v1, s[4:7], s99 offen offset:2048 sc1\n"
"v_mfma_f32_16x16x32_bf16 v[124:127], a[248:251], v[128:131], v[124:127]\n"
"v_lshlrev_b32_e32 v247, 16, v80\n"
"v_and_b32_e32 v246, 0xffff0000, v80\n"
"v_mfma_f32_16x16x32_bf16 v[136:139], a[252:255], v[128:131], v[136:139]\n"
"s_waitcnt vmcnt(13)\n"
"v_lshlrev_b32_e32 v245, 16, v81\n"
"; @B G2 b.2\n"
"v_mfma_f32_16x16x32_bf16 v[76:79], a[128:131], v[104:107], 0\n"
"v_cmp_u_f32_e32 vcc, v92, v92\n"
"v_and_b32_e32 v244, 0xffff0000, v81\n"
"v_mfma_f32_16x16x32_bf16 v[128:131], a[132:135], v[104:107], 0\n"
"s_cbranch_vccnz .Lmk_p62s\n"
".Lmk_p62r:\n"
"v_lshlrev_b32_e32 v243, 16, v82\n"
"v_mfma_f32_16x16x32_bf16 v[144:147], a[136:139], v[104:107], 0\n"
"buffer_load_dwordx4 v[148:151], v1, s[4:7], s99 offen offset:3072 sc1\n"
"v_mfma_f32_16x16x32_bf16 v[152:155], a[140:143], v[104:107], 0\n"
"v_and_b32_e32 v242, 0xffff0000, v82\n"
"v_lshlrev_b32_e32 v241, 16, v83\n"
"v_mfma_f32_16x16x32_bf16 v[76:79], a[144:147], v[24:27], v[76:79]\n"
"ds_write_b128 v3, v[92:95] offset:6144\n"
"v_mfma_f32_16x16x32_bf16 v[128:131], a[148:151], v[24:27], v[128:131]\n"
"v_and_b32_e32 v240, 0xffff0000, v83\n"
"s_waitcnt lgkmcnt(1)\n"
"v_mfma_f32_16x16x32_bf16 v[144:147], a[152:155], v[24:27], v[144:147]\n"
"ds_write_b128 v3, v[120:123] offset:7168\n"
"v_mfma_f32_16x16x32_bf16 v[152:155], a[156:159], v[24:27], v[152:155]\n"
"s_waitcnt vmcnt(12)\n"
"v_add_f32_e32 v108, v108, v255\n"
"v_mfma_f32_16x16x32_bf16 v[76:79], a[160:163], v[28:31], v[76:79]\n"
"ds_write_b128 v3, v[124:127] offset:8192\n"
"v_mfma_f32_16x16x32_bf16 v[128:131], a[164:167], v[28:31], v[128:131]\n"
"v_add_f32_e32 v109, v109, v254\n"
"v_add_f32_e32 v110, v110, v253\n"
"v_mfma_f32_16x16x32_bf16 v[144:147], a[168:171], v[28:31], v[144:147]\n"
"ds_write_b128 v3, v[136:139] offset:9216\n"
"v_mfma_f32_16x16x32_bf16 v[152:155], a[172:175], v[28:31], v[152:155]\n"
"v_add_f32_e32 v111, v111, v252\n"
"v_add_f32_e32 v112, v112, v251\n"
"v_mfma_f32_16x16x32_bf16 v[76:79], a[176:179], v[32:35], v[76:79]\n"
"s_add_u32 s99, s56, 0x400000\n"
"v_add_f32_e32 v113, v113, v250\n"
"v_mfma_f32_16x16x32_bf16 v[128:131], a[180:183], v[32:35], v[128:131]\n"
"buffer_load_dwordx4 v[24:27], v0, s[4:7], s99 offen sc1\n"
"v_mfma_f32_16x16x32_bf16 v[144:147], a[184:187], v[32:35], v[144:147]\n"
"v_add_f32_e32 v114, v114, v249\n"
"v_add_f32_e32 v115, v115, v248\n"
"v_mfma_f32_16x16x32_bf16 v[152:155], a[188:191], v[32:35], v[152:155]\n"
"s_waitcnt vmcnt(11)\n"
"v_add_f32_e32 v84, v84, v247\n"
"v_mfma_f32_16x16x32_bf16 v[76:79], a[192:195], v[36:39], v[76:79]\n"
"buffer_load_dwordx4 v[28:31], v0, s[4:7], s99 offen offset:1024 sc1\n"
"v_mfma_f32_16x16x32_bf16 v[128:131], a[196:199], v[36:39], v[128:131]\n"
"v_add_f32_e32 v85, v85, v246\n"
"v_add_f32_e32 v86, v86, v245\n"
"v_mfma_f32_16x16x32_bf16 v[144:147], a[200:203], v[36:39], v[144:147]\n"
"v_add_f32_e32 v87, v87, v244\n"
"v_add_f32_e32 v116, v116, v243\n"
"v_mfma_f32_16x16x32_bf16 v[152:155], a[204:207], v[36:39], v[152:155]\n"
"buffer_load_dwordx4 v[32:35], v0, s[4:7], s99 offen offset:2048 sc1\n"
"v_mfma_f32_16x16x32_bf16 v[76:79], a[208:211], v[40:43], v[76:79]\n"
"v_add_f32_e32 v117, v117, v242\n"
"v_add_f32_e32 v118, v118, v241\n"
"v_mfma_f32_16x16x32_bf16 v[128:131], a[212:215], v[40:43], v[128:131]\n"
"v_add_f32_e32 v119, v119, v240\n"
"v_mfma_f32_16x16x32_bf16 v[144:147], a[216:219], v[40:43], v[144:147]\n"
"buffer_load_dwordx4 v[36:39], v0, s[4:7], s99 offen offset:3072 sc1\n"
"v_mfma_f32_16x16x32_bf16 v[152:155], a[220:223], v[40:43], v[152:155]\n"
"s_waitcnt vmcnt(12)\n"
"v_mfma_f32_16x16x32_bf16 v[76:79], a[224:227], v[60:63], v[76:79]\n"
"s_waitcnt vmcnt(10)\n"
"v_max_u32_e32 v255, v132, v133\n"
"v_mfma_f32_16x16x32_bf16 v[128:131], a[228:231], v[60:63], v[128:131]\n"
"v_max3_u32 v255, v255, v134, v135\n"
"v_max3_u32 v255, v255, v68, v69\n"
"v_mfma_f32_16x16x32_bf16 v[144:147], a[232:235], v[60:63], v[144:147]\n"
"v_max3_u32 v255, v255, v70, v71\n"
"s_waitcnt vmcnt(8)\n"
"v_mfma_f32_16x16x32_bf16 v[152:155], a[236:239], v[60:63], v[152:155]\n"
"v_max3_u32 v255, v255, v64, v65\n"
"v_max3_u32 v255, v255, v66, v67\n"
"v_mfma_f32_16x16x32_bf16 v[76:79], a[240:243], v[88:91], v[76:79]\n"
"v_max3_u32 v255, v255, v100, v101\n"
"v_max3_u32 v255, v255, v102, v103\n"
"v_mfma_f32_16x16x32_bf16 v[128:131], a[244:247], v[88:91], v[128:131]\n"
"v_cmp_eq_u32_e32 vcc, -1, v255\n"
"v_mfma_f32_16x16x32_bf16 v[144:147], a[248:251], v[88:91], v[144:147]\n"
"s_cbranch_vccnz .Lmk_q64s\n"
".Lmk_q64r:\n"
"v_mfma_f32_16x16x32_bf16 v[152:155], a[252:255], v[88:91], v[152:155]\n"
"; @B G2 b.3\n"
"v_mfma_f32_16x16x32_bf16 v[108:111], a[128:131], v[132:135], v[108:111]\n"
"v_cmp_u_f32_e32 vcc, v76, v76\n"
"v_mfma_f32_16x16x32_bf16 v[112:115], a[132:135], v[132:135], v[112:115]\n"
"s_cbranch_vccnz .Lmk_p63s\n"
".Lmk_p63r:\n"
"v_mfma_f32_16x16x32_bf16 v[84:87], a[136:139], v[132:135], v[84:87]\n"
"buffer_load_dwordx4 v[40:43], v1, s[4:7], s99 offen sc1\n"
"v_mfma_f32_16x16x32_bf16 v[116:119], a[140:143], v[132:135], v[116:119]\n"
"v_mfma_f32_16x16x32_bf16 v[108:111], a[144:147], v[68:71], v[108:111]\n"
"ds_write_b128 v3, v[76:79] offset:12288\n"
"v_mfma_f32_16x16x32_bf16 v[112:115], a[148:151], v[68:71], v[112:115]\n"
"buffer_load_dwordx4 v[60:63], v1, s[4:7], s99 offen offset:1024 sc1\n"
"v_mfma_f32_16x16x32_bf16 v[84:87], a[152:155], v[68:71], v[84:87]\n"
"ds_write_b128 v3, v[128:131] offset:13312\n"
"v_mfma_f32_16x16x32_bf16 v[116:119], a[156:159], v[68:71], v[116:119]\n"
"v_mfma_f32_16x16x32_bf16 v[108:111], a[160:163], v[64:67], v[108:111]\n"
"s_waitcnt vmcnt(8)\n"
"v_max_u32_e32 v255, v96, v97\n"
"v_mfma_f32_16x16x32_bf16 v[112:115], a[164:167], v[64:67], v[112:115]\n"
"v_max3_u32 v255, v255, v98, v99\n"
"v_max3_u32 v255, v255, v140, v141\n"
"v_mfma_f32_16x16x32_bf16 v[84:87], a[168:171], v[64:67], v[84:87]\n"
"v_max3_u32 v255, v255, v142, v143\n"
"s_waitcnt vmcnt(6)\n"
"v_mfma_f32_16x16x32_bf16 v[116:119], a[172:175], v[64:67], v[116:119]\n"
"v_max3_u32 v255, v255, v72, v73\n"
"v_max3_u32 v255, v255, v74, v75\n"
"v_mfma_f32_16x16x32_bf16 v[108:111], a[176:179], v[100:103], v[108:111]\n"
"v_max3_u32 v255, v255, v148, v149\n"
"v_max3_u32 v255, v255, v150, v151\n"
"v_mfma_f32_16x16x32_bf16 v[112:115], a[180:183], v[100:103], v[112:115]\n"
"v_cmp_eq_u32_e32 vcc, -1, v255\n"
"v_mfma_f32_16x16x32_bf16 v[84:87], a[184:187], v[100:103], v[84:87]\n"
"s_cbranch_vccnz .Lmk_q65s\n"
".Lmk_q65r:\n"
"v_mfma_f32_16x16x32_bf16 v[116:119], a[188:191], v[100:103], v[116:119]\n"
"v_mfma_f32_16x16x32_bf16 v[108:111], a[192:195], v[96:99], v[108:111]\n"
"ds_write_b128 v3, v[144:147] offset:14336\n"
"v_mfma_f32_16x16x32_bf16 v[112:115], a[196:199], v[96:99], v[112:115]\n"
"buffer_load_dwordx4 v[64:67], v1, s[4:7], s99 offen offset:2048 sc1\n"
"v_mfma_f32_16x16x32_bf16 v[84:87], a[200:203], v[96:99], v[84:87]\n"
"ds_write_b128 v3, v[152:155] offset:15360\n"
"v_mfma_f32_16x16x32_bf16 v[116:119], a[204:207], v[96:99], v[116:119]\n"
"v_mfma_f32_16x16x32_bf16 v[108:111], a[208:211], v[140:143], v[108:111]\n"
"buffer_load_dwordx4 v[68:71], v1, s[4:7], s99 offen offset:3072 sc1\n"
"v_mfma_f32_16x16x32_bf16 v[112:115], a[212:215], v[140:143], v[112:115]\n"
"s_waitcnt lgkmcnt(0)\n"
"s_barrier\n"
"v_mfma_f32_16x16x32_bf16 v[84:87], a[216:219], v[140:143], v[84:87]\n"
"s_add_u32 s99, s57, 0x400000\n"
"ds_read_b128 v[76:79], v5\n"
"v_mfma_f32_16x16x32_bf16 v[116:119], a[220:223], v[140:143], v[116:119]\n"
"ds_read_b128 v[80:83], v6\n"
"v_mfma_f32_16x16x32_bf16 v[108:111], a[224:227], v[72:75], v[108:111]\n"
"buffer_load_dwordx4 v[88:91], v0, s[4:7], s99 offen sc1\n"
"v_mfma_f32_16x16x32_bf16 v[112:115], a[228:231], v[72:75], v[112:115]\n"
"ds_read_b128 v[92:95], v5 offset:1024\n"
"ds_read_b128 v[96:99], v6 offset:1024\n"
"v_mfma_f32_16x16x32_bf16 v[84:87], a[232:235], v[72:75], v[84:87]\n"
"ds_read_b128 v[100:103], v5 offset:2048\n"
"ds_read_b128 v[104:107], v6 offset:2048\n"
"v_mfma_f32_16x16x32_bf16 v[116:119], a[236:239], v[72:75], v[116:119]\n"
"buffer_load_dwordx4 v[72:75], v0, s[4:7], s99 offen offset:1024 sc1\n"
"v_mfma_f32_16x16x32_bf16 v[108:111], a[240:243], v[148:151], v[108:111]\n"
"ds_read_b128 v[120:123], v5 offset:3072\n"
"ds_read_b128 v[124:127], v6 offset:3072\n"
"v_mfma_f32_16x16x32_bf16 v[112:115], a[244:247], v[148:151], v[112:115]\n"
"v_mfma_f32_16x16x32_bf16 v[84:87], a[248:251], v[148:151], v[84:87]\n"
"buffer_load_dwordx4 v[128:131], v0, s[4:7], s99 offen offset:2048 sc1\n"
"v_mfma_f32_16x16x32_bf16 v[116:119], a[252:255], v[148:151], v[116:119]\n"
"s_waitcnt lgkmcnt(4)\n"
"v_add_f32_e32 v76, v76, v80\n"
"v_add_f32_e32 v77, v77, v81\n"
"v_add_f32_e32 v78, v78, v82\n"
"v_add_f32_e32 v79, v79, v83\n"
"v_add_f32_e32 v92, v92, v96\n"
"v_add_f32_e32 v93, v93, v97\n"
"v_add_f32_e32 v94, v94, v98\n"
"v_add_f32_e32 v95, v95, v99\n"
"buffer_load_dwordx4 v[80:83], v0, s[4:7], s99 offen offset:3072 sc1\n"
"s_waitcnt lgkmcnt(0)\n"
"v_add_f32_e32 v100, v100, v104\n"
"v_add_f32_e32 v101, v101, v105\n"
"v_add_f32_e32 v102, v102, v106\n"
"v_add_f32_e32 v103, v103, v107\n"
"v_add_f32_e32 v120, v120, v124\n"
"v_add_f32_e32 v121, v121, v125\n"
"v_add_f32_e32 v122, v122, v126\n"
"v_add_f32_e32 v123, v123, v127\n"
"buffer_load_dwordx4 v[96:99], v1, s[4:7], s99 offen sc1\n"
"buffer_load_dwordx4 v[104:107], v1, s[4:7], s99 offen offset:1024 sc1\n"
"buffer_load_dwordx4 v[124:127], v1, s[4:7], s99 offen offset:2048 sc1\n"
"buffer_load_dwordx4 v[132:135], v1, s[4:7], s99 offen offset:3072 sc1\n"
"s_waitcnt vmcnt(15)\n"
"; @B G2 a.0\n"
"v_mfma_f32_16x16x32_bf16 v[136:139], a[128:131], v[24:27], 0\n"
"s_add_u32 s99, s58, 0x400000\n"
"v_mfma_f32_16x16x32_bf16 v[140:143], a[132:135], v[24:27], 0\n"
"buffer_load_dwordx4 v[144:147], v0, s[4:7], s99 offen sc1\n"
"v_mfma_f32_16x16x32_bf16 v[148:151], a[136:139], v[24:27], 0\n"
"v_add_f32_e32 v76, v108, v76\n"
"v_add_f32_e32 v77, v109, v77\n"
"v_mfma_f32_16x16x32_bf16 v[152:155], a[140:143], v[24:27], 0\n"
"s_waitcnt vmcnt(15)\n"
"v_add_f32_e32 v78, v110, v78\n"
"v_mfma_f32_16x16x32_bf16 v[136:139], a[144:147], v[28:31], v[136:139]\n"
"buffer_load_dwordx4 v[24:27], v0, s[4:7], s99 offen offset:1024 sc1\n"
"v_mfma_f32_16x16x32_bf16 v[140:143], a[148:151], v[28:31], v[140:143]\n"
"v_add_f32_e32 v79, v111, v79\n"
"v_add_f32_e32 v92, v112, v92\n"
"v_mfma_f32_16x16x32_bf16 v[148:151], a[152:155], v[28:31], v[148:151]\n"
"v_add_f32_e32 v93, v113, v93\n"
"v_add_f32_e32 v94, v114, v94\n"
"v_mfma_f32_16x16x32_bf16 v[152:155], a[156:159], v[28:31], v[152:155]\n"
"s_waitcnt vmcnt(15)\n"
"v_add_f32_e32 v95, v115, v95\n"
"v_mfma_f32_16x16x32_bf16 v[136:139], a[160:163], v[32:35], v[136:139]\n"
"buffer_load_dwordx4 v[28:31], v0, s[4:7], s99 offen offset:2048 sc1\n"
"v_mfma_f32_16x16x32_bf16 v[140:143], a[164:167], v[32:35], v[140:143]\n"
"v_cvt_pk_bf16_f32 v108, v76, v77\n"
"v_cvt_pk_bf16_f32 v109, v78, v79\n"
"v_mfma_f32_16x16x32_bf16 v[148:151], a[168:171], v[32:35], v[148:151]\n"
"v_cvt_pk_bf16_f32 v110, v92, v93\n"
"v_cvt_pk_bf16_f32 v111, v94, v95\n"
"v_mfma_f32_16x16x32_bf16 v[152:155], a[172:175], v[32:35], v[152:155]\n"
"s_waitcnt vmcnt(15)\n"
"v_add_f32_e32 v100, v84, v100\n"
"v_mfma_f32_16x16x32_bf16 v[136:139], a[176:179], v[36:39], v[136:139]\n"
"buffer_load_dwordx4 v[32:35], v0, s[4:7], s99 offen offset:3072 sc1\n"
"v_mfma_f32_16x16x32_bf16 v[140:143], a[180:183], v[36:39], v[140:143]\n"
"v_add_f32_e32 v101, v85, v101\n"
"v_add_f32_e32 v102, v86, v102\n"
"v_mfma_f32_16x16x32_bf16 v[148:151], a[184:187], v[36:39], v[148:151]\n"
"v_add_f32_e32 v103, v87, v103\n"
"v_add_f32_e32 v120, v116, v120\n"
"v_mfma_f32_16x16x32_bf16 v[152:155], a[188:191], v[36:39], v[152:155]\n"
"s_waitcnt vmcnt(15)\n"
"v_add_f32_e32 v121, v117, v121\n"
"v_mfma_f32_16x16x32_bf16 v[136:139], a[192:195], v[40:43], v[136:139]\n"
"buffer_load_dwordx4 v[36:39], v1, s[4:7], s99 offen sc1\n"
"v_mfma_f32_16x16x32_bf16 v[140:143], a[196:199], v[40:43], v[140:143]\n"
"v_add_f32_e32 v122, v118, v122\n"
"v_add_f32_e32 v123, v119, v123\n"
"v_mfma_f32_16x16x32_bf16 v[148:151], a[200:203], v[40:43], v[148:151]\n"
"v_cvt_pk_bf16_f32 v76, v100, v101\n"
"v_cvt_pk_bf16_f32 v77, v102, v103\n"
"v_mfma_f32_16x16x32_bf16 v[152:155], a[204:207], v[40:43], v[152:155]\n"
"s_waitcnt vmcnt(15)\n"
"v_cvt_pk_bf16_f32 v78, v120, v121\n"
"v_mfma_f32_16x16x32_bf16 v[136:139], a[208:211], v[60:63], v[136:139]\n"
"buffer_load_dwordx4 v[40:43], v1, s[4:7], s99 offen offset:1024 sc1\n"
"v_mfma_f32_16x16x32_bf16 v[140:143], a[212:215], v[60:63], v[140:143]\n"
"v_cvt_pk_bf16_f32 v79, v122, v123\n"
"v_mfma_f32_16x16x32_bf16 v[148:151], a[216:219], v[60:63], v[148:151]\n"
"v_permlane16_swap_b32_e32 v108, v110\n"
"v_permlane16_swap_b32_e32 v109, v111\n"
"v_mfma_f32_16x16x32_bf16 v[152:155], a[220:223], v[60:63], v[152:155]\n"
"s_waitcnt vmcnt(15)\n"
"v_permlane16_swap_b32_e32 v76, v78\n"
"v_mfma_f32_16x16x32_bf16 v[136:139], a[224:227], v[64:67], v[136:139]\n"
"buffer_load_dwordx4 v[60:63], v1, s[4:7], s99 offen offset:2048 sc1\n"
"v_mfma_f32_16x16x32_bf16 v[140:143], a[228:231], v[64:67], v[140:143]\n"
"v_permlane16_swap_b32_e32 v77, v79\n"
"v_mfma_f32_16x16x32_bf16 v[148:151], a[232:235], v[64:67], v[148:151]\n"
"v_mfma_f32_16x16x32_bf16 v[152:155], a[236:239], v[64:67], v[152:155]\n"
"s_waitcnt vmcnt(15)\n"
"v_mfma_f32_16x16x32_bf16 v[136:139], a[240:243], v[68:71], v[136:139]\n"
"buffer_load_dwordx4 v[64:67], v1, s[4:7], s99 offen offset:3072 sc1\n"
"v_mfma_f32_16x16x32_bf16 v[140:143], a[244:247], v[68:71], v[140:143]\n"
"s_add_u32 s99, s59, 0x400000\n"
"v_mfma_f32_16x16x32_bf16 v[148:151], a[248:251], v[68:71], v[148:151]\n"
"v_mfma_f32_16x16x32_bf16 v[152:155], a[252:255], v[68:71], v[152:155]\n"
"s_waitcnt vmcnt(14)\n"
"; @B G2 a.1\n"
"v_mfma_f32_16x16x32_bf16 v[68:71], a[128:131], v[88:91], 0\n"
"s_nop 0\n"
"v_cmp_u_f32_e32 vcc, v136, v136\n"
"v_mfma_f32_16x16x32_bf16 v[84:87], a[132:135], v[88:91], 0\n"
"s_cbranch_vccnz .Lmk_p66s\n"
".Lmk_p66r:\n"
"v_mfma_f32_16x16x32_bf16 v[92:95], a[136:139], v[88:91], 0\n"
"buffer_load_dwordx4 v[100:103], v0, s[4:7], s99 offen sc1\n"
"v_mfma_f32_16x16x32_bf16 v[112:115], a[140:143], v[88:91], 0\n"
"v_mfma_f32_16x16x32_bf16 v[68:71], a[144:147], v[72:75], v[68:71]\n"
"ds_write_b128 v3, v[136:139]\n"
"v_mfma_f32_16x16x32_bf16 v[84:87], a[148:151], v[72:75], v[84:87]\n"
"buffer_load_dwordx4 v[88:91], v0, s[4:7], s99 offen offset:1024 sc1\n"
"v_mfma_f32_16x16x32_bf16 v[92:95], a[152:155], v[72:75], v[92:95]\n"
"ds_write_b128 v3, v[140:143] offset:1024\n"
"v_mfma_f32_16x16x32_bf16 v[112:115], a[156:159], v[72:75], v[112:115]\n"
"s_waitcnt vmcnt(14)\n"
"v_mfma_f32_16x16x32_bf16 v[68:71], a[160:163], v[128:131], v[68:71]\n"
"ds_write_b128 v3, v[148:151] offset:2048\n"
"v_mfma_f32_16x16x32_bf16 v[84:87], a[164:167], v[128:131], v[84:87]\n"
"buffer_load_dwordx4 v[72:75], v0, s[4:7], s99 offen offset:2048 sc1\n"
"v_mfma_f32_16x16x32_bf16 v[92:95], a[168:171], v[128:131], v[92:95]\n"
"ds_write_b128 v3, v[152:155] offset:3072\n"
"v_mfma_f32_16x16x32_bf16 v[112:115], a[172:175], v[128:131], v[112:115]\n"
"v_mfma_f32_16x16x32_bf16 v[68:71], a[176:179], v[80:83], v[68:71]\n"
"buffer_load_dwordx4 v[116:119], v0, s[4:7], s99 offen offset:3072 sc1\n"
"v_mfma_f32_16x16x32_bf16 v[84:87], a[180:183], v[80:83], v[84:87]\n"
"ds_read_b128 v[120:123], v2\n"
"ds_read_b128 v[128:131], v2 offset:1024\n"
"v_mfma_f32_16x16x32_bf16 v[92:95], a[184:187], v[80:83], v[92:95]\n"
"v_mfma_f32_16x16x32_bf16 v[112:115], a[188:191], v[80:83], v[112:115]\n"
"s_waitcnt vmcnt(14)\n"
"v_mfma_f32_16x16x32_bf16 v[68:71], a[192:195], v[96:99], v[68:71]\n"
"buffer_load_dwordx4 v[80:83], v1, s[4:7], s99 offen sc1\n"
"v_mfma_f32_16x16x32_bf16 v[84:87], a[196:199], v[96:99], v[84:87]\n"
"v_mfma_f32_16x16x32_bf16 v[92:95], a[200:203], v[96:99], v[92:95]\n"
"s_waitcnt lgkmcnt(0)\n"
"v_lshlrev_b32_e32 v255, 16, v120\n"
"v_mfma_f32_16x16x32_bf16 v[112:115], a[204:207], v[96:99], v[112:115]\n"
"s_waitcnt lgkmcnt(0)\n"
"s_barrier\n"
"v_mfma_f32_16x16x32_bf16 v[68:71], a[208:211], v[104:107], v[68:71]\n"
"buffer_load_dwordx4 v[96:99], v1, s[4:7], s99 offen offset:1024 sc1\n"
"v_mfma_f32_16x16x32_bf16 v[84:87], a[212:215], v[104:107], v[84:87]\n"
"ds_read_b128 v[136:139], v4\n"
"ds_read_b128 v[140:143], v4 offset:1024\n"
"v_mfma_f32_16x16x32_bf16 v[92:95], a[216:219], v[104:107], v[92:95]\n"
"ds_read_b128 v[148:151], v4 offset:2048\n"
"ds_read_b128 v[152:155], v4 offset:3072\n"
"v_mfma_f32_16x16x32_bf16 v[112:115], a[220:223], v[104:107], v[112:115]\n"
"s_waitcnt vmcnt(14)\n"
"v_and_b32_e32 v254, 0xffff0000, v120\n"
"v_mfma_f32_16x16x32_bf16 v[68:71], a[224:227], v[124:127], v[68:71]\n"
"buffer_load_dwordx4 v[104:107], v1, s[4:7], s99 offen offset:2048 sc1\n"
"v_mfma_f32_16x16x32_bf16 v[84:87], a[228:231], v[124:127], v[84:87]\n"
"v_lshlrev_b32_e32 v253, 16, v121\n"
"v_and_b32_e32 v252, 0xffff0000, v121\n"
"v_mfma_f32_16x16x32_bf16 v[92:95], a[232:235], v[124:127], v[92:95]\n"
"v_lshlrev_b32_e32 v251, 16, v122\n"
"v_and_b32_e32 v250, 0xffff0000, v122\n"
"v_mfma_f32_16x16x32_bf16 v[112:115], a[236:239], v[124:127], v[112:115]\n"
"buffer_load_dwordx4 v[124:127], v1, s[4:7], s99 offen offset:3072 sc1\n"
"v_mfma_f32_16x16x32_bf16 v[68:71], a[240:243], v[132:135], v[68:71]\n"
"s_add_u32 s99, s74, 0x40000\n"
"v_lshlrev_b32_e32 v249, 16, v123\n"
"v_mfma_f32_16x16x32_bf16 v[84:87], a[244:247], v[132:135], v[84:87]\n"
"buffer_store_dwordx4 v[108:111], v20, s[16:19], s99 offen\n"
"v_mfma_f32_16x16x32_bf16 v[92:95], a[248:251], v[132:135], v[92:95]\n"
"v_and_b32_e32 v248, 0xffff0000, v123\n"
"v_lshlrev_b32_e32 v247, 16, v128\n"
"v_mfma_f32_16x16x32_bf16 v[112:115], a[252:255], v[132:135], v[112:115]\n"
"s_waitcnt vmcnt(15)\n"
"v_and_b32_e32 v246, 0xffff0000, v128\n"
"; @B G2 a.2\n"
"v_mfma_f32_16x16x32_bf16 v[108:111], a[128:131], v[144:147], 0\n"
"v_cmp_u_f32_e32 vcc, v68, v68\n"
"v_lshlrev_b32_e32 v245, 16, v129\n"
"v_mfma_f32_16x16x32_bf16 v[120:123], a[132:135], v[144:147], 0\n"
"s_cbranch_vccnz .Lmk_p67s\n"
".Lmk_p67r:\n"
"v_and_b32_e32 v244, 0xffff0000, v129\n"
"v_mfma_f32_16x16x32_bf16 v[132:135], a[136:139], v[144:147], 0\n"
"v_lshlrev_b32_e32 v243, 16, v130\n"
"v_and_b32_e32 v242, 0xffff0000, v130\n"
"v_mfma_f32_16x16x32_bf16 v[156:159], a[140:143], v[144:147], 0\n"
"v_lshlrev_b32_e32 v241, 16, v131\n"
"v_and_b32_e32 v240, 0xffff0000, v131\n"
"v_mfma_f32_16x16x32_bf16 v[108:111], a[144:147], v[24:27], v[108:111]\n"
"ds_write_b128 v3, v[68:71] offset:6144\n"
"v_mfma_f32_16x16x32_bf16 v[120:123], a[148:151], v[24:27], v[120:123]\n"
"s_waitcnt lgkmcnt(1)\n"
"v_add_f32_e32 v136, v136, v255\n"
"v_mfma_f32_16x16x32_bf16 v[132:135], a[152:155], v[24:27], v[132:135]\n"
"ds_write_b128 v3, v[84:87] offset:7168\n"
"v_mfma_f32_16x16x32_bf16 v[156:159], a[156:159], v[24:27], v[156:159]\n"
"s_waitcnt vmcnt(13)\n"
"s_add_u32 s99, s74, 0x40000\n"
"v_mfma_f32_16x16x32_bf16 v[108:111], a[160:163], v[28:31], v[108:111]\n"
"ds_write_b128 v3, v[92:95] offset:8192\n"
"v_mfma_f32_16x16x32_bf16 v[120:123], a[164:167], v[28:31], v[120:123]\n"
"buffer_store_dwordx4 v[76:79], v20, s[16:19], s99 offen offset:64\n"
"v_mfma_f32_16x16x32_bf16 v[132:135], a[168:171], v[28:31], v[132:135]\n"
"ds_write_b128 v3, v[112:115] offset:9216\n"
"v_mfma_f32_16x16x32_bf16 v[156:159], a[172:175], v[28:31], v[156:159]\n"
"v_add_f32_e32 v137, v137, v254\n"
"v_add_f32_e32 v138, v138, v253\n"
"v_mfma_f32_16x16x32_bf16 v[108:111], a[176:179], v[32:35], v[108:111]\n"
"v_add_f32_e32 v139, v139, v252\n"
"v_add_f32_e32 v140, v140, v251\n"
"v_mfma_f32_16x16x32_bf16 v[120:123], a[180:183], v[32:35], v[120:123]\n"
"v_add_f32_e32 v141, v141, v250\n"
"v_add_f32_e32 v142, v142, v249\n"
"v_mfma_f32_16x16x32_bf16 v[132:135], a[184:187], v[32:35], v[132:135]\n"
"v_add_f32_e32 v143, v143, v248\n"
"v_add_f32_e32 v148, v148, v247\n"
"v_mfma_f32_16x16x32_bf16 v[156:159], a[188:191], v[32:35], v[156:159]\n"
"s_waitcnt vmcnt(12)\n"
"v_add_f32_e32 v149, v149, v246\n"
"v_mfma_f32_16x16x32_bf16 v[108:111], a[192:195], v[36:39], v[108:111]\n"
"v_add_f32_e32 v150, v150, v245\n"
"v_add_f32_e32 v151, v151, v244\n"
"v_mfma_f32_16x16x32_bf16 v[120:123], a[196:199], v[36:39], v[120:123]\n"
"v_add_f32_e32 v152, v152, v243\n"
"v_add_f32_e32 v153, v153, v242\n"
"v_mfma_f32_16x16x32_bf16 v[132:135], a[200:203], v[36:39], v[132:135]\n"
"v_add_f32_e32 v154, v154, v241\n"
"v_add_f32_e32 v155, v155, v240\n"
"v_mfma_f32_16x16x32_bf16 v[156:159], a[204:207], v[36:39], v[156:159]\n"
"v_mfma_f32_16x16x32_bf16 v[108:111], a[208:211], v[40:43], v[108:111]\n"
"v_mfma_f32_16x16x32_bf16 v[120:123], a[212:215], v[40:43], v[120:123]\n"
"v_mfma_f32_16x16x32_bf16 v[132:135], a[216:219], v[40:43], v[132:135]\n"
"v_mfma_f32_16x16x32_bf16 v[156:159], a[220:223], v[40:43], v[156:159]\n"
"s_waitcnt vmcnt(10)\n"
"v_mfma_f32_16x16x32_bf16 v[108:111], a[224:227], v[60:63], v[108:111]\n"
"s_waitcnt vmcnt(8)\n"
"v_max_u32_e32 v255, v100, v101\n"
"v_mfma_f32_16x16x32_bf16 v[120:123], a[228:231], v[60:63], v[120:123]\n"
"v_max3_u32 v255, v255, v102, v103\n"
"v_max3_u32 v255, v255, v88, v89\n"
"v_mfma_f32_16x16x32_bf16 v[132:135], a[232:235], v[60:63], v[132:135]\n"
"v_max3_u32 v255, v255, v90, v91\n"
"s_waitcnt vmcnt(6)\n"
"v_mfma_f32_16x16x32_bf16 v[156:159], a[236:239], v[60:63], v[156:159]\n"
"v_max3_u32 v255, v255, v72, v73\n"
"v_max3_u32 v255, v255, v74, v75\n"
"v_mfma_f32_16x16x32_bf16 v[108:111], a[240:243], v[64:67], v[108:111]\n"
"v_max3_u32 v255, v255, v116, v117\n"
"v_max3_u32 v255, v255, v118, v119\n"
"v_mfma_f32_16x16x32_bf16 v[120:123], a[244:247], v[64:67], v[120:123]\n"
"v_cmp_eq_u32_e32 vcc, -1, v255\n"
"v_mfma_f32_16x16x32_bf16 v[132:135], a[248:251], v[64:67], v[132:135]\n"
"s_cbranch_vccnz .Lmk_q69s\n"
".Lmk_q69r:\n"
"v_mfma_f32_16x16x32_bf16 v[156:159], a[252:255], v[64:67], v[156:159]\n"
"; @B G2 a.3\n"
"v_mfma_f32_16x16x32_bf16 v[136:139], a[128:131], v[100:103], v[136:139]\n"
"v_cmp_u_f32_e32 vcc, v108, v108\n"
"v_mfma_f32_16x16x32_bf16 v[140:143], a[132:135], v[100:103], v[140:143]\n"
"s_cbranch_vccnz .Lmk_p68s\n"
".Lmk_p68r:\n"
"v_mfma_f32_16x16x32_bf16 v[148:151], a[136:139], v[100:103], v[148:151]\n"
"v_mfma_f32_16x16x32_bf16 v[152:155], a[140:143], v[100:103], v[152:155]\n"
"v_mfma_f32_16x16x32_bf16 v[136:139], a[144:147], v[88:91], v[136:139]\n"
"ds_write_b128 v3, v[108:111] offset:12288\n"
"v_mfma_f32_16x16x32_bf16 v[140:143], a[148:151], v[88:91], v[140:143]\n"
"v_mfma_f32_16x16x32_bf16 v[148:151], a[152:155], v[88:91], v[148:151]\n"
"ds_write_b128 v3, v[120:123] offset:13312\n"
"v_mfma_f32_16x16x32_bf16 v[152:155], a[156:159], v[88:91], v[152:155]\n"
"v_mfma_f32_16x16x32_bf16 v[136:139], a[160:163], v[72:75], v[136:139]\n"
"s_waitcnt vmcnt(4)\n"
"v_max_u32_e32 v255, v80, v81\n"
"v_mfma_f32_16x16x32_bf16 v[140:143], a[164:167], v[72:75], v[140:143]\n"
"v_max3_u32 v255, v255, v82, v83\n"
"v_max3_u32 v255, v255, v96, v97\n"
"v_mfma_f32_16x16x32_bf16 v[148:151], a[168:171], v[72:75], v[148:151]\n"
"v_max3_u32 v255, v255, v98, v99\n"
"s_waitcnt vmcnt(2)\n"
"v_mfma_f32_16x16x32_bf16 v[152:155], a[172:175], v[72:75], v[152:155]\n"
"v_max3_u32 v255, v255, v104, v105\n"
"v_max3_u32 v255, v255, v106, v107\n"
"v_mfma_f32_16x16x32_bf16 v[136:139], a[176:179], v[116:119], v[136:139]\n"
"v_max3_u32 v255, v255, v124, v125\n"
"v_max3_u32 v255, v255, v126, v127\n"
"v_mfma_f32_16x16x32_bf16 v[140:143], a[180:183], v[116:119], v[140:143]\n"
"v_cmp_eq_u32_e32 vcc, -1, v255\n"
"v_mfma_f32_16x16x32_bf16 v[148:151], a[184:187], v[116:119], v[148:151]\n"
"s_cbranch_vccnz .Lmk_q70s\n"
".Lmk_q70r:\n"
"v_mfma_f32_16x16x32_bf16 v[152:155], a[188:191], v[116:119], v[152:155]\n"
"v_mfma_f32_16x16x32_bf16 v[136:139], a[192:195], v[80:83], v[136:139]\n"
"ds_write_b128 v3, v[132:135] offset:14336\n"
"v_mfma_f32_16x16x32_bf16 v[140:143], a[196:199], v[80:83], v[140:143]\n"
"v_mfma_f32_16x16x32_bf16 v[148:151], a[200:203], v[80:83], v[148:151]\n"
"ds_write_b128 v3, v[156:159] offset:15360\n"
"v_mfma_f32_16x16x32_bf16 v[152:155], a[204:207], v[80:83], v[152:155]\n"
"v_mfma_f32_16x16x32_bf16 v[136:139], a[208:211], v[96:99], v[136:139]\n"
"v_mfma_f32_16x16x32_bf16 v[140:143], a[212:215], v[96:99], v[140:143]\n"
"s_waitcnt lgkmcnt(0)\n"
"s_barrier\n"
"v_mfma_f32_16x16x32_bf16 v[148:151], a[216:219], v[96:99], v[148:151]\n"
"ds_read_b128 v[24:27], v5\n"
"ds_read_b128 v[28:31], v6\n"
"v_mfma_f32_16x16x32_bf16 v[152:155], a[220:223], v[96:99], v[152:155]\n"
"ds_read_b128 v[32:35], v5 offset:1024\n"
"v_mfma_f32_16x16x32_bf16 v[136:139], a[224:227], v[104:107], v[136:139]\n"
"ds_read_b128 v[36:39], v6 offset:1024\n"
"v_mfma_f32_16x16x32_bf16 v[140:143], a[228:231], v[104:107], v[140:143]\n"
"ds_read_b128 v[40:43], v5 offset:2048\n"
"ds_read_b128 v[60:63], v6 offset:2048\n"
"v_mfma_f32_16x16x32_bf16 v[148:151], a[232:235], v[104:107], v[148:151]\n"
"ds_read_b128 v[64:67], v5 offset:3072\n"
"ds_read_b128 v[68:71], v6 offset:3072\n"
"v_mfma_f32_16x16x32_bf16 v[152:155], a[236:239], v[104:107], v[152:155]\n"
"v_mfma_f32_16x16x32_bf16 v[136:139], a[240:243], v[124:127], v[136:139]\n"
"v_mfma_f32_16x16x32_bf16 v[140:143], a[244:247], v[124:127], v[140:143]\n"
"s_waitcnt lgkmcnt(4)\n"
"v_add_f32_e32 v24, v24, v28\n"
"v_mfma_f32_16x16x32_bf16 v[148:151], a[248:251], v[124:127], v[148:151]\n"
"v_add_f32_e32 v25, v25, v29\n"
"v_add_f32_e32 v26, v26, v30\n"
"v_mfma_f32_16x16x32_bf16 v[152:155], a[252:255], v[124:127], v[152:155]\n"
"v_add_f32_e32 v27, v27, v31\n"
"v_add_f32_e32 v32, v32, v36\n"
"v_add_f32_e32 v33, v33, v37\n"
"v_add_f32_e32 v34, v34, v38\n"
"v_add_f32_e32 v35, v35, v39\n"
"s_waitcnt lgkmcnt(0)\n"
"v_add_f32_e32 v40, v40, v60\n"
"v_add_f32_e32 v41, v41, v61\n"
"v_add_f32_e32 v42, v42, v62\n"
"v_add_f32_e32 v43, v43, v63\n"
"v_add_f32_e32 v64, v64, v68\n"
"v_add_f32_e32 v65, v65, v69\n"
"v_add_f32_e32 v66, v66, v70\n"
"v_add_f32_e32 v67, v67, v71\n"
"v_add_f32_e32 v24, v136, v24\n"
"v_add_f32_e32 v25, v137, v25\n"
"v_add_f32_e32 v26, v138, v26\n"
"v_add_f32_e32 v27, v139, v27\n"
"v_add_f32_e32 v32, v140, v32\n"
"v_add_f32_e32 v33, v141, v33\n"
"v_add_f32_e32 v34, v142, v34\n"
"v_add_f32_e32 v35, v143, v35\n"
"v_cvt_pk_bf16_f32 v28, v24, v25\n"
"v_cvt_pk_bf16_f32 v29, v26, v27\n"
"v_cvt_pk_bf16_f32 v30, v32, v33\n"
"v_cvt_pk_bf16_f32 v31, v34, v35\n"
"v_add_f32_e32 v40, v148, v40\n"
"v_add_f32_e32 v41, v149, v41\n"
"v_add_f32_e32 v42, v150, v42\n"
"v_add_f32_e32 v43, v151, v43\n"
"v_add_f32_e32 v64, v152, v64\n"
"v_add_f32_e32 v65, v153, v65\n"
"v_add_f32_e32 v66, v154, v66\n"
"v_add_f32_e32 v67, v155, v67\n"
"v_cvt_pk_bf16_f32 v24, v40, v41\n"
"v_cvt_pk_bf16_f32 v25, v42, v43\n"
"v_cvt_pk_bf16_f32 v26, v64, v65\n"
"v_cvt_pk_bf16_f32 v27, v66, v67\n"
"v_permlane16_swap_b32_e32 v28, v30\n"
"v_permlane16_swap_b32_e32 v29, v31\n"
"buffer_store_dwordx4 v[28:31], v20, s[16:19], s74 offen\n"
"v_permlane16_swap_b32_e32 v24, v26\n"
"v_permlane16_swap_b32_e32 v25, v27\n"
"buffer_store_dwordx4 v[24:27], v20, s[16:19], s74 offen offset:64\n"
"s_cmp_eq_u32 s81, 0\n"
"s_cbranch_scc1 .Lmk_fin\n"
"buffer_store_dwordx2 v[18:19], v11, s[4:7], s81 offen sc1\n"
".Lmk_fin:\n"
"s_endpgm\n"
".Lmk_end:\n"
    :: "{s[4:7]}"(wsr), "{s[8:11]}"(wsr), "{s[12:15]}"(wsk), "{s[16:19]}"(outr), "{s[52:67]}"(sa), "{s[68:83]}"(sb),
       "{s[20:27]}"(pa), "{s[28:31]}"(wa), "{s[32:35]}"(wb), "{s[36:39]}"(oa), "{s[40:43]}"(ob), "{s[44:51]}"(pc),
       "{v[0:15]}"(va), "{v[16:23]}"(vb), "{v[32:47]}"(pz), "{v[48:49]}"(sta), "{v50}"(pf)
    : "memory", "v255", "a255", "s99", "vcc");
  __builtin_unreachable();
}

}

extern "C" void kernel_launch(void* const* d_in, const int* in_sizes, int n_in, void* d_out, int out_size,
                              void* d_ws, size_t ws_size, hipStream_t stream) {
  (void)hipFuncSetAttribute((const void*)mega_kernel, hipFuncAttributeMaxDynamicSharedMemorySize, LDS_BYTES);
  mega_kernel<<<256, 256, LDS_BYTES, stream>>>((const u16*)d_in[0], (const u16*)d_in[1], (const u16*)d_in[2],
                                               (const u16*)d_in[4], (const u16*)d_in[5], (u16*)d_out, (char*)d_ws);
}
```

```cpp
#include <hip/hip_runtime.h>
#include <stdint.h>

namespace {

typedef unsigned short u16;
typedef unsigned long long u64;
typedef short s16x4 __attribute__((ext_vector_type(4)));
typedef float f32x4 __attribute__((ext_vector_type(4)));
typedef unsigned u32x4 __attribute__((ext_vector_type(4)));
typedef unsigned u32x2 __attribute__((ext_vector_type(2)));
typedef unsigned u32x8 __attribute__((ext_vector_type(8)));
typedef unsigned u32x16 __attribute__((ext_vector_type(16)));
typedef __amdgpu_buffer_rsrc_t rsrc_t;
#define LAS __attribute__((address_space(3)))
#define DI __device__ __forceinline__
#define LDSP(a) ((LAS char*)(size_t)(a))

constexpr unsigned MB = 1u << 20;
constexpr unsigned WS_XF = 0;
constexpr unsigned WS_OF = 16 * MB;
constexpr unsigned WS_KV = 32 * MB;
constexpr unsigned WS_WF = 64 * MB;
constexpr unsigned WS_WO = 76 * MB;
constexpr unsigned WS_ST = 80 * MB;
constexpr unsigned WS_CS = 81 * MB;
constexpr unsigned WS_FL = 82 * MB;
constexpr unsigned WS_EP = WS_FL + 2048 * 128;
constexpr unsigned WS_DBG = 96 * MB;
constexpr unsigned OOB = 0x80000000u;
constexpr int L_RES = 0, L_K = 16384, L_V = 49152, L_INB = 81920, L_CS = 155648, L_PM = L_CS + 1024, L_MB = L_PM + 2048, L_SCR = L_MB + 2048;
constexpr int L_WS = 57344;
constexpr int LDS_BYTES = 163840;

template <int N> DI void wait_vm() { asm volatile("s_waitcnt vmcnt(%0)" ::"n"(N) : "memory"); }
DI void lds_barrier() { asm volatile("s_waitcnt lgkmcnt(0)\n\ts_barrier" ::: "memory"); }
template <int N> DI void nops() { asm volatile("s_nop %0" ::"n"(N - 1)); }
template <int OFF> DI void ldv(u32x4& d, rsrc_t r, unsigned v, unsigned s) {
  asm volatile("buffer_load_dwordx4 %0, %1, %2, %3 offen offset:%4 sc1" : "=v"(d) : "v"(v), "s"(r), "s"(s), "i"(OFF) : "memory");
}
template <int OFF> DI void ld8(u32x2& d, rsrc_t r, unsigned v, unsigned s) {
  asm volatile("buffer_load_dwordx2 %0, %1, %2, %3 offen offset:%4 sc1" : "=v"(d) : "v"(v), "s"(r), "s"(s), "i"(OFF) : "memory");
}
template <int OFF> DI void st16(const u32x4& d, rsrc_t r, unsigned v, unsigned s) {
  asm volatile("buffer_store_dwordx4 %0, %1, %2, %3 offen offset:%4 sc1\n\ts_nop 1" :: "v"(d), "v"(v), "s"(r), "s"(s), "i"(OFF) : "memory");
}
template <int OFF> DI void st8(const u32x2& d, rsrc_t r, unsigned v, unsigned s) {
  asm volatile("buffer_store_dwordx2 %0, %1, %2, %3 offen offset:%4 sc1" :: "v"(d), "v"(v), "s"(r), "s"(s), "i"(OFF) : "memory");
}
DI void dma(rsrc_t r, unsigned v, unsigned s, unsigned lm) {
  asm volatile("s_mov_b32 m0, %3\n\ts_nop 0\n\tbuffer_load_dwordx4 %0, %1, %2 offen lds" :: "v"(v), "s"(r), "s"(s), "s"(lm) : "memory");
}
DI void touch(rsrc_t r, unsigned v) {
  asm volatile("s_mov_b32 m0, %2\n\ts_nop 0\n\tbuffer_load_dword %0, %1, 0 offen lds" :: "v"(v), "s"(r), "s"((unsigned)L_SCR) : "memory");
}
DI void mma_v(f32x4& c, const u32x4& a, const u32x4& b) { asm volatile("v_mfma_f32_16x16x32_bf16 %0, %1, %2, %0" : "+v"(c) : "v"(a), "v"(b)); }
DI void mm0_v(f32x4& c, const u32x4& a, const u32x4& b) { asm volatile("v_mfma_f32_16x16x32_bf16 %0, %1, %2, 0" : "=v"(c) : "v"(a), "v"(b)); }
DI unsigned uni(unsigned v) { v = __builtin_amdgcn_readfirstlane(v); asm volatile("" : "+s"(v)); return v; }
template <class T> DI void pin(T& v) { asm volatile("" : "+v"(v)); }
DI float sum_x16(float v) {
  auto r = __builtin_amdgcn_permlane16_swap(__float_as_uint(v), __float_as_uint(v), false, false);
  return __uint_as_float(r[0]) + __uint_as_float(r[1]);
}
DI float sum_x32(float v) {
  auto r = __builtin_amdgcn_permlane32_swap(__float_as_uint(v), __float_as_uint(v), false, false);
  return __uint_as_float(r[0]) + __uint_as_float(r[1]);
}
DI u32x4 swap16(const u32x4& x) {
  auto r0 = __builtin_amdgcn_permlane16_swap(x[0], x[2], false, false);
  auto r1 = __builtin_amdgcn_permlane16_swap(x[1], x[3], false, false);
  return (u32x4){(unsigned)r0[0], (unsigned)r1[0], (unsigned)r0[1], (unsigned)r1[1]};
}
DI u32x2 lds_tr(unsigned a) { return __builtin_bit_cast(u32x2, __builtin_amdgcn_ds_read_tr16_b64_v4i16((LAS s16x4*)LDSP(a))); }
DI u32x4 cat(const u32x2& a, const u32x2& b) { return __builtin_shufflevector(a, b, 0, 1, 2, 3); }
DI u32x2 lds_r2(unsigned a) { return *(LAS u32x2*)LDSP(a); }
DI void lds_w4(unsigned a, const u32x4& v) { *(LAS u32x4*)LDSP(a) = v; }
DI unsigned rot(unsigned g, unsigned sl) { return (g + (0x641u >> 4 * sl)) & 15; }
DI int swzK(int u) { return ((((u >> 1) ^ u) & 1) << 1) | (u >> 1); }

#define STAMP(i)

__global__ __launch_bounds__(256, 1) void mega_kernel(const u16* __restrict__ x0, const u16* __restrict__ WqA,
                                                      const u16* __restrict__ WoA, const u16* __restrict__ WqB,
                                                      const u16* __restrict__ WoB, u16* __restrict__ out, char* ws) {
  const unsigned bid = blockIdx.x, xcd = bid & 7, bix = bid >> 3;
  const unsigned w = __builtin_amdgcn_readfirstlane(threadIdx.x >> 6), lane = threadIdx.x & 63;
  const unsigned c = (xcd >> 2) * 4 + (bix >> 3), h = (xcd & 3) * 8 + (bix & 7), par = h & 1, g = h >> 1;
  const unsigned p2 = h >> 3 & 1, g2 = (h >> 4) * 8 + (h & 7);
  const unsigned fr = lane & 15, fq = lane >> 4;
  const unsigned dl = 0, wr = (w + dl) & 3;
  const rsrc_t wsr = __builtin_amdgcn_make_buffer_rsrc(ws, 0, 128 * MB, 0x00020000);
  unsigned stw = 0;
  STAMP(0);
  const unsigned l16 = lane * 16, lw = w * 2048 + l16;
  const unsigned fl0 = lane == 0 ? 0u : OOB;
  const unsigned myfl = WS_FL + (c * 64 + h) * 128 + w * 8;
  const unsigned xo = (fr * 1024 + (((fq & 1) << 4) | ((fq >> 1) << 3))) * 2;
  u64 pc_; unsigned len_;
  asm volatile("s_getpc_b64 %0\n.Lmk_here:\n\ts_mov_b32 %1, .Lmk_end-.Lmk_here" : "=s"(pc_), "=s"(len_));
  const rsrc_t cr = __builtin_amdgcn_make_buffer_rsrc((void*)(pc_ & ~15ull), 0, len_ & ~15u, 0x00020000);
  const rsrc_t xr = __builtin_amdgcn_make_buffer_rsrc((void*)x0, 0, 4 * MB, 0x00020000);
  const unsigned osrc0 = (lane >> 3) * 2048 + (lane & 7) * 16;
  auto get_x = [&](int s) __attribute__((always_inline)) {
#pragma unroll
    for (int i = 0; i < 2; ++i) dma(xr, osrc0, ((c * 16 + s * 8 + 2 * wr + p2) * 16 + i * 8) * 2048 + g2 * 128, L_RES + s * 8192 + w * 2048 + i * 1024);
  };
  u32x2 tag;
  const rsrc_t wa = __builtin_amdgcn_make_buffer_rsrc((void*)WqA, 0, 6 * MB, 0x00020000);
  const unsigned rho = 2 * (c & 3), wbase = w * 256 * 6144 + h * 64;
  const unsigned wv = (lane >> 2) * 6144 + ((lane & 3) ^ ((lane >> 3) & 2)) * 16;
  auto piece = [&](int q, int j) __attribute__((always_inline)) {
    const unsigned k = j ^ par, src = uni(wbase + (((rho + 2 * q) & 7) + k) * 196608), dst = uni(L_WS + ((q & 1) * 8 + k * 4 + w) * 6144);
#pragma unroll
    for (int i = 0; i < 6; ++i) dma(wa, wv, src + (i & 1) * 98304 + (i >> 1) * 2048, dst + i * 1024);
  };
  const unsigned kb = 2 * c + par;
  const rsrc_t oa = __builtin_amdgcn_make_buffer_rsrc((void*)WoA, 0, 2 * MB, 0x00020000);
  const rsrc_t wb = __builtin_amdgcn_make_buffer_rsrc((void*)WqB, 0, 6 * MB, 0x00020000);
  const rsrc_t ob = __builtin_amdgcn_make_buffer_rsrc((void*)WoB, 0, 2 * MB, 0x00020000);
  const unsigned wsrc = (w * 16 + (lane >> 3)) * 6144 + (lane & 7) * 16, osrc = w * 16 * 2048 + osrc0;
  auto get_o = [&](int t) __attribute__((always_inline)) {
#pragma unroll
    for (int i = 0; i < 2; ++i) dma(t ? ob : oa, osrc, (kb * 64 + i * 8) * 2048 + rot(g, 1 + t) * 128, L_K + 24576 + t * 8192 + w * 2048 + i * 1024);
  };
  auto get_q = [&](int t) __attribute__((always_inline)) {
#pragma unroll
    for (int i = 0; i < 6; ++i)
      dma(t ? wb : wa, wsrc, (kb * 64 + (i & 1) * 8) * 6144 + (i >> 1) * 2048 + rot(g, i >> 1) * 128, (t ? L_K : L_INB) + (i >> 1) * 8192 + w * 2048 + (i & 1) * 1024);
  };
  const unsigned fr8 = fq == fr >> 2 ? fr * 8 : OOB;
  u32x4 ones = {0x3F803F80u, 0x3F803F80u, 0x3F803F80u, 0x3F803F80u};
  u32x2 sta = {};
  auto publish = [&](int s, bool now) __attribute__((always_inline)) {
    const unsigned a = L_RES + s * 8192 + w * 2048 + fr * 128 + fq * 8, T = c * 16 + 8 * s + 2 * wr + p2;
    u32x4 y0 = cat(lds_r2(a), lds_r2(a + 32)), y1 = cat(lds_r2(a + 64), lds_r2(a + 96));
    f32x4 sm, gm;
    pin(y0); pin(y1); pin(ones);
    nops<2>();
    mm0_v(sm, ones, y0); mm0_v(gm, y0, y0); mma_v(sm, ones, y1); mma_v(gm, y1, y1);
    if (now) { st16<0>(y0, wsr, l16, WS_XF + T * 32768 + g2 * 2048); st16<1024>(y1, wsr, l16, WS_XF + T * 32768 + g2 * 2048); }
    lds_w4(lw + L_RES + s * 8192, y0); lds_w4(lw + L_RES + s * 8192 + 1024, y1);
    nops<8>();
    pin(sm); pin(gm);
    const u32x2 st = {__float_as_uint(sm[0]), __float_as_uint(fr & 2 ? (fr & 1 ? gm[3] : gm[2]) : (fr & 1 ? gm[1] : gm[0]))};
    if (now) st8<0>(st, wsr, fr8, WS_ST + T * 2048 + g2 * 128); else sta = st;
  };
  const unsigned cb = c * 16 * 32768, skv = WS_KV + (c * 32 + h) * 32768;
  const unsigned soo = WS_OF + cb + wr * 65536 + h * 1024, sxo = WS_XF + cb + (2 * wr + p2) * 32768 + g2 * 2048, sso = WS_ST + (c * 16 + 2 * wr + p2) * 2048 + g2 * 128;
  auto clear = [&](int from, int to) __attribute__((always_inline)) {
    u32x4 z = {~0u, ~0u, ~0u, ~0u};
    pin(z);
#pragma unroll
    for (int k = from; k < to; ++k) {
      if (k < 8) st16<0>(z, wsr, l16, soo + (k == 4 ? 9 : k == 5 ? 8 : k - 6) * 32768);
      else if (k < 10) st16<0>(z, wsr, l16, sxo + 4 * MB + 8 * 32768 + (k - 8) * 1024);
      else st8<0>((u32x2){z[0], z[1]}, wsr, fr8, sso + 136 * 2048);
    }
  };
  dma(cr, l16, (bix * 4 + w) * 1024, L_SCR + 1024);
  get_x(1);
  get_x(0);
  ld8<0>(tag, wsr, 0u, WS_EP);
  piece(0, 0);
  touch(wsr, lane < 2 ? WS_FL + (w * 512 + c * 64 + lane * 32 + h) * 128 : OOB);
  STAMP(29);

  unsigned ps[16];
  u32x8 pv = {};
#pragma unroll
  for (int j = 0; j < 3; ++j) {
    const unsigned ct = (w & 1) * 3 + j;
    pv[1 + j] = L_K + (ct >> 1) * 8192 + (4 * fq + (fr >> 2)) * 128 + (w >> 1) * 64 + (ct & 1) * 32 + (lane & 3) * 8;
    ps[j] = WS_WF + 6 * MB + ((2 * rot(g, ct >> 1) + (w >> 1)) * 192 + kb * 12 + ct) * 1024;
    ps[10 + j] = kb * 64 * 6144 + j * 2048 + rot(g, j) * 128;
  }
  { const unsigned ct = (w & 1) * 3 + (fr >> 1);
    pv[4] = ((2 * rot(g, ct >> 1) + (w >> 1)) * 16384 + ct * 128 + (fr & 1) * 64 + fq * 16) | (fr < 6 ? 0u : OOB); }
  ps[3] = WS_CS + 524288 + kb * 1024;
  ps[6] = (c >> 2) | par << 1 | (bix * 4 + w) << 10;                                            ps[7] = L_WS + w * 6144;
  ps[8] = wbase;                                             ps[9] = rho;
  ps[13] = kb * 64 * 2048 + rot(g, 1) * 128;                 ps[14] = kb * 64 * 2048 + rot(g, 2) * 128;
  ps[15] = WS_WF + (h * 4 + w) * 49152 + (rho + (c >> 2)) * 6144;
  pv[5] = wsrc;                                              pv[6] = osrc;
  u32x16 pw = {wv, L_WS + w * 6144 + (4 * fq + (fr >> 2)) * 64 + (fq & 1) * 32 + (lane & 3) * 8,
                    l16 + rho * 1024, l16 + ((rho + 2) & 7) * 1024, l16 + ((rho + 4) & 7) * 1024, l16 + ((rho + 6) & 7) * 1024,
                    L_WS + w * 6144 + (fr == 0 ? fq * 16 : 1024 + l16), L_WS + (w * 24 + (lane < 24 ? lane : 0)) * 4,
                    (w * 128 + (lane >> 3 & 1) * 64 + 2 * ((g2 - rot(0, 1 + (fq & 1))) & 15) + (lane >> 2 & 1)) * 128 + (lane & 3) * 8};
  if (w < 2) {
    u32x4 pm;
#pragma unroll
    for (int k = 0; k < 4; ++k)
      pm[k] = (8 * fq + 2 * k > 16 * w + fr ? 0xFFFFu : 0u) | (8 * fq + 2 * k + 1 > 16 * w + fr ? 0xFFFF0000u : 0u);
    lds_w4(L_PM + w * 1024 + l16, pm);
  } else {
    const unsigned want = (unsigned)((0x5555ull << 16) >> (fr + 25 - 8 * fq));
    u32x4 mb;
#pragma unroll
    for (int k = 0; k < 4; ++k) mb[k] = (want >> (15 - 4 * w - k)) & 1 ? 0u : 0xFF800000u;
    lds_w4(L_MB + (w - 2) * 1024 + l16, mb);
  }
  pv[0] = L_K + 24576 + (16 * (fq & 1) + 4 * (fq >> 1) + (fr >> 2)) * 128 + w * 32 + (lane & 3) * 8;
  pv[7] = lane < 2 ? lane * 32 * 128 : OOB;
  ps[4] = WS_WO + (rot(g, 1) * 128 + kb * 8 + w) * 1024;
  ps[5] = WS_WO + 2 * MB + (rot(g, 2) * 128 + kb * 8 + w) * 1024;
  const unsigned krow = (8 * (fr >> 2) + (fr & 3)) * 64 + wr * 2048 + L_K;
  const unsigned kv0 = 16384 + wr * 2048 + fr * 64 + ((fq ^ ((fr >> 3) << 1) ^ ((fr >> 1) & 3)) * 16);
  u32x16 va = {l16, l16 + 4096, lw, L_INB + w * 18432 + l16, L_INB + ((w + 3) & 3) * 18432 + l16, L_INB + ((w + 2) & 3) * 18432 + 6144 + l16,
                     L_INB + ((w + 1) & 3) * 18432 + 12288 + l16, kv0, kv0 ^ 48, L_CS + fq * 16, fq * 512 + fr * 8, fl0,
                     ((lane & 31) >> 2) * 128 + (lane & 3) * 8,
                     fq < 3 ? (w * 128 + (lane >> 3 & 1) * 64 + 2 * ((2 * g - rot(g, fq % 3)) & 15) + (lane >> 2 & 1)) * 128 + (lane & 3) * 8 : ((lane & 7) * 64 + h) * 128 + w * 8,
                     fr8, krow + (fq ^ swzK(fr >> 2) ^ ((fr >> 1) & 1)) * 16};
  u32x8 vb = {L_V + wr * 2048 + (8 * fq + (fr >> 2)) * 64 + 8 * (lane & 1) + (((lane >> 1) & 1) ^ ((fq & 1) << 1) ^ (fr >> 3)) * 16,
                    krow + (fq ^ swzK(((fr >> 2) + 2) & 3) ^ ((fr >> 1) & 1)) * 16, 0, 0, xo,
                    lane < 24 ? L_CS + w * 96 + lane * 4 : L_SCR, w == 0 ? bid * 1024 + lane * 4 : OOB, 0};
  u32x16 sa, sb;
#pragma unroll
  for (int i = 0; i < 4; ++i) {
    sa[i] = WS_XF + cb + w * 8192 + ((wr + 1 + i) & 3) * 65536;
    sa[4 + i] = sa[i] + WS_OF + p2 * 32768;
  }
  sa[8] = soo;                                              sa[9] = sxo;
  sa[10] = WS_ST + (c * 16 + 2 * wr) * 2048;                sa[11] = sso;
  sa[12] = skv - 16384;                                     sa[13] = c ? skv - 32 * 32768 : WS_XF + (h & 15) * 32768;
  sa[14] = c ? skv - 32 * 32768 + 8 * MB : sa[13];          sa[15] = WS_FL + (c * 64 + 8 * w) * 128;
  sb[0] = myfl;                                             sb[1] = WS_WF + (h * 4 + w) * 49152;
  sb[2] = WS_WO + (g2 * 4 + w) * 32768;                     sb[3] = c ? myfl - w * 8 + ((w - dl) & 3) * 8 - 32 * 128 + 64 : myfl + 4096;
  sb[4] = w * 96;                                           sb[5] = c ? myfl + 65536 - w * 8 + ((3 - dl) & 3) * 8 - 32 * 128 + 64 : myfl + 4096;
  sb[6] = ((c * 16 + 2 * wr + p2) * 16 * 1024 + g2 * 64) * 2;  sb[7] = WS_FL + 96;
  sb[8] = WS_CS + h * 16384;                                sb[9] = w * 2048;
  sb[10] = c ? -8 : 8 - wr;                                  sb[11] = c ? 16 * MB : 0;
  sb[12] = (2 * g2 + p2 - h) * 128;                                sb[13] = bid == 0 && w == 0 ? WS_EP : 0;
  sb[14] = c == 0 && wr == 0 ? ~0u : 0u;                     sb[15] = 0;
#pragma unroll
  for (int i = 0; i < 16; ++i) { sa[i] = uni(sa[i]); sb[i] = uni(sb[i]); }
  const rsrc_t wsk = __builtin_amdgcn_make_buffer_rsrc(ws, 0, wr == 3 ? 128 * MB : 0u, 0x00020000);
  const rsrc_t outr = __builtin_amdgcn_make_buffer_rsrc(out, 0, 4 * MB, 0x00020000);
  u32x8 pa, pc;
#pragma unroll
  for (int i = 0; i < 8; ++i) { pa[i] = uni(ps[i]); pc[i] = uni(ps[8 + i]); }
  u32x16 pz = {pw[2], pw[3], pw[4], pw[5], pv[1], pv[2], pv[3], pv[4], pv[0], pw[8], pw[0], pw[1], pw[6], pw[7], pv[5], pv[6]};
  unsigned pf = pv[7];
  pin(va); pin(vb); pin(pz); pin(pf);
  auto convert = [&]() __attribute__((always_inline)) {
    f32x4 cs[3];
    u32x4 f[2][3];
    lds_barrier();
#pragma unroll
    for (int kk = 0; kk < 2; ++kk)
#pragma unroll
      for (int j = 0; j < 3; ++j) {
        const unsigned a = pz[4 + j] + (L_INB - L_K) + kk * 4096;
        f[kk][j] = cat(lds_tr(a), lds_tr(a + 2048));
      }
    pin(ones);
#pragma unroll
    for (int kk = 0; kk < 2; ++kk)
#pragma unroll
      for (int j = 0; j < 3; ++j) {
        if (kk) mma_v(cs[j], f[kk][j], ones); else mm0_v(cs[j], f[kk][j], ones);
        st16<0>(f[kk][j], wsr, l16, pa[j] - 6 * MB + kk * 6144);
      }
    nops<8>();
    pin(cs[0]); pin(cs[1]); pin(cs[2]);
    const f32x4 v = fr < 2 ? cs[0] : fr < 4 ? cs[1] : cs[2];
    st16<0>(__builtin_bit_cast(u32x4, v), wsr, pz[7], pa[3] - 524288);
  };
  STAMP(17);
  wait_vm<7>();
  pin(tag);
  STAMP(18);
  publish(1, true);
  publish(0, true);
  { const u64 t = (((u64)tag[1] << 32) | tag[0]) + 1; vb[2] = (unsigned)t; vb[3] = (unsigned)(t >> 32); }
  STAMP(19);
  vb[7] = stw;
  asm volatile(
"; @entry\n"
"s_mov_b32 s0, 0\n"
"s_mov_b32 s3, 0\n"
"v_xor_b32_e32 v255, 32, v43\n"
"v_add_u32_e32 v254, 0xc000, v43\n"
"v_add_u32_e32 v253, 0xc000, v255\n"
"v_mov_b32_e32 v24, 0x3f803f80\n"
"v_mov_b32_e32 v25, 0x3f803f80\n"
"v_mov_b32_e32 v26, 0x3f803f80\n"
"v_mov_b32_e32 v27, 0x3f803f80\n"
"s_waitcnt lgkmcnt(0)\n"
"s_bfe_u32 s98, s26, 0x10001\n"
"s_xor_b32 s98, s98, 1\n"
"s_mul_i32 s97, s98, 0x6000\n"
"s_add_u32 s97, s97, s27\n"
"s_add_u32 s96, s45, 0\n"
"s_and_b32 s96, s96, 7\n"
"s_add_u32 s96, s96, s98\n"
"s_mul_i32 s96, s96, 0x30000\n"
"s_add_u32 s96, s96, s44\n"
"s_add_u32 m0, s97, 0\n"
"s_add_u32 s98, s96, 0\n"
"buffer_load_dwordx4 v42, s[28:31], s98 offen lds\n"
"s_add_u32 m0, s97, 0x400\n"
"s_add_u32 s98, s96, 0x18000\n"
"buffer_load_dwordx4 v42, s[28:31], s98 offen lds\n"
"s_add_u32 m0, s97, 0x800\n"
"s_add_u32 s98, s96, 0x800\n"
"buffer_load_dwordx4 v42, s[28:31], s98 offen lds\n"
"s_add_u32 m0, s97, 0xc00\n"
"s_add_u32 s98, s96, 0x18800\n"
"buffer_load_dwordx4 v42, s[28:31], s98 offen lds\n"
"s_add_u32 m0, s97, 0x1000\n"
"s_add_u32 s98, s96, 0x1000\n"
"buffer_load_dwordx4 v42, s[28:31], s98 offen lds\n"
"s_add_u32 m0, s97, 0x1400\n"
"s_add_u32 s98, s96, 0x19000\n"
"buffer_load_dwordx4 v42, s[28:31], s98 offen lds\n"
"s_add_u32 s98, s68, s80\n"
"s_waitcnt vmcnt(6)\n"
"buffer_store_dwordx2 v[18:19], v50, s[4:7], s98 offen sc1\n"
"s_bfe_u32 s98, s26, 0x10001\n"
"s_xor_b32 s98, s98, 0\n"
"s_mul_i32 s97, s98, 0x6000\n"
"s_add_u32 s97, s97, s27\n"
"s_add_u32 s96, s45, 2\n"
"s_and_b32 s96, s96, 7\n"
"s_add_u32 s96, s96, s98\n"
"s_mul_i32 s96, s96, 0x30000\n"
"s_add_u32 s96, s96, s44\n"
"s_add_u32 m0, s97, 0xc000\n"
"s_add_u32 s98, s96, 0\n"
"buffer_load_dwordx4 v42, s[28:31], s98 offen lds\n"
"s_add_u32 m0, s97, 0xc400\n"
"s_add_u32 s98, s96, 0x18000\n"
"buffer_load_dwordx4 v42, s[28:31], s98 offen lds\n"
"s_add_u32 m0, s97, 0xc800\n"
"s_add_u32 s98, s96, 0x800\n"
"buffer_load_dwordx4 v42, s[28:31], s98 offen lds\n"
"s_add_u32 m0, s97, 0xcc00\n"
"s_add_u32 s98, s96, 0x18800\n"
"buffer_load_dwordx4 v42, s[28:31], s98 offen lds\n"
"s_add_u32 m0, s97, 0xd000\n"
"s_add_u32 s98, s96, 0x1000\n"
"buffer_load_dwordx4 v42, s[28:31], s98 offen lds\n"
"s_add_u32 m0, s97, 0xd400\n"
"s_add_u32 s98, s96, 0x19000\n"
"buffer_load_dwordx4 v42, s[28:31], s98 offen lds\n"
"s_bfe_u32 s98, s26, 0x10001\n"
"s_xor_b32 s98, s98, 1\n"
"s_mul_i32 s97, s98, 0x6000\n"
"s_add_u32 s97, s97, s27\n"
"s_add_u32 s96, s45, 2\n"
"s_and_b32 s96, s96, 7\n"
"s_add_u32 s96, s96, s98\n"
"s_mul_i32 s96, s96, 0x30000\n"
"s_add_u32 s96, s96, s44\n"
"s_add_u32 m0, s97, 0xc000\n"
"s_add_u32 s98, s96, 0\n"
"buffer_load_dwordx4 v42, s[28:31], s98 offen lds\n"
"s_add_u32 m0, s97, 0xc400\n"
"s_add_u32 s98, s96, 0x18000\n"
"buffer_load_dwordx4 v42, s[28:31], s98 offen lds\n"
"s_add_u32 m0, s97, 0xc800\n"
"s_add_u32 s98, s96, 0x800\n"
"buffer_load_dwordx4 v42, s[28:31], s98 offen lds\n"
"s_add_u32 m0, s97, 0xcc00\n"
"s_add_u32 s98, s96, 0x18800\n"
"buffer_load_dwordx4 v42, s[28:31], s98 offen lds\n"
"s_add_u32 m0, s97, 0xd000\n"
"s_add_u32 s98, s96, 0x1000\n"
"buffer_load_dwordx4 v42, s[28:31], s98 offen lds\n"
"s_add_u32 m0, s97, 0xd400\n"
"s_add_u32 s98, s96, 0x19000\n"
"buffer_load_dwordx4 v42, s[28:31], s98 offen lds\n"
"s_waitcnt vmcnt(13)\n"
"ds_read_b64_tr_b16 a[0:1], v43\n"
"ds_read_b64_tr_b16 a[2:3], v43 offset:1024\n"
"ds_read_b64_tr_b16 a[4:5], v255\n"
"ds_read_b64_tr_b16 a[6:7], v255 offset:1024\n"
"ds_read_b64_tr_b16 a[8:9], v43 offset:2048\n"
"ds_read_b64_tr_b16 a[10:11], v43 offset:3072\n"
"ds_read_b64_tr_b16 a[12:13], v255 offset:2048\n"
"ds_read_b64_tr_b16 a[14:15], v255 offset:3072\n"
"ds_read_b64_tr_b16 a[16:17], v43 offset:4096\n"
"ds_read_b64_tr_b16 a[18:19], v43 offset:5120\n"
"ds_read_b64_tr_b16 a[20:21], v255 offset:4096\n"
"ds_read_b64_tr_b16 a[22:23], v255 offset:5120\n"
"ds_read_b64_tr_b16 a[24:25], v43 offset:24576\n"
"ds_read_b64_tr_b16 a[26:27], v43 offset:25600\n"
"ds_read_b64_tr_b16 a[28:29], v255 offset:24576\n"
"s_waitcnt lgkmcnt(13)\n"
"ds_read_b64_tr_b16 a[30:31], v255 offset:25600\n"
"ds_read_b64_tr_b16 a[32:33], v43 offset:26624\n"
"s_waitcnt lgkmcnt(13)\n"
"ds_read_b64_tr_b16 a[34:35], v43 offset:27648\n"
"ds_read_b64_tr_b16 a[36:37], v255 offset:26624\n"
"s_waitcnt lgkmcnt(13)\n"
"ds_read_b64_tr_b16 a[38:39], v255 offset:27648\n"
"ds_read_b64_tr_b16 a[40:41], v43 offset:28672\n"
"s_waitcnt lgkmcnt(13)\n"
"ds_read_b64_tr_b16 a[42:43], v43 offset:29696\n"
"ds_read_b64_tr_b16 a[44:45], v255 offset:28672\n"
"s_waitcnt lgkmcnt(13)\n"
"ds_read_b64_tr_b16 a[46:47], v255 offset:29696\n"
"s_waitcnt lgkmcnt(12)\n"
"v_mfma_f32_16x16x32_bf16 v[28:31], a[0:3], v[24:27], 0\n"
"v_mfma_f32_16x16x32_bf16 v[48:51], a[4:7], v[24:27], 0\n"
"v_mfma_f32_16x16x32_bf16 v[52:55], a[8:11], v[24:27], 0\n"
"v_mfma_f32_16x16x32_bf16 v[56:59], a[12:15], v[24:27], 0\n"
"v_mfma_f32_16x16x32_bf16 v[60:63], a[16:19], v[24:27], 0\n"
"v_mfma_f32_16x16x32_bf16 v[64:67], a[20:23], v[24:27], 0\n"
"s_waitcnt lgkmcnt(0)\n"
"v_mfma_f32_16x16x32_bf16 v[28:31], a[24:27], v[24:27], v[28:31]\n"
"v_mfma_f32_16x16x32_bf16 v[48:51], a[28:31], v[24:27], v[48:51]\n"
"v_mfma_f32_16x16x32_bf16 v[52:55], a[32:35], v[24:27], v[52:55]\n"
"v_mfma_f32_16x16x32_bf16 v[56:59], a[36:39], v[24:27], v[56:59]\n"
"v_mfma_f32_16x16x32_bf16 v[60:63], a[40:43], v[24:27], v[60:63]\n"
"v_mfma_f32_16x16x32_bf16 v[64:67], a[44:47], v[24:27], v[64:67]\n"
"s_bfe_u32 s98, s26, 0x10001\n"
"s_xor_b32 s98, s98, 0\n"
"s_mul_i32 s97, s98, 0x6000\n"
"s_add_u32 s97, s97, s27\n"
"s_add_u32 s96, s45, 4\n"
"s_and_b32 s96, s96, 7\n"
"s_add_u32 s96, s96, s98\n"
"s_mul_i32 s96, s96, 0x30000\n"
"s_add_u32 s96, s96, s44\n"
"s_add_u32 m0, s97, 0\n"
"s_add_u32 s98, s96, 0\n"
"buffer_load_dwordx4 v42, s[28:31], s98 offen lds\n"
"s_add_u32 m0, s97, 0x400\n"
"s_add_u32 s98, s96, 0x18000\n"
"buffer_load_dwordx4 v42, s[28:31], s98 offen lds\n"
"s_add_u32 m0, s97, 0x800\n"
"s_add_u32 s98, s96, 0x800\n"
"buffer_load_dwordx4 v42, s[28:31], s98 offen lds\n"
"s_add_u32 m0, s97, 0xc00\n"
"s_add_u32 s98, s96, 0x18800\n"
"buffer_load_dwordx4 v42, s[28:31], s98 offen lds\n"
"s_add_u32 m0, s97, 0x1000\n"
"s_add_u32 s98, s96, 0x1000\n"
"buffer_load_dwordx4 v42, s[28:31], s98 offen lds\n"
"s_add_u32 m0, s97, 0x1400\n"
"s_add_u32 s98, s96, 0x19000\n"
"buffer_load_dwordx4 v42, s[28:31], s98 offen lds\n"
"s_bfe_u32 s98, s26, 0x10001\n"
"s_xor_b32 s98, s98, 1\n"
"s_mul_i32 s97, s98, 0x6000\n"
"s_add_u32 s97, s97, s27\n"
"s_add_u32 s96, s45, 4\n"
"s_and_b32 s96, s96, 7\n"
"s_add_u32 s96, s96, s98\n"
"s_mul_i32 s96, s96, 0x30000\n"
"s_add_u32 s96, s96, s44\n"
"s_add_u32 m0, s97, 0\n"
"s_add_u32 s98, s96, 0\n"
"buffer_load_dwordx4 v42, s[28:31], s98 offen lds\n"
"s_add_u32 m0, s97, 0x400\n"
"s_add_u32 s98, s96, 0x18000\n"
"buffer_load_dwordx4 v42, s[28:31], s98 offen lds\n"
"s_add_u32 m0, s97, 0x800\n"
"s_add_u32 s98, s96, 0x800\n"
"buffer_load_dwordx4 v42, s[28:31], s98 offen lds\n"
"s_add_u32 m0, s97, 0xc00\n"
"s_add_u32 s98, s96, 0x18800\n"
"buffer_load_dwordx4 v42, s[28:31], s98 offen lds\n"
"s_add_u32 m0, s97, 0x1000\n"
"s_add_u32 s98, s96, 0x1000\n"
"buffer_load_dwordx4 v42, s[28:31], s98 offen lds\n"
"s_add_u32 m0, s97, 0x1400\n"
"s_add_u32 s98, s96, 0x19000\n"
"buffer_load_dwordx4 v42, s[28:31], s98 offen lds\n"
"s_waitcnt vmcnt(12)\n"
"ds_read_b64_tr_b16 a[48:49], v254\n"
"ds_read_b64_tr_b16 a[50:51], v254 offset:1024\n"
"ds_read_b64_tr_b16 a[52:53], v253\n"
"ds_read_b64_tr_b16 a[54:55], v253 offset:1024\n"
"ds_read_b64_tr_b16 a[56:57], v254 offset:2048\n"
"ds_read_b64_tr_b16 a[58:59], v254 offset:3072\n"
"ds_read_b64_tr_b16 a[60:61], v253 offset:2048\n"
"ds_read_b64_tr_b16 a[62:63], v253 offset:3072\n"
"ds_read_b64_tr_b16 a[64:65], v254 offset:4096\n"
"ds_read_b64_tr_b16 a[66:67], v254 offset:5120\n"
"ds_read_b64_tr_b16 a[68:69], v253 offset:4096\n"
"ds_read_b64_tr_b16 a[70:71], v253 offset:5120\n"
"ds_read_b64_tr_b16 a[72:73], v254 offset:24576\n"
"ds_read_b64_tr_b16 a[74:75], v254 offset:25600\n"
"ds_read_b64_tr_b16 a[76:77], v253 offset:24576\n"
"s_waitcnt lgkmcnt(13)\n"
"ds_read_b64_tr_b16 a[78:79], v253 offset:25600\n"
"ds_read_b64_tr_b16 a[80:81], v254 offset:26624\n"
"s_waitcnt lgkmcnt(13)\n"
"ds_read_b64_tr_b16 a[82:83], v254 offset:27648\n"
"ds_read_b64_tr_b16 a[84:85], v253 offset:26624\n"
"s_waitcnt lgkmcnt(13)\n"
"ds_read_b64_tr_b16 a[86:87], v253 offset:27648\n"
"ds_read_b64_tr_b16 a[88:89], v254 offset:28672\n"
"s_waitcnt lgkmcnt(13)\n"
"ds_read_b64_tr_b16 a[90:91], v254 offset:29696\n"
"ds_read_b64_tr_b16 a[92:93], v253 offset:28672\n"
"s_waitcnt lgkmcnt(13)\n"
"ds_read_b64_tr_b16 a[94:95], v253 offset:29696\n"
"s_waitcnt lgkmcnt(12)\n"
"v_mfma_f32_16x16x32_bf16 v[28:31], a[48:51], v[24:27], v[28:31]\n"
"v_mfma_f32_16x16x32_bf16 v[48:51], a[52:55], v[24:27], v[48:51]\n"
"v_mfma_f32_16x16x32_bf16 v[52:55], a[56:59], v[24:27], v[52:55]\n"
"v_mfma_f32_16x16x32_bf16 v[56:59], a[60:63], v[24:27], v[56:59]\n"
"v_mfma_f32_16x16x32_bf16 v[60:63], a[64:67], v[24:27], v[60:63]\n"
"v_mfma_f32_16x16x32_bf16 v[64:67], a[68:71], v[24:27], v[64:67]\n"
"s_waitcnt lgkmcnt(0)\n"
"v_mfma_f32_16x16x32_bf16 v[28:31], a[72:75], v[24:27], v[28:31]\n"
"v_mfma_f32_16x16x32_bf16 v[48:51], a[76:79], v[24:27], v[48:51]\n"
"v_mfma_f32_16x16x32_bf16 v[52:55], a[80:83], v[24:27], v[52:55]\n"
"v_mfma_f32_16x16x32_bf16 v[56:59], a[84:87], v[24:27], v[56:59]\n"
"v_mfma_f32_16x16x32_bf16 v[60:63], a[88:91], v[24:27], v[60:63]\n"
"v_mfma_f32_16x16x32_bf16 v[64:67], a[92:95], v[24:27], v[64:67]\n"
"s_bfe_u32 s98, s26, 0x10001\n"
"s_xor_b32 s98, s98, 0\n"
"s_mul_i32 s97, s98, 0x6000\n"
"s_add_u32 s97, s97, s27\n"
"s_add_u32 s96, s45, 6\n"
"s_and_b32 s96, s96, 7\n"
"s_add_u32 s96, s96, s98\n"
"s_mul_i32 s96, s96, 0x30000\n"
"s_add_u32 s96, s96, s44\n"
"s_add_u32 m0, s97, 0xc000\n"
"s_add_u32 s98, s96, 0\n"
"buffer_load_dwordx4 v42, s[28:31], s98 offen lds\n"
"s_add_u32 m0, s97, 0xc400\n"
"s_add_u32 s98, s96, 0x18000\n"
"buffer_load_dwordx4 v42, s[28:31], s98 offen lds\n"
"s_add_u32 m0, s97, 0xc800\n"
"s_add_u32 s98, s96, 0x800\n"
"buffer_load_dwordx4 v42, s[28:31], s98 offen lds\n"
"s_add_u32 m0, s97, 0xcc00\n"
"s_add_u32 s98, s96, 0x18800\n"
"buffer_load_dwordx4 v42, s[28:31], s98 offen lds\n"
"s_add_u32 m0, s97, 0xd000\n"
"s_add_u32 s98, s96, 0x1000\n"
"buffer_load_dwordx4 v42, s[28:31], s98 offen lds\n"
"s_add_u32 m0, s97, 0xd400\n"
"s_add_u32 s98, s96, 0x19000\n"
"buffer_load_dwordx4 v42, s[28:31], s98 offen lds\n"
"s_bfe_u32 s98, s26, 0x10001\n"
"s_xor_b32 s98, s98, 1\n"
"s_mul_i32 s97, s98, 0x6000\n"
"s_add_u32 s97, s97, s27\n"
"s_add_u32 s96, s45, 6\n"
"s_and_b32 s96, s96, 7\n"
"s_add_u32 s96, s96, s98\n"
"s_mul_i32 s96, s96, 0x30000\n"
"s_add_u32 s96, s96, s44\n"
"s_add_u32 m0, s97, 0xc000\n"
"s_add_u32 s98, s96, 0\n"
"buffer_load_dwordx4 v42, s[28:31], s98 offen lds\n"
"s_add_u32 m0, s97, 0xc400\n"
"s_add_u32 s98, s96, 0x18000\n"
"buffer_load_dwordx4 v42, s[28:31], s98 offen lds\n"
"s_add_u32 m0, s97, 0xc800\n"
"s_add_u32 s98, s96, 0x800\n"
"buffer_load_dwordx4 v42, s[28:31], s98 offen lds\n"
"s_add_u32 m0, s97, 0xcc00\n"
"s_add_u32 s98, s96, 0x18800\n"
"buffer_load_dwordx4 v42, s[28:31], s98 offen lds\n"
"s_add_u32 m0, s97, 0xd000\n"
"s_add_u32 s98, s96, 0x1000\n"
"buffer_load_dwordx4 v42, s[28:31], s98 offen lds\n"
"s_add_u32 m0, s97, 0xd400\n"
"s_add_u32 s98, s96, 0x19000\n"
"buffer_load_dwordx4 v42, s[28:31], s98 offen lds\n"
"s_add_u32 s98, s67, 0x1000\n"
"buffer_load_dwordx2 v[250:251], v12, s[4:7], s98 offen sc1\n"
"s_waitcnt vmcnt(13)\n"
"ds_read_b64_tr_b16 a[96:97], v43\n"
"ds_read_b64_tr_b16 a[98:99], v43 offset:1024\n"
"ds_read_b64_tr_b16 a[100:101], v255\n"
"ds_read_b64_tr_b16 a[102:103], v255 offset:1024\n"
"ds_read_b64_tr_b16 a[104:105], v43 offset:2048\n"
"ds_read_b64_tr_b16 a[106:107], v43 offset:3072\n"
"ds_read_b64_tr_b16 a[108:109], v255 offset:2048\n"
"ds_read_b64_tr_b16 a[110:111], v255 offset:3072\n"
"ds_read_b64_tr_b16 a[112:113], v43 offset:4096\n"
"ds_read_b64_tr_b16 a[114:115], v43 offset:5120\n"
"ds_read_b64_tr_b16 a[116:117], v255 offset:4096\n"
"ds_read_b64_tr_b16 a[118:119], v255 offset:5120\n"
"ds_read_b64_tr_b16 a[120:121], v43 offset:24576\n"
"ds_read_b64_tr_b16 a[122:123], v43 offset:25600\n"
"ds_read_b64_tr_b16 a[124:125], v255 offset:24576\n"
"s_waitcnt lgkmcnt(13)\n"
"ds_read_b64_tr_b16 a[126:127], v255 offset:25600\n"
"ds_read_b64_tr_b16 v[68:69], v43 offset:26624\n"
"s_waitcnt lgkmcnt(13)\n"
"ds_read_b64_tr_b16 v[70:71], v43 offset:27648\n"
"ds_read_b64_tr_b16 v[72:73], v255 offset:26624\n"
"s_waitcnt lgkmcnt(13)\n"
"ds_read_b64_tr_b16 v[74:75], v255 offset:27648\n"
"ds_read_b64_tr_b16 v[76:77], v43 offset:28672\n"
"s_waitcnt lgkmcnt(13)\n"
"ds_read_b64_tr_b16 v[78:79], v43 offset:29696\n"
"ds_read_b64_tr_b16 v[80:81], v255 offset:28672\n"
"s_waitcnt lgkmcnt(13)\n"
"ds_read_b64_tr_b16 v[82:83], v255 offset:29696\n"
"s_waitcnt lgkmcnt(12)\n"
"v_mfma_f32_16x16x32_bf16 v[28:31], a[96:99], v[24:27], v[28:31]\n"
"v_mfma_f32_16x16x32_bf16 v[48:51], a[100:103], v[24:27], v[48:51]\n"
"v_mfma_f32_16x16x32_bf16 v[52:55], a[104:107], v[24:27], v[52:55]\n"
"v_mfma_f32_16x16x32_bf16 v[56:59], a[108:111], v[24:27], v[56:59]\n"
"v_mfma_f32_16x16x32_bf16 v[60:63], a[112:115], v[24:27], v[60:63]\n"
"v_mfma_f32_16x16x32_bf16 v[64:67], a[116:119], v[24:27], v[64:67]\n"
"s_waitcnt lgkmcnt(0)\n"
"v_mfma_f32_16x16x32_bf16 v[28:31], a[120:123], v[24:27], v[28:31]\n"
"v_mfma_f32_16x16x32_bf16 v[48:51], a[124:127], v[24:27], v[48:51]\n"
"v_mfma_f32_16x16x32_bf16 v[52:55], v[68:71], v[24:27], v[52:55]\n"
"v_mfma_f32_16x16x32_bf16 v[56:59], v[72:75], v[24:27], v[56:59]\n"
"v_mfma_f32_16x16x32_bf16 v[60:63], v[76:79], v[24:27], v[60:63]\n"
"v_mfma_f32_16x16x32_bf16 v[64:67], v[80:83], v[24:27], v[64:67]\n"
"s_waitcnt vmcnt(0)\n"
"v_cmp_ne_u64_e32 vcc, v[250:251], v[18:19]\n"
"s_cbranch_vccnz .Lmk_f1s\n"
".Lmk_f1r:\n"
"s_add_u32 s98, s52, 0x40000\n"
"buffer_load_dwordx4 v[84:87], v32, s[4:7], s98 offen sc1\n"
"buffer_load_dwordx4 v[88:91], v32, s[4:7], s98 offen offset:1024 sc1\n"
"buffer_load_dwordx4 v[92:95], v33, s[4:7], s98 offen sc1\n"
"buffer_load_dwordx4 v[96:99], v33, s[4:7], s98 offen offset:1024 sc1\n"
"buffer_load_dwordx4 v[100:103], v34, s[4:7], s98 offen sc1\n"
"buffer_load_dwordx4 v[104:107], v34, s[4:7], s98 offen offset:1024 sc1\n"
"buffer_load_dwordx4 v[108:111], v35, s[4:7], s98 offen sc1\n"
"buffer_load_dwordx4 v[112:115], v35, s[4:7], s98 offen offset:1024 sc1\n"
"s_add_u32 s98, s53, 0x40000\n"
"buffer_load_dwordx4 v[116:119], v32, s[4:7], s98 offen sc1\n"
"buffer_load_dwordx4 v[120:123], v32, s[4:7], s98 offen offset:1024 sc1\n"
"buffer_load_dwordx4 v[124:127], v33, s[4:7], s98 offen sc1\n"
"buffer_load_dwordx4 v[128:131], v33, s[4:7], s98 offen offset:1024 sc1\n"
"buffer_load_dwordx4 v[132:135], v34, s[4:7], s98 offen sc1\n"
"buffer_load_dwordx4 v[136:139], v34, s[4:7], s98 offen offset:1024 sc1\n"
"buffer_load_dwordx4 v[140:143], v35, s[4:7], s98 offen sc1\n"
"buffer_load_dwordx4 v[144:147], v35, s[4:7], s98 offen offset:1024 sc1\n"
"s_getpc_b64 s[84:85]\n"
".Lmk_h2:\n"
"s_and_b32 s84, s84, -16\n"
"s_and_b32 s85, s85, 0xffff\n"
"s_mov_b32 s86, (.Lmk_end-.Lmk_h2)&0xFFFFFFF0\n"
"s_mov_b32 s87, 0x20000\n"
"s_and_b32 s98, s26, 0xfffffc00\n"
"s_mov_b32 m0, 0x27800\n"
"s_add_u32 s98, s98, 0x1e000\n"
"buffer_load_dwordx4 v0, s[84:87], s98 offen lds\n"
"ds_read_b64_tr_b16 v[148:149], v254\n"
"ds_read_b64_tr_b16 v[150:151], v254 offset:1024\n"
"ds_read_b64_tr_b16 v[152:153], v253\n"
"ds_read_b64_tr_b16 v[154:155], v253 offset:1024\n"
"ds_read_b64_tr_b16 v[156:157], v254 offset:2048\n"
"ds_read_b64_tr_b16 v[158:159], v254 offset:3072\n"
"ds_read_b64_tr_b16 v[160:161], v253 offset:2048\n"
"ds_read_b64_tr_b16 v[162:163], v253 offset:3072\n"
"ds_read_b64_tr_b16 v[164:165], v254 offset:4096\n"
"ds_read_b64_tr_b16 v[166:167], v254 offset:5120\n"
"ds_read_b64_tr_b16 v[168:169], v253 offset:4096\n"
"ds_read_b64_tr_b16 v[170:171], v253 offset:5120\n"
"ds_read_b64_tr_b16 v[172:173], v254 offset:24576\n"
"ds_read_b64_tr_b16 v[174:175], v254 offset:25600\n"
"ds_read_b64_tr_b16 v[176:177], v253 offset:24576\n"
"s_waitcnt lgkmcnt(13)\n"
"ds_read_b64_tr_b16 v[178:179], v253 offset:25600\n"
"ds_read_b64_tr_b16 v[180:181], v254 offset:26624\n"
"s_waitcnt lgkmcnt(13)\n"
"ds_read_b64_tr_b16 v[182:183], v254 offset:27648\n"
"ds_read_b64_tr_b16 v[184:185], v253 offset:26624\n"
"s_waitcnt lgkmcnt(13)\n"
"ds_read_b64_tr_b16 v[186:187], v253 offset:27648\n"
"ds_read_b64_tr_b16 v[188:189], v254 offset:28672\n"
"s_waitcnt lgkmcnt(13)\n"
"ds_read_b64_tr_b16 v[190:191], v254 offset:29696\n"
"ds_read_b64_tr_b16 v[192:193], v253 offset:28672\n"
"s_waitcnt lgkmcnt(13)\n"
"ds_read_b64_tr_b16 v[194:195], v253 offset:29696\n"
"s_waitcnt lgkmcnt(12)\n"
"v_mfma_f32_16x16x32_bf16 v[28:31], v[148:151], v[24:27], v[28:31]\n"
"v_mfma_f32_16x16x32_bf16 v[48:51], v[152:155], v[24:27], v[48:51]\n"
"v_mfma_f32_16x16x32_bf16 v[52:55], v[156:159], v[24:27], v[52:55]\n"
"v_mfma_f32_16x16x32_bf16 v[56:59], v[160:163], v[24:27], v[56:59]\n"
"v_mfma_f32_16x16x32_bf16 v[60:63], v[164:167], v[24:27], v[60:63]\n"
"v_mfma_f32_16x16x32_bf16 v[64:67], v[168:171], v[24:27], v[64:67]\n"
"s_waitcnt lgkmcnt(0)\n"
"v_mfma_f32_16x16x32_bf16 v[28:31], v[172:175], v[24:27], v[28:31]\n"
"v_mfma_f32_16x16x32_bf16 v[48:51], v[176:179], v[24:27], v[48:51]\n"
"v_mfma_f32_16x16x32_bf16 v[52:55], v[180:183], v[24:27], v[52:55]\n"
"v_mfma_f32_16x16x32_bf16 v[56:59], v[184:187], v[24:27], v[56:59]\n"
"v_mfma_f32_16x16x32_bf16 v[60:63], v[188:191], v[24:27], v[60:63]\n"
"v_mfma_f32_16x16x32_bf16 v[64:67], v[192:195], v[24:27], v[64:67]\n"
"s_nop 7\n"
"ds_write_b128 v44, v[28:31]\n"
"ds_write_b128 v44, v[48:51] offset:64\n"
"ds_write_b128 v44, v[52:55] offset:128\n"
"ds_write_b128 v44, v[56:59] offset:192\n"
"ds_write_b128 v44, v[60:63] offset:256\n"
"ds_write_b128 v44, v[64:67] offset:320\n"
"s_waitcnt lgkmcnt(0)\n"
"s_barrier\n"
"ds_read_b32 v255, v45\n"
"ds_read_b32 v254, v45 offset:6144\n"
"ds_read_b32 v253, v45 offset:12288\n"
"ds_read_b32 v252, v45 offset:18432\n"
"s_waitcnt lgkmcnt(0)\n"
"v_add_f32_e32 v255, v255, v254\n"
"v_add_f32_e32 v253, v253, v252\n"
"v_add_f32_e32 v255, v255, v253\n"
"ds_write_b32 v21, v255\n"
".Lmk_z0:\n"
"s_waitcnt vmcnt(16)\n"
"; @A G1 0.0\n"
"v_mfma_f32_16x16x32_bf16 v[24:27], a[0:3], v[84:87], 0\n"
"s_add_u32 s98, s62, 0x4000\n"
"v_mfma_f32_16x16x32_bf16 v[28:31], a[4:7], v[84:87], 0\n"
"s_add_u32 m0, s77, 0xa000\n"
"s_add_u32 s97, s49, 0\n"
"buffer_load_dwordx4 v47, s[36:39], s97 offen lds\n"
"v_mfma_f32_16x16x32_bf16 v[48:51], a[8:11], v[84:87], 0\n"
"s_add_u32 m0, s77, 0xa400\n"
"s_add_u32 s97, s49, 0x4000\n"
"v_mfma_f32_16x16x32_bf16 v[52:55], a[12:15], v[84:87], 0\n"
"buffer_load_dwordx2 v[254:255], v10, s[4:7], s98 offen sc1\n"
"v_mfma_f32_16x16x32_bf16 v[56:59], a[16:19], v[84:87], 0\n"
"s_add_u32 s96, s54, 0x40000\n"
"buffer_load_dwordx4 v47, s[36:39], s97 offen lds\n"
"v_mfma_f32_16x16x32_bf16 v[60:63], a[20:23], v[84:87], 0\n"
"s_waitcnt vmcnt(18)\n"
"s_add_u32 m0, s77, 0xc000\n"
"s_add_u32 s97, s50, 0\n"
"v_mfma_f32_16x16x32_bf16 v[24:27], a[24:27], v[88:91], v[24:27]\n"
"buffer_load_dwordx4 v[64:67], v32, s[4:7], s96 offen sc1\n"
"v_mfma_f32_16x16x32_bf16 v[28:31], a[28:31], v[88:91], v[28:31]\n"
"buffer_load_dwordx4 v47, s[40:43], s97 offen lds\n"
"v_mfma_f32_16x16x32_bf16 v[48:51], a[32:35], v[88:91], v[48:51]\n"
"s_add_u32 m0, s77, 0xc400\n"
"s_add_u32 s97, s50, 0x4000\n"
"v_mfma_f32_16x16x32_bf16 v[52:55], a[36:39], v[88:91], v[52:55]\n"
"buffer_load_dwordx2 v[252:253], v10, s[4:7], s98 offen offset:128 sc1\n"
"v_mfma_f32_16x16x32_bf16 v[56:59], a[40:43], v[88:91], v[56:59]\n"
"buffer_load_dwordx4 v[84:87], v32, s[4:7], s96 offen offset:1024 sc1\n"
"buffer_load_dwordx4 v47, s[40:43], s97 offen lds\n"
"v_mfma_f32_16x16x32_bf16 v[60:63], a[44:47], v[88:91], v[60:63]\n"
"s_waitcnt vmcnt(22)\n"
"s_add_u32 m0, s77, 0x4000\n"
"s_add_u32 s97, s46, 0\n"
"v_mfma_f32_16x16x32_bf16 v[24:27], a[48:51], v[92:95], v[24:27]\n"
"buffer_load_dwordx2 v[250:251], v10, s[4:7], s98 offen offset:256 sc1\n"
"v_mfma_f32_16x16x32_bf16 v[28:31], a[52:55], v[92:95], v[28:31]\n"
"buffer_load_dwordx4 v46, s[32:35], s97 offen lds\n"
"v_mfma_f32_16x16x32_bf16 v[48:51], a[56:59], v[92:95], v[48:51]\n"
"s_add_u32 m0, s77, 0x4400\n"
"s_add_u32 s97, s46, 0xc000\n"
"v_mfma_f32_16x16x32_bf16 v[52:55], a[60:63], v[92:95], v[52:55]\n"
"buffer_load_dwordx2 v[248:249], v10, s[4:7], s98 offen offset:384 sc1\n"
"v_mfma_f32_16x16x32_bf16 v[56:59], a[64:67], v[92:95], v[56:59]\n"
"buffer_load_dwordx4 v[88:91], v33, s[4:7], s96 offen sc1\n"
"buffer_load_dwordx4 v46, s[32:35], s97 offen lds\n"
"v_mfma_f32_16x16x32_bf16 v[60:63], a[68:71], v[92:95], v[60:63]\n"
"s_waitcnt vmcnt(26)\n"
"s_add_u32 m0, s77, 0x6000\n"
"s_add_u32 s98, s47, 0\n"
"v_mfma_f32_16x16x32_bf16 v[24:27], a[72:75], v[96:99], v[24:27]\n"
"v_mfma_f32_16x16x32_bf16 v[28:31], a[76:79], v[96:99], v[28:31]\n"
"buffer_load_dwordx4 v46, s[32:35], s98 offen lds\n"
"v_mfma_f32_16x16x32_bf16 v[48:51], a[80:83], v[96:99], v[48:51]\n"
"s_add_u32 m0, s77, 0x6400\n"
"s_add_u32 s98, s47, 0xc000\n"
"v_mfma_f32_16x16x32_bf16 v[52:55], a[84:87], v[96:99], v[52:55]\n"
"v_mfma_f32_16x16x32_bf16 v[56:59], a[88:91], v[96:99], v[56:59]\n"
"buffer_load_dwordx4 v[92:95], v33, s[4:7], s96 offen offset:1024 sc1\n"
"buffer_load_dwordx4 v46, s[32:35], s98 offen lds\n"
"v_mfma_f32_16x16x32_bf16 v[60:63], a[92:95], v[96:99], v[60:63]\n"
"s_waitcnt vmcnt(28)\n"
"s_add_u32 m0, s77, 0x8000\n"
"s_add_u32 s98, s48, 0\n"
"v_mfma_f32_16x16x32_bf16 v[24:27], a[96:99], v[100:103], v[24:27]\n"
"v_mfma_f32_16x16x32_bf16 v[28:31], a[100:103], v[100:103], v[28:31]\n"
"buffer_load_dwordx4 v46, s[32:35], s98 offen lds\n"
"v_mfma_f32_16x16x32_bf16 v[48:51], a[104:107], v[100:103], v[48:51]\n"
"s_add_u32 m0, s77, 0x8400\n"
"s_add_u32 s98, s48, 0xc000\n"
"v_mfma_f32_16x16x32_bf16 v[52:55], a[108:111], v[100:103], v[52:55]\n"
"v_mfma_f32_16x16x32_bf16 v[56:59], a[112:115], v[100:103], v[56:59]\n"
"buffer_load_dwordx4 v[96:99], v34, s[4:7], s96 offen sc1\n"
"buffer_load_dwordx4 v46, s[32:35], s98 offen lds\n"
"v_mfma_f32_16x16x32_bf16 v[60:63], a[116:119], v[100:103], v[60:63]\n"
"s_waitcnt vmcnt(30)\n"
"v_mfma_f32_16x16x32_bf16 v[24:27], a[120:123], v[104:107], v[24:27]\n"
"v_mfma_f32_16x16x32_bf16 v[28:31], a[124:127], v[104:107], v[28:31]\n"
"v_mfma_f32_16x16x32_bf16 v[48:51], v[68:71], v[104:107], v[48:51]\n"
"v_mfma_f32_16x16x32_bf16 v[52:55], v[72:75], v[104:107], v[52:55]\n"
"v_mfma_f32_16x16x32_bf16 v[56:59], v[76:79], v[104:107], v[56:59]\n"
"buffer_load_dwordx4 v[44:47], v34, s[4:7], s96 offen offset:1024 sc1\n"
"v_mfma_f32_16x16x32_bf16 v[60:63], v[80:83], v[104:107], v[60:63]\n"
"s_waitcnt vmcnt(30)\n"
"v_mfma_f32_16x16x32_bf16 v[24:27], v[148:151], v[108:111], v[24:27]\n"
"v_mfma_f32_16x16x32_bf16 v[28:31], v[152:155], v[108:111], v[28:31]\n"
"v_mfma_f32_16x16x32_bf16 v[48:51], v[156:159], v[108:111], v[48:51]\n"
"v_mfma_f32_16x16x32_bf16 v[52:55], v[160:163], v[108:111], v[52:55]\n"
"v_mfma_f32_16x16x32_bf16 v[56:59], v[164:167], v[108:111], v[56:59]\n"
"buffer_load_dwordx4 v[100:103], v35, s[4:7], s96 offen sc1\n"
"v_mfma_f32_16x16x32_bf16 v[60:63], v[168:171], v[108:111], v[60:63]\n"
"s_waitcnt vmcnt(30)\n"
"v_mfma_f32_16x16x32_bf16 v[24:27], v[172:175], v[112:115], v[24:27]\n"
"v_mfma_f32_16x16x32_bf16 v[28:31], v[176:179], v[112:115], v[28:31]\n"
"v_mfma_f32_16x16x32_bf16 v[48:51], v[180:183], v[112:115], v[48:51]\n"
"v_mfma_f32_16x16x32_bf16 v[52:55], v[184:187], v[112:115], v[52:55]\n"
"v_mfma_f32_16x16x32_bf16 v[56:59], v[188:191], v[112:115], v[56:59]\n"
"buffer_load_dwordx4 v[104:107], v35, s[4:7], s96 offen offset:1024 sc1\n"
"v_mfma_f32_16x16x32_bf16 v[60:63], v[192:195], v[112:115], v[60:63]\n"
"s_waitcnt vmcnt(29)\n"
"; @A G1 0.1\n"
"v_mfma_f32_16x16x32_bf16 v[108:111], a[0:3], v[116:119], 0\n"
"v_mfma_f32_16x16x32_bf16 v[112:115], a[4:7], v[116:119], 0\n"
"v_mfma_f32_16x16x32_bf16 v[196:199], a[8:11], v[116:119], 0\n"
"v_mfma_f32_16x16x32_bf16 v[200:203], a[12:15], v[116:119], 0\n"
"ds_write_b128 v3, v[24:27]\n"
"v_mfma_f32_16x16x32_bf16 v[24:27], a[16:19], v[116:119], 0\n"
"s_add_u32 s98, s55, 0x40000\n"
"v_mfma_f32_16x16x32_bf16 v[204:207], a[20:23], v[116:119], 0\n"
"buffer_load_dwordx4 v[116:119], v32, s[4:7], s98 offen sc1\n"
"v_mfma_f32_16x16x32_bf16 v[108:111], a[24:27], v[120:123], v[108:111]\n"
"ds_write_b128 v3, v[28:31] offset:1024\n"
"v_mfma_f32_16x16x32_bf16 v[112:115], a[28:31], v[120:123], v[112:115]\n"
"v_mfma_f32_16x16x32_bf16 v[196:199], a[32:35], v[120:123], v[196:199]\n"
"v_mfma_f32_16x16x32_bf16 v[200:203], a[36:39], v[120:123], v[200:203]\n"
"ds_write_b128 v3, v[48:51] offset:2048\n"
"v_mfma_f32_16x16x32_bf16 v[24:27], a[40:43], v[120:123], v[24:27]\n"
"buffer_load_dwordx4 v[28:31], v32, s[4:7], s98 offen offset:1024 sc1\n"
"v_mfma_f32_16x16x32_bf16 v[204:207], a[44:47], v[120:123], v[204:207]\n"
"s_waitcnt vmcnt(29)\n"
"v_mfma_f32_16x16x32_bf16 v[108:111], a[48:51], v[124:127], v[108:111]\n"
"ds_write_b128 v3, v[52:55] offset:3072\n"
"v_mfma_f32_16x16x32_bf16 v[112:115], a[52:55], v[124:127], v[112:115]\n"
"v_mfma_f32_16x16x32_bf16 v[196:199], a[56:59], v[124:127], v[196:199]\n"
"v_mfma_f32_16x16x32_bf16 v[200:203], a[60:63], v[124:127], v[200:203]\n"
"ds_write_b128 v3, v[56:59] offset:4096\n"
"v_mfma_f32_16x16x32_bf16 v[24:27], a[64:67], v[124:127], v[24:27]\n"
"buffer_load_dwordx4 v[48:51], v33, s[4:7], s98 offen sc1\n"
"v_mfma_f32_16x16x32_bf16 v[204:207], a[68:71], v[124:127], v[204:207]\n"
"v_mfma_f32_16x16x32_bf16 v[108:111], a[72:75], v[128:131], v[108:111]\n"
"ds_write_b128 v3, v[60:63] offset:5120\n"
"v_mfma_f32_16x16x32_bf16 v[112:115], a[76:79], v[128:131], v[112:115]\n"
"s_waitcnt vmcnt(14)\n"
"v_add_f32_e32 v254, v254, v252\n"
"v_mfma_f32_16x16x32_bf16 v[196:199], a[80:83], v[128:131], v[196:199]\n"
"v_add_f32_e32 v255, v255, v253\n"
"v_add_f32_e32 v250, v250, v248\n"
"v_mfma_f32_16x16x32_bf16 v[200:203], a[84:87], v[128:131], v[200:203]\n"
"v_add_f32_e32 v251, v251, v249\n"
"v_add_f32_e32 v254, v254, v250\n"
"v_mfma_f32_16x16x32_bf16 v[24:27], a[88:91], v[128:131], v[24:27]\n"
"buffer_load_dwordx4 v[52:55], v33, s[4:7], s98 offen offset:1024 sc1\n"
"v_mfma_f32_16x16x32_bf16 v[204:207], a[92:95], v[128:131], v[204:207]\n"
"v_add_f32_e32 v255, v255, v251\n"
"v_mfma_f32_16x16x32_bf16 v[108:111], a[96:99], v[132:135], v[108:111]\n"
"s_nop 0\n"
"v_permlane16_swap_b32_e32 v254, v255\n"
"v_add_f32_e32 v254, v254, v255\n"
"v_mfma_f32_16x16x32_bf16 v[112:115], a[100:103], v[132:135], v[112:115]\n"
"v_mov_b32_e32 v255, v254\n"
"v_mfma_f32_16x16x32_bf16 v[196:199], a[104:107], v[132:135], v[196:199]\n"
"s_nop 0\n"
"v_permlane32_swap_b32_e32 v254, v255\n"
"v_add_f32_e32 v254, v254, v255\n"
"v_mfma_f32_16x16x32_bf16 v[200:203], a[108:111], v[132:135], v[200:203]\n"
"v_mul_f32_e32 v254, 0x3a800000, v254\n"
"v_mov_b32_e32 v255, v254\n"
"v_mfma_f32_16x16x32_bf16 v[24:27], a[112:115], v[132:135], v[24:27]\n"
"buffer_load_dwordx4 v[56:59], v34, s[4:7], s98 offen sc1\n"
"v_mfma_f32_16x16x32_bf16 v[204:207], a[116:119], v[132:135], v[204:207]\n"
"s_waitcnt vmcnt(8)\n"
"s_waitcnt lgkmcnt(0)\n"
"s_barrier\n"
"v_mfma_f32_16x16x32_bf16 v[108:111], a[120:123], v[136:139], v[108:111]\n"
"v_permlane16_swap_b32_e32 v254, v255\n"
"v_cvt_pk_bf16_f32 v253, -v254, 0\n"
"v_mfma_f32_16x16x32_bf16 v[112:115], a[124:127], v[136:139], v[112:115]\n"
"v_fma_f32 v255, -v254, v254, v255\n"
"v_lshlrev_b32_e32 v253, 16, v253\n"
"ds_read_b64_tr_b16 v[60:61], v40\n"
"v_mfma_f32_16x16x32_bf16 v[196:199], v[68:71], v[136:139], v[196:199]\n"
"ds_read_b128 v[120:123], v4\n"
"ds_read_b128 v[124:127], v9\n"
"ds_read_b64_tr_b16 v[62:63], v40 offset:1024\n"
"v_mfma_f32_16x16x32_bf16 v[200:203], v[72:75], v[136:139], v[200:203]\n"
"ds_read_b128 v[128:131], v4 offset:1024\n"
"ds_read_b128 v[132:135], v9 offset:64\n"
"v_mfma_f32_16x16x32_bf16 v[24:27], v[76:79], v[136:139], v[24:27]\n"
"buffer_load_dwordx4 v[208:211], v34, s[4:7], s98 offen offset:1024 sc1\n"
"v_mfma_f32_16x16x32_bf16 v[204:207], v[80:83], v[136:139], v[204:207]\n"
"ds_read_b128 v[136:139], v4 offset:2048\n"
"v_mfma_f32_16x16x32_bf16 v[108:111], v[148:151], v[140:143], v[108:111]\n"
"ds_read_b128 v[212:215], v9 offset:128\n"
"v_max_f32_e32 v255, 0, v255\n"
"v_mfma_f32_16x16x32_bf16 v[112:115], v[152:155], v[140:143], v[112:115]\n"
"v_cvt_pk_bf16_f32 v255, v255, 0\n"
"v_lshlrev_b32_e32 v255, 16, v255\n"
"s_waitcnt lgkmcnt(4)\n"
"buffer_store_dwordx4 v[60:63], v0, s[4:7], s24 offen sc1\n"
"v_mfma_f32_16x16x32_bf16 v[196:199], v[156:159], v[140:143], v[196:199]\n"
"v_add_f32_e32 v255, 0x3727c5ac, v255\n"
"v_cvt_pk_bf16_f32 v255, v255, 0\n"
"v_mfma_f32_16x16x32_bf16 v[200:203], v[160:163], v[140:143], v[200:203]\n"
"v_lshlrev_b32_e32 v255, 16, v255\n"
"v_mfma_f32_16x16x32_bf16 v[24:27], v[164:167], v[140:143], v[24:27]\n"
"buffer_load_dwordx4 v[60:63], v35, s[4:7], s98 offen sc1\n"
"v_mfma_f32_16x16x32_bf16 v[204:207], v[168:171], v[140:143], v[204:207]\n"
"s_waitcnt lgkmcnt(0)\n"
"v_fmac_f32_e32 v120, v253, v124\n"
"v_mfma_f32_16x16x32_bf16 v[108:111], v[172:175], v[144:147], v[108:111]\n"
"v_fmac_f32_e32 v121, v253, v125\n"
"v_fmac_f32_e32 v122, v253, v126\n"
"v_mfma_f32_16x16x32_bf16 v[112:115], v[176:179], v[144:147], v[112:115]\n"
"v_fmac_f32_e32 v123, v253, v127\n"
"v_fmac_f32_e32 v128, v253, v132\n"
"v_mfma_f32_16x16x32_bf16 v[196:199], v[180:183], v[144:147], v[196:199]\n"
"v_fmac_f32_e32 v129, v253, v133\n"
"v_fmac_f32_e32 v130, v253, v134\n"
"v_mfma_f32_16x16x32_bf16 v[200:203], v[184:187], v[144:147], v[200:203]\n"
"v_fmac_f32_e32 v131, v253, v135\n"
"v_fmac_f32_e32 v136, v253, v212\n"
"v_mfma_f32_16x16x32_bf16 v[24:27], v[188:191], v[144:147], v[24:27]\n"
"buffer_load_dwordx4 v[124:127], v35, s[4:7], s98 offen offset:1024 sc1\n"
"ds_read_b64_tr_b16 v[132:133], v40 offset:4096\n"
"v_mfma_f32_16x16x32_bf16 v[204:207], v[192:195], v[144:147], v[204:207]\n"
"v_fmac_f32_e32 v137, v253, v213\n"
"ds_read_b64_tr_b16 v[134:135], v40 offset:5120\n"
"; @A G1 0.2\n"
"v_mfma_f32_16x16x32_bf16 v[140:143], a[0:3], v[64:67], 0\n"
"v_fmac_f32_e32 v138, v253, v214\n"
"v_fmac_f32_e32 v139, v253, v215\n"
"v_mfma_f32_16x16x32_bf16 v[144:147], a[4:7], v[64:67], 0\n"
"ds_read_b128 v[212:215], v4 offset:3072\n"
"ds_read_b128 v[216:219], v9 offset:192\n"
"v_mfma_f32_16x16x32_bf16 v[220:223], a[8:11], v[64:67], 0\n"
"ds_read_b128 v[224:227], v4 offset:4096\n"
"ds_read_b128 v[228:231], v9 offset:256\n"
"v_mfma_f32_16x16x32_bf16 v[232:235], a[12:15], v[64:67], 0\n"
"ds_write_b128 v3, v[108:111] offset:6144\n"
"v_mfma_f32_16x16x32_bf16 v[108:111], a[16:19], v[64:67], 0\n"
"s_add_u32 s98, s52, 0x48000\n"
"ds_read_b128 v[236:239], v4 offset:5120\n"
"s_waitcnt lgkmcnt(6)\n"
"s_add_u32 s97, s24, 0x1000\n"
"v_mfma_f32_16x16x32_bf16 v[240:243], a[20:23], v[64:67], 0\n"
"buffer_load_dwordx4 v[64:67], v32, s[4:7], s98 offen sc1\n"
"buffer_store_dwordx4 v[132:135], v0, s[4:7], s97 offen sc1\n"
"v_mfma_f32_16x16x32_bf16 v[140:143], a[24:27], v[84:87], v[140:143]\n"
"ds_write_b128 v3, v[112:115] offset:7168\n"
"v_mfma_f32_16x16x32_bf16 v[144:147], a[28:31], v[84:87], v[144:147]\n"
"ds_read_b128 v[112:115], v9 offset:320\n"
"v_mfma_f32_16x16x32_bf16 v[220:223], a[32:35], v[84:87], v[220:223]\n"
"v_sqrt_f32_e32 v255, v255\n"
"v_mfma_f32_16x16x32_bf16 v[232:235], a[36:39], v[84:87], v[232:235]\n"
"ds_write_b128 v3, v[196:199] offset:8192\n"
"v_mfma_f32_16x16x32_bf16 v[108:111], a[40:43], v[84:87], v[108:111]\n"
"buffer_load_dwordx4 v[132:135], v32, s[4:7], s98 offen offset:1024 sc1\n"
"v_mfma_f32_16x16x32_bf16 v[240:243], a[44:47], v[84:87], v[240:243]\n"
"v_cvt_pk_bf16_f32 v255, v255, 0\n"
"v_mfma_f32_16x16x32_bf16 v[140:143], a[48:51], v[88:91], v[140:143]\n"
"ds_write_b128 v3, v[200:203] offset:9216\n"
"v_mfma_f32_16x16x32_bf16 v[144:147], a[52:55], v[88:91], v[144:147]\n"
"s_waitcnt lgkmcnt(2)\n"
"v_fmac_f32_e32 v212, v253, v216\n"
"v_mfma_f32_16x16x32_bf16 v[220:223], a[56:59], v[88:91], v[220:223]\n"
"v_fmac_f32_e32 v213, v253, v217\n"
"v_fmac_f32_e32 v214, v253, v218\n"
"v_mfma_f32_16x16x32_bf16 v[232:235], a[60:63], v[88:91], v[232:235]\n"
"ds_write_b128 v3, v[24:27] offset:10240\n"
"v_mfma_f32_16x16x32_bf16 v[108:111], a[64:67], v[88:91], v[108:111]\n"
"buffer_load_dwordx4 v[24:27], v33, s[4:7], s98 offen sc1\n"
"v_mfma_f32_16x16x32_bf16 v[240:243], a[68:71], v[88:91], v[240:243]\n"
"v_fmac_f32_e32 v215, v253, v219\n"
"v_fmac_f32_e32 v224, v253, v228\n"
"v_mfma_f32_16x16x32_bf16 v[140:143], a[72:75], v[92:95], v[140:143]\n"
"ds_write_b128 v3, v[204:207] offset:11264\n"
"v_mfma_f32_16x16x32_bf16 v[144:147], a[76:79], v[92:95], v[144:147]\n"
"v_fmac_f32_e32 v225, v253, v229\n"
"v_fmac_f32_e32 v226, v253, v230\n"
"v_mfma_f32_16x16x32_bf16 v[220:223], a[80:83], v[92:95], v[220:223]\n"
"v_fmac_f32_e32 v227, v253, v231\n"
"v_fmac_f32_e32 v236, v253, v112\n"
"v_mfma_f32_16x16x32_bf16 v[232:235], a[84:87], v[92:95], v[232:235]\n"
"v_fmac_f32_e32 v237, v253, v113\n"
"v_fmac_f32_e32 v238, v253, v114\n"
"v_mfma_f32_16x16x32_bf16 v[108:111], a[88:91], v[92:95], v[108:111]\n"
"buffer_load_dwordx4 v[84:87], v33, s[4:7], s98 offen offset:1024 sc1\n"
"ds_read_b64_tr_b16 v[88:89], v40 offset:8192\n"
"v_mfma_f32_16x16x32_bf16 v[240:243], a[92:95], v[92:95], v[240:243]\n"
"s_waitcnt vmcnt(16)\n"
"v_fmac_f32_e32 v239, v253, v115\n"
"ds_read_b64_tr_b16 v[90:91], v40 offset:9216\n"
"v_mfma_f32_16x16x32_bf16 v[140:143], a[96:99], v[96:99], v[140:143]\n"
"v_lshlrev_b32_e32 v255, 16, v255\n"
"v_mfma_f32_16x16x32_bf16 v[144:147], a[100:103], v[96:99], v[144:147]\n"
"v_rcp_f32_e32 v253, v255\n"
"v_mfma_f32_16x16x32_bf16 v[220:223], a[104:107], v[96:99], v[220:223]\n"
"v_mul_f32_e32 v255, 0x3e8293ee, v253\n"
"v_mfma_f32_16x16x32_bf16 v[232:235], a[108:111], v[96:99], v[232:235]\n"
"v_mfma_f32_16x16x32_bf16 v[108:111], a[112:115], v[96:99], v[108:111]\n"
"buffer_load_dwordx4 v[92:95], v34, s[4:7], s98 offen sc1\n"
"s_waitcnt lgkmcnt(0)\n"
"buffer_store_dwordx4 v[88:91], v0, s[4:7], s25 offen sc1\n"
"v_mfma_f32_16x16x32_bf16 v[240:243], a[116:119], v[96:99], v[240:243]\n"
"v_mfma_f32_16x16x32_bf16 v[140:143], a[120:123], v[44:47], v[140:143]\n"
"v_mfma_f32_16x16x32_bf16 v[144:147], a[124:127], v[44:47], v[144:147]\n"
"v_mfma_f32_16x16x32_bf16 v[220:223], v[68:71], v[44:47], v[220:223]\n"
"v_mfma_f32_16x16x32_bf16 v[232:235], v[72:75], v[44:47], v[232:235]\n"
"v_mfma_f32_16x16x32_bf16 v[108:111], v[76:79], v[44:47], v[108:111]\n"
"buffer_load_dwordx4 v[88:91], v34, s[4:7], s98 offen offset:1024 sc1\n"
"v_mfma_f32_16x16x32_bf16 v[240:243], v[80:83], v[44:47], v[240:243]\n"
"s_waitcnt vmcnt(17)\n"
"v_mfma_f32_16x16x32_bf16 v[140:143], v[148:151], v[100:103], v[140:143]\n"
"v_mfma_f32_16x16x32_bf16 v[144:147], v[152:155], v[100:103], v[144:147]\n"
"v_mfma_f32_16x16x32_bf16 v[220:223], v[156:159], v[100:103], v[220:223]\n"
"v_mfma_f32_16x16x32_bf16 v[232:235], v[160:163], v[100:103], v[232:235]\n"
"v_mfma_f32_16x16x32_bf16 v[108:111], v[164:167], v[100:103], v[108:111]\n"
"buffer_load_dwordx4 v[44:47], v35, s[4:7], s98 offen sc1\n"
"v_mfma_f32_16x16x32_bf16 v[240:243], v[168:171], v[100:103], v[240:243]\n"
"v_mfma_f32_16x16x32_bf16 v[140:143], v[172:175], v[104:107], v[140:143]\n"
"v_mfma_f32_16x16x32_bf16 v[144:147], v[176:179], v[104:107], v[144:147]\n"
"v_mfma_f32_16x16x32_bf16 v[220:223], v[180:183], v[104:107], v[220:223]\n"
"v_mfma_f32_16x16x32_bf16 v[232:235], v[184:187], v[104:107], v[232:235]\n"
"v_mfma_f32_16x16x32_bf16 v[108:111], v[188:191], v[104:107], v[108:111]\n"
"buffer_load_dwordx4 v[96:99], v35, s[4:7], s98 offen offset:1024 sc1\n"
"ds_read_b64_tr_b16 v[100:101], v40 offset:12288\n"
"v_mfma_f32_16x16x32_bf16 v[240:243], v[192:195], v[104:107], v[240:243]\n"
"s_waitcnt vmcnt(17)\n"
"ds_read_b64_tr_b16 v[102:103], v40 offset:13312\n"
"; @A G1 0.3\n"
"v_mfma_f32_16x16x32_bf16 v[120:123], a[0:3], v[116:119], v[120:123]\n"
"v_mfma_f32_16x16x32_bf16 v[128:131], a[4:7], v[116:119], v[128:131]\n"
"v_mfma_f32_16x16x32_bf16 v[136:139], a[8:11], v[116:119], v[136:139]\n"
"v_mfma_f32_16x16x32_bf16 v[212:215], a[12:15], v[116:119], v[212:215]\n"
"ds_write_b128 v3, v[140:143] offset:12288\n"
"v_mfma_f32_16x16x32_bf16 v[224:227], a[16:19], v[116:119], v[224:227]\n"
"s_add_u32 s98, s53, 0x48000\n"
"s_waitcnt lgkmcnt(1)\n"
"s_add_u32 s97, s25, 0x1000\n"
"buffer_store_dwordx4 v[100:103], v0, s[4:7], s97 offen sc1\n"
"v_mfma_f32_16x16x32_bf16 v[236:239], a[20:23], v[116:119], v[236:239]\n"
"buffer_load_dwordx4 v[100:103], v32, s[4:7], s98 offen sc1\n"
"v_mfma_f32_16x16x32_bf16 v[120:123], a[24:27], v[28:31], v[120:123]\n"
"ds_write_b128 v3, v[144:147] offset:13312\n"
"v_mfma_f32_16x16x32_bf16 v[128:131], a[28:31], v[28:31], v[128:131]\n"
"v_mfma_f32_16x16x32_bf16 v[136:139], a[32:35], v[28:31], v[136:139]\n"
"v_mfma_f32_16x16x32_bf16 v[212:215], a[36:39], v[28:31], v[212:215]\n"
"ds_write_b128 v3, v[220:223] offset:14336\n"
"v_mfma_f32_16x16x32_bf16 v[224:227], a[40:43], v[28:31], v[224:227]\n"
"buffer_load_dwordx4 v[104:107], v32, s[4:7], s98 offen offset:1024 sc1\n"
"v_mfma_f32_16x16x32_bf16 v[236:239], a[44:47], v[28:31], v[236:239]\n"
"s_waitcnt vmcnt(18)\n"
"v_mfma_f32_16x16x32_bf16 v[120:123], a[48:51], v[48:51], v[120:123]\n"
"ds_write_b128 v3, v[232:235] offset:15360\n"
"v_mfma_f32_16x16x32_bf16 v[128:131], a[52:55], v[48:51], v[128:131]\n"
"v_mfma_f32_16x16x32_bf16 v[136:139], a[56:59], v[48:51], v[136:139]\n"
"v_mfma_f32_16x16x32_bf16 v[212:215], a[60:63], v[48:51], v[212:215]\n"
"ds_write_b128 v3, v[108:111] offset:16384\n"
"v_mfma_f32_16x16x32_bf16 v[224:227], a[64:67], v[48:51], v[224:227]\n"
"buffer_load_dwordx4 v[28:31], v33, s[4:7], s98 offen sc1\n"
"v_mfma_f32_16x16x32_bf16 v[236:239], a[68:71], v[48:51], v[236:239]\n"
"v_mfma_f32_16x16x32_bf16 v[120:123], a[72:75], v[52:55], v[120:123]\n"
"ds_write_b128 v3, v[240:243] offset:17408\n"
"v_mfma_f32_16x16x32_bf16 v[128:131], a[76:79], v[52:55], v[128:131]\n"
"v_mfma_f32_16x16x32_bf16 v[136:139], a[80:83], v[52:55], v[136:139]\n"
"v_mfma_f32_16x16x32_bf16 v[212:215], a[84:87], v[52:55], v[212:215]\n"
"s_waitcnt lgkmcnt(0)\n"
"s_barrier\n"
"v_mfma_f32_16x16x32_bf16 v[224:227], a[88:91], v[52:55], v[224:227]\n"
"buffer_load_dwordx4 v[48:51], v33, s[4:7], s98 offen offset:1024 sc1\n"
"v_mfma_f32_16x16x32_bf16 v[236:239], a[92:95], v[52:55], v[236:239]\n"
"s_waitcnt vmcnt(18)\n"
"ds_read_b128 v[52:55], v5 offset:2048\n"
"v_mfma_f32_16x16x32_bf16 v[120:123], a[96:99], v[56:59], v[120:123]\n"
"ds_read_b128 v[108:111], v6 offset:2048\n"
"ds_read_b128 v[112:115], v5 offset:3072\n"
"v_mfma_f32_16x16x32_bf16 v[128:131], a[100:103], v[56:59], v[128:131]\n"
"ds_read_b128 v[116:119], v6 offset:3072\n"
"v_mfma_f32_16x16x32_bf16 v[136:139], a[104:107], v[56:59], v[136:139]\n"
"v_mfma_f32_16x16x32_bf16 v[212:215], a[108:111], v[56:59], v[212:215]\n"
"v_mfma_f32_16x16x32_bf16 v[224:227], a[112:115], v[56:59], v[224:227]\n"
"buffer_load_dwordx4 v[140:143], v34, s[4:7], s98 offen sc1\n"
"v_mfma_f32_16x16x32_bf16 v[236:239], a[116:119], v[56:59], v[236:239]\n"
"ds_read_b128 v[56:59], v5 offset:4096\n"
"ds_read_b128 v[144:147], v6 offset:4096\n"
"v_mfma_f32_16x16x32_bf16 v[120:123], a[120:123], v[208:211], v[120:123]\n"
"ds_read_b128 v[196:199], v5 offset:5120\n"
"ds_read_b128 v[200:203], v6 offset:5120\n"
"v_mfma_f32_16x16x32_bf16 v[128:131], a[124:127], v[208:211], v[128:131]\n"
"s_waitcnt lgkmcnt(4)\n"
"v_add_f32_e32 v52, v52, v108\n"
"v_mfma_f32_16x16x32_bf16 v[136:139], v[68:71], v[208:211], v[136:139]\n"
"v_add_f32_e32 v53, v53, v109\n"
"v_add_f32_e32 v54, v54, v110\n"
"v_mfma_f32_16x16x32_bf16 v[212:215], v[72:75], v[208:211], v[212:215]\n"
"ds_read_b128 v[204:207], v5\n"
"ds_read_b128 v[216:219], v6\n"
"v_mfma_f32_16x16x32_bf16 v[224:227], v[76:79], v[208:211], v[224:227]\n"
"buffer_load_dwordx4 v[220:223], v34, s[4:7], s98 offen offset:1024 sc1\n"
"v_mfma_f32_16x16x32_bf16 v[236:239], v[80:83], v[208:211], v[236:239]\n"
"s_waitcnt vmcnt(17)\n"
"ds_read_b128 v[208:211], v5 offset:1024\n"
"v_mfma_f32_16x16x32_bf16 v[120:123], v[148:151], v[60:63], v[120:123]\n"
"ds_read_b128 v[228:231], v6 offset:1024\n"
"v_add_f32_e32 v55, v55, v111\n"
"v_mfma_f32_16x16x32_bf16 v[128:131], v[152:155], v[60:63], v[128:131]\n"
"v_add_f32_e32 v112, v112, v116\n"
"v_add_f32_e32 v113, v113, v117\n"
"v_mfma_f32_16x16x32_bf16 v[136:139], v[156:159], v[60:63], v[136:139]\n"
"v_add_f32_e32 v114, v114, v118\n"
"v_add_f32_e32 v115, v115, v119\n"
"v_mfma_f32_16x16x32_bf16 v[212:215], v[160:163], v[60:63], v[212:215]\n"
"s_waitcnt lgkmcnt(4)\n"
"v_add_f32_e32 v56, v56, v144\n"
"v_mfma_f32_16x16x32_bf16 v[224:227], v[164:167], v[60:63], v[224:227]\n"
"buffer_load_dwordx4 v[108:111], v35, s[4:7], s98 offen sc1\n"
"v_mfma_f32_16x16x32_bf16 v[236:239], v[168:171], v[60:63], v[236:239]\n"
"v_add_f32_e32 v57, v57, v145\n"
"v_add_f32_e32 v58, v58, v146\n"
"v_mfma_f32_16x16x32_bf16 v[120:123], v[172:175], v[124:127], v[120:123]\n"
"v_add_f32_e32 v59, v59, v147\n"
"v_add_f32_e32 v196, v196, v200\n"
"v_mfma_f32_16x16x32_bf16 v[128:131], v[176:179], v[124:127], v[128:131]\n"
"v_add_f32_e32 v197, v197, v201\n"
"v_add_f32_e32 v198, v198, v202\n"
"v_mfma_f32_16x16x32_bf16 v[136:139], v[180:183], v[124:127], v[136:139]\n"
"v_add_f32_e32 v199, v199, v203\n"
"s_waitcnt lgkmcnt(0)\n"
"v_mfma_f32_16x16x32_bf16 v[212:215], v[184:187], v[124:127], v[212:215]\n"
"v_add_f32_e32 v204, v204, v216\n"
"v_add_f32_e32 v205, v205, v217\n"
"v_mfma_f32_16x16x32_bf16 v[224:227], v[188:191], v[124:127], v[224:227]\n"
"buffer_load_dwordx4 v[60:63], v35, s[4:7], s98 offen offset:1024 sc1\n"
"v_mfma_f32_16x16x32_bf16 v[236:239], v[192:195], v[124:127], v[236:239]\n"
"v_add_f32_e32 v206, v206, v218\n"
"v_add_f32_e32 v207, v207, v219\n"
"v_add_f32_e32 v208, v208, v228\n"
"v_add_f32_e32 v209, v209, v229\n"
"v_add_f32_e32 v210, v210, v230\n"
"v_add_f32_e32 v211, v211, v231\n"
".Lmk_z1:\n"
"s_waitcnt vmcnt(16)\n"
"; @A G1 1.0\n"
"v_mfma_f32_16x16x32_bf16 v[116:119], a[0:3], v[64:67], 0\n"
"s_add_u32 s98, s62, 0x4800\n"
"v_mfma_f32_16x16x32_bf16 v[124:127], a[4:7], v[64:67], 0\n"
"buffer_load_dwordx2 v[250:251], v10, s[4:7], s98 offen sc1\n"
"v_mfma_f32_16x16x32_bf16 v[144:147], a[8:11], v[64:67], 0\n"
"buffer_load_dwordx2 v[248:249], v10, s[4:7], s98 offen offset:128 sc1\n"
"v_mfma_f32_16x16x32_bf16 v[200:203], a[12:15], v[64:67], 0\n"
"buffer_load_dwordx2 v[246:247], v10, s[4:7], s98 offen offset:256 sc1\n"
"v_mfma_f32_16x16x32_bf16 v[216:219], a[16:19], v[64:67], 0\n"
"s_add_u32 s97, s54, 0x48000\n"
"v_add_f32_e32 v136, v136, v52\n"
"v_mfma_f32_16x16x32_bf16 v[228:231], a[20:23], v[64:67], 0\n"
"buffer_load_dwordx4 v[64:67], v32, s[4:7], s97 offen sc1\n"
"v_mfma_f32_16x16x32_bf16 v[116:119], a[24:27], v[132:135], v[116:119]\n"
"buffer_load_dwordx2 v[244:245], v10, s[4:7], s98 offen offset:384 sc1\n"
"v_mfma_f32_16x16x32_bf16 v[124:127], a[28:31], v[132:135], v[124:127]\n"
"v_add_f32_e32 v137, v137, v53\n"
"v_add_f32_e32 v138, v138, v54\n"
"v_mfma_f32_16x16x32_bf16 v[144:147], a[32:35], v[132:135], v[144:147]\n"
"v_add_f32_e32 v139, v139, v55\n"
"v_add_f32_e32 v212, v212, v112\n"
"v_mfma_f32_16x16x32_bf16 v[200:203], a[36:39], v[132:135], v[200:203]\n"
"v_add_f32_e32 v213, v213, v113\n"
"v_add_f32_e32 v214, v214, v114\n"
"v_mfma_f32_16x16x32_bf16 v[216:219], a[40:43], v[132:135], v[216:219]\n"
"buffer_load_dwordx4 v[52:55], v32, s[4:7], s97 offen offset:1024 sc1\n"
"v_mfma_f32_16x16x32_bf16 v[228:231], a[44:47], v[132:135], v[228:231]\n"
"s_waitcnt vmcnt(20)\n"
"s_waitcnt vmcnt(14)\n"
"v_mfma_f32_16x16x32_bf16 v[116:119], a[48:51], v[24:27], v[116:119]\n"
"buffer_store_dwordx2 v[18:19], v11, s[4:7], s68 offen offset:96 sc1\n"
"v_mfma_f32_16x16x32_bf16 v[124:127], a[52:55], v[24:27], v[124:127]\n"
"v_add_f32_e32 v215, v215, v115\n"
"v_mul_f32_e32 v136, v136, v253\n"
"v_mfma_f32_16x16x32_bf16 v[144:147], a[56:59], v[24:27], v[144:147]\n"
"v_mul_f32_e32 v137, v137, v253\n"
"v_mul_f32_e32 v138, v138, v253\n"
"v_mfma_f32_16x16x32_bf16 v[200:203], a[60:63], v[24:27], v[200:203]\n"
"v_mul_f32_e32 v139, v139, v253\n"
"v_mul_f32_e32 v212, v212, v253\n"
"v_mfma_f32_16x16x32_bf16 v[216:219], a[64:67], v[24:27], v[216:219]\n"
"buffer_load_dwordx4 v[112:115], v33, s[4:7], s97 offen sc1\n"
"v_mfma_f32_16x16x32_bf16 v[228:231], a[68:71], v[24:27], v[228:231]\n"
"v_mul_f32_e32 v213, v213, v253\n"
"v_mul_f32_e32 v214, v214, v253\n"
"v_mfma_f32_16x16x32_bf16 v[116:119], a[72:75], v[84:87], v[116:119]\n"
"v_mul_f32_e32 v215, v215, v253\n"
"v_cvt_pk_bf16_f32 v24, v136, v137\n"
"v_mfma_f32_16x16x32_bf16 v[124:127], a[76:79], v[84:87], v[124:127]\n"
"v_cvt_pk_bf16_f32 v25, v138, v139\n"
"v_cvt_pk_bf16_f32 v26, v212, v213\n"
"v_mfma_f32_16x16x32_bf16 v[144:147], a[80:83], v[84:87], v[144:147]\n"
"v_cvt_pk_bf16_f32 v27, v214, v215\n"
"v_mfma_f32_16x16x32_bf16 v[200:203], a[84:87], v[84:87], v[200:203]\n"
"ds_write_b128 v7, v[24:27] offset:24576\n"
"v_mfma_f32_16x16x32_bf16 v[216:219], a[88:91], v[84:87], v[216:219]\n"
"buffer_load_dwordx4 v[132:135], v33, s[4:7], s97 offen offset:1024 sc1\n"
"v_mfma_f32_16x16x32_bf16 v[228:231], a[92:95], v[84:87], v[228:231]\n"
"v_add_f32_e32 v224, v224, v56\n"
"v_mfma_f32_16x16x32_bf16 v[116:119], a[96:99], v[92:95], v[116:119]\n"
"v_add_f32_e32 v225, v225, v57\n"
"v_add_f32_e32 v226, v226, v58\n"
"v_mfma_f32_16x16x32_bf16 v[124:127], a[100:103], v[92:95], v[124:127]\n"
"v_add_f32_e32 v227, v227, v59\n"
"v_add_f32_e32 v236, v236, v196\n"
"v_mfma_f32_16x16x32_bf16 v[144:147], a[104:107], v[92:95], v[144:147]\n"
"v_add_f32_e32 v237, v237, v197\n"
"v_add_f32_e32 v238, v238, v198\n"
"v_mfma_f32_16x16x32_bf16 v[200:203], a[108:111], v[92:95], v[200:203]\n"
"v_add_f32_e32 v239, v239, v199\n"
"v_mul_f32_e32 v224, v224, v253\n"
"v_mfma_f32_16x16x32_bf16 v[216:219], a[112:115], v[92:95], v[216:219]\n"
"buffer_load_dwordx4 v[56:59], v34, s[4:7], s97 offen sc1\n"
"v_mfma_f32_16x16x32_bf16 v[228:231], a[116:119], v[92:95], v[228:231]\n"
"v_mul_f32_e32 v225, v225, v253\n"
"v_mul_f32_e32 v226, v226, v253\n"
"v_mfma_f32_16x16x32_bf16 v[116:119], a[120:123], v[88:91], v[116:119]\n"
"v_mul_f32_e32 v227, v227, v253\n"
"v_mul_f32_e32 v236, v236, v253\n"
"v_mfma_f32_16x16x32_bf16 v[124:127], a[124:127], v[88:91], v[124:127]\n"
"v_mul_f32_e32 v237, v237, v253\n"
"v_mul_f32_e32 v238, v238, v253\n"
"v_mfma_f32_16x16x32_bf16 v[144:147], v[68:71], v[88:91], v[144:147]\n"
"v_mul_f32_e32 v239, v239, v253\n"
"v_cvt_pk_bf16_f32 v84, v224, v225\n"
"v_mfma_f32_16x16x32_bf16 v[200:203], v[72:75], v[88:91], v[200:203]\n"
"v_cvt_pk_bf16_f32 v85, v226, v227\n"
"v_cvt_pk_bf16_f32 v86, v236, v237\n"
"v_mfma_f32_16x16x32_bf16 v[216:219], v[76:79], v[88:91], v[216:219]\n"
"buffer_load_dwordx4 v[92:95], v34, s[4:7], s97 offen offset:1024 sc1\n"
"v_mfma_f32_16x16x32_bf16 v[228:231], v[80:83], v[88:91], v[228:231]\n"
"v_cvt_pk_bf16_f32 v87, v238, v239\n"
"v_mfma_f32_16x16x32_bf16 v[116:119], v[148:151], v[44:47], v[116:119]\n"
"ds_write_b128 v7, v[84:87] offset:57344\n"
"v_mfma_f32_16x16x32_bf16 v[124:127], v[152:155], v[44:47], v[124:127]\n"
"v_add_f32_e32 v120, v120, v204\n"
"v_add_f32_e32 v121, v121, v205\n"
"v_mfma_f32_16x16x32_bf16 v[144:147], v[156:159], v[44:47], v[144:147]\n"
"v_add_f32_e32 v122, v122, v206\n"
"v_add_f32_e32 v123, v123, v207\n"
"v_mfma_f32_16x16x32_bf16 v[200:203], v[160:163], v[44:47], v[200:203]\n"
"v_add_f32_e32 v128, v128, v208\n"
"v_add_f32_e32 v129, v129, v209\n"
"v_mfma_f32_16x16x32_bf16 v[216:219], v[164:167], v[44:47], v[216:219]\n"
"buffer_load_dwordx4 v[88:91], v35, s[4:7], s97 offen sc1\n"
"v_mfma_f32_16x16x32_bf16 v[228:231], v[168:171], v[44:47], v[228:231]\n"
"v_add_f32_e32 v130, v130, v210\n"
"v_add_f32_e32 v131, v131, v211\n"
"v_mfma_f32_16x16x32_bf16 v[116:119], v[172:175], v[96:99], v[116:119]\n"
"v_mul_f32_e32 v120, v120, v255\n"
"v_mul_f32_e32 v121, v121, v255\n"
"v_mfma_f32_16x16x32_bf16 v[124:127], v[176:179], v[96:99], v[124:127]\n"
"v_mul_f32_e32 v122, v122, v255\n"
"v_mul_f32_e32 v123, v123, v255\n"
"v_mfma_f32_16x16x32_bf16 v[144:147], v[180:183], v[96:99], v[144:147]\n"
"v_mul_f32_e32 v128, v128, v255\n"
"v_mul_f32_e32 v129, v129, v255\n"
"v_mfma_f32_16x16x32_bf16 v[200:203], v[184:187], v[96:99], v[200:203]\n"
"v_mul_f32_e32 v130, v130, v255\n"
"v_mul_f32_e32 v131, v131, v255\n"
"v_mfma_f32_16x16x32_bf16 v[216:219], v[188:191], v[96:99], v[216:219]\n"
"buffer_load_dwordx4 v[44:47], v35, s[4:7], s97 offen offset:1024 sc1\n"
"v_mfma_f32_16x16x32_bf16 v[228:231], v[192:195], v[96:99], v[228:231]\n"
"s_waitcnt vmcnt(19)\n"
"v_cvt_pk_bf16_f32 v96, v120, v121\n"
"; @A G1 1.1\n"
"v_mfma_f32_16x16x32_bf16 v[136:139], a[0:3], v[100:103], 0\n"
"v_cvt_pk_bf16_f32 v97, v122, v123\n"
"v_cvt_pk_bf16_f32 v98, v128, v129\n"
"v_mfma_f32_16x16x32_bf16 v[120:123], a[4:7], v[100:103], 0\n"
"v_cvt_pk_bf16_f32 v99, v130, v131\n"
"v_mov_b32_e32 v128, -1\n"
"v_mfma_f32_16x16x32_bf16 v[196:199], a[8:11], v[100:103], 0\n"
"ds_write_b128 v2, v[96:99] offset:49152\n"
"v_mfma_f32_16x16x32_bf16 v[96:99], a[12:15], v[100:103], 0\n"
"ds_write_b128 v3, v[116:119]\n"
"v_mfma_f32_16x16x32_bf16 v[116:119], a[16:19], v[100:103], 0\n"
"s_add_u32 s98, s55, 0x48000\n"
"v_mov_b32_e32 v129, -1\n"
"v_mfma_f32_16x16x32_bf16 v[204:207], a[20:23], v[100:103], 0\n"
"buffer_load_dwordx4 v[100:103], v32, s[4:7], s98 offen sc1\n"
"v_mfma_f32_16x16x32_bf16 v[136:139], a[24:27], v[104:107], v[136:139]\n"
"ds_write_b128 v3, v[124:127] offset:1024\n"
"v_mfma_f32_16x16x32_bf16 v[120:123], a[28:31], v[104:107], v[120:123]\n"
"v_mov_b32_e32 v130, -1\n"
"v_mov_b32_e32 v131, -1\n"
"v_mfma_f32_16x16x32_bf16 v[196:199], a[32:35], v[104:107], v[196:199]\n"
"ds_read_b64_tr_b16 v[124:125], v36\n"
"ds_read_b64_tr_b16 v[126:127], v36 offset:2048\n"
"s_mov_b32 s96, 0x30003\n"
"v_mfma_f32_16x16x32_bf16 v[96:99], a[36:39], v[104:107], v[96:99]\n"
"ds_write_b128 v3, v[144:147] offset:2048\n"
"v_mfma_f32_16x16x32_bf16 v[116:119], a[40:43], v[104:107], v[116:119]\n"
"buffer_load_dwordx4 v[144:147], v32, s[4:7], s98 offen offset:1024 sc1\n"
"s_mov_b32 s97, 0x30003\n"
"v_mfma_f32_16x16x32_bf16 v[204:207], a[44:47], v[104:107], v[204:207]\n"
"s_waitcnt vmcnt(19)\n"
"v_mov_b32_e32 v107, 0x3f803f80\n"
"v_cndmask_b32_e64 v104, 0, v107, s[96:97]\n"
"v_mfma_f32_16x16x32_bf16 v[136:139], a[48:51], v[28:31], v[136:139]\n"
"ds_write_b128 v3, v[200:203] offset:3072\n"
"v_mfma_f32_16x16x32_bf16 v[120:123], a[52:55], v[28:31], v[120:123]\n"
"s_add_u32 s95, s67, 0\n"
"v_cndmask_b32_e64 v105, 0, v107, s[96:97]\n"
"v_cndmask_b32_e64 v106, 0, v107, s[96:97]\n"
"v_mfma_f32_16x16x32_bf16 v[196:199], a[56:59], v[28:31], v[196:199]\n"
"buffer_load_dwordx2 v[254:255], v12, s[4:7], s95 offen sc1\n"
"s_add_u32 s94, s64, 0x2000\n"
"v_mfma_f32_16x16x32_bf16 v[96:99], a[60:63], v[28:31], v[96:99]\n"
"ds_write_b128 v3, v[216:219] offset:4096\n"
"v_mfma_f32_16x16x32_bf16 v[116:119], a[64:67], v[28:31], v[116:119]\n"
"buffer_load_dwordx4 v[200:203], v33, s[4:7], s98 offen sc1\n"
"v_cndmask_b32_e64 v107, 0, v107, s[96:97]\n"
"v_mfma_f32_16x16x32_bf16 v[204:207], a[68:71], v[28:31], v[204:207]\n"
"buffer_store_dwordx4 v[24:27], v7, s[4:7], s94 offen sc1\n"
"v_mfma_f32_16x16x32_bf16 v[136:139], a[72:75], v[48:51], v[136:139]\n"
"ds_write_b128 v3, v[228:231] offset:5120\n"
"v_mfma_f32_16x16x32_bf16 v[120:123], a[76:79], v[48:51], v[120:123]\n"
"s_waitcnt lgkmcnt(4)\n"
"v_mfma_f32_16x16x32_bf16 v[24:27], v[124:127], v[104:107], 0\n"
"buffer_store_dwordx4 v[124:127], v0, s[4:7], s20 offen sc1\n"
"v_mfma_f32_16x16x32_bf16 v[196:199], a[80:83], v[48:51], v[196:199]\n"
"ds_read_b64_tr_b16 v[28:29], v36 offset:4096\n"
"ds_read_b64_tr_b16 v[30:31], v36 offset:6144\n"
"s_mov_b32 s96, 0x30003\n"
"v_mfma_f32_16x16x32_bf16 v[96:99], a[84:87], v[48:51], v[96:99]\n"
"s_add_u32 s94, s64, 0x6000\n"
"s_mov_b32 s97, 0x30003\n"
"v_mov_b32_e32 v107, 0x3f803f80\n"
"v_mfma_f32_16x16x32_bf16 v[116:119], a[88:91], v[48:51], v[116:119]\n"
"buffer_load_dwordx4 v[124:127], v33, s[4:7], s98 offen offset:1024 sc1\n"
"v_cndmask_b32_e64 v104, 0, v107, s[96:97]\n"
"v_mfma_f32_16x16x32_bf16 v[204:207], a[92:95], v[48:51], v[204:207]\n"
"s_waitcnt vmcnt(22)\n"
"buffer_store_dwordx4 v[84:87], v7, s[4:7], s94 offen sc1\n"
"v_mfma_f32_16x16x32_bf16 v[136:139], a[96:99], v[140:143], v[136:139]\n"
"v_cndmask_b32_e64 v105, 0, v107, s[96:97]\n"
"v_cndmask_b32_e64 v106, 0, v107, s[96:97]\n"
"v_cndmask_b32_e64 v107, 0, v107, s[96:97]\n"
"v_mfma_f32_16x16x32_bf16 v[120:123], a[100:103], v[140:143], v[120:123]\n"
"s_waitcnt lgkmcnt(0)\n"
"v_mfma_f32_16x16x32_bf16 v[24:27], v[28:31], v[104:107], v[24:27]\n"
"s_add_u32 s97, s20, 0x1000\n"
"buffer_store_dwordx4 v[28:31], v0, s[4:7], s97 offen offset:2048 sc1\n"
"v_mfma_f32_16x16x32_bf16 v[196:199], a[104:107], v[140:143], v[196:199]\n"
"v_mfma_f32_16x16x32_bf16 v[96:99], a[108:111], v[140:143], v[96:99]\n"
"v_mfma_f32_16x16x32_bf16 v[116:119], a[112:115], v[140:143], v[116:119]\n"
"buffer_load_dwordx4 v[28:31], v34, s[4:7], s98 offen sc1\n"
"v_mfma_f32_16x16x32_bf16 v[204:207], a[116:119], v[140:143], v[204:207]\n"
"s_waitcnt lgkmcnt(0)\n"
"s_barrier\n"
"v_mfma_f32_16x16x32_bf16 v[136:139], a[120:123], v[220:223], v[136:139]\n"
"s_waitcnt vmcnt(18)\n"
"v_add_f32_e32 v250, v250, v248\n"
"v_mfma_f32_16x16x32_bf16 v[120:123], a[124:127], v[220:223], v[120:123]\n"
"v_add_f32_e32 v251, v251, v249\n"
"v_add_f32_e32 v246, v246, v244\n"
"v_mfma_f32_16x16x32_bf16 v[196:199], v[68:71], v[220:223], v[196:199]\n"
"v_add_f32_e32 v247, v247, v245\n"
"v_add_f32_e32 v250, v250, v246\n"
"v_mfma_f32_16x16x32_bf16 v[96:99], v[72:75], v[220:223], v[96:99]\n"
"v_add_f32_e32 v251, v251, v247\n"
"v_mfma_f32_16x16x32_bf16 v[116:119], v[76:79], v[220:223], v[116:119]\n"
"buffer_load_dwordx4 v[48:51], v34, s[4:7], s98 offen offset:1024 sc1\n"
"v_mfma_f32_16x16x32_bf16 v[204:207], v[80:83], v[220:223], v[204:207]\n"
"v_permlane16_swap_b32_e32 v250, v251\n"
"v_mfma_f32_16x16x32_bf16 v[136:139], v[148:151], v[108:111], v[136:139]\n"
"v_add_f32_e32 v250, v250, v251\n"
"v_mov_b32_e32 v251, v250\n"
"ds_read_b64_tr_b16 v[84:85], v37\n"
"v_mfma_f32_16x16x32_bf16 v[120:123], v[152:155], v[108:111], v[120:123]\n"
"v_permlane32_swap_b32_e32 v250, v251\n"
"v_add_f32_e32 v250, v250, v251\n"
"ds_read_b64_tr_b16 v[86:87], v37 offset:2048\n"
"v_mfma_f32_16x16x32_bf16 v[196:199], v[156:159], v[108:111], v[196:199]\n"
"v_mul_f32_e32 v250, 0x3a800000, v250\n"
"v_mov_b32_e32 v251, v250\n"
"s_mov_b32 s96, 0xc000c\n"
"v_mfma_f32_16x16x32_bf16 v[96:99], v[160:163], v[108:111], v[96:99]\n"
"v_permlane16_swap_b32_e32 v250, v251\n"
"v_cvt_pk_bf16_f32 v253, -v250, 0\n"
"s_mov_b32 s97, 0xc000c\n"
"v_mfma_f32_16x16x32_bf16 v[116:119], v[164:167], v[108:111], v[116:119]\n"
"buffer_load_dwordx4 v[104:107], v35, s[4:7], s98 offen sc1\n"
"v_mov_b32_e32 v143, 0x3f803f80\n"
"v_mfma_f32_16x16x32_bf16 v[204:207], v[168:171], v[108:111], v[204:207]\n"
"v_fma_f32 v251, -v250, v250, v251\n"
"v_lshlrev_b32_e32 v253, 16, v253\n"
"v_cndmask_b32_e64 v140, 0, v143, s[96:97]\n"
"v_mfma_f32_16x16x32_bf16 v[136:139], v[172:175], v[60:63], v[136:139]\n"
"ds_read_b128 v[108:111], v4\n"
"ds_read_b128 v[208:211], v9\n"
"v_cndmask_b32_e64 v141, 0, v143, s[96:97]\n"
"v_mfma_f32_16x16x32_bf16 v[120:123], v[176:179], v[60:63], v[120:123]\n"
"ds_read_b128 v[212:215], v4 offset:1024\n"
"ds_read_b128 v[216:219], v9 offset:64\n"
"v_cndmask_b32_e64 v142, 0, v143, s[96:97]\n"
"v_mfma_f32_16x16x32_bf16 v[196:199], v[180:183], v[60:63], v[196:199]\n"
"ds_read_b128 v[220:223], v4 offset:2048\n"
"ds_read_b128 v[224:227], v9 offset:128\n"
"v_cndmask_b32_e64 v143, 0, v143, s[96:97]\n"
"v_mfma_f32_16x16x32_bf16 v[96:99], v[184:187], v[60:63], v[96:99]\n"
"v_max_f32_e32 v251, 0, v251\n"
"v_cvt_pk_bf16_f32 v251, v251, 0\n"
"s_waitcnt lgkmcnt(6)\n"
"v_mfma_f32_16x16x32_bf16 v[24:27], v[84:87], v[140:143], v[24:27]\n"
"buffer_store_dwordx4 v[84:87], v0, s[4:7], s21 offen sc1\n"
"v_mfma_f32_16x16x32_bf16 v[116:119], v[188:191], v[60:63], v[116:119]\n"
"buffer_load_dwordx4 v[84:87], v35, s[4:7], s98 offen offset:1024 sc1\n"
"v_mfma_f32_16x16x32_bf16 v[204:207], v[192:195], v[60:63], v[204:207]\n"
"s_waitcnt vmcnt(21)\n"
"v_lshlrev_b32_e32 v251, 16, v251\n"
"; @A G1 1.2\n"
"v_mfma_f32_16x16x32_bf16 v[60:63], a[0:3], v[64:67], 0\n"
"v_add_f32_e32 v251, 0x3727c5ac, v251\n"
"v_cvt_pk_bf16_f32 v251, v251, 0\n"
"v_mfma_f32_16x16x32_bf16 v[140:143], a[4:7], v[64:67], 0\n"
"s_waitcnt vmcnt(11)\n"
"v_cmp_ne_u64_e32 vcc, v[254:255], v[18:19]\n"
"v_mfma_f32_16x16x32_bf16 v[228:231], a[8:11], v[64:67], 0\n"
"s_cbranch_vccnz .Lmk_f2s\n"
".Lmk_f2r:\n"
"s_waitcnt lgkmcnt(0)\n"
"v_mfma_f32_16x16x32_bf16 v[232:235], a[12:15], v[64:67], 0\n"
"ds_write_b128 v3, v[136:139] offset:6144\n"
"v_mfma_f32_16x16x32_bf16 v[136:139], a[16:19], v[64:67], 0\n"
"s_add_u32 s98, s52, 0x8000\n"
"v_fmac_f32_e32 v108, v253, v208\n"
"v_mfma_f32_16x16x32_bf16 v[236:239], a[20:23], v[64:67], 0\n"
"buffer_load_dwordx4 v[64:67], v32, s[4:7], s98 offen sc1\n"
"v_mfma_f32_16x16x32_bf16 v[60:63], a[24:27], v[52:55], v[60:63]\n"
"ds_write_b128 v3, v[120:123] offset:7168\n"
"v_mfma_f32_16x16x32_bf16 v[140:143], a[28:31], v[52:55], v[140:143]\n"
"v_fmac_f32_e32 v109, v253, v209\n"
"v_fmac_f32_e32 v110, v253, v210\n"
"v_mfma_f32_16x16x32_bf16 v[228:231], a[32:35], v[52:55], v[228:231]\n"
"v_fmac_f32_e32 v111, v253, v211\n"
"v_fmac_f32_e32 v212, v253, v216\n"
"v_mfma_f32_16x16x32_bf16 v[232:235], a[36:39], v[52:55], v[232:235]\n"
"ds_write_b128 v3, v[196:199] offset:8192\n"
"v_mfma_f32_16x16x32_bf16 v[136:139], a[40:43], v[52:55], v[136:139]\n"
"buffer_load_dwordx4 v[120:123], v32, s[4:7], s98 offen offset:1024 sc1\n"
"ds_read_b64_tr_b16 v[196:197], v37 offset:4096\n"
"v_mfma_f32_16x16x32_bf16 v[236:239], a[44:47], v[52:55], v[236:239]\n"
"v_fmac_f32_e32 v213, v253, v217\n"
"ds_read_b64_tr_b16 v[198:199], v37 offset:6144\n"
"v_mfma_f32_16x16x32_bf16 v[60:63], a[48:51], v[112:115], v[60:63]\n"
"ds_write_b128 v3, v[96:99] offset:9216\n"
"v_mfma_f32_16x16x32_bf16 v[140:143], a[52:55], v[112:115], v[140:143]\n"
"v_fmac_f32_e32 v214, v253, v218\n"
"v_fmac_f32_e32 v215, v253, v219\n"
"s_mov_b32 s96, 0xc000c\n"
"v_mfma_f32_16x16x32_bf16 v[228:231], a[56:59], v[112:115], v[228:231]\n"
"v_fmac_f32_e32 v220, v253, v224\n"
"v_fmac_f32_e32 v221, v253, v225\n"
"s_mov_b32 s97, 0xc000c\n"
"v_mfma_f32_16x16x32_bf16 v[232:235], a[60:63], v[112:115], v[232:235]\n"
"ds_write_b128 v3, v[116:119] offset:10240\n"
"v_mfma_f32_16x16x32_bf16 v[136:139], a[64:67], v[112:115], v[136:139]\n"
"buffer_load_dwordx4 v[52:55], v33, s[4:7], s98 offen sc1\n"
"v_mov_b32_e32 v99, 0x3f803f80\n"
"v_mfma_f32_16x16x32_bf16 v[236:239], a[68:71], v[112:115], v[236:239]\n"
"v_fmac_f32_e32 v222, v253, v226\n"
"v_fmac_f32_e32 v223, v253, v227\n"
"v_cndmask_b32_e64 v96, 0, v99, s[96:97]\n"
"v_mfma_f32_16x16x32_bf16 v[60:63], a[72:75], v[132:135], v[60:63]\n"
"ds_write_b128 v3, v[204:207] offset:11264\n"
"v_mfma_f32_16x16x32_bf16 v[140:143], a[76:79], v[132:135], v[140:143]\n"
"ds_read_b128 v[112:115], v4 offset:3072\n"
"ds_read_b128 v[116:119], v9 offset:192\n"
"v_cndmask_b32_e64 v97, 0, v99, s[96:97]\n"
"v_mfma_f32_16x16x32_bf16 v[228:231], a[80:83], v[132:135], v[228:231]\n"
"ds_read_b128 v[204:207], v4 offset:4096\n"
"ds_read_b128 v[208:211], v9 offset:256\n"
"v_cndmask_b32_e64 v98, 0, v99, s[96:97]\n"
"v_mfma_f32_16x16x32_bf16 v[232:235], a[84:87], v[132:135], v[232:235]\n"
"ds_read_b128 v[216:219], v4 offset:5120\n"
"ds_read_b128 v[224:227], v9 offset:320\n"
"v_cndmask_b32_e64 v99, 0, v99, s[96:97]\n"
"v_mfma_f32_16x16x32_bf16 v[136:139], a[88:91], v[132:135], v[136:139]\n"
"buffer_load_dwordx4 v[240:243], v33, s[4:7], s98 offen offset:1024 sc1\n"
"s_waitcnt lgkmcnt(9)\n"
"v_mfma_f32_16x16x32_bf16 v[24:27], v[196:199], v[96:99], v[24:27]\n"
"s_add_u32 s97, s21, 0x1000\n"
"v_mfma_f32_16x16x32_bf16 v[236:239], a[92:95], v[132:135], v[236:239]\n"
"v_lshlrev_b32_e32 v251, 16, v251\n"
"buffer_store_dwordx4 v[196:199], v0, s[4:7], s97 offen offset:2048 sc1\n"
"v_mfma_f32_16x16x32_bf16 v[60:63], a[96:99], v[56:59], v[60:63]\n"
"v_sqrt_f32_e32 v251, v251\n"
"v_mfma_f32_16x16x32_bf16 v[140:143], a[100:103], v[56:59], v[140:143]\n"
"v_cvt_pk_bf16_f32 v251, v251, 0\n"
"v_lshlrev_b32_e32 v251, 16, v251\n"
"v_mfma_f32_16x16x32_bf16 v[228:231], a[104:107], v[56:59], v[228:231]\n"
"s_waitcnt lgkmcnt(0)\n"
"v_fmac_f32_e32 v112, v253, v116\n"
"v_mfma_f32_16x16x32_bf16 v[232:235], a[108:111], v[56:59], v[232:235]\n"
"v_fmac_f32_e32 v113, v253, v117\n"
"v_fmac_f32_e32 v114, v253, v118\n"
"v_mfma_f32_16x16x32_bf16 v[136:139], a[112:115], v[56:59], v[136:139]\n"
"buffer_load_dwordx4 v[96:99], v34, s[4:7], s98 offen sc1\n"
"v_mfma_f32_16x16x32_bf16 v[236:239], a[116:119], v[56:59], v[236:239]\n"
"v_fmac_f32_e32 v115, v253, v119\n"
"v_fmac_f32_e32 v204, v253, v208\n"
"v_mfma_f32_16x16x32_bf16 v[60:63], a[120:123], v[92:95], v[60:63]\n"
"v_fmac_f32_e32 v205, v253, v209\n"
"v_fmac_f32_e32 v206, v253, v210\n"
"v_mfma_f32_16x16x32_bf16 v[140:143], a[124:127], v[92:95], v[140:143]\n"
"v_fmac_f32_e32 v207, v253, v211\n"
"v_fmac_f32_e32 v216, v253, v224\n"
"v_mfma_f32_16x16x32_bf16 v[228:231], v[68:71], v[92:95], v[228:231]\n"
"v_fmac_f32_e32 v217, v253, v225\n"
"v_fmac_f32_e32 v218, v253, v226\n"
"ds_read_b64_tr_b16 v[56:57], v38\n"
"v_mfma_f32_16x16x32_bf16 v[232:235], v[72:75], v[92:95], v[232:235]\n"
"v_fmac_f32_e32 v219, v253, v227\n"
"ds_read_b64_tr_b16 v[58:59], v38 offset:2048\n"
"s_mov_b32 s96, 0x300030\n"
"v_mfma_f32_16x16x32_bf16 v[136:139], v[76:79], v[92:95], v[136:139]\n"
"buffer_load_dwordx4 v[116:119], v34, s[4:7], s98 offen offset:1024 sc1\n"
"s_mov_b32 s97, 0x300030\n"
"v_mfma_f32_16x16x32_bf16 v[236:239], v[80:83], v[92:95], v[236:239]\n"
"v_mov_b32_e32 v95, 0x3f803f80\n"
"v_cndmask_b32_e64 v92, 0, v95, s[96:97]\n"
"v_mfma_f32_16x16x32_bf16 v[60:63], v[148:151], v[88:91], v[60:63]\n"
"v_rcp_f32_e32 v255, v251\n"
"v_cndmask_b32_e64 v93, 0, v95, s[96:97]\n"
"v_mfma_f32_16x16x32_bf16 v[140:143], v[152:155], v[88:91], v[140:143]\n"
"v_mul_f32_e32 v254, 0x3e8293ee, v255\n"
"v_cndmask_b32_e64 v94, 0, v95, s[96:97]\n"
"v_cndmask_b32_e64 v95, 0, v95, s[96:97]\n"
"v_mfma_f32_16x16x32_bf16 v[228:231], v[156:159], v[88:91], v[228:231]\n"
"s_waitcnt lgkmcnt(0)\n"
"v_mfma_f32_16x16x32_bf16 v[24:27], v[56:59], v[92:95], v[24:27]\n"
"buffer_store_dwordx4 v[56:59], v0, s[4:7], s22 offen sc1\n"
"v_mfma_f32_16x16x32_bf16 v[232:235], v[160:163], v[88:91], v[232:235]\n"
"v_mfma_f32_16x16x32_bf16 v[136:139], v[164:167], v[88:91], v[136:139]\n"
"buffer_load_dwordx4 v[56:59], v35, s[4:7], s98 offen sc1\n"
"v_mfma_f32_16x16x32_bf16 v[236:239], v[168:171], v[88:91], v[236:239]\n"
"v_mfma_f32_16x16x32_bf16 v[60:63], v[172:175], v[44:47], v[60:63]\n"
"v_mfma_f32_16x16x32_bf16 v[140:143], v[176:179], v[44:47], v[140:143]\n"
"v_mfma_f32_16x16x32_bf16 v[228:231], v[180:183], v[44:47], v[228:231]\n"
"s_add_u32 s97, s75, 0\n"
"v_mfma_f32_16x16x32_bf16 v[232:235], v[184:187], v[44:47], v[232:235]\n"
"buffer_load_dwordx2 v[252:253], v41, s[4:7], s97 offen sc1\n"
"v_mfma_f32_16x16x32_bf16 v[136:139], v[188:191], v[44:47], v[136:139]\n"
"buffer_load_dwordx4 v[88:91], v35, s[4:7], s98 offen offset:1024 sc1\n"
"v_mfma_f32_16x16x32_bf16 v[236:239], v[192:195], v[44:47], v[236:239]\n"
"; @A G1 1.3\n"
"v_mfma_f32_16x16x32_bf16 v[108:111], a[0:3], v[100:103], v[108:111]\n"
"v_mfma_f32_16x16x32_bf16 v[212:215], a[4:7], v[100:103], v[212:215]\n"
"v_mfma_f32_16x16x32_bf16 v[220:223], a[8:11], v[100:103], v[220:223]\n"
"v_mfma_f32_16x16x32_bf16 v[112:115], a[12:15], v[100:103], v[112:115]\n"
"ds_write_b128 v3, v[60:63] offset:12288\n"
"v_mfma_f32_16x16x32_bf16 v[204:207], a[16:19], v[100:103], v[204:207]\n"
"s_add_u32 s98, s53, 0x8000\n"
"v_mfma_f32_16x16x32_bf16 v[216:219], a[20:23], v[100:103], v[216:219]\n"
"buffer_load_dwordx4 v[44:47], v32, s[4:7], s98 offen sc1\n"
"v_mfma_f32_16x16x32_bf16 v[108:111], a[24:27], v[144:147], v[108:111]\n"
"ds_write_b128 v3, v[140:143] offset:13312\n"
"v_mfma_f32_16x16x32_bf16 v[212:215], a[28:31], v[144:147], v[212:215]\n"
"v_mfma_f32_16x16x32_bf16 v[220:223], a[32:35], v[144:147], v[220:223]\n"
"v_mfma_f32_16x16x32_bf16 v[112:115], a[36:39], v[144:147], v[112:115]\n"
"ds_write_b128 v3, v[228:231] offset:14336\n"
"v_mfma_f32_16x16x32_bf16 v[204:207], a[40:43], v[144:147], v[204:207]\n"
"buffer_load_dwordx4 v[60:63], v32, s[4:7], s98 offen offset:1024 sc1\n"
"v_mfma_f32_16x16x32_bf16 v[216:219], a[44:47], v[144:147], v[216:219]\n"
"s_waitcnt vmcnt(20)\n"
"v_mfma_f32_16x16x32_bf16 v[108:111], a[48:51], v[200:203], v[108:111]\n"
"ds_write_b128 v3, v[232:235] offset:15360\n"
"v_mfma_f32_16x16x32_bf16 v[212:215], a[52:55], v[200:203], v[212:215]\n"
"v_mfma_f32_16x16x32_bf16 v[220:223], a[56:59], v[200:203], v[220:223]\n"
"s_add_u32 s96, s60, 0x48000\n"
"buffer_store_dwordx4 v[128:131], v0, s[4:7], s96 offen sc1\n"
"v_mfma_f32_16x16x32_bf16 v[112:115], a[60:63], v[200:203], v[112:115]\n"
"ds_write_b128 v3, v[136:139] offset:16384\n"
"v_mfma_f32_16x16x32_bf16 v[204:207], a[64:67], v[200:203], v[204:207]\n"
"buffer_load_dwordx4 v[92:95], v33, s[4:7], s98 offen sc1\n"
"v_mfma_f32_16x16x32_bf16 v[216:219], a[68:71], v[200:203], v[216:219]\n"
"v_mfma_f32_16x16x32_bf16 v[108:111], a[72:75], v[124:127], v[108:111]\n"
"ds_write_b128 v3, v[236:239] offset:17408\n"
"v_mfma_f32_16x16x32_bf16 v[212:215], a[76:79], v[124:127], v[212:215]\n"
"v_mfma_f32_16x16x32_bf16 v[220:223], a[80:83], v[124:127], v[220:223]\n"
"v_mfma_f32_16x16x32_bf16 v[112:115], a[84:87], v[124:127], v[112:115]\n"
"s_waitcnt lgkmcnt(0)\n"
"s_barrier\n"
"v_mfma_f32_16x16x32_bf16 v[204:207], a[88:91], v[124:127], v[204:207]\n"
"buffer_load_dwordx4 v[100:103], v33, s[4:7], s98 offen offset:1024 sc1\n"
"v_mfma_f32_16x16x32_bf16 v[216:219], a[92:95], v[124:127], v[216:219]\n"
"s_waitcnt vmcnt(19)\n"
"ds_read_b128 v[124:127], v5 offset:2048\n"
"v_mfma_f32_16x16x32_bf16 v[108:111], a[96:99], v[28:31], v[108:111]\n"
"ds_read_b128 v[132:135], v6 offset:2048\n"
"ds_read_b128 v[136:139], v5 offset:3072\n"
"v_mfma_f32_16x16x32_bf16 v[212:215], a[100:103], v[28:31], v[212:215]\n"
"ds_read_b128 v[140:143], v6 offset:3072\n"
"v_mfma_f32_16x16x32_bf16 v[220:223], a[104:107], v[28:31], v[220:223]\n"
"v_mfma_f32_16x16x32_bf16 v[112:115], a[108:111], v[28:31], v[112:115]\n"
"v_mfma_f32_16x16x32_bf16 v[204:207], a[112:115], v[28:31], v[204:207]\n"
"buffer_load_dwordx4 v[144:147], v34, s[4:7], s98 offen sc1\n"
"v_mfma_f32_16x16x32_bf16 v[216:219], a[116:119], v[28:31], v[216:219]\n"
"ds_read_b128 v[28:31], v5 offset:4096\n"
"ds_read_b128 v[196:199], v6 offset:4096\n"
"v_mfma_f32_16x16x32_bf16 v[108:111], a[120:123], v[48:51], v[108:111]\n"
"ds_read_b128 v[200:203], v5 offset:5120\n"
"ds_read_b128 v[208:211], v6 offset:5120\n"
"v_mfma_f32_16x16x32_bf16 v[212:215], a[124:127], v[48:51], v[212:215]\n"
"s_waitcnt lgkmcnt(4)\n"
"v_add_f32_e32 v124, v124, v132\n"
"v_mfma_f32_16x16x32_bf16 v[220:223], v[68:71], v[48:51], v[220:223]\n"
"v_add_f32_e32 v125, v125, v133\n"
"v_add_f32_e32 v126, v126, v134\n"
"v_mfma_f32_16x16x32_bf16 v[112:115], v[72:75], v[48:51], v[112:115]\n"
"ds_read_b128 v[224:227], v5\n"
"ds_read_b128 v[228:231], v6\n"
"v_mfma_f32_16x16x32_bf16 v[204:207], v[76:79], v[48:51], v[204:207]\n"
"buffer_load_dwordx4 v[232:235], v34, s[4:7], s98 offen offset:1024 sc1\n"
"v_mfma_f32_16x16x32_bf16 v[216:219], v[80:83], v[48:51], v[216:219]\n"
"s_waitcnt vmcnt(18)\n"
"ds_read_b128 v[48:51], v5 offset:1024\n"
"v_mfma_f32_16x16x32_bf16 v[108:111], v[148:151], v[104:107], v[108:111]\n"
"ds_read_b128 v[236:239], v6 offset:1024\n"
"v_add_f32_e32 v127, v127, v135\n"
"s_add_u32 s96, s60, 0x40000\n"
"buffer_store_dwordx4 v[128:131], v0, s[4:7], s96 offen sc1\n"
"v_mfma_f32_16x16x32_bf16 v[212:215], v[152:155], v[104:107], v[212:215]\n"
"v_add_f32_e32 v136, v136, v140\n"
"v_add_f32_e32 v137, v137, v141\n"
"v_mfma_f32_16x16x32_bf16 v[220:223], v[156:159], v[104:107], v[220:223]\n"
"v_add_f32_e32 v138, v138, v142\n"
"v_add_f32_e32 v139, v139, v143\n"
"v_mfma_f32_16x16x32_bf16 v[112:115], v[160:163], v[104:107], v[112:115]\n"
"s_waitcnt lgkmcnt(4)\n"
"v_add_f32_e32 v28, v28, v196\n"
"v_mfma_f32_16x16x32_bf16 v[204:207], v[164:167], v[104:107], v[204:207]\n"
"buffer_load_dwordx4 v[128:131], v35, s[4:7], s98 offen sc1\n"
"v_mfma_f32_16x16x32_bf16 v[216:219], v[168:171], v[104:107], v[216:219]\n"
"s_waitcnt vmcnt(10)\n"
"v_cmp_ne_u64_e32 vcc, v[252:253], v[18:19]\n"
"v_mfma_f32_16x16x32_bf16 v[108:111], v[172:175], v[84:87], v[108:111]\n"
"s_cbranch_vccnz .Lmk_f5s\n"
".Lmk_f5r:\n"
"v_add_f32_e32 v29, v29, v197\n"
"v_mfma_f32_16x16x32_bf16 v[212:215], v[176:179], v[84:87], v[212:215]\n"
"v_add_f32_e32 v30, v30, v198\n"
"v_add_f32_e32 v31, v31, v199\n"
"v_mfma_f32_16x16x32_bf16 v[220:223], v[180:183], v[84:87], v[220:223]\n"
"v_add_f32_e32 v200, v200, v208\n"
"v_add_f32_e32 v201, v201, v209\n"
"v_mfma_f32_16x16x32_bf16 v[112:115], v[184:187], v[84:87], v[112:115]\n"
"v_add_f32_e32 v202, v202, v210\n"
"v_add_f32_e32 v203, v203, v211\n"
"v_mfma_f32_16x16x32_bf16 v[204:207], v[188:191], v[84:87], v[204:207]\n"
"buffer_load_dwordx4 v[40:43], v35, s[4:7], s98 offen offset:1024 sc1\n"
"v_mfma_f32_16x16x32_bf16 v[216:219], v[192:195], v[84:87], v[216:219]\n"
"s_waitcnt lgkmcnt(0)\n"
"v_add_f32_e32 v224, v224, v228\n"
"v_add_f32_e32 v225, v225, v229\n"
"v_add_f32_e32 v226, v226, v230\n"
"v_add_f32_e32 v227, v227, v231\n"
"v_add_f32_e32 v48, v48, v236\n"
"v_add_f32_e32 v49, v49, v237\n"
"v_add_f32_e32 v50, v50, v238\n"
"v_add_f32_e32 v51, v51, v239\n"
".Lmk_z2:\n"
"; @A G1 2.0\n"
"v_mfma_f32_16x16x32_bf16 v[84:87], a[0:3], v[64:67], 0\n"
"s_add_u32 s98, s62, 0x800\n"
"v_mfma_f32_16x16x32_bf16 v[104:107], a[4:7], v[64:67], 0\n"
"buffer_load_dwordx2 v[252:253], v10, s[4:7], s98 offen sc1\n"
"v_mfma_f32_16x16x32_bf16 v[132:135], a[8:11], v[64:67], 0\n"
"buffer_load_dwordx2 v[250:251], v10, s[4:7], s98 offen offset:128 sc1\n"
"v_mfma_f32_16x16x32_bf16 v[140:143], a[12:15], v[64:67], 0\n"
"buffer_load_dwordx2 v[248:249], v10, s[4:7], s98 offen offset:256 sc1\n"
"v_mfma_f32_16x16x32_bf16 v[196:199], a[16:19], v[64:67], 0\n"
"s_add_u32 s97, s54, 0x8000\n"
"v_add_f32_e32 v220, v220, v124\n"
"v_mfma_f32_16x16x32_bf16 v[208:211], a[20:23], v[64:67], 0\n"
"buffer_load_dwordx4 v[64:67], v32, s[4:7], s97 offen sc1\n"
"v_mfma_f32_16x16x32_bf16 v[84:87], a[24:27], v[120:123], v[84:87]\n"
"buffer_load_dwordx2 v[246:247], v10, s[4:7], s98 offen offset:384 sc1\n"
"v_mfma_f32_16x16x32_bf16 v[104:107], a[28:31], v[120:123], v[104:107]\n"
"v_add_f32_e32 v221, v221, v125\n"
"v_add_f32_e32 v222, v222, v126\n"
"v_mfma_f32_16x16x32_bf16 v[132:135], a[32:35], v[120:123], v[132:135]\n"
"s_add_u32 s98, s70, 0\n"
"v_add_f32_e32 v223, v223, v127\n"
"v_mfma_f32_16x16x32_bf16 v[140:143], a[36:39], v[120:123], v[140:143]\n"
"buffer_load_dwordx4 a[128:131], v0, s[4:7], s98 offen sc1\n"
"v_mfma_f32_16x16x32_bf16 v[196:199], a[40:43], v[120:123], v[196:199]\n"
"buffer_load_dwordx4 v[124:127], v32, s[4:7], s97 offen offset:1024 sc1\n"
"v_mfma_f32_16x16x32_bf16 v[208:211], a[44:47], v[120:123], v[208:211]\n"
"v_add_f32_e32 v112, v112, v136\n"
"v_mfma_f32_16x16x32_bf16 v[84:87], a[48:51], v[52:55], v[84:87]\n"
"v_add_f32_e32 v113, v113, v137\n"
"v_add_f32_e32 v114, v114, v138\n"
"v_mfma_f32_16x16x32_bf16 v[104:107], a[52:55], v[52:55], v[104:107]\n"
"v_add_f32_e32 v115, v115, v139\n"
"v_mul_f32_e32 v220, v220, v255\n"
"v_mfma_f32_16x16x32_bf16 v[132:135], a[56:59], v[52:55], v[132:135]\n"
"v_mul_f32_e32 v221, v221, v255\n"
"v_mul_f32_e32 v222, v222, v255\n"
"v_mfma_f32_16x16x32_bf16 v[140:143], a[60:63], v[52:55], v[140:143]\n"
"v_mul_f32_e32 v223, v223, v255\n"
"v_mul_f32_e32 v112, v112, v255\n"
"v_mfma_f32_16x16x32_bf16 v[196:199], a[64:67], v[52:55], v[196:199]\n"
"buffer_load_dwordx4 v[120:123], v33, s[4:7], s97 offen sc1\n"
"v_mfma_f32_16x16x32_bf16 v[208:211], a[68:71], v[52:55], v[208:211]\n"
"v_mul_f32_e32 v113, v113, v255\n"
"v_mul_f32_e32 v114, v114, v255\n"
"v_mfma_f32_16x16x32_bf16 v[84:87], a[72:75], v[240:243], v[84:87]\n"
"v_mul_f32_e32 v115, v115, v255\n"
"v_cvt_pk_bf16_f32 v52, v220, v221\n"
"v_mfma_f32_16x16x32_bf16 v[104:107], a[76:79], v[240:243], v[104:107]\n"
"buffer_load_dwordx4 a[132:135], v0, s[4:7], s98 offen offset:1024 sc1\n"
"v_mfma_f32_16x16x32_bf16 v[132:135], a[80:83], v[240:243], v[132:135]\n"
"v_cvt_pk_bf16_f32 v53, v222, v223\n"
"v_cvt_pk_bf16_f32 v54, v112, v113\n"
"v_mfma_f32_16x16x32_bf16 v[140:143], a[84:87], v[240:243], v[140:143]\n"
"v_cvt_pk_bf16_f32 v55, v114, v115\n"
"v_mfma_f32_16x16x32_bf16 v[196:199], a[88:91], v[240:243], v[196:199]\n"
"buffer_load_dwordx4 v[112:115], v33, s[4:7], s97 offen offset:1024 sc1\n"
"v_mfma_f32_16x16x32_bf16 v[208:211], a[92:95], v[240:243], v[208:211]\n"
"v_mfma_f32_16x16x32_bf16 v[84:87], a[96:99], v[96:99], v[84:87]\n"
"ds_write_b128 v8, v[52:55] offset:25600\n"
"v_mfma_f32_16x16x32_bf16 v[104:107], a[100:103], v[96:99], v[104:107]\n"
"s_add_u32 s96, s64, 0x2000\n"
"v_mfma_f32_16x16x32_bf16 v[132:135], a[104:107], v[96:99], v[132:135]\n"
"buffer_store_dwordx4 v[52:55], v8, s[4:7], s96 offen offset:1024 sc1\n"
"v_mfma_f32_16x16x32_bf16 v[140:143], a[108:111], v[96:99], v[140:143]\n"
"v_add_f32_e32 v204, v204, v28\n"
"v_add_f32_e32 v205, v205, v29\n"
"v_mfma_f32_16x16x32_bf16 v[196:199], a[112:115], v[96:99], v[196:199]\n"
"buffer_load_dwordx4 v[52:55], v34, s[4:7], s97 offen sc1\n"
"v_mfma_f32_16x16x32_bf16 v[208:211], a[116:119], v[96:99], v[208:211]\n"
"buffer_load_dwordx4 a[136:139], v0, s[4:7], s98 offen offset:2048 sc1\n"
"v_mfma_f32_16x16x32_bf16 v[84:87], a[120:123], v[116:119], v[84:87]\n"
"v_add_f32_e32 v206, v206, v30\n"
"v_add_f32_e32 v207, v207, v31\n"
"v_mfma_f32_16x16x32_bf16 v[104:107], a[124:127], v[116:119], v[104:107]\n"
"v_add_f32_e32 v216, v216, v200\n"
"v_add_f32_e32 v217, v217, v201\n"
"v_mfma_f32_16x16x32_bf16 v[132:135], v[68:71], v[116:119], v[132:135]\n"
"v_add_f32_e32 v218, v218, v202\n"
"v_add_f32_e32 v219, v219, v203\n"
"v_mfma_f32_16x16x32_bf16 v[140:143], v[72:75], v[116:119], v[140:143]\n"
"v_mul_f32_e32 v204, v204, v255\n"
"v_mul_f32_e32 v205, v205, v255\n"
"v_mfma_f32_16x16x32_bf16 v[196:199], v[76:79], v[116:119], v[196:199]\n"
"buffer_load_dwordx4 v[28:31], v34, s[4:7], s97 offen offset:1024 sc1\n"
"v_mfma_f32_16x16x32_bf16 v[208:211], v[80:83], v[116:119], v[208:211]\n"
"s_waitcnt vmcnt(24)\n"
"v_mul_f32_e32 v206, v206, v255\n"
"v_mfma_f32_16x16x32_bf16 v[84:87], v[148:151], v[56:59], v[84:87]\n"
"v_mul_f32_e32 v207, v207, v255\n"
"v_mul_f32_e32 v216, v216, v255\n"
"v_mfma_f32_16x16x32_bf16 v[104:107], v[152:155], v[56:59], v[104:107]\n"
"v_mul_f32_e32 v217, v217, v255\n"
"v_mul_f32_e32 v218, v218, v255\n"
"v_mfma_f32_16x16x32_bf16 v[132:135], v[156:159], v[56:59], v[132:135]\n"
"v_mul_f32_e32 v219, v219, v255\n"
"v_cvt_pk_bf16_f32 v96, v204, v205\n"
"v_mfma_f32_16x16x32_bf16 v[140:143], v[160:163], v[56:59], v[140:143]\n"
"buffer_load_dwordx4 a[140:143], v0, s[4:7], s98 offen offset:3072 sc1\n"
"v_mfma_f32_16x16x32_bf16 v[196:199], v[164:167], v[56:59], v[196:199]\n"
"buffer_load_dwordx4 v[116:119], v35, s[4:7], s97 offen sc1\n"
"v_mfma_f32_16x16x32_bf16 v[208:211], v[168:171], v[56:59], v[208:211]\n"
"v_cvt_pk_bf16_f32 v97, v206, v207\n"
"v_cvt_pk_bf16_f32 v98, v216, v217\n"
"v_mfma_f32_16x16x32_bf16 v[84:87], v[172:175], v[88:91], v[84:87]\n"
"v_cvt_pk_bf16_f32 v99, v218, v219\n"
"v_mfma_f32_16x16x32_bf16 v[104:107], v[176:179], v[88:91], v[104:107]\n"
"ds_write_b128 v7, v[96:99] offset:58368\n"
"v_mfma_f32_16x16x32_bf16 v[132:135], v[180:183], v[88:91], v[132:135]\n"
"s_add_u32 s96, s64, 0x6000\n"
"v_mfma_f32_16x16x32_bf16 v[140:143], v[184:187], v[88:91], v[140:143]\n"
"buffer_store_dwordx4 v[96:99], v7, s[4:7], s96 offen offset:1024 sc1\n"
"v_mfma_f32_16x16x32_bf16 v[196:199], v[188:191], v[88:91], v[196:199]\n"
"buffer_load_dwordx4 v[56:59], v35, s[4:7], s97 offen offset:1024 sc1\n"
"v_mfma_f32_16x16x32_bf16 v[208:211], v[192:195], v[88:91], v[208:211]\n"
"s_waitcnt vmcnt(26)\n"
"v_add_f32_e32 v108, v108, v224\n"
"; @A G1 2.1\n"
"v_mfma_f32_16x16x32_bf16 v[88:91], a[0:3], v[44:47], 0\n"
"v_add_f32_e32 v109, v109, v225\n"
"v_add_f32_e32 v110, v110, v226\n"
"v_mfma_f32_16x16x32_bf16 v[96:99], a[4:7], v[44:47], 0\n"
"buffer_load_dwordx4 a[144:147], v1, s[4:7], s98 offen sc1\n"
"v_mfma_f32_16x16x32_bf16 v[136:139], a[8:11], v[44:47], 0\n"
"v_add_f32_e32 v111, v111, v227\n"
"v_add_f32_e32 v212, v212, v48\n"
"v_mfma_f32_16x16x32_bf16 v[200:203], a[12:15], v[44:47], 0\n"
"ds_write_b128 v3, v[84:87]\n"
"v_mfma_f32_16x16x32_bf16 v[84:87], a[16:19], v[44:47], 0\n"
"s_add_u32 s97, s55, 0x8000\n"
"v_add_f32_e32 v213, v213, v49\n"
"v_mfma_f32_16x16x32_bf16 v[204:207], a[20:23], v[44:47], 0\n"
"buffer_load_dwordx4 v[44:47], v32, s[4:7], s97 offen sc1\n"
"v_mfma_f32_16x16x32_bf16 v[88:91], a[24:27], v[60:63], v[88:91]\n"
"ds_write_b128 v3, v[104:107] offset:1024\n"
"v_mfma_f32_16x16x32_bf16 v[96:99], a[28:31], v[60:63], v[96:99]\n"
"v_add_f32_e32 v214, v214, v50\n"
"v_add_f32_e32 v215, v215, v51\n"
"v_mfma_f32_16x16x32_bf16 v[136:139], a[32:35], v[60:63], v[136:139]\n"
"v_mul_f32_e32 v108, v108, v254\n"
"v_mul_f32_e32 v109, v109, v254\n"
"v_mfma_f32_16x16x32_bf16 v[200:203], a[36:39], v[60:63], v[200:203]\n"
"ds_write_b128 v3, v[132:135] offset:2048\n"
"v_mfma_f32_16x16x32_bf16 v[84:87], a[40:43], v[60:63], v[84:87]\n"
"buffer_load_dwordx4 v[48:51], v32, s[4:7], s97 offen offset:1024 sc1\n"
"v_mfma_f32_16x16x32_bf16 v[204:207], a[44:47], v[60:63], v[204:207]\n"
"s_waitcnt vmcnt(26)\n"
"v_mul_f32_e32 v110, v110, v254\n"
"v_mfma_f32_16x16x32_bf16 v[88:91], a[48:51], v[92:95], v[88:91]\n"
"ds_write_b128 v3, v[140:143] offset:3072\n"
"v_mfma_f32_16x16x32_bf16 v[96:99], a[52:55], v[92:95], v[96:99]\n"
"buffer_load_dwordx4 a[148:151], v1, s[4:7], s98 offen offset:1024 sc1\n"
"v_mfma_f32_16x16x32_bf16 v[136:139], a[56:59], v[92:95], v[136:139]\n"
"v_mul_f32_e32 v111, v111, v254\n"
"v_mul_f32_e32 v212, v212, v254\n"
"v_mfma_f32_16x16x32_bf16 v[200:203], a[60:63], v[92:95], v[200:203]\n"
"ds_write_b128 v3, v[196:199] offset:4096\n"
"v_mfma_f32_16x16x32_bf16 v[84:87], a[64:67], v[92:95], v[84:87]\n"
"buffer_load_dwordx4 v[60:63], v33, s[4:7], s97 offen sc1\n"
"v_mfma_f32_16x16x32_bf16 v[204:207], a[68:71], v[92:95], v[204:207]\n"
"v_mul_f32_e32 v213, v213, v254\n"
"v_mul_f32_e32 v214, v214, v254\n"
"v_mfma_f32_16x16x32_bf16 v[88:91], a[72:75], v[100:103], v[88:91]\n"
"ds_write_b128 v3, v[208:211] offset:5120\n"
"v_mfma_f32_16x16x32_bf16 v[96:99], a[76:79], v[100:103], v[96:99]\n"
"v_mul_f32_e32 v215, v215, v254\n"
"v_cvt_pk_bf16_f32 v92, v108, v109\n"
"v_mfma_f32_16x16x32_bf16 v[136:139], a[80:83], v[100:103], v[136:139]\n"
"v_cvt_pk_bf16_f32 v93, v110, v111\n"
"v_cvt_pk_bf16_f32 v94, v212, v213\n"
"v_mfma_f32_16x16x32_bf16 v[200:203], a[84:87], v[100:103], v[200:203]\n"
"v_cvt_pk_bf16_f32 v95, v214, v215\n"
"v_mov_b32_e32 v104, -1\n"
"v_mfma_f32_16x16x32_bf16 v[84:87], a[88:91], v[100:103], v[84:87]\n"
"buffer_load_dwordx4 v[108:111], v33, s[4:7], s97 offen offset:1024 sc1\n"
"v_mfma_f32_16x16x32_bf16 v[204:207], a[92:95], v[100:103], v[204:207]\n"
"s_waitcnt vmcnt(27)\n"
"v_mov_b32_e32 v105, -1\n"
"v_mfma_f32_16x16x32_bf16 v[88:91], a[96:99], v[144:147], v[88:91]\n"
"buffer_load_dwordx4 a[152:155], v1, s[4:7], s98 offen offset:2048 sc1\n"
"v_mfma_f32_16x16x32_bf16 v[96:99], a[100:103], v[144:147], v[96:99]\n"
"ds_write_b128 v2, v[92:95] offset:50176\n"
"v_mfma_f32_16x16x32_bf16 v[136:139], a[104:107], v[144:147], v[136:139]\n"
"v_mov_b32_e32 v106, -1\n"
"v_mov_b32_e32 v107, -1\n"
"v_mfma_f32_16x16x32_bf16 v[200:203], a[108:111], v[144:147], v[200:203]\n"
"ds_read_b64_tr_b16 v[92:93], v38 offset:4096\n"
"ds_read_b64_tr_b16 v[94:95], v38 offset:6144\n"
"s_mov_b32 s94, 0x300030\n"
"v_mfma_f32_16x16x32_bf16 v[84:87], a[112:115], v[144:147], v[84:87]\n"
"buffer_load_dwordx4 v[100:103], v34, s[4:7], s97 offen sc1\n"
"s_mov_b32 s95, 0x300030\n"
"v_mfma_f32_16x16x32_bf16 v[204:207], a[116:119], v[144:147], v[204:207]\n"
"s_waitcnt lgkmcnt(0)\n"
"s_barrier\n"
"v_mov_b32_e32 v135, 0x3f803f80\n"
"v_mfma_f32_16x16x32_bf16 v[88:91], a[120:123], v[232:235], v[88:91]\n"
"s_waitcnt vmcnt(21)\n"
"v_add_f32_e32 v252, v252, v250\n"
"v_cndmask_b32_e64 v132, 0, v135, s[94:95]\n"
"v_mfma_f32_16x16x32_bf16 v[96:99], a[124:127], v[232:235], v[96:99]\n"
"v_add_f32_e32 v253, v253, v251\n"
"v_add_f32_e32 v248, v248, v246\n"
"v_cndmask_b32_e64 v133, 0, v135, s[94:95]\n"
"v_mfma_f32_16x16x32_bf16 v[136:139], v[68:71], v[232:235], v[136:139]\n"
"v_add_f32_e32 v249, v249, v247\n"
"v_add_f32_e32 v252, v252, v248\n"
"v_cndmask_b32_e64 v134, 0, v135, s[94:95]\n"
"v_mfma_f32_16x16x32_bf16 v[200:203], v[72:75], v[232:235], v[200:203]\n"
"v_add_f32_e32 v253, v253, v249\n"
"v_cndmask_b32_e64 v135, 0, v135, s[94:95]\n"
"v_mfma_f32_16x16x32_bf16 v[84:87], v[76:79], v[232:235], v[84:87]\n"
"buffer_load_dwordx4 v[140:143], v34, s[4:7], s97 offen offset:1024 sc1\n"
"v_mfma_f32_16x16x32_bf16 v[24:27], v[92:95], v[132:135], v[24:27]\n"
"s_add_u32 s96, s22, 0x1000\n"
"v_mfma_f32_16x16x32_bf16 v[204:207], v[80:83], v[232:235], v[204:207]\n"
"v_permlane16_swap_b32_e32 v252, v253\n"
"buffer_store_dwordx4 v[92:95], v0, s[4:7], s96 offen offset:2048 sc1\n"
"v_mfma_f32_16x16x32_bf16 v[88:91], v[148:151], v[128:131], v[88:91]\n"
"buffer_load_dwordx4 a[156:159], v1, s[4:7], s98 offen offset:3072 sc1\n"
"s_nop 1\n"
"buffer_store_dwordx4 v[24:27], v39, s[4:7], s23 offen sc1\n"
"v_mfma_f32_16x16x32_bf16 v[96:99], v[152:155], v[128:131], v[96:99]\n"
"v_add_f32_e32 v252, v252, v253\n"
"v_mov_b32_e32 v253, v252\n"
"s_add_u32 s98, s51, 0\n"
"v_mfma_f32_16x16x32_bf16 v[136:139], v[156:159], v[128:131], v[136:139]\n"
"v_permlane32_swap_b32_e32 v252, v253\n"
"v_add_f32_e32 v252, v252, v253\n"
"s_bitcmp1_b32 s26, 0\n"
"s_cbranch_scc1 .Lmk_w7s\n"
"buffer_store_dwordx4 a[0:3], v0, s[4:7], s98 offen sc1\n"
".Lmk_w7r:\n"
"v_mfma_f32_16x16x32_bf16 v[200:203], v[160:163], v[128:131], v[200:203]\n"
"v_mul_f32_e32 v252, 0x3a800000, v252\n"
"v_mov_b32_e32 v253, v252\n"
"v_mfma_f32_16x16x32_bf16 v[84:87], v[164:167], v[128:131], v[84:87]\n"
"buffer_load_dwordx4 v[24:27], v35, s[4:7], s97 offen sc1\n"
"v_mfma_f32_16x16x32_bf16 v[204:207], v[168:171], v[128:131], v[204:207]\n"
"v_permlane16_swap_b32_e32 v252, v253\n"
"v_cvt_pk_bf16_f32 v255, -v252, 0\n"
"v_mfma_f32_16x16x32_bf16 v[88:91], v[172:175], v[40:43], v[88:91]\n"
"v_fma_f32 v253, -v252, v252, v253\n"
"v_lshlrev_b32_e32 v255, 16, v255\n"
"v_mfma_f32_16x16x32_bf16 v[96:99], v[176:179], v[40:43], v[96:99]\n"
"ds_read_b128 v[36:39], v4\n"
"ds_read_b128 v[92:95], v9\n"
"s_add_u32 s98, s51, 0x400\n"
"v_mfma_f32_16x16x32_bf16 v[136:139], v[180:183], v[40:43], v[136:139]\n"
"s_add_u32 s96, s70, 0x2000\n"
"ds_read_b128 v[128:131], v4 offset:1024\n"
"s_bitcmp1_b32 s26, 0\n"
"s_cbranch_scc1 .Lmk_w8s\n"
"buffer_store_dwordx4 a[4:7], v0, s[4:7], s98 offen sc1\n"
".Lmk_w8r:\n"
"v_mfma_f32_16x16x32_bf16 v[200:203], v[184:187], v[40:43], v[200:203]\n"
"ds_read_b128 v[132:135], v9 offset:64\n"
"ds_read_b128 v[144:147], v4 offset:2048\n"
"v_mfma_f32_16x16x32_bf16 v[84:87], v[188:191], v[40:43], v[84:87]\n"
"buffer_load_dwordx4 v[196:199], v35, s[4:7], s97 offen offset:1024 sc1\n"
"v_mfma_f32_16x16x32_bf16 v[204:207], v[192:195], v[40:43], v[204:207]\n"
"s_waitcnt vmcnt(27)\n"
"ds_read_b128 v[40:43], v9 offset:128\n"
"; @A G1 2.2\n"
"v_mfma_f32_16x16x32_bf16 v[208:211], a[0:3], v[64:67], 0\n"
"buffer_load_dwordx4 a[160:163], v0, s[4:7], s96 offen sc1\n"
"v_mfma_f32_16x16x32_bf16 v[212:215], a[4:7], v[64:67], 0\n"
"v_max_f32_e32 v253, 0, v253\n"
"v_cvt_pk_bf16_f32 v253, v253, 0\n"
"v_mfma_f32_16x16x32_bf16 v[216:219], a[8:11], v[64:67], 0\n"
"ds_read_b128 v[220:223], v4 offset:3072\n"
"ds_read_b128 v[224:227], v9 offset:192\n"
"v_mfma_f32_16x16x32_bf16 v[228:231], a[12:15], v[64:67], 0\n"
"ds_write_b128 v3, v[88:91] offset:6144\n"
"v_mfma_f32_16x16x32_bf16 v[88:91], a[16:19], v[64:67], 0\n"
"s_add_u32 s98, s52, 0\n"
"s_mov_b32 s97, .Lmk_z4-.Lmk_g16\n"
"s_getpc_b64 s[84:85]\n"
".Lmk_g16:\n"
"v_mfma_f32_16x16x32_bf16 v[232:235], a[20:23], v[64:67], 0\n"
"buffer_load_dwordx4 v[64:67], v32, s[4:7], s98 offen sc1\n"
"s_add_u32 s84, s84, s97\n"
"s_addc_u32 s85, s85, 0\n"
"v_mfma_f32_16x16x32_bf16 v[208:211], a[24:27], v[124:127], v[208:211]\n"
"ds_write_b128 v3, v[96:99] offset:7168\n"
"v_mfma_f32_16x16x32_bf16 v[212:215], a[28:31], v[124:127], v[212:215]\n"
"s_and_b32 s84, s84, -16\n"
"s_and_b32 s85, s85, 0xffff\n"
"s_sub_u32 s97, .Lmk_end-.Lmk_g16, s97\n"
"s_min_u32 s86, s97, 0x4000\n"
"v_mfma_f32_16x16x32_bf16 v[216:219], a[32:35], v[124:127], v[216:219]\n"
"s_mov_b32 s87, 0x20000\n"
"s_waitcnt lgkmcnt(4)\n"
"v_mfma_f32_16x16x32_bf16 v[228:231], a[36:39], v[124:127], v[228:231]\n"
"ds_write_b128 v3, v[136:139] offset:8192\n"
"v_mfma_f32_16x16x32_bf16 v[88:91], a[40:43], v[124:127], v[88:91]\n"
"buffer_load_dwordx4 v[96:99], v32, s[4:7], s98 offen offset:1024 sc1\n"
"buffer_store_dwordx4 v[104:107], v0, s[4:7], s60 offen sc1\n"
"v_mfma_f32_16x16x32_bf16 v[232:235], a[44:47], v[124:127], v[232:235]\n"
"s_waitcnt vmcnt(28)\n"
"v_fmac_f32_e32 v36, v255, v92\n"
"v_mfma_f32_16x16x32_bf16 v[208:211], a[48:51], v[120:123], v[208:211]\n"
"ds_write_b128 v3, v[200:203] offset:9216\n"
"v_mfma_f32_16x16x32_bf16 v[212:215], a[52:55], v[120:123], v[212:215]\n"
"buffer_load_dwordx4 a[164:167], v0, s[4:7], s96 offen offset:1024 sc1\n"
"v_mfma_f32_16x16x32_bf16 v[216:219], a[56:59], v[120:123], v[216:219]\n"
"s_mov_b32 m0, 0x27800\n"
"s_nop 0\n"
"buffer_load_dwordx4 v0, s[84:87], s99 offen lds\n"
"v_mfma_f32_16x16x32_bf16 v[228:231], a[60:63], v[120:123], v[228:231]\n"
"ds_write_b128 v3, v[84:87] offset:10240\n"
"v_mfma_f32_16x16x32_bf16 v[88:91], a[64:67], v[120:123], v[88:91]\n"
"buffer_load_dwordx4 v[84:87], v33, s[4:7], s98 offen sc1\n"
"v_mfma_f32_16x16x32_bf16 v[232:235], a[68:71], v[120:123], v[232:235]\n"
"v_fmac_f32_e32 v37, v255, v93\n"
"v_fmac_f32_e32 v38, v255, v94\n"
"v_mfma_f32_16x16x32_bf16 v[208:211], a[72:75], v[112:115], v[208:211]\n"
"ds_write_b128 v3, v[204:207] offset:11264\n"
"v_mfma_f32_16x16x32_bf16 v[212:215], a[76:79], v[112:115], v[212:215]\n"
"v_fmac_f32_e32 v39, v255, v95\n"
"v_fmac_f32_e32 v128, v255, v132\n"
"v_mfma_f32_16x16x32_bf16 v[216:219], a[80:83], v[112:115], v[216:219]\n"
"v_fmac_f32_e32 v129, v255, v133\n"
"v_fmac_f32_e32 v130, v255, v134\n"
"v_mfma_f32_16x16x32_bf16 v[228:231], a[84:87], v[112:115], v[228:231]\n"
"v_fmac_f32_e32 v131, v255, v135\n"
"v_fmac_f32_e32 v144, v255, v40\n"
"v_mfma_f32_16x16x32_bf16 v[88:91], a[88:91], v[112:115], v[88:91]\n"
"buffer_load_dwordx4 v[92:95], v33, s[4:7], s98 offen offset:1024 sc1\n"
"v_mfma_f32_16x16x32_bf16 v[232:235], a[92:95], v[112:115], v[232:235]\n"
"s_waitcnt vmcnt(28)\n"
"v_fmac_f32_e32 v145, v255, v41\n"
"v_mfma_f32_16x16x32_bf16 v[208:211], a[96:99], v[52:55], v[208:211]\n"
"buffer_load_dwordx4 a[168:171], v0, s[4:7], s96 offen offset:2048 sc1\n"
"s_add_u32 s97, s60, 0x8000\n"
"buffer_store_dwordx4 v[104:107], v0, s[4:7], s97 offen sc1\n"
"v_mfma_f32_16x16x32_bf16 v[212:215], a[100:103], v[52:55], v[212:215]\n"
"v_fmac_f32_e32 v146, v255, v42\n"
"v_fmac_f32_e32 v147, v255, v43\n"
"v_mfma_f32_16x16x32_bf16 v[216:219], a[104:107], v[52:55], v[216:219]\n"
"ds_read_b128 v[40:43], v4 offset:4096\n"
"ds_read_b128 v[104:107], v9 offset:256\n"
"v_mfma_f32_16x16x32_bf16 v[228:231], a[108:111], v[52:55], v[228:231]\n"
"ds_read_b128 v[112:115], v4 offset:5120\n"
"ds_read_b128 v[120:123], v9 offset:320\n"
"v_mfma_f32_16x16x32_bf16 v[88:91], a[112:115], v[52:55], v[88:91]\n"
"buffer_load_dwordx4 v[124:127], v34, s[4:7], s98 offen sc1\n"
"v_mfma_f32_16x16x32_bf16 v[232:235], a[116:119], v[52:55], v[232:235]\n"
"v_lshlrev_b32_e32 v253, 16, v253\n"
"v_add_f32_e32 v253, 0x3727c5ac, v253\n"
"v_mfma_f32_16x16x32_bf16 v[208:211], a[120:123], v[28:31], v[208:211]\n"
"v_cvt_pk_bf16_f32 v253, v253, 0\n"
"v_lshlrev_b32_e32 v253, 16, v253\n"
"v_mfma_f32_16x16x32_bf16 v[212:215], a[124:127], v[28:31], v[212:215]\n"
"v_sqrt_f32_e32 v253, v253\n"
"v_mfma_f32_16x16x32_bf16 v[216:219], v[68:71], v[28:31], v[216:219]\n"
"s_waitcnt lgkmcnt(0)\n"
"v_fmac_f32_e32 v220, v255, v224\n"
"v_mfma_f32_16x16x32_bf16 v[228:231], v[72:75], v[28:31], v[228:231]\n"
"v_fmac_f32_e32 v221, v255, v225\n"
"v_fmac_f32_e32 v222, v255, v226\n"
"v_mfma_f32_16x16x32_bf16 v[88:91], v[76:79], v[28:31], v[88:91]\n"
"buffer_load_dwordx4 v[52:55], v34, s[4:7], s98 offen offset:1024 sc1\n"
"v_mfma_f32_16x16x32_bf16 v[232:235], v[80:83], v[28:31], v[232:235]\n"
"s_waitcnt vmcnt(28)\n"
"v_mfma_f32_16x16x32_bf16 v[208:211], v[148:151], v[116:119], v[208:211]\n"
"s_add_u32 s97, s68, 0x1000\n"
"v_fmac_f32_e32 v223, v255, v227\n"
"v_mfma_f32_16x16x32_bf16 v[212:215], v[152:155], v[116:119], v[212:215]\n"
"buffer_store_dwordx2 v[18:19], v11, s[4:7], s97 offen offset:64 sc1\n"
"v_mfma_f32_16x16x32_bf16 v[216:219], v[156:159], v[116:119], v[216:219]\n"
"buffer_load_dwordx4 a[172:175], v0, s[4:7], s96 offen offset:3072 sc1\n"
"v_mfma_f32_16x16x32_bf16 v[228:231], v[160:163], v[116:119], v[228:231]\n"
"v_fmac_f32_e32 v40, v255, v104\n"
"v_fmac_f32_e32 v41, v255, v105\n"
"v_mfma_f32_16x16x32_bf16 v[88:91], v[164:167], v[116:119], v[88:91]\n"
"buffer_load_dwordx4 v[28:31], v35, s[4:7], s98 offen sc1\n"
"v_mfma_f32_16x16x32_bf16 v[232:235], v[168:171], v[116:119], v[232:235]\n"
"v_fmac_f32_e32 v42, v255, v106\n"
"v_fmac_f32_e32 v43, v255, v107\n"
"v_mfma_f32_16x16x32_bf16 v[208:211], v[172:175], v[56:59], v[208:211]\n"
"v_fmac_f32_e32 v112, v255, v120\n"
"v_fmac_f32_e32 v113, v255, v121\n"
"v_mfma_f32_16x16x32_bf16 v[212:215], v[176:179], v[56:59], v[212:215]\n"
"v_fmac_f32_e32 v114, v255, v122\n"
"v_fmac_f32_e32 v115, v255, v123\n"
"v_mfma_f32_16x16x32_bf16 v[216:219], v[180:183], v[56:59], v[216:219]\n"
"v_cvt_pk_bf16_f32 v253, v253, 0\n"
"v_lshlrev_b32_e32 v253, 16, v253\n"
"v_mfma_f32_16x16x32_bf16 v[228:231], v[184:187], v[56:59], v[228:231]\n"
"v_rcp_f32_e32 v255, v253\n"
"v_mfma_f32_16x16x32_bf16 v[88:91], v[188:191], v[56:59], v[88:91]\n"
"buffer_load_dwordx4 v[104:107], v35, s[4:7], s98 offen offset:1024 sc1\n"
"v_mfma_f32_16x16x32_bf16 v[232:235], v[192:195], v[56:59], v[232:235]\n"
"s_waitcnt vmcnt(29)\n"
"v_mul_f32_e32 v254, 0x3e8293ee, v255\n"
"; @A G1 2.3\n"
"v_mfma_f32_16x16x32_bf16 v[36:39], a[0:3], v[44:47], v[36:39]\n"
"buffer_load_dwordx4 a[176:179], v1, s[4:7], s96 offen sc1\n"
"v_mfma_f32_16x16x32_bf16 v[128:131], a[4:7], v[44:47], v[128:131]\n"
"v_mfma_f32_16x16x32_bf16 v[144:147], a[8:11], v[44:47], v[144:147]\n"
"v_mfma_f32_16x16x32_bf16 v[220:223], a[12:15], v[44:47], v[220:223]\n"
"ds_write_b128 v3, v[208:211] offset:12288\n"
"v_mfma_f32_16x16x32_bf16 v[40:43], a[16:19], v[44:47], v[40:43]\n"
"s_add_u32 s98, s53, 0\n"
"v_mfma_f32_16x16x32_bf16 v[112:115], a[20:23], v[44:47], v[112:115]\n"
"buffer_load_dwordx4 v[44:47], v32, s[4:7], s98 offen sc1\n"
"v_mfma_f32_16x16x32_bf16 v[36:39], a[24:27], v[48:51], v[36:39]\n"
"ds_write_b128 v3, v[212:215] offset:13312\n"
"v_mfma_f32_16x16x32_bf16 v[128:131], a[28:31], v[48:51], v[128:131]\n"
"v_mfma_f32_16x16x32_bf16 v[144:147], a[32:35], v[48:51], v[144:147]\n"
"v_mfma_f32_16x16x32_bf16 v[220:223], a[36:39], v[48:51], v[220:223]\n"
"ds_write_b128 v3, v[216:219] offset:14336\n"
"v_mfma_f32_16x16x32_bf16 v[40:43], a[40:43], v[48:51], v[40:43]\n"
"buffer_load_dwordx4 v[56:59], v32, s[4:7], s98 offen offset:1024 sc1\n"
"v_mfma_f32_16x16x32_bf16 v[112:115], a[44:47], v[48:51], v[112:115]\n"
"s_waitcnt vmcnt(29)\n"
"v_mfma_f32_16x16x32_bf16 v[36:39], a[48:51], v[60:63], v[36:39]\n"
"ds_write_b128 v3, v[228:231] offset:15360\n"
"v_mfma_f32_16x16x32_bf16 v[128:131], a[52:55], v[60:63], v[128:131]\n"
"buffer_load_dwordx4 a[180:183], v1, s[4:7], s96 offen offset:1024 sc1\n"
"v_mfma_f32_16x16x32_bf16 v[144:147], a[56:59], v[60:63], v[144:147]\n"
"v_mfma_f32_16x16x32_bf16 v[220:223], a[60:63], v[60:63], v[220:223]\n"
"ds_write_b128 v3, v[88:91] offset:16384\n"
"v_mfma_f32_16x16x32_bf16 v[40:43], a[64:67], v[60:63], v[40:43]\n"
"buffer_load_dwordx4 v[48:51], v33, s[4:7], s98 offen sc1\n"
"v_mfma_f32_16x16x32_bf16 v[112:115], a[68:71], v[60:63], v[112:115]\n"
"v_mfma_f32_16x16x32_bf16 v[36:39], a[72:75], v[108:111], v[36:39]\n"
"ds_write_b128 v3, v[232:235] offset:17408\n"
"v_mfma_f32_16x16x32_bf16 v[128:131], a[76:79], v[108:111], v[128:131]\n"
"v_mfma_f32_16x16x32_bf16 v[144:147], a[80:83], v[108:111], v[144:147]\n"
"v_mfma_f32_16x16x32_bf16 v[220:223], a[84:87], v[108:111], v[220:223]\n"
"s_waitcnt lgkmcnt(0)\n"
"s_barrier\n"
"v_mfma_f32_16x16x32_bf16 v[40:43], a[88:91], v[108:111], v[40:43]\n"
"buffer_load_dwordx4 v[60:63], v33, s[4:7], s98 offen offset:1024 sc1\n"
"v_mfma_f32_16x16x32_bf16 v[112:115], a[92:95], v[108:111], v[112:115]\n"
"s_waitcnt vmcnt(29)\n"
"ds_read_b128 v[88:91], v5 offset:2048\n"
"v_mfma_f32_16x16x32_bf16 v[36:39], a[96:99], v[100:103], v[36:39]\n"
"buffer_load_dwordx4 a[184:187], v1, s[4:7], s96 offen offset:2048 sc1\n"
"v_mfma_f32_16x16x32_bf16 v[128:131], a[100:103], v[100:103], v[128:131]\n"
"ds_read_b128 v[108:111], v6 offset:2048\n"
"ds_read_b128 v[116:119], v5 offset:3072\n"
"v_mfma_f32_16x16x32_bf16 v[144:147], a[104:107], v[100:103], v[144:147]\n"
"ds_read_b128 v[120:123], v6 offset:3072\n"
"s_waitcnt vmcnt(24)\n"
"s_add_u32 s97, s68, 0x1000\n"
"buffer_store_dwordx2 v[18:19], v11, s[4:7], s97 offen offset:96 sc1\n"
"v_mfma_f32_16x16x32_bf16 v[220:223], a[108:111], v[100:103], v[220:223]\n"
"v_mfma_f32_16x16x32_bf16 v[40:43], a[112:115], v[100:103], v[40:43]\n"
"buffer_load_dwordx4 v[132:135], v34, s[4:7], s98 offen sc1\n"
"v_mfma_f32_16x16x32_bf16 v[112:115], a[116:119], v[100:103], v[112:115]\n"
"ds_read_b128 v[100:103], v5 offset:4096\n"
"ds_read_b128 v[136:139], v6 offset:4096\n"
"v_mfma_f32_16x16x32_bf16 v[36:39], a[120:123], v[140:143], v[36:39]\n"
"ds_read_b128 v[200:203], v5 offset:5120\n"
"ds_read_b128 v[204:207], v6 offset:5120\n"
"v_mfma_f32_16x16x32_bf16 v[128:131], a[124:127], v[140:143], v[128:131]\n"
"s_waitcnt lgkmcnt(4)\n"
"v_add_f32_e32 v88, v88, v108\n"
"v_mfma_f32_16x16x32_bf16 v[144:147], v[68:71], v[140:143], v[144:147]\n"
"v_add_f32_e32 v89, v89, v109\n"
"v_add_f32_e32 v90, v90, v110\n"
"v_mfma_f32_16x16x32_bf16 v[220:223], v[72:75], v[140:143], v[220:223]\n"
"ds_read_b128 v[208:211], v5\n"
"ds_read_b128 v[212:215], v6\n"
"v_mfma_f32_16x16x32_bf16 v[40:43], v[76:79], v[140:143], v[40:43]\n"
"buffer_load_dwordx4 v[216:219], v34, s[4:7], s98 offen offset:1024 sc1\n"
"v_mfma_f32_16x16x32_bf16 v[112:115], v[80:83], v[140:143], v[112:115]\n"
"s_waitcnt vmcnt(26)\n"
"ds_read_b128 v[140:143], v5 offset:1024\n"
"v_mfma_f32_16x16x32_bf16 v[36:39], v[148:151], v[24:27], v[36:39]\n"
"buffer_load_dwordx4 a[188:191], v1, s[4:7], s96 offen offset:3072 sc1\n"
"v_mfma_f32_16x16x32_bf16 v[128:131], v[152:155], v[24:27], v[128:131]\n"
"ds_read_b128 v[224:227], v6 offset:1024\n"
"v_add_f32_e32 v91, v91, v111\n"
"v_mfma_f32_16x16x32_bf16 v[144:147], v[156:159], v[24:27], v[144:147]\n"
"v_add_f32_e32 v116, v116, v120\n"
"v_add_f32_e32 v117, v117, v121\n"
"v_mfma_f32_16x16x32_bf16 v[220:223], v[160:163], v[24:27], v[220:223]\n"
"v_add_f32_e32 v118, v118, v122\n"
"v_add_f32_e32 v119, v119, v123\n"
"v_mfma_f32_16x16x32_bf16 v[40:43], v[164:167], v[24:27], v[40:43]\n"
"buffer_load_dwordx4 v[108:111], v35, s[4:7], s98 offen sc1\n"
"v_mfma_f32_16x16x32_bf16 v[112:115], v[168:171], v[24:27], v[112:115]\n"
"s_waitcnt lgkmcnt(4)\n"
"v_add_f32_e32 v100, v100, v136\n"
"v_mfma_f32_16x16x32_bf16 v[36:39], v[172:175], v[196:199], v[36:39]\n"
"v_add_f32_e32 v101, v101, v137\n"
"v_add_f32_e32 v102, v102, v138\n"
"v_mfma_f32_16x16x32_bf16 v[128:131], v[176:179], v[196:199], v[128:131]\n"
"v_add_f32_e32 v103, v103, v139\n"
"v_add_f32_e32 v200, v200, v204\n"
"v_mfma_f32_16x16x32_bf16 v[144:147], v[180:183], v[196:199], v[144:147]\n"
"s_add_u32 s97, s70, 0x4000\n"
"v_add_f32_e32 v201, v201, v205\n"
"v_mfma_f32_16x16x32_bf16 v[220:223], v[184:187], v[196:199], v[220:223]\n"
"v_add_f32_e32 v202, v202, v206\n"
"v_add_f32_e32 v203, v203, v207\n"
"v_mfma_f32_16x16x32_bf16 v[40:43], v[188:191], v[196:199], v[40:43]\n"
"buffer_load_dwordx4 v[24:27], v35, s[4:7], s98 offen offset:1024 sc1\n"
"v_mfma_f32_16x16x32_bf16 v[112:115], v[192:195], v[196:199], v[112:115]\n"
"buffer_load_dwordx4 a[192:195], v0, s[4:7], s97 offen sc1\n"
"s_waitcnt lgkmcnt(0)\n"
"v_add_f32_e32 v208, v208, v212\n"
"v_add_f32_e32 v209, v209, v213\n"
"v_add_f32_e32 v210, v210, v214\n"
"v_add_f32_e32 v211, v211, v215\n"
"v_add_f32_e32 v140, v140, v224\n"
"v_add_f32_e32 v141, v141, v225\n"
"v_add_f32_e32 v142, v142, v226\n"
"v_add_f32_e32 v143, v143, v227\n"
"buffer_load_dwordx4 a[196:199], v0, s[4:7], s97 offen offset:1024 sc1\n"
".Lmk_z3:\n"
"s_waitcnt vmcnt(28)\n"
"; @A G1 3.0\n"
"v_mfma_f32_16x16x32_bf16 v[120:123], a[0:3], v[64:67], 0\n"
"s_add_u32 s98, s62, 0\n"
"v_mfma_f32_16x16x32_bf16 v[136:139], a[4:7], v[64:67], 0\n"
"buffer_load_dwordx2 v[252:253], v10, s[4:7], s98 offen sc1\n"
"v_mfma_f32_16x16x32_bf16 v[196:199], a[8:11], v[64:67], 0\n"
"buffer_load_dwordx2 v[250:251], v10, s[4:7], s98 offen offset:128 sc1\n"
"v_mfma_f32_16x16x32_bf16 v[204:207], a[12:15], v[64:67], 0\n"
"buffer_load_dwordx2 v[248:249], v10, s[4:7], s98 offen offset:256 sc1\n"
"v_mfma_f32_16x16x32_bf16 v[212:215], a[16:19], v[64:67], 0\n"
"s_add_u32 s96, s54, 0\n"
"v_add_f32_e32 v144, v144, v88\n"
"v_mfma_f32_16x16x32_bf16 v[224:227], a[20:23], v[64:67], 0\n"
"buffer_load_dwordx4 v[64:67], v32, s[4:7], s96 offen sc1\n"
"v_mfma_f32_16x16x32_bf16 v[120:123], a[24:27], v[96:99], v[120:123]\n"
"buffer_load_dwordx2 v[246:247], v10, s[4:7], s98 offen offset:384 sc1\n"
"v_mfma_f32_16x16x32_bf16 v[136:139], a[28:31], v[96:99], v[136:139]\n"
"v_add_f32_e32 v145, v145, v89\n"
"v_add_f32_e32 v146, v146, v90\n"
"v_mfma_f32_16x16x32_bf16 v[196:199], a[32:35], v[96:99], v[196:199]\n"
"buffer_load_dwordx4 a[200:203], v0, s[4:7], s97 offen offset:2048 sc1\n"
"v_mfma_f32_16x16x32_bf16 v[204:207], a[36:39], v[96:99], v[204:207]\n"
"v_add_f32_e32 v147, v147, v91\n"
"v_add_f32_e32 v220, v220, v116\n"
"v_mfma_f32_16x16x32_bf16 v[212:215], a[40:43], v[96:99], v[212:215]\n"
"buffer_load_dwordx4 v[88:91], v32, s[4:7], s96 offen offset:1024 sc1\n"
"v_mfma_f32_16x16x32_bf16 v[224:227], a[44:47], v[96:99], v[224:227]\n"
"s_waitcnt vmcnt(30)\n"
"v_add_f32_e32 v221, v221, v117\n"
"v_mfma_f32_16x16x32_bf16 v[120:123], a[48:51], v[84:87], v[120:123]\n"
"v_add_f32_e32 v222, v222, v118\n"
"v_add_f32_e32 v223, v223, v119\n"
"v_mfma_f32_16x16x32_bf16 v[136:139], a[52:55], v[84:87], v[136:139]\n"
"v_mul_f32_e32 v144, v144, v255\n"
"v_mul_f32_e32 v145, v145, v255\n"
"v_mfma_f32_16x16x32_bf16 v[196:199], a[56:59], v[84:87], v[196:199]\n"
"v_mul_f32_e32 v146, v146, v255\n"
"v_mul_f32_e32 v147, v147, v255\n"
"v_mfma_f32_16x16x32_bf16 v[204:207], a[60:63], v[84:87], v[204:207]\n"
"v_mul_f32_e32 v220, v220, v255\n"
"v_mul_f32_e32 v221, v221, v255\n"
"v_mfma_f32_16x16x32_bf16 v[212:215], a[64:67], v[84:87], v[212:215]\n"
"buffer_load_dwordx4 v[96:99], v33, s[4:7], s96 offen sc1\n"
"v_mfma_f32_16x16x32_bf16 v[224:227], a[68:71], v[84:87], v[224:227]\n"
"v_mul_f32_e32 v222, v222, v255\n"
"v_mul_f32_e32 v223, v223, v255\n"
"v_mfma_f32_16x16x32_bf16 v[120:123], a[72:75], v[92:95], v[120:123]\n"
"v_cvt_pk_bf16_f32 v84, v144, v145\n"
"v_cvt_pk_bf16_f32 v85, v146, v147\n"
"v_mfma_f32_16x16x32_bf16 v[136:139], a[76:79], v[92:95], v[136:139]\n"
"v_cvt_pk_bf16_f32 v86, v220, v221\n"
"v_cvt_pk_bf16_f32 v87, v222, v223\n"
"v_mfma_f32_16x16x32_bf16 v[196:199], a[80:83], v[92:95], v[196:199]\n"
"ds_write_b128 v8, v[84:87] offset:17408\n"
"v_mfma_f32_16x16x32_bf16 v[204:207], a[84:87], v[92:95], v[204:207]\n"
"buffer_store_dwordx4 v[84:87], v8, s[4:7], s64 offen offset:1024 sc1\n"
"v_mfma_f32_16x16x32_bf16 v[212:215], a[88:91], v[92:95], v[212:215]\n"
"buffer_load_dwordx4 v[84:87], v33, s[4:7], s96 offen offset:1024 sc1\n"
"v_mfma_f32_16x16x32_bf16 v[224:227], a[92:95], v[92:95], v[224:227]\n"
"s_waitcnt vmcnt(29)\n"
"v_add_f32_e32 v40, v40, v100\n"
"v_mfma_f32_16x16x32_bf16 v[120:123], a[96:99], v[124:127], v[120:123]\n"
"v_add_f32_e32 v41, v41, v101\n"
"v_add_f32_e32 v42, v42, v102\n"
"v_mfma_f32_16x16x32_bf16 v[136:139], a[100:103], v[124:127], v[136:139]\n"
"v_add_f32_e32 v43, v43, v103\n"
"v_add_f32_e32 v112, v112, v200\n"
"v_mfma_f32_16x16x32_bf16 v[196:199], a[104:107], v[124:127], v[196:199]\n"
"buffer_load_dwordx4 a[204:207], v0, s[4:7], s97 offen offset:3072 sc1\n"
"v_mfma_f32_16x16x32_bf16 v[204:207], a[108:111], v[124:127], v[204:207]\n"
"v_add_f32_e32 v113, v113, v201\n"
"v_add_f32_e32 v114, v114, v202\n"
"v_mfma_f32_16x16x32_bf16 v[212:215], a[112:115], v[124:127], v[212:215]\n"
"buffer_load_dwordx4 v[92:95], v34, s[4:7], s96 offen sc1\n"
"v_mfma_f32_16x16x32_bf16 v[224:227], a[116:119], v[124:127], v[224:227]\n"
"v_add_f32_e32 v115, v115, v203\n"
"v_mul_f32_e32 v40, v40, v255\n"
"v_mfma_f32_16x16x32_bf16 v[120:123], a[120:123], v[52:55], v[120:123]\n"
"v_mul_f32_e32 v41, v41, v255\n"
"v_mul_f32_e32 v42, v42, v255\n"
"v_mfma_f32_16x16x32_bf16 v[136:139], a[124:127], v[52:55], v[136:139]\n"
"v_mul_f32_e32 v43, v43, v255\n"
"v_mul_f32_e32 v112, v112, v255\n"
"v_mfma_f32_16x16x32_bf16 v[196:199], v[68:71], v[52:55], v[196:199]\n"
"v_mul_f32_e32 v113, v113, v255\n"
"v_mul_f32_e32 v114, v114, v255\n"
"v_mfma_f32_16x16x32_bf16 v[204:207], v[72:75], v[52:55], v[204:207]\n"
"v_mul_f32_e32 v115, v115, v255\n"
"v_cvt_pk_bf16_f32 v100, v40, v41\n"
"v_mfma_f32_16x16x32_bf16 v[212:215], v[76:79], v[52:55], v[212:215]\n"
"buffer_load_dwordx4 v[116:119], v34, s[4:7], s96 offen offset:1024 sc1\n"
"v_mfma_f32_16x16x32_bf16 v[224:227], v[80:83], v[52:55], v[224:227]\n"
"s_waitcnt vmcnt(28)\n"
"v_cvt_pk_bf16_f32 v101, v42, v43\n"
"v_mfma_f32_16x16x32_bf16 v[120:123], v[148:151], v[28:31], v[120:123]\n"
"v_cvt_pk_bf16_f32 v102, v112, v113\n"
"v_cvt_pk_bf16_f32 v103, v114, v115\n"
"v_mfma_f32_16x16x32_bf16 v[136:139], v[152:155], v[28:31], v[136:139]\n"
"ds_write_b128 v7, v[100:103] offset:50176\n"
"v_mfma_f32_16x16x32_bf16 v[196:199], v[156:159], v[28:31], v[196:199]\n"
"s_add_u32 s98, s64, 0x4000\n"
"v_mfma_f32_16x16x32_bf16 v[204:207], v[160:163], v[28:31], v[204:207]\n"
"buffer_store_dwordx4 v[100:103], v7, s[4:7], s98 offen offset:1024 sc1\n"
"v_mfma_f32_16x16x32_bf16 v[212:215], v[164:167], v[28:31], v[212:215]\n"
"buffer_load_dwordx4 v[40:43], v35, s[4:7], s96 offen sc1\n"
"v_mfma_f32_16x16x32_bf16 v[224:227], v[168:171], v[28:31], v[224:227]\n"
"v_add_f32_e32 v36, v36, v208\n"
"v_add_f32_e32 v37, v37, v209\n"
"v_mfma_f32_16x16x32_bf16 v[120:123], v[172:175], v[104:107], v[120:123]\n"
"v_add_f32_e32 v38, v38, v210\n"
"v_add_f32_e32 v39, v39, v211\n"
"v_mfma_f32_16x16x32_bf16 v[136:139], v[176:179], v[104:107], v[136:139]\n"
"v_add_f32_e32 v128, v128, v140\n"
"v_add_f32_e32 v129, v129, v141\n"
"v_mfma_f32_16x16x32_bf16 v[196:199], v[180:183], v[104:107], v[196:199]\n"
"buffer_load_dwordx4 a[208:211], v1, s[4:7], s97 offen sc1\n"
"v_mfma_f32_16x16x32_bf16 v[204:207], v[184:187], v[104:107], v[204:207]\n"
"v_add_f32_e32 v130, v130, v142\n"
"v_add_f32_e32 v131, v131, v143\n"
"v_mfma_f32_16x16x32_bf16 v[212:215], v[188:191], v[104:107], v[212:215]\n"
"buffer_load_dwordx4 v[28:31], v35, s[4:7], s96 offen offset:1024 sc1\n"
"v_mfma_f32_16x16x32_bf16 v[224:227], v[192:195], v[104:107], v[224:227]\n"
"s_waitcnt vmcnt(29)\n"
"v_mul_f32_e32 v36, v36, v254\n"
"; @A G1 3.1\n"
"v_mfma_f32_16x16x32_bf16 v[52:55], a[0:3], v[44:47], 0\n"
"v_mul_f32_e32 v37, v37, v254\n"
"v_mul_f32_e32 v38, v38, v254\n"
"v_mfma_f32_16x16x32_bf16 v[100:103], a[4:7], v[44:47], 0\n"
"v_mul_f32_e32 v39, v39, v254\n"
"v_mul_f32_e32 v128, v128, v254\n"
"v_mfma_f32_16x16x32_bf16 v[104:107], a[8:11], v[44:47], 0\n"
"v_mul_f32_e32 v129, v129, v254\n"
"v_mul_f32_e32 v130, v130, v254\n"
"v_mfma_f32_16x16x32_bf16 v[112:115], a[12:15], v[44:47], 0\n"
"ds_write_b128 v3, v[120:123]\n"
"v_mfma_f32_16x16x32_bf16 v[120:123], a[16:19], v[44:47], 0\n"
"s_add_u32 s98, s55, 0\n"
"v_mul_f32_e32 v131, v131, v254\n"
"v_mfma_f32_16x16x32_bf16 v[124:127], a[20:23], v[44:47], 0\n"
"buffer_load_dwordx4 v[44:47], v32, s[4:7], s98 offen sc1\n"
"v_mfma_f32_16x16x32_bf16 v[52:55], a[24:27], v[56:59], v[52:55]\n"
"ds_write_b128 v3, v[136:139] offset:1024\n"
"v_mfma_f32_16x16x32_bf16 v[100:103], a[28:31], v[56:59], v[100:103]\n"
"v_cvt_pk_bf16_f32 v136, v36, v37\n"
"v_cvt_pk_bf16_f32 v137, v38, v39\n"
"v_mfma_f32_16x16x32_bf16 v[104:107], a[32:35], v[56:59], v[104:107]\n"
"v_cvt_pk_bf16_f32 v138, v128, v129\n"
"v_cvt_pk_bf16_f32 v139, v130, v131\n"
"v_mfma_f32_16x16x32_bf16 v[112:115], a[36:39], v[56:59], v[112:115]\n"
"ds_write_b128 v3, v[196:199] offset:2048\n"
"v_mfma_f32_16x16x32_bf16 v[120:123], a[40:43], v[56:59], v[120:123]\n"
"buffer_load_dwordx4 v[36:39], v32, s[4:7], s98 offen offset:1024 sc1\n"
"v_mfma_f32_16x16x32_bf16 v[124:127], a[44:47], v[56:59], v[124:127]\n"
"s_waitcnt vmcnt(28)\n"
"v_mov_b32_e32 v56, -1\n"
"v_mfma_f32_16x16x32_bf16 v[52:55], a[48:51], v[48:51], v[52:55]\n"
"ds_write_b128 v3, v[204:207] offset:3072\n"
"v_mfma_f32_16x16x32_bf16 v[100:103], a[52:55], v[48:51], v[100:103]\n"
"ds_write_b128 v2, v[136:139] offset:17408\n"
"v_mfma_f32_16x16x32_bf16 v[104:107], a[56:59], v[48:51], v[104:107]\n"
"buffer_load_dwordx4 a[212:215], v1, s[4:7], s97 offen offset:1024 sc1\n"
"v_mfma_f32_16x16x32_bf16 v[112:115], a[60:63], v[48:51], v[112:115]\n"
"ds_write_b128 v3, v[212:215] offset:4096\n"
"v_mfma_f32_16x16x32_bf16 v[120:123], a[64:67], v[48:51], v[120:123]\n"
"buffer_load_dwordx4 v[128:131], v33, s[4:7], s98 offen sc1\n"
"v_mfma_f32_16x16x32_bf16 v[124:127], a[68:71], v[48:51], v[124:127]\n"
"v_mov_b32_e32 v57, -1\n"
"v_mov_b32_e32 v58, -1\n"
"v_mfma_f32_16x16x32_bf16 v[52:55], a[72:75], v[60:63], v[52:55]\n"
"ds_write_b128 v3, v[224:227] offset:5120\n"
"v_mfma_f32_16x16x32_bf16 v[100:103], a[76:79], v[60:63], v[100:103]\n"
"v_mov_b32_e32 v59, -1\n"
"v_mfma_f32_16x16x32_bf16 v[104:107], a[80:83], v[60:63], v[104:107]\n"
"s_add_u32 s96, s51, 0x800\n"
"s_bitcmp1_b32 s26, 0\n"
"s_cbranch_scc1 .Lmk_w9s\n"
"buffer_store_dwordx4 a[8:11], v0, s[4:7], s96 offen sc1\n"
".Lmk_w9r:\n"
"v_mfma_f32_16x16x32_bf16 v[112:115], a[84:87], v[60:63], v[112:115]\n"
"s_add_u32 s96, s51, 0xc00\n"
"s_bitcmp1_b32 s26, 0\n"
"s_cbranch_scc1 .Lmk_w10s\n"
"buffer_store_dwordx4 a[12:15], v0, s[4:7], s96 offen sc1\n"
".Lmk_w10r:\n"
"v_mfma_f32_16x16x32_bf16 v[120:123], a[88:91], v[60:63], v[120:123]\n"
"buffer_load_dwordx4 v[48:51], v33, s[4:7], s98 offen offset:1024 sc1\n"
"v_mfma_f32_16x16x32_bf16 v[124:127], a[92:95], v[60:63], v[124:127]\n"
"s_waitcnt vmcnt(29)\n"
"v_mfma_f32_16x16x32_bf16 v[52:55], a[96:99], v[132:135], v[52:55]\n"
"s_add_u32 s96, s51, 0x1000\n"
"s_bitcmp1_b32 s26, 0\n"
"s_cbranch_scc1 .Lmk_w11s\n"
"buffer_store_dwordx4 a[16:19], v0, s[4:7], s96 offen sc1\n"
".Lmk_w11r:\n"
"v_mfma_f32_16x16x32_bf16 v[100:103], a[100:103], v[132:135], v[100:103]\n"
"v_mfma_f32_16x16x32_bf16 v[104:107], a[104:107], v[132:135], v[104:107]\n"
"v_mfma_f32_16x16x32_bf16 v[112:115], a[108:111], v[132:135], v[112:115]\n"
"v_mfma_f32_16x16x32_bf16 v[120:123], a[112:115], v[132:135], v[120:123]\n"
"buffer_load_dwordx4 v[60:63], v34, s[4:7], s98 offen sc1\n"
"v_mfma_f32_16x16x32_bf16 v[124:127], a[116:119], v[132:135], v[124:127]\n"
"s_waitcnt lgkmcnt(0)\n"
"s_barrier\n"
"v_mfma_f32_16x16x32_bf16 v[52:55], a[120:123], v[216:219], v[52:55]\n"
"s_waitcnt vmcnt(21)\n"
"v_add_f32_e32 v252, v252, v250\n"
"v_mfma_f32_16x16x32_bf16 v[100:103], a[124:127], v[216:219], v[100:103]\n"
"v_add_f32_e32 v253, v253, v251\n"
"v_add_f32_e32 v248, v248, v246\n"
"v_mfma_f32_16x16x32_bf16 v[104:107], v[68:71], v[216:219], v[104:107]\n"
"buffer_load_dwordx4 a[216:219], v1, s[4:7], s97 offen offset:2048 sc1\n"
"v_mfma_f32_16x16x32_bf16 v[112:115], v[72:75], v[216:219], v[112:115]\n"
"v_add_f32_e32 v249, v249, v247\n"
"v_add_f32_e32 v252, v252, v248\n"
"v_mfma_f32_16x16x32_bf16 v[120:123], v[76:79], v[216:219], v[120:123]\n"
"buffer_load_dwordx4 v[132:135], v34, s[4:7], s98 offen offset:1024 sc1\n"
"v_mfma_f32_16x16x32_bf16 v[124:127], v[80:83], v[216:219], v[124:127]\n"
"v_add_f32_e32 v253, v253, v249\n"
"v_mfma_f32_16x16x32_bf16 v[52:55], v[148:151], v[108:111], v[52:55]\n"
"s_nop 0\n"
"v_permlane16_swap_b32_e32 v252, v253\n"
"v_add_f32_e32 v252, v252, v253\n"
"s_add_u32 s96, s51, 0x1400\n"
"v_mfma_f32_16x16x32_bf16 v[100:103], v[152:155], v[108:111], v[100:103]\n"
"v_mov_b32_e32 v253, v252\n"
"s_bitcmp1_b32 s26, 0\n"
"s_cbranch_scc1 .Lmk_w12s\n"
"buffer_store_dwordx4 a[20:23], v0, s[4:7], s96 offen sc1\n"
".Lmk_w12r:\n"
"v_mfma_f32_16x16x32_bf16 v[104:107], v[156:159], v[108:111], v[104:107]\n"
"s_add_u32 s96, s75, 0x1000\n"
"v_permlane32_swap_b32_e32 v252, v253\n"
"v_mfma_f32_16x16x32_bf16 v[112:115], v[160:163], v[108:111], v[112:115]\n"
"buffer_load_dwordx2 v[254:255], v13, s[4:7], s96 offen sc1\n"
"v_mfma_f32_16x16x32_bf16 v[120:123], v[164:167], v[108:111], v[120:123]\n"
"buffer_load_dwordx4 v[136:139], v35, s[4:7], s98 offen sc1\n"
"v_mfma_f32_16x16x32_bf16 v[124:127], v[168:171], v[108:111], v[124:127]\n"
"v_add_f32_e32 v252, v252, v253\n"
"v_mul_f32_e32 v252, 0x3a800000, v252\n"
"v_mfma_f32_16x16x32_bf16 v[52:55], v[172:175], v[24:27], v[52:55]\n"
"s_add_u32 s95, s71, 0\n"
"v_mov_b32_e32 v253, v252\n"
"v_mfma_f32_16x16x32_bf16 v[100:103], v[176:179], v[24:27], v[100:103]\n"
"buffer_load_dwordx2 v[250:251], off, s[4:7], s95 sc1\n"
"v_mfma_f32_16x16x32_bf16 v[104:107], v[180:183], v[24:27], v[104:107]\n"
"v_permlane16_swap_b32_e32 v252, v253\n"
"v_cvt_pk_bf16_f32 v249, -v252, 0\n"
"v_mfma_f32_16x16x32_bf16 v[112:115], v[184:187], v[24:27], v[112:115]\n"
"v_fma_f32 v253, -v252, v252, v253\n"
"v_lshlrev_b32_e32 v249, 16, v249\n"
"v_mfma_f32_16x16x32_bf16 v[120:123], v[188:191], v[24:27], v[120:123]\n"
"buffer_load_dwordx4 v[108:111], v35, s[4:7], s98 offen offset:1024 sc1\n"
"v_mfma_f32_16x16x32_bf16 v[124:127], v[192:195], v[24:27], v[124:127]\n"
"s_waitcnt vmcnt(26)\n"
"ds_read_b128 v[24:27], v4\n"
"; @A G1 3.2\n"
"v_mfma_f32_16x16x32_bf16 v[32:35], a[0:3], v[64:67], 0\n"
"ds_read_b128 v[140:143], v9\n"
"ds_read_b128 v[144:147], v4 offset:1024\n"
"s_add_u32 s98, s61, 0x440000\n"
"buffer_store_dwordx4 v[56:59], v0, s[4:7], s98 offen sc1\n"
"v_mfma_f32_16x16x32_bf16 v[196:199], a[4:7], v[64:67], 0\n"
"ds_read_b128 v[200:203], v9 offset:64\n"
"ds_read_b128 v[204:207], v4 offset:2048\n"
"v_mfma_f32_16x16x32_bf16 v[208:211], a[8:11], v[64:67], 0\n"
"buffer_load_dwordx4 a[220:223], v1, s[4:7], s97 offen offset:3072 sc1\n"
"v_mfma_f32_16x16x32_bf16 v[212:215], a[12:15], v[64:67], 0\n"
"ds_write_b128 v3, v[52:55] offset:6144\n"
"v_mfma_f32_16x16x32_bf16 v[52:55], a[16:19], v[64:67], 0\n"
"s_add_u32 s98, s70, 0x6000\n"
"ds_read_b128 v[216:219], v9 offset:128\n"
"v_mfma_f32_16x16x32_bf16 v[220:223], a[20:23], v[64:67], 0\n"
"ds_read_b128 v[64:67], v4 offset:3072\n"
"ds_read_b128 v[224:227], v9 offset:192\n"
"v_mfma_f32_16x16x32_bf16 v[32:35], a[24:27], v[88:91], v[32:35]\n"
"ds_write_b128 v3, v[100:103] offset:7168\n"
"v_mfma_f32_16x16x32_bf16 v[196:199], a[28:31], v[88:91], v[196:199]\n"
"buffer_load_dwordx4 a[224:227], v0, s[4:7], s98 offen sc1\n"
"v_mfma_f32_16x16x32_bf16 v[208:211], a[32:35], v[88:91], v[208:211]\n"
"ds_read_b128 v[100:103], v4 offset:4096\n"
"ds_read_b128 v[228:231], v9 offset:256\n"
"v_mfma_f32_16x16x32_bf16 v[212:215], a[36:39], v[88:91], v[212:215]\n"
"ds_write_b128 v3, v[104:107] offset:8192\n"
"v_mfma_f32_16x16x32_bf16 v[52:55], a[40:43], v[88:91], v[52:55]\n"
"s_waitcnt lgkmcnt(6)\n"
"v_fmac_f32_e32 v24, v249, v140\n"
"v_mfma_f32_16x16x32_bf16 v[220:223], a[44:47], v[88:91], v[220:223]\n"
"s_waitcnt vmcnt(26)\n"
"v_fmac_f32_e32 v25, v249, v141\n"
"v_mfma_f32_16x16x32_bf16 v[32:35], a[48:51], v[96:99], v[32:35]\n"
"ds_write_b128 v3, v[112:115] offset:9216\n"
"s_add_u32 s97, s61, 0x440000\n"
"v_mfma_f32_16x16x32_bf16 v[196:199], a[52:55], v[96:99], v[196:199]\n"
"buffer_load_dwordx4 a[228:231], v0, s[4:7], s98 offen offset:1024 sc1\n"
"buffer_store_dwordx4 v[56:59], v0, s[4:7], s97 offen offset:1024 sc1\n"
"v_mfma_f32_16x16x32_bf16 v[208:211], a[56:59], v[96:99], v[208:211]\n"
"v_fmac_f32_e32 v26, v249, v142\n"
"v_fmac_f32_e32 v27, v249, v143\n"
"v_mfma_f32_16x16x32_bf16 v[212:215], a[60:63], v[96:99], v[212:215]\n"
"ds_write_b128 v3, v[120:123] offset:10240\n"
"v_mfma_f32_16x16x32_bf16 v[52:55], a[64:67], v[96:99], v[52:55]\n"
"v_fmac_f32_e32 v144, v249, v200\n"
"v_fmac_f32_e32 v145, v249, v201\n"
"v_mfma_f32_16x16x32_bf16 v[220:223], a[68:71], v[96:99], v[220:223]\n"
"buffer_load_dwordx4 a[232:235], v0, s[4:7], s98 offen offset:2048 sc1\n"
"v_mfma_f32_16x16x32_bf16 v[32:35], a[72:75], v[84:87], v[32:35]\n"
"ds_write_b128 v3, v[124:127] offset:11264\n"
"v_mfma_f32_16x16x32_bf16 v[196:199], a[76:79], v[84:87], v[196:199]\n"
"v_fmac_f32_e32 v146, v249, v202\n"
"v_fmac_f32_e32 v147, v249, v203\n"
"v_mfma_f32_16x16x32_bf16 v[208:211], a[80:83], v[84:87], v[208:211]\n"
"v_fmac_f32_e32 v204, v249, v216\n"
"v_fmac_f32_e32 v205, v249, v217\n"
"v_mfma_f32_16x16x32_bf16 v[212:215], a[84:87], v[84:87], v[212:215]\n"
"buffer_load_dwordx4 a[236:239], v0, s[4:7], s98 offen offset:3072 sc1\n"
"v_mfma_f32_16x16x32_bf16 v[52:55], a[88:91], v[84:87], v[52:55]\n"
"v_fmac_f32_e32 v206, v249, v218\n"
"v_fmac_f32_e32 v207, v249, v219\n"
"v_mfma_f32_16x16x32_bf16 v[220:223], a[92:95], v[84:87], v[220:223]\n"
"s_waitcnt vmcnt(27)\n"
"ds_read_b128 v[84:87], v4 offset:5120\n"
"v_mfma_f32_16x16x32_bf16 v[32:35], a[96:99], v[92:95], v[32:35]\n"
"ds_read_b128 v[88:91], v9 offset:320\n"
"v_max_f32_e32 v253, 0, v253\n"
"s_add_u32 s97, s63, 0x44000\n"
"buffer_store_dwordx2 v[56:57], v14, s[4:7], s97 offen sc1\n"
"v_mfma_f32_16x16x32_bf16 v[196:199], a[100:103], v[92:95], v[196:199]\n"
"buffer_load_dwordx4 a[240:243], v1, s[4:7], s98 offen sc1\n"
"v_mfma_f32_16x16x32_bf16 v[208:211], a[104:107], v[92:95], v[208:211]\n"
"v_cvt_pk_bf16_f32 v253, v253, 0\n"
"v_lshlrev_b32_e32 v253, 16, v253\n"
"v_mfma_f32_16x16x32_bf16 v[212:215], a[108:111], v[92:95], v[212:215]\n"
"v_add_f32_e32 v253, 0x3727c5ac, v253\n"
"v_cvt_pk_bf16_f32 v253, v253, 0\n"
"v_mfma_f32_16x16x32_bf16 v[52:55], a[112:115], v[92:95], v[52:55]\n"
"v_lshlrev_b32_e32 v253, 16, v253\n"
"s_mov_b32 s20, 0xff0000\n"
"v_mfma_f32_16x16x32_bf16 v[220:223], a[116:119], v[92:95], v[220:223]\n"
"buffer_load_dwordx4 a[244:247], v1, s[4:7], s98 offen offset:1024 sc1\n"
"v_mfma_f32_16x16x32_bf16 v[32:35], a[120:123], v[116:119], v[32:35]\n"
"s_waitcnt lgkmcnt(0)\n"
"v_fmac_f32_e32 v64, v249, v224\n"
"v_mfma_f32_16x16x32_bf16 v[196:199], a[124:127], v[116:119], v[196:199]\n"
"v_fmac_f32_e32 v65, v249, v225\n"
"v_fmac_f32_e32 v66, v249, v226\n"
"v_mfma_f32_16x16x32_bf16 v[208:211], v[68:71], v[116:119], v[208:211]\n"
"v_fmac_f32_e32 v67, v249, v227\n"
"v_fmac_f32_e32 v100, v249, v228\n"
"v_mfma_f32_16x16x32_bf16 v[212:215], v[72:75], v[116:119], v[212:215]\n"
"buffer_load_dwordx4 a[248:251], v1, s[4:7], s98 offen offset:2048 sc1\n"
"v_mfma_f32_16x16x32_bf16 v[52:55], v[76:79], v[116:119], v[52:55]\n"
"v_fmac_f32_e32 v101, v249, v229\n"
"v_fmac_f32_e32 v102, v249, v230\n"
"v_mfma_f32_16x16x32_bf16 v[220:223], v[80:83], v[116:119], v[220:223]\n"
"s_waitcnt vmcnt(27)\n"
"v_fmac_f32_e32 v103, v249, v231\n"
"v_mfma_f32_16x16x32_bf16 v[32:35], v[148:151], v[40:43], v[32:35]\n"
"s_waitcnt vmcnt(14)\n"
"v_cmp_ne_u64_e32 vcc, v[254:255], v[18:19]\n"
"v_mfma_f32_16x16x32_bf16 v[196:199], v[152:155], v[40:43], v[196:199]\n"
"s_cbranch_vccnz .Lmk_f6s\n"
".Lmk_f6r:\n"
"v_fmac_f32_e32 v84, v249, v88\n"
"v_mfma_f32_16x16x32_bf16 v[208:211], v[156:159], v[40:43], v[208:211]\n"
"buffer_load_dwordx4 a[252:255], v1, s[4:7], s98 offen offset:3072 sc1\n"
"v_mfma_f32_16x16x32_bf16 v[212:215], v[160:163], v[40:43], v[212:215]\n"
"v_fmac_f32_e32 v85, v249, v89\n"
"v_fmac_f32_e32 v86, v249, v90\n"
"v_mfma_f32_16x16x32_bf16 v[52:55], v[164:167], v[40:43], v[52:55]\n"
"v_fmac_f32_e32 v87, v249, v91\n"
"s_mov_b32 s21, 0xffffffff\n"
"v_mfma_f32_16x16x32_bf16 v[220:223], v[168:171], v[40:43], v[220:223]\n"
"v_sqrt_f32_e32 v253, v253\n"
"v_mfma_f32_16x16x32_bf16 v[32:35], v[172:175], v[28:31], v[32:35]\n"
"v_cvt_pk_bf16_f32 v253, v253, 0\n"
"v_lshlrev_b32_e32 v253, 16, v253\n"
"v_mfma_f32_16x16x32_bf16 v[196:199], v[176:179], v[28:31], v[196:199]\n"
"v_rcp_f32_e32 v255, v253\n"
"v_mfma_f32_16x16x32_bf16 v[208:211], v[180:183], v[28:31], v[208:211]\n"
"s_waitcnt vmcnt(13)\n"
"v_cmp_ne_u64_e32 vcc, v[250:251], v[18:19]\n"
"v_mfma_f32_16x16x32_bf16 v[212:215], v[184:187], v[28:31], v[212:215]\n"
"s_cbranch_vccnz .Lmk_f3s\n"
".Lmk_f3r:\n"
"s_add_u32 m0, s77, 0x6000\n"
"v_mfma_f32_16x16x32_bf16 v[52:55], v[188:191], v[28:31], v[52:55]\n"
"s_add_u32 s98, s65, 0x2000\n"
"v_mul_f32_e32 v254, 0x3e8293ee, v255\n"
"v_mfma_f32_16x16x32_bf16 v[220:223], v[192:195], v[28:31], v[220:223]\n"
"s_mov_b32 s22, 0x1ff0001\n"
"; @A G1 3.3\n"
"v_mfma_f32_16x16x32_bf16 v[24:27], a[0:3], v[44:47], v[24:27]\n"
"buffer_load_dwordx4 v2, s[4:7], s98 offen sc1 lds\n"
"v_mfma_f32_16x16x32_bf16 v[144:147], a[4:7], v[44:47], v[144:147]\n"
"buffer_load_dwordx4 v2, s[4:7], s98 offen offset:1024 sc1 lds\n"
"v_mfma_f32_16x16x32_bf16 v[204:207], a[8:11], v[44:47], v[204:207]\n"
"s_mov_b32 s23, 0xffffffff\n"
"s_mov_b32 s24, 0x3ff0003\n"
"v_mfma_f32_16x16x32_bf16 v[64:67], a[12:15], v[44:47], v[64:67]\n"
"ds_write_b128 v3, v[32:35] offset:12288\n"
"v_mfma_f32_16x16x32_bf16 v[100:103], a[16:19], v[44:47], v[100:103]\n"
"s_mov_b32 s25, 0xffffffff\n"
"s_mov_b32 s26, 0x7ff0007\n"
"v_mfma_f32_16x16x32_bf16 v[84:87], a[20:23], v[44:47], v[84:87]\n"
"s_mov_b32 s27, 0xffffffff\n"
"s_mov_b32 s28, 0xfff000f\n"
"v_mfma_f32_16x16x32_bf16 v[24:27], a[24:27], v[36:39], v[24:27]\n"
"ds_write_b128 v3, v[196:199] offset:13312\n"
"v_mfma_f32_16x16x32_bf16 v[144:147], a[28:31], v[36:39], v[144:147]\n"
"s_add_u32 m0, s77, 0xe000\n"
"s_add_u32 s98, s65, 0x6000\n"
"v_mfma_f32_16x16x32_bf16 v[204:207], a[32:35], v[36:39], v[204:207]\n"
"buffer_load_dwordx4 v2, s[4:7], s98 offen sc1 lds\n"
"v_mfma_f32_16x16x32_bf16 v[64:67], a[36:39], v[36:39], v[64:67]\n"
"ds_write_b128 v3, v[208:211] offset:14336\n"
"v_mfma_f32_16x16x32_bf16 v[100:103], a[40:43], v[36:39], v[100:103]\n"
"buffer_load_dwordx4 v2, s[4:7], s98 offen offset:1024 sc1 lds\n"
"v_mfma_f32_16x16x32_bf16 v[84:87], a[44:47], v[36:39], v[84:87]\n"
"s_mov_b32 s29, 0xffffffff\n"
"v_mfma_f32_16x16x32_bf16 v[24:27], a[48:51], v[128:131], v[24:27]\n"
"ds_write_b128 v3, v[212:215] offset:15360\n"
"v_mfma_f32_16x16x32_bf16 v[144:147], a[52:55], v[128:131], v[144:147]\n"
"s_mov_b32 s30, 0x1fff001f\n"
"s_mov_b32 s31, 0xffffffff\n"
"v_mfma_f32_16x16x32_bf16 v[204:207], a[56:59], v[128:131], v[204:207]\n"
"s_mov_b32 s32, 0x3fff003f\n"
"s_mov_b32 s33, 0xffffffff\n"
"v_mfma_f32_16x16x32_bf16 v[64:67], a[60:63], v[128:131], v[64:67]\n"
"ds_write_b128 v3, v[52:55] offset:16384\n"
"v_mfma_f32_16x16x32_bf16 v[100:103], a[64:67], v[128:131], v[100:103]\n"
"s_add_u32 s98, s69, 0x600000\n"
"s_mov_b32 s34, 0x7fff007f\n"
"v_mfma_f32_16x16x32_bf16 v[84:87], a[68:71], v[128:131], v[84:87]\n"
"buffer_load_dwordx4 a[0:3], v0, s[4:7], s98 offen sc1\n"
"v_mfma_f32_16x16x32_bf16 v[24:27], a[72:75], v[48:51], v[24:27]\n"
"ds_write_b128 v3, v[220:223] offset:17408\n"
"v_mfma_f32_16x16x32_bf16 v[144:147], a[76:79], v[48:51], v[144:147]\n"
"s_mov_b32 s35, 0xffffffff\n"
"s_mov_b32 s36, 0\n"
"v_mfma_f32_16x16x32_bf16 v[204:207], a[80:83], v[48:51], v[204:207]\n"
"s_mov_b32 s37, 0xff0000\n"
"s_mov_b32 s38, 0\n"
"v_mfma_f32_16x16x32_bf16 v[64:67], a[84:87], v[48:51], v[64:67]\n"
"s_waitcnt lgkmcnt(0)\n"
"s_barrier\n"
"v_mfma_f32_16x16x32_bf16 v[100:103], a[88:91], v[48:51], v[100:103]\n"
"buffer_load_dwordx4 a[4:7], v0, s[4:7], s98 offen offset:1024 sc1\n"
"v_mfma_f32_16x16x32_bf16 v[84:87], a[92:95], v[48:51], v[84:87]\n"
"ds_read_b128 v[28:31], v5 offset:2048\n"
"v_mfma_f32_16x16x32_bf16 v[24:27], a[96:99], v[60:63], v[24:27]\n"
"ds_read_b128 v[32:35], v6 offset:2048\n"
"ds_read_b128 v[36:39], v5 offset:3072\n"
"v_mfma_f32_16x16x32_bf16 v[144:147], a[100:103], v[60:63], v[144:147]\n"
"ds_read_b128 v[40:43], v6 offset:3072\n"
"s_mov_b32 s39, 0x1ff0001\n"
"v_mfma_f32_16x16x32_bf16 v[204:207], a[104:107], v[60:63], v[204:207]\n"
"s_mov_b32 s40, 0\n"
"s_mov_b32 s41, 0x3ff0003\n"
"v_mfma_f32_16x16x32_bf16 v[64:67], a[108:111], v[60:63], v[64:67]\n"
"buffer_load_dwordx4 a[8:11], v0, s[4:7], s98 offen offset:2048 sc1\n"
"v_mfma_f32_16x16x32_bf16 v[100:103], a[112:115], v[60:63], v[100:103]\n"
"ds_read_b128 v[44:47], v5 offset:4096\n"
"ds_read_b128 v[48:51], v6 offset:4096\n"
"v_mfma_f32_16x16x32_bf16 v[84:87], a[116:119], v[60:63], v[84:87]\n"
"ds_read_b128 v[52:55], v5 offset:5120\n"
"ds_read_b128 v[56:59], v6 offset:5120\n"
"v_mfma_f32_16x16x32_bf16 v[24:27], a[120:123], v[132:135], v[24:27]\n"
"s_waitcnt lgkmcnt(4)\n"
"v_add_f32_e32 v28, v28, v32\n"
"v_mfma_f32_16x16x32_bf16 v[144:147], a[124:127], v[132:135], v[144:147]\n"
"v_add_f32_e32 v29, v29, v33\n"
"v_add_f32_e32 v30, v30, v34\n"
"v_mfma_f32_16x16x32_bf16 v[204:207], v[68:71], v[132:135], v[204:207]\n"
"buffer_load_dwordx4 a[12:15], v0, s[4:7], s98 offen offset:3072 sc1\n"
"v_mfma_f32_16x16x32_bf16 v[64:67], v[72:75], v[132:135], v[64:67]\n"
"ds_read_b128 v[60:63], v5\n"
"ds_read_b128 v[68:71], v6\n"
"v_mfma_f32_16x16x32_bf16 v[100:103], v[76:79], v[132:135], v[100:103]\n"
"ds_read_b128 v[72:75], v5 offset:1024\n"
"ds_read_b128 v[76:79], v6 offset:1024\n"
"v_mfma_f32_16x16x32_bf16 v[84:87], v[80:83], v[132:135], v[84:87]\n"
"s_waitcnt vmcnt(20)\n"
"v_add_f32_e32 v31, v31, v35\n"
"v_mfma_f32_16x16x32_bf16 v[24:27], v[148:151], v[136:139], v[24:27]\n"
"v_add_f32_e32 v36, v36, v40\n"
"v_add_f32_e32 v37, v37, v41\n"
"s_waitcnt vmcnt(12)\n"
"buffer_store_dwordx2 v[18:19], v11, s[4:7], s68 offen offset:32 sc1\n"
"v_mfma_f32_16x16x32_bf16 v[144:147], v[152:155], v[136:139], v[144:147]\n"
"buffer_load_dwordx4 a[16:19], v1, s[4:7], s98 offen sc1\n"
"v_mfma_f32_16x16x32_bf16 v[204:207], v[156:159], v[136:139], v[204:207]\n"
"v_add_f32_e32 v38, v38, v42\n"
"v_add_f32_e32 v39, v39, v43\n"
"v_mfma_f32_16x16x32_bf16 v[64:67], v[160:163], v[136:139], v[64:67]\n"
"s_waitcnt lgkmcnt(4)\n"
"v_add_f32_e32 v44, v44, v48\n"
"v_mfma_f32_16x16x32_bf16 v[100:103], v[164:167], v[136:139], v[100:103]\n"
"v_add_f32_e32 v45, v45, v49\n"
"v_add_f32_e32 v46, v46, v50\n"
"v_mfma_f32_16x16x32_bf16 v[84:87], v[168:171], v[136:139], v[84:87]\n"
"v_add_f32_e32 v47, v47, v51\n"
"v_add_f32_e32 v52, v52, v56\n"
"v_mfma_f32_16x16x32_bf16 v[24:27], v[172:175], v[108:111], v[24:27]\n"
"buffer_load_dwordx4 a[20:23], v1, s[4:7], s98 offen offset:1024 sc1\n"
"v_mfma_f32_16x16x32_bf16 v[144:147], v[176:179], v[108:111], v[144:147]\n"
"v_add_f32_e32 v53, v53, v57\n"
"v_add_f32_e32 v54, v54, v58\n"
"v_mfma_f32_16x16x32_bf16 v[204:207], v[180:183], v[108:111], v[204:207]\n"
"v_add_f32_e32 v55, v55, v59\n"
"s_waitcnt lgkmcnt(0)\n"
"v_mfma_f32_16x16x32_bf16 v[64:67], v[184:187], v[108:111], v[64:67]\n"
"v_add_f32_e32 v60, v60, v68\n"
"v_add_f32_e32 v61, v61, v69\n"
"v_mfma_f32_16x16x32_bf16 v[100:103], v[188:191], v[108:111], v[100:103]\n"
"v_add_f32_e32 v62, v62, v70\n"
"v_add_f32_e32 v63, v63, v71\n"
"v_mfma_f32_16x16x32_bf16 v[84:87], v[192:195], v[108:111], v[84:87]\n"
"buffer_load_dwordx4 a[24:27], v1, s[4:7], s98 offen offset:2048 sc1\n"
"v_add_f32_e32 v72, v72, v76\n"
"v_add_f32_e32 v73, v73, v77\n"
"v_add_f32_e32 v74, v74, v78\n"
"v_add_f32_e32 v75, v75, v79\n"
"s_mov_b32 s42, 0\n"
"s_mov_b32 s43, 0x7ff0007\n"
"s_mov_b32 s44, 0\n"
"s_mov_b32 s45, 0xfff000f\n"
"s_mov_b32 s46, 0\n"
"s_mov_b32 s47, 0x1fff001f\n"
"s_mov_b32 s48, 0\n"
"s_mov_b32 s49, 0x3fff003f\n"
"s_mov_b32 s50, 0\n"
"s_mov_b32 s51, 0x7fff007f\n"
"v_xor_b32_e32 v253, 32, v16\n"
"v_xor_b32_e32 v252, 32, v15\n"
"v_mov_b32_e32 v88, 0x3f803f80\n"
"v_mov_b32_e32 v89, 0x3f803f80\n"
"v_mov_b32_e32 v90, 0x3f803f80\n"
"v_mov_b32_e32 v91, 0x3f803f80\n"
"v_mov_b32_e32 v251, 0x53800000\n"
"buffer_load_dwordx4 a[28:31], v1, s[4:7], s98 offen offset:3072 sc1\n"
".Lmk_z4:\n"
"; @A tail\n"
"v_add_f32_e32 v204, v204, v28\n"
"v_add_f32_e32 v205, v205, v29\n"
"v_add_f32_e32 v206, v206, v30\n"
"v_add_f32_e32 v207, v207, v31\n"
"v_add_f32_e32 v64, v64, v36\n"
"v_add_f32_e32 v65, v65, v37\n"
"v_add_f32_e32 v66, v66, v38\n"
"v_add_f32_e32 v67, v67, v39\n"
"v_mul_f32_e32 v204, v204, v255\n"
"v_mul_f32_e32 v205, v205, v255\n"
"v_mul_f32_e32 v206, v206, v255\n"
"v_mul_f32_e32 v207, v207, v255\n"
"v_mul_f32_e32 v64, v64, v255\n"
"v_mul_f32_e32 v65, v65, v255\n"
"v_mul_f32_e32 v66, v66, v255\n"
"v_mul_f32_e32 v67, v67, v255\n"
"v_cvt_pk_bf16_f32 v28, v204, v205\n"
"v_cvt_pk_bf16_f32 v29, v206, v207\n"
"v_cvt_pk_bf16_f32 v30, v64, v65\n"
"v_cvt_pk_bf16_f32 v31, v66, v67\n"
"ds_write_b128 v7, v[28:31] offset:16384\n"
"buffer_store_dwordx4 v[28:31], v7, s[4:7], s64 offen sc1\n"
"v_add_f32_e32 v100, v100, v44\n"
"v_add_f32_e32 v101, v101, v45\n"
"v_add_f32_e32 v102, v102, v46\n"
"v_add_f32_e32 v103, v103, v47\n"
"v_add_f32_e32 v84, v84, v52\n"
"v_add_f32_e32 v85, v85, v53\n"
"v_add_f32_e32 v86, v86, v54\n"
"v_add_f32_e32 v87, v87, v55\n"
"v_mul_f32_e32 v100, v100, v255\n"
"v_mul_f32_e32 v101, v101, v255\n"
"v_mul_f32_e32 v102, v102, v255\n"
"v_mul_f32_e32 v103, v103, v255\n"
"v_mul_f32_e32 v84, v84, v255\n"
"v_mul_f32_e32 v85, v85, v255\n"
"v_mul_f32_e32 v86, v86, v255\n"
"v_mul_f32_e32 v87, v87, v255\n"
"v_cvt_pk_bf16_f32 v28, v100, v101\n"
"v_cvt_pk_bf16_f32 v29, v102, v103\n"
"v_cvt_pk_bf16_f32 v30, v84, v85\n"
"v_cvt_pk_bf16_f32 v31, v86, v87\n"
"ds_write_b128 v7, v[28:31] offset:49152\n"
"s_add_u32 s98, s64, 0x4000\n"
"buffer_store_dwordx4 v[28:31], v7, s[4:7], s98 offen sc1\n"
"v_add_f32_e32 v24, v24, v60\n"
"v_add_f32_e32 v25, v25, v61\n"
"v_add_f32_e32 v26, v26, v62\n"
"v_add_f32_e32 v27, v27, v63\n"
"v_add_f32_e32 v144, v144, v72\n"
"v_add_f32_e32 v145, v145, v73\n"
"v_add_f32_e32 v146, v146, v74\n"
"v_add_f32_e32 v147, v147, v75\n"
"v_mul_f32_e32 v24, v24, v254\n"
"v_mul_f32_e32 v25, v25, v254\n"
"v_mul_f32_e32 v26, v26, v254\n"
"v_mul_f32_e32 v27, v27, v254\n"
"v_mul_f32_e32 v144, v144, v254\n"
"v_mul_f32_e32 v145, v145, v254\n"
"v_mul_f32_e32 v146, v146, v254\n"
"v_mul_f32_e32 v147, v147, v254\n"
"v_cvt_pk_bf16_f32 v28, v24, v25\n"
"v_cvt_pk_bf16_f32 v29, v26, v27\n"
"v_cvt_pk_bf16_f32 v30, v144, v145\n"
"v_cvt_pk_bf16_f32 v31, v146, v147\n"
"ds_write_b128 v2, v[28:31] offset:16384\n"
"s_waitcnt lgkmcnt(0)\n"
"s_barrier\n"
"ds_read_b128 v[28:31], v2 offset:49152\n"
"ds_read_b128 v[32:35], v2 offset:50176\n"
"ds_read_b128 v[92:95], v2 offset:16384\n"
"ds_read_b128 v[96:99], v2 offset:17408\n"
"; @A AT b\n"
"s_mov_b32 s2, 0\n"
"v_mov_b32_e32 v255, 0xff800000\n"
"v_mov_b32_e32 v254, 0xff800000\n"
"v_mov_b32_e32 v250, 0x7f800000\n"
"v_mov_b32_e32 v249, 0x7f800000\n"
".Lmk_a20:\n"
"ds_read_b128 v[24:27], v15 offset:24576\n"
"ds_read_b128 v[36:39], v252 offset:24832\n"
"ds_read_b128 v[40:43], v15 offset:22528\n"
"ds_read_b128 v[44:47], v252 offset:22784\n"
"s_cmp_gt_i32 s78, 11\n"
"s_cselect_b32 s98, 0x80000000, 0\n"
"s_or_b32 s98, s98, 0x7f800000\n"
"s_waitcnt lgkmcnt(2)\n"
"v_mfma_f32_16x16x32_bf16 v[48:51], v[24:27], v[28:31], 0\n"
"v_mfma_f32_16x16x32_bf16 v[52:55], v[24:27], v[32:35], 0\n"
"v_mfma_f32_16x16x32_bf16 v[56:59], v[36:39], v[28:31], 0\n"
"v_mfma_f32_16x16x32_bf16 v[60:63], v[36:39], v[32:35], 0\n"
"s_nop 4\n"
"v_max_f32_e32 v248, v48, v49\n"
"v_max_f32_e32 v247, v52, v53\n"
"v_max3_f32 v248, v248, v50, v51\n"
"v_max3_f32 v247, v247, v54, v55\n"
"v_max3_f32 v248, v248, v56, v57\n"
"v_max3_f32 v247, v247, v60, v61\n"
"v_max3_f32 v248, v248, v58, v59\n"
"v_max3_f32 v247, v247, v62, v63\n"
"v_mov_b32_e32 v246, v248\n"
"v_mov_b32_e32 v245, v247\n"
"s_nop 0\n"
"v_permlane16_swap_b32_e32 v248, v246\n"
"v_permlane16_swap_b32_e32 v247, v245\n"
"v_max_f32_e32 v248, v248, v246\n"
"v_max_f32_e32 v247, v247, v245\n"
"v_mov_b32_e32 v246, v248\n"
"v_mov_b32_e32 v245, v247\n"
"s_nop 0\n"
"v_permlane32_swap_b32_e32 v248, v246\n"
"v_permlane32_swap_b32_e32 v247, v245\n"
"v_max_f32_e32 v248, v248, v246\n"
"v_max_f32_e32 v247, v247, v245\n"
"v_med3_f32 v248, v248, v255, v250\n"
"v_med3_f32 v247, v247, v254, v249\n"
"v_min_f32_e64 v24, s98, -v248\n"
"v_min_f32_e64 v25, s98, -v248\n"
"v_min_f32_e64 v26, s98, -v248\n"
"v_min_f32_e64 v27, s98, -v248\n"
"v_min_f32_e64 v36, s98, -v247\n"
"v_min_f32_e64 v37, s98, -v247\n"
"v_min_f32_e64 v38, s98, -v247\n"
"v_min_f32_e64 v39, s98, -v247\n"
"s_waitcnt lgkmcnt(0)\n"
"v_mfma_f32_16x16x32_bf16 v[64:67], v[40:43], v[28:31], v[24:27]\n"
"ds_read_b128 v[68:71], v15 offset:20480\n"
"v_sub_f32_e32 v48, v48, v248\n"
"v_sub_f32_e32 v49, v49, v248\n"
"v_sub_f32_e32 v50, v50, v248\n"
"s_add_u32 s98, s69, 0x602000\n"
"buffer_load_dwordx4 a[32:35], v0, s[4:7], s98 offen sc1\n"
"v_mfma_f32_16x16x32_bf16 v[72:75], v[40:43], v[32:35], v[36:39]\n"
"v_sub_f32_e32 v51, v51, v248\n"
"v_sub_f32_e32 v56, v56, v248\n"
"v_sub_f32_e32 v57, v57, v248\n"
"v_sub_f32_e32 v58, v58, v248\n"
"v_sub_f32_e32 v59, v59, v248\n"
"buffer_load_dwordx4 a[36:39], v0, s[4:7], s98 offen offset:1024 sc1\n"
"v_mfma_f32_16x16x32_bf16 v[40:43], v[44:47], v[28:31], v[24:27]\n"
"ds_read_b128 v[76:79], v252 offset:20736\n"
"v_sub_f32_e32 v52, v52, v247\n"
"v_sub_f32_e32 v53, v53, v247\n"
"v_sub_f32_e32 v54, v54, v247\n"
"v_sub_f32_e32 v55, v55, v247\n"
"v_mfma_f32_16x16x32_bf16 v[80:83], v[44:47], v[32:35], v[36:39]\n"
"buffer_load_dwordx4 a[40:43], v0, s[4:7], s98 offen offset:2048 sc1\n"
"v_sub_f32_e32 v60, v60, v247\n"
"v_sub_f32_e32 v61, v61, v247\n"
"v_sub_f32_e32 v62, v62, v247\n"
"v_sub_f32_e32 v63, v63, v247\n"
"s_waitcnt lgkmcnt(0)\n"
"v_mfma_f32_16x16x32_bf16 v[44:47], v[68:71], v[28:31], v[24:27]\n"
"ds_read_b128 v[84:87], v15 offset:18432\n"
"v_exp_f32_e32 v64, v64\n"
"v_exp_f32_e32 v65, v65\n"
"v_exp_f32_e32 v66, v66\n"
"buffer_load_dwordx4 a[44:47], v0, s[4:7], s98 offen offset:3072 sc1\n"
"ds_read_b128 v[100:103], v252 offset:18688\n"
"v_exp_f32_e32 v67, v67\n"
"v_exp_f32_e32 v40, v40\n"
"v_exp_f32_e32 v41, v41\n"
"ds_read_b64_tr_b16 v[104:105], v16 offset:22528\n"
"v_mfma_f32_16x16x32_bf16 v[108:111], v[68:71], v[32:35], v[36:39]\n"
"v_exp_f32_e32 v42, v42\n"
"buffer_load_dwordx4 a[48:51], v1, s[4:7], s98 offen sc1\n"
"v_exp_f32_e32 v43, v43\n"
"v_cvt_pk_bf16_f32 v68, v64, v65\n"
"v_cvt_pk_bf16_f32 v69, v66, v67\n"
"ds_read_b64_tr_b16 v[106:107], v253 offset:22784\n"
"v_cvt_pk_bf16_f32 v70, v40, v41\n"
"v_cvt_pk_bf16_f32 v71, v42, v43\n"
"v_exp_f32_e32 v72, v72\n"
"buffer_load_dwordx4 a[52:55], v1, s[4:7], s98 offen offset:1024 sc1\n"
"v_exp_f32_e32 v73, v73\n"
"v_mfma_f32_16x16x32_bf16 v[40:43], v[76:79], v[28:31], v[24:27]\n"
"ds_read_b64_tr_b16 v[64:65], v253 offset:22528\n"
"v_exp_f32_e32 v74, v74\n"
"v_exp_f32_e32 v75, v75\n"
"v_exp_f32_e32 v80, v80\n"
"v_bfe_u32 v248, v0, 2, 7\n"
"v_add_u32_e32 v248, s72, v248\n"
"v_and_b32_e32 v247, 0xffffffc0, v248\n"
"v_add_u32_e32 v248, v248, v247\n"
"v_and_b32_e32 v247, 0x200, v0\n"
"v_lshl_add_u32 v248, v247, 4, v248\n"
"s_add_u32 s97, s76, 0x80000\n"
"buffer_load_dword v247, v248, s[4:7], s97 offen sc1\n"
"ds_read_b64_tr_b16 v[66:67], v16 offset:22784\n"
"v_mfma_f32_16x16x32_bf16 v[112:115], v[76:79], v[32:35], v[36:39]\n"
"v_exp_f32_e32 v81, v81\n"
"v_exp_f32_e32 v82, v82\n"
"v_exp_f32_e32 v83, v83\n"
"s_add_u32 s96, s68, 0x10000\n"
"buffer_store_dwordx2 v[18:19], v11, s[4:7], s96 offen offset:96 sc1\n"
"buffer_load_dword v246, v248, s[4:7], s97 offen offset:1024 sc1\n"
"v_cvt_pk_bf16_f32 v76, v72, v73\n"
"v_cvt_pk_bf16_f32 v77, v74, v75\n"
"v_cvt_pk_bf16_f32 v78, v80, v81\n"
"v_cvt_pk_bf16_f32 v79, v82, v83\n"
"s_waitcnt lgkmcnt(4)\n"
"v_mfma_f32_16x16x32_bf16 v[72:75], v[84:87], v[28:31], v[24:27]\n"
"ds_read_b128 v[80:83], v15 offset:16384\n"
"v_exp_f32_e32 v44, v44\n"
"v_exp_f32_e32 v45, v45\n"
"v_exp_f32_e32 v46, v46\n"
"v_mfma_f32_16x16x32_bf16 v[116:119], v[84:87], v[32:35], v[36:39]\n"
"buffer_load_dwordx4 a[56:59], v1, s[4:7], s98 offen offset:2048 sc1\n"
"ds_read_b128 v[84:87], v252 offset:16640\n"
"v_exp_f32_e32 v47, v47\n"
"v_exp_f32_e32 v40, v40\n"
"v_mfma_f32_16x16x32_bf16 v[120:123], v[100:103], v[28:31], v[24:27]\n"
"v_exp_f32_e32 v41, v41\n"
"ds_read_b64_tr_b16 v[124:125], v16 offset:20480\n"
"v_exp_f32_e32 v42, v42\n"
"v_mfma_f32_16x16x32_bf16 v[128:131], v[100:103], v[32:35], v[36:39]\n"
"buffer_load_dwordx4 a[60:63], v1, s[4:7], s98 offen offset:3072 sc1\n"
"v_exp_f32_e32 v43, v43\n"
"v_cvt_pk_bf16_f32 v100, v44, v45\n"
"v_cvt_pk_bf16_f32 v101, v46, v47\n"
"s_waitcnt lgkmcnt(3)\n"
"v_mfma_f32_16x16x32_bf16 v[44:47], v[88:91], v[68:71], 0\n"
"ds_read_b64_tr_b16 v[126:127], v253 offset:20736\n"
"v_cvt_pk_bf16_f32 v102, v40, v41\n"
"v_cvt_pk_bf16_f32 v103, v42, v43\n"
"v_exp_f32_e32 v108, v108\n"
"s_add_u32 s98, s69, 0x604000\n"
"buffer_load_dwordx4 a[64:67], v0, s[4:7], s98 offen sc1\n"
"v_mfma_f32_16x16x32_bf16 v[40:43], v[104:107], v[68:71], 0\n"
"v_exp_f32_e32 v109, v109\n"
"ds_read_b64_tr_b16 v[132:133], v253 offset:20480\n"
"v_exp_f32_e32 v110, v110\n"
"v_mfma_f32_16x16x32_bf16 v[136:139], v[64:67], v[68:71], 0\n"
"v_exp_f32_e32 v111, v111\n"
"buffer_load_dword v245, v248, s[4:7], s97 offen offset:2048 sc1\n"
"v_exp_f32_e32 v112, v112\n"
"v_mfma_f32_16x16x32_bf16 v[68:71], v[88:91], v[76:79], 0\n"
"ds_read_b64_tr_b16 v[134:135], v16 offset:20736\n"
"v_exp_f32_e32 v113, v113\n"
"v_exp_f32_e32 v114, v114\n"
"v_mfma_f32_16x16x32_bf16 v[140:143], v[104:107], v[76:79], 0\n"
"v_exp_f32_e32 v115, v115\n"
"buffer_load_dword v244, v248, s[4:7], s97 offen offset:3072 sc1\n"
"s_waitcnt vmcnt(14)\n"
"buffer_store_dwordx2 v[18:19], v11, s[4:7], s68 offen offset:64 sc1\n"
"v_mfma_f32_16x16x32_bf16 v[104:107], v[64:67], v[76:79], 0\n"
"v_cvt_pk_bf16_f32 v64, v108, v109\n"
"v_cvt_pk_bf16_f32 v65, v110, v111\n"
"v_cvt_pk_bf16_f32 v66, v112, v113\n"
"v_cvt_pk_bf16_f32 v67, v114, v115\n"
"s_cmp_lg_u32 s2, 0\n"
"s_cbranch_scc1 .Lmk_s22\n"
"s_barrier\n"
".Lmk_s22:\n"
"s_waitcnt lgkmcnt(4)\n"
"v_mfma_f32_16x16x32_bf16 v[76:79], v[80:83], v[28:31], v[24:27]\n"
"ds_read_b128 v[108:111], v15 offset:14336\n"
"v_exp_f32_e32 v72, v72\n"
"v_exp_f32_e32 v73, v73\n"
"v_exp_f32_e32 v74, v74\n"
"v_mfma_f32_16x16x32_bf16 v[112:115], v[80:83], v[32:35], v[36:39]\n"
"buffer_load_dwordx4 a[68:71], v0, s[4:7], s98 offen offset:1024 sc1\n"
"v_exp_f32_e32 v75, v75\n"
"ds_read_b128 v[80:83], v252 offset:14592\n"
"v_mfma_f32_16x16x32_bf16 v[144:147], v[84:87], v[28:31], v[24:27]\n"
"v_exp_f32_e32 v120, v120\n"
"v_exp_f32_e32 v121, v121\n"
"v_exp_f32_e32 v122, v122\n"
"v_mfma_f32_16x16x32_bf16 v[148:151], v[84:87], v[32:35], v[36:39]\n"
"buffer_load_dwordx4 a[72:75], v0, s[4:7], s98 offen offset:2048 sc1\n"
"ds_read_b64_tr_b16 v[84:85], v16 offset:18432\n"
"v_exp_f32_e32 v123, v123\n"
"s_waitcnt lgkmcnt(3)\n"
"v_mfma_f32_16x16x32_bf16 v[44:47], v[88:91], v[100:103], v[44:47]\n"
"v_cvt_pk_bf16_f32 v152, v72, v73\n"
"v_cvt_pk_bf16_f32 v153, v74, v75\n"
"v_cvt_pk_bf16_f32 v154, v120, v121\n"
"v_cvt_pk_bf16_f32 v155, v122, v123\n"
"buffer_load_dwordx4 a[76:79], v0, s[4:7], s98 offen offset:3072 sc1\n"
"v_mfma_f32_16x16x32_bf16 v[40:43], v[124:127], v[100:103], v[40:43]\n"
"ds_read_b64_tr_b16 v[86:87], v253 offset:18688\n"
"v_exp_f32_e32 v116, v116\n"
"v_exp_f32_e32 v117, v117\n"
"v_exp_f32_e32 v118, v118\n"
"v_mfma_f32_16x16x32_bf16 v[136:139], v[132:135], v[100:103], v[136:139]\n"
"v_exp_f32_e32 v119, v119\n"
"s_add_u32 s97, s76, 0x81000\n"
"buffer_load_dword v243, v248, s[4:7], s97 offen sc1\n"
"v_mfma_f32_16x16x32_bf16 v[68:71], v[88:91], v[64:67], v[68:71]\n"
"ds_read_b64_tr_b16 v[72:73], v253 offset:18432\n"
"v_exp_f32_e32 v128, v128\n"
"v_exp_f32_e32 v129, v129\n"
"v_mfma_f32_16x16x32_bf16 v[140:143], v[124:127], v[64:67], v[140:143]\n"
"v_exp_f32_e32 v130, v130\n"
"buffer_load_dword v242, v248, s[4:7], s97 offen offset:1024 sc1\n"
"ds_read_b64_tr_b16 v[74:75], v16 offset:18688\n"
"v_exp_f32_e32 v131, v131\n"
"v_mfma_f32_16x16x32_bf16 v[104:107], v[132:135], v[64:67], v[104:107]\n"
"v_cvt_pk_bf16_f32 v64, v116, v117\n"
"v_cvt_pk_bf16_f32 v65, v118, v119\n"
"v_cvt_pk_bf16_f32 v66, v128, v129\n"
"v_cvt_pk_bf16_f32 v67, v130, v131\n"
"s_waitcnt lgkmcnt(4)\n"
"s_cmp_eq_i32 s78, 8\n"
"s_cbranch_scc1 .Lmk_x23s\n"
".Lmk_x23r:\n"
"v_mfma_f32_16x16x32_bf16 v[100:103], v[108:111], v[28:31], v[24:27]\n"
"ds_read_b128 v[116:119], v15 offset:12288\n"
"v_exp_f32_e32 v76, v76\n"
"v_exp_f32_e32 v77, v77\n"
"v_exp_f32_e32 v78, v78\n"
"v_mfma_f32_16x16x32_bf16 v[120:123], v[108:111], v[32:35], v[36:39]\n"
"buffer_load_dwordx4 a[80:83], v1, s[4:7], s98 offen sc1\n"
"v_exp_f32_e32 v79, v79\n"
"ds_read_b128 v[108:111], v252 offset:12544\n"
"v_mfma_f32_16x16x32_bf16 v[124:127], v[80:83], v[28:31], v[24:27]\n"
"v_exp_f32_e32 v144, v144\n"
"v_exp_f32_e32 v145, v145\n"
"v_exp_f32_e32 v146, v146\n"
"v_mfma_f32_16x16x32_bf16 v[128:131], v[80:83], v[32:35], v[36:39]\n"
"buffer_load_dwordx4 a[84:87], v1, s[4:7], s98 offen offset:1024 sc1\n"
"ds_read_b64_tr_b16 v[80:81], v16 offset:16384\n"
"v_exp_f32_e32 v147, v147\n"
"s_waitcnt lgkmcnt(3)\n"
"v_mfma_f32_16x16x32_bf16 v[44:47], v[88:91], v[152:155], v[44:47]\n"
"v_cvt_pk_bf16_f32 v132, v76, v77\n"
"v_cvt_pk_bf16_f32 v133, v78, v79\n"
"v_cvt_pk_bf16_f32 v134, v144, v145\n"
"v_cvt_pk_bf16_f32 v135, v146, v147\n"
"buffer_load_dwordx4 a[88:91], v1, s[4:7], s98 offen offset:2048 sc1\n"
"v_mfma_f32_16x16x32_bf16 v[40:43], v[84:87], v[152:155], v[40:43]\n"
"ds_read_b64_tr_b16 v[82:83], v253 offset:16640\n"
"v_exp_f32_e32 v112, v112\n"
"v_exp_f32_e32 v113, v113\n"
"v_mfma_f32_16x16x32_bf16 v[136:139], v[72:75], v[152:155], v[136:139]\n"
"v_exp_f32_e32 v114, v114\n"
"v_exp_f32_e32 v115, v115\n"
"buffer_load_dword v241, v248, s[4:7], s97 offen offset:2048 sc1\n"
"v_mfma_f32_16x16x32_bf16 v[68:71], v[88:91], v[64:67], v[68:71]\n"
"ds_read_b64_tr_b16 v[76:77], v253 offset:16384\n"
"v_exp_f32_e32 v148, v148\n"
"v_exp_f32_e32 v149, v149\n"
"v_mfma_f32_16x16x32_bf16 v[140:143], v[84:87], v[64:67], v[140:143]\n"
"v_exp_f32_e32 v150, v150\n"
"buffer_load_dword v240, v248, s[4:7], s97 offen offset:3072 sc1\n"
"ds_read_b64_tr_b16 v[78:79], v16 offset:16640\n"
"v_exp_f32_e32 v151, v151\n"
"v_mfma_f32_16x16x32_bf16 v[104:107], v[72:75], v[64:67], v[104:107]\n"
"v_cvt_pk_bf16_f32 v64, v112, v113\n"
"v_cvt_pk_bf16_f32 v65, v114, v115\n"
"v_cvt_pk_bf16_f32 v66, v148, v149\n"
"v_cvt_pk_bf16_f32 v67, v150, v151\n"
"s_waitcnt lgkmcnt(4)\n"
"s_cmp_eq_i32 s78, 7\n"
"s_cbranch_scc1 .Lmk_x24s\n"
".Lmk_x24r:\n"
"v_mfma_f32_16x16x32_bf16 v[72:75], v[116:119], v[28:31], v[24:27]\n"
"ds_read_b128 v[84:87], v15 offset:10240\n"
"v_exp_f32_e32 v100, v100\n"
"v_exp_f32_e32 v101, v101\n"
"v_exp_f32_e32 v102, v102\n"
"v_mfma_f32_16x16x32_bf16 v[112:115], v[116:119], v[32:35], v[36:39]\n"
"buffer_load_dwordx4 a[92:95], v1, s[4:7], s98 offen offset:3072 sc1\n"
"v_exp_f32_e32 v103, v103\n"
"v_mfma_f32_16x16x32_bf16 v[116:119], v[108:111], v[28:31], v[24:27]\n"
"ds_read_b128 v[144:147], v252 offset:10496\n"
"v_exp_f32_e32 v124, v124\n"
"v_exp_f32_e32 v125, v125\n"
"v_mfma_f32_16x16x32_bf16 v[148:151], v[108:111], v[32:35], v[36:39]\n"
"v_exp_f32_e32 v126, v126\n"
"ds_read_b64_tr_b16 v[108:109], v16 offset:14336\n"
"v_exp_f32_e32 v127, v127\n"
"v_cvt_pk_bf16_f32 v152, v100, v101\n"
"s_waitcnt lgkmcnt(3)\n"
"v_mfma_f32_16x16x32_bf16 v[44:47], v[88:91], v[132:135], v[44:47]\n"
"s_add_u32 s98, s69, 0x606000\n"
"buffer_load_dwordx4 a[96:99], v0, s[4:7], s98 offen sc1\n"
"v_cvt_pk_bf16_f32 v153, v102, v103\n"
"v_cvt_pk_bf16_f32 v154, v124, v125\n"
"v_mfma_f32_16x16x32_bf16 v[40:43], v[80:83], v[132:135], v[40:43]\n"
"v_cvt_pk_bf16_f32 v155, v126, v127\n"
"ds_read_b64_tr_b16 v[110:111], v253 offset:14592\n"
"v_exp_f32_e32 v120, v120\n"
"v_exp_f32_e32 v121, v121\n"
"v_mfma_f32_16x16x32_bf16 v[136:139], v[76:79], v[132:135], v[136:139]\n"
"v_exp_f32_e32 v122, v122\n"
"v_exp_f32_e32 v123, v123\n"
"ds_read_b64_tr_b16 v[100:101], v253 offset:14336\n"
"v_mfma_f32_16x16x32_bf16 v[68:71], v[88:91], v[64:67], v[68:71]\n"
"buffer_load_dwordx4 a[100:103], v0, s[4:7], s98 offen offset:1024 sc1\n"
"v_exp_f32_e32 v128, v128\n"
"v_exp_f32_e32 v129, v129\n"
"v_mfma_f32_16x16x32_bf16 v[140:143], v[80:83], v[64:67], v[140:143]\n"
"v_exp_f32_e32 v130, v130\n"
"ds_read_b64_tr_b16 v[102:103], v16 offset:14592\n"
"v_exp_f32_e32 v131, v131\n"
"v_mfma_f32_16x16x32_bf16 v[104:107], v[76:79], v[64:67], v[104:107]\n"
"v_cvt_pk_bf16_f32 v64, v120, v121\n"
"v_cvt_pk_bf16_f32 v65, v122, v123\n"
"v_cvt_pk_bf16_f32 v66, v128, v129\n"
"v_cvt_pk_bf16_f32 v67, v130, v131\n"
"s_waitcnt lgkmcnt(4)\n"
"s_cmp_eq_i32 s78, 6\n"
"s_cbranch_scc1 .Lmk_x25s\n"
".Lmk_x25r:\n"
"v_mfma_f32_16x16x32_bf16 v[76:79], v[84:87], v[28:31], v[24:27]\n"
"ds_read_b128 v[80:83], v15 offset:8192\n"
"v_exp_f32_e32 v72, v72\n"
"v_exp_f32_e32 v73, v73\n"
"v_exp_f32_e32 v74, v74\n"
"v_mfma_f32_16x16x32_bf16 v[120:123], v[84:87], v[32:35], v[36:39]\n"
"buffer_load_dwordx4 a[104:107], v0, s[4:7], s98 offen offset:2048 sc1\n"
"v_exp_f32_e32 v75, v75\n"
"v_mfma_f32_16x16x32_bf16 v[84:87], v[144:147], v[28:31], v[24:27]\n"
"ds_read_b128 v[124:127], v252 offset:8448\n"
"v_exp_f32_e32 v116, v116\n"
"v_exp_f32_e32 v117, v117\n"
"v_mfma_f32_16x16x32_bf16 v[128:131], v[144:147], v[32:35], v[36:39]\n"
"v_exp_f32_e32 v118, v118\n"
"ds_read_b64_tr_b16 v[132:133], v16 offset:12288\n"
"v_exp_f32_e32 v119, v119\n"
"s_waitcnt lgkmcnt(3)\n"
"v_mfma_f32_16x16x32_bf16 v[44:47], v[88:91], v[152:155], v[44:47]\n"
"v_cvt_pk_bf16_f32 v144, v72, v73\n"
"buffer_load_dwordx4 a[108:111], v0, s[4:7], s98 offen offset:3072 sc1\n"
"v_cvt_pk_bf16_f32 v145, v74, v75\n"
"v_cvt_pk_bf16_f32 v146, v116, v117\n"
"v_mfma_f32_16x16x32_bf16 v[40:43], v[108:111], v[152:155], v[40:43]\n"
"v_cvt_pk_bf16_f32 v147, v118, v119\n"
"ds_read_b64_tr_b16 v[134:135], v253 offset:12544\n"
"v_exp_f32_e32 v112, v112\n"
"v_exp_f32_e32 v113, v113\n"
"v_mfma_f32_16x16x32_bf16 v[136:139], v[100:103], v[152:155], v[136:139]\n"
"v_exp_f32_e32 v114, v114\n"
"v_exp_f32_e32 v115, v115\n"
"ds_read_b64_tr_b16 v[72:73], v253 offset:12288\n"
"v_mfma_f32_16x16x32_bf16 v[68:71], v[88:91], v[64:67], v[68:71]\n"
"buffer_load_dwordx4 a[112:115], v1, s[4:7], s98 offen sc1\n"
"v_exp_f32_e32 v148, v148\n"
"v_exp_f32_e32 v149, v149\n"
"v_mfma_f32_16x16x32_bf16 v[140:143], v[108:111], v[64:67], v[140:143]\n"
"v_exp_f32_e32 v150, v150\n"
"ds_read_b64_tr_b16 v[74:75], v16 offset:12544\n"
"v_exp_f32_e32 v151, v151\n"
"v_mfma_f32_16x16x32_bf16 v[104:107], v[100:103], v[64:67], v[104:107]\n"
"v_cvt_pk_bf16_f32 v64, v112, v113\n"
"v_cvt_pk_bf16_f32 v65, v114, v115\n"
"v_cvt_pk_bf16_f32 v66, v148, v149\n"
"v_cvt_pk_bf16_f32 v67, v150, v151\n"
"s_waitcnt lgkmcnt(4)\n"
"s_cmp_eq_i32 s78, 5\n"
"s_cbranch_scc1 .Lmk_x26s\n"
".Lmk_x26r:\n"
"v_mfma_f32_16x16x32_bf16 v[100:103], v[80:83], v[28:31], v[24:27]\n"
"ds_read_b64_tr_b16 v[108:109], v16 offset:10240\n"
"v_exp_f32_e32 v76, v76\n"
"v_exp_f32_e32 v77, v77\n"
"ds_read_b64_tr_b16 v[110:111], v253 offset:10496\n"
"v_mfma_f32_16x16x32_bf16 v[112:115], v[80:83], v[32:35], v[36:39]\n"
"s_add_u32 s97, s71, 0xfffff000\n"
"buffer_load_dwordx2 v[238:239], off, s[4:7], s97 sc1\n"
"v_exp_f32_e32 v78, v78\n"
"v_exp_f32_e32 v79, v79\n"
"v_mfma_f32_16x16x32_bf16 v[80:83], v[124:127], v[28:31], v[24:27]\n"
"ds_read_b64_tr_b16 v[116:117], v253 offset:10240\n"
"v_exp_f32_e32 v84, v84\n"
"v_exp_f32_e32 v85, v85\n"
"v_mfma_f32_16x16x32_bf16 v[148:151], v[124:127], v[32:35], v[36:39]\n"
"ds_read_b64_tr_b16 v[118:119], v16 offset:10496\n"
"v_exp_f32_e32 v86, v86\n"
"buffer_load_dwordx4 a[116:119], v1, s[4:7], s98 offen offset:1024 sc1\n"
"v_exp_f32_e32 v87, v87\n"
"s_waitcnt lgkmcnt(4)\n"
"v_mfma_f32_16x16x32_bf16 v[44:47], v[88:91], v[144:147], v[44:47]\n"
"ds_read_b64_tr_b16 v[36:37], v16 offset:24576\n"
"v_cvt_pk_bf16_f32 v124, v76, v77\n"
"v_cvt_pk_bf16_f32 v125, v78, v79\n"
"v_cvt_pk_bf16_f32 v126, v84, v85\n"
"ds_read_b64_tr_b16 v[38:39], v253 offset:24832\n"
"v_mfma_f32_16x16x32_bf16 v[40:43], v[132:135], v[144:147], v[40:43]\n"
"v_cvt_pk_bf16_f32 v127, v86, v87\n"
"v_exp_f32_e32 v120, v120\n"
"ds_read_b64_tr_b16 v[76:77], v253 offset:24576\n"
"buffer_load_dwordx4 a[120:123], v1, s[4:7], s98 offen offset:2048 sc1\n"
"v_mfma_f32_16x16x32_bf16 v[136:139], v[72:75], v[144:147], v[136:139]\n"
"v_exp_f32_e32 v121, v121\n"
"v_exp_f32_e32 v122, v122\n"
"ds_read_b64_tr_b16 v[78:79], v16 offset:24832\n"
"v_exp_f32_e32 v123, v123\n"
"v_mfma_f32_16x16x32_bf16 v[68:71], v[88:91], v[64:67], v[68:71]\n"
"v_exp_f32_e32 v128, v128\n"
"v_add_u32_e32 v248, 0x26400, v0\n"
"v_exp_f32_e32 v129, v129\n"
"v_mfma_f32_16x16x32_bf16 v[140:143], v[132:135], v[64:67], v[140:143]\n"
"buffer_load_dwordx4 a[124:127], v1, s[4:7], s98 offen offset:3072 sc1\n"
"v_exp_f32_e32 v130, v130\n"
"ds_read_b128 v[84:87], v248\n"
"v_exp_f32_e32 v131, v131\n"
"v_mfma_f32_16x16x32_bf16 v[104:107], v[72:75], v[64:67], v[104:107]\n"
"v_cvt_pk_bf16_f32 v64, v120, v121\n"
"ds_read_b128 v[72:75], v248 offset:1024\n"
"v_cvt_pk_bf16_f32 v65, v122, v123\n"
"v_cvt_pk_bf16_f32 v66, v128, v129\n"
"v_cvt_pk_bf16_f32 v67, v130, v131\n"
"s_waitcnt lgkmcnt(6)\n"
"v_mfma_f32_16x16x32_bf16 v[44:47], v[88:91], v[124:127], v[44:47]\n"
"ds_read_b64_tr_b16 v[120:121], v16 offset:8192\n"
"v_cndmask_b32_e64 v100, v48, v100, s[20:21]\n"
"v_cndmask_b32_e64 v101, v49, v101, s[22:23]\n"
"v_cndmask_b32_e64 v102, v50, v102, s[24:25]\n"
"s_add_u32 s98, s69, 0x608000\n"
"buffer_load_dwordx4 v[24:27], v0, s[4:7], s98 offen sc1\n"
"v_cndmask_b32_e64 v103, v51, v103, s[26:27]\n"
"ds_read_b64_tr_b16 v[122:123], v253 offset:8448\n"
"v_cndmask_b32_e64 v80, v56, v80, s[28:29]\n"
"v_cndmask_b32_e64 v81, v57, v81, s[30:31]\n"
"v_cndmask_b32_e64 v82, v58, v82, s[32:33]\n"
"ds_read_b64_tr_b16 v[48:49], v253 offset:8192\n"
"v_mfma_f32_16x16x32_bf16 v[40:43], v[108:111], v[124:127], v[40:43]\n"
"v_cndmask_b32_e64 v83, v59, v83, s[34:35]\n"
"v_exp_f32_e32 v100, v100\n"
"v_exp_f32_e32 v101, v101\n"
"ds_read_b64_tr_b16 v[50:51], v16 offset:8448\n"
"v_exp_f32_e32 v102, v102\n"
"s_waitcnt vmcnt(11)\n"
"v_exp_f32_e32 v103, v103\n"
"v_exp_f32_e32 v80, v80\n"
"v_mfma_f32_16x16x32_bf16 v[136:139], v[116:119], v[124:127], v[136:139]\n"
"v_add_f32_e32 v247, v247, v246\n"
"v_exp_f32_e32 v81, v81\n"
"v_exp_f32_e32 v82, v82\n"
"v_add_f32_e32 v247, v247, v245\n"
"v_exp_f32_e32 v83, v83\n"
"v_cvt_pk_bf16_f32 v56, v100, v101\n"
"v_add_f32_e32 v247, v247, v244\n"
"v_cvt_pk_bf16_f32 v57, v102, v103\n"
"v_cvt_pk_bf16_f32 v58, v80, v81\n"
"v_mfma_f32_16x16x32_bf16 v[68:71], v[88:91], v[64:67], v[68:71]\n"
"v_cvt_pk_bf16_f32 v59, v82, v83\n"
"v_add_f32_e32 v247, v247, v243\n"
"v_cndmask_b32_e64 v112, v52, v112, s[36:37]\n"
"v_cndmask_b32_e64 v113, v53, v113, s[38:39]\n"
"v_cndmask_b32_e64 v114, v54, v114, s[40:41]\n"
"v_cndmask_b32_e64 v115, v55, v115, s[42:43]\n"
"v_add_f32_e32 v247, v247, v242\n"
"v_cndmask_b32_e64 v148, v60, v148, s[44:45]\n"
"v_cndmask_b32_e64 v149, v61, v149, s[46:47]\n"
"v_cndmask_b32_e64 v150, v62, v150, s[48:49]\n"
"v_add_f32_e32 v247, v247, v241\n"
"v_cndmask_b32_e64 v151, v63, v151, s[50:51]\n"
"v_exp_f32_e32 v112, v112\n"
"v_mfma_f32_16x16x32_bf16 v[140:143], v[108:111], v[64:67], v[140:143]\n"
"v_exp_f32_e32 v113, v113\n"
"v_add_f32_e32 v247, v247, v240\n"
"v_exp_f32_e32 v114, v114\n"
"v_mov_b32_e32 v246, v247\n"
"v_exp_f32_e32 v115, v115\n"
"v_exp_f32_e32 v148, v148\n"
"v_permlane32_swap_b32_e32 v247, v246\n"
"v_exp_f32_e32 v149, v149\n"
"v_mfma_f32_16x16x32_bf16 v[104:107], v[116:119], v[64:67], v[104:107]\n"
"v_exp_f32_e32 v150, v150\n"
"v_add_f32_e32 v247, v247, v246\n"
"v_exp_f32_e32 v151, v151\n"
"v_cvt_pk_bf16_f32 v52, v112, v113\n"
"ds_write_b32 v21, v247 offset:512\n"
"v_cvt_pk_bf16_f32 v53, v114, v115\n"
"v_cvt_pk_bf16_f32 v54, v148, v149\n"
"v_cvt_pk_bf16_f32 v55, v150, v151\n"
"s_waitcnt lgkmcnt(1)\n"
"v_mfma_f32_16x16x32_bf16 v[44:47], v[88:91], v[56:59], v[44:47]\n"
"v_and_b32_e32 v84, v56, v84\n"
"v_and_b32_e32 v85, v57, v85\n"
"v_and_b32_e32 v86, v58, v86\n"
"v_and_b32_e32 v87, v59, v87\n"
"v_mfma_f32_16x16x32_bf16 v[68:71], v[88:91], v[52:55], v[68:71]\n"
"v_xor_b32_e32 v60, v56, v84\n"
"v_xor_b32_e32 v61, v57, v85\n"
"v_xor_b32_e32 v62, v58, v86\n"
"v_xor_b32_e32 v63, v59, v87\n"
"v_mfma_f32_16x16x32_bf16 v[40:43], v[120:123], v[84:87], v[40:43]\n"
"v_and_b32_e32 v72, v52, v72\n"
"v_and_b32_e32 v73, v53, v73\n"
"v_mfma_f32_16x16x32_bf16 v[136:139], v[48:51], v[84:87], v[136:139]\n"
"v_and_b32_e32 v74, v54, v74\n"
"v_and_b32_e32 v75, v55, v75\n"
"v_mfma_f32_16x16x32_bf16 v[40:43], v[36:39], v[60:63], v[40:43]\n"
"v_xor_b32_e32 v56, v52, v72\n"
"v_xor_b32_e32 v57, v53, v73\n"
"v_mfma_f32_16x16x32_bf16 v[136:139], v[76:79], v[60:63], v[136:139]\n"
"v_xor_b32_e32 v58, v54, v74\n"
"v_xor_b32_e32 v59, v55, v75\n"
"v_mfma_f32_16x16x32_bf16 v[140:143], v[120:123], v[72:75], v[140:143]\n"
"s_waitcnt vmcnt(4)\n"
"v_cmp_ne_u64_e32 vcc, v[238:239], v[18:19]\n"
"s_nop 0\n"
"s_cbranch_vccnz .Lmk_f19s\n"
".Lmk_f19r:\n"
"v_mfma_f32_16x16x32_bf16 v[104:107], v[48:51], v[72:75], v[104:107]\n"
"s_add_u32 m0, s77, 0x4000\n"
"s_add_u32 s97, s65, 0\n"
"buffer_load_dwordx4 v2, s[4:7], s97 offen sc1 lds\n"
"buffer_load_dwordx4 v2, s[4:7], s97 offen offset:1024 sc1 lds\n"
"v_mfma_f32_16x16x32_bf16 v[140:143], v[36:39], v[56:59], v[140:143]\n"
"s_add_u32 m0, s77, 0xc000\n"
"s_add_u32 s97, s65, 0x4000\n"
"buffer_load_dwordx4 v2, s[4:7], s97 offen sc1 lds\n"
"buffer_load_dwordx4 v2, s[4:7], s97 offen offset:1024 sc1 lds\n"
"v_mfma_f32_16x16x32_bf16 v[104:107], v[76:79], v[56:59], v[104:107]\n"
"v_max_f32_e32 v248, v44, v68\n"
"v_rcp_f32_e32 v44, v44\n"
"v_rcp_f32_e32 v68, v68\n"
"s_nop 0\n"
"v_max3_f32 v248, v248, v44, v68\n"
"v_cmp_nlt_f32_e32 vcc, v248, v251\n"
"s_cbranch_vccnz .Lmk_b21\n"
".Lmk_k27:\n"
"v_mul_f32_e32 v40, v40, v44\n"
"v_mul_f32_e32 v41, v41, v44\n"
"v_mul_f32_e32 v42, v42, v44\n"
"v_mul_f32_e32 v43, v43, v44\n"
"v_mul_f32_e32 v136, v136, v44\n"
"v_mul_f32_e32 v137, v137, v44\n"
"v_mul_f32_e32 v138, v138, v44\n"
"v_mul_f32_e32 v139, v139, v44\n"
"v_cvt_pk_bf16_f32 v28, v40, v41\n"
"v_cvt_pk_bf16_f32 v29, v42, v43\n"
"v_cvt_pk_bf16_f32 v30, v136, v137\n"
"v_cvt_pk_bf16_f32 v31, v138, v139\n"
"s_add_u32 s97, s60, 0x40000\n"
"buffer_store_dwordx4 v[28:31], v0, s[4:7], s97 offen sc1\n"
"v_mul_f32_e32 v140, v140, v68\n"
"v_mul_f32_e32 v141, v141, v68\n"
"v_mul_f32_e32 v142, v142, v68\n"
"v_mul_f32_e32 v143, v143, v68\n"
"v_mul_f32_e32 v104, v104, v68\n"
"v_mul_f32_e32 v105, v105, v68\n"
"v_mul_f32_e32 v106, v106, v68\n"
"v_mul_f32_e32 v107, v107, v68\n"
"v_cvt_pk_bf16_f32 v28, v140, v141\n"
"v_cvt_pk_bf16_f32 v29, v142, v143\n"
"v_cvt_pk_bf16_f32 v30, v104, v105\n"
"v_cvt_pk_bf16_f32 v31, v106, v107\n"
"s_add_u32 s97, s60, 0x48000\n"
"buffer_store_dwordx4 v[28:31], v0, s[4:7], s97 offen sc1\n"
"; @A AT a\n"
"s_mov_b32 s2, 0\n"
"v_mov_b32_e32 v255, 0xff800000\n"
"v_mov_b32_e32 v254, 0xff800000\n"
"v_mov_b32_e32 v250, 0x7f800000\n"
"v_mov_b32_e32 v249, 0x7f800000\n"
".Lmk_a28:\n"
"ds_read_b128 v[28:31], v15 offset:16384\n"
"ds_read_b128 v[32:35], v252 offset:16640\n"
"ds_read_b128 v[36:39], v15 offset:14336\n"
"ds_read_b128 v[40:43], v252 offset:14592\n"
"s_cmp_gt_i32 s78, 7\n"
"s_cselect_b32 s97, 0x80000000, 0\n"
"s_or_b32 s97, s97, 0x7f800000\n"
"s_add_u32 s96, s67, 32\n"
"buffer_load_dwordx2 v[246:247], v12, s[4:7], s96 offen sc1\n"
"s_waitcnt lgkmcnt(2)\n"
"v_mfma_f32_16x16x32_bf16 v[100:103], v[28:31], v[92:95], 0\n"
"v_mfma_f32_16x16x32_bf16 v[104:107], v[28:31], v[96:99], 0\n"
"v_mfma_f32_16x16x32_bf16 v[108:111], v[32:35], v[92:95], 0\n"
"v_mfma_f32_16x16x32_bf16 v[112:115], v[32:35], v[96:99], 0\n"
"s_nop 4\n"
"v_max_f32_e32 v248, v100, v101\n"
"v_max_f32_e32 v245, v104, v105\n"
"v_max3_f32 v248, v248, v102, v103\n"
"v_max3_f32 v245, v245, v106, v107\n"
"v_max3_f32 v248, v248, v108, v109\n"
"v_max3_f32 v245, v245, v112, v113\n"
"v_max3_f32 v248, v248, v110, v111\n"
"v_max3_f32 v245, v245, v114, v115\n"
"v_mov_b32_e32 v244, v248\n"
"v_mov_b32_e32 v243, v245\n"
"s_nop 0\n"
"v_permlane16_swap_b32_e32 v248, v244\n"
"v_permlane16_swap_b32_e32 v245, v243\n"
"v_max_f32_e32 v248, v248, v244\n"
"v_max_f32_e32 v245, v245, v243\n"
"v_mov_b32_e32 v244, v248\n"
"v_mov_b32_e32 v243, v245\n"
"s_nop 0\n"
"v_permlane32_swap_b32_e32 v248, v244\n"
"v_permlane32_swap_b32_e32 v245, v243\n"
"v_max_f32_e32 v248, v248, v244\n"
"v_max_f32_e32 v245, v245, v243\n"
"v_med3_f32 v248, v248, v255, v250\n"
"v_med3_f32 v245, v245, v254, v249\n"
"v_min_f32_e64 v116, s97, -v248\n"
"v_min_f32_e64 v117, s97, -v248\n"
"v_min_f32_e64 v118, s97, -v248\n"
"v_min_f32_e64 v119, s97, -v248\n"
"v_min_f32_e64 v120, s97, -v245\n"
"v_min_f32_e64 v121, s97, -v245\n"
"v_min_f32_e64 v122, s97, -v245\n"
"v_min_f32_e64 v123, s97, -v245\n"
"s_waitcnt lgkmcnt(0)\n"
"v_mfma_f32_16x16x32_bf16 v[44:47], v[36:39], v[92:95], v[116:119]\n"
"ds_read_b128 v[32:35], v15 offset:12288\n"
"v_sub_f32_e32 v100, v100, v248\n"
"v_sub_f32_e32 v101, v101, v248\n"
"v_sub_f32_e32 v102, v102, v248\n"
"v_sub_f32_e32 v103, v103, v248\n"
"v_mfma_f32_16x16x32_bf16 v[48:51], v[36:39], v[96:99], v[120:123]\n"
"v_sub_f32_e32 v108, v108, v248\n"
"v_sub_f32_e32 v109, v109, v248\n"
"v_sub_f32_e32 v110, v110, v248\n"
"v_sub_f32_e32 v111, v111, v248\n"
"v_mfma_f32_16x16x32_bf16 v[36:39], v[40:43], v[92:95], v[116:119]\n"
"ds_read_b128 v[52:55], v252 offset:12544\n"
"v_sub_f32_e32 v104, v104, v245\n"
"v_sub_f32_e32 v105, v105, v245\n"
"v_sub_f32_e32 v106, v106, v245\n"
"v_sub_f32_e32 v107, v107, v245\n"
"v_mfma_f32_16x16x32_bf16 v[56:59], v[40:43], v[96:99], v[120:123]\n"
"v_sub_f32_e32 v112, v112, v245\n"
"v_sub_f32_e32 v113, v113, v245\n"
"v_sub_f32_e32 v114, v114, v245\n"
"v_sub_f32_e32 v115, v115, v245\n"
"s_waitcnt lgkmcnt(0)\n"
"s_cmp_eq_i32 s78, 7\n"
"s_cbranch_scc1 .Lmk_x30s\n"
".Lmk_x30r:\n"
"v_mfma_f32_16x16x32_bf16 v[60:63], v[32:35], v[92:95], v[116:119]\n"
"ds_read_b128 v[40:43], v15 offset:10240\n"
"v_exp_f32_e32 v44, v44\n"
"v_exp_f32_e32 v45, v45\n"
"v_exp_f32_e32 v46, v46\n"
"buffer_load_dwordx4 v[28:31], v0, s[4:7], s98 offen offset:1024 sc1\n"
"v_exp_f32_e32 v47, v47\n"
"ds_read_b128 v[64:67], v252 offset:10496\n"
"v_exp_f32_e32 v36, v36\n"
"v_mfma_f32_16x16x32_bf16 v[68:71], v[32:35], v[96:99], v[120:123]\n"
"v_exp_f32_e32 v37, v37\n"
"v_exp_f32_e32 v38, v38\n"
"ds_read_b64_tr_b16 v[72:73], v16 offset:14336\n"
"v_exp_f32_e32 v39, v39\n"
"v_cvt_pk_bf16_f32 v76, v44, v45\n"
"buffer_load_dwordx4 v[32:35], v0, s[4:7], s98 offen offset:2048 sc1\n"
"v_cvt_pk_bf16_f32 v77, v46, v47\n"
"v_cvt_pk_bf16_f32 v78, v36, v37\n"
"v_mfma_f32_16x16x32_bf16 v[80:83], v[52:55], v[92:95], v[116:119]\n"
"v_cvt_pk_bf16_f32 v79, v38, v39\n"
"ds_read_b64_tr_b16 v[74:75], v253 offset:14592\n"
"v_exp_f32_e32 v48, v48\n"
"v_exp_f32_e32 v49, v49\n"
"v_exp_f32_e32 v50, v50\n"
"v_exp_f32_e32 v51, v51\n"
"ds_read_b64_tr_b16 v[84:85], v253 offset:14336\n"
"buffer_load_dwordx4 v[36:39], v0, s[4:7], s98 offen offset:3072 sc1\n"
"v_mfma_f32_16x16x32_bf16 v[124:127], v[52:55], v[96:99], v[120:123]\n"
"v_exp_f32_e32 v56, v56\n"
"v_exp_f32_e32 v57, v57\n"
"v_exp_f32_e32 v58, v58\n"
"ds_read_b64_tr_b16 v[86:87], v16 offset:14592\n"
"v_exp_f32_e32 v59, v59\n"
"v_cvt_pk_bf16_f32 v52, v48, v49\n"
"v_cvt_pk_bf16_f32 v53, v50, v51\n"
"v_cvt_pk_bf16_f32 v54, v56, v57\n"
"v_cvt_pk_bf16_f32 v55, v58, v59\n"
"s_waitcnt lgkmcnt(4)\n"
"s_cmp_eq_i32 s78, 6\n"
"s_cbranch_scc1 .Lmk_x31s\n"
".Lmk_x31r:\n"
"v_mfma_f32_16x16x32_bf16 v[56:59], v[40:43], v[92:95], v[116:119]\n"
"ds_read_b128 v[128:131], v15 offset:8192\n"
"v_exp_f32_e32 v60, v60\n"
"v_exp_f32_e32 v61, v61\n"
"v_exp_f32_e32 v62, v62\n"
"v_mfma_f32_16x16x32_bf16 v[132:135], v[40:43], v[96:99], v[120:123]\n"
"buffer_load_dwordx4 v[40:43], v1, s[4:7], s98 offen sc1\n"
"v_exp_f32_e32 v63, v63\n"
"v_mfma_f32_16x16x32_bf16 v[136:139], v[64:67], v[92:95], v[116:119]\n"
"ds_read_b128 v[140:143], v252 offset:8448\n"
"v_exp_f32_e32 v80, v80\n"
"v_exp_f32_e32 v81, v81\n"
"v_mfma_f32_16x16x32_bf16 v[144:147], v[64:67], v[96:99], v[120:123]\n"
"v_exp_f32_e32 v82, v82\n"
"ds_read_b64_tr_b16 v[64:65], v16 offset:12288\n"
"v_exp_f32_e32 v83, v83\n"
"s_waitcnt lgkmcnt(3)\n"
"v_mfma_f32_16x16x32_bf16 v[148:151], v[88:91], v[76:79], 0\n"
"v_cvt_pk_bf16_f32 v152, v60, v61\n"
"buffer_load_dwordx4 v[44:47], v1, s[4:7], s98 offen offset:1024 sc1\n"
"v_cvt_pk_bf16_f32 v153, v62, v63\n"
"v_cvt_pk_bf16_f32 v154, v80, v81\n"
"v_mfma_f32_16x16x32_bf16 v[156:159], v[72:75], v[76:79], 0\n"
"v_cvt_pk_bf16_f32 v155, v82, v83\n"
"ds_read_b64_tr_b16 v[66:67], v253 offset:12544\n"
"v_exp_f32_e32 v68, v68\n"
"v_exp_f32_e32 v69, v69\n"
"v_mfma_f32_16x16x32_bf16 v[160:163], v[84:87], v[76:79], 0\n"
"v_exp_f32_e32 v70, v70\n"
"v_exp_f32_e32 v71, v71\n"
"ds_read_b64_tr_b16 v[76:77], v253 offset:12288\n"
"v_mfma_f32_16x16x32_bf16 v[164:167], v[88:91], v[52:55], 0\n"
"buffer_load_dwordx4 v[48:51], v1, s[4:7], s98 offen offset:2048 sc1\n"
"v_exp_f32_e32 v124, v124\n"
"v_exp_f32_e32 v125, v125\n"
"v_mfma_f32_16x16x32_bf16 v[168:171], v[72:75], v[52:55], 0\n"
"v_exp_f32_e32 v126, v126\n"
"ds_read_b64_tr_b16 v[78:79], v16 offset:12544\n"
"v_exp_f32_e32 v127, v127\n"
"v_mfma_f32_16x16x32_bf16 v[172:175], v[84:87], v[52:55], 0\n"
"v_cvt_pk_bf16_f32 v72, v68, v69\n"
"v_cvt_pk_bf16_f32 v73, v70, v71\n"
"v_cvt_pk_bf16_f32 v74, v124, v125\n"
"v_cvt_pk_bf16_f32 v75, v126, v127\n"
"s_waitcnt lgkmcnt(4)\n"
"s_cmp_eq_i32 s78, 5\n"
"s_cbranch_scc1 .Lmk_x32s\n"
".Lmk_x32r:\n"
"v_mfma_f32_16x16x32_bf16 v[80:83], v[128:131], v[92:95], v[116:119]\n"
"ds_read_b64_tr_b16 v[68:69], v16 offset:10240\n"
"v_exp_f32_e32 v56, v56\n"
"v_exp_f32_e32 v57, v57\n"
"v_exp_f32_e32 v58, v58\n"
"v_mfma_f32_16x16x32_bf16 v[84:87], v[128:131], v[96:99], v[120:123]\n"
"buffer_load_dwordx4 v[52:55], v1, s[4:7], s98 offen offset:3072 sc1\n"
"v_exp_f32_e32 v59, v59\n"
"v_mfma_f32_16x16x32_bf16 v[124:127], v[140:143], v[92:95], v[116:119]\n"
"v_exp_f32_e32 v136, v136\n"
"ds_read_b64_tr_b16 v[70:71], v253 offset:10496\n"
"v_exp_f32_e32 v137, v137\n"
"v_mfma_f32_16x16x32_bf16 v[128:131], v[140:143], v[96:99], v[120:123]\n"
"v_exp_f32_e32 v138, v138\n"
"v_exp_f32_e32 v139, v139\n"
"v_cvt_pk_bf16_f32 v140, v56, v57\n"
"s_waitcnt lgkmcnt(2)\n"
"v_mfma_f32_16x16x32_bf16 v[148:151], v[88:91], v[152:155], v[148:151]\n"
"v_cvt_pk_bf16_f32 v141, v58, v59\n"
"s_add_u32 s97, s69, 0x60a000\n"
"buffer_load_dwordx4 v[56:59], v0, s[4:7], s97 offen sc1\n"
"v_cvt_pk_bf16_f32 v142, v136, v137\n"
"v_mfma_f32_16x16x32_bf16 v[156:159], v[64:67], v[152:155], v[156:159]\n"
"v_cvt_pk_bf16_f32 v143, v138, v139\n"
"ds_read_b64_tr_b16 v[136:137], v253 offset:10240\n"
"v_exp_f32_e32 v132, v132\n"
"v_exp_f32_e32 v133, v133\n"
"v_mfma_f32_16x16x32_bf16 v[160:163], v[76:79], v[152:155], v[160:163]\n"
"v_exp_f32_e32 v134, v134\n"
"v_exp_f32_e32 v135, v135\n"
"v_mfma_f32_16x16x32_bf16 v[164:167], v[88:91], v[72:75], v[164:167]\n"
"v_exp_f32_e32 v144, v144\n"
"buffer_load_dwordx4 v[60:63], v0, s[4:7], s97 offen offset:1024 sc1\n"
"ds_read_b64_tr_b16 v[138:139], v16 offset:10496\n"
"v_mfma_f32_16x16x32_bf16 v[168:171], v[64:67], v[72:75], v[168:171]\n"
"v_exp_f32_e32 v145, v145\n"
"v_exp_f32_e32 v146, v146\n"
"v_exp_f32_e32 v147, v147\n"
"v_mfma_f32_16x16x32_bf16 v[172:175], v[76:79], v[72:75], v[172:175]\n"
"v_cvt_pk_bf16_f32 v72, v132, v133\n"
"v_cvt_pk_bf16_f32 v73, v134, v135\n"
"v_cvt_pk_bf16_f32 v74, v144, v145\n"
"v_cvt_pk_bf16_f32 v75, v146, v147\n"
"s_waitcnt vmcnt(12)\n"
"s_cmp_lg_u32 s2, 0\n"
"s_cbranch_scc1 .Lmk_s33\n"
"s_barrier\n"
".Lmk_s33:\n"
"ds_read_b128 v[76:79], v15 offset:6144\n"
"ds_read_b128 v[132:135], v252 offset:6400\n"
"s_waitcnt lgkmcnt(2)\n"
"v_mfma_f32_16x16x32_bf16 v[148:151], v[88:91], v[140:143], v[148:151]\n"
"ds_read_b128 v[144:147], v15 offset:4096\n"
"v_exp_f32_e32 v80, v80\n"
"v_exp_f32_e32 v81, v81\n"
"v_exp_f32_e32 v82, v82\n"
"v_mfma_f32_16x16x32_bf16 v[156:159], v[68:71], v[140:143], v[156:159]\n"
"buffer_load_dwordx4 v[64:67], v0, s[4:7], s97 offen offset:2048 sc1\n"
"ds_read_b128 v[152:155], v252 offset:4352\n"
"v_exp_f32_e32 v83, v83\n"
"v_mfma_f32_16x16x32_bf16 v[160:163], v[136:139], v[140:143], v[160:163]\n"
"v_exp_f32_e32 v124, v124\n"
"ds_read_b64_tr_b16 v[140:141], v16 offset:8192\n"
"v_exp_f32_e32 v125, v125\n"
"v_mfma_f32_16x16x32_bf16 v[164:167], v[88:91], v[72:75], v[164:167]\n"
"v_exp_f32_e32 v126, v126\n"
"v_exp_f32_e32 v127, v127\n"
"ds_read_b64_tr_b16 v[142:143], v253 offset:8448\n"
"v_mfma_f32_16x16x32_bf16 v[168:171], v[68:71], v[72:75], v[168:171]\n"
"v_cvt_pk_bf16_f32 v176, v80, v81\n"
"buffer_load_dwordx4 v[68:71], v0, s[4:7], s97 offen offset:3072 sc1\n"
"v_cvt_pk_bf16_f32 v177, v82, v83\n"
"v_cvt_pk_bf16_f32 v178, v124, v125\n"
"v_cvt_pk_bf16_f32 v179, v126, v127\n"
"v_mfma_f32_16x16x32_bf16 v[172:175], v[136:139], v[72:75], v[172:175]\n"
"ds_read_b64_tr_b16 v[124:125], v253 offset:8192\n"
"v_exp_f32_e32 v84, v84\n"
"v_exp_f32_e32 v85, v85\n"
"s_waitcnt lgkmcnt(5)\n"
"v_mfma_f32_16x16x32_bf16 v[136:139], v[76:79], v[92:95], v[116:119]\n"
"v_exp_f32_e32 v86, v86\n"
"ds_read_b64_tr_b16 v[126:127], v16 offset:8448\n"
"v_exp_f32_e32 v87, v87\n"
"v_exp_f32_e32 v128, v128\n"
"v_mfma_f32_16x16x32_bf16 v[180:183], v[76:79], v[96:99], v[120:123]\n"
"buffer_load_dwordx4 v[72:75], v1, s[4:7], s97 offen sc1\n"
"s_waitcnt vmcnt(12)\n"
"v_cmp_ne_u64_e32 vcc, v[246:247], v[18:19]\n"
"v_mfma_f32_16x16x32_bf16 v[184:187], v[132:135], v[92:95], v[116:119]\n"
"v_exp_f32_e32 v129, v129\n"
"v_exp_f32_e32 v130, v130\n"
"v_exp_f32_e32 v131, v131\n"
"v_mfma_f32_16x16x32_bf16 v[188:191], v[132:135], v[96:99], v[120:123]\n"
"s_cbranch_vccnz .Lmk_f4s\n"
".Lmk_f4r:\n"
"v_cvt_pk_bf16_f32 v132, v84, v85\n"
"v_cvt_pk_bf16_f32 v133, v86, v87\n"
"v_cvt_pk_bf16_f32 v134, v128, v129\n"
"v_cvt_pk_bf16_f32 v135, v130, v131\n"
"s_waitcnt lgkmcnt(4)\n"
"v_mfma_f32_16x16x32_bf16 v[128:131], v[144:147], v[92:95], v[116:119]\n"
"ds_read_b128 v[192:195], v15 offset:2048\n"
"v_exp_f32_e32 v136, v136\n"
"v_exp_f32_e32 v137, v137\n"
"v_exp_f32_e32 v138, v138\n"
"v_mfma_f32_16x16x32_bf16 v[196:199], v[144:147], v[96:99], v[120:123]\n"
"buffer_load_dwordx4 v[76:79], v1, s[4:7], s97 offen offset:1024 sc1\n"
"v_exp_f32_e32 v139, v139\n"
"v_mfma_f32_16x16x32_bf16 v[144:147], v[152:155], v[92:95], v[116:119]\n"
"ds_read_b128 v[200:203], v252 offset:2304\n"
"v_exp_f32_e32 v184, v184\n"
"v_exp_f32_e32 v185, v185\n"
"v_mfma_f32_16x16x32_bf16 v[204:207], v[152:155], v[96:99], v[120:123]\n"
"v_exp_f32_e32 v186, v186\n"
"ds_read_b64_tr_b16 v[152:153], v16 offset:6144\n"
"v_exp_f32_e32 v187, v187\n"
"s_waitcnt lgkmcnt(3)\n"
"v_mfma_f32_16x16x32_bf16 v[148:151], v[88:91], v[176:179], v[148:151]\n"
"v_cvt_pk_bf16_f32 v208, v136, v137\n"
"buffer_load_dwordx4 v[80:83], v1, s[4:7], s97 offen offset:2048 sc1\n"
"v_cvt_pk_bf16_f32 v209, v138, v139\n"
"v_cvt_pk_bf16_f32 v210, v184, v185\n"
"v_mfma_f32_16x16x32_bf16 v[156:159], v[140:143], v[176:179], v[156:159]\n"
"v_cvt_pk_bf16_f32 v211, v186, v187\n"
"ds_read_b64_tr_b16 v[154:155], v253 offset:6400\n"
"v_exp_f32_e32 v180, v180\n"
"v_exp_f32_e32 v181, v181\n"
"v_mfma_f32_16x16x32_bf16 v[160:163], v[124:127], v[176:179], v[160:163]\n"
"v_exp_f32_e32 v182, v182\n"
"v_exp_f32_e32 v183, v183\n"
"ds_read_b64_tr_b16 v[136:137], v253 offset:6144\n"
"v_mfma_f32_16x16x32_bf16 v[164:167], v[88:91], v[132:135], v[164:167]\n"
"buffer_load_dwordx4 v[84:87], v1, s[4:7], s97 offen offset:3072 sc1\n"
"v_exp_f32_e32 v188, v188\n"
"v_exp_f32_e32 v189, v189\n"
"v_mfma_f32_16x16x32_bf16 v[168:171], v[140:143], v[132:135], v[168:171]\n"
"v_exp_f32_e32 v190, v190\n"
"ds_read_b64_tr_b16 v[138:139], v16 offset:6400\n"
"v_exp_f32_e32 v191, v191\n"
"v_mfma_f32_16x16x32_bf16 v[172:175], v[124:127], v[132:135], v[172:175]\n"
"v_cvt_pk_bf16_f32 v124, v180, v181\n"
"v_cvt_pk_bf16_f32 v125, v182, v183\n"
"v_cvt_pk_bf16_f32 v126, v188, v189\n"
"v_cvt_pk_bf16_f32 v127, v190, v191\n"
"s_waitcnt lgkmcnt(4)\n"
"v_mfma_f32_16x16x32_bf16 v[132:135], v[192:195], v[92:95], v[116:119]\n"
"ds_read_b128 v[140:143], v15\n"
"v_exp_f32_e32 v128, v128\n"
"v_exp_f32_e32 v129, v129\n"
"v_exp_f32_e32 v130, v130\n"
"v_mfma_f32_16x16x32_bf16 v[176:179], v[192:195], v[96:99], v[120:123]\n"
"s_add_u32 s97, s56, 0x40000\n"
"buffer_load_dwordx4 v[180:183], v0, s[4:7], s97 offen sc1\n"
"v_exp_f32_e32 v131, v131\n"
"v_mfma_f32_16x16x32_bf16 v[184:187], v[200:203], v[92:95], v[116:119]\n"
"ds_read_b128 v[188:191], v252 offset:256\n"
"v_exp_f32_e32 v144, v144\n"
"v_exp_f32_e32 v145, v145\n"
"v_exp_f32_e32 v146, v146\n"
"v_mfma_f32_16x16x32_bf16 v[192:195], v[200:203], v[96:99], v[120:123]\n"
"buffer_load_dwordx4 v[200:203], v0, s[4:7], s97 offen offset:1024 sc1\n"
"ds_read_b64_tr_b16 v[212:213], v16 offset:4096\n"
"v_exp_f32_e32 v147, v147\n"
"s_waitcnt lgkmcnt(3)\n"
"v_mfma_f32_16x16x32_bf16 v[148:151], v[88:91], v[208:211], v[148:151]\n"
"v_cvt_pk_bf16_f32 v216, v128, v129\n"
"v_cvt_pk_bf16_f32 v217, v130, v131\n"
"v_cvt_pk_bf16_f32 v218, v144, v145\n"
"v_cvt_pk_bf16_f32 v219, v146, v147\n"
"buffer_load_dwordx4 v[128:131], v0, s[4:7], s97 offen offset:2048 sc1\n"
"v_mfma_f32_16x16x32_bf16 v[156:159], v[152:155], v[208:211], v[156:159]\n"
"ds_read_b64_tr_b16 v[214:215], v253 offset:4352\n"
"v_exp_f32_e32 v196, v196\n"
"v_exp_f32_e32 v197, v197\n"
"v_mfma_f32_16x16x32_bf16 v[160:163], v[136:139], v[208:211], v[160:163]\n"
"v_exp_f32_e32 v198, v198\n"
"v_exp_f32_e32 v199, v199\n"
"buffer_load_dwordx4 v[144:147], v0, s[4:7], s97 offen offset:3072 sc1\n"
"v_mfma_f32_16x16x32_bf16 v[164:167], v[88:91], v[124:127], v[164:167]\n"
"ds_read_b64_tr_b16 v[208:209], v253 offset:4096\n"
"v_exp_f32_e32 v204, v204\n"
"v_exp_f32_e32 v205, v205\n"
"v_mfma_f32_16x16x32_bf16 v[168:171], v[152:155], v[124:127], v[168:171]\n"
"v_exp_f32_e32 v206, v206\n"
"buffer_load_dwordx4 v[152:155], v1, s[4:7], s97 offen sc1\n"
"ds_read_b64_tr_b16 v[210:211], v16 offset:4352\n"
"v_exp_f32_e32 v207, v207\n"
"v_mfma_f32_16x16x32_bf16 v[172:175], v[136:139], v[124:127], v[172:175]\n"
"v_cvt_pk_bf16_f32 v124, v196, v197\n"
"v_cvt_pk_bf16_f32 v125, v198, v199\n"
"v_cvt_pk_bf16_f32 v126, v204, v205\n"
"v_cvt_pk_bf16_f32 v127, v206, v207\n"
"s_waitcnt lgkmcnt(4)\n"
"v_mfma_f32_16x16x32_bf16 v[136:139], v[140:143], v[92:95], v[116:119]\n"
"ds_read_b64_tr_b16 v[196:197], v16 offset:2048\n"
"v_exp_f32_e32 v132, v132\n"
"v_exp_f32_e32 v133, v133\n"
"ds_read_b64_tr_b16 v[198:199], v253 offset:2304\n"
"buffer_load_dwordx4 v[204:207], v1, s[4:7], s97 offen offset:1024 sc1\n"
"v_mfma_f32_16x16x32_bf16 v[220:223], v[140:143], v[96:99], v[120:123]\n"
"v_exp_f32_e32 v134, v134\n"
"v_exp_f32_e32 v135, v135\n"
"ds_read_b64_tr_b16 v[140:141], v253 offset:2048\n"
"v_mfma_f32_16x16x32_bf16 v[224:227], v[188:191], v[92:95], v[116:119]\n"
"v_exp_f32_e32 v184, v184\n"
"v_exp_f32_e32 v185, v185\n"
"buffer_load_dwordx4 v[116:119], v1, s[4:7], s97 offen offset:2048 sc1\n"
"v_mfma_f32_16x16x32_bf16 v[228:231], v[188:191], v[96:99], v[120:123]\n"
"ds_read_b64_tr_b16 v[142:143], v16 offset:2304\n"
"v_exp_f32_e32 v186, v186\n"
"v_exp_f32_e32 v187, v187\n"
"ds_read_b64_tr_b16 v[120:121], v16 offset:16384\n"
"s_waitcnt lgkmcnt(5)\n"
"v_mfma_f32_16x16x32_bf16 v[148:151], v[88:91], v[216:219], v[148:151]\n"
"v_cvt_pk_bf16_f32 v188, v132, v133\n"
"v_cvt_pk_bf16_f32 v189, v134, v135\n"
"v_cvt_pk_bf16_f32 v190, v184, v185\n"
"buffer_load_dwordx4 v[132:135], v1, s[4:7], s97 offen offset:3072 sc1\n"
"ds_read_b64_tr_b16 v[122:123], v253 offset:16640\n"
"v_mfma_f32_16x16x32_bf16 v[156:159], v[212:215], v[216:219], v[156:159]\n"
"v_cvt_pk_bf16_f32 v191, v186, v187\n"
"v_exp_f32_e32 v176, v176\n"
"ds_read_b64_tr_b16 v[184:185], v253 offset:16384\n"
"v_exp_f32_e32 v177, v177\n"
"v_exp_f32_e32 v178, v178\n"
"v_mfma_f32_16x16x32_bf16 v[160:163], v[208:211], v[216:219], v[160:163]\n"
"s_add_u32 s97, s57, 0x40000\n"
"buffer_load_dwordx4 v[216:219], v0, s[4:7], s97 offen sc1\n"
"ds_read_b64_tr_b16 v[186:187], v16 offset:16640\n"
"v_exp_f32_e32 v179, v179\n"
"v_mfma_f32_16x16x32_bf16 v[164:167], v[88:91], v[124:127], v[164:167]\n"
"v_exp_f32_e32 v192, v192\n"
"v_add_u32_e32 v248, 0x26400, v0\n"
"v_exp_f32_e32 v193, v193\n"
"v_mfma_f32_16x16x32_bf16 v[168:171], v[212:215], v[124:127], v[168:171]\n"
"v_exp_f32_e32 v194, v194\n"
"buffer_load_dwordx4 v[212:215], v0, s[4:7], s97 offen offset:1024 sc1\n"
"ds_read_b128 v[232:235], v248\n"
"v_exp_f32_e32 v195, v195\n"
"v_mfma_f32_16x16x32_bf16 v[172:175], v[208:211], v[124:127], v[172:175]\n"
"v_cvt_pk_bf16_f32 v124, v176, v177\n"
"ds_read_b128 v[208:211], v248 offset:1024\n"
"v_cvt_pk_bf16_f32 v125, v178, v179\n"
"v_cvt_pk_bf16_f32 v126, v192, v193\n"
"v_cvt_pk_bf16_f32 v127, v194, v195\n"
"s_waitcnt lgkmcnt(6)\n"
"v_mfma_f32_16x16x32_bf16 v[148:151], v[88:91], v[188:191], v[148:151]\n"
"ds_read_b64_tr_b16 v[176:177], v16\n"
"v_cndmask_b32_e64 v136, v100, v136, s[20:21]\n"
"v_cndmask_b32_e64 v137, v101, v137, s[22:23]\n"
"v_cndmask_b32_e64 v138, v102, v138, s[24:25]\n"
"buffer_load_dwordx4 v[192:195], v0, s[4:7], s97 offen offset:2048 sc1\n"
"v_cndmask_b32_e64 v139, v103, v139, s[26:27]\n"
"v_cndmask_b32_e64 v224, v108, v224, s[28:29]\n"
"v_cndmask_b32_e64 v225, v109, v225, s[30:31]\n"
"v_cndmask_b32_e64 v226, v110, v226, s[32:33]\n"
"v_cndmask_b32_e64 v227, v111, v227, s[34:35]\n"
"v_exp_f32_e32 v136, v136\n"
"v_mfma_f32_16x16x32_bf16 v[156:159], v[196:199], v[188:191], v[156:159]\n"
"v_exp_f32_e32 v137, v137\n"
"v_exp_f32_e32 v138, v138\n"
"ds_read_b64_tr_b16 v[178:179], v253 offset:256\n"
"buffer_load_dwordx4 v[100:103], v0, s[4:7], s97 offen offset:3072 sc1\n"
"v_exp_f32_e32 v139, v139\n"
"v_exp_f32_e32 v224, v224\n"
"v_mfma_f32_16x16x32_bf16 v[160:163], v[140:143], v[188:191], v[160:163]\n"
"v_exp_f32_e32 v225, v225\n"
"v_exp_f32_e32 v226, v226\n"
"v_exp_f32_e32 v227, v227\n"
"v_cvt_pk_bf16_f32 v108, v136, v137\n"
"buffer_load_dwordx4 v[188:191], v1, s[4:7], s97 offen sc1\n"
"v_cvt_pk_bf16_f32 v109, v138, v139\n"
"v_cvt_pk_bf16_f32 v110, v224, v225\n"
"v_mfma_f32_16x16x32_bf16 v[164:167], v[88:91], v[124:127], v[164:167]\n"
"v_cvt_pk_bf16_f32 v111, v226, v227\n"
"ds_read_b64_tr_b16 v[136:137], v253\n"
"v_cndmask_b32_e64 v220, v104, v220, s[36:37]\n"
"v_cndmask_b32_e64 v221, v105, v221, s[38:39]\n"
"v_cndmask_b32_e64 v222, v106, v222, s[40:41]\n"
"v_cndmask_b32_e64 v223, v107, v223, s[42:43]\n"
"v_cndmask_b32_e64 v228, v112, v228, s[44:45]\n"
"v_cndmask_b32_e64 v229, v113, v229, s[46:47]\n"
"v_cndmask_b32_e64 v230, v114, v230, s[48:49]\n"
"buffer_load_dwordx4 v[104:107], v1, s[4:7], s97 offen offset:1024 sc1\n"
"v_cndmask_b32_e64 v231, v115, v231, s[50:51]\n"
"v_mfma_f32_16x16x32_bf16 v[168:171], v[196:199], v[124:127], v[168:171]\n"
"v_exp_f32_e32 v220, v220\n"
"v_exp_f32_e32 v221, v221\n"
"v_exp_f32_e32 v222, v222\n"
"ds_read_b64_tr_b16 v[138:139], v16 offset:256\n"
"v_exp_f32_e32 v223, v223\n"
"v_exp_f32_e32 v228, v228\n"
"buffer_load_dwordx4 v[112:115], v1, s[4:7], s97 offen offset:2048 sc1\n"
"v_mfma_f32_16x16x32_bf16 v[172:175], v[140:143], v[124:127], v[172:175]\n"
"v_exp_f32_e32 v229, v229\n"
"v_exp_f32_e32 v230, v230\n"
"v_exp_f32_e32 v231, v231\n"
"v_cvt_pk_bf16_f32 v124, v220, v221\n"
"v_cvt_pk_bf16_f32 v125, v222, v223\n"
"v_cvt_pk_bf16_f32 v126, v228, v229\n"
"v_cvt_pk_bf16_f32 v127, v230, v231\n"
"s_waitcnt lgkmcnt(0)\n"
"v_mfma_f32_16x16x32_bf16 v[148:151], v[88:91], v[108:111], v[148:151]\n"
"v_and_b32_e32 v232, v108, v232\n"
"v_and_b32_e32 v233, v109, v233\n"
"v_and_b32_e32 v234, v110, v234\n"
"v_and_b32_e32 v235, v111, v235\n"
"v_mfma_f32_16x16x32_bf16 v[164:167], v[88:91], v[124:127], v[164:167]\n"
"v_xor_b32_e32 v140, v108, v232\n"
"v_xor_b32_e32 v141, v109, v233\n"
"v_xor_b32_e32 v142, v110, v234\n"
"v_xor_b32_e32 v143, v111, v235\n"
"v_mfma_f32_16x16x32_bf16 v[156:159], v[176:179], v[232:235], v[156:159]\n"
"v_and_b32_e32 v208, v124, v208\n"
"v_and_b32_e32 v209, v125, v209\n"
"v_mfma_f32_16x16x32_bf16 v[160:163], v[136:139], v[232:235], v[160:163]\n"
"v_and_b32_e32 v210, v126, v210\n"
"v_and_b32_e32 v211, v127, v211\n"
"v_mfma_f32_16x16x32_bf16 v[156:159], v[120:123], v[140:143], v[156:159]\n"
"v_xor_b32_e32 v108, v124, v208\n"
"v_xor_b32_e32 v109, v125, v209\n"
"v_mfma_f32_16x16x32_bf16 v[160:163], v[184:187], v[140:143], v[160:163]\n"
"v_xor_b32_e32 v110, v126, v210\n"
"v_xor_b32_e32 v111, v127, v211\n"
"v_mfma_f32_16x16x32_bf16 v[168:171], v[176:179], v[208:211], v[168:171]\n"
"buffer_load_dwordx4 v[124:127], v1, s[4:7], s97 offen offset:3072 sc1\n"
"s_add_u32 s97, s58, 0x40000\n"
"buffer_load_dwordx4 v[140:143], v0, s[4:7], s97 offen sc1\n"
"v_mfma_f32_16x16x32_bf16 v[172:175], v[136:139], v[208:211], v[172:175]\n"
"buffer_load_dwordx4 v[136:139], v0, s[4:7], s97 offen offset:1024 sc1\n"
"v_mfma_f32_16x16x32_bf16 v[168:171], v[120:123], v[108:111], v[168:171]\n"
"buffer_load_dwordx4 v[120:123], v0, s[4:7], s97 offen offset:2048 sc1\n"
"v_mfma_f32_16x16x32_bf16 v[172:175], v[184:187], v[108:111], v[172:175]\n"
"buffer_load_dwordx4 v[108:111], v0, s[4:7], s97 offen offset:3072 sc1\n"
"v_max_f32_e32 v248, v148, v164\n"
"v_rcp_f32_e32 v148, v148\n"
"v_rcp_f32_e32 v164, v164\n"
"s_nop 0\n"
"v_max3_f32 v248, v248, v148, v164\n"
"v_cmp_nlt_f32_e32 vcc, v248, v251\n"
"s_cbranch_vccnz .Lmk_b29\n"
".Lmk_k34:\n"
"v_mul_f32_e32 v156, v156, v148\n"
"v_mul_f32_e32 v157, v157, v148\n"
"v_mul_f32_e32 v158, v158, v148\n"
"v_mul_f32_e32 v159, v159, v148\n"
"v_mul_f32_e32 v160, v160, v148\n"
"v_mul_f32_e32 v161, v161, v148\n"
"v_mul_f32_e32 v162, v162, v148\n"
"v_mul_f32_e32 v163, v163, v148\n"
"v_cvt_pk_bf16_f32 v88, v156, v157\n"
"v_cvt_pk_bf16_f32 v89, v158, v159\n"
"v_cvt_pk_bf16_f32 v90, v160, v161\n"
"v_cvt_pk_bf16_f32 v91, v162, v163\n"
"buffer_store_dwordx4 v[88:91], v0, s[4:7], s60 offen sc1\n"
"v_mul_f32_e32 v168, v168, v164\n"
"v_mul_f32_e32 v169, v169, v164\n"
"v_mul_f32_e32 v170, v170, v164\n"
"v_mul_f32_e32 v171, v171, v164\n"
"v_mul_f32_e32 v172, v172, v164\n"
"v_mul_f32_e32 v173, v173, v164\n"
"v_mul_f32_e32 v174, v174, v164\n"
"v_mul_f32_e32 v175, v175, v164\n"
"v_cvt_pk_bf16_f32 v88, v168, v169\n"
"v_cvt_pk_bf16_f32 v89, v170, v171\n"
"v_cvt_pk_bf16_f32 v90, v172, v173\n"
"v_cvt_pk_bf16_f32 v91, v174, v175\n"
"s_add_u32 s98, s60, 0x8000\n"
"buffer_store_dwordx4 v[88:91], v0, s[4:7], s98 offen sc1\n"
"s_waitcnt vmcnt(21)\n"
"; @A G2 b.0\n"
"s_nop 0\n"
"v_mfma_f32_16x16x32_bf16 v[88:91], a[128:131], v[180:183], 0\n"
"v_mfma_f32_16x16x32_bf16 v[92:95], a[132:135], v[180:183], 0\n"
"v_mfma_f32_16x16x32_bf16 v[96:99], a[136:139], v[180:183], 0\n"
"buffer_load_dwordx4 v[148:151], v1, s[4:7], s97 offen sc1\n"
"v_mfma_f32_16x16x32_bf16 v[156:159], a[140:143], v[180:183], 0\n"
"s_waitcnt vmcnt(21)\n"
"v_mfma_f32_16x16x32_bf16 v[88:91], a[144:147], v[200:203], v[88:91]\n"
"v_mfma_f32_16x16x32_bf16 v[92:95], a[148:151], v[200:203], v[92:95]\n"
"v_mfma_f32_16x16x32_bf16 v[96:99], a[152:155], v[200:203], v[96:99]\n"
"buffer_load_dwordx4 v[160:163], v1, s[4:7], s97 offen offset:1024 sc1\n"
"v_mfma_f32_16x16x32_bf16 v[156:159], a[156:159], v[200:203], v[156:159]\n"
"s_waitcnt vmcnt(21)\n"
"v_mfma_f32_16x16x32_bf16 v[88:91], a[160:163], v[128:131], v[88:91]\n"
"v_mfma_f32_16x16x32_bf16 v[92:95], a[164:167], v[128:131], v[92:95]\n"
"v_mfma_f32_16x16x32_bf16 v[96:99], a[168:171], v[128:131], v[96:99]\n"
"buffer_load_dwordx4 v[164:167], v1, s[4:7], s97 offen offset:2048 sc1\n"
"v_mfma_f32_16x16x32_bf16 v[156:159], a[172:175], v[128:131], v[156:159]\n"
"s_waitcnt vmcnt(21)\n"
"v_mfma_f32_16x16x32_bf16 v[88:91], a[176:179], v[144:147], v[88:91]\n"
"v_mfma_f32_16x16x32_bf16 v[92:95], a[180:183], v[144:147], v[92:95]\n"
"v_mfma_f32_16x16x32_bf16 v[96:99], a[184:187], v[144:147], v[96:99]\n"
"buffer_load_dwordx4 v[128:131], v1, s[4:7], s97 offen offset:3072 sc1\n"
"v_mfma_f32_16x16x32_bf16 v[156:159], a[188:191], v[144:147], v[156:159]\n"
"s_waitcnt vmcnt(21)\n"
"v_mfma_f32_16x16x32_bf16 v[88:91], a[192:195], v[152:155], v[88:91]\n"
"s_add_u32 s98, s59, 0x40000\n"
"v_mfma_f32_16x16x32_bf16 v[92:95], a[196:199], v[152:155], v[92:95]\n"
"v_mfma_f32_16x16x32_bf16 v[96:99], a[200:203], v[152:155], v[96:99]\n"
"buffer_load_dwordx4 v[144:147], v0, s[4:7], s98 offen sc1\n"
"v_mfma_f32_16x16x32_bf16 v[156:159], a[204:207], v[152:155], v[156:159]\n"
"s_waitcnt vmcnt(21)\n"
"v_mfma_f32_16x16x32_bf16 v[88:91], a[208:211], v[204:207], v[88:91]\n"
"v_mfma_f32_16x16x32_bf16 v[92:95], a[212:215], v[204:207], v[92:95]\n"
"v_mfma_f32_16x16x32_bf16 v[96:99], a[216:219], v[204:207], v[96:99]\n"
"buffer_load_dwordx4 v[152:155], v0, s[4:7], s98 offen offset:1024 sc1\n"
"v_mfma_f32_16x16x32_bf16 v[156:159], a[220:223], v[204:207], v[156:159]\n"
"s_waitcnt vmcnt(21)\n"
"v_mfma_f32_16x16x32_bf16 v[88:91], a[224:227], v[116:119], v[88:91]\n"
"v_mfma_f32_16x16x32_bf16 v[92:95], a[228:231], v[116:119], v[92:95]\n"
"v_mfma_f32_16x16x32_bf16 v[96:99], a[232:235], v[116:119], v[96:99]\n"
"buffer_load_dwordx4 v[168:171], v0, s[4:7], s98 offen offset:2048 sc1\n"
"v_mfma_f32_16x16x32_bf16 v[156:159], a[236:239], v[116:119], v[156:159]\n"
"s_waitcnt vmcnt(21)\n"
"v_mfma_f32_16x16x32_bf16 v[88:91], a[240:243], v[132:135], v[88:91]\n"
"v_mfma_f32_16x16x32_bf16 v[92:95], a[244:247], v[132:135], v[92:95]\n"
"v_mfma_f32_16x16x32_bf16 v[96:99], a[248:251], v[132:135], v[96:99]\n"
"buffer_load_dwordx4 v[116:119], v0, s[4:7], s98 offen offset:3072 sc1\n"
"v_mfma_f32_16x16x32_bf16 v[156:159], a[252:255], v[132:135], v[156:159]\n"
"s_waitcnt vmcnt(20)\n"
"; @A G2 b.1\n"
"v_mfma_f32_16x16x32_bf16 v[132:135], a[128:131], v[216:219], 0\n"
"s_nop 1\n"
"v_cmp_u_f32_e32 vcc, v88, v88\n"
"v_mfma_f32_16x16x32_bf16 v[172:175], a[132:135], v[216:219], 0\n"
"s_cbranch_vccnz .Lmk_p35s\n"
".Lmk_p35r:\n"
"v_mfma_f32_16x16x32_bf16 v[176:179], a[136:139], v[216:219], 0\n"
"buffer_load_dwordx4 v[180:183], v1, s[4:7], s98 offen sc1\n"
"v_mfma_f32_16x16x32_bf16 v[184:187], a[140:143], v[216:219], 0\n"
"v_mfma_f32_16x16x32_bf16 v[132:135], a[144:147], v[212:215], v[132:135]\n"
"ds_write_b128 v3, v[88:91]\n"
"v_mfma_f32_16x16x32_bf16 v[172:175], a[148:151], v[212:215], v[172:175]\n"
"v_mfma_f32_16x16x32_bf16 v[176:179], a[152:155], v[212:215], v[176:179]\n"
"ds_write_b128 v3, v[92:95] offset:1024\n"
"v_mfma_f32_16x16x32_bf16 v[184:187], a[156:159], v[212:215], v[184:187]\n"
"s_waitcnt vmcnt(19)\n"
"v_mfma_f32_16x16x32_bf16 v[132:135], a[160:163], v[192:195], v[132:135]\n"
"ds_write_b128 v3, v[96:99] offset:2048\n"
"v_mfma_f32_16x16x32_bf16 v[172:175], a[164:167], v[192:195], v[172:175]\n"
"buffer_load_dwordx4 v[88:91], v1, s[4:7], s98 offen offset:1024 sc1\n"
"v_mfma_f32_16x16x32_bf16 v[176:179], a[168:171], v[192:195], v[176:179]\n"
"ds_write_b128 v3, v[156:159] offset:3072\n"
"v_mfma_f32_16x16x32_bf16 v[184:187], a[172:175], v[192:195], v[184:187]\n"
"v_mfma_f32_16x16x32_bf16 v[132:135], a[176:179], v[100:103], v[132:135]\n"
"v_mfma_f32_16x16x32_bf16 v[172:175], a[180:183], v[100:103], v[172:175]\n"
"buffer_load_dwordx4 v[92:95], v1, s[4:7], s98 offen offset:2048 sc1\n"
"v_mfma_f32_16x16x32_bf16 v[176:179], a[184:187], v[100:103], v[176:179]\n"
"ds_read_b128 v[96:99], v2 offset:8192\n"
"ds_read_b128 v[156:159], v2 offset:9216\n"
"v_mfma_f32_16x16x32_bf16 v[184:187], a[188:191], v[100:103], v[184:187]\n"
"s_waitcnt vmcnt(19)\n"
"v_mfma_f32_16x16x32_bf16 v[132:135], a[192:195], v[188:191], v[132:135]\n"
"v_mfma_f32_16x16x32_bf16 v[172:175], a[196:199], v[188:191], v[172:175]\n"
"buffer_load_dwordx4 v[100:103], v1, s[4:7], s98 offen offset:3072 sc1\n"
"v_mfma_f32_16x16x32_bf16 v[176:179], a[200:203], v[188:191], v[176:179]\n"
"v_mfma_f32_16x16x32_bf16 v[184:187], a[204:207], v[188:191], v[184:187]\n"
"s_waitcnt lgkmcnt(0)\n"
"s_barrier\n"
"v_mfma_f32_16x16x32_bf16 v[132:135], a[208:211], v[104:107], v[132:135]\n"
"v_lshlrev_b32_e32 v255, 16, v96\n"
"v_mfma_f32_16x16x32_bf16 v[172:175], a[212:215], v[104:107], v[172:175]\n"
"ds_read_b128 v[188:191], v4\n"
"ds_read_b128 v[192:195], v4 offset:1024\n"
"v_mfma_f32_16x16x32_bf16 v[176:179], a[216:219], v[104:107], v[176:179]\n"
"ds_read_b128 v[196:199], v4 offset:2048\n"
"ds_read_b128 v[200:203], v4 offset:3072\n"
"v_mfma_f32_16x16x32_bf16 v[184:187], a[220:223], v[104:107], v[184:187]\n"
"s_waitcnt vmcnt(18)\n"
"v_and_b32_e32 v254, 0xffff0000, v96\n"
"v_mfma_f32_16x16x32_bf16 v[132:135], a[224:227], v[112:115], v[132:135]\n"
"s_mov_b32 s98, .Lmk_b0-.Lmk_g40\n"
"s_getpc_b64 s[84:85]\n"
".Lmk_g40:\n"
"s_add_u32 s84, s84, s98\n"
"s_addc_u32 s85, s85, 0\n"
"v_mfma_f32_16x16x32_bf16 v[172:175], a[228:231], v[112:115], v[172:175]\n"
"s_and_b32 s84, s84, -16\n"
"s_and_b32 s85, s85, 0xffff\n"
"s_sub_u32 s98, .Lmk_end-.Lmk_g40, s98\n"
"s_min_u32 s86, s98, 0x3000\n"
"v_mfma_f32_16x16x32_bf16 v[176:179], a[232:235], v[112:115], v[176:179]\n"
"s_mov_b32 s87, 0x20000\n"
"v_lshlrev_b32_e32 v253, 16, v97\n"
"v_mfma_f32_16x16x32_bf16 v[184:187], a[236:239], v[112:115], v[184:187]\n"
"s_mov_b32 m0, 0x27800\n"
"s_nop 0\n"
"buffer_load_dwordx4 v0, s[84:87], s99 offen lds\n"
"v_mfma_f32_16x16x32_bf16 v[132:135], a[240:243], v[124:127], v[132:135]\n"
"v_and_b32_e32 v252, 0xffff0000, v97\n"
"v_lshlrev_b32_e32 v251, 16, v98\n"
"v_mfma_f32_16x16x32_bf16 v[172:175], a[244:247], v[124:127], v[172:175]\n"
"v_and_b32_e32 v250, 0xffff0000, v98\n"
"v_lshlrev_b32_e32 v249, 16, v99\n"
"v_mfma_f32_16x16x32_bf16 v[176:179], a[248:251], v[124:127], v[176:179]\n"
"v_and_b32_e32 v248, 0xffff0000, v99\n"
"v_lshlrev_b32_e32 v247, 16, v156\n"
"v_mfma_f32_16x16x32_bf16 v[184:187], a[252:255], v[124:127], v[184:187]\n"
"s_waitcnt vmcnt(17)\n"
"v_and_b32_e32 v246, 0xffff0000, v156\n"
"; @A G2 b.2\n"
"v_mfma_f32_16x16x32_bf16 v[96:99], a[128:131], v[140:143], 0\n"
"v_cmp_u_f32_e32 vcc, v132, v132\n"
"v_lshlrev_b32_e32 v245, 16, v157\n"
"v_mfma_f32_16x16x32_bf16 v[104:107], a[132:135], v[140:143], 0\n"
"s_cbranch_vccnz .Lmk_p36s\n"
".Lmk_p36r:\n"
"v_and_b32_e32 v244, 0xffff0000, v157\n"
"v_mfma_f32_16x16x32_bf16 v[112:115], a[136:139], v[140:143], 0\n"
"v_lshlrev_b32_e32 v243, 16, v158\n"
"v_and_b32_e32 v242, 0xffff0000, v158\n"
"v_mfma_f32_16x16x32_bf16 v[124:127], a[140:143], v[140:143], 0\n"
"v_lshlrev_b32_e32 v241, 16, v159\n"
"v_and_b32_e32 v240, 0xffff0000, v159\n"
"v_mfma_f32_16x16x32_bf16 v[96:99], a[144:147], v[136:139], v[96:99]\n"
"ds_write_b128 v3, v[132:135] offset:6144\n"
"v_mfma_f32_16x16x32_bf16 v[104:107], a[148:151], v[136:139], v[104:107]\n"
"s_waitcnt lgkmcnt(1)\n"
"v_add_f32_e32 v188, v188, v255\n"
"v_mfma_f32_16x16x32_bf16 v[112:115], a[152:155], v[136:139], v[112:115]\n"
"ds_write_b128 v3, v[172:175] offset:7168\n"
"v_mfma_f32_16x16x32_bf16 v[124:127], a[156:159], v[136:139], v[124:127]\n"
"s_waitcnt vmcnt(15)\n"
"v_add_f32_e32 v189, v189, v254\n"
"v_mfma_f32_16x16x32_bf16 v[96:99], a[160:163], v[120:123], v[96:99]\n"
"ds_write_b128 v3, v[176:179] offset:8192\n"
"v_mfma_f32_16x16x32_bf16 v[104:107], a[164:167], v[120:123], v[104:107]\n"
"v_add_f32_e32 v190, v190, v253\n"
"v_add_f32_e32 v191, v191, v252\n"
"v_mfma_f32_16x16x32_bf16 v[112:115], a[168:171], v[120:123], v[112:115]\n"
"ds_write_b128 v3, v[184:187] offset:9216\n"
"v_mfma_f32_16x16x32_bf16 v[124:127], a[172:175], v[120:123], v[124:127]\n"
"v_add_f32_e32 v192, v192, v251\n"
"v_add_f32_e32 v193, v193, v250\n"
"v_mfma_f32_16x16x32_bf16 v[96:99], a[176:179], v[108:111], v[96:99]\n"
"s_add_u32 s98, s56, 0\n"
"v_add_f32_e32 v194, v194, v249\n"
"v_mfma_f32_16x16x32_bf16 v[104:107], a[180:183], v[108:111], v[104:107]\n"
"buffer_load_dwordx4 v[120:123], v0, s[4:7], s98 offen sc1\n"
"v_mfma_f32_16x16x32_bf16 v[112:115], a[184:187], v[108:111], v[112:115]\n"
"v_add_f32_e32 v195, v195, v248\n"
"v_add_f32_e32 v196, v196, v247\n"
"v_mfma_f32_16x16x32_bf16 v[124:127], a[188:191], v[108:111], v[124:127]\n"
"s_waitcnt vmcnt(12)\n"
"v_add_f32_e32 v197, v197, v246\n"
"v_mfma_f32_16x16x32_bf16 v[96:99], a[192:195], v[148:151], v[96:99]\n"
"buffer_load_dwordx4 v[108:111], v0, s[4:7], s98 offen offset:1024 sc1\n"
"v_mfma_f32_16x16x32_bf16 v[104:107], a[196:199], v[148:151], v[104:107]\n"
"v_add_f32_e32 v198, v198, v245\n"
"v_add_f32_e32 v199, v199, v244\n"
"v_mfma_f32_16x16x32_bf16 v[112:115], a[200:203], v[148:151], v[112:115]\n"
"v_add_f32_e32 v200, v200, v243\n"
"v_add_f32_e32 v201, v201, v242\n"
"v_mfma_f32_16x16x32_bf16 v[124:127], a[204:207], v[148:151], v[124:127]\n"
"buffer_load_dwordx4 v[132:135], v0, s[4:7], s98 offen offset:2048 sc1\n"
"v_mfma_f32_16x16x32_bf16 v[96:99], a[208:211], v[160:163], v[96:99]\n"
"v_add_f32_e32 v202, v202, v241\n"
"v_add_f32_e32 v203, v203, v240\n"
"v_mfma_f32_16x16x32_bf16 v[104:107], a[212:215], v[160:163], v[104:107]\n"
"v_mfma_f32_16x16x32_bf16 v[112:115], a[216:219], v[160:163], v[112:115]\n"
"buffer_load_dwordx4 v[136:139], v0, s[4:7], s98 offen offset:3072 sc1\n"
"v_mfma_f32_16x16x32_bf16 v[124:127], a[220:223], v[160:163], v[124:127]\n"
"s_waitcnt vmcnt(13)\n"
"v_mfma_f32_16x16x32_bf16 v[96:99], a[224:227], v[164:167], v[96:99]\n"
"s_waitcnt vmcnt(11)\n"
"v_max_u32_e32 v255, v144, v145\n"
"v_mfma_f32_16x16x32_bf16 v[104:107], a[228:231], v[164:167], v[104:107]\n"
"v_max3_u32 v255, v255, v146, v147\n"
"v_max3_u32 v255, v255, v152, v153\n"
"v_mfma_f32_16x16x32_bf16 v[112:115], a[232:235], v[164:167], v[112:115]\n"
"v_max3_u32 v255, v255, v154, v155\n"
"s_waitcnt vmcnt(9)\n"
"v_mfma_f32_16x16x32_bf16 v[124:127], a[236:239], v[164:167], v[124:127]\n"
"v_max3_u32 v255, v255, v168, v169\n"
"v_max3_u32 v255, v255, v170, v171\n"
"v_mfma_f32_16x16x32_bf16 v[96:99], a[240:243], v[128:131], v[96:99]\n"
"v_max3_u32 v255, v255, v116, v117\n"
"v_max3_u32 v255, v255, v118, v119\n"
"v_mfma_f32_16x16x32_bf16 v[104:107], a[244:247], v[128:131], v[104:107]\n"
"v_cmp_eq_u32_e32 vcc, -1, v255\n"
"v_mfma_f32_16x16x32_bf16 v[112:115], a[248:251], v[128:131], v[112:115]\n"
"s_cbranch_vccnz .Lmk_q38s\n"
".Lmk_q38r:\n"
"v_mfma_f32_16x16x32_bf16 v[124:127], a[252:255], v[128:131], v[124:127]\n"
"; @A G2 b.3\n"
"v_mfma_f32_16x16x32_bf16 v[188:191], a[128:131], v[144:147], v[188:191]\n"
"v_cmp_u_f32_e32 vcc, v96, v96\n"
"v_mfma_f32_16x16x32_bf16 v[192:195], a[132:135], v[144:147], v[192:195]\n"
"s_cbranch_vccnz .Lmk_p37s\n"
".Lmk_p37r:\n"
"v_mfma_f32_16x16x32_bf16 v[196:199], a[136:139], v[144:147], v[196:199]\n"
"buffer_load_dwordx4 v[128:131], v1, s[4:7], s98 offen sc1\n"
"v_mfma_f32_16x16x32_bf16 v[200:203], a[140:143], v[144:147], v[200:203]\n"
"v_mfma_f32_16x16x32_bf16 v[188:191], a[144:147], v[152:155], v[188:191]\n"
"ds_write_b128 v3, v[96:99] offset:12288\n"
"v_mfma_f32_16x16x32_bf16 v[192:195], a[148:151], v[152:155], v[192:195]\n"
"buffer_load_dwordx4 v[96:99], v1, s[4:7], s98 offen offset:1024 sc1\n"
"v_mfma_f32_16x16x32_bf16 v[196:199], a[152:155], v[152:155], v[196:199]\n"
"ds_write_b128 v3, v[104:107] offset:13312\n"
"v_mfma_f32_16x16x32_bf16 v[200:203], a[156:159], v[152:155], v[200:203]\n"
"v_mfma_f32_16x16x32_bf16 v[188:191], a[160:163], v[168:171], v[188:191]\n"
"s_waitcnt vmcnt(9)\n"
"v_max_u32_e32 v255, v180, v181\n"
"v_mfma_f32_16x16x32_bf16 v[192:195], a[164:167], v[168:171], v[192:195]\n"
"v_max3_u32 v255, v255, v182, v183\n"
"v_max3_u32 v255, v255, v88, v89\n"
"v_mfma_f32_16x16x32_bf16 v[196:199], a[168:171], v[168:171], v[196:199]\n"
"v_max3_u32 v255, v255, v90, v91\n"
"s_waitcnt vmcnt(7)\n"
"v_mfma_f32_16x16x32_bf16 v[200:203], a[172:175], v[168:171], v[200:203]\n"
"v_max3_u32 v255, v255, v92, v93\n"
"v_max3_u32 v255, v255, v94, v95\n"
"v_mfma_f32_16x16x32_bf16 v[188:191], a[176:179], v[116:119], v[188:191]\n"
"v_max3_u32 v255, v255, v100, v101\n"
"v_max3_u32 v255, v255, v102, v103\n"
"v_mfma_f32_16x16x32_bf16 v[192:195], a[180:183], v[116:119], v[192:195]\n"
"v_cmp_eq_u32_e32 vcc, -1, v255\n"
"v_mfma_f32_16x16x32_bf16 v[196:199], a[184:187], v[116:119], v[196:199]\n"
"s_cbranch_vccnz .Lmk_q39s\n"
".Lmk_q39r:\n"
"v_mfma_f32_16x16x32_bf16 v[200:203], a[188:191], v[116:119], v[200:203]\n"
"v_mfma_f32_16x16x32_bf16 v[188:191], a[192:195], v[180:183], v[188:191]\n"
"ds_write_b128 v3, v[112:115] offset:14336\n"
"v_mfma_f32_16x16x32_bf16 v[192:195], a[196:199], v[180:183], v[192:195]\n"
"buffer_load_dwordx4 v[104:107], v1, s[4:7], s98 offen offset:2048 sc1\n"
"v_mfma_f32_16x16x32_bf16 v[196:199], a[200:203], v[180:183], v[196:199]\n"
"ds_write_b128 v3, v[124:127] offset:15360\n"
"v_mfma_f32_16x16x32_bf16 v[200:203], a[204:207], v[180:183], v[200:203]\n"
"v_mfma_f32_16x16x32_bf16 v[188:191], a[208:211], v[88:91], v[188:191]\n"
"buffer_load_dwordx4 v[112:115], v1, s[4:7], s98 offen offset:3072 sc1\n"
"v_mfma_f32_16x16x32_bf16 v[192:195], a[212:215], v[88:91], v[192:195]\n"
"s_waitcnt lgkmcnt(0)\n"
"s_barrier\n"
"v_mfma_f32_16x16x32_bf16 v[196:199], a[216:219], v[88:91], v[196:199]\n"
"s_add_u32 s98, s57, 0\n"
"ds_read_b128 v[116:119], v5\n"
"v_mfma_f32_16x16x32_bf16 v[200:203], a[220:223], v[88:91], v[200:203]\n"
"ds_read_b128 v[88:91], v6\n"
"v_mfma_f32_16x16x32_bf16 v[188:191], a[224:227], v[92:95], v[188:191]\n"
"buffer_load_dwordx4 v[124:127], v0, s[4:7], s98 offen sc1\n"
"v_mfma_f32_16x16x32_bf16 v[192:195], a[228:231], v[92:95], v[192:195]\n"
"ds_read_b128 v[140:143], v5 offset:1024\n"
"ds_read_b128 v[144:147], v6 offset:1024\n"
"v_mfma_f32_16x16x32_bf16 v[196:199], a[232:235], v[92:95], v[196:199]\n"
"ds_read_b128 v[148:151], v5 offset:2048\n"
"ds_read_b128 v[152:155], v6 offset:2048\n"
"v_mfma_f32_16x16x32_bf16 v[200:203], a[236:239], v[92:95], v[200:203]\n"
"buffer_load_dwordx4 v[92:95], v0, s[4:7], s98 offen offset:1024 sc1\n"
"v_mfma_f32_16x16x32_bf16 v[188:191], a[240:243], v[100:103], v[188:191]\n"
"ds_read_b128 v[156:159], v5 offset:3072\n"
"ds_read_b128 v[160:163], v6 offset:3072\n"
"v_mfma_f32_16x16x32_bf16 v[192:195], a[244:247], v[100:103], v[192:195]\n"
"v_mfma_f32_16x16x32_bf16 v[196:199], a[248:251], v[100:103], v[196:199]\n"
"buffer_load_dwordx4 v[164:167], v0, s[4:7], s98 offen offset:2048 sc1\n"
"v_mfma_f32_16x16x32_bf16 v[200:203], a[252:255], v[100:103], v[200:203]\n"
"s_waitcnt lgkmcnt(4)\n"
"v_add_f32_e32 v116, v116, v88\n"
"v_add_f32_e32 v117, v117, v89\n"
"v_add_f32_e32 v118, v118, v90\n"
"v_add_f32_e32 v119, v119, v91\n"
"v_add_f32_e32 v140, v140, v144\n"
"v_add_f32_e32 v141, v141, v145\n"
"v_add_f32_e32 v142, v142, v146\n"
"v_add_f32_e32 v143, v143, v147\n"
"buffer_load_dwordx4 v[88:91], v0, s[4:7], s98 offen offset:3072 sc1\n"
"s_waitcnt lgkmcnt(0)\n"
"v_add_f32_e32 v148, v148, v152\n"
"v_add_f32_e32 v149, v149, v153\n"
"v_add_f32_e32 v150, v150, v154\n"
"v_add_f32_e32 v151, v151, v155\n"
"v_add_f32_e32 v156, v156, v160\n"
"v_add_f32_e32 v157, v157, v161\n"
"v_add_f32_e32 v158, v158, v162\n"
"v_add_f32_e32 v159, v159, v163\n"
"buffer_load_dwordx4 v[100:103], v1, s[4:7], s98 offen sc1\n"
"buffer_load_dwordx4 v[144:147], v1, s[4:7], s98 offen offset:1024 sc1\n"
"buffer_load_dwordx4 v[152:155], v1, s[4:7], s98 offen offset:2048 sc1\n"
"buffer_load_dwordx4 v[160:163], v1, s[4:7], s98 offen offset:3072 sc1\n"
"; @A fin b\n"
"v_mov_b32_e32 v168, 0x3f803f80\n"
"v_mov_b32_e32 v169, 0x3f803f80\n"
"v_mov_b32_e32 v170, 0x3f803f80\n"
"v_mov_b32_e32 v171, 0x3f803f80\n"
"v_add_f32_e32 v116, v188, v116\n"
"v_add_f32_e32 v117, v189, v117\n"
"v_add_f32_e32 v118, v190, v118\n"
"v_add_f32_e32 v119, v191, v119\n"
"v_add_f32_e32 v140, v192, v140\n"
"v_add_f32_e32 v141, v193, v141\n"
"v_add_f32_e32 v142, v194, v142\n"
"v_add_f32_e32 v143, v195, v143\n"
"v_cvt_pk_bf16_f32 v172, v116, v117\n"
"v_cvt_pk_bf16_f32 v173, v118, v119\n"
"v_cvt_pk_bf16_f32 v174, v140, v141\n"
"v_cvt_pk_bf16_f32 v175, v142, v143\n"
"s_add_u32 s98, s61, 0x440000\n"
"buffer_store_dwordx4 v[172:175], v0, s[4:7], s98 offen sc1\n"
"ds_write_b128 v2, v[172:175] offset:8192\n"
"v_add_f32_e32 v148, v196, v148\n"
"v_add_f32_e32 v149, v197, v149\n"
"v_add_f32_e32 v150, v198, v150\n"
"v_add_f32_e32 v151, v199, v151\n"
"v_add_f32_e32 v156, v200, v156\n"
"v_add_f32_e32 v157, v201, v157\n"
"v_add_f32_e32 v158, v202, v158\n"
"v_add_f32_e32 v159, v203, v159\n"
"v_cvt_pk_bf16_f32 v116, v148, v149\n"
"v_cvt_pk_bf16_f32 v117, v150, v151\n"
"v_cvt_pk_bf16_f32 v118, v156, v157\n"
"v_cvt_pk_bf16_f32 v119, v158, v159\n"
"s_add_u32 s98, s61, 0x440000\n"
"buffer_store_dwordx4 v[116:119], v0, s[4:7], s98 offen offset:1024 sc1\n"
"ds_write_b128 v2, v[116:119] offset:9216\n"
"v_mfma_f32_16x16x32_bf16 v[140:143], v[168:171], v[172:175], 0\n"
"v_mfma_f32_16x16x32_bf16 v[148:151], v[172:175], v[172:175], 0\n"
"v_mfma_f32_16x16x32_bf16 v[140:143], v[168:171], v[116:119], v[140:143]\n"
"v_mfma_f32_16x16x32_bf16 v[148:151], v[116:119], v[116:119], v[148:151]\n"
"s_mov_b32 s96, 0xaaaaaaaa\n"
"s_mov_b32 s97, 0xaaaaaaaa\n"
"s_nop 5\n"
"v_cndmask_b32_e64 v148, v148, v149, s[96:97]\n"
"v_cndmask_b32_e64 v150, v150, v151, s[96:97]\n"
"s_mov_b32 s96, 0xcccccccc\n"
"s_mov_b32 s97, 0xcccccccc\n"
"v_cndmask_b32_e64 v141, v148, v150, s[96:97]\n"
"s_add_u32 s98, s63, 0x44000\n"
"buffer_store_dwordx2 v[140:141], v14, s[4:7], s98 offen sc1\n"
"s_waitcnt vmcnt(18)\n"
"; @A G2 a.0\n"
"v_mfma_f32_16x16x32_bf16 v[116:119], a[128:131], v[120:123], 0\n"
"s_add_u32 s98, s58, 0\n"
"v_mfma_f32_16x16x32_bf16 v[140:143], a[132:135], v[120:123], 0\n"
"buffer_load_dwordx4 v[148:151], v0, s[4:7], s98 offen sc1\n"
"v_mfma_f32_16x16x32_bf16 v[156:159], a[136:139], v[120:123], 0\n"
"v_mfma_f32_16x16x32_bf16 v[168:171], a[140:143], v[120:123], 0\n"
"s_waitcnt vmcnt(18)\n"
"v_mfma_f32_16x16x32_bf16 v[116:119], a[144:147], v[108:111], v[116:119]\n"
"v_mfma_f32_16x16x32_bf16 v[140:143], a[148:151], v[108:111], v[140:143]\n"
"buffer_load_dwordx4 v[120:123], v0, s[4:7], s98 offen offset:1024 sc1\n"
"v_mfma_f32_16x16x32_bf16 v[156:159], a[152:155], v[108:111], v[156:159]\n"
"v_mfma_f32_16x16x32_bf16 v[168:171], a[156:159], v[108:111], v[168:171]\n"
"s_waitcnt vmcnt(18)\n"
"v_mfma_f32_16x16x32_bf16 v[116:119], a[160:163], v[132:135], v[116:119]\n"
"v_mfma_f32_16x16x32_bf16 v[140:143], a[164:167], v[132:135], v[140:143]\n"
"buffer_load_dwordx4 v[172:175], v0, s[4:7], s98 offen offset:2048 sc1\n"
"v_mfma_f32_16x16x32_bf16 v[156:159], a[168:171], v[132:135], v[156:159]\n"
"v_mfma_f32_16x16x32_bf16 v[168:171], a[172:175], v[132:135], v[168:171]\n"
"s_waitcnt vmcnt(18)\n"
"v_mfma_f32_16x16x32_bf16 v[116:119], a[176:179], v[136:139], v[116:119]\n"
"v_mfma_f32_16x16x32_bf16 v[140:143], a[180:183], v[136:139], v[140:143]\n"
"buffer_load_dwordx4 v[132:135], v0, s[4:7], s98 offen offset:3072 sc1\n"
"v_mfma_f32_16x16x32_bf16 v[156:159], a[184:187], v[136:139], v[156:159]\n"
"v_mfma_f32_16x16x32_bf16 v[168:171], a[188:191], v[136:139], v[168:171]\n"
"s_waitcnt vmcnt(18)\n"
"v_mfma_f32_16x16x32_bf16 v[116:119], a[192:195], v[128:131], v[116:119]\n"
"v_mfma_f32_16x16x32_bf16 v[140:143], a[196:199], v[128:131], v[140:143]\n"
"buffer_load_dwordx4 v[136:139], v1, s[4:7], s98 offen sc1\n"
"v_mfma_f32_16x16x32_bf16 v[156:159], a[200:203], v[128:131], v[156:159]\n"
"v_mfma_f32_16x16x32_bf16 v[168:171], a[204:207], v[128:131], v[168:171]\n"
"s_waitcnt vmcnt(18)\n"
"v_mfma_f32_16x16x32_bf16 v[116:119], a[208:211], v[96:99], v[116:119]\n"
"v_mfma_f32_16x16x32_bf16 v[140:143], a[212:215], v[96:99], v[140:143]\n"
"buffer_load_dwordx4 v[128:131], v1, s[4:7], s98 offen offset:1024 sc1\n"
"v_mfma_f32_16x16x32_bf16 v[156:159], a[216:219], v[96:99], v[156:159]\n"
"v_mfma_f32_16x16x32_bf16 v[168:171], a[220:223], v[96:99], v[168:171]\n"
"s_waitcnt vmcnt(18)\n"
"v_mfma_f32_16x16x32_bf16 v[116:119], a[224:227], v[104:107], v[116:119]\n"
"v_mfma_f32_16x16x32_bf16 v[140:143], a[228:231], v[104:107], v[140:143]\n"
"buffer_load_dwordx4 v[176:179], v1, s[4:7], s98 offen offset:2048 sc1\n"
"v_mfma_f32_16x16x32_bf16 v[156:159], a[232:235], v[104:107], v[156:159]\n"
"v_mfma_f32_16x16x32_bf16 v[168:171], a[236:239], v[104:107], v[168:171]\n"
"s_waitcnt vmcnt(18)\n"
"v_mfma_f32_16x16x32_bf16 v[116:119], a[240:243], v[112:115], v[116:119]\n"
"v_mfma_f32_16x16x32_bf16 v[140:143], a[244:247], v[112:115], v[140:143]\n"
"buffer_load_dwordx4 v[180:183], v1, s[4:7], s98 offen offset:3072 sc1\n"
"v_mfma_f32_16x16x32_bf16 v[156:159], a[248:251], v[112:115], v[156:159]\n"
"v_mfma_f32_16x16x32_bf16 v[168:171], a[252:255], v[112:115], v[168:171]\n"
"s_waitcnt vmcnt(17)\n"
"s_add_u32 s98, s59, 0\n"
"; @A G2 a.1\n"
"v_mfma_f32_16x16x32_bf16 v[108:111], a[128:131], v[124:127], 0\n"
"s_nop 0\n"
"v_cmp_u_f32_e32 vcc, v116, v116\n"
"v_mfma_f32_16x16x32_bf16 v[112:115], a[132:135], v[124:127], 0\n"
"s_cbranch_vccnz .Lmk_p41s\n"
".Lmk_p41r:\n"
"v_mfma_f32_16x16x32_bf16 v[184:187], a[136:139], v[124:127], 0\n"
"buffer_load_dwordx4 v[188:191], v0, s[4:7], s98 offen sc1\n"
"v_mfma_f32_16x16x32_bf16 v[192:195], a[140:143], v[124:127], 0\n"
"v_mfma_f32_16x16x32_bf16 v[108:111], a[144:147], v[92:95], v[108:111]\n"
"ds_write_b128 v3, v[116:119]\n"
"v_mfma_f32_16x16x32_bf16 v[112:115], a[148:151], v[92:95], v[112:115]\n"
"v_mfma_f32_16x16x32_bf16 v[184:187], a[152:155], v[92:95], v[184:187]\n"
"ds_write_b128 v3, v[140:143] offset:1024\n"
"v_mfma_f32_16x16x32_bf16 v[192:195], a[156:159], v[92:95], v[192:195]\n"
"s_waitcnt vmcnt(16)\n"
"v_mfma_f32_16x16x32_bf16 v[108:111], a[160:163], v[164:167], v[108:111]\n"
"ds_write_b128 v3, v[156:159] offset:2048\n"
"v_mfma_f32_16x16x32_bf16 v[112:115], a[164:167], v[164:167], v[112:115]\n"
"buffer_load_dwordx4 v[140:143], v0, s[4:7], s98 offen offset:1024 sc1\n"
"v_mfma_f32_16x16x32_bf16 v[184:187], a[168:171], v[164:167], v[184:187]\n"
"ds_write_b128 v3, v[168:171] offset:3072\n"
"v_mfma_f32_16x16x32_bf16 v[192:195], a[172:175], v[164:167], v[192:195]\n"
"s_add_u32 s97, s75, 0x10000\n"
"v_mfma_f32_16x16x32_bf16 v[108:111], a[176:179], v[88:91], v[108:111]\n"
"buffer_load_dwordx2 v[254:255], v13, s[4:7], s97 offen sc1\n"
"v_mfma_f32_16x16x32_bf16 v[112:115], a[180:183], v[88:91], v[112:115]\n"
"buffer_load_dwordx4 v[156:159], v0, s[4:7], s98 offen offset:2048 sc1\n"
"v_mfma_f32_16x16x32_bf16 v[184:187], a[184:187], v[88:91], v[184:187]\n"
"ds_read_b128 v[104:107], v2\n"
"ds_read_b128 v[116:119], v2 offset:1024\n"
"v_mfma_f32_16x16x32_bf16 v[192:195], a[188:191], v[88:91], v[192:195]\n"
"s_waitcnt vmcnt(17)\n"
"v_mfma_f32_16x16x32_bf16 v[108:111], a[192:195], v[100:103], v[108:111]\n"
"v_mfma_f32_16x16x32_bf16 v[112:115], a[196:199], v[100:103], v[112:115]\n"
"buffer_load_dwordx4 v[164:167], v0, s[4:7], s98 offen offset:3072 sc1\n"
"v_mfma_f32_16x16x32_bf16 v[184:187], a[200:203], v[100:103], v[184:187]\n"
"v_mfma_f32_16x16x32_bf16 v[192:195], a[204:207], v[100:103], v[192:195]\n"
"s_waitcnt lgkmcnt(0)\n"
"s_barrier\n"
"v_mfma_f32_16x16x32_bf16 v[108:111], a[208:211], v[144:147], v[108:111]\n"
"v_lshlrev_b32_e32 v253, 16, v104\n"
"v_mfma_f32_16x16x32_bf16 v[112:115], a[212:215], v[144:147], v[112:115]\n"
"buffer_load_dwordx4 v[168:171], v1, s[4:7], s98 offen sc1\n"
"v_mfma_f32_16x16x32_bf16 v[184:187], a[216:219], v[144:147], v[184:187]\n"
"ds_read_b128 v[88:91], v4\n"
"ds_read_b128 v[92:95], v4 offset:1024\n"
"v_mfma_f32_16x16x32_bf16 v[192:195], a[220:223], v[144:147], v[192:195]\n"
"s_waitcnt vmcnt(17)\n"
"ds_read_b128 v[96:99], v4 offset:2048\n"
"v_mfma_f32_16x16x32_bf16 v[108:111], a[224:227], v[152:155], v[108:111]\n"
"ds_read_b128 v[100:103], v4 offset:3072\n"
"v_and_b32_e32 v252, 0xffff0000, v104\n"
"v_mfma_f32_16x16x32_bf16 v[112:115], a[228:231], v[152:155], v[112:115]\n"
"buffer_load_dwordx4 v[144:147], v1, s[4:7], s98 offen offset:1024 sc1\n"
"v_mfma_f32_16x16x32_bf16 v[184:187], a[232:235], v[152:155], v[184:187]\n"
"v_lshlrev_b32_e32 v251, 16, v105\n"
"v_and_b32_e32 v250, 0xffff0000, v105\n"
"v_mfma_f32_16x16x32_bf16 v[192:195], a[236:239], v[152:155], v[192:195]\n"
"v_lshlrev_b32_e32 v249, 16, v106\n"
"v_and_b32_e32 v248, 0xffff0000, v106\n"
"v_mfma_f32_16x16x32_bf16 v[108:111], a[240:243], v[160:163], v[108:111]\n"
"v_lshlrev_b32_e32 v247, 16, v107\n"
"v_and_b32_e32 v246, 0xffff0000, v107\n"
"v_mfma_f32_16x16x32_bf16 v[112:115], a[244:247], v[160:163], v[112:115]\n"
"buffer_load_dwordx4 v[152:155], v1, s[4:7], s98 offen offset:2048 sc1\n"
"v_mfma_f32_16x16x32_bf16 v[184:187], a[248:251], v[160:163], v[184:187]\n"
"v_lshlrev_b32_e32 v245, 16, v116\n"
"v_and_b32_e32 v244, 0xffff0000, v116\n"
"v_mfma_f32_16x16x32_bf16 v[192:195], a[252:255], v[160:163], v[192:195]\n"
"s_waitcnt vmcnt(14)\n"
"v_lshlrev_b32_e32 v243, 16, v117\n"
"; @A G2 a.2\n"
"v_mfma_f32_16x16x32_bf16 v[160:163], a[128:131], v[148:151], 0\n"
"v_cmp_u_f32_e32 vcc, v108, v108\n"
"s_add_u32 s96, s52, 0x440000\n"
"v_mfma_f32_16x16x32_bf16 v[196:199], a[132:135], v[148:151], 0\n"
"s_cbranch_vccnz .Lmk_p42s\n"
".Lmk_p42r:\n"
"v_and_b32_e32 v242, 0xffff0000, v117\n"
"v_mfma_f32_16x16x32_bf16 v[200:203], a[136:139], v[148:151], 0\n"
"buffer_load_dwordx4 v[204:207], v1, s[4:7], s98 offen offset:3072 sc1\n"
"v_mfma_f32_16x16x32_bf16 v[208:211], a[140:143], v[148:151], 0\n"
"buffer_load_dwordx4 v[104:107], v0, s[4:7], s96 offen sc1\n"
"v_mfma_f32_16x16x32_bf16 v[160:163], a[144:147], v[120:123], v[160:163]\n"
"ds_write_b128 v3, v[108:111] offset:6144\n"
"v_mfma_f32_16x16x32_bf16 v[196:199], a[148:151], v[120:123], v[196:199]\n"
"v_lshlrev_b32_e32 v241, 16, v118\n"
"v_and_b32_e32 v240, 0xffff0000, v118\n"
"v_mfma_f32_16x16x32_bf16 v[200:203], a[152:155], v[120:123], v[200:203]\n"
"ds_write_b128 v3, v[112:115] offset:7168\n"
"v_mfma_f32_16x16x32_bf16 v[208:211], a[156:159], v[120:123], v[208:211]\n"
"s_waitcnt vmcnt(14)\n"
"v_lshlrev_b32_e32 v239, 16, v119\n"
"v_mfma_f32_16x16x32_bf16 v[160:163], a[160:163], v[172:175], v[160:163]\n"
"ds_write_b128 v3, v[184:187] offset:8192\n"
"v_mfma_f32_16x16x32_bf16 v[196:199], a[164:167], v[172:175], v[196:199]\n"
"buffer_load_dwordx4 v[108:111], v0, s[4:7], s96 offen offset:1024 sc1\n"
"v_mfma_f32_16x16x32_bf16 v[200:203], a[168:171], v[172:175], v[200:203]\n"
"ds_write_b128 v3, v[192:195] offset:9216\n"
"v_mfma_f32_16x16x32_bf16 v[208:211], a[172:175], v[172:175], v[208:211]\n"
"v_and_b32_e32 v238, 0xffff0000, v119\n"
"s_waitcnt lgkmcnt(4)\n"
"v_mfma_f32_16x16x32_bf16 v[160:163], a[176:179], v[132:135], v[160:163]\n"
"v_add_f32_e32 v88, v88, v253\n"
"v_add_f32_e32 v89, v89, v252\n"
"v_mfma_f32_16x16x32_bf16 v[196:199], a[180:183], v[132:135], v[196:199]\n"
"buffer_load_dwordx4 v[112:115], v0, s[4:7], s96 offen offset:2048 sc1\n"
"v_mfma_f32_16x16x32_bf16 v[200:203], a[184:187], v[132:135], v[200:203]\n"
"v_add_f32_e32 v90, v90, v251\n"
"v_add_f32_e32 v91, v91, v250\n"
"v_mfma_f32_16x16x32_bf16 v[208:211], a[188:191], v[132:135], v[208:211]\n"
"s_waitcnt vmcnt(14)\n"
"v_add_f32_e32 v92, v92, v249\n"
"v_mfma_f32_16x16x32_bf16 v[160:163], a[192:195], v[136:139], v[160:163]\n"
"v_add_f32_e32 v93, v93, v248\n"
"v_add_f32_e32 v94, v94, v247\n"
"v_mfma_f32_16x16x32_bf16 v[196:199], a[196:199], v[136:139], v[196:199]\n"
"buffer_load_dwordx4 v[116:119], v0, s[4:7], s96 offen offset:3072 sc1\n"
"v_mfma_f32_16x16x32_bf16 v[200:203], a[200:203], v[136:139], v[200:203]\n"
"v_add_f32_e32 v95, v95, v246\n"
"v_add_f32_e32 v96, v96, v245\n"
"v_mfma_f32_16x16x32_bf16 v[208:211], a[204:207], v[136:139], v[208:211]\n"
"v_add_f32_e32 v97, v97, v244\n"
"v_add_f32_e32 v98, v98, v243\n"
"v_mfma_f32_16x16x32_bf16 v[160:163], a[208:211], v[128:131], v[160:163]\n"
"v_add_f32_e32 v99, v99, v242\n"
"v_add_f32_e32 v100, v100, v241\n"
"v_mfma_f32_16x16x32_bf16 v[196:199], a[212:215], v[128:131], v[196:199]\n"
"buffer_load_dwordx4 v[120:123], v1, s[4:7], s96 offen sc1\n"
"v_mfma_f32_16x16x32_bf16 v[200:203], a[216:219], v[128:131], v[200:203]\n"
"v_add_f32_e32 v101, v101, v240\n"
"v_add_f32_e32 v102, v102, v239\n"
"v_mfma_f32_16x16x32_bf16 v[208:211], a[220:223], v[128:131], v[208:211]\n"
"s_waitcnt vmcnt(14)\n"
"v_add_f32_e32 v103, v103, v238\n"
"v_mfma_f32_16x16x32_bf16 v[160:163], a[224:227], v[176:179], v[160:163]\n"
"s_waitcnt vmcnt(12)\n"
"v_max_u32_e32 v253, v188, v189\n"
"v_mfma_f32_16x16x32_bf16 v[196:199], a[228:231], v[176:179], v[196:199]\n"
"v_max3_u32 v253, v253, v190, v191\n"
"v_max3_u32 v253, v253, v140, v141\n"
"v_mfma_f32_16x16x32_bf16 v[200:203], a[232:235], v[176:179], v[200:203]\n"
"v_max3_u32 v253, v253, v142, v143\n"
"s_waitcnt vmcnt(9)\n"
"v_mfma_f32_16x16x32_bf16 v[208:211], a[236:239], v[176:179], v[208:211]\n"
"v_max3_u32 v253, v253, v156, v157\n"
"v_max3_u32 v253, v253, v158, v159\n"
"v_mfma_f32_16x16x32_bf16 v[160:163], a[240:243], v[180:183], v[160:163]\n"
"v_max3_u32 v253, v253, v164, v165\n"
"v_max3_u32 v253, v253, v166, v167\n"
"v_mfma_f32_16x16x32_bf16 v[196:199], a[244:247], v[180:183], v[196:199]\n"
"v_cmp_eq_u32_e32 vcc, -1, v253\n"
"v_mfma_f32_16x16x32_bf16 v[200:203], a[248:251], v[180:183], v[200:203]\n"
"s_cbranch_vccnz .Lmk_q44s\n"
".Lmk_q44r:\n"
"v_mfma_f32_16x16x32_bf16 v[208:211], a[252:255], v[180:183], v[208:211]\n"
"; @A G2 a.3\n"
"v_mfma_f32_16x16x32_bf16 v[88:91], a[128:131], v[188:191], v[88:91]\n"
"v_cmp_u_f32_e32 vcc, v160, v160\n"
"v_mfma_f32_16x16x32_bf16 v[92:95], a[132:135], v[188:191], v[92:95]\n"
"s_cbranch_vccnz .Lmk_p43s\n"
".Lmk_p43r:\n"
"v_mfma_f32_16x16x32_bf16 v[96:99], a[136:139], v[188:191], v[96:99]\n"
"buffer_load_dwordx4 v[124:127], v1, s[4:7], s96 offen offset:1024 sc1\n"
"v_mfma_f32_16x16x32_bf16 v[100:103], a[140:143], v[188:191], v[100:103]\n"
"v_mfma_f32_16x16x32_bf16 v[88:91], a[144:147], v[140:143], v[88:91]\n"
"ds_write_b128 v3, v[160:163] offset:12288\n"
"v_mfma_f32_16x16x32_bf16 v[92:95], a[148:151], v[140:143], v[92:95]\n"
"v_cmp_ne_u64_e32 vcc, v[254:255], v[18:19]\n"
"v_mfma_f32_16x16x32_bf16 v[96:99], a[152:155], v[140:143], v[96:99]\n"
"ds_write_b128 v3, v[196:199] offset:13312\n"
"v_mfma_f32_16x16x32_bf16 v[100:103], a[156:159], v[140:143], v[100:103]\n"
"s_cbranch_vccnz .Lmk_f13s\n"
".Lmk_f13r:\n"
"v_mfma_f32_16x16x32_bf16 v[88:91], a[160:163], v[156:159], v[88:91]\n"
"s_waitcnt vmcnt(8)\n"
"v_max_u32_e32 v255, v168, v169\n"
"v_mfma_f32_16x16x32_bf16 v[92:95], a[164:167], v[156:159], v[92:95]\n"
"v_max3_u32 v255, v255, v170, v171\n"
"v_max3_u32 v255, v255, v144, v145\n"
"v_mfma_f32_16x16x32_bf16 v[96:99], a[168:171], v[156:159], v[96:99]\n"
"v_max3_u32 v255, v255, v146, v147\n"
"s_waitcnt vmcnt(6)\n"
"v_mfma_f32_16x16x32_bf16 v[100:103], a[172:175], v[156:159], v[100:103]\n"
"v_max3_u32 v255, v255, v152, v153\n"
"v_max3_u32 v255, v255, v154, v155\n"
"v_mfma_f32_16x16x32_bf16 v[88:91], a[176:179], v[164:167], v[88:91]\n"
"v_max3_u32 v255, v255, v204, v205\n"
"v_max3_u32 v255, v255, v206, v207\n"
"v_mfma_f32_16x16x32_bf16 v[92:95], a[180:183], v[164:167], v[92:95]\n"
"v_cmp_eq_u32_e32 vcc, -1, v255\n"
"v_mfma_f32_16x16x32_bf16 v[96:99], a[184:187], v[164:167], v[96:99]\n"
"s_cbranch_vccnz .Lmk_q45s\n"
".Lmk_q45r:\n"
"v_mfma_f32_16x16x32_bf16 v[100:103], a[188:191], v[164:167], v[100:103]\n"
"v_mfma_f32_16x16x32_bf16 v[88:91], a[192:195], v[168:171], v[88:91]\n"
"ds_write_b128 v3, v[200:203] offset:14336\n"
"v_mfma_f32_16x16x32_bf16 v[92:95], a[196:199], v[168:171], v[92:95]\n"
"buffer_load_dwordx4 v[128:131], v1, s[4:7], s96 offen offset:2048 sc1\n"
"v_mfma_f32_16x16x32_bf16 v[96:99], a[200:203], v[168:171], v[96:99]\n"
"ds_write_b128 v3, v[208:211] offset:15360\n"
"v_mfma_f32_16x16x32_bf16 v[100:103], a[204:207], v[168:171], v[100:103]\n"
"v_mfma_f32_16x16x32_bf16 v[88:91], a[208:211], v[144:147], v[88:91]\n"
"v_mfma_f32_16x16x32_bf16 v[92:95], a[212:215], v[144:147], v[92:95]\n"
"s_waitcnt lgkmcnt(0)\n"
"s_barrier\n"
"v_mfma_f32_16x16x32_bf16 v[96:99], a[216:219], v[144:147], v[96:99]\n"
"buffer_load_dwordx4 v[132:135], v1, s[4:7], s96 offen offset:3072 sc1\n"
"v_mfma_f32_16x16x32_bf16 v[100:103], a[220:223], v[144:147], v[100:103]\n"
"ds_read_b128 v[136:139], v5\n"
"v_mfma_f32_16x16x32_bf16 v[88:91], a[224:227], v[152:155], v[88:91]\n"
"s_add_u32 s98, s53, 0x440000\n"
"ds_read_b128 v[160:163], v6\n"
"v_mfma_f32_16x16x32_bf16 v[92:95], a[228:231], v[152:155], v[92:95]\n"
"ds_read_b128 v[140:143], v5 offset:1024\n"
"ds_read_b128 v[164:167], v6 offset:1024\n"
"v_mfma_f32_16x16x32_bf16 v[96:99], a[232:235], v[152:155], v[96:99]\n"
"buffer_load_dwordx4 v[144:147], v0, s[4:7], s98 offen sc1\n"
"v_mfma_f32_16x16x32_bf16 v[100:103], a[236:239], v[152:155], v[100:103]\n"
"ds_read_b128 v[148:151], v5 offset:2048\n"
"ds_read_b128 v[168:171], v6 offset:2048\n"
"v_mfma_f32_16x16x32_bf16 v[88:91], a[240:243], v[204:207], v[88:91]\n"
"ds_read_b128 v[152:155], v5 offset:3072\n"
"ds_read_b128 v[172:175], v6 offset:3072\n"
"v_mfma_f32_16x16x32_bf16 v[92:95], a[244:247], v[204:207], v[92:95]\n"
"v_mfma_f32_16x16x32_bf16 v[96:99], a[248:251], v[204:207], v[96:99]\n"
"buffer_load_dwordx4 v[156:159], v0, s[4:7], s98 offen offset:1024 sc1\n"
"v_mfma_f32_16x16x32_bf16 v[100:103], a[252:255], v[204:207], v[100:103]\n"
"s_waitcnt lgkmcnt(4)\n"
"v_add_f32_e32 v136, v136, v160\n"
"v_add_f32_e32 v137, v137, v161\n"
"v_add_f32_e32 v138, v138, v162\n"
"v_add_f32_e32 v139, v139, v163\n"
"v_add_f32_e32 v140, v140, v164\n"
"v_add_f32_e32 v141, v141, v165\n"
"v_add_f32_e32 v142, v142, v166\n"
"v_add_f32_e32 v143, v143, v167\n"
"s_waitcnt lgkmcnt(0)\n"
"v_add_f32_e32 v148, v148, v168\n"
"v_add_f32_e32 v149, v149, v169\n"
"v_add_f32_e32 v150, v150, v170\n"
"v_add_f32_e32 v151, v151, v171\n"
"v_add_f32_e32 v152, v152, v172\n"
"v_add_f32_e32 v153, v153, v173\n"
"v_add_f32_e32 v154, v154, v174\n"
"v_add_f32_e32 v155, v155, v175\n"
"buffer_load_dwordx4 v[160:163], v0, s[4:7], s98 offen offset:2048 sc1\n"
"buffer_load_dwordx4 v[164:167], v0, s[4:7], s98 offen offset:3072 sc1\n"
"buffer_load_dwordx4 v[168:171], v1, s[4:7], s98 offen sc1\n"
"buffer_load_dwordx4 v[172:175], v1, s[4:7], s98 offen offset:1024 sc1\n"
"buffer_load_dwordx4 v[176:179], v1, s[4:7], s98 offen offset:2048 sc1\n"
"buffer_load_dwordx4 v[180:183], v1, s[4:7], s98 offen offset:3072 sc1\n"
".Lmk_loop:\n"
".Lmk_b0:\n"
"s_waitcnt vmcnt(15)\n"
"; @B G1 0.0\n"
"v_mfma_f32_16x16x32_bf16 v[184:187], a[0:3], v[104:107], 0\n"
"s_add_u32 s98, s62, 0x44000\n"
"v_mfma_f32_16x16x32_bf16 v[188:191], a[4:7], v[104:107], 0\n"
"buffer_load_dwordx2 v[254:255], v10, s[4:7], s98 offen sc1\n"
"v_mfma_f32_16x16x32_bf16 v[192:195], a[8:11], v[104:107], 0\n"
"buffer_load_dwordx2 v[252:253], v10, s[4:7], s98 offen offset:128 sc1\n"
"v_mfma_f32_16x16x32_bf16 v[196:199], a[12:15], v[104:107], 0\n"
"buffer_load_dwordx2 v[250:251], v10, s[4:7], s98 offen offset:256 sc1\n"
"v_mfma_f32_16x16x32_bf16 v[200:203], a[16:19], v[104:107], 0\n"
"s_add_u32 s97, s54, 0x440000\n"
"v_mov_b32_e32 v204, 0x3f803f80\n"
"v_mfma_f32_16x16x32_bf16 v[208:211], a[20:23], v[104:107], 0\n"
"s_waitcnt vmcnt(17)\n"
"v_mov_b32_e32 v205, 0x3f803f80\n"
"v_mfma_f32_16x16x32_bf16 v[184:187], a[24:27], v[108:111], v[184:187]\n"
"buffer_load_dwordx4 v[104:107], v0, s[4:7], s97 offen sc1\n"
"v_mfma_f32_16x16x32_bf16 v[188:191], a[28:31], v[108:111], v[188:191]\n"
"buffer_load_dwordx2 v[248:249], v10, s[4:7], s98 offen offset:384 sc1\n"
"v_mfma_f32_16x16x32_bf16 v[192:195], a[32:35], v[108:111], v[192:195]\n"
"v_mov_b32_e32 v206, 0x3f803f80\n"
"v_mov_b32_e32 v207, 0x3f803f80\n"
"v_mfma_f32_16x16x32_bf16 v[196:199], a[36:39], v[108:111], v[196:199]\n"
"v_add_f32_e32 v136, v88, v136\n"
"v_add_f32_e32 v137, v89, v137\n"
"v_mfma_f32_16x16x32_bf16 v[200:203], a[40:43], v[108:111], v[200:203]\n"
"buffer_load_dwordx4 v[212:215], v0, s[4:7], s97 offen offset:1024 sc1\n"
"v_mfma_f32_16x16x32_bf16 v[208:211], a[44:47], v[108:111], v[208:211]\n"
"s_waitcnt vmcnt(19)\n"
"v_add_f32_e32 v138, v90, v138\n"
"v_mfma_f32_16x16x32_bf16 v[184:187], a[48:51], v[112:115], v[184:187]\n"
"v_add_f32_e32 v139, v91, v139\n"
"v_add_f32_e32 v140, v92, v140\n"
"v_mfma_f32_16x16x32_bf16 v[188:191], a[52:55], v[112:115], v[188:191]\n"
"v_add_f32_e32 v141, v93, v141\n"
"v_add_f32_e32 v142, v94, v142\n"
"v_mfma_f32_16x16x32_bf16 v[192:195], a[56:59], v[112:115], v[192:195]\n"
"v_add_f32_e32 v143, v95, v143\n"
"v_cvt_pk_bf16_f32 v88, v136, v137\n"
"v_mfma_f32_16x16x32_bf16 v[196:199], a[60:63], v[112:115], v[196:199]\n"
"v_cvt_pk_bf16_f32 v89, v138, v139\n"
"v_cvt_pk_bf16_f32 v90, v140, v141\n"
"v_mfma_f32_16x16x32_bf16 v[200:203], a[64:67], v[112:115], v[200:203]\n"
"buffer_load_dwordx4 v[92:95], v0, s[4:7], s97 offen offset:2048 sc1\n"
"v_mfma_f32_16x16x32_bf16 v[208:211], a[68:71], v[112:115], v[208:211]\n"
"s_waitcnt vmcnt(19)\n"
"v_cvt_pk_bf16_f32 v91, v142, v143\n"
"v_mfma_f32_16x16x32_bf16 v[184:187], a[72:75], v[116:119], v[184:187]\n"
"s_add_u32 s98, s61, 0x400000\n"
"v_mfma_f32_16x16x32_bf16 v[188:191], a[76:79], v[116:119], v[188:191]\n"
"buffer_store_dwordx4 v[88:91], v0, s[4:7], s98 offen sc1\n"
"v_mfma_f32_16x16x32_bf16 v[192:195], a[80:83], v[116:119], v[192:195]\n"
"ds_write_b128 v2, v[88:91]\n"
"v_mfma_f32_16x16x32_bf16 v[196:199], a[84:87], v[116:119], v[196:199]\n"
"v_add_f32_e32 v148, v96, v148\n"
"v_add_f32_e32 v149, v97, v149\n"
"v_mfma_f32_16x16x32_bf16 v[200:203], a[88:91], v[116:119], v[200:203]\n"
"buffer_load_dwordx4 v[108:111], v0, s[4:7], s97 offen offset:3072 sc1\n"
"v_mfma_f32_16x16x32_bf16 v[208:211], a[92:95], v[116:119], v[208:211]\n"
"s_waitcnt vmcnt(20)\n"
"v_add_f32_e32 v150, v98, v150\n"
"v_mfma_f32_16x16x32_bf16 v[184:187], a[96:99], v[120:123], v[184:187]\n"
"v_add_f32_e32 v151, v99, v151\n"
"v_add_f32_e32 v152, v100, v152\n"
"v_mfma_f32_16x16x32_bf16 v[188:191], a[100:103], v[120:123], v[188:191]\n"
"v_add_f32_e32 v153, v101, v153\n"
"v_add_f32_e32 v154, v102, v154\n"
"v_mfma_f32_16x16x32_bf16 v[192:195], a[104:107], v[120:123], v[192:195]\n"
"v_add_f32_e32 v155, v103, v155\n"
"v_cvt_pk_bf16_f32 v96, v148, v149\n"
"v_mfma_f32_16x16x32_bf16 v[196:199], a[108:111], v[120:123], v[196:199]\n"
"v_cvt_pk_bf16_f32 v97, v150, v151\n"
"v_cvt_pk_bf16_f32 v98, v152, v153\n"
"v_mfma_f32_16x16x32_bf16 v[200:203], a[112:115], v[120:123], v[200:203]\n"
"buffer_load_dwordx4 v[100:103], v1, s[4:7], s97 offen sc1\n"
"v_mfma_f32_16x16x32_bf16 v[208:211], a[116:119], v[120:123], v[208:211]\n"
"s_waitcnt vmcnt(20)\n"
"v_cvt_pk_bf16_f32 v99, v154, v155\n"
"v_mfma_f32_16x16x32_bf16 v[184:187], a[120:123], v[124:127], v[184:187]\n"
"v_mov_b32_e32 v112, -1\n"
"v_mov_b32_e32 v113, -1\n"
"v_mfma_f32_16x16x32_bf16 v[188:191], a[124:127], v[124:127], v[188:191]\n"
"v_mov_b32_e32 v114, -1\n"
"v_mov_b32_e32 v115, -1\n"
"v_mfma_f32_16x16x32_bf16 v[192:195], v[24:27], v[124:127], v[192:195]\n"
"s_add_u32 s98, s60, 0x448000\n"
"s_add_u32 s96, s61, 0x400000\n"
"v_mfma_f32_16x16x32_bf16 v[196:199], v[28:31], v[124:127], v[196:199]\n"
"buffer_store_dwordx4 v[112:115], v0, s[4:7], s98 offen sc1\n"
"v_mfma_f32_16x16x32_bf16 v[200:203], v[32:35], v[124:127], v[200:203]\n"
"buffer_load_dwordx4 v[116:119], v1, s[4:7], s97 offen offset:1024 sc1\n"
"v_mfma_f32_16x16x32_bf16 v[208:211], v[36:39], v[124:127], v[208:211]\n"
"s_waitcnt vmcnt(21)\n"
"v_mfma_f32_16x16x32_bf16 v[184:187], v[40:43], v[128:131], v[184:187]\n"
"buffer_store_dwordx4 v[96:99], v0, s[4:7], s96 offen offset:1024 sc1\n"
"v_mfma_f32_16x16x32_bf16 v[188:191], v[44:47], v[128:131], v[188:191]\n"
"ds_write_b128 v2, v[96:99] offset:1024\n"
"v_mfma_f32_16x16x32_bf16 v[120:123], v[204:207], v[88:91], 0\n"
"v_mfma_f32_16x16x32_bf16 v[124:127], v[88:91], v[88:91], 0\n"
"v_mfma_f32_16x16x32_bf16 v[120:123], v[204:207], v[96:99], v[120:123]\n"
"v_mfma_f32_16x16x32_bf16 v[124:127], v[96:99], v[96:99], v[124:127]\n"
"v_mfma_f32_16x16x32_bf16 v[192:195], v[48:51], v[128:131], v[192:195]\n"
"s_mov_b32 s94, 0xaaaaaaaa\n"
"s_mov_b32 s95, 0xaaaaaaaa\n"
"v_mfma_f32_16x16x32_bf16 v[196:199], v[52:55], v[128:131], v[196:199]\n"
"v_mfma_f32_16x16x32_bf16 v[200:203], v[56:59], v[128:131], v[200:203]\n"
"buffer_load_dwordx4 v[88:91], v1, s[4:7], s97 offen offset:2048 sc1\n"
"v_mfma_f32_16x16x32_bf16 v[208:211], v[60:63], v[128:131], v[208:211]\n"
"s_waitcnt vmcnt(22)\n"
"v_cndmask_b32_e64 v124, v124, v125, s[94:95]\n"
"v_mfma_f32_16x16x32_bf16 v[184:187], v[64:67], v[132:135], v[184:187]\n"
"v_cndmask_b32_e64 v126, v126, v127, s[94:95]\n"
"v_mfma_f32_16x16x32_bf16 v[188:191], v[68:71], v[132:135], v[188:191]\n"
"s_mov_b32 s94, 0xcccccccc\n"
"s_mov_b32 s95, 0xcccccccc\n"
"v_mfma_f32_16x16x32_bf16 v[192:195], v[72:75], v[132:135], v[192:195]\n"
"s_add_u32 s98, s70, 0x200000\n"
"v_cndmask_b32_e64 v121, v124, v126, s[94:95]\n"
"v_mfma_f32_16x16x32_bf16 v[196:199], v[76:79], v[132:135], v[196:199]\n"
"buffer_load_dwordx4 a[128:131], v0, s[4:7], s98 offen sc1\n"
"v_mfma_f32_16x16x32_bf16 v[200:203], v[80:83], v[132:135], v[200:203]\n"
"buffer_load_dwordx4 v[96:99], v1, s[4:7], s97 offen offset:3072 sc1\n"
"v_mfma_f32_16x16x32_bf16 v[208:211], v[84:87], v[132:135], v[208:211]\n"
"v_cmp_u_f32_e32 vcc, v184, v184\n"
"s_waitcnt vmcnt(22)\n"
"; @B G1 0.1\n"
"v_mfma_f32_16x16x32_bf16 v[124:127], a[0:3], v[144:147], 0\n"
"s_cbranch_vccnz .Lmk_p50s\n"
".Lmk_p50r:\n"
"s_add_u32 s97, s63, 0x40000\n"
"v_mfma_f32_16x16x32_bf16 v[128:131], a[4:7], v[144:147], 0\n"
"buffer_store_dwordx2 v[120:121], v14, s[4:7], s97 offen sc1\n"
"v_mfma_f32_16x16x32_bf16 v[120:123], a[8:11], v[144:147], 0\n"
"v_mfma_f32_16x16x32_bf16 v[132:135], a[12:15], v[144:147], 0\n"
"ds_write_b128 v3, v[184:187]\n"
"v_mfma_f32_16x16x32_bf16 v[136:139], a[16:19], v[144:147], 0\n"
"s_add_u32 s97, s55, 0x440000\n"
"v_mfma_f32_16x16x32_bf16 v[140:143], a[20:23], v[144:147], 0\n"
"buffer_load_dwordx4 v[144:147], v0, s[4:7], s97 offen sc1\n"
"v_mfma_f32_16x16x32_bf16 v[124:127], a[24:27], v[156:159], v[124:127]\n"
"ds_write_b128 v3, v[188:191] offset:1024\n"
"v_mfma_f32_16x16x32_bf16 v[128:131], a[28:31], v[156:159], v[128:131]\n"
"v_mfma_f32_16x16x32_bf16 v[120:123], a[32:35], v[156:159], v[120:123]\n"
"v_mfma_f32_16x16x32_bf16 v[132:135], a[36:39], v[156:159], v[132:135]\n"
"ds_write_b128 v3, v[192:195] offset:2048\n"
"v_mfma_f32_16x16x32_bf16 v[136:139], a[40:43], v[156:159], v[136:139]\n"
"buffer_load_dwordx4 v[148:151], v0, s[4:7], s97 offen offset:1024 sc1\n"
"v_mfma_f32_16x16x32_bf16 v[140:143], a[44:47], v[156:159], v[140:143]\n"
"s_waitcnt vmcnt(23)\n"
"v_mfma_f32_16x16x32_bf16 v[124:127], a[48:51], v[160:163], v[124:127]\n"
"ds_write_b128 v3, v[196:199] offset:3072\n"
"v_mfma_f32_16x16x32_bf16 v[128:131], a[52:55], v[160:163], v[128:131]\n"
"v_mfma_f32_16x16x32_bf16 v[120:123], a[56:59], v[160:163], v[120:123]\n"
"v_mfma_f32_16x16x32_bf16 v[132:135], a[60:63], v[160:163], v[132:135]\n"
"ds_write_b128 v3, v[200:203] offset:4096\n"
"v_mfma_f32_16x16x32_bf16 v[136:139], a[64:67], v[160:163], v[136:139]\n"
"buffer_load_dwordx4 v[152:155], v0, s[4:7], s97 offen offset:2048 sc1\n"
"v_mfma_f32_16x16x32_bf16 v[140:143], a[68:71], v[160:163], v[140:143]\n"
"v_mfma_f32_16x16x32_bf16 v[124:127], a[72:75], v[164:167], v[124:127]\n"
"ds_write_b128 v3, v[208:211] offset:5120\n"
"v_mfma_f32_16x16x32_bf16 v[128:131], a[76:79], v[164:167], v[128:131]\n"
"s_waitcnt vmcnt(15)\n"
"v_add_f32_e32 v254, v254, v252\n"
"v_mfma_f32_16x16x32_bf16 v[120:123], a[80:83], v[164:167], v[120:123]\n"
"v_add_f32_e32 v255, v255, v253\n"
"v_add_f32_e32 v250, v250, v248\n"
"v_mfma_f32_16x16x32_bf16 v[132:135], a[84:87], v[164:167], v[132:135]\n"
"buffer_load_dwordx4 a[132:135], v0, s[4:7], s98 offen offset:1024 sc1\n"
"v_mfma_f32_16x16x32_bf16 v[136:139], a[88:91], v[164:167], v[136:139]\n"
"buffer_load_dwordx4 v[156:159], v0, s[4:7], s97 offen offset:3072 sc1\n"
"v_mfma_f32_16x16x32_bf16 v[140:143], a[92:95], v[164:167], v[140:143]\n"
"v_add_f32_e32 v251, v251, v249\n"
"v_mfma_f32_16x16x32_bf16 v[124:127], a[96:99], v[168:171], v[124:127]\n"
"v_add_f32_e32 v254, v254, v250\n"
"v_add_f32_e32 v255, v255, v251\n"
"v_mfma_f32_16x16x32_bf16 v[128:131], a[100:103], v[168:171], v[128:131]\n"
"v_cmp_u_f32_e32 vcc, v254, v255\n"
"v_mfma_f32_16x16x32_bf16 v[120:123], a[104:107], v[168:171], v[120:123]\n"
"s_cbranch_vccnz .Lmk_m55s\n"
".Lmk_m55r:\n"
"v_mfma_f32_16x16x32_bf16 v[132:135], a[108:111], v[168:171], v[132:135]\n"
"v_permlane16_swap_b32_e32 v254, v255\n"
"v_add_f32_e32 v254, v254, v255\n"
"v_mfma_f32_16x16x32_bf16 v[136:139], a[112:115], v[168:171], v[136:139]\n"
"buffer_load_dwordx4 v[160:163], v1, s[4:7], s97 offen sc1\n"
"v_mfma_f32_16x16x32_bf16 v[140:143], a[116:119], v[168:171], v[140:143]\n"
"s_waitcnt lgkmcnt(0)\n"
"s_barrier\n"
"v_mfma_f32_16x16x32_bf16 v[124:127], a[120:123], v[172:175], v[124:127]\n"
"v_mov_b32_e32 v255, v254\n"
"v_mfma_f32_16x16x32_bf16 v[128:131], a[124:127], v[172:175], v[128:131]\n"
"s_nop 0\n"
"v_permlane32_swap_b32_e32 v254, v255\n"
"v_add_f32_e32 v254, v254, v255\n"
"v_mfma_f32_16x16x32_bf16 v[120:123], v[24:27], v[172:175], v[120:123]\n"
"v_mul_f32_e32 v254, 0x3a800000, v254\n"
"v_mov_b32_e32 v255, v254\n"
"v_mfma_f32_16x16x32_bf16 v[132:135], v[28:31], v[172:175], v[132:135]\n"
"s_nop 0\n"
"v_permlane16_swap_b32_e32 v254, v255\n"
"v_cvt_pk_bf16_f32 v253, -v254, 0\n"
"v_mfma_f32_16x16x32_bf16 v[136:139], v[32:35], v[172:175], v[136:139]\n"
"buffer_load_dwordx4 v[164:167], v1, s[4:7], s97 offen offset:1024 sc1\n"
"v_mfma_f32_16x16x32_bf16 v[140:143], v[36:39], v[172:175], v[140:143]\n"
"v_fma_f32 v255, -v254, v254, v255\n"
"v_mfma_f32_16x16x32_bf16 v[124:127], v[40:43], v[176:179], v[124:127]\n"
"v_lshlrev_b32_e32 v253, 16, v253\n"
"ds_read_b128 v[168:171], v4\n"
"v_mfma_f32_16x16x32_bf16 v[128:131], v[44:47], v[176:179], v[128:131]\n"
"ds_read_b128 v[172:175], v9 offset:512\n"
"ds_read_b128 v[184:187], v4 offset:1024\n"
"v_mfma_f32_16x16x32_bf16 v[120:123], v[48:51], v[176:179], v[120:123]\n"
"ds_read_b128 v[188:191], v9 offset:576\n"
"ds_read_b128 v[192:195], v4 offset:2048\n"
"v_mfma_f32_16x16x32_bf16 v[132:135], v[52:55], v[176:179], v[132:135]\n"
"ds_read_b128 v[196:199], v9 offset:640\n"
"v_max_f32_e32 v255, 0, v255\n"
"v_mfma_f32_16x16x32_bf16 v[136:139], v[56:59], v[176:179], v[136:139]\n"
"buffer_load_dwordx4 v[200:203], v1, s[4:7], s97 offen offset:2048 sc1\n"
"v_mfma_f32_16x16x32_bf16 v[140:143], v[60:63], v[176:179], v[140:143]\n"
"v_cvt_pk_bf16_f32 v255, v255, 0\n"
"v_lshlrev_b32_e32 v255, 16, v255\n"
"v_mfma_f32_16x16x32_bf16 v[124:127], v[64:67], v[180:183], v[124:127]\n"
"v_add_f32_e32 v255, 0x3727c5ac, v255\n"
"v_cvt_pk_bf16_f32 v255, v255, 0\n"
"v_mfma_f32_16x16x32_bf16 v[128:131], v[68:71], v[180:183], v[128:131]\n"
"v_lshlrev_b32_e32 v255, 16, v255\n"
"v_mfma_f32_16x16x32_bf16 v[120:123], v[72:75], v[180:183], v[120:123]\n"
"s_waitcnt lgkmcnt(0)\n"
"v_fmac_f32_e32 v168, v253, v172\n"
"v_mfma_f32_16x16x32_bf16 v[132:135], v[76:79], v[180:183], v[132:135]\n"
"buffer_load_dwordx4 a[136:139], v0, s[4:7], s98 offen offset:2048 sc1\n"
"v_mfma_f32_16x16x32_bf16 v[136:139], v[80:83], v[180:183], v[136:139]\n"
"buffer_load_dwordx4 v[176:179], v1, s[4:7], s97 offen offset:3072 sc1\n"
"v_mfma_f32_16x16x32_bf16 v[140:143], v[84:87], v[180:183], v[140:143]\n"
"v_cmp_u_f32_e32 vcc, v124, v124\n"
"s_waitcnt vmcnt(21)\n"
"; @B G1 0.2\n"
"v_mfma_f32_16x16x32_bf16 v[180:183], a[0:3], v[104:107], 0\n"
"s_cbranch_vccnz .Lmk_p51s\n"
".Lmk_p51r:\n"
"v_fmac_f32_e32 v169, v253, v173\n"
"v_mfma_f32_16x16x32_bf16 v[204:207], a[4:7], v[104:107], 0\n"
"v_fmac_f32_e32 v170, v253, v174\n"
"v_fmac_f32_e32 v171, v253, v175\n"
"v_mfma_f32_16x16x32_bf16 v[172:175], a[8:11], v[104:107], 0\n"
"v_fmac_f32_e32 v184, v253, v188\n"
"v_fmac_f32_e32 v185, v253, v189\n"
"v_mfma_f32_16x16x32_bf16 v[208:211], a[12:15], v[104:107], 0\n"
"ds_write_b128 v3, v[124:127] offset:6144\n"
"v_mfma_f32_16x16x32_bf16 v[124:127], a[16:19], v[104:107], 0\n"
"s_add_u32 s97, s52, 0x448000\n"
"s_mul_i32 s96, s0, .Lmk_exit-.Lmk_b2\n"
"s_add_u32 s96, s96, .Lmk_b2-.Lmk_g49\n"
"s_getpc_b64 s[84:85]\n"
".Lmk_g49:\n"
"v_mfma_f32_16x16x32_bf16 v[216:219], a[20:23], v[104:107], 0\n"
"buffer_load_dwordx4 v[104:107], v0, s[4:7], s97 offen sc1\n"
"s_add_u32 s84, s84, s96\n"
"s_addc_u32 s85, s85, 0\n"
"v_mfma_f32_16x16x32_bf16 v[180:183], a[24:27], v[212:215], v[180:183]\n"
"ds_write_b128 v3, v[128:131] offset:7168\n"
"v_mfma_f32_16x16x32_bf16 v[204:207], a[28:31], v[212:215], v[204:207]\n"
"s_and_b32 s84, s84, -16\n"
"s_and_b32 s85, s85, 0xffff\n"
"s_sub_u32 s96, .Lmk_end-.Lmk_g49, s96\n"
"s_min_u32 s86, s96, 0x4000\n"
"v_mfma_f32_16x16x32_bf16 v[172:175], a[32:35], v[212:215], v[172:175]\n"
"s_mov_b32 s87, 0x20000\n"
"v_fmac_f32_e32 v186, v253, v190\n"
"v_mfma_f32_16x16x32_bf16 v[208:211], a[36:39], v[212:215], v[208:211]\n"
"ds_write_b128 v3, v[120:123] offset:8192\n"
"v_mfma_f32_16x16x32_bf16 v[124:127], a[40:43], v[212:215], v[124:127]\n"
"buffer_load_dwordx4 v[120:123], v0, s[4:7], s97 offen offset:1024 sc1\n"
"v_mfma_f32_16x16x32_bf16 v[216:219], a[44:47], v[212:215], v[216:219]\n"
"s_waitcnt vmcnt(20)\n"
"s_add_u32 s96, s60, 0x440000\n"
"v_mfma_f32_16x16x32_bf16 v[180:183], a[48:51], v[92:95], v[180:183]\n"
"ds_write_b128 v3, v[132:135] offset:9216\n"
"v_mfma_f32_16x16x32_bf16 v[204:207], a[52:55], v[92:95], v[204:207]\n"
"buffer_store_dwordx4 v[112:115], v0, s[4:7], s96 offen sc1\n"
"v_mfma_f32_16x16x32_bf16 v[172:175], a[56:59], v[92:95], v[172:175]\n"
"s_mov_b32 m0, 0x27800\n"
"s_nop 0\n"
"buffer_load_dwordx4 v0, s[84:87], s99 offen lds\n"
"v_mfma_f32_16x16x32_bf16 v[208:211], a[60:63], v[92:95], v[208:211]\n"
"ds_write_b128 v3, v[136:139] offset:10240\n"
"v_mfma_f32_16x16x32_bf16 v[124:127], a[64:67], v[92:95], v[124:127]\n"
"buffer_load_dwordx4 v[112:115], v0, s[4:7], s97 offen offset:2048 sc1\n"
"v_mfma_f32_16x16x32_bf16 v[216:219], a[68:71], v[92:95], v[216:219]\n"
"v_fmac_f32_e32 v187, v253, v191\n"
"v_fmac_f32_e32 v192, v253, v196\n"
"v_mfma_f32_16x16x32_bf16 v[180:183], a[72:75], v[108:111], v[180:183]\n"
"ds_write_b128 v3, v[140:143] offset:11264\n"
"v_mfma_f32_16x16x32_bf16 v[204:207], a[76:79], v[108:111], v[204:207]\n"
"v_fmac_f32_e32 v193, v253, v197\n"
"v_fmac_f32_e32 v194, v253, v198\n"
"v_mfma_f32_16x16x32_bf16 v[172:175], a[80:83], v[108:111], v[172:175]\n"
"v_fmac_f32_e32 v195, v253, v199\n"
"ds_read_b128 v[92:95], v4 offset:3072\n"
"v_mfma_f32_16x16x32_bf16 v[208:211], a[84:87], v[108:111], v[208:211]\n"
"buffer_load_dwordx4 a[140:143], v0, s[4:7], s98 offen offset:3072 sc1\n"
"v_mfma_f32_16x16x32_bf16 v[124:127], a[88:91], v[108:111], v[124:127]\n"
"buffer_load_dwordx4 v[128:131], v0, s[4:7], s97 offen offset:3072 sc1\n"
"v_mfma_f32_16x16x32_bf16 v[216:219], a[92:95], v[108:111], v[216:219]\n"
"s_waitcnt vmcnt(22)\n"
"ds_read_b128 v[108:111], v9 offset:704\n"
"v_mfma_f32_16x16x32_bf16 v[180:183], a[96:99], v[100:103], v[180:183]\n"
"ds_read_b128 v[132:135], v4 offset:4096\n"
"ds_read_b128 v[136:139], v9 offset:768\n"
"v_mfma_f32_16x16x32_bf16 v[204:207], a[100:103], v[100:103], v[204:207]\n"
"ds_read_b128 v[140:143], v4 offset:5120\n"
"ds_read_b128 v[188:191], v9 offset:832\n"
"v_mfma_f32_16x16x32_bf16 v[172:175], a[104:107], v[100:103], v[172:175]\n"
"v_sqrt_f32_e32 v255, v255\n"
"v_mfma_f32_16x16x32_bf16 v[208:211], a[108:111], v[100:103], v[208:211]\n"
"v_cvt_pk_bf16_f32 v255, v255, 0\n"
"v_lshlrev_b32_e32 v255, 16, v255\n"
"v_mfma_f32_16x16x32_bf16 v[124:127], a[112:115], v[100:103], v[124:127]\n"
"buffer_load_dwordx4 v[196:199], v1, s[4:7], s97 offen sc1\n"
"v_mfma_f32_16x16x32_bf16 v[216:219], a[116:119], v[100:103], v[216:219]\n"
"v_rcp_f32_e32 v252, v255\n"
"v_mfma_f32_16x16x32_bf16 v[180:183], a[120:123], v[116:119], v[180:183]\n"
"s_waitcnt lgkmcnt(0)\n"
"v_fmac_f32_e32 v92, v253, v108\n"
"v_mfma_f32_16x16x32_bf16 v[204:207], a[124:127], v[116:119], v[204:207]\n"
"v_fmac_f32_e32 v93, v253, v109\n"
"v_fmac_f32_e32 v94, v253, v110\n"
"v_mfma_f32_16x16x32_bf16 v[172:175], v[24:27], v[116:119], v[172:175]\n"
"v_fmac_f32_e32 v95, v253, v111\n"
"v_fmac_f32_e32 v132, v253, v136\n"
"v_mfma_f32_16x16x32_bf16 v[208:211], v[28:31], v[116:119], v[208:211]\n"
"v_fmac_f32_e32 v133, v253, v137\n"
"v_fmac_f32_e32 v134, v253, v138\n"
"v_mfma_f32_16x16x32_bf16 v[124:127], v[32:35], v[116:119], v[124:127]\n"
"buffer_load_dwordx4 v[100:103], v1, s[4:7], s97 offen offset:1024 sc1\n"
"v_mfma_f32_16x16x32_bf16 v[216:219], v[36:39], v[116:119], v[216:219]\n"
"s_waitcnt vmcnt(20)\n"
"v_fmac_f32_e32 v135, v253, v139\n"
"v_mfma_f32_16x16x32_bf16 v[180:183], v[40:43], v[88:91], v[180:183]\n"
"v_fmac_f32_e32 v140, v253, v188\n"
"v_fmac_f32_e32 v141, v253, v189\n"
"v_mfma_f32_16x16x32_bf16 v[204:207], v[44:47], v[88:91], v[204:207]\n"
"v_fmac_f32_e32 v142, v253, v190\n"
"v_fmac_f32_e32 v143, v253, v191\n"
"v_mfma_f32_16x16x32_bf16 v[172:175], v[48:51], v[88:91], v[172:175]\n"
"v_mul_f32_e32 v255, 0x3e8293ee, v252\n"
"v_mfma_f32_16x16x32_bf16 v[208:211], v[52:55], v[88:91], v[208:211]\n"
"v_mfma_f32_16x16x32_bf16 v[124:127], v[56:59], v[88:91], v[124:127]\n"
"s_waitcnt vmcnt(17)\n"
"v_max_u32_e32 v254, v144, v145\n"
"v_mfma_f32_16x16x32_bf16 v[216:219], v[60:63], v[88:91], v[216:219]\n"
"v_max3_u32 v254, v254, v146, v147\n"
"v_max3_u32 v254, v254, v148, v149\n"
"v_mfma_f32_16x16x32_bf16 v[180:183], v[64:67], v[96:99], v[180:183]\n"
"v_max3_u32 v254, v254, v150, v151\n"
"s_waitcnt vmcnt(14)\n"
"v_mfma_f32_16x16x32_bf16 v[204:207], v[68:71], v[96:99], v[204:207]\n"
"v_max3_u32 v254, v254, v152, v153\n"
"v_max3_u32 v254, v254, v154, v155\n"
"v_mfma_f32_16x16x32_bf16 v[172:175], v[72:75], v[96:99], v[172:175]\n"
"v_max3_u32 v254, v254, v156, v157\n"
"v_max3_u32 v254, v254, v158, v159\n"
"v_mfma_f32_16x16x32_bf16 v[208:211], v[76:79], v[96:99], v[208:211]\n"
"v_cmp_eq_u32_e32 vcc, -1, v254\n"
"s_add_u32 s96, s68, s80\n"
"v_mfma_f32_16x16x32_bf16 v[124:127], v[80:83], v[96:99], v[124:127]\n"
"s_cbranch_vccnz .Lmk_q53s\n"
".Lmk_q53r:\n"
"s_add_u32 s96, s96, 0x10000\n"
"v_mfma_f32_16x16x32_bf16 v[216:219], v[84:87], v[96:99], v[216:219]\n"
"v_cmp_u_f32_e32 vcc, v180, v180\n"
"; @B G1 0.3\n"
"v_mfma_f32_16x16x32_bf16 v[168:171], a[0:3], v[144:147], v[168:171]\n"
"s_cbranch_vccnz .Lmk_p52s\n"
".Lmk_p52r:\n"
"v_mfma_f32_16x16x32_bf16 v[184:187], a[4:7], v[144:147], v[184:187]\n"
"buffer_load_dwordx4 v[88:91], v1, s[4:7], s97 offen offset:2048 sc1\n"
"v_mfma_f32_16x16x32_bf16 v[192:195], a[8:11], v[144:147], v[192:195]\n"
"buffer_load_dwordx4 v[96:99], v1, s[4:7], s97 offen offset:3072 sc1\n"
"v_mfma_f32_16x16x32_bf16 v[92:95], a[12:15], v[144:147], v[92:95]\n"
"ds_write_b128 v3, v[180:183] offset:12288\n"
"v_mfma_f32_16x16x32_bf16 v[132:135], a[16:19], v[144:147], v[132:135]\n"
"s_add_u32 s97, s53, 0x448000\n"
"v_mfma_f32_16x16x32_bf16 v[140:143], a[20:23], v[144:147], v[140:143]\n"
"buffer_load_dwordx4 v[108:111], v0, s[4:7], s97 offen sc1\n"
"v_mfma_f32_16x16x32_bf16 v[168:171], a[24:27], v[148:151], v[168:171]\n"
"ds_write_b128 v3, v[204:207] offset:13312\n"
"v_mfma_f32_16x16x32_bf16 v[184:187], a[28:31], v[148:151], v[184:187]\n"
"buffer_store_dwordx2 v[18:19], v11, s[4:7], s96 offen sc1\n"
"v_mfma_f32_16x16x32_bf16 v[192:195], a[32:35], v[148:151], v[192:195]\n"
"buffer_load_dwordx4 a[144:147], v1, s[4:7], s98 offen sc1\n"
"v_mfma_f32_16x16x32_bf16 v[92:95], a[36:39], v[148:151], v[92:95]\n"
"ds_write_b128 v3, v[172:175] offset:14336\n"
"v_mfma_f32_16x16x32_bf16 v[132:135], a[40:43], v[148:151], v[132:135]\n"
"buffer_load_dwordx4 v[116:119], v0, s[4:7], s97 offen offset:1024 sc1\n"
"v_mfma_f32_16x16x32_bf16 v[140:143], a[44:47], v[148:151], v[140:143]\n"
"v_mfma_f32_16x16x32_bf16 v[168:171], a[48:51], v[152:155], v[168:171]\n"
"ds_write_b128 v3, v[208:211] offset:15360\n"
"v_mfma_f32_16x16x32_bf16 v[184:187], a[52:55], v[152:155], v[184:187]\n"
"v_mfma_f32_16x16x32_bf16 v[192:195], a[56:59], v[152:155], v[192:195]\n"
"v_mfma_f32_16x16x32_bf16 v[92:95], a[60:63], v[152:155], v[92:95]\n"
"ds_write_b128 v3, v[124:127] offset:16384\n"
"v_mfma_f32_16x16x32_bf16 v[132:135], a[64:67], v[152:155], v[132:135]\n"
"s_waitcnt vmcnt(18)\n"
"v_max_u32_e32 v254, v160, v161\n"
"v_mfma_f32_16x16x32_bf16 v[140:143], a[68:71], v[152:155], v[140:143]\n"
"v_max3_u32 v254, v254, v162, v163\n"
"v_max3_u32 v254, v254, v164, v165\n"
"v_mfma_f32_16x16x32_bf16 v[168:171], a[72:75], v[156:159], v[168:171]\n"
"v_max3_u32 v254, v254, v166, v167\n"
"s_waitcnt vmcnt(15)\n"
"v_mfma_f32_16x16x32_bf16 v[184:187], a[76:79], v[156:159], v[184:187]\n"
"v_max3_u32 v254, v254, v200, v201\n"
"v_max3_u32 v254, v254, v202, v203\n"
"v_mfma_f32_16x16x32_bf16 v[192:195], a[80:83], v[156:159], v[192:195]\n"
"v_max3_u32 v254, v254, v176, v177\n"
"v_max3_u32 v254, v254, v178, v179\n"
"v_mfma_f32_16x16x32_bf16 v[92:95], a[84:87], v[156:159], v[92:95]\n"
"v_cmp_eq_u32_e32 vcc, -1, v254\n"
"v_mfma_f32_16x16x32_bf16 v[132:135], a[88:91], v[156:159], v[132:135]\n"
"s_cbranch_vccnz .Lmk_q54s\n"
".Lmk_q54r:\n"
"v_mfma_f32_16x16x32_bf16 v[140:143], a[92:95], v[156:159], v[140:143]\n"
"v_mfma_f32_16x16x32_bf16 v[168:171], a[96:99], v[160:163], v[168:171]\n"
"buffer_load_dwordx4 v[124:127], v0, s[4:7], s97 offen offset:2048 sc1\n"
"v_mfma_f32_16x16x32_bf16 v[184:187], a[100:103], v[160:163], v[184:187]\n"
"buffer_load_dwordx4 v[136:139], v0, s[4:7], s97 offen offset:3072 sc1\n"
"v_mfma_f32_16x16x32_bf16 v[192:195], a[104:107], v[160:163], v[192:195]\n"
"ds_write_b128 v3, v[216:219] offset:17408\n"
"v_mfma_f32_16x16x32_bf16 v[92:95], a[108:111], v[160:163], v[92:95]\n"
"v_mfma_f32_16x16x32_bf16 v[132:135], a[112:115], v[160:163], v[132:135]\n"
"buffer_load_dwordx4 v[144:147], v1, s[4:7], s97 offen sc1\n"
"v_mfma_f32_16x16x32_bf16 v[140:143], a[116:119], v[160:163], v[140:143]\n"
"s_waitcnt lgkmcnt(0)\n"
"s_barrier\n"
"v_mfma_f32_16x16x32_bf16 v[168:171], a[120:123], v[164:167], v[168:171]\n"
"ds_read_b128 v[148:151], v5 offset:2048\n"
"ds_read_b128 v[152:155], v6 offset:2048\n"
"v_mfma_f32_16x16x32_bf16 v[184:187], a[124:127], v[164:167], v[184:187]\n"
"ds_read_b128 v[156:159], v5 offset:3072\n"
"ds_read_b128 v[160:163], v6 offset:3072\n"
"v_mfma_f32_16x16x32_bf16 v[192:195], v[24:27], v[164:167], v[192:195]\n"
"buffer_load_dwordx4 a[148:151], v1, s[4:7], s98 offen offset:1024 sc1\n"
"v_mfma_f32_16x16x32_bf16 v[92:95], v[28:31], v[164:167], v[92:95]\n"
"v_mfma_f32_16x16x32_bf16 v[132:135], v[32:35], v[164:167], v[132:135]\n"
"buffer_load_dwordx4 v[172:175], v1, s[4:7], s97 offen offset:1024 sc1\n"
"v_mfma_f32_16x16x32_bf16 v[140:143], v[36:39], v[164:167], v[140:143]\n"
"ds_read_b128 v[164:167], v5 offset:4096\n"
"v_mfma_f32_16x16x32_bf16 v[168:171], v[40:43], v[200:203], v[168:171]\n"
"ds_read_b128 v[180:183], v6 offset:4096\n"
"ds_read_b128 v[188:191], v5 offset:5120\n"
"v_mfma_f32_16x16x32_bf16 v[184:187], v[44:47], v[200:203], v[184:187]\n"
"ds_read_b128 v[204:207], v6 offset:5120\n"
"s_waitcnt lgkmcnt(4)\n"
"v_mfma_f32_16x16x32_bf16 v[192:195], v[48:51], v[200:203], v[192:195]\n"
"v_add_f32_e32 v148, v148, v152\n"
"v_add_f32_e32 v149, v149, v153\n"
"v_mfma_f32_16x16x32_bf16 v[92:95], v[52:55], v[200:203], v[92:95]\n"
"v_add_f32_e32 v150, v150, v154\n"
"v_add_f32_e32 v151, v151, v155\n"
"v_mfma_f32_16x16x32_bf16 v[132:135], v[56:59], v[200:203], v[132:135]\n"
"buffer_load_dwordx4 v[152:155], v1, s[4:7], s97 offen offset:2048 sc1\n"
"v_mfma_f32_16x16x32_bf16 v[140:143], v[60:63], v[200:203], v[140:143]\n"
"ds_read_b128 v[200:203], v5\n"
"ds_read_b128 v[208:211], v6\n"
"v_mfma_f32_16x16x32_bf16 v[168:171], v[64:67], v[176:179], v[168:171]\n"
"ds_read_b128 v[212:215], v5 offset:1024\n"
"ds_read_b128 v[216:219], v6 offset:1024\n"
"v_mfma_f32_16x16x32_bf16 v[184:187], v[68:71], v[176:179], v[184:187]\n"
"v_add_f32_e32 v156, v156, v160\n"
"v_add_f32_e32 v157, v157, v161\n"
"v_mfma_f32_16x16x32_bf16 v[192:195], v[72:75], v[176:179], v[192:195]\n"
"v_add_f32_e32 v158, v158, v162\n"
"v_add_f32_e32 v159, v159, v163\n"
"v_mfma_f32_16x16x32_bf16 v[92:95], v[76:79], v[176:179], v[92:95]\n"
"s_waitcnt lgkmcnt(4)\n"
"v_add_f32_e32 v164, v164, v180\n"
"v_mfma_f32_16x16x32_bf16 v[132:135], v[80:83], v[176:179], v[132:135]\n"
"buffer_load_dwordx4 v[160:163], v1, s[4:7], s97 offen offset:3072 sc1\n"
"v_mfma_f32_16x16x32_bf16 v[140:143], v[84:87], v[176:179], v[140:143]\n"
"v_add_f32_e32 v165, v165, v181\n"
"v_add_f32_e32 v166, v166, v182\n"
"v_add_f32_e32 v167, v167, v183\n"
"v_add_f32_e32 v188, v188, v204\n"
"v_add_f32_e32 v189, v189, v205\n"
"v_add_f32_e32 v190, v190, v206\n"
"v_add_f32_e32 v191, v191, v207\n"
"s_waitcnt lgkmcnt(0)\n"
"v_add_f32_e32 v200, v200, v208\n"
"v_add_f32_e32 v201, v201, v209\n"
"v_add_f32_e32 v202, v202, v210\n"
"v_add_f32_e32 v203, v203, v211\n"
"v_add_f32_e32 v212, v212, v216\n"
"v_add_f32_e32 v213, v213, v217\n"
"v_add_f32_e32 v214, v214, v218\n"
"v_add_f32_e32 v215, v215, v219\n"
".Lmk_b1:\n"
"s_waitcnt vmcnt(20)\n"
"; @B G1 1.0\n"
"v_mfma_f32_16x16x32_bf16 v[176:179], a[0:3], v[104:107], 0\n"
"s_add_u32 s97, s62, 0x44800\n"
"v_mfma_f32_16x16x32_bf16 v[180:183], a[4:7], v[104:107], 0\n"
"buffer_load_dwordx2 v[250:251], v10, s[4:7], s97 offen sc1\n"
"v_mfma_f32_16x16x32_bf16 v[204:207], a[8:11], v[104:107], 0\n"
"buffer_load_dwordx2 v[248:249], v10, s[4:7], s97 offen offset:128 sc1\n"
"v_mfma_f32_16x16x32_bf16 v[208:211], a[12:15], v[104:107], 0\n"
"buffer_load_dwordx2 v[246:247], v10, s[4:7], s97 offen offset:256 sc1\n"
"v_mfma_f32_16x16x32_bf16 v[216:219], a[16:19], v[104:107], 0\n"
"s_add_u32 s96, s54, 0x448000\n"
"v_add_f32_e32 v192, v192, v148\n"
"v_mfma_f32_16x16x32_bf16 v[220:223], a[20:23], v[104:107], 0\n"
"buffer_load_dwordx4 v[104:107], v0, s[4:7], s96 offen sc1\n"
"v_mfma_f32_16x16x32_bf16 v[176:179], a[24:27], v[120:123], v[176:179]\n"
"buffer_load_dwordx2 v[244:245], v10, s[4:7], s97 offen offset:384 sc1\n"
"v_mfma_f32_16x16x32_bf16 v[180:183], a[28:31], v[120:123], v[180:183]\n"
"v_add_f32_e32 v193, v193, v149\n"
"v_add_f32_e32 v194, v194, v150\n"
"v_mfma_f32_16x16x32_bf16 v[204:207], a[32:35], v[120:123], v[204:207]\n"
"buffer_load_dwordx4 a[152:155], v1, s[4:7], s98 offen offset:2048 sc1\n"
"v_mfma_f32_16x16x32_bf16 v[208:211], a[36:39], v[120:123], v[208:211]\n"
"v_add_f32_e32 v195, v195, v151\n"
"v_add_f32_e32 v92, v92, v156\n"
"v_mfma_f32_16x16x32_bf16 v[216:219], a[40:43], v[120:123], v[216:219]\n"
"buffer_load_dwordx4 v[148:151], v0, s[4:7], s96 offen offset:1024 sc1\n"
"v_mfma_f32_16x16x32_bf16 v[220:223], a[44:47], v[120:123], v[220:223]\n"
"s_waitcnt vmcnt(22)\n"
"v_add_f32_e32 v93, v93, v157\n"
"v_mfma_f32_16x16x32_bf16 v[176:179], a[48:51], v[112:115], v[176:179]\n"
"v_add_f32_e32 v94, v94, v158\n"
"v_add_f32_e32 v95, v95, v159\n"
"v_mfma_f32_16x16x32_bf16 v[180:183], a[52:55], v[112:115], v[180:183]\n"
"v_mul_f32_e32 v192, v192, v252\n"
"v_mul_f32_e32 v193, v193, v252\n"
"v_mfma_f32_16x16x32_bf16 v[204:207], a[56:59], v[112:115], v[204:207]\n"
"v_mul_f32_e32 v194, v194, v252\n"
"v_mul_f32_e32 v195, v195, v252\n"
"v_mfma_f32_16x16x32_bf16 v[208:211], a[60:63], v[112:115], v[208:211]\n"
"v_mul_f32_e32 v92, v92, v252\n"
"v_mul_f32_e32 v93, v93, v252\n"
"v_mfma_f32_16x16x32_bf16 v[216:219], a[64:67], v[112:115], v[216:219]\n"
"buffer_load_dwordx4 v[120:123], v0, s[4:7], s96 offen offset:2048 sc1\n"
"v_mfma_f32_16x16x32_bf16 v[220:223], a[68:71], v[112:115], v[220:223]\n"
"v_mul_f32_e32 v94, v94, v252\n"
"v_mul_f32_e32 v95, v95, v252\n"
"v_mfma_f32_16x16x32_bf16 v[176:179], a[72:75], v[128:131], v[176:179]\n"
"v_cvt_pk_bf16_f32 v112, v192, v193\n"
"v_cvt_pk_bf16_f32 v113, v194, v195\n"
"v_mfma_f32_16x16x32_bf16 v[180:183], a[76:79], v[128:131], v[180:183]\n"
"v_cvt_pk_bf16_f32 v114, v92, v93\n"
"v_cvt_pk_bf16_f32 v115, v94, v95\n"
"v_mfma_f32_16x16x32_bf16 v[204:207], a[80:83], v[128:131], v[204:207]\n"
"ds_write_b128 v7, v[112:115] offset:24576\n"
"v_mfma_f32_16x16x32_bf16 v[208:211], a[84:87], v[128:131], v[208:211]\n"
"v_add_f32_e32 v132, v132, v164\n"
"v_add_f32_e32 v133, v133, v165\n"
"v_mfma_f32_16x16x32_bf16 v[216:219], a[88:91], v[128:131], v[216:219]\n"
"buffer_load_dwordx4 v[92:95], v0, s[4:7], s96 offen offset:3072 sc1\n"
"v_mfma_f32_16x16x32_bf16 v[220:223], a[92:95], v[128:131], v[220:223]\n"
"s_waitcnt vmcnt(22)\n"
"v_add_f32_e32 v134, v134, v166\n"
"v_mfma_f32_16x16x32_bf16 v[176:179], a[96:99], v[196:199], v[176:179]\n"
"v_add_f32_e32 v135, v135, v167\n"
"v_add_f32_e32 v140, v140, v188\n"
"v_mfma_f32_16x16x32_bf16 v[180:183], a[100:103], v[196:199], v[180:183]\n"
"v_add_f32_e32 v141, v141, v189\n"
"v_add_f32_e32 v142, v142, v190\n"
"v_mfma_f32_16x16x32_bf16 v[204:207], a[104:107], v[196:199], v[204:207]\n"
"v_add_f32_e32 v143, v143, v191\n"
"v_mul_f32_e32 v132, v132, v252\n"
"v_mfma_f32_16x16x32_bf16 v[208:211], a[108:111], v[196:199], v[208:211]\n"
"v_mul_f32_e32 v133, v133, v252\n"
"v_mul_f32_e32 v134, v134, v252\n"
"v_mfma_f32_16x16x32_bf16 v[216:219], a[112:115], v[196:199], v[216:219]\n"
"buffer_load_dwordx4 v[112:115], v1, s[4:7], s96 offen sc1\n"
"v_mfma_f32_16x16x32_bf16 v[220:223], a[116:119], v[196:199], v[220:223]\n"
"v_mul_f32_e32 v135, v135, v252\n"
"v_mul_f32_e32 v140, v140, v252\n"
"v_mfma_f32_16x16x32_bf16 v[176:179], a[120:123], v[100:103], v[176:179]\n"
"v_mul_f32_e32 v141, v141, v252\n"
"v_mul_f32_e32 v142, v142, v252\n"
"v_mfma_f32_16x16x32_bf16 v[180:183], a[124:127], v[100:103], v[180:183]\n"
"v_mul_f32_e32 v143, v143, v252\n"
"v_cvt_pk_bf16_f32 v128, v132, v133\n"
"v_mfma_f32_16x16x32_bf16 v[204:207], v[24:27], v[100:103], v[204:207]\n"
"buffer_load_dwordx4 a[156:159], v1, s[4:7], s98 offen offset:3072 sc1\n"
"v_mfma_f32_16x16x32_bf16 v[208:211], v[28:31], v[100:103], v[208:211]\n"
"v_cvt_pk_bf16_f32 v129, v134, v135\n"
"v_cvt_pk_bf16_f32 v130, v140, v141\n"
"v_mfma_f32_16x16x32_bf16 v[216:219], v[32:35], v[100:103], v[216:219]\n"
"buffer_load_dwordx4 v[132:135], v1, s[4:7], s96 offen offset:1024 sc1\n"
"v_mfma_f32_16x16x32_bf16 v[220:223], v[36:39], v[100:103], v[220:223]\n"
"s_waitcnt vmcnt(23)\n"
"v_cvt_pk_bf16_f32 v131, v142, v143\n"
"v_mfma_f32_16x16x32_bf16 v[176:179], v[40:43], v[88:91], v[176:179]\n"
"ds_write_b128 v7, v[128:131] offset:57344\n"
"v_mfma_f32_16x16x32_bf16 v[180:183], v[44:47], v[88:91], v[180:183]\n"
"v_add_f32_e32 v168, v168, v200\n"
"v_add_f32_e32 v169, v169, v201\n"
"v_mfma_f32_16x16x32_bf16 v[204:207], v[48:51], v[88:91], v[204:207]\n"
"v_add_f32_e32 v170, v170, v202\n"
"v_add_f32_e32 v171, v171, v203\n"
"v_mfma_f32_16x16x32_bf16 v[208:211], v[52:55], v[88:91], v[208:211]\n"
"v_add_f32_e32 v184, v184, v212\n"
"v_add_f32_e32 v185, v185, v213\n"
"v_mfma_f32_16x16x32_bf16 v[216:219], v[56:59], v[88:91], v[216:219]\n"
"buffer_load_dwordx4 v[100:103], v1, s[4:7], s96 offen offset:2048 sc1\n"
"v_mfma_f32_16x16x32_bf16 v[220:223], v[60:63], v[88:91], v[220:223]\n"
"v_add_f32_e32 v186, v186, v214\n"
"v_add_f32_e32 v187, v187, v215\n"
"v_mfma_f32_16x16x32_bf16 v[176:179], v[64:67], v[96:99], v[176:179]\n"
"v_mul_f32_e32 v168, v168, v255\n"
"v_mul_f32_e32 v169, v169, v255\n"
"v_mfma_f32_16x16x32_bf16 v[180:183], v[68:71], v[96:99], v[180:183]\n"
"v_mul_f32_e32 v170, v170, v255\n"
"v_mul_f32_e32 v171, v171, v255\n"
"v_mfma_f32_16x16x32_bf16 v[204:207], v[72:75], v[96:99], v[204:207]\n"
"v_mul_f32_e32 v184, v184, v255\n"
"v_mul_f32_e32 v185, v185, v255\n"
"v_mfma_f32_16x16x32_bf16 v[208:211], v[76:79], v[96:99], v[208:211]\n"
"v_mul_f32_e32 v186, v186, v255\n"
"v_mul_f32_e32 v187, v187, v255\n"
"v_mfma_f32_16x16x32_bf16 v[216:219], v[80:83], v[96:99], v[216:219]\n"
"buffer_load_dwordx4 v[88:91], v1, s[4:7], s96 offen offset:3072 sc1\n"
"v_mfma_f32_16x16x32_bf16 v[220:223], v[84:87], v[96:99], v[220:223]\n"
"v_cmp_u_f32_e32 vcc, v176, v176\n"
"s_waitcnt vmcnt(21)\n"
"; @B G1 1.1\n"
"v_mfma_f32_16x16x32_bf16 v[96:99], a[0:3], v[108:111], 0\n"
"s_cbranch_vccnz .Lmk_p56s\n"
".Lmk_p56r:\n"
"v_cvt_pk_bf16_f32 v128, v168, v169\n"
"v_mfma_f32_16x16x32_bf16 v[140:143], a[4:7], v[108:111], 0\n"
"v_cvt_pk_bf16_f32 v129, v170, v171\n"
"v_cvt_pk_bf16_f32 v130, v184, v185\n"
"v_mfma_f32_16x16x32_bf16 v[156:159], a[8:11], v[108:111], 0\n"
"v_cvt_pk_bf16_f32 v131, v186, v187\n"
"v_mfma_f32_16x16x32_bf16 v[164:167], a[12:15], v[108:111], 0\n"
"ds_write_b128 v3, v[176:179]\n"
"v_mfma_f32_16x16x32_bf16 v[168:171], a[16:19], v[108:111], 0\n"
"s_add_u32 s98, s55, 0x448000\n"
"v_mfma_f32_16x16x32_bf16 v[176:179], a[20:23], v[108:111], 0\n"
"buffer_load_dwordx4 v[108:111], v0, s[4:7], s98 offen sc1\n"
"v_mfma_f32_16x16x32_bf16 v[96:99], a[24:27], v[116:119], v[96:99]\n"
"ds_write_b128 v3, v[180:183] offset:1024\n"
"v_mfma_f32_16x16x32_bf16 v[140:143], a[28:31], v[116:119], v[140:143]\n"
"s_add_u32 s97, s70, 0x202000\n"
"v_mfma_f32_16x16x32_bf16 v[156:159], a[32:35], v[116:119], v[156:159]\n"
"buffer_load_dwordx4 a[160:163], v0, s[4:7], s97 offen sc1\n"
"v_mfma_f32_16x16x32_bf16 v[164:167], a[36:39], v[116:119], v[164:167]\n"
"ds_write_b128 v3, v[204:207] offset:2048\n"
"v_mfma_f32_16x16x32_bf16 v[168:171], a[40:43], v[116:119], v[168:171]\n"
"buffer_load_dwordx4 v[180:183], v0, s[4:7], s98 offen offset:1024 sc1\n"
"v_mfma_f32_16x16x32_bf16 v[176:179], a[44:47], v[116:119], v[176:179]\n"
"s_waitcnt vmcnt(22)\n"
"v_mfma_f32_16x16x32_bf16 v[96:99], a[48:51], v[124:127], v[96:99]\n"
"ds_write_b128 v3, v[208:211] offset:3072\n"
"v_mfma_f32_16x16x32_bf16 v[140:143], a[52:55], v[124:127], v[140:143]\n"
"s_add_u32 s96, s67, 0x10000\n"
"v_mfma_f32_16x16x32_bf16 v[156:159], a[56:59], v[124:127], v[156:159]\n"
"buffer_load_dwordx2 v[254:255], v12, s[4:7], s96 offen sc1\n"
"v_mfma_f32_16x16x32_bf16 v[164:167], a[60:63], v[124:127], v[164:167]\n"
"ds_write_b128 v3, v[216:219] offset:4096\n"
"v_mfma_f32_16x16x32_bf16 v[168:171], a[64:67], v[124:127], v[168:171]\n"
"buffer_load_dwordx4 v[116:119], v0, s[4:7], s98 offen offset:2048 sc1\n"
"v_mfma_f32_16x16x32_bf16 v[176:179], a[68:71], v[124:127], v[176:179]\n"
"ds_write_b128 v2, v[128:131] offset:49152\n"
"v_mfma_f32_16x16x32_bf16 v[96:99], a[72:75], v[136:139], v[96:99]\n"
"ds_write_b128 v3, v[220:223] offset:5120\n"
"v_mfma_f32_16x16x32_bf16 v[140:143], a[76:79], v[136:139], v[140:143]\n"
"v_mfma_f32_16x16x32_bf16 v[156:159], a[80:83], v[136:139], v[156:159]\n"
"v_mov_b32_e32 v124, -1\n"
"v_mov_b32_e32 v125, -1\n"
"v_mfma_f32_16x16x32_bf16 v[164:167], a[84:87], v[136:139], v[164:167]\n"
"v_mov_b32_e32 v126, -1\n"
"v_mov_b32_e32 v127, -1\n"
"v_mfma_f32_16x16x32_bf16 v[168:171], a[88:91], v[136:139], v[168:171]\n"
"buffer_load_dwordx4 v[128:131], v0, s[4:7], s98 offen offset:3072 sc1\n"
"v_mfma_f32_16x16x32_bf16 v[176:179], a[92:95], v[136:139], v[176:179]\n"
"s_waitcnt vmcnt(22)\n"
"s_add_u32 s95, s60, 0x400000\n"
"v_mfma_f32_16x16x32_bf16 v[96:99], a[96:99], v[144:147], v[96:99]\n"
"buffer_store_dwordx4 v[124:127], v0, s[4:7], s95 offen sc1\n"
"v_mfma_f32_16x16x32_bf16 v[140:143], a[100:103], v[144:147], v[140:143]\n"
"v_mfma_f32_16x16x32_bf16 v[156:159], a[104:107], v[144:147], v[156:159]\n"
"v_mfma_f32_16x16x32_bf16 v[164:167], a[108:111], v[144:147], v[164:167]\n"
"v_mfma_f32_16x16x32_bf16 v[168:171], a[112:115], v[144:147], v[168:171]\n"
"buffer_load_dwordx4 v[136:139], v1, s[4:7], s98 offen sc1\n"
"v_mfma_f32_16x16x32_bf16 v[176:179], a[116:119], v[144:147], v[176:179]\n"
"s_waitcnt lgkmcnt(0)\n"
"s_barrier\n"
"v_mfma_f32_16x16x32_bf16 v[96:99], a[120:123], v[172:175], v[96:99]\n"
"s_waitcnt vmcnt(17)\n"
"v_add_f32_e32 v250, v250, v248\n"
"v_mfma_f32_16x16x32_bf16 v[140:143], a[124:127], v[172:175], v[140:143]\n"
"v_add_f32_e32 v251, v251, v249\n"
"v_add_f32_e32 v246, v246, v244\n"
"v_mfma_f32_16x16x32_bf16 v[156:159], v[24:27], v[172:175], v[156:159]\n"
"buffer_load_dwordx4 a[164:167], v0, s[4:7], s97 offen offset:1024 sc1\n"
"v_mfma_f32_16x16x32_bf16 v[164:167], v[28:31], v[172:175], v[164:167]\n"
"v_add_f32_e32 v247, v247, v245\n"
"v_add_f32_e32 v250, v250, v246\n"
"v_mfma_f32_16x16x32_bf16 v[168:171], v[32:35], v[172:175], v[168:171]\n"
"buffer_load_dwordx4 v[144:147], v1, s[4:7], s98 offen offset:1024 sc1\n"
"v_mfma_f32_16x16x32_bf16 v[176:179], v[36:39], v[172:175], v[176:179]\n"
"v_add_f32_e32 v251, v251, v247\n"
"v_mfma_f32_16x16x32_bf16 v[96:99], v[40:43], v[152:155], v[96:99]\n"
"v_cmp_u_f32_e32 vcc, v250, v251\n"
"v_mfma_f32_16x16x32_bf16 v[140:143], v[44:47], v[152:155], v[140:143]\n"
"s_cbranch_vccnz .Lmk_m61s\n"
".Lmk_m61r:\n"
"v_mfma_f32_16x16x32_bf16 v[156:159], v[48:51], v[152:155], v[156:159]\n"
"v_permlane16_swap_b32_e32 v250, v251\n"
"v_add_f32_e32 v250, v250, v251\n"
"v_mfma_f32_16x16x32_bf16 v[164:167], v[52:55], v[152:155], v[164:167]\n"
"v_mov_b32_e32 v251, v250\n"
"ds_read_b128 v[172:175], v4\n"
"v_mfma_f32_16x16x32_bf16 v[168:171], v[56:59], v[152:155], v[168:171]\n"
"buffer_load_dwordx4 v[184:187], v1, s[4:7], s98 offen offset:2048 sc1\n"
"v_mfma_f32_16x16x32_bf16 v[176:179], v[60:63], v[152:155], v[176:179]\n"
"v_permlane32_swap_b32_e32 v250, v251\n"
"v_add_f32_e32 v250, v250, v251\n"
"v_mfma_f32_16x16x32_bf16 v[96:99], v[64:67], v[160:163], v[96:99]\n"
"v_mul_f32_e32 v250, 0x3a800000, v250\n"
"v_mov_b32_e32 v251, v250\n"
"v_mfma_f32_16x16x32_bf16 v[140:143], v[68:71], v[160:163], v[140:143]\n"
"s_nop 0\n"
"v_permlane16_swap_b32_e32 v250, v251\n"
"v_cvt_pk_bf16_f32 v253, -v250, 0\n"
"v_mfma_f32_16x16x32_bf16 v[156:159], v[72:75], v[160:163], v[156:159]\n"
"v_fma_f32 v251, -v250, v250, v251\n"
"v_lshlrev_b32_e32 v253, 16, v253\n"
"v_mfma_f32_16x16x32_bf16 v[164:167], v[76:79], v[160:163], v[164:167]\n"
"ds_read_b128 v[152:155], v9 offset:512\n"
"ds_read_b128 v[188:191], v4 offset:1024\n"
"v_mfma_f32_16x16x32_bf16 v[168:171], v[80:83], v[160:163], v[168:171]\n"
"buffer_load_dwordx4 v[192:195], v1, s[4:7], s98 offen offset:3072 sc1\n"
"v_mfma_f32_16x16x32_bf16 v[176:179], v[84:87], v[160:163], v[176:179]\n"
"v_cmp_u_f32_e32 vcc, v96, v96\n"
"s_waitcnt vmcnt(19)\n"
"; @B G1 1.2\n"
"v_mfma_f32_16x16x32_bf16 v[160:163], a[0:3], v[104:107], 0\n"
"s_cbranch_vccnz .Lmk_p57s\n"
".Lmk_p57r:\n"
"ds_read_b128 v[196:199], v9 offset:576\n"
"v_mfma_f32_16x16x32_bf16 v[200:203], a[4:7], v[104:107], 0\n"
"s_waitcnt vmcnt(8)\n"
"v_cmp_ne_u64_e32 vcc, v[254:255], v[18:19]\n"
"v_mfma_f32_16x16x32_bf16 v[204:207], a[8:11], v[104:107], 0\n"
"s_cbranch_vccnz .Lmk_f46s\n"
".Lmk_f46r:\n"
"ds_read_b128 v[208:211], v4 offset:2048\n"
"v_mfma_f32_16x16x32_bf16 v[212:215], a[12:15], v[104:107], 0\n"
"ds_write_b128 v3, v[96:99] offset:6144\n"
"v_mfma_f32_16x16x32_bf16 v[96:99], a[16:19], v[104:107], 0\n"
"s_add_u32 s98, s52, 0x408000\n"
"ds_read_b128 v[216:219], v9 offset:640\n"
"v_mfma_f32_16x16x32_bf16 v[220:223], a[20:23], v[104:107], 0\n"
"buffer_load_dwordx4 v[104:107], v0, s[4:7], s98 offen sc1\n"
"v_mfma_f32_16x16x32_bf16 v[160:163], a[24:27], v[148:151], v[160:163]\n"
"ds_write_b128 v3, v[140:143] offset:7168\n"
"v_mfma_f32_16x16x32_bf16 v[200:203], a[28:31], v[148:151], v[200:203]\n"
"ds_read_b128 v[140:143], v4 offset:3072\n"
"ds_read_b128 v[224:227], v9 offset:704\n"
"v_mfma_f32_16x16x32_bf16 v[204:207], a[32:35], v[148:151], v[204:207]\n"
"buffer_load_dwordx4 a[168:171], v0, s[4:7], s97 offen offset:2048 sc1\n"
"v_mfma_f32_16x16x32_bf16 v[212:215], a[36:39], v[148:151], v[212:215]\n"
"ds_write_b128 v3, v[156:159] offset:8192\n"
"v_mfma_f32_16x16x32_bf16 v[96:99], a[40:43], v[148:151], v[96:99]\n"
"buffer_load_dwordx4 v[156:159], v0, s[4:7], s98 offen offset:1024 sc1\n"
"v_mfma_f32_16x16x32_bf16 v[220:223], a[44:47], v[148:151], v[220:223]\n"
"s_add_u32 s96, s60, 0x408000\n"
"v_mfma_f32_16x16x32_bf16 v[160:163], a[48:51], v[120:123], v[160:163]\n"
"ds_write_b128 v3, v[164:167] offset:9216\n"
"v_mfma_f32_16x16x32_bf16 v[200:203], a[52:55], v[120:123], v[200:203]\n"
"buffer_store_dwordx4 v[124:127], v0, s[4:7], s96 offen sc1\n"
"v_mfma_f32_16x16x32_bf16 v[204:207], a[56:59], v[120:123], v[204:207]\n"
"s_waitcnt lgkmcnt(5)\n"
"v_fmac_f32_e32 v172, v253, v152\n"
"v_mfma_f32_16x16x32_bf16 v[212:215], a[60:63], v[120:123], v[212:215]\n"
"ds_write_b128 v3, v[168:171] offset:10240\n"
"v_mfma_f32_16x16x32_bf16 v[96:99], a[64:67], v[120:123], v[96:99]\n"
"buffer_load_dwordx4 v[148:151], v0, s[4:7], s98 offen offset:2048 sc1\n"
"v_mfma_f32_16x16x32_bf16 v[220:223], a[68:71], v[120:123], v[220:223]\n"
"v_fmac_f32_e32 v173, v253, v153\n"
"v_fmac_f32_e32 v174, v253, v154\n"
"v_mfma_f32_16x16x32_bf16 v[160:163], a[72:75], v[92:95], v[160:163]\n"
"ds_write_b128 v3, v[176:179] offset:11264\n"
"v_mfma_f32_16x16x32_bf16 v[200:203], a[76:79], v[92:95], v[200:203]\n"
"v_fmac_f32_e32 v175, v253, v155\n"
"v_fmac_f32_e32 v188, v253, v196\n"
"v_mfma_f32_16x16x32_bf16 v[204:207], a[80:83], v[92:95], v[204:207]\n"
"v_fmac_f32_e32 v189, v253, v197\n"
"v_fmac_f32_e32 v190, v253, v198\n"
"v_mfma_f32_16x16x32_bf16 v[212:215], a[84:87], v[92:95], v[212:215]\n"
"v_fmac_f32_e32 v191, v253, v199\n"
"v_fmac_f32_e32 v208, v253, v216\n"
"v_mfma_f32_16x16x32_bf16 v[96:99], a[88:91], v[92:95], v[96:99]\n"
"buffer_load_dwordx4 v[120:123], v0, s[4:7], s98 offen offset:3072 sc1\n"
"v_mfma_f32_16x16x32_bf16 v[220:223], a[92:95], v[92:95], v[220:223]\n"
"v_fmac_f32_e32 v209, v253, v217\n"
"v_mfma_f32_16x16x32_bf16 v[160:163], a[96:99], v[112:115], v[160:163]\n"
"v_fmac_f32_e32 v210, v253, v218\n"
"v_fmac_f32_e32 v211, v253, v219\n"
"v_mfma_f32_16x16x32_bf16 v[200:203], a[100:103], v[112:115], v[200:203]\n"
"ds_read_b128 v[92:95], v4 offset:4096\n"
"ds_read_b128 v[152:155], v9 offset:768\n"
"v_mfma_f32_16x16x32_bf16 v[204:207], a[104:107], v[112:115], v[204:207]\n"
"ds_read_b128 v[164:167], v4 offset:5120\n"
"ds_read_b128 v[168:171], v9 offset:832\n"
"v_mfma_f32_16x16x32_bf16 v[212:215], a[108:111], v[112:115], v[212:215]\n"
"v_max_f32_e32 v251, 0, v251\n"
"v_cvt_pk_bf16_f32 v251, v251, 0\n"
"v_mfma_f32_16x16x32_bf16 v[96:99], a[112:115], v[112:115], v[96:99]\n"
"buffer_load_dwordx4 v[176:179], v1, s[4:7], s98 offen sc1\n"
"v_mfma_f32_16x16x32_bf16 v[220:223], a[116:119], v[112:115], v[220:223]\n"
"v_lshlrev_b32_e32 v251, 16, v251\n"
"v_add_f32_e32 v251, 0x3727c5ac, v251\n"
"v_mfma_f32_16x16x32_bf16 v[160:163], a[120:123], v[132:135], v[160:163]\n"
"v_cvt_pk_bf16_f32 v251, v251, 0\n"
"v_lshlrev_b32_e32 v251, 16, v251\n"
"v_mfma_f32_16x16x32_bf16 v[200:203], a[124:127], v[132:135], v[200:203]\n"
"s_waitcnt lgkmcnt(0)\n"
"v_fmac_f32_e32 v140, v253, v224\n"
"v_mfma_f32_16x16x32_bf16 v[204:207], v[24:27], v[132:135], v[204:207]\n"
"buffer_load_dwordx4 a[172:175], v0, s[4:7], s97 offen offset:3072 sc1\n"
"v_mfma_f32_16x16x32_bf16 v[212:215], v[28:31], v[132:135], v[212:215]\n"
"v_fmac_f32_e32 v141, v253, v225\n"
"v_fmac_f32_e32 v142, v253, v226\n"
"v_mfma_f32_16x16x32_bf16 v[96:99], v[32:35], v[132:135], v[96:99]\n"
"buffer_load_dwordx4 v[112:115], v1, s[4:7], s98 offen offset:1024 sc1\n"
"v_mfma_f32_16x16x32_bf16 v[220:223], v[36:39], v[132:135], v[220:223]\n"
"v_fmac_f32_e32 v143, v253, v227\n"
"v_mfma_f32_16x16x32_bf16 v[160:163], v[40:43], v[100:103], v[160:163]\n"
"v_fmac_f32_e32 v92, v253, v152\n"
"v_fmac_f32_e32 v93, v253, v153\n"
"v_mfma_f32_16x16x32_bf16 v[200:203], v[44:47], v[100:103], v[200:203]\n"
"v_fmac_f32_e32 v94, v253, v154\n"
"v_fmac_f32_e32 v95, v253, v155\n"
"v_mfma_f32_16x16x32_bf16 v[204:207], v[48:51], v[100:103], v[204:207]\n"
"v_fmac_f32_e32 v164, v253, v168\n"
"v_fmac_f32_e32 v165, v253, v169\n"
"v_mfma_f32_16x16x32_bf16 v[212:215], v[52:55], v[100:103], v[212:215]\n"
"v_fmac_f32_e32 v166, v253, v170\n"
"v_fmac_f32_e32 v167, v253, v171\n"
"v_mfma_f32_16x16x32_bf16 v[96:99], v[56:59], v[100:103], v[96:99]\n"
"v_max_u32_e32 v255, v108, v109\n"
"v_mfma_f32_16x16x32_bf16 v[220:223], v[60:63], v[100:103], v[220:223]\n"
"v_max3_u32 v255, v255, v110, v111\n"
"v_max3_u32 v255, v255, v180, v181\n"
"v_mfma_f32_16x16x32_bf16 v[160:163], v[64:67], v[88:91], v[160:163]\n"
"v_max3_u32 v255, v255, v182, v183\n"
"s_waitcnt vmcnt(15)\n"
"v_mfma_f32_16x16x32_bf16 v[200:203], v[68:71], v[88:91], v[200:203]\n"
"v_max3_u32 v255, v255, v116, v117\n"
"v_max3_u32 v255, v255, v118, v119\n"
"v_mfma_f32_16x16x32_bf16 v[204:207], v[72:75], v[88:91], v[204:207]\n"
"v_max3_u32 v255, v255, v128, v129\n"
"v_max3_u32 v255, v255, v130, v131\n"
"v_mfma_f32_16x16x32_bf16 v[212:215], v[76:79], v[88:91], v[212:215]\n"
"v_cmp_eq_u32_e32 vcc, -1, v255\n"
"v_mfma_f32_16x16x32_bf16 v[96:99], v[80:83], v[88:91], v[96:99]\n"
"s_cbranch_vccnz .Lmk_q59s\n"
".Lmk_q59r:\n"
"v_mfma_f32_16x16x32_bf16 v[220:223], v[84:87], v[88:91], v[220:223]\n"
"v_cmp_u_f32_e32 vcc, v160, v160\n"
"; @B G1 1.3\n"
"v_mfma_f32_16x16x32_bf16 v[172:175], a[0:3], v[108:111], v[172:175]\n"
"s_cbranch_vccnz .Lmk_p58s\n"
".Lmk_p58r:\n"
"s_add_u32 s96, s61, 0x840000\n"
"v_mfma_f32_16x16x32_bf16 v[188:191], a[4:7], v[108:111], v[188:191]\n"
"buffer_load_dwordx4 v[88:91], v1, s[4:7], s98 offen offset:2048 sc1\n"
"v_mfma_f32_16x16x32_bf16 v[208:211], a[8:11], v[108:111], v[208:211]\n"
"buffer_load_dwordx4 v[100:103], v1, s[4:7], s98 offen offset:3072 sc1\n"
"v_mfma_f32_16x16x32_bf16 v[140:143], a[12:15], v[108:111], v[140:143]\n"
"ds_write_b128 v3, v[160:163] offset:12288\n"
"v_mfma_f32_16x16x32_bf16 v[92:95], a[16:19], v[108:111], v[92:95]\n"
"s_add_u32 s98, s53, 0x408000\n"
"v_mfma_f32_16x16x32_bf16 v[164:167], a[20:23], v[108:111], v[164:167]\n"
"buffer_load_dwordx4 v[108:111], v0, s[4:7], s98 offen sc1\n"
"v_mfma_f32_16x16x32_bf16 v[172:175], a[24:27], v[180:183], v[172:175]\n"
"ds_write_b128 v3, v[200:203] offset:13312\n"
"v_mfma_f32_16x16x32_bf16 v[188:191], a[28:31], v[180:183], v[188:191]\n"
"buffer_store_dwordx4 v[124:127], v0, s[8:11], s96 offen sc1\n"
"v_mfma_f32_16x16x32_bf16 v[208:211], a[32:35], v[180:183], v[208:211]\n"
"buffer_load_dwordx4 a[176:179], v1, s[4:7], s97 offen sc1\n"
"v_mfma_f32_16x16x32_bf16 v[140:143], a[36:39], v[180:183], v[140:143]\n"
"ds_write_b128 v3, v[204:207] offset:14336\n"
"v_mfma_f32_16x16x32_bf16 v[92:95], a[40:43], v[180:183], v[92:95]\n"
"buffer_load_dwordx4 v[132:135], v0, s[4:7], s98 offen offset:1024 sc1\n"
"v_mfma_f32_16x16x32_bf16 v[164:167], a[44:47], v[180:183], v[164:167]\n"
"v_mfma_f32_16x16x32_bf16 v[172:175], a[48:51], v[116:119], v[172:175]\n"
"ds_write_b128 v3, v[212:215] offset:15360\n"
"v_mfma_f32_16x16x32_bf16 v[188:191], a[52:55], v[116:119], v[188:191]\n"
"v_sqrt_f32_e32 v251, v251\n"
"v_mfma_f32_16x16x32_bf16 v[208:211], a[56:59], v[116:119], v[208:211]\n"
"v_cvt_pk_bf16_f32 v251, v251, 0\n"
"v_lshlrev_b32_e32 v251, 16, v251\n"
"v_mfma_f32_16x16x32_bf16 v[140:143], a[60:63], v[116:119], v[140:143]\n"
"ds_write_b128 v3, v[96:99] offset:16384\n"
"v_mfma_f32_16x16x32_bf16 v[92:95], a[64:67], v[116:119], v[92:95]\n"
"s_waitcnt vmcnt(17)\n"
"v_max_u32_e32 v255, v136, v137\n"
"v_mfma_f32_16x16x32_bf16 v[164:167], a[68:71], v[116:119], v[164:167]\n"
"v_max3_u32 v255, v255, v138, v139\n"
"v_max3_u32 v255, v255, v144, v145\n"
"v_mfma_f32_16x16x32_bf16 v[172:175], a[72:75], v[128:131], v[172:175]\n"
"v_max3_u32 v255, v255, v146, v147\n"
"s_waitcnt vmcnt(15)\n"
"v_mfma_f32_16x16x32_bf16 v[188:191], a[76:79], v[128:131], v[188:191]\n"
"v_max3_u32 v255, v255, v184, v185\n"
"v_max3_u32 v255, v255, v186, v187\n"
"v_mfma_f32_16x16x32_bf16 v[208:211], a[80:83], v[128:131], v[208:211]\n"
"v_max3_u32 v255, v255, v192, v193\n"
"v_max3_u32 v255, v255, v194, v195\n"
"v_mfma_f32_16x16x32_bf16 v[140:143], a[84:87], v[128:131], v[140:143]\n"
"v_cmp_eq_u32_e32 vcc, -1, v255\n"
"v_mfma_f32_16x16x32_bf16 v[92:95], a[88:91], v[128:131], v[92:95]\n"
"s_cbranch_vccnz .Lmk_q60s\n"
".Lmk_q60r:\n"
"v_mfma_f32_16x16x32_bf16 v[164:167], a[92:95], v[128:131], v[164:167]\n"
"v_mfma_f32_16x16x32_bf16 v[172:175], a[96:99], v[136:139], v[172:175]\n"
"buffer_load_dwordx4 v[96:99], v0, s[4:7], s98 offen offset:2048 sc1\n"
"v_mfma_f32_16x16x32_bf16 v[188:191], a[100:103], v[136:139], v[188:191]\n"
"buffer_load_dwordx4 v[116:119], v0, s[4:7], s98 offen offset:3072 sc1\n"
"v_mfma_f32_16x16x32_bf16 v[208:211], a[104:107], v[136:139], v[208:211]\n"
"ds_write_b128 v3, v[220:223] offset:17408\n"
"v_mfma_f32_16x16x32_bf16 v[140:143], a[108:111], v[136:139], v[140:143]\n"
"v_rcp_f32_e32 v255, v251\n"
"v_mfma_f32_16x16x32_bf16 v[92:95], a[112:115], v[136:139], v[92:95]\n"
"buffer_load_dwordx4 v[128:131], v1, s[4:7], s98 offen sc1\n"
"v_mfma_f32_16x16x32_bf16 v[164:167], a[116:119], v[136:139], v[164:167]\n"
"s_waitcnt lgkmcnt(0)\n"
"s_barrier\n"
"v_mfma_f32_16x16x32_bf16 v[172:175], a[120:123], v[144:147], v[172:175]\n"
"ds_read_b128 v[136:139], v5 offset:2048\n"
"ds_read_b128 v[152:155], v6 offset:2048\n"
"v_mfma_f32_16x16x32_bf16 v[188:191], a[124:127], v[144:147], v[188:191]\n"
"ds_read_b128 v[160:163], v5 offset:3072\n"
"ds_read_b128 v[168:171], v6 offset:3072\n"
"v_mfma_f32_16x16x32_bf16 v[208:211], v[24:27], v[144:147], v[208:211]\n"
"buffer_load_dwordx4 a[180:183], v1, s[4:7], s97 offen offset:1024 sc1\n"
"v_mfma_f32_16x16x32_bf16 v[140:143], v[28:31], v[144:147], v[140:143]\n"
"v_mul_f32_e32 v254, 0x3e8293ee, v255\n"
"v_mfma_f32_16x16x32_bf16 v[92:95], v[32:35], v[144:147], v[92:95]\n"
"buffer_load_dwordx4 v[180:183], v1, s[4:7], s98 offen offset:1024 sc1\n"
"v_mfma_f32_16x16x32_bf16 v[164:167], v[36:39], v[144:147], v[164:167]\n"
"ds_read_b128 v[144:147], v5 offset:4096\n"
"v_mfma_f32_16x16x32_bf16 v[172:175], v[40:43], v[184:187], v[172:175]\n"
"ds_read_b128 v[196:199], v6 offset:4096\n"
"ds_read_b128 v[200:203], v5 offset:5120\n"
"v_mfma_f32_16x16x32_bf16 v[188:191], v[44:47], v[184:187], v[188:191]\n"
"ds_read_b128 v[204:207], v6 offset:5120\n"
"s_waitcnt lgkmcnt(4)\n"
"v_mfma_f32_16x16x32_bf16 v[208:211], v[48:51], v[184:187], v[208:211]\n"
"v_add_f32_e32 v136, v136, v152\n"
"v_add_f32_e32 v137, v137, v153\n"
"v_mfma_f32_16x16x32_bf16 v[140:143], v[52:55], v[184:187], v[140:143]\n"
"v_add_f32_e32 v138, v138, v154\n"
"v_add_f32_e32 v139, v139, v155\n"
"v_mfma_f32_16x16x32_bf16 v[92:95], v[56:59], v[184:187], v[92:95]\n"
"buffer_load_dwordx4 v[152:155], v1, s[4:7], s98 offen offset:2048 sc1\n"
"v_mfma_f32_16x16x32_bf16 v[164:167], v[60:63], v[184:187], v[164:167]\n"
"ds_read_b128 v[184:187], v5\n"
"ds_read_b128 v[212:215], v6\n"
"v_mfma_f32_16x16x32_bf16 v[172:175], v[64:67], v[192:195], v[172:175]\n"
"s_add_u32 s96, s61, 0x840000\n"
"ds_read_b128 v[216:219], v5 offset:1024\n"
"v_mfma_f32_16x16x32_bf16 v[188:191], v[68:71], v[192:195], v[188:191]\n"
"ds_read_b128 v[220:223], v6 offset:1024\n"
"v_add_f32_e32 v160, v160, v168\n"
"v_mfma_f32_16x16x32_bf16 v[208:211], v[72:75], v[192:195], v[208:211]\n"
"buffer_store_dwordx4 v[124:127], v0, s[8:11], s96 offen offset:1024 sc1\n"
"v_mfma_f32_16x16x32_bf16 v[140:143], v[76:79], v[192:195], v[140:143]\n"
"v_add_f32_e32 v161, v161, v169\n"
"v_add_f32_e32 v162, v162, v170\n"
"v_mfma_f32_16x16x32_bf16 v[92:95], v[80:83], v[192:195], v[92:95]\n"
"buffer_load_dwordx4 v[124:127], v1, s[4:7], s98 offen offset:3072 sc1\n"
"v_mfma_f32_16x16x32_bf16 v[164:167], v[84:87], v[192:195], v[164:167]\n"
"v_add_f32_e32 v163, v163, v171\n"
"s_waitcnt lgkmcnt(4)\n"
"v_add_f32_e32 v144, v144, v196\n"
"v_add_f32_e32 v145, v145, v197\n"
"v_add_f32_e32 v146, v146, v198\n"
"v_add_f32_e32 v147, v147, v199\n"
"v_add_f32_e32 v200, v200, v204\n"
"v_add_f32_e32 v201, v201, v205\n"
"v_add_f32_e32 v202, v202, v206\n"
"v_add_f32_e32 v203, v203, v207\n"
"s_waitcnt lgkmcnt(0)\n"
"v_add_f32_e32 v184, v184, v212\n"
"v_add_f32_e32 v185, v185, v213\n"
"v_add_f32_e32 v186, v186, v214\n"
"v_add_f32_e32 v187, v187, v215\n"
"v_add_f32_e32 v216, v216, v220\n"
"v_add_f32_e32 v217, v217, v221\n"
"v_add_f32_e32 v218, v218, v222\n"
"v_add_f32_e32 v219, v219, v223\n"
".Lmk_b2:\n"
"s_waitcnt vmcnt(20)\n"
"; @B G1 2.0\n"
"v_mfma_f32_16x16x32_bf16 v[168:171], a[0:3], v[104:107], 0\n"
"s_add_u32 s98, s62, 0x40800\n"
"v_mfma_f32_16x16x32_bf16 v[192:195], a[4:7], v[104:107], 0\n"
"buffer_load_dwordx2 v[252:253], v10, s[4:7], s98 offen sc1\n"
"v_mfma_f32_16x16x32_bf16 v[196:199], a[8:11], v[104:107], 0\n"
"buffer_load_dwordx2 v[250:251], v10, s[4:7], s98 offen offset:128 sc1\n"
"v_mfma_f32_16x16x32_bf16 v[204:207], a[12:15], v[104:107], 0\n"
"buffer_load_dwordx2 v[248:249], v10, s[4:7], s98 offen offset:256 sc1\n"
"v_mfma_f32_16x16x32_bf16 v[212:215], a[16:19], v[104:107], 0\n"
"s_add_u32 s96, s54, 0x408000\n"
"v_add_f32_e32 v208, v208, v136\n"
"v_mfma_f32_16x16x32_bf16 v[220:223], a[20:23], v[104:107], 0\n"
"buffer_load_dwordx4 v[104:107], v0, s[4:7], s96 offen sc1\n"
"v_mfma_f32_16x16x32_bf16 v[168:171], a[24:27], v[156:159], v[168:171]\n"
"buffer_load_dwordx2 v[246:247], v10, s[4:7], s98 offen offset:384 sc1\n"
"v_mfma_f32_16x16x32_bf16 v[192:195], a[28:31], v[156:159], v[192:195]\n"
"v_add_f32_e32 v209, v209, v137\n"
"v_add_f32_e32 v210, v210, v138\n"
"v_mfma_f32_16x16x32_bf16 v[196:199], a[32:35], v[156:159], v[196:199]\n"
"buffer_load_dwordx4 a[184:187], v1, s[4:7], s97 offen offset:2048 sc1\n"
"v_mfma_f32_16x16x32_bf16 v[204:207], a[36:39], v[156:159], v[204:207]\n"
"v_add_f32_e32 v211, v211, v139\n"
"v_add_f32_e32 v140, v140, v160\n"
"v_mfma_f32_16x16x32_bf16 v[212:215], a[40:43], v[156:159], v[212:215]\n"
"buffer_load_dwordx4 v[136:139], v0, s[4:7], s96 offen offset:1024 sc1\n"
"v_mfma_f32_16x16x32_bf16 v[220:223], a[44:47], v[156:159], v[220:223]\n"
"s_waitcnt vmcnt(24)\n"
"v_add_f32_e32 v141, v141, v161\n"
"v_mfma_f32_16x16x32_bf16 v[168:171], a[48:51], v[148:151], v[168:171]\n"
"v_add_f32_e32 v142, v142, v162\n"
"v_add_f32_e32 v143, v143, v163\n"
"v_mfma_f32_16x16x32_bf16 v[192:195], a[52:55], v[148:151], v[192:195]\n"
"v_mul_f32_e32 v208, v208, v255\n"
"v_mul_f32_e32 v209, v209, v255\n"
"v_mfma_f32_16x16x32_bf16 v[196:199], a[56:59], v[148:151], v[196:199]\n"
"v_mul_f32_e32 v210, v210, v255\n"
"v_mul_f32_e32 v211, v211, v255\n"
"v_mfma_f32_16x16x32_bf16 v[204:207], a[60:63], v[148:151], v[204:207]\n"
"v_mul_f32_e32 v140, v140, v255\n"
"v_mul_f32_e32 v141, v141, v255\n"
"v_mfma_f32_16x16x32_bf16 v[212:215], a[64:67], v[148:151], v[212:215]\n"
"buffer_load_dwordx4 v[156:159], v0, s[4:7], s96 offen offset:2048 sc1\n"
"v_mfma_f32_16x16x32_bf16 v[220:223], a[68:71], v[148:151], v[220:223]\n"
"v_mul_f32_e32 v142, v142, v255\n"
"v_mul_f32_e32 v143, v143, v255\n"
"v_mfma_f32_16x16x32_bf16 v[168:171], a[72:75], v[120:123], v[168:171]\n"
"v_cvt_pk_bf16_f32 v148, v208, v209\n"
"v_cvt_pk_bf16_f32 v149, v210, v211\n"
"v_mfma_f32_16x16x32_bf16 v[192:195], a[76:79], v[120:123], v[192:195]\n"
"v_cvt_pk_bf16_f32 v150, v140, v141\n"
"v_cvt_pk_bf16_f32 v151, v142, v143\n"
"v_mfma_f32_16x16x32_bf16 v[196:199], a[80:83], v[120:123], v[196:199]\n"
"ds_write_b128 v8, v[148:151] offset:25600\n"
"v_mfma_f32_16x16x32_bf16 v[204:207], a[84:87], v[120:123], v[204:207]\n"
"s_add_u32 s98, s64, 0x802000\n"
"v_mfma_f32_16x16x32_bf16 v[212:215], a[88:91], v[120:123], v[212:215]\n"
"buffer_load_dwordx4 v[140:143], v0, s[4:7], s96 offen offset:3072 sc1\n"
"v_mfma_f32_16x16x32_bf16 v[220:223], a[92:95], v[120:123], v[220:223]\n"
"s_waitcnt vmcnt(23)\n"
"v_mfma_f32_16x16x32_bf16 v[168:171], a[96:99], v[176:179], v[168:171]\n"
"buffer_store_dwordx4 v[148:151], v8, s[12:15], s98 offen offset:1024 sc1\n"
"v_mfma_f32_16x16x32_bf16 v[192:195], a[100:103], v[176:179], v[192:195]\n"
"v_add_f32_e32 v92, v92, v144\n"
"v_add_f32_e32 v93, v93, v145\n"
"v_mfma_f32_16x16x32_bf16 v[196:199], a[104:107], v[176:179], v[196:199]\n"
"v_add_f32_e32 v94, v94, v146\n"
"v_add_f32_e32 v95, v95, v147\n"
"v_mfma_f32_16x16x32_bf16 v[204:207], a[108:111], v[176:179], v[204:207]\n"
"v_add_f32_e32 v164, v164, v200\n"
"v_add_f32_e32 v165, v165, v201\n"
"v_mfma_f32_16x16x32_bf16 v[212:215], a[112:115], v[176:179], v[212:215]\n"
"buffer_load_dwordx4 v[120:123], v1, s[4:7], s96 offen sc1\n"
"v_mfma_f32_16x16x32_bf16 v[220:223], a[116:119], v[176:179], v[220:223]\n"
"v_add_f32_e32 v166, v166, v202\n"
"v_add_f32_e32 v167, v167, v203\n"
"v_mfma_f32_16x16x32_bf16 v[168:171], a[120:123], v[112:115], v[168:171]\n"
"v_mul_f32_e32 v92, v92, v255\n"
"v_mul_f32_e32 v93, v93, v255\n"
"v_mfma_f32_16x16x32_bf16 v[192:195], a[124:127], v[112:115], v[192:195]\n"
"v_mul_f32_e32 v94, v94, v255\n"
"v_mul_f32_e32 v95, v95, v255\n"
"v_mfma_f32_16x16x32_bf16 v[196:199], v[24:27], v[112:115], v[196:199]\n"
"buffer_load_dwordx4 a[188:191], v1, s[4:7], s97 offen offset:3072 sc1\n"
"v_mfma_f32_16x16x32_bf16 v[204:207], v[28:31], v[112:115], v[204:207]\n"
"v_mul_f32_e32 v164, v164, v255\n"
"v_mul_f32_e32 v165, v165, v255\n"
"v_mfma_f32_16x16x32_bf16 v[212:215], v[32:35], v[112:115], v[212:215]\n"
"buffer_load_dwordx4 v[144:147], v1, s[4:7], s96 offen offset:1024 sc1\n"
"v_mfma_f32_16x16x32_bf16 v[220:223], v[36:39], v[112:115], v[220:223]\n"
"s_waitcnt vmcnt(25)\n"
"v_mul_f32_e32 v166, v166, v255\n"
"v_mfma_f32_16x16x32_bf16 v[168:171], v[40:43], v[88:91], v[168:171]\n"
"v_mul_f32_e32 v167, v167, v255\n"
"v_cvt_pk_bf16_f32 v112, v92, v93\n"
"v_mfma_f32_16x16x32_bf16 v[192:195], v[44:47], v[88:91], v[192:195]\n"
"v_cvt_pk_bf16_f32 v113, v94, v95\n"
"v_cvt_pk_bf16_f32 v114, v164, v165\n"
"v_mfma_f32_16x16x32_bf16 v[196:199], v[48:51], v[88:91], v[196:199]\n"
"v_cvt_pk_bf16_f32 v115, v166, v167\n"
"v_mfma_f32_16x16x32_bf16 v[204:207], v[52:55], v[88:91], v[204:207]\n"
"ds_write_b128 v7, v[112:115] offset:58368\n"
"v_mfma_f32_16x16x32_bf16 v[212:215], v[56:59], v[88:91], v[212:215]\n"
"buffer_load_dwordx4 v[92:95], v1, s[4:7], s96 offen offset:2048 sc1\n"
"v_mfma_f32_16x16x32_bf16 v[220:223], v[60:63], v[88:91], v[220:223]\n"
"s_add_u32 s98, s64, 0x806000\n"
"v_mfma_f32_16x16x32_bf16 v[168:171], v[64:67], v[100:103], v[168:171]\n"
"buffer_store_dwordx4 v[112:115], v7, s[12:15], s98 offen offset:1024 sc1\n"
"v_mfma_f32_16x16x32_bf16 v[192:195], v[68:71], v[100:103], v[192:195]\n"
"v_add_f32_e32 v172, v172, v184\n"
"v_add_f32_e32 v173, v173, v185\n"
"v_mfma_f32_16x16x32_bf16 v[196:199], v[72:75], v[100:103], v[196:199]\n"
"v_add_f32_e32 v174, v174, v186\n"
"v_add_f32_e32 v175, v175, v187\n"
"v_mfma_f32_16x16x32_bf16 v[204:207], v[76:79], v[100:103], v[204:207]\n"
"v_add_f32_e32 v188, v188, v216\n"
"v_add_f32_e32 v189, v189, v217\n"
"v_mfma_f32_16x16x32_bf16 v[212:215], v[80:83], v[100:103], v[212:215]\n"
"buffer_load_dwordx4 v[88:91], v1, s[4:7], s96 offen offset:3072 sc1\n"
"v_mfma_f32_16x16x32_bf16 v[220:223], v[84:87], v[100:103], v[220:223]\n"
"s_waitcnt vmcnt(24)\n"
"v_add_f32_e32 v190, v190, v218\n"
"; @B G1 2.1\n"
"v_mfma_f32_16x16x32_bf16 v[100:103], a[0:3], v[108:111], 0\n"
"v_add_f32_e32 v191, v191, v219\n"
"v_mul_f32_e32 v172, v172, v254\n"
"v_mfma_f32_16x16x32_bf16 v[112:115], a[4:7], v[108:111], 0\n"
"v_mul_f32_e32 v173, v173, v254\n"
"v_mul_f32_e32 v174, v174, v254\n"
"v_mfma_f32_16x16x32_bf16 v[148:151], a[8:11], v[108:111], 0\n"
"v_mul_f32_e32 v175, v175, v254\n"
"v_mul_f32_e32 v188, v188, v254\n"
"v_mfma_f32_16x16x32_bf16 v[160:163], a[12:15], v[108:111], 0\n"
"ds_write_b128 v3, v[168:171]\n"
"v_mfma_f32_16x16x32_bf16 v[164:167], a[16:19], v[108:111], 0\n"
"s_add_u32 s98, s55, 0x408000\n"
"v_mul_f32_e32 v189, v189, v254\n"
"v_mfma_f32_16x16x32_bf16 v[168:171], a[20:23], v[108:111], 0\n"
"buffer_load_dwordx4 v[108:111], v0, s[4:7], s98 offen sc1\n"
"v_mfma_f32_16x16x32_bf16 v[100:103], a[24:27], v[132:135], v[100:103]\n"
"ds_write_b128 v3, v[192:195] offset:1024\n"
"v_mfma_f32_16x16x32_bf16 v[112:115], a[28:31], v[132:135], v[112:115]\n"
"s_add_u32 s97, s70, 0x204000\n"
"v_mul_f32_e32 v190, v190, v254\n"
"v_mfma_f32_16x16x32_bf16 v[148:151], a[32:35], v[132:135], v[148:151]\n"
"buffer_load_dwordx4 a[192:195], v0, s[4:7], s97 offen sc1\n"
"v_mfma_f32_16x16x32_bf16 v[160:163], a[36:39], v[132:135], v[160:163]\n"
"ds_write_b128 v3, v[196:199] offset:2048\n"
"v_mfma_f32_16x16x32_bf16 v[164:167], a[40:43], v[132:135], v[164:167]\n"
"buffer_load_dwordx4 v[176:179], v0, s[4:7], s98 offen offset:1024 sc1\n"
"v_mfma_f32_16x16x32_bf16 v[168:171], a[44:47], v[132:135], v[168:171]\n"
"s_waitcnt vmcnt(25)\n"
"v_mul_f32_e32 v191, v191, v254\n"
"v_mfma_f32_16x16x32_bf16 v[100:103], a[48:51], v[96:99], v[100:103]\n"
"ds_write_b128 v3, v[204:207] offset:3072\n"
"v_mfma_f32_16x16x32_bf16 v[112:115], a[52:55], v[96:99], v[112:115]\n"
"v_cvt_pk_bf16_f32 v132, v172, v173\n"
"v_cvt_pk_bf16_f32 v133, v174, v175\n"
"v_mfma_f32_16x16x32_bf16 v[148:151], a[56:59], v[96:99], v[148:151]\n"
"v_cvt_pk_bf16_f32 v134, v188, v189\n"
"v_cvt_pk_bf16_f32 v135, v190, v191\n"
"v_mfma_f32_16x16x32_bf16 v[160:163], a[60:63], v[96:99], v[160:163]\n"
"ds_write_b128 v3, v[212:215] offset:4096\n"
"v_mfma_f32_16x16x32_bf16 v[164:167], a[64:67], v[96:99], v[164:167]\n"
"buffer_load_dwordx4 v[172:175], v0, s[4:7], s98 offen offset:2048 sc1\n"
"v_mfma_f32_16x16x32_bf16 v[168:171], a[68:71], v[96:99], v[168:171]\n"
"ds_write_b128 v2, v[132:135] offset:50176\n"
"v_mfma_f32_16x16x32_bf16 v[100:103], a[72:75], v[116:119], v[100:103]\n"
"ds_write_b128 v3, v[220:223] offset:5120\n"
"v_mfma_f32_16x16x32_bf16 v[112:115], a[76:79], v[116:119], v[112:115]\n"
"v_mfma_f32_16x16x32_bf16 v[148:151], a[80:83], v[116:119], v[148:151]\n"
"v_mov_b32_e32 v96, -1\n"
"v_mov_b32_e32 v97, -1\n"
"v_mfma_f32_16x16x32_bf16 v[160:163], a[84:87], v[116:119], v[160:163]\n"
"v_mov_b32_e32 v98, -1\n"
"v_mov_b32_e32 v99, -1\n"
"v_mfma_f32_16x16x32_bf16 v[164:167], a[88:91], v[116:119], v[164:167]\n"
"buffer_load_dwordx4 v[132:135], v0, s[4:7], s98 offen offset:3072 sc1\n"
"v_mfma_f32_16x16x32_bf16 v[168:171], a[92:95], v[116:119], v[168:171]\n"
"s_waitcnt vmcnt(24)\n"
"s_add_u32 s96, s63, 0x84000\n"
"v_mfma_f32_16x16x32_bf16 v[100:103], a[96:99], v[128:131], v[100:103]\n"
"buffer_store_dwordx2 v[96:97], v14, s[8:11], s96 offen sc1\n"
"v_mfma_f32_16x16x32_bf16 v[112:115], a[100:103], v[128:131], v[112:115]\n"
"v_mfma_f32_16x16x32_bf16 v[148:151], a[104:107], v[128:131], v[148:151]\n"
"v_mfma_f32_16x16x32_bf16 v[160:163], a[108:111], v[128:131], v[160:163]\n"
"v_mfma_f32_16x16x32_bf16 v[164:167], a[112:115], v[128:131], v[164:167]\n"
"buffer_load_dwordx4 v[116:119], v1, s[4:7], s98 offen sc1\n"
"v_mfma_f32_16x16x32_bf16 v[168:171], a[116:119], v[128:131], v[168:171]\n"
"s_waitcnt lgkmcnt(0)\n"
"s_barrier\n"
"v_mfma_f32_16x16x32_bf16 v[100:103], a[120:123], v[180:183], v[100:103]\n"
"s_waitcnt vmcnt(18)\n"
"v_add_f32_e32 v252, v252, v250\n"
"v_mfma_f32_16x16x32_bf16 v[112:115], a[124:127], v[180:183], v[112:115]\n"
"v_add_f32_e32 v253, v253, v251\n"
"v_add_f32_e32 v248, v248, v246\n"
"v_mfma_f32_16x16x32_bf16 v[148:151], v[24:27], v[180:183], v[148:151]\n"
"buffer_load_dwordx4 a[196:199], v0, s[4:7], s97 offen offset:1024 sc1\n"
"v_mfma_f32_16x16x32_bf16 v[160:163], v[28:31], v[180:183], v[160:163]\n"
"v_add_f32_e32 v249, v249, v247\n"
"v_add_f32_e32 v252, v252, v248\n"
"v_mfma_f32_16x16x32_bf16 v[164:167], v[32:35], v[180:183], v[164:167]\n"
"buffer_load_dwordx4 v[128:131], v1, s[4:7], s98 offen offset:1024 sc1\n"
"v_mfma_f32_16x16x32_bf16 v[168:171], v[36:39], v[180:183], v[168:171]\n"
"v_add_f32_e32 v253, v253, v249\n"
"v_mfma_f32_16x16x32_bf16 v[100:103], v[40:43], v[152:155], v[100:103]\n"
"s_nop 0\n"
"v_permlane16_swap_b32_e32 v252, v253\n"
"v_add_f32_e32 v252, v252, v253\n"
"v_mfma_f32_16x16x32_bf16 v[112:115], v[44:47], v[152:155], v[112:115]\n"
"v_mov_b32_e32 v253, v252\n"
"v_mfma_f32_16x16x32_bf16 v[148:151], v[48:51], v[152:155], v[148:151]\n"
"s_nop 0\n"
"v_permlane32_swap_b32_e32 v252, v253\n"
"v_add_f32_e32 v252, v252, v253\n"
"v_mfma_f32_16x16x32_bf16 v[160:163], v[52:55], v[152:155], v[160:163]\n"
"v_mul_f32_e32 v252, 0x3a800000, v252\n"
"v_mov_b32_e32 v253, v252\n"
"v_mfma_f32_16x16x32_bf16 v[164:167], v[56:59], v[152:155], v[164:167]\n"
"buffer_load_dwordx4 v[180:183], v1, s[4:7], s98 offen offset:2048 sc1\n"
"v_mfma_f32_16x16x32_bf16 v[168:171], v[60:63], v[152:155], v[168:171]\n"
"v_permlane16_swap_b32_e32 v252, v253\n"
"v_cvt_pk_bf16_f32 v255, -v252, 0\n"
"v_mfma_f32_16x16x32_bf16 v[100:103], v[64:67], v[124:127], v[100:103]\n"
"v_fma_f32 v253, -v252, v252, v253\n"
"v_lshlrev_b32_e32 v255, 16, v255\n"
"v_mfma_f32_16x16x32_bf16 v[112:115], v[68:71], v[124:127], v[112:115]\n"
"ds_read_b128 v[152:155], v4\n"
"ds_read_b128 v[184:187], v9 offset:512\n"
"v_mfma_f32_16x16x32_bf16 v[148:151], v[72:75], v[124:127], v[148:151]\n"
"ds_read_b128 v[188:191], v4 offset:1024\n"
"ds_read_b128 v[192:195], v9 offset:576\n"
"v_mfma_f32_16x16x32_bf16 v[160:163], v[76:79], v[124:127], v[160:163]\n"
"ds_read_b128 v[196:199], v4 offset:2048\n"
"ds_read_b128 v[200:203], v9 offset:640\n"
"v_mfma_f32_16x16x32_bf16 v[164:167], v[80:83], v[124:127], v[164:167]\n"
"buffer_load_dwordx4 v[204:207], v1, s[4:7], s98 offen offset:3072 sc1\n"
"v_mfma_f32_16x16x32_bf16 v[168:171], v[84:87], v[124:127], v[168:171]\n"
"s_waitcnt vmcnt(20)\n"
"v_max_f32_e32 v253, 0, v253\n"
"; @B G1 2.2\n"
"v_mfma_f32_16x16x32_bf16 v[124:127], a[0:3], v[104:107], 0\n"
"v_cvt_pk_bf16_f32 v253, v253, 0\n"
"v_lshlrev_b32_e32 v253, 16, v253\n"
"v_mfma_f32_16x16x32_bf16 v[208:211], a[4:7], v[104:107], 0\n"
"v_add_f32_e32 v253, 0x3727c5ac, v253\n"
"v_cvt_pk_bf16_f32 v253, v253, 0\n"
"v_mfma_f32_16x16x32_bf16 v[212:215], a[8:11], v[104:107], 0\n"
"s_waitcnt lgkmcnt(0)\n"
"v_fmac_f32_e32 v152, v255, v184\n"
"v_mfma_f32_16x16x32_bf16 v[216:219], a[12:15], v[104:107], 0\n"
"ds_write_b128 v3, v[100:103] offset:6144\n"
"v_mfma_f32_16x16x32_bf16 v[100:103], a[16:19], v[104:107], 0\n"
"s_add_u32 s98, s52, 0x400000\n"
"v_fmac_f32_e32 v153, v255, v185\n"
"v_mfma_f32_16x16x32_bf16 v[220:223], a[20:23], v[104:107], 0\n"
"buffer_load_dwordx4 v[104:107], v0, s[4:7], s98 offen sc1\n"
"v_mfma_f32_16x16x32_bf16 v[124:127], a[24:27], v[136:139], v[124:127]\n"
"ds_write_b128 v3, v[112:115] offset:7168\n"
"v_mfma_f32_16x16x32_bf16 v[208:211], a[28:31], v[136:139], v[208:211]\n"
"v_fmac_f32_e32 v154, v255, v186\n"
"v_fmac_f32_e32 v155, v255, v187\n"
"v_mfma_f32_16x16x32_bf16 v[212:215], a[32:35], v[136:139], v[212:215]\n"
"buffer_load_dwordx4 a[200:203], v0, s[4:7], s97 offen offset:2048 sc1\n"
"v_mfma_f32_16x16x32_bf16 v[216:219], a[36:39], v[136:139], v[216:219]\n"
"ds_write_b128 v3, v[148:151] offset:8192\n"
"v_mfma_f32_16x16x32_bf16 v[100:103], a[40:43], v[136:139], v[100:103]\n"
"buffer_load_dwordx4 v[112:115], v0, s[4:7], s98 offen offset:1024 sc1\n"
"v_mfma_f32_16x16x32_bf16 v[220:223], a[44:47], v[136:139], v[220:223]\n"
"s_waitcnt vmcnt(21)\n"
"v_fmac_f32_e32 v188, v255, v192\n"
"v_mfma_f32_16x16x32_bf16 v[124:127], a[48:51], v[156:159], v[124:127]\n"
"ds_write_b128 v3, v[160:163] offset:9216\n"
"v_mfma_f32_16x16x32_bf16 v[208:211], a[52:55], v[156:159], v[208:211]\n"
"v_fmac_f32_e32 v189, v255, v193\n"
"v_fmac_f32_e32 v190, v255, v194\n"
"v_mfma_f32_16x16x32_bf16 v[212:215], a[56:59], v[156:159], v[212:215]\n"
"v_fmac_f32_e32 v191, v255, v195\n"
"v_fmac_f32_e32 v196, v255, v200\n"
"v_mfma_f32_16x16x32_bf16 v[216:219], a[60:63], v[156:159], v[216:219]\n"
"ds_write_b128 v3, v[164:167] offset:10240\n"
"v_mfma_f32_16x16x32_bf16 v[100:103], a[64:67], v[156:159], v[100:103]\n"
"buffer_load_dwordx4 v[136:139], v0, s[4:7], s98 offen offset:2048 sc1\n"
"v_mfma_f32_16x16x32_bf16 v[220:223], a[68:71], v[156:159], v[220:223]\n"
"v_fmac_f32_e32 v197, v255, v201\n"
"v_fmac_f32_e32 v198, v255, v202\n"
"v_mfma_f32_16x16x32_bf16 v[124:127], a[72:75], v[140:143], v[124:127]\n"
"ds_write_b128 v3, v[168:171] offset:11264\n"
"v_mfma_f32_16x16x32_bf16 v[208:211], a[76:79], v[140:143], v[208:211]\n"
"v_fmac_f32_e32 v199, v255, v203\n"
"ds_read_b128 v[148:151], v4 offset:3072\n"
"v_mfma_f32_16x16x32_bf16 v[212:215], a[80:83], v[140:143], v[212:215]\n"
"ds_read_b128 v[156:159], v9 offset:704\n"
"ds_read_b128 v[160:163], v4 offset:4096\n"
"v_mfma_f32_16x16x32_bf16 v[216:219], a[84:87], v[140:143], v[216:219]\n"
"ds_read_b128 v[164:167], v9 offset:768\n"
"ds_read_b128 v[168:171], v4 offset:5120\n"
"v_mfma_f32_16x16x32_bf16 v[100:103], a[88:91], v[140:143], v[100:103]\n"
"buffer_load_dwordx4 v[184:187], v0, s[4:7], s98 offen offset:3072 sc1\n"
"v_mfma_f32_16x16x32_bf16 v[220:223], a[92:95], v[140:143], v[220:223]\n"
"s_waitcnt vmcnt(19)\n"
"ds_read_b128 v[140:143], v9 offset:832\n"
"v_mfma_f32_16x16x32_bf16 v[124:127], a[96:99], v[120:123], v[124:127]\n"
"v_lshlrev_b32_e32 v253, 16, v253\n"
"v_mfma_f32_16x16x32_bf16 v[208:211], a[100:103], v[120:123], v[208:211]\n"
"v_sqrt_f32_e32 v253, v253\n"
"v_mfma_f32_16x16x32_bf16 v[212:215], a[104:107], v[120:123], v[212:215]\n"
"v_cvt_pk_bf16_f32 v253, v253, 0\n"
"v_lshlrev_b32_e32 v253, 16, v253\n"
"v_mfma_f32_16x16x32_bf16 v[216:219], a[108:111], v[120:123], v[216:219]\n"
"v_rcp_f32_e32 v254, v253\n"
"v_mfma_f32_16x16x32_bf16 v[100:103], a[112:115], v[120:123], v[100:103]\n"
"buffer_load_dwordx4 v[192:195], v1, s[4:7], s98 offen sc1\n"
"v_mfma_f32_16x16x32_bf16 v[220:223], a[116:119], v[120:123], v[220:223]\n"
"s_waitcnt lgkmcnt(0)\n"
"v_fmac_f32_e32 v148, v255, v156\n"
"v_mfma_f32_16x16x32_bf16 v[124:127], a[120:123], v[144:147], v[124:127]\n"
"s_add_u32 s96, s60, 0x848000\n"
"v_fmac_f32_e32 v149, v255, v157\n"
"v_add_u32_e32 v253, 0x26c00, v0\n"
"v_mfma_f32_16x16x32_bf16 v[208:211], a[124:127], v[144:147], v[208:211]\n"
"v_fmac_f32_e32 v150, v255, v158\n"
"v_fmac_f32_e32 v151, v255, v159\n"
"ds_read_b128 v[120:123], v253\n"
"v_mfma_f32_16x16x32_bf16 v[212:215], v[24:27], v[144:147], v[212:215]\n"
"buffer_store_dwordx4 v[96:99], v0, s[8:11], s96 offen sc1\n"
"v_mfma_f32_16x16x32_bf16 v[216:219], v[28:31], v[144:147], v[216:219]\n"
"buffer_load_dwordx4 a[204:207], v0, s[4:7], s97 offen offset:3072 sc1\n"
"ds_read_b128 v[156:159], v253 offset:1024\n"
"v_mfma_f32_16x16x32_bf16 v[100:103], v[32:35], v[144:147], v[100:103]\n"
"buffer_load_dwordx4 v[200:203], v1, s[4:7], s98 offen offset:1024 sc1\n"
"v_xor_b32_e32 v253, 32, v15\n"
"v_mfma_f32_16x16x32_bf16 v[220:223], v[36:39], v[144:147], v[220:223]\n"
"s_waitcnt vmcnt(20)\n"
"ds_read_b128 v[144:147], v2 offset:50176\n"
"v_mfma_f32_16x16x32_bf16 v[124:127], v[40:43], v[92:95], v[124:127]\n"
"s_add_u32 s96, s68, 0x11000\n"
"v_fmac_f32_e32 v160, v255, v164\n"
"ds_read_b128 v[224:227], v15 offset:24576\n"
"v_mfma_f32_16x16x32_bf16 v[208:211], v[44:47], v[92:95], v[208:211]\n"
"buffer_store_dwordx2 v[18:19], v11, s[12:15], s96 offen offset:64 sc1\n"
"v_mfma_f32_16x16x32_bf16 v[212:215], v[48:51], v[92:95], v[212:215]\n"
"v_fmac_f32_e32 v161, v255, v165\n"
"v_fmac_f32_e32 v162, v255, v166\n"
"ds_read_b128 v[228:231], v253 offset:24832\n"
"v_mfma_f32_16x16x32_bf16 v[216:219], v[52:55], v[92:95], v[216:219]\n"
"v_fmac_f32_e32 v163, v255, v167\n"
"v_fmac_f32_e32 v168, v255, v140\n"
"v_mfma_f32_16x16x32_bf16 v[100:103], v[56:59], v[92:95], v[100:103]\n"
"buffer_load_dwordx4 v[164:167], v1, s[4:7], s98 offen offset:2048 sc1\n"
"v_mfma_f32_16x16x32_bf16 v[220:223], v[60:63], v[92:95], v[220:223]\n"
"v_fmac_f32_e32 v169, v255, v141\n"
"v_fmac_f32_e32 v170, v255, v142\n"
"v_mfma_f32_16x16x32_bf16 v[124:127], v[64:67], v[88:91], v[124:127]\n"
"v_fmac_f32_e32 v171, v255, v143\n"
"v_mul_f32_e32 v255, 0x3e8293ee, v254\n"
"v_mfma_f32_16x16x32_bf16 v[208:211], v[68:71], v[88:91], v[208:211]\n"
"s_waitcnt lgkmcnt(0)\n"
"v_mfma_f32_16x16x32_bf16 v[120:123], v[224:227], v[144:147], v[120:123]\n"
"v_mfma_f32_16x16x32_bf16 v[156:159], v[228:231], v[144:147], v[156:159]\n"
"v_xor_b32_e32 v253, 32, v16\n"
"ds_read_b64_tr_b16 v[92:93], v16 offset:24576\n"
"v_mfma_f32_16x16x32_bf16 v[212:215], v[72:75], v[88:91], v[212:215]\n"
"ds_read_b64_tr_b16 v[94:95], v253 offset:24832\n"
"ds_read_b64_tr_b16 v[140:141], v253 offset:24576\n"
"ds_read_b64_tr_b16 v[142:143], v16 offset:24832\n"
"v_mfma_f32_16x16x32_bf16 v[216:219], v[76:79], v[88:91], v[216:219]\n"
"v_mov_b32_e32 v144, 0x3f803f80\n"
"v_mov_b32_e32 v145, 0x3f803f80\n"
"v_mov_b32_e32 v146, 0x3f803f80\n"
"v_mfma_f32_16x16x32_bf16 v[100:103], v[80:83], v[88:91], v[100:103]\n"
"buffer_load_dwordx4 v[224:227], v1, s[4:7], s98 offen offset:3072 sc1\n"
"v_mov_b32_e32 v147, 0x3f803f80\n"
"v_mfma_f32_16x16x32_bf16 v[220:223], v[84:87], v[88:91], v[220:223]\n"
"s_waitcnt vmcnt(20)\n"
"v_max_f32_e32 v253, v120, v121\n"
"v_max3_f32 v253, v253, v122, v123\n"
"; @B G1 2.3\n"
"v_mfma_f32_16x16x32_bf16 v[152:155], a[0:3], v[108:111], v[152:155]\n"
"v_max3_f32 v253, v253, v156, v157\n"
"v_max3_f32 v253, v253, v158, v159\n"
"v_mov_b32_e32 v252, v253\n"
"v_mfma_f32_16x16x32_bf16 v[188:191], a[4:7], v[108:111], v[188:191]\n"
"s_nop 0\n"
"v_permlane16_swap_b32_e32 v253, v252\n"
"v_max_f32_e32 v253, v253, v252\n"
"v_mov_b32_e32 v252, v253\n"
"v_mfma_f32_16x16x32_bf16 v[196:199], a[8:11], v[108:111], v[196:199]\n"
"s_nop 0\n"
"v_permlane32_swap_b32_e32 v253, v252\n"
"v_max_f32_e32 v253, v253, v252\n"
"v_sub_f32_e32 v120, v120, v253\n"
"v_mfma_f32_16x16x32_bf16 v[148:151], a[12:15], v[108:111], v[148:151]\n"
"ds_write_b128 v3, v[124:127] offset:12288\n"
"v_mfma_f32_16x16x32_bf16 v[160:163], a[16:19], v[108:111], v[160:163]\n"
"s_add_u32 s98, s53, 0x400000\n"
"v_sub_f32_e32 v121, v121, v253\n"
"v_sub_f32_e32 v122, v122, v253\n"
"v_mfma_f32_16x16x32_bf16 v[168:171], a[20:23], v[108:111], v[168:171]\n"
"buffer_load_dwordx4 v[88:91], v0, s[4:7], s98 offen sc1\n"
"v_sub_f32_e32 v123, v123, v253\n"
"v_mfma_f32_16x16x32_bf16 v[152:155], a[24:27], v[176:179], v[152:155]\n"
"ds_write_b128 v3, v[208:211] offset:13312\n"
"v_mfma_f32_16x16x32_bf16 v[188:191], a[28:31], v[176:179], v[188:191]\n"
"v_sub_f32_e32 v156, v156, v253\n"
"v_sub_f32_e32 v157, v157, v253\n"
"v_sub_f32_e32 v158, v158, v253\n"
"v_mfma_f32_16x16x32_bf16 v[196:199], a[32:35], v[176:179], v[196:199]\n"
"v_sub_f32_e32 v159, v159, v253\n"
"v_exp_f32_e32 v120, v120\n"
"v_mfma_f32_16x16x32_bf16 v[148:151], a[36:39], v[176:179], v[148:151]\n"
"ds_write_b128 v3, v[212:215] offset:14336\n"
"v_mfma_f32_16x16x32_bf16 v[160:163], a[40:43], v[176:179], v[160:163]\n"
"buffer_load_dwordx4 v[108:111], v0, s[4:7], s98 offen offset:1024 sc1\n"
"v_mfma_f32_16x16x32_bf16 v[168:171], a[44:47], v[176:179], v[168:171]\n"
"s_waitcnt vmcnt(20)\n"
"v_exp_f32_e32 v121, v121\n"
"v_mfma_f32_16x16x32_bf16 v[152:155], a[48:51], v[172:175], v[152:155]\n"
"ds_write_b128 v3, v[216:219] offset:15360\n"
"v_mfma_f32_16x16x32_bf16 v[188:191], a[52:55], v[172:175], v[188:191]\n"
"buffer_load_dwordx4 a[208:211], v1, s[4:7], s97 offen sc1\n"
"v_mfma_f32_16x16x32_bf16 v[196:199], a[56:59], v[172:175], v[196:199]\n"
"v_exp_f32_e32 v122, v122\n"
"v_mfma_f32_16x16x32_bf16 v[148:151], a[60:63], v[172:175], v[148:151]\n"
"ds_write_b128 v3, v[100:103] offset:16384\n"
"v_mfma_f32_16x16x32_bf16 v[160:163], a[64:67], v[172:175], v[160:163]\n"
"buffer_load_dwordx4 v[100:103], v0, s[4:7], s98 offen offset:2048 sc1\n"
"v_mfma_f32_16x16x32_bf16 v[168:171], a[68:71], v[172:175], v[168:171]\n"
"v_exp_f32_e32 v123, v123\n"
"v_mfma_f32_16x16x32_bf16 v[152:155], a[72:75], v[132:135], v[152:155]\n"
"ds_write_b128 v3, v[220:223] offset:17408\n"
"v_mfma_f32_16x16x32_bf16 v[188:191], a[76:79], v[132:135], v[188:191]\n"
"v_exp_f32_e32 v156, v156\n"
"v_mfma_f32_16x16x32_bf16 v[196:199], a[80:83], v[132:135], v[196:199]\n"
"v_exp_f32_e32 v157, v157\n"
"v_mfma_f32_16x16x32_bf16 v[148:151], a[84:87], v[132:135], v[148:151]\n"
"s_waitcnt lgkmcnt(0)\n"
"s_barrier\n"
"v_mfma_f32_16x16x32_bf16 v[160:163], a[88:91], v[132:135], v[160:163]\n"
"buffer_load_dwordx4 v[124:127], v0, s[4:7], s98 offen offset:3072 sc1\n"
"v_mfma_f32_16x16x32_bf16 v[168:171], a[92:95], v[132:135], v[168:171]\n"
"s_waitcnt vmcnt(19)\n"
"ds_read_b128 v[132:135], v5 offset:2048\n"
"v_mfma_f32_16x16x32_bf16 v[152:155], a[96:99], v[116:119], v[152:155]\n"
"ds_read_b128 v[172:175], v6 offset:2048\n"
"ds_read_b128 v[176:179], v5 offset:3072\n"
"v_mfma_f32_16x16x32_bf16 v[188:191], a[100:103], v[116:119], v[188:191]\n"
"ds_read_b128 v[208:211], v6 offset:3072\n"
"v_exp_f32_e32 v158, v158\n"
"v_mfma_f32_16x16x32_bf16 v[196:199], a[104:107], v[116:119], v[196:199]\n"
"v_exp_f32_e32 v159, v159\n"
"v_cvt_pk_bf16_f32 v212, v120, v121\n"
"v_mfma_f32_16x16x32_bf16 v[148:151], a[108:111], v[116:119], v[148:151]\n"
"v_cvt_pk_bf16_f32 v213, v122, v123\n"
"v_cvt_pk_bf16_f32 v214, v156, v157\n"
"v_cvt_pk_bf16_f32 v215, v158, v159\n"
"v_mfma_f32_16x16x32_bf16 v[160:163], a[112:115], v[116:119], v[160:163]\n"
"buffer_load_dwordx4 v[120:123], v1, s[4:7], s98 offen sc1\n"
"v_mfma_f32_16x16x32_bf16 v[156:159], v[144:147], v[212:215], 0\n"
"v_mfma_f32_16x16x32_bf16 v[144:147], v[92:95], v[212:215], 0\n"
"v_mfma_f32_16x16x32_bf16 v[92:95], v[140:143], v[212:215], 0\n"
"v_mfma_f32_16x16x32_bf16 v[168:171], a[116:119], v[116:119], v[168:171]\n"
"ds_read_b128 v[116:119], v5 offset:4096\n"
"ds_read_b128 v[140:143], v6 offset:4096\n"
"v_mfma_f32_16x16x32_bf16 v[152:155], a[120:123], v[128:131], v[152:155]\n"
"ds_read_b128 v[212:215], v5 offset:5120\n"
"ds_read_b128 v[216:219], v6 offset:5120\n"
"v_mfma_f32_16x16x32_bf16 v[188:191], a[124:127], v[128:131], v[188:191]\n"
"s_waitcnt lgkmcnt(4)\n"
"v_add_f32_e32 v132, v132, v172\n"
"v_mfma_f32_16x16x32_bf16 v[196:199], v[24:27], v[128:131], v[196:199]\n"
"v_add_f32_e32 v133, v133, v173\n"
"v_add_f32_e32 v134, v134, v174\n"
"v_mfma_f32_16x16x32_bf16 v[148:151], v[28:31], v[128:131], v[148:151]\n"
"ds_read_b128 v[220:223], v5\n"
"ds_read_b128 v[228:231], v6\n"
"v_mfma_f32_16x16x32_bf16 v[160:163], v[32:35], v[128:131], v[160:163]\n"
"buffer_load_dwordx4 v[232:235], v1, s[4:7], s98 offen offset:1024 sc1\n"
"v_mfma_f32_16x16x32_bf16 v[168:171], v[36:39], v[128:131], v[168:171]\n"
"s_waitcnt vmcnt(19)\n"
"ds_read_b128 v[128:131], v5 offset:1024\n"
"v_mfma_f32_16x16x32_bf16 v[152:155], v[40:43], v[180:183], v[152:155]\n"
"ds_read_b128 v[236:239], v6 offset:1024\n"
"v_add_f32_e32 v135, v135, v175\n"
"v_mfma_f32_16x16x32_bf16 v[188:191], v[44:47], v[180:183], v[188:191]\n"
"buffer_load_dwordx4 a[212:215], v1, s[4:7], s97 offen offset:1024 sc1\n"
"v_mfma_f32_16x16x32_bf16 v[196:199], v[48:51], v[180:183], v[196:199]\n"
"s_add_u32 s96, s60, 0x840000\n"
"v_add_f32_e32 v176, v176, v208\n"
"v_mfma_f32_16x16x32_bf16 v[148:151], v[52:55], v[180:183], v[148:151]\n"
"v_add_f32_e32 v177, v177, v209\n"
"v_add_f32_e32 v178, v178, v210\n"
"v_mfma_f32_16x16x32_bf16 v[160:163], v[56:59], v[180:183], v[160:163]\n"
"buffer_load_dwordx4 v[172:175], v1, s[4:7], s98 offen offset:2048 sc1\n"
"v_mfma_f32_16x16x32_bf16 v[168:171], v[60:63], v[180:183], v[168:171]\n"
"buffer_store_dwordx4 v[96:99], v0, s[8:11], s96 offen sc1\n"
"v_mfma_f32_16x16x32_bf16 v[152:155], v[64:67], v[204:207], v[152:155]\n"
"s_add_u32 s96, s68, 0x10000\n"
"v_mfma_f32_16x16x32_bf16 v[188:191], v[68:71], v[204:207], v[188:191]\n"
"buffer_store_dwordx2 v[18:19], v11, s[4:7], s96 offen offset:32 sc1\n"
"v_mfma_f32_16x16x32_bf16 v[196:199], v[72:75], v[204:207], v[196:199]\n"
"v_add_f32_e32 v179, v179, v211\n"
"s_waitcnt lgkmcnt(4)\n"
"v_mfma_f32_16x16x32_bf16 v[148:151], v[76:79], v[204:207], v[148:151]\n"
"v_add_f32_e32 v116, v116, v140\n"
"v_add_f32_e32 v117, v117, v141\n"
"v_mfma_f32_16x16x32_bf16 v[160:163], v[80:83], v[204:207], v[160:163]\n"
"buffer_load_dwordx4 v[96:99], v1, s[4:7], s98 offen offset:3072 sc1\n"
"v_mfma_f32_16x16x32_bf16 v[168:171], v[84:87], v[204:207], v[168:171]\n"
"v_add_f32_e32 v118, v118, v142\n"
"v_add_f32_e32 v119, v119, v143\n"
"v_add_f32_e32 v212, v212, v216\n"
"v_add_f32_e32 v213, v213, v217\n"
"v_add_f32_e32 v214, v214, v218\n"
"v_add_f32_e32 v215, v215, v219\n"
"s_waitcnt lgkmcnt(0)\n"
"v_add_f32_e32 v220, v220, v228\n"
"v_add_f32_e32 v221, v221, v229\n"
"v_add_f32_e32 v222, v222, v230\n"
"v_add_f32_e32 v223, v223, v231\n"
"v_add_f32_e32 v128, v128, v236\n"
"v_add_f32_e32 v129, v129, v237\n"
"v_add_f32_e32 v130, v130, v238\n"
"v_add_f32_e32 v131, v131, v239\n"
"v_rcp_f32_e32 v156, v156\n"
"s_nop 0\n"
"v_mul_f32_e32 v144, v144, v156\n"
"v_mul_f32_e32 v145, v145, v156\n"
"v_mul_f32_e32 v146, v146, v156\n"
"v_mul_f32_e32 v147, v147, v156\n"
"v_mul_f32_e32 v92, v92, v156\n"
"v_mul_f32_e32 v93, v93, v156\n"
"v_mul_f32_e32 v94, v94, v156\n"
"v_mul_f32_e32 v95, v95, v156\n"
"v_cvt_pk_bf16_f32 v140, v144, v145\n"
"v_cvt_pk_bf16_f32 v141, v146, v147\n"
"v_cvt_pk_bf16_f32 v142, v92, v93\n"
"v_cvt_pk_bf16_f32 v143, v94, v95\n"
"s_add_u32 s98, s60, 0x448000\n"
"buffer_store_dwordx4 v[140:143], v0, s[4:7], s98 offen sc1\n"
".Lmk_b3:\n"
"s_waitcnt vmcnt(22)\n"
"; @B G1 3.0\n"
"v_mfma_f32_16x16x32_bf16 v[92:95], a[0:3], v[104:107], 0\n"
"s_add_u32 s98, s62, 0x40000\n"
"v_mfma_f32_16x16x32_bf16 v[140:143], a[4:7], v[104:107], 0\n"
"buffer_load_dwordx2 v[252:253], v10, s[4:7], s98 offen sc1\n"
"v_mfma_f32_16x16x32_bf16 v[144:147], a[8:11], v[104:107], 0\n"
"buffer_load_dwordx2 v[250:251], v10, s[4:7], s98 offen offset:128 sc1\n"
"v_mfma_f32_16x16x32_bf16 v[156:159], a[12:15], v[104:107], 0\n"
"buffer_load_dwordx2 v[248:249], v10, s[4:7], s98 offen offset:256 sc1\n"
"v_mfma_f32_16x16x32_bf16 v[180:183], a[16:19], v[104:107], 0\n"
"s_add_u32 s96, s54, 0x400000\n"
"v_add_f32_e32 v196, v196, v132\n"
"v_mfma_f32_16x16x32_bf16 v[204:207], a[20:23], v[104:107], 0\n"
"buffer_load_dwordx4 v[104:107], v0, s[4:7], s96 offen sc1\n"
"v_mfma_f32_16x16x32_bf16 v[92:95], a[24:27], v[112:115], v[92:95]\n"
"buffer_load_dwordx2 v[246:247], v10, s[4:7], s98 offen offset:384 sc1\n"
"v_mfma_f32_16x16x32_bf16 v[140:143], a[28:31], v[112:115], v[140:143]\n"
"v_add_f32_e32 v197, v197, v133\n"
"v_add_f32_e32 v198, v198, v134\n"
"v_mfma_f32_16x16x32_bf16 v[144:147], a[32:35], v[112:115], v[144:147]\n"
"buffer_load_dwordx4 a[216:219], v1, s[4:7], s97 offen offset:2048 sc1\n"
"v_mfma_f32_16x16x32_bf16 v[156:159], a[36:39], v[112:115], v[156:159]\n"
"v_add_f32_e32 v199, v199, v135\n"
"v_add_f32_e32 v148, v148, v176\n"
"v_mfma_f32_16x16x32_bf16 v[180:183], a[40:43], v[112:115], v[180:183]\n"
"buffer_load_dwordx4 v[132:135], v0, s[4:7], s96 offen offset:1024 sc1\n"
"v_mfma_f32_16x16x32_bf16 v[204:207], a[44:47], v[112:115], v[204:207]\n"
"s_waitcnt vmcnt(27)\n"
"v_add_f32_e32 v149, v149, v177\n"
"v_mfma_f32_16x16x32_bf16 v[92:95], a[48:51], v[136:139], v[92:95]\n"
"v_add_f32_e32 v150, v150, v178\n"
"v_add_f32_e32 v151, v151, v179\n"
"v_mfma_f32_16x16x32_bf16 v[140:143], a[52:55], v[136:139], v[140:143]\n"
"v_mul_f32_e32 v196, v196, v254\n"
"v_mul_f32_e32 v197, v197, v254\n"
"v_mfma_f32_16x16x32_bf16 v[144:147], a[56:59], v[136:139], v[144:147]\n"
"v_mul_f32_e32 v198, v198, v254\n"
"v_mul_f32_e32 v199, v199, v254\n"
"v_mfma_f32_16x16x32_bf16 v[156:159], a[60:63], v[136:139], v[156:159]\n"
"v_mul_f32_e32 v148, v148, v254\n"
"v_mul_f32_e32 v149, v149, v254\n"
"v_mfma_f32_16x16x32_bf16 v[180:183], a[64:67], v[136:139], v[180:183]\n"
"buffer_load_dwordx4 v[112:115], v0, s[4:7], s96 offen offset:2048 sc1\n"
"v_mfma_f32_16x16x32_bf16 v[204:207], a[68:71], v[136:139], v[204:207]\n"
"v_mul_f32_e32 v150, v150, v254\n"
"v_mul_f32_e32 v151, v151, v254\n"
"v_mfma_f32_16x16x32_bf16 v[92:95], a[72:75], v[184:187], v[92:95]\n"
"v_cvt_pk_bf16_f32 v136, v196, v197\n"
"v_cvt_pk_bf16_f32 v137, v198, v199\n"
"v_mfma_f32_16x16x32_bf16 v[140:143], a[76:79], v[184:187], v[140:143]\n"
"v_cvt_pk_bf16_f32 v138, v148, v149\n"
"v_cvt_pk_bf16_f32 v139, v150, v151\n"
"v_mfma_f32_16x16x32_bf16 v[144:147], a[80:83], v[184:187], v[144:147]\n"
"ds_write_b128 v8, v[136:139] offset:17408\n"
"v_mfma_f32_16x16x32_bf16 v[156:159], a[84:87], v[184:187], v[156:159]\n"
"v_add_f32_e32 v160, v160, v116\n"
"v_add_f32_e32 v161, v161, v117\n"
"v_mfma_f32_16x16x32_bf16 v[180:183], a[88:91], v[184:187], v[180:183]\n"
"buffer_load_dwordx4 v[136:139], v0, s[4:7], s96 offen offset:3072 sc1\n"
"v_mfma_f32_16x16x32_bf16 v[204:207], a[92:95], v[184:187], v[204:207]\n"
"s_waitcnt vmcnt(25)\n"
"v_add_f32_e32 v162, v162, v118\n"
"v_mfma_f32_16x16x32_bf16 v[92:95], a[96:99], v[192:195], v[92:95]\n"
"v_add_f32_e32 v163, v163, v119\n"
"v_add_f32_e32 v168, v168, v212\n"
"v_mfma_f32_16x16x32_bf16 v[140:143], a[100:103], v[192:195], v[140:143]\n"
"v_add_f32_e32 v169, v169, v213\n"
"v_add_f32_e32 v170, v170, v214\n"
"v_mfma_f32_16x16x32_bf16 v[144:147], a[104:107], v[192:195], v[144:147]\n"
"v_add_f32_e32 v171, v171, v215\n"
"v_mul_f32_e32 v160, v160, v254\n"
"v_mfma_f32_16x16x32_bf16 v[156:159], a[108:111], v[192:195], v[156:159]\n"
"v_mul_f32_e32 v161, v161, v254\n"
"v_mul_f32_e32 v162, v162, v254\n"
"v_mfma_f32_16x16x32_bf16 v[180:183], a[112:115], v[192:195], v[180:183]\n"
"buffer_load_dwordx4 v[116:119], v1, s[4:7], s96 offen sc1\n"
"v_mfma_f32_16x16x32_bf16 v[204:207], a[116:119], v[192:195], v[204:207]\n"
"v_mul_f32_e32 v163, v163, v254\n"
"v_mul_f32_e32 v168, v168, v254\n"
"v_mfma_f32_16x16x32_bf16 v[92:95], a[120:123], v[200:203], v[92:95]\n"
"v_mul_f32_e32 v169, v169, v254\n"
"v_mul_f32_e32 v170, v170, v254\n"
"v_mfma_f32_16x16x32_bf16 v[140:143], a[124:127], v[200:203], v[140:143]\n"
"v_mul_f32_e32 v171, v171, v254\n"
"v_cvt_pk_bf16_f32 v148, v160, v161\n"
"v_mfma_f32_16x16x32_bf16 v[144:147], v[24:27], v[200:203], v[144:147]\n"
"buffer_load_dwordx4 a[220:223], v1, s[4:7], s97 offen offset:3072 sc1\n"
"v_mfma_f32_16x16x32_bf16 v[156:159], v[28:31], v[200:203], v[156:159]\n"
"v_cvt_pk_bf16_f32 v149, v162, v163\n"
"v_cvt_pk_bf16_f32 v150, v168, v169\n"
"v_mfma_f32_16x16x32_bf16 v[180:183], v[32:35], v[200:203], v[180:183]\n"
"buffer_load_dwordx4 v[160:163], v1, s[4:7], s96 offen offset:1024 sc1\n"
"v_mfma_f32_16x16x32_bf16 v[204:207], v[36:39], v[200:203], v[204:207]\n"
"s_waitcnt vmcnt(25)\n"
"v_cvt_pk_bf16_f32 v151, v170, v171\n"
"v_mfma_f32_16x16x32_bf16 v[92:95], v[40:43], v[164:167], v[92:95]\n"
"ds_write_b128 v7, v[148:151] offset:50176\n"
"v_mfma_f32_16x16x32_bf16 v[140:143], v[44:47], v[164:167], v[140:143]\n"
"v_add_f32_e32 v152, v152, v220\n"
"v_add_f32_e32 v153, v153, v221\n"
"v_mfma_f32_16x16x32_bf16 v[144:147], v[48:51], v[164:167], v[144:147]\n"
"v_add_f32_e32 v154, v154, v222\n"
"v_add_f32_e32 v155, v155, v223\n"
"v_mfma_f32_16x16x32_bf16 v[156:159], v[52:55], v[164:167], v[156:159]\n"
"v_add_f32_e32 v188, v188, v128\n"
"v_add_f32_e32 v189, v189, v129\n"
"v_mfma_f32_16x16x32_bf16 v[180:183], v[56:59], v[164:167], v[180:183]\n"
"buffer_load_dwordx4 v[148:151], v1, s[4:7], s96 offen offset:2048 sc1\n"
"v_mfma_f32_16x16x32_bf16 v[204:207], v[60:63], v[164:167], v[204:207]\n"
"v_add_f32_e32 v190, v190, v130\n"
"v_add_f32_e32 v191, v191, v131\n"
"v_mfma_f32_16x16x32_bf16 v[92:95], v[64:67], v[224:227], v[92:95]\n"
"v_mul_f32_e32 v152, v152, v255\n"
"v_mul_f32_e32 v153, v153, v255\n"
"v_mfma_f32_16x16x32_bf16 v[140:143], v[68:71], v[224:227], v[140:143]\n"
"v_mul_f32_e32 v154, v154, v255\n"
"v_mul_f32_e32 v155, v155, v255\n"
"v_mfma_f32_16x16x32_bf16 v[144:147], v[72:75], v[224:227], v[144:147]\n"
"v_mul_f32_e32 v188, v188, v255\n"
"v_mul_f32_e32 v189, v189, v255\n"
"v_mfma_f32_16x16x32_bf16 v[156:159], v[76:79], v[224:227], v[156:159]\n"
"v_mul_f32_e32 v190, v190, v255\n"
"v_mul_f32_e32 v191, v191, v255\n"
"v_mfma_f32_16x16x32_bf16 v[180:183], v[80:83], v[224:227], v[180:183]\n"
"buffer_load_dwordx4 v[128:131], v1, s[4:7], s96 offen offset:3072 sc1\n"
"v_mfma_f32_16x16x32_bf16 v[204:207], v[84:87], v[224:227], v[204:207]\n"
"s_waitcnt vmcnt(25)\n"
"v_cvt_pk_bf16_f32 v164, v152, v153\n"
"; @B G1 3.1\n"
"v_mfma_f32_16x16x32_bf16 v[168:171], a[0:3], v[88:91], 0\n"
"v_cvt_pk_bf16_f32 v165, v154, v155\n"
"v_cvt_pk_bf16_f32 v166, v188, v189\n"
"v_mfma_f32_16x16x32_bf16 v[152:155], a[4:7], v[88:91], 0\n"
"v_cvt_pk_bf16_f32 v167, v190, v191\n"
"v_mfma_f32_16x16x32_bf16 v[176:179], a[8:11], v[88:91], 0\n"
"ds_write_b128 v2, v[164:167] offset:17408\n"
"v_mfma_f32_16x16x32_bf16 v[164:167], a[12:15], v[88:91], 0\n"
"ds_write_b128 v3, v[92:95]\n"
"v_mfma_f32_16x16x32_bf16 v[92:95], a[16:19], v[88:91], 0\n"
"s_add_u32 s98, s55, 0x400000\n"
"v_mfma_f32_16x16x32_bf16 v[184:187], a[20:23], v[88:91], 0\n"
"buffer_load_dwordx4 v[88:91], v0, s[4:7], s98 offen sc1\n"
"v_mfma_f32_16x16x32_bf16 v[168:171], a[24:27], v[108:111], v[168:171]\n"
"ds_write_b128 v3, v[140:143] offset:1024\n"
"v_mfma_f32_16x16x32_bf16 v[152:155], a[28:31], v[108:111], v[152:155]\n"
"s_add_u32 s97, s70, 0x206000\n"
"v_mfma_f32_16x16x32_bf16 v[176:179], a[32:35], v[108:111], v[176:179]\n"
"buffer_load_dwordx4 a[224:227], v0, s[4:7], s97 offen sc1\n"
"v_mfma_f32_16x16x32_bf16 v[164:167], a[36:39], v[108:111], v[164:167]\n"
"ds_write_b128 v3, v[144:147] offset:2048\n"
"v_mfma_f32_16x16x32_bf16 v[92:95], a[40:43], v[108:111], v[92:95]\n"
"buffer_load_dwordx4 v[140:143], v0, s[4:7], s98 offen offset:1024 sc1\n"
"v_mfma_f32_16x16x32_bf16 v[184:187], a[44:47], v[108:111], v[184:187]\n"
"s_waitcnt vmcnt(25)\n"
"v_mfma_f32_16x16x32_bf16 v[168:171], a[48:51], v[100:103], v[168:171]\n"
"ds_write_b128 v3, v[156:159] offset:3072\n"
"v_mfma_f32_16x16x32_bf16 v[152:155], a[52:55], v[100:103], v[152:155]\n"
"s_add_u32 s96, s73, 0\n"
"v_mfma_f32_16x16x32_bf16 v[176:179], a[56:59], v[100:103], v[176:179]\n"
"buffer_load_dwordx2 v[254:255], off, s[4:7], s96 sc1\n"
"v_mfma_f32_16x16x32_bf16 v[164:167], a[60:63], v[100:103], v[164:167]\n"
"ds_write_b128 v3, v[180:183] offset:4096\n"
"v_mfma_f32_16x16x32_bf16 v[92:95], a[64:67], v[100:103], v[92:95]\n"
"buffer_load_dwordx4 v[108:111], v0, s[4:7], s98 offen offset:2048 sc1\n"
"v_mfma_f32_16x16x32_bf16 v[184:187], a[68:71], v[100:103], v[184:187]\n"
"v_mfma_f32_16x16x32_bf16 v[168:171], a[72:75], v[124:127], v[168:171]\n"
"ds_write_b128 v3, v[204:207] offset:5120\n"
"v_mfma_f32_16x16x32_bf16 v[152:155], a[76:79], v[124:127], v[152:155]\n"
"v_mfma_f32_16x16x32_bf16 v[176:179], a[80:83], v[124:127], v[176:179]\n"
"v_mov_b32_e32 v100, -1\n"
"v_mov_b32_e32 v101, -1\n"
"v_mfma_f32_16x16x32_bf16 v[164:167], a[84:87], v[124:127], v[164:167]\n"
"v_mov_b32_e32 v102, -1\n"
"v_mov_b32_e32 v103, -1\n"
"v_mfma_f32_16x16x32_bf16 v[92:95], a[88:91], v[124:127], v[92:95]\n"
"buffer_load_dwordx4 v[144:147], v0, s[4:7], s98 offen offset:3072 sc1\n"
"v_mfma_f32_16x16x32_bf16 v[184:187], a[92:95], v[124:127], v[184:187]\n"
"s_waitcnt vmcnt(26)\n"
"s_add_u32 s95, s60, 0x800000\n"
"v_mfma_f32_16x16x32_bf16 v[168:171], a[96:99], v[120:123], v[168:171]\n"
"buffer_store_dwordx4 v[100:103], v0, s[8:11], s95 offen sc1\n"
"v_mfma_f32_16x16x32_bf16 v[152:155], a[100:103], v[120:123], v[152:155]\n"
"v_mfma_f32_16x16x32_bf16 v[176:179], a[104:107], v[120:123], v[176:179]\n"
"v_mfma_f32_16x16x32_bf16 v[164:167], a[108:111], v[120:123], v[164:167]\n"
"v_mfma_f32_16x16x32_bf16 v[92:95], a[112:115], v[120:123], v[92:95]\n"
"buffer_load_dwordx4 v[124:127], v1, s[4:7], s98 offen sc1\n"
"v_mfma_f32_16x16x32_bf16 v[184:187], a[116:119], v[120:123], v[184:187]\n"
"s_waitcnt lgkmcnt(0)\n"
"s_barrier\n"
"v_mfma_f32_16x16x32_bf16 v[168:171], a[120:123], v[232:235], v[168:171]\n"
"s_waitcnt vmcnt(17)\n"
"v_add_f32_e32 v252, v252, v250\n"
"v_mfma_f32_16x16x32_bf16 v[152:155], a[124:127], v[232:235], v[152:155]\n"
"v_add_f32_e32 v253, v253, v251\n"
"v_add_f32_e32 v248, v248, v246\n"
"v_mfma_f32_16x16x32_bf16 v[176:179], v[24:27], v[232:235], v[176:179]\n"
"buffer_load_dwordx4 a[228:231], v0, s[4:7], s97 offen offset:1024 sc1\n"
"v_mfma_f32_16x16x32_bf16 v[164:167], v[28:31], v[232:235], v[164:167]\n"
"v_add_f32_e32 v249, v249, v247\n"
"v_add_f32_e32 v252, v252, v248\n"
"v_mfma_f32_16x16x32_bf16 v[92:95], v[32:35], v[232:235], v[92:95]\n"
"buffer_load_dwordx4 v[120:123], v1, s[4:7], s98 offen offset:1024 sc1\n"
"v_mfma_f32_16x16x32_bf16 v[184:187], v[36:39], v[232:235], v[184:187]\n"
"v_add_f32_e32 v253, v253, v249\n"
"v_mfma_f32_16x16x32_bf16 v[168:171], v[40:43], v[172:175], v[168:171]\n"
"s_nop 0\n"
"v_permlane16_swap_b32_e32 v252, v253\n"
"v_add_f32_e32 v252, v252, v253\n"
"v_mfma_f32_16x16x32_bf16 v[152:155], v[44:47], v[172:175], v[152:155]\n"
"v_mov_b32_e32 v253, v252\n"
"v_add_u32_e32 v251, 0x26c00, v0\n"
"v_mfma_f32_16x16x32_bf16 v[176:179], v[48:51], v[172:175], v[176:179]\n"
"v_permlane32_swap_b32_e32 v252, v253\n"
"v_add_f32_e32 v252, v252, v253\n"
"v_mfma_f32_16x16x32_bf16 v[164:167], v[52:55], v[172:175], v[164:167]\n"
"v_mul_f32_e32 v252, 0x3a800000, v252\n"
"v_mov_b32_e32 v253, v252\n"
"v_mfma_f32_16x16x32_bf16 v[92:95], v[56:59], v[172:175], v[92:95]\n"
"buffer_load_dwordx4 v[156:159], v1, s[4:7], s98 offen offset:2048 sc1\n"
"v_mfma_f32_16x16x32_bf16 v[184:187], v[60:63], v[172:175], v[184:187]\n"
"v_permlane16_swap_b32_e32 v252, v253\n"
"v_cvt_pk_bf16_f32 v250, -v252, 0\n"
"v_mfma_f32_16x16x32_bf16 v[168:171], v[64:67], v[96:99], v[168:171]\n"
"v_fma_f32 v253, -v252, v252, v253\n"
"v_lshlrev_b32_e32 v250, 16, v250\n"
"v_mfma_f32_16x16x32_bf16 v[152:155], v[68:71], v[96:99], v[152:155]\n"
"ds_read_b128 v[172:175], v4\n"
"ds_read_b128 v[180:183], v9 offset:512\n"
"v_mfma_f32_16x16x32_bf16 v[176:179], v[72:75], v[96:99], v[176:179]\n"
"ds_read_b128 v[188:191], v4 offset:1024\n"
"ds_read_b128 v[192:195], v9 offset:576\n"
"v_mfma_f32_16x16x32_bf16 v[164:167], v[76:79], v[96:99], v[164:167]\n"
"ds_read_b128 v[196:199], v4 offset:2048\n"
"ds_read_b128 v[200:203], v9 offset:640\n"
"v_mfma_f32_16x16x32_bf16 v[92:95], v[80:83], v[96:99], v[92:95]\n"
"buffer_load_dwordx4 v[204:207], v1, s[4:7], s98 offen offset:3072 sc1\n"
"v_mfma_f32_16x16x32_bf16 v[184:187], v[84:87], v[96:99], v[184:187]\n"
"s_waitcnt vmcnt(19)\n"
"ds_read_b128 v[96:99], v251\n"
"; @B G1 3.2\n"
"v_mfma_f32_16x16x32_bf16 v[208:211], a[0:3], v[104:107], 0\n"
"ds_read_b128 v[212:215], v251 offset:1024\n"
"v_xor_b32_e32 v251, 32, v17\n"
"v_mfma_f32_16x16x32_bf16 v[216:219], a[4:7], v[104:107], 0\n"
"ds_read_b128 v[220:223], v2 offset:49152\n"
"ds_read_b128 v[224:227], v17 offset:23552\n"
"v_mfma_f32_16x16x32_bf16 v[228:231], a[8:11], v[104:107], 0\n"
"s_add_u32 s98, s60, 0x808000\n"
"s_waitcnt lgkmcnt(4)\n"
"v_mfma_f32_16x16x32_bf16 v[232:235], a[12:15], v[104:107], 0\n"
"ds_write_b128 v3, v[168:171] offset:6144\n"
"v_mfma_f32_16x16x32_bf16 v[168:171], a[16:19], v[104:107], 0\n"
"buffer_store_dwordx4 v[100:103], v0, s[8:11], s98 offen sc1\n"
"v_mfma_f32_16x16x32_bf16 v[236:239], a[20:23], v[104:107], 0\n"
"buffer_load_dwordx4 a[232:235], v0, s[4:7], s97 offen offset:2048 sc1\n"
"v_mfma_f32_16x16x32_bf16 v[208:211], a[24:27], v[132:135], v[208:211]\n"
"ds_write_b128 v3, v[152:155] offset:7168\n"
"v_mfma_f32_16x16x32_bf16 v[216:219], a[28:31], v[132:135], v[216:219]\n"
"v_fmac_f32_e32 v172, v250, v180\n"
"v_fmac_f32_e32 v173, v250, v181\n"
"v_mfma_f32_16x16x32_bf16 v[228:231], a[32:35], v[132:135], v[228:231]\n"
"v_fmac_f32_e32 v174, v250, v182\n"
"v_fmac_f32_e32 v175, v250, v183\n"
"v_mfma_f32_16x16x32_bf16 v[232:235], a[36:39], v[132:135], v[232:235]\n"
"ds_write_b128 v3, v[176:179] offset:8192\n"
"v_mfma_f32_16x16x32_bf16 v[168:171], a[40:43], v[132:135], v[168:171]\n"
"v_fmac_f32_e32 v188, v250, v192\n"
"v_fmac_f32_e32 v189, v250, v193\n"
"v_mfma_f32_16x16x32_bf16 v[236:239], a[44:47], v[132:135], v[236:239]\n"
"s_waitcnt vmcnt(19)\n"
"v_fmac_f32_e32 v190, v250, v194\n"
"v_mfma_f32_16x16x32_bf16 v[208:211], a[48:51], v[112:115], v[208:211]\n"
"ds_write_b128 v3, v[164:167] offset:9216\n"
"v_mfma_f32_16x16x32_bf16 v[216:219], a[52:55], v[112:115], v[216:219]\n"
"buffer_load_dwordx4 a[236:239], v0, s[4:7], s97 offen offset:3072 sc1\n"
"v_mfma_f32_16x16x32_bf16 v[228:231], a[56:59], v[112:115], v[228:231]\n"
"v_fmac_f32_e32 v191, v250, v195\n"
"v_fmac_f32_e32 v196, v250, v200\n"
"v_mfma_f32_16x16x32_bf16 v[232:235], a[60:63], v[112:115], v[232:235]\n"
"ds_write_b128 v3, v[92:95] offset:10240\n"
"v_mfma_f32_16x16x32_bf16 v[168:171], a[64:67], v[112:115], v[168:171]\n"
"v_fmac_f32_e32 v197, v250, v201\n"
"v_fmac_f32_e32 v198, v250, v202\n"
"v_mfma_f32_16x16x32_bf16 v[236:239], a[68:71], v[112:115], v[236:239]\n"
"v_fmac_f32_e32 v199, v250, v203\n"
"ds_read_b128 v[92:95], v4 offset:3072\n"
"v_mfma_f32_16x16x32_bf16 v[208:211], a[72:75], v[136:139], v[208:211]\n"
"ds_write_b128 v3, v[184:187] offset:11264\n"
"v_mfma_f32_16x16x32_bf16 v[216:219], a[76:79], v[136:139], v[216:219]\n"
"buffer_load_dwordx4 a[240:243], v1, s[4:7], s97 offen sc1\n"
"v_mfma_f32_16x16x32_bf16 v[228:231], a[80:83], v[136:139], v[228:231]\n"
"ds_read_b128 v[104:107], v9 offset:704\n"
"ds_read_b128 v[112:115], v4 offset:4096\n"
"v_mfma_f32_16x16x32_bf16 v[232:235], a[84:87], v[136:139], v[232:235]\n"
"ds_read_b128 v[132:135], v9 offset:768\n"
"ds_read_b128 v[152:155], v4 offset:5120\n"
"v_mfma_f32_16x16x32_bf16 v[168:171], a[88:91], v[136:139], v[168:171]\n"
"s_waitcnt lgkmcnt(13)\n"
"ds_read_b128 v[164:167], v9 offset:832\n"
"ds_read_b128 v[176:179], v251 offset:23808\n"
"v_mfma_f32_16x16x32_bf16 v[236:239], a[92:95], v[136:139], v[236:239]\n"
"s_waitcnt vmcnt(18)\n"
"v_max_f32_e32 v253, 0, v253\n"
"v_mfma_f32_16x16x32_bf16 v[208:211], a[96:99], v[116:119], v[208:211]\n"
"s_waitcnt vmcnt(12)\n"
"v_cmp_ne_u64_e32 vcc, v[254:255], v[18:19]\n"
"v_mfma_f32_16x16x32_bf16 v[216:219], a[100:103], v[116:119], v[216:219]\n"
"buffer_load_dwordx4 a[244:247], v1, s[4:7], s97 offen offset:1024 sc1\n"
"v_mfma_f32_16x16x32_bf16 v[228:231], a[104:107], v[116:119], v[228:231]\n"
"s_cbranch_vccnz .Lmk_f47s\n"
".Lmk_f47r:\n"
"s_mov_b32 m0, 0x7c00\n"
"v_mfma_f32_16x16x32_bf16 v[232:235], a[108:111], v[116:119], v[232:235]\n"
"s_add_u32 s98, s66, 0x3c00\n"
"s_waitcnt lgkmcnt(1)\n"
"v_mfma_f32_16x16x32_bf16 v[168:171], a[112:115], v[116:119], v[168:171]\n"
"buffer_load_dwordx4 v0, s[4:7], s98 offen sc1 lds\n"
"v_mfma_f32_16x16x32_bf16 v[236:239], a[116:119], v[116:119], v[236:239]\n"
"s_mov_b32 m0, 0xfc00\n"
"s_add_u32 s98, s66, 0x7c00\n"
"v_mfma_f32_16x16x32_bf16 v[208:211], a[120:123], v[160:163], v[208:211]\n"
"s_add_u32 s96, s61, 0xc40000\n"
"v_fmac_f32_e32 v92, v250, v104\n"
"v_mfma_f32_16x16x32_bf16 v[216:219], a[124:127], v[160:163], v[216:219]\n"
"buffer_load_dwordx4 a[248:251], v1, s[4:7], s97 offen offset:2048 sc1\n"
"v_mfma_f32_16x16x32_bf16 v[228:231], v[24:27], v[160:163], v[228:231]\n"
"buffer_store_dwordx4 v[100:103], v0, s[8:11], s96 offen sc1\n"
"v_mfma_f32_16x16x32_bf16 v[232:235], v[28:31], v[160:163], v[232:235]\n"
"buffer_load_dwordx4 v0, s[4:7], s98 offen sc1 lds\n"
"v_mfma_f32_16x16x32_bf16 v[168:171], v[32:35], v[160:163], v[168:171]\n"
"s_add_u32 s98, s67, 0x10020\n"
"v_fmac_f32_e32 v93, v250, v105\n"
"v_mfma_f32_16x16x32_bf16 v[236:239], v[36:39], v[160:163], v[236:239]\n"
"v_fmac_f32_e32 v94, v250, v106\n"
"v_mfma_f32_16x16x32_bf16 v[208:211], v[40:43], v[148:151], v[208:211]\n"
"buffer_load_dwordx2 v[254:255], v12, s[4:7], s98 offen sc1\n"
"v_mfma_f32_16x16x32_bf16 v[216:219], v[44:47], v[148:151], v[216:219]\n"
"buffer_load_dwordx4 a[252:255], v1, s[4:7], s97 offen offset:3072 sc1\n"
"v_mfma_f32_16x16x32_bf16 v[228:231], v[48:51], v[148:151], v[228:231]\n"
"v_fmac_f32_e32 v95, v250, v107\n"
"v_fmac_f32_e32 v112, v250, v132\n"
"v_mfma_f32_16x16x32_bf16 v[232:235], v[52:55], v[148:151], v[232:235]\n"
"v_fmac_f32_e32 v113, v250, v133\n"
"v_fmac_f32_e32 v114, v250, v134\n"
"v_mfma_f32_16x16x32_bf16 v[168:171], v[56:59], v[148:151], v[168:171]\n"
"v_fmac_f32_e32 v115, v250, v135\n"
"v_fmac_f32_e32 v152, v250, v164\n"
"v_mfma_f32_16x16x32_bf16 v[236:239], v[60:63], v[148:151], v[236:239]\n"
"v_fmac_f32_e32 v153, v250, v165\n"
"v_fmac_f32_e32 v154, v250, v166\n"
"v_mfma_f32_16x16x32_bf16 v[208:211], v[64:67], v[128:131], v[208:211]\n"
"v_fmac_f32_e32 v155, v250, v167\n"
"s_waitcnt lgkmcnt(0)\n"
"v_mfma_f32_16x16x32_bf16 v[96:99], v[224:227], v[220:223], v[96:99]\n"
"v_mfma_f32_16x16x32_bf16 v[212:215], v[176:179], v[220:223], v[212:215]\n"
"v_mfma_f32_16x16x32_bf16 v[216:219], v[68:71], v[128:131], v[216:219]\n"
"v_xor_b32_e32 v251, 32, v16\n"
"ds_read_b64_tr_b16 v[104:105], v16 offset:23552\n"
"v_mfma_f32_16x16x32_bf16 v[228:231], v[72:75], v[128:131], v[228:231]\n"
"ds_read_b64_tr_b16 v[106:107], v251 offset:23808\n"
"ds_read_b64_tr_b16 v[116:117], v251 offset:23552\n"
"v_mfma_f32_16x16x32_bf16 v[232:235], v[76:79], v[128:131], v[232:235]\n"
"ds_read_b64_tr_b16 v[118:119], v16 offset:23808\n"
"v_mov_b32_e32 v132, 0x3f803f80\n"
"v_mfma_f32_16x16x32_bf16 v[168:171], v[80:83], v[128:131], v[168:171]\n"
"v_mov_b32_e32 v133, 0x3f803f80\n"
"v_mov_b32_e32 v134, 0x3f803f80\n"
"v_mfma_f32_16x16x32_bf16 v[236:239], v[84:87], v[128:131], v[236:239]\n"
"v_mov_b32_e32 v135, 0x3f803f80\n"
"; @B G1 3.3\n"
"v_mfma_f32_16x16x32_bf16 v[172:175], a[0:3], v[88:91], v[172:175]\n"
"v_max_f32_e32 v251, v96, v97\n"
"v_max3_f32 v251, v251, v98, v99\n"
"v_mfma_f32_16x16x32_bf16 v[188:191], a[4:7], v[88:91], v[188:191]\n"
"v_max3_f32 v251, v251, v212, v213\n"
"v_max3_f32 v251, v251, v214, v215\n"
"v_mfma_f32_16x16x32_bf16 v[196:199], a[8:11], v[88:91], v[196:199]\n"
"v_mov_b32_e32 v250, v251\n"
"v_cvt_pk_bf16_f32 v253, v253, 0\n"
"v_mfma_f32_16x16x32_bf16 v[92:95], a[12:15], v[88:91], v[92:95]\n"
"ds_write_b128 v3, v[208:211] offset:12288\n"
"v_mfma_f32_16x16x32_bf16 v[112:115], a[16:19], v[88:91], v[112:115]\n"
"v_permlane16_swap_b32_e32 v251, v250\n"
"v_max_f32_e32 v251, v251, v250\n"
"v_mfma_f32_16x16x32_bf16 v[152:155], a[20:23], v[88:91], v[152:155]\n"
"v_mov_b32_e32 v250, v251\n"
"v_lshlrev_b32_e32 v253, 16, v253\n"
"v_mfma_f32_16x16x32_bf16 v[172:175], a[24:27], v[140:143], v[172:175]\n"
"ds_write_b128 v3, v[216:219] offset:13312\n"
"v_mfma_f32_16x16x32_bf16 v[188:191], a[28:31], v[140:143], v[188:191]\n"
"v_permlane32_swap_b32_e32 v251, v250\n"
"v_max_f32_e32 v251, v251, v250\n"
"v_mfma_f32_16x16x32_bf16 v[196:199], a[32:35], v[140:143], v[196:199]\n"
"v_sub_f32_e32 v96, v96, v251\n"
"v_sub_f32_e32 v97, v97, v251\n"
"v_mfma_f32_16x16x32_bf16 v[92:95], a[36:39], v[140:143], v[92:95]\n"
"ds_write_b128 v3, v[228:231] offset:14336\n"
"v_mfma_f32_16x16x32_bf16 v[112:115], a[40:43], v[140:143], v[112:115]\n"
"s_add_u32 s97, s61, 0xc40000\n"
"v_sub_f32_e32 v98, v98, v251\n"
"v_mfma_f32_16x16x32_bf16 v[152:155], a[44:47], v[140:143], v[152:155]\n"
"s_waitcnt vmcnt(17)\n"
"v_sub_f32_e32 v99, v99, v251\n"
"v_mfma_f32_16x16x32_bf16 v[172:175], a[48:51], v[108:111], v[172:175]\n"
"ds_write_b128 v3, v[232:235] offset:15360\n"
"v_mfma_f32_16x16x32_bf16 v[188:191], a[52:55], v[108:111], v[188:191]\n"
"buffer_store_dwordx4 v[100:103], v0, s[8:11], s97 offen offset:1024 sc1\n"
"v_mfma_f32_16x16x32_bf16 v[196:199], a[56:59], v[108:111], v[196:199]\n"
"v_sub_f32_e32 v212, v212, v251\n"
"v_sub_f32_e32 v213, v213, v251\n"
"v_mfma_f32_16x16x32_bf16 v[92:95], a[60:63], v[108:111], v[92:95]\n"
"ds_write_b128 v3, v[168:171] offset:16384\n"
"v_mfma_f32_16x16x32_bf16 v[112:115], a[64:67], v[108:111], v[112:115]\n"
"v_sub_f32_e32 v214, v214, v251\n"
"v_sub_f32_e32 v215, v215, v251\n"
"v_mfma_f32_16x16x32_bf16 v[152:155], a[68:71], v[108:111], v[152:155]\n"
"v_exp_f32_e32 v96, v96\n"
"v_mfma_f32_16x16x32_bf16 v[172:175], a[72:75], v[144:147], v[172:175]\n"
"ds_write_b128 v3, v[236:239] offset:17408\n"
"v_mfma_f32_16x16x32_bf16 v[188:191], a[76:79], v[144:147], v[188:191]\n"
"v_exp_f32_e32 v97, v97\n"
"v_mfma_f32_16x16x32_bf16 v[196:199], a[80:83], v[144:147], v[196:199]\n"
"v_exp_f32_e32 v98, v98\n"
"v_mfma_f32_16x16x32_bf16 v[92:95], a[84:87], v[144:147], v[92:95]\n"
"v_exp_f32_e32 v99, v99\n"
"v_mfma_f32_16x16x32_bf16 v[112:115], a[88:91], v[144:147], v[112:115]\n"
"v_exp_f32_e32 v212, v212\n"
"v_mfma_f32_16x16x32_bf16 v[152:155], a[92:95], v[144:147], v[152:155]\n"
"s_waitcnt vmcnt(14)\n"
"v_add_f32_e32 v253, 0x3727c5ac, v253\n"
"v_mfma_f32_16x16x32_bf16 v[172:175], a[96:99], v[124:127], v[172:175]\n"
"v_exp_f32_e32 v213, v213\n"
"v_mfma_f32_16x16x32_bf16 v[188:191], a[100:103], v[124:127], v[188:191]\n"
"v_exp_f32_e32 v214, v214\n"
"v_mfma_f32_16x16x32_bf16 v[196:199], a[104:107], v[124:127], v[196:199]\n"
"v_exp_f32_e32 v215, v215\n"
"v_mfma_f32_16x16x32_bf16 v[92:95], a[108:111], v[124:127], v[92:95]\n"
"v_cvt_pk_bf16_f32 v88, v96, v97\n"
"v_cvt_pk_bf16_f32 v89, v98, v99\n"
"v_mfma_f32_16x16x32_bf16 v[112:115], a[112:115], v[124:127], v[112:115]\n"
"v_cvt_pk_bf16_f32 v90, v212, v213\n"
"v_cvt_pk_bf16_f32 v91, v214, v215\n"
"v_mfma_f32_16x16x32_bf16 v[152:155], a[116:119], v[124:127], v[152:155]\n"
"s_waitcnt lgkmcnt(6)\n"
"v_mfma_f32_16x16x32_bf16 v[96:99], v[132:135], v[88:91], 0\n"
"v_mfma_f32_16x16x32_bf16 v[108:111], v[104:107], v[88:91], 0\n"
"v_mfma_f32_16x16x32_bf16 v[104:107], v[116:119], v[88:91], 0\n"
"v_cvt_pk_bf16_f32 v253, v253, 0\n"
"v_mfma_f32_16x16x32_bf16 v[172:175], a[120:123], v[120:123], v[172:175]\n"
"s_waitcnt lgkmcnt(0)\n"
"s_barrier\n"
"v_mfma_f32_16x16x32_bf16 v[188:191], a[124:127], v[120:123], v[188:191]\n"
"ds_read_b128 v[88:91], v5 offset:2048\n"
"ds_read_b128 v[116:119], v6 offset:2048\n"
"v_mfma_f32_16x16x32_bf16 v[196:199], v[24:27], v[120:123], v[196:199]\n"
"ds_read_b128 v[24:27], v5 offset:3072\n"
"ds_read_b128 v[124:127], v6 offset:3072\n"
"v_mfma_f32_16x16x32_bf16 v[92:95], v[28:31], v[120:123], v[92:95]\n"
"v_lshlrev_b32_e32 v253, 16, v253\n"
"v_add_u32_e32 v251, 0x26c00, v0\n"
"v_mfma_f32_16x16x32_bf16 v[112:115], v[32:35], v[120:123], v[112:115]\n"
"v_rcp_f32_e32 v96, v96\n"
"v_mfma_f32_16x16x32_bf16 v[152:155], v[36:39], v[120:123], v[152:155]\n"
"s_waitcnt vmcnt(12)\n"
"v_mul_f32_e32 v108, v108, v96\n"
"v_mfma_f32_16x16x32_bf16 v[172:175], v[40:43], v[156:159], v[172:175]\n"
"v_mul_f32_e32 v109, v109, v96\n"
"v_mul_f32_e32 v110, v110, v96\n"
"v_mfma_f32_16x16x32_bf16 v[188:191], v[44:47], v[156:159], v[188:191]\n"
"v_mul_f32_e32 v111, v111, v96\n"
"v_mul_f32_e32 v104, v104, v96\n"
"v_mfma_f32_16x16x32_bf16 v[196:199], v[48:51], v[156:159], v[196:199]\n"
"v_mul_f32_e32 v105, v105, v96\n"
"v_mul_f32_e32 v106, v106, v96\n"
"v_mfma_f32_16x16x32_bf16 v[92:95], v[52:55], v[156:159], v[92:95]\n"
"s_add_u32 s97, s63, 0xc4000\n"
"v_mul_f32_e32 v107, v107, v96\n"
"v_mfma_f32_16x16x32_bf16 v[112:115], v[56:59], v[156:159], v[112:115]\n"
"v_cvt_pk_bf16_f32 v28, v108, v109\n"
"v_cvt_pk_bf16_f32 v29, v110, v111\n"
"v_mfma_f32_16x16x32_bf16 v[152:155], v[60:63], v[156:159], v[152:155]\n"
"buffer_store_dwordx2 v[100:101], v14, s[8:11], s97 offen sc1\n"
"v_mfma_f32_16x16x32_bf16 v[172:175], v[64:67], v[204:207], v[172:175]\n"
"v_cvt_pk_bf16_f32 v30, v104, v105\n"
"v_cvt_pk_bf16_f32 v31, v106, v107\n"
"v_mfma_f32_16x16x32_bf16 v[188:191], v[68:71], v[204:207], v[188:191]\n"
"s_add_u32 s97, s60, 0x440000\n"
"ds_read_b128 v[32:35], v5 offset:4096\n"
"v_mfma_f32_16x16x32_bf16 v[196:199], v[72:75], v[204:207], v[196:199]\n"
"buffer_store_dwordx4 v[28:31], v0, s[4:7], s97 offen sc1\n"
"v_mfma_f32_16x16x32_bf16 v[92:95], v[76:79], v[204:207], v[92:95]\n"
"ds_read_b128 v[28:31], v6 offset:4096\n"
"ds_read_b128 v[36:39], v5 offset:5120\n"
"v_mfma_f32_16x16x32_bf16 v[112:115], v[80:83], v[204:207], v[112:115]\n"
"ds_read_b128 v[40:43], v6 offset:5120\n"
"s_waitcnt lgkmcnt(4)\n"
"v_mfma_f32_16x16x32_bf16 v[152:155], v[84:87], v[204:207], v[152:155]\n"
"v_add_f32_e32 v88, v88, v116\n"
"v_add_f32_e32 v89, v89, v117\n"
"v_add_f32_e32 v90, v90, v118\n"
"v_add_f32_e32 v91, v91, v119\n"
"v_add_f32_e32 v24, v24, v124\n"
"v_add_f32_e32 v25, v25, v125\n"
"v_add_f32_e32 v26, v26, v126\n"
"v_add_f32_e32 v27, v27, v127\n"
"v_sqrt_f32_e32 v253, v253\n"
"ds_read_b128 v[44:47], v251\n"
"ds_read_b128 v[48:51], v251 offset:1024\n"
"v_add_u32_e32 v251, 0x26c00, v0\n"
"ds_read_b128 v[52:55], v251\n"
"ds_read_b128 v[56:59], v251 offset:1024\n"
"v_mov_b32_e32 v251, 0xff800000\n"
"v_cvt_pk_bf16_f32 v253, v253, 0\n"
"v_lshlrev_b32_e32 v253, 16, v253\n"
"v_rcp_f32_e32 v250, v253\n"
"s_add_u32 s97, s69, 0\n"
"buffer_load_dwordx4 a[0:3], v0, s[8:11], s97 offen sc1\n"
"ds_read_b128 v[60:63], v5\n"
"ds_read_b128 v[64:67], v6\n"
"ds_read_b128 v[68:71], v5 offset:1024\n"
"ds_read_b128 v[72:75], v6 offset:1024\n"
"v_mul_f32_e32 v253, 0x3e8293ee, v250\n"
"s_waitcnt lgkmcnt(8)\n"
"v_add_f32_e32 v32, v32, v28\n"
"v_add_f32_e32 v33, v33, v29\n"
"v_add_f32_e32 v34, v34, v30\n"
"v_add_f32_e32 v35, v35, v31\n"
"v_add_f32_e32 v36, v36, v40\n"
"v_add_f32_e32 v37, v37, v41\n"
"v_add_f32_e32 v38, v38, v42\n"
"v_add_f32_e32 v39, v39, v43\n"
"s_waitcnt lgkmcnt(4)\n"
"v_cndmask_b32_e64 v52, v52, v251, s[82:83]\n"
"v_cndmask_b32_e64 v53, v53, v251, s[82:83]\n"
"v_cndmask_b32_e64 v54, v54, v251, s[82:83]\n"
"v_cndmask_b32_e64 v55, v55, v251, s[82:83]\n"
"v_cndmask_b32_e64 v56, v56, v251, s[82:83]\n"
"v_cndmask_b32_e64 v57, v57, v251, s[82:83]\n"
"v_cndmask_b32_e64 v58, v58, v251, s[82:83]\n"
"v_cndmask_b32_e64 v59, v59, v251, s[82:83]\n"
"buffer_load_dwordx4 a[4:7], v0, s[8:11], s97 offen offset:1024 sc1\n"
"s_waitcnt lgkmcnt(0)\n"
"v_add_f32_e32 v60, v60, v64\n"
"v_add_f32_e32 v61, v61, v65\n"
"v_add_f32_e32 v62, v62, v66\n"
"v_add_f32_e32 v63, v63, v67\n"
"v_add_f32_e32 v68, v68, v72\n"
"v_add_f32_e32 v69, v69, v73\n"
"v_add_f32_e32 v70, v70, v74\n"
"v_add_f32_e32 v71, v71, v75\n"
"buffer_load_dwordx4 a[8:11], v0, s[8:11], s97 offen offset:2048 sc1\n"
"buffer_load_dwordx4 a[12:15], v0, s[8:11], s97 offen offset:3072 sc1\n"
"buffer_load_dwordx4 a[16:19], v1, s[8:11], s97 offen sc1\n"
"buffer_load_dwordx4 a[20:23], v1, s[8:11], s97 offen offset:1024 sc1\n"
"buffer_load_dwordx4 a[24:27], v1, s[8:11], s97 offen offset:2048 sc1\n"
"buffer_load_dwordx4 a[28:31], v1, s[8:11], s97 offen offset:3072 sc1\n"
".Lmk_b4:\n"
"; @B tail\n"
"s_add_u32 s97, s69, 0x2000\n"
"buffer_load_dwordx4 a[32:35], v0, s[8:11], s97 offen sc1\n"
"v_add_f32_e32 v196, v196, v88\n"
"v_add_f32_e32 v197, v197, v89\n"
"v_add_f32_e32 v198, v198, v90\n"
"v_add_f32_e32 v199, v199, v91\n"
"v_add_f32_e32 v92, v92, v24\n"
"v_add_f32_e32 v93, v93, v25\n"
"v_add_f32_e32 v94, v94, v26\n"
"v_add_f32_e32 v95, v95, v27\n"
"v_mul_f32_e32 v196, v196, v250\n"
"v_mul_f32_e32 v197, v197, v250\n"
"v_mul_f32_e32 v198, v198, v250\n"
"v_mul_f32_e32 v199, v199, v250\n"
"v_mul_f32_e32 v92, v92, v250\n"
"buffer_load_dwordx4 a[36:39], v0, s[8:11], s97 offen offset:1024 sc1\n"
"v_mul_f32_e32 v93, v93, v250\n"
"v_mul_f32_e32 v94, v94, v250\n"
"v_mul_f32_e32 v95, v95, v250\n"
"v_cvt_pk_bf16_f32 v24, v196, v197\n"
"v_cvt_pk_bf16_f32 v25, v198, v199\n"
"v_cvt_pk_bf16_f32 v26, v92, v93\n"
"v_cvt_pk_bf16_f32 v27, v94, v95\n"
"ds_write_b128 v7, v[24:27] offset:16384\n"
"buffer_load_dwordx4 a[40:43], v0, s[8:11], s97 offen offset:2048 sc1\n"
"v_add_f32_e32 v112, v112, v32\n"
"v_add_f32_e32 v113, v113, v33\n"
"v_add_f32_e32 v114, v114, v34\n"
"v_add_f32_e32 v115, v115, v35\n"
"v_add_f32_e32 v152, v152, v36\n"
"v_add_f32_e32 v153, v153, v37\n"
"v_add_f32_e32 v154, v154, v38\n"
"v_add_f32_e32 v155, v155, v39\n"
"v_mul_f32_e32 v112, v112, v250\n"
"v_mul_f32_e32 v113, v113, v250\n"
"v_mul_f32_e32 v114, v114, v250\n"
"v_mul_f32_e32 v115, v115, v250\n"
"v_mul_f32_e32 v152, v152, v250\n"
"buffer_load_dwordx4 a[44:47], v0, s[8:11], s97 offen offset:3072 sc1\n"
"v_mul_f32_e32 v153, v153, v250\n"
"v_mul_f32_e32 v154, v154, v250\n"
"v_mul_f32_e32 v155, v155, v250\n"
"v_cvt_pk_bf16_f32 v24, v112, v113\n"
"v_cvt_pk_bf16_f32 v25, v114, v115\n"
"v_cvt_pk_bf16_f32 v26, v152, v153\n"
"v_cvt_pk_bf16_f32 v27, v154, v155\n"
"ds_write_b128 v7, v[24:27] offset:49152\n"
"buffer_load_dwordx4 a[48:51], v1, s[8:11], s97 offen sc1\n"
"v_add_f32_e32 v172, v172, v60\n"
"v_add_f32_e32 v173, v173, v61\n"
"v_add_f32_e32 v174, v174, v62\n"
"v_add_f32_e32 v175, v175, v63\n"
"v_add_f32_e32 v188, v188, v68\n"
"v_add_f32_e32 v189, v189, v69\n"
"v_add_f32_e32 v190, v190, v70\n"
"v_add_f32_e32 v191, v191, v71\n"
"v_mul_f32_e32 v172, v172, v253\n"
"v_mul_f32_e32 v173, v173, v253\n"
"v_mul_f32_e32 v174, v174, v253\n"
"v_mul_f32_e32 v175, v175, v253\n"
"v_mul_f32_e32 v188, v188, v253\n"
"buffer_load_dwordx4 a[52:55], v1, s[8:11], s97 offen offset:1024 sc1\n"
"v_mul_f32_e32 v189, v189, v253\n"
"v_mul_f32_e32 v190, v190, v253\n"
"v_mul_f32_e32 v191, v191, v253\n"
"v_cvt_pk_bf16_f32 v24, v172, v173\n"
"v_cvt_pk_bf16_f32 v25, v174, v175\n"
"v_cvt_pk_bf16_f32 v26, v188, v189\n"
"v_cvt_pk_bf16_f32 v27, v190, v191\n"
"ds_write_b128 v2, v[24:27] offset:16384\n"
"s_waitcnt vmcnt(18)\n"
"v_cmp_ne_u64_e32 vcc, v[254:255], v[18:19]\n"
"s_cbranch_vccnz .Lmk_f48s\n"
".Lmk_f48r:\n"
"s_waitcnt lgkmcnt(0)\n"
"s_barrier\n"
"; @B AT a\n"
"s_cmp_lg_u32 s0, 0\n"
"s_cbranch_scc1 .Lmk_exit\n"
"v_xor_b32_e32 v255, 32, v15\n"
"ds_read_b128 v[24:27], v2 offset:17408\n"
"ds_read_b128 v[28:31], v15 offset:16384\n"
"ds_read_b128 v[32:35], v255 offset:16640\n"
"v_xor_b32_e32 v255, 32, v17\n"
"ds_read_b128 v[36:39], v2 offset:16384\n"
"ds_read_b128 v[40:43], v17 offset:15360\n"
"ds_read_b128 v[60:63], v255 offset:15616\n"
"v_xor_b32_e32 v255, 32, v16\n"
"ds_read_b64_tr_b16 v[64:65], v16 offset:16384\n"
"ds_read_b64_tr_b16 v[66:67], v255 offset:16640\n"
"ds_read_b64_tr_b16 v[68:69], v255 offset:16384\n"
"ds_read_b64_tr_b16 v[70:71], v16 offset:16640\n"
"v_mov_b32_e32 v72, 0x3f803f80\n"
"v_mov_b32_e32 v73, 0x3f803f80\n"
"v_mov_b32_e32 v74, 0x3f803f80\n"
"v_mov_b32_e32 v75, 0x3f803f80\n"
"v_xor_b32_e32 v255, 32, v16\n"
"ds_read_b64_tr_b16 v[76:77], v16 offset:15360\n"
"ds_read_b64_tr_b16 v[78:79], v255 offset:15616\n"
"ds_read_b64_tr_b16 v[80:81], v255 offset:15360\n"
"ds_read_b64_tr_b16 v[82:83], v16 offset:15616\n"
"v_mov_b32_e32 v84, 0x3f803f80\n"
"v_mov_b32_e32 v85, 0x3f803f80\n"
"v_mov_b32_e32 v86, 0x3f803f80\n"
"v_mov_b32_e32 v87, 0x3f803f80\n"
"s_waitcnt lgkmcnt(11)\n"
"v_mfma_f32_16x16x32_bf16 v[44:47], v[28:31], v[24:27], v[44:47]\n"
"v_mfma_f32_16x16x32_bf16 v[48:51], v[32:35], v[24:27], v[48:51]\n"
"s_waitcnt lgkmcnt(8)\n"
"v_mfma_f32_16x16x32_bf16 v[52:55], v[40:43], v[36:39], v[52:55]\n"
"v_mfma_f32_16x16x32_bf16 v[56:59], v[60:63], v[36:39], v[56:59]\n"
"buffer_load_dwordx4 a[56:59], v1, s[8:11], s97 offen offset:2048 sc1\n"
"buffer_load_dwordx4 a[60:63], v1, s[8:11], s97 offen offset:3072 sc1\n"
"s_add_u32 s98, s69, 0x4000\n"
"buffer_load_dwordx4 a[64:67], v0, s[8:11], s98 offen sc1\n"
"buffer_load_dwordx4 a[68:71], v0, s[8:11], s98 offen offset:1024 sc1\n"
"v_max_f32_e32 v255, v44, v45\n"
"s_nop 0\n"
"v_max_f32_e32 v254, v52, v53\n"
"v_max3_f32 v255, v255, v46, v47\n"
"v_max3_f32 v254, v254, v54, v55\n"
"v_max3_f32 v255, v255, v48, v49\n"
"v_max3_f32 v254, v254, v56, v57\n"
"v_max3_f32 v255, v255, v50, v51\n"
"v_max3_f32 v254, v254, v58, v59\n"
"v_mov_b32_e32 v253, v255\n"
"v_mov_b32_e32 v252, v254\n"
"s_nop 0\n"
"v_permlane16_swap_b32_e32 v255, v253\n"
"v_permlane16_swap_b32_e32 v254, v252\n"
"v_max_f32_e32 v255, v255, v253\n"
"v_max_f32_e32 v254, v254, v252\n"
"v_mov_b32_e32 v253, v255\n"
"v_mov_b32_e32 v252, v254\n"
"s_nop 0\n"
"v_permlane32_swap_b32_e32 v255, v253\n"
"v_permlane32_swap_b32_e32 v254, v252\n"
"v_max_f32_e32 v255, v255, v253\n"
"v_max_f32_e32 v254, v254, v252\n"
"s_add_u32 s97, s56, 0x440000\n"
"buffer_load_dwordx4 v[24:27], v0, s[4:7], s97 offen sc1\n"
"v_sub_f32_e32 v44, v44, v255\n"
"v_sub_f32_e32 v45, v45, v255\n"
"v_sub_f32_e32 v46, v46, v255\n"
"v_sub_f32_e32 v47, v47, v255\n"
"v_sub_f32_e32 v48, v48, v255\n"
"v_sub_f32_e32 v49, v49, v255\n"
"v_sub_f32_e32 v50, v50, v255\n"
"buffer_load_dwordx4 v[28:31], v0, s[4:7], s97 offen offset:1024 sc1\n"
"v_sub_f32_e32 v51, v51, v255\n"
"v_exp_f32_e32 v44, v44\n"
"v_exp_f32_e32 v45, v45\n"
"v_exp_f32_e32 v46, v46\n"
"buffer_load_dwordx4 v[32:35], v0, s[4:7], s97 offen offset:2048 sc1\n"
"v_exp_f32_e32 v47, v47\n"
"v_exp_f32_e32 v48, v48\n"
"v_exp_f32_e32 v49, v49\n"
"v_exp_f32_e32 v50, v50\n"
"buffer_load_dwordx4 v[36:39], v0, s[4:7], s97 offen offset:3072 sc1\n"
"v_exp_f32_e32 v51, v51\n"
"v_cvt_pk_bf16_f32 v40, v44, v45\n"
"v_cvt_pk_bf16_f32 v41, v46, v47\n"
"v_cvt_pk_bf16_f32 v42, v48, v49\n"
"v_cvt_pk_bf16_f32 v43, v50, v51\n"
"s_waitcnt lgkmcnt(4)\n"
"v_sub_f32_e32 v52, v52, v254\n"
"v_sub_f32_e32 v53, v53, v254\n"
"v_sub_f32_e32 v54, v54, v254\n"
"v_sub_f32_e32 v55, v55, v254\n"
"v_sub_f32_e32 v56, v56, v254\n"
"v_sub_f32_e32 v57, v57, v254\n"
"v_sub_f32_e32 v58, v58, v254\n"
"v_mfma_f32_16x16x32_bf16 v[44:47], v[72:75], v[40:43], 0\n"
"v_sub_f32_e32 v59, v59, v254\n"
"v_exp_f32_e32 v52, v52\n"
"v_exp_f32_e32 v53, v53\n"
"v_exp_f32_e32 v54, v54\n"
"v_mfma_f32_16x16x32_bf16 v[48:51], v[64:67], v[40:43], 0\n"
"v_exp_f32_e32 v55, v55\n"
"v_exp_f32_e32 v56, v56\n"
"v_exp_f32_e32 v57, v57\n"
"v_exp_f32_e32 v58, v58\n"
"v_mfma_f32_16x16x32_bf16 v[60:63], v[68:71], v[40:43], 0\n"
"v_exp_f32_e32 v59, v59\n"
"v_cvt_pk_bf16_f32 v40, v52, v53\n"
"v_cvt_pk_bf16_f32 v41, v54, v55\n"
"v_cvt_pk_bf16_f32 v42, v56, v57\n"
"v_cvt_pk_bf16_f32 v43, v58, v59\n"
"buffer_load_dwordx4 v[52:55], v1, s[4:7], s97 offen sc1\n"
"buffer_load_dwordx4 v[56:59], v1, s[4:7], s97 offen offset:1024 sc1\n"
"buffer_load_dwordx4 v[64:67], v1, s[4:7], s97 offen offset:2048 sc1\n"
"buffer_load_dwordx4 v[68:71], v1, s[4:7], s97 offen offset:3072 sc1\n"
"s_waitcnt lgkmcnt(0)\n"
"v_rcp_f32_e32 v44, v44\n"
"s_nop 0\n"
"v_mul_f32_e32 v48, v48, v44\n"
"v_mul_f32_e32 v49, v49, v44\n"
"v_mul_f32_e32 v50, v50, v44\n"
"v_mfma_f32_16x16x32_bf16 v[72:75], v[84:87], v[40:43], 0\n"
"v_mul_f32_e32 v51, v51, v44\n"
"v_mul_f32_e32 v60, v60, v44\n"
"v_mul_f32_e32 v61, v61, v44\n"
"v_mul_f32_e32 v62, v62, v44\n"
"v_mul_f32_e32 v63, v63, v44\n"
"v_mfma_f32_16x16x32_bf16 v[44:47], v[76:79], v[40:43], 0\n"
"v_cvt_pk_bf16_f32 v76, v48, v49\n"
"v_cvt_pk_bf16_f32 v77, v50, v51\n"
"v_cvt_pk_bf16_f32 v78, v60, v61\n"
"v_cvt_pk_bf16_f32 v79, v62, v63\n"
"s_add_u32 s97, s60, 0x408000\n"
"buffer_store_dwordx4 v[76:79], v0, s[4:7], s97 offen sc1\n"
"v_mfma_f32_16x16x32_bf16 v[48:51], v[80:83], v[40:43], 0\n"
"v_rcp_f32_e32 v72, v72\n"
"s_nop 0\n"
"v_mul_f32_e32 v44, v44, v72\n"
"v_mul_f32_e32 v45, v45, v72\n"
"v_mul_f32_e32 v46, v46, v72\n"
"v_mul_f32_e32 v47, v47, v72\n"
"s_nop 1\n"
"v_mul_f32_e32 v48, v48, v72\n"
"v_mul_f32_e32 v49, v49, v72\n"
"v_mul_f32_e32 v50, v50, v72\n"
"v_mul_f32_e32 v51, v51, v72\n"
"v_cvt_pk_bf16_f32 v40, v44, v45\n"
"v_cvt_pk_bf16_f32 v41, v46, v47\n"
"v_cvt_pk_bf16_f32 v42, v48, v49\n"
"v_cvt_pk_bf16_f32 v43, v50, v51\n"
"s_add_u32 s97, s60, 0x400000\n"
"buffer_store_dwordx4 v[40:43], v0, s[4:7], s97 offen sc1\n"
"s_add_u32 s97, s57, 0x440000\n"
"buffer_load_dwordx4 v[40:43], v0, s[4:7], s97 offen sc1\n"
"buffer_load_dwordx4 v[44:47], v0, s[4:7], s97 offen offset:1024 sc1\n"
"buffer_load_dwordx4 v[48:51], v0, s[4:7], s97 offen offset:2048 sc1\n"
"buffer_load_dwordx4 v[60:63], v0, s[4:7], s97 offen offset:3072 sc1\n"
"buffer_load_dwordx4 v[72:75], v1, s[4:7], s97 offen sc1\n"
"buffer_load_dwordx4 v[76:79], v1, s[4:7], s97 offen offset:1024 sc1\n"
"buffer_load_dwordx4 v[80:83], v1, s[4:7], s97 offen offset:2048 sc1\n"
"buffer_load_dwordx4 v[84:87], v1, s[4:7], s97 offen offset:3072 sc1\n"
"s_add_u32 s97, s58, 0x440000\n"
"buffer_load_dwordx4 v[88:91], v0, s[4:7], s97 offen sc1\n"
"buffer_load_dwordx4 v[92:95], v0, s[4:7], s97 offen offset:1024 sc1\n"
"buffer_load_dwordx4 v[96:99], v0, s[4:7], s97 offen offset:2048 sc1\n"
"buffer_load_dwordx4 v[100:103], v0, s[4:7], s97 offen offset:3072 sc1\n"
"buffer_load_dwordx4 v[104:107], v1, s[4:7], s97 offen sc1\n"
"buffer_load_dwordx4 v[108:111], v1, s[4:7], s97 offen offset:1024 sc1\n"
"buffer_load_dwordx4 v[112:115], v1, s[4:7], s97 offen offset:2048 sc1\n"
"buffer_load_dwordx4 v[116:119], v1, s[4:7], s97 offen offset:3072 sc1\n"
"buffer_load_dwordx4 a[72:75], v0, s[8:11], s98 offen offset:2048 sc1\n"
"buffer_load_dwordx4 a[76:79], v0, s[8:11], s98 offen offset:3072 sc1\n"
"buffer_load_dwordx4 a[80:83], v1, s[8:11], s98 offen sc1\n"
"buffer_load_dwordx4 a[84:87], v1, s[8:11], s98 offen offset:1024 sc1\n"
"buffer_load_dwordx4 a[88:91], v1, s[8:11], s98 offen offset:2048 sc1\n"
"buffer_load_dwordx4 a[92:95], v1, s[8:11], s98 offen offset:3072 sc1\n"
"s_add_u32 s98, s69, 0x6000\n"
"buffer_load_dwordx4 a[96:99], v0, s[8:11], s98 offen sc1\n"
"buffer_load_dwordx4 a[100:103], v0, s[8:11], s98 offen offset:1024 sc1\n"
"s_waitcnt vmcnt(33)\n"
"; @B G2 b.0\n"
"v_mfma_f32_16x16x32_bf16 v[120:123], a[128:131], v[24:27], 0\n"
"v_mfma_f32_16x16x32_bf16 v[124:127], a[132:135], v[24:27], 0\n"
"v_mfma_f32_16x16x32_bf16 v[128:131], a[136:139], v[24:27], 0\n"
"s_add_u32 s97, s59, 0x440000\n"
"v_mfma_f32_16x16x32_bf16 v[132:135], a[140:143], v[24:27], 0\n"
"s_waitcnt vmcnt(32)\n"
"v_mfma_f32_16x16x32_bf16 v[120:123], a[144:147], v[28:31], v[120:123]\n"
"buffer_load_dwordx4 v[24:27], v0, s[4:7], s97 offen sc1\n"
"v_mfma_f32_16x16x32_bf16 v[124:127], a[148:151], v[28:31], v[124:127]\n"
"v_mfma_f32_16x16x32_bf16 v[128:131], a[152:155], v[28:31], v[128:131]\n"
"v_mfma_f32_16x16x32_bf16 v[132:135], a[156:159], v[28:31], v[132:135]\n"
"s_waitcnt vmcnt(32)\n"
"v_mfma_f32_16x16x32_bf16 v[120:123], a[160:163], v[32:35], v[120:123]\n"
"buffer_load_dwordx4 v[28:31], v0, s[4:7], s97 offen offset:1024 sc1\n"
"v_mfma_f32_16x16x32_bf16 v[124:127], a[164:167], v[32:35], v[124:127]\n"
"v_mfma_f32_16x16x32_bf16 v[128:131], a[168:171], v[32:35], v[128:131]\n"
"v_mfma_f32_16x16x32_bf16 v[132:135], a[172:175], v[32:35], v[132:135]\n"
"s_waitcnt vmcnt(32)\n"
"v_mfma_f32_16x16x32_bf16 v[120:123], a[176:179], v[36:39], v[120:123]\n"
"buffer_load_dwordx4 v[32:35], v0, s[4:7], s97 offen offset:2048 sc1\n"
"v_mfma_f32_16x16x32_bf16 v[124:127], a[180:183], v[36:39], v[124:127]\n"
"v_mfma_f32_16x16x32_bf16 v[128:131], a[184:187], v[36:39], v[128:131]\n"
"v_mfma_f32_16x16x32_bf16 v[132:135], a[188:191], v[36:39], v[132:135]\n"
"s_waitcnt vmcnt(32)\n"
"v_mfma_f32_16x16x32_bf16 v[120:123], a[192:195], v[52:55], v[120:123]\n"
"buffer_load_dwordx4 v[36:39], v0, s[4:7], s97 offen offset:3072 sc1\n"
"v_mfma_f32_16x16x32_bf16 v[124:127], a[196:199], v[52:55], v[124:127]\n"
"v_mfma_f32_16x16x32_bf16 v[128:131], a[200:203], v[52:55], v[128:131]\n"
"v_mfma_f32_16x16x32_bf16 v[132:135], a[204:207], v[52:55], v[132:135]\n"
"s_waitcnt vmcnt(32)\n"
"v_mfma_f32_16x16x32_bf16 v[120:123], a[208:211], v[56:59], v[120:123]\n"
"buffer_load_dwordx4 v[52:55], v1, s[4:7], s97 offen sc1\n"
"v_mfma_f32_16x16x32_bf16 v[124:127], a[212:215], v[56:59], v[124:127]\n"
"v_mfma_f32_16x16x32_bf16 v[128:131], a[216:219], v[56:59], v[128:131]\n"
"v_mfma_f32_16x16x32_bf16 v[132:135], a[220:223], v[56:59], v[132:135]\n"
"s_waitcnt vmcnt(32)\n"
"v_mfma_f32_16x16x32_bf16 v[120:123], a[224:227], v[64:67], v[120:123]\n"
"buffer_load_dwordx4 v[56:59], v1, s[4:7], s97 offen offset:1024 sc1\n"
"v_mfma_f32_16x16x32_bf16 v[124:127], a[228:231], v[64:67], v[124:127]\n"
"v_mfma_f32_16x16x32_bf16 v[128:131], a[232:235], v[64:67], v[128:131]\n"
"v_mfma_f32_16x16x32_bf16 v[132:135], a[236:239], v[64:67], v[132:135]\n"
"s_waitcnt vmcnt(32)\n"
"v_mfma_f32_16x16x32_bf16 v[120:123], a[240:243], v[68:71], v[120:123]\n"
"buffer_load_dwordx4 v[64:67], v1, s[4:7], s97 offen offset:2048 sc1\n"
"s_mov_b32 s96, .Lmk_a0-.Lmk_g79\n"
"s_getpc_b64 s[84:85]\n"
".Lmk_g79:\n"
"v_mfma_f32_16x16x32_bf16 v[124:127], a[244:247], v[68:71], v[124:127]\n"
"s_add_u32 s84, s84, s96\n"
"s_addc_u32 s85, s85, 0\n"
"s_and_b32 s84, s84, -16\n"
"s_and_b32 s85, s85, 0xffff\n"
"v_mfma_f32_16x16x32_bf16 v[128:131], a[248:251], v[68:71], v[128:131]\n"
"s_sub_u32 s96, .Lmk_end-.Lmk_g79, s96\n"
"s_min_u32 s86, s96, 0x3000\n"
"s_mov_b32 s87, 0x20000\n"
"v_mfma_f32_16x16x32_bf16 v[132:135], a[252:255], v[68:71], v[132:135]\n"
"s_waitcnt vmcnt(29)\n"
"; @B G2 b.1\n"
"v_mfma_f32_16x16x32_bf16 v[68:71], a[128:131], v[40:43], 0\n"
"v_cmp_u_f32_e32 vcc, v120, v120\n"
"v_mfma_f32_16x16x32_bf16 v[136:139], a[132:135], v[40:43], 0\n"
"s_cbranch_vccnz .Lmk_p74s\n"
".Lmk_p74r:\n"
"v_mfma_f32_16x16x32_bf16 v[140:143], a[136:139], v[40:43], 0\n"
"buffer_load_dwordx4 v[144:147], v1, s[4:7], s97 offen offset:3072 sc1\n"
"v_mfma_f32_16x16x32_bf16 v[148:151], a[140:143], v[40:43], 0\n"
"s_mov_b32 m0, 0x27800\n"
"s_nop 0\n"
"buffer_load_dwordx4 v0, s[84:87], s99 offen lds\n"
"v_mfma_f32_16x16x32_bf16 v[68:71], a[144:147], v[44:47], v[68:71]\n"
"ds_write_b128 v3, v[120:123]\n"
"v_mfma_f32_16x16x32_bf16 v[136:139], a[148:151], v[44:47], v[136:139]\n"
"v_mfma_f32_16x16x32_bf16 v[140:143], a[152:155], v[44:47], v[140:143]\n"
"ds_write_b128 v3, v[124:127] offset:1024\n"
"v_mfma_f32_16x16x32_bf16 v[148:151], a[156:159], v[44:47], v[148:151]\n"
"s_waitcnt vmcnt(29)\n"
"v_mfma_f32_16x16x32_bf16 v[68:71], a[160:163], v[48:51], v[68:71]\n"
"ds_write_b128 v3, v[128:131] offset:2048\n"
"v_mfma_f32_16x16x32_bf16 v[136:139], a[164:167], v[48:51], v[136:139]\n"
"s_add_u32 s97, s56, 0x400000\n"
"v_mfma_f32_16x16x32_bf16 v[140:143], a[168:171], v[48:51], v[140:143]\n"
"ds_write_b128 v3, v[132:135] offset:3072\n"
"v_mfma_f32_16x16x32_bf16 v[148:151], a[172:175], v[48:51], v[148:151]\n"
"buffer_load_dwordx4 v[40:43], v0, s[4:7], s97 offen sc1\n"
"v_mfma_f32_16x16x32_bf16 v[68:71], a[176:179], v[60:63], v[68:71]\n"
"v_mfma_f32_16x16x32_bf16 v[136:139], a[180:183], v[60:63], v[136:139]\n"
"ds_read_b128 v[44:47], v2 offset:8192\n"
"ds_read_b128 v[48:51], v2 offset:9216\n"
"v_mfma_f32_16x16x32_bf16 v[140:143], a[184:187], v[60:63], v[140:143]\n"
"buffer_load_dwordx4 v[120:123], v0, s[4:7], s97 offen offset:1024 sc1\n"
"v_mfma_f32_16x16x32_bf16 v[148:151], a[188:191], v[60:63], v[148:151]\n"
"s_waitcnt vmcnt(29)\n"
"v_mfma_f32_16x16x32_bf16 v[68:71], a[192:195], v[72:75], v[68:71]\n"
"v_mfma_f32_16x16x32_bf16 v[136:139], a[196:199], v[72:75], v[136:139]\n"
"buffer_load_dwordx4 v[60:63], v0, s[4:7], s97 offen offset:2048 sc1\n"
"v_mfma_f32_16x16x32_bf16 v[140:143], a[200:203], v[72:75], v[140:143]\n"
"s_waitcnt lgkmcnt(0)\n"
"v_lshlrev_b32_e32 v255, 16, v44\n"
"v_mfma_f32_16x16x32_bf16 v[148:151], a[204:207], v[72:75], v[148:151]\n"
"s_waitcnt lgkmcnt(0)\n"
"s_barrier\n"
"v_mfma_f32_16x16x32_bf16 v[68:71], a[208:211], v[76:79], v[68:71]\n"
"buffer_load_dwordx4 v[72:75], v0, s[4:7], s97 offen offset:3072 sc1\n"
"v_mfma_f32_16x16x32_bf16 v[136:139], a[212:215], v[76:79], v[136:139]\n"
"ds_read_b128 v[124:127], v4\n"
"ds_read_b128 v[128:131], v4 offset:1024\n"
"v_mfma_f32_16x16x32_bf16 v[140:143], a[216:219], v[76:79], v[140:143]\n"
"ds_read_b128 v[132:135], v4 offset:2048\n"
"ds_read_b128 v[152:155], v4 offset:3072\n"
"v_mfma_f32_16x16x32_bf16 v[148:151], a[220:223], v[76:79], v[148:151]\n"
"s_waitcnt vmcnt(29)\n"
"v_and_b32_e32 v254, 0xffff0000, v44\n"
"v_mfma_f32_16x16x32_bf16 v[68:71], a[224:227], v[80:83], v[68:71]\n"
"buffer_load_dwordx4 v[76:79], v1, s[4:7], s97 offen sc1\n"
"v_mfma_f32_16x16x32_bf16 v[136:139], a[228:231], v[80:83], v[136:139]\n"
"v_lshlrev_b32_e32 v253, 16, v45\n"
"v_and_b32_e32 v252, 0xffff0000, v45\n"
"v_mfma_f32_16x16x32_bf16 v[140:143], a[232:235], v[80:83], v[140:143]\n"
"v_lshlrev_b32_e32 v251, 16, v46\n"
"v_and_b32_e32 v250, 0xffff0000, v46\n"
"v_mfma_f32_16x16x32_bf16 v[148:151], a[236:239], v[80:83], v[148:151]\n"
"buffer_load_dwordx4 v[80:83], v1, s[4:7], s97 offen offset:1024 sc1\n"
"v_mfma_f32_16x16x32_bf16 v[68:71], a[240:243], v[84:87], v[68:71]\n"
"v_lshlrev_b32_e32 v249, 16, v47\n"
"v_and_b32_e32 v248, 0xffff0000, v47\n"
"v_mfma_f32_16x16x32_bf16 v[136:139], a[244:247], v[84:87], v[136:139]\n"
"v_lshlrev_b32_e32 v247, 16, v48\n"
"v_and_b32_e32 v246, 0xffff0000, v48\n"
"v_mfma_f32_16x16x32_bf16 v[140:143], a[248:251], v[84:87], v[140:143]\n"
"buffer_load_dwordx4 v[44:47], v1, s[4:7], s97 offen offset:2048 sc1\n"
"v_mfma_f32_16x16x32_bf16 v[148:151], a[252:255], v[84:87], v[148:151]\n"
"s_waitcnt vmcnt(30)\n"
"v_lshlrev_b32_e32 v245, 16, v49\n"
"; @B G2 b.2\n"
"v_mfma_f32_16x16x32_bf16 v[84:87], a[128:131], v[88:91], 0\n"
"v_cmp_u_f32_e32 vcc, v68, v68\n"
"v_and_b32_e32 v244, 0xffff0000, v49\n"
"v_mfma_f32_16x16x32_bf16 v[156:159], a[132:135], v[88:91], 0\n"
"s_cbranch_vccnz .Lmk_p75s\n"
".Lmk_p75r:\n"
"v_lshlrev_b32_e32 v243, 16, v50\n"
"v_mfma_f32_16x16x32_bf16 v[160:163], a[136:139], v[88:91], 0\n"
"buffer_load_dwordx4 v[164:167], v1, s[4:7], s97 offen offset:3072 sc1\n"
"v_mfma_f32_16x16x32_bf16 v[168:171], a[140:143], v[88:91], 0\n"
"s_add_u32 s97, s57, 0x400000\n"
"v_and_b32_e32 v242, 0xffff0000, v50\n"
"v_mfma_f32_16x16x32_bf16 v[84:87], a[144:147], v[92:95], v[84:87]\n"
"ds_write_b128 v3, v[68:71] offset:6144\n"
"v_mfma_f32_16x16x32_bf16 v[156:159], a[148:151], v[92:95], v[156:159]\n"
"buffer_load_dwordx4 v[68:71], v0, s[4:7], s97 offen sc1\n"
"v_mfma_f32_16x16x32_bf16 v[160:163], a[152:155], v[92:95], v[160:163]\n"
"ds_write_b128 v3, v[136:139] offset:7168\n"
"v_mfma_f32_16x16x32_bf16 v[168:171], a[156:159], v[92:95], v[168:171]\n"
"s_waitcnt vmcnt(30)\n"
"v_lshlrev_b32_e32 v241, 16, v51\n"
"v_mfma_f32_16x16x32_bf16 v[84:87], a[160:163], v[96:99], v[84:87]\n"
"ds_write_b128 v3, v[140:143] offset:8192\n"
"v_mfma_f32_16x16x32_bf16 v[156:159], a[164:167], v[96:99], v[156:159]\n"
"buffer_load_dwordx4 v[88:91], v0, s[4:7], s97 offen offset:1024 sc1\n"
"v_mfma_f32_16x16x32_bf16 v[160:163], a[168:171], v[96:99], v[160:163]\n"
"ds_write_b128 v3, v[148:151] offset:9216\n"
"v_mfma_f32_16x16x32_bf16 v[168:171], a[172:175], v[96:99], v[168:171]\n"
"v_and_b32_e32 v240, 0xffff0000, v51\n"
"s_waitcnt lgkmcnt(4)\n"
"v_mfma_f32_16x16x32_bf16 v[84:87], a[176:179], v[100:103], v[84:87]\n"
"buffer_load_dwordx4 v[48:51], v0, s[4:7], s97 offen offset:2048 sc1\n"
"v_mfma_f32_16x16x32_bf16 v[156:159], a[180:183], v[100:103], v[156:159]\n"
"v_add_f32_e32 v124, v124, v255\n"
"v_add_f32_e32 v125, v125, v254\n"
"v_mfma_f32_16x16x32_bf16 v[160:163], a[184:187], v[100:103], v[160:163]\n"
"v_add_f32_e32 v126, v126, v253\n"
"v_add_f32_e32 v127, v127, v252\n"
"v_mfma_f32_16x16x32_bf16 v[168:171], a[188:191], v[100:103], v[168:171]\n"
"s_waitcnt vmcnt(30)\n"
"v_add_f32_e32 v128, v128, v251\n"
"v_mfma_f32_16x16x32_bf16 v[84:87], a[192:195], v[104:107], v[84:87]\n"
"buffer_load_dwordx4 v[92:95], v0, s[4:7], s97 offen offset:3072 sc1\n"
"v_mfma_f32_16x16x32_bf16 v[156:159], a[196:199], v[104:107], v[156:159]\n"
"v_add_f32_e32 v129, v129, v250\n"
"v_add_f32_e32 v130, v130, v249\n"
"v_mfma_f32_16x16x32_bf16 v[160:163], a[200:203], v[104:107], v[160:163]\n"
"v_add_f32_e32 v131, v131, v248\n"
"v_add_f32_e32 v132, v132, v247\n"
"v_mfma_f32_16x16x32_bf16 v[168:171], a[204:207], v[104:107], v[168:171]\n"
"buffer_load_dwordx4 v[96:99], v1, s[4:7], s97 offen sc1\n"
"v_mfma_f32_16x16x32_bf16 v[84:87], a[208:211], v[108:111], v[84:87]\n"
"v_add_f32_e32 v133, v133, v246\n"
"v_add_f32_e32 v134, v134, v245\n"
"v_mfma_f32_16x16x32_bf16 v[156:159], a[212:215], v[108:111], v[156:159]\n"
"v_add_f32_e32 v135, v135, v244\n"
"v_add_f32_e32 v152, v152, v243\n"
"v_mfma_f32_16x16x32_bf16 v[160:163], a[216:219], v[108:111], v[160:163]\n"
"buffer_load_dwordx4 v[100:103], v1, s[4:7], s97 offen offset:1024 sc1\n"
"v_mfma_f32_16x16x32_bf16 v[168:171], a[220:223], v[108:111], v[168:171]\n"
"s_waitcnt vmcnt(31)\n"
"v_add_f32_e32 v153, v153, v242\n"
"v_mfma_f32_16x16x32_bf16 v[84:87], a[224:227], v[112:115], v[84:87]\n"
"s_waitcnt vmcnt(21)\n"
"v_max_u32_e32 v255, v24, v25\n"
"v_mfma_f32_16x16x32_bf16 v[156:159], a[228:231], v[112:115], v[156:159]\n"
"v_max3_u32 v255, v255, v26, v27\n"
"v_max3_u32 v255, v255, v28, v29\n"
"v_mfma_f32_16x16x32_bf16 v[160:163], a[232:235], v[112:115], v[160:163]\n"
"v_max3_u32 v255, v255, v30, v31\n"
"s_waitcnt vmcnt(19)\n"
"v_mfma_f32_16x16x32_bf16 v[168:171], a[236:239], v[112:115], v[168:171]\n"
"v_max3_u32 v255, v255, v32, v33\n"
"v_max3_u32 v255, v255, v34, v35\n"
"v_mfma_f32_16x16x32_bf16 v[84:87], a[240:243], v[116:119], v[84:87]\n"
"v_max3_u32 v255, v255, v36, v37\n"
"v_max3_u32 v255, v255, v38, v39\n"
"v_mfma_f32_16x16x32_bf16 v[156:159], a[244:247], v[116:119], v[156:159]\n"
"v_cmp_eq_u32_e32 vcc, -1, v255\n"
"v_add_f32_e32 v154, v154, v241\n"
"v_mfma_f32_16x16x32_bf16 v[160:163], a[248:251], v[116:119], v[160:163]\n"
"s_cbranch_vccnz .Lmk_q77s\n"
".Lmk_q77r:\n"
"v_add_f32_e32 v155, v155, v240\n"
"v_mfma_f32_16x16x32_bf16 v[168:171], a[252:255], v[116:119], v[168:171]\n"
"; @B G2 b.3\n"
"v_mfma_f32_16x16x32_bf16 v[124:127], a[128:131], v[24:27], v[124:127]\n"
"v_cmp_u_f32_e32 vcc, v84, v84\n"
"v_mfma_f32_16x16x32_bf16 v[128:131], a[132:135], v[24:27], v[128:131]\n"
"s_cbranch_vccnz .Lmk_p76s\n"
".Lmk_p76r:\n"
"v_mfma_f32_16x16x32_bf16 v[132:135], a[136:139], v[24:27], v[132:135]\n"
"buffer_load_dwordx4 v[104:107], v1, s[4:7], s97 offen offset:2048 sc1\n"
"v_mfma_f32_16x16x32_bf16 v[152:155], a[140:143], v[24:27], v[152:155]\n"
"v_mfma_f32_16x16x32_bf16 v[124:127], a[144:147], v[28:31], v[124:127]\n"
"ds_write_b128 v3, v[84:87] offset:12288\n"
"v_mfma_f32_16x16x32_bf16 v[128:131], a[148:151], v[28:31], v[128:131]\n"
"buffer_load_dwordx4 v[24:27], v1, s[4:7], s97 offen offset:3072 sc1\n"
"v_mfma_f32_16x16x32_bf16 v[132:135], a[152:155], v[28:31], v[132:135]\n"
"ds_write_b128 v3, v[156:159] offset:13312\n"
"v_mfma_f32_16x16x32_bf16 v[152:155], a[156:159], v[28:31], v[152:155]\n"
"s_add_u32 s97, s58, 0x400000\n"
"v_mfma_f32_16x16x32_bf16 v[124:127], a[160:163], v[32:35], v[124:127]\n"
"s_waitcnt vmcnt(19)\n"
"v_max_u32_e32 v255, v52, v53\n"
"v_mfma_f32_16x16x32_bf16 v[128:131], a[164:167], v[32:35], v[128:131]\n"
"v_max3_u32 v255, v255, v54, v55\n"
"v_max3_u32 v255, v255, v56, v57\n"
"v_mfma_f32_16x16x32_bf16 v[132:135], a[168:171], v[32:35], v[132:135]\n"
"v_max3_u32 v255, v255, v58, v59\n"
"s_waitcnt vmcnt(17)\n"
"v_mfma_f32_16x16x32_bf16 v[152:155], a[172:175], v[32:35], v[152:155]\n"
"v_max3_u32 v255, v255, v64, v65\n"
"v_max3_u32 v255, v255, v66, v67\n"
"v_mfma_f32_16x16x32_bf16 v[124:127], a[176:179], v[36:39], v[124:127]\n"
"v_max3_u32 v255, v255, v144, v145\n"
"v_max3_u32 v255, v255, v146, v147\n"
"v_mfma_f32_16x16x32_bf16 v[128:131], a[180:183], v[36:39], v[128:131]\n"
"v_cmp_eq_u32_e32 vcc, -1, v255\n"
"v_mfma_f32_16x16x32_bf16 v[132:135], a[184:187], v[36:39], v[132:135]\n"
"s_cbranch_vccnz .Lmk_q78s\n"
".Lmk_q78r:\n"
"v_mfma_f32_16x16x32_bf16 v[152:155], a[188:191], v[36:39], v[152:155]\n"
"v_mfma_f32_16x16x32_bf16 v[124:127], a[192:195], v[52:55], v[124:127]\n"
"ds_write_b128 v3, v[160:163] offset:14336\n"
"v_mfma_f32_16x16x32_bf16 v[128:131], a[196:199], v[52:55], v[128:131]\n"
"buffer_load_dwordx4 v[28:31], v0, s[4:7], s97 offen sc1\n"
"v_mfma_f32_16x16x32_bf16 v[132:135], a[200:203], v[52:55], v[132:135]\n"
"ds_write_b128 v3, v[168:171] offset:15360\n"
"v_mfma_f32_16x16x32_bf16 v[152:155], a[204:207], v[52:55], v[152:155]\n"
"v_mfma_f32_16x16x32_bf16 v[124:127], a[208:211], v[56:59], v[124:127]\n"
"buffer_load_dwordx4 v[32:35], v0, s[4:7], s97 offen offset:1024 sc1\n"
"v_mfma_f32_16x16x32_bf16 v[128:131], a[212:215], v[56:59], v[128:131]\n"
"s_waitcnt lgkmcnt(0)\n"
"s_barrier\n"
"v_mfma_f32_16x16x32_bf16 v[132:135], a[216:219], v[56:59], v[132:135]\n"
"ds_read_b128 v[36:39], v5\n"
"ds_read_b128 v[52:55], v6\n"
"v_mfma_f32_16x16x32_bf16 v[152:155], a[220:223], v[56:59], v[152:155]\n"
"ds_read_b128 v[56:59], v5 offset:1024\n"
"v_mfma_f32_16x16x32_bf16 v[124:127], a[224:227], v[64:67], v[124:127]\n"
"buffer_load_dwordx4 v[84:87], v0, s[4:7], s97 offen offset:2048 sc1\n"
"v_mfma_f32_16x16x32_bf16 v[128:131], a[228:231], v[64:67], v[128:131]\n"
"ds_read_b128 v[108:111], v6 offset:1024\n"
"v_mfma_f32_16x16x32_bf16 v[132:135], a[232:235], v[64:67], v[132:135]\n"
"ds_read_b128 v[112:115], v5 offset:2048\n"
"ds_read_b128 v[116:119], v6 offset:2048\n"
"v_mfma_f32_16x16x32_bf16 v[152:155], a[236:239], v[64:67], v[152:155]\n"
"buffer_load_dwordx4 v[64:67], v0, s[4:7], s97 offen offset:3072 sc1\n"
"v_mfma_f32_16x16x32_bf16 v[124:127], a[240:243], v[144:147], v[124:127]\n"
"ds_read_b128 v[136:139], v5 offset:3072\n"
"ds_read_b128 v[140:143], v6 offset:3072\n"
"v_mfma_f32_16x16x32_bf16 v[128:131], a[244:247], v[144:147], v[128:131]\n"
"v_mfma_f32_16x16x32_bf16 v[132:135], a[248:251], v[144:147], v[132:135]\n"
"buffer_load_dwordx4 v[148:151], v1, s[4:7], s97 offen sc1\n"
"v_mfma_f32_16x16x32_bf16 v[152:155], a[252:255], v[144:147], v[152:155]\n"
"s_waitcnt lgkmcnt(4)\n"
"v_add_f32_e32 v36, v36, v52\n"
"v_add_f32_e32 v37, v37, v53\n"
"v_add_f32_e32 v38, v38, v54\n"
"v_add_f32_e32 v39, v39, v55\n"
"v_add_f32_e32 v56, v56, v108\n"
"v_add_f32_e32 v57, v57, v109\n"
"v_add_f32_e32 v58, v58, v110\n"
"v_add_f32_e32 v59, v59, v111\n"
"buffer_load_dwordx4 v[52:55], v1, s[4:7], s97 offen offset:1024 sc1\n"
"s_waitcnt lgkmcnt(0)\n"
"v_add_f32_e32 v112, v112, v116\n"
"v_add_f32_e32 v113, v113, v117\n"
"v_add_f32_e32 v114, v114, v118\n"
"v_add_f32_e32 v115, v115, v119\n"
"v_add_f32_e32 v136, v136, v140\n"
"v_add_f32_e32 v137, v137, v141\n"
"v_add_f32_e32 v138, v138, v142\n"
"v_add_f32_e32 v139, v139, v143\n"
"buffer_load_dwordx4 v[108:111], v1, s[4:7], s97 offen offset:2048 sc1\n"
"buffer_load_dwordx4 v[116:119], v1, s[4:7], s97 offen offset:3072 sc1\n"
"; @B fin b\n"
"s_add_u32 s97, s59, 0x400000\n"
"buffer_load_dwordx4 v[140:143], v0, s[4:7], s97 offen sc1\n"
"v_mov_b32_e32 v144, 0x3f803f80\n"
"v_mov_b32_e32 v145, 0x3f803f80\n"
"v_mov_b32_e32 v146, 0x3f803f80\n"
"v_mov_b32_e32 v147, 0x3f803f80\n"
"v_add_f32_e32 v36, v124, v36\n"
"v_add_f32_e32 v37, v125, v37\n"
"v_add_f32_e32 v38, v126, v38\n"
"v_add_f32_e32 v39, v127, v39\n"
"v_add_f32_e32 v56, v128, v56\n"
"buffer_load_dwordx4 v[124:127], v0, s[4:7], s97 offen offset:1024 sc1\n"
"v_add_f32_e32 v57, v129, v57\n"
"v_add_f32_e32 v58, v130, v58\n"
"v_add_f32_e32 v59, v131, v59\n"
"v_cvt_pk_bf16_f32 v128, v36, v37\n"
"v_cvt_pk_bf16_f32 v129, v38, v39\n"
"v_cvt_pk_bf16_f32 v130, v56, v57\n"
"v_cvt_pk_bf16_f32 v131, v58, v59\n"
"s_add_u32 s96, s61, 0x840000\n"
"buffer_store_dwordx4 v[128:131], v0, s[4:7], s96 offen sc1\n"
"buffer_load_dwordx4 v[36:39], v0, s[4:7], s97 offen offset:2048 sc1\n"
"ds_write_b128 v2, v[128:131] offset:8192\n"
"buffer_load_dwordx4 v[56:59], v0, s[4:7], s97 offen offset:3072 sc1\n"
"v_add_f32_e32 v112, v132, v112\n"
"v_add_f32_e32 v113, v133, v113\n"
"v_add_f32_e32 v114, v134, v114\n"
"v_add_f32_e32 v115, v135, v115\n"
"v_add_f32_e32 v136, v152, v136\n"
"v_add_f32_e32 v137, v153, v137\n"
"v_add_f32_e32 v138, v154, v138\n"
"v_add_f32_e32 v139, v155, v139\n"
"buffer_load_dwordx4 v[132:135], v1, s[4:7], s97 offen sc1\n"
"v_cvt_pk_bf16_f32 v152, v112, v113\n"
"v_cvt_pk_bf16_f32 v153, v114, v115\n"
"v_cvt_pk_bf16_f32 v154, v136, v137\n"
"v_cvt_pk_bf16_f32 v155, v138, v139\n"
"s_add_u32 s96, s61, 0x840000\n"
"buffer_store_dwordx4 v[152:155], v0, s[4:7], s96 offen offset:1024 sc1\n"
"buffer_load_dwordx4 v[112:115], v1, s[4:7], s97 offen offset:1024 sc1\n"
"ds_write_b128 v2, v[152:155] offset:9216\n"
"v_mfma_f32_16x16x32_bf16 v[136:139], v[144:147], v[128:131], 0\n"
"buffer_load_dwordx4 v[156:159], v1, s[4:7], s97 offen offset:2048 sc1\n"
"v_mfma_f32_16x16x32_bf16 v[160:163], v[128:131], v[128:131], 0\n"
"v_mfma_f32_16x16x32_bf16 v[136:139], v[144:147], v[152:155], v[136:139]\n"
"v_mfma_f32_16x16x32_bf16 v[160:163], v[152:155], v[152:155], v[160:163]\n"
"s_mov_b32 s94, 0xaaaaaaaa\n"
"s_mov_b32 s95, 0xaaaaaaaa\n"
"s_nop 5\n"
"v_cndmask_b32_e64 v160, v160, v161, s[94:95]\n"
"buffer_load_dwordx4 v[128:131], v1, s[4:7], s97 offen offset:3072 sc1\n"
"v_cndmask_b32_e64 v162, v162, v163, s[94:95]\n"
"s_mov_b32 s94, 0xcccccccc\n"
"s_mov_b32 s95, 0xcccccccc\n"
"v_cndmask_b32_e64 v137, v160, v162, s[94:95]\n"
"s_add_u32 s97, s63, 0x84000\n"
"buffer_store_dwordx2 v[136:137], v14, s[4:7], s97 offen sc1\n"
"s_waitcnt vmcnt(34)\n"
"; @B G2 a.0\n"
"v_mfma_f32_16x16x32_bf16 v[136:139], a[128:131], v[40:43], 0\n"
"v_mfma_f32_16x16x32_bf16 v[144:147], a[132:135], v[40:43], 0\n"
"v_mfma_f32_16x16x32_bf16 v[152:155], a[136:139], v[40:43], 0\n"
"buffer_load_dwordx4 a[104:107], v0, s[8:11], s98 offen offset:2048 sc1\n"
"v_mfma_f32_16x16x32_bf16 v[160:163], a[140:143], v[40:43], 0\n"
"s_waitcnt vmcnt(34)\n"
"v_mfma_f32_16x16x32_bf16 v[136:139], a[144:147], v[120:123], v[136:139]\n"
"v_mfma_f32_16x16x32_bf16 v[144:147], a[148:151], v[120:123], v[144:147]\n"
"buffer_load_dwordx4 a[108:111], v0, s[8:11], s98 offen offset:3072 sc1\n"
"v_mfma_f32_16x16x32_bf16 v[152:155], a[152:155], v[120:123], v[152:155]\n"
"v_mfma_f32_16x16x32_bf16 v[160:163], a[156:159], v[120:123], v[160:163]\n"
"s_waitcnt vmcnt(34)\n"
"v_mfma_f32_16x16x32_bf16 v[136:139], a[160:163], v[60:63], v[136:139]\n"
"buffer_load_dwordx4 a[112:115], v1, s[8:11], s98 offen sc1\n"
"v_mfma_f32_16x16x32_bf16 v[144:147], a[164:167], v[60:63], v[144:147]\n"
"v_mfma_f32_16x16x32_bf16 v[152:155], a[168:171], v[60:63], v[152:155]\n"
"v_mfma_f32_16x16x32_bf16 v[160:163], a[172:175], v[60:63], v[160:163]\n"
"s_waitcnt vmcnt(34)\n"
"v_mfma_f32_16x16x32_bf16 v[136:139], a[176:179], v[72:75], v[136:139]\n"
"buffer_load_dwordx4 a[116:119], v1, s[8:11], s98 offen offset:1024 sc1\n"
"v_mfma_f32_16x16x32_bf16 v[144:147], a[180:183], v[72:75], v[144:147]\n"
"v_mfma_f32_16x16x32_bf16 v[152:155], a[184:187], v[72:75], v[152:155]\n"
"v_mfma_f32_16x16x32_bf16 v[160:163], a[188:191], v[72:75], v[160:163]\n"
"s_waitcnt vmcnt(34)\n"
"v_mfma_f32_16x16x32_bf16 v[136:139], a[192:195], v[76:79], v[136:139]\n"
"buffer_load_dwordx4 a[120:123], v1, s[8:11], s98 offen offset:2048 sc1\n"
"v_mfma_f32_16x16x32_bf16 v[144:147], a[196:199], v[76:79], v[144:147]\n"
"v_mfma_f32_16x16x32_bf16 v[152:155], a[200:203], v[76:79], v[152:155]\n"
"v_mfma_f32_16x16x32_bf16 v[160:163], a[204:207], v[76:79], v[160:163]\n"
"s_waitcnt vmcnt(34)\n"
"v_mfma_f32_16x16x32_bf16 v[136:139], a[208:211], v[80:83], v[136:139]\n"
"buffer_load_dwordx4 a[124:127], v1, s[8:11], s98 offen offset:3072 sc1\n"
"v_mfma_f32_16x16x32_bf16 v[144:147], a[212:215], v[80:83], v[144:147]\n"
"s_add_u32 s98, s69, 0x8000\n"
"v_mfma_f32_16x16x32_bf16 v[152:155], a[216:219], v[80:83], v[152:155]\n"
"v_mfma_f32_16x16x32_bf16 v[160:163], a[220:223], v[80:83], v[160:163]\n"
"s_waitcnt vmcnt(34)\n"
"v_mfma_f32_16x16x32_bf16 v[136:139], a[224:227], v[44:47], v[136:139]\n"
"buffer_load_dwordx4 v[40:43], v0, s[8:11], s98 offen sc1\n"
"v_mfma_f32_16x16x32_bf16 v[144:147], a[228:231], v[44:47], v[144:147]\n"
"v_mfma_f32_16x16x32_bf16 v[152:155], a[232:235], v[44:47], v[152:155]\n"
"v_mfma_f32_16x16x32_bf16 v[160:163], a[236:239], v[44:47], v[160:163]\n"
"s_waitcnt vmcnt(34)\n"
"v_mfma_f32_16x16x32_bf16 v[136:139], a[240:243], v[164:167], v[136:139]\n"
"buffer_load_dwordx4 v[44:47], v0, s[8:11], s98 offen offset:1024 sc1\n"
"v_mfma_f32_16x16x32_bf16 v[144:147], a[244:247], v[164:167], v[144:147]\n"
"v_mfma_f32_16x16x32_bf16 v[152:155], a[248:251], v[164:167], v[152:155]\n"
"v_mfma_f32_16x16x32_bf16 v[160:163], a[252:255], v[164:167], v[160:163]\n"
"s_waitcnt vmcnt(33)\n"
"; @B G2 a.1\n"
"v_mfma_f32_16x16x32_bf16 v[60:63], a[128:131], v[68:71], 0\n"
"s_nop 1\n"
"v_cmp_u_f32_e32 vcc, v136, v136\n"
"v_mfma_f32_16x16x32_bf16 v[72:75], a[132:135], v[68:71], 0\n"
"s_cbranch_vccnz .Lmk_p80s\n"
".Lmk_p80r:\n"
"v_mfma_f32_16x16x32_bf16 v[76:79], a[136:139], v[68:71], 0\n"
"buffer_load_dwordx4 v[80:83], v0, s[8:11], s98 offen offset:2048 sc1\n"
"v_mfma_f32_16x16x32_bf16 v[120:123], a[140:143], v[68:71], 0\n"
"v_mfma_f32_16x16x32_bf16 v[60:63], a[144:147], v[88:91], v[60:63]\n"
"ds_write_b128 v3, v[136:139]\n"
"v_mfma_f32_16x16x32_bf16 v[72:75], a[148:151], v[88:91], v[72:75]\n"
"buffer_load_dwordx4 v[68:71], v0, s[8:11], s98 offen offset:3072 sc1\n"
"v_mfma_f32_16x16x32_bf16 v[76:79], a[152:155], v[88:91], v[76:79]\n"
"ds_write_b128 v3, v[144:147] offset:1024\n"
"v_mfma_f32_16x16x32_bf16 v[120:123], a[156:159], v[88:91], v[120:123]\n"
"s_waitcnt vmcnt(33)\n"
"v_mfma_f32_16x16x32_bf16 v[60:63], a[160:163], v[48:51], v[60:63]\n"
"ds_write_b128 v3, v[152:155] offset:2048\n"
"v_mfma_f32_16x16x32_bf16 v[72:75], a[164:167], v[48:51], v[72:75]\n"
"buffer_load_dwordx4 v[88:91], v1, s[8:11], s98 offen sc1\n"
"v_mfma_f32_16x16x32_bf16 v[76:79], a[168:171], v[48:51], v[76:79]\n"
"ds_write_b128 v3, v[160:163] offset:3072\n"
"v_mfma_f32_16x16x32_bf16 v[120:123], a[172:175], v[48:51], v[120:123]\n"
"v_mfma_f32_16x16x32_bf16 v[60:63], a[176:179], v[92:95], v[60:63]\n"
"buffer_load_dwordx4 v[48:51], v1, s[8:11], s98 offen offset:1024 sc1\n"
"v_mfma_f32_16x16x32_bf16 v[72:75], a[180:183], v[92:95], v[72:75]\n"
"ds_read_b128 v[136:139], v2\n"
"ds_read_b128 v[144:147], v2 offset:1024\n"
"v_mfma_f32_16x16x32_bf16 v[76:79], a[184:187], v[92:95], v[76:79]\n"
"v_mfma_f32_16x16x32_bf16 v[120:123], a[188:191], v[92:95], v[120:123]\n"
"s_waitcnt vmcnt(33)\n"
"v_mfma_f32_16x16x32_bf16 v[60:63], a[192:195], v[96:99], v[60:63]\n"
"buffer_load_dwordx4 v[92:95], v1, s[8:11], s98 offen offset:2048 sc1\n"
"v_mfma_f32_16x16x32_bf16 v[72:75], a[196:199], v[96:99], v[72:75]\n"
"v_mfma_f32_16x16x32_bf16 v[76:79], a[200:203], v[96:99], v[76:79]\n"
"s_waitcnt lgkmcnt(0)\n"
"v_lshlrev_b32_e32 v255, 16, v136\n"
"v_mfma_f32_16x16x32_bf16 v[120:123], a[204:207], v[96:99], v[120:123]\n"
"s_waitcnt lgkmcnt(0)\n"
"s_barrier\n"
"v_mfma_f32_16x16x32_bf16 v[60:63], a[208:211], v[100:103], v[60:63]\n"
"buffer_load_dwordx4 v[96:99], v1, s[8:11], s98 offen offset:3072 sc1\n"
"v_mfma_f32_16x16x32_bf16 v[72:75], a[212:215], v[100:103], v[72:75]\n"
"s_add_u32 s98, s69, 0xa000\n"
"ds_read_b128 v[152:155], v4\n"
"v_mfma_f32_16x16x32_bf16 v[76:79], a[216:219], v[100:103], v[76:79]\n"
"ds_read_b128 v[160:163], v4 offset:1024\n"
"ds_read_b128 v[164:167], v4 offset:2048\n"
"v_mfma_f32_16x16x32_bf16 v[120:123], a[220:223], v[100:103], v[120:123]\n"
"s_waitcnt vmcnt(33)\n"
"ds_read_b128 v[100:103], v4 offset:3072\n"
"v_mfma_f32_16x16x32_bf16 v[60:63], a[224:227], v[104:107], v[60:63]\n"
"buffer_load_dwordx4 v[168:171], v0, s[8:11], s98 offen sc1\n"
"v_mfma_f32_16x16x32_bf16 v[72:75], a[228:231], v[104:107], v[72:75]\n"
"v_and_b32_e32 v254, 0xffff0000, v136\n"
"v_lshlrev_b32_e32 v253, 16, v137\n"
"v_mfma_f32_16x16x32_bf16 v[76:79], a[232:235], v[104:107], v[76:79]\n"
"v_and_b32_e32 v252, 0xffff0000, v137\n"
"v_lshlrev_b32_e32 v251, 16, v138\n"
"v_mfma_f32_16x16x32_bf16 v[120:123], a[236:239], v[104:107], v[120:123]\n"
"buffer_load_dwordx4 v[104:107], v0, s[8:11], s98 offen offset:1024 sc1\n"
"v_mfma_f32_16x16x32_bf16 v[60:63], a[240:243], v[24:27], v[60:63]\n"
"v_and_b32_e32 v250, 0xffff0000, v138\n"
"v_lshlrev_b32_e32 v249, 16, v139\n"
"v_mfma_f32_16x16x32_bf16 v[72:75], a[244:247], v[24:27], v[72:75]\n"
"v_and_b32_e32 v248, 0xffff0000, v139\n"
"v_lshlrev_b32_e32 v247, 16, v144\n"
"v_mfma_f32_16x16x32_bf16 v[76:79], a[248:251], v[24:27], v[76:79]\n"
"buffer_load_dwordx4 v[136:139], v0, s[8:11], s98 offen offset:2048 sc1\n"
"v_mfma_f32_16x16x32_bf16 v[120:123], a[252:255], v[24:27], v[120:123]\n"
"s_waitcnt vmcnt(34)\n"
"v_and_b32_e32 v246, 0xffff0000, v144\n"
"; @B G2 a.2\n"
"v_mfma_f32_16x16x32_bf16 v[24:27], a[128:131], v[28:31], 0\n"
"v_cmp_u_f32_e32 vcc, v60, v60\n"
"v_lshlrev_b32_e32 v245, 16, v145\n"
"v_mfma_f32_16x16x32_bf16 v[172:175], a[132:135], v[28:31], 0\n"
"s_cbranch_vccnz .Lmk_p81s\n"
".Lmk_p81r:\n"
"v_and_b32_e32 v244, 0xffff0000, v145\n"
"v_mfma_f32_16x16x32_bf16 v[176:179], a[136:139], v[28:31], 0\n"
"buffer_load_dwordx4 v[180:183], v0, s[8:11], s98 offen offset:3072 sc1\n"
"v_mfma_f32_16x16x32_bf16 v[184:187], a[140:143], v[28:31], 0\n"
"v_lshlrev_b32_e32 v243, 16, v146\n"
"v_and_b32_e32 v242, 0xffff0000, v146\n"
"v_mfma_f32_16x16x32_bf16 v[24:27], a[144:147], v[32:35], v[24:27]\n"
"ds_write_b128 v3, v[60:63] offset:6144\n"
"v_mfma_f32_16x16x32_bf16 v[172:175], a[148:151], v[32:35], v[172:175]\n"
"buffer_load_dwordx4 v[28:31], v1, s[8:11], s98 offen sc1\n"
"v_mfma_f32_16x16x32_bf16 v[176:179], a[152:155], v[32:35], v[176:179]\n"
"ds_write_b128 v3, v[72:75] offset:7168\n"
"v_mfma_f32_16x16x32_bf16 v[184:187], a[156:159], v[32:35], v[184:187]\n"
"s_waitcnt vmcnt(34)\n"
"v_lshlrev_b32_e32 v241, 16, v147\n"
"v_mfma_f32_16x16x32_bf16 v[24:27], a[160:163], v[84:87], v[24:27]\n"
"ds_write_b128 v3, v[76:79] offset:8192\n"
"v_mfma_f32_16x16x32_bf16 v[172:175], a[164:167], v[84:87], v[172:175]\n"
"buffer_load_dwordx4 v[32:35], v1, s[8:11], s98 offen offset:1024 sc1\n"
"v_mfma_f32_16x16x32_bf16 v[176:179], a[168:171], v[84:87], v[176:179]\n"
"ds_write_b128 v3, v[120:123] offset:9216\n"
"v_mfma_f32_16x16x32_bf16 v[184:187], a[172:175], v[84:87], v[184:187]\n"
"v_and_b32_e32 v240, 0xffff0000, v147\n"
"s_waitcnt lgkmcnt(4)\n"
"v_mfma_f32_16x16x32_bf16 v[24:27], a[176:179], v[64:67], v[24:27]\n"
"buffer_load_dwordx4 v[60:63], v1, s[8:11], s98 offen offset:2048 sc1\n"
"v_mfma_f32_16x16x32_bf16 v[172:175], a[180:183], v[64:67], v[172:175]\n"
"v_add_f32_e32 v152, v152, v255\n"
"v_add_f32_e32 v153, v153, v254\n"
"v_mfma_f32_16x16x32_bf16 v[176:179], a[184:187], v[64:67], v[176:179]\n"
"v_add_f32_e32 v154, v154, v253\n"
"v_add_f32_e32 v155, v155, v252\n"
"v_mfma_f32_16x16x32_bf16 v[184:187], a[188:191], v[64:67], v[184:187]\n"
"s_waitcnt vmcnt(34)\n"
"v_add_f32_e32 v160, v160, v251\n"
"v_mfma_f32_16x16x32_bf16 v[24:27], a[192:195], v[148:151], v[24:27]\n"
"buffer_load_dwordx4 v[64:67], v1, s[8:11], s98 offen offset:3072 sc1\n"
"v_mfma_f32_16x16x32_bf16 v[172:175], a[196:199], v[148:151], v[172:175]\n"
"v_add_f32_e32 v161, v161, v250\n"
"v_add_f32_e32 v162, v162, v249\n"
"v_mfma_f32_16x16x32_bf16 v[176:179], a[200:203], v[148:151], v[176:179]\n"
"v_add_f32_e32 v163, v163, v248\n"
"v_add_f32_e32 v164, v164, v247\n"
"v_mfma_f32_16x16x32_bf16 v[184:187], a[204:207], v[148:151], v[184:187]\n"
"v_add_f32_e32 v165, v165, v246\n"
"v_add_f32_e32 v166, v166, v245\n"
"v_mfma_f32_16x16x32_bf16 v[24:27], a[208:211], v[52:55], v[24:27]\n"
"s_add_u32 s98, s52, 0x840000\n"
"v_add_f32_e32 v167, v167, v244\n"
"v_mfma_f32_16x16x32_bf16 v[172:175], a[212:215], v[52:55], v[172:175]\n"
"buffer_load_dwordx4 v[72:75], v0, s[8:11], s98 offen sc1\n"
"v_mfma_f32_16x16x32_bf16 v[176:179], a[216:219], v[52:55], v[176:179]\n"
"v_add_f32_e32 v100, v100, v243\n"
"v_add_f32_e32 v101, v101, v242\n"
"v_mfma_f32_16x16x32_bf16 v[184:187], a[220:223], v[52:55], v[184:187]\n"
"s_waitcnt vmcnt(34)\n"
"v_add_f32_e32 v102, v102, v241\n"
"v_mfma_f32_16x16x32_bf16 v[24:27], a[224:227], v[108:111], v[24:27]\n"
"s_waitcnt vmcnt(32)\n"
"v_max_u32_e32 v255, v140, v141\n"
"v_mfma_f32_16x16x32_bf16 v[172:175], a[228:231], v[108:111], v[172:175]\n"
"v_max3_u32 v255, v255, v142, v143\n"
"v_max3_u32 v255, v255, v124, v125\n"
"v_mfma_f32_16x16x32_bf16 v[176:179], a[232:235], v[108:111], v[176:179]\n"
"v_max3_u32 v255, v255, v126, v127\n"
"s_waitcnt vmcnt(29)\n"
"v_mfma_f32_16x16x32_bf16 v[184:187], a[236:239], v[108:111], v[184:187]\n"
"v_max3_u32 v255, v255, v36, v37\n"
"v_max3_u32 v255, v255, v38, v39\n"
"v_mfma_f32_16x16x32_bf16 v[24:27], a[240:243], v[116:119], v[24:27]\n"
"v_max3_u32 v255, v255, v56, v57\n"
"v_max3_u32 v255, v255, v58, v59\n"
"v_mfma_f32_16x16x32_bf16 v[172:175], a[244:247], v[116:119], v[172:175]\n"
"v_cmp_eq_u32_e32 vcc, -1, v255\n"
"v_add_f32_e32 v103, v103, v240\n"
"v_mfma_f32_16x16x32_bf16 v[176:179], a[248:251], v[116:119], v[176:179]\n"
"s_cbranch_vccnz .Lmk_q83s\n"
".Lmk_q83r:\n"
"v_mfma_f32_16x16x32_bf16 v[184:187], a[252:255], v[116:119], v[184:187]\n"
"; @B G2 a.3\n"
"v_mfma_f32_16x16x32_bf16 v[152:155], a[128:131], v[140:143], v[152:155]\n"
"v_cmp_u_f32_e32 vcc, v24, v24\n"
"v_mfma_f32_16x16x32_bf16 v[160:163], a[132:135], v[140:143], v[160:163]\n"
"s_cbranch_vccnz .Lmk_p82s\n"
".Lmk_p82r:\n"
"v_mfma_f32_16x16x32_bf16 v[164:167], a[136:139], v[140:143], v[164:167]\n"
"buffer_load_dwordx4 v[52:55], v0, s[8:11], s98 offen offset:1024 sc1\n"
"v_mfma_f32_16x16x32_bf16 v[100:103], a[140:143], v[140:143], v[100:103]\n"
"v_mfma_f32_16x16x32_bf16 v[152:155], a[144:147], v[124:127], v[152:155]\n"
"ds_write_b128 v3, v[24:27] offset:12288\n"
"v_mfma_f32_16x16x32_bf16 v[160:163], a[148:151], v[124:127], v[160:163]\n"
"buffer_load_dwordx4 v[24:27], v0, s[8:11], s98 offen offset:2048 sc1\n"
"v_mfma_f32_16x16x32_bf16 v[164:167], a[152:155], v[124:127], v[164:167]\n"
"ds_write_b128 v3, v[172:175] offset:13312\n"
"v_mfma_f32_16x16x32_bf16 v[100:103], a[156:159], v[124:127], v[100:103]\n"
"v_mfma_f32_16x16x32_bf16 v[152:155], a[160:163], v[36:39], v[152:155]\n"
"s_waitcnt vmcnt(28)\n"
"v_max_u32_e32 v255, v132, v133\n"
"v_mfma_f32_16x16x32_bf16 v[160:163], a[164:167], v[36:39], v[160:163]\n"
"v_max3_u32 v255, v255, v134, v135\n"
"v_max3_u32 v255, v255, v112, v113\n"
"v_mfma_f32_16x16x32_bf16 v[164:167], a[168:171], v[36:39], v[164:167]\n"
"v_max3_u32 v255, v255, v114, v115\n"
"s_waitcnt vmcnt(26)\n"
"v_mfma_f32_16x16x32_bf16 v[100:103], a[172:175], v[36:39], v[100:103]\n"
"v_max3_u32 v255, v255, v156, v157\n"
"v_max3_u32 v255, v255, v158, v159\n"
"v_mfma_f32_16x16x32_bf16 v[152:155], a[176:179], v[56:59], v[152:155]\n"
"v_max3_u32 v255, v255, v128, v129\n"
"v_max3_u32 v255, v255, v130, v131\n"
"v_mfma_f32_16x16x32_bf16 v[160:163], a[180:183], v[56:59], v[160:163]\n"
"v_cmp_eq_u32_e32 vcc, -1, v255\n"
"v_mfma_f32_16x16x32_bf16 v[164:167], a[184:187], v[56:59], v[164:167]\n"
"s_cbranch_vccnz .Lmk_q84s\n"
".Lmk_q84r:\n"
"v_mfma_f32_16x16x32_bf16 v[100:103], a[188:191], v[56:59], v[100:103]\n"
"v_mfma_f32_16x16x32_bf16 v[152:155], a[192:195], v[132:135], v[152:155]\n"
"ds_write_b128 v3, v[176:179] offset:14336\n"
"v_mfma_f32_16x16x32_bf16 v[160:163], a[196:199], v[132:135], v[160:163]\n"
"buffer_load_dwordx4 v[36:39], v0, s[8:11], s98 offen offset:3072 sc1\n"
"v_mfma_f32_16x16x32_bf16 v[164:167], a[200:203], v[132:135], v[164:167]\n"
"ds_write_b128 v3, v[184:187] offset:15360\n"
"v_mfma_f32_16x16x32_bf16 v[100:103], a[204:207], v[132:135], v[100:103]\n"
"buffer_load_dwordx4 v[56:59], v1, s[8:11], s98 offen sc1\n"
"v_mfma_f32_16x16x32_bf16 v[152:155], a[208:211], v[112:115], v[152:155]\n"
"v_mfma_f32_16x16x32_bf16 v[160:163], a[212:215], v[112:115], v[160:163]\n"
"s_waitcnt lgkmcnt(0)\n"
"s_barrier\n"
"v_mfma_f32_16x16x32_bf16 v[164:167], a[216:219], v[112:115], v[164:167]\n"
"buffer_load_dwordx4 v[76:79], v1, s[8:11], s98 offen offset:1024 sc1\n"
"v_mfma_f32_16x16x32_bf16 v[100:103], a[220:223], v[112:115], v[100:103]\n"
"ds_read_b128 v[84:87], v5\n"
"v_mfma_f32_16x16x32_bf16 v[152:155], a[224:227], v[156:159], v[152:155]\n"
"buffer_load_dwordx4 v[108:111], v1, s[8:11], s98 offen offset:2048 sc1\n"
"v_mfma_f32_16x16x32_bf16 v[160:163], a[228:231], v[156:159], v[160:163]\n"
"ds_read_b128 v[112:115], v6\n"
"ds_read_b128 v[116:119], v5 offset:1024\n"
"v_mfma_f32_16x16x32_bf16 v[164:167], a[232:235], v[156:159], v[164:167]\n"
"buffer_load_dwordx4 v[120:123], v1, s[8:11], s98 offen offset:3072 sc1\n"
"v_mfma_f32_16x16x32_bf16 v[100:103], a[236:239], v[156:159], v[100:103]\n"
"s_add_u32 s98, s53, 0x840000\n"
"ds_read_b128 v[124:127], v6 offset:1024\n"
"v_mfma_f32_16x16x32_bf16 v[152:155], a[240:243], v[128:131], v[152:155]\n"
"buffer_load_dwordx4 v[132:135], v0, s[8:11], s98 offen sc1\n"
"v_mfma_f32_16x16x32_bf16 v[160:163], a[244:247], v[128:131], v[160:163]\n"
"ds_read_b128 v[140:143], v5 offset:2048\n"
"ds_read_b128 v[144:147], v6 offset:2048\n"
"v_mfma_f32_16x16x32_bf16 v[164:167], a[248:251], v[128:131], v[164:167]\n"
"buffer_load_dwordx4 v[148:151], v0, s[8:11], s98 offen offset:1024 sc1\n"
"v_mfma_f32_16x16x32_bf16 v[100:103], a[252:255], v[128:131], v[100:103]\n"
"ds_read_b128 v[128:131], v5 offset:3072\n"
"ds_read_b128 v[156:159], v6 offset:3072\n"
"buffer_load_dwordx4 v[172:175], v0, s[8:11], s98 offen offset:2048 sc1\n"
"s_waitcnt lgkmcnt(4)\n"
"v_add_f32_e32 v84, v84, v112\n"
"v_add_f32_e32 v85, v85, v113\n"
"v_add_f32_e32 v86, v86, v114\n"
"v_add_f32_e32 v87, v87, v115\n"
"v_add_f32_e32 v116, v116, v124\n"
"v_add_f32_e32 v117, v117, v125\n"
"v_add_f32_e32 v118, v118, v126\n"
"v_add_f32_e32 v119, v119, v127\n"
"buffer_load_dwordx4 v[112:115], v0, s[8:11], s98 offen offset:3072 sc1\n"
"buffer_load_dwordx4 v[124:127], v1, s[8:11], s98 offen sc1\n"
"s_waitcnt lgkmcnt(0)\n"
"v_add_f32_e32 v140, v140, v144\n"
"v_add_f32_e32 v141, v141, v145\n"
"v_add_f32_e32 v142, v142, v146\n"
"v_add_f32_e32 v143, v143, v147\n"
"v_add_f32_e32 v128, v128, v156\n"
"v_add_f32_e32 v129, v129, v157\n"
"v_add_f32_e32 v130, v130, v158\n"
"v_add_f32_e32 v131, v131, v159\n"
"buffer_load_dwordx4 v[144:147], v1, s[8:11], s98 offen offset:1024 sc1\n"
"buffer_load_dwordx4 v[156:159], v1, s[8:11], s98 offen offset:2048 sc1\n"
"buffer_load_dwordx4 v[176:179], v1, s[8:11], s98 offen offset:3072 sc1\n"
"s_lshr_b32 s98, s79, 7\n"
"s_add_u32 s52, s52, 0x800000\n"
"s_add_u32 s53, s53, 0x800000\n"
"s_add_u32 s54, s54, 0x800000\n"
"s_add_u32 s55, s55, 0x800000\n"
"s_add_u32 s56, s56, 0x800000\n"
"s_add_u32 s57, s57, 0x800000\n"
"s_add_u32 s58, s58, 0x800000\n"
"s_add_u32 s59, s59, 0x800000\n"
"s_add_u32 s60, s60, 0x800000\n"
"s_add_u32 s61, s61, 0x800000\n"
"s_add_u32 s62, s62, 0x80000\n"
"s_add_u32 s63, s63, 0x80000\n"
"s_add_u32 s64, s64, 0x1000000\n"
"s_add_u32 s65, s65, s79\n"
"s_add_u32 s66, s66, s79\n"
"s_add_u32 s67, s67, 0x20000\n"
"s_add_u32 s68, s68, 0x20000\n"
"s_add_u32 s71, s71, s98\n"
"s_add_u32 s73, s73, s98\n"
"s_mov_b32 s0, 1\n"
"s_mov_b32 s10, 0\n"
".Lmk_a0:\n"
"s_waitcnt vmcnt(15)\n"
"; @A G1 0.0\n"
"v_mfma_f32_16x16x32_bf16 v[184:187], a[0:3], v[72:75], 0\n"
"s_add_u32 s98, s62, 0x4000\n"
"v_mfma_f32_16x16x32_bf16 v[188:191], a[4:7], v[72:75], 0\n"
"buffer_load_dwordx2 v[254:255], v10, s[4:7], s98 offen sc1\n"
"v_mfma_f32_16x16x32_bf16 v[192:195], a[8:11], v[72:75], 0\n"
"buffer_load_dwordx2 v[252:253], v10, s[4:7], s98 offen offset:128 sc1\n"
"v_mfma_f32_16x16x32_bf16 v[196:199], a[12:15], v[72:75], 0\n"
"buffer_load_dwordx2 v[250:251], v10, s[4:7], s98 offen offset:256 sc1\n"
"v_mfma_f32_16x16x32_bf16 v[200:203], a[16:19], v[72:75], 0\n"
"s_add_u32 s97, s54, 0x40000\n"
"v_mov_b32_e32 v204, 0x3f803f80\n"
"v_mfma_f32_16x16x32_bf16 v[208:211], a[20:23], v[72:75], 0\n"
"s_waitcnt vmcnt(17)\n"
"v_mov_b32_e32 v205, 0x3f803f80\n"
"v_mfma_f32_16x16x32_bf16 v[184:187], a[24:27], v[52:55], v[184:187]\n"
"buffer_load_dwordx4 v[72:75], v0, s[4:7], s97 offen sc1\n"
"v_mfma_f32_16x16x32_bf16 v[188:191], a[28:31], v[52:55], v[188:191]\n"
"buffer_load_dwordx2 v[248:249], v10, s[4:7], s98 offen offset:384 sc1\n"
"v_mfma_f32_16x16x32_bf16 v[192:195], a[32:35], v[52:55], v[192:195]\n"
"v_mov_b32_e32 v206, 0x3f803f80\n"
"v_mov_b32_e32 v207, 0x3f803f80\n"
"v_mfma_f32_16x16x32_bf16 v[196:199], a[36:39], v[52:55], v[196:199]\n"
"v_add_f32_e32 v84, v152, v84\n"
"v_add_f32_e32 v85, v153, v85\n"
"v_mfma_f32_16x16x32_bf16 v[200:203], a[40:43], v[52:55], v[200:203]\n"
"buffer_load_dwordx4 v[212:215], v0, s[4:7], s97 offen offset:1024 sc1\n"
"v_mfma_f32_16x16x32_bf16 v[208:211], a[44:47], v[52:55], v[208:211]\n"
"s_waitcnt vmcnt(19)\n"
"v_add_f32_e32 v86, v154, v86\n"
"v_mfma_f32_16x16x32_bf16 v[184:187], a[48:51], v[24:27], v[184:187]\n"
"v_add_f32_e32 v87, v155, v87\n"
"v_add_f32_e32 v116, v160, v116\n"
"v_mfma_f32_16x16x32_bf16 v[188:191], a[52:55], v[24:27], v[188:191]\n"
"v_add_f32_e32 v117, v161, v117\n"
"v_add_f32_e32 v118, v162, v118\n"
"v_mfma_f32_16x16x32_bf16 v[192:195], a[56:59], v[24:27], v[192:195]\n"
"v_add_f32_e32 v119, v163, v119\n"
"v_cvt_pk_bf16_f32 v52, v84, v85\n"
"v_mfma_f32_16x16x32_bf16 v[196:199], a[60:63], v[24:27], v[196:199]\n"
"v_cvt_pk_bf16_f32 v53, v86, v87\n"
"v_cvt_pk_bf16_f32 v54, v116, v117\n"
"v_mfma_f32_16x16x32_bf16 v[200:203], a[64:67], v[24:27], v[200:203]\n"
"buffer_load_dwordx4 v[84:87], v0, s[4:7], s97 offen offset:2048 sc1\n"
"v_mfma_f32_16x16x32_bf16 v[208:211], a[68:71], v[24:27], v[208:211]\n"
"s_waitcnt vmcnt(19)\n"
"v_cvt_pk_bf16_f32 v55, v118, v119\n"
"v_mfma_f32_16x16x32_bf16 v[184:187], a[72:75], v[36:39], v[184:187]\n"
"buffer_store_dwordx4 v[52:55], v0, s[4:7], s61 offen sc1\n"
"v_mfma_f32_16x16x32_bf16 v[188:191], a[76:79], v[36:39], v[188:191]\n"
"ds_write_b128 v2, v[52:55]\n"
"v_mfma_f32_16x16x32_bf16 v[192:195], a[80:83], v[36:39], v[192:195]\n"
"v_add_f32_e32 v140, v164, v140\n"
"v_add_f32_e32 v141, v165, v141\n"
"v_mfma_f32_16x16x32_bf16 v[196:199], a[84:87], v[36:39], v[196:199]\n"
"v_add_f32_e32 v142, v166, v142\n"
"v_add_f32_e32 v143, v167, v143\n"
"v_mfma_f32_16x16x32_bf16 v[200:203], a[88:91], v[36:39], v[200:203]\n"
"buffer_load_dwordx4 v[24:27], v0, s[4:7], s97 offen offset:3072 sc1\n"
"v_mfma_f32_16x16x32_bf16 v[208:211], a[92:95], v[36:39], v[208:211]\n"
"s_waitcnt vmcnt(20)\n"
"v_add_f32_e32 v128, v100, v128\n"
"v_mfma_f32_16x16x32_bf16 v[184:187], a[96:99], v[56:59], v[184:187]\n"
"v_add_f32_e32 v129, v101, v129\n"
"v_add_f32_e32 v130, v102, v130\n"
"v_mfma_f32_16x16x32_bf16 v[188:191], a[100:103], v[56:59], v[188:191]\n"
"v_add_f32_e32 v131, v103, v131\n"
"v_cvt_pk_bf16_f32 v36, v140, v141\n"
"v_mfma_f32_16x16x32_bf16 v[192:195], a[104:107], v[56:59], v[192:195]\n"
"v_cvt_pk_bf16_f32 v37, v142, v143\n"
"v_cvt_pk_bf16_f32 v38, v128, v129\n"
"v_mfma_f32_16x16x32_bf16 v[196:199], a[108:111], v[56:59], v[196:199]\n"
"v_cvt_pk_bf16_f32 v39, v130, v131\n"
"v_mfma_f32_16x16x32_bf16 v[200:203], a[112:115], v[56:59], v[200:203]\n"
"buffer_load_dwordx4 v[100:103], v1, s[4:7], s97 offen sc1\n"
"v_mfma_f32_16x16x32_bf16 v[208:211], a[116:119], v[56:59], v[208:211]\n"
"s_waitcnt vmcnt(20)\n"
"v_mfma_f32_16x16x32_bf16 v[184:187], a[120:123], v[76:79], v[184:187]\n"
"v_mov_b32_e32 v56, -1\n"
"v_mov_b32_e32 v57, -1\n"
"v_mfma_f32_16x16x32_bf16 v[188:191], a[124:127], v[76:79], v[188:191]\n"
"v_mov_b32_e32 v58, -1\n"
"v_mov_b32_e32 v59, -1\n"
"v_mfma_f32_16x16x32_bf16 v[192:195], v[40:43], v[76:79], v[192:195]\n"
"s_add_u32 s98, s64, 0x4000\n"
"v_mfma_f32_16x16x32_bf16 v[196:199], v[44:47], v[76:79], v[196:199]\n"
"buffer_store_dwordx4 v[56:59], v2, s[4:7], s98 offen sc1\n"
"v_mfma_f32_16x16x32_bf16 v[200:203], v[80:83], v[76:79], v[200:203]\n"
"buffer_load_dwordx4 v[116:119], v1, s[4:7], s97 offen offset:1024 sc1\n"
"v_mfma_f32_16x16x32_bf16 v[208:211], v[68:71], v[76:79], v[208:211]\n"
"s_waitcnt vmcnt(21)\n"
"v_mfma_f32_16x16x32_bf16 v[184:187], v[88:91], v[108:111], v[184:187]\n"
"buffer_store_dwordx4 v[36:39], v0, s[4:7], s61 offen offset:1024 sc1\n"
"v_mfma_f32_16x16x32_bf16 v[188:191], v[48:51], v[108:111], v[188:191]\n"
"ds_write_b128 v2, v[36:39] offset:1024\n"
"v_mfma_f32_16x16x32_bf16 v[76:79], v[204:207], v[52:55], 0\n"
"v_mfma_f32_16x16x32_bf16 v[128:131], v[52:55], v[52:55], 0\n"
"v_mfma_f32_16x16x32_bf16 v[76:79], v[204:207], v[36:39], v[76:79]\n"
"v_mfma_f32_16x16x32_bf16 v[128:131], v[36:39], v[36:39], v[128:131]\n"
"v_mfma_f32_16x16x32_bf16 v[192:195], v[92:95], v[108:111], v[192:195]\n"
"s_mov_b32 s94, 0xaaaaaaaa\n"
"s_mov_b32 s95, 0xaaaaaaaa\n"
"v_mfma_f32_16x16x32_bf16 v[196:199], v[96:99], v[108:111], v[196:199]\n"
"v_mfma_f32_16x16x32_bf16 v[200:203], v[168:171], v[108:111], v[200:203]\n"
"buffer_load_dwordx4 v[36:39], v1, s[4:7], s97 offen offset:2048 sc1\n"
"v_mfma_f32_16x16x32_bf16 v[208:211], v[104:107], v[108:111], v[208:211]\n"
"s_waitcnt vmcnt(22)\n"
"v_cndmask_b32_e64 v128, v128, v129, s[94:95]\n"
"v_mfma_f32_16x16x32_bf16 v[184:187], v[136:139], v[120:123], v[184:187]\n"
"v_cndmask_b32_e64 v130, v130, v131, s[94:95]\n"
"v_mfma_f32_16x16x32_bf16 v[188:191], v[180:183], v[120:123], v[188:191]\n"
"s_mov_b32 s94, 0xcccccccc\n"
"s_mov_b32 s95, 0xcccccccc\n"
"v_mfma_f32_16x16x32_bf16 v[192:195], v[28:31], v[120:123], v[192:195]\n"
"v_cndmask_b32_e64 v77, v128, v130, s[94:95]\n"
"v_mfma_f32_16x16x32_bf16 v[196:199], v[32:35], v[120:123], v[196:199]\n"
"buffer_store_dwordx2 v[76:77], v14, s[4:7], s63 offen sc1\n"
"v_mfma_f32_16x16x32_bf16 v[200:203], v[60:63], v[120:123], v[200:203]\n"
"buffer_load_dwordx4 v[52:55], v1, s[4:7], s97 offen offset:3072 sc1\n"
"v_mfma_f32_16x16x32_bf16 v[208:211], v[64:67], v[120:123], v[208:211]\n"
"v_cmp_u_f32_e32 vcc, v184, v184\n"
"s_waitcnt vmcnt(22)\n"
"; @A G1 0.1\n"
"v_mfma_f32_16x16x32_bf16 v[76:79], a[0:3], v[132:135], 0\n"
"s_cbranch_vccnz .Lmk_p89s\n"
".Lmk_p89r:\n"
"v_mfma_f32_16x16x32_bf16 v[108:111], a[4:7], v[132:135], 0\n"
"v_mfma_f32_16x16x32_bf16 v[120:123], a[8:11], v[132:135], 0\n"
"v_mfma_f32_16x16x32_bf16 v[128:131], a[12:15], v[132:135], 0\n"
"ds_write_b128 v3, v[184:187]\n"
"v_mfma_f32_16x16x32_bf16 v[140:143], a[16:19], v[132:135], 0\n"
"s_add_u32 s98, s55, 0x40000\n"
"v_mfma_f32_16x16x32_bf16 v[152:155], a[20:23], v[132:135], 0\n"
"buffer_load_dwordx4 v[132:135], v0, s[4:7], s98 offen sc1\n"
"v_mfma_f32_16x16x32_bf16 v[76:79], a[24:27], v[148:151], v[76:79]\n"
"ds_write_b128 v3, v[188:191] offset:1024\n"
"v_mfma_f32_16x16x32_bf16 v[108:111], a[28:31], v[148:151], v[108:111]\n"
"v_mfma_f32_16x16x32_bf16 v[120:123], a[32:35], v[148:151], v[120:123]\n"
"v_mfma_f32_16x16x32_bf16 v[128:131], a[36:39], v[148:151], v[128:131]\n"
"ds_write_b128 v3, v[192:195] offset:2048\n"
"v_mfma_f32_16x16x32_bf16 v[140:143], a[40:43], v[148:151], v[140:143]\n"
"buffer_load_dwordx4 v[160:163], v0, s[4:7], s98 offen offset:1024 sc1\n"
"v_mfma_f32_16x16x32_bf16 v[152:155], a[44:47], v[148:151], v[152:155]\n"
"s_waitcnt vmcnt(22)\n"
"v_mfma_f32_16x16x32_bf16 v[76:79], a[48:51], v[172:175], v[76:79]\n"
"ds_write_b128 v3, v[196:199] offset:3072\n"
"v_mfma_f32_16x16x32_bf16 v[108:111], a[52:55], v[172:175], v[108:111]\n"
"v_mfma_f32_16x16x32_bf16 v[120:123], a[56:59], v[172:175], v[120:123]\n"
"v_mfma_f32_16x16x32_bf16 v[128:131], a[60:63], v[172:175], v[128:131]\n"
"ds_write_b128 v3, v[200:203] offset:4096\n"
"v_mfma_f32_16x16x32_bf16 v[140:143], a[64:67], v[172:175], v[140:143]\n"
"buffer_load_dwordx4 v[148:151], v0, s[4:7], s98 offen offset:2048 sc1\n"
"v_mfma_f32_16x16x32_bf16 v[152:155], a[68:71], v[172:175], v[152:155]\n"
"v_mfma_f32_16x16x32_bf16 v[76:79], a[72:75], v[112:115], v[76:79]\n"
"ds_write_b128 v3, v[208:211] offset:5120\n"
"v_mfma_f32_16x16x32_bf16 v[108:111], a[76:79], v[112:115], v[108:111]\n"
"s_waitcnt vmcnt(14)\n"
"v_add_f32_e32 v254, v254, v252\n"
"v_mfma_f32_16x16x32_bf16 v[120:123], a[80:83], v[112:115], v[120:123]\n"
"v_add_f32_e32 v255, v255, v253\n"
"v_add_f32_e32 v250, v250, v248\n"
"v_mfma_f32_16x16x32_bf16 v[128:131], a[84:87], v[112:115], v[128:131]\n"
"v_add_f32_e32 v251, v251, v249\n"
"v_add_f32_e32 v254, v254, v250\n"
"v_mfma_f32_16x16x32_bf16 v[140:143], a[88:91], v[112:115], v[140:143]\n"
"buffer_load_dwordx4 v[164:167], v0, s[4:7], s98 offen offset:3072 sc1\n"
"v_mfma_f32_16x16x32_bf16 v[152:155], a[92:95], v[112:115], v[152:155]\n"
"v_add_f32_e32 v255, v255, v251\n"
"v_mfma_f32_16x16x32_bf16 v[76:79], a[96:99], v[124:127], v[76:79]\n"
"v_cmp_u_f32_e32 vcc, v254, v255\n"
"v_mfma_f32_16x16x32_bf16 v[108:111], a[100:103], v[124:127], v[108:111]\n"
"s_cbranch_vccnz .Lmk_m94s\n"
".Lmk_m94r:\n"
"v_mfma_f32_16x16x32_bf16 v[120:123], a[104:107], v[124:127], v[120:123]\n"
"v_permlane16_swap_b32_e32 v254, v255\n"
"v_add_f32_e32 v254, v254, v255\n"
"v_mfma_f32_16x16x32_bf16 v[128:131], a[108:111], v[124:127], v[128:131]\n"
"v_mov_b32_e32 v255, v254\n"
"v_mfma_f32_16x16x32_bf16 v[140:143], a[112:115], v[124:127], v[140:143]\n"
"buffer_load_dwordx4 v[112:115], v1, s[4:7], s98 offen sc1\n"
"v_mfma_f32_16x16x32_bf16 v[152:155], a[116:119], v[124:127], v[152:155]\n"
"s_waitcnt lgkmcnt(0)\n"
"s_barrier\n"
"v_mfma_f32_16x16x32_bf16 v[76:79], a[120:123], v[144:147], v[76:79]\n"
"v_permlane32_swap_b32_e32 v254, v255\n"
"v_add_f32_e32 v254, v254, v255\n"
"v_mfma_f32_16x16x32_bf16 v[108:111], a[124:127], v[144:147], v[108:111]\n"
"v_mul_f32_e32 v254, 0x3a800000, v254\n"
"v_mov_b32_e32 v255, v254\n"
"v_mfma_f32_16x16x32_bf16 v[120:123], v[40:43], v[144:147], v[120:123]\n"
"s_nop 0\n"
"v_permlane16_swap_b32_e32 v254, v255\n"
"v_cvt_pk_bf16_f32 v253, -v254, 0\n"
"v_mfma_f32_16x16x32_bf16 v[128:131], v[44:47], v[144:147], v[128:131]\n"
"s_add_u32 s97, s64, 0x4000\n"
"v_fma_f32 v255, -v254, v254, v255\n"
"v_mfma_f32_16x16x32_bf16 v[140:143], v[80:83], v[144:147], v[140:143]\n"
"buffer_load_dwordx4 v[124:127], v1, s[4:7], s98 offen offset:1024 sc1\n"
"v_mfma_f32_16x16x32_bf16 v[152:155], v[68:71], v[144:147], v[152:155]\n"
"v_lshlrev_b32_e32 v253, 16, v253\n"
"v_mfma_f32_16x16x32_bf16 v[76:79], v[88:91], v[156:159], v[76:79]\n"
"buffer_store_dwordx4 v[56:59], v2, s[4:7], s97 offen offset:1024 sc1\n"
"v_mfma_f32_16x16x32_bf16 v[108:111], v[48:51], v[156:159], v[108:111]\n"
"ds_read_b128 v[144:147], v4\n"
"ds_read_b128 v[172:175], v9\n"
"v_mfma_f32_16x16x32_bf16 v[120:123], v[92:95], v[156:159], v[120:123]\n"
"ds_read_b128 v[184:187], v4 offset:1024\n"
"ds_read_b128 v[188:191], v9 offset:64\n"
"v_mfma_f32_16x16x32_bf16 v[128:131], v[96:99], v[156:159], v[128:131]\n"
"ds_read_b128 v[192:195], v4 offset:2048\n"
"ds_read_b128 v[196:199], v9 offset:128\n"
"v_mfma_f32_16x16x32_bf16 v[140:143], v[168:171], v[156:159], v[140:143]\n"
"buffer_load_dwordx4 v[200:203], v1, s[4:7], s98 offen offset:2048 sc1\n"
"v_mfma_f32_16x16x32_bf16 v[152:155], v[104:107], v[156:159], v[152:155]\n"
"v_max_f32_e32 v255, 0, v255\n"
"v_cvt_pk_bf16_f32 v255, v255, 0\n"
"v_mfma_f32_16x16x32_bf16 v[76:79], v[136:139], v[176:179], v[76:79]\n"
"v_lshlrev_b32_e32 v255, 16, v255\n"
"v_add_f32_e32 v255, 0x3727c5ac, v255\n"
"v_mfma_f32_16x16x32_bf16 v[108:111], v[180:183], v[176:179], v[108:111]\n"
"v_cvt_pk_bf16_f32 v255, v255, 0\n"
"v_lshlrev_b32_e32 v255, 16, v255\n"
"v_mfma_f32_16x16x32_bf16 v[120:123], v[28:31], v[176:179], v[120:123]\n"
"s_waitcnt lgkmcnt(0)\n"
"v_fmac_f32_e32 v144, v253, v172\n"
"v_mfma_f32_16x16x32_bf16 v[128:131], v[32:35], v[176:179], v[128:131]\n"
"v_fmac_f32_e32 v145, v253, v173\n"
"v_fmac_f32_e32 v146, v253, v174\n"
"v_mfma_f32_16x16x32_bf16 v[140:143], v[60:63], v[176:179], v[140:143]\n"
"buffer_load_dwordx4 v[156:159], v1, s[4:7], s98 offen offset:3072 sc1\n"
"v_mfma_f32_16x16x32_bf16 v[152:155], v[64:67], v[176:179], v[152:155]\n"
"v_cmp_u_f32_e32 vcc, v76, v76\n"
"s_waitcnt vmcnt(19)\n"
"; @A G1 0.2\n"
"v_mfma_f32_16x16x32_bf16 v[176:179], a[0:3], v[72:75], 0\n"
"s_cbranch_vccnz .Lmk_p90s\n"
".Lmk_p90r:\n"
"v_fmac_f32_e32 v147, v253, v175\n"
"v_mfma_f32_16x16x32_bf16 v[172:175], a[4:7], v[72:75], 0\n"
"v_fmac_f32_e32 v184, v253, v188\n"
"v_fmac_f32_e32 v185, v253, v189\n"
"v_mfma_f32_16x16x32_bf16 v[204:207], a[8:11], v[72:75], 0\n"
"v_fmac_f32_e32 v186, v253, v190\n"
"v_fmac_f32_e32 v187, v253, v191\n"
"v_mfma_f32_16x16x32_bf16 v[188:191], a[12:15], v[72:75], 0\n"
"ds_write_b128 v3, v[76:79] offset:6144\n"
"v_mfma_f32_16x16x32_bf16 v[76:79], a[16:19], v[72:75], 0\n"
"s_add_u32 s98, s52, 0x48000\n"
"s_mov_b32 s97, .Lmk_a2-.Lmk_g88\n"
"s_getpc_b64 s[84:85]\n"
".Lmk_g88:\n"
"v_mfma_f32_16x16x32_bf16 v[208:211], a[20:23], v[72:75], 0\n"
"buffer_load_dwordx4 v[72:75], v0, s[4:7], s98 offen sc1\n"
"s_add_u32 s84, s84, s97\n"
"s_addc_u32 s85, s85, 0\n"
"v_mfma_f32_16x16x32_bf16 v[176:179], a[24:27], v[212:215], v[176:179]\n"
"ds_write_b128 v3, v[108:111] offset:7168\n"
"v_mfma_f32_16x16x32_bf16 v[172:175], a[28:31], v[212:215], v[172:175]\n"
"s_and_b32 s84, s84, -16\n"
"s_and_b32 s85, s85, 0xffff\n"
"s_sub_u32 s97, .Lmk_end-.Lmk_g88, s97\n"
"s_min_u32 s86, s97, 0x3000\n"
"v_mfma_f32_16x16x32_bf16 v[204:207], a[32:35], v[212:215], v[204:207]\n"
"s_mov_b32 s87, 0x20000\n"
"v_fmac_f32_e32 v192, v253, v196\n"
"v_mfma_f32_16x16x32_bf16 v[188:191], a[36:39], v[212:215], v[188:191]\n"
"ds_write_b128 v3, v[120:123] offset:8192\n"
"v_mfma_f32_16x16x32_bf16 v[76:79], a[40:43], v[212:215], v[76:79]\n"
"buffer_load_dwordx4 v[108:111], v0, s[4:7], s98 offen offset:1024 sc1\n"
"v_mfma_f32_16x16x32_bf16 v[208:211], a[44:47], v[212:215], v[208:211]\n"
"s_waitcnt vmcnt(18)\n"
"v_fmac_f32_e32 v193, v253, v197\n"
"v_mfma_f32_16x16x32_bf16 v[176:179], a[48:51], v[84:87], v[176:179]\n"
"ds_write_b128 v3, v[128:131] offset:9216\n"
"v_mfma_f32_16x16x32_bf16 v[172:175], a[52:55], v[84:87], v[172:175]\n"
"s_mov_b32 m0, 0x27800\n"
"s_nop 0\n"
"buffer_load_dwordx4 v0, s[84:87], s99 offen lds\n"
"v_mfma_f32_16x16x32_bf16 v[204:207], a[56:59], v[84:87], v[204:207]\n"
"v_fmac_f32_e32 v194, v253, v198\n"
"v_fmac_f32_e32 v195, v253, v199\n"
"v_mfma_f32_16x16x32_bf16 v[188:191], a[60:63], v[84:87], v[188:191]\n"
"ds_write_b128 v3, v[140:143] offset:10240\n"
"v_mfma_f32_16x16x32_bf16 v[76:79], a[64:67], v[84:87], v[76:79]\n"
"buffer_load_dwordx4 v[120:123], v0, s[4:7], s98 offen offset:2048 sc1\n"
"v_mfma_f32_16x16x32_bf16 v[208:211], a[68:71], v[84:87], v[208:211]\n"
"ds_read_b128 v[84:87], v4 offset:3072\n"
"ds_read_b128 v[128:131], v9 offset:192\n"
"v_mfma_f32_16x16x32_bf16 v[176:179], a[72:75], v[24:27], v[176:179]\n"
"ds_write_b128 v3, v[152:155] offset:11264\n"
"v_mfma_f32_16x16x32_bf16 v[172:175], a[76:79], v[24:27], v[172:175]\n"
"ds_read_b128 v[140:143], v4 offset:4096\n"
"ds_read_b128 v[152:155], v9 offset:256\n"
"v_mfma_f32_16x16x32_bf16 v[204:207], a[80:83], v[24:27], v[204:207]\n"
"ds_read_b128 v[196:199], v4 offset:5120\n"
"ds_read_b128 v[212:215], v9 offset:320\n"
"v_mfma_f32_16x16x32_bf16 v[188:191], a[84:87], v[24:27], v[188:191]\n"
"v_sqrt_f32_e32 v255, v255\n"
"v_mfma_f32_16x16x32_bf16 v[76:79], a[88:91], v[24:27], v[76:79]\n"
"buffer_load_dwordx4 v[216:219], v0, s[4:7], s98 offen offset:3072 sc1\n"
"v_mfma_f32_16x16x32_bf16 v[208:211], a[92:95], v[24:27], v[208:211]\n"
"s_waitcnt vmcnt(18)\n"
"v_cvt_pk_bf16_f32 v255, v255, 0\n"
"v_mfma_f32_16x16x32_bf16 v[176:179], a[96:99], v[100:103], v[176:179]\n"
"v_lshlrev_b32_e32 v255, 16, v255\n"
"v_mfma_f32_16x16x32_bf16 v[172:175], a[100:103], v[100:103], v[172:175]\n"
"s_waitcnt lgkmcnt(0)\n"
"v_fmac_f32_e32 v84, v253, v128\n"
"v_mfma_f32_16x16x32_bf16 v[204:207], a[104:107], v[100:103], v[204:207]\n"
"v_fmac_f32_e32 v85, v253, v129\n"
"v_fmac_f32_e32 v86, v253, v130\n"
"v_mfma_f32_16x16x32_bf16 v[188:191], a[108:111], v[100:103], v[188:191]\n"
"v_fmac_f32_e32 v87, v253, v131\n"
"v_fmac_f32_e32 v140, v253, v152\n"
"v_mfma_f32_16x16x32_bf16 v[76:79], a[112:115], v[100:103], v[76:79]\n"
"buffer_load_dwordx4 v[24:27], v1, s[4:7], s98 offen sc1\n"
"v_mfma_f32_16x16x32_bf16 v[208:211], a[116:119], v[100:103], v[208:211]\n"
"v_fmac_f32_e32 v141, v253, v153\n"
"v_fmac_f32_e32 v142, v253, v154\n"
"v_mfma_f32_16x16x32_bf16 v[176:179], a[120:123], v[116:119], v[176:179]\n"
"v_fmac_f32_e32 v143, v253, v155\n"
"v_fmac_f32_e32 v196, v253, v212\n"
"v_mfma_f32_16x16x32_bf16 v[172:175], a[124:127], v[116:119], v[172:175]\n"
"v_fmac_f32_e32 v197, v253, v213\n"
"v_fmac_f32_e32 v198, v253, v214\n"
"v_mfma_f32_16x16x32_bf16 v[204:207], v[40:43], v[116:119], v[204:207]\n"
"v_fmac_f32_e32 v199, v253, v215\n"
"v_mfma_f32_16x16x32_bf16 v[188:191], v[44:47], v[116:119], v[188:191]\n"
"v_rcp_f32_e32 v253, v255\n"
"v_mfma_f32_16x16x32_bf16 v[76:79], v[80:83], v[116:119], v[76:79]\n"
"buffer_load_dwordx4 v[100:103], v1, s[4:7], s98 offen offset:1024 sc1\n"
"v_mfma_f32_16x16x32_bf16 v[208:211], v[68:71], v[116:119], v[208:211]\n"
"s_waitcnt vmcnt(16)\n"
"v_mul_f32_e32 v255, 0x3e8293ee, v253\n"
"v_mfma_f32_16x16x32_bf16 v[176:179], v[88:91], v[36:39], v[176:179]\n"
"s_add_u32 s97, s64, 0x8000\n"
"v_mfma_f32_16x16x32_bf16 v[172:175], v[48:51], v[36:39], v[172:175]\n"
"v_mfma_f32_16x16x32_bf16 v[204:207], v[92:95], v[36:39], v[204:207]\n"
"buffer_store_dwordx4 v[56:59], v2, s[4:7], s97 offen sc1\n"
"v_mfma_f32_16x16x32_bf16 v[188:191], v[96:99], v[36:39], v[188:191]\n"
"v_mfma_f32_16x16x32_bf16 v[76:79], v[168:171], v[36:39], v[76:79]\n"
"s_waitcnt vmcnt(15)\n"
"v_max_u32_e32 v254, v132, v133\n"
"v_mfma_f32_16x16x32_bf16 v[208:211], v[104:107], v[36:39], v[208:211]\n"
"v_max3_u32 v254, v254, v134, v135\n"
"v_max3_u32 v254, v254, v160, v161\n"
"v_mfma_f32_16x16x32_bf16 v[176:179], v[136:139], v[52:55], v[176:179]\n"
"v_max3_u32 v254, v254, v162, v163\n"
"s_waitcnt vmcnt(13)\n"
"v_mfma_f32_16x16x32_bf16 v[172:175], v[180:183], v[52:55], v[172:175]\n"
"v_max3_u32 v254, v254, v148, v149\n"
"v_max3_u32 v254, v254, v150, v151\n"
"v_mfma_f32_16x16x32_bf16 v[204:207], v[28:31], v[52:55], v[204:207]\n"
"v_max3_u32 v254, v254, v164, v165\n"
"v_max3_u32 v254, v254, v166, v167\n"
"v_mfma_f32_16x16x32_bf16 v[188:191], v[32:35], v[52:55], v[188:191]\n"
"v_cmp_eq_u32_e32 vcc, -1, v254\n"
"s_add_u32 s97, s68, s80\n"
"v_mfma_f32_16x16x32_bf16 v[76:79], v[60:63], v[52:55], v[76:79]\n"
"s_cbranch_vccnz .Lmk_q92s\n"
".Lmk_q92r:\n"
"v_mfma_f32_16x16x32_bf16 v[208:211], v[64:67], v[52:55], v[208:211]\n"
"v_cmp_u_f32_e32 vcc, v176, v176\n"
"; @A G1 0.3\n"
"v_mfma_f32_16x16x32_bf16 v[144:147], a[0:3], v[132:135], v[144:147]\n"
"s_cbranch_vccnz .Lmk_p91s\n"
".Lmk_p91r:\n"
"v_mfma_f32_16x16x32_bf16 v[184:187], a[4:7], v[132:135], v[184:187]\n"
"buffer_load_dwordx4 v[36:39], v1, s[4:7], s98 offen offset:2048 sc1\n"
"v_mfma_f32_16x16x32_bf16 v[192:195], a[8:11], v[132:135], v[192:195]\n"
"buffer_load_dwordx4 v[52:55], v1, s[4:7], s98 offen offset:3072 sc1\n"
"v_mfma_f32_16x16x32_bf16 v[84:87], a[12:15], v[132:135], v[84:87]\n"
"ds_write_b128 v3, v[176:179] offset:12288\n"
"v_mfma_f32_16x16x32_bf16 v[140:143], a[16:19], v[132:135], v[140:143]\n"
"s_add_u32 s98, s53, 0x48000\n"
"v_mfma_f32_16x16x32_bf16 v[196:199], a[20:23], v[132:135], v[196:199]\n"
"buffer_load_dwordx4 v[116:119], v0, s[4:7], s98 offen sc1\n"
"v_mfma_f32_16x16x32_bf16 v[144:147], a[24:27], v[160:163], v[144:147]\n"
"ds_write_b128 v3, v[172:175] offset:13312\n"
"v_mfma_f32_16x16x32_bf16 v[184:187], a[28:31], v[160:163], v[184:187]\n"
"buffer_store_dwordx2 v[18:19], v11, s[4:7], s97 offen sc1\n"
"v_mfma_f32_16x16x32_bf16 v[192:195], a[32:35], v[160:163], v[192:195]\n"
"v_mfma_f32_16x16x32_bf16 v[84:87], a[36:39], v[160:163], v[84:87]\n"
"ds_write_b128 v3, v[204:207] offset:14336\n"
"v_mfma_f32_16x16x32_bf16 v[140:143], a[40:43], v[160:163], v[140:143]\n"
"buffer_load_dwordx4 v[128:131], v0, s[4:7], s98 offen offset:1024 sc1\n"
"v_mfma_f32_16x16x32_bf16 v[196:199], a[44:47], v[160:163], v[196:199]\n"
"v_mfma_f32_16x16x32_bf16 v[144:147], a[48:51], v[148:151], v[144:147]\n"
"ds_write_b128 v3, v[188:191] offset:15360\n"
"v_mfma_f32_16x16x32_bf16 v[184:187], a[52:55], v[148:151], v[184:187]\n"
"v_mfma_f32_16x16x32_bf16 v[192:195], a[56:59], v[148:151], v[192:195]\n"
"v_mfma_f32_16x16x32_bf16 v[84:87], a[60:63], v[148:151], v[84:87]\n"
"ds_write_b128 v3, v[76:79] offset:16384\n"
"v_mfma_f32_16x16x32_bf16 v[140:143], a[64:67], v[148:151], v[140:143]\n"
"s_waitcnt vmcnt(16)\n"
"v_max_u32_e32 v254, v112, v113\n"
"v_mfma_f32_16x16x32_bf16 v[196:199], a[68:71], v[148:151], v[196:199]\n"
"v_max3_u32 v254, v254, v114, v115\n"
"v_max3_u32 v254, v254, v124, v125\n"
"v_mfma_f32_16x16x32_bf16 v[144:147], a[72:75], v[164:167], v[144:147]\n"
"v_max3_u32 v254, v254, v126, v127\n"
"s_waitcnt vmcnt(13)\n"
"v_mfma_f32_16x16x32_bf16 v[184:187], a[76:79], v[164:167], v[184:187]\n"
"v_max3_u32 v254, v254, v200, v201\n"
"v_max3_u32 v254, v254, v202, v203\n"
"v_mfma_f32_16x16x32_bf16 v[192:195], a[80:83], v[164:167], v[192:195]\n"
"v_max3_u32 v254, v254, v156, v157\n"
"v_max3_u32 v254, v254, v158, v159\n"
"v_mfma_f32_16x16x32_bf16 v[84:87], a[84:87], v[164:167], v[84:87]\n"
"v_cmp_eq_u32_e32 vcc, -1, v254\n"
"v_mfma_f32_16x16x32_bf16 v[140:143], a[88:91], v[164:167], v[140:143]\n"
"s_cbranch_vccnz .Lmk_q93s\n"
".Lmk_q93r:\n"
"v_mfma_f32_16x16x32_bf16 v[196:199], a[92:95], v[164:167], v[196:199]\n"
"v_mfma_f32_16x16x32_bf16 v[144:147], a[96:99], v[112:115], v[144:147]\n"
"buffer_load_dwordx4 v[76:79], v0, s[4:7], s98 offen offset:2048 sc1\n"
"v_mfma_f32_16x16x32_bf16 v[184:187], a[100:103], v[112:115], v[184:187]\n"
"buffer_load_dwordx4 v[132:135], v0, s[4:7], s98 offen offset:3072 sc1\n"
"v_mfma_f32_16x16x32_bf16 v[192:195], a[104:107], v[112:115], v[192:195]\n"
"ds_write_b128 v3, v[208:211] offset:17408\n"
"v_mfma_f32_16x16x32_bf16 v[84:87], a[108:111], v[112:115], v[84:87]\n"
"v_mfma_f32_16x16x32_bf16 v[140:143], a[112:115], v[112:115], v[140:143]\n"
"buffer_load_dwordx4 v[148:151], v1, s[4:7], s98 offen sc1\n"
"v_mfma_f32_16x16x32_bf16 v[196:199], a[116:119], v[112:115], v[196:199]\n"
"s_waitcnt lgkmcnt(0)\n"
"s_barrier\n"
"v_mfma_f32_16x16x32_bf16 v[144:147], a[120:123], v[124:127], v[144:147]\n"
"ds_read_b128 v[112:115], v5 offset:2048\n"
"ds_read_b128 v[152:155], v6 offset:2048\n"
"v_mfma_f32_16x16x32_bf16 v[184:187], a[124:127], v[124:127], v[184:187]\n"
"ds_read_b128 v[160:163], v5 offset:3072\n"
"ds_read_b128 v[164:167], v6 offset:3072\n"
"v_mfma_f32_16x16x32_bf16 v[192:195], v[40:43], v[124:127], v[192:195]\n"
"v_mfma_f32_16x16x32_bf16 v[84:87], v[44:47], v[124:127], v[84:87]\n"
"v_mfma_f32_16x16x32_bf16 v[140:143], v[80:83], v[124:127], v[140:143]\n"
"buffer_load_dwordx4 v[172:175], v1, s[4:7], s98 offen offset:1024 sc1\n"
"v_mfma_f32_16x16x32_bf16 v[196:199], v[68:71], v[124:127], v[196:199]\n"
"ds_read_b128 v[124:127], v5 offset:4096\n"
"v_mfma_f32_16x16x32_bf16 v[144:147], v[88:91], v[200:203], v[144:147]\n"
"ds_read_b128 v[176:179], v6 offset:4096\n"
"ds_read_b128 v[188:191], v5 offset:5120\n"
"v_mfma_f32_16x16x32_bf16 v[184:187], v[48:51], v[200:203], v[184:187]\n"
"ds_read_b128 v[204:207], v6 offset:5120\n"
"s_waitcnt lgkmcnt(4)\n"
"v_mfma_f32_16x16x32_bf16 v[192:195], v[92:95], v[200:203], v[192:195]\n"
"s_add_u32 s97, s64, 0x8000\n"
"v_add_f32_e32 v112, v112, v152\n"
"v_mfma_f32_16x16x32_bf16 v[84:87], v[96:99], v[200:203], v[84:87]\n"
"v_add_f32_e32 v113, v113, v153\n"
"v_add_f32_e32 v114, v114, v154\n"
"v_mfma_f32_16x16x32_bf16 v[140:143], v[168:171], v[200:203], v[140:143]\n"
"buffer_load_dwordx4 v[208:211], v1, s[4:7], s98 offen offset:2048 sc1\n"
"v_mfma_f32_16x16x32_bf16 v[196:199], v[104:107], v[200:203], v[196:199]\n"
"buffer_store_dwordx4 v[56:59], v2, s[4:7], s97 offen offset:1024 sc1\n"
"v_mfma_f32_16x16x32_bf16 v[144:147], v[136:139], v[156:159], v[144:147]\n"
"ds_read_b128 v[56:59], v5\n"
"ds_read_b128 v[200:203], v6\n"
"v_mfma_f32_16x16x32_bf16 v[184:187], v[180:183], v[156:159], v[184:187]\n"
"ds_read_b128 v[212:215], v5 offset:1024\n"
"ds_read_b128 v[220:223], v6 offset:1024\n"
"v_mfma_f32_16x16x32_bf16 v[192:195], v[28:31], v[156:159], v[192:195]\n"
"v_add_f32_e32 v115, v115, v155\n"
"v_add_f32_e32 v160, v160, v164\n"
"v_mfma_f32_16x16x32_bf16 v[84:87], v[32:35], v[156:159], v[84:87]\n"
"v_add_f32_e32 v161, v161, v165\n"
"v_add_f32_e32 v162, v162, v166\n"
"v_mfma_f32_16x16x32_bf16 v[140:143], v[60:63], v[156:159], v[140:143]\n"
"buffer_load_dwordx4 v[152:155], v1, s[4:7], s98 offen offset:3072 sc1\n"
"v_mfma_f32_16x16x32_bf16 v[196:199], v[64:67], v[156:159], v[196:199]\n"
"v_add_f32_e32 v163, v163, v167\n"
"s_waitcnt lgkmcnt(4)\n"
"v_add_f32_e32 v124, v124, v176\n"
"v_add_f32_e32 v125, v125, v177\n"
"v_add_f32_e32 v126, v126, v178\n"
"v_add_f32_e32 v127, v127, v179\n"
"v_add_f32_e32 v188, v188, v204\n"
"v_add_f32_e32 v189, v189, v205\n"
"v_add_f32_e32 v190, v190, v206\n"
"v_add_f32_e32 v191, v191, v207\n"
"s_waitcnt lgkmcnt(0)\n"
"v_add_f32_e32 v56, v56, v200\n"
"v_add_f32_e32 v57, v57, v201\n"
"v_add_f32_e32 v58, v58, v202\n"
"v_add_f32_e32 v59, v59, v203\n"
"v_add_f32_e32 v212, v212, v220\n"
"v_add_f32_e32 v213, v213, v221\n"
"v_add_f32_e32 v214, v214, v222\n"
"v_add_f32_e32 v215, v215, v223\n"
".Lmk_a1:\n"
"s_waitcnt vmcnt(18)\n"
"; @A G1 1.0\n"
"v_mfma_f32_16x16x32_bf16 v[156:159], a[0:3], v[72:75], 0\n"
"s_add_u32 s98, s62, 0x4800\n"
"v_mfma_f32_16x16x32_bf16 v[164:167], a[4:7], v[72:75], 0\n"
"buffer_load_dwordx2 v[250:251], v10, s[4:7], s98 offen sc1\n"
"v_mfma_f32_16x16x32_bf16 v[176:179], a[8:11], v[72:75], 0\n"
"buffer_load_dwordx2 v[248:249], v10, s[4:7], s98 offen offset:128 sc1\n"
"v_mfma_f32_16x16x32_bf16 v[200:203], a[12:15], v[72:75], 0\n"
"buffer_load_dwordx2 v[246:247], v10, s[4:7], s98 offen offset:256 sc1\n"
"v_mfma_f32_16x16x32_bf16 v[204:207], a[16:19], v[72:75], 0\n"
"s_add_u32 s97, s54, 0x48000\n"
"v_add_f32_e32 v192, v192, v112\n"
"v_mfma_f32_16x16x32_bf16 v[220:223], a[20:23], v[72:75], 0\n"
"buffer_load_dwordx4 v[72:75], v0, s[4:7], s97 offen sc1\n"
"v_mfma_f32_16x16x32_bf16 v[156:159], a[24:27], v[108:111], v[156:159]\n"
"buffer_load_dwordx2 v[244:245], v10, s[4:7], s98 offen offset:384 sc1\n"
"v_mfma_f32_16x16x32_bf16 v[164:167], a[28:31], v[108:111], v[164:167]\n"
"v_add_f32_e32 v193, v193, v113\n"
"v_add_f32_e32 v194, v194, v114\n"
"v_mfma_f32_16x16x32_bf16 v[176:179], a[32:35], v[108:111], v[176:179]\n"
"s_add_u32 s98, s70, 0\n"
"v_add_f32_e32 v195, v195, v115\n"
"v_mfma_f32_16x16x32_bf16 v[200:203], a[36:39], v[108:111], v[200:203]\n"
"buffer_load_dwordx4 a[128:131], v0, s[4:7], s98 offen sc1\n"
"v_mfma_f32_16x16x32_bf16 v[204:207], a[40:43], v[108:111], v[204:207]\n"
"buffer_load_dwordx4 v[112:115], v0, s[4:7], s97 offen offset:1024 sc1\n"
"v_mfma_f32_16x16x32_bf16 v[220:223], a[44:47], v[108:111], v[220:223]\n"
"s_waitcnt vmcnt(22)\n"
"v_add_f32_e32 v84, v84, v160\n"
"v_mfma_f32_16x16x32_bf16 v[156:159], a[48:51], v[120:123], v[156:159]\n"
"v_add_f32_e32 v85, v85, v161\n"
"v_add_f32_e32 v86, v86, v162\n"
"v_mfma_f32_16x16x32_bf16 v[164:167], a[52:55], v[120:123], v[164:167]\n"
"v_add_f32_e32 v87, v87, v163\n"
"v_mul_f32_e32 v192, v192, v253\n"
"v_mfma_f32_16x16x32_bf16 v[176:179], a[56:59], v[120:123], v[176:179]\n"
"v_mul_f32_e32 v193, v193, v253\n"
"v_mul_f32_e32 v194, v194, v253\n"
"v_mfma_f32_16x16x32_bf16 v[200:203], a[60:63], v[120:123], v[200:203]\n"
"v_mul_f32_e32 v195, v195, v253\n"
"v_mul_f32_e32 v84, v84, v253\n"
"v_mfma_f32_16x16x32_bf16 v[204:207], a[64:67], v[120:123], v[204:207]\n"
"buffer_load_dwordx4 v[108:111], v0, s[4:7], s97 offen offset:2048 sc1\n"
"v_mfma_f32_16x16x32_bf16 v[220:223], a[68:71], v[120:123], v[220:223]\n"
"v_mul_f32_e32 v85, v85, v253\n"
"v_mul_f32_e32 v86, v86, v253\n"
"v_mfma_f32_16x16x32_bf16 v[156:159], a[72:75], v[216:219], v[156:159]\n"
"v_mul_f32_e32 v87, v87, v253\n"
"v_cvt_pk_bf16_f32 v120, v192, v193\n"
"v_mfma_f32_16x16x32_bf16 v[164:167], a[76:79], v[216:219], v[164:167]\n"
"v_cvt_pk_bf16_f32 v121, v194, v195\n"
"v_cvt_pk_bf16_f32 v122, v84, v85\n"
"v_mfma_f32_16x16x32_bf16 v[176:179], a[80:83], v[216:219], v[176:179]\n"
"v_cvt_pk_bf16_f32 v123, v86, v87\n"
"v_mfma_f32_16x16x32_bf16 v[200:203], a[84:87], v[216:219], v[200:203]\n"
"ds_write_b128 v7, v[120:123] offset:24576\n"
"v_mfma_f32_16x16x32_bf16 v[204:207], a[88:91], v[216:219], v[204:207]\n"
"buffer_load_dwordx4 v[84:87], v0, s[4:7], s97 offen offset:3072 sc1\n"
"v_mfma_f32_16x16x32_bf16 v[220:223], a[92:95], v[216:219], v[220:223]\n"
"s_waitcnt vmcnt(22)\n"
"v_add_f32_e32 v140, v140, v124\n"
"v_mfma_f32_16x16x32_bf16 v[156:159], a[96:99], v[24:27], v[156:159]\n"
"v_add_f32_e32 v141, v141, v125\n"
"v_add_f32_e32 v142, v142, v126\n"
"v_mfma_f32_16x16x32_bf16 v[164:167], a[100:103], v[24:27], v[164:167]\n"
"v_add_f32_e32 v143, v143, v127\n"
"v_add_f32_e32 v196, v196, v188\n"
"v_mfma_f32_16x16x32_bf16 v[176:179], a[104:107], v[24:27], v[176:179]\n"
"v_add_f32_e32 v197, v197, v189\n"
"v_add_f32_e32 v198, v198, v190\n"
"v_mfma_f32_16x16x32_bf16 v[200:203], a[108:111], v[24:27], v[200:203]\n"
"v_add_f32_e32 v199, v199, v191\n"
"v_mul_f32_e32 v140, v140, v253\n"
"v_mfma_f32_16x16x32_bf16 v[204:207], a[112:115], v[24:27], v[204:207]\n"
"buffer_load_dwordx4 v[124:127], v1, s[4:7], s97 offen sc1\n"
"v_mfma_f32_16x16x32_bf16 v[220:223], a[116:119], v[24:27], v[220:223]\n"
"buffer_load_dwordx4 a[132:135], v0, s[4:7], s98 offen offset:1024 sc1\n"
"v_mfma_f32_16x16x32_bf16 v[156:159], a[120:123], v[100:103], v[156:159]\n"
"v_mul_f32_e32 v141, v141, v253\n"
"v_mul_f32_e32 v142, v142, v253\n"
"v_mfma_f32_16x16x32_bf16 v[164:167], a[124:127], v[100:103], v[164:167]\n"
"v_mul_f32_e32 v143, v143, v253\n"
"v_mul_f32_e32 v196, v196, v253\n"
"v_mfma_f32_16x16x32_bf16 v[176:179], v[40:43], v[100:103], v[176:179]\n"
"v_mul_f32_e32 v197, v197, v253\n"
"v_mul_f32_e32 v198, v198, v253\n"
"v_mfma_f32_16x16x32_bf16 v[200:203], v[44:47], v[100:103], v[200:203]\n"
"v_mul_f32_e32 v199, v199, v253\n"
"v_cvt_pk_bf16_f32 v24, v140, v141\n"
"v_mfma_f32_16x16x32_bf16 v[204:207], v[80:83], v[100:103], v[204:207]\n"
"buffer_load_dwordx4 v[160:163], v1, s[4:7], s97 offen offset:1024 sc1\n"
"v_mfma_f32_16x16x32_bf16 v[220:223], v[68:71], v[100:103], v[220:223]\n"
"s_waitcnt vmcnt(22)\n"
"v_cvt_pk_bf16_f32 v25, v142, v143\n"
"v_mfma_f32_16x16x32_bf16 v[156:159], v[88:91], v[36:39], v[156:159]\n"
"v_cvt_pk_bf16_f32 v26, v196, v197\n"
"v_cvt_pk_bf16_f32 v27, v198, v199\n"
"v_mfma_f32_16x16x32_bf16 v[164:167], v[48:51], v[36:39], v[164:167]\n"
"ds_write_b128 v7, v[24:27] offset:57344\n"
"v_mfma_f32_16x16x32_bf16 v[176:179], v[92:95], v[36:39], v[176:179]\n"
"v_add_f32_e32 v144, v144, v56\n"
"v_add_f32_e32 v145, v145, v57\n"
"v_mfma_f32_16x16x32_bf16 v[200:203], v[96:99], v[36:39], v[200:203]\n"
"v_add_f32_e32 v146, v146, v58\n"
"v_add_f32_e32 v147, v147, v59\n"
"v_mfma_f32_16x16x32_bf16 v[204:207], v[168:171], v[36:39], v[204:207]\n"
"buffer_load_dwordx4 v[56:59], v1, s[4:7], s97 offen offset:2048 sc1\n"
"v_mfma_f32_16x16x32_bf16 v[220:223], v[104:107], v[36:39], v[220:223]\n"
"v_add_f32_e32 v184, v184, v212\n"
"v_add_f32_e32 v185, v185, v213\n"
"v_mfma_f32_16x16x32_bf16 v[156:159], v[136:139], v[52:55], v[156:159]\n"
"v_add_f32_e32 v186, v186, v214\n"
"v_add_f32_e32 v187, v187, v215\n"
"v_mfma_f32_16x16x32_bf16 v[164:167], v[180:183], v[52:55], v[164:167]\n"
"v_mul_f32_e32 v144, v144, v255\n"
"v_mul_f32_e32 v145, v145, v255\n"
"v_mfma_f32_16x16x32_bf16 v[176:179], v[28:31], v[52:55], v[176:179]\n"
"v_mul_f32_e32 v146, v146, v255\n"
"v_mul_f32_e32 v147, v147, v255\n"
"v_mfma_f32_16x16x32_bf16 v[200:203], v[32:35], v[52:55], v[200:203]\n"
"v_mul_f32_e32 v184, v184, v255\n"
"v_mul_f32_e32 v185, v185, v255\n"
"v_mfma_f32_16x16x32_bf16 v[204:207], v[60:63], v[52:55], v[204:207]\n"
"buffer_load_dwordx4 v[36:39], v1, s[4:7], s97 offen offset:3072 sc1\n"
"v_mfma_f32_16x16x32_bf16 v[220:223], v[64:67], v[52:55], v[220:223]\n"
"v_cmp_u_f32_e32 vcc, v156, v156\n"
"s_waitcnt vmcnt(21)\n"
"; @A G1 1.1\n"
"v_mfma_f32_16x16x32_bf16 v[52:55], a[0:3], v[116:119], 0\n"
"s_cbranch_vccnz .Lmk_p95s\n"
".Lmk_p95r:\n"
"v_mul_f32_e32 v186, v186, v255\n"
"v_mfma_f32_16x16x32_bf16 v[100:103], a[4:7], v[116:119], 0\n"
"buffer_load_dwordx4 a[136:139], v0, s[4:7], s98 offen offset:2048 sc1\n"
"v_mfma_f32_16x16x32_bf16 v[140:143], a[8:11], v[116:119], 0\n"
"v_mul_f32_e32 v187, v187, v255\n"
"v_cvt_pk_bf16_f32 v188, v144, v145\n"
"v_mfma_f32_16x16x32_bf16 v[192:195], a[12:15], v[116:119], 0\n"
"ds_write_b128 v3, v[156:159]\n"
"v_mfma_f32_16x16x32_bf16 v[156:159], a[16:19], v[116:119], 0\n"
"s_add_u32 s97, s55, 0x48000\n"
"v_cvt_pk_bf16_f32 v189, v146, v147\n"
"v_mfma_f32_16x16x32_bf16 v[144:147], a[20:23], v[116:119], 0\n"
"buffer_load_dwordx4 v[116:119], v0, s[4:7], s97 offen sc1\n"
"v_mfma_f32_16x16x32_bf16 v[52:55], a[24:27], v[128:131], v[52:55]\n"
"ds_write_b128 v3, v[164:167] offset:1024\n"
"v_mfma_f32_16x16x32_bf16 v[100:103], a[28:31], v[128:131], v[100:103]\n"
"v_cvt_pk_bf16_f32 v190, v184, v185\n"
"v_cvt_pk_bf16_f32 v191, v186, v187\n"
"v_mfma_f32_16x16x32_bf16 v[140:143], a[32:35], v[128:131], v[140:143]\n"
"ds_write_b128 v2, v[188:191] offset:49152\n"
"v_mfma_f32_16x16x32_bf16 v[192:195], a[36:39], v[128:131], v[192:195]\n"
"ds_write_b128 v3, v[176:179] offset:2048\n"
"v_mfma_f32_16x16x32_bf16 v[156:159], a[40:43], v[128:131], v[156:159]\n"
"buffer_load_dwordx4 v[164:167], v0, s[4:7], s97 offen offset:1024 sc1\n"
"v_mfma_f32_16x16x32_bf16 v[144:147], a[44:47], v[128:131], v[144:147]\n"
"s_waitcnt vmcnt(22)\n"
"v_mfma_f32_16x16x32_bf16 v[52:55], a[48:51], v[76:79], v[52:55]\n"
"ds_write_b128 v3, v[200:203] offset:3072\n"
"v_mfma_f32_16x16x32_bf16 v[100:103], a[52:55], v[76:79], v[100:103]\n"
"s_add_u32 s96, s67, 0\n"
"v_mfma_f32_16x16x32_bf16 v[140:143], a[56:59], v[76:79], v[140:143]\n"
"buffer_load_dwordx2 v[254:255], v12, s[4:7], s96 offen sc1\n"
"s_add_u32 s95, s64, 0x2000\n"
"v_mfma_f32_16x16x32_bf16 v[192:195], a[60:63], v[76:79], v[192:195]\n"
"ds_write_b128 v3, v[204:207] offset:4096\n"
"v_mfma_f32_16x16x32_bf16 v[156:159], a[64:67], v[76:79], v[156:159]\n"
"buffer_load_dwordx4 v[128:131], v0, s[4:7], s97 offen offset:2048 sc1\n"
"v_mfma_f32_16x16x32_bf16 v[144:147], a[68:71], v[76:79], v[144:147]\n"
"buffer_store_dwordx4 v[120:123], v7, s[4:7], s95 offen sc1\n"
"v_mfma_f32_16x16x32_bf16 v[52:55], a[72:75], v[132:135], v[52:55]\n"
"ds_write_b128 v3, v[220:223] offset:5120\n"
"v_mfma_f32_16x16x32_bf16 v[100:103], a[76:79], v[132:135], v[100:103]\n"
"v_mfma_f32_16x16x32_bf16 v[140:143], a[80:83], v[132:135], v[140:143]\n"
"v_mfma_f32_16x16x32_bf16 v[192:195], a[84:87], v[132:135], v[192:195]\n"
"buffer_load_dwordx4 a[140:143], v0, s[4:7], s98 offen offset:3072 sc1\n"
"s_add_u32 s95, s64, 0x6000\n"
"v_mfma_f32_16x16x32_bf16 v[156:159], a[88:91], v[132:135], v[156:159]\n"
"buffer_load_dwordx4 v[76:79], v0, s[4:7], s97 offen offset:3072 sc1\n"
"v_mfma_f32_16x16x32_bf16 v[144:147], a[92:95], v[132:135], v[144:147]\n"
"s_waitcnt vmcnt(25)\n"
"buffer_store_dwordx4 v[24:27], v7, s[4:7], s95 offen sc1\n"
"v_mfma_f32_16x16x32_bf16 v[52:55], a[96:99], v[148:151], v[52:55]\n"
"v_mfma_f32_16x16x32_bf16 v[100:103], a[100:103], v[148:151], v[100:103]\n"
"v_mfma_f32_16x16x32_bf16 v[140:143], a[104:107], v[148:151], v[140:143]\n"
"v_mfma_f32_16x16x32_bf16 v[192:195], a[108:111], v[148:151], v[192:195]\n"
"v_mfma_f32_16x16x32_bf16 v[156:159], a[112:115], v[148:151], v[156:159]\n"
"buffer_load_dwordx4 v[24:27], v1, s[4:7], s97 offen sc1\n"
"v_mfma_f32_16x16x32_bf16 v[144:147], a[116:119], v[148:151], v[144:147]\n"
"s_waitcnt lgkmcnt(0)\n"
"s_barrier\n"
"v_mfma_f32_16x16x32_bf16 v[52:55], a[120:123], v[172:175], v[52:55]\n"
"s_waitcnt vmcnt(19)\n"
"v_add_f32_e32 v250, v250, v248\n"
"v_mfma_f32_16x16x32_bf16 v[100:103], a[124:127], v[172:175], v[100:103]\n"
"v_add_f32_e32 v251, v251, v249\n"
"v_add_f32_e32 v246, v246, v244\n"
"v_mfma_f32_16x16x32_bf16 v[140:143], v[40:43], v[172:175], v[140:143]\n"
"v_add_f32_e32 v247, v247, v245\n"
"v_add_f32_e32 v250, v250, v246\n"
"v_mfma_f32_16x16x32_bf16 v[192:195], v[44:47], v[172:175], v[192:195]\n"
"v_add_f32_e32 v251, v251, v247\n"
"v_cmp_u_f32_e32 vcc, v250, v251\n"
"v_mfma_f32_16x16x32_bf16 v[156:159], v[80:83], v[172:175], v[156:159]\n"
"buffer_load_dwordx4 v[120:123], v1, s[4:7], s97 offen offset:1024 sc1\n"
"v_mfma_f32_16x16x32_bf16 v[144:147], v[68:71], v[172:175], v[144:147]\n"
"s_cbranch_vccnz .Lmk_m100s\n"
".Lmk_m100r:\n"
"v_mfma_f32_16x16x32_bf16 v[52:55], v[88:91], v[208:211], v[52:55]\n"
"v_permlane16_swap_b32_e32 v250, v251\n"
"v_add_f32_e32 v250, v250, v251\n"
"v_mfma_f32_16x16x32_bf16 v[100:103], v[48:51], v[208:211], v[100:103]\n"
"v_mov_b32_e32 v251, v250\n"
"v_mfma_f32_16x16x32_bf16 v[140:143], v[92:95], v[208:211], v[140:143]\n"
"s_nop 0\n"
"v_permlane32_swap_b32_e32 v250, v251\n"
"v_add_f32_e32 v250, v250, v251\n"
"v_mfma_f32_16x16x32_bf16 v[192:195], v[96:99], v[208:211], v[192:195]\n"
"v_mul_f32_e32 v250, 0x3a800000, v250\n"
"v_mov_b32_e32 v251, v250\n"
"v_mfma_f32_16x16x32_bf16 v[156:159], v[168:171], v[208:211], v[156:159]\n"
"buffer_load_dwordx4 v[132:135], v1, s[4:7], s97 offen offset:2048 sc1\n"
"v_mfma_f32_16x16x32_bf16 v[144:147], v[104:107], v[208:211], v[144:147]\n"
"buffer_load_dwordx4 a[144:147], v1, s[4:7], s98 offen sc1\n"
"v_mfma_f32_16x16x32_bf16 v[52:55], v[136:139], v[152:155], v[52:55]\n"
"v_permlane16_swap_b32_e32 v250, v251\n"
"v_cvt_pk_bf16_f32 v253, -v250, 0\n"
"v_mfma_f32_16x16x32_bf16 v[100:103], v[180:183], v[152:155], v[100:103]\n"
"v_fma_f32 v251, -v250, v250, v251\n"
"v_lshlrev_b32_e32 v253, 16, v253\n"
"v_mfma_f32_16x16x32_bf16 v[140:143], v[28:31], v[152:155], v[140:143]\n"
"ds_read_b128 v[148:151], v4\n"
"ds_read_b128 v[172:175], v9\n"
"v_mfma_f32_16x16x32_bf16 v[192:195], v[32:35], v[152:155], v[192:195]\n"
"ds_read_b128 v[176:179], v4 offset:1024\n"
"ds_read_b128 v[184:187], v9 offset:64\n"
"v_mfma_f32_16x16x32_bf16 v[156:159], v[60:63], v[152:155], v[156:159]\n"
"buffer_load_dwordx4 v[188:191], v1, s[4:7], s97 offen offset:3072 sc1\n"
"v_mfma_f32_16x16x32_bf16 v[144:147], v[64:67], v[152:155], v[144:147]\n"
"v_cmp_u_f32_e32 vcc, v52, v52\n"
"s_waitcnt vmcnt(21)\n"
"; @A G1 1.2\n"
"v_mfma_f32_16x16x32_bf16 v[152:155], a[0:3], v[72:75], 0\n"
"s_cbranch_vccnz .Lmk_p96s\n"
".Lmk_p96r:\n"
"ds_read_b128 v[196:199], v4 offset:2048\n"
"v_mfma_f32_16x16x32_bf16 v[200:203], a[4:7], v[72:75], 0\n"
"s_waitcnt vmcnt(10)\n"
"v_cmp_ne_u64_e32 vcc, v[254:255], v[18:19]\n"
"v_mfma_f32_16x16x32_bf16 v[204:207], a[8:11], v[72:75], 0\n"
"s_cbranch_vccnz .Lmk_f85s\n"
".Lmk_f85r:\n"
"ds_read_b128 v[208:211], v9 offset:128\n"
"v_mfma_f32_16x16x32_bf16 v[212:215], a[12:15], v[72:75], 0\n"
"ds_write_b128 v3, v[52:55] offset:6144\n"
"v_mfma_f32_16x16x32_bf16 v[52:55], a[16:19], v[72:75], 0\n"
"s_add_u32 s97, s52, 0x8000\n"
"ds_read_b128 v[216:219], v4 offset:3072\n"
"v_mfma_f32_16x16x32_bf16 v[220:223], a[20:23], v[72:75], 0\n"
"buffer_load_dwordx4 v[72:75], v0, s[4:7], s97 offen sc1\n"
"v_mfma_f32_16x16x32_bf16 v[152:155], a[24:27], v[112:115], v[152:155]\n"
"ds_write_b128 v3, v[100:103] offset:7168\n"
"v_mfma_f32_16x16x32_bf16 v[200:203], a[28:31], v[112:115], v[200:203]\n"
"s_waitcnt lgkmcnt(3)\n"
"v_fmac_f32_e32 v148, v253, v172\n"
"v_mfma_f32_16x16x32_bf16 v[204:207], a[32:35], v[112:115], v[204:207]\n"
"v_fmac_f32_e32 v149, v253, v173\n"
"v_fmac_f32_e32 v150, v253, v174\n"
"v_mfma_f32_16x16x32_bf16 v[212:215], a[36:39], v[112:115], v[212:215]\n"
"ds_write_b128 v3, v[140:143] offset:8192\n"
"v_mfma_f32_16x16x32_bf16 v[52:55], a[40:43], v[112:115], v[52:55]\n"
"buffer_load_dwordx4 v[100:103], v0, s[4:7], s97 offen offset:1024 sc1\n"
"v_mfma_f32_16x16x32_bf16 v[220:223], a[44:47], v[112:115], v[220:223]\n"
"v_fmac_f32_e32 v151, v253, v175\n"
"v_mfma_f32_16x16x32_bf16 v[152:155], a[48:51], v[108:111], v[152:155]\n"
"ds_write_b128 v3, v[192:195] offset:9216\n"
"v_mfma_f32_16x16x32_bf16 v[200:203], a[52:55], v[108:111], v[200:203]\n"
"buffer_load_dwordx4 a[148:151], v1, s[4:7], s98 offen offset:1024 sc1\n"
"v_mfma_f32_16x16x32_bf16 v[204:207], a[56:59], v[108:111], v[204:207]\n"
"v_fmac_f32_e32 v176, v253, v184\n"
"v_fmac_f32_e32 v177, v253, v185\n"
"v_mfma_f32_16x16x32_bf16 v[212:215], a[60:63], v[108:111], v[212:215]\n"
"ds_write_b128 v3, v[156:159] offset:10240\n"
"v_mfma_f32_16x16x32_bf16 v[52:55], a[64:67], v[108:111], v[52:55]\n"
"buffer_load_dwordx4 v[112:115], v0, s[4:7], s97 offen offset:2048 sc1\n"
"v_mfma_f32_16x16x32_bf16 v[220:223], a[68:71], v[108:111], v[220:223]\n"
"v_fmac_f32_e32 v178, v253, v186\n"
"v_fmac_f32_e32 v179, v253, v187\n"
"v_mfma_f32_16x16x32_bf16 v[152:155], a[72:75], v[84:87], v[152:155]\n"
"ds_write_b128 v3, v[144:147] offset:11264\n"
"v_mfma_f32_16x16x32_bf16 v[200:203], a[76:79], v[84:87], v[200:203]\n"
"v_fmac_f32_e32 v196, v253, v208\n"
"v_fmac_f32_e32 v197, v253, v209\n"
"v_mfma_f32_16x16x32_bf16 v[204:207], a[80:83], v[84:87], v[204:207]\n"
"v_fmac_f32_e32 v198, v253, v210\n"
"v_fmac_f32_e32 v199, v253, v211\n"
"v_mfma_f32_16x16x32_bf16 v[212:215], a[84:87], v[84:87], v[212:215]\n"
"ds_read_b128 v[108:111], v9 offset:192\n"
"ds_read_b128 v[140:143], v4 offset:4096\n"
"v_mfma_f32_16x16x32_bf16 v[52:55], a[88:91], v[84:87], v[52:55]\n"
"buffer_load_dwordx4 v[144:147], v0, s[4:7], s97 offen offset:3072 sc1\n"
"v_mfma_f32_16x16x32_bf16 v[220:223], a[92:95], v[84:87], v[220:223]\n"
"ds_read_b128 v[84:87], v9 offset:256\n"
"v_mfma_f32_16x16x32_bf16 v[152:155], a[96:99], v[124:127], v[152:155]\n"
"ds_read_b128 v[156:159], v4 offset:5120\n"
"ds_read_b128 v[172:175], v9 offset:320\n"
"v_mfma_f32_16x16x32_bf16 v[200:203], a[100:103], v[124:127], v[200:203]\n"
"v_max_f32_e32 v251, 0, v251\n"
"v_cvt_pk_bf16_f32 v251, v251, 0\n"
"v_mfma_f32_16x16x32_bf16 v[204:207], a[104:107], v[124:127], v[204:207]\n"
"v_lshlrev_b32_e32 v251, 16, v251\n"
"v_add_f32_e32 v251, 0x3727c5ac, v251\n"
"v_mfma_f32_16x16x32_bf16 v[212:215], a[108:111], v[124:127], v[212:215]\n"
"v_cvt_pk_bf16_f32 v251, v251, 0\n"
"v_lshlrev_b32_e32 v251, 16, v251\n"
"v_mfma_f32_16x16x32_bf16 v[52:55], a[112:115], v[124:127], v[52:55]\n"
"buffer_load_dwordx4 v[184:187], v1, s[4:7], s97 offen sc1\n"
"v_mfma_f32_16x16x32_bf16 v[220:223], a[116:119], v[124:127], v[220:223]\n"
"s_waitcnt lgkmcnt(0)\n"
"v_fmac_f32_e32 v216, v253, v108\n"
"v_mfma_f32_16x16x32_bf16 v[152:155], a[120:123], v[160:163], v[152:155]\n"
"v_fmac_f32_e32 v217, v253, v109\n"
"v_fmac_f32_e32 v218, v253, v110\n"
"v_mfma_f32_16x16x32_bf16 v[200:203], a[124:127], v[160:163], v[200:203]\n"
"v_fmac_f32_e32 v219, v253, v111\n"
"v_fmac_f32_e32 v140, v253, v84\n"
"v_mfma_f32_16x16x32_bf16 v[204:207], v[40:43], v[160:163], v[204:207]\n"
"v_fmac_f32_e32 v141, v253, v85\n"
"v_fmac_f32_e32 v142, v253, v86\n"
"v_mfma_f32_16x16x32_bf16 v[212:215], v[44:47], v[160:163], v[212:215]\n"
"buffer_load_dwordx4 a[152:155], v1, s[4:7], s98 offen offset:2048 sc1\n"
"v_mfma_f32_16x16x32_bf16 v[52:55], v[80:83], v[160:163], v[52:55]\n"
"buffer_load_dwordx4 v[108:111], v1, s[4:7], s97 offen offset:1024 sc1\n"
"v_mfma_f32_16x16x32_bf16 v[220:223], v[68:71], v[160:163], v[220:223]\n"
"v_fmac_f32_e32 v143, v253, v87\n"
"v_mfma_f32_16x16x32_bf16 v[152:155], v[88:91], v[56:59], v[152:155]\n"
"v_fmac_f32_e32 v156, v253, v172\n"
"v_fmac_f32_e32 v157, v253, v173\n"
"v_mfma_f32_16x16x32_bf16 v[200:203], v[48:51], v[56:59], v[200:203]\n"
"v_fmac_f32_e32 v158, v253, v174\n"
"v_fmac_f32_e32 v159, v253, v175\n"
"v_mfma_f32_16x16x32_bf16 v[204:207], v[92:95], v[56:59], v[204:207]\n"
"v_sqrt_f32_e32 v251, v251\n"
"v_mfma_f32_16x16x32_bf16 v[212:215], v[96:99], v[56:59], v[212:215]\n"
"v_cvt_pk_bf16_f32 v251, v251, 0\n"
"v_lshlrev_b32_e32 v251, 16, v251\n"
"v_mfma_f32_16x16x32_bf16 v[52:55], v[168:171], v[56:59], v[52:55]\n"
"v_max_u32_e32 v255, v116, v117\n"
"v_mfma_f32_16x16x32_bf16 v[220:223], v[104:107], v[56:59], v[220:223]\n"
"v_max3_u32 v255, v255, v118, v119\n"
"v_max3_u32 v255, v255, v164, v165\n"
"v_mfma_f32_16x16x32_bf16 v[152:155], v[136:139], v[36:39], v[152:155]\n"
"v_max3_u32 v255, v255, v166, v167\n"
"s_waitcnt vmcnt(14)\n"
"v_mfma_f32_16x16x32_bf16 v[200:203], v[180:183], v[36:39], v[200:203]\n"
"v_max3_u32 v255, v255, v128, v129\n"
"v_max3_u32 v255, v255, v130, v131\n"
"v_mfma_f32_16x16x32_bf16 v[204:207], v[28:31], v[36:39], v[204:207]\n"
"v_max3_u32 v255, v255, v76, v77\n"
"v_max3_u32 v255, v255, v78, v79\n"
"v_mfma_f32_16x16x32_bf16 v[212:215], v[32:35], v[36:39], v[212:215]\n"
"v_cmp_eq_u32_e32 vcc, -1, v255\n"
"v_mfma_f32_16x16x32_bf16 v[52:55], v[60:63], v[36:39], v[52:55]\n"
"s_cbranch_vccnz .Lmk_q98s\n"
".Lmk_q98r:\n"
"v_mfma_f32_16x16x32_bf16 v[220:223], v[64:67], v[36:39], v[220:223]\n"
"v_cmp_u_f32_e32 vcc, v152, v152\n"
"; @A G1 1.3\n"
"v_mfma_f32_16x16x32_bf16 v[148:151], a[0:3], v[116:119], v[148:151]\n"
"s_cbranch_vccnz .Lmk_p97s\n"
".Lmk_p97r:\n"
"v_mfma_f32_16x16x32_bf16 v[176:179], a[4:7], v[116:119], v[176:179]\n"
"buffer_load_dwordx4 v[36:39], v1, s[4:7], s97 offen offset:2048 sc1\n"
"v_mfma_f32_16x16x32_bf16 v[196:199], a[8:11], v[116:119], v[196:199]\n"
"buffer_load_dwordx4 v[56:59], v1, s[4:7], s97 offen offset:3072 sc1\n"
"v_mfma_f32_16x16x32_bf16 v[216:219], a[12:15], v[116:119], v[216:219]\n"
"ds_write_b128 v3, v[152:155] offset:12288\n"
"v_mfma_f32_16x16x32_bf16 v[140:143], a[16:19], v[116:119], v[140:143]\n"
"s_add_u32 s97, s53, 0x8000\n"
"v_mfma_f32_16x16x32_bf16 v[156:159], a[20:23], v[116:119], v[156:159]\n"
"buffer_load_dwordx4 v[84:87], v0, s[4:7], s97 offen sc1\n"
"v_mfma_f32_16x16x32_bf16 v[148:151], a[24:27], v[164:167], v[148:151]\n"
"ds_write_b128 v3, v[200:203] offset:13312\n"
"v_mfma_f32_16x16x32_bf16 v[176:179], a[28:31], v[164:167], v[176:179]\n"
"buffer_load_dwordx4 a[156:159], v1, s[4:7], s98 offen offset:3072 sc1\n"
"v_mfma_f32_16x16x32_bf16 v[196:199], a[32:35], v[164:167], v[196:199]\n"
"v_rcp_f32_e32 v255, v251\n"
"v_mfma_f32_16x16x32_bf16 v[216:219], a[36:39], v[164:167], v[216:219]\n"
"ds_write_b128 v3, v[204:207] offset:14336\n"
"v_mfma_f32_16x16x32_bf16 v[140:143], a[40:43], v[164:167], v[140:143]\n"
"buffer_load_dwordx4 v[116:119], v0, s[4:7], s97 offen offset:1024 sc1\n"
"v_mfma_f32_16x16x32_bf16 v[156:159], a[44:47], v[164:167], v[156:159]\n"
"v_mul_f32_e32 v254, 0x3e8293ee, v255\n"
"v_mfma_f32_16x16x32_bf16 v[148:151], a[48:51], v[128:131], v[148:151]\n"
"ds_write_b128 v3, v[212:215] offset:15360\n"
"v_mfma_f32_16x16x32_bf16 v[176:179], a[52:55], v[128:131], v[176:179]\n"
"v_mfma_f32_16x16x32_bf16 v[196:199], a[56:59], v[128:131], v[196:199]\n"
"v_mfma_f32_16x16x32_bf16 v[216:219], a[60:63], v[128:131], v[216:219]\n"
"ds_write_b128 v3, v[52:55] offset:16384\n"
"v_mfma_f32_16x16x32_bf16 v[140:143], a[64:67], v[128:131], v[140:143]\n"
"s_waitcnt vmcnt(16)\n"
"v_max_u32_e32 v253, v24, v25\n"
"v_mfma_f32_16x16x32_bf16 v[156:159], a[68:71], v[128:131], v[156:159]\n"
"v_max3_u32 v253, v253, v26, v27\n"
"v_max3_u32 v253, v253, v120, v121\n"
"v_mfma_f32_16x16x32_bf16 v[148:151], a[72:75], v[76:79], v[148:151]\n"
"v_max3_u32 v253, v253, v122, v123\n"
"s_waitcnt vmcnt(13)\n"
"v_mfma_f32_16x16x32_bf16 v[176:179], a[76:79], v[76:79], v[176:179]\n"
"v_max3_u32 v253, v253, v132, v133\n"
"v_max3_u32 v253, v253, v134, v135\n"
"v_mfma_f32_16x16x32_bf16 v[196:199], a[80:83], v[76:79], v[196:199]\n"
"v_max3_u32 v253, v253, v188, v189\n"
"v_max3_u32 v253, v253, v190, v191\n"
"v_mfma_f32_16x16x32_bf16 v[216:219], a[84:87], v[76:79], v[216:219]\n"
"v_cmp_eq_u32_e32 vcc, -1, v253\n"
"v_mfma_f32_16x16x32_bf16 v[140:143], a[88:91], v[76:79], v[140:143]\n"
"s_cbranch_vccnz .Lmk_q99s\n"
".Lmk_q99r:\n"
"v_mfma_f32_16x16x32_bf16 v[156:159], a[92:95], v[76:79], v[156:159]\n"
"v_mfma_f32_16x16x32_bf16 v[148:151], a[96:99], v[24:27], v[148:151]\n"
"buffer_load_dwordx4 v[52:55], v0, s[4:7], s97 offen offset:2048 sc1\n"
"v_mfma_f32_16x16x32_bf16 v[176:179], a[100:103], v[24:27], v[176:179]\n"
"buffer_load_dwordx4 v[76:79], v0, s[4:7], s97 offen offset:3072 sc1\n"
"v_mfma_f32_16x16x32_bf16 v[196:199], a[104:107], v[24:27], v[196:199]\n"
"ds_write_b128 v3, v[220:223] offset:17408\n"
"v_mfma_f32_16x16x32_bf16 v[216:219], a[108:111], v[24:27], v[216:219]\n"
"s_add_u32 s98, s70, 0x2000\n"
"v_mfma_f32_16x16x32_bf16 v[140:143], a[112:115], v[24:27], v[140:143]\n"
"buffer_load_dwordx4 v[124:127], v1, s[4:7], s97 offen sc1\n"
"v_mfma_f32_16x16x32_bf16 v[156:159], a[116:119], v[24:27], v[156:159]\n"
"buffer_load_dwordx4 a[160:163], v0, s[4:7], s98 offen sc1\n"
"v_mfma_f32_16x16x32_bf16 v[148:151], a[120:123], v[120:123], v[148:151]\n"
"s_waitcnt lgkmcnt(0)\n"
"s_barrier\n"
"v_mfma_f32_16x16x32_bf16 v[176:179], a[124:127], v[120:123], v[176:179]\n"
"ds_read_b128 v[24:27], v5 offset:2048\n"
"ds_read_b128 v[128:131], v6 offset:2048\n"
"v_mfma_f32_16x16x32_bf16 v[196:199], v[40:43], v[120:123], v[196:199]\n"
"ds_read_b128 v[152:155], v5 offset:3072\n"
"ds_read_b128 v[160:163], v6 offset:3072\n"
"v_mfma_f32_16x16x32_bf16 v[216:219], v[44:47], v[120:123], v[216:219]\n"
"v_mfma_f32_16x16x32_bf16 v[140:143], v[80:83], v[120:123], v[140:143]\n"
"buffer_load_dwordx4 v[164:167], v1, s[4:7], s97 offen offset:1024 sc1\n"
"v_mfma_f32_16x16x32_bf16 v[156:159], v[68:71], v[120:123], v[156:159]\n"
"ds_read_b128 v[120:123], v5 offset:4096\n"
"v_mfma_f32_16x16x32_bf16 v[148:151], v[88:91], v[132:135], v[148:151]\n"
"ds_read_b128 v[172:175], v6 offset:4096\n"
"ds_read_b128 v[192:195], v5 offset:5120\n"
"v_mfma_f32_16x16x32_bf16 v[176:179], v[48:51], v[132:135], v[176:179]\n"
"ds_read_b128 v[200:203], v6 offset:5120\n"
"s_waitcnt lgkmcnt(4)\n"
"v_mfma_f32_16x16x32_bf16 v[196:199], v[92:95], v[132:135], v[196:199]\n"
"v_add_f32_e32 v24, v24, v128\n"
"v_add_f32_e32 v25, v25, v129\n"
"v_mfma_f32_16x16x32_bf16 v[216:219], v[96:99], v[132:135], v[216:219]\n"
"v_add_f32_e32 v26, v26, v130\n"
"v_add_f32_e32 v27, v27, v131\n"
"v_mfma_f32_16x16x32_bf16 v[140:143], v[168:171], v[132:135], v[140:143]\n"
"buffer_load_dwordx4 v[128:131], v1, s[4:7], s97 offen offset:2048 sc1\n"
"v_mfma_f32_16x16x32_bf16 v[156:159], v[104:107], v[132:135], v[156:159]\n"
"ds_read_b128 v[132:135], v5\n"
"ds_read_b128 v[204:207], v6\n"
"v_mfma_f32_16x16x32_bf16 v[148:151], v[136:139], v[188:191], v[148:151]\n"
"ds_read_b128 v[208:211], v5 offset:1024\n"
"ds_read_b128 v[212:215], v6 offset:1024\n"
"v_mfma_f32_16x16x32_bf16 v[176:179], v[180:183], v[188:191], v[176:179]\n"
"v_add_f32_e32 v152, v152, v160\n"
"v_add_f32_e32 v153, v153, v161\n"
"v_mfma_f32_16x16x32_bf16 v[196:199], v[28:31], v[188:191], v[196:199]\n"
"v_add_f32_e32 v154, v154, v162\n"
"v_add_f32_e32 v155, v155, v163\n"
"v_mfma_f32_16x16x32_bf16 v[216:219], v[32:35], v[188:191], v[216:219]\n"
"s_waitcnt lgkmcnt(4)\n"
"v_add_f32_e32 v120, v120, v172\n"
"v_mfma_f32_16x16x32_bf16 v[140:143], v[60:63], v[188:191], v[140:143]\n"
"buffer_load_dwordx4 v[160:163], v1, s[4:7], s97 offen offset:3072 sc1\n"
"v_mfma_f32_16x16x32_bf16 v[156:159], v[64:67], v[188:191], v[156:159]\n"
"v_add_f32_e32 v121, v121, v173\n"
"v_add_f32_e32 v122, v122, v174\n"
"v_add_f32_e32 v123, v123, v175\n"
"v_add_f32_e32 v192, v192, v200\n"
"v_add_f32_e32 v193, v193, v201\n"
"v_add_f32_e32 v194, v194, v202\n"
"v_add_f32_e32 v195, v195, v203\n"
"s_waitcnt lgkmcnt(0)\n"
"v_add_f32_e32 v132, v132, v204\n"
"v_add_f32_e32 v133, v133, v205\n"
"v_add_f32_e32 v134, v134, v206\n"
"v_add_f32_e32 v135, v135, v207\n"
"v_add_f32_e32 v208, v208, v212\n"
"v_add_f32_e32 v209, v209, v213\n"
"v_add_f32_e32 v210, v210, v214\n"
"v_add_f32_e32 v211, v211, v215\n"
".Lmk_a2:\n"
"s_waitcnt vmcnt(18)\n"
"; @A G1 2.0\n"
"v_mfma_f32_16x16x32_bf16 v[172:175], a[0:3], v[72:75], 0\n"
"s_add_u32 s97, s62, 0x800\n"
"v_mfma_f32_16x16x32_bf16 v[188:191], a[4:7], v[72:75], 0\n"
"buffer_load_dwordx2 v[252:253], v10, s[4:7], s97 offen sc1\n"
"v_mfma_f32_16x16x32_bf16 v[200:203], a[8:11], v[72:75], 0\n"
"buffer_load_dwordx2 v[250:251], v10, s[4:7], s97 offen offset:128 sc1\n"
"v_mfma_f32_16x16x32_bf16 v[204:207], a[12:15], v[72:75], 0\n"
"buffer_load_dwordx2 v[248:249], v10, s[4:7], s97 offen offset:256 sc1\n"
"v_mfma_f32_16x16x32_bf16 v[212:215], a[16:19], v[72:75], 0\n"
"s_add_u32 s96, s54, 0x8000\n"
"v_add_f32_e32 v196, v196, v24\n"
"v_mfma_f32_16x16x32_bf16 v[220:223], a[20:23], v[72:75], 0\n"
"buffer_load_dwordx4 v[72:75], v0, s[4:7], s96 offen sc1\n"
"v_mfma_f32_16x16x32_bf16 v[172:175], a[24:27], v[100:103], v[172:175]\n"
"buffer_load_dwordx2 v[246:247], v10, s[4:7], s97 offen offset:384 sc1\n"
"v_mfma_f32_16x16x32_bf16 v[188:191], a[28:31], v[100:103], v[188:191]\n"
"v_add_f32_e32 v197, v197, v25\n"
"v_add_f32_e32 v198, v198, v26\n"
"v_mfma_f32_16x16x32_bf16 v[200:203], a[32:35], v[100:103], v[200:203]\n"
"buffer_load_dwordx4 a[164:167], v0, s[4:7], s98 offen offset:1024 sc1\n"
"v_mfma_f32_16x16x32_bf16 v[204:207], a[36:39], v[100:103], v[204:207]\n"
"v_add_f32_e32 v199, v199, v27\n"
"v_add_f32_e32 v216, v216, v152\n"
"v_mfma_f32_16x16x32_bf16 v[212:215], a[40:43], v[100:103], v[212:215]\n"
"buffer_load_dwordx4 v[24:27], v0, s[4:7], s96 offen offset:1024 sc1\n"
"v_mfma_f32_16x16x32_bf16 v[220:223], a[44:47], v[100:103], v[220:223]\n"
"s_waitcnt vmcnt(22)\n"
"v_add_f32_e32 v217, v217, v153\n"
"v_mfma_f32_16x16x32_bf16 v[172:175], a[48:51], v[112:115], v[172:175]\n"
"v_add_f32_e32 v218, v218, v154\n"
"v_add_f32_e32 v219, v219, v155\n"
"v_mfma_f32_16x16x32_bf16 v[188:191], a[52:55], v[112:115], v[188:191]\n"
"v_mul_f32_e32 v196, v196, v255\n"
"v_mul_f32_e32 v197, v197, v255\n"
"v_mfma_f32_16x16x32_bf16 v[200:203], a[56:59], v[112:115], v[200:203]\n"
"v_mul_f32_e32 v198, v198, v255\n"
"v_mul_f32_e32 v199, v199, v255\n"
"v_mfma_f32_16x16x32_bf16 v[204:207], a[60:63], v[112:115], v[204:207]\n"
"v_mul_f32_e32 v216, v216, v255\n"
"v_mul_f32_e32 v217, v217, v255\n"
"v_mfma_f32_16x16x32_bf16 v[212:215], a[64:67], v[112:115], v[212:215]\n"
"buffer_load_dwordx4 v[100:103], v0, s[4:7], s96 offen offset:2048 sc1\n"
"v_mfma_f32_16x16x32_bf16 v[220:223], a[68:71], v[112:115], v[220:223]\n"
"v_mul_f32_e32 v218, v218, v255\n"
"v_mul_f32_e32 v219, v219, v255\n"
"v_mfma_f32_16x16x32_bf16 v[172:175], a[72:75], v[144:147], v[172:175]\n"
"v_cvt_pk_bf16_f32 v112, v196, v197\n"
"v_cvt_pk_bf16_f32 v113, v198, v199\n"
"v_mfma_f32_16x16x32_bf16 v[188:191], a[76:79], v[144:147], v[188:191]\n"
"v_cvt_pk_bf16_f32 v114, v216, v217\n"
"v_cvt_pk_bf16_f32 v115, v218, v219\n"
"v_mfma_f32_16x16x32_bf16 v[200:203], a[80:83], v[144:147], v[200:203]\n"
"ds_write_b128 v8, v[112:115] offset:25600\n"
"v_mfma_f32_16x16x32_bf16 v[204:207], a[84:87], v[144:147], v[204:207]\n"
"s_add_u32 s97, s64, 0x2000\n"
"v_mfma_f32_16x16x32_bf16 v[212:215], a[88:91], v[144:147], v[212:215]\n"
"buffer_load_dwordx4 v[152:155], v0, s[4:7], s96 offen offset:3072 sc1\n"
"v_mfma_f32_16x16x32_bf16 v[220:223], a[92:95], v[144:147], v[220:223]\n"
"s_waitcnt vmcnt(21)\n"
"v_mfma_f32_16x16x32_bf16 v[172:175], a[96:99], v[184:187], v[172:175]\n"
"buffer_store_dwordx4 v[112:115], v8, s[4:7], s97 offen offset:1024 sc1\n"
"v_mfma_f32_16x16x32_bf16 v[188:191], a[100:103], v[184:187], v[188:191]\n"
"v_add_f32_e32 v140, v140, v120\n"
"v_add_f32_e32 v141, v141, v121\n"
"v_mfma_f32_16x16x32_bf16 v[200:203], a[104:107], v[184:187], v[200:203]\n"
"v_add_f32_e32 v142, v142, v122\n"
"v_add_f32_e32 v143, v143, v123\n"
"v_mfma_f32_16x16x32_bf16 v[204:207], a[108:111], v[184:187], v[204:207]\n"
"v_add_f32_e32 v156, v156, v192\n"
"v_add_f32_e32 v157, v157, v193\n"
"v_mfma_f32_16x16x32_bf16 v[212:215], a[112:115], v[184:187], v[212:215]\n"
"buffer_load_dwordx4 v[112:115], v1, s[4:7], s96 offen sc1\n"
"v_mfma_f32_16x16x32_bf16 v[220:223], a[116:119], v[184:187], v[220:223]\n"
"buffer_load_dwordx4 a[168:171], v0, s[4:7], s98 offen offset:2048 sc1\n"
"v_mfma_f32_16x16x32_bf16 v[172:175], a[120:123], v[108:111], v[172:175]\n"
"v_add_f32_e32 v158, v158, v194\n"
"v_add_f32_e32 v159, v159, v195\n"
"v_mfma_f32_16x16x32_bf16 v[188:191], a[124:127], v[108:111], v[188:191]\n"
"v_mul_f32_e32 v140, v140, v255\n"
"v_mul_f32_e32 v141, v141, v255\n"
"v_mfma_f32_16x16x32_bf16 v[200:203], v[40:43], v[108:111], v[200:203]\n"
"v_mul_f32_e32 v142, v142, v255\n"
"v_mul_f32_e32 v143, v143, v255\n"
"v_mfma_f32_16x16x32_bf16 v[204:207], v[44:47], v[108:111], v[204:207]\n"
"v_mul_f32_e32 v156, v156, v255\n"
"v_mul_f32_e32 v157, v157, v255\n"
"v_mfma_f32_16x16x32_bf16 v[212:215], v[80:83], v[108:111], v[212:215]\n"
"buffer_load_dwordx4 v[120:123], v1, s[4:7], s96 offen offset:1024 sc1\n"
"v_mfma_f32_16x16x32_bf16 v[220:223], v[68:71], v[108:111], v[220:223]\n"
"s_waitcnt vmcnt(23)\n"
"v_mul_f32_e32 v158, v158, v255\n"
"v_mfma_f32_16x16x32_bf16 v[172:175], v[88:91], v[36:39], v[172:175]\n"
"v_mul_f32_e32 v159, v159, v255\n"
"v_cvt_pk_bf16_f32 v108, v140, v141\n"
"v_mfma_f32_16x16x32_bf16 v[188:191], v[48:51], v[36:39], v[188:191]\n"
"v_cvt_pk_bf16_f32 v109, v142, v143\n"
"v_cvt_pk_bf16_f32 v110, v156, v157\n"
"v_mfma_f32_16x16x32_bf16 v[200:203], v[92:95], v[36:39], v[200:203]\n"
"v_cvt_pk_bf16_f32 v111, v158, v159\n"
"v_mfma_f32_16x16x32_bf16 v[204:207], v[96:99], v[36:39], v[204:207]\n"
"ds_write_b128 v7, v[108:111] offset:58368\n"
"v_mfma_f32_16x16x32_bf16 v[212:215], v[168:171], v[36:39], v[212:215]\n"
"buffer_load_dwordx4 v[140:143], v1, s[4:7], s96 offen offset:2048 sc1\n"
"v_mfma_f32_16x16x32_bf16 v[220:223], v[104:107], v[36:39], v[220:223]\n"
"s_add_u32 s97, s64, 0x6000\n"
"v_mfma_f32_16x16x32_bf16 v[172:175], v[136:139], v[56:59], v[172:175]\n"
"buffer_store_dwordx4 v[108:111], v7, s[4:7], s97 offen offset:1024 sc1\n"
"v_mfma_f32_16x16x32_bf16 v[188:191], v[180:183], v[56:59], v[188:191]\n"
"v_add_f32_e32 v148, v148, v132\n"
"v_add_f32_e32 v149, v149, v133\n"
"v_mfma_f32_16x16x32_bf16 v[200:203], v[28:31], v[56:59], v[200:203]\n"
"v_add_f32_e32 v150, v150, v134\n"
"v_add_f32_e32 v151, v151, v135\n"
"v_mfma_f32_16x16x32_bf16 v[204:207], v[32:35], v[56:59], v[204:207]\n"
"v_add_f32_e32 v176, v176, v208\n"
"v_add_f32_e32 v177, v177, v209\n"
"v_mfma_f32_16x16x32_bf16 v[212:215], v[60:63], v[56:59], v[212:215]\n"
"buffer_load_dwordx4 v[36:39], v1, s[4:7], s96 offen offset:3072 sc1\n"
"v_mfma_f32_16x16x32_bf16 v[220:223], v[64:67], v[56:59], v[220:223]\n"
"s_waitcnt vmcnt(23)\n"
"v_add_f32_e32 v178, v178, v210\n"
"; @A G1 2.1\n"
"v_mfma_f32_16x16x32_bf16 v[56:59], a[0:3], v[84:87], 0\n"
"v_add_f32_e32 v179, v179, v211\n"
"v_mul_f32_e32 v148, v148, v254\n"
"v_mfma_f32_16x16x32_bf16 v[108:111], a[4:7], v[84:87], 0\n"
"buffer_load_dwordx4 a[172:175], v0, s[4:7], s98 offen offset:3072 sc1\n"
"v_mfma_f32_16x16x32_bf16 v[132:135], a[8:11], v[84:87], 0\n"
"v_mul_f32_e32 v149, v149, v254\n"
"v_mul_f32_e32 v150, v150, v254\n"
"v_mfma_f32_16x16x32_bf16 v[144:147], a[12:15], v[84:87], 0\n"
"ds_write_b128 v3, v[172:175]\n"
"v_mfma_f32_16x16x32_bf16 v[156:159], a[16:19], v[84:87], 0\n"
"s_add_u32 s97, s55, 0x8000\n"
"v_mul_f32_e32 v151, v151, v254\n"
"v_mfma_f32_16x16x32_bf16 v[172:175], a[20:23], v[84:87], 0\n"
"buffer_load_dwordx4 v[84:87], v0, s[4:7], s97 offen sc1\n"
"v_mfma_f32_16x16x32_bf16 v[56:59], a[24:27], v[116:119], v[56:59]\n"
"ds_write_b128 v3, v[188:191] offset:1024\n"
"v_mfma_f32_16x16x32_bf16 v[108:111], a[28:31], v[116:119], v[108:111]\n"
"v_mul_f32_e32 v176, v176, v254\n"
"v_mul_f32_e32 v177, v177, v254\n"
"v_mfma_f32_16x16x32_bf16 v[132:135], a[32:35], v[116:119], v[132:135]\n"
"v_mul_f32_e32 v178, v178, v254\n"
"v_mul_f32_e32 v179, v179, v254\n"
"v_mfma_f32_16x16x32_bf16 v[144:147], a[36:39], v[116:119], v[144:147]\n"
"ds_write_b128 v3, v[200:203] offset:2048\n"
"v_mfma_f32_16x16x32_bf16 v[156:159], a[40:43], v[116:119], v[156:159]\n"
"buffer_load_dwordx4 v[184:187], v0, s[4:7], s97 offen offset:1024 sc1\n"
"v_mfma_f32_16x16x32_bf16 v[172:175], a[44:47], v[116:119], v[172:175]\n"
"s_waitcnt vmcnt(24)\n"
"v_cvt_pk_bf16_f32 v116, v148, v149\n"
"v_mfma_f32_16x16x32_bf16 v[56:59], a[48:51], v[52:55], v[56:59]\n"
"ds_write_b128 v3, v[204:207] offset:3072\n"
"v_mfma_f32_16x16x32_bf16 v[108:111], a[52:55], v[52:55], v[108:111]\n"
"v_cvt_pk_bf16_f32 v117, v150, v151\n"
"v_cvt_pk_bf16_f32 v118, v176, v177\n"
"v_mfma_f32_16x16x32_bf16 v[132:135], a[56:59], v[52:55], v[132:135]\n"
"v_cvt_pk_bf16_f32 v119, v178, v179\n"
"v_mfma_f32_16x16x32_bf16 v[144:147], a[60:63], v[52:55], v[144:147]\n"
"ds_write_b128 v3, v[212:215] offset:4096\n"
"v_mfma_f32_16x16x32_bf16 v[156:159], a[64:67], v[52:55], v[156:159]\n"
"buffer_load_dwordx4 v[148:151], v0, s[4:7], s97 offen offset:2048 sc1\n"
"v_mfma_f32_16x16x32_bf16 v[172:175], a[68:71], v[52:55], v[172:175]\n"
"ds_write_b128 v2, v[116:119] offset:50176\n"
"v_mfma_f32_16x16x32_bf16 v[56:59], a[72:75], v[76:79], v[56:59]\n"
"ds_write_b128 v3, v[220:223] offset:5120\n"
"v_mfma_f32_16x16x32_bf16 v[108:111], a[76:79], v[76:79], v[108:111]\n"
"v_mfma_f32_16x16x32_bf16 v[132:135], a[80:83], v[76:79], v[132:135]\n"
"v_mfma_f32_16x16x32_bf16 v[144:147], a[84:87], v[76:79], v[144:147]\n"
"buffer_load_dwordx4 a[176:179], v1, s[4:7], s98 offen sc1\n"
"v_mfma_f32_16x16x32_bf16 v[156:159], a[88:91], v[76:79], v[156:159]\n"
"buffer_load_dwordx4 v[52:55], v0, s[4:7], s97 offen offset:3072 sc1\n"
"v_mfma_f32_16x16x32_bf16 v[172:175], a[92:95], v[76:79], v[172:175]\n"
"s_waitcnt vmcnt(24)\n"
"v_mfma_f32_16x16x32_bf16 v[56:59], a[96:99], v[124:127], v[56:59]\n"
"v_mfma_f32_16x16x32_bf16 v[108:111], a[100:103], v[124:127], v[108:111]\n"
"v_mfma_f32_16x16x32_bf16 v[132:135], a[104:107], v[124:127], v[132:135]\n"
"v_mfma_f32_16x16x32_bf16 v[144:147], a[108:111], v[124:127], v[144:147]\n"
"v_mfma_f32_16x16x32_bf16 v[156:159], a[112:115], v[124:127], v[156:159]\n"
"buffer_load_dwordx4 v[76:79], v1, s[4:7], s97 offen sc1\n"
"v_mfma_f32_16x16x32_bf16 v[172:175], a[116:119], v[124:127], v[172:175]\n"
"s_waitcnt lgkmcnt(0)\n"
"s_barrier\n"
"v_mfma_f32_16x16x32_bf16 v[56:59], a[120:123], v[164:167], v[56:59]\n"
"s_waitcnt vmcnt(18)\n"
"v_add_f32_e32 v252, v252, v250\n"
"v_mfma_f32_16x16x32_bf16 v[108:111], a[124:127], v[164:167], v[108:111]\n"
"v_add_f32_e32 v253, v253, v251\n"
"v_add_f32_e32 v248, v248, v246\n"
"v_mfma_f32_16x16x32_bf16 v[132:135], v[40:43], v[164:167], v[132:135]\n"
"v_add_f32_e32 v249, v249, v247\n"
"v_add_f32_e32 v252, v252, v248\n"
"v_mfma_f32_16x16x32_bf16 v[144:147], v[44:47], v[164:167], v[144:147]\n"
"v_add_f32_e32 v253, v253, v249\n"
"v_mfma_f32_16x16x32_bf16 v[156:159], v[80:83], v[164:167], v[156:159]\n"
"buffer_load_dwordx4 v[116:119], v1, s[4:7], s97 offen offset:1024 sc1\n"
"v_mfma_f32_16x16x32_bf16 v[172:175], v[68:71], v[164:167], v[172:175]\n"
"v_permlane16_swap_b32_e32 v252, v253\n"
"v_mfma_f32_16x16x32_bf16 v[56:59], v[88:91], v[128:131], v[56:59]\n"
"v_add_f32_e32 v252, v252, v253\n"
"v_mov_b32_e32 v253, v252\n"
"v_mfma_f32_16x16x32_bf16 v[108:111], v[48:51], v[128:131], v[108:111]\n"
"s_nop 0\n"
"v_permlane32_swap_b32_e32 v252, v253\n"
"v_add_f32_e32 v252, v252, v253\n"
"v_mfma_f32_16x16x32_bf16 v[132:135], v[92:95], v[128:131], v[132:135]\n"
"v_mul_f32_e32 v252, 0x3a800000, v252\n"
"v_mov_b32_e32 v253, v252\n"
"v_mfma_f32_16x16x32_bf16 v[144:147], v[96:99], v[128:131], v[144:147]\n"
"s_nop 0\n"
"v_permlane16_swap_b32_e32 v252, v253\n"
"v_cvt_pk_bf16_f32 v255, -v252, 0\n"
"v_mfma_f32_16x16x32_bf16 v[156:159], v[168:171], v[128:131], v[156:159]\n"
"buffer_load_dwordx4 v[124:127], v1, s[4:7], s97 offen offset:2048 sc1\n"
"v_mfma_f32_16x16x32_bf16 v[172:175], v[104:107], v[128:131], v[172:175]\n"
"buffer_load_dwordx4 a[180:183], v1, s[4:7], s98 offen offset:1024 sc1\n"
"v_mfma_f32_16x16x32_bf16 v[56:59], v[136:139], v[160:163], v[56:59]\n"
"v_fma_f32 v253, -v252, v252, v253\n"
"v_lshlrev_b32_e32 v255, 16, v255\n"
"v_mfma_f32_16x16x32_bf16 v[108:111], v[180:183], v[160:163], v[108:111]\n"
"ds_read_b128 v[128:131], v4\n"
"ds_read_b128 v[164:167], v9\n"
"v_mfma_f32_16x16x32_bf16 v[132:135], v[28:31], v[160:163], v[132:135]\n"
"ds_read_b128 v[176:179], v4 offset:1024\n"
"ds_read_b128 v[188:191], v9 offset:64\n"
"v_mfma_f32_16x16x32_bf16 v[144:147], v[32:35], v[160:163], v[144:147]\n"
"ds_read_b128 v[192:195], v4 offset:2048\n"
"ds_read_b128 v[196:199], v9 offset:128\n"
"v_mfma_f32_16x16x32_bf16 v[156:159], v[60:63], v[160:163], v[156:159]\n"
"buffer_load_dwordx4 v[200:203], v1, s[4:7], s97 offen offset:3072 sc1\n"
"v_mfma_f32_16x16x32_bf16 v[172:175], v[64:67], v[160:163], v[172:175]\n"
"s_waitcnt vmcnt(20)\n"
"v_max_f32_e32 v253, 0, v253\n"
"; @A G1 2.2\n"
"v_mfma_f32_16x16x32_bf16 v[160:163], a[0:3], v[72:75], 0\n"
"v_cvt_pk_bf16_f32 v253, v253, 0\n"
"v_lshlrev_b32_e32 v253, 16, v253\n"
"v_mfma_f32_16x16x32_bf16 v[204:207], a[4:7], v[72:75], 0\n"
"v_add_f32_e32 v253, 0x3727c5ac, v253\n"
"v_cvt_pk_bf16_f32 v253, v253, 0\n"
"v_mfma_f32_16x16x32_bf16 v[208:211], a[8:11], v[72:75], 0\n"
"s_waitcnt lgkmcnt(0)\n"
"v_fmac_f32_e32 v128, v255, v164\n"
"v_mfma_f32_16x16x32_bf16 v[212:215], a[12:15], v[72:75], 0\n"
"ds_write_b128 v3, v[56:59] offset:6144\n"
"v_mfma_f32_16x16x32_bf16 v[56:59], a[16:19], v[72:75], 0\n"
"s_add_u32 s97, s52, 0\n"
"s_mov_b32 s96, .Lmk_a4-.Lmk_g101\n"
"s_getpc_b64 s[84:85]\n"
".Lmk_g101:\n"
"v_mfma_f32_16x16x32_bf16 v[216:219], a[20:23], v[72:75], 0\n"
"buffer_load_dwordx4 v[72:75], v0, s[4:7], s97 offen sc1\n"
"s_add_u32 s84, s84, s96\n"
"s_addc_u32 s85, s85, 0\n"
"v_mfma_f32_16x16x32_bf16 v[160:163], a[24:27], v[24:27], v[160:163]\n"
"ds_write_b128 v3, v[108:111] offset:7168\n"
"v_mfma_f32_16x16x32_bf16 v[204:207], a[28:31], v[24:27], v[204:207]\n"
"s_and_b32 s84, s84, -16\n"
"s_and_b32 s85, s85, 0xffff\n"
"s_sub_u32 s96, .Lmk_end-.Lmk_g101, s96\n"
"s_min_u32 s86, s96, 0x4000\n"
"v_mfma_f32_16x16x32_bf16 v[208:211], a[32:35], v[24:27], v[208:211]\n"
"s_mov_b32 s87, 0x20000\n"
"v_fmac_f32_e32 v129, v255, v165\n"
"v_mfma_f32_16x16x32_bf16 v[212:215], a[36:39], v[24:27], v[212:215]\n"
"ds_write_b128 v3, v[132:135] offset:8192\n"
"v_mfma_f32_16x16x32_bf16 v[56:59], a[40:43], v[24:27], v[56:59]\n"
"buffer_load_dwordx4 v[108:111], v0, s[4:7], s97 offen offset:1024 sc1\n"
"v_mfma_f32_16x16x32_bf16 v[216:219], a[44:47], v[24:27], v[216:219]\n"
"s_waitcnt vmcnt(20)\n"
"v_fmac_f32_e32 v130, v255, v166\n"
"v_mfma_f32_16x16x32_bf16 v[160:163], a[48:51], v[100:103], v[160:163]\n"
"ds_write_b128 v3, v[144:147] offset:9216\n"
"v_mfma_f32_16x16x32_bf16 v[204:207], a[52:55], v[100:103], v[204:207]\n"
"buffer_load_dwordx4 a[184:187], v1, s[4:7], s98 offen offset:2048 sc1\n"
"v_mfma_f32_16x16x32_bf16 v[208:211], a[56:59], v[100:103], v[208:211]\n"
"s_mov_b32 m0, 0x27800\n"
"s_nop 0\n"
"buffer_load_dwordx4 v0, s[84:87], s99 offen lds\n"
"v_mfma_f32_16x16x32_bf16 v[212:215], a[60:63], v[100:103], v[212:215]\n"
"ds_write_b128 v3, v[156:159] offset:10240\n"
"v_mfma_f32_16x16x32_bf16 v[56:59], a[64:67], v[100:103], v[56:59]\n"
"buffer_load_dwordx4 v[24:27], v0, s[4:7], s97 offen offset:2048 sc1\n"
"v_mfma_f32_16x16x32_bf16 v[216:219], a[68:71], v[100:103], v[216:219]\n"
"v_fmac_f32_e32 v131, v255, v167\n"
"v_fmac_f32_e32 v176, v255, v188\n"
"v_mfma_f32_16x16x32_bf16 v[160:163], a[72:75], v[152:155], v[160:163]\n"
"ds_write_b128 v3, v[172:175] offset:11264\n"
"v_mfma_f32_16x16x32_bf16 v[204:207], a[76:79], v[152:155], v[204:207]\n"
"v_fmac_f32_e32 v177, v255, v189\n"
"v_fmac_f32_e32 v178, v255, v190\n"
"v_mfma_f32_16x16x32_bf16 v[208:211], a[80:83], v[152:155], v[208:211]\n"
"v_fmac_f32_e32 v179, v255, v191\n"
"v_fmac_f32_e32 v192, v255, v196\n"
"v_mfma_f32_16x16x32_bf16 v[212:215], a[84:87], v[152:155], v[212:215]\n"
"v_fmac_f32_e32 v193, v255, v197\n"
"v_fmac_f32_e32 v194, v255, v198\n"
"v_mfma_f32_16x16x32_bf16 v[56:59], a[88:91], v[152:155], v[56:59]\n"
"buffer_load_dwordx4 v[100:103], v0, s[4:7], s97 offen offset:3072 sc1\n"
"v_mfma_f32_16x16x32_bf16 v[216:219], a[92:95], v[152:155], v[216:219]\n"
"s_waitcnt vmcnt(20)\n"
"v_fmac_f32_e32 v195, v255, v199\n"
"v_mfma_f32_16x16x32_bf16 v[160:163], a[96:99], v[112:115], v[160:163]\n"
"ds_read_b128 v[132:135], v4 offset:3072\n"
"ds_read_b128 v[144:147], v9 offset:192\n"
"v_mfma_f32_16x16x32_bf16 v[204:207], a[100:103], v[112:115], v[204:207]\n"
"ds_read_b128 v[152:155], v4 offset:4096\n"
"ds_read_b128 v[156:159], v9 offset:256\n"
"v_mfma_f32_16x16x32_bf16 v[208:211], a[104:107], v[112:115], v[208:211]\n"
"ds_read_b128 v[164:167], v4 offset:5120\n"
"ds_read_b128 v[172:175], v9 offset:320\n"
"v_mfma_f32_16x16x32_bf16 v[212:215], a[108:111], v[112:115], v[212:215]\n"
"v_lshlrev_b32_e32 v253, 16, v253\n"
"v_mfma_f32_16x16x32_bf16 v[56:59], a[112:115], v[112:115], v[56:59]\n"
"buffer_load_dwordx4 v[188:191], v1, s[4:7], s97 offen sc1\n"
"v_mfma_f32_16x16x32_bf16 v[216:219], a[116:119], v[112:115], v[216:219]\n"
"v_sqrt_f32_e32 v253, v253\n"
"v_mfma_f32_16x16x32_bf16 v[160:163], a[120:123], v[120:123], v[160:163]\n"
"v_cvt_pk_bf16_f32 v253, v253, 0\n"
"v_lshlrev_b32_e32 v253, 16, v253\n"
"v_mfma_f32_16x16x32_bf16 v[204:207], a[124:127], v[120:123], v[204:207]\n"
"s_waitcnt lgkmcnt(0)\n"
"v_fmac_f32_e32 v132, v255, v144\n"
"v_mfma_f32_16x16x32_bf16 v[208:211], v[40:43], v[120:123], v[208:211]\n"
"v_fmac_f32_e32 v133, v255, v145\n"
"v_fmac_f32_e32 v134, v255, v146\n"
"v_mfma_f32_16x16x32_bf16 v[212:215], v[44:47], v[120:123], v[212:215]\n"
"buffer_load_dwordx4 a[188:191], v1, s[4:7], s98 offen offset:3072 sc1\n"
"v_mfma_f32_16x16x32_bf16 v[56:59], v[80:83], v[120:123], v[56:59]\n"
"buffer_load_dwordx4 v[112:115], v1, s[4:7], s97 offen offset:1024 sc1\n"
"v_mfma_f32_16x16x32_bf16 v[216:219], v[68:71], v[120:123], v[216:219]\n"
"s_waitcnt vmcnt(20)\n"
"v_mfma_f32_16x16x32_bf16 v[160:163], v[88:91], v[140:143], v[160:163]\n"
"s_add_u32 s98, s68, 0x1000\n"
"v_fmac_f32_e32 v135, v255, v147\n"
"v_mfma_f32_16x16x32_bf16 v[204:207], v[48:51], v[140:143], v[204:207]\n"
"buffer_store_dwordx2 v[18:19], v11, s[4:7], s98 offen offset:64 sc1\n"
"v_mfma_f32_16x16x32_bf16 v[208:211], v[92:95], v[140:143], v[208:211]\n"
"v_fmac_f32_e32 v152, v255, v156\n"
"v_fmac_f32_e32 v153, v255, v157\n"
"v_mfma_f32_16x16x32_bf16 v[212:215], v[96:99], v[140:143], v[212:215]\n"
"v_fmac_f32_e32 v154, v255, v158\n"
"v_fmac_f32_e32 v155, v255, v159\n"
"v_mfma_f32_16x16x32_bf16 v[56:59], v[168:171], v[140:143], v[56:59]\n"
"buffer_load_dwordx4 v[120:123], v1, s[4:7], s97 offen offset:2048 sc1\n"
"v_mfma_f32_16x16x32_bf16 v[216:219], v[104:107], v[140:143], v[216:219]\n"
"v_fmac_f32_e32 v164, v255, v172\n"
"v_fmac_f32_e32 v165, v255, v173\n"
"v_mfma_f32_16x16x32_bf16 v[160:163], v[136:139], v[36:39], v[160:163]\n"
"v_fmac_f32_e32 v166, v255, v174\n"
"v_fmac_f32_e32 v167, v255, v175\n"
"v_mfma_f32_16x16x32_bf16 v[204:207], v[180:183], v[36:39], v[204:207]\n"
"v_rcp_f32_e32 v255, v253\n"
"v_mfma_f32_16x16x32_bf16 v[208:211], v[28:31], v[36:39], v[208:211]\n"
"v_mul_f32_e32 v254, 0x3e8293ee, v255\n"
"v_mfma_f32_16x16x32_bf16 v[212:215], v[32:35], v[36:39], v[212:215]\n"
"v_mfma_f32_16x16x32_bf16 v[56:59], v[60:63], v[36:39], v[56:59]\n"
"buffer_load_dwordx4 v[140:143], v1, s[4:7], s97 offen offset:3072 sc1\n"
"v_mfma_f32_16x16x32_bf16 v[216:219], v[64:67], v[36:39], v[216:219]\n"
"s_waitcnt vmcnt(20)\n"
"; @A G1 2.3\n"
"v_mfma_f32_16x16x32_bf16 v[128:131], a[0:3], v[84:87], v[128:131]\n"
"v_mfma_f32_16x16x32_bf16 v[176:179], a[4:7], v[84:87], v[176:179]\n"
"v_mfma_f32_16x16x32_bf16 v[192:195], a[8:11], v[84:87], v[192:195]\n"
"v_mfma_f32_16x16x32_bf16 v[132:135], a[12:15], v[84:87], v[132:135]\n"
"ds_write_b128 v3, v[160:163] offset:12288\n"
"v_mfma_f32_16x16x32_bf16 v[152:155], a[16:19], v[84:87], v[152:155]\n"
"s_add_u32 s98, s53, 0\n"
"s_add_u32 s97, s70, 0x4000\n"
"v_mfma_f32_16x16x32_bf16 v[164:167], a[20:23], v[84:87], v[164:167]\n"
"buffer_load_dwordx4 v[36:39], v0, s[4:7], s98 offen sc1\n"
"v_mfma_f32_16x16x32_bf16 v[128:131], a[24:27], v[184:187], v[128:131]\n"
"ds_write_b128 v3, v[204:207] offset:13312\n"
"v_mfma_f32_16x16x32_bf16 v[176:179], a[28:31], v[184:187], v[176:179]\n"
"buffer_load_dwordx4 a[192:195], v0, s[4:7], s97 offen sc1\n"
"v_mfma_f32_16x16x32_bf16 v[192:195], a[32:35], v[184:187], v[192:195]\n"
"v_mfma_f32_16x16x32_bf16 v[132:135], a[36:39], v[184:187], v[132:135]\n"
"ds_write_b128 v3, v[208:211] offset:14336\n"
"v_mfma_f32_16x16x32_bf16 v[152:155], a[40:43], v[184:187], v[152:155]\n"
"buffer_load_dwordx4 v[84:87], v0, s[4:7], s98 offen offset:1024 sc1\n"
"v_mfma_f32_16x16x32_bf16 v[164:167], a[44:47], v[184:187], v[164:167]\n"
"s_waitcnt vmcnt(20)\n"
"v_mfma_f32_16x16x32_bf16 v[128:131], a[48:51], v[148:151], v[128:131]\n"
"ds_write_b128 v3, v[212:215] offset:15360\n"
"v_mfma_f32_16x16x32_bf16 v[176:179], a[52:55], v[148:151], v[176:179]\n"
"v_mfma_f32_16x16x32_bf16 v[192:195], a[56:59], v[148:151], v[192:195]\n"
"v_mfma_f32_16x16x32_bf16 v[132:135], a[60:63], v[148:151], v[132:135]\n"
"ds_write_b128 v3, v[56:59] offset:16384\n"
"v_mfma_f32_16x16x32_bf16 v[152:155], a[64:67], v[148:151], v[152:155]\n"
"buffer_load_dwordx4 v[56:59], v0, s[4:7], s98 offen offset:2048 sc1\n"
"v_mfma_f32_16x16x32_bf16 v[164:167], a[68:71], v[148:151], v[164:167]\n"
"v_mfma_f32_16x16x32_bf16 v[128:131], a[72:75], v[52:55], v[128:131]\n"
"ds_write_b128 v3, v[216:219] offset:17408\n"
"v_mfma_f32_16x16x32_bf16 v[176:179], a[76:79], v[52:55], v[176:179]\n"
"v_mfma_f32_16x16x32_bf16 v[192:195], a[80:83], v[52:55], v[192:195]\n"
"v_mfma_f32_16x16x32_bf16 v[132:135], a[84:87], v[52:55], v[132:135]\n"
"s_waitcnt lgkmcnt(0)\n"
"s_barrier\n"
"v_mfma_f32_16x16x32_bf16 v[152:155], a[88:91], v[52:55], v[152:155]\n"
"buffer_load_dwordx4 v[144:147], v0, s[4:7], s98 offen offset:3072 sc1\n"
"v_mfma_f32_16x16x32_bf16 v[164:167], a[92:95], v[52:55], v[164:167]\n"
"s_waitcnt vmcnt(20)\n"
"ds_read_b128 v[52:55], v5 offset:2048\n"
"v_mfma_f32_16x16x32_bf16 v[128:131], a[96:99], v[76:79], v[128:131]\n"
"ds_read_b128 v[148:151], v6 offset:2048\n"
"ds_read_b128 v[156:159], v5 offset:3072\n"
"v_mfma_f32_16x16x32_bf16 v[176:179], a[100:103], v[76:79], v[176:179]\n"
"ds_read_b128 v[160:163], v6 offset:3072\n"
"v_mfma_f32_16x16x32_bf16 v[192:195], a[104:107], v[76:79], v[192:195]\n"
"v_mfma_f32_16x16x32_bf16 v[132:135], a[108:111], v[76:79], v[132:135]\n"
"buffer_load_dwordx4 a[196:199], v0, s[4:7], s97 offen offset:1024 sc1\n"
"v_mfma_f32_16x16x32_bf16 v[152:155], a[112:115], v[76:79], v[152:155]\n"
"buffer_load_dwordx4 v[172:175], v1, s[4:7], s98 offen sc1\n"
"v_mfma_f32_16x16x32_bf16 v[164:167], a[116:119], v[76:79], v[164:167]\n"
"ds_read_b128 v[76:79], v5 offset:4096\n"
"ds_read_b128 v[184:187], v6 offset:4096\n"
"v_mfma_f32_16x16x32_bf16 v[128:131], a[120:123], v[116:119], v[128:131]\n"
"ds_read_b128 v[196:199], v5 offset:5120\n"
"ds_read_b128 v[204:207], v6 offset:5120\n"
"v_mfma_f32_16x16x32_bf16 v[176:179], a[124:127], v[116:119], v[176:179]\n"
"s_waitcnt lgkmcnt(4)\n"
"v_add_f32_e32 v52, v52, v148\n"
"v_mfma_f32_16x16x32_bf16 v[192:195], v[40:43], v[116:119], v[192:195]\n"
"v_add_f32_e32 v53, v53, v149\n"
"v_add_f32_e32 v54, v54, v150\n"
"v_mfma_f32_16x16x32_bf16 v[132:135], v[44:47], v[116:119], v[132:135]\n"
"ds_read_b128 v[208:211], v5\n"
"ds_read_b128 v[212:215], v6\n"
"v_mfma_f32_16x16x32_bf16 v[152:155], v[80:83], v[116:119], v[152:155]\n"
"buffer_load_dwordx4 v[216:219], v1, s[4:7], s98 offen offset:1024 sc1\n"
"v_mfma_f32_16x16x32_bf16 v[164:167], v[68:71], v[116:119], v[164:167]\n"
"s_waitcnt vmcnt(20)\n"
"ds_read_b128 v[116:119], v5 offset:1024\n"
"v_mfma_f32_16x16x32_bf16 v[128:131], v[88:91], v[124:127], v[128:131]\n"
"ds_read_b128 v[220:223], v6 offset:1024\n"
"v_add_f32_e32 v55, v55, v151\n"
"v_mfma_f32_16x16x32_bf16 v[176:179], v[48:51], v[124:127], v[176:179]\n"
"v_add_f32_e32 v156, v156, v160\n"
"v_add_f32_e32 v157, v157, v161\n"
"v_mfma_f32_16x16x32_bf16 v[192:195], v[92:95], v[124:127], v[192:195]\n"
"v_add_f32_e32 v158, v158, v162\n"
"v_add_f32_e32 v159, v159, v163\n"
"v_mfma_f32_16x16x32_bf16 v[132:135], v[96:99], v[124:127], v[132:135]\n"
"s_waitcnt lgkmcnt(4)\n"
"v_add_f32_e32 v76, v76, v184\n"
"v_mfma_f32_16x16x32_bf16 v[152:155], v[168:171], v[124:127], v[152:155]\n"
"buffer_load_dwordx4 v[148:151], v1, s[4:7], s98 offen offset:2048 sc1\n"
"v_mfma_f32_16x16x32_bf16 v[164:167], v[104:107], v[124:127], v[164:167]\n"
"v_add_f32_e32 v77, v77, v185\n"
"v_add_f32_e32 v78, v78, v186\n"
"v_mfma_f32_16x16x32_bf16 v[128:131], v[136:139], v[200:203], v[128:131]\n"
"v_add_f32_e32 v79, v79, v187\n"
"v_add_f32_e32 v196, v196, v204\n"
"v_mfma_f32_16x16x32_bf16 v[176:179], v[180:183], v[200:203], v[176:179]\n"
"v_add_f32_e32 v197, v197, v205\n"
"v_add_f32_e32 v198, v198, v206\n"
"v_mfma_f32_16x16x32_bf16 v[192:195], v[28:31], v[200:203], v[192:195]\n"
"v_add_f32_e32 v199, v199, v207\n"
"s_waitcnt lgkmcnt(0)\n"
"v_mfma_f32_16x16x32_bf16 v[132:135], v[32:35], v[200:203], v[132:135]\n"
"v_add_f32_e32 v208, v208, v212\n"
"v_add_f32_e32 v209, v209, v213\n"
"v_mfma_f32_16x16x32_bf16 v[152:155], v[60:63], v[200:203], v[152:155]\n"
"buffer_load_dwordx4 v[124:127], v1, s[4:7], s98 offen offset:3072 sc1\n"
"v_mfma_f32_16x16x32_bf16 v[164:167], v[64:67], v[200:203], v[164:167]\n"
"v_add_f32_e32 v210, v210, v214\n"
"v_add_f32_e32 v211, v211, v215\n"
"v_add_f32_e32 v116, v116, v220\n"
"v_add_f32_e32 v117, v117, v221\n"
"v_add_f32_e32 v118, v118, v222\n"
"v_add_f32_e32 v119, v119, v223\n"
".Lmk_a3:\n"
"s_waitcnt vmcnt(20)\n"
"; @A G1 3.0\n"
"v_mfma_f32_16x16x32_bf16 v[160:163], a[0:3], v[72:75], 0\n"
"s_add_u32 s98, s62, 0\n"
"v_mfma_f32_16x16x32_bf16 v[184:187], a[4:7], v[72:75], 0\n"
"buffer_load_dwordx2 v[252:253], v10, s[4:7], s98 offen sc1\n"
"v_mfma_f32_16x16x32_bf16 v[200:203], a[8:11], v[72:75], 0\n"
"buffer_load_dwordx2 v[250:251], v10, s[4:7], s98 offen offset:128 sc1\n"
"v_mfma_f32_16x16x32_bf16 v[204:207], a[12:15], v[72:75], 0\n"
"buffer_load_dwordx2 v[248:249], v10, s[4:7], s98 offen offset:256 sc1\n"
"v_mfma_f32_16x16x32_bf16 v[212:215], a[16:19], v[72:75], 0\n"
"s_add_u32 s96, s54, 0\n"
"v_add_f32_e32 v192, v192, v52\n"
"v_mfma_f32_16x16x32_bf16 v[220:223], a[20:23], v[72:75], 0\n"
"buffer_load_dwordx4 v[72:75], v0, s[4:7], s96 offen sc1\n"
"v_mfma_f32_16x16x32_bf16 v[160:163], a[24:27], v[108:111], v[160:163]\n"
"buffer_load_dwordx2 v[246:247], v10, s[4:7], s98 offen offset:384 sc1\n"
"v_mfma_f32_16x16x32_bf16 v[184:187], a[28:31], v[108:111], v[184:187]\n"
"v_add_f32_e32 v193, v193, v53\n"
"v_add_f32_e32 v194, v194, v54\n"
"v_mfma_f32_16x16x32_bf16 v[200:203], a[32:35], v[108:111], v[200:203]\n"
"buffer_load_dwordx4 a[200:203], v0, s[4:7], s97 offen offset:2048 sc1\n"
"v_mfma_f32_16x16x32_bf16 v[204:207], a[36:39], v[108:111], v[204:207]\n"
"v_add_f32_e32 v195, v195, v55\n"
"v_add_f32_e32 v132, v132, v156\n"
"v_mfma_f32_16x16x32_bf16 v[212:215], a[40:43], v[108:111], v[212:215]\n"
"buffer_load_dwordx4 v[52:55], v0, s[4:7], s96 offen offset:1024 sc1\n"
"v_mfma_f32_16x16x32_bf16 v[220:223], a[44:47], v[108:111], v[220:223]\n"
"s_waitcnt vmcnt(23)\n"
"v_add_f32_e32 v133, v133, v157\n"
"v_mfma_f32_16x16x32_bf16 v[160:163], a[48:51], v[24:27], v[160:163]\n"
"v_add_f32_e32 v134, v134, v158\n"
"v_add_f32_e32 v135, v135, v159\n"
"v_mfma_f32_16x16x32_bf16 v[184:187], a[52:55], v[24:27], v[184:187]\n"
"v_mul_f32_e32 v192, v192, v255\n"
"v_mul_f32_e32 v193, v193, v255\n"
"v_mfma_f32_16x16x32_bf16 v[200:203], a[56:59], v[24:27], v[200:203]\n"
"v_mul_f32_e32 v194, v194, v255\n"
"v_mul_f32_e32 v195, v195, v255\n"
"v_mfma_f32_16x16x32_bf16 v[204:207], a[60:63], v[24:27], v[204:207]\n"
"v_mul_f32_e32 v132, v132, v255\n"
"v_mul_f32_e32 v133, v133, v255\n"
"v_mfma_f32_16x16x32_bf16 v[212:215], a[64:67], v[24:27], v[212:215]\n"
"buffer_load_dwordx4 v[108:111], v0, s[4:7], s96 offen offset:2048 sc1\n"
"v_mfma_f32_16x16x32_bf16 v[220:223], a[68:71], v[24:27], v[220:223]\n"
"v_mul_f32_e32 v134, v134, v255\n"
"v_mul_f32_e32 v135, v135, v255\n"
"v_mfma_f32_16x16x32_bf16 v[160:163], a[72:75], v[100:103], v[160:163]\n"
"v_cvt_pk_bf16_f32 v24, v192, v193\n"
"v_cvt_pk_bf16_f32 v25, v194, v195\n"
"v_mfma_f32_16x16x32_bf16 v[184:187], a[76:79], v[100:103], v[184:187]\n"
"v_cvt_pk_bf16_f32 v26, v132, v133\n"
"v_cvt_pk_bf16_f32 v27, v134, v135\n"
"v_mfma_f32_16x16x32_bf16 v[200:203], a[80:83], v[100:103], v[200:203]\n"
"ds_write_b128 v8, v[24:27] offset:17408\n"
"v_mfma_f32_16x16x32_bf16 v[204:207], a[84:87], v[100:103], v[204:207]\n"
"buffer_store_dwordx4 v[24:27], v8, s[4:7], s64 offen offset:1024 sc1\n"
"v_mfma_f32_16x16x32_bf16 v[212:215], a[88:91], v[100:103], v[212:215]\n"
"buffer_load_dwordx4 v[24:27], v0, s[4:7], s96 offen offset:3072 sc1\n"
"v_mfma_f32_16x16x32_bf16 v[220:223], a[92:95], v[100:103], v[220:223]\n"
"s_waitcnt vmcnt(23)\n"
"v_add_f32_e32 v152, v152, v76\n"
"v_mfma_f32_16x16x32_bf16 v[160:163], a[96:99], v[188:191], v[160:163]\n"
"v_add_f32_e32 v153, v153, v77\n"
"v_add_f32_e32 v154, v154, v78\n"
"v_mfma_f32_16x16x32_bf16 v[184:187], a[100:103], v[188:191], v[184:187]\n"
"v_add_f32_e32 v155, v155, v79\n"
"v_add_f32_e32 v164, v164, v196\n"
"v_mfma_f32_16x16x32_bf16 v[200:203], a[104:107], v[188:191], v[200:203]\n"
"buffer_load_dwordx4 a[204:207], v0, s[4:7], s97 offen offset:3072 sc1\n"
"v_mfma_f32_16x16x32_bf16 v[204:207], a[108:111], v[188:191], v[204:207]\n"
"v_add_f32_e32 v165, v165, v197\n"
"v_add_f32_e32 v166, v166, v198\n"
"v_mfma_f32_16x16x32_bf16 v[212:215], a[112:115], v[188:191], v[212:215]\n"
"buffer_load_dwordx4 v[76:79], v1, s[4:7], s96 offen sc1\n"
"v_mfma_f32_16x16x32_bf16 v[220:223], a[116:119], v[188:191], v[220:223]\n"
"v_add_f32_e32 v167, v167, v199\n"
"v_mul_f32_e32 v152, v152, v255\n"
"v_mfma_f32_16x16x32_bf16 v[160:163], a[120:123], v[112:115], v[160:163]\n"
"v_mul_f32_e32 v153, v153, v255\n"
"v_mul_f32_e32 v154, v154, v255\n"
"v_mfma_f32_16x16x32_bf16 v[184:187], a[124:127], v[112:115], v[184:187]\n"
"v_mul_f32_e32 v155, v155, v255\n"
"v_mul_f32_e32 v164, v164, v255\n"
"v_mfma_f32_16x16x32_bf16 v[200:203], v[40:43], v[112:115], v[200:203]\n"
"v_mul_f32_e32 v165, v165, v255\n"
"v_mul_f32_e32 v166, v166, v255\n"
"v_mfma_f32_16x16x32_bf16 v[204:207], v[44:47], v[112:115], v[204:207]\n"
"v_mul_f32_e32 v167, v167, v255\n"
"v_cvt_pk_bf16_f32 v100, v152, v153\n"
"v_mfma_f32_16x16x32_bf16 v[212:215], v[80:83], v[112:115], v[212:215]\n"
"buffer_load_dwordx4 v[132:135], v1, s[4:7], s96 offen offset:1024 sc1\n"
"v_mfma_f32_16x16x32_bf16 v[220:223], v[68:71], v[112:115], v[220:223]\n"
"s_waitcnt vmcnt(23)\n"
"v_cvt_pk_bf16_f32 v101, v154, v155\n"
"v_mfma_f32_16x16x32_bf16 v[160:163], v[88:91], v[120:123], v[160:163]\n"
"v_cvt_pk_bf16_f32 v102, v164, v165\n"
"v_cvt_pk_bf16_f32 v103, v166, v167\n"
"v_mfma_f32_16x16x32_bf16 v[184:187], v[48:51], v[120:123], v[184:187]\n"
"ds_write_b128 v7, v[100:103] offset:50176\n"
"v_mfma_f32_16x16x32_bf16 v[200:203], v[92:95], v[120:123], v[200:203]\n"
"s_add_u32 s98, s64, 0x4000\n"
"v_mfma_f32_16x16x32_bf16 v[204:207], v[96:99], v[120:123], v[204:207]\n"
"buffer_store_dwordx4 v[100:103], v7, s[4:7], s98 offen offset:1024 sc1\n"
"v_mfma_f32_16x16x32_bf16 v[212:215], v[168:171], v[120:123], v[212:215]\n"
"buffer_load_dwordx4 v[100:103], v1, s[4:7], s96 offen offset:2048 sc1\n"
"v_mfma_f32_16x16x32_bf16 v[220:223], v[104:107], v[120:123], v[220:223]\n"
"v_add_f32_e32 v128, v128, v208\n"
"v_add_f32_e32 v129, v129, v209\n"
"v_mfma_f32_16x16x32_bf16 v[160:163], v[136:139], v[140:143], v[160:163]\n"
"v_add_f32_e32 v130, v130, v210\n"
"v_add_f32_e32 v131, v131, v211\n"
"v_mfma_f32_16x16x32_bf16 v[184:187], v[180:183], v[140:143], v[184:187]\n"
"v_add_f32_e32 v176, v176, v116\n"
"v_add_f32_e32 v177, v177, v117\n"
"v_mfma_f32_16x16x32_bf16 v[200:203], v[28:31], v[140:143], v[200:203]\n"
"buffer_load_dwordx4 a[208:211], v1, s[4:7], s97 offen sc1\n"
"v_mfma_f32_16x16x32_bf16 v[204:207], v[32:35], v[140:143], v[204:207]\n"
"v_add_f32_e32 v178, v178, v118\n"
"v_add_f32_e32 v179, v179, v119\n"
"v_mfma_f32_16x16x32_bf16 v[212:215], v[60:63], v[140:143], v[212:215]\n"
"buffer_load_dwordx4 v[112:115], v1, s[4:7], s96 offen offset:3072 sc1\n"
"v_mfma_f32_16x16x32_bf16 v[220:223], v[64:67], v[140:143], v[220:223]\n"
"s_waitcnt vmcnt(24)\n"
"v_mul_f32_e32 v128, v128, v254\n"
"; @A G1 3.1\n"
"v_mfma_f32_16x16x32_bf16 v[116:119], a[0:3], v[36:39], 0\n"
"v_mul_f32_e32 v129, v129, v254\n"
"v_mul_f32_e32 v130, v130, v254\n"
"v_mfma_f32_16x16x32_bf16 v[120:123], a[4:7], v[36:39], 0\n"
"v_mul_f32_e32 v131, v131, v254\n"
"v_mul_f32_e32 v176, v176, v254\n"
"v_mfma_f32_16x16x32_bf16 v[140:143], a[8:11], v[36:39], 0\n"
"v_mul_f32_e32 v177, v177, v254\n"
"v_mul_f32_e32 v178, v178, v254\n"
"v_mfma_f32_16x16x32_bf16 v[152:155], a[12:15], v[36:39], 0\n"
"ds_write_b128 v3, v[160:163]\n"
"v_mfma_f32_16x16x32_bf16 v[156:159], a[16:19], v[36:39], 0\n"
"s_add_u32 s98, s55, 0\n"
"v_mul_f32_e32 v179, v179, v254\n"
"v_mfma_f32_16x16x32_bf16 v[160:163], a[20:23], v[36:39], 0\n"
"buffer_load_dwordx4 v[36:39], v0, s[4:7], s98 offen sc1\n"
"v_mfma_f32_16x16x32_bf16 v[116:119], a[24:27], v[84:87], v[116:119]\n"
"ds_write_b128 v3, v[184:187] offset:1024\n"
"v_mfma_f32_16x16x32_bf16 v[120:123], a[28:31], v[84:87], v[120:123]\n"
"v_cvt_pk_bf16_f32 v164, v128, v129\n"
"v_cvt_pk_bf16_f32 v165, v130, v131\n"
"v_mfma_f32_16x16x32_bf16 v[140:143], a[32:35], v[84:87], v[140:143]\n"
"v_cvt_pk_bf16_f32 v166, v176, v177\n"
"v_cvt_pk_bf16_f32 v167, v178, v179\n"
"v_mfma_f32_16x16x32_bf16 v[152:155], a[36:39], v[84:87], v[152:155]\n"
"ds_write_b128 v3, v[200:203] offset:2048\n"
"v_mfma_f32_16x16x32_bf16 v[156:159], a[40:43], v[84:87], v[156:159]\n"
"buffer_load_dwordx4 v[128:131], v0, s[4:7], s98 offen offset:1024 sc1\n"
"v_mfma_f32_16x16x32_bf16 v[160:163], a[44:47], v[84:87], v[160:163]\n"
"s_waitcnt vmcnt(24)\n"
"v_mfma_f32_16x16x32_bf16 v[116:119], a[48:51], v[56:59], v[116:119]\n"
"ds_write_b128 v3, v[204:207] offset:3072\n"
"v_mfma_f32_16x16x32_bf16 v[120:123], a[52:55], v[56:59], v[120:123]\n"
"ds_write_b128 v2, v[164:167] offset:17408\n"
"v_mfma_f32_16x16x32_bf16 v[140:143], a[56:59], v[56:59], v[140:143]\n"
"buffer_load_dwordx4 a[212:215], v1, s[4:7], s97 offen offset:1024 sc1\n"
"v_mfma_f32_16x16x32_bf16 v[152:155], a[60:63], v[56:59], v[152:155]\n"
"ds_write_b128 v3, v[212:215] offset:4096\n"
"v_mfma_f32_16x16x32_bf16 v[156:159], a[64:67], v[56:59], v[156:159]\n"
"buffer_load_dwordx4 v[84:87], v0, s[4:7], s98 offen offset:2048 sc1\n"
"v_mfma_f32_16x16x32_bf16 v[160:163], a[68:71], v[56:59], v[160:163]\n"
"v_mfma_f32_16x16x32_bf16 v[116:119], a[72:75], v[144:147], v[116:119]\n"
"ds_write_b128 v3, v[220:223] offset:5120\n"
"v_mfma_f32_16x16x32_bf16 v[120:123], a[76:79], v[144:147], v[120:123]\n"
"v_mfma_f32_16x16x32_bf16 v[140:143], a[80:83], v[144:147], v[140:143]\n"
"v_mfma_f32_16x16x32_bf16 v[152:155], a[84:87], v[144:147], v[152:155]\n"
"v_mfma_f32_16x16x32_bf16 v[156:159], a[88:91], v[144:147], v[156:159]\n"
"buffer_load_dwordx4 v[56:59], v0, s[4:7], s98 offen offset:3072 sc1\n"
"v_mfma_f32_16x16x32_bf16 v[160:163], a[92:95], v[144:147], v[160:163]\n"
"s_waitcnt vmcnt(24)\n"
"v_mfma_f32_16x16x32_bf16 v[116:119], a[96:99], v[172:175], v[116:119]\n"
"v_mfma_f32_16x16x32_bf16 v[120:123], a[100:103], v[172:175], v[120:123]\n"
"v_mfma_f32_16x16x32_bf16 v[140:143], a[104:107], v[172:175], v[140:143]\n"
"v_mfma_f32_16x16x32_bf16 v[152:155], a[108:111], v[172:175], v[152:155]\n"
"v_mfma_f32_16x16x32_bf16 v[156:159], a[112:115], v[172:175], v[156:159]\n"
"buffer_load_dwordx4 v[144:147], v1, s[4:7], s98 offen sc1\n"
"v_mfma_f32_16x16x32_bf16 v[160:163], a[116:119], v[172:175], v[160:163]\n"
"s_waitcnt lgkmcnt(0)\n"
"s_barrier\n"
"v_mfma_f32_16x16x32_bf16 v[116:119], a[120:123], v[216:219], v[116:119]\n"
"s_waitcnt vmcnt(18)\n"
"v_add_f32_e32 v252, v252, v250\n"
"v_mfma_f32_16x16x32_bf16 v[120:123], a[124:127], v[216:219], v[120:123]\n"
"v_add_f32_e32 v253, v253, v251\n"
"v_add_f32_e32 v248, v248, v246\n"
"v_mfma_f32_16x16x32_bf16 v[140:143], v[40:43], v[216:219], v[140:143]\n"
"buffer_load_dwordx4 a[216:219], v1, s[4:7], s97 offen offset:2048 sc1\n"
"v_mfma_f32_16x16x32_bf16 v[152:155], v[44:47], v[216:219], v[152:155]\n"
"v_add_f32_e32 v249, v249, v247\n"
"v_add_f32_e32 v252, v252, v248\n"
"v_mfma_f32_16x16x32_bf16 v[156:159], v[80:83], v[216:219], v[156:159]\n"
"buffer_load_dwordx4 v[164:167], v1, s[4:7], s98 offen offset:1024 sc1\n"
"v_mfma_f32_16x16x32_bf16 v[160:163], v[68:71], v[216:219], v[160:163]\n"
"v_add_f32_e32 v253, v253, v249\n"
"v_mfma_f32_16x16x32_bf16 v[116:119], v[88:91], v[148:151], v[116:119]\n"
"s_nop 0\n"
"v_permlane16_swap_b32_e32 v252, v253\n"
"v_add_f32_e32 v252, v252, v253\n"
"v_mfma_f32_16x16x32_bf16 v[120:123], v[48:51], v[148:151], v[120:123]\n"
"v_mov_b32_e32 v253, v252\n"
"v_mfma_f32_16x16x32_bf16 v[140:143], v[92:95], v[148:151], v[140:143]\n"
"s_nop 0\n"
"v_permlane32_swap_b32_e32 v252, v253\n"
"v_add_f32_e32 v252, v252, v253\n"
"v_mfma_f32_16x16x32_bf16 v[152:155], v[96:99], v[148:151], v[152:155]\n"
"v_mul_f32_e32 v252, 0x3a800000, v252\n"
"v_mov_b32_e32 v253, v252\n"
"v_mfma_f32_16x16x32_bf16 v[156:159], v[168:171], v[148:151], v[156:159]\n"
"buffer_load_dwordx4 v[172:175], v1, s[4:7], s98 offen offset:2048 sc1\n"
"v_mfma_f32_16x16x32_bf16 v[160:163], v[104:107], v[148:151], v[160:163]\n"
"v_permlane16_swap_b32_e32 v252, v253\n"
"v_cvt_pk_bf16_f32 v255, -v252, 0\n"
"v_mfma_f32_16x16x32_bf16 v[116:119], v[136:139], v[124:127], v[116:119]\n"
"s_add_u32 s96, s71, 0\n"
"v_fma_f32 v253, -v252, v252, v253\n"
"v_mfma_f32_16x16x32_bf16 v[120:123], v[180:183], v[124:127], v[120:123]\n"
"buffer_load_dwordx2 v[250:251], off, s[4:7], s96 sc1\n"
"v_mfma_f32_16x16x32_bf16 v[140:143], v[28:31], v[124:127], v[140:143]\n"
"v_lshlrev_b32_e32 v255, 16, v255\n"
"ds_read_b128 v[148:151], v4\n"
"v_mfma_f32_16x16x32_bf16 v[152:155], v[32:35], v[124:127], v[152:155]\n"
"ds_read_b128 v[176:179], v9\n"
"ds_read_b128 v[184:187], v4 offset:1024\n"
"v_mfma_f32_16x16x32_bf16 v[156:159], v[60:63], v[124:127], v[156:159]\n"
"buffer_load_dwordx4 v[188:191], v1, s[4:7], s98 offen offset:3072 sc1\n"
"v_mfma_f32_16x16x32_bf16 v[160:163], v[64:67], v[124:127], v[160:163]\n"
"s_waitcnt vmcnt(21)\n"
"; @A G1 3.2\n"
"v_mfma_f32_16x16x32_bf16 v[124:127], a[0:3], v[72:75], 0\n"
"buffer_store_dwordx2 v[18:19], v11, s[4:7], s68 offen offset:32 sc1\n"
"v_mfma_f32_16x16x32_bf16 v[192:195], a[4:7], v[72:75], 0\n"
"ds_read_b128 v[196:199], v9 offset:64\n"
"ds_read_b128 v[200:203], v4 offset:2048\n"
"v_mfma_f32_16x16x32_bf16 v[204:207], a[8:11], v[72:75], 0\n"
"buffer_load_dwordx4 a[220:223], v1, s[4:7], s97 offen offset:3072 sc1\n"
"v_mfma_f32_16x16x32_bf16 v[208:211], a[12:15], v[72:75], 0\n"
"ds_write_b128 v3, v[116:119] offset:6144\n"
"v_mfma_f32_16x16x32_bf16 v[116:119], a[16:19], v[72:75], 0\n"
"s_add_u32 s98, s70, 0x6000\n"
"ds_read_b128 v[212:215], v9 offset:128\n"
"v_mfma_f32_16x16x32_bf16 v[216:219], a[20:23], v[72:75], 0\n"
"ds_read_b128 v[72:75], v4 offset:3072\n"
"ds_read_b128 v[220:223], v9 offset:192\n"
"v_mfma_f32_16x16x32_bf16 v[124:127], a[24:27], v[52:55], v[124:127]\n"
"ds_write_b128 v3, v[120:123] offset:7168\n"
"v_mfma_f32_16x16x32_bf16 v[192:195], a[28:31], v[52:55], v[192:195]\n"
"buffer_load_dwordx4 a[224:227], v0, s[4:7], s98 offen sc1\n"
"v_mfma_f32_16x16x32_bf16 v[204:207], a[32:35], v[52:55], v[204:207]\n"
"ds_read_b128 v[120:123], v4 offset:4096\n"
"ds_read_b128 v[224:227], v9 offset:256\n"
"v_mfma_f32_16x16x32_bf16 v[208:211], a[36:39], v[52:55], v[208:211]\n"
"ds_write_b128 v3, v[140:143] offset:8192\n"
"v_mfma_f32_16x16x32_bf16 v[116:119], a[40:43], v[52:55], v[116:119]\n"
"s_waitcnt lgkmcnt(6)\n"
"v_fmac_f32_e32 v148, v255, v176\n"
"v_mfma_f32_16x16x32_bf16 v[216:219], a[44:47], v[52:55], v[216:219]\n"
"s_waitcnt vmcnt(21)\n"
"v_fmac_f32_e32 v149, v255, v177\n"
"v_mfma_f32_16x16x32_bf16 v[124:127], a[48:51], v[108:111], v[124:127]\n"
"ds_write_b128 v3, v[152:155] offset:9216\n"
"v_mfma_f32_16x16x32_bf16 v[192:195], a[52:55], v[108:111], v[192:195]\n"
"buffer_load_dwordx4 a[228:231], v0, s[4:7], s98 offen offset:1024 sc1\n"
"v_mfma_f32_16x16x32_bf16 v[204:207], a[56:59], v[108:111], v[204:207]\n"
"v_fmac_f32_e32 v150, v255, v178\n"
"v_fmac_f32_e32 v151, v255, v179\n"
"v_mfma_f32_16x16x32_bf16 v[208:211], a[60:63], v[108:111], v[208:211]\n"
"ds_write_b128 v3, v[156:159] offset:10240\n"
"v_mfma_f32_16x16x32_bf16 v[116:119], a[64:67], v[108:111], v[116:119]\n"
"v_fmac_f32_e32 v184, v255, v196\n"
"v_fmac_f32_e32 v185, v255, v197\n"
"v_mfma_f32_16x16x32_bf16 v[216:219], a[68:71], v[108:111], v[216:219]\n"
"buffer_load_dwordx4 a[232:235], v0, s[4:7], s98 offen offset:2048 sc1\n"
"v_mfma_f32_16x16x32_bf16 v[124:127], a[72:75], v[24:27], v[124:127]\n"
"ds_write_b128 v3, v[160:163] offset:11264\n"
"v_mfma_f32_16x16x32_bf16 v[192:195], a[76:79], v[24:27], v[192:195]\n"
"v_fmac_f32_e32 v186, v255, v198\n"
"v_fmac_f32_e32 v187, v255, v199\n"
"v_mfma_f32_16x16x32_bf16 v[204:207], a[80:83], v[24:27], v[204:207]\n"
"v_fmac_f32_e32 v200, v255, v212\n"
"v_fmac_f32_e32 v201, v255, v213\n"
"v_mfma_f32_16x16x32_bf16 v[208:211], a[84:87], v[24:27], v[208:211]\n"
"buffer_load_dwordx4 a[236:239], v0, s[4:7], s98 offen offset:3072 sc1\n"
"v_mfma_f32_16x16x32_bf16 v[116:119], a[88:91], v[24:27], v[116:119]\n"
"v_fmac_f32_e32 v202, v255, v214\n"
"v_fmac_f32_e32 v203, v255, v215\n"
"v_mfma_f32_16x16x32_bf16 v[216:219], a[92:95], v[24:27], v[216:219]\n"
"s_waitcnt vmcnt(21)\n"
"ds_read_b128 v[24:27], v4 offset:5120\n"
"v_mfma_f32_16x16x32_bf16 v[124:127], a[96:99], v[76:79], v[124:127]\n"
"ds_read_b128 v[52:55], v9 offset:320\n"
"v_max_f32_e32 v253, 0, v253\n"
"v_mfma_f32_16x16x32_bf16 v[192:195], a[100:103], v[76:79], v[192:195]\n"
"buffer_load_dwordx4 a[240:243], v1, s[4:7], s98 offen sc1\n"
"v_mfma_f32_16x16x32_bf16 v[204:207], a[104:107], v[76:79], v[204:207]\n"
"v_cvt_pk_bf16_f32 v253, v253, 0\n"
"v_lshlrev_b32_e32 v253, 16, v253\n"
"v_mfma_f32_16x16x32_bf16 v[208:211], a[108:111], v[76:79], v[208:211]\n"
"v_add_f32_e32 v253, 0x3727c5ac, v253\n"
"v_cvt_pk_bf16_f32 v253, v253, 0\n"
"v_mfma_f32_16x16x32_bf16 v[116:119], a[112:115], v[76:79], v[116:119]\n"
"v_lshlrev_b32_e32 v253, 16, v253\n"
"v_mfma_f32_16x16x32_bf16 v[216:219], a[116:119], v[76:79], v[216:219]\n"
"buffer_load_dwordx4 a[244:247], v1, s[4:7], s98 offen offset:1024 sc1\n"
"v_mfma_f32_16x16x32_bf16 v[124:127], a[120:123], v[132:135], v[124:127]\n"
"s_waitcnt lgkmcnt(0)\n"
"v_fmac_f32_e32 v72, v255, v220\n"
"v_mfma_f32_16x16x32_bf16 v[192:195], a[124:127], v[132:135], v[192:195]\n"
"v_fmac_f32_e32 v73, v255, v221\n"
"v_fmac_f32_e32 v74, v255, v222\n"
"v_mfma_f32_16x16x32_bf16 v[204:207], v[40:43], v[132:135], v[204:207]\n"
"v_fmac_f32_e32 v75, v255, v223\n"
"v_fmac_f32_e32 v120, v255, v224\n"
"v_mfma_f32_16x16x32_bf16 v[208:211], v[44:47], v[132:135], v[208:211]\n"
"buffer_load_dwordx4 a[248:251], v1, s[4:7], s98 offen offset:2048 sc1\n"
"v_mfma_f32_16x16x32_bf16 v[116:119], v[80:83], v[132:135], v[116:119]\n"
"v_fmac_f32_e32 v121, v255, v225\n"
"v_fmac_f32_e32 v122, v255, v226\n"
"v_mfma_f32_16x16x32_bf16 v[216:219], v[68:71], v[132:135], v[216:219]\n"
"s_waitcnt vmcnt(20)\n"
"v_fmac_f32_e32 v123, v255, v227\n"
"v_mfma_f32_16x16x32_bf16 v[124:127], v[88:91], v[100:103], v[124:127]\n"
"v_fmac_f32_e32 v24, v255, v52\n"
"v_fmac_f32_e32 v25, v255, v53\n"
"v_mfma_f32_16x16x32_bf16 v[192:195], v[48:51], v[100:103], v[192:195]\n"
"buffer_load_dwordx4 a[252:255], v1, s[4:7], s98 offen offset:3072 sc1\n"
"v_mfma_f32_16x16x32_bf16 v[204:207], v[92:95], v[100:103], v[204:207]\n"
"v_fmac_f32_e32 v26, v255, v54\n"
"v_fmac_f32_e32 v27, v255, v55\n"
"v_mfma_f32_16x16x32_bf16 v[208:211], v[96:99], v[100:103], v[208:211]\n"
"v_sqrt_f32_e32 v253, v253\n"
"v_mfma_f32_16x16x32_bf16 v[116:119], v[168:171], v[100:103], v[116:119]\n"
"v_cvt_pk_bf16_f32 v253, v253, 0\n"
"v_lshlrev_b32_e32 v253, 16, v253\n"
"v_mfma_f32_16x16x32_bf16 v[216:219], v[104:107], v[100:103], v[216:219]\n"
"v_rcp_f32_e32 v255, v253\n"
"v_mfma_f32_16x16x32_bf16 v[124:127], v[136:139], v[112:115], v[124:127]\n"
"v_mul_f32_e32 v254, 0x3e8293ee, v255\n"
"v_mfma_f32_16x16x32_bf16 v[192:195], v[180:183], v[112:115], v[192:195]\n"
"v_mfma_f32_16x16x32_bf16 v[204:207], v[28:31], v[112:115], v[204:207]\n"
"s_waitcnt vmcnt(11)\n"
"v_cmp_ne_u64_e32 vcc, v[250:251], v[18:19]\n"
"v_mfma_f32_16x16x32_bf16 v[208:211], v[32:35], v[112:115], v[208:211]\n"
"s_cbranch_vccnz .Lmk_f86s\n"
".Lmk_f86r:\n"
"s_add_u32 m0, s77, 0x6000\n"
"v_mfma_f32_16x16x32_bf16 v[116:119], v[60:63], v[112:115], v[116:119]\n"
"s_add_u32 s98, s65, 0x2000\n"
"v_mfma_f32_16x16x32_bf16 v[216:219], v[64:67], v[112:115], v[216:219]\n"
"; @A G1 3.3\n"
"v_mfma_f32_16x16x32_bf16 v[148:151], a[0:3], v[36:39], v[148:151]\n"
"buffer_load_dwordx4 v2, s[4:7], s98 offen sc1 lds\n"
"v_mfma_f32_16x16x32_bf16 v[184:187], a[4:7], v[36:39], v[184:187]\n"
"buffer_load_dwordx4 v2, s[4:7], s98 offen offset:1024 sc1 lds\n"
"v_mfma_f32_16x16x32_bf16 v[200:203], a[8:11], v[36:39], v[200:203]\n"
"v_xor_b32_e32 v253, 32, v16\n"
"v_xor_b32_e32 v252, 32, v15\n"
"v_mfma_f32_16x16x32_bf16 v[72:75], a[12:15], v[36:39], v[72:75]\n"
"ds_write_b128 v3, v[124:127] offset:12288\n"
"v_mfma_f32_16x16x32_bf16 v[120:123], a[16:19], v[36:39], v[120:123]\n"
"v_mov_b32_e32 v100, 0x3f803f80\n"
"v_mov_b32_e32 v101, 0x3f803f80\n"
"v_mfma_f32_16x16x32_bf16 v[24:27], a[20:23], v[36:39], v[24:27]\n"
"v_mov_b32_e32 v102, 0x3f803f80\n"
"v_mov_b32_e32 v103, 0x3f803f80\n"
"v_mfma_f32_16x16x32_bf16 v[148:151], a[24:27], v[128:131], v[148:151]\n"
"ds_write_b128 v3, v[192:195] offset:13312\n"
"v_mfma_f32_16x16x32_bf16 v[184:187], a[28:31], v[128:131], v[184:187]\n"
"s_add_u32 m0, s77, 0xe000\n"
"s_add_u32 s98, s65, 0x6000\n"
"v_mfma_f32_16x16x32_bf16 v[200:203], a[32:35], v[128:131], v[200:203]\n"
"buffer_load_dwordx4 v2, s[4:7], s98 offen sc1 lds\n"
"v_mfma_f32_16x16x32_bf16 v[72:75], a[36:39], v[128:131], v[72:75]\n"
"ds_write_b128 v3, v[204:207] offset:14336\n"
"v_mfma_f32_16x16x32_bf16 v[120:123], a[40:43], v[128:131], v[120:123]\n"
"buffer_load_dwordx4 v2, s[4:7], s98 offen offset:1024 sc1 lds\n"
"v_mfma_f32_16x16x32_bf16 v[24:27], a[44:47], v[128:131], v[24:27]\n"
"v_mov_b32_e32 v251, 0x53800000\n"
"v_mfma_f32_16x16x32_bf16 v[148:151], a[48:51], v[84:87], v[148:151]\n"
"ds_write_b128 v3, v[208:211] offset:15360\n"
"v_mfma_f32_16x16x32_bf16 v[184:187], a[52:55], v[84:87], v[184:187]\n"
"v_mfma_f32_16x16x32_bf16 v[200:203], a[56:59], v[84:87], v[200:203]\n"
"v_mfma_f32_16x16x32_bf16 v[72:75], a[60:63], v[84:87], v[72:75]\n"
"ds_write_b128 v3, v[116:119] offset:16384\n"
"v_mfma_f32_16x16x32_bf16 v[120:123], a[64:67], v[84:87], v[120:123]\n"
"s_add_u32 s98, s69, 0x600000\n"
"v_mfma_f32_16x16x32_bf16 v[24:27], a[68:71], v[84:87], v[24:27]\n"
"buffer_load_dwordx4 a[0:3], v0, s[4:7], s98 offen sc1\n"
"v_mfma_f32_16x16x32_bf16 v[148:151], a[72:75], v[56:59], v[148:151]\n"
"ds_write_b128 v3, v[216:219] offset:17408\n"
"v_mfma_f32_16x16x32_bf16 v[184:187], a[76:79], v[56:59], v[184:187]\n"
"v_mfma_f32_16x16x32_bf16 v[200:203], a[80:83], v[56:59], v[200:203]\n"
"v_mfma_f32_16x16x32_bf16 v[72:75], a[84:87], v[56:59], v[72:75]\n"
"s_waitcnt lgkmcnt(0)\n"
"s_barrier\n"
"v_mfma_f32_16x16x32_bf16 v[120:123], a[88:91], v[56:59], v[120:123]\n"
"buffer_load_dwordx4 a[4:7], v0, s[4:7], s98 offen offset:1024 sc1\n"
"v_mfma_f32_16x16x32_bf16 v[24:27], a[92:95], v[56:59], v[24:27]\n"
"ds_read_b128 v[36:39], v5 offset:2048\n"
"v_mfma_f32_16x16x32_bf16 v[148:151], a[96:99], v[144:147], v[148:151]\n"
"ds_read_b128 v[52:55], v6 offset:2048\n"
"ds_read_b128 v[56:59], v5 offset:3072\n"
"v_mfma_f32_16x16x32_bf16 v[184:187], a[100:103], v[144:147], v[184:187]\n"
"ds_read_b128 v[76:79], v6 offset:3072\n"
"v_mfma_f32_16x16x32_bf16 v[200:203], a[104:107], v[144:147], v[200:203]\n"
"v_mfma_f32_16x16x32_bf16 v[72:75], a[108:111], v[144:147], v[72:75]\n"
"buffer_load_dwordx4 a[8:11], v0, s[4:7], s98 offen offset:2048 sc1\n"
"v_mfma_f32_16x16x32_bf16 v[120:123], a[112:115], v[144:147], v[120:123]\n"
"ds_read_b128 v[84:87], v5 offset:4096\n"
"ds_read_b128 v[108:111], v6 offset:4096\n"
"v_mfma_f32_16x16x32_bf16 v[24:27], a[116:119], v[144:147], v[24:27]\n"
"ds_read_b128 v[112:115], v5 offset:5120\n"
"ds_read_b128 v[116:119], v6 offset:5120\n"
"v_mfma_f32_16x16x32_bf16 v[148:151], a[120:123], v[164:167], v[148:151]\n"
"s_waitcnt lgkmcnt(4)\n"
"v_add_f32_e32 v36, v36, v52\n"
"v_mfma_f32_16x16x32_bf16 v[184:187], a[124:127], v[164:167], v[184:187]\n"
"v_add_f32_e32 v37, v37, v53\n"
"v_add_f32_e32 v38, v38, v54\n"
"v_mfma_f32_16x16x32_bf16 v[200:203], v[40:43], v[164:167], v[200:203]\n"
"buffer_load_dwordx4 a[12:15], v0, s[4:7], s98 offen offset:3072 sc1\n"
"v_mfma_f32_16x16x32_bf16 v[72:75], v[44:47], v[164:167], v[72:75]\n"
"ds_read_b128 v[40:43], v5\n"
"ds_read_b128 v[44:47], v6\n"
"v_mfma_f32_16x16x32_bf16 v[120:123], v[80:83], v[164:167], v[120:123]\n"
"ds_read_b128 v[80:83], v5 offset:1024\n"
"ds_read_b128 v[124:127], v6 offset:1024\n"
"v_mfma_f32_16x16x32_bf16 v[24:27], v[68:71], v[164:167], v[24:27]\n"
"s_waitcnt vmcnt(18)\n"
"v_add_f32_e32 v39, v39, v55\n"
"v_mfma_f32_16x16x32_bf16 v[148:151], v[88:91], v[172:175], v[148:151]\n"
"v_add_f32_e32 v56, v56, v76\n"
"v_add_f32_e32 v57, v57, v77\n"
"v_mfma_f32_16x16x32_bf16 v[184:187], v[48:51], v[172:175], v[184:187]\n"
"buffer_load_dwordx4 a[16:19], v1, s[4:7], s98 offen sc1\n"
"v_mfma_f32_16x16x32_bf16 v[200:203], v[92:95], v[172:175], v[200:203]\n"
"v_add_f32_e32 v58, v58, v78\n"
"v_add_f32_e32 v59, v59, v79\n"
"v_mfma_f32_16x16x32_bf16 v[72:75], v[96:99], v[172:175], v[72:75]\n"
"s_waitcnt lgkmcnt(4)\n"
"v_add_f32_e32 v84, v84, v108\n"
"v_mfma_f32_16x16x32_bf16 v[120:123], v[168:171], v[172:175], v[120:123]\n"
"v_add_f32_e32 v85, v85, v109\n"
"v_add_f32_e32 v86, v86, v110\n"
"v_mfma_f32_16x16x32_bf16 v[24:27], v[104:107], v[172:175], v[24:27]\n"
"v_add_f32_e32 v87, v87, v111\n"
"v_add_f32_e32 v112, v112, v116\n"
"v_mfma_f32_16x16x32_bf16 v[148:151], v[136:139], v[188:191], v[148:151]\n"
"buffer_load_dwordx4 a[20:23], v1, s[4:7], s98 offen offset:1024 sc1\n"
"v_mfma_f32_16x16x32_bf16 v[184:187], v[180:183], v[188:191], v[184:187]\n"
"v_add_f32_e32 v113, v113, v117\n"
"v_add_f32_e32 v114, v114, v118\n"
"v_mfma_f32_16x16x32_bf16 v[200:203], v[28:31], v[188:191], v[200:203]\n"
"v_add_f32_e32 v115, v115, v119\n"
"s_waitcnt lgkmcnt(0)\n"
"v_mfma_f32_16x16x32_bf16 v[72:75], v[32:35], v[188:191], v[72:75]\n"
"v_add_f32_e32 v40, v40, v44\n"
"v_add_f32_e32 v41, v41, v45\n"
"v_mfma_f32_16x16x32_bf16 v[120:123], v[60:63], v[188:191], v[120:123]\n"
"v_add_f32_e32 v42, v42, v46\n"
"v_add_f32_e32 v43, v43, v47\n"
"v_mfma_f32_16x16x32_bf16 v[24:27], v[64:67], v[188:191], v[24:27]\n"
"buffer_load_dwordx4 a[24:27], v1, s[4:7], s98 offen offset:2048 sc1\n"
"v_add_f32_e32 v80, v80, v124\n"
"v_add_f32_e32 v81, v81, v125\n"
"v_add_f32_e32 v82, v82, v126\n"
"v_add_f32_e32 v83, v83, v127\n"
"buffer_load_dwordx4 a[28:31], v1, s[4:7], s98 offen offset:3072 sc1\n"
".Lmk_a4:\n"
"; @A tail\n"
"v_add_f32_e32 v200, v200, v36\n"
"v_add_f32_e32 v201, v201, v37\n"
"v_add_f32_e32 v202, v202, v38\n"
"v_add_f32_e32 v203, v203, v39\n"
"v_add_f32_e32 v72, v72, v56\n"
"v_add_f32_e32 v73, v73, v57\n"
"v_add_f32_e32 v74, v74, v58\n"
"v_add_f32_e32 v75, v75, v59\n"
"v_mul_f32_e32 v200, v200, v255\n"
"v_mul_f32_e32 v201, v201, v255\n"
"v_mul_f32_e32 v202, v202, v255\n"
"v_mul_f32_e32 v203, v203, v255\n"
"v_mul_f32_e32 v72, v72, v255\n"
"v_mul_f32_e32 v73, v73, v255\n"
"v_mul_f32_e32 v74, v74, v255\n"
"v_mul_f32_e32 v75, v75, v255\n"
"v_cvt_pk_bf16_f32 v28, v200, v201\n"
"v_cvt_pk_bf16_f32 v29, v202, v203\n"
"v_cvt_pk_bf16_f32 v30, v72, v73\n"
"v_cvt_pk_bf16_f32 v31, v74, v75\n"
"ds_write_b128 v7, v[28:31] offset:16384\n"
"buffer_store_dwordx4 v[28:31], v7, s[4:7], s64 offen sc1\n"
"v_add_f32_e32 v120, v120, v84\n"
"v_add_f32_e32 v121, v121, v85\n"
"v_add_f32_e32 v122, v122, v86\n"
"v_add_f32_e32 v123, v123, v87\n"
"v_add_f32_e32 v24, v24, v112\n"
"v_add_f32_e32 v25, v25, v113\n"
"v_add_f32_e32 v26, v26, v114\n"
"v_add_f32_e32 v27, v27, v115\n"
"v_mul_f32_e32 v120, v120, v255\n"
"v_mul_f32_e32 v121, v121, v255\n"
"v_mul_f32_e32 v122, v122, v255\n"
"v_mul_f32_e32 v123, v123, v255\n"
"v_mul_f32_e32 v24, v24, v255\n"
"v_mul_f32_e32 v25, v25, v255\n"
"v_mul_f32_e32 v26, v26, v255\n"
"v_mul_f32_e32 v27, v27, v255\n"
"v_cvt_pk_bf16_f32 v28, v120, v121\n"
"v_cvt_pk_bf16_f32 v29, v122, v123\n"
"v_cvt_pk_bf16_f32 v30, v24, v25\n"
"v_cvt_pk_bf16_f32 v31, v26, v27\n"
"ds_write_b128 v7, v[28:31] offset:49152\n"
"s_add_u32 s98, s64, 0x4000\n"
"buffer_store_dwordx4 v[28:31], v7, s[4:7], s98 offen sc1\n"
"v_add_f32_e32 v148, v148, v40\n"
"v_add_f32_e32 v149, v149, v41\n"
"v_add_f32_e32 v150, v150, v42\n"
"v_add_f32_e32 v151, v151, v43\n"
"v_add_f32_e32 v184, v184, v80\n"
"v_add_f32_e32 v185, v185, v81\n"
"v_add_f32_e32 v186, v186, v82\n"
"v_add_f32_e32 v187, v187, v83\n"
"v_mul_f32_e32 v148, v148, v254\n"
"v_mul_f32_e32 v149, v149, v254\n"
"v_mul_f32_e32 v150, v150, v254\n"
"v_mul_f32_e32 v151, v151, v254\n"
"v_mul_f32_e32 v184, v184, v254\n"
"v_mul_f32_e32 v185, v185, v254\n"
"v_mul_f32_e32 v186, v186, v254\n"
"v_mul_f32_e32 v187, v187, v254\n"
"v_cvt_pk_bf16_f32 v24, v148, v149\n"
"v_cvt_pk_bf16_f32 v25, v150, v151\n"
"v_cvt_pk_bf16_f32 v26, v184, v185\n"
"v_cvt_pk_bf16_f32 v27, v186, v187\n"
"ds_write_b128 v2, v[24:27] offset:16384\n"
"s_waitcnt lgkmcnt(0)\n"
"s_barrier\n"
"ds_read_b128 v[28:31], v2 offset:49152\n"
"ds_read_b128 v[32:35], v2 offset:50176\n"
"ds_read_b128 v[88:91], v2 offset:16384\n"
"ds_read_b128 v[92:95], v2 offset:17408\n"
"; @A AT b\n"
"s_mov_b32 s2, 0\n"
"v_mov_b32_e32 v255, 0xff800000\n"
"v_mov_b32_e32 v254, 0xff800000\n"
"v_mov_b32_e32 v250, 0x7f800000\n"
"v_mov_b32_e32 v249, 0x7f800000\n"
".Lmk_a106:\n"
"ds_read_b128 v[24:27], v15 offset:24576\n"
"ds_read_b128 v[36:39], v252 offset:24832\n"
"ds_read_b128 v[40:43], v15 offset:22528\n"
"ds_read_b128 v[44:47], v252 offset:22784\n"
"s_cmp_gt_i32 s78, 11\n"
"s_cselect_b32 s98, 0x80000000, 0\n"
"s_or_b32 s98, s98, 0x7f800000\n"
"s_waitcnt lgkmcnt(2)\n"
"v_mfma_f32_16x16x32_bf16 v[48:51], v[24:27], v[28:31], 0\n"
"v_mfma_f32_16x16x32_bf16 v[52:55], v[24:27], v[32:35], 0\n"
"v_mfma_f32_16x16x32_bf16 v[24:27], v[36:39], v[28:31], 0\n"
"v_mfma_f32_16x16x32_bf16 v[56:59], v[36:39], v[32:35], 0\n"
"s_nop 4\n"
"v_max_f32_e32 v248, v48, v49\n"
"v_max_f32_e32 v247, v52, v53\n"
"v_max3_f32 v248, v248, v50, v51\n"
"v_max3_f32 v247, v247, v54, v55\n"
"v_max3_f32 v248, v248, v24, v25\n"
"v_max3_f32 v247, v247, v56, v57\n"
"v_max3_f32 v248, v248, v26, v27\n"
"v_max3_f32 v247, v247, v58, v59\n"
"v_mov_b32_e32 v246, v248\n"
"v_mov_b32_e32 v245, v247\n"
"s_nop 0\n"
"v_permlane16_swap_b32_e32 v248, v246\n"
"v_permlane16_swap_b32_e32 v247, v245\n"
"v_max_f32_e32 v248, v248, v246\n"
"v_max_f32_e32 v247, v247, v245\n"
"v_mov_b32_e32 v246, v248\n"
"v_mov_b32_e32 v245, v247\n"
"s_nop 0\n"
"v_permlane32_swap_b32_e32 v248, v246\n"
"v_permlane32_swap_b32_e32 v247, v245\n"
"v_max_f32_e32 v248, v248, v246\n"
"v_max_f32_e32 v247, v247, v245\n"
"v_med3_f32 v248, v248, v255, v250\n"
"v_med3_f32 v247, v247, v254, v249\n"
"v_min_f32_e64 v36, s98, -v248\n"
"v_min_f32_e64 v37, s98, -v248\n"
"v_min_f32_e64 v38, s98, -v248\n"
"v_min_f32_e64 v39, s98, -v248\n"
"v_min_f32_e64 v60, s98, -v247\n"
"v_min_f32_e64 v61, s98, -v247\n"
"v_min_f32_e64 v62, s98, -v247\n"
"v_min_f32_e64 v63, s98, -v247\n"
"s_waitcnt lgkmcnt(0)\n"
"v_mfma_f32_16x16x32_bf16 v[64:67], v[40:43], v[28:31], v[36:39]\n"
"ds_read_b128 v[68:71], v15 offset:20480\n"
"v_sub_f32_e32 v48, v48, v248\n"
"v_sub_f32_e32 v49, v49, v248\n"
"v_sub_f32_e32 v50, v50, v248\n"
"s_add_u32 s98, s69, 0x602000\n"
"buffer_load_dwordx4 a[32:35], v0, s[4:7], s98 offen sc1\n"
"v_mfma_f32_16x16x32_bf16 v[72:75], v[40:43], v[32:35], v[60:63]\n"
"v_sub_f32_e32 v51, v51, v248\n"
"v_sub_f32_e32 v24, v24, v248\n"
"v_sub_f32_e32 v25, v25, v248\n"
"v_sub_f32_e32 v26, v26, v248\n"
"v_sub_f32_e32 v27, v27, v248\n"
"buffer_load_dwordx4 a[36:39], v0, s[4:7], s98 offen offset:1024 sc1\n"
"v_mfma_f32_16x16x32_bf16 v[40:43], v[44:47], v[28:31], v[36:39]\n"
"ds_read_b128 v[76:79], v252 offset:20736\n"
"v_sub_f32_e32 v52, v52, v247\n"
"v_sub_f32_e32 v53, v53, v247\n"
"v_sub_f32_e32 v54, v54, v247\n"
"v_sub_f32_e32 v55, v55, v247\n"
"v_mfma_f32_16x16x32_bf16 v[80:83], v[44:47], v[32:35], v[60:63]\n"
"buffer_load_dwordx4 a[40:43], v0, s[4:7], s98 offen offset:2048 sc1\n"
"v_sub_f32_e32 v56, v56, v247\n"
"v_sub_f32_e32 v57, v57, v247\n"
"v_sub_f32_e32 v58, v58, v247\n"
"v_sub_f32_e32 v59, v59, v247\n"
"s_waitcnt lgkmcnt(0)\n"
"v_mfma_f32_16x16x32_bf16 v[44:47], v[68:71], v[28:31], v[36:39]\n"
"ds_read_b128 v[84:87], v15 offset:18432\n"
"v_exp_f32_e32 v64, v64\n"
"v_exp_f32_e32 v65, v65\n"
"v_exp_f32_e32 v66, v66\n"
"buffer_load_dwordx4 a[44:47], v0, s[4:7], s98 offen offset:3072 sc1\n"
"v_exp_f32_e32 v67, v67\n"
"ds_read_b128 v[96:99], v252 offset:18688\n"
"v_exp_f32_e32 v40, v40\n"
"v_mfma_f32_16x16x32_bf16 v[104:107], v[68:71], v[32:35], v[60:63]\n"
"v_exp_f32_e32 v41, v41\n"
"v_exp_f32_e32 v42, v42\n"
"ds_read_b64_tr_b16 v[68:69], v16 offset:22528\n"
"v_exp_f32_e32 v43, v43\n"
"v_cvt_pk_bf16_f32 v108, v64, v65\n"
"buffer_load_dwordx4 a[48:51], v1, s[4:7], s98 offen sc1\n"
"v_cvt_pk_bf16_f32 v109, v66, v67\n"
"v_cvt_pk_bf16_f32 v110, v40, v41\n"
"v_mfma_f32_16x16x32_bf16 v[64:67], v[76:79], v[28:31], v[36:39]\n"
"v_cvt_pk_bf16_f32 v111, v42, v43\n"
"ds_read_b64_tr_b16 v[70:71], v253 offset:22784\n"
"v_exp_f32_e32 v72, v72\n"
"v_exp_f32_e32 v73, v73\n"
"v_exp_f32_e32 v74, v74\n"
"v_exp_f32_e32 v75, v75\n"
"ds_read_b64_tr_b16 v[40:41], v253 offset:22528\n"
"buffer_load_dwordx4 a[52:55], v1, s[4:7], s98 offen offset:1024 sc1\n"
"v_mfma_f32_16x16x32_bf16 v[112:115], v[76:79], v[32:35], v[60:63]\n"
"v_exp_f32_e32 v80, v80\n"
"v_exp_f32_e32 v81, v81\n"
"v_exp_f32_e32 v82, v82\n"
"ds_read_b64_tr_b16 v[42:43], v16 offset:22784\n"
"v_exp_f32_e32 v83, v83\n"
"v_cvt_pk_bf16_f32 v76, v72, v73\n"
"v_cvt_pk_bf16_f32 v77, v74, v75\n"
"v_cvt_pk_bf16_f32 v78, v80, v81\n"
"v_cvt_pk_bf16_f32 v79, v82, v83\n"
"s_waitcnt lgkmcnt(4)\n"
"v_mfma_f32_16x16x32_bf16 v[72:75], v[84:87], v[28:31], v[36:39]\n"
"ds_read_b128 v[80:83], v15 offset:16384\n"
"v_exp_f32_e32 v44, v44\n"
"v_exp_f32_e32 v45, v45\n"
"v_exp_f32_e32 v46, v46\n"
"v_mfma_f32_16x16x32_bf16 v[116:119], v[84:87], v[32:35], v[60:63]\n"
"buffer_load_dwordx4 a[56:59], v1, s[4:7], s98 offen offset:2048 sc1\n"
"v_exp_f32_e32 v47, v47\n"
"v_mfma_f32_16x16x32_bf16 v[84:87], v[96:99], v[28:31], v[36:39]\n"
"ds_read_b128 v[120:123], v252 offset:16640\n"
"v_exp_f32_e32 v64, v64\n"
"v_exp_f32_e32 v65, v65\n"
"v_mfma_f32_16x16x32_bf16 v[124:127], v[96:99], v[32:35], v[60:63]\n"
"v_exp_f32_e32 v66, v66\n"
"ds_read_b64_tr_b16 v[96:97], v16 offset:20480\n"
"v_exp_f32_e32 v67, v67\n"
"v_cvt_pk_bf16_f32 v128, v44, v45\n"
"s_waitcnt lgkmcnt(3)\n"
"v_mfma_f32_16x16x32_bf16 v[132:135], v[100:103], v[108:111], 0\n"
"buffer_load_dwordx4 a[60:63], v1, s[4:7], s98 offen offset:3072 sc1\n"
"v_cvt_pk_bf16_f32 v129, v46, v47\n"
"v_cvt_pk_bf16_f32 v130, v64, v65\n"
"v_cvt_pk_bf16_f32 v131, v66, v67\n"
"v_mfma_f32_16x16x32_bf16 v[44:47], v[68:71], v[108:111], 0\n"
"ds_read_b64_tr_b16 v[98:99], v253 offset:20736\n"
"v_exp_f32_e32 v104, v104\n"
"v_exp_f32_e32 v105, v105\n"
"v_mfma_f32_16x16x32_bf16 v[64:67], v[40:43], v[108:111], 0\n"
"v_exp_f32_e32 v106, v106\n"
"v_exp_f32_e32 v107, v107\n"
"ds_read_b64_tr_b16 v[108:109], v253 offset:20480\n"
"s_add_u32 s98, s69, 0x604000\n"
"buffer_load_dwordx4 a[64:67], v0, s[4:7], s98 offen sc1\n"
"v_mfma_f32_16x16x32_bf16 v[136:139], v[100:103], v[76:79], 0\n"
"v_exp_f32_e32 v112, v112\n"
"v_exp_f32_e32 v113, v113\n"
"v_mfma_f32_16x16x32_bf16 v[140:143], v[68:71], v[76:79], 0\n"
"v_exp_f32_e32 v114, v114\n"
"ds_read_b64_tr_b16 v[110:111], v16 offset:20736\n"
"v_exp_f32_e32 v115, v115\n"
"v_mfma_f32_16x16x32_bf16 v[68:71], v[40:43], v[76:79], 0\n"
"v_cvt_pk_bf16_f32 v40, v104, v105\n"
"v_cvt_pk_bf16_f32 v41, v106, v107\n"
"v_cvt_pk_bf16_f32 v42, v112, v113\n"
"v_cvt_pk_bf16_f32 v43, v114, v115\n"
"s_waitcnt vmcnt(19)\n"
"s_cmp_lg_u32 s2, 0\n"
"s_cbranch_scc1 .Lmk_s108\n"
"s_barrier\n"
".Lmk_s108:\n"
"s_waitcnt lgkmcnt(4)\n"
"v_mfma_f32_16x16x32_bf16 v[76:79], v[80:83], v[28:31], v[36:39]\n"
"ds_read_b128 v[104:107], v15 offset:14336\n"
"v_exp_f32_e32 v72, v72\n"
"v_exp_f32_e32 v73, v73\n"
"v_exp_f32_e32 v74, v74\n"
"v_mfma_f32_16x16x32_bf16 v[112:115], v[80:83], v[32:35], v[60:63]\n"
"buffer_load_dwordx4 a[68:71], v0, s[4:7], s98 offen offset:1024 sc1\n"
"v_exp_f32_e32 v75, v75\n"
"v_mfma_f32_16x16x32_bf16 v[80:83], v[120:123], v[28:31], v[36:39]\n"
"ds_read_b128 v[144:147], v252 offset:14592\n"
"v_exp_f32_e32 v84, v84\n"
"v_exp_f32_e32 v85, v85\n"
"v_mfma_f32_16x16x32_bf16 v[148:151], v[120:123], v[32:35], v[60:63]\n"
"v_exp_f32_e32 v86, v86\n"
"ds_read_b64_tr_b16 v[120:121], v16 offset:18432\n"
"v_exp_f32_e32 v87, v87\n"
"s_waitcnt lgkmcnt(3)\n"
"v_mfma_f32_16x16x32_bf16 v[132:135], v[100:103], v[128:131], v[132:135]\n"
"v_cvt_pk_bf16_f32 v152, v72, v73\n"
"buffer_load_dwordx4 a[72:75], v0, s[4:7], s98 offen offset:2048 sc1\n"
"v_cvt_pk_bf16_f32 v153, v74, v75\n"
"v_cvt_pk_bf16_f32 v154, v84, v85\n"
"v_mfma_f32_16x16x32_bf16 v[44:47], v[96:99], v[128:131], v[44:47]\n"
"v_cvt_pk_bf16_f32 v155, v86, v87\n"
"ds_read_b64_tr_b16 v[122:123], v253 offset:18688\n"
"v_exp_f32_e32 v116, v116\n"
"v_exp_f32_e32 v117, v117\n"
"v_mfma_f32_16x16x32_bf16 v[64:67], v[108:111], v[128:131], v[64:67]\n"
"v_exp_f32_e32 v118, v118\n"
"v_exp_f32_e32 v119, v119\n"
"ds_read_b64_tr_b16 v[72:73], v253 offset:18432\n"
"v_mfma_f32_16x16x32_bf16 v[136:139], v[100:103], v[40:43], v[136:139]\n"
"buffer_load_dwordx4 a[76:79], v0, s[4:7], s98 offen offset:3072 sc1\n"
"v_exp_f32_e32 v124, v124\n"
"v_exp_f32_e32 v125, v125\n"
"v_mfma_f32_16x16x32_bf16 v[140:143], v[96:99], v[40:43], v[140:143]\n"
"v_exp_f32_e32 v126, v126\n"
"ds_read_b64_tr_b16 v[74:75], v16 offset:18688\n"
"v_exp_f32_e32 v127, v127\n"
"v_mfma_f32_16x16x32_bf16 v[68:71], v[108:111], v[40:43], v[68:71]\n"
"v_cvt_pk_bf16_f32 v40, v116, v117\n"
"v_cvt_pk_bf16_f32 v41, v118, v119\n"
"v_cvt_pk_bf16_f32 v42, v124, v125\n"
"v_cvt_pk_bf16_f32 v43, v126, v127\n"
"s_waitcnt lgkmcnt(4)\n"
"s_cmp_eq_i32 s78, 8\n"
"s_cbranch_scc1 .Lmk_x109s\n"
".Lmk_x109r:\n"
"v_mfma_f32_16x16x32_bf16 v[84:87], v[104:107], v[28:31], v[36:39]\n"
"ds_read_b128 v[96:99], v15 offset:12288\n"
"v_exp_f32_e32 v76, v76\n"
"v_exp_f32_e32 v77, v77\n"
"v_exp_f32_e32 v78, v78\n"
"v_mfma_f32_16x16x32_bf16 v[108:111], v[104:107], v[32:35], v[60:63]\n"
"buffer_load_dwordx4 a[80:83], v1, s[4:7], s98 offen sc1\n"
"v_exp_f32_e32 v79, v79\n"
"v_mfma_f32_16x16x32_bf16 v[104:107], v[144:147], v[28:31], v[36:39]\n"
"ds_read_b128 v[116:119], v252 offset:12544\n"
"v_exp_f32_e32 v80, v80\n"
"v_exp_f32_e32 v81, v81\n"
"v_mfma_f32_16x16x32_bf16 v[124:127], v[144:147], v[32:35], v[60:63]\n"
"v_exp_f32_e32 v82, v82\n"
"ds_read_b64_tr_b16 v[128:129], v16 offset:16384\n"
"v_exp_f32_e32 v83, v83\n"
"s_waitcnt lgkmcnt(3)\n"
"v_mfma_f32_16x16x32_bf16 v[132:135], v[100:103], v[152:155], v[132:135]\n"
"v_cvt_pk_bf16_f32 v144, v76, v77\n"
"buffer_load_dwordx4 a[84:87], v1, s[4:7], s98 offen offset:1024 sc1\n"
"v_cvt_pk_bf16_f32 v145, v78, v79\n"
"v_cvt_pk_bf16_f32 v146, v80, v81\n"
"v_mfma_f32_16x16x32_bf16 v[44:47], v[120:123], v[152:155], v[44:47]\n"
"v_cvt_pk_bf16_f32 v147, v82, v83\n"
"ds_read_b64_tr_b16 v[130:131], v253 offset:16640\n"
"v_exp_f32_e32 v112, v112\n"
"v_exp_f32_e32 v113, v113\n"
"v_mfma_f32_16x16x32_bf16 v[64:67], v[72:75], v[152:155], v[64:67]\n"
"v_exp_f32_e32 v114, v114\n"
"v_exp_f32_e32 v115, v115\n"
"ds_read_b64_tr_b16 v[76:77], v253 offset:16384\n"
"v_mfma_f32_16x16x32_bf16 v[136:139], v[100:103], v[40:43], v[136:139]\n"
"buffer_load_dwordx4 a[88:91], v1, s[4:7], s98 offen offset:2048 sc1\n"
"v_exp_f32_e32 v148, v148\n"
"v_exp_f32_e32 v149, v149\n"
"v_mfma_f32_16x16x32_bf16 v[140:143], v[120:123], v[40:43], v[140:143]\n"
"v_exp_f32_e32 v150, v150\n"
"ds_read_b64_tr_b16 v[78:79], v16 offset:16640\n"
"v_exp_f32_e32 v151, v151\n"
"v_mfma_f32_16x16x32_bf16 v[68:71], v[72:75], v[40:43], v[68:71]\n"
"v_cvt_pk_bf16_f32 v40, v112, v113\n"
"v_cvt_pk_bf16_f32 v41, v114, v115\n"
"v_cvt_pk_bf16_f32 v42, v148, v149\n"
"v_cvt_pk_bf16_f32 v43, v150, v151\n"
"s_waitcnt lgkmcnt(4)\n"
"s_cmp_eq_i32 s78, 7\n"
"s_cbranch_scc1 .Lmk_x110s\n"
".Lmk_x110r:\n"
"v_mfma_f32_16x16x32_bf16 v[72:75], v[96:99], v[28:31], v[36:39]\n"
"ds_read_b128 v[80:83], v15 offset:10240\n"
"v_exp_f32_e32 v84, v84\n"
"v_exp_f32_e32 v85, v85\n"
"v_exp_f32_e32 v86, v86\n"
"v_mfma_f32_16x16x32_bf16 v[112:115], v[96:99], v[32:35], v[60:63]\n"
"buffer_load_dwordx4 a[92:95], v1, s[4:7], s98 offen offset:3072 sc1\n"
"v_exp_f32_e32 v87, v87\n"
"v_mfma_f32_16x16x32_bf16 v[96:99], v[116:119], v[28:31], v[36:39]\n"
"ds_read_b128 v[120:123], v252 offset:10496\n"
"v_exp_f32_e32 v104, v104\n"
"v_exp_f32_e32 v105, v105\n"
"v_mfma_f32_16x16x32_bf16 v[148:151], v[116:119], v[32:35], v[60:63]\n"
"v_exp_f32_e32 v106, v106\n"
"ds_read_b64_tr_b16 v[116:117], v16 offset:14336\n"
"v_exp_f32_e32 v107, v107\n"
"v_cvt_pk_bf16_f32 v152, v84, v85\n"
"s_waitcnt lgkmcnt(3)\n"
"v_mfma_f32_16x16x32_bf16 v[132:135], v[100:103], v[144:147], v[132:135]\n"
"s_add_u32 s98, s69, 0x606000\n"
"buffer_load_dwordx4 a[96:99], v0, s[4:7], s98 offen sc1\n"
"v_cvt_pk_bf16_f32 v153, v86, v87\n"
"v_cvt_pk_bf16_f32 v154, v104, v105\n"
"v_mfma_f32_16x16x32_bf16 v[44:47], v[128:131], v[144:147], v[44:47]\n"
"v_cvt_pk_bf16_f32 v155, v106, v107\n"
"ds_read_b64_tr_b16 v[118:119], v253 offset:14592\n"
"v_exp_f32_e32 v108, v108\n"
"v_exp_f32_e32 v109, v109\n"
"v_mfma_f32_16x16x32_bf16 v[64:67], v[76:79], v[144:147], v[64:67]\n"
"v_exp_f32_e32 v110, v110\n"
"v_exp_f32_e32 v111, v111\n"
"ds_read_b64_tr_b16 v[84:85], v253 offset:14336\n"
"v_mfma_f32_16x16x32_bf16 v[136:139], v[100:103], v[40:43], v[136:139]\n"
"buffer_load_dwordx4 a[100:103], v0, s[4:7], s98 offen offset:1024 sc1\n"
"v_exp_f32_e32 v124, v124\n"
"v_exp_f32_e32 v125, v125\n"
"v_mfma_f32_16x16x32_bf16 v[140:143], v[128:131], v[40:43], v[140:143]\n"
"v_exp_f32_e32 v126, v126\n"
"ds_read_b64_tr_b16 v[86:87], v16 offset:14592\n"
"v_exp_f32_e32 v127, v127\n"
"v_mfma_f32_16x16x32_bf16 v[68:71], v[76:79], v[40:43], v[68:71]\n"
"v_cvt_pk_bf16_f32 v40, v108, v109\n"
"v_cvt_pk_bf16_f32 v41, v110, v111\n"
"v_cvt_pk_bf16_f32 v42, v124, v125\n"
"v_cvt_pk_bf16_f32 v43, v126, v127\n"
"s_waitcnt lgkmcnt(4)\n"
"s_cmp_eq_i32 s78, 6\n"
"s_cbranch_scc1 .Lmk_x111s\n"
".Lmk_x111r:\n"
"v_mfma_f32_16x16x32_bf16 v[76:79], v[80:83], v[28:31], v[36:39]\n"
"ds_read_b128 v[104:107], v15 offset:8192\n"
"v_exp_f32_e32 v72, v72\n"
"v_exp_f32_e32 v73, v73\n"
"v_exp_f32_e32 v74, v74\n"
"v_mfma_f32_16x16x32_bf16 v[108:111], v[80:83], v[32:35], v[60:63]\n"
"buffer_load_dwordx4 a[104:107], v0, s[4:7], s98 offen offset:2048 sc1\n"
"v_exp_f32_e32 v75, v75\n"
"v_mfma_f32_16x16x32_bf16 v[80:83], v[120:123], v[28:31], v[36:39]\n"
"ds_read_b128 v[124:127], v252 offset:8448\n"
"v_exp_f32_e32 v96, v96\n"
"v_exp_f32_e32 v97, v97\n"
"v_mfma_f32_16x16x32_bf16 v[128:131], v[120:123], v[32:35], v[60:63]\n"
"v_exp_f32_e32 v98, v98\n"
"ds_read_b64_tr_b16 v[120:121], v16 offset:12288\n"
"v_exp_f32_e32 v99, v99\n"
"s_waitcnt lgkmcnt(3)\n"
"v_mfma_f32_16x16x32_bf16 v[132:135], v[100:103], v[152:155], v[132:135]\n"
"v_cvt_pk_bf16_f32 v144, v72, v73\n"
"buffer_load_dwordx4 a[108:111], v0, s[4:7], s98 offen offset:3072 sc1\n"
"v_cvt_pk_bf16_f32 v145, v74, v75\n"
"v_cvt_pk_bf16_f32 v146, v96, v97\n"
"v_mfma_f32_16x16x32_bf16 v[44:47], v[116:119], v[152:155], v[44:47]\n"
"v_cvt_pk_bf16_f32 v147, v98, v99\n"
"ds_read_b64_tr_b16 v[122:123], v253 offset:12544\n"
"v_exp_f32_e32 v112, v112\n"
"v_exp_f32_e32 v113, v113\n"
"v_mfma_f32_16x16x32_bf16 v[64:67], v[84:87], v[152:155], v[64:67]\n"
"v_exp_f32_e32 v114, v114\n"
"v_exp_f32_e32 v115, v115\n"
"ds_read_b64_tr_b16 v[72:73], v253 offset:12288\n"
"v_mfma_f32_16x16x32_bf16 v[136:139], v[100:103], v[40:43], v[136:139]\n"
"buffer_load_dwordx4 a[112:115], v1, s[4:7], s98 offen sc1\n"
"v_exp_f32_e32 v148, v148\n"
"v_exp_f32_e32 v149, v149\n"
"v_mfma_f32_16x16x32_bf16 v[140:143], v[116:119], v[40:43], v[140:143]\n"
"v_exp_f32_e32 v150, v150\n"
"ds_read_b64_tr_b16 v[74:75], v16 offset:12544\n"
"v_exp_f32_e32 v151, v151\n"
"v_mfma_f32_16x16x32_bf16 v[68:71], v[84:87], v[40:43], v[68:71]\n"
"v_cvt_pk_bf16_f32 v40, v112, v113\n"
"v_cvt_pk_bf16_f32 v41, v114, v115\n"
"v_cvt_pk_bf16_f32 v42, v148, v149\n"
"v_cvt_pk_bf16_f32 v43, v150, v151\n"
"s_waitcnt lgkmcnt(4)\n"
"s_cmp_eq_i32 s78, 5\n"
"s_cbranch_scc1 .Lmk_x112s\n"
".Lmk_x112r:\n"
"v_mfma_f32_16x16x32_bf16 v[84:87], v[104:107], v[28:31], v[36:39]\n"
"ds_read_b64_tr_b16 v[96:97], v16 offset:10240\n"
"v_exp_f32_e32 v76, v76\n"
"v_exp_f32_e32 v77, v77\n"
"ds_read_b64_tr_b16 v[98:99], v253 offset:10496\n"
"v_mfma_f32_16x16x32_bf16 v[112:115], v[104:107], v[32:35], v[60:63]\n"
"buffer_load_dwordx4 a[116:119], v1, s[4:7], s98 offen offset:1024 sc1\n"
"v_exp_f32_e32 v78, v78\n"
"v_exp_f32_e32 v79, v79\n"
"v_mfma_f32_16x16x32_bf16 v[104:107], v[124:127], v[28:31], v[36:39]\n"
"ds_read_b64_tr_b16 v[36:37], v253 offset:10240\n"
"v_exp_f32_e32 v80, v80\n"
"v_exp_f32_e32 v81, v81\n"
"ds_read_b64_tr_b16 v[38:39], v16 offset:10496\n"
"v_mfma_f32_16x16x32_bf16 v[116:119], v[124:127], v[32:35], v[60:63]\n"
"v_exp_f32_e32 v82, v82\n"
"v_exp_f32_e32 v83, v83\n"
"ds_read_b64_tr_b16 v[60:61], v16 offset:24576\n"
"s_waitcnt lgkmcnt(5)\n"
"v_mfma_f32_16x16x32_bf16 v[132:135], v[100:103], v[144:147], v[132:135]\n"
"buffer_load_dwordx4 a[120:123], v1, s[4:7], s98 offen offset:2048 sc1\n"
"v_cvt_pk_bf16_f32 v124, v76, v77\n"
"v_cvt_pk_bf16_f32 v125, v78, v79\n"
"v_cvt_pk_bf16_f32 v126, v80, v81\n"
"ds_read_b64_tr_b16 v[62:63], v253 offset:24832\n"
"v_mfma_f32_16x16x32_bf16 v[44:47], v[120:123], v[144:147], v[44:47]\n"
"v_cvt_pk_bf16_f32 v127, v82, v83\n"
"v_exp_f32_e32 v108, v108\n"
"ds_read_b64_tr_b16 v[76:77], v253 offset:24576\n"
"v_exp_f32_e32 v109, v109\n"
"v_mfma_f32_16x16x32_bf16 v[64:67], v[72:75], v[144:147], v[64:67]\n"
"v_exp_f32_e32 v110, v110\n"
"ds_read_b64_tr_b16 v[78:79], v16 offset:24832\n"
"v_exp_f32_e32 v111, v111\n"
"v_mfma_f32_16x16x32_bf16 v[136:139], v[100:103], v[40:43], v[136:139]\n"
"buffer_load_dwordx4 a[124:127], v1, s[4:7], s98 offen offset:3072 sc1\n"
"v_exp_f32_e32 v128, v128\n"
"v_add_u32_e32 v248, 0x26400, v0\n"
"v_exp_f32_e32 v129, v129\n"
"v_mfma_f32_16x16x32_bf16 v[140:143], v[120:123], v[40:43], v[140:143]\n"
"v_exp_f32_e32 v130, v130\n"
"ds_read_b128 v[80:83], v248\n"
"v_exp_f32_e32 v131, v131\n"
"v_mfma_f32_16x16x32_bf16 v[68:71], v[72:75], v[40:43], v[68:71]\n"
"v_cvt_pk_bf16_f32 v40, v108, v109\n"
"ds_read_b128 v[72:75], v248 offset:1024\n"
"v_cvt_pk_bf16_f32 v41, v110, v111\n"
"v_cvt_pk_bf16_f32 v42, v128, v129\n"
"v_cvt_pk_bf16_f32 v43, v130, v131\n"
"s_waitcnt lgkmcnt(6)\n"
"v_mfma_f32_16x16x32_bf16 v[132:135], v[100:103], v[124:127], v[132:135]\n"
"ds_read_b64_tr_b16 v[108:109], v16 offset:8192\n"
"v_cndmask_b32_e64 v84, v48, v84, s[20:21]\n"
"v_cndmask_b32_e64 v85, v49, v85, s[22:23]\n"
"v_cndmask_b32_e64 v86, v50, v86, s[24:25]\n"
"s_add_u32 m0, s77, 0x4000\n"
"s_add_u32 s98, s65, 0\n"
"buffer_load_dwordx4 v2, s[4:7], s98 offen sc1 lds\n"
"buffer_load_dwordx4 v2, s[4:7], s98 offen offset:1024 sc1 lds\n"
"v_cndmask_b32_e64 v87, v51, v87, s[26:27]\n"
"v_cndmask_b32_e64 v104, v24, v104, s[28:29]\n"
"v_mfma_f32_16x16x32_bf16 v[44:47], v[96:99], v[124:127], v[44:47]\n"
"v_cndmask_b32_e64 v105, v25, v105, s[30:31]\n"
"v_cndmask_b32_e64 v106, v26, v106, s[32:33]\n"
"v_cndmask_b32_e64 v107, v27, v107, s[34:35]\n"
"v_exp_f32_e32 v84, v84\n"
"v_exp_f32_e32 v85, v85\n"
"v_exp_f32_e32 v86, v86\n"
"ds_read_b64_tr_b16 v[110:111], v253 offset:8448\n"
"v_exp_f32_e32 v87, v87\n"
"v_mfma_f32_16x16x32_bf16 v[64:67], v[36:39], v[124:127], v[64:67]\n"
"v_exp_f32_e32 v104, v104\n"
"v_exp_f32_e32 v105, v105\n"
"v_exp_f32_e32 v106, v106\n"
"v_exp_f32_e32 v107, v107\n"
"v_cvt_pk_bf16_f32 v48, v84, v85\n"
"v_cvt_pk_bf16_f32 v49, v86, v87\n"
"v_cvt_pk_bf16_f32 v50, v104, v105\n"
"v_mfma_f32_16x16x32_bf16 v[136:139], v[100:103], v[40:43], v[136:139]\n"
"v_cvt_pk_bf16_f32 v51, v106, v107\n"
"ds_read_b64_tr_b16 v[84:85], v253 offset:8192\n"
"v_cndmask_b32_e64 v112, v52, v112, s[36:37]\n"
"s_add_u32 s98, s69, 0x608000\n"
"buffer_load_dwordx4 v[24:27], v0, s[4:7], s98 offen sc1\n"
"v_cndmask_b32_e64 v113, v53, v113, s[38:39]\n"
"v_cndmask_b32_e64 v114, v54, v114, s[40:41]\n"
"v_cndmask_b32_e64 v115, v55, v115, s[42:43]\n"
"v_cndmask_b32_e64 v116, v56, v116, s[44:45]\n"
"v_cndmask_b32_e64 v117, v57, v117, s[46:47]\n"
"v_mfma_f32_16x16x32_bf16 v[140:143], v[96:99], v[40:43], v[140:143]\n"
"v_cndmask_b32_e64 v118, v58, v118, s[48:49]\n"
"v_cndmask_b32_e64 v119, v59, v119, s[50:51]\n"
"v_exp_f32_e32 v112, v112\n"
"v_exp_f32_e32 v113, v113\n"
"v_exp_f32_e32 v114, v114\n"
"ds_read_b64_tr_b16 v[86:87], v16 offset:8448\n"
"v_exp_f32_e32 v115, v115\n"
"v_exp_f32_e32 v116, v116\n"
"v_mfma_f32_16x16x32_bf16 v[68:71], v[36:39], v[40:43], v[68:71]\n"
"v_exp_f32_e32 v117, v117\n"
"v_exp_f32_e32 v118, v118\n"
"v_exp_f32_e32 v119, v119\n"
"v_cvt_pk_bf16_f32 v36, v112, v113\n"
"v_cvt_pk_bf16_f32 v37, v114, v115\n"
"v_cvt_pk_bf16_f32 v38, v116, v117\n"
"v_cvt_pk_bf16_f32 v39, v118, v119\n"
"s_waitcnt lgkmcnt(0)\n"
"v_mfma_f32_16x16x32_bf16 v[132:135], v[100:103], v[48:51], v[132:135]\n"
"v_and_b32_e32 v80, v48, v80\n"
"v_and_b32_e32 v81, v49, v81\n"
"v_and_b32_e32 v82, v50, v82\n"
"v_and_b32_e32 v83, v51, v83\n"
"v_mfma_f32_16x16x32_bf16 v[136:139], v[100:103], v[36:39], v[136:139]\n"
"v_xor_b32_e32 v40, v48, v80\n"
"v_xor_b32_e32 v41, v49, v81\n"
"v_xor_b32_e32 v42, v50, v82\n"
"v_xor_b32_e32 v43, v51, v83\n"
"v_mfma_f32_16x16x32_bf16 v[44:47], v[108:111], v[80:83], v[44:47]\n"
"v_and_b32_e32 v72, v36, v72\n"
"v_and_b32_e32 v73, v37, v73\n"
"v_mfma_f32_16x16x32_bf16 v[64:67], v[84:87], v[80:83], v[64:67]\n"
"v_and_b32_e32 v74, v38, v74\n"
"v_and_b32_e32 v75, v39, v75\n"
"v_mfma_f32_16x16x32_bf16 v[44:47], v[60:63], v[40:43], v[44:47]\n"
"v_xor_b32_e32 v48, v36, v72\n"
"v_xor_b32_e32 v49, v37, v73\n"
"v_mfma_f32_16x16x32_bf16 v[64:67], v[76:79], v[40:43], v[64:67]\n"
"v_xor_b32_e32 v50, v38, v74\n"
"v_xor_b32_e32 v51, v39, v75\n"
"v_mfma_f32_16x16x32_bf16 v[140:143], v[108:111], v[72:75], v[140:143]\n"
"s_add_u32 m0, s77, 0xc000\n"
"s_add_u32 s97, s65, 0x4000\n"
"buffer_load_dwordx4 v2, s[4:7], s97 offen sc1 lds\n"
"buffer_load_dwordx4 v2, s[4:7], s97 offen offset:1024 sc1 lds\n"
"v_mfma_f32_16x16x32_bf16 v[68:71], v[84:87], v[72:75], v[68:71]\n"
"v_mfma_f32_16x16x32_bf16 v[140:143], v[60:63], v[48:51], v[140:143]\n"
"v_mfma_f32_16x16x32_bf16 v[68:71], v[76:79], v[48:51], v[68:71]\n"
"v_max_f32_e32 v248, v132, v136\n"
"v_rcp_f32_e32 v132, v132\n"
"v_rcp_f32_e32 v136, v136\n"
"s_nop 0\n"
"v_max3_f32 v248, v248, v132, v136\n"
"v_cmp_nlt_f32_e32 vcc, v248, v251\n"
"s_cbranch_vccnz .Lmk_b107\n"
".Lmk_k113:\n"
"v_mul_f32_e32 v44, v44, v132\n"
"v_mul_f32_e32 v45, v45, v132\n"
"v_mul_f32_e32 v46, v46, v132\n"
"v_mul_f32_e32 v47, v47, v132\n"
"v_mul_f32_e32 v64, v64, v132\n"
"v_mul_f32_e32 v65, v65, v132\n"
"v_mul_f32_e32 v66, v66, v132\n"
"v_mul_f32_e32 v67, v67, v132\n"
"v_cvt_pk_bf16_f32 v28, v44, v45\n"
"v_cvt_pk_bf16_f32 v29, v46, v47\n"
"v_cvt_pk_bf16_f32 v30, v64, v65\n"
"v_cvt_pk_bf16_f32 v31, v66, v67\n"
"s_add_u32 s97, s60, 0x40000\n"
"buffer_store_dwordx4 v[28:31], v0, s[4:7], s97 offen sc1\n"
"v_mul_f32_e32 v140, v140, v136\n"
"v_mul_f32_e32 v141, v141, v136\n"
"v_mul_f32_e32 v142, v142, v136\n"
"v_mul_f32_e32 v143, v143, v136\n"
"v_mul_f32_e32 v68, v68, v136\n"
"v_mul_f32_e32 v69, v69, v136\n"
"v_mul_f32_e32 v70, v70, v136\n"
"v_mul_f32_e32 v71, v71, v136\n"
"v_cvt_pk_bf16_f32 v28, v140, v141\n"
"v_cvt_pk_bf16_f32 v29, v142, v143\n"
"v_cvt_pk_bf16_f32 v30, v68, v69\n"
"v_cvt_pk_bf16_f32 v31, v70, v71\n"
"s_add_u32 s97, s60, 0x48000\n"
"buffer_store_dwordx4 v[28:31], v0, s[4:7], s97 offen sc1\n"
"; @A AT a\n"
"s_mov_b32 s2, 0\n"
"v_mov_b32_e32 v255, 0xff800000\n"
"v_mov_b32_e32 v254, 0xff800000\n"
"v_mov_b32_e32 v250, 0x7f800000\n"
"v_mov_b32_e32 v249, 0x7f800000\n"
".Lmk_a114:\n"
"ds_read_b128 v[28:31], v15 offset:16384\n"
"ds_read_b128 v[32:35], v252 offset:16640\n"
"ds_read_b128 v[36:39], v15 offset:14336\n"
"ds_read_b128 v[40:43], v252 offset:14592\n"
"s_cmp_gt_i32 s78, 7\n"
"s_cselect_b32 s97, 0x80000000, 0\n"
"s_or_b32 s97, s97, 0x7f800000\n"
"s_add_u32 s96, s67, 32\n"
"buffer_load_dwordx2 v[246:247], v12, s[4:7], s96 offen sc1\n"
"s_waitcnt lgkmcnt(2)\n"
"v_mfma_f32_16x16x32_bf16 v[96:99], v[28:31], v[88:91], 0\n"
"v_mfma_f32_16x16x32_bf16 v[104:107], v[28:31], v[92:95], 0\n"
"v_mfma_f32_16x16x32_bf16 v[108:111], v[32:35], v[88:91], 0\n"
"v_mfma_f32_16x16x32_bf16 v[112:115], v[32:35], v[92:95], 0\n"
"s_nop 4\n"
"v_max_f32_e32 v248, v96, v97\n"
"v_max_f32_e32 v245, v104, v105\n"
"v_max3_f32 v248, v248, v98, v99\n"
"v_max3_f32 v245, v245, v106, v107\n"
"v_max3_f32 v248, v248, v108, v109\n"
"v_max3_f32 v245, v245, v112, v113\n"
"v_max3_f32 v248, v248, v110, v111\n"
"v_max3_f32 v245, v245, v114, v115\n"
"v_mov_b32_e32 v244, v248\n"
"v_mov_b32_e32 v243, v245\n"
"s_nop 0\n"
"v_permlane16_swap_b32_e32 v248, v244\n"
"v_permlane16_swap_b32_e32 v245, v243\n"
"v_max_f32_e32 v248, v248, v244\n"
"v_max_f32_e32 v245, v245, v243\n"
"v_mov_b32_e32 v244, v248\n"
"v_mov_b32_e32 v243, v245\n"
"s_nop 0\n"
"v_permlane32_swap_b32_e32 v248, v244\n"
"v_permlane32_swap_b32_e32 v245, v243\n"
"v_max_f32_e32 v248, v248, v244\n"
"v_max_f32_e32 v245, v245, v243\n"
"v_med3_f32 v248, v248, v255, v250\n"
"v_med3_f32 v245, v245, v254, v249\n"
"v_min_f32_e64 v116, s97, -v248\n"
"v_min_f32_e64 v117, s97, -v248\n"
"v_min_f32_e64 v118, s97, -v248\n"
"v_min_f32_e64 v119, s97, -v248\n"
"v_min_f32_e64 v120, s97, -v245\n"
"v_min_f32_e64 v121, s97, -v245\n"
"v_min_f32_e64 v122, s97, -v245\n"
"v_min_f32_e64 v123, s97, -v245\n"
"s_waitcnt lgkmcnt(0)\n"
"v_mfma_f32_16x16x32_bf16 v[44:47], v[36:39], v[88:91], v[116:119]\n"
"ds_read_b128 v[32:35], v15 offset:12288\n"
"v_sub_f32_e32 v96, v96, v248\n"
"v_sub_f32_e32 v97, v97, v248\n"
"v_sub_f32_e32 v98, v98, v248\n"
"v_sub_f32_e32 v99, v99, v248\n"
"v_mfma_f32_16x16x32_bf16 v[48:51], v[36:39], v[92:95], v[120:123]\n"
"v_sub_f32_e32 v108, v108, v248\n"
"v_sub_f32_e32 v109, v109, v248\n"
"v_sub_f32_e32 v110, v110, v248\n"
"v_sub_f32_e32 v111, v111, v248\n"
"v_mfma_f32_16x16x32_bf16 v[36:39], v[40:43], v[88:91], v[116:119]\n"
"ds_read_b128 v[52:55], v252 offset:12544\n"
"v_sub_f32_e32 v104, v104, v245\n"
"v_sub_f32_e32 v105, v105, v245\n"
"v_sub_f32_e32 v106, v106, v245\n"
"v_sub_f32_e32 v107, v107, v245\n"
"v_mfma_f32_16x16x32_bf16 v[56:59], v[40:43], v[92:95], v[120:123]\n"
"v_sub_f32_e32 v112, v112, v245\n"
"v_sub_f32_e32 v113, v113, v245\n"
"v_sub_f32_e32 v114, v114, v245\n"
"v_sub_f32_e32 v115, v115, v245\n"
"s_waitcnt lgkmcnt(0)\n"
"s_cmp_eq_i32 s78, 7\n"
"s_cbranch_scc1 .Lmk_x116s\n"
".Lmk_x116r:\n"
"v_mfma_f32_16x16x32_bf16 v[60:63], v[32:35], v[88:91], v[116:119]\n"
"ds_read_b128 v[40:43], v15 offset:10240\n"
"v_exp_f32_e32 v44, v44\n"
"v_exp_f32_e32 v45, v45\n"
"v_exp_f32_e32 v46, v46\n"
"buffer_load_dwordx4 v[28:31], v0, s[4:7], s98 offen offset:1024 sc1\n"
"v_exp_f32_e32 v47, v47\n"
"ds_read_b128 v[64:67], v252 offset:10496\n"
"v_exp_f32_e32 v36, v36\n"
"v_mfma_f32_16x16x32_bf16 v[68:71], v[32:35], v[92:95], v[120:123]\n"
"v_exp_f32_e32 v37, v37\n"
"v_exp_f32_e32 v38, v38\n"
"ds_read_b64_tr_b16 v[72:73], v16 offset:14336\n"
"v_exp_f32_e32 v39, v39\n"
"v_cvt_pk_bf16_f32 v76, v44, v45\n"
"buffer_load_dwordx4 v[32:35], v0, s[4:7], s98 offen offset:2048 sc1\n"
"v_cvt_pk_bf16_f32 v77, v46, v47\n"
"v_cvt_pk_bf16_f32 v78, v36, v37\n"
"v_mfma_f32_16x16x32_bf16 v[80:83], v[52:55], v[88:91], v[116:119]\n"
"v_cvt_pk_bf16_f32 v79, v38, v39\n"
"ds_read_b64_tr_b16 v[74:75], v253 offset:14592\n"
"v_exp_f32_e32 v48, v48\n"
"v_exp_f32_e32 v49, v49\n"
"v_exp_f32_e32 v50, v50\n"
"v_exp_f32_e32 v51, v51\n"
"ds_read_b64_tr_b16 v[84:85], v253 offset:14336\n"
"buffer_load_dwordx4 v[36:39], v0, s[4:7], s98 offen offset:3072 sc1\n"
"v_mfma_f32_16x16x32_bf16 v[124:127], v[52:55], v[92:95], v[120:123]\n"
"v_exp_f32_e32 v56, v56\n"
"v_exp_f32_e32 v57, v57\n"
"v_exp_f32_e32 v58, v58\n"
"ds_read_b64_tr_b16 v[86:87], v16 offset:14592\n"
"v_exp_f32_e32 v59, v59\n"
"v_cvt_pk_bf16_f32 v52, v48, v49\n"
"v_cvt_pk_bf16_f32 v53, v50, v51\n"
"v_cvt_pk_bf16_f32 v54, v56, v57\n"
"v_cvt_pk_bf16_f32 v55, v58, v59\n"
"s_waitcnt lgkmcnt(4)\n"
"s_cmp_eq_i32 s78, 6\n"
"s_cbranch_scc1 .Lmk_x117s\n"
".Lmk_x117r:\n"
"v_mfma_f32_16x16x32_bf16 v[128:131], v[40:43], v[88:91], v[116:119]\n"
"ds_read_b128 v[56:59], v15 offset:8192\n"
"v_exp_f32_e32 v60, v60\n"
"v_exp_f32_e32 v61, v61\n"
"ds_read_b128 v[132:135], v252 offset:8448\n"
"v_mfma_f32_16x16x32_bf16 v[136:139], v[40:43], v[92:95], v[120:123]\n"
"buffer_load_dwordx4 v[40:43], v1, s[4:7], s98 offen sc1\n"
"v_exp_f32_e32 v62, v62\n"
"v_exp_f32_e32 v63, v63\n"
"v_mfma_f32_16x16x32_bf16 v[140:143], v[64:67], v[88:91], v[116:119]\n"
"ds_read_b64_tr_b16 v[144:145], v16 offset:12288\n"
"v_exp_f32_e32 v80, v80\n"
"v_exp_f32_e32 v81, v81\n"
"ds_read_b64_tr_b16 v[146:147], v253 offset:12544\n"
"v_mfma_f32_16x16x32_bf16 v[148:151], v[64:67], v[92:95], v[120:123]\n"
"v_exp_f32_e32 v82, v82\n"
"v_exp_f32_e32 v83, v83\n"
"ds_read_b64_tr_b16 v[64:65], v253 offset:12288\n"
"s_waitcnt lgkmcnt(5)\n"
"v_mfma_f32_16x16x32_bf16 v[152:155], v[100:103], v[76:79], 0\n"
"buffer_load_dwordx4 v[44:47], v1, s[4:7], s98 offen offset:1024 sc1\n"
"v_cvt_pk_bf16_f32 v156, v60, v61\n"
"v_cvt_pk_bf16_f32 v157, v62, v63\n"
"v_cvt_pk_bf16_f32 v158, v80, v81\n"
"v_cvt_pk_bf16_f32 v159, v82, v83\n"
"v_mfma_f32_16x16x32_bf16 v[160:163], v[72:75], v[76:79], 0\n"
"ds_read_b64_tr_b16 v[66:67], v16 offset:12544\n"
"v_exp_f32_e32 v68, v68\n"
"v_exp_f32_e32 v69, v69\n"
"s_waitcnt vmcnt(8)\n"
"ds_read_b128 v[80:83], v2 offset:16384\n"
"v_mfma_f32_16x16x32_bf16 v[164:167], v[84:87], v[76:79], 0\n"
"v_exp_f32_e32 v70, v70\n"
"v_exp_f32_e32 v71, v71\n"
"v_mfma_f32_16x16x32_bf16 v[168:171], v[100:103], v[52:55], 0\n"
"buffer_load_dwordx4 v[48:51], v1, s[4:7], s98 offen offset:2048 sc1\n"
"ds_read_b128 v[76:79], v2 offset:17408\n"
"v_exp_f32_e32 v124, v124\n"
"v_exp_f32_e32 v125, v125\n"
"v_mfma_f32_16x16x32_bf16 v[172:175], v[72:75], v[52:55], 0\n"
"ds_read_b128 v[72:75], v2 offset:49152\n"
"v_exp_f32_e32 v126, v126\n"
"v_exp_f32_e32 v127, v127\n"
"v_mfma_f32_16x16x32_bf16 v[176:179], v[84:87], v[52:55], 0\n"
"ds_read_b128 v[84:87], v2 offset:50176\n"
"v_cvt_pk_bf16_f32 v180, v68, v69\n"
"v_cvt_pk_bf16_f32 v181, v70, v71\n"
"v_cvt_pk_bf16_f32 v182, v124, v125\n"
"v_cvt_pk_bf16_f32 v183, v126, v127\n"
"s_waitcnt lgkmcnt(8)\n"
"s_cmp_eq_i32 s78, 5\n"
"s_cbranch_scc1 .Lmk_x118s\n"
".Lmk_x118r:\n"
"v_mfma_f32_16x16x32_bf16 v[124:127], v[56:59], v[88:91], v[116:119]\n"
"ds_read_b64_tr_b16 v[68:69], v16 offset:10240\n"
"v_exp_f32_e32 v128, v128\n"
"v_exp_f32_e32 v129, v129\n"
"ds_read_b64_tr_b16 v[70:71], v253 offset:10496\n"
"buffer_load_dwordx4 v[52:55], v1, s[4:7], s98 offen offset:3072 sc1\n"
"v_mfma_f32_16x16x32_bf16 v[184:187], v[56:59], v[92:95], v[120:123]\n"
"v_exp_f32_e32 v130, v130\n"
"v_exp_f32_e32 v131, v131\n"
"ds_read_b64_tr_b16 v[188:189], v253 offset:10240\n"
"v_mfma_f32_16x16x32_bf16 v[192:195], v[132:135], v[88:91], v[116:119]\n"
"v_exp_f32_e32 v140, v140\n"
"ds_read_b64_tr_b16 v[190:191], v16 offset:10496\n"
"v_exp_f32_e32 v141, v141\n"
"v_exp_f32_e32 v142, v142\n"
"v_mfma_f32_16x16x32_bf16 v[196:199], v[132:135], v[92:95], v[120:123]\n"
"s_waitcnt lgkmcnt(4)\n"
"v_max_u32_e32 v248, v80, v81\n"
"v_exp_f32_e32 v143, v143\n"
"s_add_u32 s97, s69, 0x60a000\n"
"buffer_load_dwordx4 v[56:59], v0, s[4:7], s97 offen sc1\n"
"v_mfma_f32_16x16x32_bf16 v[152:155], v[100:103], v[156:159], v[152:155]\n"
"v_max3_u32 v248, v248, v82, v83\n"
"v_cvt_pk_bf16_f32 v132, v128, v129\n"
"v_cvt_pk_bf16_f32 v133, v130, v131\n"
"v_cvt_pk_bf16_f32 v134, v140, v141\n"
"v_mfma_f32_16x16x32_bf16 v[160:163], v[144:147], v[156:159], v[160:163]\n"
"v_max3_u32 v248, v248, v76, v77\n"
"v_cvt_pk_bf16_f32 v135, v142, v143\n"
"v_exp_f32_e32 v136, v136\n"
"v_max3_u32 v248, v248, v78, v79\n"
"v_exp_f32_e32 v137, v137\n"
"v_mfma_f32_16x16x32_bf16 v[164:167], v[64:67], v[156:159], v[164:167]\n"
"v_exp_f32_e32 v138, v138\n"
"v_max3_u32 v248, v248, v72, v73\n"
"v_exp_f32_e32 v139, v139\n"
"v_mfma_f32_16x16x32_bf16 v[168:171], v[100:103], v[180:183], v[168:171]\n"
"buffer_load_dwordx4 v[60:63], v0, s[4:7], s97 offen offset:1024 sc1\n"
"v_max3_u32 v248, v248, v74, v75\n"
"v_exp_f32_e32 v148, v148\n"
"v_exp_f32_e32 v149, v149\n"
"v_mfma_f32_16x16x32_bf16 v[172:175], v[144:147], v[180:183], v[172:175]\n"
"v_max3_u32 v248, v248, v84, v85\n"
"v_exp_f32_e32 v150, v150\n"
"v_max3_u32 v248, v248, v86, v87\n"
"v_exp_f32_e32 v151, v151\n"
"v_mfma_f32_16x16x32_bf16 v[176:179], v[64:67], v[180:183], v[176:179]\n"
"v_cvt_pk_bf16_f32 v128, v136, v137\n"
"v_cmp_eq_u32_e32 vcc, -1, v248\n"
"v_cvt_pk_bf16_f32 v129, v138, v139\n"
"v_cvt_pk_bf16_f32 v130, v148, v149\n"
"v_cvt_pk_bf16_f32 v131, v150, v151\n"
"s_cbranch_vccnz .Lmk_h105s\n"
".Lmk_h105r:\n"
"s_cmp_lg_u32 s2, 0\n"
"s_cbranch_scc1 .Lmk_s119\n"
"s_barrier\n"
".Lmk_s119:\n"
"ds_read_b128 v[72:75], v15 offset:6144\n"
"ds_read_b128 v[76:79], v252 offset:6400\n"
"s_waitcnt lgkmcnt(2)\n"
"v_mfma_f32_16x16x32_bf16 v[152:155], v[100:103], v[132:135], v[152:155]\n"
"ds_read_b128 v[80:83], v15 offset:4096\n"
"v_exp_f32_e32 v124, v124\n"
"v_exp_f32_e32 v125, v125\n"
"v_exp_f32_e32 v126, v126\n"
"v_mfma_f32_16x16x32_bf16 v[160:163], v[68:71], v[132:135], v[160:163]\n"
"buffer_load_dwordx4 v[64:67], v0, s[4:7], s97 offen offset:2048 sc1\n"
"ds_read_b128 v[84:87], v252 offset:4352\n"
"v_exp_f32_e32 v127, v127\n"
"v_mfma_f32_16x16x32_bf16 v[164:167], v[188:191], v[132:135], v[164:167]\n"
"v_exp_f32_e32 v192, v192\n"
"ds_read_b64_tr_b16 v[132:133], v16 offset:8192\n"
"v_exp_f32_e32 v193, v193\n"
"v_mfma_f32_16x16x32_bf16 v[168:171], v[100:103], v[128:131], v[168:171]\n"
"v_exp_f32_e32 v194, v194\n"
"v_exp_f32_e32 v195, v195\n"
"ds_read_b64_tr_b16 v[134:135], v253 offset:8448\n"
"v_mfma_f32_16x16x32_bf16 v[172:175], v[68:71], v[128:131], v[172:175]\n"
"v_cvt_pk_bf16_f32 v136, v124, v125\n"
"buffer_load_dwordx4 v[68:71], v0, s[4:7], s97 offen offset:3072 sc1\n"
"v_cvt_pk_bf16_f32 v137, v126, v127\n"
"v_cvt_pk_bf16_f32 v138, v192, v193\n"
"v_cvt_pk_bf16_f32 v139, v194, v195\n"
"v_mfma_f32_16x16x32_bf16 v[176:179], v[188:191], v[128:131], v[176:179]\n"
"ds_read_b64_tr_b16 v[124:125], v253 offset:8192\n"
"v_exp_f32_e32 v184, v184\n"
"v_exp_f32_e32 v185, v185\n"
"s_waitcnt lgkmcnt(5)\n"
"v_mfma_f32_16x16x32_bf16 v[128:131], v[72:75], v[88:91], v[116:119]\n"
"v_exp_f32_e32 v186, v186\n"
"ds_read_b64_tr_b16 v[126:127], v16 offset:8448\n"
"v_exp_f32_e32 v187, v187\n"
"v_exp_f32_e32 v196, v196\n"
"v_mfma_f32_16x16x32_bf16 v[140:143], v[72:75], v[92:95], v[120:123]\n"
"buffer_load_dwordx4 v[72:75], v1, s[4:7], s97 offen sc1\n"
"s_waitcnt vmcnt(12)\n"
"v_cmp_ne_u64_e32 vcc, v[246:247], v[18:19]\n"
"v_mfma_f32_16x16x32_bf16 v[144:147], v[76:79], v[88:91], v[116:119]\n"
"v_exp_f32_e32 v197, v197\n"
"v_exp_f32_e32 v198, v198\n"
"v_exp_f32_e32 v199, v199\n"
"v_mfma_f32_16x16x32_bf16 v[148:151], v[76:79], v[92:95], v[120:123]\n"
"s_cbranch_vccnz .Lmk_f87s\n"
".Lmk_f87r:\n"
"v_cvt_pk_bf16_f32 v156, v184, v185\n"
"v_cvt_pk_bf16_f32 v157, v186, v187\n"
"v_cvt_pk_bf16_f32 v158, v196, v197\n"
"v_cvt_pk_bf16_f32 v159, v198, v199\n"
"s_waitcnt lgkmcnt(4)\n"
"v_mfma_f32_16x16x32_bf16 v[180:183], v[80:83], v[88:91], v[116:119]\n"
"ds_read_b128 v[184:187], v15 offset:2048\n"
"v_exp_f32_e32 v128, v128\n"
"v_exp_f32_e32 v129, v129\n"
"v_exp_f32_e32 v130, v130\n"
"v_mfma_f32_16x16x32_bf16 v[188:191], v[80:83], v[92:95], v[120:123]\n"
"buffer_load_dwordx4 v[76:79], v1, s[4:7], s97 offen offset:1024 sc1\n"
"v_exp_f32_e32 v131, v131\n"
"v_mfma_f32_16x16x32_bf16 v[192:195], v[84:87], v[88:91], v[116:119]\n"
"ds_read_b128 v[196:199], v252 offset:2304\n"
"v_exp_f32_e32 v144, v144\n"
"v_exp_f32_e32 v145, v145\n"
"v_mfma_f32_16x16x32_bf16 v[200:203], v[84:87], v[92:95], v[120:123]\n"
"v_exp_f32_e32 v146, v146\n"
"ds_read_b64_tr_b16 v[204:205], v16 offset:6144\n"
"v_exp_f32_e32 v147, v147\n"
"s_waitcnt lgkmcnt(3)\n"
"v_mfma_f32_16x16x32_bf16 v[152:155], v[100:103], v[136:139], v[152:155]\n"
"v_cvt_pk_bf16_f32 v208, v128, v129\n"
"buffer_load_dwordx4 v[80:83], v1, s[4:7], s97 offen offset:2048 sc1\n"
"v_cvt_pk_bf16_f32 v209, v130, v131\n"
"v_cvt_pk_bf16_f32 v210, v144, v145\n"
"v_mfma_f32_16x16x32_bf16 v[160:163], v[132:135], v[136:139], v[160:163]\n"
"v_cvt_pk_bf16_f32 v211, v146, v147\n"
"ds_read_b64_tr_b16 v[206:207], v253 offset:6400\n"
"v_exp_f32_e32 v140, v140\n"
"v_exp_f32_e32 v141, v141\n"
"v_mfma_f32_16x16x32_bf16 v[164:167], v[124:127], v[136:139], v[164:167]\n"
"v_exp_f32_e32 v142, v142\n"
"v_exp_f32_e32 v143, v143\n"
"ds_read_b64_tr_b16 v[128:129], v253 offset:6144\n"
"v_mfma_f32_16x16x32_bf16 v[168:171], v[100:103], v[156:159], v[168:171]\n"
"buffer_load_dwordx4 v[84:87], v1, s[4:7], s97 offen offset:3072 sc1\n"
"v_exp_f32_e32 v148, v148\n"
"v_exp_f32_e32 v149, v149\n"
"v_mfma_f32_16x16x32_bf16 v[172:175], v[132:135], v[156:159], v[172:175]\n"
"v_exp_f32_e32 v150, v150\n"
"ds_read_b64_tr_b16 v[130:131], v16 offset:6400\n"
"v_exp_f32_e32 v151, v151\n"
"v_mfma_f32_16x16x32_bf16 v[176:179], v[124:127], v[156:159], v[176:179]\n"
"v_cvt_pk_bf16_f32 v124, v140, v141\n"
"v_cvt_pk_bf16_f32 v125, v142, v143\n"
"v_cvt_pk_bf16_f32 v126, v148, v149\n"
"v_cvt_pk_bf16_f32 v127, v150, v151\n"
"s_waitcnt lgkmcnt(4)\n"
"v_mfma_f32_16x16x32_bf16 v[132:135], v[184:187], v[88:91], v[116:119]\n"
"ds_read_b128 v[136:139], v15\n"
"v_exp_f32_e32 v180, v180\n"
"v_exp_f32_e32 v181, v181\n"
"v_exp_f32_e32 v182, v182\n"
"v_mfma_f32_16x16x32_bf16 v[140:143], v[184:187], v[92:95], v[120:123]\n"
"s_add_u32 s97, s56, 0x40000\n"
"buffer_load_dwordx4 v[144:147], v0, s[4:7], s97 offen sc1\n"
"v_exp_f32_e32 v183, v183\n"
"v_mfma_f32_16x16x32_bf16 v[148:151], v[196:199], v[88:91], v[116:119]\n"
"ds_read_b128 v[156:159], v252 offset:256\n"
"v_exp_f32_e32 v192, v192\n"
"v_exp_f32_e32 v193, v193\n"
"v_exp_f32_e32 v194, v194\n"
"v_mfma_f32_16x16x32_bf16 v[184:187], v[196:199], v[92:95], v[120:123]\n"
"buffer_load_dwordx4 v[196:199], v0, s[4:7], s97 offen offset:1024 sc1\n"
"ds_read_b64_tr_b16 v[212:213], v16 offset:4096\n"
"v_exp_f32_e32 v195, v195\n"
"s_waitcnt lgkmcnt(3)\n"
"v_mfma_f32_16x16x32_bf16 v[152:155], v[100:103], v[208:211], v[152:155]\n"
"v_cvt_pk_bf16_f32 v216, v180, v181\n"
"v_cvt_pk_bf16_f32 v217, v182, v183\n"
"v_cvt_pk_bf16_f32 v218, v192, v193\n"
"v_cvt_pk_bf16_f32 v219, v194, v195\n"
"buffer_load_dwordx4 v[180:183], v0, s[4:7], s97 offen offset:2048 sc1\n"
"v_mfma_f32_16x16x32_bf16 v[160:163], v[204:207], v[208:211], v[160:163]\n"
"ds_read_b64_tr_b16 v[214:215], v253 offset:4352\n"
"v_exp_f32_e32 v188, v188\n"
"v_exp_f32_e32 v189, v189\n"
"v_mfma_f32_16x16x32_bf16 v[164:167], v[128:131], v[208:211], v[164:167]\n"
"v_exp_f32_e32 v190, v190\n"
"v_exp_f32_e32 v191, v191\n"
"buffer_load_dwordx4 v[192:195], v0, s[4:7], s97 offen offset:3072 sc1\n"
"v_mfma_f32_16x16x32_bf16 v[168:171], v[100:103], v[124:127], v[168:171]\n"
"ds_read_b64_tr_b16 v[208:209], v253 offset:4096\n"
"v_exp_f32_e32 v200, v200\n"
"v_exp_f32_e32 v201, v201\n"
"v_mfma_f32_16x16x32_bf16 v[172:175], v[204:207], v[124:127], v[172:175]\n"
"v_exp_f32_e32 v202, v202\n"
"buffer_load_dwordx4 v[204:207], v1, s[4:7], s97 offen sc1\n"
"ds_read_b64_tr_b16 v[210:211], v16 offset:4352\n"
"v_exp_f32_e32 v203, v203\n"
"v_mfma_f32_16x16x32_bf16 v[176:179], v[128:131], v[124:127], v[176:179]\n"
"v_cvt_pk_bf16_f32 v124, v188, v189\n"
"v_cvt_pk_bf16_f32 v125, v190, v191\n"
"v_cvt_pk_bf16_f32 v126, v200, v201\n"
"v_cvt_pk_bf16_f32 v127, v202, v203\n"
"s_waitcnt lgkmcnt(4)\n"
"v_mfma_f32_16x16x32_bf16 v[128:131], v[136:139], v[88:91], v[116:119]\n"
"ds_read_b64_tr_b16 v[188:189], v16 offset:2048\n"
"v_exp_f32_e32 v132, v132\n"
"v_exp_f32_e32 v133, v133\n"
"ds_read_b64_tr_b16 v[190:191], v253 offset:2304\n"
"buffer_load_dwordx4 v[200:203], v1, s[4:7], s97 offen offset:1024 sc1\n"
"v_mfma_f32_16x16x32_bf16 v[220:223], v[136:139], v[92:95], v[120:123]\n"
"v_exp_f32_e32 v134, v134\n"
"v_exp_f32_e32 v135, v135\n"
"ds_read_b64_tr_b16 v[136:137], v253 offset:2048\n"
"v_mfma_f32_16x16x32_bf16 v[224:227], v[156:159], v[88:91], v[116:119]\n"
"v_exp_f32_e32 v148, v148\n"
"v_exp_f32_e32 v149, v149\n"
"buffer_load_dwordx4 v[116:119], v1, s[4:7], s97 offen offset:2048 sc1\n"
"v_mfma_f32_16x16x32_bf16 v[228:231], v[156:159], v[92:95], v[120:123]\n"
"ds_read_b64_tr_b16 v[138:139], v16 offset:2304\n"
"v_exp_f32_e32 v150, v150\n"
"v_exp_f32_e32 v151, v151\n"
"ds_read_b64_tr_b16 v[120:121], v16 offset:16384\n"
"s_waitcnt lgkmcnt(5)\n"
"v_mfma_f32_16x16x32_bf16 v[152:155], v[100:103], v[216:219], v[152:155]\n"
"v_cvt_pk_bf16_f32 v156, v132, v133\n"
"v_cvt_pk_bf16_f32 v157, v134, v135\n"
"v_cvt_pk_bf16_f32 v158, v148, v149\n"
"buffer_load_dwordx4 v[132:135], v1, s[4:7], s97 offen offset:3072 sc1\n"
"ds_read_b64_tr_b16 v[122:123], v253 offset:16640\n"
"v_mfma_f32_16x16x32_bf16 v[160:163], v[212:215], v[216:219], v[160:163]\n"
"v_cvt_pk_bf16_f32 v159, v150, v151\n"
"v_exp_f32_e32 v140, v140\n"
"ds_read_b64_tr_b16 v[148:149], v253 offset:16384\n"
"v_exp_f32_e32 v141, v141\n"
"v_exp_f32_e32 v142, v142\n"
"v_mfma_f32_16x16x32_bf16 v[164:167], v[208:211], v[216:219], v[164:167]\n"
"s_add_u32 s97, s57, 0x40000\n"
"buffer_load_dwordx4 v[216:219], v0, s[4:7], s97 offen sc1\n"
"ds_read_b64_tr_b16 v[150:151], v16 offset:16640\n"
"v_exp_f32_e32 v143, v143\n"
"v_mfma_f32_16x16x32_bf16 v[168:171], v[100:103], v[124:127], v[168:171]\n"
"v_exp_f32_e32 v184, v184\n"
"v_add_u32_e32 v248, 0x26400, v0\n"
"v_exp_f32_e32 v185, v185\n"
"v_mfma_f32_16x16x32_bf16 v[172:175], v[212:215], v[124:127], v[172:175]\n"
"v_exp_f32_e32 v186, v186\n"
"buffer_load_dwordx4 v[212:215], v0, s[4:7], s97 offen offset:1024 sc1\n"
"ds_read_b128 v[232:235], v248\n"
"v_exp_f32_e32 v187, v187\n"
"v_mfma_f32_16x16x32_bf16 v[176:179], v[208:211], v[124:127], v[176:179]\n"
"v_cvt_pk_bf16_f32 v124, v140, v141\n"
"ds_read_b128 v[208:211], v248 offset:1024\n"
"v_cvt_pk_bf16_f32 v125, v142, v143\n"
"v_cvt_pk_bf16_f32 v126, v184, v185\n"
"v_cvt_pk_bf16_f32 v127, v186, v187\n"
"s_waitcnt lgkmcnt(6)\n"
"v_mfma_f32_16x16x32_bf16 v[152:155], v[100:103], v[156:159], v[152:155]\n"
"ds_read_b64_tr_b16 v[140:141], v16\n"
"v_cndmask_b32_e64 v128, v96, v128, s[20:21]\n"
"v_cndmask_b32_e64 v129, v97, v129, s[22:23]\n"
"v_cndmask_b32_e64 v130, v98, v130, s[24:25]\n"
"buffer_load_dwordx4 v[184:187], v0, s[4:7], s97 offen offset:2048 sc1\n"
"v_cndmask_b32_e64 v131, v99, v131, s[26:27]\n"
"v_cndmask_b32_e64 v224, v108, v224, s[28:29]\n"
"v_cndmask_b32_e64 v225, v109, v225, s[30:31]\n"
"v_cndmask_b32_e64 v226, v110, v226, s[32:33]\n"
"v_cndmask_b32_e64 v227, v111, v227, s[34:35]\n"
"v_exp_f32_e32 v128, v128\n"
"v_mfma_f32_16x16x32_bf16 v[160:163], v[188:191], v[156:159], v[160:163]\n"
"v_exp_f32_e32 v129, v129\n"
"v_exp_f32_e32 v130, v130\n"
"ds_read_b64_tr_b16 v[142:143], v253 offset:256\n"
"buffer_load_dwordx4 v[96:99], v0, s[4:7], s97 offen offset:3072 sc1\n"
"v_exp_f32_e32 v131, v131\n"
"v_exp_f32_e32 v224, v224\n"
"v_mfma_f32_16x16x32_bf16 v[164:167], v[136:139], v[156:159], v[164:167]\n"
"v_exp_f32_e32 v225, v225\n"
"v_exp_f32_e32 v226, v226\n"
"v_exp_f32_e32 v227, v227\n"
"v_cvt_pk_bf16_f32 v108, v128, v129\n"
"buffer_load_dwordx4 v[156:159], v1, s[4:7], s97 offen sc1\n"
"v_cvt_pk_bf16_f32 v109, v130, v131\n"
"v_cvt_pk_bf16_f32 v110, v224, v225\n"
"v_mfma_f32_16x16x32_bf16 v[168:171], v[100:103], v[124:127], v[168:171]\n"
"v_cvt_pk_bf16_f32 v111, v226, v227\n"
"ds_read_b64_tr_b16 v[128:129], v253\n"
"v_cndmask_b32_e64 v220, v104, v220, s[36:37]\n"
"v_cndmask_b32_e64 v221, v105, v221, s[38:39]\n"
"v_cndmask_b32_e64 v222, v106, v222, s[40:41]\n"
"v_cndmask_b32_e64 v223, v107, v223, s[42:43]\n"
"v_cndmask_b32_e64 v228, v112, v228, s[44:45]\n"
"v_cndmask_b32_e64 v229, v113, v229, s[46:47]\n"
"v_cndmask_b32_e64 v230, v114, v230, s[48:49]\n"
"buffer_load_dwordx4 v[104:107], v1, s[4:7], s97 offen offset:1024 sc1\n"
"v_cndmask_b32_e64 v231, v115, v231, s[50:51]\n"
"v_mfma_f32_16x16x32_bf16 v[172:175], v[188:191], v[124:127], v[172:175]\n"
"v_exp_f32_e32 v220, v220\n"
"v_exp_f32_e32 v221, v221\n"
"v_exp_f32_e32 v222, v222\n"
"ds_read_b64_tr_b16 v[130:131], v16 offset:256\n"
"v_exp_f32_e32 v223, v223\n"
"v_exp_f32_e32 v228, v228\n"
"buffer_load_dwordx4 v[112:115], v1, s[4:7], s97 offen offset:2048 sc1\n"
"v_mfma_f32_16x16x32_bf16 v[176:179], v[136:139], v[124:127], v[176:179]\n"
"v_exp_f32_e32 v229, v229\n"
"v_exp_f32_e32 v230, v230\n"
"v_exp_f32_e32 v231, v231\n"
"v_cvt_pk_bf16_f32 v124, v220, v221\n"
"v_cvt_pk_bf16_f32 v125, v222, v223\n"
"v_cvt_pk_bf16_f32 v126, v228, v229\n"
"v_cvt_pk_bf16_f32 v127, v230, v231\n"
"s_waitcnt lgkmcnt(0)\n"
"v_mfma_f32_16x16x32_bf16 v[152:155], v[100:103], v[108:111], v[152:155]\n"
"v_and_b32_e32 v232, v108, v232\n"
"v_and_b32_e32 v233, v109, v233\n"
"v_and_b32_e32 v234, v110, v234\n"
"v_and_b32_e32 v235, v111, v235\n"
"v_mfma_f32_16x16x32_bf16 v[168:171], v[100:103], v[124:127], v[168:171]\n"
"v_xor_b32_e32 v136, v108, v232\n"
"v_xor_b32_e32 v137, v109, v233\n"
"v_xor_b32_e32 v138, v110, v234\n"
"v_xor_b32_e32 v139, v111, v235\n"
"v_mfma_f32_16x16x32_bf16 v[160:163], v[140:143], v[232:235], v[160:163]\n"
"v_and_b32_e32 v208, v124, v208\n"
"v_and_b32_e32 v209, v125, v209\n"
"v_mfma_f32_16x16x32_bf16 v[164:167], v[128:131], v[232:235], v[164:167]\n"
"v_and_b32_e32 v210, v126, v210\n"
"v_and_b32_e32 v211, v127, v211\n"
"v_mfma_f32_16x16x32_bf16 v[160:163], v[120:123], v[136:139], v[160:163]\n"
"v_xor_b32_e32 v108, v124, v208\n"
"v_xor_b32_e32 v109, v125, v209\n"
"v_mfma_f32_16x16x32_bf16 v[164:167], v[148:151], v[136:139], v[164:167]\n"
"v_xor_b32_e32 v110, v126, v210\n"
"v_xor_b32_e32 v111, v127, v211\n"
"v_mfma_f32_16x16x32_bf16 v[172:175], v[140:143], v[208:211], v[172:175]\n"
"buffer_load_dwordx4 v[124:127], v1, s[4:7], s97 offen offset:3072 sc1\n"
"s_add_u32 s97, s58, 0x40000\n"
"buffer_load_dwordx4 v[136:139], v0, s[4:7], s97 offen sc1\n"
"v_mfma_f32_16x16x32_bf16 v[176:179], v[128:131], v[208:211], v[176:179]\n"
"buffer_load_dwordx4 v[128:131], v0, s[4:7], s97 offen offset:1024 sc1\n"
"v_mfma_f32_16x16x32_bf16 v[172:175], v[120:123], v[108:111], v[172:175]\n"
"buffer_load_dwordx4 v[120:123], v0, s[4:7], s97 offen offset:2048 sc1\n"
"v_mfma_f32_16x16x32_bf16 v[176:179], v[148:151], v[108:111], v[176:179]\n"
"buffer_load_dwordx4 v[108:111], v0, s[4:7], s97 offen offset:3072 sc1\n"
"v_max_f32_e32 v248, v152, v168\n"
"v_rcp_f32_e32 v152, v152\n"
"v_rcp_f32_e32 v168, v168\n"
"s_nop 0\n"
"v_max3_f32 v248, v248, v152, v168\n"
"v_cmp_nlt_f32_e32 vcc, v248, v251\n"
"s_cbranch_vccnz .Lmk_b115\n"
".Lmk_k120:\n"
"v_mul_f32_e32 v160, v160, v152\n"
"v_mul_f32_e32 v161, v161, v152\n"
"v_mul_f32_e32 v162, v162, v152\n"
"v_mul_f32_e32 v163, v163, v152\n"
"v_mul_f32_e32 v164, v164, v152\n"
"v_mul_f32_e32 v165, v165, v152\n"
"v_mul_f32_e32 v166, v166, v152\n"
"v_mul_f32_e32 v167, v167, v152\n"
"v_cvt_pk_bf16_f32 v88, v160, v161\n"
"v_cvt_pk_bf16_f32 v89, v162, v163\n"
"v_cvt_pk_bf16_f32 v90, v164, v165\n"
"v_cvt_pk_bf16_f32 v91, v166, v167\n"
"buffer_store_dwordx4 v[88:91], v0, s[4:7], s60 offen sc1\n"
"v_mul_f32_e32 v172, v172, v168\n"
"v_mul_f32_e32 v173, v173, v168\n"
"v_mul_f32_e32 v174, v174, v168\n"
"v_mul_f32_e32 v175, v175, v168\n"
"v_mul_f32_e32 v176, v176, v168\n"
"v_mul_f32_e32 v177, v177, v168\n"
"v_mul_f32_e32 v178, v178, v168\n"
"v_mul_f32_e32 v179, v179, v168\n"
"v_cvt_pk_bf16_f32 v88, v172, v173\n"
"v_cvt_pk_bf16_f32 v89, v174, v175\n"
"v_cvt_pk_bf16_f32 v90, v176, v177\n"
"v_cvt_pk_bf16_f32 v91, v178, v179\n"
"s_add_u32 s98, s60, 0x8000\n"
"buffer_store_dwordx4 v[88:91], v0, s[4:7], s98 offen sc1\n"
"s_waitcnt vmcnt(21)\n"
"; @A G2 b.0\n"
"s_nop 0\n"
"v_mfma_f32_16x16x32_bf16 v[88:91], a[128:131], v[144:147], 0\n"
"v_mfma_f32_16x16x32_bf16 v[92:95], a[132:135], v[144:147], 0\n"
"v_mfma_f32_16x16x32_bf16 v[100:103], a[136:139], v[144:147], 0\n"
"buffer_load_dwordx4 v[140:143], v1, s[4:7], s97 offen sc1\n"
"v_mfma_f32_16x16x32_bf16 v[148:151], a[140:143], v[144:147], 0\n"
"s_waitcnt vmcnt(21)\n"
"v_mfma_f32_16x16x32_bf16 v[88:91], a[144:147], v[196:199], v[88:91]\n"
"v_mfma_f32_16x16x32_bf16 v[92:95], a[148:151], v[196:199], v[92:95]\n"
"v_mfma_f32_16x16x32_bf16 v[100:103], a[152:155], v[196:199], v[100:103]\n"
"buffer_load_dwordx4 v[144:147], v1, s[4:7], s97 offen offset:1024 sc1\n"
"v_mfma_f32_16x16x32_bf16 v[148:151], a[156:159], v[196:199], v[148:151]\n"
"s_waitcnt vmcnt(21)\n"
"v_mfma_f32_16x16x32_bf16 v[88:91], a[160:163], v[180:183], v[88:91]\n"
"v_mfma_f32_16x16x32_bf16 v[92:95], a[164:167], v[180:183], v[92:95]\n"
"v_mfma_f32_16x16x32_bf16 v[100:103], a[168:171], v[180:183], v[100:103]\n"
"buffer_load_dwordx4 v[152:155], v1, s[4:7], s97 offen offset:2048 sc1\n"
"v_mfma_f32_16x16x32_bf16 v[148:151], a[172:175], v[180:183], v[148:151]\n"
"s_waitcnt vmcnt(21)\n"
"v_mfma_f32_16x16x32_bf16 v[88:91], a[176:179], v[192:195], v[88:91]\n"
"v_mfma_f32_16x16x32_bf16 v[92:95], a[180:183], v[192:195], v[92:95]\n"
"v_mfma_f32_16x16x32_bf16 v[100:103], a[184:187], v[192:195], v[100:103]\n"
"buffer_load_dwordx4 v[160:163], v1, s[4:7], s97 offen offset:3072 sc1\n"
"v_mfma_f32_16x16x32_bf16 v[148:151], a[188:191], v[192:195], v[148:151]\n"
"s_waitcnt vmcnt(21)\n"
"v_mfma_f32_16x16x32_bf16 v[88:91], a[192:195], v[204:207], v[88:91]\n"
"s_add_u32 s98, s59, 0x40000\n"
"v_mfma_f32_16x16x32_bf16 v[92:95], a[196:199], v[204:207], v[92:95]\n"
"v_mfma_f32_16x16x32_bf16 v[100:103], a[200:203], v[204:207], v[100:103]\n"
"buffer_load_dwordx4 v[164:167], v0, s[4:7], s98 offen sc1\n"
"v_mfma_f32_16x16x32_bf16 v[148:151], a[204:207], v[204:207], v[148:151]\n"
"s_waitcnt vmcnt(21)\n"
"v_mfma_f32_16x16x32_bf16 v[88:91], a[208:211], v[200:203], v[88:91]\n"
"v_mfma_f32_16x16x32_bf16 v[92:95], a[212:215], v[200:203], v[92:95]\n"
"v_mfma_f32_16x16x32_bf16 v[100:103], a[216:219], v[200:203], v[100:103]\n"
"buffer_load_dwordx4 v[168:171], v0, s[4:7], s98 offen offset:1024 sc1\n"
"v_mfma_f32_16x16x32_bf16 v[148:151], a[220:223], v[200:203], v[148:151]\n"
"s_waitcnt vmcnt(21)\n"
"v_mfma_f32_16x16x32_bf16 v[88:91], a[224:227], v[116:119], v[88:91]\n"
"v_mfma_f32_16x16x32_bf16 v[92:95], a[228:231], v[116:119], v[92:95]\n"
"v_mfma_f32_16x16x32_bf16 v[100:103], a[232:235], v[116:119], v[100:103]\n"
"buffer_load_dwordx4 v[172:175], v0, s[4:7], s98 offen offset:2048 sc1\n"
"v_mfma_f32_16x16x32_bf16 v[148:151], a[236:239], v[116:119], v[148:151]\n"
"s_waitcnt vmcnt(21)\n"
"v_mfma_f32_16x16x32_bf16 v[88:91], a[240:243], v[132:135], v[88:91]\n"
"v_mfma_f32_16x16x32_bf16 v[92:95], a[244:247], v[132:135], v[92:95]\n"
"v_mfma_f32_16x16x32_bf16 v[100:103], a[248:251], v[132:135], v[100:103]\n"
"buffer_load_dwordx4 v[116:119], v0, s[4:7], s98 offen offset:3072 sc1\n"
"v_mfma_f32_16x16x32_bf16 v[148:151], a[252:255], v[132:135], v[148:151]\n"
"s_waitcnt vmcnt(20)\n"
"; @A G2 b.1\n"
"v_mfma_f32_16x16x32_bf16 v[132:135], a[128:131], v[216:219], 0\n"
"s_nop 1\n"
"v_cmp_u_f32_e32 vcc, v88, v88\n"
"v_mfma_f32_16x16x32_bf16 v[176:179], a[132:135], v[216:219], 0\n"
"s_cbranch_vccnz .Lmk_p121s\n"
".Lmk_p121r:\n"
"v_mfma_f32_16x16x32_bf16 v[180:183], a[136:139], v[216:219], 0\n"
"buffer_load_dwordx4 v[188:191], v1, s[4:7], s98 offen sc1\n"
"v_mfma_f32_16x16x32_bf16 v[192:195], a[140:143], v[216:219], 0\n"
"v_mfma_f32_16x16x32_bf16 v[132:135], a[144:147], v[212:215], v[132:135]\n"
"ds_write_b128 v3, v[88:91]\n"
"v_mfma_f32_16x16x32_bf16 v[176:179], a[148:151], v[212:215], v[176:179]\n"
"v_mfma_f32_16x16x32_bf16 v[180:183], a[152:155], v[212:215], v[180:183]\n"
"ds_write_b128 v3, v[92:95] offset:1024\n"
"v_mfma_f32_16x16x32_bf16 v[192:195], a[156:159], v[212:215], v[192:195]\n"
"s_waitcnt vmcnt(19)\n"
"v_mfma_f32_16x16x32_bf16 v[132:135], a[160:163], v[184:187], v[132:135]\n"
"ds_write_b128 v3, v[100:103] offset:2048\n"
"v_mfma_f32_16x16x32_bf16 v[176:179], a[164:167], v[184:187], v[176:179]\n"
"buffer_load_dwordx4 v[88:91], v1, s[4:7], s98 offen offset:1024 sc1\n"
"v_mfma_f32_16x16x32_bf16 v[180:183], a[168:171], v[184:187], v[180:183]\n"
"ds_write_b128 v3, v[148:151] offset:3072\n"
"v_mfma_f32_16x16x32_bf16 v[192:195], a[172:175], v[184:187], v[192:195]\n"
"v_mfma_f32_16x16x32_bf16 v[132:135], a[176:179], v[96:99], v[132:135]\n"
"v_mfma_f32_16x16x32_bf16 v[176:179], a[180:183], v[96:99], v[176:179]\n"
"buffer_load_dwordx4 v[92:95], v1, s[4:7], s98 offen offset:2048 sc1\n"
"v_mfma_f32_16x16x32_bf16 v[180:183], a[184:187], v[96:99], v[180:183]\n"
"ds_read_b128 v[100:103], v2 offset:8192\n"
"ds_read_b128 v[148:151], v2 offset:9216\n"
"v_mfma_f32_16x16x32_bf16 v[192:195], a[188:191], v[96:99], v[192:195]\n"
"s_waitcnt vmcnt(19)\n"
"v_mfma_f32_16x16x32_bf16 v[132:135], a[192:195], v[156:159], v[132:135]\n"
"v_mfma_f32_16x16x32_bf16 v[176:179], a[196:199], v[156:159], v[176:179]\n"
"buffer_load_dwordx4 v[96:99], v1, s[4:7], s98 offen offset:3072 sc1\n"
"v_mfma_f32_16x16x32_bf16 v[180:183], a[200:203], v[156:159], v[180:183]\n"
"v_mfma_f32_16x16x32_bf16 v[192:195], a[204:207], v[156:159], v[192:195]\n"
"s_waitcnt lgkmcnt(0)\n"
"s_barrier\n"
"v_mfma_f32_16x16x32_bf16 v[132:135], a[208:211], v[104:107], v[132:135]\n"
"v_lshlrev_b32_e32 v255, 16, v100\n"
"v_mfma_f32_16x16x32_bf16 v[176:179], a[212:215], v[104:107], v[176:179]\n"
"ds_read_b128 v[156:159], v4\n"
"ds_read_b128 v[184:187], v4 offset:1024\n"
"v_mfma_f32_16x16x32_bf16 v[180:183], a[216:219], v[104:107], v[180:183]\n"
"ds_read_b128 v[196:199], v4 offset:2048\n"
"ds_read_b128 v[200:203], v4 offset:3072\n"
"v_mfma_f32_16x16x32_bf16 v[192:195], a[220:223], v[104:107], v[192:195]\n"
"s_waitcnt vmcnt(18)\n"
"v_and_b32_e32 v254, 0xffff0000, v100\n"
"v_mfma_f32_16x16x32_bf16 v[132:135], a[224:227], v[112:115], v[132:135]\n"
"v_lshlrev_b32_e32 v253, 16, v101\n"
"v_and_b32_e32 v252, 0xffff0000, v101\n"
"v_mfma_f32_16x16x32_bf16 v[176:179], a[228:231], v[112:115], v[176:179]\n"
"v_lshlrev_b32_e32 v251, 16, v102\n"
"v_and_b32_e32 v250, 0xffff0000, v102\n"
"v_mfma_f32_16x16x32_bf16 v[180:183], a[232:235], v[112:115], v[180:183]\n"
"v_lshlrev_b32_e32 v249, 16, v103\n"
"v_and_b32_e32 v248, 0xffff0000, v103\n"
"v_mfma_f32_16x16x32_bf16 v[192:195], a[236:239], v[112:115], v[192:195]\n"
"v_lshlrev_b32_e32 v247, 16, v148\n"
"v_and_b32_e32 v246, 0xffff0000, v148\n"
"v_mfma_f32_16x16x32_bf16 v[132:135], a[240:243], v[124:127], v[132:135]\n"
"v_lshlrev_b32_e32 v245, 16, v149\n"
"v_and_b32_e32 v244, 0xffff0000, v149\n"
"v_mfma_f32_16x16x32_bf16 v[176:179], a[244:247], v[124:127], v[176:179]\n"
"v_lshlrev_b32_e32 v243, 16, v150\n"
"v_and_b32_e32 v242, 0xffff0000, v150\n"
"v_mfma_f32_16x16x32_bf16 v[180:183], a[248:251], v[124:127], v[180:183]\n"
"v_lshlrev_b32_e32 v241, 16, v151\n"
"v_and_b32_e32 v240, 0xffff0000, v151\n"
"v_mfma_f32_16x16x32_bf16 v[192:195], a[252:255], v[124:127], v[192:195]\n"
"s_waitcnt vmcnt(16)\n"
"s_waitcnt lgkmcnt(0)\n"
"; @A G2 b.2\n"
"v_mfma_f32_16x16x32_bf16 v[100:103], a[128:131], v[136:139], 0\n"
"v_cmp_u_f32_e32 vcc, v132, v132\n"
"v_add_f32_e32 v156, v156, v255\n"
"v_mfma_f32_16x16x32_bf16 v[104:107], a[132:135], v[136:139], 0\n"
"s_cbranch_vccnz .Lmk_p122s\n"
".Lmk_p122r:\n"
"v_add_f32_e32 v157, v157, v254\n"
"v_mfma_f32_16x16x32_bf16 v[112:115], a[136:139], v[136:139], 0\n"
"v_add_f32_e32 v158, v158, v253\n"
"v_add_f32_e32 v159, v159, v252\n"
"v_mfma_f32_16x16x32_bf16 v[124:127], a[140:143], v[136:139], 0\n"
"v_add_f32_e32 v184, v184, v251\n"
"v_add_f32_e32 v185, v185, v250\n"
"v_mfma_f32_16x16x32_bf16 v[100:103], a[144:147], v[128:131], v[100:103]\n"
"ds_write_b128 v3, v[132:135] offset:6144\n"
"v_mfma_f32_16x16x32_bf16 v[104:107], a[148:151], v[128:131], v[104:107]\n"
"v_add_f32_e32 v186, v186, v249\n"
"v_add_f32_e32 v187, v187, v248\n"
"v_mfma_f32_16x16x32_bf16 v[112:115], a[152:155], v[128:131], v[112:115]\n"
"ds_write_b128 v3, v[176:179] offset:7168\n"
"v_mfma_f32_16x16x32_bf16 v[124:127], a[156:159], v[128:131], v[124:127]\n"
"s_waitcnt vmcnt(14)\n"
"v_add_f32_e32 v196, v196, v247\n"
"v_mfma_f32_16x16x32_bf16 v[100:103], a[160:163], v[120:123], v[100:103]\n"
"ds_write_b128 v3, v[180:183] offset:8192\n"
"v_mfma_f32_16x16x32_bf16 v[104:107], a[164:167], v[120:123], v[104:107]\n"
"v_add_f32_e32 v197, v197, v246\n"
"v_add_f32_e32 v198, v198, v245\n"
"v_mfma_f32_16x16x32_bf16 v[112:115], a[168:171], v[120:123], v[112:115]\n"
"ds_write_b128 v3, v[192:195] offset:9216\n"
"v_mfma_f32_16x16x32_bf16 v[124:127], a[172:175], v[120:123], v[124:127]\n"
"v_add_f32_e32 v199, v199, v244\n"
"v_add_f32_e32 v200, v200, v243\n"
"v_mfma_f32_16x16x32_bf16 v[100:103], a[176:179], v[108:111], v[100:103]\n"
"s_add_u32 s98, s56, 0\n"
"v_add_f32_e32 v201, v201, v242\n"
"v_mfma_f32_16x16x32_bf16 v[104:107], a[180:183], v[108:111], v[104:107]\n"
"buffer_load_dwordx4 v[120:123], v0, s[4:7], s98 offen sc1\n"
"v_mfma_f32_16x16x32_bf16 v[112:115], a[184:187], v[108:111], v[112:115]\n"
"v_add_f32_e32 v202, v202, v241\n"
"v_add_f32_e32 v203, v203, v240\n"
"v_mfma_f32_16x16x32_bf16 v[124:127], a[188:191], v[108:111], v[124:127]\n"
"s_waitcnt vmcnt(11)\n"
"v_mfma_f32_16x16x32_bf16 v[100:103], a[192:195], v[140:143], v[100:103]\n"
"buffer_load_dwordx4 v[108:111], v0, s[4:7], s98 offen offset:1024 sc1\n"
"v_mfma_f32_16x16x32_bf16 v[104:107], a[196:199], v[140:143], v[104:107]\n"
"v_mfma_f32_16x16x32_bf16 v[112:115], a[200:203], v[140:143], v[112:115]\n"
"v_mfma_f32_16x16x32_bf16 v[124:127], a[204:207], v[140:143], v[124:127]\n"
"buffer_load_dwordx4 v[128:131], v0, s[4:7], s98 offen offset:2048 sc1\n"
"v_mfma_f32_16x16x32_bf16 v[100:103], a[208:211], v[144:147], v[100:103]\n"
"v_mfma_f32_16x16x32_bf16 v[104:107], a[212:215], v[144:147], v[104:107]\n"
"v_mfma_f32_16x16x32_bf16 v[112:115], a[216:219], v[144:147], v[112:115]\n"
"buffer_load_dwordx4 v[132:135], v0, s[4:7], s98 offen offset:3072 sc1\n"
"v_mfma_f32_16x16x32_bf16 v[124:127], a[220:223], v[144:147], v[124:127]\n"
"s_waitcnt vmcnt(12)\n"
"v_mfma_f32_16x16x32_bf16 v[100:103], a[224:227], v[152:155], v[100:103]\n"
"s_waitcnt vmcnt(10)\n"
"v_max_u32_e32 v255, v164, v165\n"
"v_mfma_f32_16x16x32_bf16 v[104:107], a[228:231], v[152:155], v[104:107]\n"
"v_max3_u32 v255, v255, v166, v167\n"
"v_max3_u32 v255, v255, v168, v169\n"
"v_mfma_f32_16x16x32_bf16 v[112:115], a[232:235], v[152:155], v[112:115]\n"
"v_max3_u32 v255, v255, v170, v171\n"
"s_waitcnt vmcnt(8)\n"
"v_mfma_f32_16x16x32_bf16 v[124:127], a[236:239], v[152:155], v[124:127]\n"
"v_max3_u32 v255, v255, v172, v173\n"
"v_max3_u32 v255, v255, v174, v175\n"
"v_mfma_f32_16x16x32_bf16 v[100:103], a[240:243], v[160:163], v[100:103]\n"
"v_max3_u32 v255, v255, v116, v117\n"
"v_max3_u32 v255, v255, v118, v119\n"
"v_mfma_f32_16x16x32_bf16 v[104:107], a[244:247], v[160:163], v[104:107]\n"
"v_cmp_eq_u32_e32 vcc, -1, v255\n"
"v_mfma_f32_16x16x32_bf16 v[112:115], a[248:251], v[160:163], v[112:115]\n"
"s_cbranch_vccnz .Lmk_q124s\n"
".Lmk_q124r:\n"
"v_mfma_f32_16x16x32_bf16 v[124:127], a[252:255], v[160:163], v[124:127]\n"
"; @A G2 b.3\n"
"v_mfma_f32_16x16x32_bf16 v[156:159], a[128:131], v[164:167], v[156:159]\n"
"v_cmp_u_f32_e32 vcc, v100, v100\n"
"v_mfma_f32_16x16x32_bf16 v[184:187], a[132:135], v[164:167], v[184:187]\n"
"s_cbranch_vccnz .Lmk_p123s\n"
".Lmk_p123r:\n"
"v_mfma_f32_16x16x32_bf16 v[196:199], a[136:139], v[164:167], v[196:199]\n"
"buffer_load_dwordx4 v[136:139], v1, s[4:7], s98 offen sc1\n"
"v_mfma_f32_16x16x32_bf16 v[200:203], a[140:143], v[164:167], v[200:203]\n"
"v_mfma_f32_16x16x32_bf16 v[156:159], a[144:147], v[168:171], v[156:159]\n"
"ds_write_b128 v3, v[100:103] offset:12288\n"
"v_mfma_f32_16x16x32_bf16 v[184:187], a[148:151], v[168:171], v[184:187]\n"
"buffer_load_dwordx4 v[100:103], v1, s[4:7], s98 offen offset:1024 sc1\n"
"v_mfma_f32_16x16x32_bf16 v[196:199], a[152:155], v[168:171], v[196:199]\n"
"ds_write_b128 v3, v[104:107] offset:13312\n"
"v_mfma_f32_16x16x32_bf16 v[200:203], a[156:159], v[168:171], v[200:203]\n"
"v_mfma_f32_16x16x32_bf16 v[156:159], a[160:163], v[172:175], v[156:159]\n"
"s_waitcnt vmcnt(8)\n"
"v_max_u32_e32 v255, v188, v189\n"
"v_mfma_f32_16x16x32_bf16 v[184:187], a[164:167], v[172:175], v[184:187]\n"
"v_max3_u32 v255, v255, v190, v191\n"
"v_max3_u32 v255, v255, v88, v89\n"
"v_mfma_f32_16x16x32_bf16 v[196:199], a[168:171], v[172:175], v[196:199]\n"
"v_max3_u32 v255, v255, v90, v91\n"
"s_waitcnt vmcnt(6)\n"
"v_mfma_f32_16x16x32_bf16 v[200:203], a[172:175], v[172:175], v[200:203]\n"
"v_max3_u32 v255, v255, v92, v93\n"
"v_max3_u32 v255, v255, v94, v95\n"
"v_mfma_f32_16x16x32_bf16 v[156:159], a[176:179], v[116:119], v[156:159]\n"
"v_max3_u32 v255, v255, v96, v97\n"
"v_max3_u32 v255, v255, v98, v99\n"
"v_mfma_f32_16x16x32_bf16 v[184:187], a[180:183], v[116:119], v[184:187]\n"
"v_cmp_eq_u32_e32 vcc, -1, v255\n"
"v_mfma_f32_16x16x32_bf16 v[196:199], a[184:187], v[116:119], v[196:199]\n"
"s_cbranch_vccnz .Lmk_q125s\n"
".Lmk_q125r:\n"
"v_mfma_f32_16x16x32_bf16 v[200:203], a[188:191], v[116:119], v[200:203]\n"
"v_mfma_f32_16x16x32_bf16 v[156:159], a[192:195], v[188:191], v[156:159]\n"
"ds_write_b128 v3, v[112:115] offset:14336\n"
"v_mfma_f32_16x16x32_bf16 v[184:187], a[196:199], v[188:191], v[184:187]\n"
"buffer_load_dwordx4 v[104:107], v1, s[4:7], s98 offen offset:2048 sc1\n"
"v_mfma_f32_16x16x32_bf16 v[196:199], a[200:203], v[188:191], v[196:199]\n"
"ds_write_b128 v3, v[124:127] offset:15360\n"
"v_mfma_f32_16x16x32_bf16 v[200:203], a[204:207], v[188:191], v[200:203]\n"
"v_mfma_f32_16x16x32_bf16 v[156:159], a[208:211], v[88:91], v[156:159]\n"
"buffer_load_dwordx4 v[112:115], v1, s[4:7], s98 offen offset:3072 sc1\n"
"v_mfma_f32_16x16x32_bf16 v[184:187], a[212:215], v[88:91], v[184:187]\n"
"s_waitcnt lgkmcnt(0)\n"
"s_barrier\n"
"v_mfma_f32_16x16x32_bf16 v[196:199], a[216:219], v[88:91], v[196:199]\n"
"s_add_u32 s98, s57, 0\n"
"ds_read_b128 v[116:119], v5\n"
"v_mfma_f32_16x16x32_bf16 v[200:203], a[220:223], v[88:91], v[200:203]\n"
"ds_read_b128 v[88:91], v6\n"
"v_mfma_f32_16x16x32_bf16 v[156:159], a[224:227], v[92:95], v[156:159]\n"
"buffer_load_dwordx4 v[124:127], v0, s[4:7], s98 offen sc1\n"
"v_mfma_f32_16x16x32_bf16 v[184:187], a[228:231], v[92:95], v[184:187]\n"
"ds_read_b128 v[140:143], v5 offset:1024\n"
"ds_read_b128 v[144:147], v6 offset:1024\n"
"v_mfma_f32_16x16x32_bf16 v[196:199], a[232:235], v[92:95], v[196:199]\n"
"ds_read_b128 v[148:151], v5 offset:2048\n"
"ds_read_b128 v[152:155], v6 offset:2048\n"
"v_mfma_f32_16x16x32_bf16 v[200:203], a[236:239], v[92:95], v[200:203]\n"
"buffer_load_dwordx4 v[92:95], v0, s[4:7], s98 offen offset:1024 sc1\n"
"v_mfma_f32_16x16x32_bf16 v[156:159], a[240:243], v[96:99], v[156:159]\n"
"ds_read_b128 v[160:163], v5 offset:3072\n"
"ds_read_b128 v[164:167], v6 offset:3072\n"
"v_mfma_f32_16x16x32_bf16 v[184:187], a[244:247], v[96:99], v[184:187]\n"
"v_mfma_f32_16x16x32_bf16 v[196:199], a[248:251], v[96:99], v[196:199]\n"
"buffer_load_dwordx4 v[168:171], v0, s[4:7], s98 offen offset:2048 sc1\n"
"v_mfma_f32_16x16x32_bf16 v[200:203], a[252:255], v[96:99], v[200:203]\n"
"s_waitcnt lgkmcnt(4)\n"
"v_add_f32_e32 v116, v116, v88\n"
"v_add_f32_e32 v117, v117, v89\n"
"v_add_f32_e32 v118, v118, v90\n"
"v_add_f32_e32 v119, v119, v91\n"
"v_add_f32_e32 v140, v140, v144\n"
"v_add_f32_e32 v141, v141, v145\n"
"v_add_f32_e32 v142, v142, v146\n"
"v_add_f32_e32 v143, v143, v147\n"
"buffer_load_dwordx4 v[88:91], v0, s[4:7], s98 offen offset:3072 sc1\n"
"s_waitcnt lgkmcnt(0)\n"
"v_add_f32_e32 v148, v148, v152\n"
"v_add_f32_e32 v149, v149, v153\n"
"v_add_f32_e32 v150, v150, v154\n"
"v_add_f32_e32 v151, v151, v155\n"
"v_add_f32_e32 v160, v160, v164\n"
"v_add_f32_e32 v161, v161, v165\n"
"v_add_f32_e32 v162, v162, v166\n"
"v_add_f32_e32 v163, v163, v167\n"
"buffer_load_dwordx4 v[96:99], v1, s[4:7], s98 offen sc1\n"
"buffer_load_dwordx4 v[144:147], v1, s[4:7], s98 offen offset:1024 sc1\n"
"buffer_load_dwordx4 v[152:155], v1, s[4:7], s98 offen offset:2048 sc1\n"
"buffer_load_dwordx4 v[164:167], v1, s[4:7], s98 offen offset:3072 sc1\n"
"; @A fin b\n"
"v_mov_b32_e32 v172, 0x3f803f80\n"
"v_mov_b32_e32 v173, 0x3f803f80\n"
"v_mov_b32_e32 v174, 0x3f803f80\n"
"v_mov_b32_e32 v175, 0x3f803f80\n"
"v_add_f32_e32 v116, v156, v116\n"
"v_add_f32_e32 v117, v157, v117\n"
"v_add_f32_e32 v118, v158, v118\n"
"v_add_f32_e32 v119, v159, v119\n"
"v_add_f32_e32 v140, v184, v140\n"
"v_add_f32_e32 v141, v185, v141\n"
"v_add_f32_e32 v142, v186, v142\n"
"v_add_f32_e32 v143, v187, v143\n"
"v_cvt_pk_bf16_f32 v156, v116, v117\n"
"v_cvt_pk_bf16_f32 v157, v118, v119\n"
"v_cvt_pk_bf16_f32 v158, v140, v141\n"
"v_cvt_pk_bf16_f32 v159, v142, v143\n"
"s_add_u32 s98, s61, 0x440000\n"
"buffer_store_dwordx4 v[156:159], v0, s[4:7], s98 offen sc1\n"
"ds_write_b128 v2, v[156:159] offset:8192\n"
"v_add_f32_e32 v148, v196, v148\n"
"v_add_f32_e32 v149, v197, v149\n"
"v_add_f32_e32 v150, v198, v150\n"
"v_add_f32_e32 v151, v199, v151\n"
"v_add_f32_e32 v160, v200, v160\n"
"v_add_f32_e32 v161, v201, v161\n"
"v_add_f32_e32 v162, v202, v162\n"
"v_add_f32_e32 v163, v203, v163\n"
"v_cvt_pk_bf16_f32 v116, v148, v149\n"
"v_cvt_pk_bf16_f32 v117, v150, v151\n"
"v_cvt_pk_bf16_f32 v118, v160, v161\n"
"v_cvt_pk_bf16_f32 v119, v162, v163\n"
"s_add_u32 s98, s61, 0x440000\n"
"buffer_store_dwordx4 v[116:119], v0, s[4:7], s98 offen offset:1024 sc1\n"
"ds_write_b128 v2, v[116:119] offset:9216\n"
"v_mfma_f32_16x16x32_bf16 v[140:143], v[172:175], v[156:159], 0\n"
"v_mfma_f32_16x16x32_bf16 v[148:151], v[156:159], v[156:159], 0\n"
"v_mfma_f32_16x16x32_bf16 v[140:143], v[172:175], v[116:119], v[140:143]\n"
"v_mfma_f32_16x16x32_bf16 v[148:151], v[116:119], v[116:119], v[148:151]\n"
"s_mov_b32 s96, 0xaaaaaaaa\n"
"s_mov_b32 s97, 0xaaaaaaaa\n"
"s_nop 5\n"
"v_cndmask_b32_e64 v148, v148, v149, s[96:97]\n"
"v_cndmask_b32_e64 v150, v150, v151, s[96:97]\n"
"s_mov_b32 s96, 0xcccccccc\n"
"s_mov_b32 s97, 0xcccccccc\n"
"v_cndmask_b32_e64 v141, v148, v150, s[96:97]\n"
"s_add_u32 s98, s63, 0x44000\n"
"buffer_store_dwordx2 v[140:141], v14, s[4:7], s98 offen sc1\n"
"s_waitcnt vmcnt(18)\n"
"; @A G2 a.0\n"
"v_mfma_f32_16x16x32_bf16 v[116:119], a[128:131], v[120:123], 0\n"
"s_add_u32 s98, s58, 0\n"
"v_mfma_f32_16x16x32_bf16 v[140:143], a[132:135], v[120:123], 0\n"
"buffer_load_dwordx4 v[148:151], v0, s[4:7], s98 offen sc1\n"
"v_mfma_f32_16x16x32_bf16 v[156:159], a[136:139], v[120:123], 0\n"
"v_mfma_f32_16x16x32_bf16 v[160:163], a[140:143], v[120:123], 0\n"
"s_waitcnt vmcnt(18)\n"
"v_mfma_f32_16x16x32_bf16 v[116:119], a[144:147], v[108:111], v[116:119]\n"
"v_mfma_f32_16x16x32_bf16 v[140:143], a[148:151], v[108:111], v[140:143]\n"
"buffer_load_dwordx4 v[120:123], v0, s[4:7], s98 offen offset:1024 sc1\n"
"v_mfma_f32_16x16x32_bf16 v[156:159], a[152:155], v[108:111], v[156:159]\n"
"v_mfma_f32_16x16x32_bf16 v[160:163], a[156:159], v[108:111], v[160:163]\n"
"s_waitcnt vmcnt(18)\n"
"v_mfma_f32_16x16x32_bf16 v[116:119], a[160:163], v[128:131], v[116:119]\n"
"v_mfma_f32_16x16x32_bf16 v[140:143], a[164:167], v[128:131], v[140:143]\n"
"buffer_load_dwordx4 v[172:175], v0, s[4:7], s98 offen offset:2048 sc1\n"
"v_mfma_f32_16x16x32_bf16 v[156:159], a[168:171], v[128:131], v[156:159]\n"
"v_mfma_f32_16x16x32_bf16 v[160:163], a[172:175], v[128:131], v[160:163]\n"
"s_waitcnt vmcnt(18)\n"
"v_mfma_f32_16x16x32_bf16 v[116:119], a[176:179], v[132:135], v[116:119]\n"
"v_mfma_f32_16x16x32_bf16 v[140:143], a[180:183], v[132:135], v[140:143]\n"
"buffer_load_dwordx4 v[128:131], v0, s[4:7], s98 offen offset:3072 sc1\n"
"v_mfma_f32_16x16x32_bf16 v[156:159], a[184:187], v[132:135], v[156:159]\n"
"v_mfma_f32_16x16x32_bf16 v[160:163], a[188:191], v[132:135], v[160:163]\n"
"s_waitcnt vmcnt(18)\n"
"v_mfma_f32_16x16x32_bf16 v[116:119], a[192:195], v[136:139], v[116:119]\n"
"v_mfma_f32_16x16x32_bf16 v[140:143], a[196:199], v[136:139], v[140:143]\n"
"buffer_load_dwordx4 v[132:135], v1, s[4:7], s98 offen sc1\n"
"v_mfma_f32_16x16x32_bf16 v[156:159], a[200:203], v[136:139], v[156:159]\n"
"v_mfma_f32_16x16x32_bf16 v[160:163], a[204:207], v[136:139], v[160:163]\n"
"s_waitcnt vmcnt(18)\n"
"v_mfma_f32_16x16x32_bf16 v[116:119], a[208:211], v[100:103], v[116:119]\n"
"v_mfma_f32_16x16x32_bf16 v[140:143], a[212:215], v[100:103], v[140:143]\n"
"buffer_load_dwordx4 v[136:139], v1, s[4:7], s98 offen offset:1024 sc1\n"
"v_mfma_f32_16x16x32_bf16 v[156:159], a[216:219], v[100:103], v[156:159]\n"
"v_mfma_f32_16x16x32_bf16 v[160:163], a[220:223], v[100:103], v[160:163]\n"
"s_waitcnt vmcnt(18)\n"
"v_mfma_f32_16x16x32_bf16 v[116:119], a[224:227], v[104:107], v[116:119]\n"
"v_mfma_f32_16x16x32_bf16 v[140:143], a[228:231], v[104:107], v[140:143]\n"
"buffer_load_dwordx4 v[176:179], v1, s[4:7], s98 offen offset:2048 sc1\n"
"v_mfma_f32_16x16x32_bf16 v[156:159], a[232:235], v[104:107], v[156:159]\n"
"v_mfma_f32_16x16x32_bf16 v[160:163], a[236:239], v[104:107], v[160:163]\n"
"s_waitcnt vmcnt(18)\n"
"v_mfma_f32_16x16x32_bf16 v[116:119], a[240:243], v[112:115], v[116:119]\n"
"v_mfma_f32_16x16x32_bf16 v[140:143], a[244:247], v[112:115], v[140:143]\n"
"buffer_load_dwordx4 v[180:183], v1, s[4:7], s98 offen offset:3072 sc1\n"
"v_mfma_f32_16x16x32_bf16 v[156:159], a[248:251], v[112:115], v[156:159]\n"
"v_mfma_f32_16x16x32_bf16 v[160:163], a[252:255], v[112:115], v[160:163]\n"
"s_waitcnt vmcnt(17)\n"
"s_add_u32 s98, s59, 0\n"
"; @A G2 a.1\n"
"v_mfma_f32_16x16x32_bf16 v[108:111], a[128:131], v[124:127], 0\n"
"s_nop 0\n"
"v_cmp_u_f32_e32 vcc, v116, v116\n"
"v_mfma_f32_16x16x32_bf16 v[112:115], a[132:135], v[124:127], 0\n"
"s_cbranch_vccnz .Lmk_p126s\n"
".Lmk_p126r:\n"
"v_mfma_f32_16x16x32_bf16 v[184:187], a[136:139], v[124:127], 0\n"
"buffer_load_dwordx4 v[188:191], v0, s[4:7], s98 offen sc1\n"
"v_mfma_f32_16x16x32_bf16 v[192:195], a[140:143], v[124:127], 0\n"
"v_mfma_f32_16x16x32_bf16 v[108:111], a[144:147], v[92:95], v[108:111]\n"
"ds_write_b128 v3, v[116:119]\n"
"v_mfma_f32_16x16x32_bf16 v[112:115], a[148:151], v[92:95], v[112:115]\n"
"v_mfma_f32_16x16x32_bf16 v[184:187], a[152:155], v[92:95], v[184:187]\n"
"ds_write_b128 v3, v[140:143] offset:1024\n"
"v_mfma_f32_16x16x32_bf16 v[192:195], a[156:159], v[92:95], v[192:195]\n"
"s_waitcnt vmcnt(16)\n"
"v_mfma_f32_16x16x32_bf16 v[108:111], a[160:163], v[168:171], v[108:111]\n"
"ds_write_b128 v3, v[156:159] offset:2048\n"
"v_mfma_f32_16x16x32_bf16 v[112:115], a[164:167], v[168:171], v[112:115]\n"
"buffer_load_dwordx4 v[140:143], v0, s[4:7], s98 offen offset:1024 sc1\n"
"v_mfma_f32_16x16x32_bf16 v[184:187], a[168:171], v[168:171], v[184:187]\n"
"ds_write_b128 v3, v[160:163] offset:3072\n"
"v_mfma_f32_16x16x32_bf16 v[192:195], a[172:175], v[168:171], v[192:195]\n"
"v_mfma_f32_16x16x32_bf16 v[108:111], a[176:179], v[88:91], v[108:111]\n"
"v_mfma_f32_16x16x32_bf16 v[112:115], a[180:183], v[88:91], v[112:115]\n"
"buffer_load_dwordx4 v[156:159], v0, s[4:7], s98 offen offset:2048 sc1\n"
"v_mfma_f32_16x16x32_bf16 v[184:187], a[184:187], v[88:91], v[184:187]\n"
"ds_read_b128 v[104:107], v2\n"
"ds_read_b128 v[116:119], v2 offset:1024\n"
"v_mfma_f32_16x16x32_bf16 v[192:195], a[188:191], v[88:91], v[192:195]\n"
"s_waitcnt vmcnt(16)\n"
"v_mfma_f32_16x16x32_bf16 v[108:111], a[192:195], v[96:99], v[108:111]\n"
"v_mfma_f32_16x16x32_bf16 v[112:115], a[196:199], v[96:99], v[112:115]\n"
"buffer_load_dwordx4 v[160:163], v0, s[4:7], s98 offen offset:3072 sc1\n"
"v_mfma_f32_16x16x32_bf16 v[184:187], a[200:203], v[96:99], v[184:187]\n"
"v_mfma_f32_16x16x32_bf16 v[192:195], a[204:207], v[96:99], v[192:195]\n"
"s_waitcnt lgkmcnt(0)\n"
"s_barrier\n"
"v_mfma_f32_16x16x32_bf16 v[108:111], a[208:211], v[144:147], v[108:111]\n"
"v_lshlrev_b32_e32 v255, 16, v104\n"
"v_mfma_f32_16x16x32_bf16 v[112:115], a[212:215], v[144:147], v[112:115]\n"
"buffer_load_dwordx4 v[168:171], v1, s[4:7], s98 offen sc1\n"
"v_mfma_f32_16x16x32_bf16 v[184:187], a[216:219], v[144:147], v[184:187]\n"
"ds_read_b128 v[88:91], v4\n"
"ds_read_b128 v[92:95], v4 offset:1024\n"
"v_mfma_f32_16x16x32_bf16 v[192:195], a[220:223], v[144:147], v[192:195]\n"
"s_waitcnt vmcnt(16)\n"
"ds_read_b128 v[96:99], v4 offset:2048\n"
"v_mfma_f32_16x16x32_bf16 v[108:111], a[224:227], v[152:155], v[108:111]\n"
"ds_read_b128 v[100:103], v4 offset:3072\n"
"v_and_b32_e32 v254, 0xffff0000, v104\n"
"v_mfma_f32_16x16x32_bf16 v[112:115], a[228:231], v[152:155], v[112:115]\n"
"buffer_load_dwordx4 v[144:147], v1, s[4:7], s98 offen offset:1024 sc1\n"
"v_mfma_f32_16x16x32_bf16 v[184:187], a[232:235], v[152:155], v[184:187]\n"
"v_lshlrev_b32_e32 v253, 16, v105\n"
"v_and_b32_e32 v252, 0xffff0000, v105\n"
"v_mfma_f32_16x16x32_bf16 v[192:195], a[236:239], v[152:155], v[192:195]\n"
"v_lshlrev_b32_e32 v251, 16, v106\n"
"v_and_b32_e32 v250, 0xffff0000, v106\n"
"v_mfma_f32_16x16x32_bf16 v[108:111], a[240:243], v[164:167], v[108:111]\n"
"v_lshlrev_b32_e32 v249, 16, v107\n"
"v_and_b32_e32 v248, 0xffff0000, v107\n"
"v_mfma_f32_16x16x32_bf16 v[112:115], a[244:247], v[164:167], v[112:115]\n"
"buffer_load_dwordx4 v[152:155], v1, s[4:7], s98 offen offset:2048 sc1\n"
"v_mfma_f32_16x16x32_bf16 v[184:187], a[248:251], v[164:167], v[184:187]\n"
"v_lshlrev_b32_e32 v247, 16, v116\n"
"v_and_b32_e32 v246, 0xffff0000, v116\n"
"v_mfma_f32_16x16x32_bf16 v[192:195], a[252:255], v[164:167], v[192:195]\n"
"s_waitcnt vmcnt(13)\n"
"v_lshlrev_b32_e32 v245, 16, v117\n"
"; @A G2 a.2\n"
"v_mfma_f32_16x16x32_bf16 v[164:167], a[128:131], v[148:151], 0\n"
"v_cmp_u_f32_e32 vcc, v108, v108\n"
"s_add_u32 s97, s52, 0x440000\n"
"v_mfma_f32_16x16x32_bf16 v[196:199], a[132:135], v[148:151], 0\n"
"s_cbranch_vccnz .Lmk_p127s\n"
".Lmk_p127r:\n"
"v_and_b32_e32 v244, 0xffff0000, v117\n"
"v_mfma_f32_16x16x32_bf16 v[200:203], a[136:139], v[148:151], 0\n"
"buffer_load_dwordx4 v[204:207], v1, s[4:7], s98 offen offset:3072 sc1\n"
"v_mfma_f32_16x16x32_bf16 v[208:211], a[140:143], v[148:151], 0\n"
"buffer_load_dwordx4 v[104:107], v0, s[4:7], s97 offen sc1\n"
"v_mfma_f32_16x16x32_bf16 v[164:167], a[144:147], v[120:123], v[164:167]\n"
"ds_write_b128 v3, v[108:111] offset:6144\n"
"v_mfma_f32_16x16x32_bf16 v[196:199], a[148:151], v[120:123], v[196:199]\n"
"v_lshlrev_b32_e32 v243, 16, v118\n"
"v_and_b32_e32 v242, 0xffff0000, v118\n"
"v_mfma_f32_16x16x32_bf16 v[200:203], a[152:155], v[120:123], v[200:203]\n"
"ds_write_b128 v3, v[112:115] offset:7168\n"
"v_mfma_f32_16x16x32_bf16 v[208:211], a[156:159], v[120:123], v[208:211]\n"
"s_waitcnt vmcnt(13)\n"
"v_lshlrev_b32_e32 v241, 16, v119\n"
"v_mfma_f32_16x16x32_bf16 v[164:167], a[160:163], v[172:175], v[164:167]\n"
"ds_write_b128 v3, v[184:187] offset:8192\n"
"v_mfma_f32_16x16x32_bf16 v[196:199], a[164:167], v[172:175], v[196:199]\n"
"buffer_load_dwordx4 v[108:111], v0, s[4:7], s97 offen offset:1024 sc1\n"
"v_mfma_f32_16x16x32_bf16 v[200:203], a[168:171], v[172:175], v[200:203]\n"
"ds_write_b128 v3, v[192:195] offset:9216\n"
"v_mfma_f32_16x16x32_bf16 v[208:211], a[172:175], v[172:175], v[208:211]\n"
"v_and_b32_e32 v240, 0xffff0000, v119\n"
"s_waitcnt lgkmcnt(4)\n"
"v_mfma_f32_16x16x32_bf16 v[164:167], a[176:179], v[128:131], v[164:167]\n"
"v_add_f32_e32 v88, v88, v255\n"
"v_add_f32_e32 v89, v89, v254\n"
"v_mfma_f32_16x16x32_bf16 v[196:199], a[180:183], v[128:131], v[196:199]\n"
"buffer_load_dwordx4 v[112:115], v0, s[4:7], s97 offen offset:2048 sc1\n"
"v_mfma_f32_16x16x32_bf16 v[200:203], a[184:187], v[128:131], v[200:203]\n"
"v_add_f32_e32 v90, v90, v253\n"
"v_add_f32_e32 v91, v91, v252\n"
"v_mfma_f32_16x16x32_bf16 v[208:211], a[188:191], v[128:131], v[208:211]\n"
"s_waitcnt vmcnt(13)\n"
"v_add_f32_e32 v92, v92, v251\n"
"v_mfma_f32_16x16x32_bf16 v[164:167], a[192:195], v[132:135], v[164:167]\n"
"v_add_f32_e32 v93, v93, v250\n"
"v_add_f32_e32 v94, v94, v249\n"
"v_mfma_f32_16x16x32_bf16 v[196:199], a[196:199], v[132:135], v[196:199]\n"
"buffer_load_dwordx4 v[116:119], v0, s[4:7], s97 offen offset:3072 sc1\n"
"v_mfma_f32_16x16x32_bf16 v[200:203], a[200:203], v[132:135], v[200:203]\n"
"v_add_f32_e32 v95, v95, v248\n"
"v_add_f32_e32 v96, v96, v247\n"
"v_mfma_f32_16x16x32_bf16 v[208:211], a[204:207], v[132:135], v[208:211]\n"
"v_add_f32_e32 v97, v97, v246\n"
"v_add_f32_e32 v98, v98, v245\n"
"v_mfma_f32_16x16x32_bf16 v[164:167], a[208:211], v[136:139], v[164:167]\n"
"v_add_f32_e32 v99, v99, v244\n"
"v_add_f32_e32 v100, v100, v243\n"
"v_mfma_f32_16x16x32_bf16 v[196:199], a[212:215], v[136:139], v[196:199]\n"
"buffer_load_dwordx4 v[120:123], v1, s[4:7], s97 offen sc1\n"
"v_mfma_f32_16x16x32_bf16 v[200:203], a[216:219], v[136:139], v[200:203]\n"
"v_add_f32_e32 v101, v101, v242\n"
"v_add_f32_e32 v102, v102, v241\n"
"v_mfma_f32_16x16x32_bf16 v[208:211], a[220:223], v[136:139], v[208:211]\n"
"s_waitcnt vmcnt(13)\n"
"v_add_f32_e32 v103, v103, v240\n"
"v_mfma_f32_16x16x32_bf16 v[164:167], a[224:227], v[176:179], v[164:167]\n"
"s_waitcnt vmcnt(11)\n"
"v_max_u32_e32 v255, v188, v189\n"
"v_mfma_f32_16x16x32_bf16 v[196:199], a[228:231], v[176:179], v[196:199]\n"
"v_max3_u32 v255, v255, v190, v191\n"
"v_max3_u32 v255, v255, v140, v141\n"
"v_mfma_f32_16x16x32_bf16 v[200:203], a[232:235], v[176:179], v[200:203]\n"
"v_max3_u32 v255, v255, v142, v143\n"
"s_waitcnt vmcnt(9)\n"
"v_mfma_f32_16x16x32_bf16 v[208:211], a[236:239], v[176:179], v[208:211]\n"
"v_max3_u32 v255, v255, v156, v157\n"
"v_max3_u32 v255, v255, v158, v159\n"
"v_mfma_f32_16x16x32_bf16 v[164:167], a[240:243], v[180:183], v[164:167]\n"
"v_max3_u32 v255, v255, v160, v161\n"
"v_max3_u32 v255, v255, v162, v163\n"
"v_mfma_f32_16x16x32_bf16 v[196:199], a[244:247], v[180:183], v[196:199]\n"
"v_cmp_eq_u32_e32 vcc, -1, v255\n"
"v_mfma_f32_16x16x32_bf16 v[200:203], a[248:251], v[180:183], v[200:203]\n"
"s_cbranch_vccnz .Lmk_q129s\n"
".Lmk_q129r:\n"
"v_mfma_f32_16x16x32_bf16 v[208:211], a[252:255], v[180:183], v[208:211]\n"
"; @A G2 a.3\n"
"v_mfma_f32_16x16x32_bf16 v[88:91], a[128:131], v[188:191], v[88:91]\n"
"v_cmp_u_f32_e32 vcc, v164, v164\n"
"v_mfma_f32_16x16x32_bf16 v[92:95], a[132:135], v[188:191], v[92:95]\n"
"s_cbranch_vccnz .Lmk_p128s\n"
".Lmk_p128r:\n"
"v_mfma_f32_16x16x32_bf16 v[96:99], a[136:139], v[188:191], v[96:99]\n"
"buffer_load_dwordx4 v[124:127], v1, s[4:7], s97 offen offset:1024 sc1\n"
"v_mfma_f32_16x16x32_bf16 v[100:103], a[140:143], v[188:191], v[100:103]\n"
"v_mfma_f32_16x16x32_bf16 v[88:91], a[144:147], v[140:143], v[88:91]\n"
"ds_write_b128 v3, v[164:167] offset:12288\n"
"v_mfma_f32_16x16x32_bf16 v[92:95], a[148:151], v[140:143], v[92:95]\n"
"v_mfma_f32_16x16x32_bf16 v[96:99], a[152:155], v[140:143], v[96:99]\n"
"ds_write_b128 v3, v[196:199] offset:13312\n"
"v_mfma_f32_16x16x32_bf16 v[100:103], a[156:159], v[140:143], v[100:103]\n"
"v_mfma_f32_16x16x32_bf16 v[88:91], a[160:163], v[156:159], v[88:91]\n"
"s_waitcnt vmcnt(8)\n"
"v_max_u32_e32 v255, v168, v169\n"
"v_mfma_f32_16x16x32_bf16 v[92:95], a[164:167], v[156:159], v[92:95]\n"
"v_max3_u32 v255, v255, v170, v171\n"
"v_max3_u32 v255, v255, v144, v145\n"
"v_mfma_f32_16x16x32_bf16 v[96:99], a[168:171], v[156:159], v[96:99]\n"
"v_max3_u32 v255, v255, v146, v147\n"
"s_waitcnt vmcnt(6)\n"
"v_mfma_f32_16x16x32_bf16 v[100:103], a[172:175], v[156:159], v[100:103]\n"
"v_max3_u32 v255, v255, v152, v153\n"
"v_max3_u32 v255, v255, v154, v155\n"
"v_mfma_f32_16x16x32_bf16 v[88:91], a[176:179], v[160:163], v[88:91]\n"
"v_max3_u32 v255, v255, v204, v205\n"
"v_max3_u32 v255, v255, v206, v207\n"
"v_mfma_f32_16x16x32_bf16 v[92:95], a[180:183], v[160:163], v[92:95]\n"
"v_cmp_eq_u32_e32 vcc, -1, v255\n"
"v_mfma_f32_16x16x32_bf16 v[96:99], a[184:187], v[160:163], v[96:99]\n"
"s_cbranch_vccnz .Lmk_q130s\n"
".Lmk_q130r:\n"
"v_mfma_f32_16x16x32_bf16 v[100:103], a[188:191], v[160:163], v[100:103]\n"
"v_mfma_f32_16x16x32_bf16 v[88:91], a[192:195], v[168:171], v[88:91]\n"
"ds_write_b128 v3, v[200:203] offset:14336\n"
"v_mfma_f32_16x16x32_bf16 v[92:95], a[196:199], v[168:171], v[92:95]\n"
"buffer_load_dwordx4 v[128:131], v1, s[4:7], s97 offen offset:2048 sc1\n"
"v_mfma_f32_16x16x32_bf16 v[96:99], a[200:203], v[168:171], v[96:99]\n"
"ds_write_b128 v3, v[208:211] offset:15360\n"
"v_mfma_f32_16x16x32_bf16 v[100:103], a[204:207], v[168:171], v[100:103]\n"
"v_mfma_f32_16x16x32_bf16 v[88:91], a[208:211], v[144:147], v[88:91]\n"
"v_mfma_f32_16x16x32_bf16 v[92:95], a[212:215], v[144:147], v[92:95]\n"
"s_waitcnt lgkmcnt(0)\n"
"s_barrier\n"
"v_mfma_f32_16x16x32_bf16 v[96:99], a[216:219], v[144:147], v[96:99]\n"
"buffer_load_dwordx4 v[132:135], v1, s[4:7], s97 offen offset:3072 sc1\n"
"v_mfma_f32_16x16x32_bf16 v[100:103], a[220:223], v[144:147], v[100:103]\n"
"ds_read_b128 v[136:139], v5\n"
"v_mfma_f32_16x16x32_bf16 v[88:91], a[224:227], v[152:155], v[88:91]\n"
"s_add_u32 s98, s53, 0x440000\n"
"ds_read_b128 v[160:163], v6\n"
"v_mfma_f32_16x16x32_bf16 v[92:95], a[228:231], v[152:155], v[92:95]\n"
"ds_read_b128 v[140:143], v5 offset:1024\n"
"ds_read_b128 v[164:167], v6 offset:1024\n"
"v_mfma_f32_16x16x32_bf16 v[96:99], a[232:235], v[152:155], v[96:99]\n"
"buffer_load_dwordx4 v[144:147], v0, s[4:7], s98 offen sc1\n"
"v_mfma_f32_16x16x32_bf16 v[100:103], a[236:239], v[152:155], v[100:103]\n"
"ds_read_b128 v[148:151], v5 offset:2048\n"
"ds_read_b128 v[168:171], v6 offset:2048\n"
"v_mfma_f32_16x16x32_bf16 v[88:91], a[240:243], v[204:207], v[88:91]\n"
"ds_read_b128 v[152:155], v5 offset:3072\n"
"ds_read_b128 v[172:175], v6 offset:3072\n"
"v_mfma_f32_16x16x32_bf16 v[92:95], a[244:247], v[204:207], v[92:95]\n"
"v_mfma_f32_16x16x32_bf16 v[96:99], a[248:251], v[204:207], v[96:99]\n"
"buffer_load_dwordx4 v[156:159], v0, s[4:7], s98 offen offset:1024 sc1\n"
"v_mfma_f32_16x16x32_bf16 v[100:103], a[252:255], v[204:207], v[100:103]\n"
"s_waitcnt lgkmcnt(4)\n"
"v_add_f32_e32 v136, v136, v160\n"
"v_add_f32_e32 v137, v137, v161\n"
"v_add_f32_e32 v138, v138, v162\n"
"v_add_f32_e32 v139, v139, v163\n"
"v_add_f32_e32 v140, v140, v164\n"
"v_add_f32_e32 v141, v141, v165\n"
"v_add_f32_e32 v142, v142, v166\n"
"v_add_f32_e32 v143, v143, v167\n"
"s_waitcnt lgkmcnt(0)\n"
"v_add_f32_e32 v148, v148, v168\n"
"v_add_f32_e32 v149, v149, v169\n"
"v_add_f32_e32 v150, v150, v170\n"
"v_add_f32_e32 v151, v151, v171\n"
"v_add_f32_e32 v152, v152, v172\n"
"v_add_f32_e32 v153, v153, v173\n"
"v_add_f32_e32 v154, v154, v174\n"
"v_add_f32_e32 v155, v155, v175\n"
"buffer_load_dwordx4 v[160:163], v0, s[4:7], s98 offen offset:2048 sc1\n"
"buffer_load_dwordx4 v[164:167], v0, s[4:7], s98 offen offset:3072 sc1\n"
"buffer_load_dwordx4 v[168:171], v1, s[4:7], s98 offen sc1\n"
"buffer_load_dwordx4 v[172:175], v1, s[4:7], s98 offen offset:1024 sc1\n"
"buffer_load_dwordx4 v[176:179], v1, s[4:7], s98 offen offset:2048 sc1\n"
"buffer_load_dwordx4 v[180:183], v1, s[4:7], s98 offen offset:3072 sc1\n"
"s_branch .Lmk_loop\n"
".Lmk_f1s:\n"
";\n"
"s_mov_b32 s1, 0x10000\n"
".Lmk_f1l:\n"
"buffer_load_dwordx2 v[250:251], v12, s[4:7], s98 offen sc1\n"
"s_waitcnt vmcnt(0)\n"
"v_cmp_ne_u64_e32 vcc, v[250:251], v[18:19]\n"
"s_cbranch_vccz .Lmk_f1r\n"
"s_sub_u32 s1, s1, 1\n"
"s_cbranch_scc0 .Lmk_f1l\n"
"s_branch .Lmk_f1r\n"
".Lmk_f2s:\n"
";\n"
"s_mov_b32 s1, 0x10000\n"
".Lmk_f2l:\n"
"buffer_load_dwordx2 v[254:255], v12, s[4:7], s95 offen sc1\n"
"s_waitcnt vmcnt(0)\n"
"v_cmp_ne_u64_e32 vcc, v[254:255], v[18:19]\n"
"s_cbranch_vccz .Lmk_f2r\n"
"s_sub_u32 s1, s1, 1\n"
"s_cbranch_scc0 .Lmk_f2l\n"
"s_branch .Lmk_f2r\n"
".Lmk_f5s:\n"
";\n"
"s_mov_b32 s1, 0x10000\n"
".Lmk_f5l:\n"
"buffer_load_dwordx2 v[252:253], v41, s[4:7], s97 offen sc1\n"
"s_waitcnt vmcnt(0)\n"
"v_cmp_ne_u64_e32 vcc, v[252:253], v[18:19]\n"
"s_cbranch_vccz .Lmk_f5r\n"
"s_sub_u32 s1, s1, 1\n"
"s_cbranch_scc0 .Lmk_f5l\n"
"s_branch .Lmk_f5r\n"
".Lmk_w7s:\n"
"buffer_store_dwordx4 a[24:27], v0, s[4:7], s98 offen sc1\n"
"s_branch .Lmk_w7r\n"
".Lmk_w8s:\n"
"buffer_store_dwordx4 a[28:31], v0, s[4:7], s98 offen sc1\n"
"s_branch .Lmk_w8r\n"
".Lmk_w9s:\n"
"buffer_store_dwordx4 a[32:35], v0, s[4:7], s96 offen sc1\n"
"s_branch .Lmk_w9r\n"
".Lmk_w10s:\n"
"buffer_store_dwordx4 a[36:39], v0, s[4:7], s96 offen sc1\n"
"s_branch .Lmk_w10r\n"
".Lmk_w11s:\n"
"buffer_store_dwordx4 a[40:43], v0, s[4:7], s96 offen sc1\n"
"s_branch .Lmk_w11r\n"
".Lmk_w12s:\n"
"buffer_store_dwordx4 a[44:47], v0, s[4:7], s96 offen sc1\n"
"s_branch .Lmk_w12r\n"
".Lmk_f6s:\n"
";\n"
"s_mov_b32 s1, 0x10000\n"
".Lmk_f6l:\n"
"buffer_load_dwordx2 v[254:255], v13, s[4:7], s96 offen sc1\n"
"s_waitcnt vmcnt(0)\n"
"v_cmp_ne_u64_e32 vcc, v[254:255], v[18:19]\n"
"s_cbranch_vccz .Lmk_f6r\n"
"s_sub_u32 s1, s1, 1\n"
"s_cbranch_scc0 .Lmk_f6l\n"
"s_branch .Lmk_f6r\n"
".Lmk_f3s:\n"
";\n"
"s_mov_b32 s1, 0x10000\n"
".Lmk_f3l:\n"
"buffer_load_dwordx2 v[250:251], off, s[4:7], s95 sc1\n"
"s_waitcnt vmcnt(0)\n"
"v_cmp_ne_u64_e32 vcc, v[250:251], v[18:19]\n"
"s_cbranch_vccz .Lmk_f3r\n"
"s_sub_u32 s1, s1, 1\n"
"s_cbranch_scc0 .Lmk_f3l\n"
"s_branch .Lmk_f3r\n"
".Lmk_x23s:\n"
"s_nop 3\n"
"v_mov_b32_e32 v24, 0xff800000\n"
"v_mov_b32_e32 v25, 0xff800000\n"
"v_mov_b32_e32 v26, 0xff800000\n"
"v_mov_b32_e32 v27, 0xff800000\n"
"v_mov_b32_e32 v36, 0xff800000\n"
"v_mov_b32_e32 v37, 0xff800000\n"
"v_mov_b32_e32 v38, 0xff800000\n"
"v_mov_b32_e32 v39, 0xff800000\n"
"s_nop 1\n"
"s_branch .Lmk_x23r\n"
".Lmk_x24s:\n"
"s_nop 3\n"
"v_mov_b32_e32 v24, 0xff800000\n"
"v_mov_b32_e32 v25, 0xff800000\n"
"v_mov_b32_e32 v26, 0xff800000\n"
"v_mov_b32_e32 v27, 0xff800000\n"
"v_mov_b32_e32 v36, 0xff800000\n"
"v_mov_b32_e32 v37, 0xff800000\n"
"v_mov_b32_e32 v38, 0xff800000\n"
"v_mov_b32_e32 v39, 0xff800000\n"
"s_nop 1\n"
"s_branch .Lmk_x24r\n"
".Lmk_x25s:\n"
"s_nop 3\n"
"v_mov_b32_e32 v24, 0xff800000\n"
"v_mov_b32_e32 v25, 0xff800000\n"
"v_mov_b32_e32 v26, 0xff800000\n"
"v_mov_b32_e32 v27, 0xff800000\n"
"v_mov_b32_e32 v36, 0xff800000\n"
"v_mov_b32_e32 v37, 0xff800000\n"
"v_mov_b32_e32 v38, 0xff800000\n"
"v_mov_b32_e32 v39, 0xff800000\n"
"s_nop 1\n"
"s_branch .Lmk_x25r\n"
".Lmk_x26s:\n"
"s_nop 3\n"
"v_mov_b32_e32 v24, 0xff800000\n"
"v_mov_b32_e32 v25, 0xff800000\n"
"v_mov_b32_e32 v26, 0xff800000\n"
"v_mov_b32_e32 v27, 0xff800000\n"
"v_mov_b32_e32 v36, 0xff800000\n"
"v_mov_b32_e32 v37, 0xff800000\n"
"v_mov_b32_e32 v38, 0xff800000\n"
"v_mov_b32_e32 v39, 0xff800000\n"
"s_nop 1\n"
"s_branch .Lmk_x26r\n"
".Lmk_f19s:\n"
";\n"
"s_mov_b32 s1, 0x10000\n"
".Lmk_f19l:\n"
"buffer_load_dwordx2 v[238:239], off, s[4:7], s97 sc1\n"
"s_waitcnt vmcnt(0)\n"
"v_cmp_ne_u64_e32 vcc, v[238:239], v[18:19]\n"
"s_cbranch_vccz .Lmk_f19r\n"
"s_sub_u32 s1, s1, 1\n"
"s_cbranch_scc0 .Lmk_f19l\n"
"s_branch .Lmk_f19r\n"
".Lmk_b21:\n"
"s_cmp_lg_u32 s2, 0\n"
"s_cbranch_scc1 .Lmk_k27\n"
"s_mov_b32 s2, 1\n"
"s_waitcnt vmcnt(0) lgkmcnt(0)\n"
"s_nop 7\n"
"s_mov_b32 s97, 11\n"
"v_add_u32_e32 v248, 0x3800, v15\n"
"v_add_u32_e32 v247, 0x3800, v252\n"
"ds_read_b128 v[36:39], v15 offset:24576\n"
"ds_read_b128 v[48:51], v252 offset:24832\n"
"s_waitcnt lgkmcnt(0)\n"
"v_mfma_f32_16x16x32_bf16 v[52:55], v[36:39], v[28:31], 0\n"
"v_mfma_f32_16x16x32_bf16 v[56:59], v[36:39], v[32:35], 0\n"
"v_mfma_f32_16x16x32_bf16 v[36:39], v[48:51], v[28:31], 0\n"
"v_mfma_f32_16x16x32_bf16 v[60:63], v[48:51], v[32:35], 0\n"
".Lmk_b21l:\n"
"ds_read_b128 v[48:51], v248 offset:8192\n"
"ds_read_b128 v[64:67], v247 offset:8448\n"
"s_cmp_gt_i32 s78, s97\n"
"s_cselect_b32 s96, 0xff800000, 0\n"
"v_mov_b32_e32 v72, s96\n"
"v_mov_b32_e32 v73, s96\n"
"v_mov_b32_e32 v74, s96\n"
"v_mov_b32_e32 v75, s96\n"
"v_subrev_u32_e32 v248, 0x800, v248\n"
"v_subrev_u32_e32 v247, 0x800, v247\n"
"s_waitcnt lgkmcnt(0)\n"
"v_mfma_f32_16x16x32_bf16 v[76:79], v[48:51], v[28:31], v[72:75]\n"
"v_mfma_f32_16x16x32_bf16 v[80:83], v[48:51], v[32:35], v[72:75]\n"
"v_mfma_f32_16x16x32_bf16 v[48:51], v[64:67], v[28:31], v[72:75]\n"
"v_mfma_f32_16x16x32_bf16 v[84:87], v[64:67], v[32:35], v[72:75]\n"
"s_nop 7\n"
"s_cmp_lg_u32 s97, 4\n"
"s_cbranch_scc1 .Lmk_b21m\n"
"v_cndmask_b32_e64 v76, v52, v76, s[20:21]\n"
"v_cndmask_b32_e64 v77, v53, v77, s[22:23]\n"
"v_cndmask_b32_e64 v78, v54, v78, s[24:25]\n"
"v_cndmask_b32_e64 v79, v55, v79, s[26:27]\n"
"v_cndmask_b32_e64 v48, v36, v48, s[28:29]\n"
"v_cndmask_b32_e64 v49, v37, v49, s[30:31]\n"
"v_cndmask_b32_e64 v50, v38, v50, s[32:33]\n"
"v_cndmask_b32_e64 v51, v39, v51, s[34:35]\n"
"v_cndmask_b32_e64 v80, v56, v80, s[36:37]\n"
"v_cndmask_b32_e64 v81, v57, v81, s[38:39]\n"
"v_cndmask_b32_e64 v82, v58, v82, s[40:41]\n"
"v_cndmask_b32_e64 v83, v59, v83, s[42:43]\n"
"v_cndmask_b32_e64 v84, v60, v84, s[44:45]\n"
"v_cndmask_b32_e64 v85, v61, v85, s[46:47]\n"
"v_cndmask_b32_e64 v86, v62, v86, s[48:49]\n"
"v_cndmask_b32_e64 v87, v63, v87, s[50:51]\n"
".Lmk_b21m:\n"
"v_max3_f32 v255, v255, v76, v77\n"
"v_max3_f32 v254, v254, v80, v81\n"
"v_max3_f32 v255, v255, v78, v79\n"
"v_max3_f32 v254, v254, v82, v83\n"
"v_max3_f32 v255, v255, v48, v49\n"
"v_max3_f32 v254, v254, v84, v85\n"
"v_max3_f32 v255, v255, v50, v51\n"
"v_max3_f32 v254, v254, v86, v87\n"
"s_sub_u32 s97, s97, 1\n"
"s_cmp_ge_i32 s97, 4\n"
"s_cbranch_scc1 .Lmk_b21l\n"
"v_mov_b32_e32 v248, v255\n"
"v_mov_b32_e32 v247, v254\n"
"s_nop 0\n"
"v_permlane16_swap_b32_e32 v255, v248\n"
"v_permlane16_swap_b32_e32 v254, v247\n"
"v_max_f32_e32 v255, v255, v248\n"
"v_max_f32_e32 v254, v254, v247\n"
"v_mov_b32_e32 v248, v255\n"
"v_mov_b32_e32 v247, v254\n"
"s_nop 0\n"
"v_permlane32_swap_b32_e32 v255, v248\n"
"v_permlane32_swap_b32_e32 v254, v247\n"
"v_max_f32_e32 v255, v255, v248\n"
"v_max_f32_e32 v254, v254, v247\n"
"v_mov_b32_e32 v250, v255\n"
"v_mov_b32_e32 v249, v254\n"
"s_branch .Lmk_a20\n"
".Lmk_x30s:\n"
"s_nop 3\n"
"v_mov_b32_e32 v116, 0xff800000\n"
"v_mov_b32_e32 v117, 0xff800000\n"
"v_mov_b32_e32 v118, 0xff800000\n"
"v_mov_b32_e32 v119, 0xff800000\n"
"v_mov_b32_e32 v120, 0xff800000\n"
"v_mov_b32_e32 v121, 0xff800000\n"
"v_mov_b32_e32 v122, 0xff800000\n"
"v_mov_b32_e32 v123, 0xff800000\n"
"s_nop 1\n"
"s_branch .Lmk_x30r\n"
".Lmk_x31s:\n"
"s_nop 3\n"
"v_mov_b32_e32 v116, 0xff800000\n"
"v_mov_b32_e32 v117, 0xff800000\n"
"v_mov_b32_e32 v118, 0xff800000\n"
"v_mov_b32_e32 v119, 0xff800000\n"
"v_mov_b32_e32 v120, 0xff800000\n"
"v_mov_b32_e32 v121, 0xff800000\n"
"v_mov_b32_e32 v122, 0xff800000\n"
"v_mov_b32_e32 v123, 0xff800000\n"
"s_nop 1\n"
"s_branch .Lmk_x31r\n"
".Lmk_x32s:\n"
"s_nop 3\n"
"v_mov_b32_e32 v116, 0xff800000\n"
"v_mov_b32_e32 v117, 0xff800000\n"
"v_mov_b32_e32 v118, 0xff800000\n"
"v_mov_b32_e32 v119, 0xff800000\n"
"v_mov_b32_e32 v120, 0xff800000\n"
"v_mov_b32_e32 v121, 0xff800000\n"
"v_mov_b32_e32 v122, 0xff800000\n"
"v_mov_b32_e32 v123, 0xff800000\n"
"s_nop 1\n"
"s_branch .Lmk_x32r\n"
".Lmk_f4s:\n"
";\n"
"s_mov_b32 s1, 0x10000\n"
".Lmk_f4l:\n"
"buffer_load_dwordx2 v[246:247], v12, s[4:7], s96 offen sc1\n"
"s_waitcnt vmcnt(0)\n"
"v_cmp_ne_u64_e32 vcc, v[246:247], v[18:19]\n"
"s_cbranch_vccz .Lmk_f4r\n"
"s_sub_u32 s1, s1, 1\n"
"s_cbranch_scc0 .Lmk_f4l\n"
"s_branch .Lmk_f4r\n"
".Lmk_b29:\n"
"s_cmp_lg_u32 s2, 0\n"
"s_cbranch_scc1 .Lmk_k34\n"
"s_mov_b32 s2, 1\n"
"s_waitcnt vmcnt(0) lgkmcnt(0)\n"
"s_nop 7\n"
"s_mov_b32 s96, 7\n"
"v_add_u32_e32 v248, 0x3800, v15\n"
"v_add_u32_e32 v247, 0x3800, v252\n"
"ds_read_b128 v[176:179], v15 offset:16384\n"
"ds_read_b128 v[184:187], v252 offset:16640\n"
"s_waitcnt lgkmcnt(0)\n"
"v_mfma_f32_16x16x32_bf16 v[196:199], v[176:179], v[92:95], 0\n"
"v_mfma_f32_16x16x32_bf16 v[208:211], v[176:179], v[96:99], 0\n"
"v_mfma_f32_16x16x32_bf16 v[176:179], v[184:187], v[92:95], 0\n"
"v_mfma_f32_16x16x32_bf16 v[220:223], v[184:187], v[96:99], 0\n"
".Lmk_b29l:\n"
"ds_read_b128 v[184:187], v248\n"
"ds_read_b128 v[224:227], v247 offset:256\n"
"s_cmp_gt_i32 s78, s96\n"
"s_cselect_b32 s95, 0xff800000, 0\n"
"v_mov_b32_e32 v228, s95\n"
"v_mov_b32_e32 v229, s95\n"
"v_mov_b32_e32 v230, s95\n"
"v_mov_b32_e32 v231, s95\n"
"v_subrev_u32_e32 v248, 0x800, v248\n"
"v_subrev_u32_e32 v247, 0x800, v247\n"
"s_waitcnt lgkmcnt(0)\n"
"v_mfma_f32_16x16x32_bf16 v[232:235], v[184:187], v[92:95], v[228:231]\n"
"v_mfma_f32_16x16x32_bf16 v[236:239], v[184:187], v[96:99], v[228:231]\n"
"v_mfma_f32_16x16x32_bf16 v[184:187], v[224:227], v[92:95], v[228:231]\n"
"v_mfma_f32_16x16x32_bf16 v[240:243], v[224:227], v[96:99], v[228:231]\n"
"s_nop 7\n"
"s_cmp_lg_u32 s96, 0\n"
"s_cbranch_scc1 .Lmk_b29m\n"
"v_cndmask_b32_e64 v232, v196, v232, s[20:21]\n"
"v_cndmask_b32_e64 v233, v197, v233, s[22:23]\n"
"v_cndmask_b32_e64 v234, v198, v234, s[24:25]\n"
"v_cndmask_b32_e64 v235, v199, v235, s[26:27]\n"
"v_cndmask_b32_e64 v184, v176, v184, s[28:29]\n"
"v_cndmask_b32_e64 v185, v177, v185, s[30:31]\n"
"v_cndmask_b32_e64 v186, v178, v186, s[32:33]\n"
"v_cndmask_b32_e64 v187, v179, v187, s[34:35]\n"
"v_cndmask_b32_e64 v236, v208, v236, s[36:37]\n"
"v_cndmask_b32_e64 v237, v209, v237, s[38:39]\n"
"v_cndmask_b32_e64 v238, v210, v238, s[40:41]\n"
"v_cndmask_b32_e64 v239, v211, v239, s[42:43]\n"
"v_cndmask_b32_e64 v240, v220, v240, s[44:45]\n"
"v_cndmask_b32_e64 v241, v221, v241, s[46:47]\n"
"v_cndmask_b32_e64 v242, v222, v242, s[48:49]\n"
"v_cndmask_b32_e64 v243, v223, v243, s[50:51]\n"
".Lmk_b29m:\n"
"v_max3_f32 v255, v255, v232, v233\n"
"v_max3_f32 v254, v254, v236, v237\n"
"v_max3_f32 v255, v255, v234, v235\n"
"v_max3_f32 v254, v254, v238, v239\n"
"v_max3_f32 v255, v255, v184, v185\n"
"v_max3_f32 v254, v254, v240, v241\n"
"v_max3_f32 v255, v255, v186, v187\n"
"v_max3_f32 v254, v254, v242, v243\n"
"s_sub_u32 s96, s96, 1\n"
"s_cmp_ge_i32 s96, 0\n"
"s_cbranch_scc1 .Lmk_b29l\n"
"v_mov_b32_e32 v248, v255\n"
"v_mov_b32_e32 v247, v254\n"
"s_nop 0\n"
"v_permlane16_swap_b32_e32 v255, v248\n"
"v_permlane16_swap_b32_e32 v254, v247\n"
"v_max_f32_e32 v255, v255, v248\n"
"v_max_f32_e32 v254, v254, v247\n"
"v_mov_b32_e32 v248, v255\n"
"v_mov_b32_e32 v247, v254\n"
"s_nop 0\n"
"v_permlane32_swap_b32_e32 v255, v248\n"
"v_permlane32_swap_b32_e32 v254, v247\n"
"v_max_f32_e32 v255, v255, v248\n"
"v_max_f32_e32 v254, v254, v247\n"
"v_mov_b32_e32 v250, v255\n"
"v_mov_b32_e32 v249, v254\n"
"s_branch .Lmk_a28\n"
".Lmk_p35s:\n"
";\n"
"s_mov_b32 s1, 0x10000\n"
".Lmk_p35l:\n"
"s_add_u32 s97, s56, 0x40000\n"
"buffer_load_dwordx4 v[140:143], v0, s[4:7], s97 offen sc1\n"
"buffer_load_dwordx4 v[136:139], v0, s[4:7], s97 offen offset:1024 sc1\n"
"buffer_load_dwordx4 v[120:123], v0, s[4:7], s97 offen offset:2048 sc1\n"
"buffer_load_dwordx4 v[108:111], v0, s[4:7], s97 offen offset:3072 sc1\n"
"buffer_load_dwordx4 v[148:151], v1, s[4:7], s97 offen sc1\n"
"buffer_load_dwordx4 v[160:163], v1, s[4:7], s97 offen offset:1024 sc1\n"
"buffer_load_dwordx4 v[164:167], v1, s[4:7], s97 offen offset:2048 sc1\n"
"buffer_load_dwordx4 v[128:131], v1, s[4:7], s97 offen offset:3072 sc1\n"
"s_add_u32 s97, s57, 0x40000\n"
"buffer_load_dwordx4 v[216:219], v0, s[4:7], s97 offen sc1\n"
"buffer_load_dwordx4 v[212:215], v0, s[4:7], s97 offen offset:1024 sc1\n"
"buffer_load_dwordx4 v[192:195], v0, s[4:7], s97 offen offset:2048 sc1\n"
"buffer_load_dwordx4 v[100:103], v0, s[4:7], s97 offen offset:3072 sc1\n"
"buffer_load_dwordx4 v[188:191], v1, s[4:7], s97 offen sc1\n"
"buffer_load_dwordx4 v[104:107], v1, s[4:7], s97 offen offset:1024 sc1\n"
"buffer_load_dwordx4 v[112:115], v1, s[4:7], s97 offen offset:2048 sc1\n"
"buffer_load_dwordx4 v[124:127], v1, s[4:7], s97 offen offset:3072 sc1\n"
"s_add_u32 s97, s59, 0x40000\n"
"buffer_load_dwordx4 v[144:147], v0, s[4:7], s97 offen sc1\n"
"buffer_load_dwordx4 v[152:155], v0, s[4:7], s97 offen offset:1024 sc1\n"
"buffer_load_dwordx4 v[168:171], v0, s[4:7], s97 offen offset:2048 sc1\n"
"buffer_load_dwordx4 v[116:119], v0, s[4:7], s97 offen offset:3072 sc1\n"
"s_waitcnt vmcnt(0)\n"
"v_max_u32_e32 v255, v140, v141\n"
"v_max3_u32 v255, v255, v142, v143\n"
"v_max3_u32 v255, v255, v136, v137\n"
"v_max3_u32 v255, v255, v138, v139\n"
"v_max3_u32 v255, v255, v120, v121\n"
"v_max3_u32 v255, v255, v122, v123\n"
"v_max3_u32 v255, v255, v108, v109\n"
"v_max3_u32 v255, v255, v110, v111\n"
"v_max3_u32 v255, v255, v148, v149\n"
"v_max3_u32 v255, v255, v150, v151\n"
"v_max3_u32 v255, v255, v160, v161\n"
"v_max3_u32 v255, v255, v162, v163\n"
"v_max3_u32 v255, v255, v164, v165\n"
"v_max3_u32 v255, v255, v166, v167\n"
"v_max3_u32 v255, v255, v128, v129\n"
"v_max3_u32 v255, v255, v130, v131\n"
"v_cmp_eq_u32_e32 vcc, -1, v255\n"
"s_cbranch_vccz .Lmk_p35k\n"
"s_sub_u32 s1, s1, 1\n"
"s_cbranch_scc0 .Lmk_p35l\n"
".Lmk_p35k:\n"
"v_mfma_f32_16x16x32_bf16 v[88:91], a[128:131], v[140:143], 0\n"
"v_mfma_f32_16x16x32_bf16 v[92:95], a[132:135], v[140:143], 0\n"
"v_mfma_f32_16x16x32_bf16 v[96:99], a[136:139], v[140:143], 0\n"
"v_mfma_f32_16x16x32_bf16 v[156:159], a[140:143], v[140:143], 0\n"
"v_mfma_f32_16x16x32_bf16 v[88:91], a[144:147], v[136:139], v[88:91]\n"
"v_mfma_f32_16x16x32_bf16 v[92:95], a[148:151], v[136:139], v[92:95]\n"
"v_mfma_f32_16x16x32_bf16 v[96:99], a[152:155], v[136:139], v[96:99]\n"
"v_mfma_f32_16x16x32_bf16 v[156:159], a[156:159], v[136:139], v[156:159]\n"
"v_mfma_f32_16x16x32_bf16 v[88:91], a[160:163], v[120:123], v[88:91]\n"
"v_mfma_f32_16x16x32_bf16 v[92:95], a[164:167], v[120:123], v[92:95]\n"
"v_mfma_f32_16x16x32_bf16 v[96:99], a[168:171], v[120:123], v[96:99]\n"
"v_mfma_f32_16x16x32_bf16 v[156:159], a[172:175], v[120:123], v[156:159]\n"
"v_mfma_f32_16x16x32_bf16 v[88:91], a[176:179], v[108:111], v[88:91]\n"
"v_mfma_f32_16x16x32_bf16 v[92:95], a[180:183], v[108:111], v[92:95]\n"
"v_mfma_f32_16x16x32_bf16 v[96:99], a[184:187], v[108:111], v[96:99]\n"
"v_mfma_f32_16x16x32_bf16 v[156:159], a[188:191], v[108:111], v[156:159]\n"
"v_mfma_f32_16x16x32_bf16 v[88:91], a[192:195], v[148:151], v[88:91]\n"
"v_mfma_f32_16x16x32_bf16 v[92:95], a[196:199], v[148:151], v[92:95]\n"
"v_mfma_f32_16x16x32_bf16 v[96:99], a[200:203], v[148:151], v[96:99]\n"
"v_mfma_f32_16x16x32_bf16 v[156:159], a[204:207], v[148:151], v[156:159]\n"
"v_mfma_f32_16x16x32_bf16 v[88:91], a[208:211], v[160:163], v[88:91]\n"
"v_mfma_f32_16x16x32_bf16 v[92:95], a[212:215], v[160:163], v[92:95]\n"
"v_mfma_f32_16x16x32_bf16 v[96:99], a[216:219], v[160:163], v[96:99]\n"
"v_mfma_f32_16x16x32_bf16 v[156:159], a[220:223], v[160:163], v[156:159]\n"
"v_mfma_f32_16x16x32_bf16 v[88:91], a[224:227], v[164:167], v[88:91]\n"
"v_mfma_f32_16x16x32_bf16 v[92:95], a[228:231], v[164:167], v[92:95]\n"
"v_mfma_f32_16x16x32_bf16 v[96:99], a[232:235], v[164:167], v[96:99]\n"
"v_mfma_f32_16x16x32_bf16 v[156:159], a[236:239], v[164:167], v[156:159]\n"
"v_mfma_f32_16x16x32_bf16 v[88:91], a[240:243], v[128:131], v[88:91]\n"
"v_mfma_f32_16x16x32_bf16 v[92:95], a[244:247], v[128:131], v[92:95]\n"
"v_mfma_f32_16x16x32_bf16 v[96:99], a[248:251], v[128:131], v[96:99]\n"
"v_mfma_f32_16x16x32_bf16 v[156:159], a[252:255], v[128:131], v[156:159]\n"
"s_add_u32 s97, s58, 0x40000\n"
"v_mov_b32_e32 v255, 0x80000000\n"
"buffer_load_dwordx4 v[140:143], v0, s[4:7], s97 offen sc1\n"
"buffer_load_dwordx4 v[136:139], v0, s[4:7], s97 offen offset:1024 sc1\n"
"buffer_load_dwordx4 v[120:123], v0, s[4:7], s97 offen offset:2048 sc1\n"
"buffer_load_dwordx4 v[108:111], v0, s[4:7], s97 offen offset:3072 sc1\n"
"buffer_store_dword v255, v255, s[4:7], 0 offen\n"
"buffer_store_dword v255, v255, s[4:7], 0 offen\n"
"buffer_load_dwordx4 v[148:151], v1, s[4:7], s97 offen sc1\n"
"buffer_load_dwordx4 v[160:163], v1, s[4:7], s97 offen offset:1024 sc1\n"
"buffer_load_dwordx4 v[164:167], v1, s[4:7], s97 offen offset:2048 sc1\n"
"buffer_load_dwordx4 v[128:131], v1, s[4:7], s97 offen offset:3072 sc1\n"
"buffer_store_dword v255, v255, s[4:7], 0 offen\n"
"buffer_store_dword v255, v255, s[4:7], 0 offen\n"
"buffer_store_dword v255, v255, s[4:7], 0 offen\n"
"buffer_store_dword v255, v255, s[4:7], 0 offen\n"
"s_nop 7\n"
"s_branch .Lmk_p35r\n"
".Lmk_p36s:\n"
";\n"
"s_mov_b32 s1, 0x10000\n"
".Lmk_p36l:\n"
"s_add_u32 s98, s57, 0x40000\n"
"buffer_load_dwordx4 v[144:147], v0, s[4:7], s98 offen sc1\n"
"buffer_load_dwordx4 v[152:155], v0, s[4:7], s98 offen offset:1024 sc1\n"
"buffer_load_dwordx4 v[168:171], v0, s[4:7], s98 offen offset:2048 sc1\n"
"buffer_load_dwordx4 v[116:119], v0, s[4:7], s98 offen offset:3072 sc1\n"
"buffer_load_dwordx4 v[180:183], v1, s[4:7], s98 offen sc1\n"
"buffer_load_dwordx4 v[88:91], v1, s[4:7], s98 offen offset:1024 sc1\n"
"buffer_load_dwordx4 v[92:95], v1, s[4:7], s98 offen offset:2048 sc1\n"
"buffer_load_dwordx4 v[100:103], v1, s[4:7], s98 offen offset:3072 sc1\n"
"s_add_u32 s98, s58, 0x40000\n"
"buffer_load_dwordx4 v[140:143], v0, s[4:7], s98 offen sc1\n"
"buffer_load_dwordx4 v[136:139], v0, s[4:7], s98 offen offset:1024 sc1\n"
"buffer_load_dwordx4 v[120:123], v0, s[4:7], s98 offen offset:2048 sc1\n"
"buffer_load_dwordx4 v[108:111], v0, s[4:7], s98 offen offset:3072 sc1\n"
"buffer_load_dwordx4 v[148:151], v1, s[4:7], s98 offen sc1\n"
"buffer_load_dwordx4 v[160:163], v1, s[4:7], s98 offen offset:1024 sc1\n"
"buffer_load_dwordx4 v[164:167], v1, s[4:7], s98 offen offset:2048 sc1\n"
"buffer_load_dwordx4 v[128:131], v1, s[4:7], s98 offen offset:3072 sc1\n"
"s_waitcnt vmcnt(0)\n"
"v_max_u32_e32 v244, v144, v145\n"
"v_max3_u32 v244, v244, v146, v147\n"
"v_max3_u32 v244, v244, v152, v153\n"
"v_max3_u32 v244, v244, v154, v155\n"
"v_max3_u32 v244, v244, v168, v169\n"
"v_max3_u32 v244, v244, v170, v171\n"
"v_max3_u32 v244, v244, v116, v117\n"
"v_max3_u32 v244, v244, v118, v119\n"
"v_max3_u32 v244, v244, v180, v181\n"
"v_max3_u32 v244, v244, v182, v183\n"
"v_max3_u32 v244, v244, v88, v89\n"
"v_max3_u32 v244, v244, v90, v91\n"
"v_max3_u32 v244, v244, v92, v93\n"
"v_max3_u32 v244, v244, v94, v95\n"
"v_max3_u32 v244, v244, v100, v101\n"
"v_max3_u32 v244, v244, v102, v103\n"
"v_cmp_eq_u32_e32 vcc, -1, v244\n"
"s_cbranch_vccz .Lmk_p36k\n"
"s_sub_u32 s1, s1, 1\n"
"s_cbranch_scc0 .Lmk_p36l\n"
".Lmk_p36k:\n"
"v_mfma_f32_16x16x32_bf16 v[132:135], a[128:131], v[144:147], 0\n"
"v_mfma_f32_16x16x32_bf16 v[172:175], a[132:135], v[144:147], 0\n"
"v_mfma_f32_16x16x32_bf16 v[176:179], a[136:139], v[144:147], 0\n"
"v_mfma_f32_16x16x32_bf16 v[184:187], a[140:143], v[144:147], 0\n"
"v_mfma_f32_16x16x32_bf16 v[132:135], a[144:147], v[152:155], v[132:135]\n"
"v_mfma_f32_16x16x32_bf16 v[172:175], a[148:151], v[152:155], v[172:175]\n"
"v_mfma_f32_16x16x32_bf16 v[176:179], a[152:155], v[152:155], v[176:179]\n"
"v_mfma_f32_16x16x32_bf16 v[184:187], a[156:159], v[152:155], v[184:187]\n"
"v_mfma_f32_16x16x32_bf16 v[132:135], a[160:163], v[168:171], v[132:135]\n"
"v_mfma_f32_16x16x32_bf16 v[172:175], a[164:167], v[168:171], v[172:175]\n"
"v_mfma_f32_16x16x32_bf16 v[176:179], a[168:171], v[168:171], v[176:179]\n"
"v_mfma_f32_16x16x32_bf16 v[184:187], a[172:175], v[168:171], v[184:187]\n"
"v_mfma_f32_16x16x32_bf16 v[132:135], a[176:179], v[116:119], v[132:135]\n"
"v_mfma_f32_16x16x32_bf16 v[172:175], a[180:183], v[116:119], v[172:175]\n"
"v_mfma_f32_16x16x32_bf16 v[176:179], a[184:187], v[116:119], v[176:179]\n"
"v_mfma_f32_16x16x32_bf16 v[184:187], a[188:191], v[116:119], v[184:187]\n"
"v_mfma_f32_16x16x32_bf16 v[132:135], a[192:195], v[180:183], v[132:135]\n"
"v_mfma_f32_16x16x32_bf16 v[172:175], a[196:199], v[180:183], v[172:175]\n"
"v_mfma_f32_16x16x32_bf16 v[176:179], a[200:203], v[180:183], v[176:179]\n"
"v_mfma_f32_16x16x32_bf16 v[184:187], a[204:207], v[180:183], v[184:187]\n"
"v_mfma_f32_16x16x32_bf16 v[132:135], a[208:211], v[88:91], v[132:135]\n"
"v_mfma_f32_16x16x32_bf16 v[172:175], a[212:215], v[88:91], v[172:175]\n"
"v_mfma_f32_16x16x32_bf16 v[176:179], a[216:219], v[88:91], v[176:179]\n"
"v_mfma_f32_16x16x32_bf16 v[184:187], a[220:223], v[88:91], v[184:187]\n"
"v_mfma_f32_16x16x32_bf16 v[132:135], a[224:227], v[92:95], v[132:135]\n"
"v_mfma_f32_16x16x32_bf16 v[172:175], a[228:231], v[92:95], v[172:175]\n"
"v_mfma_f32_16x16x32_bf16 v[176:179], a[232:235], v[92:95], v[176:179]\n"
"v_mfma_f32_16x16x32_bf16 v[184:187], a[236:239], v[92:95], v[184:187]\n"
"v_mfma_f32_16x16x32_bf16 v[132:135], a[240:243], v[100:103], v[132:135]\n"
"v_mfma_f32_16x16x32_bf16 v[172:175], a[244:247], v[100:103], v[172:175]\n"
"v_mfma_f32_16x16x32_bf16 v[176:179], a[248:251], v[100:103], v[176:179]\n"
"v_mfma_f32_16x16x32_bf16 v[184:187], a[252:255], v[100:103], v[184:187]\n"
"s_add_u32 s98, s59, 0x40000\n"
"v_mov_b32_e32 v244, 0x80000000\n"
"buffer_load_dwordx4 v[144:147], v0, s[4:7], s98 offen sc1\n"
"buffer_load_dwordx4 v[152:155], v0, s[4:7], s98 offen offset:1024 sc1\n"
"buffer_load_dwordx4 v[168:171], v0, s[4:7], s98 offen offset:2048 sc1\n"
"buffer_load_dwordx4 v[116:119], v0, s[4:7], s98 offen offset:3072 sc1\n"
"buffer_load_dwordx4 v[180:183], v1, s[4:7], s98 offen sc1\n"
"buffer_load_dwordx4 v[88:91], v1, s[4:7], s98 offen offset:1024 sc1\n"
"buffer_load_dwordx4 v[92:95], v1, s[4:7], s98 offen offset:2048 sc1\n"
"buffer_load_dwordx4 v[100:103], v1, s[4:7], s98 offen offset:3072 sc1\n"
"buffer_store_dword v244, v244, s[4:7], 0 offen\n"
"s_nop 7\n"
"s_branch .Lmk_p36r\n"
".Lmk_q38s:\n"
";\n"
"s_mov_b32 s1, 0x10000\n"
".Lmk_q38l:\n"
"s_add_u32 s97, s59, 0x40000\n"
"buffer_load_dwordx4 v[144:147], v0, s[4:7], s97 offen sc1\n"
"buffer_load_dwordx4 v[152:155], v0, s[4:7], s97 offen offset:1024 sc1\n"
"buffer_load_dwordx4 v[168:171], v0, s[4:7], s97 offen offset:2048 sc1\n"
"buffer_load_dwordx4 v[116:119], v0, s[4:7], s97 offen offset:3072 sc1\n"
"buffer_load_dwordx4 v[180:183], v1, s[4:7], s97 offen sc1\n"
"buffer_load_dwordx4 v[88:91], v1, s[4:7], s97 offen offset:1024 sc1\n"
"buffer_load_dwordx4 v[92:95], v1, s[4:7], s97 offen offset:2048 sc1\n"
"buffer_load_dwordx4 v[100:103], v1, s[4:7], s97 offen offset:3072 sc1\n"
"s_add_u32 s97, s56, 0\n"
"buffer_load_dwordx4 v[120:123], v0, s[4:7], s97 offen sc1\n"
"buffer_load_dwordx4 v[108:111], v0, s[4:7], s97 offen offset:1024 sc1\n"
"buffer_load_dwordx4 v[132:135], v0, s[4:7], s97 offen offset:2048 sc1\n"
"buffer_load_dwordx4 v[136:139], v0, s[4:7], s97 offen offset:3072 sc1\n"
"s_waitcnt vmcnt(0)\n"
"v_max_u32_e32 v255, v144, v145\n"
"v_max3_u32 v255, v255, v146, v147\n"
"v_max3_u32 v255, v255, v152, v153\n"
"v_max3_u32 v255, v255, v154, v155\n"
"v_max3_u32 v255, v255, v168, v169\n"
"v_max3_u32 v255, v255, v170, v171\n"
"v_max3_u32 v255, v255, v116, v117\n"
"v_max3_u32 v255, v255, v118, v119\n"
"v_cmp_eq_u32_e32 vcc, -1, v255\n"
"s_cbranch_vccz .Lmk_q38k\n"
"s_sub_u32 s1, s1, 1\n"
"s_cbranch_scc0 .Lmk_q38l\n"
".Lmk_q38k:\n"
"s_branch .Lmk_q38r\n"
".Lmk_p37s:\n"
";\n"
"s_mov_b32 s1, 0x10000\n"
".Lmk_p37l:\n"
"s_add_u32 s97, s58, 0x40000\n"
"buffer_load_dwordx4 v[128:131], v0, s[4:7], s97 offen sc1\n"
"buffer_load_dwordx4 v[140:143], v0, s[4:7], s97 offen offset:1024 sc1\n"
"buffer_load_dwordx4 v[148:151], v0, s[4:7], s97 offen offset:2048 sc1\n"
"buffer_load_dwordx4 v[156:159], v0, s[4:7], s97 offen offset:3072 sc1\n"
"buffer_load_dwordx4 v[160:163], v1, s[4:7], s97 offen sc1\n"
"buffer_load_dwordx4 v[164:167], v1, s[4:7], s97 offen offset:1024 sc1\n"
"buffer_load_dwordx4 v[172:175], v1, s[4:7], s97 offen offset:2048 sc1\n"
"buffer_load_dwordx4 v[176:179], v1, s[4:7], s97 offen offset:3072 sc1\n"
"s_add_u32 s97, s59, 0x40000\n"
"buffer_load_dwordx4 v[144:147], v0, s[4:7], s97 offen sc1\n"
"buffer_load_dwordx4 v[152:155], v0, s[4:7], s97 offen offset:1024 sc1\n"
"buffer_load_dwordx4 v[168:171], v0, s[4:7], s97 offen offset:2048 sc1\n"
"buffer_load_dwordx4 v[116:119], v0, s[4:7], s97 offen offset:3072 sc1\n"
"buffer_load_dwordx4 v[180:183], v1, s[4:7], s97 offen sc1\n"
"buffer_load_dwordx4 v[88:91], v1, s[4:7], s97 offen offset:1024 sc1\n"
"buffer_load_dwordx4 v[92:95], v1, s[4:7], s97 offen offset:2048 sc1\n"
"buffer_load_dwordx4 v[100:103], v1, s[4:7], s97 offen offset:3072 sc1\n"
"s_add_u32 s97, s56, 0\n"
"buffer_load_dwordx4 v[120:123], v0, s[4:7], s97 offen sc1\n"
"buffer_load_dwordx4 v[108:111], v0, s[4:7], s97 offen offset:1024 sc1\n"
"buffer_load_dwordx4 v[132:135], v0, s[4:7], s97 offen offset:2048 sc1\n"
"buffer_load_dwordx4 v[136:139], v0, s[4:7], s97 offen offset:3072 sc1\n"
"s_waitcnt vmcnt(0)\n"
"v_max_u32_e32 v255, v128, v129\n"
"v_max3_u32 v255, v255, v130, v131\n"
"v_max3_u32 v255, v255, v140, v141\n"
"v_max3_u32 v255, v255, v142, v143\n"
"v_max3_u32 v255, v255, v148, v149\n"
"v_max3_u32 v255, v255, v150, v151\n"
"v_max3_u32 v255, v255, v156, v157\n"
"v_max3_u32 v255, v255, v158, v159\n"
"v_max3_u32 v255, v255, v160, v161\n"
"v_max3_u32 v255, v255, v162, v163\n"
"v_max3_u32 v255, v255, v164, v165\n"
"v_max3_u32 v255, v255, v166, v167\n"
"v_max3_u32 v255, v255, v172, v173\n"
"v_max3_u32 v255, v255, v174, v175\n"
"v_max3_u32 v255, v255, v176, v177\n"
"v_max3_u32 v255, v255, v178, v179\n"
"v_cmp_eq_u32_e32 vcc, -1, v255\n"
"s_cbranch_vccz .Lmk_p37k\n"
"s_sub_u32 s1, s1, 1\n"
"s_cbranch_scc0 .Lmk_p37l\n"
".Lmk_p37k:\n"
"v_mfma_f32_16x16x32_bf16 v[96:99], a[128:131], v[128:131], 0\n"
"v_mfma_f32_16x16x32_bf16 v[104:107], a[132:135], v[128:131], 0\n"
"v_mfma_f32_16x16x32_bf16 v[112:115], a[136:139], v[128:131], 0\n"
"v_mfma_f32_16x16x32_bf16 v[124:127], a[140:143], v[128:131], 0\n"
"v_mfma_f32_16x16x32_bf16 v[96:99], a[144:147], v[140:143], v[96:99]\n"
"v_mfma_f32_16x16x32_bf16 v[104:107], a[148:151], v[140:143], v[104:107]\n"
"v_mfma_f32_16x16x32_bf16 v[112:115], a[152:155], v[140:143], v[112:115]\n"
"v_mfma_f32_16x16x32_bf16 v[124:127], a[156:159], v[140:143], v[124:127]\n"
"v_mfma_f32_16x16x32_bf16 v[96:99], a[160:163], v[148:151], v[96:99]\n"
"v_mfma_f32_16x16x32_bf16 v[104:107], a[164:167], v[148:151], v[104:107]\n"
"v_mfma_f32_16x16x32_bf16 v[112:115], a[168:171], v[148:151], v[112:115]\n"
"v_mfma_f32_16x16x32_bf16 v[124:127], a[172:175], v[148:151], v[124:127]\n"
"v_mfma_f32_16x16x32_bf16 v[96:99], a[176:179], v[156:159], v[96:99]\n"
"v_mfma_f32_16x16x32_bf16 v[104:107], a[180:183], v[156:159], v[104:107]\n"
"v_mfma_f32_16x16x32_bf16 v[112:115], a[184:187], v[156:159], v[112:115]\n"
"v_mfma_f32_16x16x32_bf16 v[124:127], a[188:191], v[156:159], v[124:127]\n"
"v_mfma_f32_16x16x32_bf16 v[96:99], a[192:195], v[160:163], v[96:99]\n"
"v_mfma_f32_16x16x32_bf16 v[104:107], a[196:199], v[160:163], v[104:107]\n"
"v_mfma_f32_16x16x32_bf16 v[112:115], a[200:203], v[160:163], v[112:115]\n"
"v_mfma_f32_16x16x32_bf16 v[124:127], a[204:207], v[160:163], v[124:127]\n"
"v_mfma_f32_16x16x32_bf16 v[96:99], a[208:211], v[164:167], v[96:99]\n"
"v_mfma_f32_16x16x32_bf16 v[104:107], a[212:215], v[164:167], v[104:107]\n"
"v_mfma_f32_16x16x32_bf16 v[112:115], a[216:219], v[164:167], v[112:115]\n"
"v_mfma_f32_16x16x32_bf16 v[124:127], a[220:223], v[164:167], v[124:127]\n"
"v_mfma_f32_16x16x32_bf16 v[96:99], a[224:227], v[172:175], v[96:99]\n"
"v_mfma_f32_16x16x32_bf16 v[104:107], a[228:231], v[172:175], v[104:107]\n"
"v_mfma_f32_16x16x32_bf16 v[112:115], a[232:235], v[172:175], v[112:115]\n"
"v_mfma_f32_16x16x32_bf16 v[124:127], a[236:239], v[172:175], v[124:127]\n"
"v_mfma_f32_16x16x32_bf16 v[96:99], a[240:243], v[176:179], v[96:99]\n"
"v_mfma_f32_16x16x32_bf16 v[104:107], a[244:247], v[176:179], v[104:107]\n"
"v_mfma_f32_16x16x32_bf16 v[112:115], a[248:251], v[176:179], v[112:115]\n"
"v_mfma_f32_16x16x32_bf16 v[124:127], a[252:255], v[176:179], v[124:127]\n"
"s_nop 7\n"
"s_branch .Lmk_p37r\n"
".Lmk_q39s:\n"
";\n"
"s_mov_b32 s1, 0x10000\n"
".Lmk_q39l:\n"
"s_add_u32 s97, s59, 0x40000\n"
"buffer_load_dwordx4 v[180:183], v1, s[4:7], s97 offen sc1\n"
"buffer_load_dwordx4 v[88:91], v1, s[4:7], s97 offen offset:1024 sc1\n"
"buffer_load_dwordx4 v[92:95], v1, s[4:7], s97 offen offset:2048 sc1\n"
"buffer_load_dwordx4 v[100:103], v1, s[4:7], s97 offen offset:3072 sc1\n"
"s_add_u32 s97, s56, 0\n"
"buffer_load_dwordx4 v[120:123], v0, s[4:7], s97 offen sc1\n"
"buffer_load_dwordx4 v[108:111], v0, s[4:7], s97 offen offset:1024 sc1\n"
"buffer_load_dwordx4 v[132:135], v0, s[4:7], s97 offen offset:2048 sc1\n"
"buffer_load_dwordx4 v[136:139], v0, s[4:7], s97 offen offset:3072 sc1\n"
"buffer_load_dwordx4 v[128:131], v1, s[4:7], s97 offen sc1\n"
"buffer_load_dwordx4 v[96:99], v1, s[4:7], s97 offen offset:1024 sc1\n"
"s_waitcnt vmcnt(0)\n"
"v_max_u32_e32 v255, v180, v181\n"
"v_max3_u32 v255, v255, v182, v183\n"
"v_max3_u32 v255, v255, v88, v89\n"
"v_max3_u32 v255, v255, v90, v91\n"
"v_max3_u32 v255, v255, v92, v93\n"
"v_max3_u32 v255, v255, v94, v95\n"
"v_max3_u32 v255, v255, v100, v101\n"
"v_max3_u32 v255, v255, v102, v103\n"
"v_cmp_eq_u32_e32 vcc, -1, v255\n"
"s_cbranch_vccz .Lmk_q39k\n"
"s_sub_u32 s1, s1, 1\n"
"s_cbranch_scc0 .Lmk_q39l\n"
".Lmk_q39k:\n"
"s_branch .Lmk_q39r\n"
".Lmk_p41s:\n"
";\n"
"s_mov_b32 s1, 0x10000\n"
".Lmk_p41l:\n"
"s_add_u32 s97, s56, 0\n"
"buffer_load_dwordx4 v[148:151], v0, s[4:7], s97 offen sc1\n"
"buffer_load_dwordx4 v[120:123], v0, s[4:7], s97 offen offset:1024 sc1\n"
"buffer_load_dwordx4 v[172:175], v0, s[4:7], s97 offen offset:2048 sc1\n"
"buffer_load_dwordx4 v[132:135], v0, s[4:7], s97 offen offset:3072 sc1\n"
"buffer_load_dwordx4 v[136:139], v1, s[4:7], s97 offen sc1\n"
"buffer_load_dwordx4 v[128:131], v1, s[4:7], s97 offen offset:1024 sc1\n"
"buffer_load_dwordx4 v[176:179], v1, s[4:7], s97 offen offset:2048 sc1\n"
"buffer_load_dwordx4 v[180:183], v1, s[4:7], s97 offen offset:3072 sc1\n"
"s_add_u32 s97, s57, 0\n"
"buffer_load_dwordx4 v[124:127], v0, s[4:7], s97 offen sc1\n"
"buffer_load_dwordx4 v[92:95], v0, s[4:7], s97 offen offset:1024 sc1\n"
"buffer_load_dwordx4 v[164:167], v0, s[4:7], s97 offen offset:2048 sc1\n"
"buffer_load_dwordx4 v[88:91], v0, s[4:7], s97 offen offset:3072 sc1\n"
"buffer_load_dwordx4 v[100:103], v1, s[4:7], s97 offen sc1\n"
"buffer_load_dwordx4 v[144:147], v1, s[4:7], s97 offen offset:1024 sc1\n"
"buffer_load_dwordx4 v[152:155], v1, s[4:7], s97 offen offset:2048 sc1\n"
"buffer_load_dwordx4 v[160:163], v1, s[4:7], s97 offen offset:3072 sc1\n"
"s_waitcnt vmcnt(0)\n"
"v_max_u32_e32 v255, v148, v149\n"
"v_max3_u32 v255, v255, v150, v151\n"
"v_max3_u32 v255, v255, v120, v121\n"
"v_max3_u32 v255, v255, v122, v123\n"
"v_max3_u32 v255, v255, v172, v173\n"
"v_max3_u32 v255, v255, v174, v175\n"
"v_max3_u32 v255, v255, v132, v133\n"
"v_max3_u32 v255, v255, v134, v135\n"
"v_max3_u32 v255, v255, v136, v137\n"
"v_max3_u32 v255, v255, v138, v139\n"
"v_max3_u32 v255, v255, v128, v129\n"
"v_max3_u32 v255, v255, v130, v131\n"
"v_max3_u32 v255, v255, v176, v177\n"
"v_max3_u32 v255, v255, v178, v179\n"
"v_max3_u32 v255, v255, v180, v181\n"
"v_max3_u32 v255, v255, v182, v183\n"
"v_cmp_eq_u32_e32 vcc, -1, v255\n"
"s_cbranch_vccz .Lmk_p41k\n"
"s_sub_u32 s1, s1, 1\n"
"s_cbranch_scc0 .Lmk_p41l\n"
".Lmk_p41k:\n"
"v_mfma_f32_16x16x32_bf16 v[116:119], a[128:131], v[148:151], 0\n"
"v_mfma_f32_16x16x32_bf16 v[140:143], a[132:135], v[148:151], 0\n"
"v_mfma_f32_16x16x32_bf16 v[156:159], a[136:139], v[148:151], 0\n"
"v_mfma_f32_16x16x32_bf16 v[168:171], a[140:143], v[148:151], 0\n"
"v_mfma_f32_16x16x32_bf16 v[116:119], a[144:147], v[120:123], v[116:119]\n"
"v_mfma_f32_16x16x32_bf16 v[140:143], a[148:151], v[120:123], v[140:143]\n"
"v_mfma_f32_16x16x32_bf16 v[156:159], a[152:155], v[120:123], v[156:159]\n"
"v_mfma_f32_16x16x32_bf16 v[168:171], a[156:159], v[120:123], v[168:171]\n"
"v_mfma_f32_16x16x32_bf16 v[116:119], a[160:163], v[172:175], v[116:119]\n"
"v_mfma_f32_16x16x32_bf16 v[140:143], a[164:167], v[172:175], v[140:143]\n"
"v_mfma_f32_16x16x32_bf16 v[156:159], a[168:171], v[172:175], v[156:159]\n"
"v_mfma_f32_16x16x32_bf16 v[168:171], a[172:175], v[172:175], v[168:171]\n"
"v_mfma_f32_16x16x32_bf16 v[116:119], a[176:179], v[132:135], v[116:119]\n"
"v_mfma_f32_16x16x32_bf16 v[140:143], a[180:183], v[132:135], v[140:143]\n"
"v_mfma_f32_16x16x32_bf16 v[156:159], a[184:187], v[132:135], v[156:159]\n"
"v_mfma_f32_16x16x32_bf16 v[168:171], a[188:191], v[132:135], v[168:171]\n"
"v_mfma_f32_16x16x32_bf16 v[116:119], a[192:195], v[136:139], v[116:119]\n"
"v_mfma_f32_16x16x32_bf16 v[140:143], a[196:199], v[136:139], v[140:143]\n"
"v_mfma_f32_16x16x32_bf16 v[156:159], a[200:203], v[136:139], v[156:159]\n"
"v_mfma_f32_16x16x32_bf16 v[168:171], a[204:207], v[136:139], v[168:171]\n"
"v_mfma_f32_16x16x32_bf16 v[116:119], a[208:211], v[128:131], v[116:119]\n"
"v_mfma_f32_16x16x32_bf16 v[140:143], a[212:215], v[128:131], v[140:143]\n"
"v_mfma_f32_16x16x32_bf16 v[156:159], a[216:219], v[128:131], v[156:159]\n"
"v_mfma_f32_16x16x32_bf16 v[168:171], a[220:223], v[128:131], v[168:171]\n"
"v_mfma_f32_16x16x32_bf16 v[116:119], a[224:227], v[176:179], v[116:119]\n"
"v_mfma_f32_16x16x32_bf16 v[140:143], a[228:231], v[176:179], v[140:143]\n"
"v_mfma_f32_16x16x32_bf16 v[156:159], a[232:235], v[176:179], v[156:159]\n"
"v_mfma_f32_16x16x32_bf16 v[168:171], a[236:239], v[176:179], v[168:171]\n"
"v_mfma_f32_16x16x32_bf16 v[116:119], a[240:243], v[180:183], v[116:119]\n"
"v_mfma_f32_16x16x32_bf16 v[140:143], a[244:247], v[180:183], v[140:143]\n"
"v_mfma_f32_16x16x32_bf16 v[156:159], a[248:251], v[180:183], v[156:159]\n"
"v_mfma_f32_16x16x32_bf16 v[168:171], a[252:255], v[180:183], v[168:171]\n"
"s_add_u32 s97, s58, 0\n"
"v_mov_b32_e32 v255, 0x80000000\n"
"buffer_load_dwordx4 v[148:151], v0, s[4:7], s97 offen sc1\n"
"buffer_load_dwordx4 v[120:123], v0, s[4:7], s97 offen offset:1024 sc1\n"
"buffer_load_dwordx4 v[172:175], v0, s[4:7], s97 offen offset:2048 sc1\n"
"buffer_load_dwordx4 v[132:135], v0, s[4:7], s97 offen offset:3072 sc1\n"
"buffer_load_dwordx4 v[136:139], v1, s[4:7], s97 offen sc1\n"
"buffer_load_dwordx4 v[128:131], v1, s[4:7], s97 offen offset:1024 sc1\n"
"buffer_load_dwordx4 v[176:179], v1, s[4:7], s97 offen offset:2048 sc1\n"
"buffer_load_dwordx4 v[180:183], v1, s[4:7], s97 offen offset:3072 sc1\n"
"s_nop 7\n"
"s_branch .Lmk_p41r\n"
".Lmk_p42s:\n"
";\n"
"s_mov_b32 s1, 0x10000\n"
".Lmk_p42l:\n"
"s_add_u32 s95, s57, 0\n"
"buffer_load_dwordx4 v[104:107], v0, s[4:7], s95 offen sc1\n"
"buffer_load_dwordx4 v[124:127], v0, s[4:7], s95 offen offset:1024 sc1\n"
"buffer_load_dwordx4 v[200:203], v0, s[4:7], s95 offen offset:2048 sc1\n"
"buffer_load_dwordx4 v[204:207], v0, s[4:7], s95 offen offset:3072 sc1\n"
"buffer_load_dwordx4 v[208:211], v1, s[4:7], s95 offen sc1\n"
"buffer_load_dwordx4 v[212:215], v1, s[4:7], s95 offen offset:1024 sc1\n"
"buffer_load_dwordx4 v[216:219], v1, s[4:7], s95 offen offset:2048 sc1\n"
"buffer_load_dwordx4 v[220:223], v1, s[4:7], s95 offen offset:3072 sc1\n"
"s_add_u32 s95, s58, 0\n"
"buffer_load_dwordx4 v[148:151], v0, s[4:7], s95 offen sc1\n"
"buffer_load_dwordx4 v[120:123], v0, s[4:7], s95 offen offset:1024 sc1\n"
"buffer_load_dwordx4 v[172:175], v0, s[4:7], s95 offen offset:2048 sc1\n"
"buffer_load_dwordx4 v[132:135], v0, s[4:7], s95 offen offset:3072 sc1\n"
"buffer_load_dwordx4 v[136:139], v1, s[4:7], s95 offen sc1\n"
"buffer_load_dwordx4 v[128:131], v1, s[4:7], s95 offen offset:1024 sc1\n"
"buffer_load_dwordx4 v[176:179], v1, s[4:7], s95 offen offset:2048 sc1\n"
"buffer_load_dwordx4 v[180:183], v1, s[4:7], s95 offen offset:3072 sc1\n"
"s_add_u32 s95, s59, 0\n"
"buffer_load_dwordx4 v[188:191], v0, s[4:7], s95 offen sc1\n"
"buffer_load_dwordx4 v[140:143], v0, s[4:7], s95 offen offset:1024 sc1\n"
"buffer_load_dwordx4 v[156:159], v0, s[4:7], s95 offen offset:2048 sc1\n"
"buffer_load_dwordx4 v[164:167], v0, s[4:7], s95 offen offset:3072 sc1\n"
"buffer_load_dwordx4 v[168:171], v1, s[4:7], s95 offen sc1\n"
"buffer_load_dwordx4 v[144:147], v1, s[4:7], s95 offen offset:1024 sc1\n"
"buffer_load_dwordx4 v[152:155], v1, s[4:7], s95 offen offset:2048 sc1\n"
"s_waitcnt vmcnt(0)\n"
"v_max_u32_e32 v242, v104, v105\n"
"v_max3_u32 v242, v242, v106, v107\n"
"v_max3_u32 v242, v242, v124, v125\n"
"v_max3_u32 v242, v242, v126, v127\n"
"v_max3_u32 v242, v242, v200, v201\n"
"v_max3_u32 v242, v242, v202, v203\n"
"v_max3_u32 v242, v242, v204, v205\n"
"v_max3_u32 v242, v242, v206, v207\n"
"v_max3_u32 v242, v242, v208, v209\n"
"v_max3_u32 v242, v242, v210, v211\n"
"v_max3_u32 v242, v242, v212, v213\n"
"v_max3_u32 v242, v242, v214, v215\n"
"v_max3_u32 v242, v242, v216, v217\n"
"v_max3_u32 v242, v242, v218, v219\n"
"v_max3_u32 v242, v242, v220, v221\n"
"v_max3_u32 v242, v242, v222, v223\n"
"v_cmp_eq_u32_e32 vcc, -1, v242\n"
"s_cbranch_vccz .Lmk_p42k\n"
"s_sub_u32 s1, s1, 1\n"
"s_cbranch_scc0 .Lmk_p42l\n"
".Lmk_p42k:\n"
"v_mfma_f32_16x16x32_bf16 v[108:111], a[128:131], v[104:107], 0\n"
"v_mfma_f32_16x16x32_bf16 v[112:115], a[132:135], v[104:107], 0\n"
"v_mfma_f32_16x16x32_bf16 v[184:187], a[136:139], v[104:107], 0\n"
"v_mfma_f32_16x16x32_bf16 v[192:195], a[140:143], v[104:107], 0\n"
"v_mfma_f32_16x16x32_bf16 v[108:111], a[144:147], v[124:127], v[108:111]\n"
"v_mfma_f32_16x16x32_bf16 v[112:115], a[148:151], v[124:127], v[112:115]\n"
"v_mfma_f32_16x16x32_bf16 v[184:187], a[152:155], v[124:127], v[184:187]\n"
"v_mfma_f32_16x16x32_bf16 v[192:195], a[156:159], v[124:127], v[192:195]\n"
"v_mfma_f32_16x16x32_bf16 v[108:111], a[160:163], v[200:203], v[108:111]\n"
"v_mfma_f32_16x16x32_bf16 v[112:115], a[164:167], v[200:203], v[112:115]\n"
"v_mfma_f32_16x16x32_bf16 v[184:187], a[168:171], v[200:203], v[184:187]\n"
"v_mfma_f32_16x16x32_bf16 v[192:195], a[172:175], v[200:203], v[192:195]\n"
"v_mfma_f32_16x16x32_bf16 v[108:111], a[176:179], v[204:207], v[108:111]\n"
"v_mfma_f32_16x16x32_bf16 v[112:115], a[180:183], v[204:207], v[112:115]\n"
"v_mfma_f32_16x16x32_bf16 v[184:187], a[184:187], v[204:207], v[184:187]\n"
"v_mfma_f32_16x16x32_bf16 v[192:195], a[188:191], v[204:207], v[192:195]\n"
"v_mfma_f32_16x16x32_bf16 v[108:111], a[192:195], v[208:211], v[108:111]\n"
"v_mfma_f32_16x16x32_bf16 v[112:115], a[196:199], v[208:211], v[112:115]\n"
"v_mfma_f32_16x16x32_bf16 v[184:187], a[200:203], v[208:211], v[184:187]\n"
"v_mfma_f32_16x16x32_bf16 v[192:195], a[204:207], v[208:211], v[192:195]\n"
"v_mfma_f32_16x16x32_bf16 v[108:111], a[208:211], v[212:215], v[108:111]\n"
"v_mfma_f32_16x16x32_bf16 v[112:115], a[212:215], v[212:215], v[112:115]\n"
"v_mfma_f32_16x16x32_bf16 v[184:187], a[216:219], v[212:215], v[184:187]\n"
"v_mfma_f32_16x16x32_bf16 v[192:195], a[220:223], v[212:215], v[192:195]\n"
"v_mfma_f32_16x16x32_bf16 v[108:111], a[224:227], v[216:219], v[108:111]\n"
"v_mfma_f32_16x16x32_bf16 v[112:115], a[228:231], v[216:219], v[112:115]\n"
"v_mfma_f32_16x16x32_bf16 v[184:187], a[232:235], v[216:219], v[184:187]\n"
"v_mfma_f32_16x16x32_bf16 v[192:195], a[236:239], v[216:219], v[192:195]\n"
"v_mfma_f32_16x16x32_bf16 v[108:111], a[240:243], v[220:223], v[108:111]\n"
"v_mfma_f32_16x16x32_bf16 v[112:115], a[244:247], v[220:223], v[112:115]\n"
"v_mfma_f32_16x16x32_bf16 v[184:187], a[248:251], v[220:223], v[184:187]\n"
"v_mfma_f32_16x16x32_bf16 v[192:195], a[252:255], v[220:223], v[192:195]\n"
"s_nop 7\n"
"s_branch .Lmk_p42r\n"
".Lmk_q44s:\n"
";\n"
"s_mov_b32 s1, 0x10000\n"
".Lmk_q44l:\n"
"s_add_u32 s98, s59, 0\n"
"buffer_load_dwordx4 v[188:191], v0, s[4:7], s98 offen sc1\n"
"buffer_load_dwordx4 v[140:143], v0, s[4:7], s98 offen offset:1024 sc1\n"
"buffer_load_dwordx4 v[156:159], v0, s[4:7], s98 offen offset:2048 sc1\n"
"buffer_load_dwordx4 v[164:167], v0, s[4:7], s98 offen offset:3072 sc1\n"
"buffer_load_dwordx4 v[168:171], v1, s[4:7], s98 offen sc1\n"
"buffer_load_dwordx4 v[144:147], v1, s[4:7], s98 offen offset:1024 sc1\n"
"buffer_load_dwordx4 v[152:155], v1, s[4:7], s98 offen offset:2048 sc1\n"
"buffer_load_dwordx4 v[204:207], v1, s[4:7], s98 offen offset:3072 sc1\n"
"s_add_u32 s98, s52, 0x440000\n"
"buffer_load_dwordx4 v[104:107], v0, s[4:7], s98 offen sc1\n"
"buffer_load_dwordx4 v[108:111], v0, s[4:7], s98 offen offset:1024 sc1\n"
"buffer_load_dwordx4 v[112:115], v0, s[4:7], s98 offen offset:2048 sc1\n"
"buffer_load_dwordx4 v[116:119], v0, s[4:7], s98 offen offset:3072 sc1\n"
"buffer_load_dwordx4 v[120:123], v1, s[4:7], s98 offen sc1\n"
"s_waitcnt vmcnt(0)\n"
"v_max_u32_e32 v253, v188, v189\n"
"v_max3_u32 v253, v253, v190, v191\n"
"v_max3_u32 v253, v253, v140, v141\n"
"v_max3_u32 v253, v253, v142, v143\n"
"v_max3_u32 v253, v253, v156, v157\n"
"v_max3_u32 v253, v253, v158, v159\n"
"v_max3_u32 v253, v253, v164, v165\n"
"v_max3_u32 v253, v253, v166, v167\n"
"v_cmp_eq_u32_e32 vcc, -1, v253\n"
"s_cbranch_vccz .Lmk_q44k\n"
"s_sub_u32 s1, s1, 1\n"
"s_cbranch_scc0 .Lmk_q44l\n"
".Lmk_q44k:\n"
"s_branch .Lmk_q44r\n"
".Lmk_p43s:\n"
";\n"
"s_mov_b32 s1, 0x10000\n"
".Lmk_p43l:\n"
"s_add_u32 s98, s58, 0\n"
"buffer_load_dwordx4 v[124:127], v0, s[4:7], s98 offen sc1\n"
"buffer_load_dwordx4 v[128:131], v0, s[4:7], s98 offen offset:1024 sc1\n"
"buffer_load_dwordx4 v[132:135], v0, s[4:7], s98 offen offset:2048 sc1\n"
"buffer_load_dwordx4 v[136:139], v0, s[4:7], s98 offen offset:3072 sc1\n"
"buffer_load_dwordx4 v[148:151], v1, s[4:7], s98 offen sc1\n"
"buffer_load_dwordx4 v[172:175], v1, s[4:7], s98 offen offset:1024 sc1\n"
"buffer_load_dwordx4 v[176:179], v1, s[4:7], s98 offen offset:2048 sc1\n"
"buffer_load_dwordx4 v[180:183], v1, s[4:7], s98 offen offset:3072 sc1\n"
"s_add_u32 s98, s59, 0\n"
"buffer_load_dwordx4 v[188:191], v0, s[4:7], s98 offen sc1\n"
"buffer_load_dwordx4 v[140:143], v0, s[4:7], s98 offen offset:1024 sc1\n"
"buffer_load_dwordx4 v[156:159], v0, s[4:7], s98 offen offset:2048 sc1\n"
"buffer_load_dwordx4 v[164:167], v0, s[4:7], s98 offen offset:3072 sc1\n"
"buffer_load_dwordx4 v[168:171], v1, s[4:7], s98 offen sc1\n"
"buffer_load_dwordx4 v[144:147], v1, s[4:7], s98 offen offset:1024 sc1\n"
"buffer_load_dwordx4 v[152:155], v1, s[4:7], s98 offen offset:2048 sc1\n"
"buffer_load_dwordx4 v[204:207], v1, s[4:7], s98 offen offset:3072 sc1\n"
"s_add_u32 s98, s52, 0x440000\n"
"buffer_load_dwordx4 v[104:107], v0, s[4:7], s98 offen sc1\n"
"buffer_load_dwordx4 v[108:111], v0, s[4:7], s98 offen offset:1024 sc1\n"
"buffer_load_dwordx4 v[112:115], v0, s[4:7], s98 offen offset:2048 sc1\n"
"buffer_load_dwordx4 v[116:119], v0, s[4:7], s98 offen offset:3072 sc1\n"
"buffer_load_dwordx4 v[120:123], v1, s[4:7], s98 offen sc1\n"
"s_waitcnt vmcnt(0)\n"
"v_max_u32_e32 v253, v124, v125\n"
"v_max3_u32 v253, v253, v126, v127\n"
"v_max3_u32 v253, v253, v128, v129\n"
"v_max3_u32 v253, v253, v130, v131\n"
"v_max3_u32 v253, v253, v132, v133\n"
"v_max3_u32 v253, v253, v134, v135\n"
"v_max3_u32 v253, v253, v136, v137\n"
"v_max3_u32 v253, v253, v138, v139\n"
"v_max3_u32 v253, v253, v148, v149\n"
"v_max3_u32 v253, v253, v150, v151\n"
"v_max3_u32 v253, v253, v172, v173\n"
"v_max3_u32 v253, v253, v174, v175\n"
"v_max3_u32 v253, v253, v176, v177\n"
"v_max3_u32 v253, v253, v178, v179\n"
"v_max3_u32 v253, v253, v180, v181\n"
"v_max3_u32 v253, v253, v182, v183\n"
"v_cmp_eq_u32_e32 vcc, -1, v253\n"
"s_cbranch_vccz .Lmk_p43k\n"
"s_sub_u32 s1, s1, 1\n"
"s_cbranch_scc0 .Lmk_p43l\n"
".Lmk_p43k:\n"
"v_mfma_f32_16x16x32_bf16 v[160:163], a[128:131], v[124:127], 0\n"
"v_mfma_f32_16x16x32_bf16 v[196:199], a[132:135], v[124:127], 0\n"
"v_mfma_f32_16x16x32_bf16 v[200:203], a[136:139], v[124:127], 0\n"
"v_mfma_f32_16x16x32_bf16 v[208:211], a[140:143], v[124:127], 0\n"
"v_mfma_f32_16x16x32_bf16 v[160:163], a[144:147], v[128:131], v[160:163]\n"
"v_mfma_f32_16x16x32_bf16 v[196:199], a[148:151], v[128:131], v[196:199]\n"
"v_mfma_f32_16x16x32_bf16 v[200:203], a[152:155], v[128:131], v[200:203]\n"
"v_mfma_f32_16x16x32_bf16 v[208:211], a[156:159], v[128:131], v[208:211]\n"
"v_mfma_f32_16x16x32_bf16 v[160:163], a[160:163], v[132:135], v[160:163]\n"
"v_mfma_f32_16x16x32_bf16 v[196:199], a[164:167], v[132:135], v[196:199]\n"
"v_mfma_f32_16x16x32_bf16 v[200:203], a[168:171], v[132:135], v[200:203]\n"
"v_mfma_f32_16x16x32_bf16 v[208:211], a[172:175], v[132:135], v[208:211]\n"
"v_mfma_f32_16x16x32_bf16 v[160:163], a[176:179], v[136:139], v[160:163]\n"
"v_mfma_f32_16x16x32_bf16 v[196:199], a[180:183], v[136:139], v[196:199]\n"
"v_mfma_f32_16x16x32_bf16 v[200:203], a[184:187], v[136:139], v[200:203]\n"
"v_mfma_f32_16x16x32_bf16 v[208:211], a[188:191], v[136:139], v[208:211]\n"
"v_mfma_f32_16x16x32_bf16 v[160:163], a[192:195], v[148:151], v[160:163]\n"
"v_mfma_f32_16x16x32_bf16 v[196:199], a[196:199], v[148:151], v[196:199]\n"
"v_mfma_f32_16x16x32_bf16 v[200:203], a[200:203], v[148:151], v[200:203]\n"
"v_mfma_f32_16x16x32_bf16 v[208:211], a[204:207], v[148:151], v[208:211]\n"
"v_mfma_f32_16x16x32_bf16 v[160:163], a[208:211], v[172:175], v[160:163]\n"
"v_mfma_f32_16x16x32_bf16 v[196:199], a[212:215], v[172:175], v[196:199]\n"
"v_mfma_f32_16x16x32_bf16 v[200:203], a[216:219], v[172:175], v[200:203]\n"
"v_mfma_f32_16x16x32_bf16 v[208:211], a[220:223], v[172:175], v[208:211]\n"
"v_mfma_f32_16x16x32_bf16 v[160:163], a[224:227], v[176:179], v[160:163]\n"
"v_mfma_f32_16x16x32_bf16 v[196:199], a[228:231], v[176:179], v[196:199]\n"
"v_mfma_f32_16x16x32_bf16 v[200:203], a[232:235], v[176:179], v[200:203]\n"
"v_mfma_f32_16x16x32_bf16 v[208:211], a[236:239], v[176:179], v[208:211]\n"
"v_mfma_f32_16x16x32_bf16 v[160:163], a[240:243], v[180:183], v[160:163]\n"
"v_mfma_f32_16x16x32_bf16 v[196:199], a[244:247], v[180:183], v[196:199]\n"
"v_mfma_f32_16x16x32_bf16 v[200:203], a[248:251], v[180:183], v[200:203]\n"
"v_mfma_f32_16x16x32_bf16 v[208:211], a[252:255], v[180:183], v[208:211]\n"
"s_nop 7\n"
"s_branch .Lmk_p43r\n"
".Lmk_f13s:\n"
";\n"
"s_mov_b32 s1, 0x10000\n"
".Lmk_f13l:\n"
"buffer_load_dwordx2 v[254:255], v13, s[4:7], s97 offen sc1\n"
"s_waitcnt vmcnt(0)\n"
"v_cmp_ne_u64_e32 vcc, v[254:255], v[18:19]\n"
"s_cbranch_vccz .Lmk_f13r\n"
"s_sub_u32 s1, s1, 1\n"
"s_cbranch_scc0 .Lmk_f13l\n"
"s_branch .Lmk_f13r\n"
".Lmk_q45s:\n"
";\n"
"s_mov_b32 s1, 0x10000\n"
".Lmk_q45l:\n"
"s_add_u32 s98, s59, 0\n"
"buffer_load_dwordx4 v[168:171], v1, s[4:7], s98 offen sc1\n"
"buffer_load_dwordx4 v[144:147], v1, s[4:7], s98 offen offset:1024 sc1\n"
"buffer_load_dwordx4 v[152:155], v1, s[4:7], s98 offen offset:2048 sc1\n"
"buffer_load_dwordx4 v[204:207], v1, s[4:7], s98 offen offset:3072 sc1\n"
"s_add_u32 s98, s52, 0x440000\n"
"buffer_load_dwordx4 v[104:107], v0, s[4:7], s98 offen sc1\n"
"buffer_load_dwordx4 v[108:111], v0, s[4:7], s98 offen offset:1024 sc1\n"
"buffer_load_dwordx4 v[112:115], v0, s[4:7], s98 offen offset:2048 sc1\n"
"buffer_load_dwordx4 v[116:119], v0, s[4:7], s98 offen offset:3072 sc1\n"
"buffer_load_dwordx4 v[120:123], v1, s[4:7], s98 offen sc1\n"
"buffer_load_dwordx4 v[124:127], v1, s[4:7], s98 offen offset:1024 sc1\n"
"s_waitcnt vmcnt(0)\n"
"v_max_u32_e32 v255, v168, v169\n"
"v_max3_u32 v255, v255, v170, v171\n"
"v_max3_u32 v255, v255, v144, v145\n"
"v_max3_u32 v255, v255, v146, v147\n"
"v_max3_u32 v255, v255, v152, v153\n"
"v_max3_u32 v255, v255, v154, v155\n"
"v_max3_u32 v255, v255, v204, v205\n"
"v_max3_u32 v255, v255, v206, v207\n"
"v_cmp_eq_u32_e32 vcc, -1, v255\n"
"s_cbranch_vccz .Lmk_q45k\n"
"s_sub_u32 s1, s1, 1\n"
"s_cbranch_scc0 .Lmk_q45l\n"
".Lmk_q45k:\n"
"s_branch .Lmk_q45r\n"
".Lmk_p50s:\n"
";\n"
"s_mov_b32 s1, 0x10000\n"
".Lmk_p50l:\n"
"s_add_u32 s97, s52, 0x440000\n"
"buffer_load_dwordx4 v[104:107], v0, s[4:7], s97 offen sc1\n"
"buffer_load_dwordx4 v[212:215], v0, s[4:7], s97 offen offset:1024 sc1\n"
"buffer_load_dwordx4 v[92:95], v0, s[4:7], s97 offen offset:2048 sc1\n"
"buffer_load_dwordx4 v[108:111], v0, s[4:7], s97 offen offset:3072 sc1\n"
"buffer_load_dwordx4 v[100:103], v1, s[4:7], s97 offen sc1\n"
"buffer_load_dwordx4 v[116:119], v1, s[4:7], s97 offen offset:1024 sc1\n"
"buffer_load_dwordx4 v[88:91], v1, s[4:7], s97 offen offset:2048 sc1\n"
"buffer_load_dwordx4 v[96:99], v1, s[4:7], s97 offen offset:3072 sc1\n"
"s_add_u32 s97, s53, 0x440000\n"
"buffer_load_dwordx4 v[144:147], v0, s[4:7], s97 offen sc1\n"
"buffer_load_dwordx4 v[156:159], v0, s[4:7], s97 offen offset:1024 sc1\n"
"buffer_load_dwordx4 v[160:163], v0, s[4:7], s97 offen offset:2048 sc1\n"
"buffer_load_dwordx4 v[164:167], v0, s[4:7], s97 offen offset:3072 sc1\n"
"buffer_load_dwordx4 v[168:171], v1, s[4:7], s97 offen sc1\n"
"buffer_load_dwordx4 v[172:175], v1, s[4:7], s97 offen offset:1024 sc1\n"
"buffer_load_dwordx4 v[176:179], v1, s[4:7], s97 offen offset:2048 sc1\n"
"buffer_load_dwordx4 v[180:183], v1, s[4:7], s97 offen offset:3072 sc1\n"
"s_waitcnt vmcnt(0)\n"
"v_max_u32_e32 v247, v104, v105\n"
"v_max3_u32 v247, v247, v106, v107\n"
"v_max3_u32 v247, v247, v212, v213\n"
"v_max3_u32 v247, v247, v214, v215\n"
"v_max3_u32 v247, v247, v92, v93\n"
"v_max3_u32 v247, v247, v94, v95\n"
"v_max3_u32 v247, v247, v108, v109\n"
"v_max3_u32 v247, v247, v110, v111\n"
"v_max3_u32 v247, v247, v100, v101\n"
"v_max3_u32 v247, v247, v102, v103\n"
"v_max3_u32 v247, v247, v116, v117\n"
"v_max3_u32 v247, v247, v118, v119\n"
"v_max3_u32 v247, v247, v88, v89\n"
"v_max3_u32 v247, v247, v90, v91\n"
"v_max3_u32 v247, v247, v96, v97\n"
"v_max3_u32 v247, v247, v98, v99\n"
"v_cmp_eq_u32_e32 vcc, -1, v247\n"
"s_cbranch_vccz .Lmk_p50k\n"
"s_sub_u32 s1, s1, 1\n"
"s_cbranch_scc0 .Lmk_p50l\n"
".Lmk_p50k:\n"
"v_mfma_f32_16x16x32_bf16 v[184:187], a[0:3], v[104:107], 0\n"
"v_mfma_f32_16x16x32_bf16 v[188:191], a[4:7], v[104:107], 0\n"
"v_mfma_f32_16x16x32_bf16 v[192:195], a[8:11], v[104:107], 0\n"
"v_mfma_f32_16x16x32_bf16 v[196:199], a[12:15], v[104:107], 0\n"
"v_mfma_f32_16x16x32_bf16 v[200:203], a[16:19], v[104:107], 0\n"
"v_mfma_f32_16x16x32_bf16 v[208:211], a[20:23], v[104:107], 0\n"
"v_mfma_f32_16x16x32_bf16 v[184:187], a[24:27], v[212:215], v[184:187]\n"
"v_mfma_f32_16x16x32_bf16 v[188:191], a[28:31], v[212:215], v[188:191]\n"
"v_mfma_f32_16x16x32_bf16 v[192:195], a[32:35], v[212:215], v[192:195]\n"
"v_mfma_f32_16x16x32_bf16 v[196:199], a[36:39], v[212:215], v[196:199]\n"
"v_mfma_f32_16x16x32_bf16 v[200:203], a[40:43], v[212:215], v[200:203]\n"
"v_mfma_f32_16x16x32_bf16 v[208:211], a[44:47], v[212:215], v[208:211]\n"
"v_mfma_f32_16x16x32_bf16 v[184:187], a[48:51], v[92:95], v[184:187]\n"
"v_mfma_f32_16x16x32_bf16 v[188:191], a[52:55], v[92:95], v[188:191]\n"
"v_mfma_f32_16x16x32_bf16 v[192:195], a[56:59], v[92:95], v[192:195]\n"
"v_mfma_f32_16x16x32_bf16 v[196:199], a[60:63], v[92:95], v[196:199]\n"
"v_mfma_f32_16x16x32_bf16 v[200:203], a[64:67], v[92:95], v[200:203]\n"
"v_mfma_f32_16x16x32_bf16 v[208:211], a[68:71], v[92:95], v[208:211]\n"
"v_mfma_f32_16x16x32_bf16 v[184:187], a[72:75], v[108:111], v[184:187]\n"
"v_mfma_f32_16x16x32_bf16 v[188:191], a[76:79], v[108:111], v[188:191]\n"
"v_mfma_f32_16x16x32_bf16 v[192:195], a[80:83], v[108:111], v[192:195]\n"
"v_mfma_f32_16x16x32_bf16 v[196:199], a[84:87], v[108:111], v[196:199]\n"
"v_mfma_f32_16x16x32_bf16 v[200:203], a[88:91], v[108:111], v[200:203]\n"
"v_mfma_f32_16x16x32_bf16 v[208:211], a[92:95], v[108:111], v[208:211]\n"
"v_mfma_f32_16x16x32_bf16 v[184:187], a[96:99], v[100:103], v[184:187]\n"
"v_mfma_f32_16x16x32_bf16 v[188:191], a[100:103], v[100:103], v[188:191]\n"
"v_mfma_f32_16x16x32_bf16 v[192:195], a[104:107], v[100:103], v[192:195]\n"
"v_mfma_f32_16x16x32_bf16 v[196:199], a[108:111], v[100:103], v[196:199]\n"
"v_mfma_f32_16x16x32_bf16 v[200:203], a[112:115], v[100:103], v[200:203]\n"
"v_mfma_f32_16x16x32_bf16 v[208:211], a[116:119], v[100:103], v[208:211]\n"
"v_mfma_f32_16x16x32_bf16 v[184:187], a[120:123], v[116:119], v[184:187]\n"
"v_mfma_f32_16x16x32_bf16 v[188:191], a[124:127], v[116:119], v[188:191]\n"
"v_mfma_f32_16x16x32_bf16 v[192:195], v[24:27], v[116:119], v[192:195]\n"
"v_mfma_f32_16x16x32_bf16 v[196:199], v[28:31], v[116:119], v[196:199]\n"
"v_mfma_f32_16x16x32_bf16 v[200:203], v[32:35], v[116:119], v[200:203]\n"
"v_mfma_f32_16x16x32_bf16 v[208:211], v[36:39], v[116:119], v[208:211]\n"
"v_mfma_f32_16x16x32_bf16 v[184:187], v[40:43], v[88:91], v[184:187]\n"
"v_mfma_f32_16x16x32_bf16 v[188:191], v[44:47], v[88:91], v[188:191]\n"
"v_mfma_f32_16x16x32_bf16 v[192:195], v[48:51], v[88:91], v[192:195]\n"
"v_mfma_f32_16x16x32_bf16 v[196:199], v[52:55], v[88:91], v[196:199]\n"
"v_mfma_f32_16x16x32_bf16 v[200:203], v[56:59], v[88:91], v[200:203]\n"
"v_mfma_f32_16x16x32_bf16 v[208:211], v[60:63], v[88:91], v[208:211]\n"
"v_mfma_f32_16x16x32_bf16 v[184:187], v[64:67], v[96:99], v[184:187]\n"
"v_mfma_f32_16x16x32_bf16 v[188:191], v[68:71], v[96:99], v[188:191]\n"
"v_mfma_f32_16x16x32_bf16 v[192:195], v[72:75], v[96:99], v[192:195]\n"
"v_mfma_f32_16x16x32_bf16 v[196:199], v[76:79], v[96:99], v[196:199]\n"
"v_mfma_f32_16x16x32_bf16 v[200:203], v[80:83], v[96:99], v[200:203]\n"
"v_mfma_f32_16x16x32_bf16 v[208:211], v[84:87], v[96:99], v[208:211]\n"
"s_add_u32 s97, s54, 0x440000\n"
"v_mov_b32_e32 v247, 0x80000000\n"
"buffer_load_dwordx4 v[104:107], v0, s[4:7], s97 offen sc1\n"
"buffer_store_dword v247, v247, s[4:7], 0 offen\n"
"buffer_load_dwordx4 v[212:215], v0, s[4:7], s97 offen offset:1024 sc1\n"
"buffer_load_dwordx4 v[92:95], v0, s[4:7], s97 offen offset:2048 sc1\n"
"buffer_store_dword v247, v247, s[4:7], 0 offen\n"
"buffer_load_dwordx4 v[108:111], v0, s[4:7], s97 offen offset:3072 sc1\n"
"buffer_load_dwordx4 v[100:103], v1, s[4:7], s97 offen sc1\n"
"buffer_store_dword v247, v247, s[4:7], 0 offen\n"
"buffer_load_dwordx4 v[116:119], v1, s[4:7], s97 offen offset:1024 sc1\n"
"buffer_store_dword v247, v247, s[4:7], 0 offen\n"
"buffer_load_dwordx4 v[88:91], v1, s[4:7], s97 offen offset:2048 sc1\n"
"buffer_store_dword v247, v247, s[4:7], 0 offen\n"
"buffer_load_dwordx4 v[96:99], v1, s[4:7], s97 offen offset:3072 sc1\n"
"s_nop 7\n"
"s_branch .Lmk_p50r\n"
".Lmk_m55s:\n"
";\n"
"s_mov_b32 s1, 0x10000\n"
".Lmk_m55l:\n"
"s_add_u32 s96, s62, 0x44000\n"
"buffer_load_dwordx2 v[254:255], v10, s[4:7], s96 offen sc1\n"
"buffer_load_dwordx2 v[252:253], v10, s[4:7], s96 offen offset:128 sc1\n"
"buffer_load_dwordx2 v[250:251], v10, s[4:7], s96 offen offset:256 sc1\n"
"buffer_load_dwordx2 v[248:249], v10, s[4:7], s96 offen offset:384 sc1\n"
"s_waitcnt vmcnt(0)\n"
"v_max_u32_e32 v247, v254, v255\n"
"v_max3_u32 v247, v247, v252, v253\n"
"v_max3_u32 v247, v247, v250, v251\n"
"v_max3_u32 v247, v247, v248, v249\n"
"v_cmp_eq_u32_e32 vcc, -1, v247\n"
"s_cbranch_vccz .Lmk_m55k\n"
"s_sub_u32 s1, s1, 1\n"
"s_cbranch_scc0 .Lmk_m55l\n"
".Lmk_m55k:\n"
"v_add_f32_e32 v254, v254, v252\n"
"v_add_f32_e32 v255, v255, v253\n"
"v_add_f32_e32 v250, v250, v248\n"
"v_add_f32_e32 v251, v251, v249\n"
"v_add_f32_e32 v254, v254, v250\n"
"v_add_f32_e32 v255, v255, v251\n"
"s_nop 1\n"
"s_branch .Lmk_m55r\n"
".Lmk_p51s:\n"
";\n"
"s_mov_b32 s1, 0x10000\n"
".Lmk_p51l:\n"
"s_add_u32 s97, s53, 0x440000\n"
"buffer_load_dwordx4 v[144:147], v0, s[4:7], s97 offen sc1\n"
"buffer_load_dwordx4 v[148:151], v0, s[4:7], s97 offen offset:1024 sc1\n"
"buffer_load_dwordx4 v[152:155], v0, s[4:7], s97 offen offset:2048 sc1\n"
"buffer_load_dwordx4 v[156:159], v0, s[4:7], s97 offen offset:3072 sc1\n"
"buffer_load_dwordx4 v[160:163], v1, s[4:7], s97 offen sc1\n"
"buffer_load_dwordx4 v[164:167], v1, s[4:7], s97 offen offset:1024 sc1\n"
"buffer_load_dwordx4 v[200:203], v1, s[4:7], s97 offen offset:2048 sc1\n"
"buffer_load_dwordx4 v[176:179], v1, s[4:7], s97 offen offset:3072 sc1\n"
"s_add_u32 s97, s54, 0x440000\n"
"buffer_load_dwordx4 v[104:107], v0, s[4:7], s97 offen sc1\n"
"buffer_load_dwordx4 v[212:215], v0, s[4:7], s97 offen offset:1024 sc1\n"
"buffer_load_dwordx4 v[92:95], v0, s[4:7], s97 offen offset:2048 sc1\n"
"buffer_load_dwordx4 v[108:111], v0, s[4:7], s97 offen offset:3072 sc1\n"
"buffer_load_dwordx4 v[100:103], v1, s[4:7], s97 offen sc1\n"
"buffer_load_dwordx4 v[116:119], v1, s[4:7], s97 offen offset:1024 sc1\n"
"buffer_load_dwordx4 v[88:91], v1, s[4:7], s97 offen offset:2048 sc1\n"
"buffer_load_dwordx4 v[96:99], v1, s[4:7], s97 offen offset:3072 sc1\n"
"s_waitcnt vmcnt(0)\n"
"v_max_u32_e32 v252, v144, v145\n"
"v_max3_u32 v252, v252, v146, v147\n"
"v_max3_u32 v252, v252, v148, v149\n"
"v_max3_u32 v252, v252, v150, v151\n"
"v_max3_u32 v252, v252, v152, v153\n"
"v_max3_u32 v252, v252, v154, v155\n"
"v_max3_u32 v252, v252, v156, v157\n"
"v_max3_u32 v252, v252, v158, v159\n"
"v_max3_u32 v252, v252, v160, v161\n"
"v_max3_u32 v252, v252, v162, v163\n"
"v_max3_u32 v252, v252, v164, v165\n"
"v_max3_u32 v252, v252, v166, v167\n"
"v_max3_u32 v252, v252, v200, v201\n"
"v_max3_u32 v252, v252, v202, v203\n"
"v_max3_u32 v252, v252, v176, v177\n"
"v_max3_u32 v252, v252, v178, v179\n"
"v_cmp_eq_u32_e32 vcc, -1, v252\n"
"s_cbranch_vccz .Lmk_p51k\n"
"s_sub_u32 s1, s1, 1\n"
"s_cbranch_scc0 .Lmk_p51l\n"
".Lmk_p51k:\n"
"v_mfma_f32_16x16x32_bf16 v[124:127], a[0:3], v[144:147], 0\n"
"v_mfma_f32_16x16x32_bf16 v[128:131], a[4:7], v[144:147], 0\n"
"v_mfma_f32_16x16x32_bf16 v[120:123], a[8:11], v[144:147], 0\n"
"v_mfma_f32_16x16x32_bf16 v[132:135], a[12:15], v[144:147], 0\n"
"v_mfma_f32_16x16x32_bf16 v[136:139], a[16:19], v[144:147], 0\n"
"v_mfma_f32_16x16x32_bf16 v[140:143], a[20:23], v[144:147], 0\n"
"v_mfma_f32_16x16x32_bf16 v[124:127], a[24:27], v[148:151], v[124:127]\n"
"v_mfma_f32_16x16x32_bf16 v[128:131], a[28:31], v[148:151], v[128:131]\n"
"v_mfma_f32_16x16x32_bf16 v[120:123], a[32:35], v[148:151], v[120:123]\n"
"v_mfma_f32_16x16x32_bf16 v[132:135], a[36:39], v[148:151], v[132:135]\n"
"v_mfma_f32_16x16x32_bf16 v[136:139], a[40:43], v[148:151], v[136:139]\n"
"v_mfma_f32_16x16x32_bf16 v[140:143], a[44:47], v[148:151], v[140:143]\n"
"v_mfma_f32_16x16x32_bf16 v[124:127], a[48:51], v[152:155], v[124:127]\n"
"v_mfma_f32_16x16x32_bf16 v[128:131], a[52:55], v[152:155], v[128:131]\n"
"v_mfma_f32_16x16x32_bf16 v[120:123], a[56:59], v[152:155], v[120:123]\n"
"v_mfma_f32_16x16x32_bf16 v[132:135], a[60:63], v[152:155], v[132:135]\n"
"v_mfma_f32_16x16x32_bf16 v[136:139], a[64:67], v[152:155], v[136:139]\n"
"v_mfma_f32_16x16x32_bf16 v[140:143], a[68:71], v[152:155], v[140:143]\n"
"v_mfma_f32_16x16x32_bf16 v[124:127], a[72:75], v[156:159], v[124:127]\n"
"v_mfma_f32_16x16x32_bf16 v[128:131], a[76:79], v[156:159], v[128:131]\n"
"v_mfma_f32_16x16x32_bf16 v[120:123], a[80:83], v[156:159], v[120:123]\n"
"v_mfma_f32_16x16x32_bf16 v[132:135], a[84:87], v[156:159], v[132:135]\n"
"v_mfma_f32_16x16x32_bf16 v[136:139], a[88:91], v[156:159], v[136:139]\n"
"v_mfma_f32_16x16x32_bf16 v[140:143], a[92:95], v[156:159], v[140:143]\n"
"v_mfma_f32_16x16x32_bf16 v[124:127], a[96:99], v[160:163], v[124:127]\n"
"v_mfma_f32_16x16x32_bf16 v[128:131], a[100:103], v[160:163], v[128:131]\n"
"v_mfma_f32_16x16x32_bf16 v[120:123], a[104:107], v[160:163], v[120:123]\n"
"v_mfma_f32_16x16x32_bf16 v[132:135], a[108:111], v[160:163], v[132:135]\n"
"v_mfma_f32_16x16x32_bf16 v[136:139], a[112:115], v[160:163], v[136:139]\n"
"v_mfma_f32_16x16x32_bf16 v[140:143], a[116:119], v[160:163], v[140:143]\n"
"v_mfma_f32_16x16x32_bf16 v[124:127], a[120:123], v[164:167], v[124:127]\n"
"v_mfma_f32_16x16x32_bf16 v[128:131], a[124:127], v[164:167], v[128:131]\n"
"v_mfma_f32_16x16x32_bf16 v[120:123], v[24:27], v[164:167], v[120:123]\n"
"v_mfma_f32_16x16x32_bf16 v[132:135], v[28:31], v[164:167], v[132:135]\n"
"v_mfma_f32_16x16x32_bf16 v[136:139], v[32:35], v[164:167], v[136:139]\n"
"v_mfma_f32_16x16x32_bf16 v[140:143], v[36:39], v[164:167], v[140:143]\n"
"v_mfma_f32_16x16x32_bf16 v[124:127], v[40:43], v[200:203], v[124:127]\n"
"v_mfma_f32_16x16x32_bf16 v[128:131], v[44:47], v[200:203], v[128:131]\n"
"v_mfma_f32_16x16x32_bf16 v[120:123], v[48:51], v[200:203], v[120:123]\n"
"v_mfma_f32_16x16x32_bf16 v[132:135], v[52:55], v[200:203], v[132:135]\n"
"v_mfma_f32_16x16x32_bf16 v[136:139], v[56:59], v[200:203], v[136:139]\n"
"v_mfma_f32_16x16x32_bf16 v[140:143], v[60:63], v[200:203], v[140:143]\n"
"v_mfma_f32_16x16x32_bf16 v[124:127], v[64:67], v[176:179], v[124:127]\n"
"v_mfma_f32_16x16x32_bf16 v[128:131], v[68:71], v[176:179], v[128:131]\n"
"v_mfma_f32_16x16x32_bf16 v[120:123], v[72:75], v[176:179], v[120:123]\n"
"v_mfma_f32_16x16x32_bf16 v[132:135], v[76:79], v[176:179], v[132:135]\n"
"v_mfma_f32_16x16x32_bf16 v[136:139], v[80:83], v[176:179], v[136:139]\n"
"v_mfma_f32_16x16x32_bf16 v[140:143], v[84:87], v[176:179], v[140:143]\n"
"s_add_u32 s97, s55, 0x440000\n"
"v_mov_b32_e32 v252, 0x80000000\n"
"buffer_load_dwordx4 v[144:147], v0, s[4:7], s97 offen sc1\n"
"buffer_load_dwordx4 v[148:151], v0, s[4:7], s97 offen offset:1024 sc1\n"
"buffer_load_dwordx4 v[152:155], v0, s[4:7], s97 offen offset:2048 sc1\n"
"buffer_store_dword v252, v252, s[4:7], 0 offen\n"
"buffer_load_dwordx4 v[156:159], v0, s[4:7], s97 offen offset:3072 sc1\n"
"buffer_load_dwordx4 v[160:163], v1, s[4:7], s97 offen sc1\n"
"buffer_load_dwordx4 v[164:167], v1, s[4:7], s97 offen offset:1024 sc1\n"
"buffer_load_dwordx4 v[200:203], v1, s[4:7], s97 offen offset:2048 sc1\n"
"buffer_store_dword v252, v252, s[4:7], 0 offen\n"
"buffer_load_dwordx4 v[176:179], v1, s[4:7], s97 offen offset:3072 sc1\n"
"s_nop 7\n"
"s_branch .Lmk_p51r\n"
".Lmk_q53s:\n"
";\n"
"s_mov_b32 s1, 0x10000\n"
".Lmk_q53l:\n"
"s_add_u32 s95, s55, 0x440000\n"
"buffer_load_dwordx4 v[144:147], v0, s[4:7], s95 offen sc1\n"
"buffer_load_dwordx4 v[148:151], v0, s[4:7], s95 offen offset:1024 sc1\n"
"buffer_load_dwordx4 v[152:155], v0, s[4:7], s95 offen offset:2048 sc1\n"
"buffer_load_dwordx4 v[156:159], v0, s[4:7], s95 offen offset:3072 sc1\n"
"buffer_load_dwordx4 v[160:163], v1, s[4:7], s95 offen sc1\n"
"buffer_load_dwordx4 v[164:167], v1, s[4:7], s95 offen offset:1024 sc1\n"
"buffer_load_dwordx4 v[200:203], v1, s[4:7], s95 offen offset:2048 sc1\n"
"buffer_load_dwordx4 v[176:179], v1, s[4:7], s95 offen offset:3072 sc1\n"
"s_add_u32 s95, s52, 0x448000\n"
"buffer_load_dwordx4 v[104:107], v0, s[4:7], s95 offen sc1\n"
"buffer_load_dwordx4 v[120:123], v0, s[4:7], s95 offen offset:1024 sc1\n"
"buffer_load_dwordx4 v[112:115], v0, s[4:7], s95 offen offset:2048 sc1\n"
"buffer_load_dwordx4 v[128:131], v0, s[4:7], s95 offen offset:3072 sc1\n"
"buffer_load_dwordx4 v[196:199], v1, s[4:7], s95 offen sc1\n"
"buffer_load_dwordx4 v[100:103], v1, s[4:7], s95 offen offset:1024 sc1\n"
"s_waitcnt vmcnt(0)\n"
"v_max_u32_e32 v254, v144, v145\n"
"v_max3_u32 v254, v254, v146, v147\n"
"v_max3_u32 v254, v254, v148, v149\n"
"v_max3_u32 v254, v254, v150, v151\n"
"v_max3_u32 v254, v254, v152, v153\n"
"v_max3_u32 v254, v254, v154, v155\n"
"v_max3_u32 v254, v254, v156, v157\n"
"v_max3_u32 v254, v254, v158, v159\n"
"v_cmp_eq_u32_e32 vcc, -1, v254\n"
"s_cbranch_vccz .Lmk_q53k\n"
"s_sub_u32 s1, s1, 1\n"
"s_cbranch_scc0 .Lmk_q53l\n"
".Lmk_q53k:\n"
"s_branch .Lmk_q53r\n"
".Lmk_p52s:\n"
";\n"
"s_mov_b32 s1, 0x10000\n"
".Lmk_p52l:\n"
"s_add_u32 s95, s54, 0x440000\n"
"buffer_load_dwordx4 v[88:91], v0, s[4:7], s95 offen sc1\n"
"buffer_load_dwordx4 v[96:99], v0, s[4:7], s95 offen offset:1024 sc1\n"
"buffer_load_dwordx4 v[108:111], v0, s[4:7], s95 offen offset:2048 sc1\n"
"buffer_load_dwordx4 v[116:119], v0, s[4:7], s95 offen offset:3072 sc1\n"
"buffer_load_dwordx4 v[136:139], v1, s[4:7], s95 offen sc1\n"
"buffer_load_dwordx4 v[188:191], v1, s[4:7], s95 offen offset:1024 sc1\n"
"buffer_load_dwordx4 v[212:215], v1, s[4:7], s95 offen offset:2048 sc1\n"
"buffer_load_dwordx4 v[220:223], v1, s[4:7], s95 offen offset:3072 sc1\n"
"s_add_u32 s95, s55, 0x440000\n"
"buffer_load_dwordx4 v[144:147], v0, s[4:7], s95 offen sc1\n"
"buffer_load_dwordx4 v[148:151], v0, s[4:7], s95 offen offset:1024 sc1\n"
"buffer_load_dwordx4 v[152:155], v0, s[4:7], s95 offen offset:2048 sc1\n"
"buffer_load_dwordx4 v[156:159], v0, s[4:7], s95 offen offset:3072 sc1\n"
"buffer_load_dwordx4 v[160:163], v1, s[4:7], s95 offen sc1\n"
"buffer_load_dwordx4 v[164:167], v1, s[4:7], s95 offen offset:1024 sc1\n"
"buffer_load_dwordx4 v[200:203], v1, s[4:7], s95 offen offset:2048 sc1\n"
"buffer_load_dwordx4 v[176:179], v1, s[4:7], s95 offen offset:3072 sc1\n"
"s_add_u32 s95, s52, 0x448000\n"
"buffer_load_dwordx4 v[104:107], v0, s[4:7], s95 offen sc1\n"
"buffer_load_dwordx4 v[120:123], v0, s[4:7], s95 offen offset:1024 sc1\n"
"buffer_load_dwordx4 v[112:115], v0, s[4:7], s95 offen offset:2048 sc1\n"
"buffer_load_dwordx4 v[128:131], v0, s[4:7], s95 offen offset:3072 sc1\n"
"buffer_load_dwordx4 v[196:199], v1, s[4:7], s95 offen sc1\n"
"buffer_load_dwordx4 v[100:103], v1, s[4:7], s95 offen offset:1024 sc1\n"
"s_waitcnt vmcnt(0)\n"
"v_max_u32_e32 v254, v88, v89\n"
"v_max3_u32 v254, v254, v90, v91\n"
"v_max3_u32 v254, v254, v96, v97\n"
"v_max3_u32 v254, v254, v98, v99\n"
"v_max3_u32 v254, v254, v108, v109\n"
"v_max3_u32 v254, v254, v110, v111\n"
"v_max3_u32 v254, v254, v116, v117\n"
"v_max3_u32 v254, v254, v118, v119\n"
"v_max3_u32 v254, v254, v136, v137\n"
"v_max3_u32 v254, v254, v138, v139\n"
"v_max3_u32 v254, v254, v188, v189\n"
"v_max3_u32 v254, v254, v190, v191\n"
"v_max3_u32 v254, v254, v212, v213\n"
"v_max3_u32 v254, v254, v214, v215\n"
"v_max3_u32 v254, v254, v220, v221\n"
"v_max3_u32 v254, v254, v222, v223\n"
"v_cmp_eq_u32_e32 vcc, -1, v254\n"
"s_cbranch_vccz .Lmk_p52k\n"
"s_sub_u32 s1, s1, 1\n"
"s_cbranch_scc0 .Lmk_p52l\n"
".Lmk_p52k:\n"
"v_mfma_f32_16x16x32_bf16 v[180:183], a[0:3], v[88:91], 0\n"
"v_mfma_f32_16x16x32_bf16 v[204:207], a[4:7], v[88:91], 0\n"
"v_mfma_f32_16x16x32_bf16 v[172:175], a[8:11], v[88:91], 0\n"
"v_mfma_f32_16x16x32_bf16 v[208:211], a[12:15], v[88:91], 0\n"
"v_mfma_f32_16x16x32_bf16 v[124:127], a[16:19], v[88:91], 0\n"
"v_mfma_f32_16x16x32_bf16 v[216:219], a[20:23], v[88:91], 0\n"
"v_mfma_f32_16x16x32_bf16 v[180:183], a[24:27], v[96:99], v[180:183]\n"
"v_mfma_f32_16x16x32_bf16 v[204:207], a[28:31], v[96:99], v[204:207]\n"
"v_mfma_f32_16x16x32_bf16 v[172:175], a[32:35], v[96:99], v[172:175]\n"
"v_mfma_f32_16x16x32_bf16 v[208:211], a[36:39], v[96:99], v[208:211]\n"
"v_mfma_f32_16x16x32_bf16 v[124:127], a[40:43], v[96:99], v[124:127]\n"
"v_mfma_f32_16x16x32_bf16 v[216:219], a[44:47], v[96:99], v[216:219]\n"
"v_mfma_f32_16x16x32_bf16 v[180:183], a[48:51], v[108:111], v[180:183]\n"
"v_mfma_f32_16x16x32_bf16 v[204:207], a[52:55], v[108:111], v[204:207]\n"
"v_mfma_f32_16x16x32_bf16 v[172:175], a[56:59], v[108:111], v[172:175]\n"
"v_mfma_f32_16x16x32_bf16 v[208:211], a[60:63], v[108:111], v[208:211]\n"
"v_mfma_f32_16x16x32_bf16 v[124:127], a[64:67], v[108:111], v[124:127]\n"
"v_mfma_f32_16x16x32_bf16 v[216:219], a[68:71], v[108:111], v[216:219]\n"
"v_mfma_f32_16x16x32_bf16 v[180:183], a[72:75], v[116:119], v[180:183]\n"
"v_mfma_f32_16x16x32_bf16 v[204:207], a[76:79], v[116:119], v[204:207]\n"
"v_mfma_f32_16x16x32_bf16 v[172:175], a[80:83], v[116:119], v[172:175]\n"
"v_mfma_f32_16x16x32_bf16 v[208:211], a[84:87], v[116:119], v[208:211]\n"
"v_mfma_f32_16x16x32_bf16 v[124:127], a[88:91], v[116:119], v[124:127]\n"
"v_mfma_f32_16x16x32_bf16 v[216:219], a[92:95], v[116:119], v[216:219]\n"
"v_mfma_f32_16x16x32_bf16 v[180:183], a[96:99], v[136:139], v[180:183]\n"
"v_mfma_f32_16x16x32_bf16 v[204:207], a[100:103], v[136:139], v[204:207]\n"
"v_mfma_f32_16x16x32_bf16 v[172:175], a[104:107], v[136:139], v[172:175]\n"
"v_mfma_f32_16x16x32_bf16 v[208:211], a[108:111], v[136:139], v[208:211]\n"
"v_mfma_f32_16x16x32_bf16 v[124:127], a[112:115], v[136:139], v[124:127]\n"
"v_mfma_f32_16x16x32_bf16 v[216:219], a[116:119], v[136:139], v[216:219]\n"
"v_mfma_f32_16x16x32_bf16 v[180:183], a[120:123], v[188:191], v[180:183]\n"
"v_mfma_f32_16x16x32_bf16 v[204:207], a[124:127], v[188:191], v[204:207]\n"
"v_mfma_f32_16x16x32_bf16 v[172:175], v[24:27], v[188:191], v[172:175]\n"
"v_mfma_f32_16x16x32_bf16 v[208:211], v[28:31], v[188:191], v[208:211]\n"
"v_mfma_f32_16x16x32_bf16 v[124:127], v[32:35], v[188:191], v[124:127]\n"
"v_mfma_f32_16x16x32_bf16 v[216:219], v[36:39], v[188:191], v[216:219]\n"
"v_mfma_f32_16x16x32_bf16 v[180:183], v[40:43], v[212:215], v[180:183]\n"
"v_mfma_f32_16x16x32_bf16 v[204:207], v[44:47], v[212:215], v[204:207]\n"
"v_mfma_f32_16x16x32_bf16 v[172:175], v[48:51], v[212:215], v[172:175]\n"
"v_mfma_f32_16x16x32_bf16 v[208:211], v[52:55], v[212:215], v[208:211]\n"
"v_mfma_f32_16x16x32_bf16 v[124:127], v[56:59], v[212:215], v[124:127]\n"
"v_mfma_f32_16x16x32_bf16 v[216:219], v[60:63], v[212:215], v[216:219]\n"
"v_mfma_f32_16x16x32_bf16 v[180:183], v[64:67], v[220:223], v[180:183]\n"
"v_mfma_f32_16x16x32_bf16 v[204:207], v[68:71], v[220:223], v[204:207]\n"
"v_mfma_f32_16x16x32_bf16 v[172:175], v[72:75], v[220:223], v[172:175]\n"
"v_mfma_f32_16x16x32_bf16 v[208:211], v[76:79], v[220:223], v[208:211]\n"
"v_mfma_f32_16x16x32_bf16 v[124:127], v[80:83], v[220:223], v[124:127]\n"
"v_mfma_f32_16x16x32_bf16 v[216:219], v[84:87], v[220:223], v[216:219]\n"
"s_nop 7\n"
"s_branch .Lmk_p52r\n"
".Lmk_q54s:\n"
";\n"
"s_mov_b32 s1, 0x10000\n"
".Lmk_q54l:\n"
"s_add_u32 s96, s55, 0x440000\n"
"buffer_load_dwordx4 v[160:163], v1, s[4:7], s96 offen sc1\n"
"buffer_load_dwordx4 v[164:167], v1, s[4:7], s96 offen offset:1024 sc1\n"
"buffer_load_dwordx4 v[200:203], v1, s[4:7], s96 offen offset:2048 sc1\n"
"buffer_load_dwordx4 v[176:179], v1, s[4:7], s96 offen offset:3072 sc1\n"
"s_add_u32 s96, s52, 0x448000\n"
"buffer_load_dwordx4 v[104:107], v0, s[4:7], s96 offen sc1\n"
"buffer_load_dwordx4 v[120:123], v0, s[4:7], s96 offen offset:1024 sc1\n"
"buffer_load_dwordx4 v[112:115], v0, s[4:7], s96 offen offset:2048 sc1\n"
"buffer_load_dwordx4 v[128:131], v0, s[4:7], s96 offen offset:3072 sc1\n"
"buffer_load_dwordx4 v[196:199], v1, s[4:7], s96 offen sc1\n"
"buffer_load_dwordx4 v[100:103], v1, s[4:7], s96 offen offset:1024 sc1\n"
"buffer_load_dwordx4 v[88:91], v1, s[4:7], s96 offen offset:2048 sc1\n"
"buffer_load_dwordx4 v[96:99], v1, s[4:7], s96 offen offset:3072 sc1\n"
"s_add_u32 s96, s53, 0x448000\n"
"buffer_load_dwordx4 v[108:111], v0, s[4:7], s96 offen sc1\n"
"buffer_load_dwordx4 v[116:119], v0, s[4:7], s96 offen offset:1024 sc1\n"
"s_waitcnt vmcnt(0)\n"
"v_max_u32_e32 v254, v160, v161\n"
"v_max3_u32 v254, v254, v162, v163\n"
"v_max3_u32 v254, v254, v164, v165\n"
"v_max3_u32 v254, v254, v166, v167\n"
"v_max3_u32 v254, v254, v200, v201\n"
"v_max3_u32 v254, v254, v202, v203\n"
"v_max3_u32 v254, v254, v176, v177\n"
"v_max3_u32 v254, v254, v178, v179\n"
"v_cmp_eq_u32_e32 vcc, -1, v254\n"
"s_cbranch_vccz .Lmk_q54k\n"
"s_sub_u32 s1, s1, 1\n"
"s_cbranch_scc0 .Lmk_q54l\n"
".Lmk_q54k:\n"
"s_branch .Lmk_q54r\n"
".Lmk_p56s:\n"
";\n"
"s_mov_b32 s1, 0x10000\n"
".Lmk_p56l:\n"
"s_add_u32 s98, s52, 0x448000\n"
"buffer_load_dwordx4 v[104:107], v0, s[4:7], s98 offen sc1\n"
"buffer_load_dwordx4 v[148:151], v0, s[4:7], s98 offen offset:1024 sc1\n"
"buffer_load_dwordx4 v[120:123], v0, s[4:7], s98 offen offset:2048 sc1\n"
"buffer_load_dwordx4 v[92:95], v0, s[4:7], s98 offen offset:3072 sc1\n"
"buffer_load_dwordx4 v[112:115], v1, s[4:7], s98 offen sc1\n"
"buffer_load_dwordx4 v[132:135], v1, s[4:7], s98 offen offset:1024 sc1\n"
"buffer_load_dwordx4 v[100:103], v1, s[4:7], s98 offen offset:2048 sc1\n"
"buffer_load_dwordx4 v[88:91], v1, s[4:7], s98 offen offset:3072 sc1\n"
"s_add_u32 s98, s53, 0x448000\n"
"buffer_load_dwordx4 v[108:111], v0, s[4:7], s98 offen sc1\n"
"buffer_load_dwordx4 v[116:119], v0, s[4:7], s98 offen offset:1024 sc1\n"
"buffer_load_dwordx4 v[124:127], v0, s[4:7], s98 offen offset:2048 sc1\n"
"buffer_load_dwordx4 v[136:139], v0, s[4:7], s98 offen offset:3072 sc1\n"
"buffer_load_dwordx4 v[144:147], v1, s[4:7], s98 offen sc1\n"
"buffer_load_dwordx4 v[172:175], v1, s[4:7], s98 offen offset:1024 sc1\n"
"buffer_load_dwordx4 v[152:155], v1, s[4:7], s98 offen offset:2048 sc1\n"
"buffer_load_dwordx4 v[160:163], v1, s[4:7], s98 offen offset:3072 sc1\n"
"s_waitcnt vmcnt(0)\n"
"v_max_u32_e32 v255, v104, v105\n"
"v_max3_u32 v255, v255, v106, v107\n"
"v_max3_u32 v255, v255, v148, v149\n"
"v_max3_u32 v255, v255, v150, v151\n"
"v_max3_u32 v255, v255, v120, v121\n"
"v_max3_u32 v255, v255, v122, v123\n"
"v_max3_u32 v255, v255, v92, v93\n"
"v_max3_u32 v255, v255, v94, v95\n"
"v_max3_u32 v255, v255, v112, v113\n"
"v_max3_u32 v255, v255, v114, v115\n"
"v_max3_u32 v255, v255, v132, v133\n"
"v_max3_u32 v255, v255, v134, v135\n"
"v_max3_u32 v255, v255, v100, v101\n"
"v_max3_u32 v255, v255, v102, v103\n"
"v_max3_u32 v255, v255, v88, v89\n"
"v_max3_u32 v255, v255, v90, v91\n"
"v_cmp_eq_u32_e32 vcc, -1, v255\n"
"s_cbranch_vccz .Lmk_p56k\n"
"s_sub_u32 s1, s1, 1\n"
"s_cbranch_scc0 .Lmk_p56l\n"
".Lmk_p56k:\n"
"v_mfma_f32_16x16x32_bf16 v[176:179], a[0:3], v[104:107], 0\n"
"v_mfma_f32_16x16x32_bf16 v[180:183], a[4:7], v[104:107], 0\n"
"v_mfma_f32_16x16x32_bf16 v[204:207], a[8:11], v[104:107], 0\n"
"v_mfma_f32_16x16x32_bf16 v[208:211], a[12:15], v[104:107], 0\n"
"v_mfma_f32_16x16x32_bf16 v[216:219], a[16:19], v[104:107], 0\n"
"v_mfma_f32_16x16x32_bf16 v[220:223], a[20:23], v[104:107], 0\n"
"v_mfma_f32_16x16x32_bf16 v[176:179], a[24:27], v[148:151], v[176:179]\n"
"v_mfma_f32_16x16x32_bf16 v[180:183], a[28:31], v[148:151], v[180:183]\n"
"v_mfma_f32_16x16x32_bf16 v[204:207], a[32:35], v[148:151], v[204:207]\n"
"v_mfma_f32_16x16x32_bf16 v[208:211], a[36:39], v[148:151], v[208:211]\n"
"v_mfma_f32_16x16x32_bf16 v[216:219], a[40:43], v[148:151], v[216:219]\n"
"v_mfma_f32_16x16x32_bf16 v[220:223], a[44:47], v[148:151], v[220:223]\n"
"v_mfma_f32_16x16x32_bf16 v[176:179], a[48:51], v[120:123], v[176:179]\n"
"v_mfma_f32_16x16x32_bf16 v[180:183], a[52:55], v[120:123], v[180:183]\n"
"v_mfma_f32_16x16x32_bf16 v[204:207], a[56:59], v[120:123], v[204:207]\n"
"v_mfma_f32_16x16x32_bf16 v[208:211], a[60:63], v[120:123], v[208:211]\n"
"v_mfma_f32_16x16x32_bf16 v[216:219], a[64:67], v[120:123], v[216:219]\n"
"v_mfma_f32_16x16x32_bf16 v[220:223], a[68:71], v[120:123], v[220:223]\n"
"v_mfma_f32_16x16x32_bf16 v[176:179], a[72:75], v[92:95], v[176:179]\n"
"v_mfma_f32_16x16x32_bf16 v[180:183], a[76:79], v[92:95], v[180:183]\n"
"v_mfma_f32_16x16x32_bf16 v[204:207], a[80:83], v[92:95], v[204:207]\n"
"v_mfma_f32_16x16x32_bf16 v[208:211], a[84:87], v[92:95], v[208:211]\n"
"v_mfma_f32_16x16x32_bf16 v[216:219], a[88:91], v[92:95], v[216:219]\n"
"v_mfma_f32_16x16x32_bf16 v[220:223], a[92:95], v[92:95], v[220:223]\n"
"v_mfma_f32_16x16x32_bf16 v[176:179], a[96:99], v[112:115], v[176:179]\n"
"v_mfma_f32_16x16x32_bf16 v[180:183], a[100:103], v[112:115], v[180:183]\n"
"v_mfma_f32_16x16x32_bf16 v[204:207], a[104:107], v[112:115], v[204:207]\n"
"v_mfma_f32_16x16x32_bf16 v[208:211], a[108:111], v[112:115], v[208:211]\n"
"v_mfma_f32_16x16x32_bf16 v[216:219], a[112:115], v[112:115], v[216:219]\n"
"v_mfma_f32_16x16x32_bf16 v[220:223], a[116:119], v[112:115], v[220:223]\n"
"v_mfma_f32_16x16x32_bf16 v[176:179], a[120:123], v[132:135], v[176:179]\n"
"v_mfma_f32_16x16x32_bf16 v[180:183], a[124:127], v[132:135], v[180:183]\n"
"v_mfma_f32_16x16x32_bf16 v[204:207], v[24:27], v[132:135], v[204:207]\n"
"v_mfma_f32_16x16x32_bf16 v[208:211], v[28:31], v[132:135], v[208:211]\n"
"v_mfma_f32_16x16x32_bf16 v[216:219], v[32:35], v[132:135], v[216:219]\n"
"v_mfma_f32_16x16x32_bf16 v[220:223], v[36:39], v[132:135], v[220:223]\n"
"v_mfma_f32_16x16x32_bf16 v[176:179], v[40:43], v[100:103], v[176:179]\n"
"v_mfma_f32_16x16x32_bf16 v[180:183], v[44:47], v[100:103], v[180:183]\n"
"v_mfma_f32_16x16x32_bf16 v[204:207], v[48:51], v[100:103], v[204:207]\n"
"v_mfma_f32_16x16x32_bf16 v[208:211], v[52:55], v[100:103], v[208:211]\n"
"v_mfma_f32_16x16x32_bf16 v[216:219], v[56:59], v[100:103], v[216:219]\n"
"v_mfma_f32_16x16x32_bf16 v[220:223], v[60:63], v[100:103], v[220:223]\n"
"v_mfma_f32_16x16x32_bf16 v[176:179], v[64:67], v[88:91], v[176:179]\n"
"v_mfma_f32_16x16x32_bf16 v[180:183], v[68:71], v[88:91], v[180:183]\n"
"v_mfma_f32_16x16x32_bf16 v[204:207], v[72:75], v[88:91], v[204:207]\n"
"v_mfma_f32_16x16x32_bf16 v[208:211], v[76:79], v[88:91], v[208:211]\n"
"v_mfma_f32_16x16x32_bf16 v[216:219], v[80:83], v[88:91], v[216:219]\n"
"v_mfma_f32_16x16x32_bf16 v[220:223], v[84:87], v[88:91], v[220:223]\n"
"s_add_u32 s98, s54, 0x448000\n"
"v_mov_b32_e32 v255, 0x80000000\n"
"buffer_load_dwordx4 v[104:107], v0, s[4:7], s98 offen sc1\n"
"buffer_store_dword v255, v255, s[4:7], 0 offen\n"
"buffer_store_dword v255, v255, s[4:7], 0 offen\n"
"buffer_load_dwordx4 v[148:151], v0, s[4:7], s98 offen offset:1024 sc1\n"
"buffer_load_dwordx4 v[120:123], v0, s[4:7], s98 offen offset:2048 sc1\n"
"buffer_load_dwordx4 v[92:95], v0, s[4:7], s98 offen offset:3072 sc1\n"
"buffer_load_dwordx4 v[112:115], v1, s[4:7], s98 offen sc1\n"
"buffer_store_dword v255, v255, s[4:7], 0 offen\n"
"buffer_load_dwordx4 v[132:135], v1, s[4:7], s98 offen offset:1024 sc1\n"
"buffer_load_dwordx4 v[100:103], v1, s[4:7], s98 offen offset:2048 sc1\n"
"buffer_load_dwordx4 v[88:91], v1, s[4:7], s98 offen offset:3072 sc1\n"
"s_nop 7\n"
"s_branch .Lmk_p56r\n"
".Lmk_m61s:\n"
";\n"
"s_mov_b32 s1, 0x10000\n"
".Lmk_m61l:\n"
"s_add_u32 s95, s62, 0x44800\n"
"buffer_load_dwordx2 v[250:251], v10, s[4:7], s95 offen sc1\n"
"buffer_load_dwordx2 v[248:249], v10, s[4:7], s95 offen offset:128 sc1\n"
"buffer_load_dwordx2 v[246:247], v10, s[4:7], s95 offen offset:256 sc1\n"
"buffer_load_dwordx2 v[244:245], v10, s[4:7], s95 offen offset:384 sc1\n"
"s_waitcnt vmcnt(0)\n"
"v_max_u32_e32 v253, v250, v251\n"
"v_max3_u32 v253, v253, v248, v249\n"
"v_max3_u32 v253, v253, v246, v247\n"
"v_max3_u32 v253, v253, v244, v245\n"
"v_cmp_eq_u32_e32 vcc, -1, v253\n"
"s_cbranch_vccz .Lmk_m61k\n"
"s_sub_u32 s1, s1, 1\n"
"s_cbranch_scc0 .Lmk_m61l\n"
".Lmk_m61k:\n"
"v_add_f32_e32 v250, v250, v248\n"
"v_add_f32_e32 v251, v251, v249\n"
"v_add_f32_e32 v246, v246, v244\n"
"v_add_f32_e32 v247, v247, v245\n"
"v_add_f32_e32 v250, v250, v246\n"
"v_add_f32_e32 v251, v251, v247\n"
"s_nop 1\n"
"s_branch .Lmk_m61r\n"
".Lmk_p57s:\n"
";\n"
"s_mov_b32 s1, 0x10000\n"
".Lmk_p57l:\n"
"s_add_u32 s98, s53, 0x448000\n"
"buffer_load_dwordx4 v[108:111], v0, s[4:7], s98 offen sc1\n"
"buffer_load_dwordx4 v[180:183], v0, s[4:7], s98 offen offset:1024 sc1\n"
"buffer_load_dwordx4 v[116:119], v0, s[4:7], s98 offen offset:2048 sc1\n"
"buffer_load_dwordx4 v[128:131], v0, s[4:7], s98 offen offset:3072 sc1\n"
"buffer_load_dwordx4 v[136:139], v1, s[4:7], s98 offen sc1\n"
"buffer_load_dwordx4 v[144:147], v1, s[4:7], s98 offen offset:1024 sc1\n"
"buffer_load_dwordx4 v[184:187], v1, s[4:7], s98 offen offset:2048 sc1\n"
"buffer_load_dwordx4 v[192:195], v1, s[4:7], s98 offen offset:3072 sc1\n"
"s_add_u32 s98, s54, 0x448000\n"
"buffer_load_dwordx4 v[104:107], v0, s[4:7], s98 offen sc1\n"
"buffer_load_dwordx4 v[148:151], v0, s[4:7], s98 offen offset:1024 sc1\n"
"buffer_load_dwordx4 v[120:123], v0, s[4:7], s98 offen offset:2048 sc1\n"
"buffer_load_dwordx4 v[92:95], v0, s[4:7], s98 offen offset:3072 sc1\n"
"buffer_load_dwordx4 v[112:115], v1, s[4:7], s98 offen sc1\n"
"buffer_load_dwordx4 v[132:135], v1, s[4:7], s98 offen offset:1024 sc1\n"
"buffer_load_dwordx4 v[100:103], v1, s[4:7], s98 offen offset:2048 sc1\n"
"buffer_load_dwordx4 v[88:91], v1, s[4:7], s98 offen offset:3072 sc1\n"
"s_waitcnt vmcnt(0)\n"
"v_max_u32_e32 v252, v108, v109\n"
"v_max3_u32 v252, v252, v110, v111\n"
"v_max3_u32 v252, v252, v180, v181\n"
"v_max3_u32 v252, v252, v182, v183\n"
"v_max3_u32 v252, v252, v116, v117\n"
"v_max3_u32 v252, v252, v118, v119\n"
"v_max3_u32 v252, v252, v128, v129\n"
"v_max3_u32 v252, v252, v130, v131\n"
"v_max3_u32 v252, v252, v136, v137\n"
"v_max3_u32 v252, v252, v138, v139\n"
"v_max3_u32 v252, v252, v144, v145\n"
"v_max3_u32 v252, v252, v146, v147\n"
"v_max3_u32 v252, v252, v184, v185\n"
"v_max3_u32 v252, v252, v186, v187\n"
"v_max3_u32 v252, v252, v192, v193\n"
"v_max3_u32 v252, v252, v194, v195\n"
"v_cmp_eq_u32_e32 vcc, -1, v252\n"
"s_cbranch_vccz .Lmk_p57k\n"
"s_sub_u32 s1, s1, 1\n"
"s_cbranch_scc0 .Lmk_p57l\n"
".Lmk_p57k:\n"
"v_mfma_f32_16x16x32_bf16 v[96:99], a[0:3], v[108:111], 0\n"
"v_mfma_f32_16x16x32_bf16 v[140:143], a[4:7], v[108:111], 0\n"
"v_mfma_f32_16x16x32_bf16 v[156:159], a[8:11], v[108:111], 0\n"
"v_mfma_f32_16x16x32_bf16 v[164:167], a[12:15], v[108:111], 0\n"
"v_mfma_f32_16x16x32_bf16 v[168:171], a[16:19], v[108:111], 0\n"
"v_mfma_f32_16x16x32_bf16 v[176:179], a[20:23], v[108:111], 0\n"
"v_mfma_f32_16x16x32_bf16 v[96:99], a[24:27], v[180:183], v[96:99]\n"
"v_mfma_f32_16x16x32_bf16 v[140:143], a[28:31], v[180:183], v[140:143]\n"
"v_mfma_f32_16x16x32_bf16 v[156:159], a[32:35], v[180:183], v[156:159]\n"
"v_mfma_f32_16x16x32_bf16 v[164:167], a[36:39], v[180:183], v[164:167]\n"
"v_mfma_f32_16x16x32_bf16 v[168:171], a[40:43], v[180:183], v[168:171]\n"
"v_mfma_f32_16x16x32_bf16 v[176:179], a[44:47], v[180:183], v[176:179]\n"
"v_mfma_f32_16x16x32_bf16 v[96:99], a[48:51], v[116:119], v[96:99]\n"
"v_mfma_f32_16x16x32_bf16 v[140:143], a[52:55], v[116:119], v[140:143]\n"
"v_mfma_f32_16x16x32_bf16 v[156:159], a[56:59], v[116:119], v[156:159]\n"
"v_mfma_f32_16x16x32_bf16 v[164:167], a[60:63], v[116:119], v[164:167]\n"
"v_mfma_f32_16x16x32_bf16 v[168:171], a[64:67], v[116:119], v[168:171]\n"
"v_mfma_f32_16x16x32_bf16 v[176:179], a[68:71], v[116:119], v[176:179]\n"
"v_mfma_f32_16x16x32_bf16 v[96:99], a[72:75], v[128:131], v[96:99]\n"
"v_mfma_f32_16x16x32_bf16 v[140:143], a[76:79], v[128:131], v[140:143]\n"
"v_mfma_f32_16x16x32_bf16 v[156:159], a[80:83], v[128:131], v[156:159]\n"
"v_mfma_f32_16x16x32_bf16 v[164:167], a[84:87], v[128:131], v[164:167]\n"
"v_mfma_f32_16x16x32_bf16 v[168:171], a[88:91], v[128:131], v[168:171]\n"
"v_mfma_f32_16x16x32_bf16 v[176:179], a[92:95], v[128:131], v[176:179]\n"
"v_mfma_f32_16x16x32_bf16 v[96:99], a[96:99], v[136:139], v[96:99]\n"
"v_mfma_f32_16x16x32_bf16 v[140:143], a[100:103], v[136:139], v[140:143]\n"
"v_mfma_f32_16x16x32_bf16 v[156:159], a[104:107], v[136:139], v[156:159]\n"
"v_mfma_f32_16x16x32_bf16 v[164:167], a[108:111], v[136:139], v[164:167]\n"
"v_mfma_f32_16x16x32_bf16 v[168:171], a[112:115], v[136:139], v[168:171]\n"
"v_mfma_f32_16x16x32_bf16 v[176:179], a[116:119], v[136:139], v[176:179]\n"
"v_mfma_f32_16x16x32_bf16 v[96:99], a[120:123], v[144:147], v[96:99]\n"
"v_mfma_f32_16x16x32_bf16 v[140:143], a[124:127], v[144:147], v[140:143]\n"
"v_mfma_f32_16x16x32_bf16 v[156:159], v[24:27], v[144:147], v[156:159]\n"
"v_mfma_f32_16x16x32_bf16 v[164:167], v[28:31], v[144:147], v[164:167]\n"
"v_mfma_f32_16x16x32_bf16 v[168:171], v[32:35], v[144:147], v[168:171]\n"
"v_mfma_f32_16x16x32_bf16 v[176:179], v[36:39], v[144:147], v[176:179]\n"
"v_mfma_f32_16x16x32_bf16 v[96:99], v[40:43], v[184:187], v[96:99]\n"
"v_mfma_f32_16x16x32_bf16 v[140:143], v[44:47], v[184:187], v[140:143]\n"
"v_mfma_f32_16x16x32_bf16 v[156:159], v[48:51], v[184:187], v[156:159]\n"
"v_mfma_f32_16x16x32_bf16 v[164:167], v[52:55], v[184:187], v[164:167]\n"
"v_mfma_f32_16x16x32_bf16 v[168:171], v[56:59], v[184:187], v[168:171]\n"
"v_mfma_f32_16x16x32_bf16 v[176:179], v[60:63], v[184:187], v[176:179]\n"
"v_mfma_f32_16x16x32_bf16 v[96:99], v[64:67], v[192:195], v[96:99]\n"
"v_mfma_f32_16x16x32_bf16 v[140:143], v[68:71], v[192:195], v[140:143]\n"
"v_mfma_f32_16x16x32_bf16 v[156:159], v[72:75], v[192:195], v[156:159]\n"
"v_mfma_f32_16x16x32_bf16 v[164:167], v[76:79], v[192:195], v[164:167]\n"
"v_mfma_f32_16x16x32_bf16 v[168:171], v[80:83], v[192:195], v[168:171]\n"
"v_mfma_f32_16x16x32_bf16 v[176:179], v[84:87], v[192:195], v[176:179]\n"
"s_add_u32 s98, s55, 0x448000\n"
"v_mov_b32_e32 v252, 0x80000000\n"
"buffer_load_dwordx4 v[108:111], v0, s[4:7], s98 offen sc1\n"
"buffer_store_dword v252, v252, s[4:7], 0 offen\n"
"buffer_load_dwordx4 v[180:183], v0, s[4:7], s98 offen offset:1024 sc1\n"
"buffer_store_dword v252, v252, s[4:7], 0 offen\n"
"buffer_load_dwordx4 v[116:119], v0, s[4:7], s98 offen offset:2048 sc1\n"
"buffer_load_dwordx4 v[128:131], v0, s[4:7], s98 offen offset:3072 sc1\n"
"buffer_store_dword v252, v252, s[4:7], 0 offen\n"
"buffer_load_dwordx4 v[136:139], v1, s[4:7], s98 offen sc1\n"
"buffer_store_dword v252, v252, s[4:7], 0 offen\n"
"buffer_load_dwordx4 v[144:147], v1, s[4:7], s98 offen offset:1024 sc1\n"
"buffer_load_dwordx4 v[184:187], v1, s[4:7], s98 offen offset:2048 sc1\n"
"buffer_load_dwordx4 v[192:195], v1, s[4:7], s98 offen offset:3072 sc1\n"
"s_nop 7\n"
"s_branch .Lmk_p57r\n"
".Lmk_f46s:\n"
";\n"
"s_mov_b32 s1, 0x10000\n"
".Lmk_f46l:\n"
"buffer_load_dwordx2 v[254:255], v12, s[4:7], s96 offen sc1\n"
"s_waitcnt vmcnt(0)\n"
"v_cmp_ne_u64_e32 vcc, v[254:255], v[18:19]\n"
"s_cbranch_vccz .Lmk_f46r\n"
"s_sub_u32 s1, s1, 1\n"
"s_cbranch_scc0 .Lmk_f46l\n"
"s_branch .Lmk_f46r\n"
".Lmk_q59s:\n"
";\n"
"s_mov_b32 s1, 0x10000\n"
".Lmk_q59l:\n"
"s_add_u32 s96, s55, 0x448000\n"
"buffer_load_dwordx4 v[108:111], v0, s[4:7], s96 offen sc1\n"
"buffer_load_dwordx4 v[180:183], v0, s[4:7], s96 offen offset:1024 sc1\n"
"buffer_load_dwordx4 v[116:119], v0, s[4:7], s96 offen offset:2048 sc1\n"
"buffer_load_dwordx4 v[128:131], v0, s[4:7], s96 offen offset:3072 sc1\n"
"buffer_load_dwordx4 v[136:139], v1, s[4:7], s96 offen sc1\n"
"buffer_load_dwordx4 v[144:147], v1, s[4:7], s96 offen offset:1024 sc1\n"
"buffer_load_dwordx4 v[184:187], v1, s[4:7], s96 offen offset:2048 sc1\n"
"buffer_load_dwordx4 v[192:195], v1, s[4:7], s96 offen offset:3072 sc1\n"
"s_waitcnt vmcnt(0)\n"
"v_max_u32_e32 v255, v108, v109\n"
"v_max3_u32 v255, v255, v110, v111\n"
"v_max3_u32 v255, v255, v180, v181\n"
"v_max3_u32 v255, v255, v182, v183\n"
"v_max3_u32 v255, v255, v116, v117\n"
"v_max3_u32 v255, v255, v118, v119\n"
"v_max3_u32 v255, v255, v128, v129\n"
"v_max3_u32 v255, v255, v130, v131\n"
"v_cmp_eq_u32_e32 vcc, -1, v255\n"
"s_cbranch_vccz .Lmk_q59k\n"
"s_sub_u32 s1, s1, 1\n"
"s_cbranch_scc0 .Lmk_q59l\n"
".Lmk_q59k:\n"
"s_branch .Lmk_q59r\n"
".Lmk_p58s:\n"
";\n"
"s_mov_b32 s1, 0x10000\n"
".Lmk_p58l:\n"
"s_add_u32 s96, s54, 0x448000\n"
"buffer_load_dwordx4 v[88:91], v0, s[4:7], s96 offen sc1\n"
"buffer_load_dwordx4 v[100:103], v0, s[4:7], s96 offen offset:1024 sc1\n"
"buffer_load_dwordx4 v[132:135], v0, s[4:7], s96 offen offset:2048 sc1\n"
"buffer_load_dwordx4 v[152:155], v0, s[4:7], s96 offen offset:3072 sc1\n"
"buffer_load_dwordx4 v[168:171], v1, s[4:7], s96 offen sc1\n"
"buffer_load_dwordx4 v[196:199], v1, s[4:7], s96 offen offset:1024 sc1\n"
"buffer_load_dwordx4 v[216:219], v1, s[4:7], s96 offen offset:2048 sc1\n"
"buffer_load_dwordx4 v[224:227], v1, s[4:7], s96 offen offset:3072 sc1\n"
"s_add_u32 s96, s55, 0x448000\n"
"buffer_load_dwordx4 v[108:111], v0, s[4:7], s96 offen sc1\n"
"buffer_load_dwordx4 v[180:183], v0, s[4:7], s96 offen offset:1024 sc1\n"
"buffer_load_dwordx4 v[116:119], v0, s[4:7], s96 offen offset:2048 sc1\n"
"buffer_load_dwordx4 v[128:131], v0, s[4:7], s96 offen offset:3072 sc1\n"
"buffer_load_dwordx4 v[136:139], v1, s[4:7], s96 offen sc1\n"
"buffer_load_dwordx4 v[144:147], v1, s[4:7], s96 offen offset:1024 sc1\n"
"buffer_load_dwordx4 v[184:187], v1, s[4:7], s96 offen offset:2048 sc1\n"
"buffer_load_dwordx4 v[192:195], v1, s[4:7], s96 offen offset:3072 sc1\n"
"s_waitcnt vmcnt(0)\n"
"v_max_u32_e32 v255, v88, v89\n"
"v_max3_u32 v255, v255, v90, v91\n"
"v_max3_u32 v255, v255, v100, v101\n"
"v_max3_u32 v255, v255, v102, v103\n"
"v_max3_u32 v255, v255, v132, v133\n"
"v_max3_u32 v255, v255, v134, v135\n"
"v_max3_u32 v255, v255, v152, v153\n"
"v_max3_u32 v255, v255, v154, v155\n"
"v_max3_u32 v255, v255, v168, v169\n"
"v_max3_u32 v255, v255, v170, v171\n"
"v_max3_u32 v255, v255, v196, v197\n"
"v_max3_u32 v255, v255, v198, v199\n"
"v_max3_u32 v255, v255, v216, v217\n"
"v_max3_u32 v255, v255, v218, v219\n"
"v_max3_u32 v255, v255, v224, v225\n"
"v_max3_u32 v255, v255, v226, v227\n"
"v_cmp_eq_u32_e32 vcc, -1, v255\n"
"s_cbranch_vccz .Lmk_p58k\n"
"s_sub_u32 s1, s1, 1\n"
"s_cbranch_scc0 .Lmk_p58l\n"
".Lmk_p58k:\n"
"v_mfma_f32_16x16x32_bf16 v[160:163], a[0:3], v[88:91], 0\n"
"v_mfma_f32_16x16x32_bf16 v[200:203], a[4:7], v[88:91], 0\n"
"v_mfma_f32_16x16x32_bf16 v[204:207], a[8:11], v[88:91], 0\n"
"v_mfma_f32_16x16x32_bf16 v[212:215], a[12:15], v[88:91], 0\n"
"v_mfma_f32_16x16x32_bf16 v[96:99], a[16:19], v[88:91], 0\n"
"v_mfma_f32_16x16x32_bf16 v[220:223], a[20:23], v[88:91], 0\n"
"v_mfma_f32_16x16x32_bf16 v[160:163], a[24:27], v[100:103], v[160:163]\n"
"v_mfma_f32_16x16x32_bf16 v[200:203], a[28:31], v[100:103], v[200:203]\n"
"v_mfma_f32_16x16x32_bf16 v[204:207], a[32:35], v[100:103], v[204:207]\n"
"v_mfma_f32_16x16x32_bf16 v[212:215], a[36:39], v[100:103], v[212:215]\n"
"v_mfma_f32_16x16x32_bf16 v[96:99], a[40:43], v[100:103], v[96:99]\n"
"v_mfma_f32_16x16x32_bf16 v[220:223], a[44:47], v[100:103], v[220:223]\n"
"v_mfma_f32_16x16x32_bf16 v[160:163], a[48:51], v[132:135], v[160:163]\n"
"v_mfma_f32_16x16x32_bf16 v[200:203], a[52:55], v[132:135], v[200:203]\n"
"v_mfma_f32_16x16x32_bf16 v[204:207], a[56:59], v[132:135], v[204:207]\n"
"v_mfma_f32_16x16x32_bf16 v[212:215], a[60:63], v[132:135], v[212:215]\n"
"v_mfma_f32_16x16x32_bf16 v[96:99], a[64:67], v[132:135], v[96:99]\n"
"v_mfma_f32_16x16x32_bf16 v[220:223], a[68:71], v[132:135], v[220:223]\n"
"v_mfma_f32_16x16x32_bf16 v[160:163], a[72:75], v[152:155], v[160:163]\n"
"v_mfma_f32_16x16x32_bf16 v[200:203], a[76:79], v[152:155], v[200:203]\n"
"v_mfma_f32_16x16x32_bf16 v[204:207], a[80:83], v[152:155], v[204:207]\n"
"v_mfma_f32_16x16x32_bf16 v[212:215], a[84:87], v[152:155], v[212:215]\n"
"v_mfma_f32_16x16x32_bf16 v[96:99], a[88:91], v[152:155], v[96:99]\n"
"v_mfma_f32_16x16x32_bf16 v[220:223], a[92:95], v[152:155], v[220:223]\n"
"v_mfma_f32_16x16x32_bf16 v[160:163], a[96:99], v[168:171], v[160:163]\n"
"v_mfma_f32_16x16x32_bf16 v[200:203], a[100:103], v[168:171], v[200:203]\n"
"v_mfma_f32_16x16x32_bf16 v[204:207], a[104:107], v[168:171], v[204:207]\n"
"v_mfma_f32_16x16x32_bf16 v[212:215], a[108:111], v[168:171], v[212:215]\n"
"v_mfma_f32_16x16x32_bf16 v[96:99], a[112:115], v[168:171], v[96:99]\n"
"v_mfma_f32_16x16x32_bf16 v[220:223], a[116:119], v[168:171], v[220:223]\n"
"v_mfma_f32_16x16x32_bf16 v[160:163], a[120:123], v[196:199], v[160:163]\n"
"v_mfma_f32_16x16x32_bf16 v[200:203], a[124:127], v[196:199], v[200:203]\n"
"v_mfma_f32_16x16x32_bf16 v[204:207], v[24:27], v[196:199], v[204:207]\n"
"v_mfma_f32_16x16x32_bf16 v[212:215], v[28:31], v[196:199], v[212:215]\n"
"v_mfma_f32_16x16x32_bf16 v[96:99], v[32:35], v[196:199], v[96:99]\n"
"v_mfma_f32_16x16x32_bf16 v[220:223], v[36:39], v[196:199], v[220:223]\n"
"v_mfma_f32_16x16x32_bf16 v[160:163], v[40:43], v[216:219], v[160:163]\n"
"v_mfma_f32_16x16x32_bf16 v[200:203], v[44:47], v[216:219], v[200:203]\n"
"v_mfma_f32_16x16x32_bf16 v[204:207], v[48:51], v[216:219], v[204:207]\n"
"v_mfma_f32_16x16x32_bf16 v[212:215], v[52:55], v[216:219], v[212:215]\n"
"v_mfma_f32_16x16x32_bf16 v[96:99], v[56:59], v[216:219], v[96:99]\n"
"v_mfma_f32_16x16x32_bf16 v[220:223], v[60:63], v[216:219], v[220:223]\n"
"v_mfma_f32_16x16x32_bf16 v[160:163], v[64:67], v[224:227], v[160:163]\n"
"v_mfma_f32_16x16x32_bf16 v[200:203], v[68:71], v[224:227], v[200:203]\n"
"v_mfma_f32_16x16x32_bf16 v[204:207], v[72:75], v[224:227], v[204:207]\n"
"v_mfma_f32_16x16x32_bf16 v[212:215], v[76:79], v[224:227], v[212:215]\n"
"v_mfma_f32_16x16x32_bf16 v[96:99], v[80:83], v[224:227], v[96:99]\n"
"v_mfma_f32_16x16x32_bf16 v[220:223], v[84:87], v[224:227], v[220:223]\n"
"s_nop 7\n"
"s_branch .Lmk_p58r\n"
".Lmk_q60s:\n"
";\n"
"s_mov_b32 s1, 0x10000\n"
".Lmk_q60l:\n"
"s_add_u32 s96, s55, 0x448000\n"
"buffer_load_dwordx4 v[136:139], v1, s[4:7], s96 offen sc1\n"
"buffer_load_dwordx4 v[144:147], v1, s[4:7], s96 offen offset:1024 sc1\n"
"buffer_load_dwordx4 v[184:187], v1, s[4:7], s96 offen offset:2048 sc1\n"
"buffer_load_dwordx4 v[192:195], v1, s[4:7], s96 offen offset:3072 sc1\n"
"s_waitcnt vmcnt(0)\n"
"v_max_u32_e32 v255, v136, v137\n"
"v_max3_u32 v255, v255, v138, v139\n"
"v_max3_u32 v255, v255, v144, v145\n"
"v_max3_u32 v255, v255, v146, v147\n"
"v_max3_u32 v255, v255, v184, v185\n"
"v_max3_u32 v255, v255, v186, v187\n"
"v_max3_u32 v255, v255, v192, v193\n"
"v_max3_u32 v255, v255, v194, v195\n"
"v_cmp_eq_u32_e32 vcc, -1, v255\n"
"s_cbranch_vccz .Lmk_q60k\n"
"s_sub_u32 s1, s1, 1\n"
"s_cbranch_scc0 .Lmk_q60l\n"
".Lmk_q60k:\n"
"s_branch .Lmk_q60r\n"
".Lmk_f47s:\n"
";\n"
"s_mov_b32 s1, 0x10000\n"
".Lmk_f47l:\n"
"buffer_load_dwordx2 v[254:255], off, s[4:7], s96 sc1\n"
"s_waitcnt vmcnt(0)\n"
"v_cmp_ne_u64_e32 vcc, v[254:255], v[18:19]\n"
"s_cbranch_vccz .Lmk_f47r\n"
"s_sub_u32 s1, s1, 1\n"
"s_cbranch_scc0 .Lmk_f47l\n"
"s_branch .Lmk_f47r\n"
".Lmk_f48s:\n"
";\n"
"s_mov_b32 s1, 0x10000\n"
".Lmk_f48l:\n"
"buffer_load_dwordx2 v[254:255], v12, s[4:7], s98 offen sc1\n"
"s_waitcnt vmcnt(0)\n"
"v_cmp_ne_u64_e32 vcc, v[254:255], v[18:19]\n"
"s_cbranch_vccz .Lmk_f48r\n"
"s_sub_u32 s1, s1, 1\n"
"s_cbranch_scc0 .Lmk_f48l\n"
"s_branch .Lmk_f48r\n"
".Lmk_p64s:\n"
";\n"
"s_mov_b32 s1, 0x10000\n"
".Lmk_p64l:\n"
"s_add_u32 s96, s56, 0x440000\n"
"buffer_load_dwordx4 v[104:107], v0, s[4:7], s96 offen sc1\n"
"buffer_load_dwordx4 v[24:27], v0, s[4:7], s96 offen offset:1024 sc1\n"
"buffer_load_dwordx4 v[28:31], v0, s[4:7], s96 offen offset:2048 sc1\n"
"buffer_load_dwordx4 v[32:35], v0, s[4:7], s96 offen offset:3072 sc1\n"
"buffer_load_dwordx4 v[36:39], v1, s[4:7], s96 offen sc1\n"
"buffer_load_dwordx4 v[40:43], v1, s[4:7], s96 offen offset:1024 sc1\n"
"buffer_load_dwordx4 v[60:63], v1, s[4:7], s96 offen offset:2048 sc1\n"
"buffer_load_dwordx4 v[88:91], v1, s[4:7], s96 offen offset:3072 sc1\n"
"s_add_u32 s96, s57, 0x440000\n"
"buffer_load_dwordx4 v[100:103], v0, s[4:7], s96 offen sc1\n"
"buffer_load_dwordx4 v[72:75], v0, s[4:7], s96 offen offset:1024 sc1\n"
"buffer_load_dwordx4 v[64:67], v0, s[4:7], s96 offen offset:2048 sc1\n"
"buffer_load_dwordx4 v[112:115], v0, s[4:7], s96 offen offset:3072 sc1\n"
"buffer_load_dwordx4 v[96:99], v1, s[4:7], s96 offen sc1\n"
"buffer_load_dwordx4 v[84:87], v1, s[4:7], s96 offen offset:1024 sc1\n"
"buffer_load_dwordx4 v[76:79], v1, s[4:7], s96 offen offset:2048 sc1\n"
"buffer_load_dwordx4 v[128:131], v1, s[4:7], s96 offen offset:3072 sc1\n"
"s_waitcnt vmcnt(0)\n"
"v_max_u32_e32 v255, v104, v105\n"
"v_max3_u32 v255, v255, v106, v107\n"
"v_max3_u32 v255, v255, v24, v25\n"
"v_max3_u32 v255, v255, v26, v27\n"
"v_max3_u32 v255, v255, v28, v29\n"
"v_max3_u32 v255, v255, v30, v31\n"
"v_max3_u32 v255, v255, v32, v33\n"
"v_max3_u32 v255, v255, v34, v35\n"
"v_max3_u32 v255, v255, v36, v37\n"
"v_max3_u32 v255, v255, v38, v39\n"
"v_max3_u32 v255, v255, v40, v41\n"
"v_max3_u32 v255, v255, v42, v43\n"
"v_max3_u32 v255, v255, v60, v61\n"
"v_max3_u32 v255, v255, v62, v63\n"
"v_max3_u32 v255, v255, v88, v89\n"
"v_max3_u32 v255, v255, v90, v91\n"
"v_cmp_eq_u32_e32 vcc, -1, v255\n"
"s_cbranch_vccz .Lmk_p64k\n"
"s_sub_u32 s1, s1, 1\n"
"s_cbranch_scc0 .Lmk_p64l\n"
".Lmk_p64k:\n"
"v_mfma_f32_16x16x32_bf16 v[68:71], a[128:131], v[104:107], 0\n"
"v_mfma_f32_16x16x32_bf16 v[80:83], a[132:135], v[104:107], 0\n"
"v_mfma_f32_16x16x32_bf16 v[108:111], a[136:139], v[104:107], 0\n"
"v_mfma_f32_16x16x32_bf16 v[116:119], a[140:143], v[104:107], 0\n"
"v_mfma_f32_16x16x32_bf16 v[68:71], a[144:147], v[24:27], v[68:71]\n"
"v_mfma_f32_16x16x32_bf16 v[80:83], a[148:151], v[24:27], v[80:83]\n"
"v_mfma_f32_16x16x32_bf16 v[108:111], a[152:155], v[24:27], v[108:111]\n"
"v_mfma_f32_16x16x32_bf16 v[116:119], a[156:159], v[24:27], v[116:119]\n"
"v_mfma_f32_16x16x32_bf16 v[68:71], a[160:163], v[28:31], v[68:71]\n"
"v_mfma_f32_16x16x32_bf16 v[80:83], a[164:167], v[28:31], v[80:83]\n"
"v_mfma_f32_16x16x32_bf16 v[108:111], a[168:171], v[28:31], v[108:111]\n"
"v_mfma_f32_16x16x32_bf16 v[116:119], a[172:175], v[28:31], v[116:119]\n"
"v_mfma_f32_16x16x32_bf16 v[68:71], a[176:179], v[32:35], v[68:71]\n"
"v_mfma_f32_16x16x32_bf16 v[80:83], a[180:183], v[32:35], v[80:83]\n"
"v_mfma_f32_16x16x32_bf16 v[108:111], a[184:187], v[32:35], v[108:111]\n"
"v_mfma_f32_16x16x32_bf16 v[116:119], a[188:191], v[32:35], v[116:119]\n"
"v_mfma_f32_16x16x32_bf16 v[68:71], a[192:195], v[36:39], v[68:71]\n"
"v_mfma_f32_16x16x32_bf16 v[80:83], a[196:199], v[36:39], v[80:83]\n"
"v_mfma_f32_16x16x32_bf16 v[108:111], a[200:203], v[36:39], v[108:111]\n"
"v_mfma_f32_16x16x32_bf16 v[116:119], a[204:207], v[36:39], v[116:119]\n"
"v_mfma_f32_16x16x32_bf16 v[68:71], a[208:211], v[40:43], v[68:71]\n"
"v_mfma_f32_16x16x32_bf16 v[80:83], a[212:215], v[40:43], v[80:83]\n"
"v_mfma_f32_16x16x32_bf16 v[108:111], a[216:219], v[40:43], v[108:111]\n"
"v_mfma_f32_16x16x32_bf16 v[116:119], a[220:223], v[40:43], v[116:119]\n"
"v_mfma_f32_16x16x32_bf16 v[68:71], a[224:227], v[60:63], v[68:71]\n"
"v_mfma_f32_16x16x32_bf16 v[80:83], a[228:231], v[60:63], v[80:83]\n"
"v_mfma_f32_16x16x32_bf16 v[108:111], a[232:235], v[60:63], v[108:111]\n"
"v_mfma_f32_16x16x32_bf16 v[116:119], a[236:239], v[60:63], v[116:119]\n"
"v_mfma_f32_16x16x32_bf16 v[68:71], a[240:243], v[88:91], v[68:71]\n"
"v_mfma_f32_16x16x32_bf16 v[80:83], a[244:247], v[88:91], v[80:83]\n"
"v_mfma_f32_16x16x32_bf16 v[108:111], a[248:251], v[88:91], v[108:111]\n"
"v_mfma_f32_16x16x32_bf16 v[116:119], a[252:255], v[88:91], v[116:119]\n"
"s_add_u32 s96, s58, 0x440000\n"
"v_mov_b32_e32 v255, 0x80000000\n"
"buffer_load_dwordx4 v[104:107], v0, s[4:7], s96 offen sc1\n"
"buffer_load_dwordx4 v[24:27], v0, s[4:7], s96 offen offset:1024 sc1\n"
"buffer_load_dwordx4 v[28:31], v0, s[4:7], s96 offen offset:2048 sc1\n"
"buffer_load_dwordx4 v[32:35], v0, s[4:7], s96 offen offset:3072 sc1\n"
"buffer_load_dwordx4 v[36:39], v1, s[4:7], s96 offen sc1\n"
"buffer_load_dwordx4 v[40:43], v1, s[4:7], s96 offen offset:1024 sc1\n"
"buffer_load_dwordx4 v[60:63], v1, s[4:7], s96 offen offset:2048 sc1\n"
"buffer_load_dwordx4 v[88:91], v1, s[4:7], s96 offen offset:3072 sc1\n"
"s_nop 7\n"
"s_branch .Lmk_p64r\n"
".Lmk_p65s:\n"
";\n"
"s_mov_b32 s1, 0x10000\n"
".Lmk_p65l:\n"
"s_add_u32 s96, s57, 0x440000\n"
"buffer_load_dwordx4 v[144:147], v0, s[4:7], s96 offen sc1\n"
"buffer_load_dwordx4 v[148:151], v0, s[4:7], s96 offen offset:1024 sc1\n"
"buffer_load_dwordx4 v[152:155], v0, s[4:7], s96 offen offset:2048 sc1\n"
"buffer_load_dwordx4 v[156:159], v0, s[4:7], s96 offen offset:3072 sc1\n"
"buffer_load_dwordx4 v[160:163], v1, s[4:7], s96 offen sc1\n"
"buffer_load_dwordx4 v[164:167], v1, s[4:7], s96 offen offset:1024 sc1\n"
"buffer_load_dwordx4 v[168:171], v1, s[4:7], s96 offen offset:2048 sc1\n"
"buffer_load_dwordx4 v[172:175], v1, s[4:7], s96 offen offset:3072 sc1\n"
"s_add_u32 s96, s58, 0x440000\n"
"buffer_load_dwordx4 v[104:107], v0, s[4:7], s96 offen sc1\n"
"buffer_load_dwordx4 v[24:27], v0, s[4:7], s96 offen offset:1024 sc1\n"
"buffer_load_dwordx4 v[28:31], v0, s[4:7], s96 offen offset:2048 sc1\n"
"buffer_load_dwordx4 v[32:35], v0, s[4:7], s96 offen offset:3072 sc1\n"
"buffer_load_dwordx4 v[36:39], v1, s[4:7], s96 offen sc1\n"
"buffer_load_dwordx4 v[40:43], v1, s[4:7], s96 offen offset:1024 sc1\n"
"buffer_load_dwordx4 v[60:63], v1, s[4:7], s96 offen offset:2048 sc1\n"
"buffer_load_dwordx4 v[88:91], v1, s[4:7], s96 offen offset:3072 sc1\n"
"s_add_u32 s96, s59, 0x440000\n"
"buffer_load_dwordx4 v[132:135], v0, s[4:7], s96 offen sc1\n"
"buffer_load_dwordx4 v[68:71], v0, s[4:7], s96 offen offset:1024 sc1\n"
"buffer_load_dwordx4 v[64:67], v0, s[4:7], s96 offen offset:2048 sc1\n"
"buffer_load_dwordx4 v[100:103], v0, s[4:7], s96 offen offset:3072 sc1\n"
"buffer_load_dwordx4 v[96:99], v1, s[4:7], s96 offen sc1\n"
"buffer_load_dwordx4 v[140:143], v1, s[4:7], s96 offen offset:1024 sc1\n"
"buffer_load_dwordx4 v[72:75], v1, s[4:7], s96 offen offset:2048 sc1\n"
"s_waitcnt vmcnt(0)\n"
"v_max_u32_e32 v243, v144, v145\n"
"v_max3_u32 v243, v243, v146, v147\n"
"v_max3_u32 v243, v243, v148, v149\n"
"v_max3_u32 v243, v243, v150, v151\n"
"v_max3_u32 v243, v243, v152, v153\n"
"v_max3_u32 v243, v243, v154, v155\n"
"v_max3_u32 v243, v243, v156, v157\n"
"v_max3_u32 v243, v243, v158, v159\n"
"v_max3_u32 v243, v243, v160, v161\n"
"v_max3_u32 v243, v243, v162, v163\n"
"v_max3_u32 v243, v243, v164, v165\n"
"v_max3_u32 v243, v243, v166, v167\n"
"v_max3_u32 v243, v243, v168, v169\n"
"v_max3_u32 v243, v243, v170, v171\n"
"v_max3_u32 v243, v243, v172, v173\n"
"v_max3_u32 v243, v243, v174, v175\n"
"v_cmp_eq_u32_e32 vcc, -1, v243\n"
"s_cbranch_vccz .Lmk_p65k\n"
"s_sub_u32 s1, s1, 1\n"
"s_cbranch_scc0 .Lmk_p65l\n"
".Lmk_p65k:\n"
"v_mfma_f32_16x16x32_bf16 v[92:95], a[128:131], v[144:147], 0\n"
"v_mfma_f32_16x16x32_bf16 v[120:123], a[132:135], v[144:147], 0\n"
"v_mfma_f32_16x16x32_bf16 v[124:127], a[136:139], v[144:147], 0\n"
"v_mfma_f32_16x16x32_bf16 v[136:139], a[140:143], v[144:147], 0\n"
"v_mfma_f32_16x16x32_bf16 v[92:95], a[144:147], v[148:151], v[92:95]\n"
"v_mfma_f32_16x16x32_bf16 v[120:123], a[148:151], v[148:151], v[120:123]\n"
"v_mfma_f32_16x16x32_bf16 v[124:127], a[152:155], v[148:151], v[124:127]\n"
"v_mfma_f32_16x16x32_bf16 v[136:139], a[156:159], v[148:151], v[136:139]\n"
"v_mfma_f32_16x16x32_bf16 v[92:95], a[160:163], v[152:155], v[92:95]\n"
"v_mfma_f32_16x16x32_bf16 v[120:123], a[164:167], v[152:155], v[120:123]\n"
"v_mfma_f32_16x16x32_bf16 v[124:127], a[168:171], v[152:155], v[124:127]\n"
"v_mfma_f32_16x16x32_bf16 v[136:139], a[172:175], v[152:155], v[136:139]\n"
"v_mfma_f32_16x16x32_bf16 v[92:95], a[176:179], v[156:159], v[92:95]\n"
"v_mfma_f32_16x16x32_bf16 v[120:123], a[180:183], v[156:159], v[120:123]\n"
"v_mfma_f32_16x16x32_bf16 v[124:127], a[184:187], v[156:159], v[124:127]\n"
"v_mfma_f32_16x16x32_bf16 v[136:139], a[188:191], v[156:159], v[136:139]\n"
"v_mfma_f32_16x16x32_bf16 v[92:95], a[192:195], v[160:163], v[92:95]\n"
"v_mfma_f32_16x16x32_bf16 v[120:123], a[196:199], v[160:163], v[120:123]\n"
"v_mfma_f32_16x16x32_bf16 v[124:127], a[200:203], v[160:163], v[124:127]\n"
"v_mfma_f32_16x16x32_bf16 v[136:139], a[204:207], v[160:163], v[136:139]\n"
"v_mfma_f32_16x16x32_bf16 v[92:95], a[208:211], v[164:167], v[92:95]\n"
"v_mfma_f32_16x16x32_bf16 v[120:123], a[212:215], v[164:167], v[120:123]\n"
"v_mfma_f32_16x16x32_bf16 v[124:127], a[216:219], v[164:167], v[124:127]\n"
"v_mfma_f32_16x16x32_bf16 v[136:139], a[220:223], v[164:167], v[136:139]\n"
"v_mfma_f32_16x16x32_bf16 v[92:95], a[224:227], v[168:171], v[92:95]\n"
"v_mfma_f32_16x16x32_bf16 v[120:123], a[228:231], v[168:171], v[120:123]\n"
"v_mfma_f32_16x16x32_bf16 v[124:127], a[232:235], v[168:171], v[124:127]\n"
"v_mfma_f32_16x16x32_bf16 v[136:139], a[236:239], v[168:171], v[136:139]\n"
"v_mfma_f32_16x16x32_bf16 v[92:95], a[240:243], v[172:175], v[92:95]\n"
"v_mfma_f32_16x16x32_bf16 v[120:123], a[244:247], v[172:175], v[120:123]\n"
"v_mfma_f32_16x16x32_bf16 v[124:127], a[248:251], v[172:175], v[124:127]\n"
"v_mfma_f32_16x16x32_bf16 v[136:139], a[252:255], v[172:175], v[136:139]\n"
"s_nop 7\n"
"s_branch .Lmk_p65r\n"
".Lmk_q67s:\n"
";\n"
"s_mov_b32 s1, 0x10000\n"
".Lmk_q67l:\n"
"s_add_u32 s96, s59, 0x440000\n"
"buffer_load_dwordx4 v[132:135], v0, s[4:7], s96 offen sc1\n"
"buffer_load_dwordx4 v[68:71], v0, s[4:7], s96 offen offset:1024 sc1\n"
"buffer_load_dwordx4 v[64:67], v0, s[4:7], s96 offen offset:2048 sc1\n"
"buffer_load_dwordx4 v[100:103], v0, s[4:7], s96 offen offset:3072 sc1\n"
"buffer_load_dwordx4 v[96:99], v1, s[4:7], s96 offen sc1\n"
"buffer_load_dwordx4 v[140:143], v1, s[4:7], s96 offen offset:1024 sc1\n"
"buffer_load_dwordx4 v[72:75], v1, s[4:7], s96 offen offset:2048 sc1\n"
"buffer_load_dwordx4 v[148:151], v1, s[4:7], s96 offen offset:3072 sc1\n"
"s_add_u32 s96, s56, 0x400000\n"
"buffer_load_dwordx4 v[24:27], v0, s[4:7], s96 offen sc1\n"
"buffer_load_dwordx4 v[28:31], v0, s[4:7], s96 offen offset:1024 sc1\n"
"buffer_load_dwordx4 v[32:35], v0, s[4:7], s96 offen offset:2048 sc1\n"
"buffer_load_dwordx4 v[36:39], v0, s[4:7], s96 offen offset:3072 sc1\n"
"s_waitcnt vmcnt(0)\n"
"v_max_u32_e32 v255, v132, v133\n"
"v_max3_u32 v255, v255, v134, v135\n"
"v_max3_u32 v255, v255, v68, v69\n"
"v_max3_u32 v255, v255, v70, v71\n"
"v_max3_u32 v255, v255, v64, v65\n"
"v_max3_u32 v255, v255, v66, v67\n"
"v_max3_u32 v255, v255, v100, v101\n"
"v_max3_u32 v255, v255, v102, v103\n"
"v_cmp_eq_u32_e32 vcc, -1, v255\n"
"s_cbranch_vccz .Lmk_q67k\n"
"s_sub_u32 s1, s1, 1\n"
"s_cbranch_scc0 .Lmk_q67l\n"
".Lmk_q67k:\n"
"s_branch .Lmk_q67r\n"
".Lmk_p66s:\n"
";\n"
"s_mov_b32 s1, 0x10000\n"
".Lmk_p66l:\n"
"s_add_u32 s96, s58, 0x440000\n"
"buffer_load_dwordx4 v[40:43], v0, s[4:7], s96 offen sc1\n"
"buffer_load_dwordx4 v[60:63], v0, s[4:7], s96 offen offset:1024 sc1\n"
"buffer_load_dwordx4 v[80:83], v0, s[4:7], s96 offen offset:2048 sc1\n"
"buffer_load_dwordx4 v[88:91], v0, s[4:7], s96 offen offset:3072 sc1\n"
"buffer_load_dwordx4 v[92:95], v1, s[4:7], s96 offen sc1\n"
"buffer_load_dwordx4 v[104:107], v1, s[4:7], s96 offen offset:1024 sc1\n"
"buffer_load_dwordx4 v[120:123], v1, s[4:7], s96 offen offset:2048 sc1\n"
"buffer_load_dwordx4 v[124:127], v1, s[4:7], s96 offen offset:3072 sc1\n"
"s_add_u32 s96, s59, 0x440000\n"
"buffer_load_dwordx4 v[132:135], v0, s[4:7], s96 offen sc1\n"
"buffer_load_dwordx4 v[68:71], v0, s[4:7], s96 offen offset:1024 sc1\n"
"buffer_load_dwordx4 v[64:67], v0, s[4:7], s96 offen offset:2048 sc1\n"
"buffer_load_dwordx4 v[100:103], v0, s[4:7], s96 offen offset:3072 sc1\n"
"buffer_load_dwordx4 v[96:99], v1, s[4:7], s96 offen sc1\n"
"buffer_load_dwordx4 v[140:143], v1, s[4:7], s96 offen offset:1024 sc1\n"
"buffer_load_dwordx4 v[72:75], v1, s[4:7], s96 offen offset:2048 sc1\n"
"buffer_load_dwordx4 v[148:151], v1, s[4:7], s96 offen offset:3072 sc1\n"
"s_add_u32 s96, s56, 0x400000\n"
"buffer_load_dwordx4 v[24:27], v0, s[4:7], s96 offen sc1\n"
"buffer_load_dwordx4 v[28:31], v0, s[4:7], s96 offen offset:1024 sc1\n"
"buffer_load_dwordx4 v[32:35], v0, s[4:7], s96 offen offset:2048 sc1\n"
"buffer_load_dwordx4 v[36:39], v0, s[4:7], s96 offen offset:3072 sc1\n"
"s_waitcnt vmcnt(0)\n"
"v_max_u32_e32 v255, v40, v41\n"
"v_max3_u32 v255, v255, v42, v43\n"
"v_max3_u32 v255, v255, v60, v61\n"
"v_max3_u32 v255, v255, v62, v63\n"
"v_max3_u32 v255, v255, v80, v81\n"
"v_max3_u32 v255, v255, v82, v83\n"
"v_max3_u32 v255, v255, v88, v89\n"
"v_max3_u32 v255, v255, v90, v91\n"
"v_max3_u32 v255, v255, v92, v93\n"
"v_max3_u32 v255, v255, v94, v95\n"
"v_max3_u32 v255, v255, v104, v105\n"
"v_max3_u32 v255, v255, v106, v107\n"
"v_max3_u32 v255, v255, v120, v121\n"
"v_max3_u32 v255, v255, v122, v123\n"
"v_max3_u32 v255, v255, v124, v125\n"
"v_max3_u32 v255, v255, v126, v127\n"
"v_cmp_eq_u32_e32 vcc, -1, v255\n"
"s_cbranch_vccz .Lmk_p66k\n"
"s_sub_u32 s1, s1, 1\n"
"s_cbranch_scc0 .Lmk_p66l\n"
".Lmk_p66k:\n"
"v_mfma_f32_16x16x32_bf16 v[76:79], a[128:131], v[40:43], 0\n"
"v_mfma_f32_16x16x32_bf16 v[128:131], a[132:135], v[40:43], 0\n"
"v_mfma_f32_16x16x32_bf16 v[144:147], a[136:139], v[40:43], 0\n"
"v_mfma_f32_16x16x32_bf16 v[152:155], a[140:143], v[40:43], 0\n"
"v_mfma_f32_16x16x32_bf16 v[76:79], a[144:147], v[60:63], v[76:79]\n"
"v_mfma_f32_16x16x32_bf16 v[128:131], a[148:151], v[60:63], v[128:131]\n"
"v_mfma_f32_16x16x32_bf16 v[144:147], a[152:155], v[60:63], v[144:147]\n"
"v_mfma_f32_16x16x32_bf16 v[152:155], a[156:159], v[60:63], v[152:155]\n"
"v_mfma_f32_16x16x32_bf16 v[76:79], a[160:163], v[80:83], v[76:79]\n"
"v_mfma_f32_16x16x32_bf16 v[128:131], a[164:167], v[80:83], v[128:131]\n"
"v_mfma_f32_16x16x32_bf16 v[144:147], a[168:171], v[80:83], v[144:147]\n"
"v_mfma_f32_16x16x32_bf16 v[152:155], a[172:175], v[80:83], v[152:155]\n"
"v_mfma_f32_16x16x32_bf16 v[76:79], a[176:179], v[88:91], v[76:79]\n"
"v_mfma_f32_16x16x32_bf16 v[128:131], a[180:183], v[88:91], v[128:131]\n"
"v_mfma_f32_16x16x32_bf16 v[144:147], a[184:187], v[88:91], v[144:147]\n"
"v_mfma_f32_16x16x32_bf16 v[152:155], a[188:191], v[88:91], v[152:155]\n"
"v_mfma_f32_16x16x32_bf16 v[76:79], a[192:195], v[92:95], v[76:79]\n"
"v_mfma_f32_16x16x32_bf16 v[128:131], a[196:199], v[92:95], v[128:131]\n"
"v_mfma_f32_16x16x32_bf16 v[144:147], a[200:203], v[92:95], v[144:147]\n"
"v_mfma_f32_16x16x32_bf16 v[152:155], a[204:207], v[92:95], v[152:155]\n"
"v_mfma_f32_16x16x32_bf16 v[76:79], a[208:211], v[104:107], v[76:79]\n"
"v_mfma_f32_16x16x32_bf16 v[128:131], a[212:215], v[104:107], v[128:131]\n"
"v_mfma_f32_16x16x32_bf16 v[144:147], a[216:219], v[104:107], v[144:147]\n"
"v_mfma_f32_16x16x32_bf16 v[152:155], a[220:223], v[104:107], v[152:155]\n"
"v_mfma_f32_16x16x32_bf16 v[76:79], a[224:227], v[120:123], v[76:79]\n"
"v_mfma_f32_16x16x32_bf16 v[128:131], a[228:231], v[120:123], v[128:131]\n"
"v_mfma_f32_16x16x32_bf16 v[144:147], a[232:235], v[120:123], v[144:147]\n"
"v_mfma_f32_16x16x32_bf16 v[152:155], a[236:239], v[120:123], v[152:155]\n"
"v_mfma_f32_16x16x32_bf16 v[76:79], a[240:243], v[124:127], v[76:79]\n"
"v_mfma_f32_16x16x32_bf16 v[128:131], a[244:247], v[124:127], v[128:131]\n"
"v_mfma_f32_16x16x32_bf16 v[144:147], a[248:251], v[124:127], v[144:147]\n"
"v_mfma_f32_16x16x32_bf16 v[152:155], a[252:255], v[124:127], v[152:155]\n"
"s_nop 7\n"
"s_branch .Lmk_p66r\n"
".Lmk_q68s:\n"
";\n"
"s_mov_b32 s1, 0x10000\n"
".Lmk_q68l:\n"
"s_add_u32 s96, s59, 0x440000\n"
"buffer_load_dwordx4 v[96:99], v1, s[4:7], s96 offen sc1\n"
"buffer_load_dwordx4 v[140:143], v1, s[4:7], s96 offen offset:1024 sc1\n"
"buffer_load_dwordx4 v[72:75], v1, s[4:7], s96 offen offset:2048 sc1\n"
"buffer_load_dwordx4 v[148:151], v1, s[4:7], s96 offen offset:3072 sc1\n"
"s_add_u32 s96, s56, 0x400000\n"
"buffer_load_dwordx4 v[24:27], v0, s[4:7], s96 offen sc1\n"
"buffer_load_dwordx4 v[28:31], v0, s[4:7], s96 offen offset:1024 sc1\n"
"buffer_load_dwordx4 v[32:35], v0, s[4:7], s96 offen offset:2048 sc1\n"
"buffer_load_dwordx4 v[36:39], v0, s[4:7], s96 offen offset:3072 sc1\n"
"buffer_load_dwordx4 v[40:43], v1, s[4:7], s96 offen sc1\n"
"buffer_load_dwordx4 v[60:63], v1, s[4:7], s96 offen offset:1024 sc1\n"
"s_waitcnt vmcnt(0)\n"
"v_max_u32_e32 v255, v96, v97\n"
"v_max3_u32 v255, v255, v98, v99\n"
"v_max3_u32 v255, v255, v140, v141\n"
"v_max3_u32 v255, v255, v142, v143\n"
"v_max3_u32 v255, v255, v72, v73\n"
"v_max3_u32 v255, v255, v74, v75\n"
"v_max3_u32 v255, v255, v148, v149\n"
"v_max3_u32 v255, v255, v150, v151\n"
"v_cmp_eq_u32_e32 vcc, -1, v255\n"
"s_cbranch_vccz .Lmk_q68k\n"
"s_sub_u32 s1, s1, 1\n"
"s_cbranch_scc0 .Lmk_q68l\n"
".Lmk_q68k:\n"
"s_branch .Lmk_q68r\n"
".Lmk_p69s:\n"
";\n"
"s_mov_b32 s1, 0x10000\n"
".Lmk_p69l:\n"
"s_add_u32 s96, s56, 0x400000\n"
"buffer_load_dwordx4 v[144:147], v0, s[4:7], s96 offen sc1\n"
"buffer_load_dwordx4 v[24:27], v0, s[4:7], s96 offen offset:1024 sc1\n"
"buffer_load_dwordx4 v[28:31], v0, s[4:7], s96 offen offset:2048 sc1\n"
"buffer_load_dwordx4 v[32:35], v0, s[4:7], s96 offen offset:3072 sc1\n"
"buffer_load_dwordx4 v[36:39], v1, s[4:7], s96 offen sc1\n"
"buffer_load_dwordx4 v[40:43], v1, s[4:7], s96 offen offset:1024 sc1\n"
"buffer_load_dwordx4 v[60:63], v1, s[4:7], s96 offen offset:2048 sc1\n"
"buffer_load_dwordx4 v[64:67], v1, s[4:7], s96 offen offset:3072 sc1\n"
"s_add_u32 s96, s57, 0x400000\n"
"buffer_load_dwordx4 v[88:91], v0, s[4:7], s96 offen sc1\n"
"buffer_load_dwordx4 v[72:75], v0, s[4:7], s96 offen offset:1024 sc1\n"
"buffer_load_dwordx4 v[128:131], v0, s[4:7], s96 offen offset:2048 sc1\n"
"buffer_load_dwordx4 v[80:83], v0, s[4:7], s96 offen offset:3072 sc1\n"
"buffer_load_dwordx4 v[96:99], v1, s[4:7], s96 offen sc1\n"
"buffer_load_dwordx4 v[104:107], v1, s[4:7], s96 offen offset:1024 sc1\n"
"buffer_load_dwordx4 v[124:127], v1, s[4:7], s96 offen offset:2048 sc1\n"
"buffer_load_dwordx4 v[132:135], v1, s[4:7], s96 offen offset:3072 sc1\n"
"s_waitcnt vmcnt(0)\n"
"v_max_u32_e32 v255, v144, v145\n"
"v_max3_u32 v255, v255, v146, v147\n"
"v_max3_u32 v255, v255, v24, v25\n"
"v_max3_u32 v255, v255, v26, v27\n"
"v_max3_u32 v255, v255, v28, v29\n"
"v_max3_u32 v255, v255, v30, v31\n"
"v_max3_u32 v255, v255, v32, v33\n"
"v_max3_u32 v255, v255, v34, v35\n"
"v_max3_u32 v255, v255, v36, v37\n"
"v_max3_u32 v255, v255, v38, v39\n"
"v_max3_u32 v255, v255, v40, v41\n"
"v_max3_u32 v255, v255, v42, v43\n"
"v_max3_u32 v255, v255, v60, v61\n"
"v_max3_u32 v255, v255, v62, v63\n"
"v_max3_u32 v255, v255, v64, v65\n"
"v_max3_u32 v255, v255, v66, v67\n"
"v_cmp_eq_u32_e32 vcc, -1, v255\n"
"s_cbranch_vccz .Lmk_p69k\n"
"s_sub_u32 s1, s1, 1\n"
"s_cbranch_scc0 .Lmk_p69l\n"
".Lmk_p69k:\n"
"v_mfma_f32_16x16x32_bf16 v[136:139], a[128:131], v[144:147], 0\n"
"v_mfma_f32_16x16x32_bf16 v[140:143], a[132:135], v[144:147], 0\n"
"v_mfma_f32_16x16x32_bf16 v[148:151], a[136:139], v[144:147], 0\n"
"v_mfma_f32_16x16x32_bf16 v[152:155], a[140:143], v[144:147], 0\n"
"v_mfma_f32_16x16x32_bf16 v[136:139], a[144:147], v[24:27], v[136:139]\n"
"v_mfma_f32_16x16x32_bf16 v[140:143], a[148:151], v[24:27], v[140:143]\n"
"v_mfma_f32_16x16x32_bf16 v[148:151], a[152:155], v[24:27], v[148:151]\n"
"v_mfma_f32_16x16x32_bf16 v[152:155], a[156:159], v[24:27], v[152:155]\n"
"v_mfma_f32_16x16x32_bf16 v[136:139], a[160:163], v[28:31], v[136:139]\n"
"v_mfma_f32_16x16x32_bf16 v[140:143], a[164:167], v[28:31], v[140:143]\n"
"v_mfma_f32_16x16x32_bf16 v[148:151], a[168:171], v[28:31], v[148:151]\n"
"v_mfma_f32_16x16x32_bf16 v[152:155], a[172:175], v[28:31], v[152:155]\n"
"v_mfma_f32_16x16x32_bf16 v[136:139], a[176:179], v[32:35], v[136:139]\n"
"v_mfma_f32_16x16x32_bf16 v[140:143], a[180:183], v[32:35], v[140:143]\n"
"v_mfma_f32_16x16x32_bf16 v[148:151], a[184:187], v[32:35], v[148:151]\n"
"v_mfma_f32_16x16x32_bf16 v[152:155], a[188:191], v[32:35], v[152:155]\n"
"v_mfma_f32_16x16x32_bf16 v[136:139], a[192:195], v[36:39], v[136:139]\n"
"v_mfma_f32_16x16x32_bf16 v[140:143], a[196:199], v[36:39], v[140:143]\n"
"v_mfma_f32_16x16x32_bf16 v[148:151], a[200:203], v[36:39], v[148:151]\n"
"v_mfma_f32_16x16x32_bf16 v[152:155], a[204:207], v[36:39], v[152:155]\n"
"v_mfma_f32_16x16x32_bf16 v[136:139], a[208:211], v[40:43], v[136:139]\n"
"v_mfma_f32_16x16x32_bf16 v[140:143], a[212:215], v[40:43], v[140:143]\n"
"v_mfma_f32_16x16x32_bf16 v[148:151], a[216:219], v[40:43], v[148:151]\n"
"v_mfma_f32_16x16x32_bf16 v[152:155], a[220:223], v[40:43], v[152:155]\n"
"v_mfma_f32_16x16x32_bf16 v[136:139], a[224:227], v[60:63], v[136:139]\n"
"v_mfma_f32_16x16x32_bf16 v[140:143], a[228:231], v[60:63], v[140:143]\n"
"v_mfma_f32_16x16x32_bf16 v[148:151], a[232:235], v[60:63], v[148:151]\n"
"v_mfma_f32_16x16x32_bf16 v[152:155], a[236:239], v[60:63], v[152:155]\n"
"v_mfma_f32_16x16x32_bf16 v[136:139], a[240:243], v[64:67], v[136:139]\n"
"v_mfma_f32_16x16x32_bf16 v[140:143], a[244:247], v[64:67], v[140:143]\n"
"v_mfma_f32_16x16x32_bf16 v[148:151], a[248:251], v[64:67], v[148:151]\n"
"v_mfma_f32_16x16x32_bf16 v[152:155], a[252:255], v[64:67], v[152:155]\n"
"s_add_u32 s96, s58, 0x400000\n"
"v_mov_b32_e32 v255, 0x80000000\n"
"buffer_load_dwordx4 v[144:147], v0, s[4:7], s96 offen sc1\n"
"buffer_load_dwordx4 v[24:27], v0, s[4:7], s96 offen offset:1024 sc1\n"
"buffer_load_dwordx4 v[28:31], v0, s[4:7], s96 offen offset:2048 sc1\n"
"buffer_load_dwordx4 v[32:35], v0, s[4:7], s96 offen offset:3072 sc1\n"
"buffer_load_dwordx4 v[36:39], v1, s[4:7], s96 offen sc1\n"
"buffer_load_dwordx4 v[40:43], v1, s[4:7], s96 offen offset:1024 sc1\n"
"buffer_load_dwordx4 v[60:63], v1, s[4:7], s96 offen offset:2048 sc1\n"
"buffer_load_dwordx4 v[64:67], v1, s[4:7], s96 offen offset:3072 sc1\n"
"s_nop 7\n"
"s_branch .Lmk_p69r\n"
".Lmk_p70s:\n"
";\n"
"s_mov_b32 s1, 0x10000\n"
".Lmk_p70l:\n"
"s_add_u32 s98, s57, 0x400000\n"
"buffer_load_dwordx4 v[100:103], v0, s[4:7], s98 offen sc1\n"
"buffer_load_dwordx4 v[88:91], v0, s[4:7], s98 offen offset:1024 sc1\n"
"buffer_load_dwordx4 v[72:75], v0, s[4:7], s98 offen offset:2048 sc1\n"
"buffer_load_dwordx4 v[116:119], v0, s[4:7], s98 offen offset:3072 sc1\n"
"buffer_load_dwordx4 v[80:83], v1, s[4:7], s98 offen sc1\n"
"buffer_load_dwordx4 v[96:99], v1, s[4:7], s98 offen offset:1024 sc1\n"
"buffer_load_dwordx4 v[104:107], v1, s[4:7], s98 offen offset:2048 sc1\n"
"buffer_load_dwordx4 v[124:127], v1, s[4:7], s98 offen offset:3072 sc1\n"
"s_add_u32 s98, s58, 0x400000\n"
"buffer_load_dwordx4 v[144:147], v0, s[4:7], s98 offen sc1\n"
"buffer_load_dwordx4 v[24:27], v0, s[4:7], s98 offen offset:1024 sc1\n"
"buffer_load_dwordx4 v[28:31], v0, s[4:7], s98 offen offset:2048 sc1\n"
"buffer_load_dwordx4 v[32:35], v0, s[4:7], s98 offen offset:3072 sc1\n"
"buffer_load_dwordx4 v[36:39], v1, s[4:7], s98 offen sc1\n"
"buffer_load_dwordx4 v[40:43], v1, s[4:7], s98 offen offset:1024 sc1\n"
"buffer_load_dwordx4 v[60:63], v1, s[4:7], s98 offen offset:2048 sc1\n"
"buffer_load_dwordx4 v[64:67], v1, s[4:7], s98 offen offset:3072 sc1\n"
"s_waitcnt vmcnt(0)\n"
"v_max_u32_e32 v244, v100, v101\n"
"v_max3_u32 v244, v244, v102, v103\n"
"v_max3_u32 v244, v244, v88, v89\n"
"v_max3_u32 v244, v244, v90, v91\n"
"v_max3_u32 v244, v244, v72, v73\n"
"v_max3_u32 v244, v244, v74, v75\n"
"v_max3_u32 v244, v244, v116, v117\n"
"v_max3_u32 v244, v244, v118, v119\n"
"v_max3_u32 v244, v244, v80, v81\n"
"v_max3_u32 v244, v244, v82, v83\n"
"v_max3_u32 v244, v244, v96, v97\n"
"v_max3_u32 v244, v244, v98, v99\n"
"v_max3_u32 v244, v244, v104, v105\n"
"v_max3_u32 v244, v244, v106, v107\n"
"v_max3_u32 v244, v244, v124, v125\n"
"v_max3_u32 v244, v244, v126, v127\n"
"v_cmp_eq_u32_e32 vcc, -1, v244\n"
"s_cbranch_vccz .Lmk_p70k\n"
"s_sub_u32 s1, s1, 1\n"
"s_cbranch_scc0 .Lmk_p70l\n"
".Lmk_p70k:\n"
"v_mfma_f32_16x16x32_bf16 v[68:71], a[128:131], v[100:103], 0\n"
"v_mfma_f32_16x16x32_bf16 v[84:87], a[132:135], v[100:103], 0\n"
"v_mfma_f32_16x16x32_bf16 v[92:95], a[136:139], v[100:103], 0\n"
"v_mfma_f32_16x16x32_bf16 v[112:115], a[140:143], v[100:103], 0\n"
"v_mfma_f32_16x16x32_bf16 v[68:71], a[144:147], v[88:91], v[68:71]\n"
"v_mfma_f32_16x16x32_bf16 v[84:87], a[148:151], v[88:91], v[84:87]\n"
"v_mfma_f32_16x16x32_bf16 v[92:95], a[152:155], v[88:91], v[92:95]\n"
"v_mfma_f32_16x16x32_bf16 v[112:115], a[156:159], v[88:91], v[112:115]\n"
"v_mfma_f32_16x16x32_bf16 v[68:71], a[160:163], v[72:75], v[68:71]\n"
"v_mfma_f32_16x16x32_bf16 v[84:87], a[164:167], v[72:75], v[84:87]\n"
"v_mfma_f32_16x16x32_bf16 v[92:95], a[168:171], v[72:75], v[92:95]\n"
"v_mfma_f32_16x16x32_bf16 v[112:115], a[172:175], v[72:75], v[112:115]\n"
"v_mfma_f32_16x16x32_bf16 v[68:71], a[176:179], v[116:119], v[68:71]\n"
"v_mfma_f32_16x16x32_bf16 v[84:87], a[180:183], v[116:119], v[84:87]\n"
"v_mfma_f32_16x16x32_bf16 v[92:95], a[184:187], v[116:119], v[92:95]\n"
"v_mfma_f32_16x16x32_bf16 v[112:115], a[188:191], v[116:119], v[112:115]\n"
"v_mfma_f32_16x16x32_bf16 v[68:71], a[192:195], v[80:83], v[68:71]\n"
"v_mfma_f32_16x16x32_bf16 v[84:87], a[196:199], v[80:83], v[84:87]\n"
"v_mfma_f32_16x16x32_bf16 v[92:95], a[200:203], v[80:83], v[92:95]\n"
"v_mfma_f32_16x16x32_bf16 v[112:115], a[204:207], v[80:83], v[112:115]\n"
"v_mfma_f32_16x16x32_bf16 v[68:71], a[208:211], v[96:99], v[68:71]\n"
"v_mfma_f32_16x16x32_bf16 v[84:87], a[212:215], v[96:99], v[84:87]\n"
"v_mfma_f32_16x16x32_bf16 v[92:95], a[216:219], v[96:99], v[92:95]\n"
"v_mfma_f32_16x16x32_bf16 v[112:115], a[220:223], v[96:99], v[112:115]\n"
"v_mfma_f32_16x16x32_bf16 v[68:71], a[224:227], v[104:107], v[68:71]\n"
"v_mfma_f32_16x16x32_bf16 v[84:87], a[228:231], v[104:107], v[84:87]\n"
"v_mfma_f32_16x16x32_bf16 v[92:95], a[232:235], v[104:107], v[92:95]\n"
"v_mfma_f32_16x16x32_bf16 v[112:115], a[236:239], v[104:107], v[112:115]\n"
"v_mfma_f32_16x16x32_bf16 v[68:71], a[240:243], v[124:127], v[68:71]\n"
"v_mfma_f32_16x16x32_bf16 v[84:87], a[244:247], v[124:127], v[84:87]\n"
"v_mfma_f32_16x16x32_bf16 v[92:95], a[248:251], v[124:127], v[92:95]\n"
"v_mfma_f32_16x16x32_bf16 v[112:115], a[252:255], v[124:127], v[112:115]\n"
"s_add_u32 s98, s59, 0x400000\n"
"v_mov_b32_e32 v244, 0x80000000\n"
"buffer_load_dwordx4 v[100:103], v0, s[4:7], s98 offen sc1\n"
"buffer_load_dwordx4 v[88:91], v0, s[4:7], s98 offen offset:1024 sc1\n"
"buffer_load_dwordx4 v[72:75], v0, s[4:7], s98 offen offset:2048 sc1\n"
"buffer_load_dwordx4 v[116:119], v0, s[4:7], s98 offen offset:3072 sc1\n"
"buffer_load_dwordx4 v[80:83], v1, s[4:7], s98 offen sc1\n"
"buffer_load_dwordx4 v[96:99], v1, s[4:7], s98 offen offset:1024 sc1\n"
"buffer_load_dwordx4 v[104:107], v1, s[4:7], s98 offen offset:2048 sc1\n"
"buffer_load_dwordx4 v[124:127], v1, s[4:7], s98 offen offset:3072 sc1\n"
"buffer_store_dword v244, v244, s[4:7], 0 offen\n"
"s_nop 7\n"
"s_branch .Lmk_p70r\n"
".Lmk_q72s:\n"
";\n"
"s_mov_b32 s1, 0x10000\n"
".Lmk_q72l:\n"
"s_add_u32 s98, s59, 0x400000\n"
"buffer_load_dwordx4 v[100:103], v0, s[4:7], s98 offen sc1\n"
"buffer_load_dwordx4 v[88:91], v0, s[4:7], s98 offen offset:1024 sc1\n"
"buffer_load_dwordx4 v[72:75], v0, s[4:7], s98 offen offset:2048 sc1\n"
"buffer_load_dwordx4 v[116:119], v0, s[4:7], s98 offen offset:3072 sc1\n"
"buffer_load_dwordx4 v[80:83], v1, s[4:7], s98 offen sc1\n"
"buffer_load_dwordx4 v[96:99], v1, s[4:7], s98 offen offset:1024 sc1\n"
"buffer_load_dwordx4 v[104:107], v1, s[4:7], s98 offen offset:2048 sc1\n"
"buffer_load_dwordx4 v[124:127], v1, s[4:7], s98 offen offset:3072 sc1\n"
"s_waitcnt vmcnt(0)\n"
"v_max_u32_e32 v255, v100, v101\n"
"v_max3_u32 v255, v255, v102, v103\n"
"v_max3_u32 v255, v255, v88, v89\n"
"v_max3_u32 v255, v255, v90, v91\n"
"v_max3_u32 v255, v255, v72, v73\n"
"v_max3_u32 v255, v255, v74, v75\n"
"v_max3_u32 v255, v255, v116, v117\n"
"v_max3_u32 v255, v255, v118, v119\n"
"v_cmp_eq_u32_e32 vcc, -1, v255\n"
"s_cbranch_vccz .Lmk_q72k\n"
"s_sub_u32 s1, s1, 1\n"
"s_cbranch_scc0 .Lmk_q72l\n"
".Lmk_q72k:\n"
"s_branch .Lmk_q72r\n"
".Lmk_p71s:\n"
";\n"
"s_mov_b32 s1, 0x10000\n"
".Lmk_p71l:\n"
"s_add_u32 s98, s58, 0x400000\n"
"buffer_load_dwordx4 v[24:27], v0, s[4:7], s98 offen sc1\n"
"buffer_load_dwordx4 v[28:31], v0, s[4:7], s98 offen offset:1024 sc1\n"
"buffer_load_dwordx4 v[32:35], v0, s[4:7], s98 offen offset:2048 sc1\n"
"buffer_load_dwordx4 v[36:39], v0, s[4:7], s98 offen offset:3072 sc1\n"
"buffer_load_dwordx4 v[40:43], v1, s[4:7], s98 offen sc1\n"
"buffer_load_dwordx4 v[60:63], v1, s[4:7], s98 offen offset:1024 sc1\n"
"buffer_load_dwordx4 v[64:67], v1, s[4:7], s98 offen offset:2048 sc1\n"
"buffer_load_dwordx4 v[68:71], v1, s[4:7], s98 offen offset:3072 sc1\n"
"s_add_u32 s98, s59, 0x400000\n"
"buffer_load_dwordx4 v[100:103], v0, s[4:7], s98 offen sc1\n"
"buffer_load_dwordx4 v[88:91], v0, s[4:7], s98 offen offset:1024 sc1\n"
"buffer_load_dwordx4 v[72:75], v0, s[4:7], s98 offen offset:2048 sc1\n"
"buffer_load_dwordx4 v[116:119], v0, s[4:7], s98 offen offset:3072 sc1\n"
"buffer_load_dwordx4 v[80:83], v1, s[4:7], s98 offen sc1\n"
"buffer_load_dwordx4 v[96:99], v1, s[4:7], s98 offen offset:1024 sc1\n"
"buffer_load_dwordx4 v[104:107], v1, s[4:7], s98 offen offset:2048 sc1\n"
"buffer_load_dwordx4 v[124:127], v1, s[4:7], s98 offen offset:3072 sc1\n"
"s_waitcnt vmcnt(0)\n"
"v_max_u32_e32 v255, v24, v25\n"
"v_max3_u32 v255, v255, v26, v27\n"
"v_max3_u32 v255, v255, v28, v29\n"
"v_max3_u32 v255, v255, v30, v31\n"
"v_max3_u32 v255, v255, v32, v33\n"
"v_max3_u32 v255, v255, v34, v35\n"
"v_max3_u32 v255, v255, v36, v37\n"
"v_max3_u32 v255, v255, v38, v39\n"
"v_max3_u32 v255, v255, v40, v41\n"
"v_max3_u32 v255, v255, v42, v43\n"
"v_max3_u32 v255, v255, v60, v61\n"
"v_max3_u32 v255, v255, v62, v63\n"
"v_max3_u32 v255, v255, v64, v65\n"
"v_max3_u32 v255, v255, v66, v67\n"
"v_max3_u32 v255, v255, v68, v69\n"
"v_max3_u32 v255, v255, v70, v71\n"
"v_cmp_eq_u32_e32 vcc, -1, v255\n"
"s_cbranch_vccz .Lmk_p71k\n"
"s_sub_u32 s1, s1, 1\n"
"s_cbranch_scc0 .Lmk_p71l\n"
".Lmk_p71k:\n"
"v_mfma_f32_16x16x32_bf16 v[108:111], a[128:131], v[24:27], 0\n"
"v_mfma_f32_16x16x32_bf16 v[120:123], a[132:135], v[24:27], 0\n"
"v_mfma_f32_16x16x32_bf16 v[132:135], a[136:139], v[24:27], 0\n"
"v_mfma_f32_16x16x32_bf16 v[156:159], a[140:143], v[24:27], 0\n"
"v_mfma_f32_16x16x32_bf16 v[108:111], a[144:147], v[28:31], v[108:111]\n"
"v_mfma_f32_16x16x32_bf16 v[120:123], a[148:151], v[28:31], v[120:123]\n"
"v_mfma_f32_16x16x32_bf16 v[132:135], a[152:155], v[28:31], v[132:135]\n"
"v_mfma_f32_16x16x32_bf16 v[156:159], a[156:159], v[28:31], v[156:159]\n"
"v_mfma_f32_16x16x32_bf16 v[108:111], a[160:163], v[32:35], v[108:111]\n"
"v_mfma_f32_16x16x32_bf16 v[120:123], a[164:167], v[32:35], v[120:123]\n"
"v_mfma_f32_16x16x32_bf16 v[132:135], a[168:171], v[32:35], v[132:135]\n"
"v_mfma_f32_16x16x32_bf16 v[156:159], a[172:175], v[32:35], v[156:159]\n"
"v_mfma_f32_16x16x32_bf16 v[108:111], a[176:179], v[36:39], v[108:111]\n"
"v_mfma_f32_16x16x32_bf16 v[120:123], a[180:183], v[36:39], v[120:123]\n"
"v_mfma_f32_16x16x32_bf16 v[132:135], a[184:187], v[36:39], v[132:135]\n"
"v_mfma_f32_16x16x32_bf16 v[156:159], a[188:191], v[36:39], v[156:159]\n"
"v_mfma_f32_16x16x32_bf16 v[108:111], a[192:195], v[40:43], v[108:111]\n"
"v_mfma_f32_16x16x32_bf16 v[120:123], a[196:199], v[40:43], v[120:123]\n"
"v_mfma_f32_16x16x32_bf16 v[132:135], a[200:203], v[40:43], v[132:135]\n"
"v_mfma_f32_16x16x32_bf16 v[156:159], a[204:207], v[40:43], v[156:159]\n"
"v_mfma_f32_16x16x32_bf16 v[108:111], a[208:211], v[60:63], v[108:111]\n"
"v_mfma_f32_16x16x32_bf16 v[120:123], a[212:215], v[60:63], v[120:123]\n"
"v_mfma_f32_16x16x32_bf16 v[132:135], a[216:219], v[60:63], v[132:135]\n"
"v_mfma_f32_16x16x32_bf16 v[156:159], a[220:223], v[60:63], v[156:159]\n"
"v_mfma_f32_16x16x32_bf16 v[108:111], a[224:227], v[64:67], v[108:111]\n"
"v_mfma_f32_16x16x32_bf16 v[120:123], a[228:231], v[64:67], v[120:123]\n"
"v_mfma_f32_16x16x32_bf16 v[132:135], a[232:235], v[64:67], v[132:135]\n"
"v_mfma_f32_16x16x32_bf16 v[156:159], a[236:239], v[64:67], v[156:159]\n"
"v_mfma_f32_16x16x32_bf16 v[108:111], a[240:243], v[68:71], v[108:111]\n"
"v_mfma_f32_16x16x32_bf16 v[120:123], a[244:247], v[68:71], v[120:123]\n"
"v_mfma_f32_16x16x32_bf16 v[132:135], a[248:251], v[68:71], v[132:135]\n"
"v_mfma_f32_16x16x32_bf16 v[156:159], a[252:255], v[68:71], v[156:159]\n"
"s_nop 7\n"
"s_branch .Lmk_p71r\n"
".Lmk_q73s:\n"
";\n"
"s_mov_b32 s1, 0x10000\n"
".Lmk_q73l:\n"
"s_add_u32 s98, s59, 0x400000\n"
"buffer_load_dwordx4 v[80:83], v1, s[4:7], s98 offen sc1\n"
"buffer_load_dwordx4 v[96:99], v1, s[4:7], s98 offen offset:1024 sc1\n"
"buffer_load_dwordx4 v[104:107], v1, s[4:7], s98 offen offset:2048 sc1\n"
"buffer_load_dwordx4 v[124:127], v1, s[4:7], s98 offen offset:3072 sc1\n"
"s_waitcnt vmcnt(0)\n"
"v_max_u32_e32 v255, v80, v81\n"
"v_max3_u32 v255, v255, v82, v83\n"
"v_max3_u32 v255, v255, v96, v97\n"
"v_max3_u32 v255, v255, v98, v99\n"
"v_max3_u32 v255, v255, v104, v105\n"
"v_max3_u32 v255, v255, v106, v107\n"
"v_max3_u32 v255, v255, v124, v125\n"
"v_max3_u32 v255, v255, v126, v127\n"
"v_cmp_eq_u32_e32 vcc, -1, v255\n"
"s_cbranch_vccz .Lmk_q73k\n"
"s_sub_u32 s1, s1, 1\n"
"s_cbranch_scc0 .Lmk_q73l\n"
".Lmk_q73k:\n"
"s_branch .Lmk_q73r\n"
".Lmk_p74s:\n"
";\n"
"s_mov_b32 s1, 0x10000\n"
".Lmk_p74l:\n"
"s_add_u32 s96, s56, 0x440000\n"
"buffer_load_dwordx4 v[88:91], v0, s[4:7], s96 offen sc1\n"
"buffer_load_dwordx4 v[92:95], v0, s[4:7], s96 offen offset:1024 sc1\n"
"buffer_load_dwordx4 v[96:99], v0, s[4:7], s96 offen offset:2048 sc1\n"
"buffer_load_dwordx4 v[100:103], v0, s[4:7], s96 offen offset:3072 sc1\n"
"buffer_load_dwordx4 v[104:107], v1, s[4:7], s96 offen sc1\n"
"buffer_load_dwordx4 v[108:111], v1, s[4:7], s96 offen offset:1024 sc1\n"
"buffer_load_dwordx4 v[112:115], v1, s[4:7], s96 offen offset:2048 sc1\n"
"buffer_load_dwordx4 v[116:119], v1, s[4:7], s96 offen offset:3072 sc1\n"
"s_add_u32 s96, s57, 0x440000\n"
"buffer_load_dwordx4 v[40:43], v0, s[4:7], s96 offen sc1\n"
"buffer_load_dwordx4 v[44:47], v0, s[4:7], s96 offen offset:1024 sc1\n"
"buffer_load_dwordx4 v[48:51], v0, s[4:7], s96 offen offset:2048 sc1\n"
"buffer_load_dwordx4 v[60:63], v0, s[4:7], s96 offen offset:3072 sc1\n"
"buffer_load_dwordx4 v[72:75], v1, s[4:7], s96 offen sc1\n"
"buffer_load_dwordx4 v[76:79], v1, s[4:7], s96 offen offset:1024 sc1\n"
"buffer_load_dwordx4 v[80:83], v1, s[4:7], s96 offen offset:2048 sc1\n"
"buffer_load_dwordx4 v[84:87], v1, s[4:7], s96 offen offset:3072 sc1\n"
"s_add_u32 s96, s59, 0x440000\n"
"buffer_load_dwordx4 v[24:27], v0, s[4:7], s96 offen sc1\n"
"buffer_load_dwordx4 v[28:31], v0, s[4:7], s96 offen offset:1024 sc1\n"
"buffer_load_dwordx4 v[32:35], v0, s[4:7], s96 offen offset:2048 sc1\n"
"buffer_load_dwordx4 v[36:39], v0, s[4:7], s96 offen offset:3072 sc1\n"
"buffer_load_dwordx4 v[52:55], v1, s[4:7], s96 offen sc1\n"
"buffer_load_dwordx4 v[56:59], v1, s[4:7], s96 offen offset:1024 sc1\n"
"buffer_load_dwordx4 v[64:67], v1, s[4:7], s96 offen offset:2048 sc1\n"
"s_waitcnt vmcnt(0)\n"
"v_max_u32_e32 v255, v88, v89\n"
"v_max3_u32 v255, v255, v90, v91\n"
"v_max3_u32 v255, v255, v92, v93\n"
"v_max3_u32 v255, v255, v94, v95\n"
"v_max3_u32 v255, v255, v96, v97\n"
"v_max3_u32 v255, v255, v98, v99\n"
"v_max3_u32 v255, v255, v100, v101\n"
"v_max3_u32 v255, v255, v102, v103\n"
"v_max3_u32 v255, v255, v104, v105\n"
"v_max3_u32 v255, v255, v106, v107\n"
"v_max3_u32 v255, v255, v108, v109\n"
"v_max3_u32 v255, v255, v110, v111\n"
"v_max3_u32 v255, v255, v112, v113\n"
"v_max3_u32 v255, v255, v114, v115\n"
"v_max3_u32 v255, v255, v116, v117\n"
"v_max3_u32 v255, v255, v118, v119\n"
"v_cmp_eq_u32_e32 vcc, -1, v255\n"
"s_cbranch_vccz .Lmk_p74k\n"
"s_sub_u32 s1, s1, 1\n"
"s_cbranch_scc0 .Lmk_p74l\n"
".Lmk_p74k:\n"
"v_mfma_f32_16x16x32_bf16 v[120:123], a[128:131], v[88:91], 0\n"
"v_mfma_f32_16x16x32_bf16 v[124:127], a[132:135], v[88:91], 0\n"
"v_mfma_f32_16x16x32_bf16 v[128:131], a[136:139], v[88:91], 0\n"
"v_mfma_f32_16x16x32_bf16 v[132:135], a[140:143], v[88:91], 0\n"
"v_mfma_f32_16x16x32_bf16 v[120:123], a[144:147], v[92:95], v[120:123]\n"
"v_mfma_f32_16x16x32_bf16 v[124:127], a[148:151], v[92:95], v[124:127]\n"
"v_mfma_f32_16x16x32_bf16 v[128:131], a[152:155], v[92:95], v[128:131]\n"
"v_mfma_f32_16x16x32_bf16 v[132:135], a[156:159], v[92:95], v[132:135]\n"
"v_mfma_f32_16x16x32_bf16 v[120:123], a[160:163], v[96:99], v[120:123]\n"
"v_mfma_f32_16x16x32_bf16 v[124:127], a[164:167], v[96:99], v[124:127]\n"
"v_mfma_f32_16x16x32_bf16 v[128:131], a[168:171], v[96:99], v[128:131]\n"
"v_mfma_f32_16x16x32_bf16 v[132:135], a[172:175], v[96:99], v[132:135]\n"
"v_mfma_f32_16x16x32_bf16 v[120:123], a[176:179], v[100:103], v[120:123]\n"
"v_mfma_f32_16x16x32_bf16 v[124:127], a[180:183], v[100:103], v[124:127]\n"
"v_mfma_f32_16x16x32_bf16 v[128:131], a[184:187], v[100:103], v[128:131]\n"
"v_mfma_f32_16x16x32_bf16 v[132:135], a[188:191], v[100:103], v[132:135]\n"
"v_mfma_f32_16x16x32_bf16 v[120:123], a[192:195], v[104:107], v[120:123]\n"
"v_mfma_f32_16x16x32_bf16 v[124:127], a[196:199], v[104:107], v[124:127]\n"
"v_mfma_f32_16x16x32_bf16 v[128:131], a[200:203], v[104:107], v[128:131]\n"
"v_mfma_f32_16x16x32_bf16 v[132:135], a[204:207], v[104:107], v[132:135]\n"
"v_mfma_f32_16x16x32_bf16 v[120:123], a[208:211], v[108:111], v[120:123]\n"
"v_mfma_f32_16x16x32_bf16 v[124:127], a[212:215], v[108:111], v[124:127]\n"
"v_mfma_f32_16x16x32_bf16 v[128:131], a[216:219], v[108:111], v[128:131]\n"
"v_mfma_f32_16x16x32_bf16 v[132:135], a[220:223], v[108:111], v[132:135]\n"
"v_mfma_f32_16x16x32_bf16 v[120:123], a[224:227], v[112:115], v[120:123]\n"
"v_mfma_f32_16x16x32_bf16 v[124:127], a[228:231], v[112:115], v[124:127]\n"
"v_mfma_f32_16x16x32_bf16 v[128:131], a[232:235], v[112:115], v[128:131]\n"
"v_mfma_f32_16x16x32_bf16 v[132:135], a[236:239], v[112:115], v[132:135]\n"
"v_mfma_f32_16x16x32_bf16 v[120:123], a[240:243], v[116:119], v[120:123]\n"
"v_mfma_f32_16x16x32_bf16 v[124:127], a[244:247], v[116:119], v[124:127]\n"
"v_mfma_f32_16x16x32_bf16 v[128:131], a[248:251], v[116:119], v[128:131]\n"
"v_mfma_f32_16x16x32_bf16 v[132:135], a[252:255], v[116:119], v[132:135]\n"
"s_add_u32 s96, s58, 0x440000\n"
"v_mov_b32_e32 v255, 0x80000000\n"
"buffer_load_dwordx4 v[88:91], v0, s[4:7], s96 offen sc1\n"
"buffer_load_dwordx4 v[92:95], v0, s[4:7], s96 offen offset:1024 sc1\n"
"buffer_load_dwordx4 v[96:99], v0, s[4:7], s96 offen offset:2048 sc1\n"
"buffer_load_dwordx4 v[100:103], v0, s[4:7], s96 offen offset:3072 sc1\n"
"buffer_load_dwordx4 v[104:107], v1, s[4:7], s96 offen sc1\n"
"buffer_load_dwordx4 v[108:111], v1, s[4:7], s96 offen offset:1024 sc1\n"
"buffer_load_dwordx4 v[112:115], v1, s[4:7], s96 offen offset:2048 sc1\n"
"buffer_load_dwordx4 v[116:119], v1, s[4:7], s96 offen offset:3072 sc1\n"
"buffer_store_dword v255, v255, s[4:7], 0 offen\n"
"buffer_store_dword v255, v255, s[4:7], 0 offen\n"
"buffer_store_dword v255, v255, s[4:7], 0 offen\n"
"buffer_store_dword v255, v255, s[4:7], 0 offen\n"
"buffer_store_dword v255, v255, s[4:7], 0 offen\n"
"buffer_store_dword v255, v255, s[4:7], 0 offen\n"
"buffer_store_dword v255, v255, s[4:7], 0 offen\n"
"buffer_store_dword v255, v255, s[4:7], 0 offen\n"
"buffer_store_dword v255, v255, s[4:7], 0 offen\n"
"buffer_store_dword v255, v255, s[4:7], 0 offen\n"
"buffer_store_dword v255, v255, s[4:7], 0 offen\n"
"buffer_store_dword v255, v255, s[4:7], 0 offen\n"
"buffer_store_dword v255, v255, s[4:7], 0 offen\n"
"buffer_store_dword v255, v255, s[4:7], 0 offen\n"
"buffer_store_dword v255, v255, s[4:7], 0 offen\n"
"s_nop 7\n"
"s_branch .Lmk_p74r\n"
".Lmk_p75s:\n"
";\n"
"s_mov_b32 s1, 0x10000\n"
".Lmk_p75l:\n"
"s_add_u32 s96, s57, 0x440000\n"
"buffer_load_dwordx4 v[24:27], v0, s[4:7], s96 offen sc1\n"
"buffer_load_dwordx4 v[28:31], v0, s[4:7], s96 offen offset:1024 sc1\n"
"buffer_load_dwordx4 v[32:35], v0, s[4:7], s96 offen offset:2048 sc1\n"
"buffer_load_dwordx4 v[36:39], v0, s[4:7], s96 offen offset:3072 sc1\n"
"buffer_load_dwordx4 v[52:55], v1, s[4:7], s96 offen sc1\n"
"buffer_load_dwordx4 v[56:59], v1, s[4:7], s96 offen offset:1024 sc1\n"
"buffer_load_dwordx4 v[64:67], v1, s[4:7], s96 offen offset:2048 sc1\n"
"buffer_load_dwordx4 v[144:147], v1, s[4:7], s96 offen offset:3072 sc1\n"
"s_add_u32 s96, s58, 0x440000\n"
"buffer_load_dwordx4 v[88:91], v0, s[4:7], s96 offen sc1\n"
"buffer_load_dwordx4 v[92:95], v0, s[4:7], s96 offen offset:1024 sc1\n"
"buffer_load_dwordx4 v[96:99], v0, s[4:7], s96 offen offset:2048 sc1\n"
"buffer_load_dwordx4 v[100:103], v0, s[4:7], s96 offen offset:3072 sc1\n"
"buffer_load_dwordx4 v[104:107], v1, s[4:7], s96 offen sc1\n"
"buffer_load_dwordx4 v[108:111], v1, s[4:7], s96 offen offset:1024 sc1\n"
"buffer_load_dwordx4 v[112:115], v1, s[4:7], s96 offen offset:2048 sc1\n"
"buffer_load_dwordx4 v[116:119], v1, s[4:7], s96 offen offset:3072 sc1\n"
"s_add_u32 s96, s56, 0x400000\n"
"buffer_load_dwordx4 v[40:43], v0, s[4:7], s96 offen sc1\n"
"buffer_load_dwordx4 v[120:123], v0, s[4:7], s96 offen offset:1024 sc1\n"
"buffer_load_dwordx4 v[60:63], v0, s[4:7], s96 offen offset:2048 sc1\n"
"buffer_load_dwordx4 v[72:75], v0, s[4:7], s96 offen offset:3072 sc1\n"
"buffer_load_dwordx4 v[76:79], v1, s[4:7], s96 offen sc1\n"
"buffer_load_dwordx4 v[80:83], v1, s[4:7], s96 offen offset:1024 sc1\n"
"buffer_load_dwordx4 v[44:47], v1, s[4:7], s96 offen offset:2048 sc1\n"
"s_waitcnt vmcnt(0)\n"
"v_max_u32_e32 v243, v24, v25\n"
"v_max3_u32 v243, v243, v26, v27\n"
"v_max3_u32 v243, v243, v28, v29\n"
"v_max3_u32 v243, v243, v30, v31\n"
"v_max3_u32 v243, v243, v32, v33\n"
"v_max3_u32 v243, v243, v34, v35\n"
"v_max3_u32 v243, v243, v36, v37\n"
"v_max3_u32 v243, v243, v38, v39\n"
"v_max3_u32 v243, v243, v52, v53\n"
"v_max3_u32 v243, v243, v54, v55\n"
"v_max3_u32 v243, v243, v56, v57\n"
"v_max3_u32 v243, v243, v58, v59\n"
"v_max3_u32 v243, v243, v64, v65\n"
"v_max3_u32 v243, v243, v66, v67\n"
"v_max3_u32 v243, v243, v144, v145\n"
"v_max3_u32 v243, v243, v146, v147\n"
"v_cmp_eq_u32_e32 vcc, -1, v243\n"
"s_cbranch_vccz .Lmk_p75k\n"
"s_sub_u32 s1, s1, 1\n"
"s_cbranch_scc0 .Lmk_p75l\n"
".Lmk_p75k:\n"
"v_mfma_f32_16x16x32_bf16 v[68:71], a[128:131], v[24:27], 0\n"
"v_mfma_f32_16x16x32_bf16 v[136:139], a[132:135], v[24:27], 0\n"
"v_mfma_f32_16x16x32_bf16 v[140:143], a[136:139], v[24:27], 0\n"
"v_mfma_f32_16x16x32_bf16 v[148:151], a[140:143], v[24:27], 0\n"
"v_mfma_f32_16x16x32_bf16 v[68:71], a[144:147], v[28:31], v[68:71]\n"
"v_mfma_f32_16x16x32_bf16 v[136:139], a[148:151], v[28:31], v[136:139]\n"
"v_mfma_f32_16x16x32_bf16 v[140:143], a[152:155], v[28:31], v[140:143]\n"
"v_mfma_f32_16x16x32_bf16 v[148:151], a[156:159], v[28:31], v[148:151]\n"
"v_mfma_f32_16x16x32_bf16 v[68:71], a[160:163], v[32:35], v[68:71]\n"
"v_mfma_f32_16x16x32_bf16 v[136:139], a[164:167], v[32:35], v[136:139]\n"
"v_mfma_f32_16x16x32_bf16 v[140:143], a[168:171], v[32:35], v[140:143]\n"
"v_mfma_f32_16x16x32_bf16 v[148:151], a[172:175], v[32:35], v[148:151]\n"
"v_mfma_f32_16x16x32_bf16 v[68:71], a[176:179], v[36:39], v[68:71]\n"
"v_mfma_f32_16x16x32_bf16 v[136:139], a[180:183], v[36:39], v[136:139]\n"
"v_mfma_f32_16x16x32_bf16 v[140:143], a[184:187], v[36:39], v[140:143]\n"
"v_mfma_f32_16x16x32_bf16 v[148:151], a[188:191], v[36:39], v[148:151]\n"
"v_mfma_f32_16x16x32_bf16 v[68:71], a[192:195], v[52:55], v[68:71]\n"
"v_mfma_f32_16x16x32_bf16 v[136:139], a[196:199], v[52:55], v[136:139]\n"
"v_mfma_f32_16x16x32_bf16 v[140:143], a[200:203], v[52:55], v[140:143]\n"
"v_mfma_f32_16x16x32_bf16 v[148:151], a[204:207], v[52:55], v[148:151]\n"
"v_mfma_f32_16x16x32_bf16 v[68:71], a[208:211], v[56:59], v[68:71]\n"
"v_mfma_f32_16x16x32_bf16 v[136:139], a[212:215], v[56:59], v[136:139]\n"
"v_mfma_f32_16x16x32_bf16 v[140:143], a[216:219], v[56:59], v[140:143]\n"
"v_mfma_f32_16x16x32_bf16 v[148:151], a[220:223], v[56:59], v[148:151]\n"
"v_mfma_f32_16x16x32_bf16 v[68:71], a[224:227], v[64:67], v[68:71]\n"
"v_mfma_f32_16x16x32_bf16 v[136:139], a[228:231], v[64:67], v[136:139]\n"
"v_mfma_f32_16x16x32_bf16 v[140:143], a[232:235], v[64:67], v[140:143]\n"
"v_mfma_f32_16x16x32_bf16 v[148:151], a[236:239], v[64:67], v[148:151]\n"
"v_mfma_f32_16x16x32_bf16 v[68:71], a[240:243], v[144:147], v[68:71]\n"
"v_mfma_f32_16x16x32_bf16 v[136:139], a[244:247], v[144:147], v[136:139]\n"
"v_mfma_f32_16x16x32_bf16 v[140:143], a[248:251], v[144:147], v[140:143]\n"
"v_mfma_f32_16x16x32_bf16 v[148:151], a[252:255], v[144:147], v[148:151]\n"
"s_add_u32 s96, s59, 0x440000\n"
"v_mov_b32_e32 v243, 0x80000000\n"
"buffer_load_dwordx4 v[24:27], v0, s[4:7], s96 offen sc1\n"
"buffer_load_dwordx4 v[28:31], v0, s[4:7], s96 offen offset:1024 sc1\n"
"buffer_load_dwordx4 v[32:35], v0, s[4:7], s96 offen offset:2048 sc1\n"
"buffer_load_dwordx4 v[36:39], v0, s[4:7], s96 offen offset:3072 sc1\n"
"buffer_load_dwordx4 v[52:55], v1, s[4:7], s96 offen sc1\n"
"buffer_load_dwordx4 v[56:59], v1, s[4:7], s96 offen offset:1024 sc1\n"
"buffer_load_dwordx4 v[64:67], v1, s[4:7], s96 offen offset:2048 sc1\n"
"buffer_load_dwordx4 v[144:147], v1, s[4:7], s96 offen offset:3072 sc1\n"
"buffer_store_dword v243, v243, s[4:7], 0 offen\n"
"buffer_store_dword v243, v243, s[4:7], 0 offen\n"
"buffer_store_dword v243, v243, s[4:7], 0 offen\n"
"buffer_store_dword v243, v243, s[4:7], 0 offen\n"
"buffer_store_dword v243, v243, s[4:7], 0 offen\n"
"buffer_store_dword v243, v243, s[4:7], 0 offen\n"
"buffer_store_dword v243, v243, s[4:7], 0 offen\n"
"buffer_store_dword v243, v243, s[4:7], 0 offen\n"
"s_nop 7\n"
"s_branch .Lmk_p75r\n"
".Lmk_q77s:\n"
";\n"
"s_mov_b32 s1, 0x10000\n"
".Lmk_q77l:\n"
"s_add_u32 s96, s59, 0x440000\n"
"buffer_load_dwordx4 v[24:27], v0, s[4:7], s96 offen sc1\n"
"buffer_load_dwordx4 v[28:31], v0, s[4:7], s96 offen offset:1024 sc1\n"
"buffer_load_dwordx4 v[32:35], v0, s[4:7], s96 offen offset:2048 sc1\n"
"buffer_load_dwordx4 v[36:39], v0, s[4:7], s96 offen offset:3072 sc1\n"
"buffer_load_dwordx4 v[52:55], v1, s[4:7], s96 offen sc1\n"
"buffer_load_dwordx4 v[56:59], v1, s[4:7], s96 offen offset:1024 sc1\n"
"buffer_load_dwordx4 v[64:67], v1, s[4:7], s96 offen offset:2048 sc1\n"
"buffer_load_dwordx4 v[144:147], v1, s[4:7], s96 offen offset:3072 sc1\n"
"s_add_u32 s96, s56, 0x400000\n"
"buffer_load_dwordx4 v[40:43], v0, s[4:7], s96 offen sc1\n"
"buffer_load_dwordx4 v[120:123], v0, s[4:7], s96 offen offset:1024 sc1\n"
"buffer_load_dwordx4 v[60:63], v0, s[4:7], s96 offen offset:2048 sc1\n"
"buffer_load_dwordx4 v[72:75], v0, s[4:7], s96 offen offset:3072 sc1\n"
"buffer_load_dwordx4 v[76:79], v1, s[4:7], s96 offen sc1\n"
"buffer_load_dwordx4 v[80:83], v1, s[4:7], s96 offen offset:1024 sc1\n"
"buffer_load_dwordx4 v[44:47], v1, s[4:7], s96 offen offset:2048 sc1\n"
"buffer_load_dwordx4 v[164:167], v1, s[4:7], s96 offen offset:3072 sc1\n"
"s_add_u32 s96, s57, 0x400000\n"
"buffer_load_dwordx4 v[68:71], v0, s[4:7], s96 offen sc1\n"
"buffer_load_dwordx4 v[88:91], v0, s[4:7], s96 offen offset:1024 sc1\n"
"buffer_load_dwordx4 v[48:51], v0, s[4:7], s96 offen offset:2048 sc1\n"
"buffer_load_dwordx4 v[92:95], v0, s[4:7], s96 offen offset:3072 sc1\n"
"buffer_load_dwordx4 v[96:99], v1, s[4:7], s96 offen sc1\n"
"buffer_load_dwordx4 v[100:103], v1, s[4:7], s96 offen offset:1024 sc1\n"
"s_waitcnt vmcnt(0)\n"
"v_max_u32_e32 v255, v24, v25\n"
"v_max3_u32 v255, v255, v26, v27\n"
"v_max3_u32 v255, v255, v28, v29\n"
"v_max3_u32 v255, v255, v30, v31\n"
"v_max3_u32 v255, v255, v32, v33\n"
"v_max3_u32 v255, v255, v34, v35\n"
"v_max3_u32 v255, v255, v36, v37\n"
"v_max3_u32 v255, v255, v38, v39\n"
"v_cmp_eq_u32_e32 vcc, -1, v255\n"
"s_cbranch_vccz .Lmk_q77k\n"
"s_sub_u32 s1, s1, 1\n"
"s_cbranch_scc0 .Lmk_q77l\n"
".Lmk_q77k:\n"
"s_branch .Lmk_q77r\n"
".Lmk_p76s:\n"
";\n"
"s_mov_b32 s1, 0x10000\n"
".Lmk_p76l:\n"
"s_add_u32 s96, s58, 0x440000\n"
"buffer_load_dwordx4 v[40:43], v0, s[4:7], s96 offen sc1\n"
"buffer_load_dwordx4 v[120:123], v0, s[4:7], s96 offen offset:1024 sc1\n"
"buffer_load_dwordx4 v[60:63], v0, s[4:7], s96 offen offset:2048 sc1\n"
"buffer_load_dwordx4 v[72:75], v0, s[4:7], s96 offen offset:3072 sc1\n"
"buffer_load_dwordx4 v[76:79], v1, s[4:7], s96 offen sc1\n"
"buffer_load_dwordx4 v[80:83], v1, s[4:7], s96 offen offset:1024 sc1\n"
"buffer_load_dwordx4 v[44:47], v1, s[4:7], s96 offen offset:2048 sc1\n"
"buffer_load_dwordx4 v[164:167], v1, s[4:7], s96 offen offset:3072 sc1\n"
"s_add_u32 s96, s59, 0x440000\n"
"buffer_load_dwordx4 v[24:27], v0, s[4:7], s96 offen sc1\n"
"buffer_load_dwordx4 v[28:31], v0, s[4:7], s96 offen offset:1024 sc1\n"
"buffer_load_dwordx4 v[32:35], v0, s[4:7], s96 offen offset:2048 sc1\n"
"buffer_load_dwordx4 v[36:39], v0, s[4:7], s96 offen offset:3072 sc1\n"
"buffer_load_dwordx4 v[52:55], v1, s[4:7], s96 offen sc1\n"
"buffer_load_dwordx4 v[56:59], v1, s[4:7], s96 offen offset:1024 sc1\n"
"buffer_load_dwordx4 v[64:67], v1, s[4:7], s96 offen offset:2048 sc1\n"
"buffer_load_dwordx4 v[144:147], v1, s[4:7], s96 offen offset:3072 sc1\n"
"s_add_u32 s96, s57, 0x400000\n"
"buffer_load_dwordx4 v[68:71], v0, s[4:7], s96 offen sc1\n"
"buffer_load_dwordx4 v[88:91], v0, s[4:7], s96 offen offset:1024 sc1\n"
"buffer_load_dwordx4 v[48:51], v0, s[4:7], s96 offen offset:2048 sc1\n"
"buffer_load_dwordx4 v[92:95], v0, s[4:7], s96 offen offset:3072 sc1\n"
"buffer_load_dwordx4 v[96:99], v1, s[4:7], s96 offen sc1\n"
"buffer_load_dwordx4 v[100:103], v1, s[4:7], s96 offen offset:1024 sc1\n"
"s_waitcnt vmcnt(0)\n"
"v_max_u32_e32 v255, v40, v41\n"
"v_max3_u32 v255, v255, v42, v43\n"
"v_max3_u32 v255, v255, v120, v121\n"
"v_max3_u32 v255, v255, v122, v123\n"
"v_max3_u32 v255, v255, v60, v61\n"
"v_max3_u32 v255, v255, v62, v63\n"
"v_max3_u32 v255, v255, v72, v73\n"
"v_max3_u32 v255, v255, v74, v75\n"
"v_max3_u32 v255, v255, v76, v77\n"
"v_max3_u32 v255, v255, v78, v79\n"
"v_max3_u32 v255, v255, v80, v81\n"
"v_max3_u32 v255, v255, v82, v83\n"
"v_max3_u32 v255, v255, v44, v45\n"
"v_max3_u32 v255, v255, v46, v47\n"
"v_max3_u32 v255, v255, v164, v165\n"
"v_max3_u32 v255, v255, v166, v167\n"
"v_cmp_eq_u32_e32 vcc, -1, v255\n"
"s_cbranch_vccz .Lmk_p76k\n"
"s_sub_u32 s1, s1, 1\n"
"s_cbranch_scc0 .Lmk_p76l\n"
".Lmk_p76k:\n"
"v_mfma_f32_16x16x32_bf16 v[84:87], a[128:131], v[40:43], 0\n"
"v_mfma_f32_16x16x32_bf16 v[156:159], a[132:135], v[40:43], 0\n"
"v_mfma_f32_16x16x32_bf16 v[160:163], a[136:139], v[40:43], 0\n"
"v_mfma_f32_16x16x32_bf16 v[168:171], a[140:143], v[40:43], 0\n"
"v_mfma_f32_16x16x32_bf16 v[84:87], a[144:147], v[120:123], v[84:87]\n"
"v_mfma_f32_16x16x32_bf16 v[156:159], a[148:151], v[120:123], v[156:159]\n"
"v_mfma_f32_16x16x32_bf16 v[160:163], a[152:155], v[120:123], v[160:163]\n"
"v_mfma_f32_16x16x32_bf16 v[168:171], a[156:159], v[120:123], v[168:171]\n"
"v_mfma_f32_16x16x32_bf16 v[84:87], a[160:163], v[60:63], v[84:87]\n"
"v_mfma_f32_16x16x32_bf16 v[156:159], a[164:167], v[60:63], v[156:159]\n"
"v_mfma_f32_16x16x32_bf16 v[160:163], a[168:171], v[60:63], v[160:163]\n"
"v_mfma_f32_16x16x32_bf16 v[168:171], a[172:175], v[60:63], v[168:171]\n"
"v_mfma_f32_16x16x32_bf16 v[84:87], a[176:179], v[72:75], v[84:87]\n"
"v_mfma_f32_16x16x32_bf16 v[156:159], a[180:183], v[72:75], v[156:159]\n"
"v_mfma_f32_16x16x32_bf16 v[160:163], a[184:187], v[72:75], v[160:163]\n"
"v_mfma_f32_16x16x32_bf16 v[168:171], a[188:191], v[72:75], v[168:171]\n"
"v_mfma_f32_16x16x32_bf16 v[84:87], a[192:195], v[76:79], v[84:87]\n"
"v_mfma_f32_16x16x32_bf16 v[156:159], a[196:199], v[76:79], v[156:159]\n"
"v_mfma_f32_16x16x32_bf16 v[160:163], a[200:203], v[76:79], v[160:163]\n"
"v_mfma_f32_16x16x32_bf16 v[168:171], a[204:207], v[76:79], v[168:171]\n"
"v_mfma_f32_16x16x32_bf16 v[84:87], a[208:211], v[80:83], v[84:87]\n"
"v_mfma_f32_16x16x32_bf16 v[156:159], a[212:215], v[80:83], v[156:159]\n"
"v_mfma_f32_16x16x32_bf16 v[160:163], a[216:219], v[80:83], v[160:163]\n"
"v_mfma_f32_16x16x32_bf16 v[168:171], a[220:223], v[80:83], v[168:171]\n"
"v_mfma_f32_16x16x32_bf16 v[84:87], a[224:227], v[44:47], v[84:87]\n"
"v_mfma_f32_16x16x32_bf16 v[156:159], a[228:231], v[44:47], v[156:159]\n"
"v_mfma_f32_16x16x32_bf16 v[160:163], a[232:235], v[44:47], v[160:163]\n"
"v_mfma_f32_16x16x32_bf16 v[168:171], a[236:239], v[44:47], v[168:171]\n"
"v_mfma_f32_16x16x32_bf16 v[84:87], a[240:243], v[164:167], v[84:87]\n"
"v_mfma_f32_16x16x32_bf16 v[156:159], a[244:247], v[164:167], v[156:159]\n"
"v_mfma_f32_16x16x32_bf16 v[160:163], a[248:251], v[164:167], v[160:163]\n"
"v_mfma_f32_16x16x32_bf16 v[168:171], a[252:255], v[164:167], v[168:171]\n"
"s_add_u32 s96, s56, 0x400000\n"
"v_mov_b32_e32 v255, 0x80000000\n"
"buffer_load_dwordx4 v[40:43], v0, s[4:7], s96 offen sc1\n"
"buffer_load_dwordx4 v[120:123], v0, s[4:7], s96 offen offset:1024 sc1\n"
"buffer_load_dwordx4 v[60:63], v0, s[4:7], s96 offen offset:2048 sc1\n"
"buffer_load_dwordx4 v[72:75], v0, s[4:7], s96 offen offset:3072 sc1\n"
"buffer_load_dwordx4 v[76:79], v1, s[4:7], s96 offen sc1\n"
"buffer_load_dwordx4 v[80:83], v1, s[4:7], s96 offen offset:1024 sc1\n"
"buffer_load_dwordx4 v[44:47], v1, s[4:7], s96 offen offset:2048 sc1\n"
"buffer_load_dwordx4 v[164:167], v1, s[4:7], s96 offen offset:3072 sc1\n"
"buffer_store_dword v255, v255, s[4:7], 0 offen\n"
"buffer_store_dword v255, v255, s[4:7], 0 offen\n"
"buffer_store_dword v255, v255, s[4:7], 0 offen\n"
"buffer_store_dword v255, v255, s[4:7], 0 offen\n"
"buffer_store_dword v255, v255, s[4:7], 0 offen\n"
"buffer_store_dword v255, v255, s[4:7], 0 offen\n"
"s_nop 7\n"
"s_branch .Lmk_p76r\n"
".Lmk_q78s:\n"
";\n"
"s_mov_b32 s1, 0x10000\n"
".Lmk_q78l:\n"
"s_add_u32 s96, s59, 0x440000\n"
"buffer_load_dwordx4 v[52:55], v1, s[4:7], s96 offen sc1\n"
"buffer_load_dwordx4 v[56:59], v1, s[4:7], s96 offen offset:1024 sc1\n"
"buffer_load_dwordx4 v[64:67], v1, s[4:7], s96 offen offset:2048 sc1\n"
"buffer_load_dwordx4 v[144:147], v1, s[4:7], s96 offen offset:3072 sc1\n"
"s_add_u32 s96, s56, 0x400000\n"
"buffer_load_dwordx4 v[40:43], v0, s[4:7], s96 offen sc1\n"
"buffer_load_dwordx4 v[120:123], v0, s[4:7], s96 offen offset:1024 sc1\n"
"buffer_load_dwordx4 v[60:63], v0, s[4:7], s96 offen offset:2048 sc1\n"
"buffer_load_dwordx4 v[72:75], v0, s[4:7], s96 offen offset:3072 sc1\n"
"buffer_load_dwordx4 v[76:79], v1, s[4:7], s96 offen sc1\n"
"buffer_load_dwordx4 v[80:83], v1, s[4:7], s96 offen offset:1024 sc1\n"
"buffer_load_dwordx4 v[44:47], v1, s[4:7], s96 offen offset:2048 sc1\n"
"buffer_load_dwordx4 v[164:167], v1, s[4:7], s96 offen offset:3072 sc1\n"
"s_add_u32 s96, s57, 0x400000\n"
"buffer_load_dwordx4 v[68:71], v0, s[4:7], s96 offen sc1\n"
"buffer_load_dwordx4 v[88:91], v0, s[4:7], s96 offen offset:1024 sc1\n"
"buffer_load_dwordx4 v[48:51], v0, s[4:7], s96 offen offset:2048 sc1\n"
"buffer_load_dwordx4 v[92:95], v0, s[4:7], s96 offen offset:3072 sc1\n"
"buffer_load_dwordx4 v[96:99], v1, s[4:7], s96 offen sc1\n"
"buffer_load_dwordx4 v[100:103], v1, s[4:7], s96 offen offset:1024 sc1\n"
"buffer_load_dwordx4 v[104:107], v1, s[4:7], s96 offen offset:2048 sc1\n"
"buffer_load_dwordx4 v[24:27], v1, s[4:7], s96 offen offset:3072 sc1\n"
"s_waitcnt vmcnt(0)\n"
"v_max_u32_e32 v255, v52, v53\n"
"v_max3_u32 v255, v255, v54, v55\n"
"v_max3_u32 v255, v255, v56, v57\n"
"v_max3_u32 v255, v255, v58, v59\n"
"v_max3_u32 v255, v255, v64, v65\n"
"v_max3_u32 v255, v255, v66, v67\n"
"v_max3_u32 v255, v255, v144, v145\n"
"v_max3_u32 v255, v255, v146, v147\n"
"v_cmp_eq_u32_e32 vcc, -1, v255\n"
"s_cbranch_vccz .Lmk_q78k\n"
"s_sub_u32 s1, s1, 1\n"
"s_cbranch_scc0 .Lmk_q78l\n"
".Lmk_q78k:\n"
"s_branch .Lmk_q78r\n"
".Lmk_p80s:\n"
";\n"
"s_mov_b32 s1, 0x10000\n"
".Lmk_p80l:\n"
"s_add_u32 s97, s56, 0x400000\n"
"buffer_load_dwordx4 v[140:143], v0, s[4:7], s97 offen sc1\n"
"buffer_load_dwordx4 v[124:127], v0, s[4:7], s97 offen offset:1024 sc1\n"
"buffer_load_dwordx4 v[36:39], v0, s[4:7], s97 offen offset:2048 sc1\n"
"buffer_load_dwordx4 v[56:59], v0, s[4:7], s97 offen offset:3072 sc1\n"
"buffer_load_dwordx4 v[132:135], v1, s[4:7], s97 offen sc1\n"
"buffer_load_dwordx4 v[112:115], v1, s[4:7], s97 offen offset:1024 sc1\n"
"buffer_load_dwordx4 v[156:159], v1, s[4:7], s97 offen offset:2048 sc1\n"
"buffer_load_dwordx4 v[128:131], v1, s[4:7], s97 offen offset:3072 sc1\n"
"s_add_u32 s97, s57, 0x400000\n"
"buffer_load_dwordx4 v[68:71], v0, s[4:7], s97 offen sc1\n"
"buffer_load_dwordx4 v[88:91], v0, s[4:7], s97 offen offset:1024 sc1\n"
"buffer_load_dwordx4 v[48:51], v0, s[4:7], s97 offen offset:2048 sc1\n"
"buffer_load_dwordx4 v[92:95], v0, s[4:7], s97 offen offset:3072 sc1\n"
"buffer_load_dwordx4 v[96:99], v1, s[4:7], s97 offen sc1\n"
"buffer_load_dwordx4 v[100:103], v1, s[4:7], s97 offen offset:1024 sc1\n"
"buffer_load_dwordx4 v[104:107], v1, s[4:7], s97 offen offset:2048 sc1\n"
"buffer_load_dwordx4 v[24:27], v1, s[4:7], s97 offen offset:3072 sc1\n"
"s_add_u32 s97, s58, 0x400000\n"
"buffer_load_dwordx4 v[28:31], v0, s[4:7], s97 offen sc1\n"
"buffer_load_dwordx4 v[32:35], v0, s[4:7], s97 offen offset:1024 sc1\n"
"buffer_load_dwordx4 v[84:87], v0, s[4:7], s97 offen offset:2048 sc1\n"
"buffer_load_dwordx4 v[64:67], v0, s[4:7], s97 offen offset:3072 sc1\n"
"buffer_load_dwordx4 v[148:151], v1, s[4:7], s97 offen sc1\n"
"buffer_load_dwordx4 v[52:55], v1, s[4:7], s97 offen offset:1024 sc1\n"
"buffer_load_dwordx4 v[108:111], v1, s[4:7], s97 offen offset:2048 sc1\n"
"buffer_load_dwordx4 v[116:119], v1, s[4:7], s97 offen offset:3072 sc1\n"
"s_waitcnt vmcnt(0)\n"
"v_max_u32_e32 v255, v140, v141\n"
"v_max3_u32 v255, v255, v142, v143\n"
"v_max3_u32 v255, v255, v124, v125\n"
"v_max3_u32 v255, v255, v126, v127\n"
"v_max3_u32 v255, v255, v36, v37\n"
"v_max3_u32 v255, v255, v38, v39\n"
"v_max3_u32 v255, v255, v56, v57\n"
"v_max3_u32 v255, v255, v58, v59\n"
"v_max3_u32 v255, v255, v132, v133\n"
"v_max3_u32 v255, v255, v134, v135\n"
"v_max3_u32 v255, v255, v112, v113\n"
"v_max3_u32 v255, v255, v114, v115\n"
"v_max3_u32 v255, v255, v156, v157\n"
"v_max3_u32 v255, v255, v158, v159\n"
"v_max3_u32 v255, v255, v128, v129\n"
"v_max3_u32 v255, v255, v130, v131\n"
"v_cmp_eq_u32_e32 vcc, -1, v255\n"
"s_cbranch_vccz .Lmk_p80k\n"
"s_sub_u32 s1, s1, 1\n"
"s_cbranch_scc0 .Lmk_p80l\n"
".Lmk_p80k:\n"
"v_mfma_f32_16x16x32_bf16 v[136:139], a[128:131], v[140:143], 0\n"
"v_mfma_f32_16x16x32_bf16 v[144:147], a[132:135], v[140:143], 0\n"
"v_mfma_f32_16x16x32_bf16 v[152:155], a[136:139], v[140:143], 0\n"
"v_mfma_f32_16x16x32_bf16 v[160:163], a[140:143], v[140:143], 0\n"
"v_mfma_f32_16x16x32_bf16 v[136:139], a[144:147], v[124:127], v[136:139]\n"
"v_mfma_f32_16x16x32_bf16 v[144:147], a[148:151], v[124:127], v[144:147]\n"
"v_mfma_f32_16x16x32_bf16 v[152:155], a[152:155], v[124:127], v[152:155]\n"
"v_mfma_f32_16x16x32_bf16 v[160:163], a[156:159], v[124:127], v[160:163]\n"
"v_mfma_f32_16x16x32_bf16 v[136:139], a[160:163], v[36:39], v[136:139]\n"
"v_mfma_f32_16x16x32_bf16 v[144:147], a[164:167], v[36:39], v[144:147]\n"
"v_mfma_f32_16x16x32_bf16 v[152:155], a[168:171], v[36:39], v[152:155]\n"
"v_mfma_f32_16x16x32_bf16 v[160:163], a[172:175], v[36:39], v[160:163]\n"
"v_mfma_f32_16x16x32_bf16 v[136:139], a[176:179], v[56:59], v[136:139]\n"
"v_mfma_f32_16x16x32_bf16 v[144:147], a[180:183], v[56:59], v[144:147]\n"
"v_mfma_f32_16x16x32_bf16 v[152:155], a[184:187], v[56:59], v[152:155]\n"
"v_mfma_f32_16x16x32_bf16 v[160:163], a[188:191], v[56:59], v[160:163]\n"
"v_mfma_f32_16x16x32_bf16 v[136:139], a[192:195], v[132:135], v[136:139]\n"
"v_mfma_f32_16x16x32_bf16 v[144:147], a[196:199], v[132:135], v[144:147]\n"
"v_mfma_f32_16x16x32_bf16 v[152:155], a[200:203], v[132:135], v[152:155]\n"
"v_mfma_f32_16x16x32_bf16 v[160:163], a[204:207], v[132:135], v[160:163]\n"
"v_mfma_f32_16x16x32_bf16 v[136:139], a[208:211], v[112:115], v[136:139]\n"
"v_mfma_f32_16x16x32_bf16 v[144:147], a[212:215], v[112:115], v[144:147]\n"
"v_mfma_f32_16x16x32_bf16 v[152:155], a[216:219], v[112:115], v[152:155]\n"
"v_mfma_f32_16x16x32_bf16 v[160:163], a[220:223], v[112:115], v[160:163]\n"
"v_mfma_f32_16x16x32_bf16 v[136:139], a[224:227], v[156:159], v[136:139]\n"
"v_mfma_f32_16x16x32_bf16 v[144:147], a[228:231], v[156:159], v[144:147]\n"
"v_mfma_f32_16x16x32_bf16 v[152:155], a[232:235], v[156:159], v[152:155]\n"
"v_mfma_f32_16x16x32_bf16 v[160:163], a[236:239], v[156:159], v[160:163]\n"
"v_mfma_f32_16x16x32_bf16 v[136:139], a[240:243], v[128:131], v[136:139]\n"
"v_mfma_f32_16x16x32_bf16 v[144:147], a[244:247], v[128:131], v[144:147]\n"
"v_mfma_f32_16x16x32_bf16 v[152:155], a[248:251], v[128:131], v[152:155]\n"
"v_mfma_f32_16x16x32_bf16 v[160:163], a[252:255], v[128:131], v[160:163]\n"
"s_add_u32 s97, s59, 0x400000\n"
"v_mov_b32_e32 v255, 0x80000000\n"
"buffer_load_dwordx4 v[140:143], v0, s[4:7], s97 offen sc1\n"
"buffer_load_dwordx4 v[124:127], v0, s[4:7], s97 offen offset:1024 sc1\n"
"buffer_store_dword v255, v255, s[4:7], 0 offen\n"
"buffer_load_dwordx4 v[36:39], v0, s[4:7], s97 offen offset:2048 sc1\n"
"buffer_load_dwordx4 v[56:59], v0, s[4:7], s97 offen offset:3072 sc1\n"
"buffer_load_dwordx4 v[132:135], v1, s[4:7], s97 offen sc1\n"
"buffer_store_dword v255, v255, s[4:7], 0 offen\n"
"buffer_load_dwordx4 v[112:115], v1, s[4:7], s97 offen offset:1024 sc1\n"
"buffer_load_dwordx4 v[156:159], v1, s[4:7], s97 offen offset:2048 sc1\n"
"buffer_load_dwordx4 v[128:131], v1, s[4:7], s97 offen offset:3072 sc1\n"
"buffer_store_dword v255, v255, s[4:7], 0 offen\n"
"buffer_store_dword v255, v255, s[4:7], 0 offen\n"
"buffer_store_dword v255, v255, s[4:7], 0 offen\n"
"buffer_store_dword v255, v255, s[4:7], 0 offen\n"
"buffer_store_dword v255, v255, s[4:7], 0 offen\n"
"buffer_store_dword v255, v255, s[4:7], 0 offen\n"
"buffer_store_dword v255, v255, s[4:7], 0 offen\n"
"buffer_store_dword v255, v255, s[4:7], 0 offen\n"
"buffer_store_dword v255, v255, s[4:7], 0 offen\n"
"s_nop 7\n"
"s_branch .Lmk_p80r\n"
".Lmk_p81s:\n"
";\n"
"s_mov_b32 s1, 0x10000\n"
".Lmk_p81l:\n"
"s_add_u32 s97, s57, 0x400000\n"
"buffer_load_dwordx4 v[140:143], v0, s[4:7], s97 offen sc1\n"
"buffer_load_dwordx4 v[124:127], v0, s[4:7], s97 offen offset:1024 sc1\n"
"buffer_load_dwordx4 v[36:39], v0, s[4:7], s97 offen offset:2048 sc1\n"
"buffer_load_dwordx4 v[56:59], v0, s[4:7], s97 offen offset:3072 sc1\n"
"buffer_load_dwordx4 v[132:135], v1, s[4:7], s97 offen sc1\n"
"buffer_load_dwordx4 v[112:115], v1, s[4:7], s97 offen offset:1024 sc1\n"
"buffer_load_dwordx4 v[156:159], v1, s[4:7], s97 offen offset:2048 sc1\n"
"buffer_load_dwordx4 v[128:131], v1, s[4:7], s97 offen offset:3072 sc1\n"
"s_add_u32 s97, s58, 0x400000\n"
"buffer_load_dwordx4 v[28:31], v0, s[4:7], s97 offen sc1\n"
"buffer_load_dwordx4 v[32:35], v0, s[4:7], s97 offen offset:1024 sc1\n"
"buffer_load_dwordx4 v[84:87], v0, s[4:7], s97 offen offset:2048 sc1\n"
"buffer_load_dwordx4 v[64:67], v0, s[4:7], s97 offen offset:3072 sc1\n"
"buffer_load_dwordx4 v[148:151], v1, s[4:7], s97 offen sc1\n"
"buffer_load_dwordx4 v[52:55], v1, s[4:7], s97 offen offset:1024 sc1\n"
"buffer_load_dwordx4 v[108:111], v1, s[4:7], s97 offen offset:2048 sc1\n"
"buffer_load_dwordx4 v[116:119], v1, s[4:7], s97 offen offset:3072 sc1\n"
"s_waitcnt vmcnt(0)\n"
"v_max_u32_e32 v244, v140, v141\n"
"v_max3_u32 v244, v244, v142, v143\n"
"v_max3_u32 v244, v244, v124, v125\n"
"v_max3_u32 v244, v244, v126, v127\n"
"v_max3_u32 v244, v244, v36, v37\n"
"v_max3_u32 v244, v244, v38, v39\n"
"v_max3_u32 v244, v244, v56, v57\n"
"v_max3_u32 v244, v244, v58, v59\n"
"v_max3_u32 v244, v244, v132, v133\n"
"v_max3_u32 v244, v244, v134, v135\n"
"v_max3_u32 v244, v244, v112, v113\n"
"v_max3_u32 v244, v244, v114, v115\n"
"v_max3_u32 v244, v244, v156, v157\n"
"v_max3_u32 v244, v244, v158, v159\n"
"v_max3_u32 v244, v244, v128, v129\n"
"v_max3_u32 v244, v244, v130, v131\n"
"v_cmp_eq_u32_e32 vcc, -1, v244\n"
"s_cbranch_vccz .Lmk_p81k\n"
"s_sub_u32 s1, s1, 1\n"
"s_cbranch_scc0 .Lmk_p81l\n"
".Lmk_p81k:\n"
"v_mfma_f32_16x16x32_bf16 v[60:63], a[128:131], v[140:143], 0\n"
"v_mfma_f32_16x16x32_bf16 v[72:75], a[132:135], v[140:143], 0\n"
"v_mfma_f32_16x16x32_bf16 v[76:79], a[136:139], v[140:143], 0\n"
"v_mfma_f32_16x16x32_bf16 v[120:123], a[140:143], v[140:143], 0\n"
"v_mfma_f32_16x16x32_bf16 v[60:63], a[144:147], v[124:127], v[60:63]\n"
"v_mfma_f32_16x16x32_bf16 v[72:75], a[148:151], v[124:127], v[72:75]\n"
"v_mfma_f32_16x16x32_bf16 v[76:79], a[152:155], v[124:127], v[76:79]\n"
"v_mfma_f32_16x16x32_bf16 v[120:123], a[156:159], v[124:127], v[120:123]\n"
"v_mfma_f32_16x16x32_bf16 v[60:63], a[160:163], v[36:39], v[60:63]\n"
"v_mfma_f32_16x16x32_bf16 v[72:75], a[164:167], v[36:39], v[72:75]\n"
"v_mfma_f32_16x16x32_bf16 v[76:79], a[168:171], v[36:39], v[76:79]\n"
"v_mfma_f32_16x16x32_bf16 v[120:123], a[172:175], v[36:39], v[120:123]\n"
"v_mfma_f32_16x16x32_bf16 v[60:63], a[176:179], v[56:59], v[60:63]\n"
"v_mfma_f32_16x16x32_bf16 v[72:75], a[180:183], v[56:59], v[72:75]\n"
"v_mfma_f32_16x16x32_bf16 v[76:79], a[184:187], v[56:59], v[76:79]\n"
"v_mfma_f32_16x16x32_bf16 v[120:123], a[188:191], v[56:59], v[120:123]\n"
"v_mfma_f32_16x16x32_bf16 v[60:63], a[192:195], v[132:135], v[60:63]\n"
"v_mfma_f32_16x16x32_bf16 v[72:75], a[196:199], v[132:135], v[72:75]\n"
"v_mfma_f32_16x16x32_bf16 v[76:79], a[200:203], v[132:135], v[76:79]\n"
"v_mfma_f32_16x16x32_bf16 v[120:123], a[204:207], v[132:135], v[120:123]\n"
"v_mfma_f32_16x16x32_bf16 v[60:63], a[208:211], v[112:115], v[60:63]\n"
"v_mfma_f32_16x16x32_bf16 v[72:75], a[212:215], v[112:115], v[72:75]\n"
"v_mfma_f32_16x16x32_bf16 v[76:79], a[216:219], v[112:115], v[76:79]\n"
"v_mfma_f32_16x16x32_bf16 v[120:123], a[220:223], v[112:115], v[120:123]\n"
"v_mfma_f32_16x16x32_bf16 v[60:63], a[224:227], v[156:159], v[60:63]\n"
"v_mfma_f32_16x16x32_bf16 v[72:75], a[228:231], v[156:159], v[72:75]\n"
"v_mfma_f32_16x16x32_bf16 v[76:79], a[232:235], v[156:159], v[76:79]\n"
"v_mfma_f32_16x16x32_bf16 v[120:123], a[236:239], v[156:159], v[120:123]\n"
"v_mfma_f32_16x16x32_bf16 v[60:63], a[240:243], v[128:131], v[60:63]\n"
"v_mfma_f32_16x16x32_bf16 v[72:75], a[244:247], v[128:131], v[72:75]\n"
"v_mfma_f32_16x16x32_bf16 v[76:79], a[248:251], v[128:131], v[76:79]\n"
"v_mfma_f32_16x16x32_bf16 v[120:123], a[252:255], v[128:131], v[120:123]\n"
"s_add_u32 s97, s59, 0x400000\n"
"v_mov_b32_e32 v244, 0x80000000\n"
"buffer_load_dwordx4 v[140:143], v0, s[4:7], s97 offen sc1\n"
"buffer_load_dwordx4 v[124:127], v0, s[4:7], s97 offen offset:1024 sc1\n"
"buffer_store_dword v244, v244, s[4:7], 0 offen\n"
"buffer_load_dwordx4 v[36:39], v0, s[4:7], s97 offen offset:2048 sc1\n"
"buffer_load_dwordx4 v[56:59], v0, s[4:7], s97 offen offset:3072 sc1\n"
"buffer_load_dwordx4 v[132:135], v1, s[4:7], s97 offen sc1\n"
"buffer_store_dword v244, v244, s[4:7], 0 offen\n"
"buffer_load_dwordx4 v[112:115], v1, s[4:7], s97 offen offset:1024 sc1\n"
"buffer_load_dwordx4 v[156:159], v1, s[4:7], s97 offen offset:2048 sc1\n"
"buffer_load_dwordx4 v[128:131], v1, s[4:7], s97 offen offset:3072 sc1\n"
"buffer_store_dword v244, v244, s[4:7], 0 offen\n"
"buffer_store_dword v244, v244, s[4:7], 0 offen\n"
"buffer_store_dword v244, v244, s[4:7], 0 offen\n"
"buffer_store_dword v244, v244, s[4:7], 0 offen\n"
"buffer_store_dword v244, v244, s[4:7], 0 offen\n"
"buffer_store_dword v244, v244, s[4:7], 0 offen\n"
"buffer_store_dword v244, v244, s[4:7], 0 offen\n"
"buffer_store_dword v244, v244, s[4:7], 0 offen\n"
"buffer_store_dword v244, v244, s[4:7], 0 offen\n"
"buffer_store_dword v244, v244, s[4:7], 0 offen\n"
"buffer_store_dword v244, v244, s[4:7], 0 offen\n"
"buffer_store_dword v244, v244, s[4:7], 0 offen\n"
"buffer_store_dword v244, v244, s[4:7], 0 offen\n"
"buffer_store_dword v244, v244, s[4:7], 0 offen\n"
"buffer_store_dword v244, v244, s[4:7], 0 offen\n"
"buffer_store_dword v244, v244, s[4:7], 0 offen\n"
"buffer_store_dword v244, v244, s[4:7], 0 offen\n"
"buffer_store_dword v244, v244, s[4:7], 0 offen\n"
"s_nop 7\n"
"s_branch .Lmk_p81r\n"
".Lmk_q83s:\n"
";\n"
"s_mov_b32 s1, 0x10000\n"
".Lmk_q83l:\n"
"s_add_u32 s97, s59, 0x400000\n"
"buffer_load_dwordx4 v[140:143], v0, s[4:7], s97 offen sc1\n"
"buffer_load_dwordx4 v[124:127], v0, s[4:7], s97 offen offset:1024 sc1\n"
"buffer_load_dwordx4 v[36:39], v0, s[4:7], s97 offen offset:2048 sc1\n"
"buffer_load_dwordx4 v[56:59], v0, s[4:7], s97 offen offset:3072 sc1\n"
"buffer_load_dwordx4 v[132:135], v1, s[4:7], s97 offen sc1\n"
"buffer_load_dwordx4 v[112:115], v1, s[4:7], s97 offen offset:1024 sc1\n"
"buffer_load_dwordx4 v[156:159], v1, s[4:7], s97 offen offset:2048 sc1\n"
"buffer_load_dwordx4 v[128:131], v1, s[4:7], s97 offen offset:3072 sc1\n"
"s_add_u32 s97, s52, 0x840000\n"
"buffer_load_dwordx4 v[72:75], v0, s[8:11], s97 offen sc1\n"
"s_waitcnt vmcnt(0)\n"
"v_max_u32_e32 v255, v140, v141\n"
"v_max3_u32 v255, v255, v142, v143\n"
"v_max3_u32 v255, v255, v124, v125\n"
"v_max3_u32 v255, v255, v126, v127\n"
"v_max3_u32 v255, v255, v36, v37\n"
"v_max3_u32 v255, v255, v38, v39\n"
"v_max3_u32 v255, v255, v56, v57\n"
"v_max3_u32 v255, v255, v58, v59\n"
"v_cmp_eq_u32_e32 vcc, -1, v255\n"
"s_cbranch_vccz .Lmk_q83k\n"
"s_sub_u32 s1, s1, 1\n"
"s_cbranch_scc0 .Lmk_q83l\n"
".Lmk_q83k:\n"
"s_branch .Lmk_q83r\n"
".Lmk_p82s:\n"
";\n"
"s_mov_b32 s1, 0x10000\n"
".Lmk_p82l:\n"
"s_add_u32 s97, s58, 0x400000\n"
"buffer_load_dwordx4 v[52:55], v0, s[4:7], s97 offen sc1\n"
"buffer_load_dwordx4 v[76:79], v0, s[4:7], s97 offen offset:1024 sc1\n"
"buffer_load_dwordx4 v[84:87], v0, s[4:7], s97 offen offset:2048 sc1\n"
"buffer_load_dwordx4 v[108:111], v0, s[4:7], s97 offen offset:3072 sc1\n"
"buffer_load_dwordx4 v[116:119], v1, s[4:7], s97 offen sc1\n"
"buffer_load_dwordx4 v[120:123], v1, s[4:7], s97 offen offset:1024 sc1\n"
"buffer_load_dwordx4 v[144:147], v1, s[4:7], s97 offen offset:2048 sc1\n"
"buffer_load_dwordx4 v[148:151], v1, s[4:7], s97 offen offset:3072 sc1\n"
"s_add_u32 s97, s59, 0x400000\n"
"buffer_load_dwordx4 v[140:143], v0, s[4:7], s97 offen sc1\n"
"buffer_load_dwordx4 v[124:127], v0, s[4:7], s97 offen offset:1024 sc1\n"
"buffer_load_dwordx4 v[36:39], v0, s[4:7], s97 offen offset:2048 sc1\n"
"buffer_load_dwordx4 v[56:59], v0, s[4:7], s97 offen offset:3072 sc1\n"
"buffer_load_dwordx4 v[132:135], v1, s[4:7], s97 offen sc1\n"
"buffer_load_dwordx4 v[112:115], v1, s[4:7], s97 offen offset:1024 sc1\n"
"buffer_load_dwordx4 v[156:159], v1, s[4:7], s97 offen offset:2048 sc1\n"
"buffer_load_dwordx4 v[128:131], v1, s[4:7], s97 offen offset:3072 sc1\n"
"s_add_u32 s97, s52, 0x840000\n"
"buffer_load_dwordx4 v[72:75], v0, s[8:11], s97 offen sc1\n"
"s_waitcnt vmcnt(0)\n"
"v_max_u32_e32 v255, v52, v53\n"
"v_max3_u32 v255, v255, v54, v55\n"
"v_max3_u32 v255, v255, v76, v77\n"
"v_max3_u32 v255, v255, v78, v79\n"
"v_max3_u32 v255, v255, v84, v85\n"
"v_max3_u32 v255, v255, v86, v87\n"
"v_max3_u32 v255, v255, v108, v109\n"
"v_max3_u32 v255, v255, v110, v111\n"
"v_max3_u32 v255, v255, v116, v117\n"
"v_max3_u32 v255, v255, v118, v119\n"
"v_max3_u32 v255, v255, v120, v121\n"
"v_max3_u32 v255, v255, v122, v123\n"
"v_max3_u32 v255, v255, v144, v145\n"
"v_max3_u32 v255, v255, v146, v147\n"
"v_max3_u32 v255, v255, v148, v149\n"
"v_max3_u32 v255, v255, v150, v151\n"
"v_cmp_eq_u32_e32 vcc, -1, v255\n"
"s_cbranch_vccz .Lmk_p82k\n"
"s_sub_u32 s1, s1, 1\n"
"s_cbranch_scc0 .Lmk_p82l\n"
".Lmk_p82k:\n"
"v_mfma_f32_16x16x32_bf16 v[24:27], a[128:131], v[52:55], 0\n"
"v_mfma_f32_16x16x32_bf16 v[172:175], a[132:135], v[52:55], 0\n"
"v_mfma_f32_16x16x32_bf16 v[176:179], a[136:139], v[52:55], 0\n"
"v_mfma_f32_16x16x32_bf16 v[184:187], a[140:143], v[52:55], 0\n"
"v_mfma_f32_16x16x32_bf16 v[24:27], a[144:147], v[76:79], v[24:27]\n"
"v_mfma_f32_16x16x32_bf16 v[172:175], a[148:151], v[76:79], v[172:175]\n"
"v_mfma_f32_16x16x32_bf16 v[176:179], a[152:155], v[76:79], v[176:179]\n"
"v_mfma_f32_16x16x32_bf16 v[184:187], a[156:159], v[76:79], v[184:187]\n"
"v_mfma_f32_16x16x32_bf16 v[24:27], a[160:163], v[84:87], v[24:27]\n"
"v_mfma_f32_16x16x32_bf16 v[172:175], a[164:167], v[84:87], v[172:175]\n"
"v_mfma_f32_16x16x32_bf16 v[176:179], a[168:171], v[84:87], v[176:179]\n"
"v_mfma_f32_16x16x32_bf16 v[184:187], a[172:175], v[84:87], v[184:187]\n"
"v_mfma_f32_16x16x32_bf16 v[24:27], a[176:179], v[108:111], v[24:27]\n"
"v_mfma_f32_16x16x32_bf16 v[172:175], a[180:183], v[108:111], v[172:175]\n"
"v_mfma_f32_16x16x32_bf16 v[176:179], a[184:187], v[108:111], v[176:179]\n"
"v_mfma_f32_16x16x32_bf16 v[184:187], a[188:191], v[108:111], v[184:187]\n"
"v_mfma_f32_16x16x32_bf16 v[24:27], a[192:195], v[116:119], v[24:27]\n"
"v_mfma_f32_16x16x32_bf16 v[172:175], a[196:199], v[116:119], v[172:175]\n"
"v_mfma_f32_16x16x32_bf16 v[176:179], a[200:203], v[116:119], v[176:179]\n"
"v_mfma_f32_16x16x32_bf16 v[184:187], a[204:207], v[116:119], v[184:187]\n"
"v_mfma_f32_16x16x32_bf16 v[24:27], a[208:211], v[120:123], v[24:27]\n"
"v_mfma_f32_16x16x32_bf16 v[172:175], a[212:215], v[120:123], v[172:175]\n"
"v_mfma_f32_16x16x32_bf16 v[176:179], a[216:219], v[120:123], v[176:179]\n"
"v_mfma_f32_16x16x32_bf16 v[184:187], a[220:223], v[120:123], v[184:187]\n"
"v_mfma_f32_16x16x32_bf16 v[24:27], a[224:227], v[144:147], v[24:27]\n"
"v_mfma_f32_16x16x32_bf16 v[172:175], a[228:231], v[144:147], v[172:175]\n"
"v_mfma_f32_16x16x32_bf16 v[176:179], a[232:235], v[144:147], v[176:179]\n"
"v_mfma_f32_16x16x32_bf16 v[184:187], a[236:239], v[144:147], v[184:187]\n"
"v_mfma_f32_16x16x32_bf16 v[24:27], a[240:243], v[148:151], v[24:27]\n"
"v_mfma_f32_16x16x32_bf16 v[172:175], a[244:247], v[148:151], v[172:175]\n"
"v_mfma_f32_16x16x32_bf16 v[176:179], a[248:251], v[148:151], v[176:179]\n"
"v_mfma_f32_16x16x32_bf16 v[184:187], a[252:255], v[148:151], v[184:187]\n"
"s_nop 7\n"
"s_branch .Lmk_p82r\n"
".Lmk_q84s:\n"
";\n"
"s_mov_b32 s1, 0x10000\n"
".Lmk_q84l:\n"
"s_add_u32 s97, s59, 0x400000\n"
"buffer_load_dwordx4 v[132:135], v1, s[4:7], s97 offen sc1\n"
"buffer_load_dwordx4 v[112:115], v1, s[4:7], s97 offen offset:1024 sc1\n"
"buffer_load_dwordx4 v[156:159], v1, s[4:7], s97 offen offset:2048 sc1\n"
"buffer_load_dwordx4 v[128:131], v1, s[4:7], s97 offen offset:3072 sc1\n"
"s_add_u32 s97, s52, 0x840000\n"
"buffer_load_dwordx4 v[72:75], v0, s[8:11], s97 offen sc1\n"
"buffer_load_dwordx4 v[52:55], v0, s[8:11], s97 offen offset:1024 sc1\n"
"buffer_load_dwordx4 v[24:27], v0, s[8:11], s97 offen offset:2048 sc1\n"
"s_waitcnt vmcnt(0)\n"
"v_max_u32_e32 v255, v132, v133\n"
"v_max3_u32 v255, v255, v134, v135\n"
"v_max3_u32 v255, v255, v112, v113\n"
"v_max3_u32 v255, v255, v114, v115\n"
"v_max3_u32 v255, v255, v156, v157\n"
"v_max3_u32 v255, v255, v158, v159\n"
"v_max3_u32 v255, v255, v128, v129\n"
"v_max3_u32 v255, v255, v130, v131\n"
"v_cmp_eq_u32_e32 vcc, -1, v255\n"
"s_cbranch_vccz .Lmk_q84k\n"
"s_sub_u32 s1, s1, 1\n"
"s_cbranch_scc0 .Lmk_q84l\n"
".Lmk_q84k:\n"
"s_branch .Lmk_q84r\n"
".Lmk_p89s:\n"
";\n"
"s_mov_b32 s1, 0x10000\n"
".Lmk_p89l:\n"
"s_add_u32 s98, s52, 0x40000\n"
"buffer_load_dwordx4 v[72:75], v0, s[4:7], s98 offen sc1\n"
"buffer_load_dwordx4 v[212:215], v0, s[4:7], s98 offen offset:1024 sc1\n"
"buffer_load_dwordx4 v[84:87], v0, s[4:7], s98 offen offset:2048 sc1\n"
"buffer_load_dwordx4 v[24:27], v0, s[4:7], s98 offen offset:3072 sc1\n"
"buffer_load_dwordx4 v[100:103], v1, s[4:7], s98 offen sc1\n"
"buffer_load_dwordx4 v[116:119], v1, s[4:7], s98 offen offset:1024 sc1\n"
"buffer_load_dwordx4 v[36:39], v1, s[4:7], s98 offen offset:2048 sc1\n"
"buffer_load_dwordx4 v[52:55], v1, s[4:7], s98 offen offset:3072 sc1\n"
"s_add_u32 s98, s53, 0x40000\n"
"buffer_load_dwordx4 v[132:135], v0, s[4:7], s98 offen sc1\n"
"buffer_load_dwordx4 v[148:151], v0, s[4:7], s98 offen offset:1024 sc1\n"
"buffer_load_dwordx4 v[172:175], v0, s[4:7], s98 offen offset:2048 sc1\n"
"buffer_load_dwordx4 v[112:115], v0, s[4:7], s98 offen offset:3072 sc1\n"
"buffer_load_dwordx4 v[124:127], v1, s[4:7], s98 offen sc1\n"
"buffer_load_dwordx4 v[144:147], v1, s[4:7], s98 offen offset:1024 sc1\n"
"buffer_load_dwordx4 v[156:159], v1, s[4:7], s98 offen offset:2048 sc1\n"
"buffer_load_dwordx4 v[176:179], v1, s[4:7], s98 offen offset:3072 sc1\n"
"s_waitcnt vmcnt(0)\n"
"v_max_u32_e32 v247, v72, v73\n"
"v_max3_u32 v247, v247, v74, v75\n"
"v_max3_u32 v247, v247, v212, v213\n"
"v_max3_u32 v247, v247, v214, v215\n"
"v_max3_u32 v247, v247, v84, v85\n"
"v_max3_u32 v247, v247, v86, v87\n"
"v_max3_u32 v247, v247, v24, v25\n"
"v_max3_u32 v247, v247, v26, v27\n"
"v_max3_u32 v247, v247, v100, v101\n"
"v_max3_u32 v247, v247, v102, v103\n"
"v_max3_u32 v247, v247, v116, v117\n"
"v_max3_u32 v247, v247, v118, v119\n"
"v_max3_u32 v247, v247, v36, v37\n"
"v_max3_u32 v247, v247, v38, v39\n"
"v_max3_u32 v247, v247, v52, v53\n"
"v_max3_u32 v247, v247, v54, v55\n"
"v_cmp_eq_u32_e32 vcc, -1, v247\n"
"s_cbranch_vccz .Lmk_p89k\n"
"s_sub_u32 s1, s1, 1\n"
"s_cbranch_scc0 .Lmk_p89l\n"
".Lmk_p89k:\n"
"v_mfma_f32_16x16x32_bf16 v[184:187], a[0:3], v[72:75], 0\n"
"v_mfma_f32_16x16x32_bf16 v[188:191], a[4:7], v[72:75], 0\n"
"v_mfma_f32_16x16x32_bf16 v[192:195], a[8:11], v[72:75], 0\n"
"v_mfma_f32_16x16x32_bf16 v[196:199], a[12:15], v[72:75], 0\n"
"v_mfma_f32_16x16x32_bf16 v[200:203], a[16:19], v[72:75], 0\n"
"v_mfma_f32_16x16x32_bf16 v[208:211], a[20:23], v[72:75], 0\n"
"v_mfma_f32_16x16x32_bf16 v[184:187], a[24:27], v[212:215], v[184:187]\n"
"v_mfma_f32_16x16x32_bf16 v[188:191], a[28:31], v[212:215], v[188:191]\n"
"v_mfma_f32_16x16x32_bf16 v[192:195], a[32:35], v[212:215], v[192:195]\n"
"v_mfma_f32_16x16x32_bf16 v[196:199], a[36:39], v[212:215], v[196:199]\n"
"v_mfma_f32_16x16x32_bf16 v[200:203], a[40:43], v[212:215], v[200:203]\n"
"v_mfma_f32_16x16x32_bf16 v[208:211], a[44:47], v[212:215], v[208:211]\n"
"v_mfma_f32_16x16x32_bf16 v[184:187], a[48:51], v[84:87], v[184:187]\n"
"v_mfma_f32_16x16x32_bf16 v[188:191], a[52:55], v[84:87], v[188:191]\n"
"v_mfma_f32_16x16x32_bf16 v[192:195], a[56:59], v[84:87], v[192:195]\n"
"v_mfma_f32_16x16x32_bf16 v[196:199], a[60:63], v[84:87], v[196:199]\n"
"v_mfma_f32_16x16x32_bf16 v[200:203], a[64:67], v[84:87], v[200:203]\n"
"v_mfma_f32_16x16x32_bf16 v[208:211], a[68:71], v[84:87], v[208:211]\n"
"v_mfma_f32_16x16x32_bf16 v[184:187], a[72:75], v[24:27], v[184:187]\n"
"v_mfma_f32_16x16x32_bf16 v[188:191], a[76:79], v[24:27], v[188:191]\n"
"v_mfma_f32_16x16x32_bf16 v[192:195], a[80:83], v[24:27], v[192:195]\n"
"v_mfma_f32_16x16x32_bf16 v[196:199], a[84:87], v[24:27], v[196:199]\n"
"v_mfma_f32_16x16x32_bf16 v[200:203], a[88:91], v[24:27], v[200:203]\n"
"v_mfma_f32_16x16x32_bf16 v[208:211], a[92:95], v[24:27], v[208:211]\n"
"v_mfma_f32_16x16x32_bf16 v[184:187], a[96:99], v[100:103], v[184:187]\n"
"v_mfma_f32_16x16x32_bf16 v[188:191], a[100:103], v[100:103], v[188:191]\n"
"v_mfma_f32_16x16x32_bf16 v[192:195], a[104:107], v[100:103], v[192:195]\n"
"v_mfma_f32_16x16x32_bf16 v[196:199], a[108:111], v[100:103], v[196:199]\n"
"v_mfma_f32_16x16x32_bf16 v[200:203], a[112:115], v[100:103], v[200:203]\n"
"v_mfma_f32_16x16x32_bf16 v[208:211], a[116:119], v[100:103], v[208:211]\n"
"v_mfma_f32_16x16x32_bf16 v[184:187], a[120:123], v[116:119], v[184:187]\n"
"v_mfma_f32_16x16x32_bf16 v[188:191], a[124:127], v[116:119], v[188:191]\n"
"v_mfma_f32_16x16x32_bf16 v[192:195], v[40:43], v[116:119], v[192:195]\n"
"v_mfma_f32_16x16x32_bf16 v[196:199], v[44:47], v[116:119], v[196:199]\n"
"v_mfma_f32_16x16x32_bf16 v[200:203], v[80:83], v[116:119], v[200:203]\n"
"v_mfma_f32_16x16x32_bf16 v[208:211], v[68:71], v[116:119], v[208:211]\n"
"v_mfma_f32_16x16x32_bf16 v[184:187], v[88:91], v[36:39], v[184:187]\n"
"v_mfma_f32_16x16x32_bf16 v[188:191], v[48:51], v[36:39], v[188:191]\n"
"v_mfma_f32_16x16x32_bf16 v[192:195], v[92:95], v[36:39], v[192:195]\n"
"v_mfma_f32_16x16x32_bf16 v[196:199], v[96:99], v[36:39], v[196:199]\n"
"v_mfma_f32_16x16x32_bf16 v[200:203], v[168:171], v[36:39], v[200:203]\n"
"v_mfma_f32_16x16x32_bf16 v[208:211], v[104:107], v[36:39], v[208:211]\n"
"v_mfma_f32_16x16x32_bf16 v[184:187], v[136:139], v[52:55], v[184:187]\n"
"v_mfma_f32_16x16x32_bf16 v[188:191], v[180:183], v[52:55], v[188:191]\n"
"v_mfma_f32_16x16x32_bf16 v[192:195], v[28:31], v[52:55], v[192:195]\n"
"v_mfma_f32_16x16x32_bf16 v[196:199], v[32:35], v[52:55], v[196:199]\n"
"v_mfma_f32_16x16x32_bf16 v[200:203], v[60:63], v[52:55], v[200:203]\n"
"v_mfma_f32_16x16x32_bf16 v[208:211], v[64:67], v[52:55], v[208:211]\n"
"s_add_u32 s98, s54, 0x40000\n"
"v_mov_b32_e32 v247, 0x80000000\n"
"buffer_load_dwordx4 v[72:75], v0, s[4:7], s98 offen sc1\n"
"buffer_store_dword v247, v247, s[4:7], 0 offen\n"
"buffer_load_dwordx4 v[212:215], v0, s[4:7], s98 offen offset:1024 sc1\n"
"buffer_load_dwordx4 v[84:87], v0, s[4:7], s98 offen offset:2048 sc1\n"
"buffer_store_dword v247, v247, s[4:7], 0 offen\n"
"buffer_load_dwordx4 v[24:27], v0, s[4:7], s98 offen offset:3072 sc1\n"
"buffer_load_dwordx4 v[100:103], v1, s[4:7], s98 offen sc1\n"
"buffer_store_dword v247, v247, s[4:7], 0 offen\n"
"buffer_load_dwordx4 v[116:119], v1, s[4:7], s98 offen offset:1024 sc1\n"
"buffer_store_dword v247, v247, s[4:7], 0 offen\n"
"buffer_load_dwordx4 v[36:39], v1, s[4:7], s98 offen offset:2048 sc1\n"
"buffer_store_dword v247, v247, s[4:7], 0 offen\n"
"buffer_load_dwordx4 v[52:55], v1, s[4:7], s98 offen offset:3072 sc1\n"
"s_nop 7\n"
"s_branch .Lmk_p89r\n"
".Lmk_m94s:\n"
";\n"
"s_mov_b32 s1, 0x10000\n"
".Lmk_m94l:\n"
"s_add_u32 s97, s62, 0x4000\n"
"buffer_load_dwordx2 v[254:255], v10, s[4:7], s97 offen sc1\n"
"buffer_load_dwordx2 v[252:253], v10, s[4:7], s97 offen offset:128 sc1\n"
"buffer_load_dwordx2 v[250:251], v10, s[4:7], s97 offen offset:256 sc1\n"
"buffer_load_dwordx2 v[248:249], v10, s[4:7], s97 offen offset:384 sc1\n"
"s_waitcnt vmcnt(0)\n"
"v_max_u32_e32 v247, v254, v255\n"
"v_max3_u32 v247, v247, v252, v253\n"
"v_max3_u32 v247, v247, v250, v251\n"
"v_max3_u32 v247, v247, v248, v249\n"
"v_cmp_eq_u32_e32 vcc, -1, v247\n"
"s_cbranch_vccz .Lmk_m94k\n"
"s_sub_u32 s1, s1, 1\n"
"s_cbranch_scc0 .Lmk_m94l\n"
".Lmk_m94k:\n"
"v_add_f32_e32 v254, v254, v252\n"
"v_add_f32_e32 v255, v255, v253\n"
"v_add_f32_e32 v250, v250, v248\n"
"v_add_f32_e32 v251, v251, v249\n"
"v_add_f32_e32 v254, v254, v250\n"
"v_add_f32_e32 v255, v255, v251\n"
"s_nop 1\n"
"s_branch .Lmk_m94r\n"
".Lmk_p90s:\n"
";\n"
"s_mov_b32 s1, 0x10000\n"
".Lmk_p90l:\n"
"s_add_u32 s98, s53, 0x40000\n"
"buffer_load_dwordx4 v[132:135], v0, s[4:7], s98 offen sc1\n"
"buffer_load_dwordx4 v[160:163], v0, s[4:7], s98 offen offset:1024 sc1\n"
"buffer_load_dwordx4 v[148:151], v0, s[4:7], s98 offen offset:2048 sc1\n"
"buffer_load_dwordx4 v[164:167], v0, s[4:7], s98 offen offset:3072 sc1\n"
"buffer_load_dwordx4 v[112:115], v1, s[4:7], s98 offen sc1\n"
"buffer_load_dwordx4 v[124:127], v1, s[4:7], s98 offen offset:1024 sc1\n"
"buffer_load_dwordx4 v[200:203], v1, s[4:7], s98 offen offset:2048 sc1\n"
"buffer_load_dwordx4 v[156:159], v1, s[4:7], s98 offen offset:3072 sc1\n"
"s_add_u32 s98, s54, 0x40000\n"
"buffer_load_dwordx4 v[72:75], v0, s[4:7], s98 offen sc1\n"
"buffer_load_dwordx4 v[212:215], v0, s[4:7], s98 offen offset:1024 sc1\n"
"buffer_load_dwordx4 v[84:87], v0, s[4:7], s98 offen offset:2048 sc1\n"
"buffer_load_dwordx4 v[24:27], v0, s[4:7], s98 offen offset:3072 sc1\n"
"buffer_load_dwordx4 v[100:103], v1, s[4:7], s98 offen sc1\n"
"buffer_load_dwordx4 v[116:119], v1, s[4:7], s98 offen offset:1024 sc1\n"
"buffer_load_dwordx4 v[36:39], v1, s[4:7], s98 offen offset:2048 sc1\n"
"buffer_load_dwordx4 v[52:55], v1, s[4:7], s98 offen offset:3072 sc1\n"
"s_waitcnt vmcnt(0)\n"
"v_max_u32_e32 v252, v132, v133\n"
"v_max3_u32 v252, v252, v134, v135\n"
"v_max3_u32 v252, v252, v160, v161\n"
"v_max3_u32 v252, v252, v162, v163\n"
"v_max3_u32 v252, v252, v148, v149\n"
"v_max3_u32 v252, v252, v150, v151\n"
"v_max3_u32 v252, v252, v164, v165\n"
"v_max3_u32 v252, v252, v166, v167\n"
"v_max3_u32 v252, v252, v112, v113\n"
"v_max3_u32 v252, v252, v114, v115\n"
"v_max3_u32 v252, v252, v124, v125\n"
"v_max3_u32 v252, v252, v126, v127\n"
"v_max3_u32 v252, v252, v200, v201\n"
"v_max3_u32 v252, v252, v202, v203\n"
"v_max3_u32 v252, v252, v156, v157\n"
"v_max3_u32 v252, v252, v158, v159\n"
"v_cmp_eq_u32_e32 vcc, -1, v252\n"
"s_cbranch_vccz .Lmk_p90k\n"
"s_sub_u32 s1, s1, 1\n"
"s_cbranch_scc0 .Lmk_p90l\n"
".Lmk_p90k:\n"
"v_mfma_f32_16x16x32_bf16 v[76:79], a[0:3], v[132:135], 0\n"
"v_mfma_f32_16x16x32_bf16 v[108:111], a[4:7], v[132:135], 0\n"
"v_mfma_f32_16x16x32_bf16 v[120:123], a[8:11], v[132:135], 0\n"
"v_mfma_f32_16x16x32_bf16 v[128:131], a[12:15], v[132:135], 0\n"
"v_mfma_f32_16x16x32_bf16 v[140:143], a[16:19], v[132:135], 0\n"
"v_mfma_f32_16x16x32_bf16 v[152:155], a[20:23], v[132:135], 0\n"
"v_mfma_f32_16x16x32_bf16 v[76:79], a[24:27], v[160:163], v[76:79]\n"
"v_mfma_f32_16x16x32_bf16 v[108:111], a[28:31], v[160:163], v[108:111]\n"
"v_mfma_f32_16x16x32_bf16 v[120:123], a[32:35], v[160:163], v[120:123]\n"
"v_mfma_f32_16x16x32_bf16 v[128:131], a[36:39], v[160:163], v[128:131]\n"
"v_mfma_f32_16x16x32_bf16 v[140:143], a[40:43], v[160:163], v[140:143]\n"
"v_mfma_f32_16x16x32_bf16 v[152:155], a[44:47], v[160:163], v[152:155]\n"
"v_mfma_f32_16x16x32_bf16 v[76:79], a[48:51], v[148:151], v[76:79]\n"
"v_mfma_f32_16x16x32_bf16 v[108:111], a[52:55], v[148:151], v[108:111]\n"
"v_mfma_f32_16x16x32_bf16 v[120:123], a[56:59], v[148:151], v[120:123]\n"
"v_mfma_f32_16x16x32_bf16 v[128:131], a[60:63], v[148:151], v[128:131]\n"
"v_mfma_f32_16x16x32_bf16 v[140:143], a[64:67], v[148:151], v[140:143]\n"
"v_mfma_f32_16x16x32_bf16 v[152:155], a[68:71], v[148:151], v[152:155]\n"
"v_mfma_f32_16x16x32_bf16 v[76:79], a[72:75], v[164:167], v[76:79]\n"
"v_mfma_f32_16x16x32_bf16 v[108:111], a[76:79], v[164:167], v[108:111]\n"
"v_mfma_f32_16x16x32_bf16 v[120:123], a[80:83], v[164:167], v[120:123]\n"
"v_mfma_f32_16x16x32_bf16 v[128:131], a[84:87], v[164:167], v[128:131]\n"
"v_mfma_f32_16x16x32_bf16 v[140:143], a[88:91], v[164:167], v[140:143]\n"
"v_mfma_f32_16x16x32_bf16 v[152:155], a[92:95], v[164:167], v[152:155]\n"
"v_mfma_f32_16x16x32_bf16 v[76:79], a[96:99], v[112:115], v[76:79]\n"
"v_mfma_f32_16x16x32_bf16 v[108:111], a[100:103], v[112:115], v[108:111]\n"
"v_mfma_f32_16x16x32_bf16 v[120:123], a[104:107], v[112:115], v[120:123]\n"
"v_mfma_f32_16x16x32_bf16 v[128:131], a[108:111], v[112:115], v[128:131]\n"
"v_mfma_f32_16x16x32_bf16 v[140:143], a[112:115], v[112:115], v[140:143]\n"
"v_mfma_f32_16x16x32_bf16 v[152:155], a[116:119], v[112:115], v[152:155]\n"
"v_mfma_f32_16x16x32_bf16 v[76:79], a[120:123], v[124:127], v[76:79]\n"
"v_mfma_f32_16x16x32_bf16 v[108:111], a[124:127], v[124:127], v[108:111]\n"
"v_mfma_f32_16x16x32_bf16 v[120:123], v[40:43], v[124:127], v[120:123]\n"
"v_mfma_f32_16x16x32_bf16 v[128:131], v[44:47], v[124:127], v[128:131]\n"
"v_mfma_f32_16x16x32_bf16 v[140:143], v[80:83], v[124:127], v[140:143]\n"
"v_mfma_f32_16x16x32_bf16 v[152:155], v[68:71], v[124:127], v[152:155]\n"
"v_mfma_f32_16x16x32_bf16 v[76:79], v[88:91], v[200:203], v[76:79]\n"
"v_mfma_f32_16x16x32_bf16 v[108:111], v[48:51], v[200:203], v[108:111]\n"
"v_mfma_f32_16x16x32_bf16 v[120:123], v[92:95], v[200:203], v[120:123]\n"
"v_mfma_f32_16x16x32_bf16 v[128:131], v[96:99], v[200:203], v[128:131]\n"
"v_mfma_f32_16x16x32_bf16 v[140:143], v[168:171], v[200:203], v[140:143]\n"
"v_mfma_f32_16x16x32_bf16 v[152:155], v[104:107], v[200:203], v[152:155]\n"
"v_mfma_f32_16x16x32_bf16 v[76:79], v[136:139], v[156:159], v[76:79]\n"
"v_mfma_f32_16x16x32_bf16 v[108:111], v[180:183], v[156:159], v[108:111]\n"
"v_mfma_f32_16x16x32_bf16 v[120:123], v[28:31], v[156:159], v[120:123]\n"
"v_mfma_f32_16x16x32_bf16 v[128:131], v[32:35], v[156:159], v[128:131]\n"
"v_mfma_f32_16x16x32_bf16 v[140:143], v[60:63], v[156:159], v[140:143]\n"
"v_mfma_f32_16x16x32_bf16 v[152:155], v[64:67], v[156:159], v[152:155]\n"
"s_add_u32 s98, s55, 0x40000\n"
"v_mov_b32_e32 v252, 0x80000000\n"
"buffer_load_dwordx4 v[132:135], v0, s[4:7], s98 offen sc1\n"
"buffer_load_dwordx4 v[160:163], v0, s[4:7], s98 offen offset:1024 sc1\n"
"buffer_load_dwordx4 v[148:151], v0, s[4:7], s98 offen offset:2048 sc1\n"
"buffer_load_dwordx4 v[164:167], v0, s[4:7], s98 offen offset:3072 sc1\n"
"buffer_load_dwordx4 v[112:115], v1, s[4:7], s98 offen sc1\n"
"buffer_load_dwordx4 v[124:127], v1, s[4:7], s98 offen offset:1024 sc1\n"
"buffer_store_dword v252, v252, s[4:7], 0 offen\n"
"buffer_load_dwordx4 v[200:203], v1, s[4:7], s98 offen offset:2048 sc1\n"
"buffer_load_dwordx4 v[156:159], v1, s[4:7], s98 offen offset:3072 sc1\n"
"s_nop 7\n"
"s_branch .Lmk_p90r\n"
".Lmk_q92s:\n"
";\n"
"s_mov_b32 s1, 0x10000\n"
".Lmk_q92l:\n"
"s_add_u32 s96, s55, 0x40000\n"
"buffer_load_dwordx4 v[132:135], v0, s[4:7], s96 offen sc1\n"
"buffer_load_dwordx4 v[160:163], v0, s[4:7], s96 offen offset:1024 sc1\n"
"buffer_load_dwordx4 v[148:151], v0, s[4:7], s96 offen offset:2048 sc1\n"
"buffer_load_dwordx4 v[164:167], v0, s[4:7], s96 offen offset:3072 sc1\n"
"buffer_load_dwordx4 v[112:115], v1, s[4:7], s96 offen sc1\n"
"buffer_load_dwordx4 v[124:127], v1, s[4:7], s96 offen offset:1024 sc1\n"
"buffer_load_dwordx4 v[200:203], v1, s[4:7], s96 offen offset:2048 sc1\n"
"buffer_load_dwordx4 v[156:159], v1, s[4:7], s96 offen offset:3072 sc1\n"
"s_add_u32 s96, s52, 0x48000\n"
"buffer_load_dwordx4 v[72:75], v0, s[4:7], s96 offen sc1\n"
"buffer_load_dwordx4 v[108:111], v0, s[4:7], s96 offen offset:1024 sc1\n"
"buffer_load_dwordx4 v[120:123], v0, s[4:7], s96 offen offset:2048 sc1\n"
"buffer_load_dwordx4 v[216:219], v0, s[4:7], s96 offen offset:3072 sc1\n"
"buffer_load_dwordx4 v[24:27], v1, s[4:7], s96 offen sc1\n"
"buffer_load_dwordx4 v[100:103], v1, s[4:7], s96 offen offset:1024 sc1\n"
"s_waitcnt vmcnt(0)\n"
"v_max_u32_e32 v254, v132, v133\n"
"v_max3_u32 v254, v254, v134, v135\n"
"v_max3_u32 v254, v254, v160, v161\n"
"v_max3_u32 v254, v254, v162, v163\n"
"v_max3_u32 v254, v254, v148, v149\n"
"v_max3_u32 v254, v254, v150, v151\n"
"v_max3_u32 v254, v254, v164, v165\n"
"v_max3_u32 v254, v254, v166, v167\n"
"v_cmp_eq_u32_e32 vcc, -1, v254\n"
"s_cbranch_vccz .Lmk_q92k\n"
"s_sub_u32 s1, s1, 1\n"
"s_cbranch_scc0 .Lmk_q92l\n"
".Lmk_q92k:\n"
"s_branch .Lmk_q92r\n"
".Lmk_p91s:\n"
";\n"
"s_mov_b32 s1, 0x10000\n"
".Lmk_p91l:\n"
"s_add_u32 s96, s54, 0x40000\n"
"buffer_load_dwordx4 v[36:39], v0, s[4:7], s96 offen sc1\n"
"buffer_load_dwordx4 v[52:55], v0, s[4:7], s96 offen offset:1024 sc1\n"
"buffer_load_dwordx4 v[116:119], v0, s[4:7], s96 offen offset:2048 sc1\n"
"buffer_load_dwordx4 v[128:131], v0, s[4:7], s96 offen offset:3072 sc1\n"
"buffer_load_dwordx4 v[152:155], v1, s[4:7], s96 offen sc1\n"
"buffer_load_dwordx4 v[212:215], v1, s[4:7], s96 offen offset:1024 sc1\n"
"buffer_load_dwordx4 v[220:223], v1, s[4:7], s96 offen offset:2048 sc1\n"
"buffer_load_dwordx4 v[224:227], v1, s[4:7], s96 offen offset:3072 sc1\n"
"s_add_u32 s96, s55, 0x40000\n"
"buffer_load_dwordx4 v[132:135], v0, s[4:7], s96 offen sc1\n"
"buffer_load_dwordx4 v[160:163], v0, s[4:7], s96 offen offset:1024 sc1\n"
"buffer_load_dwordx4 v[148:151], v0, s[4:7], s96 offen offset:2048 sc1\n"
"buffer_load_dwordx4 v[164:167], v0, s[4:7], s96 offen offset:3072 sc1\n"
"buffer_load_dwordx4 v[112:115], v1, s[4:7], s96 offen sc1\n"
"buffer_load_dwordx4 v[124:127], v1, s[4:7], s96 offen offset:1024 sc1\n"
"buffer_load_dwordx4 v[200:203], v1, s[4:7], s96 offen offset:2048 sc1\n"
"buffer_load_dwordx4 v[156:159], v1, s[4:7], s96 offen offset:3072 sc1\n"
"s_add_u32 s96, s52, 0x48000\n"
"buffer_load_dwordx4 v[72:75], v0, s[4:7], s96 offen sc1\n"
"buffer_load_dwordx4 v[108:111], v0, s[4:7], s96 offen offset:1024 sc1\n"
"buffer_load_dwordx4 v[120:123], v0, s[4:7], s96 offen offset:2048 sc1\n"
"buffer_load_dwordx4 v[216:219], v0, s[4:7], s96 offen offset:3072 sc1\n"
"buffer_load_dwordx4 v[24:27], v1, s[4:7], s96 offen sc1\n"
"buffer_load_dwordx4 v[100:103], v1, s[4:7], s96 offen offset:1024 sc1\n"
"s_waitcnt vmcnt(0)\n"
"v_max_u32_e32 v254, v36, v37\n"
"v_max3_u32 v254, v254, v38, v39\n"
"v_max3_u32 v254, v254, v52, v53\n"
"v_max3_u32 v254, v254, v54, v55\n"
"v_max3_u32 v254, v254, v116, v117\n"
"v_max3_u32 v254, v254, v118, v119\n"
"v_max3_u32 v254, v254, v128, v129\n"
"v_max3_u32 v254, v254, v130, v131\n"
"v_max3_u32 v254, v254, v152, v153\n"
"v_max3_u32 v254, v254, v154, v155\n"
"v_max3_u32 v254, v254, v212, v213\n"
"v_max3_u32 v254, v254, v214, v215\n"
"v_max3_u32 v254, v254, v220, v221\n"
"v_max3_u32 v254, v254, v222, v223\n"
"v_max3_u32 v254, v254, v224, v225\n"
"v_max3_u32 v254, v254, v226, v227\n"
"v_cmp_eq_u32_e32 vcc, -1, v254\n"
"s_cbranch_vccz .Lmk_p91k\n"
"s_sub_u32 s1, s1, 1\n"
"s_cbranch_scc0 .Lmk_p91l\n"
".Lmk_p91k:\n"
"v_mfma_f32_16x16x32_bf16 v[176:179], a[0:3], v[36:39], 0\n"
"v_mfma_f32_16x16x32_bf16 v[172:175], a[4:7], v[36:39], 0\n"
"v_mfma_f32_16x16x32_bf16 v[204:207], a[8:11], v[36:39], 0\n"
"v_mfma_f32_16x16x32_bf16 v[188:191], a[12:15], v[36:39], 0\n"
"v_mfma_f32_16x16x32_bf16 v[76:79], a[16:19], v[36:39], 0\n"
"v_mfma_f32_16x16x32_bf16 v[208:211], a[20:23], v[36:39], 0\n"
"v_mfma_f32_16x16x32_bf16 v[176:179], a[24:27], v[52:55], v[176:179]\n"
"v_mfma_f32_16x16x32_bf16 v[172:175], a[28:31], v[52:55], v[172:175]\n"
"v_mfma_f32_16x16x32_bf16 v[204:207], a[32:35], v[52:55], v[204:207]\n"
"v_mfma_f32_16x16x32_bf16 v[188:191], a[36:39], v[52:55], v[188:191]\n"
"v_mfma_f32_16x16x32_bf16 v[76:79], a[40:43], v[52:55], v[76:79]\n"
"v_mfma_f32_16x16x32_bf16 v[208:211], a[44:47], v[52:55], v[208:211]\n"
"v_mfma_f32_16x16x32_bf16 v[176:179], a[48:51], v[116:119], v[176:179]\n"
"v_mfma_f32_16x16x32_bf16 v[172:175], a[52:55], v[116:119], v[172:175]\n"
"v_mfma_f32_16x16x32_bf16 v[204:207], a[56:59], v[116:119], v[204:207]\n"
"v_mfma_f32_16x16x32_bf16 v[188:191], a[60:63], v[116:119], v[188:191]\n"
"v_mfma_f32_16x16x32_bf16 v[76:79], a[64:67], v[116:119], v[76:79]\n"
"v_mfma_f32_16x16x32_bf16 v[208:211], a[68:71], v[116:119], v[208:211]\n"
"v_mfma_f32_16x16x32_bf16 v[176:179], a[72:75], v[128:131], v[176:179]\n"
"v_mfma_f32_16x16x32_bf16 v[172:175], a[76:79], v[128:131], v[172:175]\n"
"v_mfma_f32_16x16x32_bf16 v[204:207], a[80:83], v[128:131], v[204:207]\n"
"v_mfma_f32_16x16x32_bf16 v[188:191], a[84:87], v[128:131], v[188:191]\n"
"v_mfma_f32_16x16x32_bf16 v[76:79], a[88:91], v[128:131], v[76:79]\n"
"v_mfma_f32_16x16x32_bf16 v[208:211], a[92:95], v[128:131], v[208:211]\n"
"v_mfma_f32_16x16x32_bf16 v[176:179], a[96:99], v[152:155], v[176:179]\n"
"v_mfma_f32_16x16x32_bf16 v[172:175], a[100:103], v[152:155], v[172:175]\n"
"v_mfma_f32_16x16x32_bf16 v[204:207], a[104:107], v[152:155], v[204:207]\n"
"v_mfma_f32_16x16x32_bf16 v[188:191], a[108:111], v[152:155], v[188:191]\n"
"v_mfma_f32_16x16x32_bf16 v[76:79], a[112:115], v[152:155], v[76:79]\n"
"v_mfma_f32_16x16x32_bf16 v[208:211], a[116:119], v[152:155], v[208:211]\n"
"v_mfma_f32_16x16x32_bf16 v[176:179], a[120:123], v[212:215], v[176:179]\n"
"v_mfma_f32_16x16x32_bf16 v[172:175], a[124:127], v[212:215], v[172:175]\n"
"v_mfma_f32_16x16x32_bf16 v[204:207], v[40:43], v[212:215], v[204:207]\n"
"v_mfma_f32_16x16x32_bf16 v[188:191], v[44:47], v[212:215], v[188:191]\n"
"v_mfma_f32_16x16x32_bf16 v[76:79], v[80:83], v[212:215], v[76:79]\n"
"v_mfma_f32_16x16x32_bf16 v[208:211], v[68:71], v[212:215], v[208:211]\n"
"v_mfma_f32_16x16x32_bf16 v[176:179], v[88:91], v[220:223], v[176:179]\n"
"v_mfma_f32_16x16x32_bf16 v[172:175], v[48:51], v[220:223], v[172:175]\n"
"v_mfma_f32_16x16x32_bf16 v[204:207], v[92:95], v[220:223], v[204:207]\n"
"v_mfma_f32_16x16x32_bf16 v[188:191], v[96:99], v[220:223], v[188:191]\n"
"v_mfma_f32_16x16x32_bf16 v[76:79], v[168:171], v[220:223], v[76:79]\n"
"v_mfma_f32_16x16x32_bf16 v[208:211], v[104:107], v[220:223], v[208:211]\n"
"v_mfma_f32_16x16x32_bf16 v[176:179], v[136:139], v[224:227], v[176:179]\n"
"v_mfma_f32_16x16x32_bf16 v[172:175], v[180:183], v[224:227], v[172:175]\n"
"v_mfma_f32_16x16x32_bf16 v[204:207], v[28:31], v[224:227], v[204:207]\n"
"v_mfma_f32_16x16x32_bf16 v[188:191], v[32:35], v[224:227], v[188:191]\n"
"v_mfma_f32_16x16x32_bf16 v[76:79], v[60:63], v[224:227], v[76:79]\n"
"v_mfma_f32_16x16x32_bf16 v[208:211], v[64:67], v[224:227], v[208:211]\n"
"s_nop 7\n"
"s_branch .Lmk_p91r\n"
".Lmk_q93s:\n"
";\n"
"s_mov_b32 s1, 0x10000\n"
".Lmk_q93l:\n"
"s_add_u32 s97, s55, 0x40000\n"
"buffer_load_dwordx4 v[112:115], v1, s[4:7], s97 offen sc1\n"
"buffer_load_dwordx4 v[124:127], v1, s[4:7], s97 offen offset:1024 sc1\n"
"buffer_load_dwordx4 v[200:203], v1, s[4:7], s97 offen offset:2048 sc1\n"
"buffer_load_dwordx4 v[156:159], v1, s[4:7], s97 offen offset:3072 sc1\n"
"s_add_u32 s97, s52, 0x48000\n"
"buffer_load_dwordx4 v[72:75], v0, s[4:7], s97 offen sc1\n"
"buffer_load_dwordx4 v[108:111], v0, s[4:7], s97 offen offset:1024 sc1\n"
"buffer_load_dwordx4 v[120:123], v0, s[4:7], s97 offen offset:2048 sc1\n"
"buffer_load_dwordx4 v[216:219], v0, s[4:7], s97 offen offset:3072 sc1\n"
"buffer_load_dwordx4 v[24:27], v1, s[4:7], s97 offen sc1\n"
"buffer_load_dwordx4 v[100:103], v1, s[4:7], s97 offen offset:1024 sc1\n"
"buffer_load_dwordx4 v[36:39], v1, s[4:7], s97 offen offset:2048 sc1\n"
"buffer_load_dwordx4 v[52:55], v1, s[4:7], s97 offen offset:3072 sc1\n"
"s_add_u32 s97, s53, 0x48000\n"
"buffer_load_dwordx4 v[116:119], v0, s[4:7], s97 offen sc1\n"
"buffer_load_dwordx4 v[128:131], v0, s[4:7], s97 offen offset:1024 sc1\n"
"s_waitcnt vmcnt(0)\n"
"v_max_u32_e32 v254, v112, v113\n"
"v_max3_u32 v254, v254, v114, v115\n"
"v_max3_u32 v254, v254, v124, v125\n"
"v_max3_u32 v254, v254, v126, v127\n"
"v_max3_u32 v254, v254, v200, v201\n"
"v_max3_u32 v254, v254, v202, v203\n"
"v_max3_u32 v254, v254, v156, v157\n"
"v_max3_u32 v254, v254, v158, v159\n"
"v_cmp_eq_u32_e32 vcc, -1, v254\n"
"s_cbranch_vccz .Lmk_q93k\n"
"s_sub_u32 s1, s1, 1\n"
"s_cbranch_scc0 .Lmk_q93l\n"
".Lmk_q93k:\n"
"s_branch .Lmk_q93r\n"
".Lmk_p95s:\n"
";\n"
"s_mov_b32 s1, 0x10000\n"
".Lmk_p95l:\n"
"s_add_u32 s97, s52, 0x48000\n"
"buffer_load_dwordx4 v[72:75], v0, s[4:7], s97 offen sc1\n"
"buffer_load_dwordx4 v[112:115], v0, s[4:7], s97 offen offset:1024 sc1\n"
"buffer_load_dwordx4 v[108:111], v0, s[4:7], s97 offen offset:2048 sc1\n"
"buffer_load_dwordx4 v[84:87], v0, s[4:7], s97 offen offset:3072 sc1\n"
"buffer_load_dwordx4 v[124:127], v1, s[4:7], s97 offen sc1\n"
"buffer_load_dwordx4 v[160:163], v1, s[4:7], s97 offen offset:1024 sc1\n"
"buffer_load_dwordx4 v[56:59], v1, s[4:7], s97 offen offset:2048 sc1\n"
"buffer_load_dwordx4 v[36:39], v1, s[4:7], s97 offen offset:3072 sc1\n"
"s_add_u32 s97, s53, 0x48000\n"
"buffer_load_dwordx4 v[116:119], v0, s[4:7], s97 offen sc1\n"
"buffer_load_dwordx4 v[128:131], v0, s[4:7], s97 offen offset:1024 sc1\n"
"buffer_load_dwordx4 v[76:79], v0, s[4:7], s97 offen offset:2048 sc1\n"
"buffer_load_dwordx4 v[132:135], v0, s[4:7], s97 offen offset:3072 sc1\n"
"buffer_load_dwordx4 v[148:151], v1, s[4:7], s97 offen sc1\n"
"buffer_load_dwordx4 v[172:175], v1, s[4:7], s97 offen offset:1024 sc1\n"
"buffer_load_dwordx4 v[208:211], v1, s[4:7], s97 offen offset:2048 sc1\n"
"buffer_load_dwordx4 v[152:155], v1, s[4:7], s97 offen offset:3072 sc1\n"
"s_waitcnt vmcnt(0)\n"
"v_max_u32_e32 v254, v72, v73\n"
"v_max3_u32 v254, v254, v74, v75\n"
"v_max3_u32 v254, v254, v112, v113\n"
"v_max3_u32 v254, v254, v114, v115\n"
"v_max3_u32 v254, v254, v108, v109\n"
"v_max3_u32 v254, v254, v110, v111\n"
"v_max3_u32 v254, v254, v84, v85\n"
"v_max3_u32 v254, v254, v86, v87\n"
"v_max3_u32 v254, v254, v124, v125\n"
"v_max3_u32 v254, v254, v126, v127\n"
"v_max3_u32 v254, v254, v160, v161\n"
"v_max3_u32 v254, v254, v162, v163\n"
"v_max3_u32 v254, v254, v56, v57\n"
"v_max3_u32 v254, v254, v58, v59\n"
"v_max3_u32 v254, v254, v36, v37\n"
"v_max3_u32 v254, v254, v38, v39\n"
"v_cmp_eq_u32_e32 vcc, -1, v254\n"
"s_cbranch_vccz .Lmk_p95k\n"
"s_sub_u32 s1, s1, 1\n"
"s_cbranch_scc0 .Lmk_p95l\n"
".Lmk_p95k:\n"
"v_mfma_f32_16x16x32_bf16 v[156:159], a[0:3], v[72:75], 0\n"
"v_mfma_f32_16x16x32_bf16 v[164:167], a[4:7], v[72:75], 0\n"
"v_mfma_f32_16x16x32_bf16 v[176:179], a[8:11], v[72:75], 0\n"
"v_mfma_f32_16x16x32_bf16 v[200:203], a[12:15], v[72:75], 0\n"
"v_mfma_f32_16x16x32_bf16 v[204:207], a[16:19], v[72:75], 0\n"
"v_mfma_f32_16x16x32_bf16 v[220:223], a[20:23], v[72:75], 0\n"
"v_mfma_f32_16x16x32_bf16 v[156:159], a[24:27], v[112:115], v[156:159]\n"
"v_mfma_f32_16x16x32_bf16 v[164:167], a[28:31], v[112:115], v[164:167]\n"
"v_mfma_f32_16x16x32_bf16 v[176:179], a[32:35], v[112:115], v[176:179]\n"
"v_mfma_f32_16x16x32_bf16 v[200:203], a[36:39], v[112:115], v[200:203]\n"
"v_mfma_f32_16x16x32_bf16 v[204:207], a[40:43], v[112:115], v[204:207]\n"
"v_mfma_f32_16x16x32_bf16 v[220:223], a[44:47], v[112:115], v[220:223]\n"
"v_mfma_f32_16x16x32_bf16 v[156:159], a[48:51], v[108:111], v[156:159]\n"
"v_mfma_f32_16x16x32_bf16 v[164:167], a[52:55], v[108:111], v[164:167]\n"
"v_mfma_f32_16x16x32_bf16 v[176:179], a[56:59], v[108:111], v[176:179]\n"
"v_mfma_f32_16x16x32_bf16 v[200:203], a[60:63], v[108:111], v[200:203]\n"
"v_mfma_f32_16x16x32_bf16 v[204:207], a[64:67], v[108:111], v[204:207]\n"
"v_mfma_f32_16x16x32_bf16 v[220:223], a[68:71], v[108:111], v[220:223]\n"
"v_mfma_f32_16x16x32_bf16 v[156:159], a[72:75], v[84:87], v[156:159]\n"
"v_mfma_f32_16x16x32_bf16 v[164:167], a[76:79], v[84:87], v[164:167]\n"
"v_mfma_f32_16x16x32_bf16 v[176:179], a[80:83], v[84:87], v[176:179]\n"
"v_mfma_f32_16x16x32_bf16 v[200:203], a[84:87], v[84:87], v[200:203]\n"
"v_mfma_f32_16x16x32_bf16 v[204:207], a[88:91], v[84:87], v[204:207]\n"
"v_mfma_f32_16x16x32_bf16 v[220:223], a[92:95], v[84:87], v[220:223]\n"
"v_mfma_f32_16x16x32_bf16 v[156:159], a[96:99], v[124:127], v[156:159]\n"
"v_mfma_f32_16x16x32_bf16 v[164:167], a[100:103], v[124:127], v[164:167]\n"
"v_mfma_f32_16x16x32_bf16 v[176:179], a[104:107], v[124:127], v[176:179]\n"
"v_mfma_f32_16x16x32_bf16 v[200:203], a[108:111], v[124:127], v[200:203]\n"
"v_mfma_f32_16x16x32_bf16 v[204:207], a[112:115], v[124:127], v[204:207]\n"
"v_mfma_f32_16x16x32_bf16 v[220:223], a[116:119], v[124:127], v[220:223]\n"
"v_mfma_f32_16x16x32_bf16 v[156:159], a[120:123], v[160:163], v[156:159]\n"
"v_mfma_f32_16x16x32_bf16 v[164:167], a[124:127], v[160:163], v[164:167]\n"
"v_mfma_f32_16x16x32_bf16 v[176:179], v[40:43], v[160:163], v[176:179]\n"
"v_mfma_f32_16x16x32_bf16 v[200:203], v[44:47], v[160:163], v[200:203]\n"
"v_mfma_f32_16x16x32_bf16 v[204:207], v[80:83], v[160:163], v[204:207]\n"
"v_mfma_f32_16x16x32_bf16 v[220:223], v[68:71], v[160:163], v[220:223]\n"
"v_mfma_f32_16x16x32_bf16 v[156:159], v[88:91], v[56:59], v[156:159]\n"
"v_mfma_f32_16x16x32_bf16 v[164:167], v[48:51], v[56:59], v[164:167]\n"
"v_mfma_f32_16x16x32_bf16 v[176:179], v[92:95], v[56:59], v[176:179]\n"
"v_mfma_f32_16x16x32_bf16 v[200:203], v[96:99], v[56:59], v[200:203]\n"
"v_mfma_f32_16x16x32_bf16 v[204:207], v[168:171], v[56:59], v[204:207]\n"
"v_mfma_f32_16x16x32_bf16 v[220:223], v[104:107], v[56:59], v[220:223]\n"
"v_mfma_f32_16x16x32_bf16 v[156:159], v[136:139], v[36:39], v[156:159]\n"
"v_mfma_f32_16x16x32_bf16 v[164:167], v[180:183], v[36:39], v[164:167]\n"
"v_mfma_f32_16x16x32_bf16 v[176:179], v[28:31], v[36:39], v[176:179]\n"
"v_mfma_f32_16x16x32_bf16 v[200:203], v[32:35], v[36:39], v[200:203]\n"
"v_mfma_f32_16x16x32_bf16 v[204:207], v[60:63], v[36:39], v[204:207]\n"
"v_mfma_f32_16x16x32_bf16 v[220:223], v[64:67], v[36:39], v[220:223]\n"
"s_add_u32 s97, s54, 0x48000\n"
"v_mov_b32_e32 v254, 0x80000000\n"
"buffer_load_dwordx4 v[72:75], v0, s[4:7], s97 offen sc1\n"
"buffer_store_dword v254, v254, s[4:7], 0 offen\n"
"buffer_store_dword v254, v254, s[4:7], 0 offen\n"
"buffer_load_dwordx4 v[112:115], v0, s[4:7], s97 offen offset:1024 sc1\n"
"buffer_load_dwordx4 v[108:111], v0, s[4:7], s97 offen offset:2048 sc1\n"
"buffer_load_dwordx4 v[84:87], v0, s[4:7], s97 offen offset:3072 sc1\n"
"buffer_load_dwordx4 v[124:127], v1, s[4:7], s97 offen sc1\n"
"buffer_store_dword v254, v254, s[4:7], 0 offen\n"
"buffer_load_dwordx4 v[160:163], v1, s[4:7], s97 offen offset:1024 sc1\n"
"buffer_load_dwordx4 v[56:59], v1, s[4:7], s97 offen offset:2048 sc1\n"
"buffer_load_dwordx4 v[36:39], v1, s[4:7], s97 offen offset:3072 sc1\n"
"s_nop 7\n"
"s_branch .Lmk_p95r\n"
".Lmk_m100s:\n"
";\n"
"s_mov_b32 s1, 0x10000\n"
".Lmk_m100l:\n"
"s_add_u32 s95, s62, 0x4800\n"
"buffer_load_dwordx2 v[250:251], v10, s[4:7], s95 offen sc1\n"
"buffer_load_dwordx2 v[248:249], v10, s[4:7], s95 offen offset:128 sc1\n"
"buffer_load_dwordx2 v[246:247], v10, s[4:7], s95 offen offset:256 sc1\n"
"buffer_load_dwordx2 v[244:245], v10, s[4:7], s95 offen offset:384 sc1\n"
"s_waitcnt vmcnt(0)\n"
"v_max_u32_e32 v253, v250, v251\n"
"v_max3_u32 v253, v253, v248, v249\n"
"v_max3_u32 v253, v253, v246, v247\n"
"v_max3_u32 v253, v253, v244, v245\n"
"v_cmp_eq_u32_e32 vcc, -1, v253\n"
"s_cbranch_vccz .Lmk_m100k\n"
"s_sub_u32 s1, s1, 1\n"
"s_cbranch_scc0 .Lmk_m100l\n"
".Lmk_m100k:\n"
"v_add_f32_e32 v250, v250, v248\n"
"v_add_f32_e32 v251, v251, v249\n"
"v_add_f32_e32 v246, v246, v244\n"
"v_add_f32_e32 v247, v247, v245\n"
"v_add_f32_e32 v250, v250, v246\n"
"v_add_f32_e32 v251, v251, v247\n"
"s_nop 1\n"
"s_branch .Lmk_m100r\n"
".Lmk_p96s:\n"
";\n"
"s_mov_b32 s1, 0x10000\n"
".Lmk_p96l:\n"
"s_add_u32 s97, s53, 0x48000\n"
"buffer_load_dwordx4 v[116:119], v0, s[4:7], s97 offen sc1\n"
"buffer_load_dwordx4 v[164:167], v0, s[4:7], s97 offen offset:1024 sc1\n"
"buffer_load_dwordx4 v[128:131], v0, s[4:7], s97 offen offset:2048 sc1\n"
"buffer_load_dwordx4 v[76:79], v0, s[4:7], s97 offen offset:3072 sc1\n"
"buffer_load_dwordx4 v[24:27], v1, s[4:7], s97 offen sc1\n"
"buffer_load_dwordx4 v[120:123], v1, s[4:7], s97 offen offset:1024 sc1\n"
"buffer_load_dwordx4 v[132:135], v1, s[4:7], s97 offen offset:2048 sc1\n"
"buffer_load_dwordx4 v[188:191], v1, s[4:7], s97 offen offset:3072 sc1\n"
"s_add_u32 s97, s54, 0x48000\n"
"buffer_load_dwordx4 v[72:75], v0, s[4:7], s97 offen sc1\n"
"buffer_load_dwordx4 v[112:115], v0, s[4:7], s97 offen offset:1024 sc1\n"
"buffer_load_dwordx4 v[108:111], v0, s[4:7], s97 offen offset:2048 sc1\n"
"buffer_load_dwordx4 v[84:87], v0, s[4:7], s97 offen offset:3072 sc1\n"
"buffer_load_dwordx4 v[124:127], v1, s[4:7], s97 offen sc1\n"
"buffer_load_dwordx4 v[160:163], v1, s[4:7], s97 offen offset:1024 sc1\n"
"buffer_load_dwordx4 v[56:59], v1, s[4:7], s97 offen offset:2048 sc1\n"
"buffer_load_dwordx4 v[36:39], v1, s[4:7], s97 offen offset:3072 sc1\n"
"s_waitcnt vmcnt(0)\n"
"v_max_u32_e32 v252, v116, v117\n"
"v_max3_u32 v252, v252, v118, v119\n"
"v_max3_u32 v252, v252, v164, v165\n"
"v_max3_u32 v252, v252, v166, v167\n"
"v_max3_u32 v252, v252, v128, v129\n"
"v_max3_u32 v252, v252, v130, v131\n"
"v_max3_u32 v252, v252, v76, v77\n"
"v_max3_u32 v252, v252, v78, v79\n"
"v_max3_u32 v252, v252, v24, v25\n"
"v_max3_u32 v252, v252, v26, v27\n"
"v_max3_u32 v252, v252, v120, v121\n"
"v_max3_u32 v252, v252, v122, v123\n"
"v_max3_u32 v252, v252, v132, v133\n"
"v_max3_u32 v252, v252, v134, v135\n"
"v_max3_u32 v252, v252, v188, v189\n"
"v_max3_u32 v252, v252, v190, v191\n"
"v_cmp_eq_u32_e32 vcc, -1, v252\n"
"s_cbranch_vccz .Lmk_p96k\n"
"s_sub_u32 s1, s1, 1\n"
"s_cbranch_scc0 .Lmk_p96l\n"
".Lmk_p96k:\n"
"v_mfma_f32_16x16x32_bf16 v[52:55], a[0:3], v[116:119], 0\n"
"v_mfma_f32_16x16x32_bf16 v[100:103], a[4:7], v[116:119], 0\n"
"v_mfma_f32_16x16x32_bf16 v[140:143], a[8:11], v[116:119], 0\n"
"v_mfma_f32_16x16x32_bf16 v[192:195], a[12:15], v[116:119], 0\n"
"v_mfma_f32_16x16x32_bf16 v[156:159], a[16:19], v[116:119], 0\n"
"v_mfma_f32_16x16x32_bf16 v[144:147], a[20:23], v[116:119], 0\n"
"v_mfma_f32_16x16x32_bf16 v[52:55], a[24:27], v[164:167], v[52:55]\n"
"v_mfma_f32_16x16x32_bf16 v[100:103], a[28:31], v[164:167], v[100:103]\n"
"v_mfma_f32_16x16x32_bf16 v[140:143], a[32:35], v[164:167], v[140:143]\n"
"v_mfma_f32_16x16x32_bf16 v[192:195], a[36:39], v[164:167], v[192:195]\n"
"v_mfma_f32_16x16x32_bf16 v[156:159], a[40:43], v[164:167], v[156:159]\n"
"v_mfma_f32_16x16x32_bf16 v[144:147], a[44:47], v[164:167], v[144:147]\n"
"v_mfma_f32_16x16x32_bf16 v[52:55], a[48:51], v[128:131], v[52:55]\n"
"v_mfma_f32_16x16x32_bf16 v[100:103], a[52:55], v[128:131], v[100:103]\n"
"v_mfma_f32_16x16x32_bf16 v[140:143], a[56:59], v[128:131], v[140:143]\n"
"v_mfma_f32_16x16x32_bf16 v[192:195], a[60:63], v[128:131], v[192:195]\n"
"v_mfma_f32_16x16x32_bf16 v[156:159], a[64:67], v[128:131], v[156:159]\n"
"v_mfma_f32_16x16x32_bf16 v[144:147], a[68:71], v[128:131], v[144:147]\n"
"v_mfma_f32_16x16x32_bf16 v[52:55], a[72:75], v[76:79], v[52:55]\n"
"v_mfma_f32_16x16x32_bf16 v[100:103], a[76:79], v[76:79], v[100:103]\n"
"v_mfma_f32_16x16x32_bf16 v[140:143], a[80:83], v[76:79], v[140:143]\n"
"v_mfma_f32_16x16x32_bf16 v[192:195], a[84:87], v[76:79], v[192:195]\n"
"v_mfma_f32_16x16x32_bf16 v[156:159], a[88:91], v[76:79], v[156:159]\n"
"v_mfma_f32_16x16x32_bf16 v[144:147], a[92:95], v[76:79], v[144:147]\n"
"v_mfma_f32_16x16x32_bf16 v[52:55], a[96:99], v[24:27], v[52:55]\n"
"v_mfma_f32_16x16x32_bf16 v[100:103], a[100:103], v[24:27], v[100:103]\n"
"v_mfma_f32_16x16x32_bf16 v[140:143], a[104:107], v[24:27], v[140:143]\n"
"v_mfma_f32_16x16x32_bf16 v[192:195], a[108:111], v[24:27], v[192:195]\n"
"v_mfma_f32_16x16x32_bf16 v[156:159], a[112:115], v[24:27], v[156:159]\n"
"v_mfma_f32_16x16x32_bf16 v[144:147], a[116:119], v[24:27], v[144:147]\n"
"v_mfma_f32_16x16x32_bf16 v[52:55], a[120:123], v[120:123], v[52:55]\n"
"v_mfma_f32_16x16x32_bf16 v[100:103], a[124:127], v[120:123], v[100:103]\n"
"v_mfma_f32_16x16x32_bf16 v[140:143], v[40:43], v[120:123], v[140:143]\n"
"v_mfma_f32_16x16x32_bf16 v[192:195], v[44:47], v[120:123], v[192:195]\n"
"v_mfma_f32_16x16x32_bf16 v[156:159], v[80:83], v[120:123], v[156:159]\n"
"v_mfma_f32_16x16x32_bf16 v[144:147], v[68:71], v[120:123], v[144:147]\n"
"v_mfma_f32_16x16x32_bf16 v[52:55], v[88:91], v[132:135], v[52:55]\n"
"v_mfma_f32_16x16x32_bf16 v[100:103], v[48:51], v[132:135], v[100:103]\n"
"v_mfma_f32_16x16x32_bf16 v[140:143], v[92:95], v[132:135], v[140:143]\n"
"v_mfma_f32_16x16x32_bf16 v[192:195], v[96:99], v[132:135], v[192:195]\n"
"v_mfma_f32_16x16x32_bf16 v[156:159], v[168:171], v[132:135], v[156:159]\n"
"v_mfma_f32_16x16x32_bf16 v[144:147], v[104:107], v[132:135], v[144:147]\n"
"v_mfma_f32_16x16x32_bf16 v[52:55], v[136:139], v[188:191], v[52:55]\n"
"v_mfma_f32_16x16x32_bf16 v[100:103], v[180:183], v[188:191], v[100:103]\n"
"v_mfma_f32_16x16x32_bf16 v[140:143], v[28:31], v[188:191], v[140:143]\n"
"v_mfma_f32_16x16x32_bf16 v[192:195], v[32:35], v[188:191], v[192:195]\n"
"v_mfma_f32_16x16x32_bf16 v[156:159], v[60:63], v[188:191], v[156:159]\n"
"v_mfma_f32_16x16x32_bf16 v[144:147], v[64:67], v[188:191], v[144:147]\n"
"s_add_u32 s97, s55, 0x48000\n"
"v_mov_b32_e32 v252, 0x80000000\n"
"buffer_load_dwordx4 v[116:119], v0, s[4:7], s97 offen sc1\n"
"buffer_load_dwordx4 v[164:167], v0, s[4:7], s97 offen offset:1024 sc1\n"
"buffer_store_dword v252, v252, s[4:7], 0 offen\n"
"buffer_load_dwordx4 v[128:131], v0, s[4:7], s97 offen offset:2048 sc1\n"
"buffer_store_dword v252, v252, s[4:7], 0 offen\n"
"buffer_store_dword v252, v252, s[4:7], 0 offen\n"
"buffer_load_dwordx4 v[76:79], v0, s[4:7], s97 offen offset:3072 sc1\n"
"buffer_store_dword v252, v252, s[4:7], 0 offen\n"
"buffer_load_dwordx4 v[24:27], v1, s[4:7], s97 offen sc1\n"
"buffer_load_dwordx4 v[120:123], v1, s[4:7], s97 offen offset:1024 sc1\n"
"buffer_load_dwordx4 v[132:135], v1, s[4:7], s97 offen offset:2048 sc1\n"
"buffer_store_dword v252, v252, s[4:7], 0 offen\n"
"buffer_load_dwordx4 v[188:191], v1, s[4:7], s97 offen offset:3072 sc1\n"
"s_nop 7\n"
"s_branch .Lmk_p96r\n"
".Lmk_f85s:\n"
";\n"
"s_mov_b32 s1, 0x10000\n"
".Lmk_f85l:\n"
"buffer_load_dwordx2 v[254:255], v12, s[4:7], s96 offen sc1\n"
"s_waitcnt vmcnt(0)\n"
"v_cmp_ne_u64_e32 vcc, v[254:255], v[18:19]\n"
"s_cbranch_vccz .Lmk_f85r\n"
"s_sub_u32 s1, s1, 1\n"
"s_cbranch_scc0 .Lmk_f85l\n"
"s_branch .Lmk_f85r\n"
".Lmk_q98s:\n"
";\n"
"s_mov_b32 s1, 0x10000\n"
".Lmk_q98l:\n"
"s_add_u32 s96, s55, 0x48000\n"
"buffer_load_dwordx4 v[116:119], v0, s[4:7], s96 offen sc1\n"
"buffer_load_dwordx4 v[164:167], v0, s[4:7], s96 offen offset:1024 sc1\n"
"buffer_load_dwordx4 v[128:131], v0, s[4:7], s96 offen offset:2048 sc1\n"
"buffer_load_dwordx4 v[76:79], v0, s[4:7], s96 offen offset:3072 sc1\n"
"buffer_load_dwordx4 v[24:27], v1, s[4:7], s96 offen sc1\n"
"buffer_load_dwordx4 v[120:123], v1, s[4:7], s96 offen offset:1024 sc1\n"
"buffer_load_dwordx4 v[132:135], v1, s[4:7], s96 offen offset:2048 sc1\n"
"buffer_load_dwordx4 v[188:191], v1, s[4:7], s96 offen offset:3072 sc1\n"
"s_waitcnt vmcnt(0)\n"
"v_max_u32_e32 v255, v116, v117\n"
"v_max3_u32 v255, v255, v118, v119\n"
"v_max3_u32 v255, v255, v164, v165\n"
"v_max3_u32 v255, v255, v166, v167\n"
"v_max3_u32 v255, v255, v128, v129\n"
"v_max3_u32 v255, v255, v130, v131\n"
"v_max3_u32 v255, v255, v76, v77\n"
"v_max3_u32 v255, v255, v78, v79\n"
"v_cmp_eq_u32_e32 vcc, -1, v255\n"
"s_cbranch_vccz .Lmk_q98k\n"
"s_sub_u32 s1, s1, 1\n"
"s_cbranch_scc0 .Lmk_q98l\n"
".Lmk_q98k:\n"
"s_branch .Lmk_q98r\n"
".Lmk_p97s:\n"
";\n"
"s_mov_b32 s1, 0x10000\n"
".Lmk_p97l:\n"
"s_add_u32 s96, s54, 0x48000\n"
"buffer_load_dwordx4 v[36:39], v0, s[4:7], s96 offen sc1\n"
"buffer_load_dwordx4 v[56:59], v0, s[4:7], s96 offen offset:1024 sc1\n"
"buffer_load_dwordx4 v[84:87], v0, s[4:7], s96 offen offset:2048 sc1\n"
"buffer_load_dwordx4 v[124:127], v0, s[4:7], s96 offen offset:3072 sc1\n"
"buffer_load_dwordx4 v[160:163], v1, s[4:7], s96 offen sc1\n"
"buffer_load_dwordx4 v[172:175], v1, s[4:7], s96 offen offset:1024 sc1\n"
"buffer_load_dwordx4 v[192:195], v1, s[4:7], s96 offen offset:2048 sc1\n"
"buffer_load_dwordx4 v[208:211], v1, s[4:7], s96 offen offset:3072 sc1\n"
"s_add_u32 s96, s55, 0x48000\n"
"buffer_load_dwordx4 v[116:119], v0, s[4:7], s96 offen sc1\n"
"buffer_load_dwordx4 v[164:167], v0, s[4:7], s96 offen offset:1024 sc1\n"
"buffer_load_dwordx4 v[128:131], v0, s[4:7], s96 offen offset:2048 sc1\n"
"buffer_load_dwordx4 v[76:79], v0, s[4:7], s96 offen offset:3072 sc1\n"
"buffer_load_dwordx4 v[24:27], v1, s[4:7], s96 offen sc1\n"
"buffer_load_dwordx4 v[120:123], v1, s[4:7], s96 offen offset:1024 sc1\n"
"buffer_load_dwordx4 v[132:135], v1, s[4:7], s96 offen offset:2048 sc1\n"
"buffer_load_dwordx4 v[188:191], v1, s[4:7], s96 offen offset:3072 sc1\n"
"s_waitcnt vmcnt(0)\n"
"v_max_u32_e32 v255, v36, v37\n"
"v_max3_u32 v255, v255, v38, v39\n"
"v_max3_u32 v255, v255, v56, v57\n"
"v_max3_u32 v255, v255, v58, v59\n"
"v_max3_u32 v255, v255, v84, v85\n"
"v_max3_u32 v255, v255, v86, v87\n"
"v_max3_u32 v255, v255, v124, v125\n"
"v_max3_u32 v255, v255, v126, v127\n"
"v_max3_u32 v255, v255, v160, v161\n"
"v_max3_u32 v255, v255, v162, v163\n"
"v_max3_u32 v255, v255, v172, v173\n"
"v_max3_u32 v255, v255, v174, v175\n"
"v_max3_u32 v255, v255, v192, v193\n"
"v_max3_u32 v255, v255, v194, v195\n"
"v_max3_u32 v255, v255, v208, v209\n"
"v_max3_u32 v255, v255, v210, v211\n"
"v_cmp_eq_u32_e32 vcc, -1, v255\n"
"s_cbranch_vccz .Lmk_p97k\n"
"s_sub_u32 s1, s1, 1\n"
"s_cbranch_scc0 .Lmk_p97l\n"
".Lmk_p97k:\n"
"v_mfma_f32_16x16x32_bf16 v[152:155], a[0:3], v[36:39], 0\n"
"v_mfma_f32_16x16x32_bf16 v[200:203], a[4:7], v[36:39], 0\n"
"v_mfma_f32_16x16x32_bf16 v[204:207], a[8:11], v[36:39], 0\n"
"v_mfma_f32_16x16x32_bf16 v[212:215], a[12:15], v[36:39], 0\n"
"v_mfma_f32_16x16x32_bf16 v[52:55], a[16:19], v[36:39], 0\n"
"v_mfma_f32_16x16x32_bf16 v[220:223], a[20:23], v[36:39], 0\n"
"v_mfma_f32_16x16x32_bf16 v[152:155], a[24:27], v[56:59], v[152:155]\n"
"v_mfma_f32_16x16x32_bf16 v[200:203], a[28:31], v[56:59], v[200:203]\n"
"v_mfma_f32_16x16x32_bf16 v[204:207], a[32:35], v[56:59], v[204:207]\n"
"v_mfma_f32_16x16x32_bf16 v[212:215], a[36:39], v[56:59], v[212:215]\n"
"v_mfma_f32_16x16x32_bf16 v[52:55], a[40:43], v[56:59], v[52:55]\n"
"v_mfma_f32_16x16x32_bf16 v[220:223], a[44:47], v[56:59], v[220:223]\n"
"v_mfma_f32_16x16x32_bf16 v[152:155], a[48:51], v[84:87], v[152:155]\n"
"v_mfma_f32_16x16x32_bf16 v[200:203], a[52:55], v[84:87], v[200:203]\n"
"v_mfma_f32_16x16x32_bf16 v[204:207], a[56:59], v[84:87], v[204:207]\n"
"v_mfma_f32_16x16x32_bf16 v[212:215], a[60:63], v[84:87], v[212:215]\n"
"v_mfma_f32_16x16x32_bf16 v[52:55], a[64:67], v[84:87], v[52:55]\n"
"v_mfma_f32_16x16x32_bf16 v[220:223], a[68:71], v[84:87], v[220:223]\n"
"v_mfma_f32_16x16x32_bf16 v[152:155], a[72:75], v[124:127], v[152:155]\n"
"v_mfma_f32_16x16x32_bf16 v[200:203], a[76:79], v[124:127], v[200:203]\n"
"v_mfma_f32_16x16x32_bf16 v[204:207], a[80:83], v[124:127], v[204:207]\n"
"v_mfma_f32_16x16x32_bf16 v[212:215], a[84:87], v[124:127], v[212:215]\n"
"v_mfma_f32_16x16x32_bf16 v[52:55], a[88:91], v[124:127], v[52:55]\n"
"v_mfma_f32_16x16x32_bf16 v[220:223], a[92:95], v[124:127], v[220:223]\n"
"v_mfma_f32_16x16x32_bf16 v[152:155], a[96:99], v[160:163], v[152:155]\n"
"v_mfma_f32_16x16x32_bf16 v[200:203], a[100:103], v[160:163], v[200:203]\n"
"v_mfma_f32_16x16x32_bf16 v[204:207], a[104:107], v[160:163], v[204:207]\n"
"v_mfma_f32_16x16x32_bf16 v[212:215], a[108:111], v[160:163], v[212:215]\n"
"v_mfma_f32_16x16x32_bf16 v[52:55], a[112:115], v[160:163], v[52:55]\n"
"v_mfma_f32_16x16x32_bf16 v[220:223], a[116:119], v[160:163], v[220:223]\n"
"v_mfma_f32_16x16x32_bf16 v[152:155], a[120:123], v[172:175], v[152:155]\n"
"v_mfma_f32_16x16x32_bf16 v[200:203], a[124:127], v[172:175], v[200:203]\n"
"v_mfma_f32_16x16x32_bf16 v[204:207], v[40:43], v[172:175], v[204:207]\n"
"v_mfma_f32_16x16x32_bf16 v[212:215], v[44:47], v[172:175], v[212:215]\n"
"v_mfma_f32_16x16x32_bf16 v[52:55], v[80:83], v[172:175], v[52:55]\n"
"v_mfma_f32_16x16x32_bf16 v[220:223], v[68:71], v[172:175], v[220:223]\n"
"v_mfma_f32_16x16x32_bf16 v[152:155], v[88:91], v[192:195], v[152:155]\n"
"v_mfma_f32_16x16x32_bf16 v[200:203], v[48:51], v[192:195], v[200:203]\n"
"v_mfma_f32_16x16x32_bf16 v[204:207], v[92:95], v[192:195], v[204:207]\n"
"v_mfma_f32_16x16x32_bf16 v[212:215], v[96:99], v[192:195], v[212:215]\n"
"v_mfma_f32_16x16x32_bf16 v[52:55], v[168:171], v[192:195], v[52:55]\n"
"v_mfma_f32_16x16x32_bf16 v[220:223], v[104:107], v[192:195], v[220:223]\n"
"v_mfma_f32_16x16x32_bf16 v[152:155], v[136:139], v[208:211], v[152:155]\n"
"v_mfma_f32_16x16x32_bf16 v[200:203], v[180:183], v[208:211], v[200:203]\n"
"v_mfma_f32_16x16x32_bf16 v[204:207], v[28:31], v[208:211], v[204:207]\n"
"v_mfma_f32_16x16x32_bf16 v[212:215], v[32:35], v[208:211], v[212:215]\n"
"v_mfma_f32_16x16x32_bf16 v[52:55], v[60:63], v[208:211], v[52:55]\n"
"v_mfma_f32_16x16x32_bf16 v[220:223], v[64:67], v[208:211], v[220:223]\n"
"s_nop 7\n"
"s_branch .Lmk_p97r\n"
".Lmk_q99s:\n"
";\n"
"s_mov_b32 s1, 0x10000\n"
".Lmk_q99l:\n"
"s_add_u32 s98, s55, 0x48000\n"
"buffer_load_dwordx4 v[24:27], v1, s[4:7], s98 offen sc1\n"
"buffer_load_dwordx4 v[120:123], v1, s[4:7], s98 offen offset:1024 sc1\n"
"buffer_load_dwordx4 v[132:135], v1, s[4:7], s98 offen offset:2048 sc1\n"
"buffer_load_dwordx4 v[188:191], v1, s[4:7], s98 offen offset:3072 sc1\n"
"s_waitcnt vmcnt(0)\n"
"v_max_u32_e32 v253, v24, v25\n"
"v_max3_u32 v253, v253, v26, v27\n"
"v_max3_u32 v253, v253, v120, v121\n"
"v_max3_u32 v253, v253, v122, v123\n"
"v_max3_u32 v253, v253, v132, v133\n"
"v_max3_u32 v253, v253, v134, v135\n"
"v_max3_u32 v253, v253, v188, v189\n"
"v_max3_u32 v253, v253, v190, v191\n"
"v_cmp_eq_u32_e32 vcc, -1, v253\n"
"s_cbranch_vccz .Lmk_q99k\n"
"s_sub_u32 s1, s1, 1\n"
"s_cbranch_scc0 .Lmk_q99l\n"
".Lmk_q99k:\n"
"s_branch .Lmk_q99r\n"
".Lmk_f86s:\n"
";\n"
"s_mov_b32 s1, 0x10000\n"
".Lmk_f86l:\n"
"buffer_load_dwordx2 v[250:251], off, s[4:7], s96 sc1\n"
"s_waitcnt vmcnt(0)\n"
"v_cmp_ne_u64_e32 vcc, v[250:251], v[18:19]\n"
"s_cbranch_vccz .Lmk_f86r\n"
"s_sub_u32 s1, s1, 1\n"
"s_cbranch_scc0 .Lmk_f86l\n"
"s_branch .Lmk_f86r\n"
".Lmk_x109s:\n"
"s_nop 3\n"
"v_mov_b32_e32 v36, 0xff800000\n"
"v_mov_b32_e32 v37, 0xff800000\n"
"v_mov_b32_e32 v38, 0xff800000\n"
"v_mov_b32_e32 v39, 0xff800000\n"
"v_mov_b32_e32 v60, 0xff800000\n"
"v_mov_b32_e32 v61, 0xff800000\n"
"v_mov_b32_e32 v62, 0xff800000\n"
"v_mov_b32_e32 v63, 0xff800000\n"
"s_nop 1\n"
"s_branch .Lmk_x109r\n"
".Lmk_x110s:\n"
"s_nop 3\n"
"v_mov_b32_e32 v36, 0xff800000\n"
"v_mov_b32_e32 v37, 0xff800000\n"
"v_mov_b32_e32 v38, 0xff800000\n"
"v_mov_b32_e32 v39, 0xff800000\n"
"v_mov_b32_e32 v60, 0xff800000\n"
"v_mov_b32_e32 v61, 0xff800000\n"
"v_mov_b32_e32 v62, 0xff800000\n"
"v_mov_b32_e32 v63, 0xff800000\n"
"s_nop 1\n"
"s_branch .Lmk_x110r\n"
".Lmk_x111s:\n"
"s_nop 3\n"
"v_mov_b32_e32 v36, 0xff800000\n"
"v_mov_b32_e32 v37, 0xff800000\n"
"v_mov_b32_e32 v38, 0xff800000\n"
"v_mov_b32_e32 v39, 0xff800000\n"
"v_mov_b32_e32 v60, 0xff800000\n"
"v_mov_b32_e32 v61, 0xff800000\n"
"v_mov_b32_e32 v62, 0xff800000\n"
"v_mov_b32_e32 v63, 0xff800000\n"
"s_nop 1\n"
"s_branch .Lmk_x111r\n"
".Lmk_x112s:\n"
"s_nop 3\n"
"v_mov_b32_e32 v36, 0xff800000\n"
"v_mov_b32_e32 v37, 0xff800000\n"
"v_mov_b32_e32 v38, 0xff800000\n"
"v_mov_b32_e32 v39, 0xff800000\n"
"v_mov_b32_e32 v60, 0xff800000\n"
"v_mov_b32_e32 v61, 0xff800000\n"
"v_mov_b32_e32 v62, 0xff800000\n"
"v_mov_b32_e32 v63, 0xff800000\n"
"s_nop 1\n"
"s_branch .Lmk_x112r\n"
".Lmk_b107:\n"
"s_cmp_lg_u32 s2, 0\n"
"s_cbranch_scc1 .Lmk_k113\n"
"s_mov_b32 s2, 1\n"
"s_waitcnt vmcnt(0) lgkmcnt(0)\n"
"s_nop 7\n"
"s_mov_b32 s97, 11\n"
"v_add_u32_e32 v248, 0x3800, v15\n"
"v_add_u32_e32 v247, 0x3800, v252\n"
"ds_read_b128 v[36:39], v15 offset:24576\n"
"ds_read_b128 v[40:43], v252 offset:24832\n"
"s_waitcnt lgkmcnt(0)\n"
"v_mfma_f32_16x16x32_bf16 v[48:51], v[36:39], v[28:31], 0\n"
"v_mfma_f32_16x16x32_bf16 v[52:55], v[36:39], v[32:35], 0\n"
"v_mfma_f32_16x16x32_bf16 v[36:39], v[40:43], v[28:31], 0\n"
"v_mfma_f32_16x16x32_bf16 v[56:59], v[40:43], v[32:35], 0\n"
".Lmk_b107l:\n"
"ds_read_b128 v[40:43], v248 offset:8192\n"
"ds_read_b128 v[60:63], v247 offset:8448\n"
"s_cmp_gt_i32 s78, s97\n"
"s_cselect_b32 s96, 0xff800000, 0\n"
"v_mov_b32_e32 v72, s96\n"
"v_mov_b32_e32 v73, s96\n"
"v_mov_b32_e32 v74, s96\n"
"v_mov_b32_e32 v75, s96\n"
"v_subrev_u32_e32 v248, 0x800, v248\n"
"v_subrev_u32_e32 v247, 0x800, v247\n"
"s_waitcnt lgkmcnt(0)\n"
"v_mfma_f32_16x16x32_bf16 v[76:79], v[40:43], v[28:31], v[72:75]\n"
"v_mfma_f32_16x16x32_bf16 v[80:83], v[40:43], v[32:35], v[72:75]\n"
"v_mfma_f32_16x16x32_bf16 v[40:43], v[60:63], v[28:31], v[72:75]\n"
"v_mfma_f32_16x16x32_bf16 v[84:87], v[60:63], v[32:35], v[72:75]\n"
"s_nop 7\n"
"s_cmp_lg_u32 s97, 4\n"
"s_cbranch_scc1 .Lmk_b107m\n"
"v_cndmask_b32_e64 v76, v48, v76, s[20:21]\n"
"v_cndmask_b32_e64 v77, v49, v77, s[22:23]\n"
"v_cndmask_b32_e64 v78, v50, v78, s[24:25]\n"
"v_cndmask_b32_e64 v79, v51, v79, s[26:27]\n"
"v_cndmask_b32_e64 v40, v36, v40, s[28:29]\n"
"v_cndmask_b32_e64 v41, v37, v41, s[30:31]\n"
"v_cndmask_b32_e64 v42, v38, v42, s[32:33]\n"
"v_cndmask_b32_e64 v43, v39, v43, s[34:35]\n"
"v_cndmask_b32_e64 v80, v52, v80, s[36:37]\n"
"v_cndmask_b32_e64 v81, v53, v81, s[38:39]\n"
"v_cndmask_b32_e64 v82, v54, v82, s[40:41]\n"
"v_cndmask_b32_e64 v83, v55, v83, s[42:43]\n"
"v_cndmask_b32_e64 v84, v56, v84, s[44:45]\n"
"v_cndmask_b32_e64 v85, v57, v85, s[46:47]\n"
"v_cndmask_b32_e64 v86, v58, v86, s[48:49]\n"
"v_cndmask_b32_e64 v87, v59, v87, s[50:51]\n"
".Lmk_b107m:\n"
"v_max3_f32 v255, v255, v76, v77\n"
"v_max3_f32 v254, v254, v80, v81\n"
"v_max3_f32 v255, v255, v78, v79\n"
"v_max3_f32 v254, v254, v82, v83\n"
"v_max3_f32 v255, v255, v40, v41\n"
"v_max3_f32 v254, v254, v84, v85\n"
"v_max3_f32 v255, v255, v42, v43\n"
"v_max3_f32 v254, v254, v86, v87\n"
"s_sub_u32 s97, s97, 1\n"
"s_cmp_ge_i32 s97, 4\n"
"s_cbranch_scc1 .Lmk_b107l\n"
"v_mov_b32_e32 v248, v255\n"
"v_mov_b32_e32 v247, v254\n"
"s_nop 0\n"
"v_permlane16_swap_b32_e32 v255, v248\n"
"v_permlane16_swap_b32_e32 v254, v247\n"
"v_max_f32_e32 v255, v255, v248\n"
"v_max_f32_e32 v254, v254, v247\n"
"v_mov_b32_e32 v248, v255\n"
"v_mov_b32_e32 v247, v254\n"
"s_nop 0\n"
"v_permlane32_swap_b32_e32 v255, v248\n"
"v_permlane32_swap_b32_e32 v254, v247\n"
"v_max_f32_e32 v255, v255, v248\n"
"v_max_f32_e32 v254, v254, v247\n"
"v_mov_b32_e32 v250, v255\n"
"v_mov_b32_e32 v249, v254\n"
"s_branch .Lmk_a106\n"
".Lmk_x116s:\n"
"s_nop 3\n"
"v_mov_b32_e32 v116, 0xff800000\n"
"v_mov_b32_e32 v117, 0xff800000\n"
"v_mov_b32_e32 v118, 0xff800000\n"
"v_mov_b32_e32 v119, 0xff800000\n"
"v_mov_b32_e32 v120, 0xff800000\n"
"v_mov_b32_e32 v121, 0xff800000\n"
"v_mov_b32_e32 v122, 0xff800000\n"
"v_mov_b32_e32 v123, 0xff800000\n"
"s_nop 1\n"
"s_branch .Lmk_x116r\n"
".Lmk_x117s:\n"
"s_nop 3\n"
"v_mov_b32_e32 v116, 0xff800000\n"
"v_mov_b32_e32 v117, 0xff800000\n"
"v_mov_b32_e32 v118, 0xff800000\n"
"v_mov_b32_e32 v119, 0xff800000\n"
"v_mov_b32_e32 v120, 0xff800000\n"
"v_mov_b32_e32 v121, 0xff800000\n"
"v_mov_b32_e32 v122, 0xff800000\n"
"v_mov_b32_e32 v123, 0xff800000\n"
"s_nop 1\n"
"s_branch .Lmk_x117r\n"
".Lmk_x118s:\n"
"s_nop 3\n"
"v_mov_b32_e32 v116, 0xff800000\n"
"v_mov_b32_e32 v117, 0xff800000\n"
"v_mov_b32_e32 v118, 0xff800000\n"
"v_mov_b32_e32 v119, 0xff800000\n"
"v_mov_b32_e32 v120, 0xff800000\n"
"v_mov_b32_e32 v121, 0xff800000\n"
"v_mov_b32_e32 v122, 0xff800000\n"
"v_mov_b32_e32 v123, 0xff800000\n"
"s_nop 1\n"
"s_branch .Lmk_x118r\n"
".Lmk_h105s:\n"
";\n"
"s_mov_b32 s1, 0x10000\n"
".Lmk_h105l:\n"
"s_add_u32 m0, s77, 0x4000\n"
"s_add_u32 s95, s65, 0\n"
"buffer_load_dwordx4 v2, s[4:7], s95 offen sc1 lds\n"
"buffer_load_dwordx4 v2, s[4:7], s95 offen offset:1024 sc1 lds\n"
"s_add_u32 m0, s77, 0xc000\n"
"s_add_u32 s95, s65, 0x4000\n"
"buffer_load_dwordx4 v2, s[4:7], s95 offen sc1 lds\n"
"buffer_load_dwordx4 v2, s[4:7], s95 offen offset:1024 sc1 lds\n"
"s_waitcnt vmcnt(0)\n"
"ds_read_b128 v[80:83], v2 offset:16384\n"
"ds_read_b128 v[76:79], v2 offset:17408\n"
"ds_read_b128 v[72:75], v2 offset:49152\n"
"ds_read_b128 v[84:87], v2 offset:50176\n"
"s_waitcnt lgkmcnt(0)\n"
"s_waitcnt vmcnt(0)\n"
"v_max_u32_e32 v248, v80, v81\n"
"v_max3_u32 v248, v248, v82, v83\n"
"v_max3_u32 v248, v248, v76, v77\n"
"v_max3_u32 v248, v248, v78, v79\n"
"v_max3_u32 v248, v248, v72, v73\n"
"v_max3_u32 v248, v248, v74, v75\n"
"v_max3_u32 v248, v248, v84, v85\n"
"v_max3_u32 v248, v248, v86, v87\n"
"v_cmp_eq_u32_e32 vcc, -1, v248\n"
"s_cbranch_vccz .Lmk_h105k\n"
"s_sub_u32 s1, s1, 1\n"
"s_cbranch_scc0 .Lmk_h105l\n"
".Lmk_h105k:\n"
"s_branch .Lmk_h105r\n"
".Lmk_f87s:\n"
";\n"
"s_mov_b32 s1, 0x10000\n"
".Lmk_f87l:\n"
"buffer_load_dwordx2 v[246:247], v12, s[4:7], s96 offen sc1\n"
"s_waitcnt vmcnt(0)\n"
"v_cmp_ne_u64_e32 vcc, v[246:247], v[18:19]\n"
"s_cbranch_vccz .Lmk_f87r\n"
"s_sub_u32 s1, s1, 1\n"
"s_cbranch_scc0 .Lmk_f87l\n"
"s_branch .Lmk_f87r\n"
".Lmk_b115:\n"
"s_cmp_lg_u32 s2, 0\n"
"s_cbranch_scc1 .Lmk_k120\n"
"s_mov_b32 s2, 1\n"
"s_waitcnt vmcnt(0) lgkmcnt(0)\n"
"s_nop 7\n"
"s_mov_b32 s96, 7\n"
"v_add_u32_e32 v248, 0x3800, v15\n"
"v_add_u32_e32 v247, 0x3800, v252\n"
"ds_read_b128 v[140:143], v15 offset:16384\n"
"ds_read_b128 v[148:151], v252 offset:16640\n"
"s_waitcnt lgkmcnt(0)\n"
"v_mfma_f32_16x16x32_bf16 v[188:191], v[140:143], v[88:91], 0\n"
"v_mfma_f32_16x16x32_bf16 v[208:211], v[140:143], v[92:95], 0\n"
"v_mfma_f32_16x16x32_bf16 v[140:143], v[148:151], v[88:91], 0\n"
"v_mfma_f32_16x16x32_bf16 v[220:223], v[148:151], v[92:95], 0\n"
".Lmk_b115l:\n"
"ds_read_b128 v[148:151], v248\n"
"ds_read_b128 v[224:227], v247 offset:256\n"
"s_cmp_gt_i32 s78, s96\n"
"s_cselect_b32 s95, 0xff800000, 0\n"
"v_mov_b32_e32 v228, s95\n"
"v_mov_b32_e32 v229, s95\n"
"v_mov_b32_e32 v230, s95\n"
"v_mov_b32_e32 v231, s95\n"
"v_subrev_u32_e32 v248, 0x800, v248\n"
"v_subrev_u32_e32 v247, 0x800, v247\n"
"s_waitcnt lgkmcnt(0)\n"
"v_mfma_f32_16x16x32_bf16 v[232:235], v[148:151], v[88:91], v[228:231]\n"
"v_mfma_f32_16x16x32_bf16 v[236:239], v[148:151], v[92:95], v[228:231]\n"
"v_mfma_f32_16x16x32_bf16 v[148:151], v[224:227], v[88:91], v[228:231]\n"
"v_mfma_f32_16x16x32_bf16 v[240:243], v[224:227], v[92:95], v[228:231]\n"
"s_nop 7\n"
"s_cmp_lg_u32 s96, 0\n"
"s_cbranch_scc1 .Lmk_b115m\n"
"v_cndmask_b32_e64 v232, v188, v232, s[20:21]\n"
"v_cndmask_b32_e64 v233, v189, v233, s[22:23]\n"
"v_cndmask_b32_e64 v234, v190, v234, s[24:25]\n"
"v_cndmask_b32_e64 v235, v191, v235, s[26:27]\n"
"v_cndmask_b32_e64 v148, v140, v148, s[28:29]\n"
"v_cndmask_b32_e64 v149, v141, v149, s[30:31]\n"
"v_cndmask_b32_e64 v150, v142, v150, s[32:33]\n"
"v_cndmask_b32_e64 v151, v143, v151, s[34:35]\n"
"v_cndmask_b32_e64 v236, v208, v236, s[36:37]\n"
"v_cndmask_b32_e64 v237, v209, v237, s[38:39]\n"
"v_cndmask_b32_e64 v238, v210, v238, s[40:41]\n"
"v_cndmask_b32_e64 v239, v211, v239, s[42:43]\n"
"v_cndmask_b32_e64 v240, v220, v240, s[44:45]\n"
"v_cndmask_b32_e64 v241, v221, v241, s[46:47]\n"
"v_cndmask_b32_e64 v242, v222, v242, s[48:49]\n"
"v_cndmask_b32_e64 v243, v223, v243, s[50:51]\n"
".Lmk_b115m:\n"
"v_max3_f32 v255, v255, v232, v233\n"
"v_max3_f32 v254, v254, v236, v237\n"
"v_max3_f32 v255, v255, v234, v235\n"
"v_max3_f32 v254, v254, v238, v239\n"
"v_max3_f32 v255, v255, v148, v149\n"
"v_max3_f32 v254, v254, v240, v241\n"
"v_max3_f32 v255, v255, v150, v151\n"
"v_max3_f32 v254, v254, v242, v243\n"
"s_sub_u32 s96, s96, 1\n"
"s_cmp_ge_i32 s96, 0\n"
"s_cbranch_scc1 .Lmk_b115l\n"
"v_mov_b32_e32 v248, v255\n"
"v_mov_b32_e32 v247, v254\n"
"s_nop 0\n"
"v_permlane16_swap_b32_e32 v255, v248\n"
"v_permlane16_swap_b32_e32 v254, v247\n"
"v_max_f32_e32 v255, v255, v248\n"
"v_max_f32_e32 v254, v254, v247\n"
"v_mov_b32_e32 v248, v255\n"
"v_mov_b32_e32 v247, v254\n"
"s_nop 0\n"
"v_permlane32_swap_b32_e32 v255, v248\n"
"v_permlane32_swap_b32_e32 v254, v247\n"
"v_max_f32_e32 v255, v255, v248\n"
"v_max_f32_e32 v254, v254, v247\n"
"v_mov_b32_e32 v250, v255\n"
"v_mov_b32_e32 v249, v254\n"
"s_branch .Lmk_a114\n"
".Lmk_p121s:\n"
";\n"
"s_mov_b32 s1, 0x10000\n"
".Lmk_p121l:\n"
"s_add_u32 s97, s56, 0x40000\n"
"buffer_load_dwordx4 v[136:139], v0, s[4:7], s97 offen sc1\n"
"buffer_load_dwordx4 v[128:131], v0, s[4:7], s97 offen offset:1024 sc1\n"
"buffer_load_dwordx4 v[120:123], v0, s[4:7], s97 offen offset:2048 sc1\n"
"buffer_load_dwordx4 v[108:111], v0, s[4:7], s97 offen offset:3072 sc1\n"
"buffer_load_dwordx4 v[140:143], v1, s[4:7], s97 offen sc1\n"
"buffer_load_dwordx4 v[144:147], v1, s[4:7], s97 offen offset:1024 sc1\n"
"buffer_load_dwordx4 v[152:155], v1, s[4:7], s97 offen offset:2048 sc1\n"
"buffer_load_dwordx4 v[160:163], v1, s[4:7], s97 offen offset:3072 sc1\n"
"s_add_u32 s97, s57, 0x40000\n"
"buffer_load_dwordx4 v[216:219], v0, s[4:7], s97 offen sc1\n"
"buffer_load_dwordx4 v[212:215], v0, s[4:7], s97 offen offset:1024 sc1\n"
"buffer_load_dwordx4 v[184:187], v0, s[4:7], s97 offen offset:2048 sc1\n"
"buffer_load_dwordx4 v[96:99], v0, s[4:7], s97 offen offset:3072 sc1\n"
"buffer_load_dwordx4 v[156:159], v1, s[4:7], s97 offen sc1\n"
"buffer_load_dwordx4 v[104:107], v1, s[4:7], s97 offen offset:1024 sc1\n"
"buffer_load_dwordx4 v[112:115], v1, s[4:7], s97 offen offset:2048 sc1\n"
"buffer_load_dwordx4 v[124:127], v1, s[4:7], s97 offen offset:3072 sc1\n"
"s_add_u32 s97, s59, 0x40000\n"
"buffer_load_dwordx4 v[164:167], v0, s[4:7], s97 offen sc1\n"
"buffer_load_dwordx4 v[168:171], v0, s[4:7], s97 offen offset:1024 sc1\n"
"buffer_load_dwordx4 v[172:175], v0, s[4:7], s97 offen offset:2048 sc1\n"
"buffer_load_dwordx4 v[116:119], v0, s[4:7], s97 offen offset:3072 sc1\n"
"s_waitcnt vmcnt(0)\n"
"v_max_u32_e32 v255, v136, v137\n"
"v_max3_u32 v255, v255, v138, v139\n"
"v_max3_u32 v255, v255, v128, v129\n"
"v_max3_u32 v255, v255, v130, v131\n"
"v_max3_u32 v255, v255, v120, v121\n"
"v_max3_u32 v255, v255, v122, v123\n"
"v_max3_u32 v255, v255, v108, v109\n"
"v_max3_u32 v255, v255, v110, v111\n"
"v_max3_u32 v255, v255, v140, v141\n"
"v_max3_u32 v255, v255, v142, v143\n"
"v_max3_u32 v255, v255, v144, v145\n"
"v_max3_u32 v255, v255, v146, v147\n"
"v_max3_u32 v255, v255, v152, v153\n"
"v_max3_u32 v255, v255, v154, v155\n"
"v_max3_u32 v255, v255, v160, v161\n"
"v_max3_u32 v255, v255, v162, v163\n"
"v_cmp_eq_u32_e32 vcc, -1, v255\n"
"s_cbranch_vccz .Lmk_p121k\n"
"s_sub_u32 s1, s1, 1\n"
"s_cbranch_scc0 .Lmk_p121l\n"
".Lmk_p121k:\n"
"v_mfma_f32_16x16x32_bf16 v[88:91], a[128:131], v[136:139], 0\n"
"v_mfma_f32_16x16x32_bf16 v[92:95], a[132:135], v[136:139], 0\n"
"v_mfma_f32_16x16x32_bf16 v[100:103], a[136:139], v[136:139], 0\n"
"v_mfma_f32_16x16x32_bf16 v[148:151], a[140:143], v[136:139], 0\n"
"v_mfma_f32_16x16x32_bf16 v[88:91], a[144:147], v[128:131], v[88:91]\n"
"v_mfma_f32_16x16x32_bf16 v[92:95], a[148:151], v[128:131], v[92:95]\n"
"v_mfma_f32_16x16x32_bf16 v[100:103], a[152:155], v[128:131], v[100:103]\n"
"v_mfma_f32_16x16x32_bf16 v[148:151], a[156:159], v[128:131], v[148:151]\n"
"v_mfma_f32_16x16x32_bf16 v[88:91], a[160:163], v[120:123], v[88:91]\n"
"v_mfma_f32_16x16x32_bf16 v[92:95], a[164:167], v[120:123], v[92:95]\n"
"v_mfma_f32_16x16x32_bf16 v[100:103], a[168:171], v[120:123], v[100:103]\n"
"v_mfma_f32_16x16x32_bf16 v[148:151], a[172:175], v[120:123], v[148:151]\n"
"v_mfma_f32_16x16x32_bf16 v[88:91], a[176:179], v[108:111], v[88:91]\n"
"v_mfma_f32_16x16x32_bf16 v[92:95], a[180:183], v[108:111], v[92:95]\n"
"v_mfma_f32_16x16x32_bf16 v[100:103], a[184:187], v[108:111], v[100:103]\n"
"v_mfma_f32_16x16x32_bf16 v[148:151], a[188:191], v[108:111], v[148:151]\n"
"v_mfma_f32_16x16x32_bf16 v[88:91], a[192:195], v[140:143], v[88:91]\n"
"v_mfma_f32_16x16x32_bf16 v[92:95], a[196:199], v[140:143], v[92:95]\n"
"v_mfma_f32_16x16x32_bf16 v[100:103], a[200:203], v[140:143], v[100:103]\n"
"v_mfma_f32_16x16x32_bf16 v[148:151], a[204:207], v[140:143], v[148:151]\n"
"v_mfma_f32_16x16x32_bf16 v[88:91], a[208:211], v[144:147], v[88:91]\n"
"v_mfma_f32_16x16x32_bf16 v[92:95], a[212:215], v[144:147], v[92:95]\n"
"v_mfma_f32_16x16x32_bf16 v[100:103], a[216:219], v[144:147], v[100:103]\n"
"v_mfma_f32_16x16x32_bf16 v[148:151], a[220:223], v[144:147], v[148:151]\n"
"v_mfma_f32_16x16x32_bf16 v[88:91], a[224:227], v[152:155], v[88:91]\n"
"v_mfma_f32_16x16x32_bf16 v[92:95], a[228:231], v[152:155], v[92:95]\n"
"v_mfma_f32_16x16x32_bf16 v[100:103], a[232:235], v[152:155], v[100:103]\n"
"v_mfma_f32_16x16x32_bf16 v[148:151], a[236:239], v[152:155], v[148:151]\n"
"v_mfma_f32_16x16x32_bf16 v[88:91], a[240:243], v[160:163], v[88:91]\n"
"v_mfma_f32_16x16x32_bf16 v[92:95], a[244:247], v[160:163], v[92:95]\n"
"v_mfma_f32_16x16x32_bf16 v[100:103], a[248:251], v[160:163], v[100:103]\n"
"v_mfma_f32_16x16x32_bf16 v[148:151], a[252:255], v[160:163], v[148:151]\n"
"s_add_u32 s97, s58, 0x40000\n"
"v_mov_b32_e32 v255, 0x80000000\n"
"buffer_load_dwordx4 v[136:139], v0, s[4:7], s97 offen sc1\n"
"buffer_load_dwordx4 v[128:131], v0, s[4:7], s97 offen offset:1024 sc1\n"
"buffer_load_dwordx4 v[120:123], v0, s[4:7], s97 offen offset:2048 sc1\n"
"buffer_load_dwordx4 v[108:111], v0, s[4:7], s97 offen offset:3072 sc1\n"
"buffer_store_dword v255, v255, s[4:7], 0 offen\n"
"buffer_store_dword v255, v255, s[4:7], 0 offen\n"
"buffer_load_dwordx4 v[140:143], v1, s[4:7], s97 offen sc1\n"
"buffer_load_dwordx4 v[144:147], v1, s[4:7], s97 offen offset:1024 sc1\n"
"buffer_load_dwordx4 v[152:155], v1, s[4:7], s97 offen offset:2048 sc1\n"
"buffer_load_dwordx4 v[160:163], v1, s[4:7], s97 offen offset:3072 sc1\n"
"buffer_store_dword v255, v255, s[4:7], 0 offen\n"
"buffer_store_dword v255, v255, s[4:7], 0 offen\n"
"buffer_store_dword v255, v255, s[4:7], 0 offen\n"
"buffer_store_dword v255, v255, s[4:7], 0 offen\n"
"s_nop 7\n"
"s_branch .Lmk_p121r\n"
".Lmk_p122s:\n"
";\n"
"s_mov_b32 s1, 0x10000\n"
".Lmk_p122l:\n"
"s_add_u32 s98, s57, 0x40000\n"
"buffer_load_dwordx4 v[164:167], v0, s[4:7], s98 offen sc1\n"
"buffer_load_dwordx4 v[168:171], v0, s[4:7], s98 offen offset:1024 sc1\n"
"buffer_load_dwordx4 v[172:175], v0, s[4:7], s98 offen offset:2048 sc1\n"
"buffer_load_dwordx4 v[116:119], v0, s[4:7], s98 offen offset:3072 sc1\n"
"buffer_load_dwordx4 v[188:191], v1, s[4:7], s98 offen sc1\n"
"buffer_load_dwordx4 v[88:91], v1, s[4:7], s98 offen offset:1024 sc1\n"
"buffer_load_dwordx4 v[92:95], v1, s[4:7], s98 offen offset:2048 sc1\n"
"buffer_load_dwordx4 v[96:99], v1, s[4:7], s98 offen offset:3072 sc1\n"
"s_add_u32 s98, s58, 0x40000\n"
"buffer_load_dwordx4 v[136:139], v0, s[4:7], s98 offen sc1\n"
"buffer_load_dwordx4 v[128:131], v0, s[4:7], s98 offen offset:1024 sc1\n"
"buffer_load_dwordx4 v[120:123], v0, s[4:7], s98 offen offset:2048 sc1\n"
"buffer_load_dwordx4 v[108:111], v0, s[4:7], s98 offen offset:3072 sc1\n"
"buffer_load_dwordx4 v[140:143], v1, s[4:7], s98 offen sc1\n"
"buffer_load_dwordx4 v[144:147], v1, s[4:7], s98 offen offset:1024 sc1\n"
"buffer_load_dwordx4 v[152:155], v1, s[4:7], s98 offen offset:2048 sc1\n"
"buffer_load_dwordx4 v[160:163], v1, s[4:7], s98 offen offset:3072 sc1\n"
"s_waitcnt vmcnt(0)\n"
"v_max_u32_e32 v255, v164, v165\n"
"v_max3_u32 v255, v255, v166, v167\n"
"v_max3_u32 v255, v255, v168, v169\n"
"v_max3_u32 v255, v255, v170, v171\n"
"v_max3_u32 v255, v255, v172, v173\n"
"v_max3_u32 v255, v255, v174, v175\n"
"v_max3_u32 v255, v255, v116, v117\n"
"v_max3_u32 v255, v255, v118, v119\n"
"v_max3_u32 v255, v255, v188, v189\n"
"v_max3_u32 v255, v255, v190, v191\n"
"v_max3_u32 v255, v255, v88, v89\n"
"v_max3_u32 v255, v255, v90, v91\n"
"v_max3_u32 v255, v255, v92, v93\n"
"v_max3_u32 v255, v255, v94, v95\n"
"v_max3_u32 v255, v255, v96, v97\n"
"v_max3_u32 v255, v255, v98, v99\n"
"v_cmp_eq_u32_e32 vcc, -1, v255\n"
"s_cbranch_vccz .Lmk_p122k\n"
"s_sub_u32 s1, s1, 1\n"
"s_cbranch_scc0 .Lmk_p122l\n"
".Lmk_p122k:\n"
"v_mfma_f32_16x16x32_bf16 v[132:135], a[128:131], v[164:167], 0\n"
"v_mfma_f32_16x16x32_bf16 v[176:179], a[132:135], v[164:167], 0\n"
"v_mfma_f32_16x16x32_bf16 v[180:183], a[136:139], v[164:167], 0\n"
"v_mfma_f32_16x16x32_bf16 v[192:195], a[140:143], v[164:167], 0\n"
"v_mfma_f32_16x16x32_bf16 v[132:135], a[144:147], v[168:171], v[132:135]\n"
"v_mfma_f32_16x16x32_bf16 v[176:179], a[148:151], v[168:171], v[176:179]\n"
"v_mfma_f32_16x16x32_bf16 v[180:183], a[152:155], v[168:171], v[180:183]\n"
"v_mfma_f32_16x16x32_bf16 v[192:195], a[156:159], v[168:171], v[192:195]\n"
"v_mfma_f32_16x16x32_bf16 v[132:135], a[160:163], v[172:175], v[132:135]\n"
"v_mfma_f32_16x16x32_bf16 v[176:179], a[164:167], v[172:175], v[176:179]\n"
"v_mfma_f32_16x16x32_bf16 v[180:183], a[168:171], v[172:175], v[180:183]\n"
"v_mfma_f32_16x16x32_bf16 v[192:195], a[172:175], v[172:175], v[192:195]\n"
"v_mfma_f32_16x16x32_bf16 v[132:135], a[176:179], v[116:119], v[132:135]\n"
"v_mfma_f32_16x16x32_bf16 v[176:179], a[180:183], v[116:119], v[176:179]\n"
"v_mfma_f32_16x16x32_bf16 v[180:183], a[184:187], v[116:119], v[180:183]\n"
"v_mfma_f32_16x16x32_bf16 v[192:195], a[188:191], v[116:119], v[192:195]\n"
"v_mfma_f32_16x16x32_bf16 v[132:135], a[192:195], v[188:191], v[132:135]\n"
"v_mfma_f32_16x16x32_bf16 v[176:179], a[196:199], v[188:191], v[176:179]\n"
"v_mfma_f32_16x16x32_bf16 v[180:183], a[200:203], v[188:191], v[180:183]\n"
"v_mfma_f32_16x16x32_bf16 v[192:195], a[204:207], v[188:191], v[192:195]\n"
"v_mfma_f32_16x16x32_bf16 v[132:135], a[208:211], v[88:91], v[132:135]\n"
"v_mfma_f32_16x16x32_bf16 v[176:179], a[212:215], v[88:91], v[176:179]\n"
"v_mfma_f32_16x16x32_bf16 v[180:183], a[216:219], v[88:91], v[180:183]\n"
"v_mfma_f32_16x16x32_bf16 v[192:195], a[220:223], v[88:91], v[192:195]\n"
"v_mfma_f32_16x16x32_bf16 v[132:135], a[224:227], v[92:95], v[132:135]\n"
"v_mfma_f32_16x16x32_bf16 v[176:179], a[228:231], v[92:95], v[176:179]\n"
"v_mfma_f32_16x16x32_bf16 v[180:183], a[232:235], v[92:95], v[180:183]\n"
"v_mfma_f32_16x16x32_bf16 v[192:195], a[236:239], v[92:95], v[192:195]\n"
"v_mfma_f32_16x16x32_bf16 v[132:135], a[240:243], v[96:99], v[132:135]\n"
"v_mfma_f32_16x16x32_bf16 v[176:179], a[244:247], v[96:99], v[176:179]\n"
"v_mfma_f32_16x16x32_bf16 v[180:183], a[248:251], v[96:99], v[180:183]\n"
"v_mfma_f32_16x16x32_bf16 v[192:195], a[252:255], v[96:99], v[192:195]\n"
"s_add_u32 s98, s59, 0x40000\n"
"v_mov_b32_e32 v255, 0x80000000\n"
"buffer_load_dwordx4 v[164:167], v0, s[4:7], s98 offen sc1\n"
"buffer_load_dwordx4 v[168:171], v0, s[4:7], s98 offen offset:1024 sc1\n"
"buffer_load_dwordx4 v[172:175], v0, s[4:7], s98 offen offset:2048 sc1\n"
"buffer_load_dwordx4 v[116:119], v0, s[4:7], s98 offen offset:3072 sc1\n"
"buffer_load_dwordx4 v[188:191], v1, s[4:7], s98 offen sc1\n"
"buffer_load_dwordx4 v[88:91], v1, s[4:7], s98 offen offset:1024 sc1\n"
"buffer_load_dwordx4 v[92:95], v1, s[4:7], s98 offen offset:2048 sc1\n"
"buffer_load_dwordx4 v[96:99], v1, s[4:7], s98 offen offset:3072 sc1\n"
"s_nop 7\n"
"s_branch .Lmk_p122r\n"
".Lmk_q124s:\n"
";\n"
"s_mov_b32 s1, 0x10000\n"
".Lmk_q124l:\n"
"s_add_u32 s97, s59, 0x40000\n"
"buffer_load_dwordx4 v[164:167], v0, s[4:7], s97 offen sc1\n"
"buffer_load_dwordx4 v[168:171], v0, s[4:7], s97 offen offset:1024 sc1\n"
"buffer_load_dwordx4 v[172:175], v0, s[4:7], s97 offen offset:2048 sc1\n"
"buffer_load_dwordx4 v[116:119], v0, s[4:7], s97 offen offset:3072 sc1\n"
"buffer_load_dwordx4 v[188:191], v1, s[4:7], s97 offen sc1\n"
"buffer_load_dwordx4 v[88:91], v1, s[4:7], s97 offen offset:1024 sc1\n"
"buffer_load_dwordx4 v[92:95], v1, s[4:7], s97 offen offset:2048 sc1\n"
"buffer_load_dwordx4 v[96:99], v1, s[4:7], s97 offen offset:3072 sc1\n"
"s_add_u32 s97, s56, 0\n"
"buffer_load_dwordx4 v[120:123], v0, s[4:7], s97 offen sc1\n"
"buffer_load_dwordx4 v[108:111], v0, s[4:7], s97 offen offset:1024 sc1\n"
"buffer_load_dwordx4 v[128:131], v0, s[4:7], s97 offen offset:2048 sc1\n"
"buffer_load_dwordx4 v[132:135], v0, s[4:7], s97 offen offset:3072 sc1\n"
"s_waitcnt vmcnt(0)\n"
"v_max_u32_e32 v255, v164, v165\n"
"v_max3_u32 v255, v255, v166, v167\n"
"v_max3_u32 v255, v255, v168, v169\n"
"v_max3_u32 v255, v255, v170, v171\n"
"v_max3_u32 v255, v255, v172, v173\n"
"v_max3_u32 v255, v255, v174, v175\n"
"v_max3_u32 v255, v255, v116, v117\n"
"v_max3_u32 v255, v255, v118, v119\n"
"v_cmp_eq_u32_e32 vcc, -1, v255\n"
"s_cbranch_vccz .Lmk_q124k\n"
"s_sub_u32 s1, s1, 1\n"
"s_cbranch_scc0 .Lmk_q124l\n"
".Lmk_q124k:\n"
"s_branch .Lmk_q124r\n"
".Lmk_p123s:\n"
";\n"
"s_mov_b32 s1, 0x10000\n"
".Lmk_p123l:\n"
"s_add_u32 s97, s58, 0x40000\n"
"buffer_load_dwordx4 v[136:139], v0, s[4:7], s97 offen sc1\n"
"buffer_load_dwordx4 v[140:143], v0, s[4:7], s97 offen offset:1024 sc1\n"
"buffer_load_dwordx4 v[144:147], v0, s[4:7], s97 offen offset:2048 sc1\n"
"buffer_load_dwordx4 v[148:151], v0, s[4:7], s97 offen offset:3072 sc1\n"
"buffer_load_dwordx4 v[152:155], v1, s[4:7], s97 offen sc1\n"
"buffer_load_dwordx4 v[160:163], v1, s[4:7], s97 offen offset:1024 sc1\n"
"buffer_load_dwordx4 v[176:179], v1, s[4:7], s97 offen offset:2048 sc1\n"
"buffer_load_dwordx4 v[180:183], v1, s[4:7], s97 offen offset:3072 sc1\n"
"s_add_u32 s97, s59, 0x40000\n"
"buffer_load_dwordx4 v[164:167], v0, s[4:7], s97 offen sc1\n"
"buffer_load_dwordx4 v[168:171], v0, s[4:7], s97 offen offset:1024 sc1\n"
"buffer_load_dwordx4 v[172:175], v0, s[4:7], s97 offen offset:2048 sc1\n"
"buffer_load_dwordx4 v[116:119], v0, s[4:7], s97 offen offset:3072 sc1\n"
"buffer_load_dwordx4 v[188:191], v1, s[4:7], s97 offen sc1\n"
"buffer_load_dwordx4 v[88:91], v1, s[4:7], s97 offen offset:1024 sc1\n"
"buffer_load_dwordx4 v[92:95], v1, s[4:7], s97 offen offset:2048 sc1\n"
"buffer_load_dwordx4 v[96:99], v1, s[4:7], s97 offen offset:3072 sc1\n"
"s_add_u32 s97, s56, 0\n"
"buffer_load_dwordx4 v[120:123], v0, s[4:7], s97 offen sc1\n"
"buffer_load_dwordx4 v[108:111], v0, s[4:7], s97 offen offset:1024 sc1\n"
"buffer_load_dwordx4 v[128:131], v0, s[4:7], s97 offen offset:2048 sc1\n"
"buffer_load_dwordx4 v[132:135], v0, s[4:7], s97 offen offset:3072 sc1\n"
"s_waitcnt vmcnt(0)\n"
"v_max_u32_e32 v255, v136, v137\n"
"v_max3_u32 v255, v255, v138, v139\n"
"v_max3_u32 v255, v255, v140, v141\n"
"v_max3_u32 v255, v255, v142, v143\n"
"v_max3_u32 v255, v255, v144, v145\n"
"v_max3_u32 v255, v255, v146, v147\n"
"v_max3_u32 v255, v255, v148, v149\n"
"v_max3_u32 v255, v255, v150, v151\n"
"v_max3_u32 v255, v255, v152, v153\n"
"v_max3_u32 v255, v255, v154, v155\n"
"v_max3_u32 v255, v255, v160, v161\n"
"v_max3_u32 v255, v255, v162, v163\n"
"v_max3_u32 v255, v255, v176, v177\n"
"v_max3_u32 v255, v255, v178, v179\n"
"v_max3_u32 v255, v255, v180, v181\n"
"v_max3_u32 v255, v255, v182, v183\n"
"v_cmp_eq_u32_e32 vcc, -1, v255\n"
"s_cbranch_vccz .Lmk_p123k\n"
"s_sub_u32 s1, s1, 1\n"
"s_cbranch_scc0 .Lmk_p123l\n"
".Lmk_p123k:\n"
"v_mfma_f32_16x16x32_bf16 v[100:103], a[128:131], v[136:139], 0\n"
"v_mfma_f32_16x16x32_bf16 v[104:107], a[132:135], v[136:139], 0\n"
"v_mfma_f32_16x16x32_bf16 v[112:115], a[136:139], v[136:139], 0\n"
"v_mfma_f32_16x16x32_bf16 v[124:127], a[140:143], v[136:139], 0\n"
"v_mfma_f32_16x16x32_bf16 v[100:103], a[144:147], v[140:143], v[100:103]\n"
"v_mfma_f32_16x16x32_bf16 v[104:107], a[148:151], v[140:143], v[104:107]\n"
"v_mfma_f32_16x16x32_bf16 v[112:115], a[152:155], v[140:143], v[112:115]\n"
"v_mfma_f32_16x16x32_bf16 v[124:127], a[156:159], v[140:143], v[124:127]\n"
"v_mfma_f32_16x16x32_bf16 v[100:103], a[160:163], v[144:147], v[100:103]\n"
"v_mfma_f32_16x16x32_bf16 v[104:107], a[164:167], v[144:147], v[104:107]\n"
"v_mfma_f32_16x16x32_bf16 v[112:115], a[168:171], v[144:147], v[112:115]\n"
"v_mfma_f32_16x16x32_bf16 v[124:127], a[172:175], v[144:147], v[124:127]\n"
"v_mfma_f32_16x16x32_bf16 v[100:103], a[176:179], v[148:151], v[100:103]\n"
"v_mfma_f32_16x16x32_bf16 v[104:107], a[180:183], v[148:151], v[104:107]\n"
"v_mfma_f32_16x16x32_bf16 v[112:115], a[184:187], v[148:151], v[112:115]\n"
"v_mfma_f32_16x16x32_bf16 v[124:127], a[188:191], v[148:151], v[124:127]\n"
"v_mfma_f32_16x16x32_bf16 v[100:103], a[192:195], v[152:155], v[100:103]\n"
"v_mfma_f32_16x16x32_bf16 v[104:107], a[196:199], v[152:155], v[104:107]\n"
"v_mfma_f32_16x16x32_bf16 v[112:115], a[200:203], v[152:155], v[112:115]\n"
"v_mfma_f32_16x16x32_bf16 v[124:127], a[204:207], v[152:155], v[124:127]\n"
"v_mfma_f32_16x16x32_bf16 v[100:103], a[208:211], v[160:163], v[100:103]\n"
"v_mfma_f32_16x16x32_bf16 v[104:107], a[212:215], v[160:163], v[104:107]\n"
"v_mfma_f32_16x16x32_bf16 v[112:115], a[216:219], v[160:163], v[112:115]\n"
"v_mfma_f32_16x16x32_bf16 v[124:127], a[220:223], v[160:163], v[124:127]\n"
"v_mfma_f32_16x16x32_bf16 v[100:103], a[224:227], v[176:179], v[100:103]\n"
"v_mfma_f32_16x16x32_bf16 v[104:107], a[228:231], v[176:179], v[104:107]\n"
"v_mfma_f32_16x16x32_bf16 v[112:115], a[232:235], v[176:179], v[112:115]\n"
"v_mfma_f32_16x16x32_bf16 v[124:127], a[236:239], v[176:179], v[124:127]\n"
"v_mfma_f32_16x16x32_bf16 v[100:103], a[240:243], v[180:183], v[100:103]\n"
"v_mfma_f32_16x16x32_bf16 v[104:107], a[244:247], v[180:183], v[104:107]\n"
"v_mfma_f32_16x16x32_bf16 v[112:115], a[248:251], v[180:183], v[112:115]\n"
"v_mfma_f32_16x16x32_bf16 v[124:127], a[252:255], v[180:183], v[124:127]\n"
"s_nop 7\n"
"s_branch .Lmk_p123r\n"
".Lmk_q125s:\n"
";\n"
"s_mov_b32 s1, 0x10000\n"
".Lmk_q125l:\n"
"s_add_u32 s97, s59, 0x40000\n"
"buffer_load_dwordx4 v[188:191], v1, s[4:7], s97 offen sc1\n"
"buffer_load_dwordx4 v[88:91], v1, s[4:7], s97 offen offset:1024 sc1\n"
"buffer_load_dwordx4 v[92:95], v1, s[4:7], s97 offen offset:2048 sc1\n"
"buffer_load_dwordx4 v[96:99], v1, s[4:7], s97 offen offset:3072 sc1\n"
"s_add_u32 s97, s56, 0\n"
"buffer_load_dwordx4 v[120:123], v0, s[4:7], s97 offen sc1\n"
"buffer_load_dwordx4 v[108:111], v0, s[4:7], s97 offen offset:1024 sc1\n"
"buffer_load_dwordx4 v[128:131], v0, s[4:7], s97 offen offset:2048 sc1\n"
"buffer_load_dwordx4 v[132:135], v0, s[4:7], s97 offen offset:3072 sc1\n"
"buffer_load_dwordx4 v[136:139], v1, s[4:7], s97 offen sc1\n"
"buffer_load_dwordx4 v[100:103], v1, s[4:7], s97 offen offset:1024 sc1\n"
"s_waitcnt vmcnt(0)\n"
"v_max_u32_e32 v255, v188, v189\n"
"v_max3_u32 v255, v255, v190, v191\n"
"v_max3_u32 v255, v255, v88, v89\n"
"v_max3_u32 v255, v255, v90, v91\n"
"v_max3_u32 v255, v255, v92, v93\n"
"v_max3_u32 v255, v255, v94, v95\n"
"v_max3_u32 v255, v255, v96, v97\n"
"v_max3_u32 v255, v255, v98, v99\n"
"v_cmp_eq_u32_e32 vcc, -1, v255\n"
"s_cbranch_vccz .Lmk_q125k\n"
"s_sub_u32 s1, s1, 1\n"
"s_cbranch_scc0 .Lmk_q125l\n"
".Lmk_q125k:\n"
"s_branch .Lmk_q125r\n"
".Lmk_p126s:\n"
";\n"
"s_mov_b32 s1, 0x10000\n"
".Lmk_p126l:\n"
"s_add_u32 s97, s56, 0\n"
"buffer_load_dwordx4 v[148:151], v0, s[4:7], s97 offen sc1\n"
"buffer_load_dwordx4 v[120:123], v0, s[4:7], s97 offen offset:1024 sc1\n"
"buffer_load_dwordx4 v[172:175], v0, s[4:7], s97 offen offset:2048 sc1\n"
"buffer_load_dwordx4 v[128:131], v0, s[4:7], s97 offen offset:3072 sc1\n"
"buffer_load_dwordx4 v[132:135], v1, s[4:7], s97 offen sc1\n"
"buffer_load_dwordx4 v[136:139], v1, s[4:7], s97 offen offset:1024 sc1\n"
"buffer_load_dwordx4 v[176:179], v1, s[4:7], s97 offen offset:2048 sc1\n"
"buffer_load_dwordx4 v[180:183], v1, s[4:7], s97 offen offset:3072 sc1\n"
"s_add_u32 s97, s57, 0\n"
"buffer_load_dwordx4 v[124:127], v0, s[4:7], s97 offen sc1\n"
"buffer_load_dwordx4 v[92:95], v0, s[4:7], s97 offen offset:1024 sc1\n"
"buffer_load_dwordx4 v[168:171], v0, s[4:7], s97 offen offset:2048 sc1\n"
"buffer_load_dwordx4 v[88:91], v0, s[4:7], s97 offen offset:3072 sc1\n"
"buffer_load_dwordx4 v[96:99], v1, s[4:7], s97 offen sc1\n"
"buffer_load_dwordx4 v[144:147], v1, s[4:7], s97 offen offset:1024 sc1\n"
"buffer_load_dwordx4 v[152:155], v1, s[4:7], s97 offen offset:2048 sc1\n"
"buffer_load_dwordx4 v[164:167], v1, s[4:7], s97 offen offset:3072 sc1\n"
"s_waitcnt vmcnt(0)\n"
"v_max_u32_e32 v255, v148, v149\n"
"v_max3_u32 v255, v255, v150, v151\n"
"v_max3_u32 v255, v255, v120, v121\n"
"v_max3_u32 v255, v255, v122, v123\n"
"v_max3_u32 v255, v255, v172, v173\n"
"v_max3_u32 v255, v255, v174, v175\n"
"v_max3_u32 v255, v255, v128, v129\n"
"v_max3_u32 v255, v255, v130, v131\n"
"v_max3_u32 v255, v255, v132, v133\n"
"v_max3_u32 v255, v255, v134, v135\n"
"v_max3_u32 v255, v255, v136, v137\n"
"v_max3_u32 v255, v255, v138, v139\n"
"v_max3_u32 v255, v255, v176, v177\n"
"v_max3_u32 v255, v255, v178, v179\n"
"v_max3_u32 v255, v255, v180, v181\n"
"v_max3_u32 v255, v255, v182, v183\n"
"v_cmp_eq_u32_e32 vcc, -1, v255\n"
"s_cbranch_vccz .Lmk_p126k\n"
"s_sub_u32 s1, s1, 1\n"
"s_cbranch_scc0 .Lmk_p126l\n"
".Lmk_p126k:\n"
"v_mfma_f32_16x16x32_bf16 v[116:119], a[128:131], v[148:151], 0\n"
"v_mfma_f32_16x16x32_bf16 v[140:143], a[132:135], v[148:151], 0\n"
"v_mfma_f32_16x16x32_bf16 v[156:159], a[136:139], v[148:151], 0\n"
"v_mfma_f32_16x16x32_bf16 v[160:163], a[140:143], v[148:151], 0\n"
"v_mfma_f32_16x16x32_bf16 v[116:119], a[144:147], v[120:123], v[116:119]\n"
"v_mfma_f32_16x16x32_bf16 v[140:143], a[148:151], v[120:123], v[140:143]\n"
"v_mfma_f32_16x16x32_bf16 v[156:159], a[152:155], v[120:123], v[156:159]\n"
"v_mfma_f32_16x16x32_bf16 v[160:163], a[156:159], v[120:123], v[160:163]\n"
"v_mfma_f32_16x16x32_bf16 v[116:119], a[160:163], v[172:175], v[116:119]\n"
"v_mfma_f32_16x16x32_bf16 v[140:143], a[164:167], v[172:175], v[140:143]\n"
"v_mfma_f32_16x16x32_bf16 v[156:159], a[168:171], v[172:175], v[156:159]\n"
"v_mfma_f32_16x16x32_bf16 v[160:163], a[172:175], v[172:175], v[160:163]\n"
"v_mfma_f32_16x16x32_bf16 v[116:119], a[176:179], v[128:131], v[116:119]\n"
"v_mfma_f32_16x16x32_bf16 v[140:143], a[180:183], v[128:131], v[140:143]\n"
"v_mfma_f32_16x16x32_bf16 v[156:159], a[184:187], v[128:131], v[156:159]\n"
"v_mfma_f32_16x16x32_bf16 v[160:163], a[188:191], v[128:131], v[160:163]\n"
"v_mfma_f32_16x16x32_bf16 v[116:119], a[192:195], v[132:135], v[116:119]\n"
"v_mfma_f32_16x16x32_bf16 v[140:143], a[196:199], v[132:135], v[140:143]\n"
"v_mfma_f32_16x16x32_bf16 v[156:159], a[200:203], v[132:135], v[156:159]\n"
"v_mfma_f32_16x16x32_bf16 v[160:163], a[204:207], v[132:135], v[160:163]\n"
"v_mfma_f32_16x16x32_bf16 v[116:119], a[208:211], v[136:139], v[116:119]\n"
"v_mfma_f32_16x16x32_bf16 v[140:143], a[212:215], v[136:139], v[140:143]\n"
"v_mfma_f32_16x16x32_bf16 v[156:159], a[216:219], v[136:139], v[156:159]\n"
"v_mfma_f32_16x16x32_bf16 v[160:163], a[220:223], v[136:139], v[160:163]\n"
"v_mfma_f32_16x16x32_bf16 v[116:119], a[224:227], v[176:179], v[116:119]\n"
"v_mfma_f32_16x16x32_bf16 v[140:143], a[228:231], v[176:179], v[140:143]\n"
"v_mfma_f32_16x16x32_bf16 v[156:159], a[232:235], v[176:179], v[156:159]\n"
"v_mfma_f32_16x16x32_bf16 v[160:163], a[236:239], v[176:179], v[160:163]\n"
"v_mfma_f32_16x16x32_bf16 v[116:119], a[240:243], v[180:183], v[116:119]\n"
"v_mfma_f32_16x16x32_bf16 v[140:143], a[244:247], v[180:183], v[140:143]\n"
"v_mfma_f32_16x16x32_bf16 v[156:159], a[248:251], v[180:183], v[156:159]\n"
"v_mfma_f32_16x16x32_bf16 v[160:163], a[252:255], v[180:183], v[160:163]\n"
"s_add_u32 s97, s58, 0\n"
"v_mov_b32_e32 v255, 0x80000000\n"
"buffer_load_dwordx4 v[148:151], v0, s[4:7], s97 offen sc1\n"
"buffer_load_dwordx4 v[120:123], v0, s[4:7], s97 offen offset:1024 sc1\n"
"buffer_load_dwordx4 v[172:175], v0, s[4:7], s97 offen offset:2048 sc1\n"
"buffer_load_dwordx4 v[128:131], v0, s[4:7], s97 offen offset:3072 sc1\n"
"buffer_load_dwordx4 v[132:135], v1, s[4:7], s97 offen sc1\n"
"buffer_load_dwordx4 v[136:139], v1, s[4:7], s97 offen offset:1024 sc1\n"
"buffer_load_dwordx4 v[176:179], v1, s[4:7], s97 offen offset:2048 sc1\n"
"buffer_load_dwordx4 v[180:183], v1, s[4:7], s97 offen offset:3072 sc1\n"
"s_nop 7\n"
"s_branch .Lmk_p126r\n"
".Lmk_p127s:\n"
";\n"
"s_mov_b32 s1, 0x10000\n"
".Lmk_p127l:\n"
"s_add_u32 s96, s57, 0\n"
"buffer_load_dwordx4 v[104:107], v0, s[4:7], s96 offen sc1\n"
"buffer_load_dwordx4 v[124:127], v0, s[4:7], s96 offen offset:1024 sc1\n"
"buffer_load_dwordx4 v[200:203], v0, s[4:7], s96 offen offset:2048 sc1\n"
"buffer_load_dwordx4 v[204:207], v0, s[4:7], s96 offen offset:3072 sc1\n"
"buffer_load_dwordx4 v[208:211], v1, s[4:7], s96 offen sc1\n"
"buffer_load_dwordx4 v[212:215], v1, s[4:7], s96 offen offset:1024 sc1\n"
"buffer_load_dwordx4 v[216:219], v1, s[4:7], s96 offen offset:2048 sc1\n"
"buffer_load_dwordx4 v[220:223], v1, s[4:7], s96 offen offset:3072 sc1\n"
"s_add_u32 s96, s58, 0\n"
"buffer_load_dwordx4 v[148:151], v0, s[4:7], s96 offen sc1\n"
"buffer_load_dwordx4 v[120:123], v0, s[4:7], s96 offen offset:1024 sc1\n"
"buffer_load_dwordx4 v[172:175], v0, s[4:7], s96 offen offset:2048 sc1\n"
"buffer_load_dwordx4 v[128:131], v0, s[4:7], s96 offen offset:3072 sc1\n"
"buffer_load_dwordx4 v[132:135], v1, s[4:7], s96 offen sc1\n"
"buffer_load_dwordx4 v[136:139], v1, s[4:7], s96 offen offset:1024 sc1\n"
"buffer_load_dwordx4 v[176:179], v1, s[4:7], s96 offen offset:2048 sc1\n"
"buffer_load_dwordx4 v[180:183], v1, s[4:7], s96 offen offset:3072 sc1\n"
"s_add_u32 s96, s59, 0\n"
"buffer_load_dwordx4 v[188:191], v0, s[4:7], s96 offen sc1\n"
"buffer_load_dwordx4 v[140:143], v0, s[4:7], s96 offen offset:1024 sc1\n"
"buffer_load_dwordx4 v[156:159], v0, s[4:7], s96 offen offset:2048 sc1\n"
"buffer_load_dwordx4 v[160:163], v0, s[4:7], s96 offen offset:3072 sc1\n"
"buffer_load_dwordx4 v[168:171], v1, s[4:7], s96 offen sc1\n"
"buffer_load_dwordx4 v[144:147], v1, s[4:7], s96 offen offset:1024 sc1\n"
"buffer_load_dwordx4 v[152:155], v1, s[4:7], s96 offen offset:2048 sc1\n"
"s_waitcnt vmcnt(0)\n"
"v_max_u32_e32 v244, v104, v105\n"
"v_max3_u32 v244, v244, v106, v107\n"
"v_max3_u32 v244, v244, v124, v125\n"
"v_max3_u32 v244, v244, v126, v127\n"
"v_max3_u32 v244, v244, v200, v201\n"
"v_max3_u32 v244, v244, v202, v203\n"
"v_max3_u32 v244, v244, v204, v205\n"
"v_max3_u32 v244, v244, v206, v207\n"
"v_max3_u32 v244, v244, v208, v209\n"
"v_max3_u32 v244, v244, v210, v211\n"
"v_max3_u32 v244, v244, v212, v213\n"
"v_max3_u32 v244, v244, v214, v215\n"
"v_max3_u32 v244, v244, v216, v217\n"
"v_max3_u32 v244, v244, v218, v219\n"
"v_max3_u32 v244, v244, v220, v221\n"
"v_max3_u32 v244, v244, v222, v223\n"
"v_cmp_eq_u32_e32 vcc, -1, v244\n"
"s_cbranch_vccz .Lmk_p127k\n"
"s_sub_u32 s1, s1, 1\n"
"s_cbranch_scc0 .Lmk_p127l\n"
".Lmk_p127k:\n"
"v_mfma_f32_16x16x32_bf16 v[108:111], a[128:131], v[104:107], 0\n"
"v_mfma_f32_16x16x32_bf16 v[112:115], a[132:135], v[104:107], 0\n"
"v_mfma_f32_16x16x32_bf16 v[184:187], a[136:139], v[104:107], 0\n"
"v_mfma_f32_16x16x32_bf16 v[192:195], a[140:143], v[104:107], 0\n"
"v_mfma_f32_16x16x32_bf16 v[108:111], a[144:147], v[124:127], v[108:111]\n"
"v_mfma_f32_16x16x32_bf16 v[112:115], a[148:151], v[124:127], v[112:115]\n"
"v_mfma_f32_16x16x32_bf16 v[184:187], a[152:155], v[124:127], v[184:187]\n"
"v_mfma_f32_16x16x32_bf16 v[192:195], a[156:159], v[124:127], v[192:195]\n"
"v_mfma_f32_16x16x32_bf16 v[108:111], a[160:163], v[200:203], v[108:111]\n"
"v_mfma_f32_16x16x32_bf16 v[112:115], a[164:167], v[200:203], v[112:115]\n"
"v_mfma_f32_16x16x32_bf16 v[184:187], a[168:171], v[200:203], v[184:187]\n"
"v_mfma_f32_16x16x32_bf16 v[192:195], a[172:175], v[200:203], v[192:195]\n"
"v_mfma_f32_16x16x32_bf16 v[108:111], a[176:179], v[204:207], v[108:111]\n"
"v_mfma_f32_16x16x32_bf16 v[112:115], a[180:183], v[204:207], v[112:115]\n"
"v_mfma_f32_16x16x32_bf16 v[184:187], a[184:187], v[204:207], v[184:187]\n"
"v_mfma_f32_16x16x32_bf16 v[192:195], a[188:191], v[204:207], v[192:195]\n"
"v_mfma_f32_16x16x32_bf16 v[108:111], a[192:195], v[208:211], v[108:111]\n"
"v_mfma_f32_16x16x32_bf16 v[112:115], a[196:199], v[208:211], v[112:115]\n"
"v_mfma_f32_16x16x32_bf16 v[184:187], a[200:203], v[208:211], v[184:187]\n"
"v_mfma_f32_16x16x32_bf16 v[192:195], a[204:207], v[208:211], v[192:195]\n"
"v_mfma_f32_16x16x32_bf16 v[108:111], a[208:211], v[212:215], v[108:111]\n"
"v_mfma_f32_16x16x32_bf16 v[112:115], a[212:215], v[212:215], v[112:115]\n"
"v_mfma_f32_16x16x32_bf16 v[184:187], a[216:219], v[212:215], v[184:187]\n"
"v_mfma_f32_16x16x32_bf16 v[192:195], a[220:223], v[212:215], v[192:195]\n"
"v_mfma_f32_16x16x32_bf16 v[108:111], a[224:227], v[216:219], v[108:111]\n"
"v_mfma_f32_16x16x32_bf16 v[112:115], a[228:231], v[216:219], v[112:115]\n"
"v_mfma_f32_16x16x32_bf16 v[184:187], a[232:235], v[216:219], v[184:187]\n"
"v_mfma_f32_16x16x32_bf16 v[192:195], a[236:239], v[216:219], v[192:195]\n"
"v_mfma_f32_16x16x32_bf16 v[108:111], a[240:243], v[220:223], v[108:111]\n"
"v_mfma_f32_16x16x32_bf16 v[112:115], a[244:247], v[220:223], v[112:115]\n"
"v_mfma_f32_16x16x32_bf16 v[184:187], a[248:251], v[220:223], v[184:187]\n"
"v_mfma_f32_16x16x32_bf16 v[192:195], a[252:255], v[220:223], v[192:195]\n"
"s_nop 7\n"
"s_branch .Lmk_p127r\n"
".Lmk_q129s:\n"
";\n"
"s_mov_b32 s1, 0x10000\n"
".Lmk_q129l:\n"
"s_add_u32 s98, s59, 0\n"
"buffer_load_dwordx4 v[188:191], v0, s[4:7], s98 offen sc1\n"
"buffer_load_dwordx4 v[140:143], v0, s[4:7], s98 offen offset:1024 sc1\n"
"buffer_load_dwordx4 v[156:159], v0, s[4:7], s98 offen offset:2048 sc1\n"
"buffer_load_dwordx4 v[160:163], v0, s[4:7], s98 offen offset:3072 sc1\n"
"buffer_load_dwordx4 v[168:171], v1, s[4:7], s98 offen sc1\n"
"buffer_load_dwordx4 v[144:147], v1, s[4:7], s98 offen offset:1024 sc1\n"
"buffer_load_dwordx4 v[152:155], v1, s[4:7], s98 offen offset:2048 sc1\n"
"buffer_load_dwordx4 v[204:207], v1, s[4:7], s98 offen offset:3072 sc1\n"
"s_add_u32 s98, s52, 0x440000\n"
"buffer_load_dwordx4 v[104:107], v0, s[4:7], s98 offen sc1\n"
"buffer_load_dwordx4 v[108:111], v0, s[4:7], s98 offen offset:1024 sc1\n"
"buffer_load_dwordx4 v[112:115], v0, s[4:7], s98 offen offset:2048 sc1\n"
"buffer_load_dwordx4 v[116:119], v0, s[4:7], s98 offen offset:3072 sc1\n"
"buffer_load_dwordx4 v[120:123], v1, s[4:7], s98 offen sc1\n"
"s_waitcnt vmcnt(0)\n"
"v_max_u32_e32 v255, v188, v189\n"
"v_max3_u32 v255, v255, v190, v191\n"
"v_max3_u32 v255, v255, v140, v141\n"
"v_max3_u32 v255, v255, v142, v143\n"
"v_max3_u32 v255, v255, v156, v157\n"
"v_max3_u32 v255, v255, v158, v159\n"
"v_max3_u32 v255, v255, v160, v161\n"
"v_max3_u32 v255, v255, v162, v163\n"
"v_cmp_eq_u32_e32 vcc, -1, v255\n"
"s_cbranch_vccz .Lmk_q129k\n"
"s_sub_u32 s1, s1, 1\n"
"s_cbranch_scc0 .Lmk_q129l\n"
".Lmk_q129k:\n"
"s_branch .Lmk_q129r\n"
".Lmk_p128s:\n"
";\n"
"s_mov_b32 s1, 0x10000\n"
".Lmk_p128l:\n"
"s_add_u32 s98, s58, 0\n"
"buffer_load_dwordx4 v[124:127], v0, s[4:7], s98 offen sc1\n"
"buffer_load_dwordx4 v[128:131], v0, s[4:7], s98 offen offset:1024 sc1\n"
"buffer_load_dwordx4 v[132:135], v0, s[4:7], s98 offen offset:2048 sc1\n"
"buffer_load_dwordx4 v[136:139], v0, s[4:7], s98 offen offset:3072 sc1\n"
"buffer_load_dwordx4 v[148:151], v1, s[4:7], s98 offen sc1\n"
"buffer_load_dwordx4 v[172:175], v1, s[4:7], s98 offen offset:1024 sc1\n"
"buffer_load_dwordx4 v[176:179], v1, s[4:7], s98 offen offset:2048 sc1\n"
"buffer_load_dwordx4 v[180:183], v1, s[4:7], s98 offen offset:3072 sc1\n"
"s_add_u32 s98, s59, 0\n"
"buffer_load_dwordx4 v[188:191], v0, s[4:7], s98 offen sc1\n"
"buffer_load_dwordx4 v[140:143], v0, s[4:7], s98 offen offset:1024 sc1\n"
"buffer_load_dwordx4 v[156:159], v0, s[4:7], s98 offen offset:2048 sc1\n"
"buffer_load_dwordx4 v[160:163], v0, s[4:7], s98 offen offset:3072 sc1\n"
"buffer_load_dwordx4 v[168:171], v1, s[4:7], s98 offen sc1\n"
"buffer_load_dwordx4 v[144:147], v1, s[4:7], s98 offen offset:1024 sc1\n"
"buffer_load_dwordx4 v[152:155], v1, s[4:7], s98 offen offset:2048 sc1\n"
"buffer_load_dwordx4 v[204:207], v1, s[4:7], s98 offen offset:3072 sc1\n"
"s_add_u32 s98, s52, 0x440000\n"
"buffer_load_dwordx4 v[104:107], v0, s[4:7], s98 offen sc1\n"
"buffer_load_dwordx4 v[108:111], v0, s[4:7], s98 offen offset:1024 sc1\n"
"buffer_load_dwordx4 v[112:115], v0, s[4:7], s98 offen offset:2048 sc1\n"
"buffer_load_dwordx4 v[116:119], v0, s[4:7], s98 offen offset:3072 sc1\n"
"buffer_load_dwordx4 v[120:123], v1, s[4:7], s98 offen sc1\n"
"s_waitcnt vmcnt(0)\n"
"v_max_u32_e32 v255, v124, v125\n"
"v_max3_u32 v255, v255, v126, v127\n"
"v_max3_u32 v255, v255, v128, v129\n"
"v_max3_u32 v255, v255, v130, v131\n"
"v_max3_u32 v255, v255, v132, v133\n"
"v_max3_u32 v255, v255, v134, v135\n"
"v_max3_u32 v255, v255, v136, v137\n"
"v_max3_u32 v255, v255, v138, v139\n"
"v_max3_u32 v255, v255, v148, v149\n"
"v_max3_u32 v255, v255, v150, v151\n"
"v_max3_u32 v255, v255, v172, v173\n"
"v_max3_u32 v255, v255, v174, v175\n"
"v_max3_u32 v255, v255, v176, v177\n"
"v_max3_u32 v255, v255, v178, v179\n"
"v_max3_u32 v255, v255, v180, v181\n"
"v_max3_u32 v255, v255, v182, v183\n"
"v_cmp_eq_u32_e32 vcc, -1, v255\n"
"s_cbranch_vccz .Lmk_p128k\n"
"s_sub_u32 s1, s1, 1\n"
"s_cbranch_scc0 .Lmk_p128l\n"
".Lmk_p128k:\n"
"v_mfma_f32_16x16x32_bf16 v[164:167], a[128:131], v[124:127], 0\n"
"v_mfma_f32_16x16x32_bf16 v[196:199], a[132:135], v[124:127], 0\n"
"v_mfma_f32_16x16x32_bf16 v[200:203], a[136:139], v[124:127], 0\n"
"v_mfma_f32_16x16x32_bf16 v[208:211], a[140:143], v[124:127], 0\n"
"v_mfma_f32_16x16x32_bf16 v[164:167], a[144:147], v[128:131], v[164:167]\n"
"v_mfma_f32_16x16x32_bf16 v[196:199], a[148:151], v[128:131], v[196:199]\n"
"v_mfma_f32_16x16x32_bf16 v[200:203], a[152:155], v[128:131], v[200:203]\n"
"v_mfma_f32_16x16x32_bf16 v[208:211], a[156:159], v[128:131], v[208:211]\n"
"v_mfma_f32_16x16x32_bf16 v[164:167], a[160:163], v[132:135], v[164:167]\n"
"v_mfma_f32_16x16x32_bf16 v[196:199], a[164:167], v[132:135], v[196:199]\n"
"v_mfma_f32_16x16x32_bf16 v[200:203], a[168:171], v[132:135], v[200:203]\n"
"v_mfma_f32_16x16x32_bf16 v[208:211], a[172:175], v[132:135], v[208:211]\n"
"v_mfma_f32_16x16x32_bf16 v[164:167], a[176:179], v[136:139], v[164:167]\n"
"v_mfma_f32_16x16x32_bf16 v[196:199], a[180:183], v[136:139], v[196:199]\n"
"v_mfma_f32_16x16x32_bf16 v[200:203], a[184:187], v[136:139], v[200:203]\n"
"v_mfma_f32_16x16x32_bf16 v[208:211], a[188:191], v[136:139], v[208:211]\n"
"v_mfma_f32_16x16x32_bf16 v[164:167], a[192:195], v[148:151], v[164:167]\n"
"v_mfma_f32_16x16x32_bf16 v[196:199], a[196:199], v[148:151], v[196:199]\n"
"v_mfma_f32_16x16x32_bf16 v[200:203], a[200:203], v[148:151], v[200:203]\n"
"v_mfma_f32_16x16x32_bf16 v[208:211], a[204:207], v[148:151], v[208:211]\n"
"v_mfma_f32_16x16x32_bf16 v[164:167], a[208:211], v[172:175], v[164:167]\n"
"v_mfma_f32_16x16x32_bf16 v[196:199], a[212:215], v[172:175], v[196:199]\n"
"v_mfma_f32_16x16x32_bf16 v[200:203], a[216:219], v[172:175], v[200:203]\n"
"v_mfma_f32_16x16x32_bf16 v[208:211], a[220:223], v[172:175], v[208:211]\n"
"v_mfma_f32_16x16x32_bf16 v[164:167], a[224:227], v[176:179], v[164:167]\n"
"v_mfma_f32_16x16x32_bf16 v[196:199], a[228:231], v[176:179], v[196:199]\n"
"v_mfma_f32_16x16x32_bf16 v[200:203], a[232:235], v[176:179], v[200:203]\n"
"v_mfma_f32_16x16x32_bf16 v[208:211], a[236:239], v[176:179], v[208:211]\n"
"v_mfma_f32_16x16x32_bf16 v[164:167], a[240:243], v[180:183], v[164:167]\n"
"v_mfma_f32_16x16x32_bf16 v[196:199], a[244:247], v[180:183], v[196:199]\n"
"v_mfma_f32_16x16x32_bf16 v[200:203], a[248:251], v[180:183], v[200:203]\n"
"v_mfma_f32_16x16x32_bf16 v[208:211], a[252:255], v[180:183], v[208:211]\n"
"s_nop 7\n"
"s_branch .Lmk_p128r\n"
".Lmk_q130s:\n"
";\n"
"s_mov_b32 s1, 0x10000\n"
".Lmk_q130l:\n"
"s_add_u32 s98, s59, 0\n"
"buffer_load_dwordx4 v[168:171], v1, s[4:7], s98 offen sc1\n"
"buffer_load_dwordx4 v[144:147], v1, s[4:7], s98 offen offset:1024 sc1\n"
"buffer_load_dwordx4 v[152:155], v1, s[4:7], s98 offen offset:2048 sc1\n"
"buffer_load_dwordx4 v[204:207], v1, s[4:7], s98 offen offset:3072 sc1\n"
"s_add_u32 s98, s52, 0x440000\n"
"buffer_load_dwordx4 v[104:107], v0, s[4:7], s98 offen sc1\n"
"buffer_load_dwordx4 v[108:111], v0, s[4:7], s98 offen offset:1024 sc1\n"
"buffer_load_dwordx4 v[112:115], v0, s[4:7], s98 offen offset:2048 sc1\n"
"buffer_load_dwordx4 v[116:119], v0, s[4:7], s98 offen offset:3072 sc1\n"
"buffer_load_dwordx4 v[120:123], v1, s[4:7], s98 offen sc1\n"
"buffer_load_dwordx4 v[124:127], v1, s[4:7], s98 offen offset:1024 sc1\n"
"s_waitcnt vmcnt(0)\n"
"v_max_u32_e32 v255, v168, v169\n"
"v_max3_u32 v255, v255, v170, v171\n"
"v_max3_u32 v255, v255, v144, v145\n"
"v_max3_u32 v255, v255, v146, v147\n"
"v_max3_u32 v255, v255, v152, v153\n"
"v_max3_u32 v255, v255, v154, v155\n"
"v_max3_u32 v255, v255, v204, v205\n"
"v_max3_u32 v255, v255, v206, v207\n"
"v_cmp_eq_u32_e32 vcc, -1, v255\n"
"s_cbranch_vccz .Lmk_q130k\n"
"s_sub_u32 s1, s1, 1\n"
"s_cbranch_scc0 .Lmk_q130l\n"
".Lmk_q130k:\n"
"s_branch .Lmk_q130r\n"
".Lmk_exit:\n"
"v_xor_b32_e32 v255, 32, v15\n"
"ds_read_b128 v[24:27], v2 offset:17408\n"
"ds_read_b128 v[28:31], v15 offset:16384\n"
"ds_read_b128 v[32:35], v255 offset:16640\n"
"v_xor_b32_e32 v255, 32, v17\n"
"ds_read_b128 v[36:39], v2 offset:16384\n"
"ds_read_b128 v[40:43], v17 offset:15360\n"
"ds_read_b128 v[60:63], v255 offset:15616\n"
"v_xor_b32_e32 v255, 32, v16\n"
"ds_read_b64_tr_b16 v[64:65], v16 offset:16384\n"
"ds_read_b64_tr_b16 v[66:67], v255 offset:16640\n"
"ds_read_b64_tr_b16 v[68:69], v255 offset:16384\n"
"ds_read_b64_tr_b16 v[70:71], v16 offset:16640\n"
"v_mov_b32_e32 v72, 0x3f803f80\n"
"v_mov_b32_e32 v73, 0x3f803f80\n"
"v_mov_b32_e32 v74, 0x3f803f80\n"
"v_mov_b32_e32 v75, 0x3f803f80\n"
"v_xor_b32_e32 v255, 32, v16\n"
"ds_read_b64_tr_b16 v[76:77], v16 offset:15360\n"
"ds_read_b64_tr_b16 v[78:79], v255 offset:15616\n"
"ds_read_b64_tr_b16 v[80:81], v255 offset:15360\n"
"ds_read_b64_tr_b16 v[82:83], v16 offset:15616\n"
"v_mov_b32_e32 v84, 0x3f803f80\n"
"v_mov_b32_e32 v85, 0x3f803f80\n"
"v_mov_b32_e32 v86, 0x3f803f80\n"
"v_mov_b32_e32 v87, 0x3f803f80\n"
"s_waitcnt lgkmcnt(11)\n"
"v_mfma_f32_16x16x32_bf16 v[44:47], v[28:31], v[24:27], v[44:47]\n"
"v_mfma_f32_16x16x32_bf16 v[48:51], v[32:35], v[24:27], v[48:51]\n"
"s_waitcnt lgkmcnt(8)\n"
"v_mfma_f32_16x16x32_bf16 v[52:55], v[40:43], v[36:39], v[52:55]\n"
"v_mfma_f32_16x16x32_bf16 v[56:59], v[60:63], v[36:39], v[56:59]\n"
"s_add_u32 s98, s56, 0x440000\n"
"buffer_load_dwordx4 v[24:27], v0, s[4:7], s98 offen sc1\n"
"buffer_load_dwordx4 v[28:31], v0, s[4:7], s98 offen offset:1024 sc1\n"
"buffer_load_dwordx4 v[32:35], v0, s[4:7], s98 offen offset:2048 sc1\n"
"buffer_load_dwordx4 v[36:39], v0, s[4:7], s98 offen offset:3072 sc1\n"
"v_max_f32_e32 v255, v44, v45\n"
"s_nop 0\n"
"v_max_f32_e32 v254, v52, v53\n"
"v_max3_f32 v255, v255, v46, v47\n"
"v_max3_f32 v254, v254, v54, v55\n"
"v_max3_f32 v255, v255, v48, v49\n"
"v_max3_f32 v254, v254, v56, v57\n"
"v_max3_f32 v255, v255, v50, v51\n"
"v_max3_f32 v254, v254, v58, v59\n"
"v_mov_b32_e32 v253, v255\n"
"v_mov_b32_e32 v252, v254\n"
"s_nop 0\n"
"v_permlane16_swap_b32_e32 v255, v253\n"
"v_permlane16_swap_b32_e32 v254, v252\n"
"v_max_f32_e32 v255, v255, v253\n"
"v_max_f32_e32 v254, v254, v252\n"
"v_mov_b32_e32 v253, v255\n"
"v_mov_b32_e32 v252, v254\n"
"s_nop 0\n"
"v_permlane32_swap_b32_e32 v255, v253\n"
"v_permlane32_swap_b32_e32 v254, v252\n"
"v_max_f32_e32 v255, v255, v253\n"
"v_max_f32_e32 v254, v254, v252\n"
"buffer_load_dwordx4 v[40:43], v1, s[4:7], s98 offen sc1\n"
"v_sub_f32_e32 v44, v44, v255\n"
"v_sub_f32_e32 v45, v45, v255\n"
"v_sub_f32_e32 v46, v46, v255\n"
"v_sub_f32_e32 v47, v47, v255\n"
"v_sub_f32_e32 v48, v48, v255\n"
"v_sub_f32_e32 v49, v49, v255\n"
"v_sub_f32_e32 v50, v50, v255\n"
"buffer_load_dwordx4 v[60:63], v1, s[4:7], s98 offen offset:1024 sc1\n"
"v_sub_f32_e32 v51, v51, v255\n"
"v_exp_f32_e32 v44, v44\n"
"v_exp_f32_e32 v45, v45\n"
"v_exp_f32_e32 v46, v46\n"
"buffer_load_dwordx4 v[88:91], v1, s[4:7], s98 offen offset:2048 sc1\n"
"v_exp_f32_e32 v47, v47\n"
"v_exp_f32_e32 v48, v48\n"
"v_exp_f32_e32 v49, v49\n"
"v_exp_f32_e32 v50, v50\n"
"buffer_load_dwordx4 v[92:95], v1, s[4:7], s98 offen offset:3072 sc1\n"
"v_exp_f32_e32 v51, v51\n"
"v_cvt_pk_bf16_f32 v96, v44, v45\n"
"v_cvt_pk_bf16_f32 v97, v46, v47\n"
"v_cvt_pk_bf16_f32 v98, v48, v49\n"
"v_cvt_pk_bf16_f32 v99, v50, v51\n"
"s_add_u32 s98, s57, 0x440000\n"
"buffer_load_dwordx4 v[100:103], v0, s[4:7], s98 offen sc1\n"
"s_waitcnt lgkmcnt(4)\n"
"v_sub_f32_e32 v52, v52, v254\n"
"v_sub_f32_e32 v53, v53, v254\n"
"v_sub_f32_e32 v54, v54, v254\n"
"v_sub_f32_e32 v55, v55, v254\n"
"v_sub_f32_e32 v56, v56, v254\n"
"v_sub_f32_e32 v57, v57, v254\n"
"v_sub_f32_e32 v58, v58, v254\n"
"v_mfma_f32_16x16x32_bf16 v[104:107], v[72:75], v[96:99], 0\n"
"buffer_load_dwordx4 v[72:75], v0, s[4:7], s98 offen offset:1024 sc1\n"
"v_sub_f32_e32 v59, v59, v254\n"
"v_exp_f32_e32 v52, v52\n"
"v_exp_f32_e32 v53, v53\n"
"v_exp_f32_e32 v54, v54\n"
"v_mfma_f32_16x16x32_bf16 v[108:111], v[64:67], v[96:99], 0\n"
"buffer_load_dwordx4 v[64:67], v0, s[4:7], s98 offen offset:2048 sc1\n"
"v_exp_f32_e32 v55, v55\n"
"v_exp_f32_e32 v56, v56\n"
"v_exp_f32_e32 v57, v57\n"
"v_exp_f32_e32 v58, v58\n"
"buffer_load_dwordx4 v[112:115], v0, s[4:7], s98 offen offset:3072 sc1\n"
"v_mfma_f32_16x16x32_bf16 v[116:119], v[68:71], v[96:99], 0\n"
"v_exp_f32_e32 v59, v59\n"
"v_cvt_pk_bf16_f32 v68, v52, v53\n"
"v_cvt_pk_bf16_f32 v69, v54, v55\n"
"v_cvt_pk_bf16_f32 v70, v56, v57\n"
"v_cvt_pk_bf16_f32 v71, v58, v59\n"
"buffer_load_dwordx4 v[96:99], v1, s[4:7], s98 offen sc1\n"
"s_waitcnt lgkmcnt(0)\n"
"v_rcp_f32_e32 v104, v104\n"
"s_nop 0\n"
"v_mul_f32_e32 v108, v108, v104\n"
"v_mul_f32_e32 v109, v109, v104\n"
"v_mfma_f32_16x16x32_bf16 v[120:123], v[84:87], v[68:71], 0\n"
"buffer_load_dwordx4 v[84:87], v1, s[4:7], s98 offen offset:1024 sc1\n"
"v_mul_f32_e32 v110, v110, v104\n"
"v_mul_f32_e32 v111, v111, v104\n"
"v_mul_f32_e32 v116, v116, v104\n"
"v_mfma_f32_16x16x32_bf16 v[124:127], v[76:79], v[68:71], 0\n"
"buffer_load_dwordx4 v[76:79], v1, s[4:7], s98 offen offset:2048 sc1\n"
"v_mul_f32_e32 v117, v117, v104\n"
"v_mul_f32_e32 v118, v118, v104\n"
"v_mul_f32_e32 v119, v119, v104\n"
"v_cvt_pk_bf16_f32 v104, v108, v109\n"
"buffer_load_dwordx4 v[128:131], v1, s[4:7], s98 offen offset:3072 sc1\n"
"v_mfma_f32_16x16x32_bf16 v[132:135], v[80:83], v[68:71], 0\n"
"v_cvt_pk_bf16_f32 v105, v110, v111\n"
"v_cvt_pk_bf16_f32 v106, v116, v117\n"
"v_cvt_pk_bf16_f32 v107, v118, v119\n"
"s_add_u32 s98, s60, 0x408000\n"
"buffer_store_dwordx4 v[104:107], v0, s[4:7], s98 offen sc1\n"
"v_rcp_f32_e32 v120, v120\n"
"s_nop 0\n"
"v_mul_f32_e32 v124, v124, v120\n"
"v_mul_f32_e32 v125, v125, v120\n"
"v_mul_f32_e32 v126, v126, v120\n"
"v_mul_f32_e32 v127, v127, v120\n"
"v_mul_f32_e32 v132, v132, v120\n"
"v_mul_f32_e32 v133, v133, v120\n"
"v_mul_f32_e32 v134, v134, v120\n"
"v_mul_f32_e32 v135, v135, v120\n"
"v_cvt_pk_bf16_f32 v68, v124, v125\n"
"v_cvt_pk_bf16_f32 v69, v126, v127\n"
"v_cvt_pk_bf16_f32 v70, v132, v133\n"
"v_cvt_pk_bf16_f32 v71, v134, v135\n"
"s_add_u32 s98, s60, 0x400000\n"
"buffer_store_dwordx4 v[68:71], v0, s[4:7], s98 offen sc1\n"
"s_waitcnt vmcnt(17)\n"
"; @B G2 b.0\n"
"s_nop 0\n"
"v_mfma_f32_16x16x32_bf16 v[68:71], a[128:131], v[24:27], 0\n"
"s_add_u32 s98, s58, 0x440000\n"
"v_mfma_f32_16x16x32_bf16 v[80:83], a[132:135], v[24:27], 0\n"
"buffer_load_dwordx4 v[104:107], v0, s[4:7], s98 offen sc1\n"
"v_mfma_f32_16x16x32_bf16 v[108:111], a[136:139], v[24:27], 0\n"
"v_mfma_f32_16x16x32_bf16 v[116:119], a[140:143], v[24:27], 0\n"
"s_waitcnt vmcnt(17)\n"
"v_mfma_f32_16x16x32_bf16 v[68:71], a[144:147], v[28:31], v[68:71]\n"
"v_mfma_f32_16x16x32_bf16 v[80:83], a[148:151], v[28:31], v[80:83]\n"
"buffer_load_dwordx4 v[24:27], v0, s[4:7], s98 offen offset:1024 sc1\n"
"v_mfma_f32_16x16x32_bf16 v[108:111], a[152:155], v[28:31], v[108:111]\n"
"v_mfma_f32_16x16x32_bf16 v[116:119], a[156:159], v[28:31], v[116:119]\n"
"s_waitcnt vmcnt(17)\n"
"v_mfma_f32_16x16x32_bf16 v[68:71], a[160:163], v[32:35], v[68:71]\n"
"v_mfma_f32_16x16x32_bf16 v[80:83], a[164:167], v[32:35], v[80:83]\n"
"buffer_load_dwordx4 v[28:31], v0, s[4:7], s98 offen offset:2048 sc1\n"
"v_mfma_f32_16x16x32_bf16 v[108:111], a[168:171], v[32:35], v[108:111]\n"
"v_mfma_f32_16x16x32_bf16 v[116:119], a[172:175], v[32:35], v[116:119]\n"
"s_waitcnt vmcnt(17)\n"
"v_mfma_f32_16x16x32_bf16 v[68:71], a[176:179], v[36:39], v[68:71]\n"
"v_mfma_f32_16x16x32_bf16 v[80:83], a[180:183], v[36:39], v[80:83]\n"
"buffer_load_dwordx4 v[32:35], v0, s[4:7], s98 offen offset:3072 sc1\n"
"v_mfma_f32_16x16x32_bf16 v[108:111], a[184:187], v[36:39], v[108:111]\n"
"v_mfma_f32_16x16x32_bf16 v[116:119], a[188:191], v[36:39], v[116:119]\n"
"s_waitcnt vmcnt(17)\n"
"v_mfma_f32_16x16x32_bf16 v[68:71], a[192:195], v[40:43], v[68:71]\n"
"v_mfma_f32_16x16x32_bf16 v[80:83], a[196:199], v[40:43], v[80:83]\n"
"buffer_load_dwordx4 v[36:39], v1, s[4:7], s98 offen sc1\n"
"v_mfma_f32_16x16x32_bf16 v[108:111], a[200:203], v[40:43], v[108:111]\n"
"v_mfma_f32_16x16x32_bf16 v[116:119], a[204:207], v[40:43], v[116:119]\n"
"s_waitcnt vmcnt(17)\n"
"v_mfma_f32_16x16x32_bf16 v[68:71], a[208:211], v[60:63], v[68:71]\n"
"v_mfma_f32_16x16x32_bf16 v[80:83], a[212:215], v[60:63], v[80:83]\n"
"buffer_load_dwordx4 v[40:43], v1, s[4:7], s98 offen offset:1024 sc1\n"
"v_mfma_f32_16x16x32_bf16 v[108:111], a[216:219], v[60:63], v[108:111]\n"
"v_mfma_f32_16x16x32_bf16 v[116:119], a[220:223], v[60:63], v[116:119]\n"
"s_waitcnt vmcnt(17)\n"
"v_mfma_f32_16x16x32_bf16 v[68:71], a[224:227], v[88:91], v[68:71]\n"
"v_mfma_f32_16x16x32_bf16 v[80:83], a[228:231], v[88:91], v[80:83]\n"
"buffer_load_dwordx4 v[60:63], v1, s[4:7], s98 offen offset:2048 sc1\n"
"v_mfma_f32_16x16x32_bf16 v[108:111], a[232:235], v[88:91], v[108:111]\n"
"v_mfma_f32_16x16x32_bf16 v[116:119], a[236:239], v[88:91], v[116:119]\n"
"s_waitcnt vmcnt(17)\n"
"v_mfma_f32_16x16x32_bf16 v[68:71], a[240:243], v[92:95], v[68:71]\n"
"v_mfma_f32_16x16x32_bf16 v[80:83], a[244:247], v[92:95], v[80:83]\n"
"buffer_load_dwordx4 v[88:91], v1, s[4:7], s98 offen offset:3072 sc1\n"
"v_mfma_f32_16x16x32_bf16 v[108:111], a[248:251], v[92:95], v[108:111]\n"
"v_mfma_f32_16x16x32_bf16 v[116:119], a[252:255], v[92:95], v[116:119]\n"
"s_waitcnt vmcnt(16)\n"
"s_add_u32 s98, s59, 0x440000\n"
"; @B G2 b.1\n"
"v_mfma_f32_16x16x32_bf16 v[92:95], a[128:131], v[100:103], 0\n"
"s_nop 0\n"
"v_cmp_u_f32_e32 vcc, v68, v68\n"
"v_mfma_f32_16x16x32_bf16 v[120:123], a[132:135], v[100:103], 0\n"
"s_cbranch_vccnz .Lmk_p64s\n"
".Lmk_p64r:\n"
"v_mfma_f32_16x16x32_bf16 v[124:127], a[136:139], v[100:103], 0\n"
"buffer_load_dwordx4 v[132:135], v0, s[4:7], s98 offen sc1\n"
"v_mfma_f32_16x16x32_bf16 v[136:139], a[140:143], v[100:103], 0\n"
"v_mfma_f32_16x16x32_bf16 v[92:95], a[144:147], v[72:75], v[92:95]\n"
"ds_write_b128 v3, v[68:71]\n"
"v_mfma_f32_16x16x32_bf16 v[120:123], a[148:151], v[72:75], v[120:123]\n"
"v_mfma_f32_16x16x32_bf16 v[124:127], a[152:155], v[72:75], v[124:127]\n"
"ds_write_b128 v3, v[80:83] offset:1024\n"
"v_mfma_f32_16x16x32_bf16 v[136:139], a[156:159], v[72:75], v[136:139]\n"
"s_waitcnt vmcnt(15)\n"
"v_mfma_f32_16x16x32_bf16 v[92:95], a[160:163], v[64:67], v[92:95]\n"
"ds_write_b128 v3, v[108:111] offset:2048\n"
"v_mfma_f32_16x16x32_bf16 v[120:123], a[164:167], v[64:67], v[120:123]\n"
"buffer_load_dwordx4 v[68:71], v0, s[4:7], s98 offen offset:1024 sc1\n"
"v_mfma_f32_16x16x32_bf16 v[124:127], a[168:171], v[64:67], v[124:127]\n"
"ds_write_b128 v3, v[116:119] offset:3072\n"
"v_mfma_f32_16x16x32_bf16 v[136:139], a[172:175], v[64:67], v[136:139]\n"
"v_mfma_f32_16x16x32_bf16 v[92:95], a[176:179], v[112:115], v[92:95]\n"
"v_mfma_f32_16x16x32_bf16 v[120:123], a[180:183], v[112:115], v[120:123]\n"
"buffer_load_dwordx4 v[64:67], v0, s[4:7], s98 offen offset:2048 sc1\n"
"v_mfma_f32_16x16x32_bf16 v[124:127], a[184:187], v[112:115], v[124:127]\n"
"ds_read_b128 v[72:75], v2 offset:8192\n"
"ds_read_b128 v[80:83], v2 offset:9216\n"
"v_mfma_f32_16x16x32_bf16 v[136:139], a[188:191], v[112:115], v[136:139]\n"
"s_waitcnt vmcnt(15)\n"
"v_mfma_f32_16x16x32_bf16 v[92:95], a[192:195], v[96:99], v[92:95]\n"
"v_mfma_f32_16x16x32_bf16 v[120:123], a[196:199], v[96:99], v[120:123]\n"
"buffer_load_dwordx4 v[100:103], v0, s[4:7], s98 offen offset:3072 sc1\n"
"v_mfma_f32_16x16x32_bf16 v[124:127], a[200:203], v[96:99], v[124:127]\n"
"v_mfma_f32_16x16x32_bf16 v[136:139], a[204:207], v[96:99], v[136:139]\n"
"s_waitcnt lgkmcnt(0)\n"
"s_barrier\n"
"v_mfma_f32_16x16x32_bf16 v[92:95], a[208:211], v[84:87], v[92:95]\n"
"v_lshlrev_b32_e32 v255, 16, v72\n"
"v_mfma_f32_16x16x32_bf16 v[120:123], a[212:215], v[84:87], v[120:123]\n"
"buffer_load_dwordx4 v[96:99], v1, s[4:7], s98 offen sc1\n"
"v_mfma_f32_16x16x32_bf16 v[124:127], a[216:219], v[84:87], v[124:127]\n"
"ds_read_b128 v[108:111], v4\n"
"ds_read_b128 v[112:115], v4 offset:1024\n"
"v_mfma_f32_16x16x32_bf16 v[136:139], a[220:223], v[84:87], v[136:139]\n"
"s_waitcnt vmcnt(15)\n"
"ds_read_b128 v[84:87], v4 offset:2048\n"
"v_mfma_f32_16x16x32_bf16 v[92:95], a[224:227], v[76:79], v[92:95]\n"
"ds_read_b128 v[116:119], v4 offset:3072\n"
"v_and_b32_e32 v254, 0xffff0000, v72\n"
"v_mfma_f32_16x16x32_bf16 v[120:123], a[228:231], v[76:79], v[120:123]\n"
"buffer_load_dwordx4 v[140:143], v1, s[4:7], s98 offen offset:1024 sc1\n"
"v_mfma_f32_16x16x32_bf16 v[124:127], a[232:235], v[76:79], v[124:127]\n"
"v_lshlrev_b32_e32 v253, 16, v73\n"
"v_and_b32_e32 v252, 0xffff0000, v73\n"
"v_mfma_f32_16x16x32_bf16 v[136:139], a[236:239], v[76:79], v[136:139]\n"
"v_lshlrev_b32_e32 v251, 16, v74\n"
"v_and_b32_e32 v250, 0xffff0000, v74\n"
"v_mfma_f32_16x16x32_bf16 v[92:95], a[240:243], v[128:131], v[92:95]\n"
"v_lshlrev_b32_e32 v249, 16, v75\n"
"v_and_b32_e32 v248, 0xffff0000, v75\n"
"v_mfma_f32_16x16x32_bf16 v[120:123], a[244:247], v[128:131], v[120:123]\n"
"buffer_load_dwordx4 v[72:75], v1, s[4:7], s98 offen offset:2048 sc1\n"
"v_mfma_f32_16x16x32_bf16 v[124:127], a[248:251], v[128:131], v[124:127]\n"
"v_lshlrev_b32_e32 v247, 16, v80\n"
"v_and_b32_e32 v246, 0xffff0000, v80\n"
"v_mfma_f32_16x16x32_bf16 v[136:139], a[252:255], v[128:131], v[136:139]\n"
"s_waitcnt vmcnt(13)\n"
"v_lshlrev_b32_e32 v245, 16, v81\n"
"; @B G2 b.2\n"
"v_mfma_f32_16x16x32_bf16 v[76:79], a[128:131], v[104:107], 0\n"
"v_cmp_u_f32_e32 vcc, v92, v92\n"
"v_and_b32_e32 v244, 0xffff0000, v81\n"
"v_mfma_f32_16x16x32_bf16 v[128:131], a[132:135], v[104:107], 0\n"
"s_cbranch_vccnz .Lmk_p65s\n"
".Lmk_p65r:\n"
"v_lshlrev_b32_e32 v243, 16, v82\n"
"v_mfma_f32_16x16x32_bf16 v[144:147], a[136:139], v[104:107], 0\n"
"buffer_load_dwordx4 v[148:151], v1, s[4:7], s98 offen offset:3072 sc1\n"
"v_mfma_f32_16x16x32_bf16 v[152:155], a[140:143], v[104:107], 0\n"
"v_and_b32_e32 v242, 0xffff0000, v82\n"
"v_lshlrev_b32_e32 v241, 16, v83\n"
"v_mfma_f32_16x16x32_bf16 v[76:79], a[144:147], v[24:27], v[76:79]\n"
"ds_write_b128 v3, v[92:95] offset:6144\n"
"v_mfma_f32_16x16x32_bf16 v[128:131], a[148:151], v[24:27], v[128:131]\n"
"v_and_b32_e32 v240, 0xffff0000, v83\n"
"s_waitcnt lgkmcnt(1)\n"
"v_mfma_f32_16x16x32_bf16 v[144:147], a[152:155], v[24:27], v[144:147]\n"
"ds_write_b128 v3, v[120:123] offset:7168\n"
"v_mfma_f32_16x16x32_bf16 v[152:155], a[156:159], v[24:27], v[152:155]\n"
"s_waitcnt vmcnt(12)\n"
"v_add_f32_e32 v108, v108, v255\n"
"v_mfma_f32_16x16x32_bf16 v[76:79], a[160:163], v[28:31], v[76:79]\n"
"ds_write_b128 v3, v[124:127] offset:8192\n"
"v_mfma_f32_16x16x32_bf16 v[128:131], a[164:167], v[28:31], v[128:131]\n"
"v_add_f32_e32 v109, v109, v254\n"
"v_add_f32_e32 v110, v110, v253\n"
"v_mfma_f32_16x16x32_bf16 v[144:147], a[168:171], v[28:31], v[144:147]\n"
"ds_write_b128 v3, v[136:139] offset:9216\n"
"v_mfma_f32_16x16x32_bf16 v[152:155], a[172:175], v[28:31], v[152:155]\n"
"v_add_f32_e32 v111, v111, v252\n"
"v_add_f32_e32 v112, v112, v251\n"
"v_mfma_f32_16x16x32_bf16 v[76:79], a[176:179], v[32:35], v[76:79]\n"
"s_add_u32 s98, s56, 0x400000\n"
"v_add_f32_e32 v113, v113, v250\n"
"v_mfma_f32_16x16x32_bf16 v[128:131], a[180:183], v[32:35], v[128:131]\n"
"buffer_load_dwordx4 v[24:27], v0, s[4:7], s98 offen sc1\n"
"v_mfma_f32_16x16x32_bf16 v[144:147], a[184:187], v[32:35], v[144:147]\n"
"v_add_f32_e32 v114, v114, v249\n"
"v_add_f32_e32 v115, v115, v248\n"
"v_mfma_f32_16x16x32_bf16 v[152:155], a[188:191], v[32:35], v[152:155]\n"
"s_waitcnt vmcnt(11)\n"
"v_add_f32_e32 v84, v84, v247\n"
"v_mfma_f32_16x16x32_bf16 v[76:79], a[192:195], v[36:39], v[76:79]\n"
"buffer_load_dwordx4 v[28:31], v0, s[4:7], s98 offen offset:1024 sc1\n"
"v_mfma_f32_16x16x32_bf16 v[128:131], a[196:199], v[36:39], v[128:131]\n"
"v_add_f32_e32 v85, v85, v246\n"
"v_add_f32_e32 v86, v86, v245\n"
"v_mfma_f32_16x16x32_bf16 v[144:147], a[200:203], v[36:39], v[144:147]\n"
"v_add_f32_e32 v87, v87, v244\n"
"v_add_f32_e32 v116, v116, v243\n"
"v_mfma_f32_16x16x32_bf16 v[152:155], a[204:207], v[36:39], v[152:155]\n"
"buffer_load_dwordx4 v[32:35], v0, s[4:7], s98 offen offset:2048 sc1\n"
"v_mfma_f32_16x16x32_bf16 v[76:79], a[208:211], v[40:43], v[76:79]\n"
"v_add_f32_e32 v117, v117, v242\n"
"v_add_f32_e32 v118, v118, v241\n"
"v_mfma_f32_16x16x32_bf16 v[128:131], a[212:215], v[40:43], v[128:131]\n"
"v_add_f32_e32 v119, v119, v240\n"
"v_mfma_f32_16x16x32_bf16 v[144:147], a[216:219], v[40:43], v[144:147]\n"
"buffer_load_dwordx4 v[36:39], v0, s[4:7], s98 offen offset:3072 sc1\n"
"v_mfma_f32_16x16x32_bf16 v[152:155], a[220:223], v[40:43], v[152:155]\n"
"s_waitcnt vmcnt(12)\n"
"v_mfma_f32_16x16x32_bf16 v[76:79], a[224:227], v[60:63], v[76:79]\n"
"s_waitcnt vmcnt(10)\n"
"v_max_u32_e32 v255, v132, v133\n"
"v_mfma_f32_16x16x32_bf16 v[128:131], a[228:231], v[60:63], v[128:131]\n"
"v_max3_u32 v255, v255, v134, v135\n"
"v_max3_u32 v255, v255, v68, v69\n"
"v_mfma_f32_16x16x32_bf16 v[144:147], a[232:235], v[60:63], v[144:147]\n"
"v_max3_u32 v255, v255, v70, v71\n"
"s_waitcnt vmcnt(8)\n"
"v_mfma_f32_16x16x32_bf16 v[152:155], a[236:239], v[60:63], v[152:155]\n"
"v_max3_u32 v255, v255, v64, v65\n"
"v_max3_u32 v255, v255, v66, v67\n"
"v_mfma_f32_16x16x32_bf16 v[76:79], a[240:243], v[88:91], v[76:79]\n"
"v_max3_u32 v255, v255, v100, v101\n"
"v_max3_u32 v255, v255, v102, v103\n"
"v_mfma_f32_16x16x32_bf16 v[128:131], a[244:247], v[88:91], v[128:131]\n"
"v_cmp_eq_u32_e32 vcc, -1, v255\n"
"v_mfma_f32_16x16x32_bf16 v[144:147], a[248:251], v[88:91], v[144:147]\n"
"s_cbranch_vccnz .Lmk_q67s\n"
".Lmk_q67r:\n"
"v_mfma_f32_16x16x32_bf16 v[152:155], a[252:255], v[88:91], v[152:155]\n"
"; @B G2 b.3\n"
"v_mfma_f32_16x16x32_bf16 v[108:111], a[128:131], v[132:135], v[108:111]\n"
"v_cmp_u_f32_e32 vcc, v76, v76\n"
"v_mfma_f32_16x16x32_bf16 v[112:115], a[132:135], v[132:135], v[112:115]\n"
"s_cbranch_vccnz .Lmk_p66s\n"
".Lmk_p66r:\n"
"v_mfma_f32_16x16x32_bf16 v[84:87], a[136:139], v[132:135], v[84:87]\n"
"buffer_load_dwordx4 v[40:43], v1, s[4:7], s98 offen sc1\n"
"v_mfma_f32_16x16x32_bf16 v[116:119], a[140:143], v[132:135], v[116:119]\n"
"v_mfma_f32_16x16x32_bf16 v[108:111], a[144:147], v[68:71], v[108:111]\n"
"ds_write_b128 v3, v[76:79] offset:12288\n"
"v_mfma_f32_16x16x32_bf16 v[112:115], a[148:151], v[68:71], v[112:115]\n"
"buffer_load_dwordx4 v[60:63], v1, s[4:7], s98 offen offset:1024 sc1\n"
"v_mfma_f32_16x16x32_bf16 v[84:87], a[152:155], v[68:71], v[84:87]\n"
"ds_write_b128 v3, v[128:131] offset:13312\n"
"v_mfma_f32_16x16x32_bf16 v[116:119], a[156:159], v[68:71], v[116:119]\n"
"v_mfma_f32_16x16x32_bf16 v[108:111], a[160:163], v[64:67], v[108:111]\n"
"s_waitcnt vmcnt(8)\n"
"v_max_u32_e32 v255, v96, v97\n"
"v_mfma_f32_16x16x32_bf16 v[112:115], a[164:167], v[64:67], v[112:115]\n"
"v_max3_u32 v255, v255, v98, v99\n"
"v_max3_u32 v255, v255, v140, v141\n"
"v_mfma_f32_16x16x32_bf16 v[84:87], a[168:171], v[64:67], v[84:87]\n"
"v_max3_u32 v255, v255, v142, v143\n"
"s_waitcnt vmcnt(6)\n"
"v_mfma_f32_16x16x32_bf16 v[116:119], a[172:175], v[64:67], v[116:119]\n"
"v_max3_u32 v255, v255, v72, v73\n"
"v_max3_u32 v255, v255, v74, v75\n"
"v_mfma_f32_16x16x32_bf16 v[108:111], a[176:179], v[100:103], v[108:111]\n"
"v_max3_u32 v255, v255, v148, v149\n"
"v_max3_u32 v255, v255, v150, v151\n"
"v_mfma_f32_16x16x32_bf16 v[112:115], a[180:183], v[100:103], v[112:115]\n"
"v_cmp_eq_u32_e32 vcc, -1, v255\n"
"v_mfma_f32_16x16x32_bf16 v[84:87], a[184:187], v[100:103], v[84:87]\n"
"s_cbranch_vccnz .Lmk_q68s\n"
".Lmk_q68r:\n"
"v_mfma_f32_16x16x32_bf16 v[116:119], a[188:191], v[100:103], v[116:119]\n"
"v_mfma_f32_16x16x32_bf16 v[108:111], a[192:195], v[96:99], v[108:111]\n"
"ds_write_b128 v3, v[144:147] offset:14336\n"
"v_mfma_f32_16x16x32_bf16 v[112:115], a[196:199], v[96:99], v[112:115]\n"
"buffer_load_dwordx4 v[64:67], v1, s[4:7], s98 offen offset:2048 sc1\n"
"v_mfma_f32_16x16x32_bf16 v[84:87], a[200:203], v[96:99], v[84:87]\n"
"ds_write_b128 v3, v[152:155] offset:15360\n"
"v_mfma_f32_16x16x32_bf16 v[116:119], a[204:207], v[96:99], v[116:119]\n"
"v_mfma_f32_16x16x32_bf16 v[108:111], a[208:211], v[140:143], v[108:111]\n"
"buffer_load_dwordx4 v[68:71], v1, s[4:7], s98 offen offset:3072 sc1\n"
"v_mfma_f32_16x16x32_bf16 v[112:115], a[212:215], v[140:143], v[112:115]\n"
"s_waitcnt lgkmcnt(0)\n"
"s_barrier\n"
"v_mfma_f32_16x16x32_bf16 v[84:87], a[216:219], v[140:143], v[84:87]\n"
"s_add_u32 s98, s57, 0x400000\n"
"ds_read_b128 v[76:79], v5\n"
"v_mfma_f32_16x16x32_bf16 v[116:119], a[220:223], v[140:143], v[116:119]\n"
"ds_read_b128 v[80:83], v6\n"
"v_mfma_f32_16x16x32_bf16 v[108:111], a[224:227], v[72:75], v[108:111]\n"
"buffer_load_dwordx4 v[88:91], v0, s[4:7], s98 offen sc1\n"
"v_mfma_f32_16x16x32_bf16 v[112:115], a[228:231], v[72:75], v[112:115]\n"
"ds_read_b128 v[92:95], v5 offset:1024\n"
"ds_read_b128 v[96:99], v6 offset:1024\n"
"v_mfma_f32_16x16x32_bf16 v[84:87], a[232:235], v[72:75], v[84:87]\n"
"ds_read_b128 v[100:103], v5 offset:2048\n"
"ds_read_b128 v[104:107], v6 offset:2048\n"
"v_mfma_f32_16x16x32_bf16 v[116:119], a[236:239], v[72:75], v[116:119]\n"
"buffer_load_dwordx4 v[72:75], v0, s[4:7], s98 offen offset:1024 sc1\n"
"v_mfma_f32_16x16x32_bf16 v[108:111], a[240:243], v[148:151], v[108:111]\n"
"ds_read_b128 v[120:123], v5 offset:3072\n"
"ds_read_b128 v[124:127], v6 offset:3072\n"
"v_mfma_f32_16x16x32_bf16 v[112:115], a[244:247], v[148:151], v[112:115]\n"
"v_mfma_f32_16x16x32_bf16 v[84:87], a[248:251], v[148:151], v[84:87]\n"
"buffer_load_dwordx4 v[128:131], v0, s[4:7], s98 offen offset:2048 sc1\n"
"v_mfma_f32_16x16x32_bf16 v[116:119], a[252:255], v[148:151], v[116:119]\n"
"s_waitcnt lgkmcnt(4)\n"
"v_add_f32_e32 v76, v76, v80\n"
"v_add_f32_e32 v77, v77, v81\n"
"v_add_f32_e32 v78, v78, v82\n"
"v_add_f32_e32 v79, v79, v83\n"
"v_add_f32_e32 v92, v92, v96\n"
"v_add_f32_e32 v93, v93, v97\n"
"v_add_f32_e32 v94, v94, v98\n"
"v_add_f32_e32 v95, v95, v99\n"
"buffer_load_dwordx4 v[80:83], v0, s[4:7], s98 offen offset:3072 sc1\n"
"s_waitcnt lgkmcnt(0)\n"
"v_add_f32_e32 v100, v100, v104\n"
"v_add_f32_e32 v101, v101, v105\n"
"v_add_f32_e32 v102, v102, v106\n"
"v_add_f32_e32 v103, v103, v107\n"
"v_add_f32_e32 v120, v120, v124\n"
"v_add_f32_e32 v121, v121, v125\n"
"v_add_f32_e32 v122, v122, v126\n"
"v_add_f32_e32 v123, v123, v127\n"
"buffer_load_dwordx4 v[96:99], v1, s[4:7], s98 offen sc1\n"
"buffer_load_dwordx4 v[104:107], v1, s[4:7], s98 offen offset:1024 sc1\n"
"buffer_load_dwordx4 v[124:127], v1, s[4:7], s98 offen offset:2048 sc1\n"
"buffer_load_dwordx4 v[132:135], v1, s[4:7], s98 offen offset:3072 sc1\n"
"s_waitcnt vmcnt(15)\n"
"; @B G2 a.0\n"
"v_mfma_f32_16x16x32_bf16 v[136:139], a[128:131], v[24:27], 0\n"
"s_add_u32 s98, s58, 0x400000\n"
"v_mfma_f32_16x16x32_bf16 v[140:143], a[132:135], v[24:27], 0\n"
"buffer_load_dwordx4 v[144:147], v0, s[4:7], s98 offen sc1\n"
"v_mfma_f32_16x16x32_bf16 v[148:151], a[136:139], v[24:27], 0\n"
"v_add_f32_e32 v76, v108, v76\n"
"v_add_f32_e32 v77, v109, v77\n"
"v_mfma_f32_16x16x32_bf16 v[152:155], a[140:143], v[24:27], 0\n"
"s_waitcnt vmcnt(15)\n"
"v_add_f32_e32 v78, v110, v78\n"
"v_mfma_f32_16x16x32_bf16 v[136:139], a[144:147], v[28:31], v[136:139]\n"
"buffer_load_dwordx4 v[24:27], v0, s[4:7], s98 offen offset:1024 sc1\n"
"v_mfma_f32_16x16x32_bf16 v[140:143], a[148:151], v[28:31], v[140:143]\n"
"v_add_f32_e32 v79, v111, v79\n"
"v_add_f32_e32 v92, v112, v92\n"
"v_mfma_f32_16x16x32_bf16 v[148:151], a[152:155], v[28:31], v[148:151]\n"
"v_add_f32_e32 v93, v113, v93\n"
"v_add_f32_e32 v94, v114, v94\n"
"v_mfma_f32_16x16x32_bf16 v[152:155], a[156:159], v[28:31], v[152:155]\n"
"s_waitcnt vmcnt(15)\n"
"v_add_f32_e32 v95, v115, v95\n"
"v_mfma_f32_16x16x32_bf16 v[136:139], a[160:163], v[32:35], v[136:139]\n"
"buffer_load_dwordx4 v[28:31], v0, s[4:7], s98 offen offset:2048 sc1\n"
"v_mfma_f32_16x16x32_bf16 v[140:143], a[164:167], v[32:35], v[140:143]\n"
"v_cvt_pk_bf16_f32 v108, v76, v77\n"
"v_cvt_pk_bf16_f32 v109, v78, v79\n"
"v_mfma_f32_16x16x32_bf16 v[148:151], a[168:171], v[32:35], v[148:151]\n"
"v_cvt_pk_bf16_f32 v110, v92, v93\n"
"v_cvt_pk_bf16_f32 v111, v94, v95\n"
"v_mfma_f32_16x16x32_bf16 v[152:155], a[172:175], v[32:35], v[152:155]\n"
"s_waitcnt vmcnt(15)\n"
"v_add_f32_e32 v100, v84, v100\n"
"v_mfma_f32_16x16x32_bf16 v[136:139], a[176:179], v[36:39], v[136:139]\n"
"buffer_load_dwordx4 v[32:35], v0, s[4:7], s98 offen offset:3072 sc1\n"
"v_mfma_f32_16x16x32_bf16 v[140:143], a[180:183], v[36:39], v[140:143]\n"
"v_add_f32_e32 v101, v85, v101\n"
"v_add_f32_e32 v102, v86, v102\n"
"v_mfma_f32_16x16x32_bf16 v[148:151], a[184:187], v[36:39], v[148:151]\n"
"v_add_f32_e32 v103, v87, v103\n"
"v_add_f32_e32 v120, v116, v120\n"
"v_mfma_f32_16x16x32_bf16 v[152:155], a[188:191], v[36:39], v[152:155]\n"
"s_waitcnt vmcnt(15)\n"
"v_add_f32_e32 v121, v117, v121\n"
"v_mfma_f32_16x16x32_bf16 v[136:139], a[192:195], v[40:43], v[136:139]\n"
"buffer_load_dwordx4 v[36:39], v1, s[4:7], s98 offen sc1\n"
"v_mfma_f32_16x16x32_bf16 v[140:143], a[196:199], v[40:43], v[140:143]\n"
"v_add_f32_e32 v122, v118, v122\n"
"v_add_f32_e32 v123, v119, v123\n"
"v_mfma_f32_16x16x32_bf16 v[148:151], a[200:203], v[40:43], v[148:151]\n"
"v_cvt_pk_bf16_f32 v76, v100, v101\n"
"v_cvt_pk_bf16_f32 v77, v102, v103\n"
"v_mfma_f32_16x16x32_bf16 v[152:155], a[204:207], v[40:43], v[152:155]\n"
"s_waitcnt vmcnt(15)\n"
"v_cvt_pk_bf16_f32 v78, v120, v121\n"
"v_mfma_f32_16x16x32_bf16 v[136:139], a[208:211], v[60:63], v[136:139]\n"
"buffer_load_dwordx4 v[40:43], v1, s[4:7], s98 offen offset:1024 sc1\n"
"v_mfma_f32_16x16x32_bf16 v[140:143], a[212:215], v[60:63], v[140:143]\n"
"v_cvt_pk_bf16_f32 v79, v122, v123\n"
"v_mfma_f32_16x16x32_bf16 v[148:151], a[216:219], v[60:63], v[148:151]\n"
"v_permlane16_swap_b32_e32 v108, v110\n"
"v_permlane16_swap_b32_e32 v109, v111\n"
"v_mfma_f32_16x16x32_bf16 v[152:155], a[220:223], v[60:63], v[152:155]\n"
"s_waitcnt vmcnt(15)\n"
"v_permlane16_swap_b32_e32 v76, v78\n"
"v_mfma_f32_16x16x32_bf16 v[136:139], a[224:227], v[64:67], v[136:139]\n"
"buffer_load_dwordx4 v[60:63], v1, s[4:7], s98 offen offset:2048 sc1\n"
"v_mfma_f32_16x16x32_bf16 v[140:143], a[228:231], v[64:67], v[140:143]\n"
"v_permlane16_swap_b32_e32 v77, v79\n"
"v_mfma_f32_16x16x32_bf16 v[148:151], a[232:235], v[64:67], v[148:151]\n"
"v_mfma_f32_16x16x32_bf16 v[152:155], a[236:239], v[64:67], v[152:155]\n"
"s_waitcnt vmcnt(15)\n"
"v_mfma_f32_16x16x32_bf16 v[136:139], a[240:243], v[68:71], v[136:139]\n"
"buffer_load_dwordx4 v[64:67], v1, s[4:7], s98 offen offset:3072 sc1\n"
"v_mfma_f32_16x16x32_bf16 v[140:143], a[244:247], v[68:71], v[140:143]\n"
"s_add_u32 s98, s59, 0x400000\n"
"v_mfma_f32_16x16x32_bf16 v[148:151], a[248:251], v[68:71], v[148:151]\n"
"v_mfma_f32_16x16x32_bf16 v[152:155], a[252:255], v[68:71], v[152:155]\n"
"s_waitcnt vmcnt(14)\n"
"; @B G2 a.1\n"
"v_mfma_f32_16x16x32_bf16 v[68:71], a[128:131], v[88:91], 0\n"
"s_nop 0\n"
"v_cmp_u_f32_e32 vcc, v136, v136\n"
"v_mfma_f32_16x16x32_bf16 v[84:87], a[132:135], v[88:91], 0\n"
"s_cbranch_vccnz .Lmk_p69s\n"
".Lmk_p69r:\n"
"v_mfma_f32_16x16x32_bf16 v[92:95], a[136:139], v[88:91], 0\n"
"buffer_load_dwordx4 v[100:103], v0, s[4:7], s98 offen sc1\n"
"v_mfma_f32_16x16x32_bf16 v[112:115], a[140:143], v[88:91], 0\n"
"v_mfma_f32_16x16x32_bf16 v[68:71], a[144:147], v[72:75], v[68:71]\n"
"ds_write_b128 v3, v[136:139]\n"
"v_mfma_f32_16x16x32_bf16 v[84:87], a[148:151], v[72:75], v[84:87]\n"
"buffer_load_dwordx4 v[88:91], v0, s[4:7], s98 offen offset:1024 sc1\n"
"v_mfma_f32_16x16x32_bf16 v[92:95], a[152:155], v[72:75], v[92:95]\n"
"ds_write_b128 v3, v[140:143] offset:1024\n"
"v_mfma_f32_16x16x32_bf16 v[112:115], a[156:159], v[72:75], v[112:115]\n"
"s_waitcnt vmcnt(14)\n"
"v_mfma_f32_16x16x32_bf16 v[68:71], a[160:163], v[128:131], v[68:71]\n"
"ds_write_b128 v3, v[148:151] offset:2048\n"
"v_mfma_f32_16x16x32_bf16 v[84:87], a[164:167], v[128:131], v[84:87]\n"
"buffer_load_dwordx4 v[72:75], v0, s[4:7], s98 offen offset:2048 sc1\n"
"v_mfma_f32_16x16x32_bf16 v[92:95], a[168:171], v[128:131], v[92:95]\n"
"ds_write_b128 v3, v[152:155] offset:3072\n"
"v_mfma_f32_16x16x32_bf16 v[112:115], a[172:175], v[128:131], v[112:115]\n"
"v_mfma_f32_16x16x32_bf16 v[68:71], a[176:179], v[80:83], v[68:71]\n"
"buffer_load_dwordx4 v[116:119], v0, s[4:7], s98 offen offset:3072 sc1\n"
"v_mfma_f32_16x16x32_bf16 v[84:87], a[180:183], v[80:83], v[84:87]\n"
"ds_read_b128 v[120:123], v2\n"
"ds_read_b128 v[128:131], v2 offset:1024\n"
"v_mfma_f32_16x16x32_bf16 v[92:95], a[184:187], v[80:83], v[92:95]\n"
"v_mfma_f32_16x16x32_bf16 v[112:115], a[188:191], v[80:83], v[112:115]\n"
"s_waitcnt vmcnt(14)\n"
"v_mfma_f32_16x16x32_bf16 v[68:71], a[192:195], v[96:99], v[68:71]\n"
"buffer_load_dwordx4 v[80:83], v1, s[4:7], s98 offen sc1\n"
"v_mfma_f32_16x16x32_bf16 v[84:87], a[196:199], v[96:99], v[84:87]\n"
"v_mfma_f32_16x16x32_bf16 v[92:95], a[200:203], v[96:99], v[92:95]\n"
"s_waitcnt lgkmcnt(0)\n"
"v_lshlrev_b32_e32 v255, 16, v120\n"
"v_mfma_f32_16x16x32_bf16 v[112:115], a[204:207], v[96:99], v[112:115]\n"
"s_waitcnt lgkmcnt(0)\n"
"s_barrier\n"
"v_mfma_f32_16x16x32_bf16 v[68:71], a[208:211], v[104:107], v[68:71]\n"
"buffer_load_dwordx4 v[96:99], v1, s[4:7], s98 offen offset:1024 sc1\n"
"v_mfma_f32_16x16x32_bf16 v[84:87], a[212:215], v[104:107], v[84:87]\n"
"ds_read_b128 v[136:139], v4\n"
"ds_read_b128 v[140:143], v4 offset:1024\n"
"v_mfma_f32_16x16x32_bf16 v[92:95], a[216:219], v[104:107], v[92:95]\n"
"ds_read_b128 v[148:151], v4 offset:2048\n"
"ds_read_b128 v[152:155], v4 offset:3072\n"
"v_mfma_f32_16x16x32_bf16 v[112:115], a[220:223], v[104:107], v[112:115]\n"
"s_waitcnt vmcnt(14)\n"
"v_and_b32_e32 v254, 0xffff0000, v120\n"
"v_mfma_f32_16x16x32_bf16 v[68:71], a[224:227], v[124:127], v[68:71]\n"
"buffer_load_dwordx4 v[104:107], v1, s[4:7], s98 offen offset:2048 sc1\n"
"v_mfma_f32_16x16x32_bf16 v[84:87], a[228:231], v[124:127], v[84:87]\n"
"v_lshlrev_b32_e32 v253, 16, v121\n"
"v_and_b32_e32 v252, 0xffff0000, v121\n"
"v_mfma_f32_16x16x32_bf16 v[92:95], a[232:235], v[124:127], v[92:95]\n"
"v_lshlrev_b32_e32 v251, 16, v122\n"
"v_and_b32_e32 v250, 0xffff0000, v122\n"
"v_mfma_f32_16x16x32_bf16 v[112:115], a[236:239], v[124:127], v[112:115]\n"
"buffer_load_dwordx4 v[124:127], v1, s[4:7], s98 offen offset:3072 sc1\n"
"v_mfma_f32_16x16x32_bf16 v[68:71], a[240:243], v[132:135], v[68:71]\n"
"s_add_u32 s98, s74, 0x40000\n"
"v_lshlrev_b32_e32 v249, 16, v123\n"
"v_mfma_f32_16x16x32_bf16 v[84:87], a[244:247], v[132:135], v[84:87]\n"
"buffer_store_dwordx4 v[108:111], v20, s[16:19], s98 offen\n"
"v_mfma_f32_16x16x32_bf16 v[92:95], a[248:251], v[132:135], v[92:95]\n"
"v_and_b32_e32 v248, 0xffff0000, v123\n"
"v_lshlrev_b32_e32 v247, 16, v128\n"
"v_mfma_f32_16x16x32_bf16 v[112:115], a[252:255], v[132:135], v[112:115]\n"
"s_waitcnt vmcnt(15)\n"
"v_and_b32_e32 v246, 0xffff0000, v128\n"
"; @B G2 a.2\n"
"v_mfma_f32_16x16x32_bf16 v[108:111], a[128:131], v[144:147], 0\n"
"v_cmp_u_f32_e32 vcc, v68, v68\n"
"v_lshlrev_b32_e32 v245, 16, v129\n"
"v_mfma_f32_16x16x32_bf16 v[120:123], a[132:135], v[144:147], 0\n"
"s_cbranch_vccnz .Lmk_p70s\n"
".Lmk_p70r:\n"
"v_and_b32_e32 v244, 0xffff0000, v129\n"
"v_mfma_f32_16x16x32_bf16 v[132:135], a[136:139], v[144:147], 0\n"
"v_lshlrev_b32_e32 v243, 16, v130\n"
"v_and_b32_e32 v242, 0xffff0000, v130\n"
"v_mfma_f32_16x16x32_bf16 v[156:159], a[140:143], v[144:147], 0\n"
"v_lshlrev_b32_e32 v241, 16, v131\n"
"v_and_b32_e32 v240, 0xffff0000, v131\n"
"v_mfma_f32_16x16x32_bf16 v[108:111], a[144:147], v[24:27], v[108:111]\n"
"ds_write_b128 v3, v[68:71] offset:6144\n"
"v_mfma_f32_16x16x32_bf16 v[120:123], a[148:151], v[24:27], v[120:123]\n"
"s_waitcnt lgkmcnt(1)\n"
"v_add_f32_e32 v136, v136, v255\n"
"v_mfma_f32_16x16x32_bf16 v[132:135], a[152:155], v[24:27], v[132:135]\n"
"ds_write_b128 v3, v[84:87] offset:7168\n"
"v_mfma_f32_16x16x32_bf16 v[156:159], a[156:159], v[24:27], v[156:159]\n"
"s_waitcnt vmcnt(13)\n"
"s_add_u32 s98, s74, 0x40000\n"
"v_mfma_f32_16x16x32_bf16 v[108:111], a[160:163], v[28:31], v[108:111]\n"
"ds_write_b128 v3, v[92:95] offset:8192\n"
"v_mfma_f32_16x16x32_bf16 v[120:123], a[164:167], v[28:31], v[120:123]\n"
"buffer_store_dwordx4 v[76:79], v20, s[16:19], s98 offen offset:64\n"
"v_mfma_f32_16x16x32_bf16 v[132:135], a[168:171], v[28:31], v[132:135]\n"
"ds_write_b128 v3, v[112:115] offset:9216\n"
"v_mfma_f32_16x16x32_bf16 v[156:159], a[172:175], v[28:31], v[156:159]\n"
"v_add_f32_e32 v137, v137, v254\n"
"v_add_f32_e32 v138, v138, v253\n"
"v_mfma_f32_16x16x32_bf16 v[108:111], a[176:179], v[32:35], v[108:111]\n"
"v_add_f32_e32 v139, v139, v252\n"
"v_add_f32_e32 v140, v140, v251\n"
"v_mfma_f32_16x16x32_bf16 v[120:123], a[180:183], v[32:35], v[120:123]\n"
"v_add_f32_e32 v141, v141, v250\n"
"v_add_f32_e32 v142, v142, v249\n"
"v_mfma_f32_16x16x32_bf16 v[132:135], a[184:187], v[32:35], v[132:135]\n"
"v_add_f32_e32 v143, v143, v248\n"
"v_add_f32_e32 v148, v148, v247\n"
"v_mfma_f32_16x16x32_bf16 v[156:159], a[188:191], v[32:35], v[156:159]\n"
"s_waitcnt vmcnt(12)\n"
"v_add_f32_e32 v149, v149, v246\n"
"v_mfma_f32_16x16x32_bf16 v[108:111], a[192:195], v[36:39], v[108:111]\n"
"v_add_f32_e32 v150, v150, v245\n"
"v_add_f32_e32 v151, v151, v244\n"
"v_mfma_f32_16x16x32_bf16 v[120:123], a[196:199], v[36:39], v[120:123]\n"
"v_add_f32_e32 v152, v152, v243\n"
"v_add_f32_e32 v153, v153, v242\n"
"v_mfma_f32_16x16x32_bf16 v[132:135], a[200:203], v[36:39], v[132:135]\n"
"v_add_f32_e32 v154, v154, v241\n"
"v_add_f32_e32 v155, v155, v240\n"
"v_mfma_f32_16x16x32_bf16 v[156:159], a[204:207], v[36:39], v[156:159]\n"
"v_mfma_f32_16x16x32_bf16 v[108:111], a[208:211], v[40:43], v[108:111]\n"
"v_mfma_f32_16x16x32_bf16 v[120:123], a[212:215], v[40:43], v[120:123]\n"
"v_mfma_f32_16x16x32_bf16 v[132:135], a[216:219], v[40:43], v[132:135]\n"
"v_mfma_f32_16x16x32_bf16 v[156:159], a[220:223], v[40:43], v[156:159]\n"
"s_waitcnt vmcnt(10)\n"
"v_mfma_f32_16x16x32_bf16 v[108:111], a[224:227], v[60:63], v[108:111]\n"
"s_waitcnt vmcnt(8)\n"
"v_max_u32_e32 v255, v100, v101\n"
"v_mfma_f32_16x16x32_bf16 v[120:123], a[228:231], v[60:63], v[120:123]\n"
"v_max3_u32 v255, v255, v102, v103\n"
"v_max3_u32 v255, v255, v88, v89\n"
"v_mfma_f32_16x16x32_bf16 v[132:135], a[232:235], v[60:63], v[132:135]\n"
"v_max3_u32 v255, v255, v90, v91\n"
"s_waitcnt vmcnt(6)\n"
"v_mfma_f32_16x16x32_bf16 v[156:159], a[236:239], v[60:63], v[156:159]\n"
"v_max3_u32 v255, v255, v72, v73\n"
"v_max3_u32 v255, v255, v74, v75\n"
"v_mfma_f32_16x16x32_bf16 v[108:111], a[240:243], v[64:67], v[108:111]\n"
"v_max3_u32 v255, v255, v116, v117\n"
"v_max3_u32 v255, v255, v118, v119\n"
"v_mfma_f32_16x16x32_bf16 v[120:123], a[244:247], v[64:67], v[120:123]\n"
"v_cmp_eq_u32_e32 vcc, -1, v255\n"
"v_mfma_f32_16x16x32_bf16 v[132:135], a[248:251], v[64:67], v[132:135]\n"
"s_cbranch_vccnz .Lmk_q72s\n"
".Lmk_q72r:\n"
"v_mfma_f32_16x16x32_bf16 v[156:159], a[252:255], v[64:67], v[156:159]\n"
"; @B G2 a.3\n"
"v_mfma_f32_16x16x32_bf16 v[136:139], a[128:131], v[100:103], v[136:139]\n"
"v_cmp_u_f32_e32 vcc, v108, v108\n"
"v_mfma_f32_16x16x32_bf16 v[140:143], a[132:135], v[100:103], v[140:143]\n"
"s_cbranch_vccnz .Lmk_p71s\n"
".Lmk_p71r:\n"
"v_mfma_f32_16x16x32_bf16 v[148:151], a[136:139], v[100:103], v[148:151]\n"
"v_mfma_f32_16x16x32_bf16 v[152:155], a[140:143], v[100:103], v[152:155]\n"
"v_mfma_f32_16x16x32_bf16 v[136:139], a[144:147], v[88:91], v[136:139]\n"
"ds_write_b128 v3, v[108:111] offset:12288\n"
"v_mfma_f32_16x16x32_bf16 v[140:143], a[148:151], v[88:91], v[140:143]\n"
"v_mfma_f32_16x16x32_bf16 v[148:151], a[152:155], v[88:91], v[148:151]\n"
"ds_write_b128 v3, v[120:123] offset:13312\n"
"v_mfma_f32_16x16x32_bf16 v[152:155], a[156:159], v[88:91], v[152:155]\n"
"v_mfma_f32_16x16x32_bf16 v[136:139], a[160:163], v[72:75], v[136:139]\n"
"s_waitcnt vmcnt(4)\n"
"v_max_u32_e32 v255, v80, v81\n"
"v_mfma_f32_16x16x32_bf16 v[140:143], a[164:167], v[72:75], v[140:143]\n"
"v_max3_u32 v255, v255, v82, v83\n"
"v_max3_u32 v255, v255, v96, v97\n"
"v_mfma_f32_16x16x32_bf16 v[148:151], a[168:171], v[72:75], v[148:151]\n"
"v_max3_u32 v255, v255, v98, v99\n"
"s_waitcnt vmcnt(2)\n"
"v_mfma_f32_16x16x32_bf16 v[152:155], a[172:175], v[72:75], v[152:155]\n"
"v_max3_u32 v255, v255, v104, v105\n"
"v_max3_u32 v255, v255, v106, v107\n"
"v_mfma_f32_16x16x32_bf16 v[136:139], a[176:179], v[116:119], v[136:139]\n"
"v_max3_u32 v255, v255, v124, v125\n"
"v_max3_u32 v255, v255, v126, v127\n"
"v_mfma_f32_16x16x32_bf16 v[140:143], a[180:183], v[116:119], v[140:143]\n"
"v_cmp_eq_u32_e32 vcc, -1, v255\n"
"v_mfma_f32_16x16x32_bf16 v[148:151], a[184:187], v[116:119], v[148:151]\n"
"s_cbranch_vccnz .Lmk_q73s\n"
".Lmk_q73r:\n"
"v_mfma_f32_16x16x32_bf16 v[152:155], a[188:191], v[116:119], v[152:155]\n"
"v_mfma_f32_16x16x32_bf16 v[136:139], a[192:195], v[80:83], v[136:139]\n"
"ds_write_b128 v3, v[132:135] offset:14336\n"
"v_mfma_f32_16x16x32_bf16 v[140:143], a[196:199], v[80:83], v[140:143]\n"
"v_mfma_f32_16x16x32_bf16 v[148:151], a[200:203], v[80:83], v[148:151]\n"
"ds_write_b128 v3, v[156:159] offset:15360\n"
"v_mfma_f32_16x16x32_bf16 v[152:155], a[204:207], v[80:83], v[152:155]\n"
"v_mfma_f32_16x16x32_bf16 v[136:139], a[208:211], v[96:99], v[136:139]\n"
"v_mfma_f32_16x16x32_bf16 v[140:143], a[212:215], v[96:99], v[140:143]\n"
"s_waitcnt lgkmcnt(0)\n"
"s_barrier\n"
"v_mfma_f32_16x16x32_bf16 v[148:151], a[216:219], v[96:99], v[148:151]\n"
"ds_read_b128 v[24:27], v5\n"
"ds_read_b128 v[28:31], v6\n"
"v_mfma_f32_16x16x32_bf16 v[152:155], a[220:223], v[96:99], v[152:155]\n"
"ds_read_b128 v[32:35], v5 offset:1024\n"
"v_mfma_f32_16x16x32_bf16 v[136:139], a[224:227], v[104:107], v[136:139]\n"
"ds_read_b128 v[36:39], v6 offset:1024\n"
"v_mfma_f32_16x16x32_bf16 v[140:143], a[228:231], v[104:107], v[140:143]\n"
"ds_read_b128 v[40:43], v5 offset:2048\n"
"ds_read_b128 v[60:63], v6 offset:2048\n"
"v_mfma_f32_16x16x32_bf16 v[148:151], a[232:235], v[104:107], v[148:151]\n"
"ds_read_b128 v[64:67], v5 offset:3072\n"
"ds_read_b128 v[68:71], v6 offset:3072\n"
"v_mfma_f32_16x16x32_bf16 v[152:155], a[236:239], v[104:107], v[152:155]\n"
"v_mfma_f32_16x16x32_bf16 v[136:139], a[240:243], v[124:127], v[136:139]\n"
"v_mfma_f32_16x16x32_bf16 v[140:143], a[244:247], v[124:127], v[140:143]\n"
"s_waitcnt lgkmcnt(4)\n"
"v_add_f32_e32 v24, v24, v28\n"
"v_mfma_f32_16x16x32_bf16 v[148:151], a[248:251], v[124:127], v[148:151]\n"
"v_add_f32_e32 v25, v25, v29\n"
"v_add_f32_e32 v26, v26, v30\n"
"v_mfma_f32_16x16x32_bf16 v[152:155], a[252:255], v[124:127], v[152:155]\n"
"v_add_f32_e32 v27, v27, v31\n"
"v_add_f32_e32 v32, v32, v36\n"
"v_add_f32_e32 v33, v33, v37\n"
"v_add_f32_e32 v34, v34, v38\n"
"v_add_f32_e32 v35, v35, v39\n"
"s_waitcnt lgkmcnt(0)\n"
"v_add_f32_e32 v40, v40, v60\n"
"v_add_f32_e32 v41, v41, v61\n"
"v_add_f32_e32 v42, v42, v62\n"
"v_add_f32_e32 v43, v43, v63\n"
"v_add_f32_e32 v64, v64, v68\n"
"v_add_f32_e32 v65, v65, v69\n"
"v_add_f32_e32 v66, v66, v70\n"
"v_add_f32_e32 v67, v67, v71\n"
"v_add_f32_e32 v24, v136, v24\n"
"v_add_f32_e32 v25, v137, v25\n"
"v_add_f32_e32 v26, v138, v26\n"
"v_add_f32_e32 v27, v139, v27\n"
"v_add_f32_e32 v32, v140, v32\n"
"v_add_f32_e32 v33, v141, v33\n"
"v_add_f32_e32 v34, v142, v34\n"
"v_add_f32_e32 v35, v143, v35\n"
"v_cvt_pk_bf16_f32 v28, v24, v25\n"
"v_cvt_pk_bf16_f32 v29, v26, v27\n"
"v_cvt_pk_bf16_f32 v30, v32, v33\n"
"v_cvt_pk_bf16_f32 v31, v34, v35\n"
"v_add_f32_e32 v40, v148, v40\n"
"v_add_f32_e32 v41, v149, v41\n"
"v_add_f32_e32 v42, v150, v42\n"
"v_add_f32_e32 v43, v151, v43\n"
"v_add_f32_e32 v64, v152, v64\n"
"v_add_f32_e32 v65, v153, v65\n"
"v_add_f32_e32 v66, v154, v66\n"
"v_add_f32_e32 v67, v155, v67\n"
"v_cvt_pk_bf16_f32 v24, v40, v41\n"
"v_cvt_pk_bf16_f32 v25, v42, v43\n"
"v_cvt_pk_bf16_f32 v26, v64, v65\n"
"v_cvt_pk_bf16_f32 v27, v66, v67\n"
"v_permlane16_swap_b32_e32 v28, v30\n"
"v_permlane16_swap_b32_e32 v29, v31\n"
"buffer_store_dwordx4 v[28:31], v20, s[16:19], s74 offen\n"
"v_permlane16_swap_b32_e32 v24, v26\n"
"v_permlane16_swap_b32_e32 v25, v27\n"
"buffer_store_dwordx4 v[24:27], v20, s[16:19], s74 offen offset:64\n"
"s_cmp_eq_u32 s81, 0\n"
"s_cbranch_scc1 .Lmk_fin\n"
"buffer_store_dwordx2 v[18:19], v11, s[4:7], s81 offen sc1\n"
".Lmk_fin:\n"
"s_endpgm\n"
".Lmk_end:\n"
    :: "{s[4:7]}"(wsr), "{s[8:11]}"(wsr), "{s[12:15]}"(wsk), "{s[16:19]}"(outr), "{s[52:67]}"(sa), "{s[68:83]}"(sb),
       "{s[20:27]}"(pa), "{s[28:31]}"(wa), "{s[32:35]}"(wb), "{s[36:39]}"(oa), "{s[40:43]}"(ob), "{s[44:51]}"(pc), "{s99}"(uni((bix * 4 + w) * 1024)),
       "{v[0:15]}"(va), "{v[16:23]}"(vb), "{v[32:47]}"(pz), "{v[48:49]}"(sta), "{v50}"(pf)
    : "memory", "v255", "a255", "s98", "vcc");
  __builtin_unreachable();
}

}

extern "C" void kernel_launch(void* const* d_in, const int* in_sizes, int n_in, void* d_out, int out_size,
                              void* d_ws, size_t ws_size, hipStream_t stream) {
  (void)hipFuncSetAttribute((const void*)mega_kernel, hipFuncAttributeMaxDynamicSharedMemorySize, LDS_BYTES);
  mega_kernel<<<256, 256, LDS_BYTES, stream>>>((const u16*)d_in[0], (const u16*)d_in[1], (const u16*)d_in[2],
                                               (const u16*)d_in[4], (const u16*)d_in[5], (u16*)d_out, (char*)d_ws);
}
```

```cpp
#include <hip/hip_runtime.h>
#include <stdint.h>

namespace {

typedef unsigned short u16;
typedef unsigned long long u64;
typedef short s16x4 __attribute__((ext_vector_type(4)));
typedef float f32x4 __attribute__((ext_vector_type(4)));
typedef unsigned u32x4 __attribute__((ext_vector_type(4)));
typedef unsigned u32x2 __attribute__((ext_vector_type(2)));
typedef unsigned u32x8 __attribute__((ext_vector_type(8)));
typedef unsigned u32x16 __attribute__((ext_vector_type(16)));
typedef __amdgpu_buffer_rsrc_t rsrc_t;
#define LAS __attribute__((address_space(3)))
#define DI __device__ __forceinline__
#define LDSP(a) ((LAS char*)(size_t)(a))

constexpr unsigned MB = 1u << 20;
constexpr unsigned WS_XF = 0;
constexpr unsigned WS_OF = 16 * MB;
constexpr unsigned WS_KV = 32 * MB;
constexpr unsigned WS_WF = 64 * MB;
constexpr unsigned WS_WO = 76 * MB;
constexpr unsigned WS_ST = 80 * MB;
constexpr unsigned WS_CS = 81 * MB;
constexpr unsigned WS_FL = 82 * MB;
constexpr unsigned WS_EP = WS_FL + 2048 * 128;
constexpr unsigned WS_DBG = 96 * MB;
constexpr unsigned OOB = 0x80000000u;
constexpr int L_RES = 0, L_K = 16384, L_V = 49152, L_INB = 81920, L_CS = 155648, L_PM = L_CS + 1024, L_MB = L_PM + 2048, L_SCR = L_MB + 2048;
constexpr int L_WS = 57344;
constexpr int LDS_BYTES = 163840;

template <int N> DI void wait_vm() { asm volatile("s_waitcnt vmcnt(%0)" ::"n"(N) : "memory"); }
DI void lds_barrier() { asm volatile("s_waitcnt lgkmcnt(0)\n\ts_barrier" ::: "memory"); }
template <int N> DI void nops() { asm volatile("s_nop %0" ::"n"(N - 1)); }
template <int OFF> DI void ldv(u32x4& d, rsrc_t r, unsigned v, unsigned s) {
  asm volatile("buffer_load_dwordx4 %0, %1, %2, %3 offen offset:%4 sc1" : "=v"(d) : "v"(v), "s"(r), "s"(s), "i"(OFF) : "memory");
}
template <int OFF> DI void ld8(u32x2& d, rsrc_t r, unsigned v, unsigned s) {
  asm volatile("buffer_load_dwordx2 %0, %1, %2, %3 offen offset:%4 sc1" : "=v"(d) : "v"(v), "s"(r), "s"(s), "i"(OFF) : "memory");
}
template <int OFF> DI void st16(const u32x4& d, rsrc_t r, unsigned v, unsigned s) {
  asm volatile("buffer_store_dwordx4 %0, %1, %2, %3 offen offset:%4 sc1\n\ts_nop 1" :: "v"(d), "v"(v), "s"(r), "s"(s), "i"(OFF) : "memory");
}
template <int OFF> DI void st8(const u32x2& d, rsrc_t r, unsigned v, unsigned s) {
  asm volatile("buffer_store_dwordx2 %0, %1, %2, %3 offen offset:%4 sc1" :: "v"(d), "v"(v), "s"(r), "s"(s), "i"(OFF) : "memory");
}
DI void st4(unsigned d, rsrc_t r, unsigned v, unsigned s) {
  asm volatile("buffer_store_dword %0, %1, %2, %3 offen sc1" :: "v"(d), "v"(v), "s"(r), "s"(s) : "memory");
}
DI void dma(rsrc_t r, unsigned v, unsigned s, unsigned lm) {
  asm volatile("s_mov_b32 m0, %3\n\ts_nop 0\n\tbuffer_load_dwordx4 %0, %1, %2 offen lds" :: "v"(v), "s"(r), "s"(s), "s"(lm) : "memory");
}
DI void touch(rsrc_t r, unsigned v) {
  asm volatile("s_mov_b32 m0, %2\n\ts_nop 0\n\tbuffer_load_dword %0, %1, 0 offen lds" :: "v"(v), "s"(r), "s"((unsigned)L_SCR) : "memory");
}
DI void mma_v(f32x4& c, const u32x4& a, const u32x4& b) { asm volatile("v_mfma_f32_16x16x32_bf16 %0, %1, %2, %0" : "+v"(c) : "v"(a), "v"(b)); }
DI void mm0_v(f32x4& c, const u32x4& a, const u32x4& b) { asm volatile("v_mfma_f32_16x16x32_bf16 %0, %1, %2, 0" : "=v"(c) : "v"(a), "v"(b)); }
DI unsigned uni(unsigned v) { v = __builtin_amdgcn_readfirstlane(v); asm volatile("" : "+s"(v)); return v; }
template <class T> DI void pin(T& v) { asm volatile("" : "+v"(v)); }
DI float sum_x16(float v) {
  auto r = __builtin_amdgcn_permlane16_swap(__float_as_uint(v), __float_as_uint(v), false, false);
  return __uint_as_float(r[0]) + __uint_as_float(r[1]);
}
DI float sum_x32(float v) {
  auto r = __builtin_amdgcn_permlane32_swap(__float_as_uint(v), __float_as_uint(v), false, false);
  return __uint_as_float(r[0]) + __uint_as_float(r[1]);
}
DI u32x4 swap16(const u32x4& x) {
  auto r0 = __builtin_amdgcn_permlane16_swap(x[0], x[2], false, false);
  auto r1 = __builtin_amdgcn_permlane16_swap(x[1], x[3], false, false);
  return (u32x4){(unsigned)r0[0], (unsigned)r1[0], (unsigned)r0[1], (unsigned)r1[1]};
}
DI u32x2 lds_tr(unsigned a) { return __builtin_bit_cast(u32x2, __builtin_amdgcn_ds_read_tr16_b64_v4i16((LAS s16x4*)LDSP(a))); }
DI u32x4 cat(const u32x2& a, const u32x2& b) { return __builtin_shufflevector(a, b, 0, 1, 2, 3); }
DI u32x2 lds_r2(unsigned a) { return *(LAS u32x2*)LDSP(a); }
DI void lds_w4(unsigned a, const u32x4& v) { *(LAS u32x4*)LDSP(a) = v; }
DI unsigned rot(unsigned g, unsigned sl) { return (g + (0x641u >> 4 * sl)) & 15; }
DI int swzK(int u) { return ((((u >> 1) ^ u) & 1) << 1) | (u >> 1); }

#define STAMP(i)

__global__ __launch_bounds__(256, 1) void mega_kernel(const u16* __restrict__ x0, const u16* __restrict__ WqA,
                                                      const u16* __restrict__ WoA, const u16* __restrict__ WqB,
                                                      const u16* __restrict__ WoB, u16* __restrict__ out, char* ws) {
  const unsigned bid = blockIdx.x, xcd = bid & 7, bix = bid >> 3;
  const unsigned w = __builtin_amdgcn_readfirstlane(threadIdx.x >> 6), lane = threadIdx.x & 63;
  const unsigned c = (xcd >> 2) * 4 + (bix >> 3), h = (xcd & 3) * 8 + (bix & 7), par = h & 1, g = h >> 1;
  const unsigned p2 = h >> 3 & 1, g2 = (h >> 4) * 8 + (h & 7);
  const unsigned fr = lane & 15, fq = lane >> 4;
  const unsigned dl = 0, wr = (w + dl) & 3;
  const rsrc_t wsr = __builtin_amdgcn_make_buffer_rsrc(ws, 0, 128 * MB, 0x00020000);
  unsigned stw = 0;
  STAMP(0);
  const unsigned l16 = lane * 16, lw = w * 2048 + l16;
  const unsigned fl0 = lane == 0 ? 0u : OOB;
  const unsigned myfl = WS_FL + (c * 64 + h) * 128 + w * 8;
  const unsigned xo = (fr * 1024 + (((fq & 1) << 4) | ((fq >> 1) << 3))) * 2;
  u64 pc_; unsigned len_;
  asm volatile("s_getpc_b64 %0\n.Lmk_here:\n\ts_mov_b32 %1, .Lmk_end-.Lmk_here" : "=s"(pc_), "=s"(len_));
  const rsrc_t cr = __builtin_amdgcn_make_buffer_rsrc((void*)(pc_ & ~15ull), 0, len_ & ~15u, 0x00020000);
  const rsrc_t xr = __builtin_amdgcn_make_buffer_rsrc((void*)x0, 0, 4 * MB, 0x00020000);
  const unsigned osrc0 = (lane >> 3) * 2048 + (lane & 7) * 16;
  auto get_x = [&](int s) __attribute__((always_inline)) {
#pragma unroll
    for (int i = 0; i < 2; ++i) dma(xr, osrc0, ((c * 16 + s * 8 + 2 * wr + p2) * 16 + i * 8) * 2048 + g2 * 128, L_RES + s * 8192 + w * 2048 + i * 1024);
  };
  u32x2 tag;
  const rsrc_t wa = __builtin_amdgcn_make_buffer_rsrc((void*)WqA, 0, 6 * MB, 0x00020000);
  const unsigned rho = 2 * (c & 3), wbase = w * 256 * 6144 + h * 64;
  const unsigned wv = (lane >> 2) * 6144 + ((lane & 3) ^ ((lane >> 3) & 2)) * 16;
  auto piece = [&](int q, int j) __attribute__((always_inline)) {
    const unsigned k = j ^ par, src = uni(wbase + (((rho + 2 * q) & 7) + k) * 196608), dst = uni(L_WS + ((q & 1) * 8 + k * 4 + w) * 6144);
#pragma unroll
    for (int i = 0; i < 6; ++i) dma(wa, wv, src + (i & 1) * 98304 + (i >> 1) * 2048, dst + i * 1024);
  };
  const unsigned kb = 2 * c + par;
  const rsrc_t oa = __builtin_amdgcn_make_buffer_rsrc((void*)WoA, 0, 2 * MB, 0x00020000);
  const rsrc_t wb = __builtin_amdgcn_make_buffer_rsrc((void*)WqB, 0, 6 * MB, 0x00020000);
  const rsrc_t ob = __builtin_amdgcn_make_buffer_rsrc((void*)WoB, 0, 2 * MB, 0x00020000);
  const unsigned wsrc = (w * 16 + (lane >> 3)) * 6144 + (lane & 7) * 16, osrc = w * 16 * 2048 + osrc0;
  auto get_o = [&](int t) __attribute__((always_inline)) {
#pragma unroll
    for (int i = 0; i < 2; ++i) dma(t ? ob : oa, osrc, (kb * 64 + i * 8) * 2048 + rot(g, 1 + t) * 128, L_K + 24576 + t * 8192 + w * 2048 + i * 1024);
  };
  auto get_q = [&](int t) __attribute__((always_inline)) {
#pragma unroll
    for (int i = 0; i < 6; ++i)
      dma(t ? wb : wa, wsrc, (kb * 64 + (i & 1) * 8) * 6144 + (i >> 1) * 2048 + rot(g, i >> 1) * 128, (t ? L_K : L_INB) + (i >> 1) * 8192 + w * 2048 + (i & 1) * 1024);
  };
  const unsigned fr8 = fq == fr >> 2 ? 64 + fr * 4 : fq == ((fr >> 2) + 1 & 3) ? fr * 4 : OOB;
  u32x4 ones = {0x3F803F80u, 0x3F803F80u, 0x3F803F80u, 0x3F803F80u};
  unsigned sta = 0;
  auto publish = [&](int s, bool now) __attribute__((always_inline)) {
    const unsigned a = L_RES + s * 8192 + w * 2048 + fr * 128 + fq * 8, T = c * 16 + 8 * s + 2 * wr + p2;
    u32x4 y0 = cat(lds_r2(a), lds_r2(a + 32)), y1 = cat(lds_r2(a + 64), lds_r2(a + 96));
    f32x4 sm, gm;
    pin(y0); pin(y1); pin(ones);
    nops<2>();
    mm0_v(sm, ones, y0); mm0_v(gm, y0, y0); mma_v(sm, ones, y1); mma_v(gm, y1, y1);
    if (now) { st16<0>(y0, wsr, l16, WS_XF + T * 32768 + g2 * 2048); st16<1024>(y1, wsr, l16, WS_XF + T * 32768 + g2 * 2048); }
    lds_w4(lw + L_RES + s * 8192, y0); lds_w4(lw + L_RES + s * 8192 + 1024, y1);
    nops<8>();
    pin(sm); pin(gm);
    const unsigned st = __float_as_uint(fq != fr >> 2 ? sm[0] : fr & 2 ? (fr & 1 ? gm[3] : gm[2]) : (fr & 1 ? gm[1] : gm[0]));
    if (now) st4(st, wsr, fr8, WS_ST + T * 2048 + g2 * 128); else sta = st;
  };
  auto next_tag = [&]() __attribute__((always_inline)) {
    pin(tag);
    const u64 t = (((u64)tag[1] << 32) | tag[0]) + 1; tag = (u32x2){(unsigned)t, (unsigned)(t >> 32)};
  };
  const unsigned cb = c * 16 * 32768, skv = WS_KV + (c * 32 + h) * 32768;
  const unsigned soo = WS_OF + cb + wr * 65536 + h * 1024, sxo = WS_XF + cb + (2 * wr + p2) * 32768 + g2 * 2048, sso = WS_ST + (c * 16 + 2 * wr + p2) * 2048 + g2 * 128;
  auto clear = [&](int from, int to) __attribute__((always_inline)) {
    u32x4 z = {~0u, ~0u, ~0u, ~0u};
    pin(z);
#pragma unroll
    for (int k = from; k < to; ++k) {
      if (k < 8) st16<0>(z, wsr, l16, soo + (k == 4 ? 9 : k == 5 ? 8 : k - 6) * 32768);
      else if (k < 10) st16<0>(z, wsr, l16, sxo + 4 * MB + 8 * 32768 + (k - 8) * 1024);
      else st4(z[0], wsr, fr8, sso + 136 * 2048);
    }
  };
  dma(cr, l16, (bix * 4 + w) * 1024, L_SCR + 1024);
  get_x(1);
  get_x(0);
  ld8<0>(tag, wsr, 0u, WS_EP);
  piece(0, 0);
  touch(wsr, lane < 2 ? WS_FL + (w * 512 + c * 64 + lane * 32 + h) * 128 : OOB);
  STAMP(29);

  unsigned ps[16];
  u32x8 pv = {};
#pragma unroll
  for (int j = 0; j < 3; ++j) {
    const unsigned ct = (w & 1) * 3 + j;
    pv[1 + j] = L_K + (ct >> 1) * 8192 + (4 * fq + (fr >> 2)) * 128 + (w >> 1) * 64 + (ct & 1) * 32 + (lane & 3) * 8;
    ps[j] = WS_WF + 6 * MB + ((2 * rot(g, ct >> 1) + (w >> 1)) * 192 + kb * 12 + ct) * 1024;
    ps[10 + j] = kb * 64 * 6144 + j * 2048 + rot(g, j) * 128;
  }
  { const unsigned ct = (w & 1) * 3 + (fr >> 1);
    pv[4] = ((2 * rot(g, ct >> 1) + (w >> 1)) * 16384 + ct * 128 + (fr & 1) * 64 + fq * 16) | (fr < 6 ? 0u : OOB); }
  ps[3] = WS_CS + 524288 + kb * 1024;
  ps[6] = (c >> 2) | par << 1 | (bix * 4 + w) << 10;                                            ps[7] = L_WS + w * 6144;
  ps[8] = wbase;                                             ps[9] = rho;
  ps[13] = kb * 64 * 2048 + rot(g, 1) * 128;                 ps[14] = kb * 64 * 2048 + rot(g, 2) * 128;
  ps[15] = WS_WF + (h * 4 + w) * 49152 + (rho + (c >> 2)) * 6144;
  pv[5] = wsrc;                                              pv[6] = osrc;
  u32x16 pw = {wv, L_WS + w * 6144 + (4 * fq + (fr >> 2)) * 64 + (fq & 1) * 32 + (lane & 3) * 8,
                    l16 + rho * 1024, l16 + ((rho + 2) & 7) * 1024, l16 + ((rho + 4) & 7) * 1024, l16 + ((rho + 6) & 7) * 1024,
                    L_WS + w * 6144 + (fr == 0 ? fq * 16 : 1024 + l16), L_WS + (w * 24 + (lane < 24 ? lane : 0)) * 4,
                    (w * 128 + (lane >> 3 & 1) * 64 + 2 * ((g2 - rot(0, 1 + (fq & 1))) & 15) + (lane >> 2 & 1)) * 128 + (lane & 3) * 8};
  if (w < 2) {
    u32x4 pm;
#pragma unroll
    for (int k = 0; k < 4; ++k)
      pm[k] = (8 * fq + 2 * k > 16 * w + fr ? 0xFFFFu : 0u) | (8 * fq + 2 * k + 1 > 16 * w + fr ? 0xFFFF0000u : 0u);
    lds_w4(L_PM + w * 1024 + l16, pm);
  } else {
    const unsigned want = (unsigned)((0x5555ull << 16) >> (fr + 25 - 8 * fq));
    u32x4 mb;
#pragma unroll
    for (int k = 0; k < 4; ++k) mb[k] = (want >> (15 - 4 * w - k)) & 1 ? 0u : 0xFF800000u;
    lds_w4(L_MB + (w - 2) * 1024 + l16, mb);
  }
  pv[0] = L_K + 24576 + (16 * (fq & 1) + 4 * (fq >> 1) + (fr >> 2)) * 128 + w * 32 + (lane & 3) * 8;
  pv[7] = lane < 2 ? lane * 32 * 128 : OOB;
  ps[4] = WS_WO + (rot(g, 1) * 128 + kb * 8 + w) * 1024;
  ps[5] = WS_WO + 2 * MB + (rot(g, 2) * 128 + kb * 8 + w) * 1024;
  const unsigned krow = (8 * (fr >> 2) + (fr & 3)) * 64 + wr * 2048 + L_K;
  const unsigned kv0 = 16384 + wr * 2048 + fr * 64 + ((fq ^ ((fr >> 3) << 1) ^ ((fr >> 1) & 3)) * 16);
  u32x16 va = {l16, l16 + 4096, lw, L_INB + w * 18432 + l16, L_INB + ((w + 3) & 3) * 18432 + l16, L_INB + ((w + 2) & 3) * 18432 + 6144 + l16,
                     L_INB + ((w + 1) & 3) * 18432 + 12288 + l16, kv0, kv0 ^ 48, L_CS + fq * 16, lane * 4, fl0,
                     ((lane & 31) >> 2) * 128 + (lane & 3) * 8,
                     fq < 3 ? (w * 128 + (lane >> 3 & 1) * 64 + 2 * ((2 * g - rot(g, fq % 3)) & 15) + (lane >> 2 & 1)) * 128 + (lane & 3) * 8 : ((lane & 7) * 64 + h) * 128 + w * 8,
                     fr8, krow + (fq ^ swzK(fr >> 2) ^ ((fr >> 1) & 1)) * 16};
  u32x8 vb = {L_V + wr * 2048 + (8 * fq + (fr >> 2)) * 64 + 8 * (lane & 1) + (((lane >> 1) & 1) ^ ((fq & 1) << 1) ^ (fr >> 3)) * 16,
                    krow + (fq ^ swzK(((fr >> 2) + 2) & 3) ^ ((fr >> 1) & 1)) * 16, 0, 0, xo,
                    lane < 24 ? L_CS + w * 96 + lane * 4 : L_SCR, w == 0 ? bid * 1024 + lane * 4 : OOB, 0};
  u32x16 pz = {pw[2], pw[3], pw[4], pw[5], pv[1], pv[2], pv[3], pv[4], pv[0], pw[8], pw[0], pw[1], pw[6], pw[7], pv[5], pv[6]};
  unsigned pf = pv[7];
  pin(va); pin(vb); pin(pz); pin(pf);
  wait_vm<7>();
  next_tag();
  STAMP(18);
  publish(1, true);
  publish(0, true);
  STAMP(1);
  piece(0, 1);
  wait_vm<6>();
  st8<0>(tag, wsr, lane < 2 ? lane * 32 * 128 : OOB, myfl + (2 * g2 + p2 - h) * 128);
  piece(1, 0);
  piece(1, 1);
  STAMP(15);

  u32x16 sa, sb;
#pragma unroll
  for (int i = 0; i < 4; ++i) {
    sa[i] = WS_XF + cb + w * 8192 + ((wr + 1 + i) & 3) * 65536;
    sa[4 + i] = sa[i] + WS_OF + p2 * 32768;
  }
  sa[8] = soo;                                              sa[9] = sxo;
  sa[10] = WS_ST + (c * 16 + 2 * wr) * 2048;                sa[11] = sso;
  sa[12] = skv - 16384;                                     sa[13] = c ? skv - 32 * 32768 : WS_XF + (h & 15) * 32768;
  sa[14] = c ? skv - 32 * 32768 + 8 * MB : sa[13];          sa[15] = WS_FL + (c * 64 + 8 * w) * 128;
  sb[0] = myfl;                                             sb[1] = WS_WF + (h * 4 + w) * 49152;
  sb[2] = WS_WO + (g2 * 4 + w) * 32768;                     sb[3] = c ? myfl - w * 8 + ((w - dl) & 3) * 8 - 32 * 128 + 64 : myfl + 4096;
  sb[4] = w * 96;                                           sb[5] = c ? myfl + 65536 - w * 8 + ((3 - dl) & 3) * 8 - 32 * 128 + 64 : myfl + 4096;
  sb[6] = ((c * 16 + 2 * wr + p2) * 16 * 1024 + g2 * 64) * 2;  sb[7] = WS_FL + 96;
  sb[8] = WS_CS + h * 16384;                                sb[9] = w * 2048;
  sb[10] = c ? -8 : 8 - wr;                                  sb[11] = c ? 16 * MB : 0;
  sb[12] = (2 * g2 + p2 - h) * 128;                                sb[13] = bid == 0 && w == 0 ? WS_EP : 0;
  sb[14] = c == 0 && wr == 0 ? ~0u : 0u;                     sb[15] = 0;
#pragma unroll
  for (int i = 0; i < 16; ++i) { sa[i] = uni(sa[i]); sb[i] = uni(sb[i]); }
  const rsrc_t wsk = __builtin_amdgcn_make_buffer_rsrc(ws, 0, wr == 3 ? 128 * MB : 0u, 0x00020000);
  const rsrc_t outr = __builtin_amdgcn_make_buffer_rsrc(out, 0, 4 * MB, 0x00020000);
  u32x8 pa, pc;
#pragma unroll
  for (int i = 0; i < 8; ++i) { pa[i] = uni(ps[i]); pc[i] = uni(ps[8 + i]); }
  auto convert = [&]() __attribute__((always_inline)) {
    f32x4 cs[3];
    u32x4 f[2][3];
    lds_barrier();
#pragma unroll
    for (int kk = 0; kk < 2; ++kk)
#pragma unroll
      for (int j = 0; j < 3; ++j) {
        const unsigned a = pz[4 + j] + (L_INB - L_K) + kk * 4096;
        f[kk][j] = cat(lds_tr(a), lds_tr(a + 2048));
      }
    pin(ones);
#pragma unroll
    for (int kk = 0; kk < 2; ++kk)
#pragma unroll
      for (int j = 0; j < 3; ++j) {
        if (kk) mma_v(cs[j], f[kk][j], ones); else mm0_v(cs[j], f[kk][j], ones);
        st16<0>(f[kk][j], wsr, l16, pa[j] - 6 * MB + kk * 6144);
      }
    nops<8>();
    pin(cs[0]); pin(cs[1]); pin(cs[2]);
    const f32x4 v = fr < 2 ? cs[0] : fr < 4 ? cs[1] : cs[2];
    st16<0>(__builtin_bit_cast(u32x4, v), wsr, pz[7], pa[3] - 524288);
  };
  STAMP(17);

  vb[2] = tag[0]; vb[3] = tag[1];
  STAMP(19);
  vb[7] = stw;
  asm volatile(
"; @entry\n"
"s_mov_b32 s0, 0\n"
"s_mov_b32 s3, 0\n"
"v_xor_b32_e32 v255, 32, v43\n"
"v_add_u32_e32 v254, 0xc000, v43\n"
"v_add_u32_e32 v253, 0xc000, v255\n"
"v_mov_b32_e32 v24, 0x3f803f80\n"
"v_mov_b32_e32 v25, 0x3f803f80\n"
"v_mov_b32_e32 v26, 0x3f803f80\n"
"v_mov_b32_e32 v27, 0x3f803f80\n"
"s_waitcnt lgkmcnt(0)\n"
"s_waitcnt vmcnt(13)\n"
"ds_read_b64_tr_b16 a[0:1], v43\n"
"ds_read_b64_tr_b16 a[2:3], v43 offset:1024\n"
"ds_read_b64_tr_b16 a[4:5], v255\n"
"ds_read_b64_tr_b16 a[6:7], v255 offset:1024\n"
"ds_read_b64_tr_b16 a[8:9], v43 offset:2048\n"
"ds_read_b64_tr_b16 a[10:11], v43 offset:3072\n"
"ds_read_b64_tr_b16 a[12:13], v255 offset:2048\n"
"ds_read_b64_tr_b16 a[14:15], v255 offset:3072\n"
"ds_read_b64_tr_b16 a[16:17], v43 offset:4096\n"
"ds_read_b64_tr_b16 a[18:19], v43 offset:5120\n"
"ds_read_b64_tr_b16 a[20:21], v255 offset:4096\n"
"ds_read_b64_tr_b16 a[22:23], v255 offset:5120\n"
"ds_read_b64_tr_b16 a[24:25], v43 offset:24576\n"
"ds_read_b64_tr_b16 a[26:27], v43 offset:25600\n"
"ds_read_b64_tr_b16 a[28:29], v255 offset:24576\n"
"s_waitcnt lgkmcnt(13)\n"
"ds_read_b64_tr_b16 a[30:31], v255 offset:25600\n"
"ds_read_b64_tr_b16 a[32:33], v43 offset:26624\n"
"s_waitcnt lgkmcnt(13)\n"
"ds_read_b64_tr_b16 a[34:35], v43 offset:27648\n"
"ds_read_b64_tr_b16 a[36:37], v255 offset:26624\n"
"s_waitcnt lgkmcnt(13)\n"
"ds_read_b64_tr_b16 a[38:39], v255 offset:27648\n"
"ds_read_b64_tr_b16 a[40:41], v43 offset:28672\n"
"s_waitcnt lgkmcnt(13)\n"
"ds_read_b64_tr_b16 a[42:43], v43 offset:29696\n"
"ds_read_b64_tr_b16 a[44:45], v255 offset:28672\n"
"s_waitcnt lgkmcnt(13)\n"
"ds_read_b64_tr_b16 a[46:47], v255 offset:29696\n"
"s_waitcnt lgkmcnt(12)\n"
"v_mfma_f32_16x16x32_bf16 v[28:31], a[0:3], v[24:27], 0\n"
"v_mfma_f32_16x16x32_bf16 v[48:51], a[4:7], v[24:27], 0\n"
"v_mfma_f32_16x16x32_bf16 v[52:55], a[8:11], v[24:27], 0\n"
"v_mfma_f32_16x16x32_bf16 v[56:59], a[12:15], v[24:27], 0\n"
"v_mfma_f32_16x16x32_bf16 v[60:63], a[16:19], v[24:27], 0\n"
"v_mfma_f32_16x16x32_bf16 v[64:67], a[20:23], v[24:27], 0\n"
"s_waitcnt lgkmcnt(0)\n"
"v_mfma_f32_16x16x32_bf16 v[28:31], a[24:27], v[24:27], v[28:31]\n"
"v_mfma_f32_16x16x32_bf16 v[48:51], a[28:31], v[24:27], v[48:51]\n"
"v_mfma_f32_16x16x32_bf16 v[52:55], a[32:35], v[24:27], v[52:55]\n"
"v_mfma_f32_16x16x32_bf16 v[56:59], a[36:39], v[24:27], v[56:59]\n"
"v_mfma_f32_16x16x32_bf16 v[60:63], a[40:43], v[24:27], v[60:63]\n"
"v_mfma_f32_16x16x32_bf16 v[64:67], a[44:47], v[24:27], v[64:67]\n"
"s_bfe_u32 s98, s26, 0x10001\n"
"s_xor_b32 s98, s98, 0\n"
"s_mul_i32 s97, s98, 0x6000\n"
"s_add_u32 s97, s97, s27\n"
"s_add_u32 s96, s45, 4\n"
"s_and_b32 s96, s96, 7\n"
"s_add_u32 s96, s96, s98\n"
"s_mul_i32 s96, s96, 0x30000\n"
"s_add_u32 s96, s96, s44\n"
"s_add_u32 m0, s97, 0\n"
"s_add_u32 s98, s96, 0\n"
"buffer_load_dwordx4 v42, s[28:31], s98 offen lds\n"
"s_add_u32 m0, s97, 0x400\n"
"s_add_u32 s98, s96, 0x18000\n"
"buffer_load_dwordx4 v42, s[28:31], s98 offen lds\n"
"s_add_u32 m0, s97, 0x800\n"
"s_add_u32 s98, s96, 0x800\n"
"buffer_load_dwordx4 v42, s[28:31], s98 offen lds\n"
"s_add_u32 m0, s97, 0xc00\n"
"s_add_u32 s98, s96, 0x18800\n"
"buffer_load_dwordx4 v42, s[28:31], s98 offen lds\n"
"s_add_u32 m0, s97, 0x1000\n"
"s_add_u32 s98, s96, 0x1000\n"
"buffer_load_dwordx4 v42, s[28:31], s98 offen lds\n"
"s_add_u32 m0, s97, 0x1400\n"
"s_add_u32 s98, s96, 0x19000\n"
"buffer_load_dwordx4 v42, s[28:31], s98 offen lds\n"
"s_bfe_u32 s98, s26, 0x10001\n"
"s_xor_b32 s98, s98, 1\n"
"s_mul_i32 s97, s98, 0x6000\n"
"s_add_u32 s97, s97, s27\n"
"s_add_u32 s96, s45, 4\n"
"s_and_b32 s96, s96, 7\n"
"s_add_u32 s96, s96, s98\n"
"s_mul_i32 s96, s96, 0x30000\n"
"s_add_u32 s96, s96, s44\n"
"s_add_u32 m0, s97, 0\n"
"s_add_u32 s98, s96, 0\n"
"buffer_load_dwordx4 v42, s[28:31], s98 offen lds\n"
"s_add_u32 m0, s97, 0x400\n"
"s_add_u32 s98, s96, 0x18000\n"
"buffer_load_dwordx4 v42, s[28:31], s98 offen lds\n"
"s_add_u32 m0, s97, 0x800\n"
"s_add_u32 s98, s96, 0x800\n"
"buffer_load_dwordx4 v42, s[28:31], s98 offen lds\n"
"s_add_u32 m0, s97, 0xc00\n"
"s_add_u32 s98, s96, 0x18800\n"
"buffer_load_dwordx4 v42, s[28:31], s98 offen lds\n"
"s_add_u32 m0, s97, 0x1000\n"
"s_add_u32 s98, s96, 0x1000\n"
"buffer_load_dwordx4 v42, s[28:31], s98 offen lds\n"
"s_add_u32 m0, s97, 0x1400\n"
"s_add_u32 s98, s96, 0x19000\n"
"buffer_load_dwordx4 v42, s[28:31], s98 offen lds\n"
"s_waitcnt vmcnt(12)\n"
"ds_read_b64_tr_b16 a[48:49], v254\n"
"ds_read_b64_tr_b16 a[50:51], v254 offset:1024\n"
"ds_read_b64_tr_b16 a[52:53], v253\n"
"ds_read_b64_tr_b16 a[54:55], v253 offset:1024\n"
"ds_read_b64_tr_b16 a[56:57], v254 offset:2048\n"
"ds_read_b64_tr_b16 a[58:59], v254 offset:3072\n"
"ds_read_b64_tr_b16 a[60:61], v253 offset:2048\n"
"ds_read_b64_tr_b16 a[62:63], v253 offset:3072\n"
"ds_read_b64_tr_b16 a[64:65], v254 offset:4096\n"
"ds_read_b64_tr_b16 a[66:67], v254 offset:5120\n"
"ds_read_b64_tr_b16 a[68:69], v253 offset:4096\n"
"ds_read_b64_tr_b16 a[70:71], v253 offset:5120\n"
"ds_read_b64_tr_b16 a[72:73], v254 offset:24576\n"
"ds_read_b64_tr_b16 a[74:75], v254 offset:25600\n"
"ds_read_b64_tr_b16 a[76:77], v253 offset:24576\n"
"s_waitcnt lgkmcnt(13)\n"
"ds_read_b64_tr_b16 a[78:79], v253 offset:25600\n"
"ds_read_b64_tr_b16 a[80:81], v254 offset:26624\n"
"s_waitcnt lgkmcnt(13)\n"
"ds_read_b64_tr_b16 a[82:83], v254 offset:27648\n"
"ds_read_b64_tr_b16 a[84:85], v253 offset:26624\n"
"s_waitcnt lgkmcnt(13)\n"
"ds_read_b64_tr_b16 a[86:87], v253 offset:27648\n"
"ds_read_b64_tr_b16 a[88:89], v254 offset:28672\n"
"s_waitcnt lgkmcnt(13)\n"
"ds_read_b64_tr_b16 a[90:91], v254 offset:29696\n"
"ds_read_b64_tr_b16 a[92:93], v253 offset:28672\n"
"s_waitcnt lgkmcnt(13)\n"
"ds_read_b64_tr_b16 a[94:95], v253 offset:29696\n"
"s_waitcnt lgkmcnt(12)\n"
"v_mfma_f32_16x16x32_bf16 v[28:31], a[48:51], v[24:27], v[28:31]\n"
"v_mfma_f32_16x16x32_bf16 v[48:51], a[52:55], v[24:27], v[48:51]\n"
"v_mfma_f32_16x16x32_bf16 v[52:55], a[56:59], v[24:27], v[52:55]\n"
"v_mfma_f32_16x16x32_bf16 v[56:59], a[60:63], v[24:27], v[56:59]\n"
"v_mfma_f32_16x16x32_bf16 v[60:63], a[64:67], v[24:27], v[60:63]\n"
"v_mfma_f32_16x16x32_bf16 v[64:67], a[68:71], v[24:27], v[64:67]\n"
"s_waitcnt lgkmcnt(0)\n"
"v_mfma_f32_16x16x32_bf16 v[28:31], a[72:75], v[24:27], v[28:31]\n"
"v_mfma_f32_16x16x32_bf16 v[48:51], a[76:79], v[24:27], v[48:51]\n"
"v_mfma_f32_16x16x32_bf16 v[52:55], a[80:83], v[24:27], v[52:55]\n"
"v_mfma_f32_16x16x32_bf16 v[56:59], a[84:87], v[24:27], v[56:59]\n"
"v_mfma_f32_16x16x32_bf16 v[60:63], a[88:91], v[24:27], v[60:63]\n"
"v_mfma_f32_16x16x32_bf16 v[64:67], a[92:95], v[24:27], v[64:67]\n"
"s_bfe_u32 s98, s26, 0x10001\n"
"s_xor_b32 s98, s98, 0\n"
"s_mul_i32 s97, s98, 0x6000\n"
"s_add_u32 s97, s97, s27\n"
"s_add_u32 s96, s45, 6\n"
"s_and_b32 s96, s96, 7\n"
"s_add_u32 s96, s96, s98\n"
"s_mul_i32 s96, s96, 0x30000\n"
"s_add_u32 s96, s96, s44\n"
"s_add_u32 m0, s97, 0xc000\n"
"s_add_u32 s98, s96, 0\n"
"buffer_load_dwordx4 v42, s[28:31], s98 offen lds\n"
"s_add_u32 m0, s97, 0xc400\n"
"s_add_u32 s98, s96, 0x18000\n"
"buffer_load_dwordx4 v42, s[28:31], s98 offen lds\n"
"s_add_u32 m0, s97, 0xc800\n"
"s_add_u32 s98, s96, 0x800\n"
"buffer_load_dwordx4 v42, s[28:31], s98 offen lds\n"
"s_add_u32 m0, s97, 0xcc00\n"
"s_add_u32 s98, s96, 0x18800\n"
"buffer_load_dwordx4 v42, s[28:31], s98 offen lds\n"
"s_add_u32 m0, s97, 0xd000\n"
"s_add_u32 s98, s96, 0x1000\n"
"buffer_load_dwordx4 v42, s[28:31], s98 offen lds\n"
"s_add_u32 m0, s97, 0xd400\n"
"s_add_u32 s98, s96, 0x19000\n"
"buffer_load_dwordx4 v42, s[28:31], s98 offen lds\n"
"s_bfe_u32 s98, s26, 0x10001\n"
"s_xor_b32 s98, s98, 1\n"
"s_mul_i32 s97, s98, 0x6000\n"
"s_add_u32 s97, s97, s27\n"
"s_add_u32 s96, s45, 6\n"
"s_and_b32 s96, s96, 7\n"
"s_add_u32 s96, s96, s98\n"
"s_mul_i32 s96, s96, 0x30000\n"
"s_add_u32 s96, s96, s44\n"
"s_add_u32 m0, s97, 0xc000\n"
"s_add_u32 s98, s96, 0\n"
"buffer_load_dwordx4 v42, s[28:31], s98 offen lds\n"
"s_add_u32 m0, s97, 0xc400\n"
"s_add_u32 s98, s96, 0x18000\n"
"buffer_load_dwordx4 v42, s[28:31], s98 offen lds\n"
"s_add_u32 m0, s97, 0xc800\n"
"s_add_u32 s98, s96, 0x800\n"
"buffer_load_dwordx4 v42, s[28:31], s98 offen lds\n"
"s_add_u32 m0, s97, 0xcc00\n"
"s_add_u32 s98, s96, 0x18800\n"
"buffer_load_dwordx4 v42, s[28:31], s98 offen lds\n"
"s_add_u32 m0, s97, 0xd000\n"
"s_add_u32 s98, s96, 0x1000\n"
"buffer_load_dwordx4 v42, s[28:31], s98 offen lds\n"
"s_add_u32 m0, s97, 0xd400\n"
"s_add_u32 s98, s96, 0x19000\n"
"buffer_load_dwordx4 v42, s[28:31], s98 offen lds\n"
"s_add_u32 s98, s67, 0x1000\n"
"buffer_load_dwordx2 v[250:251], v12, s[4:7], s98 offen sc1\n"
"s_waitcnt vmcnt(13)\n"
"ds_read_b64_tr_b16 a[96:97], v43\n"
"ds_read_b64_tr_b16 a[98:99], v43 offset:1024\n"
"ds_read_b64_tr_b16 a[100:101], v255\n"
"ds_read_b64_tr_b16 a[102:103], v255 offset:1024\n"
"ds_read_b64_tr_b16 a[104:105], v43 offset:2048\n"
"ds_read_b64_tr_b16 a[106:107], v43 offset:3072\n"
"ds_read_b64_tr_b16 a[108:109], v255 offset:2048\n"
"ds_read_b64_tr_b16 a[110:111], v255 offset:3072\n"
"ds_read_b64_tr_b16 a[112:113], v43 offset:4096\n"
"ds_read_b64_tr_b16 a[114:115], v43 offset:5120\n"
"ds_read_b64_tr_b16 a[116:117], v255 offset:4096\n"
"ds_read_b64_tr_b16 a[118:119], v255 offset:5120\n"
"ds_read_b64_tr_b16 a[120:121], v43 offset:24576\n"
"ds_read_b64_tr_b16 a[122:123], v43 offset:25600\n"
"ds_read_b64_tr_b16 a[124:125], v255 offset:24576\n"
"s_waitcnt lgkmcnt(13)\n"
"ds_read_b64_tr_b16 a[126:127], v255 offset:25600\n"
"ds_read_b64_tr_b16 v[68:69], v43 offset:26624\n"
"s_waitcnt lgkmcnt(13)\n"
"ds_read_b64_tr_b16 v[70:71], v43 offset:27648\n"
"ds_read_b64_tr_b16 v[72:73], v255 offset:26624\n"
"s_waitcnt lgkmcnt(13)\n"
"ds_read_b64_tr_b16 v[74:75], v255 offset:27648\n"
"ds_read_b64_tr_b16 v[76:77], v43 offset:28672\n"
"s_waitcnt lgkmcnt(13)\n"
"ds_read_b64_tr_b16 v[78:79], v43 offset:29696\n"
"ds_read_b64_tr_b16 v[80:81], v255 offset:28672\n"
"s_waitcnt lgkmcnt(13)\n"
"ds_read_b64_tr_b16 v[82:83], v255 offset:29696\n"
"s_waitcnt lgkmcnt(12)\n"
"v_mfma_f32_16x16x32_bf16 v[28:31], a[96:99], v[24:27], v[28:31]\n"
"v_mfma_f32_16x16x32_bf16 v[48:51], a[100:103], v[24:27], v[48:51]\n"
"v_mfma_f32_16x16x32_bf16 v[52:55], a[104:107], v[24:27], v[52:55]\n"
"v_mfma_f32_16x16x32_bf16 v[56:59], a[108:111], v[24:27], v[56:59]\n"
"v_mfma_f32_16x16x32_bf16 v[60:63], a[112:115], v[24:27], v[60:63]\n"
"v_mfma_f32_16x16x32_bf16 v[64:67], a[116:119], v[24:27], v[64:67]\n"
"s_waitcnt lgkmcnt(0)\n"
"v_mfma_f32_16x16x32_bf16 v[28:31], a[120:123], v[24:27], v[28:31]\n"
"v_mfma_f32_16x16x32_bf16 v[48:51], a[124:127], v[24:27], v[48:51]\n"
"v_mfma_f32_16x16x32_bf16 v[52:55], v[68:71], v[24:27], v[52:55]\n"
"v_mfma_f32_16x16x32_bf16 v[56:59], v[72:75], v[24:27], v[56:59]\n"
"v_mfma_f32_16x16x32_bf16 v[60:63], v[76:79], v[24:27], v[60:63]\n"
"v_mfma_f32_16x16x32_bf16 v[64:67], v[80:83], v[24:27], v[64:67]\n"
"s_waitcnt vmcnt(0)\n"
"v_cmp_ne_u64_e32 vcc, v[250:251], v[18:19]\n"
"s_cbranch_vccnz .Lmk_f1s\n"
".Lmk_f1r:\n"
"s_add_u32 s98, s52, 0x40000\n"
"buffer_load_dwordx4 v[84:87], v32, s[4:7], s98 offen sc1\n"
"buffer_load_dwordx4 v[88:91], v32, s[4:7], s98 offen offset:1024 sc1\n"
"buffer_load_dwordx4 v[92:95], v33, s[4:7], s98 offen sc1\n"
"buffer_load_dwordx4 v[96:99], v33, s[4:7], s98 offen offset:1024 sc1\n"
"buffer_load_dwordx4 v[100:103], v34, s[4:7], s98 offen sc1\n"
"buffer_load_dwordx4 v[104:107], v34, s[4:7], s98 offen offset:1024 sc1\n"
"buffer_load_dwordx4 v[108:111], v35, s[4:7], s98 offen sc1\n"
"buffer_load_dwordx4 v[112:115], v35, s[4:7], s98 offen offset:1024 sc1\n"
"s_add_u32 s98, s53, 0x40000\n"
"buffer_load_dwordx4 v[116:119], v32, s[4:7], s98 offen sc1\n"
"buffer_load_dwordx4 v[120:123], v32, s[4:7], s98 offen offset:1024 sc1\n"
"buffer_load_dwordx4 v[124:127], v33, s[4:7], s98 offen sc1\n"
"buffer_load_dwordx4 v[128:131], v33, s[4:7], s98 offen offset:1024 sc1\n"
"buffer_load_dwordx4 v[132:135], v34, s[4:7], s98 offen sc1\n"
"buffer_load_dwordx4 v[136:139], v34, s[4:7], s98 offen offset:1024 sc1\n"
"buffer_load_dwordx4 v[140:143], v35, s[4:7], s98 offen sc1\n"
"buffer_load_dwordx4 v[144:147], v35, s[4:7], s98 offen offset:1024 sc1\n"
"s_getpc_b64 s[84:85]\n"
".Lmk_h2:\n"
"s_and_b32 s84, s84, -16\n"
"s_and_b32 s85, s85, 0xffff\n"
"s_mov_b32 s86, (.Lmk_end-.Lmk_h2)&0xFFFFFFF0\n"
"s_mov_b32 s87, 0x20000\n"
"s_and_b32 s98, s26, 0xfffffc00\n"
"s_mov_b32 m0, 0x27800\n"
"s_add_u32 s98, s98, 0x1e000\n"
"buffer_load_dwordx4 v0, s[84:87], s98 offen lds\n"
"ds_read_b64_tr_b16 v[148:149], v254\n"
"ds_read_b64_tr_b16 v[150:151], v254 offset:1024\n"
"ds_read_b64_tr_b16 v[152:153], v253\n"
"ds_read_b64_tr_b16 v[154:155], v253 offset:1024\n"
"ds_read_b64_tr_b16 v[156:157], v254 offset:2048\n"
"ds_read_b64_tr_b16 v[158:159], v254 offset:3072\n"
"ds_read_b64_tr_b16 v[160:161], v253 offset:2048\n"
"ds_read_b64_tr_b16 v[162:163], v253 offset:3072\n"
"ds_read_b64_tr_b16 v[164:165], v254 offset:4096\n"
"ds_read_b64_tr_b16 v[166:167], v254 offset:5120\n"
"ds_read_b64_tr_b16 v[168:169], v253 offset:4096\n"
"ds_read_b64_tr_b16 v[170:171], v253 offset:5120\n"
"ds_read_b64_tr_b16 v[172:173], v254 offset:24576\n"
"ds_read_b64_tr_b16 v[174:175], v254 offset:25600\n"
"ds_read_b64_tr_b16 v[176:177], v253 offset:24576\n"
"s_waitcnt lgkmcnt(13)\n"
"ds_read_b64_tr_b16 v[178:179], v253 offset:25600\n"
"ds_read_b64_tr_b16 v[180:181], v254 offset:26624\n"
"s_waitcnt lgkmcnt(13)\n"
"ds_read_b64_tr_b16 v[182:183], v254 offset:27648\n"
"ds_read_b64_tr_b16 v[184:185], v253 offset:26624\n"
"s_waitcnt lgkmcnt(13)\n"
"ds_read_b64_tr_b16 v[186:187], v253 offset:27648\n"
"ds_read_b64_tr_b16 v[188:189], v254 offset:28672\n"
"s_waitcnt lgkmcnt(13)\n"
"ds_read_b64_tr_b16 v[190:191], v254 offset:29696\n"
"ds_read_b64_tr_b16 v[192:193], v253 offset:28672\n"
"s_waitcnt lgkmcnt(13)\n"
"ds_read_b64_tr_b16 v[194:195], v253 offset:29696\n"
"s_waitcnt lgkmcnt(12)\n"
"v_mfma_f32_16x16x32_bf16 v[28:31], v[148:151], v[24:27], v[28:31]\n"
"v_mfma_f32_16x16x32_bf16 v[48:51], v[152:155], v[24:27], v[48:51]\n"
"v_mfma_f32_16x16x32_bf16 v[52:55], v[156:159], v[24:27], v[52:55]\n"
"v_mfma_f32_16x16x32_bf16 v[56:59], v[160:163], v[24:27], v[56:59]\n"
"v_mfma_f32_16x16x32_bf16 v[60:63], v[164:167], v[24:27], v[60:63]\n"
"v_mfma_f32_16x16x32_bf16 v[64:67], v[168:171], v[24:27], v[64:67]\n"
"s_waitcnt lgkmcnt(0)\n"
"v_mfma_f32_16x16x32_bf16 v[28:31], v[172:175], v[24:27], v[28:31]\n"
"v_mfma_f32_16x16x32_bf16 v[48:51], v[176:179], v[24:27], v[48:51]\n"
"v_mfma_f32_16x16x32_bf16 v[52:55], v[180:183], v[24:27], v[52:55]\n"
"v_mfma_f32_16x16x32_bf16 v[56:59], v[184:187], v[24:27], v[56:59]\n"
"v_mfma_f32_16x16x32_bf16 v[60:63], v[188:191], v[24:27], v[60:63]\n"
"v_mfma_f32_16x16x32_bf16 v[64:67], v[192:195], v[24:27], v[64:67]\n"
"s_nop 7\n"
"ds_write_b128 v44, v[28:31]\n"
"ds_write_b128 v44, v[48:51] offset:64\n"
"ds_write_b128 v44, v[52:55] offset:128\n"
"ds_write_b128 v44, v[56:59] offset:192\n"
"ds_write_b128 v44, v[60:63] offset:256\n"
"ds_write_b128 v44, v[64:67] offset:320\n"
"s_waitcnt lgkmcnt(0)\n"
"s_barrier\n"
"ds_read_b32 v255, v45\n"
"ds_read_b32 v254, v45 offset:6144\n"
"ds_read_b32 v253, v45 offset:12288\n"
"ds_read_b32 v252, v45 offset:18432\n"
"s_waitcnt lgkmcnt(0)\n"
"v_add_f32_e32 v255, v255, v254\n"
"v_add_f32_e32 v253, v253, v252\n"
"v_add_f32_e32 v255, v255, v253\n"
"ds_write_b32 v21, v255\n"
".Lmk_z0:\n"
"s_waitcnt vmcnt(16)\n"
"; @A G1 0.0\n"
"v_mfma_f32_16x16x32_bf16 v[24:27], a[0:3], v[84:87], 0\n"
"v_mfma_f32_16x16x32_bf16 v[28:31], a[4:7], v[84:87], 0\n"
"s_add_u32 m0, s77, 0xa000\n"
"s_add_u32 s98, s49, 0\n"
"buffer_load_dwordx4 v47, s[36:39], s98 offen lds\n"
"v_mfma_f32_16x16x32_bf16 v[48:51], a[8:11], v[84:87], 0\n"
"s_add_u32 m0, s77, 0xa400\n"
"s_add_u32 s98, s49, 0x4000\n"
"s_add_u32 s97, s62, 0x4000\n"
"v_mfma_f32_16x16x32_bf16 v[52:55], a[12:15], v[84:87], 0\n"
"v_mfma_f32_16x16x32_bf16 v[56:59], a[16:19], v[84:87], 0\n"
"s_add_u32 s96, s54, 0x40000\n"
"buffer_load_dwordx4 v[60:63], v32, s[4:7], s96 offen sc1\n"
"v_mfma_f32_16x16x32_bf16 v[64:67], a[20:23], v[84:87], 0\n"
"s_waitcnt vmcnt(17)\n"
"buffer_load_dwordx4 v47, s[36:39], s98 offen lds\n"
"v_mfma_f32_16x16x32_bf16 v[24:27], a[24:27], v[88:91], v[24:27]\n"
"s_add_u32 m0, s77, 0xc000\n"
"s_add_u32 s98, s50, 0\n"
"v_mfma_f32_16x16x32_bf16 v[28:31], a[28:31], v[88:91], v[28:31]\n"
"buffer_load_dword v255, v10, s[4:7], s97 offen sc1\n"
"v_mfma_f32_16x16x32_bf16 v[48:51], a[32:35], v[88:91], v[48:51]\n"
"buffer_load_dwordx4 v47, s[40:43], s98 offen lds\n"
"v_mfma_f32_16x16x32_bf16 v[52:55], a[36:39], v[88:91], v[52:55]\n"
"s_add_u32 m0, s77, 0xc400\n"
"s_add_u32 s98, s50, 0x4000\n"
"v_mfma_f32_16x16x32_bf16 v[56:59], a[40:43], v[88:91], v[56:59]\n"
"buffer_load_dwordx4 v[84:87], v32, s[4:7], s96 offen offset:1024 sc1\n"
"v_mfma_f32_16x16x32_bf16 v[64:67], a[44:47], v[88:91], v[64:67]\n"
"s_waitcnt vmcnt(20)\n"
"buffer_load_dwordx4 v47, s[40:43], s98 offen lds\n"
"v_mfma_f32_16x16x32_bf16 v[24:27], a[48:51], v[92:95], v[24:27]\n"
"s_add_u32 m0, s77, 0x4000\n"
"s_add_u32 s98, s46, 0\n"
"v_mfma_f32_16x16x32_bf16 v[28:31], a[52:55], v[92:95], v[28:31]\n"
"buffer_load_dword v254, v10, s[4:7], s97 offen offset:256 sc1\n"
"v_mfma_f32_16x16x32_bf16 v[48:51], a[56:59], v[92:95], v[48:51]\n"
"buffer_load_dwordx4 v46, s[32:35], s98 offen lds\n"
"v_mfma_f32_16x16x32_bf16 v[52:55], a[60:63], v[92:95], v[52:55]\n"
"s_add_u32 m0, s77, 0x4400\n"
"s_add_u32 s98, s46, 0xc000\n"
"v_mfma_f32_16x16x32_bf16 v[56:59], a[64:67], v[92:95], v[56:59]\n"
"buffer_load_dwordx4 v[88:91], v33, s[4:7], s96 offen sc1\n"
"v_mfma_f32_16x16x32_bf16 v[64:67], a[68:71], v[92:95], v[64:67]\n"
"s_waitcnt vmcnt(23)\n"
"buffer_load_dwordx4 v46, s[32:35], s98 offen lds\n"
"v_mfma_f32_16x16x32_bf16 v[24:27], a[72:75], v[96:99], v[24:27]\n"
"s_add_u32 m0, s77, 0x6000\n"
"s_add_u32 s98, s47, 0\n"
"v_mfma_f32_16x16x32_bf16 v[28:31], a[76:79], v[96:99], v[28:31]\n"
"buffer_load_dword v253, v10, s[4:7], s97 offen offset:512 sc1\n"
"v_mfma_f32_16x16x32_bf16 v[48:51], a[80:83], v[96:99], v[48:51]\n"
"buffer_load_dwordx4 v46, s[32:35], s98 offen lds\n"
"v_mfma_f32_16x16x32_bf16 v[52:55], a[84:87], v[96:99], v[52:55]\n"
"s_add_u32 m0, s77, 0x6400\n"
"s_add_u32 s98, s47, 0xc000\n"
"v_mfma_f32_16x16x32_bf16 v[56:59], a[88:91], v[96:99], v[56:59]\n"
"buffer_load_dwordx4 v[92:95], v33, s[4:7], s96 offen offset:1024 sc1\n"
"v_mfma_f32_16x16x32_bf16 v[64:67], a[92:95], v[96:99], v[64:67]\n"
"s_waitcnt vmcnt(26)\n"
"buffer_load_dwordx4 v46, s[32:35], s98 offen lds\n"
"v_mfma_f32_16x16x32_bf16 v[24:27], a[96:99], v[100:103], v[24:27]\n"
"s_add_u32 m0, s77, 0x8000\n"
"s_add_u32 s98, s48, 0\n"
"v_mfma_f32_16x16x32_bf16 v[28:31], a[100:103], v[100:103], v[28:31]\n"
"buffer_load_dword v252, v10, s[4:7], s97 offen offset:768 sc1\n"
"v_mfma_f32_16x16x32_bf16 v[48:51], a[104:107], v[100:103], v[48:51]\n"
"buffer_load_dwordx4 v46, s[32:35], s98 offen lds\n"
"v_mfma_f32_16x16x32_bf16 v[52:55], a[108:111], v[100:103], v[52:55]\n"
"s_add_u32 m0, s77, 0x8400\n"
"s_add_u32 s98, s48, 0xc000\n"
"v_mfma_f32_16x16x32_bf16 v[56:59], a[112:115], v[100:103], v[56:59]\n"
"buffer_load_dwordx4 v[96:99], v34, s[4:7], s96 offen sc1\n"
"v_mfma_f32_16x16x32_bf16 v[64:67], a[116:119], v[100:103], v[64:67]\n"
"s_waitcnt vmcnt(29)\n"
"buffer_load_dwordx4 v46, s[32:35], s98 offen lds\n"
"v_mfma_f32_16x16x32_bf16 v[24:27], a[120:123], v[104:107], v[24:27]\n"
"buffer_load_dword v251, v10, s[4:7], s97 offen offset:1024 sc1\n"
"v_mfma_f32_16x16x32_bf16 v[28:31], a[124:127], v[104:107], v[28:31]\n"
"v_mfma_f32_16x16x32_bf16 v[48:51], v[68:71], v[104:107], v[48:51]\n"
"v_mfma_f32_16x16x32_bf16 v[52:55], v[72:75], v[104:107], v[52:55]\n"
"buffer_load_dword v250, v10, s[4:7], s97 offen offset:1280 sc1\n"
"v_mfma_f32_16x16x32_bf16 v[56:59], v[76:79], v[104:107], v[56:59]\n"
"buffer_load_dwordx4 v[44:47], v34, s[4:7], s96 offen offset:1024 sc1\n"
"v_mfma_f32_16x16x32_bf16 v[64:67], v[80:83], v[104:107], v[64:67]\n"
"s_waitcnt vmcnt(32)\n"
"v_mfma_f32_16x16x32_bf16 v[24:27], v[148:151], v[108:111], v[24:27]\n"
"buffer_load_dword v249, v10, s[4:7], s97 offen offset:1536 sc1\n"
"v_mfma_f32_16x16x32_bf16 v[28:31], v[152:155], v[108:111], v[28:31]\n"
"v_mfma_f32_16x16x32_bf16 v[48:51], v[156:159], v[108:111], v[48:51]\n"
"v_mfma_f32_16x16x32_bf16 v[52:55], v[160:163], v[108:111], v[52:55]\n"
"buffer_load_dword v248, v10, s[4:7], s97 offen offset:1792 sc1\n"
"v_mfma_f32_16x16x32_bf16 v[56:59], v[164:167], v[108:111], v[56:59]\n"
"buffer_load_dwordx4 v[100:103], v35, s[4:7], s96 offen sc1\n"
"v_mfma_f32_16x16x32_bf16 v[64:67], v[168:171], v[108:111], v[64:67]\n"
"s_waitcnt vmcnt(34)\n"
"v_mfma_f32_16x16x32_bf16 v[24:27], v[172:175], v[112:115], v[24:27]\n"
"v_mfma_f32_16x16x32_bf16 v[28:31], v[176:179], v[112:115], v[28:31]\n"
"v_mfma_f32_16x16x32_bf16 v[48:51], v[180:183], v[112:115], v[48:51]\n"
"v_mfma_f32_16x16x32_bf16 v[52:55], v[184:187], v[112:115], v[52:55]\n"
"v_mfma_f32_16x16x32_bf16 v[56:59], v[188:191], v[112:115], v[56:59]\n"
"buffer_load_dwordx4 v[104:107], v35, s[4:7], s96 offen offset:1024 sc1\n"
"v_mfma_f32_16x16x32_bf16 v[64:67], v[192:195], v[112:115], v[64:67]\n"
"s_waitcnt vmcnt(33)\n"
"; @A G1 0.1\n"
"v_mfma_f32_16x16x32_bf16 v[108:111], a[0:3], v[116:119], 0\n"
"v_mfma_f32_16x16x32_bf16 v[112:115], a[4:7], v[116:119], 0\n"
"v_mfma_f32_16x16x32_bf16 v[196:199], a[8:11], v[116:119], 0\n"
"v_mfma_f32_16x16x32_bf16 v[200:203], a[12:15], v[116:119], 0\n"
"ds_write_b128 v3, v[24:27]\n"
"v_mfma_f32_16x16x32_bf16 v[24:27], a[16:19], v[116:119], 0\n"
"s_add_u32 s98, s55, 0x40000\n"
"buffer_load_dwordx4 v[204:207], v32, s[4:7], s98 offen sc1\n"
"v_mfma_f32_16x16x32_bf16 v[208:211], a[20:23], v[116:119], 0\n"
"v_mfma_f32_16x16x32_bf16 v[108:111], a[24:27], v[120:123], v[108:111]\n"
"ds_write_b128 v3, v[28:31] offset:1024\n"
"v_mfma_f32_16x16x32_bf16 v[112:115], a[28:31], v[120:123], v[112:115]\n"
"v_mfma_f32_16x16x32_bf16 v[196:199], a[32:35], v[120:123], v[196:199]\n"
"v_mfma_f32_16x16x32_bf16 v[200:203], a[36:39], v[120:123], v[200:203]\n"
"ds_write_b128 v3, v[48:51] offset:2048\n"
"v_mfma_f32_16x16x32_bf16 v[24:27], a[40:43], v[120:123], v[24:27]\n"
"buffer_load_dwordx4 v[28:31], v32, s[4:7], s98 offen offset:1024 sc1\n"
"v_mfma_f32_16x16x32_bf16 v[208:211], a[44:47], v[120:123], v[208:211]\n"
"s_waitcnt vmcnt(33)\n"
"v_mfma_f32_16x16x32_bf16 v[108:111], a[48:51], v[124:127], v[108:111]\n"
"ds_write_b128 v3, v[52:55] offset:3072\n"
"v_mfma_f32_16x16x32_bf16 v[112:115], a[52:55], v[124:127], v[112:115]\n"
"v_mfma_f32_16x16x32_bf16 v[196:199], a[56:59], v[124:127], v[196:199]\n"
"v_mfma_f32_16x16x32_bf16 v[200:203], a[60:63], v[124:127], v[200:203]\n"
"ds_write_b128 v3, v[56:59] offset:4096\n"
"v_mfma_f32_16x16x32_bf16 v[24:27], a[64:67], v[124:127], v[24:27]\n"
"buffer_load_dwordx4 v[48:51], v33, s[4:7], s98 offen sc1\n"
"v_mfma_f32_16x16x32_bf16 v[208:211], a[68:71], v[124:127], v[208:211]\n"
"v_mfma_f32_16x16x32_bf16 v[108:111], a[72:75], v[128:131], v[108:111]\n"
"ds_write_b128 v3, v[64:67] offset:5120\n"
"v_mfma_f32_16x16x32_bf16 v[112:115], a[76:79], v[128:131], v[112:115]\n"
"s_waitcnt vmcnt(5)\n"
"v_add_f32_e32 v255, v255, v251\n"
"v_add_f32_e32 v254, v254, v250\n"
"v_mfma_f32_16x16x32_bf16 v[196:199], a[80:83], v[128:131], v[196:199]\n"
"v_add_f32_e32 v253, v253, v249\n"
"v_add_f32_e32 v252, v252, v248\n"
"v_mfma_f32_16x16x32_bf16 v[200:203], a[84:87], v[128:131], v[200:203]\n"
"v_add_f32_e32 v255, v255, v253\n"
"v_add_f32_e32 v254, v254, v252\n"
"v_mfma_f32_16x16x32_bf16 v[24:27], a[88:91], v[128:131], v[24:27]\n"
"buffer_load_dwordx4 v[52:55], v33, s[4:7], s98 offen offset:1024 sc1\n"
"v_mfma_f32_16x16x32_bf16 v[208:211], a[92:95], v[128:131], v[208:211]\n"
"v_add_f32_e32 v255, v255, v254\n"
"v_mov_b32_e32 v254, v255\n"
"v_mfma_f32_16x16x32_bf16 v[108:111], a[96:99], v[132:135], v[108:111]\n"
"s_nop 0\n"
"v_permlane32_swap_b32_e32 v255, v254\n"
"v_add_f32_e32 v255, v255, v254\n"
"v_mfma_f32_16x16x32_bf16 v[112:115], a[100:103], v[132:135], v[112:115]\n"
"v_mul_f32_e32 v255, 0x3a800000, v255\n"
"v_mov_b32_e32 v254, v255\n"
"v_mfma_f32_16x16x32_bf16 v[196:199], a[104:107], v[132:135], v[196:199]\n"
"s_nop 0\n"
"v_permlane16_swap_b32_e32 v255, v254\n"
"v_cvt_pk_bf16_f32 v253, -v255, 0\n"
"v_mfma_f32_16x16x32_bf16 v[200:203], a[108:111], v[132:135], v[200:203]\n"
"v_fma_f32 v254, -v255, v255, v254\n"
"v_lshlrev_b32_e32 v253, 16, v253\n"
"v_mfma_f32_16x16x32_bf16 v[24:27], a[112:115], v[132:135], v[24:27]\n"
"buffer_load_dwordx4 v[56:59], v34, s[4:7], s98 offen sc1\n"
"v_mfma_f32_16x16x32_bf16 v[208:211], a[116:119], v[132:135], v[208:211]\n"
"s_waitcnt lgkmcnt(0)\n"
"s_barrier\n"
"v_mfma_f32_16x16x32_bf16 v[108:111], a[120:123], v[136:139], v[108:111]\n"
"v_max_f32_e32 v254, 0, v254\n"
"v_cvt_pk_bf16_f32 v254, v254, 0\n"
"v_mfma_f32_16x16x32_bf16 v[112:115], a[124:127], v[136:139], v[112:115]\n"
"ds_read_b64_tr_b16 v[64:65], v40\n"
"v_lshlrev_b32_e32 v254, 16, v254\n"
"v_add_f32_e32 v254, 0x3727c5ac, v254\n"
"v_mfma_f32_16x16x32_bf16 v[196:199], v[68:71], v[136:139], v[196:199]\n"
"ds_read_b128 v[116:119], v4\n"
"ds_read_b128 v[120:123], v9\n"
"ds_read_b64_tr_b16 v[66:67], v40 offset:1024\n"
"v_mfma_f32_16x16x32_bf16 v[200:203], v[72:75], v[136:139], v[200:203]\n"
"ds_read_b128 v[124:127], v4 offset:1024\n"
"ds_read_b128 v[128:131], v9 offset:64\n"
"v_mfma_f32_16x16x32_bf16 v[24:27], v[76:79], v[136:139], v[24:27]\n"
"buffer_load_dwordx4 v[132:135], v34, s[4:7], s98 offen offset:1024 sc1\n"
"v_mfma_f32_16x16x32_bf16 v[208:211], v[80:83], v[136:139], v[208:211]\n"
"ds_read_b128 v[136:139], v4 offset:2048\n"
"ds_read_b128 v[212:215], v9 offset:128\n"
"v_mfma_f32_16x16x32_bf16 v[108:111], v[148:151], v[140:143], v[108:111]\n"
"v_cvt_pk_bf16_f32 v254, v254, 0\n"
"v_lshlrev_b32_e32 v254, 16, v254\n"
"v_mfma_f32_16x16x32_bf16 v[112:115], v[152:155], v[140:143], v[112:115]\n"
"s_waitcnt lgkmcnt(4)\n"
"buffer_store_dwordx4 v[64:67], v0, s[4:7], s24 offen sc1\n"
"v_sqrt_f32_e32 v254, v254\n"
"v_mfma_f32_16x16x32_bf16 v[196:199], v[156:159], v[140:143], v[196:199]\n"
"v_cvt_pk_bf16_f32 v254, v254, 0\n"
"v_lshlrev_b32_e32 v254, 16, v254\n"
"v_mfma_f32_16x16x32_bf16 v[200:203], v[160:163], v[140:143], v[200:203]\n"
"v_rcp_f32_e32 v255, v254\n"
"v_mfma_f32_16x16x32_bf16 v[24:27], v[164:167], v[140:143], v[24:27]\n"
"buffer_load_dwordx4 v[64:67], v35, s[4:7], s98 offen sc1\n"
"v_mfma_f32_16x16x32_bf16 v[208:211], v[168:171], v[140:143], v[208:211]\n"
"s_waitcnt lgkmcnt(0)\n"
"v_fmac_f32_e32 v116, v253, v120\n"
"v_fmac_f32_e32 v117, v253, v121\n"
"v_mfma_f32_16x16x32_bf16 v[108:111], v[172:175], v[144:147], v[108:111]\n"
"v_fmac_f32_e32 v118, v253, v122\n"
"v_fmac_f32_e32 v119, v253, v123\n"
"v_mfma_f32_16x16x32_bf16 v[112:115], v[176:179], v[144:147], v[112:115]\n"
"v_fmac_f32_e32 v124, v253, v128\n"
"v_fmac_f32_e32 v125, v253, v129\n"
"v_mfma_f32_16x16x32_bf16 v[196:199], v[180:183], v[144:147], v[196:199]\n"
"v_fmac_f32_e32 v126, v253, v130\n"
"v_fmac_f32_e32 v127, v253, v131\n"
"v_mfma_f32_16x16x32_bf16 v[200:203], v[184:187], v[144:147], v[200:203]\n"
"v_fmac_f32_e32 v136, v253, v212\n"
"v_fmac_f32_e32 v137, v253, v213\n"
"v_mfma_f32_16x16x32_bf16 v[24:27], v[188:191], v[144:147], v[24:27]\n"
"buffer_load_dwordx4 v[120:123], v35, s[4:7], s98 offen offset:1024 sc1\n"
"ds_read_b64_tr_b16 v[128:129], v40 offset:4096\n"
"v_mfma_f32_16x16x32_bf16 v[208:211], v[192:195], v[144:147], v[208:211]\n"
"ds_read_b64_tr_b16 v[130:131], v40 offset:5120\n"
"v_fmac_f32_e32 v138, v253, v214\n"
"v_fmac_f32_e32 v139, v253, v215\n"
"; @A G1 0.2\n"
"v_mfma_f32_16x16x32_bf16 v[140:143], a[0:3], v[60:63], 0\n"
"ds_read_b128 v[144:147], v4 offset:3072\n"
"ds_read_b128 v[212:215], v9 offset:192\n"
"v_mfma_f32_16x16x32_bf16 v[216:219], a[4:7], v[60:63], 0\n"
"ds_read_b128 v[220:223], v4 offset:4096\n"
"ds_read_b128 v[224:227], v9 offset:256\n"
"v_mfma_f32_16x16x32_bf16 v[228:231], a[8:11], v[60:63], 0\n"
"ds_read_b128 v[232:235], v4 offset:5120\n"
"ds_read_b128 v[236:239], v9 offset:320\n"
"v_mfma_f32_16x16x32_bf16 v[240:243], a[12:15], v[60:63], 0\n"
"ds_write_b128 v3, v[108:111] offset:6144\n"
"v_mfma_f32_16x16x32_bf16 v[108:111], a[16:19], v[60:63], 0\n"
"s_add_u32 s98, s52, 0x48000\n"
"buffer_load_dwordx4 v[244:247], v32, s[4:7], s98 offen sc1\n"
"v_mfma_f32_16x16x32_bf16 v[248:251], a[20:23], v[60:63], 0\n"
"s_waitcnt lgkmcnt(7)\n"
"s_add_u32 s97, s24, 0x1000\n"
"buffer_store_dwordx4 v[128:131], v0, s[4:7], s97 offen sc1\n"
"v_mul_f32_e32 v254, 0x3e8293ee, v255\n"
"v_mfma_f32_16x16x32_bf16 v[140:143], a[24:27], v[84:87], v[140:143]\n"
"ds_write_b128 v3, v[112:115] offset:7168\n"
"v_mfma_f32_16x16x32_bf16 v[216:219], a[28:31], v[84:87], v[216:219]\n"
"s_waitcnt lgkmcnt(2)\n"
"v_fmac_f32_e32 v144, v253, v212\n"
"v_fmac_f32_e32 v145, v253, v213\n"
"v_mfma_f32_16x16x32_bf16 v[228:231], a[32:35], v[84:87], v[228:231]\n"
"v_fmac_f32_e32 v146, v253, v214\n"
"v_fmac_f32_e32 v147, v253, v215\n"
"v_mfma_f32_16x16x32_bf16 v[240:243], a[36:39], v[84:87], v[240:243]\n"
"ds_write_b128 v3, v[196:199] offset:8192\n"
"v_mfma_f32_16x16x32_bf16 v[108:111], a[40:43], v[84:87], v[108:111]\n"
"buffer_load_dwordx4 v[60:63], v32, s[4:7], s98 offen offset:1024 sc1\n"
"v_mfma_f32_16x16x32_bf16 v[248:251], a[44:47], v[84:87], v[248:251]\n"
"v_fmac_f32_e32 v220, v253, v224\n"
"v_fmac_f32_e32 v221, v253, v225\n"
"v_mfma_f32_16x16x32_bf16 v[140:143], a[48:51], v[88:91], v[140:143]\n"
"ds_write_b128 v3, v[200:203] offset:9216\n"
"v_mfma_f32_16x16x32_bf16 v[216:219], a[52:55], v[88:91], v[216:219]\n"
"v_fmac_f32_e32 v222, v253, v226\n"
"v_fmac_f32_e32 v223, v253, v227\n"
"v_mfma_f32_16x16x32_bf16 v[228:231], a[56:59], v[88:91], v[228:231]\n"
"v_fmac_f32_e32 v232, v253, v236\n"
"v_fmac_f32_e32 v233, v253, v237\n"
"v_mfma_f32_16x16x32_bf16 v[240:243], a[60:63], v[88:91], v[240:243]\n"
"ds_write_b128 v3, v[24:27] offset:10240\n"
"v_mfma_f32_16x16x32_bf16 v[108:111], a[64:67], v[88:91], v[108:111]\n"
"buffer_load_dwordx4 v[24:27], v33, s[4:7], s98 offen sc1\n"
"v_mfma_f32_16x16x32_bf16 v[248:251], a[68:71], v[88:91], v[248:251]\n"
"v_fmac_f32_e32 v234, v253, v238\n"
"v_fmac_f32_e32 v235, v253, v239\n"
"v_mfma_f32_16x16x32_bf16 v[140:143], a[72:75], v[92:95], v[140:143]\n"
"ds_write_b128 v3, v[208:211] offset:11264\n"
"v_mfma_f32_16x16x32_bf16 v[216:219], a[76:79], v[92:95], v[216:219]\n"
"v_mfma_f32_16x16x32_bf16 v[228:231], a[80:83], v[92:95], v[228:231]\n"
"v_mfma_f32_16x16x32_bf16 v[240:243], a[84:87], v[92:95], v[240:243]\n"
"v_mfma_f32_16x16x32_bf16 v[108:111], a[88:91], v[92:95], v[108:111]\n"
"buffer_load_dwordx4 v[84:87], v33, s[4:7], s98 offen offset:1024 sc1\n"
"ds_read_b64_tr_b16 v[88:89], v40 offset:8192\n"
"v_mfma_f32_16x16x32_bf16 v[248:251], a[92:95], v[92:95], v[248:251]\n"
"ds_read_b64_tr_b16 v[90:91], v40 offset:9216\n"
"v_mfma_f32_16x16x32_bf16 v[140:143], a[96:99], v[96:99], v[140:143]\n"
"v_mfma_f32_16x16x32_bf16 v[216:219], a[100:103], v[96:99], v[216:219]\n"
"v_mfma_f32_16x16x32_bf16 v[228:231], a[104:107], v[96:99], v[228:231]\n"
"v_mfma_f32_16x16x32_bf16 v[240:243], a[108:111], v[96:99], v[240:243]\n"
"v_mfma_f32_16x16x32_bf16 v[108:111], a[112:115], v[96:99], v[108:111]\n"
"buffer_load_dwordx4 v[92:95], v34, s[4:7], s98 offen sc1\n"
"s_waitcnt lgkmcnt(0)\n"
"buffer_store_dwordx4 v[88:91], v0, s[4:7], s25 offen sc1\n"
"v_mfma_f32_16x16x32_bf16 v[248:251], a[116:119], v[96:99], v[248:251]\n"
"v_mfma_f32_16x16x32_bf16 v[140:143], a[120:123], v[44:47], v[140:143]\n"
"v_mfma_f32_16x16x32_bf16 v[216:219], a[124:127], v[44:47], v[216:219]\n"
"v_mfma_f32_16x16x32_bf16 v[228:231], v[68:71], v[44:47], v[228:231]\n"
"v_mfma_f32_16x16x32_bf16 v[240:243], v[72:75], v[44:47], v[240:243]\n"
"v_mfma_f32_16x16x32_bf16 v[108:111], v[76:79], v[44:47], v[108:111]\n"
"buffer_load_dwordx4 v[88:91], v34, s[4:7], s98 offen offset:1024 sc1\n"
"v_mfma_f32_16x16x32_bf16 v[248:251], v[80:83], v[44:47], v[248:251]\n"
"s_waitcnt vmcnt(17)\n"
"v_mfma_f32_16x16x32_bf16 v[140:143], v[148:151], v[100:103], v[140:143]\n"
"v_mfma_f32_16x16x32_bf16 v[216:219], v[152:155], v[100:103], v[216:219]\n"
"v_mfma_f32_16x16x32_bf16 v[228:231], v[156:159], v[100:103], v[228:231]\n"
"v_mfma_f32_16x16x32_bf16 v[240:243], v[160:163], v[100:103], v[240:243]\n"
"v_mfma_f32_16x16x32_bf16 v[108:111], v[164:167], v[100:103], v[108:111]\n"
"buffer_load_dwordx4 v[44:47], v35, s[4:7], s98 offen sc1\n"
"v_mfma_f32_16x16x32_bf16 v[248:251], v[168:171], v[100:103], v[248:251]\n"
"v_mfma_f32_16x16x32_bf16 v[140:143], v[172:175], v[104:107], v[140:143]\n"
"v_mfma_f32_16x16x32_bf16 v[216:219], v[176:179], v[104:107], v[216:219]\n"
"v_mfma_f32_16x16x32_bf16 v[228:231], v[180:183], v[104:107], v[228:231]\n"
"v_mfma_f32_16x16x32_bf16 v[240:243], v[184:187], v[104:107], v[240:243]\n"
"v_mfma_f32_16x16x32_bf16 v[108:111], v[188:191], v[104:107], v[108:111]\n"
"buffer_load_dwordx4 v[96:99], v35, s[4:7], s98 offen offset:1024 sc1\n"
"ds_read_b64_tr_b16 v[100:101], v40 offset:12288\n"
"v_mfma_f32_16x16x32_bf16 v[248:251], v[192:195], v[104:107], v[248:251]\n"
"s_waitcnt vmcnt(17)\n"
"ds_read_b64_tr_b16 v[102:103], v40 offset:13312\n"
"; @A G1 0.3\n"
"v_mfma_f32_16x16x32_bf16 v[116:119], a[0:3], v[204:207], v[116:119]\n"
"v_mfma_f32_16x16x32_bf16 v[124:127], a[4:7], v[204:207], v[124:127]\n"
"v_mfma_f32_16x16x32_bf16 v[136:139], a[8:11], v[204:207], v[136:139]\n"
"v_mfma_f32_16x16x32_bf16 v[144:147], a[12:15], v[204:207], v[144:147]\n"
"ds_write_b128 v3, v[140:143] offset:12288\n"
"v_mfma_f32_16x16x32_bf16 v[220:223], a[16:19], v[204:207], v[220:223]\n"
"s_add_u32 s98, s53, 0x48000\n"
"buffer_load_dwordx4 v[104:107], v32, s[4:7], s98 offen sc1\n"
"v_mfma_f32_16x16x32_bf16 v[232:235], a[20:23], v[204:207], v[232:235]\n"
"s_waitcnt lgkmcnt(1)\n"
"s_add_u32 s97, s25, 0x1000\n"
"buffer_store_dwordx4 v[100:103], v0, s[4:7], s97 offen sc1\n"
"v_mfma_f32_16x16x32_bf16 v[116:119], a[24:27], v[28:31], v[116:119]\n"
"ds_write_b128 v3, v[216:219] offset:13312\n"
"v_mfma_f32_16x16x32_bf16 v[124:127], a[28:31], v[28:31], v[124:127]\n"
"v_mfma_f32_16x16x32_bf16 v[136:139], a[32:35], v[28:31], v[136:139]\n"
"v_mfma_f32_16x16x32_bf16 v[144:147], a[36:39], v[28:31], v[144:147]\n"
"ds_write_b128 v3, v[228:231] offset:14336\n"
"v_mfma_f32_16x16x32_bf16 v[220:223], a[40:43], v[28:31], v[220:223]\n"
"buffer_load_dwordx4 v[100:103], v32, s[4:7], s98 offen offset:1024 sc1\n"
"v_mfma_f32_16x16x32_bf16 v[232:235], a[44:47], v[28:31], v[232:235]\n"
"s_waitcnt vmcnt(18)\n"
"v_mfma_f32_16x16x32_bf16 v[116:119], a[48:51], v[48:51], v[116:119]\n"
"ds_write_b128 v3, v[240:243] offset:15360\n"
"v_mfma_f32_16x16x32_bf16 v[124:127], a[52:55], v[48:51], v[124:127]\n"
"v_mfma_f32_16x16x32_bf16 v[136:139], a[56:59], v[48:51], v[136:139]\n"
"v_mfma_f32_16x16x32_bf16 v[144:147], a[60:63], v[48:51], v[144:147]\n"
"ds_write_b128 v3, v[108:111] offset:16384\n"
"v_mfma_f32_16x16x32_bf16 v[220:223], a[64:67], v[48:51], v[220:223]\n"
"buffer_load_dwordx4 v[28:31], v33, s[4:7], s98 offen sc1\n"
"v_mfma_f32_16x16x32_bf16 v[232:235], a[68:71], v[48:51], v[232:235]\n"
"v_mfma_f32_16x16x32_bf16 v[116:119], a[72:75], v[52:55], v[116:119]\n"
"ds_write_b128 v3, v[248:251] offset:17408\n"
"v_mfma_f32_16x16x32_bf16 v[124:127], a[76:79], v[52:55], v[124:127]\n"
"v_mfma_f32_16x16x32_bf16 v[136:139], a[80:83], v[52:55], v[136:139]\n"
"v_mfma_f32_16x16x32_bf16 v[144:147], a[84:87], v[52:55], v[144:147]\n"
"s_waitcnt lgkmcnt(0)\n"
"s_barrier\n"
"v_mfma_f32_16x16x32_bf16 v[220:223], a[88:91], v[52:55], v[220:223]\n"
"buffer_load_dwordx4 v[48:51], v33, s[4:7], s98 offen offset:1024 sc1\n"
"v_mfma_f32_16x16x32_bf16 v[232:235], a[92:95], v[52:55], v[232:235]\n"
"s_waitcnt vmcnt(18)\n"
"ds_read_b128 v[52:55], v5 offset:2048\n"
"ds_read_b128 v[108:111], v6 offset:2048\n"
"v_mfma_f32_16x16x32_bf16 v[116:119], a[96:99], v[56:59], v[116:119]\n"
"ds_read_b128 v[112:115], v5 offset:3072\n"
"ds_read_b128 v[128:131], v6 offset:3072\n"
"v_mfma_f32_16x16x32_bf16 v[124:127], a[100:103], v[56:59], v[124:127]\n"
"v_mfma_f32_16x16x32_bf16 v[136:139], a[104:107], v[56:59], v[136:139]\n"
"v_mfma_f32_16x16x32_bf16 v[144:147], a[108:111], v[56:59], v[144:147]\n"
"ds_read_b128 v[140:143], v5 offset:4096\n"
"ds_read_b128 v[196:199], v6 offset:4096\n"
"v_mfma_f32_16x16x32_bf16 v[220:223], a[112:115], v[56:59], v[220:223]\n"
"buffer_load_dwordx4 v[200:203], v34, s[4:7], s98 offen sc1\n"
"v_mfma_f32_16x16x32_bf16 v[232:235], a[116:119], v[56:59], v[232:235]\n"
"ds_read_b128 v[56:59], v5 offset:5120\n"
"ds_read_b128 v[204:207], v6 offset:5120\n"
"v_mfma_f32_16x16x32_bf16 v[116:119], a[120:123], v[132:135], v[116:119]\n"
"s_waitcnt lgkmcnt(4)\n"
"v_add_f32_e32 v52, v52, v108\n"
"v_add_f32_e32 v53, v53, v109\n"
"v_mfma_f32_16x16x32_bf16 v[124:127], a[124:127], v[132:135], v[124:127]\n"
"v_add_f32_e32 v54, v54, v110\n"
"v_add_f32_e32 v55, v55, v111\n"
"v_mfma_f32_16x16x32_bf16 v[136:139], v[68:71], v[132:135], v[136:139]\n"
"ds_read_b128 v[108:111], v5\n"
"ds_read_b128 v[208:211], v6\n"
"v_mfma_f32_16x16x32_bf16 v[144:147], v[72:75], v[132:135], v[144:147]\n"
"ds_read_b128 v[212:215], v5 offset:1024\n"
"ds_read_b128 v[216:219], v6 offset:1024\n"
"v_mfma_f32_16x16x32_bf16 v[220:223], v[76:79], v[132:135], v[220:223]\n"
"buffer_load_dwordx4 v[224:227], v34, s[4:7], s98 offen offset:1024 sc1\n"
"v_mfma_f32_16x16x32_bf16 v[232:235], v[80:83], v[132:135], v[232:235]\n"
"s_waitcnt vmcnt(17)\n"
"v_add_f32_e32 v112, v112, v128\n"
"v_add_f32_e32 v113, v113, v129\n"
"v_mfma_f32_16x16x32_bf16 v[116:119], v[148:151], v[64:67], v[116:119]\n"
"s_waitcnt lgkmcnt(4)\n"
"v_add_f32_e32 v114, v114, v130\n"
"v_add_f32_e32 v115, v115, v131\n"
"v_mfma_f32_16x16x32_bf16 v[124:127], v[152:155], v[64:67], v[124:127]\n"
"v_add_f32_e32 v140, v140, v196\n"
"v_add_f32_e32 v141, v141, v197\n"
"v_mfma_f32_16x16x32_bf16 v[136:139], v[156:159], v[64:67], v[136:139]\n"
"s_waitcnt lgkmcnt(0)\n"
"v_add_f32_e32 v142, v142, v198\n"
"v_add_f32_e32 v143, v143, v199\n"
"v_mfma_f32_16x16x32_bf16 v[144:147], v[160:163], v[64:67], v[144:147]\n"
"v_add_f32_e32 v56, v56, v204\n"
"v_add_f32_e32 v57, v57, v205\n"
"v_mfma_f32_16x16x32_bf16 v[220:223], v[164:167], v[64:67], v[220:223]\n"
"buffer_load_dwordx4 v[128:131], v35, s[4:7], s98 offen sc1\n"
"v_mfma_f32_16x16x32_bf16 v[232:235], v[168:171], v[64:67], v[232:235]\n"
"v_add_f32_e32 v58, v58, v206\n"
"v_add_f32_e32 v59, v59, v207\n"
"v_mfma_f32_16x16x32_bf16 v[116:119], v[172:175], v[120:123], v[116:119]\n"
"v_add_f32_e32 v108, v108, v208\n"
"v_add_f32_e32 v109, v109, v209\n"
"v_mfma_f32_16x16x32_bf16 v[124:127], v[176:179], v[120:123], v[124:127]\n"
"v_add_f32_e32 v110, v110, v210\n"
"v_add_f32_e32 v111, v111, v211\n"
"v_mfma_f32_16x16x32_bf16 v[136:139], v[180:183], v[120:123], v[136:139]\n"
"v_add_f32_e32 v212, v212, v216\n"
"v_add_f32_e32 v213, v213, v217\n"
"v_mfma_f32_16x16x32_bf16 v[144:147], v[184:187], v[120:123], v[144:147]\n"
"v_add_f32_e32 v214, v214, v218\n"
"v_add_f32_e32 v215, v215, v219\n"
"v_mfma_f32_16x16x32_bf16 v[220:223], v[188:191], v[120:123], v[220:223]\n"
"buffer_load_dwordx4 v[64:67], v35, s[4:7], s98 offen offset:1024 sc1\n"
"v_mfma_f32_16x16x32_bf16 v[232:235], v[192:195], v[120:123], v[232:235]\n"
".Lmk_z1:\n"
"s_waitcnt vmcnt(16)\n"
"; @A G1 1.0\n"
"v_mfma_f32_16x16x32_bf16 v[120:123], a[0:3], v[244:247], 0\n"
"v_mfma_f32_16x16x32_bf16 v[132:135], a[4:7], v[244:247], 0\n"
"s_add_u32 s98, s62, 0x4800\n"
"v_mfma_f32_16x16x32_bf16 v[196:199], a[8:11], v[244:247], 0\n"
"v_mfma_f32_16x16x32_bf16 v[204:207], a[12:15], v[244:247], 0\n"
"v_add_f32_e32 v136, v136, v52\n"
"v_add_f32_e32 v137, v137, v53\n"
"v_mfma_f32_16x16x32_bf16 v[208:211], a[16:19], v[244:247], 0\n"
"s_add_u32 s97, s54, 0x48000\n"
"buffer_load_dwordx4 v[216:219], v32, s[4:7], s97 offen sc1\n"
"v_mfma_f32_16x16x32_bf16 v[228:231], a[20:23], v[244:247], 0\n"
"buffer_load_dword v253, v10, s[4:7], s98 offen sc1\n"
"v_mfma_f32_16x16x32_bf16 v[120:123], a[24:27], v[60:63], v[120:123]\n"
"v_add_f32_e32 v138, v138, v54\n"
"v_add_f32_e32 v139, v139, v55\n"
"v_mfma_f32_16x16x32_bf16 v[132:135], a[28:31], v[60:63], v[132:135]\n"
"v_add_f32_e32 v144, v144, v112\n"
"v_add_f32_e32 v145, v145, v113\n"
"v_mfma_f32_16x16x32_bf16 v[196:199], a[32:35], v[60:63], v[196:199]\n"
"buffer_load_dword v252, v10, s[4:7], s98 offen offset:256 sc1\n"
"v_mfma_f32_16x16x32_bf16 v[204:207], a[36:39], v[60:63], v[204:207]\n"
"v_add_f32_e32 v146, v146, v114\n"
"v_add_f32_e32 v147, v147, v115\n"
"v_mfma_f32_16x16x32_bf16 v[208:211], a[40:43], v[60:63], v[208:211]\n"
"buffer_load_dwordx4 v[52:55], v32, s[4:7], s97 offen offset:1024 sc1\n"
"v_mfma_f32_16x16x32_bf16 v[228:231], a[44:47], v[60:63], v[228:231]\n"
"s_waitcnt vmcnt(18)\n"
"s_waitcnt vmcnt(11)\n"
"v_mul_f32_e32 v136, v136, v255\n"
"v_mfma_f32_16x16x32_bf16 v[120:123], a[48:51], v[24:27], v[120:123]\n"
"buffer_store_dwordx2 v[18:19], v11, s[4:7], s68 offen offset:96 sc1\n"
"v_mfma_f32_16x16x32_bf16 v[132:135], a[52:55], v[24:27], v[132:135]\n"
"buffer_load_dword v251, v10, s[4:7], s98 offen offset:512 sc1\n"
"v_mfma_f32_16x16x32_bf16 v[196:199], a[56:59], v[24:27], v[196:199]\n"
"v_mul_f32_e32 v137, v137, v255\n"
"v_mul_f32_e32 v138, v138, v255\n"
"v_mfma_f32_16x16x32_bf16 v[204:207], a[60:63], v[24:27], v[204:207]\n"
"v_mul_f32_e32 v139, v139, v255\n"
"v_mul_f32_e32 v144, v144, v255\n"
"v_mfma_f32_16x16x32_bf16 v[208:211], a[64:67], v[24:27], v[208:211]\n"
"buffer_load_dwordx4 v[60:63], v33, s[4:7], s97 offen sc1\n"
"v_mfma_f32_16x16x32_bf16 v[228:231], a[68:71], v[24:27], v[228:231]\n"
"buffer_load_dword v250, v10, s[4:7], s98 offen offset:768 sc1\n"
"v_mfma_f32_16x16x32_bf16 v[120:123], a[72:75], v[84:87], v[120:123]\n"
"v_mul_f32_e32 v145, v145, v255\n"
"v_mul_f32_e32 v146, v146, v255\n"
"v_mfma_f32_16x16x32_bf16 v[132:135], a[76:79], v[84:87], v[132:135]\n"
"v_mul_f32_e32 v147, v147, v255\n"
"v_cvt_pk_bf16_f32 v24, v136, v137\n"
"v_mfma_f32_16x16x32_bf16 v[196:199], a[80:83], v[84:87], v[196:199]\n"
"buffer_load_dword v249, v10, s[4:7], s98 offen offset:1024 sc1\n"
"v_mfma_f32_16x16x32_bf16 v[204:207], a[84:87], v[84:87], v[204:207]\n"
"v_cvt_pk_bf16_f32 v25, v138, v139\n"
"v_cvt_pk_bf16_f32 v26, v144, v145\n"
"v_mfma_f32_16x16x32_bf16 v[208:211], a[88:91], v[84:87], v[208:211]\n"
"buffer_load_dwordx4 v[112:115], v33, s[4:7], s97 offen offset:1024 sc1\n"
"v_mfma_f32_16x16x32_bf16 v[228:231], a[92:95], v[84:87], v[228:231]\n"
"buffer_load_dword v248, v10, s[4:7], s98 offen offset:1280 sc1\n"
"v_mfma_f32_16x16x32_bf16 v[120:123], a[96:99], v[92:95], v[120:123]\n"
"v_cvt_pk_bf16_f32 v27, v146, v147\n"
"v_mfma_f32_16x16x32_bf16 v[132:135], a[100:103], v[92:95], v[132:135]\n"
"ds_write_b128 v7, v[24:27] offset:24576\n"
"v_mfma_f32_16x16x32_bf16 v[196:199], a[104:107], v[92:95], v[196:199]\n"
"buffer_load_dword v247, v10, s[4:7], s98 offen offset:1536 sc1\n"
"v_mfma_f32_16x16x32_bf16 v[204:207], a[108:111], v[92:95], v[204:207]\n"
"v_add_f32_e32 v220, v220, v140\n"
"v_add_f32_e32 v221, v221, v141\n"
"v_mfma_f32_16x16x32_bf16 v[208:211], a[112:115], v[92:95], v[208:211]\n"
"buffer_load_dwordx4 v[84:87], v34, s[4:7], s97 offen sc1\n"
"v_mfma_f32_16x16x32_bf16 v[228:231], a[116:119], v[92:95], v[228:231]\n"
"buffer_load_dword v246, v10, s[4:7], s98 offen offset:1792 sc1\n"
"v_mfma_f32_16x16x32_bf16 v[120:123], a[120:123], v[88:91], v[120:123]\n"
"v_add_f32_e32 v222, v222, v142\n"
"v_add_f32_e32 v223, v223, v143\n"
"v_mfma_f32_16x16x32_bf16 v[132:135], a[124:127], v[88:91], v[132:135]\n"
"v_add_f32_e32 v232, v232, v56\n"
"v_add_f32_e32 v233, v233, v57\n"
"v_mfma_f32_16x16x32_bf16 v[196:199], v[68:71], v[88:91], v[196:199]\n"
"v_add_f32_e32 v234, v234, v58\n"
"v_add_f32_e32 v235, v235, v59\n"
"v_mfma_f32_16x16x32_bf16 v[204:207], v[72:75], v[88:91], v[204:207]\n"
"v_mul_f32_e32 v220, v220, v255\n"
"v_mul_f32_e32 v221, v221, v255\n"
"v_mfma_f32_16x16x32_bf16 v[208:211], v[76:79], v[88:91], v[208:211]\n"
"buffer_load_dwordx4 v[56:59], v34, s[4:7], s97 offen offset:1024 sc1\n"
"v_mfma_f32_16x16x32_bf16 v[228:231], v[80:83], v[88:91], v[228:231]\n"
"v_mul_f32_e32 v222, v222, v255\n"
"v_mul_f32_e32 v223, v223, v255\n"
"v_mfma_f32_16x16x32_bf16 v[120:123], v[148:151], v[44:47], v[120:123]\n"
"v_mul_f32_e32 v232, v232, v255\n"
"v_mul_f32_e32 v233, v233, v255\n"
"v_mfma_f32_16x16x32_bf16 v[132:135], v[152:155], v[44:47], v[132:135]\n"
"v_mul_f32_e32 v234, v234, v255\n"
"v_mul_f32_e32 v235, v235, v255\n"
"v_mfma_f32_16x16x32_bf16 v[196:199], v[156:159], v[44:47], v[196:199]\n"
"v_cvt_pk_bf16_f32 v88, v220, v221\n"
"v_cvt_pk_bf16_f32 v89, v222, v223\n"
"v_mfma_f32_16x16x32_bf16 v[204:207], v[160:163], v[44:47], v[204:207]\n"
"v_cvt_pk_bf16_f32 v90, v232, v233\n"
"v_cvt_pk_bf16_f32 v91, v234, v235\n"
"v_mfma_f32_16x16x32_bf16 v[208:211], v[164:167], v[44:47], v[208:211]\n"
"buffer_load_dwordx4 v[92:95], v35, s[4:7], s97 offen sc1\n"
"v_mfma_f32_16x16x32_bf16 v[228:231], v[168:171], v[44:47], v[228:231]\n"
"ds_write_b128 v7, v[88:91] offset:57344\n"
"v_mfma_f32_16x16x32_bf16 v[120:123], v[172:175], v[96:99], v[120:123]\n"
"v_add_f32_e32 v116, v116, v108\n"
"v_add_f32_e32 v117, v117, v109\n"
"v_mfma_f32_16x16x32_bf16 v[132:135], v[176:179], v[96:99], v[132:135]\n"
"v_add_f32_e32 v118, v118, v110\n"
"v_add_f32_e32 v119, v119, v111\n"
"v_mfma_f32_16x16x32_bf16 v[196:199], v[180:183], v[96:99], v[196:199]\n"
"v_add_f32_e32 v124, v124, v212\n"
"v_add_f32_e32 v125, v125, v213\n"
"v_mfma_f32_16x16x32_bf16 v[204:207], v[184:187], v[96:99], v[204:207]\n"
"v_add_f32_e32 v126, v126, v214\n"
"v_add_f32_e32 v127, v127, v215\n"
"v_mfma_f32_16x16x32_bf16 v[208:211], v[188:191], v[96:99], v[208:211]\n"
"buffer_load_dwordx4 v[44:47], v35, s[4:7], s97 offen offset:1024 sc1\n"
"v_mfma_f32_16x16x32_bf16 v[228:231], v[192:195], v[96:99], v[228:231]\n"
"s_waitcnt vmcnt(23)\n"
"v_mul_f32_e32 v116, v116, v254\n"
"v_mul_f32_e32 v117, v117, v254\n"
"; @A G1 1.1\n"
"v_mfma_f32_16x16x32_bf16 v[96:99], a[0:3], v[104:107], 0\n"
"v_mul_f32_e32 v118, v118, v254\n"
"v_mul_f32_e32 v119, v119, v254\n"
"v_mfma_f32_16x16x32_bf16 v[108:111], a[4:7], v[104:107], 0\n"
"v_mul_f32_e32 v124, v124, v254\n"
"v_mul_f32_e32 v125, v125, v254\n"
"v_mfma_f32_16x16x32_bf16 v[136:139], a[8:11], v[104:107], 0\n"
"v_mul_f32_e32 v126, v126, v254\n"
"v_mul_f32_e32 v127, v127, v254\n"
"v_mfma_f32_16x16x32_bf16 v[140:143], a[12:15], v[104:107], 0\n"
"ds_write_b128 v3, v[120:123]\n"
"v_mfma_f32_16x16x32_bf16 v[120:123], a[16:19], v[104:107], 0\n"
"s_add_u32 s98, s55, 0x48000\n"
"buffer_load_dwordx4 v[144:147], v32, s[4:7], s98 offen sc1\n"
"v_mfma_f32_16x16x32_bf16 v[212:215], a[20:23], v[104:107], 0\n"
"v_cvt_pk_bf16_f32 v104, v116, v117\n"
"v_cvt_pk_bf16_f32 v105, v118, v119\n"
"v_mfma_f32_16x16x32_bf16 v[96:99], a[24:27], v[100:103], v[96:99]\n"
"ds_write_b128 v3, v[132:135] offset:1024\n"
"v_mfma_f32_16x16x32_bf16 v[108:111], a[28:31], v[100:103], v[108:111]\n"
"v_cvt_pk_bf16_f32 v106, v124, v125\n"
"v_cvt_pk_bf16_f32 v107, v126, v127\n"
"v_mfma_f32_16x16x32_bf16 v[136:139], a[32:35], v[100:103], v[136:139]\n"
"ds_write_b128 v2, v[104:107] offset:49152\n"
"v_mfma_f32_16x16x32_bf16 v[140:143], a[36:39], v[100:103], v[140:143]\n"
"ds_write_b128 v3, v[196:199] offset:2048\n"
"v_mfma_f32_16x16x32_bf16 v[120:123], a[40:43], v[100:103], v[120:123]\n"
"buffer_load_dwordx4 v[104:107], v32, s[4:7], s98 offen offset:1024 sc1\n"
"v_mfma_f32_16x16x32_bf16 v[212:215], a[44:47], v[100:103], v[212:215]\n"
"s_waitcnt vmcnt(23)\n"
"v_mov_b32_e32 v100, -1\n"
"v_mov_b32_e32 v101, -1\n"
"v_mfma_f32_16x16x32_bf16 v[96:99], a[48:51], v[28:31], v[96:99]\n"
"ds_write_b128 v3, v[204:207] offset:3072\n"
"v_mfma_f32_16x16x32_bf16 v[108:111], a[52:55], v[28:31], v[108:111]\n"
"s_add_u32 s97, s67, 0\n"
"buffer_load_dwordx2 v[254:255], v12, s[4:7], s97 offen sc1\n"
"v_mfma_f32_16x16x32_bf16 v[136:139], a[56:59], v[28:31], v[136:139]\n"
"s_add_u32 s96, s64, 0x2000\n"
"buffer_store_dwordx4 v[24:27], v7, s[4:7], s96 offen sc1\n"
"v_mfma_f32_16x16x32_bf16 v[140:143], a[60:63], v[28:31], v[140:143]\n"
"ds_write_b128 v3, v[208:211] offset:4096\n"
"v_mfma_f32_16x16x32_bf16 v[120:123], a[64:67], v[28:31], v[120:123]\n"
"buffer_load_dwordx4 v[24:27], v33, s[4:7], s98 offen sc1\n"
"v_mfma_f32_16x16x32_bf16 v[212:215], a[68:71], v[28:31], v[212:215]\n"
"v_mov_b32_e32 v102, -1\n"
"v_mov_b32_e32 v103, -1\n"
"v_mfma_f32_16x16x32_bf16 v[96:99], a[72:75], v[48:51], v[96:99]\n"
"ds_write_b128 v3, v[228:231] offset:5120\n"
"s_add_u32 s96, s64, 0x6000\n"
"v_mfma_f32_16x16x32_bf16 v[108:111], a[76:79], v[48:51], v[108:111]\n"
"ds_read_b64_tr_b16 v[28:29], v36\n"
"ds_read_b64_tr_b16 v[30:31], v36 offset:2048\n"
"s_mov_b32 s94, 0x30003\n"
"v_mfma_f32_16x16x32_bf16 v[136:139], a[80:83], v[48:51], v[136:139]\n"
"buffer_store_dwordx4 v[88:91], v7, s[4:7], s96 offen sc1\n"
"v_mfma_f32_16x16x32_bf16 v[140:143], a[84:87], v[48:51], v[140:143]\n"
"s_mov_b32 s95, 0x30003\n"
"v_mov_b32_e32 v91, 0x3f803f80\n"
"v_cndmask_b32_e64 v88, 0, v91, s[94:95]\n"
"v_cndmask_b32_e64 v89, 0, v91, s[94:95]\n"
"v_mfma_f32_16x16x32_bf16 v[120:123], a[88:91], v[48:51], v[120:123]\n"
"buffer_load_dwordx4 v[116:119], v33, s[4:7], s98 offen offset:1024 sc1\n"
"v_cndmask_b32_e64 v90, 0, v91, s[94:95]\n"
"v_mfma_f32_16x16x32_bf16 v[212:215], a[92:95], v[48:51], v[212:215]\n"
"s_waitcnt vmcnt(26)\n"
"v_cndmask_b32_e64 v91, 0, v91, s[94:95]\n"
"v_mfma_f32_16x16x32_bf16 v[96:99], a[96:99], v[200:203], v[96:99]\n"
"s_waitcnt lgkmcnt(0)\n"
"v_mfma_f32_16x16x32_bf16 v[48:51], v[28:31], v[88:91], 0\n"
"buffer_store_dwordx4 v[28:31], v0, s[4:7], s20 offen sc1\n"
"v_mfma_f32_16x16x32_bf16 v[108:111], a[100:103], v[200:203], v[108:111]\n"
"ds_read_b64_tr_b16 v[28:29], v36 offset:4096\n"
"ds_read_b64_tr_b16 v[30:31], v36 offset:6144\n"
"s_mov_b32 s94, 0x30003\n"
"v_mfma_f32_16x16x32_bf16 v[136:139], a[104:107], v[200:203], v[136:139]\n"
"s_mov_b32 s95, 0x30003\n"
"v_mov_b32_e32 v91, 0x3f803f80\n"
"v_cndmask_b32_e64 v88, 0, v91, s[94:95]\n"
"v_cndmask_b32_e64 v89, 0, v91, s[94:95]\n"
"v_mfma_f32_16x16x32_bf16 v[140:143], a[108:111], v[200:203], v[140:143]\n"
"v_cndmask_b32_e64 v90, 0, v91, s[94:95]\n"
"v_cndmask_b32_e64 v91, 0, v91, s[94:95]\n"
"v_mfma_f32_16x16x32_bf16 v[120:123], a[112:115], v[200:203], v[120:123]\n"
"buffer_load_dwordx4 v[124:127], v34, s[4:7], s98 offen sc1\n"
"s_waitcnt lgkmcnt(0)\n"
"v_mfma_f32_16x16x32_bf16 v[48:51], v[28:31], v[88:91], v[48:51]\n"
"s_add_u32 s96, s20, 0x1000\n"
"v_mfma_f32_16x16x32_bf16 v[212:215], a[116:119], v[200:203], v[212:215]\n"
"s_waitcnt lgkmcnt(0)\n"
"s_barrier\n"
"buffer_store_dwordx4 v[28:31], v0, s[4:7], s96 offen offset:2048 sc1\n"
"v_mfma_f32_16x16x32_bf16 v[96:99], a[120:123], v[224:227], v[96:99]\n"
"s_waitcnt vmcnt(13)\n"
"v_add_f32_e32 v253, v253, v249\n"
"v_add_f32_e32 v252, v252, v248\n"
"v_mfma_f32_16x16x32_bf16 v[108:111], a[124:127], v[224:227], v[108:111]\n"
"v_add_f32_e32 v251, v251, v247\n"
"v_add_f32_e32 v250, v250, v246\n"
"v_mfma_f32_16x16x32_bf16 v[136:139], v[68:71], v[224:227], v[136:139]\n"
"v_add_f32_e32 v253, v253, v251\n"
"v_add_f32_e32 v252, v252, v250\n"
"v_mfma_f32_16x16x32_bf16 v[140:143], v[72:75], v[224:227], v[140:143]\n"
"v_add_f32_e32 v253, v253, v252\n"
"v_mov_b32_e32 v252, v253\n"
"v_mfma_f32_16x16x32_bf16 v[120:123], v[76:79], v[224:227], v[120:123]\n"
"buffer_load_dwordx4 v[28:31], v34, s[4:7], s98 offen offset:1024 sc1\n"
"v_mfma_f32_16x16x32_bf16 v[212:215], v[80:83], v[224:227], v[212:215]\n"
"v_permlane32_swap_b32_e32 v253, v252\n"
"v_add_f32_e32 v253, v253, v252\n"
"v_mfma_f32_16x16x32_bf16 v[96:99], v[148:151], v[128:131], v[96:99]\n"
"ds_read_b64_tr_b16 v[88:89], v37\n"
"v_mul_f32_e32 v253, 0x3a800000, v253\n"
"v_mov_b32_e32 v252, v253\n"
"v_mfma_f32_16x16x32_bf16 v[108:111], v[152:155], v[128:131], v[108:111]\n"
"ds_read_b64_tr_b16 v[90:91], v37 offset:2048\n"
"v_permlane16_swap_b32_e32 v253, v252\n"
"v_cvt_pk_bf16_f32 v251, -v253, 0\n"
"v_mfma_f32_16x16x32_bf16 v[136:139], v[156:159], v[128:131], v[136:139]\n"
"s_mov_b32 s94, 0xc000c\n"
"v_fma_f32 v252, -v253, v253, v252\n"
"v_lshlrev_b32_e32 v251, 16, v251\n"
"s_mov_b32 s95, 0xc000c\n"
"v_mfma_f32_16x16x32_bf16 v[140:143], v[160:163], v[128:131], v[140:143]\n"
"ds_read_b128 v[132:135], v4\n"
"ds_read_b128 v[196:199], v9\n"
"v_mov_b32_e32 v203, 0x3f803f80\n"
"v_mfma_f32_16x16x32_bf16 v[120:123], v[164:167], v[128:131], v[120:123]\n"
"buffer_load_dwordx4 v[204:207], v35, s[4:7], s98 offen sc1\n"
"v_cndmask_b32_e64 v200, 0, v203, s[94:95]\n"
"v_mfma_f32_16x16x32_bf16 v[212:215], v[168:171], v[128:131], v[212:215]\n"
"ds_read_b128 v[128:131], v4 offset:1024\n"
"ds_read_b128 v[208:211], v9 offset:64\n"
"v_cndmask_b32_e64 v201, 0, v203, s[94:95]\n"
"v_mfma_f32_16x16x32_bf16 v[96:99], v[172:175], v[64:67], v[96:99]\n"
"ds_read_b128 v[220:223], v4 offset:2048\n"
"ds_read_b128 v[224:227], v9 offset:128\n"
"v_cndmask_b32_e64 v202, 0, v203, s[94:95]\n"
"v_mfma_f32_16x16x32_bf16 v[108:111], v[176:179], v[64:67], v[108:111]\n"
"v_max_f32_e32 v252, 0, v252\n"
"v_cvt_pk_bf16_f32 v252, v252, 0\n"
"v_cndmask_b32_e64 v203, 0, v203, s[94:95]\n"
"v_mfma_f32_16x16x32_bf16 v[136:139], v[180:183], v[64:67], v[136:139]\n"
"s_waitcnt lgkmcnt(6)\n"
"v_lshlrev_b32_e32 v252, 16, v252\n"
"v_add_f32_e32 v252, 0x3727c5ac, v252\n"
"v_mfma_f32_16x16x32_bf16 v[48:51], v[88:91], v[200:203], v[48:51]\n"
"buffer_store_dwordx4 v[88:91], v0, s[4:7], s21 offen sc1\n"
"v_mfma_f32_16x16x32_bf16 v[140:143], v[184:187], v[64:67], v[140:143]\n"
"v_cvt_pk_bf16_f32 v252, v252, 0\n"
"v_lshlrev_b32_e32 v252, 16, v252\n"
"v_mfma_f32_16x16x32_bf16 v[120:123], v[188:191], v[64:67], v[120:123]\n"
"buffer_load_dwordx4 v[88:91], v35, s[4:7], s98 offen offset:1024 sc1\n"
"v_mfma_f32_16x16x32_bf16 v[212:215], v[192:195], v[64:67], v[212:215]\n"
"s_waitcnt lgkmcnt(0)\n"
"v_fmac_f32_e32 v132, v251, v196\n"
"; @A G1 1.2\n"
"v_mfma_f32_16x16x32_bf16 v[64:67], a[0:3], v[216:219], 0\n"
"v_fmac_f32_e32 v133, v251, v197\n"
"v_fmac_f32_e32 v134, v251, v198\n"
"v_mfma_f32_16x16x32_bf16 v[200:203], a[4:7], v[216:219], 0\n"
"s_waitcnt vmcnt(11)\n"
"v_cmp_ne_u64_e32 vcc, v[254:255], v[18:19]\n"
"v_mfma_f32_16x16x32_bf16 v[228:231], a[8:11], v[216:219], 0\n"
"s_cbranch_vccnz .Lmk_f2s\n"
".Lmk_f2r:\n"
"v_fmac_f32_e32 v135, v251, v199\n"
"v_mfma_f32_16x16x32_bf16 v[196:199], a[12:15], v[216:219], 0\n"
"ds_write_b128 v3, v[96:99] offset:6144\n"
"v_mfma_f32_16x16x32_bf16 v[96:99], a[16:19], v[216:219], 0\n"
"s_add_u32 s98, s52, 0x8000\n"
"buffer_load_dwordx4 v[232:235], v32, s[4:7], s98 offen sc1\n"
"v_mfma_f32_16x16x32_bf16 v[236:239], a[20:23], v[216:219], 0\n"
"v_fmac_f32_e32 v128, v251, v208\n"
"v_fmac_f32_e32 v129, v251, v209\n"
"v_mfma_f32_16x16x32_bf16 v[64:67], a[24:27], v[52:55], v[64:67]\n"
"ds_write_b128 v3, v[108:111] offset:7168\n"
"v_mfma_f32_16x16x32_bf16 v[200:203], a[28:31], v[52:55], v[200:203]\n"
"v_fmac_f32_e32 v130, v251, v210\n"
"v_fmac_f32_e32 v131, v251, v211\n"
"v_mfma_f32_16x16x32_bf16 v[228:231], a[32:35], v[52:55], v[228:231]\n"
"v_fmac_f32_e32 v220, v251, v224\n"
"v_fmac_f32_e32 v221, v251, v225\n"
"v_mfma_f32_16x16x32_bf16 v[196:199], a[36:39], v[52:55], v[196:199]\n"
"ds_write_b128 v3, v[136:139] offset:8192\n"
"v_mfma_f32_16x16x32_bf16 v[96:99], a[40:43], v[52:55], v[96:99]\n"
"buffer_load_dwordx4 v[108:111], v32, s[4:7], s98 offen offset:1024 sc1\n"
"ds_read_b64_tr_b16 v[136:137], v37 offset:4096\n"
"v_mfma_f32_16x16x32_bf16 v[236:239], a[44:47], v[52:55], v[236:239]\n"
"ds_read_b64_tr_b16 v[138:139], v37 offset:6144\n"
"v_fmac_f32_e32 v222, v251, v226\n"
"v_fmac_f32_e32 v223, v251, v227\n"
"v_mfma_f32_16x16x32_bf16 v[64:67], a[48:51], v[60:63], v[64:67]\n"
"ds_write_b128 v3, v[140:143] offset:9216\n"
"v_mfma_f32_16x16x32_bf16 v[200:203], a[52:55], v[60:63], v[200:203]\n"
"ds_read_b128 v[52:55], v4 offset:3072\n"
"ds_read_b128 v[140:143], v9 offset:192\n"
"s_mov_b32 s96, 0xc000c\n"
"v_mfma_f32_16x16x32_bf16 v[228:231], a[56:59], v[60:63], v[228:231]\n"
"ds_read_b128 v[208:211], v4 offset:4096\n"
"ds_read_b128 v[216:219], v9 offset:256\n"
"s_mov_b32 s97, 0xc000c\n"
"v_mfma_f32_16x16x32_bf16 v[196:199], a[60:63], v[60:63], v[196:199]\n"
"ds_write_b128 v3, v[120:123] offset:10240\n"
"v_mfma_f32_16x16x32_bf16 v[96:99], a[64:67], v[60:63], v[96:99]\n"
"buffer_load_dwordx4 v[120:123], v33, s[4:7], s98 offen sc1\n"
"v_mov_b32_e32 v227, 0x3f803f80\n"
"v_mfma_f32_16x16x32_bf16 v[236:239], a[68:71], v[60:63], v[236:239]\n"
"ds_read_b128 v[60:63], v4 offset:5120\n"
"ds_read_b128 v[240:243], v9 offset:320\n"
"v_cndmask_b32_e64 v224, 0, v227, s[96:97]\n"
"v_mfma_f32_16x16x32_bf16 v[64:67], a[72:75], v[112:115], v[64:67]\n"
"ds_write_b128 v3, v[212:215] offset:11264\n"
"v_mfma_f32_16x16x32_bf16 v[200:203], a[76:79], v[112:115], v[200:203]\n"
"v_sqrt_f32_e32 v252, v252\n"
"v_cndmask_b32_e64 v225, 0, v227, s[96:97]\n"
"v_mfma_f32_16x16x32_bf16 v[228:231], a[80:83], v[112:115], v[228:231]\n"
"v_cvt_pk_bf16_f32 v252, v252, 0\n"
"v_lshlrev_b32_e32 v252, 16, v252\n"
"v_cndmask_b32_e64 v226, 0, v227, s[96:97]\n"
"v_mfma_f32_16x16x32_bf16 v[196:199], a[84:87], v[112:115], v[196:199]\n"
"v_rcp_f32_e32 v255, v252\n"
"v_cndmask_b32_e64 v227, 0, v227, s[96:97]\n"
"v_mfma_f32_16x16x32_bf16 v[96:99], a[88:91], v[112:115], v[96:99]\n"
"buffer_load_dwordx4 v[212:215], v33, s[4:7], s98 offen offset:1024 sc1\n"
"s_waitcnt lgkmcnt(9)\n"
"v_mfma_f32_16x16x32_bf16 v[48:51], v[136:139], v[224:227], v[48:51]\n"
"s_add_u32 s97, s21, 0x1000\n"
"v_mfma_f32_16x16x32_bf16 v[236:239], a[92:95], v[112:115], v[236:239]\n"
"s_waitcnt lgkmcnt(1)\n"
"buffer_store_dwordx4 v[136:139], v0, s[4:7], s97 offen offset:2048 sc1\n"
"v_fmac_f32_e32 v52, v251, v140\n"
"v_mfma_f32_16x16x32_bf16 v[64:67], a[96:99], v[84:87], v[64:67]\n"
"v_fmac_f32_e32 v53, v251, v141\n"
"v_fmac_f32_e32 v54, v251, v142\n"
"v_mfma_f32_16x16x32_bf16 v[200:203], a[100:103], v[84:87], v[200:203]\n"
"v_fmac_f32_e32 v55, v251, v143\n"
"v_fmac_f32_e32 v208, v251, v216\n"
"v_mfma_f32_16x16x32_bf16 v[228:231], a[104:107], v[84:87], v[228:231]\n"
"v_fmac_f32_e32 v209, v251, v217\n"
"v_fmac_f32_e32 v210, v251, v218\n"
"v_mfma_f32_16x16x32_bf16 v[196:199], a[108:111], v[84:87], v[196:199]\n"
"v_fmac_f32_e32 v211, v251, v219\n"
"v_fmac_f32_e32 v60, v251, v240\n"
"v_mfma_f32_16x16x32_bf16 v[96:99], a[112:115], v[84:87], v[96:99]\n"
"buffer_load_dwordx4 v[112:115], v34, s[4:7], s98 offen sc1\n"
"v_mfma_f32_16x16x32_bf16 v[236:239], a[116:119], v[84:87], v[236:239]\n"
"v_fmac_f32_e32 v61, v251, v241\n"
"v_fmac_f32_e32 v62, v251, v242\n"
"v_mfma_f32_16x16x32_bf16 v[64:67], a[120:123], v[56:59], v[64:67]\n"
"v_fmac_f32_e32 v63, v251, v243\n"
"v_mul_f32_e32 v254, 0x3e8293ee, v255\n"
"v_mfma_f32_16x16x32_bf16 v[200:203], a[124:127], v[56:59], v[200:203]\n"
"v_mfma_f32_16x16x32_bf16 v[228:231], v[68:71], v[56:59], v[228:231]\n"
"ds_read_b64_tr_b16 v[84:85], v38\n"
"ds_read_b64_tr_b16 v[86:87], v38 offset:2048\n"
"s_mov_b32 s96, 0x300030\n"
"v_mfma_f32_16x16x32_bf16 v[196:199], v[72:75], v[56:59], v[196:199]\n"
"s_mov_b32 s97, 0x300030\n"
"v_mov_b32_e32 v139, 0x3f803f80\n"
"v_cndmask_b32_e64 v136, 0, v139, s[96:97]\n"
"v_cndmask_b32_e64 v137, 0, v139, s[96:97]\n"
"v_mfma_f32_16x16x32_bf16 v[96:99], v[76:79], v[56:59], v[96:99]\n"
"buffer_load_dwordx4 v[140:143], v34, s[4:7], s98 offen offset:1024 sc1\n"
"v_cndmask_b32_e64 v138, 0, v139, s[96:97]\n"
"v_mfma_f32_16x16x32_bf16 v[236:239], v[80:83], v[56:59], v[236:239]\n"
"v_cndmask_b32_e64 v139, 0, v139, s[96:97]\n"
"v_mfma_f32_16x16x32_bf16 v[64:67], v[148:151], v[92:95], v[64:67]\n"
"s_waitcnt lgkmcnt(0)\n"
"v_mfma_f32_16x16x32_bf16 v[48:51], v[84:87], v[136:139], v[48:51]\n"
"buffer_store_dwordx4 v[84:87], v0, s[4:7], s22 offen sc1\n"
"v_mfma_f32_16x16x32_bf16 v[200:203], v[152:155], v[92:95], v[200:203]\n"
"v_mfma_f32_16x16x32_bf16 v[228:231], v[156:159], v[92:95], v[228:231]\n"
"v_mfma_f32_16x16x32_bf16 v[196:199], v[160:163], v[92:95], v[196:199]\n"
"v_mfma_f32_16x16x32_bf16 v[96:99], v[164:167], v[92:95], v[96:99]\n"
"buffer_load_dwordx4 v[56:59], v35, s[4:7], s98 offen sc1\n"
"v_mfma_f32_16x16x32_bf16 v[236:239], v[168:171], v[92:95], v[236:239]\n"
"v_mfma_f32_16x16x32_bf16 v[64:67], v[172:175], v[44:47], v[64:67]\n"
"v_mfma_f32_16x16x32_bf16 v[200:203], v[176:179], v[44:47], v[200:203]\n"
"v_mfma_f32_16x16x32_bf16 v[228:231], v[180:183], v[44:47], v[228:231]\n"
"s_add_u32 s97, s75, 0\n"
"buffer_load_dwordx2 v[252:253], v41, s[4:7], s97 offen sc1\n"
"v_mfma_f32_16x16x32_bf16 v[196:199], v[184:187], v[44:47], v[196:199]\n"
"v_mfma_f32_16x16x32_bf16 v[96:99], v[188:191], v[44:47], v[96:99]\n"
"buffer_load_dwordx4 v[84:87], v35, s[4:7], s98 offen offset:1024 sc1\n"
"v_mfma_f32_16x16x32_bf16 v[236:239], v[192:195], v[44:47], v[236:239]\n"
"; @A G1 1.3\n"
"v_mfma_f32_16x16x32_bf16 v[132:135], a[0:3], v[144:147], v[132:135]\n"
"v_mfma_f32_16x16x32_bf16 v[128:131], a[4:7], v[144:147], v[128:131]\n"
"v_mfma_f32_16x16x32_bf16 v[220:223], a[8:11], v[144:147], v[220:223]\n"
"v_mfma_f32_16x16x32_bf16 v[52:55], a[12:15], v[144:147], v[52:55]\n"
"ds_write_b128 v3, v[64:67] offset:12288\n"
"v_mfma_f32_16x16x32_bf16 v[208:211], a[16:19], v[144:147], v[208:211]\n"
"s_add_u32 s98, s53, 0x8000\n"
"buffer_load_dwordx4 v[44:47], v32, s[4:7], s98 offen sc1\n"
"v_mfma_f32_16x16x32_bf16 v[60:63], a[20:23], v[144:147], v[60:63]\n"
"v_mfma_f32_16x16x32_bf16 v[132:135], a[24:27], v[104:107], v[132:135]\n"
"ds_write_b128 v3, v[200:203] offset:13312\n"
"v_mfma_f32_16x16x32_bf16 v[128:131], a[28:31], v[104:107], v[128:131]\n"
"v_mfma_f32_16x16x32_bf16 v[220:223], a[32:35], v[104:107], v[220:223]\n"
"v_mfma_f32_16x16x32_bf16 v[52:55], a[36:39], v[104:107], v[52:55]\n"
"ds_write_b128 v3, v[228:231] offset:14336\n"
"v_mfma_f32_16x16x32_bf16 v[208:211], a[40:43], v[104:107], v[208:211]\n"
"buffer_load_dwordx4 v[64:67], v32, s[4:7], s98 offen offset:1024 sc1\n"
"v_mfma_f32_16x16x32_bf16 v[60:63], a[44:47], v[104:107], v[60:63]\n"
"s_waitcnt vmcnt(20)\n"
"v_mfma_f32_16x16x32_bf16 v[132:135], a[48:51], v[24:27], v[132:135]\n"
"ds_write_b128 v3, v[196:199] offset:15360\n"
"v_mfma_f32_16x16x32_bf16 v[128:131], a[52:55], v[24:27], v[128:131]\n"
"v_mfma_f32_16x16x32_bf16 v[220:223], a[56:59], v[24:27], v[220:223]\n"
"s_add_u32 s96, s60, 0x48000\n"
"buffer_store_dwordx4 v[100:103], v0, s[4:7], s96 offen sc1\n"
"v_mfma_f32_16x16x32_bf16 v[52:55], a[60:63], v[24:27], v[52:55]\n"
"ds_write_b128 v3, v[96:99] offset:16384\n"
"v_mfma_f32_16x16x32_bf16 v[208:211], a[64:67], v[24:27], v[208:211]\n"
"buffer_load_dwordx4 v[92:95], v33, s[4:7], s98 offen sc1\n"
"v_mfma_f32_16x16x32_bf16 v[60:63], a[68:71], v[24:27], v[60:63]\n"
"v_mfma_f32_16x16x32_bf16 v[132:135], a[72:75], v[116:119], v[132:135]\n"
"ds_write_b128 v3, v[236:239] offset:17408\n"
"v_mfma_f32_16x16x32_bf16 v[128:131], a[76:79], v[116:119], v[128:131]\n"
"v_mfma_f32_16x16x32_bf16 v[220:223], a[80:83], v[116:119], v[220:223]\n"
"v_mfma_f32_16x16x32_bf16 v[52:55], a[84:87], v[116:119], v[52:55]\n"
"s_waitcnt lgkmcnt(0)\n"
"s_barrier\n"
"v_mfma_f32_16x16x32_bf16 v[208:211], a[88:91], v[116:119], v[208:211]\n"
"buffer_load_dwordx4 v[24:27], v33, s[4:7], s98 offen offset:1024 sc1\n"
"v_mfma_f32_16x16x32_bf16 v[60:63], a[92:95], v[116:119], v[60:63]\n"
"s_waitcnt vmcnt(19)\n"
"ds_read_b128 v[96:99], v5 offset:2048\n"
"ds_read_b128 v[104:107], v6 offset:2048\n"
"v_mfma_f32_16x16x32_bf16 v[132:135], a[96:99], v[124:127], v[132:135]\n"
"ds_read_b128 v[116:119], v5 offset:3072\n"
"ds_read_b128 v[136:139], v6 offset:3072\n"
"v_mfma_f32_16x16x32_bf16 v[128:131], a[100:103], v[124:127], v[128:131]\n"
"v_mfma_f32_16x16x32_bf16 v[220:223], a[104:107], v[124:127], v[220:223]\n"
"v_mfma_f32_16x16x32_bf16 v[52:55], a[108:111], v[124:127], v[52:55]\n"
"ds_read_b128 v[144:147], v5 offset:4096\n"
"ds_read_b128 v[196:199], v6 offset:4096\n"
"v_mfma_f32_16x16x32_bf16 v[208:211], a[112:115], v[124:127], v[208:211]\n"
"buffer_load_dwordx4 v[200:203], v34, s[4:7], s98 offen sc1\n"
"v_mfma_f32_16x16x32_bf16 v[60:63], a[116:119], v[124:127], v[60:63]\n"
"ds_read_b128 v[124:127], v5 offset:5120\n"
"ds_read_b128 v[216:219], v6 offset:5120\n"
"v_mfma_f32_16x16x32_bf16 v[132:135], a[120:123], v[28:31], v[132:135]\n"
"s_waitcnt lgkmcnt(4)\n"
"v_add_f32_e32 v96, v96, v104\n"
"v_add_f32_e32 v97, v97, v105\n"
"v_mfma_f32_16x16x32_bf16 v[128:131], a[124:127], v[28:31], v[128:131]\n"
"v_add_f32_e32 v98, v98, v106\n"
"v_add_f32_e32 v99, v99, v107\n"
"v_mfma_f32_16x16x32_bf16 v[220:223], v[68:71], v[28:31], v[220:223]\n"
"ds_read_b128 v[104:107], v5\n"
"ds_read_b128 v[224:227], v6\n"
"v_mfma_f32_16x16x32_bf16 v[52:55], v[72:75], v[28:31], v[52:55]\n"
"ds_read_b128 v[228:231], v5 offset:1024\n"
"ds_read_b128 v[236:239], v6 offset:1024\n"
"v_mfma_f32_16x16x32_bf16 v[208:211], v[76:79], v[28:31], v[208:211]\n"
"buffer_load_dwordx4 v[240:243], v34, s[4:7], s98 offen offset:1024 sc1\n"
"v_mfma_f32_16x16x32_bf16 v[60:63], v[80:83], v[28:31], v[60:63]\n"
"s_waitcnt vmcnt(18)\n"
"v_add_f32_e32 v116, v116, v136\n"
"v_add_f32_e32 v117, v117, v137\n"
"v_mfma_f32_16x16x32_bf16 v[132:135], v[148:151], v[204:207], v[132:135]\n"
"s_waitcnt lgkmcnt(4)\n"
"s_add_u32 s96, s60, 0x40000\n"
"buffer_store_dwordx4 v[100:103], v0, s[4:7], s96 offen sc1\n"
"v_add_f32_e32 v118, v118, v138\n"
"v_add_f32_e32 v119, v119, v139\n"
"v_mfma_f32_16x16x32_bf16 v[128:131], v[152:155], v[204:207], v[128:131]\n"
"v_add_f32_e32 v144, v144, v196\n"
"v_add_f32_e32 v145, v145, v197\n"
"v_mfma_f32_16x16x32_bf16 v[220:223], v[156:159], v[204:207], v[220:223]\n"
"s_waitcnt lgkmcnt(0)\n"
"v_add_f32_e32 v146, v146, v198\n"
"v_add_f32_e32 v147, v147, v199\n"
"v_mfma_f32_16x16x32_bf16 v[52:55], v[160:163], v[204:207], v[52:55]\n"
"v_add_f32_e32 v124, v124, v216\n"
"v_add_f32_e32 v125, v125, v217\n"
"v_mfma_f32_16x16x32_bf16 v[208:211], v[164:167], v[204:207], v[208:211]\n"
"buffer_load_dwordx4 v[28:31], v35, s[4:7], s98 offen sc1\n"
"v_mfma_f32_16x16x32_bf16 v[60:63], v[168:171], v[204:207], v[60:63]\n"
"s_waitcnt vmcnt(10)\n"
"v_cmp_ne_u64_e32 vcc, v[252:253], v[18:19]\n"
"v_mfma_f32_16x16x32_bf16 v[132:135], v[172:175], v[88:91], v[132:135]\n"
"s_cbranch_vccnz .Lmk_f5s\n"
".Lmk_f5r:\n"
"v_add_f32_e32 v126, v126, v218\n"
"v_mfma_f32_16x16x32_bf16 v[128:131], v[176:179], v[88:91], v[128:131]\n"
"v_add_f32_e32 v127, v127, v219\n"
"v_add_f32_e32 v104, v104, v224\n"
"v_mfma_f32_16x16x32_bf16 v[220:223], v[180:183], v[88:91], v[220:223]\n"
"v_add_f32_e32 v105, v105, v225\n"
"v_add_f32_e32 v106, v106, v226\n"
"v_mfma_f32_16x16x32_bf16 v[52:55], v[184:187], v[88:91], v[52:55]\n"
"v_add_f32_e32 v107, v107, v227\n"
"v_add_f32_e32 v228, v228, v236\n"
"v_mfma_f32_16x16x32_bf16 v[208:211], v[188:191], v[88:91], v[208:211]\n"
"buffer_load_dwordx4 v[40:43], v35, s[4:7], s98 offen offset:1024 sc1\n"
"v_mfma_f32_16x16x32_bf16 v[60:63], v[192:195], v[88:91], v[60:63]\n"
"v_add_f32_e32 v229, v229, v237\n"
"v_add_f32_e32 v230, v230, v238\n"
"v_add_f32_e32 v231, v231, v239\n"
".Lmk_z2:\n"
"; @A G1 2.0\n"
"v_mfma_f32_16x16x32_bf16 v[88:91], a[0:3], v[232:235], 0\n"
"v_mfma_f32_16x16x32_bf16 v[100:103], a[4:7], v[232:235], 0\n"
"s_add_u32 s98, s62, 0x800\n"
"v_mfma_f32_16x16x32_bf16 v[136:139], a[8:11], v[232:235], 0\n"
"v_mfma_f32_16x16x32_bf16 v[196:199], a[12:15], v[232:235], 0\n"
"v_add_f32_e32 v220, v220, v96\n"
"v_add_f32_e32 v221, v221, v97\n"
"v_mfma_f32_16x16x32_bf16 v[204:207], a[16:19], v[232:235], 0\n"
"s_add_u32 s97, s54, 0x8000\n"
"buffer_load_dwordx4 v[216:219], v32, s[4:7], s97 offen sc1\n"
"v_mfma_f32_16x16x32_bf16 v[224:227], a[20:23], v[232:235], 0\n"
"buffer_load_dword v253, v10, s[4:7], s98 offen sc1\n"
"v_mfma_f32_16x16x32_bf16 v[88:91], a[24:27], v[108:111], v[88:91]\n"
"v_add_f32_e32 v222, v222, v98\n"
"v_add_f32_e32 v223, v223, v99\n"
"v_mfma_f32_16x16x32_bf16 v[100:103], a[28:31], v[108:111], v[100:103]\n"
"v_add_f32_e32 v52, v52, v116\n"
"v_add_f32_e32 v53, v53, v117\n"
"v_mfma_f32_16x16x32_bf16 v[136:139], a[32:35], v[108:111], v[136:139]\n"
"s_add_u32 s96, s70, 0\n"
"buffer_load_dwordx4 a[128:131], v0, s[4:7], s96 offen sc1\n"
"v_mfma_f32_16x16x32_bf16 v[196:199], a[36:39], v[108:111], v[196:199]\n"
"buffer_load_dword v252, v10, s[4:7], s98 offen offset:256 sc1\n"
"v_mfma_f32_16x16x32_bf16 v[204:207], a[40:43], v[108:111], v[204:207]\n"
"buffer_load_dwordx4 v[96:99], v32, s[4:7], s97 offen offset:1024 sc1\n"
"v_mfma_f32_16x16x32_bf16 v[224:227], a[44:47], v[108:111], v[224:227]\n"
"v_add_f32_e32 v54, v54, v118\n"
"v_add_f32_e32 v55, v55, v119\n"
"v_mfma_f32_16x16x32_bf16 v[88:91], a[48:51], v[120:123], v[88:91]\n"
"buffer_load_dword v251, v10, s[4:7], s98 offen offset:512 sc1\n"
"v_mfma_f32_16x16x32_bf16 v[100:103], a[52:55], v[120:123], v[100:103]\n"
"v_mul_f32_e32 v220, v220, v255\n"
"v_mul_f32_e32 v221, v221, v255\n"
"v_mfma_f32_16x16x32_bf16 v[136:139], a[56:59], v[120:123], v[136:139]\n"
"v_mul_f32_e32 v222, v222, v255\n"
"v_mul_f32_e32 v223, v223, v255\n"
"v_mfma_f32_16x16x32_bf16 v[196:199], a[60:63], v[120:123], v[196:199]\n"
"buffer_load_dword v250, v10, s[4:7], s98 offen offset:768 sc1\n"
"v_mfma_f32_16x16x32_bf16 v[204:207], a[64:67], v[120:123], v[204:207]\n"
"buffer_load_dwordx4 v[108:111], v33, s[4:7], s97 offen sc1\n"
"v_mfma_f32_16x16x32_bf16 v[224:227], a[68:71], v[120:123], v[224:227]\n"
"v_mul_f32_e32 v52, v52, v255\n"
"v_mul_f32_e32 v53, v53, v255\n"
"v_mfma_f32_16x16x32_bf16 v[88:91], a[72:75], v[212:215], v[88:91]\n"
"buffer_load_dwordx4 a[132:135], v0, s[4:7], s96 offen offset:1024 sc1\n"
"v_mfma_f32_16x16x32_bf16 v[100:103], a[76:79], v[212:215], v[100:103]\n"
"buffer_load_dword v249, v10, s[4:7], s98 offen offset:1024 sc1\n"
"v_mfma_f32_16x16x32_bf16 v[136:139], a[80:83], v[212:215], v[136:139]\n"
"v_mul_f32_e32 v54, v54, v255\n"
"v_mul_f32_e32 v55, v55, v255\n"
"v_mfma_f32_16x16x32_bf16 v[196:199], a[84:87], v[212:215], v[196:199]\n"
"v_cvt_pk_bf16_f32 v116, v220, v221\n"
"v_cvt_pk_bf16_f32 v117, v222, v223\n"
"v_mfma_f32_16x16x32_bf16 v[204:207], a[88:91], v[212:215], v[204:207]\n"
"buffer_load_dwordx4 v[120:123], v33, s[4:7], s97 offen offset:1024 sc1\n"
"v_mfma_f32_16x16x32_bf16 v[224:227], a[92:95], v[212:215], v[224:227]\n"
"buffer_load_dword v248, v10, s[4:7], s98 offen offset:1280 sc1\n"
"v_mfma_f32_16x16x32_bf16 v[88:91], a[96:99], v[112:115], v[88:91]\n"
"v_cvt_pk_bf16_f32 v118, v52, v53\n"
"v_cvt_pk_bf16_f32 v119, v54, v55\n"
"v_mfma_f32_16x16x32_bf16 v[100:103], a[100:103], v[112:115], v[100:103]\n"
"ds_write_b128 v8, v[116:119] offset:25600\n"
"v_mfma_f32_16x16x32_bf16 v[136:139], a[104:107], v[112:115], v[136:139]\n"
"buffer_load_dword v247, v10, s[4:7], s98 offen offset:1536 sc1\n"
"v_mfma_f32_16x16x32_bf16 v[196:199], a[108:111], v[112:115], v[196:199]\n"
"s_add_u32 s95, s64, 0x2000\n"
"buffer_store_dwordx4 v[116:119], v8, s[4:7], s95 offen offset:1024 sc1\n"
"v_mfma_f32_16x16x32_bf16 v[204:207], a[112:115], v[112:115], v[204:207]\n"
"buffer_load_dwordx4 v[52:55], v34, s[4:7], s97 offen sc1\n"
"v_mfma_f32_16x16x32_bf16 v[224:227], a[116:119], v[112:115], v[224:227]\n"
"buffer_load_dwordx4 a[136:139], v0, s[4:7], s96 offen offset:2048 sc1\n"
"v_mfma_f32_16x16x32_bf16 v[88:91], a[120:123], v[140:143], v[88:91]\n"
"buffer_load_dword v246, v10, s[4:7], s98 offen offset:1792 sc1\n"
"v_mfma_f32_16x16x32_bf16 v[100:103], a[124:127], v[140:143], v[100:103]\n"
"v_add_f32_e32 v208, v208, v144\n"
"v_add_f32_e32 v209, v209, v145\n"
"v_mfma_f32_16x16x32_bf16 v[136:139], v[68:71], v[140:143], v[136:139]\n"
"v_add_f32_e32 v210, v210, v146\n"
"v_add_f32_e32 v211, v211, v147\n"
"v_mfma_f32_16x16x32_bf16 v[196:199], v[72:75], v[140:143], v[196:199]\n"
"v_add_f32_e32 v60, v60, v124\n"
"v_add_f32_e32 v61, v61, v125\n"
"v_mfma_f32_16x16x32_bf16 v[204:207], v[76:79], v[140:143], v[204:207]\n"
"buffer_load_dwordx4 v[112:115], v34, s[4:7], s97 offen offset:1024 sc1\n"
"v_mfma_f32_16x16x32_bf16 v[224:227], v[80:83], v[140:143], v[224:227]\n"
"s_waitcnt vmcnt(28)\n"
"v_add_f32_e32 v62, v62, v126\n"
"v_add_f32_e32 v63, v63, v127\n"
"v_mfma_f32_16x16x32_bf16 v[88:91], v[148:151], v[56:59], v[88:91]\n"
"v_mul_f32_e32 v208, v208, v255\n"
"v_mul_f32_e32 v209, v209, v255\n"
"v_mfma_f32_16x16x32_bf16 v[100:103], v[152:155], v[56:59], v[100:103]\n"
"v_mul_f32_e32 v210, v210, v255\n"
"v_mul_f32_e32 v211, v211, v255\n"
"v_mfma_f32_16x16x32_bf16 v[136:139], v[156:159], v[56:59], v[136:139]\n"
"v_mul_f32_e32 v60, v60, v255\n"
"v_mul_f32_e32 v61, v61, v255\n"
"v_mfma_f32_16x16x32_bf16 v[196:199], v[160:163], v[56:59], v[196:199]\n"
"buffer_load_dwordx4 a[140:143], v0, s[4:7], s96 offen offset:3072 sc1\n"
"v_mfma_f32_16x16x32_bf16 v[204:207], v[164:167], v[56:59], v[204:207]\n"
"buffer_load_dwordx4 v[116:119], v35, s[4:7], s97 offen sc1\n"
"v_mfma_f32_16x16x32_bf16 v[224:227], v[168:171], v[56:59], v[224:227]\n"
"v_mul_f32_e32 v62, v62, v255\n"
"v_mul_f32_e32 v63, v63, v255\n"
"v_mfma_f32_16x16x32_bf16 v[88:91], v[172:175], v[84:87], v[88:91]\n"
"v_cvt_pk_bf16_f32 v56, v208, v209\n"
"v_cvt_pk_bf16_f32 v57, v210, v211\n"
"v_mfma_f32_16x16x32_bf16 v[100:103], v[176:179], v[84:87], v[100:103]\n"
"v_cvt_pk_bf16_f32 v58, v60, v61\n"
"v_cvt_pk_bf16_f32 v59, v62, v63\n"
"v_mfma_f32_16x16x32_bf16 v[136:139], v[180:183], v[84:87], v[136:139]\n"
"ds_write_b128 v7, v[56:59] offset:58368\n"
"v_mfma_f32_16x16x32_bf16 v[196:199], v[184:187], v[84:87], v[196:199]\n"
"s_add_u32 s98, s64, 0x6000\n"
"buffer_store_dwordx4 v[56:59], v7, s[4:7], s98 offen offset:1024 sc1\n"
"v_mfma_f32_16x16x32_bf16 v[204:207], v[188:191], v[84:87], v[204:207]\n"
"buffer_load_dwordx4 v[56:59], v35, s[4:7], s97 offen offset:1024 sc1\n"
"v_mfma_f32_16x16x32_bf16 v[224:227], v[192:195], v[84:87], v[224:227]\n"
"s_waitcnt vmcnt(30)\n"
"v_add_f32_e32 v132, v132, v104\n"
"v_add_f32_e32 v133, v133, v105\n"
"; @A G1 2.1\n"
"v_mfma_f32_16x16x32_bf16 v[60:63], a[0:3], v[44:47], 0\n"
"v_add_f32_e32 v134, v134, v106\n"
"v_add_f32_e32 v135, v135, v107\n"
"v_mfma_f32_16x16x32_bf16 v[84:87], a[4:7], v[44:47], 0\n"
"buffer_load_dwordx4 a[144:147], v1, s[4:7], s96 offen sc1\n"
"v_mfma_f32_16x16x32_bf16 v[104:107], a[8:11], v[44:47], 0\n"
"v_add_f32_e32 v128, v128, v228\n"
"v_add_f32_e32 v129, v129, v229\n"
"v_mfma_f32_16x16x32_bf16 v[124:127], a[12:15], v[44:47], 0\n"
"ds_write_b128 v3, v[88:91]\n"
"v_mfma_f32_16x16x32_bf16 v[88:91], a[16:19], v[44:47], 0\n"
"s_add_u32 s98, s55, 0x8000\n"
"buffer_load_dwordx4 v[140:143], v32, s[4:7], s98 offen sc1\n"
"v_mfma_f32_16x16x32_bf16 v[144:147], a[20:23], v[44:47], 0\n"
"v_add_f32_e32 v130, v130, v230\n"
"v_add_f32_e32 v131, v131, v231\n"
"v_mfma_f32_16x16x32_bf16 v[60:63], a[24:27], v[64:67], v[60:63]\n"
"ds_write_b128 v3, v[100:103] offset:1024\n"
"v_mfma_f32_16x16x32_bf16 v[84:87], a[28:31], v[64:67], v[84:87]\n"
"v_mul_f32_e32 v132, v132, v254\n"
"v_mul_f32_e32 v133, v133, v254\n"
"v_mfma_f32_16x16x32_bf16 v[104:107], a[32:35], v[64:67], v[104:107]\n"
"v_mul_f32_e32 v134, v134, v254\n"
"v_mul_f32_e32 v135, v135, v254\n"
"v_mfma_f32_16x16x32_bf16 v[124:127], a[36:39], v[64:67], v[124:127]\n"
"ds_write_b128 v3, v[136:139] offset:2048\n"
"v_mfma_f32_16x16x32_bf16 v[88:91], a[40:43], v[64:67], v[88:91]\n"
"buffer_load_dwordx4 v[44:47], v32, s[4:7], s98 offen offset:1024 sc1\n"
"v_mfma_f32_16x16x32_bf16 v[144:147], a[44:47], v[64:67], v[144:147]\n"
"s_waitcnt vmcnt(30)\n"
"buffer_load_dwordx4 a[148:151], v1, s[4:7], s96 offen offset:1024 sc1\n"
"v_mfma_f32_16x16x32_bf16 v[60:63], a[48:51], v[92:95], v[60:63]\n"
"ds_write_b128 v3, v[196:199] offset:3072\n"
"v_mfma_f32_16x16x32_bf16 v[84:87], a[52:55], v[92:95], v[84:87]\n"
"v_mul_f32_e32 v128, v128, v254\n"
"v_mul_f32_e32 v129, v129, v254\n"
"v_mfma_f32_16x16x32_bf16 v[104:107], a[56:59], v[92:95], v[104:107]\n"
"v_mul_f32_e32 v130, v130, v254\n"
"v_mul_f32_e32 v131, v131, v254\n"
"v_mfma_f32_16x16x32_bf16 v[124:127], a[60:63], v[92:95], v[124:127]\n"
"ds_write_b128 v3, v[204:207] offset:4096\n"
"v_mfma_f32_16x16x32_bf16 v[88:91], a[64:67], v[92:95], v[88:91]\n"
"buffer_load_dwordx4 v[64:67], v33, s[4:7], s98 offen sc1\n"
"v_mfma_f32_16x16x32_bf16 v[144:147], a[68:71], v[92:95], v[144:147]\n"
"v_cvt_pk_bf16_f32 v92, v132, v133\n"
"v_cvt_pk_bf16_f32 v93, v134, v135\n"
"v_mfma_f32_16x16x32_bf16 v[60:63], a[72:75], v[24:27], v[60:63]\n"
"ds_write_b128 v3, v[224:227] offset:5120\n"
"v_mfma_f32_16x16x32_bf16 v[84:87], a[76:79], v[24:27], v[84:87]\n"
"v_cvt_pk_bf16_f32 v94, v128, v129\n"
"v_cvt_pk_bf16_f32 v95, v130, v131\n"
"v_mfma_f32_16x16x32_bf16 v[104:107], a[80:83], v[24:27], v[104:107]\n"
"ds_write_b128 v2, v[92:95] offset:50176\n"
"v_mfma_f32_16x16x32_bf16 v[124:127], a[84:87], v[24:27], v[124:127]\n"
"buffer_load_dwordx4 a[152:155], v1, s[4:7], s96 offen offset:2048 sc1\n"
"v_mfma_f32_16x16x32_bf16 v[88:91], a[88:91], v[24:27], v[88:91]\n"
"buffer_load_dwordx4 v[92:95], v33, s[4:7], s98 offen offset:1024 sc1\n"
"v_mfma_f32_16x16x32_bf16 v[144:147], a[92:95], v[24:27], v[144:147]\n"
"s_waitcnt vmcnt(32)\n"
"v_mov_b32_e32 v24, -1\n"
"v_mov_b32_e32 v25, -1\n"
"v_mfma_f32_16x16x32_bf16 v[60:63], a[96:99], v[200:203], v[60:63]\n"
"v_mov_b32_e32 v26, -1\n"
"v_mov_b32_e32 v27, -1\n"
"v_mfma_f32_16x16x32_bf16 v[84:87], a[100:103], v[200:203], v[84:87]\n"
"ds_read_b64_tr_b16 v[100:101], v38 offset:4096\n"
"ds_read_b64_tr_b16 v[102:103], v38 offset:6144\n"
"s_mov_b32 s94, 0x300030\n"
"v_mfma_f32_16x16x32_bf16 v[104:107], a[104:107], v[200:203], v[104:107]\n"
"s_mov_b32 s95, 0x300030\n"
"v_mov_b32_e32 v131, 0x3f803f80\n"
"v_cndmask_b32_e64 v128, 0, v131, s[94:95]\n"
"v_cndmask_b32_e64 v129, 0, v131, s[94:95]\n"
"v_mfma_f32_16x16x32_bf16 v[124:127], a[108:111], v[200:203], v[124:127]\n"
"v_cndmask_b32_e64 v130, 0, v131, s[94:95]\n"
"v_cndmask_b32_e64 v131, 0, v131, s[94:95]\n"
"v_mfma_f32_16x16x32_bf16 v[88:91], a[112:115], v[200:203], v[88:91]\n"
"buffer_load_dwordx4 v[132:135], v34, s[4:7], s98 offen sc1\n"
"s_waitcnt lgkmcnt(0)\n"
"v_mfma_f32_16x16x32_bf16 v[48:51], v[100:103], v[128:131], v[48:51]\n"
"s_add_u32 s97, s22, 0x1000\n"
"v_mfma_f32_16x16x32_bf16 v[144:147], a[116:119], v[200:203], v[144:147]\n"
"s_waitcnt lgkmcnt(0)\n"
"s_barrier\n"
"buffer_store_dwordx4 v[100:103], v0, s[4:7], s97 offen offset:2048 sc1\n"
"v_mfma_f32_16x16x32_bf16 v[60:63], a[120:123], v[240:243], v[60:63]\n"
"s_waitcnt vmcnt(14)\n"
"s_nop 0\n"
"buffer_store_dwordx4 v[48:51], v39, s[4:7], s23 offen sc1\n"
"v_add_f32_e32 v253, v253, v249\n"
"v_add_f32_e32 v252, v252, v248\n"
"v_mfma_f32_16x16x32_bf16 v[84:87], a[124:127], v[240:243], v[84:87]\n"
"buffer_load_dwordx4 a[156:159], v1, s[4:7], s96 offen offset:3072 sc1\n"
"s_add_u32 s97, s51, 0\n"
"v_mfma_f32_16x16x32_bf16 v[104:107], v[68:71], v[240:243], v[104:107]\n"
"s_bitcmp1_b32 s26, 0\n"
"v_add_f32_e32 v251, v251, v247\n"
"v_add_f32_e32 v250, v250, v246\n"
"s_cbranch_scc1 .Lmk_w7s\n"
"buffer_store_dwordx4 a[0:3], v0, s[4:7], s97 offen sc1\n"
".Lmk_w7r:\n"
"v_mfma_f32_16x16x32_bf16 v[124:127], v[72:75], v[240:243], v[124:127]\n"
"v_add_f32_e32 v253, v253, v251\n"
"v_add_f32_e32 v252, v252, v250\n"
"v_mfma_f32_16x16x32_bf16 v[88:91], v[76:79], v[240:243], v[88:91]\n"
"buffer_load_dwordx4 v[36:39], v34, s[4:7], s98 offen offset:1024 sc1\n"
"v_mfma_f32_16x16x32_bf16 v[144:147], v[80:83], v[240:243], v[144:147]\n"
"v_add_f32_e32 v253, v253, v252\n"
"v_mov_b32_e32 v252, v253\n"
"v_mfma_f32_16x16x32_bf16 v[60:63], v[148:151], v[28:31], v[60:63]\n"
"s_nop 0\n"
"v_permlane32_swap_b32_e32 v253, v252\n"
"v_add_f32_e32 v253, v253, v252\n"
"v_mfma_f32_16x16x32_bf16 v[84:87], v[152:155], v[28:31], v[84:87]\n"
"v_mul_f32_e32 v253, 0x3a800000, v253\n"
"v_mov_b32_e32 v252, v253\n"
"v_mfma_f32_16x16x32_bf16 v[104:107], v[156:159], v[28:31], v[104:107]\n"
"s_nop 0\n"
"v_permlane16_swap_b32_e32 v253, v252\n"
"v_cvt_pk_bf16_f32 v255, -v253, 0\n"
"v_mfma_f32_16x16x32_bf16 v[124:127], v[160:163], v[28:31], v[124:127]\n"
"s_add_u32 s97, s70, 0x2000\n"
"v_fma_f32 v252, -v253, v253, v252\n"
"v_lshlrev_b32_e32 v255, 16, v255\n"
"v_mfma_f32_16x16x32_bf16 v[88:91], v[164:167], v[28:31], v[88:91]\n"
"buffer_load_dwordx4 v[48:51], v35, s[4:7], s98 offen sc1\n"
"v_mfma_f32_16x16x32_bf16 v[144:147], v[168:171], v[28:31], v[144:147]\n"
"buffer_load_dwordx4 a[160:163], v0, s[4:7], s97 offen sc1\n"
"v_mfma_f32_16x16x32_bf16 v[60:63], v[172:175], v[40:43], v[60:63]\n"
"ds_read_b128 v[28:31], v4\n"
"ds_read_b128 v[100:103], v9\n"
"v_mfma_f32_16x16x32_bf16 v[84:87], v[176:179], v[40:43], v[84:87]\n"
"ds_read_b128 v[128:131], v4 offset:1024\n"
"ds_read_b128 v[136:139], v9 offset:64\n"
"s_add_u32 s96, s51, 0x400\n"
"v_mfma_f32_16x16x32_bf16 v[104:107], v[180:183], v[40:43], v[104:107]\n"
"ds_read_b128 v[196:199], v4 offset:2048\n"
"ds_read_b128 v[200:203], v9 offset:128\n"
"s_bitcmp1_b32 s26, 0\n"
"s_cbranch_scc1 .Lmk_w8s\n"
"buffer_store_dwordx4 a[4:7], v0, s[4:7], s96 offen sc1\n"
".Lmk_w8r:\n"
"v_mfma_f32_16x16x32_bf16 v[124:127], v[184:187], v[40:43], v[124:127]\n"
"v_max_f32_e32 v252, 0, v252\n"
"v_cvt_pk_bf16_f32 v252, v252, 0\n"
"v_mfma_f32_16x16x32_bf16 v[88:91], v[188:191], v[40:43], v[88:91]\n"
"buffer_load_dwordx4 v[204:207], v35, s[4:7], s98 offen offset:1024 sc1\n"
"v_mfma_f32_16x16x32_bf16 v[144:147], v[192:195], v[40:43], v[144:147]\n"
"v_lshlrev_b32_e32 v252, 16, v252\n"
"v_add_f32_e32 v252, 0x3727c5ac, v252\n"
"; @A G1 2.2\n"
"v_mfma_f32_16x16x32_bf16 v[40:43], a[0:3], v[216:219], 0\n"
"v_cvt_pk_bf16_f32 v252, v252, 0\n"
"v_lshlrev_b32_e32 v252, 16, v252\n"
"v_mfma_f32_16x16x32_bf16 v[208:211], a[4:7], v[216:219], 0\n"
"s_waitcnt lgkmcnt(0)\n"
"v_fmac_f32_e32 v28, v255, v100\n"
"v_fmac_f32_e32 v29, v255, v101\n"
"v_mfma_f32_16x16x32_bf16 v[212:215], a[8:11], v[216:219], 0\n"
"v_fmac_f32_e32 v30, v255, v102\n"
"v_fmac_f32_e32 v31, v255, v103\n"
"v_mfma_f32_16x16x32_bf16 v[100:103], a[12:15], v[216:219], 0\n"
"ds_write_b128 v3, v[60:63] offset:6144\n"
"v_mfma_f32_16x16x32_bf16 v[60:63], a[16:19], v[216:219], 0\n"
"s_add_u32 s98, s52, 0\n"
"buffer_load_dwordx4 v[220:223], v32, s[4:7], s98 offen sc1\n"
"s_mov_b32 s96, .Lmk_z4-.Lmk_g16\n"
"v_mfma_f32_16x16x32_bf16 v[224:227], a[20:23], v[216:219], 0\n"
"buffer_load_dwordx4 a[164:167], v0, s[4:7], s97 offen offset:1024 sc1\n"
"s_getpc_b64 s[84:85]\n"
".Lmk_g16:\n"
"v_mfma_f32_16x16x32_bf16 v[40:43], a[24:27], v[96:99], v[40:43]\n"
"ds_write_b128 v3, v[84:87] offset:7168\n"
"v_mfma_f32_16x16x32_bf16 v[208:211], a[28:31], v[96:99], v[208:211]\n"
"s_add_u32 s84, s84, s96\n"
"s_addc_u32 s85, s85, 0\n"
"s_and_b32 s84, s84, -16\n"
"s_and_b32 s85, s85, 0xffff\n"
"v_mfma_f32_16x16x32_bf16 v[212:215], a[32:35], v[96:99], v[212:215]\n"
"s_sub_u32 s96, .Lmk_end-.Lmk_g16, s96\n"
"s_min_u32 s86, s96, 0x4000\n"
"s_mov_b32 s87, 0x20000\n"
"v_mfma_f32_16x16x32_bf16 v[100:103], a[36:39], v[96:99], v[100:103]\n"
"ds_write_b128 v3, v[104:107] offset:8192\n"
"v_mfma_f32_16x16x32_bf16 v[60:63], a[40:43], v[96:99], v[60:63]\n"
"buffer_load_dwordx4 v[84:87], v32, s[4:7], s98 offen offset:1024 sc1\n"
"buffer_store_dwordx4 v[24:27], v0, s[4:7], s60 offen sc1\n"
"v_mfma_f32_16x16x32_bf16 v[224:227], a[44:47], v[96:99], v[224:227]\n"
"s_mov_b32 m0, 0x27800\n"
"s_nop 0\n"
"buffer_load_dwordx4 v0, s[84:87], s99 offen lds\n"
"v_mfma_f32_16x16x32_bf16 v[40:43], a[48:51], v[108:111], v[40:43]\n"
"ds_write_b128 v3, v[124:127] offset:9216\n"
"v_mfma_f32_16x16x32_bf16 v[208:211], a[52:55], v[108:111], v[208:211]\n"
"v_fmac_f32_e32 v128, v255, v136\n"
"v_fmac_f32_e32 v129, v255, v137\n"
"v_mfma_f32_16x16x32_bf16 v[212:215], a[56:59], v[108:111], v[212:215]\n"
"v_fmac_f32_e32 v130, v255, v138\n"
"v_fmac_f32_e32 v131, v255, v139\n"
"v_mfma_f32_16x16x32_bf16 v[100:103], a[60:63], v[108:111], v[100:103]\n"
"ds_write_b128 v3, v[88:91] offset:10240\n"
"v_mfma_f32_16x16x32_bf16 v[60:63], a[64:67], v[108:111], v[60:63]\n"
"buffer_load_dwordx4 v[88:91], v33, s[4:7], s98 offen sc1\n"
"v_mfma_f32_16x16x32_bf16 v[224:227], a[68:71], v[108:111], v[224:227]\n"
"buffer_load_dwordx4 a[168:171], v0, s[4:7], s97 offen offset:2048 sc1\n"
"v_mfma_f32_16x16x32_bf16 v[40:43], a[72:75], v[120:123], v[40:43]\n"
"ds_write_b128 v3, v[144:147] offset:11264\n"
"v_mfma_f32_16x16x32_bf16 v[208:211], a[76:79], v[120:123], v[208:211]\n"
"v_fmac_f32_e32 v196, v255, v200\n"
"v_fmac_f32_e32 v197, v255, v201\n"
"v_mfma_f32_16x16x32_bf16 v[212:215], a[80:83], v[120:123], v[212:215]\n"
"v_fmac_f32_e32 v198, v255, v202\n"
"v_fmac_f32_e32 v199, v255, v203\n"
"v_mfma_f32_16x16x32_bf16 v[100:103], a[84:87], v[120:123], v[100:103]\n"
"ds_read_b128 v[96:99], v4 offset:3072\n"
"ds_read_b128 v[104:107], v9 offset:192\n"
"v_mfma_f32_16x16x32_bf16 v[60:63], a[88:91], v[120:123], v[60:63]\n"
"buffer_load_dwordx4 v[108:111], v33, s[4:7], s98 offen offset:1024 sc1\n"
"v_mfma_f32_16x16x32_bf16 v[224:227], a[92:95], v[120:123], v[224:227]\n"
"s_waitcnt vmcnt(29)\n"
"ds_read_b128 v[120:123], v4 offset:4096\n"
"ds_read_b128 v[124:127], v9 offset:256\n"
"v_mfma_f32_16x16x32_bf16 v[40:43], a[96:99], v[52:55], v[40:43]\n"
"ds_read_b128 v[136:139], v4 offset:5120\n"
"ds_read_b128 v[144:147], v9 offset:320\n"
"s_add_u32 s96, s60, 0x8000\n"
"buffer_store_dwordx4 v[24:27], v0, s[4:7], s96 offen sc1\n"
"v_mfma_f32_16x16x32_bf16 v[208:211], a[100:103], v[52:55], v[208:211]\n"
"v_sqrt_f32_e32 v252, v252\n"
"v_mfma_f32_16x16x32_bf16 v[212:215], a[104:107], v[52:55], v[212:215]\n"
"v_cvt_pk_bf16_f32 v252, v252, 0\n"
"v_lshlrev_b32_e32 v252, 16, v252\n"
"v_mfma_f32_16x16x32_bf16 v[100:103], a[108:111], v[52:55], v[100:103]\n"
"buffer_load_dwordx4 a[172:175], v0, s[4:7], s97 offen offset:3072 sc1\n"
"v_mfma_f32_16x16x32_bf16 v[60:63], a[112:115], v[52:55], v[60:63]\n"
"buffer_load_dwordx4 v[24:27], v34, s[4:7], s98 offen sc1\n"
"v_mfma_f32_16x16x32_bf16 v[224:227], a[116:119], v[52:55], v[224:227]\n"
"s_waitcnt lgkmcnt(0)\n"
"v_fmac_f32_e32 v96, v255, v104\n"
"v_fmac_f32_e32 v97, v255, v105\n"
"v_mfma_f32_16x16x32_bf16 v[40:43], a[120:123], v[112:115], v[40:43]\n"
"v_fmac_f32_e32 v98, v255, v106\n"
"v_fmac_f32_e32 v99, v255, v107\n"
"v_mfma_f32_16x16x32_bf16 v[208:211], a[124:127], v[112:115], v[208:211]\n"
"v_fmac_f32_e32 v120, v255, v124\n"
"v_fmac_f32_e32 v121, v255, v125\n"
"v_mfma_f32_16x16x32_bf16 v[212:215], v[68:71], v[112:115], v[212:215]\n"
"v_fmac_f32_e32 v122, v255, v126\n"
"v_fmac_f32_e32 v123, v255, v127\n"
"v_mfma_f32_16x16x32_bf16 v[100:103], v[72:75], v[112:115], v[100:103]\n"
"v_fmac_f32_e32 v136, v255, v144\n"
"v_fmac_f32_e32 v137, v255, v145\n"
"v_mfma_f32_16x16x32_bf16 v[60:63], v[76:79], v[112:115], v[60:63]\n"
"buffer_load_dwordx4 v[52:55], v34, s[4:7], s98 offen offset:1024 sc1\n"
"v_mfma_f32_16x16x32_bf16 v[224:227], v[80:83], v[112:115], v[224:227]\n"
"s_waitcnt vmcnt(29)\n"
"s_add_u32 s96, s68, 0x1000\n"
"v_mfma_f32_16x16x32_bf16 v[40:43], v[148:151], v[116:119], v[40:43]\n"
"buffer_store_dwordx2 v[18:19], v11, s[4:7], s96 offen offset:64 sc1\n"
"v_mfma_f32_16x16x32_bf16 v[208:211], v[152:155], v[116:119], v[208:211]\n"
"buffer_load_dwordx4 a[176:179], v1, s[4:7], s97 offen sc1\n"
"v_mfma_f32_16x16x32_bf16 v[212:215], v[156:159], v[116:119], v[212:215]\n"
"v_fmac_f32_e32 v138, v255, v146\n"
"v_fmac_f32_e32 v139, v255, v147\n"
"v_mfma_f32_16x16x32_bf16 v[100:103], v[160:163], v[116:119], v[100:103]\n"
"v_rcp_f32_e32 v255, v252\n"
"v_mfma_f32_16x16x32_bf16 v[60:63], v[164:167], v[116:119], v[60:63]\n"
"buffer_load_dwordx4 v[104:107], v35, s[4:7], s98 offen sc1\n"
"v_mfma_f32_16x16x32_bf16 v[224:227], v[168:171], v[116:119], v[224:227]\n"
"v_mul_f32_e32 v254, 0x3e8293ee, v255\n"
"v_mfma_f32_16x16x32_bf16 v[40:43], v[172:175], v[56:59], v[40:43]\n"
"v_mfma_f32_16x16x32_bf16 v[208:211], v[176:179], v[56:59], v[208:211]\n"
"v_mfma_f32_16x16x32_bf16 v[212:215], v[180:183], v[56:59], v[212:215]\n"
"v_mfma_f32_16x16x32_bf16 v[100:103], v[184:187], v[56:59], v[100:103]\n"
"v_mfma_f32_16x16x32_bf16 v[60:63], v[188:191], v[56:59], v[60:63]\n"
"buffer_load_dwordx4 v[112:115], v35, s[4:7], s98 offen offset:1024 sc1\n"
"v_mfma_f32_16x16x32_bf16 v[224:227], v[192:195], v[56:59], v[224:227]\n"
"s_waitcnt vmcnt(30)\n"
"buffer_load_dwordx4 a[180:183], v1, s[4:7], s97 offen offset:1024 sc1\n"
"; @A G1 2.3\n"
"v_mfma_f32_16x16x32_bf16 v[28:31], a[0:3], v[140:143], v[28:31]\n"
"v_mfma_f32_16x16x32_bf16 v[128:131], a[4:7], v[140:143], v[128:131]\n"
"v_mfma_f32_16x16x32_bf16 v[196:199], a[8:11], v[140:143], v[196:199]\n"
"v_mfma_f32_16x16x32_bf16 v[96:99], a[12:15], v[140:143], v[96:99]\n"
"ds_write_b128 v3, v[40:43] offset:12288\n"
"v_mfma_f32_16x16x32_bf16 v[120:123], a[16:19], v[140:143], v[120:123]\n"
"s_add_u32 s98, s53, 0\n"
"buffer_load_dwordx4 v[40:43], v32, s[4:7], s98 offen sc1\n"
"v_mfma_f32_16x16x32_bf16 v[136:139], a[20:23], v[140:143], v[136:139]\n"
"v_mfma_f32_16x16x32_bf16 v[28:31], a[24:27], v[44:47], v[28:31]\n"
"ds_write_b128 v3, v[208:211] offset:13312\n"
"v_mfma_f32_16x16x32_bf16 v[128:131], a[28:31], v[44:47], v[128:131]\n"
"v_mfma_f32_16x16x32_bf16 v[196:199], a[32:35], v[44:47], v[196:199]\n"
"v_mfma_f32_16x16x32_bf16 v[96:99], a[36:39], v[44:47], v[96:99]\n"
"ds_write_b128 v3, v[212:215] offset:14336\n"
"v_mfma_f32_16x16x32_bf16 v[120:123], a[40:43], v[44:47], v[120:123]\n"
"buffer_load_dwordx4 v[56:59], v32, s[4:7], s98 offen offset:1024 sc1\n"
"v_mfma_f32_16x16x32_bf16 v[136:139], a[44:47], v[44:47], v[136:139]\n"
"s_waitcnt vmcnt(29)\n"
"buffer_load_dwordx4 a[184:187], v1, s[4:7], s97 offen offset:2048 sc1\n"
"v_mfma_f32_16x16x32_bf16 v[28:31], a[48:51], v[64:67], v[28:31]\n"
"ds_write_b128 v3, v[100:103] offset:15360\n"
"v_mfma_f32_16x16x32_bf16 v[128:131], a[52:55], v[64:67], v[128:131]\n"
"v_mfma_f32_16x16x32_bf16 v[196:199], a[56:59], v[64:67], v[196:199]\n"
"v_mfma_f32_16x16x32_bf16 v[96:99], a[60:63], v[64:67], v[96:99]\n"
"ds_write_b128 v3, v[60:63] offset:16384\n"
"v_mfma_f32_16x16x32_bf16 v[120:123], a[64:67], v[64:67], v[120:123]\n"
"buffer_load_dwordx4 v[44:47], v33, s[4:7], s98 offen sc1\n"
"v_mfma_f32_16x16x32_bf16 v[136:139], a[68:71], v[64:67], v[136:139]\n"
"v_mfma_f32_16x16x32_bf16 v[28:31], a[72:75], v[92:95], v[28:31]\n"
"ds_write_b128 v3, v[224:227] offset:17408\n"
"v_mfma_f32_16x16x32_bf16 v[128:131], a[76:79], v[92:95], v[128:131]\n"
"v_mfma_f32_16x16x32_bf16 v[196:199], a[80:83], v[92:95], v[196:199]\n"
"v_mfma_f32_16x16x32_bf16 v[96:99], a[84:87], v[92:95], v[96:99]\n"
"buffer_load_dwordx4 a[188:191], v1, s[4:7], s97 offen offset:3072 sc1\n"
"v_mfma_f32_16x16x32_bf16 v[120:123], a[88:91], v[92:95], v[120:123]\n"
"buffer_load_dwordx4 v[60:63], v33, s[4:7], s98 offen offset:1024 sc1\n"
"v_mfma_f32_16x16x32_bf16 v[136:139], a[92:95], v[92:95], v[136:139]\n"
"s_waitcnt vmcnt(27)\n"
"s_waitcnt lgkmcnt(0)\n"
"s_barrier\n"
"v_mfma_f32_16x16x32_bf16 v[28:31], a[96:99], v[132:135], v[28:31]\n"
"ds_read_b128 v[64:67], v5 offset:2048\n"
"ds_read_b128 v[92:95], v6 offset:2048\n"
"v_mfma_f32_16x16x32_bf16 v[128:131], a[100:103], v[132:135], v[128:131]\n"
"ds_read_b128 v[100:103], v5 offset:3072\n"
"ds_read_b128 v[116:119], v6 offset:3072\n"
"v_mfma_f32_16x16x32_bf16 v[196:199], a[104:107], v[132:135], v[196:199]\n"
"s_waitcnt vmcnt(24)\n"
"s_add_u32 s97, s68, 0x1000\n"
"buffer_store_dwordx2 v[18:19], v11, s[4:7], s97 offen offset:96 sc1\n"
"v_mfma_f32_16x16x32_bf16 v[96:99], a[108:111], v[132:135], v[96:99]\n"
"v_mfma_f32_16x16x32_bf16 v[120:123], a[112:115], v[132:135], v[120:123]\n"
"buffer_load_dwordx4 v[124:127], v34, s[4:7], s98 offen sc1\n"
"v_mfma_f32_16x16x32_bf16 v[136:139], a[116:119], v[132:135], v[136:139]\n"
"s_add_u32 s97, s70, 0x4000\n"
"ds_read_b128 v[132:135], v5 offset:4096\n"
"ds_read_b128 v[140:143], v6 offset:4096\n"
"v_mfma_f32_16x16x32_bf16 v[28:31], a[120:123], v[36:39], v[28:31]\n"
"ds_read_b128 v[144:147], v5 offset:5120\n"
"ds_read_b128 v[200:203], v6 offset:5120\n"
"v_mfma_f32_16x16x32_bf16 v[128:131], a[124:127], v[36:39], v[128:131]\n"
"buffer_load_dwordx4 a[192:195], v0, s[4:7], s97 offen sc1\n"
"v_mfma_f32_16x16x32_bf16 v[196:199], v[68:71], v[36:39], v[196:199]\n"
"s_waitcnt lgkmcnt(4)\n"
"v_add_f32_e32 v64, v64, v92\n"
"v_add_f32_e32 v65, v65, v93\n"
"v_mfma_f32_16x16x32_bf16 v[96:99], v[72:75], v[36:39], v[96:99]\n"
"ds_read_b128 v[208:211], v5\n"
"ds_read_b128 v[212:215], v6\n"
"v_mfma_f32_16x16x32_bf16 v[120:123], v[76:79], v[36:39], v[120:123]\n"
"buffer_load_dwordx4 v[216:219], v34, s[4:7], s98 offen offset:1024 sc1\n"
"v_mfma_f32_16x16x32_bf16 v[136:139], v[80:83], v[36:39], v[136:139]\n"
"s_waitcnt vmcnt(27)\n"
"ds_read_b128 v[36:39], v5 offset:1024\n"
"ds_read_b128 v[224:227], v6 offset:1024\n"
"v_mfma_f32_16x16x32_bf16 v[28:31], v[148:151], v[48:51], v[28:31]\n"
"s_waitcnt lgkmcnt(4)\n"
"v_add_f32_e32 v66, v66, v94\n"
"v_add_f32_e32 v67, v67, v95\n"
"v_mfma_f32_16x16x32_bf16 v[128:131], v[152:155], v[48:51], v[128:131]\n"
"v_add_f32_e32 v100, v100, v116\n"
"v_add_f32_e32 v101, v101, v117\n"
"v_mfma_f32_16x16x32_bf16 v[196:199], v[156:159], v[48:51], v[196:199]\n"
"v_add_f32_e32 v102, v102, v118\n"
"v_add_f32_e32 v103, v103, v119\n"
"v_mfma_f32_16x16x32_bf16 v[96:99], v[160:163], v[48:51], v[96:99]\n"
"v_add_f32_e32 v132, v132, v140\n"
"v_add_f32_e32 v133, v133, v141\n"
"v_mfma_f32_16x16x32_bf16 v[120:123], v[164:167], v[48:51], v[120:123]\n"
"buffer_load_dwordx4 v[92:95], v35, s[4:7], s98 offen sc1\n"
"v_mfma_f32_16x16x32_bf16 v[136:139], v[168:171], v[48:51], v[136:139]\n"
"buffer_load_dwordx4 a[196:199], v0, s[4:7], s97 offen offset:1024 sc1\n"
"v_mfma_f32_16x16x32_bf16 v[28:31], v[172:175], v[204:207], v[28:31]\n"
"s_waitcnt lgkmcnt(0)\n"
"v_add_f32_e32 v134, v134, v142\n"
"v_add_f32_e32 v135, v135, v143\n"
"v_mfma_f32_16x16x32_bf16 v[128:131], v[176:179], v[204:207], v[128:131]\n"
"v_add_f32_e32 v144, v144, v200\n"
"v_add_f32_e32 v145, v145, v201\n"
"v_mfma_f32_16x16x32_bf16 v[196:199], v[180:183], v[204:207], v[196:199]\n"
"v_add_f32_e32 v146, v146, v202\n"
"v_add_f32_e32 v147, v147, v203\n"
"v_mfma_f32_16x16x32_bf16 v[96:99], v[184:187], v[204:207], v[96:99]\n"
"v_add_f32_e32 v208, v208, v212\n"
"v_add_f32_e32 v209, v209, v213\n"
"v_mfma_f32_16x16x32_bf16 v[120:123], v[188:191], v[204:207], v[120:123]\n"
"buffer_load_dwordx4 v[48:51], v35, s[4:7], s98 offen offset:1024 sc1\n"
"v_mfma_f32_16x16x32_bf16 v[136:139], v[192:195], v[204:207], v[136:139]\n"
"v_add_f32_e32 v210, v210, v214\n"
"v_add_f32_e32 v211, v211, v215\n"
"v_add_f32_e32 v36, v36, v224\n"
"v_add_f32_e32 v37, v37, v225\n"
"v_add_f32_e32 v38, v38, v226\n"
"v_add_f32_e32 v39, v39, v227\n"
".Lmk_z3:\n"
"s_waitcnt vmcnt(27)\n"
"; @A G1 3.0\n"
"v_mfma_f32_16x16x32_bf16 v[116:119], a[0:3], v[220:223], 0\n"
"v_mfma_f32_16x16x32_bf16 v[140:143], a[4:7], v[220:223], 0\n"
"s_add_u32 s98, s62, 0\n"
"v_mfma_f32_16x16x32_bf16 v[200:203], a[8:11], v[220:223], 0\n"
"v_mfma_f32_16x16x32_bf16 v[204:207], a[12:15], v[220:223], 0\n"
"v_add_f32_e32 v196, v196, v64\n"
"v_add_f32_e32 v197, v197, v65\n"
"v_mfma_f32_16x16x32_bf16 v[212:215], a[16:19], v[220:223], 0\n"
"s_add_u32 s96, s54, 0\n"
"buffer_load_dwordx4 v[224:227], v32, s[4:7], s96 offen sc1\n"
"v_mfma_f32_16x16x32_bf16 v[228:231], a[20:23], v[220:223], 0\n"
"buffer_load_dword v253, v10, s[4:7], s98 offen sc1\n"
"v_mfma_f32_16x16x32_bf16 v[116:119], a[24:27], v[84:87], v[116:119]\n"
"v_add_f32_e32 v198, v198, v66\n"
"v_add_f32_e32 v199, v199, v67\n"
"v_mfma_f32_16x16x32_bf16 v[140:143], a[28:31], v[84:87], v[140:143]\n"
"v_add_f32_e32 v96, v96, v100\n"
"v_add_f32_e32 v97, v97, v101\n"
"v_mfma_f32_16x16x32_bf16 v[200:203], a[32:35], v[84:87], v[200:203]\n"
"buffer_load_dwordx4 a[200:203], v0, s[4:7], s97 offen offset:2048 sc1\n"
"v_mfma_f32_16x16x32_bf16 v[204:207], a[36:39], v[84:87], v[204:207]\n"
"buffer_load_dword v252, v10, s[4:7], s98 offen offset:256 sc1\n"
"v_mfma_f32_16x16x32_bf16 v[212:215], a[40:43], v[84:87], v[212:215]\n"
"buffer_load_dwordx4 v[64:67], v32, s[4:7], s96 offen offset:1024 sc1\n"
"v_mfma_f32_16x16x32_bf16 v[228:231], a[44:47], v[84:87], v[228:231]\n"
"s_waitcnt vmcnt(27)\n"
"v_add_f32_e32 v98, v98, v102\n"
"v_add_f32_e32 v99, v99, v103\n"
"v_mfma_f32_16x16x32_bf16 v[116:119], a[48:51], v[88:91], v[116:119]\n"
"buffer_load_dword v251, v10, s[4:7], s98 offen offset:512 sc1\n"
"v_mfma_f32_16x16x32_bf16 v[140:143], a[52:55], v[88:91], v[140:143]\n"
"v_mul_f32_e32 v196, v196, v255\n"
"v_mul_f32_e32 v197, v197, v255\n"
"v_mfma_f32_16x16x32_bf16 v[200:203], a[56:59], v[88:91], v[200:203]\n"
"v_mul_f32_e32 v198, v198, v255\n"
"v_mul_f32_e32 v199, v199, v255\n"
"v_mfma_f32_16x16x32_bf16 v[204:207], a[60:63], v[88:91], v[204:207]\n"
"buffer_load_dword v250, v10, s[4:7], s98 offen offset:768 sc1\n"
"v_mfma_f32_16x16x32_bf16 v[212:215], a[64:67], v[88:91], v[212:215]\n"
"buffer_load_dwordx4 v[84:87], v33, s[4:7], s96 offen sc1\n"
"v_mfma_f32_16x16x32_bf16 v[228:231], a[68:71], v[88:91], v[228:231]\n"
"v_mul_f32_e32 v96, v96, v255\n"
"v_mul_f32_e32 v97, v97, v255\n"
"v_mfma_f32_16x16x32_bf16 v[116:119], a[72:75], v[108:111], v[116:119]\n"
"buffer_load_dword v249, v10, s[4:7], s98 offen offset:1024 sc1\n"
"v_mfma_f32_16x16x32_bf16 v[140:143], a[76:79], v[108:111], v[140:143]\n"
"v_mul_f32_e32 v98, v98, v255\n"
"v_mul_f32_e32 v99, v99, v255\n"
"v_mfma_f32_16x16x32_bf16 v[200:203], a[80:83], v[108:111], v[200:203]\n"
"v_cvt_pk_bf16_f32 v88, v196, v197\n"
"v_cvt_pk_bf16_f32 v89, v198, v199\n"
"v_mfma_f32_16x16x32_bf16 v[204:207], a[84:87], v[108:111], v[204:207]\n"
"buffer_load_dword v248, v10, s[4:7], s98 offen offset:1280 sc1\n"
"v_mfma_f32_16x16x32_bf16 v[212:215], a[88:91], v[108:111], v[212:215]\n"
"buffer_load_dwordx4 v[100:103], v33, s[4:7], s96 offen offset:1024 sc1\n"
"v_mfma_f32_16x16x32_bf16 v[228:231], a[92:95], v[108:111], v[228:231]\n"
"s_waitcnt vmcnt(29)\n"
"v_cvt_pk_bf16_f32 v90, v96, v97\n"
"v_cvt_pk_bf16_f32 v91, v98, v99\n"
"v_mfma_f32_16x16x32_bf16 v[116:119], a[96:99], v[24:27], v[116:119]\n"
"buffer_load_dword v247, v10, s[4:7], s98 offen offset:1536 sc1\n"
"v_mfma_f32_16x16x32_bf16 v[140:143], a[100:103], v[24:27], v[140:143]\n"
"ds_write_b128 v8, v[88:91] offset:17408\n"
"v_mfma_f32_16x16x32_bf16 v[200:203], a[104:107], v[24:27], v[200:203]\n"
"buffer_load_dwordx4 a[204:207], v0, s[4:7], s97 offen offset:3072 sc1\n"
"v_mfma_f32_16x16x32_bf16 v[204:207], a[108:111], v[24:27], v[204:207]\n"
"buffer_load_dword v246, v10, s[4:7], s98 offen offset:1792 sc1\n"
"v_mfma_f32_16x16x32_bf16 v[212:215], a[112:115], v[24:27], v[212:215]\n"
"buffer_load_dwordx4 v[96:99], v34, s[4:7], s96 offen sc1\n"
"v_mfma_f32_16x16x32_bf16 v[228:231], a[116:119], v[24:27], v[228:231]\n"
"buffer_store_dwordx4 v[88:91], v8, s[4:7], s64 offen offset:1024 sc1\n"
"v_mfma_f32_16x16x32_bf16 v[116:119], a[120:123], v[52:55], v[116:119]\n"
"v_add_f32_e32 v120, v120, v132\n"
"v_add_f32_e32 v121, v121, v133\n"
"v_mfma_f32_16x16x32_bf16 v[140:143], a[124:127], v[52:55], v[140:143]\n"
"v_add_f32_e32 v122, v122, v134\n"
"v_add_f32_e32 v123, v123, v135\n"
"v_mfma_f32_16x16x32_bf16 v[200:203], v[68:71], v[52:55], v[200:203]\n"
"v_add_f32_e32 v136, v136, v144\n"
"v_add_f32_e32 v137, v137, v145\n"
"v_mfma_f32_16x16x32_bf16 v[204:207], v[72:75], v[52:55], v[204:207]\n"
"v_add_f32_e32 v138, v138, v146\n"
"v_add_f32_e32 v139, v139, v147\n"
"v_mfma_f32_16x16x32_bf16 v[212:215], v[76:79], v[52:55], v[212:215]\n"
"buffer_load_dwordx4 v[24:27], v34, s[4:7], s96 offen offset:1024 sc1\n"
"v_mfma_f32_16x16x32_bf16 v[228:231], v[80:83], v[52:55], v[228:231]\n"
"s_waitcnt vmcnt(31)\n"
"v_mul_f32_e32 v120, v120, v255\n"
"v_mul_f32_e32 v121, v121, v255\n"
"v_mfma_f32_16x16x32_bf16 v[116:119], v[148:151], v[104:107], v[116:119]\n"
"v_mul_f32_e32 v122, v122, v255\n"
"v_mul_f32_e32 v123, v123, v255\n"
"v_mfma_f32_16x16x32_bf16 v[140:143], v[152:155], v[104:107], v[140:143]\n"
"v_mul_f32_e32 v136, v136, v255\n"
"v_mul_f32_e32 v137, v137, v255\n"
"v_mfma_f32_16x16x32_bf16 v[200:203], v[156:159], v[104:107], v[200:203]\n"
"v_mul_f32_e32 v138, v138, v255\n"
"v_mul_f32_e32 v139, v139, v255\n"
"v_mfma_f32_16x16x32_bf16 v[204:207], v[160:163], v[104:107], v[204:207]\n"
"v_cvt_pk_bf16_f32 v52, v120, v121\n"
"v_cvt_pk_bf16_f32 v53, v122, v123\n"
"v_mfma_f32_16x16x32_bf16 v[212:215], v[164:167], v[104:107], v[212:215]\n"
"buffer_load_dwordx4 v[88:91], v35, s[4:7], s96 offen sc1\n"
"v_mfma_f32_16x16x32_bf16 v[228:231], v[168:171], v[104:107], v[228:231]\n"
"v_cvt_pk_bf16_f32 v54, v136, v137\n"
"v_cvt_pk_bf16_f32 v55, v138, v139\n"
"v_mfma_f32_16x16x32_bf16 v[116:119], v[172:175], v[112:115], v[116:119]\n"
"ds_write_b128 v7, v[52:55] offset:50176\n"
"v_mfma_f32_16x16x32_bf16 v[140:143], v[176:179], v[112:115], v[140:143]\n"
"s_add_u32 s98, s64, 0x4000\n"
"buffer_store_dwordx4 v[52:55], v7, s[4:7], s98 offen offset:1024 sc1\n"
"v_mfma_f32_16x16x32_bf16 v[200:203], v[180:183], v[112:115], v[200:203]\n"
"buffer_load_dwordx4 a[208:211], v1, s[4:7], s97 offen sc1\n"
"v_mfma_f32_16x16x32_bf16 v[204:207], v[184:187], v[112:115], v[204:207]\n"
"v_add_f32_e32 v28, v28, v208\n"
"v_add_f32_e32 v29, v29, v209\n"
"v_mfma_f32_16x16x32_bf16 v[212:215], v[188:191], v[112:115], v[212:215]\n"
"buffer_load_dwordx4 v[52:55], v35, s[4:7], s96 offen offset:1024 sc1\n"
"v_mfma_f32_16x16x32_bf16 v[228:231], v[192:195], v[112:115], v[228:231]\n"
"s_waitcnt vmcnt(32)\n"
"v_add_f32_e32 v30, v30, v210\n"
"v_add_f32_e32 v31, v31, v211\n"
"; @A G1 3.1\n"
"v_mfma_f32_16x16x32_bf16 v[104:107], a[0:3], v[40:43], 0\n"
"v_add_f32_e32 v128, v128, v36\n"
"v_add_f32_e32 v129, v129, v37\n"
"v_mfma_f32_16x16x32_bf16 v[108:111], a[4:7], v[40:43], 0\n"
"v_add_f32_e32 v130, v130, v38\n"
"v_add_f32_e32 v131, v131, v39\n"
"v_mfma_f32_16x16x32_bf16 v[36:39], a[8:11], v[40:43], 0\n"
"v_mul_f32_e32 v28, v28, v254\n"
"v_mul_f32_e32 v29, v29, v254\n"
"v_mfma_f32_16x16x32_bf16 v[112:115], a[12:15], v[40:43], 0\n"
"ds_write_b128 v3, v[116:119]\n"
"v_mfma_f32_16x16x32_bf16 v[116:119], a[16:19], v[40:43], 0\n"
"s_add_u32 s98, s55, 0\n"
"buffer_load_dwordx4 v[120:123], v32, s[4:7], s98 offen sc1\n"
"v_mfma_f32_16x16x32_bf16 v[132:135], a[20:23], v[40:43], 0\n"
"v_mul_f32_e32 v30, v30, v254\n"
"v_mul_f32_e32 v31, v31, v254\n"
"v_mfma_f32_16x16x32_bf16 v[104:107], a[24:27], v[56:59], v[104:107]\n"
"ds_write_b128 v3, v[140:143] offset:1024\n"
"v_mfma_f32_16x16x32_bf16 v[108:111], a[28:31], v[56:59], v[108:111]\n"
"v_mul_f32_e32 v128, v128, v254\n"
"v_mul_f32_e32 v129, v129, v254\n"
"v_mfma_f32_16x16x32_bf16 v[36:39], a[32:35], v[56:59], v[36:39]\n"
"v_mul_f32_e32 v130, v130, v254\n"
"v_mul_f32_e32 v131, v131, v254\n"
"v_mfma_f32_16x16x32_bf16 v[112:115], a[36:39], v[56:59], v[112:115]\n"
"ds_write_b128 v3, v[200:203] offset:2048\n"
"v_mfma_f32_16x16x32_bf16 v[116:119], a[40:43], v[56:59], v[116:119]\n"
"buffer_load_dwordx4 v[40:43], v32, s[4:7], s98 offen offset:1024 sc1\n"
"v_mfma_f32_16x16x32_bf16 v[132:135], a[44:47], v[56:59], v[132:135]\n"
"s_waitcnt vmcnt(30)\n"
"v_cvt_pk_bf16_f32 v56, v28, v29\n"
"v_cvt_pk_bf16_f32 v57, v30, v31\n"
"v_mfma_f32_16x16x32_bf16 v[104:107], a[48:51], v[44:47], v[104:107]\n"
"ds_write_b128 v3, v[204:207] offset:3072\n"
"v_mfma_f32_16x16x32_bf16 v[108:111], a[52:55], v[44:47], v[108:111]\n"
"v_cvt_pk_bf16_f32 v58, v128, v129\n"
"v_cvt_pk_bf16_f32 v59, v130, v131\n"
"v_mfma_f32_16x16x32_bf16 v[36:39], a[56:59], v[44:47], v[36:39]\n"
"buffer_load_dwordx4 a[212:215], v1, s[4:7], s97 offen offset:1024 sc1\n"
"v_mfma_f32_16x16x32_bf16 v[112:115], a[60:63], v[44:47], v[112:115]\n"
"ds_write_b128 v3, v[212:215] offset:4096\n"
"v_mfma_f32_16x16x32_bf16 v[116:119], a[64:67], v[44:47], v[116:119]\n"
"buffer_load_dwordx4 v[28:31], v33, s[4:7], s98 offen sc1\n"
"v_mfma_f32_16x16x32_bf16 v[132:135], a[68:71], v[44:47], v[132:135]\n"
"ds_write_b128 v2, v[56:59] offset:17408\n"
"v_mfma_f32_16x16x32_bf16 v[104:107], a[72:75], v[60:63], v[104:107]\n"
"ds_write_b128 v3, v[228:231] offset:5120\n"
"v_mfma_f32_16x16x32_bf16 v[108:111], a[76:79], v[60:63], v[108:111]\n"
"v_mov_b32_e32 v44, -1\n"
"v_mov_b32_e32 v45, -1\n"
"v_mfma_f32_16x16x32_bf16 v[36:39], a[80:83], v[60:63], v[36:39]\n"
"v_mov_b32_e32 v46, -1\n"
"v_mov_b32_e32 v47, -1\n"
"v_mfma_f32_16x16x32_bf16 v[112:115], a[84:87], v[60:63], v[112:115]\n"
"s_add_u32 s96, s51, 0x800\n"
"s_bitcmp1_b32 s26, 0\n"
"s_cbranch_scc1 .Lmk_w9s\n"
"buffer_store_dwordx4 a[8:11], v0, s[4:7], s96 offen sc1\n"
".Lmk_w9r:\n"
"v_mfma_f32_16x16x32_bf16 v[116:119], a[88:91], v[60:63], v[116:119]\n"
"buffer_load_dwordx4 v[56:59], v33, s[4:7], s98 offen offset:1024 sc1\n"
"s_add_u32 s96, s51, 0xc00\n"
"v_mfma_f32_16x16x32_bf16 v[132:135], a[92:95], v[60:63], v[132:135]\n"
"s_waitcnt vmcnt(30)\n"
"s_bitcmp1_b32 s26, 0\n"
"s_cbranch_scc1 .Lmk_w10s\n"
"buffer_store_dwordx4 a[12:15], v0, s[4:7], s96 offen sc1\n"
".Lmk_w10r:\n"
"v_mfma_f32_16x16x32_bf16 v[104:107], a[96:99], v[124:127], v[104:107]\n"
"s_add_u32 s96, s51, 0x1000\n"
"s_bitcmp1_b32 s26, 0\n"
"s_cbranch_scc1 .Lmk_w11s\n"
"buffer_store_dwordx4 a[16:19], v0, s[4:7], s96 offen sc1\n"
".Lmk_w11r:\n"
"v_mfma_f32_16x16x32_bf16 v[108:111], a[100:103], v[124:127], v[108:111]\n"
"v_mfma_f32_16x16x32_bf16 v[36:39], a[104:107], v[124:127], v[36:39]\n"
"v_mfma_f32_16x16x32_bf16 v[112:115], a[108:111], v[124:127], v[112:115]\n"
"v_mfma_f32_16x16x32_bf16 v[116:119], a[112:115], v[124:127], v[116:119]\n"
"buffer_load_dwordx4 v[60:63], v34, s[4:7], s98 offen sc1\n"
"v_mfma_f32_16x16x32_bf16 v[132:135], a[116:119], v[124:127], v[132:135]\n"
"s_waitcnt lgkmcnt(0)\n"
"s_barrier\n"
"v_mfma_f32_16x16x32_bf16 v[104:107], a[120:123], v[216:219], v[104:107]\n"
"s_waitcnt vmcnt(16)\n"
"v_add_f32_e32 v253, v253, v249\n"
"v_add_f32_e32 v252, v252, v248\n"
"v_mfma_f32_16x16x32_bf16 v[108:111], a[124:127], v[216:219], v[108:111]\n"
"v_add_f32_e32 v251, v251, v247\n"
"v_add_f32_e32 v250, v250, v246\n"
"v_mfma_f32_16x16x32_bf16 v[36:39], v[68:71], v[216:219], v[36:39]\n"
"buffer_load_dwordx4 a[216:219], v1, s[4:7], s97 offen offset:2048 sc1\n"
"v_mfma_f32_16x16x32_bf16 v[112:115], v[72:75], v[216:219], v[112:115]\n"
"v_add_f32_e32 v253, v253, v251\n"
"v_add_f32_e32 v252, v252, v250\n"
"v_mfma_f32_16x16x32_bf16 v[116:119], v[76:79], v[216:219], v[116:119]\n"
"buffer_load_dwordx4 v[124:127], v34, s[4:7], s98 offen offset:1024 sc1\n"
"v_mfma_f32_16x16x32_bf16 v[132:135], v[80:83], v[216:219], v[132:135]\n"
"v_add_f32_e32 v253, v253, v252\n"
"v_mov_b32_e32 v252, v253\n"
"v_mfma_f32_16x16x32_bf16 v[104:107], v[148:151], v[92:95], v[104:107]\n"
"s_add_u32 s96, s51, 0x1400\n"
"v_permlane32_swap_b32_e32 v253, v252\n"
"v_add_f32_e32 v253, v253, v252\n"
"s_bitcmp1_b32 s26, 0\n"
"s_cbranch_scc1 .Lmk_w12s\n"
"buffer_store_dwordx4 a[20:23], v0, s[4:7], s96 offen sc1\n"
".Lmk_w12r:\n"
"v_mfma_f32_16x16x32_bf16 v[108:111], v[152:155], v[92:95], v[108:111]\n"
"v_mul_f32_e32 v253, 0x3a800000, v253\n"
"v_mov_b32_e32 v252, v253\n"
"v_mfma_f32_16x16x32_bf16 v[36:39], v[156:159], v[92:95], v[36:39]\n"
"s_add_u32 s96, s75, 0x1000\n"
"buffer_load_dwordx2 v[254:255], v13, s[4:7], s96 offen sc1\n"
"v_mfma_f32_16x16x32_bf16 v[112:115], v[160:163], v[92:95], v[112:115]\n"
"v_permlane16_swap_b32_e32 v253, v252\n"
"v_cvt_pk_bf16_f32 v251, -v253, 0\n"
"v_mfma_f32_16x16x32_bf16 v[116:119], v[164:167], v[92:95], v[116:119]\n"
"buffer_load_dwordx4 v[128:131], v35, s[4:7], s98 offen sc1\n"
"v_mfma_f32_16x16x32_bf16 v[132:135], v[168:171], v[92:95], v[132:135]\n"
"v_fma_f32 v252, -v253, v253, v252\n"
"v_lshlrev_b32_e32 v251, 16, v251\n"
"v_mfma_f32_16x16x32_bf16 v[104:107], v[172:175], v[48:51], v[104:107]\n"
"s_add_u32 s95, s71, 0\n"
"buffer_load_dwordx2 v[248:249], off, s[4:7], s95 sc1\n"
"v_mfma_f32_16x16x32_bf16 v[108:111], v[176:179], v[48:51], v[108:111]\n"
"ds_read_b128 v[92:95], v4\n"
"ds_read_b128 v[136:139], v9\n"
"v_mfma_f32_16x16x32_bf16 v[36:39], v[180:183], v[48:51], v[36:39]\n"
"ds_read_b128 v[140:143], v4 offset:1024\n"
"ds_read_b128 v[144:147], v9 offset:64\n"
"v_mfma_f32_16x16x32_bf16 v[112:115], v[184:187], v[48:51], v[112:115]\n"
"ds_read_b128 v[196:199], v4 offset:2048\n"
"ds_read_b128 v[200:203], v9 offset:128\n"
"v_mfma_f32_16x16x32_bf16 v[116:119], v[188:191], v[48:51], v[116:119]\n"
"buffer_load_dwordx4 v[204:207], v35, s[4:7], s98 offen offset:1024 sc1\n"
"v_mfma_f32_16x16x32_bf16 v[132:135], v[192:195], v[48:51], v[132:135]\n"
"v_max_f32_e32 v252, 0, v252\n"
"v_cvt_pk_bf16_f32 v252, v252, 0\n"
"; @A G1 3.2\n"
"v_mfma_f32_16x16x32_bf16 v[32:35], a[0:3], v[224:227], 0\n"
"s_add_u32 s98, s61, 0x440000\n"
"buffer_store_dwordx4 v[44:47], v0, s[4:7], s98 offen sc1\n"
"v_lshlrev_b32_e32 v252, 16, v252\n"
"v_add_f32_e32 v252, 0x3727c5ac, v252\n"
"v_mfma_f32_16x16x32_bf16 v[48:51], a[4:7], v[224:227], 0\n"
"v_cvt_pk_bf16_f32 v252, v252, 0\n"
"v_lshlrev_b32_e32 v252, 16, v252\n"
"v_mfma_f32_16x16x32_bf16 v[208:211], a[8:11], v[224:227], 0\n"
"buffer_load_dwordx4 a[220:223], v1, s[4:7], s97 offen offset:3072 sc1\n"
"v_mfma_f32_16x16x32_bf16 v[212:215], a[12:15], v[224:227], 0\n"
"ds_write_b128 v3, v[104:107] offset:6144\n"
"v_mfma_f32_16x16x32_bf16 v[104:107], a[16:19], v[224:227], 0\n"
"s_add_u32 s98, s70, 0x6000\n"
"s_waitcnt lgkmcnt(1)\n"
"v_fmac_f32_e32 v92, v251, v136\n"
"v_mfma_f32_16x16x32_bf16 v[216:219], a[20:23], v[224:227], 0\n"
"v_fmac_f32_e32 v93, v251, v137\n"
"v_fmac_f32_e32 v94, v251, v138\n"
"v_mfma_f32_16x16x32_bf16 v[32:35], a[24:27], v[64:67], v[32:35]\n"
"ds_write_b128 v3, v[108:111] offset:7168\n"
"v_mfma_f32_16x16x32_bf16 v[48:51], a[28:31], v[64:67], v[48:51]\n"
"buffer_load_dwordx4 a[224:227], v0, s[4:7], s98 offen sc1\n"
"v_mfma_f32_16x16x32_bf16 v[208:211], a[32:35], v[64:67], v[208:211]\n"
"v_fmac_f32_e32 v95, v251, v139\n"
"v_fmac_f32_e32 v140, v251, v144\n"
"v_mfma_f32_16x16x32_bf16 v[212:215], a[36:39], v[64:67], v[212:215]\n"
"ds_write_b128 v3, v[36:39] offset:8192\n"
"v_mfma_f32_16x16x32_bf16 v[104:107], a[40:43], v[64:67], v[104:107]\n"
"v_fmac_f32_e32 v141, v251, v145\n"
"v_fmac_f32_e32 v142, v251, v146\n"
"v_mfma_f32_16x16x32_bf16 v[216:219], a[44:47], v[64:67], v[216:219]\n"
"buffer_load_dwordx4 a[228:231], v0, s[4:7], s98 offen offset:1024 sc1\n"
"v_mfma_f32_16x16x32_bf16 v[32:35], a[48:51], v[84:87], v[32:35]\n"
"ds_write_b128 v3, v[112:115] offset:9216\n"
"s_add_u32 s97, s61, 0x440000\n"
"v_mfma_f32_16x16x32_bf16 v[48:51], a[52:55], v[84:87], v[48:51]\n"
"buffer_store_dwordx4 v[44:47], v0, s[4:7], s97 offen offset:1024 sc1\n"
"v_fmac_f32_e32 v143, v251, v147\n"
"v_fmac_f32_e32 v196, v251, v200\n"
"v_mfma_f32_16x16x32_bf16 v[208:211], a[56:59], v[84:87], v[208:211]\n"
"v_fmac_f32_e32 v197, v251, v201\n"
"v_fmac_f32_e32 v198, v251, v202\n"
"v_mfma_f32_16x16x32_bf16 v[212:215], a[60:63], v[84:87], v[212:215]\n"
"ds_write_b128 v3, v[116:119] offset:10240\n"
"v_mfma_f32_16x16x32_bf16 v[104:107], a[64:67], v[84:87], v[104:107]\n"
"buffer_load_dwordx4 a[232:235], v0, s[4:7], s98 offen offset:2048 sc1\n"
"v_mfma_f32_16x16x32_bf16 v[216:219], a[68:71], v[84:87], v[216:219]\n"
"ds_read_b128 v[36:39], v4 offset:3072\n"
"v_fmac_f32_e32 v199, v251, v203\n"
"v_mfma_f32_16x16x32_bf16 v[32:35], a[72:75], v[100:103], v[32:35]\n"
"ds_write_b128 v3, v[132:135] offset:11264\n"
"v_mfma_f32_16x16x32_bf16 v[48:51], a[76:79], v[100:103], v[48:51]\n"
"ds_read_b128 v[64:67], v9 offset:192\n"
"ds_read_b128 v[84:87], v4 offset:4096\n"
"v_mfma_f32_16x16x32_bf16 v[208:211], a[80:83], v[100:103], v[208:211]\n"
"buffer_load_dwordx4 a[236:239], v0, s[4:7], s98 offen offset:3072 sc1\n"
"v_mfma_f32_16x16x32_bf16 v[212:215], a[84:87], v[100:103], v[212:215]\n"
"ds_read_b128 v[108:111], v9 offset:256\n"
"ds_read_b128 v[112:115], v4 offset:5120\n"
"v_mfma_f32_16x16x32_bf16 v[104:107], a[88:91], v[100:103], v[104:107]\n"
"ds_read_b128 v[116:119], v9 offset:320\n"
"v_mfma_f32_16x16x32_bf16 v[216:219], a[92:95], v[100:103], v[216:219]\n"
"s_waitcnt vmcnt(27)\n"
"v_sqrt_f32_e32 v252, v252\n"
"v_mfma_f32_16x16x32_bf16 v[32:35], a[96:99], v[96:99], v[32:35]\n"
"buffer_load_dwordx4 a[240:243], v1, s[4:7], s98 offen sc1\n"
"s_add_u32 s97, s63, 0x44000\n"
"buffer_store_dword v44, v14, s[4:7], s97 offen sc1\n"
"v_mfma_f32_16x16x32_bf16 v[48:51], a[100:103], v[96:99], v[48:51]\n"
"s_mov_b32 s20, 0xff0000\n"
"v_cvt_pk_bf16_f32 v252, v252, 0\n"
"v_lshlrev_b32_e32 v252, 16, v252\n"
"v_mfma_f32_16x16x32_bf16 v[208:211], a[104:107], v[96:99], v[208:211]\n"
"s_mov_b32 s21, 0xffffffff\n"
"v_rcp_f32_e32 v253, v252\n"
"v_mfma_f32_16x16x32_bf16 v[212:215], a[108:111], v[96:99], v[212:215]\n"
"s_waitcnt lgkmcnt(0)\n"
"v_fmac_f32_e32 v36, v251, v64\n"
"v_fmac_f32_e32 v37, v251, v65\n"
"v_mfma_f32_16x16x32_bf16 v[104:107], a[112:115], v[96:99], v[104:107]\n"
"buffer_load_dwordx4 a[244:247], v1, s[4:7], s98 offen offset:1024 sc1\n"
"v_mfma_f32_16x16x32_bf16 v[216:219], a[116:119], v[96:99], v[216:219]\n"
"s_mov_b32 s22, 0x1ff0001\n"
"v_fmac_f32_e32 v38, v251, v66\n"
"v_fmac_f32_e32 v39, v251, v67\n"
"v_mfma_f32_16x16x32_bf16 v[32:35], a[120:123], v[24:27], v[32:35]\n"
"s_mov_b32 s23, 0xffffffff\n"
"v_fmac_f32_e32 v84, v251, v108\n"
"v_fmac_f32_e32 v85, v251, v109\n"
"v_mfma_f32_16x16x32_bf16 v[48:51], a[124:127], v[24:27], v[48:51]\n"
"s_mov_b32 s24, 0x3ff0003\n"
"v_fmac_f32_e32 v86, v251, v110\n"
"v_fmac_f32_e32 v87, v251, v111\n"
"v_mfma_f32_16x16x32_bf16 v[208:211], v[68:71], v[24:27], v[208:211]\n"
"buffer_load_dwordx4 a[248:251], v1, s[4:7], s98 offen offset:2048 sc1\n"
"v_mfma_f32_16x16x32_bf16 v[212:215], v[72:75], v[24:27], v[212:215]\n"
"s_mov_b32 s25, 0xffffffff\n"
"v_fmac_f32_e32 v112, v251, v116\n"
"v_fmac_f32_e32 v113, v251, v117\n"
"v_mfma_f32_16x16x32_bf16 v[104:107], v[76:79], v[24:27], v[104:107]\n"
"s_mov_b32 s26, 0x7ff0007\n"
"v_fmac_f32_e32 v114, v251, v118\n"
"v_fmac_f32_e32 v115, v251, v119\n"
"v_mfma_f32_16x16x32_bf16 v[216:219], v[80:83], v[24:27], v[216:219]\n"
"s_waitcnt vmcnt(27)\n"
"v_mul_f32_e32 v252, 0x3e8293ee, v253\n"
"s_mov_b32 s27, 0xffffffff\n"
"v_mfma_f32_16x16x32_bf16 v[32:35], v[148:151], v[88:91], v[32:35]\n"
"s_waitcnt vmcnt(14)\n"
"v_cmp_ne_u64_e32 vcc, v[254:255], v[18:19]\n"
"v_mfma_f32_16x16x32_bf16 v[48:51], v[152:155], v[88:91], v[48:51]\n"
"s_cbranch_vccnz .Lmk_f6s\n"
".Lmk_f6r:\n"
"s_mov_b32 s28, 0xfff000f\n"
"v_mfma_f32_16x16x32_bf16 v[208:211], v[156:159], v[88:91], v[208:211]\n"
"buffer_load_dwordx4 a[252:255], v1, s[4:7], s98 offen offset:3072 sc1\n"
"v_mfma_f32_16x16x32_bf16 v[212:215], v[160:163], v[88:91], v[212:215]\n"
"s_mov_b32 s29, 0xffffffff\n"
"s_mov_b32 s30, 0x1fff001f\n"
"s_mov_b32 s31, 0xffffffff\n"
"v_mfma_f32_16x16x32_bf16 v[104:107], v[164:167], v[88:91], v[104:107]\n"
"s_mov_b32 s32, 0x3fff003f\n"
"s_mov_b32 s33, 0xffffffff\n"
"s_mov_b32 s34, 0x7fff007f\n"
"v_mfma_f32_16x16x32_bf16 v[216:219], v[168:171], v[88:91], v[216:219]\n"
"s_mov_b32 s35, 0xffffffff\n"
"s_mov_b32 s36, 0\n"
"s_mov_b32 s37, 0xff0000\n"
"v_mfma_f32_16x16x32_bf16 v[32:35], v[172:175], v[52:55], v[32:35]\n"
"s_mov_b32 s38, 0\n"
"s_mov_b32 s39, 0x1ff0001\n"
"s_mov_b32 s40, 0\n"
"v_mfma_f32_16x16x32_bf16 v[48:51], v[176:179], v[52:55], v[48:51]\n"
"s_mov_b32 s41, 0x3ff0003\n"
"s_mov_b32 s42, 0\n"
"s_mov_b32 s43, 0x7ff0007\n"
"v_mfma_f32_16x16x32_bf16 v[208:211], v[180:183], v[52:55], v[208:211]\n"
"s_waitcnt vmcnt(13)\n"
"v_cmp_ne_u64_e32 vcc, v[248:249], v[18:19]\n"
"v_mfma_f32_16x16x32_bf16 v[212:215], v[184:187], v[52:55], v[212:215]\n"
"s_cbranch_vccnz .Lmk_f3s\n"
".Lmk_f3r:\n"
"s_add_u32 m0, s77, 0x6000\n"
"v_mfma_f32_16x16x32_bf16 v[104:107], v[188:191], v[52:55], v[104:107]\n"
"s_add_u32 s98, s65, 0x2000\n"
"buffer_load_dwordx4 v2, s[4:7], s98 offen sc1 lds\n"
"v_mfma_f32_16x16x32_bf16 v[216:219], v[192:195], v[52:55], v[216:219]\n"
"buffer_load_dwordx4 v2, s[4:7], s98 offen offset:1024 sc1 lds\n"
"; @A G1 3.3\n"
"v_mfma_f32_16x16x32_bf16 v[92:95], a[0:3], v[120:123], v[92:95]\n"
"s_mov_b32 s44, 0\n"
"s_mov_b32 s45, 0xfff000f\n"
"s_mov_b32 s46, 0\n"
"v_mfma_f32_16x16x32_bf16 v[140:143], a[4:7], v[120:123], v[140:143]\n"
"s_mov_b32 s47, 0x1fff001f\n"
"s_mov_b32 s48, 0\n"
"s_mov_b32 s49, 0x3fff003f\n"
"v_mfma_f32_16x16x32_bf16 v[196:199], a[8:11], v[120:123], v[196:199]\n"
"s_mov_b32 s50, 0\n"
"s_mov_b32 s51, 0x7fff007f\n"
"v_xor_b32_e32 v255, 32, v16\n"
"v_mfma_f32_16x16x32_bf16 v[36:39], a[12:15], v[120:123], v[36:39]\n"
"ds_write_b128 v3, v[32:35] offset:12288\n"
"v_mfma_f32_16x16x32_bf16 v[84:87], a[16:19], v[120:123], v[84:87]\n"
"v_xor_b32_e32 v254, 32, v15\n"
"v_mov_b32_e32 v88, 0x3f803f80\n"
"v_mfma_f32_16x16x32_bf16 v[112:115], a[20:23], v[120:123], v[112:115]\n"
"s_add_u32 m0, s77, 0xe000\n"
"s_add_u32 s98, s65, 0x6000\n"
"v_mov_b32_e32 v89, 0x3f803f80\n"
"v_mfma_f32_16x16x32_bf16 v[92:95], a[24:27], v[40:43], v[92:95]\n"
"ds_write_b128 v3, v[48:51] offset:13312\n"
"v_mfma_f32_16x16x32_bf16 v[140:143], a[28:31], v[40:43], v[140:143]\n"
"buffer_load_dwordx4 v2, s[4:7], s98 offen sc1 lds\n"
"v_mfma_f32_16x16x32_bf16 v[196:199], a[32:35], v[40:43], v[196:199]\n"
"buffer_load_dwordx4 v2, s[4:7], s98 offen offset:1024 sc1 lds\n"
"v_mfma_f32_16x16x32_bf16 v[36:39], a[36:39], v[40:43], v[36:39]\n"
"ds_write_b128 v3, v[208:211] offset:14336\n"
"v_mfma_f32_16x16x32_bf16 v[84:87], a[40:43], v[40:43], v[84:87]\n"
"v_mov_b32_e32 v90, 0x3f803f80\n"
"v_mov_b32_e32 v91, 0x3f803f80\n"
"v_mfma_f32_16x16x32_bf16 v[112:115], a[44:47], v[40:43], v[112:115]\n"
"v_mov_b32_e32 v251, 0x53800000\n"
"v_mfma_f32_16x16x32_bf16 v[92:95], a[48:51], v[28:31], v[92:95]\n"
"ds_write_b128 v3, v[212:215] offset:15360\n"
"v_mfma_f32_16x16x32_bf16 v[140:143], a[52:55], v[28:31], v[140:143]\n"
"s_add_u32 s98, s69, 0x600000\n"
"buffer_load_dwordx4 a[0:3], v0, s[4:7], s98 offen sc1\n"
"v_mfma_f32_16x16x32_bf16 v[196:199], a[56:59], v[28:31], v[196:199]\n"
"v_mfma_f32_16x16x32_bf16 v[36:39], a[60:63], v[28:31], v[36:39]\n"
"ds_write_b128 v3, v[104:107] offset:16384\n"
"v_mfma_f32_16x16x32_bf16 v[84:87], a[64:67], v[28:31], v[84:87]\n"
"v_mfma_f32_16x16x32_bf16 v[112:115], a[68:71], v[28:31], v[112:115]\n"
"v_mfma_f32_16x16x32_bf16 v[92:95], a[72:75], v[56:59], v[92:95]\n"
"ds_write_b128 v3, v[216:219] offset:17408\n"
"v_mfma_f32_16x16x32_bf16 v[140:143], a[76:79], v[56:59], v[140:143]\n"
"buffer_load_dwordx4 a[4:7], v0, s[4:7], s98 offen offset:1024 sc1\n"
"v_mfma_f32_16x16x32_bf16 v[196:199], a[80:83], v[56:59], v[196:199]\n"
"v_mfma_f32_16x16x32_bf16 v[36:39], a[84:87], v[56:59], v[36:39]\n"
"s_waitcnt lgkmcnt(0)\n"
"s_barrier\n"
"v_mfma_f32_16x16x32_bf16 v[84:87], a[88:91], v[56:59], v[84:87]\n"
"ds_read_b128 v[24:27], v5 offset:2048\n"
"ds_read_b128 v[28:31], v6 offset:2048\n"
"v_mfma_f32_16x16x32_bf16 v[112:115], a[92:95], v[56:59], v[112:115]\n"
"ds_read_b128 v[32:35], v5 offset:3072\n"
"ds_read_b128 v[40:43], v6 offset:3072\n"
"v_mfma_f32_16x16x32_bf16 v[92:95], a[96:99], v[60:63], v[92:95]\n"
"buffer_load_dwordx4 a[8:11], v0, s[4:7], s98 offen offset:2048 sc1\n"
"v_mfma_f32_16x16x32_bf16 v[140:143], a[100:103], v[60:63], v[140:143]\n"
"v_mfma_f32_16x16x32_bf16 v[196:199], a[104:107], v[60:63], v[196:199]\n"
"ds_read_b128 v[44:47], v5 offset:4096\n"
"ds_read_b128 v[48:51], v6 offset:4096\n"
"v_mfma_f32_16x16x32_bf16 v[36:39], a[108:111], v[60:63], v[36:39]\n"
"ds_read_b128 v[52:55], v5 offset:5120\n"
"ds_read_b128 v[56:59], v6 offset:5120\n"
"v_mfma_f32_16x16x32_bf16 v[84:87], a[112:115], v[60:63], v[84:87]\n"
"s_waitcnt lgkmcnt(4)\n"
"v_add_f32_e32 v24, v24, v28\n"
"v_add_f32_e32 v25, v25, v29\n"
"v_mfma_f32_16x16x32_bf16 v[112:115], a[116:119], v[60:63], v[112:115]\n"
"buffer_load_dwordx4 a[12:15], v0, s[4:7], s98 offen offset:3072 sc1\n"
"v_mfma_f32_16x16x32_bf16 v[92:95], a[120:123], v[124:127], v[92:95]\n"
"ds_read_b128 v[60:63], v5\n"
"ds_read_b128 v[64:67], v6\n"
"v_mfma_f32_16x16x32_bf16 v[140:143], a[124:127], v[124:127], v[140:143]\n"
"ds_read_b128 v[96:99], v5 offset:1024\n"
"ds_read_b128 v[100:103], v6 offset:1024\n"
"v_mfma_f32_16x16x32_bf16 v[196:199], v[68:71], v[124:127], v[196:199]\n"
"s_waitcnt lgkmcnt(4)\n"
"v_add_f32_e32 v26, v26, v30\n"
"v_add_f32_e32 v27, v27, v31\n"
"v_mfma_f32_16x16x32_bf16 v[36:39], v[72:75], v[124:127], v[36:39]\n"
"v_add_f32_e32 v32, v32, v40\n"
"v_add_f32_e32 v33, v33, v41\n"
"v_mfma_f32_16x16x32_bf16 v[84:87], v[76:79], v[124:127], v[84:87]\n"
"buffer_load_dwordx4 a[16:19], v1, s[4:7], s98 offen sc1\n"
"v_mfma_f32_16x16x32_bf16 v[112:115], v[80:83], v[124:127], v[112:115]\n"
"s_waitcnt vmcnt(21)\n"
"v_add_f32_e32 v34, v34, v42\n"
"v_add_f32_e32 v35, v35, v43\n"
"v_mfma_f32_16x16x32_bf16 v[92:95], v[148:151], v[128:131], v[92:95]\n"
"s_waitcnt lgkmcnt(0)\n"
"s_waitcnt vmcnt(12)\n"
"buffer_store_dwordx2 v[18:19], v11, s[4:7], s68 offen offset:32 sc1\n"
"v_add_f32_e32 v44, v44, v48\n"
"v_add_f32_e32 v45, v45, v49\n"
"v_mfma_f32_16x16x32_bf16 v[140:143], v[152:155], v[128:131], v[140:143]\n"
"v_add_f32_e32 v46, v46, v50\n"
"v_add_f32_e32 v47, v47, v51\n"
"v_mfma_f32_16x16x32_bf16 v[196:199], v[156:159], v[128:131], v[196:199]\n"
"v_add_f32_e32 v52, v52, v56\n"
"v_add_f32_e32 v53, v53, v57\n"
"v_mfma_f32_16x16x32_bf16 v[36:39], v[160:163], v[128:131], v[36:39]\n"
"buffer_load_dwordx4 a[20:23], v1, s[4:7], s98 offen offset:1024 sc1\n"
"v_mfma_f32_16x16x32_bf16 v[84:87], v[164:167], v[128:131], v[84:87]\n"
"v_add_f32_e32 v54, v54, v58\n"
"v_add_f32_e32 v55, v55, v59\n"
"v_mfma_f32_16x16x32_bf16 v[112:115], v[168:171], v[128:131], v[112:115]\n"
"v_add_f32_e32 v60, v60, v64\n"
"v_add_f32_e32 v61, v61, v65\n"
"v_mfma_f32_16x16x32_bf16 v[92:95], v[172:175], v[204:207], v[92:95]\n"
"v_add_f32_e32 v62, v62, v66\n"
"v_add_f32_e32 v63, v63, v67\n"
"v_mfma_f32_16x16x32_bf16 v[140:143], v[176:179], v[204:207], v[140:143]\n"
"v_add_f32_e32 v96, v96, v100\n"
"v_add_f32_e32 v97, v97, v101\n"
"v_mfma_f32_16x16x32_bf16 v[196:199], v[180:183], v[204:207], v[196:199]\n"
"buffer_load_dwordx4 a[24:27], v1, s[4:7], s98 offen offset:2048 sc1\n"
"v_mfma_f32_16x16x32_bf16 v[36:39], v[184:187], v[204:207], v[36:39]\n"
"v_add_f32_e32 v98, v98, v102\n"
"v_add_f32_e32 v99, v99, v103\n"
"v_mfma_f32_16x16x32_bf16 v[84:87], v[188:191], v[204:207], v[84:87]\n"
"v_mfma_f32_16x16x32_bf16 v[112:115], v[192:195], v[204:207], v[112:115]\n"
"buffer_load_dwordx4 a[28:31], v1, s[4:7], s98 offen offset:3072 sc1\n"
".Lmk_z4:\n"
"; @A tail\n"
"s_nop 0\n"
"v_add_f32_e32 v196, v196, v24\n"
"v_add_f32_e32 v197, v197, v25\n"
"v_add_f32_e32 v198, v198, v26\n"
"v_add_f32_e32 v199, v199, v27\n"
"v_add_f32_e32 v36, v36, v32\n"
"v_add_f32_e32 v37, v37, v33\n"
"v_add_f32_e32 v38, v38, v34\n"
"v_add_f32_e32 v39, v39, v35\n"
"v_mul_f32_e32 v196, v196, v253\n"
"v_mul_f32_e32 v197, v197, v253\n"
"v_mul_f32_e32 v198, v198, v253\n"
"v_mul_f32_e32 v199, v199, v253\n"
"v_mul_f32_e32 v36, v36, v253\n"
"v_mul_f32_e32 v37, v37, v253\n"
"v_mul_f32_e32 v38, v38, v253\n"
"v_mul_f32_e32 v39, v39, v253\n"
"v_cvt_pk_bf16_f32 v24, v196, v197\n"
"v_cvt_pk_bf16_f32 v25, v198, v199\n"
"v_cvt_pk_bf16_f32 v26, v36, v37\n"
"v_cvt_pk_bf16_f32 v27, v38, v39\n"
"ds_write_b128 v7, v[24:27] offset:16384\n"
"buffer_store_dwordx4 v[24:27], v7, s[4:7], s64 offen sc1\n"
"v_add_f32_e32 v84, v84, v44\n"
"v_add_f32_e32 v85, v85, v45\n"
"v_add_f32_e32 v86, v86, v46\n"
"v_add_f32_e32 v87, v87, v47\n"
"v_add_f32_e32 v112, v112, v52\n"
"v_add_f32_e32 v113, v113, v53\n"
"v_add_f32_e32 v114, v114, v54\n"
"v_add_f32_e32 v115, v115, v55\n"
"v_mul_f32_e32 v84, v84, v253\n"
"v_mul_f32_e32 v85, v85, v253\n"
"v_mul_f32_e32 v86, v86, v253\n"
"v_mul_f32_e32 v87, v87, v253\n"
"v_mul_f32_e32 v112, v112, v253\n"
"v_mul_f32_e32 v113, v113, v253\n"
"v_mul_f32_e32 v114, v114, v253\n"
"v_mul_f32_e32 v115, v115, v253\n"
"v_cvt_pk_bf16_f32 v24, v84, v85\n"
"v_cvt_pk_bf16_f32 v25, v86, v87\n"
"v_cvt_pk_bf16_f32 v26, v112, v113\n"
"v_cvt_pk_bf16_f32 v27, v114, v115\n"
"ds_write_b128 v7, v[24:27] offset:49152\n"
"s_add_u32 s98, s64, 0x4000\n"
"buffer_store_dwordx4 v[24:27], v7, s[4:7], s98 offen sc1\n"
"v_add_f32_e32 v92, v92, v60\n"
"v_add_f32_e32 v93, v93, v61\n"
"v_add_f32_e32 v94, v94, v62\n"
"v_add_f32_e32 v95, v95, v63\n"
"v_add_f32_e32 v140, v140, v96\n"
"v_add_f32_e32 v141, v141, v97\n"
"v_add_f32_e32 v142, v142, v98\n"
"v_add_f32_e32 v143, v143, v99\n"
"v_mul_f32_e32 v92, v92, v252\n"
"v_mul_f32_e32 v93, v93, v252\n"
"v_mul_f32_e32 v94, v94, v252\n"
"v_mul_f32_e32 v95, v95, v252\n"
"v_mul_f32_e32 v140, v140, v252\n"
"v_mul_f32_e32 v141, v141, v252\n"
"v_mul_f32_e32 v142, v142, v252\n"
"v_mul_f32_e32 v143, v143, v252\n"
"v_cvt_pk_bf16_f32 v24, v92, v93\n"
"v_cvt_pk_bf16_f32 v25, v94, v95\n"
"v_cvt_pk_bf16_f32 v26, v140, v141\n"
"v_cvt_pk_bf16_f32 v27, v142, v143\n"
"ds_write_b128 v2, v[24:27] offset:16384\n"
"s_waitcnt lgkmcnt(0)\n"
"s_barrier\n"
"ds_read_b128 v[28:31], v2 offset:49152\n"
"ds_read_b128 v[32:35], v2 offset:50176\n"
"ds_read_b128 v[92:95], v2 offset:16384\n"
"ds_read_b128 v[96:99], v2 offset:17408\n"
"; @A AT b\n"
"s_mov_b32 s2, 0\n"
"v_mov_b32_e32 v253, 0xff800000\n"
"v_mov_b32_e32 v252, 0xff800000\n"
"v_mov_b32_e32 v250, 0x7f800000\n"
"v_mov_b32_e32 v249, 0x7f800000\n"
".Lmk_a20:\n"
"ds_read_b128 v[24:27], v15 offset:24576\n"
"ds_read_b128 v[36:39], v254 offset:24832\n"
"ds_read_b128 v[40:43], v15 offset:22528\n"
"ds_read_b128 v[44:47], v254 offset:22784\n"
"s_cmp_gt_i32 s78, 11\n"
"s_cselect_b32 s98, 0x80000000, 0\n"
"s_or_b32 s98, s98, 0x7f800000\n"
"s_waitcnt lgkmcnt(2)\n"
"v_mfma_f32_16x16x32_bf16 v[48:51], v[24:27], v[28:31], 0\n"
"v_mfma_f32_16x16x32_bf16 v[52:55], v[24:27], v[32:35], 0\n"
"v_mfma_f32_16x16x32_bf16 v[56:59], v[36:39], v[28:31], 0\n"
"v_mfma_f32_16x16x32_bf16 v[60:63], v[36:39], v[32:35], 0\n"
"s_waitcnt lgkmcnt(0)\n"
"s_nop 3\n"
"v_max_f32_e32 v248, v48, v49\n"
"v_max_f32_e32 v247, v52, v53\n"
"v_max3_f32 v248, v248, v50, v51\n"
"v_max3_f32 v247, v247, v54, v55\n"
"v_max3_f32 v248, v248, v56, v57\n"
"v_max3_f32 v247, v247, v60, v61\n"
"v_max3_f32 v248, v248, v58, v59\n"
"v_max3_f32 v247, v247, v62, v63\n"
"v_mov_b32_e32 v246, v248\n"
"v_mov_b32_e32 v245, v247\n"
"s_nop 0\n"
"v_permlane16_swap_b32_e32 v248, v246\n"
"v_permlane16_swap_b32_e32 v247, v245\n"
"v_max_f32_e32 v248, v248, v246\n"
"v_max_f32_e32 v247, v247, v245\n"
"v_mov_b32_e32 v246, v248\n"
"v_mov_b32_e32 v245, v247\n"
"s_nop 0\n"
"v_permlane32_swap_b32_e32 v248, v246\n"
"v_permlane32_swap_b32_e32 v247, v245\n"
"v_max_f32_e32 v248, v248, v246\n"
"v_max_f32_e32 v247, v247, v245\n"
"v_med3_f32 v248, v248, v253, v250\n"
"v_med3_f32 v247, v247, v252, v249\n"
"v_min_f32_e64 v24, s98, -v248\n"
"v_min_f32_e64 v25, s98, -v248\n"
"v_min_f32_e64 v26, s98, -v248\n"
"v_min_f32_e64 v27, s98, -v248\n"
"v_min_f32_e64 v36, s98, -v247\n"
"v_min_f32_e64 v37, s98, -v247\n"
"v_min_f32_e64 v38, s98, -v247\n"
"v_min_f32_e64 v39, s98, -v247\n"
"v_mfma_f32_16x16x32_bf16 v[64:67], v[40:43], v[28:31], v[24:27]\n"
"ds_read_b128 v[68:71], v15 offset:20480\n"
"s_add_u32 s98, s69, 0x602000\n"
"buffer_load_dwordx4 a[32:35], v0, s[4:7], s98 offen sc1\n"
"v_sub_f32_e32 v48, v48, v248\n"
"v_sub_f32_e32 v49, v49, v248\n"
"v_sub_f32_e32 v50, v50, v248\n"
"v_mfma_f32_16x16x32_bf16 v[72:75], v[40:43], v[32:35], v[36:39]\n"
"buffer_load_dwordx4 a[36:39], v0, s[4:7], s98 offen offset:1024 sc1\n"
"v_sub_f32_e32 v51, v51, v248\n"
"v_sub_f32_e32 v56, v56, v248\n"
"v_sub_f32_e32 v57, v57, v248\n"
"v_sub_f32_e32 v58, v58, v248\n"
"v_sub_f32_e32 v59, v59, v248\n"
"v_mfma_f32_16x16x32_bf16 v[40:43], v[44:47], v[28:31], v[24:27]\n"
"ds_read_b128 v[76:79], v254 offset:20736\n"
"v_sub_f32_e32 v52, v52, v247\n"
"v_sub_f32_e32 v53, v53, v247\n"
"v_sub_f32_e32 v54, v54, v247\n"
"v_sub_f32_e32 v55, v55, v247\n"
"v_mfma_f32_16x16x32_bf16 v[80:83], v[44:47], v[32:35], v[36:39]\n"
"buffer_load_dwordx4 a[40:43], v0, s[4:7], s98 offen offset:2048 sc1\n"
"v_sub_f32_e32 v60, v60, v247\n"
"v_sub_f32_e32 v61, v61, v247\n"
"v_sub_f32_e32 v62, v62, v247\n"
"v_sub_f32_e32 v63, v63, v247\n"
"s_waitcnt lgkmcnt(0)\n"
"v_mfma_f32_16x16x32_bf16 v[44:47], v[68:71], v[28:31], v[24:27]\n"
"ds_read_b128 v[84:87], v15 offset:18432\n"
"buffer_load_dwordx4 a[44:47], v0, s[4:7], s98 offen offset:3072 sc1\n"
"ds_read_b128 v[100:103], v254 offset:18688\n"
"ds_read_b64_tr_b16 v[104:105], v16 offset:22528\n"
"v_exp_f32_e32 v64, v64\n"
"v_exp_f32_e32 v65, v65\n"
"v_exp_f32_e32 v66, v66\n"
"v_exp_f32_e32 v67, v67\n"
"v_exp_f32_e32 v40, v40\n"
"v_exp_f32_e32 v41, v41\n"
"v_mfma_f32_16x16x32_bf16 v[108:111], v[68:71], v[32:35], v[36:39]\n"
"buffer_load_dwordx4 a[48:51], v1, s[4:7], s98 offen sc1\n"
"ds_read_b64_tr_b16 v[106:107], v255 offset:22784\n"
"buffer_load_dwordx4 a[52:55], v1, s[4:7], s98 offen offset:1024 sc1\n"
"v_exp_f32_e32 v42, v42\n"
"v_exp_f32_e32 v43, v43\n"
"v_cvt_pk_bf16_f32 v68, v64, v65\n"
"v_cvt_pk_bf16_f32 v69, v66, v67\n"
"v_cvt_pk_bf16_f32 v70, v40, v41\n"
"v_cvt_pk_bf16_f32 v71, v42, v43\n"
"v_exp_f32_e32 v72, v72\n"
"v_exp_f32_e32 v73, v73\n"
"v_mfma_f32_16x16x32_bf16 v[40:43], v[76:79], v[28:31], v[24:27]\n"
"ds_read_b64_tr_b16 v[64:65], v255 offset:22528\n"
"s_add_u32 s97, s76, 0x80000\n"
"v_exp_f32_e32 v74, v74\n"
"v_exp_f32_e32 v75, v75\n"
"v_exp_f32_e32 v80, v80\n"
"v_bfe_u32 v248, v0, 2, 7\n"
"v_add_u32_e32 v248, s72, v248\n"
"v_and_b32_e32 v247, 0xffffffc0, v248\n"
"v_add_u32_e32 v248, v248, v247\n"
"v_and_b32_e32 v247, 0x200, v0\n"
"v_lshl_add_u32 v248, v247, 4, v248\n"
"v_mfma_f32_16x16x32_bf16 v[112:115], v[76:79], v[32:35], v[36:39]\n"
"buffer_load_dword v247, v248, s[4:7], s97 offen sc1\n"
"ds_read_b64_tr_b16 v[66:67], v16 offset:22784\n"
"s_add_u32 s96, s68, 0x10000\n"
"buffer_store_dwordx2 v[18:19], v11, s[4:7], s96 offen offset:96 sc1\n"
"buffer_load_dword v246, v248, s[4:7], s97 offen offset:1024 sc1\n"
"v_exp_f32_e32 v81, v81\n"
"v_exp_f32_e32 v82, v82\n"
"v_exp_f32_e32 v83, v83\n"
"v_cvt_pk_bf16_f32 v76, v72, v73\n"
"v_cvt_pk_bf16_f32 v77, v74, v75\n"
"v_cvt_pk_bf16_f32 v78, v80, v81\n"
"v_cvt_pk_bf16_f32 v79, v82, v83\n"
"s_waitcnt lgkmcnt(4)\n"
"v_mfma_f32_16x16x32_bf16 v[72:75], v[84:87], v[28:31], v[24:27]\n"
"ds_read_b128 v[80:83], v15 offset:16384\n"
"v_exp_f32_e32 v44, v44\n"
"v_exp_f32_e32 v45, v45\n"
"v_exp_f32_e32 v46, v46\n"
"v_mfma_f32_16x16x32_bf16 v[116:119], v[84:87], v[32:35], v[36:39]\n"
"buffer_load_dwordx4 a[56:59], v1, s[4:7], s98 offen offset:2048 sc1\n"
"ds_read_b128 v[84:87], v254 offset:16640\n"
"v_exp_f32_e32 v47, v47\n"
"v_exp_f32_e32 v40, v40\n"
"v_mfma_f32_16x16x32_bf16 v[120:123], v[100:103], v[28:31], v[24:27]\n"
"ds_read_b64_tr_b16 v[124:125], v16 offset:20480\n"
"v_exp_f32_e32 v41, v41\n"
"v_exp_f32_e32 v42, v42\n"
"v_mfma_f32_16x16x32_bf16 v[128:131], v[100:103], v[32:35], v[36:39]\n"
"buffer_load_dwordx4 a[60:63], v1, s[4:7], s98 offen offset:3072 sc1\n"
"v_exp_f32_e32 v43, v43\n"
"v_cvt_pk_bf16_f32 v100, v44, v45\n"
"v_cvt_pk_bf16_f32 v101, v46, v47\n"
"s_waitcnt lgkmcnt(3)\n"
"v_mfma_f32_16x16x32_bf16 v[44:47], v[88:91], v[68:71], 0\n"
"ds_read_b64_tr_b16 v[126:127], v255 offset:20736\n"
"s_add_u32 s98, s69, 0x604000\n"
"buffer_load_dwordx4 a[64:67], v0, s[4:7], s98 offen sc1\n"
"v_cvt_pk_bf16_f32 v102, v40, v41\n"
"v_cvt_pk_bf16_f32 v103, v42, v43\n"
"v_exp_f32_e32 v108, v108\n"
"v_mfma_f32_16x16x32_bf16 v[40:43], v[104:107], v[68:71], 0\n"
"ds_read_b64_tr_b16 v[132:133], v255 offset:20480\n"
"v_exp_f32_e32 v109, v109\n"
"v_exp_f32_e32 v110, v110\n"
"v_mfma_f32_16x16x32_bf16 v[136:139], v[64:67], v[68:71], 0\n"
"buffer_load_dword v245, v248, s[4:7], s97 offen offset:2048 sc1\n"
"v_exp_f32_e32 v111, v111\n"
"v_exp_f32_e32 v112, v112\n"
"v_mfma_f32_16x16x32_bf16 v[68:71], v[88:91], v[76:79], 0\n"
"ds_read_b64_tr_b16 v[134:135], v16 offset:20736\n"
"v_exp_f32_e32 v113, v113\n"
"v_exp_f32_e32 v114, v114\n"
"v_mfma_f32_16x16x32_bf16 v[140:143], v[104:107], v[76:79], 0\n"
"buffer_load_dword v244, v248, s[4:7], s97 offen offset:3072 sc1\n"
"s_waitcnt vmcnt(14)\n"
"buffer_store_dwordx2 v[18:19], v11, s[4:7], s68 offen offset:64 sc1\n"
"v_exp_f32_e32 v115, v115\n"
"v_mfma_f32_16x16x32_bf16 v[104:107], v[64:67], v[76:79], 0\n"
"s_cmp_lg_u32 s2, 0\n"
"v_cvt_pk_bf16_f32 v64, v108, v109\n"
"v_cvt_pk_bf16_f32 v65, v110, v111\n"
"v_cvt_pk_bf16_f32 v66, v112, v113\n"
"v_cvt_pk_bf16_f32 v67, v114, v115\n"
"s_cbranch_scc1 .Lmk_s22\n"
"s_barrier\n"
".Lmk_s22:\n"
"s_waitcnt lgkmcnt(4)\n"
"v_mfma_f32_16x16x32_bf16 v[76:79], v[80:83], v[28:31], v[24:27]\n"
"ds_read_b128 v[108:111], v15 offset:14336\n"
"v_exp_f32_e32 v72, v72\n"
"v_exp_f32_e32 v73, v73\n"
"v_exp_f32_e32 v74, v74\n"
"v_mfma_f32_16x16x32_bf16 v[112:115], v[80:83], v[32:35], v[36:39]\n"
"buffer_load_dwordx4 a[68:71], v0, s[4:7], s98 offen offset:1024 sc1\n"
"ds_read_b128 v[80:83], v254 offset:14592\n"
"v_exp_f32_e32 v75, v75\n"
"v_mfma_f32_16x16x32_bf16 v[144:147], v[84:87], v[28:31], v[24:27]\n"
"v_exp_f32_e32 v120, v120\n"
"v_exp_f32_e32 v121, v121\n"
"v_exp_f32_e32 v122, v122\n"
"v_mfma_f32_16x16x32_bf16 v[148:151], v[84:87], v[32:35], v[36:39]\n"
"buffer_load_dwordx4 a[72:75], v0, s[4:7], s98 offen offset:2048 sc1\n"
"ds_read_b64_tr_b16 v[84:85], v16 offset:18432\n"
"v_exp_f32_e32 v123, v123\n"
"s_waitcnt lgkmcnt(3)\n"
"v_mfma_f32_16x16x32_bf16 v[44:47], v[88:91], v[100:103], v[44:47]\n"
"buffer_load_dwordx4 a[76:79], v0, s[4:7], s98 offen offset:3072 sc1\n"
"v_cvt_pk_bf16_f32 v152, v72, v73\n"
"v_cvt_pk_bf16_f32 v153, v74, v75\n"
"v_cvt_pk_bf16_f32 v154, v120, v121\n"
"v_cvt_pk_bf16_f32 v155, v122, v123\n"
"v_mfma_f32_16x16x32_bf16 v[40:43], v[124:127], v[100:103], v[40:43]\n"
"ds_read_b64_tr_b16 v[86:87], v255 offset:18688\n"
"v_exp_f32_e32 v116, v116\n"
"v_exp_f32_e32 v117, v117\n"
"v_exp_f32_e32 v118, v118\n"
"v_mfma_f32_16x16x32_bf16 v[136:139], v[132:135], v[100:103], v[136:139]\n"
"s_add_u32 s97, s76, 0x81000\n"
"buffer_load_dword v243, v248, s[4:7], s97 offen sc1\n"
"v_exp_f32_e32 v119, v119\n"
"v_mfma_f32_16x16x32_bf16 v[68:71], v[88:91], v[64:67], v[68:71]\n"
"ds_read_b64_tr_b16 v[72:73], v255 offset:18432\n"
"v_exp_f32_e32 v128, v128\n"
"v_exp_f32_e32 v129, v129\n"
"v_mfma_f32_16x16x32_bf16 v[140:143], v[124:127], v[64:67], v[140:143]\n"
"buffer_load_dword v242, v248, s[4:7], s97 offen offset:1024 sc1\n"
"ds_read_b64_tr_b16 v[74:75], v16 offset:18688\n"
"v_exp_f32_e32 v130, v130\n"
"v_exp_f32_e32 v131, v131\n"
"v_mfma_f32_16x16x32_bf16 v[104:107], v[132:135], v[64:67], v[104:107]\n"
"s_waitcnt lgkmcnt(4)\n"
"v_cvt_pk_bf16_f32 v64, v116, v117\n"
"v_cvt_pk_bf16_f32 v65, v118, v119\n"
"v_cvt_pk_bf16_f32 v66, v128, v129\n"
"v_cvt_pk_bf16_f32 v67, v130, v131\n"
"s_cmp_eq_i32 s78, 8\n"
"s_cbranch_scc1 .Lmk_x23s\n"
".Lmk_x23r:\n"
"v_mfma_f32_16x16x32_bf16 v[100:103], v[108:111], v[28:31], v[24:27]\n"
"ds_read_b128 v[116:119], v15 offset:12288\n"
"v_exp_f32_e32 v76, v76\n"
"v_exp_f32_e32 v77, v77\n"
"v_exp_f32_e32 v78, v78\n"
"v_mfma_f32_16x16x32_bf16 v[120:123], v[108:111], v[32:35], v[36:39]\n"
"buffer_load_dwordx4 a[80:83], v1, s[4:7], s98 offen sc1\n"
"ds_read_b128 v[108:111], v254 offset:12544\n"
"v_exp_f32_e32 v79, v79\n"
"v_mfma_f32_16x16x32_bf16 v[124:127], v[80:83], v[28:31], v[24:27]\n"
"v_exp_f32_e32 v144, v144\n"
"v_exp_f32_e32 v145, v145\n"
"v_exp_f32_e32 v146, v146\n"
"v_mfma_f32_16x16x32_bf16 v[128:131], v[80:83], v[32:35], v[36:39]\n"
"buffer_load_dwordx4 a[84:87], v1, s[4:7], s98 offen offset:1024 sc1\n"
"ds_read_b64_tr_b16 v[80:81], v16 offset:16384\n"
"v_exp_f32_e32 v147, v147\n"
"s_waitcnt lgkmcnt(3)\n"
"v_mfma_f32_16x16x32_bf16 v[44:47], v[88:91], v[152:155], v[44:47]\n"
"buffer_load_dwordx4 a[88:91], v1, s[4:7], s98 offen offset:2048 sc1\n"
"v_cvt_pk_bf16_f32 v132, v76, v77\n"
"v_cvt_pk_bf16_f32 v133, v78, v79\n"
"v_cvt_pk_bf16_f32 v134, v144, v145\n"
"v_cvt_pk_bf16_f32 v135, v146, v147\n"
"v_mfma_f32_16x16x32_bf16 v[40:43], v[84:87], v[152:155], v[40:43]\n"
"ds_read_b64_tr_b16 v[82:83], v255 offset:16640\n"
"v_exp_f32_e32 v112, v112\n"
"v_exp_f32_e32 v113, v113\n"
"v_mfma_f32_16x16x32_bf16 v[136:139], v[72:75], v[152:155], v[136:139]\n"
"buffer_load_dword v241, v248, s[4:7], s97 offen offset:2048 sc1\n"
"v_exp_f32_e32 v114, v114\n"
"v_exp_f32_e32 v115, v115\n"
"v_mfma_f32_16x16x32_bf16 v[68:71], v[88:91], v[64:67], v[68:71]\n"
"ds_read_b64_tr_b16 v[76:77], v255 offset:16384\n"
"v_exp_f32_e32 v148, v148\n"
"v_exp_f32_e32 v149, v149\n"
"v_mfma_f32_16x16x32_bf16 v[140:143], v[84:87], v[64:67], v[140:143]\n"
"buffer_load_dword v240, v248, s[4:7], s97 offen offset:3072 sc1\n"
"ds_read_b64_tr_b16 v[78:79], v16 offset:16640\n"
"v_exp_f32_e32 v150, v150\n"
"v_exp_f32_e32 v151, v151\n"
"v_mfma_f32_16x16x32_bf16 v[104:107], v[72:75], v[64:67], v[104:107]\n"
"s_waitcnt lgkmcnt(4)\n"
"v_cvt_pk_bf16_f32 v64, v112, v113\n"
"v_cvt_pk_bf16_f32 v65, v114, v115\n"
"v_cvt_pk_bf16_f32 v66, v148, v149\n"
"v_cvt_pk_bf16_f32 v67, v150, v151\n"
"s_cmp_eq_i32 s78, 7\n"
"s_cbranch_scc1 .Lmk_x24s\n"
".Lmk_x24r:\n"
"v_mfma_f32_16x16x32_bf16 v[72:75], v[116:119], v[28:31], v[24:27]\n"
"ds_read_b128 v[84:87], v15 offset:10240\n"
"v_exp_f32_e32 v100, v100\n"
"v_exp_f32_e32 v101, v101\n"
"v_exp_f32_e32 v102, v102\n"
"v_mfma_f32_16x16x32_bf16 v[112:115], v[116:119], v[32:35], v[36:39]\n"
"buffer_load_dwordx4 a[92:95], v1, s[4:7], s98 offen offset:3072 sc1\n"
"v_exp_f32_e32 v103, v103\n"
"v_mfma_f32_16x16x32_bf16 v[116:119], v[108:111], v[28:31], v[24:27]\n"
"ds_read_b128 v[144:147], v254 offset:10496\n"
"v_exp_f32_e32 v124, v124\n"
"v_exp_f32_e32 v125, v125\n"
"v_mfma_f32_16x16x32_bf16 v[148:151], v[108:111], v[32:35], v[36:39]\n"
"ds_read_b64_tr_b16 v[108:109], v16 offset:14336\n"
"v_exp_f32_e32 v126, v126\n"
"v_exp_f32_e32 v127, v127\n"
"v_cvt_pk_bf16_f32 v152, v100, v101\n"
"s_waitcnt lgkmcnt(3)\n"
"v_mfma_f32_16x16x32_bf16 v[44:47], v[88:91], v[132:135], v[44:47]\n"
"s_add_u32 s98, s69, 0x606000\n"
"buffer_load_dwordx4 a[96:99], v0, s[4:7], s98 offen sc1\n"
"v_cvt_pk_bf16_f32 v153, v102, v103\n"
"v_cvt_pk_bf16_f32 v154, v124, v125\n"
"v_mfma_f32_16x16x32_bf16 v[40:43], v[80:83], v[132:135], v[40:43]\n"
"ds_read_b64_tr_b16 v[110:111], v255 offset:14592\n"
"v_cvt_pk_bf16_f32 v155, v126, v127\n"
"v_exp_f32_e32 v120, v120\n"
"v_exp_f32_e32 v121, v121\n"
"v_mfma_f32_16x16x32_bf16 v[136:139], v[76:79], v[132:135], v[136:139]\n"
"ds_read_b64_tr_b16 v[100:101], v255 offset:14336\n"
"v_exp_f32_e32 v122, v122\n"
"v_exp_f32_e32 v123, v123\n"
"v_mfma_f32_16x16x32_bf16 v[68:71], v[88:91], v[64:67], v[68:71]\n"
"buffer_load_dwordx4 a[100:103], v0, s[4:7], s98 offen offset:1024 sc1\n"
"v_exp_f32_e32 v128, v128\n"
"v_exp_f32_e32 v129, v129\n"
"v_mfma_f32_16x16x32_bf16 v[140:143], v[80:83], v[64:67], v[140:143]\n"
"ds_read_b64_tr_b16 v[102:103], v16 offset:14592\n"
"v_exp_f32_e32 v130, v130\n"
"v_exp_f32_e32 v131, v131\n"
"v_mfma_f32_16x16x32_bf16 v[104:107], v[76:79], v[64:67], v[104:107]\n"
"s_waitcnt lgkmcnt(4)\n"
"v_cvt_pk_bf16_f32 v64, v120, v121\n"
"v_cvt_pk_bf16_f32 v65, v122, v123\n"
"v_cvt_pk_bf16_f32 v66, v128, v129\n"
"v_cvt_pk_bf16_f32 v67, v130, v131\n"
"s_cmp_eq_i32 s78, 6\n"
"s_cbranch_scc1 .Lmk_x25s\n"
".Lmk_x25r:\n"
"v_mfma_f32_16x16x32_bf16 v[76:79], v[84:87], v[28:31], v[24:27]\n"
"ds_read_b128 v[80:83], v15 offset:8192\n"
"v_exp_f32_e32 v72, v72\n"
"v_exp_f32_e32 v73, v73\n"
"v_exp_f32_e32 v74, v74\n"
"v_mfma_f32_16x16x32_bf16 v[120:123], v[84:87], v[32:35], v[36:39]\n"
"buffer_load_dwordx4 a[104:107], v0, s[4:7], s98 offen offset:2048 sc1\n"
"v_exp_f32_e32 v75, v75\n"
"v_mfma_f32_16x16x32_bf16 v[84:87], v[144:147], v[28:31], v[24:27]\n"
"ds_read_b128 v[124:127], v254 offset:8448\n"
"v_exp_f32_e32 v116, v116\n"
"v_exp_f32_e32 v117, v117\n"
"v_mfma_f32_16x16x32_bf16 v[128:131], v[144:147], v[32:35], v[36:39]\n"
"ds_read_b64_tr_b16 v[132:133], v16 offset:12288\n"
"v_exp_f32_e32 v118, v118\n"
"v_exp_f32_e32 v119, v119\n"
"s_waitcnt lgkmcnt(3)\n"
"v_mfma_f32_16x16x32_bf16 v[44:47], v[88:91], v[152:155], v[44:47]\n"
"buffer_load_dwordx4 a[108:111], v0, s[4:7], s98 offen offset:3072 sc1\n"
"v_cvt_pk_bf16_f32 v144, v72, v73\n"
"v_cvt_pk_bf16_f32 v145, v74, v75\n"
"v_cvt_pk_bf16_f32 v146, v116, v117\n"
"v_mfma_f32_16x16x32_bf16 v[40:43], v[108:111], v[152:155], v[40:43]\n"
"ds_read_b64_tr_b16 v[134:135], v255 offset:12544\n"
"v_cvt_pk_bf16_f32 v147, v118, v119\n"
"v_exp_f32_e32 v112, v112\n"
"v_exp_f32_e32 v113, v113\n"
"v_mfma_f32_16x16x32_bf16 v[136:139], v[100:103], v[152:155], v[136:139]\n"
"ds_read_b64_tr_b16 v[72:73], v255 offset:12288\n"
"v_exp_f32_e32 v114, v114\n"
"v_exp_f32_e32 v115, v115\n"
"v_mfma_f32_16x16x32_bf16 v[68:71], v[88:91], v[64:67], v[68:71]\n"
"buffer_load_dwordx4 a[112:115], v1, s[4:7], s98 offen sc1\n"
"v_exp_f32_e32 v148, v148\n"
"v_exp_f32_e32 v149, v149\n"
"v_mfma_f32_16x16x32_bf16 v[140:143], v[108:111], v[64:67], v[140:143]\n"
"ds_read_b64_tr_b16 v[74:75], v16 offset:12544\n"
"v_exp_f32_e32 v150, v150\n"
"v_exp_f32_e32 v151, v151\n"
"v_mfma_f32_16x16x32_bf16 v[104:107], v[100:103], v[64:67], v[104:107]\n"
"s_waitcnt lgkmcnt(4)\n"
"v_cvt_pk_bf16_f32 v64, v112, v113\n"
"v_cvt_pk_bf16_f32 v65, v114, v115\n"
"v_cvt_pk_bf16_f32 v66, v148, v149\n"
"v_cvt_pk_bf16_f32 v67, v150, v151\n"
"s_cmp_eq_i32 s78, 5\n"
"s_cbranch_scc1 .Lmk_x26s\n"
".Lmk_x26r:\n"
"v_mfma_f32_16x16x32_bf16 v[100:103], v[80:83], v[28:31], v[24:27]\n"
"ds_read_b64_tr_b16 v[108:109], v16 offset:10240\n"
"ds_read_b64_tr_b16 v[110:111], v255 offset:10496\n"
"v_exp_f32_e32 v76, v76\n"
"v_exp_f32_e32 v77, v77\n"
"v_mfma_f32_16x16x32_bf16 v[112:115], v[80:83], v[32:35], v[36:39]\n"
"s_add_u32 s97, s71, 0xfffff000\n"
"buffer_load_dwordx2 v[238:239], off, s[4:7], s97 sc1\n"
"v_exp_f32_e32 v78, v78\n"
"v_exp_f32_e32 v79, v79\n"
"v_mfma_f32_16x16x32_bf16 v[80:83], v[124:127], v[28:31], v[24:27]\n"
"ds_read_b64_tr_b16 v[116:117], v255 offset:10240\n"
"v_exp_f32_e32 v84, v84\n"
"v_exp_f32_e32 v85, v85\n"
"v_mfma_f32_16x16x32_bf16 v[148:151], v[124:127], v[32:35], v[36:39]\n"
"ds_read_b64_tr_b16 v[118:119], v16 offset:10496\n"
"buffer_load_dwordx4 a[116:119], v1, s[4:7], s98 offen offset:1024 sc1\n"
"v_exp_f32_e32 v86, v86\n"
"v_exp_f32_e32 v87, v87\n"
"s_waitcnt lgkmcnt(4)\n"
"v_mfma_f32_16x16x32_bf16 v[44:47], v[88:91], v[144:147], v[44:47]\n"
"ds_read_b64_tr_b16 v[36:37], v16 offset:24576\n"
"ds_read_b64_tr_b16 v[38:39], v255 offset:24832\n"
"v_cvt_pk_bf16_f32 v124, v76, v77\n"
"v_cvt_pk_bf16_f32 v125, v78, v79\n"
"v_cvt_pk_bf16_f32 v126, v84, v85\n"
"v_mfma_f32_16x16x32_bf16 v[40:43], v[132:135], v[144:147], v[40:43]\n"
"ds_read_b64_tr_b16 v[76:77], v255 offset:24576\n"
"buffer_load_dwordx4 a[120:123], v1, s[4:7], s98 offen offset:2048 sc1\n"
"v_cvt_pk_bf16_f32 v127, v86, v87\n"
"v_exp_f32_e32 v120, v120\n"
"v_mfma_f32_16x16x32_bf16 v[136:139], v[72:75], v[144:147], v[136:139]\n"
"ds_read_b64_tr_b16 v[78:79], v16 offset:24832\n"
"v_exp_f32_e32 v121, v121\n"
"v_exp_f32_e32 v122, v122\n"
"v_exp_f32_e32 v123, v123\n"
"v_mfma_f32_16x16x32_bf16 v[68:71], v[88:91], v[64:67], v[68:71]\n"
"v_exp_f32_e32 v128, v128\n"
"v_add_u32_e32 v248, 0x26400, v0\n"
"v_exp_f32_e32 v129, v129\n"
"v_mfma_f32_16x16x32_bf16 v[140:143], v[132:135], v[64:67], v[140:143]\n"
"buffer_load_dwordx4 a[124:127], v1, s[4:7], s98 offen offset:3072 sc1\n"
"ds_read_b128 v[84:87], v248\n"
"v_exp_f32_e32 v130, v130\n"
"v_exp_f32_e32 v131, v131\n"
"v_mfma_f32_16x16x32_bf16 v[104:107], v[72:75], v[64:67], v[104:107]\n"
"ds_read_b128 v[72:75], v248 offset:1024\n"
"v_cvt_pk_bf16_f32 v64, v120, v121\n"
"v_cvt_pk_bf16_f32 v65, v122, v123\n"
"v_cvt_pk_bf16_f32 v66, v128, v129\n"
"v_cvt_pk_bf16_f32 v67, v130, v131\n"
"s_waitcnt lgkmcnt(6)\n"
"v_mfma_f32_16x16x32_bf16 v[44:47], v[88:91], v[124:127], v[44:47]\n"
"ds_read_b64_tr_b16 v[120:121], v16 offset:8192\n"
"s_add_u32 s98, s69, 0x608000\n"
"buffer_load_dwordx4 v[24:27], v0, s[4:7], s98 offen sc1\n"
"ds_read_b64_tr_b16 v[122:123], v255 offset:8448\n"
"v_cndmask_b32_e64 v100, v48, v100, s[20:21]\n"
"v_cndmask_b32_e64 v101, v49, v101, s[22:23]\n"
"v_cndmask_b32_e64 v102, v50, v102, s[24:25]\n"
"v_cndmask_b32_e64 v103, v51, v103, s[26:27]\n"
"v_cndmask_b32_e64 v80, v56, v80, s[28:29]\n"
"v_cndmask_b32_e64 v81, v57, v81, s[30:31]\n"
"v_cndmask_b32_e64 v82, v58, v82, s[32:33]\n"
"v_mfma_f32_16x16x32_bf16 v[40:43], v[108:111], v[124:127], v[40:43]\n"
"ds_read_b64_tr_b16 v[48:49], v255 offset:8192\n"
"ds_read_b64_tr_b16 v[50:51], v16 offset:8448\n"
"v_cndmask_b32_e64 v83, v59, v83, s[34:35]\n"
"v_exp_f32_e32 v100, v100\n"
"v_exp_f32_e32 v101, v101\n"
"v_exp_f32_e32 v102, v102\n"
"s_waitcnt vmcnt(11)\n"
"v_exp_f32_e32 v103, v103\n"
"v_exp_f32_e32 v80, v80\n"
"v_mfma_f32_16x16x32_bf16 v[136:139], v[116:119], v[124:127], v[136:139]\n"
"v_add_f32_e32 v247, v247, v246\n"
"v_exp_f32_e32 v81, v81\n"
"v_exp_f32_e32 v82, v82\n"
"v_add_f32_e32 v247, v247, v245\n"
"v_exp_f32_e32 v83, v83\n"
"v_cvt_pk_bf16_f32 v56, v100, v101\n"
"v_add_f32_e32 v247, v247, v244\n"
"v_cvt_pk_bf16_f32 v57, v102, v103\n"
"v_cvt_pk_bf16_f32 v58, v80, v81\n"
"v_mfma_f32_16x16x32_bf16 v[68:71], v[88:91], v[64:67], v[68:71]\n"
"v_cvt_pk_bf16_f32 v59, v82, v83\n"
"v_add_f32_e32 v247, v247, v243\n"
"v_cndmask_b32_e64 v112, v52, v112, s[36:37]\n"
"v_cndmask_b32_e64 v113, v53, v113, s[38:39]\n"
"v_cndmask_b32_e64 v114, v54, v114, s[40:41]\n"
"v_cndmask_b32_e64 v115, v55, v115, s[42:43]\n"
"v_add_f32_e32 v247, v247, v242\n"
"v_cndmask_b32_e64 v148, v60, v148, s[44:45]\n"
"v_cndmask_b32_e64 v149, v61, v149, s[46:47]\n"
"v_cndmask_b32_e64 v150, v62, v150, s[48:49]\n"
"v_add_f32_e32 v247, v247, v241\n"
"v_cndmask_b32_e64 v151, v63, v151, s[50:51]\n"
"v_exp_f32_e32 v112, v112\n"
"v_mfma_f32_16x16x32_bf16 v[140:143], v[108:111], v[64:67], v[140:143]\n"
"v_exp_f32_e32 v113, v113\n"
"v_add_f32_e32 v247, v247, v240\n"
"v_exp_f32_e32 v114, v114\n"
"v_mov_b32_e32 v246, v247\n"
"v_exp_f32_e32 v115, v115\n"
"v_exp_f32_e32 v148, v148\n"
"v_permlane32_swap_b32_e32 v247, v246\n"
"v_exp_f32_e32 v149, v149\n"
"v_mfma_f32_16x16x32_bf16 v[104:107], v[116:119], v[64:67], v[104:107]\n"
"v_exp_f32_e32 v150, v150\n"
"v_add_f32_e32 v247, v247, v246\n"
"v_exp_f32_e32 v151, v151\n"
"v_cvt_pk_bf16_f32 v52, v112, v113\n"
"ds_write_b32 v21, v247 offset:512\n"
"v_cvt_pk_bf16_f32 v53, v114, v115\n"
"v_cvt_pk_bf16_f32 v54, v148, v149\n"
"v_cvt_pk_bf16_f32 v55, v150, v151\n"
"s_waitcnt lgkmcnt(1)\n"
"v_mfma_f32_16x16x32_bf16 v[44:47], v[88:91], v[56:59], v[44:47]\n"
"v_and_b32_e32 v84, v56, v84\n"
"v_and_b32_e32 v85, v57, v85\n"
"v_and_b32_e32 v86, v58, v86\n"
"v_and_b32_e32 v87, v59, v87\n"
"v_mfma_f32_16x16x32_bf16 v[68:71], v[88:91], v[52:55], v[68:71]\n"
"v_xor_b32_e32 v60, v56, v84\n"
"v_xor_b32_e32 v61, v57, v85\n"
"v_xor_b32_e32 v62, v58, v86\n"
"v_xor_b32_e32 v63, v59, v87\n"
"v_mfma_f32_16x16x32_bf16 v[40:43], v[120:123], v[84:87], v[40:43]\n"
"v_and_b32_e32 v72, v52, v72\n"
"v_and_b32_e32 v73, v53, v73\n"
"v_mfma_f32_16x16x32_bf16 v[136:139], v[48:51], v[84:87], v[136:139]\n"
"v_and_b32_e32 v74, v54, v74\n"
"v_and_b32_e32 v75, v55, v75\n"
"v_mfma_f32_16x16x32_bf16 v[40:43], v[36:39], v[60:63], v[40:43]\n"
"v_xor_b32_e32 v56, v52, v72\n"
"v_xor_b32_e32 v57, v53, v73\n"
"v_mfma_f32_16x16x32_bf16 v[136:139], v[76:79], v[60:63], v[136:139]\n"
"v_xor_b32_e32 v58, v54, v74\n"
"v_xor_b32_e32 v59, v55, v75\n"
"v_mfma_f32_16x16x32_bf16 v[140:143], v[120:123], v[72:75], v[140:143]\n"
"s_waitcnt vmcnt(4)\n"
"v_cmp_ne_u64_e32 vcc, v[238:239], v[18:19]\n"
"s_nop 0\n"
"s_cbranch_vccnz .Lmk_f19s\n"
".Lmk_f19r:\n"
"v_mfma_f32_16x16x32_bf16 v[104:107], v[48:51], v[72:75], v[104:107]\n"
"s_add_u32 m0, s77, 0x4000\n"
"s_add_u32 s97, s65, 0\n"
"buffer_load_dwordx4 v2, s[4:7], s97 offen sc1 lds\n"
"buffer_load_dwordx4 v2, s[4:7], s97 offen offset:1024 sc1 lds\n"
"v_mfma_f32_16x16x32_bf16 v[140:143], v[36:39], v[56:59], v[140:143]\n"
"s_add_u32 m0, s77, 0xc000\n"
"s_add_u32 s97, s65, 0x4000\n"
"buffer_load_dwordx4 v2, s[4:7], s97 offen sc1 lds\n"
"buffer_load_dwordx4 v2, s[4:7], s97 offen offset:1024 sc1 lds\n"
"v_mfma_f32_16x16x32_bf16 v[104:107], v[76:79], v[56:59], v[104:107]\n"
"v_max_f32_e32 v248, v44, v68\n"
"v_rcp_f32_e32 v44, v44\n"
"v_rcp_f32_e32 v68, v68\n"
"s_nop 0\n"
"v_max3_f32 v248, v248, v44, v68\n"
"v_cmp_nlt_f32_e32 vcc, v248, v251\n"
"s_cbranch_vccnz .Lmk_b21\n"
".Lmk_k27:\n"
"s_add_u32 s97, s60, 0x40000\n"
"v_mul_f32_e32 v40, v40, v44\n"
"v_mul_f32_e32 v41, v41, v44\n"
"v_mul_f32_e32 v42, v42, v44\n"
"v_mul_f32_e32 v43, v43, v44\n"
"v_mul_f32_e32 v136, v136, v44\n"
"v_mul_f32_e32 v137, v137, v44\n"
"v_mul_f32_e32 v138, v138, v44\n"
"v_mul_f32_e32 v139, v139, v44\n"
"v_cvt_pk_bf16_f32 v28, v40, v41\n"
"v_cvt_pk_bf16_f32 v29, v42, v43\n"
"v_cvt_pk_bf16_f32 v30, v136, v137\n"
"v_cvt_pk_bf16_f32 v31, v138, v139\n"
"buffer_store_dwordx4 v[28:31], v0, s[4:7], s97 offen sc1\n"
"s_add_u32 s97, s60, 0x48000\n"
"v_mul_f32_e32 v140, v140, v68\n"
"v_mul_f32_e32 v141, v141, v68\n"
"v_mul_f32_e32 v142, v142, v68\n"
"v_mul_f32_e32 v143, v143, v68\n"
"v_mul_f32_e32 v104, v104, v68\n"
"v_mul_f32_e32 v105, v105, v68\n"
"v_mul_f32_e32 v106, v106, v68\n"
"v_mul_f32_e32 v107, v107, v68\n"
"v_cvt_pk_bf16_f32 v28, v140, v141\n"
"v_cvt_pk_bf16_f32 v29, v142, v143\n"
"v_cvt_pk_bf16_f32 v30, v104, v105\n"
"v_cvt_pk_bf16_f32 v31, v106, v107\n"
"buffer_store_dwordx4 v[28:31], v0, s[4:7], s97 offen sc1\n"
"; @A AT a\n"
"s_mov_b32 s2, 0\n"
"v_mov_b32_e32 v253, 0xff800000\n"
"v_mov_b32_e32 v252, 0xff800000\n"
"v_mov_b32_e32 v250, 0x7f800000\n"
"v_mov_b32_e32 v249, 0x7f800000\n"
".Lmk_a28:\n"
"ds_read_b128 v[28:31], v15 offset:16384\n"
"ds_read_b128 v[32:35], v254 offset:16640\n"
"ds_read_b128 v[36:39], v15 offset:14336\n"
"ds_read_b128 v[40:43], v254 offset:14592\n"
"s_cmp_gt_i32 s78, 7\n"
"s_cselect_b32 s97, 0x80000000, 0\n"
"s_or_b32 s97, s97, 0x7f800000\n"
"s_add_u32 s96, s67, 32\n"
"buffer_load_dwordx2 v[246:247], v12, s[4:7], s96 offen sc1\n"
"s_waitcnt lgkmcnt(2)\n"
"v_mfma_f32_16x16x32_bf16 v[100:103], v[28:31], v[92:95], 0\n"
"v_mfma_f32_16x16x32_bf16 v[104:107], v[28:31], v[96:99], 0\n"
"v_mfma_f32_16x16x32_bf16 v[108:111], v[32:35], v[92:95], 0\n"
"v_mfma_f32_16x16x32_bf16 v[112:115], v[32:35], v[96:99], 0\n"
"s_waitcnt lgkmcnt(0)\n"
"s_nop 3\n"
"v_max_f32_e32 v248, v100, v101\n"
"v_max_f32_e32 v245, v104, v105\n"
"v_max3_f32 v248, v248, v102, v103\n"
"v_max3_f32 v245, v245, v106, v107\n"
"v_max3_f32 v248, v248, v108, v109\n"
"v_max3_f32 v245, v245, v112, v113\n"
"v_max3_f32 v248, v248, v110, v111\n"
"v_max3_f32 v245, v245, v114, v115\n"
"v_mov_b32_e32 v244, v248\n"
"v_mov_b32_e32 v243, v245\n"
"s_nop 0\n"
"v_permlane16_swap_b32_e32 v248, v244\n"
"v_permlane16_swap_b32_e32 v245, v243\n"
"v_max_f32_e32 v248, v248, v244\n"
"v_max_f32_e32 v245, v245, v243\n"
"v_mov_b32_e32 v244, v248\n"
"v_mov_b32_e32 v243, v245\n"
"s_nop 0\n"
"v_permlane32_swap_b32_e32 v248, v244\n"
"v_permlane32_swap_b32_e32 v245, v243\n"
"v_max_f32_e32 v248, v248, v244\n"
"v_max_f32_e32 v245, v245, v243\n"
"v_med3_f32 v248, v248, v253, v250\n"
"v_med3_f32 v245, v245, v252, v249\n"
"v_min_f32_e64 v116, s97, -v248\n"
"v_min_f32_e64 v117, s97, -v248\n"
"v_min_f32_e64 v118, s97, -v248\n"
"v_min_f32_e64 v119, s97, -v248\n"
"v_min_f32_e64 v120, s97, -v245\n"
"v_min_f32_e64 v121, s97, -v245\n"
"v_min_f32_e64 v122, s97, -v245\n"
"v_min_f32_e64 v123, s97, -v245\n"
"v_mfma_f32_16x16x32_bf16 v[44:47], v[36:39], v[92:95], v[116:119]\n"
"ds_read_b128 v[32:35], v15 offset:12288\n"
"v_sub_f32_e32 v100, v100, v248\n"
"v_sub_f32_e32 v101, v101, v248\n"
"v_sub_f32_e32 v102, v102, v248\n"
"v_sub_f32_e32 v103, v103, v248\n"
"v_mfma_f32_16x16x32_bf16 v[48:51], v[36:39], v[96:99], v[120:123]\n"
"v_sub_f32_e32 v108, v108, v248\n"
"v_sub_f32_e32 v109, v109, v248\n"
"v_sub_f32_e32 v110, v110, v248\n"
"v_sub_f32_e32 v111, v111, v248\n"
"v_mfma_f32_16x16x32_bf16 v[36:39], v[40:43], v[92:95], v[116:119]\n"
"ds_read_b128 v[52:55], v254 offset:12544\n"
"v_sub_f32_e32 v104, v104, v245\n"
"v_sub_f32_e32 v105, v105, v245\n"
"v_sub_f32_e32 v106, v106, v245\n"
"v_sub_f32_e32 v107, v107, v245\n"
"v_mfma_f32_16x16x32_bf16 v[56:59], v[40:43], v[96:99], v[120:123]\n"
"s_waitcnt lgkmcnt(0)\n"
"v_sub_f32_e32 v112, v112, v245\n"
"v_sub_f32_e32 v113, v113, v245\n"
"v_sub_f32_e32 v114, v114, v245\n"
"v_sub_f32_e32 v115, v115, v245\n"
"s_cmp_eq_i32 s78, 7\n"
"s_cbranch_scc1 .Lmk_x30s\n"
".Lmk_x30r:\n"
"v_mfma_f32_16x16x32_bf16 v[60:63], v[32:35], v[92:95], v[116:119]\n"
"ds_read_b128 v[40:43], v15 offset:10240\n"
"buffer_load_dwordx4 v[28:31], v0, s[4:7], s98 offen offset:1024 sc1\n"
"ds_read_b128 v[64:67], v254 offset:10496\n"
"v_exp_f32_e32 v44, v44\n"
"v_exp_f32_e32 v45, v45\n"
"v_exp_f32_e32 v46, v46\n"
"v_exp_f32_e32 v47, v47\n"
"v_exp_f32_e32 v36, v36\n"
"v_mfma_f32_16x16x32_bf16 v[68:71], v[32:35], v[96:99], v[120:123]\n"
"ds_read_b64_tr_b16 v[72:73], v16 offset:14336\n"
"buffer_load_dwordx4 v[32:35], v0, s[4:7], s98 offen offset:2048 sc1\n"
"v_exp_f32_e32 v37, v37\n"
"v_exp_f32_e32 v38, v38\n"
"v_exp_f32_e32 v39, v39\n"
"v_cvt_pk_bf16_f32 v76, v44, v45\n"
"v_cvt_pk_bf16_f32 v77, v46, v47\n"
"v_cvt_pk_bf16_f32 v78, v36, v37\n"
"v_mfma_f32_16x16x32_bf16 v[80:83], v[52:55], v[92:95], v[116:119]\n"
"ds_read_b64_tr_b16 v[74:75], v255 offset:14592\n"
"ds_read_b64_tr_b16 v[84:85], v255 offset:14336\n"
"v_cvt_pk_bf16_f32 v79, v38, v39\n"
"v_exp_f32_e32 v48, v48\n"
"v_exp_f32_e32 v49, v49\n"
"v_exp_f32_e32 v50, v50\n"
"v_exp_f32_e32 v51, v51\n"
"v_mfma_f32_16x16x32_bf16 v[124:127], v[52:55], v[96:99], v[120:123]\n"
"buffer_load_dwordx4 v[36:39], v0, s[4:7], s98 offen offset:3072 sc1\n"
"ds_read_b64_tr_b16 v[86:87], v16 offset:14592\n"
"v_exp_f32_e32 v56, v56\n"
"v_exp_f32_e32 v57, v57\n"
"v_exp_f32_e32 v58, v58\n"
"v_exp_f32_e32 v59, v59\n"
"v_cvt_pk_bf16_f32 v52, v48, v49\n"
"v_cvt_pk_bf16_f32 v53, v50, v51\n"
"v_cvt_pk_bf16_f32 v54, v56, v57\n"
"v_cvt_pk_bf16_f32 v55, v58, v59\n"
"s_waitcnt lgkmcnt(4)\n"
"s_cmp_eq_i32 s78, 6\n"
"s_cbranch_scc1 .Lmk_x31s\n"
".Lmk_x31r:\n"
"v_mfma_f32_16x16x32_bf16 v[56:59], v[40:43], v[92:95], v[116:119]\n"
"ds_read_b128 v[128:131], v15 offset:8192\n"
"v_exp_f32_e32 v60, v60\n"
"v_exp_f32_e32 v61, v61\n"
"v_exp_f32_e32 v62, v62\n"
"v_mfma_f32_16x16x32_bf16 v[132:135], v[40:43], v[96:99], v[120:123]\n"
"buffer_load_dwordx4 v[40:43], v1, s[4:7], s98 offen sc1\n"
"v_exp_f32_e32 v63, v63\n"
"v_mfma_f32_16x16x32_bf16 v[136:139], v[64:67], v[92:95], v[116:119]\n"
"ds_read_b128 v[140:143], v254 offset:8448\n"
"v_exp_f32_e32 v80, v80\n"
"v_exp_f32_e32 v81, v81\n"
"v_mfma_f32_16x16x32_bf16 v[144:147], v[64:67], v[96:99], v[120:123]\n"
"ds_read_b64_tr_b16 v[64:65], v16 offset:12288\n"
"v_exp_f32_e32 v82, v82\n"
"v_exp_f32_e32 v83, v83\n"
"s_waitcnt lgkmcnt(3)\n"
"v_mfma_f32_16x16x32_bf16 v[148:151], v[88:91], v[76:79], 0\n"
"buffer_load_dwordx4 v[44:47], v1, s[4:7], s98 offen offset:1024 sc1\n"
"v_cvt_pk_bf16_f32 v152, v60, v61\n"
"v_cvt_pk_bf16_f32 v153, v62, v63\n"
"v_cvt_pk_bf16_f32 v154, v80, v81\n"
"v_mfma_f32_16x16x32_bf16 v[156:159], v[72:75], v[76:79], 0\n"
"ds_read_b64_tr_b16 v[66:67], v255 offset:12544\n"
"v_cvt_pk_bf16_f32 v155, v82, v83\n"
"v_exp_f32_e32 v68, v68\n"
"v_exp_f32_e32 v69, v69\n"
"v_mfma_f32_16x16x32_bf16 v[160:163], v[84:87], v[76:79], 0\n"
"ds_read_b64_tr_b16 v[76:77], v255 offset:12288\n"
"v_exp_f32_e32 v70, v70\n"
"v_exp_f32_e32 v71, v71\n"
"v_mfma_f32_16x16x32_bf16 v[164:167], v[88:91], v[52:55], 0\n"
"buffer_load_dwordx4 v[48:51], v1, s[4:7], s98 offen offset:2048 sc1\n"
"v_exp_f32_e32 v124, v124\n"
"v_exp_f32_e32 v125, v125\n"
"v_mfma_f32_16x16x32_bf16 v[168:171], v[72:75], v[52:55], 0\n"
"ds_read_b64_tr_b16 v[78:79], v16 offset:12544\n"
"v_exp_f32_e32 v126, v126\n"
"v_exp_f32_e32 v127, v127\n"
"v_mfma_f32_16x16x32_bf16 v[172:175], v[84:87], v[52:55], 0\n"
"s_waitcnt lgkmcnt(4)\n"
"v_cvt_pk_bf16_f32 v72, v68, v69\n"
"v_cvt_pk_bf16_f32 v73, v70, v71\n"
"v_cvt_pk_bf16_f32 v74, v124, v125\n"
"v_cvt_pk_bf16_f32 v75, v126, v127\n"
"s_cmp_eq_i32 s78, 5\n"
"s_cbranch_scc1 .Lmk_x32s\n"
".Lmk_x32r:\n"
"v_mfma_f32_16x16x32_bf16 v[80:83], v[128:131], v[92:95], v[116:119]\n"
"ds_read_b64_tr_b16 v[68:69], v16 offset:10240\n"
"v_exp_f32_e32 v56, v56\n"
"v_exp_f32_e32 v57, v57\n"
"v_exp_f32_e32 v58, v58\n"
"v_mfma_f32_16x16x32_bf16 v[84:87], v[128:131], v[96:99], v[120:123]\n"
"buffer_load_dwordx4 v[52:55], v1, s[4:7], s98 offen offset:3072 sc1\n"
"v_exp_f32_e32 v59, v59\n"
"v_mfma_f32_16x16x32_bf16 v[124:127], v[140:143], v[92:95], v[116:119]\n"
"ds_read_b64_tr_b16 v[70:71], v255 offset:10496\n"
"v_exp_f32_e32 v136, v136\n"
"v_exp_f32_e32 v137, v137\n"
"v_mfma_f32_16x16x32_bf16 v[128:131], v[140:143], v[96:99], v[120:123]\n"
"s_waitcnt lgkmcnt(2)\n"
"v_exp_f32_e32 v138, v138\n"
"v_exp_f32_e32 v139, v139\n"
"v_cvt_pk_bf16_f32 v140, v56, v57\n"
"v_mfma_f32_16x16x32_bf16 v[148:151], v[88:91], v[152:155], v[148:151]\n"
"s_add_u32 s97, s69, 0x60a000\n"
"v_cvt_pk_bf16_f32 v141, v58, v59\n"
"v_cvt_pk_bf16_f32 v142, v136, v137\n"
"v_mfma_f32_16x16x32_bf16 v[156:159], v[64:67], v[152:155], v[156:159]\n"
"buffer_load_dwordx4 v[56:59], v0, s[4:7], s97 offen sc1\n"
"ds_read_b64_tr_b16 v[136:137], v255 offset:10240\n"
"v_cvt_pk_bf16_f32 v143, v138, v139\n"
"v_exp_f32_e32 v132, v132\n"
"v_exp_f32_e32 v133, v133\n"
"v_mfma_f32_16x16x32_bf16 v[160:163], v[76:79], v[152:155], v[160:163]\n"
"v_exp_f32_e32 v134, v134\n"
"v_exp_f32_e32 v135, v135\n"
"v_mfma_f32_16x16x32_bf16 v[164:167], v[88:91], v[72:75], v[164:167]\n"
"buffer_load_dwordx4 v[60:63], v0, s[4:7], s97 offen offset:1024 sc1\n"
"ds_read_b64_tr_b16 v[138:139], v16 offset:10496\n"
"v_exp_f32_e32 v144, v144\n"
"v_mfma_f32_16x16x32_bf16 v[168:171], v[64:67], v[72:75], v[168:171]\n"
"v_exp_f32_e32 v145, v145\n"
"v_exp_f32_e32 v146, v146\n"
"v_exp_f32_e32 v147, v147\n"
"v_mfma_f32_16x16x32_bf16 v[172:175], v[76:79], v[72:75], v[172:175]\n"
"s_waitcnt vmcnt(12)\n"
"v_cvt_pk_bf16_f32 v72, v132, v133\n"
"v_cvt_pk_bf16_f32 v73, v134, v135\n"
"v_cvt_pk_bf16_f32 v74, v144, v145\n"
"v_cvt_pk_bf16_f32 v75, v146, v147\n"
"s_cmp_lg_u32 s2, 0\n"
"s_cbranch_scc1 .Lmk_s33\n"
"s_barrier\n"
".Lmk_s33:\n"
"ds_read_b128 v[76:79], v15 offset:6144\n"
"ds_read_b128 v[132:135], v254 offset:6400\n"
"s_waitcnt lgkmcnt(2)\n"
"v_mfma_f32_16x16x32_bf16 v[148:151], v[88:91], v[140:143], v[148:151]\n"
"ds_read_b128 v[144:147], v15 offset:4096\n"
"v_exp_f32_e32 v80, v80\n"
"v_exp_f32_e32 v81, v81\n"
"v_exp_f32_e32 v82, v82\n"
"v_mfma_f32_16x16x32_bf16 v[156:159], v[68:71], v[140:143], v[156:159]\n"
"buffer_load_dwordx4 v[64:67], v0, s[4:7], s97 offen offset:2048 sc1\n"
"ds_read_b128 v[152:155], v254 offset:4352\n"
"v_exp_f32_e32 v83, v83\n"
"v_mfma_f32_16x16x32_bf16 v[160:163], v[136:139], v[140:143], v[160:163]\n"
"ds_read_b64_tr_b16 v[140:141], v16 offset:8192\n"
"v_exp_f32_e32 v124, v124\n"
"v_exp_f32_e32 v125, v125\n"
"v_mfma_f32_16x16x32_bf16 v[164:167], v[88:91], v[72:75], v[164:167]\n"
"ds_read_b64_tr_b16 v[142:143], v255 offset:8448\n"
"v_exp_f32_e32 v126, v126\n"
"v_exp_f32_e32 v127, v127\n"
"v_mfma_f32_16x16x32_bf16 v[168:171], v[68:71], v[72:75], v[168:171]\n"
"buffer_load_dwordx4 v[68:71], v0, s[4:7], s97 offen offset:3072 sc1\n"
"v_cvt_pk_bf16_f32 v176, v80, v81\n"
"v_cvt_pk_bf16_f32 v177, v82, v83\n"
"v_cvt_pk_bf16_f32 v178, v124, v125\n"
"v_cvt_pk_bf16_f32 v179, v126, v127\n"
"v_mfma_f32_16x16x32_bf16 v[172:175], v[136:139], v[72:75], v[172:175]\n"
"ds_read_b64_tr_b16 v[124:125], v255 offset:8192\n"
"v_exp_f32_e32 v84, v84\n"
"v_exp_f32_e32 v85, v85\n"
"s_waitcnt lgkmcnt(5)\n"
"v_mfma_f32_16x16x32_bf16 v[136:139], v[76:79], v[92:95], v[116:119]\n"
"ds_read_b64_tr_b16 v[126:127], v16 offset:8448\n"
"v_exp_f32_e32 v86, v86\n"
"v_exp_f32_e32 v87, v87\n"
"v_exp_f32_e32 v128, v128\n"
"v_mfma_f32_16x16x32_bf16 v[180:183], v[76:79], v[96:99], v[120:123]\n"
"buffer_load_dwordx4 v[72:75], v1, s[4:7], s97 offen sc1\n"
"s_waitcnt vmcnt(12)\n"
"v_cmp_ne_u64_e32 vcc, v[246:247], v[18:19]\n"
"v_mfma_f32_16x16x32_bf16 v[184:187], v[132:135], v[92:95], v[116:119]\n"
"v_exp_f32_e32 v129, v129\n"
"v_exp_f32_e32 v130, v130\n"
"v_exp_f32_e32 v131, v131\n"
"v_mfma_f32_16x16x32_bf16 v[188:191], v[132:135], v[96:99], v[120:123]\n"
"s_cbranch_vccnz .Lmk_f4s\n"
".Lmk_f4r:\n"
"v_cvt_pk_bf16_f32 v132, v84, v85\n"
"v_cvt_pk_bf16_f32 v133, v86, v87\n"
"v_cvt_pk_bf16_f32 v134, v128, v129\n"
"v_cvt_pk_bf16_f32 v135, v130, v131\n"
"s_waitcnt lgkmcnt(4)\n"
"v_mfma_f32_16x16x32_bf16 v[128:131], v[144:147], v[92:95], v[116:119]\n"
"ds_read_b128 v[192:195], v15 offset:2048\n"
"v_exp_f32_e32 v136, v136\n"
"v_exp_f32_e32 v137, v137\n"
"v_exp_f32_e32 v138, v138\n"
"v_mfma_f32_16x16x32_bf16 v[196:199], v[144:147], v[96:99], v[120:123]\n"
"buffer_load_dwordx4 v[76:79], v1, s[4:7], s97 offen offset:1024 sc1\n"
"v_exp_f32_e32 v139, v139\n"
"v_mfma_f32_16x16x32_bf16 v[144:147], v[152:155], v[92:95], v[116:119]\n"
"ds_read_b128 v[200:203], v254 offset:2304\n"
"v_exp_f32_e32 v184, v184\n"
"v_exp_f32_e32 v185, v185\n"
"v_mfma_f32_16x16x32_bf16 v[204:207], v[152:155], v[96:99], v[120:123]\n"
"ds_read_b64_tr_b16 v[152:153], v16 offset:6144\n"
"v_exp_f32_e32 v186, v186\n"
"v_exp_f32_e32 v187, v187\n"
"s_waitcnt lgkmcnt(3)\n"
"v_mfma_f32_16x16x32_bf16 v[148:151], v[88:91], v[176:179], v[148:151]\n"
"buffer_load_dwordx4 v[80:83], v1, s[4:7], s97 offen offset:2048 sc1\n"
"v_cvt_pk_bf16_f32 v208, v136, v137\n"
"v_cvt_pk_bf16_f32 v209, v138, v139\n"
"v_cvt_pk_bf16_f32 v210, v184, v185\n"
"v_mfma_f32_16x16x32_bf16 v[156:159], v[140:143], v[176:179], v[156:159]\n"
"ds_read_b64_tr_b16 v[154:155], v255 offset:6400\n"
"v_cvt_pk_bf16_f32 v211, v186, v187\n"
"v_exp_f32_e32 v180, v180\n"
"v_exp_f32_e32 v181, v181\n"
"v_mfma_f32_16x16x32_bf16 v[160:163], v[124:127], v[176:179], v[160:163]\n"
"ds_read_b64_tr_b16 v[136:137], v255 offset:6144\n"
"v_exp_f32_e32 v182, v182\n"
"v_exp_f32_e32 v183, v183\n"
"v_mfma_f32_16x16x32_bf16 v[164:167], v[88:91], v[132:135], v[164:167]\n"
"buffer_load_dwordx4 v[84:87], v1, s[4:7], s97 offen offset:3072 sc1\n"
"v_exp_f32_e32 v188, v188\n"
"v_exp_f32_e32 v189, v189\n"
"v_mfma_f32_16x16x32_bf16 v[168:171], v[140:143], v[132:135], v[168:171]\n"
"ds_read_b64_tr_b16 v[138:139], v16 offset:6400\n"
"v_exp_f32_e32 v190, v190\n"
"v_exp_f32_e32 v191, v191\n"
"v_mfma_f32_16x16x32_bf16 v[172:175], v[124:127], v[132:135], v[172:175]\n"
"s_waitcnt lgkmcnt(4)\n"
"v_cvt_pk_bf16_f32 v124, v180, v181\n"
"v_cvt_pk_bf16_f32 v125, v182, v183\n"
"v_cvt_pk_bf16_f32 v126, v188, v189\n"
"v_cvt_pk_bf16_f32 v127, v190, v191\n"
"v_mfma_f32_16x16x32_bf16 v[132:135], v[192:195], v[92:95], v[116:119]\n"
"ds_read_b128 v[140:143], v15\n"
"v_exp_f32_e32 v128, v128\n"
"v_exp_f32_e32 v129, v129\n"
"v_exp_f32_e32 v130, v130\n"
"v_mfma_f32_16x16x32_bf16 v[176:179], v[192:195], v[96:99], v[120:123]\n"
"s_add_u32 s97, s56, 0x40000\n"
"buffer_load_dwordx4 v[180:183], v0, s[4:7], s97 offen sc1\n"
"v_exp_f32_e32 v131, v131\n"
"v_mfma_f32_16x16x32_bf16 v[184:187], v[200:203], v[92:95], v[116:119]\n"
"ds_read_b128 v[188:191], v254 offset:256\n"
"v_exp_f32_e32 v144, v144\n"
"v_exp_f32_e32 v145, v145\n"
"v_exp_f32_e32 v146, v146\n"
"v_mfma_f32_16x16x32_bf16 v[192:195], v[200:203], v[96:99], v[120:123]\n"
"buffer_load_dwordx4 v[200:203], v0, s[4:7], s97 offen offset:1024 sc1\n"
"ds_read_b64_tr_b16 v[212:213], v16 offset:4096\n"
"v_exp_f32_e32 v147, v147\n"
"s_waitcnt lgkmcnt(3)\n"
"v_mfma_f32_16x16x32_bf16 v[148:151], v[88:91], v[208:211], v[148:151]\n"
"v_cvt_pk_bf16_f32 v216, v128, v129\n"
"v_cvt_pk_bf16_f32 v217, v130, v131\n"
"v_cvt_pk_bf16_f32 v218, v144, v145\n"
"v_cvt_pk_bf16_f32 v219, v146, v147\n"
"v_mfma_f32_16x16x32_bf16 v[156:159], v[152:155], v[208:211], v[156:159]\n"
"buffer_load_dwordx4 v[128:131], v0, s[4:7], s97 offen offset:2048 sc1\n"
"ds_read_b64_tr_b16 v[214:215], v255 offset:4352\n"
"v_exp_f32_e32 v196, v196\n"
"v_exp_f32_e32 v197, v197\n"
"v_mfma_f32_16x16x32_bf16 v[160:163], v[136:139], v[208:211], v[160:163]\n"
"buffer_load_dwordx4 v[144:147], v0, s[4:7], s97 offen offset:3072 sc1\n"
"v_exp_f32_e32 v198, v198\n"
"v_exp_f32_e32 v199, v199\n"
"v_mfma_f32_16x16x32_bf16 v[164:167], v[88:91], v[124:127], v[164:167]\n"
"ds_read_b64_tr_b16 v[208:209], v255 offset:4096\n"
"v_exp_f32_e32 v204, v204\n"
"v_exp_f32_e32 v205, v205\n"
"v_mfma_f32_16x16x32_bf16 v[168:171], v[152:155], v[124:127], v[168:171]\n"
"buffer_load_dwordx4 v[152:155], v1, s[4:7], s97 offen sc1\n"
"ds_read_b64_tr_b16 v[210:211], v16 offset:4352\n"
"v_exp_f32_e32 v206, v206\n"
"v_exp_f32_e32 v207, v207\n"
"v_mfma_f32_16x16x32_bf16 v[172:175], v[136:139], v[124:127], v[172:175]\n"
"s_waitcnt lgkmcnt(4)\n"
"v_cvt_pk_bf16_f32 v124, v196, v197\n"
"v_cvt_pk_bf16_f32 v125, v198, v199\n"
"v_cvt_pk_bf16_f32 v126, v204, v205\n"
"v_cvt_pk_bf16_f32 v127, v206, v207\n"
"v_mfma_f32_16x16x32_bf16 v[136:139], v[140:143], v[92:95], v[116:119]\n"
"ds_read_b64_tr_b16 v[196:197], v16 offset:2048\n"
"ds_read_b64_tr_b16 v[198:199], v255 offset:2304\n"
"buffer_load_dwordx4 v[204:207], v1, s[4:7], s97 offen offset:1024 sc1\n"
"v_exp_f32_e32 v132, v132\n"
"v_exp_f32_e32 v133, v133\n"
"v_mfma_f32_16x16x32_bf16 v[220:223], v[140:143], v[96:99], v[120:123]\n"
"ds_read_b64_tr_b16 v[140:141], v255 offset:2048\n"
"v_exp_f32_e32 v134, v134\n"
"v_exp_f32_e32 v135, v135\n"
"v_mfma_f32_16x16x32_bf16 v[224:227], v[188:191], v[92:95], v[116:119]\n"
"buffer_load_dwordx4 v[116:119], v1, s[4:7], s97 offen offset:2048 sc1\n"
"v_exp_f32_e32 v184, v184\n"
"v_exp_f32_e32 v185, v185\n"
"v_mfma_f32_16x16x32_bf16 v[228:231], v[188:191], v[96:99], v[120:123]\n"
"ds_read_b64_tr_b16 v[142:143], v16 offset:2304\n"
"ds_read_b64_tr_b16 v[120:121], v16 offset:16384\n"
"v_exp_f32_e32 v186, v186\n"
"v_exp_f32_e32 v187, v187\n"
"s_waitcnt lgkmcnt(5)\n"
"v_mfma_f32_16x16x32_bf16 v[148:151], v[88:91], v[216:219], v[148:151]\n"
"v_cvt_pk_bf16_f32 v188, v132, v133\n"
"v_cvt_pk_bf16_f32 v189, v134, v135\n"
"v_cvt_pk_bf16_f32 v190, v184, v185\n"
"v_mfma_f32_16x16x32_bf16 v[156:159], v[212:215], v[216:219], v[156:159]\n"
"buffer_load_dwordx4 v[132:135], v1, s[4:7], s97 offen offset:3072 sc1\n"
"ds_read_b64_tr_b16 v[122:123], v255 offset:16640\n"
"ds_read_b64_tr_b16 v[184:185], v255 offset:16384\n"
"v_cvt_pk_bf16_f32 v191, v186, v187\n"
"v_exp_f32_e32 v176, v176\n"
"v_exp_f32_e32 v177, v177\n"
"v_exp_f32_e32 v178, v178\n"
"v_mfma_f32_16x16x32_bf16 v[160:163], v[208:211], v[216:219], v[160:163]\n"
"s_add_u32 s97, s57, 0x40000\n"
"buffer_load_dwordx4 v[216:219], v0, s[4:7], s97 offen sc1\n"
"ds_read_b64_tr_b16 v[186:187], v16 offset:16640\n"
"v_exp_f32_e32 v179, v179\n"
"v_mfma_f32_16x16x32_bf16 v[164:167], v[88:91], v[124:127], v[164:167]\n"
"v_exp_f32_e32 v192, v192\n"
"v_add_u32_e32 v248, 0x26400, v0\n"
"v_exp_f32_e32 v193, v193\n"
"v_mfma_f32_16x16x32_bf16 v[168:171], v[212:215], v[124:127], v[168:171]\n"
"buffer_load_dwordx4 v[212:215], v0, s[4:7], s97 offen offset:1024 sc1\n"
"ds_read_b128 v[232:235], v248\n"
"v_exp_f32_e32 v194, v194\n"
"v_exp_f32_e32 v195, v195\n"
"v_mfma_f32_16x16x32_bf16 v[172:175], v[208:211], v[124:127], v[172:175]\n"
"ds_read_b128 v[208:211], v248 offset:1024\n"
"v_cvt_pk_bf16_f32 v124, v176, v177\n"
"v_cvt_pk_bf16_f32 v125, v178, v179\n"
"v_cvt_pk_bf16_f32 v126, v192, v193\n"
"v_cvt_pk_bf16_f32 v127, v194, v195\n"
"s_waitcnt lgkmcnt(6)\n"
"v_mfma_f32_16x16x32_bf16 v[148:151], v[88:91], v[188:191], v[148:151]\n"
"ds_read_b64_tr_b16 v[176:177], v16\n"
"buffer_load_dwordx4 v[192:195], v0, s[4:7], s97 offen offset:2048 sc1\n"
"v_cndmask_b32_e64 v136, v100, v136, s[20:21]\n"
"v_cndmask_b32_e64 v137, v101, v137, s[22:23]\n"
"v_cndmask_b32_e64 v138, v102, v138, s[24:25]\n"
"v_cndmask_b32_e64 v139, v103, v139, s[26:27]\n"
"v_cndmask_b32_e64 v224, v108, v224, s[28:29]\n"
"v_cndmask_b32_e64 v225, v109, v225, s[30:31]\n"
"v_cndmask_b32_e64 v226, v110, v226, s[32:33]\n"
"v_cndmask_b32_e64 v227, v111, v227, s[34:35]\n"
"v_exp_f32_e32 v136, v136\n"
"v_mfma_f32_16x16x32_bf16 v[156:159], v[196:199], v[188:191], v[156:159]\n"
"ds_read_b64_tr_b16 v[178:179], v255 offset:256\n"
"buffer_load_dwordx4 v[100:103], v0, s[4:7], s97 offen offset:3072 sc1\n"
"v_exp_f32_e32 v137, v137\n"
"v_exp_f32_e32 v138, v138\n"
"v_exp_f32_e32 v139, v139\n"
"v_exp_f32_e32 v224, v224\n"
"v_mfma_f32_16x16x32_bf16 v[160:163], v[140:143], v[188:191], v[160:163]\n"
"buffer_load_dwordx4 v[188:191], v1, s[4:7], s97 offen sc1\n"
"v_exp_f32_e32 v225, v225\n"
"v_exp_f32_e32 v226, v226\n"
"v_exp_f32_e32 v227, v227\n"
"v_cvt_pk_bf16_f32 v108, v136, v137\n"
"v_cvt_pk_bf16_f32 v109, v138, v139\n"
"v_cvt_pk_bf16_f32 v110, v224, v225\n"
"v_mfma_f32_16x16x32_bf16 v[164:167], v[88:91], v[124:127], v[164:167]\n"
"ds_read_b64_tr_b16 v[136:137], v255\n"
"v_cvt_pk_bf16_f32 v111, v226, v227\n"
"v_cndmask_b32_e64 v220, v104, v220, s[36:37]\n"
"v_cndmask_b32_e64 v221, v105, v221, s[38:39]\n"
"v_cndmask_b32_e64 v222, v106, v222, s[40:41]\n"
"v_cndmask_b32_e64 v223, v107, v223, s[42:43]\n"
"v_cndmask_b32_e64 v228, v112, v228, s[44:45]\n"
"v_cndmask_b32_e64 v229, v113, v229, s[46:47]\n"
"v_cndmask_b32_e64 v230, v114, v230, s[48:49]\n"
"v_cndmask_b32_e64 v231, v115, v231, s[50:51]\n"
"v_mfma_f32_16x16x32_bf16 v[168:171], v[196:199], v[124:127], v[168:171]\n"
"buffer_load_dwordx4 v[104:107], v1, s[4:7], s97 offen offset:1024 sc1\n"
"ds_read_b64_tr_b16 v[138:139], v16 offset:256\n"
"buffer_load_dwordx4 v[112:115], v1, s[4:7], s97 offen offset:2048 sc1\n"
"v_exp_f32_e32 v220, v220\n"
"v_exp_f32_e32 v221, v221\n"
"v_exp_f32_e32 v222, v222\n"
"v_exp_f32_e32 v223, v223\n"
"v_exp_f32_e32 v228, v228\n"
"v_mfma_f32_16x16x32_bf16 v[172:175], v[140:143], v[124:127], v[172:175]\n"
"s_waitcnt lgkmcnt(0)\n"
"v_exp_f32_e32 v229, v229\n"
"v_exp_f32_e32 v230, v230\n"
"v_exp_f32_e32 v231, v231\n"
"v_cvt_pk_bf16_f32 v124, v220, v221\n"
"v_cvt_pk_bf16_f32 v125, v222, v223\n"
"v_cvt_pk_bf16_f32 v126, v228, v229\n"
"v_cvt_pk_bf16_f32 v127, v230, v231\n"
"v_mfma_f32_16x16x32_bf16 v[148:151], v[88:91], v[108:111], v[148:151]\n"
"v_and_b32_e32 v232, v108, v232\n"
"v_and_b32_e32 v233, v109, v233\n"
"v_and_b32_e32 v234, v110, v234\n"
"v_and_b32_e32 v235, v111, v235\n"
"v_mfma_f32_16x16x32_bf16 v[164:167], v[88:91], v[124:127], v[164:167]\n"
"v_xor_b32_e32 v140, v108, v232\n"
"v_xor_b32_e32 v141, v109, v233\n"
"v_xor_b32_e32 v142, v110, v234\n"
"v_xor_b32_e32 v143, v111, v235\n"
"v_mfma_f32_16x16x32_bf16 v[156:159], v[176:179], v[232:235], v[156:159]\n"
"v_and_b32_e32 v208, v124, v208\n"
"v_and_b32_e32 v209, v125, v209\n"
"v_mfma_f32_16x16x32_bf16 v[160:163], v[136:139], v[232:235], v[160:163]\n"
"v_and_b32_e32 v210, v126, v210\n"
"v_and_b32_e32 v211, v127, v211\n"
"v_mfma_f32_16x16x32_bf16 v[156:159], v[120:123], v[140:143], v[156:159]\n"
"v_xor_b32_e32 v108, v124, v208\n"
"v_xor_b32_e32 v109, v125, v209\n"
"v_mfma_f32_16x16x32_bf16 v[160:163], v[184:187], v[140:143], v[160:163]\n"
"v_xor_b32_e32 v110, v126, v210\n"
"v_xor_b32_e32 v111, v127, v211\n"
"v_mfma_f32_16x16x32_bf16 v[168:171], v[176:179], v[208:211], v[168:171]\n"
"buffer_load_dwordx4 v[124:127], v1, s[4:7], s97 offen offset:3072 sc1\n"
"s_add_u32 s97, s58, 0x40000\n"
"buffer_load_dwordx4 v[140:143], v0, s[4:7], s97 offen sc1\n"
"v_mfma_f32_16x16x32_bf16 v[172:175], v[136:139], v[208:211], v[172:175]\n"
"buffer_load_dwordx4 v[136:139], v0, s[4:7], s97 offen offset:1024 sc1\n"
"v_mfma_f32_16x16x32_bf16 v[168:171], v[120:123], v[108:111], v[168:171]\n"
"buffer_load_dwordx4 v[120:123], v0, s[4:7], s97 offen offset:2048 sc1\n"
"v_mfma_f32_16x16x32_bf16 v[172:175], v[184:187], v[108:111], v[172:175]\n"
"buffer_load_dwordx4 v[108:111], v0, s[4:7], s97 offen offset:3072 sc1\n"
"v_max_f32_e32 v248, v148, v164\n"
"v_rcp_f32_e32 v148, v148\n"
"v_rcp_f32_e32 v164, v164\n"
"s_nop 0\n"
"v_max3_f32 v248, v248, v148, v164\n"
"v_cmp_nlt_f32_e32 vcc, v248, v251\n"
"s_cbranch_vccnz .Lmk_b29\n"
".Lmk_k34:\n"
"v_mul_f32_e32 v156, v156, v148\n"
"v_mul_f32_e32 v157, v157, v148\n"
"v_mul_f32_e32 v158, v158, v148\n"
"v_mul_f32_e32 v159, v159, v148\n"
"v_mul_f32_e32 v160, v160, v148\n"
"v_mul_f32_e32 v161, v161, v148\n"
"v_mul_f32_e32 v162, v162, v148\n"
"v_mul_f32_e32 v163, v163, v148\n"
"v_cvt_pk_bf16_f32 v88, v156, v157\n"
"v_cvt_pk_bf16_f32 v89, v158, v159\n"
"v_cvt_pk_bf16_f32 v90, v160, v161\n"
"v_cvt_pk_bf16_f32 v91, v162, v163\n"
"buffer_store_dwordx4 v[88:91], v0, s[4:7], s60 offen sc1\n"
"s_add_u32 s98, s60, 0x8000\n"
"v_mul_f32_e32 v168, v168, v164\n"
"v_mul_f32_e32 v169, v169, v164\n"
"v_mul_f32_e32 v170, v170, v164\n"
"v_mul_f32_e32 v171, v171, v164\n"
"v_mul_f32_e32 v172, v172, v164\n"
"v_mul_f32_e32 v173, v173, v164\n"
"v_mul_f32_e32 v174, v174, v164\n"
"v_mul_f32_e32 v175, v175, v164\n"
"v_cvt_pk_bf16_f32 v88, v168, v169\n"
"v_cvt_pk_bf16_f32 v89, v170, v171\n"
"v_cvt_pk_bf16_f32 v90, v172, v173\n"
"v_cvt_pk_bf16_f32 v91, v174, v175\n"
"buffer_store_dwordx4 v[88:91], v0, s[4:7], s98 offen sc1\n"
"s_waitcnt vmcnt(21)\n"
"; @A G2 b.0\n"
"s_nop 0\n"
"v_mfma_f32_16x16x32_bf16 v[88:91], a[128:131], v[180:183], 0\n"
"v_mfma_f32_16x16x32_bf16 v[92:95], a[132:135], v[180:183], 0\n"
"v_mfma_f32_16x16x32_bf16 v[96:99], a[136:139], v[180:183], 0\n"
"buffer_load_dwordx4 v[148:151], v1, s[4:7], s97 offen sc1\n"
"v_mfma_f32_16x16x32_bf16 v[156:159], a[140:143], v[180:183], 0\n"
"s_waitcnt vmcnt(21)\n"
"v_mfma_f32_16x16x32_bf16 v[88:91], a[144:147], v[200:203], v[88:91]\n"
"v_mfma_f32_16x16x32_bf16 v[92:95], a[148:151], v[200:203], v[92:95]\n"
"v_mfma_f32_16x16x32_bf16 v[96:99], a[152:155], v[200:203], v[96:99]\n"
"buffer_load_dwordx4 v[160:163], v1, s[4:7], s97 offen offset:1024 sc1\n"
"v_mfma_f32_16x16x32_bf16 v[156:159], a[156:159], v[200:203], v[156:159]\n"
"s_waitcnt vmcnt(21)\n"
"v_mfma_f32_16x16x32_bf16 v[88:91], a[160:163], v[128:131], v[88:91]\n"
"v_mfma_f32_16x16x32_bf16 v[92:95], a[164:167], v[128:131], v[92:95]\n"
"v_mfma_f32_16x16x32_bf16 v[96:99], a[168:171], v[128:131], v[96:99]\n"
"buffer_load_dwordx4 v[164:167], v1, s[4:7], s97 offen offset:2048 sc1\n"
"v_mfma_f32_16x16x32_bf16 v[156:159], a[172:175], v[128:131], v[156:159]\n"
"s_waitcnt vmcnt(21)\n"
"v_mfma_f32_16x16x32_bf16 v[88:91], a[176:179], v[144:147], v[88:91]\n"
"v_mfma_f32_16x16x32_bf16 v[92:95], a[180:183], v[144:147], v[92:95]\n"
"v_mfma_f32_16x16x32_bf16 v[96:99], a[184:187], v[144:147], v[96:99]\n"
"buffer_load_dwordx4 v[128:131], v1, s[4:7], s97 offen offset:3072 sc1\n"
"v_mfma_f32_16x16x32_bf16 v[156:159], a[188:191], v[144:147], v[156:159]\n"
"s_waitcnt vmcnt(21)\n"
"v_mfma_f32_16x16x32_bf16 v[88:91], a[192:195], v[152:155], v[88:91]\n"
"s_add_u32 s98, s59, 0x40000\n"
"v_mfma_f32_16x16x32_bf16 v[92:95], a[196:199], v[152:155], v[92:95]\n"
"v_mfma_f32_16x16x32_bf16 v[96:99], a[200:203], v[152:155], v[96:99]\n"
"buffer_load_dwordx4 v[144:147], v0, s[4:7], s98 offen sc1\n"
"v_mfma_f32_16x16x32_bf16 v[156:159], a[204:207], v[152:155], v[156:159]\n"
"s_waitcnt vmcnt(21)\n"
"v_mfma_f32_16x16x32_bf16 v[88:91], a[208:211], v[204:207], v[88:91]\n"
"v_mfma_f32_16x16x32_bf16 v[92:95], a[212:215], v[204:207], v[92:95]\n"
"v_mfma_f32_16x16x32_bf16 v[96:99], a[216:219], v[204:207], v[96:99]\n"
"buffer_load_dwordx4 v[152:155], v0, s[4:7], s98 offen offset:1024 sc1\n"
"v_mfma_f32_16x16x32_bf16 v[156:159], a[220:223], v[204:207], v[156:159]\n"
"s_waitcnt vmcnt(21)\n"
"v_mfma_f32_16x16x32_bf16 v[88:91], a[224:227], v[116:119], v[88:91]\n"
"v_mfma_f32_16x16x32_bf16 v[92:95], a[228:231], v[116:119], v[92:95]\n"
"v_mfma_f32_16x16x32_bf16 v[96:99], a[232:235], v[116:119], v[96:99]\n"
"buffer_load_dwordx4 v[168:171], v0, s[4:7], s98 offen offset:2048 sc1\n"
"v_mfma_f32_16x16x32_bf16 v[156:159], a[236:239], v[116:119], v[156:159]\n"
"s_waitcnt vmcnt(21)\n"
"v_mfma_f32_16x16x32_bf16 v[88:91], a[240:243], v[132:135], v[88:91]\n"
"v_mfma_f32_16x16x32_bf16 v[92:95], a[244:247], v[132:135], v[92:95]\n"
"v_mfma_f32_16x16x32_bf16 v[96:99], a[248:251], v[132:135], v[96:99]\n"
"buffer_load_dwordx4 v[116:119], v0, s[4:7], s98 offen offset:3072 sc1\n"
"v_mfma_f32_16x16x32_bf16 v[156:159], a[252:255], v[132:135], v[156:159]\n"
"s_waitcnt vmcnt(20)\n"
"; @A G2 b.1\n"
"v_mfma_f32_16x16x32_bf16 v[132:135], a[128:131], v[216:219], 0\n"
"s_nop 1\n"
"v_cmp_u_f32_e32 vcc, v88, v88\n"
"v_mfma_f32_16x16x32_bf16 v[172:175], a[132:135], v[216:219], 0\n"
"s_cbranch_vccnz .Lmk_p35s\n"
".Lmk_p35r:\n"
"v_mfma_f32_16x16x32_bf16 v[176:179], a[136:139], v[216:219], 0\n"
"buffer_load_dwordx4 v[180:183], v1, s[4:7], s98 offen sc1\n"
"v_mfma_f32_16x16x32_bf16 v[184:187], a[140:143], v[216:219], 0\n"
"v_mfma_f32_16x16x32_bf16 v[132:135], a[144:147], v[212:215], v[132:135]\n"
"ds_write_b128 v3, v[88:91]\n"
"v_mfma_f32_16x16x32_bf16 v[172:175], a[148:151], v[212:215], v[172:175]\n"
"v_mfma_f32_16x16x32_bf16 v[176:179], a[152:155], v[212:215], v[176:179]\n"
"ds_write_b128 v3, v[92:95] offset:1024\n"
"v_mfma_f32_16x16x32_bf16 v[184:187], a[156:159], v[212:215], v[184:187]\n"
"s_waitcnt vmcnt(19)\n"
"buffer_load_dwordx4 v[88:91], v1, s[4:7], s98 offen offset:1024 sc1\n"
"v_mfma_f32_16x16x32_bf16 v[132:135], a[160:163], v[192:195], v[132:135]\n"
"ds_write_b128 v3, v[96:99] offset:2048\n"
"v_mfma_f32_16x16x32_bf16 v[172:175], a[164:167], v[192:195], v[172:175]\n"
"v_mfma_f32_16x16x32_bf16 v[176:179], a[168:171], v[192:195], v[176:179]\n"
"ds_write_b128 v3, v[156:159] offset:3072\n"
"v_mfma_f32_16x16x32_bf16 v[184:187], a[172:175], v[192:195], v[184:187]\n"
"buffer_load_dwordx4 v[92:95], v1, s[4:7], s98 offen offset:2048 sc1\n"
"v_mfma_f32_16x16x32_bf16 v[132:135], a[176:179], v[100:103], v[132:135]\n"
"v_mfma_f32_16x16x32_bf16 v[172:175], a[180:183], v[100:103], v[172:175]\n"
"ds_read_b128 v[96:99], v2 offset:8192\n"
"ds_read_b128 v[156:159], v2 offset:9216\n"
"v_mfma_f32_16x16x32_bf16 v[176:179], a[184:187], v[100:103], v[176:179]\n"
"v_mfma_f32_16x16x32_bf16 v[184:187], a[188:191], v[100:103], v[184:187]\n"
"s_waitcnt vmcnt(19)\n"
"buffer_load_dwordx4 v[100:103], v1, s[4:7], s98 offen offset:3072 sc1\n"
"v_mfma_f32_16x16x32_bf16 v[132:135], a[192:195], v[188:191], v[132:135]\n"
"v_mfma_f32_16x16x32_bf16 v[172:175], a[196:199], v[188:191], v[172:175]\n"
"v_mfma_f32_16x16x32_bf16 v[176:179], a[200:203], v[188:191], v[176:179]\n"
"s_waitcnt lgkmcnt(0)\n"
"v_lshlrev_b32_e32 v255, 16, v96\n"
"v_and_b32_e32 v254, 0xffff0000, v96\n"
"v_mfma_f32_16x16x32_bf16 v[184:187], a[204:207], v[188:191], v[184:187]\n"
"s_waitcnt lgkmcnt(0)\n"
"s_barrier\n"
"v_mfma_f32_16x16x32_bf16 v[132:135], a[208:211], v[104:107], v[132:135]\n"
"v_lshlrev_b32_e32 v253, 16, v97\n"
"v_and_b32_e32 v252, 0xffff0000, v97\n"
"v_mfma_f32_16x16x32_bf16 v[172:175], a[212:215], v[104:107], v[172:175]\n"
"ds_read_b128 v[188:191], v4\n"
"ds_read_b128 v[192:195], v4 offset:1024\n"
"v_mfma_f32_16x16x32_bf16 v[176:179], a[216:219], v[104:107], v[176:179]\n"
"ds_read_b128 v[196:199], v4 offset:2048\n"
"ds_read_b128 v[200:203], v4 offset:3072\n"
"v_mfma_f32_16x16x32_bf16 v[184:187], a[220:223], v[104:107], v[184:187]\n"
"s_waitcnt vmcnt(18)\n"
"v_lshlrev_b32_e32 v251, 16, v98\n"
"v_and_b32_e32 v250, 0xffff0000, v98\n"
"v_mfma_f32_16x16x32_bf16 v[132:135], a[224:227], v[112:115], v[132:135]\n"
"s_mov_b32 s98, .Lmk_b0-.Lmk_g40\n"
"s_getpc_b64 s[84:85]\n"
".Lmk_g40:\n"
"s_add_u32 s84, s84, s98\n"
"s_addc_u32 s85, s85, 0\n"
"s_and_b32 s84, s84, -16\n"
"v_mfma_f32_16x16x32_bf16 v[172:175], a[228:231], v[112:115], v[172:175]\n"
"s_and_b32 s85, s85, 0xffff\n"
"s_sub_u32 s98, .Lmk_end-.Lmk_g40, s98\n"
"s_min_u32 s86, s98, 0x3000\n"
"s_mov_b32 s87, 0x20000\n"
"v_mfma_f32_16x16x32_bf16 v[176:179], a[232:235], v[112:115], v[176:179]\n"
"s_mov_b32 m0, 0x27800\n"
"s_nop 0\n"
"buffer_load_dwordx4 v0, s[84:87], s99 offen lds\n"
"v_mfma_f32_16x16x32_bf16 v[184:187], a[236:239], v[112:115], v[184:187]\n"
"v_lshlrev_b32_e32 v249, 16, v99\n"
"v_and_b32_e32 v248, 0xffff0000, v99\n"
"v_mfma_f32_16x16x32_bf16 v[132:135], a[240:243], v[124:127], v[132:135]\n"
"v_lshlrev_b32_e32 v247, 16, v156\n"
"v_and_b32_e32 v246, 0xffff0000, v156\n"
"v_mfma_f32_16x16x32_bf16 v[172:175], a[244:247], v[124:127], v[172:175]\n"
"v_lshlrev_b32_e32 v245, 16, v157\n"
"v_and_b32_e32 v244, 0xffff0000, v157\n"
"v_mfma_f32_16x16x32_bf16 v[176:179], a[248:251], v[124:127], v[176:179]\n"
"v_lshlrev_b32_e32 v243, 16, v158\n"
"v_and_b32_e32 v242, 0xffff0000, v158\n"
"v_mfma_f32_16x16x32_bf16 v[184:187], a[252:255], v[124:127], v[184:187]\n"
"s_waitcnt vmcnt(17)\n"
"v_lshlrev_b32_e32 v241, 16, v159\n"
"v_and_b32_e32 v240, 0xffff0000, v159\n"
"; @A G2 b.2\n"
"v_mfma_f32_16x16x32_bf16 v[96:99], a[128:131], v[140:143], 0\n"
"s_waitcnt lgkmcnt(0)\n"
"v_cmp_u_f32_e32 vcc, v132, v132\n"
"v_add_f32_e32 v188, v188, v255\n"
"v_mfma_f32_16x16x32_bf16 v[104:107], a[132:135], v[140:143], 0\n"
"s_cbranch_vccnz .Lmk_p36s\n"
".Lmk_p36r:\n"
"v_add_f32_e32 v189, v189, v254\n"
"v_mfma_f32_16x16x32_bf16 v[112:115], a[136:139], v[140:143], 0\n"
"v_add_f32_e32 v190, v190, v253\n"
"v_add_f32_e32 v191, v191, v252\n"
"v_mfma_f32_16x16x32_bf16 v[124:127], a[140:143], v[140:143], 0\n"
"v_add_f32_e32 v192, v192, v251\n"
"v_add_f32_e32 v193, v193, v250\n"
"v_mfma_f32_16x16x32_bf16 v[96:99], a[144:147], v[136:139], v[96:99]\n"
"ds_write_b128 v3, v[132:135] offset:6144\n"
"v_mfma_f32_16x16x32_bf16 v[104:107], a[148:151], v[136:139], v[104:107]\n"
"v_add_f32_e32 v194, v194, v249\n"
"v_add_f32_e32 v195, v195, v248\n"
"v_mfma_f32_16x16x32_bf16 v[112:115], a[152:155], v[136:139], v[112:115]\n"
"ds_write_b128 v3, v[172:175] offset:7168\n"
"v_mfma_f32_16x16x32_bf16 v[124:127], a[156:159], v[136:139], v[124:127]\n"
"s_waitcnt vmcnt(15)\n"
"v_add_f32_e32 v196, v196, v247\n"
"v_add_f32_e32 v197, v197, v246\n"
"v_mfma_f32_16x16x32_bf16 v[96:99], a[160:163], v[120:123], v[96:99]\n"
"ds_write_b128 v3, v[176:179] offset:8192\n"
"v_mfma_f32_16x16x32_bf16 v[104:107], a[164:167], v[120:123], v[104:107]\n"
"v_add_f32_e32 v198, v198, v245\n"
"v_add_f32_e32 v199, v199, v244\n"
"v_mfma_f32_16x16x32_bf16 v[112:115], a[168:171], v[120:123], v[112:115]\n"
"ds_write_b128 v3, v[184:187] offset:9216\n"
"v_mfma_f32_16x16x32_bf16 v[124:127], a[172:175], v[120:123], v[124:127]\n"
"v_add_f32_e32 v200, v200, v243\n"
"v_add_f32_e32 v201, v201, v242\n"
"v_mfma_f32_16x16x32_bf16 v[96:99], a[176:179], v[108:111], v[96:99]\n"
"s_add_u32 s98, s56, 0\n"
"buffer_load_dwordx4 v[120:123], v0, s[4:7], s98 offen sc1\n"
"v_mfma_f32_16x16x32_bf16 v[104:107], a[180:183], v[108:111], v[104:107]\n"
"v_add_f32_e32 v202, v202, v241\n"
"v_add_f32_e32 v203, v203, v240\n"
"v_mfma_f32_16x16x32_bf16 v[112:115], a[184:187], v[108:111], v[112:115]\n"
"v_mfma_f32_16x16x32_bf16 v[124:127], a[188:191], v[108:111], v[124:127]\n"
"s_waitcnt vmcnt(12)\n"
"buffer_load_dwordx4 v[108:111], v0, s[4:7], s98 offen offset:1024 sc1\n"
"v_mfma_f32_16x16x32_bf16 v[96:99], a[192:195], v[148:151], v[96:99]\n"
"v_mfma_f32_16x16x32_bf16 v[104:107], a[196:199], v[148:151], v[104:107]\n"
"v_mfma_f32_16x16x32_bf16 v[112:115], a[200:203], v[148:151], v[112:115]\n"
"buffer_load_dwordx4 v[132:135], v0, s[4:7], s98 offen offset:2048 sc1\n"
"v_mfma_f32_16x16x32_bf16 v[124:127], a[204:207], v[148:151], v[124:127]\n"
"v_mfma_f32_16x16x32_bf16 v[96:99], a[208:211], v[160:163], v[96:99]\n"
"v_mfma_f32_16x16x32_bf16 v[104:107], a[212:215], v[160:163], v[104:107]\n"
"buffer_load_dwordx4 v[136:139], v0, s[4:7], s98 offen offset:3072 sc1\n"
"v_mfma_f32_16x16x32_bf16 v[112:115], a[216:219], v[160:163], v[112:115]\n"
"v_mfma_f32_16x16x32_bf16 v[124:127], a[220:223], v[160:163], v[124:127]\n"
"s_waitcnt vmcnt(13)\n"
"v_mfma_f32_16x16x32_bf16 v[96:99], a[224:227], v[164:167], v[96:99]\n"
"s_waitcnt vmcnt(11)\n"
"v_max_u32_e32 v255, v144, v145\n"
"v_max3_u32 v255, v255, v146, v147\n"
"v_mfma_f32_16x16x32_bf16 v[104:107], a[228:231], v[164:167], v[104:107]\n"
"s_waitcnt vmcnt(9)\n"
"v_max3_u32 v255, v255, v152, v153\n"
"v_max3_u32 v255, v255, v154, v155\n"
"v_mfma_f32_16x16x32_bf16 v[112:115], a[232:235], v[164:167], v[112:115]\n"
"v_max3_u32 v255, v255, v168, v169\n"
"v_max3_u32 v255, v255, v170, v171\n"
"v_mfma_f32_16x16x32_bf16 v[124:127], a[236:239], v[164:167], v[124:127]\n"
"v_max3_u32 v255, v255, v116, v117\n"
"v_max3_u32 v255, v255, v118, v119\n"
"v_mfma_f32_16x16x32_bf16 v[96:99], a[240:243], v[128:131], v[96:99]\n"
"v_cmp_eq_u32_e32 vcc, -1, v255\n"
"v_mfma_f32_16x16x32_bf16 v[104:107], a[244:247], v[128:131], v[104:107]\n"
"s_cbranch_vccnz .Lmk_q38s\n"
".Lmk_q38r:\n"
"v_mfma_f32_16x16x32_bf16 v[112:115], a[248:251], v[128:131], v[112:115]\n"
"buffer_load_dwordx4 v[140:143], v1, s[4:7], s98 offen sc1\n"
"v_mfma_f32_16x16x32_bf16 v[124:127], a[252:255], v[128:131], v[124:127]\n"
"; @A G2 b.3\n"
"v_mfma_f32_16x16x32_bf16 v[188:191], a[128:131], v[144:147], v[188:191]\n"
"s_nop 0\n"
"v_cmp_u_f32_e32 vcc, v96, v96\n"
"v_mfma_f32_16x16x32_bf16 v[192:195], a[132:135], v[144:147], v[192:195]\n"
"s_cbranch_vccnz .Lmk_p37s\n"
".Lmk_p37r:\n"
"v_mfma_f32_16x16x32_bf16 v[196:199], a[136:139], v[144:147], v[196:199]\n"
"buffer_load_dwordx4 v[128:131], v1, s[4:7], s98 offen offset:1024 sc1\n"
"v_mfma_f32_16x16x32_bf16 v[200:203], a[140:143], v[144:147], v[200:203]\n"
"v_mfma_f32_16x16x32_bf16 v[188:191], a[144:147], v[152:155], v[188:191]\n"
"ds_write_b128 v3, v[96:99] offset:12288\n"
"v_mfma_f32_16x16x32_bf16 v[192:195], a[148:151], v[152:155], v[192:195]\n"
"buffer_load_dwordx4 v[96:99], v1, s[4:7], s98 offen offset:2048 sc1\n"
"v_mfma_f32_16x16x32_bf16 v[196:199], a[152:155], v[152:155], v[196:199]\n"
"ds_write_b128 v3, v[104:107] offset:13312\n"
"v_mfma_f32_16x16x32_bf16 v[200:203], a[156:159], v[152:155], v[200:203]\n"
"v_mfma_f32_16x16x32_bf16 v[188:191], a[160:163], v[168:171], v[188:191]\n"
"s_waitcnt vmcnt(10)\n"
"v_max_u32_e32 v255, v180, v181\n"
"v_max3_u32 v255, v255, v182, v183\n"
"v_mfma_f32_16x16x32_bf16 v[192:195], a[164:167], v[168:171], v[192:195]\n"
"s_waitcnt vmcnt(8)\n"
"v_max3_u32 v255, v255, v88, v89\n"
"v_max3_u32 v255, v255, v90, v91\n"
"v_mfma_f32_16x16x32_bf16 v[196:199], a[168:171], v[168:171], v[196:199]\n"
"v_max3_u32 v255, v255, v92, v93\n"
"v_max3_u32 v255, v255, v94, v95\n"
"v_mfma_f32_16x16x32_bf16 v[200:203], a[172:175], v[168:171], v[200:203]\n"
"v_max3_u32 v255, v255, v100, v101\n"
"v_max3_u32 v255, v255, v102, v103\n"
"v_mfma_f32_16x16x32_bf16 v[188:191], a[176:179], v[116:119], v[188:191]\n"
"v_cmp_eq_u32_e32 vcc, -1, v255\n"
"v_mfma_f32_16x16x32_bf16 v[192:195], a[180:183], v[116:119], v[192:195]\n"
"s_cbranch_vccnz .Lmk_q39s\n"
".Lmk_q39r:\n"
"v_mfma_f32_16x16x32_bf16 v[196:199], a[184:187], v[116:119], v[196:199]\n"
"ds_write_b128 v3, v[112:115] offset:14336\n"
"v_mfma_f32_16x16x32_bf16 v[200:203], a[188:191], v[116:119], v[200:203]\n"
"buffer_load_dwordx4 v[104:107], v1, s[4:7], s98 offen offset:3072 sc1\n"
"v_mfma_f32_16x16x32_bf16 v[188:191], a[192:195], v[180:183], v[188:191]\n"
"ds_write_b128 v3, v[124:127] offset:15360\n"
"v_mfma_f32_16x16x32_bf16 v[192:195], a[196:199], v[180:183], v[192:195]\n"
"s_add_u32 s98, s57, 0\n"
"v_mfma_f32_16x16x32_bf16 v[196:199], a[200:203], v[180:183], v[196:199]\n"
"buffer_load_dwordx4 v[112:115], v0, s[4:7], s98 offen sc1\n"
"v_mfma_f32_16x16x32_bf16 v[200:203], a[204:207], v[180:183], v[200:203]\n"
"s_waitcnt lgkmcnt(0)\n"
"s_barrier\n"
"v_mfma_f32_16x16x32_bf16 v[188:191], a[208:211], v[88:91], v[188:191]\n"
"ds_read_b128 v[116:119], v5\n"
"ds_read_b128 v[124:127], v6\n"
"v_mfma_f32_16x16x32_bf16 v[192:195], a[212:215], v[88:91], v[192:195]\n"
"buffer_load_dwordx4 v[144:147], v0, s[4:7], s98 offen offset:1024 sc1\n"
"v_mfma_f32_16x16x32_bf16 v[196:199], a[216:219], v[88:91], v[196:199]\n"
"ds_read_b128 v[148:151], v5 offset:1024\n"
"ds_read_b128 v[152:155], v6 offset:1024\n"
"v_mfma_f32_16x16x32_bf16 v[200:203], a[220:223], v[88:91], v[200:203]\n"
"ds_read_b128 v[88:91], v5 offset:2048\n"
"ds_read_b128 v[156:159], v6 offset:2048\n"
"v_mfma_f32_16x16x32_bf16 v[188:191], a[224:227], v[92:95], v[188:191]\n"
"buffer_load_dwordx4 v[160:163], v0, s[4:7], s98 offen offset:2048 sc1\n"
"v_mfma_f32_16x16x32_bf16 v[192:195], a[228:231], v[92:95], v[192:195]\n"
"ds_read_b128 v[164:167], v5 offset:3072\n"
"ds_read_b128 v[168:171], v6 offset:3072\n"
"v_mfma_f32_16x16x32_bf16 v[196:199], a[232:235], v[92:95], v[196:199]\n"
"v_mfma_f32_16x16x32_bf16 v[200:203], a[236:239], v[92:95], v[200:203]\n"
"buffer_load_dwordx4 v[92:95], v0, s[4:7], s98 offen offset:3072 sc1\n"
"v_mfma_f32_16x16x32_bf16 v[188:191], a[240:243], v[100:103], v[188:191]\n"
"s_waitcnt lgkmcnt(4)\n"
"v_add_f32_e32 v116, v116, v124\n"
"v_add_f32_e32 v117, v117, v125\n"
"v_mfma_f32_16x16x32_bf16 v[192:195], a[244:247], v[100:103], v[192:195]\n"
"v_add_f32_e32 v118, v118, v126\n"
"v_add_f32_e32 v119, v119, v127\n"
"v_mfma_f32_16x16x32_bf16 v[196:199], a[248:251], v[100:103], v[196:199]\n"
"buffer_load_dwordx4 v[124:127], v1, s[4:7], s98 offen sc1\n"
"v_mfma_f32_16x16x32_bf16 v[200:203], a[252:255], v[100:103], v[200:203]\n"
"s_waitcnt lgkmcnt(0)\n"
"v_add_f32_e32 v148, v148, v152\n"
"v_add_f32_e32 v149, v149, v153\n"
"v_add_f32_e32 v150, v150, v154\n"
"v_add_f32_e32 v151, v151, v155\n"
"v_add_f32_e32 v88, v88, v156\n"
"v_add_f32_e32 v89, v89, v157\n"
"v_add_f32_e32 v90, v90, v158\n"
"v_add_f32_e32 v91, v91, v159\n"
"v_add_f32_e32 v164, v164, v168\n"
"v_add_f32_e32 v165, v165, v169\n"
"v_add_f32_e32 v166, v166, v170\n"
"v_add_f32_e32 v167, v167, v171\n"
"buffer_load_dwordx4 v[152:155], v1, s[4:7], s98 offen offset:1024 sc1\n"
"buffer_load_dwordx4 v[156:159], v1, s[4:7], s98 offen offset:2048 sc1\n"
"buffer_load_dwordx4 v[168:171], v1, s[4:7], s98 offen offset:3072 sc1\n"
"; @A fin b\n"
"s_add_u32 s98, s61, 0x440000\n"
"v_mov_b32_e32 v100, 0x3f803f80\n"
"v_mov_b32_e32 v101, 0x3f803f80\n"
"v_mov_b32_e32 v102, 0x3f803f80\n"
"v_mov_b32_e32 v103, 0x3f803f80\n"
"v_add_f32_e32 v116, v188, v116\n"
"v_add_f32_e32 v117, v189, v117\n"
"v_add_f32_e32 v118, v190, v118\n"
"v_add_f32_e32 v119, v191, v119\n"
"v_add_f32_e32 v148, v192, v148\n"
"v_add_f32_e32 v149, v193, v149\n"
"v_add_f32_e32 v150, v194, v150\n"
"v_add_f32_e32 v151, v195, v151\n"
"v_cvt_pk_bf16_f32 v172, v116, v117\n"
"v_cvt_pk_bf16_f32 v173, v118, v119\n"
"v_cvt_pk_bf16_f32 v174, v148, v149\n"
"v_cvt_pk_bf16_f32 v175, v150, v151\n"
"buffer_store_dwordx4 v[172:175], v0, s[4:7], s98 offen sc1\n"
"ds_write_b128 v2, v[172:175] offset:8192\n"
"s_add_u32 s98, s61, 0x440000\n"
"v_add_f32_e32 v88, v196, v88\n"
"v_add_f32_e32 v89, v197, v89\n"
"v_add_f32_e32 v90, v198, v90\n"
"v_add_f32_e32 v91, v199, v91\n"
"v_add_f32_e32 v164, v200, v164\n"
"v_add_f32_e32 v165, v201, v165\n"
"v_add_f32_e32 v166, v202, v166\n"
"v_add_f32_e32 v167, v203, v167\n"
"v_cvt_pk_bf16_f32 v116, v88, v89\n"
"v_cvt_pk_bf16_f32 v117, v90, v91\n"
"v_cvt_pk_bf16_f32 v118, v164, v165\n"
"v_cvt_pk_bf16_f32 v119, v166, v167\n"
"v_mfma_f32_16x16x32_bf16 v[88:91], v[100:103], v[172:175], 0\n"
"buffer_store_dwordx4 v[116:119], v0, s[4:7], s98 offen offset:1024 sc1\n"
"ds_write_b128 v2, v[116:119] offset:9216\n"
"v_mfma_f32_16x16x32_bf16 v[148:151], v[172:175], v[172:175], 0\n"
"v_mfma_f32_16x16x32_bf16 v[88:91], v[100:103], v[116:119], v[88:91]\n"
"v_mfma_f32_16x16x32_bf16 v[148:151], v[116:119], v[116:119], v[148:151]\n"
"s_mov_b32 s96, 0xaaaaaaaa\n"
"s_mov_b32 s97, 0xaaaaaaaa\n"
"s_nop 5\n"
"v_cndmask_b32_e64 v148, v148, v149, s[96:97]\n"
"v_cndmask_b32_e64 v150, v150, v151, s[96:97]\n"
"s_mov_b32 s96, 0xcccccccc\n"
"s_mov_b32 s97, 0xcccccccc\n"
"v_cndmask_b32_e64 v89, v148, v150, s[96:97]\n"
"s_mov_b32 s96, 0xf0000f\n"
"s_mov_b32 s97, 0xf0000f00\n"
"s_add_u32 s98, s63, 0x44000\n"
"v_cndmask_b32_e64 v88, v88, v89, s[96:97]\n"
"buffer_store_dword v88, v14, s[4:7], s98 offen sc1\n"
"s_waitcnt vmcnt(18)\n"
"; @A G2 a.0\n"
"v_mfma_f32_16x16x32_bf16 v[88:91], a[128:131], v[120:123], 0\n"
"s_add_u32 s98, s58, 0\n"
"buffer_load_dwordx4 v[116:119], v0, s[4:7], s98 offen sc1\n"
"v_mfma_f32_16x16x32_bf16 v[100:103], a[132:135], v[120:123], 0\n"
"v_mfma_f32_16x16x32_bf16 v[148:151], a[136:139], v[120:123], 0\n"
"v_mfma_f32_16x16x32_bf16 v[164:167], a[140:143], v[120:123], 0\n"
"s_waitcnt vmcnt(18)\n"
"v_mfma_f32_16x16x32_bf16 v[88:91], a[144:147], v[108:111], v[88:91]\n"
"buffer_load_dwordx4 v[120:123], v0, s[4:7], s98 offen offset:1024 sc1\n"
"v_mfma_f32_16x16x32_bf16 v[100:103], a[148:151], v[108:111], v[100:103]\n"
"v_mfma_f32_16x16x32_bf16 v[148:151], a[152:155], v[108:111], v[148:151]\n"
"v_mfma_f32_16x16x32_bf16 v[164:167], a[156:159], v[108:111], v[164:167]\n"
"s_waitcnt vmcnt(18)\n"
"v_mfma_f32_16x16x32_bf16 v[88:91], a[160:163], v[132:135], v[88:91]\n"
"buffer_load_dwordx4 v[172:175], v0, s[4:7], s98 offen offset:2048 sc1\n"
"v_mfma_f32_16x16x32_bf16 v[100:103], a[164:167], v[132:135], v[100:103]\n"
"v_mfma_f32_16x16x32_bf16 v[148:151], a[168:171], v[132:135], v[148:151]\n"
"v_mfma_f32_16x16x32_bf16 v[164:167], a[172:175], v[132:135], v[164:167]\n"
"s_waitcnt vmcnt(18)\n"
"v_mfma_f32_16x16x32_bf16 v[88:91], a[176:179], v[136:139], v[88:91]\n"
"buffer_load_dwordx4 v[132:135], v0, s[4:7], s98 offen offset:3072 sc1\n"
"v_mfma_f32_16x16x32_bf16 v[100:103], a[180:183], v[136:139], v[100:103]\n"
"v_mfma_f32_16x16x32_bf16 v[148:151], a[184:187], v[136:139], v[148:151]\n"
"v_mfma_f32_16x16x32_bf16 v[164:167], a[188:191], v[136:139], v[164:167]\n"
"s_waitcnt vmcnt(18)\n"
"v_mfma_f32_16x16x32_bf16 v[88:91], a[192:195], v[140:143], v[88:91]\n"
"buffer_load_dwordx4 v[136:139], v1, s[4:7], s98 offen sc1\n"
"v_mfma_f32_16x16x32_bf16 v[100:103], a[196:199], v[140:143], v[100:103]\n"
"v_mfma_f32_16x16x32_bf16 v[148:151], a[200:203], v[140:143], v[148:151]\n"
"v_mfma_f32_16x16x32_bf16 v[164:167], a[204:207], v[140:143], v[164:167]\n"
"s_waitcnt vmcnt(18)\n"
"v_mfma_f32_16x16x32_bf16 v[88:91], a[208:211], v[128:131], v[88:91]\n"
"buffer_load_dwordx4 v[140:143], v1, s[4:7], s98 offen offset:1024 sc1\n"
"v_mfma_f32_16x16x32_bf16 v[100:103], a[212:215], v[128:131], v[100:103]\n"
"v_mfma_f32_16x16x32_bf16 v[148:151], a[216:219], v[128:131], v[148:151]\n"
"v_mfma_f32_16x16x32_bf16 v[164:167], a[220:223], v[128:131], v[164:167]\n"
"s_waitcnt vmcnt(18)\n"
"v_mfma_f32_16x16x32_bf16 v[88:91], a[224:227], v[96:99], v[88:91]\n"
"buffer_load_dwordx4 v[128:131], v1, s[4:7], s98 offen offset:2048 sc1\n"
"v_mfma_f32_16x16x32_bf16 v[100:103], a[228:231], v[96:99], v[100:103]\n"
"v_mfma_f32_16x16x32_bf16 v[148:151], a[232:235], v[96:99], v[148:151]\n"
"v_mfma_f32_16x16x32_bf16 v[164:167], a[236:239], v[96:99], v[164:167]\n"
"s_waitcnt vmcnt(18)\n"
"v_mfma_f32_16x16x32_bf16 v[88:91], a[240:243], v[104:107], v[88:91]\n"
"buffer_load_dwordx4 v[176:179], v1, s[4:7], s98 offen offset:3072 sc1\n"
"v_mfma_f32_16x16x32_bf16 v[100:103], a[244:247], v[104:107], v[100:103]\n"
"v_mfma_f32_16x16x32_bf16 v[148:151], a[248:251], v[104:107], v[148:151]\n"
"s_add_u32 s98, s59, 0\n"
"v_mfma_f32_16x16x32_bf16 v[164:167], a[252:255], v[104:107], v[164:167]\n"
"s_waitcnt vmcnt(17)\n"
"; @A G2 a.1\n"
"v_mfma_f32_16x16x32_bf16 v[108:111], a[128:131], v[112:115], 0\n"
"s_nop 0\n"
"v_cmp_u_f32_e32 vcc, v88, v88\n"
"v_mfma_f32_16x16x32_bf16 v[180:183], a[132:135], v[112:115], 0\n"
"s_cbranch_vccnz .Lmk_p41s\n"
".Lmk_p41r:\n"
"v_mfma_f32_16x16x32_bf16 v[184:187], a[136:139], v[112:115], 0\n"
"buffer_load_dwordx4 v[188:191], v0, s[4:7], s98 offen sc1\n"
"v_mfma_f32_16x16x32_bf16 v[192:195], a[140:143], v[112:115], 0\n"
"v_mfma_f32_16x16x32_bf16 v[108:111], a[144:147], v[144:147], v[108:111]\n"
"ds_write_b128 v3, v[88:91]\n"
"v_mfma_f32_16x16x32_bf16 v[180:183], a[148:151], v[144:147], v[180:183]\n"
"v_mfma_f32_16x16x32_bf16 v[184:187], a[152:155], v[144:147], v[184:187]\n"
"ds_write_b128 v3, v[100:103] offset:1024\n"
"v_mfma_f32_16x16x32_bf16 v[192:195], a[156:159], v[144:147], v[192:195]\n"
"s_waitcnt vmcnt(16)\n"
"buffer_load_dwordx4 v[144:147], v0, s[4:7], s98 offen offset:1024 sc1\n"
"v_mfma_f32_16x16x32_bf16 v[108:111], a[160:163], v[160:163], v[108:111]\n"
"ds_write_b128 v3, v[148:151] offset:2048\n"
"v_mfma_f32_16x16x32_bf16 v[180:183], a[164:167], v[160:163], v[180:183]\n"
"s_add_u32 s97, s75, 0x10000\n"
"buffer_load_dwordx2 v[254:255], v13, s[4:7], s97 offen sc1\n"
"v_mfma_f32_16x16x32_bf16 v[184:187], a[168:171], v[160:163], v[184:187]\n"
"ds_write_b128 v3, v[164:167] offset:3072\n"
"v_mfma_f32_16x16x32_bf16 v[192:195], a[172:175], v[160:163], v[192:195]\n"
"buffer_load_dwordx4 v[148:151], v0, s[4:7], s98 offen offset:2048 sc1\n"
"v_mfma_f32_16x16x32_bf16 v[108:111], a[176:179], v[92:95], v[108:111]\n"
"v_mfma_f32_16x16x32_bf16 v[180:183], a[180:183], v[92:95], v[180:183]\n"
"ds_read_b128 v[104:107], v2\n"
"ds_read_b128 v[112:115], v2 offset:1024\n"
"v_mfma_f32_16x16x32_bf16 v[184:187], a[184:187], v[92:95], v[184:187]\n"
"v_mfma_f32_16x16x32_bf16 v[192:195], a[188:191], v[92:95], v[192:195]\n"
"s_waitcnt vmcnt(17)\n"
"buffer_load_dwordx4 v[160:163], v0, s[4:7], s98 offen offset:3072 sc1\n"
"v_mfma_f32_16x16x32_bf16 v[108:111], a[192:195], v[124:127], v[108:111]\n"
"v_mfma_f32_16x16x32_bf16 v[180:183], a[196:199], v[124:127], v[180:183]\n"
"v_mfma_f32_16x16x32_bf16 v[184:187], a[200:203], v[124:127], v[184:187]\n"
"s_waitcnt lgkmcnt(0)\n"
"v_lshlrev_b32_e32 v253, 16, v104\n"
"v_and_b32_e32 v252, 0xffff0000, v104\n"
"v_mfma_f32_16x16x32_bf16 v[192:195], a[204:207], v[124:127], v[192:195]\n"
"s_waitcnt lgkmcnt(0)\n"
"s_barrier\n"
"v_mfma_f32_16x16x32_bf16 v[108:111], a[208:211], v[152:155], v[108:111]\n"
"buffer_load_dwordx4 v[164:167], v1, s[4:7], s98 offen sc1\n"
"v_mfma_f32_16x16x32_bf16 v[180:183], a[212:215], v[152:155], v[180:183]\n"
"ds_read_b128 v[88:91], v4\n"
"ds_read_b128 v[92:95], v4 offset:1024\n"
"v_mfma_f32_16x16x32_bf16 v[184:187], a[216:219], v[152:155], v[184:187]\n"
"ds_read_b128 v[96:99], v4 offset:2048\n"
"ds_read_b128 v[100:103], v4 offset:3072\n"
"v_mfma_f32_16x16x32_bf16 v[192:195], a[220:223], v[152:155], v[192:195]\n"
"s_waitcnt vmcnt(17)\n"
"v_lshlrev_b32_e32 v251, 16, v105\n"
"v_and_b32_e32 v250, 0xffff0000, v105\n"
"v_mfma_f32_16x16x32_bf16 v[108:111], a[224:227], v[156:159], v[108:111]\n"
"buffer_load_dwordx4 v[152:155], v1, s[4:7], s98 offen offset:1024 sc1\n"
"v_mfma_f32_16x16x32_bf16 v[180:183], a[228:231], v[156:159], v[180:183]\n"
"v_lshlrev_b32_e32 v249, 16, v106\n"
"v_and_b32_e32 v248, 0xffff0000, v106\n"
"v_mfma_f32_16x16x32_bf16 v[184:187], a[232:235], v[156:159], v[184:187]\n"
"v_lshlrev_b32_e32 v247, 16, v107\n"
"v_and_b32_e32 v246, 0xffff0000, v107\n"
"v_mfma_f32_16x16x32_bf16 v[192:195], a[236:239], v[156:159], v[192:195]\n"
"v_lshlrev_b32_e32 v245, 16, v112\n"
"v_and_b32_e32 v244, 0xffff0000, v112\n"
"v_mfma_f32_16x16x32_bf16 v[108:111], a[240:243], v[168:171], v[108:111]\n"
"buffer_load_dwordx4 v[196:199], v1, s[4:7], s98 offen offset:2048 sc1\n"
"v_mfma_f32_16x16x32_bf16 v[180:183], a[244:247], v[168:171], v[180:183]\n"
"v_lshlrev_b32_e32 v243, 16, v113\n"
"v_and_b32_e32 v242, 0xffff0000, v113\n"
"v_mfma_f32_16x16x32_bf16 v[184:187], a[248:251], v[168:171], v[184:187]\n"
"v_lshlrev_b32_e32 v241, 16, v114\n"
"v_and_b32_e32 v240, 0xffff0000, v114\n"
"v_mfma_f32_16x16x32_bf16 v[192:195], a[252:255], v[168:171], v[192:195]\n"
"s_waitcnt vmcnt(14)\n"
"v_lshlrev_b32_e32 v239, 16, v115\n"
"v_and_b32_e32 v238, 0xffff0000, v115\n"
"; @A G2 a.2\n"
"v_mfma_f32_16x16x32_bf16 v[156:159], a[128:131], v[116:119], 0\n"
"s_add_u32 s96, s52, 0x440000\n"
"v_cmp_u_f32_e32 vcc, v108, v108\n"
"s_waitcnt lgkmcnt(0)\n"
"v_mfma_f32_16x16x32_bf16 v[168:171], a[132:135], v[116:119], 0\n"
"s_cbranch_vccnz .Lmk_p42s\n"
".Lmk_p42r:\n"
"v_add_f32_e32 v88, v88, v253\n"
"v_mfma_f32_16x16x32_bf16 v[200:203], a[136:139], v[116:119], 0\n"
"buffer_load_dwordx4 v[204:207], v1, s[4:7], s98 offen offset:3072 sc1\n"
"v_mfma_f32_16x16x32_bf16 v[208:211], a[140:143], v[116:119], 0\n"
"buffer_load_dwordx4 v[104:107], v0, s[4:7], s96 offen sc1\n"
"v_mfma_f32_16x16x32_bf16 v[156:159], a[144:147], v[120:123], v[156:159]\n"
"ds_write_b128 v3, v[108:111] offset:6144\n"
"v_mfma_f32_16x16x32_bf16 v[168:171], a[148:151], v[120:123], v[168:171]\n"
"v_add_f32_e32 v89, v89, v252\n"
"v_add_f32_e32 v90, v90, v251\n"
"v_mfma_f32_16x16x32_bf16 v[200:203], a[152:155], v[120:123], v[200:203]\n"
"ds_write_b128 v3, v[180:183] offset:7168\n"
"v_mfma_f32_16x16x32_bf16 v[208:211], a[156:159], v[120:123], v[208:211]\n"
"s_waitcnt vmcnt(14)\n"
"buffer_load_dwordx4 v[108:111], v0, s[4:7], s96 offen offset:1024 sc1\n"
"v_mfma_f32_16x16x32_bf16 v[156:159], a[160:163], v[172:175], v[156:159]\n"
"ds_write_b128 v3, v[184:187] offset:8192\n"
"v_mfma_f32_16x16x32_bf16 v[168:171], a[164:167], v[172:175], v[168:171]\n"
"v_add_f32_e32 v91, v91, v250\n"
"v_add_f32_e32 v92, v92, v249\n"
"v_mfma_f32_16x16x32_bf16 v[200:203], a[168:171], v[172:175], v[200:203]\n"
"ds_write_b128 v3, v[192:195] offset:9216\n"
"v_mfma_f32_16x16x32_bf16 v[208:211], a[172:175], v[172:175], v[208:211]\n"
"buffer_load_dwordx4 v[112:115], v0, s[4:7], s96 offen offset:2048 sc1\n"
"v_mfma_f32_16x16x32_bf16 v[156:159], a[176:179], v[132:135], v[156:159]\n"
"v_add_f32_e32 v93, v93, v248\n"
"v_add_f32_e32 v94, v94, v247\n"
"v_mfma_f32_16x16x32_bf16 v[168:171], a[180:183], v[132:135], v[168:171]\n"
"v_add_f32_e32 v95, v95, v246\n"
"v_add_f32_e32 v96, v96, v245\n"
"v_mfma_f32_16x16x32_bf16 v[200:203], a[184:187], v[132:135], v[200:203]\n"
"v_add_f32_e32 v97, v97, v244\n"
"v_add_f32_e32 v98, v98, v243\n"
"v_mfma_f32_16x16x32_bf16 v[208:211], a[188:191], v[132:135], v[208:211]\n"
"s_waitcnt vmcnt(14)\n"
"buffer_load_dwordx4 v[116:119], v0, s[4:7], s96 offen offset:3072 sc1\n"
"v_mfma_f32_16x16x32_bf16 v[156:159], a[192:195], v[136:139], v[156:159]\n"
"v_add_f32_e32 v99, v99, v242\n"
"v_add_f32_e32 v100, v100, v241\n"
"v_mfma_f32_16x16x32_bf16 v[168:171], a[196:199], v[136:139], v[168:171]\n"
"v_add_f32_e32 v101, v101, v240\n"
"v_add_f32_e32 v102, v102, v239\n"
"v_mfma_f32_16x16x32_bf16 v[200:203], a[200:203], v[136:139], v[200:203]\n"
"v_add_f32_e32 v103, v103, v238\n"
"v_mfma_f32_16x16x32_bf16 v[208:211], a[204:207], v[136:139], v[208:211]\n"
"buffer_load_dwordx4 v[120:123], v1, s[4:7], s96 offen sc1\n"
"v_mfma_f32_16x16x32_bf16 v[156:159], a[208:211], v[140:143], v[156:159]\n"
"v_mfma_f32_16x16x32_bf16 v[168:171], a[212:215], v[140:143], v[168:171]\n"
"v_mfma_f32_16x16x32_bf16 v[200:203], a[216:219], v[140:143], v[200:203]\n"
"v_mfma_f32_16x16x32_bf16 v[208:211], a[220:223], v[140:143], v[208:211]\n"
"s_waitcnt vmcnt(14)\n"
"buffer_load_dwordx4 v[124:127], v1, s[4:7], s96 offen offset:1024 sc1\n"
"v_mfma_f32_16x16x32_bf16 v[156:159], a[224:227], v[128:131], v[156:159]\n"
"s_waitcnt vmcnt(13)\n"
"v_max_u32_e32 v253, v188, v189\n"
"v_max3_u32 v253, v253, v190, v191\n"
"v_mfma_f32_16x16x32_bf16 v[168:171], a[228:231], v[128:131], v[168:171]\n"
"s_waitcnt vmcnt(10)\n"
"v_max3_u32 v253, v253, v144, v145\n"
"v_max3_u32 v253, v253, v146, v147\n"
"v_mfma_f32_16x16x32_bf16 v[200:203], a[232:235], v[128:131], v[200:203]\n"
"v_max3_u32 v253, v253, v148, v149\n"
"v_max3_u32 v253, v253, v150, v151\n"
"v_mfma_f32_16x16x32_bf16 v[208:211], a[236:239], v[128:131], v[208:211]\n"
"v_max3_u32 v253, v253, v160, v161\n"
"v_max3_u32 v253, v253, v162, v163\n"
"v_mfma_f32_16x16x32_bf16 v[156:159], a[240:243], v[176:179], v[156:159]\n"
"v_cmp_eq_u32_e32 vcc, -1, v253\n"
"v_mfma_f32_16x16x32_bf16 v[168:171], a[244:247], v[176:179], v[168:171]\n"
"s_cbranch_vccnz .Lmk_q44s\n"
".Lmk_q44r:\n"
"v_mfma_f32_16x16x32_bf16 v[200:203], a[248:251], v[176:179], v[200:203]\n"
"buffer_load_dwordx4 v[128:131], v1, s[4:7], s96 offen offset:2048 sc1\n"
"v_mfma_f32_16x16x32_bf16 v[208:211], a[252:255], v[176:179], v[208:211]\n"
"; @A G2 a.3\n"
"v_mfma_f32_16x16x32_bf16 v[88:91], a[128:131], v[188:191], v[88:91]\n"
"s_nop 0\n"
"v_cmp_u_f32_e32 vcc, v156, v156\n"
"v_mfma_f32_16x16x32_bf16 v[92:95], a[132:135], v[188:191], v[92:95]\n"
"s_cbranch_vccnz .Lmk_p43s\n"
".Lmk_p43r:\n"
"v_mfma_f32_16x16x32_bf16 v[96:99], a[136:139], v[188:191], v[96:99]\n"
"buffer_load_dwordx4 v[132:135], v1, s[4:7], s96 offen offset:3072 sc1\n"
"v_mfma_f32_16x16x32_bf16 v[100:103], a[140:143], v[188:191], v[100:103]\n"
"v_mfma_f32_16x16x32_bf16 v[88:91], a[144:147], v[144:147], v[88:91]\n"
"ds_write_b128 v3, v[156:159] offset:12288\n"
"v_mfma_f32_16x16x32_bf16 v[92:95], a[148:151], v[144:147], v[92:95]\n"
"v_cmp_ne_u64_e32 vcc, v[254:255], v[18:19]\n"
"v_mfma_f32_16x16x32_bf16 v[96:99], a[152:155], v[144:147], v[96:99]\n"
"ds_write_b128 v3, v[168:171] offset:13312\n"
"v_mfma_f32_16x16x32_bf16 v[100:103], a[156:159], v[144:147], v[100:103]\n"
"s_cbranch_vccnz .Lmk_f13s\n"
".Lmk_f13r:\n"
"s_add_u32 s98, s53, 0x440000\n"
"v_mfma_f32_16x16x32_bf16 v[88:91], a[160:163], v[148:151], v[88:91]\n"
"s_waitcnt vmcnt(10)\n"
"v_max_u32_e32 v255, v164, v165\n"
"v_max3_u32 v255, v255, v166, v167\n"
"v_mfma_f32_16x16x32_bf16 v[92:95], a[164:167], v[148:151], v[92:95]\n"
"s_waitcnt vmcnt(8)\n"
"v_max3_u32 v255, v255, v152, v153\n"
"v_max3_u32 v255, v255, v154, v155\n"
"v_mfma_f32_16x16x32_bf16 v[96:99], a[168:171], v[148:151], v[96:99]\n"
"v_max3_u32 v255, v255, v196, v197\n"
"v_max3_u32 v255, v255, v198, v199\n"
"v_mfma_f32_16x16x32_bf16 v[100:103], a[172:175], v[148:151], v[100:103]\n"
"v_max3_u32 v255, v255, v204, v205\n"
"v_max3_u32 v255, v255, v206, v207\n"
"v_mfma_f32_16x16x32_bf16 v[88:91], a[176:179], v[160:163], v[88:91]\n"
"v_cmp_eq_u32_e32 vcc, -1, v255\n"
"v_mfma_f32_16x16x32_bf16 v[92:95], a[180:183], v[160:163], v[92:95]\n"
"s_cbranch_vccnz .Lmk_q45s\n"
".Lmk_q45r:\n"
"v_mfma_f32_16x16x32_bf16 v[96:99], a[184:187], v[160:163], v[96:99]\n"
"ds_write_b128 v3, v[200:203] offset:14336\n"
"v_mfma_f32_16x16x32_bf16 v[100:103], a[188:191], v[160:163], v[100:103]\n"
"buffer_load_dwordx4 v[136:139], v0, s[4:7], s98 offen sc1\n"
"v_mfma_f32_16x16x32_bf16 v[88:91], a[192:195], v[164:167], v[88:91]\n"
"ds_write_b128 v3, v[208:211] offset:15360\n"
"v_mfma_f32_16x16x32_bf16 v[92:95], a[196:199], v[164:167], v[92:95]\n"
"v_mfma_f32_16x16x32_bf16 v[96:99], a[200:203], v[164:167], v[96:99]\n"
"v_mfma_f32_16x16x32_bf16 v[100:103], a[204:207], v[164:167], v[100:103]\n"
"s_waitcnt lgkmcnt(0)\n"
"s_barrier\n"
"v_mfma_f32_16x16x32_bf16 v[88:91], a[208:211], v[152:155], v[88:91]\n"
"buffer_load_dwordx4 v[140:143], v0, s[4:7], s98 offen offset:1024 sc1\n"
"v_mfma_f32_16x16x32_bf16 v[92:95], a[212:215], v[152:155], v[92:95]\n"
"ds_read_b128 v[144:147], v5\n"
"ds_read_b128 v[168:171], v6\n"
"v_mfma_f32_16x16x32_bf16 v[96:99], a[216:219], v[152:155], v[96:99]\n"
"ds_read_b128 v[148:151], v5 offset:1024\n"
"ds_read_b128 v[172:175], v6 offset:1024\n"
"v_mfma_f32_16x16x32_bf16 v[100:103], a[220:223], v[152:155], v[100:103]\n"
"ds_read_b128 v[152:155], v5 offset:2048\n"
"ds_read_b128 v[176:179], v6 offset:2048\n"
"v_mfma_f32_16x16x32_bf16 v[88:91], a[224:227], v[196:199], v[88:91]\n"
"buffer_load_dwordx4 v[156:159], v0, s[4:7], s98 offen offset:2048 sc1\n"
"v_mfma_f32_16x16x32_bf16 v[92:95], a[228:231], v[196:199], v[92:95]\n"
"ds_read_b128 v[160:163], v5 offset:3072\n"
"ds_read_b128 v[180:183], v6 offset:3072\n"
"v_mfma_f32_16x16x32_bf16 v[96:99], a[232:235], v[196:199], v[96:99]\n"
"v_mfma_f32_16x16x32_bf16 v[100:103], a[236:239], v[196:199], v[100:103]\n"
"s_waitcnt lgkmcnt(4)\n"
"v_add_f32_e32 v144, v144, v168\n"
"v_add_f32_e32 v145, v145, v169\n"
"v_mfma_f32_16x16x32_bf16 v[88:91], a[240:243], v[204:207], v[88:91]\n"
"buffer_load_dwordx4 v[164:167], v0, s[4:7], s98 offen offset:3072 sc1\n"
"v_mfma_f32_16x16x32_bf16 v[92:95], a[244:247], v[204:207], v[92:95]\n"
"v_add_f32_e32 v146, v146, v170\n"
"v_add_f32_e32 v147, v147, v171\n"
"v_mfma_f32_16x16x32_bf16 v[96:99], a[248:251], v[204:207], v[96:99]\n"
"s_waitcnt lgkmcnt(0)\n"
"v_add_f32_e32 v148, v148, v172\n"
"v_add_f32_e32 v149, v149, v173\n"
"v_mfma_f32_16x16x32_bf16 v[100:103], a[252:255], v[204:207], v[100:103]\n"
"buffer_load_dwordx4 v[168:171], v1, s[4:7], s98 offen sc1\n"
"v_add_f32_e32 v150, v150, v174\n"
"v_add_f32_e32 v151, v151, v175\n"
"v_add_f32_e32 v152, v152, v176\n"
"v_add_f32_e32 v153, v153, v177\n"
"v_add_f32_e32 v154, v154, v178\n"
"v_add_f32_e32 v155, v155, v179\n"
"v_add_f32_e32 v160, v160, v180\n"
"v_add_f32_e32 v161, v161, v181\n"
"v_add_f32_e32 v162, v162, v182\n"
"v_add_f32_e32 v163, v163, v183\n"
"buffer_load_dwordx4 v[172:175], v1, s[4:7], s98 offen offset:1024 sc1\n"
"buffer_load_dwordx4 v[176:179], v1, s[4:7], s98 offen offset:2048 sc1\n"
"buffer_load_dwordx4 v[180:183], v1, s[4:7], s98 offen offset:3072 sc1\n"
".Lmk_loop:\n"
".Lmk_b0:\n"
"s_waitcnt vmcnt(15)\n"
"; @B G1 0.0\n"
"v_mfma_f32_16x16x32_bf16 v[184:187], a[0:3], v[104:107], 0\n"
"v_mfma_f32_16x16x32_bf16 v[188:191], a[4:7], v[104:107], 0\n"
"s_add_u32 s98, s62, 0x44000\n"
"v_mfma_f32_16x16x32_bf16 v[192:195], a[8:11], v[104:107], 0\n"
"v_mfma_f32_16x16x32_bf16 v[196:199], a[12:15], v[104:107], 0\n"
"v_mfma_f32_16x16x32_bf16 v[200:203], a[16:19], v[104:107], 0\n"
"s_add_u32 s97, s54, 0x440000\n"
"buffer_load_dwordx4 v[204:207], v0, s[4:7], s97 offen sc1\n"
"v_mfma_f32_16x16x32_bf16 v[208:211], a[20:23], v[104:107], 0\n"
"s_waitcnt vmcnt(15)\n"
"buffer_load_dword v255, v10, s[4:7], s98 offen sc1\n"
"v_mfma_f32_16x16x32_bf16 v[184:187], a[24:27], v[108:111], v[184:187]\n"
"v_mov_b32_e32 v104, 0x3f803f80\n"
"v_mov_b32_e32 v105, 0x3f803f80\n"
"v_mfma_f32_16x16x32_bf16 v[188:191], a[28:31], v[108:111], v[188:191]\n"
"v_mov_b32_e32 v106, 0x3f803f80\n"
"v_mov_b32_e32 v107, 0x3f803f80\n"
"v_mfma_f32_16x16x32_bf16 v[192:195], a[32:35], v[108:111], v[192:195]\n"
"buffer_load_dword v254, v10, s[4:7], s98 offen offset:256 sc1\n"
"v_mfma_f32_16x16x32_bf16 v[196:199], a[36:39], v[108:111], v[196:199]\n"
"v_add_f32_e32 v144, v88, v144\n"
"v_add_f32_e32 v145, v89, v145\n"
"v_mfma_f32_16x16x32_bf16 v[200:203], a[40:43], v[108:111], v[200:203]\n"
"buffer_load_dwordx4 v[212:215], v0, s[4:7], s97 offen offset:1024 sc1\n"
"v_mfma_f32_16x16x32_bf16 v[208:211], a[44:47], v[108:111], v[208:211]\n"
"s_waitcnt vmcnt(17)\n"
"buffer_load_dword v253, v10, s[4:7], s98 offen offset:512 sc1\n"
"v_mfma_f32_16x16x32_bf16 v[184:187], a[48:51], v[112:115], v[184:187]\n"
"v_add_f32_e32 v146, v90, v146\n"
"v_add_f32_e32 v147, v91, v147\n"
"v_mfma_f32_16x16x32_bf16 v[188:191], a[52:55], v[112:115], v[188:191]\n"
"v_add_f32_e32 v148, v92, v148\n"
"v_add_f32_e32 v149, v93, v149\n"
"v_mfma_f32_16x16x32_bf16 v[192:195], a[56:59], v[112:115], v[192:195]\n"
"buffer_load_dword v252, v10, s[4:7], s98 offen offset:768 sc1\n"
"v_mfma_f32_16x16x32_bf16 v[196:199], a[60:63], v[112:115], v[196:199]\n"
"v_add_f32_e32 v150, v94, v150\n"
"v_add_f32_e32 v151, v95, v151\n"
"v_mfma_f32_16x16x32_bf16 v[200:203], a[64:67], v[112:115], v[200:203]\n"
"buffer_load_dwordx4 v[88:91], v0, s[4:7], s97 offen offset:2048 sc1\n"
"v_mfma_f32_16x16x32_bf16 v[208:211], a[68:71], v[112:115], v[208:211]\n"
"s_waitcnt vmcnt(19)\n"
"buffer_load_dword v251, v10, s[4:7], s98 offen offset:1024 sc1\n"
"v_mfma_f32_16x16x32_bf16 v[184:187], a[72:75], v[116:119], v[184:187]\n"
"v_cvt_pk_bf16_f32 v92, v144, v145\n"
"v_cvt_pk_bf16_f32 v93, v146, v147\n"
"v_mfma_f32_16x16x32_bf16 v[188:191], a[76:79], v[116:119], v[188:191]\n"
"s_add_u32 s96, s61, 0x400000\n"
"v_cvt_pk_bf16_f32 v94, v148, v149\n"
"v_cvt_pk_bf16_f32 v95, v150, v151\n"
"v_mfma_f32_16x16x32_bf16 v[192:195], a[80:83], v[116:119], v[192:195]\n"
"buffer_load_dword v250, v10, s[4:7], s98 offen offset:1280 sc1\n"
"v_mfma_f32_16x16x32_bf16 v[196:199], a[84:87], v[116:119], v[196:199]\n"
"buffer_store_dwordx4 v[92:95], v0, s[4:7], s96 offen sc1\n"
"v_mfma_f32_16x16x32_bf16 v[200:203], a[88:91], v[116:119], v[200:203]\n"
"buffer_load_dwordx4 v[108:111], v0, s[4:7], s97 offen offset:3072 sc1\n"
"v_mfma_f32_16x16x32_bf16 v[208:211], a[92:95], v[116:119], v[208:211]\n"
"s_waitcnt vmcnt(22)\n"
"buffer_load_dword v249, v10, s[4:7], s98 offen offset:1536 sc1\n"
"v_mfma_f32_16x16x32_bf16 v[184:187], a[96:99], v[120:123], v[184:187]\n"
"ds_write_b128 v2, v[92:95]\n"
"v_mfma_f32_16x16x32_bf16 v[188:191], a[100:103], v[120:123], v[188:191]\n"
"v_add_f32_e32 v152, v96, v152\n"
"v_add_f32_e32 v153, v97, v153\n"
"v_mfma_f32_16x16x32_bf16 v[192:195], a[104:107], v[120:123], v[192:195]\n"
"buffer_load_dword v248, v10, s[4:7], s98 offen offset:1792 sc1\n"
"v_mfma_f32_16x16x32_bf16 v[196:199], a[108:111], v[120:123], v[196:199]\n"
"v_add_f32_e32 v154, v98, v154\n"
"v_add_f32_e32 v155, v99, v155\n"
"v_mfma_f32_16x16x32_bf16 v[200:203], a[112:115], v[120:123], v[200:203]\n"
"buffer_load_dwordx4 v[96:99], v1, s[4:7], s97 offen sc1\n"
"v_mfma_f32_16x16x32_bf16 v[208:211], a[116:119], v[120:123], v[208:211]\n"
"s_waitcnt vmcnt(24)\n"
"v_add_f32_e32 v160, v100, v160\n"
"v_add_f32_e32 v161, v101, v161\n"
"v_mfma_f32_16x16x32_bf16 v[184:187], a[120:123], v[124:127], v[184:187]\n"
"v_mov_b32_e32 v112, -1\n"
"v_mov_b32_e32 v113, -1\n"
"v_mfma_f32_16x16x32_bf16 v[188:191], a[124:127], v[124:127], v[188:191]\n"
"v_mov_b32_e32 v114, -1\n"
"v_mov_b32_e32 v115, -1\n"
"v_mfma_f32_16x16x32_bf16 v[192:195], v[24:27], v[124:127], v[192:195]\n"
"s_add_u32 s98, s60, 0x448000\n"
"buffer_store_dwordx4 v[112:115], v0, s[4:7], s98 offen sc1\n"
"v_mfma_f32_16x16x32_bf16 v[196:199], v[28:31], v[124:127], v[196:199]\n"
"v_add_f32_e32 v162, v102, v162\n"
"v_add_f32_e32 v163, v103, v163\n"
"v_mfma_f32_16x16x32_bf16 v[200:203], v[32:35], v[124:127], v[200:203]\n"
"buffer_load_dwordx4 v[100:103], v1, s[4:7], s97 offen offset:1024 sc1\n"
"v_mfma_f32_16x16x32_bf16 v[208:211], v[36:39], v[124:127], v[208:211]\n"
"s_waitcnt vmcnt(25)\n"
"v_cvt_pk_bf16_f32 v116, v152, v153\n"
"v_cvt_pk_bf16_f32 v117, v154, v155\n"
"v_mfma_f32_16x16x32_bf16 v[184:187], v[40:43], v[128:131], v[184:187]\n"
"s_add_u32 s98, s61, 0x400000\n"
"v_cvt_pk_bf16_f32 v118, v160, v161\n"
"v_cvt_pk_bf16_f32 v119, v162, v163\n"
"v_mfma_f32_16x16x32_bf16 v[188:191], v[44:47], v[128:131], v[188:191]\n"
"buffer_store_dwordx4 v[116:119], v0, s[4:7], s98 offen offset:1024 sc1\n"
"v_mfma_f32_16x16x32_bf16 v[192:195], v[48:51], v[128:131], v[192:195]\n"
"ds_write_b128 v2, v[116:119] offset:1024\n"
"v_mfma_f32_16x16x32_bf16 v[120:123], v[104:107], v[92:95], 0\n"
"v_mfma_f32_16x16x32_bf16 v[124:127], v[92:95], v[92:95], 0\n"
"v_mfma_f32_16x16x32_bf16 v[120:123], v[104:107], v[116:119], v[120:123]\n"
"v_mfma_f32_16x16x32_bf16 v[124:127], v[116:119], v[116:119], v[124:127]\n"
"v_mfma_f32_16x16x32_bf16 v[196:199], v[52:55], v[128:131], v[196:199]\n"
"s_mov_b32 s94, 0xaaaaaaaa\n"
"s_mov_b32 s95, 0xaaaaaaaa\n"
"v_mfma_f32_16x16x32_bf16 v[200:203], v[56:59], v[128:131], v[200:203]\n"
"buffer_load_dwordx4 v[92:95], v1, s[4:7], s97 offen offset:2048 sc1\n"
"v_mfma_f32_16x16x32_bf16 v[208:211], v[60:63], v[128:131], v[208:211]\n"
"s_waitcnt vmcnt(26)\n"
"v_mfma_f32_16x16x32_bf16 v[184:187], v[64:67], v[132:135], v[184:187]\n"
"v_cndmask_b32_e64 v124, v124, v125, s[94:95]\n"
"v_cndmask_b32_e64 v126, v126, v127, s[94:95]\n"
"v_mfma_f32_16x16x32_bf16 v[188:191], v[68:71], v[132:135], v[188:191]\n"
"s_mov_b32 s94, 0xcccccccc\n"
"s_mov_b32 s95, 0xcccccccc\n"
"v_mfma_f32_16x16x32_bf16 v[192:195], v[72:75], v[132:135], v[192:195]\n"
"s_add_u32 s98, s70, 0x200000\n"
"buffer_load_dwordx4 a[128:131], v0, s[4:7], s98 offen sc1\n"
"v_mfma_f32_16x16x32_bf16 v[196:199], v[76:79], v[132:135], v[196:199]\n"
"v_cndmask_b32_e64 v121, v124, v126, s[94:95]\n"
"v_mfma_f32_16x16x32_bf16 v[200:203], v[80:83], v[132:135], v[200:203]\n"
"buffer_load_dwordx4 v[104:107], v1, s[4:7], s97 offen offset:3072 sc1\n"
"v_mfma_f32_16x16x32_bf16 v[208:211], v[84:87], v[132:135], v[208:211]\n"
"s_waitcnt vmcnt(26)\n"
"v_cmp_u_f32_e32 vcc, v184, v184\n"
"; @B G1 0.1\n"
"v_mfma_f32_16x16x32_bf16 v[116:119], a[0:3], v[136:139], 0\n"
"s_cbranch_vccnz .Lmk_p50s\n"
".Lmk_p50r:\n"
"v_mfma_f32_16x16x32_bf16 v[124:127], a[4:7], v[136:139], 0\n"
"s_mov_b32 s94, 0xf0000f\n"
"s_mov_b32 s95, 0xf0000f00\n"
"v_mfma_f32_16x16x32_bf16 v[128:131], a[8:11], v[136:139], 0\n"
"s_add_u32 s97, s63, 0x40000\n"
"v_cndmask_b32_e64 v120, v120, v121, s[94:95]\n"
"v_mfma_f32_16x16x32_bf16 v[132:135], a[12:15], v[136:139], 0\n"
"ds_write_b128 v3, v[184:187]\n"
"v_mfma_f32_16x16x32_bf16 v[144:147], a[16:19], v[136:139], 0\n"
"s_add_u32 s96, s55, 0x440000\n"
"buffer_load_dwordx4 v[148:151], v0, s[4:7], s96 offen sc1\n"
"v_mfma_f32_16x16x32_bf16 v[152:155], a[20:23], v[136:139], 0\n"
"buffer_store_dword v120, v14, s[4:7], s97 offen sc1\n"
"v_mfma_f32_16x16x32_bf16 v[116:119], a[24:27], v[140:143], v[116:119]\n"
"ds_write_b128 v3, v[188:191] offset:1024\n"
"v_mfma_f32_16x16x32_bf16 v[124:127], a[28:31], v[140:143], v[124:127]\n"
"v_mfma_f32_16x16x32_bf16 v[128:131], a[32:35], v[140:143], v[128:131]\n"
"v_mfma_f32_16x16x32_bf16 v[132:135], a[36:39], v[140:143], v[132:135]\n"
"ds_write_b128 v3, v[192:195] offset:2048\n"
"v_mfma_f32_16x16x32_bf16 v[144:147], a[40:43], v[140:143], v[144:147]\n"
"buffer_load_dwordx4 v[120:123], v0, s[4:7], s96 offen offset:1024 sc1\n"
"v_mfma_f32_16x16x32_bf16 v[152:155], a[44:47], v[140:143], v[152:155]\n"
"s_waitcnt vmcnt(27)\n"
"v_mfma_f32_16x16x32_bf16 v[116:119], a[48:51], v[156:159], v[116:119]\n"
"ds_write_b128 v3, v[196:199] offset:3072\n"
"v_mfma_f32_16x16x32_bf16 v[124:127], a[52:55], v[156:159], v[124:127]\n"
"v_mfma_f32_16x16x32_bf16 v[128:131], a[56:59], v[156:159], v[128:131]\n"
"v_mfma_f32_16x16x32_bf16 v[132:135], a[60:63], v[156:159], v[132:135]\n"
"ds_write_b128 v3, v[200:203] offset:4096\n"
"v_mfma_f32_16x16x32_bf16 v[144:147], a[64:67], v[156:159], v[144:147]\n"
"buffer_load_dwordx4 v[136:139], v0, s[4:7], s96 offen offset:2048 sc1\n"
"v_mfma_f32_16x16x32_bf16 v[152:155], a[68:71], v[156:159], v[152:155]\n"
"v_mfma_f32_16x16x32_bf16 v[116:119], a[72:75], v[164:167], v[116:119]\n"
"ds_write_b128 v3, v[208:211] offset:5120\n"
"v_mfma_f32_16x16x32_bf16 v[124:127], a[76:79], v[164:167], v[124:127]\n"
"s_waitcnt vmcnt(11)\n"
"v_add_f32_e32 v255, v255, v251\n"
"v_add_f32_e32 v254, v254, v250\n"
"v_mfma_f32_16x16x32_bf16 v[128:131], a[80:83], v[164:167], v[128:131]\n"
"buffer_load_dwordx4 a[132:135], v0, s[4:7], s98 offen offset:1024 sc1\n"
"v_mfma_f32_16x16x32_bf16 v[132:135], a[84:87], v[164:167], v[132:135]\n"
"v_add_f32_e32 v253, v253, v249\n"
"v_add_f32_e32 v252, v252, v248\n"
"v_mfma_f32_16x16x32_bf16 v[144:147], a[88:91], v[164:167], v[144:147]\n"
"buffer_load_dwordx4 v[140:143], v0, s[4:7], s96 offen offset:3072 sc1\n"
"v_mfma_f32_16x16x32_bf16 v[152:155], a[92:95], v[164:167], v[152:155]\n"
"v_add_f32_e32 v255, v255, v253\n"
"v_add_f32_e32 v254, v254, v252\n"
"v_mfma_f32_16x16x32_bf16 v[116:119], a[96:99], v[168:171], v[116:119]\n"
"v_add_f32_e32 v255, v255, v254\n"
"v_cmp_u_f32_e32 vcc, v255, v255\n"
"v_mfma_f32_16x16x32_bf16 v[124:127], a[100:103], v[168:171], v[124:127]\n"
"s_cbranch_vccnz .Lmk_m55s\n"
".Lmk_m55r:\n"
"v_mov_b32_e32 v254, v255\n"
"v_mfma_f32_16x16x32_bf16 v[128:131], a[104:107], v[168:171], v[128:131]\n"
"s_nop 0\n"
"v_permlane32_swap_b32_e32 v255, v254\n"
"v_add_f32_e32 v255, v255, v254\n"
"v_mfma_f32_16x16x32_bf16 v[132:135], a[108:111], v[168:171], v[132:135]\n"
"v_mul_f32_e32 v255, 0x3a800000, v255\n"
"v_mov_b32_e32 v254, v255\n"
"v_mfma_f32_16x16x32_bf16 v[144:147], a[112:115], v[168:171], v[144:147]\n"
"buffer_load_dwordx4 v[156:159], v1, s[4:7], s96 offen sc1\n"
"v_mfma_f32_16x16x32_bf16 v[152:155], a[116:119], v[168:171], v[152:155]\n"
"s_waitcnt lgkmcnt(0)\n"
"s_barrier\n"
"v_mfma_f32_16x16x32_bf16 v[116:119], a[120:123], v[172:175], v[116:119]\n"
"v_permlane16_swap_b32_e32 v255, v254\n"
"v_cvt_pk_bf16_f32 v253, -v255, 0\n"
"v_mfma_f32_16x16x32_bf16 v[124:127], a[124:127], v[172:175], v[124:127]\n"
"v_fma_f32 v254, -v255, v255, v254\n"
"v_lshlrev_b32_e32 v253, 16, v253\n"
"v_mfma_f32_16x16x32_bf16 v[128:131], v[24:27], v[172:175], v[128:131]\n"
"ds_read_b128 v[160:163], v4\n"
"ds_read_b128 v[164:167], v9 offset:512\n"
"v_mfma_f32_16x16x32_bf16 v[132:135], v[28:31], v[172:175], v[132:135]\n"
"ds_read_b128 v[168:171], v4 offset:1024\n"
"ds_read_b128 v[184:187], v9 offset:576\n"
"v_mfma_f32_16x16x32_bf16 v[144:147], v[32:35], v[172:175], v[144:147]\n"
"buffer_load_dwordx4 v[188:191], v1, s[4:7], s96 offen offset:1024 sc1\n"
"v_mfma_f32_16x16x32_bf16 v[152:155], v[36:39], v[172:175], v[152:155]\n"
"ds_read_b128 v[172:175], v4 offset:2048\n"
"ds_read_b128 v[192:195], v9 offset:640\n"
"v_mfma_f32_16x16x32_bf16 v[116:119], v[40:43], v[176:179], v[116:119]\n"
"v_max_f32_e32 v254, 0, v254\n"
"v_cvt_pk_bf16_f32 v254, v254, 0\n"
"v_mfma_f32_16x16x32_bf16 v[124:127], v[44:47], v[176:179], v[124:127]\n"
"v_lshlrev_b32_e32 v254, 16, v254\n"
"v_add_f32_e32 v254, 0x3727c5ac, v254\n"
"v_mfma_f32_16x16x32_bf16 v[128:131], v[48:51], v[176:179], v[128:131]\n"
"v_cvt_pk_bf16_f32 v254, v254, 0\n"
"v_lshlrev_b32_e32 v254, 16, v254\n"
"v_mfma_f32_16x16x32_bf16 v[132:135], v[52:55], v[176:179], v[132:135]\n"
"v_sqrt_f32_e32 v254, v254\n"
"v_mfma_f32_16x16x32_bf16 v[144:147], v[56:59], v[176:179], v[144:147]\n"
"buffer_load_dwordx4 v[196:199], v1, s[4:7], s96 offen offset:2048 sc1\n"
"v_mfma_f32_16x16x32_bf16 v[152:155], v[60:63], v[176:179], v[152:155]\n"
"s_waitcnt lgkmcnt(0)\n"
"v_fmac_f32_e32 v160, v253, v164\n"
"v_fmac_f32_e32 v161, v253, v165\n"
"v_mfma_f32_16x16x32_bf16 v[116:119], v[64:67], v[180:183], v[116:119]\n"
"v_fmac_f32_e32 v162, v253, v166\n"
"v_fmac_f32_e32 v163, v253, v167\n"
"v_mfma_f32_16x16x32_bf16 v[124:127], v[68:71], v[180:183], v[124:127]\n"
"v_fmac_f32_e32 v168, v253, v184\n"
"v_fmac_f32_e32 v169, v253, v185\n"
"v_mfma_f32_16x16x32_bf16 v[128:131], v[72:75], v[180:183], v[128:131]\n"
"buffer_load_dwordx4 a[136:139], v0, s[4:7], s98 offen offset:2048 sc1\n"
"v_mfma_f32_16x16x32_bf16 v[132:135], v[76:79], v[180:183], v[132:135]\n"
"v_fmac_f32_e32 v170, v253, v186\n"
"v_fmac_f32_e32 v171, v253, v187\n"
"v_mfma_f32_16x16x32_bf16 v[144:147], v[80:83], v[180:183], v[144:147]\n"
"buffer_load_dwordx4 v[164:167], v1, s[4:7], s96 offen offset:3072 sc1\n"
"v_mfma_f32_16x16x32_bf16 v[152:155], v[84:87], v[180:183], v[152:155]\n"
"v_cmp_u_f32_e32 vcc, v116, v116\n"
"v_fmac_f32_e32 v172, v253, v192\n"
"; @B G1 0.2\n"
"v_mfma_f32_16x16x32_bf16 v[176:179], a[0:3], v[204:207], 0\n"
"s_cbranch_vccnz .Lmk_p51s\n"
".Lmk_p51r:\n"
"v_fmac_f32_e32 v173, v253, v193\n"
"v_mfma_f32_16x16x32_bf16 v[180:183], a[4:7], v[204:207], 0\n"
"v_fmac_f32_e32 v174, v253, v194\n"
"v_fmac_f32_e32 v175, v253, v195\n"
"v_mfma_f32_16x16x32_bf16 v[184:187], a[8:11], v[204:207], 0\n"
"ds_read_b128 v[192:195], v4 offset:3072\n"
"ds_read_b128 v[200:203], v9 offset:704\n"
"v_mfma_f32_16x16x32_bf16 v[208:211], a[12:15], v[204:207], 0\n"
"ds_write_b128 v3, v[116:119] offset:6144\n"
"v_mfma_f32_16x16x32_bf16 v[116:119], a[16:19], v[204:207], 0\n"
"s_add_u32 s97, s52, 0x448000\n"
"buffer_load_dwordx4 v[216:219], v0, s[4:7], s97 offen sc1\n"
"s_mul_i32 s96, s0, .Lmk_exit-.Lmk_b2\n"
"v_mfma_f32_16x16x32_bf16 v[220:223], a[20:23], v[204:207], 0\n"
"s_add_u32 s96, s96, .Lmk_b2-.Lmk_g49\n"
"s_getpc_b64 s[84:85]\n"
".Lmk_g49:\n"
"s_add_u32 s84, s84, s96\n"
"s_addc_u32 s85, s85, 0\n"
"s_and_b32 s84, s84, -16\n"
"v_mfma_f32_16x16x32_bf16 v[176:179], a[24:27], v[212:215], v[176:179]\n"
"ds_write_b128 v3, v[124:127] offset:7168\n"
"v_mfma_f32_16x16x32_bf16 v[180:183], a[28:31], v[212:215], v[180:183]\n"
"s_and_b32 s85, s85, 0xffff\n"
"s_sub_u32 s96, .Lmk_end-.Lmk_g49, s96\n"
"s_min_u32 s86, s96, 0x4000\n"
"s_mov_b32 s87, 0x20000\n"
"v_mfma_f32_16x16x32_bf16 v[184:187], a[32:35], v[212:215], v[184:187]\n"
"s_mov_b32 m0, 0x27800\n"
"s_nop 0\n"
"buffer_load_dwordx4 v0, s[84:87], s99 offen lds\n"
"v_mfma_f32_16x16x32_bf16 v[208:211], a[36:39], v[212:215], v[208:211]\n"
"ds_write_b128 v3, v[128:131] offset:8192\n"
"v_mfma_f32_16x16x32_bf16 v[116:119], a[40:43], v[212:215], v[116:119]\n"
"buffer_load_dwordx4 v[124:127], v0, s[4:7], s97 offen offset:1024 sc1\n"
"v_mfma_f32_16x16x32_bf16 v[220:223], a[44:47], v[212:215], v[220:223]\n"
"s_add_u32 s96, s60, 0x440000\n"
"ds_read_b128 v[128:131], v4 offset:4096\n"
"v_mfma_f32_16x16x32_bf16 v[176:179], a[48:51], v[88:91], v[176:179]\n"
"ds_write_b128 v3, v[132:135] offset:9216\n"
"v_mfma_f32_16x16x32_bf16 v[180:183], a[52:55], v[88:91], v[180:183]\n"
"buffer_store_dwordx4 v[112:115], v0, s[4:7], s96 offen sc1\n"
"v_mfma_f32_16x16x32_bf16 v[184:187], a[56:59], v[88:91], v[184:187]\n"
"ds_read_b128 v[112:115], v9 offset:768\n"
"ds_read_b128 v[132:135], v4 offset:5120\n"
"v_mfma_f32_16x16x32_bf16 v[208:211], a[60:63], v[88:91], v[208:211]\n"
"ds_write_b128 v3, v[144:147] offset:10240\n"
"v_mfma_f32_16x16x32_bf16 v[116:119], a[64:67], v[88:91], v[116:119]\n"
"buffer_load_dwordx4 v[144:147], v0, s[4:7], s97 offen offset:2048 sc1\n"
"v_mfma_f32_16x16x32_bf16 v[220:223], a[68:71], v[88:91], v[220:223]\n"
"ds_read_b128 v[88:91], v9 offset:832\n"
"v_cvt_pk_bf16_f32 v254, v254, 0\n"
"v_mfma_f32_16x16x32_bf16 v[176:179], a[72:75], v[108:111], v[176:179]\n"
"ds_write_b128 v3, v[152:155] offset:11264\n"
"v_mfma_f32_16x16x32_bf16 v[180:183], a[76:79], v[108:111], v[180:183]\n"
"v_lshlrev_b32_e32 v254, 16, v254\n"
"v_mfma_f32_16x16x32_bf16 v[184:187], a[80:83], v[108:111], v[184:187]\n"
"buffer_load_dwordx4 a[140:143], v0, s[4:7], s98 offen offset:3072 sc1\n"
"v_mfma_f32_16x16x32_bf16 v[208:211], a[84:87], v[108:111], v[208:211]\n"
"v_rcp_f32_e32 v255, v254\n"
"v_mfma_f32_16x16x32_bf16 v[116:119], a[88:91], v[108:111], v[116:119]\n"
"buffer_load_dwordx4 v[152:155], v0, s[4:7], s97 offen offset:3072 sc1\n"
"v_mfma_f32_16x16x32_bf16 v[220:223], a[92:95], v[108:111], v[220:223]\n"
"s_waitcnt vmcnt(22)\n"
"s_waitcnt lgkmcnt(1)\n"
"v_fmac_f32_e32 v192, v253, v200\n"
"v_mfma_f32_16x16x32_bf16 v[176:179], a[96:99], v[96:99], v[176:179]\n"
"v_fmac_f32_e32 v193, v253, v201\n"
"v_fmac_f32_e32 v194, v253, v202\n"
"v_mfma_f32_16x16x32_bf16 v[180:183], a[100:103], v[96:99], v[180:183]\n"
"v_fmac_f32_e32 v195, v253, v203\n"
"v_fmac_f32_e32 v128, v253, v112\n"
"v_mfma_f32_16x16x32_bf16 v[184:187], a[104:107], v[96:99], v[184:187]\n"
"v_fmac_f32_e32 v129, v253, v113\n"
"v_fmac_f32_e32 v130, v253, v114\n"
"v_mfma_f32_16x16x32_bf16 v[208:211], a[108:111], v[96:99], v[208:211]\n"
"v_fmac_f32_e32 v131, v253, v115\n"
"v_fmac_f32_e32 v132, v253, v88\n"
"v_mfma_f32_16x16x32_bf16 v[116:119], a[112:115], v[96:99], v[116:119]\n"
"buffer_load_dwordx4 v[108:111], v1, s[4:7], s97 offen sc1\n"
"v_mfma_f32_16x16x32_bf16 v[220:223], a[116:119], v[96:99], v[220:223]\n"
"v_fmac_f32_e32 v133, v253, v89\n"
"v_fmac_f32_e32 v134, v253, v90\n"
"v_mfma_f32_16x16x32_bf16 v[176:179], a[120:123], v[100:103], v[176:179]\n"
"v_fmac_f32_e32 v135, v253, v91\n"
"v_mul_f32_e32 v254, 0x3e8293ee, v255\n"
"v_mfma_f32_16x16x32_bf16 v[180:183], a[124:127], v[100:103], v[180:183]\n"
"v_mfma_f32_16x16x32_bf16 v[184:187], v[24:27], v[100:103], v[184:187]\n"
"v_mfma_f32_16x16x32_bf16 v[208:211], v[28:31], v[100:103], v[208:211]\n"
"v_mfma_f32_16x16x32_bf16 v[116:119], v[32:35], v[100:103], v[116:119]\n"
"buffer_load_dwordx4 v[88:91], v1, s[4:7], s97 offen offset:1024 sc1\n"
"v_mfma_f32_16x16x32_bf16 v[220:223], v[36:39], v[100:103], v[220:223]\n"
"s_waitcnt vmcnt(20)\n"
"v_mfma_f32_16x16x32_bf16 v[176:179], v[40:43], v[92:95], v[176:179]\n"
"v_mfma_f32_16x16x32_bf16 v[180:183], v[44:47], v[92:95], v[180:183]\n"
"v_mfma_f32_16x16x32_bf16 v[184:187], v[48:51], v[92:95], v[184:187]\n"
"v_mfma_f32_16x16x32_bf16 v[208:211], v[52:55], v[92:95], v[208:211]\n"
"v_mfma_f32_16x16x32_bf16 v[116:119], v[56:59], v[92:95], v[116:119]\n"
"s_waitcnt vmcnt(17)\n"
"v_max_u32_e32 v253, v148, v149\n"
"v_max3_u32 v253, v253, v150, v151\n"
"v_mfma_f32_16x16x32_bf16 v[220:223], v[60:63], v[92:95], v[220:223]\n"
"s_waitcnt vmcnt(14)\n"
"v_max3_u32 v253, v253, v120, v121\n"
"v_max3_u32 v253, v253, v122, v123\n"
"v_mfma_f32_16x16x32_bf16 v[176:179], v[64:67], v[104:107], v[176:179]\n"
"s_add_u32 s96, s68, s80\n"
"v_max3_u32 v253, v253, v136, v137\n"
"v_max3_u32 v253, v253, v138, v139\n"
"v_mfma_f32_16x16x32_bf16 v[180:183], v[68:71], v[104:107], v[180:183]\n"
"s_add_u32 s96, s96, 0x10000\n"
"v_max3_u32 v253, v253, v140, v141\n"
"v_max3_u32 v253, v253, v142, v143\n"
"v_mfma_f32_16x16x32_bf16 v[184:187], v[72:75], v[104:107], v[184:187]\n"
"v_cmp_eq_u32_e32 vcc, -1, v253\n"
"v_mfma_f32_16x16x32_bf16 v[208:211], v[76:79], v[104:107], v[208:211]\n"
"s_cbranch_vccnz .Lmk_q53s\n"
".Lmk_q53r:\n"
"v_mfma_f32_16x16x32_bf16 v[116:119], v[80:83], v[104:107], v[116:119]\n"
"buffer_load_dwordx4 v[92:95], v1, s[4:7], s97 offen offset:2048 sc1\n"
"v_mfma_f32_16x16x32_bf16 v[220:223], v[84:87], v[104:107], v[220:223]\n"
"v_cmp_u_f32_e32 vcc, v176, v176\n"
"; @B G1 0.3\n"
"v_mfma_f32_16x16x32_bf16 v[160:163], a[0:3], v[148:151], v[160:163]\n"
"s_cbranch_vccnz .Lmk_p52s\n"
".Lmk_p52r:\n"
"v_mfma_f32_16x16x32_bf16 v[168:171], a[4:7], v[148:151], v[168:171]\n"
"buffer_load_dwordx4 v[96:99], v1, s[4:7], s97 offen offset:3072 sc1\n"
"v_mfma_f32_16x16x32_bf16 v[172:175], a[8:11], v[148:151], v[172:175]\n"
"buffer_store_dwordx2 v[18:19], v11, s[4:7], s96 offen sc1\n"
"v_mfma_f32_16x16x32_bf16 v[192:195], a[12:15], v[148:151], v[192:195]\n"
"ds_write_b128 v3, v[176:179] offset:12288\n"
"v_mfma_f32_16x16x32_bf16 v[128:131], a[16:19], v[148:151], v[128:131]\n"
"s_add_u32 s97, s53, 0x448000\n"
"buffer_load_dwordx4 v[100:103], v0, s[4:7], s97 offen sc1\n"
"v_mfma_f32_16x16x32_bf16 v[132:135], a[20:23], v[148:151], v[132:135]\n"
"buffer_load_dwordx4 a[144:147], v1, s[4:7], s98 offen sc1\n"
"v_mfma_f32_16x16x32_bf16 v[160:163], a[24:27], v[120:123], v[160:163]\n"
"ds_write_b128 v3, v[180:183] offset:13312\n"
"v_mfma_f32_16x16x32_bf16 v[168:171], a[28:31], v[120:123], v[168:171]\n"
"v_mfma_f32_16x16x32_bf16 v[172:175], a[32:35], v[120:123], v[172:175]\n"
"v_mfma_f32_16x16x32_bf16 v[192:195], a[36:39], v[120:123], v[192:195]\n"
"ds_write_b128 v3, v[184:187] offset:14336\n"
"v_mfma_f32_16x16x32_bf16 v[128:131], a[40:43], v[120:123], v[128:131]\n"
"buffer_load_dwordx4 v[104:107], v0, s[4:7], s97 offen offset:1024 sc1\n"
"v_mfma_f32_16x16x32_bf16 v[132:135], a[44:47], v[120:123], v[132:135]\n"
"v_mfma_f32_16x16x32_bf16 v[160:163], a[48:51], v[136:139], v[160:163]\n"
"ds_write_b128 v3, v[208:211] offset:15360\n"
"v_mfma_f32_16x16x32_bf16 v[168:171], a[52:55], v[136:139], v[168:171]\n"
"v_mfma_f32_16x16x32_bf16 v[172:175], a[56:59], v[136:139], v[172:175]\n"
"v_mfma_f32_16x16x32_bf16 v[192:195], a[60:63], v[136:139], v[192:195]\n"
"ds_write_b128 v3, v[116:119] offset:16384\n"
"v_mfma_f32_16x16x32_bf16 v[128:131], a[64:67], v[136:139], v[128:131]\n"
"s_waitcnt vmcnt(18)\n"
"v_max_u32_e32 v253, v156, v157\n"
"v_max3_u32 v253, v253, v158, v159\n"
"v_mfma_f32_16x16x32_bf16 v[132:135], a[68:71], v[136:139], v[132:135]\n"
"s_waitcnt vmcnt(15)\n"
"v_max3_u32 v253, v253, v188, v189\n"
"v_max3_u32 v253, v253, v190, v191\n"
"v_mfma_f32_16x16x32_bf16 v[160:163], a[72:75], v[140:143], v[160:163]\n"
"v_max3_u32 v253, v253, v196, v197\n"
"v_max3_u32 v253, v253, v198, v199\n"
"v_mfma_f32_16x16x32_bf16 v[168:171], a[76:79], v[140:143], v[168:171]\n"
"v_max3_u32 v253, v253, v164, v165\n"
"v_max3_u32 v253, v253, v166, v167\n"
"v_mfma_f32_16x16x32_bf16 v[172:175], a[80:83], v[140:143], v[172:175]\n"
"v_cmp_eq_u32_e32 vcc, -1, v253\n"
"v_mfma_f32_16x16x32_bf16 v[192:195], a[84:87], v[140:143], v[192:195]\n"
"s_cbranch_vccnz .Lmk_q54s\n"
".Lmk_q54r:\n"
"v_mfma_f32_16x16x32_bf16 v[128:131], a[88:91], v[140:143], v[128:131]\n"
"buffer_load_dwordx4 v[112:115], v0, s[4:7], s97 offen offset:2048 sc1\n"
"v_mfma_f32_16x16x32_bf16 v[132:135], a[92:95], v[140:143], v[132:135]\n"
"buffer_load_dwordx4 v[116:119], v0, s[4:7], s97 offen offset:3072 sc1\n"
"v_mfma_f32_16x16x32_bf16 v[160:163], a[96:99], v[156:159], v[160:163]\n"
"ds_write_b128 v3, v[220:223] offset:17408\n"
"v_mfma_f32_16x16x32_bf16 v[168:171], a[100:103], v[156:159], v[168:171]\n"
"v_mfma_f32_16x16x32_bf16 v[172:175], a[104:107], v[156:159], v[172:175]\n"
"v_mfma_f32_16x16x32_bf16 v[192:195], a[108:111], v[156:159], v[192:195]\n"
"s_waitcnt lgkmcnt(0)\n"
"s_barrier\n"
"v_mfma_f32_16x16x32_bf16 v[128:131], a[112:115], v[156:159], v[128:131]\n"
"buffer_load_dwordx4 v[120:123], v1, s[4:7], s97 offen sc1\n"
"v_mfma_f32_16x16x32_bf16 v[132:135], a[116:119], v[156:159], v[132:135]\n"
"buffer_load_dwordx4 a[148:151], v1, s[4:7], s98 offen offset:1024 sc1\n"
"v_mfma_f32_16x16x32_bf16 v[160:163], a[120:123], v[188:191], v[160:163]\n"
"ds_read_b128 v[136:139], v5 offset:2048\n"
"ds_read_b128 v[140:143], v6 offset:2048\n"
"v_mfma_f32_16x16x32_bf16 v[168:171], a[124:127], v[188:191], v[168:171]\n"
"ds_read_b128 v[148:151], v5 offset:3072\n"
"ds_read_b128 v[156:159], v6 offset:3072\n"
"v_mfma_f32_16x16x32_bf16 v[172:175], v[24:27], v[188:191], v[172:175]\n"
"v_mfma_f32_16x16x32_bf16 v[192:195], v[28:31], v[188:191], v[192:195]\n"
"v_mfma_f32_16x16x32_bf16 v[128:131], v[32:35], v[188:191], v[128:131]\n"
"buffer_load_dwordx4 v[176:179], v1, s[4:7], s97 offen offset:1024 sc1\n"
"v_mfma_f32_16x16x32_bf16 v[132:135], v[36:39], v[188:191], v[132:135]\n"
"ds_read_b128 v[180:183], v5 offset:4096\n"
"ds_read_b128 v[184:187], v6 offset:4096\n"
"v_mfma_f32_16x16x32_bf16 v[160:163], v[40:43], v[196:199], v[160:163]\n"
"ds_read_b128 v[188:191], v5 offset:5120\n"
"ds_read_b128 v[200:203], v6 offset:5120\n"
"v_mfma_f32_16x16x32_bf16 v[168:171], v[44:47], v[196:199], v[168:171]\n"
"s_waitcnt lgkmcnt(4)\n"
"v_add_f32_e32 v136, v136, v140\n"
"v_add_f32_e32 v137, v137, v141\n"
"v_mfma_f32_16x16x32_bf16 v[172:175], v[48:51], v[196:199], v[172:175]\n"
"v_add_f32_e32 v138, v138, v142\n"
"v_add_f32_e32 v139, v139, v143\n"
"v_mfma_f32_16x16x32_bf16 v[192:195], v[52:55], v[196:199], v[192:195]\n"
"ds_read_b128 v[140:143], v5\n"
"ds_read_b128 v[204:207], v6\n"
"v_mfma_f32_16x16x32_bf16 v[128:131], v[56:59], v[196:199], v[128:131]\n"
"buffer_load_dwordx4 v[208:211], v1, s[4:7], s97 offen offset:2048 sc1\n"
"v_mfma_f32_16x16x32_bf16 v[132:135], v[60:63], v[196:199], v[132:135]\n"
"ds_read_b128 v[196:199], v5 offset:1024\n"
"ds_read_b128 v[212:215], v6 offset:1024\n"
"v_mfma_f32_16x16x32_bf16 v[160:163], v[64:67], v[164:167], v[160:163]\n"
"s_waitcnt lgkmcnt(4)\n"
"v_add_f32_e32 v148, v148, v156\n"
"v_add_f32_e32 v149, v149, v157\n"
"v_mfma_f32_16x16x32_bf16 v[168:171], v[68:71], v[164:167], v[168:171]\n"
"v_add_f32_e32 v150, v150, v158\n"
"v_add_f32_e32 v151, v151, v159\n"
"v_mfma_f32_16x16x32_bf16 v[172:175], v[72:75], v[164:167], v[172:175]\n"
"v_add_f32_e32 v180, v180, v184\n"
"v_add_f32_e32 v181, v181, v185\n"
"v_mfma_f32_16x16x32_bf16 v[192:195], v[76:79], v[164:167], v[192:195]\n"
"v_add_f32_e32 v182, v182, v186\n"
"v_add_f32_e32 v183, v183, v187\n"
"v_mfma_f32_16x16x32_bf16 v[128:131], v[80:83], v[164:167], v[128:131]\n"
"buffer_load_dwordx4 v[156:159], v1, s[4:7], s97 offen offset:3072 sc1\n"
"v_mfma_f32_16x16x32_bf16 v[132:135], v[84:87], v[164:167], v[132:135]\n"
"s_waitcnt lgkmcnt(0)\n"
"v_add_f32_e32 v188, v188, v200\n"
"v_add_f32_e32 v189, v189, v201\n"
"v_add_f32_e32 v190, v190, v202\n"
"v_add_f32_e32 v191, v191, v203\n"
"v_add_f32_e32 v140, v140, v204\n"
"v_add_f32_e32 v141, v141, v205\n"
"v_add_f32_e32 v142, v142, v206\n"
"v_add_f32_e32 v143, v143, v207\n"
"v_add_f32_e32 v196, v196, v212\n"
"v_add_f32_e32 v197, v197, v213\n"
"v_add_f32_e32 v198, v198, v214\n"
"v_add_f32_e32 v199, v199, v215\n"
".Lmk_b1:\n"
"s_waitcnt vmcnt(19)\n"
"; @B G1 1.0\n"
"v_mfma_f32_16x16x32_bf16 v[164:167], a[0:3], v[216:219], 0\n"
"v_mfma_f32_16x16x32_bf16 v[184:187], a[4:7], v[216:219], 0\n"
"s_add_u32 s97, s62, 0x44800\n"
"v_mfma_f32_16x16x32_bf16 v[200:203], a[8:11], v[216:219], 0\n"
"v_mfma_f32_16x16x32_bf16 v[204:207], a[12:15], v[216:219], 0\n"
"v_add_f32_e32 v172, v172, v136\n"
"v_add_f32_e32 v173, v173, v137\n"
"v_mfma_f32_16x16x32_bf16 v[212:215], a[16:19], v[216:219], 0\n"
"s_add_u32 s96, s54, 0x448000\n"
"buffer_load_dwordx4 v[220:223], v0, s[4:7], s96 offen sc1\n"
"v_mfma_f32_16x16x32_bf16 v[224:227], a[20:23], v[216:219], 0\n"
"buffer_load_dword v253, v10, s[4:7], s97 offen sc1\n"
"v_mfma_f32_16x16x32_bf16 v[164:167], a[24:27], v[124:127], v[164:167]\n"
"v_add_f32_e32 v174, v174, v138\n"
"v_add_f32_e32 v175, v175, v139\n"
"v_mfma_f32_16x16x32_bf16 v[184:187], a[28:31], v[124:127], v[184:187]\n"
"v_add_f32_e32 v192, v192, v148\n"
"v_add_f32_e32 v193, v193, v149\n"
"v_mfma_f32_16x16x32_bf16 v[200:203], a[32:35], v[124:127], v[200:203]\n"
"buffer_load_dwordx4 a[152:155], v1, s[4:7], s98 offen offset:2048 sc1\n"
"v_mfma_f32_16x16x32_bf16 v[204:207], a[36:39], v[124:127], v[204:207]\n"
"buffer_load_dword v252, v10, s[4:7], s97 offen offset:256 sc1\n"
"v_mfma_f32_16x16x32_bf16 v[212:215], a[40:43], v[124:127], v[212:215]\n"
"buffer_load_dwordx4 v[136:139], v0, s[4:7], s96 offen offset:1024 sc1\n"
"v_mfma_f32_16x16x32_bf16 v[224:227], a[44:47], v[124:127], v[224:227]\n"
"s_waitcnt vmcnt(20)\n"
"v_add_f32_e32 v194, v194, v150\n"
"v_add_f32_e32 v195, v195, v151\n"
"v_mfma_f32_16x16x32_bf16 v[164:167], a[48:51], v[144:147], v[164:167]\n"
"buffer_load_dword v251, v10, s[4:7], s97 offen offset:512 sc1\n"
"v_mfma_f32_16x16x32_bf16 v[184:187], a[52:55], v[144:147], v[184:187]\n"
"v_mul_f32_e32 v172, v172, v255\n"
"v_mul_f32_e32 v173, v173, v255\n"
"v_mfma_f32_16x16x32_bf16 v[200:203], a[56:59], v[144:147], v[200:203]\n"
"v_mul_f32_e32 v174, v174, v255\n"
"v_mul_f32_e32 v175, v175, v255\n"
"v_mfma_f32_16x16x32_bf16 v[204:207], a[60:63], v[144:147], v[204:207]\n"
"buffer_load_dword v250, v10, s[4:7], s97 offen offset:768 sc1\n"
"v_mfma_f32_16x16x32_bf16 v[212:215], a[64:67], v[144:147], v[212:215]\n"
"buffer_load_dwordx4 v[124:127], v0, s[4:7], s96 offen offset:2048 sc1\n"
"v_mfma_f32_16x16x32_bf16 v[224:227], a[68:71], v[144:147], v[224:227]\n"
"v_mul_f32_e32 v192, v192, v255\n"
"v_mul_f32_e32 v193, v193, v255\n"
"v_mfma_f32_16x16x32_bf16 v[164:167], a[72:75], v[152:155], v[164:167]\n"
"buffer_load_dword v249, v10, s[4:7], s97 offen offset:1024 sc1\n"
"v_mfma_f32_16x16x32_bf16 v[184:187], a[76:79], v[152:155], v[184:187]\n"
"v_mul_f32_e32 v194, v194, v255\n"
"v_mul_f32_e32 v195, v195, v255\n"
"v_mfma_f32_16x16x32_bf16 v[200:203], a[80:83], v[152:155], v[200:203]\n"
"v_cvt_pk_bf16_f32 v144, v172, v173\n"
"v_cvt_pk_bf16_f32 v145, v174, v175\n"
"v_mfma_f32_16x16x32_bf16 v[204:207], a[84:87], v[152:155], v[204:207]\n"
"buffer_load_dword v248, v10, s[4:7], s97 offen offset:1280 sc1\n"
"v_mfma_f32_16x16x32_bf16 v[212:215], a[88:91], v[152:155], v[212:215]\n"
"buffer_load_dwordx4 v[148:151], v0, s[4:7], s96 offen offset:3072 sc1\n"
"v_mfma_f32_16x16x32_bf16 v[224:227], a[92:95], v[152:155], v[224:227]\n"
"s_waitcnt vmcnt(24)\n"
"v_cvt_pk_bf16_f32 v146, v192, v193\n"
"v_cvt_pk_bf16_f32 v147, v194, v195\n"
"v_mfma_f32_16x16x32_bf16 v[164:167], a[96:99], v[108:111], v[164:167]\n"
"buffer_load_dword v247, v10, s[4:7], s97 offen offset:1536 sc1\n"
"v_mfma_f32_16x16x32_bf16 v[184:187], a[100:103], v[108:111], v[184:187]\n"
"ds_write_b128 v7, v[144:147] offset:24576\n"
"v_mfma_f32_16x16x32_bf16 v[200:203], a[104:107], v[108:111], v[200:203]\n"
"v_add_f32_e32 v128, v128, v180\n"
"v_add_f32_e32 v129, v129, v181\n"
"v_mfma_f32_16x16x32_bf16 v[204:207], a[108:111], v[108:111], v[204:207]\n"
"buffer_load_dword v246, v10, s[4:7], s97 offen offset:1792 sc1\n"
"v_mfma_f32_16x16x32_bf16 v[212:215], a[112:115], v[108:111], v[212:215]\n"
"buffer_load_dwordx4 v[144:147], v1, s[4:7], s96 offen sc1\n"
"v_mfma_f32_16x16x32_bf16 v[224:227], a[116:119], v[108:111], v[224:227]\n"
"v_add_f32_e32 v130, v130, v182\n"
"v_add_f32_e32 v131, v131, v183\n"
"v_mfma_f32_16x16x32_bf16 v[164:167], a[120:123], v[88:91], v[164:167]\n"
"v_add_f32_e32 v132, v132, v188\n"
"v_add_f32_e32 v133, v133, v189\n"
"v_mfma_f32_16x16x32_bf16 v[184:187], a[124:127], v[88:91], v[184:187]\n"
"v_add_f32_e32 v134, v134, v190\n"
"v_add_f32_e32 v135, v135, v191\n"
"v_mfma_f32_16x16x32_bf16 v[200:203], v[24:27], v[88:91], v[200:203]\n"
"buffer_load_dwordx4 a[156:159], v1, s[4:7], s98 offen offset:3072 sc1\n"
"v_mfma_f32_16x16x32_bf16 v[204:207], v[28:31], v[88:91], v[204:207]\n"
"v_mul_f32_e32 v128, v128, v255\n"
"v_mul_f32_e32 v129, v129, v255\n"
"v_mfma_f32_16x16x32_bf16 v[212:215], v[32:35], v[88:91], v[212:215]\n"
"buffer_load_dwordx4 v[108:111], v1, s[4:7], s96 offen offset:1024 sc1\n"
"v_mfma_f32_16x16x32_bf16 v[224:227], v[36:39], v[88:91], v[224:227]\n"
"s_waitcnt vmcnt(27)\n"
"v_mul_f32_e32 v130, v130, v255\n"
"v_mul_f32_e32 v131, v131, v255\n"
"v_mfma_f32_16x16x32_bf16 v[164:167], v[40:43], v[92:95], v[164:167]\n"
"v_mul_f32_e32 v132, v132, v255\n"
"v_mul_f32_e32 v133, v133, v255\n"
"v_mfma_f32_16x16x32_bf16 v[184:187], v[44:47], v[92:95], v[184:187]\n"
"v_mul_f32_e32 v134, v134, v255\n"
"v_mul_f32_e32 v135, v135, v255\n"
"v_mfma_f32_16x16x32_bf16 v[200:203], v[48:51], v[92:95], v[200:203]\n"
"v_cvt_pk_bf16_f32 v88, v128, v129\n"
"v_cvt_pk_bf16_f32 v89, v130, v131\n"
"v_mfma_f32_16x16x32_bf16 v[204:207], v[52:55], v[92:95], v[204:207]\n"
"v_cvt_pk_bf16_f32 v90, v132, v133\n"
"v_cvt_pk_bf16_f32 v91, v134, v135\n"
"v_mfma_f32_16x16x32_bf16 v[212:215], v[56:59], v[92:95], v[212:215]\n"
"buffer_load_dwordx4 v[128:131], v1, s[4:7], s96 offen offset:2048 sc1\n"
"v_mfma_f32_16x16x32_bf16 v[224:227], v[60:63], v[92:95], v[224:227]\n"
"ds_write_b128 v7, v[88:91] offset:57344\n"
"v_mfma_f32_16x16x32_bf16 v[164:167], v[64:67], v[96:99], v[164:167]\n"
"v_add_f32_e32 v160, v160, v140\n"
"v_add_f32_e32 v161, v161, v141\n"
"v_mfma_f32_16x16x32_bf16 v[184:187], v[68:71], v[96:99], v[184:187]\n"
"v_add_f32_e32 v162, v162, v142\n"
"v_add_f32_e32 v163, v163, v143\n"
"v_mfma_f32_16x16x32_bf16 v[200:203], v[72:75], v[96:99], v[200:203]\n"
"v_add_f32_e32 v168, v168, v196\n"
"v_add_f32_e32 v169, v169, v197\n"
"v_mfma_f32_16x16x32_bf16 v[204:207], v[76:79], v[96:99], v[204:207]\n"
"v_add_f32_e32 v170, v170, v198\n"
"v_add_f32_e32 v171, v171, v199\n"
"v_mfma_f32_16x16x32_bf16 v[212:215], v[80:83], v[96:99], v[212:215]\n"
"buffer_load_dwordx4 v[88:91], v1, s[4:7], s96 offen offset:3072 sc1\n"
"v_mfma_f32_16x16x32_bf16 v[224:227], v[84:87], v[96:99], v[224:227]\n"
"s_waitcnt vmcnt(25)\n"
"v_cmp_u_f32_e32 vcc, v164, v164\n"
"v_mul_f32_e32 v160, v160, v254\n"
"; @B G1 1.1\n"
"v_mfma_f32_16x16x32_bf16 v[92:95], a[0:3], v[100:103], 0\n"
"s_cbranch_vccnz .Lmk_p56s\n"
".Lmk_p56r:\n"
"v_mul_f32_e32 v161, v161, v254\n"
"v_mfma_f32_16x16x32_bf16 v[96:99], a[4:7], v[100:103], 0\n"
"v_mul_f32_e32 v162, v162, v254\n"
"v_mul_f32_e32 v163, v163, v254\n"
"v_mfma_f32_16x16x32_bf16 v[132:135], a[8:11], v[100:103], 0\n"
"v_mul_f32_e32 v168, v168, v254\n"
"v_mul_f32_e32 v169, v169, v254\n"
"v_mfma_f32_16x16x32_bf16 v[140:143], a[12:15], v[100:103], 0\n"
"ds_write_b128 v3, v[164:167]\n"
"v_mfma_f32_16x16x32_bf16 v[152:155], a[16:19], v[100:103], 0\n"
"s_add_u32 s98, s55, 0x448000\n"
"buffer_load_dwordx4 v[164:167], v0, s[4:7], s98 offen sc1\n"
"v_mfma_f32_16x16x32_bf16 v[172:175], a[20:23], v[100:103], 0\n"
"v_mul_f32_e32 v170, v170, v254\n"
"v_mul_f32_e32 v171, v171, v254\n"
"v_mfma_f32_16x16x32_bf16 v[92:95], a[24:27], v[104:107], v[92:95]\n"
"ds_write_b128 v3, v[184:187] offset:1024\n"
"v_mfma_f32_16x16x32_bf16 v[96:99], a[28:31], v[104:107], v[96:99]\n"
"s_add_u32 s97, s70, 0x202000\n"
"v_cvt_pk_bf16_f32 v100, v160, v161\n"
"v_cvt_pk_bf16_f32 v101, v162, v163\n"
"v_mfma_f32_16x16x32_bf16 v[132:135], a[32:35], v[104:107], v[132:135]\n"
"buffer_load_dwordx4 a[160:163], v0, s[4:7], s97 offen sc1\n"
"v_mfma_f32_16x16x32_bf16 v[140:143], a[36:39], v[104:107], v[140:143]\n"
"ds_write_b128 v3, v[200:203] offset:2048\n"
"v_mfma_f32_16x16x32_bf16 v[152:155], a[40:43], v[104:107], v[152:155]\n"
"buffer_load_dwordx4 v[160:163], v0, s[4:7], s98 offen offset:1024 sc1\n"
"v_mfma_f32_16x16x32_bf16 v[172:175], a[44:47], v[104:107], v[172:175]\n"
"s_waitcnt vmcnt(26)\n"
"v_cvt_pk_bf16_f32 v102, v168, v169\n"
"v_cvt_pk_bf16_f32 v103, v170, v171\n"
"v_mfma_f32_16x16x32_bf16 v[92:95], a[48:51], v[112:115], v[92:95]\n"
"ds_write_b128 v3, v[204:207] offset:3072\n"
"v_mfma_f32_16x16x32_bf16 v[96:99], a[52:55], v[112:115], v[96:99]\n"
"s_add_u32 s96, s67, 0x10000\n"
"buffer_load_dwordx2 v[254:255], v12, s[4:7], s96 offen sc1\n"
"v_mfma_f32_16x16x32_bf16 v[132:135], a[56:59], v[112:115], v[132:135]\n"
"ds_write_b128 v2, v[100:103] offset:49152\n"
"v_mfma_f32_16x16x32_bf16 v[140:143], a[60:63], v[112:115], v[140:143]\n"
"ds_write_b128 v3, v[212:215] offset:4096\n"
"v_mfma_f32_16x16x32_bf16 v[152:155], a[64:67], v[112:115], v[152:155]\n"
"buffer_load_dwordx4 v[100:103], v0, s[4:7], s98 offen offset:2048 sc1\n"
"v_mfma_f32_16x16x32_bf16 v[172:175], a[68:71], v[112:115], v[172:175]\n"
"v_mfma_f32_16x16x32_bf16 v[92:95], a[72:75], v[116:119], v[92:95]\n"
"ds_write_b128 v3, v[224:227] offset:5120\n"
"v_mfma_f32_16x16x32_bf16 v[96:99], a[76:79], v[116:119], v[96:99]\n"
"v_mfma_f32_16x16x32_bf16 v[132:135], a[80:83], v[116:119], v[132:135]\n"
"v_mov_b32_e32 v104, -1\n"
"v_mov_b32_e32 v105, -1\n"
"v_mfma_f32_16x16x32_bf16 v[140:143], a[84:87], v[116:119], v[140:143]\n"
"v_mov_b32_e32 v106, -1\n"
"v_mov_b32_e32 v107, -1\n"
"v_mfma_f32_16x16x32_bf16 v[152:155], a[88:91], v[116:119], v[152:155]\n"
"buffer_load_dwordx4 v[112:115], v0, s[4:7], s98 offen offset:3072 sc1\n"
"v_mfma_f32_16x16x32_bf16 v[172:175], a[92:95], v[116:119], v[172:175]\n"
"s_waitcnt vmcnt(26)\n"
"s_add_u32 s95, s60, 0x400000\n"
"v_mfma_f32_16x16x32_bf16 v[92:95], a[96:99], v[120:123], v[92:95]\n"
"buffer_store_dwordx4 v[104:107], v0, s[4:7], s95 offen sc1\n"
"v_mfma_f32_16x16x32_bf16 v[96:99], a[100:103], v[120:123], v[96:99]\n"
"v_mfma_f32_16x16x32_bf16 v[132:135], a[104:107], v[120:123], v[132:135]\n"
"v_mfma_f32_16x16x32_bf16 v[140:143], a[108:111], v[120:123], v[140:143]\n"
"v_mfma_f32_16x16x32_bf16 v[152:155], a[112:115], v[120:123], v[152:155]\n"
"buffer_load_dwordx4 v[116:119], v1, s[4:7], s98 offen sc1\n"
"v_mfma_f32_16x16x32_bf16 v[172:175], a[116:119], v[120:123], v[172:175]\n"
"s_waitcnt lgkmcnt(0)\n"
"s_barrier\n"
"v_mfma_f32_16x16x32_bf16 v[92:95], a[120:123], v[176:179], v[92:95]\n"
"s_waitcnt vmcnt(13)\n"
"v_add_f32_e32 v253, v253, v249\n"
"v_add_f32_e32 v252, v252, v248\n"
"v_mfma_f32_16x16x32_bf16 v[96:99], a[124:127], v[176:179], v[96:99]\n"
"v_add_f32_e32 v251, v251, v247\n"
"v_add_f32_e32 v250, v250, v246\n"
"v_mfma_f32_16x16x32_bf16 v[132:135], v[24:27], v[176:179], v[132:135]\n"
"buffer_load_dwordx4 a[164:167], v0, s[4:7], s97 offen offset:1024 sc1\n"
"v_mfma_f32_16x16x32_bf16 v[140:143], v[28:31], v[176:179], v[140:143]\n"
"v_add_f32_e32 v253, v253, v251\n"
"v_add_f32_e32 v252, v252, v250\n"
"v_mfma_f32_16x16x32_bf16 v[152:155], v[32:35], v[176:179], v[152:155]\n"
"buffer_load_dwordx4 v[120:123], v1, s[4:7], s98 offen offset:1024 sc1\n"
"v_mfma_f32_16x16x32_bf16 v[172:175], v[36:39], v[176:179], v[172:175]\n"
"v_add_f32_e32 v253, v253, v252\n"
"v_cmp_u_f32_e32 vcc, v253, v253\n"
"v_mfma_f32_16x16x32_bf16 v[92:95], v[40:43], v[208:211], v[92:95]\n"
"s_cbranch_vccnz .Lmk_m61s\n"
".Lmk_m61r:\n"
"v_mov_b32_e32 v252, v253\n"
"v_mfma_f32_16x16x32_bf16 v[96:99], v[44:47], v[208:211], v[96:99]\n"
"s_nop 0\n"
"v_permlane32_swap_b32_e32 v253, v252\n"
"v_add_f32_e32 v253, v253, v252\n"
"v_mfma_f32_16x16x32_bf16 v[132:135], v[48:51], v[208:211], v[132:135]\n"
"v_mul_f32_e32 v253, 0x3a800000, v253\n"
"v_mov_b32_e32 v252, v253\n"
"v_mfma_f32_16x16x32_bf16 v[140:143], v[52:55], v[208:211], v[140:143]\n"
"s_nop 0\n"
"v_permlane16_swap_b32_e32 v253, v252\n"
"v_cvt_pk_bf16_f32 v251, -v253, 0\n"
"v_mfma_f32_16x16x32_bf16 v[152:155], v[56:59], v[208:211], v[152:155]\n"
"buffer_load_dwordx4 v[168:171], v1, s[4:7], s98 offen offset:2048 sc1\n"
"v_mfma_f32_16x16x32_bf16 v[172:175], v[60:63], v[208:211], v[172:175]\n"
"v_fma_f32 v252, -v253, v253, v252\n"
"v_lshlrev_b32_e32 v251, 16, v251\n"
"v_mfma_f32_16x16x32_bf16 v[92:95], v[64:67], v[156:159], v[92:95]\n"
"ds_read_b128 v[176:179], v4\n"
"ds_read_b128 v[180:183], v9 offset:512\n"
"v_mfma_f32_16x16x32_bf16 v[96:99], v[68:71], v[156:159], v[96:99]\n"
"ds_read_b128 v[184:187], v4 offset:1024\n"
"ds_read_b128 v[188:191], v9 offset:576\n"
"v_mfma_f32_16x16x32_bf16 v[132:135], v[72:75], v[156:159], v[132:135]\n"
"ds_read_b128 v[192:195], v4 offset:2048\n"
"ds_read_b128 v[196:199], v9 offset:640\n"
"v_mfma_f32_16x16x32_bf16 v[140:143], v[76:79], v[156:159], v[140:143]\n"
"v_max_f32_e32 v252, 0, v252\n"
"v_cvt_pk_bf16_f32 v252, v252, 0\n"
"v_mfma_f32_16x16x32_bf16 v[152:155], v[80:83], v[156:159], v[152:155]\n"
"buffer_load_dwordx4 v[200:203], v1, s[4:7], s98 offen offset:3072 sc1\n"
"v_mfma_f32_16x16x32_bf16 v[172:175], v[84:87], v[156:159], v[172:175]\n"
"v_cmp_u_f32_e32 vcc, v92, v92\n"
"v_lshlrev_b32_e32 v252, 16, v252\n"
"; @B G1 1.2\n"
"v_mfma_f32_16x16x32_bf16 v[156:159], a[0:3], v[220:223], 0\n"
"s_cbranch_vccnz .Lmk_p57s\n"
".Lmk_p57r:\n"
"v_add_f32_e32 v252, 0x3727c5ac, v252\n"
"v_mfma_f32_16x16x32_bf16 v[204:207], a[4:7], v[220:223], 0\n"
"s_waitcnt vmcnt(8)\n"
"v_cmp_ne_u64_e32 vcc, v[254:255], v[18:19]\n"
"v_mfma_f32_16x16x32_bf16 v[208:211], a[8:11], v[220:223], 0\n"
"s_cbranch_vccnz .Lmk_f46s\n"
".Lmk_f46r:\n"
"s_waitcnt lgkmcnt(0)\n"
"v_mfma_f32_16x16x32_bf16 v[212:215], a[12:15], v[220:223], 0\n"
"ds_write_b128 v3, v[92:95] offset:6144\n"
"v_mfma_f32_16x16x32_bf16 v[92:95], a[16:19], v[220:223], 0\n"
"s_add_u32 s98, s52, 0x408000\n"
"buffer_load_dwordx4 v[216:219], v0, s[4:7], s98 offen sc1\n"
"v_mfma_f32_16x16x32_bf16 v[224:227], a[20:23], v[220:223], 0\n"
"v_fmac_f32_e32 v176, v251, v180\n"
"v_fmac_f32_e32 v177, v251, v181\n"
"v_mfma_f32_16x16x32_bf16 v[156:159], a[24:27], v[136:139], v[156:159]\n"
"ds_write_b128 v3, v[96:99] offset:7168\n"
"v_mfma_f32_16x16x32_bf16 v[204:207], a[28:31], v[136:139], v[204:207]\n"
"v_fmac_f32_e32 v178, v251, v182\n"
"v_fmac_f32_e32 v179, v251, v183\n"
"v_mfma_f32_16x16x32_bf16 v[208:211], a[32:35], v[136:139], v[208:211]\n"
"buffer_load_dwordx4 a[168:171], v0, s[4:7], s97 offen offset:2048 sc1\n"
"v_mfma_f32_16x16x32_bf16 v[212:215], a[36:39], v[136:139], v[212:215]\n"
"ds_write_b128 v3, v[132:135] offset:8192\n"
"v_mfma_f32_16x16x32_bf16 v[92:95], a[40:43], v[136:139], v[92:95]\n"
"buffer_load_dwordx4 v[96:99], v0, s[4:7], s98 offen offset:1024 sc1\n"
"v_mfma_f32_16x16x32_bf16 v[224:227], a[44:47], v[136:139], v[224:227]\n"
"s_add_u32 s96, s60, 0x408000\n"
"v_fmac_f32_e32 v184, v251, v188\n"
"v_mfma_f32_16x16x32_bf16 v[156:159], a[48:51], v[124:127], v[156:159]\n"
"ds_write_b128 v3, v[140:143] offset:9216\n"
"v_mfma_f32_16x16x32_bf16 v[204:207], a[52:55], v[124:127], v[204:207]\n"
"buffer_store_dwordx4 v[104:107], v0, s[4:7], s96 offen sc1\n"
"v_mfma_f32_16x16x32_bf16 v[208:211], a[56:59], v[124:127], v[208:211]\n"
"v_fmac_f32_e32 v185, v251, v189\n"
"v_fmac_f32_e32 v186, v251, v190\n"
"v_mfma_f32_16x16x32_bf16 v[212:215], a[60:63], v[124:127], v[212:215]\n"
"ds_write_b128 v3, v[152:155] offset:10240\n"
"v_mfma_f32_16x16x32_bf16 v[92:95], a[64:67], v[124:127], v[92:95]\n"
"buffer_load_dwordx4 v[132:135], v0, s[4:7], s98 offen offset:2048 sc1\n"
"v_mfma_f32_16x16x32_bf16 v[224:227], a[68:71], v[124:127], v[224:227]\n"
"v_fmac_f32_e32 v187, v251, v191\n"
"v_fmac_f32_e32 v192, v251, v196\n"
"v_mfma_f32_16x16x32_bf16 v[156:159], a[72:75], v[148:151], v[156:159]\n"
"ds_write_b128 v3, v[172:175] offset:11264\n"
"v_mfma_f32_16x16x32_bf16 v[204:207], a[76:79], v[148:151], v[204:207]\n"
"v_fmac_f32_e32 v193, v251, v197\n"
"v_fmac_f32_e32 v194, v251, v198\n"
"v_mfma_f32_16x16x32_bf16 v[208:211], a[80:83], v[148:151], v[208:211]\n"
"ds_read_b128 v[124:127], v4 offset:3072\n"
"v_fmac_f32_e32 v195, v251, v199\n"
"v_mfma_f32_16x16x32_bf16 v[212:215], a[84:87], v[148:151], v[212:215]\n"
"ds_read_b128 v[136:139], v9 offset:704\n"
"ds_read_b128 v[140:143], v4 offset:4096\n"
"v_mfma_f32_16x16x32_bf16 v[92:95], a[88:91], v[148:151], v[92:95]\n"
"buffer_load_dwordx4 v[152:155], v0, s[4:7], s98 offen offset:3072 sc1\n"
"v_mfma_f32_16x16x32_bf16 v[224:227], a[92:95], v[148:151], v[224:227]\n"
"ds_read_b128 v[148:151], v9 offset:768\n"
"ds_read_b128 v[172:175], v4 offset:5120\n"
"v_mfma_f32_16x16x32_bf16 v[156:159], a[96:99], v[144:147], v[156:159]\n"
"ds_read_b128 v[180:183], v9 offset:832\n"
"v_cvt_pk_bf16_f32 v252, v252, 0\n"
"v_mfma_f32_16x16x32_bf16 v[204:207], a[100:103], v[144:147], v[204:207]\n"
"v_lshlrev_b32_e32 v252, 16, v252\n"
"v_mfma_f32_16x16x32_bf16 v[208:211], a[104:107], v[144:147], v[208:211]\n"
"v_sqrt_f32_e32 v252, v252\n"
"v_mfma_f32_16x16x32_bf16 v[212:215], a[108:111], v[144:147], v[212:215]\n"
"v_cvt_pk_bf16_f32 v252, v252, 0\n"
"v_lshlrev_b32_e32 v252, 16, v252\n"
"v_mfma_f32_16x16x32_bf16 v[92:95], a[112:115], v[144:147], v[92:95]\n"
"buffer_load_dwordx4 v[188:191], v1, s[4:7], s98 offen sc1\n"
"v_mfma_f32_16x16x32_bf16 v[224:227], a[116:119], v[144:147], v[224:227]\n"
"s_waitcnt lgkmcnt(0)\n"
"v_fmac_f32_e32 v124, v251, v136\n"
"v_fmac_f32_e32 v125, v251, v137\n"
"v_mfma_f32_16x16x32_bf16 v[156:159], a[120:123], v[108:111], v[156:159]\n"
"v_fmac_f32_e32 v126, v251, v138\n"
"v_fmac_f32_e32 v127, v251, v139\n"
"v_mfma_f32_16x16x32_bf16 v[204:207], a[124:127], v[108:111], v[204:207]\n"
"v_fmac_f32_e32 v140, v251, v148\n"
"v_fmac_f32_e32 v141, v251, v149\n"
"v_mfma_f32_16x16x32_bf16 v[208:211], v[24:27], v[108:111], v[208:211]\n"
"buffer_load_dwordx4 a[172:175], v0, s[4:7], s97 offen offset:3072 sc1\n"
"v_mfma_f32_16x16x32_bf16 v[212:215], v[28:31], v[108:111], v[212:215]\n"
"v_fmac_f32_e32 v142, v251, v150\n"
"v_fmac_f32_e32 v143, v251, v151\n"
"v_mfma_f32_16x16x32_bf16 v[92:95], v[32:35], v[108:111], v[92:95]\n"
"buffer_load_dwordx4 v[136:139], v1, s[4:7], s98 offen offset:1024 sc1\n"
"v_mfma_f32_16x16x32_bf16 v[224:227], v[36:39], v[108:111], v[224:227]\n"
"v_fmac_f32_e32 v172, v251, v180\n"
"v_fmac_f32_e32 v173, v251, v181\n"
"v_mfma_f32_16x16x32_bf16 v[156:159], v[40:43], v[128:131], v[156:159]\n"
"v_fmac_f32_e32 v174, v251, v182\n"
"v_fmac_f32_e32 v175, v251, v183\n"
"v_mfma_f32_16x16x32_bf16 v[204:207], v[44:47], v[128:131], v[204:207]\n"
"v_rcp_f32_e32 v255, v252\n"
"v_mfma_f32_16x16x32_bf16 v[208:211], v[48:51], v[128:131], v[208:211]\n"
"v_mul_f32_e32 v254, 0x3e8293ee, v255\n"
"v_mfma_f32_16x16x32_bf16 v[212:215], v[52:55], v[128:131], v[212:215]\n"
"v_mfma_f32_16x16x32_bf16 v[92:95], v[56:59], v[128:131], v[92:95]\n"
"v_max_u32_e32 v253, v164, v165\n"
"v_max3_u32 v253, v253, v166, v167\n"
"v_mfma_f32_16x16x32_bf16 v[224:227], v[60:63], v[128:131], v[224:227]\n"
"s_waitcnt vmcnt(15)\n"
"v_max3_u32 v253, v253, v160, v161\n"
"v_max3_u32 v253, v253, v162, v163\n"
"v_mfma_f32_16x16x32_bf16 v[156:159], v[64:67], v[88:91], v[156:159]\n"
"v_max3_u32 v253, v253, v100, v101\n"
"v_max3_u32 v253, v253, v102, v103\n"
"v_mfma_f32_16x16x32_bf16 v[204:207], v[68:71], v[88:91], v[204:207]\n"
"v_max3_u32 v253, v253, v112, v113\n"
"v_max3_u32 v253, v253, v114, v115\n"
"v_mfma_f32_16x16x32_bf16 v[208:211], v[72:75], v[88:91], v[208:211]\n"
"v_cmp_eq_u32_e32 vcc, -1, v253\n"
"v_mfma_f32_16x16x32_bf16 v[212:215], v[76:79], v[88:91], v[212:215]\n"
"s_cbranch_vccnz .Lmk_q59s\n"
".Lmk_q59r:\n"
"v_mfma_f32_16x16x32_bf16 v[92:95], v[80:83], v[88:91], v[92:95]\n"
"buffer_load_dwordx4 v[108:111], v1, s[4:7], s98 offen offset:2048 sc1\n"
"v_mfma_f32_16x16x32_bf16 v[224:227], v[84:87], v[88:91], v[224:227]\n"
"v_cmp_u_f32_e32 vcc, v156, v156\n"
"; @B G1 1.3\n"
"v_mfma_f32_16x16x32_bf16 v[176:179], a[0:3], v[164:167], v[176:179]\n"
"s_cbranch_vccnz .Lmk_p58s\n"
".Lmk_p58r:\n"
"s_add_u32 s96, s61, 0x840000\n"
"v_mfma_f32_16x16x32_bf16 v[184:187], a[4:7], v[164:167], v[184:187]\n"
"buffer_load_dwordx4 v[88:91], v1, s[4:7], s98 offen offset:3072 sc1\n"
"v_mfma_f32_16x16x32_bf16 v[192:195], a[8:11], v[164:167], v[192:195]\n"
"buffer_store_dwordx4 v[104:107], v0, s[8:11], s96 offen sc1\n"
"v_mfma_f32_16x16x32_bf16 v[124:127], a[12:15], v[164:167], v[124:127]\n"
"ds_write_b128 v3, v[156:159] offset:12288\n"
"v_mfma_f32_16x16x32_bf16 v[140:143], a[16:19], v[164:167], v[140:143]\n"
"s_add_u32 s98, s53, 0x408000\n"
"buffer_load_dwordx4 v[128:131], v0, s[4:7], s98 offen sc1\n"
"v_mfma_f32_16x16x32_bf16 v[172:175], a[20:23], v[164:167], v[172:175]\n"
"v_mfma_f32_16x16x32_bf16 v[176:179], a[24:27], v[160:163], v[176:179]\n"
"ds_write_b128 v3, v[204:207] offset:13312\n"
"v_mfma_f32_16x16x32_bf16 v[184:187], a[28:31], v[160:163], v[184:187]\n"
"v_mfma_f32_16x16x32_bf16 v[192:195], a[32:35], v[160:163], v[192:195]\n"
"buffer_load_dwordx4 a[176:179], v1, s[4:7], s97 offen sc1\n"
"v_mfma_f32_16x16x32_bf16 v[124:127], a[36:39], v[160:163], v[124:127]\n"
"ds_write_b128 v3, v[208:211] offset:14336\n"
"v_mfma_f32_16x16x32_bf16 v[140:143], a[40:43], v[160:163], v[140:143]\n"
"buffer_load_dwordx4 v[144:147], v0, s[4:7], s98 offen offset:1024 sc1\n"
"v_mfma_f32_16x16x32_bf16 v[172:175], a[44:47], v[160:163], v[172:175]\n"
"v_mfma_f32_16x16x32_bf16 v[176:179], a[48:51], v[100:103], v[176:179]\n"
"ds_write_b128 v3, v[212:215] offset:15360\n"
"v_mfma_f32_16x16x32_bf16 v[184:187], a[52:55], v[100:103], v[184:187]\n"
"v_mfma_f32_16x16x32_bf16 v[192:195], a[56:59], v[100:103], v[192:195]\n"
"v_mfma_f32_16x16x32_bf16 v[124:127], a[60:63], v[100:103], v[124:127]\n"
"ds_write_b128 v3, v[92:95] offset:16384\n"
"v_mfma_f32_16x16x32_bf16 v[140:143], a[64:67], v[100:103], v[140:143]\n"
"s_waitcnt vmcnt(17)\n"
"v_max_u32_e32 v253, v116, v117\n"
"v_max3_u32 v253, v253, v118, v119\n"
"v_mfma_f32_16x16x32_bf16 v[172:175], a[68:71], v[100:103], v[172:175]\n"
"s_waitcnt vmcnt(15)\n"
"v_max3_u32 v253, v253, v120, v121\n"
"v_max3_u32 v253, v253, v122, v123\n"
"v_mfma_f32_16x16x32_bf16 v[176:179], a[72:75], v[112:115], v[176:179]\n"
"v_max3_u32 v253, v253, v168, v169\n"
"v_max3_u32 v253, v253, v170, v171\n"
"v_mfma_f32_16x16x32_bf16 v[184:187], a[76:79], v[112:115], v[184:187]\n"
"v_max3_u32 v253, v253, v200, v201\n"
"v_max3_u32 v253, v253, v202, v203\n"
"v_mfma_f32_16x16x32_bf16 v[192:195], a[80:83], v[112:115], v[192:195]\n"
"v_cmp_eq_u32_e32 vcc, -1, v253\n"
"v_mfma_f32_16x16x32_bf16 v[124:127], a[84:87], v[112:115], v[124:127]\n"
"s_cbranch_vccnz .Lmk_q60s\n"
".Lmk_q60r:\n"
"v_mfma_f32_16x16x32_bf16 v[140:143], a[88:91], v[112:115], v[140:143]\n"
"buffer_load_dwordx4 v[92:95], v0, s[4:7], s98 offen offset:2048 sc1\n"
"v_mfma_f32_16x16x32_bf16 v[172:175], a[92:95], v[112:115], v[172:175]\n"
"buffer_load_dwordx4 v[100:103], v0, s[4:7], s98 offen offset:3072 sc1\n"
"v_mfma_f32_16x16x32_bf16 v[176:179], a[96:99], v[116:119], v[176:179]\n"
"ds_write_b128 v3, v[224:227] offset:17408\n"
"v_mfma_f32_16x16x32_bf16 v[184:187], a[100:103], v[116:119], v[184:187]\n"
"v_mfma_f32_16x16x32_bf16 v[192:195], a[104:107], v[116:119], v[192:195]\n"
"v_mfma_f32_16x16x32_bf16 v[124:127], a[108:111], v[116:119], v[124:127]\n"
"s_waitcnt lgkmcnt(0)\n"
"s_barrier\n"
"v_mfma_f32_16x16x32_bf16 v[140:143], a[112:115], v[116:119], v[140:143]\n"
"buffer_load_dwordx4 v[112:115], v1, s[4:7], s98 offen sc1\n"
"v_mfma_f32_16x16x32_bf16 v[172:175], a[116:119], v[116:119], v[172:175]\n"
"ds_read_b128 v[116:119], v5 offset:2048\n"
"ds_read_b128 v[148:151], v6 offset:2048\n"
"v_mfma_f32_16x16x32_bf16 v[176:179], a[120:123], v[120:123], v[176:179]\n"
"ds_read_b128 v[156:159], v5 offset:3072\n"
"ds_read_b128 v[160:163], v6 offset:3072\n"
"v_mfma_f32_16x16x32_bf16 v[184:187], a[124:127], v[120:123], v[184:187]\n"
"v_mfma_f32_16x16x32_bf16 v[192:195], v[24:27], v[120:123], v[192:195]\n"
"buffer_load_dwordx4 a[180:183], v1, s[4:7], s97 offen offset:1024 sc1\n"
"v_mfma_f32_16x16x32_bf16 v[124:127], v[28:31], v[120:123], v[124:127]\n"
"ds_read_b128 v[164:167], v5 offset:4096\n"
"ds_read_b128 v[180:183], v6 offset:4096\n"
"v_mfma_f32_16x16x32_bf16 v[140:143], v[32:35], v[120:123], v[140:143]\n"
"buffer_load_dwordx4 v[196:199], v1, s[4:7], s98 offen offset:1024 sc1\n"
"v_mfma_f32_16x16x32_bf16 v[172:175], v[36:39], v[120:123], v[172:175]\n"
"ds_read_b128 v[120:123], v5 offset:5120\n"
"ds_read_b128 v[204:207], v6 offset:5120\n"
"v_mfma_f32_16x16x32_bf16 v[176:179], v[40:43], v[168:171], v[176:179]\n"
"s_waitcnt lgkmcnt(4)\n"
"v_add_f32_e32 v116, v116, v148\n"
"v_add_f32_e32 v117, v117, v149\n"
"v_mfma_f32_16x16x32_bf16 v[184:187], v[44:47], v[168:171], v[184:187]\n"
"s_add_u32 s96, s61, 0x840000\n"
"v_add_f32_e32 v118, v118, v150\n"
"v_add_f32_e32 v119, v119, v151\n"
"v_mfma_f32_16x16x32_bf16 v[192:195], v[48:51], v[168:171], v[192:195]\n"
"ds_read_b128 v[148:151], v5\n"
"ds_read_b128 v[208:211], v6\n"
"v_mfma_f32_16x16x32_bf16 v[124:127], v[52:55], v[168:171], v[124:127]\n"
"buffer_store_dwordx4 v[104:107], v0, s[8:11], s96 offen offset:1024 sc1\n"
"v_mfma_f32_16x16x32_bf16 v[140:143], v[56:59], v[168:171], v[140:143]\n"
"buffer_load_dwordx4 v[104:107], v1, s[4:7], s98 offen offset:2048 sc1\n"
"v_mfma_f32_16x16x32_bf16 v[172:175], v[60:63], v[168:171], v[172:175]\n"
"ds_read_b128 v[168:171], v5 offset:1024\n"
"ds_read_b128 v[212:215], v6 offset:1024\n"
"v_mfma_f32_16x16x32_bf16 v[176:179], v[64:67], v[200:203], v[176:179]\n"
"s_waitcnt lgkmcnt(4)\n"
"v_add_f32_e32 v156, v156, v160\n"
"v_add_f32_e32 v157, v157, v161\n"
"v_mfma_f32_16x16x32_bf16 v[184:187], v[68:71], v[200:203], v[184:187]\n"
"v_add_f32_e32 v158, v158, v162\n"
"v_add_f32_e32 v159, v159, v163\n"
"v_mfma_f32_16x16x32_bf16 v[192:195], v[72:75], v[200:203], v[192:195]\n"
"v_add_f32_e32 v164, v164, v180\n"
"v_add_f32_e32 v165, v165, v181\n"
"v_mfma_f32_16x16x32_bf16 v[124:127], v[76:79], v[200:203], v[124:127]\n"
"v_add_f32_e32 v166, v166, v182\n"
"v_add_f32_e32 v167, v167, v183\n"
"v_mfma_f32_16x16x32_bf16 v[140:143], v[80:83], v[200:203], v[140:143]\n"
"buffer_load_dwordx4 v[160:163], v1, s[4:7], s98 offen offset:3072 sc1\n"
"v_mfma_f32_16x16x32_bf16 v[172:175], v[84:87], v[200:203], v[172:175]\n"
"s_waitcnt lgkmcnt(0)\n"
"v_add_f32_e32 v120, v120, v204\n"
"v_add_f32_e32 v121, v121, v205\n"
"v_add_f32_e32 v122, v122, v206\n"
"v_add_f32_e32 v123, v123, v207\n"
"v_add_f32_e32 v148, v148, v208\n"
"v_add_f32_e32 v149, v149, v209\n"
"v_add_f32_e32 v150, v150, v210\n"
"v_add_f32_e32 v151, v151, v211\n"
"v_add_f32_e32 v168, v168, v212\n"
"v_add_f32_e32 v169, v169, v213\n"
"v_add_f32_e32 v170, v170, v214\n"
"v_add_f32_e32 v171, v171, v215\n"
".Lmk_b2:\n"
"s_waitcnt vmcnt(20)\n"
"; @B G1 2.0\n"
"v_mfma_f32_16x16x32_bf16 v[180:183], a[0:3], v[216:219], 0\n"
"v_mfma_f32_16x16x32_bf16 v[200:203], a[4:7], v[216:219], 0\n"
"s_add_u32 s98, s62, 0x40800\n"
"v_mfma_f32_16x16x32_bf16 v[204:207], a[8:11], v[216:219], 0\n"
"v_mfma_f32_16x16x32_bf16 v[208:211], a[12:15], v[216:219], 0\n"
"v_add_f32_e32 v192, v192, v116\n"
"v_add_f32_e32 v193, v193, v117\n"
"v_mfma_f32_16x16x32_bf16 v[212:215], a[16:19], v[216:219], 0\n"
"s_add_u32 s96, s54, 0x408000\n"
"buffer_load_dwordx4 v[220:223], v0, s[4:7], s96 offen sc1\n"
"v_mfma_f32_16x16x32_bf16 v[224:227], a[20:23], v[216:219], 0\n"
"buffer_load_dword v253, v10, s[4:7], s98 offen sc1\n"
"v_mfma_f32_16x16x32_bf16 v[180:183], a[24:27], v[96:99], v[180:183]\n"
"v_add_f32_e32 v194, v194, v118\n"
"v_add_f32_e32 v195, v195, v119\n"
"v_mfma_f32_16x16x32_bf16 v[200:203], a[28:31], v[96:99], v[200:203]\n"
"v_add_f32_e32 v124, v124, v156\n"
"v_add_f32_e32 v125, v125, v157\n"
"v_mfma_f32_16x16x32_bf16 v[204:207], a[32:35], v[96:99], v[204:207]\n"
"buffer_load_dwordx4 a[184:187], v1, s[4:7], s97 offen offset:2048 sc1\n"
"v_mfma_f32_16x16x32_bf16 v[208:211], a[36:39], v[96:99], v[208:211]\n"
"buffer_load_dword v252, v10, s[4:7], s98 offen offset:256 sc1\n"
"v_mfma_f32_16x16x32_bf16 v[212:215], a[40:43], v[96:99], v[212:215]\n"
"buffer_load_dwordx4 v[116:119], v0, s[4:7], s96 offen offset:1024 sc1\n"
"v_mfma_f32_16x16x32_bf16 v[224:227], a[44:47], v[96:99], v[224:227]\n"
"s_waitcnt vmcnt(22)\n"
"v_add_f32_e32 v126, v126, v158\n"
"v_add_f32_e32 v127, v127, v159\n"
"v_mfma_f32_16x16x32_bf16 v[180:183], a[48:51], v[132:135], v[180:183]\n"
"buffer_load_dword v251, v10, s[4:7], s98 offen offset:512 sc1\n"
"v_mfma_f32_16x16x32_bf16 v[200:203], a[52:55], v[132:135], v[200:203]\n"
"v_mul_f32_e32 v192, v192, v255\n"
"v_mul_f32_e32 v193, v193, v255\n"
"v_mfma_f32_16x16x32_bf16 v[204:207], a[56:59], v[132:135], v[204:207]\n"
"v_mul_f32_e32 v194, v194, v255\n"
"v_mul_f32_e32 v195, v195, v255\n"
"v_mfma_f32_16x16x32_bf16 v[208:211], a[60:63], v[132:135], v[208:211]\n"
"buffer_load_dword v250, v10, s[4:7], s98 offen offset:768 sc1\n"
"v_mfma_f32_16x16x32_bf16 v[212:215], a[64:67], v[132:135], v[212:215]\n"
"buffer_load_dwordx4 v[96:99], v0, s[4:7], s96 offen offset:2048 sc1\n"
"v_mfma_f32_16x16x32_bf16 v[224:227], a[68:71], v[132:135], v[224:227]\n"
"v_mul_f32_e32 v124, v124, v255\n"
"v_mul_f32_e32 v125, v125, v255\n"
"v_mfma_f32_16x16x32_bf16 v[180:183], a[72:75], v[152:155], v[180:183]\n"
"buffer_load_dword v249, v10, s[4:7], s98 offen offset:1024 sc1\n"
"v_mfma_f32_16x16x32_bf16 v[200:203], a[76:79], v[152:155], v[200:203]\n"
"v_mul_f32_e32 v126, v126, v255\n"
"v_mul_f32_e32 v127, v127, v255\n"
"v_mfma_f32_16x16x32_bf16 v[204:207], a[80:83], v[152:155], v[204:207]\n"
"v_cvt_pk_bf16_f32 v132, v192, v193\n"
"v_cvt_pk_bf16_f32 v133, v194, v195\n"
"v_mfma_f32_16x16x32_bf16 v[208:211], a[84:87], v[152:155], v[208:211]\n"
"buffer_load_dword v248, v10, s[4:7], s98 offen offset:1280 sc1\n"
"v_mfma_f32_16x16x32_bf16 v[212:215], a[88:91], v[152:155], v[212:215]\n"
"buffer_load_dwordx4 v[156:159], v0, s[4:7], s96 offen offset:3072 sc1\n"
"v_mfma_f32_16x16x32_bf16 v[224:227], a[92:95], v[152:155], v[224:227]\n"
"s_waitcnt vmcnt(25)\n"
"v_cvt_pk_bf16_f32 v134, v124, v125\n"
"v_cvt_pk_bf16_f32 v135, v126, v127\n"
"v_mfma_f32_16x16x32_bf16 v[180:183], a[96:99], v[188:191], v[180:183]\n"
"buffer_load_dword v247, v10, s[4:7], s98 offen offset:1536 sc1\n"
"v_mfma_f32_16x16x32_bf16 v[200:203], a[100:103], v[188:191], v[200:203]\n"
"ds_write_b128 v8, v[132:135] offset:25600\n"
"v_mfma_f32_16x16x32_bf16 v[204:207], a[104:107], v[188:191], v[204:207]\n"
"s_add_u32 s95, s64, 0x802000\n"
"buffer_store_dwordx4 v[132:135], v8, s[12:15], s95 offen offset:1024 sc1\n"
"v_mfma_f32_16x16x32_bf16 v[208:211], a[108:111], v[188:191], v[208:211]\n"
"buffer_load_dword v246, v10, s[4:7], s98 offen offset:1792 sc1\n"
"v_mfma_f32_16x16x32_bf16 v[212:215], a[112:115], v[188:191], v[212:215]\n"
"buffer_load_dwordx4 v[124:127], v1, s[4:7], s96 offen sc1\n"
"v_mfma_f32_16x16x32_bf16 v[224:227], a[116:119], v[188:191], v[224:227]\n"
"v_add_f32_e32 v140, v140, v164\n"
"v_add_f32_e32 v141, v141, v165\n"
"v_mfma_f32_16x16x32_bf16 v[180:183], a[120:123], v[136:139], v[180:183]\n"
"v_add_f32_e32 v142, v142, v166\n"
"v_add_f32_e32 v143, v143, v167\n"
"v_mfma_f32_16x16x32_bf16 v[200:203], a[124:127], v[136:139], v[200:203]\n"
"v_add_f32_e32 v172, v172, v120\n"
"v_add_f32_e32 v173, v173, v121\n"
"v_mfma_f32_16x16x32_bf16 v[204:207], v[24:27], v[136:139], v[204:207]\n"
"buffer_load_dwordx4 a[188:191], v1, s[4:7], s97 offen offset:3072 sc1\n"
"v_mfma_f32_16x16x32_bf16 v[208:211], v[28:31], v[136:139], v[208:211]\n"
"v_add_f32_e32 v174, v174, v122\n"
"v_add_f32_e32 v175, v175, v123\n"
"v_mfma_f32_16x16x32_bf16 v[212:215], v[32:35], v[136:139], v[212:215]\n"
"buffer_load_dwordx4 v[120:123], v1, s[4:7], s96 offen offset:1024 sc1\n"
"v_mfma_f32_16x16x32_bf16 v[224:227], v[36:39], v[136:139], v[224:227]\n"
"s_waitcnt vmcnt(29)\n"
"v_mul_f32_e32 v140, v140, v255\n"
"v_mul_f32_e32 v141, v141, v255\n"
"v_mfma_f32_16x16x32_bf16 v[180:183], v[40:43], v[108:111], v[180:183]\n"
"v_mul_f32_e32 v142, v142, v255\n"
"v_mul_f32_e32 v143, v143, v255\n"
"v_mfma_f32_16x16x32_bf16 v[200:203], v[44:47], v[108:111], v[200:203]\n"
"v_mul_f32_e32 v172, v172, v255\n"
"v_mul_f32_e32 v173, v173, v255\n"
"v_mfma_f32_16x16x32_bf16 v[204:207], v[48:51], v[108:111], v[204:207]\n"
"v_mul_f32_e32 v174, v174, v255\n"
"v_mul_f32_e32 v175, v175, v255\n"
"v_mfma_f32_16x16x32_bf16 v[208:211], v[52:55], v[108:111], v[208:211]\n"
"v_cvt_pk_bf16_f32 v132, v140, v141\n"
"v_cvt_pk_bf16_f32 v133, v142, v143\n"
"v_mfma_f32_16x16x32_bf16 v[212:215], v[56:59], v[108:111], v[212:215]\n"
"buffer_load_dwordx4 v[136:139], v1, s[4:7], s96 offen offset:2048 sc1\n"
"v_mfma_f32_16x16x32_bf16 v[224:227], v[60:63], v[108:111], v[224:227]\n"
"v_cvt_pk_bf16_f32 v134, v172, v173\n"
"v_cvt_pk_bf16_f32 v135, v174, v175\n"
"v_mfma_f32_16x16x32_bf16 v[180:183], v[64:67], v[88:91], v[180:183]\n"
"ds_write_b128 v7, v[132:135] offset:58368\n"
"v_mfma_f32_16x16x32_bf16 v[200:203], v[68:71], v[88:91], v[200:203]\n"
"s_add_u32 s98, s64, 0x806000\n"
"buffer_store_dwordx4 v[132:135], v7, s[12:15], s98 offen offset:1024 sc1\n"
"v_mfma_f32_16x16x32_bf16 v[204:207], v[72:75], v[88:91], v[204:207]\n"
"v_add_f32_e32 v176, v176, v148\n"
"v_add_f32_e32 v177, v177, v149\n"
"v_mfma_f32_16x16x32_bf16 v[208:211], v[76:79], v[88:91], v[208:211]\n"
"v_add_f32_e32 v178, v178, v150\n"
"v_add_f32_e32 v179, v179, v151\n"
"v_mfma_f32_16x16x32_bf16 v[212:215], v[80:83], v[88:91], v[212:215]\n"
"buffer_load_dwordx4 v[108:111], v1, s[4:7], s96 offen offset:3072 sc1\n"
"v_mfma_f32_16x16x32_bf16 v[224:227], v[84:87], v[88:91], v[224:227]\n"
"s_waitcnt vmcnt(28)\n"
"v_add_f32_e32 v184, v184, v168\n"
"v_add_f32_e32 v185, v185, v169\n"
"; @B G1 2.1\n"
"v_mfma_f32_16x16x32_bf16 v[88:91], a[0:3], v[128:131], 0\n"
"v_add_f32_e32 v186, v186, v170\n"
"v_add_f32_e32 v187, v187, v171\n"
"v_mfma_f32_16x16x32_bf16 v[132:135], a[4:7], v[128:131], 0\n"
"v_mul_f32_e32 v176, v176, v254\n"
"v_mul_f32_e32 v177, v177, v254\n"
"v_mfma_f32_16x16x32_bf16 v[140:143], a[8:11], v[128:131], 0\n"
"v_mul_f32_e32 v178, v178, v254\n"
"v_mul_f32_e32 v179, v179, v254\n"
"v_mfma_f32_16x16x32_bf16 v[148:151], a[12:15], v[128:131], 0\n"
"ds_write_b128 v3, v[180:183]\n"
"v_mfma_f32_16x16x32_bf16 v[152:155], a[16:19], v[128:131], 0\n"
"s_add_u32 s98, s55, 0x408000\n"
"buffer_load_dwordx4 v[164:167], v0, s[4:7], s98 offen sc1\n"
"v_mfma_f32_16x16x32_bf16 v[168:171], a[20:23], v[128:131], 0\n"
"v_mul_f32_e32 v184, v184, v254\n"
"v_mul_f32_e32 v185, v185, v254\n"
"v_mfma_f32_16x16x32_bf16 v[88:91], a[24:27], v[144:147], v[88:91]\n"
"ds_write_b128 v3, v[200:203] offset:1024\n"
"v_mfma_f32_16x16x32_bf16 v[132:135], a[28:31], v[144:147], v[132:135]\n"
"s_add_u32 s97, s70, 0x204000\n"
"v_mul_f32_e32 v186, v186, v254\n"
"v_mul_f32_e32 v187, v187, v254\n"
"v_mfma_f32_16x16x32_bf16 v[140:143], a[32:35], v[144:147], v[140:143]\n"
"buffer_load_dwordx4 a[192:195], v0, s[4:7], s97 offen sc1\n"
"v_mfma_f32_16x16x32_bf16 v[148:151], a[36:39], v[144:147], v[148:151]\n"
"ds_write_b128 v3, v[204:207] offset:2048\n"
"v_mfma_f32_16x16x32_bf16 v[152:155], a[40:43], v[144:147], v[152:155]\n"
"buffer_load_dwordx4 v[128:131], v0, s[4:7], s98 offen offset:1024 sc1\n"
"v_mfma_f32_16x16x32_bf16 v[168:171], a[44:47], v[144:147], v[168:171]\n"
"s_waitcnt vmcnt(29)\n"
"v_cvt_pk_bf16_f32 v144, v176, v177\n"
"v_cvt_pk_bf16_f32 v145, v178, v179\n"
"v_mfma_f32_16x16x32_bf16 v[88:91], a[48:51], v[92:95], v[88:91]\n"
"ds_write_b128 v3, v[208:211] offset:3072\n"
"v_mfma_f32_16x16x32_bf16 v[132:135], a[52:55], v[92:95], v[132:135]\n"
"v_cvt_pk_bf16_f32 v146, v184, v185\n"
"v_cvt_pk_bf16_f32 v147, v186, v187\n"
"v_mfma_f32_16x16x32_bf16 v[140:143], a[56:59], v[92:95], v[140:143]\n"
"ds_write_b128 v2, v[144:147] offset:50176\n"
"v_mfma_f32_16x16x32_bf16 v[148:151], a[60:63], v[92:95], v[148:151]\n"
"ds_write_b128 v3, v[212:215] offset:4096\n"
"v_mfma_f32_16x16x32_bf16 v[152:155], a[64:67], v[92:95], v[152:155]\n"
"buffer_load_dwordx4 v[144:147], v0, s[4:7], s98 offen offset:2048 sc1\n"
"v_mfma_f32_16x16x32_bf16 v[168:171], a[68:71], v[92:95], v[168:171]\n"
"v_mfma_f32_16x16x32_bf16 v[88:91], a[72:75], v[100:103], v[88:91]\n"
"ds_write_b128 v3, v[224:227] offset:5120\n"
"v_mfma_f32_16x16x32_bf16 v[132:135], a[76:79], v[100:103], v[132:135]\n"
"v_mfma_f32_16x16x32_bf16 v[140:143], a[80:83], v[100:103], v[140:143]\n"
"v_mov_b32_e32 v92, -1\n"
"v_mov_b32_e32 v93, -1\n"
"v_mfma_f32_16x16x32_bf16 v[148:151], a[84:87], v[100:103], v[148:151]\n"
"v_mov_b32_e32 v94, -1\n"
"v_mov_b32_e32 v95, -1\n"
"v_mfma_f32_16x16x32_bf16 v[152:155], a[88:91], v[100:103], v[152:155]\n"
"buffer_load_dwordx4 v[172:175], v0, s[4:7], s98 offen offset:3072 sc1\n"
"v_mfma_f32_16x16x32_bf16 v[168:171], a[92:95], v[100:103], v[168:171]\n"
"s_waitcnt vmcnt(28)\n"
"s_add_u32 s96, s63, 0x84000\n"
"v_mfma_f32_16x16x32_bf16 v[88:91], a[96:99], v[112:115], v[88:91]\n"
"buffer_store_dword v92, v14, s[8:11], s96 offen sc1\n"
"v_mfma_f32_16x16x32_bf16 v[132:135], a[100:103], v[112:115], v[132:135]\n"
"v_mfma_f32_16x16x32_bf16 v[140:143], a[104:107], v[112:115], v[140:143]\n"
"v_mfma_f32_16x16x32_bf16 v[148:151], a[108:111], v[112:115], v[148:151]\n"
"v_mfma_f32_16x16x32_bf16 v[152:155], a[112:115], v[112:115], v[152:155]\n"
"buffer_load_dwordx4 v[100:103], v1, s[4:7], s98 offen sc1\n"
"v_mfma_f32_16x16x32_bf16 v[168:171], a[116:119], v[112:115], v[168:171]\n"
"s_waitcnt lgkmcnt(0)\n"
"s_barrier\n"
"v_mfma_f32_16x16x32_bf16 v[88:91], a[120:123], v[196:199], v[88:91]\n"
"s_waitcnt vmcnt(13)\n"
"v_add_f32_e32 v253, v253, v249\n"
"v_add_f32_e32 v252, v252, v248\n"
"v_mfma_f32_16x16x32_bf16 v[132:135], a[124:127], v[196:199], v[132:135]\n"
"v_add_f32_e32 v251, v251, v247\n"
"v_add_f32_e32 v250, v250, v246\n"
"v_mfma_f32_16x16x32_bf16 v[140:143], v[24:27], v[196:199], v[140:143]\n"
"buffer_load_dwordx4 a[196:199], v0, s[4:7], s97 offen offset:1024 sc1\n"
"v_mfma_f32_16x16x32_bf16 v[148:151], v[28:31], v[196:199], v[148:151]\n"
"v_add_f32_e32 v253, v253, v251\n"
"v_add_f32_e32 v252, v252, v250\n"
"v_mfma_f32_16x16x32_bf16 v[152:155], v[32:35], v[196:199], v[152:155]\n"
"buffer_load_dwordx4 v[112:115], v1, s[4:7], s98 offen offset:1024 sc1\n"
"v_mfma_f32_16x16x32_bf16 v[168:171], v[36:39], v[196:199], v[168:171]\n"
"v_add_f32_e32 v253, v253, v252\n"
"v_mov_b32_e32 v252, v253\n"
"v_mfma_f32_16x16x32_bf16 v[88:91], v[40:43], v[104:107], v[88:91]\n"
"s_nop 0\n"
"v_permlane32_swap_b32_e32 v253, v252\n"
"v_add_f32_e32 v253, v253, v252\n"
"v_mfma_f32_16x16x32_bf16 v[132:135], v[44:47], v[104:107], v[132:135]\n"
"v_mul_f32_e32 v253, 0x3a800000, v253\n"
"v_mov_b32_e32 v252, v253\n"
"v_mfma_f32_16x16x32_bf16 v[140:143], v[48:51], v[104:107], v[140:143]\n"
"s_nop 0\n"
"v_permlane16_swap_b32_e32 v253, v252\n"
"v_cvt_pk_bf16_f32 v255, -v253, 0\n"
"v_mfma_f32_16x16x32_bf16 v[148:151], v[52:55], v[104:107], v[148:151]\n"
"v_fma_f32 v252, -v253, v253, v252\n"
"v_lshlrev_b32_e32 v255, 16, v255\n"
"v_mfma_f32_16x16x32_bf16 v[152:155], v[56:59], v[104:107], v[152:155]\n"
"buffer_load_dwordx4 v[176:179], v1, s[4:7], s98 offen offset:2048 sc1\n"
"v_mfma_f32_16x16x32_bf16 v[168:171], v[60:63], v[104:107], v[168:171]\n"
"ds_read_b128 v[104:107], v4\n"
"ds_read_b128 v[180:183], v9 offset:512\n"
"v_mfma_f32_16x16x32_bf16 v[88:91], v[64:67], v[160:163], v[88:91]\n"
"ds_read_b128 v[184:187], v4 offset:1024\n"
"ds_read_b128 v[188:191], v9 offset:576\n"
"v_mfma_f32_16x16x32_bf16 v[132:135], v[68:71], v[160:163], v[132:135]\n"
"ds_read_b128 v[192:195], v4 offset:2048\n"
"ds_read_b128 v[196:199], v9 offset:640\n"
"v_mfma_f32_16x16x32_bf16 v[140:143], v[72:75], v[160:163], v[140:143]\n"
"v_max_f32_e32 v252, 0, v252\n"
"v_cvt_pk_bf16_f32 v252, v252, 0\n"
"v_mfma_f32_16x16x32_bf16 v[148:151], v[76:79], v[160:163], v[148:151]\n"
"v_lshlrev_b32_e32 v252, 16, v252\n"
"v_add_f32_e32 v252, 0x3727c5ac, v252\n"
"v_mfma_f32_16x16x32_bf16 v[152:155], v[80:83], v[160:163], v[152:155]\n"
"buffer_load_dwordx4 v[200:203], v1, s[4:7], s98 offen offset:3072 sc1\n"
"v_mfma_f32_16x16x32_bf16 v[168:171], v[84:87], v[160:163], v[168:171]\n"
"v_cvt_pk_bf16_f32 v252, v252, 0\n"
"v_lshlrev_b32_e32 v252, 16, v252\n"
"; @B G1 2.2\n"
"v_mfma_f32_16x16x32_bf16 v[160:163], a[0:3], v[220:223], 0\n"
"s_waitcnt lgkmcnt(0)\n"
"v_fmac_f32_e32 v104, v255, v180\n"
"v_fmac_f32_e32 v105, v255, v181\n"
"v_mfma_f32_16x16x32_bf16 v[204:207], a[4:7], v[220:223], 0\n"
"v_fmac_f32_e32 v106, v255, v182\n"
"v_fmac_f32_e32 v107, v255, v183\n"
"v_mfma_f32_16x16x32_bf16 v[180:183], a[8:11], v[220:223], 0\n"
"v_fmac_f32_e32 v184, v255, v188\n"
"v_fmac_f32_e32 v185, v255, v189\n"
"v_mfma_f32_16x16x32_bf16 v[208:211], a[12:15], v[220:223], 0\n"
"ds_write_b128 v3, v[88:91] offset:6144\n"
"v_mfma_f32_16x16x32_bf16 v[88:91], a[16:19], v[220:223], 0\n"
"s_add_u32 s98, s52, 0x400000\n"
"buffer_load_dwordx4 v[212:215], v0, s[4:7], s98 offen sc1\n"
"v_mfma_f32_16x16x32_bf16 v[216:219], a[20:23], v[220:223], 0\n"
"v_fmac_f32_e32 v186, v255, v190\n"
"v_fmac_f32_e32 v187, v255, v191\n"
"v_mfma_f32_16x16x32_bf16 v[160:163], a[24:27], v[116:119], v[160:163]\n"
"ds_write_b128 v3, v[132:135] offset:7168\n"
"v_mfma_f32_16x16x32_bf16 v[204:207], a[28:31], v[116:119], v[204:207]\n"
"v_fmac_f32_e32 v192, v255, v196\n"
"v_fmac_f32_e32 v193, v255, v197\n"
"v_mfma_f32_16x16x32_bf16 v[180:183], a[32:35], v[116:119], v[180:183]\n"
"buffer_load_dwordx4 a[200:203], v0, s[4:7], s97 offen offset:2048 sc1\n"
"v_mfma_f32_16x16x32_bf16 v[208:211], a[36:39], v[116:119], v[208:211]\n"
"ds_write_b128 v3, v[140:143] offset:8192\n"
"v_mfma_f32_16x16x32_bf16 v[88:91], a[40:43], v[116:119], v[88:91]\n"
"buffer_load_dwordx4 v[132:135], v0, s[4:7], s98 offen offset:1024 sc1\n"
"v_mfma_f32_16x16x32_bf16 v[216:219], a[44:47], v[116:119], v[216:219]\n"
"v_fmac_f32_e32 v194, v255, v198\n"
"v_fmac_f32_e32 v195, v255, v199\n"
"v_mfma_f32_16x16x32_bf16 v[160:163], a[48:51], v[96:99], v[160:163]\n"
"ds_write_b128 v3, v[148:151] offset:9216\n"
"v_mfma_f32_16x16x32_bf16 v[204:207], a[52:55], v[96:99], v[204:207]\n"
"ds_read_b128 v[116:119], v4 offset:3072\n"
"ds_read_b128 v[140:143], v9 offset:704\n"
"v_mfma_f32_16x16x32_bf16 v[180:183], a[56:59], v[96:99], v[180:183]\n"
"ds_read_b128 v[148:151], v4 offset:4096\n"
"ds_read_b128 v[188:191], v9 offset:768\n"
"v_mfma_f32_16x16x32_bf16 v[208:211], a[60:63], v[96:99], v[208:211]\n"
"ds_write_b128 v3, v[152:155] offset:10240\n"
"v_mfma_f32_16x16x32_bf16 v[88:91], a[64:67], v[96:99], v[88:91]\n"
"buffer_load_dwordx4 v[152:155], v0, s[4:7], s98 offen offset:2048 sc1\n"
"v_mfma_f32_16x16x32_bf16 v[216:219], a[68:71], v[96:99], v[216:219]\n"
"ds_read_b128 v[96:99], v4 offset:5120\n"
"ds_read_b128 v[196:199], v9 offset:832\n"
"v_mfma_f32_16x16x32_bf16 v[160:163], a[72:75], v[156:159], v[160:163]\n"
"ds_write_b128 v3, v[168:171] offset:11264\n"
"v_mfma_f32_16x16x32_bf16 v[204:207], a[76:79], v[156:159], v[204:207]\n"
"v_sqrt_f32_e32 v252, v252\n"
"v_mfma_f32_16x16x32_bf16 v[180:183], a[80:83], v[156:159], v[180:183]\n"
"v_cvt_pk_bf16_f32 v252, v252, 0\n"
"v_lshlrev_b32_e32 v252, 16, v252\n"
"v_mfma_f32_16x16x32_bf16 v[208:211], a[84:87], v[156:159], v[208:211]\n"
"v_rcp_f32_e32 v254, v252\n"
"v_mfma_f32_16x16x32_bf16 v[88:91], a[88:91], v[156:159], v[88:91]\n"
"buffer_load_dwordx4 v[168:171], v0, s[4:7], s98 offen offset:3072 sc1\n"
"v_mfma_f32_16x16x32_bf16 v[216:219], a[92:95], v[156:159], v[216:219]\n"
"s_waitcnt vmcnt(19)\n"
"s_waitcnt lgkmcnt(1)\n"
"v_fmac_f32_e32 v116, v255, v140\n"
"v_mfma_f32_16x16x32_bf16 v[160:163], a[96:99], v[124:127], v[160:163]\n"
"v_fmac_f32_e32 v117, v255, v141\n"
"v_fmac_f32_e32 v118, v255, v142\n"
"v_mfma_f32_16x16x32_bf16 v[204:207], a[100:103], v[124:127], v[204:207]\n"
"v_fmac_f32_e32 v119, v255, v143\n"
"v_fmac_f32_e32 v148, v255, v188\n"
"v_mfma_f32_16x16x32_bf16 v[180:183], a[104:107], v[124:127], v[180:183]\n"
"v_fmac_f32_e32 v149, v255, v189\n"
"v_fmac_f32_e32 v150, v255, v190\n"
"v_mfma_f32_16x16x32_bf16 v[208:211], a[108:111], v[124:127], v[208:211]\n"
"v_fmac_f32_e32 v151, v255, v191\n"
"v_fmac_f32_e32 v96, v255, v196\n"
"v_mfma_f32_16x16x32_bf16 v[88:91], a[112:115], v[124:127], v[88:91]\n"
"buffer_load_dwordx4 v[140:143], v1, s[4:7], s98 offen sc1\n"
"v_mfma_f32_16x16x32_bf16 v[216:219], a[116:119], v[124:127], v[216:219]\n"
"v_fmac_f32_e32 v97, v255, v197\n"
"v_fmac_f32_e32 v98, v255, v198\n"
"v_mfma_f32_16x16x32_bf16 v[160:163], a[120:123], v[120:123], v[160:163]\n"
"s_add_u32 s96, s60, 0x848000\n"
"v_fmac_f32_e32 v99, v255, v199\n"
"v_mul_f32_e32 v255, 0x3e8293ee, v254\n"
"v_add_u32_e32 v253, 0x26c00, v0\n"
"v_mfma_f32_16x16x32_bf16 v[204:207], a[124:127], v[120:123], v[204:207]\n"
"ds_read_b128 v[124:127], v253\n"
"ds_read_b128 v[156:159], v253 offset:1024\n"
"v_xor_b32_e32 v253, 32, v15\n"
"v_mfma_f32_16x16x32_bf16 v[180:183], v[24:27], v[120:123], v[180:183]\n"
"buffer_store_dwordx4 v[92:95], v0, s[8:11], s96 offen sc1\n"
"v_mfma_f32_16x16x32_bf16 v[208:211], v[28:31], v[120:123], v[208:211]\n"
"buffer_load_dwordx4 a[204:207], v0, s[4:7], s97 offen offset:3072 sc1\n"
"ds_read_b128 v[188:191], v2 offset:50176\n"
"v_mfma_f32_16x16x32_bf16 v[88:91], v[32:35], v[120:123], v[88:91]\n"
"buffer_load_dwordx4 v[196:199], v1, s[4:7], s98 offen offset:1024 sc1\n"
"ds_read_b128 v[220:223], v15 offset:24576\n"
"v_mfma_f32_16x16x32_bf16 v[216:219], v[36:39], v[120:123], v[216:219]\n"
"s_waitcnt vmcnt(20)\n"
"s_add_u32 s96, s68, 0x11000\n"
"ds_read_b128 v[120:123], v253 offset:24832\n"
"v_mfma_f32_16x16x32_bf16 v[160:163], v[40:43], v[136:139], v[160:163]\n"
"buffer_store_dwordx2 v[18:19], v11, s[12:15], s96 offen offset:64 sc1\n"
"v_mfma_f32_16x16x32_bf16 v[204:207], v[44:47], v[136:139], v[204:207]\n"
"v_mfma_f32_16x16x32_bf16 v[180:183], v[48:51], v[136:139], v[180:183]\n"
"v_mfma_f32_16x16x32_bf16 v[208:211], v[52:55], v[136:139], v[208:211]\n"
"v_mfma_f32_16x16x32_bf16 v[88:91], v[56:59], v[136:139], v[88:91]\n"
"buffer_load_dwordx4 v[224:227], v1, s[4:7], s98 offen offset:2048 sc1\n"
"s_waitcnt lgkmcnt(0)\n"
"v_mfma_f32_16x16x32_bf16 v[124:127], v[220:223], v[188:191], v[124:127]\n"
"v_mfma_f32_16x16x32_bf16 v[156:159], v[120:123], v[188:191], v[156:159]\n"
"v_mfma_f32_16x16x32_bf16 v[216:219], v[60:63], v[136:139], v[216:219]\n"
"ds_read_b64_tr_b16 v[120:121], v16 offset:24576\n"
"v_xor_b32_e32 v253, 32, v16\n"
"v_mfma_f32_16x16x32_bf16 v[160:163], v[64:67], v[108:111], v[160:163]\n"
"ds_read_b64_tr_b16 v[122:123], v253 offset:24832\n"
"ds_read_b64_tr_b16 v[136:137], v253 offset:24576\n"
"ds_read_b64_tr_b16 v[138:139], v16 offset:24832\n"
"v_mov_b32_e32 v188, 0x3f803f80\n"
"v_mfma_f32_16x16x32_bf16 v[204:207], v[68:71], v[108:111], v[204:207]\n"
"v_mov_b32_e32 v189, 0x3f803f80\n"
"v_mov_b32_e32 v190, 0x3f803f80\n"
"v_mov_b32_e32 v191, 0x3f803f80\n"
"v_mfma_f32_16x16x32_bf16 v[180:183], v[72:75], v[108:111], v[180:183]\n"
"v_max_f32_e32 v253, v124, v125\n"
"v_max3_f32 v253, v253, v126, v127\n"
"v_max3_f32 v253, v253, v156, v157\n"
"v_mfma_f32_16x16x32_bf16 v[208:211], v[76:79], v[108:111], v[208:211]\n"
"v_max3_f32 v253, v253, v158, v159\n"
"v_mov_b32_e32 v252, v253\n"
"v_mfma_f32_16x16x32_bf16 v[88:91], v[80:83], v[108:111], v[88:91]\n"
"buffer_load_dwordx4 v[220:223], v1, s[4:7], s98 offen offset:3072 sc1\n"
"v_permlane16_swap_b32_e32 v253, v252\n"
"v_mfma_f32_16x16x32_bf16 v[216:219], v[84:87], v[108:111], v[216:219]\n"
"s_waitcnt vmcnt(20)\n"
"v_max_f32_e32 v253, v253, v252\n"
"v_mov_b32_e32 v252, v253\n"
"; @B G1 2.3\n"
"v_mfma_f32_16x16x32_bf16 v[104:107], a[0:3], v[164:167], v[104:107]\n"
"s_nop 0\n"
"v_permlane32_swap_b32_e32 v253, v252\n"
"v_max_f32_e32 v253, v253, v252\n"
"v_sub_f32_e32 v124, v124, v253\n"
"v_mfma_f32_16x16x32_bf16 v[184:187], a[4:7], v[164:167], v[184:187]\n"
"v_sub_f32_e32 v125, v125, v253\n"
"v_sub_f32_e32 v126, v126, v253\n"
"v_sub_f32_e32 v127, v127, v253\n"
"v_mfma_f32_16x16x32_bf16 v[192:195], a[8:11], v[164:167], v[192:195]\n"
"v_sub_f32_e32 v156, v156, v253\n"
"v_sub_f32_e32 v157, v157, v253\n"
"v_sub_f32_e32 v158, v158, v253\n"
"v_mfma_f32_16x16x32_bf16 v[116:119], a[12:15], v[164:167], v[116:119]\n"
"ds_write_b128 v3, v[160:163] offset:12288\n"
"v_mfma_f32_16x16x32_bf16 v[148:151], a[16:19], v[164:167], v[148:151]\n"
"s_add_u32 s98, s53, 0x400000\n"
"buffer_load_dwordx4 v[108:111], v0, s[4:7], s98 offen sc1\n"
"v_mfma_f32_16x16x32_bf16 v[96:99], a[20:23], v[164:167], v[96:99]\n"
"v_sub_f32_e32 v159, v159, v253\n"
"v_exp_f32_e32 v124, v124\n"
"v_mfma_f32_16x16x32_bf16 v[104:107], a[24:27], v[128:131], v[104:107]\n"
"ds_write_b128 v3, v[204:207] offset:13312\n"
"v_mfma_f32_16x16x32_bf16 v[184:187], a[28:31], v[128:131], v[184:187]\n"
"v_exp_f32_e32 v125, v125\n"
"v_mfma_f32_16x16x32_bf16 v[192:195], a[32:35], v[128:131], v[192:195]\n"
"v_exp_f32_e32 v126, v126\n"
"v_mfma_f32_16x16x32_bf16 v[116:119], a[36:39], v[128:131], v[116:119]\n"
"ds_write_b128 v3, v[180:183] offset:14336\n"
"v_mfma_f32_16x16x32_bf16 v[148:151], a[40:43], v[128:131], v[148:151]\n"
"buffer_load_dwordx4 v[160:163], v0, s[4:7], s98 offen offset:1024 sc1\n"
"v_mfma_f32_16x16x32_bf16 v[96:99], a[44:47], v[128:131], v[96:99]\n"
"s_waitcnt vmcnt(20)\n"
"buffer_load_dwordx4 a[208:211], v1, s[4:7], s97 offen sc1\n"
"v_mfma_f32_16x16x32_bf16 v[104:107], a[48:51], v[144:147], v[104:107]\n"
"ds_write_b128 v3, v[208:211] offset:15360\n"
"v_mfma_f32_16x16x32_bf16 v[184:187], a[52:55], v[144:147], v[184:187]\n"
"v_exp_f32_e32 v127, v127\n"
"v_mfma_f32_16x16x32_bf16 v[192:195], a[56:59], v[144:147], v[192:195]\n"
"v_exp_f32_e32 v156, v156\n"
"v_mfma_f32_16x16x32_bf16 v[116:119], a[60:63], v[144:147], v[116:119]\n"
"ds_write_b128 v3, v[88:91] offset:16384\n"
"v_mfma_f32_16x16x32_bf16 v[148:151], a[64:67], v[144:147], v[148:151]\n"
"buffer_load_dwordx4 v[88:91], v0, s[4:7], s98 offen offset:2048 sc1\n"
"v_mfma_f32_16x16x32_bf16 v[96:99], a[68:71], v[144:147], v[96:99]\n"
"v_exp_f32_e32 v157, v157\n"
"v_mfma_f32_16x16x32_bf16 v[104:107], a[72:75], v[172:175], v[104:107]\n"
"ds_write_b128 v3, v[216:219] offset:17408\n"
"v_mfma_f32_16x16x32_bf16 v[184:187], a[76:79], v[172:175], v[184:187]\n"
"v_exp_f32_e32 v158, v158\n"
"v_mfma_f32_16x16x32_bf16 v[192:195], a[80:83], v[172:175], v[192:195]\n"
"v_exp_f32_e32 v159, v159\n"
"v_cvt_pk_bf16_f32 v128, v124, v125\n"
"v_mfma_f32_16x16x32_bf16 v[116:119], a[84:87], v[172:175], v[116:119]\n"
"s_waitcnt lgkmcnt(0)\n"
"s_barrier\n"
"v_cvt_pk_bf16_f32 v129, v126, v127\n"
"v_mfma_f32_16x16x32_bf16 v[148:151], a[88:91], v[172:175], v[148:151]\n"
"buffer_load_dwordx4 v[124:127], v0, s[4:7], s98 offen offset:3072 sc1\n"
"v_cvt_pk_bf16_f32 v130, v156, v157\n"
"v_mfma_f32_16x16x32_bf16 v[96:99], a[92:95], v[172:175], v[96:99]\n"
"s_waitcnt vmcnt(19)\n"
"ds_read_b128 v[144:147], v5 offset:2048\n"
"ds_read_b128 v[164:167], v6 offset:2048\n"
"v_cvt_pk_bf16_f32 v131, v158, v159\n"
"v_mfma_f32_16x16x32_bf16 v[104:107], a[96:99], v[100:103], v[104:107]\n"
"ds_read_b128 v[156:159], v5 offset:3072\n"
"ds_read_b128 v[172:175], v6 offset:3072\n"
"v_mfma_f32_16x16x32_bf16 v[180:183], v[188:191], v[128:131], 0\n"
"v_mfma_f32_16x16x32_bf16 v[188:191], v[120:123], v[128:131], 0\n"
"v_mfma_f32_16x16x32_bf16 v[120:123], v[136:139], v[128:131], 0\n"
"v_mfma_f32_16x16x32_bf16 v[184:187], a[100:103], v[100:103], v[184:187]\n"
"v_mfma_f32_16x16x32_bf16 v[192:195], a[104:107], v[100:103], v[192:195]\n"
"v_mfma_f32_16x16x32_bf16 v[116:119], a[108:111], v[100:103], v[116:119]\n"
"ds_read_b128 v[128:131], v5 offset:4096\n"
"ds_read_b128 v[136:139], v6 offset:4096\n"
"v_mfma_f32_16x16x32_bf16 v[148:151], a[112:115], v[100:103], v[148:151]\n"
"buffer_load_dwordx4 v[204:207], v1, s[4:7], s98 offen sc1\n"
"v_mfma_f32_16x16x32_bf16 v[96:99], a[116:119], v[100:103], v[96:99]\n"
"ds_read_b128 v[100:103], v5 offset:5120\n"
"ds_read_b128 v[208:211], v6 offset:5120\n"
"v_mfma_f32_16x16x32_bf16 v[104:107], a[120:123], v[112:115], v[104:107]\n"
"s_waitcnt lgkmcnt(4)\n"
"v_add_f32_e32 v144, v144, v164\n"
"v_add_f32_e32 v145, v145, v165\n"
"v_mfma_f32_16x16x32_bf16 v[184:187], a[124:127], v[112:115], v[184:187]\n"
"v_add_f32_e32 v146, v146, v166\n"
"v_add_f32_e32 v147, v147, v167\n"
"v_mfma_f32_16x16x32_bf16 v[192:195], v[24:27], v[112:115], v[192:195]\n"
"ds_read_b128 v[164:167], v5\n"
"ds_read_b128 v[216:219], v6\n"
"v_mfma_f32_16x16x32_bf16 v[116:119], v[28:31], v[112:115], v[116:119]\n"
"ds_read_b128 v[228:231], v5 offset:1024\n"
"ds_read_b128 v[232:235], v6 offset:1024\n"
"v_mfma_f32_16x16x32_bf16 v[148:151], v[32:35], v[112:115], v[148:151]\n"
"buffer_load_dwordx4 v[236:239], v1, s[4:7], s98 offen offset:1024 sc1\n"
"v_mfma_f32_16x16x32_bf16 v[96:99], v[36:39], v[112:115], v[96:99]\n"
"s_waitcnt vmcnt(19)\n"
"buffer_load_dwordx4 a[212:215], v1, s[4:7], s97 offen offset:1024 sc1\n"
"v_mfma_f32_16x16x32_bf16 v[104:107], v[40:43], v[176:179], v[104:107]\n"
"s_waitcnt lgkmcnt(4)\n"
"v_add_f32_e32 v156, v156, v172\n"
"v_add_f32_e32 v157, v157, v173\n"
"v_mfma_f32_16x16x32_bf16 v[184:187], v[44:47], v[176:179], v[184:187]\n"
"v_add_f32_e32 v158, v158, v174\n"
"v_add_f32_e32 v159, v159, v175\n"
"v_mfma_f32_16x16x32_bf16 v[192:195], v[48:51], v[176:179], v[192:195]\n"
"s_add_u32 s96, s60, 0x840000\n"
"v_add_f32_e32 v128, v128, v136\n"
"v_add_f32_e32 v129, v129, v137\n"
"v_mfma_f32_16x16x32_bf16 v[116:119], v[52:55], v[176:179], v[116:119]\n"
"s_waitcnt lgkmcnt(0)\n"
"v_add_f32_e32 v130, v130, v138\n"
"v_add_f32_e32 v131, v131, v139\n"
"v_mfma_f32_16x16x32_bf16 v[148:151], v[56:59], v[176:179], v[148:151]\n"
"buffer_load_dwordx4 v[112:115], v1, s[4:7], s98 offen offset:2048 sc1\n"
"v_mfma_f32_16x16x32_bf16 v[96:99], v[60:63], v[176:179], v[96:99]\n"
"buffer_store_dwordx4 v[92:95], v0, s[8:11], s96 offen sc1\n"
"v_mfma_f32_16x16x32_bf16 v[104:107], v[64:67], v[200:203], v[104:107]\n"
"s_add_u32 s96, s68, 0x10000\n"
"v_add_f32_e32 v100, v100, v208\n"
"v_mfma_f32_16x16x32_bf16 v[184:187], v[68:71], v[200:203], v[184:187]\n"
"buffer_store_dwordx2 v[18:19], v11, s[4:7], s96 offen offset:32 sc1\n"
"v_mfma_f32_16x16x32_bf16 v[192:195], v[72:75], v[200:203], v[192:195]\n"
"v_add_f32_e32 v101, v101, v209\n"
"v_add_f32_e32 v102, v102, v210\n"
"v_mfma_f32_16x16x32_bf16 v[116:119], v[76:79], v[200:203], v[116:119]\n"
"v_add_f32_e32 v103, v103, v211\n"
"v_add_f32_e32 v164, v164, v216\n"
"v_mfma_f32_16x16x32_bf16 v[148:151], v[80:83], v[200:203], v[148:151]\n"
"buffer_load_dwordx4 v[92:95], v1, s[4:7], s98 offen offset:3072 sc1\n"
"v_mfma_f32_16x16x32_bf16 v[96:99], v[84:87], v[200:203], v[96:99]\n"
"s_add_u32 s98, s60, 0x448000\n"
"v_add_f32_e32 v165, v165, v217\n"
"v_add_f32_e32 v166, v166, v218\n"
"v_add_f32_e32 v167, v167, v219\n"
"v_add_f32_e32 v228, v228, v232\n"
"v_add_f32_e32 v229, v229, v233\n"
"v_add_f32_e32 v230, v230, v234\n"
"v_add_f32_e32 v231, v231, v235\n"
"v_rcp_f32_e32 v180, v180\n"
"s_nop 0\n"
"v_mul_f32_e32 v188, v188, v180\n"
"v_mul_f32_e32 v189, v189, v180\n"
"v_mul_f32_e32 v190, v190, v180\n"
"v_mul_f32_e32 v191, v191, v180\n"
"v_mul_f32_e32 v120, v120, v180\n"
"v_mul_f32_e32 v121, v121, v180\n"
"v_mul_f32_e32 v122, v122, v180\n"
"v_mul_f32_e32 v123, v123, v180\n"
"v_cvt_pk_bf16_f32 v136, v188, v189\n"
"v_cvt_pk_bf16_f32 v137, v190, v191\n"
"v_cvt_pk_bf16_f32 v138, v120, v121\n"
"v_cvt_pk_bf16_f32 v139, v122, v123\n"
"buffer_store_dwordx4 v[136:139], v0, s[4:7], s98 offen sc1\n"
".Lmk_b3:\n"
"s_waitcnt vmcnt(22)\n"
"; @B G1 3.0\n"
"v_mfma_f32_16x16x32_bf16 v[120:123], a[0:3], v[212:215], 0\n"
"v_mfma_f32_16x16x32_bf16 v[136:139], a[4:7], v[212:215], 0\n"
"s_add_u32 s98, s62, 0x40000\n"
"v_mfma_f32_16x16x32_bf16 v[172:175], a[8:11], v[212:215], 0\n"
"v_mfma_f32_16x16x32_bf16 v[176:179], a[12:15], v[212:215], 0\n"
"v_add_f32_e32 v192, v192, v144\n"
"v_add_f32_e32 v193, v193, v145\n"
"v_mfma_f32_16x16x32_bf16 v[180:183], a[16:19], v[212:215], 0\n"
"s_add_u32 s96, s54, 0x400000\n"
"buffer_load_dwordx4 v[188:191], v0, s[4:7], s96 offen sc1\n"
"v_mfma_f32_16x16x32_bf16 v[200:203], a[20:23], v[212:215], 0\n"
"buffer_load_dword v253, v10, s[4:7], s98 offen sc1\n"
"v_mfma_f32_16x16x32_bf16 v[120:123], a[24:27], v[132:135], v[120:123]\n"
"v_add_f32_e32 v194, v194, v146\n"
"v_add_f32_e32 v195, v195, v147\n"
"v_mfma_f32_16x16x32_bf16 v[136:139], a[28:31], v[132:135], v[136:139]\n"
"v_add_f32_e32 v116, v116, v156\n"
"v_add_f32_e32 v117, v117, v157\n"
"v_mfma_f32_16x16x32_bf16 v[172:175], a[32:35], v[132:135], v[172:175]\n"
"buffer_load_dwordx4 a[216:219], v1, s[4:7], s97 offen offset:2048 sc1\n"
"v_mfma_f32_16x16x32_bf16 v[176:179], a[36:39], v[132:135], v[176:179]\n"
"buffer_load_dword v252, v10, s[4:7], s98 offen offset:256 sc1\n"
"v_mfma_f32_16x16x32_bf16 v[180:183], a[40:43], v[132:135], v[180:183]\n"
"buffer_load_dwordx4 v[144:147], v0, s[4:7], s96 offen offset:1024 sc1\n"
"v_mfma_f32_16x16x32_bf16 v[200:203], a[44:47], v[132:135], v[200:203]\n"
"s_waitcnt vmcnt(25)\n"
"v_add_f32_e32 v118, v118, v158\n"
"v_add_f32_e32 v119, v119, v159\n"
"v_mfma_f32_16x16x32_bf16 v[120:123], a[48:51], v[152:155], v[120:123]\n"
"buffer_load_dword v251, v10, s[4:7], s98 offen offset:512 sc1\n"
"v_mfma_f32_16x16x32_bf16 v[136:139], a[52:55], v[152:155], v[136:139]\n"
"v_mul_f32_e32 v192, v192, v254\n"
"v_mul_f32_e32 v193, v193, v254\n"
"v_mfma_f32_16x16x32_bf16 v[172:175], a[56:59], v[152:155], v[172:175]\n"
"v_mul_f32_e32 v194, v194, v254\n"
"v_mul_f32_e32 v195, v195, v254\n"
"v_mfma_f32_16x16x32_bf16 v[176:179], a[60:63], v[152:155], v[176:179]\n"
"buffer_load_dword v250, v10, s[4:7], s98 offen offset:768 sc1\n"
"v_mfma_f32_16x16x32_bf16 v[180:183], a[64:67], v[152:155], v[180:183]\n"
"buffer_load_dwordx4 v[132:135], v0, s[4:7], s96 offen offset:2048 sc1\n"
"v_mfma_f32_16x16x32_bf16 v[200:203], a[68:71], v[152:155], v[200:203]\n"
"v_mul_f32_e32 v116, v116, v254\n"
"v_mul_f32_e32 v117, v117, v254\n"
"v_mfma_f32_16x16x32_bf16 v[120:123], a[72:75], v[168:171], v[120:123]\n"
"buffer_load_dword v249, v10, s[4:7], s98 offen offset:1024 sc1\n"
"v_mfma_f32_16x16x32_bf16 v[136:139], a[76:79], v[168:171], v[136:139]\n"
"v_mul_f32_e32 v118, v118, v254\n"
"v_mul_f32_e32 v119, v119, v254\n"
"v_mfma_f32_16x16x32_bf16 v[172:175], a[80:83], v[168:171], v[172:175]\n"
"v_cvt_pk_bf16_f32 v152, v192, v193\n"
"v_cvt_pk_bf16_f32 v153, v194, v195\n"
"v_mfma_f32_16x16x32_bf16 v[176:179], a[84:87], v[168:171], v[176:179]\n"
"buffer_load_dword v248, v10, s[4:7], s98 offen offset:1280 sc1\n"
"v_mfma_f32_16x16x32_bf16 v[180:183], a[88:91], v[168:171], v[180:183]\n"
"buffer_load_dwordx4 v[156:159], v0, s[4:7], s96 offen offset:3072 sc1\n"
"v_mfma_f32_16x16x32_bf16 v[200:203], a[92:95], v[168:171], v[200:203]\n"
"s_waitcnt vmcnt(27)\n"
"v_cvt_pk_bf16_f32 v154, v116, v117\n"
"v_cvt_pk_bf16_f32 v155, v118, v119\n"
"v_mfma_f32_16x16x32_bf16 v[120:123], a[96:99], v[140:143], v[120:123]\n"
"buffer_load_dword v247, v10, s[4:7], s98 offen offset:1536 sc1\n"
"v_mfma_f32_16x16x32_bf16 v[136:139], a[100:103], v[140:143], v[136:139]\n"
"ds_write_b128 v8, v[152:155] offset:17408\n"
"v_mfma_f32_16x16x32_bf16 v[172:175], a[104:107], v[140:143], v[172:175]\n"
"v_add_f32_e32 v148, v148, v128\n"
"v_add_f32_e32 v149, v149, v129\n"
"v_mfma_f32_16x16x32_bf16 v[176:179], a[108:111], v[140:143], v[176:179]\n"
"buffer_load_dword v246, v10, s[4:7], s98 offen offset:1792 sc1\n"
"v_mfma_f32_16x16x32_bf16 v[180:183], a[112:115], v[140:143], v[180:183]\n"
"buffer_load_dwordx4 v[116:119], v1, s[4:7], s96 offen sc1\n"
"v_mfma_f32_16x16x32_bf16 v[200:203], a[116:119], v[140:143], v[200:203]\n"
"v_add_f32_e32 v150, v150, v130\n"
"v_add_f32_e32 v151, v151, v131\n"
"v_mfma_f32_16x16x32_bf16 v[120:123], a[120:123], v[196:199], v[120:123]\n"
"v_add_f32_e32 v96, v96, v100\n"
"v_add_f32_e32 v97, v97, v101\n"
"v_mfma_f32_16x16x32_bf16 v[136:139], a[124:127], v[196:199], v[136:139]\n"
"v_add_f32_e32 v98, v98, v102\n"
"v_add_f32_e32 v99, v99, v103\n"
"v_mfma_f32_16x16x32_bf16 v[172:175], v[24:27], v[196:199], v[172:175]\n"
"buffer_load_dwordx4 a[220:223], v1, s[4:7], s97 offen offset:3072 sc1\n"
"v_mfma_f32_16x16x32_bf16 v[176:179], v[28:31], v[196:199], v[176:179]\n"
"v_mul_f32_e32 v148, v148, v254\n"
"v_mul_f32_e32 v149, v149, v254\n"
"v_mfma_f32_16x16x32_bf16 v[180:183], v[32:35], v[196:199], v[180:183]\n"
"buffer_load_dwordx4 v[100:103], v1, s[4:7], s96 offen offset:1024 sc1\n"
"v_mfma_f32_16x16x32_bf16 v[200:203], v[36:39], v[196:199], v[200:203]\n"
"s_waitcnt vmcnt(29)\n"
"v_mul_f32_e32 v150, v150, v254\n"
"v_mul_f32_e32 v151, v151, v254\n"
"v_mfma_f32_16x16x32_bf16 v[120:123], v[40:43], v[224:227], v[120:123]\n"
"v_mul_f32_e32 v96, v96, v254\n"
"v_mul_f32_e32 v97, v97, v254\n"
"v_mfma_f32_16x16x32_bf16 v[136:139], v[44:47], v[224:227], v[136:139]\n"
"v_mul_f32_e32 v98, v98, v254\n"
"v_mul_f32_e32 v99, v99, v254\n"
"v_mfma_f32_16x16x32_bf16 v[172:175], v[48:51], v[224:227], v[172:175]\n"
"v_cvt_pk_bf16_f32 v128, v148, v149\n"
"v_cvt_pk_bf16_f32 v129, v150, v151\n"
"v_mfma_f32_16x16x32_bf16 v[176:179], v[52:55], v[224:227], v[176:179]\n"
"v_cvt_pk_bf16_f32 v130, v96, v97\n"
"v_cvt_pk_bf16_f32 v131, v98, v99\n"
"v_mfma_f32_16x16x32_bf16 v[180:183], v[56:59], v[224:227], v[180:183]\n"
"buffer_load_dwordx4 v[96:99], v1, s[4:7], s96 offen offset:2048 sc1\n"
"v_mfma_f32_16x16x32_bf16 v[200:203], v[60:63], v[224:227], v[200:203]\n"
"ds_write_b128 v7, v[128:131] offset:50176\n"
"v_mfma_f32_16x16x32_bf16 v[120:123], v[64:67], v[220:223], v[120:123]\n"
"v_add_f32_e32 v104, v104, v164\n"
"v_add_f32_e32 v105, v105, v165\n"
"v_mfma_f32_16x16x32_bf16 v[136:139], v[68:71], v[220:223], v[136:139]\n"
"v_add_f32_e32 v106, v106, v166\n"
"v_add_f32_e32 v107, v107, v167\n"
"v_mfma_f32_16x16x32_bf16 v[172:175], v[72:75], v[220:223], v[172:175]\n"
"v_add_f32_e32 v184, v184, v228\n"
"v_add_f32_e32 v185, v185, v229\n"
"v_mfma_f32_16x16x32_bf16 v[176:179], v[76:79], v[220:223], v[176:179]\n"
"v_add_f32_e32 v186, v186, v230\n"
"v_add_f32_e32 v187, v187, v231\n"
"v_mfma_f32_16x16x32_bf16 v[180:183], v[80:83], v[220:223], v[180:183]\n"
"buffer_load_dwordx4 v[128:131], v1, s[4:7], s96 offen offset:3072 sc1\n"
"v_mfma_f32_16x16x32_bf16 v[200:203], v[84:87], v[220:223], v[200:203]\n"
"s_waitcnt vmcnt(29)\n"
"v_mul_f32_e32 v104, v104, v255\n"
"v_mul_f32_e32 v105, v105, v255\n"
"; @B G1 3.1\n"
"v_mfma_f32_16x16x32_bf16 v[140:143], a[0:3], v[108:111], 0\n"
"v_mul_f32_e32 v106, v106, v255\n"
"v_mul_f32_e32 v107, v107, v255\n"
"v_mfma_f32_16x16x32_bf16 v[148:151], a[4:7], v[108:111], 0\n"
"v_mul_f32_e32 v184, v184, v255\n"
"v_mul_f32_e32 v185, v185, v255\n"
"v_mfma_f32_16x16x32_bf16 v[152:155], a[8:11], v[108:111], 0\n"
"v_mul_f32_e32 v186, v186, v255\n"
"v_mul_f32_e32 v187, v187, v255\n"
"v_mfma_f32_16x16x32_bf16 v[164:167], a[12:15], v[108:111], 0\n"
"ds_write_b128 v3, v[120:123]\n"
"v_mfma_f32_16x16x32_bf16 v[120:123], a[16:19], v[108:111], 0\n"
"s_add_u32 s98, s55, 0x400000\n"
"buffer_load_dwordx4 v[168:171], v0, s[4:7], s98 offen sc1\n"
"v_mfma_f32_16x16x32_bf16 v[192:195], a[20:23], v[108:111], 0\n"
"v_cvt_pk_bf16_f32 v108, v104, v105\n"
"v_cvt_pk_bf16_f32 v109, v106, v107\n"
"v_mfma_f32_16x16x32_bf16 v[140:143], a[24:27], v[160:163], v[140:143]\n"
"ds_write_b128 v3, v[136:139] offset:1024\n"
"v_mfma_f32_16x16x32_bf16 v[148:151], a[28:31], v[160:163], v[148:151]\n"
"s_add_u32 s97, s70, 0x206000\n"
"v_cvt_pk_bf16_f32 v110, v184, v185\n"
"v_cvt_pk_bf16_f32 v111, v186, v187\n"
"v_mfma_f32_16x16x32_bf16 v[152:155], a[32:35], v[160:163], v[152:155]\n"
"buffer_load_dwordx4 a[224:227], v0, s[4:7], s97 offen sc1\n"
"v_mfma_f32_16x16x32_bf16 v[164:167], a[36:39], v[160:163], v[164:167]\n"
"ds_write_b128 v3, v[172:175] offset:2048\n"
"v_mfma_f32_16x16x32_bf16 v[120:123], a[40:43], v[160:163], v[120:123]\n"
"buffer_load_dwordx4 v[104:107], v0, s[4:7], s98 offen offset:1024 sc1\n"
"v_mfma_f32_16x16x32_bf16 v[192:195], a[44:47], v[160:163], v[192:195]\n"
"s_waitcnt vmcnt(29)\n"
"ds_write_b128 v2, v[108:111] offset:17408\n"
"v_mfma_f32_16x16x32_bf16 v[140:143], a[48:51], v[88:91], v[140:143]\n"
"ds_write_b128 v3, v[176:179] offset:3072\n"
"v_mfma_f32_16x16x32_bf16 v[148:151], a[52:55], v[88:91], v[148:151]\n"
"s_add_u32 s96, s73, 0\n"
"buffer_load_dwordx2 v[254:255], off, s[4:7], s96 sc1\n"
"v_mfma_f32_16x16x32_bf16 v[152:155], a[56:59], v[88:91], v[152:155]\n"
"v_mfma_f32_16x16x32_bf16 v[164:167], a[60:63], v[88:91], v[164:167]\n"
"ds_write_b128 v3, v[180:183] offset:4096\n"
"v_mfma_f32_16x16x32_bf16 v[120:123], a[64:67], v[88:91], v[120:123]\n"
"buffer_load_dwordx4 v[108:111], v0, s[4:7], s98 offen offset:2048 sc1\n"
"v_mfma_f32_16x16x32_bf16 v[192:195], a[68:71], v[88:91], v[192:195]\n"
"v_mfma_f32_16x16x32_bf16 v[140:143], a[72:75], v[124:127], v[140:143]\n"
"ds_write_b128 v3, v[200:203] offset:5120\n"
"v_mfma_f32_16x16x32_bf16 v[148:151], a[76:79], v[124:127], v[148:151]\n"
"v_mfma_f32_16x16x32_bf16 v[152:155], a[80:83], v[124:127], v[152:155]\n"
"v_mov_b32_e32 v88, -1\n"
"v_mov_b32_e32 v89, -1\n"
"v_mfma_f32_16x16x32_bf16 v[164:167], a[84:87], v[124:127], v[164:167]\n"
"v_mov_b32_e32 v90, -1\n"
"v_mov_b32_e32 v91, -1\n"
"v_mfma_f32_16x16x32_bf16 v[120:123], a[88:91], v[124:127], v[120:123]\n"
"buffer_load_dwordx4 v[136:139], v0, s[4:7], s98 offen offset:3072 sc1\n"
"v_mfma_f32_16x16x32_bf16 v[192:195], a[92:95], v[124:127], v[192:195]\n"
"s_waitcnt vmcnt(30)\n"
"s_add_u32 s95, s60, 0x800000\n"
"v_mfma_f32_16x16x32_bf16 v[140:143], a[96:99], v[204:207], v[140:143]\n"
"buffer_store_dwordx4 v[88:91], v0, s[8:11], s95 offen sc1\n"
"v_mfma_f32_16x16x32_bf16 v[148:151], a[100:103], v[204:207], v[148:151]\n"
"v_mfma_f32_16x16x32_bf16 v[152:155], a[104:107], v[204:207], v[152:155]\n"
"v_mfma_f32_16x16x32_bf16 v[164:167], a[108:111], v[204:207], v[164:167]\n"
"v_mfma_f32_16x16x32_bf16 v[120:123], a[112:115], v[204:207], v[120:123]\n"
"buffer_load_dwordx4 v[124:127], v1, s[4:7], s98 offen sc1\n"
"v_mfma_f32_16x16x32_bf16 v[192:195], a[116:119], v[204:207], v[192:195]\n"
"s_waitcnt lgkmcnt(0)\n"
"s_barrier\n"
"v_mfma_f32_16x16x32_bf16 v[140:143], a[120:123], v[236:239], v[140:143]\n"
"s_waitcnt vmcnt(13)\n"
"v_add_f32_e32 v253, v253, v249\n"
"v_add_f32_e32 v252, v252, v248\n"
"v_mfma_f32_16x16x32_bf16 v[148:151], a[124:127], v[236:239], v[148:151]\n"
"v_add_f32_e32 v251, v251, v247\n"
"v_add_f32_e32 v250, v250, v246\n"
"v_mfma_f32_16x16x32_bf16 v[152:155], v[24:27], v[236:239], v[152:155]\n"
"buffer_load_dwordx4 a[228:231], v0, s[4:7], s97 offen offset:1024 sc1\n"
"v_mfma_f32_16x16x32_bf16 v[164:167], v[28:31], v[236:239], v[164:167]\n"
"v_add_f32_e32 v253, v253, v251\n"
"v_add_f32_e32 v252, v252, v250\n"
"v_mfma_f32_16x16x32_bf16 v[120:123], v[32:35], v[236:239], v[120:123]\n"
"buffer_load_dwordx4 v[160:163], v1, s[4:7], s98 offen offset:1024 sc1\n"
"v_mfma_f32_16x16x32_bf16 v[192:195], v[36:39], v[236:239], v[192:195]\n"
"v_add_f32_e32 v253, v253, v252\n"
"v_mov_b32_e32 v252, v253\n"
"v_mfma_f32_16x16x32_bf16 v[140:143], v[40:43], v[112:115], v[140:143]\n"
"s_nop 0\n"
"v_permlane32_swap_b32_e32 v253, v252\n"
"v_add_f32_e32 v253, v253, v252\n"
"v_mfma_f32_16x16x32_bf16 v[148:151], v[44:47], v[112:115], v[148:151]\n"
"v_mul_f32_e32 v253, 0x3a800000, v253\n"
"v_mov_b32_e32 v252, v253\n"
"v_mfma_f32_16x16x32_bf16 v[152:155], v[48:51], v[112:115], v[152:155]\n"
"s_nop 0\n"
"v_permlane16_swap_b32_e32 v253, v252\n"
"v_cvt_pk_bf16_f32 v251, -v253, 0\n"
"v_mfma_f32_16x16x32_bf16 v[164:167], v[52:55], v[112:115], v[164:167]\n"
"v_fma_f32 v252, -v253, v253, v252\n"
"v_lshlrev_b32_e32 v251, 16, v251\n"
"v_mfma_f32_16x16x32_bf16 v[120:123], v[56:59], v[112:115], v[120:123]\n"
"buffer_load_dwordx4 v[172:175], v1, s[4:7], s98 offen offset:2048 sc1\n"
"v_mfma_f32_16x16x32_bf16 v[192:195], v[60:63], v[112:115], v[192:195]\n"
"ds_read_b128 v[112:115], v4\n"
"ds_read_b128 v[176:179], v9 offset:512\n"
"v_mfma_f32_16x16x32_bf16 v[140:143], v[64:67], v[92:95], v[140:143]\n"
"ds_read_b128 v[180:183], v4 offset:1024\n"
"ds_read_b128 v[184:187], v9 offset:576\n"
"v_mfma_f32_16x16x32_bf16 v[148:151], v[68:71], v[92:95], v[148:151]\n"
"ds_read_b128 v[196:199], v4 offset:2048\n"
"ds_read_b128 v[200:203], v9 offset:640\n"
"v_mfma_f32_16x16x32_bf16 v[152:155], v[72:75], v[92:95], v[152:155]\n"
"v_add_u32_e32 v253, 0x26c00, v0\n"
"v_mfma_f32_16x16x32_bf16 v[164:167], v[76:79], v[92:95], v[164:167]\n"
"ds_read_b128 v[204:207], v253\n"
"ds_read_b128 v[208:211], v253 offset:1024\n"
"v_xor_b32_e32 v253, 32, v17\n"
"v_mfma_f32_16x16x32_bf16 v[120:123], v[80:83], v[92:95], v[120:123]\n"
"buffer_load_dwordx4 v[212:215], v1, s[4:7], s98 offen offset:3072 sc1\n"
"v_mfma_f32_16x16x32_bf16 v[192:195], v[84:87], v[92:95], v[192:195]\n"
"ds_read_b128 v[92:95], v2 offset:49152\n"
"ds_read_b128 v[216:219], v17 offset:23552\n"
"; @B G1 3.2\n"
"v_mfma_f32_16x16x32_bf16 v[220:223], a[0:3], v[188:191], 0\n"
"s_waitcnt lgkmcnt(4)\n"
"v_fmac_f32_e32 v112, v251, v176\n"
"v_fmac_f32_e32 v113, v251, v177\n"
"v_mfma_f32_16x16x32_bf16 v[224:227], a[4:7], v[188:191], 0\n"
"v_fmac_f32_e32 v114, v251, v178\n"
"v_fmac_f32_e32 v115, v251, v179\n"
"v_mfma_f32_16x16x32_bf16 v[176:179], a[8:11], v[188:191], 0\n"
"s_add_u32 s98, s60, 0x808000\n"
"v_fmac_f32_e32 v180, v251, v184\n"
"v_fmac_f32_e32 v181, v251, v185\n"
"v_mfma_f32_16x16x32_bf16 v[228:231], a[12:15], v[188:191], 0\n"
"ds_write_b128 v3, v[140:143] offset:6144\n"
"v_mfma_f32_16x16x32_bf16 v[140:143], a[16:19], v[188:191], 0\n"
"buffer_store_dwordx4 v[88:91], v0, s[8:11], s98 offen sc1\n"
"v_mfma_f32_16x16x32_bf16 v[232:235], a[20:23], v[188:191], 0\n"
"buffer_load_dwordx4 a[232:235], v0, s[4:7], s97 offen offset:2048 sc1\n"
"v_mfma_f32_16x16x32_bf16 v[220:223], a[24:27], v[144:147], v[220:223]\n"
"ds_write_b128 v3, v[148:151] offset:7168\n"
"v_mfma_f32_16x16x32_bf16 v[224:227], a[28:31], v[144:147], v[224:227]\n"
"v_fmac_f32_e32 v182, v251, v186\n"
"v_fmac_f32_e32 v183, v251, v187\n"
"v_mfma_f32_16x16x32_bf16 v[176:179], a[32:35], v[144:147], v[176:179]\n"
"v_fmac_f32_e32 v196, v251, v200\n"
"v_fmac_f32_e32 v197, v251, v201\n"
"v_mfma_f32_16x16x32_bf16 v[228:231], a[36:39], v[144:147], v[228:231]\n"
"ds_write_b128 v3, v[152:155] offset:8192\n"
"v_mfma_f32_16x16x32_bf16 v[140:143], a[40:43], v[144:147], v[140:143]\n"
"v_fmac_f32_e32 v198, v251, v202\n"
"v_fmac_f32_e32 v199, v251, v203\n"
"v_mfma_f32_16x16x32_bf16 v[232:235], a[44:47], v[144:147], v[232:235]\n"
"buffer_load_dwordx4 a[236:239], v0, s[4:7], s97 offen offset:3072 sc1\n"
"v_mfma_f32_16x16x32_bf16 v[220:223], a[48:51], v[132:135], v[220:223]\n"
"ds_write_b128 v3, v[164:167] offset:9216\n"
"v_mfma_f32_16x16x32_bf16 v[224:227], a[52:55], v[132:135], v[224:227]\n"
"ds_read_b128 v[144:147], v4 offset:3072\n"
"ds_read_b128 v[148:151], v9 offset:704\n"
"v_mfma_f32_16x16x32_bf16 v[176:179], a[56:59], v[132:135], v[176:179]\n"
"ds_read_b128 v[152:155], v4 offset:4096\n"
"ds_read_b128 v[164:167], v9 offset:768\n"
"v_mfma_f32_16x16x32_bf16 v[228:231], a[60:63], v[132:135], v[228:231]\n"
"ds_write_b128 v3, v[120:123] offset:10240\n"
"v_mfma_f32_16x16x32_bf16 v[140:143], a[64:67], v[132:135], v[140:143]\n"
"ds_read_b128 v[120:123], v4 offset:5120\n"
"ds_read_b128 v[184:187], v9 offset:832\n"
"v_mfma_f32_16x16x32_bf16 v[232:235], a[68:71], v[132:135], v[232:235]\n"
"buffer_load_dwordx4 a[240:243], v1, s[4:7], s97 offen sc1\n"
"v_mfma_f32_16x16x32_bf16 v[220:223], a[72:75], v[156:159], v[220:223]\n"
"s_waitcnt lgkmcnt(13)\n"
"ds_write_b128 v3, v[192:195] offset:11264\n"
"v_mfma_f32_16x16x32_bf16 v[224:227], a[76:79], v[156:159], v[224:227]\n"
"ds_read_b128 v[132:135], v253 offset:23808\n"
"v_max_f32_e32 v252, 0, v252\n"
"v_mfma_f32_16x16x32_bf16 v[176:179], a[80:83], v[156:159], v[176:179]\n"
"v_cvt_pk_bf16_f32 v252, v252, 0\n"
"v_lshlrev_b32_e32 v252, 16, v252\n"
"v_mfma_f32_16x16x32_bf16 v[228:231], a[84:87], v[156:159], v[228:231]\n"
"s_waitcnt lgkmcnt(2)\n"
"v_fmac_f32_e32 v144, v251, v148\n"
"v_fmac_f32_e32 v145, v251, v149\n"
"v_mfma_f32_16x16x32_bf16 v[140:143], a[88:91], v[156:159], v[140:143]\n"
"v_fmac_f32_e32 v146, v251, v150\n"
"v_fmac_f32_e32 v147, v251, v151\n"
"v_mfma_f32_16x16x32_bf16 v[232:235], a[92:95], v[156:159], v[232:235]\n"
"s_waitcnt vmcnt(18)\n"
"buffer_load_dwordx4 a[244:247], v1, s[4:7], s97 offen offset:1024 sc1\n"
"v_mfma_f32_16x16x32_bf16 v[220:223], a[96:99], v[116:119], v[220:223]\n"
"s_waitcnt vmcnt(13)\n"
"v_cmp_ne_u64_e32 vcc, v[254:255], v[18:19]\n"
"v_mfma_f32_16x16x32_bf16 v[224:227], a[100:103], v[116:119], v[224:227]\n"
"s_cbranch_vccnz .Lmk_f47s\n"
".Lmk_f47r:\n"
"s_mov_b32 m0, 0x7c00\n"
"v_mfma_f32_16x16x32_bf16 v[176:179], a[104:107], v[116:119], v[176:179]\n"
"s_add_u32 s98, s66, 0x3c00\n"
"buffer_load_dwordx4 v0, s[4:7], s98 offen sc1 lds\n"
"v_mfma_f32_16x16x32_bf16 v[228:231], a[108:111], v[116:119], v[228:231]\n"
"s_mov_b32 m0, 0xfc00\n"
"s_add_u32 s98, s66, 0x7c00\n"
"v_fmac_f32_e32 v152, v251, v164\n"
"v_mfma_f32_16x16x32_bf16 v[140:143], a[112:115], v[116:119], v[140:143]\n"
"buffer_load_dwordx4 v0, s[4:7], s98 offen sc1 lds\n"
"v_mfma_f32_16x16x32_bf16 v[232:235], a[116:119], v[116:119], v[232:235]\n"
"buffer_load_dwordx4 a[248:251], v1, s[4:7], s97 offen offset:2048 sc1\n"
"v_mfma_f32_16x16x32_bf16 v[220:223], a[120:123], v[100:103], v[220:223]\n"
"s_add_u32 s98, s61, 0xc40000\n"
"v_fmac_f32_e32 v153, v251, v165\n"
"v_fmac_f32_e32 v154, v251, v166\n"
"v_mfma_f32_16x16x32_bf16 v[224:227], a[124:127], v[100:103], v[224:227]\n"
"s_waitcnt lgkmcnt(0)\n"
"v_fmac_f32_e32 v155, v251, v167\n"
"v_fmac_f32_e32 v120, v251, v184\n"
"v_mfma_f32_16x16x32_bf16 v[204:207], v[216:219], v[92:95], v[204:207]\n"
"v_mfma_f32_16x16x32_bf16 v[208:211], v[132:135], v[92:95], v[208:211]\n"
"v_mfma_f32_16x16x32_bf16 v[176:179], v[24:27], v[100:103], v[176:179]\n"
"buffer_store_dwordx4 v[88:91], v0, s[8:11], s98 offen sc1\n"
"v_mfma_f32_16x16x32_bf16 v[228:231], v[28:31], v[100:103], v[228:231]\n"
"v_fmac_f32_e32 v121, v251, v185\n"
"v_fmac_f32_e32 v122, v251, v186\n"
"v_mfma_f32_16x16x32_bf16 v[140:143], v[32:35], v[100:103], v[140:143]\n"
"s_add_u32 s98, s67, 0x10020\n"
"buffer_load_dwordx2 v[254:255], v12, s[4:7], s98 offen sc1\n"
"v_mfma_f32_16x16x32_bf16 v[232:235], v[36:39], v[100:103], v[232:235]\n"
"buffer_load_dwordx4 a[252:255], v1, s[4:7], s97 offen offset:3072 sc1\n"
"v_mfma_f32_16x16x32_bf16 v[220:223], v[40:43], v[96:99], v[220:223]\n"
"v_fmac_f32_e32 v123, v251, v187\n"
"v_xor_b32_e32 v253, 32, v16\n"
"v_mfma_f32_16x16x32_bf16 v[224:227], v[44:47], v[96:99], v[224:227]\n"
"ds_read_b64_tr_b16 v[92:93], v16 offset:23552\n"
"ds_read_b64_tr_b16 v[94:95], v253 offset:23808\n"
"v_mfma_f32_16x16x32_bf16 v[176:179], v[48:51], v[96:99], v[176:179]\n"
"ds_read_b64_tr_b16 v[100:101], v253 offset:23552\n"
"ds_read_b64_tr_b16 v[102:103], v16 offset:23808\n"
"v_mfma_f32_16x16x32_bf16 v[228:231], v[52:55], v[96:99], v[228:231]\n"
"v_mov_b32_e32 v116, 0x3f803f80\n"
"v_mov_b32_e32 v117, 0x3f803f80\n"
"v_mfma_f32_16x16x32_bf16 v[140:143], v[56:59], v[96:99], v[140:143]\n"
"v_mov_b32_e32 v118, 0x3f803f80\n"
"v_mov_b32_e32 v119, 0x3f803f80\n"
"v_mfma_f32_16x16x32_bf16 v[232:235], v[60:63], v[96:99], v[232:235]\n"
"v_max_f32_e32 v253, v204, v205\n"
"v_max3_f32 v253, v253, v206, v207\n"
"v_mfma_f32_16x16x32_bf16 v[220:223], v[64:67], v[128:131], v[220:223]\n"
"v_max3_f32 v253, v253, v208, v209\n"
"v_max3_f32 v253, v253, v210, v211\n"
"v_mfma_f32_16x16x32_bf16 v[224:227], v[68:71], v[128:131], v[224:227]\n"
"v_mov_b32_e32 v251, v253\n"
"v_add_f32_e32 v252, 0x3727c5ac, v252\n"
"v_mfma_f32_16x16x32_bf16 v[176:179], v[72:75], v[128:131], v[176:179]\n"
"v_permlane16_swap_b32_e32 v253, v251\n"
"v_max_f32_e32 v253, v253, v251\n"
"v_mfma_f32_16x16x32_bf16 v[228:231], v[76:79], v[128:131], v[228:231]\n"
"v_mov_b32_e32 v251, v253\n"
"v_cvt_pk_bf16_f32 v252, v252, 0\n"
"v_mfma_f32_16x16x32_bf16 v[140:143], v[80:83], v[128:131], v[140:143]\n"
"v_permlane32_swap_b32_e32 v253, v251\n"
"v_max_f32_e32 v253, v253, v251\n"
"v_mfma_f32_16x16x32_bf16 v[232:235], v[84:87], v[128:131], v[232:235]\n"
"v_sub_f32_e32 v204, v204, v253\n"
"v_sub_f32_e32 v205, v205, v253\n"
"; @B G1 3.3\n"
"v_mfma_f32_16x16x32_bf16 v[112:115], a[0:3], v[168:171], v[112:115]\n"
"v_sub_f32_e32 v206, v206, v253\n"
"v_sub_f32_e32 v207, v207, v253\n"
"v_mfma_f32_16x16x32_bf16 v[180:183], a[4:7], v[168:171], v[180:183]\n"
"v_sub_f32_e32 v208, v208, v253\n"
"v_sub_f32_e32 v209, v209, v253\n"
"v_mfma_f32_16x16x32_bf16 v[196:199], a[8:11], v[168:171], v[196:199]\n"
"v_sub_f32_e32 v210, v210, v253\n"
"v_sub_f32_e32 v211, v211, v253\n"
"v_mfma_f32_16x16x32_bf16 v[144:147], a[12:15], v[168:171], v[144:147]\n"
"ds_write_b128 v3, v[220:223] offset:12288\n"
"v_mfma_f32_16x16x32_bf16 v[152:155], a[16:19], v[168:171], v[152:155]\n"
"v_exp_f32_e32 v204, v204\n"
"v_mfma_f32_16x16x32_bf16 v[120:123], a[20:23], v[168:171], v[120:123]\n"
"v_exp_f32_e32 v205, v205\n"
"v_mfma_f32_16x16x32_bf16 v[112:115], a[24:27], v[104:107], v[112:115]\n"
"ds_write_b128 v3, v[224:227] offset:13312\n"
"v_mfma_f32_16x16x32_bf16 v[180:183], a[28:31], v[104:107], v[180:183]\n"
"v_exp_f32_e32 v206, v206\n"
"v_mfma_f32_16x16x32_bf16 v[196:199], a[32:35], v[104:107], v[196:199]\n"
"v_exp_f32_e32 v207, v207\n"
"v_mfma_f32_16x16x32_bf16 v[144:147], a[36:39], v[104:107], v[144:147]\n"
"ds_write_b128 v3, v[176:179] offset:14336\n"
"v_mfma_f32_16x16x32_bf16 v[152:155], a[40:43], v[104:107], v[152:155]\n"
"s_add_u32 s97, s61, 0xc40000\n"
"v_exp_f32_e32 v208, v208\n"
"v_mfma_f32_16x16x32_bf16 v[120:123], a[44:47], v[104:107], v[120:123]\n"
"s_waitcnt vmcnt(17)\n"
"v_exp_f32_e32 v209, v209\n"
"v_mfma_f32_16x16x32_bf16 v[112:115], a[48:51], v[108:111], v[112:115]\n"
"ds_write_b128 v3, v[228:231] offset:15360\n"
"v_mfma_f32_16x16x32_bf16 v[180:183], a[52:55], v[108:111], v[180:183]\n"
"buffer_store_dwordx4 v[88:91], v0, s[8:11], s97 offen offset:1024 sc1\n"
"v_mfma_f32_16x16x32_bf16 v[196:199], a[56:59], v[108:111], v[196:199]\n"
"v_exp_f32_e32 v210, v210\n"
"v_mfma_f32_16x16x32_bf16 v[144:147], a[60:63], v[108:111], v[144:147]\n"
"ds_write_b128 v3, v[140:143] offset:16384\n"
"v_mfma_f32_16x16x32_bf16 v[152:155], a[64:67], v[108:111], v[152:155]\n"
"v_exp_f32_e32 v211, v211\n"
"v_mfma_f32_16x16x32_bf16 v[120:123], a[68:71], v[108:111], v[120:123]\n"
"v_cvt_pk_bf16_f32 v96, v204, v205\n"
"v_cvt_pk_bf16_f32 v97, v206, v207\n"
"v_mfma_f32_16x16x32_bf16 v[112:115], a[72:75], v[136:139], v[112:115]\n"
"ds_write_b128 v3, v[232:235] offset:17408\n"
"v_mfma_f32_16x16x32_bf16 v[180:183], a[76:79], v[136:139], v[180:183]\n"
"v_cvt_pk_bf16_f32 v98, v208, v209\n"
"v_cvt_pk_bf16_f32 v99, v210, v211\n"
"v_mfma_f32_16x16x32_bf16 v[196:199], a[80:83], v[136:139], v[196:199]\n"
"s_waitcnt lgkmcnt(6)\n"
"v_mfma_f32_16x16x32_bf16 v[104:107], v[116:119], v[96:99], 0\n"
"v_mfma_f32_16x16x32_bf16 v[108:111], v[92:95], v[96:99], 0\n"
"v_mfma_f32_16x16x32_bf16 v[92:95], v[100:103], v[96:99], 0\n"
"v_lshlrev_b32_e32 v252, 16, v252\n"
"v_add_u32_e32 v253, 0x26c00, v0\n"
"v_mfma_f32_16x16x32_bf16 v[144:147], a[84:87], v[136:139], v[144:147]\n"
"s_waitcnt lgkmcnt(0)\n"
"s_barrier\n"
"v_mfma_f32_16x16x32_bf16 v[152:155], a[88:91], v[136:139], v[152:155]\n"
"ds_read_b128 v[96:99], v5 offset:2048\n"
"ds_read_b128 v[100:103], v6 offset:2048\n"
"v_mfma_f32_16x16x32_bf16 v[120:123], a[92:95], v[136:139], v[120:123]\n"
"s_waitcnt vmcnt(14)\n"
"ds_read_b128 v[116:119], v5 offset:3072\n"
"ds_read_b128 v[128:131], v6 offset:3072\n"
"v_mfma_f32_16x16x32_bf16 v[112:115], a[96:99], v[124:127], v[112:115]\n"
"v_sqrt_f32_e32 v252, v252\n"
"v_mfma_f32_16x16x32_bf16 v[180:183], a[100:103], v[124:127], v[180:183]\n"
"v_rcp_f32_e32 v104, v104\n"
"v_mfma_f32_16x16x32_bf16 v[196:199], a[104:107], v[124:127], v[196:199]\n"
"v_mul_f32_e32 v108, v108, v104\n"
"v_mul_f32_e32 v109, v109, v104\n"
"v_mfma_f32_16x16x32_bf16 v[144:147], a[108:111], v[124:127], v[144:147]\n"
"v_mul_f32_e32 v110, v110, v104\n"
"v_mul_f32_e32 v111, v111, v104\n"
"v_mfma_f32_16x16x32_bf16 v[152:155], a[112:115], v[124:127], v[152:155]\n"
"s_waitcnt lgkmcnt(0)\n"
"v_mul_f32_e32 v92, v92, v104\n"
"v_mul_f32_e32 v93, v93, v104\n"
"v_mfma_f32_16x16x32_bf16 v[120:123], a[116:119], v[124:127], v[120:123]\n"
"v_mul_f32_e32 v94, v94, v104\n"
"v_mul_f32_e32 v95, v95, v104\n"
"v_mfma_f32_16x16x32_bf16 v[112:115], a[120:123], v[160:163], v[112:115]\n"
"v_cvt_pk_bf16_f32 v104, v108, v109\n"
"v_cvt_pk_bf16_f32 v105, v110, v111\n"
"v_mfma_f32_16x16x32_bf16 v[180:183], a[124:127], v[160:163], v[180:183]\n"
"s_add_u32 s97, s60, 0x440000\n"
"v_cvt_pk_bf16_f32 v106, v92, v93\n"
"v_cvt_pk_bf16_f32 v107, v94, v95\n"
"v_mfma_f32_16x16x32_bf16 v[196:199], v[24:27], v[160:163], v[196:199]\n"
"buffer_store_dwordx4 v[104:107], v0, s[4:7], s97 offen sc1\n"
"v_mfma_f32_16x16x32_bf16 v[144:147], v[28:31], v[160:163], v[144:147]\n"
"ds_read_b128 v[24:27], v5 offset:4096\n"
"ds_read_b128 v[28:31], v6 offset:4096\n"
"v_mfma_f32_16x16x32_bf16 v[152:155], v[32:35], v[160:163], v[152:155]\n"
"ds_read_b128 v[32:35], v5 offset:5120\n"
"ds_read_b128 v[92:95], v6 offset:5120\n"
"v_mfma_f32_16x16x32_bf16 v[120:123], v[36:39], v[160:163], v[120:123]\n"
"s_waitcnt vmcnt(13)\n"
"v_add_f32_e32 v96, v96, v100\n"
"v_add_f32_e32 v97, v97, v101\n"
"v_mfma_f32_16x16x32_bf16 v[112:115], v[40:43], v[172:175], v[112:115]\n"
"v_add_f32_e32 v98, v98, v102\n"
"v_add_f32_e32 v99, v99, v103\n"
"v_mfma_f32_16x16x32_bf16 v[180:183], v[44:47], v[172:175], v[180:183]\n"
"s_add_u32 s97, s69, 0\n"
"buffer_load_dwordx4 a[0:3], v0, s[8:11], s97 offen sc1\n"
"v_mfma_f32_16x16x32_bf16 v[196:199], v[48:51], v[172:175], v[196:199]\n"
"ds_read_b128 v[36:39], v5\n"
"ds_read_b128 v[40:43], v6\n"
"v_mfma_f32_16x16x32_bf16 v[144:147], v[52:55], v[172:175], v[144:147]\n"
"s_add_u32 s96, s63, 0xc4000\n"
"ds_read_b128 v[44:47], v5 offset:1024\n"
"ds_read_b128 v[48:51], v6 offset:1024\n"
"v_mfma_f32_16x16x32_bf16 v[152:155], v[56:59], v[172:175], v[152:155]\n"
"s_waitcnt lgkmcnt(4)\n"
"v_add_f32_e32 v116, v116, v128\n"
"v_add_f32_e32 v117, v117, v129\n"
"v_mfma_f32_16x16x32_bf16 v[120:123], v[60:63], v[172:175], v[120:123]\n"
"buffer_store_dword v88, v14, s[8:11], s96 offen sc1\n"
"v_mfma_f32_16x16x32_bf16 v[112:115], v[64:67], v[212:215], v[112:115]\n"
"buffer_load_dwordx4 a[4:7], v0, s[8:11], s97 offen offset:1024 sc1\n"
"v_mfma_f32_16x16x32_bf16 v[180:183], v[68:71], v[212:215], v[180:183]\n"
"v_add_f32_e32 v118, v118, v130\n"
"v_add_f32_e32 v119, v119, v131\n"
"v_mfma_f32_16x16x32_bf16 v[196:199], v[72:75], v[212:215], v[196:199]\n"
"s_waitcnt lgkmcnt(0)\n"
"v_add_f32_e32 v24, v24, v28\n"
"v_add_f32_e32 v25, v25, v29\n"
"v_mfma_f32_16x16x32_bf16 v[144:147], v[76:79], v[212:215], v[144:147]\n"
"v_add_f32_e32 v26, v26, v30\n"
"v_add_f32_e32 v27, v27, v31\n"
"v_mfma_f32_16x16x32_bf16 v[152:155], v[80:83], v[212:215], v[152:155]\n"
"v_add_f32_e32 v32, v32, v92\n"
"v_add_f32_e32 v33, v33, v93\n"
"v_mfma_f32_16x16x32_bf16 v[120:123], v[84:87], v[212:215], v[120:123]\n"
"buffer_load_dwordx4 a[8:11], v0, s[8:11], s97 offen offset:2048 sc1\n"
"ds_read_b128 v[28:31], v253\n"
"v_add_f32_e32 v34, v34, v94\n"
"v_add_f32_e32 v35, v35, v95\n"
"v_add_f32_e32 v36, v36, v40\n"
"v_add_f32_e32 v37, v37, v41\n"
"v_add_f32_e32 v38, v38, v42\n"
"v_add_f32_e32 v39, v39, v43\n"
"v_add_f32_e32 v44, v44, v48\n"
"v_add_f32_e32 v45, v45, v49\n"
"v_add_f32_e32 v46, v46, v50\n"
"v_add_f32_e32 v47, v47, v51\n"
"v_cvt_pk_bf16_f32 v252, v252, 0\n"
"v_lshlrev_b32_e32 v252, 16, v252\n"
"v_rcp_f32_e32 v251, v252\n"
"ds_read_b128 v[40:43], v253 offset:1024\n"
"v_add_u32_e32 v253, 0x26c00, v0\n"
"ds_read_b128 v[48:51], v253\n"
"ds_read_b128 v[52:55], v253 offset:1024\n"
"buffer_load_dwordx4 a[12:15], v0, s[8:11], s97 offen offset:3072 sc1\n"
"s_waitcnt lgkmcnt(0)\n"
"buffer_load_dwordx4 a[16:19], v1, s[8:11], s97 offen sc1\n"
"buffer_load_dwordx4 a[20:23], v1, s[8:11], s97 offen offset:1024 sc1\n"
"buffer_load_dwordx4 a[24:27], v1, s[8:11], s97 offen offset:2048 sc1\n"
"buffer_load_dwordx4 a[28:31], v1, s[8:11], s97 offen offset:3072 sc1\n"
"v_mov_b32_e32 v253, 0xff800000\n"
"v_mul_f32_e32 v252, 0x3e8293ee, v251\n"
"v_cndmask_b32_e64 v48, v48, v253, s[82:83]\n"
"v_cndmask_b32_e64 v49, v49, v253, s[82:83]\n"
"v_cndmask_b32_e64 v50, v50, v253, s[82:83]\n"
"v_cndmask_b32_e64 v51, v51, v253, s[82:83]\n"
"v_cndmask_b32_e64 v52, v52, v253, s[82:83]\n"
"v_cndmask_b32_e64 v53, v53, v253, s[82:83]\n"
"v_cndmask_b32_e64 v54, v54, v253, s[82:83]\n"
"v_cndmask_b32_e64 v55, v55, v253, s[82:83]\n"
".Lmk_b4:\n"
"; @B tail\n"
"s_add_u32 s97, s69, 0x2000\n"
"buffer_load_dwordx4 a[32:35], v0, s[8:11], s97 offen sc1\n"
"buffer_load_dwordx4 a[36:39], v0, s[8:11], s97 offen offset:1024 sc1\n"
"v_add_f32_e32 v196, v196, v96\n"
"v_add_f32_e32 v197, v197, v97\n"
"v_add_f32_e32 v198, v198, v98\n"
"v_add_f32_e32 v199, v199, v99\n"
"v_add_f32_e32 v144, v144, v116\n"
"v_add_f32_e32 v145, v145, v117\n"
"v_add_f32_e32 v146, v146, v118\n"
"v_add_f32_e32 v147, v147, v119\n"
"v_mul_f32_e32 v196, v196, v251\n"
"v_mul_f32_e32 v197, v197, v251\n"
"v_mul_f32_e32 v198, v198, v251\n"
"v_mul_f32_e32 v199, v199, v251\n"
"v_mul_f32_e32 v144, v144, v251\n"
"v_mul_f32_e32 v145, v145, v251\n"
"v_mul_f32_e32 v146, v146, v251\n"
"v_mul_f32_e32 v147, v147, v251\n"
"v_cvt_pk_bf16_f32 v56, v196, v197\n"
"v_cvt_pk_bf16_f32 v57, v198, v199\n"
"v_cvt_pk_bf16_f32 v58, v144, v145\n"
"v_cvt_pk_bf16_f32 v59, v146, v147\n"
"ds_write_b128 v7, v[56:59] offset:16384\n"
"buffer_load_dwordx4 a[40:43], v0, s[8:11], s97 offen offset:2048 sc1\n"
"buffer_load_dwordx4 a[44:47], v0, s[8:11], s97 offen offset:3072 sc1\n"
"v_add_f32_e32 v152, v152, v24\n"
"v_add_f32_e32 v153, v153, v25\n"
"v_add_f32_e32 v154, v154, v26\n"
"v_add_f32_e32 v155, v155, v27\n"
"v_add_f32_e32 v120, v120, v32\n"
"v_add_f32_e32 v121, v121, v33\n"
"v_add_f32_e32 v122, v122, v34\n"
"v_add_f32_e32 v123, v123, v35\n"
"v_mul_f32_e32 v152, v152, v251\n"
"v_mul_f32_e32 v153, v153, v251\n"
"v_mul_f32_e32 v154, v154, v251\n"
"v_mul_f32_e32 v155, v155, v251\n"
"v_mul_f32_e32 v120, v120, v251\n"
"v_mul_f32_e32 v121, v121, v251\n"
"v_mul_f32_e32 v122, v122, v251\n"
"v_mul_f32_e32 v123, v123, v251\n"
"v_cvt_pk_bf16_f32 v24, v152, v153\n"
"v_cvt_pk_bf16_f32 v25, v154, v155\n"
"v_cvt_pk_bf16_f32 v26, v120, v121\n"
"v_cvt_pk_bf16_f32 v27, v122, v123\n"
"ds_write_b128 v7, v[24:27] offset:49152\n"
"buffer_load_dwordx4 a[48:51], v1, s[8:11], s97 offen sc1\n"
"buffer_load_dwordx4 a[52:55], v1, s[8:11], s97 offen offset:1024 sc1\n"
"v_add_f32_e32 v112, v112, v36\n"
"v_add_f32_e32 v113, v113, v37\n"
"v_add_f32_e32 v114, v114, v38\n"
"v_add_f32_e32 v115, v115, v39\n"
"v_add_f32_e32 v180, v180, v44\n"
"v_add_f32_e32 v181, v181, v45\n"
"v_add_f32_e32 v182, v182, v46\n"
"v_add_f32_e32 v183, v183, v47\n"
"v_mul_f32_e32 v112, v112, v252\n"
"v_mul_f32_e32 v113, v113, v252\n"
"v_mul_f32_e32 v114, v114, v252\n"
"v_mul_f32_e32 v115, v115, v252\n"
"v_mul_f32_e32 v180, v180, v252\n"
"v_mul_f32_e32 v181, v181, v252\n"
"v_mul_f32_e32 v182, v182, v252\n"
"v_mul_f32_e32 v183, v183, v252\n"
"v_cvt_pk_bf16_f32 v24, v112, v113\n"
"v_cvt_pk_bf16_f32 v25, v114, v115\n"
"v_cvt_pk_bf16_f32 v26, v180, v181\n"
"v_cvt_pk_bf16_f32 v27, v182, v183\n"
"ds_write_b128 v2, v[24:27] offset:16384\n"
"s_waitcnt vmcnt(18)\n"
"v_cmp_ne_u64_e32 vcc, v[254:255], v[18:19]\n"
"s_cbranch_vccnz .Lmk_f48s\n"
".Lmk_f48r:\n"
"s_waitcnt lgkmcnt(0)\n"
"s_barrier\n"
"; @B AT a\n"
"s_cmp_lg_u32 s0, 0\n"
"s_cbranch_scc1 .Lmk_exit\n"
"ds_read_b128 v[24:27], v2 offset:17408\n"
"ds_read_b128 v[32:35], v15 offset:16384\n"
"v_xor_b32_e32 v255, 32, v15\n"
"ds_read_b128 v[36:39], v255 offset:16640\n"
"ds_read_b128 v[44:47], v2 offset:16384\n"
"ds_read_b128 v[56:59], v17 offset:15360\n"
"v_xor_b32_e32 v255, 32, v17\n"
"ds_read_b128 v[60:63], v255 offset:15616\n"
"ds_read_b64_tr_b16 v[64:65], v16 offset:16384\n"
"v_xor_b32_e32 v255, 32, v16\n"
"ds_read_b64_tr_b16 v[66:67], v255 offset:16640\n"
"ds_read_b64_tr_b16 v[68:69], v255 offset:16384\n"
"ds_read_b64_tr_b16 v[70:71], v16 offset:16640\n"
"ds_read_b64_tr_b16 v[76:77], v16 offset:15360\n"
"v_mov_b32_e32 v72, 0x3f803f80\n"
"v_mov_b32_e32 v73, 0x3f803f80\n"
"v_mov_b32_e32 v74, 0x3f803f80\n"
"v_mov_b32_e32 v75, 0x3f803f80\n"
"v_xor_b32_e32 v255, 32, v16\n"
"ds_read_b64_tr_b16 v[78:79], v255 offset:15616\n"
"ds_read_b64_tr_b16 v[80:81], v255 offset:15360\n"
"ds_read_b64_tr_b16 v[82:83], v16 offset:15616\n"
"v_mov_b32_e32 v84, 0x3f803f80\n"
"v_mov_b32_e32 v85, 0x3f803f80\n"
"v_mov_b32_e32 v86, 0x3f803f80\n"
"v_mov_b32_e32 v87, 0x3f803f80\n"
"s_waitcnt lgkmcnt(11)\n"
"v_mfma_f32_16x16x32_bf16 v[28:31], v[32:35], v[24:27], v[28:31]\n"
"v_mfma_f32_16x16x32_bf16 v[40:43], v[36:39], v[24:27], v[40:43]\n"
"s_waitcnt lgkmcnt(8)\n"
"v_mfma_f32_16x16x32_bf16 v[48:51], v[56:59], v[44:47], v[48:51]\n"
"v_mfma_f32_16x16x32_bf16 v[52:55], v[60:63], v[44:47], v[52:55]\n"
"buffer_load_dwordx4 a[56:59], v1, s[8:11], s97 offen offset:2048 sc1\n"
"buffer_load_dwordx4 a[60:63], v1, s[8:11], s97 offen offset:3072 sc1\n"
"s_add_u32 s98, s69, 0x4000\n"
"buffer_load_dwordx4 a[64:67], v0, s[8:11], s98 offen sc1\n"
"buffer_load_dwordx4 a[68:71], v0, s[8:11], s98 offen offset:1024 sc1\n"
"s_add_u32 s97, s56, 0x440000\n"
"buffer_load_dwordx4 v[24:27], v0, s[4:7], s97 offen sc1\n"
"buffer_load_dwordx4 v[32:35], v0, s[4:7], s97 offen offset:1024 sc1\n"
"buffer_load_dwordx4 v[36:39], v0, s[4:7], s97 offen offset:2048 sc1\n"
"buffer_load_dwordx4 v[44:47], v0, s[4:7], s97 offen offset:3072 sc1\n"
"v_max_f32_e32 v255, v28, v29\n"
"v_max_f32_e32 v254, v48, v49\n"
"v_max3_f32 v255, v255, v30, v31\n"
"v_max3_f32 v254, v254, v50, v51\n"
"v_max3_f32 v255, v255, v40, v41\n"
"v_max3_f32 v254, v254, v52, v53\n"
"v_max3_f32 v255, v255, v42, v43\n"
"v_max3_f32 v254, v254, v54, v55\n"
"v_mov_b32_e32 v253, v255\n"
"v_mov_b32_e32 v252, v254\n"
"s_nop 0\n"
"v_permlane16_swap_b32_e32 v255, v253\n"
"v_permlane16_swap_b32_e32 v254, v252\n"
"v_max_f32_e32 v255, v255, v253\n"
"v_max_f32_e32 v254, v254, v252\n"
"v_mov_b32_e32 v253, v255\n"
"v_mov_b32_e32 v252, v254\n"
"s_nop 0\n"
"v_permlane32_swap_b32_e32 v255, v253\n"
"v_permlane32_swap_b32_e32 v254, v252\n"
"v_max_f32_e32 v255, v255, v253\n"
"v_max_f32_e32 v254, v254, v252\n"
"v_sub_f32_e32 v28, v28, v255\n"
"v_sub_f32_e32 v29, v29, v255\n"
"v_sub_f32_e32 v30, v30, v255\n"
"v_sub_f32_e32 v31, v31, v255\n"
"v_sub_f32_e32 v40, v40, v255\n"
"v_sub_f32_e32 v41, v41, v255\n"
"v_sub_f32_e32 v42, v42, v255\n"
"v_sub_f32_e32 v43, v43, v255\n"
"v_exp_f32_e32 v28, v28\n"
"v_exp_f32_e32 v29, v29\n"
"v_exp_f32_e32 v30, v30\n"
"v_exp_f32_e32 v31, v31\n"
"v_exp_f32_e32 v40, v40\n"
"v_exp_f32_e32 v41, v41\n"
"v_exp_f32_e32 v42, v42\n"
"v_exp_f32_e32 v43, v43\n"
"v_cvt_pk_bf16_f32 v56, v28, v29\n"
"v_cvt_pk_bf16_f32 v57, v30, v31\n"
"v_cvt_pk_bf16_f32 v58, v40, v41\n"
"v_cvt_pk_bf16_f32 v59, v42, v43\n"
"s_waitcnt lgkmcnt(4)\n"
"v_sub_f32_e32 v48, v48, v254\n"
"v_sub_f32_e32 v49, v49, v254\n"
"v_sub_f32_e32 v50, v50, v254\n"
"v_sub_f32_e32 v51, v51, v254\n"
"v_sub_f32_e32 v52, v52, v254\n"
"v_sub_f32_e32 v53, v53, v254\n"
"v_sub_f32_e32 v54, v54, v254\n"
"v_mfma_f32_16x16x32_bf16 v[28:31], v[72:75], v[56:59], 0\n"
"v_sub_f32_e32 v55, v55, v254\n"
"v_exp_f32_e32 v48, v48\n"
"v_exp_f32_e32 v49, v49\n"
"v_exp_f32_e32 v50, v50\n"
"v_mfma_f32_16x16x32_bf16 v[40:43], v[64:67], v[56:59], 0\n"
"v_exp_f32_e32 v51, v51\n"
"v_exp_f32_e32 v52, v52\n"
"v_exp_f32_e32 v53, v53\n"
"v_exp_f32_e32 v54, v54\n"
"v_mfma_f32_16x16x32_bf16 v[60:63], v[68:71], v[56:59], 0\n"
"v_exp_f32_e32 v55, v55\n"
"v_cvt_pk_bf16_f32 v56, v48, v49\n"
"v_cvt_pk_bf16_f32 v57, v50, v51\n"
"v_cvt_pk_bf16_f32 v58, v52, v53\n"
"v_cvt_pk_bf16_f32 v59, v54, v55\n"
"buffer_load_dwordx4 v[48:51], v1, s[4:7], s97 offen sc1\n"
"buffer_load_dwordx4 v[52:55], v1, s[4:7], s97 offen offset:1024 sc1\n"
"buffer_load_dwordx4 v[64:67], v1, s[4:7], s97 offen offset:2048 sc1\n"
"buffer_load_dwordx4 v[68:71], v1, s[4:7], s97 offen offset:3072 sc1\n"
"s_waitcnt lgkmcnt(0)\n"
"v_rcp_f32_e32 v28, v28\n"
"s_nop 0\n"
"v_mul_f32_e32 v40, v40, v28\n"
"v_mul_f32_e32 v41, v41, v28\n"
"v_mul_f32_e32 v42, v42, v28\n"
"v_mfma_f32_16x16x32_bf16 v[72:75], v[84:87], v[56:59], 0\n"
"v_mul_f32_e32 v43, v43, v28\n"
"v_mul_f32_e32 v60, v60, v28\n"
"v_mul_f32_e32 v61, v61, v28\n"
"v_mul_f32_e32 v62, v62, v28\n"
"v_mul_f32_e32 v63, v63, v28\n"
"v_mfma_f32_16x16x32_bf16 v[28:31], v[76:79], v[56:59], 0\n"
"s_add_u32 s97, s60, 0x408000\n"
"v_cvt_pk_bf16_f32 v76, v40, v41\n"
"v_cvt_pk_bf16_f32 v77, v42, v43\n"
"v_cvt_pk_bf16_f32 v78, v60, v61\n"
"v_cvt_pk_bf16_f32 v79, v62, v63\n"
"v_mfma_f32_16x16x32_bf16 v[40:43], v[80:83], v[56:59], 0\n"
"buffer_store_dwordx4 v[76:79], v0, s[4:7], s97 offen sc1\n"
"s_add_u32 s97, s60, 0x400000\n"
"v_rcp_f32_e32 v72, v72\n"
"s_nop 0\n"
"v_mul_f32_e32 v28, v28, v72\n"
"v_mul_f32_e32 v29, v29, v72\n"
"v_mul_f32_e32 v30, v30, v72\n"
"v_mul_f32_e32 v31, v31, v72\n"
"v_mul_f32_e32 v40, v40, v72\n"
"v_mul_f32_e32 v41, v41, v72\n"
"v_mul_f32_e32 v42, v42, v72\n"
"v_mul_f32_e32 v43, v43, v72\n"
"v_cvt_pk_bf16_f32 v56, v28, v29\n"
"v_cvt_pk_bf16_f32 v57, v30, v31\n"
"v_cvt_pk_bf16_f32 v58, v40, v41\n"
"v_cvt_pk_bf16_f32 v59, v42, v43\n"
"buffer_store_dwordx4 v[56:59], v0, s[4:7], s97 offen sc1\n"
"s_add_u32 s97, s57, 0x440000\n"
"buffer_load_dwordx4 v[28:31], v0, s[4:7], s97 offen sc1\n"
"buffer_load_dwordx4 v[40:43], v0, s[4:7], s97 offen offset:1024 sc1\n"
"buffer_load_dwordx4 v[56:59], v0, s[4:7], s97 offen offset:2048 sc1\n"
"buffer_load_dwordx4 v[60:63], v0, s[4:7], s97 offen offset:3072 sc1\n"
"buffer_load_dwordx4 v[72:75], v1, s[4:7], s97 offen sc1\n"
"buffer_load_dwordx4 v[76:79], v1, s[4:7], s97 offen offset:1024 sc1\n"
"buffer_load_dwordx4 v[80:83], v1, s[4:7], s97 offen offset:2048 sc1\n"
"buffer_load_dwordx4 v[84:87], v1, s[4:7], s97 offen offset:3072 sc1\n"
"s_add_u32 s97, s58, 0x440000\n"
"buffer_load_dwordx4 v[88:91], v0, s[4:7], s97 offen sc1\n"
"buffer_load_dwordx4 v[92:95], v0, s[4:7], s97 offen offset:1024 sc1\n"
"buffer_load_dwordx4 v[96:99], v0, s[4:7], s97 offen offset:2048 sc1\n"
"buffer_load_dwordx4 v[100:103], v0, s[4:7], s97 offen offset:3072 sc1\n"
"buffer_load_dwordx4 v[104:107], v1, s[4:7], s97 offen sc1\n"
"buffer_load_dwordx4 v[108:111], v1, s[4:7], s97 offen offset:1024 sc1\n"
"buffer_load_dwordx4 v[112:115], v1, s[4:7], s97 offen offset:2048 sc1\n"
"buffer_load_dwordx4 v[116:119], v1, s[4:7], s97 offen offset:3072 sc1\n"
"buffer_load_dwordx4 a[72:75], v0, s[8:11], s98 offen offset:2048 sc1\n"
"buffer_load_dwordx4 a[76:79], v0, s[8:11], s98 offen offset:3072 sc1\n"
"buffer_load_dwordx4 a[80:83], v1, s[8:11], s98 offen sc1\n"
"buffer_load_dwordx4 a[84:87], v1, s[8:11], s98 offen offset:1024 sc1\n"
"buffer_load_dwordx4 a[88:91], v1, s[8:11], s98 offen offset:2048 sc1\n"
"buffer_load_dwordx4 a[92:95], v1, s[8:11], s98 offen offset:3072 sc1\n"
"s_add_u32 s98, s69, 0x6000\n"
"buffer_load_dwordx4 a[96:99], v0, s[8:11], s98 offen sc1\n"
"buffer_load_dwordx4 a[100:103], v0, s[8:11], s98 offen offset:1024 sc1\n"
"s_waitcnt vmcnt(33)\n"
"; @B G2 b.0\n"
"v_mfma_f32_16x16x32_bf16 v[120:123], a[128:131], v[24:27], 0\n"
"v_mfma_f32_16x16x32_bf16 v[124:127], a[132:135], v[24:27], 0\n"
"v_mfma_f32_16x16x32_bf16 v[128:131], a[136:139], v[24:27], 0\n"
"s_add_u32 s97, s59, 0x440000\n"
"buffer_load_dwordx4 v[132:135], v0, s[4:7], s97 offen sc1\n"
"v_mfma_f32_16x16x32_bf16 v[136:139], a[140:143], v[24:27], 0\n"
"s_waitcnt vmcnt(33)\n"
"v_mfma_f32_16x16x32_bf16 v[120:123], a[144:147], v[32:35], v[120:123]\n"
"v_mfma_f32_16x16x32_bf16 v[124:127], a[148:151], v[32:35], v[124:127]\n"
"buffer_load_dwordx4 v[24:27], v0, s[4:7], s97 offen offset:1024 sc1\n"
"v_mfma_f32_16x16x32_bf16 v[128:131], a[152:155], v[32:35], v[128:131]\n"
"v_mfma_f32_16x16x32_bf16 v[136:139], a[156:159], v[32:35], v[136:139]\n"
"s_waitcnt vmcnt(33)\n"
"v_mfma_f32_16x16x32_bf16 v[120:123], a[160:163], v[36:39], v[120:123]\n"
"buffer_load_dwordx4 v[32:35], v0, s[4:7], s97 offen offset:2048 sc1\n"
"v_mfma_f32_16x16x32_bf16 v[124:127], a[164:167], v[36:39], v[124:127]\n"
"v_mfma_f32_16x16x32_bf16 v[128:131], a[168:171], v[36:39], v[128:131]\n"
"v_mfma_f32_16x16x32_bf16 v[136:139], a[172:175], v[36:39], v[136:139]\n"
"s_waitcnt vmcnt(33)\n"
"buffer_load_dwordx4 v[36:39], v0, s[4:7], s97 offen offset:3072 sc1\n"
"v_mfma_f32_16x16x32_bf16 v[120:123], a[176:179], v[44:47], v[120:123]\n"
"v_mfma_f32_16x16x32_bf16 v[124:127], a[180:183], v[44:47], v[124:127]\n"
"v_mfma_f32_16x16x32_bf16 v[128:131], a[184:187], v[44:47], v[128:131]\n"
"buffer_load_dwordx4 v[140:143], v1, s[4:7], s97 offen sc1\n"
"v_mfma_f32_16x16x32_bf16 v[136:139], a[188:191], v[44:47], v[136:139]\n"
"s_waitcnt vmcnt(34)\n"
"v_mfma_f32_16x16x32_bf16 v[120:123], a[192:195], v[48:51], v[120:123]\n"
"v_mfma_f32_16x16x32_bf16 v[124:127], a[196:199], v[48:51], v[124:127]\n"
"buffer_load_dwordx4 v[44:47], v1, s[4:7], s97 offen offset:1024 sc1\n"
"v_mfma_f32_16x16x32_bf16 v[128:131], a[200:203], v[48:51], v[128:131]\n"
"v_mfma_f32_16x16x32_bf16 v[136:139], a[204:207], v[48:51], v[136:139]\n"
"s_waitcnt vmcnt(34)\n"
"v_mfma_f32_16x16x32_bf16 v[120:123], a[208:211], v[52:55], v[120:123]\n"
"buffer_load_dwordx4 v[48:51], v1, s[4:7], s97 offen offset:2048 sc1\n"
"v_mfma_f32_16x16x32_bf16 v[124:127], a[212:215], v[52:55], v[124:127]\n"
"v_mfma_f32_16x16x32_bf16 v[128:131], a[216:219], v[52:55], v[128:131]\n"
"v_mfma_f32_16x16x32_bf16 v[136:139], a[220:223], v[52:55], v[136:139]\n"
"s_waitcnt vmcnt(34)\n"
"buffer_load_dwordx4 v[52:55], v1, s[4:7], s97 offen offset:3072 sc1\n"
"v_mfma_f32_16x16x32_bf16 v[120:123], a[224:227], v[64:67], v[120:123]\n"
"v_mfma_f32_16x16x32_bf16 v[124:127], a[228:231], v[64:67], v[124:127]\n"
"v_mfma_f32_16x16x32_bf16 v[128:131], a[232:235], v[64:67], v[128:131]\n"
"v_mfma_f32_16x16x32_bf16 v[136:139], a[236:239], v[64:67], v[136:139]\n"
"s_waitcnt vmcnt(34)\n"
"v_mfma_f32_16x16x32_bf16 v[120:123], a[240:243], v[68:71], v[120:123]\n"
"s_mov_b32 s97, .Lmk_a0-.Lmk_g79\n"
"s_getpc_b64 s[84:85]\n"
".Lmk_g79:\n"
"s_add_u32 s84, s84, s97\n"
"s_addc_u32 s85, s85, 0\n"
"s_and_b32 s84, s84, -16\n"
"v_mfma_f32_16x16x32_bf16 v[124:127], a[244:247], v[68:71], v[124:127]\n"
"s_and_b32 s85, s85, 0xffff\n"
"s_sub_u32 s97, .Lmk_end-.Lmk_g79, s97\n"
"s_min_u32 s86, s97, 0x3000\n"
"s_mov_b32 s87, 0x20000\n"
"v_mfma_f32_16x16x32_bf16 v[128:131], a[248:251], v[68:71], v[128:131]\n"
"s_mov_b32 m0, 0x27800\n"
"s_nop 0\n"
"buffer_load_dwordx4 v0, s[84:87], s99 offen lds\n"
"v_mfma_f32_16x16x32_bf16 v[136:139], a[252:255], v[68:71], v[136:139]\n"
"s_waitcnt vmcnt(31)\n"
"; @B G2 b.1\n"
"v_mfma_f32_16x16x32_bf16 v[64:67], a[128:131], v[28:31], 0\n"
"v_cmp_u_f32_e32 vcc, v120, v120\n"
"v_mfma_f32_16x16x32_bf16 v[68:71], a[132:135], v[28:31], 0\n"
"s_cbranch_vccnz .Lmk_p74s\n"
".Lmk_p74r:\n"
"v_mfma_f32_16x16x32_bf16 v[144:147], a[136:139], v[28:31], 0\n"
"v_mfma_f32_16x16x32_bf16 v[148:151], a[140:143], v[28:31], 0\n"
"v_mfma_f32_16x16x32_bf16 v[64:67], a[144:147], v[40:43], v[64:67]\n"
"ds_write_b128 v3, v[120:123]\n"
"v_mfma_f32_16x16x32_bf16 v[68:71], a[148:151], v[40:43], v[68:71]\n"
"v_mfma_f32_16x16x32_bf16 v[144:147], a[152:155], v[40:43], v[144:147]\n"
"ds_write_b128 v3, v[124:127] offset:1024\n"
"v_mfma_f32_16x16x32_bf16 v[148:151], a[156:159], v[40:43], v[148:151]\n"
"s_waitcnt vmcnt(29)\n"
"v_mfma_f32_16x16x32_bf16 v[64:67], a[160:163], v[56:59], v[64:67]\n"
"ds_write_b128 v3, v[128:131] offset:2048\n"
"v_mfma_f32_16x16x32_bf16 v[68:71], a[164:167], v[56:59], v[68:71]\n"
"s_add_u32 s97, s56, 0x400000\n"
"buffer_load_dwordx4 v[28:31], v0, s[4:7], s97 offen sc1\n"
"v_mfma_f32_16x16x32_bf16 v[144:147], a[168:171], v[56:59], v[144:147]\n"
"ds_write_b128 v3, v[136:139] offset:3072\n"
"v_mfma_f32_16x16x32_bf16 v[148:151], a[172:175], v[56:59], v[148:151]\n"
"v_mfma_f32_16x16x32_bf16 v[64:67], a[176:179], v[60:63], v[64:67]\n"
"buffer_load_dwordx4 v[40:43], v0, s[4:7], s97 offen offset:1024 sc1\n"
"v_mfma_f32_16x16x32_bf16 v[68:71], a[180:183], v[60:63], v[68:71]\n"
"ds_read_b128 v[56:59], v2 offset:8192\n"
"ds_read_b128 v[120:123], v2 offset:9216\n"
"v_mfma_f32_16x16x32_bf16 v[144:147], a[184:187], v[60:63], v[144:147]\n"
"v_mfma_f32_16x16x32_bf16 v[148:151], a[188:191], v[60:63], v[148:151]\n"
"s_waitcnt vmcnt(29)\n"
"buffer_load_dwordx4 v[60:63], v0, s[4:7], s97 offen offset:2048 sc1\n"
"v_mfma_f32_16x16x32_bf16 v[64:67], a[192:195], v[72:75], v[64:67]\n"
"v_mfma_f32_16x16x32_bf16 v[68:71], a[196:199], v[72:75], v[68:71]\n"
"v_mfma_f32_16x16x32_bf16 v[144:147], a[200:203], v[72:75], v[144:147]\n"
"buffer_load_dwordx4 v[124:127], v0, s[4:7], s97 offen offset:3072 sc1\n"
"v_mfma_f32_16x16x32_bf16 v[148:151], a[204:207], v[72:75], v[148:151]\n"
"s_waitcnt lgkmcnt(0)\n"
"s_barrier\n"
"v_mfma_f32_16x16x32_bf16 v[64:67], a[208:211], v[76:79], v[64:67]\n"
"v_lshlrev_b32_e32 v255, 16, v56\n"
"v_and_b32_e32 v254, 0xffff0000, v56\n"
"v_mfma_f32_16x16x32_bf16 v[68:71], a[212:215], v[76:79], v[68:71]\n"
"buffer_load_dwordx4 v[72:75], v1, s[4:7], s97 offen sc1\n"
"v_mfma_f32_16x16x32_bf16 v[144:147], a[216:219], v[76:79], v[144:147]\n"
"ds_read_b128 v[128:131], v4\n"
"ds_read_b128 v[136:139], v4 offset:1024\n"
"v_mfma_f32_16x16x32_bf16 v[148:151], a[220:223], v[76:79], v[148:151]\n"
"s_waitcnt vmcnt(30)\n"
"ds_read_b128 v[76:79], v4 offset:2048\n"
"ds_read_b128 v[152:155], v4 offset:3072\n"
"v_mfma_f32_16x16x32_bf16 v[64:67], a[224:227], v[80:83], v[64:67]\n"
"buffer_load_dwordx4 v[156:159], v1, s[4:7], s97 offen offset:1024 sc1\n"
"v_mfma_f32_16x16x32_bf16 v[68:71], a[228:231], v[80:83], v[68:71]\n"
"v_lshlrev_b32_e32 v253, 16, v57\n"
"v_and_b32_e32 v252, 0xffff0000, v57\n"
"v_mfma_f32_16x16x32_bf16 v[144:147], a[232:235], v[80:83], v[144:147]\n"
"v_lshlrev_b32_e32 v251, 16, v58\n"
"v_and_b32_e32 v250, 0xffff0000, v58\n"
"v_mfma_f32_16x16x32_bf16 v[148:151], a[236:239], v[80:83], v[148:151]\n"
"buffer_load_dwordx4 v[80:83], v1, s[4:7], s97 offen offset:2048 sc1\n"
"v_mfma_f32_16x16x32_bf16 v[64:67], a[240:243], v[84:87], v[64:67]\n"
"v_lshlrev_b32_e32 v249, 16, v59\n"
"v_and_b32_e32 v248, 0xffff0000, v59\n"
"v_mfma_f32_16x16x32_bf16 v[68:71], a[244:247], v[84:87], v[68:71]\n"
"v_lshlrev_b32_e32 v247, 16, v120\n"
"v_and_b32_e32 v246, 0xffff0000, v120\n"
"v_mfma_f32_16x16x32_bf16 v[144:147], a[248:251], v[84:87], v[144:147]\n"
"buffer_load_dwordx4 v[56:59], v1, s[4:7], s97 offen offset:3072 sc1\n"
"v_mfma_f32_16x16x32_bf16 v[148:151], a[252:255], v[84:87], v[148:151]\n"
"s_waitcnt vmcnt(31)\n"
"s_add_u32 s97, s57, 0x400000\n"
"v_lshlrev_b32_e32 v245, 16, v121\n"
"; @B G2 b.2\n"
"v_mfma_f32_16x16x32_bf16 v[84:87], a[128:131], v[88:91], 0\n"
"v_cmp_u_f32_e32 vcc, v64, v64\n"
"v_and_b32_e32 v244, 0xffff0000, v121\n"
"v_mfma_f32_16x16x32_bf16 v[160:163], a[132:135], v[88:91], 0\n"
"s_cbranch_vccnz .Lmk_p75s\n"
".Lmk_p75r:\n"
"v_lshlrev_b32_e32 v243, 16, v122\n"
"v_mfma_f32_16x16x32_bf16 v[164:167], a[136:139], v[88:91], 0\n"
"buffer_load_dwordx4 v[168:171], v0, s[4:7], s97 offen sc1\n"
"v_mfma_f32_16x16x32_bf16 v[172:175], a[140:143], v[88:91], 0\n"
"v_and_b32_e32 v242, 0xffff0000, v122\n"
"v_lshlrev_b32_e32 v241, 16, v123\n"
"v_mfma_f32_16x16x32_bf16 v[84:87], a[144:147], v[92:95], v[84:87]\n"
"ds_write_b128 v3, v[64:67] offset:6144\n"
"v_mfma_f32_16x16x32_bf16 v[160:163], a[148:151], v[92:95], v[160:163]\n"
"buffer_load_dwordx4 v[64:67], v0, s[4:7], s97 offen offset:1024 sc1\n"
"v_mfma_f32_16x16x32_bf16 v[164:167], a[152:155], v[92:95], v[164:167]\n"
"ds_write_b128 v3, v[68:71] offset:7168\n"
"v_mfma_f32_16x16x32_bf16 v[172:175], a[156:159], v[92:95], v[172:175]\n"
"s_waitcnt vmcnt(31)\n"
"v_and_b32_e32 v240, 0xffff0000, v123\n"
"s_waitcnt lgkmcnt(2)\n"
"v_mfma_f32_16x16x32_bf16 v[84:87], a[160:163], v[96:99], v[84:87]\n"
"ds_write_b128 v3, v[144:147] offset:8192\n"
"v_mfma_f32_16x16x32_bf16 v[160:163], a[164:167], v[96:99], v[160:163]\n"
"buffer_load_dwordx4 v[68:71], v0, s[4:7], s97 offen offset:2048 sc1\n"
"v_mfma_f32_16x16x32_bf16 v[164:167], a[168:171], v[96:99], v[164:167]\n"
"ds_write_b128 v3, v[148:151] offset:9216\n"
"v_mfma_f32_16x16x32_bf16 v[172:175], a[172:175], v[96:99], v[172:175]\n"
"v_add_f32_e32 v128, v128, v255\n"
"v_add_f32_e32 v129, v129, v254\n"
"v_mfma_f32_16x16x32_bf16 v[84:87], a[176:179], v[100:103], v[84:87]\n"
"buffer_load_dwordx4 v[88:91], v0, s[4:7], s97 offen offset:3072 sc1\n"
"v_mfma_f32_16x16x32_bf16 v[160:163], a[180:183], v[100:103], v[160:163]\n"
"v_add_f32_e32 v130, v130, v253\n"
"v_add_f32_e32 v131, v131, v252\n"
"v_mfma_f32_16x16x32_bf16 v[164:167], a[184:187], v[100:103], v[164:167]\n"
"v_add_f32_e32 v136, v136, v251\n"
"v_add_f32_e32 v137, v137, v250\n"
"v_mfma_f32_16x16x32_bf16 v[172:175], a[188:191], v[100:103], v[172:175]\n"
"s_waitcnt vmcnt(31)\n"
"buffer_load_dwordx4 v[92:95], v1, s[4:7], s97 offen sc1\n"
"v_mfma_f32_16x16x32_bf16 v[84:87], a[192:195], v[104:107], v[84:87]\n"
"v_add_f32_e32 v138, v138, v249\n"
"v_add_f32_e32 v139, v139, v248\n"
"v_mfma_f32_16x16x32_bf16 v[160:163], a[196:199], v[104:107], v[160:163]\n"
"v_add_f32_e32 v76, v76, v247\n"
"v_add_f32_e32 v77, v77, v246\n"
"v_mfma_f32_16x16x32_bf16 v[164:167], a[200:203], v[104:107], v[164:167]\n"
"buffer_load_dwordx4 v[96:99], v1, s[4:7], s97 offen offset:1024 sc1\n"
"v_mfma_f32_16x16x32_bf16 v[172:175], a[204:207], v[104:107], v[172:175]\n"
"v_add_f32_e32 v78, v78, v245\n"
"v_add_f32_e32 v79, v79, v244\n"
"v_mfma_f32_16x16x32_bf16 v[84:87], a[208:211], v[108:111], v[84:87]\n"
"v_add_f32_e32 v152, v152, v243\n"
"v_add_f32_e32 v153, v153, v242\n"
"v_mfma_f32_16x16x32_bf16 v[160:163], a[212:215], v[108:111], v[160:163]\n"
"buffer_load_dwordx4 v[100:103], v1, s[4:7], s97 offen offset:2048 sc1\n"
"v_mfma_f32_16x16x32_bf16 v[164:167], a[216:219], v[108:111], v[164:167]\n"
"v_add_f32_e32 v154, v154, v241\n"
"v_add_f32_e32 v155, v155, v240\n"
"v_mfma_f32_16x16x32_bf16 v[172:175], a[220:223], v[108:111], v[172:175]\n"
"s_waitcnt vmcnt(32)\n"
"v_mfma_f32_16x16x32_bf16 v[84:87], a[224:227], v[112:115], v[84:87]\n"
"s_waitcnt vmcnt(22)\n"
"v_max_u32_e32 v255, v132, v133\n"
"v_max3_u32 v255, v255, v134, v135\n"
"v_mfma_f32_16x16x32_bf16 v[160:163], a[228:231], v[112:115], v[160:163]\n"
"s_waitcnt vmcnt(20)\n"
"v_max3_u32 v255, v255, v24, v25\n"
"v_max3_u32 v255, v255, v26, v27\n"
"v_mfma_f32_16x16x32_bf16 v[164:167], a[232:235], v[112:115], v[164:167]\n"
"v_max3_u32 v255, v255, v32, v33\n"
"v_max3_u32 v255, v255, v34, v35\n"
"v_mfma_f32_16x16x32_bf16 v[172:175], a[236:239], v[112:115], v[172:175]\n"
"v_max3_u32 v255, v255, v36, v37\n"
"v_max3_u32 v255, v255, v38, v39\n"
"v_mfma_f32_16x16x32_bf16 v[84:87], a[240:243], v[116:119], v[84:87]\n"
"v_cmp_eq_u32_e32 vcc, -1, v255\n"
"v_mfma_f32_16x16x32_bf16 v[160:163], a[244:247], v[116:119], v[160:163]\n"
"s_cbranch_vccnz .Lmk_q77s\n"
".Lmk_q77r:\n"
"v_mfma_f32_16x16x32_bf16 v[164:167], a[248:251], v[116:119], v[164:167]\n"
"buffer_load_dwordx4 v[104:107], v1, s[4:7], s97 offen offset:3072 sc1\n"
"v_mfma_f32_16x16x32_bf16 v[172:175], a[252:255], v[116:119], v[172:175]\n"
"s_add_u32 s97, s58, 0x400000\n"
"; @B G2 b.3\n"
"v_mfma_f32_16x16x32_bf16 v[128:131], a[128:131], v[132:135], v[128:131]\n"
"v_cmp_u_f32_e32 vcc, v84, v84\n"
"v_mfma_f32_16x16x32_bf16 v[136:139], a[132:135], v[132:135], v[136:139]\n"
"s_cbranch_vccnz .Lmk_p76s\n"
".Lmk_p76r:\n"
"v_mfma_f32_16x16x32_bf16 v[76:79], a[136:139], v[132:135], v[76:79]\n"
"buffer_load_dwordx4 v[108:111], v0, s[4:7], s97 offen sc1\n"
"v_mfma_f32_16x16x32_bf16 v[152:155], a[140:143], v[132:135], v[152:155]\n"
"v_mfma_f32_16x16x32_bf16 v[128:131], a[144:147], v[24:27], v[128:131]\n"
"ds_write_b128 v3, v[84:87] offset:12288\n"
"v_mfma_f32_16x16x32_bf16 v[136:139], a[148:151], v[24:27], v[136:139]\n"
"buffer_load_dwordx4 v[84:87], v0, s[4:7], s97 offen offset:1024 sc1\n"
"v_mfma_f32_16x16x32_bf16 v[76:79], a[152:155], v[24:27], v[76:79]\n"
"ds_write_b128 v3, v[160:163] offset:13312\n"
"v_mfma_f32_16x16x32_bf16 v[152:155], a[156:159], v[24:27], v[152:155]\n"
"v_mfma_f32_16x16x32_bf16 v[128:131], a[160:163], v[32:35], v[128:131]\n"
"s_waitcnt vmcnt(21)\n"
"v_max_u32_e32 v255, v140, v141\n"
"v_max3_u32 v255, v255, v142, v143\n"
"v_mfma_f32_16x16x32_bf16 v[136:139], a[164:167], v[32:35], v[136:139]\n"
"s_waitcnt vmcnt(19)\n"
"v_max3_u32 v255, v255, v44, v45\n"
"v_max3_u32 v255, v255, v46, v47\n"
"v_mfma_f32_16x16x32_bf16 v[76:79], a[168:171], v[32:35], v[76:79]\n"
"v_max3_u32 v255, v255, v48, v49\n"
"v_max3_u32 v255, v255, v50, v51\n"
"v_mfma_f32_16x16x32_bf16 v[152:155], a[172:175], v[32:35], v[152:155]\n"
"v_max3_u32 v255, v255, v52, v53\n"
"v_max3_u32 v255, v255, v54, v55\n"
"v_mfma_f32_16x16x32_bf16 v[128:131], a[176:179], v[36:39], v[128:131]\n"
"v_cmp_eq_u32_e32 vcc, -1, v255\n"
"v_mfma_f32_16x16x32_bf16 v[136:139], a[180:183], v[36:39], v[136:139]\n"
"s_cbranch_vccnz .Lmk_q78s\n"
".Lmk_q78r:\n"
"v_mfma_f32_16x16x32_bf16 v[76:79], a[184:187], v[36:39], v[76:79]\n"
"ds_write_b128 v3, v[164:167] offset:14336\n"
"v_mfma_f32_16x16x32_bf16 v[152:155], a[188:191], v[36:39], v[152:155]\n"
"buffer_load_dwordx4 v[24:27], v0, s[4:7], s97 offen offset:2048 sc1\n"
"v_mfma_f32_16x16x32_bf16 v[128:131], a[192:195], v[140:143], v[128:131]\n"
"ds_write_b128 v3, v[172:175] offset:15360\n"
"v_mfma_f32_16x16x32_bf16 v[136:139], a[196:199], v[140:143], v[136:139]\n"
"v_mfma_f32_16x16x32_bf16 v[76:79], a[200:203], v[140:143], v[76:79]\n"
"buffer_load_dwordx4 v[32:35], v0, s[4:7], s97 offen offset:3072 sc1\n"
"v_mfma_f32_16x16x32_bf16 v[152:155], a[204:207], v[140:143], v[152:155]\n"
"s_waitcnt lgkmcnt(0)\n"
"s_barrier\n"
"v_mfma_f32_16x16x32_bf16 v[128:131], a[208:211], v[44:47], v[128:131]\n"
"ds_read_b128 v[36:39], v5\n"
"ds_read_b128 v[112:115], v6\n"
"v_mfma_f32_16x16x32_bf16 v[136:139], a[212:215], v[44:47], v[136:139]\n"
"buffer_load_dwordx4 v[116:119], v1, s[4:7], s97 offen sc1\n"
"v_mfma_f32_16x16x32_bf16 v[76:79], a[216:219], v[44:47], v[76:79]\n"
"ds_read_b128 v[120:123], v5 offset:1024\n"
"ds_read_b128 v[132:135], v6 offset:1024\n"
"v_mfma_f32_16x16x32_bf16 v[152:155], a[220:223], v[44:47], v[152:155]\n"
"ds_read_b128 v[44:47], v5 offset:2048\n"
"ds_read_b128 v[140:143], v6 offset:2048\n"
"v_mfma_f32_16x16x32_bf16 v[128:131], a[224:227], v[48:51], v[128:131]\n"
"buffer_load_dwordx4 v[144:147], v1, s[4:7], s97 offen offset:1024 sc1\n"
"v_mfma_f32_16x16x32_bf16 v[136:139], a[228:231], v[48:51], v[136:139]\n"
"ds_read_b128 v[148:151], v5 offset:3072\n"
"ds_read_b128 v[160:163], v6 offset:3072\n"
"v_mfma_f32_16x16x32_bf16 v[76:79], a[232:235], v[48:51], v[76:79]\n"
"v_mfma_f32_16x16x32_bf16 v[152:155], a[236:239], v[48:51], v[152:155]\n"
"buffer_load_dwordx4 v[48:51], v1, s[4:7], s97 offen offset:2048 sc1\n"
"v_mfma_f32_16x16x32_bf16 v[128:131], a[240:243], v[52:55], v[128:131]\n"
"s_waitcnt lgkmcnt(4)\n"
"v_add_f32_e32 v36, v36, v112\n"
"v_add_f32_e32 v37, v37, v113\n"
"v_mfma_f32_16x16x32_bf16 v[136:139], a[244:247], v[52:55], v[136:139]\n"
"v_add_f32_e32 v38, v38, v114\n"
"v_add_f32_e32 v39, v39, v115\n"
"v_mfma_f32_16x16x32_bf16 v[76:79], a[248:251], v[52:55], v[76:79]\n"
"buffer_load_dwordx4 v[112:115], v1, s[4:7], s97 offen offset:3072 sc1\n"
"v_mfma_f32_16x16x32_bf16 v[152:155], a[252:255], v[52:55], v[152:155]\n"
"s_waitcnt lgkmcnt(0)\n"
"v_add_f32_e32 v120, v120, v132\n"
"v_add_f32_e32 v121, v121, v133\n"
"v_add_f32_e32 v122, v122, v134\n"
"v_add_f32_e32 v123, v123, v135\n"
"v_add_f32_e32 v44, v44, v140\n"
"v_add_f32_e32 v45, v45, v141\n"
"v_add_f32_e32 v46, v46, v142\n"
"v_add_f32_e32 v47, v47, v143\n"
"v_add_f32_e32 v148, v148, v160\n"
"v_add_f32_e32 v149, v149, v161\n"
"v_add_f32_e32 v150, v150, v162\n"
"v_add_f32_e32 v151, v151, v163\n"
"; @B fin b\n"
"s_add_u32 s97, s59, 0x400000\n"
"buffer_load_dwordx4 v[52:55], v0, s[4:7], s97 offen sc1\n"
"v_mov_b32_e32 v132, 0x3f803f80\n"
"v_mov_b32_e32 v133, 0x3f803f80\n"
"v_mov_b32_e32 v134, 0x3f803f80\n"
"v_mov_b32_e32 v135, 0x3f803f80\n"
"v_add_f32_e32 v36, v128, v36\n"
"v_add_f32_e32 v37, v129, v37\n"
"v_add_f32_e32 v38, v130, v38\n"
"v_add_f32_e32 v39, v131, v39\n"
"v_add_f32_e32 v120, v136, v120\n"
"buffer_load_dwordx4 v[128:131], v0, s[4:7], s97 offen offset:1024 sc1\n"
"s_add_u32 s96, s61, 0x840000\n"
"v_add_f32_e32 v121, v137, v121\n"
"v_add_f32_e32 v122, v138, v122\n"
"v_add_f32_e32 v123, v139, v123\n"
"v_cvt_pk_bf16_f32 v136, v36, v37\n"
"v_cvt_pk_bf16_f32 v137, v38, v39\n"
"v_cvt_pk_bf16_f32 v138, v120, v121\n"
"v_cvt_pk_bf16_f32 v139, v122, v123\n"
"buffer_store_dwordx4 v[136:139], v0, s[4:7], s96 offen sc1\n"
"buffer_load_dwordx4 v[36:39], v0, s[4:7], s97 offen offset:2048 sc1\n"
"ds_write_b128 v2, v[136:139] offset:8192\n"
"buffer_load_dwordx4 v[120:123], v0, s[4:7], s97 offen offset:3072 sc1\n"
"buffer_load_dwordx4 v[140:143], v1, s[4:7], s97 offen sc1\n"
"s_add_u32 s96, s61, 0x840000\n"
"v_add_f32_e32 v44, v76, v44\n"
"v_add_f32_e32 v45, v77, v45\n"
"v_add_f32_e32 v46, v78, v46\n"
"v_add_f32_e32 v47, v79, v47\n"
"v_add_f32_e32 v148, v152, v148\n"
"v_add_f32_e32 v149, v153, v149\n"
"v_add_f32_e32 v150, v154, v150\n"
"v_add_f32_e32 v151, v155, v151\n"
"v_cvt_pk_bf16_f32 v76, v44, v45\n"
"v_cvt_pk_bf16_f32 v77, v46, v47\n"
"v_cvt_pk_bf16_f32 v78, v148, v149\n"
"v_cvt_pk_bf16_f32 v79, v150, v151\n"
"buffer_store_dwordx4 v[76:79], v0, s[4:7], s96 offen offset:1024 sc1\n"
"buffer_load_dwordx4 v[44:47], v1, s[4:7], s97 offen offset:1024 sc1\n"
"ds_write_b128 v2, v[76:79] offset:9216\n"
"v_mfma_f32_16x16x32_bf16 v[148:151], v[132:135], v[136:139], 0\n"
"v_mfma_f32_16x16x32_bf16 v[152:155], v[136:139], v[136:139], 0\n"
"buffer_load_dwordx4 v[136:139], v1, s[4:7], s97 offen offset:2048 sc1\n"
"v_mfma_f32_16x16x32_bf16 v[148:151], v[132:135], v[76:79], v[148:151]\n"
"v_mfma_f32_16x16x32_bf16 v[152:155], v[76:79], v[76:79], v[152:155]\n"
"s_mov_b32 s94, 0xaaaaaaaa\n"
"s_mov_b32 s95, 0xaaaaaaaa\n"
"s_nop 5\n"
"v_cndmask_b32_e64 v152, v152, v153, s[94:95]\n"
"v_cndmask_b32_e64 v154, v154, v155, s[94:95]\n"
"s_mov_b32 s94, 0xcccccccc\n"
"s_mov_b32 s95, 0xcccccccc\n"
"buffer_load_dwordx4 v[76:79], v1, s[4:7], s97 offen offset:3072 sc1\n"
"v_cndmask_b32_e64 v149, v152, v154, s[94:95]\n"
"s_mov_b32 s94, 0xf0000f\n"
"s_mov_b32 s95, 0xf0000f00\n"
"s_add_u32 s97, s63, 0x84000\n"
"v_cndmask_b32_e64 v148, v148, v149, s[94:95]\n"
"buffer_store_dword v148, v14, s[4:7], s97 offen sc1\n"
"s_waitcnt vmcnt(34)\n"
"; @B G2 a.0\n"
"v_mfma_f32_16x16x32_bf16 v[132:135], a[128:131], v[28:31], 0\n"
"v_mfma_f32_16x16x32_bf16 v[148:151], a[132:135], v[28:31], 0\n"
"v_mfma_f32_16x16x32_bf16 v[152:155], a[136:139], v[28:31], 0\n"
"buffer_load_dwordx4 a[104:107], v0, s[8:11], s98 offen offset:2048 sc1\n"
"v_mfma_f32_16x16x32_bf16 v[160:163], a[140:143], v[28:31], 0\n"
"s_waitcnt vmcnt(34)\n"
"v_mfma_f32_16x16x32_bf16 v[132:135], a[144:147], v[40:43], v[132:135]\n"
"v_mfma_f32_16x16x32_bf16 v[148:151], a[148:151], v[40:43], v[148:151]\n"
"buffer_load_dwordx4 a[108:111], v0, s[8:11], s98 offen offset:3072 sc1\n"
"v_mfma_f32_16x16x32_bf16 v[152:155], a[152:155], v[40:43], v[152:155]\n"
"v_mfma_f32_16x16x32_bf16 v[160:163], a[156:159], v[40:43], v[160:163]\n"
"s_waitcnt vmcnt(34)\n"
"v_mfma_f32_16x16x32_bf16 v[132:135], a[160:163], v[60:63], v[132:135]\n"
"buffer_load_dwordx4 a[112:115], v1, s[8:11], s98 offen sc1\n"
"v_mfma_f32_16x16x32_bf16 v[148:151], a[164:167], v[60:63], v[148:151]\n"
"v_mfma_f32_16x16x32_bf16 v[152:155], a[168:171], v[60:63], v[152:155]\n"
"v_mfma_f32_16x16x32_bf16 v[160:163], a[172:175], v[60:63], v[160:163]\n"
"s_waitcnt vmcnt(34)\n"
"buffer_load_dwordx4 a[116:119], v1, s[8:11], s98 offen offset:1024 sc1\n"
"v_mfma_f32_16x16x32_bf16 v[132:135], a[176:179], v[124:127], v[132:135]\n"
"v_mfma_f32_16x16x32_bf16 v[148:151], a[180:183], v[124:127], v[148:151]\n"
"v_mfma_f32_16x16x32_bf16 v[152:155], a[184:187], v[124:127], v[152:155]\n"
"buffer_load_dwordx4 a[120:123], v1, s[8:11], s98 offen offset:2048 sc1\n"
"v_mfma_f32_16x16x32_bf16 v[160:163], a[188:191], v[124:127], v[160:163]\n"
"s_waitcnt vmcnt(35)\n"
"v_mfma_f32_16x16x32_bf16 v[132:135], a[192:195], v[72:75], v[132:135]\n"
"v_mfma_f32_16x16x32_bf16 v[148:151], a[196:199], v[72:75], v[148:151]\n"
"buffer_load_dwordx4 a[124:127], v1, s[8:11], s98 offen offset:3072 sc1\n"
"v_mfma_f32_16x16x32_bf16 v[152:155], a[200:203], v[72:75], v[152:155]\n"
"s_add_u32 s98, s69, 0x8000\n"
"v_mfma_f32_16x16x32_bf16 v[160:163], a[204:207], v[72:75], v[160:163]\n"
"s_waitcnt vmcnt(35)\n"
"v_mfma_f32_16x16x32_bf16 v[132:135], a[208:211], v[156:159], v[132:135]\n"
"buffer_load_dwordx4 v[28:31], v0, s[8:11], s98 offen sc1\n"
"v_mfma_f32_16x16x32_bf16 v[148:151], a[212:215], v[156:159], v[148:151]\n"
"v_mfma_f32_16x16x32_bf16 v[152:155], a[216:219], v[156:159], v[152:155]\n"
"v_mfma_f32_16x16x32_bf16 v[160:163], a[220:223], v[156:159], v[160:163]\n"
"s_waitcnt vmcnt(35)\n"
"buffer_load_dwordx4 v[40:43], v0, s[8:11], s98 offen offset:1024 sc1\n"
"v_mfma_f32_16x16x32_bf16 v[132:135], a[224:227], v[80:83], v[132:135]\n"
"v_mfma_f32_16x16x32_bf16 v[148:151], a[228:231], v[80:83], v[148:151]\n"
"v_mfma_f32_16x16x32_bf16 v[152:155], a[232:235], v[80:83], v[152:155]\n"
"buffer_load_dwordx4 v[60:63], v0, s[8:11], s98 offen offset:2048 sc1\n"
"v_mfma_f32_16x16x32_bf16 v[160:163], a[236:239], v[80:83], v[160:163]\n"
"s_waitcnt vmcnt(36)\n"
"v_mfma_f32_16x16x32_bf16 v[132:135], a[240:243], v[56:59], v[132:135]\n"
"v_mfma_f32_16x16x32_bf16 v[148:151], a[244:247], v[56:59], v[148:151]\n"
"buffer_load_dwordx4 v[72:75], v0, s[8:11], s98 offen offset:3072 sc1\n"
"v_mfma_f32_16x16x32_bf16 v[152:155], a[248:251], v[56:59], v[152:155]\n"
"v_mfma_f32_16x16x32_bf16 v[160:163], a[252:255], v[56:59], v[160:163]\n"
"s_waitcnt vmcnt(35)\n"
"; @B G2 a.1\n"
"v_mfma_f32_16x16x32_bf16 v[56:59], a[128:131], v[168:171], 0\n"
"s_nop 1\n"
"v_cmp_u_f32_e32 vcc, v132, v132\n"
"v_mfma_f32_16x16x32_bf16 v[80:83], a[132:135], v[168:171], 0\n"
"s_cbranch_vccnz .Lmk_p80s\n"
".Lmk_p80r:\n"
"v_mfma_f32_16x16x32_bf16 v[124:127], a[136:139], v[168:171], 0\n"
"buffer_load_dwordx4 v[156:159], v1, s[8:11], s98 offen sc1\n"
"v_mfma_f32_16x16x32_bf16 v[164:167], a[140:143], v[168:171], 0\n"
"v_mfma_f32_16x16x32_bf16 v[56:59], a[144:147], v[64:67], v[56:59]\n"
"ds_write_b128 v3, v[132:135]\n"
"v_mfma_f32_16x16x32_bf16 v[80:83], a[148:151], v[64:67], v[80:83]\n"
"buffer_load_dwordx4 v[132:135], v1, s[8:11], s98 offen offset:1024 sc1\n"
"v_mfma_f32_16x16x32_bf16 v[124:127], a[152:155], v[64:67], v[124:127]\n"
"ds_write_b128 v3, v[148:151] offset:1024\n"
"v_mfma_f32_16x16x32_bf16 v[164:167], a[156:159], v[64:67], v[164:167]\n"
"s_waitcnt vmcnt(35)\n"
"v_mfma_f32_16x16x32_bf16 v[56:59], a[160:163], v[68:71], v[56:59]\n"
"ds_write_b128 v3, v[152:155] offset:2048\n"
"v_mfma_f32_16x16x32_bf16 v[80:83], a[164:167], v[68:71], v[80:83]\n"
"buffer_load_dwordx4 v[64:67], v1, s[8:11], s98 offen offset:2048 sc1\n"
"v_mfma_f32_16x16x32_bf16 v[124:127], a[168:171], v[68:71], v[124:127]\n"
"ds_write_b128 v3, v[160:163] offset:3072\n"
"v_mfma_f32_16x16x32_bf16 v[164:167], a[172:175], v[68:71], v[164:167]\n"
"v_mfma_f32_16x16x32_bf16 v[56:59], a[176:179], v[88:91], v[56:59]\n"
"buffer_load_dwordx4 v[68:71], v1, s[8:11], s98 offen offset:3072 sc1\n"
"v_mfma_f32_16x16x32_bf16 v[80:83], a[180:183], v[88:91], v[80:83]\n"
"s_add_u32 s98, s69, 0xa000\n"
"ds_read_b128 v[148:151], v2\n"
"ds_read_b128 v[152:155], v2 offset:1024\n"
"v_mfma_f32_16x16x32_bf16 v[124:127], a[184:187], v[88:91], v[124:127]\n"
"v_mfma_f32_16x16x32_bf16 v[164:167], a[188:191], v[88:91], v[164:167]\n"
"s_waitcnt vmcnt(35)\n"
"buffer_load_dwordx4 v[88:91], v0, s[8:11], s98 offen sc1\n"
"v_mfma_f32_16x16x32_bf16 v[56:59], a[192:195], v[92:95], v[56:59]\n"
"v_mfma_f32_16x16x32_bf16 v[80:83], a[196:199], v[92:95], v[80:83]\n"
"v_mfma_f32_16x16x32_bf16 v[124:127], a[200:203], v[92:95], v[124:127]\n"
"buffer_load_dwordx4 v[160:163], v0, s[8:11], s98 offen offset:1024 sc1\n"
"v_mfma_f32_16x16x32_bf16 v[164:167], a[204:207], v[92:95], v[164:167]\n"
"s_waitcnt lgkmcnt(0)\n"
"s_barrier\n"
"v_mfma_f32_16x16x32_bf16 v[56:59], a[208:211], v[96:99], v[56:59]\n"
"v_lshlrev_b32_e32 v255, 16, v148\n"
"v_and_b32_e32 v254, 0xffff0000, v148\n"
"v_mfma_f32_16x16x32_bf16 v[80:83], a[212:215], v[96:99], v[80:83]\n"
"buffer_load_dwordx4 v[92:95], v0, s[8:11], s98 offen offset:2048 sc1\n"
"v_mfma_f32_16x16x32_bf16 v[124:127], a[216:219], v[96:99], v[124:127]\n"
"ds_read_b128 v[168:171], v4\n"
"ds_read_b128 v[172:175], v4 offset:1024\n"
"v_mfma_f32_16x16x32_bf16 v[164:167], a[220:223], v[96:99], v[164:167]\n"
"s_waitcnt vmcnt(36)\n"
"ds_read_b128 v[96:99], v4 offset:2048\n"
"ds_read_b128 v[176:179], v4 offset:3072\n"
"v_mfma_f32_16x16x32_bf16 v[56:59], a[224:227], v[100:103], v[56:59]\n"
"buffer_load_dwordx4 v[180:183], v0, s[8:11], s98 offen offset:3072 sc1\n"
"v_mfma_f32_16x16x32_bf16 v[80:83], a[228:231], v[100:103], v[80:83]\n"
"v_lshlrev_b32_e32 v253, 16, v149\n"
"v_and_b32_e32 v252, 0xffff0000, v149\n"
"v_mfma_f32_16x16x32_bf16 v[124:127], a[232:235], v[100:103], v[124:127]\n"
"v_lshlrev_b32_e32 v251, 16, v150\n"
"v_and_b32_e32 v250, 0xffff0000, v150\n"
"v_mfma_f32_16x16x32_bf16 v[164:167], a[236:239], v[100:103], v[164:167]\n"
"buffer_load_dwordx4 v[100:103], v1, s[8:11], s98 offen sc1\n"
"v_mfma_f32_16x16x32_bf16 v[56:59], a[240:243], v[104:107], v[56:59]\n"
"v_lshlrev_b32_e32 v249, 16, v151\n"
"v_and_b32_e32 v248, 0xffff0000, v151\n"
"v_mfma_f32_16x16x32_bf16 v[80:83], a[244:247], v[104:107], v[80:83]\n"
"v_lshlrev_b32_e32 v247, 16, v152\n"
"v_and_b32_e32 v246, 0xffff0000, v152\n"
"v_mfma_f32_16x16x32_bf16 v[124:127], a[248:251], v[104:107], v[124:127]\n"
"buffer_load_dwordx4 v[148:151], v1, s[8:11], s98 offen offset:1024 sc1\n"
"v_mfma_f32_16x16x32_bf16 v[164:167], a[252:255], v[104:107], v[164:167]\n"
"s_waitcnt vmcnt(37)\n"
"v_lshlrev_b32_e32 v245, 16, v153\n"
"v_and_b32_e32 v244, 0xffff0000, v153\n"
"; @B G2 a.2\n"
"v_mfma_f32_16x16x32_bf16 v[104:107], a[128:131], v[108:111], 0\n"
"v_cmp_u_f32_e32 vcc, v56, v56\n"
"v_lshlrev_b32_e32 v243, 16, v154\n"
"v_mfma_f32_16x16x32_bf16 v[184:187], a[132:135], v[108:111], 0\n"
"s_cbranch_vccnz .Lmk_p81s\n"
".Lmk_p81r:\n"
"v_and_b32_e32 v242, 0xffff0000, v154\n"
"v_mfma_f32_16x16x32_bf16 v[188:191], a[136:139], v[108:111], 0\n"
"buffer_load_dwordx4 v[192:195], v1, s[8:11], s98 offen offset:2048 sc1\n"
"v_mfma_f32_16x16x32_bf16 v[196:199], a[140:143], v[108:111], 0\n"
"s_waitcnt lgkmcnt(0)\n"
"v_lshlrev_b32_e32 v241, 16, v155\n"
"v_and_b32_e32 v240, 0xffff0000, v155\n"
"v_mfma_f32_16x16x32_bf16 v[104:107], a[144:147], v[84:87], v[104:107]\n"
"ds_write_b128 v3, v[56:59] offset:6144\n"
"v_mfma_f32_16x16x32_bf16 v[184:187], a[148:151], v[84:87], v[184:187]\n"
"buffer_load_dwordx4 v[56:59], v1, s[8:11], s98 offen offset:3072 sc1\n"
"v_mfma_f32_16x16x32_bf16 v[188:191], a[152:155], v[84:87], v[188:191]\n"
"ds_write_b128 v3, v[80:83] offset:7168\n"
"v_mfma_f32_16x16x32_bf16 v[196:199], a[156:159], v[84:87], v[196:199]\n"
"s_waitcnt vmcnt(37)\n"
"v_add_f32_e32 v168, v168, v255\n"
"v_add_f32_e32 v169, v169, v254\n"
"v_mfma_f32_16x16x32_bf16 v[104:107], a[160:163], v[24:27], v[104:107]\n"
"ds_write_b128 v3, v[124:127] offset:8192\n"
"v_mfma_f32_16x16x32_bf16 v[184:187], a[164:167], v[24:27], v[184:187]\n"
"v_add_f32_e32 v170, v170, v253\n"
"v_add_f32_e32 v171, v171, v252\n"
"v_mfma_f32_16x16x32_bf16 v[188:191], a[168:171], v[24:27], v[188:191]\n"
"ds_write_b128 v3, v[164:167] offset:9216\n"
"v_mfma_f32_16x16x32_bf16 v[196:199], a[172:175], v[24:27], v[196:199]\n"
"v_add_f32_e32 v172, v172, v251\n"
"v_add_f32_e32 v173, v173, v250\n"
"v_mfma_f32_16x16x32_bf16 v[104:107], a[176:179], v[32:35], v[104:107]\n"
"v_add_f32_e32 v174, v174, v249\n"
"v_add_f32_e32 v175, v175, v248\n"
"v_mfma_f32_16x16x32_bf16 v[184:187], a[180:183], v[32:35], v[184:187]\n"
"v_add_f32_e32 v96, v96, v247\n"
"v_add_f32_e32 v97, v97, v246\n"
"v_mfma_f32_16x16x32_bf16 v[188:191], a[184:187], v[32:35], v[188:191]\n"
"v_add_f32_e32 v98, v98, v245\n"
"v_add_f32_e32 v99, v99, v244\n"
"v_mfma_f32_16x16x32_bf16 v[196:199], a[188:191], v[32:35], v[196:199]\n"
"s_waitcnt vmcnt(35)\n"
"v_add_f32_e32 v176, v176, v243\n"
"v_add_f32_e32 v177, v177, v242\n"
"v_mfma_f32_16x16x32_bf16 v[104:107], a[192:195], v[116:119], v[104:107]\n"
"v_add_f32_e32 v178, v178, v241\n"
"v_add_f32_e32 v179, v179, v240\n"
"v_mfma_f32_16x16x32_bf16 v[184:187], a[196:199], v[116:119], v[184:187]\n"
"v_mfma_f32_16x16x32_bf16 v[188:191], a[200:203], v[116:119], v[188:191]\n"
"v_mfma_f32_16x16x32_bf16 v[196:199], a[204:207], v[116:119], v[196:199]\n"
"v_mfma_f32_16x16x32_bf16 v[104:107], a[208:211], v[144:147], v[104:107]\n"
"s_add_u32 s98, s52, 0x840000\n"
"buffer_load_dwordx4 v[24:27], v0, s[8:11], s98 offen sc1\n"
"v_mfma_f32_16x16x32_bf16 v[184:187], a[212:215], v[144:147], v[184:187]\n"
"v_mfma_f32_16x16x32_bf16 v[188:191], a[216:219], v[144:147], v[188:191]\n"
"buffer_load_dwordx4 v[32:35], v0, s[8:11], s98 offen offset:1024 sc1\n"
"v_mfma_f32_16x16x32_bf16 v[196:199], a[220:223], v[144:147], v[196:199]\n"
"s_waitcnt vmcnt(35)\n"
"v_mfma_f32_16x16x32_bf16 v[104:107], a[224:227], v[48:51], v[104:107]\n"
"s_waitcnt vmcnt(33)\n"
"v_max_u32_e32 v255, v52, v53\n"
"v_max3_u32 v255, v255, v54, v55\n"
"v_mfma_f32_16x16x32_bf16 v[184:187], a[228:231], v[48:51], v[184:187]\n"
"s_waitcnt vmcnt(30)\n"
"v_max3_u32 v255, v255, v128, v129\n"
"v_max3_u32 v255, v255, v130, v131\n"
"v_mfma_f32_16x16x32_bf16 v[188:191], a[232:235], v[48:51], v[188:191]\n"
"v_max3_u32 v255, v255, v36, v37\n"
"v_max3_u32 v255, v255, v38, v39\n"
"v_mfma_f32_16x16x32_bf16 v[196:199], a[236:239], v[48:51], v[196:199]\n"
"v_max3_u32 v255, v255, v120, v121\n"
"v_max3_u32 v255, v255, v122, v123\n"
"v_mfma_f32_16x16x32_bf16 v[104:107], a[240:243], v[112:115], v[104:107]\n"
"v_cmp_eq_u32_e32 vcc, -1, v255\n"
"v_mfma_f32_16x16x32_bf16 v[184:187], a[244:247], v[112:115], v[184:187]\n"
"s_cbranch_vccnz .Lmk_q83s\n"
".Lmk_q83r:\n"
"v_mfma_f32_16x16x32_bf16 v[188:191], a[248:251], v[112:115], v[188:191]\n"
"buffer_load_dwordx4 v[48:51], v0, s[8:11], s98 offen offset:2048 sc1\n"
"v_mfma_f32_16x16x32_bf16 v[196:199], a[252:255], v[112:115], v[196:199]\n"
"; @B G2 a.3\n"
"v_mfma_f32_16x16x32_bf16 v[168:171], a[128:131], v[52:55], v[168:171]\n"
"s_nop 0\n"
"v_cmp_u_f32_e32 vcc, v104, v104\n"
"v_mfma_f32_16x16x32_bf16 v[172:175], a[132:135], v[52:55], v[172:175]\n"
"s_cbranch_vccnz .Lmk_p82s\n"
".Lmk_p82r:\n"
"v_mfma_f32_16x16x32_bf16 v[96:99], a[136:139], v[52:55], v[96:99]\n"
"buffer_load_dwordx4 v[80:83], v0, s[8:11], s98 offen offset:3072 sc1\n"
"v_mfma_f32_16x16x32_bf16 v[176:179], a[140:143], v[52:55], v[176:179]\n"
"v_mfma_f32_16x16x32_bf16 v[168:171], a[144:147], v[128:131], v[168:171]\n"
"ds_write_b128 v3, v[104:107] offset:12288\n"
"v_mfma_f32_16x16x32_bf16 v[172:175], a[148:151], v[128:131], v[172:175]\n"
"buffer_load_dwordx4 v[52:55], v1, s[8:11], s98 offen sc1\n"
"v_mfma_f32_16x16x32_bf16 v[96:99], a[152:155], v[128:131], v[96:99]\n"
"ds_write_b128 v3, v[184:187] offset:13312\n"
"v_mfma_f32_16x16x32_bf16 v[176:179], a[156:159], v[128:131], v[176:179]\n"
"buffer_load_dwordx4 v[84:87], v1, s[8:11], s98 offen offset:1024 sc1\n"
"v_mfma_f32_16x16x32_bf16 v[168:171], a[160:163], v[36:39], v[168:171]\n"
"s_waitcnt vmcnt(31)\n"
"v_max_u32_e32 v255, v140, v141\n"
"v_max3_u32 v255, v255, v142, v143\n"
"v_mfma_f32_16x16x32_bf16 v[172:175], a[164:167], v[36:39], v[172:175]\n"
"s_waitcnt vmcnt(29)\n"
"v_max3_u32 v255, v255, v44, v45\n"
"v_max3_u32 v255, v255, v46, v47\n"
"v_mfma_f32_16x16x32_bf16 v[96:99], a[168:171], v[36:39], v[96:99]\n"
"v_max3_u32 v255, v255, v136, v137\n"
"v_max3_u32 v255, v255, v138, v139\n"
"v_mfma_f32_16x16x32_bf16 v[176:179], a[172:175], v[36:39], v[176:179]\n"
"v_max3_u32 v255, v255, v76, v77\n"
"v_max3_u32 v255, v255, v78, v79\n"
"v_mfma_f32_16x16x32_bf16 v[168:171], a[176:179], v[120:123], v[168:171]\n"
"v_cmp_eq_u32_e32 vcc, -1, v255\n"
"v_mfma_f32_16x16x32_bf16 v[172:175], a[180:183], v[120:123], v[172:175]\n"
"s_cbranch_vccnz .Lmk_q84s\n"
".Lmk_q84r:\n"
"v_mfma_f32_16x16x32_bf16 v[96:99], a[184:187], v[120:123], v[96:99]\n"
"ds_write_b128 v3, v[188:191] offset:14336\n"
"v_mfma_f32_16x16x32_bf16 v[176:179], a[188:191], v[120:123], v[176:179]\n"
"buffer_load_dwordx4 v[36:39], v1, s[8:11], s98 offen offset:2048 sc1\n"
"v_mfma_f32_16x16x32_bf16 v[168:171], a[192:195], v[140:143], v[168:171]\n"
"ds_write_b128 v3, v[196:199] offset:15360\n"
"v_mfma_f32_16x16x32_bf16 v[172:175], a[196:199], v[140:143], v[172:175]\n"
"buffer_load_dwordx4 v[104:107], v1, s[8:11], s98 offen offset:3072 sc1\n"
"v_mfma_f32_16x16x32_bf16 v[96:99], a[200:203], v[140:143], v[96:99]\n"
"s_add_u32 s98, s53, 0x840000\n"
"v_mfma_f32_16x16x32_bf16 v[176:179], a[204:207], v[140:143], v[176:179]\n"
"s_waitcnt lgkmcnt(0)\n"
"s_barrier\n"
"v_mfma_f32_16x16x32_bf16 v[168:171], a[208:211], v[44:47], v[168:171]\n"
"buffer_load_dwordx4 v[108:111], v0, s[8:11], s98 offen sc1\n"
"v_mfma_f32_16x16x32_bf16 v[172:175], a[212:215], v[44:47], v[172:175]\n"
"ds_read_b128 v[112:115], v5\n"
"ds_read_b128 v[116:119], v6\n"
"v_mfma_f32_16x16x32_bf16 v[96:99], a[216:219], v[44:47], v[96:99]\n"
"buffer_load_dwordx4 v[120:123], v0, s[8:11], s98 offen offset:1024 sc1\n"
"v_mfma_f32_16x16x32_bf16 v[176:179], a[220:223], v[44:47], v[176:179]\n"
"ds_read_b128 v[44:47], v5 offset:1024\n"
"ds_read_b128 v[124:127], v6 offset:1024\n"
"v_mfma_f32_16x16x32_bf16 v[168:171], a[224:227], v[136:139], v[168:171]\n"
"buffer_load_dwordx4 v[128:131], v0, s[8:11], s98 offen offset:2048 sc1\n"
"v_mfma_f32_16x16x32_bf16 v[172:175], a[228:231], v[136:139], v[172:175]\n"
"ds_read_b128 v[140:143], v5 offset:2048\n"
"ds_read_b128 v[144:147], v6 offset:2048\n"
"v_mfma_f32_16x16x32_bf16 v[96:99], a[232:235], v[136:139], v[96:99]\n"
"buffer_load_dwordx4 v[152:155], v0, s[8:11], s98 offen offset:3072 sc1\n"
"v_mfma_f32_16x16x32_bf16 v[176:179], a[236:239], v[136:139], v[176:179]\n"
"ds_read_b128 v[136:139], v5 offset:3072\n"
"ds_read_b128 v[164:167], v6 offset:3072\n"
"v_mfma_f32_16x16x32_bf16 v[168:171], a[240:243], v[76:79], v[168:171]\n"
"buffer_load_dwordx4 v[184:187], v1, s[8:11], s98 offen sc1\n"
"v_mfma_f32_16x16x32_bf16 v[172:175], a[244:247], v[76:79], v[172:175]\n"
"s_waitcnt lgkmcnt(4)\n"
"v_add_f32_e32 v112, v112, v116\n"
"v_add_f32_e32 v113, v113, v117\n"
"v_mfma_f32_16x16x32_bf16 v[96:99], a[248:251], v[76:79], v[96:99]\n"
"buffer_load_dwordx4 v[188:191], v1, s[8:11], s98 offen offset:1024 sc1\n"
"v_mfma_f32_16x16x32_bf16 v[176:179], a[252:255], v[76:79], v[176:179]\n"
"buffer_load_dwordx4 v[76:79], v1, s[8:11], s98 offen offset:2048 sc1\n"
"s_waitcnt lgkmcnt(0)\n"
"v_add_f32_e32 v114, v114, v118\n"
"v_add_f32_e32 v115, v115, v119\n"
"v_add_f32_e32 v44, v44, v124\n"
"v_add_f32_e32 v45, v45, v125\n"
"v_add_f32_e32 v46, v46, v126\n"
"v_add_f32_e32 v47, v47, v127\n"
"buffer_load_dwordx4 v[116:119], v1, s[8:11], s98 offen offset:3072 sc1\n"
"v_add_f32_e32 v140, v140, v144\n"
"v_add_f32_e32 v141, v141, v145\n"
"v_add_f32_e32 v142, v142, v146\n"
"v_add_f32_e32 v143, v143, v147\n"
"v_add_f32_e32 v136, v136, v164\n"
"v_add_f32_e32 v137, v137, v165\n"
"v_add_f32_e32 v138, v138, v166\n"
"v_add_f32_e32 v139, v139, v167\n"
"s_lshr_b32 s98, s79, 7\n"
"s_add_u32 s52, s52, 0x800000\n"
"s_add_u32 s53, s53, 0x800000\n"
"s_add_u32 s54, s54, 0x800000\n"
"s_add_u32 s55, s55, 0x800000\n"
"s_add_u32 s56, s56, 0x800000\n"
"s_add_u32 s57, s57, 0x800000\n"
"s_add_u32 s58, s58, 0x800000\n"
"s_add_u32 s59, s59, 0x800000\n"
"s_add_u32 s60, s60, 0x800000\n"
"s_add_u32 s61, s61, 0x800000\n"
"s_add_u32 s62, s62, 0x80000\n"
"s_add_u32 s63, s63, 0x80000\n"
"s_add_u32 s64, s64, 0x1000000\n"
"s_add_u32 s65, s65, s79\n"
"s_add_u32 s66, s66, s79\n"
"s_add_u32 s67, s67, 0x20000\n"
"s_add_u32 s68, s68, 0x20000\n"
"s_add_u32 s71, s71, s98\n"
"s_add_u32 s73, s73, s98\n"
"s_mov_b32 s0, 1\n"
"s_mov_b32 s10, 0\n"
".Lmk_a0:\n"
"s_waitcnt vmcnt(15)\n"
"; @A G1 0.0\n"
"v_mfma_f32_16x16x32_bf16 v[124:127], a[0:3], v[24:27], 0\n"
"v_mfma_f32_16x16x32_bf16 v[144:147], a[4:7], v[24:27], 0\n"
"s_add_u32 s98, s62, 0x4000\n"
"v_mfma_f32_16x16x32_bf16 v[164:167], a[8:11], v[24:27], 0\n"
"v_mfma_f32_16x16x32_bf16 v[196:199], a[12:15], v[24:27], 0\n"
"v_mfma_f32_16x16x32_bf16 v[200:203], a[16:19], v[24:27], 0\n"
"s_add_u32 s97, s54, 0x40000\n"
"buffer_load_dwordx4 v[204:207], v0, s[4:7], s97 offen sc1\n"
"v_mfma_f32_16x16x32_bf16 v[208:211], a[20:23], v[24:27], 0\n"
"s_waitcnt vmcnt(15)\n"
"buffer_load_dword v255, v10, s[4:7], s98 offen sc1\n"
"v_mfma_f32_16x16x32_bf16 v[124:127], a[24:27], v[32:35], v[124:127]\n"
"v_mov_b32_e32 v24, 0x3f803f80\n"
"v_mov_b32_e32 v25, 0x3f803f80\n"
"v_mfma_f32_16x16x32_bf16 v[144:147], a[28:31], v[32:35], v[144:147]\n"
"v_mov_b32_e32 v26, 0x3f803f80\n"
"v_mov_b32_e32 v27, 0x3f803f80\n"
"v_mfma_f32_16x16x32_bf16 v[164:167], a[32:35], v[32:35], v[164:167]\n"
"buffer_load_dword v254, v10, s[4:7], s98 offen offset:256 sc1\n"
"v_mfma_f32_16x16x32_bf16 v[196:199], a[36:39], v[32:35], v[196:199]\n"
"v_add_f32_e32 v112, v168, v112\n"
"v_add_f32_e32 v113, v169, v113\n"
"v_mfma_f32_16x16x32_bf16 v[200:203], a[40:43], v[32:35], v[200:203]\n"
"buffer_load_dwordx4 v[212:215], v0, s[4:7], s97 offen offset:1024 sc1\n"
"v_mfma_f32_16x16x32_bf16 v[208:211], a[44:47], v[32:35], v[208:211]\n"
"s_waitcnt vmcnt(17)\n"
"buffer_load_dword v253, v10, s[4:7], s98 offen offset:512 sc1\n"
"v_mfma_f32_16x16x32_bf16 v[124:127], a[48:51], v[48:51], v[124:127]\n"
"v_add_f32_e32 v114, v170, v114\n"
"v_add_f32_e32 v115, v171, v115\n"
"v_mfma_f32_16x16x32_bf16 v[144:147], a[52:55], v[48:51], v[144:147]\n"
"v_add_f32_e32 v44, v172, v44\n"
"v_add_f32_e32 v45, v173, v45\n"
"v_mfma_f32_16x16x32_bf16 v[164:167], a[56:59], v[48:51], v[164:167]\n"
"buffer_load_dword v252, v10, s[4:7], s98 offen offset:768 sc1\n"
"v_mfma_f32_16x16x32_bf16 v[196:199], a[60:63], v[48:51], v[196:199]\n"
"v_add_f32_e32 v46, v174, v46\n"
"v_add_f32_e32 v47, v175, v47\n"
"v_mfma_f32_16x16x32_bf16 v[200:203], a[64:67], v[48:51], v[200:203]\n"
"buffer_load_dwordx4 v[32:35], v0, s[4:7], s97 offen offset:2048 sc1\n"
"v_mfma_f32_16x16x32_bf16 v[208:211], a[68:71], v[48:51], v[208:211]\n"
"s_waitcnt vmcnt(19)\n"
"buffer_load_dword v251, v10, s[4:7], s98 offen offset:1024 sc1\n"
"v_mfma_f32_16x16x32_bf16 v[124:127], a[72:75], v[80:83], v[124:127]\n"
"v_cvt_pk_bf16_f32 v48, v112, v113\n"
"v_cvt_pk_bf16_f32 v49, v114, v115\n"
"v_mfma_f32_16x16x32_bf16 v[144:147], a[76:79], v[80:83], v[144:147]\n"
"v_cvt_pk_bf16_f32 v50, v44, v45\n"
"v_cvt_pk_bf16_f32 v51, v46, v47\n"
"v_mfma_f32_16x16x32_bf16 v[164:167], a[80:83], v[80:83], v[164:167]\n"
"buffer_load_dword v250, v10, s[4:7], s98 offen offset:1280 sc1\n"
"v_mfma_f32_16x16x32_bf16 v[196:199], a[84:87], v[80:83], v[196:199]\n"
"buffer_store_dwordx4 v[48:51], v0, s[4:7], s61 offen sc1\n"
"v_mfma_f32_16x16x32_bf16 v[200:203], a[88:91], v[80:83], v[200:203]\n"
"buffer_load_dwordx4 v[44:47], v0, s[4:7], s97 offen offset:3072 sc1\n"
"v_mfma_f32_16x16x32_bf16 v[208:211], a[92:95], v[80:83], v[208:211]\n"
"s_waitcnt vmcnt(22)\n"
"buffer_load_dword v249, v10, s[4:7], s98 offen offset:1536 sc1\n"
"v_mfma_f32_16x16x32_bf16 v[124:127], a[96:99], v[52:55], v[124:127]\n"
"ds_write_b128 v2, v[48:51]\n"
"v_mfma_f32_16x16x32_bf16 v[144:147], a[100:103], v[52:55], v[144:147]\n"
"v_add_f32_e32 v140, v96, v140\n"
"v_add_f32_e32 v141, v97, v141\n"
"v_mfma_f32_16x16x32_bf16 v[164:167], a[104:107], v[52:55], v[164:167]\n"
"buffer_load_dword v248, v10, s[4:7], s98 offen offset:1792 sc1\n"
"v_mfma_f32_16x16x32_bf16 v[196:199], a[108:111], v[52:55], v[196:199]\n"
"v_add_f32_e32 v142, v98, v142\n"
"v_add_f32_e32 v143, v99, v143\n"
"v_mfma_f32_16x16x32_bf16 v[200:203], a[112:115], v[52:55], v[200:203]\n"
"buffer_load_dwordx4 v[80:83], v1, s[4:7], s97 offen sc1\n"
"v_mfma_f32_16x16x32_bf16 v[208:211], a[116:119], v[52:55], v[208:211]\n"
"s_waitcnt vmcnt(24)\n"
"v_add_f32_e32 v136, v176, v136\n"
"v_add_f32_e32 v137, v177, v137\n"
"v_mfma_f32_16x16x32_bf16 v[124:127], a[120:123], v[84:87], v[124:127]\n"
"v_mov_b32_e32 v52, -1\n"
"v_mov_b32_e32 v53, -1\n"
"v_mfma_f32_16x16x32_bf16 v[144:147], a[124:127], v[84:87], v[144:147]\n"
"v_mov_b32_e32 v54, -1\n"
"v_mov_b32_e32 v55, -1\n"
"v_mfma_f32_16x16x32_bf16 v[164:167], v[28:31], v[84:87], v[164:167]\n"
"s_add_u32 s98, s64, 0x4000\n"
"buffer_store_dwordx4 v[52:55], v2, s[4:7], s98 offen sc1\n"
"v_mfma_f32_16x16x32_bf16 v[196:199], v[40:43], v[84:87], v[196:199]\n"
"v_add_f32_e32 v138, v178, v138\n"
"v_add_f32_e32 v139, v179, v139\n"
"v_mfma_f32_16x16x32_bf16 v[200:203], v[60:63], v[84:87], v[200:203]\n"
"buffer_load_dwordx4 v[96:99], v1, s[4:7], s97 offen offset:1024 sc1\n"
"v_mfma_f32_16x16x32_bf16 v[208:211], v[72:75], v[84:87], v[208:211]\n"
"s_waitcnt vmcnt(25)\n"
"v_cvt_pk_bf16_f32 v84, v140, v141\n"
"v_cvt_pk_bf16_f32 v85, v142, v143\n"
"v_mfma_f32_16x16x32_bf16 v[124:127], v[156:159], v[36:39], v[124:127]\n"
"v_cvt_pk_bf16_f32 v86, v136, v137\n"
"v_cvt_pk_bf16_f32 v87, v138, v139\n"
"v_mfma_f32_16x16x32_bf16 v[144:147], v[132:135], v[36:39], v[144:147]\n"
"buffer_store_dwordx4 v[84:87], v0, s[4:7], s61 offen offset:1024 sc1\n"
"v_mfma_f32_16x16x32_bf16 v[164:167], v[64:67], v[36:39], v[164:167]\n"
"ds_write_b128 v2, v[84:87] offset:1024\n"
"v_mfma_f32_16x16x32_bf16 v[112:115], v[24:27], v[48:51], 0\n"
"v_mfma_f32_16x16x32_bf16 v[136:139], v[48:51], v[48:51], 0\n"
"v_mfma_f32_16x16x32_bf16 v[112:115], v[24:27], v[84:87], v[112:115]\n"
"v_mfma_f32_16x16x32_bf16 v[136:139], v[84:87], v[84:87], v[136:139]\n"
"v_mfma_f32_16x16x32_bf16 v[196:199], v[68:71], v[36:39], v[196:199]\n"
"s_mov_b32 s94, 0xaaaaaaaa\n"
"s_mov_b32 s95, 0xaaaaaaaa\n"
"v_mfma_f32_16x16x32_bf16 v[200:203], v[88:91], v[36:39], v[200:203]\n"
"buffer_load_dwordx4 v[24:27], v1, s[4:7], s97 offen offset:2048 sc1\n"
"v_mfma_f32_16x16x32_bf16 v[208:211], v[160:163], v[36:39], v[208:211]\n"
"s_waitcnt vmcnt(26)\n"
"v_mfma_f32_16x16x32_bf16 v[124:127], v[92:95], v[104:107], v[124:127]\n"
"v_cndmask_b32_e64 v136, v136, v137, s[94:95]\n"
"v_cndmask_b32_e64 v138, v138, v139, s[94:95]\n"
"v_mfma_f32_16x16x32_bf16 v[144:147], v[180:183], v[104:107], v[144:147]\n"
"s_mov_b32 s94, 0xcccccccc\n"
"s_mov_b32 s95, 0xcccccccc\n"
"v_mfma_f32_16x16x32_bf16 v[164:167], v[100:103], v[104:107], v[164:167]\n"
"v_cndmask_b32_e64 v113, v136, v138, s[94:95]\n"
"v_mfma_f32_16x16x32_bf16 v[196:199], v[148:151], v[104:107], v[196:199]\n"
"s_mov_b32 s94, 0xf0000f\n"
"s_mov_b32 s95, 0xf0000f00\n"
"v_mfma_f32_16x16x32_bf16 v[200:203], v[192:195], v[104:107], v[200:203]\n"
"buffer_load_dwordx4 v[36:39], v1, s[4:7], s97 offen offset:3072 sc1\n"
"v_mfma_f32_16x16x32_bf16 v[208:211], v[56:59], v[104:107], v[208:211]\n"
"s_waitcnt vmcnt(25)\n"
"v_cmp_u_f32_e32 vcc, v124, v124\n"
"v_cndmask_b32_e64 v112, v112, v113, s[94:95]\n"
"; @A G1 0.1\n"
"v_mfma_f32_16x16x32_bf16 v[48:51], a[0:3], v[108:111], 0\n"
"s_cbranch_vccnz .Lmk_p89s\n"
".Lmk_p89r:\n"
"v_mfma_f32_16x16x32_bf16 v[84:87], a[4:7], v[108:111], 0\n"
"buffer_store_dword v112, v14, s[4:7], s63 offen sc1\n"
"v_mfma_f32_16x16x32_bf16 v[104:107], a[8:11], v[108:111], 0\n"
"v_mfma_f32_16x16x32_bf16 v[112:115], a[12:15], v[108:111], 0\n"
"ds_write_b128 v3, v[124:127]\n"
"v_mfma_f32_16x16x32_bf16 v[124:127], a[16:19], v[108:111], 0\n"
"s_add_u32 s98, s55, 0x40000\n"
"buffer_load_dwordx4 v[136:139], v0, s[4:7], s98 offen sc1\n"
"v_mfma_f32_16x16x32_bf16 v[140:143], a[20:23], v[108:111], 0\n"
"v_mfma_f32_16x16x32_bf16 v[48:51], a[24:27], v[120:123], v[48:51]\n"
"ds_write_b128 v3, v[144:147] offset:1024\n"
"v_mfma_f32_16x16x32_bf16 v[84:87], a[28:31], v[120:123], v[84:87]\n"
"v_mfma_f32_16x16x32_bf16 v[104:107], a[32:35], v[120:123], v[104:107]\n"
"v_mfma_f32_16x16x32_bf16 v[112:115], a[36:39], v[120:123], v[112:115]\n"
"ds_write_b128 v3, v[164:167] offset:2048\n"
"v_mfma_f32_16x16x32_bf16 v[124:127], a[40:43], v[120:123], v[124:127]\n"
"buffer_load_dwordx4 v[108:111], v0, s[4:7], s98 offen offset:1024 sc1\n"
"v_mfma_f32_16x16x32_bf16 v[140:143], a[44:47], v[120:123], v[140:143]\n"
"s_waitcnt vmcnt(26)\n"
"v_mfma_f32_16x16x32_bf16 v[48:51], a[48:51], v[128:131], v[48:51]\n"
"ds_write_b128 v3, v[196:199] offset:3072\n"
"v_mfma_f32_16x16x32_bf16 v[84:87], a[52:55], v[128:131], v[84:87]\n"
"v_mfma_f32_16x16x32_bf16 v[104:107], a[56:59], v[128:131], v[104:107]\n"
"v_mfma_f32_16x16x32_bf16 v[112:115], a[60:63], v[128:131], v[112:115]\n"
"ds_write_b128 v3, v[200:203] offset:4096\n"
"v_mfma_f32_16x16x32_bf16 v[124:127], a[64:67], v[128:131], v[124:127]\n"
"buffer_load_dwordx4 v[120:123], v0, s[4:7], s98 offen offset:2048 sc1\n"
"v_mfma_f32_16x16x32_bf16 v[140:143], a[68:71], v[128:131], v[140:143]\n"
"v_mfma_f32_16x16x32_bf16 v[48:51], a[72:75], v[152:155], v[48:51]\n"
"ds_write_b128 v3, v[208:211] offset:5120\n"
"v_mfma_f32_16x16x32_bf16 v[84:87], a[76:79], v[152:155], v[84:87]\n"
"s_waitcnt vmcnt(10)\n"
"v_add_f32_e32 v255, v255, v251\n"
"v_add_f32_e32 v254, v254, v250\n"
"v_mfma_f32_16x16x32_bf16 v[104:107], a[80:83], v[152:155], v[104:107]\n"
"v_add_f32_e32 v253, v253, v249\n"
"v_add_f32_e32 v252, v252, v248\n"
"v_mfma_f32_16x16x32_bf16 v[112:115], a[84:87], v[152:155], v[112:115]\n"
"v_add_f32_e32 v255, v255, v253\n"
"v_add_f32_e32 v254, v254, v252\n"
"v_mfma_f32_16x16x32_bf16 v[124:127], a[88:91], v[152:155], v[124:127]\n"
"buffer_load_dwordx4 v[128:131], v0, s[4:7], s98 offen offset:3072 sc1\n"
"v_mfma_f32_16x16x32_bf16 v[140:143], a[92:95], v[152:155], v[140:143]\n"
"v_add_f32_e32 v255, v255, v254\n"
"v_cmp_u_f32_e32 vcc, v255, v255\n"
"v_mfma_f32_16x16x32_bf16 v[48:51], a[96:99], v[184:187], v[48:51]\n"
"s_cbranch_vccnz .Lmk_m94s\n"
".Lmk_m94r:\n"
"v_mov_b32_e32 v254, v255\n"
"v_mfma_f32_16x16x32_bf16 v[84:87], a[100:103], v[184:187], v[84:87]\n"
"s_nop 0\n"
"v_permlane32_swap_b32_e32 v255, v254\n"
"v_add_f32_e32 v255, v255, v254\n"
"v_mfma_f32_16x16x32_bf16 v[104:107], a[104:107], v[184:187], v[104:107]\n"
"v_mul_f32_e32 v255, 0x3a800000, v255\n"
"v_mov_b32_e32 v254, v255\n"
"v_mfma_f32_16x16x32_bf16 v[112:115], a[108:111], v[184:187], v[112:115]\n"
"s_nop 0\n"
"v_permlane16_swap_b32_e32 v255, v254\n"
"v_cvt_pk_bf16_f32 v253, -v255, 0\n"
"v_mfma_f32_16x16x32_bf16 v[124:127], a[112:115], v[184:187], v[124:127]\n"
"buffer_load_dwordx4 v[144:147], v1, s[4:7], s98 offen sc1\n"
"v_mfma_f32_16x16x32_bf16 v[140:143], a[116:119], v[184:187], v[140:143]\n"
"s_waitcnt lgkmcnt(0)\n"
"s_barrier\n"
"v_mfma_f32_16x16x32_bf16 v[48:51], a[120:123], v[188:191], v[48:51]\n"
"v_fma_f32 v254, -v255, v255, v254\n"
"v_lshlrev_b32_e32 v253, 16, v253\n"
"v_mfma_f32_16x16x32_bf16 v[84:87], a[124:127], v[188:191], v[84:87]\n"
"v_max_f32_e32 v254, 0, v254\n"
"v_cvt_pk_bf16_f32 v254, v254, 0\n"
"v_mfma_f32_16x16x32_bf16 v[104:107], v[28:31], v[188:191], v[104:107]\n"
"s_add_u32 s97, s64, 0x4000\n"
"ds_read_b128 v[152:155], v4\n"
"ds_read_b128 v[164:167], v9\n"
"v_mfma_f32_16x16x32_bf16 v[112:115], v[40:43], v[188:191], v[112:115]\n"
"ds_read_b128 v[168:171], v4 offset:1024\n"
"ds_read_b128 v[172:175], v9 offset:64\n"
"v_mfma_f32_16x16x32_bf16 v[124:127], v[60:63], v[188:191], v[124:127]\n"
"buffer_load_dwordx4 v[176:179], v1, s[4:7], s98 offen offset:1024 sc1\n"
"v_mfma_f32_16x16x32_bf16 v[140:143], v[72:75], v[188:191], v[140:143]\n"
"buffer_store_dwordx4 v[52:55], v2, s[4:7], s97 offen offset:1024 sc1\n"
"v_mfma_f32_16x16x32_bf16 v[48:51], v[156:159], v[76:79], v[48:51]\n"
"ds_read_b128 v[184:187], v4 offset:2048\n"
"ds_read_b128 v[188:191], v9 offset:128\n"
"v_mfma_f32_16x16x32_bf16 v[84:87], v[132:135], v[76:79], v[84:87]\n"
"v_lshlrev_b32_e32 v254, 16, v254\n"
"v_add_f32_e32 v254, 0x3727c5ac, v254\n"
"v_mfma_f32_16x16x32_bf16 v[104:107], v[64:67], v[76:79], v[104:107]\n"
"v_cvt_pk_bf16_f32 v254, v254, 0\n"
"v_lshlrev_b32_e32 v254, 16, v254\n"
"v_mfma_f32_16x16x32_bf16 v[112:115], v[68:71], v[76:79], v[112:115]\n"
"v_sqrt_f32_e32 v254, v254\n"
"v_mfma_f32_16x16x32_bf16 v[124:127], v[88:91], v[76:79], v[124:127]\n"
"buffer_load_dwordx4 v[196:199], v1, s[4:7], s98 offen offset:2048 sc1\n"
"v_mfma_f32_16x16x32_bf16 v[140:143], v[160:163], v[76:79], v[140:143]\n"
"s_waitcnt lgkmcnt(0)\n"
"v_fmac_f32_e32 v152, v253, v164\n"
"v_fmac_f32_e32 v153, v253, v165\n"
"v_mfma_f32_16x16x32_bf16 v[48:51], v[92:95], v[116:119], v[48:51]\n"
"v_fmac_f32_e32 v154, v253, v166\n"
"v_fmac_f32_e32 v155, v253, v167\n"
"v_mfma_f32_16x16x32_bf16 v[84:87], v[180:183], v[116:119], v[84:87]\n"
"v_fmac_f32_e32 v168, v253, v172\n"
"v_fmac_f32_e32 v169, v253, v173\n"
"v_mfma_f32_16x16x32_bf16 v[104:107], v[100:103], v[116:119], v[104:107]\n"
"v_fmac_f32_e32 v170, v253, v174\n"
"v_fmac_f32_e32 v171, v253, v175\n"
"v_mfma_f32_16x16x32_bf16 v[112:115], v[148:151], v[116:119], v[112:115]\n"
"v_fmac_f32_e32 v184, v253, v188\n"
"v_fmac_f32_e32 v185, v253, v189\n"
"v_mfma_f32_16x16x32_bf16 v[124:127], v[192:195], v[116:119], v[124:127]\n"
"buffer_load_dwordx4 v[76:79], v1, s[4:7], s98 offen offset:3072 sc1\n"
"v_mfma_f32_16x16x32_bf16 v[140:143], v[56:59], v[116:119], v[140:143]\n"
"v_cmp_u_f32_e32 vcc, v48, v48\n"
"v_fmac_f32_e32 v186, v253, v190\n"
"; @A G1 0.2\n"
"v_mfma_f32_16x16x32_bf16 v[116:119], a[0:3], v[204:207], 0\n"
"s_cbranch_vccnz .Lmk_p90s\n"
".Lmk_p90r:\n"
"v_fmac_f32_e32 v187, v253, v191\n"
"v_mfma_f32_16x16x32_bf16 v[164:167], a[4:7], v[204:207], 0\n"
"ds_read_b128 v[172:175], v4 offset:3072\n"
"ds_read_b128 v[188:191], v9 offset:192\n"
"v_mfma_f32_16x16x32_bf16 v[200:203], a[8:11], v[204:207], 0\n"
"ds_read_b128 v[208:211], v4 offset:4096\n"
"ds_read_b128 v[216:219], v9 offset:256\n"
"v_mfma_f32_16x16x32_bf16 v[220:223], a[12:15], v[204:207], 0\n"
"ds_write_b128 v3, v[48:51] offset:6144\n"
"v_mfma_f32_16x16x32_bf16 v[48:51], a[16:19], v[204:207], 0\n"
"s_add_u32 s98, s52, 0x48000\n"
"buffer_load_dwordx4 v[224:227], v0, s[4:7], s98 offen sc1\n"
"s_mov_b32 s97, .Lmk_a2-.Lmk_g88\n"
"v_mfma_f32_16x16x32_bf16 v[228:231], a[20:23], v[204:207], 0\n"
"s_getpc_b64 s[84:85]\n"
".Lmk_g88:\n"
"s_add_u32 s84, s84, s97\n"
"s_addc_u32 s85, s85, 0\n"
"s_and_b32 s84, s84, -16\n"
"v_mfma_f32_16x16x32_bf16 v[116:119], a[24:27], v[212:215], v[116:119]\n"
"ds_write_b128 v3, v[84:87] offset:7168\n"
"v_mfma_f32_16x16x32_bf16 v[164:167], a[28:31], v[212:215], v[164:167]\n"
"s_and_b32 s85, s85, 0xffff\n"
"s_sub_u32 s97, .Lmk_end-.Lmk_g88, s97\n"
"s_min_u32 s86, s97, 0x3000\n"
"s_mov_b32 s87, 0x20000\n"
"v_mfma_f32_16x16x32_bf16 v[200:203], a[32:35], v[212:215], v[200:203]\n"
"s_mov_b32 m0, 0x27800\n"
"s_nop 0\n"
"buffer_load_dwordx4 v0, s[84:87], s99 offen lds\n"
"v_mfma_f32_16x16x32_bf16 v[220:223], a[36:39], v[212:215], v[220:223]\n"
"ds_write_b128 v3, v[104:107] offset:8192\n"
"v_mfma_f32_16x16x32_bf16 v[48:51], a[40:43], v[212:215], v[48:51]\n"
"buffer_load_dwordx4 v[84:87], v0, s[4:7], s98 offen offset:1024 sc1\n"
"v_mfma_f32_16x16x32_bf16 v[228:231], a[44:47], v[212:215], v[228:231]\n"
"ds_read_b128 v[104:107], v4 offset:5120\n"
"ds_read_b128 v[204:207], v9 offset:320\n"
"v_mfma_f32_16x16x32_bf16 v[116:119], a[48:51], v[32:35], v[116:119]\n"
"ds_write_b128 v3, v[112:115] offset:9216\n"
"v_mfma_f32_16x16x32_bf16 v[164:167], a[52:55], v[32:35], v[164:167]\n"
"v_cvt_pk_bf16_f32 v254, v254, 0\n"
"v_lshlrev_b32_e32 v254, 16, v254\n"
"v_mfma_f32_16x16x32_bf16 v[200:203], a[56:59], v[32:35], v[200:203]\n"
"v_rcp_f32_e32 v255, v254\n"
"v_mfma_f32_16x16x32_bf16 v[220:223], a[60:63], v[32:35], v[220:223]\n"
"ds_write_b128 v3, v[124:127] offset:10240\n"
"v_mfma_f32_16x16x32_bf16 v[48:51], a[64:67], v[32:35], v[48:51]\n"
"buffer_load_dwordx4 v[112:115], v0, s[4:7], s98 offen offset:2048 sc1\n"
"v_mfma_f32_16x16x32_bf16 v[228:231], a[68:71], v[32:35], v[228:231]\n"
"s_waitcnt lgkmcnt(2)\n"
"v_fmac_f32_e32 v172, v253, v188\n"
"v_fmac_f32_e32 v173, v253, v189\n"
"v_mfma_f32_16x16x32_bf16 v[116:119], a[72:75], v[44:47], v[116:119]\n"
"ds_write_b128 v3, v[140:143] offset:11264\n"
"v_mfma_f32_16x16x32_bf16 v[164:167], a[76:79], v[44:47], v[164:167]\n"
"v_fmac_f32_e32 v174, v253, v190\n"
"v_fmac_f32_e32 v175, v253, v191\n"
"v_mfma_f32_16x16x32_bf16 v[200:203], a[80:83], v[44:47], v[200:203]\n"
"v_fmac_f32_e32 v208, v253, v216\n"
"v_fmac_f32_e32 v209, v253, v217\n"
"v_mfma_f32_16x16x32_bf16 v[220:223], a[84:87], v[44:47], v[220:223]\n"
"v_fmac_f32_e32 v210, v253, v218\n"
"v_fmac_f32_e32 v211, v253, v219\n"
"v_mfma_f32_16x16x32_bf16 v[48:51], a[88:91], v[44:47], v[48:51]\n"
"buffer_load_dwordx4 v[32:35], v0, s[4:7], s98 offen offset:3072 sc1\n"
"v_mfma_f32_16x16x32_bf16 v[228:231], a[92:95], v[44:47], v[228:231]\n"
"s_waitcnt vmcnt(18)\n"
"v_fmac_f32_e32 v104, v253, v204\n"
"v_fmac_f32_e32 v105, v253, v205\n"
"v_mfma_f32_16x16x32_bf16 v[116:119], a[96:99], v[80:83], v[116:119]\n"
"v_fmac_f32_e32 v106, v253, v206\n"
"v_fmac_f32_e32 v107, v253, v207\n"
"v_mfma_f32_16x16x32_bf16 v[164:167], a[100:103], v[80:83], v[164:167]\n"
"v_mul_f32_e32 v254, 0x3e8293ee, v255\n"
"v_mfma_f32_16x16x32_bf16 v[200:203], a[104:107], v[80:83], v[200:203]\n"
"v_mfma_f32_16x16x32_bf16 v[220:223], a[108:111], v[80:83], v[220:223]\n"
"v_mfma_f32_16x16x32_bf16 v[48:51], a[112:115], v[80:83], v[48:51]\n"
"buffer_load_dwordx4 v[44:47], v1, s[4:7], s98 offen sc1\n"
"v_mfma_f32_16x16x32_bf16 v[228:231], a[116:119], v[80:83], v[228:231]\n"
"v_mfma_f32_16x16x32_bf16 v[116:119], a[120:123], v[96:99], v[116:119]\n"
"v_mfma_f32_16x16x32_bf16 v[164:167], a[124:127], v[96:99], v[164:167]\n"
"v_mfma_f32_16x16x32_bf16 v[200:203], v[28:31], v[96:99], v[200:203]\n"
"v_mfma_f32_16x16x32_bf16 v[220:223], v[40:43], v[96:99], v[220:223]\n"
"v_mfma_f32_16x16x32_bf16 v[48:51], v[60:63], v[96:99], v[48:51]\n"
"buffer_load_dwordx4 v[80:83], v1, s[4:7], s98 offen offset:1024 sc1\n"
"v_mfma_f32_16x16x32_bf16 v[228:231], v[72:75], v[96:99], v[228:231]\n"
"s_waitcnt vmcnt(17)\n"
"s_add_u32 s97, s64, 0x8000\n"
"v_mfma_f32_16x16x32_bf16 v[116:119], v[156:159], v[24:27], v[116:119]\n"
"v_mfma_f32_16x16x32_bf16 v[164:167], v[132:135], v[24:27], v[164:167]\n"
"buffer_store_dwordx4 v[52:55], v2, s[4:7], s97 offen sc1\n"
"v_mfma_f32_16x16x32_bf16 v[200:203], v[64:67], v[24:27], v[200:203]\n"
"v_mfma_f32_16x16x32_bf16 v[220:223], v[68:71], v[24:27], v[220:223]\n"
"v_mfma_f32_16x16x32_bf16 v[48:51], v[88:91], v[24:27], v[48:51]\n"
"s_waitcnt vmcnt(15)\n"
"v_max_u32_e32 v253, v136, v137\n"
"v_max3_u32 v253, v253, v138, v139\n"
"v_mfma_f32_16x16x32_bf16 v[228:231], v[160:163], v[24:27], v[228:231]\n"
"s_waitcnt vmcnt(13)\n"
"v_max3_u32 v253, v253, v108, v109\n"
"v_max3_u32 v253, v253, v110, v111\n"
"v_mfma_f32_16x16x32_bf16 v[116:119], v[92:95], v[36:39], v[116:119]\n"
"s_add_u32 s97, s68, s80\n"
"v_max3_u32 v253, v253, v120, v121\n"
"v_max3_u32 v253, v253, v122, v123\n"
"v_mfma_f32_16x16x32_bf16 v[164:167], v[180:183], v[36:39], v[164:167]\n"
"v_max3_u32 v253, v253, v128, v129\n"
"v_max3_u32 v253, v253, v130, v131\n"
"v_mfma_f32_16x16x32_bf16 v[200:203], v[100:103], v[36:39], v[200:203]\n"
"v_cmp_eq_u32_e32 vcc, -1, v253\n"
"v_mfma_f32_16x16x32_bf16 v[220:223], v[148:151], v[36:39], v[220:223]\n"
"s_cbranch_vccnz .Lmk_q92s\n"
".Lmk_q92r:\n"
"v_mfma_f32_16x16x32_bf16 v[48:51], v[192:195], v[36:39], v[48:51]\n"
"buffer_load_dwordx4 v[24:27], v1, s[4:7], s98 offen offset:2048 sc1\n"
"v_mfma_f32_16x16x32_bf16 v[228:231], v[56:59], v[36:39], v[228:231]\n"
"v_cmp_u_f32_e32 vcc, v116, v116\n"
"; @A G1 0.3\n"
"v_mfma_f32_16x16x32_bf16 v[152:155], a[0:3], v[136:139], v[152:155]\n"
"s_cbranch_vccnz .Lmk_p91s\n"
".Lmk_p91r:\n"
"v_mfma_f32_16x16x32_bf16 v[168:171], a[4:7], v[136:139], v[168:171]\n"
"buffer_load_dwordx4 v[36:39], v1, s[4:7], s98 offen offset:3072 sc1\n"
"v_mfma_f32_16x16x32_bf16 v[184:187], a[8:11], v[136:139], v[184:187]\n"
"buffer_store_dwordx2 v[18:19], v11, s[4:7], s97 offen sc1\n"
"v_mfma_f32_16x16x32_bf16 v[172:175], a[12:15], v[136:139], v[172:175]\n"
"ds_write_b128 v3, v[116:119] offset:12288\n"
"v_mfma_f32_16x16x32_bf16 v[208:211], a[16:19], v[136:139], v[208:211]\n"
"s_add_u32 s98, s53, 0x48000\n"
"buffer_load_dwordx4 v[96:99], v0, s[4:7], s98 offen sc1\n"
"v_mfma_f32_16x16x32_bf16 v[104:107], a[20:23], v[136:139], v[104:107]\n"
"v_mfma_f32_16x16x32_bf16 v[152:155], a[24:27], v[108:111], v[152:155]\n"
"ds_write_b128 v3, v[164:167] offset:13312\n"
"v_mfma_f32_16x16x32_bf16 v[168:171], a[28:31], v[108:111], v[168:171]\n"
"v_mfma_f32_16x16x32_bf16 v[184:187], a[32:35], v[108:111], v[184:187]\n"
"v_mfma_f32_16x16x32_bf16 v[172:175], a[36:39], v[108:111], v[172:175]\n"
"ds_write_b128 v3, v[200:203] offset:14336\n"
"v_mfma_f32_16x16x32_bf16 v[208:211], a[40:43], v[108:111], v[208:211]\n"
"buffer_load_dwordx4 v[116:119], v0, s[4:7], s98 offen offset:1024 sc1\n"
"v_mfma_f32_16x16x32_bf16 v[104:107], a[44:47], v[108:111], v[104:107]\n"
"v_mfma_f32_16x16x32_bf16 v[152:155], a[48:51], v[120:123], v[152:155]\n"
"ds_write_b128 v3, v[220:223] offset:15360\n"
"v_mfma_f32_16x16x32_bf16 v[168:171], a[52:55], v[120:123], v[168:171]\n"
"v_mfma_f32_16x16x32_bf16 v[184:187], a[56:59], v[120:123], v[184:187]\n"
"v_mfma_f32_16x16x32_bf16 v[172:175], a[60:63], v[120:123], v[172:175]\n"
"ds_write_b128 v3, v[48:51] offset:16384\n"
"v_mfma_f32_16x16x32_bf16 v[208:211], a[64:67], v[120:123], v[208:211]\n"
"s_waitcnt vmcnt(16)\n"
"v_max_u32_e32 v253, v144, v145\n"
"v_max3_u32 v253, v253, v146, v147\n"
"v_mfma_f32_16x16x32_bf16 v[104:107], a[68:71], v[120:123], v[104:107]\n"
"s_waitcnt vmcnt(13)\n"
"v_max3_u32 v253, v253, v176, v177\n"
"v_max3_u32 v253, v253, v178, v179\n"
"v_mfma_f32_16x16x32_bf16 v[152:155], a[72:75], v[128:131], v[152:155]\n"
"v_max3_u32 v253, v253, v196, v197\n"
"v_max3_u32 v253, v253, v198, v199\n"
"v_mfma_f32_16x16x32_bf16 v[168:171], a[76:79], v[128:131], v[168:171]\n"
"v_max3_u32 v253, v253, v76, v77\n"
"v_max3_u32 v253, v253, v78, v79\n"
"v_mfma_f32_16x16x32_bf16 v[184:187], a[80:83], v[128:131], v[184:187]\n"
"v_cmp_eq_u32_e32 vcc, -1, v253\n"
"v_mfma_f32_16x16x32_bf16 v[172:175], a[84:87], v[128:131], v[172:175]\n"
"s_cbranch_vccnz .Lmk_q93s\n"
".Lmk_q93r:\n"
"v_mfma_f32_16x16x32_bf16 v[208:211], a[88:91], v[128:131], v[208:211]\n"
"buffer_load_dwordx4 v[48:51], v0, s[4:7], s98 offen offset:2048 sc1\n"
"v_mfma_f32_16x16x32_bf16 v[104:107], a[92:95], v[128:131], v[104:107]\n"
"buffer_load_dwordx4 v[108:111], v0, s[4:7], s98 offen offset:3072 sc1\n"
"v_mfma_f32_16x16x32_bf16 v[152:155], a[96:99], v[144:147], v[152:155]\n"
"ds_write_b128 v3, v[228:231] offset:17408\n"
"v_mfma_f32_16x16x32_bf16 v[168:171], a[100:103], v[144:147], v[168:171]\n"
"v_mfma_f32_16x16x32_bf16 v[184:187], a[104:107], v[144:147], v[184:187]\n"
"v_mfma_f32_16x16x32_bf16 v[172:175], a[108:111], v[144:147], v[172:175]\n"
"s_waitcnt lgkmcnt(0)\n"
"s_barrier\n"
"v_mfma_f32_16x16x32_bf16 v[208:211], a[112:115], v[144:147], v[208:211]\n"
"buffer_load_dwordx4 v[120:123], v1, s[4:7], s98 offen sc1\n"
"v_mfma_f32_16x16x32_bf16 v[104:107], a[116:119], v[144:147], v[104:107]\n"
"ds_read_b128 v[124:127], v5 offset:2048\n"
"ds_read_b128 v[128:131], v6 offset:2048\n"
"v_mfma_f32_16x16x32_bf16 v[152:155], a[120:123], v[176:179], v[152:155]\n"
"ds_read_b128 v[136:139], v5 offset:3072\n"
"ds_read_b128 v[140:143], v6 offset:3072\n"
"v_mfma_f32_16x16x32_bf16 v[168:171], a[124:127], v[176:179], v[168:171]\n"
"v_mfma_f32_16x16x32_bf16 v[184:187], v[28:31], v[176:179], v[184:187]\n"
"v_mfma_f32_16x16x32_bf16 v[172:175], v[40:43], v[176:179], v[172:175]\n"
"ds_read_b128 v[144:147], v5 offset:4096\n"
"ds_read_b128 v[164:167], v6 offset:4096\n"
"v_mfma_f32_16x16x32_bf16 v[208:211], v[60:63], v[176:179], v[208:211]\n"
"buffer_load_dwordx4 v[188:191], v1, s[4:7], s98 offen offset:1024 sc1\n"
"v_mfma_f32_16x16x32_bf16 v[104:107], v[72:75], v[176:179], v[104:107]\n"
"ds_read_b128 v[176:179], v5 offset:5120\n"
"ds_read_b128 v[200:203], v6 offset:5120\n"
"v_mfma_f32_16x16x32_bf16 v[152:155], v[156:159], v[196:199], v[152:155]\n"
"s_waitcnt lgkmcnt(4)\n"
"v_add_f32_e32 v124, v124, v128\n"
"v_add_f32_e32 v125, v125, v129\n"
"v_mfma_f32_16x16x32_bf16 v[168:171], v[132:135], v[196:199], v[168:171]\n"
"s_add_u32 s97, s64, 0x8000\n"
"v_add_f32_e32 v126, v126, v130\n"
"v_add_f32_e32 v127, v127, v131\n"
"v_mfma_f32_16x16x32_bf16 v[184:187], v[64:67], v[196:199], v[184:187]\n"
"ds_read_b128 v[128:131], v5\n"
"ds_read_b128 v[204:207], v6\n"
"v_mfma_f32_16x16x32_bf16 v[172:175], v[68:71], v[196:199], v[172:175]\n"
"buffer_store_dwordx4 v[52:55], v2, s[4:7], s97 offen offset:1024 sc1\n"
"v_mfma_f32_16x16x32_bf16 v[208:211], v[88:91], v[196:199], v[208:211]\n"
"buffer_load_dwordx4 v[52:55], v1, s[4:7], s98 offen offset:2048 sc1\n"
"v_mfma_f32_16x16x32_bf16 v[104:107], v[160:163], v[196:199], v[104:107]\n"
"ds_read_b128 v[196:199], v5 offset:1024\n"
"ds_read_b128 v[212:215], v6 offset:1024\n"
"v_mfma_f32_16x16x32_bf16 v[152:155], v[92:95], v[76:79], v[152:155]\n"
"s_waitcnt lgkmcnt(4)\n"
"v_add_f32_e32 v136, v136, v140\n"
"v_add_f32_e32 v137, v137, v141\n"
"v_mfma_f32_16x16x32_bf16 v[168:171], v[180:183], v[76:79], v[168:171]\n"
"v_add_f32_e32 v138, v138, v142\n"
"v_add_f32_e32 v139, v139, v143\n"
"v_mfma_f32_16x16x32_bf16 v[184:187], v[100:103], v[76:79], v[184:187]\n"
"v_add_f32_e32 v144, v144, v164\n"
"v_add_f32_e32 v145, v145, v165\n"
"v_mfma_f32_16x16x32_bf16 v[172:175], v[148:151], v[76:79], v[172:175]\n"
"v_add_f32_e32 v146, v146, v166\n"
"v_add_f32_e32 v147, v147, v167\n"
"v_mfma_f32_16x16x32_bf16 v[208:211], v[192:195], v[76:79], v[208:211]\n"
"buffer_load_dwordx4 v[140:143], v1, s[4:7], s98 offen offset:3072 sc1\n"
"v_mfma_f32_16x16x32_bf16 v[104:107], v[56:59], v[76:79], v[104:107]\n"
"s_waitcnt lgkmcnt(0)\n"
"v_add_f32_e32 v176, v176, v200\n"
"v_add_f32_e32 v177, v177, v201\n"
"v_add_f32_e32 v178, v178, v202\n"
"v_add_f32_e32 v179, v179, v203\n"
"v_add_f32_e32 v128, v128, v204\n"
"v_add_f32_e32 v129, v129, v205\n"
"v_add_f32_e32 v130, v130, v206\n"
"v_add_f32_e32 v131, v131, v207\n"
"v_add_f32_e32 v196, v196, v212\n"
"v_add_f32_e32 v197, v197, v213\n"
"v_add_f32_e32 v198, v198, v214\n"
"v_add_f32_e32 v199, v199, v215\n"
".Lmk_a1:\n"
"s_waitcnt vmcnt(17)\n"
"; @A G1 1.0\n"
"v_mfma_f32_16x16x32_bf16 v[76:79], a[0:3], v[224:227], 0\n"
"v_mfma_f32_16x16x32_bf16 v[164:167], a[4:7], v[224:227], 0\n"
"s_add_u32 s98, s62, 0x4800\n"
"v_mfma_f32_16x16x32_bf16 v[200:203], a[8:11], v[224:227], 0\n"
"v_mfma_f32_16x16x32_bf16 v[204:207], a[12:15], v[224:227], 0\n"
"v_add_f32_e32 v184, v184, v124\n"
"v_add_f32_e32 v185, v185, v125\n"
"v_mfma_f32_16x16x32_bf16 v[212:215], a[16:19], v[224:227], 0\n"
"s_add_u32 s97, s54, 0x48000\n"
"buffer_load_dwordx4 v[216:219], v0, s[4:7], s97 offen sc1\n"
"v_mfma_f32_16x16x32_bf16 v[220:223], a[20:23], v[224:227], 0\n"
"buffer_load_dword v253, v10, s[4:7], s98 offen sc1\n"
"v_mfma_f32_16x16x32_bf16 v[76:79], a[24:27], v[84:87], v[76:79]\n"
"v_add_f32_e32 v186, v186, v126\n"
"v_add_f32_e32 v187, v187, v127\n"
"v_mfma_f32_16x16x32_bf16 v[164:167], a[28:31], v[84:87], v[164:167]\n"
"v_add_f32_e32 v172, v172, v136\n"
"v_add_f32_e32 v173, v173, v137\n"
"v_mfma_f32_16x16x32_bf16 v[200:203], a[32:35], v[84:87], v[200:203]\n"
"s_add_u32 s96, s70, 0\n"
"buffer_load_dwordx4 a[128:131], v0, s[4:7], s96 offen sc1\n"
"v_mfma_f32_16x16x32_bf16 v[204:207], a[36:39], v[84:87], v[204:207]\n"
"buffer_load_dword v252, v10, s[4:7], s98 offen offset:256 sc1\n"
"v_mfma_f32_16x16x32_bf16 v[212:215], a[40:43], v[84:87], v[212:215]\n"
"buffer_load_dwordx4 v[124:127], v0, s[4:7], s97 offen offset:1024 sc1\n"
"v_mfma_f32_16x16x32_bf16 v[220:223], a[44:47], v[84:87], v[220:223]\n"
"s_waitcnt vmcnt(20)\n"
"v_add_f32_e32 v174, v174, v138\n"
"v_add_f32_e32 v175, v175, v139\n"
"v_mfma_f32_16x16x32_bf16 v[76:79], a[48:51], v[112:115], v[76:79]\n"
"buffer_load_dword v251, v10, s[4:7], s98 offen offset:512 sc1\n"
"v_mfma_f32_16x16x32_bf16 v[164:167], a[52:55], v[112:115], v[164:167]\n"
"v_mul_f32_e32 v184, v184, v255\n"
"v_mul_f32_e32 v185, v185, v255\n"
"v_mfma_f32_16x16x32_bf16 v[200:203], a[56:59], v[112:115], v[200:203]\n"
"v_mul_f32_e32 v186, v186, v255\n"
"v_mul_f32_e32 v187, v187, v255\n"
"v_mfma_f32_16x16x32_bf16 v[204:207], a[60:63], v[112:115], v[204:207]\n"
"buffer_load_dword v250, v10, s[4:7], s98 offen offset:768 sc1\n"
"v_mfma_f32_16x16x32_bf16 v[212:215], a[64:67], v[112:115], v[212:215]\n"
"buffer_load_dwordx4 v[84:87], v0, s[4:7], s97 offen offset:2048 sc1\n"
"v_mfma_f32_16x16x32_bf16 v[220:223], a[68:71], v[112:115], v[220:223]\n"
"v_mul_f32_e32 v172, v172, v255\n"
"v_mul_f32_e32 v173, v173, v255\n"
"v_mfma_f32_16x16x32_bf16 v[76:79], a[72:75], v[32:35], v[76:79]\n"
"buffer_load_dword v249, v10, s[4:7], s98 offen offset:1024 sc1\n"
"v_mfma_f32_16x16x32_bf16 v[164:167], a[76:79], v[32:35], v[164:167]\n"
"v_mul_f32_e32 v174, v174, v255\n"
"v_mul_f32_e32 v175, v175, v255\n"
"v_mfma_f32_16x16x32_bf16 v[200:203], a[80:83], v[32:35], v[200:203]\n"
"v_cvt_pk_bf16_f32 v112, v184, v185\n"
"v_cvt_pk_bf16_f32 v113, v186, v187\n"
"v_mfma_f32_16x16x32_bf16 v[204:207], a[84:87], v[32:35], v[204:207]\n"
"buffer_load_dword v248, v10, s[4:7], s98 offen offset:1280 sc1\n"
"v_mfma_f32_16x16x32_bf16 v[212:215], a[88:91], v[32:35], v[212:215]\n"
"buffer_load_dwordx4 v[136:139], v0, s[4:7], s97 offen offset:3072 sc1\n"
"v_mfma_f32_16x16x32_bf16 v[220:223], a[92:95], v[32:35], v[220:223]\n"
"s_waitcnt vmcnt(24)\n"
"v_cvt_pk_bf16_f32 v114, v172, v173\n"
"v_cvt_pk_bf16_f32 v115, v174, v175\n"
"v_mfma_f32_16x16x32_bf16 v[76:79], a[96:99], v[44:47], v[76:79]\n"
"buffer_load_dword v247, v10, s[4:7], s98 offen offset:1536 sc1\n"
"v_mfma_f32_16x16x32_bf16 v[164:167], a[100:103], v[44:47], v[164:167]\n"
"ds_write_b128 v7, v[112:115] offset:24576\n"
"v_mfma_f32_16x16x32_bf16 v[200:203], a[104:107], v[44:47], v[200:203]\n"
"v_add_f32_e32 v208, v208, v144\n"
"v_add_f32_e32 v209, v209, v145\n"
"v_mfma_f32_16x16x32_bf16 v[204:207], a[108:111], v[44:47], v[204:207]\n"
"buffer_load_dword v246, v10, s[4:7], s98 offen offset:1792 sc1\n"
"v_mfma_f32_16x16x32_bf16 v[212:215], a[112:115], v[44:47], v[212:215]\n"
"buffer_load_dwordx4 v[32:35], v1, s[4:7], s97 offen sc1\n"
"v_mfma_f32_16x16x32_bf16 v[220:223], a[116:119], v[44:47], v[220:223]\n"
"buffer_load_dwordx4 a[132:135], v0, s[4:7], s96 offen offset:1024 sc1\n"
"v_mfma_f32_16x16x32_bf16 v[76:79], a[120:123], v[80:83], v[76:79]\n"
"v_add_f32_e32 v210, v210, v146\n"
"v_add_f32_e32 v211, v211, v147\n"
"v_mfma_f32_16x16x32_bf16 v[164:167], a[124:127], v[80:83], v[164:167]\n"
"v_add_f32_e32 v104, v104, v176\n"
"v_add_f32_e32 v105, v105, v177\n"
"v_mfma_f32_16x16x32_bf16 v[200:203], v[28:31], v[80:83], v[200:203]\n"
"v_add_f32_e32 v106, v106, v178\n"
"v_add_f32_e32 v107, v107, v179\n"
"v_mfma_f32_16x16x32_bf16 v[204:207], v[40:43], v[80:83], v[204:207]\n"
"v_mul_f32_e32 v208, v208, v255\n"
"v_mul_f32_e32 v209, v209, v255\n"
"v_mfma_f32_16x16x32_bf16 v[212:215], v[60:63], v[80:83], v[212:215]\n"
"buffer_load_dwordx4 v[44:47], v1, s[4:7], s97 offen offset:1024 sc1\n"
"v_mfma_f32_16x16x32_bf16 v[220:223], v[72:75], v[80:83], v[220:223]\n"
"s_waitcnt vmcnt(26)\n"
"v_mul_f32_e32 v210, v210, v255\n"
"v_mul_f32_e32 v211, v211, v255\n"
"v_mfma_f32_16x16x32_bf16 v[76:79], v[156:159], v[24:27], v[76:79]\n"
"v_mul_f32_e32 v104, v104, v255\n"
"v_mul_f32_e32 v105, v105, v255\n"
"v_mfma_f32_16x16x32_bf16 v[164:167], v[132:135], v[24:27], v[164:167]\n"
"v_mul_f32_e32 v106, v106, v255\n"
"v_mul_f32_e32 v107, v107, v255\n"
"v_mfma_f32_16x16x32_bf16 v[200:203], v[64:67], v[24:27], v[200:203]\n"
"v_cvt_pk_bf16_f32 v80, v208, v209\n"
"v_cvt_pk_bf16_f32 v81, v210, v211\n"
"v_mfma_f32_16x16x32_bf16 v[204:207], v[68:71], v[24:27], v[204:207]\n"
"v_cvt_pk_bf16_f32 v82, v104, v105\n"
"v_cvt_pk_bf16_f32 v83, v106, v107\n"
"v_mfma_f32_16x16x32_bf16 v[212:215], v[88:91], v[24:27], v[212:215]\n"
"buffer_load_dwordx4 v[104:107], v1, s[4:7], s97 offen offset:2048 sc1\n"
"v_mfma_f32_16x16x32_bf16 v[220:223], v[160:163], v[24:27], v[220:223]\n"
"ds_write_b128 v7, v[80:83] offset:57344\n"
"v_mfma_f32_16x16x32_bf16 v[76:79], v[92:95], v[36:39], v[76:79]\n"
"v_add_f32_e32 v152, v152, v128\n"
"v_add_f32_e32 v153, v153, v129\n"
"v_mfma_f32_16x16x32_bf16 v[164:167], v[180:183], v[36:39], v[164:167]\n"
"v_add_f32_e32 v154, v154, v130\n"
"v_add_f32_e32 v155, v155, v131\n"
"v_mfma_f32_16x16x32_bf16 v[200:203], v[100:103], v[36:39], v[200:203]\n"
"v_add_f32_e32 v168, v168, v196\n"
"v_add_f32_e32 v169, v169, v197\n"
"v_mfma_f32_16x16x32_bf16 v[204:207], v[148:151], v[36:39], v[204:207]\n"
"v_add_f32_e32 v170, v170, v198\n"
"v_add_f32_e32 v171, v171, v199\n"
"v_mfma_f32_16x16x32_bf16 v[212:215], v[192:195], v[36:39], v[212:215]\n"
"buffer_load_dwordx4 v[24:27], v1, s[4:7], s97 offen offset:3072 sc1\n"
"v_mfma_f32_16x16x32_bf16 v[220:223], v[56:59], v[36:39], v[220:223]\n"
"s_waitcnt vmcnt(25)\n"
"v_cmp_u_f32_e32 vcc, v76, v76\n"
"v_mul_f32_e32 v152, v152, v254\n"
"; @A G1 1.1\n"
"v_mfma_f32_16x16x32_bf16 v[36:39], a[0:3], v[96:99], 0\n"
"s_cbranch_vccnz .Lmk_p95s\n"
".Lmk_p95r:\n"
"v_mul_f32_e32 v153, v153, v254\n"
"v_mfma_f32_16x16x32_bf16 v[128:131], a[4:7], v[96:99], 0\n"
"buffer_load_dwordx4 a[136:139], v0, s[4:7], s96 offen offset:2048 sc1\n"
"v_mfma_f32_16x16x32_bf16 v[144:147], a[8:11], v[96:99], 0\n"
"v_mul_f32_e32 v154, v154, v254\n"
"v_mul_f32_e32 v155, v155, v254\n"
"v_mfma_f32_16x16x32_bf16 v[172:175], a[12:15], v[96:99], 0\n"
"ds_write_b128 v3, v[76:79]\n"
"v_mfma_f32_16x16x32_bf16 v[76:79], a[16:19], v[96:99], 0\n"
"s_add_u32 s98, s55, 0x48000\n"
"buffer_load_dwordx4 v[176:179], v0, s[4:7], s98 offen sc1\n"
"v_mfma_f32_16x16x32_bf16 v[184:187], a[20:23], v[96:99], 0\n"
"v_mul_f32_e32 v168, v168, v254\n"
"v_mul_f32_e32 v169, v169, v254\n"
"v_mfma_f32_16x16x32_bf16 v[36:39], a[24:27], v[116:119], v[36:39]\n"
"ds_write_b128 v3, v[164:167] offset:1024\n"
"v_mfma_f32_16x16x32_bf16 v[128:131], a[28:31], v[116:119], v[128:131]\n"
"v_mul_f32_e32 v170, v170, v254\n"
"v_mul_f32_e32 v171, v171, v254\n"
"v_mfma_f32_16x16x32_bf16 v[144:147], a[32:35], v[116:119], v[144:147]\n"
"v_cvt_pk_bf16_f32 v96, v152, v153\n"
"v_cvt_pk_bf16_f32 v97, v154, v155\n"
"v_mfma_f32_16x16x32_bf16 v[172:175], a[36:39], v[116:119], v[172:175]\n"
"ds_write_b128 v3, v[200:203] offset:2048\n"
"v_mfma_f32_16x16x32_bf16 v[76:79], a[40:43], v[116:119], v[76:79]\n"
"buffer_load_dwordx4 v[152:155], v0, s[4:7], s98 offen offset:1024 sc1\n"
"v_mfma_f32_16x16x32_bf16 v[184:187], a[44:47], v[116:119], v[184:187]\n"
"s_waitcnt vmcnt(26)\n"
"v_cvt_pk_bf16_f32 v98, v168, v169\n"
"v_cvt_pk_bf16_f32 v99, v170, v171\n"
"v_mfma_f32_16x16x32_bf16 v[36:39], a[48:51], v[48:51], v[36:39]\n"
"ds_write_b128 v3, v[204:207] offset:3072\n"
"v_mfma_f32_16x16x32_bf16 v[128:131], a[52:55], v[48:51], v[128:131]\n"
"s_add_u32 s97, s67, 0\n"
"buffer_load_dwordx2 v[254:255], v12, s[4:7], s97 offen sc1\n"
"v_mfma_f32_16x16x32_bf16 v[144:147], a[56:59], v[48:51], v[144:147]\n"
"ds_write_b128 v2, v[96:99] offset:49152\n"
"s_add_u32 s95, s64, 0x2000\n"
"v_mfma_f32_16x16x32_bf16 v[172:175], a[60:63], v[48:51], v[172:175]\n"
"ds_write_b128 v3, v[212:215] offset:4096\n"
"v_mfma_f32_16x16x32_bf16 v[76:79], a[64:67], v[48:51], v[76:79]\n"
"buffer_load_dwordx4 v[96:99], v0, s[4:7], s98 offen offset:2048 sc1\n"
"v_mfma_f32_16x16x32_bf16 v[184:187], a[68:71], v[48:51], v[184:187]\n"
"buffer_store_dwordx4 v[112:115], v7, s[4:7], s95 offen sc1\n"
"v_mfma_f32_16x16x32_bf16 v[36:39], a[72:75], v[108:111], v[36:39]\n"
"ds_write_b128 v3, v[220:223] offset:5120\n"
"v_mfma_f32_16x16x32_bf16 v[128:131], a[76:79], v[108:111], v[128:131]\n"
"v_mfma_f32_16x16x32_bf16 v[144:147], a[80:83], v[108:111], v[144:147]\n"
"v_mfma_f32_16x16x32_bf16 v[172:175], a[84:87], v[108:111], v[172:175]\n"
"buffer_load_dwordx4 a[140:143], v0, s[4:7], s96 offen offset:3072 sc1\n"
"s_add_u32 s95, s64, 0x6000\n"
"v_mfma_f32_16x16x32_bf16 v[76:79], a[88:91], v[108:111], v[76:79]\n"
"buffer_load_dwordx4 v[48:51], v0, s[4:7], s98 offen offset:3072 sc1\n"
"v_mfma_f32_16x16x32_bf16 v[184:187], a[92:95], v[108:111], v[184:187]\n"
"s_waitcnt vmcnt(29)\n"
"buffer_store_dwordx4 v[80:83], v7, s[4:7], s95 offen sc1\n"
"v_mfma_f32_16x16x32_bf16 v[36:39], a[96:99], v[120:123], v[36:39]\n"
"v_mfma_f32_16x16x32_bf16 v[128:131], a[100:103], v[120:123], v[128:131]\n"
"v_mfma_f32_16x16x32_bf16 v[144:147], a[104:107], v[120:123], v[144:147]\n"
"v_mfma_f32_16x16x32_bf16 v[172:175], a[108:111], v[120:123], v[172:175]\n"
"v_mfma_f32_16x16x32_bf16 v[76:79], a[112:115], v[120:123], v[76:79]\n"
"buffer_load_dwordx4 v[80:83], v1, s[4:7], s98 offen sc1\n"
"v_mfma_f32_16x16x32_bf16 v[184:187], a[116:119], v[120:123], v[184:187]\n"
"s_waitcnt lgkmcnt(0)\n"
"s_barrier\n"
"v_mfma_f32_16x16x32_bf16 v[36:39], a[120:123], v[188:191], v[36:39]\n"
"s_waitcnt vmcnt(15)\n"
"v_add_f32_e32 v253, v253, v249\n"
"v_add_f32_e32 v252, v252, v248\n"
"v_mfma_f32_16x16x32_bf16 v[128:131], a[124:127], v[188:191], v[128:131]\n"
"v_add_f32_e32 v251, v251, v247\n"
"v_add_f32_e32 v250, v250, v246\n"
"v_mfma_f32_16x16x32_bf16 v[144:147], v[28:31], v[188:191], v[144:147]\n"
"v_add_f32_e32 v253, v253, v251\n"
"v_add_f32_e32 v252, v252, v250\n"
"v_mfma_f32_16x16x32_bf16 v[172:175], v[40:43], v[188:191], v[172:175]\n"
"v_add_f32_e32 v253, v253, v252\n"
"v_cmp_u_f32_e32 vcc, v253, v253\n"
"v_mfma_f32_16x16x32_bf16 v[76:79], v[60:63], v[188:191], v[76:79]\n"
"buffer_load_dwordx4 v[108:111], v1, s[4:7], s98 offen offset:1024 sc1\n"
"v_mfma_f32_16x16x32_bf16 v[184:187], v[72:75], v[188:191], v[184:187]\n"
"s_cbranch_vccnz .Lmk_m100s\n"
".Lmk_m100r:\n"
"v_mov_b32_e32 v252, v253\n"
"v_mfma_f32_16x16x32_bf16 v[36:39], v[156:159], v[52:55], v[36:39]\n"
"s_nop 0\n"
"v_permlane32_swap_b32_e32 v253, v252\n"
"v_add_f32_e32 v253, v253, v252\n"
"v_mfma_f32_16x16x32_bf16 v[128:131], v[132:135], v[52:55], v[128:131]\n"
"v_mul_f32_e32 v253, 0x3a800000, v253\n"
"v_mov_b32_e32 v252, v253\n"
"v_mfma_f32_16x16x32_bf16 v[144:147], v[64:67], v[52:55], v[144:147]\n"
"s_nop 0\n"
"v_permlane16_swap_b32_e32 v253, v252\n"
"v_cvt_pk_bf16_f32 v251, -v253, 0\n"
"v_mfma_f32_16x16x32_bf16 v[172:175], v[68:71], v[52:55], v[172:175]\n"
"v_fma_f32 v252, -v253, v253, v252\n"
"v_lshlrev_b32_e32 v251, 16, v251\n"
"v_mfma_f32_16x16x32_bf16 v[76:79], v[88:91], v[52:55], v[76:79]\n"
"buffer_load_dwordx4 v[112:115], v1, s[4:7], s98 offen offset:2048 sc1\n"
"v_mfma_f32_16x16x32_bf16 v[184:187], v[160:163], v[52:55], v[184:187]\n"
"buffer_load_dwordx4 a[144:147], v1, s[4:7], s96 offen sc1\n"
"v_mfma_f32_16x16x32_bf16 v[36:39], v[92:95], v[140:143], v[36:39]\n"
"ds_read_b128 v[52:55], v4\n"
"ds_read_b128 v[116:119], v9\n"
"v_mfma_f32_16x16x32_bf16 v[128:131], v[180:183], v[140:143], v[128:131]\n"
"ds_read_b128 v[120:123], v4 offset:1024\n"
"ds_read_b128 v[164:167], v9 offset:64\n"
"v_mfma_f32_16x16x32_bf16 v[144:147], v[100:103], v[140:143], v[144:147]\n"
"ds_read_b128 v[168:171], v4 offset:2048\n"
"ds_read_b128 v[188:191], v9 offset:128\n"
"v_mfma_f32_16x16x32_bf16 v[172:175], v[148:151], v[140:143], v[172:175]\n"
"v_max_f32_e32 v252, 0, v252\n"
"v_cvt_pk_bf16_f32 v252, v252, 0\n"
"v_mfma_f32_16x16x32_bf16 v[76:79], v[192:195], v[140:143], v[76:79]\n"
"buffer_load_dwordx4 v[196:199], v1, s[4:7], s98 offen offset:3072 sc1\n"
"v_mfma_f32_16x16x32_bf16 v[184:187], v[56:59], v[140:143], v[184:187]\n"
"v_cmp_u_f32_e32 vcc, v36, v36\n"
"v_lshlrev_b32_e32 v252, 16, v252\n"
"; @A G1 1.2\n"
"v_mfma_f32_16x16x32_bf16 v[140:143], a[0:3], v[216:219], 0\n"
"s_cbranch_vccnz .Lmk_p96s\n"
".Lmk_p96r:\n"
"v_add_f32_e32 v252, 0x3727c5ac, v252\n"
"v_mfma_f32_16x16x32_bf16 v[200:203], a[4:7], v[216:219], 0\n"
"s_waitcnt vmcnt(10)\n"
"v_cmp_ne_u64_e32 vcc, v[254:255], v[18:19]\n"
"v_mfma_f32_16x16x32_bf16 v[204:207], a[8:11], v[216:219], 0\n"
"s_cbranch_vccnz .Lmk_f85s\n"
".Lmk_f85r:\n"
"s_waitcnt lgkmcnt(0)\n"
"v_mfma_f32_16x16x32_bf16 v[208:211], a[12:15], v[216:219], 0\n"
"ds_write_b128 v3, v[36:39] offset:6144\n"
"v_mfma_f32_16x16x32_bf16 v[36:39], a[16:19], v[216:219], 0\n"
"s_add_u32 s98, s52, 0x8000\n"
"buffer_load_dwordx4 v[212:215], v0, s[4:7], s98 offen sc1\n"
"v_mfma_f32_16x16x32_bf16 v[220:223], a[20:23], v[216:219], 0\n"
"v_fmac_f32_e32 v52, v251, v116\n"
"v_fmac_f32_e32 v53, v251, v117\n"
"v_mfma_f32_16x16x32_bf16 v[140:143], a[24:27], v[124:127], v[140:143]\n"
"ds_write_b128 v3, v[128:131] offset:7168\n"
"v_mfma_f32_16x16x32_bf16 v[200:203], a[28:31], v[124:127], v[200:203]\n"
"v_fmac_f32_e32 v54, v251, v118\n"
"v_fmac_f32_e32 v55, v251, v119\n"
"v_mfma_f32_16x16x32_bf16 v[204:207], a[32:35], v[124:127], v[204:207]\n"
"v_fmac_f32_e32 v120, v251, v164\n"
"v_fmac_f32_e32 v121, v251, v165\n"
"v_mfma_f32_16x16x32_bf16 v[208:211], a[36:39], v[124:127], v[208:211]\n"
"ds_write_b128 v3, v[144:147] offset:8192\n"
"v_mfma_f32_16x16x32_bf16 v[36:39], a[40:43], v[124:127], v[36:39]\n"
"buffer_load_dwordx4 v[116:119], v0, s[4:7], s98 offen offset:1024 sc1\n"
"v_mfma_f32_16x16x32_bf16 v[220:223], a[44:47], v[124:127], v[220:223]\n"
"v_fmac_f32_e32 v122, v251, v166\n"
"v_fmac_f32_e32 v123, v251, v167\n"
"v_mfma_f32_16x16x32_bf16 v[140:143], a[48:51], v[84:87], v[140:143]\n"
"ds_write_b128 v3, v[172:175] offset:9216\n"
"v_mfma_f32_16x16x32_bf16 v[200:203], a[52:55], v[84:87], v[200:203]\n"
"buffer_load_dwordx4 a[148:151], v1, s[4:7], s96 offen offset:1024 sc1\n"
"v_mfma_f32_16x16x32_bf16 v[204:207], a[56:59], v[84:87], v[204:207]\n"
"v_fmac_f32_e32 v168, v251, v188\n"
"v_fmac_f32_e32 v169, v251, v189\n"
"v_mfma_f32_16x16x32_bf16 v[208:211], a[60:63], v[84:87], v[208:211]\n"
"ds_write_b128 v3, v[76:79] offset:10240\n"
"v_mfma_f32_16x16x32_bf16 v[36:39], a[64:67], v[84:87], v[36:39]\n"
"buffer_load_dwordx4 v[76:79], v0, s[4:7], s98 offen offset:2048 sc1\n"
"v_mfma_f32_16x16x32_bf16 v[220:223], a[68:71], v[84:87], v[220:223]\n"
"v_fmac_f32_e32 v170, v251, v190\n"
"v_fmac_f32_e32 v171, v251, v191\n"
"v_mfma_f32_16x16x32_bf16 v[140:143], a[72:75], v[136:139], v[140:143]\n"
"ds_write_b128 v3, v[184:187] offset:11264\n"
"v_mfma_f32_16x16x32_bf16 v[200:203], a[76:79], v[136:139], v[200:203]\n"
"ds_read_b128 v[84:87], v4 offset:3072\n"
"ds_read_b128 v[124:127], v9 offset:192\n"
"v_mfma_f32_16x16x32_bf16 v[204:207], a[80:83], v[136:139], v[204:207]\n"
"ds_read_b128 v[128:131], v4 offset:4096\n"
"ds_read_b128 v[144:147], v9 offset:256\n"
"v_mfma_f32_16x16x32_bf16 v[208:211], a[84:87], v[136:139], v[208:211]\n"
"ds_read_b128 v[164:167], v4 offset:5120\n"
"ds_read_b128 v[172:175], v9 offset:320\n"
"v_mfma_f32_16x16x32_bf16 v[36:39], a[88:91], v[136:139], v[36:39]\n"
"buffer_load_dwordx4 v[184:187], v0, s[4:7], s98 offen offset:3072 sc1\n"
"v_mfma_f32_16x16x32_bf16 v[220:223], a[92:95], v[136:139], v[220:223]\n"
"v_cvt_pk_bf16_f32 v252, v252, 0\n"
"v_lshlrev_b32_e32 v252, 16, v252\n"
"v_mfma_f32_16x16x32_bf16 v[140:143], a[96:99], v[32:35], v[140:143]\n"
"v_sqrt_f32_e32 v252, v252\n"
"v_mfma_f32_16x16x32_bf16 v[200:203], a[100:103], v[32:35], v[200:203]\n"
"v_cvt_pk_bf16_f32 v252, v252, 0\n"
"v_lshlrev_b32_e32 v252, 16, v252\n"
"v_mfma_f32_16x16x32_bf16 v[204:207], a[104:107], v[32:35], v[204:207]\n"
"s_waitcnt lgkmcnt(0)\n"
"v_fmac_f32_e32 v84, v251, v124\n"
"v_fmac_f32_e32 v85, v251, v125\n"
"v_mfma_f32_16x16x32_bf16 v[208:211], a[108:111], v[32:35], v[208:211]\n"
"v_fmac_f32_e32 v86, v251, v126\n"
"v_fmac_f32_e32 v87, v251, v127\n"
"v_mfma_f32_16x16x32_bf16 v[36:39], a[112:115], v[32:35], v[36:39]\n"
"buffer_load_dwordx4 v[124:127], v1, s[4:7], s98 offen sc1\n"
"v_mfma_f32_16x16x32_bf16 v[220:223], a[116:119], v[32:35], v[220:223]\n"
"v_fmac_f32_e32 v128, v251, v144\n"
"v_fmac_f32_e32 v129, v251, v145\n"
"v_mfma_f32_16x16x32_bf16 v[140:143], a[120:123], v[44:47], v[140:143]\n"
"v_fmac_f32_e32 v130, v251, v146\n"
"v_fmac_f32_e32 v131, v251, v147\n"
"v_mfma_f32_16x16x32_bf16 v[200:203], a[124:127], v[44:47], v[200:203]\n"
"v_fmac_f32_e32 v164, v251, v172\n"
"v_fmac_f32_e32 v165, v251, v173\n"
"v_mfma_f32_16x16x32_bf16 v[204:207], v[28:31], v[44:47], v[204:207]\n"
"v_fmac_f32_e32 v166, v251, v174\n"
"v_fmac_f32_e32 v167, v251, v175\n"
"v_mfma_f32_16x16x32_bf16 v[208:211], v[40:43], v[44:47], v[208:211]\n"
"buffer_load_dwordx4 a[152:155], v1, s[4:7], s96 offen offset:2048 sc1\n"
"v_mfma_f32_16x16x32_bf16 v[36:39], v[60:63], v[44:47], v[36:39]\n"
"buffer_load_dwordx4 v[32:35], v1, s[4:7], s98 offen offset:1024 sc1\n"
"v_mfma_f32_16x16x32_bf16 v[220:223], v[72:75], v[44:47], v[220:223]\n"
"v_rcp_f32_e32 v255, v252\n"
"v_mfma_f32_16x16x32_bf16 v[140:143], v[156:159], v[104:107], v[140:143]\n"
"v_mul_f32_e32 v254, 0x3e8293ee, v255\n"
"v_mfma_f32_16x16x32_bf16 v[200:203], v[132:135], v[104:107], v[200:203]\n"
"v_mfma_f32_16x16x32_bf16 v[204:207], v[64:67], v[104:107], v[204:207]\n"
"v_mfma_f32_16x16x32_bf16 v[208:211], v[68:71], v[104:107], v[208:211]\n"
"v_mfma_f32_16x16x32_bf16 v[36:39], v[88:91], v[104:107], v[36:39]\n"
"v_max_u32_e32 v253, v176, v177\n"
"v_max3_u32 v253, v253, v178, v179\n"
"v_mfma_f32_16x16x32_bf16 v[220:223], v[160:163], v[104:107], v[220:223]\n"
"s_waitcnt vmcnt(14)\n"
"v_max3_u32 v253, v253, v152, v153\n"
"v_max3_u32 v253, v253, v154, v155\n"
"v_mfma_f32_16x16x32_bf16 v[140:143], v[92:95], v[24:27], v[140:143]\n"
"v_max3_u32 v253, v253, v96, v97\n"
"v_max3_u32 v253, v253, v98, v99\n"
"v_mfma_f32_16x16x32_bf16 v[200:203], v[180:183], v[24:27], v[200:203]\n"
"v_max3_u32 v253, v253, v48, v49\n"
"v_max3_u32 v253, v253, v50, v51\n"
"v_mfma_f32_16x16x32_bf16 v[204:207], v[100:103], v[24:27], v[204:207]\n"
"v_cmp_eq_u32_e32 vcc, -1, v253\n"
"v_mfma_f32_16x16x32_bf16 v[208:211], v[148:151], v[24:27], v[208:211]\n"
"s_cbranch_vccnz .Lmk_q98s\n"
".Lmk_q98r:\n"
"v_mfma_f32_16x16x32_bf16 v[36:39], v[192:195], v[24:27], v[36:39]\n"
"buffer_load_dwordx4 v[44:47], v1, s[4:7], s98 offen offset:2048 sc1\n"
"v_mfma_f32_16x16x32_bf16 v[220:223], v[56:59], v[24:27], v[220:223]\n"
"v_cmp_u_f32_e32 vcc, v140, v140\n"
"; @A G1 1.3\n"
"v_mfma_f32_16x16x32_bf16 v[52:55], a[0:3], v[176:179], v[52:55]\n"
"s_cbranch_vccnz .Lmk_p97s\n"
".Lmk_p97r:\n"
"v_mfma_f32_16x16x32_bf16 v[120:123], a[4:7], v[176:179], v[120:123]\n"
"buffer_load_dwordx4 v[24:27], v1, s[4:7], s98 offen offset:3072 sc1\n"
"v_mfma_f32_16x16x32_bf16 v[168:171], a[8:11], v[176:179], v[168:171]\n"
"v_mfma_f32_16x16x32_bf16 v[84:87], a[12:15], v[176:179], v[84:87]\n"
"ds_write_b128 v3, v[140:143] offset:12288\n"
"v_mfma_f32_16x16x32_bf16 v[128:131], a[16:19], v[176:179], v[128:131]\n"
"s_add_u32 s98, s53, 0x8000\n"
"buffer_load_dwordx4 v[104:107], v0, s[4:7], s98 offen sc1\n"
"v_mfma_f32_16x16x32_bf16 v[164:167], a[20:23], v[176:179], v[164:167]\n"
"buffer_load_dwordx4 a[156:159], v1, s[4:7], s96 offen offset:3072 sc1\n"
"v_mfma_f32_16x16x32_bf16 v[52:55], a[24:27], v[152:155], v[52:55]\n"
"ds_write_b128 v3, v[200:203] offset:13312\n"
"v_mfma_f32_16x16x32_bf16 v[120:123], a[28:31], v[152:155], v[120:123]\n"
"v_mfma_f32_16x16x32_bf16 v[168:171], a[32:35], v[152:155], v[168:171]\n"
"v_mfma_f32_16x16x32_bf16 v[84:87], a[36:39], v[152:155], v[84:87]\n"
"ds_write_b128 v3, v[204:207] offset:14336\n"
"v_mfma_f32_16x16x32_bf16 v[128:131], a[40:43], v[152:155], v[128:131]\n"
"buffer_load_dwordx4 v[136:139], v0, s[4:7], s98 offen offset:1024 sc1\n"
"v_mfma_f32_16x16x32_bf16 v[164:167], a[44:47], v[152:155], v[164:167]\n"
"v_mfma_f32_16x16x32_bf16 v[52:55], a[48:51], v[96:99], v[52:55]\n"
"ds_write_b128 v3, v[208:211] offset:15360\n"
"v_mfma_f32_16x16x32_bf16 v[120:123], a[52:55], v[96:99], v[120:123]\n"
"v_mfma_f32_16x16x32_bf16 v[168:171], a[56:59], v[96:99], v[168:171]\n"
"v_mfma_f32_16x16x32_bf16 v[84:87], a[60:63], v[96:99], v[84:87]\n"
"ds_write_b128 v3, v[36:39] offset:16384\n"
"v_mfma_f32_16x16x32_bf16 v[128:131], a[64:67], v[96:99], v[128:131]\n"
"s_waitcnt vmcnt(16)\n"
"v_max_u32_e32 v253, v80, v81\n"
"v_max3_u32 v253, v253, v82, v83\n"
"v_mfma_f32_16x16x32_bf16 v[164:167], a[68:71], v[96:99], v[164:167]\n"
"s_waitcnt vmcnt(13)\n"
"v_max3_u32 v253, v253, v108, v109\n"
"v_max3_u32 v253, v253, v110, v111\n"
"v_mfma_f32_16x16x32_bf16 v[52:55], a[72:75], v[48:51], v[52:55]\n"
"v_max3_u32 v253, v253, v112, v113\n"
"v_max3_u32 v253, v253, v114, v115\n"
"v_mfma_f32_16x16x32_bf16 v[120:123], a[76:79], v[48:51], v[120:123]\n"
"v_max3_u32 v253, v253, v196, v197\n"
"v_max3_u32 v253, v253, v198, v199\n"
"v_mfma_f32_16x16x32_bf16 v[168:171], a[80:83], v[48:51], v[168:171]\n"
"v_cmp_eq_u32_e32 vcc, -1, v253\n"
"v_mfma_f32_16x16x32_bf16 v[84:87], a[84:87], v[48:51], v[84:87]\n"
"s_cbranch_vccnz .Lmk_q99s\n"
".Lmk_q99r:\n"
"v_mfma_f32_16x16x32_bf16 v[128:131], a[88:91], v[48:51], v[128:131]\n"
"buffer_load_dwordx4 v[36:39], v0, s[4:7], s98 offen offset:2048 sc1\n"
"v_mfma_f32_16x16x32_bf16 v[164:167], a[92:95], v[48:51], v[164:167]\n"
"buffer_load_dwordx4 v[48:51], v0, s[4:7], s98 offen offset:3072 sc1\n"
"v_mfma_f32_16x16x32_bf16 v[52:55], a[96:99], v[80:83], v[52:55]\n"
"ds_write_b128 v3, v[220:223] offset:17408\n"
"v_mfma_f32_16x16x32_bf16 v[120:123], a[100:103], v[80:83], v[120:123]\n"
"s_add_u32 s97, s70, 0x2000\n"
"buffer_load_dwordx4 a[160:163], v0, s[4:7], s97 offen sc1\n"
"v_mfma_f32_16x16x32_bf16 v[168:171], a[104:107], v[80:83], v[168:171]\n"
"v_mfma_f32_16x16x32_bf16 v[84:87], a[108:111], v[80:83], v[84:87]\n"
"s_waitcnt lgkmcnt(0)\n"
"s_barrier\n"
"v_mfma_f32_16x16x32_bf16 v[128:131], a[112:115], v[80:83], v[128:131]\n"
"buffer_load_dwordx4 v[96:99], v1, s[4:7], s98 offen sc1\n"
"v_mfma_f32_16x16x32_bf16 v[164:167], a[116:119], v[80:83], v[164:167]\n"
"ds_read_b128 v[80:83], v5 offset:2048\n"
"ds_read_b128 v[140:143], v6 offset:2048\n"
"v_mfma_f32_16x16x32_bf16 v[52:55], a[120:123], v[108:111], v[52:55]\n"
"ds_read_b128 v[144:147], v5 offset:3072\n"
"ds_read_b128 v[152:155], v6 offset:3072\n"
"v_mfma_f32_16x16x32_bf16 v[120:123], a[124:127], v[108:111], v[120:123]\n"
"v_mfma_f32_16x16x32_bf16 v[168:171], v[28:31], v[108:111], v[168:171]\n"
"v_mfma_f32_16x16x32_bf16 v[84:87], v[40:43], v[108:111], v[84:87]\n"
"ds_read_b128 v[172:175], v5 offset:4096\n"
"ds_read_b128 v[176:179], v6 offset:4096\n"
"v_mfma_f32_16x16x32_bf16 v[128:131], v[60:63], v[108:111], v[128:131]\n"
"buffer_load_dwordx4 v[188:191], v1, s[4:7], s98 offen offset:1024 sc1\n"
"v_mfma_f32_16x16x32_bf16 v[164:167], v[72:75], v[108:111], v[164:167]\n"
"ds_read_b128 v[108:111], v5 offset:5120\n"
"ds_read_b128 v[200:203], v6 offset:5120\n"
"v_mfma_f32_16x16x32_bf16 v[52:55], v[156:159], v[112:115], v[52:55]\n"
"s_waitcnt lgkmcnt(4)\n"
"v_add_f32_e32 v80, v80, v140\n"
"v_add_f32_e32 v81, v81, v141\n"
"v_mfma_f32_16x16x32_bf16 v[120:123], v[132:135], v[112:115], v[120:123]\n"
"v_add_f32_e32 v82, v82, v142\n"
"v_add_f32_e32 v83, v83, v143\n"
"v_mfma_f32_16x16x32_bf16 v[168:171], v[64:67], v[112:115], v[168:171]\n"
"ds_read_b128 v[140:143], v5\n"
"ds_read_b128 v[204:207], v6\n"
"v_mfma_f32_16x16x32_bf16 v[84:87], v[68:71], v[112:115], v[84:87]\n"
"ds_read_b128 v[208:211], v5 offset:1024\n"
"ds_read_b128 v[216:219], v6 offset:1024\n"
"v_mfma_f32_16x16x32_bf16 v[128:131], v[88:91], v[112:115], v[128:131]\n"
"buffer_load_dwordx4 v[220:223], v1, s[4:7], s98 offen offset:2048 sc1\n"
"v_mfma_f32_16x16x32_bf16 v[164:167], v[160:163], v[112:115], v[164:167]\n"
"s_waitcnt lgkmcnt(4)\n"
"v_add_f32_e32 v144, v144, v152\n"
"v_add_f32_e32 v145, v145, v153\n"
"v_mfma_f32_16x16x32_bf16 v[52:55], v[92:95], v[196:199], v[52:55]\n"
"v_add_f32_e32 v146, v146, v154\n"
"v_add_f32_e32 v147, v147, v155\n"
"v_mfma_f32_16x16x32_bf16 v[120:123], v[180:183], v[196:199], v[120:123]\n"
"v_add_f32_e32 v172, v172, v176\n"
"v_add_f32_e32 v173, v173, v177\n"
"v_mfma_f32_16x16x32_bf16 v[168:171], v[100:103], v[196:199], v[168:171]\n"
"s_waitcnt lgkmcnt(0)\n"
"v_add_f32_e32 v174, v174, v178\n"
"v_add_f32_e32 v175, v175, v179\n"
"v_mfma_f32_16x16x32_bf16 v[84:87], v[148:151], v[196:199], v[84:87]\n"
"v_add_f32_e32 v108, v108, v200\n"
"v_add_f32_e32 v109, v109, v201\n"
"v_mfma_f32_16x16x32_bf16 v[128:131], v[192:195], v[196:199], v[128:131]\n"
"buffer_load_dwordx4 v[112:115], v1, s[4:7], s98 offen offset:3072 sc1\n"
"v_mfma_f32_16x16x32_bf16 v[164:167], v[56:59], v[196:199], v[164:167]\n"
"v_add_f32_e32 v110, v110, v202\n"
"v_add_f32_e32 v111, v111, v203\n"
"v_add_f32_e32 v140, v140, v204\n"
"v_add_f32_e32 v141, v141, v205\n"
"v_add_f32_e32 v142, v142, v206\n"
"v_add_f32_e32 v143, v143, v207\n"
"v_add_f32_e32 v208, v208, v216\n"
"v_add_f32_e32 v209, v209, v217\n"
"v_add_f32_e32 v210, v210, v218\n"
"v_add_f32_e32 v211, v211, v219\n"
".Lmk_a2:\n"
"s_waitcnt vmcnt(18)\n"
"; @A G1 2.0\n"
"v_mfma_f32_16x16x32_bf16 v[152:155], a[0:3], v[212:215], 0\n"
"v_mfma_f32_16x16x32_bf16 v[176:179], a[4:7], v[212:215], 0\n"
"s_add_u32 s98, s62, 0x800\n"
"v_mfma_f32_16x16x32_bf16 v[196:199], a[8:11], v[212:215], 0\n"
"v_mfma_f32_16x16x32_bf16 v[200:203], a[12:15], v[212:215], 0\n"
"v_add_f32_e32 v168, v168, v80\n"
"v_add_f32_e32 v169, v169, v81\n"
"v_mfma_f32_16x16x32_bf16 v[204:207], a[16:19], v[212:215], 0\n"
"s_add_u32 s96, s54, 0x8000\n"
"buffer_load_dwordx4 v[216:219], v0, s[4:7], s96 offen sc1\n"
"v_mfma_f32_16x16x32_bf16 v[224:227], a[20:23], v[212:215], 0\n"
"buffer_load_dword v253, v10, s[4:7], s98 offen sc1\n"
"v_mfma_f32_16x16x32_bf16 v[152:155], a[24:27], v[116:119], v[152:155]\n"
"v_add_f32_e32 v170, v170, v82\n"
"v_add_f32_e32 v171, v171, v83\n"
"v_mfma_f32_16x16x32_bf16 v[176:179], a[28:31], v[116:119], v[176:179]\n"
"v_add_f32_e32 v84, v84, v144\n"
"v_add_f32_e32 v85, v85, v145\n"
"v_mfma_f32_16x16x32_bf16 v[196:199], a[32:35], v[116:119], v[196:199]\n"
"buffer_load_dwordx4 a[164:167], v0, s[4:7], s97 offen offset:1024 sc1\n"
"v_mfma_f32_16x16x32_bf16 v[200:203], a[36:39], v[116:119], v[200:203]\n"
"buffer_load_dword v252, v10, s[4:7], s98 offen offset:256 sc1\n"
"v_mfma_f32_16x16x32_bf16 v[204:207], a[40:43], v[116:119], v[204:207]\n"
"buffer_load_dwordx4 v[80:83], v0, s[4:7], s96 offen offset:1024 sc1\n"
"v_mfma_f32_16x16x32_bf16 v[224:227], a[44:47], v[116:119], v[224:227]\n"
"s_waitcnt vmcnt(20)\n"
"v_add_f32_e32 v86, v86, v146\n"
"v_add_f32_e32 v87, v87, v147\n"
"v_mfma_f32_16x16x32_bf16 v[152:155], a[48:51], v[76:79], v[152:155]\n"
"buffer_load_dword v251, v10, s[4:7], s98 offen offset:512 sc1\n"
"v_mfma_f32_16x16x32_bf16 v[176:179], a[52:55], v[76:79], v[176:179]\n"
"v_mul_f32_e32 v168, v168, v255\n"
"v_mul_f32_e32 v169, v169, v255\n"
"v_mfma_f32_16x16x32_bf16 v[196:199], a[56:59], v[76:79], v[196:199]\n"
"v_mul_f32_e32 v170, v170, v255\n"
"v_mul_f32_e32 v171, v171, v255\n"
"v_mfma_f32_16x16x32_bf16 v[200:203], a[60:63], v[76:79], v[200:203]\n"
"buffer_load_dword v250, v10, s[4:7], s98 offen offset:768 sc1\n"
"v_mfma_f32_16x16x32_bf16 v[204:207], a[64:67], v[76:79], v[204:207]\n"
"buffer_load_dwordx4 v[116:119], v0, s[4:7], s96 offen offset:2048 sc1\n"
"v_mfma_f32_16x16x32_bf16 v[224:227], a[68:71], v[76:79], v[224:227]\n"
"v_mul_f32_e32 v84, v84, v255\n"
"v_mul_f32_e32 v85, v85, v255\n"
"v_mfma_f32_16x16x32_bf16 v[152:155], a[72:75], v[184:187], v[152:155]\n"
"buffer_load_dword v249, v10, s[4:7], s98 offen offset:1024 sc1\n"
"v_mfma_f32_16x16x32_bf16 v[176:179], a[76:79], v[184:187], v[176:179]\n"
"v_mul_f32_e32 v86, v86, v255\n"
"v_mul_f32_e32 v87, v87, v255\n"
"v_mfma_f32_16x16x32_bf16 v[196:199], a[80:83], v[184:187], v[196:199]\n"
"v_cvt_pk_bf16_f32 v76, v168, v169\n"
"v_cvt_pk_bf16_f32 v77, v170, v171\n"
"v_mfma_f32_16x16x32_bf16 v[200:203], a[84:87], v[184:187], v[200:203]\n"
"buffer_load_dword v248, v10, s[4:7], s98 offen offset:1280 sc1\n"
"v_mfma_f32_16x16x32_bf16 v[204:207], a[88:91], v[184:187], v[204:207]\n"
"buffer_load_dwordx4 v[144:147], v0, s[4:7], s96 offen offset:3072 sc1\n"
"v_mfma_f32_16x16x32_bf16 v[224:227], a[92:95], v[184:187], v[224:227]\n"
"s_waitcnt vmcnt(23)\n"
"v_cvt_pk_bf16_f32 v78, v84, v85\n"
"v_cvt_pk_bf16_f32 v79, v86, v87\n"
"v_mfma_f32_16x16x32_bf16 v[152:155], a[96:99], v[124:127], v[152:155]\n"
"buffer_load_dword v247, v10, s[4:7], s98 offen offset:1536 sc1\n"
"v_mfma_f32_16x16x32_bf16 v[176:179], a[100:103], v[124:127], v[176:179]\n"
"ds_write_b128 v8, v[76:79] offset:25600\n"
"v_mfma_f32_16x16x32_bf16 v[196:199], a[104:107], v[124:127], v[196:199]\n"
"s_add_u32 s95, s64, 0x2000\n"
"buffer_store_dwordx4 v[76:79], v8, s[4:7], s95 offen offset:1024 sc1\n"
"v_mfma_f32_16x16x32_bf16 v[200:203], a[108:111], v[124:127], v[200:203]\n"
"buffer_load_dword v246, v10, s[4:7], s98 offen offset:1792 sc1\n"
"v_mfma_f32_16x16x32_bf16 v[204:207], a[112:115], v[124:127], v[204:207]\n"
"buffer_load_dwordx4 v[76:79], v1, s[4:7], s96 offen sc1\n"
"v_mfma_f32_16x16x32_bf16 v[224:227], a[116:119], v[124:127], v[224:227]\n"
"buffer_load_dwordx4 a[168:171], v0, s[4:7], s97 offen offset:2048 sc1\n"
"v_mfma_f32_16x16x32_bf16 v[152:155], a[120:123], v[32:35], v[152:155]\n"
"v_add_f32_e32 v128, v128, v172\n"
"v_add_f32_e32 v129, v129, v173\n"
"v_mfma_f32_16x16x32_bf16 v[176:179], a[124:127], v[32:35], v[176:179]\n"
"v_add_f32_e32 v130, v130, v174\n"
"v_add_f32_e32 v131, v131, v175\n"
"v_mfma_f32_16x16x32_bf16 v[196:199], v[28:31], v[32:35], v[196:199]\n"
"v_add_f32_e32 v164, v164, v108\n"
"v_add_f32_e32 v165, v165, v109\n"
"v_mfma_f32_16x16x32_bf16 v[200:203], v[40:43], v[32:35], v[200:203]\n"
"v_add_f32_e32 v166, v166, v110\n"
"v_add_f32_e32 v167, v167, v111\n"
"v_mfma_f32_16x16x32_bf16 v[204:207], v[60:63], v[32:35], v[204:207]\n"
"buffer_load_dwordx4 v[84:87], v1, s[4:7], s96 offen offset:1024 sc1\n"
"v_mfma_f32_16x16x32_bf16 v[224:227], v[72:75], v[32:35], v[224:227]\n"
"s_waitcnt vmcnt(27)\n"
"v_mul_f32_e32 v128, v128, v255\n"
"v_mul_f32_e32 v129, v129, v255\n"
"v_mfma_f32_16x16x32_bf16 v[152:155], v[156:159], v[44:47], v[152:155]\n"
"v_mul_f32_e32 v130, v130, v255\n"
"v_mul_f32_e32 v131, v131, v255\n"
"v_mfma_f32_16x16x32_bf16 v[176:179], v[132:135], v[44:47], v[176:179]\n"
"v_mul_f32_e32 v164, v164, v255\n"
"v_mul_f32_e32 v165, v165, v255\n"
"v_mfma_f32_16x16x32_bf16 v[196:199], v[64:67], v[44:47], v[196:199]\n"
"v_mul_f32_e32 v166, v166, v255\n"
"v_mul_f32_e32 v167, v167, v255\n"
"v_mfma_f32_16x16x32_bf16 v[200:203], v[68:71], v[44:47], v[200:203]\n"
"v_cvt_pk_bf16_f32 v32, v128, v129\n"
"v_cvt_pk_bf16_f32 v33, v130, v131\n"
"v_mfma_f32_16x16x32_bf16 v[204:207], v[88:91], v[44:47], v[204:207]\n"
"buffer_load_dwordx4 v[108:111], v1, s[4:7], s96 offen offset:2048 sc1\n"
"v_mfma_f32_16x16x32_bf16 v[224:227], v[160:163], v[44:47], v[224:227]\n"
"v_cvt_pk_bf16_f32 v34, v164, v165\n"
"v_cvt_pk_bf16_f32 v35, v166, v167\n"
"v_mfma_f32_16x16x32_bf16 v[152:155], v[92:95], v[24:27], v[152:155]\n"
"ds_write_b128 v7, v[32:35] offset:58368\n"
"v_mfma_f32_16x16x32_bf16 v[176:179], v[180:183], v[24:27], v[176:179]\n"
"s_add_u32 s98, s64, 0x6000\n"
"buffer_store_dwordx4 v[32:35], v7, s[4:7], s98 offen offset:1024 sc1\n"
"v_mfma_f32_16x16x32_bf16 v[196:199], v[100:103], v[24:27], v[196:199]\n"
"v_add_f32_e32 v52, v52, v140\n"
"v_add_f32_e32 v53, v53, v141\n"
"v_mfma_f32_16x16x32_bf16 v[200:203], v[148:151], v[24:27], v[200:203]\n"
"v_add_f32_e32 v54, v54, v142\n"
"v_add_f32_e32 v55, v55, v143\n"
"v_mfma_f32_16x16x32_bf16 v[204:207], v[192:195], v[24:27], v[204:207]\n"
"buffer_load_dwordx4 v[32:35], v1, s[4:7], s96 offen offset:3072 sc1\n"
"v_mfma_f32_16x16x32_bf16 v[224:227], v[56:59], v[24:27], v[224:227]\n"
"s_waitcnt vmcnt(27)\n"
"v_add_f32_e32 v120, v120, v208\n"
"v_add_f32_e32 v121, v121, v209\n"
"; @A G1 2.1\n"
"v_mfma_f32_16x16x32_bf16 v[24:27], a[0:3], v[104:107], 0\n"
"v_add_f32_e32 v122, v122, v210\n"
"v_add_f32_e32 v123, v123, v211\n"
"v_mfma_f32_16x16x32_bf16 v[44:47], a[4:7], v[104:107], 0\n"
"buffer_load_dwordx4 a[172:175], v0, s[4:7], s97 offen offset:3072 sc1\n"
"v_mfma_f32_16x16x32_bf16 v[124:127], a[8:11], v[104:107], 0\n"
"v_mul_f32_e32 v52, v52, v254\n"
"v_mul_f32_e32 v53, v53, v254\n"
"v_mfma_f32_16x16x32_bf16 v[128:131], a[12:15], v[104:107], 0\n"
"ds_write_b128 v3, v[152:155]\n"
"v_mfma_f32_16x16x32_bf16 v[140:143], a[16:19], v[104:107], 0\n"
"s_add_u32 s98, s55, 0x8000\n"
"buffer_load_dwordx4 v[152:155], v0, s[4:7], s98 offen sc1\n"
"v_mfma_f32_16x16x32_bf16 v[164:167], a[20:23], v[104:107], 0\n"
"v_mul_f32_e32 v54, v54, v254\n"
"v_mul_f32_e32 v55, v55, v254\n"
"v_mfma_f32_16x16x32_bf16 v[24:27], a[24:27], v[136:139], v[24:27]\n"
"ds_write_b128 v3, v[176:179] offset:1024\n"
"v_mfma_f32_16x16x32_bf16 v[44:47], a[28:31], v[136:139], v[44:47]\n"
"v_mul_f32_e32 v120, v120, v254\n"
"v_mul_f32_e32 v121, v121, v254\n"
"v_mfma_f32_16x16x32_bf16 v[124:127], a[32:35], v[136:139], v[124:127]\n"
"v_mul_f32_e32 v122, v122, v254\n"
"v_mul_f32_e32 v123, v123, v254\n"
"v_mfma_f32_16x16x32_bf16 v[128:131], a[36:39], v[136:139], v[128:131]\n"
"ds_write_b128 v3, v[196:199] offset:2048\n"
"v_mfma_f32_16x16x32_bf16 v[140:143], a[40:43], v[136:139], v[140:143]\n"
"buffer_load_dwordx4 v[104:107], v0, s[4:7], s98 offen offset:1024 sc1\n"
"v_mfma_f32_16x16x32_bf16 v[164:167], a[44:47], v[136:139], v[164:167]\n"
"s_waitcnt vmcnt(28)\n"
"v_cvt_pk_bf16_f32 v136, v52, v53\n"
"v_cvt_pk_bf16_f32 v137, v54, v55\n"
"v_mfma_f32_16x16x32_bf16 v[24:27], a[48:51], v[36:39], v[24:27]\n"
"ds_write_b128 v3, v[200:203] offset:3072\n"
"v_mfma_f32_16x16x32_bf16 v[44:47], a[52:55], v[36:39], v[44:47]\n"
"v_cvt_pk_bf16_f32 v138, v120, v121\n"
"v_cvt_pk_bf16_f32 v139, v122, v123\n"
"v_mfma_f32_16x16x32_bf16 v[124:127], a[56:59], v[36:39], v[124:127]\n"
"ds_write_b128 v2, v[136:139] offset:50176\n"
"v_mfma_f32_16x16x32_bf16 v[128:131], a[60:63], v[36:39], v[128:131]\n"
"ds_write_b128 v3, v[204:207] offset:4096\n"
"v_mfma_f32_16x16x32_bf16 v[140:143], a[64:67], v[36:39], v[140:143]\n"
"buffer_load_dwordx4 v[52:55], v0, s[4:7], s98 offen offset:2048 sc1\n"
"v_mfma_f32_16x16x32_bf16 v[164:167], a[68:71], v[36:39], v[164:167]\n"
"v_mfma_f32_16x16x32_bf16 v[24:27], a[72:75], v[48:51], v[24:27]\n"
"ds_write_b128 v3, v[224:227] offset:5120\n"
"v_mfma_f32_16x16x32_bf16 v[44:47], a[76:79], v[48:51], v[44:47]\n"
"v_mfma_f32_16x16x32_bf16 v[124:127], a[80:83], v[48:51], v[124:127]\n"
"v_mfma_f32_16x16x32_bf16 v[128:131], a[84:87], v[48:51], v[128:131]\n"
"buffer_load_dwordx4 a[176:179], v1, s[4:7], s97 offen sc1\n"
"v_mfma_f32_16x16x32_bf16 v[140:143], a[88:91], v[48:51], v[140:143]\n"
"buffer_load_dwordx4 v[36:39], v0, s[4:7], s98 offen offset:3072 sc1\n"
"v_mfma_f32_16x16x32_bf16 v[164:167], a[92:95], v[48:51], v[164:167]\n"
"s_waitcnt vmcnt(28)\n"
"v_mfma_f32_16x16x32_bf16 v[24:27], a[96:99], v[96:99], v[24:27]\n"
"v_mfma_f32_16x16x32_bf16 v[44:47], a[100:103], v[96:99], v[44:47]\n"
"v_mfma_f32_16x16x32_bf16 v[124:127], a[104:107], v[96:99], v[124:127]\n"
"v_mfma_f32_16x16x32_bf16 v[128:131], a[108:111], v[96:99], v[128:131]\n"
"v_mfma_f32_16x16x32_bf16 v[140:143], a[112:115], v[96:99], v[140:143]\n"
"buffer_load_dwordx4 v[48:51], v1, s[4:7], s98 offen sc1\n"
"v_mfma_f32_16x16x32_bf16 v[164:167], a[116:119], v[96:99], v[164:167]\n"
"s_waitcnt lgkmcnt(0)\n"
"s_barrier\n"
"v_mfma_f32_16x16x32_bf16 v[24:27], a[120:123], v[188:191], v[24:27]\n"
"s_waitcnt vmcnt(13)\n"
"v_add_f32_e32 v253, v253, v249\n"
"v_add_f32_e32 v252, v252, v248\n"
"v_mfma_f32_16x16x32_bf16 v[44:47], a[124:127], v[188:191], v[44:47]\n"
"v_add_f32_e32 v251, v251, v247\n"
"v_add_f32_e32 v250, v250, v246\n"
"v_mfma_f32_16x16x32_bf16 v[124:127], v[28:31], v[188:191], v[124:127]\n"
"v_add_f32_e32 v253, v253, v251\n"
"v_add_f32_e32 v252, v252, v250\n"
"v_mfma_f32_16x16x32_bf16 v[128:131], v[40:43], v[188:191], v[128:131]\n"
"v_add_f32_e32 v253, v253, v252\n"
"v_mov_b32_e32 v252, v253\n"
"v_mfma_f32_16x16x32_bf16 v[140:143], v[60:63], v[188:191], v[140:143]\n"
"buffer_load_dwordx4 v[96:99], v1, s[4:7], s98 offen offset:1024 sc1\n"
"v_mfma_f32_16x16x32_bf16 v[164:167], v[72:75], v[188:191], v[164:167]\n"
"v_permlane32_swap_b32_e32 v253, v252\n"
"v_add_f32_e32 v253, v253, v252\n"
"v_mfma_f32_16x16x32_bf16 v[24:27], v[156:159], v[220:223], v[24:27]\n"
"v_mul_f32_e32 v253, 0x3a800000, v253\n"
"v_mov_b32_e32 v252, v253\n"
"v_mfma_f32_16x16x32_bf16 v[44:47], v[132:135], v[220:223], v[44:47]\n"
"s_nop 0\n"
"v_permlane16_swap_b32_e32 v253, v252\n"
"v_cvt_pk_bf16_f32 v255, -v253, 0\n"
"v_mfma_f32_16x16x32_bf16 v[124:127], v[64:67], v[220:223], v[124:127]\n"
"v_fma_f32 v252, -v253, v253, v252\n"
"v_lshlrev_b32_e32 v255, 16, v255\n"
"v_mfma_f32_16x16x32_bf16 v[128:131], v[68:71], v[220:223], v[128:131]\n"
"ds_read_b128 v[120:123], v4\n"
"ds_read_b128 v[136:139], v9\n"
"v_mfma_f32_16x16x32_bf16 v[140:143], v[88:91], v[220:223], v[140:143]\n"
"buffer_load_dwordx4 v[168:171], v1, s[4:7], s98 offen offset:2048 sc1\n"
"v_mfma_f32_16x16x32_bf16 v[164:167], v[160:163], v[220:223], v[164:167]\n"
"buffer_load_dwordx4 a[180:183], v1, s[4:7], s97 offen offset:1024 sc1\n"
"v_mfma_f32_16x16x32_bf16 v[24:27], v[92:95], v[112:115], v[24:27]\n"
"ds_read_b128 v[172:175], v4 offset:1024\n"
"ds_read_b128 v[176:179], v9 offset:64\n"
"v_mfma_f32_16x16x32_bf16 v[44:47], v[180:183], v[112:115], v[44:47]\n"
"ds_read_b128 v[184:187], v4 offset:2048\n"
"ds_read_b128 v[188:191], v9 offset:128\n"
"v_mfma_f32_16x16x32_bf16 v[124:127], v[100:103], v[112:115], v[124:127]\n"
"v_max_f32_e32 v252, 0, v252\n"
"v_cvt_pk_bf16_f32 v252, v252, 0\n"
"v_mfma_f32_16x16x32_bf16 v[128:131], v[148:151], v[112:115], v[128:131]\n"
"v_lshlrev_b32_e32 v252, 16, v252\n"
"v_add_f32_e32 v252, 0x3727c5ac, v252\n"
"v_mfma_f32_16x16x32_bf16 v[140:143], v[192:195], v[112:115], v[140:143]\n"
"buffer_load_dwordx4 v[196:199], v1, s[4:7], s98 offen offset:3072 sc1\n"
"v_mfma_f32_16x16x32_bf16 v[164:167], v[56:59], v[112:115], v[164:167]\n"
"v_cvt_pk_bf16_f32 v252, v252, 0\n"
"v_lshlrev_b32_e32 v252, 16, v252\n"
"; @A G1 2.2\n"
"v_mfma_f32_16x16x32_bf16 v[112:115], a[0:3], v[216:219], 0\n"
"s_waitcnt lgkmcnt(0)\n"
"v_fmac_f32_e32 v120, v255, v136\n"
"v_fmac_f32_e32 v121, v255, v137\n"
"v_mfma_f32_16x16x32_bf16 v[200:203], a[4:7], v[216:219], 0\n"
"v_fmac_f32_e32 v122, v255, v138\n"
"v_fmac_f32_e32 v123, v255, v139\n"
"v_mfma_f32_16x16x32_bf16 v[136:139], a[8:11], v[216:219], 0\n"
"v_fmac_f32_e32 v172, v255, v176\n"
"v_fmac_f32_e32 v173, v255, v177\n"
"v_mfma_f32_16x16x32_bf16 v[204:207], a[12:15], v[216:219], 0\n"
"ds_write_b128 v3, v[24:27] offset:6144\n"
"v_mfma_f32_16x16x32_bf16 v[24:27], a[16:19], v[216:219], 0\n"
"s_add_u32 s98, s52, 0\n"
"buffer_load_dwordx4 v[208:211], v0, s[4:7], s98 offen sc1\n"
"s_mov_b32 s96, .Lmk_a4-.Lmk_g101\n"
"v_mfma_f32_16x16x32_bf16 v[212:215], a[20:23], v[216:219], 0\n"
"s_getpc_b64 s[84:85]\n"
".Lmk_g101:\n"
"s_add_u32 s84, s84, s96\n"
"s_addc_u32 s85, s85, 0\n"
"s_and_b32 s84, s84, -16\n"
"v_mfma_f32_16x16x32_bf16 v[112:115], a[24:27], v[80:83], v[112:115]\n"
"ds_write_b128 v3, v[44:47] offset:7168\n"
"v_mfma_f32_16x16x32_bf16 v[200:203], a[28:31], v[80:83], v[200:203]\n"
"s_and_b32 s85, s85, 0xffff\n"
"s_sub_u32 s96, .Lmk_end-.Lmk_g101, s96\n"
"s_min_u32 s86, s96, 0x4000\n"
"s_mov_b32 s87, 0x20000\n"
"v_mfma_f32_16x16x32_bf16 v[136:139], a[32:35], v[80:83], v[136:139]\n"
"s_mov_b32 m0, 0x27800\n"
"s_nop 0\n"
"buffer_load_dwordx4 v0, s[84:87], s99 offen lds\n"
"v_mfma_f32_16x16x32_bf16 v[204:207], a[36:39], v[80:83], v[204:207]\n"
"ds_write_b128 v3, v[124:127] offset:8192\n"
"v_mfma_f32_16x16x32_bf16 v[24:27], a[40:43], v[80:83], v[24:27]\n"
"buffer_load_dwordx4 v[44:47], v0, s[4:7], s98 offen offset:1024 sc1\n"
"v_mfma_f32_16x16x32_bf16 v[212:215], a[44:47], v[80:83], v[212:215]\n"
"v_fmac_f32_e32 v174, v255, v178\n"
"v_fmac_f32_e32 v175, v255, v179\n"
"v_mfma_f32_16x16x32_bf16 v[112:115], a[48:51], v[116:119], v[112:115]\n"
"ds_write_b128 v3, v[128:131] offset:9216\n"
"v_mfma_f32_16x16x32_bf16 v[200:203], a[52:55], v[116:119], v[200:203]\n"
"buffer_load_dwordx4 a[184:187], v1, s[4:7], s97 offen offset:2048 sc1\n"
"v_mfma_f32_16x16x32_bf16 v[136:139], a[56:59], v[116:119], v[136:139]\n"
"v_fmac_f32_e32 v184, v255, v188\n"
"v_fmac_f32_e32 v185, v255, v189\n"
"v_mfma_f32_16x16x32_bf16 v[204:207], a[60:63], v[116:119], v[204:207]\n"
"ds_write_b128 v3, v[140:143] offset:10240\n"
"v_mfma_f32_16x16x32_bf16 v[24:27], a[64:67], v[116:119], v[24:27]\n"
"buffer_load_dwordx4 v[80:83], v0, s[4:7], s98 offen offset:2048 sc1\n"
"v_mfma_f32_16x16x32_bf16 v[212:215], a[68:71], v[116:119], v[212:215]\n"
"v_fmac_f32_e32 v186, v255, v190\n"
"v_fmac_f32_e32 v187, v255, v191\n"
"v_mfma_f32_16x16x32_bf16 v[112:115], a[72:75], v[144:147], v[112:115]\n"
"ds_write_b128 v3, v[164:167] offset:11264\n"
"v_mfma_f32_16x16x32_bf16 v[200:203], a[76:79], v[144:147], v[200:203]\n"
"ds_read_b128 v[116:119], v4 offset:3072\n"
"ds_read_b128 v[124:127], v9 offset:192\n"
"v_mfma_f32_16x16x32_bf16 v[136:139], a[80:83], v[144:147], v[136:139]\n"
"ds_read_b128 v[128:131], v4 offset:4096\n"
"ds_read_b128 v[140:143], v9 offset:256\n"
"v_mfma_f32_16x16x32_bf16 v[204:207], a[84:87], v[144:147], v[204:207]\n"
"ds_read_b128 v[164:167], v4 offset:5120\n"
"ds_read_b128 v[176:179], v9 offset:320\n"
"v_mfma_f32_16x16x32_bf16 v[24:27], a[88:91], v[144:147], v[24:27]\n"
"buffer_load_dwordx4 v[188:191], v0, s[4:7], s98 offen offset:3072 sc1\n"
"v_mfma_f32_16x16x32_bf16 v[212:215], a[92:95], v[144:147], v[212:215]\n"
"s_waitcnt vmcnt(20)\n"
"v_sqrt_f32_e32 v252, v252\n"
"v_mfma_f32_16x16x32_bf16 v[112:115], a[96:99], v[76:79], v[112:115]\n"
"v_cvt_pk_bf16_f32 v252, v252, 0\n"
"v_lshlrev_b32_e32 v252, 16, v252\n"
"v_mfma_f32_16x16x32_bf16 v[200:203], a[100:103], v[76:79], v[200:203]\n"
"v_rcp_f32_e32 v254, v252\n"
"v_mfma_f32_16x16x32_bf16 v[136:139], a[104:107], v[76:79], v[136:139]\n"
"s_waitcnt lgkmcnt(0)\n"
"v_fmac_f32_e32 v116, v255, v124\n"
"v_fmac_f32_e32 v117, v255, v125\n"
"v_mfma_f32_16x16x32_bf16 v[204:207], a[108:111], v[76:79], v[204:207]\n"
"v_fmac_f32_e32 v118, v255, v126\n"
"v_fmac_f32_e32 v119, v255, v127\n"
"v_mfma_f32_16x16x32_bf16 v[24:27], a[112:115], v[76:79], v[24:27]\n"
"buffer_load_dwordx4 v[124:127], v1, s[4:7], s98 offen sc1\n"
"v_mfma_f32_16x16x32_bf16 v[212:215], a[116:119], v[76:79], v[212:215]\n"
"v_fmac_f32_e32 v128, v255, v140\n"
"v_fmac_f32_e32 v129, v255, v141\n"
"v_mfma_f32_16x16x32_bf16 v[112:115], a[120:123], v[84:87], v[112:115]\n"
"v_fmac_f32_e32 v130, v255, v142\n"
"v_fmac_f32_e32 v131, v255, v143\n"
"v_mfma_f32_16x16x32_bf16 v[200:203], a[124:127], v[84:87], v[200:203]\n"
"v_fmac_f32_e32 v164, v255, v176\n"
"v_fmac_f32_e32 v165, v255, v177\n"
"v_mfma_f32_16x16x32_bf16 v[136:139], v[28:31], v[84:87], v[136:139]\n"
"v_fmac_f32_e32 v166, v255, v178\n"
"v_fmac_f32_e32 v167, v255, v179\n"
"v_mfma_f32_16x16x32_bf16 v[204:207], v[40:43], v[84:87], v[204:207]\n"
"buffer_load_dwordx4 a[188:191], v1, s[4:7], s97 offen offset:3072 sc1\n"
"v_mfma_f32_16x16x32_bf16 v[24:27], v[60:63], v[84:87], v[24:27]\n"
"buffer_load_dwordx4 v[76:79], v1, s[4:7], s98 offen offset:1024 sc1\n"
"v_mfma_f32_16x16x32_bf16 v[212:215], v[72:75], v[84:87], v[212:215]\n"
"s_waitcnt vmcnt(20)\n"
"s_add_u32 s97, s68, 0x1000\n"
"v_mfma_f32_16x16x32_bf16 v[112:115], v[156:159], v[108:111], v[112:115]\n"
"buffer_store_dwordx2 v[18:19], v11, s[4:7], s97 offen offset:64 sc1\n"
"v_mfma_f32_16x16x32_bf16 v[200:203], v[132:135], v[108:111], v[200:203]\n"
"v_mul_f32_e32 v255, 0x3e8293ee, v254\n"
"v_mfma_f32_16x16x32_bf16 v[136:139], v[64:67], v[108:111], v[136:139]\n"
"v_mfma_f32_16x16x32_bf16 v[204:207], v[68:71], v[108:111], v[204:207]\n"
"v_mfma_f32_16x16x32_bf16 v[24:27], v[88:91], v[108:111], v[24:27]\n"
"buffer_load_dwordx4 v[84:87], v1, s[4:7], s98 offen offset:2048 sc1\n"
"v_mfma_f32_16x16x32_bf16 v[212:215], v[160:163], v[108:111], v[212:215]\n"
"v_mfma_f32_16x16x32_bf16 v[112:115], v[92:95], v[32:35], v[112:115]\n"
"v_mfma_f32_16x16x32_bf16 v[200:203], v[180:183], v[32:35], v[200:203]\n"
"v_mfma_f32_16x16x32_bf16 v[136:139], v[100:103], v[32:35], v[136:139]\n"
"v_mfma_f32_16x16x32_bf16 v[204:207], v[148:151], v[32:35], v[204:207]\n"
"v_mfma_f32_16x16x32_bf16 v[24:27], v[192:195], v[32:35], v[24:27]\n"
"buffer_load_dwordx4 v[108:111], v1, s[4:7], s98 offen offset:3072 sc1\n"
"v_mfma_f32_16x16x32_bf16 v[212:215], v[56:59], v[32:35], v[212:215]\n"
"s_waitcnt vmcnt(20)\n"
"; @A G1 2.3\n"
"v_mfma_f32_16x16x32_bf16 v[120:123], a[0:3], v[152:155], v[120:123]\n"
"v_mfma_f32_16x16x32_bf16 v[172:175], a[4:7], v[152:155], v[172:175]\n"
"v_mfma_f32_16x16x32_bf16 v[184:187], a[8:11], v[152:155], v[184:187]\n"
"v_mfma_f32_16x16x32_bf16 v[116:119], a[12:15], v[152:155], v[116:119]\n"
"ds_write_b128 v3, v[112:115] offset:12288\n"
"v_mfma_f32_16x16x32_bf16 v[128:131], a[16:19], v[152:155], v[128:131]\n"
"s_add_u32 s98, s53, 0\n"
"buffer_load_dwordx4 v[32:35], v0, s[4:7], s98 offen sc1\n"
"v_mfma_f32_16x16x32_bf16 v[164:167], a[20:23], v[152:155], v[164:167]\n"
"s_add_u32 s97, s70, 0x4000\n"
"buffer_load_dwordx4 a[192:195], v0, s[4:7], s97 offen sc1\n"
"v_mfma_f32_16x16x32_bf16 v[120:123], a[24:27], v[104:107], v[120:123]\n"
"ds_write_b128 v3, v[200:203] offset:13312\n"
"v_mfma_f32_16x16x32_bf16 v[172:175], a[28:31], v[104:107], v[172:175]\n"
"v_mfma_f32_16x16x32_bf16 v[184:187], a[32:35], v[104:107], v[184:187]\n"
"v_mfma_f32_16x16x32_bf16 v[116:119], a[36:39], v[104:107], v[116:119]\n"
"ds_write_b128 v3, v[136:139] offset:14336\n"
"v_mfma_f32_16x16x32_bf16 v[128:131], a[40:43], v[104:107], v[128:131]\n"
"buffer_load_dwordx4 v[112:115], v0, s[4:7], s98 offen offset:1024 sc1\n"
"v_mfma_f32_16x16x32_bf16 v[164:167], a[44:47], v[104:107], v[164:167]\n"
"s_waitcnt vmcnt(20)\n"
"v_mfma_f32_16x16x32_bf16 v[120:123], a[48:51], v[52:55], v[120:123]\n"
"ds_write_b128 v3, v[204:207] offset:15360\n"
"v_mfma_f32_16x16x32_bf16 v[172:175], a[52:55], v[52:55], v[172:175]\n"
"v_mfma_f32_16x16x32_bf16 v[184:187], a[56:59], v[52:55], v[184:187]\n"
"v_mfma_f32_16x16x32_bf16 v[116:119], a[60:63], v[52:55], v[116:119]\n"
"ds_write_b128 v3, v[24:27] offset:16384\n"
"v_mfma_f32_16x16x32_bf16 v[128:131], a[64:67], v[52:55], v[128:131]\n"
"buffer_load_dwordx4 v[24:27], v0, s[4:7], s98 offen offset:2048 sc1\n"
"v_mfma_f32_16x16x32_bf16 v[164:167], a[68:71], v[52:55], v[164:167]\n"
"v_mfma_f32_16x16x32_bf16 v[120:123], a[72:75], v[36:39], v[120:123]\n"
"ds_write_b128 v3, v[212:215] offset:17408\n"
"v_mfma_f32_16x16x32_bf16 v[172:175], a[76:79], v[36:39], v[172:175]\n"
"v_mfma_f32_16x16x32_bf16 v[184:187], a[80:83], v[36:39], v[184:187]\n"
"v_mfma_f32_16x16x32_bf16 v[116:119], a[84:87], v[36:39], v[116:119]\n"
"s_waitcnt lgkmcnt(0)\n"
"s_barrier\n"
"v_mfma_f32_16x16x32_bf16 v[128:131], a[88:91], v[36:39], v[128:131]\n"
"buffer_load_dwordx4 v[52:55], v0, s[4:7], s98 offen offset:3072 sc1\n"
"v_mfma_f32_16x16x32_bf16 v[164:167], a[92:95], v[36:39], v[164:167]\n"
"s_waitcnt vmcnt(20)\n"
"ds_read_b128 v[36:39], v5 offset:2048\n"
"ds_read_b128 v[104:107], v6 offset:2048\n"
"v_mfma_f32_16x16x32_bf16 v[120:123], a[96:99], v[48:51], v[120:123]\n"
"ds_read_b128 v[136:139], v5 offset:3072\n"
"ds_read_b128 v[140:143], v6 offset:3072\n"
"v_mfma_f32_16x16x32_bf16 v[172:175], a[100:103], v[48:51], v[172:175]\n"
"buffer_load_dwordx4 a[196:199], v0, s[4:7], s97 offen offset:1024 sc1\n"
"v_mfma_f32_16x16x32_bf16 v[184:187], a[104:107], v[48:51], v[184:187]\n"
"v_mfma_f32_16x16x32_bf16 v[116:119], a[108:111], v[48:51], v[116:119]\n"
"ds_read_b128 v[144:147], v5 offset:4096\n"
"ds_read_b128 v[152:155], v6 offset:4096\n"
"v_mfma_f32_16x16x32_bf16 v[128:131], a[112:115], v[48:51], v[128:131]\n"
"buffer_load_dwordx4 v[176:179], v1, s[4:7], s98 offen sc1\n"
"v_mfma_f32_16x16x32_bf16 v[164:167], a[116:119], v[48:51], v[164:167]\n"
"ds_read_b128 v[48:51], v5 offset:5120\n"
"ds_read_b128 v[200:203], v6 offset:5120\n"
"v_mfma_f32_16x16x32_bf16 v[120:123], a[120:123], v[96:99], v[120:123]\n"
"s_waitcnt lgkmcnt(4)\n"
"v_add_f32_e32 v36, v36, v104\n"
"v_add_f32_e32 v37, v37, v105\n"
"v_mfma_f32_16x16x32_bf16 v[172:175], a[124:127], v[96:99], v[172:175]\n"
"v_add_f32_e32 v38, v38, v106\n"
"v_add_f32_e32 v39, v39, v107\n"
"v_mfma_f32_16x16x32_bf16 v[184:187], v[28:31], v[96:99], v[184:187]\n"
"ds_read_b128 v[104:107], v5\n"
"ds_read_b128 v[204:207], v6\n"
"v_mfma_f32_16x16x32_bf16 v[116:119], v[40:43], v[96:99], v[116:119]\n"
"ds_read_b128 v[212:215], v5 offset:1024\n"
"ds_read_b128 v[216:219], v6 offset:1024\n"
"v_mfma_f32_16x16x32_bf16 v[128:131], v[60:63], v[96:99], v[128:131]\n"
"buffer_load_dwordx4 v[220:223], v1, s[4:7], s98 offen offset:1024 sc1\n"
"v_mfma_f32_16x16x32_bf16 v[164:167], v[72:75], v[96:99], v[164:167]\n"
"s_waitcnt vmcnt(20)\n"
"v_add_f32_e32 v136, v136, v140\n"
"v_add_f32_e32 v137, v137, v141\n"
"v_mfma_f32_16x16x32_bf16 v[120:123], v[156:159], v[168:171], v[120:123]\n"
"s_waitcnt lgkmcnt(4)\n"
"v_add_f32_e32 v138, v138, v142\n"
"v_add_f32_e32 v139, v139, v143\n"
"v_mfma_f32_16x16x32_bf16 v[172:175], v[132:135], v[168:171], v[172:175]\n"
"v_add_f32_e32 v144, v144, v152\n"
"v_add_f32_e32 v145, v145, v153\n"
"v_mfma_f32_16x16x32_bf16 v[184:187], v[64:67], v[168:171], v[184:187]\n"
"s_waitcnt lgkmcnt(0)\n"
"v_add_f32_e32 v146, v146, v154\n"
"v_add_f32_e32 v147, v147, v155\n"
"v_mfma_f32_16x16x32_bf16 v[116:119], v[68:71], v[168:171], v[116:119]\n"
"v_add_f32_e32 v48, v48, v200\n"
"v_add_f32_e32 v49, v49, v201\n"
"v_mfma_f32_16x16x32_bf16 v[128:131], v[88:91], v[168:171], v[128:131]\n"
"buffer_load_dwordx4 v[96:99], v1, s[4:7], s98 offen offset:2048 sc1\n"
"v_mfma_f32_16x16x32_bf16 v[164:167], v[160:163], v[168:171], v[164:167]\n"
"v_add_f32_e32 v50, v50, v202\n"
"v_add_f32_e32 v51, v51, v203\n"
"v_mfma_f32_16x16x32_bf16 v[120:123], v[92:95], v[196:199], v[120:123]\n"
"v_add_f32_e32 v104, v104, v204\n"
"v_add_f32_e32 v105, v105, v205\n"
"v_mfma_f32_16x16x32_bf16 v[172:175], v[180:183], v[196:199], v[172:175]\n"
"v_add_f32_e32 v106, v106, v206\n"
"v_add_f32_e32 v107, v107, v207\n"
"v_mfma_f32_16x16x32_bf16 v[184:187], v[100:103], v[196:199], v[184:187]\n"
"v_add_f32_e32 v212, v212, v216\n"
"v_add_f32_e32 v213, v213, v217\n"
"v_mfma_f32_16x16x32_bf16 v[116:119], v[148:151], v[196:199], v[116:119]\n"
"v_add_f32_e32 v214, v214, v218\n"
"v_add_f32_e32 v215, v215, v219\n"
"v_mfma_f32_16x16x32_bf16 v[128:131], v[192:195], v[196:199], v[128:131]\n"
"buffer_load_dwordx4 v[140:143], v1, s[4:7], s98 offen offset:3072 sc1\n"
"v_mfma_f32_16x16x32_bf16 v[164:167], v[56:59], v[196:199], v[164:167]\n"
".Lmk_a3:\n"
"s_waitcnt vmcnt(19)\n"
"; @A G1 3.0\n"
"v_mfma_f32_16x16x32_bf16 v[152:155], a[0:3], v[208:211], 0\n"
"v_mfma_f32_16x16x32_bf16 v[168:171], a[4:7], v[208:211], 0\n"
"s_add_u32 s98, s62, 0\n"
"v_mfma_f32_16x16x32_bf16 v[196:199], a[8:11], v[208:211], 0\n"
"v_mfma_f32_16x16x32_bf16 v[200:203], a[12:15], v[208:211], 0\n"
"v_add_f32_e32 v184, v184, v36\n"
"v_add_f32_e32 v185, v185, v37\n"
"v_mfma_f32_16x16x32_bf16 v[204:207], a[16:19], v[208:211], 0\n"
"s_add_u32 s96, s54, 0\n"
"buffer_load_dwordx4 v[216:219], v0, s[4:7], s96 offen sc1\n"
"v_mfma_f32_16x16x32_bf16 v[224:227], a[20:23], v[208:211], 0\n"
"buffer_load_dword v253, v10, s[4:7], s98 offen sc1\n"
"v_mfma_f32_16x16x32_bf16 v[152:155], a[24:27], v[44:47], v[152:155]\n"
"v_add_f32_e32 v186, v186, v38\n"
"v_add_f32_e32 v187, v187, v39\n"
"v_mfma_f32_16x16x32_bf16 v[168:171], a[28:31], v[44:47], v[168:171]\n"
"v_add_f32_e32 v116, v116, v136\n"
"v_add_f32_e32 v117, v117, v137\n"
"v_mfma_f32_16x16x32_bf16 v[196:199], a[32:35], v[44:47], v[196:199]\n"
"buffer_load_dwordx4 a[200:203], v0, s[4:7], s97 offen offset:2048 sc1\n"
"v_mfma_f32_16x16x32_bf16 v[200:203], a[36:39], v[44:47], v[200:203]\n"
"buffer_load_dword v252, v10, s[4:7], s98 offen offset:256 sc1\n"
"v_mfma_f32_16x16x32_bf16 v[204:207], a[40:43], v[44:47], v[204:207]\n"
"buffer_load_dwordx4 v[36:39], v0, s[4:7], s96 offen offset:1024 sc1\n"
"v_mfma_f32_16x16x32_bf16 v[224:227], a[44:47], v[44:47], v[224:227]\n"
"s_waitcnt vmcnt(21)\n"
"v_add_f32_e32 v118, v118, v138\n"
"v_add_f32_e32 v119, v119, v139\n"
"v_mfma_f32_16x16x32_bf16 v[152:155], a[48:51], v[80:83], v[152:155]\n"
"buffer_load_dword v251, v10, s[4:7], s98 offen offset:512 sc1\n"
"v_mfma_f32_16x16x32_bf16 v[168:171], a[52:55], v[80:83], v[168:171]\n"
"v_mul_f32_e32 v184, v184, v254\n"
"v_mul_f32_e32 v185, v185, v254\n"
"v_mfma_f32_16x16x32_bf16 v[196:199], a[56:59], v[80:83], v[196:199]\n"
"v_mul_f32_e32 v186, v186, v254\n"
"v_mul_f32_e32 v187, v187, v254\n"
"v_mfma_f32_16x16x32_bf16 v[200:203], a[60:63], v[80:83], v[200:203]\n"
"buffer_load_dword v250, v10, s[4:7], s98 offen offset:768 sc1\n"
"v_mfma_f32_16x16x32_bf16 v[204:207], a[64:67], v[80:83], v[204:207]\n"
"buffer_load_dwordx4 v[44:47], v0, s[4:7], s96 offen offset:2048 sc1\n"
"v_mfma_f32_16x16x32_bf16 v[224:227], a[68:71], v[80:83], v[224:227]\n"
"v_mul_f32_e32 v116, v116, v254\n"
"v_mul_f32_e32 v117, v117, v254\n"
"v_mfma_f32_16x16x32_bf16 v[152:155], a[72:75], v[188:191], v[152:155]\n"
"buffer_load_dword v249, v10, s[4:7], s98 offen offset:1024 sc1\n"
"v_mfma_f32_16x16x32_bf16 v[168:171], a[76:79], v[188:191], v[168:171]\n"
"v_mul_f32_e32 v118, v118, v254\n"
"v_mul_f32_e32 v119, v119, v254\n"
"v_mfma_f32_16x16x32_bf16 v[196:199], a[80:83], v[188:191], v[196:199]\n"
"v_cvt_pk_bf16_f32 v80, v184, v185\n"
"v_cvt_pk_bf16_f32 v81, v186, v187\n"
"v_mfma_f32_16x16x32_bf16 v[200:203], a[84:87], v[188:191], v[200:203]\n"
"buffer_load_dword v248, v10, s[4:7], s98 offen offset:1280 sc1\n"
"v_mfma_f32_16x16x32_bf16 v[204:207], a[88:91], v[188:191], v[204:207]\n"
"buffer_load_dwordx4 v[136:139], v0, s[4:7], s96 offen offset:3072 sc1\n"
"v_mfma_f32_16x16x32_bf16 v[224:227], a[92:95], v[188:191], v[224:227]\n"
"s_waitcnt vmcnt(24)\n"
"v_cvt_pk_bf16_f32 v82, v116, v117\n"
"v_cvt_pk_bf16_f32 v83, v118, v119\n"
"v_mfma_f32_16x16x32_bf16 v[152:155], a[96:99], v[124:127], v[152:155]\n"
"buffer_load_dword v247, v10, s[4:7], s98 offen offset:1536 sc1\n"
"v_mfma_f32_16x16x32_bf16 v[168:171], a[100:103], v[124:127], v[168:171]\n"
"ds_write_b128 v8, v[80:83] offset:17408\n"
"v_mfma_f32_16x16x32_bf16 v[196:199], a[104:107], v[124:127], v[196:199]\n"
"buffer_load_dwordx4 a[204:207], v0, s[4:7], s97 offen offset:3072 sc1\n"
"v_mfma_f32_16x16x32_bf16 v[200:203], a[108:111], v[124:127], v[200:203]\n"
"buffer_load_dword v246, v10, s[4:7], s98 offen offset:1792 sc1\n"
"v_mfma_f32_16x16x32_bf16 v[204:207], a[112:115], v[124:127], v[204:207]\n"
"buffer_load_dwordx4 v[116:119], v1, s[4:7], s96 offen sc1\n"
"v_mfma_f32_16x16x32_bf16 v[224:227], a[116:119], v[124:127], v[224:227]\n"
"buffer_store_dwordx4 v[80:83], v8, s[4:7], s64 offen offset:1024 sc1\n"
"v_mfma_f32_16x16x32_bf16 v[152:155], a[120:123], v[76:79], v[152:155]\n"
"v_add_f32_e32 v128, v128, v144\n"
"v_add_f32_e32 v129, v129, v145\n"
"v_mfma_f32_16x16x32_bf16 v[168:171], a[124:127], v[76:79], v[168:171]\n"
"v_add_f32_e32 v130, v130, v146\n"
"v_add_f32_e32 v131, v131, v147\n"
"v_mfma_f32_16x16x32_bf16 v[196:199], v[28:31], v[76:79], v[196:199]\n"
"v_add_f32_e32 v164, v164, v48\n"
"v_add_f32_e32 v165, v165, v49\n"
"v_mfma_f32_16x16x32_bf16 v[200:203], v[40:43], v[76:79], v[200:203]\n"
"v_add_f32_e32 v166, v166, v50\n"
"v_add_f32_e32 v167, v167, v51\n"
"v_mfma_f32_16x16x32_bf16 v[204:207], v[60:63], v[76:79], v[204:207]\n"
"buffer_load_dwordx4 v[48:51], v1, s[4:7], s96 offen offset:1024 sc1\n"
"v_mfma_f32_16x16x32_bf16 v[224:227], v[72:75], v[76:79], v[224:227]\n"
"s_waitcnt vmcnt(27)\n"
"v_mul_f32_e32 v128, v128, v254\n"
"v_mul_f32_e32 v129, v129, v254\n"
"v_mfma_f32_16x16x32_bf16 v[152:155], v[156:159], v[84:87], v[152:155]\n"
"v_mul_f32_e32 v130, v130, v254\n"
"v_mul_f32_e32 v131, v131, v254\n"
"v_mfma_f32_16x16x32_bf16 v[168:171], v[132:135], v[84:87], v[168:171]\n"
"v_mul_f32_e32 v164, v164, v254\n"
"v_mul_f32_e32 v165, v165, v254\n"
"v_mfma_f32_16x16x32_bf16 v[196:199], v[64:67], v[84:87], v[196:199]\n"
"v_mul_f32_e32 v166, v166, v254\n"
"v_mul_f32_e32 v167, v167, v254\n"
"v_mfma_f32_16x16x32_bf16 v[200:203], v[68:71], v[84:87], v[200:203]\n"
"v_cvt_pk_bf16_f32 v76, v128, v129\n"
"v_cvt_pk_bf16_f32 v77, v130, v131\n"
"v_mfma_f32_16x16x32_bf16 v[204:207], v[88:91], v[84:87], v[204:207]\n"
"buffer_load_dwordx4 v[80:83], v1, s[4:7], s96 offen offset:2048 sc1\n"
"v_mfma_f32_16x16x32_bf16 v[224:227], v[160:163], v[84:87], v[224:227]\n"
"v_cvt_pk_bf16_f32 v78, v164, v165\n"
"v_cvt_pk_bf16_f32 v79, v166, v167\n"
"v_mfma_f32_16x16x32_bf16 v[152:155], v[92:95], v[108:111], v[152:155]\n"
"ds_write_b128 v7, v[76:79] offset:50176\n"
"v_mfma_f32_16x16x32_bf16 v[168:171], v[180:183], v[108:111], v[168:171]\n"
"s_add_u32 s98, s64, 0x4000\n"
"buffer_store_dwordx4 v[76:79], v7, s[4:7], s98 offen offset:1024 sc1\n"
"v_mfma_f32_16x16x32_bf16 v[196:199], v[100:103], v[108:111], v[196:199]\n"
"buffer_load_dwordx4 a[208:211], v1, s[4:7], s97 offen sc1\n"
"v_mfma_f32_16x16x32_bf16 v[200:203], v[148:151], v[108:111], v[200:203]\n"
"v_add_f32_e32 v120, v120, v104\n"
"v_add_f32_e32 v121, v121, v105\n"
"v_mfma_f32_16x16x32_bf16 v[204:207], v[192:195], v[108:111], v[204:207]\n"
"buffer_load_dwordx4 v[76:79], v1, s[4:7], s96 offen offset:3072 sc1\n"
"v_mfma_f32_16x16x32_bf16 v[224:227], v[56:59], v[108:111], v[224:227]\n"
"s_waitcnt vmcnt(28)\n"
"v_add_f32_e32 v122, v122, v106\n"
"v_add_f32_e32 v123, v123, v107\n"
"; @A G1 3.1\n"
"v_mfma_f32_16x16x32_bf16 v[84:87], a[0:3], v[32:35], 0\n"
"v_add_f32_e32 v172, v172, v212\n"
"v_add_f32_e32 v173, v173, v213\n"
"v_mfma_f32_16x16x32_bf16 v[104:107], a[4:7], v[32:35], 0\n"
"v_add_f32_e32 v174, v174, v214\n"
"v_add_f32_e32 v175, v175, v215\n"
"v_mfma_f32_16x16x32_bf16 v[108:111], a[8:11], v[32:35], 0\n"
"v_mul_f32_e32 v120, v120, v255\n"
"v_mul_f32_e32 v121, v121, v255\n"
"v_mfma_f32_16x16x32_bf16 v[124:127], a[12:15], v[32:35], 0\n"
"ds_write_b128 v3, v[152:155]\n"
"v_mfma_f32_16x16x32_bf16 v[128:131], a[16:19], v[32:35], 0\n"
"s_add_u32 s98, s55, 0\n"
"buffer_load_dwordx4 v[144:147], v0, s[4:7], s98 offen sc1\n"
"v_mfma_f32_16x16x32_bf16 v[152:155], a[20:23], v[32:35], 0\n"
"v_mul_f32_e32 v122, v122, v255\n"
"v_mul_f32_e32 v123, v123, v255\n"
"v_mfma_f32_16x16x32_bf16 v[84:87], a[24:27], v[112:115], v[84:87]\n"
"ds_write_b128 v3, v[168:171] offset:1024\n"
"v_mfma_f32_16x16x32_bf16 v[104:107], a[28:31], v[112:115], v[104:107]\n"
"v_mul_f32_e32 v172, v172, v255\n"
"v_mul_f32_e32 v173, v173, v255\n"
"v_mfma_f32_16x16x32_bf16 v[108:111], a[32:35], v[112:115], v[108:111]\n"
"v_mul_f32_e32 v174, v174, v255\n"
"v_mul_f32_e32 v175, v175, v255\n"
"v_mfma_f32_16x16x32_bf16 v[124:127], a[36:39], v[112:115], v[124:127]\n"
"ds_write_b128 v3, v[196:199] offset:2048\n"
"v_mfma_f32_16x16x32_bf16 v[128:131], a[40:43], v[112:115], v[128:131]\n"
"buffer_load_dwordx4 v[32:35], v0, s[4:7], s98 offen offset:1024 sc1\n"
"v_mfma_f32_16x16x32_bf16 v[152:155], a[44:47], v[112:115], v[152:155]\n"
"s_waitcnt vmcnt(28)\n"
"v_cvt_pk_bf16_f32 v112, v120, v121\n"
"v_cvt_pk_bf16_f32 v113, v122, v123\n"
"v_mfma_f32_16x16x32_bf16 v[84:87], a[48:51], v[24:27], v[84:87]\n"
"ds_write_b128 v3, v[200:203] offset:3072\n"
"v_mfma_f32_16x16x32_bf16 v[104:107], a[52:55], v[24:27], v[104:107]\n"
"v_cvt_pk_bf16_f32 v114, v172, v173\n"
"v_cvt_pk_bf16_f32 v115, v174, v175\n"
"v_mfma_f32_16x16x32_bf16 v[108:111], a[56:59], v[24:27], v[108:111]\n"
"buffer_load_dwordx4 a[212:215], v1, s[4:7], s97 offen offset:1024 sc1\n"
"v_mfma_f32_16x16x32_bf16 v[124:127], a[60:63], v[24:27], v[124:127]\n"
"ds_write_b128 v3, v[204:207] offset:4096\n"
"v_mfma_f32_16x16x32_bf16 v[128:131], a[64:67], v[24:27], v[128:131]\n"
"buffer_load_dwordx4 v[120:123], v0, s[4:7], s98 offen offset:2048 sc1\n"
"v_mfma_f32_16x16x32_bf16 v[152:155], a[68:71], v[24:27], v[152:155]\n"
"ds_write_b128 v2, v[112:115] offset:17408\n"
"v_mfma_f32_16x16x32_bf16 v[84:87], a[72:75], v[52:55], v[84:87]\n"
"ds_write_b128 v3, v[224:227] offset:5120\n"
"v_mfma_f32_16x16x32_bf16 v[104:107], a[76:79], v[52:55], v[104:107]\n"
"v_mfma_f32_16x16x32_bf16 v[108:111], a[80:83], v[52:55], v[108:111]\n"
"v_mfma_f32_16x16x32_bf16 v[124:127], a[84:87], v[52:55], v[124:127]\n"
"v_mfma_f32_16x16x32_bf16 v[128:131], a[88:91], v[52:55], v[128:131]\n"
"buffer_load_dwordx4 v[24:27], v0, s[4:7], s98 offen offset:3072 sc1\n"
"v_mfma_f32_16x16x32_bf16 v[152:155], a[92:95], v[52:55], v[152:155]\n"
"s_waitcnt vmcnt(28)\n"
"v_mfma_f32_16x16x32_bf16 v[84:87], a[96:99], v[176:179], v[84:87]\n"
"v_mfma_f32_16x16x32_bf16 v[104:107], a[100:103], v[176:179], v[104:107]\n"
"v_mfma_f32_16x16x32_bf16 v[108:111], a[104:107], v[176:179], v[108:111]\n"
"v_mfma_f32_16x16x32_bf16 v[124:127], a[108:111], v[176:179], v[124:127]\n"
"v_mfma_f32_16x16x32_bf16 v[128:131], a[112:115], v[176:179], v[128:131]\n"
"buffer_load_dwordx4 v[52:55], v1, s[4:7], s98 offen sc1\n"
"v_mfma_f32_16x16x32_bf16 v[152:155], a[116:119], v[176:179], v[152:155]\n"
"s_waitcnt lgkmcnt(0)\n"
"s_barrier\n"
"v_mfma_f32_16x16x32_bf16 v[84:87], a[120:123], v[220:223], v[84:87]\n"
"s_waitcnt vmcnt(13)\n"
"v_add_f32_e32 v253, v253, v249\n"
"v_add_f32_e32 v252, v252, v248\n"
"v_mfma_f32_16x16x32_bf16 v[104:107], a[124:127], v[220:223], v[104:107]\n"
"v_add_f32_e32 v251, v251, v247\n"
"v_add_f32_e32 v250, v250, v246\n"
"v_mfma_f32_16x16x32_bf16 v[108:111], v[28:31], v[220:223], v[108:111]\n"
"buffer_load_dwordx4 a[216:219], v1, s[4:7], s97 offen offset:2048 sc1\n"
"v_mfma_f32_16x16x32_bf16 v[124:127], v[40:43], v[220:223], v[124:127]\n"
"v_add_f32_e32 v253, v253, v251\n"
"v_add_f32_e32 v252, v252, v250\n"
"v_mfma_f32_16x16x32_bf16 v[128:131], v[60:63], v[220:223], v[128:131]\n"
"buffer_load_dwordx4 v[112:115], v1, s[4:7], s98 offen offset:1024 sc1\n"
"v_mfma_f32_16x16x32_bf16 v[152:155], v[72:75], v[220:223], v[152:155]\n"
"v_add_f32_e32 v253, v253, v252\n"
"v_mov_b32_e32 v252, v253\n"
"v_mfma_f32_16x16x32_bf16 v[84:87], v[156:159], v[96:99], v[84:87]\n"
"s_nop 0\n"
"v_permlane32_swap_b32_e32 v253, v252\n"
"v_add_f32_e32 v253, v253, v252\n"
"v_mfma_f32_16x16x32_bf16 v[104:107], v[132:135], v[96:99], v[104:107]\n"
"v_mul_f32_e32 v253, 0x3a800000, v253\n"
"v_mov_b32_e32 v252, v253\n"
"v_mfma_f32_16x16x32_bf16 v[108:111], v[64:67], v[96:99], v[108:111]\n"
"s_nop 0\n"
"v_permlane16_swap_b32_e32 v253, v252\n"
"v_cvt_pk_bf16_f32 v255, -v253, 0\n"
"v_mfma_f32_16x16x32_bf16 v[124:127], v[68:71], v[96:99], v[124:127]\n"
"v_fma_f32 v252, -v253, v253, v252\n"
"v_lshlrev_b32_e32 v255, 16, v255\n"
"v_mfma_f32_16x16x32_bf16 v[128:131], v[88:91], v[96:99], v[128:131]\n"
"buffer_load_dwordx4 v[164:167], v1, s[4:7], s98 offen offset:2048 sc1\n"
"v_mfma_f32_16x16x32_bf16 v[152:155], v[160:163], v[96:99], v[152:155]\n"
"ds_read_b128 v[96:99], v4\n"
"ds_read_b128 v[168:171], v9\n"
"v_mfma_f32_16x16x32_bf16 v[84:87], v[92:95], v[140:143], v[84:87]\n"
"s_add_u32 s96, s71, 0\n"
"buffer_load_dwordx2 v[250:251], off, s[4:7], s96 sc1\n"
"v_mfma_f32_16x16x32_bf16 v[104:107], v[180:183], v[140:143], v[104:107]\n"
"ds_read_b128 v[172:175], v4 offset:1024\n"
"ds_read_b128 v[176:179], v9 offset:64\n"
"v_mfma_f32_16x16x32_bf16 v[108:111], v[100:103], v[140:143], v[108:111]\n"
"ds_read_b128 v[184:187], v4 offset:2048\n"
"ds_read_b128 v[188:191], v9 offset:128\n"
"v_mfma_f32_16x16x32_bf16 v[124:127], v[148:151], v[140:143], v[124:127]\n"
"v_max_f32_e32 v252, 0, v252\n"
"v_cvt_pk_bf16_f32 v252, v252, 0\n"
"v_mfma_f32_16x16x32_bf16 v[128:131], v[192:195], v[140:143], v[128:131]\n"
"buffer_load_dwordx4 v[196:199], v1, s[4:7], s98 offen offset:3072 sc1\n"
"v_mfma_f32_16x16x32_bf16 v[152:155], v[56:59], v[140:143], v[152:155]\n"
"v_lshlrev_b32_e32 v252, 16, v252\n"
"; @A G1 3.2\n"
"v_mfma_f32_16x16x32_bf16 v[140:143], a[0:3], v[216:219], 0\n"
"buffer_store_dwordx2 v[18:19], v11, s[4:7], s68 offen offset:32 sc1\n"
"v_mfma_f32_16x16x32_bf16 v[200:203], a[4:7], v[216:219], 0\n"
"s_waitcnt lgkmcnt(0)\n"
"v_fmac_f32_e32 v96, v255, v168\n"
"v_fmac_f32_e32 v97, v255, v169\n"
"v_mfma_f32_16x16x32_bf16 v[204:207], a[8:11], v[216:219], 0\n"
"buffer_load_dwordx4 a[220:223], v1, s[4:7], s97 offen offset:3072 sc1\n"
"v_mfma_f32_16x16x32_bf16 v[208:211], a[12:15], v[216:219], 0\n"
"ds_write_b128 v3, v[84:87] offset:6144\n"
"v_mfma_f32_16x16x32_bf16 v[84:87], a[16:19], v[216:219], 0\n"
"s_add_u32 s98, s70, 0x6000\n"
"v_fmac_f32_e32 v98, v255, v170\n"
"v_fmac_f32_e32 v99, v255, v171\n"
"v_mfma_f32_16x16x32_bf16 v[168:171], a[20:23], v[216:219], 0\n"
"v_fmac_f32_e32 v172, v255, v176\n"
"v_fmac_f32_e32 v173, v255, v177\n"
"v_mfma_f32_16x16x32_bf16 v[140:143], a[24:27], v[36:39], v[140:143]\n"
"ds_write_b128 v3, v[104:107] offset:7168\n"
"v_mfma_f32_16x16x32_bf16 v[200:203], a[28:31], v[36:39], v[200:203]\n"
"buffer_load_dwordx4 a[224:227], v0, s[4:7], s98 offen sc1\n"
"v_mfma_f32_16x16x32_bf16 v[204:207], a[32:35], v[36:39], v[204:207]\n"
"v_fmac_f32_e32 v174, v255, v178\n"
"v_fmac_f32_e32 v175, v255, v179\n"
"v_mfma_f32_16x16x32_bf16 v[208:211], a[36:39], v[36:39], v[208:211]\n"
"ds_write_b128 v3, v[108:111] offset:8192\n"
"v_mfma_f32_16x16x32_bf16 v[84:87], a[40:43], v[36:39], v[84:87]\n"
"v_fmac_f32_e32 v184, v255, v188\n"
"v_fmac_f32_e32 v185, v255, v189\n"
"v_mfma_f32_16x16x32_bf16 v[168:171], a[44:47], v[36:39], v[168:171]\n"
"buffer_load_dwordx4 a[228:231], v0, s[4:7], s98 offen offset:1024 sc1\n"
"v_mfma_f32_16x16x32_bf16 v[140:143], a[48:51], v[44:47], v[140:143]\n"
"ds_write_b128 v3, v[124:127] offset:9216\n"
"v_mfma_f32_16x16x32_bf16 v[200:203], a[52:55], v[44:47], v[200:203]\n"
"v_fmac_f32_e32 v186, v255, v190\n"
"v_fmac_f32_e32 v187, v255, v191\n"
"v_mfma_f32_16x16x32_bf16 v[204:207], a[56:59], v[44:47], v[204:207]\n"
"ds_read_b128 v[36:39], v4 offset:3072\n"
"ds_read_b128 v[104:107], v9 offset:192\n"
"v_mfma_f32_16x16x32_bf16 v[208:211], a[60:63], v[44:47], v[208:211]\n"
"ds_write_b128 v3, v[128:131] offset:10240\n"
"v_mfma_f32_16x16x32_bf16 v[84:87], a[64:67], v[44:47], v[84:87]\n"
"buffer_load_dwordx4 a[232:235], v0, s[4:7], s98 offen offset:2048 sc1\n"
"v_mfma_f32_16x16x32_bf16 v[168:171], a[68:71], v[44:47], v[168:171]\n"
"ds_read_b128 v[44:47], v4 offset:4096\n"
"ds_read_b128 v[108:111], v9 offset:256\n"
"v_mfma_f32_16x16x32_bf16 v[140:143], a[72:75], v[136:139], v[140:143]\n"
"ds_write_b128 v3, v[152:155] offset:11264\n"
"v_mfma_f32_16x16x32_bf16 v[200:203], a[76:79], v[136:139], v[200:203]\n"
"ds_read_b128 v[124:127], v4 offset:5120\n"
"ds_read_b128 v[128:131], v9 offset:320\n"
"v_mfma_f32_16x16x32_bf16 v[204:207], a[80:83], v[136:139], v[204:207]\n"
"buffer_load_dwordx4 a[236:239], v0, s[4:7], s98 offen offset:3072 sc1\n"
"v_mfma_f32_16x16x32_bf16 v[208:211], a[84:87], v[136:139], v[208:211]\n"
"v_add_f32_e32 v252, 0x3727c5ac, v252\n"
"v_cvt_pk_bf16_f32 v252, v252, 0\n"
"v_mfma_f32_16x16x32_bf16 v[84:87], a[88:91], v[136:139], v[84:87]\n"
"v_lshlrev_b32_e32 v252, 16, v252\n"
"v_mfma_f32_16x16x32_bf16 v[168:171], a[92:95], v[136:139], v[168:171]\n"
"s_waitcnt vmcnt(21)\n"
"v_sqrt_f32_e32 v252, v252\n"
"v_mfma_f32_16x16x32_bf16 v[140:143], a[96:99], v[116:119], v[140:143]\n"
"buffer_load_dwordx4 a[240:243], v1, s[4:7], s98 offen sc1\n"
"v_mfma_f32_16x16x32_bf16 v[200:203], a[100:103], v[116:119], v[200:203]\n"
"s_waitcnt lgkmcnt(0)\n"
"v_fmac_f32_e32 v36, v255, v104\n"
"v_fmac_f32_e32 v37, v255, v105\n"
"v_mfma_f32_16x16x32_bf16 v[204:207], a[104:107], v[116:119], v[204:207]\n"
"v_fmac_f32_e32 v38, v255, v106\n"
"v_fmac_f32_e32 v39, v255, v107\n"
"v_mfma_f32_16x16x32_bf16 v[208:211], a[108:111], v[116:119], v[208:211]\n"
"v_fmac_f32_e32 v44, v255, v108\n"
"v_fmac_f32_e32 v45, v255, v109\n"
"v_mfma_f32_16x16x32_bf16 v[84:87], a[112:115], v[116:119], v[84:87]\n"
"buffer_load_dwordx4 a[244:247], v1, s[4:7], s98 offen offset:1024 sc1\n"
"v_mfma_f32_16x16x32_bf16 v[168:171], a[116:119], v[116:119], v[168:171]\n"
"v_fmac_f32_e32 v46, v255, v110\n"
"v_fmac_f32_e32 v47, v255, v111\n"
"v_mfma_f32_16x16x32_bf16 v[140:143], a[120:123], v[48:51], v[140:143]\n"
"v_fmac_f32_e32 v124, v255, v128\n"
"v_fmac_f32_e32 v125, v255, v129\n"
"v_mfma_f32_16x16x32_bf16 v[200:203], a[124:127], v[48:51], v[200:203]\n"
"v_fmac_f32_e32 v126, v255, v130\n"
"v_fmac_f32_e32 v127, v255, v131\n"
"v_mfma_f32_16x16x32_bf16 v[204:207], v[28:31], v[48:51], v[204:207]\n"
"buffer_load_dwordx4 a[248:251], v1, s[4:7], s98 offen offset:2048 sc1\n"
"v_mfma_f32_16x16x32_bf16 v[208:211], v[40:43], v[48:51], v[208:211]\n"
"v_cvt_pk_bf16_f32 v252, v252, 0\n"
"v_lshlrev_b32_e32 v252, 16, v252\n"
"v_mfma_f32_16x16x32_bf16 v[84:87], v[60:63], v[48:51], v[84:87]\n"
"v_rcp_f32_e32 v255, v252\n"
"v_mfma_f32_16x16x32_bf16 v[168:171], v[72:75], v[48:51], v[168:171]\n"
"s_waitcnt vmcnt(20)\n"
"v_mul_f32_e32 v254, 0x3e8293ee, v255\n"
"v_mfma_f32_16x16x32_bf16 v[140:143], v[156:159], v[80:83], v[140:143]\n"
"buffer_load_dwordx4 a[252:255], v1, s[4:7], s98 offen offset:3072 sc1\n"
"v_mfma_f32_16x16x32_bf16 v[200:203], v[132:135], v[80:83], v[200:203]\n"
"v_mfma_f32_16x16x32_bf16 v[204:207], v[64:67], v[80:83], v[204:207]\n"
"v_mfma_f32_16x16x32_bf16 v[208:211], v[68:71], v[80:83], v[208:211]\n"
"v_mfma_f32_16x16x32_bf16 v[84:87], v[88:91], v[80:83], v[84:87]\n"
"v_mfma_f32_16x16x32_bf16 v[168:171], v[160:163], v[80:83], v[168:171]\n"
"v_mfma_f32_16x16x32_bf16 v[140:143], v[92:95], v[76:79], v[140:143]\n"
"v_mfma_f32_16x16x32_bf16 v[200:203], v[180:183], v[76:79], v[200:203]\n"
"v_mfma_f32_16x16x32_bf16 v[204:207], v[100:103], v[76:79], v[204:207]\n"
"s_waitcnt vmcnt(11)\n"
"v_cmp_ne_u64_e32 vcc, v[250:251], v[18:19]\n"
"v_mfma_f32_16x16x32_bf16 v[208:211], v[148:151], v[76:79], v[208:211]\n"
"s_cbranch_vccnz .Lmk_f86s\n"
".Lmk_f86r:\n"
"s_add_u32 m0, s77, 0x6000\n"
"v_mfma_f32_16x16x32_bf16 v[84:87], v[192:195], v[76:79], v[84:87]\n"
"s_add_u32 s98, s65, 0x2000\n"
"buffer_load_dwordx4 v2, s[4:7], s98 offen sc1 lds\n"
"v_mfma_f32_16x16x32_bf16 v[168:171], v[56:59], v[76:79], v[168:171]\n"
"buffer_load_dwordx4 v2, s[4:7], s98 offen offset:1024 sc1 lds\n"
"; @A G1 3.3\n"
"v_mfma_f32_16x16x32_bf16 v[96:99], a[0:3], v[144:147], v[96:99]\n"
"v_mfma_f32_16x16x32_bf16 v[172:175], a[4:7], v[144:147], v[172:175]\n"
"v_mfma_f32_16x16x32_bf16 v[184:187], a[8:11], v[144:147], v[184:187]\n"
"v_xor_b32_e32 v253, 32, v16\n"
"v_xor_b32_e32 v252, 32, v15\n"
"v_mfma_f32_16x16x32_bf16 v[36:39], a[12:15], v[144:147], v[36:39]\n"
"ds_write_b128 v3, v[140:143] offset:12288\n"
"v_mfma_f32_16x16x32_bf16 v[44:47], a[16:19], v[144:147], v[44:47]\n"
"v_mov_b32_e32 v104, 0x3f803f80\n"
"v_mov_b32_e32 v105, 0x3f803f80\n"
"v_mfma_f32_16x16x32_bf16 v[124:127], a[20:23], v[144:147], v[124:127]\n"
"s_add_u32 m0, s77, 0xe000\n"
"s_add_u32 s98, s65, 0x6000\n"
"v_mov_b32_e32 v106, 0x3f803f80\n"
"v_mfma_f32_16x16x32_bf16 v[96:99], a[24:27], v[32:35], v[96:99]\n"
"ds_write_b128 v3, v[200:203] offset:13312\n"
"v_mfma_f32_16x16x32_bf16 v[172:175], a[28:31], v[32:35], v[172:175]\n"
"buffer_load_dwordx4 v2, s[4:7], s98 offen sc1 lds\n"
"v_mfma_f32_16x16x32_bf16 v[184:187], a[32:35], v[32:35], v[184:187]\n"
"buffer_load_dwordx4 v2, s[4:7], s98 offen offset:1024 sc1 lds\n"
"v_mfma_f32_16x16x32_bf16 v[36:39], a[36:39], v[32:35], v[36:39]\n"
"ds_write_b128 v3, v[204:207] offset:14336\n"
"v_mfma_f32_16x16x32_bf16 v[44:47], a[40:43], v[32:35], v[44:47]\n"
"v_mov_b32_e32 v107, 0x3f803f80\n"
"v_mov_b32_e32 v251, 0x53800000\n"
"v_mfma_f32_16x16x32_bf16 v[124:127], a[44:47], v[32:35], v[124:127]\n"
"v_mfma_f32_16x16x32_bf16 v[96:99], a[48:51], v[120:123], v[96:99]\n"
"ds_write_b128 v3, v[208:211] offset:15360\n"
"v_mfma_f32_16x16x32_bf16 v[172:175], a[52:55], v[120:123], v[172:175]\n"
"s_add_u32 s98, s69, 0x600000\n"
"buffer_load_dwordx4 a[0:3], v0, s[4:7], s98 offen sc1\n"
"v_mfma_f32_16x16x32_bf16 v[184:187], a[56:59], v[120:123], v[184:187]\n"
"v_mfma_f32_16x16x32_bf16 v[36:39], a[60:63], v[120:123], v[36:39]\n"
"ds_write_b128 v3, v[84:87] offset:16384\n"
"v_mfma_f32_16x16x32_bf16 v[44:47], a[64:67], v[120:123], v[44:47]\n"
"v_mfma_f32_16x16x32_bf16 v[124:127], a[68:71], v[120:123], v[124:127]\n"
"v_mfma_f32_16x16x32_bf16 v[96:99], a[72:75], v[24:27], v[96:99]\n"
"ds_write_b128 v3, v[168:171] offset:17408\n"
"v_mfma_f32_16x16x32_bf16 v[172:175], a[76:79], v[24:27], v[172:175]\n"
"buffer_load_dwordx4 a[4:7], v0, s[4:7], s98 offen offset:1024 sc1\n"
"v_mfma_f32_16x16x32_bf16 v[184:187], a[80:83], v[24:27], v[184:187]\n"
"v_mfma_f32_16x16x32_bf16 v[36:39], a[84:87], v[24:27], v[36:39]\n"
"s_waitcnt lgkmcnt(0)\n"
"s_barrier\n"
"v_mfma_f32_16x16x32_bf16 v[44:47], a[88:91], v[24:27], v[44:47]\n"
"ds_read_b128 v[32:35], v5 offset:2048\n"
"ds_read_b128 v[48:51], v6 offset:2048\n"
"v_mfma_f32_16x16x32_bf16 v[124:127], a[92:95], v[24:27], v[124:127]\n"
"ds_read_b128 v[24:27], v5 offset:3072\n"
"ds_read_b128 v[76:79], v6 offset:3072\n"
"v_mfma_f32_16x16x32_bf16 v[96:99], a[96:99], v[52:55], v[96:99]\n"
"buffer_load_dwordx4 a[8:11], v0, s[4:7], s98 offen offset:2048 sc1\n"
"v_mfma_f32_16x16x32_bf16 v[172:175], a[100:103], v[52:55], v[172:175]\n"
"v_mfma_f32_16x16x32_bf16 v[184:187], a[104:107], v[52:55], v[184:187]\n"
"ds_read_b128 v[80:83], v5 offset:4096\n"
"ds_read_b128 v[84:87], v6 offset:4096\n"
"v_mfma_f32_16x16x32_bf16 v[36:39], a[108:111], v[52:55], v[36:39]\n"
"ds_read_b128 v[108:111], v5 offset:5120\n"
"ds_read_b128 v[116:119], v6 offset:5120\n"
"v_mfma_f32_16x16x32_bf16 v[44:47], a[112:115], v[52:55], v[44:47]\n"
"s_waitcnt lgkmcnt(4)\n"
"v_add_f32_e32 v32, v32, v48\n"
"v_add_f32_e32 v33, v33, v49\n"
"v_mfma_f32_16x16x32_bf16 v[124:127], a[116:119], v[52:55], v[124:127]\n"
"buffer_load_dwordx4 a[12:15], v0, s[4:7], s98 offen offset:3072 sc1\n"
"v_mfma_f32_16x16x32_bf16 v[96:99], a[120:123], v[112:115], v[96:99]\n"
"ds_read_b128 v[52:55], v5\n"
"ds_read_b128 v[120:123], v6\n"
"v_mfma_f32_16x16x32_bf16 v[172:175], a[124:127], v[112:115], v[172:175]\n"
"ds_read_b128 v[128:131], v5 offset:1024\n"
"ds_read_b128 v[136:139], v6 offset:1024\n"
"v_mfma_f32_16x16x32_bf16 v[184:187], v[28:31], v[112:115], v[184:187]\n"
"s_waitcnt lgkmcnt(4)\n"
"v_add_f32_e32 v34, v34, v50\n"
"v_add_f32_e32 v35, v35, v51\n"
"v_mfma_f32_16x16x32_bf16 v[36:39], v[40:43], v[112:115], v[36:39]\n"
"v_add_f32_e32 v24, v24, v76\n"
"v_add_f32_e32 v25, v25, v77\n"
"v_mfma_f32_16x16x32_bf16 v[44:47], v[60:63], v[112:115], v[44:47]\n"
"buffer_load_dwordx4 a[16:19], v1, s[4:7], s98 offen sc1\n"
"v_mfma_f32_16x16x32_bf16 v[124:127], v[72:75], v[112:115], v[124:127]\n"
"s_waitcnt vmcnt(19)\n"
"v_add_f32_e32 v26, v26, v78\n"
"v_add_f32_e32 v27, v27, v79\n"
"v_mfma_f32_16x16x32_bf16 v[96:99], v[156:159], v[164:167], v[96:99]\n"
"s_waitcnt lgkmcnt(0)\n"
"v_add_f32_e32 v80, v80, v84\n"
"v_add_f32_e32 v81, v81, v85\n"
"v_mfma_f32_16x16x32_bf16 v[172:175], v[132:135], v[164:167], v[172:175]\n"
"v_add_f32_e32 v82, v82, v86\n"
"v_add_f32_e32 v83, v83, v87\n"
"v_mfma_f32_16x16x32_bf16 v[184:187], v[64:67], v[164:167], v[184:187]\n"
"v_add_f32_e32 v108, v108, v116\n"
"v_add_f32_e32 v109, v109, v117\n"
"v_mfma_f32_16x16x32_bf16 v[36:39], v[68:71], v[164:167], v[36:39]\n"
"buffer_load_dwordx4 a[20:23], v1, s[4:7], s98 offen offset:1024 sc1\n"
"v_mfma_f32_16x16x32_bf16 v[44:47], v[88:91], v[164:167], v[44:47]\n"
"v_add_f32_e32 v110, v110, v118\n"
"v_add_f32_e32 v111, v111, v119\n"
"v_mfma_f32_16x16x32_bf16 v[124:127], v[160:163], v[164:167], v[124:127]\n"
"v_add_f32_e32 v52, v52, v120\n"
"v_add_f32_e32 v53, v53, v121\n"
"v_mfma_f32_16x16x32_bf16 v[96:99], v[92:95], v[196:199], v[96:99]\n"
"v_add_f32_e32 v54, v54, v122\n"
"v_add_f32_e32 v55, v55, v123\n"
"v_mfma_f32_16x16x32_bf16 v[172:175], v[180:183], v[196:199], v[172:175]\n"
"v_add_f32_e32 v128, v128, v136\n"
"v_add_f32_e32 v129, v129, v137\n"
"v_mfma_f32_16x16x32_bf16 v[184:187], v[100:103], v[196:199], v[184:187]\n"
"buffer_load_dwordx4 a[24:27], v1, s[4:7], s98 offen offset:2048 sc1\n"
"v_mfma_f32_16x16x32_bf16 v[36:39], v[148:151], v[196:199], v[36:39]\n"
"v_add_f32_e32 v130, v130, v138\n"
"v_add_f32_e32 v131, v131, v139\n"
"v_mfma_f32_16x16x32_bf16 v[44:47], v[192:195], v[196:199], v[44:47]\n"
"v_mfma_f32_16x16x32_bf16 v[124:127], v[56:59], v[196:199], v[124:127]\n"
"buffer_load_dwordx4 a[28:31], v1, s[4:7], s98 offen offset:3072 sc1\n"
".Lmk_a4:\n"
"; @A tail\n"
"s_nop 0\n"
"v_add_f32_e32 v184, v184, v32\n"
"v_add_f32_e32 v185, v185, v33\n"
"v_add_f32_e32 v186, v186, v34\n"
"v_add_f32_e32 v187, v187, v35\n"
"v_add_f32_e32 v36, v36, v24\n"
"v_add_f32_e32 v37, v37, v25\n"
"v_add_f32_e32 v38, v38, v26\n"
"v_add_f32_e32 v39, v39, v27\n"
"v_mul_f32_e32 v184, v184, v255\n"
"v_mul_f32_e32 v185, v185, v255\n"
"v_mul_f32_e32 v186, v186, v255\n"
"v_mul_f32_e32 v187, v187, v255\n"
"v_mul_f32_e32 v36, v36, v255\n"
"v_mul_f32_e32 v37, v37, v255\n"
"v_mul_f32_e32 v38, v38, v255\n"
"v_mul_f32_e32 v39, v39, v255\n"
"v_cvt_pk_bf16_f32 v24, v184, v185\n"
"v_cvt_pk_bf16_f32 v25, v186, v187\n"
"v_cvt_pk_bf16_f32 v26, v36, v37\n"
"v_cvt_pk_bf16_f32 v27, v38, v39\n"
"ds_write_b128 v7, v[24:27] offset:16384\n"
"buffer_store_dwordx4 v[24:27], v7, s[4:7], s64 offen sc1\n"
"v_add_f32_e32 v44, v44, v80\n"
"v_add_f32_e32 v45, v45, v81\n"
"v_add_f32_e32 v46, v46, v82\n"
"v_add_f32_e32 v47, v47, v83\n"
"v_add_f32_e32 v124, v124, v108\n"
"v_add_f32_e32 v125, v125, v109\n"
"v_add_f32_e32 v126, v126, v110\n"
"v_add_f32_e32 v127, v127, v111\n"
"v_mul_f32_e32 v44, v44, v255\n"
"v_mul_f32_e32 v45, v45, v255\n"
"v_mul_f32_e32 v46, v46, v255\n"
"v_mul_f32_e32 v47, v47, v255\n"
"v_mul_f32_e32 v124, v124, v255\n"
"v_mul_f32_e32 v125, v125, v255\n"
"v_mul_f32_e32 v126, v126, v255\n"
"v_mul_f32_e32 v127, v127, v255\n"
"v_cvt_pk_bf16_f32 v24, v44, v45\n"
"v_cvt_pk_bf16_f32 v25, v46, v47\n"
"v_cvt_pk_bf16_f32 v26, v124, v125\n"
"v_cvt_pk_bf16_f32 v27, v126, v127\n"
"ds_write_b128 v7, v[24:27] offset:49152\n"
"s_add_u32 s98, s64, 0x4000\n"
"buffer_store_dwordx4 v[24:27], v7, s[4:7], s98 offen sc1\n"
"v_add_f32_e32 v96, v96, v52\n"
"v_add_f32_e32 v97, v97, v53\n"
"v_add_f32_e32 v98, v98, v54\n"
"v_add_f32_e32 v99, v99, v55\n"
"v_add_f32_e32 v172, v172, v128\n"
"v_add_f32_e32 v173, v173, v129\n"
"v_add_f32_e32 v174, v174, v130\n"
"v_add_f32_e32 v175, v175, v131\n"
"v_mul_f32_e32 v96, v96, v254\n"
"v_mul_f32_e32 v97, v97, v254\n"
"v_mul_f32_e32 v98, v98, v254\n"
"v_mul_f32_e32 v99, v99, v254\n"
"v_mul_f32_e32 v172, v172, v254\n"
"v_mul_f32_e32 v173, v173, v254\n"
"v_mul_f32_e32 v174, v174, v254\n"
"v_mul_f32_e32 v175, v175, v254\n"
"v_cvt_pk_bf16_f32 v24, v96, v97\n"
"v_cvt_pk_bf16_f32 v25, v98, v99\n"
"v_cvt_pk_bf16_f32 v26, v172, v173\n"
"v_cvt_pk_bf16_f32 v27, v174, v175\n"
"ds_write_b128 v2, v[24:27] offset:16384\n"
"s_waitcnt lgkmcnt(0)\n"
"s_barrier\n"
"ds_read_b128 v[28:31], v2 offset:49152\n"
"ds_read_b128 v[32:35], v2 offset:50176\n"
"ds_read_b128 v[88:91], v2 offset:16384\n"
"ds_read_b128 v[92:95], v2 offset:17408\n"
"; @A AT b\n"
"s_mov_b32 s2, 0\n"
"v_mov_b32_e32 v255, 0xff800000\n"
"v_mov_b32_e32 v254, 0xff800000\n"
"v_mov_b32_e32 v250, 0x7f800000\n"
"v_mov_b32_e32 v249, 0x7f800000\n"
".Lmk_a106:\n"
"ds_read_b128 v[24:27], v15 offset:24576\n"
"ds_read_b128 v[36:39], v252 offset:24832\n"
"ds_read_b128 v[40:43], v15 offset:22528\n"
"ds_read_b128 v[44:47], v252 offset:22784\n"
"s_cmp_gt_i32 s78, 11\n"
"s_cselect_b32 s98, 0x80000000, 0\n"
"s_or_b32 s98, s98, 0x7f800000\n"
"s_waitcnt lgkmcnt(2)\n"
"v_mfma_f32_16x16x32_bf16 v[48:51], v[24:27], v[28:31], 0\n"
"v_mfma_f32_16x16x32_bf16 v[52:55], v[24:27], v[32:35], 0\n"
"v_mfma_f32_16x16x32_bf16 v[24:27], v[36:39], v[28:31], 0\n"
"v_mfma_f32_16x16x32_bf16 v[56:59], v[36:39], v[32:35], 0\n"
"s_waitcnt lgkmcnt(0)\n"
"s_nop 3\n"
"v_max_f32_e32 v248, v48, v49\n"
"v_max_f32_e32 v247, v52, v53\n"
"v_max3_f32 v248, v248, v50, v51\n"
"v_max3_f32 v247, v247, v54, v55\n"
"v_max3_f32 v248, v248, v24, v25\n"
"v_max3_f32 v247, v247, v56, v57\n"
"v_max3_f32 v248, v248, v26, v27\n"
"v_max3_f32 v247, v247, v58, v59\n"
"v_mov_b32_e32 v246, v248\n"
"v_mov_b32_e32 v245, v247\n"
"s_nop 0\n"
"v_permlane16_swap_b32_e32 v248, v246\n"
"v_permlane16_swap_b32_e32 v247, v245\n"
"v_max_f32_e32 v248, v248, v246\n"
"v_max_f32_e32 v247, v247, v245\n"
"v_mov_b32_e32 v246, v248\n"
"v_mov_b32_e32 v245, v247\n"
"s_nop 0\n"
"v_permlane32_swap_b32_e32 v248, v246\n"
"v_permlane32_swap_b32_e32 v247, v245\n"
"v_max_f32_e32 v248, v248, v246\n"
"v_max_f32_e32 v247, v247, v245\n"
"v_med3_f32 v248, v248, v255, v250\n"
"v_med3_f32 v247, v247, v254, v249\n"
"v_min_f32_e64 v36, s98, -v248\n"
"v_min_f32_e64 v37, s98, -v248\n"
"v_min_f32_e64 v38, s98, -v248\n"
"v_min_f32_e64 v39, s98, -v248\n"
"v_min_f32_e64 v60, s98, -v247\n"
"v_min_f32_e64 v61, s98, -v247\n"
"v_min_f32_e64 v62, s98, -v247\n"
"v_min_f32_e64 v63, s98, -v247\n"
"v_mfma_f32_16x16x32_bf16 v[64:67], v[40:43], v[28:31], v[36:39]\n"
"ds_read_b128 v[68:71], v15 offset:20480\n"
"s_add_u32 s98, s69, 0x602000\n"
"buffer_load_dwordx4 a[32:35], v0, s[4:7], s98 offen sc1\n"
"v_sub_f32_e32 v48, v48, v248\n"
"v_sub_f32_e32 v49, v49, v248\n"
"v_sub_f32_e32 v50, v50, v248\n"
"v_mfma_f32_16x16x32_bf16 v[72:75], v[40:43], v[32:35], v[60:63]\n"
"buffer_load_dwordx4 a[36:39], v0, s[4:7], s98 offen offset:1024 sc1\n"
"v_sub_f32_e32 v51, v51, v248\n"
"v_sub_f32_e32 v24, v24, v248\n"
"v_sub_f32_e32 v25, v25, v248\n"
"v_sub_f32_e32 v26, v26, v248\n"
"v_sub_f32_e32 v27, v27, v248\n"
"v_mfma_f32_16x16x32_bf16 v[40:43], v[44:47], v[28:31], v[36:39]\n"
"ds_read_b128 v[76:79], v252 offset:20736\n"
"v_sub_f32_e32 v52, v52, v247\n"
"v_sub_f32_e32 v53, v53, v247\n"
"v_sub_f32_e32 v54, v54, v247\n"
"v_sub_f32_e32 v55, v55, v247\n"
"v_mfma_f32_16x16x32_bf16 v[80:83], v[44:47], v[32:35], v[60:63]\n"
"buffer_load_dwordx4 a[40:43], v0, s[4:7], s98 offen offset:2048 sc1\n"
"v_sub_f32_e32 v56, v56, v247\n"
"v_sub_f32_e32 v57, v57, v247\n"
"v_sub_f32_e32 v58, v58, v247\n"
"v_sub_f32_e32 v59, v59, v247\n"
"s_waitcnt lgkmcnt(0)\n"
"v_mfma_f32_16x16x32_bf16 v[44:47], v[68:71], v[28:31], v[36:39]\n"
"ds_read_b128 v[84:87], v15 offset:18432\n"
"buffer_load_dwordx4 a[44:47], v0, s[4:7], s98 offen offset:3072 sc1\n"
"ds_read_b128 v[96:99], v252 offset:18688\n"
"v_exp_f32_e32 v64, v64\n"
"v_exp_f32_e32 v65, v65\n"
"v_exp_f32_e32 v66, v66\n"
"v_exp_f32_e32 v67, v67\n"
"v_exp_f32_e32 v40, v40\n"
"v_mfma_f32_16x16x32_bf16 v[100:103], v[68:71], v[32:35], v[60:63]\n"
"ds_read_b64_tr_b16 v[68:69], v16 offset:22528\n"
"buffer_load_dwordx4 a[48:51], v1, s[4:7], s98 offen sc1\n"
"v_exp_f32_e32 v41, v41\n"
"v_exp_f32_e32 v42, v42\n"
"v_exp_f32_e32 v43, v43\n"
"v_cvt_pk_bf16_f32 v108, v64, v65\n"
"v_cvt_pk_bf16_f32 v109, v66, v67\n"
"v_cvt_pk_bf16_f32 v110, v40, v41\n"
"v_mfma_f32_16x16x32_bf16 v[64:67], v[76:79], v[28:31], v[36:39]\n"
"ds_read_b64_tr_b16 v[70:71], v253 offset:22784\n"
"ds_read_b64_tr_b16 v[40:41], v253 offset:22528\n"
"buffer_load_dwordx4 a[52:55], v1, s[4:7], s98 offen offset:1024 sc1\n"
"v_cvt_pk_bf16_f32 v111, v42, v43\n"
"v_exp_f32_e32 v72, v72\n"
"v_exp_f32_e32 v73, v73\n"
"v_exp_f32_e32 v74, v74\n"
"v_exp_f32_e32 v75, v75\n"
"v_mfma_f32_16x16x32_bf16 v[112:115], v[76:79], v[32:35], v[60:63]\n"
"ds_read_b64_tr_b16 v[42:43], v16 offset:22784\n"
"v_exp_f32_e32 v80, v80\n"
"v_exp_f32_e32 v81, v81\n"
"v_exp_f32_e32 v82, v82\n"
"v_exp_f32_e32 v83, v83\n"
"v_cvt_pk_bf16_f32 v76, v72, v73\n"
"v_cvt_pk_bf16_f32 v77, v74, v75\n"
"v_cvt_pk_bf16_f32 v78, v80, v81\n"
"v_cvt_pk_bf16_f32 v79, v82, v83\n"
"s_waitcnt lgkmcnt(4)\n"
"v_mfma_f32_16x16x32_bf16 v[72:75], v[84:87], v[28:31], v[36:39]\n"
"ds_read_b128 v[80:83], v15 offset:16384\n"
"v_exp_f32_e32 v44, v44\n"
"v_exp_f32_e32 v45, v45\n"
"v_exp_f32_e32 v46, v46\n"
"v_mfma_f32_16x16x32_bf16 v[116:119], v[84:87], v[32:35], v[60:63]\n"
"buffer_load_dwordx4 a[56:59], v1, s[4:7], s98 offen offset:2048 sc1\n"
"v_exp_f32_e32 v47, v47\n"
"v_mfma_f32_16x16x32_bf16 v[84:87], v[96:99], v[28:31], v[36:39]\n"
"ds_read_b128 v[120:123], v252 offset:16640\n"
"v_exp_f32_e32 v64, v64\n"
"v_exp_f32_e32 v65, v65\n"
"v_mfma_f32_16x16x32_bf16 v[124:127], v[96:99], v[32:35], v[60:63]\n"
"ds_read_b64_tr_b16 v[96:97], v16 offset:20480\n"
"v_exp_f32_e32 v66, v66\n"
"v_exp_f32_e32 v67, v67\n"
"v_cvt_pk_bf16_f32 v128, v44, v45\n"
"s_waitcnt lgkmcnt(3)\n"
"v_mfma_f32_16x16x32_bf16 v[132:135], v[104:107], v[108:111], 0\n"
"buffer_load_dwordx4 a[60:63], v1, s[4:7], s98 offen offset:3072 sc1\n"
"v_cvt_pk_bf16_f32 v129, v46, v47\n"
"v_cvt_pk_bf16_f32 v130, v64, v65\n"
"v_cvt_pk_bf16_f32 v131, v66, v67\n"
"v_mfma_f32_16x16x32_bf16 v[44:47], v[68:71], v[108:111], 0\n"
"ds_read_b64_tr_b16 v[98:99], v253 offset:20736\n"
"v_exp_f32_e32 v100, v100\n"
"v_exp_f32_e32 v101, v101\n"
"v_mfma_f32_16x16x32_bf16 v[64:67], v[40:43], v[108:111], 0\n"
"ds_read_b64_tr_b16 v[108:109], v253 offset:20480\n"
"s_add_u32 s98, s69, 0x604000\n"
"buffer_load_dwordx4 a[64:67], v0, s[4:7], s98 offen sc1\n"
"v_exp_f32_e32 v102, v102\n"
"v_exp_f32_e32 v103, v103\n"
"v_mfma_f32_16x16x32_bf16 v[136:139], v[104:107], v[76:79], 0\n"
"v_exp_f32_e32 v112, v112\n"
"v_exp_f32_e32 v113, v113\n"
"v_mfma_f32_16x16x32_bf16 v[140:143], v[68:71], v[76:79], 0\n"
"ds_read_b64_tr_b16 v[110:111], v16 offset:20736\n"
"v_exp_f32_e32 v114, v114\n"
"v_exp_f32_e32 v115, v115\n"
"v_mfma_f32_16x16x32_bf16 v[68:71], v[40:43], v[76:79], 0\n"
"s_waitcnt vmcnt(19)\n"
"v_cvt_pk_bf16_f32 v40, v100, v101\n"
"v_cvt_pk_bf16_f32 v41, v102, v103\n"
"v_cvt_pk_bf16_f32 v42, v112, v113\n"
"v_cvt_pk_bf16_f32 v43, v114, v115\n"
"s_cmp_lg_u32 s2, 0\n"
"s_cbranch_scc1 .Lmk_s108\n"
"s_barrier\n"
".Lmk_s108:\n"
"s_waitcnt lgkmcnt(4)\n"
"v_mfma_f32_16x16x32_bf16 v[76:79], v[80:83], v[28:31], v[36:39]\n"
"ds_read_b128 v[100:103], v15 offset:14336\n"
"v_exp_f32_e32 v72, v72\n"
"v_exp_f32_e32 v73, v73\n"
"v_exp_f32_e32 v74, v74\n"
"v_mfma_f32_16x16x32_bf16 v[112:115], v[80:83], v[32:35], v[60:63]\n"
"buffer_load_dwordx4 a[68:71], v0, s[4:7], s98 offen offset:1024 sc1\n"
"v_exp_f32_e32 v75, v75\n"
"v_mfma_f32_16x16x32_bf16 v[80:83], v[120:123], v[28:31], v[36:39]\n"
"ds_read_b128 v[144:147], v252 offset:14592\n"
"v_exp_f32_e32 v84, v84\n"
"v_exp_f32_e32 v85, v85\n"
"v_mfma_f32_16x16x32_bf16 v[148:151], v[120:123], v[32:35], v[60:63]\n"
"ds_read_b64_tr_b16 v[120:121], v16 offset:18432\n"
"v_exp_f32_e32 v86, v86\n"
"v_exp_f32_e32 v87, v87\n"
"s_waitcnt lgkmcnt(3)\n"
"v_mfma_f32_16x16x32_bf16 v[132:135], v[104:107], v[128:131], v[132:135]\n"
"buffer_load_dwordx4 a[72:75], v0, s[4:7], s98 offen offset:2048 sc1\n"
"v_cvt_pk_bf16_f32 v152, v72, v73\n"
"v_cvt_pk_bf16_f32 v153, v74, v75\n"
"v_cvt_pk_bf16_f32 v154, v84, v85\n"
"v_mfma_f32_16x16x32_bf16 v[44:47], v[96:99], v[128:131], v[44:47]\n"
"ds_read_b64_tr_b16 v[122:123], v253 offset:18688\n"
"v_cvt_pk_bf16_f32 v155, v86, v87\n"
"v_exp_f32_e32 v116, v116\n"
"v_exp_f32_e32 v117, v117\n"
"v_mfma_f32_16x16x32_bf16 v[64:67], v[108:111], v[128:131], v[64:67]\n"
"ds_read_b64_tr_b16 v[72:73], v253 offset:18432\n"
"v_exp_f32_e32 v118, v118\n"
"v_exp_f32_e32 v119, v119\n"
"v_mfma_f32_16x16x32_bf16 v[136:139], v[104:107], v[40:43], v[136:139]\n"
"buffer_load_dwordx4 a[76:79], v0, s[4:7], s98 offen offset:3072 sc1\n"
"v_exp_f32_e32 v124, v124\n"
"v_exp_f32_e32 v125, v125\n"
"v_mfma_f32_16x16x32_bf16 v[140:143], v[96:99], v[40:43], v[140:143]\n"
"ds_read_b64_tr_b16 v[74:75], v16 offset:18688\n"
"v_exp_f32_e32 v126, v126\n"
"v_exp_f32_e32 v127, v127\n"
"v_mfma_f32_16x16x32_bf16 v[68:71], v[108:111], v[40:43], v[68:71]\n"
"s_waitcnt lgkmcnt(4)\n"
"v_cvt_pk_bf16_f32 v40, v116, v117\n"
"v_cvt_pk_bf16_f32 v41, v118, v119\n"
"v_cvt_pk_bf16_f32 v42, v124, v125\n"
"v_cvt_pk_bf16_f32 v43, v126, v127\n"
"s_cmp_eq_i32 s78, 8\n"
"s_cbranch_scc1 .Lmk_x109s\n"
".Lmk_x109r:\n"
"v_mfma_f32_16x16x32_bf16 v[84:87], v[100:103], v[28:31], v[36:39]\n"
"ds_read_b128 v[96:99], v15 offset:12288\n"
"v_exp_f32_e32 v76, v76\n"
"v_exp_f32_e32 v77, v77\n"
"v_exp_f32_e32 v78, v78\n"
"v_mfma_f32_16x16x32_bf16 v[108:111], v[100:103], v[32:35], v[60:63]\n"
"buffer_load_dwordx4 a[80:83], v1, s[4:7], s98 offen sc1\n"
"v_exp_f32_e32 v79, v79\n"
"v_mfma_f32_16x16x32_bf16 v[100:103], v[144:147], v[28:31], v[36:39]\n"
"ds_read_b128 v[116:119], v252 offset:12544\n"
"v_exp_f32_e32 v80, v80\n"
"v_exp_f32_e32 v81, v81\n"
"v_mfma_f32_16x16x32_bf16 v[124:127], v[144:147], v[32:35], v[60:63]\n"
"ds_read_b64_tr_b16 v[128:129], v16 offset:16384\n"
"v_exp_f32_e32 v82, v82\n"
"v_exp_f32_e32 v83, v83\n"
"s_waitcnt lgkmcnt(3)\n"
"v_mfma_f32_16x16x32_bf16 v[132:135], v[104:107], v[152:155], v[132:135]\n"
"buffer_load_dwordx4 a[84:87], v1, s[4:7], s98 offen offset:1024 sc1\n"
"v_cvt_pk_bf16_f32 v144, v76, v77\n"
"v_cvt_pk_bf16_f32 v145, v78, v79\n"
"v_cvt_pk_bf16_f32 v146, v80, v81\n"
"v_mfma_f32_16x16x32_bf16 v[44:47], v[120:123], v[152:155], v[44:47]\n"
"ds_read_b64_tr_b16 v[130:131], v253 offset:16640\n"
"v_cvt_pk_bf16_f32 v147, v82, v83\n"
"v_exp_f32_e32 v112, v112\n"
"v_exp_f32_e32 v113, v113\n"
"v_mfma_f32_16x16x32_bf16 v[64:67], v[72:75], v[152:155], v[64:67]\n"
"ds_read_b64_tr_b16 v[76:77], v253 offset:16384\n"
"v_exp_f32_e32 v114, v114\n"
"v_exp_f32_e32 v115, v115\n"
"v_mfma_f32_16x16x32_bf16 v[136:139], v[104:107], v[40:43], v[136:139]\n"
"buffer_load_dwordx4 a[88:91], v1, s[4:7], s98 offen offset:2048 sc1\n"
"v_exp_f32_e32 v148, v148\n"
"v_exp_f32_e32 v149, v149\n"
"v_mfma_f32_16x16x32_bf16 v[140:143], v[120:123], v[40:43], v[140:143]\n"
"ds_read_b64_tr_b16 v[78:79], v16 offset:16640\n"
"v_exp_f32_e32 v150, v150\n"
"v_exp_f32_e32 v151, v151\n"
"v_mfma_f32_16x16x32_bf16 v[68:71], v[72:75], v[40:43], v[68:71]\n"
"s_waitcnt lgkmcnt(4)\n"
"v_cvt_pk_bf16_f32 v40, v112, v113\n"
"v_cvt_pk_bf16_f32 v41, v114, v115\n"
"v_cvt_pk_bf16_f32 v42, v148, v149\n"
"v_cvt_pk_bf16_f32 v43, v150, v151\n"
"s_cmp_eq_i32 s78, 7\n"
"s_cbranch_scc1 .Lmk_x110s\n"
".Lmk_x110r:\n"
"v_mfma_f32_16x16x32_bf16 v[72:75], v[96:99], v[28:31], v[36:39]\n"
"ds_read_b128 v[80:83], v15 offset:10240\n"
"v_exp_f32_e32 v84, v84\n"
"v_exp_f32_e32 v85, v85\n"
"v_exp_f32_e32 v86, v86\n"
"v_mfma_f32_16x16x32_bf16 v[112:115], v[96:99], v[32:35], v[60:63]\n"
"buffer_load_dwordx4 a[92:95], v1, s[4:7], s98 offen offset:3072 sc1\n"
"v_exp_f32_e32 v87, v87\n"
"v_mfma_f32_16x16x32_bf16 v[96:99], v[116:119], v[28:31], v[36:39]\n"
"ds_read_b128 v[120:123], v252 offset:10496\n"
"v_exp_f32_e32 v100, v100\n"
"v_exp_f32_e32 v101, v101\n"
"v_mfma_f32_16x16x32_bf16 v[148:151], v[116:119], v[32:35], v[60:63]\n"
"ds_read_b64_tr_b16 v[116:117], v16 offset:14336\n"
"v_exp_f32_e32 v102, v102\n"
"v_exp_f32_e32 v103, v103\n"
"v_cvt_pk_bf16_f32 v152, v84, v85\n"
"s_waitcnt lgkmcnt(3)\n"
"v_mfma_f32_16x16x32_bf16 v[132:135], v[104:107], v[144:147], v[132:135]\n"
"s_add_u32 s98, s69, 0x606000\n"
"buffer_load_dwordx4 a[96:99], v0, s[4:7], s98 offen sc1\n"
"v_cvt_pk_bf16_f32 v153, v86, v87\n"
"v_cvt_pk_bf16_f32 v154, v100, v101\n"
"v_mfma_f32_16x16x32_bf16 v[44:47], v[128:131], v[144:147], v[44:47]\n"
"ds_read_b64_tr_b16 v[118:119], v253 offset:14592\n"
"v_cvt_pk_bf16_f32 v155, v102, v103\n"
"v_exp_f32_e32 v108, v108\n"
"v_exp_f32_e32 v109, v109\n"
"v_mfma_f32_16x16x32_bf16 v[64:67], v[76:79], v[144:147], v[64:67]\n"
"ds_read_b64_tr_b16 v[84:85], v253 offset:14336\n"
"v_exp_f32_e32 v110, v110\n"
"v_exp_f32_e32 v111, v111\n"
"v_mfma_f32_16x16x32_bf16 v[136:139], v[104:107], v[40:43], v[136:139]\n"
"buffer_load_dwordx4 a[100:103], v0, s[4:7], s98 offen offset:1024 sc1\n"
"v_exp_f32_e32 v124, v124\n"
"v_exp_f32_e32 v125, v125\n"
"v_mfma_f32_16x16x32_bf16 v[140:143], v[128:131], v[40:43], v[140:143]\n"
"ds_read_b64_tr_b16 v[86:87], v16 offset:14592\n"
"v_exp_f32_e32 v126, v126\n"
"v_exp_f32_e32 v127, v127\n"
"v_mfma_f32_16x16x32_bf16 v[68:71], v[76:79], v[40:43], v[68:71]\n"
"s_waitcnt lgkmcnt(4)\n"
"v_cvt_pk_bf16_f32 v40, v108, v109\n"
"v_cvt_pk_bf16_f32 v41, v110, v111\n"
"v_cvt_pk_bf16_f32 v42, v124, v125\n"
"v_cvt_pk_bf16_f32 v43, v126, v127\n"
"s_cmp_eq_i32 s78, 6\n"
"s_cbranch_scc1 .Lmk_x111s\n"
".Lmk_x111r:\n"
"v_mfma_f32_16x16x32_bf16 v[76:79], v[80:83], v[28:31], v[36:39]\n"
"ds_read_b128 v[100:103], v15 offset:8192\n"
"v_exp_f32_e32 v72, v72\n"
"v_exp_f32_e32 v73, v73\n"
"v_exp_f32_e32 v74, v74\n"
"v_mfma_f32_16x16x32_bf16 v[108:111], v[80:83], v[32:35], v[60:63]\n"
"buffer_load_dwordx4 a[104:107], v0, s[4:7], s98 offen offset:2048 sc1\n"
"v_exp_f32_e32 v75, v75\n"
"v_mfma_f32_16x16x32_bf16 v[80:83], v[120:123], v[28:31], v[36:39]\n"
"ds_read_b128 v[124:127], v252 offset:8448\n"
"v_exp_f32_e32 v96, v96\n"
"v_exp_f32_e32 v97, v97\n"
"v_mfma_f32_16x16x32_bf16 v[128:131], v[120:123], v[32:35], v[60:63]\n"
"ds_read_b64_tr_b16 v[120:121], v16 offset:12288\n"
"v_exp_f32_e32 v98, v98\n"
"v_exp_f32_e32 v99, v99\n"
"s_waitcnt lgkmcnt(3)\n"
"v_mfma_f32_16x16x32_bf16 v[132:135], v[104:107], v[152:155], v[132:135]\n"
"buffer_load_dwordx4 a[108:111], v0, s[4:7], s98 offen offset:3072 sc1\n"
"v_cvt_pk_bf16_f32 v144, v72, v73\n"
"v_cvt_pk_bf16_f32 v145, v74, v75\n"
"v_cvt_pk_bf16_f32 v146, v96, v97\n"
"v_mfma_f32_16x16x32_bf16 v[44:47], v[116:119], v[152:155], v[44:47]\n"
"ds_read_b64_tr_b16 v[122:123], v253 offset:12544\n"
"v_cvt_pk_bf16_f32 v147, v98, v99\n"
"v_exp_f32_e32 v112, v112\n"
"v_exp_f32_e32 v113, v113\n"
"v_mfma_f32_16x16x32_bf16 v[64:67], v[84:87], v[152:155], v[64:67]\n"
"ds_read_b64_tr_b16 v[72:73], v253 offset:12288\n"
"v_exp_f32_e32 v114, v114\n"
"v_exp_f32_e32 v115, v115\n"
"v_mfma_f32_16x16x32_bf16 v[136:139], v[104:107], v[40:43], v[136:139]\n"
"buffer_load_dwordx4 a[112:115], v1, s[4:7], s98 offen sc1\n"
"v_exp_f32_e32 v148, v148\n"
"v_exp_f32_e32 v149, v149\n"
"v_mfma_f32_16x16x32_bf16 v[140:143], v[116:119], v[40:43], v[140:143]\n"
"ds_read_b64_tr_b16 v[74:75], v16 offset:12544\n"
"v_exp_f32_e32 v150, v150\n"
"v_exp_f32_e32 v151, v151\n"
"v_mfma_f32_16x16x32_bf16 v[68:71], v[84:87], v[40:43], v[68:71]\n"
"s_waitcnt lgkmcnt(4)\n"
"v_cvt_pk_bf16_f32 v40, v112, v113\n"
"v_cvt_pk_bf16_f32 v41, v114, v115\n"
"v_cvt_pk_bf16_f32 v42, v148, v149\n"
"v_cvt_pk_bf16_f32 v43, v150, v151\n"
"s_cmp_eq_i32 s78, 5\n"
"s_cbranch_scc1 .Lmk_x112s\n"
".Lmk_x112r:\n"
"v_mfma_f32_16x16x32_bf16 v[84:87], v[100:103], v[28:31], v[36:39]\n"
"ds_read_b64_tr_b16 v[96:97], v16 offset:10240\n"
"ds_read_b64_tr_b16 v[98:99], v253 offset:10496\n"
"v_exp_f32_e32 v76, v76\n"
"v_exp_f32_e32 v77, v77\n"
"v_mfma_f32_16x16x32_bf16 v[112:115], v[100:103], v[32:35], v[60:63]\n"
"buffer_load_dwordx4 a[116:119], v1, s[4:7], s98 offen offset:1024 sc1\n"
"v_exp_f32_e32 v78, v78\n"
"v_exp_f32_e32 v79, v79\n"
"v_mfma_f32_16x16x32_bf16 v[100:103], v[124:127], v[28:31], v[36:39]\n"
"ds_read_b64_tr_b16 v[36:37], v253 offset:10240\n"
"ds_read_b64_tr_b16 v[38:39], v16 offset:10496\n"
"v_exp_f32_e32 v80, v80\n"
"v_exp_f32_e32 v81, v81\n"
"v_mfma_f32_16x16x32_bf16 v[116:119], v[124:127], v[32:35], v[60:63]\n"
"ds_read_b64_tr_b16 v[60:61], v16 offset:24576\n"
"v_exp_f32_e32 v82, v82\n"
"v_exp_f32_e32 v83, v83\n"
"s_waitcnt lgkmcnt(5)\n"
"v_mfma_f32_16x16x32_bf16 v[132:135], v[104:107], v[144:147], v[132:135]\n"
"buffer_load_dwordx4 a[120:123], v1, s[4:7], s98 offen offset:2048 sc1\n"
"ds_read_b64_tr_b16 v[62:63], v253 offset:24832\n"
"v_cvt_pk_bf16_f32 v124, v76, v77\n"
"v_cvt_pk_bf16_f32 v125, v78, v79\n"
"v_cvt_pk_bf16_f32 v126, v80, v81\n"
"v_mfma_f32_16x16x32_bf16 v[44:47], v[120:123], v[144:147], v[44:47]\n"
"ds_read_b64_tr_b16 v[76:77], v253 offset:24576\n"
"v_cvt_pk_bf16_f32 v127, v82, v83\n"
"v_exp_f32_e32 v108, v108\n"
"v_exp_f32_e32 v109, v109\n"
"v_mfma_f32_16x16x32_bf16 v[64:67], v[72:75], v[144:147], v[64:67]\n"
"ds_read_b64_tr_b16 v[78:79], v16 offset:24832\n"
"v_exp_f32_e32 v110, v110\n"
"v_exp_f32_e32 v111, v111\n"
"v_mfma_f32_16x16x32_bf16 v[136:139], v[104:107], v[40:43], v[136:139]\n"
"buffer_load_dwordx4 a[124:127], v1, s[4:7], s98 offen offset:3072 sc1\n"
"v_exp_f32_e32 v128, v128\n"
"v_add_u32_e32 v248, 0x26400, v0\n"
"v_exp_f32_e32 v129, v129\n"
"v_mfma_f32_16x16x32_bf16 v[140:143], v[120:123], v[40:43], v[140:143]\n"
"ds_read_b128 v[80:83], v248\n"
"v_exp_f32_e32 v130, v130\n"
"v_exp_f32_e32 v131, v131\n"
"v_mfma_f32_16x16x32_bf16 v[68:71], v[72:75], v[40:43], v[68:71]\n"
"ds_read_b128 v[72:75], v248 offset:1024\n"
"v_cvt_pk_bf16_f32 v40, v108, v109\n"
"v_cvt_pk_bf16_f32 v41, v110, v111\n"
"v_cvt_pk_bf16_f32 v42, v128, v129\n"
"v_cvt_pk_bf16_f32 v43, v130, v131\n"
"s_waitcnt lgkmcnt(6)\n"
"v_mfma_f32_16x16x32_bf16 v[132:135], v[104:107], v[124:127], v[132:135]\n"
"ds_read_b64_tr_b16 v[108:109], v16 offset:8192\n"
"v_cndmask_b32_e64 v84, v48, v84, s[20:21]\n"
"v_cndmask_b32_e64 v85, v49, v85, s[22:23]\n"
"v_cndmask_b32_e64 v86, v50, v86, s[24:25]\n"
"s_add_u32 m0, s77, 0x4000\n"
"s_add_u32 s98, s65, 0\n"
"buffer_load_dwordx4 v2, s[4:7], s98 offen sc1 lds\n"
"buffer_load_dwordx4 v2, s[4:7], s98 offen offset:1024 sc1 lds\n"
"v_cndmask_b32_e64 v87, v51, v87, s[26:27]\n"
"v_cndmask_b32_e64 v100, v24, v100, s[28:29]\n"
"v_mfma_f32_16x16x32_bf16 v[44:47], v[96:99], v[124:127], v[44:47]\n"
"ds_read_b64_tr_b16 v[110:111], v253 offset:8448\n"
"v_cndmask_b32_e64 v101, v25, v101, s[30:31]\n"
"v_cndmask_b32_e64 v102, v26, v102, s[32:33]\n"
"v_cndmask_b32_e64 v103, v27, v103, s[34:35]\n"
"v_exp_f32_e32 v84, v84\n"
"v_exp_f32_e32 v85, v85\n"
"v_exp_f32_e32 v86, v86\n"
"v_exp_f32_e32 v87, v87\n"
"v_mfma_f32_16x16x32_bf16 v[64:67], v[36:39], v[124:127], v[64:67]\n"
"v_exp_f32_e32 v100, v100\n"
"v_exp_f32_e32 v101, v101\n"
"v_exp_f32_e32 v102, v102\n"
"v_exp_f32_e32 v103, v103\n"
"v_cvt_pk_bf16_f32 v48, v84, v85\n"
"v_cvt_pk_bf16_f32 v49, v86, v87\n"
"v_cvt_pk_bf16_f32 v50, v100, v101\n"
"v_mfma_f32_16x16x32_bf16 v[136:139], v[104:107], v[40:43], v[136:139]\n"
"ds_read_b64_tr_b16 v[84:85], v253 offset:8192\n"
"s_add_u32 s98, s69, 0x608000\n"
"buffer_load_dwordx4 v[24:27], v0, s[4:7], s98 offen sc1\n"
"v_cvt_pk_bf16_f32 v51, v102, v103\n"
"v_cndmask_b32_e64 v112, v52, v112, s[36:37]\n"
"v_cndmask_b32_e64 v113, v53, v113, s[38:39]\n"
"v_cndmask_b32_e64 v114, v54, v114, s[40:41]\n"
"v_cndmask_b32_e64 v115, v55, v115, s[42:43]\n"
"v_cndmask_b32_e64 v116, v56, v116, s[44:45]\n"
"v_cndmask_b32_e64 v117, v57, v117, s[46:47]\n"
"v_mfma_f32_16x16x32_bf16 v[140:143], v[96:99], v[40:43], v[140:143]\n"
"ds_read_b64_tr_b16 v[86:87], v16 offset:8448\n"
"v_cndmask_b32_e64 v118, v58, v118, s[48:49]\n"
"v_cndmask_b32_e64 v119, v59, v119, s[50:51]\n"
"v_exp_f32_e32 v112, v112\n"
"v_exp_f32_e32 v113, v113\n"
"v_exp_f32_e32 v114, v114\n"
"v_exp_f32_e32 v115, v115\n"
"v_exp_f32_e32 v116, v116\n"
"v_mfma_f32_16x16x32_bf16 v[68:71], v[36:39], v[40:43], v[68:71]\n"
"s_waitcnt lgkmcnt(0)\n"
"v_exp_f32_e32 v117, v117\n"
"v_exp_f32_e32 v118, v118\n"
"v_exp_f32_e32 v119, v119\n"
"v_cvt_pk_bf16_f32 v36, v112, v113\n"
"v_cvt_pk_bf16_f32 v37, v114, v115\n"
"v_cvt_pk_bf16_f32 v38, v116, v117\n"
"v_cvt_pk_bf16_f32 v39, v118, v119\n"
"v_mfma_f32_16x16x32_bf16 v[132:135], v[104:107], v[48:51], v[132:135]\n"
"v_and_b32_e32 v80, v48, v80\n"
"v_and_b32_e32 v81, v49, v81\n"
"v_and_b32_e32 v82, v50, v82\n"
"v_and_b32_e32 v83, v51, v83\n"
"v_mfma_f32_16x16x32_bf16 v[136:139], v[104:107], v[36:39], v[136:139]\n"
"v_xor_b32_e32 v40, v48, v80\n"
"v_xor_b32_e32 v41, v49, v81\n"
"v_xor_b32_e32 v42, v50, v82\n"
"v_xor_b32_e32 v43, v51, v83\n"
"v_mfma_f32_16x16x32_bf16 v[44:47], v[108:111], v[80:83], v[44:47]\n"
"v_and_b32_e32 v72, v36, v72\n"
"v_and_b32_e32 v73, v37, v73\n"
"v_mfma_f32_16x16x32_bf16 v[64:67], v[84:87], v[80:83], v[64:67]\n"
"v_and_b32_e32 v74, v38, v74\n"
"v_and_b32_e32 v75, v39, v75\n"
"v_mfma_f32_16x16x32_bf16 v[44:47], v[60:63], v[40:43], v[44:47]\n"
"v_xor_b32_e32 v48, v36, v72\n"
"v_xor_b32_e32 v49, v37, v73\n"
"v_mfma_f32_16x16x32_bf16 v[64:67], v[76:79], v[40:43], v[64:67]\n"
"v_xor_b32_e32 v50, v38, v74\n"
"v_xor_b32_e32 v51, v39, v75\n"
"v_mfma_f32_16x16x32_bf16 v[140:143], v[108:111], v[72:75], v[140:143]\n"
"s_add_u32 m0, s77, 0xc000\n"
"s_add_u32 s97, s65, 0x4000\n"
"buffer_load_dwordx4 v2, s[4:7], s97 offen sc1 lds\n"
"buffer_load_dwordx4 v2, s[4:7], s97 offen offset:1024 sc1 lds\n"
"v_mfma_f32_16x16x32_bf16 v[68:71], v[84:87], v[72:75], v[68:71]\n"
"v_mfma_f32_16x16x32_bf16 v[140:143], v[60:63], v[48:51], v[140:143]\n"
"v_mfma_f32_16x16x32_bf16 v[68:71], v[76:79], v[48:51], v[68:71]\n"
"v_max_f32_e32 v248, v132, v136\n"
"v_rcp_f32_e32 v132, v132\n"
"v_rcp_f32_e32 v136, v136\n"
"s_nop 0\n"
"v_max3_f32 v248, v248, v132, v136\n"
"v_cmp_nlt_f32_e32 vcc, v248, v251\n"
"s_cbranch_vccnz .Lmk_b107\n"
".Lmk_k113:\n"
"s_add_u32 s97, s60, 0x40000\n"
"v_mul_f32_e32 v44, v44, v132\n"
"v_mul_f32_e32 v45, v45, v132\n"
"v_mul_f32_e32 v46, v46, v132\n"
"v_mul_f32_e32 v47, v47, v132\n"
"v_mul_f32_e32 v64, v64, v132\n"
"v_mul_f32_e32 v65, v65, v132\n"
"v_mul_f32_e32 v66, v66, v132\n"
"v_mul_f32_e32 v67, v67, v132\n"
"v_cvt_pk_bf16_f32 v28, v44, v45\n"
"v_cvt_pk_bf16_f32 v29, v46, v47\n"
"v_cvt_pk_bf16_f32 v30, v64, v65\n"
"v_cvt_pk_bf16_f32 v31, v66, v67\n"
"buffer_store_dwordx4 v[28:31], v0, s[4:7], s97 offen sc1\n"
"s_add_u32 s97, s60, 0x48000\n"
"v_mul_f32_e32 v140, v140, v136\n"
"v_mul_f32_e32 v141, v141, v136\n"
"v_mul_f32_e32 v142, v142, v136\n"
"v_mul_f32_e32 v143, v143, v136\n"
"v_mul_f32_e32 v68, v68, v136\n"
"v_mul_f32_e32 v69, v69, v136\n"
"v_mul_f32_e32 v70, v70, v136\n"
"v_mul_f32_e32 v71, v71, v136\n"
"v_cvt_pk_bf16_f32 v28, v140, v141\n"
"v_cvt_pk_bf16_f32 v29, v142, v143\n"
"v_cvt_pk_bf16_f32 v30, v68, v69\n"
"v_cvt_pk_bf16_f32 v31, v70, v71\n"
"buffer_store_dwordx4 v[28:31], v0, s[4:7], s97 offen sc1\n"
"; @A AT a\n"
"s_mov_b32 s2, 0\n"
"v_mov_b32_e32 v255, 0xff800000\n"
"v_mov_b32_e32 v254, 0xff800000\n"
"v_mov_b32_e32 v250, 0x7f800000\n"
"v_mov_b32_e32 v249, 0x7f800000\n"
".Lmk_a114:\n"
"ds_read_b128 v[28:31], v15 offset:16384\n"
"ds_read_b128 v[32:35], v252 offset:16640\n"
"ds_read_b128 v[36:39], v15 offset:14336\n"
"ds_read_b128 v[40:43], v252 offset:14592\n"
"s_cmp_gt_i32 s78, 7\n"
"s_cselect_b32 s97, 0x80000000, 0\n"
"s_or_b32 s97, s97, 0x7f800000\n"
"s_add_u32 s96, s67, 32\n"
"buffer_load_dwordx2 v[246:247], v12, s[4:7], s96 offen sc1\n"
"s_waitcnt lgkmcnt(2)\n"
"v_mfma_f32_16x16x32_bf16 v[96:99], v[28:31], v[88:91], 0\n"
"v_mfma_f32_16x16x32_bf16 v[100:103], v[28:31], v[92:95], 0\n"
"v_mfma_f32_16x16x32_bf16 v[108:111], v[32:35], v[88:91], 0\n"
"v_mfma_f32_16x16x32_bf16 v[112:115], v[32:35], v[92:95], 0\n"
"s_waitcnt lgkmcnt(0)\n"
"s_nop 3\n"
"v_max_f32_e32 v248, v96, v97\n"
"v_max_f32_e32 v245, v100, v101\n"
"v_max3_f32 v248, v248, v98, v99\n"
"v_max3_f32 v245, v245, v102, v103\n"
"v_max3_f32 v248, v248, v108, v109\n"
"v_max3_f32 v245, v245, v112, v113\n"
"v_max3_f32 v248, v248, v110, v111\n"
"v_max3_f32 v245, v245, v114, v115\n"
"v_mov_b32_e32 v244, v248\n"
"v_mov_b32_e32 v243, v245\n"
"s_nop 0\n"
"v_permlane16_swap_b32_e32 v248, v244\n"
"v_permlane16_swap_b32_e32 v245, v243\n"
"v_max_f32_e32 v248, v248, v244\n"
"v_max_f32_e32 v245, v245, v243\n"
"v_mov_b32_e32 v244, v248\n"
"v_mov_b32_e32 v243, v245\n"
"s_nop 0\n"
"v_permlane32_swap_b32_e32 v248, v244\n"
"v_permlane32_swap_b32_e32 v245, v243\n"
"v_max_f32_e32 v248, v248, v244\n"
"v_max_f32_e32 v245, v245, v243\n"
"v_med3_f32 v248, v248, v255, v250\n"
"v_med3_f32 v245, v245, v254, v249\n"
"v_min_f32_e64 v116, s97, -v248\n"
"v_min_f32_e64 v117, s97, -v248\n"
"v_min_f32_e64 v118, s97, -v248\n"
"v_min_f32_e64 v119, s97, -v248\n"
"v_min_f32_e64 v120, s97, -v245\n"
"v_min_f32_e64 v121, s97, -v245\n"
"v_min_f32_e64 v122, s97, -v245\n"
"v_min_f32_e64 v123, s97, -v245\n"
"v_mfma_f32_16x16x32_bf16 v[44:47], v[36:39], v[88:91], v[116:119]\n"
"ds_read_b128 v[32:35], v15 offset:12288\n"
"v_sub_f32_e32 v96, v96, v248\n"
"v_sub_f32_e32 v97, v97, v248\n"
"v_sub_f32_e32 v98, v98, v248\n"
"v_sub_f32_e32 v99, v99, v248\n"
"v_mfma_f32_16x16x32_bf16 v[48:51], v[36:39], v[92:95], v[120:123]\n"
"v_sub_f32_e32 v108, v108, v248\n"
"v_sub_f32_e32 v109, v109, v248\n"
"v_sub_f32_e32 v110, v110, v248\n"
"v_sub_f32_e32 v111, v111, v248\n"
"v_mfma_f32_16x16x32_bf16 v[36:39], v[40:43], v[88:91], v[116:119]\n"
"ds_read_b128 v[52:55], v252 offset:12544\n"
"v_sub_f32_e32 v100, v100, v245\n"
"v_sub_f32_e32 v101, v101, v245\n"
"v_sub_f32_e32 v102, v102, v245\n"
"v_sub_f32_e32 v103, v103, v245\n"
"v_mfma_f32_16x16x32_bf16 v[56:59], v[40:43], v[92:95], v[120:123]\n"
"s_waitcnt lgkmcnt(0)\n"
"v_sub_f32_e32 v112, v112, v245\n"
"v_sub_f32_e32 v113, v113, v245\n"
"v_sub_f32_e32 v114, v114, v245\n"
"v_sub_f32_e32 v115, v115, v245\n"
"s_cmp_eq_i32 s78, 7\n"
"s_cbranch_scc1 .Lmk_x116s\n"
".Lmk_x116r:\n"
"v_mfma_f32_16x16x32_bf16 v[60:63], v[32:35], v[88:91], v[116:119]\n"
"ds_read_b128 v[40:43], v15 offset:10240\n"
"buffer_load_dwordx4 v[28:31], v0, s[4:7], s98 offen offset:1024 sc1\n"
"ds_read_b128 v[64:67], v252 offset:10496\n"
"v_exp_f32_e32 v44, v44\n"
"v_exp_f32_e32 v45, v45\n"
"v_exp_f32_e32 v46, v46\n"
"v_exp_f32_e32 v47, v47\n"
"v_exp_f32_e32 v36, v36\n"
"v_mfma_f32_16x16x32_bf16 v[68:71], v[32:35], v[92:95], v[120:123]\n"
"ds_read_b64_tr_b16 v[72:73], v16 offset:14336\n"
"buffer_load_dwordx4 v[32:35], v0, s[4:7], s98 offen offset:2048 sc1\n"
"v_exp_f32_e32 v37, v37\n"
"v_exp_f32_e32 v38, v38\n"
"v_exp_f32_e32 v39, v39\n"
"v_cvt_pk_bf16_f32 v76, v44, v45\n"
"v_cvt_pk_bf16_f32 v77, v46, v47\n"
"v_cvt_pk_bf16_f32 v78, v36, v37\n"
"v_mfma_f32_16x16x32_bf16 v[80:83], v[52:55], v[88:91], v[116:119]\n"
"ds_read_b64_tr_b16 v[74:75], v253 offset:14592\n"
"ds_read_b64_tr_b16 v[84:85], v253 offset:14336\n"
"v_cvt_pk_bf16_f32 v79, v38, v39\n"
"v_exp_f32_e32 v48, v48\n"
"v_exp_f32_e32 v49, v49\n"
"v_exp_f32_e32 v50, v50\n"
"v_exp_f32_e32 v51, v51\n"
"v_mfma_f32_16x16x32_bf16 v[124:127], v[52:55], v[92:95], v[120:123]\n"
"buffer_load_dwordx4 v[36:39], v0, s[4:7], s98 offen offset:3072 sc1\n"
"ds_read_b64_tr_b16 v[86:87], v16 offset:14592\n"
"v_exp_f32_e32 v56, v56\n"
"v_exp_f32_e32 v57, v57\n"
"v_exp_f32_e32 v58, v58\n"
"v_exp_f32_e32 v59, v59\n"
"v_cvt_pk_bf16_f32 v52, v48, v49\n"
"v_cvt_pk_bf16_f32 v53, v50, v51\n"
"v_cvt_pk_bf16_f32 v54, v56, v57\n"
"v_cvt_pk_bf16_f32 v55, v58, v59\n"
"s_waitcnt lgkmcnt(4)\n"
"s_cmp_eq_i32 s78, 6\n"
"s_cbranch_scc1 .Lmk_x117s\n"
".Lmk_x117r:\n"
"v_mfma_f32_16x16x32_bf16 v[128:131], v[40:43], v[88:91], v[116:119]\n"
"ds_read_b128 v[56:59], v15 offset:8192\n"
"ds_read_b128 v[132:135], v252 offset:8448\n"
"v_exp_f32_e32 v60, v60\n"
"v_exp_f32_e32 v61, v61\n"
"v_mfma_f32_16x16x32_bf16 v[136:139], v[40:43], v[92:95], v[120:123]\n"
"buffer_load_dwordx4 v[40:43], v1, s[4:7], s98 offen sc1\n"
"v_exp_f32_e32 v62, v62\n"
"v_exp_f32_e32 v63, v63\n"
"v_mfma_f32_16x16x32_bf16 v[140:143], v[64:67], v[88:91], v[116:119]\n"
"ds_read_b64_tr_b16 v[144:145], v16 offset:12288\n"
"ds_read_b64_tr_b16 v[146:147], v253 offset:12544\n"
"v_exp_f32_e32 v80, v80\n"
"v_exp_f32_e32 v81, v81\n"
"v_mfma_f32_16x16x32_bf16 v[148:151], v[64:67], v[92:95], v[120:123]\n"
"ds_read_b64_tr_b16 v[64:65], v253 offset:12288\n"
"v_exp_f32_e32 v82, v82\n"
"v_exp_f32_e32 v83, v83\n"
"s_waitcnt lgkmcnt(5)\n"
"v_mfma_f32_16x16x32_bf16 v[152:155], v[104:107], v[76:79], 0\n"
"buffer_load_dwordx4 v[44:47], v1, s[4:7], s98 offen offset:1024 sc1\n"
"v_cvt_pk_bf16_f32 v156, v60, v61\n"
"v_cvt_pk_bf16_f32 v157, v62, v63\n"
"v_cvt_pk_bf16_f32 v158, v80, v81\n"
"v_cvt_pk_bf16_f32 v159, v82, v83\n"
"v_mfma_f32_16x16x32_bf16 v[160:163], v[72:75], v[76:79], 0\n"
"ds_read_b64_tr_b16 v[66:67], v16 offset:12544\n"
"s_waitcnt vmcnt(8)\n"
"ds_read_b128 v[80:83], v2 offset:16384\n"
"v_exp_f32_e32 v68, v68\n"
"v_exp_f32_e32 v69, v69\n"
"v_mfma_f32_16x16x32_bf16 v[164:167], v[84:87], v[76:79], 0\n"
"v_exp_f32_e32 v70, v70\n"
"v_exp_f32_e32 v71, v71\n"
"v_mfma_f32_16x16x32_bf16 v[168:171], v[104:107], v[52:55], 0\n"
"buffer_load_dwordx4 v[48:51], v1, s[4:7], s98 offen offset:2048 sc1\n"
"ds_read_b128 v[76:79], v2 offset:17408\n"
"v_exp_f32_e32 v124, v124\n"
"v_exp_f32_e32 v125, v125\n"
"v_mfma_f32_16x16x32_bf16 v[172:175], v[72:75], v[52:55], 0\n"
"ds_read_b128 v[72:75], v2 offset:49152\n"
"v_exp_f32_e32 v126, v126\n"
"v_exp_f32_e32 v127, v127\n"
"v_mfma_f32_16x16x32_bf16 v[176:179], v[84:87], v[52:55], 0\n"
"ds_read_b128 v[84:87], v2 offset:50176\n"
"v_cvt_pk_bf16_f32 v180, v68, v69\n"
"v_cvt_pk_bf16_f32 v181, v70, v71\n"
"v_cvt_pk_bf16_f32 v182, v124, v125\n"
"v_cvt_pk_bf16_f32 v183, v126, v127\n"
"s_waitcnt lgkmcnt(8)\n"
"s_cmp_eq_i32 s78, 5\n"
"s_cbranch_scc1 .Lmk_x118s\n"
".Lmk_x118r:\n"
"v_mfma_f32_16x16x32_bf16 v[124:127], v[56:59], v[88:91], v[116:119]\n"
"ds_read_b64_tr_b16 v[68:69], v16 offset:10240\n"
"ds_read_b64_tr_b16 v[70:71], v253 offset:10496\n"
"buffer_load_dwordx4 v[52:55], v1, s[4:7], s98 offen offset:3072 sc1\n"
"v_exp_f32_e32 v128, v128\n"
"v_exp_f32_e32 v129, v129\n"
"v_mfma_f32_16x16x32_bf16 v[184:187], v[56:59], v[92:95], v[120:123]\n"
"ds_read_b64_tr_b16 v[188:189], v253 offset:10240\n"
"v_exp_f32_e32 v130, v130\n"
"v_exp_f32_e32 v131, v131\n"
"v_mfma_f32_16x16x32_bf16 v[192:195], v[132:135], v[88:91], v[116:119]\n"
"ds_read_b64_tr_b16 v[190:191], v16 offset:10496\n"
"v_exp_f32_e32 v140, v140\n"
"v_exp_f32_e32 v141, v141\n"
"v_exp_f32_e32 v142, v142\n"
"v_mfma_f32_16x16x32_bf16 v[196:199], v[132:135], v[92:95], v[120:123]\n"
"s_waitcnt lgkmcnt(4)\n"
"s_add_u32 s97, s69, 0x60a000\n"
"buffer_load_dwordx4 v[56:59], v0, s[4:7], s97 offen sc1\n"
"v_max_u32_e32 v248, v80, v81\n"
"v_exp_f32_e32 v143, v143\n"
"v_mfma_f32_16x16x32_bf16 v[152:155], v[104:107], v[156:159], v[152:155]\n"
"v_max3_u32 v248, v248, v82, v83\n"
"v_cvt_pk_bf16_f32 v132, v128, v129\n"
"v_cvt_pk_bf16_f32 v133, v130, v131\n"
"v_cvt_pk_bf16_f32 v134, v140, v141\n"
"v_mfma_f32_16x16x32_bf16 v[160:163], v[144:147], v[156:159], v[160:163]\n"
"v_max3_u32 v248, v248, v76, v77\n"
"v_cvt_pk_bf16_f32 v135, v142, v143\n"
"v_exp_f32_e32 v136, v136\n"
"v_max3_u32 v248, v248, v78, v79\n"
"v_exp_f32_e32 v137, v137\n"
"v_mfma_f32_16x16x32_bf16 v[164:167], v[64:67], v[156:159], v[164:167]\n"
"v_exp_f32_e32 v138, v138\n"
"v_max3_u32 v248, v248, v72, v73\n"
"v_exp_f32_e32 v139, v139\n"
"v_mfma_f32_16x16x32_bf16 v[168:171], v[104:107], v[180:183], v[168:171]\n"
"buffer_load_dwordx4 v[60:63], v0, s[4:7], s97 offen offset:1024 sc1\n"
"v_max3_u32 v248, v248, v74, v75\n"
"v_exp_f32_e32 v148, v148\n"
"v_exp_f32_e32 v149, v149\n"
"v_mfma_f32_16x16x32_bf16 v[172:175], v[144:147], v[180:183], v[172:175]\n"
"v_max3_u32 v248, v248, v84, v85\n"
"v_exp_f32_e32 v150, v150\n"
"v_max3_u32 v248, v248, v86, v87\n"
"v_exp_f32_e32 v151, v151\n"
"v_mfma_f32_16x16x32_bf16 v[176:179], v[64:67], v[180:183], v[176:179]\n"
"v_cvt_pk_bf16_f32 v128, v136, v137\n"
"v_cmp_eq_u32_e32 vcc, -1, v248\n"
"v_cvt_pk_bf16_f32 v129, v138, v139\n"
"v_cvt_pk_bf16_f32 v130, v148, v149\n"
"v_cvt_pk_bf16_f32 v131, v150, v151\n"
"s_cbranch_vccnz .Lmk_h105s\n"
".Lmk_h105r:\n"
"s_cmp_lg_u32 s2, 0\n"
"s_cbranch_scc1 .Lmk_s119\n"
"s_barrier\n"
".Lmk_s119:\n"
"ds_read_b128 v[72:75], v15 offset:6144\n"
"ds_read_b128 v[76:79], v252 offset:6400\n"
"s_waitcnt lgkmcnt(2)\n"
"v_mfma_f32_16x16x32_bf16 v[152:155], v[104:107], v[132:135], v[152:155]\n"
"ds_read_b128 v[80:83], v15 offset:4096\n"
"v_exp_f32_e32 v124, v124\n"
"v_exp_f32_e32 v125, v125\n"
"v_exp_f32_e32 v126, v126\n"
"v_mfma_f32_16x16x32_bf16 v[160:163], v[68:71], v[132:135], v[160:163]\n"
"buffer_load_dwordx4 v[64:67], v0, s[4:7], s97 offen offset:2048 sc1\n"
"ds_read_b128 v[84:87], v252 offset:4352\n"
"v_exp_f32_e32 v127, v127\n"
"v_mfma_f32_16x16x32_bf16 v[164:167], v[188:191], v[132:135], v[164:167]\n"
"ds_read_b64_tr_b16 v[132:133], v16 offset:8192\n"
"v_exp_f32_e32 v192, v192\n"
"v_exp_f32_e32 v193, v193\n"
"v_mfma_f32_16x16x32_bf16 v[168:171], v[104:107], v[128:131], v[168:171]\n"
"ds_read_b64_tr_b16 v[134:135], v253 offset:8448\n"
"v_exp_f32_e32 v194, v194\n"
"v_exp_f32_e32 v195, v195\n"
"v_mfma_f32_16x16x32_bf16 v[172:175], v[68:71], v[128:131], v[172:175]\n"
"buffer_load_dwordx4 v[68:71], v0, s[4:7], s97 offen offset:3072 sc1\n"
"v_cvt_pk_bf16_f32 v136, v124, v125\n"
"v_cvt_pk_bf16_f32 v137, v126, v127\n"
"v_cvt_pk_bf16_f32 v138, v192, v193\n"
"v_cvt_pk_bf16_f32 v139, v194, v195\n"
"v_mfma_f32_16x16x32_bf16 v[176:179], v[188:191], v[128:131], v[176:179]\n"
"ds_read_b64_tr_b16 v[124:125], v253 offset:8192\n"
"v_exp_f32_e32 v184, v184\n"
"v_exp_f32_e32 v185, v185\n"
"s_waitcnt lgkmcnt(5)\n"
"v_mfma_f32_16x16x32_bf16 v[128:131], v[72:75], v[88:91], v[116:119]\n"
"ds_read_b64_tr_b16 v[126:127], v16 offset:8448\n"
"v_exp_f32_e32 v186, v186\n"
"v_exp_f32_e32 v187, v187\n"
"v_exp_f32_e32 v196, v196\n"
"v_mfma_f32_16x16x32_bf16 v[140:143], v[72:75], v[92:95], v[120:123]\n"
"buffer_load_dwordx4 v[72:75], v1, s[4:7], s97 offen sc1\n"
"s_waitcnt vmcnt(12)\n"
"v_cmp_ne_u64_e32 vcc, v[246:247], v[18:19]\n"
"v_mfma_f32_16x16x32_bf16 v[144:147], v[76:79], v[88:91], v[116:119]\n"
"v_exp_f32_e32 v197, v197\n"
"v_exp_f32_e32 v198, v198\n"
"v_exp_f32_e32 v199, v199\n"
"v_mfma_f32_16x16x32_bf16 v[148:151], v[76:79], v[92:95], v[120:123]\n"
"s_cbranch_vccnz .Lmk_f87s\n"
".Lmk_f87r:\n"
"v_cvt_pk_bf16_f32 v156, v184, v185\n"
"v_cvt_pk_bf16_f32 v157, v186, v187\n"
"v_cvt_pk_bf16_f32 v158, v196, v197\n"
"v_cvt_pk_bf16_f32 v159, v198, v199\n"
"s_waitcnt lgkmcnt(4)\n"
"v_mfma_f32_16x16x32_bf16 v[180:183], v[80:83], v[88:91], v[116:119]\n"
"ds_read_b128 v[184:187], v15 offset:2048\n"
"v_exp_f32_e32 v128, v128\n"
"v_exp_f32_e32 v129, v129\n"
"v_exp_f32_e32 v130, v130\n"
"v_mfma_f32_16x16x32_bf16 v[188:191], v[80:83], v[92:95], v[120:123]\n"
"buffer_load_dwordx4 v[76:79], v1, s[4:7], s97 offen offset:1024 sc1\n"
"v_exp_f32_e32 v131, v131\n"
"v_mfma_f32_16x16x32_bf16 v[192:195], v[84:87], v[88:91], v[116:119]\n"
"ds_read_b128 v[196:199], v252 offset:2304\n"
"v_exp_f32_e32 v144, v144\n"
"v_exp_f32_e32 v145, v145\n"
"v_mfma_f32_16x16x32_bf16 v[200:203], v[84:87], v[92:95], v[120:123]\n"
"ds_read_b64_tr_b16 v[204:205], v16 offset:6144\n"
"v_exp_f32_e32 v146, v146\n"
"v_exp_f32_e32 v147, v147\n"
"s_waitcnt lgkmcnt(3)\n"
"v_mfma_f32_16x16x32_bf16 v[152:155], v[104:107], v[136:139], v[152:155]\n"
"buffer_load_dwordx4 v[80:83], v1, s[4:7], s97 offen offset:2048 sc1\n"
"v_cvt_pk_bf16_f32 v208, v128, v129\n"
"v_cvt_pk_bf16_f32 v209, v130, v131\n"
"v_cvt_pk_bf16_f32 v210, v144, v145\n"
"v_mfma_f32_16x16x32_bf16 v[160:163], v[132:135], v[136:139], v[160:163]\n"
"ds_read_b64_tr_b16 v[206:207], v253 offset:6400\n"
"v_cvt_pk_bf16_f32 v211, v146, v147\n"
"v_exp_f32_e32 v140, v140\n"
"v_exp_f32_e32 v141, v141\n"
"v_mfma_f32_16x16x32_bf16 v[164:167], v[124:127], v[136:139], v[164:167]\n"
"ds_read_b64_tr_b16 v[128:129], v253 offset:6144\n"
"v_exp_f32_e32 v142, v142\n"
"v_exp_f32_e32 v143, v143\n"
"v_mfma_f32_16x16x32_bf16 v[168:171], v[104:107], v[156:159], v[168:171]\n"
"buffer_load_dwordx4 v[84:87], v1, s[4:7], s97 offen offset:3072 sc1\n"
"v_exp_f32_e32 v148, v148\n"
"v_exp_f32_e32 v149, v149\n"
"v_mfma_f32_16x16x32_bf16 v[172:175], v[132:135], v[156:159], v[172:175]\n"
"ds_read_b64_tr_b16 v[130:131], v16 offset:6400\n"
"v_exp_f32_e32 v150, v150\n"
"v_exp_f32_e32 v151, v151\n"
"v_mfma_f32_16x16x32_bf16 v[176:179], v[124:127], v[156:159], v[176:179]\n"
"s_waitcnt lgkmcnt(4)\n"
"v_cvt_pk_bf16_f32 v124, v140, v141\n"
"v_cvt_pk_bf16_f32 v125, v142, v143\n"
"v_cvt_pk_bf16_f32 v126, v148, v149\n"
"v_cvt_pk_bf16_f32 v127, v150, v151\n"
"v_mfma_f32_16x16x32_bf16 v[132:135], v[184:187], v[88:91], v[116:119]\n"
"ds_read_b128 v[136:139], v15\n"
"v_exp_f32_e32 v180, v180\n"
"v_exp_f32_e32 v181, v181\n"
"v_exp_f32_e32 v182, v182\n"
"v_mfma_f32_16x16x32_bf16 v[140:143], v[184:187], v[92:95], v[120:123]\n"
"s_add_u32 s97, s56, 0x40000\n"
"buffer_load_dwordx4 v[144:147], v0, s[4:7], s97 offen sc1\n"
"v_exp_f32_e32 v183, v183\n"
"v_mfma_f32_16x16x32_bf16 v[148:151], v[196:199], v[88:91], v[116:119]\n"
"ds_read_b128 v[156:159], v252 offset:256\n"
"v_exp_f32_e32 v192, v192\n"
"v_exp_f32_e32 v193, v193\n"
"v_exp_f32_e32 v194, v194\n"
"v_mfma_f32_16x16x32_bf16 v[184:187], v[196:199], v[92:95], v[120:123]\n"
"buffer_load_dwordx4 v[196:199], v0, s[4:7], s97 offen offset:1024 sc1\n"
"ds_read_b64_tr_b16 v[212:213], v16 offset:4096\n"
"v_exp_f32_e32 v195, v195\n"
"s_waitcnt lgkmcnt(3)\n"
"v_mfma_f32_16x16x32_bf16 v[152:155], v[104:107], v[208:211], v[152:155]\n"
"v_cvt_pk_bf16_f32 v216, v180, v181\n"
"v_cvt_pk_bf16_f32 v217, v182, v183\n"
"v_cvt_pk_bf16_f32 v218, v192, v193\n"
"v_cvt_pk_bf16_f32 v219, v194, v195\n"
"v_mfma_f32_16x16x32_bf16 v[160:163], v[204:207], v[208:211], v[160:163]\n"
"buffer_load_dwordx4 v[180:183], v0, s[4:7], s97 offen offset:2048 sc1\n"
"ds_read_b64_tr_b16 v[214:215], v253 offset:4352\n"
"v_exp_f32_e32 v188, v188\n"
"v_exp_f32_e32 v189, v189\n"
"v_mfma_f32_16x16x32_bf16 v[164:167], v[128:131], v[208:211], v[164:167]\n"
"buffer_load_dwordx4 v[192:195], v0, s[4:7], s97 offen offset:3072 sc1\n"
"v_exp_f32_e32 v190, v190\n"
"v_exp_f32_e32 v191, v191\n"
"v_mfma_f32_16x16x32_bf16 v[168:171], v[104:107], v[124:127], v[168:171]\n"
"ds_read_b64_tr_b16 v[208:209], v253 offset:4096\n"
"v_exp_f32_e32 v200, v200\n"
"v_exp_f32_e32 v201, v201\n"
"v_mfma_f32_16x16x32_bf16 v[172:175], v[204:207], v[124:127], v[172:175]\n"
"buffer_load_dwordx4 v[204:207], v1, s[4:7], s97 offen sc1\n"
"ds_read_b64_tr_b16 v[210:211], v16 offset:4352\n"
"v_exp_f32_e32 v202, v202\n"
"v_exp_f32_e32 v203, v203\n"
"v_mfma_f32_16x16x32_bf16 v[176:179], v[128:131], v[124:127], v[176:179]\n"
"s_waitcnt lgkmcnt(4)\n"
"v_cvt_pk_bf16_f32 v124, v188, v189\n"
"v_cvt_pk_bf16_f32 v125, v190, v191\n"
"v_cvt_pk_bf16_f32 v126, v200, v201\n"
"v_cvt_pk_bf16_f32 v127, v202, v203\n"
"v_mfma_f32_16x16x32_bf16 v[128:131], v[136:139], v[88:91], v[116:119]\n"
"ds_read_b64_tr_b16 v[188:189], v16 offset:2048\n"
"ds_read_b64_tr_b16 v[190:191], v253 offset:2304\n"
"buffer_load_dwordx4 v[200:203], v1, s[4:7], s97 offen offset:1024 sc1\n"
"v_exp_f32_e32 v132, v132\n"
"v_exp_f32_e32 v133, v133\n"
"v_mfma_f32_16x16x32_bf16 v[220:223], v[136:139], v[92:95], v[120:123]\n"
"ds_read_b64_tr_b16 v[136:137], v253 offset:2048\n"
"v_exp_f32_e32 v134, v134\n"
"v_exp_f32_e32 v135, v135\n"
"v_mfma_f32_16x16x32_bf16 v[224:227], v[156:159], v[88:91], v[116:119]\n"
"buffer_load_dwordx4 v[116:119], v1, s[4:7], s97 offen offset:2048 sc1\n"
"v_exp_f32_e32 v148, v148\n"
"v_exp_f32_e32 v149, v149\n"
"v_mfma_f32_16x16x32_bf16 v[228:231], v[156:159], v[92:95], v[120:123]\n"
"ds_read_b64_tr_b16 v[138:139], v16 offset:2304\n"
"ds_read_b64_tr_b16 v[120:121], v16 offset:16384\n"
"v_exp_f32_e32 v150, v150\n"
"v_exp_f32_e32 v151, v151\n"
"s_waitcnt lgkmcnt(5)\n"
"v_mfma_f32_16x16x32_bf16 v[152:155], v[104:107], v[216:219], v[152:155]\n"
"v_cvt_pk_bf16_f32 v156, v132, v133\n"
"v_cvt_pk_bf16_f32 v157, v134, v135\n"
"v_cvt_pk_bf16_f32 v158, v148, v149\n"
"v_mfma_f32_16x16x32_bf16 v[160:163], v[212:215], v[216:219], v[160:163]\n"
"buffer_load_dwordx4 v[132:135], v1, s[4:7], s97 offen offset:3072 sc1\n"
"ds_read_b64_tr_b16 v[122:123], v253 offset:16640\n"
"ds_read_b64_tr_b16 v[148:149], v253 offset:16384\n"
"v_cvt_pk_bf16_f32 v159, v150, v151\n"
"v_exp_f32_e32 v140, v140\n"
"v_exp_f32_e32 v141, v141\n"
"v_exp_f32_e32 v142, v142\n"
"v_mfma_f32_16x16x32_bf16 v[164:167], v[208:211], v[216:219], v[164:167]\n"
"s_add_u32 s97, s57, 0x40000\n"
"buffer_load_dwordx4 v[216:219], v0, s[4:7], s97 offen sc1\n"
"ds_read_b64_tr_b16 v[150:151], v16 offset:16640\n"
"v_exp_f32_e32 v143, v143\n"
"v_mfma_f32_16x16x32_bf16 v[168:171], v[104:107], v[124:127], v[168:171]\n"
"v_exp_f32_e32 v184, v184\n"
"v_add_u32_e32 v248, 0x26400, v0\n"
"v_exp_f32_e32 v185, v185\n"
"v_mfma_f32_16x16x32_bf16 v[172:175], v[212:215], v[124:127], v[172:175]\n"
"buffer_load_dwordx4 v[212:215], v0, s[4:7], s97 offen offset:1024 sc1\n"
"ds_read_b128 v[232:235], v248\n"
"v_exp_f32_e32 v186, v186\n"
"v_exp_f32_e32 v187, v187\n"
"v_mfma_f32_16x16x32_bf16 v[176:179], v[208:211], v[124:127], v[176:179]\n"
"ds_read_b128 v[208:211], v248 offset:1024\n"
"v_cvt_pk_bf16_f32 v124, v140, v141\n"
"v_cvt_pk_bf16_f32 v125, v142, v143\n"
"v_cvt_pk_bf16_f32 v126, v184, v185\n"
"v_cvt_pk_bf16_f32 v127, v186, v187\n"
"s_waitcnt lgkmcnt(6)\n"
"v_mfma_f32_16x16x32_bf16 v[152:155], v[104:107], v[156:159], v[152:155]\n"
"ds_read_b64_tr_b16 v[140:141], v16\n"
"buffer_load_dwordx4 v[184:187], v0, s[4:7], s97 offen offset:2048 sc1\n"
"v_cndmask_b32_e64 v128, v96, v128, s[20:21]\n"
"v_cndmask_b32_e64 v129, v97, v129, s[22:23]\n"
"v_cndmask_b32_e64 v130, v98, v130, s[24:25]\n"
"v_cndmask_b32_e64 v131, v99, v131, s[26:27]\n"
"v_cndmask_b32_e64 v224, v108, v224, s[28:29]\n"
"v_cndmask_b32_e64 v225, v109, v225, s[30:31]\n"
"v_cndmask_b32_e64 v226, v110, v226, s[32:33]\n"
"v_cndmask_b32_e64 v227, v111, v227, s[34:35]\n"
"v_exp_f32_e32 v128, v128\n"
"v_mfma_f32_16x16x32_bf16 v[160:163], v[188:191], v[156:159], v[160:163]\n"
"ds_read_b64_tr_b16 v[142:143], v253 offset:256\n"
"buffer_load_dwordx4 v[96:99], v0, s[4:7], s97 offen offset:3072 sc1\n"
"v_exp_f32_e32 v129, v129\n"
"v_exp_f32_e32 v130, v130\n"
"v_exp_f32_e32 v131, v131\n"
"v_exp_f32_e32 v224, v224\n"
"v_mfma_f32_16x16x32_bf16 v[164:167], v[136:139], v[156:159], v[164:167]\n"
"buffer_load_dwordx4 v[156:159], v1, s[4:7], s97 offen sc1\n"
"v_exp_f32_e32 v225, v225\n"
"v_exp_f32_e32 v226, v226\n"
"v_exp_f32_e32 v227, v227\n"
"v_cvt_pk_bf16_f32 v108, v128, v129\n"
"v_cvt_pk_bf16_f32 v109, v130, v131\n"
"v_cvt_pk_bf16_f32 v110, v224, v225\n"
"v_mfma_f32_16x16x32_bf16 v[168:171], v[104:107], v[124:127], v[168:171]\n"
"ds_read_b64_tr_b16 v[128:129], v253\n"
"v_cvt_pk_bf16_f32 v111, v226, v227\n"
"v_cndmask_b32_e64 v220, v100, v220, s[36:37]\n"
"v_cndmask_b32_e64 v221, v101, v221, s[38:39]\n"
"v_cndmask_b32_e64 v222, v102, v222, s[40:41]\n"
"v_cndmask_b32_e64 v223, v103, v223, s[42:43]\n"
"v_cndmask_b32_e64 v228, v112, v228, s[44:45]\n"
"v_cndmask_b32_e64 v229, v113, v229, s[46:47]\n"
"v_cndmask_b32_e64 v230, v114, v230, s[48:49]\n"
"v_cndmask_b32_e64 v231, v115, v231, s[50:51]\n"
"v_mfma_f32_16x16x32_bf16 v[172:175], v[188:191], v[124:127], v[172:175]\n"
"buffer_load_dwordx4 v[100:103], v1, s[4:7], s97 offen offset:1024 sc1\n"
"ds_read_b64_tr_b16 v[130:131], v16 offset:256\n"
"buffer_load_dwordx4 v[112:115], v1, s[4:7], s97 offen offset:2048 sc1\n"
"v_exp_f32_e32 v220, v220\n"
"v_exp_f32_e32 v221, v221\n"
"v_exp_f32_e32 v222, v222\n"
"v_exp_f32_e32 v223, v223\n"
"v_exp_f32_e32 v228, v228\n"
"v_mfma_f32_16x16x32_bf16 v[176:179], v[136:139], v[124:127], v[176:179]\n"
"s_waitcnt lgkmcnt(0)\n"
"v_exp_f32_e32 v229, v229\n"
"v_exp_f32_e32 v230, v230\n"
"v_exp_f32_e32 v231, v231\n"
"v_cvt_pk_bf16_f32 v124, v220, v221\n"
"v_cvt_pk_bf16_f32 v125, v222, v223\n"
"v_cvt_pk_bf16_f32 v126, v228, v229\n"
"v_cvt_pk_bf16_f32 v127, v230, v231\n"
"v_mfma_f32_16x16x32_bf16 v[152:155], v[104:107], v[108:111], v[152:155]\n"
"v_and_b32_e32 v232, v108, v232\n"
"v_and_b32_e32 v233, v109, v233\n"
"v_and_b32_e32 v234, v110, v234\n"
"v_and_b32_e32 v235, v111, v235\n"
"v_mfma_f32_16x16x32_bf16 v[168:171], v[104:107], v[124:127], v[168:171]\n"
"v_xor_b32_e32 v136, v108, v232\n"
"v_xor_b32_e32 v137, v109, v233\n"
"v_xor_b32_e32 v138, v110, v234\n"
"v_xor_b32_e32 v139, v111, v235\n"
"v_mfma_f32_16x16x32_bf16 v[160:163], v[140:143], v[232:235], v[160:163]\n"
"v_and_b32_e32 v208, v124, v208\n"
"v_and_b32_e32 v209, v125, v209\n"
"v_mfma_f32_16x16x32_bf16 v[164:167], v[128:131], v[232:235], v[164:167]\n"
"v_and_b32_e32 v210, v126, v210\n"
"v_and_b32_e32 v211, v127, v211\n"
"v_mfma_f32_16x16x32_bf16 v[160:163], v[120:123], v[136:139], v[160:163]\n"
"v_xor_b32_e32 v108, v124, v208\n"
"v_xor_b32_e32 v109, v125, v209\n"
"v_mfma_f32_16x16x32_bf16 v[164:167], v[148:151], v[136:139], v[164:167]\n"
"v_xor_b32_e32 v110, v126, v210\n"
"v_xor_b32_e32 v111, v127, v211\n"
"v_mfma_f32_16x16x32_bf16 v[172:175], v[140:143], v[208:211], v[172:175]\n"
"buffer_load_dwordx4 v[124:127], v1, s[4:7], s97 offen offset:3072 sc1\n"
"s_add_u32 s97, s58, 0x40000\n"
"buffer_load_dwordx4 v[136:139], v0, s[4:7], s97 offen sc1\n"
"v_mfma_f32_16x16x32_bf16 v[176:179], v[128:131], v[208:211], v[176:179]\n"
"buffer_load_dwordx4 v[128:131], v0, s[4:7], s97 offen offset:1024 sc1\n"
"v_mfma_f32_16x16x32_bf16 v[172:175], v[120:123], v[108:111], v[172:175]\n"
"buffer_load_dwordx4 v[120:123], v0, s[4:7], s97 offen offset:2048 sc1\n"
"v_mfma_f32_16x16x32_bf16 v[176:179], v[148:151], v[108:111], v[176:179]\n"
"buffer_load_dwordx4 v[108:111], v0, s[4:7], s97 offen offset:3072 sc1\n"
"v_max_f32_e32 v248, v152, v168\n"
"v_rcp_f32_e32 v152, v152\n"
"v_rcp_f32_e32 v168, v168\n"
"s_nop 0\n"
"v_max3_f32 v248, v248, v152, v168\n"
"v_cmp_nlt_f32_e32 vcc, v248, v251\n"
"s_cbranch_vccnz .Lmk_b115\n"
".Lmk_k120:\n"
"v_mul_f32_e32 v160, v160, v152\n"
"v_mul_f32_e32 v161, v161, v152\n"
"v_mul_f32_e32 v162, v162, v152\n"
"v_mul_f32_e32 v163, v163, v152\n"
"v_mul_f32_e32 v164, v164, v152\n"
"v_mul_f32_e32 v165, v165, v152\n"
"v_mul_f32_e32 v166, v166, v152\n"
"v_mul_f32_e32 v167, v167, v152\n"
"v_cvt_pk_bf16_f32 v88, v160, v161\n"
"v_cvt_pk_bf16_f32 v89, v162, v163\n"
"v_cvt_pk_bf16_f32 v90, v164, v165\n"
"v_cvt_pk_bf16_f32 v91, v166, v167\n"
"buffer_store_dwordx4 v[88:91], v0, s[4:7], s60 offen sc1\n"
"s_add_u32 s98, s60, 0x8000\n"
"v_mul_f32_e32 v172, v172, v168\n"
"v_mul_f32_e32 v173, v173, v168\n"
"v_mul_f32_e32 v174, v174, v168\n"
"v_mul_f32_e32 v175, v175, v168\n"
"v_mul_f32_e32 v176, v176, v168\n"
"v_mul_f32_e32 v177, v177, v168\n"
"v_mul_f32_e32 v178, v178, v168\n"
"v_mul_f32_e32 v179, v179, v168\n"
"v_cvt_pk_bf16_f32 v88, v172, v173\n"
"v_cvt_pk_bf16_f32 v89, v174, v175\n"
"v_cvt_pk_bf16_f32 v90, v176, v177\n"
"v_cvt_pk_bf16_f32 v91, v178, v179\n"
"buffer_store_dwordx4 v[88:91], v0, s[4:7], s98 offen sc1\n"
"s_waitcnt vmcnt(21)\n"
"; @A G2 b.0\n"
"s_nop 0\n"
"v_mfma_f32_16x16x32_bf16 v[88:91], a[128:131], v[144:147], 0\n"
"v_mfma_f32_16x16x32_bf16 v[92:95], a[132:135], v[144:147], 0\n"
"v_mfma_f32_16x16x32_bf16 v[104:107], a[136:139], v[144:147], 0\n"
"buffer_load_dwordx4 v[140:143], v1, s[4:7], s97 offen sc1\n"
"v_mfma_f32_16x16x32_bf16 v[148:151], a[140:143], v[144:147], 0\n"
"s_waitcnt vmcnt(21)\n"
"v_mfma_f32_16x16x32_bf16 v[88:91], a[144:147], v[196:199], v[88:91]\n"
"v_mfma_f32_16x16x32_bf16 v[92:95], a[148:151], v[196:199], v[92:95]\n"
"v_mfma_f32_16x16x32_bf16 v[104:107], a[152:155], v[196:199], v[104:107]\n"
"buffer_load_dwordx4 v[144:147], v1, s[4:7], s97 offen offset:1024 sc1\n"
"v_mfma_f32_16x16x32_bf16 v[148:151], a[156:159], v[196:199], v[148:151]\n"
"s_waitcnt vmcnt(21)\n"
"v_mfma_f32_16x16x32_bf16 v[88:91], a[160:163], v[180:183], v[88:91]\n"
"v_mfma_f32_16x16x32_bf16 v[92:95], a[164:167], v[180:183], v[92:95]\n"
"v_mfma_f32_16x16x32_bf16 v[104:107], a[168:171], v[180:183], v[104:107]\n"
"buffer_load_dwordx4 v[152:155], v1, s[4:7], s97 offen offset:2048 sc1\n"
"v_mfma_f32_16x16x32_bf16 v[148:151], a[172:175], v[180:183], v[148:151]\n"
"s_waitcnt vmcnt(21)\n"
"v_mfma_f32_16x16x32_bf16 v[88:91], a[176:179], v[192:195], v[88:91]\n"
"v_mfma_f32_16x16x32_bf16 v[92:95], a[180:183], v[192:195], v[92:95]\n"
"v_mfma_f32_16x16x32_bf16 v[104:107], a[184:187], v[192:195], v[104:107]\n"
"buffer_load_dwordx4 v[160:163], v1, s[4:7], s97 offen offset:3072 sc1\n"
"v_mfma_f32_16x16x32_bf16 v[148:151], a[188:191], v[192:195], v[148:151]\n"
"s_waitcnt vmcnt(21)\n"
"v_mfma_f32_16x16x32_bf16 v[88:91], a[192:195], v[204:207], v[88:91]\n"
"s_add_u32 s98, s59, 0x40000\n"
"v_mfma_f32_16x16x32_bf16 v[92:95], a[196:199], v[204:207], v[92:95]\n"
"v_mfma_f32_16x16x32_bf16 v[104:107], a[200:203], v[204:207], v[104:107]\n"
"buffer_load_dwordx4 v[164:167], v0, s[4:7], s98 offen sc1\n"
"v_mfma_f32_16x16x32_bf16 v[148:151], a[204:207], v[204:207], v[148:151]\n"
"s_waitcnt vmcnt(21)\n"
"v_mfma_f32_16x16x32_bf16 v[88:91], a[208:211], v[200:203], v[88:91]\n"
"v_mfma_f32_16x16x32_bf16 v[92:95], a[212:215], v[200:203], v[92:95]\n"
"v_mfma_f32_16x16x32_bf16 v[104:107], a[216:219], v[200:203], v[104:107]\n"
"buffer_load_dwordx4 v[168:171], v0, s[4:7], s98 offen offset:1024 sc1\n"
"v_mfma_f32_16x16x32_bf16 v[148:151], a[220:223], v[200:203], v[148:151]\n"
"s_waitcnt vmcnt(21)\n"
"v_mfma_f32_16x16x32_bf16 v[88:91], a[224:227], v[116:119], v[88:91]\n"
"v_mfma_f32_16x16x32_bf16 v[92:95], a[228:231], v[116:119], v[92:95]\n"
"v_mfma_f32_16x16x32_bf16 v[104:107], a[232:235], v[116:119], v[104:107]\n"
"buffer_load_dwordx4 v[172:175], v0, s[4:7], s98 offen offset:2048 sc1\n"
"v_mfma_f32_16x16x32_bf16 v[148:151], a[236:239], v[116:119], v[148:151]\n"
"s_waitcnt vmcnt(21)\n"
"v_mfma_f32_16x16x32_bf16 v[88:91], a[240:243], v[132:135], v[88:91]\n"
"v_mfma_f32_16x16x32_bf16 v[92:95], a[244:247], v[132:135], v[92:95]\n"
"v_mfma_f32_16x16x32_bf16 v[104:107], a[248:251], v[132:135], v[104:107]\n"
"buffer_load_dwordx4 v[116:119], v0, s[4:7], s98 offen offset:3072 sc1\n"
"v_mfma_f32_16x16x32_bf16 v[148:151], a[252:255], v[132:135], v[148:151]\n"
"s_waitcnt vmcnt(20)\n"
"; @A G2 b.1\n"
"v_mfma_f32_16x16x32_bf16 v[132:135], a[128:131], v[216:219], 0\n"
"s_nop 1\n"
"v_cmp_u_f32_e32 vcc, v88, v88\n"
"v_mfma_f32_16x16x32_bf16 v[176:179], a[132:135], v[216:219], 0\n"
"s_cbranch_vccnz .Lmk_p121s\n"
".Lmk_p121r:\n"
"v_mfma_f32_16x16x32_bf16 v[180:183], a[136:139], v[216:219], 0\n"
"buffer_load_dwordx4 v[188:191], v1, s[4:7], s98 offen sc1\n"
"v_mfma_f32_16x16x32_bf16 v[192:195], a[140:143], v[216:219], 0\n"
"v_mfma_f32_16x16x32_bf16 v[132:135], a[144:147], v[212:215], v[132:135]\n"
"ds_write_b128 v3, v[88:91]\n"
"v_mfma_f32_16x16x32_bf16 v[176:179], a[148:151], v[212:215], v[176:179]\n"
"v_mfma_f32_16x16x32_bf16 v[180:183], a[152:155], v[212:215], v[180:183]\n"
"ds_write_b128 v3, v[92:95] offset:1024\n"
"v_mfma_f32_16x16x32_bf16 v[192:195], a[156:159], v[212:215], v[192:195]\n"
"s_waitcnt vmcnt(19)\n"
"buffer_load_dwordx4 v[88:91], v1, s[4:7], s98 offen offset:1024 sc1\n"
"v_mfma_f32_16x16x32_bf16 v[132:135], a[160:163], v[184:187], v[132:135]\n"
"ds_write_b128 v3, v[104:107] offset:2048\n"
"v_mfma_f32_16x16x32_bf16 v[176:179], a[164:167], v[184:187], v[176:179]\n"
"v_mfma_f32_16x16x32_bf16 v[180:183], a[168:171], v[184:187], v[180:183]\n"
"ds_write_b128 v3, v[148:151] offset:3072\n"
"v_mfma_f32_16x16x32_bf16 v[192:195], a[172:175], v[184:187], v[192:195]\n"
"buffer_load_dwordx4 v[92:95], v1, s[4:7], s98 offen offset:2048 sc1\n"
"v_mfma_f32_16x16x32_bf16 v[132:135], a[176:179], v[96:99], v[132:135]\n"
"v_mfma_f32_16x16x32_bf16 v[176:179], a[180:183], v[96:99], v[176:179]\n"
"ds_read_b128 v[104:107], v2 offset:8192\n"
"ds_read_b128 v[148:151], v2 offset:9216\n"
"v_mfma_f32_16x16x32_bf16 v[180:183], a[184:187], v[96:99], v[180:183]\n"
"v_mfma_f32_16x16x32_bf16 v[192:195], a[188:191], v[96:99], v[192:195]\n"
"s_waitcnt vmcnt(19)\n"
"buffer_load_dwordx4 v[96:99], v1, s[4:7], s98 offen offset:3072 sc1\n"
"v_mfma_f32_16x16x32_bf16 v[132:135], a[192:195], v[156:159], v[132:135]\n"
"v_mfma_f32_16x16x32_bf16 v[176:179], a[196:199], v[156:159], v[176:179]\n"
"v_mfma_f32_16x16x32_bf16 v[180:183], a[200:203], v[156:159], v[180:183]\n"
"s_waitcnt lgkmcnt(0)\n"
"v_lshlrev_b32_e32 v255, 16, v104\n"
"v_and_b32_e32 v254, 0xffff0000, v104\n"
"v_mfma_f32_16x16x32_bf16 v[192:195], a[204:207], v[156:159], v[192:195]\n"
"s_waitcnt lgkmcnt(0)\n"
"s_barrier\n"
"v_mfma_f32_16x16x32_bf16 v[132:135], a[208:211], v[100:103], v[132:135]\n"
"v_lshlrev_b32_e32 v253, 16, v105\n"
"v_and_b32_e32 v252, 0xffff0000, v105\n"
"v_mfma_f32_16x16x32_bf16 v[176:179], a[212:215], v[100:103], v[176:179]\n"
"ds_read_b128 v[156:159], v4\n"
"ds_read_b128 v[184:187], v4 offset:1024\n"
"v_mfma_f32_16x16x32_bf16 v[180:183], a[216:219], v[100:103], v[180:183]\n"
"ds_read_b128 v[196:199], v4 offset:2048\n"
"ds_read_b128 v[200:203], v4 offset:3072\n"
"v_mfma_f32_16x16x32_bf16 v[192:195], a[220:223], v[100:103], v[192:195]\n"
"s_waitcnt vmcnt(18)\n"
"v_lshlrev_b32_e32 v251, 16, v106\n"
"v_and_b32_e32 v250, 0xffff0000, v106\n"
"v_mfma_f32_16x16x32_bf16 v[132:135], a[224:227], v[112:115], v[132:135]\n"
"v_lshlrev_b32_e32 v249, 16, v107\n"
"v_and_b32_e32 v248, 0xffff0000, v107\n"
"v_mfma_f32_16x16x32_bf16 v[176:179], a[228:231], v[112:115], v[176:179]\n"
"v_lshlrev_b32_e32 v247, 16, v148\n"
"v_and_b32_e32 v246, 0xffff0000, v148\n"
"v_mfma_f32_16x16x32_bf16 v[180:183], a[232:235], v[112:115], v[180:183]\n"
"v_lshlrev_b32_e32 v245, 16, v149\n"
"v_and_b32_e32 v244, 0xffff0000, v149\n"
"v_mfma_f32_16x16x32_bf16 v[192:195], a[236:239], v[112:115], v[192:195]\n"
"v_lshlrev_b32_e32 v243, 16, v150\n"
"v_and_b32_e32 v242, 0xffff0000, v150\n"
"v_mfma_f32_16x16x32_bf16 v[132:135], a[240:243], v[124:127], v[132:135]\n"
"s_waitcnt lgkmcnt(0)\n"
"v_lshlrev_b32_e32 v241, 16, v151\n"
"v_and_b32_e32 v240, 0xffff0000, v151\n"
"v_mfma_f32_16x16x32_bf16 v[176:179], a[244:247], v[124:127], v[176:179]\n"
"v_add_f32_e32 v156, v156, v255\n"
"v_add_f32_e32 v157, v157, v254\n"
"v_mfma_f32_16x16x32_bf16 v[180:183], a[248:251], v[124:127], v[180:183]\n"
"v_add_f32_e32 v158, v158, v253\n"
"v_add_f32_e32 v159, v159, v252\n"
"v_mfma_f32_16x16x32_bf16 v[192:195], a[252:255], v[124:127], v[192:195]\n"
"s_waitcnt vmcnt(16)\n"
"v_add_f32_e32 v184, v184, v251\n"
"v_add_f32_e32 v185, v185, v250\n"
"; @A G2 b.2\n"
"v_mfma_f32_16x16x32_bf16 v[100:103], a[128:131], v[136:139], 0\n"
"v_cmp_u_f32_e32 vcc, v132, v132\n"
"v_add_f32_e32 v186, v186, v249\n"
"v_mfma_f32_16x16x32_bf16 v[104:107], a[132:135], v[136:139], 0\n"
"s_cbranch_vccnz .Lmk_p122s\n"
".Lmk_p122r:\n"
"v_add_f32_e32 v187, v187, v248\n"
"v_mfma_f32_16x16x32_bf16 v[112:115], a[136:139], v[136:139], 0\n"
"v_add_f32_e32 v196, v196, v247\n"
"v_add_f32_e32 v197, v197, v246\n"
"v_mfma_f32_16x16x32_bf16 v[124:127], a[140:143], v[136:139], 0\n"
"v_add_f32_e32 v198, v198, v245\n"
"v_add_f32_e32 v199, v199, v244\n"
"v_mfma_f32_16x16x32_bf16 v[100:103], a[144:147], v[128:131], v[100:103]\n"
"ds_write_b128 v3, v[132:135] offset:6144\n"
"v_mfma_f32_16x16x32_bf16 v[104:107], a[148:151], v[128:131], v[104:107]\n"
"v_add_f32_e32 v200, v200, v243\n"
"v_add_f32_e32 v201, v201, v242\n"
"v_mfma_f32_16x16x32_bf16 v[112:115], a[152:155], v[128:131], v[112:115]\n"
"ds_write_b128 v3, v[176:179] offset:7168\n"
"v_mfma_f32_16x16x32_bf16 v[124:127], a[156:159], v[128:131], v[124:127]\n"
"s_waitcnt vmcnt(14)\n"
"v_add_f32_e32 v202, v202, v241\n"
"v_add_f32_e32 v203, v203, v240\n"
"v_mfma_f32_16x16x32_bf16 v[100:103], a[160:163], v[120:123], v[100:103]\n"
"ds_write_b128 v3, v[180:183] offset:8192\n"
"v_mfma_f32_16x16x32_bf16 v[104:107], a[164:167], v[120:123], v[104:107]\n"
"v_mfma_f32_16x16x32_bf16 v[112:115], a[168:171], v[120:123], v[112:115]\n"
"ds_write_b128 v3, v[192:195] offset:9216\n"
"v_mfma_f32_16x16x32_bf16 v[124:127], a[172:175], v[120:123], v[124:127]\n"
"v_mfma_f32_16x16x32_bf16 v[100:103], a[176:179], v[108:111], v[100:103]\n"
"s_add_u32 s98, s56, 0\n"
"buffer_load_dwordx4 v[120:123], v0, s[4:7], s98 offen sc1\n"
"v_mfma_f32_16x16x32_bf16 v[104:107], a[180:183], v[108:111], v[104:107]\n"
"v_mfma_f32_16x16x32_bf16 v[112:115], a[184:187], v[108:111], v[112:115]\n"
"v_mfma_f32_16x16x32_bf16 v[124:127], a[188:191], v[108:111], v[124:127]\n"
"s_waitcnt vmcnt(11)\n"
"buffer_load_dwordx4 v[108:111], v0, s[4:7], s98 offen offset:1024 sc1\n"
"v_mfma_f32_16x16x32_bf16 v[100:103], a[192:195], v[140:143], v[100:103]\n"
"v_mfma_f32_16x16x32_bf16 v[104:107], a[196:199], v[140:143], v[104:107]\n"
"v_mfma_f32_16x16x32_bf16 v[112:115], a[200:203], v[140:143], v[112:115]\n"
"buffer_load_dwordx4 v[128:131], v0, s[4:7], s98 offen offset:2048 sc1\n"
"v_mfma_f32_16x16x32_bf16 v[124:127], a[204:207], v[140:143], v[124:127]\n"
"v_mfma_f32_16x16x32_bf16 v[100:103], a[208:211], v[144:147], v[100:103]\n"
"v_mfma_f32_16x16x32_bf16 v[104:107], a[212:215], v[144:147], v[104:107]\n"
"buffer_load_dwordx4 v[132:135], v0, s[4:7], s98 offen offset:3072 sc1\n"
"v_mfma_f32_16x16x32_bf16 v[112:115], a[216:219], v[144:147], v[112:115]\n"
"v_mfma_f32_16x16x32_bf16 v[124:127], a[220:223], v[144:147], v[124:127]\n"
"s_waitcnt vmcnt(12)\n"
"v_mfma_f32_16x16x32_bf16 v[100:103], a[224:227], v[152:155], v[100:103]\n"
"s_waitcnt vmcnt(10)\n"
"v_max_u32_e32 v255, v164, v165\n"
"v_max3_u32 v255, v255, v166, v167\n"
"v_mfma_f32_16x16x32_bf16 v[104:107], a[228:231], v[152:155], v[104:107]\n"
"s_waitcnt vmcnt(8)\n"
"v_max3_u32 v255, v255, v168, v169\n"
"v_max3_u32 v255, v255, v170, v171\n"
"v_mfma_f32_16x16x32_bf16 v[112:115], a[232:235], v[152:155], v[112:115]\n"
"v_max3_u32 v255, v255, v172, v173\n"
"v_max3_u32 v255, v255, v174, v175\n"
"v_mfma_f32_16x16x32_bf16 v[124:127], a[236:239], v[152:155], v[124:127]\n"
"v_max3_u32 v255, v255, v116, v117\n"
"v_max3_u32 v255, v255, v118, v119\n"
"v_mfma_f32_16x16x32_bf16 v[100:103], a[240:243], v[160:163], v[100:103]\n"
"v_cmp_eq_u32_e32 vcc, -1, v255\n"
"v_mfma_f32_16x16x32_bf16 v[104:107], a[244:247], v[160:163], v[104:107]\n"
"s_cbranch_vccnz .Lmk_q124s\n"
".Lmk_q124r:\n"
"v_mfma_f32_16x16x32_bf16 v[112:115], a[248:251], v[160:163], v[112:115]\n"
"buffer_load_dwordx4 v[136:139], v1, s[4:7], s98 offen sc1\n"
"v_mfma_f32_16x16x32_bf16 v[124:127], a[252:255], v[160:163], v[124:127]\n"
"; @A G2 b.3\n"
"v_mfma_f32_16x16x32_bf16 v[156:159], a[128:131], v[164:167], v[156:159]\n"
"s_nop 0\n"
"v_cmp_u_f32_e32 vcc, v100, v100\n"
"v_mfma_f32_16x16x32_bf16 v[184:187], a[132:135], v[164:167], v[184:187]\n"
"s_cbranch_vccnz .Lmk_p123s\n"
".Lmk_p123r:\n"
"v_mfma_f32_16x16x32_bf16 v[196:199], a[136:139], v[164:167], v[196:199]\n"
"buffer_load_dwordx4 v[140:143], v1, s[4:7], s98 offen offset:1024 sc1\n"
"v_mfma_f32_16x16x32_bf16 v[200:203], a[140:143], v[164:167], v[200:203]\n"
"v_mfma_f32_16x16x32_bf16 v[156:159], a[144:147], v[168:171], v[156:159]\n"
"ds_write_b128 v3, v[100:103] offset:12288\n"
"v_mfma_f32_16x16x32_bf16 v[184:187], a[148:151], v[168:171], v[184:187]\n"
"buffer_load_dwordx4 v[100:103], v1, s[4:7], s98 offen offset:2048 sc1\n"
"v_mfma_f32_16x16x32_bf16 v[196:199], a[152:155], v[168:171], v[196:199]\n"
"ds_write_b128 v3, v[104:107] offset:13312\n"
"v_mfma_f32_16x16x32_bf16 v[200:203], a[156:159], v[168:171], v[200:203]\n"
"v_mfma_f32_16x16x32_bf16 v[156:159], a[160:163], v[172:175], v[156:159]\n"
"s_waitcnt vmcnt(9)\n"
"v_max_u32_e32 v255, v188, v189\n"
"v_max3_u32 v255, v255, v190, v191\n"
"v_mfma_f32_16x16x32_bf16 v[184:187], a[164:167], v[172:175], v[184:187]\n"
"s_waitcnt vmcnt(7)\n"
"v_max3_u32 v255, v255, v88, v89\n"
"v_max3_u32 v255, v255, v90, v91\n"
"v_mfma_f32_16x16x32_bf16 v[196:199], a[168:171], v[172:175], v[196:199]\n"
"v_max3_u32 v255, v255, v92, v93\n"
"v_max3_u32 v255, v255, v94, v95\n"
"v_mfma_f32_16x16x32_bf16 v[200:203], a[172:175], v[172:175], v[200:203]\n"
"v_max3_u32 v255, v255, v96, v97\n"
"v_max3_u32 v255, v255, v98, v99\n"
"v_mfma_f32_16x16x32_bf16 v[156:159], a[176:179], v[116:119], v[156:159]\n"
"v_cmp_eq_u32_e32 vcc, -1, v255\n"
"v_mfma_f32_16x16x32_bf16 v[184:187], a[180:183], v[116:119], v[184:187]\n"
"s_cbranch_vccnz .Lmk_q125s\n"
".Lmk_q125r:\n"
"v_mfma_f32_16x16x32_bf16 v[196:199], a[184:187], v[116:119], v[196:199]\n"
"ds_write_b128 v3, v[112:115] offset:14336\n"
"v_mfma_f32_16x16x32_bf16 v[200:203], a[188:191], v[116:119], v[200:203]\n"
"buffer_load_dwordx4 v[104:107], v1, s[4:7], s98 offen offset:3072 sc1\n"
"v_mfma_f32_16x16x32_bf16 v[156:159], a[192:195], v[188:191], v[156:159]\n"
"ds_write_b128 v3, v[124:127] offset:15360\n"
"v_mfma_f32_16x16x32_bf16 v[184:187], a[196:199], v[188:191], v[184:187]\n"
"s_add_u32 s98, s57, 0\n"
"v_mfma_f32_16x16x32_bf16 v[196:199], a[200:203], v[188:191], v[196:199]\n"
"buffer_load_dwordx4 v[112:115], v0, s[4:7], s98 offen sc1\n"
"v_mfma_f32_16x16x32_bf16 v[200:203], a[204:207], v[188:191], v[200:203]\n"
"s_waitcnt lgkmcnt(0)\n"
"s_barrier\n"
"v_mfma_f32_16x16x32_bf16 v[156:159], a[208:211], v[88:91], v[156:159]\n"
"ds_read_b128 v[116:119], v5\n"
"ds_read_b128 v[124:127], v6\n"
"v_mfma_f32_16x16x32_bf16 v[184:187], a[212:215], v[88:91], v[184:187]\n"
"buffer_load_dwordx4 v[144:147], v0, s[4:7], s98 offen offset:1024 sc1\n"
"v_mfma_f32_16x16x32_bf16 v[196:199], a[216:219], v[88:91], v[196:199]\n"
"ds_read_b128 v[148:151], v5 offset:1024\n"
"ds_read_b128 v[152:155], v6 offset:1024\n"
"v_mfma_f32_16x16x32_bf16 v[200:203], a[220:223], v[88:91], v[200:203]\n"
"ds_read_b128 v[88:91], v5 offset:2048\n"
"ds_read_b128 v[160:163], v6 offset:2048\n"
"v_mfma_f32_16x16x32_bf16 v[156:159], a[224:227], v[92:95], v[156:159]\n"
"buffer_load_dwordx4 v[164:167], v0, s[4:7], s98 offen offset:2048 sc1\n"
"v_mfma_f32_16x16x32_bf16 v[184:187], a[228:231], v[92:95], v[184:187]\n"
"ds_read_b128 v[168:171], v5 offset:3072\n"
"ds_read_b128 v[172:175], v6 offset:3072\n"
"v_mfma_f32_16x16x32_bf16 v[196:199], a[232:235], v[92:95], v[196:199]\n"
"v_mfma_f32_16x16x32_bf16 v[200:203], a[236:239], v[92:95], v[200:203]\n"
"buffer_load_dwordx4 v[92:95], v0, s[4:7], s98 offen offset:3072 sc1\n"
"v_mfma_f32_16x16x32_bf16 v[156:159], a[240:243], v[96:99], v[156:159]\n"
"s_waitcnt lgkmcnt(4)\n"
"v_add_f32_e32 v116, v116, v124\n"
"v_add_f32_e32 v117, v117, v125\n"
"v_mfma_f32_16x16x32_bf16 v[184:187], a[244:247], v[96:99], v[184:187]\n"
"v_add_f32_e32 v118, v118, v126\n"
"v_add_f32_e32 v119, v119, v127\n"
"v_mfma_f32_16x16x32_bf16 v[196:199], a[248:251], v[96:99], v[196:199]\n"
"buffer_load_dwordx4 v[124:127], v1, s[4:7], s98 offen sc1\n"
"v_mfma_f32_16x16x32_bf16 v[200:203], a[252:255], v[96:99], v[200:203]\n"
"s_waitcnt lgkmcnt(0)\n"
"v_add_f32_e32 v148, v148, v152\n"
"v_add_f32_e32 v149, v149, v153\n"
"v_add_f32_e32 v150, v150, v154\n"
"v_add_f32_e32 v151, v151, v155\n"
"v_add_f32_e32 v88, v88, v160\n"
"v_add_f32_e32 v89, v89, v161\n"
"v_add_f32_e32 v90, v90, v162\n"
"v_add_f32_e32 v91, v91, v163\n"
"v_add_f32_e32 v168, v168, v172\n"
"v_add_f32_e32 v169, v169, v173\n"
"v_add_f32_e32 v170, v170, v174\n"
"v_add_f32_e32 v171, v171, v175\n"
"buffer_load_dwordx4 v[152:155], v1, s[4:7], s98 offen offset:1024 sc1\n"
"buffer_load_dwordx4 v[160:163], v1, s[4:7], s98 offen offset:2048 sc1\n"
"buffer_load_dwordx4 v[172:175], v1, s[4:7], s98 offen offset:3072 sc1\n"
"; @A fin b\n"
"s_add_u32 s98, s61, 0x440000\n"
"v_mov_b32_e32 v96, 0x3f803f80\n"
"v_mov_b32_e32 v97, 0x3f803f80\n"
"v_mov_b32_e32 v98, 0x3f803f80\n"
"v_mov_b32_e32 v99, 0x3f803f80\n"
"v_add_f32_e32 v116, v156, v116\n"
"v_add_f32_e32 v117, v157, v117\n"
"v_add_f32_e32 v118, v158, v118\n"
"v_add_f32_e32 v119, v159, v119\n"
"v_add_f32_e32 v148, v184, v148\n"
"v_add_f32_e32 v149, v185, v149\n"
"v_add_f32_e32 v150, v186, v150\n"
"v_add_f32_e32 v151, v187, v151\n"
"v_cvt_pk_bf16_f32 v156, v116, v117\n"
"v_cvt_pk_bf16_f32 v157, v118, v119\n"
"v_cvt_pk_bf16_f32 v158, v148, v149\n"
"v_cvt_pk_bf16_f32 v159, v150, v151\n"
"buffer_store_dwordx4 v[156:159], v0, s[4:7], s98 offen sc1\n"
"ds_write_b128 v2, v[156:159] offset:8192\n"
"s_add_u32 s98, s61, 0x440000\n"
"v_add_f32_e32 v88, v196, v88\n"
"v_add_f32_e32 v89, v197, v89\n"
"v_add_f32_e32 v90, v198, v90\n"
"v_add_f32_e32 v91, v199, v91\n"
"v_add_f32_e32 v168, v200, v168\n"
"v_add_f32_e32 v169, v201, v169\n"
"v_add_f32_e32 v170, v202, v170\n"
"v_add_f32_e32 v171, v203, v171\n"
"v_cvt_pk_bf16_f32 v116, v88, v89\n"
"v_cvt_pk_bf16_f32 v117, v90, v91\n"
"v_cvt_pk_bf16_f32 v118, v168, v169\n"
"v_cvt_pk_bf16_f32 v119, v170, v171\n"
"v_mfma_f32_16x16x32_bf16 v[88:91], v[96:99], v[156:159], 0\n"
"buffer_store_dwordx4 v[116:119], v0, s[4:7], s98 offen offset:1024 sc1\n"
"ds_write_b128 v2, v[116:119] offset:9216\n"
"v_mfma_f32_16x16x32_bf16 v[148:151], v[156:159], v[156:159], 0\n"
"v_mfma_f32_16x16x32_bf16 v[88:91], v[96:99], v[116:119], v[88:91]\n"
"v_mfma_f32_16x16x32_bf16 v[148:151], v[116:119], v[116:119], v[148:151]\n"
"s_mov_b32 s96, 0xaaaaaaaa\n"
"s_mov_b32 s97, 0xaaaaaaaa\n"
"s_nop 5\n"
"v_cndmask_b32_e64 v148, v148, v149, s[96:97]\n"
"v_cndmask_b32_e64 v150, v150, v151, s[96:97]\n"
"s_mov_b32 s96, 0xcccccccc\n"
"s_mov_b32 s97, 0xcccccccc\n"
"v_cndmask_b32_e64 v89, v148, v150, s[96:97]\n"
"s_mov_b32 s96, 0xf0000f\n"
"s_mov_b32 s97, 0xf0000f00\n"
"s_add_u32 s98, s63, 0x44000\n"
"v_cndmask_b32_e64 v88, v88, v89, s[96:97]\n"
"buffer_store_dword v88, v14, s[4:7], s98 offen sc1\n"
"s_waitcnt vmcnt(18)\n"
"; @A G2 a.0\n"
"v_mfma_f32_16x16x32_bf16 v[88:91], a[128:131], v[120:123], 0\n"
"s_add_u32 s98, s58, 0\n"
"buffer_load_dwordx4 v[116:119], v0, s[4:7], s98 offen sc1\n"
"v_mfma_f32_16x16x32_bf16 v[96:99], a[132:135], v[120:123], 0\n"
"v_mfma_f32_16x16x32_bf16 v[148:151], a[136:139], v[120:123], 0\n"
"v_mfma_f32_16x16x32_bf16 v[156:159], a[140:143], v[120:123], 0\n"
"s_waitcnt vmcnt(18)\n"
"v_mfma_f32_16x16x32_bf16 v[88:91], a[144:147], v[108:111], v[88:91]\n"
"buffer_load_dwordx4 v[120:123], v0, s[4:7], s98 offen offset:1024 sc1\n"
"v_mfma_f32_16x16x32_bf16 v[96:99], a[148:151], v[108:111], v[96:99]\n"
"v_mfma_f32_16x16x32_bf16 v[148:151], a[152:155], v[108:111], v[148:151]\n"
"v_mfma_f32_16x16x32_bf16 v[156:159], a[156:159], v[108:111], v[156:159]\n"
"s_waitcnt vmcnt(18)\n"
"v_mfma_f32_16x16x32_bf16 v[88:91], a[160:163], v[128:131], v[88:91]\n"
"buffer_load_dwordx4 v[168:171], v0, s[4:7], s98 offen offset:2048 sc1\n"
"v_mfma_f32_16x16x32_bf16 v[96:99], a[164:167], v[128:131], v[96:99]\n"
"v_mfma_f32_16x16x32_bf16 v[148:151], a[168:171], v[128:131], v[148:151]\n"
"v_mfma_f32_16x16x32_bf16 v[156:159], a[172:175], v[128:131], v[156:159]\n"
"s_waitcnt vmcnt(18)\n"
"v_mfma_f32_16x16x32_bf16 v[88:91], a[176:179], v[132:135], v[88:91]\n"
"buffer_load_dwordx4 v[128:131], v0, s[4:7], s98 offen offset:3072 sc1\n"
"v_mfma_f32_16x16x32_bf16 v[96:99], a[180:183], v[132:135], v[96:99]\n"
"v_mfma_f32_16x16x32_bf16 v[148:151], a[184:187], v[132:135], v[148:151]\n"
"v_mfma_f32_16x16x32_bf16 v[156:159], a[188:191], v[132:135], v[156:159]\n"
"s_waitcnt vmcnt(18)\n"
"v_mfma_f32_16x16x32_bf16 v[88:91], a[192:195], v[136:139], v[88:91]\n"
"buffer_load_dwordx4 v[132:135], v1, s[4:7], s98 offen sc1\n"
"v_mfma_f32_16x16x32_bf16 v[96:99], a[196:199], v[136:139], v[96:99]\n"
"v_mfma_f32_16x16x32_bf16 v[148:151], a[200:203], v[136:139], v[148:151]\n"
"v_mfma_f32_16x16x32_bf16 v[156:159], a[204:207], v[136:139], v[156:159]\n"
"s_waitcnt vmcnt(18)\n"
"v_mfma_f32_16x16x32_bf16 v[88:91], a[208:211], v[140:143], v[88:91]\n"
"buffer_load_dwordx4 v[136:139], v1, s[4:7], s98 offen offset:1024 sc1\n"
"v_mfma_f32_16x16x32_bf16 v[96:99], a[212:215], v[140:143], v[96:99]\n"
"v_mfma_f32_16x16x32_bf16 v[148:151], a[216:219], v[140:143], v[148:151]\n"
"v_mfma_f32_16x16x32_bf16 v[156:159], a[220:223], v[140:143], v[156:159]\n"
"s_waitcnt vmcnt(18)\n"
"v_mfma_f32_16x16x32_bf16 v[88:91], a[224:227], v[100:103], v[88:91]\n"
"buffer_load_dwordx4 v[140:143], v1, s[4:7], s98 offen offset:2048 sc1\n"
"v_mfma_f32_16x16x32_bf16 v[96:99], a[228:231], v[100:103], v[96:99]\n"
"v_mfma_f32_16x16x32_bf16 v[148:151], a[232:235], v[100:103], v[148:151]\n"
"v_mfma_f32_16x16x32_bf16 v[156:159], a[236:239], v[100:103], v[156:159]\n"
"s_waitcnt vmcnt(18)\n"
"v_mfma_f32_16x16x32_bf16 v[88:91], a[240:243], v[104:107], v[88:91]\n"
"buffer_load_dwordx4 v[176:179], v1, s[4:7], s98 offen offset:3072 sc1\n"
"v_mfma_f32_16x16x32_bf16 v[96:99], a[244:247], v[104:107], v[96:99]\n"
"v_mfma_f32_16x16x32_bf16 v[148:151], a[248:251], v[104:107], v[148:151]\n"
"s_add_u32 s98, s59, 0\n"
"v_mfma_f32_16x16x32_bf16 v[156:159], a[252:255], v[104:107], v[156:159]\n"
"s_waitcnt vmcnt(17)\n"
"; @A G2 a.1\n"
"v_mfma_f32_16x16x32_bf16 v[108:111], a[128:131], v[112:115], 0\n"
"s_nop 0\n"
"v_cmp_u_f32_e32 vcc, v88, v88\n"
"v_mfma_f32_16x16x32_bf16 v[180:183], a[132:135], v[112:115], 0\n"
"s_cbranch_vccnz .Lmk_p126s\n"
".Lmk_p126r:\n"
"v_mfma_f32_16x16x32_bf16 v[184:187], a[136:139], v[112:115], 0\n"
"buffer_load_dwordx4 v[188:191], v0, s[4:7], s98 offen sc1\n"
"v_mfma_f32_16x16x32_bf16 v[192:195], a[140:143], v[112:115], 0\n"
"v_mfma_f32_16x16x32_bf16 v[108:111], a[144:147], v[144:147], v[108:111]\n"
"ds_write_b128 v3, v[88:91]\n"
"v_mfma_f32_16x16x32_bf16 v[180:183], a[148:151], v[144:147], v[180:183]\n"
"v_mfma_f32_16x16x32_bf16 v[184:187], a[152:155], v[144:147], v[184:187]\n"
"ds_write_b128 v3, v[96:99] offset:1024\n"
"v_mfma_f32_16x16x32_bf16 v[192:195], a[156:159], v[144:147], v[192:195]\n"
"s_waitcnt vmcnt(16)\n"
"buffer_load_dwordx4 v[144:147], v0, s[4:7], s98 offen offset:1024 sc1\n"
"v_mfma_f32_16x16x32_bf16 v[108:111], a[160:163], v[164:167], v[108:111]\n"
"ds_write_b128 v3, v[148:151] offset:2048\n"
"v_mfma_f32_16x16x32_bf16 v[180:183], a[164:167], v[164:167], v[180:183]\n"
"v_mfma_f32_16x16x32_bf16 v[184:187], a[168:171], v[164:167], v[184:187]\n"
"ds_write_b128 v3, v[156:159] offset:3072\n"
"v_mfma_f32_16x16x32_bf16 v[192:195], a[172:175], v[164:167], v[192:195]\n"
"buffer_load_dwordx4 v[148:151], v0, s[4:7], s98 offen offset:2048 sc1\n"
"v_mfma_f32_16x16x32_bf16 v[108:111], a[176:179], v[92:95], v[108:111]\n"
"v_mfma_f32_16x16x32_bf16 v[180:183], a[180:183], v[92:95], v[180:183]\n"
"ds_read_b128 v[104:107], v2\n"
"ds_read_b128 v[112:115], v2 offset:1024\n"
"v_mfma_f32_16x16x32_bf16 v[184:187], a[184:187], v[92:95], v[184:187]\n"
"v_mfma_f32_16x16x32_bf16 v[192:195], a[188:191], v[92:95], v[192:195]\n"
"s_waitcnt vmcnt(16)\n"
"buffer_load_dwordx4 v[156:159], v0, s[4:7], s98 offen offset:3072 sc1\n"
"v_mfma_f32_16x16x32_bf16 v[108:111], a[192:195], v[124:127], v[108:111]\n"
"v_mfma_f32_16x16x32_bf16 v[180:183], a[196:199], v[124:127], v[180:183]\n"
"v_mfma_f32_16x16x32_bf16 v[184:187], a[200:203], v[124:127], v[184:187]\n"
"s_waitcnt lgkmcnt(0)\n"
"v_lshlrev_b32_e32 v255, 16, v104\n"
"v_and_b32_e32 v254, 0xffff0000, v104\n"
"v_mfma_f32_16x16x32_bf16 v[192:195], a[204:207], v[124:127], v[192:195]\n"
"s_waitcnt lgkmcnt(0)\n"
"s_barrier\n"
"v_mfma_f32_16x16x32_bf16 v[108:111], a[208:211], v[152:155], v[108:111]\n"
"buffer_load_dwordx4 v[164:167], v1, s[4:7], s98 offen sc1\n"
"v_mfma_f32_16x16x32_bf16 v[180:183], a[212:215], v[152:155], v[180:183]\n"
"ds_read_b128 v[88:91], v4\n"
"ds_read_b128 v[92:95], v4 offset:1024\n"
"v_mfma_f32_16x16x32_bf16 v[184:187], a[216:219], v[152:155], v[184:187]\n"
"ds_read_b128 v[96:99], v4 offset:2048\n"
"ds_read_b128 v[100:103], v4 offset:3072\n"
"v_mfma_f32_16x16x32_bf16 v[192:195], a[220:223], v[152:155], v[192:195]\n"
"s_waitcnt vmcnt(16)\n"
"v_lshlrev_b32_e32 v253, 16, v105\n"
"v_and_b32_e32 v252, 0xffff0000, v105\n"
"v_mfma_f32_16x16x32_bf16 v[108:111], a[224:227], v[160:163], v[108:111]\n"
"buffer_load_dwordx4 v[152:155], v1, s[4:7], s98 offen offset:1024 sc1\n"
"v_mfma_f32_16x16x32_bf16 v[180:183], a[228:231], v[160:163], v[180:183]\n"
"v_lshlrev_b32_e32 v251, 16, v106\n"
"v_and_b32_e32 v250, 0xffff0000, v106\n"
"v_mfma_f32_16x16x32_bf16 v[184:187], a[232:235], v[160:163], v[184:187]\n"
"v_lshlrev_b32_e32 v249, 16, v107\n"
"v_and_b32_e32 v248, 0xffff0000, v107\n"
"v_mfma_f32_16x16x32_bf16 v[192:195], a[236:239], v[160:163], v[192:195]\n"
"v_lshlrev_b32_e32 v247, 16, v112\n"
"v_and_b32_e32 v246, 0xffff0000, v112\n"
"v_mfma_f32_16x16x32_bf16 v[108:111], a[240:243], v[172:175], v[108:111]\n"
"buffer_load_dwordx4 v[196:199], v1, s[4:7], s98 offen offset:2048 sc1\n"
"v_mfma_f32_16x16x32_bf16 v[180:183], a[244:247], v[172:175], v[180:183]\n"
"v_lshlrev_b32_e32 v245, 16, v113\n"
"v_and_b32_e32 v244, 0xffff0000, v113\n"
"v_mfma_f32_16x16x32_bf16 v[184:187], a[248:251], v[172:175], v[184:187]\n"
"v_lshlrev_b32_e32 v243, 16, v114\n"
"v_and_b32_e32 v242, 0xffff0000, v114\n"
"v_mfma_f32_16x16x32_bf16 v[192:195], a[252:255], v[172:175], v[192:195]\n"
"s_waitcnt vmcnt(13)\n"
"v_lshlrev_b32_e32 v241, 16, v115\n"
"v_and_b32_e32 v240, 0xffff0000, v115\n"
"; @A G2 a.2\n"
"v_mfma_f32_16x16x32_bf16 v[160:163], a[128:131], v[116:119], 0\n"
"s_add_u32 s97, s52, 0x440000\n"
"v_cmp_u_f32_e32 vcc, v108, v108\n"
"s_waitcnt lgkmcnt(0)\n"
"v_mfma_f32_16x16x32_bf16 v[172:175], a[132:135], v[116:119], 0\n"
"s_cbranch_vccnz .Lmk_p127s\n"
".Lmk_p127r:\n"
"v_add_f32_e32 v88, v88, v255\n"
"v_mfma_f32_16x16x32_bf16 v[200:203], a[136:139], v[116:119], 0\n"
"buffer_load_dwordx4 v[204:207], v1, s[4:7], s98 offen offset:3072 sc1\n"
"v_mfma_f32_16x16x32_bf16 v[208:211], a[140:143], v[116:119], 0\n"
"buffer_load_dwordx4 v[104:107], v0, s[4:7], s97 offen sc1\n"
"v_mfma_f32_16x16x32_bf16 v[160:163], a[144:147], v[120:123], v[160:163]\n"
"ds_write_b128 v3, v[108:111] offset:6144\n"
"v_mfma_f32_16x16x32_bf16 v[172:175], a[148:151], v[120:123], v[172:175]\n"
"v_add_f32_e32 v89, v89, v254\n"
"v_add_f32_e32 v90, v90, v253\n"
"v_mfma_f32_16x16x32_bf16 v[200:203], a[152:155], v[120:123], v[200:203]\n"
"ds_write_b128 v3, v[180:183] offset:7168\n"
"v_mfma_f32_16x16x32_bf16 v[208:211], a[156:159], v[120:123], v[208:211]\n"
"s_waitcnt vmcnt(13)\n"
"buffer_load_dwordx4 v[108:111], v0, s[4:7], s97 offen offset:1024 sc1\n"
"v_mfma_f32_16x16x32_bf16 v[160:163], a[160:163], v[168:171], v[160:163]\n"
"ds_write_b128 v3, v[184:187] offset:8192\n"
"v_mfma_f32_16x16x32_bf16 v[172:175], a[164:167], v[168:171], v[172:175]\n"
"v_add_f32_e32 v91, v91, v252\n"
"v_add_f32_e32 v92, v92, v251\n"
"v_mfma_f32_16x16x32_bf16 v[200:203], a[168:171], v[168:171], v[200:203]\n"
"ds_write_b128 v3, v[192:195] offset:9216\n"
"v_mfma_f32_16x16x32_bf16 v[208:211], a[172:175], v[168:171], v[208:211]\n"
"buffer_load_dwordx4 v[112:115], v0, s[4:7], s97 offen offset:2048 sc1\n"
"v_mfma_f32_16x16x32_bf16 v[160:163], a[176:179], v[128:131], v[160:163]\n"
"v_add_f32_e32 v93, v93, v250\n"
"v_add_f32_e32 v94, v94, v249\n"
"v_mfma_f32_16x16x32_bf16 v[172:175], a[180:183], v[128:131], v[172:175]\n"
"v_add_f32_e32 v95, v95, v248\n"
"v_add_f32_e32 v96, v96, v247\n"
"v_mfma_f32_16x16x32_bf16 v[200:203], a[184:187], v[128:131], v[200:203]\n"
"v_add_f32_e32 v97, v97, v246\n"
"v_add_f32_e32 v98, v98, v245\n"
"v_mfma_f32_16x16x32_bf16 v[208:211], a[188:191], v[128:131], v[208:211]\n"
"s_waitcnt vmcnt(13)\n"
"buffer_load_dwordx4 v[116:119], v0, s[4:7], s97 offen offset:3072 sc1\n"
"v_mfma_f32_16x16x32_bf16 v[160:163], a[192:195], v[132:135], v[160:163]\n"
"v_add_f32_e32 v99, v99, v244\n"
"v_add_f32_e32 v100, v100, v243\n"
"v_mfma_f32_16x16x32_bf16 v[172:175], a[196:199], v[132:135], v[172:175]\n"
"v_add_f32_e32 v101, v101, v242\n"
"v_add_f32_e32 v102, v102, v241\n"
"v_mfma_f32_16x16x32_bf16 v[200:203], a[200:203], v[132:135], v[200:203]\n"
"v_add_f32_e32 v103, v103, v240\n"
"v_mfma_f32_16x16x32_bf16 v[208:211], a[204:207], v[132:135], v[208:211]\n"
"buffer_load_dwordx4 v[120:123], v1, s[4:7], s97 offen sc1\n"
"v_mfma_f32_16x16x32_bf16 v[160:163], a[208:211], v[136:139], v[160:163]\n"
"v_mfma_f32_16x16x32_bf16 v[172:175], a[212:215], v[136:139], v[172:175]\n"
"v_mfma_f32_16x16x32_bf16 v[200:203], a[216:219], v[136:139], v[200:203]\n"
"v_mfma_f32_16x16x32_bf16 v[208:211], a[220:223], v[136:139], v[208:211]\n"
"s_waitcnt vmcnt(13)\n"
"buffer_load_dwordx4 v[124:127], v1, s[4:7], s97 offen offset:1024 sc1\n"
"v_mfma_f32_16x16x32_bf16 v[160:163], a[224:227], v[140:143], v[160:163]\n"
"s_waitcnt vmcnt(12)\n"
"v_max_u32_e32 v255, v188, v189\n"
"v_max3_u32 v255, v255, v190, v191\n"
"v_mfma_f32_16x16x32_bf16 v[172:175], a[228:231], v[140:143], v[172:175]\n"
"s_waitcnt vmcnt(10)\n"
"v_max3_u32 v255, v255, v144, v145\n"
"v_max3_u32 v255, v255, v146, v147\n"
"v_mfma_f32_16x16x32_bf16 v[200:203], a[232:235], v[140:143], v[200:203]\n"
"v_max3_u32 v255, v255, v148, v149\n"
"v_max3_u32 v255, v255, v150, v151\n"
"v_mfma_f32_16x16x32_bf16 v[208:211], a[236:239], v[140:143], v[208:211]\n"
"v_max3_u32 v255, v255, v156, v157\n"
"v_max3_u32 v255, v255, v158, v159\n"
"v_mfma_f32_16x16x32_bf16 v[160:163], a[240:243], v[176:179], v[160:163]\n"
"v_cmp_eq_u32_e32 vcc, -1, v255\n"
"v_mfma_f32_16x16x32_bf16 v[172:175], a[244:247], v[176:179], v[172:175]\n"
"s_cbranch_vccnz .Lmk_q129s\n"
".Lmk_q129r:\n"
"v_mfma_f32_16x16x32_bf16 v[200:203], a[248:251], v[176:179], v[200:203]\n"
"buffer_load_dwordx4 v[128:131], v1, s[4:7], s97 offen offset:2048 sc1\n"
"v_mfma_f32_16x16x32_bf16 v[208:211], a[252:255], v[176:179], v[208:211]\n"
"; @A G2 a.3\n"
"v_mfma_f32_16x16x32_bf16 v[88:91], a[128:131], v[188:191], v[88:91]\n"
"s_nop 0\n"
"v_cmp_u_f32_e32 vcc, v160, v160\n"
"v_mfma_f32_16x16x32_bf16 v[92:95], a[132:135], v[188:191], v[92:95]\n"
"s_cbranch_vccnz .Lmk_p128s\n"
".Lmk_p128r:\n"
"v_mfma_f32_16x16x32_bf16 v[96:99], a[136:139], v[188:191], v[96:99]\n"
"buffer_load_dwordx4 v[132:135], v1, s[4:7], s97 offen offset:3072 sc1\n"
"v_mfma_f32_16x16x32_bf16 v[100:103], a[140:143], v[188:191], v[100:103]\n"
"v_mfma_f32_16x16x32_bf16 v[88:91], a[144:147], v[144:147], v[88:91]\n"
"ds_write_b128 v3, v[160:163] offset:12288\n"
"v_mfma_f32_16x16x32_bf16 v[92:95], a[148:151], v[144:147], v[92:95]\n"
"s_add_u32 s98, s53, 0x440000\n"
"v_mfma_f32_16x16x32_bf16 v[96:99], a[152:155], v[144:147], v[96:99]\n"
"ds_write_b128 v3, v[172:175] offset:13312\n"
"v_mfma_f32_16x16x32_bf16 v[100:103], a[156:159], v[144:147], v[100:103]\n"
"buffer_load_dwordx4 v[136:139], v0, s[4:7], s98 offen sc1\n"
"v_mfma_f32_16x16x32_bf16 v[88:91], a[160:163], v[148:151], v[88:91]\n"
"s_waitcnt vmcnt(11)\n"
"v_max_u32_e32 v255, v164, v165\n"
"v_max3_u32 v255, v255, v166, v167\n"
"v_mfma_f32_16x16x32_bf16 v[92:95], a[164:167], v[148:151], v[92:95]\n"
"s_waitcnt vmcnt(9)\n"
"v_max3_u32 v255, v255, v152, v153\n"
"v_max3_u32 v255, v255, v154, v155\n"
"v_mfma_f32_16x16x32_bf16 v[96:99], a[168:171], v[148:151], v[96:99]\n"
"v_max3_u32 v255, v255, v196, v197\n"
"v_max3_u32 v255, v255, v198, v199\n"
"v_mfma_f32_16x16x32_bf16 v[100:103], a[172:175], v[148:151], v[100:103]\n"
"v_max3_u32 v255, v255, v204, v205\n"
"v_max3_u32 v255, v255, v206, v207\n"
"v_mfma_f32_16x16x32_bf16 v[88:91], a[176:179], v[156:159], v[88:91]\n"
"v_cmp_eq_u32_e32 vcc, -1, v255\n"
"v_mfma_f32_16x16x32_bf16 v[92:95], a[180:183], v[156:159], v[92:95]\n"
"s_cbranch_vccnz .Lmk_q130s\n"
".Lmk_q130r:\n"
"v_mfma_f32_16x16x32_bf16 v[96:99], a[184:187], v[156:159], v[96:99]\n"
"ds_write_b128 v3, v[200:203] offset:14336\n"
"v_mfma_f32_16x16x32_bf16 v[100:103], a[188:191], v[156:159], v[100:103]\n"
"buffer_load_dwordx4 v[140:143], v0, s[4:7], s98 offen offset:1024 sc1\n"
"v_mfma_f32_16x16x32_bf16 v[88:91], a[192:195], v[164:167], v[88:91]\n"
"ds_write_b128 v3, v[208:211] offset:15360\n"
"v_mfma_f32_16x16x32_bf16 v[92:95], a[196:199], v[164:167], v[92:95]\n"
"v_mfma_f32_16x16x32_bf16 v[96:99], a[200:203], v[164:167], v[96:99]\n"
"v_mfma_f32_16x16x32_bf16 v[100:103], a[204:207], v[164:167], v[100:103]\n"
"s_waitcnt lgkmcnt(0)\n"
"s_barrier\n"
"v_mfma_f32_16x16x32_bf16 v[88:91], a[208:211], v[152:155], v[88:91]\n"
"buffer_load_dwordx4 v[156:159], v0, s[4:7], s98 offen offset:2048 sc1\n"
"v_mfma_f32_16x16x32_bf16 v[92:95], a[212:215], v[152:155], v[92:95]\n"
"ds_read_b128 v[144:147], v5\n"
"ds_read_b128 v[172:175], v6\n"
"v_mfma_f32_16x16x32_bf16 v[96:99], a[216:219], v[152:155], v[96:99]\n"
"ds_read_b128 v[148:151], v5 offset:1024\n"
"ds_read_b128 v[176:179], v6 offset:1024\n"
"v_mfma_f32_16x16x32_bf16 v[100:103], a[220:223], v[152:155], v[100:103]\n"
"ds_read_b128 v[152:155], v5 offset:2048\n"
"ds_read_b128 v[180:183], v6 offset:2048\n"
"v_mfma_f32_16x16x32_bf16 v[88:91], a[224:227], v[196:199], v[88:91]\n"
"buffer_load_dwordx4 v[164:167], v0, s[4:7], s98 offen offset:3072 sc1\n"
"v_mfma_f32_16x16x32_bf16 v[92:95], a[228:231], v[196:199], v[92:95]\n"
"ds_read_b128 v[160:163], v5 offset:3072\n"
"ds_read_b128 v[184:187], v6 offset:3072\n"
"v_mfma_f32_16x16x32_bf16 v[96:99], a[232:235], v[196:199], v[96:99]\n"
"v_mfma_f32_16x16x32_bf16 v[100:103], a[236:239], v[196:199], v[100:103]\n"
"s_waitcnt lgkmcnt(4)\n"
"v_add_f32_e32 v144, v144, v172\n"
"v_add_f32_e32 v145, v145, v173\n"
"v_mfma_f32_16x16x32_bf16 v[88:91], a[240:243], v[204:207], v[88:91]\n"
"buffer_load_dwordx4 v[168:171], v1, s[4:7], s98 offen sc1\n"
"v_mfma_f32_16x16x32_bf16 v[92:95], a[244:247], v[204:207], v[92:95]\n"
"v_add_f32_e32 v146, v146, v174\n"
"v_add_f32_e32 v147, v147, v175\n"
"v_mfma_f32_16x16x32_bf16 v[96:99], a[248:251], v[204:207], v[96:99]\n"
"s_waitcnt lgkmcnt(0)\n"
"v_add_f32_e32 v148, v148, v176\n"
"v_add_f32_e32 v149, v149, v177\n"
"v_mfma_f32_16x16x32_bf16 v[100:103], a[252:255], v[204:207], v[100:103]\n"
"buffer_load_dwordx4 v[172:175], v1, s[4:7], s98 offen offset:1024 sc1\n"
"v_add_f32_e32 v150, v150, v178\n"
"v_add_f32_e32 v151, v151, v179\n"
"v_add_f32_e32 v152, v152, v180\n"
"v_add_f32_e32 v153, v153, v181\n"
"v_add_f32_e32 v154, v154, v182\n"
"v_add_f32_e32 v155, v155, v183\n"
"v_add_f32_e32 v160, v160, v184\n"
"v_add_f32_e32 v161, v161, v185\n"
"v_add_f32_e32 v162, v162, v186\n"
"v_add_f32_e32 v163, v163, v187\n"
"buffer_load_dwordx4 v[176:179], v1, s[4:7], s98 offen offset:2048 sc1\n"
"buffer_load_dwordx4 v[180:183], v1, s[4:7], s98 offen offset:3072 sc1\n"
"s_branch .Lmk_loop\n"
".Lmk_f1s:\n"
";\n"
"s_mov_b32 s1, 0x10000\n"
".Lmk_f1l:\n"
"buffer_load_dwordx2 v[250:251], v12, s[4:7], s98 offen sc1\n"
"s_waitcnt vmcnt(0)\n"
"v_cmp_ne_u64_e32 vcc, v[250:251], v[18:19]\n"
"s_cbranch_vccz .Lmk_f1r\n"
"s_sub_u32 s1, s1, 1\n"
"s_cbranch_scc0 .Lmk_f1l\n"
"s_branch .Lmk_f1r\n"
".Lmk_f2s:\n"
";\n"
"s_mov_b32 s1, 0x10000\n"
".Lmk_f2l:\n"
"buffer_load_dwordx2 v[254:255], v12, s[4:7], s97 offen sc1\n"
"s_waitcnt vmcnt(0)\n"
"v_cmp_ne_u64_e32 vcc, v[254:255], v[18:19]\n"
"s_cbranch_vccz .Lmk_f2r\n"
"s_sub_u32 s1, s1, 1\n"
"s_cbranch_scc0 .Lmk_f2l\n"
"s_branch .Lmk_f2r\n"
".Lmk_f5s:\n"
";\n"
"s_mov_b32 s1, 0x10000\n"
".Lmk_f5l:\n"
"buffer_load_dwordx2 v[252:253], v41, s[4:7], s97 offen sc1\n"
"s_waitcnt vmcnt(0)\n"
"v_cmp_ne_u64_e32 vcc, v[252:253], v[18:19]\n"
"s_cbranch_vccz .Lmk_f5r\n"
"s_sub_u32 s1, s1, 1\n"
"s_cbranch_scc0 .Lmk_f5l\n"
"s_branch .Lmk_f5r\n"
".Lmk_w7s:\n"
"buffer_store_dwordx4 a[24:27], v0, s[4:7], s97 offen sc1\n"
"s_branch .Lmk_w7r\n"
".Lmk_w8s:\n"
"buffer_store_dwordx4 a[28:31], v0, s[4:7], s96 offen sc1\n"
"s_branch .Lmk_w8r\n"
".Lmk_w9s:\n"
"buffer_store_dwordx4 a[32:35], v0, s[4:7], s96 offen sc1\n"
"s_branch .Lmk_w9r\n"
".Lmk_w10s:\n"
"buffer_store_dwordx4 a[36:39], v0, s[4:7], s96 offen sc1\n"
"s_branch .Lmk_w10r\n"
".Lmk_w11s:\n"
"buffer_store_dwordx4 a[40:43], v0, s[4:7], s96 offen sc1\n"
"s_branch .Lmk_w11r\n"
".Lmk_w12s:\n"
"buffer_store_dwordx4 a[44:47], v0, s[4:7], s96 offen sc1\n"
"s_branch .Lmk_w12r\n"
".Lmk_f6s:\n"
";\n"
"s_mov_b32 s1, 0x10000\n"
".Lmk_f6l:\n"
"buffer_load_dwordx2 v[254:255], v13, s[4:7], s96 offen sc1\n"
"s_waitcnt vmcnt(0)\n"
"v_cmp_ne_u64_e32 vcc, v[254:255], v[18:19]\n"
"s_cbranch_vccz .Lmk_f6r\n"
"s_sub_u32 s1, s1, 1\n"
"s_cbranch_scc0 .Lmk_f6l\n"
"s_branch .Lmk_f6r\n"
".Lmk_f3s:\n"
";\n"
"s_mov_b32 s1, 0x10000\n"
".Lmk_f3l:\n"
"buffer_load_dwordx2 v[248:249], off, s[4:7], s95 sc1\n"
"s_waitcnt vmcnt(0)\n"
"v_cmp_ne_u64_e32 vcc, v[248:249], v[18:19]\n"
"s_cbranch_vccz .Lmk_f3r\n"
"s_sub_u32 s1, s1, 1\n"
"s_cbranch_scc0 .Lmk_f3l\n"
"s_branch .Lmk_f3r\n"
".Lmk_x23s:\n"
"s_nop 3\n"
"v_mov_b32_e32 v24, 0xff800000\n"
"v_mov_b32_e32 v25, 0xff800000\n"
"v_mov_b32_e32 v26, 0xff800000\n"
"v_mov_b32_e32 v27, 0xff800000\n"
"v_mov_b32_e32 v36, 0xff800000\n"
"v_mov_b32_e32 v37, 0xff800000\n"
"v_mov_b32_e32 v38, 0xff800000\n"
"v_mov_b32_e32 v39, 0xff800000\n"
"s_nop 1\n"
"s_branch .Lmk_x23r\n"
".Lmk_x24s:\n"
"s_nop 3\n"
"v_mov_b32_e32 v24, 0xff800000\n"
"v_mov_b32_e32 v25, 0xff800000\n"
"v_mov_b32_e32 v26, 0xff800000\n"
"v_mov_b32_e32 v27, 0xff800000\n"
"v_mov_b32_e32 v36, 0xff800000\n"
"v_mov_b32_e32 v37, 0xff800000\n"
"v_mov_b32_e32 v38, 0xff800000\n"
"v_mov_b32_e32 v39, 0xff800000\n"
"s_nop 1\n"
"s_branch .Lmk_x24r\n"
".Lmk_x25s:\n"
"s_nop 3\n"
"v_mov_b32_e32 v24, 0xff800000\n"
"v_mov_b32_e32 v25, 0xff800000\n"
"v_mov_b32_e32 v26, 0xff800000\n"
"v_mov_b32_e32 v27, 0xff800000\n"
"v_mov_b32_e32 v36, 0xff800000\n"
"v_mov_b32_e32 v37, 0xff800000\n"
"v_mov_b32_e32 v38, 0xff800000\n"
"v_mov_b32_e32 v39, 0xff800000\n"
"s_nop 1\n"
"s_branch .Lmk_x25r\n"
".Lmk_x26s:\n"
"s_nop 3\n"
"v_mov_b32_e32 v24, 0xff800000\n"
"v_mov_b32_e32 v25, 0xff800000\n"
"v_mov_b32_e32 v26, 0xff800000\n"
"v_mov_b32_e32 v27, 0xff800000\n"
"v_mov_b32_e32 v36, 0xff800000\n"
"v_mov_b32_e32 v37, 0xff800000\n"
"v_mov_b32_e32 v38, 0xff800000\n"
"v_mov_b32_e32 v39, 0xff800000\n"
"s_nop 1\n"
"s_branch .Lmk_x26r\n"
".Lmk_f19s:\n"
";\n"
"s_mov_b32 s1, 0x10000\n"
".Lmk_f19l:\n"
"buffer_load_dwordx2 v[238:239], off, s[4:7], s97 sc1\n"
"s_waitcnt vmcnt(0)\n"
"v_cmp_ne_u64_e32 vcc, v[238:239], v[18:19]\n"
"s_cbranch_vccz .Lmk_f19r\n"
"s_sub_u32 s1, s1, 1\n"
"s_cbranch_scc0 .Lmk_f19l\n"
"s_branch .Lmk_f19r\n"
".Lmk_b21:\n"
"s_cmp_lg_u32 s2, 0\n"
"s_cbranch_scc1 .Lmk_k27\n"
"s_mov_b32 s2, 1\n"
"s_waitcnt vmcnt(0) lgkmcnt(0)\n"
"s_nop 7\n"
"s_mov_b32 s97, 11\n"
"v_add_u32_e32 v248, 0x3800, v15\n"
"v_add_u32_e32 v247, 0x3800, v254\n"
"ds_read_b128 v[36:39], v15 offset:24576\n"
"ds_read_b128 v[48:51], v254 offset:24832\n"
"s_waitcnt lgkmcnt(0)\n"
"v_mfma_f32_16x16x32_bf16 v[52:55], v[36:39], v[28:31], 0\n"
"v_mfma_f32_16x16x32_bf16 v[56:59], v[36:39], v[32:35], 0\n"
"v_mfma_f32_16x16x32_bf16 v[36:39], v[48:51], v[28:31], 0\n"
"v_mfma_f32_16x16x32_bf16 v[60:63], v[48:51], v[32:35], 0\n"
".Lmk_b21l:\n"
"ds_read_b128 v[48:51], v248 offset:8192\n"
"ds_read_b128 v[64:67], v247 offset:8448\n"
"s_cmp_gt_i32 s78, s97\n"
"s_cselect_b32 s96, 0xff800000, 0\n"
"v_mov_b32_e32 v72, s96\n"
"v_mov_b32_e32 v73, s96\n"
"v_mov_b32_e32 v74, s96\n"
"v_mov_b32_e32 v75, s96\n"
"v_subrev_u32_e32 v248, 0x800, v248\n"
"v_subrev_u32_e32 v247, 0x800, v247\n"
"s_waitcnt lgkmcnt(0)\n"
"v_mfma_f32_16x16x32_bf16 v[76:79], v[48:51], v[28:31], v[72:75]\n"
"v_mfma_f32_16x16x32_bf16 v[80:83], v[48:51], v[32:35], v[72:75]\n"
"v_mfma_f32_16x16x32_bf16 v[48:51], v[64:67], v[28:31], v[72:75]\n"
"v_mfma_f32_16x16x32_bf16 v[84:87], v[64:67], v[32:35], v[72:75]\n"
"s_nop 7\n"
"s_cmp_lg_u32 s97, 4\n"
"s_cbranch_scc1 .Lmk_b21m\n"
"v_cndmask_b32_e64 v76, v52, v76, s[20:21]\n"
"v_cndmask_b32_e64 v77, v53, v77, s[22:23]\n"
"v_cndmask_b32_e64 v78, v54, v78, s[24:25]\n"
"v_cndmask_b32_e64 v79, v55, v79, s[26:27]\n"
"v_cndmask_b32_e64 v48, v36, v48, s[28:29]\n"
"v_cndmask_b32_e64 v49, v37, v49, s[30:31]\n"
"v_cndmask_b32_e64 v50, v38, v50, s[32:33]\n"
"v_cndmask_b32_e64 v51, v39, v51, s[34:35]\n"
"v_cndmask_b32_e64 v80, v56, v80, s[36:37]\n"
"v_cndmask_b32_e64 v81, v57, v81, s[38:39]\n"
"v_cndmask_b32_e64 v82, v58, v82, s[40:41]\n"
"v_cndmask_b32_e64 v83, v59, v83, s[42:43]\n"
"v_cndmask_b32_e64 v84, v60, v84, s[44:45]\n"
"v_cndmask_b32_e64 v85, v61, v85, s[46:47]\n"
"v_cndmask_b32_e64 v86, v62, v86, s[48:49]\n"
"v_cndmask_b32_e64 v87, v63, v87, s[50:51]\n"
".Lmk_b21m:\n"
"v_max3_f32 v253, v253, v76, v77\n"
"v_max3_f32 v252, v252, v80, v81\n"
"v_max3_f32 v253, v253, v78, v79\n"
"v_max3_f32 v252, v252, v82, v83\n"
"v_max3_f32 v253, v253, v48, v49\n"
"v_max3_f32 v252, v252, v84, v85\n"
"v_max3_f32 v253, v253, v50, v51\n"
"v_max3_f32 v252, v252, v86, v87\n"
"s_sub_u32 s97, s97, 1\n"
"s_cmp_ge_i32 s97, 4\n"
"s_cbranch_scc1 .Lmk_b21l\n"
"v_mov_b32_e32 v248, v253\n"
"v_mov_b32_e32 v247, v252\n"
"s_nop 0\n"
"v_permlane16_swap_b32_e32 v253, v248\n"
"v_permlane16_swap_b32_e32 v252, v247\n"
"v_max_f32_e32 v253, v253, v248\n"
"v_max_f32_e32 v252, v252, v247\n"
"v_mov_b32_e32 v248, v253\n"
"v_mov_b32_e32 v247, v252\n"
"s_nop 0\n"
"v_permlane32_swap_b32_e32 v253, v248\n"
"v_permlane32_swap_b32_e32 v252, v247\n"
"v_max_f32_e32 v253, v253, v248\n"
"v_max_f32_e32 v252, v252, v247\n"
"v_mov_b32_e32 v250, v253\n"
"v_mov_b32_e32 v249, v252\n"
"s_branch .Lmk_a20\n"
".Lmk_x30s:\n"
"s_nop 3\n"
"v_mov_b32_e32 v116, 0xff800000\n"
"v_mov_b32_e32 v117, 0xff800000\n"
"v_mov_b32_e32 v118, 0xff800000\n"
"v_mov_b32_e32 v119, 0xff800000\n"
"v_mov_b32_e32 v120, 0xff800000\n"
"v_mov_b32_e32 v121, 0xff800000\n"
"v_mov_b32_e32 v122, 0xff800000\n"
"v_mov_b32_e32 v123, 0xff800000\n"
"s_nop 1\n"
"s_branch .Lmk_x30r\n"
".Lmk_x31s:\n"
"s_nop 3\n"
"v_mov_b32_e32 v116, 0xff800000\n"
"v_mov_b32_e32 v117, 0xff800000\n"
"v_mov_b32_e32 v118, 0xff800000\n"
"v_mov_b32_e32 v119, 0xff800000\n"
"v_mov_b32_e32 v120, 0xff800000\n"
"v_mov_b32_e32 v121, 0xff800000\n"
"v_mov_b32_e32 v122, 0xff800000\n"
"v_mov_b32_e32 v123, 0xff800000\n"
"s_nop 1\n"
"s_branch .Lmk_x31r\n"
".Lmk_x32s:\n"
"s_nop 3\n"
"v_mov_b32_e32 v116, 0xff800000\n"
"v_mov_b32_e32 v117, 0xff800000\n"
"v_mov_b32_e32 v118, 0xff800000\n"
"v_mov_b32_e32 v119, 0xff800000\n"
"v_mov_b32_e32 v120, 0xff800000\n"
"v_mov_b32_e32 v121, 0xff800000\n"
"v_mov_b32_e32 v122, 0xff800000\n"
"v_mov_b32_e32 v123, 0xff800000\n"
"s_nop 1\n"
"s_branch .Lmk_x32r\n"
".Lmk_f4s:\n"
";\n"
"s_mov_b32 s1, 0x10000\n"
".Lmk_f4l:\n"
"buffer_load_dwordx2 v[246:247], v12, s[4:7], s96 offen sc1\n"
"s_waitcnt vmcnt(0)\n"
"v_cmp_ne_u64_e32 vcc, v[246:247], v[18:19]\n"
"s_cbranch_vccz .Lmk_f4r\n"
"s_sub_u32 s1, s1, 1\n"
"s_cbranch_scc0 .Lmk_f4l\n"
"s_branch .Lmk_f4r\n"
".Lmk_b29:\n"
"s_cmp_lg_u32 s2, 0\n"
"s_cbranch_scc1 .Lmk_k34\n"
"s_mov_b32 s2, 1\n"
"s_waitcnt vmcnt(0) lgkmcnt(0)\n"
"s_nop 7\n"
"s_mov_b32 s96, 7\n"
"v_add_u32_e32 v248, 0x3800, v15\n"
"v_add_u32_e32 v247, 0x3800, v254\n"
"ds_read_b128 v[176:179], v15 offset:16384\n"
"ds_read_b128 v[184:187], v254 offset:16640\n"
"s_waitcnt lgkmcnt(0)\n"
"v_mfma_f32_16x16x32_bf16 v[196:199], v[176:179], v[92:95], 0\n"
"v_mfma_f32_16x16x32_bf16 v[208:211], v[176:179], v[96:99], 0\n"
"v_mfma_f32_16x16x32_bf16 v[176:179], v[184:187], v[92:95], 0\n"
"v_mfma_f32_16x16x32_bf16 v[220:223], v[184:187], v[96:99], 0\n"
".Lmk_b29l:\n"
"ds_read_b128 v[184:187], v248\n"
"ds_read_b128 v[224:227], v247 offset:256\n"
"s_cmp_gt_i32 s78, s96\n"
"s_cselect_b32 s95, 0xff800000, 0\n"
"v_mov_b32_e32 v228, s95\n"
"v_mov_b32_e32 v229, s95\n"
"v_mov_b32_e32 v230, s95\n"
"v_mov_b32_e32 v231, s95\n"
"v_subrev_u32_e32 v248, 0x800, v248\n"
"v_subrev_u32_e32 v247, 0x800, v247\n"
"s_waitcnt lgkmcnt(0)\n"
"v_mfma_f32_16x16x32_bf16 v[232:235], v[184:187], v[92:95], v[228:231]\n"
"v_mfma_f32_16x16x32_bf16 v[236:239], v[184:187], v[96:99], v[228:231]\n"
"v_mfma_f32_16x16x32_bf16 v[184:187], v[224:227], v[92:95], v[228:231]\n"
"v_mfma_f32_16x16x32_bf16 v[240:243], v[224:227], v[96:99], v[228:231]\n"
"s_nop 7\n"
"s_cmp_lg_u32 s96, 0\n"
"s_cbranch_scc1 .Lmk_b29m\n"
"v_cndmask_b32_e64 v232, v196, v232, s[20:21]\n"
"v_cndmask_b32_e64 v233, v197, v233, s[22:23]\n"
"v_cndmask_b32_e64 v234, v198, v234, s[24:25]\n"
"v_cndmask_b32_e64 v235, v199, v235, s[26:27]\n"
"v_cndmask_b32_e64 v184, v176, v184, s[28:29]\n"
"v_cndmask_b32_e64 v185, v177, v185, s[30:31]\n"
"v_cndmask_b32_e64 v186, v178, v186, s[32:33]\n"
"v_cndmask_b32_e64 v187, v179, v187, s[34:35]\n"
"v_cndmask_b32_e64 v236, v208, v236, s[36:37]\n"
"v_cndmask_b32_e64 v237, v209, v237, s[38:39]\n"
"v_cndmask_b32_e64 v238, v210, v238, s[40:41]\n"
"v_cndmask_b32_e64 v239, v211, v239, s[42:43]\n"
"v_cndmask_b32_e64 v240, v220, v240, s[44:45]\n"
"v_cndmask_b32_e64 v241, v221, v241, s[46:47]\n"
"v_cndmask_b32_e64 v242, v222, v242, s[48:49]\n"
"v_cndmask_b32_e64 v243, v223, v243, s[50:51]\n"
".Lmk_b29m:\n"
"v_max3_f32 v253, v253, v232, v233\n"
"v_max3_f32 v252, v252, v236, v237\n"
"v_max3_f32 v253, v253, v234, v235\n"
"v_max3_f32 v252, v252, v238, v239\n"
"v_max3_f32 v253, v253, v184, v185\n"
"v_max3_f32 v252, v252, v240, v241\n"
"v_max3_f32 v253, v253, v186, v187\n"
"v_max3_f32 v252, v252, v242, v243\n"
"s_sub_u32 s96, s96, 1\n"
"s_cmp_ge_i32 s96, 0\n"
"s_cbranch_scc1 .Lmk_b29l\n"
"v_mov_b32_e32 v248, v253\n"
"v_mov_b32_e32 v247, v252\n"
"s_nop 0\n"
"v_permlane16_swap_b32_e32 v253, v248\n"
"v_permlane16_swap_b32_e32 v252, v247\n"
"v_max_f32_e32 v253, v253, v248\n"
"v_max_f32_e32 v252, v252, v247\n"
"v_mov_b32_e32 v248, v253\n"
"v_mov_b32_e32 v247, v252\n"
"s_nop 0\n"
"v_permlane32_swap_b32_e32 v253, v248\n"
"v_permlane32_swap_b32_e32 v252, v247\n"
"v_max_f32_e32 v253, v253, v248\n"
"v_max_f32_e32 v252, v252, v247\n"
"v_mov_b32_e32 v250, v253\n"
"v_mov_b32_e32 v249, v252\n"
"s_branch .Lmk_a28\n"
".Lmk_p35s:\n"
";\n"
"s_mov_b32 s1, 0x10000\n"
".Lmk_p35l:\n"
"s_add_u32 s97, s56, 0x40000\n"
"buffer_load_dwordx4 v[140:143], v0, s[4:7], s97 offen sc1\n"
"buffer_load_dwordx4 v[136:139], v0, s[4:7], s97 offen offset:1024 sc1\n"
"buffer_load_dwordx4 v[120:123], v0, s[4:7], s97 offen offset:2048 sc1\n"
"buffer_load_dwordx4 v[108:111], v0, s[4:7], s97 offen offset:3072 sc1\n"
"buffer_load_dwordx4 v[148:151], v1, s[4:7], s97 offen sc1\n"
"buffer_load_dwordx4 v[160:163], v1, s[4:7], s97 offen offset:1024 sc1\n"
"buffer_load_dwordx4 v[164:167], v1, s[4:7], s97 offen offset:2048 sc1\n"
"buffer_load_dwordx4 v[128:131], v1, s[4:7], s97 offen offset:3072 sc1\n"
"s_add_u32 s97, s57, 0x40000\n"
"buffer_load_dwordx4 v[216:219], v0, s[4:7], s97 offen sc1\n"
"buffer_load_dwordx4 v[212:215], v0, s[4:7], s97 offen offset:1024 sc1\n"
"buffer_load_dwordx4 v[192:195], v0, s[4:7], s97 offen offset:2048 sc1\n"
"buffer_load_dwordx4 v[100:103], v0, s[4:7], s97 offen offset:3072 sc1\n"
"buffer_load_dwordx4 v[188:191], v1, s[4:7], s97 offen sc1\n"
"buffer_load_dwordx4 v[104:107], v1, s[4:7], s97 offen offset:1024 sc1\n"
"buffer_load_dwordx4 v[112:115], v1, s[4:7], s97 offen offset:2048 sc1\n"
"buffer_load_dwordx4 v[124:127], v1, s[4:7], s97 offen offset:3072 sc1\n"
"s_add_u32 s97, s59, 0x40000\n"
"buffer_load_dwordx4 v[144:147], v0, s[4:7], s97 offen sc1\n"
"buffer_load_dwordx4 v[152:155], v0, s[4:7], s97 offen offset:1024 sc1\n"
"buffer_load_dwordx4 v[168:171], v0, s[4:7], s97 offen offset:2048 sc1\n"
"buffer_load_dwordx4 v[116:119], v0, s[4:7], s97 offen offset:3072 sc1\n"
"s_waitcnt vmcnt(0)\n"
"v_max_u32_e32 v255, v140, v141\n"
"v_max3_u32 v255, v255, v142, v143\n"
"v_max3_u32 v255, v255, v136, v137\n"
"v_max3_u32 v255, v255, v138, v139\n"
"v_max3_u32 v255, v255, v120, v121\n"
"v_max3_u32 v255, v255, v122, v123\n"
"v_max3_u32 v255, v255, v108, v109\n"
"v_max3_u32 v255, v255, v110, v111\n"
"v_max3_u32 v255, v255, v148, v149\n"
"v_max3_u32 v255, v255, v150, v151\n"
"v_max3_u32 v255, v255, v160, v161\n"
"v_max3_u32 v255, v255, v162, v163\n"
"v_max3_u32 v255, v255, v164, v165\n"
"v_max3_u32 v255, v255, v166, v167\n"
"v_max3_u32 v255, v255, v128, v129\n"
"v_max3_u32 v255, v255, v130, v131\n"
"v_cmp_eq_u32_e32 vcc, -1, v255\n"
"s_cbranch_vccz .Lmk_p35k\n"
"s_sub_u32 s1, s1, 1\n"
"s_cbranch_scc0 .Lmk_p35l\n"
".Lmk_p35k:\n"
"v_mfma_f32_16x16x32_bf16 v[88:91], a[128:131], v[140:143], 0\n"
"v_mfma_f32_16x16x32_bf16 v[92:95], a[132:135], v[140:143], 0\n"
"v_mfma_f32_16x16x32_bf16 v[96:99], a[136:139], v[140:143], 0\n"
"v_mfma_f32_16x16x32_bf16 v[156:159], a[140:143], v[140:143], 0\n"
"v_mfma_f32_16x16x32_bf16 v[88:91], a[144:147], v[136:139], v[88:91]\n"
"v_mfma_f32_16x16x32_bf16 v[92:95], a[148:151], v[136:139], v[92:95]\n"
"v_mfma_f32_16x16x32_bf16 v[96:99], a[152:155], v[136:139], v[96:99]\n"
"v_mfma_f32_16x16x32_bf16 v[156:159], a[156:159], v[136:139], v[156:159]\n"
"v_mfma_f32_16x16x32_bf16 v[88:91], a[160:163], v[120:123], v[88:91]\n"
"v_mfma_f32_16x16x32_bf16 v[92:95], a[164:167], v[120:123], v[92:95]\n"
"v_mfma_f32_16x16x32_bf16 v[96:99], a[168:171], v[120:123], v[96:99]\n"
"v_mfma_f32_16x16x32_bf16 v[156:159], a[172:175], v[120:123], v[156:159]\n"
"v_mfma_f32_16x16x32_bf16 v[88:91], a[176:179], v[108:111], v[88:91]\n"
"v_mfma_f32_16x16x32_bf16 v[92:95], a[180:183], v[108:111], v[92:95]\n"
"v_mfma_f32_16x16x32_bf16 v[96:99], a[184:187], v[108:111], v[96:99]\n"
"v_mfma_f32_16x16x32_bf16 v[156:159], a[188:191], v[108:111], v[156:159]\n"
"v_mfma_f32_16x16x32_bf16 v[88:91], a[192:195], v[148:151], v[88:91]\n"
"v_mfma_f32_16x16x32_bf16 v[92:95], a[196:199], v[148:151], v[92:95]\n"
"v_mfma_f32_16x16x32_bf16 v[96:99], a[200:203], v[148:151], v[96:99]\n"
"v_mfma_f32_16x16x32_bf16 v[156:159], a[204:207], v[148:151], v[156:159]\n"
"v_mfma_f32_16x16x32_bf16 v[88:91], a[208:211], v[160:163], v[88:91]\n"
"v_mfma_f32_16x16x32_bf16 v[92:95], a[212:215], v[160:163], v[92:95]\n"
"v_mfma_f32_16x16x32_bf16 v[96:99], a[216:219], v[160:163], v[96:99]\n"
"v_mfma_f32_16x16x32_bf16 v[156:159], a[220:223], v[160:163], v[156:159]\n"
"v_mfma_f32_16x16x32_bf16 v[88:91], a[224:227], v[164:167], v[88:91]\n"
"v_mfma_f32_16x16x32_bf16 v[92:95], a[228:231], v[164:167], v[92:95]\n"
"v_mfma_f32_16x16x32_bf16 v[96:99], a[232:235], v[164:167], v[96:99]\n"
"v_mfma_f32_16x16x32_bf16 v[156:159], a[236:239], v[164:167], v[156:159]\n"
"v_mfma_f32_16x16x32_bf16 v[88:91], a[240:243], v[128:131], v[88:91]\n"
"v_mfma_f32_16x16x32_bf16 v[92:95], a[244:247], v[128:131], v[92:95]\n"
"v_mfma_f32_16x16x32_bf16 v[96:99], a[248:251], v[128:131], v[96:99]\n"
"v_mfma_f32_16x16x32_bf16 v[156:159], a[252:255], v[128:131], v[156:159]\n"
"s_add_u32 s97, s58, 0x40000\n"
"v_mov_b32_e32 v255, 0x80000000\n"
"buffer_load_dwordx4 v[140:143], v0, s[4:7], s97 offen sc1\n"
"buffer_load_dwordx4 v[136:139], v0, s[4:7], s97 offen offset:1024 sc1\n"
"buffer_load_dwordx4 v[120:123], v0, s[4:7], s97 offen offset:2048 sc1\n"
"buffer_load_dwordx4 v[108:111], v0, s[4:7], s97 offen offset:3072 sc1\n"
"buffer_store_dword v255, v255, s[4:7], 0 offen\n"
"buffer_store_dword v255, v255, s[4:7], 0 offen\n"
"buffer_load_dwordx4 v[148:151], v1, s[4:7], s97 offen sc1\n"
"buffer_load_dwordx4 v[160:163], v1, s[4:7], s97 offen offset:1024 sc1\n"
"buffer_load_dwordx4 v[164:167], v1, s[4:7], s97 offen offset:2048 sc1\n"
"buffer_load_dwordx4 v[128:131], v1, s[4:7], s97 offen offset:3072 sc1\n"
"buffer_store_dword v255, v255, s[4:7], 0 offen\n"
"buffer_store_dword v255, v255, s[4:7], 0 offen\n"
"buffer_store_dword v255, v255, s[4:7], 0 offen\n"
"buffer_store_dword v255, v255, s[4:7], 0 offen\n"
"s_nop 7\n"
"s_branch .Lmk_p35r\n"
".Lmk_p36s:\n"
";\n"
"s_mov_b32 s1, 0x10000\n"
".Lmk_p36l:\n"
"s_add_u32 s98, s57, 0x40000\n"
"buffer_load_dwordx4 v[144:147], v0, s[4:7], s98 offen sc1\n"
"buffer_load_dwordx4 v[152:155], v0, s[4:7], s98 offen offset:1024 sc1\n"
"buffer_load_dwordx4 v[168:171], v0, s[4:7], s98 offen offset:2048 sc1\n"
"buffer_load_dwordx4 v[116:119], v0, s[4:7], s98 offen offset:3072 sc1\n"
"buffer_load_dwordx4 v[180:183], v1, s[4:7], s98 offen sc1\n"
"buffer_load_dwordx4 v[88:91], v1, s[4:7], s98 offen offset:1024 sc1\n"
"buffer_load_dwordx4 v[92:95], v1, s[4:7], s98 offen offset:2048 sc1\n"
"buffer_load_dwordx4 v[100:103], v1, s[4:7], s98 offen offset:3072 sc1\n"
"s_add_u32 s98, s58, 0x40000\n"
"buffer_load_dwordx4 v[140:143], v0, s[4:7], s98 offen sc1\n"
"buffer_load_dwordx4 v[136:139], v0, s[4:7], s98 offen offset:1024 sc1\n"
"buffer_load_dwordx4 v[120:123], v0, s[4:7], s98 offen offset:2048 sc1\n"
"buffer_load_dwordx4 v[108:111], v0, s[4:7], s98 offen offset:3072 sc1\n"
"buffer_load_dwordx4 v[148:151], v1, s[4:7], s98 offen sc1\n"
"buffer_load_dwordx4 v[160:163], v1, s[4:7], s98 offen offset:1024 sc1\n"
"buffer_load_dwordx4 v[164:167], v1, s[4:7], s98 offen offset:2048 sc1\n"
"buffer_load_dwordx4 v[128:131], v1, s[4:7], s98 offen offset:3072 sc1\n"
"s_waitcnt vmcnt(0)\n"
"v_max_u32_e32 v255, v144, v145\n"
"v_max3_u32 v255, v255, v146, v147\n"
"v_max3_u32 v255, v255, v152, v153\n"
"v_max3_u32 v255, v255, v154, v155\n"
"v_max3_u32 v255, v255, v168, v169\n"
"v_max3_u32 v255, v255, v170, v171\n"
"v_max3_u32 v255, v255, v116, v117\n"
"v_max3_u32 v255, v255, v118, v119\n"
"v_max3_u32 v255, v255, v180, v181\n"
"v_max3_u32 v255, v255, v182, v183\n"
"v_max3_u32 v255, v255, v88, v89\n"
"v_max3_u32 v255, v255, v90, v91\n"
"v_max3_u32 v255, v255, v92, v93\n"
"v_max3_u32 v255, v255, v94, v95\n"
"v_max3_u32 v255, v255, v100, v101\n"
"v_max3_u32 v255, v255, v102, v103\n"
"v_cmp_eq_u32_e32 vcc, -1, v255\n"
"s_cbranch_vccz .Lmk_p36k\n"
"s_sub_u32 s1, s1, 1\n"
"s_cbranch_scc0 .Lmk_p36l\n"
".Lmk_p36k:\n"
"v_mfma_f32_16x16x32_bf16 v[132:135], a[128:131], v[144:147], 0\n"
"v_mfma_f32_16x16x32_bf16 v[172:175], a[132:135], v[144:147], 0\n"
"v_mfma_f32_16x16x32_bf16 v[176:179], a[136:139], v[144:147], 0\n"
"v_mfma_f32_16x16x32_bf16 v[184:187], a[140:143], v[144:147], 0\n"
"v_mfma_f32_16x16x32_bf16 v[132:135], a[144:147], v[152:155], v[132:135]\n"
"v_mfma_f32_16x16x32_bf16 v[172:175], a[148:151], v[152:155], v[172:175]\n"
"v_mfma_f32_16x16x32_bf16 v[176:179], a[152:155], v[152:155], v[176:179]\n"
"v_mfma_f32_16x16x32_bf16 v[184:187], a[156:159], v[152:155], v[184:187]\n"
"v_mfma_f32_16x16x32_bf16 v[132:135], a[160:163], v[168:171], v[132:135]\n"
"v_mfma_f32_16x16x32_bf16 v[172:175], a[164:167], v[168:171], v[172:175]\n"
"v_mfma_f32_16x16x32_bf16 v[176:179], a[168:171], v[168:171], v[176:179]\n"
"v_mfma_f32_16x16x32_bf16 v[184:187], a[172:175], v[168:171], v[184:187]\n"
"v_mfma_f32_16x16x32_bf16 v[132:135], a[176:179], v[116:119], v[132:135]\n"
"v_mfma_f32_16x16x32_bf16 v[172:175], a[180:183], v[116:119], v[172:175]\n"
"v_mfma_f32_16x16x32_bf16 v[176:179], a[184:187], v[116:119], v[176:179]\n"
"v_mfma_f32_16x16x32_bf16 v[184:187], a[188:191], v[116:119], v[184:187]\n"
"v_mfma_f32_16x16x32_bf16 v[132:135], a[192:195], v[180:183], v[132:135]\n"
"v_mfma_f32_16x16x32_bf16 v[172:175], a[196:199], v[180:183], v[172:175]\n"
"v_mfma_f32_16x16x32_bf16 v[176:179], a[200:203], v[180:183], v[176:179]\n"
"v_mfma_f32_16x16x32_bf16 v[184:187], a[204:207], v[180:183], v[184:187]\n"
"v_mfma_f32_16x16x32_bf16 v[132:135], a[208:211], v[88:91], v[132:135]\n"
"v_mfma_f32_16x16x32_bf16 v[172:175], a[212:215], v[88:91], v[172:175]\n"
"v_mfma_f32_16x16x32_bf16 v[176:179], a[216:219], v[88:91], v[176:179]\n"
"v_mfma_f32_16x16x32_bf16 v[184:187], a[220:223], v[88:91], v[184:187]\n"
"v_mfma_f32_16x16x32_bf16 v[132:135], a[224:227], v[92:95], v[132:135]\n"
"v_mfma_f32_16x16x32_bf16 v[172:175], a[228:231], v[92:95], v[172:175]\n"
"v_mfma_f32_16x16x32_bf16 v[176:179], a[232:235], v[92:95], v[176:179]\n"
"v_mfma_f32_16x16x32_bf16 v[184:187], a[236:239], v[92:95], v[184:187]\n"
"v_mfma_f32_16x16x32_bf16 v[132:135], a[240:243], v[100:103], v[132:135]\n"
"v_mfma_f32_16x16x32_bf16 v[172:175], a[244:247], v[100:103], v[172:175]\n"
"v_mfma_f32_16x16x32_bf16 v[176:179], a[248:251], v[100:103], v[176:179]\n"
"v_mfma_f32_16x16x32_bf16 v[184:187], a[252:255], v[100:103], v[184:187]\n"
"s_add_u32 s98, s59, 0x40000\n"
"v_mov_b32_e32 v255, 0x80000000\n"
"buffer_load_dwordx4 v[144:147], v0, s[4:7], s98 offen sc1\n"
"buffer_load_dwordx4 v[152:155], v0, s[4:7], s98 offen offset:1024 sc1\n"
"buffer_load_dwordx4 v[168:171], v0, s[4:7], s98 offen offset:2048 sc1\n"
"buffer_load_dwordx4 v[116:119], v0, s[4:7], s98 offen offset:3072 sc1\n"
"buffer_load_dwordx4 v[180:183], v1, s[4:7], s98 offen sc1\n"
"buffer_load_dwordx4 v[88:91], v1, s[4:7], s98 offen offset:1024 sc1\n"
"buffer_load_dwordx4 v[92:95], v1, s[4:7], s98 offen offset:2048 sc1\n"
"buffer_load_dwordx4 v[100:103], v1, s[4:7], s98 offen offset:3072 sc1\n"
"buffer_store_dword v255, v255, s[4:7], 0 offen\n"
"s_nop 7\n"
"s_branch .Lmk_p36r\n"
".Lmk_q38s:\n"
";\n"
"s_mov_b32 s1, 0x10000\n"
".Lmk_q38l:\n"
"s_add_u32 s97, s59, 0x40000\n"
"buffer_load_dwordx4 v[144:147], v0, s[4:7], s97 offen sc1\n"
"buffer_load_dwordx4 v[152:155], v0, s[4:7], s97 offen offset:1024 sc1\n"
"buffer_load_dwordx4 v[168:171], v0, s[4:7], s97 offen offset:2048 sc1\n"
"buffer_load_dwordx4 v[116:119], v0, s[4:7], s97 offen offset:3072 sc1\n"
"buffer_load_dwordx4 v[180:183], v1, s[4:7], s97 offen sc1\n"
"buffer_load_dwordx4 v[88:91], v1, s[4:7], s97 offen offset:1024 sc1\n"
"buffer_load_dwordx4 v[92:95], v1, s[4:7], s97 offen offset:2048 sc1\n"
"buffer_load_dwordx4 v[100:103], v1, s[4:7], s97 offen offset:3072 sc1\n"
"s_add_u32 s97, s56, 0\n"
"buffer_load_dwordx4 v[120:123], v0, s[4:7], s97 offen sc1\n"
"buffer_load_dwordx4 v[108:111], v0, s[4:7], s97 offen offset:1024 sc1\n"
"buffer_load_dwordx4 v[132:135], v0, s[4:7], s97 offen offset:2048 sc1\n"
"buffer_load_dwordx4 v[136:139], v0, s[4:7], s97 offen offset:3072 sc1\n"
"s_waitcnt vmcnt(0)\n"
"v_max_u32_e32 v255, v144, v145\n"
"v_max3_u32 v255, v255, v146, v147\n"
"v_max3_u32 v255, v255, v152, v153\n"
"v_max3_u32 v255, v255, v154, v155\n"
"v_max3_u32 v255, v255, v168, v169\n"
"v_max3_u32 v255, v255, v170, v171\n"
"v_max3_u32 v255, v255, v116, v117\n"
"v_max3_u32 v255, v255, v118, v119\n"
"v_cmp_eq_u32_e32 vcc, -1, v255\n"
"s_cbranch_vccz .Lmk_q38k\n"
"s_sub_u32 s1, s1, 1\n"
"s_cbranch_scc0 .Lmk_q38l\n"
".Lmk_q38k:\n"
"s_branch .Lmk_q38r\n"
".Lmk_p37s:\n"
";\n"
"s_mov_b32 s1, 0x10000\n"
".Lmk_p37l:\n"
"s_add_u32 s97, s58, 0x40000\n"
"buffer_load_dwordx4 v[128:131], v0, s[4:7], s97 offen sc1\n"
"buffer_load_dwordx4 v[148:151], v0, s[4:7], s97 offen offset:1024 sc1\n"
"buffer_load_dwordx4 v[156:159], v0, s[4:7], s97 offen offset:2048 sc1\n"
"buffer_load_dwordx4 v[160:163], v0, s[4:7], s97 offen offset:3072 sc1\n"
"buffer_load_dwordx4 v[164:167], v1, s[4:7], s97 offen sc1\n"
"buffer_load_dwordx4 v[172:175], v1, s[4:7], s97 offen offset:1024 sc1\n"
"buffer_load_dwordx4 v[176:179], v1, s[4:7], s97 offen offset:2048 sc1\n"
"buffer_load_dwordx4 v[184:187], v1, s[4:7], s97 offen offset:3072 sc1\n"
"s_add_u32 s97, s59, 0x40000\n"
"buffer_load_dwordx4 v[144:147], v0, s[4:7], s97 offen sc1\n"
"buffer_load_dwordx4 v[152:155], v0, s[4:7], s97 offen offset:1024 sc1\n"
"buffer_load_dwordx4 v[168:171], v0, s[4:7], s97 offen offset:2048 sc1\n"
"buffer_load_dwordx4 v[116:119], v0, s[4:7], s97 offen offset:3072 sc1\n"
"buffer_load_dwordx4 v[180:183], v1, s[4:7], s97 offen sc1\n"
"buffer_load_dwordx4 v[88:91], v1, s[4:7], s97 offen offset:1024 sc1\n"
"buffer_load_dwordx4 v[92:95], v1, s[4:7], s97 offen offset:2048 sc1\n"
"buffer_load_dwordx4 v[100:103], v1, s[4:7], s97 offen offset:3072 sc1\n"
"s_add_u32 s97, s56, 0\n"
"buffer_load_dwordx4 v[120:123], v0, s[4:7], s97 offen sc1\n"
"buffer_load_dwordx4 v[108:111], v0, s[4:7], s97 offen offset:1024 sc1\n"
"buffer_load_dwordx4 v[132:135], v0, s[4:7], s97 offen offset:2048 sc1\n"
"buffer_load_dwordx4 v[136:139], v0, s[4:7], s97 offen offset:3072 sc1\n"
"buffer_load_dwordx4 v[140:143], v1, s[4:7], s97 offen sc1\n"
"s_waitcnt vmcnt(0)\n"
"v_max_u32_e32 v255, v128, v129\n"
"v_max3_u32 v255, v255, v130, v131\n"
"v_max3_u32 v255, v255, v148, v149\n"
"v_max3_u32 v255, v255, v150, v151\n"
"v_max3_u32 v255, v255, v156, v157\n"
"v_max3_u32 v255, v255, v158, v159\n"
"v_max3_u32 v255, v255, v160, v161\n"
"v_max3_u32 v255, v255, v162, v163\n"
"v_max3_u32 v255, v255, v164, v165\n"
"v_max3_u32 v255, v255, v166, v167\n"
"v_max3_u32 v255, v255, v172, v173\n"
"v_max3_u32 v255, v255, v174, v175\n"
"v_max3_u32 v255, v255, v176, v177\n"
"v_max3_u32 v255, v255, v178, v179\n"
"v_max3_u32 v255, v255, v184, v185\n"
"v_max3_u32 v255, v255, v186, v187\n"
"v_cmp_eq_u32_e32 vcc, -1, v255\n"
"s_cbranch_vccz .Lmk_p37k\n"
"s_sub_u32 s1, s1, 1\n"
"s_cbranch_scc0 .Lmk_p37l\n"
".Lmk_p37k:\n"
"v_mfma_f32_16x16x32_bf16 v[96:99], a[128:131], v[128:131], 0\n"
"v_mfma_f32_16x16x32_bf16 v[104:107], a[132:135], v[128:131], 0\n"
"v_mfma_f32_16x16x32_bf16 v[112:115], a[136:139], v[128:131], 0\n"
"v_mfma_f32_16x16x32_bf16 v[124:127], a[140:143], v[128:131], 0\n"
"v_mfma_f32_16x16x32_bf16 v[96:99], a[144:147], v[148:151], v[96:99]\n"
"v_mfma_f32_16x16x32_bf16 v[104:107], a[148:151], v[148:151], v[104:107]\n"
"v_mfma_f32_16x16x32_bf16 v[112:115], a[152:155], v[148:151], v[112:115]\n"
"v_mfma_f32_16x16x32_bf16 v[124:127], a[156:159], v[148:151], v[124:127]\n"
"v_mfma_f32_16x16x32_bf16 v[96:99], a[160:163], v[156:159], v[96:99]\n"
"v_mfma_f32_16x16x32_bf16 v[104:107], a[164:167], v[156:159], v[104:107]\n"
"v_mfma_f32_16x16x32_bf16 v[112:115], a[168:171], v[156:159], v[112:115]\n"
"v_mfma_f32_16x16x32_bf16 v[124:127], a[172:175], v[156:159], v[124:127]\n"
"v_mfma_f32_16x16x32_bf16 v[96:99], a[176:179], v[160:163], v[96:99]\n"
"v_mfma_f32_16x16x32_bf16 v[104:107], a[180:183], v[160:163], v[104:107]\n"
"v_mfma_f32_16x16x32_bf16 v[112:115], a[184:187], v[160:163], v[112:115]\n"
"v_mfma_f32_16x16x32_bf16 v[124:127], a[188:191], v[160:163], v[124:127]\n"
"v_mfma_f32_16x16x32_bf16 v[96:99], a[192:195], v[164:167], v[96:99]\n"
"v_mfma_f32_16x16x32_bf16 v[104:107], a[196:199], v[164:167], v[104:107]\n"
"v_mfma_f32_16x16x32_bf16 v[112:115], a[200:203], v[164:167], v[112:115]\n"
"v_mfma_f32_16x16x32_bf16 v[124:127], a[204:207], v[164:167], v[124:127]\n"
"v_mfma_f32_16x16x32_bf16 v[96:99], a[208:211], v[172:175], v[96:99]\n"
"v_mfma_f32_16x16x32_bf16 v[104:107], a[212:215], v[172:175], v[104:107]\n"
"v_mfma_f32_16x16x32_bf16 v[112:115], a[216:219], v[172:175], v[112:115]\n"
"v_mfma_f32_16x16x32_bf16 v[124:127], a[220:223], v[172:175], v[124:127]\n"
"v_mfma_f32_16x16x32_bf16 v[96:99], a[224:227], v[176:179], v[96:99]\n"
"v_mfma_f32_16x16x32_bf16 v[104:107], a[228:231], v[176:179], v[104:107]\n"
"v_mfma_f32_16x16x32_bf16 v[112:115], a[232:235], v[176:179], v[112:115]\n"
"v_mfma_f32_16x16x32_bf16 v[124:127], a[236:239], v[176:179], v[124:127]\n"
"v_mfma_f32_16x16x32_bf16 v[96:99], a[240:243], v[184:187], v[96:99]\n"
"v_mfma_f32_16x16x32_bf16 v[104:107], a[244:247], v[184:187], v[104:107]\n"
"v_mfma_f32_16x16x32_bf16 v[112:115], a[248:251], v[184:187], v[112:115]\n"
"v_mfma_f32_16x16x32_bf16 v[124:127], a[252:255], v[184:187], v[124:127]\n"
"s_nop 7\n"
"s_branch .Lmk_p37r\n"
".Lmk_q39s:\n"
";\n"
"s_mov_b32 s1, 0x10000\n"
".Lmk_q39l:\n"
"s_add_u32 s97, s59, 0x40000\n"
"buffer_load_dwordx4 v[180:183], v1, s[4:7], s97 offen sc1\n"
"buffer_load_dwordx4 v[88:91], v1, s[4:7], s97 offen offset:1024 sc1\n"
"buffer_load_dwordx4 v[92:95], v1, s[4:7], s97 offen offset:2048 sc1\n"
"buffer_load_dwordx4 v[100:103], v1, s[4:7], s97 offen offset:3072 sc1\n"
"s_add_u32 s97, s56, 0\n"
"buffer_load_dwordx4 v[120:123], v0, s[4:7], s97 offen sc1\n"
"buffer_load_dwordx4 v[108:111], v0, s[4:7], s97 offen offset:1024 sc1\n"
"buffer_load_dwordx4 v[132:135], v0, s[4:7], s97 offen offset:2048 sc1\n"
"buffer_load_dwordx4 v[136:139], v0, s[4:7], s97 offen offset:3072 sc1\n"
"buffer_load_dwordx4 v[140:143], v1, s[4:7], s97 offen sc1\n"
"buffer_load_dwordx4 v[128:131], v1, s[4:7], s97 offen offset:1024 sc1\n"
"buffer_load_dwordx4 v[96:99], v1, s[4:7], s97 offen offset:2048 sc1\n"
"s_waitcnt vmcnt(0)\n"
"v_max_u32_e32 v255, v180, v181\n"
"v_max3_u32 v255, v255, v182, v183\n"
"v_max3_u32 v255, v255, v88, v89\n"
"v_max3_u32 v255, v255, v90, v91\n"
"v_max3_u32 v255, v255, v92, v93\n"
"v_max3_u32 v255, v255, v94, v95\n"
"v_max3_u32 v255, v255, v100, v101\n"
"v_max3_u32 v255, v255, v102, v103\n"
"v_cmp_eq_u32_e32 vcc, -1, v255\n"
"s_cbranch_vccz .Lmk_q39k\n"
"s_sub_u32 s1, s1, 1\n"
"s_cbranch_scc0 .Lmk_q39l\n"
".Lmk_q39k:\n"
"s_branch .Lmk_q39r\n"
".Lmk_p41s:\n"
";\n"
"s_mov_b32 s1, 0x10000\n"
".Lmk_p41l:\n"
"s_add_u32 s97, s56, 0\n"
"buffer_load_dwordx4 v[116:119], v0, s[4:7], s97 offen sc1\n"
"buffer_load_dwordx4 v[120:123], v0, s[4:7], s97 offen offset:1024 sc1\n"
"buffer_load_dwordx4 v[172:175], v0, s[4:7], s97 offen offset:2048 sc1\n"
"buffer_load_dwordx4 v[132:135], v0, s[4:7], s97 offen offset:3072 sc1\n"
"buffer_load_dwordx4 v[136:139], v1, s[4:7], s97 offen sc1\n"
"buffer_load_dwordx4 v[140:143], v1, s[4:7], s97 offen offset:1024 sc1\n"
"buffer_load_dwordx4 v[128:131], v1, s[4:7], s97 offen offset:2048 sc1\n"
"buffer_load_dwordx4 v[176:179], v1, s[4:7], s97 offen offset:3072 sc1\n"
"s_add_u32 s97, s57, 0\n"
"buffer_load_dwordx4 v[112:115], v0, s[4:7], s97 offen sc1\n"
"buffer_load_dwordx4 v[144:147], v0, s[4:7], s97 offen offset:1024 sc1\n"
"buffer_load_dwordx4 v[160:163], v0, s[4:7], s97 offen offset:2048 sc1\n"
"buffer_load_dwordx4 v[92:95], v0, s[4:7], s97 offen offset:3072 sc1\n"
"buffer_load_dwordx4 v[124:127], v1, s[4:7], s97 offen sc1\n"
"buffer_load_dwordx4 v[152:155], v1, s[4:7], s97 offen offset:1024 sc1\n"
"buffer_load_dwordx4 v[156:159], v1, s[4:7], s97 offen offset:2048 sc1\n"
"buffer_load_dwordx4 v[168:171], v1, s[4:7], s97 offen offset:3072 sc1\n"
"s_waitcnt vmcnt(0)\n"
"v_max_u32_e32 v255, v116, v117\n"
"v_max3_u32 v255, v255, v118, v119\n"
"v_max3_u32 v255, v255, v120, v121\n"
"v_max3_u32 v255, v255, v122, v123\n"
"v_max3_u32 v255, v255, v172, v173\n"
"v_max3_u32 v255, v255, v174, v175\n"
"v_max3_u32 v255, v255, v132, v133\n"
"v_max3_u32 v255, v255, v134, v135\n"
"v_max3_u32 v255, v255, v136, v137\n"
"v_max3_u32 v255, v255, v138, v139\n"
"v_max3_u32 v255, v255, v140, v141\n"
"v_max3_u32 v255, v255, v142, v143\n"
"v_max3_u32 v255, v255, v128, v129\n"
"v_max3_u32 v255, v255, v130, v131\n"
"v_max3_u32 v255, v255, v176, v177\n"
"v_max3_u32 v255, v255, v178, v179\n"
"v_cmp_eq_u32_e32 vcc, -1, v255\n"
"s_cbranch_vccz .Lmk_p41k\n"
"s_sub_u32 s1, s1, 1\n"
"s_cbranch_scc0 .Lmk_p41l\n"
".Lmk_p41k:\n"
"v_mfma_f32_16x16x32_bf16 v[88:91], a[128:131], v[116:119], 0\n"
"v_mfma_f32_16x16x32_bf16 v[100:103], a[132:135], v[116:119], 0\n"
"v_mfma_f32_16x16x32_bf16 v[148:151], a[136:139], v[116:119], 0\n"
"v_mfma_f32_16x16x32_bf16 v[164:167], a[140:143], v[116:119], 0\n"
"v_mfma_f32_16x16x32_bf16 v[88:91], a[144:147], v[120:123], v[88:91]\n"
"v_mfma_f32_16x16x32_bf16 v[100:103], a[148:151], v[120:123], v[100:103]\n"
"v_mfma_f32_16x16x32_bf16 v[148:151], a[152:155], v[120:123], v[148:151]\n"
"v_mfma_f32_16x16x32_bf16 v[164:167], a[156:159], v[120:123], v[164:167]\n"
"v_mfma_f32_16x16x32_bf16 v[88:91], a[160:163], v[172:175], v[88:91]\n"
"v_mfma_f32_16x16x32_bf16 v[100:103], a[164:167], v[172:175], v[100:103]\n"
"v_mfma_f32_16x16x32_bf16 v[148:151], a[168:171], v[172:175], v[148:151]\n"
"v_mfma_f32_16x16x32_bf16 v[164:167], a[172:175], v[172:175], v[164:167]\n"
"v_mfma_f32_16x16x32_bf16 v[88:91], a[176:179], v[132:135], v[88:91]\n"
"v_mfma_f32_16x16x32_bf16 v[100:103], a[180:183], v[132:135], v[100:103]\n"
"v_mfma_f32_16x16x32_bf16 v[148:151], a[184:187], v[132:135], v[148:151]\n"
"v_mfma_f32_16x16x32_bf16 v[164:167], a[188:191], v[132:135], v[164:167]\n"
"v_mfma_f32_16x16x32_bf16 v[88:91], a[192:195], v[136:139], v[88:91]\n"
"v_mfma_f32_16x16x32_bf16 v[100:103], a[196:199], v[136:139], v[100:103]\n"
"v_mfma_f32_16x16x32_bf16 v[148:151], a[200:203], v[136:139], v[148:151]\n"
"v_mfma_f32_16x16x32_bf16 v[164:167], a[204:207], v[136:139], v[164:167]\n"
"v_mfma_f32_16x16x32_bf16 v[88:91], a[208:211], v[140:143], v[88:91]\n"
"v_mfma_f32_16x16x32_bf16 v[100:103], a[212:215], v[140:143], v[100:103]\n"
"v_mfma_f32_16x16x32_bf16 v[148:151], a[216:219], v[140:143], v[148:151]\n"
"v_mfma_f32_16x16x32_bf16 v[164:167], a[220:223], v[140:143], v[164:167]\n"
"v_mfma_f32_16x16x32_bf16 v[88:91], a[224:227], v[128:131], v[88:91]\n"
"v_mfma_f32_16x16x32_bf16 v[100:103], a[228:231], v[128:131], v[100:103]\n"
"v_mfma_f32_16x16x32_bf16 v[148:151], a[232:235], v[128:131], v[148:151]\n"
"v_mfma_f32_16x16x32_bf16 v[164:167], a[236:239], v[128:131], v[164:167]\n"
"v_mfma_f32_16x16x32_bf16 v[88:91], a[240:243], v[176:179], v[88:91]\n"
"v_mfma_f32_16x16x32_bf16 v[100:103], a[244:247], v[176:179], v[100:103]\n"
"v_mfma_f32_16x16x32_bf16 v[148:151], a[248:251], v[176:179], v[148:151]\n"
"v_mfma_f32_16x16x32_bf16 v[164:167], a[252:255], v[176:179], v[164:167]\n"
"s_add_u32 s97, s58, 0\n"
"v_mov_b32_e32 v255, 0x80000000\n"
"buffer_load_dwordx4 v[116:119], v0, s[4:7], s97 offen sc1\n"
"buffer_load_dwordx4 v[120:123], v0, s[4:7], s97 offen offset:1024 sc1\n"
"buffer_load_dwordx4 v[172:175], v0, s[4:7], s97 offen offset:2048 sc1\n"
"buffer_load_dwordx4 v[132:135], v0, s[4:7], s97 offen offset:3072 sc1\n"
"buffer_load_dwordx4 v[136:139], v1, s[4:7], s97 offen sc1\n"
"buffer_load_dwordx4 v[140:143], v1, s[4:7], s97 offen offset:1024 sc1\n"
"buffer_load_dwordx4 v[128:131], v1, s[4:7], s97 offen offset:2048 sc1\n"
"buffer_load_dwordx4 v[176:179], v1, s[4:7], s97 offen offset:3072 sc1\n"
"s_nop 7\n"
"s_branch .Lmk_p41r\n"
".Lmk_p42s:\n"
";\n"
"s_mov_b32 s1, 0x10000\n"
".Lmk_p42l:\n"
"s_add_u32 s95, s57, 0\n"
"buffer_load_dwordx4 v[104:107], v0, s[4:7], s95 offen sc1\n"
"buffer_load_dwordx4 v[112:115], v0, s[4:7], s95 offen offset:1024 sc1\n"
"buffer_load_dwordx4 v[124:127], v0, s[4:7], s95 offen offset:2048 sc1\n"
"buffer_load_dwordx4 v[200:203], v0, s[4:7], s95 offen offset:3072 sc1\n"
"buffer_load_dwordx4 v[204:207], v1, s[4:7], s95 offen sc1\n"
"buffer_load_dwordx4 v[208:211], v1, s[4:7], s95 offen offset:1024 sc1\n"
"buffer_load_dwordx4 v[212:215], v1, s[4:7], s95 offen offset:2048 sc1\n"
"buffer_load_dwordx4 v[216:219], v1, s[4:7], s95 offen offset:3072 sc1\n"
"s_add_u32 s95, s58, 0\n"
"buffer_load_dwordx4 v[116:119], v0, s[4:7], s95 offen sc1\n"
"buffer_load_dwordx4 v[120:123], v0, s[4:7], s95 offen offset:1024 sc1\n"
"buffer_load_dwordx4 v[172:175], v0, s[4:7], s95 offen offset:2048 sc1\n"
"buffer_load_dwordx4 v[132:135], v0, s[4:7], s95 offen offset:3072 sc1\n"
"buffer_load_dwordx4 v[136:139], v1, s[4:7], s95 offen sc1\n"
"buffer_load_dwordx4 v[140:143], v1, s[4:7], s95 offen offset:1024 sc1\n"
"buffer_load_dwordx4 v[128:131], v1, s[4:7], s95 offen offset:2048 sc1\n"
"buffer_load_dwordx4 v[176:179], v1, s[4:7], s95 offen offset:3072 sc1\n"
"s_add_u32 s95, s59, 0\n"
"buffer_load_dwordx4 v[188:191], v0, s[4:7], s95 offen sc1\n"
"buffer_load_dwordx4 v[144:147], v0, s[4:7], s95 offen offset:1024 sc1\n"
"buffer_load_dwordx4 v[148:151], v0, s[4:7], s95 offen offset:2048 sc1\n"
"buffer_load_dwordx4 v[160:163], v0, s[4:7], s95 offen offset:3072 sc1\n"
"buffer_load_dwordx4 v[164:167], v1, s[4:7], s95 offen sc1\n"
"buffer_load_dwordx4 v[152:155], v1, s[4:7], s95 offen offset:1024 sc1\n"
"buffer_load_dwordx4 v[196:199], v1, s[4:7], s95 offen offset:2048 sc1\n"
"s_waitcnt vmcnt(0)\n"
"v_max_u32_e32 v237, v104, v105\n"
"v_max3_u32 v237, v237, v106, v107\n"
"v_max3_u32 v237, v237, v112, v113\n"
"v_max3_u32 v237, v237, v114, v115\n"
"v_max3_u32 v237, v237, v124, v125\n"
"v_max3_u32 v237, v237, v126, v127\n"
"v_max3_u32 v237, v237, v200, v201\n"
"v_max3_u32 v237, v237, v202, v203\n"
"v_max3_u32 v237, v237, v204, v205\n"
"v_max3_u32 v237, v237, v206, v207\n"
"v_max3_u32 v237, v237, v208, v209\n"
"v_max3_u32 v237, v237, v210, v211\n"
"v_max3_u32 v237, v237, v212, v213\n"
"v_max3_u32 v237, v237, v214, v215\n"
"v_max3_u32 v237, v237, v216, v217\n"
"v_max3_u32 v237, v237, v218, v219\n"
"v_cmp_eq_u32_e32 vcc, -1, v237\n"
"s_cbranch_vccz .Lmk_p42k\n"
"s_sub_u32 s1, s1, 1\n"
"s_cbranch_scc0 .Lmk_p42l\n"
".Lmk_p42k:\n"
"v_mfma_f32_16x16x32_bf16 v[108:111], a[128:131], v[104:107], 0\n"
"v_mfma_f32_16x16x32_bf16 v[180:183], a[132:135], v[104:107], 0\n"
"v_mfma_f32_16x16x32_bf16 v[184:187], a[136:139], v[104:107], 0\n"
"v_mfma_f32_16x16x32_bf16 v[192:195], a[140:143], v[104:107], 0\n"
"v_mfma_f32_16x16x32_bf16 v[108:111], a[144:147], v[112:115], v[108:111]\n"
"v_mfma_f32_16x16x32_bf16 v[180:183], a[148:151], v[112:115], v[180:183]\n"
"v_mfma_f32_16x16x32_bf16 v[184:187], a[152:155], v[112:115], v[184:187]\n"
"v_mfma_f32_16x16x32_bf16 v[192:195], a[156:159], v[112:115], v[192:195]\n"
"v_mfma_f32_16x16x32_bf16 v[108:111], a[160:163], v[124:127], v[108:111]\n"
"v_mfma_f32_16x16x32_bf16 v[180:183], a[164:167], v[124:127], v[180:183]\n"
"v_mfma_f32_16x16x32_bf16 v[184:187], a[168:171], v[124:127], v[184:187]\n"
"v_mfma_f32_16x16x32_bf16 v[192:195], a[172:175], v[124:127], v[192:195]\n"
"v_mfma_f32_16x16x32_bf16 v[108:111], a[176:179], v[200:203], v[108:111]\n"
"v_mfma_f32_16x16x32_bf16 v[180:183], a[180:183], v[200:203], v[180:183]\n"
"v_mfma_f32_16x16x32_bf16 v[184:187], a[184:187], v[200:203], v[184:187]\n"
"v_mfma_f32_16x16x32_bf16 v[192:195], a[188:191], v[200:203], v[192:195]\n"
"v_mfma_f32_16x16x32_bf16 v[108:111], a[192:195], v[204:207], v[108:111]\n"
"v_mfma_f32_16x16x32_bf16 v[180:183], a[196:199], v[204:207], v[180:183]\n"
"v_mfma_f32_16x16x32_bf16 v[184:187], a[200:203], v[204:207], v[184:187]\n"
"v_mfma_f32_16x16x32_bf16 v[192:195], a[204:207], v[204:207], v[192:195]\n"
"v_mfma_f32_16x16x32_bf16 v[108:111], a[208:211], v[208:211], v[108:111]\n"
"v_mfma_f32_16x16x32_bf16 v[180:183], a[212:215], v[208:211], v[180:183]\n"
"v_mfma_f32_16x16x32_bf16 v[184:187], a[216:219], v[208:211], v[184:187]\n"
"v_mfma_f32_16x16x32_bf16 v[192:195], a[220:223], v[208:211], v[192:195]\n"
"v_mfma_f32_16x16x32_bf16 v[108:111], a[224:227], v[212:215], v[108:111]\n"
"v_mfma_f32_16x16x32_bf16 v[180:183], a[228:231], v[212:215], v[180:183]\n"
"v_mfma_f32_16x16x32_bf16 v[184:187], a[232:235], v[212:215], v[184:187]\n"
"v_mfma_f32_16x16x32_bf16 v[192:195], a[236:239], v[212:215], v[192:195]\n"
"v_mfma_f32_16x16x32_bf16 v[108:111], a[240:243], v[216:219], v[108:111]\n"
"v_mfma_f32_16x16x32_bf16 v[180:183], a[244:247], v[216:219], v[180:183]\n"
"v_mfma_f32_16x16x32_bf16 v[184:187], a[248:251], v[216:219], v[184:187]\n"
"v_mfma_f32_16x16x32_bf16 v[192:195], a[252:255], v[216:219], v[192:195]\n"
"s_nop 7\n"
"s_branch .Lmk_p42r\n"
".Lmk_q44s:\n"
";\n"
"s_mov_b32 s1, 0x10000\n"
".Lmk_q44l:\n"
"s_add_u32 s98, s59, 0\n"
"buffer_load_dwordx4 v[188:191], v0, s[4:7], s98 offen sc1\n"
"buffer_load_dwordx4 v[144:147], v0, s[4:7], s98 offen offset:1024 sc1\n"
"buffer_load_dwordx4 v[148:151], v0, s[4:7], s98 offen offset:2048 sc1\n"
"buffer_load_dwordx4 v[160:163], v0, s[4:7], s98 offen offset:3072 sc1\n"
"buffer_load_dwordx4 v[164:167], v1, s[4:7], s98 offen sc1\n"
"buffer_load_dwordx4 v[152:155], v1, s[4:7], s98 offen offset:1024 sc1\n"
"buffer_load_dwordx4 v[196:199], v1, s[4:7], s98 offen offset:2048 sc1\n"
"buffer_load_dwordx4 v[204:207], v1, s[4:7], s98 offen offset:3072 sc1\n"
"s_add_u32 s98, s52, 0x440000\n"
"buffer_load_dwordx4 v[104:107], v0, s[4:7], s98 offen sc1\n"
"buffer_load_dwordx4 v[108:111], v0, s[4:7], s98 offen offset:1024 sc1\n"
"buffer_load_dwordx4 v[112:115], v0, s[4:7], s98 offen offset:2048 sc1\n"
"buffer_load_dwordx4 v[116:119], v0, s[4:7], s98 offen offset:3072 sc1\n"
"buffer_load_dwordx4 v[120:123], v1, s[4:7], s98 offen sc1\n"
"buffer_load_dwordx4 v[124:127], v1, s[4:7], s98 offen offset:1024 sc1\n"
"s_waitcnt vmcnt(0)\n"
"v_max_u32_e32 v253, v188, v189\n"
"v_max3_u32 v253, v253, v190, v191\n"
"v_max3_u32 v253, v253, v144, v145\n"
"v_max3_u32 v253, v253, v146, v147\n"
"v_max3_u32 v253, v253, v148, v149\n"
"v_max3_u32 v253, v253, v150, v151\n"
"v_max3_u32 v253, v253, v160, v161\n"
"v_max3_u32 v253, v253, v162, v163\n"
"v_cmp_eq_u32_e32 vcc, -1, v253\n"
"s_cbranch_vccz .Lmk_q44k\n"
"s_sub_u32 s1, s1, 1\n"
"s_cbranch_scc0 .Lmk_q44l\n"
".Lmk_q44k:\n"
"s_branch .Lmk_q44r\n"
".Lmk_p43s:\n"
";\n"
"s_mov_b32 s1, 0x10000\n"
".Lmk_p43l:\n"
"s_add_u32 s98, s58, 0\n"
"buffer_load_dwordx4 v[132:135], v0, s[4:7], s98 offen sc1\n"
"buffer_load_dwordx4 v[136:139], v0, s[4:7], s98 offen offset:1024 sc1\n"
"buffer_load_dwordx4 v[140:143], v0, s[4:7], s98 offen offset:2048 sc1\n"
"buffer_load_dwordx4 v[172:175], v0, s[4:7], s98 offen offset:3072 sc1\n"
"buffer_load_dwordx4 v[176:179], v1, s[4:7], s98 offen sc1\n"
"buffer_load_dwordx4 v[180:183], v1, s[4:7], s98 offen offset:1024 sc1\n"
"buffer_load_dwordx4 v[184:187], v1, s[4:7], s98 offen offset:2048 sc1\n"
"buffer_load_dwordx4 v[192:195], v1, s[4:7], s98 offen offset:3072 sc1\n"
"s_add_u32 s98, s59, 0\n"
"buffer_load_dwordx4 v[188:191], v0, s[4:7], s98 offen sc1\n"
"buffer_load_dwordx4 v[144:147], v0, s[4:7], s98 offen offset:1024 sc1\n"
"buffer_load_dwordx4 v[148:151], v0, s[4:7], s98 offen offset:2048 sc1\n"
"buffer_load_dwordx4 v[160:163], v0, s[4:7], s98 offen offset:3072 sc1\n"
"buffer_load_dwordx4 v[164:167], v1, s[4:7], s98 offen sc1\n"
"buffer_load_dwordx4 v[152:155], v1, s[4:7], s98 offen offset:1024 sc1\n"
"buffer_load_dwordx4 v[196:199], v1, s[4:7], s98 offen offset:2048 sc1\n"
"buffer_load_dwordx4 v[204:207], v1, s[4:7], s98 offen offset:3072 sc1\n"
"s_add_u32 s98, s52, 0x440000\n"
"buffer_load_dwordx4 v[104:107], v0, s[4:7], s98 offen sc1\n"
"buffer_load_dwordx4 v[108:111], v0, s[4:7], s98 offen offset:1024 sc1\n"
"buffer_load_dwordx4 v[112:115], v0, s[4:7], s98 offen offset:2048 sc1\n"
"buffer_load_dwordx4 v[116:119], v0, s[4:7], s98 offen offset:3072 sc1\n"
"buffer_load_dwordx4 v[120:123], v1, s[4:7], s98 offen sc1\n"
"buffer_load_dwordx4 v[124:127], v1, s[4:7], s98 offen offset:1024 sc1\n"
"buffer_load_dwordx4 v[128:131], v1, s[4:7], s98 offen offset:2048 sc1\n"
"s_waitcnt vmcnt(0)\n"
"v_max_u32_e32 v253, v132, v133\n"
"v_max3_u32 v253, v253, v134, v135\n"
"v_max3_u32 v253, v253, v136, v137\n"
"v_max3_u32 v253, v253, v138, v139\n"
"v_max3_u32 v253, v253, v140, v141\n"
"v_max3_u32 v253, v253, v142, v143\n"
"v_max3_u32 v253, v253, v172, v173\n"
"v_max3_u32 v253, v253, v174, v175\n"
"v_max3_u32 v253, v253, v176, v177\n"
"v_max3_u32 v253, v253, v178, v179\n"
"v_max3_u32 v253, v253, v180, v181\n"
"v_max3_u32 v253, v253, v182, v183\n"
"v_max3_u32 v253, v253, v184, v185\n"
"v_max3_u32 v253, v253, v186, v187\n"
"v_max3_u32 v253, v253, v192, v193\n"
"v_max3_u32 v253, v253, v194, v195\n"
"v_cmp_eq_u32_e32 vcc, -1, v253\n"
"s_cbranch_vccz .Lmk_p43k\n"
"s_sub_u32 s1, s1, 1\n"
"s_cbranch_scc0 .Lmk_p43l\n"
".Lmk_p43k:\n"
"v_mfma_f32_16x16x32_bf16 v[156:159], a[128:131], v[132:135], 0\n"
"v_mfma_f32_16x16x32_bf16 v[168:171], a[132:135], v[132:135], 0\n"
"v_mfma_f32_16x16x32_bf16 v[200:203], a[136:139], v[132:135], 0\n"
"v_mfma_f32_16x16x32_bf16 v[208:211], a[140:143], v[132:135], 0\n"
"v_mfma_f32_16x16x32_bf16 v[156:159], a[144:147], v[136:139], v[156:159]\n"
"v_mfma_f32_16x16x32_bf16 v[168:171], a[148:151], v[136:139], v[168:171]\n"
"v_mfma_f32_16x16x32_bf16 v[200:203], a[152:155], v[136:139], v[200:203]\n"
"v_mfma_f32_16x16x32_bf16 v[208:211], a[156:159], v[136:139], v[208:211]\n"
"v_mfma_f32_16x16x32_bf16 v[156:159], a[160:163], v[140:143], v[156:159]\n"
"v_mfma_f32_16x16x32_bf16 v[168:171], a[164:167], v[140:143], v[168:171]\n"
"v_mfma_f32_16x16x32_bf16 v[200:203], a[168:171], v[140:143], v[200:203]\n"
"v_mfma_f32_16x16x32_bf16 v[208:211], a[172:175], v[140:143], v[208:211]\n"
"v_mfma_f32_16x16x32_bf16 v[156:159], a[176:179], v[172:175], v[156:159]\n"
"v_mfma_f32_16x16x32_bf16 v[168:171], a[180:183], v[172:175], v[168:171]\n"
"v_mfma_f32_16x16x32_bf16 v[200:203], a[184:187], v[172:175], v[200:203]\n"
"v_mfma_f32_16x16x32_bf16 v[208:211], a[188:191], v[172:175], v[208:211]\n"
"v_mfma_f32_16x16x32_bf16 v[156:159], a[192:195], v[176:179], v[156:159]\n"
"v_mfma_f32_16x16x32_bf16 v[168:171], a[196:199], v[176:179], v[168:171]\n"
"v_mfma_f32_16x16x32_bf16 v[200:203], a[200:203], v[176:179], v[200:203]\n"
"v_mfma_f32_16x16x32_bf16 v[208:211], a[204:207], v[176:179], v[208:211]\n"
"v_mfma_f32_16x16x32_bf16 v[156:159], a[208:211], v[180:183], v[156:159]\n"
"v_mfma_f32_16x16x32_bf16 v[168:171], a[212:215], v[180:183], v[168:171]\n"
"v_mfma_f32_16x16x32_bf16 v[200:203], a[216:219], v[180:183], v[200:203]\n"
"v_mfma_f32_16x16x32_bf16 v[208:211], a[220:223], v[180:183], v[208:211]\n"
"v_mfma_f32_16x16x32_bf16 v[156:159], a[224:227], v[184:187], v[156:159]\n"
"v_mfma_f32_16x16x32_bf16 v[168:171], a[228:231], v[184:187], v[168:171]\n"
"v_mfma_f32_16x16x32_bf16 v[200:203], a[232:235], v[184:187], v[200:203]\n"
"v_mfma_f32_16x16x32_bf16 v[208:211], a[236:239], v[184:187], v[208:211]\n"
"v_mfma_f32_16x16x32_bf16 v[156:159], a[240:243], v[192:195], v[156:159]\n"
"v_mfma_f32_16x16x32_bf16 v[168:171], a[244:247], v[192:195], v[168:171]\n"
"v_mfma_f32_16x16x32_bf16 v[200:203], a[248:251], v[192:195], v[200:203]\n"
"v_mfma_f32_16x16x32_bf16 v[208:211], a[252:255], v[192:195], v[208:211]\n"
"s_nop 7\n"
"s_branch .Lmk_p43r\n"
".Lmk_f13s:\n"
";\n"
"s_mov_b32 s1, 0x10000\n"
".Lmk_f13l:\n"
"buffer_load_dwordx2 v[254:255], v13, s[4:7], s97 offen sc1\n"
"s_waitcnt vmcnt(0)\n"
"v_cmp_ne_u64_e32 vcc, v[254:255], v[18:19]\n"
"s_cbranch_vccz .Lmk_f13r\n"
"s_sub_u32 s1, s1, 1\n"
"s_cbranch_scc0 .Lmk_f13l\n"
"s_branch .Lmk_f13r\n"
".Lmk_q45s:\n"
";\n"
"s_mov_b32 s1, 0x10000\n"
".Lmk_q45l:\n"
"s_add_u32 s97, s59, 0\n"
"buffer_load_dwordx4 v[164:167], v1, s[4:7], s97 offen sc1\n"
"buffer_load_dwordx4 v[152:155], v1, s[4:7], s97 offen offset:1024 sc1\n"
"buffer_load_dwordx4 v[196:199], v1, s[4:7], s97 offen offset:2048 sc1\n"
"buffer_load_dwordx4 v[204:207], v1, s[4:7], s97 offen offset:3072 sc1\n"
"s_add_u32 s97, s52, 0x440000\n"
"buffer_load_dwordx4 v[104:107], v0, s[4:7], s97 offen sc1\n"
"buffer_load_dwordx4 v[108:111], v0, s[4:7], s97 offen offset:1024 sc1\n"
"buffer_load_dwordx4 v[112:115], v0, s[4:7], s97 offen offset:2048 sc1\n"
"buffer_load_dwordx4 v[116:119], v0, s[4:7], s97 offen offset:3072 sc1\n"
"buffer_load_dwordx4 v[120:123], v1, s[4:7], s97 offen sc1\n"
"buffer_load_dwordx4 v[124:127], v1, s[4:7], s97 offen offset:1024 sc1\n"
"buffer_load_dwordx4 v[128:131], v1, s[4:7], s97 offen offset:2048 sc1\n"
"buffer_load_dwordx4 v[132:135], v1, s[4:7], s97 offen offset:3072 sc1\n"
"s_waitcnt vmcnt(0)\n"
"v_max_u32_e32 v255, v164, v165\n"
"v_max3_u32 v255, v255, v166, v167\n"
"v_max3_u32 v255, v255, v152, v153\n"
"v_max3_u32 v255, v255, v154, v155\n"
"v_max3_u32 v255, v255, v196, v197\n"
"v_max3_u32 v255, v255, v198, v199\n"
"v_max3_u32 v255, v255, v204, v205\n"
"v_max3_u32 v255, v255, v206, v207\n"
"v_cmp_eq_u32_e32 vcc, -1, v255\n"
"s_cbranch_vccz .Lmk_q45k\n"
"s_sub_u32 s1, s1, 1\n"
"s_cbranch_scc0 .Lmk_q45l\n"
".Lmk_q45k:\n"
"s_branch .Lmk_q45r\n"
".Lmk_p50s:\n"
";\n"
"s_mov_b32 s1, 0x10000\n"
".Lmk_p50l:\n"
"s_add_u32 s97, s52, 0x440000\n"
"buffer_load_dwordx4 v[204:207], v0, s[4:7], s97 offen sc1\n"
"buffer_load_dwordx4 v[212:215], v0, s[4:7], s97 offen offset:1024 sc1\n"
"buffer_load_dwordx4 v[88:91], v0, s[4:7], s97 offen offset:2048 sc1\n"
"buffer_load_dwordx4 v[108:111], v0, s[4:7], s97 offen offset:3072 sc1\n"
"buffer_load_dwordx4 v[96:99], v1, s[4:7], s97 offen sc1\n"
"buffer_load_dwordx4 v[100:103], v1, s[4:7], s97 offen offset:1024 sc1\n"
"buffer_load_dwordx4 v[92:95], v1, s[4:7], s97 offen offset:2048 sc1\n"
"buffer_load_dwordx4 v[104:107], v1, s[4:7], s97 offen offset:3072 sc1\n"
"s_add_u32 s97, s53, 0x440000\n"
"buffer_load_dwordx4 v[136:139], v0, s[4:7], s97 offen sc1\n"
"buffer_load_dwordx4 v[140:143], v0, s[4:7], s97 offen offset:1024 sc1\n"
"buffer_load_dwordx4 v[156:159], v0, s[4:7], s97 offen offset:2048 sc1\n"
"buffer_load_dwordx4 v[164:167], v0, s[4:7], s97 offen offset:3072 sc1\n"
"buffer_load_dwordx4 v[168:171], v1, s[4:7], s97 offen sc1\n"
"buffer_load_dwordx4 v[172:175], v1, s[4:7], s97 offen offset:1024 sc1\n"
"buffer_load_dwordx4 v[176:179], v1, s[4:7], s97 offen offset:2048 sc1\n"
"buffer_load_dwordx4 v[180:183], v1, s[4:7], s97 offen offset:3072 sc1\n"
"s_waitcnt vmcnt(0)\n"
"v_max_u32_e32 v247, v204, v205\n"
"v_max3_u32 v247, v247, v206, v207\n"
"v_max3_u32 v247, v247, v212, v213\n"
"v_max3_u32 v247, v247, v214, v215\n"
"v_max3_u32 v247, v247, v88, v89\n"
"v_max3_u32 v247, v247, v90, v91\n"
"v_max3_u32 v247, v247, v108, v109\n"
"v_max3_u32 v247, v247, v110, v111\n"
"v_max3_u32 v247, v247, v96, v97\n"
"v_max3_u32 v247, v247, v98, v99\n"
"v_max3_u32 v247, v247, v100, v101\n"
"v_max3_u32 v247, v247, v102, v103\n"
"v_max3_u32 v247, v247, v92, v93\n"
"v_max3_u32 v247, v247, v94, v95\n"
"v_max3_u32 v247, v247, v104, v105\n"
"v_max3_u32 v247, v247, v106, v107\n"
"v_cmp_eq_u32_e32 vcc, -1, v247\n"
"s_cbranch_vccz .Lmk_p50k\n"
"s_sub_u32 s1, s1, 1\n"
"s_cbranch_scc0 .Lmk_p50l\n"
".Lmk_p50k:\n"
"v_mfma_f32_16x16x32_bf16 v[184:187], a[0:3], v[204:207], 0\n"
"v_mfma_f32_16x16x32_bf16 v[188:191], a[4:7], v[204:207], 0\n"
"v_mfma_f32_16x16x32_bf16 v[192:195], a[8:11], v[204:207], 0\n"
"v_mfma_f32_16x16x32_bf16 v[196:199], a[12:15], v[204:207], 0\n"
"v_mfma_f32_16x16x32_bf16 v[200:203], a[16:19], v[204:207], 0\n"
"v_mfma_f32_16x16x32_bf16 v[208:211], a[20:23], v[204:207], 0\n"
"v_mfma_f32_16x16x32_bf16 v[184:187], a[24:27], v[212:215], v[184:187]\n"
"v_mfma_f32_16x16x32_bf16 v[188:191], a[28:31], v[212:215], v[188:191]\n"
"v_mfma_f32_16x16x32_bf16 v[192:195], a[32:35], v[212:215], v[192:195]\n"
"v_mfma_f32_16x16x32_bf16 v[196:199], a[36:39], v[212:215], v[196:199]\n"
"v_mfma_f32_16x16x32_bf16 v[200:203], a[40:43], v[212:215], v[200:203]\n"
"v_mfma_f32_16x16x32_bf16 v[208:211], a[44:47], v[212:215], v[208:211]\n"
"v_mfma_f32_16x16x32_bf16 v[184:187], a[48:51], v[88:91], v[184:187]\n"
"v_mfma_f32_16x16x32_bf16 v[188:191], a[52:55], v[88:91], v[188:191]\n"
"v_mfma_f32_16x16x32_bf16 v[192:195], a[56:59], v[88:91], v[192:195]\n"
"v_mfma_f32_16x16x32_bf16 v[196:199], a[60:63], v[88:91], v[196:199]\n"
"v_mfma_f32_16x16x32_bf16 v[200:203], a[64:67], v[88:91], v[200:203]\n"
"v_mfma_f32_16x16x32_bf16 v[208:211], a[68:71], v[88:91], v[208:211]\n"
"v_mfma_f32_16x16x32_bf16 v[184:187], a[72:75], v[108:111], v[184:187]\n"
"v_mfma_f32_16x16x32_bf16 v[188:191], a[76:79], v[108:111], v[188:191]\n"
"v_mfma_f32_16x16x32_bf16 v[192:195], a[80:83], v[108:111], v[192:195]\n"
"v_mfma_f32_16x16x32_bf16 v[196:199], a[84:87], v[108:111], v[196:199]\n"
"v_mfma_f32_16x16x32_bf16 v[200:203], a[88:91], v[108:111], v[200:203]\n"
"v_mfma_f32_16x16x32_bf16 v[208:211], a[92:95], v[108:111], v[208:211]\n"
"v_mfma_f32_16x16x32_bf16 v[184:187], a[96:99], v[96:99], v[184:187]\n"
"v_mfma_f32_16x16x32_bf16 v[188:191], a[100:103], v[96:99], v[188:191]\n"
"v_mfma_f32_16x16x32_bf16 v[192:195], a[104:107], v[96:99], v[192:195]\n"
"v_mfma_f32_16x16x32_bf16 v[196:199], a[108:111], v[96:99], v[196:199]\n"
"v_mfma_f32_16x16x32_bf16 v[200:203], a[112:115], v[96:99], v[200:203]\n"
"v_mfma_f32_16x16x32_bf16 v[208:211], a[116:119], v[96:99], v[208:211]\n"
"v_mfma_f32_16x16x32_bf16 v[184:187], a[120:123], v[100:103], v[184:187]\n"
"v_mfma_f32_16x16x32_bf16 v[188:191], a[124:127], v[100:103], v[188:191]\n"
"v_mfma_f32_16x16x32_bf16 v[192:195], v[24:27], v[100:103], v[192:195]\n"
"v_mfma_f32_16x16x32_bf16 v[196:199], v[28:31], v[100:103], v[196:199]\n"
"v_mfma_f32_16x16x32_bf16 v[200:203], v[32:35], v[100:103], v[200:203]\n"
"v_mfma_f32_16x16x32_bf16 v[208:211], v[36:39], v[100:103], v[208:211]\n"
"v_mfma_f32_16x16x32_bf16 v[184:187], v[40:43], v[92:95], v[184:187]\n"
"v_mfma_f32_16x16x32_bf16 v[188:191], v[44:47], v[92:95], v[188:191]\n"
"v_mfma_f32_16x16x32_bf16 v[192:195], v[48:51], v[92:95], v[192:195]\n"
"v_mfma_f32_16x16x32_bf16 v[196:199], v[52:55], v[92:95], v[196:199]\n"
"v_mfma_f32_16x16x32_bf16 v[200:203], v[56:59], v[92:95], v[200:203]\n"
"v_mfma_f32_16x16x32_bf16 v[208:211], v[60:63], v[92:95], v[208:211]\n"
"v_mfma_f32_16x16x32_bf16 v[184:187], v[64:67], v[104:107], v[184:187]\n"
"v_mfma_f32_16x16x32_bf16 v[188:191], v[68:71], v[104:107], v[188:191]\n"
"v_mfma_f32_16x16x32_bf16 v[192:195], v[72:75], v[104:107], v[192:195]\n"
"v_mfma_f32_16x16x32_bf16 v[196:199], v[76:79], v[104:107], v[196:199]\n"
"v_mfma_f32_16x16x32_bf16 v[200:203], v[80:83], v[104:107], v[200:203]\n"
"v_mfma_f32_16x16x32_bf16 v[208:211], v[84:87], v[104:107], v[208:211]\n"
"s_add_u32 s97, s54, 0x440000\n"
"v_mov_b32_e32 v247, 0x80000000\n"
"buffer_load_dwordx4 v[204:207], v0, s[4:7], s97 offen sc1\n"
"buffer_store_dword v247, v247, s[4:7], 0 offen\n"
"buffer_store_dword v247, v247, s[4:7], 0 offen\n"
"buffer_load_dwordx4 v[212:215], v0, s[4:7], s97 offen offset:1024 sc1\n"
"buffer_store_dword v247, v247, s[4:7], 0 offen\n"
"buffer_store_dword v247, v247, s[4:7], 0 offen\n"
"buffer_load_dwordx4 v[88:91], v0, s[4:7], s97 offen offset:2048 sc1\n"
"buffer_store_dword v247, v247, s[4:7], 0 offen\n"
"buffer_store_dword v247, v247, s[4:7], 0 offen\n"
"buffer_store_dword v247, v247, s[4:7], 0 offen\n"
"buffer_load_dwordx4 v[108:111], v0, s[4:7], s97 offen offset:3072 sc1\n"
"buffer_store_dword v247, v247, s[4:7], 0 offen\n"
"buffer_store_dword v247, v247, s[4:7], 0 offen\n"
"buffer_load_dwordx4 v[96:99], v1, s[4:7], s97 offen sc1\n"
"buffer_store_dword v247, v247, s[4:7], 0 offen\n"
"buffer_load_dwordx4 v[100:103], v1, s[4:7], s97 offen offset:1024 sc1\n"
"buffer_store_dword v247, v247, s[4:7], 0 offen\n"
"buffer_load_dwordx4 v[92:95], v1, s[4:7], s97 offen offset:2048 sc1\n"
"buffer_store_dword v247, v247, s[4:7], 0 offen\n"
"buffer_load_dwordx4 v[104:107], v1, s[4:7], s97 offen offset:3072 sc1\n"
"s_nop 7\n"
"s_branch .Lmk_p50r\n"
".Lmk_m55s:\n"
";\n"
"s_mov_b32 s1, 0x10000\n"
".Lmk_m55l:\n"
"s_add_u32 s97, s62, 0x44000\n"
"buffer_load_dword v255, v10, s[4:7], s97 offen sc1\n"
"buffer_load_dword v254, v10, s[4:7], s97 offen offset:256 sc1\n"
"buffer_load_dword v253, v10, s[4:7], s97 offen offset:512 sc1\n"
"buffer_load_dword v252, v10, s[4:7], s97 offen offset:768 sc1\n"
"buffer_load_dword v251, v10, s[4:7], s97 offen offset:1024 sc1\n"
"buffer_load_dword v250, v10, s[4:7], s97 offen offset:1280 sc1\n"
"buffer_load_dword v249, v10, s[4:7], s97 offen offset:1536 sc1\n"
"buffer_load_dword v248, v10, s[4:7], s97 offen offset:1792 sc1\n"
"s_waitcnt vmcnt(0)\n"
"v_max_u32_e32 v247, v255, v254\n"
"v_max3_u32 v247, v247, v253, v252\n"
"v_max3_u32 v247, v247, v251, v250\n"
"v_max3_u32 v247, v247, v249, v248\n"
"v_cmp_eq_u32_e32 vcc, -1, v247\n"
"s_cbranch_vccz .Lmk_m55k\n"
"s_sub_u32 s1, s1, 1\n"
"s_cbranch_scc0 .Lmk_m55l\n"
".Lmk_m55k:\n"
"v_add_f32_e32 v255, v255, v251\n"
"v_add_f32_e32 v254, v254, v250\n"
"v_add_f32_e32 v253, v253, v249\n"
"v_add_f32_e32 v252, v252, v248\n"
"v_add_f32_e32 v255, v255, v253\n"
"v_add_f32_e32 v254, v254, v252\n"
"v_add_f32_e32 v255, v255, v254\n"
"s_nop 1\n"
"s_branch .Lmk_m55r\n"
".Lmk_p51s:\n"
";\n"
"s_mov_b32 s1, 0x10000\n"
".Lmk_p51l:\n"
"s_add_u32 s97, s53, 0x440000\n"
"buffer_load_dwordx4 v[148:151], v0, s[4:7], s97 offen sc1\n"
"buffer_load_dwordx4 v[120:123], v0, s[4:7], s97 offen offset:1024 sc1\n"
"buffer_load_dwordx4 v[136:139], v0, s[4:7], s97 offen offset:2048 sc1\n"
"buffer_load_dwordx4 v[140:143], v0, s[4:7], s97 offen offset:3072 sc1\n"
"buffer_load_dwordx4 v[156:159], v1, s[4:7], s97 offen sc1\n"
"buffer_load_dwordx4 v[188:191], v1, s[4:7], s97 offen offset:1024 sc1\n"
"buffer_load_dwordx4 v[196:199], v1, s[4:7], s97 offen offset:2048 sc1\n"
"buffer_load_dwordx4 v[164:167], v1, s[4:7], s97 offen offset:3072 sc1\n"
"s_add_u32 s97, s54, 0x440000\n"
"buffer_load_dwordx4 v[204:207], v0, s[4:7], s97 offen sc1\n"
"buffer_load_dwordx4 v[212:215], v0, s[4:7], s97 offen offset:1024 sc1\n"
"buffer_load_dwordx4 v[88:91], v0, s[4:7], s97 offen offset:2048 sc1\n"
"buffer_load_dwordx4 v[108:111], v0, s[4:7], s97 offen offset:3072 sc1\n"
"buffer_load_dwordx4 v[96:99], v1, s[4:7], s97 offen sc1\n"
"buffer_load_dwordx4 v[100:103], v1, s[4:7], s97 offen offset:1024 sc1\n"
"buffer_load_dwordx4 v[92:95], v1, s[4:7], s97 offen offset:2048 sc1\n"
"buffer_load_dwordx4 v[104:107], v1, s[4:7], s97 offen offset:3072 sc1\n"
"s_waitcnt vmcnt(0)\n"
"v_max_u32_e32 v255, v148, v149\n"
"v_max3_u32 v255, v255, v150, v151\n"
"v_max3_u32 v255, v255, v120, v121\n"
"v_max3_u32 v255, v255, v122, v123\n"
"v_max3_u32 v255, v255, v136, v137\n"
"v_max3_u32 v255, v255, v138, v139\n"
"v_max3_u32 v255, v255, v140, v141\n"
"v_max3_u32 v255, v255, v142, v143\n"
"v_max3_u32 v255, v255, v156, v157\n"
"v_max3_u32 v255, v255, v158, v159\n"
"v_max3_u32 v255, v255, v188, v189\n"
"v_max3_u32 v255, v255, v190, v191\n"
"v_max3_u32 v255, v255, v196, v197\n"
"v_max3_u32 v255, v255, v198, v199\n"
"v_max3_u32 v255, v255, v164, v165\n"
"v_max3_u32 v255, v255, v166, v167\n"
"v_cmp_eq_u32_e32 vcc, -1, v255\n"
"s_cbranch_vccz .Lmk_p51k\n"
"s_sub_u32 s1, s1, 1\n"
"s_cbranch_scc0 .Lmk_p51l\n"
".Lmk_p51k:\n"
"v_mfma_f32_16x16x32_bf16 v[116:119], a[0:3], v[148:151], 0\n"
"v_mfma_f32_16x16x32_bf16 v[124:127], a[4:7], v[148:151], 0\n"
"v_mfma_f32_16x16x32_bf16 v[128:131], a[8:11], v[148:151], 0\n"
"v_mfma_f32_16x16x32_bf16 v[132:135], a[12:15], v[148:151], 0\n"
"v_mfma_f32_16x16x32_bf16 v[144:147], a[16:19], v[148:151], 0\n"
"v_mfma_f32_16x16x32_bf16 v[152:155], a[20:23], v[148:151], 0\n"
"v_mfma_f32_16x16x32_bf16 v[116:119], a[24:27], v[120:123], v[116:119]\n"
"v_mfma_f32_16x16x32_bf16 v[124:127], a[28:31], v[120:123], v[124:127]\n"
"v_mfma_f32_16x16x32_bf16 v[128:131], a[32:35], v[120:123], v[128:131]\n"
"v_mfma_f32_16x16x32_bf16 v[132:135], a[36:39], v[120:123], v[132:135]\n"
"v_mfma_f32_16x16x32_bf16 v[144:147], a[40:43], v[120:123], v[144:147]\n"
"v_mfma_f32_16x16x32_bf16 v[152:155], a[44:47], v[120:123], v[152:155]\n"
"v_mfma_f32_16x16x32_bf16 v[116:119], a[48:51], v[136:139], v[116:119]\n"
"v_mfma_f32_16x16x32_bf16 v[124:127], a[52:55], v[136:139], v[124:127]\n"
"v_mfma_f32_16x16x32_bf16 v[128:131], a[56:59], v[136:139], v[128:131]\n"
"v_mfma_f32_16x16x32_bf16 v[132:135], a[60:63], v[136:139], v[132:135]\n"
"v_mfma_f32_16x16x32_bf16 v[144:147], a[64:67], v[136:139], v[144:147]\n"
"v_mfma_f32_16x16x32_bf16 v[152:155], a[68:71], v[136:139], v[152:155]\n"
"v_mfma_f32_16x16x32_bf16 v[116:119], a[72:75], v[140:143], v[116:119]\n"
"v_mfma_f32_16x16x32_bf16 v[124:127], a[76:79], v[140:143], v[124:127]\n"
"v_mfma_f32_16x16x32_bf16 v[128:131], a[80:83], v[140:143], v[128:131]\n"
"v_mfma_f32_16x16x32_bf16 v[132:135], a[84:87], v[140:143], v[132:135]\n"
"v_mfma_f32_16x16x32_bf16 v[144:147], a[88:91], v[140:143], v[144:147]\n"
"v_mfma_f32_16x16x32_bf16 v[152:155], a[92:95], v[140:143], v[152:155]\n"
"v_mfma_f32_16x16x32_bf16 v[116:119], a[96:99], v[156:159], v[116:119]\n"
"v_mfma_f32_16x16x32_bf16 v[124:127], a[100:103], v[156:159], v[124:127]\n"
"v_mfma_f32_16x16x32_bf16 v[128:131], a[104:107], v[156:159], v[128:131]\n"
"v_mfma_f32_16x16x32_bf16 v[132:135], a[108:111], v[156:159], v[132:135]\n"
"v_mfma_f32_16x16x32_bf16 v[144:147], a[112:115], v[156:159], v[144:147]\n"
"v_mfma_f32_16x16x32_bf16 v[152:155], a[116:119], v[156:159], v[152:155]\n"
"v_mfma_f32_16x16x32_bf16 v[116:119], a[120:123], v[188:191], v[116:119]\n"
"v_mfma_f32_16x16x32_bf16 v[124:127], a[124:127], v[188:191], v[124:127]\n"
"v_mfma_f32_16x16x32_bf16 v[128:131], v[24:27], v[188:191], v[128:131]\n"
"v_mfma_f32_16x16x32_bf16 v[132:135], v[28:31], v[188:191], v[132:135]\n"
"v_mfma_f32_16x16x32_bf16 v[144:147], v[32:35], v[188:191], v[144:147]\n"
"v_mfma_f32_16x16x32_bf16 v[152:155], v[36:39], v[188:191], v[152:155]\n"
"v_mfma_f32_16x16x32_bf16 v[116:119], v[40:43], v[196:199], v[116:119]\n"
"v_mfma_f32_16x16x32_bf16 v[124:127], v[44:47], v[196:199], v[124:127]\n"
"v_mfma_f32_16x16x32_bf16 v[128:131], v[48:51], v[196:199], v[128:131]\n"
"v_mfma_f32_16x16x32_bf16 v[132:135], v[52:55], v[196:199], v[132:135]\n"
"v_mfma_f32_16x16x32_bf16 v[144:147], v[56:59], v[196:199], v[144:147]\n"
"v_mfma_f32_16x16x32_bf16 v[152:155], v[60:63], v[196:199], v[152:155]\n"
"v_mfma_f32_16x16x32_bf16 v[116:119], v[64:67], v[164:167], v[116:119]\n"
"v_mfma_f32_16x16x32_bf16 v[124:127], v[68:71], v[164:167], v[124:127]\n"
"v_mfma_f32_16x16x32_bf16 v[128:131], v[72:75], v[164:167], v[128:131]\n"
"v_mfma_f32_16x16x32_bf16 v[132:135], v[76:79], v[164:167], v[132:135]\n"
"v_mfma_f32_16x16x32_bf16 v[144:147], v[80:83], v[164:167], v[144:147]\n"
"v_mfma_f32_16x16x32_bf16 v[152:155], v[84:87], v[164:167], v[152:155]\n"
"s_add_u32 s97, s55, 0x440000\n"
"v_mov_b32_e32 v255, 0x80000000\n"
"buffer_load_dwordx4 v[148:151], v0, s[4:7], s97 offen sc1\n"
"buffer_store_dword v255, v255, s[4:7], 0 offen\n"
"buffer_load_dwordx4 v[120:123], v0, s[4:7], s97 offen offset:1024 sc1\n"
"buffer_load_dwordx4 v[136:139], v0, s[4:7], s97 offen offset:2048 sc1\n"
"buffer_store_dword v255, v255, s[4:7], 0 offen\n"
"buffer_load_dwordx4 v[140:143], v0, s[4:7], s97 offen offset:3072 sc1\n"
"buffer_load_dwordx4 v[156:159], v1, s[4:7], s97 offen sc1\n"
"buffer_load_dwordx4 v[188:191], v1, s[4:7], s97 offen offset:1024 sc1\n"
"buffer_load_dwordx4 v[196:199], v1, s[4:7], s97 offen offset:2048 sc1\n"
"buffer_store_dword v255, v255, s[4:7], 0 offen\n"
"buffer_load_dwordx4 v[164:167], v1, s[4:7], s97 offen offset:3072 sc1\n"
"s_nop 7\n"
"s_branch .Lmk_p51r\n"
".Lmk_q53s:\n"
";\n"
"s_mov_b32 s1, 0x10000\n"
".Lmk_q53l:\n"
"s_add_u32 s95, s55, 0x440000\n"
"buffer_load_dwordx4 v[148:151], v0, s[4:7], s95 offen sc1\n"
"buffer_load_dwordx4 v[120:123], v0, s[4:7], s95 offen offset:1024 sc1\n"
"buffer_load_dwordx4 v[136:139], v0, s[4:7], s95 offen offset:2048 sc1\n"
"buffer_load_dwordx4 v[140:143], v0, s[4:7], s95 offen offset:3072 sc1\n"
"buffer_load_dwordx4 v[156:159], v1, s[4:7], s95 offen sc1\n"
"buffer_load_dwordx4 v[188:191], v1, s[4:7], s95 offen offset:1024 sc1\n"
"buffer_load_dwordx4 v[196:199], v1, s[4:7], s95 offen offset:2048 sc1\n"
"buffer_load_dwordx4 v[164:167], v1, s[4:7], s95 offen offset:3072 sc1\n"
"s_add_u32 s95, s52, 0x448000\n"
"buffer_load_dwordx4 v[216:219], v0, s[4:7], s95 offen sc1\n"
"buffer_load_dwordx4 v[124:127], v0, s[4:7], s95 offen offset:1024 sc1\n"
"buffer_load_dwordx4 v[144:147], v0, s[4:7], s95 offen offset:2048 sc1\n"
"buffer_load_dwordx4 v[152:155], v0, s[4:7], s95 offen offset:3072 sc1\n"
"buffer_load_dwordx4 v[108:111], v1, s[4:7], s95 offen sc1\n"
"buffer_load_dwordx4 v[88:91], v1, s[4:7], s95 offen offset:1024 sc1\n"
"s_waitcnt vmcnt(0)\n"
"v_max_u32_e32 v253, v148, v149\n"
"v_max3_u32 v253, v253, v150, v151\n"
"v_max3_u32 v253, v253, v120, v121\n"
"v_max3_u32 v253, v253, v122, v123\n"
"v_max3_u32 v253, v253, v136, v137\n"
"v_max3_u32 v253, v253, v138, v139\n"
"v_max3_u32 v253, v253, v140, v141\n"
"v_max3_u32 v253, v253, v142, v143\n"
"v_cmp_eq_u32_e32 vcc, -1, v253\n"
"s_cbranch_vccz .Lmk_q53k\n"
"s_sub_u32 s1, s1, 1\n"
"s_cbranch_scc0 .Lmk_q53l\n"
".Lmk_q53k:\n"
"s_branch .Lmk_q53r\n"
".Lmk_p52s:\n"
";\n"
"s_mov_b32 s1, 0x10000\n"
".Lmk_p52l:\n"
"s_add_u32 s95, s54, 0x440000\n"
"buffer_load_dwordx4 v[96:99], v0, s[4:7], s95 offen sc1\n"
"buffer_load_dwordx4 v[100:103], v0, s[4:7], s95 offen offset:1024 sc1\n"
"buffer_load_dwordx4 v[104:107], v0, s[4:7], s95 offen offset:2048 sc1\n"
"buffer_load_dwordx4 v[112:115], v0, s[4:7], s95 offen offset:3072 sc1\n"
"buffer_load_dwordx4 v[200:203], v1, s[4:7], s95 offen sc1\n"
"buffer_load_dwordx4 v[204:207], v1, s[4:7], s95 offen offset:1024 sc1\n"
"buffer_load_dwordx4 v[212:215], v1, s[4:7], s95 offen offset:2048 sc1\n"
"buffer_load_dwordx4 v[224:227], v1, s[4:7], s95 offen offset:3072 sc1\n"
"s_add_u32 s95, s55, 0x440000\n"
"buffer_load_dwordx4 v[148:151], v0, s[4:7], s95 offen sc1\n"
"buffer_load_dwordx4 v[120:123], v0, s[4:7], s95 offen offset:1024 sc1\n"
"buffer_load_dwordx4 v[136:139], v0, s[4:7], s95 offen offset:2048 sc1\n"
"buffer_load_dwordx4 v[140:143], v0, s[4:7], s95 offen offset:3072 sc1\n"
"buffer_load_dwordx4 v[156:159], v1, s[4:7], s95 offen sc1\n"
"buffer_load_dwordx4 v[188:191], v1, s[4:7], s95 offen offset:1024 sc1\n"
"buffer_load_dwordx4 v[196:199], v1, s[4:7], s95 offen offset:2048 sc1\n"
"buffer_load_dwordx4 v[164:167], v1, s[4:7], s95 offen offset:3072 sc1\n"
"s_add_u32 s95, s52, 0x448000\n"
"buffer_load_dwordx4 v[216:219], v0, s[4:7], s95 offen sc1\n"
"buffer_load_dwordx4 v[124:127], v0, s[4:7], s95 offen offset:1024 sc1\n"
"buffer_load_dwordx4 v[144:147], v0, s[4:7], s95 offen offset:2048 sc1\n"
"buffer_load_dwordx4 v[152:155], v0, s[4:7], s95 offen offset:3072 sc1\n"
"buffer_load_dwordx4 v[108:111], v1, s[4:7], s95 offen sc1\n"
"buffer_load_dwordx4 v[88:91], v1, s[4:7], s95 offen offset:1024 sc1\n"
"buffer_load_dwordx4 v[92:95], v1, s[4:7], s95 offen offset:2048 sc1\n"
"s_waitcnt vmcnt(0)\n"
"v_max_u32_e32 v253, v96, v97\n"
"v_max3_u32 v253, v253, v98, v99\n"
"v_max3_u32 v253, v253, v100, v101\n"
"v_max3_u32 v253, v253, v102, v103\n"
"v_max3_u32 v253, v253, v104, v105\n"
"v_max3_u32 v253, v253, v106, v107\n"
"v_max3_u32 v253, v253, v112, v113\n"
"v_max3_u32 v253, v253, v114, v115\n"
"v_max3_u32 v253, v253, v200, v201\n"
"v_max3_u32 v253, v253, v202, v203\n"
"v_max3_u32 v253, v253, v204, v205\n"
"v_max3_u32 v253, v253, v206, v207\n"
"v_max3_u32 v253, v253, v212, v213\n"
"v_max3_u32 v253, v253, v214, v215\n"
"v_max3_u32 v253, v253, v224, v225\n"
"v_max3_u32 v253, v253, v226, v227\n"
"v_cmp_eq_u32_e32 vcc, -1, v253\n"
"s_cbranch_vccz .Lmk_p52k\n"
"s_sub_u32 s1, s1, 1\n"
"s_cbranch_scc0 .Lmk_p52l\n"
".Lmk_p52k:\n"
"v_mfma_f32_16x16x32_bf16 v[176:179], a[0:3], v[96:99], 0\n"
"v_mfma_f32_16x16x32_bf16 v[180:183], a[4:7], v[96:99], 0\n"
"v_mfma_f32_16x16x32_bf16 v[184:187], a[8:11], v[96:99], 0\n"
"v_mfma_f32_16x16x32_bf16 v[208:211], a[12:15], v[96:99], 0\n"
"v_mfma_f32_16x16x32_bf16 v[116:119], a[16:19], v[96:99], 0\n"
"v_mfma_f32_16x16x32_bf16 v[220:223], a[20:23], v[96:99], 0\n"
"v_mfma_f32_16x16x32_bf16 v[176:179], a[24:27], v[100:103], v[176:179]\n"
"v_mfma_f32_16x16x32_bf16 v[180:183], a[28:31], v[100:103], v[180:183]\n"
"v_mfma_f32_16x16x32_bf16 v[184:187], a[32:35], v[100:103], v[184:187]\n"
"v_mfma_f32_16x16x32_bf16 v[208:211], a[36:39], v[100:103], v[208:211]\n"
"v_mfma_f32_16x16x32_bf16 v[116:119], a[40:43], v[100:103], v[116:119]\n"
"v_mfma_f32_16x16x32_bf16 v[220:223], a[44:47], v[100:103], v[220:223]\n"
"v_mfma_f32_16x16x32_bf16 v[176:179], a[48:51], v[104:107], v[176:179]\n"
"v_mfma_f32_16x16x32_bf16 v[180:183], a[52:55], v[104:107], v[180:183]\n"
"v_mfma_f32_16x16x32_bf16 v[184:187], a[56:59], v[104:107], v[184:187]\n"
"v_mfma_f32_16x16x32_bf16 v[208:211], a[60:63], v[104:107], v[208:211]\n"
"v_mfma_f32_16x16x32_bf16 v[116:119], a[64:67], v[104:107], v[116:119]\n"
"v_mfma_f32_16x16x32_bf16 v[220:223], a[68:71], v[104:107], v[220:223]\n"
"v_mfma_f32_16x16x32_bf16 v[176:179], a[72:75], v[112:115], v[176:179]\n"
"v_mfma_f32_16x16x32_bf16 v[180:183], a[76:79], v[112:115], v[180:183]\n"
"v_mfma_f32_16x16x32_bf16 v[184:187], a[80:83], v[112:115], v[184:187]\n"
"v_mfma_f32_16x16x32_bf16 v[208:211], a[84:87], v[112:115], v[208:211]\n"
"v_mfma_f32_16x16x32_bf16 v[116:119], a[88:91], v[112:115], v[116:119]\n"
"v_mfma_f32_16x16x32_bf16 v[220:223], a[92:95], v[112:115], v[220:223]\n"
"v_mfma_f32_16x16x32_bf16 v[176:179], a[96:99], v[200:203], v[176:179]\n"
"v_mfma_f32_16x16x32_bf16 v[180:183], a[100:103], v[200:203], v[180:183]\n"
"v_mfma_f32_16x16x32_bf16 v[184:187], a[104:107], v[200:203], v[184:187]\n"
"v_mfma_f32_16x16x32_bf16 v[208:211], a[108:111], v[200:203], v[208:211]\n"
"v_mfma_f32_16x16x32_bf16 v[116:119], a[112:115], v[200:203], v[116:119]\n"
"v_mfma_f32_16x16x32_bf16 v[220:223], a[116:119], v[200:203], v[220:223]\n"
"v_mfma_f32_16x16x32_bf16 v[176:179], a[120:123], v[204:207], v[176:179]\n"
"v_mfma_f32_16x16x32_bf16 v[180:183], a[124:127], v[204:207], v[180:183]\n"
"v_mfma_f32_16x16x32_bf16 v[184:187], v[24:27], v[204:207], v[184:187]\n"
"v_mfma_f32_16x16x32_bf16 v[208:211], v[28:31], v[204:207], v[208:211]\n"
"v_mfma_f32_16x16x32_bf16 v[116:119], v[32:35], v[204:207], v[116:119]\n"
"v_mfma_f32_16x16x32_bf16 v[220:223], v[36:39], v[204:207], v[220:223]\n"
"v_mfma_f32_16x16x32_bf16 v[176:179], v[40:43], v[212:215], v[176:179]\n"
"v_mfma_f32_16x16x32_bf16 v[180:183], v[44:47], v[212:215], v[180:183]\n"
"v_mfma_f32_16x16x32_bf16 v[184:187], v[48:51], v[212:215], v[184:187]\n"
"v_mfma_f32_16x16x32_bf16 v[208:211], v[52:55], v[212:215], v[208:211]\n"
"v_mfma_f32_16x16x32_bf16 v[116:119], v[56:59], v[212:215], v[116:119]\n"
"v_mfma_f32_16x16x32_bf16 v[220:223], v[60:63], v[212:215], v[220:223]\n"
"v_mfma_f32_16x16x32_bf16 v[176:179], v[64:67], v[224:227], v[176:179]\n"
"v_mfma_f32_16x16x32_bf16 v[180:183], v[68:71], v[224:227], v[180:183]\n"
"v_mfma_f32_16x16x32_bf16 v[184:187], v[72:75], v[224:227], v[184:187]\n"
"v_mfma_f32_16x16x32_bf16 v[208:211], v[76:79], v[224:227], v[208:211]\n"
"v_mfma_f32_16x16x32_bf16 v[116:119], v[80:83], v[224:227], v[116:119]\n"
"v_mfma_f32_16x16x32_bf16 v[220:223], v[84:87], v[224:227], v[220:223]\n"
"s_nop 7\n"
"s_branch .Lmk_p52r\n"
".Lmk_q54s:\n"
";\n"
"s_mov_b32 s1, 0x10000\n"
".Lmk_q54l:\n"
"s_add_u32 s96, s55, 0x440000\n"
"buffer_load_dwordx4 v[156:159], v1, s[4:7], s96 offen sc1\n"
"buffer_load_dwordx4 v[188:191], v1, s[4:7], s96 offen offset:1024 sc1\n"
"buffer_load_dwordx4 v[196:199], v1, s[4:7], s96 offen offset:2048 sc1\n"
"buffer_load_dwordx4 v[164:167], v1, s[4:7], s96 offen offset:3072 sc1\n"
"s_add_u32 s96, s52, 0x448000\n"
"buffer_load_dwordx4 v[216:219], v0, s[4:7], s96 offen sc1\n"
"buffer_load_dwordx4 v[124:127], v0, s[4:7], s96 offen offset:1024 sc1\n"
"buffer_load_dwordx4 v[144:147], v0, s[4:7], s96 offen offset:2048 sc1\n"
"buffer_load_dwordx4 v[152:155], v0, s[4:7], s96 offen offset:3072 sc1\n"
"buffer_load_dwordx4 v[108:111], v1, s[4:7], s96 offen sc1\n"
"buffer_load_dwordx4 v[88:91], v1, s[4:7], s96 offen offset:1024 sc1\n"
"buffer_load_dwordx4 v[92:95], v1, s[4:7], s96 offen offset:2048 sc1\n"
"buffer_load_dwordx4 v[96:99], v1, s[4:7], s96 offen offset:3072 sc1\n"
"s_add_u32 s96, s53, 0x448000\n"
"buffer_load_dwordx4 v[100:103], v0, s[4:7], s96 offen sc1\n"
"buffer_load_dwordx4 v[104:107], v0, s[4:7], s96 offen offset:1024 sc1\n"
"s_waitcnt vmcnt(0)\n"
"v_max_u32_e32 v253, v156, v157\n"
"v_max3_u32 v253, v253, v158, v159\n"
"v_max3_u32 v253, v253, v188, v189\n"
"v_max3_u32 v253, v253, v190, v191\n"
"v_max3_u32 v253, v253, v196, v197\n"
"v_max3_u32 v253, v253, v198, v199\n"
"v_max3_u32 v253, v253, v164, v165\n"
"v_max3_u32 v253, v253, v166, v167\n"
"v_cmp_eq_u32_e32 vcc, -1, v253\n"
"s_cbranch_vccz .Lmk_q54k\n"
"s_sub_u32 s1, s1, 1\n"
"s_cbranch_scc0 .Lmk_q54l\n"
".Lmk_q54k:\n"
"s_branch .Lmk_q54r\n"
".Lmk_p56s:\n"
";\n"
"s_mov_b32 s1, 0x10000\n"
".Lmk_p56l:\n"
"s_add_u32 s98, s52, 0x448000\n"
"buffer_load_dwordx4 v[220:223], v0, s[4:7], s98 offen sc1\n"
"buffer_load_dwordx4 v[136:139], v0, s[4:7], s98 offen offset:1024 sc1\n"
"buffer_load_dwordx4 v[124:127], v0, s[4:7], s98 offen offset:2048 sc1\n"
"buffer_load_dwordx4 v[148:151], v0, s[4:7], s98 offen offset:3072 sc1\n"
"buffer_load_dwordx4 v[144:147], v1, s[4:7], s98 offen sc1\n"
"buffer_load_dwordx4 v[108:111], v1, s[4:7], s98 offen offset:1024 sc1\n"
"buffer_load_dwordx4 v[128:131], v1, s[4:7], s98 offen offset:2048 sc1\n"
"buffer_load_dwordx4 v[88:91], v1, s[4:7], s98 offen offset:3072 sc1\n"
"s_add_u32 s98, s53, 0x448000\n"
"buffer_load_dwordx4 v[100:103], v0, s[4:7], s98 offen sc1\n"
"buffer_load_dwordx4 v[104:107], v0, s[4:7], s98 offen offset:1024 sc1\n"
"buffer_load_dwordx4 v[112:115], v0, s[4:7], s98 offen offset:2048 sc1\n"
"buffer_load_dwordx4 v[116:119], v0, s[4:7], s98 offen offset:3072 sc1\n"
"buffer_load_dwordx4 v[120:123], v1, s[4:7], s98 offen sc1\n"
"buffer_load_dwordx4 v[176:179], v1, s[4:7], s98 offen offset:1024 sc1\n"
"buffer_load_dwordx4 v[208:211], v1, s[4:7], s98 offen offset:2048 sc1\n"
"buffer_load_dwordx4 v[156:159], v1, s[4:7], s98 offen offset:3072 sc1\n"
"s_waitcnt vmcnt(0)\n"
"v_max_u32_e32 v255, v220, v221\n"
"v_max3_u32 v255, v255, v222, v223\n"
"v_max3_u32 v255, v255, v136, v137\n"
"v_max3_u32 v255, v255, v138, v139\n"
"v_max3_u32 v255, v255, v124, v125\n"
"v_max3_u32 v255, v255, v126, v127\n"
"v_max3_u32 v255, v255, v148, v149\n"
"v_max3_u32 v255, v255, v150, v151\n"
"v_max3_u32 v255, v255, v144, v145\n"
"v_max3_u32 v255, v255, v146, v147\n"
"v_max3_u32 v255, v255, v108, v109\n"
"v_max3_u32 v255, v255, v110, v111\n"
"v_max3_u32 v255, v255, v128, v129\n"
"v_max3_u32 v255, v255, v130, v131\n"
"v_max3_u32 v255, v255, v88, v89\n"
"v_max3_u32 v255, v255, v90, v91\n"
"v_cmp_eq_u32_e32 vcc, -1, v255\n"
"s_cbranch_vccz .Lmk_p56k\n"
"s_sub_u32 s1, s1, 1\n"
"s_cbranch_scc0 .Lmk_p56l\n"
".Lmk_p56k:\n"
"v_mfma_f32_16x16x32_bf16 v[164:167], a[0:3], v[220:223], 0\n"
"v_mfma_f32_16x16x32_bf16 v[184:187], a[4:7], v[220:223], 0\n"
"v_mfma_f32_16x16x32_bf16 v[200:203], a[8:11], v[220:223], 0\n"
"v_mfma_f32_16x16x32_bf16 v[204:207], a[12:15], v[220:223], 0\n"
"v_mfma_f32_16x16x32_bf16 v[212:215], a[16:19], v[220:223], 0\n"
"v_mfma_f32_16x16x32_bf16 v[224:227], a[20:23], v[220:223], 0\n"
"v_mfma_f32_16x16x32_bf16 v[164:167], a[24:27], v[136:139], v[164:167]\n"
"v_mfma_f32_16x16x32_bf16 v[184:187], a[28:31], v[136:139], v[184:187]\n"
"v_mfma_f32_16x16x32_bf16 v[200:203], a[32:35], v[136:139], v[200:203]\n"
"v_mfma_f32_16x16x32_bf16 v[204:207], a[36:39], v[136:139], v[204:207]\n"
"v_mfma_f32_16x16x32_bf16 v[212:215], a[40:43], v[136:139], v[212:215]\n"
"v_mfma_f32_16x16x32_bf16 v[224:227], a[44:47], v[136:139], v[224:227]\n"
"v_mfma_f32_16x16x32_bf16 v[164:167], a[48:51], v[124:127], v[164:167]\n"
"v_mfma_f32_16x16x32_bf16 v[184:187], a[52:55], v[124:127], v[184:187]\n"
"v_mfma_f32_16x16x32_bf16 v[200:203], a[56:59], v[124:127], v[200:203]\n"
"v_mfma_f32_16x16x32_bf16 v[204:207], a[60:63], v[124:127], v[204:207]\n"
"v_mfma_f32_16x16x32_bf16 v[212:215], a[64:67], v[124:127], v[212:215]\n"
"v_mfma_f32_16x16x32_bf16 v[224:227], a[68:71], v[124:127], v[224:227]\n"
"v_mfma_f32_16x16x32_bf16 v[164:167], a[72:75], v[148:151], v[164:167]\n"
"v_mfma_f32_16x16x32_bf16 v[184:187], a[76:79], v[148:151], v[184:187]\n"
"v_mfma_f32_16x16x32_bf16 v[200:203], a[80:83], v[148:151], v[200:203]\n"
"v_mfma_f32_16x16x32_bf16 v[204:207], a[84:87], v[148:151], v[204:207]\n"
"v_mfma_f32_16x16x32_bf16 v[212:215], a[88:91], v[148:151], v[212:215]\n"
"v_mfma_f32_16x16x32_bf16 v[224:227], a[92:95], v[148:151], v[224:227]\n"
"v_mfma_f32_16x16x32_bf16 v[164:167], a[96:99], v[144:147], v[164:167]\n"
"v_mfma_f32_16x16x32_bf16 v[184:187], a[100:103], v[144:147], v[184:187]\n"
"v_mfma_f32_16x16x32_bf16 v[200:203], a[104:107], v[144:147], v[200:203]\n"
"v_mfma_f32_16x16x32_bf16 v[204:207], a[108:111], v[144:147], v[204:207]\n"
"v_mfma_f32_16x16x32_bf16 v[212:215], a[112:115], v[144:147], v[212:215]\n"
"v_mfma_f32_16x16x32_bf16 v[224:227], a[116:119], v[144:147], v[224:227]\n"
"v_mfma_f32_16x16x32_bf16 v[164:167], a[120:123], v[108:111], v[164:167]\n"
"v_mfma_f32_16x16x32_bf16 v[184:187], a[124:127], v[108:111], v[184:187]\n"
"v_mfma_f32_16x16x32_bf16 v[200:203], v[24:27], v[108:111], v[200:203]\n"
"v_mfma_f32_16x16x32_bf16 v[204:207], v[28:31], v[108:111], v[204:207]\n"
"v_mfma_f32_16x16x32_bf16 v[212:215], v[32:35], v[108:111], v[212:215]\n"
"v_mfma_f32_16x16x32_bf16 v[224:227], v[36:39], v[108:111], v[224:227]\n"
"v_mfma_f32_16x16x32_bf16 v[164:167], v[40:43], v[128:131], v[164:167]\n"
"v_mfma_f32_16x16x32_bf16 v[184:187], v[44:47], v[128:131], v[184:187]\n"
"v_mfma_f32_16x16x32_bf16 v[200:203], v[48:51], v[128:131], v[200:203]\n"
"v_mfma_f32_16x16x32_bf16 v[204:207], v[52:55], v[128:131], v[204:207]\n"
"v_mfma_f32_16x16x32_bf16 v[212:215], v[56:59], v[128:131], v[212:215]\n"
"v_mfma_f32_16x16x32_bf16 v[224:227], v[60:63], v[128:131], v[224:227]\n"
"v_mfma_f32_16x16x32_bf16 v[164:167], v[64:67], v[88:91], v[164:167]\n"
"v_mfma_f32_16x16x32_bf16 v[184:187], v[68:71], v[88:91], v[184:187]\n"
"v_mfma_f32_16x16x32_bf16 v[200:203], v[72:75], v[88:91], v[200:203]\n"
"v_mfma_f32_16x16x32_bf16 v[204:207], v[76:79], v[88:91], v[204:207]\n"
"v_mfma_f32_16x16x32_bf16 v[212:215], v[80:83], v[88:91], v[212:215]\n"
"v_mfma_f32_16x16x32_bf16 v[224:227], v[84:87], v[88:91], v[224:227]\n"
"s_add_u32 s98, s54, 0x448000\n"
"v_mov_b32_e32 v255, 0x80000000\n"
"buffer_load_dwordx4 v[220:223], v0, s[4:7], s98 offen sc1\n"
"buffer_store_dword v255, v255, s[4:7], 0 offen\n"
"buffer_store_dword v255, v255, s[4:7], 0 offen\n"
"buffer_store_dword v255, v255, s[4:7], 0 offen\n"
"buffer_load_dwordx4 v[136:139], v0, s[4:7], s98 offen offset:1024 sc1\n"
"buffer_store_dword v255, v255, s[4:7], 0 offen\n"
"buffer_store_dword v255, v255, s[4:7], 0 offen\n"
"buffer_load_dwordx4 v[124:127], v0, s[4:7], s98 offen offset:2048 sc1\n"
"buffer_store_dword v255, v255, s[4:7], 0 offen\n"
"buffer_store_dword v255, v255, s[4:7], 0 offen\n"
"buffer_load_dwordx4 v[148:151], v0, s[4:7], s98 offen offset:3072 sc1\n"
"buffer_store_dword v255, v255, s[4:7], 0 offen\n"
"buffer_store_dword v255, v255, s[4:7], 0 offen\n"
"buffer_load_dwordx4 v[144:147], v1, s[4:7], s98 offen sc1\n"
"buffer_store_dword v255, v255, s[4:7], 0 offen\n"
"buffer_load_dwordx4 v[108:111], v1, s[4:7], s98 offen offset:1024 sc1\n"
"buffer_load_dwordx4 v[128:131], v1, s[4:7], s98 offen offset:2048 sc1\n"
"buffer_load_dwordx4 v[88:91], v1, s[4:7], s98 offen offset:3072 sc1\n"
"s_nop 7\n"
"s_branch .Lmk_p56r\n"
".Lmk_m61s:\n"
";\n"
"s_mov_b32 s1, 0x10000\n"
".Lmk_m61l:\n"
"s_add_u32 s95, s62, 0x44800\n"
"buffer_load_dword v253, v10, s[4:7], s95 offen sc1\n"
"buffer_load_dword v252, v10, s[4:7], s95 offen offset:256 sc1\n"
"buffer_load_dword v251, v10, s[4:7], s95 offen offset:512 sc1\n"
"buffer_load_dword v250, v10, s[4:7], s95 offen offset:768 sc1\n"
"buffer_load_dword v249, v10, s[4:7], s95 offen offset:1024 sc1\n"
"buffer_load_dword v248, v10, s[4:7], s95 offen offset:1280 sc1\n"
"buffer_load_dword v247, v10, s[4:7], s95 offen offset:1536 sc1\n"
"buffer_load_dword v246, v10, s[4:7], s95 offen offset:1792 sc1\n"
"s_waitcnt vmcnt(0)\n"
"v_max_u32_e32 v245, v253, v252\n"
"v_max3_u32 v245, v245, v251, v250\n"
"v_max3_u32 v245, v245, v249, v248\n"
"v_max3_u32 v245, v245, v247, v246\n"
"v_cmp_eq_u32_e32 vcc, -1, v245\n"
"s_cbranch_vccz .Lmk_m61k\n"
"s_sub_u32 s1, s1, 1\n"
"s_cbranch_scc0 .Lmk_m61l\n"
".Lmk_m61k:\n"
"v_add_f32_e32 v253, v253, v249\n"
"v_add_f32_e32 v252, v252, v248\n"
"v_add_f32_e32 v251, v251, v247\n"
"v_add_f32_e32 v250, v250, v246\n"
"v_add_f32_e32 v253, v253, v251\n"
"v_add_f32_e32 v252, v252, v250\n"
"v_add_f32_e32 v253, v253, v252\n"
"s_nop 1\n"
"s_branch .Lmk_m61r\n"
".Lmk_p57s:\n"
";\n"
"s_mov_b32 s1, 0x10000\n"
".Lmk_p57l:\n"
"s_add_u32 s98, s53, 0x448000\n"
"buffer_load_dwordx4 v[164:167], v0, s[4:7], s98 offen sc1\n"
"buffer_load_dwordx4 v[160:163], v0, s[4:7], s98 offen offset:1024 sc1\n"
"buffer_load_dwordx4 v[100:103], v0, s[4:7], s98 offen offset:2048 sc1\n"
"buffer_load_dwordx4 v[112:115], v0, s[4:7], s98 offen offset:3072 sc1\n"
"buffer_load_dwordx4 v[116:119], v1, s[4:7], s98 offen sc1\n"
"buffer_load_dwordx4 v[120:123], v1, s[4:7], s98 offen offset:1024 sc1\n"
"buffer_load_dwordx4 v[168:171], v1, s[4:7], s98 offen offset:2048 sc1\n"
"buffer_load_dwordx4 v[200:203], v1, s[4:7], s98 offen offset:3072 sc1\n"
"s_add_u32 s98, s54, 0x448000\n"
"buffer_load_dwordx4 v[220:223], v0, s[4:7], s98 offen sc1\n"
"buffer_load_dwordx4 v[136:139], v0, s[4:7], s98 offen offset:1024 sc1\n"
"buffer_load_dwordx4 v[124:127], v0, s[4:7], s98 offen offset:2048 sc1\n"
"buffer_load_dwordx4 v[148:151], v0, s[4:7], s98 offen offset:3072 sc1\n"
"buffer_load_dwordx4 v[144:147], v1, s[4:7], s98 offen sc1\n"
"buffer_load_dwordx4 v[108:111], v1, s[4:7], s98 offen offset:1024 sc1\n"
"buffer_load_dwordx4 v[128:131], v1, s[4:7], s98 offen offset:2048 sc1\n"
"buffer_load_dwordx4 v[88:91], v1, s[4:7], s98 offen offset:3072 sc1\n"
"s_waitcnt vmcnt(0)\n"
"v_max_u32_e32 v253, v164, v165\n"
"v_max3_u32 v253, v253, v166, v167\n"
"v_max3_u32 v253, v253, v160, v161\n"
"v_max3_u32 v253, v253, v162, v163\n"
"v_max3_u32 v253, v253, v100, v101\n"
"v_max3_u32 v253, v253, v102, v103\n"
"v_max3_u32 v253, v253, v112, v113\n"
"v_max3_u32 v253, v253, v114, v115\n"
"v_max3_u32 v253, v253, v116, v117\n"
"v_max3_u32 v253, v253, v118, v119\n"
"v_max3_u32 v253, v253, v120, v121\n"
"v_max3_u32 v253, v253, v122, v123\n"
"v_max3_u32 v253, v253, v168, v169\n"
"v_max3_u32 v253, v253, v170, v171\n"
"v_max3_u32 v253, v253, v200, v201\n"
"v_max3_u32 v253, v253, v202, v203\n"
"v_cmp_eq_u32_e32 vcc, -1, v253\n"
"s_cbranch_vccz .Lmk_p57k\n"
"s_sub_u32 s1, s1, 1\n"
"s_cbranch_scc0 .Lmk_p57l\n"
".Lmk_p57k:\n"
"v_mfma_f32_16x16x32_bf16 v[92:95], a[0:3], v[164:167], 0\n"
"v_mfma_f32_16x16x32_bf16 v[96:99], a[4:7], v[164:167], 0\n"
"v_mfma_f32_16x16x32_bf16 v[132:135], a[8:11], v[164:167], 0\n"
"v_mfma_f32_16x16x32_bf16 v[140:143], a[12:15], v[164:167], 0\n"
"v_mfma_f32_16x16x32_bf16 v[152:155], a[16:19], v[164:167], 0\n"
"v_mfma_f32_16x16x32_bf16 v[172:175], a[20:23], v[164:167], 0\n"
"v_mfma_f32_16x16x32_bf16 v[92:95], a[24:27], v[160:163], v[92:95]\n"
"v_mfma_f32_16x16x32_bf16 v[96:99], a[28:31], v[160:163], v[96:99]\n"
"v_mfma_f32_16x16x32_bf16 v[132:135], a[32:35], v[160:163], v[132:135]\n"
"v_mfma_f32_16x16x32_bf16 v[140:143], a[36:39], v[160:163], v[140:143]\n"
"v_mfma_f32_16x16x32_bf16 v[152:155], a[40:43], v[160:163], v[152:155]\n"
"v_mfma_f32_16x16x32_bf16 v[172:175], a[44:47], v[160:163], v[172:175]\n"
"v_mfma_f32_16x16x32_bf16 v[92:95], a[48:51], v[100:103], v[92:95]\n"
"v_mfma_f32_16x16x32_bf16 v[96:99], a[52:55], v[100:103], v[96:99]\n"
"v_mfma_f32_16x16x32_bf16 v[132:135], a[56:59], v[100:103], v[132:135]\n"
"v_mfma_f32_16x16x32_bf16 v[140:143], a[60:63], v[100:103], v[140:143]\n"
"v_mfma_f32_16x16x32_bf16 v[152:155], a[64:67], v[100:103], v[152:155]\n"
"v_mfma_f32_16x16x32_bf16 v[172:175], a[68:71], v[100:103], v[172:175]\n"
"v_mfma_f32_16x16x32_bf16 v[92:95], a[72:75], v[112:115], v[92:95]\n"
"v_mfma_f32_16x16x32_bf16 v[96:99], a[76:79], v[112:115], v[96:99]\n"
"v_mfma_f32_16x16x32_bf16 v[132:135], a[80:83], v[112:115], v[132:135]\n"
"v_mfma_f32_16x16x32_bf16 v[140:143], a[84:87], v[112:115], v[140:143]\n"
"v_mfma_f32_16x16x32_bf16 v[152:155], a[88:91], v[112:115], v[152:155]\n"
"v_mfma_f32_16x16x32_bf16 v[172:175], a[92:95], v[112:115], v[172:175]\n"
"v_mfma_f32_16x16x32_bf16 v[92:95], a[96:99], v[116:119], v[92:95]\n"
"v_mfma_f32_16x16x32_bf16 v[96:99], a[100:103], v[116:119], v[96:99]\n"
"v_mfma_f32_16x16x32_bf16 v[132:135], a[104:107], v[116:119], v[132:135]\n"
"v_mfma_f32_16x16x32_bf16 v[140:143], a[108:111], v[116:119], v[140:143]\n"
"v_mfma_f32_16x16x32_bf16 v[152:155], a[112:115], v[116:119], v[152:155]\n"
"v_mfma_f32_16x16x32_bf16 v[172:175], a[116:119], v[116:119], v[172:175]\n"
"v_mfma_f32_16x16x32_bf16 v[92:95], a[120:123], v[120:123], v[92:95]\n"
"v_mfma_f32_16x16x32_bf16 v[96:99], a[124:127], v[120:123], v[96:99]\n"
"v_mfma_f32_16x16x32_bf16 v[132:135], v[24:27], v[120:123], v[132:135]\n"
"v_mfma_f32_16x16x32_bf16 v[140:143], v[28:31], v[120:123], v[140:143]\n"
"v_mfma_f32_16x16x32_bf16 v[152:155], v[32:35], v[120:123], v[152:155]\n"
"v_mfma_f32_16x16x32_bf16 v[172:175], v[36:39], v[120:123], v[172:175]\n"
"v_mfma_f32_16x16x32_bf16 v[92:95], v[40:43], v[168:171], v[92:95]\n"
"v_mfma_f32_16x16x32_bf16 v[96:99], v[44:47], v[168:171], v[96:99]\n"
"v_mfma_f32_16x16x32_bf16 v[132:135], v[48:51], v[168:171], v[132:135]\n"
"v_mfma_f32_16x16x32_bf16 v[140:143], v[52:55], v[168:171], v[140:143]\n"
"v_mfma_f32_16x16x32_bf16 v[152:155], v[56:59], v[168:171], v[152:155]\n"
"v_mfma_f32_16x16x32_bf16 v[172:175], v[60:63], v[168:171], v[172:175]\n"
"v_mfma_f32_16x16x32_bf16 v[92:95], v[64:67], v[200:203], v[92:95]\n"
"v_mfma_f32_16x16x32_bf16 v[96:99], v[68:71], v[200:203], v[96:99]\n"
"v_mfma_f32_16x16x32_bf16 v[132:135], v[72:75], v[200:203], v[132:135]\n"
"v_mfma_f32_16x16x32_bf16 v[140:143], v[76:79], v[200:203], v[140:143]\n"
"v_mfma_f32_16x16x32_bf16 v[152:155], v[80:83], v[200:203], v[152:155]\n"
"v_mfma_f32_16x16x32_bf16 v[172:175], v[84:87], v[200:203], v[172:175]\n"
"s_add_u32 s98, s55, 0x448000\n"
"v_mov_b32_e32 v253, 0x80000000\n"
"buffer_load_dwordx4 v[164:167], v0, s[4:7], s98 offen sc1\n"
"buffer_store_dword v253, v253, s[4:7], 0 offen\n"
"buffer_load_dwordx4 v[160:163], v0, s[4:7], s98 offen offset:1024 sc1\n"
"buffer_store_dword v253, v253, s[4:7], 0 offen\n"
"buffer_load_dwordx4 v[100:103], v0, s[4:7], s98 offen offset:2048 sc1\n"
"buffer_load_dwordx4 v[112:115], v0, s[4:7], s98 offen offset:3072 sc1\n"
"buffer_store_dword v253, v253, s[4:7], 0 offen\n"
"buffer_load_dwordx4 v[116:119], v1, s[4:7], s98 offen sc1\n"
"buffer_store_dword v253, v253, s[4:7], 0 offen\n"
"buffer_load_dwordx4 v[120:123], v1, s[4:7], s98 offen offset:1024 sc1\n"
"buffer_load_dwordx4 v[168:171], v1, s[4:7], s98 offen offset:2048 sc1\n"
"buffer_load_dwordx4 v[200:203], v1, s[4:7], s98 offen offset:3072 sc1\n"
"s_nop 7\n"
"s_branch .Lmk_p57r\n"
".Lmk_f46s:\n"
";\n"
"s_mov_b32 s1, 0x10000\n"
".Lmk_f46l:\n"
"buffer_load_dwordx2 v[254:255], v12, s[4:7], s96 offen sc1\n"
"s_waitcnt vmcnt(0)\n"
"v_cmp_ne_u64_e32 vcc, v[254:255], v[18:19]\n"
"s_cbranch_vccz .Lmk_f46r\n"
"s_sub_u32 s1, s1, 1\n"
"s_cbranch_scc0 .Lmk_f46l\n"
"s_branch .Lmk_f46r\n"
".Lmk_q59s:\n"
";\n"
"s_mov_b32 s1, 0x10000\n"
".Lmk_q59l:\n"
"s_add_u32 s96, s55, 0x448000\n"
"buffer_load_dwordx4 v[164:167], v0, s[4:7], s96 offen sc1\n"
"buffer_load_dwordx4 v[160:163], v0, s[4:7], s96 offen offset:1024 sc1\n"
"buffer_load_dwordx4 v[100:103], v0, s[4:7], s96 offen offset:2048 sc1\n"
"buffer_load_dwordx4 v[112:115], v0, s[4:7], s96 offen offset:3072 sc1\n"
"buffer_load_dwordx4 v[116:119], v1, s[4:7], s96 offen sc1\n"
"buffer_load_dwordx4 v[120:123], v1, s[4:7], s96 offen offset:1024 sc1\n"
"buffer_load_dwordx4 v[168:171], v1, s[4:7], s96 offen offset:2048 sc1\n"
"buffer_load_dwordx4 v[200:203], v1, s[4:7], s96 offen offset:3072 sc1\n"
"s_waitcnt vmcnt(0)\n"
"v_max_u32_e32 v253, v164, v165\n"
"v_max3_u32 v253, v253, v166, v167\n"
"v_max3_u32 v253, v253, v160, v161\n"
"v_max3_u32 v253, v253, v162, v163\n"
"v_max3_u32 v253, v253, v100, v101\n"
"v_max3_u32 v253, v253, v102, v103\n"
"v_max3_u32 v253, v253, v112, v113\n"
"v_max3_u32 v253, v253, v114, v115\n"
"v_cmp_eq_u32_e32 vcc, -1, v253\n"
"s_cbranch_vccz .Lmk_q59k\n"
"s_sub_u32 s1, s1, 1\n"
"s_cbranch_scc0 .Lmk_q59l\n"
".Lmk_q59k:\n"
"s_branch .Lmk_q59r\n"
".Lmk_p58s:\n"
";\n"
"s_mov_b32 s1, 0x10000\n"
".Lmk_p58l:\n"
"s_add_u32 s96, s54, 0x448000\n"
"buffer_load_dwordx4 v[88:91], v0, s[4:7], s96 offen sc1\n"
"buffer_load_dwordx4 v[128:131], v0, s[4:7], s96 offen offset:1024 sc1\n"
"buffer_load_dwordx4 v[144:147], v0, s[4:7], s96 offen offset:2048 sc1\n"
"buffer_load_dwordx4 v[148:151], v0, s[4:7], s96 offen offset:3072 sc1\n"
"buffer_load_dwordx4 v[180:183], v1, s[4:7], s96 offen sc1\n"
"buffer_load_dwordx4 v[196:199], v1, s[4:7], s96 offen offset:1024 sc1\n"
"buffer_load_dwordx4 v[220:223], v1, s[4:7], s96 offen offset:2048 sc1\n"
"buffer_load_dwordx4 v[228:231], v1, s[4:7], s96 offen offset:3072 sc1\n"
"s_add_u32 s96, s55, 0x448000\n"
"buffer_load_dwordx4 v[164:167], v0, s[4:7], s96 offen sc1\n"
"buffer_load_dwordx4 v[160:163], v0, s[4:7], s96 offen offset:1024 sc1\n"
"buffer_load_dwordx4 v[100:103], v0, s[4:7], s96 offen offset:2048 sc1\n"
"buffer_load_dwordx4 v[112:115], v0, s[4:7], s96 offen offset:3072 sc1\n"
"buffer_load_dwordx4 v[116:119], v1, s[4:7], s96 offen sc1\n"
"buffer_load_dwordx4 v[120:123], v1, s[4:7], s96 offen offset:1024 sc1\n"
"buffer_load_dwordx4 v[168:171], v1, s[4:7], s96 offen offset:2048 sc1\n"
"buffer_load_dwordx4 v[200:203], v1, s[4:7], s96 offen offset:3072 sc1\n"
"s_waitcnt vmcnt(0)\n"
"v_max_u32_e32 v253, v88, v89\n"
"v_max3_u32 v253, v253, v90, v91\n"
"v_max3_u32 v253, v253, v128, v129\n"
"v_max3_u32 v253, v253, v130, v131\n"
"v_max3_u32 v253, v253, v144, v145\n"
"v_max3_u32 v253, v253, v146, v147\n"
"v_max3_u32 v253, v253, v148, v149\n"
"v_max3_u32 v253, v253, v150, v151\n"
"v_max3_u32 v253, v253, v180, v181\n"
"v_max3_u32 v253, v253, v182, v183\n"
"v_max3_u32 v253, v253, v196, v197\n"
"v_max3_u32 v253, v253, v198, v199\n"
"v_max3_u32 v253, v253, v220, v221\n"
"v_max3_u32 v253, v253, v222, v223\n"
"v_max3_u32 v253, v253, v228, v229\n"
"v_max3_u32 v253, v253, v230, v231\n"
"v_cmp_eq_u32_e32 vcc, -1, v253\n"
"s_cbranch_vccz .Lmk_p58k\n"
"s_sub_u32 s1, s1, 1\n"
"s_cbranch_scc0 .Lmk_p58l\n"
".Lmk_p58k:\n"
"v_mfma_f32_16x16x32_bf16 v[156:159], a[0:3], v[88:91], 0\n"
"v_mfma_f32_16x16x32_bf16 v[204:207], a[4:7], v[88:91], 0\n"
"v_mfma_f32_16x16x32_bf16 v[208:211], a[8:11], v[88:91], 0\n"
"v_mfma_f32_16x16x32_bf16 v[212:215], a[12:15], v[88:91], 0\n"
"v_mfma_f32_16x16x32_bf16 v[92:95], a[16:19], v[88:91], 0\n"
"v_mfma_f32_16x16x32_bf16 v[224:227], a[20:23], v[88:91], 0\n"
"v_mfma_f32_16x16x32_bf16 v[156:159], a[24:27], v[128:131], v[156:159]\n"
"v_mfma_f32_16x16x32_bf16 v[204:207], a[28:31], v[128:131], v[204:207]\n"
"v_mfma_f32_16x16x32_bf16 v[208:211], a[32:35], v[128:131], v[208:211]\n"
"v_mfma_f32_16x16x32_bf16 v[212:215], a[36:39], v[128:131], v[212:215]\n"
"v_mfma_f32_16x16x32_bf16 v[92:95], a[40:43], v[128:131], v[92:95]\n"
"v_mfma_f32_16x16x32_bf16 v[224:227], a[44:47], v[128:131], v[224:227]\n"
"v_mfma_f32_16x16x32_bf16 v[156:159], a[48:51], v[144:147], v[156:159]\n"
"v_mfma_f32_16x16x32_bf16 v[204:207], a[52:55], v[144:147], v[204:207]\n"
"v_mfma_f32_16x16x32_bf16 v[208:211], a[56:59], v[144:147], v[208:211]\n"
"v_mfma_f32_16x16x32_bf16 v[212:215], a[60:63], v[144:147], v[212:215]\n"
"v_mfma_f32_16x16x32_bf16 v[92:95], a[64:67], v[144:147], v[92:95]\n"
"v_mfma_f32_16x16x32_bf16 v[224:227], a[68:71], v[144:147], v[224:227]\n"
"v_mfma_f32_16x16x32_bf16 v[156:159], a[72:75], v[148:151], v[156:159]\n"
"v_mfma_f32_16x16x32_bf16 v[204:207], a[76:79], v[148:151], v[204:207]\n"
"v_mfma_f32_16x16x32_bf16 v[208:211], a[80:83], v[148:151], v[208:211]\n"
"v_mfma_f32_16x16x32_bf16 v[212:215], a[84:87], v[148:151], v[212:215]\n"
"v_mfma_f32_16x16x32_bf16 v[92:95], a[88:91], v[148:151], v[92:95]\n"
"v_mfma_f32_16x16x32_bf16 v[224:227], a[92:95], v[148:151], v[224:227]\n"
"v_mfma_f32_16x16x32_bf16 v[156:159], a[96:99], v[180:183], v[156:159]\n"
"v_mfma_f32_16x16x32_bf16 v[204:207], a[100:103], v[180:183], v[204:207]\n"
"v_mfma_f32_16x16x32_bf16 v[208:211], a[104:107], v[180:183], v[208:211]\n"
"v_mfma_f32_16x16x32_bf16 v[212:215], a[108:111], v[180:183], v[212:215]\n"
"v_mfma_f32_16x16x32_bf16 v[92:95], a[112:115], v[180:183], v[92:95]\n"
"v_mfma_f32_16x16x32_bf16 v[224:227], a[116:119], v[180:183], v[224:227]\n"
"v_mfma_f32_16x16x32_bf16 v[156:159], a[120:123], v[196:199], v[156:159]\n"
"v_mfma_f32_16x16x32_bf16 v[204:207], a[124:127], v[196:199], v[204:207]\n"
"v_mfma_f32_16x16x32_bf16 v[208:211], v[24:27], v[196:199], v[208:211]\n"
"v_mfma_f32_16x16x32_bf16 v[212:215], v[28:31], v[196:199], v[212:215]\n"
"v_mfma_f32_16x16x32_bf16 v[92:95], v[32:35], v[196:199], v[92:95]\n"
"v_mfma_f32_16x16x32_bf16 v[224:227], v[36:39], v[196:199], v[224:227]\n"
"v_mfma_f32_16x16x32_bf16 v[156:159], v[40:43], v[220:223], v[156:159]\n"
"v_mfma_f32_16x16x32_bf16 v[204:207], v[44:47], v[220:223], v[204:207]\n"
"v_mfma_f32_16x16x32_bf16 v[208:211], v[48:51], v[220:223], v[208:211]\n"
"v_mfma_f32_16x16x32_bf16 v[212:215], v[52:55], v[220:223], v[212:215]\n"
"v_mfma_f32_16x16x32_bf16 v[92:95], v[56:59], v[220:223], v[92:95]\n"
"v_mfma_f32_16x16x32_bf16 v[224:227], v[60:63], v[220:223], v[224:227]\n"
"v_mfma_f32_16x16x32_bf16 v[156:159], v[64:67], v[228:231], v[156:159]\n"
"v_mfma_f32_16x16x32_bf16 v[204:207], v[68:71], v[228:231], v[204:207]\n"
"v_mfma_f32_16x16x32_bf16 v[208:211], v[72:75], v[228:231], v[208:211]\n"
"v_mfma_f32_16x16x32_bf16 v[212:215], v[76:79], v[228:231], v[212:215]\n"
"v_mfma_f32_16x16x32_bf16 v[92:95], v[80:83], v[228:231], v[92:95]\n"
"v_mfma_f32_16x16x32_bf16 v[224:227], v[84:87], v[228:231], v[224:227]\n"
"s_nop 7\n"
"s_branch .Lmk_p58r\n"
".Lmk_q60s:\n"
";\n"
"s_mov_b32 s1, 0x10000\n"
".Lmk_q60l:\n"
"s_add_u32 s96, s55, 0x448000\n"
"buffer_load_dwordx4 v[116:119], v1, s[4:7], s96 offen sc1\n"
"buffer_load_dwordx4 v[120:123], v1, s[4:7], s96 offen offset:1024 sc1\n"
"buffer_load_dwordx4 v[168:171], v1, s[4:7], s96 offen offset:2048 sc1\n"
"buffer_load_dwordx4 v[200:203], v1, s[4:7], s96 offen offset:3072 sc1\n"
"s_waitcnt vmcnt(0)\n"
"v_max_u32_e32 v253, v116, v117\n"
"v_max3_u32 v253, v253, v118, v119\n"
"v_max3_u32 v253, v253, v120, v121\n"
"v_max3_u32 v253, v253, v122, v123\n"
"v_max3_u32 v253, v253, v168, v169\n"
"v_max3_u32 v253, v253, v170, v171\n"
"v_max3_u32 v253, v253, v200, v201\n"
"v_max3_u32 v253, v253, v202, v203\n"
"v_cmp_eq_u32_e32 vcc, -1, v253\n"
"s_cbranch_vccz .Lmk_q60k\n"
"s_sub_u32 s1, s1, 1\n"
"s_cbranch_scc0 .Lmk_q60l\n"
".Lmk_q60k:\n"
"s_branch .Lmk_q60r\n"
".Lmk_f47s:\n"
";\n"
"s_mov_b32 s1, 0x10000\n"
".Lmk_f47l:\n"
"buffer_load_dwordx2 v[254:255], off, s[4:7], s96 sc1\n"
"s_waitcnt vmcnt(0)\n"
"v_cmp_ne_u64_e32 vcc, v[254:255], v[18:19]\n"
"s_cbranch_vccz .Lmk_f47r\n"
"s_sub_u32 s1, s1, 1\n"
"s_cbranch_scc0 .Lmk_f47l\n"
"s_branch .Lmk_f47r\n"
".Lmk_f48s:\n"
";\n"
"s_mov_b32 s1, 0x10000\n"
".Lmk_f48l:\n"
"buffer_load_dwordx2 v[254:255], v12, s[4:7], s98 offen sc1\n"
"s_waitcnt vmcnt(0)\n"
"v_cmp_ne_u64_e32 vcc, v[254:255], v[18:19]\n"
"s_cbranch_vccz .Lmk_f48r\n"
"s_sub_u32 s1, s1, 1\n"
"s_cbranch_scc0 .Lmk_f48l\n"
"s_branch .Lmk_f48r\n"
".Lmk_p64s:\n"
";\n"
"s_mov_b32 s1, 0x10000\n"
".Lmk_p64l:\n"
"s_add_u32 s96, s56, 0x440000\n"
"buffer_load_dwordx4 v[80:83], v0, s[4:7], s96 offen sc1\n"
"buffer_load_dwordx4 v[24:27], v0, s[4:7], s96 offen offset:1024 sc1\n"
"buffer_load_dwordx4 v[32:35], v0, s[4:7], s96 offen offset:2048 sc1\n"
"buffer_load_dwordx4 v[36:39], v0, s[4:7], s96 offen offset:3072 sc1\n"
"buffer_load_dwordx4 v[44:47], v1, s[4:7], s96 offen sc1\n"
"buffer_load_dwordx4 v[56:59], v1, s[4:7], s96 offen offset:1024 sc1\n"
"buffer_load_dwordx4 v[60:63], v1, s[4:7], s96 offen offset:2048 sc1\n"
"buffer_load_dwordx4 v[88:91], v1, s[4:7], s96 offen offset:3072 sc1\n"
"s_add_u32 s96, s57, 0x440000\n"
"buffer_load_dwordx4 v[100:103], v0, s[4:7], s96 offen sc1\n"
"buffer_load_dwordx4 v[72:75], v0, s[4:7], s96 offen offset:1024 sc1\n"
"buffer_load_dwordx4 v[64:67], v0, s[4:7], s96 offen offset:2048 sc1\n"
"buffer_load_dwordx4 v[112:115], v0, s[4:7], s96 offen offset:3072 sc1\n"
"buffer_load_dwordx4 v[96:99], v1, s[4:7], s96 offen sc1\n"
"buffer_load_dwordx4 v[84:87], v1, s[4:7], s96 offen offset:1024 sc1\n"
"buffer_load_dwordx4 v[76:79], v1, s[4:7], s96 offen offset:2048 sc1\n"
"buffer_load_dwordx4 v[128:131], v1, s[4:7], s96 offen offset:3072 sc1\n"
"s_waitcnt vmcnt(0)\n"
"v_max_u32_e32 v255, v80, v81\n"
"v_max3_u32 v255, v255, v82, v83\n"
"v_max3_u32 v255, v255, v24, v25\n"
"v_max3_u32 v255, v255, v26, v27\n"
"v_max3_u32 v255, v255, v32, v33\n"
"v_max3_u32 v255, v255, v34, v35\n"
"v_max3_u32 v255, v255, v36, v37\n"
"v_max3_u32 v255, v255, v38, v39\n"
"v_max3_u32 v255, v255, v44, v45\n"
"v_max3_u32 v255, v255, v46, v47\n"
"v_max3_u32 v255, v255, v56, v57\n"
"v_max3_u32 v255, v255, v58, v59\n"
"v_max3_u32 v255, v255, v60, v61\n"
"v_max3_u32 v255, v255, v62, v63\n"
"v_max3_u32 v255, v255, v88, v89\n"
"v_max3_u32 v255, v255, v90, v91\n"
"v_cmp_eq_u32_e32 vcc, -1, v255\n"
"s_cbranch_vccz .Lmk_p64k\n"
"s_sub_u32 s1, s1, 1\n"
"s_cbranch_scc0 .Lmk_p64l\n"
".Lmk_p64k:\n"
"v_mfma_f32_16x16x32_bf16 v[68:71], a[128:131], v[80:83], 0\n"
"v_mfma_f32_16x16x32_bf16 v[104:107], a[132:135], v[80:83], 0\n"
"v_mfma_f32_16x16x32_bf16 v[108:111], a[136:139], v[80:83], 0\n"
"v_mfma_f32_16x16x32_bf16 v[116:119], a[140:143], v[80:83], 0\n"
"v_mfma_f32_16x16x32_bf16 v[68:71], a[144:147], v[24:27], v[68:71]\n"
"v_mfma_f32_16x16x32_bf16 v[104:107], a[148:151], v[24:27], v[104:107]\n"
"v_mfma_f32_16x16x32_bf16 v[108:111], a[152:155], v[24:27], v[108:111]\n"
"v_mfma_f32_16x16x32_bf16 v[116:119], a[156:159], v[24:27], v[116:119]\n"
"v_mfma_f32_16x16x32_bf16 v[68:71], a[160:163], v[32:35], v[68:71]\n"
"v_mfma_f32_16x16x32_bf16 v[104:107], a[164:167], v[32:35], v[104:107]\n"
"v_mfma_f32_16x16x32_bf16 v[108:111], a[168:171], v[32:35], v[108:111]\n"
"v_mfma_f32_16x16x32_bf16 v[116:119], a[172:175], v[32:35], v[116:119]\n"
"v_mfma_f32_16x16x32_bf16 v[68:71], a[176:179], v[36:39], v[68:71]\n"
"v_mfma_f32_16x16x32_bf16 v[104:107], a[180:183], v[36:39], v[104:107]\n"
"v_mfma_f32_16x16x32_bf16 v[108:111], a[184:187], v[36:39], v[108:111]\n"
"v_mfma_f32_16x16x32_bf16 v[116:119], a[188:191], v[36:39], v[116:119]\n"
"v_mfma_f32_16x16x32_bf16 v[68:71], a[192:195], v[44:47], v[68:71]\n"
"v_mfma_f32_16x16x32_bf16 v[104:107], a[196:199], v[44:47], v[104:107]\n"
"v_mfma_f32_16x16x32_bf16 v[108:111], a[200:203], v[44:47], v[108:111]\n"
"v_mfma_f32_16x16x32_bf16 v[116:119], a[204:207], v[44:47], v[116:119]\n"
"v_mfma_f32_16x16x32_bf16 v[68:71], a[208:211], v[56:59], v[68:71]\n"
"v_mfma_f32_16x16x32_bf16 v[104:107], a[212:215], v[56:59], v[104:107]\n"
"v_mfma_f32_16x16x32_bf16 v[108:111], a[216:219], v[56:59], v[108:111]\n"
"v_mfma_f32_16x16x32_bf16 v[116:119], a[220:223], v[56:59], v[116:119]\n"
"v_mfma_f32_16x16x32_bf16 v[68:71], a[224:227], v[60:63], v[68:71]\n"
"v_mfma_f32_16x16x32_bf16 v[104:107], a[228:231], v[60:63], v[104:107]\n"
"v_mfma_f32_16x16x32_bf16 v[108:111], a[232:235], v[60:63], v[108:111]\n"
"v_mfma_f32_16x16x32_bf16 v[116:119], a[236:239], v[60:63], v[116:119]\n"
"v_mfma_f32_16x16x32_bf16 v[68:71], a[240:243], v[88:91], v[68:71]\n"
"v_mfma_f32_16x16x32_bf16 v[104:107], a[244:247], v[88:91], v[104:107]\n"
"v_mfma_f32_16x16x32_bf16 v[108:111], a[248:251], v[88:91], v[108:111]\n"
"v_mfma_f32_16x16x32_bf16 v[116:119], a[252:255], v[88:91], v[116:119]\n"
"s_add_u32 s96, s58, 0x440000\n"
"v_mov_b32_e32 v255, 0x80000000\n"
"buffer_load_dwordx4 v[80:83], v0, s[4:7], s96 offen sc1\n"
"buffer_load_dwordx4 v[24:27], v0, s[4:7], s96 offen offset:1024 sc1\n"
"buffer_load_dwordx4 v[32:35], v0, s[4:7], s96 offen offset:2048 sc1\n"
"buffer_load_dwordx4 v[36:39], v0, s[4:7], s96 offen offset:3072 sc1\n"
"buffer_load_dwordx4 v[44:47], v1, s[4:7], s96 offen sc1\n"
"buffer_load_dwordx4 v[56:59], v1, s[4:7], s96 offen offset:1024 sc1\n"
"buffer_load_dwordx4 v[60:63], v1, s[4:7], s96 offen offset:2048 sc1\n"
"buffer_load_dwordx4 v[88:91], v1, s[4:7], s96 offen offset:3072 sc1\n"
"s_nop 7\n"
"s_branch .Lmk_p64r\n"
".Lmk_p65s:\n"
";\n"
"s_mov_b32 s1, 0x10000\n"
".Lmk_p65l:\n"
"s_add_u32 s96, s57, 0x440000\n"
"buffer_load_dwordx4 v[128:131], v0, s[4:7], s96 offen sc1\n"
"buffer_load_dwordx4 v[144:147], v0, s[4:7], s96 offen offset:1024 sc1\n"
"buffer_load_dwordx4 v[148:151], v0, s[4:7], s96 offen offset:2048 sc1\n"
"buffer_load_dwordx4 v[152:155], v0, s[4:7], s96 offen offset:3072 sc1\n"
"buffer_load_dwordx4 v[156:159], v1, s[4:7], s96 offen sc1\n"
"buffer_load_dwordx4 v[160:163], v1, s[4:7], s96 offen offset:1024 sc1\n"
"buffer_load_dwordx4 v[164:167], v1, s[4:7], s96 offen offset:2048 sc1\n"
"buffer_load_dwordx4 v[168:171], v1, s[4:7], s96 offen offset:3072 sc1\n"
"s_add_u32 s96, s58, 0x440000\n"
"buffer_load_dwordx4 v[80:83], v0, s[4:7], s96 offen sc1\n"
"buffer_load_dwordx4 v[24:27], v0, s[4:7], s96 offen offset:1024 sc1\n"
"buffer_load_dwordx4 v[32:35], v0, s[4:7], s96 offen offset:2048 sc1\n"
"buffer_load_dwordx4 v[36:39], v0, s[4:7], s96 offen offset:3072 sc1\n"
"buffer_load_dwordx4 v[44:47], v1, s[4:7], s96 offen sc1\n"
"buffer_load_dwordx4 v[56:59], v1, s[4:7], s96 offen offset:1024 sc1\n"
"buffer_load_dwordx4 v[60:63], v1, s[4:7], s96 offen offset:2048 sc1\n"
"buffer_load_dwordx4 v[88:91], v1, s[4:7], s96 offen offset:3072 sc1\n"
"s_add_u32 s96, s59, 0x440000\n"
"buffer_load_dwordx4 v[132:135], v0, s[4:7], s96 offen sc1\n"
"buffer_load_dwordx4 v[68:71], v0, s[4:7], s96 offen offset:1024 sc1\n"
"buffer_load_dwordx4 v[64:67], v0, s[4:7], s96 offen offset:2048 sc1\n"
"buffer_load_dwordx4 v[104:107], v0, s[4:7], s96 offen offset:3072 sc1\n"
"buffer_load_dwordx4 v[96:99], v1, s[4:7], s96 offen sc1\n"
"buffer_load_dwordx4 v[84:87], v1, s[4:7], s96 offen offset:1024 sc1\n"
"buffer_load_dwordx4 v[72:75], v1, s[4:7], s96 offen offset:2048 sc1\n"
"s_waitcnt vmcnt(0)\n"
"v_max_u32_e32 v255, v128, v129\n"
"v_max3_u32 v255, v255, v130, v131\n"
"v_max3_u32 v255, v255, v144, v145\n"
"v_max3_u32 v255, v255, v146, v147\n"
"v_max3_u32 v255, v255, v148, v149\n"
"v_max3_u32 v255, v255, v150, v151\n"
"v_max3_u32 v255, v255, v152, v153\n"
"v_max3_u32 v255, v255, v154, v155\n"
"v_max3_u32 v255, v255, v156, v157\n"
"v_max3_u32 v255, v255, v158, v159\n"
"v_max3_u32 v255, v255, v160, v161\n"
"v_max3_u32 v255, v255, v162, v163\n"
"v_max3_u32 v255, v255, v164, v165\n"
"v_max3_u32 v255, v255, v166, v167\n"
"v_max3_u32 v255, v255, v168, v169\n"
"v_max3_u32 v255, v255, v170, v171\n"
"v_cmp_eq_u32_e32 vcc, -1, v255\n"
"s_cbranch_vccz .Lmk_p65k\n"
"s_sub_u32 s1, s1, 1\n"
"s_cbranch_scc0 .Lmk_p65l\n"
".Lmk_p65k:\n"
"v_mfma_f32_16x16x32_bf16 v[92:95], a[128:131], v[128:131], 0\n"
"v_mfma_f32_16x16x32_bf16 v[120:123], a[132:135], v[128:131], 0\n"
"v_mfma_f32_16x16x32_bf16 v[124:127], a[136:139], v[128:131], 0\n"
"v_mfma_f32_16x16x32_bf16 v[136:139], a[140:143], v[128:131], 0\n"
"v_mfma_f32_16x16x32_bf16 v[92:95], a[144:147], v[144:147], v[92:95]\n"
"v_mfma_f32_16x16x32_bf16 v[120:123], a[148:151], v[144:147], v[120:123]\n"
"v_mfma_f32_16x16x32_bf16 v[124:127], a[152:155], v[144:147], v[124:127]\n"
"v_mfma_f32_16x16x32_bf16 v[136:139], a[156:159], v[144:147], v[136:139]\n"
"v_mfma_f32_16x16x32_bf16 v[92:95], a[160:163], v[148:151], v[92:95]\n"
"v_mfma_f32_16x16x32_bf16 v[120:123], a[164:167], v[148:151], v[120:123]\n"
"v_mfma_f32_16x16x32_bf16 v[124:127], a[168:171], v[148:151], v[124:127]\n"
"v_mfma_f32_16x16x32_bf16 v[136:139], a[172:175], v[148:151], v[136:139]\n"
"v_mfma_f32_16x16x32_bf16 v[92:95], a[176:179], v[152:155], v[92:95]\n"
"v_mfma_f32_16x16x32_bf16 v[120:123], a[180:183], v[152:155], v[120:123]\n"
"v_mfma_f32_16x16x32_bf16 v[124:127], a[184:187], v[152:155], v[124:127]\n"
"v_mfma_f32_16x16x32_bf16 v[136:139], a[188:191], v[152:155], v[136:139]\n"
"v_mfma_f32_16x16x32_bf16 v[92:95], a[192:195], v[156:159], v[92:95]\n"
"v_mfma_f32_16x16x32_bf16 v[120:123], a[196:199], v[156:159], v[120:123]\n"
"v_mfma_f32_16x16x32_bf16 v[124:127], a[200:203], v[156:159], v[124:127]\n"
"v_mfma_f32_16x16x32_bf16 v[136:139], a[204:207], v[156:159], v[136:139]\n"
"v_mfma_f32_16x16x32_bf16 v[92:95], a[208:211], v[160:163], v[92:95]\n"
"v_mfma_f32_16x16x32_bf16 v[120:123], a[212:215], v[160:163], v[120:123]\n"
"v_mfma_f32_16x16x32_bf16 v[124:127], a[216:219], v[160:163], v[124:127]\n"
"v_mfma_f32_16x16x32_bf16 v[136:139], a[220:223], v[160:163], v[136:139]\n"
"v_mfma_f32_16x16x32_bf16 v[92:95], a[224:227], v[164:167], v[92:95]\n"
"v_mfma_f32_16x16x32_bf16 v[120:123], a[228:231], v[164:167], v[120:123]\n"
"v_mfma_f32_16x16x32_bf16 v[124:127], a[232:235], v[164:167], v[124:127]\n"
"v_mfma_f32_16x16x32_bf16 v[136:139], a[236:239], v[164:167], v[136:139]\n"
"v_mfma_f32_16x16x32_bf16 v[92:95], a[240:243], v[168:171], v[92:95]\n"
"v_mfma_f32_16x16x32_bf16 v[120:123], a[244:247], v[168:171], v[120:123]\n"
"v_mfma_f32_16x16x32_bf16 v[124:127], a[248:251], v[168:171], v[124:127]\n"
"v_mfma_f32_16x16x32_bf16 v[136:139], a[252:255], v[168:171], v[136:139]\n"
"s_nop 7\n"
"s_branch .Lmk_p65r\n"
".Lmk_q67s:\n"
";\n"
"s_mov_b32 s1, 0x10000\n"
".Lmk_q67l:\n"
"s_add_u32 s96, s59, 0x440000\n"
"buffer_load_dwordx4 v[132:135], v0, s[4:7], s96 offen sc1\n"
"buffer_load_dwordx4 v[68:71], v0, s[4:7], s96 offen offset:1024 sc1\n"
"buffer_load_dwordx4 v[64:67], v0, s[4:7], s96 offen offset:2048 sc1\n"
"buffer_load_dwordx4 v[104:107], v0, s[4:7], s96 offen offset:3072 sc1\n"
"buffer_load_dwordx4 v[96:99], v1, s[4:7], s96 offen sc1\n"
"buffer_load_dwordx4 v[84:87], v1, s[4:7], s96 offen offset:1024 sc1\n"
"buffer_load_dwordx4 v[72:75], v1, s[4:7], s96 offen offset:2048 sc1\n"
"buffer_load_dwordx4 v[144:147], v1, s[4:7], s96 offen offset:3072 sc1\n"
"s_add_u32 s96, s56, 0x400000\n"
"buffer_load_dwordx4 v[24:27], v0, s[4:7], s96 offen sc1\n"
"buffer_load_dwordx4 v[32:35], v0, s[4:7], s96 offen offset:1024 sc1\n"
"buffer_load_dwordx4 v[36:39], v0, s[4:7], s96 offen offset:2048 sc1\n"
"buffer_load_dwordx4 v[44:47], v0, s[4:7], s96 offen offset:3072 sc1\n"
"s_waitcnt vmcnt(0)\n"
"v_max_u32_e32 v255, v132, v133\n"
"v_max3_u32 v255, v255, v134, v135\n"
"v_max3_u32 v255, v255, v68, v69\n"
"v_max3_u32 v255, v255, v70, v71\n"
"v_max3_u32 v255, v255, v64, v65\n"
"v_max3_u32 v255, v255, v66, v67\n"
"v_max3_u32 v255, v255, v104, v105\n"
"v_max3_u32 v255, v255, v106, v107\n"
"v_cmp_eq_u32_e32 vcc, -1, v255\n"
"s_cbranch_vccz .Lmk_q67k\n"
"s_sub_u32 s1, s1, 1\n"
"s_cbranch_scc0 .Lmk_q67l\n"
".Lmk_q67k:\n"
"s_branch .Lmk_q67r\n"
".Lmk_p66s:\n"
";\n"
"s_mov_b32 s1, 0x10000\n"
".Lmk_p66l:\n"
"s_add_u32 s96, s58, 0x440000\n"
"buffer_load_dwordx4 v[60:63], v0, s[4:7], s96 offen sc1\n"
"buffer_load_dwordx4 v[80:83], v0, s[4:7], s96 offen offset:1024 sc1\n"
"buffer_load_dwordx4 v[88:91], v0, s[4:7], s96 offen offset:2048 sc1\n"
"buffer_load_dwordx4 v[92:95], v0, s[4:7], s96 offen offset:3072 sc1\n"
"buffer_load_dwordx4 v[120:123], v1, s[4:7], s96 offen sc1\n"
"buffer_load_dwordx4 v[124:127], v1, s[4:7], s96 offen offset:1024 sc1\n"
"buffer_load_dwordx4 v[136:139], v1, s[4:7], s96 offen offset:2048 sc1\n"
"buffer_load_dwordx4 v[152:155], v1, s[4:7], s96 offen offset:3072 sc1\n"
"s_add_u32 s96, s59, 0x440000\n"
"buffer_load_dwordx4 v[132:135], v0, s[4:7], s96 offen sc1\n"
"buffer_load_dwordx4 v[68:71], v0, s[4:7], s96 offen offset:1024 sc1\n"
"buffer_load_dwordx4 v[64:67], v0, s[4:7], s96 offen offset:2048 sc1\n"
"buffer_load_dwordx4 v[104:107], v0, s[4:7], s96 offen offset:3072 sc1\n"
"buffer_load_dwordx4 v[96:99], v1, s[4:7], s96 offen sc1\n"
"buffer_load_dwordx4 v[84:87], v1, s[4:7], s96 offen offset:1024 sc1\n"
"buffer_load_dwordx4 v[72:75], v1, s[4:7], s96 offen offset:2048 sc1\n"
"buffer_load_dwordx4 v[144:147], v1, s[4:7], s96 offen offset:3072 sc1\n"
"s_add_u32 s96, s56, 0x400000\n"
"buffer_load_dwordx4 v[24:27], v0, s[4:7], s96 offen sc1\n"
"buffer_load_dwordx4 v[32:35], v0, s[4:7], s96 offen offset:1024 sc1\n"
"buffer_load_dwordx4 v[36:39], v0, s[4:7], s96 offen offset:2048 sc1\n"
"buffer_load_dwordx4 v[44:47], v0, s[4:7], s96 offen offset:3072 sc1\n"
"buffer_load_dwordx4 v[56:59], v1, s[4:7], s96 offen sc1\n"
"s_waitcnt vmcnt(0)\n"
"v_max_u32_e32 v255, v60, v61\n"
"v_max3_u32 v255, v255, v62, v63\n"
"v_max3_u32 v255, v255, v80, v81\n"
"v_max3_u32 v255, v255, v82, v83\n"
"v_max3_u32 v255, v255, v88, v89\n"
"v_max3_u32 v255, v255, v90, v91\n"
"v_max3_u32 v255, v255, v92, v93\n"
"v_max3_u32 v255, v255, v94, v95\n"
"v_max3_u32 v255, v255, v120, v121\n"
"v_max3_u32 v255, v255, v122, v123\n"
"v_max3_u32 v255, v255, v124, v125\n"
"v_max3_u32 v255, v255, v126, v127\n"
"v_max3_u32 v255, v255, v136, v137\n"
"v_max3_u32 v255, v255, v138, v139\n"
"v_max3_u32 v255, v255, v152, v153\n"
"v_max3_u32 v255, v255, v154, v155\n"
"v_cmp_eq_u32_e32 vcc, -1, v255\n"
"s_cbranch_vccz .Lmk_p66k\n"
"s_sub_u32 s1, s1, 1\n"
"s_cbranch_scc0 .Lmk_p66l\n"
".Lmk_p66k:\n"
"v_mfma_f32_16x16x32_bf16 v[76:79], a[128:131], v[60:63], 0\n"
"v_mfma_f32_16x16x32_bf16 v[100:103], a[132:135], v[60:63], 0\n"
"v_mfma_f32_16x16x32_bf16 v[128:131], a[136:139], v[60:63], 0\n"
"v_mfma_f32_16x16x32_bf16 v[148:151], a[140:143], v[60:63], 0\n"
"v_mfma_f32_16x16x32_bf16 v[76:79], a[144:147], v[80:83], v[76:79]\n"
"v_mfma_f32_16x16x32_bf16 v[100:103], a[148:151], v[80:83], v[100:103]\n"
"v_mfma_f32_16x16x32_bf16 v[128:131], a[152:155], v[80:83], v[128:131]\n"
"v_mfma_f32_16x16x32_bf16 v[148:151], a[156:159], v[80:83], v[148:151]\n"
"v_mfma_f32_16x16x32_bf16 v[76:79], a[160:163], v[88:91], v[76:79]\n"
"v_mfma_f32_16x16x32_bf16 v[100:103], a[164:167], v[88:91], v[100:103]\n"
"v_mfma_f32_16x16x32_bf16 v[128:131], a[168:171], v[88:91], v[128:131]\n"
"v_mfma_f32_16x16x32_bf16 v[148:151], a[172:175], v[88:91], v[148:151]\n"
"v_mfma_f32_16x16x32_bf16 v[76:79], a[176:179], v[92:95], v[76:79]\n"
"v_mfma_f32_16x16x32_bf16 v[100:103], a[180:183], v[92:95], v[100:103]\n"
"v_mfma_f32_16x16x32_bf16 v[128:131], a[184:187], v[92:95], v[128:131]\n"
"v_mfma_f32_16x16x32_bf16 v[148:151], a[188:191], v[92:95], v[148:151]\n"
"v_mfma_f32_16x16x32_bf16 v[76:79], a[192:195], v[120:123], v[76:79]\n"
"v_mfma_f32_16x16x32_bf16 v[100:103], a[196:199], v[120:123], v[100:103]\n"
"v_mfma_f32_16x16x32_bf16 v[128:131], a[200:203], v[120:123], v[128:131]\n"
"v_mfma_f32_16x16x32_bf16 v[148:151], a[204:207], v[120:123], v[148:151]\n"
"v_mfma_f32_16x16x32_bf16 v[76:79], a[208:211], v[124:127], v[76:79]\n"
"v_mfma_f32_16x16x32_bf16 v[100:103], a[212:215], v[124:127], v[100:103]\n"
"v_mfma_f32_16x16x32_bf16 v[128:131], a[216:219], v[124:127], v[128:131]\n"
"v_mfma_f32_16x16x32_bf16 v[148:151], a[220:223], v[124:127], v[148:151]\n"
"v_mfma_f32_16x16x32_bf16 v[76:79], a[224:227], v[136:139], v[76:79]\n"
"v_mfma_f32_16x16x32_bf16 v[100:103], a[228:231], v[136:139], v[100:103]\n"
"v_mfma_f32_16x16x32_bf16 v[128:131], a[232:235], v[136:139], v[128:131]\n"
"v_mfma_f32_16x16x32_bf16 v[148:151], a[236:239], v[136:139], v[148:151]\n"
"v_mfma_f32_16x16x32_bf16 v[76:79], a[240:243], v[152:155], v[76:79]\n"
"v_mfma_f32_16x16x32_bf16 v[100:103], a[244:247], v[152:155], v[100:103]\n"
"v_mfma_f32_16x16x32_bf16 v[128:131], a[248:251], v[152:155], v[128:131]\n"
"v_mfma_f32_16x16x32_bf16 v[148:151], a[252:255], v[152:155], v[148:151]\n"
"s_nop 7\n"
"s_branch .Lmk_p66r\n"
".Lmk_q68s:\n"
";\n"
"s_mov_b32 s1, 0x10000\n"
".Lmk_q68l:\n"
"s_add_u32 s96, s59, 0x440000\n"
"buffer_load_dwordx4 v[96:99], v1, s[4:7], s96 offen sc1\n"
"buffer_load_dwordx4 v[84:87], v1, s[4:7], s96 offen offset:1024 sc1\n"
"buffer_load_dwordx4 v[72:75], v1, s[4:7], s96 offen offset:2048 sc1\n"
"buffer_load_dwordx4 v[144:147], v1, s[4:7], s96 offen offset:3072 sc1\n"
"s_add_u32 s96, s56, 0x400000\n"
"buffer_load_dwordx4 v[24:27], v0, s[4:7], s96 offen sc1\n"
"buffer_load_dwordx4 v[32:35], v0, s[4:7], s96 offen offset:1024 sc1\n"
"buffer_load_dwordx4 v[36:39], v0, s[4:7], s96 offen offset:2048 sc1\n"
"buffer_load_dwordx4 v[44:47], v0, s[4:7], s96 offen offset:3072 sc1\n"
"buffer_load_dwordx4 v[56:59], v1, s[4:7], s96 offen sc1\n"
"buffer_load_dwordx4 v[60:63], v1, s[4:7], s96 offen offset:1024 sc1\n"
"buffer_load_dwordx4 v[76:79], v1, s[4:7], s96 offen offset:2048 sc1\n"
"s_waitcnt vmcnt(0)\n"
"v_max_u32_e32 v255, v96, v97\n"
"v_max3_u32 v255, v255, v98, v99\n"
"v_max3_u32 v255, v255, v84, v85\n"
"v_max3_u32 v255, v255, v86, v87\n"
"v_max3_u32 v255, v255, v72, v73\n"
"v_max3_u32 v255, v255, v74, v75\n"
"v_max3_u32 v255, v255, v144, v145\n"
"v_max3_u32 v255, v255, v146, v147\n"
"v_cmp_eq_u32_e32 vcc, -1, v255\n"
"s_cbranch_vccz .Lmk_q68k\n"
"s_sub_u32 s1, s1, 1\n"
"s_cbranch_scc0 .Lmk_q68l\n"
".Lmk_q68k:\n"
"s_branch .Lmk_q68r\n"
".Lmk_p69s:\n"
";\n"
"s_mov_b32 s1, 0x10000\n"
".Lmk_p69l:\n"
"s_add_u32 s96, s56, 0x400000\n"
"buffer_load_dwordx4 v[136:139], v0, s[4:7], s96 offen sc1\n"
"buffer_load_dwordx4 v[24:27], v0, s[4:7], s96 offen offset:1024 sc1\n"
"buffer_load_dwordx4 v[112:115], v0, s[4:7], s96 offen offset:2048 sc1\n"
"buffer_load_dwordx4 v[80:83], v0, s[4:7], s96 offen offset:3072 sc1\n"
"buffer_load_dwordx4 v[36:39], v1, s[4:7], s96 offen sc1\n"
"buffer_load_dwordx4 v[44:47], v1, s[4:7], s96 offen offset:1024 sc1\n"
"buffer_load_dwordx4 v[84:87], v1, s[4:7], s96 offen offset:2048 sc1\n"
"buffer_load_dwordx4 v[56:59], v1, s[4:7], s96 offen offset:3072 sc1\n"
"s_add_u32 s96, s57, 0x400000\n"
"buffer_load_dwordx4 v[68:71], v0, s[4:7], s96 offen sc1\n"
"buffer_load_dwordx4 v[92:95], v0, s[4:7], s96 offen offset:1024 sc1\n"
"buffer_load_dwordx4 v[120:123], v0, s[4:7], s96 offen offset:2048 sc1\n"
"buffer_load_dwordx4 v[72:75], v0, s[4:7], s96 offen offset:3072 sc1\n"
"buffer_load_dwordx4 v[88:91], v1, s[4:7], s96 offen sc1\n"
"buffer_load_dwordx4 v[100:103], v1, s[4:7], s96 offen offset:1024 sc1\n"
"buffer_load_dwordx4 v[104:107], v1, s[4:7], s96 offen offset:2048 sc1\n"
"buffer_load_dwordx4 v[128:131], v1, s[4:7], s96 offen offset:3072 sc1\n"
"s_add_u32 s96, s59, 0x400000\n"
"buffer_load_dwordx4 v[60:63], v0, s[4:7], s96 offen sc1\n"
"buffer_load_dwordx4 v[76:79], v0, s[4:7], s96 offen offset:1024 sc1\n"
"buffer_load_dwordx4 v[116:119], v0, s[4:7], s96 offen offset:2048 sc1\n"
"s_waitcnt vmcnt(0)\n"
"v_max_u32_e32 v255, v136, v137\n"
"v_max3_u32 v255, v255, v138, v139\n"
"v_max3_u32 v255, v255, v24, v25\n"
"v_max3_u32 v255, v255, v26, v27\n"
"v_max3_u32 v255, v255, v112, v113\n"
"v_max3_u32 v255, v255, v114, v115\n"
"v_max3_u32 v255, v255, v80, v81\n"
"v_max3_u32 v255, v255, v82, v83\n"
"v_max3_u32 v255, v255, v36, v37\n"
"v_max3_u32 v255, v255, v38, v39\n"
"v_max3_u32 v255, v255, v44, v45\n"
"v_max3_u32 v255, v255, v46, v47\n"
"v_max3_u32 v255, v255, v84, v85\n"
"v_max3_u32 v255, v255, v86, v87\n"
"v_max3_u32 v255, v255, v56, v57\n"
"v_max3_u32 v255, v255, v58, v59\n"
"v_cmp_eq_u32_e32 vcc, -1, v255\n"
"s_cbranch_vccz .Lmk_p69k\n"
"s_sub_u32 s1, s1, 1\n"
"s_cbranch_scc0 .Lmk_p69l\n"
".Lmk_p69k:\n"
"v_mfma_f32_16x16x32_bf16 v[132:135], a[128:131], v[136:139], 0\n"
"v_mfma_f32_16x16x32_bf16 v[144:147], a[132:135], v[136:139], 0\n"
"v_mfma_f32_16x16x32_bf16 v[148:151], a[136:139], v[136:139], 0\n"
"v_mfma_f32_16x16x32_bf16 v[108:111], a[140:143], v[136:139], 0\n"
"v_mfma_f32_16x16x32_bf16 v[132:135], a[144:147], v[24:27], v[132:135]\n"
"v_mfma_f32_16x16x32_bf16 v[144:147], a[148:151], v[24:27], v[144:147]\n"
"v_mfma_f32_16x16x32_bf16 v[148:151], a[152:155], v[24:27], v[148:151]\n"
"v_mfma_f32_16x16x32_bf16 v[108:111], a[156:159], v[24:27], v[108:111]\n"
"v_mfma_f32_16x16x32_bf16 v[132:135], a[160:163], v[112:115], v[132:135]\n"
"v_mfma_f32_16x16x32_bf16 v[144:147], a[164:167], v[112:115], v[144:147]\n"
"v_mfma_f32_16x16x32_bf16 v[148:151], a[168:171], v[112:115], v[148:151]\n"
"v_mfma_f32_16x16x32_bf16 v[108:111], a[172:175], v[112:115], v[108:111]\n"
"v_mfma_f32_16x16x32_bf16 v[132:135], a[176:179], v[80:83], v[132:135]\n"
"v_mfma_f32_16x16x32_bf16 v[144:147], a[180:183], v[80:83], v[144:147]\n"
"v_mfma_f32_16x16x32_bf16 v[148:151], a[184:187], v[80:83], v[148:151]\n"
"v_mfma_f32_16x16x32_bf16 v[108:111], a[188:191], v[80:83], v[108:111]\n"
"v_mfma_f32_16x16x32_bf16 v[132:135], a[192:195], v[36:39], v[132:135]\n"
"v_mfma_f32_16x16x32_bf16 v[144:147], a[196:199], v[36:39], v[144:147]\n"
"v_mfma_f32_16x16x32_bf16 v[148:151], a[200:203], v[36:39], v[148:151]\n"
"v_mfma_f32_16x16x32_bf16 v[108:111], a[204:207], v[36:39], v[108:111]\n"
"v_mfma_f32_16x16x32_bf16 v[132:135], a[208:211], v[44:47], v[132:135]\n"
"v_mfma_f32_16x16x32_bf16 v[144:147], a[212:215], v[44:47], v[144:147]\n"
"v_mfma_f32_16x16x32_bf16 v[148:151], a[216:219], v[44:47], v[148:151]\n"
"v_mfma_f32_16x16x32_bf16 v[108:111], a[220:223], v[44:47], v[108:111]\n"
"v_mfma_f32_16x16x32_bf16 v[132:135], a[224:227], v[84:87], v[132:135]\n"
"v_mfma_f32_16x16x32_bf16 v[144:147], a[228:231], v[84:87], v[144:147]\n"
"v_mfma_f32_16x16x32_bf16 v[148:151], a[232:235], v[84:87], v[148:151]\n"
"v_mfma_f32_16x16x32_bf16 v[108:111], a[236:239], v[84:87], v[108:111]\n"
"v_mfma_f32_16x16x32_bf16 v[132:135], a[240:243], v[56:59], v[132:135]\n"
"v_mfma_f32_16x16x32_bf16 v[144:147], a[244:247], v[56:59], v[144:147]\n"
"v_mfma_f32_16x16x32_bf16 v[148:151], a[248:251], v[56:59], v[148:151]\n"
"v_mfma_f32_16x16x32_bf16 v[108:111], a[252:255], v[56:59], v[108:111]\n"
"s_add_u32 s96, s58, 0x400000\n"
"v_mov_b32_e32 v255, 0x80000000\n"
"buffer_load_dwordx4 v[136:139], v0, s[4:7], s96 offen sc1\n"
"buffer_load_dwordx4 v[24:27], v0, s[4:7], s96 offen offset:1024 sc1\n"
"buffer_load_dwordx4 v[112:115], v0, s[4:7], s96 offen offset:2048 sc1\n"
"buffer_load_dwordx4 v[80:83], v0, s[4:7], s96 offen offset:3072 sc1\n"
"buffer_load_dwordx4 v[36:39], v1, s[4:7], s96 offen sc1\n"
"buffer_load_dwordx4 v[44:47], v1, s[4:7], s96 offen offset:1024 sc1\n"
"buffer_load_dwordx4 v[84:87], v1, s[4:7], s96 offen offset:2048 sc1\n"
"buffer_load_dwordx4 v[56:59], v1, s[4:7], s96 offen offset:3072 sc1\n"
"buffer_store_dword v255, v255, s[4:7], 0 offen\n"
"buffer_store_dword v255, v255, s[4:7], 0 offen\n"
"buffer_store_dword v255, v255, s[4:7], 0 offen\n"
"s_nop 7\n"
"s_branch .Lmk_p69r\n"
".Lmk_p70s:\n"
";\n"
"s_mov_b32 s1, 0x10000\n"
".Lmk_p70l:\n"
"s_add_u32 s98, s57, 0x400000\n"
"buffer_load_dwordx4 v[60:63], v0, s[4:7], s98 offen sc1\n"
"buffer_load_dwordx4 v[76:79], v0, s[4:7], s98 offen offset:1024 sc1\n"
"buffer_load_dwordx4 v[116:119], v0, s[4:7], s98 offen offset:2048 sc1\n"
"buffer_load_dwordx4 v[152:155], v0, s[4:7], s98 offen offset:3072 sc1\n"
"buffer_load_dwordx4 v[68:71], v1, s[4:7], s98 offen sc1\n"
"buffer_load_dwordx4 v[92:95], v1, s[4:7], s98 offen offset:1024 sc1\n"
"buffer_load_dwordx4 v[108:111], v1, s[4:7], s98 offen offset:2048 sc1\n"
"buffer_load_dwordx4 v[72:75], v1, s[4:7], s98 offen offset:3072 sc1\n"
"s_add_u32 s98, s58, 0x400000\n"
"buffer_load_dwordx4 v[136:139], v0, s[4:7], s98 offen sc1\n"
"buffer_load_dwordx4 v[24:27], v0, s[4:7], s98 offen offset:1024 sc1\n"
"buffer_load_dwordx4 v[112:115], v0, s[4:7], s98 offen offset:2048 sc1\n"
"buffer_load_dwordx4 v[80:83], v0, s[4:7], s98 offen offset:3072 sc1\n"
"buffer_load_dwordx4 v[36:39], v1, s[4:7], s98 offen sc1\n"
"buffer_load_dwordx4 v[44:47], v1, s[4:7], s98 offen offset:1024 sc1\n"
"buffer_load_dwordx4 v[84:87], v1, s[4:7], s98 offen offset:2048 sc1\n"
"buffer_load_dwordx4 v[56:59], v1, s[4:7], s98 offen offset:3072 sc1\n"
"s_waitcnt vmcnt(0)\n"
"v_max_u32_e32 v255, v60, v61\n"
"v_max3_u32 v255, v255, v62, v63\n"
"v_max3_u32 v255, v255, v76, v77\n"
"v_max3_u32 v255, v255, v78, v79\n"
"v_max3_u32 v255, v255, v116, v117\n"
"v_max3_u32 v255, v255, v118, v119\n"
"v_max3_u32 v255, v255, v152, v153\n"
"v_max3_u32 v255, v255, v154, v155\n"
"v_max3_u32 v255, v255, v68, v69\n"
"v_max3_u32 v255, v255, v70, v71\n"
"v_max3_u32 v255, v255, v92, v93\n"
"v_max3_u32 v255, v255, v94, v95\n"
"v_max3_u32 v255, v255, v108, v109\n"
"v_max3_u32 v255, v255, v110, v111\n"
"v_max3_u32 v255, v255, v72, v73\n"
"v_max3_u32 v255, v255, v74, v75\n"
"v_cmp_eq_u32_e32 vcc, -1, v255\n"
"s_cbranch_vccz .Lmk_p70k\n"
"s_sub_u32 s1, s1, 1\n"
"s_cbranch_scc0 .Lmk_p70l\n"
".Lmk_p70k:\n"
"v_mfma_f32_16x16x32_bf16 v[64:67], a[128:131], v[60:63], 0\n"
"v_mfma_f32_16x16x32_bf16 v[124:127], a[132:135], v[60:63], 0\n"
"v_mfma_f32_16x16x32_bf16 v[140:143], a[136:139], v[60:63], 0\n"
"v_mfma_f32_16x16x32_bf16 v[156:159], a[140:143], v[60:63], 0\n"
"v_mfma_f32_16x16x32_bf16 v[64:67], a[144:147], v[76:79], v[64:67]\n"
"v_mfma_f32_16x16x32_bf16 v[124:127], a[148:151], v[76:79], v[124:127]\n"
"v_mfma_f32_16x16x32_bf16 v[140:143], a[152:155], v[76:79], v[140:143]\n"
"v_mfma_f32_16x16x32_bf16 v[156:159], a[156:159], v[76:79], v[156:159]\n"
"v_mfma_f32_16x16x32_bf16 v[64:67], a[160:163], v[116:119], v[64:67]\n"
"v_mfma_f32_16x16x32_bf16 v[124:127], a[164:167], v[116:119], v[124:127]\n"
"v_mfma_f32_16x16x32_bf16 v[140:143], a[168:171], v[116:119], v[140:143]\n"
"v_mfma_f32_16x16x32_bf16 v[156:159], a[172:175], v[116:119], v[156:159]\n"
"v_mfma_f32_16x16x32_bf16 v[64:67], a[176:179], v[152:155], v[64:67]\n"
"v_mfma_f32_16x16x32_bf16 v[124:127], a[180:183], v[152:155], v[124:127]\n"
"v_mfma_f32_16x16x32_bf16 v[140:143], a[184:187], v[152:155], v[140:143]\n"
"v_mfma_f32_16x16x32_bf16 v[156:159], a[188:191], v[152:155], v[156:159]\n"
"v_mfma_f32_16x16x32_bf16 v[64:67], a[192:195], v[68:71], v[64:67]\n"
"v_mfma_f32_16x16x32_bf16 v[124:127], a[196:199], v[68:71], v[124:127]\n"
"v_mfma_f32_16x16x32_bf16 v[140:143], a[200:203], v[68:71], v[140:143]\n"
"v_mfma_f32_16x16x32_bf16 v[156:159], a[204:207], v[68:71], v[156:159]\n"
"v_mfma_f32_16x16x32_bf16 v[64:67], a[208:211], v[92:95], v[64:67]\n"
"v_mfma_f32_16x16x32_bf16 v[124:127], a[212:215], v[92:95], v[124:127]\n"
"v_mfma_f32_16x16x32_bf16 v[140:143], a[216:219], v[92:95], v[140:143]\n"
"v_mfma_f32_16x16x32_bf16 v[156:159], a[220:223], v[92:95], v[156:159]\n"
"v_mfma_f32_16x16x32_bf16 v[64:67], a[224:227], v[108:111], v[64:67]\n"
"v_mfma_f32_16x16x32_bf16 v[124:127], a[228:231], v[108:111], v[124:127]\n"
"v_mfma_f32_16x16x32_bf16 v[140:143], a[232:235], v[108:111], v[140:143]\n"
"v_mfma_f32_16x16x32_bf16 v[156:159], a[236:239], v[108:111], v[156:159]\n"
"v_mfma_f32_16x16x32_bf16 v[64:67], a[240:243], v[72:75], v[64:67]\n"
"v_mfma_f32_16x16x32_bf16 v[124:127], a[244:247], v[72:75], v[124:127]\n"
"v_mfma_f32_16x16x32_bf16 v[140:143], a[248:251], v[72:75], v[140:143]\n"
"v_mfma_f32_16x16x32_bf16 v[156:159], a[252:255], v[72:75], v[156:159]\n"
"s_add_u32 s98, s59, 0x400000\n"
"v_mov_b32_e32 v255, 0x80000000\n"
"buffer_load_dwordx4 v[60:63], v0, s[4:7], s98 offen sc1\n"
"buffer_load_dwordx4 v[76:79], v0, s[4:7], s98 offen offset:1024 sc1\n"
"buffer_load_dwordx4 v[116:119], v0, s[4:7], s98 offen offset:2048 sc1\n"
"buffer_load_dwordx4 v[152:155], v0, s[4:7], s98 offen offset:3072 sc1\n"
"buffer_load_dwordx4 v[68:71], v1, s[4:7], s98 offen sc1\n"
"buffer_load_dwordx4 v[92:95], v1, s[4:7], s98 offen offset:1024 sc1\n"
"buffer_load_dwordx4 v[108:111], v1, s[4:7], s98 offen offset:2048 sc1\n"
"buffer_load_dwordx4 v[72:75], v1, s[4:7], s98 offen offset:3072 sc1\n"
"buffer_store_dword v255, v255, s[4:7], 0 offen\n"
"s_nop 7\n"
"s_branch .Lmk_p70r\n"
".Lmk_q72s:\n"
";\n"
"s_mov_b32 s1, 0x10000\n"
".Lmk_q72l:\n"
"s_add_u32 s98, s59, 0x400000\n"
"buffer_load_dwordx4 v[60:63], v0, s[4:7], s98 offen sc1\n"
"buffer_load_dwordx4 v[76:79], v0, s[4:7], s98 offen offset:1024 sc1\n"
"buffer_load_dwordx4 v[116:119], v0, s[4:7], s98 offen offset:2048 sc1\n"
"buffer_load_dwordx4 v[152:155], v0, s[4:7], s98 offen offset:3072 sc1\n"
"buffer_load_dwordx4 v[68:71], v1, s[4:7], s98 offen sc1\n"
"buffer_load_dwordx4 v[92:95], v1, s[4:7], s98 offen offset:1024 sc1\n"
"buffer_load_dwordx4 v[108:111], v1, s[4:7], s98 offen offset:2048 sc1\n"
"buffer_load_dwordx4 v[72:75], v1, s[4:7], s98 offen offset:3072 sc1\n"
"s_waitcnt vmcnt(0)\n"
"v_max_u32_e32 v255, v60, v61\n"
"v_max3_u32 v255, v255, v62, v63\n"
"v_max3_u32 v255, v255, v76, v77\n"
"v_max3_u32 v255, v255, v78, v79\n"
"v_max3_u32 v255, v255, v116, v117\n"
"v_max3_u32 v255, v255, v118, v119\n"
"v_max3_u32 v255, v255, v152, v153\n"
"v_max3_u32 v255, v255, v154, v155\n"
"v_cmp_eq_u32_e32 vcc, -1, v255\n"
"s_cbranch_vccz .Lmk_q72k\n"
"s_sub_u32 s1, s1, 1\n"
"s_cbranch_scc0 .Lmk_q72l\n"
".Lmk_q72k:\n"
"s_branch .Lmk_q72r\n"
".Lmk_p71s:\n"
";\n"
"s_mov_b32 s1, 0x10000\n"
".Lmk_p71l:\n"
"s_add_u32 s98, s58, 0x400000\n"
"buffer_load_dwordx4 v[24:27], v0, s[4:7], s98 offen sc1\n"
"buffer_load_dwordx4 v[36:39], v0, s[4:7], s98 offen offset:1024 sc1\n"
"buffer_load_dwordx4 v[44:47], v0, s[4:7], s98 offen offset:2048 sc1\n"
"buffer_load_dwordx4 v[56:59], v0, s[4:7], s98 offen offset:3072 sc1\n"
"buffer_load_dwordx4 v[64:67], v1, s[4:7], s98 offen sc1\n"
"buffer_load_dwordx4 v[80:83], v1, s[4:7], s98 offen offset:1024 sc1\n"
"buffer_load_dwordx4 v[84:87], v1, s[4:7], s98 offen offset:2048 sc1\n"
"buffer_load_dwordx4 v[96:99], v1, s[4:7], s98 offen offset:3072 sc1\n"
"s_add_u32 s98, s59, 0x400000\n"
"buffer_load_dwordx4 v[60:63], v0, s[4:7], s98 offen sc1\n"
"buffer_load_dwordx4 v[76:79], v0, s[4:7], s98 offen offset:1024 sc1\n"
"buffer_load_dwordx4 v[116:119], v0, s[4:7], s98 offen offset:2048 sc1\n"
"buffer_load_dwordx4 v[152:155], v0, s[4:7], s98 offen offset:3072 sc1\n"
"buffer_load_dwordx4 v[68:71], v1, s[4:7], s98 offen sc1\n"
"buffer_load_dwordx4 v[92:95], v1, s[4:7], s98 offen offset:1024 sc1\n"
"buffer_load_dwordx4 v[108:111], v1, s[4:7], s98 offen offset:2048 sc1\n"
"buffer_load_dwordx4 v[72:75], v1, s[4:7], s98 offen offset:3072 sc1\n"
"s_waitcnt vmcnt(0)\n"
"v_max_u32_e32 v255, v24, v25\n"
"v_max3_u32 v255, v255, v26, v27\n"
"v_max3_u32 v255, v255, v36, v37\n"
"v_max3_u32 v255, v255, v38, v39\n"
"v_max3_u32 v255, v255, v44, v45\n"
"v_max3_u32 v255, v255, v46, v47\n"
"v_max3_u32 v255, v255, v56, v57\n"
"v_max3_u32 v255, v255, v58, v59\n"
"v_max3_u32 v255, v255, v64, v65\n"
"v_max3_u32 v255, v255, v66, v67\n"
"v_max3_u32 v255, v255, v80, v81\n"
"v_max3_u32 v255, v255, v82, v83\n"
"v_max3_u32 v255, v255, v84, v85\n"
"v_max3_u32 v255, v255, v86, v87\n"
"v_max3_u32 v255, v255, v96, v97\n"
"v_max3_u32 v255, v255, v98, v99\n"
"v_cmp_eq_u32_e32 vcc, -1, v255\n"
"s_cbranch_vccz .Lmk_p71k\n"
"s_sub_u32 s1, s1, 1\n"
"s_cbranch_scc0 .Lmk_p71l\n"
".Lmk_p71k:\n"
"v_mfma_f32_16x16x32_bf16 v[32:35], a[128:131], v[24:27], 0\n"
"v_mfma_f32_16x16x32_bf16 v[100:103], a[132:135], v[24:27], 0\n"
"v_mfma_f32_16x16x32_bf16 v[104:107], a[136:139], v[24:27], 0\n"
"v_mfma_f32_16x16x32_bf16 v[120:123], a[140:143], v[24:27], 0\n"
"v_mfma_f32_16x16x32_bf16 v[32:35], a[144:147], v[36:39], v[32:35]\n"
"v_mfma_f32_16x16x32_bf16 v[100:103], a[148:151], v[36:39], v[100:103]\n"
"v_mfma_f32_16x16x32_bf16 v[104:107], a[152:155], v[36:39], v[104:107]\n"
"v_mfma_f32_16x16x32_bf16 v[120:123], a[156:159], v[36:39], v[120:123]\n"
"v_mfma_f32_16x16x32_bf16 v[32:35], a[160:163], v[44:47], v[32:35]\n"
"v_mfma_f32_16x16x32_bf16 v[100:103], a[164:167], v[44:47], v[100:103]\n"
"v_mfma_f32_16x16x32_bf16 v[104:107], a[168:171], v[44:47], v[104:107]\n"
"v_mfma_f32_16x16x32_bf16 v[120:123], a[172:175], v[44:47], v[120:123]\n"
"v_mfma_f32_16x16x32_bf16 v[32:35], a[176:179], v[56:59], v[32:35]\n"
"v_mfma_f32_16x16x32_bf16 v[100:103], a[180:183], v[56:59], v[100:103]\n"
"v_mfma_f32_16x16x32_bf16 v[104:107], a[184:187], v[56:59], v[104:107]\n"
"v_mfma_f32_16x16x32_bf16 v[120:123], a[188:191], v[56:59], v[120:123]\n"
"v_mfma_f32_16x16x32_bf16 v[32:35], a[192:195], v[64:67], v[32:35]\n"
"v_mfma_f32_16x16x32_bf16 v[100:103], a[196:199], v[64:67], v[100:103]\n"
"v_mfma_f32_16x16x32_bf16 v[104:107], a[200:203], v[64:67], v[104:107]\n"
"v_mfma_f32_16x16x32_bf16 v[120:123], a[204:207], v[64:67], v[120:123]\n"
"v_mfma_f32_16x16x32_bf16 v[32:35], a[208:211], v[80:83], v[32:35]\n"
"v_mfma_f32_16x16x32_bf16 v[100:103], a[212:215], v[80:83], v[100:103]\n"
"v_mfma_f32_16x16x32_bf16 v[104:107], a[216:219], v[80:83], v[104:107]\n"
"v_mfma_f32_16x16x32_bf16 v[120:123], a[220:223], v[80:83], v[120:123]\n"
"v_mfma_f32_16x16x32_bf16 v[32:35], a[224:227], v[84:87], v[32:35]\n"
"v_mfma_f32_16x16x32_bf16 v[100:103], a[228:231], v[84:87], v[100:103]\n"
"v_mfma_f32_16x16x32_bf16 v[104:107], a[232:235], v[84:87], v[104:107]\n"
"v_mfma_f32_16x16x32_bf16 v[120:123], a[236:239], v[84:87], v[120:123]\n"
"v_mfma_f32_16x16x32_bf16 v[32:35], a[240:243], v[96:99], v[32:35]\n"
"v_mfma_f32_16x16x32_bf16 v[100:103], a[244:247], v[96:99], v[100:103]\n"
"v_mfma_f32_16x16x32_bf16 v[104:107], a[248:251], v[96:99], v[104:107]\n"
"v_mfma_f32_16x16x32_bf16 v[120:123], a[252:255], v[96:99], v[120:123]\n"
"s_nop 7\n"
"s_branch .Lmk_p71r\n"
".Lmk_q73s:\n"
";\n"
"s_mov_b32 s1, 0x10000\n"
".Lmk_q73l:\n"
"s_add_u32 s98, s59, 0x400000\n"
"buffer_load_dwordx4 v[68:71], v1, s[4:7], s98 offen sc1\n"
"buffer_load_dwordx4 v[92:95], v1, s[4:7], s98 offen offset:1024 sc1\n"
"buffer_load_dwordx4 v[108:111], v1, s[4:7], s98 offen offset:2048 sc1\n"
"buffer_load_dwordx4 v[72:75], v1, s[4:7], s98 offen offset:3072 sc1\n"
"s_waitcnt vmcnt(0)\n"
"v_max_u32_e32 v255, v68, v69\n"
"v_max3_u32 v255, v255, v70, v71\n"
"v_max3_u32 v255, v255, v92, v93\n"
"v_max3_u32 v255, v255, v94, v95\n"
"v_max3_u32 v255, v255, v108, v109\n"
"v_max3_u32 v255, v255, v110, v111\n"
"v_max3_u32 v255, v255, v72, v73\n"
"v_max3_u32 v255, v255, v74, v75\n"
"v_cmp_eq_u32_e32 vcc, -1, v255\n"
"s_cbranch_vccz .Lmk_q73k\n"
"s_sub_u32 s1, s1, 1\n"
"s_cbranch_scc0 .Lmk_q73l\n"
".Lmk_q73k:\n"
"s_branch .Lmk_q73r\n"
".Lmk_p74s:\n"
";\n"
"s_mov_b32 s1, 0x10000\n"
".Lmk_p74l:\n"
"s_add_u32 s97, s56, 0x440000\n"
"buffer_load_dwordx4 v[132:135], v0, s[4:7], s97 offen sc1\n"
"buffer_load_dwordx4 v[24:27], v0, s[4:7], s97 offen offset:1024 sc1\n"
"buffer_load_dwordx4 v[32:35], v0, s[4:7], s97 offen offset:2048 sc1\n"
"buffer_load_dwordx4 v[36:39], v0, s[4:7], s97 offen offset:3072 sc1\n"
"buffer_load_dwordx4 v[140:143], v1, s[4:7], s97 offen sc1\n"
"buffer_load_dwordx4 v[44:47], v1, s[4:7], s97 offen offset:1024 sc1\n"
"buffer_load_dwordx4 v[48:51], v1, s[4:7], s97 offen offset:2048 sc1\n"
"buffer_load_dwordx4 v[52:55], v1, s[4:7], s97 offen offset:3072 sc1\n"
"s_add_u32 s97, s57, 0x440000\n"
"buffer_load_dwordx4 v[28:31], v0, s[4:7], s97 offen sc1\n"
"buffer_load_dwordx4 v[40:43], v0, s[4:7], s97 offen offset:1024 sc1\n"
"buffer_load_dwordx4 v[56:59], v0, s[4:7], s97 offen offset:2048 sc1\n"
"buffer_load_dwordx4 v[60:63], v0, s[4:7], s97 offen offset:3072 sc1\n"
"buffer_load_dwordx4 v[72:75], v1, s[4:7], s97 offen sc1\n"
"buffer_load_dwordx4 v[76:79], v1, s[4:7], s97 offen offset:1024 sc1\n"
"buffer_load_dwordx4 v[80:83], v1, s[4:7], s97 offen offset:2048 sc1\n"
"buffer_load_dwordx4 v[84:87], v1, s[4:7], s97 offen offset:3072 sc1\n"
"s_add_u32 s97, s58, 0x440000\n"
"buffer_load_dwordx4 v[88:91], v0, s[4:7], s97 offen sc1\n"
"buffer_load_dwordx4 v[92:95], v0, s[4:7], s97 offen offset:1024 sc1\n"
"buffer_load_dwordx4 v[96:99], v0, s[4:7], s97 offen offset:2048 sc1\n"
"buffer_load_dwordx4 v[100:103], v0, s[4:7], s97 offen offset:3072 sc1\n"
"buffer_load_dwordx4 v[104:107], v1, s[4:7], s97 offen sc1\n"
"buffer_load_dwordx4 v[108:111], v1, s[4:7], s97 offen offset:1024 sc1\n"
"buffer_load_dwordx4 v[112:115], v1, s[4:7], s97 offen offset:2048 sc1\n"
"buffer_load_dwordx4 v[116:119], v1, s[4:7], s97 offen offset:3072 sc1\n"
"s_waitcnt vmcnt(0)\n"
"v_max_u32_e32 v255, v132, v133\n"
"v_max3_u32 v255, v255, v134, v135\n"
"v_max3_u32 v255, v255, v24, v25\n"
"v_max3_u32 v255, v255, v26, v27\n"
"v_max3_u32 v255, v255, v32, v33\n"
"v_max3_u32 v255, v255, v34, v35\n"
"v_max3_u32 v255, v255, v36, v37\n"
"v_max3_u32 v255, v255, v38, v39\n"
"v_max3_u32 v255, v255, v140, v141\n"
"v_max3_u32 v255, v255, v142, v143\n"
"v_max3_u32 v255, v255, v44, v45\n"
"v_max3_u32 v255, v255, v46, v47\n"
"v_max3_u32 v255, v255, v48, v49\n"
"v_max3_u32 v255, v255, v50, v51\n"
"v_max3_u32 v255, v255, v52, v53\n"
"v_max3_u32 v255, v255, v54, v55\n"
"v_cmp_eq_u32_e32 vcc, -1, v255\n"
"s_cbranch_vccz .Lmk_p74k\n"
"s_sub_u32 s1, s1, 1\n"
"s_cbranch_scc0 .Lmk_p74l\n"
".Lmk_p74k:\n"
"v_mfma_f32_16x16x32_bf16 v[120:123], a[128:131], v[132:135], 0\n"
"v_mfma_f32_16x16x32_bf16 v[124:127], a[132:135], v[132:135], 0\n"
"v_mfma_f32_16x16x32_bf16 v[128:131], a[136:139], v[132:135], 0\n"
"v_mfma_f32_16x16x32_bf16 v[136:139], a[140:143], v[132:135], 0\n"
"v_mfma_f32_16x16x32_bf16 v[120:123], a[144:147], v[24:27], v[120:123]\n"
"v_mfma_f32_16x16x32_bf16 v[124:127], a[148:151], v[24:27], v[124:127]\n"
"v_mfma_f32_16x16x32_bf16 v[128:131], a[152:155], v[24:27], v[128:131]\n"
"v_mfma_f32_16x16x32_bf16 v[136:139], a[156:159], v[24:27], v[136:139]\n"
"v_mfma_f32_16x16x32_bf16 v[120:123], a[160:163], v[32:35], v[120:123]\n"
"v_mfma_f32_16x16x32_bf16 v[124:127], a[164:167], v[32:35], v[124:127]\n"
"v_mfma_f32_16x16x32_bf16 v[128:131], a[168:171], v[32:35], v[128:131]\n"
"v_mfma_f32_16x16x32_bf16 v[136:139], a[172:175], v[32:35], v[136:139]\n"
"v_mfma_f32_16x16x32_bf16 v[120:123], a[176:179], v[36:39], v[120:123]\n"
"v_mfma_f32_16x16x32_bf16 v[124:127], a[180:183], v[36:39], v[124:127]\n"
"v_mfma_f32_16x16x32_bf16 v[128:131], a[184:187], v[36:39], v[128:131]\n"
"v_mfma_f32_16x16x32_bf16 v[136:139], a[188:191], v[36:39], v[136:139]\n"
"v_mfma_f32_16x16x32_bf16 v[120:123], a[192:195], v[140:143], v[120:123]\n"
"v_mfma_f32_16x16x32_bf16 v[124:127], a[196:199], v[140:143], v[124:127]\n"
"v_mfma_f32_16x16x32_bf16 v[128:131], a[200:203], v[140:143], v[128:131]\n"
"v_mfma_f32_16x16x32_bf16 v[136:139], a[204:207], v[140:143], v[136:139]\n"
"v_mfma_f32_16x16x32_bf16 v[120:123], a[208:211], v[44:47], v[120:123]\n"
"v_mfma_f32_16x16x32_bf16 v[124:127], a[212:215], v[44:47], v[124:127]\n"
"v_mfma_f32_16x16x32_bf16 v[128:131], a[216:219], v[44:47], v[128:131]\n"
"v_mfma_f32_16x16x32_bf16 v[136:139], a[220:223], v[44:47], v[136:139]\n"
"v_mfma_f32_16x16x32_bf16 v[120:123], a[224:227], v[48:51], v[120:123]\n"
"v_mfma_f32_16x16x32_bf16 v[124:127], a[228:231], v[48:51], v[124:127]\n"
"v_mfma_f32_16x16x32_bf16 v[128:131], a[232:235], v[48:51], v[128:131]\n"
"v_mfma_f32_16x16x32_bf16 v[136:139], a[236:239], v[48:51], v[136:139]\n"
"v_mfma_f32_16x16x32_bf16 v[120:123], a[240:243], v[52:55], v[120:123]\n"
"v_mfma_f32_16x16x32_bf16 v[124:127], a[244:247], v[52:55], v[124:127]\n"
"v_mfma_f32_16x16x32_bf16 v[128:131], a[248:251], v[52:55], v[128:131]\n"
"v_mfma_f32_16x16x32_bf16 v[136:139], a[252:255], v[52:55], v[136:139]\n"
"s_add_u32 s97, s59, 0x440000\n"
"v_mov_b32_e32 v255, 0x80000000\n"
"buffer_load_dwordx4 v[132:135], v0, s[4:7], s97 offen sc1\n"
"buffer_load_dwordx4 v[24:27], v0, s[4:7], s97 offen offset:1024 sc1\n"
"buffer_load_dwordx4 v[32:35], v0, s[4:7], s97 offen offset:2048 sc1\n"
"buffer_load_dwordx4 v[36:39], v0, s[4:7], s97 offen offset:3072 sc1\n"
"buffer_load_dwordx4 v[140:143], v1, s[4:7], s97 offen sc1\n"
"buffer_load_dwordx4 v[44:47], v1, s[4:7], s97 offen offset:1024 sc1\n"
"buffer_load_dwordx4 v[48:51], v1, s[4:7], s97 offen offset:2048 sc1\n"
"buffer_load_dwordx4 v[52:55], v1, s[4:7], s97 offen offset:3072 sc1\n"
"buffer_store_dword v255, v255, s[4:7], 0 offen\n"
"s_nop 7\n"
"s_branch .Lmk_p74r\n"
".Lmk_p75s:\n"
";\n"
"s_mov_b32 s1, 0x10000\n"
".Lmk_p75l:\n"
"s_add_u32 s96, s57, 0x440000\n"
"buffer_load_dwordx4 v[28:31], v0, s[4:7], s96 offen sc1\n"
"buffer_load_dwordx4 v[40:43], v0, s[4:7], s96 offen offset:1024 sc1\n"
"buffer_load_dwordx4 v[60:63], v0, s[4:7], s96 offen offset:2048 sc1\n"
"buffer_load_dwordx4 v[124:127], v0, s[4:7], s96 offen offset:3072 sc1\n"
"buffer_load_dwordx4 v[72:75], v1, s[4:7], s96 offen sc1\n"
"buffer_load_dwordx4 v[156:159], v1, s[4:7], s96 offen offset:1024 sc1\n"
"buffer_load_dwordx4 v[80:83], v1, s[4:7], s96 offen offset:2048 sc1\n"
"buffer_load_dwordx4 v[56:59], v1, s[4:7], s96 offen offset:3072 sc1\n"
"s_add_u32 s96, s58, 0x440000\n"
"buffer_load_dwordx4 v[88:91], v0, s[4:7], s96 offen sc1\n"
"buffer_load_dwordx4 v[92:95], v0, s[4:7], s96 offen offset:1024 sc1\n"
"buffer_load_dwordx4 v[96:99], v0, s[4:7], s96 offen offset:2048 sc1\n"
"buffer_load_dwordx4 v[100:103], v0, s[4:7], s96 offen offset:3072 sc1\n"
"buffer_load_dwordx4 v[104:107], v1, s[4:7], s96 offen sc1\n"
"buffer_load_dwordx4 v[108:111], v1, s[4:7], s96 offen offset:1024 sc1\n"
"buffer_load_dwordx4 v[112:115], v1, s[4:7], s96 offen offset:2048 sc1\n"
"buffer_load_dwordx4 v[116:119], v1, s[4:7], s96 offen offset:3072 sc1\n"
"s_add_u32 s96, s59, 0x440000\n"
"buffer_load_dwordx4 v[132:135], v0, s[4:7], s96 offen sc1\n"
"buffer_load_dwordx4 v[24:27], v0, s[4:7], s96 offen offset:1024 sc1\n"
"buffer_load_dwordx4 v[32:35], v0, s[4:7], s96 offen offset:2048 sc1\n"
"buffer_load_dwordx4 v[36:39], v0, s[4:7], s96 offen offset:3072 sc1\n"
"buffer_load_dwordx4 v[140:143], v1, s[4:7], s96 offen sc1\n"
"buffer_load_dwordx4 v[44:47], v1, s[4:7], s96 offen offset:1024 sc1\n"
"buffer_load_dwordx4 v[48:51], v1, s[4:7], s96 offen offset:2048 sc1\n"
"buffer_load_dwordx4 v[52:55], v1, s[4:7], s96 offen offset:3072 sc1\n"
"s_waitcnt vmcnt(0)\n"
"v_max_u32_e32 v243, v28, v29\n"
"v_max3_u32 v243, v243, v30, v31\n"
"v_max3_u32 v243, v243, v40, v41\n"
"v_max3_u32 v243, v243, v42, v43\n"
"v_max3_u32 v243, v243, v60, v61\n"
"v_max3_u32 v243, v243, v62, v63\n"
"v_max3_u32 v243, v243, v124, v125\n"
"v_max3_u32 v243, v243, v126, v127\n"
"v_max3_u32 v243, v243, v72, v73\n"
"v_max3_u32 v243, v243, v74, v75\n"
"v_max3_u32 v243, v243, v156, v157\n"
"v_max3_u32 v243, v243, v158, v159\n"
"v_max3_u32 v243, v243, v80, v81\n"
"v_max3_u32 v243, v243, v82, v83\n"
"v_max3_u32 v243, v243, v56, v57\n"
"v_max3_u32 v243, v243, v58, v59\n"
"v_cmp_eq_u32_e32 vcc, -1, v243\n"
"s_cbranch_vccz .Lmk_p75k\n"
"s_sub_u32 s1, s1, 1\n"
"s_cbranch_scc0 .Lmk_p75l\n"
".Lmk_p75k:\n"
"v_mfma_f32_16x16x32_bf16 v[64:67], a[128:131], v[28:31], 0\n"
"v_mfma_f32_16x16x32_bf16 v[68:71], a[132:135], v[28:31], 0\n"
"v_mfma_f32_16x16x32_bf16 v[144:147], a[136:139], v[28:31], 0\n"
"v_mfma_f32_16x16x32_bf16 v[148:151], a[140:143], v[28:31], 0\n"
"v_mfma_f32_16x16x32_bf16 v[64:67], a[144:147], v[40:43], v[64:67]\n"
"v_mfma_f32_16x16x32_bf16 v[68:71], a[148:151], v[40:43], v[68:71]\n"
"v_mfma_f32_16x16x32_bf16 v[144:147], a[152:155], v[40:43], v[144:147]\n"
"v_mfma_f32_16x16x32_bf16 v[148:151], a[156:159], v[40:43], v[148:151]\n"
"v_mfma_f32_16x16x32_bf16 v[64:67], a[160:163], v[60:63], v[64:67]\n"
"v_mfma_f32_16x16x32_bf16 v[68:71], a[164:167], v[60:63], v[68:71]\n"
"v_mfma_f32_16x16x32_bf16 v[144:147], a[168:171], v[60:63], v[144:147]\n"
"v_mfma_f32_16x16x32_bf16 v[148:151], a[172:175], v[60:63], v[148:151]\n"
"v_mfma_f32_16x16x32_bf16 v[64:67], a[176:179], v[124:127], v[64:67]\n"
"v_mfma_f32_16x16x32_bf16 v[68:71], a[180:183], v[124:127], v[68:71]\n"
"v_mfma_f32_16x16x32_bf16 v[144:147], a[184:187], v[124:127], v[144:147]\n"
"v_mfma_f32_16x16x32_bf16 v[148:151], a[188:191], v[124:127], v[148:151]\n"
"v_mfma_f32_16x16x32_bf16 v[64:67], a[192:195], v[72:75], v[64:67]\n"
"v_mfma_f32_16x16x32_bf16 v[68:71], a[196:199], v[72:75], v[68:71]\n"
"v_mfma_f32_16x16x32_bf16 v[144:147], a[200:203], v[72:75], v[144:147]\n"
"v_mfma_f32_16x16x32_bf16 v[148:151], a[204:207], v[72:75], v[148:151]\n"
"v_mfma_f32_16x16x32_bf16 v[64:67], a[208:211], v[156:159], v[64:67]\n"
"v_mfma_f32_16x16x32_bf16 v[68:71], a[212:215], v[156:159], v[68:71]\n"
"v_mfma_f32_16x16x32_bf16 v[144:147], a[216:219], v[156:159], v[144:147]\n"
"v_mfma_f32_16x16x32_bf16 v[148:151], a[220:223], v[156:159], v[148:151]\n"
"v_mfma_f32_16x16x32_bf16 v[64:67], a[224:227], v[80:83], v[64:67]\n"
"v_mfma_f32_16x16x32_bf16 v[68:71], a[228:231], v[80:83], v[68:71]\n"
"v_mfma_f32_16x16x32_bf16 v[144:147], a[232:235], v[80:83], v[144:147]\n"
"v_mfma_f32_16x16x32_bf16 v[148:151], a[236:239], v[80:83], v[148:151]\n"
"v_mfma_f32_16x16x32_bf16 v[64:67], a[240:243], v[56:59], v[64:67]\n"
"v_mfma_f32_16x16x32_bf16 v[68:71], a[244:247], v[56:59], v[68:71]\n"
"v_mfma_f32_16x16x32_bf16 v[144:147], a[248:251], v[56:59], v[144:147]\n"
"v_mfma_f32_16x16x32_bf16 v[148:151], a[252:255], v[56:59], v[148:151]\n"
"s_add_u32 s96, s56, 0x400000\n"
"v_mov_b32_e32 v243, 0x80000000\n"
"buffer_load_dwordx4 v[28:31], v0, s[4:7], s96 offen sc1\n"
"buffer_load_dwordx4 v[40:43], v0, s[4:7], s96 offen offset:1024 sc1\n"
"buffer_load_dwordx4 v[60:63], v0, s[4:7], s96 offen offset:2048 sc1\n"
"buffer_load_dwordx4 v[124:127], v0, s[4:7], s96 offen offset:3072 sc1\n"
"buffer_load_dwordx4 v[72:75], v1, s[4:7], s96 offen sc1\n"
"buffer_load_dwordx4 v[156:159], v1, s[4:7], s96 offen offset:1024 sc1\n"
"buffer_load_dwordx4 v[80:83], v1, s[4:7], s96 offen offset:2048 sc1\n"
"buffer_load_dwordx4 v[56:59], v1, s[4:7], s96 offen offset:3072 sc1\n"
"s_nop 7\n"
"s_branch .Lmk_p75r\n"
".Lmk_q77s:\n"
";\n"
"s_mov_b32 s1, 0x10000\n"
".Lmk_q77l:\n"
"s_add_u32 s96, s59, 0x440000\n"
"buffer_load_dwordx4 v[132:135], v0, s[4:7], s96 offen sc1\n"
"buffer_load_dwordx4 v[24:27], v0, s[4:7], s96 offen offset:1024 sc1\n"
"buffer_load_dwordx4 v[32:35], v0, s[4:7], s96 offen offset:2048 sc1\n"
"buffer_load_dwordx4 v[36:39], v0, s[4:7], s96 offen offset:3072 sc1\n"
"buffer_load_dwordx4 v[140:143], v1, s[4:7], s96 offen sc1\n"
"buffer_load_dwordx4 v[44:47], v1, s[4:7], s96 offen offset:1024 sc1\n"
"buffer_load_dwordx4 v[48:51], v1, s[4:7], s96 offen offset:2048 sc1\n"
"buffer_load_dwordx4 v[52:55], v1, s[4:7], s96 offen offset:3072 sc1\n"
"s_add_u32 s96, s56, 0x400000\n"
"buffer_load_dwordx4 v[28:31], v0, s[4:7], s96 offen sc1\n"
"buffer_load_dwordx4 v[40:43], v0, s[4:7], s96 offen offset:1024 sc1\n"
"buffer_load_dwordx4 v[60:63], v0, s[4:7], s96 offen offset:2048 sc1\n"
"buffer_load_dwordx4 v[124:127], v0, s[4:7], s96 offen offset:3072 sc1\n"
"buffer_load_dwordx4 v[72:75], v1, s[4:7], s96 offen sc1\n"
"buffer_load_dwordx4 v[156:159], v1, s[4:7], s96 offen offset:1024 sc1\n"
"buffer_load_dwordx4 v[80:83], v1, s[4:7], s96 offen offset:2048 sc1\n"
"buffer_load_dwordx4 v[56:59], v1, s[4:7], s96 offen offset:3072 sc1\n"
"s_add_u32 s96, s57, 0x400000\n"
"buffer_load_dwordx4 v[168:171], v0, s[4:7], s96 offen sc1\n"
"buffer_load_dwordx4 v[64:67], v0, s[4:7], s96 offen offset:1024 sc1\n"
"buffer_load_dwordx4 v[68:71], v0, s[4:7], s96 offen offset:2048 sc1\n"
"buffer_load_dwordx4 v[88:91], v0, s[4:7], s96 offen offset:3072 sc1\n"
"buffer_load_dwordx4 v[92:95], v1, s[4:7], s96 offen sc1\n"
"buffer_load_dwordx4 v[96:99], v1, s[4:7], s96 offen offset:1024 sc1\n"
"buffer_load_dwordx4 v[100:103], v1, s[4:7], s96 offen offset:2048 sc1\n"
"s_waitcnt vmcnt(0)\n"
"v_max_u32_e32 v255, v132, v133\n"
"v_max3_u32 v255, v255, v134, v135\n"
"v_max3_u32 v255, v255, v24, v25\n"
"v_max3_u32 v255, v255, v26, v27\n"
"v_max3_u32 v255, v255, v32, v33\n"
"v_max3_u32 v255, v255, v34, v35\n"
"v_max3_u32 v255, v255, v36, v37\n"
"v_max3_u32 v255, v255, v38, v39\n"
"v_cmp_eq_u32_e32 vcc, -1, v255\n"
"s_cbranch_vccz .Lmk_q77k\n"
"s_sub_u32 s1, s1, 1\n"
"s_cbranch_scc0 .Lmk_q77l\n"
".Lmk_q77k:\n"
"s_branch .Lmk_q77r\n"
".Lmk_p76s:\n"
";\n"
"s_mov_b32 s1, 0x10000\n"
".Lmk_p76l:\n"
"s_add_u32 s96, s58, 0x440000\n"
"buffer_load_dwordx4 v[168:171], v0, s[4:7], s96 offen sc1\n"
"buffer_load_dwordx4 v[64:67], v0, s[4:7], s96 offen offset:1024 sc1\n"
"buffer_load_dwordx4 v[68:71], v0, s[4:7], s96 offen offset:2048 sc1\n"
"buffer_load_dwordx4 v[88:91], v0, s[4:7], s96 offen offset:3072 sc1\n"
"buffer_load_dwordx4 v[92:95], v1, s[4:7], s96 offen sc1\n"
"buffer_load_dwordx4 v[96:99], v1, s[4:7], s96 offen offset:1024 sc1\n"
"buffer_load_dwordx4 v[100:103], v1, s[4:7], s96 offen offset:2048 sc1\n"
"buffer_load_dwordx4 v[104:107], v1, s[4:7], s96 offen offset:3072 sc1\n"
"s_add_u32 s96, s59, 0x440000\n"
"buffer_load_dwordx4 v[132:135], v0, s[4:7], s96 offen sc1\n"
"buffer_load_dwordx4 v[24:27], v0, s[4:7], s96 offen offset:1024 sc1\n"
"buffer_load_dwordx4 v[32:35], v0, s[4:7], s96 offen offset:2048 sc1\n"
"buffer_load_dwordx4 v[36:39], v0, s[4:7], s96 offen offset:3072 sc1\n"
"buffer_load_dwordx4 v[140:143], v1, s[4:7], s96 offen sc1\n"
"buffer_load_dwordx4 v[44:47], v1, s[4:7], s96 offen offset:1024 sc1\n"
"buffer_load_dwordx4 v[48:51], v1, s[4:7], s96 offen offset:2048 sc1\n"
"buffer_load_dwordx4 v[52:55], v1, s[4:7], s96 offen offset:3072 sc1\n"
"s_add_u32 s96, s56, 0x400000\n"
"buffer_load_dwordx4 v[28:31], v0, s[4:7], s96 offen sc1\n"
"buffer_load_dwordx4 v[40:43], v0, s[4:7], s96 offen offset:1024 sc1\n"
"buffer_load_dwordx4 v[60:63], v0, s[4:7], s96 offen offset:2048 sc1\n"
"buffer_load_dwordx4 v[124:127], v0, s[4:7], s96 offen offset:3072 sc1\n"
"buffer_load_dwordx4 v[72:75], v1, s[4:7], s96 offen sc1\n"
"buffer_load_dwordx4 v[156:159], v1, s[4:7], s96 offen offset:1024 sc1\n"
"buffer_load_dwordx4 v[80:83], v1, s[4:7], s96 offen offset:2048 sc1\n"
"buffer_load_dwordx4 v[56:59], v1, s[4:7], s96 offen offset:3072 sc1\n"
"s_waitcnt vmcnt(0)\n"
"v_max_u32_e32 v255, v168, v169\n"
"v_max3_u32 v255, v255, v170, v171\n"
"v_max3_u32 v255, v255, v64, v65\n"
"v_max3_u32 v255, v255, v66, v67\n"
"v_max3_u32 v255, v255, v68, v69\n"
"v_max3_u32 v255, v255, v70, v71\n"
"v_max3_u32 v255, v255, v88, v89\n"
"v_max3_u32 v255, v255, v90, v91\n"
"v_max3_u32 v255, v255, v92, v93\n"
"v_max3_u32 v255, v255, v94, v95\n"
"v_max3_u32 v255, v255, v96, v97\n"
"v_max3_u32 v255, v255, v98, v99\n"
"v_max3_u32 v255, v255, v100, v101\n"
"v_max3_u32 v255, v255, v102, v103\n"
"v_max3_u32 v255, v255, v104, v105\n"
"v_max3_u32 v255, v255, v106, v107\n"
"v_cmp_eq_u32_e32 vcc, -1, v255\n"
"s_cbranch_vccz .Lmk_p76k\n"
"s_sub_u32 s1, s1, 1\n"
"s_cbranch_scc0 .Lmk_p76l\n"
".Lmk_p76k:\n"
"v_mfma_f32_16x16x32_bf16 v[84:87], a[128:131], v[168:171], 0\n"
"v_mfma_f32_16x16x32_bf16 v[160:163], a[132:135], v[168:171], 0\n"
"v_mfma_f32_16x16x32_bf16 v[164:167], a[136:139], v[168:171], 0\n"
"v_mfma_f32_16x16x32_bf16 v[172:175], a[140:143], v[168:171], 0\n"
"v_mfma_f32_16x16x32_bf16 v[84:87], a[144:147], v[64:67], v[84:87]\n"
"v_mfma_f32_16x16x32_bf16 v[160:163], a[148:151], v[64:67], v[160:163]\n"
"v_mfma_f32_16x16x32_bf16 v[164:167], a[152:155], v[64:67], v[164:167]\n"
"v_mfma_f32_16x16x32_bf16 v[172:175], a[156:159], v[64:67], v[172:175]\n"
"v_mfma_f32_16x16x32_bf16 v[84:87], a[160:163], v[68:71], v[84:87]\n"
"v_mfma_f32_16x16x32_bf16 v[160:163], a[164:167], v[68:71], v[160:163]\n"
"v_mfma_f32_16x16x32_bf16 v[164:167], a[168:171], v[68:71], v[164:167]\n"
"v_mfma_f32_16x16x32_bf16 v[172:175], a[172:175], v[68:71], v[172:175]\n"
"v_mfma_f32_16x16x32_bf16 v[84:87], a[176:179], v[88:91], v[84:87]\n"
"v_mfma_f32_16x16x32_bf16 v[160:163], a[180:183], v[88:91], v[160:163]\n"
"v_mfma_f32_16x16x32_bf16 v[164:167], a[184:187], v[88:91], v[164:167]\n"
"v_mfma_f32_16x16x32_bf16 v[172:175], a[188:191], v[88:91], v[172:175]\n"
"v_mfma_f32_16x16x32_bf16 v[84:87], a[192:195], v[92:95], v[84:87]\n"
"v_mfma_f32_16x16x32_bf16 v[160:163], a[196:199], v[92:95], v[160:163]\n"
"v_mfma_f32_16x16x32_bf16 v[164:167], a[200:203], v[92:95], v[164:167]\n"
"v_mfma_f32_16x16x32_bf16 v[172:175], a[204:207], v[92:95], v[172:175]\n"
"v_mfma_f32_16x16x32_bf16 v[84:87], a[208:211], v[96:99], v[84:87]\n"
"v_mfma_f32_16x16x32_bf16 v[160:163], a[212:215], v[96:99], v[160:163]\n"
"v_mfma_f32_16x16x32_bf16 v[164:167], a[216:219], v[96:99], v[164:167]\n"
"v_mfma_f32_16x16x32_bf16 v[172:175], a[220:223], v[96:99], v[172:175]\n"
"v_mfma_f32_16x16x32_bf16 v[84:87], a[224:227], v[100:103], v[84:87]\n"
"v_mfma_f32_16x16x32_bf16 v[160:163], a[228:231], v[100:103], v[160:163]\n"
"v_mfma_f32_16x16x32_bf16 v[164:167], a[232:235], v[100:103], v[164:167]\n"
"v_mfma_f32_16x16x32_bf16 v[172:175], a[236:239], v[100:103], v[172:175]\n"
"v_mfma_f32_16x16x32_bf16 v[84:87], a[240:243], v[104:107], v[84:87]\n"
"v_mfma_f32_16x16x32_bf16 v[160:163], a[244:247], v[104:107], v[160:163]\n"
"v_mfma_f32_16x16x32_bf16 v[164:167], a[248:251], v[104:107], v[164:167]\n"
"v_mfma_f32_16x16x32_bf16 v[172:175], a[252:255], v[104:107], v[172:175]\n"
"s_add_u32 s96, s57, 0x400000\n"
"v_mov_b32_e32 v255, 0x80000000\n"
"buffer_load_dwordx4 v[168:171], v0, s[4:7], s96 offen sc1\n"
"buffer_load_dwordx4 v[64:67], v0, s[4:7], s96 offen offset:1024 sc1\n"
"buffer_load_dwordx4 v[68:71], v0, s[4:7], s96 offen offset:2048 sc1\n"
"buffer_load_dwordx4 v[88:91], v0, s[4:7], s96 offen offset:3072 sc1\n"
"buffer_load_dwordx4 v[92:95], v1, s[4:7], s96 offen sc1\n"
"buffer_load_dwordx4 v[96:99], v1, s[4:7], s96 offen offset:1024 sc1\n"
"buffer_load_dwordx4 v[100:103], v1, s[4:7], s96 offen offset:2048 sc1\n"
"buffer_load_dwordx4 v[104:107], v1, s[4:7], s96 offen offset:3072 sc1\n"
"s_nop 7\n"
"s_branch .Lmk_p76r\n"
".Lmk_q78s:\n"
";\n"
"s_mov_b32 s1, 0x10000\n"
".Lmk_q78l:\n"
"s_add_u32 s96, s59, 0x440000\n"
"buffer_load_dwordx4 v[140:143], v1, s[4:7], s96 offen sc1\n"
"buffer_load_dwordx4 v[44:47], v1, s[4:7], s96 offen offset:1024 sc1\n"
"buffer_load_dwordx4 v[48:51], v1, s[4:7], s96 offen offset:2048 sc1\n"
"buffer_load_dwordx4 v[52:55], v1, s[4:7], s96 offen offset:3072 sc1\n"
"s_add_u32 s96, s56, 0x400000\n"
"buffer_load_dwordx4 v[28:31], v0, s[4:7], s96 offen sc1\n"
"buffer_load_dwordx4 v[40:43], v0, s[4:7], s96 offen offset:1024 sc1\n"
"buffer_load_dwordx4 v[60:63], v0, s[4:7], s96 offen offset:2048 sc1\n"
"buffer_load_dwordx4 v[124:127], v0, s[4:7], s96 offen offset:3072 sc1\n"
"buffer_load_dwordx4 v[72:75], v1, s[4:7], s96 offen sc1\n"
"buffer_load_dwordx4 v[156:159], v1, s[4:7], s96 offen offset:1024 sc1\n"
"buffer_load_dwordx4 v[80:83], v1, s[4:7], s96 offen offset:2048 sc1\n"
"buffer_load_dwordx4 v[56:59], v1, s[4:7], s96 offen offset:3072 sc1\n"
"s_add_u32 s96, s57, 0x400000\n"
"buffer_load_dwordx4 v[168:171], v0, s[4:7], s96 offen sc1\n"
"buffer_load_dwordx4 v[64:67], v0, s[4:7], s96 offen offset:1024 sc1\n"
"buffer_load_dwordx4 v[68:71], v0, s[4:7], s96 offen offset:2048 sc1\n"
"buffer_load_dwordx4 v[88:91], v0, s[4:7], s96 offen offset:3072 sc1\n"
"buffer_load_dwordx4 v[92:95], v1, s[4:7], s96 offen sc1\n"
"buffer_load_dwordx4 v[96:99], v1, s[4:7], s96 offen offset:1024 sc1\n"
"buffer_load_dwordx4 v[100:103], v1, s[4:7], s96 offen offset:2048 sc1\n"
"buffer_load_dwordx4 v[104:107], v1, s[4:7], s96 offen offset:3072 sc1\n"
"s_add_u32 s96, s58, 0x400000\n"
"buffer_load_dwordx4 v[108:111], v0, s[4:7], s96 offen sc1\n"
"buffer_load_dwordx4 v[84:87], v0, s[4:7], s96 offen offset:1024 sc1\n"
"s_waitcnt vmcnt(0)\n"
"v_max_u32_e32 v255, v140, v141\n"
"v_max3_u32 v255, v255, v142, v143\n"
"v_max3_u32 v255, v255, v44, v45\n"
"v_max3_u32 v255, v255, v46, v47\n"
"v_max3_u32 v255, v255, v48, v49\n"
"v_max3_u32 v255, v255, v50, v51\n"
"v_max3_u32 v255, v255, v52, v53\n"
"v_max3_u32 v255, v255, v54, v55\n"
"v_cmp_eq_u32_e32 vcc, -1, v255\n"
"s_cbranch_vccz .Lmk_q78k\n"
"s_sub_u32 s1, s1, 1\n"
"s_cbranch_scc0 .Lmk_q78l\n"
".Lmk_q78k:\n"
"s_branch .Lmk_q78r\n"
".Lmk_p80s:\n"
";\n"
"s_mov_b32 s1, 0x10000\n"
".Lmk_p80l:\n"
"s_add_u32 s97, s56, 0x400000\n"
"buffer_load_dwordx4 v[52:55], v0, s[4:7], s97 offen sc1\n"
"buffer_load_dwordx4 v[128:131], v0, s[4:7], s97 offen offset:1024 sc1\n"
"buffer_load_dwordx4 v[36:39], v0, s[4:7], s97 offen offset:2048 sc1\n"
"buffer_load_dwordx4 v[120:123], v0, s[4:7], s97 offen offset:3072 sc1\n"
"buffer_load_dwordx4 v[140:143], v1, s[4:7], s97 offen sc1\n"
"buffer_load_dwordx4 v[44:47], v1, s[4:7], s97 offen offset:1024 sc1\n"
"buffer_load_dwordx4 v[136:139], v1, s[4:7], s97 offen offset:2048 sc1\n"
"buffer_load_dwordx4 v[76:79], v1, s[4:7], s97 offen offset:3072 sc1\n"
"s_add_u32 s97, s57, 0x400000\n"
"buffer_load_dwordx4 v[168:171], v0, s[4:7], s97 offen sc1\n"
"buffer_load_dwordx4 v[64:67], v0, s[4:7], s97 offen offset:1024 sc1\n"
"buffer_load_dwordx4 v[68:71], v0, s[4:7], s97 offen offset:2048 sc1\n"
"buffer_load_dwordx4 v[88:91], v0, s[4:7], s97 offen offset:3072 sc1\n"
"buffer_load_dwordx4 v[92:95], v1, s[4:7], s97 offen sc1\n"
"buffer_load_dwordx4 v[96:99], v1, s[4:7], s97 offen offset:1024 sc1\n"
"buffer_load_dwordx4 v[100:103], v1, s[4:7], s97 offen offset:2048 sc1\n"
"buffer_load_dwordx4 v[104:107], v1, s[4:7], s97 offen offset:3072 sc1\n"
"s_add_u32 s97, s58, 0x400000\n"
"buffer_load_dwordx4 v[108:111], v0, s[4:7], s97 offen sc1\n"
"buffer_load_dwordx4 v[84:87], v0, s[4:7], s97 offen offset:1024 sc1\n"
"buffer_load_dwordx4 v[24:27], v0, s[4:7], s97 offen offset:2048 sc1\n"
"buffer_load_dwordx4 v[32:35], v0, s[4:7], s97 offen offset:3072 sc1\n"
"buffer_load_dwordx4 v[116:119], v1, s[4:7], s97 offen sc1\n"
"buffer_load_dwordx4 v[144:147], v1, s[4:7], s97 offen offset:1024 sc1\n"
"buffer_load_dwordx4 v[48:51], v1, s[4:7], s97 offen offset:2048 sc1\n"
"buffer_load_dwordx4 v[112:115], v1, s[4:7], s97 offen offset:3072 sc1\n"
"s_waitcnt vmcnt(0)\n"
"v_max_u32_e32 v255, v52, v53\n"
"v_max3_u32 v255, v255, v54, v55\n"
"v_max3_u32 v255, v255, v128, v129\n"
"v_max3_u32 v255, v255, v130, v131\n"
"v_max3_u32 v255, v255, v36, v37\n"
"v_max3_u32 v255, v255, v38, v39\n"
"v_max3_u32 v255, v255, v120, v121\n"
"v_max3_u32 v255, v255, v122, v123\n"
"v_max3_u32 v255, v255, v140, v141\n"
"v_max3_u32 v255, v255, v142, v143\n"
"v_max3_u32 v255, v255, v44, v45\n"
"v_max3_u32 v255, v255, v46, v47\n"
"v_max3_u32 v255, v255, v136, v137\n"
"v_max3_u32 v255, v255, v138, v139\n"
"v_max3_u32 v255, v255, v76, v77\n"
"v_max3_u32 v255, v255, v78, v79\n"
"v_cmp_eq_u32_e32 vcc, -1, v255\n"
"s_cbranch_vccz .Lmk_p80k\n"
"s_sub_u32 s1, s1, 1\n"
"s_cbranch_scc0 .Lmk_p80l\n"
".Lmk_p80k:\n"
"v_mfma_f32_16x16x32_bf16 v[132:135], a[128:131], v[52:55], 0\n"
"v_mfma_f32_16x16x32_bf16 v[148:151], a[132:135], v[52:55], 0\n"
"v_mfma_f32_16x16x32_bf16 v[152:155], a[136:139], v[52:55], 0\n"
"v_mfma_f32_16x16x32_bf16 v[160:163], a[140:143], v[52:55], 0\n"
"v_mfma_f32_16x16x32_bf16 v[132:135], a[144:147], v[128:131], v[132:135]\n"
"v_mfma_f32_16x16x32_bf16 v[148:151], a[148:151], v[128:131], v[148:151]\n"
"v_mfma_f32_16x16x32_bf16 v[152:155], a[152:155], v[128:131], v[152:155]\n"
"v_mfma_f32_16x16x32_bf16 v[160:163], a[156:159], v[128:131], v[160:163]\n"
"v_mfma_f32_16x16x32_bf16 v[132:135], a[160:163], v[36:39], v[132:135]\n"
"v_mfma_f32_16x16x32_bf16 v[148:151], a[164:167], v[36:39], v[148:151]\n"
"v_mfma_f32_16x16x32_bf16 v[152:155], a[168:171], v[36:39], v[152:155]\n"
"v_mfma_f32_16x16x32_bf16 v[160:163], a[172:175], v[36:39], v[160:163]\n"
"v_mfma_f32_16x16x32_bf16 v[132:135], a[176:179], v[120:123], v[132:135]\n"
"v_mfma_f32_16x16x32_bf16 v[148:151], a[180:183], v[120:123], v[148:151]\n"
"v_mfma_f32_16x16x32_bf16 v[152:155], a[184:187], v[120:123], v[152:155]\n"
"v_mfma_f32_16x16x32_bf16 v[160:163], a[188:191], v[120:123], v[160:163]\n"
"v_mfma_f32_16x16x32_bf16 v[132:135], a[192:195], v[140:143], v[132:135]\n"
"v_mfma_f32_16x16x32_bf16 v[148:151], a[196:199], v[140:143], v[148:151]\n"
"v_mfma_f32_16x16x32_bf16 v[152:155], a[200:203], v[140:143], v[152:155]\n"
"v_mfma_f32_16x16x32_bf16 v[160:163], a[204:207], v[140:143], v[160:163]\n"
"v_mfma_f32_16x16x32_bf16 v[132:135], a[208:211], v[44:47], v[132:135]\n"
"v_mfma_f32_16x16x32_bf16 v[148:151], a[212:215], v[44:47], v[148:151]\n"
"v_mfma_f32_16x16x32_bf16 v[152:155], a[216:219], v[44:47], v[152:155]\n"
"v_mfma_f32_16x16x32_bf16 v[160:163], a[220:223], v[44:47], v[160:163]\n"
"v_mfma_f32_16x16x32_bf16 v[132:135], a[224:227], v[136:139], v[132:135]\n"
"v_mfma_f32_16x16x32_bf16 v[148:151], a[228:231], v[136:139], v[148:151]\n"
"v_mfma_f32_16x16x32_bf16 v[152:155], a[232:235], v[136:139], v[152:155]\n"
"v_mfma_f32_16x16x32_bf16 v[160:163], a[236:239], v[136:139], v[160:163]\n"
"v_mfma_f32_16x16x32_bf16 v[132:135], a[240:243], v[76:79], v[132:135]\n"
"v_mfma_f32_16x16x32_bf16 v[148:151], a[244:247], v[76:79], v[148:151]\n"
"v_mfma_f32_16x16x32_bf16 v[152:155], a[248:251], v[76:79], v[152:155]\n"
"v_mfma_f32_16x16x32_bf16 v[160:163], a[252:255], v[76:79], v[160:163]\n"
"s_add_u32 s97, s59, 0x400000\n"
"v_mov_b32_e32 v255, 0x80000000\n"
"buffer_load_dwordx4 v[52:55], v0, s[4:7], s97 offen sc1\n"
"buffer_load_dwordx4 v[128:131], v0, s[4:7], s97 offen offset:1024 sc1\n"
"buffer_store_dword v255, v255, s[4:7], 0 offen\n"
"buffer_load_dwordx4 v[36:39], v0, s[4:7], s97 offen offset:2048 sc1\n"
"buffer_load_dwordx4 v[120:123], v0, s[4:7], s97 offen offset:3072 sc1\n"
"buffer_load_dwordx4 v[140:143], v1, s[4:7], s97 offen sc1\n"
"buffer_store_dword v255, v255, s[4:7], 0 offen\n"
"buffer_load_dwordx4 v[44:47], v1, s[4:7], s97 offen offset:1024 sc1\n"
"buffer_load_dwordx4 v[136:139], v1, s[4:7], s97 offen offset:2048 sc1\n"
"buffer_load_dwordx4 v[76:79], v1, s[4:7], s97 offen offset:3072 sc1\n"
"buffer_store_dword v255, v255, s[4:7], 0 offen\n"
"buffer_store_dword v255, v255, s[4:7], 0 offen\n"
"buffer_store_dword v255, v255, s[4:7], 0 offen\n"
"buffer_store_dword v255, v255, s[4:7], 0 offen\n"
"buffer_store_dword v255, v255, s[4:7], 0 offen\n"
"buffer_store_dword v255, v255, s[4:7], 0 offen\n"
"buffer_store_dword v255, v255, s[4:7], 0 offen\n"
"buffer_store_dword v255, v255, s[4:7], 0 offen\n"
"buffer_store_dword v255, v255, s[4:7], 0 offen\n"
"buffer_store_dword v255, v255, s[4:7], 0 offen\n"
"buffer_store_dword v255, v255, s[4:7], 0 offen\n"
"s_nop 7\n"
"s_branch .Lmk_p80r\n"
".Lmk_p81s:\n"
";\n"
"s_mov_b32 s1, 0x10000\n"
".Lmk_p81l:\n"
"s_add_u32 s97, s57, 0x400000\n"
"buffer_load_dwordx4 v[52:55], v0, s[4:7], s97 offen sc1\n"
"buffer_load_dwordx4 v[128:131], v0, s[4:7], s97 offen offset:1024 sc1\n"
"buffer_load_dwordx4 v[36:39], v0, s[4:7], s97 offen offset:2048 sc1\n"
"buffer_load_dwordx4 v[120:123], v0, s[4:7], s97 offen offset:3072 sc1\n"
"buffer_load_dwordx4 v[140:143], v1, s[4:7], s97 offen sc1\n"
"buffer_load_dwordx4 v[44:47], v1, s[4:7], s97 offen offset:1024 sc1\n"
"buffer_load_dwordx4 v[136:139], v1, s[4:7], s97 offen offset:2048 sc1\n"
"buffer_load_dwordx4 v[76:79], v1, s[4:7], s97 offen offset:3072 sc1\n"
"s_add_u32 s97, s58, 0x400000\n"
"buffer_load_dwordx4 v[108:111], v0, s[4:7], s97 offen sc1\n"
"buffer_load_dwordx4 v[84:87], v0, s[4:7], s97 offen offset:1024 sc1\n"
"buffer_load_dwordx4 v[24:27], v0, s[4:7], s97 offen offset:2048 sc1\n"
"buffer_load_dwordx4 v[32:35], v0, s[4:7], s97 offen offset:3072 sc1\n"
"buffer_load_dwordx4 v[116:119], v1, s[4:7], s97 offen sc1\n"
"buffer_load_dwordx4 v[144:147], v1, s[4:7], s97 offen offset:1024 sc1\n"
"buffer_load_dwordx4 v[48:51], v1, s[4:7], s97 offen offset:2048 sc1\n"
"buffer_load_dwordx4 v[112:115], v1, s[4:7], s97 offen offset:3072 sc1\n"
"s_waitcnt vmcnt(0)\n"
"v_max_u32_e32 v242, v52, v53\n"
"v_max3_u32 v242, v242, v54, v55\n"
"v_max3_u32 v242, v242, v128, v129\n"
"v_max3_u32 v242, v242, v130, v131\n"
"v_max3_u32 v242, v242, v36, v37\n"
"v_max3_u32 v242, v242, v38, v39\n"
"v_max3_u32 v242, v242, v120, v121\n"
"v_max3_u32 v242, v242, v122, v123\n"
"v_max3_u32 v242, v242, v140, v141\n"
"v_max3_u32 v242, v242, v142, v143\n"
"v_max3_u32 v242, v242, v44, v45\n"
"v_max3_u32 v242, v242, v46, v47\n"
"v_max3_u32 v242, v242, v136, v137\n"
"v_max3_u32 v242, v242, v138, v139\n"
"v_max3_u32 v242, v242, v76, v77\n"
"v_max3_u32 v242, v242, v78, v79\n"
"v_cmp_eq_u32_e32 vcc, -1, v242\n"
"s_cbranch_vccz .Lmk_p81k\n"
"s_sub_u32 s1, s1, 1\n"
"s_cbranch_scc0 .Lmk_p81l\n"
".Lmk_p81k:\n"
"v_mfma_f32_16x16x32_bf16 v[56:59], a[128:131], v[52:55], 0\n"
"v_mfma_f32_16x16x32_bf16 v[80:83], a[132:135], v[52:55], 0\n"
"v_mfma_f32_16x16x32_bf16 v[124:127], a[136:139], v[52:55], 0\n"
"v_mfma_f32_16x16x32_bf16 v[164:167], a[140:143], v[52:55], 0\n"
"v_mfma_f32_16x16x32_bf16 v[56:59], a[144:147], v[128:131], v[56:59]\n"
"v_mfma_f32_16x16x32_bf16 v[80:83], a[148:151], v[128:131], v[80:83]\n"
"v_mfma_f32_16x16x32_bf16 v[124:127], a[152:155], v[128:131], v[124:127]\n"
"v_mfma_f32_16x16x32_bf16 v[164:167], a[156:159], v[128:131], v[164:167]\n"
"v_mfma_f32_16x16x32_bf16 v[56:59], a[160:163], v[36:39], v[56:59]\n"
"v_mfma_f32_16x16x32_bf16 v[80:83], a[164:167], v[36:39], v[80:83]\n"
"v_mfma_f32_16x16x32_bf16 v[124:127], a[168:171], v[36:39], v[124:127]\n"
"v_mfma_f32_16x16x32_bf16 v[164:167], a[172:175], v[36:39], v[164:167]\n"
"v_mfma_f32_16x16x32_bf16 v[56:59], a[176:179], v[120:123], v[56:59]\n"
"v_mfma_f32_16x16x32_bf16 v[80:83], a[180:183], v[120:123], v[80:83]\n"
"v_mfma_f32_16x16x32_bf16 v[124:127], a[184:187], v[120:123], v[124:127]\n"
"v_mfma_f32_16x16x32_bf16 v[164:167], a[188:191], v[120:123], v[164:167]\n"
"v_mfma_f32_16x16x32_bf16 v[56:59], a[192:195], v[140:143], v[56:59]\n"
"v_mfma_f32_16x16x32_bf16 v[80:83], a[196:199], v[140:143], v[80:83]\n"
"v_mfma_f32_16x16x32_bf16 v[124:127], a[200:203], v[140:143], v[124:127]\n"
"v_mfma_f32_16x16x32_bf16 v[164:167], a[204:207], v[140:143], v[164:167]\n"
"v_mfma_f32_16x16x32_bf16 v[56:59], a[208:211], v[44:47], v[56:59]\n"
"v_mfma_f32_16x16x32_bf16 v[80:83], a[212:215], v[44:47], v[80:83]\n"
"v_mfma_f32_16x16x32_bf16 v[124:127], a[216:219], v[44:47], v[124:127]\n"
"v_mfma_f32_16x16x32_bf16 v[164:167], a[220:223], v[44:47], v[164:167]\n"
"v_mfma_f32_16x16x32_bf16 v[56:59], a[224:227], v[136:139], v[56:59]\n"
"v_mfma_f32_16x16x32_bf16 v[80:83], a[228:231], v[136:139], v[80:83]\n"
"v_mfma_f32_16x16x32_bf16 v[124:127], a[232:235], v[136:139], v[124:127]\n"
"v_mfma_f32_16x16x32_bf16 v[164:167], a[236:239], v[136:139], v[164:167]\n"
"v_mfma_f32_16x16x32_bf16 v[56:59], a[240:243], v[76:79], v[56:59]\n"
"v_mfma_f32_16x16x32_bf16 v[80:83], a[244:247], v[76:79], v[80:83]\n"
"v_mfma_f32_16x16x32_bf16 v[124:127], a[248:251], v[76:79], v[124:127]\n"
"v_mfma_f32_16x16x32_bf16 v[164:167], a[252:255], v[76:79], v[164:167]\n"
"s_add_u32 s97, s59, 0x400000\n"
"v_mov_b32_e32 v242, 0x80000000\n"
"buffer_load_dwordx4 v[52:55], v0, s[4:7], s97 offen sc1\n"
"buffer_load_dwordx4 v[128:131], v0, s[4:7], s97 offen offset:1024 sc1\n"
"buffer_store_dword v242, v242, s[4:7], 0 offen\n"
"buffer_load_dwordx4 v[36:39], v0, s[4:7], s97 offen offset:2048 sc1\n"
"buffer_load_dwordx4 v[120:123], v0, s[4:7], s97 offen offset:3072 sc1\n"
"buffer_load_dwordx4 v[140:143], v1, s[4:7], s97 offen sc1\n"
"buffer_store_dword v242, v242, s[4:7], 0 offen\n"
"buffer_load_dwordx4 v[44:47], v1, s[4:7], s97 offen offset:1024 sc1\n"
"buffer_load_dwordx4 v[136:139], v1, s[4:7], s97 offen offset:2048 sc1\n"
"buffer_load_dwordx4 v[76:79], v1, s[4:7], s97 offen offset:3072 sc1\n"
"buffer_store_dword v242, v242, s[4:7], 0 offen\n"
"buffer_store_dword v242, v242, s[4:7], 0 offen\n"
"buffer_store_dword v242, v242, s[4:7], 0 offen\n"
"buffer_store_dword v242, v242, s[4:7], 0 offen\n"
"buffer_store_dword v242, v242, s[4:7], 0 offen\n"
"buffer_store_dword v242, v242, s[4:7], 0 offen\n"
"buffer_store_dword v242, v242, s[4:7], 0 offen\n"
"buffer_store_dword v242, v242, s[4:7], 0 offen\n"
"buffer_store_dword v242, v242, s[4:7], 0 offen\n"
"buffer_store_dword v242, v242, s[4:7], 0 offen\n"
"buffer_store_dword v242, v242, s[4:7], 0 offen\n"
"buffer_store_dword v242, v242, s[4:7], 0 offen\n"
"buffer_store_dword v242, v242, s[4:7], 0 offen\n"
"buffer_store_dword v242, v242, s[4:7], 0 offen\n"
"buffer_store_dword v242, v242, s[4:7], 0 offen\n"
"buffer_store_dword v242, v242, s[4:7], 0 offen\n"
"buffer_store_dword v242, v242, s[4:7], 0 offen\n"
"buffer_store_dword v242, v242, s[4:7], 0 offen\n"
"buffer_store_dword v242, v242, s[4:7], 0 offen\n"
"buffer_store_dword v242, v242, s[4:7], 0 offen\n"
"buffer_store_dword v242, v242, s[4:7], 0 offen\n"
"s_nop 7\n"
"s_branch .Lmk_p81r\n"
".Lmk_q83s:\n"
";\n"
"s_mov_b32 s1, 0x10000\n"
".Lmk_q83l:\n"
"s_add_u32 s97, s59, 0x400000\n"
"buffer_load_dwordx4 v[52:55], v0, s[4:7], s97 offen sc1\n"
"buffer_load_dwordx4 v[128:131], v0, s[4:7], s97 offen offset:1024 sc1\n"
"buffer_load_dwordx4 v[36:39], v0, s[4:7], s97 offen offset:2048 sc1\n"
"buffer_load_dwordx4 v[120:123], v0, s[4:7], s97 offen offset:3072 sc1\n"
"buffer_load_dwordx4 v[140:143], v1, s[4:7], s97 offen sc1\n"
"buffer_load_dwordx4 v[44:47], v1, s[4:7], s97 offen offset:1024 sc1\n"
"buffer_load_dwordx4 v[136:139], v1, s[4:7], s97 offen offset:2048 sc1\n"
"buffer_load_dwordx4 v[76:79], v1, s[4:7], s97 offen offset:3072 sc1\n"
"s_add_u32 s97, s52, 0x840000\n"
"buffer_load_dwordx4 v[24:27], v0, s[8:11], s97 offen sc1\n"
"buffer_load_dwordx4 v[32:35], v0, s[8:11], s97 offen offset:1024 sc1\n"
"s_waitcnt vmcnt(0)\n"
"v_max_u32_e32 v255, v52, v53\n"
"v_max3_u32 v255, v255, v54, v55\n"
"v_max3_u32 v255, v255, v128, v129\n"
"v_max3_u32 v255, v255, v130, v131\n"
"v_max3_u32 v255, v255, v36, v37\n"
"v_max3_u32 v255, v255, v38, v39\n"
"v_max3_u32 v255, v255, v120, v121\n"
"v_max3_u32 v255, v255, v122, v123\n"
"v_cmp_eq_u32_e32 vcc, -1, v255\n"
"s_cbranch_vccz .Lmk_q83k\n"
"s_sub_u32 s1, s1, 1\n"
"s_cbranch_scc0 .Lmk_q83l\n"
".Lmk_q83k:\n"
"s_branch .Lmk_q83r\n"
".Lmk_p82s:\n"
";\n"
"s_mov_b32 s1, 0x10000\n"
".Lmk_p82l:\n"
"s_add_u32 s97, s58, 0x400000\n"
"buffer_load_dwordx4 v[80:83], v0, s[4:7], s97 offen sc1\n"
"buffer_load_dwordx4 v[84:87], v0, s[4:7], s97 offen offset:1024 sc1\n"
"buffer_load_dwordx4 v[108:111], v0, s[4:7], s97 offen offset:2048 sc1\n"
"buffer_load_dwordx4 v[112:115], v0, s[4:7], s97 offen offset:3072 sc1\n"
"buffer_load_dwordx4 v[116:119], v1, s[4:7], s97 offen sc1\n"
"buffer_load_dwordx4 v[124:127], v1, s[4:7], s97 offen offset:1024 sc1\n"
"buffer_load_dwordx4 v[144:147], v1, s[4:7], s97 offen offset:2048 sc1\n"
"buffer_load_dwordx4 v[152:155], v1, s[4:7], s97 offen offset:3072 sc1\n"
"s_add_u32 s97, s59, 0x400000\n"
"buffer_load_dwordx4 v[52:55], v0, s[4:7], s97 offen sc1\n"
"buffer_load_dwordx4 v[128:131], v0, s[4:7], s97 offen offset:1024 sc1\n"
"buffer_load_dwordx4 v[36:39], v0, s[4:7], s97 offen offset:2048 sc1\n"
"buffer_load_dwordx4 v[120:123], v0, s[4:7], s97 offen offset:3072 sc1\n"
"buffer_load_dwordx4 v[140:143], v1, s[4:7], s97 offen sc1\n"
"buffer_load_dwordx4 v[44:47], v1, s[4:7], s97 offen offset:1024 sc1\n"
"buffer_load_dwordx4 v[136:139], v1, s[4:7], s97 offen offset:2048 sc1\n"
"buffer_load_dwordx4 v[76:79], v1, s[4:7], s97 offen offset:3072 sc1\n"
"s_add_u32 s97, s52, 0x840000\n"
"buffer_load_dwordx4 v[24:27], v0, s[8:11], s97 offen sc1\n"
"buffer_load_dwordx4 v[32:35], v0, s[8:11], s97 offen offset:1024 sc1\n"
"buffer_load_dwordx4 v[48:51], v0, s[8:11], s97 offen offset:2048 sc1\n"
"s_waitcnt vmcnt(0)\n"
"v_max_u32_e32 v255, v80, v81\n"
"v_max3_u32 v255, v255, v82, v83\n"
"v_max3_u32 v255, v255, v84, v85\n"
"v_max3_u32 v255, v255, v86, v87\n"
"v_max3_u32 v255, v255, v108, v109\n"
"v_max3_u32 v255, v255, v110, v111\n"
"v_max3_u32 v255, v255, v112, v113\n"
"v_max3_u32 v255, v255, v114, v115\n"
"v_max3_u32 v255, v255, v116, v117\n"
"v_max3_u32 v255, v255, v118, v119\n"
"v_max3_u32 v255, v255, v124, v125\n"
"v_max3_u32 v255, v255, v126, v127\n"
"v_max3_u32 v255, v255, v144, v145\n"
"v_max3_u32 v255, v255, v146, v147\n"
"v_max3_u32 v255, v255, v152, v153\n"
"v_max3_u32 v255, v255, v154, v155\n"
"v_cmp_eq_u32_e32 vcc, -1, v255\n"
"s_cbranch_vccz .Lmk_p82k\n"
"s_sub_u32 s1, s1, 1\n"
"s_cbranch_scc0 .Lmk_p82l\n"
".Lmk_p82k:\n"
"v_mfma_f32_16x16x32_bf16 v[104:107], a[128:131], v[80:83], 0\n"
"v_mfma_f32_16x16x32_bf16 v[184:187], a[132:135], v[80:83], 0\n"
"v_mfma_f32_16x16x32_bf16 v[188:191], a[136:139], v[80:83], 0\n"
"v_mfma_f32_16x16x32_bf16 v[196:199], a[140:143], v[80:83], 0\n"
"v_mfma_f32_16x16x32_bf16 v[104:107], a[144:147], v[84:87], v[104:107]\n"
"v_mfma_f32_16x16x32_bf16 v[184:187], a[148:151], v[84:87], v[184:187]\n"
"v_mfma_f32_16x16x32_bf16 v[188:191], a[152:155], v[84:87], v[188:191]\n"
"v_mfma_f32_16x16x32_bf16 v[196:199], a[156:159], v[84:87], v[196:199]\n"
"v_mfma_f32_16x16x32_bf16 v[104:107], a[160:163], v[108:111], v[104:107]\n"
"v_mfma_f32_16x16x32_bf16 v[184:187], a[164:167], v[108:111], v[184:187]\n"
"v_mfma_f32_16x16x32_bf16 v[188:191], a[168:171], v[108:111], v[188:191]\n"
"v_mfma_f32_16x16x32_bf16 v[196:199], a[172:175], v[108:111], v[196:199]\n"
"v_mfma_f32_16x16x32_bf16 v[104:107], a[176:179], v[112:115], v[104:107]\n"
"v_mfma_f32_16x16x32_bf16 v[184:187], a[180:183], v[112:115], v[184:187]\n"
"v_mfma_f32_16x16x32_bf16 v[188:191], a[184:187], v[112:115], v[188:191]\n"
"v_mfma_f32_16x16x32_bf16 v[196:199], a[188:191], v[112:115], v[196:199]\n"
"v_mfma_f32_16x16x32_bf16 v[104:107], a[192:195], v[116:119], v[104:107]\n"
"v_mfma_f32_16x16x32_bf16 v[184:187], a[196:199], v[116:119], v[184:187]\n"
"v_mfma_f32_16x16x32_bf16 v[188:191], a[200:203], v[116:119], v[188:191]\n"
"v_mfma_f32_16x16x32_bf16 v[196:199], a[204:207], v[116:119], v[196:199]\n"
"v_mfma_f32_16x16x32_bf16 v[104:107], a[208:211], v[124:127], v[104:107]\n"
"v_mfma_f32_16x16x32_bf16 v[184:187], a[212:215], v[124:127], v[184:187]\n"
"v_mfma_f32_16x16x32_bf16 v[188:191], a[216:219], v[124:127], v[188:191]\n"
"v_mfma_f32_16x16x32_bf16 v[196:199], a[220:223], v[124:127], v[196:199]\n"
"v_mfma_f32_16x16x32_bf16 v[104:107], a[224:227], v[144:147], v[104:107]\n"
"v_mfma_f32_16x16x32_bf16 v[184:187], a[228:231], v[144:147], v[184:187]\n"
"v_mfma_f32_16x16x32_bf16 v[188:191], a[232:235], v[144:147], v[188:191]\n"
"v_mfma_f32_16x16x32_bf16 v[196:199], a[236:239], v[144:147], v[196:199]\n"
"v_mfma_f32_16x16x32_bf16 v[104:107], a[240:243], v[152:155], v[104:107]\n"
"v_mfma_f32_16x16x32_bf16 v[184:187], a[244:247], v[152:155], v[184:187]\n"
"v_mfma_f32_16x16x32_bf16 v[188:191], a[248:251], v[152:155], v[188:191]\n"
"v_mfma_f32_16x16x32_bf16 v[196:199], a[252:255], v[152:155], v[196:199]\n"
"s_nop 7\n"
"s_branch .Lmk_p82r\n"
".Lmk_q84s:\n"
";\n"
"s_mov_b32 s1, 0x10000\n"
".Lmk_q84l:\n"
"s_add_u32 s97, s59, 0x400000\n"
"buffer_load_dwordx4 v[140:143], v1, s[4:7], s97 offen sc1\n"
"buffer_load_dwordx4 v[44:47], v1, s[4:7], s97 offen offset:1024 sc1\n"
"buffer_load_dwordx4 v[136:139], v1, s[4:7], s97 offen offset:2048 sc1\n"
"buffer_load_dwordx4 v[76:79], v1, s[4:7], s97 offen offset:3072 sc1\n"
"s_add_u32 s97, s52, 0x840000\n"
"buffer_load_dwordx4 v[24:27], v0, s[8:11], s97 offen sc1\n"
"buffer_load_dwordx4 v[32:35], v0, s[8:11], s97 offen offset:1024 sc1\n"
"buffer_load_dwordx4 v[48:51], v0, s[8:11], s97 offen offset:2048 sc1\n"
"buffer_load_dwordx4 v[80:83], v0, s[8:11], s97 offen offset:3072 sc1\n"
"buffer_load_dwordx4 v[52:55], v1, s[8:11], s97 offen sc1\n"
"buffer_load_dwordx4 v[84:87], v1, s[8:11], s97 offen offset:1024 sc1\n"
"s_waitcnt vmcnt(0)\n"
"v_max_u32_e32 v255, v140, v141\n"
"v_max3_u32 v255, v255, v142, v143\n"
"v_max3_u32 v255, v255, v44, v45\n"
"v_max3_u32 v255, v255, v46, v47\n"
"v_max3_u32 v255, v255, v136, v137\n"
"v_max3_u32 v255, v255, v138, v139\n"
"v_max3_u32 v255, v255, v76, v77\n"
"v_max3_u32 v255, v255, v78, v79\n"
"v_cmp_eq_u32_e32 vcc, -1, v255\n"
"s_cbranch_vccz .Lmk_q84k\n"
"s_sub_u32 s1, s1, 1\n"
"s_cbranch_scc0 .Lmk_q84l\n"
".Lmk_q84k:\n"
"s_branch .Lmk_q84r\n"
".Lmk_p89s:\n"
";\n"
"s_mov_b32 s1, 0x10000\n"
".Lmk_p89l:\n"
"s_add_u32 s98, s52, 0x40000\n"
"buffer_load_dwordx4 v[204:207], v0, s[4:7], s98 offen sc1\n"
"buffer_load_dwordx4 v[212:215], v0, s[4:7], s98 offen offset:1024 sc1\n"
"buffer_load_dwordx4 v[32:35], v0, s[4:7], s98 offen offset:2048 sc1\n"
"buffer_load_dwordx4 v[44:47], v0, s[4:7], s98 offen offset:3072 sc1\n"
"buffer_load_dwordx4 v[80:83], v1, s[4:7], s98 offen sc1\n"
"buffer_load_dwordx4 v[96:99], v1, s[4:7], s98 offen offset:1024 sc1\n"
"buffer_load_dwordx4 v[24:27], v1, s[4:7], s98 offen offset:2048 sc1\n"
"buffer_load_dwordx4 v[36:39], v1, s[4:7], s98 offen offset:3072 sc1\n"
"s_add_u32 s98, s53, 0x40000\n"
"buffer_load_dwordx4 v[108:111], v0, s[4:7], s98 offen sc1\n"
"buffer_load_dwordx4 v[120:123], v0, s[4:7], s98 offen offset:1024 sc1\n"
"buffer_load_dwordx4 v[128:131], v0, s[4:7], s98 offen offset:2048 sc1\n"
"buffer_load_dwordx4 v[152:155], v0, s[4:7], s98 offen offset:3072 sc1\n"
"buffer_load_dwordx4 v[184:187], v1, s[4:7], s98 offen sc1\n"
"buffer_load_dwordx4 v[188:191], v1, s[4:7], s98 offen offset:1024 sc1\n"
"buffer_load_dwordx4 v[76:79], v1, s[4:7], s98 offen offset:2048 sc1\n"
"buffer_load_dwordx4 v[116:119], v1, s[4:7], s98 offen offset:3072 sc1\n"
"s_waitcnt vmcnt(0)\n"
"v_max_u32_e32 v247, v204, v205\n"
"v_max3_u32 v247, v247, v206, v207\n"
"v_max3_u32 v247, v247, v212, v213\n"
"v_max3_u32 v247, v247, v214, v215\n"
"v_max3_u32 v247, v247, v32, v33\n"
"v_max3_u32 v247, v247, v34, v35\n"
"v_max3_u32 v247, v247, v44, v45\n"
"v_max3_u32 v247, v247, v46, v47\n"
"v_max3_u32 v247, v247, v80, v81\n"
"v_max3_u32 v247, v247, v82, v83\n"
"v_max3_u32 v247, v247, v96, v97\n"
"v_max3_u32 v247, v247, v98, v99\n"
"v_max3_u32 v247, v247, v24, v25\n"
"v_max3_u32 v247, v247, v26, v27\n"
"v_max3_u32 v247, v247, v36, v37\n"
"v_max3_u32 v247, v247, v38, v39\n"
"v_cmp_eq_u32_e32 vcc, -1, v247\n"
"s_cbranch_vccz .Lmk_p89k\n"
"s_sub_u32 s1, s1, 1\n"
"s_cbranch_scc0 .Lmk_p89l\n"
".Lmk_p89k:\n"
"v_mfma_f32_16x16x32_bf16 v[124:127], a[0:3], v[204:207], 0\n"
"v_mfma_f32_16x16x32_bf16 v[144:147], a[4:7], v[204:207], 0\n"
"v_mfma_f32_16x16x32_bf16 v[164:167], a[8:11], v[204:207], 0\n"
"v_mfma_f32_16x16x32_bf16 v[196:199], a[12:15], v[204:207], 0\n"
"v_mfma_f32_16x16x32_bf16 v[200:203], a[16:19], v[204:207], 0\n"
"v_mfma_f32_16x16x32_bf16 v[208:211], a[20:23], v[204:207], 0\n"
"v_mfma_f32_16x16x32_bf16 v[124:127], a[24:27], v[212:215], v[124:127]\n"
"v_mfma_f32_16x16x32_bf16 v[144:147], a[28:31], v[212:215], v[144:147]\n"
"v_mfma_f32_16x16x32_bf16 v[164:167], a[32:35], v[212:215], v[164:167]\n"
"v_mfma_f32_16x16x32_bf16 v[196:199], a[36:39], v[212:215], v[196:199]\n"
"v_mfma_f32_16x16x32_bf16 v[200:203], a[40:43], v[212:215], v[200:203]\n"
"v_mfma_f32_16x16x32_bf16 v[208:211], a[44:47], v[212:215], v[208:211]\n"
"v_mfma_f32_16x16x32_bf16 v[124:127], a[48:51], v[32:35], v[124:127]\n"
"v_mfma_f32_16x16x32_bf16 v[144:147], a[52:55], v[32:35], v[144:147]\n"
"v_mfma_f32_16x16x32_bf16 v[164:167], a[56:59], v[32:35], v[164:167]\n"
"v_mfma_f32_16x16x32_bf16 v[196:199], a[60:63], v[32:35], v[196:199]\n"
"v_mfma_f32_16x16x32_bf16 v[200:203], a[64:67], v[32:35], v[200:203]\n"
"v_mfma_f32_16x16x32_bf16 v[208:211], a[68:71], v[32:35], v[208:211]\n"
"v_mfma_f32_16x16x32_bf16 v[124:127], a[72:75], v[44:47], v[124:127]\n"
"v_mfma_f32_16x16x32_bf16 v[144:147], a[76:79], v[44:47], v[144:147]\n"
"v_mfma_f32_16x16x32_bf16 v[164:167], a[80:83], v[44:47], v[164:167]\n"
"v_mfma_f32_16x16x32_bf16 v[196:199], a[84:87], v[44:47], v[196:199]\n"
"v_mfma_f32_16x16x32_bf16 v[200:203], a[88:91], v[44:47], v[200:203]\n"
"v_mfma_f32_16x16x32_bf16 v[208:211], a[92:95], v[44:47], v[208:211]\n"
"v_mfma_f32_16x16x32_bf16 v[124:127], a[96:99], v[80:83], v[124:127]\n"
"v_mfma_f32_16x16x32_bf16 v[144:147], a[100:103], v[80:83], v[144:147]\n"
"v_mfma_f32_16x16x32_bf16 v[164:167], a[104:107], v[80:83], v[164:167]\n"
"v_mfma_f32_16x16x32_bf16 v[196:199], a[108:111], v[80:83], v[196:199]\n"
"v_mfma_f32_16x16x32_bf16 v[200:203], a[112:115], v[80:83], v[200:203]\n"
"v_mfma_f32_16x16x32_bf16 v[208:211], a[116:119], v[80:83], v[208:211]\n"
"v_mfma_f32_16x16x32_bf16 v[124:127], a[120:123], v[96:99], v[124:127]\n"
"v_mfma_f32_16x16x32_bf16 v[144:147], a[124:127], v[96:99], v[144:147]\n"
"v_mfma_f32_16x16x32_bf16 v[164:167], v[28:31], v[96:99], v[164:167]\n"
"v_mfma_f32_16x16x32_bf16 v[196:199], v[40:43], v[96:99], v[196:199]\n"
"v_mfma_f32_16x16x32_bf16 v[200:203], v[60:63], v[96:99], v[200:203]\n"
"v_mfma_f32_16x16x32_bf16 v[208:211], v[72:75], v[96:99], v[208:211]\n"
"v_mfma_f32_16x16x32_bf16 v[124:127], v[156:159], v[24:27], v[124:127]\n"
"v_mfma_f32_16x16x32_bf16 v[144:147], v[132:135], v[24:27], v[144:147]\n"
"v_mfma_f32_16x16x32_bf16 v[164:167], v[64:67], v[24:27], v[164:167]\n"
"v_mfma_f32_16x16x32_bf16 v[196:199], v[68:71], v[24:27], v[196:199]\n"
"v_mfma_f32_16x16x32_bf16 v[200:203], v[88:91], v[24:27], v[200:203]\n"
"v_mfma_f32_16x16x32_bf16 v[208:211], v[160:163], v[24:27], v[208:211]\n"
"v_mfma_f32_16x16x32_bf16 v[124:127], v[92:95], v[36:39], v[124:127]\n"
"v_mfma_f32_16x16x32_bf16 v[144:147], v[180:183], v[36:39], v[144:147]\n"
"v_mfma_f32_16x16x32_bf16 v[164:167], v[100:103], v[36:39], v[164:167]\n"
"v_mfma_f32_16x16x32_bf16 v[196:199], v[148:151], v[36:39], v[196:199]\n"
"v_mfma_f32_16x16x32_bf16 v[200:203], v[192:195], v[36:39], v[200:203]\n"
"v_mfma_f32_16x16x32_bf16 v[208:211], v[56:59], v[36:39], v[208:211]\n"
"s_add_u32 s98, s54, 0x40000\n"
"v_mov_b32_e32 v247, 0x80000000\n"
"buffer_load_dwordx4 v[204:207], v0, s[4:7], s98 offen sc1\n"
"buffer_store_dword v247, v247, s[4:7], 0 offen\n"
"buffer_store_dword v247, v247, s[4:7], 0 offen\n"
"buffer_load_dwordx4 v[212:215], v0, s[4:7], s98 offen offset:1024 sc1\n"
"buffer_store_dword v247, v247, s[4:7], 0 offen\n"
"buffer_store_dword v247, v247, s[4:7], 0 offen\n"
"buffer_load_dwordx4 v[32:35], v0, s[4:7], s98 offen offset:2048 sc1\n"
"buffer_store_dword v247, v247, s[4:7], 0 offen\n"
"buffer_store_dword v247, v247, s[4:7], 0 offen\n"
"buffer_store_dword v247, v247, s[4:7], 0 offen\n"
"buffer_load_dwordx4 v[44:47], v0, s[4:7], s98 offen offset:3072 sc1\n"
"buffer_store_dword v247, v247, s[4:7], 0 offen\n"
"buffer_store_dword v247, v247, s[4:7], 0 offen\n"
"buffer_load_dwordx4 v[80:83], v1, s[4:7], s98 offen sc1\n"
"buffer_store_dword v247, v247, s[4:7], 0 offen\n"
"buffer_load_dwordx4 v[96:99], v1, s[4:7], s98 offen offset:1024 sc1\n"
"buffer_store_dword v247, v247, s[4:7], 0 offen\n"
"buffer_load_dwordx4 v[24:27], v1, s[4:7], s98 offen offset:2048 sc1\n"
"buffer_load_dwordx4 v[36:39], v1, s[4:7], s98 offen offset:3072 sc1\n"
"s_nop 7\n"
"s_branch .Lmk_p89r\n"
".Lmk_m94s:\n"
";\n"
"s_mov_b32 s1, 0x10000\n"
".Lmk_m94l:\n"
"s_add_u32 s97, s62, 0x4000\n"
"buffer_load_dword v255, v10, s[4:7], s97 offen sc1\n"
"buffer_load_dword v254, v10, s[4:7], s97 offen offset:256 sc1\n"
"buffer_load_dword v253, v10, s[4:7], s97 offen offset:512 sc1\n"
"buffer_load_dword v252, v10, s[4:7], s97 offen offset:768 sc1\n"
"buffer_load_dword v251, v10, s[4:7], s97 offen offset:1024 sc1\n"
"buffer_load_dword v250, v10, s[4:7], s97 offen offset:1280 sc1\n"
"buffer_load_dword v249, v10, s[4:7], s97 offen offset:1536 sc1\n"
"buffer_load_dword v248, v10, s[4:7], s97 offen offset:1792 sc1\n"
"s_waitcnt vmcnt(0)\n"
"v_max_u32_e32 v247, v255, v254\n"
"v_max3_u32 v247, v247, v253, v252\n"
"v_max3_u32 v247, v247, v251, v250\n"
"v_max3_u32 v247, v247, v249, v248\n"
"v_cmp_eq_u32_e32 vcc, -1, v247\n"
"s_cbranch_vccz .Lmk_m94k\n"
"s_sub_u32 s1, s1, 1\n"
"s_cbranch_scc0 .Lmk_m94l\n"
".Lmk_m94k:\n"
"v_add_f32_e32 v255, v255, v251\n"
"v_add_f32_e32 v254, v254, v250\n"
"v_add_f32_e32 v253, v253, v249\n"
"v_add_f32_e32 v252, v252, v248\n"
"v_add_f32_e32 v255, v255, v253\n"
"v_add_f32_e32 v254, v254, v252\n"
"v_add_f32_e32 v255, v255, v254\n"
"s_nop 1\n"
"s_branch .Lmk_m94r\n"
".Lmk_p90s:\n"
";\n"
"s_mov_b32 s1, 0x10000\n"
".Lmk_p90l:\n"
"s_add_u32 s98, s53, 0x40000\n"
"buffer_load_dwordx4 v[136:139], v0, s[4:7], s98 offen sc1\n"
"buffer_load_dwordx4 v[108:111], v0, s[4:7], s98 offen offset:1024 sc1\n"
"buffer_load_dwordx4 v[120:123], v0, s[4:7], s98 offen offset:2048 sc1\n"
"buffer_load_dwordx4 v[128:131], v0, s[4:7], s98 offen offset:3072 sc1\n"
"buffer_load_dwordx4 v[144:147], v1, s[4:7], s98 offen sc1\n"
"buffer_load_dwordx4 v[176:179], v1, s[4:7], s98 offen offset:1024 sc1\n"
"buffer_load_dwordx4 v[196:199], v1, s[4:7], s98 offen offset:2048 sc1\n"
"buffer_load_dwordx4 v[76:79], v1, s[4:7], s98 offen offset:3072 sc1\n"
"s_add_u32 s98, s54, 0x40000\n"
"buffer_load_dwordx4 v[204:207], v0, s[4:7], s98 offen sc1\n"
"buffer_load_dwordx4 v[212:215], v0, s[4:7], s98 offen offset:1024 sc1\n"
"buffer_load_dwordx4 v[32:35], v0, s[4:7], s98 offen offset:2048 sc1\n"
"buffer_load_dwordx4 v[44:47], v0, s[4:7], s98 offen offset:3072 sc1\n"
"buffer_load_dwordx4 v[80:83], v1, s[4:7], s98 offen sc1\n"
"buffer_load_dwordx4 v[96:99], v1, s[4:7], s98 offen offset:1024 sc1\n"
"buffer_load_dwordx4 v[24:27], v1, s[4:7], s98 offen offset:2048 sc1\n"
"buffer_load_dwordx4 v[36:39], v1, s[4:7], s98 offen offset:3072 sc1\n"
"s_waitcnt vmcnt(0)\n"
"v_max_u32_e32 v255, v136, v137\n"
"v_max3_u32 v255, v255, v138, v139\n"
"v_max3_u32 v255, v255, v108, v109\n"
"v_max3_u32 v255, v255, v110, v111\n"
"v_max3_u32 v255, v255, v120, v121\n"
"v_max3_u32 v255, v255, v122, v123\n"
"v_max3_u32 v255, v255, v128, v129\n"
"v_max3_u32 v255, v255, v130, v131\n"
"v_max3_u32 v255, v255, v144, v145\n"
"v_max3_u32 v255, v255, v146, v147\n"
"v_max3_u32 v255, v255, v176, v177\n"
"v_max3_u32 v255, v255, v178, v179\n"
"v_max3_u32 v255, v255, v196, v197\n"
"v_max3_u32 v255, v255, v198, v199\n"
"v_max3_u32 v255, v255, v76, v77\n"
"v_max3_u32 v255, v255, v78, v79\n"
"v_cmp_eq_u32_e32 vcc, -1, v255\n"
"s_cbranch_vccz .Lmk_p90k\n"
"s_sub_u32 s1, s1, 1\n"
"s_cbranch_scc0 .Lmk_p90l\n"
".Lmk_p90k:\n"
"v_mfma_f32_16x16x32_bf16 v[48:51], a[0:3], v[136:139], 0\n"
"v_mfma_f32_16x16x32_bf16 v[84:87], a[4:7], v[136:139], 0\n"
"v_mfma_f32_16x16x32_bf16 v[104:107], a[8:11], v[136:139], 0\n"
"v_mfma_f32_16x16x32_bf16 v[112:115], a[12:15], v[136:139], 0\n"
"v_mfma_f32_16x16x32_bf16 v[124:127], a[16:19], v[136:139], 0\n"
"v_mfma_f32_16x16x32_bf16 v[140:143], a[20:23], v[136:139], 0\n"
"v_mfma_f32_16x16x32_bf16 v[48:51], a[24:27], v[108:111], v[48:51]\n"
"v_mfma_f32_16x16x32_bf16 v[84:87], a[28:31], v[108:111], v[84:87]\n"
"v_mfma_f32_16x16x32_bf16 v[104:107], a[32:35], v[108:111], v[104:107]\n"
"v_mfma_f32_16x16x32_bf16 v[112:115], a[36:39], v[108:111], v[112:115]\n"
"v_mfma_f32_16x16x32_bf16 v[124:127], a[40:43], v[108:111], v[124:127]\n"
"v_mfma_f32_16x16x32_bf16 v[140:143], a[44:47], v[108:111], v[140:143]\n"
"v_mfma_f32_16x16x32_bf16 v[48:51], a[48:51], v[120:123], v[48:51]\n"
"v_mfma_f32_16x16x32_bf16 v[84:87], a[52:55], v[120:123], v[84:87]\n"
"v_mfma_f32_16x16x32_bf16 v[104:107], a[56:59], v[120:123], v[104:107]\n"
"v_mfma_f32_16x16x32_bf16 v[112:115], a[60:63], v[120:123], v[112:115]\n"
"v_mfma_f32_16x16x32_bf16 v[124:127], a[64:67], v[120:123], v[124:127]\n"
"v_mfma_f32_16x16x32_bf16 v[140:143], a[68:71], v[120:123], v[140:143]\n"
"v_mfma_f32_16x16x32_bf16 v[48:51], a[72:75], v[128:131], v[48:51]\n"
"v_mfma_f32_16x16x32_bf16 v[84:87], a[76:79], v[128:131], v[84:87]\n"
"v_mfma_f32_16x16x32_bf16 v[104:107], a[80:83], v[128:131], v[104:107]\n"
"v_mfma_f32_16x16x32_bf16 v[112:115], a[84:87], v[128:131], v[112:115]\n"
"v_mfma_f32_16x16x32_bf16 v[124:127], a[88:91], v[128:131], v[124:127]\n"
"v_mfma_f32_16x16x32_bf16 v[140:143], a[92:95], v[128:131], v[140:143]\n"
"v_mfma_f32_16x16x32_bf16 v[48:51], a[96:99], v[144:147], v[48:51]\n"
"v_mfma_f32_16x16x32_bf16 v[84:87], a[100:103], v[144:147], v[84:87]\n"
"v_mfma_f32_16x16x32_bf16 v[104:107], a[104:107], v[144:147], v[104:107]\n"
"v_mfma_f32_16x16x32_bf16 v[112:115], a[108:111], v[144:147], v[112:115]\n"
"v_mfma_f32_16x16x32_bf16 v[124:127], a[112:115], v[144:147], v[124:127]\n"
"v_mfma_f32_16x16x32_bf16 v[140:143], a[116:119], v[144:147], v[140:143]\n"
"v_mfma_f32_16x16x32_bf16 v[48:51], a[120:123], v[176:179], v[48:51]\n"
"v_mfma_f32_16x16x32_bf16 v[84:87], a[124:127], v[176:179], v[84:87]\n"
"v_mfma_f32_16x16x32_bf16 v[104:107], v[28:31], v[176:179], v[104:107]\n"
"v_mfma_f32_16x16x32_bf16 v[112:115], v[40:43], v[176:179], v[112:115]\n"
"v_mfma_f32_16x16x32_bf16 v[124:127], v[60:63], v[176:179], v[124:127]\n"
"v_mfma_f32_16x16x32_bf16 v[140:143], v[72:75], v[176:179], v[140:143]\n"
"v_mfma_f32_16x16x32_bf16 v[48:51], v[156:159], v[196:199], v[48:51]\n"
"v_mfma_f32_16x16x32_bf16 v[84:87], v[132:135], v[196:199], v[84:87]\n"
"v_mfma_f32_16x16x32_bf16 v[104:107], v[64:67], v[196:199], v[104:107]\n"
"v_mfma_f32_16x16x32_bf16 v[112:115], v[68:71], v[196:199], v[112:115]\n"
"v_mfma_f32_16x16x32_bf16 v[124:127], v[88:91], v[196:199], v[124:127]\n"
"v_mfma_f32_16x16x32_bf16 v[140:143], v[160:163], v[196:199], v[140:143]\n"
"v_mfma_f32_16x16x32_bf16 v[48:51], v[92:95], v[76:79], v[48:51]\n"
"v_mfma_f32_16x16x32_bf16 v[84:87], v[180:183], v[76:79], v[84:87]\n"
"v_mfma_f32_16x16x32_bf16 v[104:107], v[100:103], v[76:79], v[104:107]\n"
"v_mfma_f32_16x16x32_bf16 v[112:115], v[148:151], v[76:79], v[112:115]\n"
"v_mfma_f32_16x16x32_bf16 v[124:127], v[192:195], v[76:79], v[124:127]\n"
"v_mfma_f32_16x16x32_bf16 v[140:143], v[56:59], v[76:79], v[140:143]\n"
"s_add_u32 s98, s55, 0x40000\n"
"v_mov_b32_e32 v255, 0x80000000\n"
"buffer_load_dwordx4 v[136:139], v0, s[4:7], s98 offen sc1\n"
"buffer_load_dwordx4 v[108:111], v0, s[4:7], s98 offen offset:1024 sc1\n"
"buffer_load_dwordx4 v[120:123], v0, s[4:7], s98 offen offset:2048 sc1\n"
"buffer_load_dwordx4 v[128:131], v0, s[4:7], s98 offen offset:3072 sc1\n"
"buffer_load_dwordx4 v[144:147], v1, s[4:7], s98 offen sc1\n"
"buffer_load_dwordx4 v[176:179], v1, s[4:7], s98 offen offset:1024 sc1\n"
"buffer_store_dword v255, v255, s[4:7], 0 offen\n"
"buffer_load_dwordx4 v[196:199], v1, s[4:7], s98 offen offset:2048 sc1\n"
"buffer_load_dwordx4 v[76:79], v1, s[4:7], s98 offen offset:3072 sc1\n"
"s_nop 7\n"
"s_branch .Lmk_p90r\n"
".Lmk_q92s:\n"
";\n"
"s_mov_b32 s1, 0x10000\n"
".Lmk_q92l:\n"
"s_add_u32 s96, s55, 0x40000\n"
"buffer_load_dwordx4 v[136:139], v0, s[4:7], s96 offen sc1\n"
"buffer_load_dwordx4 v[108:111], v0, s[4:7], s96 offen offset:1024 sc1\n"
"buffer_load_dwordx4 v[120:123], v0, s[4:7], s96 offen offset:2048 sc1\n"
"buffer_load_dwordx4 v[128:131], v0, s[4:7], s96 offen offset:3072 sc1\n"
"buffer_load_dwordx4 v[144:147], v1, s[4:7], s96 offen sc1\n"
"buffer_load_dwordx4 v[176:179], v1, s[4:7], s96 offen offset:1024 sc1\n"
"buffer_load_dwordx4 v[196:199], v1, s[4:7], s96 offen offset:2048 sc1\n"
"buffer_load_dwordx4 v[76:79], v1, s[4:7], s96 offen offset:3072 sc1\n"
"s_add_u32 s96, s52, 0x48000\n"
"buffer_load_dwordx4 v[224:227], v0, s[4:7], s96 offen sc1\n"
"buffer_load_dwordx4 v[84:87], v0, s[4:7], s96 offen offset:1024 sc1\n"
"buffer_load_dwordx4 v[112:115], v0, s[4:7], s96 offen offset:2048 sc1\n"
"buffer_load_dwordx4 v[32:35], v0, s[4:7], s96 offen offset:3072 sc1\n"
"buffer_load_dwordx4 v[44:47], v1, s[4:7], s96 offen sc1\n"
"buffer_load_dwordx4 v[80:83], v1, s[4:7], s96 offen offset:1024 sc1\n"
"s_waitcnt vmcnt(0)\n"
"v_max_u32_e32 v253, v136, v137\n"
"v_max3_u32 v253, v253, v138, v139\n"
"v_max3_u32 v253, v253, v108, v109\n"
"v_max3_u32 v253, v253, v110, v111\n"
"v_max3_u32 v253, v253, v120, v121\n"
"v_max3_u32 v253, v253, v122, v123\n"
"v_max3_u32 v253, v253, v128, v129\n"
"v_max3_u32 v253, v253, v130, v131\n"
"v_cmp_eq_u32_e32 vcc, -1, v253\n"
"s_cbranch_vccz .Lmk_q92k\n"
"s_sub_u32 s1, s1, 1\n"
"s_cbranch_scc0 .Lmk_q92l\n"
".Lmk_q92k:\n"
"s_branch .Lmk_q92r\n"
".Lmk_p91s:\n"
";\n"
"s_mov_b32 s1, 0x10000\n"
".Lmk_p91l:\n"
"s_add_u32 s96, s54, 0x40000\n"
"buffer_load_dwordx4 v[36:39], v0, s[4:7], s96 offen sc1\n"
"buffer_load_dwordx4 v[96:99], v0, s[4:7], s96 offen offset:1024 sc1\n"
"buffer_load_dwordx4 v[124:127], v0, s[4:7], s96 offen offset:2048 sc1\n"
"buffer_load_dwordx4 v[140:143], v0, s[4:7], s96 offen offset:3072 sc1\n"
"buffer_load_dwordx4 v[188:191], v1, s[4:7], s96 offen sc1\n"
"buffer_load_dwordx4 v[204:207], v1, s[4:7], s96 offen offset:1024 sc1\n"
"buffer_load_dwordx4 v[212:215], v1, s[4:7], s96 offen offset:2048 sc1\n"
"buffer_load_dwordx4 v[216:219], v1, s[4:7], s96 offen offset:3072 sc1\n"
"s_add_u32 s96, s55, 0x40000\n"
"buffer_load_dwordx4 v[136:139], v0, s[4:7], s96 offen sc1\n"
"buffer_load_dwordx4 v[108:111], v0, s[4:7], s96 offen offset:1024 sc1\n"
"buffer_load_dwordx4 v[120:123], v0, s[4:7], s96 offen offset:2048 sc1\n"
"buffer_load_dwordx4 v[128:131], v0, s[4:7], s96 offen offset:3072 sc1\n"
"buffer_load_dwordx4 v[144:147], v1, s[4:7], s96 offen sc1\n"
"buffer_load_dwordx4 v[176:179], v1, s[4:7], s96 offen offset:1024 sc1\n"
"buffer_load_dwordx4 v[196:199], v1, s[4:7], s96 offen offset:2048 sc1\n"
"buffer_load_dwordx4 v[76:79], v1, s[4:7], s96 offen offset:3072 sc1\n"
"s_add_u32 s96, s52, 0x48000\n"
"buffer_load_dwordx4 v[224:227], v0, s[4:7], s96 offen sc1\n"
"buffer_load_dwordx4 v[84:87], v0, s[4:7], s96 offen offset:1024 sc1\n"
"buffer_load_dwordx4 v[112:115], v0, s[4:7], s96 offen offset:2048 sc1\n"
"buffer_load_dwordx4 v[32:35], v0, s[4:7], s96 offen offset:3072 sc1\n"
"buffer_load_dwordx4 v[44:47], v1, s[4:7], s96 offen sc1\n"
"buffer_load_dwordx4 v[80:83], v1, s[4:7], s96 offen offset:1024 sc1\n"
"buffer_load_dwordx4 v[24:27], v1, s[4:7], s96 offen offset:2048 sc1\n"
"s_waitcnt vmcnt(0)\n"
"v_max_u32_e32 v253, v36, v37\n"
"v_max3_u32 v253, v253, v38, v39\n"
"v_max3_u32 v253, v253, v96, v97\n"
"v_max3_u32 v253, v253, v98, v99\n"
"v_max3_u32 v253, v253, v124, v125\n"
"v_max3_u32 v253, v253, v126, v127\n"
"v_max3_u32 v253, v253, v140, v141\n"
"v_max3_u32 v253, v253, v142, v143\n"
"v_max3_u32 v253, v253, v188, v189\n"
"v_max3_u32 v253, v253, v190, v191\n"
"v_max3_u32 v253, v253, v204, v205\n"
"v_max3_u32 v253, v253, v206, v207\n"
"v_max3_u32 v253, v253, v212, v213\n"
"v_max3_u32 v253, v253, v214, v215\n"
"v_max3_u32 v253, v253, v216, v217\n"
"v_max3_u32 v253, v253, v218, v219\n"
"v_cmp_eq_u32_e32 vcc, -1, v253\n"
"s_cbranch_vccz .Lmk_p91k\n"
"s_sub_u32 s1, s1, 1\n"
"s_cbranch_scc0 .Lmk_p91l\n"
".Lmk_p91k:\n"
"v_mfma_f32_16x16x32_bf16 v[116:119], a[0:3], v[36:39], 0\n"
"v_mfma_f32_16x16x32_bf16 v[164:167], a[4:7], v[36:39], 0\n"
"v_mfma_f32_16x16x32_bf16 v[200:203], a[8:11], v[36:39], 0\n"
"v_mfma_f32_16x16x32_bf16 v[220:223], a[12:15], v[36:39], 0\n"
"v_mfma_f32_16x16x32_bf16 v[48:51], a[16:19], v[36:39], 0\n"
"v_mfma_f32_16x16x32_bf16 v[228:231], a[20:23], v[36:39], 0\n"
"v_mfma_f32_16x16x32_bf16 v[116:119], a[24:27], v[96:99], v[116:119]\n"
"v_mfma_f32_16x16x32_bf16 v[164:167], a[28:31], v[96:99], v[164:167]\n"
"v_mfma_f32_16x16x32_bf16 v[200:203], a[32:35], v[96:99], v[200:203]\n"
"v_mfma_f32_16x16x32_bf16 v[220:223], a[36:39], v[96:99], v[220:223]\n"
"v_mfma_f32_16x16x32_bf16 v[48:51], a[40:43], v[96:99], v[48:51]\n"
"v_mfma_f32_16x16x32_bf16 v[228:231], a[44:47], v[96:99], v[228:231]\n"
"v_mfma_f32_16x16x32_bf16 v[116:119], a[48:51], v[124:127], v[116:119]\n"
"v_mfma_f32_16x16x32_bf16 v[164:167], a[52:55], v[124:127], v[164:167]\n"
"v_mfma_f32_16x16x32_bf16 v[200:203], a[56:59], v[124:127], v[200:203]\n"
"v_mfma_f32_16x16x32_bf16 v[220:223], a[60:63], v[124:127], v[220:223]\n"
"v_mfma_f32_16x16x32_bf16 v[48:51], a[64:67], v[124:127], v[48:51]\n"
"v_mfma_f32_16x16x32_bf16 v[228:231], a[68:71], v[124:127], v[228:231]\n"
"v_mfma_f32_16x16x32_bf16 v[116:119], a[72:75], v[140:143], v[116:119]\n"
"v_mfma_f32_16x16x32_bf16 v[164:167], a[76:79], v[140:143], v[164:167]\n"
"v_mfma_f32_16x16x32_bf16 v[200:203], a[80:83], v[140:143], v[200:203]\n"
"v_mfma_f32_16x16x32_bf16 v[220:223], a[84:87], v[140:143], v[220:223]\n"
"v_mfma_f32_16x16x32_bf16 v[48:51], a[88:91], v[140:143], v[48:51]\n"
"v_mfma_f32_16x16x32_bf16 v[228:231], a[92:95], v[140:143], v[228:231]\n"
"v_mfma_f32_16x16x32_bf16 v[116:119], a[96:99], v[188:191], v[116:119]\n"
"v_mfma_f32_16x16x32_bf16 v[164:167], a[100:103], v[188:191], v[164:167]\n"
"v_mfma_f32_16x16x32_bf16 v[200:203], a[104:107], v[188:191], v[200:203]\n"
"v_mfma_f32_16x16x32_bf16 v[220:223], a[108:111], v[188:191], v[220:223]\n"
"v_mfma_f32_16x16x32_bf16 v[48:51], a[112:115], v[188:191], v[48:51]\n"
"v_mfma_f32_16x16x32_bf16 v[228:231], a[116:119], v[188:191], v[228:231]\n"
"v_mfma_f32_16x16x32_bf16 v[116:119], a[120:123], v[204:207], v[116:119]\n"
"v_mfma_f32_16x16x32_bf16 v[164:167], a[124:127], v[204:207], v[164:167]\n"
"v_mfma_f32_16x16x32_bf16 v[200:203], v[28:31], v[204:207], v[200:203]\n"
"v_mfma_f32_16x16x32_bf16 v[220:223], v[40:43], v[204:207], v[220:223]\n"
"v_mfma_f32_16x16x32_bf16 v[48:51], v[60:63], v[204:207], v[48:51]\n"
"v_mfma_f32_16x16x32_bf16 v[228:231], v[72:75], v[204:207], v[228:231]\n"
"v_mfma_f32_16x16x32_bf16 v[116:119], v[156:159], v[212:215], v[116:119]\n"
"v_mfma_f32_16x16x32_bf16 v[164:167], v[132:135], v[212:215], v[164:167]\n"
"v_mfma_f32_16x16x32_bf16 v[200:203], v[64:67], v[212:215], v[200:203]\n"
"v_mfma_f32_16x16x32_bf16 v[220:223], v[68:71], v[212:215], v[220:223]\n"
"v_mfma_f32_16x16x32_bf16 v[48:51], v[88:91], v[212:215], v[48:51]\n"
"v_mfma_f32_16x16x32_bf16 v[228:231], v[160:163], v[212:215], v[228:231]\n"
"v_mfma_f32_16x16x32_bf16 v[116:119], v[92:95], v[216:219], v[116:119]\n"
"v_mfma_f32_16x16x32_bf16 v[164:167], v[180:183], v[216:219], v[164:167]\n"
"v_mfma_f32_16x16x32_bf16 v[200:203], v[100:103], v[216:219], v[200:203]\n"
"v_mfma_f32_16x16x32_bf16 v[220:223], v[148:151], v[216:219], v[220:223]\n"
"v_mfma_f32_16x16x32_bf16 v[48:51], v[192:195], v[216:219], v[48:51]\n"
"v_mfma_f32_16x16x32_bf16 v[228:231], v[56:59], v[216:219], v[228:231]\n"
"s_nop 7\n"
"s_branch .Lmk_p91r\n"
".Lmk_q93s:\n"
";\n"
"s_mov_b32 s1, 0x10000\n"
".Lmk_q93l:\n"
"s_add_u32 s97, s55, 0x40000\n"
"buffer_load_dwordx4 v[144:147], v1, s[4:7], s97 offen sc1\n"
"buffer_load_dwordx4 v[176:179], v1, s[4:7], s97 offen offset:1024 sc1\n"
"buffer_load_dwordx4 v[196:199], v1, s[4:7], s97 offen offset:2048 sc1\n"
"buffer_load_dwordx4 v[76:79], v1, s[4:7], s97 offen offset:3072 sc1\n"
"s_add_u32 s97, s52, 0x48000\n"
"buffer_load_dwordx4 v[224:227], v0, s[4:7], s97 offen sc1\n"
"buffer_load_dwordx4 v[84:87], v0, s[4:7], s97 offen offset:1024 sc1\n"
"buffer_load_dwordx4 v[112:115], v0, s[4:7], s97 offen offset:2048 sc1\n"
"buffer_load_dwordx4 v[32:35], v0, s[4:7], s97 offen offset:3072 sc1\n"
"buffer_load_dwordx4 v[44:47], v1, s[4:7], s97 offen sc1\n"
"buffer_load_dwordx4 v[80:83], v1, s[4:7], s97 offen offset:1024 sc1\n"
"buffer_load_dwordx4 v[24:27], v1, s[4:7], s97 offen offset:2048 sc1\n"
"buffer_load_dwordx4 v[36:39], v1, s[4:7], s97 offen offset:3072 sc1\n"
"s_add_u32 s97, s53, 0x48000\n"
"buffer_load_dwordx4 v[96:99], v0, s[4:7], s97 offen sc1\n"
"buffer_load_dwordx4 v[116:119], v0, s[4:7], s97 offen offset:1024 sc1\n"
"s_waitcnt vmcnt(0)\n"
"v_max_u32_e32 v253, v144, v145\n"
"v_max3_u32 v253, v253, v146, v147\n"
"v_max3_u32 v253, v253, v176, v177\n"
"v_max3_u32 v253, v253, v178, v179\n"
"v_max3_u32 v253, v253, v196, v197\n"
"v_max3_u32 v253, v253, v198, v199\n"
"v_max3_u32 v253, v253, v76, v77\n"
"v_max3_u32 v253, v253, v78, v79\n"
"v_cmp_eq_u32_e32 vcc, -1, v253\n"
"s_cbranch_vccz .Lmk_q93k\n"
"s_sub_u32 s1, s1, 1\n"
"s_cbranch_scc0 .Lmk_q93l\n"
".Lmk_q93k:\n"
"s_branch .Lmk_q93r\n"
".Lmk_p95s:\n"
";\n"
"s_mov_b32 s1, 0x10000\n"
".Lmk_p95l:\n"
"s_add_u32 s98, s52, 0x48000\n"
"buffer_load_dwordx4 v[216:219], v0, s[4:7], s98 offen sc1\n"
"buffer_load_dwordx4 v[124:127], v0, s[4:7], s98 offen offset:1024 sc1\n"
"buffer_load_dwordx4 v[84:87], v0, s[4:7], s98 offen offset:2048 sc1\n"
"buffer_load_dwordx4 v[136:139], v0, s[4:7], s98 offen offset:3072 sc1\n"
"buffer_load_dwordx4 v[32:35], v1, s[4:7], s98 offen sc1\n"
"buffer_load_dwordx4 v[44:47], v1, s[4:7], s98 offen offset:1024 sc1\n"
"buffer_load_dwordx4 v[104:107], v1, s[4:7], s98 offen offset:2048 sc1\n"
"buffer_load_dwordx4 v[24:27], v1, s[4:7], s98 offen offset:3072 sc1\n"
"s_add_u32 s98, s53, 0x48000\n"
"buffer_load_dwordx4 v[96:99], v0, s[4:7], s98 offen sc1\n"
"buffer_load_dwordx4 v[116:119], v0, s[4:7], s98 offen offset:1024 sc1\n"
"buffer_load_dwordx4 v[48:51], v0, s[4:7], s98 offen offset:2048 sc1\n"
"buffer_load_dwordx4 v[108:111], v0, s[4:7], s98 offen offset:3072 sc1\n"
"buffer_load_dwordx4 v[120:123], v1, s[4:7], s98 offen sc1\n"
"buffer_load_dwordx4 v[188:191], v1, s[4:7], s98 offen offset:1024 sc1\n"
"buffer_load_dwordx4 v[52:55], v1, s[4:7], s98 offen offset:2048 sc1\n"
"buffer_load_dwordx4 v[140:143], v1, s[4:7], s98 offen offset:3072 sc1\n"
"s_waitcnt vmcnt(0)\n"
"v_max_u32_e32 v255, v216, v217\n"
"v_max3_u32 v255, v255, v218, v219\n"
"v_max3_u32 v255, v255, v124, v125\n"
"v_max3_u32 v255, v255, v126, v127\n"
"v_max3_u32 v255, v255, v84, v85\n"
"v_max3_u32 v255, v255, v86, v87\n"
"v_max3_u32 v255, v255, v136, v137\n"
"v_max3_u32 v255, v255, v138, v139\n"
"v_max3_u32 v255, v255, v32, v33\n"
"v_max3_u32 v255, v255, v34, v35\n"
"v_max3_u32 v255, v255, v44, v45\n"
"v_max3_u32 v255, v255, v46, v47\n"
"v_max3_u32 v255, v255, v104, v105\n"
"v_max3_u32 v255, v255, v106, v107\n"
"v_max3_u32 v255, v255, v24, v25\n"
"v_max3_u32 v255, v255, v26, v27\n"
"v_cmp_eq_u32_e32 vcc, -1, v255\n"
"s_cbranch_vccz .Lmk_p95k\n"
"s_sub_u32 s1, s1, 1\n"
"s_cbranch_scc0 .Lmk_p95l\n"
".Lmk_p95k:\n"
"v_mfma_f32_16x16x32_bf16 v[76:79], a[0:3], v[216:219], 0\n"
"v_mfma_f32_16x16x32_bf16 v[164:167], a[4:7], v[216:219], 0\n"
"v_mfma_f32_16x16x32_bf16 v[200:203], a[8:11], v[216:219], 0\n"
"v_mfma_f32_16x16x32_bf16 v[204:207], a[12:15], v[216:219], 0\n"
"v_mfma_f32_16x16x32_bf16 v[212:215], a[16:19], v[216:219], 0\n"
"v_mfma_f32_16x16x32_bf16 v[220:223], a[20:23], v[216:219], 0\n"
"v_mfma_f32_16x16x32_bf16 v[76:79], a[24:27], v[124:127], v[76:79]\n"
"v_mfma_f32_16x16x32_bf16 v[164:167], a[28:31], v[124:127], v[164:167]\n"
"v_mfma_f32_16x16x32_bf16 v[200:203], a[32:35], v[124:127], v[200:203]\n"
"v_mfma_f32_16x16x32_bf16 v[204:207], a[36:39], v[124:127], v[204:207]\n"
"v_mfma_f32_16x16x32_bf16 v[212:215], a[40:43], v[124:127], v[212:215]\n"
"v_mfma_f32_16x16x32_bf16 v[220:223], a[44:47], v[124:127], v[220:223]\n"
"v_mfma_f32_16x16x32_bf16 v[76:79], a[48:51], v[84:87], v[76:79]\n"
"v_mfma_f32_16x16x32_bf16 v[164:167], a[52:55], v[84:87], v[164:167]\n"
"v_mfma_f32_16x16x32_bf16 v[200:203], a[56:59], v[84:87], v[200:203]\n"
"v_mfma_f32_16x16x32_bf16 v[204:207], a[60:63], v[84:87], v[204:207]\n"
"v_mfma_f32_16x16x32_bf16 v[212:215], a[64:67], v[84:87], v[212:215]\n"
"v_mfma_f32_16x16x32_bf16 v[220:223], a[68:71], v[84:87], v[220:223]\n"
"v_mfma_f32_16x16x32_bf16 v[76:79], a[72:75], v[136:139], v[76:79]\n"
"v_mfma_f32_16x16x32_bf16 v[164:167], a[76:79], v[136:139], v[164:167]\n"
"v_mfma_f32_16x16x32_bf16 v[200:203], a[80:83], v[136:139], v[200:203]\n"
"v_mfma_f32_16x16x32_bf16 v[204:207], a[84:87], v[136:139], v[204:207]\n"
"v_mfma_f32_16x16x32_bf16 v[212:215], a[88:91], v[136:139], v[212:215]\n"
"v_mfma_f32_16x16x32_bf16 v[220:223], a[92:95], v[136:139], v[220:223]\n"
"v_mfma_f32_16x16x32_bf16 v[76:79], a[96:99], v[32:35], v[76:79]\n"
"v_mfma_f32_16x16x32_bf16 v[164:167], a[100:103], v[32:35], v[164:167]\n"
"v_mfma_f32_16x16x32_bf16 v[200:203], a[104:107], v[32:35], v[200:203]\n"
"v_mfma_f32_16x16x32_bf16 v[204:207], a[108:111], v[32:35], v[204:207]\n"
"v_mfma_f32_16x16x32_bf16 v[212:215], a[112:115], v[32:35], v[212:215]\n"
"v_mfma_f32_16x16x32_bf16 v[220:223], a[116:119], v[32:35], v[220:223]\n"
"v_mfma_f32_16x16x32_bf16 v[76:79], a[120:123], v[44:47], v[76:79]\n"
"v_mfma_f32_16x16x32_bf16 v[164:167], a[124:127], v[44:47], v[164:167]\n"
"v_mfma_f32_16x16x32_bf16 v[200:203], v[28:31], v[44:47], v[200:203]\n"
"v_mfma_f32_16x16x32_bf16 v[204:207], v[40:43], v[44:47], v[204:207]\n"
"v_mfma_f32_16x16x32_bf16 v[212:215], v[60:63], v[44:47], v[212:215]\n"
"v_mfma_f32_16x16x32_bf16 v[220:223], v[72:75], v[44:47], v[220:223]\n"
"v_mfma_f32_16x16x32_bf16 v[76:79], v[156:159], v[104:107], v[76:79]\n"
"v_mfma_f32_16x16x32_bf16 v[164:167], v[132:135], v[104:107], v[164:167]\n"
"v_mfma_f32_16x16x32_bf16 v[200:203], v[64:67], v[104:107], v[200:203]\n"
"v_mfma_f32_16x16x32_bf16 v[204:207], v[68:71], v[104:107], v[204:207]\n"
"v_mfma_f32_16x16x32_bf16 v[212:215], v[88:91], v[104:107], v[212:215]\n"
"v_mfma_f32_16x16x32_bf16 v[220:223], v[160:163], v[104:107], v[220:223]\n"
"v_mfma_f32_16x16x32_bf16 v[76:79], v[92:95], v[24:27], v[76:79]\n"
"v_mfma_f32_16x16x32_bf16 v[164:167], v[180:183], v[24:27], v[164:167]\n"
"v_mfma_f32_16x16x32_bf16 v[200:203], v[100:103], v[24:27], v[200:203]\n"
"v_mfma_f32_16x16x32_bf16 v[204:207], v[148:151], v[24:27], v[204:207]\n"
"v_mfma_f32_16x16x32_bf16 v[212:215], v[192:195], v[24:27], v[212:215]\n"
"v_mfma_f32_16x16x32_bf16 v[220:223], v[56:59], v[24:27], v[220:223]\n"
"s_add_u32 s98, s54, 0x48000\n"
"v_mov_b32_e32 v255, 0x80000000\n"
"buffer_load_dwordx4 v[216:219], v0, s[4:7], s98 offen sc1\n"
"buffer_store_dword v255, v255, s[4:7], 0 offen\n"
"buffer_store_dword v255, v255, s[4:7], 0 offen\n"
"buffer_store_dword v255, v255, s[4:7], 0 offen\n"
"buffer_load_dwordx4 v[124:127], v0, s[4:7], s98 offen offset:1024 sc1\n"
"buffer_store_dword v255, v255, s[4:7], 0 offen\n"
"buffer_store_dword v255, v255, s[4:7], 0 offen\n"
"buffer_load_dwordx4 v[84:87], v0, s[4:7], s98 offen offset:2048 sc1\n"
"buffer_store_dword v255, v255, s[4:7], 0 offen\n"
"buffer_store_dword v255, v255, s[4:7], 0 offen\n"
"buffer_load_dwordx4 v[136:139], v0, s[4:7], s98 offen offset:3072 sc1\n"
"buffer_store_dword v255, v255, s[4:7], 0 offen\n"
"buffer_store_dword v255, v255, s[4:7], 0 offen\n"
"buffer_load_dwordx4 v[32:35], v1, s[4:7], s98 offen sc1\n"
"buffer_store_dword v255, v255, s[4:7], 0 offen\n"
"buffer_load_dwordx4 v[44:47], v1, s[4:7], s98 offen offset:1024 sc1\n"
"buffer_load_dwordx4 v[104:107], v1, s[4:7], s98 offen offset:2048 sc1\n"
"buffer_load_dwordx4 v[24:27], v1, s[4:7], s98 offen offset:3072 sc1\n"
"s_nop 7\n"
"s_branch .Lmk_p95r\n"
".Lmk_m100s:\n"
";\n"
"s_mov_b32 s1, 0x10000\n"
".Lmk_m100l:\n"
"s_add_u32 s95, s62, 0x4800\n"
"buffer_load_dword v253, v10, s[4:7], s95 offen sc1\n"
"buffer_load_dword v252, v10, s[4:7], s95 offen offset:256 sc1\n"
"buffer_load_dword v251, v10, s[4:7], s95 offen offset:512 sc1\n"
"buffer_load_dword v250, v10, s[4:7], s95 offen offset:768 sc1\n"
"buffer_load_dword v249, v10, s[4:7], s95 offen offset:1024 sc1\n"
"buffer_load_dword v248, v10, s[4:7], s95 offen offset:1280 sc1\n"
"buffer_load_dword v247, v10, s[4:7], s95 offen offset:1536 sc1\n"
"buffer_load_dword v246, v10, s[4:7], s95 offen offset:1792 sc1\n"
"s_waitcnt vmcnt(0)\n"
"v_max_u32_e32 v245, v253, v252\n"
"v_max3_u32 v245, v245, v251, v250\n"
"v_max3_u32 v245, v245, v249, v248\n"
"v_max3_u32 v245, v245, v247, v246\n"
"v_cmp_eq_u32_e32 vcc, -1, v245\n"
"s_cbranch_vccz .Lmk_m100k\n"
"s_sub_u32 s1, s1, 1\n"
"s_cbranch_scc0 .Lmk_m100l\n"
".Lmk_m100k:\n"
"v_add_f32_e32 v253, v253, v249\n"
"v_add_f32_e32 v252, v252, v248\n"
"v_add_f32_e32 v251, v251, v247\n"
"v_add_f32_e32 v250, v250, v246\n"
"v_add_f32_e32 v253, v253, v251\n"
"v_add_f32_e32 v252, v252, v250\n"
"v_add_f32_e32 v253, v253, v252\n"
"s_nop 1\n"
"s_branch .Lmk_m100r\n"
".Lmk_p96s:\n"
";\n"
"s_mov_b32 s1, 0x10000\n"
".Lmk_p96l:\n"
"s_add_u32 s98, s53, 0x48000\n"
"buffer_load_dwordx4 v[176:179], v0, s[4:7], s98 offen sc1\n"
"buffer_load_dwordx4 v[152:155], v0, s[4:7], s98 offen offset:1024 sc1\n"
"buffer_load_dwordx4 v[96:99], v0, s[4:7], s98 offen offset:2048 sc1\n"
"buffer_load_dwordx4 v[48:51], v0, s[4:7], s98 offen offset:3072 sc1\n"
"buffer_load_dwordx4 v[80:83], v1, s[4:7], s98 offen sc1\n"
"buffer_load_dwordx4 v[108:111], v1, s[4:7], s98 offen offset:1024 sc1\n"
"buffer_load_dwordx4 v[112:115], v1, s[4:7], s98 offen offset:2048 sc1\n"
"buffer_load_dwordx4 v[196:199], v1, s[4:7], s98 offen offset:3072 sc1\n"
"s_add_u32 s98, s54, 0x48000\n"
"buffer_load_dwordx4 v[216:219], v0, s[4:7], s98 offen sc1\n"
"buffer_load_dwordx4 v[124:127], v0, s[4:7], s98 offen offset:1024 sc1\n"
"buffer_load_dwordx4 v[84:87], v0, s[4:7], s98 offen offset:2048 sc1\n"
"buffer_load_dwordx4 v[136:139], v0, s[4:7], s98 offen offset:3072 sc1\n"
"buffer_load_dwordx4 v[32:35], v1, s[4:7], s98 offen sc1\n"
"buffer_load_dwordx4 v[44:47], v1, s[4:7], s98 offen offset:1024 sc1\n"
"buffer_load_dwordx4 v[104:107], v1, s[4:7], s98 offen offset:2048 sc1\n"
"buffer_load_dwordx4 v[24:27], v1, s[4:7], s98 offen offset:3072 sc1\n"
"s_waitcnt vmcnt(0)\n"
"v_max_u32_e32 v253, v176, v177\n"
"v_max3_u32 v253, v253, v178, v179\n"
"v_max3_u32 v253, v253, v152, v153\n"
"v_max3_u32 v253, v253, v154, v155\n"
"v_max3_u32 v253, v253, v96, v97\n"
"v_max3_u32 v253, v253, v98, v99\n"
"v_max3_u32 v253, v253, v48, v49\n"
"v_max3_u32 v253, v253, v50, v51\n"
"v_max3_u32 v253, v253, v80, v81\n"
"v_max3_u32 v253, v253, v82, v83\n"
"v_max3_u32 v253, v253, v108, v109\n"
"v_max3_u32 v253, v253, v110, v111\n"
"v_max3_u32 v253, v253, v112, v113\n"
"v_max3_u32 v253, v253, v114, v115\n"
"v_max3_u32 v253, v253, v196, v197\n"
"v_max3_u32 v253, v253, v198, v199\n"
"v_cmp_eq_u32_e32 vcc, -1, v253\n"
"s_cbranch_vccz .Lmk_p96k\n"
"s_sub_u32 s1, s1, 1\n"
"s_cbranch_scc0 .Lmk_p96l\n"
".Lmk_p96k:\n"
"v_mfma_f32_16x16x32_bf16 v[36:39], a[0:3], v[176:179], 0\n"
"v_mfma_f32_16x16x32_bf16 v[128:131], a[4:7], v[176:179], 0\n"
"v_mfma_f32_16x16x32_bf16 v[144:147], a[8:11], v[176:179], 0\n"
"v_mfma_f32_16x16x32_bf16 v[172:175], a[12:15], v[176:179], 0\n"
"v_mfma_f32_16x16x32_bf16 v[76:79], a[16:19], v[176:179], 0\n"
"v_mfma_f32_16x16x32_bf16 v[184:187], a[20:23], v[176:179], 0\n"
"v_mfma_f32_16x16x32_bf16 v[36:39], a[24:27], v[152:155], v[36:39]\n"
"v_mfma_f32_16x16x32_bf16 v[128:131], a[28:31], v[152:155], v[128:131]\n"
"v_mfma_f32_16x16x32_bf16 v[144:147], a[32:35], v[152:155], v[144:147]\n"
"v_mfma_f32_16x16x32_bf16 v[172:175], a[36:39], v[152:155], v[172:175]\n"
"v_mfma_f32_16x16x32_bf16 v[76:79], a[40:43], v[152:155], v[76:79]\n"
"v_mfma_f32_16x16x32_bf16 v[184:187], a[44:47], v[152:155], v[184:187]\n"
"v_mfma_f32_16x16x32_bf16 v[36:39], a[48:51], v[96:99], v[36:39]\n"
"v_mfma_f32_16x16x32_bf16 v[128:131], a[52:55], v[96:99], v[128:131]\n"
"v_mfma_f32_16x16x32_bf16 v[144:147], a[56:59], v[96:99], v[144:147]\n"
"v_mfma_f32_16x16x32_bf16 v[172:175], a[60:63], v[96:99], v[172:175]\n"
"v_mfma_f32_16x16x32_bf16 v[76:79], a[64:67], v[96:99], v[76:79]\n"
"v_mfma_f32_16x16x32_bf16 v[184:187], a[68:71], v[96:99], v[184:187]\n"
"v_mfma_f32_16x16x32_bf16 v[36:39], a[72:75], v[48:51], v[36:39]\n"
"v_mfma_f32_16x16x32_bf16 v[128:131], a[76:79], v[48:51], v[128:131]\n"
"v_mfma_f32_16x16x32_bf16 v[144:147], a[80:83], v[48:51], v[144:147]\n"
"v_mfma_f32_16x16x32_bf16 v[172:175], a[84:87], v[48:51], v[172:175]\n"
"v_mfma_f32_16x16x32_bf16 v[76:79], a[88:91], v[48:51], v[76:79]\n"
"v_mfma_f32_16x16x32_bf16 v[184:187], a[92:95], v[48:51], v[184:187]\n"
"v_mfma_f32_16x16x32_bf16 v[36:39], a[96:99], v[80:83], v[36:39]\n"
"v_mfma_f32_16x16x32_bf16 v[128:131], a[100:103], v[80:83], v[128:131]\n"
"v_mfma_f32_16x16x32_bf16 v[144:147], a[104:107], v[80:83], v[144:147]\n"
"v_mfma_f32_16x16x32_bf16 v[172:175], a[108:111], v[80:83], v[172:175]\n"
"v_mfma_f32_16x16x32_bf16 v[76:79], a[112:115], v[80:83], v[76:79]\n"
"v_mfma_f32_16x16x32_bf16 v[184:187], a[116:119], v[80:83], v[184:187]\n"
"v_mfma_f32_16x16x32_bf16 v[36:39], a[120:123], v[108:111], v[36:39]\n"
"v_mfma_f32_16x16x32_bf16 v[128:131], a[124:127], v[108:111], v[128:131]\n"
"v_mfma_f32_16x16x32_bf16 v[144:147], v[28:31], v[108:111], v[144:147]\n"
"v_mfma_f32_16x16x32_bf16 v[172:175], v[40:43], v[108:111], v[172:175]\n"
"v_mfma_f32_16x16x32_bf16 v[76:79], v[60:63], v[108:111], v[76:79]\n"
"v_mfma_f32_16x16x32_bf16 v[184:187], v[72:75], v[108:111], v[184:187]\n"
"v_mfma_f32_16x16x32_bf16 v[36:39], v[156:159], v[112:115], v[36:39]\n"
"v_mfma_f32_16x16x32_bf16 v[128:131], v[132:135], v[112:115], v[128:131]\n"
"v_mfma_f32_16x16x32_bf16 v[144:147], v[64:67], v[112:115], v[144:147]\n"
"v_mfma_f32_16x16x32_bf16 v[172:175], v[68:71], v[112:115], v[172:175]\n"
"v_mfma_f32_16x16x32_bf16 v[76:79], v[88:91], v[112:115], v[76:79]\n"
"v_mfma_f32_16x16x32_bf16 v[184:187], v[160:163], v[112:115], v[184:187]\n"
"v_mfma_f32_16x16x32_bf16 v[36:39], v[92:95], v[196:199], v[36:39]\n"
"v_mfma_f32_16x16x32_bf16 v[128:131], v[180:183], v[196:199], v[128:131]\n"
"v_mfma_f32_16x16x32_bf16 v[144:147], v[100:103], v[196:199], v[144:147]\n"
"v_mfma_f32_16x16x32_bf16 v[172:175], v[148:151], v[196:199], v[172:175]\n"
"v_mfma_f32_16x16x32_bf16 v[76:79], v[192:195], v[196:199], v[76:79]\n"
"v_mfma_f32_16x16x32_bf16 v[184:187], v[56:59], v[196:199], v[184:187]\n"
"s_add_u32 s98, s55, 0x48000\n"
"v_mov_b32_e32 v253, 0x80000000\n"
"buffer_load_dwordx4 v[176:179], v0, s[4:7], s98 offen sc1\n"
"buffer_load_dwordx4 v[152:155], v0, s[4:7], s98 offen offset:1024 sc1\n"
"buffer_store_dword v253, v253, s[4:7], 0 offen\n"
"buffer_load_dwordx4 v[96:99], v0, s[4:7], s98 offen offset:2048 sc1\n"
"buffer_store_dword v253, v253, s[4:7], 0 offen\n"
"buffer_store_dword v253, v253, s[4:7], 0 offen\n"
"buffer_load_dwordx4 v[48:51], v0, s[4:7], s98 offen offset:3072 sc1\n"
"buffer_store_dword v253, v253, s[4:7], 0 offen\n"
"buffer_load_dwordx4 v[80:83], v1, s[4:7], s98 offen sc1\n"
"buffer_load_dwordx4 v[108:111], v1, s[4:7], s98 offen offset:1024 sc1\n"
"buffer_load_dwordx4 v[112:115], v1, s[4:7], s98 offen offset:2048 sc1\n"
"buffer_store_dword v253, v253, s[4:7], 0 offen\n"
"buffer_load_dwordx4 v[196:199], v1, s[4:7], s98 offen offset:3072 sc1\n"
"s_nop 7\n"
"s_branch .Lmk_p96r\n"
".Lmk_f85s:\n"
";\n"
"s_mov_b32 s1, 0x10000\n"
".Lmk_f85l:\n"
"buffer_load_dwordx2 v[254:255], v12, s[4:7], s97 offen sc1\n"
"s_waitcnt vmcnt(0)\n"
"v_cmp_ne_u64_e32 vcc, v[254:255], v[18:19]\n"
"s_cbranch_vccz .Lmk_f85r\n"
"s_sub_u32 s1, s1, 1\n"
"s_cbranch_scc0 .Lmk_f85l\n"
"s_branch .Lmk_f85r\n"
".Lmk_q98s:\n"
";\n"
"s_mov_b32 s1, 0x10000\n"
".Lmk_q98l:\n"
"s_add_u32 s97, s55, 0x48000\n"
"buffer_load_dwordx4 v[176:179], v0, s[4:7], s97 offen sc1\n"
"buffer_load_dwordx4 v[152:155], v0, s[4:7], s97 offen offset:1024 sc1\n"
"buffer_load_dwordx4 v[96:99], v0, s[4:7], s97 offen offset:2048 sc1\n"
"buffer_load_dwordx4 v[48:51], v0, s[4:7], s97 offen offset:3072 sc1\n"
"buffer_load_dwordx4 v[80:83], v1, s[4:7], s97 offen sc1\n"
"buffer_load_dwordx4 v[108:111], v1, s[4:7], s97 offen offset:1024 sc1\n"
"buffer_load_dwordx4 v[112:115], v1, s[4:7], s97 offen offset:2048 sc1\n"
"buffer_load_dwordx4 v[196:199], v1, s[4:7], s97 offen offset:3072 sc1\n"
"s_waitcnt vmcnt(0)\n"
"v_max_u32_e32 v253, v176, v177\n"
"v_max3_u32 v253, v253, v178, v179\n"
"v_max3_u32 v253, v253, v152, v153\n"
"v_max3_u32 v253, v253, v154, v155\n"
"v_max3_u32 v253, v253, v96, v97\n"
"v_max3_u32 v253, v253, v98, v99\n"
"v_max3_u32 v253, v253, v48, v49\n"
"v_max3_u32 v253, v253, v50, v51\n"
"v_cmp_eq_u32_e32 vcc, -1, v253\n"
"s_cbranch_vccz .Lmk_q98k\n"
"s_sub_u32 s1, s1, 1\n"
"s_cbranch_scc0 .Lmk_q98l\n"
".Lmk_q98k:\n"
"s_branch .Lmk_q98r\n"
".Lmk_p97s:\n"
";\n"
"s_mov_b32 s1, 0x10000\n"
".Lmk_p97l:\n"
"s_add_u32 s97, s54, 0x48000\n"
"buffer_load_dwordx4 v[24:27], v0, s[4:7], s97 offen sc1\n"
"buffer_load_dwordx4 v[104:107], v0, s[4:7], s97 offen offset:1024 sc1\n"
"buffer_load_dwordx4 v[136:139], v0, s[4:7], s97 offen offset:2048 sc1\n"
"buffer_load_dwordx4 v[144:147], v0, s[4:7], s97 offen offset:3072 sc1\n"
"buffer_load_dwordx4 v[172:175], v1, s[4:7], s97 offen sc1\n"
"buffer_load_dwordx4 v[188:191], v1, s[4:7], s97 offen offset:1024 sc1\n"
"buffer_load_dwordx4 v[216:219], v1, s[4:7], s97 offen offset:2048 sc1\n"
"buffer_load_dwordx4 v[224:227], v1, s[4:7], s97 offen offset:3072 sc1\n"
"s_add_u32 s97, s55, 0x48000\n"
"buffer_load_dwordx4 v[176:179], v0, s[4:7], s97 offen sc1\n"
"buffer_load_dwordx4 v[152:155], v0, s[4:7], s97 offen offset:1024 sc1\n"
"buffer_load_dwordx4 v[96:99], v0, s[4:7], s97 offen offset:2048 sc1\n"
"buffer_load_dwordx4 v[48:51], v0, s[4:7], s97 offen offset:3072 sc1\n"
"buffer_load_dwordx4 v[80:83], v1, s[4:7], s97 offen sc1\n"
"buffer_load_dwordx4 v[108:111], v1, s[4:7], s97 offen offset:1024 sc1\n"
"buffer_load_dwordx4 v[112:115], v1, s[4:7], s97 offen offset:2048 sc1\n"
"buffer_load_dwordx4 v[196:199], v1, s[4:7], s97 offen offset:3072 sc1\n"
"s_waitcnt vmcnt(0)\n"
"v_max_u32_e32 v253, v24, v25\n"
"v_max3_u32 v253, v253, v26, v27\n"
"v_max3_u32 v253, v253, v104, v105\n"
"v_max3_u32 v253, v253, v106, v107\n"
"v_max3_u32 v253, v253, v136, v137\n"
"v_max3_u32 v253, v253, v138, v139\n"
"v_max3_u32 v253, v253, v144, v145\n"
"v_max3_u32 v253, v253, v146, v147\n"
"v_max3_u32 v253, v253, v172, v173\n"
"v_max3_u32 v253, v253, v174, v175\n"
"v_max3_u32 v253, v253, v188, v189\n"
"v_max3_u32 v253, v253, v190, v191\n"
"v_max3_u32 v253, v253, v216, v217\n"
"v_max3_u32 v253, v253, v218, v219\n"
"v_max3_u32 v253, v253, v224, v225\n"
"v_max3_u32 v253, v253, v226, v227\n"
"v_cmp_eq_u32_e32 vcc, -1, v253\n"
"s_cbranch_vccz .Lmk_p97k\n"
"s_sub_u32 s1, s1, 1\n"
"s_cbranch_scc0 .Lmk_p97l\n"
".Lmk_p97k:\n"
"v_mfma_f32_16x16x32_bf16 v[140:143], a[0:3], v[24:27], 0\n"
"v_mfma_f32_16x16x32_bf16 v[200:203], a[4:7], v[24:27], 0\n"
"v_mfma_f32_16x16x32_bf16 v[204:207], a[8:11], v[24:27], 0\n"
"v_mfma_f32_16x16x32_bf16 v[208:211], a[12:15], v[24:27], 0\n"
"v_mfma_f32_16x16x32_bf16 v[36:39], a[16:19], v[24:27], 0\n"
"v_mfma_f32_16x16x32_bf16 v[220:223], a[20:23], v[24:27], 0\n"
"v_mfma_f32_16x16x32_bf16 v[140:143], a[24:27], v[104:107], v[140:143]\n"
"v_mfma_f32_16x16x32_bf16 v[200:203], a[28:31], v[104:107], v[200:203]\n"
"v_mfma_f32_16x16x32_bf16 v[204:207], a[32:35], v[104:107], v[204:207]\n"
"v_mfma_f32_16x16x32_bf16 v[208:211], a[36:39], v[104:107], v[208:211]\n"
"v_mfma_f32_16x16x32_bf16 v[36:39], a[40:43], v[104:107], v[36:39]\n"
"v_mfma_f32_16x16x32_bf16 v[220:223], a[44:47], v[104:107], v[220:223]\n"
"v_mfma_f32_16x16x32_bf16 v[140:143], a[48:51], v[136:139], v[140:143]\n"
"v_mfma_f32_16x16x32_bf16 v[200:203], a[52:55], v[136:139], v[200:203]\n"
"v_mfma_f32_16x16x32_bf16 v[204:207], a[56:59], v[136:139], v[204:207]\n"
"v_mfma_f32_16x16x32_bf16 v[208:211], a[60:63], v[136:139], v[208:211]\n"
"v_mfma_f32_16x16x32_bf16 v[36:39], a[64:67], v[136:139], v[36:39]\n"
"v_mfma_f32_16x16x32_bf16 v[220:223], a[68:71], v[136:139], v[220:223]\n"
"v_mfma_f32_16x16x32_bf16 v[140:143], a[72:75], v[144:147], v[140:143]\n"
"v_mfma_f32_16x16x32_bf16 v[200:203], a[76:79], v[144:147], v[200:203]\n"
"v_mfma_f32_16x16x32_bf16 v[204:207], a[80:83], v[144:147], v[204:207]\n"
"v_mfma_f32_16x16x32_bf16 v[208:211], a[84:87], v[144:147], v[208:211]\n"
"v_mfma_f32_16x16x32_bf16 v[36:39], a[88:91], v[144:147], v[36:39]\n"
"v_mfma_f32_16x16x32_bf16 v[220:223], a[92:95], v[144:147], v[220:223]\n"
"v_mfma_f32_16x16x32_bf16 v[140:143], a[96:99], v[172:175], v[140:143]\n"
"v_mfma_f32_16x16x32_bf16 v[200:203], a[100:103], v[172:175], v[200:203]\n"
"v_mfma_f32_16x16x32_bf16 v[204:207], a[104:107], v[172:175], v[204:207]\n"
"v_mfma_f32_16x16x32_bf16 v[208:211], a[108:111], v[172:175], v[208:211]\n"
"v_mfma_f32_16x16x32_bf16 v[36:39], a[112:115], v[172:175], v[36:39]\n"
"v_mfma_f32_16x16x32_bf16 v[220:223], a[116:119], v[172:175], v[220:223]\n"
"v_mfma_f32_16x16x32_bf16 v[140:143], a[120:123], v[188:191], v[140:143]\n"
"v_mfma_f32_16x16x32_bf16 v[200:203], a[124:127], v[188:191], v[200:203]\n"
"v_mfma_f32_16x16x32_bf16 v[204:207], v[28:31], v[188:191], v[204:207]\n"
"v_mfma_f32_16x16x32_bf16 v[208:211], v[40:43], v[188:191], v[208:211]\n"
"v_mfma_f32_16x16x32_bf16 v[36:39], v[60:63], v[188:191], v[36:39]\n"
"v_mfma_f32_16x16x32_bf16 v[220:223], v[72:75], v[188:191], v[220:223]\n"
"v_mfma_f32_16x16x32_bf16 v[140:143], v[156:159], v[216:219], v[140:143]\n"
"v_mfma_f32_16x16x32_bf16 v[200:203], v[132:135], v[216:219], v[200:203]\n"
"v_mfma_f32_16x16x32_bf16 v[204:207], v[64:67], v[216:219], v[204:207]\n"
"v_mfma_f32_16x16x32_bf16 v[208:211], v[68:71], v[216:219], v[208:211]\n"
"v_mfma_f32_16x16x32_bf16 v[36:39], v[88:91], v[216:219], v[36:39]\n"
"v_mfma_f32_16x16x32_bf16 v[220:223], v[160:163], v[216:219], v[220:223]\n"
"v_mfma_f32_16x16x32_bf16 v[140:143], v[92:95], v[224:227], v[140:143]\n"
"v_mfma_f32_16x16x32_bf16 v[200:203], v[180:183], v[224:227], v[200:203]\n"
"v_mfma_f32_16x16x32_bf16 v[204:207], v[100:103], v[224:227], v[204:207]\n"
"v_mfma_f32_16x16x32_bf16 v[208:211], v[148:151], v[224:227], v[208:211]\n"
"v_mfma_f32_16x16x32_bf16 v[36:39], v[192:195], v[224:227], v[36:39]\n"
"v_mfma_f32_16x16x32_bf16 v[220:223], v[56:59], v[224:227], v[220:223]\n"
"s_nop 7\n"
"s_branch .Lmk_p97r\n"
".Lmk_q99s:\n"
";\n"
"s_mov_b32 s1, 0x10000\n"
".Lmk_q99l:\n"
"s_add_u32 s97, s55, 0x48000\n"
"buffer_load_dwordx4 v[80:83], v1, s[4:7], s97 offen sc1\n"
"buffer_load_dwordx4 v[108:111], v1, s[4:7], s97 offen offset:1024 sc1\n"
"buffer_load_dwordx4 v[112:115], v1, s[4:7], s97 offen offset:2048 sc1\n"
"buffer_load_dwordx4 v[196:199], v1, s[4:7], s97 offen offset:3072 sc1\n"
"s_waitcnt vmcnt(0)\n"
"v_max_u32_e32 v253, v80, v81\n"
"v_max3_u32 v253, v253, v82, v83\n"
"v_max3_u32 v253, v253, v108, v109\n"
"v_max3_u32 v253, v253, v110, v111\n"
"v_max3_u32 v253, v253, v112, v113\n"
"v_max3_u32 v253, v253, v114, v115\n"
"v_max3_u32 v253, v253, v196, v197\n"
"v_max3_u32 v253, v253, v198, v199\n"
"v_cmp_eq_u32_e32 vcc, -1, v253\n"
"s_cbranch_vccz .Lmk_q99k\n"
"s_sub_u32 s1, s1, 1\n"
"s_cbranch_scc0 .Lmk_q99l\n"
".Lmk_q99k:\n"
"s_branch .Lmk_q99r\n"
".Lmk_f86s:\n"
";\n"
"s_mov_b32 s1, 0x10000\n"
".Lmk_f86l:\n"
"buffer_load_dwordx2 v[250:251], off, s[4:7], s96 sc1\n"
"s_waitcnt vmcnt(0)\n"
"v_cmp_ne_u64_e32 vcc, v[250:251], v[18:19]\n"
"s_cbranch_vccz .Lmk_f86r\n"
"s_sub_u32 s1, s1, 1\n"
"s_cbranch_scc0 .Lmk_f86l\n"
"s_branch .Lmk_f86r\n"
".Lmk_x109s:\n"
"s_nop 3\n"
"v_mov_b32_e32 v36, 0xff800000\n"
"v_mov_b32_e32 v37, 0xff800000\n"
"v_mov_b32_e32 v38, 0xff800000\n"
"v_mov_b32_e32 v39, 0xff800000\n"
"v_mov_b32_e32 v60, 0xff800000\n"
"v_mov_b32_e32 v61, 0xff800000\n"
"v_mov_b32_e32 v62, 0xff800000\n"
"v_mov_b32_e32 v63, 0xff800000\n"
"s_nop 1\n"
"s_branch .Lmk_x109r\n"
".Lmk_x110s:\n"
"s_nop 3\n"
"v_mov_b32_e32 v36, 0xff800000\n"
"v_mov_b32_e32 v37, 0xff800000\n"
"v_mov_b32_e32 v38, 0xff800000\n"
"v_mov_b32_e32 v39, 0xff800000\n"
"v_mov_b32_e32 v60, 0xff800000\n"
"v_mov_b32_e32 v61, 0xff800000\n"
"v_mov_b32_e32 v62, 0xff800000\n"
"v_mov_b32_e32 v63, 0xff800000\n"
"s_nop 1\n"
"s_branch .Lmk_x110r\n"
".Lmk_x111s:\n"
"s_nop 3\n"
"v_mov_b32_e32 v36, 0xff800000\n"
"v_mov_b32_e32 v37, 0xff800000\n"
"v_mov_b32_e32 v38, 0xff800000\n"
"v_mov_b32_e32 v39, 0xff800000\n"
"v_mov_b32_e32 v60, 0xff800000\n"
"v_mov_b32_e32 v61, 0xff800000\n"
"v_mov_b32_e32 v62, 0xff800000\n"
"v_mov_b32_e32 v63, 0xff800000\n"
"s_nop 1\n"
"s_branch .Lmk_x111r\n"
".Lmk_x112s:\n"
"s_nop 3\n"
"v_mov_b32_e32 v36, 0xff800000\n"
"v_mov_b32_e32 v37, 0xff800000\n"
"v_mov_b32_e32 v38, 0xff800000\n"
"v_mov_b32_e32 v39, 0xff800000\n"
"v_mov_b32_e32 v60, 0xff800000\n"
"v_mov_b32_e32 v61, 0xff800000\n"
"v_mov_b32_e32 v62, 0xff800000\n"
"v_mov_b32_e32 v63, 0xff800000\n"
"s_nop 1\n"
"s_branch .Lmk_x112r\n"
".Lmk_b107:\n"
"s_cmp_lg_u32 s2, 0\n"
"s_cbranch_scc1 .Lmk_k113\n"
"s_mov_b32 s2, 1\n"
"s_waitcnt vmcnt(0) lgkmcnt(0)\n"
"s_nop 7\n"
"s_mov_b32 s97, 11\n"
"v_add_u32_e32 v248, 0x3800, v15\n"
"v_add_u32_e32 v247, 0x3800, v252\n"
"ds_read_b128 v[36:39], v15 offset:24576\n"
"ds_read_b128 v[40:43], v252 offset:24832\n"
"s_waitcnt lgkmcnt(0)\n"
"v_mfma_f32_16x16x32_bf16 v[48:51], v[36:39], v[28:31], 0\n"
"v_mfma_f32_16x16x32_bf16 v[52:55], v[36:39], v[32:35], 0\n"
"v_mfma_f32_16x16x32_bf16 v[36:39], v[40:43], v[28:31], 0\n"
"v_mfma_f32_16x16x32_bf16 v[56:59], v[40:43], v[32:35], 0\n"
".Lmk_b107l:\n"
"ds_read_b128 v[40:43], v248 offset:8192\n"
"ds_read_b128 v[60:63], v247 offset:8448\n"
"s_cmp_gt_i32 s78, s97\n"
"s_cselect_b32 s96, 0xff800000, 0\n"
"v_mov_b32_e32 v72, s96\n"
"v_mov_b32_e32 v73, s96\n"
"v_mov_b32_e32 v74, s96\n"
"v_mov_b32_e32 v75, s96\n"
"v_subrev_u32_e32 v248, 0x800, v248\n"
"v_subrev_u32_e32 v247, 0x800, v247\n"
"s_waitcnt lgkmcnt(0)\n"
"v_mfma_f32_16x16x32_bf16 v[76:79], v[40:43], v[28:31], v[72:75]\n"
"v_mfma_f32_16x16x32_bf16 v[80:83], v[40:43], v[32:35], v[72:75]\n"
"v_mfma_f32_16x16x32_bf16 v[40:43], v[60:63], v[28:31], v[72:75]\n"
"v_mfma_f32_16x16x32_bf16 v[84:87], v[60:63], v[32:35], v[72:75]\n"
"s_nop 7\n"
"s_cmp_lg_u32 s97, 4\n"
"s_cbranch_scc1 .Lmk_b107m\n"
"v_cndmask_b32_e64 v76, v48, v76, s[20:21]\n"
"v_cndmask_b32_e64 v77, v49, v77, s[22:23]\n"
"v_cndmask_b32_e64 v78, v50, v78, s[24:25]\n"
"v_cndmask_b32_e64 v79, v51, v79, s[26:27]\n"
"v_cndmask_b32_e64 v40, v36, v40, s[28:29]\n"
"v_cndmask_b32_e64 v41, v37, v41, s[30:31]\n"
"v_cndmask_b32_e64 v42, v38, v42, s[32:33]\n"
"v_cndmask_b32_e64 v43, v39, v43, s[34:35]\n"
"v_cndmask_b32_e64 v80, v52, v80, s[36:37]\n"
"v_cndmask_b32_e64 v81, v53, v81, s[38:39]\n"
"v_cndmask_b32_e64 v82, v54, v82, s[40:41]\n"
"v_cndmask_b32_e64 v83, v55, v83, s[42:43]\n"
"v_cndmask_b32_e64 v84, v56, v84, s[44:45]\n"
"v_cndmask_b32_e64 v85, v57, v85, s[46:47]\n"
"v_cndmask_b32_e64 v86, v58, v86, s[48:49]\n"
"v_cndmask_b32_e64 v87, v59, v87, s[50:51]\n"
".Lmk_b107m:\n"
"v_max3_f32 v255, v255, v76, v77\n"
"v_max3_f32 v254, v254, v80, v81\n"
"v_max3_f32 v255, v255, v78, v79\n"
"v_max3_f32 v254, v254, v82, v83\n"
"v_max3_f32 v255, v255, v40, v41\n"
"v_max3_f32 v254, v254, v84, v85\n"
"v_max3_f32 v255, v255, v42, v43\n"
"v_max3_f32 v254, v254, v86, v87\n"
"s_sub_u32 s97, s97, 1\n"
"s_cmp_ge_i32 s97, 4\n"
"s_cbranch_scc1 .Lmk_b107l\n"
"v_mov_b32_e32 v248, v255\n"
"v_mov_b32_e32 v247, v254\n"
"s_nop 0\n"
"v_permlane16_swap_b32_e32 v255, v248\n"
"v_permlane16_swap_b32_e32 v254, v247\n"
"v_max_f32_e32 v255, v255, v248\n"
"v_max_f32_e32 v254, v254, v247\n"
"v_mov_b32_e32 v248, v255\n"
"v_mov_b32_e32 v247, v254\n"
"s_nop 0\n"
"v_permlane32_swap_b32_e32 v255, v248\n"
"v_permlane32_swap_b32_e32 v254, v247\n"
"v_max_f32_e32 v255, v255, v248\n"
"v_max_f32_e32 v254, v254, v247\n"
"v_mov_b32_e32 v250, v255\n"
"v_mov_b32_e32 v249, v254\n"
"s_branch .Lmk_a106\n"
".Lmk_x116s:\n"
"s_nop 3\n"
"v_mov_b32_e32 v116, 0xff800000\n"
"v_mov_b32_e32 v117, 0xff800000\n"
"v_mov_b32_e32 v118, 0xff800000\n"
"v_mov_b32_e32 v119, 0xff800000\n"
"v_mov_b32_e32 v120, 0xff800000\n"
"v_mov_b32_e32 v121, 0xff800000\n"
"v_mov_b32_e32 v122, 0xff800000\n"
"v_mov_b32_e32 v123, 0xff800000\n"
"s_nop 1\n"
"s_branch .Lmk_x116r\n"
".Lmk_x117s:\n"
"s_nop 3\n"
"v_mov_b32_e32 v116, 0xff800000\n"
"v_mov_b32_e32 v117, 0xff800000\n"
"v_mov_b32_e32 v118, 0xff800000\n"
"v_mov_b32_e32 v119, 0xff800000\n"
"v_mov_b32_e32 v120, 0xff800000\n"
"v_mov_b32_e32 v121, 0xff800000\n"
"v_mov_b32_e32 v122, 0xff800000\n"
"v_mov_b32_e32 v123, 0xff800000\n"
"s_nop 1\n"
"s_branch .Lmk_x117r\n"
".Lmk_x118s:\n"
"s_nop 3\n"
"v_mov_b32_e32 v116, 0xff800000\n"
"v_mov_b32_e32 v117, 0xff800000\n"
"v_mov_b32_e32 v118, 0xff800000\n"
"v_mov_b32_e32 v119, 0xff800000\n"
"v_mov_b32_e32 v120, 0xff800000\n"
"v_mov_b32_e32 v121, 0xff800000\n"
"v_mov_b32_e32 v122, 0xff800000\n"
"v_mov_b32_e32 v123, 0xff800000\n"
"s_nop 1\n"
"s_branch .Lmk_x118r\n"
".Lmk_h105s:\n"
";\n"
"s_mov_b32 s1, 0x10000\n"
".Lmk_h105l:\n"
"s_add_u32 m0, s77, 0x4000\n"
"s_add_u32 s95, s65, 0\n"
"buffer_load_dwordx4 v2, s[4:7], s95 offen sc1 lds\n"
"buffer_load_dwordx4 v2, s[4:7], s95 offen offset:1024 sc1 lds\n"
"s_add_u32 m0, s77, 0xc000\n"
"s_add_u32 s95, s65, 0x4000\n"
"buffer_load_dwordx4 v2, s[4:7], s95 offen sc1 lds\n"
"buffer_load_dwordx4 v2, s[4:7], s95 offen offset:1024 sc1 lds\n"
"s_waitcnt vmcnt(0)\n"
"ds_read_b128 v[80:83], v2 offset:16384\n"
"ds_read_b128 v[76:79], v2 offset:17408\n"
"ds_read_b128 v[72:75], v2 offset:49152\n"
"ds_read_b128 v[84:87], v2 offset:50176\n"
"s_waitcnt lgkmcnt(0)\n"
"s_waitcnt vmcnt(0)\n"
"v_max_u32_e32 v248, v80, v81\n"
"v_max3_u32 v248, v248, v82, v83\n"
"v_max3_u32 v248, v248, v76, v77\n"
"v_max3_u32 v248, v248, v78, v79\n"
"v_max3_u32 v248, v248, v72, v73\n"
"v_max3_u32 v248, v248, v74, v75\n"
"v_max3_u32 v248, v248, v84, v85\n"
"v_max3_u32 v248, v248, v86, v87\n"
"v_cmp_eq_u32_e32 vcc, -1, v248\n"
"s_cbranch_vccz .Lmk_h105k\n"
"s_sub_u32 s1, s1, 1\n"
"s_cbranch_scc0 .Lmk_h105l\n"
".Lmk_h105k:\n"
"s_branch .Lmk_h105r\n"
".Lmk_f87s:\n"
";\n"
"s_mov_b32 s1, 0x10000\n"
".Lmk_f87l:\n"
"buffer_load_dwordx2 v[246:247], v12, s[4:7], s96 offen sc1\n"
"s_waitcnt vmcnt(0)\n"
"v_cmp_ne_u64_e32 vcc, v[246:247], v[18:19]\n"
"s_cbranch_vccz .Lmk_f87r\n"
"s_sub_u32 s1, s1, 1\n"
"s_cbranch_scc0 .Lmk_f87l\n"
"s_branch .Lmk_f87r\n"
".Lmk_b115:\n"
"s_cmp_lg_u32 s2, 0\n"
"s_cbranch_scc1 .Lmk_k120\n"
"s_mov_b32 s2, 1\n"
"s_waitcnt vmcnt(0) lgkmcnt(0)\n"
"s_nop 7\n"
"s_mov_b32 s96, 7\n"
"v_add_u32_e32 v248, 0x3800, v15\n"
"v_add_u32_e32 v247, 0x3800, v252\n"
"ds_read_b128 v[140:143], v15 offset:16384\n"
"ds_read_b128 v[148:151], v252 offset:16640\n"
"s_waitcnt lgkmcnt(0)\n"
"v_mfma_f32_16x16x32_bf16 v[188:191], v[140:143], v[88:91], 0\n"
"v_mfma_f32_16x16x32_bf16 v[208:211], v[140:143], v[92:95], 0\n"
"v_mfma_f32_16x16x32_bf16 v[140:143], v[148:151], v[88:91], 0\n"
"v_mfma_f32_16x16x32_bf16 v[220:223], v[148:151], v[92:95], 0\n"
".Lmk_b115l:\n"
"ds_read_b128 v[148:151], v248\n"
"ds_read_b128 v[224:227], v247 offset:256\n"
"s_cmp_gt_i32 s78, s96\n"
"s_cselect_b32 s95, 0xff800000, 0\n"
"v_mov_b32_e32 v228, s95\n"
"v_mov_b32_e32 v229, s95\n"
"v_mov_b32_e32 v230, s95\n"
"v_mov_b32_e32 v231, s95\n"
"v_subrev_u32_e32 v248, 0x800, v248\n"
"v_subrev_u32_e32 v247, 0x800, v247\n"
"s_waitcnt lgkmcnt(0)\n"
"v_mfma_f32_16x16x32_bf16 v[232:235], v[148:151], v[88:91], v[228:231]\n"
"v_mfma_f32_16x16x32_bf16 v[236:239], v[148:151], v[92:95], v[228:231]\n"
"v_mfma_f32_16x16x32_bf16 v[148:151], v[224:227], v[88:91], v[228:231]\n"
"v_mfma_f32_16x16x32_bf16 v[240:243], v[224:227], v[92:95], v[228:231]\n"
"s_nop 7\n"
"s_cmp_lg_u32 s96, 0\n"
"s_cbranch_scc1 .Lmk_b115m\n"
"v_cndmask_b32_e64 v232, v188, v232, s[20:21]\n"
"v_cndmask_b32_e64 v233, v189, v233, s[22:23]\n"
"v_cndmask_b32_e64 v234, v190, v234, s[24:25]\n"
"v_cndmask_b32_e64 v235, v191, v235, s[26:27]\n"
"v_cndmask_b32_e64 v148, v140, v148, s[28:29]\n"
"v_cndmask_b32_e64 v149, v141, v149, s[30:31]\n"
"v_cndmask_b32_e64 v150, v142, v150, s[32:33]\n"
"v_cndmask_b32_e64 v151, v143, v151, s[34:35]\n"
"v_cndmask_b32_e64 v236, v208, v236, s[36:37]\n"
"v_cndmask_b32_e64 v237, v209, v237, s[38:39]\n"
"v_cndmask_b32_e64 v238, v210, v238, s[40:41]\n"
"v_cndmask_b32_e64 v239, v211, v239, s[42:43]\n"
"v_cndmask_b32_e64 v240, v220, v240, s[44:45]\n"
"v_cndmask_b32_e64 v241, v221, v241, s[46:47]\n"
"v_cndmask_b32_e64 v242, v222, v242, s[48:49]\n"
"v_cndmask_b32_e64 v243, v223, v243, s[50:51]\n"
".Lmk_b115m:\n"
"v_max3_f32 v255, v255, v232, v233\n"
"v_max3_f32 v254, v254, v236, v237\n"
"v_max3_f32 v255, v255, v234, v235\n"
"v_max3_f32 v254, v254, v238, v239\n"
"v_max3_f32 v255, v255, v148, v149\n"
"v_max3_f32 v254, v254, v240, v241\n"
"v_max3_f32 v255, v255, v150, v151\n"
"v_max3_f32 v254, v254, v242, v243\n"
"s_sub_u32 s96, s96, 1\n"
"s_cmp_ge_i32 s96, 0\n"
"s_cbranch_scc1 .Lmk_b115l\n"
"v_mov_b32_e32 v248, v255\n"
"v_mov_b32_e32 v247, v254\n"
"s_nop 0\n"
"v_permlane16_swap_b32_e32 v255, v248\n"
"v_permlane16_swap_b32_e32 v254, v247\n"
"v_max_f32_e32 v255, v255, v248\n"
"v_max_f32_e32 v254, v254, v247\n"
"v_mov_b32_e32 v248, v255\n"
"v_mov_b32_e32 v247, v254\n"
"s_nop 0\n"
"v_permlane32_swap_b32_e32 v255, v248\n"
"v_permlane32_swap_b32_e32 v254, v247\n"
"v_max_f32_e32 v255, v255, v248\n"
"v_max_f32_e32 v254, v254, v247\n"
"v_mov_b32_e32 v250, v255\n"
"v_mov_b32_e32 v249, v254\n"
"s_branch .Lmk_a114\n"
".Lmk_p121s:\n"
";\n"
"s_mov_b32 s1, 0x10000\n"
".Lmk_p121l:\n"
"s_add_u32 s97, s56, 0x40000\n"
"buffer_load_dwordx4 v[136:139], v0, s[4:7], s97 offen sc1\n"
"buffer_load_dwordx4 v[128:131], v0, s[4:7], s97 offen offset:1024 sc1\n"
"buffer_load_dwordx4 v[120:123], v0, s[4:7], s97 offen offset:2048 sc1\n"
"buffer_load_dwordx4 v[108:111], v0, s[4:7], s97 offen offset:3072 sc1\n"
"buffer_load_dwordx4 v[140:143], v1, s[4:7], s97 offen sc1\n"
"buffer_load_dwordx4 v[144:147], v1, s[4:7], s97 offen offset:1024 sc1\n"
"buffer_load_dwordx4 v[152:155], v1, s[4:7], s97 offen offset:2048 sc1\n"
"buffer_load_dwordx4 v[160:163], v1, s[4:7], s97 offen offset:3072 sc1\n"
"s_add_u32 s97, s57, 0x40000\n"
"buffer_load_dwordx4 v[216:219], v0, s[4:7], s97 offen sc1\n"
"buffer_load_dwordx4 v[212:215], v0, s[4:7], s97 offen offset:1024 sc1\n"
"buffer_load_dwordx4 v[184:187], v0, s[4:7], s97 offen offset:2048 sc1\n"
"buffer_load_dwordx4 v[96:99], v0, s[4:7], s97 offen offset:3072 sc1\n"
"buffer_load_dwordx4 v[156:159], v1, s[4:7], s97 offen sc1\n"
"buffer_load_dwordx4 v[100:103], v1, s[4:7], s97 offen offset:1024 sc1\n"
"buffer_load_dwordx4 v[112:115], v1, s[4:7], s97 offen offset:2048 sc1\n"
"buffer_load_dwordx4 v[124:127], v1, s[4:7], s97 offen offset:3072 sc1\n"
"s_add_u32 s97, s59, 0x40000\n"
"buffer_load_dwordx4 v[164:167], v0, s[4:7], s97 offen sc1\n"
"buffer_load_dwordx4 v[168:171], v0, s[4:7], s97 offen offset:1024 sc1\n"
"buffer_load_dwordx4 v[172:175], v0, s[4:7], s97 offen offset:2048 sc1\n"
"buffer_load_dwordx4 v[116:119], v0, s[4:7], s97 offen offset:3072 sc1\n"
"s_waitcnt vmcnt(0)\n"
"v_max_u32_e32 v255, v136, v137\n"
"v_max3_u32 v255, v255, v138, v139\n"
"v_max3_u32 v255, v255, v128, v129\n"
"v_max3_u32 v255, v255, v130, v131\n"
"v_max3_u32 v255, v255, v120, v121\n"
"v_max3_u32 v255, v255, v122, v123\n"
"v_max3_u32 v255, v255, v108, v109\n"
"v_max3_u32 v255, v255, v110, v111\n"
"v_max3_u32 v255, v255, v140, v141\n"
"v_max3_u32 v255, v255, v142, v143\n"
"v_max3_u32 v255, v255, v144, v145\n"
"v_max3_u32 v255, v255, v146, v147\n"
"v_max3_u32 v255, v255, v152, v153\n"
"v_max3_u32 v255, v255, v154, v155\n"
"v_max3_u32 v255, v255, v160, v161\n"
"v_max3_u32 v255, v255, v162, v163\n"
"v_cmp_eq_u32_e32 vcc, -1, v255\n"
"s_cbranch_vccz .Lmk_p121k\n"
"s_sub_u32 s1, s1, 1\n"
"s_cbranch_scc0 .Lmk_p121l\n"
".Lmk_p121k:\n"
"v_mfma_f32_16x16x32_bf16 v[88:91], a[128:131], v[136:139], 0\n"
"v_mfma_f32_16x16x32_bf16 v[92:95], a[132:135], v[136:139], 0\n"
"v_mfma_f32_16x16x32_bf16 v[104:107], a[136:139], v[136:139], 0\n"
"v_mfma_f32_16x16x32_bf16 v[148:151], a[140:143], v[136:139], 0\n"
"v_mfma_f32_16x16x32_bf16 v[88:91], a[144:147], v[128:131], v[88:91]\n"
"v_mfma_f32_16x16x32_bf16 v[92:95], a[148:151], v[128:131], v[92:95]\n"
"v_mfma_f32_16x16x32_bf16 v[104:107], a[152:155], v[128:131], v[104:107]\n"
"v_mfma_f32_16x16x32_bf16 v[148:151], a[156:159], v[128:131], v[148:151]\n"
"v_mfma_f32_16x16x32_bf16 v[88:91], a[160:163], v[120:123], v[88:91]\n"
"v_mfma_f32_16x16x32_bf16 v[92:95], a[164:167], v[120:123], v[92:95]\n"
"v_mfma_f32_16x16x32_bf16 v[104:107], a[168:171], v[120:123], v[104:107]\n"
"v_mfma_f32_16x16x32_bf16 v[148:151], a[172:175], v[120:123], v[148:151]\n"
"v_mfma_f32_16x16x32_bf16 v[88:91], a[176:179], v[108:111], v[88:91]\n"
"v_mfma_f32_16x16x32_bf16 v[92:95], a[180:183], v[108:111], v[92:95]\n"
"v_mfma_f32_16x16x32_bf16 v[104:107], a[184:187], v[108:111], v[104:107]\n"
"v_mfma_f32_16x16x32_bf16 v[148:151], a[188:191], v[108:111], v[148:151]\n"
"v_mfma_f32_16x16x32_bf16 v[88:91], a[192:195], v[140:143], v[88:91]\n"
"v_mfma_f32_16x16x32_bf16 v[92:95], a[196:199], v[140:143], v[92:95]\n"
"v_mfma_f32_16x16x32_bf16 v[104:107], a[200:203], v[140:143], v[104:107]\n"
"v_mfma_f32_16x16x32_bf16 v[148:151], a[204:207], v[140:143], v[148:151]\n"
"v_mfma_f32_16x16x32_bf16 v[88:91], a[208:211], v[144:147], v[88:91]\n"
"v_mfma_f32_16x16x32_bf16 v[92:95], a[212:215], v[144:147], v[92:95]\n"
"v_mfma_f32_16x16x32_bf16 v[104:107], a[216:219], v[144:147], v[104:107]\n"
"v_mfma_f32_16x16x32_bf16 v[148:151], a[220:223], v[144:147], v[148:151]\n"
"v_mfma_f32_16x16x32_bf16 v[88:91], a[224:227], v[152:155], v[88:91]\n"
"v_mfma_f32_16x16x32_bf16 v[92:95], a[228:231], v[152:155], v[92:95]\n"
"v_mfma_f32_16x16x32_bf16 v[104:107], a[232:235], v[152:155], v[104:107]\n"
"v_mfma_f32_16x16x32_bf16 v[148:151], a[236:239], v[152:155], v[148:151]\n"
"v_mfma_f32_16x16x32_bf16 v[88:91], a[240:243], v[160:163], v[88:91]\n"
"v_mfma_f32_16x16x32_bf16 v[92:95], a[244:247], v[160:163], v[92:95]\n"
"v_mfma_f32_16x16x32_bf16 v[104:107], a[248:251], v[160:163], v[104:107]\n"
"v_mfma_f32_16x16x32_bf16 v[148:151], a[252:255], v[160:163], v[148:151]\n"
"s_add_u32 s97, s58, 0x40000\n"
"v_mov_b32_e32 v255, 0x80000000\n"
"buffer_load_dwordx4 v[136:139], v0, s[4:7], s97 offen sc1\n"
"buffer_load_dwordx4 v[128:131], v0, s[4:7], s97 offen offset:1024 sc1\n"
"buffer_load_dwordx4 v[120:123], v0, s[4:7], s97 offen offset:2048 sc1\n"
"buffer_load_dwordx4 v[108:111], v0, s[4:7], s97 offen offset:3072 sc1\n"
"buffer_store_dword v255, v255, s[4:7], 0 offen\n"
"buffer_store_dword v255, v255, s[4:7], 0 offen\n"
"buffer_load_dwordx4 v[140:143], v1, s[4:7], s97 offen sc1\n"
"buffer_load_dwordx4 v[144:147], v1, s[4:7], s97 offen offset:1024 sc1\n"
"buffer_load_dwordx4 v[152:155], v1, s[4:7], s97 offen offset:2048 sc1\n"
"buffer_load_dwordx4 v[160:163], v1, s[4:7], s97 offen offset:3072 sc1\n"
"buffer_store_dword v255, v255, s[4:7], 0 offen\n"
"buffer_store_dword v255, v255, s[4:7], 0 offen\n"
"buffer_store_dword v255, v255, s[4:7], 0 offen\n"
"buffer_store_dword v255, v255, s[4:7], 0 offen\n"
"s_nop 7\n"
"s_branch .Lmk_p121r\n"
".Lmk_p122s:\n"
";\n"
"s_mov_b32 s1, 0x10000\n"
".Lmk_p122l:\n"
"s_add_u32 s98, s57, 0x40000\n"
"buffer_load_dwordx4 v[164:167], v0, s[4:7], s98 offen sc1\n"
"buffer_load_dwordx4 v[168:171], v0, s[4:7], s98 offen offset:1024 sc1\n"
"buffer_load_dwordx4 v[172:175], v0, s[4:7], s98 offen offset:2048 sc1\n"
"buffer_load_dwordx4 v[116:119], v0, s[4:7], s98 offen offset:3072 sc1\n"
"buffer_load_dwordx4 v[188:191], v1, s[4:7], s98 offen sc1\n"
"buffer_load_dwordx4 v[88:91], v1, s[4:7], s98 offen offset:1024 sc1\n"
"buffer_load_dwordx4 v[92:95], v1, s[4:7], s98 offen offset:2048 sc1\n"
"buffer_load_dwordx4 v[96:99], v1, s[4:7], s98 offen offset:3072 sc1\n"
"s_add_u32 s98, s58, 0x40000\n"
"buffer_load_dwordx4 v[136:139], v0, s[4:7], s98 offen sc1\n"
"buffer_load_dwordx4 v[128:131], v0, s[4:7], s98 offen offset:1024 sc1\n"
"buffer_load_dwordx4 v[120:123], v0, s[4:7], s98 offen offset:2048 sc1\n"
"buffer_load_dwordx4 v[108:111], v0, s[4:7], s98 offen offset:3072 sc1\n"
"buffer_load_dwordx4 v[140:143], v1, s[4:7], s98 offen sc1\n"
"buffer_load_dwordx4 v[144:147], v1, s[4:7], s98 offen offset:1024 sc1\n"
"buffer_load_dwordx4 v[152:155], v1, s[4:7], s98 offen offset:2048 sc1\n"
"buffer_load_dwordx4 v[160:163], v1, s[4:7], s98 offen offset:3072 sc1\n"
"s_waitcnt vmcnt(0)\n"
"v_max_u32_e32 v255, v164, v165\n"
"v_max3_u32 v255, v255, v166, v167\n"
"v_max3_u32 v255, v255, v168, v169\n"
"v_max3_u32 v255, v255, v170, v171\n"
"v_max3_u32 v255, v255, v172, v173\n"
"v_max3_u32 v255, v255, v174, v175\n"
"v_max3_u32 v255, v255, v116, v117\n"
"v_max3_u32 v255, v255, v118, v119\n"
"v_max3_u32 v255, v255, v188, v189\n"
"v_max3_u32 v255, v255, v190, v191\n"
"v_max3_u32 v255, v255, v88, v89\n"
"v_max3_u32 v255, v255, v90, v91\n"
"v_max3_u32 v255, v255, v92, v93\n"
"v_max3_u32 v255, v255, v94, v95\n"
"v_max3_u32 v255, v255, v96, v97\n"
"v_max3_u32 v255, v255, v98, v99\n"
"v_cmp_eq_u32_e32 vcc, -1, v255\n"
"s_cbranch_vccz .Lmk_p122k\n"
"s_sub_u32 s1, s1, 1\n"
"s_cbranch_scc0 .Lmk_p122l\n"
".Lmk_p122k:\n"
"v_mfma_f32_16x16x32_bf16 v[132:135], a[128:131], v[164:167], 0\n"
"v_mfma_f32_16x16x32_bf16 v[176:179], a[132:135], v[164:167], 0\n"
"v_mfma_f32_16x16x32_bf16 v[180:183], a[136:139], v[164:167], 0\n"
"v_mfma_f32_16x16x32_bf16 v[192:195], a[140:143], v[164:167], 0\n"
"v_mfma_f32_16x16x32_bf16 v[132:135], a[144:147], v[168:171], v[132:135]\n"
"v_mfma_f32_16x16x32_bf16 v[176:179], a[148:151], v[168:171], v[176:179]\n"
"v_mfma_f32_16x16x32_bf16 v[180:183], a[152:155], v[168:171], v[180:183]\n"
"v_mfma_f32_16x16x32_bf16 v[192:195], a[156:159], v[168:171], v[192:195]\n"
"v_mfma_f32_16x16x32_bf16 v[132:135], a[160:163], v[172:175], v[132:135]\n"
"v_mfma_f32_16x16x32_bf16 v[176:179], a[164:167], v[172:175], v[176:179]\n"
"v_mfma_f32_16x16x32_bf16 v[180:183], a[168:171], v[172:175], v[180:183]\n"
"v_mfma_f32_16x16x32_bf16 v[192:195], a[172:175], v[172:175], v[192:195]\n"
"v_mfma_f32_16x16x32_bf16 v[132:135], a[176:179], v[116:119], v[132:135]\n"
"v_mfma_f32_16x16x32_bf16 v[176:179], a[180:183], v[116:119], v[176:179]\n"
"v_mfma_f32_16x16x32_bf16 v[180:183], a[184:187], v[116:119], v[180:183]\n"
"v_mfma_f32_16x16x32_bf16 v[192:195], a[188:191], v[116:119], v[192:195]\n"
"v_mfma_f32_16x16x32_bf16 v[132:135], a[192:195], v[188:191], v[132:135]\n"
"v_mfma_f32_16x16x32_bf16 v[176:179], a[196:199], v[188:191], v[176:179]\n"
"v_mfma_f32_16x16x32_bf16 v[180:183], a[200:203], v[188:191], v[180:183]\n"
"v_mfma_f32_16x16x32_bf16 v[192:195], a[204:207], v[188:191], v[192:195]\n"
"v_mfma_f32_16x16x32_bf16 v[132:135], a[208:211], v[88:91], v[132:135]\n"
"v_mfma_f32_16x16x32_bf16 v[176:179], a[212:215], v[88:91], v[176:179]\n"
"v_mfma_f32_16x16x32_bf16 v[180:183], a[216:219], v[88:91], v[180:183]\n"
"v_mfma_f32_16x16x32_bf16 v[192:195], a[220:223], v[88:91], v[192:195]\n"
"v_mfma_f32_16x16x32_bf16 v[132:135], a[224:227], v[92:95], v[132:135]\n"
"v_mfma_f32_16x16x32_bf16 v[176:179], a[228:231], v[92:95], v[176:179]\n"
"v_mfma_f32_16x16x32_bf16 v[180:183], a[232:235], v[92:95], v[180:183]\n"
"v_mfma_f32_16x16x32_bf16 v[192:195], a[236:239], v[92:95], v[192:195]\n"
"v_mfma_f32_16x16x32_bf16 v[132:135], a[240:243], v[96:99], v[132:135]\n"
"v_mfma_f32_16x16x32_bf16 v[176:179], a[244:247], v[96:99], v[176:179]\n"
"v_mfma_f32_16x16x32_bf16 v[180:183], a[248:251], v[96:99], v[180:183]\n"
"v_mfma_f32_16x16x32_bf16 v[192:195], a[252:255], v[96:99], v[192:195]\n"
"s_add_u32 s98, s59, 0x40000\n"
"v_mov_b32_e32 v255, 0x80000000\n"
"buffer_load_dwordx4 v[164:167], v0, s[4:7], s98 offen sc1\n"
"buffer_load_dwordx4 v[168:171], v0, s[4:7], s98 offen offset:1024 sc1\n"
"buffer_load_dwordx4 v[172:175], v0, s[4:7], s98 offen offset:2048 sc1\n"
"buffer_load_dwordx4 v[116:119], v0, s[4:7], s98 offen offset:3072 sc1\n"
"buffer_load_dwordx4 v[188:191], v1, s[4:7], s98 offen sc1\n"
"buffer_load_dwordx4 v[88:91], v1, s[4:7], s98 offen offset:1024 sc1\n"
"buffer_load_dwordx4 v[92:95], v1, s[4:7], s98 offen offset:2048 sc1\n"
"buffer_load_dwordx4 v[96:99], v1, s[4:7], s98 offen offset:3072 sc1\n"
"s_nop 7\n"
"s_branch .Lmk_p122r\n"
".Lmk_q124s:\n"
";\n"
"s_mov_b32 s1, 0x10000\n"
".Lmk_q124l:\n"
"s_add_u32 s97, s59, 0x40000\n"
"buffer_load_dwordx4 v[164:167], v0, s[4:7], s97 offen sc1\n"
"buffer_load_dwordx4 v[168:171], v0, s[4:7], s97 offen offset:1024 sc1\n"
"buffer_load_dwordx4 v[172:175], v0, s[4:7], s97 offen offset:2048 sc1\n"
"buffer_load_dwordx4 v[116:119], v0, s[4:7], s97 offen offset:3072 sc1\n"
"buffer_load_dwordx4 v[188:191], v1, s[4:7], s97 offen sc1\n"
"buffer_load_dwordx4 v[88:91], v1, s[4:7], s97 offen offset:1024 sc1\n"
"buffer_load_dwordx4 v[92:95], v1, s[4:7], s97 offen offset:2048 sc1\n"
"buffer_load_dwordx4 v[96:99], v1, s[4:7], s97 offen offset:3072 sc1\n"
"s_add_u32 s97, s56, 0\n"
"buffer_load_dwordx4 v[120:123], v0, s[4:7], s97 offen sc1\n"
"buffer_load_dwordx4 v[108:111], v0, s[4:7], s97 offen offset:1024 sc1\n"
"buffer_load_dwordx4 v[128:131], v0, s[4:7], s97 offen offset:2048 sc1\n"
"buffer_load_dwordx4 v[132:135], v0, s[4:7], s97 offen offset:3072 sc1\n"
"s_waitcnt vmcnt(0)\n"
"v_max_u32_e32 v255, v164, v165\n"
"v_max3_u32 v255, v255, v166, v167\n"
"v_max3_u32 v255, v255, v168, v169\n"
"v_max3_u32 v255, v255, v170, v171\n"
"v_max3_u32 v255, v255, v172, v173\n"
"v_max3_u32 v255, v255, v174, v175\n"
"v_max3_u32 v255, v255, v116, v117\n"
"v_max3_u32 v255, v255, v118, v119\n"
"v_cmp_eq_u32_e32 vcc, -1, v255\n"
"s_cbranch_vccz .Lmk_q124k\n"
"s_sub_u32 s1, s1, 1\n"
"s_cbranch_scc0 .Lmk_q124l\n"
".Lmk_q124k:\n"
"s_branch .Lmk_q124r\n"
".Lmk_p123s:\n"
";\n"
"s_mov_b32 s1, 0x10000\n"
".Lmk_p123l:\n"
"s_add_u32 s97, s58, 0x40000\n"
"buffer_load_dwordx4 v[140:143], v0, s[4:7], s97 offen sc1\n"
"buffer_load_dwordx4 v[144:147], v0, s[4:7], s97 offen offset:1024 sc1\n"
"buffer_load_dwordx4 v[148:151], v0, s[4:7], s97 offen offset:2048 sc1\n"
"buffer_load_dwordx4 v[152:155], v0, s[4:7], s97 offen offset:3072 sc1\n"
"buffer_load_dwordx4 v[160:163], v1, s[4:7], s97 offen sc1\n"
"buffer_load_dwordx4 v[176:179], v1, s[4:7], s97 offen offset:1024 sc1\n"
"buffer_load_dwordx4 v[180:183], v1, s[4:7], s97 offen offset:2048 sc1\n"
"buffer_load_dwordx4 v[192:195], v1, s[4:7], s97 offen offset:3072 sc1\n"
"s_add_u32 s97, s59, 0x40000\n"
"buffer_load_dwordx4 v[164:167], v0, s[4:7], s97 offen sc1\n"
"buffer_load_dwordx4 v[168:171], v0, s[4:7], s97 offen offset:1024 sc1\n"
"buffer_load_dwordx4 v[172:175], v0, s[4:7], s97 offen offset:2048 sc1\n"
"buffer_load_dwordx4 v[116:119], v0, s[4:7], s97 offen offset:3072 sc1\n"
"buffer_load_dwordx4 v[188:191], v1, s[4:7], s97 offen sc1\n"
"buffer_load_dwordx4 v[88:91], v1, s[4:7], s97 offen offset:1024 sc1\n"
"buffer_load_dwordx4 v[92:95], v1, s[4:7], s97 offen offset:2048 sc1\n"
"buffer_load_dwordx4 v[96:99], v1, s[4:7], s97 offen offset:3072 sc1\n"
"s_add_u32 s97, s56, 0\n"
"buffer_load_dwordx4 v[120:123], v0, s[4:7], s97 offen sc1\n"
"buffer_load_dwordx4 v[108:111], v0, s[4:7], s97 offen offset:1024 sc1\n"
"buffer_load_dwordx4 v[128:131], v0, s[4:7], s97 offen offset:2048 sc1\n"
"buffer_load_dwordx4 v[132:135], v0, s[4:7], s97 offen offset:3072 sc1\n"
"buffer_load_dwordx4 v[136:139], v1, s[4:7], s97 offen sc1\n"
"s_waitcnt vmcnt(0)\n"
"v_max_u32_e32 v255, v140, v141\n"
"v_max3_u32 v255, v255, v142, v143\n"
"v_max3_u32 v255, v255, v144, v145\n"
"v_max3_u32 v255, v255, v146, v147\n"
"v_max3_u32 v255, v255, v148, v149\n"
"v_max3_u32 v255, v255, v150, v151\n"
"v_max3_u32 v255, v255, v152, v153\n"
"v_max3_u32 v255, v255, v154, v155\n"
"v_max3_u32 v255, v255, v160, v161\n"
"v_max3_u32 v255, v255, v162, v163\n"
"v_max3_u32 v255, v255, v176, v177\n"
"v_max3_u32 v255, v255, v178, v179\n"
"v_max3_u32 v255, v255, v180, v181\n"
"v_max3_u32 v255, v255, v182, v183\n"
"v_max3_u32 v255, v255, v192, v193\n"
"v_max3_u32 v255, v255, v194, v195\n"
"v_cmp_eq_u32_e32 vcc, -1, v255\n"
"s_cbranch_vccz .Lmk_p123k\n"
"s_sub_u32 s1, s1, 1\n"
"s_cbranch_scc0 .Lmk_p123l\n"
".Lmk_p123k:\n"
"v_mfma_f32_16x16x32_bf16 v[100:103], a[128:131], v[140:143], 0\n"
"v_mfma_f32_16x16x32_bf16 v[104:107], a[132:135], v[140:143], 0\n"
"v_mfma_f32_16x16x32_bf16 v[112:115], a[136:139], v[140:143], 0\n"
"v_mfma_f32_16x16x32_bf16 v[124:127], a[140:143], v[140:143], 0\n"
"v_mfma_f32_16x16x32_bf16 v[100:103], a[144:147], v[144:147], v[100:103]\n"
"v_mfma_f32_16x16x32_bf16 v[104:107], a[148:151], v[144:147], v[104:107]\n"
"v_mfma_f32_16x16x32_bf16 v[112:115], a[152:155], v[144:147], v[112:115]\n"
"v_mfma_f32_16x16x32_bf16 v[124:127], a[156:159], v[144:147], v[124:127]\n"
"v_mfma_f32_16x16x32_bf16 v[100:103], a[160:163], v[148:151], v[100:103]\n"
"v_mfma_f32_16x16x32_bf16 v[104:107], a[164:167], v[148:151], v[104:107]\n"
"v_mfma_f32_16x16x32_bf16 v[112:115], a[168:171], v[148:151], v[112:115]\n"
"v_mfma_f32_16x16x32_bf16 v[124:127], a[172:175], v[148:151], v[124:127]\n"
"v_mfma_f32_16x16x32_bf16 v[100:103], a[176:179], v[152:155], v[100:103]\n"
"v_mfma_f32_16x16x32_bf16 v[104:107], a[180:183], v[152:155], v[104:107]\n"
"v_mfma_f32_16x16x32_bf16 v[112:115], a[184:187], v[152:155], v[112:115]\n"
"v_mfma_f32_16x16x32_bf16 v[124:127], a[188:191], v[152:155], v[124:127]\n"
"v_mfma_f32_16x16x32_bf16 v[100:103], a[192:195], v[160:163], v[100:103]\n"
"v_mfma_f32_16x16x32_bf16 v[104:107], a[196:199], v[160:163], v[104:107]\n"
"v_mfma_f32_16x16x32_bf16 v[112:115], a[200:203], v[160:163], v[112:115]\n"
"v_mfma_f32_16x16x32_bf16 v[124:127], a[204:207], v[160:163], v[124:127]\n"
"v_mfma_f32_16x16x32_bf16 v[100:103], a[208:211], v[176:179], v[100:103]\n"
"v_mfma_f32_16x16x32_bf16 v[104:107], a[212:215], v[176:179], v[104:107]\n"
"v_mfma_f32_16x16x32_bf16 v[112:115], a[216:219], v[176:179], v[112:115]\n"
"v_mfma_f32_16x16x32_bf16 v[124:127], a[220:223], v[176:179], v[124:127]\n"
"v_mfma_f32_16x16x32_bf16 v[100:103], a[224:227], v[180:183], v[100:103]\n"
"v_mfma_f32_16x16x32_bf16 v[104:107], a[228:231], v[180:183], v[104:107]\n"
"v_mfma_f32_16x16x32_bf16 v[112:115], a[232:235], v[180:183], v[112:115]\n"
"v_mfma_f32_16x16x32_bf16 v[124:127], a[236:239], v[180:183], v[124:127]\n"
"v_mfma_f32_16x16x32_bf16 v[100:103], a[240:243], v[192:195], v[100:103]\n"
"v_mfma_f32_16x16x32_bf16 v[104:107], a[244:247], v[192:195], v[104:107]\n"
"v_mfma_f32_16x16x32_bf16 v[112:115], a[248:251], v[192:195], v[112:115]\n"
"v_mfma_f32_16x16x32_bf16 v[124:127], a[252:255], v[192:195], v[124:127]\n"
"s_nop 7\n"
"s_branch .Lmk_p123r\n"
".Lmk_q125s:\n"
";\n"
"s_mov_b32 s1, 0x10000\n"
".Lmk_q125l:\n"
"s_add_u32 s97, s59, 0x40000\n"
"buffer_load_dwordx4 v[188:191], v1, s[4:7], s97 offen sc1\n"
"buffer_load_dwordx4 v[88:91], v1, s[4:7], s97 offen offset:1024 sc1\n"
"buffer_load_dwordx4 v[92:95], v1, s[4:7], s97 offen offset:2048 sc1\n"
"buffer_load_dwordx4 v[96:99], v1, s[4:7], s97 offen offset:3072 sc1\n"
"s_add_u32 s97, s56, 0\n"
"buffer_load_dwordx4 v[120:123], v0, s[4:7], s97 offen sc1\n"
"buffer_load_dwordx4 v[108:111], v0, s[4:7], s97 offen offset:1024 sc1\n"
"buffer_load_dwordx4 v[128:131], v0, s[4:7], s97 offen offset:2048 sc1\n"
"buffer_load_dwordx4 v[132:135], v0, s[4:7], s97 offen offset:3072 sc1\n"
"buffer_load_dwordx4 v[136:139], v1, s[4:7], s97 offen sc1\n"
"buffer_load_dwordx4 v[140:143], v1, s[4:7], s97 offen offset:1024 sc1\n"
"buffer_load_dwordx4 v[100:103], v1, s[4:7], s97 offen offset:2048 sc1\n"
"s_waitcnt vmcnt(0)\n"
"v_max_u32_e32 v255, v188, v189\n"
"v_max3_u32 v255, v255, v190, v191\n"
"v_max3_u32 v255, v255, v88, v89\n"
"v_max3_u32 v255, v255, v90, v91\n"
"v_max3_u32 v255, v255, v92, v93\n"
"v_max3_u32 v255, v255, v94, v95\n"
"v_max3_u32 v255, v255, v96, v97\n"
"v_max3_u32 v255, v255, v98, v99\n"
"v_cmp_eq_u32_e32 vcc, -1, v255\n"
"s_cbranch_vccz .Lmk_q125k\n"
"s_sub_u32 s1, s1, 1\n"
"s_cbranch_scc0 .Lmk_q125l\n"
".Lmk_q125k:\n"
"s_branch .Lmk_q125r\n"
".Lmk_p126s:\n"
";\n"
"s_mov_b32 s1, 0x10000\n"
".Lmk_p126l:\n"
"s_add_u32 s97, s56, 0\n"
"buffer_load_dwordx4 v[116:119], v0, s[4:7], s97 offen sc1\n"
"buffer_load_dwordx4 v[120:123], v0, s[4:7], s97 offen offset:1024 sc1\n"
"buffer_load_dwordx4 v[168:171], v0, s[4:7], s97 offen offset:2048 sc1\n"
"buffer_load_dwordx4 v[128:131], v0, s[4:7], s97 offen offset:3072 sc1\n"
"buffer_load_dwordx4 v[132:135], v1, s[4:7], s97 offen sc1\n"
"buffer_load_dwordx4 v[136:139], v1, s[4:7], s97 offen offset:1024 sc1\n"
"buffer_load_dwordx4 v[140:143], v1, s[4:7], s97 offen offset:2048 sc1\n"
"buffer_load_dwordx4 v[176:179], v1, s[4:7], s97 offen offset:3072 sc1\n"
"s_add_u32 s97, s57, 0\n"
"buffer_load_dwordx4 v[112:115], v0, s[4:7], s97 offen sc1\n"
"buffer_load_dwordx4 v[144:147], v0, s[4:7], s97 offen offset:1024 sc1\n"
"buffer_load_dwordx4 v[164:167], v0, s[4:7], s97 offen offset:2048 sc1\n"
"buffer_load_dwordx4 v[92:95], v0, s[4:7], s97 offen offset:3072 sc1\n"
"buffer_load_dwordx4 v[124:127], v1, s[4:7], s97 offen sc1\n"
"buffer_load_dwordx4 v[152:155], v1, s[4:7], s97 offen offset:1024 sc1\n"
"buffer_load_dwordx4 v[160:163], v1, s[4:7], s97 offen offset:2048 sc1\n"
"buffer_load_dwordx4 v[172:175], v1, s[4:7], s97 offen offset:3072 sc1\n"
"s_waitcnt vmcnt(0)\n"
"v_max_u32_e32 v255, v116, v117\n"
"v_max3_u32 v255, v255, v118, v119\n"
"v_max3_u32 v255, v255, v120, v121\n"
"v_max3_u32 v255, v255, v122, v123\n"
"v_max3_u32 v255, v255, v168, v169\n"
"v_max3_u32 v255, v255, v170, v171\n"
"v_max3_u32 v255, v255, v128, v129\n"
"v_max3_u32 v255, v255, v130, v131\n"
"v_max3_u32 v255, v255, v132, v133\n"
"v_max3_u32 v255, v255, v134, v135\n"
"v_max3_u32 v255, v255, v136, v137\n"
"v_max3_u32 v255, v255, v138, v139\n"
"v_max3_u32 v255, v255, v140, v141\n"
"v_max3_u32 v255, v255, v142, v143\n"
"v_max3_u32 v255, v255, v176, v177\n"
"v_max3_u32 v255, v255, v178, v179\n"
"v_cmp_eq_u32_e32 vcc, -1, v255\n"
"s_cbranch_vccz .Lmk_p126k\n"
"s_sub_u32 s1, s1, 1\n"
"s_cbranch_scc0 .Lmk_p126l\n"
".Lmk_p126k:\n"
"v_mfma_f32_16x16x32_bf16 v[88:91], a[128:131], v[116:119], 0\n"
"v_mfma_f32_16x16x32_bf16 v[96:99], a[132:135], v[116:119], 0\n"
"v_mfma_f32_16x16x32_bf16 v[148:151], a[136:139], v[116:119], 0\n"
"v_mfma_f32_16x16x32_bf16 v[156:159], a[140:143], v[116:119], 0\n"
"v_mfma_f32_16x16x32_bf16 v[88:91], a[144:147], v[120:123], v[88:91]\n"
"v_mfma_f32_16x16x32_bf16 v[96:99], a[148:151], v[120:123], v[96:99]\n"
"v_mfma_f32_16x16x32_bf16 v[148:151], a[152:155], v[120:123], v[148:151]\n"
"v_mfma_f32_16x16x32_bf16 v[156:159], a[156:159], v[120:123], v[156:159]\n"
"v_mfma_f32_16x16x32_bf16 v[88:91], a[160:163], v[168:171], v[88:91]\n"
"v_mfma_f32_16x16x32_bf16 v[96:99], a[164:167], v[168:171], v[96:99]\n"
"v_mfma_f32_16x16x32_bf16 v[148:151], a[168:171], v[168:171], v[148:151]\n"
"v_mfma_f32_16x16x32_bf16 v[156:159], a[172:175], v[168:171], v[156:159]\n"
"v_mfma_f32_16x16x32_bf16 v[88:91], a[176:179], v[128:131], v[88:91]\n"
"v_mfma_f32_16x16x32_bf16 v[96:99], a[180:183], v[128:131], v[96:99]\n"
"v_mfma_f32_16x16x32_bf16 v[148:151], a[184:187], v[128:131], v[148:151]\n"
"v_mfma_f32_16x16x32_bf16 v[156:159], a[188:191], v[128:131], v[156:159]\n"
"v_mfma_f32_16x16x32_bf16 v[88:91], a[192:195], v[132:135], v[88:91]\n"
"v_mfma_f32_16x16x32_bf16 v[96:99], a[196:199], v[132:135], v[96:99]\n"
"v_mfma_f32_16x16x32_bf16 v[148:151], a[200:203], v[132:135], v[148:151]\n"
"v_mfma_f32_16x16x32_bf16 v[156:159], a[204:207], v[132:135], v[156:159]\n"
"v_mfma_f32_16x16x32_bf16 v[88:91], a[208:211], v[136:139], v[88:91]\n"
"v_mfma_f32_16x16x32_bf16 v[96:99], a[212:215], v[136:139], v[96:99]\n"
"v_mfma_f32_16x16x32_bf16 v[148:151], a[216:219], v[136:139], v[148:151]\n"
"v_mfma_f32_16x16x32_bf16 v[156:159], a[220:223], v[136:139], v[156:159]\n"
"v_mfma_f32_16x16x32_bf16 v[88:91], a[224:227], v[140:143], v[88:91]\n"
"v_mfma_f32_16x16x32_bf16 v[96:99], a[228:231], v[140:143], v[96:99]\n"
"v_mfma_f32_16x16x32_bf16 v[148:151], a[232:235], v[140:143], v[148:151]\n"
"v_mfma_f32_16x16x32_bf16 v[156:159], a[236:239], v[140:143], v[156:159]\n"
"v_mfma_f32_16x16x32_bf16 v[88:91], a[240:243], v[176:179], v[88:91]\n"
"v_mfma_f32_16x16x32_bf16 v[96:99], a[244:247], v[176:179], v[96:99]\n"
"v_mfma_f32_16x16x32_bf16 v[148:151], a[248:251], v[176:179], v[148:151]\n"
"v_mfma_f32_16x16x32_bf16 v[156:159], a[252:255], v[176:179], v[156:159]\n"
"s_add_u32 s97, s58, 0\n"
"v_mov_b32_e32 v255, 0x80000000\n"
"buffer_load_dwordx4 v[116:119], v0, s[4:7], s97 offen sc1\n"
"buffer_load_dwordx4 v[120:123], v0, s[4:7], s97 offen offset:1024 sc1\n"
"buffer_load_dwordx4 v[168:171], v0, s[4:7], s97 offen offset:2048 sc1\n"
"buffer_load_dwordx4 v[128:131], v0, s[4:7], s97 offen offset:3072 sc1\n"
"buffer_load_dwordx4 v[132:135], v1, s[4:7], s97 offen sc1\n"
"buffer_load_dwordx4 v[136:139], v1, s[4:7], s97 offen offset:1024 sc1\n"
"buffer_load_dwordx4 v[140:143], v1, s[4:7], s97 offen offset:2048 sc1\n"
"buffer_load_dwordx4 v[176:179], v1, s[4:7], s97 offen offset:3072 sc1\n"
"s_nop 7\n"
"s_branch .Lmk_p126r\n"
".Lmk_p127s:\n"
";\n"
"s_mov_b32 s1, 0x10000\n"
".Lmk_p127l:\n"
"s_add_u32 s96, s57, 0\n"
"buffer_load_dwordx4 v[104:107], v0, s[4:7], s96 offen sc1\n"
"buffer_load_dwordx4 v[112:115], v0, s[4:7], s96 offen offset:1024 sc1\n"
"buffer_load_dwordx4 v[124:127], v0, s[4:7], s96 offen offset:2048 sc1\n"
"buffer_load_dwordx4 v[200:203], v0, s[4:7], s96 offen offset:3072 sc1\n"
"buffer_load_dwordx4 v[204:207], v1, s[4:7], s96 offen sc1\n"
"buffer_load_dwordx4 v[208:211], v1, s[4:7], s96 offen offset:1024 sc1\n"
"buffer_load_dwordx4 v[212:215], v1, s[4:7], s96 offen offset:2048 sc1\n"
"buffer_load_dwordx4 v[216:219], v1, s[4:7], s96 offen offset:3072 sc1\n"
"s_add_u32 s96, s58, 0\n"
"buffer_load_dwordx4 v[116:119], v0, s[4:7], s96 offen sc1\n"
"buffer_load_dwordx4 v[120:123], v0, s[4:7], s96 offen offset:1024 sc1\n"
"buffer_load_dwordx4 v[168:171], v0, s[4:7], s96 offen offset:2048 sc1\n"
"buffer_load_dwordx4 v[128:131], v0, s[4:7], s96 offen offset:3072 sc1\n"
"buffer_load_dwordx4 v[132:135], v1, s[4:7], s96 offen sc1\n"
"buffer_load_dwordx4 v[136:139], v1, s[4:7], s96 offen offset:1024 sc1\n"
"buffer_load_dwordx4 v[140:143], v1, s[4:7], s96 offen offset:2048 sc1\n"
"buffer_load_dwordx4 v[176:179], v1, s[4:7], s96 offen offset:3072 sc1\n"
"s_add_u32 s96, s59, 0\n"
"buffer_load_dwordx4 v[188:191], v0, s[4:7], s96 offen sc1\n"
"buffer_load_dwordx4 v[144:147], v0, s[4:7], s96 offen offset:1024 sc1\n"
"buffer_load_dwordx4 v[148:151], v0, s[4:7], s96 offen offset:2048 sc1\n"
"buffer_load_dwordx4 v[156:159], v0, s[4:7], s96 offen offset:3072 sc1\n"
"buffer_load_dwordx4 v[164:167], v1, s[4:7], s96 offen sc1\n"
"buffer_load_dwordx4 v[152:155], v1, s[4:7], s96 offen offset:1024 sc1\n"
"buffer_load_dwordx4 v[196:199], v1, s[4:7], s96 offen offset:2048 sc1\n"
"s_waitcnt vmcnt(0)\n"
"v_max_u32_e32 v239, v104, v105\n"
"v_max3_u32 v239, v239, v106, v107\n"
"v_max3_u32 v239, v239, v112, v113\n"
"v_max3_u32 v239, v239, v114, v115\n"
"v_max3_u32 v239, v239, v124, v125\n"
"v_max3_u32 v239, v239, v126, v127\n"
"v_max3_u32 v239, v239, v200, v201\n"
"v_max3_u32 v239, v239, v202, v203\n"
"v_max3_u32 v239, v239, v204, v205\n"
"v_max3_u32 v239, v239, v206, v207\n"
"v_max3_u32 v239, v239, v208, v209\n"
"v_max3_u32 v239, v239, v210, v211\n"
"v_max3_u32 v239, v239, v212, v213\n"
"v_max3_u32 v239, v239, v214, v215\n"
"v_max3_u32 v239, v239, v216, v217\n"
"v_max3_u32 v239, v239, v218, v219\n"
"v_cmp_eq_u32_e32 vcc, -1, v239\n"
"s_cbranch_vccz .Lmk_p127k\n"
"s_sub_u32 s1, s1, 1\n"
"s_cbranch_scc0 .Lmk_p127l\n"
".Lmk_p127k:\n"
"v_mfma_f32_16x16x32_bf16 v[108:111], a[128:131], v[104:107], 0\n"
"v_mfma_f32_16x16x32_bf16 v[180:183], a[132:135], v[104:107], 0\n"
"v_mfma_f32_16x16x32_bf16 v[184:187], a[136:139], v[104:107], 0\n"
"v_mfma_f32_16x16x32_bf16 v[192:195], a[140:143], v[104:107], 0\n"
"v_mfma_f32_16x16x32_bf16 v[108:111], a[144:147], v[112:115], v[108:111]\n"
"v_mfma_f32_16x16x32_bf16 v[180:183], a[148:151], v[112:115], v[180:183]\n"
"v_mfma_f32_16x16x32_bf16 v[184:187], a[152:155], v[112:115], v[184:187]\n"
"v_mfma_f32_16x16x32_bf16 v[192:195], a[156:159], v[112:115], v[192:195]\n"
"v_mfma_f32_16x16x32_bf16 v[108:111], a[160:163], v[124:127], v[108:111]\n"
"v_mfma_f32_16x16x32_bf16 v[180:183], a[164:167], v[124:127], v[180:183]\n"
"v_mfma_f32_16x16x32_bf16 v[184:187], a[168:171], v[124:127], v[184:187]\n"
"v_mfma_f32_16x16x32_bf16 v[192:195], a[172:175], v[124:127], v[192:195]\n"
"v_mfma_f32_16x16x32_bf16 v[108:111], a[176:179], v[200:203], v[108:111]\n"
"v_mfma_f32_16x16x32_bf16 v[180:183], a[180:183], v[200:203], v[180:183]\n"
"v_mfma_f32_16x16x32_bf16 v[184:187], a[184:187], v[200:203], v[184:187]\n"
"v_mfma_f32_16x16x32_bf16 v[192:195], a[188:191], v[200:203], v[192:195]\n"
"v_mfma_f32_16x16x32_bf16 v[108:111], a[192:195], v[204:207], v[108:111]\n"
"v_mfma_f32_16x16x32_bf16 v[180:183], a[196:199], v[204:207], v[180:183]\n"
"v_mfma_f32_16x16x32_bf16 v[184:187], a[200:203], v[204:207], v[184:187]\n"
"v_mfma_f32_16x16x32_bf16 v[192:195], a[204:207], v[204:207], v[192:195]\n"
"v_mfma_f32_16x16x32_bf16 v[108:111], a[208:211], v[208:211], v[108:111]\n"
"v_mfma_f32_16x16x32_bf16 v[180:183], a[212:215], v[208:211], v[180:183]\n"
"v_mfma_f32_16x16x32_bf16 v[184:187], a[216:219], v[208:211], v[184:187]\n"
"v_mfma_f32_16x16x32_bf16 v[192:195], a[220:223], v[208:211], v[192:195]\n"
"v_mfma_f32_16x16x32_bf16 v[108:111], a[224:227], v[212:215], v[108:111]\n"
"v_mfma_f32_16x16x32_bf16 v[180:183], a[228:231], v[212:215], v[180:183]\n"
"v_mfma_f32_16x16x32_bf16 v[184:187], a[232:235], v[212:215], v[184:187]\n"
"v_mfma_f32_16x16x32_bf16 v[192:195], a[236:239], v[212:215], v[192:195]\n"
"v_mfma_f32_16x16x32_bf16 v[108:111], a[240:243], v[216:219], v[108:111]\n"
"v_mfma_f32_16x16x32_bf16 v[180:183], a[244:247], v[216:219], v[180:183]\n"
"v_mfma_f32_16x16x32_bf16 v[184:187], a[248:251], v[216:219], v[184:187]\n"
"v_mfma_f32_16x16x32_bf16 v[192:195], a[252:255], v[216:219], v[192:195]\n"
"s_nop 7\n"
"s_branch .Lmk_p127r\n"
".Lmk_q129s:\n"
";\n"
"s_mov_b32 s1, 0x10000\n"
".Lmk_q129l:\n"
"s_add_u32 s98, s59, 0\n"
"buffer_load_dwordx4 v[188:191], v0, s[4:7], s98 offen sc1\n"
"buffer_load_dwordx4 v[144:147], v0, s[4:7], s98 offen offset:1024 sc1\n"
"buffer_load_dwordx4 v[148:151], v0, s[4:7], s98 offen offset:2048 sc1\n"
"buffer_load_dwordx4 v[156:159], v0, s[4:7], s98 offen offset:3072 sc1\n"
"buffer_load_dwordx4 v[164:167], v1, s[4:7], s98 offen sc1\n"
"buffer_load_dwordx4 v[152:155], v1, s[4:7], s98 offen offset:1024 sc1\n"
"buffer_load_dwordx4 v[196:199], v1, s[4:7], s98 offen offset:2048 sc1\n"
"buffer_load_dwordx4 v[204:207], v1, s[4:7], s98 offen offset:3072 sc1\n"
"s_add_u32 s98, s52, 0x440000\n"
"buffer_load_dwordx4 v[104:107], v0, s[4:7], s98 offen sc1\n"
"buffer_load_dwordx4 v[108:111], v0, s[4:7], s98 offen offset:1024 sc1\n"
"buffer_load_dwordx4 v[112:115], v0, s[4:7], s98 offen offset:2048 sc1\n"
"buffer_load_dwordx4 v[116:119], v0, s[4:7], s98 offen offset:3072 sc1\n"
"buffer_load_dwordx4 v[120:123], v1, s[4:7], s98 offen sc1\n"
"buffer_load_dwordx4 v[124:127], v1, s[4:7], s98 offen offset:1024 sc1\n"
"s_waitcnt vmcnt(0)\n"
"v_max_u32_e32 v255, v188, v189\n"
"v_max3_u32 v255, v255, v190, v191\n"
"v_max3_u32 v255, v255, v144, v145\n"
"v_max3_u32 v255, v255, v146, v147\n"
"v_max3_u32 v255, v255, v148, v149\n"
"v_max3_u32 v255, v255, v150, v151\n"
"v_max3_u32 v255, v255, v156, v157\n"
"v_max3_u32 v255, v255, v158, v159\n"
"v_cmp_eq_u32_e32 vcc, -1, v255\n"
"s_cbranch_vccz .Lmk_q129k\n"
"s_sub_u32 s1, s1, 1\n"
"s_cbranch_scc0 .Lmk_q129l\n"
".Lmk_q129k:\n"
"s_branch .Lmk_q129r\n"
".Lmk_p128s:\n"
";\n"
"s_mov_b32 s1, 0x10000\n"
".Lmk_p128l:\n"
"s_add_u32 s98, s58, 0\n"
"buffer_load_dwordx4 v[132:135], v0, s[4:7], s98 offen sc1\n"
"buffer_load_dwordx4 v[136:139], v0, s[4:7], s98 offen offset:1024 sc1\n"
"buffer_load_dwordx4 v[140:143], v0, s[4:7], s98 offen offset:2048 sc1\n"
"buffer_load_dwordx4 v[168:171], v0, s[4:7], s98 offen offset:3072 sc1\n"
"buffer_load_dwordx4 v[176:179], v1, s[4:7], s98 offen sc1\n"
"buffer_load_dwordx4 v[180:183], v1, s[4:7], s98 offen offset:1024 sc1\n"
"buffer_load_dwordx4 v[184:187], v1, s[4:7], s98 offen offset:2048 sc1\n"
"buffer_load_dwordx4 v[192:195], v1, s[4:7], s98 offen offset:3072 sc1\n"
"s_add_u32 s98, s59, 0\n"
"buffer_load_dwordx4 v[188:191], v0, s[4:7], s98 offen sc1\n"
"buffer_load_dwordx4 v[144:147], v0, s[4:7], s98 offen offset:1024 sc1\n"
"buffer_load_dwordx4 v[148:151], v0, s[4:7], s98 offen offset:2048 sc1\n"
"buffer_load_dwordx4 v[156:159], v0, s[4:7], s98 offen offset:3072 sc1\n"
"buffer_load_dwordx4 v[164:167], v1, s[4:7], s98 offen sc1\n"
"buffer_load_dwordx4 v[152:155], v1, s[4:7], s98 offen offset:1024 sc1\n"
"buffer_load_dwordx4 v[196:199], v1, s[4:7], s98 offen offset:2048 sc1\n"
"buffer_load_dwordx4 v[204:207], v1, s[4:7], s98 offen offset:3072 sc1\n"
"s_add_u32 s98, s52, 0x440000\n"
"buffer_load_dwordx4 v[104:107], v0, s[4:7], s98 offen sc1\n"
"buffer_load_dwordx4 v[108:111], v0, s[4:7], s98 offen offset:1024 sc1\n"
"buffer_load_dwordx4 v[112:115], v0, s[4:7], s98 offen offset:2048 sc1\n"
"buffer_load_dwordx4 v[116:119], v0, s[4:7], s98 offen offset:3072 sc1\n"
"buffer_load_dwordx4 v[120:123], v1, s[4:7], s98 offen sc1\n"
"buffer_load_dwordx4 v[124:127], v1, s[4:7], s98 offen offset:1024 sc1\n"
"buffer_load_dwordx4 v[128:131], v1, s[4:7], s98 offen offset:2048 sc1\n"
"s_waitcnt vmcnt(0)\n"
"v_max_u32_e32 v255, v132, v133\n"
"v_max3_u32 v255, v255, v134, v135\n"
"v_max3_u32 v255, v255, v136, v137\n"
"v_max3_u32 v255, v255, v138, v139\n"
"v_max3_u32 v255, v255, v140, v141\n"
"v_max3_u32 v255, v255, v142, v143\n"
"v_max3_u32 v255, v255, v168, v169\n"
"v_max3_u32 v255, v255, v170, v171\n"
"v_max3_u32 v255, v255, v176, v177\n"
"v_max3_u32 v255, v255, v178, v179\n"
"v_max3_u32 v255, v255, v180, v181\n"
"v_max3_u32 v255, v255, v182, v183\n"
"v_max3_u32 v255, v255, v184, v185\n"
"v_max3_u32 v255, v255, v186, v187\n"
"v_max3_u32 v255, v255, v192, v193\n"
"v_max3_u32 v255, v255, v194, v195\n"
"v_cmp_eq_u32_e32 vcc, -1, v255\n"
"s_cbranch_vccz .Lmk_p128k\n"
"s_sub_u32 s1, s1, 1\n"
"s_cbranch_scc0 .Lmk_p128l\n"
".Lmk_p128k:\n"
"v_mfma_f32_16x16x32_bf16 v[160:163], a[128:131], v[132:135], 0\n"
"v_mfma_f32_16x16x32_bf16 v[172:175], a[132:135], v[132:135], 0\n"
"v_mfma_f32_16x16x32_bf16 v[200:203], a[136:139], v[132:135], 0\n"
"v_mfma_f32_16x16x32_bf16 v[208:211], a[140:143], v[132:135], 0\n"
"v_mfma_f32_16x16x32_bf16 v[160:163], a[144:147], v[136:139], v[160:163]\n"
"v_mfma_f32_16x16x32_bf16 v[172:175], a[148:151], v[136:139], v[172:175]\n"
"v_mfma_f32_16x16x32_bf16 v[200:203], a[152:155], v[136:139], v[200:203]\n"
"v_mfma_f32_16x16x32_bf16 v[208:211], a[156:159], v[136:139], v[208:211]\n"
"v_mfma_f32_16x16x32_bf16 v[160:163], a[160:163], v[140:143], v[160:163]\n"
"v_mfma_f32_16x16x32_bf16 v[172:175], a[164:167], v[140:143], v[172:175]\n"
"v_mfma_f32_16x16x32_bf16 v[200:203], a[168:171], v[140:143], v[200:203]\n"
"v_mfma_f32_16x16x32_bf16 v[208:211], a[172:175], v[140:143], v[208:211]\n"
"v_mfma_f32_16x16x32_bf16 v[160:163], a[176:179], v[168:171], v[160:163]\n"
"v_mfma_f32_16x16x32_bf16 v[172:175], a[180:183], v[168:171], v[172:175]\n"
"v_mfma_f32_16x16x32_bf16 v[200:203], a[184:187], v[168:171], v[200:203]\n"
"v_mfma_f32_16x16x32_bf16 v[208:211], a[188:191], v[168:171], v[208:211]\n"
"v_mfma_f32_16x16x32_bf16 v[160:163], a[192:195], v[176:179], v[160:163]\n"
"v_mfma_f32_16x16x32_bf16 v[172:175], a[196:199], v[176:179], v[172:175]\n"
"v_mfma_f32_16x16x32_bf16 v[200:203], a[200:203], v[176:179], v[200:203]\n"
"v_mfma_f32_16x16x32_bf16 v[208:211], a[204:207], v[176:179], v[208:211]\n"
"v_mfma_f32_16x16x32_bf16 v[160:163], a[208:211], v[180:183], v[160:163]\n"
"v_mfma_f32_16x16x32_bf16 v[172:175], a[212:215], v[180:183], v[172:175]\n"
"v_mfma_f32_16x16x32_bf16 v[200:203], a[216:219], v[180:183], v[200:203]\n"
"v_mfma_f32_16x16x32_bf16 v[208:211], a[220:223], v[180:183], v[208:211]\n"
"v_mfma_f32_16x16x32_bf16 v[160:163], a[224:227], v[184:187], v[160:163]\n"
"v_mfma_f32_16x16x32_bf16 v[172:175], a[228:231], v[184:187], v[172:175]\n"
"v_mfma_f32_16x16x32_bf16 v[200:203], a[232:235], v[184:187], v[200:203]\n"
"v_mfma_f32_16x16x32_bf16 v[208:211], a[236:239], v[184:187], v[208:211]\n"
"v_mfma_f32_16x16x32_bf16 v[160:163], a[240:243], v[192:195], v[160:163]\n"
"v_mfma_f32_16x16x32_bf16 v[172:175], a[244:247], v[192:195], v[172:175]\n"
"v_mfma_f32_16x16x32_bf16 v[200:203], a[248:251], v[192:195], v[200:203]\n"
"v_mfma_f32_16x16x32_bf16 v[208:211], a[252:255], v[192:195], v[208:211]\n"
"s_nop 7\n"
"s_branch .Lmk_p128r\n"
".Lmk_q130s:\n"
";\n"
"s_mov_b32 s1, 0x10000\n"
".Lmk_q130l:\n"
"s_add_u32 s97, s59, 0\n"
"buffer_load_dwordx4 v[164:167], v1, s[4:7], s97 offen sc1\n"
"buffer_load_dwordx4 v[152:155], v1, s[4:7], s97 offen offset:1024 sc1\n"
"buffer_load_dwordx4 v[196:199], v1, s[4:7], s97 offen offset:2048 sc1\n"
"buffer_load_dwordx4 v[204:207], v1, s[4:7], s97 offen offset:3072 sc1\n"
"s_add_u32 s97, s52, 0x440000\n"
"buffer_load_dwordx4 v[104:107], v0, s[4:7], s97 offen sc1\n"
"buffer_load_dwordx4 v[108:111], v0, s[4:7], s97 offen offset:1024 sc1\n"
"buffer_load_dwordx4 v[112:115], v0, s[4:7], s97 offen offset:2048 sc1\n"
"buffer_load_dwordx4 v[116:119], v0, s[4:7], s97 offen offset:3072 sc1\n"
"buffer_load_dwordx4 v[120:123], v1, s[4:7], s97 offen sc1\n"
"buffer_load_dwordx4 v[124:127], v1, s[4:7], s97 offen offset:1024 sc1\n"
"buffer_load_dwordx4 v[128:131], v1, s[4:7], s97 offen offset:2048 sc1\n"
"buffer_load_dwordx4 v[132:135], v1, s[4:7], s97 offen offset:3072 sc1\n"
"s_add_u32 s97, s53, 0x440000\n"
"buffer_load_dwordx4 v[136:139], v0, s[4:7], s97 offen sc1\n"
"s_waitcnt vmcnt(0)\n"
"v_max_u32_e32 v255, v164, v165\n"
"v_max3_u32 v255, v255, v166, v167\n"
"v_max3_u32 v255, v255, v152, v153\n"
"v_max3_u32 v255, v255, v154, v155\n"
"v_max3_u32 v255, v255, v196, v197\n"
"v_max3_u32 v255, v255, v198, v199\n"
"v_max3_u32 v255, v255, v204, v205\n"
"v_max3_u32 v255, v255, v206, v207\n"
"v_cmp_eq_u32_e32 vcc, -1, v255\n"
"s_cbranch_vccz .Lmk_q130k\n"
"s_sub_u32 s1, s1, 1\n"
"s_cbranch_scc0 .Lmk_q130l\n"
".Lmk_q130k:\n"
"s_branch .Lmk_q130r\n"
".Lmk_exit:\n"
"v_xor_b32_e32 v255, 32, v15\n"
"ds_read_b128 v[24:27], v2 offset:17408\n"
"ds_read_b128 v[32:35], v15 offset:16384\n"
"ds_read_b128 v[36:39], v255 offset:16640\n"
"v_xor_b32_e32 v255, 32, v17\n"
"ds_read_b128 v[44:47], v2 offset:16384\n"
"ds_read_b128 v[56:59], v17 offset:15360\n"
"ds_read_b128 v[60:63], v255 offset:15616\n"
"v_xor_b32_e32 v255, 32, v16\n"
"ds_read_b64_tr_b16 v[64:65], v16 offset:16384\n"
"ds_read_b64_tr_b16 v[66:67], v255 offset:16640\n"
"ds_read_b64_tr_b16 v[68:69], v255 offset:16384\n"
"ds_read_b64_tr_b16 v[70:71], v16 offset:16640\n"
"v_mov_b32_e32 v72, 0x3f803f80\n"
"v_mov_b32_e32 v73, 0x3f803f80\n"
"v_mov_b32_e32 v74, 0x3f803f80\n"
"v_mov_b32_e32 v75, 0x3f803f80\n"
"v_xor_b32_e32 v255, 32, v16\n"
"ds_read_b64_tr_b16 v[76:77], v16 offset:15360\n"
"ds_read_b64_tr_b16 v[78:79], v255 offset:15616\n"
"ds_read_b64_tr_b16 v[80:81], v255 offset:15360\n"
"ds_read_b64_tr_b16 v[82:83], v16 offset:15616\n"
"v_mov_b32_e32 v84, 0x3f803f80\n"
"v_mov_b32_e32 v85, 0x3f803f80\n"
"v_mov_b32_e32 v86, 0x3f803f80\n"
"v_mov_b32_e32 v87, 0x3f803f80\n"
"s_waitcnt lgkmcnt(11)\n"
"v_mfma_f32_16x16x32_bf16 v[28:31], v[32:35], v[24:27], v[28:31]\n"
"v_mfma_f32_16x16x32_bf16 v[40:43], v[36:39], v[24:27], v[40:43]\n"
"s_waitcnt lgkmcnt(8)\n"
"v_mfma_f32_16x16x32_bf16 v[48:51], v[56:59], v[44:47], v[48:51]\n"
"v_mfma_f32_16x16x32_bf16 v[52:55], v[60:63], v[44:47], v[52:55]\n"
"s_add_u32 s98, s56, 0x440000\n"
"buffer_load_dwordx4 v[24:27], v0, s[4:7], s98 offen sc1\n"
"buffer_load_dwordx4 v[32:35], v0, s[4:7], s98 offen offset:1024 sc1\n"
"buffer_load_dwordx4 v[36:39], v0, s[4:7], s98 offen offset:2048 sc1\n"
"buffer_load_dwordx4 v[44:47], v0, s[4:7], s98 offen offset:3072 sc1\n"
"v_max_f32_e32 v255, v28, v29\n"
"s_nop 0\n"
"v_max_f32_e32 v254, v48, v49\n"
"v_max3_f32 v255, v255, v30, v31\n"
"v_max3_f32 v254, v254, v50, v51\n"
"v_max3_f32 v255, v255, v40, v41\n"
"v_max3_f32 v254, v254, v52, v53\n"
"v_max3_f32 v255, v255, v42, v43\n"
"v_max3_f32 v254, v254, v54, v55\n"
"v_mov_b32_e32 v253, v255\n"
"v_mov_b32_e32 v252, v254\n"
"s_nop 0\n"
"v_permlane16_swap_b32_e32 v255, v253\n"
"v_permlane16_swap_b32_e32 v254, v252\n"
"v_max_f32_e32 v255, v255, v253\n"
"v_max_f32_e32 v254, v254, v252\n"
"v_mov_b32_e32 v253, v255\n"
"v_mov_b32_e32 v252, v254\n"
"s_nop 0\n"
"v_permlane32_swap_b32_e32 v255, v253\n"
"v_permlane32_swap_b32_e32 v254, v252\n"
"v_max_f32_e32 v255, v255, v253\n"
"v_max_f32_e32 v254, v254, v252\n"
"buffer_load_dwordx4 v[56:59], v1, s[4:7], s98 offen sc1\n"
"v_sub_f32_e32 v28, v28, v255\n"
"v_sub_f32_e32 v29, v29, v255\n"
"v_sub_f32_e32 v30, v30, v255\n"
"v_sub_f32_e32 v31, v31, v255\n"
"v_sub_f32_e32 v40, v40, v255\n"
"v_sub_f32_e32 v41, v41, v255\n"
"v_sub_f32_e32 v42, v42, v255\n"
"buffer_load_dwordx4 v[60:63], v1, s[4:7], s98 offen offset:1024 sc1\n"
"v_sub_f32_e32 v43, v43, v255\n"
"v_exp_f32_e32 v28, v28\n"
"v_exp_f32_e32 v29, v29\n"
"v_exp_f32_e32 v30, v30\n"
"buffer_load_dwordx4 v[88:91], v1, s[4:7], s98 offen offset:2048 sc1\n"
"v_exp_f32_e32 v31, v31\n"
"v_exp_f32_e32 v40, v40\n"
"v_exp_f32_e32 v41, v41\n"
"v_exp_f32_e32 v42, v42\n"
"buffer_load_dwordx4 v[92:95], v1, s[4:7], s98 offen offset:3072 sc1\n"
"v_exp_f32_e32 v43, v43\n"
"v_cvt_pk_bf16_f32 v96, v28, v29\n"
"v_cvt_pk_bf16_f32 v97, v30, v31\n"
"v_cvt_pk_bf16_f32 v98, v40, v41\n"
"v_cvt_pk_bf16_f32 v99, v42, v43\n"
"s_add_u32 s98, s57, 0x440000\n"
"buffer_load_dwordx4 v[100:103], v0, s[4:7], s98 offen sc1\n"
"s_waitcnt lgkmcnt(4)\n"
"v_sub_f32_e32 v48, v48, v254\n"
"v_sub_f32_e32 v49, v49, v254\n"
"v_sub_f32_e32 v50, v50, v254\n"
"v_sub_f32_e32 v51, v51, v254\n"
"v_sub_f32_e32 v52, v52, v254\n"
"v_sub_f32_e32 v53, v53, v254\n"
"v_sub_f32_e32 v54, v54, v254\n"
"v_mfma_f32_16x16x32_bf16 v[104:107], v[72:75], v[96:99], 0\n"
"buffer_load_dwordx4 v[72:75], v0, s[4:7], s98 offen offset:1024 sc1\n"
"v_sub_f32_e32 v55, v55, v254\n"
"v_exp_f32_e32 v48, v48\n"
"v_exp_f32_e32 v49, v49\n"
"v_exp_f32_e32 v50, v50\n"
"v_mfma_f32_16x16x32_bf16 v[108:111], v[64:67], v[96:99], 0\n"
"buffer_load_dwordx4 v[64:67], v0, s[4:7], s98 offen offset:2048 sc1\n"
"v_exp_f32_e32 v51, v51\n"
"v_exp_f32_e32 v52, v52\n"
"v_exp_f32_e32 v53, v53\n"
"v_exp_f32_e32 v54, v54\n"
"buffer_load_dwordx4 v[112:115], v0, s[4:7], s98 offen offset:3072 sc1\n"
"v_mfma_f32_16x16x32_bf16 v[116:119], v[68:71], v[96:99], 0\n"
"v_exp_f32_e32 v55, v55\n"
"v_cvt_pk_bf16_f32 v68, v48, v49\n"
"v_cvt_pk_bf16_f32 v69, v50, v51\n"
"v_cvt_pk_bf16_f32 v70, v52, v53\n"
"v_cvt_pk_bf16_f32 v71, v54, v55\n"
"buffer_load_dwordx4 v[96:99], v1, s[4:7], s98 offen sc1\n"
"s_waitcnt lgkmcnt(0)\n"
"v_rcp_f32_e32 v104, v104\n"
"s_nop 0\n"
"v_mul_f32_e32 v108, v108, v104\n"
"v_mul_f32_e32 v109, v109, v104\n"
"v_mfma_f32_16x16x32_bf16 v[120:123], v[84:87], v[68:71], 0\n"
"buffer_load_dwordx4 v[84:87], v1, s[4:7], s98 offen offset:1024 sc1\n"
"v_mul_f32_e32 v110, v110, v104\n"
"v_mul_f32_e32 v111, v111, v104\n"
"v_mul_f32_e32 v116, v116, v104\n"
"v_mfma_f32_16x16x32_bf16 v[124:127], v[76:79], v[68:71], 0\n"
"buffer_load_dwordx4 v[76:79], v1, s[4:7], s98 offen offset:2048 sc1\n"
"v_mul_f32_e32 v117, v117, v104\n"
"v_mul_f32_e32 v118, v118, v104\n"
"v_mul_f32_e32 v119, v119, v104\n"
"v_cvt_pk_bf16_f32 v104, v108, v109\n"
"buffer_load_dwordx4 v[128:131], v1, s[4:7], s98 offen offset:3072 sc1\n"
"v_mfma_f32_16x16x32_bf16 v[132:135], v[80:83], v[68:71], 0\n"
"v_cvt_pk_bf16_f32 v105, v110, v111\n"
"v_cvt_pk_bf16_f32 v106, v116, v117\n"
"v_cvt_pk_bf16_f32 v107, v118, v119\n"
"s_add_u32 s98, s60, 0x408000\n"
"buffer_store_dwordx4 v[104:107], v0, s[4:7], s98 offen sc1\n"
"v_rcp_f32_e32 v120, v120\n"
"s_nop 0\n"
"v_mul_f32_e32 v124, v124, v120\n"
"v_mul_f32_e32 v125, v125, v120\n"
"v_mul_f32_e32 v126, v126, v120\n"
"v_mul_f32_e32 v127, v127, v120\n"
"v_mul_f32_e32 v132, v132, v120\n"
"v_mul_f32_e32 v133, v133, v120\n"
"v_mul_f32_e32 v134, v134, v120\n"
"v_mul_f32_e32 v135, v135, v120\n"
"v_cvt_pk_bf16_f32 v68, v124, v125\n"
"v_cvt_pk_bf16_f32 v69, v126, v127\n"
"v_cvt_pk_bf16_f32 v70, v132, v133\n"
"v_cvt_pk_bf16_f32 v71, v134, v135\n"
"s_add_u32 s98, s60, 0x400000\n"
"buffer_store_dwordx4 v[68:71], v0, s[4:7], s98 offen sc1\n"
"s_waitcnt vmcnt(17)\n"
"; @B G2 b.0\n"
"s_nop 0\n"
"v_mfma_f32_16x16x32_bf16 v[68:71], a[128:131], v[24:27], 0\n"
"s_add_u32 s98, s58, 0x440000\n"
"buffer_load_dwordx4 v[80:83], v0, s[4:7], s98 offen sc1\n"
"v_mfma_f32_16x16x32_bf16 v[104:107], a[132:135], v[24:27], 0\n"
"v_mfma_f32_16x16x32_bf16 v[108:111], a[136:139], v[24:27], 0\n"
"v_mfma_f32_16x16x32_bf16 v[116:119], a[140:143], v[24:27], 0\n"
"s_waitcnt vmcnt(17)\n"
"v_mfma_f32_16x16x32_bf16 v[68:71], a[144:147], v[32:35], v[68:71]\n"
"buffer_load_dwordx4 v[24:27], v0, s[4:7], s98 offen offset:1024 sc1\n"
"v_mfma_f32_16x16x32_bf16 v[104:107], a[148:151], v[32:35], v[104:107]\n"
"v_mfma_f32_16x16x32_bf16 v[108:111], a[152:155], v[32:35], v[108:111]\n"
"v_mfma_f32_16x16x32_bf16 v[116:119], a[156:159], v[32:35], v[116:119]\n"
"s_waitcnt vmcnt(17)\n"
"v_mfma_f32_16x16x32_bf16 v[68:71], a[160:163], v[36:39], v[68:71]\n"
"buffer_load_dwordx4 v[32:35], v0, s[4:7], s98 offen offset:2048 sc1\n"
"v_mfma_f32_16x16x32_bf16 v[104:107], a[164:167], v[36:39], v[104:107]\n"
"v_mfma_f32_16x16x32_bf16 v[108:111], a[168:171], v[36:39], v[108:111]\n"
"v_mfma_f32_16x16x32_bf16 v[116:119], a[172:175], v[36:39], v[116:119]\n"
"s_waitcnt vmcnt(17)\n"
"v_mfma_f32_16x16x32_bf16 v[68:71], a[176:179], v[44:47], v[68:71]\n"
"buffer_load_dwordx4 v[36:39], v0, s[4:7], s98 offen offset:3072 sc1\n"
"v_mfma_f32_16x16x32_bf16 v[104:107], a[180:183], v[44:47], v[104:107]\n"
"v_mfma_f32_16x16x32_bf16 v[108:111], a[184:187], v[44:47], v[108:111]\n"
"v_mfma_f32_16x16x32_bf16 v[116:119], a[188:191], v[44:47], v[116:119]\n"
"s_waitcnt vmcnt(17)\n"
"v_mfma_f32_16x16x32_bf16 v[68:71], a[192:195], v[56:59], v[68:71]\n"
"buffer_load_dwordx4 v[44:47], v1, s[4:7], s98 offen sc1\n"
"v_mfma_f32_16x16x32_bf16 v[104:107], a[196:199], v[56:59], v[104:107]\n"
"v_mfma_f32_16x16x32_bf16 v[108:111], a[200:203], v[56:59], v[108:111]\n"
"v_mfma_f32_16x16x32_bf16 v[116:119], a[204:207], v[56:59], v[116:119]\n"
"s_waitcnt vmcnt(17)\n"
"v_mfma_f32_16x16x32_bf16 v[68:71], a[208:211], v[60:63], v[68:71]\n"
"buffer_load_dwordx4 v[56:59], v1, s[4:7], s98 offen offset:1024 sc1\n"
"v_mfma_f32_16x16x32_bf16 v[104:107], a[212:215], v[60:63], v[104:107]\n"
"v_mfma_f32_16x16x32_bf16 v[108:111], a[216:219], v[60:63], v[108:111]\n"
"v_mfma_f32_16x16x32_bf16 v[116:119], a[220:223], v[60:63], v[116:119]\n"
"s_waitcnt vmcnt(17)\n"
"v_mfma_f32_16x16x32_bf16 v[68:71], a[224:227], v[88:91], v[68:71]\n"
"buffer_load_dwordx4 v[60:63], v1, s[4:7], s98 offen offset:2048 sc1\n"
"v_mfma_f32_16x16x32_bf16 v[104:107], a[228:231], v[88:91], v[104:107]\n"
"v_mfma_f32_16x16x32_bf16 v[108:111], a[232:235], v[88:91], v[108:111]\n"
"v_mfma_f32_16x16x32_bf16 v[116:119], a[236:239], v[88:91], v[116:119]\n"
"s_waitcnt vmcnt(17)\n"
"v_mfma_f32_16x16x32_bf16 v[68:71], a[240:243], v[92:95], v[68:71]\n"
"buffer_load_dwordx4 v[88:91], v1, s[4:7], s98 offen offset:3072 sc1\n"
"v_mfma_f32_16x16x32_bf16 v[104:107], a[244:247], v[92:95], v[104:107]\n"
"v_mfma_f32_16x16x32_bf16 v[108:111], a[248:251], v[92:95], v[108:111]\n"
"s_add_u32 s98, s59, 0x440000\n"
"v_mfma_f32_16x16x32_bf16 v[116:119], a[252:255], v[92:95], v[116:119]\n"
"s_waitcnt vmcnt(16)\n"
"; @B G2 b.1\n"
"v_mfma_f32_16x16x32_bf16 v[92:95], a[128:131], v[100:103], 0\n"
"s_nop 0\n"
"v_cmp_u_f32_e32 vcc, v68, v68\n"
"v_mfma_f32_16x16x32_bf16 v[120:123], a[132:135], v[100:103], 0\n"
"s_cbranch_vccnz .Lmk_p64s\n"
".Lmk_p64r:\n"
"v_mfma_f32_16x16x32_bf16 v[124:127], a[136:139], v[100:103], 0\n"
"buffer_load_dwordx4 v[132:135], v0, s[4:7], s98 offen sc1\n"
"v_mfma_f32_16x16x32_bf16 v[136:139], a[140:143], v[100:103], 0\n"
"v_mfma_f32_16x16x32_bf16 v[92:95], a[144:147], v[72:75], v[92:95]\n"
"ds_write_b128 v3, v[68:71]\n"
"v_mfma_f32_16x16x32_bf16 v[120:123], a[148:151], v[72:75], v[120:123]\n"
"v_mfma_f32_16x16x32_bf16 v[124:127], a[152:155], v[72:75], v[124:127]\n"
"ds_write_b128 v3, v[104:107] offset:1024\n"
"v_mfma_f32_16x16x32_bf16 v[136:139], a[156:159], v[72:75], v[136:139]\n"
"s_waitcnt vmcnt(15)\n"
"buffer_load_dwordx4 v[68:71], v0, s[4:7], s98 offen offset:1024 sc1\n"
"v_mfma_f32_16x16x32_bf16 v[92:95], a[160:163], v[64:67], v[92:95]\n"
"ds_write_b128 v3, v[108:111] offset:2048\n"
"v_mfma_f32_16x16x32_bf16 v[120:123], a[164:167], v[64:67], v[120:123]\n"
"v_mfma_f32_16x16x32_bf16 v[124:127], a[168:171], v[64:67], v[124:127]\n"
"ds_write_b128 v3, v[116:119] offset:3072\n"
"v_mfma_f32_16x16x32_bf16 v[136:139], a[172:175], v[64:67], v[136:139]\n"
"buffer_load_dwordx4 v[64:67], v0, s[4:7], s98 offen offset:2048 sc1\n"
"v_mfma_f32_16x16x32_bf16 v[92:95], a[176:179], v[112:115], v[92:95]\n"
"v_mfma_f32_16x16x32_bf16 v[120:123], a[180:183], v[112:115], v[120:123]\n"
"ds_read_b128 v[72:75], v2 offset:8192\n"
"ds_read_b128 v[100:103], v2 offset:9216\n"
"v_mfma_f32_16x16x32_bf16 v[124:127], a[184:187], v[112:115], v[124:127]\n"
"v_mfma_f32_16x16x32_bf16 v[136:139], a[188:191], v[112:115], v[136:139]\n"
"s_waitcnt vmcnt(15)\n"
"buffer_load_dwordx4 v[104:107], v0, s[4:7], s98 offen offset:3072 sc1\n"
"v_mfma_f32_16x16x32_bf16 v[92:95], a[192:195], v[96:99], v[92:95]\n"
"v_mfma_f32_16x16x32_bf16 v[120:123], a[196:199], v[96:99], v[120:123]\n"
"v_mfma_f32_16x16x32_bf16 v[124:127], a[200:203], v[96:99], v[124:127]\n"
"s_waitcnt lgkmcnt(0)\n"
"v_lshlrev_b32_e32 v255, 16, v72\n"
"v_and_b32_e32 v254, 0xffff0000, v72\n"
"v_mfma_f32_16x16x32_bf16 v[136:139], a[204:207], v[96:99], v[136:139]\n"
"s_waitcnt lgkmcnt(0)\n"
"s_barrier\n"
"v_mfma_f32_16x16x32_bf16 v[92:95], a[208:211], v[84:87], v[92:95]\n"
"buffer_load_dwordx4 v[96:99], v1, s[4:7], s98 offen sc1\n"
"v_mfma_f32_16x16x32_bf16 v[120:123], a[212:215], v[84:87], v[120:123]\n"
"ds_read_b128 v[108:111], v4\n"
"ds_read_b128 v[112:115], v4 offset:1024\n"
"v_mfma_f32_16x16x32_bf16 v[124:127], a[216:219], v[84:87], v[124:127]\n"
"ds_read_b128 v[116:119], v4 offset:2048\n"
"ds_read_b128 v[140:143], v4 offset:3072\n"
"v_mfma_f32_16x16x32_bf16 v[136:139], a[220:223], v[84:87], v[136:139]\n"
"s_waitcnt vmcnt(15)\n"
"v_lshlrev_b32_e32 v253, 16, v73\n"
"v_and_b32_e32 v252, 0xffff0000, v73\n"
"v_mfma_f32_16x16x32_bf16 v[92:95], a[224:227], v[76:79], v[92:95]\n"
"buffer_load_dwordx4 v[84:87], v1, s[4:7], s98 offen offset:1024 sc1\n"
"v_mfma_f32_16x16x32_bf16 v[120:123], a[228:231], v[76:79], v[120:123]\n"
"v_lshlrev_b32_e32 v251, 16, v74\n"
"v_and_b32_e32 v250, 0xffff0000, v74\n"
"v_mfma_f32_16x16x32_bf16 v[124:127], a[232:235], v[76:79], v[124:127]\n"
"v_lshlrev_b32_e32 v249, 16, v75\n"
"v_and_b32_e32 v248, 0xffff0000, v75\n"
"v_mfma_f32_16x16x32_bf16 v[136:139], a[236:239], v[76:79], v[136:139]\n"
"v_lshlrev_b32_e32 v247, 16, v100\n"
"v_and_b32_e32 v246, 0xffff0000, v100\n"
"v_mfma_f32_16x16x32_bf16 v[92:95], a[240:243], v[128:131], v[92:95]\n"
"buffer_load_dwordx4 v[72:75], v1, s[4:7], s98 offen offset:2048 sc1\n"
"v_mfma_f32_16x16x32_bf16 v[120:123], a[244:247], v[128:131], v[120:123]\n"
"v_lshlrev_b32_e32 v245, 16, v101\n"
"v_and_b32_e32 v244, 0xffff0000, v101\n"
"v_mfma_f32_16x16x32_bf16 v[124:127], a[248:251], v[128:131], v[124:127]\n"
"v_lshlrev_b32_e32 v243, 16, v102\n"
"v_and_b32_e32 v242, 0xffff0000, v102\n"
"v_mfma_f32_16x16x32_bf16 v[136:139], a[252:255], v[128:131], v[136:139]\n"
"s_waitcnt vmcnt(13)\n"
"v_lshlrev_b32_e32 v241, 16, v103\n"
"v_and_b32_e32 v240, 0xffff0000, v103\n"
"; @B G2 b.2\n"
"v_mfma_f32_16x16x32_bf16 v[76:79], a[128:131], v[80:83], 0\n"
"s_waitcnt lgkmcnt(0)\n"
"v_cmp_u_f32_e32 vcc, v92, v92\n"
"v_add_f32_e32 v108, v108, v255\n"
"v_mfma_f32_16x16x32_bf16 v[100:103], a[132:135], v[80:83], 0\n"
"s_cbranch_vccnz .Lmk_p65s\n"
".Lmk_p65r:\n"
"v_add_f32_e32 v109, v109, v254\n"
"v_mfma_f32_16x16x32_bf16 v[128:131], a[136:139], v[80:83], 0\n"
"buffer_load_dwordx4 v[144:147], v1, s[4:7], s98 offen offset:3072 sc1\n"
"v_mfma_f32_16x16x32_bf16 v[148:151], a[140:143], v[80:83], 0\n"
"v_add_f32_e32 v110, v110, v253\n"
"v_add_f32_e32 v111, v111, v252\n"
"v_mfma_f32_16x16x32_bf16 v[76:79], a[144:147], v[24:27], v[76:79]\n"
"ds_write_b128 v3, v[92:95] offset:6144\n"
"v_mfma_f32_16x16x32_bf16 v[100:103], a[148:151], v[24:27], v[100:103]\n"
"v_add_f32_e32 v112, v112, v251\n"
"v_add_f32_e32 v113, v113, v250\n"
"v_mfma_f32_16x16x32_bf16 v[128:131], a[152:155], v[24:27], v[128:131]\n"
"ds_write_b128 v3, v[120:123] offset:7168\n"
"v_mfma_f32_16x16x32_bf16 v[148:151], a[156:159], v[24:27], v[148:151]\n"
"s_waitcnt vmcnt(12)\n"
"v_add_f32_e32 v114, v114, v249\n"
"v_add_f32_e32 v115, v115, v248\n"
"v_mfma_f32_16x16x32_bf16 v[76:79], a[160:163], v[32:35], v[76:79]\n"
"ds_write_b128 v3, v[124:127] offset:8192\n"
"v_mfma_f32_16x16x32_bf16 v[100:103], a[164:167], v[32:35], v[100:103]\n"
"v_add_f32_e32 v116, v116, v247\n"
"v_add_f32_e32 v117, v117, v246\n"
"v_mfma_f32_16x16x32_bf16 v[128:131], a[168:171], v[32:35], v[128:131]\n"
"ds_write_b128 v3, v[136:139] offset:9216\n"
"v_mfma_f32_16x16x32_bf16 v[148:151], a[172:175], v[32:35], v[148:151]\n"
"v_add_f32_e32 v118, v118, v245\n"
"v_add_f32_e32 v119, v119, v244\n"
"v_mfma_f32_16x16x32_bf16 v[76:79], a[176:179], v[36:39], v[76:79]\n"
"s_add_u32 s98, s56, 0x400000\n"
"buffer_load_dwordx4 v[24:27], v0, s[4:7], s98 offen sc1\n"
"v_mfma_f32_16x16x32_bf16 v[100:103], a[180:183], v[36:39], v[100:103]\n"
"v_add_f32_e32 v140, v140, v243\n"
"v_add_f32_e32 v141, v141, v242\n"
"v_mfma_f32_16x16x32_bf16 v[128:131], a[184:187], v[36:39], v[128:131]\n"
"v_add_f32_e32 v142, v142, v241\n"
"v_add_f32_e32 v143, v143, v240\n"
"v_mfma_f32_16x16x32_bf16 v[148:151], a[188:191], v[36:39], v[148:151]\n"
"s_waitcnt vmcnt(11)\n"
"buffer_load_dwordx4 v[32:35], v0, s[4:7], s98 offen offset:1024 sc1\n"
"v_mfma_f32_16x16x32_bf16 v[76:79], a[192:195], v[44:47], v[76:79]\n"
"v_mfma_f32_16x16x32_bf16 v[100:103], a[196:199], v[44:47], v[100:103]\n"
"v_mfma_f32_16x16x32_bf16 v[128:131], a[200:203], v[44:47], v[128:131]\n"
"buffer_load_dwordx4 v[36:39], v0, s[4:7], s98 offen offset:2048 sc1\n"
"v_mfma_f32_16x16x32_bf16 v[148:151], a[204:207], v[44:47], v[148:151]\n"
"v_mfma_f32_16x16x32_bf16 v[76:79], a[208:211], v[56:59], v[76:79]\n"
"v_mfma_f32_16x16x32_bf16 v[100:103], a[212:215], v[56:59], v[100:103]\n"
"buffer_load_dwordx4 v[44:47], v0, s[4:7], s98 offen offset:3072 sc1\n"
"v_mfma_f32_16x16x32_bf16 v[128:131], a[216:219], v[56:59], v[128:131]\n"
"v_mfma_f32_16x16x32_bf16 v[148:151], a[220:223], v[56:59], v[148:151]\n"
"s_waitcnt vmcnt(12)\n"
"v_mfma_f32_16x16x32_bf16 v[76:79], a[224:227], v[60:63], v[76:79]\n"
"s_waitcnt vmcnt(10)\n"
"v_max_u32_e32 v255, v132, v133\n"
"v_max3_u32 v255, v255, v134, v135\n"
"v_mfma_f32_16x16x32_bf16 v[100:103], a[228:231], v[60:63], v[100:103]\n"
"s_waitcnt vmcnt(8)\n"
"v_max3_u32 v255, v255, v68, v69\n"
"v_max3_u32 v255, v255, v70, v71\n"
"v_mfma_f32_16x16x32_bf16 v[128:131], a[232:235], v[60:63], v[128:131]\n"
"v_max3_u32 v255, v255, v64, v65\n"
"v_max3_u32 v255, v255, v66, v67\n"
"v_mfma_f32_16x16x32_bf16 v[148:151], a[236:239], v[60:63], v[148:151]\n"
"v_max3_u32 v255, v255, v104, v105\n"
"v_max3_u32 v255, v255, v106, v107\n"
"v_mfma_f32_16x16x32_bf16 v[76:79], a[240:243], v[88:91], v[76:79]\n"
"v_cmp_eq_u32_e32 vcc, -1, v255\n"
"v_mfma_f32_16x16x32_bf16 v[100:103], a[244:247], v[88:91], v[100:103]\n"
"s_cbranch_vccnz .Lmk_q67s\n"
".Lmk_q67r:\n"
"v_mfma_f32_16x16x32_bf16 v[128:131], a[248:251], v[88:91], v[128:131]\n"
"buffer_load_dwordx4 v[56:59], v1, s[4:7], s98 offen sc1\n"
"v_mfma_f32_16x16x32_bf16 v[148:151], a[252:255], v[88:91], v[148:151]\n"
"; @B G2 b.3\n"
"v_mfma_f32_16x16x32_bf16 v[108:111], a[128:131], v[132:135], v[108:111]\n"
"s_nop 0\n"
"v_cmp_u_f32_e32 vcc, v76, v76\n"
"v_mfma_f32_16x16x32_bf16 v[112:115], a[132:135], v[132:135], v[112:115]\n"
"s_cbranch_vccnz .Lmk_p66s\n"
".Lmk_p66r:\n"
"v_mfma_f32_16x16x32_bf16 v[116:119], a[136:139], v[132:135], v[116:119]\n"
"buffer_load_dwordx4 v[60:63], v1, s[4:7], s98 offen offset:1024 sc1\n"
"v_mfma_f32_16x16x32_bf16 v[140:143], a[140:143], v[132:135], v[140:143]\n"
"v_mfma_f32_16x16x32_bf16 v[108:111], a[144:147], v[68:71], v[108:111]\n"
"ds_write_b128 v3, v[76:79] offset:12288\n"
"v_mfma_f32_16x16x32_bf16 v[112:115], a[148:151], v[68:71], v[112:115]\n"
"buffer_load_dwordx4 v[76:79], v1, s[4:7], s98 offen offset:2048 sc1\n"
"v_mfma_f32_16x16x32_bf16 v[116:119], a[152:155], v[68:71], v[116:119]\n"
"ds_write_b128 v3, v[100:103] offset:13312\n"
"v_mfma_f32_16x16x32_bf16 v[140:143], a[156:159], v[68:71], v[140:143]\n"
"v_mfma_f32_16x16x32_bf16 v[108:111], a[160:163], v[64:67], v[108:111]\n"
"s_waitcnt vmcnt(9)\n"
"v_max_u32_e32 v255, v96, v97\n"
"v_max3_u32 v255, v255, v98, v99\n"
"v_mfma_f32_16x16x32_bf16 v[112:115], a[164:167], v[64:67], v[112:115]\n"
"s_waitcnt vmcnt(7)\n"
"v_max3_u32 v255, v255, v84, v85\n"
"v_max3_u32 v255, v255, v86, v87\n"
"v_mfma_f32_16x16x32_bf16 v[116:119], a[168:171], v[64:67], v[116:119]\n"
"v_max3_u32 v255, v255, v72, v73\n"
"v_max3_u32 v255, v255, v74, v75\n"
"v_mfma_f32_16x16x32_bf16 v[140:143], a[172:175], v[64:67], v[140:143]\n"
"v_max3_u32 v255, v255, v144, v145\n"
"v_max3_u32 v255, v255, v146, v147\n"
"v_mfma_f32_16x16x32_bf16 v[108:111], a[176:179], v[104:107], v[108:111]\n"
"v_cmp_eq_u32_e32 vcc, -1, v255\n"
"v_mfma_f32_16x16x32_bf16 v[112:115], a[180:183], v[104:107], v[112:115]\n"
"s_cbranch_vccnz .Lmk_q68s\n"
".Lmk_q68r:\n"
"v_mfma_f32_16x16x32_bf16 v[116:119], a[184:187], v[104:107], v[116:119]\n"
"ds_write_b128 v3, v[128:131] offset:14336\n"
"v_mfma_f32_16x16x32_bf16 v[140:143], a[188:191], v[104:107], v[140:143]\n"
"buffer_load_dwordx4 v[64:67], v1, s[4:7], s98 offen offset:3072 sc1\n"
"v_mfma_f32_16x16x32_bf16 v[108:111], a[192:195], v[96:99], v[108:111]\n"
"ds_write_b128 v3, v[148:151] offset:15360\n"
"v_mfma_f32_16x16x32_bf16 v[112:115], a[196:199], v[96:99], v[112:115]\n"
"s_add_u32 s98, s57, 0x400000\n"
"v_mfma_f32_16x16x32_bf16 v[116:119], a[200:203], v[96:99], v[116:119]\n"
"buffer_load_dwordx4 v[68:71], v0, s[4:7], s98 offen sc1\n"
"v_mfma_f32_16x16x32_bf16 v[140:143], a[204:207], v[96:99], v[140:143]\n"
"s_waitcnt lgkmcnt(0)\n"
"s_barrier\n"
"v_mfma_f32_16x16x32_bf16 v[108:111], a[208:211], v[84:87], v[108:111]\n"
"ds_read_b128 v[80:83], v5\n"
"ds_read_b128 v[88:91], v6\n"
"v_mfma_f32_16x16x32_bf16 v[112:115], a[212:215], v[84:87], v[112:115]\n"
"buffer_load_dwordx4 v[92:95], v0, s[4:7], s98 offen offset:1024 sc1\n"
"v_mfma_f32_16x16x32_bf16 v[116:119], a[216:219], v[84:87], v[116:119]\n"
"ds_read_b128 v[96:99], v5 offset:1024\n"
"ds_read_b128 v[100:103], v6 offset:1024\n"
"v_mfma_f32_16x16x32_bf16 v[140:143], a[220:223], v[84:87], v[140:143]\n"
"ds_read_b128 v[84:87], v5 offset:2048\n"
"ds_read_b128 v[104:107], v6 offset:2048\n"
"v_mfma_f32_16x16x32_bf16 v[108:111], a[224:227], v[72:75], v[108:111]\n"
"buffer_load_dwordx4 v[120:123], v0, s[4:7], s98 offen offset:2048 sc1\n"
"v_mfma_f32_16x16x32_bf16 v[112:115], a[228:231], v[72:75], v[112:115]\n"
"ds_read_b128 v[124:127], v5 offset:3072\n"
"ds_read_b128 v[128:131], v6 offset:3072\n"
"v_mfma_f32_16x16x32_bf16 v[116:119], a[232:235], v[72:75], v[116:119]\n"
"v_mfma_f32_16x16x32_bf16 v[140:143], a[236:239], v[72:75], v[140:143]\n"
"buffer_load_dwordx4 v[72:75], v0, s[4:7], s98 offen offset:3072 sc1\n"
"v_mfma_f32_16x16x32_bf16 v[108:111], a[240:243], v[144:147], v[108:111]\n"
"s_waitcnt lgkmcnt(4)\n"
"v_add_f32_e32 v80, v80, v88\n"
"v_add_f32_e32 v81, v81, v89\n"
"v_mfma_f32_16x16x32_bf16 v[112:115], a[244:247], v[144:147], v[112:115]\n"
"v_add_f32_e32 v82, v82, v90\n"
"v_add_f32_e32 v83, v83, v91\n"
"v_mfma_f32_16x16x32_bf16 v[116:119], a[248:251], v[144:147], v[116:119]\n"
"buffer_load_dwordx4 v[88:91], v1, s[4:7], s98 offen sc1\n"
"v_mfma_f32_16x16x32_bf16 v[140:143], a[252:255], v[144:147], v[140:143]\n"
"s_waitcnt lgkmcnt(0)\n"
"v_add_f32_e32 v96, v96, v100\n"
"v_add_f32_e32 v97, v97, v101\n"
"v_add_f32_e32 v98, v98, v102\n"
"v_add_f32_e32 v99, v99, v103\n"
"v_add_f32_e32 v84, v84, v104\n"
"v_add_f32_e32 v85, v85, v105\n"
"v_add_f32_e32 v86, v86, v106\n"
"v_add_f32_e32 v87, v87, v107\n"
"v_add_f32_e32 v124, v124, v128\n"
"v_add_f32_e32 v125, v125, v129\n"
"v_add_f32_e32 v126, v126, v130\n"
"v_add_f32_e32 v127, v127, v131\n"
"buffer_load_dwordx4 v[100:103], v1, s[4:7], s98 offen offset:1024 sc1\n"
"buffer_load_dwordx4 v[104:107], v1, s[4:7], s98 offen offset:2048 sc1\n"
"buffer_load_dwordx4 v[128:131], v1, s[4:7], s98 offen offset:3072 sc1\n"
"s_waitcnt vmcnt(15)\n"
"; @B G2 a.0\n"
"v_mfma_f32_16x16x32_bf16 v[132:135], a[128:131], v[24:27], 0\n"
"s_add_u32 s98, s58, 0x400000\n"
"buffer_load_dwordx4 v[136:139], v0, s[4:7], s98 offen sc1\n"
"v_mfma_f32_16x16x32_bf16 v[144:147], a[132:135], v[24:27], 0\n"
"v_add_f32_e32 v80, v108, v80\n"
"v_add_f32_e32 v81, v109, v81\n"
"v_mfma_f32_16x16x32_bf16 v[148:151], a[136:139], v[24:27], 0\n"
"v_add_f32_e32 v82, v110, v82\n"
"v_add_f32_e32 v83, v111, v83\n"
"v_mfma_f32_16x16x32_bf16 v[108:111], a[140:143], v[24:27], 0\n"
"s_waitcnt vmcnt(15)\n"
"buffer_load_dwordx4 v[24:27], v0, s[4:7], s98 offen offset:1024 sc1\n"
"v_mfma_f32_16x16x32_bf16 v[132:135], a[144:147], v[32:35], v[132:135]\n"
"v_add_f32_e32 v96, v112, v96\n"
"v_add_f32_e32 v97, v113, v97\n"
"v_mfma_f32_16x16x32_bf16 v[144:147], a[148:151], v[32:35], v[144:147]\n"
"v_add_f32_e32 v98, v114, v98\n"
"v_add_f32_e32 v99, v115, v99\n"
"v_mfma_f32_16x16x32_bf16 v[148:151], a[152:155], v[32:35], v[148:151]\n"
"buffer_load_dwordx4 v[112:115], v0, s[4:7], s98 offen offset:2048 sc1\n"
"v_mfma_f32_16x16x32_bf16 v[108:111], a[156:159], v[32:35], v[108:111]\n"
"s_waitcnt vmcnt(16)\n"
"v_cvt_pk_bf16_f32 v32, v80, v81\n"
"v_cvt_pk_bf16_f32 v33, v82, v83\n"
"v_mfma_f32_16x16x32_bf16 v[132:135], a[160:163], v[36:39], v[132:135]\n"
"v_cvt_pk_bf16_f32 v34, v96, v97\n"
"v_cvt_pk_bf16_f32 v35, v98, v99\n"
"v_mfma_f32_16x16x32_bf16 v[144:147], a[164:167], v[36:39], v[144:147]\n"
"buffer_load_dwordx4 v[80:83], v0, s[4:7], s98 offen offset:3072 sc1\n"
"v_mfma_f32_16x16x32_bf16 v[148:151], a[168:171], v[36:39], v[148:151]\n"
"v_add_f32_e32 v84, v116, v84\n"
"v_add_f32_e32 v85, v117, v85\n"
"v_mfma_f32_16x16x32_bf16 v[108:111], a[172:175], v[36:39], v[108:111]\n"
"s_waitcnt vmcnt(16)\n"
"v_add_f32_e32 v86, v118, v86\n"
"v_add_f32_e32 v87, v119, v87\n"
"v_mfma_f32_16x16x32_bf16 v[132:135], a[176:179], v[44:47], v[132:135]\n"
"buffer_load_dwordx4 v[36:39], v1, s[4:7], s98 offen sc1\n"
"v_mfma_f32_16x16x32_bf16 v[144:147], a[180:183], v[44:47], v[144:147]\n"
"v_add_f32_e32 v124, v140, v124\n"
"v_add_f32_e32 v125, v141, v125\n"
"v_mfma_f32_16x16x32_bf16 v[148:151], a[184:187], v[44:47], v[148:151]\n"
"v_add_f32_e32 v126, v142, v126\n"
"v_add_f32_e32 v127, v143, v127\n"
"v_mfma_f32_16x16x32_bf16 v[108:111], a[188:191], v[44:47], v[108:111]\n"
"s_waitcnt vmcnt(16)\n"
"buffer_load_dwordx4 v[44:47], v1, s[4:7], s98 offen offset:1024 sc1\n"
"v_mfma_f32_16x16x32_bf16 v[132:135], a[192:195], v[56:59], v[132:135]\n"
"v_cvt_pk_bf16_f32 v96, v84, v85\n"
"v_cvt_pk_bf16_f32 v97, v86, v87\n"
"v_mfma_f32_16x16x32_bf16 v[144:147], a[196:199], v[56:59], v[144:147]\n"
"v_cvt_pk_bf16_f32 v98, v124, v125\n"
"v_cvt_pk_bf16_f32 v99, v126, v127\n"
"v_mfma_f32_16x16x32_bf16 v[148:151], a[200:203], v[56:59], v[148:151]\n"
"buffer_load_dwordx4 v[84:87], v1, s[4:7], s98 offen offset:2048 sc1\n"
"v_mfma_f32_16x16x32_bf16 v[108:111], a[204:207], v[56:59], v[108:111]\n"
"s_waitcnt vmcnt(17)\n"
"v_permlane16_swap_b32_e32 v32, v34\n"
"v_permlane16_swap_b32_e32 v33, v35\n"
"v_mfma_f32_16x16x32_bf16 v[132:135], a[208:211], v[60:63], v[132:135]\n"
"v_permlane16_swap_b32_e32 v96, v98\n"
"v_permlane16_swap_b32_e32 v97, v99\n"
"v_mfma_f32_16x16x32_bf16 v[144:147], a[212:215], v[60:63], v[144:147]\n"
"buffer_load_dwordx4 v[56:59], v1, s[4:7], s98 offen offset:3072 sc1\n"
"v_mfma_f32_16x16x32_bf16 v[148:151], a[216:219], v[60:63], v[148:151]\n"
"s_add_u32 s98, s59, 0x400000\n"
"v_mfma_f32_16x16x32_bf16 v[108:111], a[220:223], v[60:63], v[108:111]\n"
"s_waitcnt vmcnt(17)\n"
"v_mfma_f32_16x16x32_bf16 v[132:135], a[224:227], v[76:79], v[132:135]\n"
"buffer_load_dwordx4 v[60:63], v0, s[4:7], s98 offen sc1\n"
"v_mfma_f32_16x16x32_bf16 v[144:147], a[228:231], v[76:79], v[144:147]\n"
"v_mfma_f32_16x16x32_bf16 v[148:151], a[232:235], v[76:79], v[148:151]\n"
"v_mfma_f32_16x16x32_bf16 v[108:111], a[236:239], v[76:79], v[108:111]\n"
"s_waitcnt vmcnt(17)\n"
"buffer_load_dwordx4 v[76:79], v0, s[4:7], s98 offen offset:1024 sc1\n"
"v_mfma_f32_16x16x32_bf16 v[132:135], a[240:243], v[64:67], v[132:135]\n"
"v_mfma_f32_16x16x32_bf16 v[144:147], a[244:247], v[64:67], v[144:147]\n"
"v_mfma_f32_16x16x32_bf16 v[148:151], a[248:251], v[64:67], v[148:151]\n"
"buffer_load_dwordx4 v[116:119], v0, s[4:7], s98 offen offset:2048 sc1\n"
"v_mfma_f32_16x16x32_bf16 v[108:111], a[252:255], v[64:67], v[108:111]\n"
"s_waitcnt vmcnt(17)\n"
"; @B G2 a.1\n"
"v_mfma_f32_16x16x32_bf16 v[64:67], a[128:131], v[68:71], 0\n"
"s_nop 1\n"
"v_cmp_u_f32_e32 vcc, v132, v132\n"
"v_mfma_f32_16x16x32_bf16 v[124:127], a[132:135], v[68:71], 0\n"
"s_cbranch_vccnz .Lmk_p69s\n"
".Lmk_p69r:\n"
"v_mfma_f32_16x16x32_bf16 v[140:143], a[136:139], v[68:71], 0\n"
"buffer_load_dwordx4 v[152:155], v0, s[4:7], s98 offen offset:3072 sc1\n"
"v_mfma_f32_16x16x32_bf16 v[156:159], a[140:143], v[68:71], 0\n"
"v_mfma_f32_16x16x32_bf16 v[64:67], a[144:147], v[92:95], v[64:67]\n"
"ds_write_b128 v3, v[132:135]\n"
"v_mfma_f32_16x16x32_bf16 v[124:127], a[148:151], v[92:95], v[124:127]\n"
"buffer_load_dwordx4 v[68:71], v1, s[4:7], s98 offen sc1\n"
"v_mfma_f32_16x16x32_bf16 v[140:143], a[152:155], v[92:95], v[140:143]\n"
"ds_write_b128 v3, v[144:147] offset:1024\n"
"v_mfma_f32_16x16x32_bf16 v[156:159], a[156:159], v[92:95], v[156:159]\n"
"s_waitcnt vmcnt(17)\n"
"v_mfma_f32_16x16x32_bf16 v[64:67], a[160:163], v[120:123], v[64:67]\n"
"ds_write_b128 v3, v[148:151] offset:2048\n"
"v_mfma_f32_16x16x32_bf16 v[124:127], a[164:167], v[120:123], v[124:127]\n"
"buffer_load_dwordx4 v[92:95], v1, s[4:7], s98 offen offset:1024 sc1\n"
"v_mfma_f32_16x16x32_bf16 v[140:143], a[168:171], v[120:123], v[140:143]\n"
"ds_write_b128 v3, v[108:111] offset:3072\n"
"v_mfma_f32_16x16x32_bf16 v[156:159], a[172:175], v[120:123], v[156:159]\n"
"v_mfma_f32_16x16x32_bf16 v[64:67], a[176:179], v[72:75], v[64:67]\n"
"buffer_load_dwordx4 v[108:111], v1, s[4:7], s98 offen offset:2048 sc1\n"
"v_mfma_f32_16x16x32_bf16 v[124:127], a[180:183], v[72:75], v[124:127]\n"
"ds_read_b128 v[120:123], v2\n"
"ds_read_b128 v[132:135], v2 offset:1024\n"
"v_mfma_f32_16x16x32_bf16 v[140:143], a[184:187], v[72:75], v[140:143]\n"
"v_mfma_f32_16x16x32_bf16 v[156:159], a[188:191], v[72:75], v[156:159]\n"
"s_waitcnt vmcnt(17)\n"
"buffer_load_dwordx4 v[72:75], v1, s[4:7], s98 offen offset:3072 sc1\n"
"v_mfma_f32_16x16x32_bf16 v[64:67], a[192:195], v[88:91], v[64:67]\n"
"v_mfma_f32_16x16x32_bf16 v[124:127], a[196:199], v[88:91], v[124:127]\n"
"v_mfma_f32_16x16x32_bf16 v[140:143], a[200:203], v[88:91], v[140:143]\n"
"s_waitcnt lgkmcnt(0)\n"
"v_lshlrev_b32_e32 v255, 16, v120\n"
"v_and_b32_e32 v254, 0xffff0000, v120\n"
"v_mfma_f32_16x16x32_bf16 v[156:159], a[204:207], v[88:91], v[156:159]\n"
"s_waitcnt lgkmcnt(0)\n"
"s_barrier\n"
"v_mfma_f32_16x16x32_bf16 v[64:67], a[208:211], v[100:103], v[64:67]\n"
"v_lshlrev_b32_e32 v253, 16, v121\n"
"v_and_b32_e32 v252, 0xffff0000, v121\n"
"v_mfma_f32_16x16x32_bf16 v[124:127], a[212:215], v[100:103], v[124:127]\n"
"ds_read_b128 v[88:91], v4\n"
"ds_read_b128 v[144:147], v4 offset:1024\n"
"v_mfma_f32_16x16x32_bf16 v[140:143], a[216:219], v[100:103], v[140:143]\n"
"ds_read_b128 v[148:151], v4 offset:2048\n"
"ds_read_b128 v[160:163], v4 offset:3072\n"
"v_mfma_f32_16x16x32_bf16 v[156:159], a[220:223], v[100:103], v[156:159]\n"
"s_waitcnt vmcnt(16)\n"
"v_lshlrev_b32_e32 v251, 16, v122\n"
"v_and_b32_e32 v250, 0xffff0000, v122\n"
"v_mfma_f32_16x16x32_bf16 v[64:67], a[224:227], v[104:107], v[64:67]\n"
"v_lshlrev_b32_e32 v249, 16, v123\n"
"v_and_b32_e32 v248, 0xffff0000, v123\n"
"v_mfma_f32_16x16x32_bf16 v[124:127], a[228:231], v[104:107], v[124:127]\n"
"v_lshlrev_b32_e32 v247, 16, v132\n"
"v_and_b32_e32 v246, 0xffff0000, v132\n"
"v_mfma_f32_16x16x32_bf16 v[140:143], a[232:235], v[104:107], v[140:143]\n"
"v_lshlrev_b32_e32 v245, 16, v133\n"
"v_and_b32_e32 v244, 0xffff0000, v133\n"
"v_mfma_f32_16x16x32_bf16 v[156:159], a[236:239], v[104:107], v[156:159]\n"
"v_lshlrev_b32_e32 v243, 16, v134\n"
"v_and_b32_e32 v242, 0xffff0000, v134\n"
"v_mfma_f32_16x16x32_bf16 v[64:67], a[240:243], v[128:131], v[64:67]\n"
"s_add_u32 s98, s74, 0x40000\n"
"buffer_store_dwordx4 v[32:35], v20, s[16:19], s98 offen\n"
"v_mfma_f32_16x16x32_bf16 v[124:127], a[244:247], v[128:131], v[124:127]\n"
"s_waitcnt lgkmcnt(0)\n"
"v_lshlrev_b32_e32 v241, 16, v135\n"
"v_and_b32_e32 v240, 0xffff0000, v135\n"
"v_mfma_f32_16x16x32_bf16 v[140:143], a[248:251], v[128:131], v[140:143]\n"
"v_add_f32_e32 v88, v88, v255\n"
"v_add_f32_e32 v89, v89, v254\n"
"v_mfma_f32_16x16x32_bf16 v[156:159], a[252:255], v[128:131], v[156:159]\n"
"s_waitcnt vmcnt(15)\n"
"v_add_f32_e32 v90, v90, v253\n"
"v_add_f32_e32 v91, v91, v252\n"
"; @B G2 a.2\n"
"v_mfma_f32_16x16x32_bf16 v[32:35], a[128:131], v[136:139], 0\n"
"v_cmp_u_f32_e32 vcc, v64, v64\n"
"v_add_f32_e32 v144, v144, v251\n"
"v_mfma_f32_16x16x32_bf16 v[100:103], a[132:135], v[136:139], 0\n"
"s_cbranch_vccnz .Lmk_p70s\n"
".Lmk_p70r:\n"
"v_add_f32_e32 v145, v145, v250\n"
"v_mfma_f32_16x16x32_bf16 v[104:107], a[136:139], v[136:139], 0\n"
"v_add_f32_e32 v146, v146, v249\n"
"v_add_f32_e32 v147, v147, v248\n"
"v_mfma_f32_16x16x32_bf16 v[120:123], a[140:143], v[136:139], 0\n"
"v_add_f32_e32 v148, v148, v247\n"
"v_add_f32_e32 v149, v149, v246\n"
"v_mfma_f32_16x16x32_bf16 v[32:35], a[144:147], v[24:27], v[32:35]\n"
"ds_write_b128 v3, v[64:67] offset:6144\n"
"v_mfma_f32_16x16x32_bf16 v[100:103], a[148:151], v[24:27], v[100:103]\n"
"v_add_f32_e32 v150, v150, v245\n"
"v_add_f32_e32 v151, v151, v244\n"
"v_mfma_f32_16x16x32_bf16 v[104:107], a[152:155], v[24:27], v[104:107]\n"
"ds_write_b128 v3, v[124:127] offset:7168\n"
"v_mfma_f32_16x16x32_bf16 v[120:123], a[156:159], v[24:27], v[120:123]\n"
"s_waitcnt vmcnt(13)\n"
"s_add_u32 s98, s74, 0x40000\n"
"v_add_f32_e32 v160, v160, v243\n"
"v_mfma_f32_16x16x32_bf16 v[32:35], a[160:163], v[112:115], v[32:35]\n"
"ds_write_b128 v3, v[140:143] offset:8192\n"
"v_mfma_f32_16x16x32_bf16 v[100:103], a[164:167], v[112:115], v[100:103]\n"
"buffer_store_dwordx4 v[96:99], v20, s[16:19], s98 offen offset:64\n"
"v_mfma_f32_16x16x32_bf16 v[104:107], a[168:171], v[112:115], v[104:107]\n"
"ds_write_b128 v3, v[156:159] offset:9216\n"
"v_mfma_f32_16x16x32_bf16 v[120:123], a[172:175], v[112:115], v[120:123]\n"
"v_add_f32_e32 v161, v161, v242\n"
"v_add_f32_e32 v162, v162, v241\n"
"v_mfma_f32_16x16x32_bf16 v[32:35], a[176:179], v[80:83], v[32:35]\n"
"v_add_f32_e32 v163, v163, v240\n"
"v_mfma_f32_16x16x32_bf16 v[100:103], a[180:183], v[80:83], v[100:103]\n"
"v_mfma_f32_16x16x32_bf16 v[104:107], a[184:187], v[80:83], v[104:107]\n"
"v_mfma_f32_16x16x32_bf16 v[120:123], a[188:191], v[80:83], v[120:123]\n"
"s_waitcnt vmcnt(12)\n"
"v_mfma_f32_16x16x32_bf16 v[32:35], a[192:195], v[36:39], v[32:35]\n"
"v_mfma_f32_16x16x32_bf16 v[100:103], a[196:199], v[36:39], v[100:103]\n"
"v_mfma_f32_16x16x32_bf16 v[104:107], a[200:203], v[36:39], v[104:107]\n"
"v_mfma_f32_16x16x32_bf16 v[120:123], a[204:207], v[36:39], v[120:123]\n"
"v_mfma_f32_16x16x32_bf16 v[32:35], a[208:211], v[44:47], v[32:35]\n"
"v_mfma_f32_16x16x32_bf16 v[100:103], a[212:215], v[44:47], v[100:103]\n"
"v_mfma_f32_16x16x32_bf16 v[104:107], a[216:219], v[44:47], v[104:107]\n"
"v_mfma_f32_16x16x32_bf16 v[120:123], a[220:223], v[44:47], v[120:123]\n"
"s_waitcnt vmcnt(10)\n"
"v_mfma_f32_16x16x32_bf16 v[32:35], a[224:227], v[84:87], v[32:35]\n"
"s_waitcnt vmcnt(8)\n"
"v_max_u32_e32 v255, v60, v61\n"
"v_max3_u32 v255, v255, v62, v63\n"
"v_mfma_f32_16x16x32_bf16 v[100:103], a[228:231], v[84:87], v[100:103]\n"
"s_waitcnt vmcnt(6)\n"
"v_max3_u32 v255, v255, v76, v77\n"
"v_max3_u32 v255, v255, v78, v79\n"
"v_mfma_f32_16x16x32_bf16 v[104:107], a[232:235], v[84:87], v[104:107]\n"
"v_max3_u32 v255, v255, v116, v117\n"
"v_max3_u32 v255, v255, v118, v119\n"
"v_mfma_f32_16x16x32_bf16 v[120:123], a[236:239], v[84:87], v[120:123]\n"
"v_max3_u32 v255, v255, v152, v153\n"
"v_max3_u32 v255, v255, v154, v155\n"
"v_mfma_f32_16x16x32_bf16 v[32:35], a[240:243], v[56:59], v[32:35]\n"
"v_cmp_eq_u32_e32 vcc, -1, v255\n"
"v_mfma_f32_16x16x32_bf16 v[100:103], a[244:247], v[56:59], v[100:103]\n"
"s_cbranch_vccnz .Lmk_q72s\n"
".Lmk_q72r:\n"
"v_mfma_f32_16x16x32_bf16 v[104:107], a[248:251], v[56:59], v[104:107]\n"
"v_mfma_f32_16x16x32_bf16 v[120:123], a[252:255], v[56:59], v[120:123]\n"
"; @B G2 a.3\n"
"v_mfma_f32_16x16x32_bf16 v[88:91], a[128:131], v[60:63], v[88:91]\n"
"s_nop 1\n"
"v_cmp_u_f32_e32 vcc, v32, v32\n"
"v_mfma_f32_16x16x32_bf16 v[144:147], a[132:135], v[60:63], v[144:147]\n"
"s_cbranch_vccnz .Lmk_p71s\n"
".Lmk_p71r:\n"
"v_mfma_f32_16x16x32_bf16 v[148:151], a[136:139], v[60:63], v[148:151]\n"
"v_mfma_f32_16x16x32_bf16 v[160:163], a[140:143], v[60:63], v[160:163]\n"
"v_mfma_f32_16x16x32_bf16 v[88:91], a[144:147], v[76:79], v[88:91]\n"
"ds_write_b128 v3, v[32:35] offset:12288\n"
"v_mfma_f32_16x16x32_bf16 v[144:147], a[148:151], v[76:79], v[144:147]\n"
"v_mfma_f32_16x16x32_bf16 v[148:151], a[152:155], v[76:79], v[148:151]\n"
"ds_write_b128 v3, v[100:103] offset:13312\n"
"v_mfma_f32_16x16x32_bf16 v[160:163], a[156:159], v[76:79], v[160:163]\n"
"v_mfma_f32_16x16x32_bf16 v[88:91], a[160:163], v[116:119], v[88:91]\n"
"s_waitcnt vmcnt(4)\n"
"v_max_u32_e32 v255, v68, v69\n"
"v_max3_u32 v255, v255, v70, v71\n"
"v_mfma_f32_16x16x32_bf16 v[144:147], a[164:167], v[116:119], v[144:147]\n"
"s_waitcnt vmcnt(2)\n"
"v_max3_u32 v255, v255, v92, v93\n"
"v_max3_u32 v255, v255, v94, v95\n"
"v_mfma_f32_16x16x32_bf16 v[148:151], a[168:171], v[116:119], v[148:151]\n"
"v_max3_u32 v255, v255, v108, v109\n"
"v_max3_u32 v255, v255, v110, v111\n"
"v_mfma_f32_16x16x32_bf16 v[160:163], a[172:175], v[116:119], v[160:163]\n"
"v_max3_u32 v255, v255, v72, v73\n"
"v_max3_u32 v255, v255, v74, v75\n"
"v_mfma_f32_16x16x32_bf16 v[88:91], a[176:179], v[152:155], v[88:91]\n"
"v_cmp_eq_u32_e32 vcc, -1, v255\n"
"v_mfma_f32_16x16x32_bf16 v[144:147], a[180:183], v[152:155], v[144:147]\n"
"s_cbranch_vccnz .Lmk_q73s\n"
".Lmk_q73r:\n"
"v_mfma_f32_16x16x32_bf16 v[148:151], a[184:187], v[152:155], v[148:151]\n"
"ds_write_b128 v3, v[104:107] offset:14336\n"
"v_mfma_f32_16x16x32_bf16 v[160:163], a[188:191], v[152:155], v[160:163]\n"
"v_mfma_f32_16x16x32_bf16 v[88:91], a[192:195], v[68:71], v[88:91]\n"
"ds_write_b128 v3, v[120:123] offset:15360\n"
"v_mfma_f32_16x16x32_bf16 v[144:147], a[196:199], v[68:71], v[144:147]\n"
"v_mfma_f32_16x16x32_bf16 v[148:151], a[200:203], v[68:71], v[148:151]\n"
"v_mfma_f32_16x16x32_bf16 v[160:163], a[204:207], v[68:71], v[160:163]\n"
"s_waitcnt lgkmcnt(0)\n"
"s_barrier\n"
"v_mfma_f32_16x16x32_bf16 v[88:91], a[208:211], v[92:95], v[88:91]\n"
"ds_read_b128 v[24:27], v5\n"
"ds_read_b128 v[32:35], v6\n"
"v_mfma_f32_16x16x32_bf16 v[144:147], a[212:215], v[92:95], v[144:147]\n"
"ds_read_b128 v[36:39], v5 offset:1024\n"
"ds_read_b128 v[44:47], v6 offset:1024\n"
"v_mfma_f32_16x16x32_bf16 v[148:151], a[216:219], v[92:95], v[148:151]\n"
"ds_read_b128 v[56:59], v5 offset:2048\n"
"ds_read_b128 v[60:63], v6 offset:2048\n"
"v_mfma_f32_16x16x32_bf16 v[160:163], a[220:223], v[92:95], v[160:163]\n"
"ds_read_b128 v[64:67], v5 offset:3072\n"
"ds_read_b128 v[68:71], v6 offset:3072\n"
"v_mfma_f32_16x16x32_bf16 v[88:91], a[224:227], v[108:111], v[88:91]\n"
"v_mfma_f32_16x16x32_bf16 v[144:147], a[228:231], v[108:111], v[144:147]\n"
"v_mfma_f32_16x16x32_bf16 v[148:151], a[232:235], v[108:111], v[148:151]\n"
"s_waitcnt lgkmcnt(4)\n"
"v_add_f32_e32 v24, v24, v32\n"
"v_add_f32_e32 v25, v25, v33\n"
"v_mfma_f32_16x16x32_bf16 v[160:163], a[236:239], v[108:111], v[160:163]\n"
"v_add_f32_e32 v26, v26, v34\n"
"v_add_f32_e32 v27, v27, v35\n"
"v_mfma_f32_16x16x32_bf16 v[88:91], a[240:243], v[72:75], v[88:91]\n"
"s_waitcnt lgkmcnt(0)\n"
"v_add_f32_e32 v36, v36, v44\n"
"v_add_f32_e32 v37, v37, v45\n"
"v_mfma_f32_16x16x32_bf16 v[144:147], a[244:247], v[72:75], v[144:147]\n"
"v_add_f32_e32 v38, v38, v46\n"
"v_add_f32_e32 v39, v39, v47\n"
"v_mfma_f32_16x16x32_bf16 v[148:151], a[248:251], v[72:75], v[148:151]\n"
"v_add_f32_e32 v56, v56, v60\n"
"v_add_f32_e32 v57, v57, v61\n"
"v_mfma_f32_16x16x32_bf16 v[160:163], a[252:255], v[72:75], v[160:163]\n"
"v_add_f32_e32 v58, v58, v62\n"
"v_add_f32_e32 v59, v59, v63\n"
"v_add_f32_e32 v64, v64, v68\n"
"v_add_f32_e32 v65, v65, v69\n"
"v_add_f32_e32 v66, v66, v70\n"
"v_add_f32_e32 v67, v67, v71\n"
"v_add_f32_e32 v24, v88, v24\n"
"v_add_f32_e32 v25, v89, v25\n"
"v_add_f32_e32 v26, v90, v26\n"
"v_add_f32_e32 v27, v91, v27\n"
"v_add_f32_e32 v36, v144, v36\n"
"v_add_f32_e32 v37, v145, v37\n"
"v_add_f32_e32 v38, v146, v38\n"
"v_add_f32_e32 v39, v147, v39\n"
"v_cvt_pk_bf16_f32 v32, v24, v25\n"
"v_cvt_pk_bf16_f32 v33, v26, v27\n"
"v_cvt_pk_bf16_f32 v34, v36, v37\n"
"v_cvt_pk_bf16_f32 v35, v38, v39\n"
"v_add_f32_e32 v56, v148, v56\n"
"v_add_f32_e32 v57, v149, v57\n"
"v_add_f32_e32 v58, v150, v58\n"
"v_add_f32_e32 v59, v151, v59\n"
"v_add_f32_e32 v64, v160, v64\n"
"v_add_f32_e32 v65, v161, v65\n"
"v_add_f32_e32 v66, v162, v66\n"
"v_add_f32_e32 v67, v163, v67\n"
"v_cvt_pk_bf16_f32 v24, v56, v57\n"
"v_cvt_pk_bf16_f32 v25, v58, v59\n"
"v_cvt_pk_bf16_f32 v26, v64, v65\n"
"v_cvt_pk_bf16_f32 v27, v66, v67\n"
"v_permlane16_swap_b32_e32 v32, v34\n"
"v_permlane16_swap_b32_e32 v33, v35\n"
"buffer_store_dwordx4 v[32:35], v20, s[16:19], s74 offen\n"
"v_permlane16_swap_b32_e32 v24, v26\n"
"v_permlane16_swap_b32_e32 v25, v27\n"
"buffer_store_dwordx4 v[24:27], v20, s[16:19], s74 offen offset:64\n"
"s_cmp_eq_u32 s81, 0\n"
"s_cbranch_scc1 .Lmk_fin\n"
"buffer_store_dwordx2 v[18:19], v11, s[4:7], s81 offen sc1\n"
".Lmk_fin:\n"
"s_endpgm\n"
".Lmk_end:\n"
    :: "{s[4:7]}"(wsr), "{s[8:11]}"(wsr), "{s[12:15]}"(wsk), "{s[16:19]}"(outr), "{s[52:67]}"(sa), "{s[68:83]}"(sb),
       "{s[20:27]}"(pa), "{s[28:31]}"(wa), "{s[32:35]}"(wb), "{s[36:39]}"(oa), "{s[40:43]}"(ob), "{s[44:51]}"(pc), "{s99}"(uni((bix * 4 + w) * 1024)),
       "{v[0:15]}"(va), "{v[16:23]}"(vb), "{v[32:47]}"(pz), "{v48}"(sta), "{v50}"(pf)
    : "memory", "v255", "a255", "s98", "vcc");
  __builtin_unreachable();
}

}

extern "C" void kernel_launch(void* const* d_in, const int* in_sizes, int n_in, void* d_out, int out_size,
                              void* d_ws, size_t ws_size, hipStream_t stream) {
  (void)hipFuncSetAttribute((const void*)mega_kernel, hipFuncAttributeMaxDynamicSharedMemorySize, LDS_BYTES);
  mega_kernel<<<256, 256, LDS_BYTES, stream>>>((const u16*)d_in[0], (const u16*)d_in[1], (const u16*)d_in[2],
                                               (const u16*)d_in[4], (const u16*)d_in[5], (u16*)d_out, (char*)d_ws);
}
```

```cpp
#include <hip/hip_runtime.h>
#include <stdint.h>

namespace {

typedef unsigned short u16;
typedef unsigned long long u64;
typedef short s16x4 __attribute__((ext_vector_type(4)));
typedef float f32x4 __attribute__((ext_vector_type(4)));
typedef unsigned u32x4 __attribute__((ext_vector_type(4)));
typedef unsigned u32x2 __attribute__((ext_vector_type(2)));
typedef unsigned u32x8 __attribute__((ext_vector_type(8)));
typedef unsigned u32x16 __attribute__((ext_vector_type(16)));
typedef __amdgpu_buffer_rsrc_t rsrc_t;
#define LAS __attribute__((address_space(3)))
#define DI __device__ __forceinline__
#define LDSP(a) ((LAS char*)(size_t)(a))

constexpr unsigned MB = 1u << 20;
constexpr unsigned WS_XF = 0;
constexpr unsigned WS_OF = 16 * MB;
constexpr unsigned WS_KV = 32 * MB;
constexpr unsigned WS_WF = 64 * MB;
constexpr unsigned WS_WO = 76 * MB;
constexpr unsigned WS_ST = 80 * MB;
constexpr unsigned WS_CS = 81 * MB;
constexpr unsigned WS_FL = 82 * MB;
constexpr unsigned WS_EP = WS_FL + 2048 * 128;
constexpr unsigned WS_DBG = 96 * MB;
constexpr unsigned OOB = 0x80000000u;
constexpr int L_RES = 0, L_K = 16384, L_V = 49152, L_INB = 81920, L_CS = 155648, L_PM = L_CS + 1024, L_MB = L_PM + 2048, L_SCR = L_MB + 2048;
constexpr int L_WS = 57344;
constexpr int LDS_BYTES = 163840;

template <int N> DI void wait_vm() { asm volatile("s_waitcnt vmcnt(%0)" ::"n"(N) : "memory"); }
DI void lds_barrier() { asm volatile("s_waitcnt lgkmcnt(0)\n\ts_barrier" ::: "memory"); }
template <int N> DI void nops() { asm volatile("s_nop %0" ::"n"(N - 1)); }
template <int OFF> DI void ldv(u32x4& d, rsrc_t r, unsigned v, unsigned s) {
  asm volatile("buffer_load_dwordx4 %0, %1, %2, %3 offen offset:%4 sc1" : "=v"(d) : "v"(v), "s"(r), "s"(s), "i"(OFF) : "memory");
}
template <int OFF> DI void ld8(u32x2& d, rsrc_t r, unsigned v, unsigned s) {
  asm volatile("buffer_load_dwordx2 %0, %1, %2, %3 offen offset:%4 sc1" : "=v"(d) : "v"(v), "s"(r), "s"(s), "i"(OFF) : "memory");
}
template <int OFF> DI void st16(const u32x4& d, rsrc_t r, unsigned v, unsigned s) {
  asm volatile("buffer_store_dwordx4 %0, %1, %2, %3 offen offset:%4 sc1\n\ts_nop 1" :: "v"(d), "v"(v), "s"(r), "s"(s), "i"(OFF) : "memory");
}
template <int OFF> DI void st8(const u32x2& d, rsrc_t r, unsigned v, unsigned s) {
  asm volatile("buffer_store_dwordx2 %0, %1, %2, %3 offen offset:%4 sc1" :: "v"(d), "v"(v), "s"(r), "s"(s), "i"(OFF) : "memory");
}
DI void st4(unsigned d, rsrc_t r, unsigned v, unsigned s) {
  asm volatile("buffer_store_dword %0, %1, %2, %3 offen sc1" :: "v"(d), "v"(v), "s"(r), "s"(s) : "memory");
}
DI void dma(rsrc_t r, unsigned v, unsigned s, unsigned lm) {
  asm volatile("s_mov_b32 m0, %3\n\ts_nop 0\n\tbuffer_load_dwordx4 %0, %1, %2 offen lds" :: "v"(v), "s"(r), "s"(s), "s"(lm) : "memory");
}
DI void touch(rsrc_t r, unsigned v) {
  asm volatile("s_mov_b32 m0, %2\n\ts_nop 0\n\tbuffer_load_dword %0, %1, 0 offen lds" :: "v"(v), "s"(r), "s"((unsigned)L_SCR) : "memory");
}
DI void mma_v(f32x4& c, const u32x4& a, const u32x4& b) { asm volatile("v_mfma_f32_16x16x32_bf16 %0, %1, %2, %0" : "+v"(c) : "v"(a), "v"(b)); }
DI void mm0_v(f32x4& c, const u32x4& a, const u32x4& b) { asm volatile("v_mfma_f32_16x16x32_bf16 %0, %1, %2, 0" : "=v"(c) : "v"(a), "v"(b)); }
DI unsigned uni(unsigned v) { v = __builtin_amdgcn_readfirstlane(v); asm volatile("" : "+s"(v)); return v; }
template <class T> DI void pin(T& v) { asm volatile("" : "+v"(v)); }
DI float sum_x16(float v) {
  auto r = __builtin_amdgcn_permlane16_swap(__float_as_uint(v), __float_as_uint(v), false, false);
  return __uint_as_float(r[0]) + __uint_as_float(r[1]);
}
DI float sum_x32(float v) {
  auto r = __builtin_amdgcn_permlane32_swap(__float_as_uint(v), __float_as_uint(v), false, false);
  return __uint_as_float(r[0]) + __uint_as_float(r[1]);
}
DI u32x4 swap16(const u32x4& x) {
  auto r0 = __builtin_amdgcn_permlane16_swap(x[0], x[2], false, false);
  auto r1 = __builtin_amdgcn_permlane16_swap(x[1], x[3], false, false);
  return (u32x4){(unsigned)r0[0], (unsigned)r1[0], (unsigned)r0[1], (unsigned)r1[1]};
}
DI u32x2 lds_tr(unsigned a) { return __builtin_bit_cast(u32x2, __builtin_amdgcn_ds_read_tr16_b64_v4i16((LAS s16x4*)LDSP(a))); }
DI u32x4 cat(const u32x2& a, const u32x2& b) { return __builtin_shufflevector(a, b, 0, 1, 2, 3); }
DI u32x2 lds_r2(unsigned a) { return *(LAS u32x2*)LDSP(a); }
DI void lds_w4(unsigned a, const u32x4& v) { *(LAS u32x4*)LDSP(a) = v; }
DI unsigned rot(unsigned g, unsigned sl) { return (g + (0x641u >> 4 * sl)) & 15; }
DI int swzK(int u) { return ((((u >> 1) ^ u) & 1) << 1) | (u >> 1); }

#define STAMP(i)

__global__ __launch_bounds__(256, 1) void mega_kernel(const u16* __restrict__ x0, const u16* __restrict__ WqA,
                                                      const u16* __restrict__ WoA, const u16* __restrict__ WqB,
                                                      const u16* __restrict__ WoB, u16* __restrict__ out, char* ws) {
  const unsigned bid = blockIdx.x, xcd = bid & 7, bix = bid >> 3;
  const unsigned w = __builtin_amdgcn_readfirstlane(threadIdx.x >> 6), lane = threadIdx.x & 63;
  const unsigned c = (xcd >> 2) * 4 + (bix >> 3), h = (xcd & 3) * 8 + (bix & 7), par = h & 1, g = h >> 1;
  const unsigned p2 = h >> 3 & 1, g2 = (h >> 4) * 8 + (h & 7);
  const unsigned fr = lane & 15, fq = lane >> 4;
  const unsigned dl = 0, wr = (w + dl) & 3;
  const rsrc_t wsr = __builtin_amdgcn_make_buffer_rsrc(ws, 0, 128 * MB, 0x00020000);
  unsigned stw = 0;
  STAMP(0);
  const unsigned l16 = lane * 16, lw = w * 2048 + l16;
  const unsigned fl0 = lane == 0 ? 0u : OOB;
  const unsigned myfl = WS_FL + (c * 64 + h) * 128 + w * 8;
  const unsigned xo = (fr * 1024 + (((fq & 1) << 4) | ((fq >> 1) << 3))) * 2;
  u64 pc_; unsigned len_;
  asm volatile("s_getpc_b64 %0\n.Lmk_here:\n\ts_mov_b32 %1, .Lmk_end-.Lmk_here" : "=s"(pc_), "=s"(len_));
  const rsrc_t cr = __builtin_amdgcn_make_buffer_rsrc((void*)(pc_ & ~15ull), 0, len_ & ~15u, 0x00020000);
  const rsrc_t xr = __builtin_amdgcn_make_buffer_rsrc((void*)x0, 0, 4 * MB, 0x00020000);
  const unsigned osrc0 = (lane >> 3) * 2048 + (lane & 7) * 16;
  auto get_x = [&](int s) __attribute__((always_inline)) {
#pragma unroll
    for (int i = 0; i < 2; ++i) dma(xr, osrc0, ((c * 16 + s * 8 + 2 * wr + p2) * 16 + i * 8) * 2048 + g2 * 128, L_RES + s * 8192 + w * 2048 + i * 1024);
  };
  u32x2 tag;
  const rsrc_t wa = __builtin_amdgcn_make_buffer_rsrc((void*)WqA, 0, 6 * MB, 0x00020000);
  const unsigned rho = 2 * (c & 3), wbase = w * 256 * 6144 + h * 64;
  const unsigned wv = (lane >> 2) * 6144 + ((lane & 3) ^ ((lane >> 3) & 2)) * 16;
  auto piece = [&](int q, int j) __attribute__((always_inline)) {
    const unsigned k = j ^ par, src = uni(wbase + (((rho + 2 * q) & 7) + k) * 196608), dst = uni(L_WS + ((q & 1) * 8 + k * 4 + w) * 6144);
#pragma unroll
    for (int i = 0; i < 6; ++i) dma(wa, wv, src + (i & 1) * 98304 + (i >> 1) * 2048, dst + i * 1024);
  };
  const unsigned kb = 2 * c + par;
  const rsrc_t oa = __builtin_amdgcn_make_buffer_rsrc((void*)WoA, 0, 2 * MB, 0x00020000);
  const rsrc_t wb = __builtin_amdgcn_make_buffer_rsrc((void*)WqB, 0, 6 * MB, 0x00020000);
  const rsrc_t ob = __builtin_amdgcn_make_buffer_rsrc((void*)WoB, 0, 2 * MB, 0x00020000);
  const unsigned wsrc = (w * 16 + (lane >> 3)) * 6144 + (lane & 7) * 16, osrc = w * 16 * 2048 + osrc0;
  auto get_o = [&](int t) __attribute__((always_inline)) {
#pragma unroll
    for (int i = 0; i < 2; ++i) dma(t ? ob : oa, osrc, (kb * 64 + i * 8) * 2048 + rot(g, 1 + t) * 128, L_K + 24576 + t * 8192 + w * 2048 + i * 1024);
  };
  auto get_q = [&](int t) __attribute__((always_inline)) {
#pragma unroll
    for (int i = 0; i < 6; ++i)
      dma(t ? wb : wa, wsrc, (kb * 64 + (i & 1) * 8) * 6144 + (i >> 1) * 2048 + rot(g, i >> 1) * 128, (t ? L_K : L_INB) + (i >> 1) * 8192 + w * 2048 + (i & 1) * 1024);
  };
  const unsigned fr8 = fq == fr >> 2 ? 64 + fr * 4 : fq == ((fr >> 2) + 1 & 3) ? fr * 4 : OOB;
  u32x4 ones = {0x3F803F80u, 0x3F803F80u, 0x3F803F80u, 0x3F803F80u};
  unsigned sta = 0;
  auto publish = [&](int s, bool now) __attribute__((always_inline)) {
    const unsigned a = L_RES + s * 8192 + w * 2048 + fr * 128 + fq * 8, T = c * 16 + 8 * s + 2 * wr + p2;
    u32x4 y0 = cat(lds_r2(a), lds_r2(a + 32)), y1 = cat(lds_r2(a + 64), lds_r2(a + 96));
    f32x4 sm, gm;
    pin(y0); pin(y1); pin(ones);
    nops<2>();
    mm0_v(sm, ones, y0); mm0_v(gm, y0, y0); mma_v(sm, ones, y1); mma_v(gm, y1, y1);
    if (now) { st16<0>(y0, wsr, l16, WS_XF + T * 32768 + g2 * 2048); st16<1024>(y1, wsr, l16, WS_XF + T * 32768 + g2 * 2048); }
    lds_w4(lw + L_RES + s * 8192, y0); lds_w4(lw + L_RES + s * 8192 + 1024, y1);
    nops<8>();
    pin(sm); pin(gm);
    const unsigned st = __float_as_uint(fq != fr >> 2 ? sm[0] : fr & 2 ? (fr & 1 ? gm[3] : gm[2]) : (fr & 1 ? gm[1] : gm[0]));
    if (now) st4(st, wsr, fr8, WS_ST + T * 2048 + g2 * 128); else sta = st;
  };
  auto next_tag = [&]() __attribute__((always_inline)) {
    pin(tag);
    const u64 t = (((u64)tag[1] << 32) | tag[0]) + 1; tag = (u32x2){(unsigned)t, (unsigned)(t >> 32)};
  };
  const unsigned cb = c * 16 * 32768, skv = WS_KV + (c * 32 + h) * 32768;
  const unsigned soo = WS_OF + cb + wr * 65536 + h * 1024, sxo = WS_XF + cb + (2 * wr + p2) * 32768 + g2 * 2048, sso = WS_ST + (c * 16 + 2 * wr + p2) * 2048 + g2 * 128;
  auto clear = [&](int from, int to) __attribute__((always_inline)) {
    u32x4 z = {~0u, ~0u, ~0u, ~0u};
    pin(z);
#pragma unroll
    for (int k = from; k < to; ++k) {
      if (k < 8) st16<0>(z, wsr, l16, soo + (k == 4 ? 9 : k == 5 ? 8 : k - 6) * 32768);
      else if (k < 10) st16<0>(z, wsr, l16, sxo + 4 * MB + 8 * 32768 + (k - 8) * 1024);
      else st4(z[0], wsr, fr8, sso + 136 * 2048);
    }
  };
  dma(cr, l16, (bix * 4 + w) * 1024, L_SCR + 1024);
  get_x(1);
  get_x(0);
  ld8<0>(tag, wsr, 0u, WS_EP);
  piece(0, 0);
  touch(wsr, lane < 4 ? WS_FL + (lane >> 1) * 524288 + (w * 512 + c * 64 + (lane & 1) * 32 + h) * 128 : OOB);
  STAMP(29);

  unsigned ps[16];
  u32x8 pv = {};
#pragma unroll
  for (int j = 0; j < 3; ++j) {
    const unsigned ct = (w & 1) * 3 + j;
    pv[1 + j] = L_K + (ct >> 1) * 8192 + (4 * fq + (fr >> 2)) * 128 + (w >> 1) * 64 + (ct & 1) * 32 + (lane & 3) * 8;
    ps[j] = WS_WF + 6 * MB + ((2 * rot(g, ct >> 1) + (w >> 1)) * 192 + (kb >> 2) * 48 + (ct + 4) % 6 * 8 + (kb & 3) * 2) * 1024;
    ps[10 + j] = kb * 64 * 6144 + j * 2048 + rot(g, j) * 128;
  }
  { const unsigned ct = (w & 1) * 3 + (fr >> 1);
    pv[4] = ((2 * rot(g, ct >> 1) + (w >> 1)) * 16384 + ct * 128 + (fr & 1) * 64 + fq * 16) | (fr < 6 ? 0u : OOB); }
  ps[3] = WS_CS + 524288 + kb * 1024;
  ps[6] = (c >> 2) | par << 1 | (bix * 4 + w) << 10;                                            ps[7] = L_WS + w * 6144;
  ps[8] = wbase;                                             ps[9] = rho;
  ps[13] = kb * 64 * 2048 + rot(g, 1) * 128;                 ps[14] = kb * 64 * 2048 + rot(g, 2) * 128;
  ps[15] = WS_WF + (h * 4 + w) * 49152 + (rho + (c >> 2)) * 1024;
  pv[5] = wsrc;                                              pv[6] = osrc;
  u32x16 pw = {wv, L_WS + w * 6144 + (4 * fq + (fr >> 2)) * 64 + (fq & 1) * 32 + (lane & 3) * 8,
                    l16 + rho * 1024, l16 + ((rho + 2) & 7) * 1024, l16 + ((rho + 4) & 7) * 1024, l16 + ((rho + 6) & 7) * 1024,
                    L_WS + w * 6144 + (fr == 0 ? fq * 16 : 1024 + l16), L_WS + (w * 24 + (lane < 24 ? lane : 0)) * 4,
                    (w * 128 + (lane >> 3 & 1) * 64 + 2 * ((g2 - rot(0, 1 + (fq & 1))) & 15) + (lane >> 2 & 1)) * 128 + (lane & 3) * 8};
  if (w < 2) {
    u32x4 pm;
#pragma unroll
    for (int k = 0; k < 4; ++k)
      pm[k] = (8 * fq + 2 * k > 16 * w + fr ? 0xFFFFu : 0u) | (8 * fq + 2 * k + 1 > 16 * w + fr ? 0xFFFF0000u : 0u);
    lds_w4(L_PM + w * 1024 + l16, pm);
  } else {
    const unsigned want = (unsigned)((0x5555ull << 16) >> (fr + 25 - 8 * fq));
    u32x4 mb;
#pragma unroll
    for (int k = 0; k < 4; ++k) mb[k] = (want >> (15 - 4 * w - k)) & 1 ? 0u : 0xFF800000u;
    lds_w4(L_MB + (w - 2) * 1024 + l16, mb);
  }
  pv[0] = L_K + 24576 + (16 * (fq & 1) + 4 * (fq >> 1) + (fr >> 2)) * 128 + w * 32 + (lane & 3) * 8;
  pv[7] = lane < 2 ? lane * 32 * 128 : OOB;
  ps[4] = WS_WO + (rot(g, 1) * 128 + (kb >> 2) * 32 + w * 8 + (kb & 3) * 2) * 1024;
  ps[5] = WS_WO + 2 * MB + (rot(g, 2) * 128 + (kb >> 2) * 32 + w * 8 + (kb & 3) * 2) * 1024;
  const unsigned krow = (8 * (fr >> 2) + (fr & 3)) * 64 + wr * 2048 + L_K;
  const unsigned kv0 = 16384 + wr * 2048 + fr * 64 + ((fq ^ ((fr >> 3) << 1) ^ ((fr >> 1) & 3)) * 16);
  u32x16 va = {l16, l16 + 4096, lw, L_INB + w * 18432 + l16, L_INB + ((w + 3) & 3) * 18432 + l16, L_INB + ((w + 2) & 3) * 18432 + 6144 + l16,
                     L_INB + ((w + 1) & 3) * 18432 + 12288 + l16, kv0, kv0 ^ 48, L_CS + fq * 16, lane * 4, fl0,
                     ((lane & 31) >> 2) * 128 + (lane & 3) * 8,
                     fq < 3 ? (w * 128 + (lane >> 3 & 1) * 64 + 2 * ((2 * g - rot(g, fq % 3)) & 15) + (lane >> 2 & 1)) * 128 + (lane & 3) * 8 : ((lane & 7) * 64 + h) * 128 + w * 8,
                     fr8, krow + (fq ^ swzK(fr >> 2) ^ ((fr >> 1) & 1)) * 16};
  u32x8 vb = {L_V + wr * 2048 + (8 * fq + (fr >> 2)) * 64 + 8 * (lane & 1) + (((lane >> 1) & 1) ^ ((fq & 1) << 1) ^ (fr >> 3)) * 16,
                    krow + (fq ^ swzK(((fr >> 2) + 2) & 3) ^ ((fr >> 1) & 1)) * 16, 0, 0, xo,
                    lane < 24 ? L_CS + w * 96 + lane * 4 : L_SCR, w == 0 ? bid * 1024 + lane * 4 : OOB, 0};
  u32x16 pz = {pw[2], pw[3], pw[4], pw[5], pv[1], pv[2], pv[3], pv[4], pv[0], pw[8], pw[0], pw[1], pw[6], pw[7], pv[5], pv[6]};
  unsigned pf = pv[7];
  pin(va); pin(vb); pin(pz); pin(pf);
  piece(0, 1);
  wait_vm<13>();
  next_tag();
  STAMP(18);
  publish(1, true);
  publish(0, true);
  STAMP(1);
  piece(1, 0);
  wait_vm<6>();
  st8<0>(tag, wsr, lane < 2 ? lane * 32 * 128 : OOB, myfl + (2 * g2 + p2 - h) * 128);
  piece(1, 1);
  STAMP(15);

  u32x16 sa, sb;
#pragma unroll
  for (int i = 0; i < 4; ++i) {
    sa[i] = WS_XF + cb + w * 8192 + ((wr + 1 + i) & 3) * 65536;
    sa[4 + i] = sa[i] + WS_OF + p2 * 32768;
  }
  sa[8] = soo;                                              sa[9] = sxo;
  sa[10] = WS_ST + (c * 16 + 2 * wr) * 2048;                sa[11] = sso;
  sa[12] = skv - 16384;                                     sa[13] = c ? skv - 32 * 32768 : WS_XF + (h & 15) * 32768;
  sa[14] = c ? skv - 32 * 32768 + 8 * MB : sa[13];          sa[15] = WS_FL + (c * 64 + 8 * w) * 128;
  sb[0] = myfl;                                             sb[1] = WS_WF + (h * 4 + w) * 49152;
  sb[2] = WS_WO + (g2 * 4 + w) * 32768;                     sb[3] = c ? myfl - w * 8 + ((w - dl) & 3) * 8 - 32 * 128 + 64 : myfl + 4096;
  sb[4] = w * 96;                                           sb[5] = c ? myfl + 65536 - w * 8 + ((3 - dl) & 3) * 8 - 32 * 128 + 64 : myfl + 4096;
  sb[6] = ((c * 16 + 2 * wr + p2) * 16 * 1024 + g2 * 64) * 2;  sb[7] = WS_FL + 96;
  sb[8] = WS_CS + h * 16384;                                sb[9] = w * 2048;
  sb[10] = c ? -8 : 8 - wr;                                  sb[11] = c ? 16 * MB : 0;
  sb[12] = (2 * g2 + p2 - h) * 128;                                sb[13] = bid == 0 && w == 0 ? WS_EP : 0;
  sb[14] = c == 0 && wr == 0 ? ~0u : 0u;                     sb[15] = 0;
#pragma unroll
  for (int i = 0; i < 16; ++i) { sa[i] = uni(sa[i]); sb[i] = uni(sb[i]); }
  const rsrc_t wsk = __builtin_amdgcn_make_buffer_rsrc(ws, 0, wr == 3 ? 128 * MB : 0u, 0x00020000);
  const rsrc_t outr = __builtin_amdgcn_make_buffer_rsrc(out, 0, 4 * MB, 0x00020000);
  u32x8 pa, pc;
#pragma unroll
  for (int i = 0; i < 8; ++i) { pa[i] = uni(ps[i]); pc[i] = uni(ps[8 + i]); }
  STAMP(17);

  vb[2] = tag[0]; vb[3] = tag[1];
  STAMP(19);
  vb[7] = stw;
  asm volatile(
"; @entry\n"
"s_mov_b32 s0, 0\n"
"s_mov_b32 s3, 0\n"
"v_xor_b32_e32 v255, 32, v43\n"
"v_add_u32_e32 v254, 0xc000, v43\n"
"v_add_u32_e32 v253, 0xc000, v255\n"
"v_mov_b32_e32 v24, 0x3f803f80\n"
"v_mov_b32_e32 v25, 0x3f803f80\n"
"v_mov_b32_e32 v26, 0x3f803f80\n"
"v_mov_b32_e32 v27, 0x3f803f80\n"
"s_waitcnt lgkmcnt(0)\n"
"s_waitcnt vmcnt(19)\n"
"ds_read_b64_tr_b16 v[28:29], v43\n"
"ds_read_b64_tr_b16 v[30:31], v43 offset:1024\n"
"ds_read_b64_tr_b16 v[48:49], v255\n"
"ds_read_b64_tr_b16 v[50:51], v255 offset:1024\n"
"ds_read_b64_tr_b16 a[0:1], v43 offset:2048\n"
"ds_read_b64_tr_b16 a[2:3], v43 offset:3072\n"
"ds_read_b64_tr_b16 a[32:33], v255 offset:2048\n"
"ds_read_b64_tr_b16 a[34:35], v255 offset:3072\n"
"ds_read_b64_tr_b16 a[64:65], v43 offset:4096\n"
"ds_read_b64_tr_b16 a[66:67], v43 offset:5120\n"
"ds_read_b64_tr_b16 a[96:97], v255 offset:4096\n"
"ds_read_b64_tr_b16 a[98:99], v255 offset:5120\n"
"ds_read_b64_tr_b16 v[52:53], v43 offset:24576\n"
"ds_read_b64_tr_b16 v[54:55], v43 offset:25600\n"
"ds_read_b64_tr_b16 v[56:57], v255 offset:24576\n"
"s_waitcnt lgkmcnt(13)\n"
"ds_read_b64_tr_b16 v[58:59], v255 offset:25600\n"
"ds_read_b64_tr_b16 a[4:5], v43 offset:26624\n"
"s_waitcnt lgkmcnt(13)\n"
"ds_read_b64_tr_b16 a[6:7], v43 offset:27648\n"
"ds_read_b64_tr_b16 a[36:37], v255 offset:26624\n"
"s_waitcnt lgkmcnt(13)\n"
"ds_read_b64_tr_b16 a[38:39], v255 offset:27648\n"
"ds_read_b64_tr_b16 a[68:69], v43 offset:28672\n"
"s_waitcnt lgkmcnt(13)\n"
"ds_read_b64_tr_b16 a[70:71], v43 offset:29696\n"
"ds_read_b64_tr_b16 a[100:101], v255 offset:28672\n"
"s_waitcnt lgkmcnt(13)\n"
"ds_read_b64_tr_b16 a[102:103], v255 offset:29696\n"
"s_waitcnt lgkmcnt(12)\n"
"v_mfma_f32_16x16x32_bf16 v[60:63], v[28:31], v[24:27], 0\n"
"v_mfma_f32_16x16x32_bf16 v[64:67], v[48:51], v[24:27], 0\n"
"v_mfma_f32_16x16x32_bf16 v[68:71], a[0:3], v[24:27], 0\n"
"v_mfma_f32_16x16x32_bf16 v[72:75], a[32:35], v[24:27], 0\n"
"v_mfma_f32_16x16x32_bf16 v[76:79], a[64:67], v[24:27], 0\n"
"v_mfma_f32_16x16x32_bf16 v[80:83], a[96:99], v[24:27], 0\n"
"s_waitcnt lgkmcnt(0)\n"
"v_mfma_f32_16x16x32_bf16 v[60:63], v[52:55], v[24:27], v[60:63]\n"
"v_mfma_f32_16x16x32_bf16 v[64:67], v[56:59], v[24:27], v[64:67]\n"
"v_mfma_f32_16x16x32_bf16 v[68:71], a[4:7], v[24:27], v[68:71]\n"
"v_mfma_f32_16x16x32_bf16 v[72:75], a[36:39], v[24:27], v[72:75]\n"
"v_mfma_f32_16x16x32_bf16 v[76:79], a[68:71], v[24:27], v[76:79]\n"
"v_mfma_f32_16x16x32_bf16 v[80:83], a[100:103], v[24:27], v[80:83]\n"
"s_bfe_u32 s98, s26, 0x10001\n"
"s_xor_b32 s98, s98, 0\n"
"s_mul_i32 s97, s98, 0x6000\n"
"s_add_u32 s97, s97, s27\n"
"s_add_u32 s96, s45, 4\n"
"s_and_b32 s96, s96, 7\n"
"s_add_u32 s96, s96, s98\n"
"s_mul_i32 s96, s96, 0x30000\n"
"s_add_u32 s96, s96, s44\n"
"s_add_u32 m0, s97, 0\n"
"s_add_u32 s98, s96, 0\n"
"buffer_load_dwordx4 v42, s[28:31], s98 offen lds\n"
"s_add_u32 m0, s97, 0x400\n"
"s_add_u32 s98, s96, 0x18000\n"
"buffer_load_dwordx4 v42, s[28:31], s98 offen lds\n"
"s_add_u32 m0, s97, 0x800\n"
"s_add_u32 s98, s96, 0x800\n"
"buffer_load_dwordx4 v42, s[28:31], s98 offen lds\n"
"s_add_u32 m0, s97, 0xc00\n"
"s_add_u32 s98, s96, 0x18800\n"
"buffer_load_dwordx4 v42, s[28:31], s98 offen lds\n"
"s_add_u32 m0, s97, 0x1000\n"
"s_add_u32 s98, s96, 0x1000\n"
"buffer_load_dwordx4 v42, s[28:31], s98 offen lds\n"
"s_add_u32 m0, s97, 0x1400\n"
"s_add_u32 s98, s96, 0x19000\n"
"buffer_load_dwordx4 v42, s[28:31], s98 offen lds\n"
"s_bfe_u32 s98, s26, 0x10001\n"
"s_xor_b32 s98, s98, 1\n"
"s_mul_i32 s97, s98, 0x6000\n"
"s_add_u32 s97, s97, s27\n"
"s_add_u32 s96, s45, 4\n"
"s_and_b32 s96, s96, 7\n"
"s_add_u32 s96, s96, s98\n"
"s_mul_i32 s96, s96, 0x30000\n"
"s_add_u32 s96, s96, s44\n"
"s_add_u32 m0, s97, 0\n"
"s_add_u32 s98, s96, 0\n"
"buffer_load_dwordx4 v42, s[28:31], s98 offen lds\n"
"s_add_u32 m0, s97, 0x400\n"
"s_add_u32 s98, s96, 0x18000\n"
"buffer_load_dwordx4 v42, s[28:31], s98 offen lds\n"
"s_add_u32 m0, s97, 0x800\n"
"s_add_u32 s98, s96, 0x800\n"
"buffer_load_dwordx4 v42, s[28:31], s98 offen lds\n"
"s_add_u32 m0, s97, 0xc00\n"
"s_add_u32 s98, s96, 0x18800\n"
"buffer_load_dwordx4 v42, s[28:31], s98 offen lds\n"
"s_add_u32 m0, s97, 0x1000\n"
"s_add_u32 s98, s96, 0x1000\n"
"buffer_load_dwordx4 v42, s[28:31], s98 offen lds\n"
"s_add_u32 m0, s97, 0x1400\n"
"s_add_u32 s98, s96, 0x19000\n"
"buffer_load_dwordx4 v42, s[28:31], s98 offen lds\n"
"s_waitcnt vmcnt(12)\n"
"ds_read_b64_tr_b16 v[84:85], v254\n"
"ds_read_b64_tr_b16 v[86:87], v254 offset:1024\n"
"ds_read_b64_tr_b16 v[88:89], v253\n"
"ds_read_b64_tr_b16 v[90:91], v253 offset:1024\n"
"ds_read_b64_tr_b16 a[8:9], v254 offset:2048\n"
"ds_read_b64_tr_b16 a[10:11], v254 offset:3072\n"
"ds_read_b64_tr_b16 a[40:41], v253 offset:2048\n"
"ds_read_b64_tr_b16 a[42:43], v253 offset:3072\n"
"ds_read_b64_tr_b16 a[72:73], v254 offset:4096\n"
"ds_read_b64_tr_b16 a[74:75], v254 offset:5120\n"
"ds_read_b64_tr_b16 a[104:105], v253 offset:4096\n"
"ds_read_b64_tr_b16 a[106:107], v253 offset:5120\n"
"ds_read_b64_tr_b16 v[92:93], v254 offset:24576\n"
"ds_read_b64_tr_b16 v[94:95], v254 offset:25600\n"
"ds_read_b64_tr_b16 v[96:97], v253 offset:24576\n"
"s_waitcnt lgkmcnt(13)\n"
"ds_read_b64_tr_b16 v[98:99], v253 offset:25600\n"
"ds_read_b64_tr_b16 a[12:13], v254 offset:26624\n"
"s_waitcnt lgkmcnt(13)\n"
"ds_read_b64_tr_b16 a[14:15], v254 offset:27648\n"
"ds_read_b64_tr_b16 a[44:45], v253 offset:26624\n"
"s_waitcnt lgkmcnt(13)\n"
"ds_read_b64_tr_b16 a[46:47], v253 offset:27648\n"
"ds_read_b64_tr_b16 a[76:77], v254 offset:28672\n"
"s_waitcnt lgkmcnt(13)\n"
"ds_read_b64_tr_b16 a[78:79], v254 offset:29696\n"
"ds_read_b64_tr_b16 a[108:109], v253 offset:28672\n"
"s_waitcnt lgkmcnt(13)\n"
"ds_read_b64_tr_b16 a[110:111], v253 offset:29696\n"
"s_waitcnt lgkmcnt(12)\n"
"v_mfma_f32_16x16x32_bf16 v[60:63], v[84:87], v[24:27], v[60:63]\n"
"v_mfma_f32_16x16x32_bf16 v[64:67], v[88:91], v[24:27], v[64:67]\n"
"v_mfma_f32_16x16x32_bf16 v[68:71], a[8:11], v[24:27], v[68:71]\n"
"v_mfma_f32_16x16x32_bf16 v[72:75], a[40:43], v[24:27], v[72:75]\n"
"v_mfma_f32_16x16x32_bf16 v[76:79], a[72:75], v[24:27], v[76:79]\n"
"v_mfma_f32_16x16x32_bf16 v[80:83], a[104:107], v[24:27], v[80:83]\n"
"s_waitcnt lgkmcnt(0)\n"
"v_mfma_f32_16x16x32_bf16 v[60:63], v[92:95], v[24:27], v[60:63]\n"
"v_mfma_f32_16x16x32_bf16 v[64:67], v[96:99], v[24:27], v[64:67]\n"
"v_mfma_f32_16x16x32_bf16 v[68:71], a[12:15], v[24:27], v[68:71]\n"
"v_mfma_f32_16x16x32_bf16 v[72:75], a[44:47], v[24:27], v[72:75]\n"
"v_mfma_f32_16x16x32_bf16 v[76:79], a[76:79], v[24:27], v[76:79]\n"
"v_mfma_f32_16x16x32_bf16 v[80:83], a[108:111], v[24:27], v[80:83]\n"
"s_bfe_u32 s98, s26, 0x10001\n"
"s_xor_b32 s98, s98, 0\n"
"s_mul_i32 s97, s98, 0x6000\n"
"s_add_u32 s97, s97, s27\n"
"s_add_u32 s96, s45, 6\n"
"s_and_b32 s96, s96, 7\n"
"s_add_u32 s96, s96, s98\n"
"s_mul_i32 s96, s96, 0x30000\n"
"s_add_u32 s96, s96, s44\n"
"s_add_u32 m0, s97, 0xc000\n"
"s_add_u32 s98, s96, 0\n"
"buffer_load_dwordx4 v42, s[28:31], s98 offen lds\n"
"s_add_u32 m0, s97, 0xc400\n"
"s_add_u32 s98, s96, 0x18000\n"
"buffer_load_dwordx4 v42, s[28:31], s98 offen lds\n"
"s_add_u32 m0, s97, 0xc800\n"
"s_add_u32 s98, s96, 0x800\n"
"buffer_load_dwordx4 v42, s[28:31], s98 offen lds\n"
"s_add_u32 m0, s97, 0xcc00\n"
"s_add_u32 s98, s96, 0x18800\n"
"buffer_load_dwordx4 v42, s[28:31], s98 offen lds\n"
"s_add_u32 m0, s97, 0xd000\n"
"s_add_u32 s98, s96, 0x1000\n"
"buffer_load_dwordx4 v42, s[28:31], s98 offen lds\n"
"s_add_u32 m0, s97, 0xd400\n"
"s_add_u32 s98, s96, 0x19000\n"
"buffer_load_dwordx4 v42, s[28:31], s98 offen lds\n"
"s_bfe_u32 s98, s26, 0x10001\n"
"s_xor_b32 s98, s98, 1\n"
"s_mul_i32 s97, s98, 0x6000\n"
"s_add_u32 s97, s97, s27\n"
"s_add_u32 s96, s45, 6\n"
"s_and_b32 s96, s96, 7\n"
"s_add_u32 s96, s96, s98\n"
"s_mul_i32 s96, s96, 0x30000\n"
"s_add_u32 s96, s96, s44\n"
"s_add_u32 m0, s97, 0xc000\n"
"s_add_u32 s98, s96, 0\n"
"buffer_load_dwordx4 v42, s[28:31], s98 offen lds\n"
"s_add_u32 m0, s97, 0xc400\n"
"s_add_u32 s98, s96, 0x18000\n"
"buffer_load_dwordx4 v42, s[28:31], s98 offen lds\n"
"s_add_u32 m0, s97, 0xc800\n"
"s_add_u32 s98, s96, 0x800\n"
"buffer_load_dwordx4 v42, s[28:31], s98 offen lds\n"
"s_add_u32 m0, s97, 0xcc00\n"
"s_add_u32 s98, s96, 0x18800\n"
"buffer_load_dwordx4 v42, s[28:31], s98 offen lds\n"
"s_add_u32 m0, s97, 0xd000\n"
"s_add_u32 s98, s96, 0x1000\n"
"buffer_load_dwordx4 v42, s[28:31], s98 offen lds\n"
"s_add_u32 m0, s97, 0xd400\n"
"s_add_u32 s98, s96, 0x19000\n"
"buffer_load_dwordx4 v42, s[28:31], s98 offen lds\n"
"s_add_u32 s98, s67, 0x1000\n"
"buffer_load_dwordx2 v[250:251], v12, s[4:7], s98 offen sc1\n"
"s_waitcnt vmcnt(13)\n"
"ds_read_b64_tr_b16 v[100:101], v43\n"
"ds_read_b64_tr_b16 v[102:103], v43 offset:1024\n"
"ds_read_b64_tr_b16 v[104:105], v255\n"
"ds_read_b64_tr_b16 v[106:107], v255 offset:1024\n"
"ds_read_b64_tr_b16 a[16:17], v43 offset:2048\n"
"ds_read_b64_tr_b16 a[18:19], v43 offset:3072\n"
"ds_read_b64_tr_b16 a[48:49], v255 offset:2048\n"
"ds_read_b64_tr_b16 a[50:51], v255 offset:3072\n"
"ds_read_b64_tr_b16 a[80:81], v43 offset:4096\n"
"ds_read_b64_tr_b16 a[82:83], v43 offset:5120\n"
"ds_read_b64_tr_b16 a[112:113], v255 offset:4096\n"
"ds_read_b64_tr_b16 a[114:115], v255 offset:5120\n"
"ds_read_b64_tr_b16 v[108:109], v43 offset:24576\n"
"ds_read_b64_tr_b16 v[110:111], v43 offset:25600\n"
"ds_read_b64_tr_b16 v[112:113], v255 offset:24576\n"
"s_waitcnt lgkmcnt(13)\n"
"ds_read_b64_tr_b16 v[114:115], v255 offset:25600\n"
"ds_read_b64_tr_b16 a[20:21], v43 offset:26624\n"
"s_waitcnt lgkmcnt(13)\n"
"ds_read_b64_tr_b16 a[22:23], v43 offset:27648\n"
"ds_read_b64_tr_b16 a[52:53], v255 offset:26624\n"
"s_waitcnt lgkmcnt(13)\n"
"ds_read_b64_tr_b16 a[54:55], v255 offset:27648\n"
"ds_read_b64_tr_b16 a[84:85], v43 offset:28672\n"
"s_waitcnt lgkmcnt(13)\n"
"ds_read_b64_tr_b16 a[86:87], v43 offset:29696\n"
"ds_read_b64_tr_b16 a[116:117], v255 offset:28672\n"
"s_waitcnt lgkmcnt(13)\n"
"ds_read_b64_tr_b16 a[118:119], v255 offset:29696\n"
"s_waitcnt lgkmcnt(12)\n"
"v_mfma_f32_16x16x32_bf16 v[60:63], v[100:103], v[24:27], v[60:63]\n"
"v_mfma_f32_16x16x32_bf16 v[64:67], v[104:107], v[24:27], v[64:67]\n"
"v_mfma_f32_16x16x32_bf16 v[68:71], a[16:19], v[24:27], v[68:71]\n"
"v_mfma_f32_16x16x32_bf16 v[72:75], a[48:51], v[24:27], v[72:75]\n"
"v_mfma_f32_16x16x32_bf16 v[76:79], a[80:83], v[24:27], v[76:79]\n"
"v_mfma_f32_16x16x32_bf16 v[80:83], a[112:115], v[24:27], v[80:83]\n"
"s_waitcnt lgkmcnt(0)\n"
"v_mfma_f32_16x16x32_bf16 v[60:63], v[108:111], v[24:27], v[60:63]\n"
"v_mfma_f32_16x16x32_bf16 v[64:67], v[112:115], v[24:27], v[64:67]\n"
"v_mfma_f32_16x16x32_bf16 v[68:71], a[20:23], v[24:27], v[68:71]\n"
"v_mfma_f32_16x16x32_bf16 v[72:75], a[52:55], v[24:27], v[72:75]\n"
"v_mfma_f32_16x16x32_bf16 v[76:79], a[84:87], v[24:27], v[76:79]\n"
"v_mfma_f32_16x16x32_bf16 v[80:83], a[116:119], v[24:27], v[80:83]\n"
"s_waitcnt vmcnt(0)\n"
"v_cmp_ne_u64_e32 vcc, v[250:251], v[18:19]\n"
"s_cbranch_vccnz .Lmk_f1s\n"
".Lmk_f1r:\n"
"s_add_u32 s98, s52, 0x40000\n"
"buffer_load_dwordx4 v[116:119], v32, s[4:7], s98 offen sc1\n"
"buffer_load_dwordx4 v[120:123], v32, s[4:7], s98 offen offset:1024 sc1\n"
"buffer_load_dwordx4 v[124:127], v33, s[4:7], s98 offen sc1\n"
"buffer_load_dwordx4 v[128:131], v33, s[4:7], s98 offen offset:1024 sc1\n"
"buffer_load_dwordx4 v[132:135], v34, s[4:7], s98 offen sc1\n"
"buffer_load_dwordx4 v[136:139], v34, s[4:7], s98 offen offset:1024 sc1\n"
"buffer_load_dwordx4 v[140:143], v35, s[4:7], s98 offen sc1\n"
"buffer_load_dwordx4 v[144:147], v35, s[4:7], s98 offen offset:1024 sc1\n"
"s_add_u32 s98, s53, 0x40000\n"
"buffer_load_dwordx4 v[148:151], v32, s[4:7], s98 offen sc1\n"
"buffer_load_dwordx4 v[152:155], v32, s[4:7], s98 offen offset:1024 sc1\n"
"buffer_load_dwordx4 v[156:159], v33, s[4:7], s98 offen sc1\n"
"buffer_load_dwordx4 v[160:163], v33, s[4:7], s98 offen offset:1024 sc1\n"
"buffer_load_dwordx4 v[164:167], v34, s[4:7], s98 offen sc1\n"
"buffer_load_dwordx4 v[168:171], v34, s[4:7], s98 offen offset:1024 sc1\n"
"buffer_load_dwordx4 v[172:175], v35, s[4:7], s98 offen sc1\n"
"buffer_load_dwordx4 v[176:179], v35, s[4:7], s98 offen offset:1024 sc1\n"
"s_getpc_b64 s[84:85]\n"
".Lmk_h2:\n"
"s_and_b32 s84, s84, -16\n"
"s_and_b32 s85, s85, 0xffff\n"
"s_mov_b32 s86, (.Lmk_end-.Lmk_h2)&0xFFFFFFF0\n"
"s_mov_b32 s87, 0x20000\n"
"s_and_b32 s98, s26, 0xfffffc00\n"
"s_mov_b32 m0, 0x27800\n"
"s_add_u32 s98, s98, 0x1e000\n"
"buffer_load_dwordx4 v0, s[84:87], s98 offen lds\n"
"ds_read_b64_tr_b16 v[180:181], v254\n"
"ds_read_b64_tr_b16 v[182:183], v254 offset:1024\n"
"ds_read_b64_tr_b16 v[184:185], v253\n"
"ds_read_b64_tr_b16 v[186:187], v253 offset:1024\n"
"ds_read_b64_tr_b16 a[24:25], v254 offset:2048\n"
"ds_read_b64_tr_b16 a[26:27], v254 offset:3072\n"
"ds_read_b64_tr_b16 a[56:57], v253 offset:2048\n"
"ds_read_b64_tr_b16 a[58:59], v253 offset:3072\n"
"ds_read_b64_tr_b16 a[88:89], v254 offset:4096\n"
"ds_read_b64_tr_b16 a[90:91], v254 offset:5120\n"
"ds_read_b64_tr_b16 a[120:121], v253 offset:4096\n"
"ds_read_b64_tr_b16 a[122:123], v253 offset:5120\n"
"ds_read_b64_tr_b16 v[188:189], v254 offset:24576\n"
"ds_read_b64_tr_b16 v[190:191], v254 offset:25600\n"
"ds_read_b64_tr_b16 v[192:193], v253 offset:24576\n"
"s_waitcnt lgkmcnt(13)\n"
"ds_read_b64_tr_b16 v[194:195], v253 offset:25600\n"
"ds_read_b64_tr_b16 a[28:29], v254 offset:26624\n"
"s_waitcnt lgkmcnt(13)\n"
"ds_read_b64_tr_b16 a[30:31], v254 offset:27648\n"
"ds_read_b64_tr_b16 a[60:61], v253 offset:26624\n"
"s_waitcnt lgkmcnt(13)\n"
"ds_read_b64_tr_b16 a[62:63], v253 offset:27648\n"
"ds_read_b64_tr_b16 a[92:93], v254 offset:28672\n"
"s_waitcnt lgkmcnt(13)\n"
"ds_read_b64_tr_b16 a[94:95], v254 offset:29696\n"
"ds_read_b64_tr_b16 a[124:125], v253 offset:28672\n"
"s_waitcnt lgkmcnt(13)\n"
"ds_read_b64_tr_b16 a[126:127], v253 offset:29696\n"
"s_waitcnt lgkmcnt(12)\n"
"v_mfma_f32_16x16x32_bf16 v[60:63], v[180:183], v[24:27], v[60:63]\n"
"v_mfma_f32_16x16x32_bf16 v[64:67], v[184:187], v[24:27], v[64:67]\n"
"v_mfma_f32_16x16x32_bf16 v[68:71], a[24:27], v[24:27], v[68:71]\n"
"v_mfma_f32_16x16x32_bf16 v[72:75], a[56:59], v[24:27], v[72:75]\n"
"v_mfma_f32_16x16x32_bf16 v[76:79], a[88:91], v[24:27], v[76:79]\n"
"v_mfma_f32_16x16x32_bf16 v[80:83], a[120:123], v[24:27], v[80:83]\n"
"s_waitcnt lgkmcnt(0)\n"
"v_mfma_f32_16x16x32_bf16 v[60:63], v[188:191], v[24:27], v[60:63]\n"
"v_mfma_f32_16x16x32_bf16 v[64:67], v[192:195], v[24:27], v[64:67]\n"
"v_mfma_f32_16x16x32_bf16 v[68:71], a[28:31], v[24:27], v[68:71]\n"
"v_mfma_f32_16x16x32_bf16 v[72:75], a[60:63], v[24:27], v[72:75]\n"
"v_mfma_f32_16x16x32_bf16 v[76:79], a[92:95], v[24:27], v[76:79]\n"
"v_mfma_f32_16x16x32_bf16 v[80:83], a[124:127], v[24:27], v[80:83]\n"
"s_nop 7\n"
"ds_write_b128 v44, v[60:63]\n"
"ds_write_b128 v44, v[64:67] offset:64\n"
"ds_write_b128 v44, v[68:71] offset:128\n"
"ds_write_b128 v44, v[72:75] offset:192\n"
"ds_write_b128 v44, v[76:79] offset:256\n"
"ds_write_b128 v44, v[80:83] offset:320\n"
"s_waitcnt lgkmcnt(0)\n"
"s_barrier\n"
"ds_read_b32 v255, v45\n"
"ds_read_b32 v254, v45 offset:6144\n"
"ds_read_b32 v253, v45 offset:12288\n"
"ds_read_b32 v252, v45 offset:18432\n"
"s_waitcnt lgkmcnt(0)\n"
"v_add_f32_e32 v255, v255, v254\n"
"v_add_f32_e32 v253, v253, v252\n"
"v_add_f32_e32 v255, v255, v253\n"
"ds_write_b32 v21, v255\n"
".Lmk_z0:\n"
"s_waitcnt vmcnt(13)\n"
"; @A G1 0.0\n"
"v_mfma_f32_16x16x32_bf16 v[24:27], a[0:3], v[116:119], 0\n"
"v_mfma_f32_16x16x32_bf16 v[24:27], a[4:7], v[120:123], v[24:27]\n"
"s_add_u32 m0, s77, 0xa000\n"
"s_add_u32 s98, s49, 0\n"
"buffer_load_dwordx4 v47, s[36:39], s98 offen lds\n"
"v_mfma_f32_16x16x32_bf16 v[24:27], a[8:11], v[124:127], v[24:27]\n"
"s_add_u32 s98, s54, 0x40000\n"
"buffer_load_dwordx4 v[60:63], v32, s[4:7], s98 offen sc1\n"
"s_add_u32 m0, s77, 0xa400\n"
"v_mfma_f32_16x16x32_bf16 v[24:27], a[12:15], v[128:131], v[24:27]\n"
"s_waitcnt vmcnt(11)\n"
"s_add_u32 s97, s49, 0x4000\n"
"s_add_u32 s96, s62, 0x4000\n"
"v_mfma_f32_16x16x32_bf16 v[24:27], a[16:19], v[132:135], v[24:27]\n"
"buffer_load_dwordx4 v47, s[36:39], s97 offen lds\n"
"v_mfma_f32_16x16x32_bf16 v[24:27], a[20:23], v[136:139], v[24:27]\n"
"s_add_u32 m0, s77, 0xc000\n"
"s_add_u32 s97, s50, 0\n"
"v_mfma_f32_16x16x32_bf16 v[24:27], a[24:27], v[140:143], v[24:27]\n"
"buffer_load_dword v255, v10, s[4:7], s96 offen sc1\n"
"v_mfma_f32_16x16x32_bf16 v[24:27], a[28:31], v[144:147], v[24:27]\n"
"buffer_load_dwordx4 v[64:67], v32, s[4:7], s98 offen offset:1024 sc1\n"
"buffer_load_dwordx4 v47, s[40:43], s97 offen lds\n"
"v_mfma_f32_16x16x32_bf16 v[68:71], a[32:35], v[116:119], 0\n"
"s_add_u32 m0, s77, 0xc400\n"
"s_add_u32 s97, s50, 0x4000\n"
"v_mfma_f32_16x16x32_bf16 v[68:71], a[36:39], v[120:123], v[68:71]\n"
"buffer_load_dword v254, v10, s[4:7], s96 offen offset:256 sc1\n"
"v_mfma_f32_16x16x32_bf16 v[68:71], a[40:43], v[124:127], v[68:71]\n"
"buffer_load_dwordx4 v47, s[40:43], s97 offen lds\n"
"v_mfma_f32_16x16x32_bf16 v[68:71], a[44:47], v[128:131], v[68:71]\n"
"s_add_u32 m0, s77, 0x4000\n"
"s_add_u32 s97, s46, 0\n"
"v_mfma_f32_16x16x32_bf16 v[68:71], a[48:51], v[132:135], v[68:71]\n"
"buffer_load_dwordx4 v[72:75], v33, s[4:7], s98 offen sc1\n"
"v_mfma_f32_16x16x32_bf16 v[68:71], a[52:55], v[136:139], v[68:71]\n"
"ds_write_b128 v3, v[24:27]\n"
"v_mfma_f32_16x16x32_bf16 v[68:71], a[56:59], v[140:143], v[68:71]\n"
"buffer_load_dwordx4 v46, s[32:35], s97 offen lds\n"
"v_mfma_f32_16x16x32_bf16 v[68:71], a[60:63], v[144:147], v[68:71]\n"
"s_add_u32 m0, s77, 0x4400\n"
"s_add_u32 s97, s46, 0xc000\n"
"v_mfma_f32_16x16x32_bf16 v[24:27], a[64:67], v[116:119], 0\n"
"buffer_load_dword v253, v10, s[4:7], s96 offen offset:512 sc1\n"
"v_mfma_f32_16x16x32_bf16 v[24:27], a[68:71], v[120:123], v[24:27]\n"
"buffer_load_dwordx4 v[76:79], v33, s[4:7], s98 offen offset:1024 sc1\n"
"buffer_load_dwordx4 v46, s[32:35], s97 offen lds\n"
"v_mfma_f32_16x16x32_bf16 v[24:27], a[72:75], v[124:127], v[24:27]\n"
"s_add_u32 m0, s77, 0x6000\n"
"s_add_u32 s97, s47, 0\n"
"v_mfma_f32_16x16x32_bf16 v[24:27], a[76:79], v[128:131], v[24:27]\n"
"buffer_load_dword v252, v10, s[4:7], s96 offen offset:768 sc1\n"
"v_mfma_f32_16x16x32_bf16 v[24:27], a[80:83], v[132:135], v[24:27]\n"
"buffer_load_dwordx4 v46, s[32:35], s97 offen lds\n"
"v_mfma_f32_16x16x32_bf16 v[24:27], a[84:87], v[136:139], v[24:27]\n"
"ds_write_b128 v3, v[68:71] offset:1024\n"
"v_mfma_f32_16x16x32_bf16 v[24:27], a[88:91], v[140:143], v[24:27]\n"
"buffer_load_dwordx4 v[68:71], v34, s[4:7], s98 offen sc1\n"
"s_add_u32 m0, s77, 0x6400\n"
"s_add_u32 s97, s47, 0xc000\n"
"v_mfma_f32_16x16x32_bf16 v[24:27], a[92:95], v[144:147], v[24:27]\n"
"buffer_load_dwordx4 v46, s[32:35], s97 offen lds\n"
"v_mfma_f32_16x16x32_bf16 v[80:83], a[96:99], v[116:119], 0\n"
"s_add_u32 m0, s77, 0x8000\n"
"s_add_u32 s97, s48, 0\n"
"v_mfma_f32_16x16x32_bf16 v[80:83], a[100:103], v[120:123], v[80:83]\n"
"buffer_load_dword v251, v10, s[4:7], s96 offen offset:1024 sc1\n"
"v_mfma_f32_16x16x32_bf16 v[80:83], a[104:107], v[124:127], v[80:83]\n"
"buffer_load_dwordx4 v46, s[32:35], s97 offen lds\n"
"v_mfma_f32_16x16x32_bf16 v[80:83], a[108:111], v[128:131], v[80:83]\n"
"buffer_load_dwordx4 v[196:199], v34, s[4:7], s98 offen offset:1024 sc1\n"
"s_add_u32 m0, s77, 0x8400\n"
"s_add_u32 s97, s48, 0xc000\n"
"v_mfma_f32_16x16x32_bf16 v[80:83], a[112:115], v[132:135], v[80:83]\n"
"buffer_load_dword v250, v10, s[4:7], s96 offen offset:1280 sc1\n"
"v_mfma_f32_16x16x32_bf16 v[80:83], a[116:119], v[136:139], v[80:83]\n"
"ds_write_b128 v3, v[24:27] offset:2048\n"
"v_mfma_f32_16x16x32_bf16 v[80:83], a[120:123], v[140:143], v[80:83]\n"
"buffer_load_dwordx4 v46, s[32:35], s97 offen lds\n"
"v_mfma_f32_16x16x32_bf16 v[80:83], a[124:127], v[144:147], v[80:83]\n"
"buffer_load_dword v249, v10, s[4:7], s96 offen offset:1536 sc1\n"
"v_mfma_f32_16x16x32_bf16 v[24:27], v[28:31], v[116:119], 0\n"
"buffer_load_dwordx4 v[44:47], v35, s[4:7], s98 offen sc1\n"
"v_mfma_f32_16x16x32_bf16 v[24:27], v[52:55], v[120:123], v[24:27]\n"
"v_mfma_f32_16x16x32_bf16 v[24:27], v[84:87], v[124:127], v[24:27]\n"
"buffer_load_dword v248, v10, s[4:7], s96 offen offset:1792 sc1\n"
"v_mfma_f32_16x16x32_bf16 v[24:27], v[92:95], v[128:131], v[24:27]\n"
"v_mfma_f32_16x16x32_bf16 v[24:27], v[100:103], v[132:135], v[24:27]\n"
"v_mfma_f32_16x16x32_bf16 v[24:27], v[108:111], v[136:139], v[24:27]\n"
"buffer_load_dwordx4 v[200:203], v35, s[4:7], s98 offen offset:1024 sc1\n"
"v_mfma_f32_16x16x32_bf16 v[24:27], v[180:183], v[140:143], v[24:27]\n"
"ds_write_b128 v3, v[80:83] offset:3072\n"
"v_mfma_f32_16x16x32_bf16 v[24:27], v[188:191], v[144:147], v[24:27]\n"
"v_mfma_f32_16x16x32_bf16 v[80:83], v[48:51], v[116:119], 0\n"
"v_mfma_f32_16x16x32_bf16 v[80:83], v[56:59], v[120:123], v[80:83]\n"
"v_mfma_f32_16x16x32_bf16 v[80:83], v[88:91], v[124:127], v[80:83]\n"
"v_mfma_f32_16x16x32_bf16 v[80:83], v[96:99], v[128:131], v[80:83]\n"
"v_mfma_f32_16x16x32_bf16 v[80:83], v[104:107], v[132:135], v[80:83]\n"
"v_mfma_f32_16x16x32_bf16 v[80:83], v[112:115], v[136:139], v[80:83]\n"
"s_nop 1\n"
"ds_write_b128 v3, v[24:27] offset:4096\n"
"v_mfma_f32_16x16x32_bf16 v[80:83], v[184:187], v[140:143], v[80:83]\n"
"v_mfma_f32_16x16x32_bf16 v[80:83], v[192:195], v[144:147], v[80:83]\n"
"s_waitcnt vmcnt(31)\n"
"; @A G1 0.1\n"
"v_mfma_f32_16x16x32_bf16 v[24:27], a[0:3], v[148:151], 0\n"
"v_mfma_f32_16x16x32_bf16 v[24:27], a[4:7], v[152:155], v[24:27]\n"
"v_mfma_f32_16x16x32_bf16 v[24:27], a[8:11], v[156:159], v[24:27]\n"
"s_add_u32 s98, s55, 0x40000\n"
"buffer_load_dwordx4 v[116:119], v32, s[4:7], s98 offen sc1\n"
"v_mfma_f32_16x16x32_bf16 v[24:27], a[12:15], v[160:163], v[24:27]\n"
"s_waitcnt vmcnt(28)\n"
"v_mfma_f32_16x16x32_bf16 v[24:27], a[16:19], v[164:167], v[24:27]\n"
"v_mfma_f32_16x16x32_bf16 v[24:27], a[20:23], v[168:171], v[24:27]\n"
"ds_write_b128 v3, v[80:83] offset:5120\n"
"v_mfma_f32_16x16x32_bf16 v[24:27], a[24:27], v[172:175], v[24:27]\n"
"v_mfma_f32_16x16x32_bf16 v[24:27], a[28:31], v[176:179], v[24:27]\n"
"buffer_load_dwordx4 v[80:83], v32, s[4:7], s98 offen offset:1024 sc1\n"
"v_mfma_f32_16x16x32_bf16 v[120:123], a[32:35], v[148:151], 0\n"
"v_mfma_f32_16x16x32_bf16 v[120:123], a[36:39], v[152:155], v[120:123]\n"
"v_mfma_f32_16x16x32_bf16 v[120:123], a[40:43], v[156:159], v[120:123]\n"
"v_mfma_f32_16x16x32_bf16 v[120:123], a[44:47], v[160:163], v[120:123]\n"
"v_mfma_f32_16x16x32_bf16 v[120:123], a[48:51], v[164:167], v[120:123]\n"
"buffer_load_dwordx4 v[124:127], v33, s[4:7], s98 offen sc1\n"
"v_mfma_f32_16x16x32_bf16 v[120:123], a[52:55], v[168:171], v[120:123]\n"
"ds_write_b128 v3, v[24:27] offset:6144\n"
"v_mfma_f32_16x16x32_bf16 v[120:123], a[56:59], v[172:175], v[120:123]\n"
"v_mfma_f32_16x16x32_bf16 v[120:123], a[60:63], v[176:179], v[120:123]\n"
"v_mfma_f32_16x16x32_bf16 v[24:27], a[64:67], v[148:151], 0\n"
"v_mfma_f32_16x16x32_bf16 v[24:27], a[68:71], v[152:155], v[24:27]\n"
"buffer_load_dwordx4 v[128:131], v33, s[4:7], s98 offen offset:1024 sc1\n"
"v_mfma_f32_16x16x32_bf16 v[24:27], a[72:75], v[156:159], v[24:27]\n"
"v_mfma_f32_16x16x32_bf16 v[24:27], a[76:79], v[160:163], v[24:27]\n"
"v_mfma_f32_16x16x32_bf16 v[24:27], a[80:83], v[164:167], v[24:27]\n"
"v_mfma_f32_16x16x32_bf16 v[24:27], a[84:87], v[168:171], v[24:27]\n"
"s_nop 0\n"
"ds_write_b128 v3, v[120:123] offset:7168\n"
"v_mfma_f32_16x16x32_bf16 v[24:27], a[88:91], v[172:175], v[24:27]\n"
"buffer_load_dwordx4 v[120:123], v34, s[4:7], s98 offen sc1\n"
"v_mfma_f32_16x16x32_bf16 v[24:27], a[92:95], v[176:179], v[24:27]\n"
"v_mfma_f32_16x16x32_bf16 v[132:135], a[96:99], v[148:151], 0\n"
"v_mfma_f32_16x16x32_bf16 v[132:135], a[100:103], v[152:155], v[132:135]\n"
"v_mfma_f32_16x16x32_bf16 v[132:135], a[104:107], v[156:159], v[132:135]\n"
"v_mfma_f32_16x16x32_bf16 v[132:135], a[108:111], v[160:163], v[132:135]\n"
"buffer_load_dwordx4 v[136:139], v34, s[4:7], s98 offen offset:1024 sc1\n"
"v_mfma_f32_16x16x32_bf16 v[132:135], a[112:115], v[164:167], v[132:135]\n"
"v_mfma_f32_16x16x32_bf16 v[132:135], a[116:119], v[168:171], v[132:135]\n"
"s_nop 0\n"
"ds_write_b128 v3, v[24:27] offset:8192\n"
"v_mfma_f32_16x16x32_bf16 v[132:135], a[120:123], v[172:175], v[132:135]\n"
"v_mfma_f32_16x16x32_bf16 v[132:135], a[124:127], v[176:179], v[132:135]\n"
"v_mfma_f32_16x16x32_bf16 v[24:27], v[28:31], v[148:151], 0\n"
"buffer_load_dwordx4 v[140:143], v35, s[4:7], s98 offen sc1\n"
"v_mfma_f32_16x16x32_bf16 v[24:27], v[52:55], v[152:155], v[24:27]\n"
"v_mfma_f32_16x16x32_bf16 v[24:27], v[84:87], v[156:159], v[24:27]\n"
"v_mfma_f32_16x16x32_bf16 v[24:27], v[92:95], v[160:163], v[24:27]\n"
"v_mfma_f32_16x16x32_bf16 v[24:27], v[100:103], v[164:167], v[24:27]\n"
"s_waitcnt vmcnt(8)\n"
"v_add_f32_e32 v255, v255, v251\n"
"v_add_f32_e32 v254, v254, v250\n"
"v_mfma_f32_16x16x32_bf16 v[24:27], v[108:111], v[168:171], v[24:27]\n"
"buffer_load_dwordx4 v[144:147], v35, s[4:7], s98 offen offset:1024 sc1\n"
"v_mfma_f32_16x16x32_bf16 v[24:27], v[180:183], v[172:175], v[24:27]\n"
"ds_write_b128 v3, v[132:135] offset:9216\n"
"v_mfma_f32_16x16x32_bf16 v[24:27], v[188:191], v[176:179], v[24:27]\n"
"v_add_f32_e32 v253, v253, v249\n"
"v_add_f32_e32 v252, v252, v248\n"
"v_mfma_f32_16x16x32_bf16 v[132:135], v[48:51], v[148:151], 0\n"
"v_add_f32_e32 v255, v255, v253\n"
"v_add_f32_e32 v254, v254, v252\n"
"v_mfma_f32_16x16x32_bf16 v[132:135], v[56:59], v[152:155], v[132:135]\n"
"v_add_f32_e32 v255, v255, v254\n"
"v_mov_b32_e32 v254, v255\n"
"v_mfma_f32_16x16x32_bf16 v[132:135], v[88:91], v[156:159], v[132:135]\n"
"s_nop 0\n"
"v_permlane32_swap_b32_e32 v255, v254\n"
"v_add_f32_e32 v255, v255, v254\n"
"v_mfma_f32_16x16x32_bf16 v[132:135], v[96:99], v[160:163], v[132:135]\n"
"v_mul_f32_e32 v255, 0x3a800000, v255\n"
"v_mov_b32_e32 v254, v255\n"
"v_mfma_f32_16x16x32_bf16 v[132:135], v[104:107], v[164:167], v[132:135]\n"
"s_nop 0\n"
"v_permlane16_swap_b32_e32 v255, v254\n"
"v_cvt_pk_bf16_f32 v253, -v255, 0\n"
"v_mfma_f32_16x16x32_bf16 v[132:135], v[112:115], v[168:171], v[132:135]\n"
"ds_write_b128 v3, v[24:27] offset:10240\n"
"v_mfma_f32_16x16x32_bf16 v[132:135], v[184:187], v[172:175], v[132:135]\n"
"v_fma_f32 v254, -v255, v255, v254\n"
"v_lshlrev_b32_e32 v253, 16, v253\n"
"v_mfma_f32_16x16x32_bf16 v[132:135], v[192:195], v[176:179], v[132:135]\n"
"v_max_f32_e32 v254, 0, v254\n"
"v_cvt_pk_bf16_f32 v254, v254, 0\n"
"; @A G1 0.2\n"
"v_mfma_f32_16x16x32_bf16 v[24:27], a[0:3], v[60:63], 0\n"
"v_lshlrev_b32_e32 v254, 16, v254\n"
"v_add_f32_e32 v254, 0x3727c5ac, v254\n"
"v_mfma_f32_16x16x32_bf16 v[24:27], a[4:7], v[64:67], v[24:27]\n"
"v_cvt_pk_bf16_f32 v254, v254, 0\n"
"v_lshlrev_b32_e32 v254, 16, v254\n"
"v_mfma_f32_16x16x32_bf16 v[24:27], a[8:11], v[72:75], v[24:27]\n"
"v_sqrt_f32_e32 v254, v254\n"
"v_mfma_f32_16x16x32_bf16 v[24:27], a[12:15], v[76:79], v[24:27]\n"
"s_waitcnt vmcnt(8)\n"
"v_cvt_pk_bf16_f32 v254, v254, 0\n"
"v_lshlrev_b32_e32 v254, 16, v254\n"
"v_mfma_f32_16x16x32_bf16 v[24:27], a[16:19], v[68:71], v[24:27]\n"
"v_rcp_f32_e32 v255, v254\n"
"v_mfma_f32_16x16x32_bf16 v[24:27], a[20:23], v[196:199], v[24:27]\n"
"ds_write_b128 v3, v[132:135] offset:11264\n"
"v_mfma_f32_16x16x32_bf16 v[24:27], a[24:27], v[44:47], v[24:27]\n"
"v_mul_f32_e32 v254, 0x3e8293ee, v255\n"
"v_mfma_f32_16x16x32_bf16 v[24:27], a[28:31], v[200:203], v[24:27]\n"
"v_mfma_f32_16x16x32_bf16 v[132:135], a[32:35], v[60:63], 0\n"
"v_mfma_f32_16x16x32_bf16 v[132:135], a[36:39], v[64:67], v[132:135]\n"
"v_mfma_f32_16x16x32_bf16 v[132:135], a[40:43], v[72:75], v[132:135]\n"
"s_add_u32 s98, s52, 0x48000\n"
"buffer_load_dwordx4 v[148:151], v32, s[4:7], s98 offen sc1\n"
"v_mfma_f32_16x16x32_bf16 v[132:135], a[44:47], v[76:79], v[132:135]\n"
"s_waitcnt lgkmcnt(0)\n"
"s_barrier\n"
"v_mfma_f32_16x16x32_bf16 v[132:135], a[48:51], v[68:71], v[132:135]\n"
"ds_read_b128 v[152:155], v4\n"
"ds_read_b128 v[156:159], v9 offset:128\n"
"v_mfma_f32_16x16x32_bf16 v[132:135], a[52:55], v[196:199], v[132:135]\n"
"ds_write_b128 v3, v[24:27] offset:12288\n"
"v_mfma_f32_16x16x32_bf16 v[132:135], a[56:59], v[44:47], v[132:135]\n"
"ds_read_b128 v[24:27], v5\n"
"ds_read_b64_tr_b16 v[160:161], v40\n"
"ds_read_b64_tr_b16 v[162:163], v40 offset:1024\n"
"v_mfma_f32_16x16x32_bf16 v[132:135], a[60:63], v[200:203], v[132:135]\n"
"buffer_load_dwordx4 v[164:167], v32, s[4:7], s98 offen offset:1024 sc1\n"
"v_mfma_f32_16x16x32_bf16 v[168:171], a[64:67], v[60:63], 0\n"
"v_mfma_f32_16x16x32_bf16 v[168:171], a[68:71], v[64:67], v[168:171]\n"
"v_mfma_f32_16x16x32_bf16 v[168:171], a[72:75], v[72:75], v[168:171]\n"
"v_mfma_f32_16x16x32_bf16 v[168:171], a[76:79], v[76:79], v[168:171]\n"
"ds_read_b128 v[172:175], v4 offset:1024\n"
"ds_read_b128 v[176:179], v9 offset:192\n"
"s_waitcnt lgkmcnt(2)\n"
"buffer_store_dwordx4 v[160:163], v0, s[4:7], s24 offen sc1\n"
"v_mfma_f32_16x16x32_bf16 v[168:171], a[80:83], v[68:71], v[168:171]\n"
"buffer_load_dwordx4 v[160:163], v33, s[4:7], s98 offen sc1\n"
"ds_read_b64_tr_b16 v[204:205], v40 offset:4096\n"
"v_mfma_f32_16x16x32_bf16 v[168:171], a[84:87], v[196:199], v[168:171]\n"
"ds_write_b128 v3, v[132:135] offset:13312\n"
"v_mfma_f32_16x16x32_bf16 v[168:171], a[88:91], v[44:47], v[168:171]\n"
"ds_read_b128 v[132:135], v5 offset:1024\n"
"ds_read_b64_tr_b16 v[206:207], v40 offset:5120\n"
"v_mfma_f32_16x16x32_bf16 v[168:171], a[92:95], v[200:203], v[168:171]\n"
"v_fmac_f32_e32 v152, v253, v156\n"
"v_fmac_f32_e32 v153, v253, v157\n"
"v_mfma_f32_16x16x32_bf16 v[208:211], a[96:99], v[60:63], 0\n"
"v_fmac_f32_e32 v154, v253, v158\n"
"v_fmac_f32_e32 v155, v253, v159\n"
"v_mfma_f32_16x16x32_bf16 v[208:211], a[100:103], v[64:67], v[208:211]\n"
"buffer_load_dwordx4 v[156:159], v33, s[4:7], s98 offen offset:1024 sc1\n"
"v_mfma_f32_16x16x32_bf16 v[208:211], a[104:107], v[72:75], v[208:211]\n"
"ds_read_b128 v[212:215], v4 offset:2048\n"
"ds_read_b128 v[216:219], v9 offset:256\n"
"v_mfma_f32_16x16x32_bf16 v[208:211], a[108:111], v[76:79], v[208:211]\n"
"ds_read_b128 v[220:223], v5 offset:2048\n"
"s_waitcnt lgkmcnt(3)\n"
"buffer_store_dwordx4 v[204:207], v0, s[4:7], s24 offen offset:1024 sc1\n"
"v_add_f32_e32 v152, v152, v24\n"
"v_mfma_f32_16x16x32_bf16 v[208:211], a[112:115], v[68:71], v[208:211]\n"
"v_add_f32_e32 v153, v153, v25\n"
"v_add_f32_e32 v154, v154, v26\n"
"v_mfma_f32_16x16x32_bf16 v[208:211], a[116:119], v[196:199], v[208:211]\n"
"ds_write_b128 v3, v[168:171] offset:14336\n"
"v_mfma_f32_16x16x32_bf16 v[208:211], a[120:123], v[44:47], v[208:211]\n"
"buffer_load_dwordx4 v[168:171], v34, s[4:7], s98 offen sc1\n"
"v_mfma_f32_16x16x32_bf16 v[208:211], a[124:127], v[200:203], v[208:211]\n"
"v_add_f32_e32 v155, v155, v27\n"
"v_fmac_f32_e32 v172, v253, v176\n"
"v_mfma_f32_16x16x32_bf16 v[24:27], v[28:31], v[60:63], 0\n"
"ds_read_b64_tr_b16 v[204:205], v40 offset:8192\n"
"v_fmac_f32_e32 v173, v253, v177\n"
"v_fmac_f32_e32 v174, v253, v178\n"
"v_mfma_f32_16x16x32_bf16 v[24:27], v[52:55], v[64:67], v[24:27]\n"
"ds_read_b128 v[224:227], v4 offset:3072\n"
"ds_read_b128 v[228:231], v9 offset:320\n"
"ds_read_b64_tr_b16 v[206:207], v40 offset:9216\n"
"v_mfma_f32_16x16x32_bf16 v[24:27], v[84:87], v[72:75], v[24:27]\n"
"ds_read_b128 v[232:235], v5 offset:3072\n"
"v_fmac_f32_e32 v175, v253, v179\n"
"v_mfma_f32_16x16x32_bf16 v[24:27], v[92:95], v[76:79], v[24:27]\n"
"buffer_load_dwordx4 v[176:179], v34, s[4:7], s98 offen offset:1024 sc1\n"
"v_mfma_f32_16x16x32_bf16 v[24:27], v[100:103], v[68:71], v[24:27]\n"
"v_add_f32_e32 v172, v172, v132\n"
"v_add_f32_e32 v173, v173, v133\n"
"v_mfma_f32_16x16x32_bf16 v[24:27], v[108:111], v[196:199], v[24:27]\n"
"ds_write_b128 v3, v[208:211] offset:15360\n"
"v_mfma_f32_16x16x32_bf16 v[24:27], v[180:183], v[44:47], v[24:27]\n"
"s_waitcnt lgkmcnt(7)\n"
"s_waitcnt lgkmcnt(2)\n"
"buffer_store_dwordx4 v[204:207], v0, s[4:7], s25 offen sc1\n"
"v_add_f32_e32 v174, v174, v134\n"
"v_add_f32_e32 v175, v175, v135\n"
"v_mfma_f32_16x16x32_bf16 v[24:27], v[188:191], v[200:203], v[24:27]\n"
"v_fmac_f32_e32 v212, v253, v216\n"
"v_fmac_f32_e32 v213, v253, v217\n"
"v_mfma_f32_16x16x32_bf16 v[132:135], v[48:51], v[60:63], 0\n"
"buffer_load_dwordx4 v[60:63], v35, s[4:7], s98 offen sc1\n"
"v_mfma_f32_16x16x32_bf16 v[132:135], v[56:59], v[64:67], v[132:135]\n"
"ds_read_b128 v[64:67], v4 offset:4096\n"
"ds_read_b128 v[204:207], v9\n"
"v_mfma_f32_16x16x32_bf16 v[132:135], v[88:91], v[72:75], v[132:135]\n"
"ds_read_b128 v[72:75], v5 offset:4096\n"
"v_fmac_f32_e32 v214, v253, v218\n"
"v_mfma_f32_16x16x32_bf16 v[132:135], v[96:99], v[76:79], v[132:135]\n"
"v_fmac_f32_e32 v215, v253, v219\n"
"v_add_f32_e32 v212, v212, v220\n"
"v_mfma_f32_16x16x32_bf16 v[132:135], v[104:107], v[68:71], v[132:135]\n"
"v_add_f32_e32 v213, v213, v221\n"
"v_add_f32_e32 v214, v214, v222\n"
"v_mfma_f32_16x16x32_bf16 v[132:135], v[112:115], v[196:199], v[132:135]\n"
"buffer_load_dwordx4 v[68:71], v35, s[4:7], s98 offen offset:1024 sc1\n"
"v_mfma_f32_16x16x32_bf16 v[132:135], v[184:187], v[44:47], v[132:135]\n"
"ds_write_b128 v3, v[24:27] offset:16384\n"
"v_mfma_f32_16x16x32_bf16 v[132:135], v[192:195], v[200:203], v[132:135]\n"
"s_waitcnt vmcnt(15)\n"
"ds_read_b128 v[24:27], v4 offset:5120\n"
"ds_read_b128 v[44:47], v9 offset:64\n"
"ds_read_b64_tr_b16 v[76:77], v40 offset:12288\n"
"; @A G1 0.3\n"
"v_mfma_f32_16x16x32_bf16 v[152:155], a[0:3], v[116:119], v[152:155]\n"
"ds_read_b128 v[196:199], v5 offset:5120\n"
"ds_read_b64_tr_b16 v[78:79], v40 offset:13312\n"
"v_add_f32_e32 v215, v215, v223\n"
"v_mfma_f32_16x16x32_bf16 v[152:155], a[4:7], v[80:83], v[152:155]\n"
"s_waitcnt lgkmcnt(10)\n"
"v_fmac_f32_e32 v224, v253, v228\n"
"v_fmac_f32_e32 v225, v253, v229\n"
"v_mfma_f32_16x16x32_bf16 v[152:155], a[8:11], v[124:127], v[152:155]\n"
"s_add_u32 s98, s53, 0x48000\n"
"buffer_load_dwordx4 v[200:203], v32, s[4:7], s98 offen sc1\n"
"v_mfma_f32_16x16x32_bf16 v[152:155], a[12:15], v[128:131], v[152:155]\n"
"s_waitcnt vmcnt(12)\n"
"v_fmac_f32_e32 v226, v253, v230\n"
"v_fmac_f32_e32 v227, v253, v231\n"
"v_mfma_f32_16x16x32_bf16 v[152:155], a[16:19], v[120:123], v[152:155]\n"
"v_add_f32_e32 v224, v224, v232\n"
"v_add_f32_e32 v225, v225, v233\n"
"v_mfma_f32_16x16x32_bf16 v[152:155], a[20:23], v[136:139], v[152:155]\n"
"ds_write_b128 v3, v[132:135] offset:17408\n"
"v_mfma_f32_16x16x32_bf16 v[152:155], a[24:27], v[140:143], v[152:155]\n"
"s_waitcnt lgkmcnt(7)\n"
"s_waitcnt lgkmcnt(1)\n"
"buffer_store_dwordx4 v[76:79], v0, s[4:7], s25 offen offset:1024 sc1\n"
"v_add_f32_e32 v226, v226, v234\n"
"v_add_f32_e32 v227, v227, v235\n"
"v_mfma_f32_16x16x32_bf16 v[152:155], a[28:31], v[144:147], v[152:155]\n"
"buffer_load_dwordx4 v[76:79], v32, s[4:7], s98 offen offset:1024 sc1\n"
"v_mfma_f32_16x16x32_bf16 v[172:175], a[32:35], v[116:119], v[172:175]\n"
"v_fmac_f32_e32 v64, v253, v204\n"
"v_fmac_f32_e32 v65, v253, v205\n"
"v_mfma_f32_16x16x32_bf16 v[172:175], a[36:39], v[80:83], v[172:175]\n"
"v_fmac_f32_e32 v66, v253, v206\n"
"v_fmac_f32_e32 v67, v253, v207\n"
"v_mfma_f32_16x16x32_bf16 v[172:175], a[40:43], v[124:127], v[172:175]\n"
"s_waitcnt lgkmcnt(0)\n"
"s_barrier\n"
"v_mfma_f32_16x16x32_bf16 v[172:175], a[44:47], v[128:131], v[172:175]\n"
"ds_read_b128 v[132:135], v6\n"
"ds_read_b128 v[204:207], v6 offset:1024\n"
"v_mfma_f32_16x16x32_bf16 v[172:175], a[48:51], v[120:123], v[172:175]\n"
"buffer_load_dwordx4 v[208:211], v33, s[4:7], s98 offen sc1\n"
"v_mfma_f32_16x16x32_bf16 v[172:175], a[52:55], v[136:139], v[172:175]\n"
"v_add_f32_e32 v64, v64, v72\n"
"v_add_f32_e32 v65, v65, v73\n"
"v_mfma_f32_16x16x32_bf16 v[172:175], a[56:59], v[140:143], v[172:175]\n"
"v_add_f32_e32 v66, v66, v74\n"
"v_add_f32_e32 v67, v67, v75\n"
"v_mfma_f32_16x16x32_bf16 v[172:175], a[60:63], v[144:147], v[172:175]\n"
"v_fmac_f32_e32 v24, v253, v44\n"
"v_fmac_f32_e32 v25, v253, v45\n"
"v_mfma_f32_16x16x32_bf16 v[212:215], a[64:67], v[116:119], v[212:215]\n"
"v_fmac_f32_e32 v26, v253, v46\n"
"v_fmac_f32_e32 v27, v253, v47\n"
"v_mfma_f32_16x16x32_bf16 v[212:215], a[68:71], v[80:83], v[212:215]\n"
"buffer_load_dwordx4 v[44:47], v33, s[4:7], s98 offen offset:1024 sc1\n"
"v_mfma_f32_16x16x32_bf16 v[212:215], a[72:75], v[124:127], v[212:215]\n"
"v_add_f32_e32 v24, v24, v196\n"
"v_add_f32_e32 v25, v25, v197\n"
"v_mfma_f32_16x16x32_bf16 v[212:215], a[76:79], v[128:131], v[212:215]\n"
"v_add_f32_e32 v26, v26, v198\n"
"v_add_f32_e32 v27, v27, v199\n"
"v_mfma_f32_16x16x32_bf16 v[212:215], a[80:83], v[120:123], v[212:215]\n"
"s_waitcnt lgkmcnt(0)\n"
"v_add_f32_e32 v152, v152, v132\n"
"v_add_f32_e32 v153, v153, v133\n"
"v_mfma_f32_16x16x32_bf16 v[212:215], a[84:87], v[136:139], v[212:215]\n"
"v_add_f32_e32 v154, v154, v134\n"
"v_add_f32_e32 v155, v155, v135\n"
"v_mfma_f32_16x16x32_bf16 v[212:215], a[88:91], v[140:143], v[212:215]\n"
"buffer_load_dwordx4 v[72:75], v34, s[4:7], s98 offen sc1\n"
"v_mfma_f32_16x16x32_bf16 v[212:215], a[92:95], v[144:147], v[212:215]\n"
"ds_read_b128 v[132:135], v6 offset:2048\n"
"ds_read_b128 v[196:199], v6 offset:3072\n"
"v_mfma_f32_16x16x32_bf16 v[224:227], a[96:99], v[116:119], v[224:227]\n"
"v_add_f32_e32 v172, v172, v204\n"
"v_add_f32_e32 v173, v173, v205\n"
"v_mfma_f32_16x16x32_bf16 v[224:227], a[100:103], v[80:83], v[224:227]\n"
"v_add_f32_e32 v174, v174, v206\n"
"v_add_f32_e32 v175, v175, v207\n"
"v_mfma_f32_16x16x32_bf16 v[224:227], a[104:107], v[124:127], v[224:227]\n"
"v_mul_f32_e32 v152, v152, v255\n"
"v_mul_f32_e32 v153, v153, v255\n"
"v_mfma_f32_16x16x32_bf16 v[224:227], a[108:111], v[128:131], v[224:227]\n"
"buffer_load_dwordx4 v[204:207], v34, s[4:7], s98 offen offset:1024 sc1\n"
"v_mfma_f32_16x16x32_bf16 v[224:227], a[112:115], v[120:123], v[224:227]\n"
"v_mul_f32_e32 v154, v154, v255\n"
"v_mul_f32_e32 v155, v155, v255\n"
"v_mfma_f32_16x16x32_bf16 v[224:227], a[116:119], v[136:139], v[224:227]\n"
"v_mul_f32_e32 v172, v172, v255\n"
"v_mul_f32_e32 v173, v173, v255\n"
"v_mfma_f32_16x16x32_bf16 v[224:227], a[120:123], v[140:143], v[224:227]\n"
"v_mul_f32_e32 v174, v174, v255\n"
"v_mul_f32_e32 v175, v175, v255\n"
"v_mfma_f32_16x16x32_bf16 v[224:227], a[124:127], v[144:147], v[224:227]\n"
"v_cvt_pk_bf16_f32 v216, v152, v153\n"
"v_cvt_pk_bf16_f32 v217, v154, v155\n"
"v_mfma_f32_16x16x32_bf16 v[64:67], v[28:31], v[116:119], v[64:67]\n"
"buffer_load_dwordx4 v[152:155], v35, s[4:7], s98 offen sc1\n"
"v_mfma_f32_16x16x32_bf16 v[64:67], v[52:55], v[80:83], v[64:67]\n"
"v_cvt_pk_bf16_f32 v218, v172, v173\n"
"v_cvt_pk_bf16_f32 v219, v174, v175\n"
"v_mfma_f32_16x16x32_bf16 v[64:67], v[84:87], v[124:127], v[64:67]\n"
"ds_write_b128 v7, v[216:219] offset:24576\n"
"v_mfma_f32_16x16x32_bf16 v[64:67], v[92:95], v[128:131], v[64:67]\n"
"v_mfma_f32_16x16x32_bf16 v[64:67], v[100:103], v[120:123], v[64:67]\n"
"s_waitcnt lgkmcnt(1)\n"
"v_add_f32_e32 v212, v212, v132\n"
"v_add_f32_e32 v213, v213, v133\n"
"v_mfma_f32_16x16x32_bf16 v[64:67], v[108:111], v[136:139], v[64:67]\n"
"buffer_load_dwordx4 v[172:175], v35, s[4:7], s98 offen offset:1024 sc1\n"
"v_mfma_f32_16x16x32_bf16 v[64:67], v[180:183], v[140:143], v[64:67]\n"
"ds_read_b128 v[220:223], v6 offset:4096\n"
"ds_read_b128 v[228:231], v6 offset:5120\n"
"v_mfma_f32_16x16x32_bf16 v[64:67], v[188:191], v[144:147], v[64:67]\n"
"v_add_f32_e32 v214, v214, v134\n"
"v_add_f32_e32 v215, v215, v135\n"
"v_mfma_f32_16x16x32_bf16 v[24:27], v[48:51], v[116:119], v[24:27]\n"
"v_add_f32_e32 v224, v224, v196\n"
"v_add_f32_e32 v225, v225, v197\n"
"v_mfma_f32_16x16x32_bf16 v[24:27], v[56:59], v[80:83], v[24:27]\n"
"v_add_f32_e32 v226, v226, v198\n"
"v_add_f32_e32 v227, v227, v199\n"
"v_mfma_f32_16x16x32_bf16 v[24:27], v[88:91], v[124:127], v[24:27]\n"
"v_mul_f32_e32 v212, v212, v255\n"
"v_mul_f32_e32 v213, v213, v255\n"
"v_mfma_f32_16x16x32_bf16 v[24:27], v[96:99], v[128:131], v[24:27]\n"
"v_mul_f32_e32 v214, v214, v255\n"
"v_mul_f32_e32 v215, v215, v255\n"
"v_mfma_f32_16x16x32_bf16 v[24:27], v[104:107], v[120:123], v[24:27]\n"
"v_mul_f32_e32 v224, v224, v255\n"
"v_mul_f32_e32 v225, v225, v255\n"
"v_mfma_f32_16x16x32_bf16 v[24:27], v[112:115], v[136:139], v[24:27]\n"
"v_mul_f32_e32 v226, v226, v255\n"
"v_mul_f32_e32 v227, v227, v255\n"
"v_mfma_f32_16x16x32_bf16 v[24:27], v[184:187], v[140:143], v[24:27]\n"
"v_cvt_pk_bf16_f32 v80, v212, v213\n"
"v_cvt_pk_bf16_f32 v81, v214, v215\n"
"v_mfma_f32_16x16x32_bf16 v[24:27], v[192:195], v[144:147], v[24:27]\n"
"v_cvt_pk_bf16_f32 v82, v224, v225\n"
"v_cvt_pk_bf16_f32 v83, v226, v227\n"
"ds_write_b128 v7, v[80:83] offset:57344\n"
".Lmk_z1:\n"
"s_waitcnt vmcnt(15)\n"
"; @A G1 1.0\n"
"v_mfma_f32_16x16x32_bf16 v[116:119], a[0:3], v[148:151], 0\n"
"v_mfma_f32_16x16x32_bf16 v[116:119], a[4:7], v[164:167], v[116:119]\n"
"s_add_u32 s98, s62, 0x4800\n"
"v_mfma_f32_16x16x32_bf16 v[116:119], a[8:11], v[160:163], v[116:119]\n"
"s_add_u32 s97, s54, 0x48000\n"
"buffer_load_dwordx4 v[120:123], v32, s[4:7], s97 offen sc1\n"
"v_mfma_f32_16x16x32_bf16 v[116:119], a[12:15], v[156:159], v[116:119]\n"
"s_waitcnt vmcnt(10)\n"
"v_mfma_f32_16x16x32_bf16 v[116:119], a[16:19], v[168:171], v[116:119]\n"
"buffer_load_dword v255, v10, s[4:7], s98 offen sc1\n"
"v_mfma_f32_16x16x32_bf16 v[116:119], a[20:23], v[176:179], v[116:119]\n"
"s_waitcnt lgkmcnt(1)\n"
"v_add_f32_e32 v64, v64, v220\n"
"v_add_f32_e32 v65, v65, v221\n"
"v_mfma_f32_16x16x32_bf16 v[116:119], a[24:27], v[60:63], v[116:119]\n"
"v_add_f32_e32 v66, v66, v222\n"
"v_add_f32_e32 v67, v67, v223\n"
"v_mfma_f32_16x16x32_bf16 v[116:119], a[28:31], v[68:71], v[116:119]\n"
"buffer_load_dwordx4 v[124:127], v32, s[4:7], s97 offen offset:1024 sc1\n"
"v_mfma_f32_16x16x32_bf16 v[128:131], a[32:35], v[148:151], 0\n"
"buffer_load_dword v253, v10, s[4:7], s98 offen offset:256 sc1\n"
"v_mfma_f32_16x16x32_bf16 v[128:131], a[36:39], v[164:167], v[128:131]\n"
"v_add_f32_e32 v24, v24, v228\n"
"v_add_f32_e32 v25, v25, v229\n"
"v_mfma_f32_16x16x32_bf16 v[128:131], a[40:43], v[160:163], v[128:131]\n"
"s_waitcnt vmcnt(11)\n"
"buffer_store_dwordx2 v[18:19], v11, s[4:7], s68 offen offset:96 sc1\n"
"v_mfma_f32_16x16x32_bf16 v[128:131], a[44:47], v[156:159], v[128:131]\n"
"buffer_load_dword v252, v10, s[4:7], s98 offen offset:512 sc1\n"
"v_mfma_f32_16x16x32_bf16 v[128:131], a[48:51], v[168:171], v[128:131]\n"
"buffer_load_dwordx4 v[132:135], v33, s[4:7], s97 offen sc1\n"
"v_mfma_f32_16x16x32_bf16 v[128:131], a[52:55], v[176:179], v[128:131]\n"
"ds_write_b128 v3, v[116:119]\n"
"v_mfma_f32_16x16x32_bf16 v[128:131], a[56:59], v[60:63], v[128:131]\n"
"buffer_load_dword v251, v10, s[4:7], s98 offen offset:768 sc1\n"
"v_mfma_f32_16x16x32_bf16 v[128:131], a[60:63], v[68:71], v[128:131]\n"
"v_add_f32_e32 v26, v26, v230\n"
"v_add_f32_e32 v27, v27, v231\n"
"v_mfma_f32_16x16x32_bf16 v[116:119], a[64:67], v[148:151], 0\n"
"v_mul_f32_e32 v64, v64, v254\n"
"v_mul_f32_e32 v65, v65, v254\n"
"v_mfma_f32_16x16x32_bf16 v[116:119], a[68:71], v[164:167], v[116:119]\n"
"buffer_load_dwordx4 v[136:139], v33, s[4:7], s97 offen offset:1024 sc1\n"
"v_mfma_f32_16x16x32_bf16 v[116:119], a[72:75], v[160:163], v[116:119]\n"
"buffer_load_dword v250, v10, s[4:7], s98 offen offset:1024 sc1\n"
"v_mfma_f32_16x16x32_bf16 v[116:119], a[76:79], v[156:159], v[116:119]\n"
"v_mul_f32_e32 v66, v66, v254\n"
"v_mul_f32_e32 v67, v67, v254\n"
"v_mfma_f32_16x16x32_bf16 v[116:119], a[80:83], v[168:171], v[116:119]\n"
"v_mul_f32_e32 v24, v24, v254\n"
"v_mul_f32_e32 v25, v25, v254\n"
"v_mfma_f32_16x16x32_bf16 v[116:119], a[84:87], v[176:179], v[116:119]\n"
"ds_write_b128 v3, v[128:131] offset:1024\n"
"v_mfma_f32_16x16x32_bf16 v[116:119], a[88:91], v[60:63], v[116:119]\n"
"buffer_load_dwordx4 v[128:131], v34, s[4:7], s97 offen sc1\n"
"v_mfma_f32_16x16x32_bf16 v[116:119], a[92:95], v[68:71], v[116:119]\n"
"buffer_load_dword v249, v10, s[4:7], s98 offen offset:1280 sc1\n"
"v_mfma_f32_16x16x32_bf16 v[140:143], a[96:99], v[148:151], 0\n"
"v_mul_f32_e32 v26, v26, v254\n"
"v_mul_f32_e32 v27, v27, v254\n"
"v_mfma_f32_16x16x32_bf16 v[140:143], a[100:103], v[164:167], v[140:143]\n"
"v_cvt_pk_bf16_f32 v144, v64, v65\n"
"v_cvt_pk_bf16_f32 v145, v66, v67\n"
"v_mfma_f32_16x16x32_bf16 v[140:143], a[104:107], v[160:163], v[140:143]\n"
"buffer_load_dword v254, v10, s[4:7], s98 offen offset:1536 sc1\n"
"v_mfma_f32_16x16x32_bf16 v[140:143], a[108:111], v[156:159], v[140:143]\n"
"buffer_load_dwordx4 v[64:67], v34, s[4:7], s97 offen offset:1024 sc1\n"
"v_mfma_f32_16x16x32_bf16 v[140:143], a[112:115], v[168:171], v[140:143]\n"
"v_cvt_pk_bf16_f32 v146, v24, v25\n"
"v_cvt_pk_bf16_f32 v147, v26, v27\n"
"v_mfma_f32_16x16x32_bf16 v[140:143], a[116:119], v[176:179], v[140:143]\n"
"ds_write_b128 v3, v[116:119] offset:2048\n"
"v_mfma_f32_16x16x32_bf16 v[140:143], a[120:123], v[60:63], v[140:143]\n"
"buffer_load_dword v248, v10, s[4:7], s98 offen offset:1792 sc1\n"
"v_mfma_f32_16x16x32_bf16 v[140:143], a[124:127], v[68:71], v[140:143]\n"
"ds_write_b128 v2, v[144:147] offset:49152\n"
"v_mfma_f32_16x16x32_bf16 v[24:27], v[28:31], v[148:151], 0\n"
"buffer_load_dwordx4 v[116:119], v35, s[4:7], s97 offen sc1\n"
"v_mfma_f32_16x16x32_bf16 v[24:27], v[52:55], v[164:167], v[24:27]\n"
"v_mfma_f32_16x16x32_bf16 v[24:27], v[84:87], v[160:163], v[24:27]\n"
"v_mfma_f32_16x16x32_bf16 v[24:27], v[92:95], v[156:159], v[24:27]\n"
"v_mfma_f32_16x16x32_bf16 v[24:27], v[100:103], v[168:171], v[24:27]\n"
"v_mfma_f32_16x16x32_bf16 v[24:27], v[108:111], v[176:179], v[24:27]\n"
"buffer_load_dwordx4 v[144:147], v35, s[4:7], s97 offen offset:1024 sc1\n"
"v_mfma_f32_16x16x32_bf16 v[24:27], v[180:183], v[60:63], v[24:27]\n"
"ds_write_b128 v3, v[140:143] offset:3072\n"
"v_mfma_f32_16x16x32_bf16 v[24:27], v[188:191], v[68:71], v[24:27]\n"
"v_mfma_f32_16x16x32_bf16 v[140:143], v[48:51], v[148:151], 0\n"
"v_mfma_f32_16x16x32_bf16 v[140:143], v[56:59], v[164:167], v[140:143]\n"
"v_mfma_f32_16x16x32_bf16 v[140:143], v[88:91], v[160:163], v[140:143]\n"
"v_mfma_f32_16x16x32_bf16 v[140:143], v[96:99], v[156:159], v[140:143]\n"
"v_mfma_f32_16x16x32_bf16 v[140:143], v[104:107], v[168:171], v[140:143]\n"
"v_mfma_f32_16x16x32_bf16 v[140:143], v[112:115], v[176:179], v[140:143]\n"
"s_nop 1\n"
"ds_write_b128 v3, v[24:27] offset:4096\n"
"v_mfma_f32_16x16x32_bf16 v[140:143], v[184:187], v[60:63], v[140:143]\n"
"v_mfma_f32_16x16x32_bf16 v[140:143], v[192:195], v[68:71], v[140:143]\n"
"s_waitcnt vmcnt(21)\n"
"; @A G1 1.1\n"
"v_mfma_f32_16x16x32_bf16 v[24:27], a[0:3], v[200:203], 0\n"
"v_mov_b32_e32 v60, -1\n"
"v_mov_b32_e32 v61, -1\n"
"v_mfma_f32_16x16x32_bf16 v[24:27], a[4:7], v[76:79], v[24:27]\n"
"v_mov_b32_e32 v62, -1\n"
"v_mov_b32_e32 v63, -1\n"
"v_mfma_f32_16x16x32_bf16 v[24:27], a[8:11], v[208:211], v[24:27]\n"
"s_add_u32 s98, s55, 0x48000\n"
"buffer_load_dwordx4 v[68:71], v32, s[4:7], s98 offen sc1\n"
"v_mfma_f32_16x16x32_bf16 v[24:27], a[12:15], v[44:47], v[24:27]\n"
"s_waitcnt vmcnt(18)\n"
"ds_read_b64_tr_b16 v[148:149], v36\n"
"ds_read_b64_tr_b16 v[150:151], v36 offset:2048\n"
"s_mov_b32 s96, 0x30003\n"
"v_mfma_f32_16x16x32_bf16 v[24:27], a[16:19], v[72:75], v[24:27]\n"
"s_mov_b32 s97, 0x30003\n"
"v_mov_b32_e32 v159, 0x3f803f80\n"
"v_cndmask_b32_e64 v156, 0, v159, s[96:97]\n"
"v_cndmask_b32_e64 v157, 0, v159, s[96:97]\n"
"v_mfma_f32_16x16x32_bf16 v[24:27], a[20:23], v[204:207], v[24:27]\n"
"ds_write_b128 v3, v[140:143] offset:5120\n"
"v_mfma_f32_16x16x32_bf16 v[24:27], a[24:27], v[152:155], v[24:27]\n"
"v_cndmask_b32_e64 v158, 0, v159, s[96:97]\n"
"v_cndmask_b32_e64 v159, 0, v159, s[96:97]\n"
"v_mfma_f32_16x16x32_bf16 v[24:27], a[28:31], v[172:175], v[24:27]\n"
"buffer_load_dwordx4 v[140:143], v32, s[4:7], s98 offen offset:1024 sc1\n"
"s_waitcnt lgkmcnt(1)\n"
"v_mfma_f32_16x16x32_bf16 v[160:163], v[148:151], v[156:159], 0\n"
"buffer_store_dwordx4 v[148:151], v0, s[4:7], s20 offen sc1\n"
"s_nop 1\n"
"v_mfma_f32_16x16x32_bf16 v[148:151], a[32:35], v[200:203], 0\n"
"v_mfma_f32_16x16x32_bf16 v[148:151], a[36:39], v[76:79], v[148:151]\n"
"v_mfma_f32_16x16x32_bf16 v[148:151], a[40:43], v[208:211], v[148:151]\n"
"v_mfma_f32_16x16x32_bf16 v[148:151], a[44:47], v[44:47], v[148:151]\n"
"v_mfma_f32_16x16x32_bf16 v[148:151], a[48:51], v[72:75], v[148:151]\n"
"buffer_load_dwordx4 v[156:159], v33, s[4:7], s98 offen sc1\n"
"v_mfma_f32_16x16x32_bf16 v[148:151], a[52:55], v[204:207], v[148:151]\n"
"ds_write_b128 v3, v[24:27] offset:6144\n"
"v_mfma_f32_16x16x32_bf16 v[148:151], a[56:59], v[152:155], v[148:151]\n"
"s_add_u32 s97, s67, 0\n"
"buffer_load_dwordx2 v[246:247], v12, s[4:7], s97 offen sc1\n"
"s_add_u32 s96, s64, 0x2000\n"
"v_mfma_f32_16x16x32_bf16 v[148:151], a[60:63], v[172:175], v[148:151]\n"
"buffer_store_dwordx4 v[216:219], v7, s[4:7], s96 offen sc1\n"
"v_mfma_f32_16x16x32_bf16 v[24:27], a[64:67], v[200:203], 0\n"
"ds_read_b64_tr_b16 v[164:165], v36 offset:4096\n"
"ds_read_b64_tr_b16 v[166:167], v36 offset:6144\n"
"s_mov_b32 s94, 0x30003\n"
"v_mfma_f32_16x16x32_bf16 v[24:27], a[68:71], v[76:79], v[24:27]\n"
"buffer_load_dwordx4 v[168:171], v33, s[4:7], s98 offen offset:1024 sc1\n"
"s_mov_b32 s95, 0x30003\n"
"v_mfma_f32_16x16x32_bf16 v[24:27], a[72:75], v[208:211], v[24:27]\n"
"v_mov_b32_e32 v179, 0x3f803f80\n"
"v_cndmask_b32_e64 v176, 0, v179, s[94:95]\n"
"v_cndmask_b32_e64 v177, 0, v179, s[94:95]\n"
"v_mfma_f32_16x16x32_bf16 v[24:27], a[76:79], v[44:47], v[24:27]\n"
"v_cndmask_b32_e64 v178, 0, v179, s[94:95]\n"
"v_cndmask_b32_e64 v179, 0, v179, s[94:95]\n"
"v_mfma_f32_16x16x32_bf16 v[24:27], a[80:83], v[72:75], v[24:27]\n"
"s_waitcnt lgkmcnt(0)\n"
"v_mfma_f32_16x16x32_bf16 v[160:163], v[164:167], v[176:179], v[160:163]\n"
"buffer_store_dwordx4 v[164:167], v0, s[4:7], s20 offen offset:1024 sc1\n"
"v_mfma_f32_16x16x32_bf16 v[24:27], a[84:87], v[204:207], v[24:27]\n"
"ds_write_b128 v3, v[148:151] offset:7168\n"
"v_mfma_f32_16x16x32_bf16 v[24:27], a[88:91], v[152:155], v[24:27]\n"
"buffer_load_dwordx4 v[148:151], v34, s[4:7], s98 offen sc1\n"
"v_mfma_f32_16x16x32_bf16 v[24:27], a[92:95], v[172:175], v[24:27]\n"
"v_mfma_f32_16x16x32_bf16 v[164:167], a[96:99], v[200:203], 0\n"
"v_mfma_f32_16x16x32_bf16 v[164:167], a[100:103], v[76:79], v[164:167]\n"
"v_mfma_f32_16x16x32_bf16 v[164:167], a[104:107], v[208:211], v[164:167]\n"
"v_mfma_f32_16x16x32_bf16 v[164:167], a[108:111], v[44:47], v[164:167]\n"
"buffer_load_dwordx4 v[176:179], v34, s[4:7], s98 offen offset:1024 sc1\n"
"v_mfma_f32_16x16x32_bf16 v[164:167], a[112:115], v[72:75], v[164:167]\n"
"v_mfma_f32_16x16x32_bf16 v[164:167], a[116:119], v[204:207], v[164:167]\n"
"s_nop 0\n"
"ds_write_b128 v3, v[24:27] offset:8192\n"
"v_mfma_f32_16x16x32_bf16 v[164:167], a[120:123], v[152:155], v[164:167]\n"
"v_mfma_f32_16x16x32_bf16 v[164:167], a[124:127], v[172:175], v[164:167]\n"
"v_mfma_f32_16x16x32_bf16 v[24:27], v[28:31], v[200:203], 0\n"
"buffer_load_dwordx4 v[196:199], v35, s[4:7], s98 offen sc1\n"
"v_mfma_f32_16x16x32_bf16 v[24:27], v[52:55], v[76:79], v[24:27]\n"
"v_mfma_f32_16x16x32_bf16 v[24:27], v[84:87], v[208:211], v[24:27]\n"
"v_mfma_f32_16x16x32_bf16 v[24:27], v[92:95], v[44:47], v[24:27]\n"
"v_mfma_f32_16x16x32_bf16 v[24:27], v[100:103], v[72:75], v[24:27]\n"
"s_waitcnt vmcnt(13)\n"
"ds_read_b64_tr_b16 v[212:213], v37\n"
"v_add_f32_e32 v255, v255, v250\n"
"v_add_f32_e32 v253, v253, v249\n"
"v_mfma_f32_16x16x32_bf16 v[24:27], v[108:111], v[204:207], v[24:27]\n"
"buffer_load_dwordx4 v[216:219], v35, s[4:7], s98 offen offset:1024 sc1\n"
"s_add_u32 s98, s64, 0x6000\n"
"v_mfma_f32_16x16x32_bf16 v[24:27], v[180:183], v[152:155], v[24:27]\n"
"ds_write_b128 v3, v[164:167] offset:9216\n"
"v_mfma_f32_16x16x32_bf16 v[24:27], v[188:191], v[172:175], v[24:27]\n"
"buffer_store_dwordx4 v[80:83], v7, s[4:7], s98 offen sc1\n"
"s_nop 1\n"
"v_mfma_f32_16x16x32_bf16 v[80:83], v[48:51], v[200:203], 0\n"
"ds_read_b64_tr_b16 v[214:215], v37 offset:2048\n"
"v_add_f32_e32 v252, v252, v254\n"
"v_add_f32_e32 v251, v251, v248\n"
"v_mfma_f32_16x16x32_bf16 v[80:83], v[56:59], v[76:79], v[80:83]\n"
"s_mov_b32 s94, 0xc000c\n"
"v_add_f32_e32 v255, v255, v252\n"
"v_add_f32_e32 v253, v253, v251\n"
"s_mov_b32 s95, 0xc000c\n"
"v_mfma_f32_16x16x32_bf16 v[80:83], v[88:91], v[208:211], v[80:83]\n"
"v_add_f32_e32 v255, v255, v253\n"
"v_mov_b32_e32 v253, v255\n"
"v_mov_b32_e32 v79, 0x3f803f80\n"
"v_mfma_f32_16x16x32_bf16 v[80:83], v[96:99], v[44:47], v[80:83]\n"
"v_permlane32_swap_b32_e32 v255, v253\n"
"v_add_f32_e32 v255, v255, v253\n"
"v_cndmask_b32_e64 v76, 0, v79, s[94:95]\n"
"v_mfma_f32_16x16x32_bf16 v[80:83], v[104:107], v[72:75], v[80:83]\n"
"v_mul_f32_e32 v255, 0x3a800000, v255\n"
"v_mov_b32_e32 v253, v255\n"
"v_cndmask_b32_e64 v77, 0, v79, s[94:95]\n"
"v_mfma_f32_16x16x32_bf16 v[80:83], v[112:115], v[204:207], v[80:83]\n"
"ds_write_b128 v3, v[24:27] offset:10240\n"
"v_mfma_f32_16x16x32_bf16 v[80:83], v[184:187], v[152:155], v[80:83]\n"
"v_permlane16_swap_b32_e32 v255, v253\n"
"v_cvt_pk_bf16_f32 v254, -v255, 0\n"
"v_cndmask_b32_e64 v78, 0, v79, s[94:95]\n"
"v_mfma_f32_16x16x32_bf16 v[80:83], v[192:195], v[172:175], v[80:83]\n"
"v_fma_f32 v253, -v255, v255, v253\n"
"v_lshlrev_b32_e32 v254, 16, v254\n"
"v_cndmask_b32_e64 v79, 0, v79, s[94:95]\n"
"; @A G1 1.2\n"
"v_mfma_f32_16x16x32_bf16 v[24:27], a[0:3], v[120:123], 0\n"
"s_waitcnt lgkmcnt(1)\n"
"v_max_f32_e32 v253, 0, v253\n"
"v_cvt_pk_bf16_f32 v253, v253, 0\n"
"v_mfma_f32_16x16x32_bf16 v[160:163], v[212:215], v[76:79], v[160:163]\n"
"buffer_store_dwordx4 v[212:215], v0, s[4:7], s21 offen sc1\n"
"v_mfma_f32_16x16x32_bf16 v[24:27], a[4:7], v[124:127], v[24:27]\n"
"s_waitcnt vmcnt(9)\n"
"v_cmp_ne_u64_e32 vcc, v[246:247], v[18:19]\n"
"v_mfma_f32_16x16x32_bf16 v[24:27], a[8:11], v[132:135], v[24:27]\n"
"s_cbranch_vccnz .Lmk_f2s\n"
".Lmk_f2r:\n"
"v_lshlrev_b32_e32 v253, 16, v253\n"
"v_mfma_f32_16x16x32_bf16 v[24:27], a[12:15], v[136:139], v[24:27]\n"
"v_add_f32_e32 v253, 0x3727c5ac, v253\n"
"v_cvt_pk_bf16_f32 v253, v253, 0\n"
"v_mfma_f32_16x16x32_bf16 v[24:27], a[16:19], v[128:131], v[24:27]\n"
"v_lshlrev_b32_e32 v253, 16, v253\n"
"v_mfma_f32_16x16x32_bf16 v[24:27], a[20:23], v[64:67], v[24:27]\n"
"ds_write_b128 v3, v[80:83] offset:11264\n"
"v_mfma_f32_16x16x32_bf16 v[24:27], a[24:27], v[116:119], v[24:27]\n"
"v_sqrt_f32_e32 v253, v253\n"
"v_mfma_f32_16x16x32_bf16 v[24:27], a[28:31], v[144:147], v[24:27]\n"
"v_cvt_pk_bf16_f32 v253, v253, 0\n"
"v_lshlrev_b32_e32 v253, 16, v253\n"
"v_mfma_f32_16x16x32_bf16 v[44:47], a[32:35], v[120:123], 0\n"
"v_rcp_f32_e32 v255, v253\n"
"v_mfma_f32_16x16x32_bf16 v[44:47], a[36:39], v[124:127], v[44:47]\n"
"v_mul_f32_e32 v253, 0x3e8293ee, v255\n"
"v_mfma_f32_16x16x32_bf16 v[44:47], a[40:43], v[132:135], v[44:47]\n"
"s_add_u32 s98, s52, 0x8000\n"
"buffer_load_dwordx4 v[72:75], v32, s[4:7], s98 offen sc1\n"
"v_mfma_f32_16x16x32_bf16 v[44:47], a[44:47], v[136:139], v[44:47]\n"
"s_waitcnt lgkmcnt(0)\n"
"s_barrier\n"
"ds_read_b64_tr_b16 v[76:77], v37 offset:4096\n"
"v_mfma_f32_16x16x32_bf16 v[44:47], a[48:51], v[128:131], v[44:47]\n"
"ds_read_b128 v[80:83], v4\n"
"ds_read_b128 v[152:155], v9 offset:128\n"
"ds_read_b64_tr_b16 v[78:79], v37 offset:6144\n"
"v_mfma_f32_16x16x32_bf16 v[44:47], a[52:55], v[64:67], v[44:47]\n"
"ds_write_b128 v3, v[24:27] offset:12288\n"
"v_mfma_f32_16x16x32_bf16 v[44:47], a[56:59], v[116:119], v[44:47]\n"
"ds_read_b128 v[24:27], v5\n"
"s_mov_b32 s96, 0xc000c\n"
"s_mov_b32 s97, 0xc000c\n"
"v_mfma_f32_16x16x32_bf16 v[44:47], a[60:63], v[144:147], v[44:47]\n"
"buffer_load_dwordx4 v[164:167], v32, s[4:7], s98 offen offset:1024 sc1\n"
"v_mov_b32_e32 v175, 0x3f803f80\n"
"v_mfma_f32_16x16x32_bf16 v[200:203], a[64:67], v[120:123], 0\n"
"v_cndmask_b32_e64 v172, 0, v175, s[96:97]\n"
"v_cndmask_b32_e64 v173, 0, v175, s[96:97]\n"
"v_cndmask_b32_e64 v174, 0, v175, s[96:97]\n"
"v_mfma_f32_16x16x32_bf16 v[200:203], a[68:71], v[124:127], v[200:203]\n"
"v_cndmask_b32_e64 v175, 0, v175, s[96:97]\n"
"v_mfma_f32_16x16x32_bf16 v[200:203], a[72:75], v[132:135], v[200:203]\n"
"s_waitcnt lgkmcnt(2)\n"
"v_mfma_f32_16x16x32_bf16 v[160:163], v[76:79], v[172:175], v[160:163]\n"
"buffer_store_dwordx4 v[76:79], v0, s[4:7], s21 offen offset:1024 sc1\n"
"v_mfma_f32_16x16x32_bf16 v[200:203], a[76:79], v[136:139], v[200:203]\n"
"ds_read_b128 v[76:79], v4 offset:1024\n"
"ds_read_b128 v[172:175], v9 offset:192\n"
"v_mfma_f32_16x16x32_bf16 v[200:203], a[80:83], v[128:131], v[200:203]\n"
"buffer_load_dwordx4 v[204:207], v33, s[4:7], s98 offen sc1\n"
"v_mfma_f32_16x16x32_bf16 v[200:203], a[84:87], v[64:67], v[200:203]\n"
"ds_write_b128 v3, v[44:47] offset:13312\n"
"v_mfma_f32_16x16x32_bf16 v[200:203], a[88:91], v[116:119], v[200:203]\n"
"ds_read_b128 v[44:47], v5 offset:1024\n"
"s_waitcnt lgkmcnt(4)\n"
"v_mfma_f32_16x16x32_bf16 v[200:203], a[92:95], v[144:147], v[200:203]\n"
"v_fmac_f32_e32 v80, v254, v152\n"
"v_fmac_f32_e32 v81, v254, v153\n"
"v_mfma_f32_16x16x32_bf16 v[208:211], a[96:99], v[120:123], 0\n"
"v_fmac_f32_e32 v82, v254, v154\n"
"v_fmac_f32_e32 v83, v254, v155\n"
"v_mfma_f32_16x16x32_bf16 v[208:211], a[100:103], v[124:127], v[208:211]\n"
"buffer_load_dwordx4 v[152:155], v33, s[4:7], s98 offen offset:1024 sc1\n"
"v_mfma_f32_16x16x32_bf16 v[208:211], a[104:107], v[132:135], v[208:211]\n"
"ds_read_b128 v[212:215], v4 offset:2048\n"
"ds_read_b128 v[220:223], v9 offset:256\n"
"v_mfma_f32_16x16x32_bf16 v[208:211], a[108:111], v[136:139], v[208:211]\n"
"ds_read_b128 v[224:227], v5 offset:2048\n"
"v_add_f32_e32 v80, v80, v24\n"
"v_mfma_f32_16x16x32_bf16 v[208:211], a[112:115], v[128:131], v[208:211]\n"
"v_add_f32_e32 v81, v81, v25\n"
"v_add_f32_e32 v82, v82, v26\n"
"v_mfma_f32_16x16x32_bf16 v[208:211], a[116:119], v[64:67], v[208:211]\n"
"ds_write_b128 v3, v[200:203] offset:14336\n"
"v_mfma_f32_16x16x32_bf16 v[208:211], a[120:123], v[116:119], v[208:211]\n"
"buffer_load_dwordx4 v[200:203], v34, s[4:7], s98 offen sc1\n"
"v_mfma_f32_16x16x32_bf16 v[208:211], a[124:127], v[144:147], v[208:211]\n"
"s_waitcnt lgkmcnt(4)\n"
"v_add_f32_e32 v83, v83, v27\n"
"v_fmac_f32_e32 v76, v254, v172\n"
"v_mfma_f32_16x16x32_bf16 v[24:27], v[28:31], v[120:123], 0\n"
"ds_read_b64_tr_b16 v[228:229], v38\n"
"v_fmac_f32_e32 v77, v254, v173\n"
"v_fmac_f32_e32 v78, v254, v174\n"
"v_mfma_f32_16x16x32_bf16 v[24:27], v[52:55], v[124:127], v[24:27]\n"
"ds_read_b128 v[232:235], v4 offset:3072\n"
"ds_read_b128 v[236:239], v9 offset:320\n"
"ds_read_b64_tr_b16 v[230:231], v38 offset:2048\n"
"v_mfma_f32_16x16x32_bf16 v[24:27], v[84:87], v[132:135], v[24:27]\n"
"ds_read_b128 v[240:243], v5 offset:3072\n"
"v_fmac_f32_e32 v79, v254, v175\n"
"s_mov_b32 s96, 0x300030\n"
"v_mfma_f32_16x16x32_bf16 v[24:27], v[92:95], v[136:139], v[24:27]\n"
"buffer_load_dwordx4 v[172:175], v34, s[4:7], s98 offen offset:1024 sc1\n"
"s_mov_b32 s97, 0x300030\n"
"v_mfma_f32_16x16x32_bf16 v[24:27], v[100:103], v[128:131], v[24:27]\n"
"v_add_f32_e32 v76, v76, v44\n"
"v_add_f32_e32 v77, v77, v45\n"
"v_mov_b32_e32 v247, 0x3f803f80\n"
"v_mfma_f32_16x16x32_bf16 v[24:27], v[108:111], v[64:67], v[24:27]\n"
"ds_write_b128 v3, v[208:211] offset:15360\n"
"v_mfma_f32_16x16x32_bf16 v[24:27], v[180:183], v[116:119], v[24:27]\n"
"s_waitcnt lgkmcnt(7)\n"
"v_add_f32_e32 v78, v78, v46\n"
"v_add_f32_e32 v79, v79, v47\n"
"v_cndmask_b32_e64 v244, 0, v247, s[96:97]\n"
"v_mfma_f32_16x16x32_bf16 v[24:27], v[188:191], v[144:147], v[24:27]\n"
"v_fmac_f32_e32 v212, v254, v220\n"
"v_fmac_f32_e32 v213, v254, v221\n"
"v_cndmask_b32_e64 v245, 0, v247, s[96:97]\n"
"v_mfma_f32_16x16x32_bf16 v[44:47], v[48:51], v[120:123], 0\n"
"buffer_load_dwordx4 v[120:123], v35, s[4:7], s98 offen sc1\n"
"v_cndmask_b32_e64 v246, 0, v247, s[96:97]\n"
"v_mfma_f32_16x16x32_bf16 v[44:47], v[56:59], v[124:127], v[44:47]\n"
"ds_read_b128 v[124:127], v4 offset:4096\n"
"ds_read_b128 v[208:211], v9\n"
"v_cndmask_b32_e64 v247, 0, v247, s[96:97]\n"
"v_mfma_f32_16x16x32_bf16 v[44:47], v[88:91], v[132:135], v[44:47]\n"
"ds_read_b128 v[132:135], v5 offset:4096\n"
"v_fmac_f32_e32 v214, v254, v222\n"
"s_waitcnt lgkmcnt(5)\n"
"v_mfma_f32_16x16x32_bf16 v[160:163], v[228:231], v[244:247], v[160:163]\n"
"buffer_store_dwordx4 v[228:231], v0, s[4:7], s22 offen sc1\n"
"v_mfma_f32_16x16x32_bf16 v[44:47], v[96:99], v[136:139], v[44:47]\n"
"v_fmac_f32_e32 v215, v254, v223\n"
"v_add_f32_e32 v212, v212, v224\n"
"v_mfma_f32_16x16x32_bf16 v[44:47], v[104:107], v[128:131], v[44:47]\n"
"s_add_u32 s97, s75, 0\n"
"buffer_load_dwordx2 v[250:251], v41, s[4:7], s97 offen sc1\n"
"v_mfma_f32_16x16x32_bf16 v[44:47], v[112:115], v[64:67], v[44:47]\n"
"buffer_load_dwordx4 v[64:67], v35, s[4:7], s98 offen offset:1024 sc1\n"
"v_mfma_f32_16x16x32_bf16 v[44:47], v[184:187], v[116:119], v[44:47]\n"
"ds_write_b128 v3, v[24:27] offset:16384\n"
"v_mfma_f32_16x16x32_bf16 v[44:47], v[192:195], v[144:147], v[44:47]\n"
"s_waitcnt vmcnt(18)\n"
"ds_read_b128 v[24:27], v4 offset:5120\n"
"ds_read_b128 v[116:119], v9 offset:64\n"
"; @A G1 1.3\n"
"v_mfma_f32_16x16x32_bf16 v[80:83], a[0:3], v[68:71], v[80:83]\n"
"ds_read_b128 v[128:131], v5 offset:5120\n"
"v_add_f32_e32 v213, v213, v225\n"
"v_mfma_f32_16x16x32_bf16 v[80:83], a[4:7], v[140:143], v[80:83]\n"
"s_waitcnt lgkmcnt(8)\n"
"v_add_f32_e32 v214, v214, v226\n"
"v_add_f32_e32 v215, v215, v227\n"
"v_mfma_f32_16x16x32_bf16 v[80:83], a[8:11], v[156:159], v[80:83]\n"
"s_add_u32 s98, s53, 0x8000\n"
"buffer_load_dwordx4 v[136:139], v32, s[4:7], s98 offen sc1\n"
"v_mfma_f32_16x16x32_bf16 v[80:83], a[12:15], v[168:171], v[80:83]\n"
"s_waitcnt vmcnt(14)\n"
"v_fmac_f32_e32 v232, v254, v236\n"
"v_fmac_f32_e32 v233, v254, v237\n"
"v_mfma_f32_16x16x32_bf16 v[80:83], a[16:19], v[148:151], v[80:83]\n"
"v_fmac_f32_e32 v234, v254, v238\n"
"v_fmac_f32_e32 v235, v254, v239\n"
"v_mfma_f32_16x16x32_bf16 v[80:83], a[20:23], v[176:179], v[80:83]\n"
"ds_write_b128 v3, v[44:47] offset:17408\n"
"v_mfma_f32_16x16x32_bf16 v[80:83], a[24:27], v[196:199], v[80:83]\n"
"v_add_f32_e32 v232, v232, v240\n"
"v_add_f32_e32 v233, v233, v241\n"
"v_mfma_f32_16x16x32_bf16 v[80:83], a[28:31], v[216:219], v[80:83]\n"
"buffer_load_dwordx4 v[44:47], v32, s[4:7], s98 offen offset:1024 sc1\n"
"v_mfma_f32_16x16x32_bf16 v[76:79], a[32:35], v[68:71], v[76:79]\n"
"s_waitcnt lgkmcnt(5)\n"
"v_add_f32_e32 v234, v234, v242\n"
"v_add_f32_e32 v235, v235, v243\n"
"v_mfma_f32_16x16x32_bf16 v[76:79], a[36:39], v[140:143], v[76:79]\n"
"v_fmac_f32_e32 v124, v254, v208\n"
"v_fmac_f32_e32 v125, v254, v209\n"
"v_mfma_f32_16x16x32_bf16 v[76:79], a[40:43], v[156:159], v[76:79]\n"
"s_waitcnt lgkmcnt(0)\n"
"s_barrier\n"
"v_mfma_f32_16x16x32_bf16 v[76:79], a[44:47], v[168:171], v[76:79]\n"
"ds_read_b128 v[144:147], v6\n"
"ds_read_b128 v[220:223], v6 offset:1024\n"
"v_mfma_f32_16x16x32_bf16 v[76:79], a[48:51], v[148:151], v[76:79]\n"
"buffer_load_dwordx4 v[224:227], v33, s[4:7], s98 offen sc1\n"
"v_mfma_f32_16x16x32_bf16 v[76:79], a[52:55], v[176:179], v[76:79]\n"
"v_fmac_f32_e32 v126, v254, v210\n"
"v_fmac_f32_e32 v127, v254, v211\n"
"v_mfma_f32_16x16x32_bf16 v[76:79], a[56:59], v[196:199], v[76:79]\n"
"s_add_u32 s96, s60, 0x48000\n"
"buffer_store_dwordx4 v[60:63], v0, s[4:7], s96 offen sc1\n"
"v_add_f32_e32 v124, v124, v132\n"
"v_add_f32_e32 v125, v125, v133\n"
"v_mfma_f32_16x16x32_bf16 v[76:79], a[60:63], v[216:219], v[76:79]\n"
"v_add_f32_e32 v126, v126, v134\n"
"v_add_f32_e32 v127, v127, v135\n"
"v_mfma_f32_16x16x32_bf16 v[212:215], a[64:67], v[68:71], v[212:215]\n"
"v_fmac_f32_e32 v24, v254, v116\n"
"v_fmac_f32_e32 v25, v254, v117\n"
"v_mfma_f32_16x16x32_bf16 v[212:215], a[68:71], v[140:143], v[212:215]\n"
"buffer_load_dwordx4 v[132:135], v33, s[4:7], s98 offen offset:1024 sc1\n"
"v_mfma_f32_16x16x32_bf16 v[212:215], a[72:75], v[156:159], v[212:215]\n"
"v_fmac_f32_e32 v26, v254, v118\n"
"v_fmac_f32_e32 v27, v254, v119\n"
"v_mfma_f32_16x16x32_bf16 v[212:215], a[76:79], v[168:171], v[212:215]\n"
"v_add_f32_e32 v24, v24, v128\n"
"v_add_f32_e32 v25, v25, v129\n"
"v_mfma_f32_16x16x32_bf16 v[212:215], a[80:83], v[148:151], v[212:215]\n"
"s_waitcnt lgkmcnt(0)\n"
"v_add_f32_e32 v26, v26, v130\n"
"v_add_f32_e32 v27, v27, v131\n"
"v_mfma_f32_16x16x32_bf16 v[212:215], a[84:87], v[176:179], v[212:215]\n"
"v_add_f32_e32 v80, v80, v144\n"
"v_add_f32_e32 v81, v81, v145\n"
"v_mfma_f32_16x16x32_bf16 v[212:215], a[88:91], v[196:199], v[212:215]\n"
"buffer_load_dwordx4 v[116:119], v34, s[4:7], s98 offen sc1\n"
"v_mfma_f32_16x16x32_bf16 v[212:215], a[92:95], v[216:219], v[212:215]\n"
"ds_read_b128 v[128:131], v6 offset:2048\n"
"ds_read_b128 v[208:211], v6 offset:3072\n"
"v_mfma_f32_16x16x32_bf16 v[232:235], a[96:99], v[68:71], v[232:235]\n"
"v_add_f32_e32 v82, v82, v146\n"
"v_add_f32_e32 v83, v83, v147\n"
"v_mfma_f32_16x16x32_bf16 v[232:235], a[100:103], v[140:143], v[232:235]\n"
"v_add_f32_e32 v76, v76, v220\n"
"v_add_f32_e32 v77, v77, v221\n"
"v_mfma_f32_16x16x32_bf16 v[232:235], a[104:107], v[156:159], v[232:235]\n"
"v_add_f32_e32 v78, v78, v222\n"
"v_add_f32_e32 v79, v79, v223\n"
"v_mfma_f32_16x16x32_bf16 v[232:235], a[108:111], v[168:171], v[232:235]\n"
"buffer_load_dwordx4 v[144:147], v34, s[4:7], s98 offen offset:1024 sc1\n"
"v_mfma_f32_16x16x32_bf16 v[232:235], a[112:115], v[148:151], v[232:235]\n"
"v_mul_f32_e32 v80, v80, v255\n"
"v_mul_f32_e32 v81, v81, v255\n"
"v_mfma_f32_16x16x32_bf16 v[232:235], a[116:119], v[176:179], v[232:235]\n"
"v_mul_f32_e32 v82, v82, v255\n"
"v_mul_f32_e32 v83, v83, v255\n"
"v_mfma_f32_16x16x32_bf16 v[232:235], a[120:123], v[196:199], v[232:235]\n"
"v_mul_f32_e32 v76, v76, v255\n"
"v_mul_f32_e32 v77, v77, v255\n"
"v_mfma_f32_16x16x32_bf16 v[232:235], a[124:127], v[216:219], v[232:235]\n"
"v_mul_f32_e32 v78, v78, v255\n"
"v_mul_f32_e32 v79, v79, v255\n"
"v_mfma_f32_16x16x32_bf16 v[124:127], v[28:31], v[68:71], v[124:127]\n"
"buffer_load_dwordx4 v[220:223], v35, s[4:7], s98 offen sc1\n"
"v_mfma_f32_16x16x32_bf16 v[124:127], v[52:55], v[140:143], v[124:127]\n"
"v_cvt_pk_bf16_f32 v228, v80, v81\n"
"v_cvt_pk_bf16_f32 v229, v82, v83\n"
"v_mfma_f32_16x16x32_bf16 v[124:127], v[84:87], v[156:159], v[124:127]\n"
"v_cvt_pk_bf16_f32 v230, v76, v77\n"
"v_cvt_pk_bf16_f32 v231, v78, v79\n"
"v_mfma_f32_16x16x32_bf16 v[124:127], v[92:95], v[168:171], v[124:127]\n"
"ds_write_b128 v8, v[228:231] offset:25600\n"
"v_mfma_f32_16x16x32_bf16 v[124:127], v[100:103], v[148:151], v[124:127]\n"
"s_waitcnt lgkmcnt(1)\n"
"s_add_u32 s96, s60, 0x40000\n"
"buffer_store_dwordx4 v[60:63], v0, s[4:7], s96 offen sc1\n"
"v_add_f32_e32 v212, v212, v128\n"
"v_add_f32_e32 v213, v213, v129\n"
"v_mfma_f32_16x16x32_bf16 v[124:127], v[108:111], v[176:179], v[124:127]\n"
"buffer_load_dwordx4 v[60:63], v35, s[4:7], s98 offen offset:1024 sc1\n"
"v_mfma_f32_16x16x32_bf16 v[124:127], v[180:183], v[196:199], v[124:127]\n"
"ds_read_b128 v[76:79], v6 offset:4096\n"
"ds_read_b128 v[80:83], v6 offset:5120\n"
"v_mfma_f32_16x16x32_bf16 v[124:127], v[188:191], v[216:219], v[124:127]\n"
"v_add_f32_e32 v214, v214, v130\n"
"v_add_f32_e32 v215, v215, v131\n"
"v_mfma_f32_16x16x32_bf16 v[24:27], v[48:51], v[68:71], v[24:27]\n"
"s_waitcnt vmcnt(11)\n"
"v_cmp_ne_u64_e32 vcc, v[250:251], v[18:19]\n"
"v_mfma_f32_16x16x32_bf16 v[24:27], v[56:59], v[140:143], v[24:27]\n"
"s_cbranch_vccnz .Lmk_f5s\n"
".Lmk_f5r:\n"
"v_add_f32_e32 v232, v232, v208\n"
"v_mfma_f32_16x16x32_bf16 v[24:27], v[88:91], v[156:159], v[24:27]\n"
"v_add_f32_e32 v233, v233, v209\n"
"v_add_f32_e32 v234, v234, v210\n"
"v_mfma_f32_16x16x32_bf16 v[24:27], v[96:99], v[168:171], v[24:27]\n"
"v_add_f32_e32 v235, v235, v211\n"
"v_mul_f32_e32 v212, v212, v255\n"
"v_mfma_f32_16x16x32_bf16 v[24:27], v[104:107], v[148:151], v[24:27]\n"
"v_mul_f32_e32 v213, v213, v255\n"
"v_mul_f32_e32 v214, v214, v255\n"
"v_mfma_f32_16x16x32_bf16 v[24:27], v[112:115], v[176:179], v[24:27]\n"
"v_mul_f32_e32 v215, v215, v255\n"
"v_mul_f32_e32 v232, v232, v255\n"
"v_mfma_f32_16x16x32_bf16 v[24:27], v[184:187], v[196:199], v[24:27]\n"
"v_mul_f32_e32 v233, v233, v255\n"
"v_mul_f32_e32 v234, v234, v255\n"
"v_mfma_f32_16x16x32_bf16 v[24:27], v[192:195], v[216:219], v[24:27]\n"
"v_mul_f32_e32 v235, v235, v255\n"
"v_cvt_pk_bf16_f32 v40, v212, v213\n"
"v_cvt_pk_bf16_f32 v41, v214, v215\n"
"v_cvt_pk_bf16_f32 v42, v232, v233\n"
"v_cvt_pk_bf16_f32 v43, v234, v235\n"
"ds_write_b128 v7, v[40:43] offset:58368\n"
".Lmk_z2:\n"
"; @A G1 2.0\n"
"v_mfma_f32_16x16x32_bf16 v[68:71], a[0:3], v[72:75], 0\n"
"v_mfma_f32_16x16x32_bf16 v[68:71], a[4:7], v[164:167], v[68:71]\n"
"s_add_u32 s98, s62, 0x800\n"
"v_mfma_f32_16x16x32_bf16 v[68:71], a[8:11], v[204:207], v[68:71]\n"
"s_add_u32 s97, s54, 0x8000\n"
"buffer_load_dwordx4 v[128:131], v32, s[4:7], s97 offen sc1\n"
"v_mfma_f32_16x16x32_bf16 v[68:71], a[12:15], v[152:155], v[68:71]\n"
"s_waitcnt vmcnt(11)\n"
"v_mfma_f32_16x16x32_bf16 v[68:71], a[16:19], v[200:203], v[68:71]\n"
"buffer_load_dword v255, v10, s[4:7], s98 offen sc1\n"
"v_mfma_f32_16x16x32_bf16 v[68:71], a[20:23], v[172:175], v[68:71]\n"
"s_waitcnt lgkmcnt(1)\n"
"v_add_f32_e32 v124, v124, v76\n"
"v_add_f32_e32 v125, v125, v77\n"
"v_mfma_f32_16x16x32_bf16 v[68:71], a[24:27], v[120:123], v[68:71]\n"
"v_add_f32_e32 v126, v126, v78\n"
"v_add_f32_e32 v127, v127, v79\n"
"v_mfma_f32_16x16x32_bf16 v[68:71], a[28:31], v[64:67], v[68:71]\n"
"buffer_load_dwordx4 v[76:79], v32, s[4:7], s97 offen offset:1024 sc1\n"
"v_mfma_f32_16x16x32_bf16 v[140:143], a[32:35], v[72:75], 0\n"
"s_add_u32 s96, s70, 0\n"
"buffer_load_dwordx4 a[128:131], v0, s[4:7], s96 offen sc1\n"
"v_mfma_f32_16x16x32_bf16 v[140:143], a[36:39], v[164:167], v[140:143]\n"
"buffer_load_dword v254, v10, s[4:7], s98 offen offset:256 sc1\n"
"v_mfma_f32_16x16x32_bf16 v[140:143], a[40:43], v[204:207], v[140:143]\n"
"v_add_f32_e32 v24, v24, v80\n"
"v_add_f32_e32 v25, v25, v81\n"
"v_mfma_f32_16x16x32_bf16 v[140:143], a[44:47], v[152:155], v[140:143]\n"
"v_add_f32_e32 v26, v26, v82\n"
"v_add_f32_e32 v27, v27, v83\n"
"v_mfma_f32_16x16x32_bf16 v[140:143], a[48:51], v[200:203], v[140:143]\n"
"buffer_load_dwordx4 v[80:83], v33, s[4:7], s97 offen sc1\n"
"v_mfma_f32_16x16x32_bf16 v[140:143], a[52:55], v[172:175], v[140:143]\n"
"ds_write_b128 v3, v[68:71]\n"
"v_mfma_f32_16x16x32_bf16 v[140:143], a[56:59], v[120:123], v[140:143]\n"
"buffer_load_dword v252, v10, s[4:7], s98 offen offset:512 sc1\n"
"v_mfma_f32_16x16x32_bf16 v[140:143], a[60:63], v[64:67], v[140:143]\n"
"v_mul_f32_e32 v124, v124, v253\n"
"v_mul_f32_e32 v125, v125, v253\n"
"v_mfma_f32_16x16x32_bf16 v[68:71], a[64:67], v[72:75], 0\n"
"v_mul_f32_e32 v126, v126, v253\n"
"v_mul_f32_e32 v127, v127, v253\n"
"v_mfma_f32_16x16x32_bf16 v[68:71], a[68:71], v[164:167], v[68:71]\n"
"buffer_load_dwordx4 v[148:151], v33, s[4:7], s97 offen offset:1024 sc1\n"
"v_mfma_f32_16x16x32_bf16 v[68:71], a[72:75], v[204:207], v[68:71]\n"
"buffer_load_dwordx4 a[132:135], v0, s[4:7], s96 offen offset:1024 sc1\n"
"v_mfma_f32_16x16x32_bf16 v[68:71], a[76:79], v[152:155], v[68:71]\n"
"buffer_load_dword v251, v10, s[4:7], s98 offen offset:768 sc1\n"
"v_mfma_f32_16x16x32_bf16 v[68:71], a[80:83], v[200:203], v[68:71]\n"
"v_mul_f32_e32 v24, v24, v253\n"
"v_mul_f32_e32 v25, v25, v253\n"
"v_mfma_f32_16x16x32_bf16 v[68:71], a[84:87], v[172:175], v[68:71]\n"
"ds_write_b128 v3, v[140:143] offset:1024\n"
"v_mfma_f32_16x16x32_bf16 v[68:71], a[88:91], v[120:123], v[68:71]\n"
"buffer_load_dwordx4 v[140:143], v34, s[4:7], s97 offen sc1\n"
"v_mfma_f32_16x16x32_bf16 v[68:71], a[92:95], v[64:67], v[68:71]\n"
"buffer_load_dword v250, v10, s[4:7], s98 offen offset:1024 sc1\n"
"v_mfma_f32_16x16x32_bf16 v[156:159], a[96:99], v[72:75], 0\n"
"v_mul_f32_e32 v26, v26, v253\n"
"v_mul_f32_e32 v27, v27, v253\n"
"v_mfma_f32_16x16x32_bf16 v[156:159], a[100:103], v[164:167], v[156:159]\n"
"v_cvt_pk_bf16_f32 v168, v124, v125\n"
"v_cvt_pk_bf16_f32 v169, v126, v127\n"
"v_mfma_f32_16x16x32_bf16 v[156:159], a[104:107], v[204:207], v[156:159]\n"
"buffer_load_dword v253, v10, s[4:7], s98 offen offset:1280 sc1\n"
"v_mfma_f32_16x16x32_bf16 v[156:159], a[108:111], v[152:155], v[156:159]\n"
"buffer_load_dwordx4 v[124:127], v34, s[4:7], s97 offen offset:1024 sc1\n"
"v_mfma_f32_16x16x32_bf16 v[156:159], a[112:115], v[200:203], v[156:159]\n"
"buffer_load_dwordx4 a[136:139], v0, s[4:7], s96 offen offset:2048 sc1\n"
"v_mfma_f32_16x16x32_bf16 v[156:159], a[116:119], v[172:175], v[156:159]\n"
"ds_write_b128 v3, v[68:71] offset:2048\n"
"v_mfma_f32_16x16x32_bf16 v[156:159], a[120:123], v[120:123], v[156:159]\n"
"buffer_load_dword v249, v10, s[4:7], s98 offen offset:1536 sc1\n"
"v_mfma_f32_16x16x32_bf16 v[156:159], a[124:127], v[64:67], v[156:159]\n"
"v_cvt_pk_bf16_f32 v170, v24, v25\n"
"v_cvt_pk_bf16_f32 v171, v26, v27\n"
"v_mfma_f32_16x16x32_bf16 v[24:27], v[28:31], v[72:75], 0\n"
"buffer_load_dwordx4 v[68:71], v35, s[4:7], s97 offen sc1\n"
"v_mfma_f32_16x16x32_bf16 v[24:27], v[52:55], v[164:167], v[24:27]\n"
"buffer_load_dword v248, v10, s[4:7], s98 offen offset:1792 sc1\n"
"v_mfma_f32_16x16x32_bf16 v[24:27], v[84:87], v[204:207], v[24:27]\n"
"ds_write_b128 v2, v[168:171] offset:50176\n"
"v_mfma_f32_16x16x32_bf16 v[24:27], v[92:95], v[152:155], v[24:27]\n"
"s_add_u32 s98, s64, 0x2000\n"
"buffer_store_dwordx4 v[228:231], v8, s[4:7], s98 offen offset:1024 sc1\n"
"v_mfma_f32_16x16x32_bf16 v[24:27], v[100:103], v[200:203], v[24:27]\n"
"v_mfma_f32_16x16x32_bf16 v[24:27], v[108:111], v[172:175], v[24:27]\n"
"buffer_load_dwordx4 v[168:171], v35, s[4:7], s97 offen offset:1024 sc1\n"
"v_mfma_f32_16x16x32_bf16 v[24:27], v[180:183], v[120:123], v[24:27]\n"
"ds_write_b128 v3, v[156:159] offset:3072\n"
"v_mfma_f32_16x16x32_bf16 v[24:27], v[188:191], v[64:67], v[24:27]\n"
"buffer_load_dwordx4 a[140:143], v0, s[4:7], s96 offen offset:3072 sc1\n"
"v_mfma_f32_16x16x32_bf16 v[156:159], v[48:51], v[72:75], 0\n"
"v_mfma_f32_16x16x32_bf16 v[156:159], v[56:59], v[164:167], v[156:159]\n"
"v_mfma_f32_16x16x32_bf16 v[156:159], v[88:91], v[204:207], v[156:159]\n"
"v_mfma_f32_16x16x32_bf16 v[156:159], v[96:99], v[152:155], v[156:159]\n"
"v_mfma_f32_16x16x32_bf16 v[156:159], v[104:107], v[200:203], v[156:159]\n"
"v_mfma_f32_16x16x32_bf16 v[156:159], v[112:115], v[172:175], v[156:159]\n"
"s_nop 0\n"
"ds_write_b128 v3, v[24:27] offset:4096\n"
"v_mfma_f32_16x16x32_bf16 v[156:159], v[184:187], v[120:123], v[156:159]\n"
"v_mfma_f32_16x16x32_bf16 v[156:159], v[192:195], v[64:67], v[156:159]\n"
"s_waitcnt vmcnt(26)\n"
"; @A G1 2.1\n"
"v_mfma_f32_16x16x32_bf16 v[24:27], a[0:3], v[136:139], 0\n"
"v_mov_b32_e32 v64, -1\n"
"v_mov_b32_e32 v65, -1\n"
"v_mfma_f32_16x16x32_bf16 v[24:27], a[4:7], v[44:47], v[24:27]\n"
"buffer_load_dwordx4 a[144:147], v1, s[4:7], s96 offen sc1\n"
"v_mfma_f32_16x16x32_bf16 v[24:27], a[8:11], v[224:227], v[24:27]\n"
"s_add_u32 s98, s55, 0x8000\n"
"buffer_load_dwordx4 v[72:75], v32, s[4:7], s98 offen sc1\n"
"v_mfma_f32_16x16x32_bf16 v[24:27], a[12:15], v[132:135], v[24:27]\n"
"s_waitcnt vmcnt(23)\n"
"v_mov_b32_e32 v66, -1\n"
"v_mov_b32_e32 v67, -1\n"
"v_mfma_f32_16x16x32_bf16 v[24:27], a[16:19], v[116:119], v[24:27]\n"
"ds_read_b64_tr_b16 v[120:121], v38 offset:4096\n"
"ds_read_b64_tr_b16 v[122:123], v38 offset:6144\n"
"s_mov_b32 s94, 0x300030\n"
"v_mfma_f32_16x16x32_bf16 v[24:27], a[20:23], v[144:147], v[24:27]\n"
"ds_write_b128 v3, v[156:159] offset:5120\n"
"v_mfma_f32_16x16x32_bf16 v[24:27], a[24:27], v[220:223], v[24:27]\n"
"s_mov_b32 s95, 0x300030\n"
"v_mov_b32_e32 v155, 0x3f803f80\n"
"v_cndmask_b32_e64 v152, 0, v155, s[94:95]\n"
"v_cndmask_b32_e64 v153, 0, v155, s[94:95]\n"
"v_mfma_f32_16x16x32_bf16 v[24:27], a[28:31], v[60:63], v[24:27]\n"
"buffer_load_dwordx4 v[156:159], v32, s[4:7], s98 offen offset:1024 sc1\n"
"v_cndmask_b32_e64 v154, 0, v155, s[94:95]\n"
"v_mfma_f32_16x16x32_bf16 v[164:167], a[32:35], v[136:139], 0\n"
"v_cndmask_b32_e64 v155, 0, v155, s[94:95]\n"
"v_mfma_f32_16x16x32_bf16 v[164:167], a[36:39], v[44:47], v[164:167]\n"
"s_add_u32 s97, s64, 0x6000\n"
"s_waitcnt lgkmcnt(1)\n"
"v_mfma_f32_16x16x32_bf16 v[160:163], v[120:123], v[152:155], v[160:163]\n"
"buffer_store_dwordx4 v[120:123], v0, s[4:7], s22 offen offset:1024 sc1\n"
"v_mfma_f32_16x16x32_bf16 v[164:167], a[40:43], v[224:227], v[164:167]\n"
"v_mfma_f32_16x16x32_bf16 v[164:167], a[44:47], v[132:135], v[164:167]\n"
"buffer_load_dwordx4 a[148:151], v1, s[4:7], s96 offen offset:1024 sc1\n"
"v_mfma_f32_16x16x32_bf16 v[164:167], a[48:51], v[116:119], v[164:167]\n"
"buffer_load_dwordx4 v[120:123], v33, s[4:7], s98 offen sc1\n"
"v_mfma_f32_16x16x32_bf16 v[164:167], a[52:55], v[144:147], v[164:167]\n"
"ds_write_b128 v3, v[24:27] offset:6144\n"
"v_mfma_f32_16x16x32_bf16 v[164:167], a[56:59], v[220:223], v[164:167]\n"
"buffer_store_dwordx4 v[40:43], v7, s[4:7], s97 offen offset:1024 sc1\n"
"v_mfma_f32_16x16x32_bf16 v[164:167], a[60:63], v[60:63], v[164:167]\n"
"buffer_store_dwordx4 v[160:163], v39, s[4:7], s23 offen sc1\n"
"v_mfma_f32_16x16x32_bf16 v[24:27], a[64:67], v[136:139], 0\n"
"v_mfma_f32_16x16x32_bf16 v[24:27], a[68:71], v[44:47], v[24:27]\n"
"buffer_load_dwordx4 v[36:39], v33, s[4:7], s98 offen offset:1024 sc1\n"
"v_mfma_f32_16x16x32_bf16 v[24:27], a[72:75], v[224:227], v[24:27]\n"
"v_mfma_f32_16x16x32_bf16 v[24:27], a[76:79], v[132:135], v[24:27]\n"
"v_mfma_f32_16x16x32_bf16 v[24:27], a[80:83], v[116:119], v[24:27]\n"
"v_mfma_f32_16x16x32_bf16 v[24:27], a[84:87], v[144:147], v[24:27]\n"
"ds_write_b128 v3, v[164:167] offset:7168\n"
"v_mfma_f32_16x16x32_bf16 v[24:27], a[88:91], v[220:223], v[24:27]\n"
"buffer_load_dwordx4 v[40:43], v34, s[4:7], s98 offen sc1\n"
"v_mfma_f32_16x16x32_bf16 v[24:27], a[92:95], v[60:63], v[24:27]\n"
"buffer_load_dwordx4 a[152:155], v1, s[4:7], s96 offen offset:2048 sc1\n"
"v_mfma_f32_16x16x32_bf16 v[152:155], a[96:99], v[136:139], 0\n"
"v_mfma_f32_16x16x32_bf16 v[152:155], a[100:103], v[44:47], v[152:155]\n"
"v_mfma_f32_16x16x32_bf16 v[152:155], a[104:107], v[224:227], v[152:155]\n"
"v_mfma_f32_16x16x32_bf16 v[152:155], a[108:111], v[132:135], v[152:155]\n"
"buffer_load_dwordx4 v[160:163], v34, s[4:7], s98 offen offset:1024 sc1\n"
"v_mfma_f32_16x16x32_bf16 v[152:155], a[112:115], v[116:119], v[152:155]\n"
"s_add_u32 s97, s51, 0\n"
"s_bitcmp1_b32 s26, 0\n"
"s_cbranch_scc1 .Lmk_w7s\n"
"buffer_store_dwordx4 a[0:3], v0, s[4:7], s97 offen sc1\n"
".Lmk_w7r:\n"
"v_mfma_f32_16x16x32_bf16 v[152:155], a[116:119], v[144:147], v[152:155]\n"
"ds_write_b128 v3, v[24:27] offset:8192\n"
"v_mfma_f32_16x16x32_bf16 v[152:155], a[120:123], v[220:223], v[152:155]\n"
"v_mfma_f32_16x16x32_bf16 v[152:155], a[124:127], v[60:63], v[152:155]\n"
"v_mfma_f32_16x16x32_bf16 v[24:27], v[28:31], v[136:139], 0\n"
"buffer_load_dwordx4 v[164:167], v35, s[4:7], s98 offen sc1\n"
"v_mfma_f32_16x16x32_bf16 v[24:27], v[52:55], v[44:47], v[24:27]\n"
"buffer_load_dwordx4 a[156:159], v1, s[4:7], s96 offen offset:3072 sc1\n"
"v_mfma_f32_16x16x32_bf16 v[24:27], v[84:87], v[224:227], v[24:27]\n"
"v_mfma_f32_16x16x32_bf16 v[24:27], v[92:95], v[132:135], v[24:27]\n"
"v_mfma_f32_16x16x32_bf16 v[24:27], v[100:103], v[116:119], v[24:27]\n"
"s_waitcnt vmcnt(18)\n"
"v_add_f32_e32 v255, v255, v250\n"
"v_add_f32_e32 v254, v254, v253\n"
"v_mfma_f32_16x16x32_bf16 v[24:27], v[108:111], v[144:147], v[24:27]\n"
"buffer_load_dwordx4 v[172:175], v35, s[4:7], s98 offen offset:1024 sc1\n"
"v_mfma_f32_16x16x32_bf16 v[24:27], v[180:183], v[220:223], v[24:27]\n"
"ds_write_b128 v3, v[152:155] offset:9216\n"
"v_mfma_f32_16x16x32_bf16 v[24:27], v[188:191], v[60:63], v[24:27]\n"
"v_add_f32_e32 v252, v252, v249\n"
"v_add_f32_e32 v251, v251, v248\n"
"v_mfma_f32_16x16x32_bf16 v[152:155], v[48:51], v[136:139], 0\n"
"v_add_f32_e32 v255, v255, v252\n"
"v_add_f32_e32 v254, v254, v251\n"
"v_mfma_f32_16x16x32_bf16 v[152:155], v[56:59], v[44:47], v[152:155]\n"
"s_add_u32 s98, s70, 0x2000\n"
"v_add_f32_e32 v255, v255, v254\n"
"v_mov_b32_e32 v254, v255\n"
"v_mfma_f32_16x16x32_bf16 v[152:155], v[88:91], v[224:227], v[152:155]\n"
"s_nop 0\n"
"v_permlane32_swap_b32_e32 v255, v254\n"
"v_add_f32_e32 v255, v255, v254\n"
"v_mfma_f32_16x16x32_bf16 v[152:155], v[96:99], v[132:135], v[152:155]\n"
"buffer_load_dwordx4 a[160:163], v0, s[4:7], s98 offen sc1\n"
"s_add_u32 s97, s51, 0x2000\n"
"v_mfma_f32_16x16x32_bf16 v[152:155], v[104:107], v[116:119], v[152:155]\n"
"s_bitcmp1_b32 s26, 0\n"
"v_mul_f32_e32 v255, 0x3a800000, v255\n"
"v_mov_b32_e32 v254, v255\n"
"s_cbranch_scc1 .Lmk_w8s\n"
"buffer_store_dwordx4 a[32:35], v0, s[4:7], s97 offen sc1\n"
".Lmk_w8r:\n"
"v_mfma_f32_16x16x32_bf16 v[152:155], v[112:115], v[144:147], v[152:155]\n"
"ds_write_b128 v3, v[24:27] offset:10240\n"
"v_mfma_f32_16x16x32_bf16 v[152:155], v[184:187], v[220:223], v[152:155]\n"
"v_permlane16_swap_b32_e32 v255, v254\n"
"v_cvt_pk_bf16_f32 v253, -v255, 0\n"
"v_mfma_f32_16x16x32_bf16 v[152:155], v[192:195], v[60:63], v[152:155]\n"
"v_fma_f32 v254, -v255, v255, v254\n"
"v_lshlrev_b32_e32 v253, 16, v253\n"
"; @A G1 2.2\n"
"v_mfma_f32_16x16x32_bf16 v[24:27], a[0:3], v[128:131], 0\n"
"v_max_f32_e32 v254, 0, v254\n"
"v_cvt_pk_bf16_f32 v254, v254, 0\n"
"v_mfma_f32_16x16x32_bf16 v[24:27], a[4:7], v[76:79], v[24:27]\n"
"v_lshlrev_b32_e32 v254, 16, v254\n"
"v_add_f32_e32 v254, 0x3727c5ac, v254\n"
"v_mfma_f32_16x16x32_bf16 v[24:27], a[8:11], v[80:83], v[24:27]\n"
"v_cvt_pk_bf16_f32 v254, v254, 0\n"
"v_lshlrev_b32_e32 v254, 16, v254\n"
"v_mfma_f32_16x16x32_bf16 v[24:27], a[12:15], v[148:151], v[24:27]\n"
"s_waitcnt vmcnt(19)\n"
"v_sqrt_f32_e32 v254, v254\n"
"v_mfma_f32_16x16x32_bf16 v[24:27], a[16:19], v[140:143], v[24:27]\n"
"s_mov_b32 s97, .Lmk_z4-.Lmk_g16\n"
"s_getpc_b64 s[84:85]\n"
".Lmk_g16:\n"
"s_add_u32 s84, s84, s97\n"
"s_addc_u32 s85, s85, 0\n"
"s_and_b32 s84, s84, -16\n"
"v_mfma_f32_16x16x32_bf16 v[24:27], a[20:23], v[124:127], v[24:27]\n"
"ds_write_b128 v3, v[152:155] offset:11264\n"
"v_mfma_f32_16x16x32_bf16 v[24:27], a[24:27], v[68:71], v[24:27]\n"
"buffer_load_dwordx4 a[164:167], v0, s[4:7], s98 offen offset:1024 sc1\n"
"s_and_b32 s85, s85, 0xffff\n"
"s_sub_u32 s97, .Lmk_end-.Lmk_g16, s97\n"
"v_mfma_f32_16x16x32_bf16 v[24:27], a[28:31], v[168:171], v[24:27]\n"
"s_min_u32 s86, s97, 0x4000\n"
"s_mov_b32 s87, 0x20000\n"
"v_cvt_pk_bf16_f32 v254, v254, 0\n"
"v_mfma_f32_16x16x32_bf16 v[44:47], a[32:35], v[128:131], 0\n"
"s_mov_b32 m0, 0x27800\n"
"s_nop 0\n"
"buffer_load_dwordx4 v0, s[84:87], s99 offen lds\n"
"v_mfma_f32_16x16x32_bf16 v[44:47], a[36:39], v[76:79], v[44:47]\n"
"buffer_store_dwordx4 v[64:67], v0, s[4:7], s60 offen sc1\n"
"v_lshlrev_b32_e32 v254, 16, v254\n"
"v_mfma_f32_16x16x32_bf16 v[44:47], a[40:43], v[80:83], v[44:47]\n"
"s_add_u32 s97, s52, 0\n"
"buffer_load_dwordx4 v[60:63], v32, s[4:7], s97 offen sc1\n"
"v_mfma_f32_16x16x32_bf16 v[44:47], a[44:47], v[148:151], v[44:47]\n"
"s_waitcnt lgkmcnt(0)\n"
"s_barrier\n"
"v_mfma_f32_16x16x32_bf16 v[44:47], a[48:51], v[140:143], v[44:47]\n"
"v_rcp_f32_e32 v255, v254\n"
"v_mfma_f32_16x16x32_bf16 v[44:47], a[52:55], v[124:127], v[44:47]\n"
"ds_write_b128 v3, v[24:27] offset:12288\n"
"v_mfma_f32_16x16x32_bf16 v[44:47], a[56:59], v[68:71], v[44:47]\n"
"ds_read_b128 v[24:27], v4\n"
"v_mul_f32_e32 v254, 0x3e8293ee, v255\n"
"v_mfma_f32_16x16x32_bf16 v[44:47], a[60:63], v[168:171], v[44:47]\n"
"buffer_load_dwordx4 v[116:119], v32, s[4:7], s97 offen offset:1024 sc1\n"
"v_mfma_f32_16x16x32_bf16 v[132:135], a[64:67], v[128:131], 0\n"
"buffer_load_dwordx4 a[168:171], v0, s[4:7], s98 offen offset:2048 sc1\n"
"v_mfma_f32_16x16x32_bf16 v[132:135], a[68:71], v[76:79], v[132:135]\n"
"ds_read_b128 v[136:139], v9 offset:128\n"
"ds_read_b128 v[144:147], v5\n"
"v_mfma_f32_16x16x32_bf16 v[132:135], a[72:75], v[80:83], v[132:135]\n"
"v_mfma_f32_16x16x32_bf16 v[132:135], a[76:79], v[148:151], v[132:135]\n"
"ds_read_b128 v[152:155], v4 offset:1024\n"
"ds_read_b128 v[176:179], v9 offset:192\n"
"v_mfma_f32_16x16x32_bf16 v[132:135], a[80:83], v[140:143], v[132:135]\n"
"buffer_load_dwordx4 v[196:199], v33, s[4:7], s97 offen sc1\n"
"v_mfma_f32_16x16x32_bf16 v[132:135], a[84:87], v[124:127], v[132:135]\n"
"ds_write_b128 v3, v[44:47] offset:13312\n"
"v_mfma_f32_16x16x32_bf16 v[132:135], a[88:91], v[68:71], v[132:135]\n"
"ds_read_b128 v[44:47], v5 offset:1024\n"
"s_waitcnt lgkmcnt(4)\n"
"v_mfma_f32_16x16x32_bf16 v[132:135], a[92:95], v[168:171], v[132:135]\n"
"v_fmac_f32_e32 v24, v253, v136\n"
"v_fmac_f32_e32 v25, v253, v137\n"
"v_mfma_f32_16x16x32_bf16 v[200:203], a[96:99], v[128:131], 0\n"
"s_add_u32 s96, s60, 0x8000\n"
"buffer_store_dwordx4 v[64:67], v0, s[4:7], s96 offen sc1\n"
"v_fmac_f32_e32 v26, v253, v138\n"
"v_fmac_f32_e32 v27, v253, v139\n"
"v_mfma_f32_16x16x32_bf16 v[200:203], a[100:103], v[76:79], v[200:203]\n"
"buffer_load_dwordx4 v[64:67], v33, s[4:7], s97 offen offset:1024 sc1\n"
"v_mfma_f32_16x16x32_bf16 v[200:203], a[104:107], v[80:83], v[200:203]\n"
"buffer_load_dwordx4 a[172:175], v0, s[4:7], s98 offen offset:3072 sc1\n"
"v_mfma_f32_16x16x32_bf16 v[200:203], a[108:111], v[148:151], v[200:203]\n"
"ds_read_b128 v[136:139], v4 offset:2048\n"
"ds_read_b128 v[204:207], v9 offset:256\n"
"v_mfma_f32_16x16x32_bf16 v[200:203], a[112:115], v[140:143], v[200:203]\n"
"ds_read_b128 v[208:211], v5 offset:2048\n"
"v_add_f32_e32 v24, v24, v144\n"
"v_mfma_f32_16x16x32_bf16 v[200:203], a[116:119], v[124:127], v[200:203]\n"
"ds_write_b128 v3, v[132:135] offset:14336\n"
"v_mfma_f32_16x16x32_bf16 v[200:203], a[120:123], v[68:71], v[200:203]\n"
"buffer_load_dwordx4 v[132:135], v34, s[4:7], s97 offen sc1\n"
"v_mfma_f32_16x16x32_bf16 v[200:203], a[124:127], v[168:171], v[200:203]\n"
"v_add_f32_e32 v25, v25, v145\n"
"v_add_f32_e32 v26, v26, v146\n"
"v_mfma_f32_16x16x32_bf16 v[212:215], v[28:31], v[128:131], 0\n"
"s_waitcnt lgkmcnt(4)\n"
"v_add_f32_e32 v27, v27, v147\n"
"v_fmac_f32_e32 v152, v253, v176\n"
"v_mfma_f32_16x16x32_bf16 v[212:215], v[52:55], v[76:79], v[212:215]\n"
"ds_read_b128 v[144:147], v4 offset:3072\n"
"ds_read_b128 v[216:219], v9 offset:320\n"
"v_mfma_f32_16x16x32_bf16 v[212:215], v[84:87], v[80:83], v[212:215]\n"
"s_waitcnt vmcnt(22)\n"
"s_add_u32 s96, s68, 0x1000\n"
"ds_read_b128 v[220:223], v5 offset:3072\n"
"v_mfma_f32_16x16x32_bf16 v[212:215], v[92:95], v[148:151], v[212:215]\n"
"buffer_load_dwordx4 v[224:227], v34, s[4:7], s97 offen offset:1024 sc1\n"
"v_mfma_f32_16x16x32_bf16 v[212:215], v[100:103], v[140:143], v[212:215]\n"
"buffer_store_dwordx2 v[18:19], v11, s[4:7], s96 offen offset:64 sc1\n"
"v_mfma_f32_16x16x32_bf16 v[212:215], v[108:111], v[124:127], v[212:215]\n"
"ds_write_b128 v3, v[200:203] offset:15360\n"
"v_mfma_f32_16x16x32_bf16 v[212:215], v[180:183], v[68:71], v[212:215]\n"
"buffer_load_dwordx4 a[176:179], v1, s[4:7], s98 offen sc1\n"
"v_mfma_f32_16x16x32_bf16 v[212:215], v[188:191], v[168:171], v[212:215]\n"
"v_fmac_f32_e32 v153, v253, v177\n"
"v_fmac_f32_e32 v154, v253, v178\n"
"v_mfma_f32_16x16x32_bf16 v[200:203], v[48:51], v[128:131], 0\n"
"buffer_load_dwordx4 v[128:131], v35, s[4:7], s97 offen sc1\n"
"v_mfma_f32_16x16x32_bf16 v[200:203], v[56:59], v[76:79], v[200:203]\n"
"ds_read_b128 v[76:79], v4 offset:4096\n"
"ds_read_b128 v[228:231], v9\n"
"v_mfma_f32_16x16x32_bf16 v[200:203], v[88:91], v[80:83], v[200:203]\n"
"ds_read_b128 v[80:83], v5 offset:4096\n"
"v_fmac_f32_e32 v155, v253, v179\n"
"v_mfma_f32_16x16x32_bf16 v[200:203], v[96:99], v[148:151], v[200:203]\n"
"v_add_f32_e32 v152, v152, v44\n"
"v_add_f32_e32 v153, v153, v45\n"
"v_mfma_f32_16x16x32_bf16 v[200:203], v[104:107], v[140:143], v[200:203]\n"
"s_waitcnt lgkmcnt(8)\n"
"v_add_f32_e32 v154, v154, v46\n"
"v_add_f32_e32 v155, v155, v47\n"
"v_mfma_f32_16x16x32_bf16 v[200:203], v[112:115], v[124:127], v[200:203]\n"
"buffer_load_dwordx4 v[44:47], v35, s[4:7], s97 offen offset:1024 sc1\n"
"v_mfma_f32_16x16x32_bf16 v[200:203], v[184:187], v[68:71], v[200:203]\n"
"ds_write_b128 v3, v[212:215] offset:16384\n"
"v_mfma_f32_16x16x32_bf16 v[200:203], v[192:195], v[168:171], v[200:203]\n"
"s_waitcnt vmcnt(25)\n"
"ds_read_b128 v[68:71], v4 offset:5120\n"
"ds_read_b128 v[124:127], v9 offset:64\n"
"; @A G1 2.3\n"
"v_mfma_f32_16x16x32_bf16 v[24:27], a[0:3], v[72:75], v[24:27]\n"
"buffer_load_dwordx4 a[180:183], v1, s[4:7], s98 offen offset:1024 sc1\n"
"v_mfma_f32_16x16x32_bf16 v[24:27], a[4:7], v[156:159], v[24:27]\n"
"ds_read_b128 v[140:143], v5 offset:5120\n"
"v_fmac_f32_e32 v136, v253, v204\n"
"v_mfma_f32_16x16x32_bf16 v[24:27], a[8:11], v[120:123], v[24:27]\n"
"s_add_u32 s97, s53, 0\n"
"buffer_load_dwordx4 v[148:151], v32, s[4:7], s97 offen sc1\n"
"v_mfma_f32_16x16x32_bf16 v[24:27], a[12:15], v[36:39], v[24:27]\n"
"s_waitcnt vmcnt(20)\n"
"v_fmac_f32_e32 v137, v253, v205\n"
"v_fmac_f32_e32 v138, v253, v206\n"
"v_mfma_f32_16x16x32_bf16 v[24:27], a[16:19], v[40:43], v[24:27]\n"
"v_fmac_f32_e32 v139, v253, v207\n"
"v_add_f32_e32 v136, v136, v208\n"
"v_mfma_f32_16x16x32_bf16 v[24:27], a[20:23], v[160:163], v[24:27]\n"
"ds_write_b128 v3, v[200:203] offset:17408\n"
"v_mfma_f32_16x16x32_bf16 v[24:27], a[24:27], v[164:167], v[24:27]\n"
"v_add_f32_e32 v137, v137, v209\n"
"v_add_f32_e32 v138, v138, v210\n"
"v_mfma_f32_16x16x32_bf16 v[24:27], a[28:31], v[172:175], v[24:27]\n"
"buffer_load_dwordx4 v[168:171], v32, s[4:7], s97 offen offset:1024 sc1\n"
"v_mfma_f32_16x16x32_bf16 v[152:155], a[32:35], v[72:75], v[152:155]\n"
"s_waitcnt lgkmcnt(9)\n"
"v_add_f32_e32 v139, v139, v211\n"
"v_fmac_f32_e32 v144, v253, v216\n"
"v_mfma_f32_16x16x32_bf16 v[152:155], a[36:39], v[156:159], v[152:155]\n"
"v_fmac_f32_e32 v145, v253, v217\n"
"v_fmac_f32_e32 v146, v253, v218\n"
"v_mfma_f32_16x16x32_bf16 v[152:155], a[40:43], v[120:123], v[152:155]\n"
"s_waitcnt lgkmcnt(0)\n"
"s_barrier\n"
"v_mfma_f32_16x16x32_bf16 v[152:155], a[44:47], v[36:39], v[152:155]\n"
"buffer_load_dwordx4 a[184:187], v1, s[4:7], s98 offen offset:2048 sc1\n"
"v_mfma_f32_16x16x32_bf16 v[152:155], a[48:51], v[40:43], v[152:155]\n"
"buffer_load_dwordx4 v[176:179], v33, s[4:7], s97 offen sc1\n"
"v_mfma_f32_16x16x32_bf16 v[152:155], a[52:55], v[160:163], v[152:155]\n"
"ds_read_b128 v[200:203], v6\n"
"ds_read_b128 v[204:207], v6 offset:1024\n"
"v_mfma_f32_16x16x32_bf16 v[152:155], a[56:59], v[164:167], v[152:155]\n"
"v_fmac_f32_e32 v147, v253, v219\n"
"v_add_f32_e32 v144, v144, v220\n"
"v_mfma_f32_16x16x32_bf16 v[152:155], a[60:63], v[172:175], v[152:155]\n"
"v_add_f32_e32 v145, v145, v221\n"
"v_add_f32_e32 v146, v146, v222\n"
"v_mfma_f32_16x16x32_bf16 v[136:139], a[64:67], v[72:75], v[136:139]\n"
"v_add_f32_e32 v147, v147, v223\n"
"v_fmac_f32_e32 v76, v253, v228\n"
"v_mfma_f32_16x16x32_bf16 v[136:139], a[68:71], v[156:159], v[136:139]\n"
"buffer_load_dwordx4 v[208:211], v33, s[4:7], s97 offen offset:1024 sc1\n"
"v_mfma_f32_16x16x32_bf16 v[136:139], a[72:75], v[120:123], v[136:139]\n"
"v_fmac_f32_e32 v77, v253, v229\n"
"v_fmac_f32_e32 v78, v253, v230\n"
"v_mfma_f32_16x16x32_bf16 v[136:139], a[76:79], v[36:39], v[136:139]\n"
"v_fmac_f32_e32 v79, v253, v231\n"
"v_add_f32_e32 v76, v76, v80\n"
"v_mfma_f32_16x16x32_bf16 v[136:139], a[80:83], v[40:43], v[136:139]\n"
"s_waitcnt lgkmcnt(0)\n"
"v_add_f32_e32 v77, v77, v81\n"
"v_add_f32_e32 v78, v78, v82\n"
"v_mfma_f32_16x16x32_bf16 v[136:139], a[84:87], v[160:163], v[136:139]\n"
"buffer_load_dwordx4 a[188:191], v1, s[4:7], s98 offen offset:3072 sc1\n"
"v_mfma_f32_16x16x32_bf16 v[136:139], a[88:91], v[164:167], v[136:139]\n"
"buffer_load_dwordx4 v[212:215], v34, s[4:7], s97 offen sc1\n"
"v_mfma_f32_16x16x32_bf16 v[136:139], a[92:95], v[172:175], v[136:139]\n"
"ds_read_b128 v[216:219], v6 offset:2048\n"
"ds_read_b128 v[220:223], v6 offset:3072\n"
"v_mfma_f32_16x16x32_bf16 v[144:147], a[96:99], v[72:75], v[144:147]\n"
"v_add_f32_e32 v79, v79, v83\n"
"v_fmac_f32_e32 v68, v253, v124\n"
"v_mfma_f32_16x16x32_bf16 v[144:147], a[100:103], v[156:159], v[144:147]\n"
"v_fmac_f32_e32 v69, v253, v125\n"
"v_fmac_f32_e32 v70, v253, v126\n"
"v_mfma_f32_16x16x32_bf16 v[144:147], a[104:107], v[120:123], v[144:147]\n"
"s_waitcnt vmcnt(24)\n"
"s_add_u32 s98, s68, 0x1000\n"
"buffer_store_dwordx2 v[18:19], v11, s[4:7], s98 offen offset:96 sc1\n"
"v_fmac_f32_e32 v71, v253, v127\n"
"v_add_f32_e32 v68, v68, v140\n"
"v_mfma_f32_16x16x32_bf16 v[144:147], a[108:111], v[36:39], v[144:147]\n"
"buffer_load_dwordx4 v[80:83], v34, s[4:7], s97 offen offset:1024 sc1\n"
"v_mfma_f32_16x16x32_bf16 v[144:147], a[112:115], v[40:43], v[144:147]\n"
"v_add_f32_e32 v69, v69, v141\n"
"v_add_f32_e32 v70, v70, v142\n"
"v_mfma_f32_16x16x32_bf16 v[144:147], a[116:119], v[160:163], v[144:147]\n"
"s_add_u32 s98, s70, 0x4000\n"
"v_add_f32_e32 v71, v71, v143\n"
"v_add_f32_e32 v24, v24, v200\n"
"v_mfma_f32_16x16x32_bf16 v[144:147], a[120:123], v[164:167], v[144:147]\n"
"v_add_f32_e32 v25, v25, v201\n"
"v_add_f32_e32 v26, v26, v202\n"
"v_mfma_f32_16x16x32_bf16 v[144:147], a[124:127], v[172:175], v[144:147]\n"
"buffer_load_dwordx4 a[192:195], v0, s[4:7], s98 offen sc1\n"
"v_mfma_f32_16x16x32_bf16 v[76:79], v[28:31], v[72:75], v[76:79]\n"
"buffer_load_dwordx4 v[124:127], v35, s[4:7], s97 offen sc1\n"
"v_mfma_f32_16x16x32_bf16 v[76:79], v[52:55], v[156:159], v[76:79]\n"
"v_add_f32_e32 v27, v27, v203\n"
"v_add_f32_e32 v152, v152, v204\n"
"v_mfma_f32_16x16x32_bf16 v[76:79], v[84:87], v[120:123], v[76:79]\n"
"v_add_f32_e32 v153, v153, v205\n"
"v_add_f32_e32 v154, v154, v206\n"
"v_mfma_f32_16x16x32_bf16 v[76:79], v[92:95], v[36:39], v[76:79]\n"
"v_add_f32_e32 v155, v155, v207\n"
"v_mul_f32_e32 v24, v24, v255\n"
"v_mfma_f32_16x16x32_bf16 v[76:79], v[100:103], v[40:43], v[76:79]\n"
"s_waitcnt lgkmcnt(0)\n"
"v_mul_f32_e32 v25, v25, v255\n"
"v_mul_f32_e32 v26, v26, v255\n"
"v_mfma_f32_16x16x32_bf16 v[76:79], v[108:111], v[160:163], v[76:79]\n"
"buffer_load_dwordx4 v[140:143], v35, s[4:7], s97 offen offset:1024 sc1\n"
"v_mfma_f32_16x16x32_bf16 v[76:79], v[180:183], v[164:167], v[76:79]\n"
"ds_read_b128 v[200:203], v6 offset:4096\n"
"ds_read_b128 v[204:207], v6 offset:5120\n"
"v_mfma_f32_16x16x32_bf16 v[76:79], v[188:191], v[172:175], v[76:79]\n"
"v_mul_f32_e32 v27, v27, v255\n"
"v_mul_f32_e32 v152, v152, v255\n"
"v_mfma_f32_16x16x32_bf16 v[68:71], v[48:51], v[72:75], v[68:71]\n"
"v_mul_f32_e32 v153, v153, v255\n"
"v_mul_f32_e32 v154, v154, v255\n"
"v_mfma_f32_16x16x32_bf16 v[68:71], v[56:59], v[156:159], v[68:71]\n"
"buffer_load_dwordx4 a[196:199], v0, s[4:7], s98 offen offset:1024 sc1\n"
"v_mfma_f32_16x16x32_bf16 v[68:71], v[88:91], v[120:123], v[68:71]\n"
"v_mul_f32_e32 v155, v155, v255\n"
"v_cvt_pk_bf16_f32 v72, v24, v25\n"
"v_mfma_f32_16x16x32_bf16 v[68:71], v[96:99], v[36:39], v[68:71]\n"
"v_cvt_pk_bf16_f32 v73, v26, v27\n"
"v_cvt_pk_bf16_f32 v74, v152, v153\n"
"v_mfma_f32_16x16x32_bf16 v[68:71], v[104:107], v[40:43], v[68:71]\n"
"v_cvt_pk_bf16_f32 v75, v154, v155\n"
"v_add_f32_e32 v136, v136, v216\n"
"v_mfma_f32_16x16x32_bf16 v[68:71], v[112:115], v[160:163], v[68:71]\n"
"ds_write_b128 v8, v[72:75] offset:17408\n"
"v_mfma_f32_16x16x32_bf16 v[68:71], v[184:187], v[164:167], v[68:71]\n"
"v_add_f32_e32 v137, v137, v217\n"
"v_add_f32_e32 v138, v138, v218\n"
"v_mfma_f32_16x16x32_bf16 v[68:71], v[192:195], v[172:175], v[68:71]\n"
"v_add_f32_e32 v139, v139, v219\n"
"v_add_f32_e32 v144, v144, v220\n"
"v_add_f32_e32 v145, v145, v221\n"
"v_add_f32_e32 v146, v146, v222\n"
"v_add_f32_e32 v147, v147, v223\n"
"v_mul_f32_e32 v136, v136, v255\n"
"v_mul_f32_e32 v137, v137, v255\n"
"v_mul_f32_e32 v138, v138, v255\n"
"v_mul_f32_e32 v139, v139, v255\n"
"v_mul_f32_e32 v144, v144, v255\n"
"v_mul_f32_e32 v145, v145, v255\n"
"v_mul_f32_e32 v146, v146, v255\n"
"v_mul_f32_e32 v147, v147, v255\n"
"v_cvt_pk_bf16_f32 v24, v136, v137\n"
"v_cvt_pk_bf16_f32 v25, v138, v139\n"
"v_cvt_pk_bf16_f32 v26, v144, v145\n"
"v_cvt_pk_bf16_f32 v27, v146, v147\n"
"ds_write_b128 v7, v[24:27] offset:50176\n"
".Lmk_z3:\n"
"s_waitcnt vmcnt(21)\n"
"; @A G1 3.0\n"
"v_mfma_f32_16x16x32_bf16 v[36:39], a[0:3], v[60:63], 0\n"
"v_mfma_f32_16x16x32_bf16 v[36:39], a[4:7], v[116:119], v[36:39]\n"
"s_add_u32 s97, s62, 0\n"
"v_mfma_f32_16x16x32_bf16 v[36:39], a[8:11], v[196:199], v[36:39]\n"
"s_add_u32 s96, s54, 0\n"
"buffer_load_dwordx4 v[40:43], v32, s[4:7], s96 offen sc1\n"
"v_mfma_f32_16x16x32_bf16 v[36:39], a[12:15], v[64:67], v[36:39]\n"
"s_waitcnt vmcnt(15)\n"
"v_mfma_f32_16x16x32_bf16 v[36:39], a[16:19], v[132:135], v[36:39]\n"
"buffer_load_dword v255, v10, s[4:7], s97 offen sc1\n"
"v_mfma_f32_16x16x32_bf16 v[36:39], a[20:23], v[224:227], v[36:39]\n"
"s_waitcnt lgkmcnt(2)\n"
"v_add_f32_e32 v76, v76, v200\n"
"v_add_f32_e32 v77, v77, v201\n"
"v_mfma_f32_16x16x32_bf16 v[36:39], a[24:27], v[128:131], v[36:39]\n"
"v_add_f32_e32 v78, v78, v202\n"
"v_add_f32_e32 v79, v79, v203\n"
"v_mfma_f32_16x16x32_bf16 v[36:39], a[28:31], v[44:47], v[36:39]\n"
"buffer_load_dwordx4 v[120:123], v32, s[4:7], s96 offen offset:1024 sc1\n"
"v_mfma_f32_16x16x32_bf16 v[136:139], a[32:35], v[60:63], 0\n"
"buffer_load_dwordx4 a[200:203], v0, s[4:7], s98 offen offset:2048 sc1\n"
"v_mfma_f32_16x16x32_bf16 v[136:139], a[36:39], v[116:119], v[136:139]\n"
"buffer_load_dword v253, v10, s[4:7], s97 offen offset:256 sc1\n"
"v_mfma_f32_16x16x32_bf16 v[136:139], a[40:43], v[196:199], v[136:139]\n"
"v_add_f32_e32 v68, v68, v204\n"
"v_add_f32_e32 v69, v69, v205\n"
"v_mfma_f32_16x16x32_bf16 v[136:139], a[44:47], v[64:67], v[136:139]\n"
"v_add_f32_e32 v70, v70, v206\n"
"v_add_f32_e32 v71, v71, v207\n"
"v_mfma_f32_16x16x32_bf16 v[136:139], a[48:51], v[132:135], v[136:139]\n"
"buffer_load_dwordx4 v[144:147], v33, s[4:7], s96 offen sc1\n"
"v_mfma_f32_16x16x32_bf16 v[136:139], a[52:55], v[224:227], v[136:139]\n"
"ds_write_b128 v3, v[36:39]\n"
"v_mfma_f32_16x16x32_bf16 v[136:139], a[56:59], v[128:131], v[136:139]\n"
"buffer_load_dword v252, v10, s[4:7], s97 offen offset:512 sc1\n"
"v_mfma_f32_16x16x32_bf16 v[136:139], a[60:63], v[44:47], v[136:139]\n"
"v_mul_f32_e32 v76, v76, v254\n"
"v_mul_f32_e32 v77, v77, v254\n"
"v_mfma_f32_16x16x32_bf16 v[36:39], a[64:67], v[60:63], 0\n"
"v_mul_f32_e32 v78, v78, v254\n"
"v_mul_f32_e32 v79, v79, v254\n"
"v_mfma_f32_16x16x32_bf16 v[36:39], a[68:71], v[116:119], v[36:39]\n"
"buffer_load_dwordx4 v[152:155], v33, s[4:7], s96 offen offset:1024 sc1\n"
"v_mfma_f32_16x16x32_bf16 v[36:39], a[72:75], v[196:199], v[36:39]\n"
"buffer_load_dword v251, v10, s[4:7], s97 offen offset:768 sc1\n"
"v_mfma_f32_16x16x32_bf16 v[36:39], a[76:79], v[64:67], v[36:39]\n"
"v_mul_f32_e32 v68, v68, v254\n"
"v_mul_f32_e32 v69, v69, v254\n"
"v_mfma_f32_16x16x32_bf16 v[36:39], a[80:83], v[132:135], v[36:39]\n"
"v_mul_f32_e32 v70, v70, v254\n"
"v_mul_f32_e32 v71, v71, v254\n"
"v_mfma_f32_16x16x32_bf16 v[36:39], a[84:87], v[224:227], v[36:39]\n"
"ds_write_b128 v3, v[136:139] offset:1024\n"
"v_mfma_f32_16x16x32_bf16 v[36:39], a[88:91], v[128:131], v[36:39]\n"
"buffer_load_dwordx4 v[136:139], v34, s[4:7], s96 offen sc1\n"
"v_mfma_f32_16x16x32_bf16 v[36:39], a[92:95], v[44:47], v[36:39]\n"
"buffer_load_dword v254, v10, s[4:7], s97 offen offset:1024 sc1\n"
"v_mfma_f32_16x16x32_bf16 v[156:159], a[96:99], v[60:63], 0\n"
"v_cvt_pk_bf16_f32 v160, v76, v77\n"
"v_cvt_pk_bf16_f32 v161, v78, v79\n"
"v_mfma_f32_16x16x32_bf16 v[156:159], a[100:103], v[116:119], v[156:159]\n"
"v_cvt_pk_bf16_f32 v162, v68, v69\n"
"v_cvt_pk_bf16_f32 v163, v70, v71\n"
"v_mfma_f32_16x16x32_bf16 v[156:159], a[104:107], v[196:199], v[156:159]\n"
"buffer_load_dwordx4 a[204:207], v0, s[4:7], s98 offen offset:3072 sc1\n"
"v_mfma_f32_16x16x32_bf16 v[156:159], a[108:111], v[64:67], v[156:159]\n"
"buffer_load_dwordx4 v[68:71], v34, s[4:7], s96 offen offset:1024 sc1\n"
"v_mfma_f32_16x16x32_bf16 v[156:159], a[112:115], v[132:135], v[156:159]\n"
"buffer_load_dword v250, v10, s[4:7], s97 offen offset:1280 sc1\n"
"v_mfma_f32_16x16x32_bf16 v[156:159], a[116:119], v[224:227], v[156:159]\n"
"ds_write_b128 v3, v[36:39] offset:2048\n"
"v_mfma_f32_16x16x32_bf16 v[156:159], a[120:123], v[128:131], v[156:159]\n"
"ds_write_b128 v2, v[160:163] offset:17408\n"
"v_mfma_f32_16x16x32_bf16 v[156:159], a[124:127], v[44:47], v[156:159]\n"
"buffer_load_dword v249, v10, s[4:7], s97 offen offset:1536 sc1\n"
"v_mfma_f32_16x16x32_bf16 v[36:39], v[28:31], v[60:63], 0\n"
"buffer_load_dwordx4 v[76:79], v35, s[4:7], s96 offen sc1\n"
"v_mfma_f32_16x16x32_bf16 v[36:39], v[52:55], v[116:119], v[36:39]\n"
"buffer_store_dwordx4 v[72:75], v8, s[4:7], s64 offen offset:1024 sc1\n"
"v_mfma_f32_16x16x32_bf16 v[36:39], v[84:87], v[196:199], v[36:39]\n"
"buffer_load_dword v248, v10, s[4:7], s97 offen offset:1792 sc1\n"
"v_mfma_f32_16x16x32_bf16 v[36:39], v[92:95], v[64:67], v[36:39]\n"
"v_mfma_f32_16x16x32_bf16 v[36:39], v[100:103], v[132:135], v[36:39]\n"
"v_mfma_f32_16x16x32_bf16 v[36:39], v[108:111], v[224:227], v[36:39]\n"
"buffer_load_dwordx4 v[72:75], v35, s[4:7], s96 offen offset:1024 sc1\n"
"v_mfma_f32_16x16x32_bf16 v[36:39], v[180:183], v[128:131], v[36:39]\n"
"ds_write_b128 v3, v[156:159] offset:3072\n"
"v_mfma_f32_16x16x32_bf16 v[36:39], v[188:191], v[44:47], v[36:39]\n"
"v_mfma_f32_16x16x32_bf16 v[156:159], v[48:51], v[60:63], 0\n"
"v_mfma_f32_16x16x32_bf16 v[156:159], v[56:59], v[116:119], v[156:159]\n"
"v_mfma_f32_16x16x32_bf16 v[156:159], v[88:91], v[196:199], v[156:159]\n"
"v_mfma_f32_16x16x32_bf16 v[156:159], v[96:99], v[64:67], v[156:159]\n"
"v_mfma_f32_16x16x32_bf16 v[156:159], v[104:107], v[132:135], v[156:159]\n"
"buffer_load_dwordx4 a[208:211], v1, s[4:7], s98 offen sc1\n"
"v_mfma_f32_16x16x32_bf16 v[156:159], v[112:115], v[224:227], v[156:159]\n"
"s_nop 0\n"
"ds_write_b128 v3, v[36:39] offset:4096\n"
"v_mfma_f32_16x16x32_bf16 v[156:159], v[184:187], v[128:131], v[156:159]\n"
"v_mfma_f32_16x16x32_bf16 v[156:159], v[192:195], v[44:47], v[156:159]\n"
"s_waitcnt vmcnt(28)\n"
"; @A G1 3.1\n"
"v_mfma_f32_16x16x32_bf16 v[36:39], a[0:3], v[148:151], 0\n"
"v_mov_b32_e32 v44, -1\n"
"v_mov_b32_e32 v45, -1\n"
"v_mfma_f32_16x16x32_bf16 v[36:39], a[4:7], v[168:171], v[36:39]\n"
"v_mov_b32_e32 v46, -1\n"
"v_mov_b32_e32 v47, -1\n"
"v_mfma_f32_16x16x32_bf16 v[36:39], a[8:11], v[176:179], v[36:39]\n"
"s_add_u32 s97, s55, 0\n"
"buffer_load_dwordx4 v[60:63], v32, s[4:7], s97 offen sc1\n"
"v_mfma_f32_16x16x32_bf16 v[36:39], a[12:15], v[208:211], v[36:39]\n"
"s_waitcnt vmcnt(22)\n"
"s_add_u32 s96, s51, 0x4000\n"
"s_bitcmp1_b32 s26, 0\n"
"s_cbranch_scc1 .Lmk_w9s\n"
"buffer_store_dwordx4 a[64:67], v0, s[4:7], s96 offen sc1\n"
".Lmk_w9r:\n"
"v_mfma_f32_16x16x32_bf16 v[36:39], a[16:19], v[212:215], v[36:39]\n"
"v_mfma_f32_16x16x32_bf16 v[36:39], a[20:23], v[80:83], v[36:39]\n"
"ds_write_b128 v3, v[156:159] offset:5120\n"
"v_mfma_f32_16x16x32_bf16 v[36:39], a[24:27], v[124:127], v[36:39]\n"
"v_mfma_f32_16x16x32_bf16 v[36:39], a[28:31], v[140:143], v[36:39]\n"
"buffer_load_dwordx4 v[64:67], v32, s[4:7], s97 offen offset:1024 sc1\n"
"s_add_u32 s96, s64, 0x4000\n"
"v_mfma_f32_16x16x32_bf16 v[116:119], a[32:35], v[148:151], 0\n"
"v_mfma_f32_16x16x32_bf16 v[116:119], a[36:39], v[168:171], v[116:119]\n"
"buffer_store_dwordx4 v[24:27], v7, s[4:7], s96 offen offset:1024 sc1\n"
"v_mfma_f32_16x16x32_bf16 v[116:119], a[40:43], v[176:179], v[116:119]\n"
"v_mfma_f32_16x16x32_bf16 v[116:119], a[44:47], v[208:211], v[116:119]\n"
"v_mfma_f32_16x16x32_bf16 v[116:119], a[48:51], v[212:215], v[116:119]\n"
"buffer_load_dwordx4 v[24:27], v33, s[4:7], s97 offen sc1\n"
"s_add_u32 s96, s51, 0x6000\n"
"v_mfma_f32_16x16x32_bf16 v[116:119], a[52:55], v[80:83], v[116:119]\n"
"ds_write_b128 v3, v[36:39] offset:6144\n"
"v_mfma_f32_16x16x32_bf16 v[116:119], a[56:59], v[124:127], v[116:119]\n"
"buffer_load_dwordx4 a[212:215], v1, s[4:7], s98 offen offset:1024 sc1\n"
"s_bitcmp1_b32 s26, 0\n"
"s_cbranch_scc1 .Lmk_w10s\n"
"buffer_store_dwordx4 a[96:99], v0, s[4:7], s96 offen sc1\n"
".Lmk_w10r:\n"
"v_mfma_f32_16x16x32_bf16 v[116:119], a[60:63], v[140:143], v[116:119]\n"
"v_mfma_f32_16x16x32_bf16 v[36:39], a[64:67], v[148:151], 0\n"
"v_mfma_f32_16x16x32_bf16 v[36:39], a[68:71], v[168:171], v[36:39]\n"
"buffer_load_dwordx4 v[128:131], v33, s[4:7], s97 offen offset:1024 sc1\n"
"v_mfma_f32_16x16x32_bf16 v[36:39], a[72:75], v[176:179], v[36:39]\n"
"v_mfma_f32_16x16x32_bf16 v[36:39], a[76:79], v[208:211], v[36:39]\n"
"v_mfma_f32_16x16x32_bf16 v[36:39], a[80:83], v[212:215], v[36:39]\n"
"v_mfma_f32_16x16x32_bf16 v[36:39], a[84:87], v[80:83], v[36:39]\n"
"s_nop 0\n"
"ds_write_b128 v3, v[116:119] offset:7168\n"
"v_mfma_f32_16x16x32_bf16 v[36:39], a[88:91], v[124:127], v[36:39]\n"
"buffer_load_dwordx4 v[116:119], v34, s[4:7], s97 offen sc1\n"
"v_mfma_f32_16x16x32_bf16 v[36:39], a[92:95], v[140:143], v[36:39]\n"
"v_mfma_f32_16x16x32_bf16 v[132:135], a[96:99], v[148:151], 0\n"
"s_add_u32 s96, s51, 0x8000\n"
"s_bitcmp1_b32 s26, 0\n"
"s_cbranch_scc1 .Lmk_w11s\n"
"buffer_store_dwordx4 v[28:31], v0, s[4:7], s96 offen sc1\n"
".Lmk_w11r:\n"
"v_mfma_f32_16x16x32_bf16 v[132:135], a[100:103], v[168:171], v[132:135]\n"
"v_mfma_f32_16x16x32_bf16 v[132:135], a[104:107], v[176:179], v[132:135]\n"
"v_mfma_f32_16x16x32_bf16 v[132:135], a[108:111], v[208:211], v[132:135]\n"
"buffer_load_dwordx4 v[156:159], v34, s[4:7], s97 offen offset:1024 sc1\n"
"v_mfma_f32_16x16x32_bf16 v[132:135], a[112:115], v[212:215], v[132:135]\n"
"v_mfma_f32_16x16x32_bf16 v[132:135], a[116:119], v[80:83], v[132:135]\n"
"ds_write_b128 v3, v[36:39] offset:8192\n"
"v_mfma_f32_16x16x32_bf16 v[132:135], a[120:123], v[124:127], v[132:135]\n"
"v_mfma_f32_16x16x32_bf16 v[132:135], a[124:127], v[140:143], v[132:135]\n"
"v_mfma_f32_16x16x32_bf16 v[36:39], v[28:31], v[148:151], 0\n"
"buffer_load_dwordx4 v[160:163], v35, s[4:7], s97 offen sc1\n"
"v_mfma_f32_16x16x32_bf16 v[36:39], v[52:55], v[168:171], v[36:39]\n"
"buffer_load_dwordx4 a[216:219], v1, s[4:7], s98 offen offset:2048 sc1\n"
"v_mfma_f32_16x16x32_bf16 v[36:39], v[84:87], v[176:179], v[36:39]\n"
"v_mfma_f32_16x16x32_bf16 v[36:39], v[92:95], v[208:211], v[36:39]\n"
"v_mfma_f32_16x16x32_bf16 v[36:39], v[100:103], v[212:215], v[36:39]\n"
"s_waitcnt vmcnt(15)\n"
"v_add_f32_e32 v255, v255, v254\n"
"v_add_f32_e32 v253, v253, v250\n"
"s_add_u32 s96, s51, 0xa000\n"
"v_mfma_f32_16x16x32_bf16 v[36:39], v[108:111], v[80:83], v[36:39]\n"
"buffer_load_dwordx4 v[164:167], v35, s[4:7], s97 offen offset:1024 sc1\n"
"s_bitcmp1_b32 s26, 0\n"
"s_cbranch_scc1 .Lmk_w12s\n"
"buffer_store_dwordx4 v[48:51], v0, s[4:7], s96 offen sc1\n"
".Lmk_w12r:\n"
"v_mfma_f32_16x16x32_bf16 v[36:39], v[180:183], v[124:127], v[36:39]\n"
"ds_write_b128 v3, v[132:135] offset:9216\n"
"v_mfma_f32_16x16x32_bf16 v[36:39], v[188:191], v[140:143], v[36:39]\n"
"s_add_u32 s97, s75, 0x1000\n"
"buffer_load_dwordx2 v[246:247], v13, s[4:7], s97 offen sc1\n"
"v_mfma_f32_16x16x32_bf16 v[32:35], v[48:51], v[148:151], 0\n"
"v_add_f32_e32 v252, v252, v249\n"
"v_add_f32_e32 v251, v251, v248\n"
"v_mfma_f32_16x16x32_bf16 v[32:35], v[56:59], v[168:171], v[32:35]\n"
"v_add_f32_e32 v255, v255, v252\n"
"v_add_f32_e32 v253, v253, v251\n"
"v_mfma_f32_16x16x32_bf16 v[32:35], v[88:91], v[176:179], v[32:35]\n"
"s_add_u32 s96, s71, 0\n"
"buffer_load_dwordx2 v[250:251], off, s[4:7], s96 sc1\n"
"v_mfma_f32_16x16x32_bf16 v[32:35], v[96:99], v[208:211], v[32:35]\n"
"v_add_f32_e32 v255, v255, v253\n"
"v_mov_b32_e32 v253, v255\n"
"v_mfma_f32_16x16x32_bf16 v[32:35], v[104:107], v[212:215], v[32:35]\n"
"s_nop 0\n"
"v_permlane32_swap_b32_e32 v255, v253\n"
"v_add_f32_e32 v255, v255, v253\n"
"v_mfma_f32_16x16x32_bf16 v[32:35], v[112:115], v[80:83], v[32:35]\n"
"ds_write_b128 v3, v[36:39] offset:10240\n"
"v_mfma_f32_16x16x32_bf16 v[32:35], v[184:187], v[124:127], v[32:35]\n"
"v_mul_f32_e32 v255, 0x3a800000, v255\n"
"v_mov_b32_e32 v253, v255\n"
"v_mfma_f32_16x16x32_bf16 v[32:35], v[192:195], v[140:143], v[32:35]\n"
"s_nop 0\n"
"v_permlane16_swap_b32_e32 v255, v253\n"
"v_cvt_pk_bf16_f32 v254, -v255, 0\n"
"; @A G1 3.2\n"
"v_mfma_f32_16x16x32_bf16 v[36:39], a[0:3], v[40:43], 0\n"
"s_add_u32 s95, s61, 0x440000\n"
"buffer_store_dwordx4 v[44:47], v0, s[4:7], s95 offen sc1\n"
"v_fma_f32 v253, -v255, v255, v253\n"
"v_lshlrev_b32_e32 v254, 16, v254\n"
"v_mfma_f32_16x16x32_bf16 v[36:39], a[4:7], v[120:123], v[36:39]\n"
"v_max_f32_e32 v253, 0, v253\n"
"v_cvt_pk_bf16_f32 v253, v253, 0\n"
"v_mfma_f32_16x16x32_bf16 v[36:39], a[8:11], v[144:147], v[36:39]\n"
"buffer_load_dwordx4 a[220:223], v1, s[4:7], s98 offen offset:3072 sc1\n"
"v_mfma_f32_16x16x32_bf16 v[36:39], a[12:15], v[152:155], v[36:39]\n"
"s_waitcnt vmcnt(20)\n"
"v_lshlrev_b32_e32 v253, 16, v253\n"
"v_add_f32_e32 v253, 0x3727c5ac, v253\n"
"v_mfma_f32_16x16x32_bf16 v[36:39], a[16:19], v[136:139], v[36:39]\n"
"s_add_u32 s98, s70, 0x6000\n"
"v_cvt_pk_bf16_f32 v253, v253, 0\n"
"v_lshlrev_b32_e32 v253, 16, v253\n"
"v_mfma_f32_16x16x32_bf16 v[36:39], a[20:23], v[68:71], v[36:39]\n"
"ds_write_b128 v3, v[32:35] offset:11264\n"
"v_mfma_f32_16x16x32_bf16 v[36:39], a[24:27], v[76:79], v[36:39]\n"
"buffer_load_dwordx4 a[224:227], v0, s[4:7], s98 offen sc1\n"
"v_mfma_f32_16x16x32_bf16 v[36:39], a[28:31], v[72:75], v[36:39]\n"
"v_sqrt_f32_e32 v253, v253\n"
"v_mfma_f32_16x16x32_bf16 v[32:35], a[32:35], v[40:43], 0\n"
"v_cvt_pk_bf16_f32 v253, v253, 0\n"
"v_lshlrev_b32_e32 v253, 16, v253\n"
"v_mfma_f32_16x16x32_bf16 v[32:35], a[36:39], v[120:123], v[32:35]\n"
"v_rcp_f32_e32 v255, v253\n"
"v_mfma_f32_16x16x32_bf16 v[32:35], a[40:43], v[144:147], v[32:35]\n"
"s_waitcnt lgkmcnt(0)\n"
"s_barrier\n"
"v_mfma_f32_16x16x32_bf16 v[32:35], a[44:47], v[152:155], v[32:35]\n"
"buffer_load_dwordx4 a[228:231], v0, s[4:7], s98 offen offset:1024 sc1\n"
"v_mfma_f32_16x16x32_bf16 v[32:35], a[48:51], v[136:139], v[32:35]\n"
"ds_read_b128 v[80:83], v4\n"
"s_add_u32 s95, s61, 0x440000\n"
"buffer_store_dwordx4 v[44:47], v0, s[4:7], s95 offen offset:1024 sc1\n"
"v_mul_f32_e32 v253, 0x3e8293ee, v255\n"
"v_mfma_f32_16x16x32_bf16 v[32:35], a[52:55], v[68:71], v[32:35]\n"
"ds_write_b128 v3, v[36:39] offset:12288\n"
"v_mfma_f32_16x16x32_bf16 v[32:35], a[56:59], v[76:79], v[32:35]\n"
"ds_read_b128 v[36:39], v9 offset:128\n"
"ds_read_b128 v[124:127], v5\n"
"v_mfma_f32_16x16x32_bf16 v[32:35], a[60:63], v[72:75], v[32:35]\n"
"buffer_load_dwordx4 a[232:235], v0, s[4:7], s98 offen offset:2048 sc1\n"
"v_mfma_f32_16x16x32_bf16 v[132:135], a[64:67], v[40:43], 0\n"
"v_mfma_f32_16x16x32_bf16 v[132:135], a[68:71], v[120:123], v[132:135]\n"
"v_mfma_f32_16x16x32_bf16 v[132:135], a[72:75], v[144:147], v[132:135]\n"
"ds_read_b128 v[140:143], v4 offset:1024\n"
"ds_read_b128 v[148:151], v9 offset:192\n"
"v_mfma_f32_16x16x32_bf16 v[132:135], a[76:79], v[152:155], v[132:135]\n"
"buffer_load_dwordx4 a[236:239], v0, s[4:7], s98 offen offset:3072 sc1\n"
"v_mfma_f32_16x16x32_bf16 v[132:135], a[80:83], v[136:139], v[132:135]\n"
"ds_read_b128 v[168:171], v5 offset:1024\n"
"s_waitcnt lgkmcnt(3)\n"
"v_mfma_f32_16x16x32_bf16 v[132:135], a[84:87], v[68:71], v[132:135]\n"
"ds_write_b128 v3, v[32:35] offset:13312\n"
"v_mfma_f32_16x16x32_bf16 v[132:135], a[88:91], v[76:79], v[132:135]\n"
"v_fmac_f32_e32 v80, v254, v36\n"
"v_fmac_f32_e32 v81, v254, v37\n"
"v_mfma_f32_16x16x32_bf16 v[132:135], a[92:95], v[72:75], v[132:135]\n"
"buffer_load_dwordx4 a[240:243], v1, s[4:7], s98 offen sc1\n"
"v_mfma_f32_16x16x32_bf16 v[32:35], a[96:99], v[40:43], 0\n"
"s_add_u32 s95, s63, 0x44000\n"
"buffer_store_dword v44, v14, s[4:7], s95 offen sc1\n"
"v_fmac_f32_e32 v82, v254, v38\n"
"v_fmac_f32_e32 v83, v254, v39\n"
"v_mfma_f32_16x16x32_bf16 v[32:35], a[100:103], v[120:123], v[32:35]\n"
"ds_read_b128 v[36:39], v4 offset:2048\n"
"ds_read_b128 v[44:47], v9 offset:256\n"
"v_mfma_f32_16x16x32_bf16 v[32:35], a[104:107], v[144:147], v[32:35]\n"
"ds_read_b128 v[172:175], v5 offset:2048\n"
"v_add_f32_e32 v80, v80, v124\n"
"v_mfma_f32_16x16x32_bf16 v[32:35], a[108:111], v[152:155], v[32:35]\n"
"buffer_load_dwordx4 a[244:247], v1, s[4:7], s98 offen offset:1024 sc1\n"
"v_mfma_f32_16x16x32_bf16 v[32:35], a[112:115], v[136:139], v[32:35]\n"
"s_mov_b32 s20, 0xff0000\n"
"v_add_f32_e32 v81, v81, v125\n"
"v_add_f32_e32 v82, v82, v126\n"
"v_mfma_f32_16x16x32_bf16 v[32:35], a[116:119], v[68:71], v[32:35]\n"
"ds_write_b128 v3, v[132:135] offset:14336\n"
"v_mfma_f32_16x16x32_bf16 v[32:35], a[120:123], v[76:79], v[32:35]\n"
"s_waitcnt lgkmcnt(5)\n"
"v_add_f32_e32 v83, v83, v127\n"
"v_fmac_f32_e32 v140, v254, v148\n"
"v_mfma_f32_16x16x32_bf16 v[32:35], a[124:127], v[72:75], v[32:35]\n"
"buffer_load_dwordx4 a[248:251], v1, s[4:7], s98 offen offset:2048 sc1\n"
"v_mfma_f32_16x16x32_bf16 v[124:127], v[28:31], v[40:43], 0\n"
"ds_read_b128 v[132:135], v4 offset:3072\n"
"ds_read_b128 v[176:179], v9 offset:320\n"
"v_mfma_f32_16x16x32_bf16 v[124:127], v[52:55], v[120:123], v[124:127]\n"
"ds_read_b128 v[196:199], v5 offset:3072\n"
"v_fmac_f32_e32 v141, v254, v149\n"
"v_mfma_f32_16x16x32_bf16 v[124:127], v[84:87], v[144:147], v[124:127]\n"
"s_mov_b32 s21, 0xffffffff\n"
"v_fmac_f32_e32 v142, v254, v150\n"
"v_fmac_f32_e32 v143, v254, v151\n"
"v_mfma_f32_16x16x32_bf16 v[124:127], v[92:95], v[152:155], v[124:127]\n"
"buffer_load_dwordx4 a[252:255], v1, s[4:7], s98 offen offset:3072 sc1\n"
"v_mfma_f32_16x16x32_bf16 v[124:127], v[100:103], v[136:139], v[124:127]\n"
"s_waitcnt vmcnt(13)\n"
"v_cmp_ne_u64_e32 vcc, v[246:247], v[18:19]\n"
"v_mfma_f32_16x16x32_bf16 v[124:127], v[108:111], v[68:71], v[124:127]\n"
"ds_write_b128 v3, v[32:35] offset:15360\n"
"v_mfma_f32_16x16x32_bf16 v[124:127], v[180:183], v[76:79], v[124:127]\n"
"s_cbranch_vccnz .Lmk_f6s\n"
".Lmk_f6r:\n"
"v_add_f32_e32 v140, v140, v168\n"
"v_mfma_f32_16x16x32_bf16 v[124:127], v[188:191], v[72:75], v[124:127]\n"
"ds_read_b128 v[32:35], v4 offset:4096\n"
"ds_read_b128 v[148:151], v9\n"
"v_mfma_f32_16x16x32_bf16 v[200:203], v[48:51], v[40:43], 0\n"
"ds_read_b128 v[40:43], v5 offset:4096\n"
"v_add_f32_e32 v141, v141, v169\n"
"v_mfma_f32_16x16x32_bf16 v[200:203], v[56:59], v[120:123], v[200:203]\n"
"s_waitcnt lgkmcnt(8)\n"
"v_add_f32_e32 v142, v142, v170\n"
"v_add_f32_e32 v143, v143, v171\n"
"v_mfma_f32_16x16x32_bf16 v[200:203], v[88:91], v[144:147], v[200:203]\n"
"s_mov_b32 s22, 0x1ff0001\n"
"v_fmac_f32_e32 v36, v254, v44\n"
"v_fmac_f32_e32 v37, v254, v45\n"
"v_mfma_f32_16x16x32_bf16 v[200:203], v[96:99], v[152:155], v[200:203]\n"
"s_mov_b32 s23, 0xffffffff\n"
"v_fmac_f32_e32 v38, v254, v46\n"
"v_fmac_f32_e32 v39, v254, v47\n"
"v_mfma_f32_16x16x32_bf16 v[200:203], v[104:107], v[136:139], v[200:203]\n"
"s_waitcnt vmcnt(12)\n"
"v_cmp_ne_u64_e32 vcc, v[250:251], v[18:19]\n"
"v_mfma_f32_16x16x32_bf16 v[200:203], v[112:115], v[68:71], v[200:203]\n"
"ds_write_b128 v3, v[124:127] offset:16384\n"
"v_mfma_f32_16x16x32_bf16 v[200:203], v[184:187], v[76:79], v[200:203]\n"
"s_cbranch_vccnz .Lmk_f3s\n"
".Lmk_f3r:\n"
"s_add_u32 m0, s77, 0x6000\n"
"v_mfma_f32_16x16x32_bf16 v[200:203], v[192:195], v[72:75], v[200:203]\n"
"s_add_u32 s98, s65, 0x2000\n"
"ds_read_b128 v[44:47], v4 offset:5120\n"
"; @A G1 3.3\n"
"v_mfma_f32_16x16x32_bf16 v[80:83], a[0:3], v[60:63], v[80:83]\n"
"buffer_load_dwordx4 v2, s[4:7], s98 offen sc1 lds\n"
"v_mfma_f32_16x16x32_bf16 v[80:83], a[4:7], v[64:67], v[80:83]\n"
"buffer_load_dwordx4 v2, s[4:7], s98 offen offset:1024 sc1 lds\n"
"v_mfma_f32_16x16x32_bf16 v[80:83], a[8:11], v[24:27], v[80:83]\n"
"ds_read_b128 v[68:71], v9 offset:64\n"
"ds_read_b128 v[72:75], v5 offset:5120\n"
"v_mfma_f32_16x16x32_bf16 v[80:83], a[12:15], v[128:131], v[80:83]\n"
"v_add_f32_e32 v36, v36, v172\n"
"v_add_f32_e32 v37, v37, v173\n"
"v_mfma_f32_16x16x32_bf16 v[80:83], a[16:19], v[116:119], v[80:83]\n"
"s_waitcnt lgkmcnt(8)\n"
"v_add_f32_e32 v38, v38, v174\n"
"v_add_f32_e32 v39, v39, v175\n"
"v_mfma_f32_16x16x32_bf16 v[80:83], a[20:23], v[156:159], v[80:83]\n"
"ds_write_b128 v3, v[200:203] offset:17408\n"
"v_mfma_f32_16x16x32_bf16 v[80:83], a[24:27], v[160:163], v[80:83]\n"
"s_mov_b32 s24, 0x3ff0003\n"
"v_fmac_f32_e32 v132, v254, v176\n"
"v_fmac_f32_e32 v133, v254, v177\n"
"v_mfma_f32_16x16x32_bf16 v[80:83], a[28:31], v[164:167], v[80:83]\n"
"s_add_u32 m0, s77, 0xe000\n"
"s_add_u32 s98, s65, 0x6000\n"
"v_fmac_f32_e32 v134, v254, v178\n"
"v_mfma_f32_16x16x32_bf16 v[140:143], a[32:35], v[60:63], v[140:143]\n"
"buffer_load_dwordx4 v2, s[4:7], s98 offen sc1 lds\n"
"v_mfma_f32_16x16x32_bf16 v[140:143], a[36:39], v[64:67], v[140:143]\n"
"buffer_load_dwordx4 v2, s[4:7], s98 offen offset:1024 sc1 lds\n"
"v_mfma_f32_16x16x32_bf16 v[140:143], a[40:43], v[24:27], v[140:143]\n"
"s_waitcnt lgkmcnt(0)\n"
"s_barrier\n"
"v_mfma_f32_16x16x32_bf16 v[140:143], a[44:47], v[128:131], v[140:143]\n"
"ds_read_b128 v[76:79], v6\n"
"ds_read_b128 v[120:123], v6 offset:1024\n"
"v_mfma_f32_16x16x32_bf16 v[140:143], a[48:51], v[116:119], v[140:143]\n"
"s_mov_b32 s25, 0xffffffff\n"
"v_fmac_f32_e32 v135, v254, v179\n"
"v_add_f32_e32 v132, v132, v196\n"
"v_mfma_f32_16x16x32_bf16 v[140:143], a[52:55], v[156:159], v[140:143]\n"
"s_mov_b32 s26, 0x7ff0007\n"
"v_add_f32_e32 v133, v133, v197\n"
"v_add_f32_e32 v134, v134, v198\n"
"v_mfma_f32_16x16x32_bf16 v[140:143], a[56:59], v[160:163], v[140:143]\n"
"s_add_u32 s98, s69, 0x600000\n"
"buffer_load_dwordx4 a[0:3], v0, s[4:7], s98 offen sc1\n"
"v_mfma_f32_16x16x32_bf16 v[140:143], a[60:63], v[164:167], v[140:143]\n"
"v_add_f32_e32 v135, v135, v199\n"
"v_fmac_f32_e32 v32, v254, v148\n"
"v_mfma_f32_16x16x32_bf16 v[36:39], a[64:67], v[60:63], v[36:39]\n"
"s_mov_b32 s27, 0xffffffff\n"
"v_fmac_f32_e32 v33, v254, v149\n"
"v_fmac_f32_e32 v34, v254, v150\n"
"v_mfma_f32_16x16x32_bf16 v[36:39], a[68:71], v[64:67], v[36:39]\n"
"s_mov_b32 s28, 0xfff000f\n"
"v_fmac_f32_e32 v35, v254, v151\n"
"v_add_f32_e32 v32, v32, v40\n"
"v_mfma_f32_16x16x32_bf16 v[36:39], a[72:75], v[24:27], v[36:39]\n"
"s_mov_b32 s29, 0xffffffff\n"
"v_add_f32_e32 v33, v33, v41\n"
"v_add_f32_e32 v34, v34, v42\n"
"v_mfma_f32_16x16x32_bf16 v[36:39], a[76:79], v[128:131], v[36:39]\n"
"buffer_load_dwordx4 a[4:7], v0, s[4:7], s98 offen offset:1024 sc1\n"
"v_mfma_f32_16x16x32_bf16 v[36:39], a[80:83], v[116:119], v[36:39]\n"
"v_add_f32_e32 v35, v35, v43\n"
"v_fmac_f32_e32 v44, v254, v68\n"
"v_mfma_f32_16x16x32_bf16 v[36:39], a[84:87], v[156:159], v[36:39]\n"
"s_waitcnt lgkmcnt(0)\n"
"v_fmac_f32_e32 v45, v254, v69\n"
"v_fmac_f32_e32 v46, v254, v70\n"
"v_mfma_f32_16x16x32_bf16 v[36:39], a[88:91], v[160:163], v[36:39]\n"
"ds_read_b128 v[40:43], v6 offset:2048\n"
"ds_read_b128 v[124:127], v6 offset:3072\n"
"v_mfma_f32_16x16x32_bf16 v[36:39], a[92:95], v[164:167], v[36:39]\n"
"s_mov_b32 s30, 0x1fff001f\n"
"v_fmac_f32_e32 v47, v254, v71\n"
"v_add_f32_e32 v44, v44, v72\n"
"v_mfma_f32_16x16x32_bf16 v[132:135], a[96:99], v[60:63], v[132:135]\n"
"buffer_load_dwordx4 a[8:11], v0, s[4:7], s98 offen offset:2048 sc1\n"
"v_mfma_f32_16x16x32_bf16 v[132:135], a[100:103], v[64:67], v[132:135]\n"
"s_mov_b32 s31, 0xffffffff\n"
"v_add_f32_e32 v45, v45, v73\n"
"v_add_f32_e32 v46, v46, v74\n"
"v_mfma_f32_16x16x32_bf16 v[132:135], a[104:107], v[24:27], v[132:135]\n"
"s_mov_b32 s32, 0x3fff003f\n"
"v_add_f32_e32 v47, v47, v75\n"
"v_add_f32_e32 v80, v80, v76\n"
"v_mfma_f32_16x16x32_bf16 v[132:135], a[108:111], v[128:131], v[132:135]\n"
"s_mov_b32 s33, 0xffffffff\n"
"v_add_f32_e32 v81, v81, v77\n"
"v_add_f32_e32 v82, v82, v78\n"
"v_mfma_f32_16x16x32_bf16 v[132:135], a[112:115], v[116:119], v[132:135]\n"
"s_mov_b32 s34, 0x7fff007f\n"
"v_add_f32_e32 v83, v83, v79\n"
"v_add_f32_e32 v140, v140, v120\n"
"v_mfma_f32_16x16x32_bf16 v[132:135], a[116:119], v[156:159], v[132:135]\n"
"buffer_load_dwordx4 a[12:15], v0, s[4:7], s98 offen offset:3072 sc1\n"
"v_mfma_f32_16x16x32_bf16 v[132:135], a[120:123], v[160:163], v[132:135]\n"
"s_mov_b32 s35, 0xffffffff\n"
"v_add_f32_e32 v141, v141, v121\n"
"v_add_f32_e32 v142, v142, v122\n"
"v_mfma_f32_16x16x32_bf16 v[132:135], a[124:127], v[164:167], v[132:135]\n"
"s_mov_b32 s36, 0\n"
"v_add_f32_e32 v143, v143, v123\n"
"v_mul_f32_e32 v80, v80, v255\n"
"v_mfma_f32_16x16x32_bf16 v[32:35], v[28:31], v[60:63], v[32:35]\n"
"s_mov_b32 s37, 0xff0000\n"
"v_mul_f32_e32 v81, v81, v255\n"
"v_mul_f32_e32 v82, v82, v255\n"
"v_mfma_f32_16x16x32_bf16 v[32:35], v[52:55], v[64:67], v[32:35]\n"
"s_mov_b32 s38, 0\n"
"v_mul_f32_e32 v83, v83, v255\n"
"v_mul_f32_e32 v140, v140, v255\n"
"v_mfma_f32_16x16x32_bf16 v[32:35], v[84:87], v[24:27], v[32:35]\n"
"buffer_load_dwordx4 a[16:19], v1, s[4:7], s98 offen sc1\n"
"v_mfma_f32_16x16x32_bf16 v[32:35], v[92:95], v[128:131], v[32:35]\n"
"s_mov_b32 s39, 0x1ff0001\n"
"v_mul_f32_e32 v141, v141, v255\n"
"v_mul_f32_e32 v142, v142, v255\n"
"v_mfma_f32_16x16x32_bf16 v[32:35], v[100:103], v[116:119], v[32:35]\n"
"s_waitcnt lgkmcnt(0)\n"
"s_waitcnt vmcnt(12)\n"
"buffer_store_dwordx2 v[18:19], v11, s[4:7], s68 offen offset:32 sc1\n"
"v_mul_f32_e32 v143, v143, v255\n"
"v_cvt_pk_bf16_f32 v28, v80, v81\n"
"v_mfma_f32_16x16x32_bf16 v[32:35], v[108:111], v[156:159], v[32:35]\n"
"s_mov_b32 s40, 0\n"
"v_cvt_pk_bf16_f32 v29, v82, v83\n"
"v_cvt_pk_bf16_f32 v30, v140, v141\n"
"v_mfma_f32_16x16x32_bf16 v[32:35], v[180:183], v[160:163], v[32:35]\n"
"ds_read_b128 v[52:55], v6 offset:4096\n"
"ds_read_b128 v[68:71], v6 offset:5120\n"
"v_mfma_f32_16x16x32_bf16 v[32:35], v[188:191], v[164:167], v[32:35]\n"
"buffer_load_dwordx4 a[20:23], v1, s[4:7], s98 offen offset:1024 sc1\n"
"v_mfma_f32_16x16x32_bf16 v[44:47], v[48:51], v[60:63], v[44:47]\n"
"s_mov_b32 s41, 0x3ff0003\n"
"v_cvt_pk_bf16_f32 v31, v142, v143\n"
"v_add_f32_e32 v36, v36, v40\n"
"v_mfma_f32_16x16x32_bf16 v[44:47], v[56:59], v[64:67], v[44:47]\n"
"ds_write_b128 v7, v[28:31] offset:16384\n"
"v_mfma_f32_16x16x32_bf16 v[44:47], v[88:91], v[24:27], v[44:47]\n"
"buffer_store_dwordx4 v[28:31], v7, s[4:7], s64 offen sc1\n"
"v_mfma_f32_16x16x32_bf16 v[44:47], v[96:99], v[128:131], v[44:47]\n"
"s_mov_b32 s42, 0\n"
"v_add_f32_e32 v37, v37, v41\n"
"v_add_f32_e32 v38, v38, v42\n"
"v_mfma_f32_16x16x32_bf16 v[44:47], v[104:107], v[116:119], v[44:47]\n"
"buffer_load_dwordx4 a[24:27], v1, s[4:7], s98 offen offset:2048 sc1\n"
"v_mfma_f32_16x16x32_bf16 v[44:47], v[112:115], v[156:159], v[44:47]\n"
"s_mov_b32 s43, 0x7ff0007\n"
"v_add_f32_e32 v39, v39, v43\n"
"v_add_f32_e32 v132, v132, v124\n"
"v_mfma_f32_16x16x32_bf16 v[44:47], v[184:187], v[160:163], v[44:47]\n"
"s_mov_b32 s44, 0\n"
"v_add_f32_e32 v133, v133, v125\n"
"v_add_f32_e32 v134, v134, v126\n"
"v_mfma_f32_16x16x32_bf16 v[44:47], v[192:195], v[164:167], v[44:47]\n"
"v_add_f32_e32 v135, v135, v127\n"
"v_mul_f32_e32 v36, v36, v255\n"
"v_mul_f32_e32 v37, v37, v255\n"
"v_mul_f32_e32 v38, v38, v255\n"
"v_mul_f32_e32 v39, v39, v255\n"
"v_mul_f32_e32 v132, v132, v255\n"
"v_mul_f32_e32 v133, v133, v255\n"
"v_mul_f32_e32 v134, v134, v255\n"
"v_mul_f32_e32 v135, v135, v255\n"
"v_cvt_pk_bf16_f32 v24, v36, v37\n"
"v_cvt_pk_bf16_f32 v25, v38, v39\n"
"v_cvt_pk_bf16_f32 v26, v132, v133\n"
"v_cvt_pk_bf16_f32 v27, v134, v135\n"
"ds_write_b128 v7, v[24:27] offset:49152\n"
"s_mov_b32 s45, 0xfff000f\n"
"s_mov_b32 s46, 0\n"
"s_mov_b32 s47, 0x1fff001f\n"
"s_mov_b32 s48, 0\n"
"s_mov_b32 s49, 0x3fff003f\n"
"s_mov_b32 s50, 0\n"
"s_mov_b32 s51, 0x7fff007f\n"
"buffer_load_dwordx4 a[28:31], v1, s[4:7], s98 offen offset:3072 sc1\n"
"v_xor_b32_e32 v255, 32, v16\n"
"v_xor_b32_e32 v254, 32, v15\n"
"v_mov_b32_e32 v88, 0x3f803f80\n"
"v_mov_b32_e32 v89, 0x3f803f80\n"
"v_mov_b32_e32 v90, 0x3f803f80\n"
"v_mov_b32_e32 v91, 0x3f803f80\n"
"v_mov_b32_e32 v252, 0x53800000\n"
".Lmk_z4:\n"
"; @A tail\n"
"s_add_u32 s98, s64, 0x4000\n"
"buffer_store_dwordx4 v[24:27], v7, s[4:7], s98 offen sc1\n"
"s_waitcnt lgkmcnt(2)\n"
"v_add_f32_e32 v32, v32, v52\n"
"v_add_f32_e32 v33, v33, v53\n"
"v_add_f32_e32 v34, v34, v54\n"
"v_add_f32_e32 v35, v35, v55\n"
"v_add_f32_e32 v44, v44, v68\n"
"v_add_f32_e32 v45, v45, v69\n"
"v_add_f32_e32 v46, v46, v70\n"
"v_add_f32_e32 v47, v47, v71\n"
"v_mul_f32_e32 v32, v32, v253\n"
"v_mul_f32_e32 v33, v33, v253\n"
"v_mul_f32_e32 v34, v34, v253\n"
"v_mul_f32_e32 v35, v35, v253\n"
"v_mul_f32_e32 v44, v44, v253\n"
"v_mul_f32_e32 v45, v45, v253\n"
"v_mul_f32_e32 v46, v46, v253\n"
"v_mul_f32_e32 v47, v47, v253\n"
"v_cvt_pk_bf16_f32 v24, v32, v33\n"
"v_cvt_pk_bf16_f32 v25, v34, v35\n"
"v_cvt_pk_bf16_f32 v26, v44, v45\n"
"v_cvt_pk_bf16_f32 v27, v46, v47\n"
"ds_write_b128 v2, v[24:27] offset:16384\n"
"s_waitcnt lgkmcnt(0)\n"
"s_barrier\n"
"ds_read_b128 v[28:31], v2 offset:49152\n"
"ds_read_b128 v[32:35], v2 offset:50176\n"
"ds_read_b128 v[92:95], v2 offset:16384\n"
"ds_read_b128 v[96:99], v2 offset:17408\n"
"; @A AT b\n"
"s_mov_b32 s2, 0\n"
"v_mov_b32_e32 v253, 0xff800000\n"
"v_mov_b32_e32 v251, 0xff800000\n"
"v_mov_b32_e32 v250, 0x7f800000\n"
"v_mov_b32_e32 v249, 0x7f800000\n"
".Lmk_a20:\n"
"ds_read_b128 v[24:27], v15 offset:24576\n"
"ds_read_b128 v[36:39], v254 offset:24832\n"
"ds_read_b128 v[40:43], v15 offset:22528\n"
"ds_read_b128 v[44:47], v254 offset:22784\n"
"s_cmp_gt_i32 s78, 11\n"
"s_cselect_b32 s98, 0x80000000, 0\n"
"s_or_b32 s98, s98, 0x7f800000\n"
"s_waitcnt lgkmcnt(2)\n"
"v_mfma_f32_16x16x32_bf16 v[48:51], v[24:27], v[28:31], 0\n"
"v_mfma_f32_16x16x32_bf16 v[52:55], v[24:27], v[32:35], 0\n"
"v_mfma_f32_16x16x32_bf16 v[56:59], v[36:39], v[28:31], 0\n"
"v_mfma_f32_16x16x32_bf16 v[60:63], v[36:39], v[32:35], 0\n"
"s_waitcnt lgkmcnt(0)\n"
"s_nop 3\n"
"v_max_f32_e32 v248, v48, v49\n"
"v_max_f32_e32 v247, v52, v53\n"
"v_max3_f32 v248, v248, v50, v51\n"
"v_max3_f32 v247, v247, v54, v55\n"
"v_max3_f32 v248, v248, v56, v57\n"
"v_max3_f32 v247, v247, v60, v61\n"
"v_max3_f32 v248, v248, v58, v59\n"
"v_max3_f32 v247, v247, v62, v63\n"
"v_mov_b32_e32 v246, v248\n"
"v_mov_b32_e32 v245, v247\n"
"s_nop 0\n"
"v_permlane16_swap_b32_e32 v248, v246\n"
"v_permlane16_swap_b32_e32 v247, v245\n"
"v_max_f32_e32 v248, v248, v246\n"
"v_max_f32_e32 v247, v247, v245\n"
"v_mov_b32_e32 v246, v248\n"
"v_mov_b32_e32 v245, v247\n"
"s_nop 0\n"
"v_permlane32_swap_b32_e32 v248, v246\n"
"v_permlane32_swap_b32_e32 v247, v245\n"
"v_max_f32_e32 v248, v248, v246\n"
"v_max_f32_e32 v247, v247, v245\n"
"v_med3_f32 v248, v248, v253, v250\n"
"v_med3_f32 v247, v247, v251, v249\n"
"v_min_f32_e64 v24, s98, -v248\n"
"v_min_f32_e64 v25, s98, -v248\n"
"v_min_f32_e64 v26, s98, -v248\n"
"v_min_f32_e64 v27, s98, -v248\n"
"v_min_f32_e64 v36, s98, -v247\n"
"v_min_f32_e64 v37, s98, -v247\n"
"v_min_f32_e64 v38, s98, -v247\n"
"v_min_f32_e64 v39, s98, -v247\n"
"v_mfma_f32_16x16x32_bf16 v[64:67], v[40:43], v[28:31], v[24:27]\n"
"ds_read_b128 v[68:71], v15 offset:20480\n"
"s_add_u32 s98, s69, 0x602000\n"
"buffer_load_dwordx4 a[32:35], v0, s[4:7], s98 offen sc1\n"
"v_sub_f32_e32 v48, v48, v248\n"
"v_sub_f32_e32 v49, v49, v248\n"
"v_sub_f32_e32 v50, v50, v248\n"
"v_mfma_f32_16x16x32_bf16 v[72:75], v[40:43], v[32:35], v[36:39]\n"
"buffer_load_dwordx4 a[36:39], v0, s[4:7], s98 offen offset:1024 sc1\n"
"v_sub_f32_e32 v51, v51, v248\n"
"v_sub_f32_e32 v56, v56, v248\n"
"v_sub_f32_e32 v57, v57, v248\n"
"v_sub_f32_e32 v58, v58, v248\n"
"v_sub_f32_e32 v59, v59, v248\n"
"v_mfma_f32_16x16x32_bf16 v[40:43], v[44:47], v[28:31], v[24:27]\n"
"ds_read_b128 v[76:79], v254 offset:20736\n"
"v_sub_f32_e32 v52, v52, v247\n"
"v_sub_f32_e32 v53, v53, v247\n"
"v_sub_f32_e32 v54, v54, v247\n"
"v_sub_f32_e32 v55, v55, v247\n"
"v_mfma_f32_16x16x32_bf16 v[80:83], v[44:47], v[32:35], v[36:39]\n"
"buffer_load_dwordx4 a[40:43], v0, s[4:7], s98 offen offset:2048 sc1\n"
"v_sub_f32_e32 v60, v60, v247\n"
"v_sub_f32_e32 v61, v61, v247\n"
"v_sub_f32_e32 v62, v62, v247\n"
"v_sub_f32_e32 v63, v63, v247\n"
"s_waitcnt lgkmcnt(0)\n"
"v_mfma_f32_16x16x32_bf16 v[44:47], v[68:71], v[28:31], v[24:27]\n"
"ds_read_b128 v[84:87], v15 offset:18432\n"
"buffer_load_dwordx4 a[44:47], v0, s[4:7], s98 offen offset:3072 sc1\n"
"ds_read_b128 v[100:103], v254 offset:18688\n"
"ds_read_b64_tr_b16 v[104:105], v16 offset:22528\n"
"v_exp_f32_e32 v64, v64\n"
"v_exp_f32_e32 v65, v65\n"
"v_exp_f32_e32 v66, v66\n"
"v_exp_f32_e32 v67, v67\n"
"v_exp_f32_e32 v40, v40\n"
"v_exp_f32_e32 v41, v41\n"
"v_mfma_f32_16x16x32_bf16 v[108:111], v[68:71], v[32:35], v[36:39]\n"
"buffer_load_dwordx4 a[48:51], v1, s[4:7], s98 offen sc1\n"
"ds_read_b64_tr_b16 v[106:107], v255 offset:22784\n"
"buffer_load_dwordx4 a[52:55], v1, s[4:7], s98 offen offset:1024 sc1\n"
"v_exp_f32_e32 v42, v42\n"
"v_exp_f32_e32 v43, v43\n"
"v_cvt_pk_bf16_f32 v68, v64, v65\n"
"v_cvt_pk_bf16_f32 v69, v66, v67\n"
"v_cvt_pk_bf16_f32 v70, v40, v41\n"
"v_cvt_pk_bf16_f32 v71, v42, v43\n"
"v_exp_f32_e32 v72, v72\n"
"v_exp_f32_e32 v73, v73\n"
"v_mfma_f32_16x16x32_bf16 v[40:43], v[76:79], v[28:31], v[24:27]\n"
"ds_read_b64_tr_b16 v[64:65], v255 offset:22528\n"
"s_add_u32 s97, s76, 0x80000\n"
"v_exp_f32_e32 v74, v74\n"
"v_exp_f32_e32 v75, v75\n"
"v_exp_f32_e32 v80, v80\n"
"v_bfe_u32 v248, v0, 2, 7\n"
"v_add_u32_e32 v248, s72, v248\n"
"v_and_b32_e32 v247, 0xffffffc0, v248\n"
"v_add_u32_e32 v248, v248, v247\n"
"v_and_b32_e32 v247, 0x200, v0\n"
"v_lshl_add_u32 v248, v247, 4, v248\n"
"v_mfma_f32_16x16x32_bf16 v[112:115], v[76:79], v[32:35], v[36:39]\n"
"buffer_load_dword v247, v248, s[4:7], s97 offen sc1\n"
"ds_read_b64_tr_b16 v[66:67], v16 offset:22784\n"
"s_add_u32 s96, s68, 0x10000\n"
"buffer_store_dwordx2 v[18:19], v11, s[4:7], s96 offen offset:96 sc1\n"
"buffer_load_dword v246, v248, s[4:7], s97 offen offset:1024 sc1\n"
"v_exp_f32_e32 v81, v81\n"
"v_exp_f32_e32 v82, v82\n"
"v_exp_f32_e32 v83, v83\n"
"v_cvt_pk_bf16_f32 v76, v72, v73\n"
"v_cvt_pk_bf16_f32 v77, v74, v75\n"
"v_cvt_pk_bf16_f32 v78, v80, v81\n"
"v_cvt_pk_bf16_f32 v79, v82, v83\n"
"s_waitcnt lgkmcnt(4)\n"
"v_mfma_f32_16x16x32_bf16 v[72:75], v[84:87], v[28:31], v[24:27]\n"
"ds_read_b128 v[80:83], v15 offset:16384\n"
"v_exp_f32_e32 v44, v44\n"
"v_exp_f32_e32 v45, v45\n"
"v_exp_f32_e32 v46, v46\n"
"v_mfma_f32_16x16x32_bf16 v[116:119], v[84:87], v[32:35], v[36:39]\n"
"buffer_load_dwordx4 a[56:59], v1, s[4:7], s98 offen offset:2048 sc1\n"
"ds_read_b128 v[84:87], v254 offset:16640\n"
"v_exp_f32_e32 v47, v47\n"
"v_exp_f32_e32 v40, v40\n"
"v_mfma_f32_16x16x32_bf16 v[120:123], v[100:103], v[28:31], v[24:27]\n"
"ds_read_b64_tr_b16 v[124:125], v16 offset:20480\n"
"v_exp_f32_e32 v41, v41\n"
"v_exp_f32_e32 v42, v42\n"
"v_mfma_f32_16x16x32_bf16 v[128:131], v[100:103], v[32:35], v[36:39]\n"
"buffer_load_dwordx4 a[60:63], v1, s[4:7], s98 offen offset:3072 sc1\n"
"v_exp_f32_e32 v43, v43\n"
"v_cvt_pk_bf16_f32 v100, v44, v45\n"
"v_cvt_pk_bf16_f32 v101, v46, v47\n"
"s_waitcnt lgkmcnt(3)\n"
"v_mfma_f32_16x16x32_bf16 v[44:47], v[88:91], v[68:71], 0\n"
"ds_read_b64_tr_b16 v[126:127], v255 offset:20736\n"
"s_add_u32 s98, s69, 0x604000\n"
"buffer_load_dwordx4 a[64:67], v0, s[4:7], s98 offen sc1\n"
"v_cvt_pk_bf16_f32 v102, v40, v41\n"
"v_cvt_pk_bf16_f32 v103, v42, v43\n"
"v_exp_f32_e32 v108, v108\n"
"v_mfma_f32_16x16x32_bf16 v[40:43], v[104:107], v[68:71], 0\n"
"ds_read_b64_tr_b16 v[132:133], v255 offset:20480\n"
"v_exp_f32_e32 v109, v109\n"
"v_exp_f32_e32 v110, v110\n"
"v_mfma_f32_16x16x32_bf16 v[136:139], v[64:67], v[68:71], 0\n"
"buffer_load_dword v245, v248, s[4:7], s97 offen offset:2048 sc1\n"
"v_exp_f32_e32 v111, v111\n"
"v_exp_f32_e32 v112, v112\n"
"v_mfma_f32_16x16x32_bf16 v[68:71], v[88:91], v[76:79], 0\n"
"ds_read_b64_tr_b16 v[134:135], v16 offset:20736\n"
"v_exp_f32_e32 v113, v113\n"
"v_exp_f32_e32 v114, v114\n"
"v_mfma_f32_16x16x32_bf16 v[140:143], v[104:107], v[76:79], 0\n"
"buffer_load_dword v244, v248, s[4:7], s97 offen offset:3072 sc1\n"
"s_waitcnt vmcnt(14)\n"
"buffer_store_dwordx2 v[18:19], v11, s[4:7], s68 offen offset:64 sc1\n"
"v_exp_f32_e32 v115, v115\n"
"v_mfma_f32_16x16x32_bf16 v[104:107], v[64:67], v[76:79], 0\n"
"s_cmp_lg_u32 s2, 0\n"
"v_cvt_pk_bf16_f32 v64, v108, v109\n"
"v_cvt_pk_bf16_f32 v65, v110, v111\n"
"v_cvt_pk_bf16_f32 v66, v112, v113\n"
"v_cvt_pk_bf16_f32 v67, v114, v115\n"
"s_cbranch_scc1 .Lmk_s22\n"
"s_barrier\n"
".Lmk_s22:\n"
"s_waitcnt lgkmcnt(4)\n"
"v_mfma_f32_16x16x32_bf16 v[76:79], v[80:83], v[28:31], v[24:27]\n"
"ds_read_b128 v[108:111], v15 offset:14336\n"
"v_exp_f32_e32 v72, v72\n"
"v_exp_f32_e32 v73, v73\n"
"v_exp_f32_e32 v74, v74\n"
"v_mfma_f32_16x16x32_bf16 v[112:115], v[80:83], v[32:35], v[36:39]\n"
"buffer_load_dwordx4 a[68:71], v0, s[4:7], s98 offen offset:1024 sc1\n"
"ds_read_b128 v[80:83], v254 offset:14592\n"
"v_exp_f32_e32 v75, v75\n"
"v_mfma_f32_16x16x32_bf16 v[144:147], v[84:87], v[28:31], v[24:27]\n"
"v_exp_f32_e32 v120, v120\n"
"v_exp_f32_e32 v121, v121\n"
"v_exp_f32_e32 v122, v122\n"
"v_mfma_f32_16x16x32_bf16 v[148:151], v[84:87], v[32:35], v[36:39]\n"
"buffer_load_dwordx4 a[72:75], v0, s[4:7], s98 offen offset:2048 sc1\n"
"ds_read_b64_tr_b16 v[84:85], v16 offset:18432\n"
"v_exp_f32_e32 v123, v123\n"
"s_waitcnt lgkmcnt(3)\n"
"v_mfma_f32_16x16x32_bf16 v[44:47], v[88:91], v[100:103], v[44:47]\n"
"buffer_load_dwordx4 a[76:79], v0, s[4:7], s98 offen offset:3072 sc1\n"
"v_cvt_pk_bf16_f32 v152, v72, v73\n"
"v_cvt_pk_bf16_f32 v153, v74, v75\n"
"v_cvt_pk_bf16_f32 v154, v120, v121\n"
"v_cvt_pk_bf16_f32 v155, v122, v123\n"
"v_mfma_f32_16x16x32_bf16 v[40:43], v[124:127], v[100:103], v[40:43]\n"
"ds_read_b64_tr_b16 v[86:87], v255 offset:18688\n"
"v_exp_f32_e32 v116, v116\n"
"v_exp_f32_e32 v117, v117\n"
"v_exp_f32_e32 v118, v118\n"
"v_mfma_f32_16x16x32_bf16 v[136:139], v[132:135], v[100:103], v[136:139]\n"
"s_add_u32 s97, s76, 0x81000\n"
"buffer_load_dword v243, v248, s[4:7], s97 offen sc1\n"
"v_exp_f32_e32 v119, v119\n"
"v_mfma_f32_16x16x32_bf16 v[68:71], v[88:91], v[64:67], v[68:71]\n"
"ds_read_b64_tr_b16 v[72:73], v255 offset:18432\n"
"v_exp_f32_e32 v128, v128\n"
"v_exp_f32_e32 v129, v129\n"
"v_mfma_f32_16x16x32_bf16 v[140:143], v[124:127], v[64:67], v[140:143]\n"
"buffer_load_dword v242, v248, s[4:7], s97 offen offset:1024 sc1\n"
"ds_read_b64_tr_b16 v[74:75], v16 offset:18688\n"
"v_exp_f32_e32 v130, v130\n"
"v_exp_f32_e32 v131, v131\n"
"v_mfma_f32_16x16x32_bf16 v[104:107], v[132:135], v[64:67], v[104:107]\n"
"s_waitcnt lgkmcnt(4)\n"
"v_cvt_pk_bf16_f32 v64, v116, v117\n"
"v_cvt_pk_bf16_f32 v65, v118, v119\n"
"v_cvt_pk_bf16_f32 v66, v128, v129\n"
"v_cvt_pk_bf16_f32 v67, v130, v131\n"
"s_cmp_eq_i32 s78, 8\n"
"s_cbranch_scc1 .Lmk_x23s\n"
".Lmk_x23r:\n"
"v_mfma_f32_16x16x32_bf16 v[100:103], v[108:111], v[28:31], v[24:27]\n"
"ds_read_b128 v[116:119], v15 offset:12288\n"
"v_exp_f32_e32 v76, v76\n"
"v_exp_f32_e32 v77, v77\n"
"v_exp_f32_e32 v78, v78\n"
"v_mfma_f32_16x16x32_bf16 v[120:123], v[108:111], v[32:35], v[36:39]\n"
"buffer_load_dwordx4 a[80:83], v1, s[4:7], s98 offen sc1\n"
"ds_read_b128 v[108:111], v254 offset:12544\n"
"v_exp_f32_e32 v79, v79\n"
"v_mfma_f32_16x16x32_bf16 v[124:127], v[80:83], v[28:31], v[24:27]\n"
"v_exp_f32_e32 v144, v144\n"
"v_exp_f32_e32 v145, v145\n"
"v_exp_f32_e32 v146, v146\n"
"v_mfma_f32_16x16x32_bf16 v[128:131], v[80:83], v[32:35], v[36:39]\n"
"buffer_load_dwordx4 a[84:87], v1, s[4:7], s98 offen offset:1024 sc1\n"
"ds_read_b64_tr_b16 v[80:81], v16 offset:16384\n"
"v_exp_f32_e32 v147, v147\n"
"s_waitcnt lgkmcnt(3)\n"
"v_mfma_f32_16x16x32_bf16 v[44:47], v[88:91], v[152:155], v[44:47]\n"
"buffer_load_dwordx4 a[88:91], v1, s[4:7], s98 offen offset:2048 sc1\n"
"v_cvt_pk_bf16_f32 v132, v76, v77\n"
"v_cvt_pk_bf16_f32 v133, v78, v79\n"
"v_cvt_pk_bf16_f32 v134, v144, v145\n"
"v_cvt_pk_bf16_f32 v135, v146, v147\n"
"v_mfma_f32_16x16x32_bf16 v[40:43], v[84:87], v[152:155], v[40:43]\n"
"ds_read_b64_tr_b16 v[82:83], v255 offset:16640\n"
"v_exp_f32_e32 v112, v112\n"
"v_exp_f32_e32 v113, v113\n"
"v_mfma_f32_16x16x32_bf16 v[136:139], v[72:75], v[152:155], v[136:139]\n"
"buffer_load_dword v241, v248, s[4:7], s97 offen offset:2048 sc1\n"
"v_exp_f32_e32 v114, v114\n"
"v_exp_f32_e32 v115, v115\n"
"v_mfma_f32_16x16x32_bf16 v[68:71], v[88:91], v[64:67], v[68:71]\n"
"ds_read_b64_tr_b16 v[76:77], v255 offset:16384\n"
"v_exp_f32_e32 v148, v148\n"
"v_exp_f32_e32 v149, v149\n"
"v_mfma_f32_16x16x32_bf16 v[140:143], v[84:87], v[64:67], v[140:143]\n"
"buffer_load_dword v240, v248, s[4:7], s97 offen offset:3072 sc1\n"
"ds_read_b64_tr_b16 v[78:79], v16 offset:16640\n"
"v_exp_f32_e32 v150, v150\n"
"v_exp_f32_e32 v151, v151\n"
"v_mfma_f32_16x16x32_bf16 v[104:107], v[72:75], v[64:67], v[104:107]\n"
"s_waitcnt lgkmcnt(4)\n"
"v_cvt_pk_bf16_f32 v64, v112, v113\n"
"v_cvt_pk_bf16_f32 v65, v114, v115\n"
"v_cvt_pk_bf16_f32 v66, v148, v149\n"
"v_cvt_pk_bf16_f32 v67, v150, v151\n"
"s_cmp_eq_i32 s78, 7\n"
"s_cbranch_scc1 .Lmk_x24s\n"
".Lmk_x24r:\n"
"v_mfma_f32_16x16x32_bf16 v[72:75], v[116:119], v[28:31], v[24:27]\n"
"ds_read_b128 v[84:87], v15 offset:10240\n"
"v_exp_f32_e32 v100, v100\n"
"v_exp_f32_e32 v101, v101\n"
"v_exp_f32_e32 v102, v102\n"
"v_mfma_f32_16x16x32_bf16 v[112:115], v[116:119], v[32:35], v[36:39]\n"
"buffer_load_dwordx4 a[92:95], v1, s[4:7], s98 offen offset:3072 sc1\n"
"v_exp_f32_e32 v103, v103\n"
"v_mfma_f32_16x16x32_bf16 v[116:119], v[108:111], v[28:31], v[24:27]\n"
"ds_read_b128 v[144:147], v254 offset:10496\n"
"v_exp_f32_e32 v124, v124\n"
"v_exp_f32_e32 v125, v125\n"
"v_mfma_f32_16x16x32_bf16 v[148:151], v[108:111], v[32:35], v[36:39]\n"
"ds_read_b64_tr_b16 v[108:109], v16 offset:14336\n"
"v_exp_f32_e32 v126, v126\n"
"v_exp_f32_e32 v127, v127\n"
"v_cvt_pk_bf16_f32 v152, v100, v101\n"
"s_waitcnt lgkmcnt(3)\n"
"v_mfma_f32_16x16x32_bf16 v[44:47], v[88:91], v[132:135], v[44:47]\n"
"s_add_u32 s98, s69, 0x606000\n"
"buffer_load_dwordx4 a[96:99], v0, s[4:7], s98 offen sc1\n"
"v_cvt_pk_bf16_f32 v153, v102, v103\n"
"v_cvt_pk_bf16_f32 v154, v124, v125\n"
"v_mfma_f32_16x16x32_bf16 v[40:43], v[80:83], v[132:135], v[40:43]\n"
"ds_read_b64_tr_b16 v[110:111], v255 offset:14592\n"
"v_cvt_pk_bf16_f32 v155, v126, v127\n"
"v_exp_f32_e32 v120, v120\n"
"v_exp_f32_e32 v121, v121\n"
"v_mfma_f32_16x16x32_bf16 v[136:139], v[76:79], v[132:135], v[136:139]\n"
"ds_read_b64_tr_b16 v[100:101], v255 offset:14336\n"
"v_exp_f32_e32 v122, v122\n"
"v_exp_f32_e32 v123, v123\n"
"v_mfma_f32_16x16x32_bf16 v[68:71], v[88:91], v[64:67], v[68:71]\n"
"buffer_load_dwordx4 a[100:103], v0, s[4:7], s98 offen offset:1024 sc1\n"
"v_exp_f32_e32 v128, v128\n"
"v_exp_f32_e32 v129, v129\n"
"v_mfma_f32_16x16x32_bf16 v[140:143], v[80:83], v[64:67], v[140:143]\n"
"ds_read_b64_tr_b16 v[102:103], v16 offset:14592\n"
"v_exp_f32_e32 v130, v130\n"
"v_exp_f32_e32 v131, v131\n"
"v_mfma_f32_16x16x32_bf16 v[104:107], v[76:79], v[64:67], v[104:107]\n"
"s_waitcnt lgkmcnt(4)\n"
"v_cvt_pk_bf16_f32 v64, v120, v121\n"
"v_cvt_pk_bf16_f32 v65, v122, v123\n"
"v_cvt_pk_bf16_f32 v66, v128, v129\n"
"v_cvt_pk_bf16_f32 v67, v130, v131\n"
"s_cmp_eq_i32 s78, 6\n"
"s_cbranch_scc1 .Lmk_x25s\n"
".Lmk_x25r:\n"
"v_mfma_f32_16x16x32_bf16 v[76:79], v[84:87], v[28:31], v[24:27]\n"
"ds_read_b128 v[80:83], v15 offset:8192\n"
"v_exp_f32_e32 v72, v72\n"
"v_exp_f32_e32 v73, v73\n"
"v_exp_f32_e32 v74, v74\n"
"v_mfma_f32_16x16x32_bf16 v[120:123], v[84:87], v[32:35], v[36:39]\n"
"buffer_load_dwordx4 a[104:107], v0, s[4:7], s98 offen offset:2048 sc1\n"
"v_exp_f32_e32 v75, v75\n"
"v_mfma_f32_16x16x32_bf16 v[84:87], v[144:147], v[28:31], v[24:27]\n"
"ds_read_b128 v[124:127], v254 offset:8448\n"
"v_exp_f32_e32 v116, v116\n"
"v_exp_f32_e32 v117, v117\n"
"v_mfma_f32_16x16x32_bf16 v[128:131], v[144:147], v[32:35], v[36:39]\n"
"ds_read_b64_tr_b16 v[132:133], v16 offset:12288\n"
"v_exp_f32_e32 v118, v118\n"
"v_exp_f32_e32 v119, v119\n"
"s_waitcnt lgkmcnt(3)\n"
"v_mfma_f32_16x16x32_bf16 v[44:47], v[88:91], v[152:155], v[44:47]\n"
"buffer_load_dwordx4 a[108:111], v0, s[4:7], s98 offen offset:3072 sc1\n"
"v_cvt_pk_bf16_f32 v144, v72, v73\n"
"v_cvt_pk_bf16_f32 v145, v74, v75\n"
"v_cvt_pk_bf16_f32 v146, v116, v117\n"
"v_mfma_f32_16x16x32_bf16 v[40:43], v[108:111], v[152:155], v[40:43]\n"
"ds_read_b64_tr_b16 v[134:135], v255 offset:12544\n"
"v_cvt_pk_bf16_f32 v147, v118, v119\n"
"v_exp_f32_e32 v112, v112\n"
"v_exp_f32_e32 v113, v113\n"
"v_mfma_f32_16x16x32_bf16 v[136:139], v[100:103], v[152:155], v[136:139]\n"
"ds_read_b64_tr_b16 v[72:73], v255 offset:12288\n"
"v_exp_f32_e32 v114, v114\n"
"v_exp_f32_e32 v115, v115\n"
"v_mfma_f32_16x16x32_bf16 v[68:71], v[88:91], v[64:67], v[68:71]\n"
"buffer_load_dwordx4 a[112:115], v1, s[4:7], s98 offen sc1\n"
"v_exp_f32_e32 v148, v148\n"
"v_exp_f32_e32 v149, v149\n"
"v_mfma_f32_16x16x32_bf16 v[140:143], v[108:111], v[64:67], v[140:143]\n"
"ds_read_b64_tr_b16 v[74:75], v16 offset:12544\n"
"v_exp_f32_e32 v150, v150\n"
"v_exp_f32_e32 v151, v151\n"
"v_mfma_f32_16x16x32_bf16 v[104:107], v[100:103], v[64:67], v[104:107]\n"
"s_waitcnt lgkmcnt(4)\n"
"v_cvt_pk_bf16_f32 v64, v112, v113\n"
"v_cvt_pk_bf16_f32 v65, v114, v115\n"
"v_cvt_pk_bf16_f32 v66, v148, v149\n"
"v_cvt_pk_bf16_f32 v67, v150, v151\n"
"s_cmp_eq_i32 s78, 5\n"
"s_cbranch_scc1 .Lmk_x26s\n"
".Lmk_x26r:\n"
"v_mfma_f32_16x16x32_bf16 v[100:103], v[80:83], v[28:31], v[24:27]\n"
"ds_read_b64_tr_b16 v[108:109], v16 offset:10240\n"
"ds_read_b64_tr_b16 v[110:111], v255 offset:10496\n"
"v_exp_f32_e32 v76, v76\n"
"v_exp_f32_e32 v77, v77\n"
"v_mfma_f32_16x16x32_bf16 v[112:115], v[80:83], v[32:35], v[36:39]\n"
"s_add_u32 s97, s71, 0xfffff000\n"
"buffer_load_dwordx2 v[238:239], off, s[4:7], s97 sc1\n"
"v_exp_f32_e32 v78, v78\n"
"v_exp_f32_e32 v79, v79\n"
"v_mfma_f32_16x16x32_bf16 v[80:83], v[124:127], v[28:31], v[24:27]\n"
"ds_read_b64_tr_b16 v[116:117], v255 offset:10240\n"
"v_exp_f32_e32 v84, v84\n"
"v_exp_f32_e32 v85, v85\n"
"v_mfma_f32_16x16x32_bf16 v[148:151], v[124:127], v[32:35], v[36:39]\n"
"ds_read_b64_tr_b16 v[118:119], v16 offset:10496\n"
"buffer_load_dwordx4 a[116:119], v1, s[4:7], s98 offen offset:1024 sc1\n"
"v_exp_f32_e32 v86, v86\n"
"v_exp_f32_e32 v87, v87\n"
"s_waitcnt lgkmcnt(4)\n"
"v_mfma_f32_16x16x32_bf16 v[44:47], v[88:91], v[144:147], v[44:47]\n"
"ds_read_b64_tr_b16 v[36:37], v16 offset:24576\n"
"ds_read_b64_tr_b16 v[38:39], v255 offset:24832\n"
"v_cvt_pk_bf16_f32 v124, v76, v77\n"
"v_cvt_pk_bf16_f32 v125, v78, v79\n"
"v_cvt_pk_bf16_f32 v126, v84, v85\n"
"v_mfma_f32_16x16x32_bf16 v[40:43], v[132:135], v[144:147], v[40:43]\n"
"ds_read_b64_tr_b16 v[76:77], v255 offset:24576\n"
"buffer_load_dwordx4 a[120:123], v1, s[4:7], s98 offen offset:2048 sc1\n"
"v_cvt_pk_bf16_f32 v127, v86, v87\n"
"v_exp_f32_e32 v120, v120\n"
"v_mfma_f32_16x16x32_bf16 v[136:139], v[72:75], v[144:147], v[136:139]\n"
"ds_read_b64_tr_b16 v[78:79], v16 offset:24832\n"
"v_exp_f32_e32 v121, v121\n"
"v_exp_f32_e32 v122, v122\n"
"v_exp_f32_e32 v123, v123\n"
"v_mfma_f32_16x16x32_bf16 v[68:71], v[88:91], v[64:67], v[68:71]\n"
"v_exp_f32_e32 v128, v128\n"
"v_add_u32_e32 v248, 0x26400, v0\n"
"v_exp_f32_e32 v129, v129\n"
"v_mfma_f32_16x16x32_bf16 v[140:143], v[132:135], v[64:67], v[140:143]\n"
"buffer_load_dwordx4 a[124:127], v1, s[4:7], s98 offen offset:3072 sc1\n"
"ds_read_b128 v[84:87], v248\n"
"v_exp_f32_e32 v130, v130\n"
"v_exp_f32_e32 v131, v131\n"
"v_mfma_f32_16x16x32_bf16 v[104:107], v[72:75], v[64:67], v[104:107]\n"
"ds_read_b128 v[72:75], v248 offset:1024\n"
"v_cvt_pk_bf16_f32 v64, v120, v121\n"
"v_cvt_pk_bf16_f32 v65, v122, v123\n"
"v_cvt_pk_bf16_f32 v66, v128, v129\n"
"v_cvt_pk_bf16_f32 v67, v130, v131\n"
"s_waitcnt lgkmcnt(6)\n"
"v_mfma_f32_16x16x32_bf16 v[44:47], v[88:91], v[124:127], v[44:47]\n"
"ds_read_b64_tr_b16 v[120:121], v16 offset:8192\n"
"s_add_u32 s98, s69, 0x608000\n"
"buffer_load_dwordx4 v[24:27], v0, s[4:7], s98 offen sc1\n"
"ds_read_b64_tr_b16 v[122:123], v255 offset:8448\n"
"v_cndmask_b32_e64 v100, v48, v100, s[20:21]\n"
"v_cndmask_b32_e64 v101, v49, v101, s[22:23]\n"
"v_cndmask_b32_e64 v102, v50, v102, s[24:25]\n"
"v_cndmask_b32_e64 v103, v51, v103, s[26:27]\n"
"v_cndmask_b32_e64 v80, v56, v80, s[28:29]\n"
"v_cndmask_b32_e64 v81, v57, v81, s[30:31]\n"
"v_cndmask_b32_e64 v82, v58, v82, s[32:33]\n"
"v_mfma_f32_16x16x32_bf16 v[40:43], v[108:111], v[124:127], v[40:43]\n"
"ds_read_b64_tr_b16 v[48:49], v255 offset:8192\n"
"ds_read_b64_tr_b16 v[50:51], v16 offset:8448\n"
"v_cndmask_b32_e64 v83, v59, v83, s[34:35]\n"
"v_exp_f32_e32 v100, v100\n"
"v_exp_f32_e32 v101, v101\n"
"v_exp_f32_e32 v102, v102\n"
"s_waitcnt vmcnt(11)\n"
"v_exp_f32_e32 v103, v103\n"
"v_exp_f32_e32 v80, v80\n"
"v_mfma_f32_16x16x32_bf16 v[136:139], v[116:119], v[124:127], v[136:139]\n"
"v_add_f32_e32 v247, v247, v246\n"
"v_exp_f32_e32 v81, v81\n"
"v_exp_f32_e32 v82, v82\n"
"v_add_f32_e32 v247, v247, v245\n"
"v_exp_f32_e32 v83, v83\n"
"v_cvt_pk_bf16_f32 v56, v100, v101\n"
"v_add_f32_e32 v247, v247, v244\n"
"v_cvt_pk_bf16_f32 v57, v102, v103\n"
"v_cvt_pk_bf16_f32 v58, v80, v81\n"
"v_mfma_f32_16x16x32_bf16 v[68:71], v[88:91], v[64:67], v[68:71]\n"
"v_cvt_pk_bf16_f32 v59, v82, v83\n"
"v_add_f32_e32 v247, v247, v243\n"
"v_cndmask_b32_e64 v112, v52, v112, s[36:37]\n"
"v_cndmask_b32_e64 v113, v53, v113, s[38:39]\n"
"v_cndmask_b32_e64 v114, v54, v114, s[40:41]\n"
"v_cndmask_b32_e64 v115, v55, v115, s[42:43]\n"
"v_add_f32_e32 v247, v247, v242\n"
"v_cndmask_b32_e64 v148, v60, v148, s[44:45]\n"
"v_cndmask_b32_e64 v149, v61, v149, s[46:47]\n"
"v_cndmask_b32_e64 v150, v62, v150, s[48:49]\n"
"v_add_f32_e32 v247, v247, v241\n"
"v_cndmask_b32_e64 v151, v63, v151, s[50:51]\n"
"v_exp_f32_e32 v112, v112\n"
"v_mfma_f32_16x16x32_bf16 v[140:143], v[108:111], v[64:67], v[140:143]\n"
"v_exp_f32_e32 v113, v113\n"
"v_add_f32_e32 v247, v247, v240\n"
"v_exp_f32_e32 v114, v114\n"
"v_mov_b32_e32 v246, v247\n"
"v_exp_f32_e32 v115, v115\n"
"v_exp_f32_e32 v148, v148\n"
"v_permlane32_swap_b32_e32 v247, v246\n"
"v_exp_f32_e32 v149, v149\n"
"v_mfma_f32_16x16x32_bf16 v[104:107], v[116:119], v[64:67], v[104:107]\n"
"v_exp_f32_e32 v150, v150\n"
"v_add_f32_e32 v247, v247, v246\n"
"v_exp_f32_e32 v151, v151\n"
"v_cvt_pk_bf16_f32 v52, v112, v113\n"
"ds_write_b32 v21, v247 offset:512\n"
"v_cvt_pk_bf16_f32 v53, v114, v115\n"
"v_cvt_pk_bf16_f32 v54, v148, v149\n"
"v_cvt_pk_bf16_f32 v55, v150, v151\n"
"s_waitcnt lgkmcnt(1)\n"
"v_mfma_f32_16x16x32_bf16 v[44:47], v[88:91], v[56:59], v[44:47]\n"
"v_and_b32_e32 v84, v56, v84\n"
"v_and_b32_e32 v85, v57, v85\n"
"v_and_b32_e32 v86, v58, v86\n"
"v_and_b32_e32 v87, v59, v87\n"
"v_mfma_f32_16x16x32_bf16 v[68:71], v[88:91], v[52:55], v[68:71]\n"
"v_xor_b32_e32 v60, v56, v84\n"
"v_xor_b32_e32 v61, v57, v85\n"
"v_xor_b32_e32 v62, v58, v86\n"
"v_xor_b32_e32 v63, v59, v87\n"
"v_mfma_f32_16x16x32_bf16 v[40:43], v[120:123], v[84:87], v[40:43]\n"
"v_and_b32_e32 v72, v52, v72\n"
"v_and_b32_e32 v73, v53, v73\n"
"v_mfma_f32_16x16x32_bf16 v[136:139], v[48:51], v[84:87], v[136:139]\n"
"v_and_b32_e32 v74, v54, v74\n"
"v_and_b32_e32 v75, v55, v75\n"
"v_mfma_f32_16x16x32_bf16 v[40:43], v[36:39], v[60:63], v[40:43]\n"
"v_xor_b32_e32 v56, v52, v72\n"
"v_xor_b32_e32 v57, v53, v73\n"
"v_mfma_f32_16x16x32_bf16 v[136:139], v[76:79], v[60:63], v[136:139]\n"
"v_xor_b32_e32 v58, v54, v74\n"
"v_xor_b32_e32 v59, v55, v75\n"
"v_mfma_f32_16x16x32_bf16 v[140:143], v[120:123], v[72:75], v[140:143]\n"
"s_waitcnt vmcnt(4)\n"
"v_cmp_ne_u64_e32 vcc, v[238:239], v[18:19]\n"
"s_nop 0\n"
"s_cbranch_vccnz .Lmk_f19s\n"
".Lmk_f19r:\n"
"v_mfma_f32_16x16x32_bf16 v[104:107], v[48:51], v[72:75], v[104:107]\n"
"s_add_u32 m0, s77, 0x4000\n"
"s_add_u32 s97, s65, 0\n"
"buffer_load_dwordx4 v2, s[4:7], s97 offen sc1 lds\n"
"buffer_load_dwordx4 v2, s[4:7], s97 offen offset:1024 sc1 lds\n"
"v_mfma_f32_16x16x32_bf16 v[140:143], v[36:39], v[56:59], v[140:143]\n"
"s_add_u32 m0, s77, 0xc000\n"
"s_add_u32 s97, s65, 0x4000\n"
"buffer_load_dwordx4 v2, s[4:7], s97 offen sc1 lds\n"
"buffer_load_dwordx4 v2, s[4:7], s97 offen offset:1024 sc1 lds\n"
"v_mfma_f32_16x16x32_bf16 v[104:107], v[76:79], v[56:59], v[104:107]\n"
"v_max_f32_e32 v248, v44, v68\n"
"v_rcp_f32_e32 v44, v44\n"
"v_rcp_f32_e32 v68, v68\n"
"s_nop 0\n"
"v_max3_f32 v248, v248, v44, v68\n"
"v_cmp_nlt_f32_e32 vcc, v248, v252\n"
"s_cbranch_vccnz .Lmk_b21\n"
".Lmk_k27:\n"
"s_add_u32 s97, s60, 0x40000\n"
"v_mul_f32_e32 v40, v40, v44\n"
"v_mul_f32_e32 v41, v41, v44\n"
"v_mul_f32_e32 v42, v42, v44\n"
"v_mul_f32_e32 v43, v43, v44\n"
"v_mul_f32_e32 v136, v136, v44\n"
"v_mul_f32_e32 v137, v137, v44\n"
"v_mul_f32_e32 v138, v138, v44\n"
"v_mul_f32_e32 v139, v139, v44\n"
"v_cvt_pk_bf16_f32 v28, v40, v41\n"
"v_cvt_pk_bf16_f32 v29, v42, v43\n"
"v_cvt_pk_bf16_f32 v30, v136, v137\n"
"v_cvt_pk_bf16_f32 v31, v138, v139\n"
"buffer_store_dwordx4 v[28:31], v0, s[4:7], s97 offen sc1\n"
"s_add_u32 s97, s60, 0x48000\n"
"v_mul_f32_e32 v140, v140, v68\n"
"v_mul_f32_e32 v141, v141, v68\n"
"v_mul_f32_e32 v142, v142, v68\n"
"v_mul_f32_e32 v143, v143, v68\n"
"v_mul_f32_e32 v104, v104, v68\n"
"v_mul_f32_e32 v105, v105, v68\n"
"v_mul_f32_e32 v106, v106, v68\n"
"v_mul_f32_e32 v107, v107, v68\n"
"v_cvt_pk_bf16_f32 v28, v140, v141\n"
"v_cvt_pk_bf16_f32 v29, v142, v143\n"
"v_cvt_pk_bf16_f32 v30, v104, v105\n"
"v_cvt_pk_bf16_f32 v31, v106, v107\n"
"buffer_store_dwordx4 v[28:31], v0, s[4:7], s97 offen sc1\n"
";\n"
"; @A AT a\n"
"s_mov_b32 s2, 0\n"
"v_mov_b32_e32 v253, 0xff800000\n"
"v_mov_b32_e32 v251, 0xff800000\n"
"v_mov_b32_e32 v250, 0x7f800000\n"
"v_mov_b32_e32 v249, 0x7f800000\n"
".Lmk_a28:\n"
"ds_read_b128 v[28:31], v15 offset:16384\n"
"ds_read_b128 v[32:35], v254 offset:16640\n"
"ds_read_b128 v[36:39], v15 offset:14336\n"
"ds_read_b128 v[40:43], v254 offset:14592\n"
"s_cmp_gt_i32 s78, 7\n"
"s_cselect_b32 s97, 0x80000000, 0\n"
"s_or_b32 s97, s97, 0x7f800000\n"
"s_add_u32 s96, s67, 32\n"
"buffer_load_dwordx2 v[246:247], v12, s[4:7], s96 offen sc1\n"
"s_waitcnt lgkmcnt(2)\n"
"v_mfma_f32_16x16x32_bf16 v[100:103], v[28:31], v[92:95], 0\n"
"v_mfma_f32_16x16x32_bf16 v[104:107], v[28:31], v[96:99], 0\n"
"v_mfma_f32_16x16x32_bf16 v[108:111], v[32:35], v[92:95], 0\n"
"v_mfma_f32_16x16x32_bf16 v[112:115], v[32:35], v[96:99], 0\n"
"s_waitcnt lgkmcnt(0)\n"
"s_nop 3\n"
"v_max_f32_e32 v248, v100, v101\n"
"v_max_f32_e32 v245, v104, v105\n"
"v_max3_f32 v248, v248, v102, v103\n"
"v_max3_f32 v245, v245, v106, v107\n"
"v_max3_f32 v248, v248, v108, v109\n"
"v_max3_f32 v245, v245, v112, v113\n"
"v_max3_f32 v248, v248, v110, v111\n"
"v_max3_f32 v245, v245, v114, v115\n"
"v_mov_b32_e32 v244, v248\n"
"v_mov_b32_e32 v243, v245\n"
"s_nop 0\n"
"v_permlane16_swap_b32_e32 v248, v244\n"
"v_permlane16_swap_b32_e32 v245, v243\n"
"v_max_f32_e32 v248, v248, v244\n"
"v_max_f32_e32 v245, v245, v243\n"
"v_mov_b32_e32 v244, v248\n"
"v_mov_b32_e32 v243, v245\n"
"s_nop 0\n"
"v_permlane32_swap_b32_e32 v248, v244\n"
"v_permlane32_swap_b32_e32 v245, v243\n"
"v_max_f32_e32 v248, v248, v244\n"
"v_max_f32_e32 v245, v245, v243\n"
"v_med3_f32 v248, v248, v253, v250\n"
"v_med3_f32 v245, v245, v251, v249\n"
"v_min_f32_e64 v116, s97, -v248\n"
"v_min_f32_e64 v117, s97, -v248\n"
"v_min_f32_e64 v118, s97, -v248\n"
"v_min_f32_e64 v119, s97, -v248\n"
"v_min_f32_e64 v120, s97, -v245\n"
"v_min_f32_e64 v121, s97, -v245\n"
"v_min_f32_e64 v122, s97, -v245\n"
"v_min_f32_e64 v123, s97, -v245\n"
"v_mfma_f32_16x16x32_bf16 v[44:47], v[36:39], v[92:95], v[116:119]\n"
"ds_read_b128 v[32:35], v15 offset:12288\n"
"v_sub_f32_e32 v100, v100, v248\n"
"v_sub_f32_e32 v101, v101, v248\n"
"v_sub_f32_e32 v102, v102, v248\n"
"v_sub_f32_e32 v103, v103, v248\n"
"v_mfma_f32_16x16x32_bf16 v[48:51], v[36:39], v[96:99], v[120:123]\n"
"v_sub_f32_e32 v108, v108, v248\n"
"v_sub_f32_e32 v109, v109, v248\n"
"v_sub_f32_e32 v110, v110, v248\n"
"v_sub_f32_e32 v111, v111, v248\n"
"v_mfma_f32_16x16x32_bf16 v[36:39], v[40:43], v[92:95], v[116:119]\n"
"ds_read_b128 v[52:55], v254 offset:12544\n"
"v_sub_f32_e32 v104, v104, v245\n"
"v_sub_f32_e32 v105, v105, v245\n"
"v_sub_f32_e32 v106, v106, v245\n"
"v_sub_f32_e32 v107, v107, v245\n"
"v_mfma_f32_16x16x32_bf16 v[56:59], v[40:43], v[96:99], v[120:123]\n"
"s_waitcnt lgkmcnt(0)\n"
"v_sub_f32_e32 v112, v112, v245\n"
"v_sub_f32_e32 v113, v113, v245\n"
"v_sub_f32_e32 v114, v114, v245\n"
"v_sub_f32_e32 v115, v115, v245\n"
"s_cmp_eq_i32 s78, 7\n"
"s_cbranch_scc1 .Lmk_x30s\n"
".Lmk_x30r:\n"
"v_mfma_f32_16x16x32_bf16 v[60:63], v[32:35], v[92:95], v[116:119]\n"
"ds_read_b128 v[40:43], v15 offset:10240\n"
"buffer_load_dwordx4 v[28:31], v0, s[4:7], s98 offen offset:1024 sc1\n"
"ds_read_b128 v[64:67], v254 offset:10496\n"
"v_exp_f32_e32 v44, v44\n"
"v_exp_f32_e32 v45, v45\n"
"v_exp_f32_e32 v46, v46\n"
"v_exp_f32_e32 v47, v47\n"
"v_exp_f32_e32 v36, v36\n"
"v_mfma_f32_16x16x32_bf16 v[68:71], v[32:35], v[96:99], v[120:123]\n"
"ds_read_b64_tr_b16 v[72:73], v16 offset:14336\n"
"buffer_load_dwordx4 v[32:35], v0, s[4:7], s98 offen offset:2048 sc1\n"
"v_exp_f32_e32 v37, v37\n"
"v_exp_f32_e32 v38, v38\n"
"v_exp_f32_e32 v39, v39\n"
"v_cvt_pk_bf16_f32 v76, v44, v45\n"
"v_cvt_pk_bf16_f32 v77, v46, v47\n"
"v_cvt_pk_bf16_f32 v78, v36, v37\n"
"v_mfma_f32_16x16x32_bf16 v[80:83], v[52:55], v[92:95], v[116:119]\n"
"ds_read_b64_tr_b16 v[74:75], v255 offset:14592\n"
"ds_read_b64_tr_b16 v[84:85], v255 offset:14336\n"
"v_cvt_pk_bf16_f32 v79, v38, v39\n"
"v_exp_f32_e32 v48, v48\n"
"v_exp_f32_e32 v49, v49\n"
"v_exp_f32_e32 v50, v50\n"
"v_exp_f32_e32 v51, v51\n"
"v_mfma_f32_16x16x32_bf16 v[124:127], v[52:55], v[96:99], v[120:123]\n"
"buffer_load_dwordx4 v[36:39], v0, s[4:7], s98 offen offset:3072 sc1\n"
"ds_read_b64_tr_b16 v[86:87], v16 offset:14592\n"
"v_exp_f32_e32 v56, v56\n"
"v_exp_f32_e32 v57, v57\n"
"v_exp_f32_e32 v58, v58\n"
"v_exp_f32_e32 v59, v59\n"
"v_cvt_pk_bf16_f32 v52, v48, v49\n"
"v_cvt_pk_bf16_f32 v53, v50, v51\n"
"v_cvt_pk_bf16_f32 v54, v56, v57\n"
"v_cvt_pk_bf16_f32 v55, v58, v59\n"
"s_waitcnt lgkmcnt(4)\n"
"s_cmp_eq_i32 s78, 6\n"
"s_cbranch_scc1 .Lmk_x31s\n"
".Lmk_x31r:\n"
"v_mfma_f32_16x16x32_bf16 v[56:59], v[40:43], v[92:95], v[116:119]\n"
"ds_read_b128 v[128:131], v15 offset:8192\n"
"v_exp_f32_e32 v60, v60\n"
"v_exp_f32_e32 v61, v61\n"
"v_exp_f32_e32 v62, v62\n"
"v_mfma_f32_16x16x32_bf16 v[132:135], v[40:43], v[96:99], v[120:123]\n"
"buffer_load_dwordx4 v[40:43], v1, s[4:7], s98 offen sc1\n"
"v_exp_f32_e32 v63, v63\n"
"v_mfma_f32_16x16x32_bf16 v[136:139], v[64:67], v[92:95], v[116:119]\n"
"ds_read_b128 v[140:143], v254 offset:8448\n"
"v_exp_f32_e32 v80, v80\n"
"v_exp_f32_e32 v81, v81\n"
"v_mfma_f32_16x16x32_bf16 v[144:147], v[64:67], v[96:99], v[120:123]\n"
"ds_read_b64_tr_b16 v[64:65], v16 offset:12288\n"
"v_exp_f32_e32 v82, v82\n"
"v_exp_f32_e32 v83, v83\n"
"s_waitcnt lgkmcnt(3)\n"
"v_mfma_f32_16x16x32_bf16 v[148:151], v[88:91], v[76:79], 0\n"
"buffer_load_dwordx4 v[44:47], v1, s[4:7], s98 offen offset:1024 sc1\n"
"v_cvt_pk_bf16_f32 v152, v60, v61\n"
"v_cvt_pk_bf16_f32 v153, v62, v63\n"
"v_cvt_pk_bf16_f32 v154, v80, v81\n"
"v_mfma_f32_16x16x32_bf16 v[156:159], v[72:75], v[76:79], 0\n"
"ds_read_b64_tr_b16 v[66:67], v255 offset:12544\n"
"v_cvt_pk_bf16_f32 v155, v82, v83\n"
"v_exp_f32_e32 v68, v68\n"
"v_exp_f32_e32 v69, v69\n"
"v_mfma_f32_16x16x32_bf16 v[160:163], v[84:87], v[76:79], 0\n"
"ds_read_b64_tr_b16 v[76:77], v255 offset:12288\n"
"v_exp_f32_e32 v70, v70\n"
"v_exp_f32_e32 v71, v71\n"
"v_mfma_f32_16x16x32_bf16 v[164:167], v[88:91], v[52:55], 0\n"
"buffer_load_dwordx4 v[48:51], v1, s[4:7], s98 offen offset:2048 sc1\n"
"v_exp_f32_e32 v124, v124\n"
"v_exp_f32_e32 v125, v125\n"
"v_mfma_f32_16x16x32_bf16 v[168:171], v[72:75], v[52:55], 0\n"
"ds_read_b64_tr_b16 v[78:79], v16 offset:12544\n"
"v_exp_f32_e32 v126, v126\n"
"v_exp_f32_e32 v127, v127\n"
"v_mfma_f32_16x16x32_bf16 v[172:175], v[84:87], v[52:55], 0\n"
"s_waitcnt lgkmcnt(4)\n"
"v_cvt_pk_bf16_f32 v72, v68, v69\n"
"v_cvt_pk_bf16_f32 v73, v70, v71\n"
"v_cvt_pk_bf16_f32 v74, v124, v125\n"
"v_cvt_pk_bf16_f32 v75, v126, v127\n"
"s_cmp_eq_i32 s78, 5\n"
"s_cbranch_scc1 .Lmk_x32s\n"
".Lmk_x32r:\n"
"v_mfma_f32_16x16x32_bf16 v[80:83], v[128:131], v[92:95], v[116:119]\n"
"ds_read_b64_tr_b16 v[68:69], v16 offset:10240\n"
"v_exp_f32_e32 v56, v56\n"
"v_exp_f32_e32 v57, v57\n"
"v_exp_f32_e32 v58, v58\n"
"v_mfma_f32_16x16x32_bf16 v[84:87], v[128:131], v[96:99], v[120:123]\n"
"buffer_load_dwordx4 v[52:55], v1, s[4:7], s98 offen offset:3072 sc1\n"
"v_exp_f32_e32 v59, v59\n"
"v_mfma_f32_16x16x32_bf16 v[124:127], v[140:143], v[92:95], v[116:119]\n"
"ds_read_b64_tr_b16 v[70:71], v255 offset:10496\n"
"v_exp_f32_e32 v136, v136\n"
"v_exp_f32_e32 v137, v137\n"
"v_mfma_f32_16x16x32_bf16 v[128:131], v[140:143], v[96:99], v[120:123]\n"
"s_waitcnt lgkmcnt(2)\n"
"v_exp_f32_e32 v138, v138\n"
"v_exp_f32_e32 v139, v139\n"
"v_cvt_pk_bf16_f32 v140, v56, v57\n"
"v_mfma_f32_16x16x32_bf16 v[148:151], v[88:91], v[152:155], v[148:151]\n"
"s_add_u32 s97, s69, 0x60a000\n"
"v_cvt_pk_bf16_f32 v141, v58, v59\n"
"v_cvt_pk_bf16_f32 v142, v136, v137\n"
"v_mfma_f32_16x16x32_bf16 v[156:159], v[64:67], v[152:155], v[156:159]\n"
"buffer_load_dwordx4 v[56:59], v0, s[4:7], s97 offen sc1\n"
"ds_read_b64_tr_b16 v[136:137], v255 offset:10240\n"
"v_cvt_pk_bf16_f32 v143, v138, v139\n"
"v_exp_f32_e32 v132, v132\n"
"v_exp_f32_e32 v133, v133\n"
"v_mfma_f32_16x16x32_bf16 v[160:163], v[76:79], v[152:155], v[160:163]\n"
"v_exp_f32_e32 v134, v134\n"
"v_exp_f32_e32 v135, v135\n"
"v_mfma_f32_16x16x32_bf16 v[164:167], v[88:91], v[72:75], v[164:167]\n"
"buffer_load_dwordx4 v[60:63], v0, s[4:7], s97 offen offset:1024 sc1\n"
"ds_read_b64_tr_b16 v[138:139], v16 offset:10496\n"
"v_exp_f32_e32 v144, v144\n"
"v_mfma_f32_16x16x32_bf16 v[168:171], v[64:67], v[72:75], v[168:171]\n"
"v_exp_f32_e32 v145, v145\n"
"v_exp_f32_e32 v146, v146\n"
"v_exp_f32_e32 v147, v147\n"
"v_mfma_f32_16x16x32_bf16 v[172:175], v[76:79], v[72:75], v[172:175]\n"
"s_waitcnt vmcnt(12)\n"
"v_cvt_pk_bf16_f32 v72, v132, v133\n"
"v_cvt_pk_bf16_f32 v73, v134, v135\n"
"v_cvt_pk_bf16_f32 v74, v144, v145\n"
"v_cvt_pk_bf16_f32 v75, v146, v147\n"
"s_cmp_lg_u32 s2, 0\n"
"s_cbranch_scc1 .Lmk_s33\n"
"s_barrier\n"
".Lmk_s33:\n"
"ds_read_b128 v[76:79], v15 offset:6144\n"
"ds_read_b128 v[132:135], v254 offset:6400\n"
"s_waitcnt lgkmcnt(2)\n"
"v_mfma_f32_16x16x32_bf16 v[148:151], v[88:91], v[140:143], v[148:151]\n"
"ds_read_b128 v[144:147], v15 offset:4096\n"
"v_exp_f32_e32 v80, v80\n"
"v_exp_f32_e32 v81, v81\n"
"v_exp_f32_e32 v82, v82\n"
"v_mfma_f32_16x16x32_bf16 v[156:159], v[68:71], v[140:143], v[156:159]\n"
"buffer_load_dwordx4 v[64:67], v0, s[4:7], s97 offen offset:2048 sc1\n"
"ds_read_b128 v[152:155], v254 offset:4352\n"
"v_exp_f32_e32 v83, v83\n"
"v_mfma_f32_16x16x32_bf16 v[160:163], v[136:139], v[140:143], v[160:163]\n"
"ds_read_b64_tr_b16 v[140:141], v16 offset:8192\n"
"v_exp_f32_e32 v124, v124\n"
"v_exp_f32_e32 v125, v125\n"
"v_mfma_f32_16x16x32_bf16 v[164:167], v[88:91], v[72:75], v[164:167]\n"
"ds_read_b64_tr_b16 v[142:143], v255 offset:8448\n"
"v_exp_f32_e32 v126, v126\n"
"v_exp_f32_e32 v127, v127\n"
"v_mfma_f32_16x16x32_bf16 v[168:171], v[68:71], v[72:75], v[168:171]\n"
"buffer_load_dwordx4 v[68:71], v0, s[4:7], s97 offen offset:3072 sc1\n"
"v_cvt_pk_bf16_f32 v176, v80, v81\n"
"v_cvt_pk_bf16_f32 v177, v82, v83\n"
"v_cvt_pk_bf16_f32 v178, v124, v125\n"
"v_cvt_pk_bf16_f32 v179, v126, v127\n"
"v_mfma_f32_16x16x32_bf16 v[172:175], v[136:139], v[72:75], v[172:175]\n"
"ds_read_b64_tr_b16 v[124:125], v255 offset:8192\n"
"v_exp_f32_e32 v84, v84\n"
"v_exp_f32_e32 v85, v85\n"
"s_waitcnt lgkmcnt(5)\n"
"v_mfma_f32_16x16x32_bf16 v[136:139], v[76:79], v[92:95], v[116:119]\n"
"ds_read_b64_tr_b16 v[126:127], v16 offset:8448\n"
"v_exp_f32_e32 v86, v86\n"
"v_exp_f32_e32 v87, v87\n"
"v_exp_f32_e32 v128, v128\n"
"v_mfma_f32_16x16x32_bf16 v[180:183], v[76:79], v[96:99], v[120:123]\n"
"buffer_load_dwordx4 v[72:75], v1, s[4:7], s97 offen sc1\n"
"s_waitcnt vmcnt(12)\n"
"v_cmp_ne_u64_e32 vcc, v[246:247], v[18:19]\n"
"v_mfma_f32_16x16x32_bf16 v[184:187], v[132:135], v[92:95], v[116:119]\n"
"v_exp_f32_e32 v129, v129\n"
"v_exp_f32_e32 v130, v130\n"
"v_exp_f32_e32 v131, v131\n"
"v_mfma_f32_16x16x32_bf16 v[188:191], v[132:135], v[96:99], v[120:123]\n"
"s_cbranch_vccnz .Lmk_f4s\n"
".Lmk_f4r:\n"
"v_cvt_pk_bf16_f32 v132, v84, v85\n"
"v_cvt_pk_bf16_f32 v133, v86, v87\n"
"v_cvt_pk_bf16_f32 v134, v128, v129\n"
"v_cvt_pk_bf16_f32 v135, v130, v131\n"
"s_waitcnt lgkmcnt(4)\n"
"v_mfma_f32_16x16x32_bf16 v[128:131], v[144:147], v[92:95], v[116:119]\n"
"ds_read_b128 v[192:195], v15 offset:2048\n"
"v_exp_f32_e32 v136, v136\n"
"v_exp_f32_e32 v137, v137\n"
"v_exp_f32_e32 v138, v138\n"
"v_mfma_f32_16x16x32_bf16 v[196:199], v[144:147], v[96:99], v[120:123]\n"
"buffer_load_dwordx4 v[76:79], v1, s[4:7], s97 offen offset:1024 sc1\n"
"v_exp_f32_e32 v139, v139\n"
"v_mfma_f32_16x16x32_bf16 v[144:147], v[152:155], v[92:95], v[116:119]\n"
"ds_read_b128 v[200:203], v254 offset:2304\n"
"v_exp_f32_e32 v184, v184\n"
"v_exp_f32_e32 v185, v185\n"
"v_mfma_f32_16x16x32_bf16 v[204:207], v[152:155], v[96:99], v[120:123]\n"
"ds_read_b64_tr_b16 v[152:153], v16 offset:6144\n"
"v_exp_f32_e32 v186, v186\n"
"v_exp_f32_e32 v187, v187\n"
"s_waitcnt lgkmcnt(3)\n"
"v_mfma_f32_16x16x32_bf16 v[148:151], v[88:91], v[176:179], v[148:151]\n"
"buffer_load_dwordx4 v[80:83], v1, s[4:7], s97 offen offset:2048 sc1\n"
"v_cvt_pk_bf16_f32 v208, v136, v137\n"
"v_cvt_pk_bf16_f32 v209, v138, v139\n"
"v_cvt_pk_bf16_f32 v210, v184, v185\n"
"v_mfma_f32_16x16x32_bf16 v[156:159], v[140:143], v[176:179], v[156:159]\n"
"ds_read_b64_tr_b16 v[154:155], v255 offset:6400\n"
"v_cvt_pk_bf16_f32 v211, v186, v187\n"
"v_exp_f32_e32 v180, v180\n"
"v_exp_f32_e32 v181, v181\n"
"v_mfma_f32_16x16x32_bf16 v[160:163], v[124:127], v[176:179], v[160:163]\n"
"ds_read_b64_tr_b16 v[136:137], v255 offset:6144\n"
"v_exp_f32_e32 v182, v182\n"
"v_exp_f32_e32 v183, v183\n"
"v_mfma_f32_16x16x32_bf16 v[164:167], v[88:91], v[132:135], v[164:167]\n"
"buffer_load_dwordx4 v[84:87], v1, s[4:7], s97 offen offset:3072 sc1\n"
"v_exp_f32_e32 v188, v188\n"
"v_exp_f32_e32 v189, v189\n"
"v_mfma_f32_16x16x32_bf16 v[168:171], v[140:143], v[132:135], v[168:171]\n"
"ds_read_b64_tr_b16 v[138:139], v16 offset:6400\n"
"v_exp_f32_e32 v190, v190\n"
"v_exp_f32_e32 v191, v191\n"
"v_mfma_f32_16x16x32_bf16 v[172:175], v[124:127], v[132:135], v[172:175]\n"
"s_waitcnt lgkmcnt(4)\n"
"v_cvt_pk_bf16_f32 v124, v180, v181\n"
"v_cvt_pk_bf16_f32 v125, v182, v183\n"
"v_cvt_pk_bf16_f32 v126, v188, v189\n"
"v_cvt_pk_bf16_f32 v127, v190, v191\n"
"v_mfma_f32_16x16x32_bf16 v[132:135], v[192:195], v[92:95], v[116:119]\n"
"ds_read_b128 v[140:143], v15\n"
"v_exp_f32_e32 v128, v128\n"
"v_exp_f32_e32 v129, v129\n"
"v_exp_f32_e32 v130, v130\n"
"v_mfma_f32_16x16x32_bf16 v[176:179], v[192:195], v[96:99], v[120:123]\n"
"s_add_u32 s97, s56, 0x40000\n"
"buffer_load_dwordx4 v[180:183], v0, s[4:7], s97 offen sc1\n"
"v_exp_f32_e32 v131, v131\n"
"v_mfma_f32_16x16x32_bf16 v[184:187], v[200:203], v[92:95], v[116:119]\n"
"ds_read_b128 v[188:191], v254 offset:256\n"
"v_exp_f32_e32 v144, v144\n"
"v_exp_f32_e32 v145, v145\n"
"v_exp_f32_e32 v146, v146\n"
"v_mfma_f32_16x16x32_bf16 v[192:195], v[200:203], v[96:99], v[120:123]\n"
"buffer_load_dwordx4 v[200:203], v0, s[4:7], s97 offen offset:1024 sc1\n"
"ds_read_b64_tr_b16 v[212:213], v16 offset:4096\n"
"v_exp_f32_e32 v147, v147\n"
"s_waitcnt lgkmcnt(3)\n"
"v_mfma_f32_16x16x32_bf16 v[148:151], v[88:91], v[208:211], v[148:151]\n"
"v_cvt_pk_bf16_f32 v216, v128, v129\n"
"v_cvt_pk_bf16_f32 v217, v130, v131\n"
"v_cvt_pk_bf16_f32 v218, v144, v145\n"
"v_cvt_pk_bf16_f32 v219, v146, v147\n"
"v_mfma_f32_16x16x32_bf16 v[156:159], v[152:155], v[208:211], v[156:159]\n"
"buffer_load_dwordx4 v[128:131], v0, s[4:7], s97 offen offset:2048 sc1\n"
"ds_read_b64_tr_b16 v[214:215], v255 offset:4352\n"
"v_exp_f32_e32 v196, v196\n"
"v_exp_f32_e32 v197, v197\n"
"v_mfma_f32_16x16x32_bf16 v[160:163], v[136:139], v[208:211], v[160:163]\n"
"buffer_load_dwordx4 v[144:147], v0, s[4:7], s97 offen offset:3072 sc1\n"
"v_exp_f32_e32 v198, v198\n"
"v_exp_f32_e32 v199, v199\n"
"v_mfma_f32_16x16x32_bf16 v[164:167], v[88:91], v[124:127], v[164:167]\n"
"ds_read_b64_tr_b16 v[208:209], v255 offset:4096\n"
"v_exp_f32_e32 v204, v204\n"
"v_exp_f32_e32 v205, v205\n"
"v_mfma_f32_16x16x32_bf16 v[168:171], v[152:155], v[124:127], v[168:171]\n"
"buffer_load_dwordx4 v[152:155], v1, s[4:7], s97 offen sc1\n"
"ds_read_b64_tr_b16 v[210:211], v16 offset:4352\n"
"v_exp_f32_e32 v206, v206\n"
"v_exp_f32_e32 v207, v207\n"
"v_mfma_f32_16x16x32_bf16 v[172:175], v[136:139], v[124:127], v[172:175]\n"
"s_waitcnt lgkmcnt(4)\n"
"v_cvt_pk_bf16_f32 v124, v196, v197\n"
"v_cvt_pk_bf16_f32 v125, v198, v199\n"
"v_cvt_pk_bf16_f32 v126, v204, v205\n"
"v_cvt_pk_bf16_f32 v127, v206, v207\n"
"v_mfma_f32_16x16x32_bf16 v[136:139], v[140:143], v[92:95], v[116:119]\n"
"ds_read_b64_tr_b16 v[196:197], v16 offset:2048\n"
"ds_read_b64_tr_b16 v[198:199], v255 offset:2304\n"
"buffer_load_dwordx4 v[204:207], v1, s[4:7], s97 offen offset:1024 sc1\n"
"v_exp_f32_e32 v132, v132\n"
"v_exp_f32_e32 v133, v133\n"
"v_mfma_f32_16x16x32_bf16 v[220:223], v[140:143], v[96:99], v[120:123]\n"
"ds_read_b64_tr_b16 v[140:141], v255 offset:2048\n"
"v_exp_f32_e32 v134, v134\n"
"v_exp_f32_e32 v135, v135\n"
"v_mfma_f32_16x16x32_bf16 v[224:227], v[188:191], v[92:95], v[116:119]\n"
"buffer_load_dwordx4 v[116:119], v1, s[4:7], s97 offen offset:2048 sc1\n"
"v_exp_f32_e32 v184, v184\n"
"v_exp_f32_e32 v185, v185\n"
"v_mfma_f32_16x16x32_bf16 v[228:231], v[188:191], v[96:99], v[120:123]\n"
"ds_read_b64_tr_b16 v[142:143], v16 offset:2304\n"
"ds_read_b64_tr_b16 v[120:121], v16 offset:16384\n"
"v_exp_f32_e32 v186, v186\n"
"v_exp_f32_e32 v187, v187\n"
"s_waitcnt lgkmcnt(5)\n"
"v_mfma_f32_16x16x32_bf16 v[148:151], v[88:91], v[216:219], v[148:151]\n"
"v_cvt_pk_bf16_f32 v188, v132, v133\n"
"v_cvt_pk_bf16_f32 v189, v134, v135\n"
"v_cvt_pk_bf16_f32 v190, v184, v185\n"
"v_mfma_f32_16x16x32_bf16 v[156:159], v[212:215], v[216:219], v[156:159]\n"
"buffer_load_dwordx4 v[132:135], v1, s[4:7], s97 offen offset:3072 sc1\n"
"ds_read_b64_tr_b16 v[122:123], v255 offset:16640\n"
"ds_read_b64_tr_b16 v[184:185], v255 offset:16384\n"
"v_cvt_pk_bf16_f32 v191, v186, v187\n"
"v_exp_f32_e32 v176, v176\n"
"v_exp_f32_e32 v177, v177\n"
"v_exp_f32_e32 v178, v178\n"
"v_mfma_f32_16x16x32_bf16 v[160:163], v[208:211], v[216:219], v[160:163]\n"
"s_add_u32 s97, s57, 0x40000\n"
"buffer_load_dwordx4 v[216:219], v0, s[4:7], s97 offen sc1\n"
"ds_read_b64_tr_b16 v[186:187], v16 offset:16640\n"
"v_exp_f32_e32 v179, v179\n"
"v_mfma_f32_16x16x32_bf16 v[164:167], v[88:91], v[124:127], v[164:167]\n"
"v_exp_f32_e32 v192, v192\n"
"v_add_u32_e32 v248, 0x26400, v0\n"
"v_exp_f32_e32 v193, v193\n"
"v_mfma_f32_16x16x32_bf16 v[168:171], v[212:215], v[124:127], v[168:171]\n"
"buffer_load_dwordx4 v[212:215], v0, s[4:7], s97 offen offset:1024 sc1\n"
"ds_read_b128 v[232:235], v248\n"
"v_exp_f32_e32 v194, v194\n"
"v_exp_f32_e32 v195, v195\n"
"v_mfma_f32_16x16x32_bf16 v[172:175], v[208:211], v[124:127], v[172:175]\n"
"ds_read_b128 v[208:211], v248 offset:1024\n"
"v_cvt_pk_bf16_f32 v124, v176, v177\n"
"v_cvt_pk_bf16_f32 v125, v178, v179\n"
"v_cvt_pk_bf16_f32 v126, v192, v193\n"
"v_cvt_pk_bf16_f32 v127, v194, v195\n"
"s_waitcnt lgkmcnt(6)\n"
"v_mfma_f32_16x16x32_bf16 v[148:151], v[88:91], v[188:191], v[148:151]\n"
"ds_read_b64_tr_b16 v[176:177], v16\n"
"buffer_load_dwordx4 v[192:195], v0, s[4:7], s97 offen offset:2048 sc1\n"
"v_cndmask_b32_e64 v136, v100, v136, s[20:21]\n"
"v_cndmask_b32_e64 v137, v101, v137, s[22:23]\n"
"v_cndmask_b32_e64 v138, v102, v138, s[24:25]\n"
"v_cndmask_b32_e64 v139, v103, v139, s[26:27]\n"
"v_cndmask_b32_e64 v224, v108, v224, s[28:29]\n"
"v_cndmask_b32_e64 v225, v109, v225, s[30:31]\n"
"v_cndmask_b32_e64 v226, v110, v226, s[32:33]\n"
"v_cndmask_b32_e64 v227, v111, v227, s[34:35]\n"
"v_exp_f32_e32 v136, v136\n"
"v_mfma_f32_16x16x32_bf16 v[156:159], v[196:199], v[188:191], v[156:159]\n"
"ds_read_b64_tr_b16 v[178:179], v255 offset:256\n"
"buffer_load_dwordx4 v[100:103], v0, s[4:7], s97 offen offset:3072 sc1\n"
"v_exp_f32_e32 v137, v137\n"
"v_exp_f32_e32 v138, v138\n"
"v_exp_f32_e32 v139, v139\n"
"v_exp_f32_e32 v224, v224\n"
"v_mfma_f32_16x16x32_bf16 v[160:163], v[140:143], v[188:191], v[160:163]\n"
"buffer_load_dwordx4 v[188:191], v1, s[4:7], s97 offen sc1\n"
"v_exp_f32_e32 v225, v225\n"
"v_exp_f32_e32 v226, v226\n"
"v_exp_f32_e32 v227, v227\n"
"v_cvt_pk_bf16_f32 v108, v136, v137\n"
"v_cvt_pk_bf16_f32 v109, v138, v139\n"
"v_cvt_pk_bf16_f32 v110, v224, v225\n"
"v_mfma_f32_16x16x32_bf16 v[164:167], v[88:91], v[124:127], v[164:167]\n"
"ds_read_b64_tr_b16 v[136:137], v255\n"
"v_cvt_pk_bf16_f32 v111, v226, v227\n"
"v_cndmask_b32_e64 v220, v104, v220, s[36:37]\n"
"v_cndmask_b32_e64 v221, v105, v221, s[38:39]\n"
"v_cndmask_b32_e64 v222, v106, v222, s[40:41]\n"
"v_cndmask_b32_e64 v223, v107, v223, s[42:43]\n"
"v_cndmask_b32_e64 v228, v112, v228, s[44:45]\n"
"v_cndmask_b32_e64 v229, v113, v229, s[46:47]\n"
"v_cndmask_b32_e64 v230, v114, v230, s[48:49]\n"
"v_cndmask_b32_e64 v231, v115, v231, s[50:51]\n"
"v_mfma_f32_16x16x32_bf16 v[168:171], v[196:199], v[124:127], v[168:171]\n"
"buffer_load_dwordx4 v[104:107], v1, s[4:7], s97 offen offset:1024 sc1\n"
"ds_read_b64_tr_b16 v[138:139], v16 offset:256\n"
"buffer_load_dwordx4 v[112:115], v1, s[4:7], s97 offen offset:2048 sc1\n"
"v_exp_f32_e32 v220, v220\n"
"v_exp_f32_e32 v221, v221\n"
"v_exp_f32_e32 v222, v222\n"
"v_exp_f32_e32 v223, v223\n"
"v_exp_f32_e32 v228, v228\n"
"v_mfma_f32_16x16x32_bf16 v[172:175], v[140:143], v[124:127], v[172:175]\n"
"s_waitcnt lgkmcnt(0)\n"
"v_exp_f32_e32 v229, v229\n"
"v_exp_f32_e32 v230, v230\n"
"v_exp_f32_e32 v231, v231\n"
"v_cvt_pk_bf16_f32 v124, v220, v221\n"
"v_cvt_pk_bf16_f32 v125, v222, v223\n"
"v_cvt_pk_bf16_f32 v126, v228, v229\n"
"v_cvt_pk_bf16_f32 v127, v230, v231\n"
"v_mfma_f32_16x16x32_bf16 v[148:151], v[88:91], v[108:111], v[148:151]\n"
"v_and_b32_e32 v232, v108, v232\n"
"v_and_b32_e32 v233, v109, v233\n"
"v_and_b32_e32 v234, v110, v234\n"
"v_and_b32_e32 v235, v111, v235\n"
"v_mfma_f32_16x16x32_bf16 v[164:167], v[88:91], v[124:127], v[164:167]\n"
"v_xor_b32_e32 v140, v108, v232\n"
"v_xor_b32_e32 v141, v109, v233\n"
"v_xor_b32_e32 v142, v110, v234\n"
"v_xor_b32_e32 v143, v111, v235\n"
"v_mfma_f32_16x16x32_bf16 v[156:159], v[176:179], v[232:235], v[156:159]\n"
"v_and_b32_e32 v208, v124, v208\n"
"v_and_b32_e32 v209, v125, v209\n"
"v_mfma_f32_16x16x32_bf16 v[160:163], v[136:139], v[232:235], v[160:163]\n"
"v_and_b32_e32 v210, v126, v210\n"
"v_and_b32_e32 v211, v127, v211\n"
"v_mfma_f32_16x16x32_bf16 v[156:159], v[120:123], v[140:143], v[156:159]\n"
"v_xor_b32_e32 v108, v124, v208\n"
"v_xor_b32_e32 v109, v125, v209\n"
"v_mfma_f32_16x16x32_bf16 v[160:163], v[184:187], v[140:143], v[160:163]\n"
"v_xor_b32_e32 v110, v126, v210\n"
"v_xor_b32_e32 v111, v127, v211\n"
"v_mfma_f32_16x16x32_bf16 v[168:171], v[176:179], v[208:211], v[168:171]\n"
"buffer_load_dwordx4 v[124:127], v1, s[4:7], s97 offen offset:3072 sc1\n"
"s_add_u32 s97, s58, 0x40000\n"
"buffer_load_dwordx4 v[140:143], v0, s[4:7], s97 offen sc1\n"
"v_mfma_f32_16x16x32_bf16 v[172:175], v[136:139], v[208:211], v[172:175]\n"
"buffer_load_dwordx4 v[136:139], v0, s[4:7], s97 offen offset:1024 sc1\n"
"v_mfma_f32_16x16x32_bf16 v[168:171], v[120:123], v[108:111], v[168:171]\n"
"buffer_load_dwordx4 v[120:123], v0, s[4:7], s97 offen offset:2048 sc1\n"
"v_mfma_f32_16x16x32_bf16 v[172:175], v[184:187], v[108:111], v[172:175]\n"
"buffer_load_dwordx4 v[108:111], v0, s[4:7], s97 offen offset:3072 sc1\n"
"v_max_f32_e32 v248, v148, v164\n"
"v_rcp_f32_e32 v148, v148\n"
"v_rcp_f32_e32 v164, v164\n"
"s_nop 0\n"
"v_max3_f32 v248, v248, v148, v164\n"
"v_cmp_nlt_f32_e32 vcc, v248, v252\n"
"s_cbranch_vccnz .Lmk_b29\n"
".Lmk_k34:\n"
"v_mul_f32_e32 v156, v156, v148\n"
"v_mul_f32_e32 v157, v157, v148\n"
"v_mul_f32_e32 v158, v158, v148\n"
"v_mul_f32_e32 v159, v159, v148\n"
"v_mul_f32_e32 v160, v160, v148\n"
"v_mul_f32_e32 v161, v161, v148\n"
"v_mul_f32_e32 v162, v162, v148\n"
"v_mul_f32_e32 v163, v163, v148\n"
"v_cvt_pk_bf16_f32 v88, v156, v157\n"
"v_cvt_pk_bf16_f32 v89, v158, v159\n"
"v_cvt_pk_bf16_f32 v90, v160, v161\n"
"v_cvt_pk_bf16_f32 v91, v162, v163\n"
"buffer_store_dwordx4 v[88:91], v0, s[4:7], s60 offen sc1\n"
"s_add_u32 s98, s60, 0x8000\n"
"v_mul_f32_e32 v168, v168, v164\n"
"v_mul_f32_e32 v169, v169, v164\n"
"v_mul_f32_e32 v170, v170, v164\n"
"v_mul_f32_e32 v171, v171, v164\n"
"v_mul_f32_e32 v172, v172, v164\n"
"v_mul_f32_e32 v173, v173, v164\n"
"v_mul_f32_e32 v174, v174, v164\n"
"v_mul_f32_e32 v175, v175, v164\n"
"v_cvt_pk_bf16_f32 v88, v168, v169\n"
"v_cvt_pk_bf16_f32 v89, v170, v171\n"
"v_cvt_pk_bf16_f32 v90, v172, v173\n"
"v_cvt_pk_bf16_f32 v91, v174, v175\n"
"buffer_store_dwordx4 v[88:91], v0, s[4:7], s98 offen sc1\n"
";\n"
"s_waitcnt vmcnt(20)\n"
"; @A G2 b.0\n"
"s_nop 0\n"
"v_mfma_f32_16x16x32_bf16 v[88:91], a[128:131], v[180:183], 0\n"
"v_mfma_f32_16x16x32_bf16 v[88:91], a[132:135], v[200:203], v[88:91]\n"
"s_waitcnt vmcnt(18)\n"
"v_mfma_f32_16x16x32_bf16 v[88:91], a[136:139], v[128:131], v[88:91]\n"
"buffer_load_dwordx4 v[92:95], v1, s[4:7], s97 offen sc1\n"
"v_mfma_f32_16x16x32_bf16 v[88:91], a[140:143], v[144:147], v[88:91]\n"
"s_waitcnt vmcnt(17)\n"
"v_mfma_f32_16x16x32_bf16 v[88:91], a[144:147], v[152:155], v[88:91]\n"
"v_mfma_f32_16x16x32_bf16 v[88:91], a[148:151], v[204:207], v[88:91]\n"
"s_waitcnt vmcnt(15)\n"
"v_mfma_f32_16x16x32_bf16 v[88:91], a[152:155], v[116:119], v[88:91]\n"
"buffer_load_dwordx4 v[96:99], v1, s[4:7], s97 offen offset:1024 sc1\n"
"v_mfma_f32_16x16x32_bf16 v[88:91], a[156:159], v[132:135], v[88:91]\n"
"v_mfma_f32_16x16x32_bf16 v[148:151], a[160:163], v[180:183], 0\n"
"v_mfma_f32_16x16x32_bf16 v[148:151], a[164:167], v[200:203], v[148:151]\n"
"v_mfma_f32_16x16x32_bf16 v[148:151], a[168:171], v[128:131], v[148:151]\n"
"buffer_load_dwordx4 v[156:159], v1, s[4:7], s97 offen offset:2048 sc1\n"
"v_mfma_f32_16x16x32_bf16 v[148:151], a[172:175], v[144:147], v[148:151]\n"
"v_mfma_f32_16x16x32_bf16 v[148:151], a[176:179], v[152:155], v[148:151]\n"
"s_nop 1\n"
"v_cmp_u_f32_e32 vcc, v88, v88\n"
"v_mfma_f32_16x16x32_bf16 v[148:151], a[180:183], v[204:207], v[148:151]\n"
"s_cbranch_vccnz .Lmk_p35s\n"
".Lmk_p35r:\n"
"v_mfma_f32_16x16x32_bf16 v[148:151], a[184:187], v[116:119], v[148:151]\n"
"ds_write_b128 v3, v[88:91]\n"
"v_mfma_f32_16x16x32_bf16 v[148:151], a[188:191], v[132:135], v[148:151]\n"
"buffer_load_dwordx4 v[88:91], v1, s[4:7], s97 offen offset:3072 sc1\n"
"v_mfma_f32_16x16x32_bf16 v[160:163], a[192:195], v[180:183], 0\n"
"v_mfma_f32_16x16x32_bf16 v[160:163], a[196:199], v[200:203], v[160:163]\n"
"s_add_u32 s98, s59, 0x40000\n"
"v_mfma_f32_16x16x32_bf16 v[160:163], a[200:203], v[128:131], v[160:163]\n"
"v_mfma_f32_16x16x32_bf16 v[160:163], a[204:207], v[144:147], v[160:163]\n"
"buffer_load_dwordx4 v[164:167], v0, s[4:7], s98 offen sc1\n"
"v_mfma_f32_16x16x32_bf16 v[160:163], a[208:211], v[152:155], v[160:163]\n"
"v_mfma_f32_16x16x32_bf16 v[160:163], a[212:215], v[204:207], v[160:163]\n"
"ds_write_b128 v3, v[148:151] offset:1024\n"
"v_mfma_f32_16x16x32_bf16 v[160:163], a[216:219], v[116:119], v[160:163]\n"
"v_mfma_f32_16x16x32_bf16 v[160:163], a[220:223], v[132:135], v[160:163]\n"
"buffer_load_dwordx4 v[148:151], v0, s[4:7], s98 offen offset:1024 sc1\n"
"v_mfma_f32_16x16x32_bf16 v[168:171], a[224:227], v[180:183], 0\n"
"v_mfma_f32_16x16x32_bf16 v[168:171], a[228:231], v[200:203], v[168:171]\n"
"v_mfma_f32_16x16x32_bf16 v[168:171], a[232:235], v[128:131], v[168:171]\n"
"v_mfma_f32_16x16x32_bf16 v[168:171], a[236:239], v[144:147], v[168:171]\n"
"buffer_load_dwordx4 v[128:131], v0, s[4:7], s98 offen offset:2048 sc1\n"
"v_mfma_f32_16x16x32_bf16 v[168:171], a[240:243], v[152:155], v[168:171]\n"
"v_mfma_f32_16x16x32_bf16 v[168:171], a[244:247], v[204:207], v[168:171]\n"
"ds_write_b128 v3, v[160:163] offset:2048\n"
"v_mfma_f32_16x16x32_bf16 v[168:171], a[248:251], v[116:119], v[168:171]\n"
"v_mfma_f32_16x16x32_bf16 v[168:171], a[252:255], v[132:135], v[168:171]\n"
"s_waitcnt vmcnt(17)\n"
"buffer_load_dwordx4 v[116:119], v0, s[4:7], s98 offen offset:3072 sc1\n"
"; @A G2 b.1\n"
"v_mfma_f32_16x16x32_bf16 v[132:135], a[128:131], v[216:219], 0\n"
"v_mfma_f32_16x16x32_bf16 v[132:135], a[132:135], v[212:215], v[132:135]\n"
"v_mfma_f32_16x16x32_bf16 v[132:135], a[136:139], v[192:195], v[132:135]\n"
"v_mfma_f32_16x16x32_bf16 v[132:135], a[140:143], v[100:103], v[132:135]\n"
"s_waitcnt vmcnt(14)\n"
"buffer_load_dwordx4 v[144:147], v1, s[4:7], s98 offen sc1\n"
"v_mfma_f32_16x16x32_bf16 v[132:135], a[144:147], v[188:191], v[132:135]\n"
"v_mfma_f32_16x16x32_bf16 v[132:135], a[148:151], v[104:107], v[132:135]\n"
"ds_write_b128 v3, v[168:171] offset:3072\n"
"v_mfma_f32_16x16x32_bf16 v[132:135], a[152:155], v[112:115], v[132:135]\n"
"v_mfma_f32_16x16x32_bf16 v[132:135], a[156:159], v[124:127], v[132:135]\n"
"buffer_load_dwordx4 v[152:155], v1, s[4:7], s98 offen offset:1024 sc1\n"
"v_mfma_f32_16x16x32_bf16 v[160:163], a[160:163], v[216:219], 0\n"
"v_mfma_f32_16x16x32_bf16 v[160:163], a[164:167], v[212:215], v[160:163]\n"
"v_mfma_f32_16x16x32_bf16 v[160:163], a[168:171], v[192:195], v[160:163]\n"
"v_mfma_f32_16x16x32_bf16 v[160:163], a[172:175], v[100:103], v[160:163]\n"
"buffer_load_dwordx4 v[168:171], v1, s[4:7], s98 offen offset:2048 sc1\n"
"v_mfma_f32_16x16x32_bf16 v[160:163], a[176:179], v[188:191], v[160:163]\n"
"s_nop 0\n"
"v_cmp_u_f32_e32 vcc, v132, v132\n"
"v_mfma_f32_16x16x32_bf16 v[160:163], a[180:183], v[104:107], v[160:163]\n"
"s_cbranch_vccnz .Lmk_p36s\n"
".Lmk_p36r:\n"
"v_mfma_f32_16x16x32_bf16 v[160:163], a[184:187], v[112:115], v[160:163]\n"
"ds_write_b128 v3, v[132:135] offset:6144\n"
"v_mfma_f32_16x16x32_bf16 v[160:163], a[188:191], v[124:127], v[160:163]\n"
"buffer_load_dwordx4 v[132:135], v1, s[4:7], s98 offen offset:3072 sc1\n"
"v_mfma_f32_16x16x32_bf16 v[172:175], a[192:195], v[216:219], 0\n"
"v_mfma_f32_16x16x32_bf16 v[172:175], a[196:199], v[212:215], v[172:175]\n"
"v_mfma_f32_16x16x32_bf16 v[172:175], a[200:203], v[192:195], v[172:175]\n"
"v_mfma_f32_16x16x32_bf16 v[172:175], a[204:207], v[100:103], v[172:175]\n"
"v_mfma_f32_16x16x32_bf16 v[172:175], a[208:211], v[188:191], v[172:175]\n"
"ds_read_b128 v[176:179], v2 offset:8192\n"
"ds_read_b128 v[180:183], v2 offset:9216\n"
"v_mfma_f32_16x16x32_bf16 v[172:175], a[212:215], v[104:107], v[172:175]\n"
"ds_write_b128 v3, v[160:163] offset:7168\n"
"v_mfma_f32_16x16x32_bf16 v[172:175], a[216:219], v[112:115], v[172:175]\n"
"v_mfma_f32_16x16x32_bf16 v[172:175], a[220:223], v[124:127], v[172:175]\n"
"v_mfma_f32_16x16x32_bf16 v[160:163], a[224:227], v[216:219], 0\n"
"s_mov_b32 s98, .Lmk_b0-.Lmk_g39\n"
"s_getpc_b64 s[84:85]\n"
".Lmk_g39:\n"
"s_add_u32 s84, s84, s98\n"
"s_addc_u32 s85, s85, 0\n"
"s_and_b32 s84, s84, -16\n"
"v_mfma_f32_16x16x32_bf16 v[160:163], a[228:231], v[212:215], v[160:163]\n"
"s_and_b32 s85, s85, 0xffff\n"
"s_sub_u32 s98, .Lmk_end-.Lmk_g39, s98\n"
"s_min_u32 s86, s98, 0x3000\n"
"s_mov_b32 s87, 0x20000\n"
"v_mfma_f32_16x16x32_bf16 v[160:163], a[232:235], v[192:195], v[160:163]\n"
"s_mov_b32 m0, 0x27800\n"
"s_nop 0\n"
"buffer_load_dwordx4 v0, s[84:87], s99 offen lds\n"
"v_mfma_f32_16x16x32_bf16 v[160:163], a[236:239], v[100:103], v[160:163]\n"
"s_waitcnt lgkmcnt(1)\n"
"v_lshlrev_b32_e32 v255, 16, v176\n"
"v_and_b32_e32 v254, 0xffff0000, v176\n"
"v_mfma_f32_16x16x32_bf16 v[160:163], a[240:243], v[188:191], v[160:163]\n"
"v_lshlrev_b32_e32 v253, 16, v177\n"
"v_and_b32_e32 v252, 0xffff0000, v177\n"
"v_mfma_f32_16x16x32_bf16 v[160:163], a[244:247], v[104:107], v[160:163]\n"
"ds_write_b128 v3, v[172:175] offset:8192\n"
"v_mfma_f32_16x16x32_bf16 v[160:163], a[248:251], v[112:115], v[160:163]\n"
"v_lshlrev_b32_e32 v251, 16, v178\n"
"v_and_b32_e32 v250, 0xffff0000, v178\n"
"v_mfma_f32_16x16x32_bf16 v[160:163], a[252:255], v[124:127], v[160:163]\n"
"s_waitcnt vmcnt(15)\n"
"v_lshlrev_b32_e32 v249, 16, v179\n"
"v_and_b32_e32 v248, 0xffff0000, v179\n"
"; @A G2 b.2\n"
"v_mfma_f32_16x16x32_bf16 v[100:103], a[128:131], v[140:143], 0\n"
"v_lshlrev_b32_e32 v247, 16, v180\n"
"v_and_b32_e32 v246, 0xffff0000, v180\n"
"v_mfma_f32_16x16x32_bf16 v[100:103], a[132:135], v[136:139], v[100:103]\n"
"v_lshlrev_b32_e32 v245, 16, v181\n"
"v_and_b32_e32 v244, 0xffff0000, v181\n"
"v_mfma_f32_16x16x32_bf16 v[100:103], a[136:139], v[120:123], v[100:103]\n"
"v_lshlrev_b32_e32 v243, 16, v182\n"
"v_and_b32_e32 v242, 0xffff0000, v182\n"
"v_mfma_f32_16x16x32_bf16 v[100:103], a[140:143], v[108:111], v[100:103]\n"
"s_waitcnt vmcnt(9)\n"
"v_lshlrev_b32_e32 v241, 16, v183\n"
"v_and_b32_e32 v240, 0xffff0000, v183\n"
"v_mfma_f32_16x16x32_bf16 v[100:103], a[144:147], v[92:95], v[100:103]\n"
"v_mfma_f32_16x16x32_bf16 v[100:103], a[148:151], v[96:99], v[100:103]\n"
"ds_write_b128 v3, v[160:163] offset:9216\n"
"v_mfma_f32_16x16x32_bf16 v[100:103], a[152:155], v[156:159], v[100:103]\n"
"v_mfma_f32_16x16x32_bf16 v[100:103], a[156:159], v[88:91], v[100:103]\n"
"v_mfma_f32_16x16x32_bf16 v[104:107], a[160:163], v[140:143], 0\n"
"v_mfma_f32_16x16x32_bf16 v[104:107], a[164:167], v[136:139], v[104:107]\n"
"s_waitcnt lgkmcnt(0)\n"
"s_barrier\n"
"v_mfma_f32_16x16x32_bf16 v[104:107], a[168:171], v[120:123], v[104:107]\n"
"ds_read_b128 v[112:115], v4\n"
"ds_read_b128 v[124:127], v5\n"
"v_mfma_f32_16x16x32_bf16 v[104:107], a[172:175], v[108:111], v[104:107]\n"
"v_mfma_f32_16x16x32_bf16 v[104:107], a[176:179], v[92:95], v[104:107]\n"
"s_add_u32 s98, s56, 0\n"
"v_cmp_u_f32_e32 vcc, v100, v100\n"
"v_mfma_f32_16x16x32_bf16 v[104:107], a[180:183], v[96:99], v[104:107]\n"
"s_cbranch_vccnz .Lmk_p37s\n"
".Lmk_p37r:\n"
"v_mfma_f32_16x16x32_bf16 v[104:107], a[184:187], v[156:159], v[104:107]\n"
"ds_write_b128 v3, v[100:103] offset:12288\n"
"v_mfma_f32_16x16x32_bf16 v[104:107], a[188:191], v[88:91], v[104:107]\n"
"buffer_load_dwordx4 v[100:103], v0, s[4:7], s98 offen sc1\n"
"v_mfma_f32_16x16x32_bf16 v[160:163], a[192:195], v[140:143], 0\n"
"ds_read_b128 v[172:175], v4 offset:1024\n"
"ds_read_b128 v[176:179], v5 offset:1024\n"
"v_mfma_f32_16x16x32_bf16 v[160:163], a[196:199], v[136:139], v[160:163]\n"
"s_waitcnt lgkmcnt(3)\n"
"v_add_f32_e32 v112, v112, v255\n"
"v_add_f32_e32 v113, v113, v254\n"
"v_mfma_f32_16x16x32_bf16 v[160:163], a[200:203], v[120:123], v[160:163]\n"
"buffer_load_dwordx4 v[180:183], v0, s[4:7], s98 offen offset:1024 sc1\n"
"v_mfma_f32_16x16x32_bf16 v[160:163], a[204:207], v[108:111], v[160:163]\n"
"v_add_f32_e32 v114, v114, v253\n"
"v_add_f32_e32 v115, v115, v252\n"
"v_mfma_f32_16x16x32_bf16 v[160:163], a[208:211], v[92:95], v[160:163]\n"
"v_add_f32_e32 v112, v112, v124\n"
"v_add_f32_e32 v113, v113, v125\n"
"v_mfma_f32_16x16x32_bf16 v[160:163], a[212:215], v[96:99], v[160:163]\n"
"ds_write_b128 v3, v[104:107] offset:13312\n"
"v_mfma_f32_16x16x32_bf16 v[160:163], a[216:219], v[156:159], v[160:163]\n"
"buffer_load_dwordx4 v[104:107], v0, s[4:7], s98 offen offset:2048 sc1\n"
"v_mfma_f32_16x16x32_bf16 v[160:163], a[220:223], v[88:91], v[160:163]\n"
"ds_read_b128 v[184:187], v4 offset:2048\n"
"ds_read_b128 v[188:191], v5 offset:2048\n"
"v_mfma_f32_16x16x32_bf16 v[192:195], a[224:227], v[140:143], 0\n"
"s_waitcnt lgkmcnt(3)\n"
"v_add_f32_e32 v114, v114, v126\n"
"v_add_f32_e32 v115, v115, v127\n"
"v_mfma_f32_16x16x32_bf16 v[192:195], a[228:231], v[136:139], v[192:195]\n"
"buffer_load_dwordx4 v[124:127], v0, s[4:7], s98 offen offset:3072 sc1\n"
"v_mfma_f32_16x16x32_bf16 v[192:195], a[232:235], v[120:123], v[192:195]\n"
"v_add_f32_e32 v172, v172, v251\n"
"v_add_f32_e32 v173, v173, v250\n"
"v_mfma_f32_16x16x32_bf16 v[192:195], a[236:239], v[108:111], v[192:195]\n"
"v_add_f32_e32 v174, v174, v249\n"
"v_add_f32_e32 v175, v175, v248\n"
"v_mfma_f32_16x16x32_bf16 v[192:195], a[240:243], v[92:95], v[192:195]\n"
"buffer_load_dwordx4 v[92:95], v1, s[4:7], s98 offen sc1\n"
"v_mfma_f32_16x16x32_bf16 v[192:195], a[244:247], v[96:99], v[192:195]\n"
"ds_write_b128 v3, v[160:163] offset:14336\n"
"v_mfma_f32_16x16x32_bf16 v[192:195], a[248:251], v[156:159], v[192:195]\n"
"ds_read_b128 v[96:99], v4 offset:3072\n"
"ds_read_b128 v[108:111], v5 offset:3072\n"
"v_mfma_f32_16x16x32_bf16 v[192:195], a[252:255], v[88:91], v[192:195]\n"
"s_waitcnt vmcnt(10)\n"
"buffer_load_dwordx4 v[88:91], v1, s[4:7], s98 offen offset:1024 sc1\n"
"; @A G2 b.3\n"
"v_mfma_f32_16x16x32_bf16 v[120:123], a[128:131], v[164:167], v[112:115]\n"
"v_add_f32_e32 v172, v172, v176\n"
"v_add_f32_e32 v173, v173, v177\n"
"v_mfma_f32_16x16x32_bf16 v[120:123], a[132:135], v[148:151], v[120:123]\n"
"s_waitcnt lgkmcnt(3)\n"
"v_add_f32_e32 v174, v174, v178\n"
"v_add_f32_e32 v175, v175, v179\n"
"v_mfma_f32_16x16x32_bf16 v[120:123], a[136:139], v[128:131], v[120:123]\n"
"buffer_load_dwordx4 v[136:139], v1, s[4:7], s98 offen offset:2048 sc1\n"
"v_mfma_f32_16x16x32_bf16 v[120:123], a[140:143], v[116:119], v[120:123]\n"
"s_waitcnt vmcnt(8)\n"
"v_add_f32_e32 v184, v184, v247\n"
"v_add_f32_e32 v185, v185, v246\n"
"v_mfma_f32_16x16x32_bf16 v[120:123], a[144:147], v[144:147], v[120:123]\n"
"v_add_f32_e32 v186, v186, v245\n"
"v_add_f32_e32 v187, v187, v244\n"
"v_mfma_f32_16x16x32_bf16 v[120:123], a[148:151], v[152:155], v[120:123]\n"
"ds_write_b128 v3, v[192:195] offset:15360\n"
"v_mfma_f32_16x16x32_bf16 v[120:123], a[152:155], v[168:171], v[120:123]\n"
"buffer_load_dwordx4 v[140:143], v1, s[4:7], s98 offen offset:3072 sc1\n"
"v_mfma_f32_16x16x32_bf16 v[120:123], a[156:159], v[132:135], v[120:123]\n"
"s_add_u32 s98, s57, 0\n"
"v_add_f32_e32 v184, v184, v188\n"
"v_add_f32_e32 v185, v185, v189\n"
"v_mfma_f32_16x16x32_bf16 v[156:159], a[160:163], v[164:167], v[172:175]\n"
"s_waitcnt lgkmcnt(1)\n"
"v_add_f32_e32 v186, v186, v190\n"
"v_add_f32_e32 v187, v187, v191\n"
"v_mfma_f32_16x16x32_bf16 v[156:159], a[164:167], v[148:151], v[156:159]\n"
"s_waitcnt lgkmcnt(0)\n"
"s_barrier\n"
"v_mfma_f32_16x16x32_bf16 v[156:159], a[168:171], v[128:131], v[156:159]\n"
"buffer_load_dwordx4 v[160:163], v0, s[4:7], s98 offen sc1\n"
"v_mfma_f32_16x16x32_bf16 v[156:159], a[172:175], v[116:119], v[156:159]\n"
"v_add_f32_e32 v96, v96, v243\n"
"v_add_f32_e32 v97, v97, v242\n"
"v_mfma_f32_16x16x32_bf16 v[156:159], a[176:179], v[144:147], v[156:159]\n"
"v_cmp_u_f32_e32 vcc, v120, v120\n"
"v_add_f32_e32 v98, v98, v241\n"
"v_mfma_f32_16x16x32_bf16 v[156:159], a[180:183], v[152:155], v[156:159]\n"
"s_cbranch_vccnz .Lmk_p38s\n"
".Lmk_p38r:\n"
"v_add_f32_e32 v99, v99, v240\n"
"v_mfma_f32_16x16x32_bf16 v[156:159], a[184:187], v[168:171], v[156:159]\n"
"buffer_load_dwordx4 v[112:115], v0, s[4:7], s98 offen offset:1024 sc1\n"
"v_mfma_f32_16x16x32_bf16 v[156:159], a[188:191], v[132:135], v[156:159]\n"
"v_add_f32_e32 v96, v96, v108\n"
"v_add_f32_e32 v97, v97, v109\n"
"v_mfma_f32_16x16x32_bf16 v[184:187], a[192:195], v[164:167], v[184:187]\n"
"v_add_f32_e32 v98, v98, v110\n"
"v_add_f32_e32 v99, v99, v111\n"
"v_mfma_f32_16x16x32_bf16 v[184:187], a[196:199], v[148:151], v[184:187]\n"
"buffer_load_dwordx4 v[108:111], v0, s[4:7], s98 offen offset:2048 sc1\n"
"v_mfma_f32_16x16x32_bf16 v[184:187], a[200:203], v[128:131], v[184:187]\n"
"ds_read_b128 v[172:175], v6\n"
"ds_read_b128 v[176:179], v6 offset:1024\n"
"v_mfma_f32_16x16x32_bf16 v[184:187], a[204:207], v[116:119], v[184:187]\n"
"ds_read_b128 v[188:191], v6 offset:2048\n"
"ds_read_b128 v[192:195], v6 offset:3072\n"
"v_mfma_f32_16x16x32_bf16 v[184:187], a[208:211], v[144:147], v[184:187]\n"
"buffer_load_dwordx4 v[196:199], v0, s[4:7], s98 offen offset:3072 sc1\n"
"v_mfma_f32_16x16x32_bf16 v[184:187], a[212:215], v[152:155], v[184:187]\n"
"v_mfma_f32_16x16x32_bf16 v[184:187], a[216:219], v[168:171], v[184:187]\n"
"v_mfma_f32_16x16x32_bf16 v[184:187], a[220:223], v[132:135], v[184:187]\n"
"buffer_load_dwordx4 v[200:203], v1, s[4:7], s98 offen sc1\n"
"v_mfma_f32_16x16x32_bf16 v[96:99], a[224:227], v[164:167], v[96:99]\n"
"s_waitcnt lgkmcnt(0)\n"
"v_add_f32_e32 v172, v120, v172\n"
"v_add_f32_e32 v173, v121, v173\n"
"v_mfma_f32_16x16x32_bf16 v[96:99], a[228:231], v[148:151], v[96:99]\n"
"v_add_f32_e32 v174, v122, v174\n"
"v_add_f32_e32 v175, v123, v175\n"
"v_mfma_f32_16x16x32_bf16 v[96:99], a[232:235], v[128:131], v[96:99]\n"
"buffer_load_dwordx4 v[120:123], v1, s[4:7], s98 offen offset:1024 sc1\n"
"v_mfma_f32_16x16x32_bf16 v[96:99], a[236:239], v[116:119], v[96:99]\n"
"v_add_f32_e32 v176, v156, v176\n"
"v_add_f32_e32 v177, v157, v177\n"
"v_mfma_f32_16x16x32_bf16 v[96:99], a[240:243], v[144:147], v[96:99]\n"
"v_add_f32_e32 v178, v158, v178\n"
"v_add_f32_e32 v179, v159, v179\n"
"v_mfma_f32_16x16x32_bf16 v[96:99], a[244:247], v[152:155], v[96:99]\n"
"buffer_load_dwordx4 v[116:119], v1, s[4:7], s98 offen offset:2048 sc1\n"
"v_mfma_f32_16x16x32_bf16 v[96:99], a[248:251], v[168:171], v[96:99]\n"
"v_cvt_pk_bf16_f32 v128, v172, v173\n"
"v_cvt_pk_bf16_f32 v129, v174, v175\n"
"v_mfma_f32_16x16x32_bf16 v[96:99], a[252:255], v[132:135], v[96:99]\n"
"s_add_u32 s97, s61, 0x440000\n"
"v_cvt_pk_bf16_f32 v130, v176, v177\n"
"v_cvt_pk_bf16_f32 v131, v178, v179\n"
"buffer_store_dwordx4 v[128:131], v0, s[4:7], s97 offen sc1\n"
"ds_write_b128 v2, v[128:131] offset:8192\n"
"buffer_load_dwordx4 v[132:135], v1, s[4:7], s98 offen offset:3072 sc1\n"
"; @A fin b\n"
"s_add_u32 s98, s61, 0x440000\n"
"v_mov_b32_e32 v144, 0x3f803f80\n"
"v_mov_b32_e32 v145, 0x3f803f80\n"
"v_mov_b32_e32 v146, 0x3f803f80\n"
"v_mov_b32_e32 v147, 0x3f803f80\n"
"v_add_f32_e32 v188, v184, v188\n"
"v_add_f32_e32 v189, v185, v189\n"
"v_add_f32_e32 v190, v186, v190\n"
"v_add_f32_e32 v191, v187, v191\n"
"v_add_f32_e32 v192, v96, v192\n"
"v_add_f32_e32 v193, v97, v193\n"
"v_add_f32_e32 v194, v98, v194\n"
"v_add_f32_e32 v195, v99, v195\n"
"v_cvt_pk_bf16_f32 v96, v188, v189\n"
"v_cvt_pk_bf16_f32 v97, v190, v191\n"
"v_cvt_pk_bf16_f32 v98, v192, v193\n"
"v_cvt_pk_bf16_f32 v99, v194, v195\n"
"v_mfma_f32_16x16x32_bf16 v[148:151], v[144:147], v[128:131], 0\n"
"buffer_store_dwordx4 v[96:99], v0, s[4:7], s98 offen offset:1024 sc1\n"
"ds_write_b128 v2, v[96:99] offset:9216\n"
"v_mfma_f32_16x16x32_bf16 v[152:155], v[128:131], v[128:131], 0\n"
"v_mfma_f32_16x16x32_bf16 v[148:151], v[144:147], v[96:99], v[148:151]\n"
"v_mfma_f32_16x16x32_bf16 v[152:155], v[96:99], v[96:99], v[152:155]\n"
"s_mov_b32 s96, 0xaaaaaaaa\n"
"s_mov_b32 s97, 0xaaaaaaaa\n"
"s_nop 5\n"
"v_cndmask_b32_e64 v152, v152, v153, s[96:97]\n"
"v_cndmask_b32_e64 v154, v154, v155, s[96:97]\n"
"s_mov_b32 s96, 0xcccccccc\n"
"s_mov_b32 s97, 0xcccccccc\n"
"v_cndmask_b32_e64 v149, v152, v154, s[96:97]\n"
"s_mov_b32 s96, 0xf0000f\n"
"s_mov_b32 s97, 0xf0000f00\n"
"s_add_u32 s98, s63, 0x44000\n"
"v_cndmask_b32_e64 v148, v148, v149, s[96:97]\n"
"buffer_store_dword v148, v14, s[4:7], s98 offen sc1\n"
"s_waitcnt vmcnt(17)\n"
"; @A G2 a.0\n"
"v_mfma_f32_16x16x32_bf16 v[96:99], a[128:131], v[100:103], 0\n"
"s_add_u32 s98, s58, 0\n"
"buffer_load_dwordx4 v[128:131], v0, s[4:7], s98 offen sc1\n"
"v_mfma_f32_16x16x32_bf16 v[96:99], a[132:135], v[180:183], v[96:99]\n"
"s_waitcnt vmcnt(16)\n"
"v_mfma_f32_16x16x32_bf16 v[96:99], a[136:139], v[104:107], v[96:99]\n"
"v_mfma_f32_16x16x32_bf16 v[96:99], a[140:143], v[124:127], v[96:99]\n"
"s_waitcnt vmcnt(14)\n"
"v_mfma_f32_16x16x32_bf16 v[96:99], a[144:147], v[92:95], v[96:99]\n"
"buffer_load_dwordx4 v[144:147], v0, s[4:7], s98 offen offset:1024 sc1\n"
"v_mfma_f32_16x16x32_bf16 v[96:99], a[148:151], v[88:91], v[96:99]\n"
"s_waitcnt vmcnt(13)\n"
"v_mfma_f32_16x16x32_bf16 v[96:99], a[152:155], v[136:139], v[96:99]\n"
"v_mfma_f32_16x16x32_bf16 v[96:99], a[156:159], v[140:143], v[96:99]\n"
"v_mfma_f32_16x16x32_bf16 v[148:151], a[160:163], v[100:103], 0\n"
"buffer_load_dwordx4 v[152:155], v0, s[4:7], s98 offen offset:2048 sc1\n"
"v_mfma_f32_16x16x32_bf16 v[148:151], a[164:167], v[180:183], v[148:151]\n"
"v_mfma_f32_16x16x32_bf16 v[148:151], a[168:171], v[104:107], v[148:151]\n"
"v_mfma_f32_16x16x32_bf16 v[148:151], a[172:175], v[124:127], v[148:151]\n"
"v_mfma_f32_16x16x32_bf16 v[148:151], a[176:179], v[92:95], v[148:151]\n"
"s_nop 1\n"
"v_cmp_u_f32_e32 vcc, v96, v96\n"
"v_mfma_f32_16x16x32_bf16 v[148:151], a[180:183], v[88:91], v[148:151]\n"
"s_cbranch_vccnz .Lmk_p40s\n"
".Lmk_p40r:\n"
"v_mfma_f32_16x16x32_bf16 v[148:151], a[184:187], v[136:139], v[148:151]\n"
"ds_write_b128 v3, v[96:99]\n"
"v_mfma_f32_16x16x32_bf16 v[148:151], a[188:191], v[140:143], v[148:151]\n"
"buffer_load_dwordx4 v[156:159], v0, s[4:7], s98 offen offset:3072 sc1\n"
"v_mfma_f32_16x16x32_bf16 v[96:99], a[192:195], v[100:103], 0\n"
"v_mfma_f32_16x16x32_bf16 v[96:99], a[196:199], v[180:183], v[96:99]\n"
"v_mfma_f32_16x16x32_bf16 v[96:99], a[200:203], v[104:107], v[96:99]\n"
"v_mfma_f32_16x16x32_bf16 v[96:99], a[204:207], v[124:127], v[96:99]\n"
"buffer_load_dwordx4 v[164:167], v1, s[4:7], s98 offen sc1\n"
"v_mfma_f32_16x16x32_bf16 v[96:99], a[208:211], v[92:95], v[96:99]\n"
"v_mfma_f32_16x16x32_bf16 v[96:99], a[212:215], v[88:91], v[96:99]\n"
"ds_write_b128 v3, v[148:151] offset:1024\n"
"v_mfma_f32_16x16x32_bf16 v[96:99], a[216:219], v[136:139], v[96:99]\n"
"v_mfma_f32_16x16x32_bf16 v[96:99], a[220:223], v[140:143], v[96:99]\n"
"buffer_load_dwordx4 v[148:151], v1, s[4:7], s98 offen offset:1024 sc1\n"
"v_mfma_f32_16x16x32_bf16 v[168:171], a[224:227], v[100:103], 0\n"
"v_mfma_f32_16x16x32_bf16 v[168:171], a[228:231], v[180:183], v[168:171]\n"
"v_mfma_f32_16x16x32_bf16 v[168:171], a[232:235], v[104:107], v[168:171]\n"
"v_mfma_f32_16x16x32_bf16 v[168:171], a[236:239], v[124:127], v[168:171]\n"
"buffer_load_dwordx4 v[124:127], v1, s[4:7], s98 offen offset:2048 sc1\n"
"v_mfma_f32_16x16x32_bf16 v[168:171], a[240:243], v[92:95], v[168:171]\n"
"v_mfma_f32_16x16x32_bf16 v[168:171], a[244:247], v[88:91], v[168:171]\n"
"ds_write_b128 v3, v[96:99] offset:2048\n"
"v_mfma_f32_16x16x32_bf16 v[168:171], a[248:251], v[136:139], v[168:171]\n"
"v_mfma_f32_16x16x32_bf16 v[168:171], a[252:255], v[140:143], v[168:171]\n"
"s_waitcnt vmcnt(14)\n"
"buffer_load_dwordx4 v[136:139], v1, s[4:7], s98 offen offset:3072 sc1\n"
"; @A G2 a.1\n"
"v_mfma_f32_16x16x32_bf16 v[88:91], a[128:131], v[160:163], 0\n"
"v_mfma_f32_16x16x32_bf16 v[88:91], a[132:135], v[112:115], v[88:91]\n"
"s_add_u32 s98, s59, 0\n"
"v_mfma_f32_16x16x32_bf16 v[88:91], a[136:139], v[108:111], v[88:91]\n"
"v_mfma_f32_16x16x32_bf16 v[88:91], a[140:143], v[196:199], v[88:91]\n"
"s_waitcnt vmcnt(10)\n"
"buffer_load_dwordx4 v[172:175], v0, s[4:7], s98 offen sc1\n"
"v_mfma_f32_16x16x32_bf16 v[88:91], a[144:147], v[200:203], v[88:91]\n"
"v_mfma_f32_16x16x32_bf16 v[88:91], a[148:151], v[120:123], v[88:91]\n"
"ds_write_b128 v3, v[168:171] offset:3072\n"
"v_mfma_f32_16x16x32_bf16 v[88:91], a[152:155], v[116:119], v[88:91]\n"
"v_mfma_f32_16x16x32_bf16 v[88:91], a[156:159], v[132:135], v[88:91]\n"
"buffer_load_dwordx4 v[168:171], v0, s[4:7], s98 offen offset:1024 sc1\n"
"v_mfma_f32_16x16x32_bf16 v[92:95], a[160:163], v[160:163], 0\n"
"s_add_u32 s97, s75, 0x10000\n"
"buffer_load_dwordx2 v[254:255], v13, s[4:7], s97 offen sc1\n"
"v_mfma_f32_16x16x32_bf16 v[92:95], a[164:167], v[112:115], v[92:95]\n"
"v_mfma_f32_16x16x32_bf16 v[92:95], a[168:171], v[108:111], v[92:95]\n"
"v_mfma_f32_16x16x32_bf16 v[92:95], a[172:175], v[196:199], v[92:95]\n"
"buffer_load_dwordx4 v[176:179], v0, s[4:7], s98 offen offset:2048 sc1\n"
"v_mfma_f32_16x16x32_bf16 v[92:95], a[176:179], v[200:203], v[92:95]\n"
"v_cmp_u_f32_e32 vcc, v88, v88\n"
"v_mfma_f32_16x16x32_bf16 v[92:95], a[180:183], v[120:123], v[92:95]\n"
"s_cbranch_vccnz .Lmk_p41s\n"
".Lmk_p41r:\n"
"v_mfma_f32_16x16x32_bf16 v[92:95], a[184:187], v[116:119], v[92:95]\n"
"ds_write_b128 v3, v[88:91] offset:6144\n"
"v_mfma_f32_16x16x32_bf16 v[92:95], a[188:191], v[132:135], v[92:95]\n"
"buffer_load_dwordx4 v[180:183], v0, s[4:7], s98 offen offset:3072 sc1\n"
"v_mfma_f32_16x16x32_bf16 v[88:91], a[192:195], v[160:163], 0\n"
"v_mfma_f32_16x16x32_bf16 v[88:91], a[196:199], v[112:115], v[88:91]\n"
"v_mfma_f32_16x16x32_bf16 v[88:91], a[200:203], v[108:111], v[88:91]\n"
"v_mfma_f32_16x16x32_bf16 v[88:91], a[204:207], v[196:199], v[88:91]\n"
"buffer_load_dwordx4 v[184:187], v1, s[4:7], s98 offen sc1\n"
"v_mfma_f32_16x16x32_bf16 v[88:91], a[208:211], v[200:203], v[88:91]\n"
"ds_read_b128 v[96:99], v2\n"
"ds_read_b128 v[100:103], v2 offset:1024\n"
"v_mfma_f32_16x16x32_bf16 v[88:91], a[212:215], v[120:123], v[88:91]\n"
"ds_write_b128 v3, v[92:95] offset:7168\n"
"v_mfma_f32_16x16x32_bf16 v[88:91], a[216:219], v[116:119], v[88:91]\n"
"v_mfma_f32_16x16x32_bf16 v[88:91], a[220:223], v[132:135], v[88:91]\n"
"buffer_load_dwordx4 v[188:191], v1, s[4:7], s98 offen offset:1024 sc1\n"
"v_mfma_f32_16x16x32_bf16 v[92:95], a[224:227], v[160:163], 0\n"
"v_mfma_f32_16x16x32_bf16 v[92:95], a[228:231], v[112:115], v[92:95]\n"
"s_waitcnt lgkmcnt(1)\n"
"v_lshlrev_b32_e32 v253, 16, v96\n"
"v_and_b32_e32 v252, 0xffff0000, v96\n"
"v_mfma_f32_16x16x32_bf16 v[92:95], a[232:235], v[108:111], v[92:95]\n"
"v_lshlrev_b32_e32 v251, 16, v97\n"
"v_and_b32_e32 v250, 0xffff0000, v97\n"
"v_mfma_f32_16x16x32_bf16 v[92:95], a[236:239], v[196:199], v[92:95]\n"
"buffer_load_dwordx4 v[192:195], v1, s[4:7], s98 offen offset:2048 sc1\n"
"v_mfma_f32_16x16x32_bf16 v[92:95], a[240:243], v[200:203], v[92:95]\n"
"v_lshlrev_b32_e32 v249, 16, v98\n"
"v_and_b32_e32 v248, 0xffff0000, v98\n"
"v_mfma_f32_16x16x32_bf16 v[92:95], a[244:247], v[120:123], v[92:95]\n"
"ds_write_b128 v3, v[88:91] offset:8192\n"
"v_mfma_f32_16x16x32_bf16 v[92:95], a[248:251], v[116:119], v[92:95]\n"
"v_lshlrev_b32_e32 v247, 16, v99\n"
"v_and_b32_e32 v246, 0xffff0000, v99\n"
"v_mfma_f32_16x16x32_bf16 v[92:95], a[252:255], v[132:135], v[92:95]\n"
"s_waitcnt vmcnt(12)\n"
"buffer_load_dwordx4 v[196:199], v1, s[4:7], s98 offen offset:3072 sc1\n"
"; @A G2 a.2\n"
"v_mfma_f32_16x16x32_bf16 v[96:99], a[128:131], v[128:131], 0\n"
"v_lshlrev_b32_e32 v245, 16, v100\n"
"v_and_b32_e32 v244, 0xffff0000, v100\n"
"v_mfma_f32_16x16x32_bf16 v[96:99], a[132:135], v[144:147], v[96:99]\n"
"v_lshlrev_b32_e32 v243, 16, v101\n"
"v_and_b32_e32 v242, 0xffff0000, v101\n"
"v_mfma_f32_16x16x32_bf16 v[96:99], a[136:139], v[152:155], v[96:99]\n"
"v_lshlrev_b32_e32 v241, 16, v102\n"
"v_and_b32_e32 v240, 0xffff0000, v102\n"
"v_mfma_f32_16x16x32_bf16 v[96:99], a[140:143], v[156:159], v[96:99]\n"
"s_waitcnt vmcnt(9)\n"
"v_lshlrev_b32_e32 v239, 16, v103\n"
"v_and_b32_e32 v238, 0xffff0000, v103\n"
"v_mfma_f32_16x16x32_bf16 v[96:99], a[144:147], v[164:167], v[96:99]\n"
"v_mfma_f32_16x16x32_bf16 v[96:99], a[148:151], v[148:151], v[96:99]\n"
"ds_write_b128 v3, v[92:95] offset:9216\n"
"v_mfma_f32_16x16x32_bf16 v[96:99], a[152:155], v[124:127], v[96:99]\n"
"v_mfma_f32_16x16x32_bf16 v[96:99], a[156:159], v[136:139], v[96:99]\n"
"v_mfma_f32_16x16x32_bf16 v[104:107], a[160:163], v[128:131], 0\n"
"s_add_u32 s98, s52, 0x440000\n"
"buffer_load_dwordx4 v[88:91], v0, s[4:7], s98 offen sc1\n"
"v_mfma_f32_16x16x32_bf16 v[104:107], a[164:167], v[144:147], v[104:107]\n"
"s_waitcnt lgkmcnt(0)\n"
"s_barrier\n"
"v_mfma_f32_16x16x32_bf16 v[104:107], a[168:171], v[152:155], v[104:107]\n"
"ds_read_b128 v[140:143], v4\n"
"ds_read_b128 v[112:115], v5\n"
"v_mfma_f32_16x16x32_bf16 v[104:107], a[172:175], v[156:159], v[104:107]\n"
"buffer_load_dwordx4 v[92:95], v0, s[4:7], s98 offen offset:1024 sc1\n"
"v_mfma_f32_16x16x32_bf16 v[104:107], a[176:179], v[164:167], v[104:107]\n"
"v_cmp_u_f32_e32 vcc, v96, v96\n"
"v_mfma_f32_16x16x32_bf16 v[104:107], a[180:183], v[148:151], v[104:107]\n"
"s_cbranch_vccnz .Lmk_p42s\n"
".Lmk_p42r:\n"
"v_mfma_f32_16x16x32_bf16 v[104:107], a[184:187], v[124:127], v[104:107]\n"
"ds_write_b128 v3, v[96:99] offset:12288\n"
"v_mfma_f32_16x16x32_bf16 v[104:107], a[188:191], v[136:139], v[104:107]\n"
"buffer_load_dwordx4 v[96:99], v0, s[4:7], s98 offen offset:2048 sc1\n"
"v_mfma_f32_16x16x32_bf16 v[120:123], a[192:195], v[128:131], 0\n"
"ds_read_b128 v[160:163], v4 offset:1024\n"
"ds_read_b128 v[132:135], v5 offset:1024\n"
"v_mfma_f32_16x16x32_bf16 v[120:123], a[196:199], v[144:147], v[120:123]\n"
"s_waitcnt lgkmcnt(3)\n"
"v_add_f32_e32 v140, v140, v253\n"
"v_add_f32_e32 v141, v141, v252\n"
"v_mfma_f32_16x16x32_bf16 v[120:123], a[200:203], v[152:155], v[120:123]\n"
"buffer_load_dwordx4 v[100:103], v0, s[4:7], s98 offen offset:3072 sc1\n"
"v_mfma_f32_16x16x32_bf16 v[120:123], a[204:207], v[156:159], v[120:123]\n"
"v_add_f32_e32 v142, v142, v251\n"
"v_add_f32_e32 v143, v143, v250\n"
"v_mfma_f32_16x16x32_bf16 v[120:123], a[208:211], v[164:167], v[120:123]\n"
"v_add_f32_e32 v140, v140, v112\n"
"v_add_f32_e32 v141, v141, v113\n"
"v_mfma_f32_16x16x32_bf16 v[120:123], a[212:215], v[148:151], v[120:123]\n"
"ds_write_b128 v3, v[104:107] offset:13312\n"
"v_mfma_f32_16x16x32_bf16 v[120:123], a[216:219], v[124:127], v[120:123]\n"
"buffer_load_dwordx4 v[104:107], v1, s[4:7], s98 offen sc1\n"
"v_mfma_f32_16x16x32_bf16 v[120:123], a[220:223], v[136:139], v[120:123]\n"
"ds_read_b128 v[108:111], v4 offset:2048\n"
"ds_read_b128 v[200:203], v5 offset:2048\n"
"v_mfma_f32_16x16x32_bf16 v[204:207], a[224:227], v[128:131], 0\n"
"s_waitcnt lgkmcnt(3)\n"
"v_add_f32_e32 v142, v142, v114\n"
"v_add_f32_e32 v143, v143, v115\n"
"v_mfma_f32_16x16x32_bf16 v[204:207], a[228:231], v[144:147], v[204:207]\n"
"buffer_load_dwordx4 v[112:115], v1, s[4:7], s98 offen offset:1024 sc1\n"
"v_mfma_f32_16x16x32_bf16 v[204:207], a[232:235], v[152:155], v[204:207]\n"
"v_add_f32_e32 v160, v160, v249\n"
"v_add_f32_e32 v161, v161, v248\n"
"v_mfma_f32_16x16x32_bf16 v[204:207], a[236:239], v[156:159], v[204:207]\n"
"v_add_f32_e32 v162, v162, v247\n"
"v_add_f32_e32 v163, v163, v246\n"
"v_mfma_f32_16x16x32_bf16 v[204:207], a[240:243], v[164:167], v[204:207]\n"
"buffer_load_dwordx4 v[116:119], v1, s[4:7], s98 offen offset:2048 sc1\n"
"v_mfma_f32_16x16x32_bf16 v[204:207], a[244:247], v[148:151], v[204:207]\n"
"ds_write_b128 v3, v[120:123] offset:14336\n"
"v_mfma_f32_16x16x32_bf16 v[204:207], a[248:251], v[124:127], v[204:207]\n"
"ds_read_b128 v[120:123], v4 offset:3072\n"
"ds_read_b128 v[148:151], v5 offset:3072\n"
"v_mfma_f32_16x16x32_bf16 v[204:207], a[252:255], v[136:139], v[204:207]\n"
"s_waitcnt vmcnt(11)\n"
"buffer_load_dwordx4 v[124:127], v1, s[4:7], s98 offen offset:3072 sc1\n"
"; @A G2 a.3\n"
"v_mfma_f32_16x16x32_bf16 v[208:211], a[128:131], v[172:175], v[140:143]\n"
"s_add_u32 s98, s53, 0x440000\n"
"v_add_f32_e32 v160, v160, v132\n"
"v_add_f32_e32 v161, v161, v133\n"
"v_mfma_f32_16x16x32_bf16 v[208:211], a[132:135], v[168:171], v[208:211]\n"
"s_waitcnt lgkmcnt(3)\n"
"v_add_f32_e32 v162, v162, v134\n"
"v_add_f32_e32 v163, v163, v135\n"
"v_mfma_f32_16x16x32_bf16 v[208:211], a[136:139], v[176:179], v[208:211]\n"
"buffer_load_dwordx4 v[128:131], v0, s[4:7], s98 offen sc1\n"
"v_mfma_f32_16x16x32_bf16 v[208:211], a[140:143], v[180:183], v[208:211]\n"
"s_waitcnt vmcnt(9)\n"
"v_add_f32_e32 v108, v108, v245\n"
"v_add_f32_e32 v109, v109, v244\n"
"v_mfma_f32_16x16x32_bf16 v[208:211], a[144:147], v[184:187], v[208:211]\n"
"v_cmp_ne_u64_e32 vcc, v[254:255], v[18:19]\n"
"v_mfma_f32_16x16x32_bf16 v[208:211], a[148:151], v[188:191], v[208:211]\n"
"ds_write_b128 v3, v[204:207] offset:15360\n"
"v_mfma_f32_16x16x32_bf16 v[208:211], a[152:155], v[192:195], v[208:211]\n"
"s_cbranch_vccnz .Lmk_f13s\n"
".Lmk_f13r:\n"
"v_add_f32_e32 v110, v110, v243\n"
"v_mfma_f32_16x16x32_bf16 v[208:211], a[156:159], v[196:199], v[208:211]\n"
"buffer_load_dwordx4 v[132:135], v0, s[4:7], s98 offen offset:1024 sc1\n"
"v_mfma_f32_16x16x32_bf16 v[204:207], a[160:163], v[172:175], v[160:163]\n"
"v_add_f32_e32 v111, v111, v242\n"
"v_add_f32_e32 v108, v108, v200\n"
"v_mfma_f32_16x16x32_bf16 v[204:207], a[164:167], v[168:171], v[204:207]\n"
"s_waitcnt lgkmcnt(0)\n"
"s_barrier\n"
"v_mfma_f32_16x16x32_bf16 v[204:207], a[168:171], v[176:179], v[204:207]\n"
"buffer_load_dwordx4 v[136:139], v0, s[4:7], s98 offen offset:2048 sc1\n"
"v_mfma_f32_16x16x32_bf16 v[204:207], a[172:175], v[180:183], v[204:207]\n"
"v_add_f32_e32 v109, v109, v201\n"
"v_add_f32_e32 v110, v110, v202\n"
"v_mfma_f32_16x16x32_bf16 v[204:207], a[176:179], v[184:187], v[204:207]\n"
"v_cmp_u_f32_e32 vcc, v208, v208\n"
"v_add_f32_e32 v111, v111, v203\n"
"v_mfma_f32_16x16x32_bf16 v[204:207], a[180:183], v[188:191], v[204:207]\n"
"s_cbranch_vccnz .Lmk_p43s\n"
".Lmk_p43r:\n"
"v_add_f32_e32 v120, v120, v241\n"
"v_mfma_f32_16x16x32_bf16 v[204:207], a[184:187], v[192:195], v[204:207]\n"
"buffer_load_dwordx4 v[140:143], v0, s[4:7], s98 offen offset:3072 sc1\n"
"v_mfma_f32_16x16x32_bf16 v[204:207], a[188:191], v[196:199], v[204:207]\n"
"v_add_f32_e32 v121, v121, v240\n"
"v_add_f32_e32 v122, v122, v239\n"
"v_mfma_f32_16x16x32_bf16 v[108:111], a[192:195], v[172:175], v[108:111]\n"
"v_add_f32_e32 v123, v123, v238\n"
"v_add_f32_e32 v120, v120, v148\n"
"v_mfma_f32_16x16x32_bf16 v[108:111], a[196:199], v[168:171], v[108:111]\n"
"buffer_load_dwordx4 v[144:147], v1, s[4:7], s98 offen sc1\n"
"v_mfma_f32_16x16x32_bf16 v[108:111], a[200:203], v[176:179], v[108:111]\n"
"v_add_f32_e32 v121, v121, v149\n"
"v_add_f32_e32 v122, v122, v150\n"
"v_mfma_f32_16x16x32_bf16 v[108:111], a[204:207], v[180:183], v[108:111]\n"
"ds_read_b128 v[200:203], v6\n"
"v_add_f32_e32 v123, v123, v151\n"
"v_mfma_f32_16x16x32_bf16 v[108:111], a[208:211], v[184:187], v[108:111]\n"
"buffer_load_dwordx4 v[148:151], v1, s[4:7], s98 offen offset:1024 sc1\n"
"v_mfma_f32_16x16x32_bf16 v[108:111], a[212:215], v[188:191], v[108:111]\n"
"ds_read_b128 v[212:215], v6 offset:1024\n"
"ds_read_b128 v[152:155], v6 offset:2048\n"
"v_mfma_f32_16x16x32_bf16 v[108:111], a[216:219], v[192:195], v[108:111]\n"
"ds_read_b128 v[156:159], v6 offset:3072\n"
"v_mfma_f32_16x16x32_bf16 v[108:111], a[220:223], v[196:199], v[108:111]\n"
"buffer_load_dwordx4 v[160:163], v1, s[4:7], s98 offen offset:2048 sc1\n"
"v_mfma_f32_16x16x32_bf16 v[120:123], a[224:227], v[172:175], v[120:123]\n"
"v_mfma_f32_16x16x32_bf16 v[120:123], a[228:231], v[168:171], v[120:123]\n"
"v_mfma_f32_16x16x32_bf16 v[120:123], a[232:235], v[176:179], v[120:123]\n"
"buffer_load_dwordx4 v[164:167], v1, s[4:7], s98 offen offset:3072 sc1\n"
"v_mfma_f32_16x16x32_bf16 v[120:123], a[236:239], v[180:183], v[120:123]\n"
"s_waitcnt lgkmcnt(0)\n"
"v_add_f32_e32 v200, v208, v200\n"
"v_add_f32_e32 v201, v209, v201\n"
"v_mfma_f32_16x16x32_bf16 v[120:123], a[240:243], v[184:187], v[120:123]\n"
"v_add_f32_e32 v202, v210, v202\n"
"v_add_f32_e32 v203, v211, v203\n"
"v_mfma_f32_16x16x32_bf16 v[120:123], a[244:247], v[188:191], v[120:123]\n"
"v_add_f32_e32 v212, v204, v212\n"
"v_add_f32_e32 v213, v205, v213\n"
"v_mfma_f32_16x16x32_bf16 v[120:123], a[248:251], v[192:195], v[120:123]\n"
"v_add_f32_e32 v214, v206, v214\n"
"v_add_f32_e32 v215, v207, v215\n"
"v_mfma_f32_16x16x32_bf16 v[120:123], a[252:255], v[196:199], v[120:123]\n"
"s_add_u32 s98, s61, 0x400000\n"
"v_cvt_pk_bf16_f32 v168, v200, v201\n"
"v_cvt_pk_bf16_f32 v169, v202, v203\n"
"v_cvt_pk_bf16_f32 v170, v212, v213\n"
"v_cvt_pk_bf16_f32 v171, v214, v215\n"
"buffer_store_dwordx4 v[168:171], v0, s[4:7], s98 offen sc1\n"
"ds_write_b128 v2, v[168:171]\n"
".Lmk_loop:\n"
".Lmk_b0:\n"
"s_waitcnt vmcnt(13)\n"
"; @B G1 0.0\n"
"v_mfma_f32_16x16x32_bf16 v[172:175], a[0:3], v[88:91], 0\n"
"v_mfma_f32_16x16x32_bf16 v[172:175], a[4:7], v[92:95], v[172:175]\n"
"s_add_u32 s98, s62, 0x44000\n"
"v_mfma_f32_16x16x32_bf16 v[172:175], a[8:11], v[96:99], v[172:175]\n"
"s_add_u32 s97, s54, 0x440000\n"
"buffer_load_dwordx4 v[176:179], v0, s[4:7], s97 offen sc1\n"
"v_mfma_f32_16x16x32_bf16 v[172:175], a[12:15], v[100:103], v[172:175]\n"
"s_waitcnt vmcnt(10)\n"
"v_mfma_f32_16x16x32_bf16 v[172:175], a[16:19], v[104:107], v[172:175]\n"
"buffer_load_dword v255, v10, s[4:7], s98 offen sc1\n"
"v_mfma_f32_16x16x32_bf16 v[172:175], a[20:23], v[112:115], v[172:175]\n"
"v_mov_b32_e32 v180, 0x3f803f80\n"
"v_mov_b32_e32 v181, 0x3f803f80\n"
"v_mfma_f32_16x16x32_bf16 v[172:175], a[24:27], v[116:119], v[172:175]\n"
"v_mov_b32_e32 v182, 0x3f803f80\n"
"v_mov_b32_e32 v183, 0x3f803f80\n"
"v_mfma_f32_16x16x32_bf16 v[172:175], a[28:31], v[124:127], v[172:175]\n"
"buffer_load_dwordx4 v[184:187], v0, s[4:7], s97 offen offset:1024 sc1\n"
"v_mfma_f32_16x16x32_bf16 v[188:191], a[32:35], v[88:91], 0\n"
"buffer_load_dword v254, v10, s[4:7], s98 offen offset:256 sc1\n"
"v_mfma_f32_16x16x32_bf16 v[188:191], a[36:39], v[92:95], v[188:191]\n"
"v_add_f32_e32 v152, v108, v152\n"
"v_add_f32_e32 v153, v109, v153\n"
"v_mfma_f32_16x16x32_bf16 v[188:191], a[40:43], v[96:99], v[188:191]\n"
"v_add_f32_e32 v154, v110, v154\n"
"v_add_f32_e32 v155, v111, v155\n"
"v_mfma_f32_16x16x32_bf16 v[188:191], a[44:47], v[100:103], v[188:191]\n"
"buffer_load_dword v253, v10, s[4:7], s98 offen offset:512 sc1\n"
"v_mfma_f32_16x16x32_bf16 v[188:191], a[48:51], v[104:107], v[188:191]\n"
"v_cmp_u_f32_e32 vcc, v172, v172\n"
"v_add_f32_e32 v156, v120, v156\n"
"v_mfma_f32_16x16x32_bf16 v[188:191], a[52:55], v[112:115], v[188:191]\n"
"s_cbranch_vccnz .Lmk_p48s\n"
".Lmk_p48r:\n"
"v_add_f32_e32 v157, v121, v157\n"
"v_mfma_f32_16x16x32_bf16 v[188:191], a[56:59], v[116:119], v[188:191]\n"
"buffer_load_dwordx4 v[108:111], v0, s[4:7], s97 offen offset:2048 sc1\n"
"v_mfma_f32_16x16x32_bf16 v[188:191], a[60:63], v[124:127], v[188:191]\n"
"ds_write_b128 v3, v[172:175]\n"
"v_mfma_f32_16x16x32_bf16 v[172:175], a[64:67], v[88:91], 0\n"
"buffer_load_dword v252, v10, s[4:7], s98 offen offset:768 sc1\n"
"v_mfma_f32_16x16x32_bf16 v[172:175], a[68:71], v[92:95], v[172:175]\n"
"buffer_load_dwordx4 v[192:195], v0, s[4:7], s97 offen offset:3072 sc1\n"
"v_mfma_f32_16x16x32_bf16 v[172:175], a[72:75], v[96:99], v[172:175]\n"
"v_add_f32_e32 v158, v122, v158\n"
"v_add_f32_e32 v159, v123, v159\n"
"v_mfma_f32_16x16x32_bf16 v[172:175], a[76:79], v[100:103], v[172:175]\n"
"buffer_load_dword v251, v10, s[4:7], s98 offen offset:1024 sc1\n"
"v_mfma_f32_16x16x32_bf16 v[172:175], a[80:83], v[104:107], v[172:175]\n"
"v_cvt_pk_bf16_f32 v120, v152, v153\n"
"v_cvt_pk_bf16_f32 v121, v154, v155\n"
"v_mfma_f32_16x16x32_bf16 v[172:175], a[84:87], v[112:115], v[172:175]\n"
"ds_write_b128 v3, v[188:191] offset:1024\n"
"v_mfma_f32_16x16x32_bf16 v[172:175], a[88:91], v[116:119], v[172:175]\n"
"buffer_load_dwordx4 v[152:155], v1, s[4:7], s97 offen sc1\n"
"v_mfma_f32_16x16x32_bf16 v[172:175], a[92:95], v[124:127], v[172:175]\n"
"buffer_load_dword v250, v10, s[4:7], s98 offen offset:1280 sc1\n"
"v_mfma_f32_16x16x32_bf16 v[188:191], a[96:99], v[88:91], 0\n"
"s_add_u32 s96, s61, 0x400000\n"
"v_cvt_pk_bf16_f32 v122, v156, v157\n"
"v_cvt_pk_bf16_f32 v123, v158, v159\n"
"v_mfma_f32_16x16x32_bf16 v[188:191], a[100:103], v[92:95], v[188:191]\n"
"buffer_store_dwordx4 v[120:123], v0, s[4:7], s96 offen offset:1024 sc1\n"
"v_mfma_f32_16x16x32_bf16 v[188:191], a[104:107], v[96:99], v[188:191]\n"
"buffer_load_dword v249, v10, s[4:7], s98 offen offset:1536 sc1\n"
"v_mfma_f32_16x16x32_bf16 v[188:191], a[108:111], v[100:103], v[188:191]\n"
"buffer_load_dwordx4 v[156:159], v1, s[4:7], s97 offen offset:1024 sc1\n"
"v_mfma_f32_16x16x32_bf16 v[188:191], a[112:115], v[104:107], v[188:191]\n"
"ds_write_b128 v2, v[120:123] offset:1024\n"
"v_mfma_f32_16x16x32_bf16 v[196:199], v[180:183], v[168:171], 0\n"
"v_mfma_f32_16x16x32_bf16 v[200:203], v[168:171], v[168:171], 0\n"
"v_mfma_f32_16x16x32_bf16 v[196:199], v[180:183], v[120:123], v[196:199]\n"
"v_mfma_f32_16x16x32_bf16 v[200:203], v[120:123], v[120:123], v[200:203]\n"
"v_mfma_f32_16x16x32_bf16 v[188:191], a[116:119], v[112:115], v[188:191]\n"
"ds_write_b128 v3, v[172:175] offset:2048\n"
"v_mfma_f32_16x16x32_bf16 v[188:191], a[120:123], v[116:119], v[188:191]\n"
"s_mov_b32 s94, 0xaaaaaaaa\n"
"v_mov_b32_e32 v120, -1\n"
"v_mov_b32_e32 v121, -1\n"
"v_mfma_f32_16x16x32_bf16 v[188:191], a[124:127], v[124:127], v[188:191]\n"
"v_mov_b32_e32 v122, -1\n"
"v_mov_b32_e32 v123, -1\n"
"v_mfma_f32_16x16x32_bf16 v[168:171], v[24:27], v[88:91], 0\n"
"buffer_load_dwordx4 v[172:175], v1, s[4:7], s97 offen offset:2048 sc1\n"
"v_mfma_f32_16x16x32_bf16 v[168:171], v[28:31], v[92:95], v[168:171]\n"
"s_add_u32 s96, s60, 0x448000\n"
"buffer_store_dwordx4 v[120:123], v0, s[4:7], s96 offen sc1\n"
"v_mfma_f32_16x16x32_bf16 v[168:171], v[32:35], v[96:99], v[168:171]\n"
"buffer_load_dword v248, v10, s[4:7], s98 offen offset:1792 sc1\n"
"v_mfma_f32_16x16x32_bf16 v[168:171], v[36:39], v[100:103], v[168:171]\n"
"s_mov_b32 s95, 0xaaaaaaaa\n"
"v_mfma_f32_16x16x32_bf16 v[168:171], v[40:43], v[104:107], v[168:171]\n"
"v_mfma_f32_16x16x32_bf16 v[168:171], v[44:47], v[112:115], v[168:171]\n"
"buffer_load_dwordx4 v[180:183], v1, s[4:7], s97 offen offset:3072 sc1\n"
"v_mfma_f32_16x16x32_bf16 v[168:171], v[48:51], v[116:119], v[168:171]\n"
"ds_write_b128 v3, v[188:191] offset:3072\n"
"v_mfma_f32_16x16x32_bf16 v[168:171], v[52:55], v[124:127], v[168:171]\n"
"v_cndmask_b32_e64 v200, v200, v201, s[94:95]\n"
"v_cndmask_b32_e64 v202, v202, v203, s[94:95]\n"
"v_mfma_f32_16x16x32_bf16 v[188:191], v[56:59], v[88:91], 0\n"
"s_mov_b32 s94, 0xcccccccc\n"
"s_mov_b32 s95, 0xcccccccc\n"
"v_mfma_f32_16x16x32_bf16 v[188:191], v[60:63], v[92:95], v[188:191]\n"
"v_cndmask_b32_e64 v197, v200, v202, s[94:95]\n"
"v_mfma_f32_16x16x32_bf16 v[188:191], v[64:67], v[96:99], v[188:191]\n"
"s_mov_b32 s94, 0xf0000f\n"
"s_mov_b32 s95, 0xf0000f00\n"
"v_mfma_f32_16x16x32_bf16 v[188:191], v[68:71], v[100:103], v[188:191]\n"
"s_add_u32 s98, s63, 0x40000\n"
"v_cndmask_b32_e64 v196, v196, v197, s[94:95]\n"
"v_mfma_f32_16x16x32_bf16 v[188:191], v[72:75], v[104:107], v[188:191]\n"
"s_add_u32 s97, s70, 0x200000\n"
"buffer_load_dwordx4 a[128:131], v0, s[4:7], s97 offen sc1\n"
"v_mfma_f32_16x16x32_bf16 v[188:191], v[76:79], v[112:115], v[188:191]\n"
"ds_write_b128 v3, v[168:171] offset:4096\n"
"v_mfma_f32_16x16x32_bf16 v[188:191], v[80:83], v[116:119], v[188:191]\n"
"buffer_store_dword v196, v14, s[4:7], s98 offen sc1\n"
"v_mfma_f32_16x16x32_bf16 v[188:191], v[84:87], v[124:127], v[188:191]\n"
"s_waitcnt vmcnt(25)\n"
"; @B G1 0.1\n"
"v_mfma_f32_16x16x32_bf16 v[88:91], a[0:3], v[128:131], 0\n"
"v_mfma_f32_16x16x32_bf16 v[88:91], a[4:7], v[132:135], v[88:91]\n"
"v_mfma_f32_16x16x32_bf16 v[88:91], a[8:11], v[136:139], v[88:91]\n"
"s_add_u32 s98, s55, 0x440000\n"
"buffer_load_dwordx4 v[92:95], v0, s[4:7], s98 offen sc1\n"
"v_mfma_f32_16x16x32_bf16 v[88:91], a[12:15], v[140:143], v[88:91]\n"
"s_waitcnt vmcnt(22)\n"
"v_mfma_f32_16x16x32_bf16 v[88:91], a[16:19], v[144:147], v[88:91]\n"
"v_mfma_f32_16x16x32_bf16 v[88:91], a[20:23], v[148:151], v[88:91]\n"
"ds_write_b128 v3, v[188:191] offset:5120\n"
"v_mfma_f32_16x16x32_bf16 v[88:91], a[24:27], v[160:163], v[88:91]\n"
"v_mfma_f32_16x16x32_bf16 v[88:91], a[28:31], v[164:167], v[88:91]\n"
"buffer_load_dwordx4 v[96:99], v0, s[4:7], s98 offen offset:1024 sc1\n"
"v_mfma_f32_16x16x32_bf16 v[100:103], a[32:35], v[128:131], 0\n"
"v_mfma_f32_16x16x32_bf16 v[100:103], a[36:39], v[132:135], v[100:103]\n"
"v_mfma_f32_16x16x32_bf16 v[100:103], a[40:43], v[136:139], v[100:103]\n"
"v_mfma_f32_16x16x32_bf16 v[100:103], a[44:47], v[140:143], v[100:103]\n"
"v_mfma_f32_16x16x32_bf16 v[100:103], a[48:51], v[144:147], v[100:103]\n"
"s_nop 1\n"
"v_cmp_u_f32_e32 vcc, v88, v88\n"
"v_mfma_f32_16x16x32_bf16 v[100:103], a[52:55], v[148:151], v[100:103]\n"
"s_cbranch_vccnz .Lmk_p49s\n"
".Lmk_p49r:\n"
"v_mfma_f32_16x16x32_bf16 v[100:103], a[56:59], v[160:163], v[100:103]\n"
"buffer_load_dwordx4 v[104:107], v0, s[4:7], s98 offen offset:2048 sc1\n"
"v_mfma_f32_16x16x32_bf16 v[100:103], a[60:63], v[164:167], v[100:103]\n"
"ds_write_b128 v3, v[88:91] offset:6144\n"
"v_mfma_f32_16x16x32_bf16 v[88:91], a[64:67], v[128:131], 0\n"
"v_mfma_f32_16x16x32_bf16 v[88:91], a[68:71], v[132:135], v[88:91]\n"
"buffer_load_dwordx4 v[112:115], v0, s[4:7], s98 offen offset:3072 sc1\n"
"v_mfma_f32_16x16x32_bf16 v[88:91], a[72:75], v[136:139], v[88:91]\n"
"v_mfma_f32_16x16x32_bf16 v[88:91], a[76:79], v[140:143], v[88:91]\n"
"v_mfma_f32_16x16x32_bf16 v[88:91], a[80:83], v[144:147], v[88:91]\n"
"buffer_load_dwordx4 a[132:135], v0, s[4:7], s97 offen offset:1024 sc1\n"
"v_mfma_f32_16x16x32_bf16 v[88:91], a[84:87], v[148:151], v[88:91]\n"
"ds_write_b128 v3, v[100:103] offset:7168\n"
"v_mfma_f32_16x16x32_bf16 v[88:91], a[88:91], v[160:163], v[88:91]\n"
"buffer_load_dwordx4 v[100:103], v1, s[4:7], s98 offen sc1\n"
"v_mfma_f32_16x16x32_bf16 v[88:91], a[92:95], v[164:167], v[88:91]\n"
"v_mfma_f32_16x16x32_bf16 v[116:119], a[96:99], v[128:131], 0\n"
"v_mfma_f32_16x16x32_bf16 v[116:119], a[100:103], v[132:135], v[116:119]\n"
"v_mfma_f32_16x16x32_bf16 v[116:119], a[104:107], v[136:139], v[116:119]\n"
"v_mfma_f32_16x16x32_bf16 v[116:119], a[108:111], v[140:143], v[116:119]\n"
"buffer_load_dwordx4 v[124:127], v1, s[4:7], s98 offen offset:1024 sc1\n"
"v_mfma_f32_16x16x32_bf16 v[116:119], a[112:115], v[144:147], v[116:119]\n"
"v_mfma_f32_16x16x32_bf16 v[116:119], a[116:119], v[148:151], v[116:119]\n"
"s_nop 0\n"
"ds_write_b128 v3, v[88:91] offset:8192\n"
"v_mfma_f32_16x16x32_bf16 v[116:119], a[120:123], v[160:163], v[116:119]\n"
"v_mfma_f32_16x16x32_bf16 v[116:119], a[124:127], v[164:167], v[116:119]\n"
"v_mfma_f32_16x16x32_bf16 v[88:91], v[24:27], v[128:131], 0\n"
"buffer_load_dwordx4 v[168:171], v1, s[4:7], s98 offen offset:2048 sc1\n"
"v_mfma_f32_16x16x32_bf16 v[88:91], v[28:31], v[132:135], v[88:91]\n"
"v_mfma_f32_16x16x32_bf16 v[88:91], v[32:35], v[136:139], v[88:91]\n"
"v_mfma_f32_16x16x32_bf16 v[88:91], v[36:39], v[140:143], v[88:91]\n"
"v_mfma_f32_16x16x32_bf16 v[88:91], v[40:43], v[144:147], v[88:91]\n"
"s_waitcnt vmcnt(11)\n"
"v_add_f32_e32 v255, v255, v251\n"
"v_add_f32_e32 v254, v254, v250\n"
"v_mfma_f32_16x16x32_bf16 v[88:91], v[44:47], v[148:151], v[88:91]\n"
"buffer_load_dwordx4 v[188:191], v1, s[4:7], s98 offen offset:3072 sc1\n"
"v_mfma_f32_16x16x32_bf16 v[88:91], v[48:51], v[160:163], v[88:91]\n"
"ds_write_b128 v3, v[116:119] offset:9216\n"
"v_mfma_f32_16x16x32_bf16 v[88:91], v[52:55], v[164:167], v[88:91]\n"
"v_add_f32_e32 v253, v253, v249\n"
"v_add_f32_e32 v252, v252, v248\n"
"v_mfma_f32_16x16x32_bf16 v[116:119], v[56:59], v[128:131], 0\n"
"v_add_f32_e32 v255, v255, v253\n"
"v_add_f32_e32 v254, v254, v252\n"
"v_mfma_f32_16x16x32_bf16 v[116:119], v[60:63], v[132:135], v[116:119]\n"
"v_add_f32_e32 v255, v255, v254\n"
"v_cmp_u_f32_e32 vcc, v255, v255\n"
"v_mfma_f32_16x16x32_bf16 v[116:119], v[64:67], v[136:139], v[116:119]\n"
"s_cbranch_vccnz .Lmk_m52s\n"
".Lmk_m52r:\n"
"v_mov_b32_e32 v254, v255\n"
"v_mfma_f32_16x16x32_bf16 v[116:119], v[68:71], v[140:143], v[116:119]\n"
"s_nop 0\n"
"v_permlane32_swap_b32_e32 v255, v254\n"
"v_add_f32_e32 v255, v255, v254\n"
"v_mfma_f32_16x16x32_bf16 v[116:119], v[72:75], v[144:147], v[116:119]\n"
"buffer_load_dwordx4 a[136:139], v0, s[4:7], s97 offen offset:2048 sc1\n"
"v_mfma_f32_16x16x32_bf16 v[116:119], v[76:79], v[148:151], v[116:119]\n"
"ds_write_b128 v3, v[88:91] offset:10240\n"
"v_mfma_f32_16x16x32_bf16 v[116:119], v[80:83], v[160:163], v[116:119]\n"
"v_mul_f32_e32 v255, 0x3a800000, v255\n"
"v_mov_b32_e32 v254, v255\n"
"v_mfma_f32_16x16x32_bf16 v[116:119], v[84:87], v[164:167], v[116:119]\n"
"s_nop 0\n"
"v_permlane16_swap_b32_e32 v255, v254\n"
"v_cvt_pk_bf16_f32 v253, -v255, 0\n"
"; @B G1 0.2\n"
"v_mfma_f32_16x16x32_bf16 v[88:91], a[0:3], v[176:179], 0\n"
"v_fma_f32 v254, -v255, v255, v254\n"
"v_lshlrev_b32_e32 v253, 16, v253\n"
"v_mfma_f32_16x16x32_bf16 v[88:91], a[4:7], v[184:187], v[88:91]\n"
"v_max_f32_e32 v254, 0, v254\n"
"v_cvt_pk_bf16_f32 v254, v254, 0\n"
"v_mfma_f32_16x16x32_bf16 v[88:91], a[8:11], v[108:111], v[88:91]\n"
"v_lshlrev_b32_e32 v254, 16, v254\n"
"v_add_f32_e32 v254, 0x3727c5ac, v254\n"
"v_mfma_f32_16x16x32_bf16 v[88:91], a[12:15], v[192:195], v[88:91]\n"
"s_waitcnt vmcnt(12)\n"
"v_cvt_pk_bf16_f32 v254, v254, 0\n"
"v_lshlrev_b32_e32 v254, 16, v254\n"
"v_mfma_f32_16x16x32_bf16 v[88:91], a[16:19], v[152:155], v[88:91]\n"
"s_mul_i32 s98, s0, .Lmk_exit-.Lmk_b2\n"
"s_add_u32 s98, s98, .Lmk_b2-.Lmk_g47\n"
"s_getpc_b64 s[84:85]\n"
".Lmk_g47:\n"
"s_add_u32 s84, s84, s98\n"
"s_addc_u32 s85, s85, 0\n"
"v_mfma_f32_16x16x32_bf16 v[88:91], a[20:23], v[156:159], v[88:91]\n"
"ds_write_b128 v3, v[116:119] offset:11264\n"
"v_mfma_f32_16x16x32_bf16 v[88:91], a[24:27], v[172:175], v[88:91]\n"
"s_and_b32 s84, s84, -16\n"
"s_and_b32 s85, s85, 0xffff\n"
"s_sub_u32 s98, .Lmk_end-.Lmk_g47, s98\n"
"s_min_u32 s86, s98, 0x4000\n"
"s_mov_b32 s87, 0x20000\n"
"v_mfma_f32_16x16x32_bf16 v[88:91], a[28:31], v[180:183], v[88:91]\n"
"s_mov_b32 m0, 0x27800\n"
"s_nop 0\n"
"buffer_load_dwordx4 v0, s[84:87], s99 offen lds\n"
"v_mfma_f32_16x16x32_bf16 v[116:119], a[32:35], v[176:179], 0\n"
"v_sqrt_f32_e32 v254, v254\n"
"v_mfma_f32_16x16x32_bf16 v[116:119], a[36:39], v[184:187], v[116:119]\n"
"v_cvt_pk_bf16_f32 v254, v254, 0\n"
"v_lshlrev_b32_e32 v254, 16, v254\n"
"v_mfma_f32_16x16x32_bf16 v[116:119], a[40:43], v[108:111], v[116:119]\n"
"s_add_u32 s98, s52, 0x448000\n"
"buffer_load_dwordx4 v[128:131], v0, s[4:7], s98 offen sc1\n"
"v_mfma_f32_16x16x32_bf16 v[116:119], a[44:47], v[192:195], v[116:119]\n"
"s_waitcnt lgkmcnt(0)\n"
"s_barrier\n"
"v_mfma_f32_16x16x32_bf16 v[116:119], a[48:51], v[152:155], v[116:119]\n"
"s_add_u32 s96, s60, 0x440000\n"
"ds_read_b128 v[132:135], v4\n"
"v_cmp_u_f32_e32 vcc, v88, v88\n"
"v_mfma_f32_16x16x32_bf16 v[116:119], a[52:55], v[156:159], v[116:119]\n"
"s_cbranch_vccnz .Lmk_p50s\n"
".Lmk_p50r:\n"
"ds_read_b128 v[136:139], v9 offset:640\n"
"v_mfma_f32_16x16x32_bf16 v[116:119], a[56:59], v[172:175], v[116:119]\n"
"ds_write_b128 v3, v[88:91] offset:12288\n"
"v_mfma_f32_16x16x32_bf16 v[116:119], a[60:63], v[180:183], v[116:119]\n"
"buffer_load_dwordx4 v[88:91], v0, s[4:7], s98 offen offset:1024 sc1\n"
"v_mfma_f32_16x16x32_bf16 v[140:143], a[64:67], v[176:179], 0\n"
"buffer_store_dwordx4 v[120:123], v0, s[4:7], s96 offen sc1\n"
"v_mfma_f32_16x16x32_bf16 v[140:143], a[68:71], v[184:187], v[140:143]\n"
"v_rcp_f32_e32 v255, v254\n"
"v_mfma_f32_16x16x32_bf16 v[140:143], a[72:75], v[108:111], v[140:143]\n"
"ds_read_b128 v[120:123], v5\n"
"v_mul_f32_e32 v254, 0x3e8293ee, v255\n"
"v_mfma_f32_16x16x32_bf16 v[140:143], a[76:79], v[192:195], v[140:143]\n"
"ds_read_b128 v[144:147], v4 offset:1024\n"
"ds_read_b128 v[148:151], v9 offset:704\n"
"v_mfma_f32_16x16x32_bf16 v[140:143], a[80:83], v[152:155], v[140:143]\n"
"buffer_load_dwordx4 v[160:163], v0, s[4:7], s98 offen offset:2048 sc1\n"
"v_mfma_f32_16x16x32_bf16 v[140:143], a[84:87], v[156:159], v[140:143]\n"
"ds_write_b128 v3, v[116:119] offset:13312\n"
"v_mfma_f32_16x16x32_bf16 v[140:143], a[88:91], v[172:175], v[140:143]\n"
"buffer_load_dwordx4 a[140:143], v0, s[4:7], s97 offen offset:3072 sc1\n"
"v_mfma_f32_16x16x32_bf16 v[140:143], a[92:95], v[180:183], v[140:143]\n"
"ds_read_b128 v[116:119], v5 offset:1024\n"
"s_waitcnt lgkmcnt(4)\n"
"v_mfma_f32_16x16x32_bf16 v[164:167], a[96:99], v[176:179], 0\n"
"v_fmac_f32_e32 v132, v253, v136\n"
"v_fmac_f32_e32 v133, v253, v137\n"
"v_mfma_f32_16x16x32_bf16 v[164:167], a[100:103], v[184:187], v[164:167]\n"
"buffer_load_dwordx4 v[196:199], v0, s[4:7], s98 offen offset:3072 sc1\n"
"v_mfma_f32_16x16x32_bf16 v[164:167], a[104:107], v[108:111], v[164:167]\n"
"ds_read_b128 v[200:203], v4 offset:2048\n"
"ds_read_b128 v[204:207], v9 offset:768\n"
"v_mfma_f32_16x16x32_bf16 v[164:167], a[108:111], v[192:195], v[164:167]\n"
"ds_read_b128 v[208:211], v5 offset:2048\n"
"v_fmac_f32_e32 v134, v253, v138\n"
"v_mfma_f32_16x16x32_bf16 v[164:167], a[112:115], v[152:155], v[164:167]\n"
"v_fmac_f32_e32 v135, v253, v139\n"
"v_add_f32_e32 v132, v132, v120\n"
"v_mfma_f32_16x16x32_bf16 v[164:167], a[116:119], v[156:159], v[164:167]\n"
"ds_write_b128 v3, v[140:143] offset:14336\n"
"v_mfma_f32_16x16x32_bf16 v[164:167], a[120:123], v[172:175], v[164:167]\n"
"buffer_load_dwordx4 v[136:139], v1, s[4:7], s98 offen sc1\n"
"v_mfma_f32_16x16x32_bf16 v[164:167], a[124:127], v[180:183], v[164:167]\n"
"v_add_f32_e32 v133, v133, v121\n"
"v_add_f32_e32 v134, v134, v122\n"
"v_mfma_f32_16x16x32_bf16 v[140:143], v[24:27], v[176:179], 0\n"
"s_waitcnt lgkmcnt(4)\n"
"v_add_f32_e32 v135, v135, v123\n"
"v_fmac_f32_e32 v144, v253, v148\n"
"v_mfma_f32_16x16x32_bf16 v[140:143], v[28:31], v[184:187], v[140:143]\n"
"ds_read_b128 v[120:123], v4 offset:3072\n"
"ds_read_b128 v[212:215], v9 offset:832\n"
"v_mfma_f32_16x16x32_bf16 v[140:143], v[32:35], v[108:111], v[140:143]\n"
"ds_read_b128 v[216:219], v5 offset:3072\n"
"v_fmac_f32_e32 v145, v253, v149\n"
"v_mfma_f32_16x16x32_bf16 v[140:143], v[36:39], v[192:195], v[140:143]\n"
"buffer_load_dwordx4 v[220:223], v1, s[4:7], s98 offen offset:1024 sc1\n"
"v_mfma_f32_16x16x32_bf16 v[140:143], v[40:43], v[152:155], v[140:143]\n"
"v_fmac_f32_e32 v146, v253, v150\n"
"v_fmac_f32_e32 v147, v253, v151\n"
"v_mfma_f32_16x16x32_bf16 v[140:143], v[44:47], v[156:159], v[140:143]\n"
"ds_write_b128 v3, v[164:167] offset:15360\n"
"v_mfma_f32_16x16x32_bf16 v[140:143], v[48:51], v[172:175], v[140:143]\n"
"v_add_f32_e32 v144, v144, v116\n"
"v_add_f32_e32 v145, v145, v117\n"
"v_mfma_f32_16x16x32_bf16 v[140:143], v[52:55], v[180:183], v[140:143]\n"
"s_waitcnt lgkmcnt(5)\n"
"v_add_f32_e32 v146, v146, v118\n"
"v_add_f32_e32 v147, v147, v119\n"
"v_mfma_f32_16x16x32_bf16 v[116:119], v[56:59], v[176:179], 0\n"
"buffer_load_dwordx4 v[148:151], v1, s[4:7], s98 offen offset:2048 sc1\n"
"v_mfma_f32_16x16x32_bf16 v[116:119], v[60:63], v[184:187], v[116:119]\n"
"s_add_u32 s96, s68, s80\n"
"s_add_u32 s96, s96, 0x10000\n"
"s_waitcnt vmcnt(20)\n"
"v_mfma_f32_16x16x32_bf16 v[116:119], v[64:67], v[108:111], v[116:119]\n"
"buffer_store_dwordx2 v[18:19], v11, s[4:7], s96 offen sc1\n"
"v_mfma_f32_16x16x32_bf16 v[116:119], v[68:71], v[192:195], v[116:119]\n"
"ds_read_b128 v[108:111], v4 offset:4096\n"
"ds_read_b128 v[164:167], v9 offset:512\n"
"v_mfma_f32_16x16x32_bf16 v[116:119], v[72:75], v[152:155], v[116:119]\n"
"ds_read_b128 v[152:155], v5 offset:4096\n"
"v_fmac_f32_e32 v200, v253, v204\n"
"v_mfma_f32_16x16x32_bf16 v[116:119], v[76:79], v[156:159], v[116:119]\n"
"buffer_load_dwordx4 v[156:159], v1, s[4:7], s98 offen offset:3072 sc1\n"
"v_mfma_f32_16x16x32_bf16 v[116:119], v[80:83], v[172:175], v[116:119]\n"
"ds_write_b128 v3, v[140:143] offset:16384\n"
"v_mfma_f32_16x16x32_bf16 v[116:119], v[84:87], v[180:183], v[116:119]\n"
"s_waitcnt vmcnt(18)\n"
"buffer_load_dwordx4 a[144:147], v1, s[4:7], s97 offen sc1\n"
"; @B G1 0.3\n"
"v_mfma_f32_16x16x32_bf16 v[140:143], a[0:3], v[92:95], v[132:135]\n"
"ds_read_b128 v[172:175], v4 offset:5120\n"
"ds_read_b128 v[176:179], v9 offset:576\n"
"v_mfma_f32_16x16x32_bf16 v[140:143], a[4:7], v[96:99], v[140:143]\n"
"ds_read_b128 v[180:183], v5 offset:5120\n"
"v_fmac_f32_e32 v201, v253, v205\n"
"v_mfma_f32_16x16x32_bf16 v[140:143], a[8:11], v[104:107], v[140:143]\n"
"s_add_u32 s98, s53, 0x448000\n"
"buffer_load_dwordx4 v[184:187], v0, s[4:7], s98 offen sc1\n"
"v_mfma_f32_16x16x32_bf16 v[140:143], a[12:15], v[112:115], v[140:143]\n"
"s_waitcnt vmcnt(15)\n"
"v_fmac_f32_e32 v202, v253, v206\n"
"v_fmac_f32_e32 v203, v253, v207\n"
"v_mfma_f32_16x16x32_bf16 v[140:143], a[16:19], v[100:103], v[140:143]\n"
"v_add_f32_e32 v200, v200, v208\n"
"v_add_f32_e32 v201, v201, v209\n"
"v_mfma_f32_16x16x32_bf16 v[140:143], a[20:23], v[124:127], v[140:143]\n"
"ds_write_b128 v3, v[116:119] offset:17408\n"
"v_mfma_f32_16x16x32_bf16 v[140:143], a[24:27], v[168:171], v[140:143]\n"
"s_waitcnt lgkmcnt(9)\n"
"v_add_f32_e32 v202, v202, v210\n"
"v_add_f32_e32 v203, v203, v211\n"
"v_mfma_f32_16x16x32_bf16 v[140:143], a[28:31], v[188:191], v[140:143]\n"
"buffer_load_dwordx4 v[116:119], v0, s[4:7], s98 offen offset:1024 sc1\n"
"v_mfma_f32_16x16x32_bf16 v[192:195], a[32:35], v[92:95], v[144:147]\n"
"v_fmac_f32_e32 v120, v253, v212\n"
"v_fmac_f32_e32 v121, v253, v213\n"
"v_mfma_f32_16x16x32_bf16 v[192:195], a[36:39], v[96:99], v[192:195]\n"
"v_fmac_f32_e32 v122, v253, v214\n"
"v_fmac_f32_e32 v123, v253, v215\n"
"v_mfma_f32_16x16x32_bf16 v[192:195], a[40:43], v[104:107], v[192:195]\n"
"s_waitcnt lgkmcnt(0)\n"
"s_barrier\n"
"v_mfma_f32_16x16x32_bf16 v[192:195], a[44:47], v[112:115], v[192:195]\n"
"ds_read_b128 v[204:207], v6\n"
"ds_read_b128 v[208:211], v6 offset:1024\n"
"v_mfma_f32_16x16x32_bf16 v[192:195], a[48:51], v[100:103], v[192:195]\n"
"v_cmp_u_f32_e32 vcc, v140, v140\n"
"v_add_f32_e32 v120, v120, v216\n"
"v_mfma_f32_16x16x32_bf16 v[192:195], a[52:55], v[124:127], v[192:195]\n"
"s_cbranch_vccnz .Lmk_p51s\n"
".Lmk_p51r:\n"
"v_add_f32_e32 v121, v121, v217\n"
"v_mfma_f32_16x16x32_bf16 v[192:195], a[56:59], v[168:171], v[192:195]\n"
"buffer_load_dwordx4 v[132:135], v0, s[4:7], s98 offen offset:2048 sc1\n"
"v_mfma_f32_16x16x32_bf16 v[192:195], a[60:63], v[188:191], v[192:195]\n"
"v_add_f32_e32 v122, v122, v218\n"
"v_add_f32_e32 v123, v123, v219\n"
"v_mfma_f32_16x16x32_bf16 v[200:203], a[64:67], v[92:95], v[200:203]\n"
"v_fmac_f32_e32 v108, v253, v164\n"
"v_fmac_f32_e32 v109, v253, v165\n"
"v_mfma_f32_16x16x32_bf16 v[200:203], a[68:71], v[96:99], v[200:203]\n"
"buffer_load_dwordx4 v[144:147], v0, s[4:7], s98 offen offset:3072 sc1\n"
"v_mfma_f32_16x16x32_bf16 v[200:203], a[72:75], v[104:107], v[200:203]\n"
"v_fmac_f32_e32 v110, v253, v166\n"
"v_fmac_f32_e32 v111, v253, v167\n"
"v_mfma_f32_16x16x32_bf16 v[200:203], a[76:79], v[112:115], v[200:203]\n"
"v_add_f32_e32 v108, v108, v152\n"
"v_add_f32_e32 v109, v109, v153\n"
"v_mfma_f32_16x16x32_bf16 v[200:203], a[80:83], v[100:103], v[200:203]\n"
"v_add_f32_e32 v110, v110, v154\n"
"v_add_f32_e32 v111, v111, v155\n"
"v_mfma_f32_16x16x32_bf16 v[200:203], a[84:87], v[124:127], v[200:203]\n"
"s_waitcnt lgkmcnt(0)\n"
"v_fmac_f32_e32 v172, v253, v176\n"
"v_fmac_f32_e32 v173, v253, v177\n"
"v_mfma_f32_16x16x32_bf16 v[200:203], a[88:91], v[168:171], v[200:203]\n"
"buffer_load_dwordx4 v[152:155], v1, s[4:7], s98 offen sc1\n"
"v_mfma_f32_16x16x32_bf16 v[200:203], a[92:95], v[188:191], v[200:203]\n"
"buffer_load_dwordx4 a[148:151], v1, s[4:7], s97 offen offset:1024 sc1\n"
"v_mfma_f32_16x16x32_bf16 v[120:123], a[96:99], v[92:95], v[120:123]\n"
"ds_read_b128 v[164:167], v6 offset:2048\n"
"ds_read_b128 v[212:215], v6 offset:3072\n"
"v_mfma_f32_16x16x32_bf16 v[120:123], a[100:103], v[96:99], v[120:123]\n"
"v_fmac_f32_e32 v174, v253, v178\n"
"v_fmac_f32_e32 v175, v253, v179\n"
"v_mfma_f32_16x16x32_bf16 v[120:123], a[104:107], v[104:107], v[120:123]\n"
"v_add_f32_e32 v172, v172, v180\n"
"v_add_f32_e32 v173, v173, v181\n"
"v_mfma_f32_16x16x32_bf16 v[120:123], a[108:111], v[112:115], v[120:123]\n"
"buffer_load_dwordx4 v[176:179], v1, s[4:7], s98 offen offset:1024 sc1\n"
"v_mfma_f32_16x16x32_bf16 v[120:123], a[112:115], v[100:103], v[120:123]\n"
"v_add_f32_e32 v174, v174, v182\n"
"v_add_f32_e32 v175, v175, v183\n"
"v_mfma_f32_16x16x32_bf16 v[120:123], a[116:119], v[124:127], v[120:123]\n"
"v_add_f32_e32 v140, v140, v204\n"
"v_add_f32_e32 v141, v141, v205\n"
"v_mfma_f32_16x16x32_bf16 v[120:123], a[120:123], v[168:171], v[120:123]\n"
"v_add_f32_e32 v142, v142, v206\n"
"v_add_f32_e32 v143, v143, v207\n"
"v_mfma_f32_16x16x32_bf16 v[120:123], a[124:127], v[188:191], v[120:123]\n"
"v_add_f32_e32 v192, v192, v208\n"
"v_add_f32_e32 v193, v193, v209\n"
"v_mfma_f32_16x16x32_bf16 v[108:111], v[24:27], v[92:95], v[108:111]\n"
"buffer_load_dwordx4 v[180:183], v1, s[4:7], s98 offen offset:2048 sc1\n"
"v_mfma_f32_16x16x32_bf16 v[108:111], v[28:31], v[96:99], v[108:111]\n"
"v_add_f32_e32 v194, v194, v210\n"
"v_add_f32_e32 v195, v195, v211\n"
"v_mfma_f32_16x16x32_bf16 v[108:111], v[32:35], v[104:107], v[108:111]\n"
"v_mul_f32_e32 v140, v140, v255\n"
"v_mul_f32_e32 v141, v141, v255\n"
"v_mfma_f32_16x16x32_bf16 v[108:111], v[36:39], v[112:115], v[108:111]\n"
"v_mul_f32_e32 v142, v142, v255\n"
"v_mul_f32_e32 v143, v143, v255\n"
"v_mfma_f32_16x16x32_bf16 v[108:111], v[40:43], v[100:103], v[108:111]\n"
"s_waitcnt lgkmcnt(0)\n"
"v_mul_f32_e32 v192, v192, v255\n"
"v_mul_f32_e32 v193, v193, v255\n"
"v_mfma_f32_16x16x32_bf16 v[108:111], v[44:47], v[124:127], v[108:111]\n"
"buffer_load_dwordx4 v[204:207], v1, s[4:7], s98 offen offset:3072 sc1\n"
"v_mfma_f32_16x16x32_bf16 v[108:111], v[48:51], v[168:171], v[108:111]\n"
"ds_read_b128 v[208:211], v6 offset:4096\n"
"ds_read_b128 v[216:219], v6 offset:5120\n"
"v_mfma_f32_16x16x32_bf16 v[108:111], v[52:55], v[188:191], v[108:111]\n"
"v_mul_f32_e32 v194, v194, v255\n"
"v_mul_f32_e32 v195, v195, v255\n"
"v_mfma_f32_16x16x32_bf16 v[172:175], v[56:59], v[92:95], v[172:175]\n"
"v_cvt_pk_bf16_f32 v92, v140, v141\n"
"v_cvt_pk_bf16_f32 v93, v142, v143\n"
"v_mfma_f32_16x16x32_bf16 v[172:175], v[60:63], v[96:99], v[172:175]\n"
"v_cvt_pk_bf16_f32 v94, v192, v193\n"
"v_cvt_pk_bf16_f32 v95, v194, v195\n"
"v_mfma_f32_16x16x32_bf16 v[172:175], v[64:67], v[104:107], v[172:175]\n"
"ds_write_b128 v7, v[92:95] offset:24576\n"
"v_mfma_f32_16x16x32_bf16 v[172:175], v[68:71], v[112:115], v[172:175]\n"
"v_add_f32_e32 v200, v200, v164\n"
"v_add_f32_e32 v201, v201, v165\n"
"v_mfma_f32_16x16x32_bf16 v[172:175], v[72:75], v[100:103], v[172:175]\n"
"v_add_f32_e32 v202, v202, v166\n"
"v_add_f32_e32 v203, v203, v167\n"
"v_mfma_f32_16x16x32_bf16 v[172:175], v[76:79], v[124:127], v[172:175]\n"
"v_add_f32_e32 v120, v120, v212\n"
"v_add_f32_e32 v121, v121, v213\n"
"v_mfma_f32_16x16x32_bf16 v[172:175], v[80:83], v[168:171], v[172:175]\n"
"v_add_f32_e32 v122, v122, v214\n"
"v_add_f32_e32 v123, v123, v215\n"
"v_mfma_f32_16x16x32_bf16 v[172:175], v[84:87], v[188:191], v[172:175]\n"
"v_mul_f32_e32 v200, v200, v255\n"
"v_mul_f32_e32 v201, v201, v255\n"
"v_mul_f32_e32 v202, v202, v255\n"
"v_mul_f32_e32 v203, v203, v255\n"
"v_mul_f32_e32 v120, v120, v255\n"
"v_mul_f32_e32 v121, v121, v255\n"
"v_mul_f32_e32 v122, v122, v255\n"
"v_mul_f32_e32 v123, v123, v255\n"
"v_cvt_pk_bf16_f32 v92, v200, v201\n"
"v_cvt_pk_bf16_f32 v93, v202, v203\n"
"v_cvt_pk_bf16_f32 v94, v120, v121\n"
"v_cvt_pk_bf16_f32 v95, v122, v123\n"
"ds_write_b128 v7, v[92:95] offset:57344\n"
".Lmk_b1:\n"
"s_waitcnt vmcnt(15)\n"
"; @B G1 1.0\n"
"v_mfma_f32_16x16x32_bf16 v[92:95], a[0:3], v[128:131], 0\n"
"v_mfma_f32_16x16x32_bf16 v[92:95], a[4:7], v[88:91], v[92:95]\n"
"s_add_u32 s98, s62, 0x44800\n"
"v_mfma_f32_16x16x32_bf16 v[92:95], a[8:11], v[160:163], v[92:95]\n"
"s_add_u32 s96, s54, 0x448000\n"
"buffer_load_dwordx4 v[96:99], v0, s[4:7], s96 offen sc1\n"
"v_mfma_f32_16x16x32_bf16 v[92:95], a[12:15], v[196:199], v[92:95]\n"
"s_waitcnt vmcnt(11)\n"
"v_mfma_f32_16x16x32_bf16 v[92:95], a[16:19], v[136:139], v[92:95]\n"
"buffer_load_dword v255, v10, s[4:7], s98 offen sc1\n"
"v_mfma_f32_16x16x32_bf16 v[92:95], a[20:23], v[220:223], v[92:95]\n"
"s_waitcnt lgkmcnt(2)\n"
"v_add_f32_e32 v108, v108, v208\n"
"v_add_f32_e32 v109, v109, v209\n"
"v_mfma_f32_16x16x32_bf16 v[92:95], a[24:27], v[148:151], v[92:95]\n"
"v_add_f32_e32 v110, v110, v210\n"
"v_add_f32_e32 v111, v111, v211\n"
"v_mfma_f32_16x16x32_bf16 v[92:95], a[28:31], v[156:159], v[92:95]\n"
"buffer_load_dwordx4 v[100:103], v0, s[4:7], s96 offen offset:1024 sc1\n"
"v_mfma_f32_16x16x32_bf16 v[104:107], a[32:35], v[128:131], 0\n"
"buffer_load_dwordx4 a[152:155], v1, s[4:7], s97 offen offset:2048 sc1\n"
"v_mfma_f32_16x16x32_bf16 v[104:107], a[36:39], v[88:91], v[104:107]\n"
"buffer_load_dword v253, v10, s[4:7], s98 offen offset:256 sc1\n"
"v_mfma_f32_16x16x32_bf16 v[104:107], a[40:43], v[160:163], v[104:107]\n"
"v_add_f32_e32 v172, v172, v216\n"
"v_add_f32_e32 v173, v173, v217\n"
"v_mfma_f32_16x16x32_bf16 v[104:107], a[44:47], v[196:199], v[104:107]\n"
"v_add_f32_e32 v174, v174, v218\n"
"v_add_f32_e32 v175, v175, v219\n"
"v_mfma_f32_16x16x32_bf16 v[104:107], a[48:51], v[136:139], v[104:107]\n"
"v_cmp_u_f32_e32 vcc, v92, v92\n"
"v_mul_f32_e32 v108, v108, v254\n"
"v_mfma_f32_16x16x32_bf16 v[104:107], a[52:55], v[220:223], v[104:107]\n"
"s_cbranch_vccnz .Lmk_p53s\n"
".Lmk_p53r:\n"
"v_mul_f32_e32 v109, v109, v254\n"
"v_mfma_f32_16x16x32_bf16 v[104:107], a[56:59], v[148:151], v[104:107]\n"
"buffer_load_dwordx4 v[112:115], v0, s[4:7], s96 offen offset:2048 sc1\n"
"v_mfma_f32_16x16x32_bf16 v[104:107], a[60:63], v[156:159], v[104:107]\n"
"ds_write_b128 v3, v[92:95]\n"
"v_mfma_f32_16x16x32_bf16 v[92:95], a[64:67], v[128:131], 0\n"
"buffer_load_dword v252, v10, s[4:7], s98 offen offset:512 sc1\n"
"v_mfma_f32_16x16x32_bf16 v[92:95], a[68:71], v[88:91], v[92:95]\n"
"buffer_load_dwordx4 v[120:123], v0, s[4:7], s96 offen offset:3072 sc1\n"
"v_mfma_f32_16x16x32_bf16 v[92:95], a[72:75], v[160:163], v[92:95]\n"
"v_mul_f32_e32 v110, v110, v254\n"
"v_mul_f32_e32 v111, v111, v254\n"
"v_mfma_f32_16x16x32_bf16 v[92:95], a[76:79], v[196:199], v[92:95]\n"
"buffer_load_dword v251, v10, s[4:7], s98 offen offset:768 sc1\n"
"v_mfma_f32_16x16x32_bf16 v[92:95], a[80:83], v[136:139], v[92:95]\n"
"v_mul_f32_e32 v172, v172, v254\n"
"v_mul_f32_e32 v173, v173, v254\n"
"v_mfma_f32_16x16x32_bf16 v[92:95], a[84:87], v[220:223], v[92:95]\n"
"ds_write_b128 v3, v[104:107] offset:1024\n"
"v_mfma_f32_16x16x32_bf16 v[92:95], a[88:91], v[148:151], v[92:95]\n"
"buffer_load_dwordx4 v[104:107], v1, s[4:7], s96 offen sc1\n"
"v_mfma_f32_16x16x32_bf16 v[92:95], a[92:95], v[156:159], v[92:95]\n"
"buffer_load_dword v250, v10, s[4:7], s98 offen offset:1024 sc1\n"
"v_mfma_f32_16x16x32_bf16 v[124:127], a[96:99], v[128:131], 0\n"
"v_mul_f32_e32 v174, v174, v254\n"
"v_mul_f32_e32 v175, v175, v254\n"
"v_mfma_f32_16x16x32_bf16 v[124:127], a[100:103], v[88:91], v[124:127]\n"
"v_cvt_pk_bf16_f32 v140, v108, v109\n"
"v_cvt_pk_bf16_f32 v141, v110, v111\n"
"v_mfma_f32_16x16x32_bf16 v[124:127], a[104:107], v[160:163], v[124:127]\n"
"buffer_load_dword v254, v10, s[4:7], s98 offen offset:1280 sc1\n"
"v_mfma_f32_16x16x32_bf16 v[124:127], a[108:111], v[196:199], v[124:127]\n"
"buffer_load_dwordx4 v[108:111], v1, s[4:7], s96 offen offset:1024 sc1\n"
"v_mfma_f32_16x16x32_bf16 v[124:127], a[112:115], v[136:139], v[124:127]\n"
"v_cvt_pk_bf16_f32 v142, v172, v173\n"
"v_cvt_pk_bf16_f32 v143, v174, v175\n"
"v_mfma_f32_16x16x32_bf16 v[124:127], a[116:119], v[220:223], v[124:127]\n"
"ds_write_b128 v3, v[92:95] offset:2048\n"
"v_mfma_f32_16x16x32_bf16 v[124:127], a[120:123], v[148:151], v[124:127]\n"
"buffer_load_dword v249, v10, s[4:7], s98 offen offset:1536 sc1\n"
"v_mfma_f32_16x16x32_bf16 v[124:127], a[124:127], v[156:159], v[124:127]\n"
"ds_write_b128 v2, v[140:143] offset:49152\n"
"v_mfma_f32_16x16x32_bf16 v[92:95], v[24:27], v[128:131], 0\n"
"buffer_load_dwordx4 v[140:143], v1, s[4:7], s96 offen offset:2048 sc1\n"
"v_mfma_f32_16x16x32_bf16 v[92:95], v[28:31], v[88:91], v[92:95]\n"
"buffer_load_dwordx4 a[156:159], v1, s[4:7], s97 offen offset:3072 sc1\n"
"v_mfma_f32_16x16x32_bf16 v[92:95], v[32:35], v[160:163], v[92:95]\n"
"buffer_load_dword v248, v10, s[4:7], s98 offen offset:1792 sc1\n"
"v_mfma_f32_16x16x32_bf16 v[92:95], v[36:39], v[196:199], v[92:95]\n"
"v_mfma_f32_16x16x32_bf16 v[92:95], v[40:43], v[136:139], v[92:95]\n"
"v_mfma_f32_16x16x32_bf16 v[92:95], v[44:47], v[220:223], v[92:95]\n"
"buffer_load_dwordx4 v[164:167], v1, s[4:7], s96 offen offset:3072 sc1\n"
"v_mfma_f32_16x16x32_bf16 v[92:95], v[48:51], v[148:151], v[92:95]\n"
"ds_write_b128 v3, v[124:127] offset:3072\n"
"v_mfma_f32_16x16x32_bf16 v[92:95], v[52:55], v[156:159], v[92:95]\n"
"v_mfma_f32_16x16x32_bf16 v[124:127], v[56:59], v[128:131], 0\n"
"v_mfma_f32_16x16x32_bf16 v[124:127], v[60:63], v[88:91], v[124:127]\n"
"v_mfma_f32_16x16x32_bf16 v[124:127], v[64:67], v[160:163], v[124:127]\n"
"v_mfma_f32_16x16x32_bf16 v[124:127], v[68:71], v[196:199], v[124:127]\n"
"v_mfma_f32_16x16x32_bf16 v[124:127], v[72:75], v[136:139], v[124:127]\n"
"v_mfma_f32_16x16x32_bf16 v[124:127], v[76:79], v[220:223], v[124:127]\n"
"s_nop 1\n"
"ds_write_b128 v3, v[92:95] offset:4096\n"
"v_mfma_f32_16x16x32_bf16 v[124:127], v[80:83], v[148:151], v[124:127]\n"
"v_mfma_f32_16x16x32_bf16 v[124:127], v[84:87], v[156:159], v[124:127]\n"
"s_waitcnt vmcnt(23)\n"
"; @B G1 1.1\n"
"v_mfma_f32_16x16x32_bf16 v[88:91], a[0:3], v[184:187], 0\n"
"v_mfma_f32_16x16x32_bf16 v[88:91], a[4:7], v[116:119], v[88:91]\n"
"v_mfma_f32_16x16x32_bf16 v[88:91], a[8:11], v[132:135], v[88:91]\n"
"s_add_u32 s98, s55, 0x448000\n"
"buffer_load_dwordx4 v[92:95], v0, s[4:7], s98 offen sc1\n"
"v_mfma_f32_16x16x32_bf16 v[88:91], a[12:15], v[144:147], v[88:91]\n"
"s_waitcnt vmcnt(19)\n"
"v_mfma_f32_16x16x32_bf16 v[88:91], a[16:19], v[152:155], v[88:91]\n"
"v_mfma_f32_16x16x32_bf16 v[88:91], a[20:23], v[176:179], v[88:91]\n"
"ds_write_b128 v3, v[124:127] offset:5120\n"
"v_mfma_f32_16x16x32_bf16 v[88:91], a[24:27], v[180:183], v[88:91]\n"
"v_mfma_f32_16x16x32_bf16 v[88:91], a[28:31], v[204:207], v[88:91]\n"
"buffer_load_dwordx4 v[124:127], v0, s[4:7], s98 offen offset:1024 sc1\n"
"v_mfma_f32_16x16x32_bf16 v[128:131], a[32:35], v[184:187], 0\n"
"s_add_u32 s97, s70, 0x202000\n"
"v_mfma_f32_16x16x32_bf16 v[128:131], a[36:39], v[116:119], v[128:131]\n"
"buffer_load_dwordx4 a[160:163], v0, s[4:7], s97 offen sc1\n"
"v_mfma_f32_16x16x32_bf16 v[128:131], a[40:43], v[132:135], v[128:131]\n"
"v_mfma_f32_16x16x32_bf16 v[128:131], a[44:47], v[144:147], v[128:131]\n"
"v_mfma_f32_16x16x32_bf16 v[128:131], a[48:51], v[152:155], v[128:131]\n"
"s_add_u32 s96, s67, 0x10000\n"
"v_cmp_u_f32_e32 vcc, v88, v88\n"
"v_mfma_f32_16x16x32_bf16 v[128:131], a[52:55], v[176:179], v[128:131]\n"
"s_cbranch_vccnz .Lmk_p54s\n"
".Lmk_p54r:\n"
"v_mfma_f32_16x16x32_bf16 v[128:131], a[56:59], v[180:183], v[128:131]\n"
"buffer_load_dwordx4 v[136:139], v0, s[4:7], s98 offen offset:2048 sc1\n"
"v_mfma_f32_16x16x32_bf16 v[128:131], a[60:63], v[204:207], v[128:131]\n"
"ds_write_b128 v3, v[88:91] offset:6144\n"
"v_mfma_f32_16x16x32_bf16 v[88:91], a[64:67], v[184:187], 0\n"
"buffer_load_dwordx2 v[246:247], v12, s[4:7], s96 offen sc1\n"
"v_mfma_f32_16x16x32_bf16 v[88:91], a[68:71], v[116:119], v[88:91]\n"
"buffer_load_dwordx4 v[148:151], v0, s[4:7], s98 offen offset:3072 sc1\n"
"v_mfma_f32_16x16x32_bf16 v[88:91], a[72:75], v[132:135], v[88:91]\n"
"v_mfma_f32_16x16x32_bf16 v[88:91], a[76:79], v[144:147], v[88:91]\n"
"v_mfma_f32_16x16x32_bf16 v[88:91], a[80:83], v[152:155], v[88:91]\n"
"v_mov_b32_e32 v156, -1\n"
"v_mov_b32_e32 v157, -1\n"
"v_mfma_f32_16x16x32_bf16 v[88:91], a[84:87], v[176:179], v[88:91]\n"
"ds_write_b128 v3, v[128:131] offset:7168\n"
"v_mfma_f32_16x16x32_bf16 v[88:91], a[88:91], v[180:183], v[88:91]\n"
"buffer_load_dwordx4 v[128:131], v1, s[4:7], s98 offen sc1\n"
"v_mfma_f32_16x16x32_bf16 v[88:91], a[92:95], v[204:207], v[88:91]\n"
"v_mov_b32_e32 v158, -1\n"
"v_mov_b32_e32 v159, -1\n"
"v_mfma_f32_16x16x32_bf16 v[160:163], a[96:99], v[184:187], 0\n"
"s_add_u32 s95, s60, 0x400000\n"
"buffer_store_dwordx4 v[156:159], v0, s[4:7], s95 offen sc1\n"
"v_mfma_f32_16x16x32_bf16 v[160:163], a[100:103], v[116:119], v[160:163]\n"
"v_mfma_f32_16x16x32_bf16 v[160:163], a[104:107], v[132:135], v[160:163]\n"
"v_mfma_f32_16x16x32_bf16 v[160:163], a[108:111], v[144:147], v[160:163]\n"
"buffer_load_dwordx4 v[168:171], v1, s[4:7], s98 offen offset:1024 sc1\n"
"v_mfma_f32_16x16x32_bf16 v[160:163], a[112:115], v[152:155], v[160:163]\n"
"v_mfma_f32_16x16x32_bf16 v[160:163], a[116:119], v[176:179], v[160:163]\n"
"ds_write_b128 v3, v[88:91] offset:8192\n"
"v_mfma_f32_16x16x32_bf16 v[160:163], a[120:123], v[180:183], v[160:163]\n"
"v_mfma_f32_16x16x32_bf16 v[160:163], a[124:127], v[204:207], v[160:163]\n"
"v_mfma_f32_16x16x32_bf16 v[88:91], v[24:27], v[184:187], 0\n"
"buffer_load_dwordx4 v[172:175], v1, s[4:7], s98 offen offset:2048 sc1\n"
"v_mfma_f32_16x16x32_bf16 v[88:91], v[28:31], v[116:119], v[88:91]\n"
"buffer_load_dwordx4 a[164:167], v0, s[4:7], s97 offen offset:1024 sc1\n"
"v_mfma_f32_16x16x32_bf16 v[88:91], v[32:35], v[132:135], v[88:91]\n"
"v_mfma_f32_16x16x32_bf16 v[88:91], v[36:39], v[144:147], v[88:91]\n"
"v_mfma_f32_16x16x32_bf16 v[88:91], v[40:43], v[152:155], v[88:91]\n"
"s_waitcnt vmcnt(12)\n"
"v_add_f32_e32 v255, v255, v250\n"
"v_add_f32_e32 v253, v253, v254\n"
"v_mfma_f32_16x16x32_bf16 v[88:91], v[44:47], v[176:179], v[88:91]\n"
"buffer_load_dwordx4 v[188:191], v1, s[4:7], s98 offen offset:3072 sc1\n"
"v_mfma_f32_16x16x32_bf16 v[88:91], v[48:51], v[180:183], v[88:91]\n"
"ds_write_b128 v3, v[160:163] offset:9216\n"
"v_mfma_f32_16x16x32_bf16 v[88:91], v[52:55], v[204:207], v[88:91]\n"
"v_add_f32_e32 v252, v252, v249\n"
"v_add_f32_e32 v251, v251, v248\n"
"v_mfma_f32_16x16x32_bf16 v[160:163], v[56:59], v[184:187], 0\n"
"v_add_f32_e32 v255, v255, v252\n"
"v_add_f32_e32 v253, v253, v251\n"
"v_mfma_f32_16x16x32_bf16 v[160:163], v[60:63], v[116:119], v[160:163]\n"
"v_add_f32_e32 v255, v255, v253\n"
"v_cmp_u_f32_e32 vcc, v255, v255\n"
"v_mfma_f32_16x16x32_bf16 v[160:163], v[64:67], v[132:135], v[160:163]\n"
"s_cbranch_vccnz .Lmk_m57s\n"
".Lmk_m57r:\n"
"v_mov_b32_e32 v253, v255\n"
"v_mfma_f32_16x16x32_bf16 v[160:163], v[68:71], v[144:147], v[160:163]\n"
"s_nop 0\n"
"v_permlane32_swap_b32_e32 v255, v253\n"
"v_add_f32_e32 v255, v255, v253\n"
"v_mfma_f32_16x16x32_bf16 v[160:163], v[72:75], v[152:155], v[160:163]\n"
"v_mul_f32_e32 v255, 0x3a800000, v255\n"
"v_mov_b32_e32 v253, v255\n"
"v_mfma_f32_16x16x32_bf16 v[160:163], v[76:79], v[176:179], v[160:163]\n"
"ds_write_b128 v3, v[88:91] offset:10240\n"
"v_mfma_f32_16x16x32_bf16 v[160:163], v[80:83], v[180:183], v[160:163]\n"
"v_permlane16_swap_b32_e32 v255, v253\n"
"v_cvt_pk_bf16_f32 v254, -v255, 0\n"
"v_mfma_f32_16x16x32_bf16 v[160:163], v[84:87], v[204:207], v[160:163]\n"
"v_fma_f32 v253, -v255, v255, v253\n"
"v_lshlrev_b32_e32 v254, 16, v254\n"
"; @B G1 1.2\n"
"v_mfma_f32_16x16x32_bf16 v[88:91], a[0:3], v[96:99], 0\n"
"v_max_f32_e32 v253, 0, v253\n"
"v_cvt_pk_bf16_f32 v253, v253, 0\n"
"v_mfma_f32_16x16x32_bf16 v[88:91], a[4:7], v[100:103], v[88:91]\n"
"s_waitcnt vmcnt(7)\n"
"v_cmp_ne_u64_e32 vcc, v[246:247], v[18:19]\n"
"v_mfma_f32_16x16x32_bf16 v[88:91], a[8:11], v[112:115], v[88:91]\n"
"s_cbranch_vccnz .Lmk_f44s\n"
".Lmk_f44r:\n"
"v_lshlrev_b32_e32 v253, 16, v253\n"
"v_mfma_f32_16x16x32_bf16 v[88:91], a[12:15], v[120:123], v[88:91]\n"
"v_add_f32_e32 v253, 0x3727c5ac, v253\n"
"v_cvt_pk_bf16_f32 v253, v253, 0\n"
"v_mfma_f32_16x16x32_bf16 v[88:91], a[16:19], v[104:107], v[88:91]\n"
"v_lshlrev_b32_e32 v253, 16, v253\n"
"v_mfma_f32_16x16x32_bf16 v[88:91], a[20:23], v[108:111], v[88:91]\n"
"ds_write_b128 v3, v[160:163] offset:11264\n"
"v_mfma_f32_16x16x32_bf16 v[88:91], a[24:27], v[140:143], v[88:91]\n"
"v_sqrt_f32_e32 v253, v253\n"
"v_mfma_f32_16x16x32_bf16 v[88:91], a[28:31], v[164:167], v[88:91]\n"
"v_cvt_pk_bf16_f32 v253, v253, 0\n"
"v_lshlrev_b32_e32 v253, 16, v253\n"
"v_mfma_f32_16x16x32_bf16 v[116:119], a[32:35], v[96:99], 0\n"
"v_rcp_f32_e32 v255, v253\n"
"v_mfma_f32_16x16x32_bf16 v[116:119], a[36:39], v[100:103], v[116:119]\n"
"buffer_load_dwordx4 a[168:171], v0, s[4:7], s97 offen offset:2048 sc1\n"
"v_mfma_f32_16x16x32_bf16 v[116:119], a[40:43], v[112:115], v[116:119]\n"
"s_add_u32 s98, s52, 0x408000\n"
"buffer_load_dwordx4 v[132:135], v0, s[4:7], s98 offen sc1\n"
"v_mfma_f32_16x16x32_bf16 v[116:119], a[44:47], v[120:123], v[116:119]\n"
"s_waitcnt lgkmcnt(0)\n"
"s_barrier\n"
"v_mfma_f32_16x16x32_bf16 v[116:119], a[48:51], v[104:107], v[116:119]\n"
"s_add_u32 s96, s60, 0x408000\n"
"v_cmp_u_f32_e32 vcc, v88, v88\n"
"v_mul_f32_e32 v253, 0x3e8293ee, v255\n"
"v_mfma_f32_16x16x32_bf16 v[116:119], a[52:55], v[108:111], v[116:119]\n"
"s_cbranch_vccnz .Lmk_p55s\n"
".Lmk_p55r:\n"
"ds_read_b128 v[144:147], v4\n"
"v_mfma_f32_16x16x32_bf16 v[116:119], a[56:59], v[140:143], v[116:119]\n"
"ds_write_b128 v3, v[88:91] offset:12288\n"
"v_mfma_f32_16x16x32_bf16 v[116:119], a[60:63], v[164:167], v[116:119]\n"
"buffer_load_dwordx4 v[88:91], v0, s[4:7], s98 offen offset:1024 sc1\n"
"v_mfma_f32_16x16x32_bf16 v[152:155], a[64:67], v[96:99], 0\n"
"buffer_store_dwordx4 v[156:159], v0, s[4:7], s96 offen sc1\n"
"v_mfma_f32_16x16x32_bf16 v[152:155], a[68:71], v[100:103], v[152:155]\n"
"ds_read_b128 v[160:163], v9 offset:640\n"
"ds_read_b128 v[176:179], v5\n"
"v_mfma_f32_16x16x32_bf16 v[152:155], a[72:75], v[112:115], v[152:155]\n"
"v_mfma_f32_16x16x32_bf16 v[152:155], a[76:79], v[120:123], v[152:155]\n"
"ds_read_b128 v[180:183], v4 offset:1024\n"
"ds_read_b128 v[184:187], v9 offset:704\n"
"v_mfma_f32_16x16x32_bf16 v[152:155], a[80:83], v[104:107], v[152:155]\n"
"buffer_load_dwordx4 v[192:195], v0, s[4:7], s98 offen offset:2048 sc1\n"
"v_mfma_f32_16x16x32_bf16 v[152:155], a[84:87], v[108:111], v[152:155]\n"
"ds_write_b128 v3, v[116:119] offset:13312\n"
"v_mfma_f32_16x16x32_bf16 v[152:155], a[88:91], v[140:143], v[152:155]\n"
"ds_read_b128 v[116:119], v5 offset:1024\n"
"s_waitcnt lgkmcnt(4)\n"
"v_mfma_f32_16x16x32_bf16 v[152:155], a[92:95], v[164:167], v[152:155]\n"
"v_fmac_f32_e32 v144, v254, v160\n"
"v_fmac_f32_e32 v145, v254, v161\n"
"v_mfma_f32_16x16x32_bf16 v[196:199], a[96:99], v[96:99], 0\n"
"v_fmac_f32_e32 v146, v254, v162\n"
"v_fmac_f32_e32 v147, v254, v163\n"
"v_mfma_f32_16x16x32_bf16 v[196:199], a[100:103], v[100:103], v[196:199]\n"
"buffer_load_dwordx4 v[160:163], v0, s[4:7], s98 offen offset:3072 sc1\n"
"v_mfma_f32_16x16x32_bf16 v[196:199], a[104:107], v[112:115], v[196:199]\n"
"ds_read_b128 v[200:203], v4 offset:2048\n"
"ds_read_b128 v[204:207], v9 offset:768\n"
"v_mfma_f32_16x16x32_bf16 v[196:199], a[108:111], v[120:123], v[196:199]\n"
"ds_read_b128 v[208:211], v5 offset:2048\n"
"v_add_f32_e32 v144, v144, v176\n"
"v_mfma_f32_16x16x32_bf16 v[196:199], a[112:115], v[104:107], v[196:199]\n"
"v_add_f32_e32 v145, v145, v177\n"
"v_add_f32_e32 v146, v146, v178\n"
"v_mfma_f32_16x16x32_bf16 v[196:199], a[116:119], v[108:111], v[196:199]\n"
"ds_write_b128 v3, v[152:155] offset:14336\n"
"v_mfma_f32_16x16x32_bf16 v[196:199], a[120:123], v[140:143], v[196:199]\n"
"buffer_load_dwordx4 v[152:155], v1, s[4:7], s98 offen sc1\n"
"v_mfma_f32_16x16x32_bf16 v[196:199], a[124:127], v[164:167], v[196:199]\n"
"s_waitcnt lgkmcnt(4)\n"
"v_add_f32_e32 v147, v147, v179\n"
"v_fmac_f32_e32 v180, v254, v184\n"
"v_mfma_f32_16x16x32_bf16 v[176:179], v[24:27], v[96:99], 0\n"
"v_fmac_f32_e32 v181, v254, v185\n"
"v_fmac_f32_e32 v182, v254, v186\n"
"v_mfma_f32_16x16x32_bf16 v[176:179], v[28:31], v[100:103], v[176:179]\n"
"buffer_load_dwordx4 a[172:175], v0, s[4:7], s97 offen offset:3072 sc1\n"
"v_mfma_f32_16x16x32_bf16 v[176:179], v[32:35], v[112:115], v[176:179]\n"
"ds_read_b128 v[212:215], v4 offset:3072\n"
"ds_read_b128 v[216:219], v9 offset:832\n"
"v_mfma_f32_16x16x32_bf16 v[176:179], v[36:39], v[120:123], v[176:179]\n"
"buffer_load_dwordx4 v[220:223], v1, s[4:7], s98 offen offset:1024 sc1\n"
"v_mfma_f32_16x16x32_bf16 v[176:179], v[40:43], v[104:107], v[176:179]\n"
"ds_read_b128 v[224:227], v5 offset:3072\n"
"v_fmac_f32_e32 v183, v254, v187\n"
"v_mfma_f32_16x16x32_bf16 v[176:179], v[44:47], v[108:111], v[176:179]\n"
"ds_write_b128 v3, v[196:199] offset:15360\n"
"v_mfma_f32_16x16x32_bf16 v[176:179], v[48:51], v[140:143], v[176:179]\n"
"v_add_f32_e32 v180, v180, v116\n"
"v_add_f32_e32 v181, v181, v117\n"
"v_mfma_f32_16x16x32_bf16 v[176:179], v[52:55], v[164:167], v[176:179]\n"
"s_waitcnt lgkmcnt(5)\n"
"v_add_f32_e32 v182, v182, v118\n"
"v_add_f32_e32 v183, v183, v119\n"
"v_mfma_f32_16x16x32_bf16 v[116:119], v[56:59], v[96:99], 0\n"
"buffer_load_dwordx4 v[96:99], v1, s[4:7], s98 offen offset:2048 sc1\n"
"v_mfma_f32_16x16x32_bf16 v[116:119], v[60:63], v[100:103], v[116:119]\n"
"ds_read_b128 v[100:103], v4 offset:4096\n"
"ds_read_b128 v[184:187], v9 offset:512\n"
"v_mfma_f32_16x16x32_bf16 v[116:119], v[64:67], v[112:115], v[116:119]\n"
"ds_read_b128 v[112:115], v5 offset:4096\n"
"v_fmac_f32_e32 v200, v254, v204\n"
"v_mfma_f32_16x16x32_bf16 v[116:119], v[68:71], v[120:123], v[116:119]\n"
"v_fmac_f32_e32 v201, v254, v205\n"
"v_fmac_f32_e32 v202, v254, v206\n"
"v_mfma_f32_16x16x32_bf16 v[116:119], v[72:75], v[104:107], v[116:119]\n"
"v_fmac_f32_e32 v203, v254, v207\n"
"v_add_f32_e32 v200, v200, v208\n"
"v_mfma_f32_16x16x32_bf16 v[116:119], v[76:79], v[108:111], v[116:119]\n"
"buffer_load_dwordx4 v[104:107], v1, s[4:7], s98 offen offset:3072 sc1\n"
"v_mfma_f32_16x16x32_bf16 v[116:119], v[80:83], v[140:143], v[116:119]\n"
"ds_write_b128 v3, v[176:179] offset:16384\n"
"v_mfma_f32_16x16x32_bf16 v[116:119], v[84:87], v[164:167], v[116:119]\n"
"s_waitcnt vmcnt(17)\n"
"ds_read_b128 v[108:111], v4 offset:5120\n"
"ds_read_b128 v[120:123], v9 offset:576\n"
"; @B G1 1.3\n"
"v_mfma_f32_16x16x32_bf16 v[140:143], a[0:3], v[92:95], v[144:147]\n"
"ds_read_b128 v[164:167], v5 offset:5120\n"
"v_add_f32_e32 v201, v201, v209\n"
"v_mfma_f32_16x16x32_bf16 v[140:143], a[4:7], v[124:127], v[140:143]\n"
"s_waitcnt lgkmcnt(8)\n"
"v_add_f32_e32 v202, v202, v210\n"
"v_add_f32_e32 v203, v203, v211\n"
"v_mfma_f32_16x16x32_bf16 v[140:143], a[8:11], v[136:139], v[140:143]\n"
"s_add_u32 s98, s53, 0x408000\n"
"buffer_load_dwordx4 v[176:179], v0, s[4:7], s98 offen sc1\n"
"v_mfma_f32_16x16x32_bf16 v[140:143], a[12:15], v[148:151], v[140:143]\n"
"s_waitcnt vmcnt(12)\n"
"s_add_u32 s96, s61, 0x840000\n"
"v_fmac_f32_e32 v212, v254, v216\n"
"v_mfma_f32_16x16x32_bf16 v[140:143], a[16:19], v[128:131], v[140:143]\n"
"v_fmac_f32_e32 v213, v254, v217\n"
"v_fmac_f32_e32 v214, v254, v218\n"
"v_mfma_f32_16x16x32_bf16 v[140:143], a[20:23], v[168:171], v[140:143]\n"
"ds_write_b128 v3, v[116:119] offset:17408\n"
"v_mfma_f32_16x16x32_bf16 v[140:143], a[24:27], v[172:175], v[140:143]\n"
"buffer_store_dwordx4 v[156:159], v0, s[8:11], s96 offen sc1\n"
"v_mfma_f32_16x16x32_bf16 v[140:143], a[28:31], v[188:191], v[140:143]\n"
"buffer_load_dwordx4 v[116:119], v0, s[4:7], s98 offen offset:1024 sc1\n"
"v_mfma_f32_16x16x32_bf16 v[196:199], a[32:35], v[92:95], v[180:183]\n"
"v_fmac_f32_e32 v215, v254, v219\n"
"v_add_f32_e32 v212, v212, v224\n"
"v_mfma_f32_16x16x32_bf16 v[196:199], a[36:39], v[124:127], v[196:199]\n"
"buffer_load_dwordx4 a[176:179], v1, s[4:7], s97 offen sc1\n"
"v_mfma_f32_16x16x32_bf16 v[196:199], a[40:43], v[136:139], v[196:199]\n"
"s_waitcnt lgkmcnt(0)\n"
"s_barrier\n"
"v_mfma_f32_16x16x32_bf16 v[196:199], a[44:47], v[148:151], v[196:199]\n"
"ds_read_b128 v[204:207], v6\n"
"ds_read_b128 v[208:211], v6 offset:1024\n"
"v_mfma_f32_16x16x32_bf16 v[196:199], a[48:51], v[128:131], v[196:199]\n"
"v_cmp_u_f32_e32 vcc, v140, v140\n"
"v_add_f32_e32 v213, v213, v225\n"
"v_mfma_f32_16x16x32_bf16 v[196:199], a[52:55], v[168:171], v[196:199]\n"
"s_cbranch_vccnz .Lmk_p56s\n"
".Lmk_p56r:\n"
"v_add_f32_e32 v214, v214, v226\n"
"v_mfma_f32_16x16x32_bf16 v[196:199], a[56:59], v[172:175], v[196:199]\n"
"buffer_load_dwordx4 v[144:147], v0, s[4:7], s98 offen offset:2048 sc1\n"
"v_mfma_f32_16x16x32_bf16 v[196:199], a[60:63], v[188:191], v[196:199]\n"
"v_add_f32_e32 v215, v215, v227\n"
"v_fmac_f32_e32 v100, v254, v184\n"
"v_mfma_f32_16x16x32_bf16 v[200:203], a[64:67], v[92:95], v[200:203]\n"
"v_fmac_f32_e32 v101, v254, v185\n"
"v_fmac_f32_e32 v102, v254, v186\n"
"v_mfma_f32_16x16x32_bf16 v[200:203], a[68:71], v[124:127], v[200:203]\n"
"buffer_load_dwordx4 v[180:183], v0, s[4:7], s98 offen offset:3072 sc1\n"
"v_mfma_f32_16x16x32_bf16 v[200:203], a[72:75], v[136:139], v[200:203]\n"
"v_fmac_f32_e32 v103, v254, v187\n"
"v_add_f32_e32 v100, v100, v112\n"
"v_mfma_f32_16x16x32_bf16 v[200:203], a[76:79], v[148:151], v[200:203]\n"
"v_add_f32_e32 v101, v101, v113\n"
"v_add_f32_e32 v102, v102, v114\n"
"v_mfma_f32_16x16x32_bf16 v[200:203], a[80:83], v[128:131], v[200:203]\n"
"v_add_f32_e32 v103, v103, v115\n"
"v_fmac_f32_e32 v108, v254, v120\n"
"v_mfma_f32_16x16x32_bf16 v[200:203], a[84:87], v[168:171], v[200:203]\n"
"s_waitcnt lgkmcnt(0)\n"
"v_fmac_f32_e32 v109, v254, v121\n"
"v_fmac_f32_e32 v110, v254, v122\n"
"v_mfma_f32_16x16x32_bf16 v[200:203], a[88:91], v[172:175], v[200:203]\n"
"buffer_load_dwordx4 v[112:115], v1, s[4:7], s98 offen sc1\n"
"v_mfma_f32_16x16x32_bf16 v[200:203], a[92:95], v[188:191], v[200:203]\n"
"ds_read_b128 v[184:187], v6 offset:2048\n"
"ds_read_b128 v[216:219], v6 offset:3072\n"
"v_mfma_f32_16x16x32_bf16 v[212:215], a[96:99], v[92:95], v[212:215]\n"
"v_fmac_f32_e32 v111, v254, v123\n"
"v_add_f32_e32 v108, v108, v164\n"
"v_mfma_f32_16x16x32_bf16 v[212:215], a[100:103], v[124:127], v[212:215]\n"
"v_add_f32_e32 v109, v109, v165\n"
"v_add_f32_e32 v110, v110, v166\n"
"v_mfma_f32_16x16x32_bf16 v[212:215], a[104:107], v[136:139], v[212:215]\n"
"v_add_f32_e32 v111, v111, v167\n"
"v_add_f32_e32 v140, v140, v204\n"
"v_mfma_f32_16x16x32_bf16 v[212:215], a[108:111], v[148:151], v[212:215]\n"
"buffer_load_dwordx4 v[120:123], v1, s[4:7], s98 offen offset:1024 sc1\n"
"v_mfma_f32_16x16x32_bf16 v[212:215], a[112:115], v[128:131], v[212:215]\n"
"v_add_f32_e32 v141, v141, v205\n"
"v_add_f32_e32 v142, v142, v206\n"
"v_mfma_f32_16x16x32_bf16 v[212:215], a[116:119], v[168:171], v[212:215]\n"
"v_add_f32_e32 v143, v143, v207\n"
"v_add_f32_e32 v196, v196, v208\n"
"v_mfma_f32_16x16x32_bf16 v[212:215], a[120:123], v[172:175], v[212:215]\n"
"v_add_f32_e32 v197, v197, v209\n"
"v_add_f32_e32 v198, v198, v210\n"
"v_mfma_f32_16x16x32_bf16 v[212:215], a[124:127], v[188:191], v[212:215]\n"
"v_add_f32_e32 v199, v199, v211\n"
"v_mul_f32_e32 v140, v140, v255\n"
"v_mfma_f32_16x16x32_bf16 v[100:103], v[24:27], v[92:95], v[100:103]\n"
"buffer_load_dwordx4 v[164:167], v1, s[4:7], s98 offen offset:2048 sc1\n"
"v_mfma_f32_16x16x32_bf16 v[100:103], v[28:31], v[124:127], v[100:103]\n"
"buffer_load_dwordx4 a[180:183], v1, s[4:7], s97 offen offset:1024 sc1\n"
"v_mfma_f32_16x16x32_bf16 v[100:103], v[32:35], v[136:139], v[100:103]\n"
"v_mul_f32_e32 v141, v141, v255\n"
"v_mul_f32_e32 v142, v142, v255\n"
"v_mfma_f32_16x16x32_bf16 v[100:103], v[36:39], v[148:151], v[100:103]\n"
"v_mul_f32_e32 v143, v143, v255\n"
"v_mul_f32_e32 v196, v196, v255\n"
"v_mfma_f32_16x16x32_bf16 v[100:103], v[40:43], v[128:131], v[100:103]\n"
"s_waitcnt lgkmcnt(0)\n"
"v_mul_f32_e32 v197, v197, v255\n"
"v_mul_f32_e32 v198, v198, v255\n"
"v_mfma_f32_16x16x32_bf16 v[100:103], v[44:47], v[168:171], v[100:103]\n"
"buffer_load_dwordx4 v[204:207], v1, s[4:7], s98 offen offset:3072 sc1\n"
"v_mfma_f32_16x16x32_bf16 v[100:103], v[48:51], v[172:175], v[100:103]\n"
"ds_read_b128 v[208:211], v6 offset:4096\n"
"ds_read_b128 v[224:227], v6 offset:5120\n"
"v_mfma_f32_16x16x32_bf16 v[100:103], v[52:55], v[188:191], v[100:103]\n"
"v_mul_f32_e32 v199, v199, v255\n"
"v_cvt_pk_bf16_f32 v228, v140, v141\n"
"v_mfma_f32_16x16x32_bf16 v[108:111], v[56:59], v[92:95], v[108:111]\n"
"v_cvt_pk_bf16_f32 v229, v142, v143\n"
"v_cvt_pk_bf16_f32 v230, v196, v197\n"
"v_mfma_f32_16x16x32_bf16 v[108:111], v[60:63], v[124:127], v[108:111]\n"
"s_add_u32 s98, s61, 0x840000\n"
"v_cvt_pk_bf16_f32 v231, v198, v199\n"
"v_add_f32_e32 v200, v200, v184\n"
"v_mfma_f32_16x16x32_bf16 v[108:111], v[64:67], v[136:139], v[108:111]\n"
"ds_write_b128 v8, v[228:231] offset:25600\n"
"v_mfma_f32_16x16x32_bf16 v[108:111], v[68:71], v[148:151], v[108:111]\n"
"buffer_store_dwordx4 v[156:159], v0, s[8:11], s98 offen offset:1024 sc1\n"
"v_mfma_f32_16x16x32_bf16 v[108:111], v[72:75], v[128:131], v[108:111]\n"
"v_add_f32_e32 v201, v201, v185\n"
"v_add_f32_e32 v202, v202, v186\n"
"v_mfma_f32_16x16x32_bf16 v[108:111], v[76:79], v[168:171], v[108:111]\n"
"v_add_f32_e32 v203, v203, v187\n"
"v_add_f32_e32 v212, v212, v216\n"
"v_mfma_f32_16x16x32_bf16 v[108:111], v[80:83], v[172:175], v[108:111]\n"
"v_add_f32_e32 v213, v213, v217\n"
"v_add_f32_e32 v214, v214, v218\n"
"v_mfma_f32_16x16x32_bf16 v[108:111], v[84:87], v[188:191], v[108:111]\n"
"v_add_f32_e32 v215, v215, v219\n"
"v_mul_f32_e32 v200, v200, v255\n"
"v_mul_f32_e32 v201, v201, v255\n"
"v_mul_f32_e32 v202, v202, v255\n"
"v_mul_f32_e32 v203, v203, v255\n"
"v_mul_f32_e32 v212, v212, v255\n"
"v_mul_f32_e32 v213, v213, v255\n"
"v_mul_f32_e32 v214, v214, v255\n"
"v_mul_f32_e32 v215, v215, v255\n"
"v_cvt_pk_bf16_f32 v92, v200, v201\n"
"v_cvt_pk_bf16_f32 v93, v202, v203\n"
"v_cvt_pk_bf16_f32 v94, v212, v213\n"
"v_cvt_pk_bf16_f32 v95, v214, v215\n"
"ds_write_b128 v7, v[92:95] offset:58368\n"
".Lmk_b2:\n"
"s_waitcnt vmcnt(17)\n"
"; @B G1 2.0\n"
"v_mfma_f32_16x16x32_bf16 v[124:127], a[0:3], v[132:135], 0\n"
"v_mfma_f32_16x16x32_bf16 v[124:127], a[4:7], v[88:91], v[124:127]\n"
"s_add_u32 s98, s62, 0x40800\n"
"v_mfma_f32_16x16x32_bf16 v[124:127], a[8:11], v[192:195], v[124:127]\n"
"s_add_u32 s96, s54, 0x408000\n"
"buffer_load_dwordx4 v[128:131], v0, s[4:7], s96 offen sc1\n"
"v_mfma_f32_16x16x32_bf16 v[124:127], a[12:15], v[160:163], v[124:127]\n"
"s_waitcnt vmcnt(13)\n"
"v_mfma_f32_16x16x32_bf16 v[124:127], a[16:19], v[152:155], v[124:127]\n"
"buffer_load_dword v255, v10, s[4:7], s98 offen sc1\n"
"v_mfma_f32_16x16x32_bf16 v[124:127], a[20:23], v[220:223], v[124:127]\n"
"s_waitcnt lgkmcnt(2)\n"
"v_add_f32_e32 v100, v100, v208\n"
"v_add_f32_e32 v101, v101, v209\n"
"v_mfma_f32_16x16x32_bf16 v[124:127], a[24:27], v[96:99], v[124:127]\n"
"v_add_f32_e32 v102, v102, v210\n"
"v_add_f32_e32 v103, v103, v211\n"
"v_mfma_f32_16x16x32_bf16 v[124:127], a[28:31], v[104:107], v[124:127]\n"
"buffer_load_dwordx4 v[136:139], v0, s[4:7], s96 offen offset:1024 sc1\n"
"v_mfma_f32_16x16x32_bf16 v[140:143], a[32:35], v[132:135], 0\n"
"buffer_load_dwordx4 a[184:187], v1, s[4:7], s97 offen offset:2048 sc1\n"
"v_mfma_f32_16x16x32_bf16 v[140:143], a[36:39], v[88:91], v[140:143]\n"
"buffer_load_dword v254, v10, s[4:7], s98 offen offset:256 sc1\n"
"v_mfma_f32_16x16x32_bf16 v[140:143], a[40:43], v[192:195], v[140:143]\n"
"v_add_f32_e32 v108, v108, v224\n"
"v_add_f32_e32 v109, v109, v225\n"
"v_mfma_f32_16x16x32_bf16 v[140:143], a[44:47], v[160:163], v[140:143]\n"
"v_add_f32_e32 v110, v110, v226\n"
"v_add_f32_e32 v111, v111, v227\n"
"v_mfma_f32_16x16x32_bf16 v[140:143], a[48:51], v[152:155], v[140:143]\n"
"buffer_load_dwordx4 v[148:151], v0, s[4:7], s96 offen offset:2048 sc1\n"
"v_mfma_f32_16x16x32_bf16 v[140:143], a[52:55], v[220:223], v[140:143]\n"
"ds_write_b128 v3, v[124:127]\n"
"v_mfma_f32_16x16x32_bf16 v[140:143], a[56:59], v[96:99], v[140:143]\n"
"buffer_load_dword v252, v10, s[4:7], s98 offen offset:512 sc1\n"
"v_mfma_f32_16x16x32_bf16 v[140:143], a[60:63], v[104:107], v[140:143]\n"
"v_mul_f32_e32 v100, v100, v253\n"
"v_mul_f32_e32 v101, v101, v253\n"
"v_mfma_f32_16x16x32_bf16 v[124:127], a[64:67], v[132:135], 0\n"
"v_mul_f32_e32 v102, v102, v253\n"
"v_mul_f32_e32 v103, v103, v253\n"
"v_mfma_f32_16x16x32_bf16 v[124:127], a[68:71], v[88:91], v[124:127]\n"
"buffer_load_dwordx4 v[156:159], v0, s[4:7], s96 offen offset:3072 sc1\n"
"v_mfma_f32_16x16x32_bf16 v[124:127], a[72:75], v[192:195], v[124:127]\n"
"buffer_load_dword v251, v10, s[4:7], s98 offen offset:768 sc1\n"
"v_mfma_f32_16x16x32_bf16 v[124:127], a[76:79], v[160:163], v[124:127]\n"
"v_mul_f32_e32 v108, v108, v253\n"
"v_mul_f32_e32 v109, v109, v253\n"
"v_mfma_f32_16x16x32_bf16 v[124:127], a[80:83], v[152:155], v[124:127]\n"
"v_mul_f32_e32 v110, v110, v253\n"
"v_mul_f32_e32 v111, v111, v253\n"
"v_mfma_f32_16x16x32_bf16 v[124:127], a[84:87], v[220:223], v[124:127]\n"
"ds_write_b128 v3, v[140:143] offset:1024\n"
"v_mfma_f32_16x16x32_bf16 v[124:127], a[88:91], v[96:99], v[124:127]\n"
"buffer_load_dwordx4 v[140:143], v1, s[4:7], s96 offen sc1\n"
"v_mfma_f32_16x16x32_bf16 v[124:127], a[92:95], v[104:107], v[124:127]\n"
"buffer_load_dword v253, v10, s[4:7], s98 offen offset:1024 sc1\n"
"v_mfma_f32_16x16x32_bf16 v[168:171], a[96:99], v[132:135], 0\n"
"v_cvt_pk_bf16_f32 v172, v100, v101\n"
"v_cvt_pk_bf16_f32 v173, v102, v103\n"
"v_mfma_f32_16x16x32_bf16 v[168:171], a[100:103], v[88:91], v[168:171]\n"
"v_cvt_pk_bf16_f32 v174, v108, v109\n"
"v_cvt_pk_bf16_f32 v175, v110, v111\n"
"v_mfma_f32_16x16x32_bf16 v[168:171], a[104:107], v[192:195], v[168:171]\n"
"buffer_load_dword v250, v10, s[4:7], s98 offen offset:1280 sc1\n"
"v_mfma_f32_16x16x32_bf16 v[168:171], a[108:111], v[160:163], v[168:171]\n"
"buffer_load_dwordx4 v[100:103], v1, s[4:7], s96 offen offset:1024 sc1\n"
"v_mfma_f32_16x16x32_bf16 v[168:171], a[112:115], v[152:155], v[168:171]\n"
"ds_write_b128 v2, v[172:175] offset:50176\n"
"v_mfma_f32_16x16x32_bf16 v[168:171], a[116:119], v[220:223], v[168:171]\n"
"ds_write_b128 v3, v[124:127] offset:2048\n"
"s_add_u32 s95, s64, 0x802000\n"
"v_mfma_f32_16x16x32_bf16 v[168:171], a[120:123], v[96:99], v[168:171]\n"
"buffer_load_dword v249, v10, s[4:7], s98 offen offset:1536 sc1\n"
"v_mfma_f32_16x16x32_bf16 v[168:171], a[124:127], v[104:107], v[168:171]\n"
"buffer_store_dwordx4 v[228:231], v8, s[12:15], s95 offen offset:1024 sc1\n"
"v_mfma_f32_16x16x32_bf16 v[108:111], v[24:27], v[132:135], 0\n"
"buffer_load_dwordx4 v[124:127], v1, s[4:7], s96 offen offset:2048 sc1\n"
"v_mfma_f32_16x16x32_bf16 v[108:111], v[28:31], v[88:91], v[108:111]\n"
"buffer_load_dwordx4 a[188:191], v1, s[4:7], s97 offen offset:3072 sc1\n"
"v_mfma_f32_16x16x32_bf16 v[108:111], v[32:35], v[192:195], v[108:111]\n"
"buffer_load_dword v248, v10, s[4:7], s98 offen offset:1792 sc1\n"
"v_mfma_f32_16x16x32_bf16 v[108:111], v[36:39], v[160:163], v[108:111]\n"
"v_add_u32_e32 v247, 0x26c00, v0\n"
"v_mfma_f32_16x16x32_bf16 v[108:111], v[40:43], v[152:155], v[108:111]\n"
"ds_read_b128 v[172:175], v247\n"
"ds_read_b128 v[184:187], v247 offset:1024\n"
"v_xor_b32_e32 v247, 32, v15\n"
"v_mfma_f32_16x16x32_bf16 v[108:111], v[44:47], v[220:223], v[108:111]\n"
"buffer_load_dwordx4 v[188:191], v1, s[4:7], s96 offen offset:3072 sc1\n"
"v_mfma_f32_16x16x32_bf16 v[108:111], v[48:51], v[96:99], v[108:111]\n"
"ds_write_b128 v3, v[168:171] offset:3072\n"
"v_mfma_f32_16x16x32_bf16 v[108:111], v[52:55], v[104:107], v[108:111]\n"
"ds_read_b128 v[168:171], v2 offset:50176\n"
"ds_read_b128 v[196:199], v15 offset:24576\n"
"v_mfma_f32_16x16x32_bf16 v[200:203], v[56:59], v[132:135], 0\n"
"ds_read_b128 v[132:135], v247 offset:24832\n"
"v_mfma_f32_16x16x32_bf16 v[200:203], v[60:63], v[88:91], v[200:203]\n"
"v_mfma_f32_16x16x32_bf16 v[200:203], v[64:67], v[192:195], v[200:203]\n"
"v_mfma_f32_16x16x32_bf16 v[200:203], v[68:71], v[160:163], v[200:203]\n"
"v_mfma_f32_16x16x32_bf16 v[200:203], v[72:75], v[152:155], v[200:203]\n"
"v_mfma_f32_16x16x32_bf16 v[200:203], v[76:79], v[220:223], v[200:203]\n"
"ds_write_b128 v3, v[108:111] offset:4096\n"
"v_mfma_f32_16x16x32_bf16 v[200:203], v[80:83], v[96:99], v[200:203]\n"
"s_waitcnt lgkmcnt(1)\n"
"v_mfma_f32_16x16x32_bf16 v[172:175], v[196:199], v[168:171], v[172:175]\n"
"v_mfma_f32_16x16x32_bf16 v[184:187], v[132:135], v[168:171], v[184:187]\n"
"ds_read_b64_tr_b16 v[88:89], v16 offset:24576\n"
"v_xor_b32_e32 v247, 32, v16\n"
"v_mfma_f32_16x16x32_bf16 v[200:203], v[84:87], v[104:107], v[200:203]\n"
"s_waitcnt vmcnt(25)\n"
"ds_read_b64_tr_b16 v[90:91], v247 offset:24832\n"
"ds_read_b64_tr_b16 v[96:97], v247 offset:24576\n"
"; @B G1 2.1\n"
"v_mfma_f32_16x16x32_bf16 v[104:107], a[0:3], v[176:179], 0\n"
"ds_read_b64_tr_b16 v[98:99], v16 offset:24832\n"
"v_mov_b32_e32 v108, 0x3f803f80\n"
"v_mfma_f32_16x16x32_bf16 v[104:107], a[4:7], v[116:119], v[104:107]\n"
"v_mov_b32_e32 v109, 0x3f803f80\n"
"v_mov_b32_e32 v110, 0x3f803f80\n"
"v_mfma_f32_16x16x32_bf16 v[104:107], a[8:11], v[144:147], v[104:107]\n"
"s_add_u32 s98, s55, 0x408000\n"
"buffer_load_dwordx4 v[132:135], v0, s[4:7], s98 offen sc1\n"
"v_mfma_f32_16x16x32_bf16 v[104:107], a[12:15], v[180:183], v[104:107]\n"
"s_waitcnt vmcnt(21)\n"
"v_mov_b32_e32 v111, 0x3f803f80\n"
"v_mfma_f32_16x16x32_bf16 v[104:107], a[16:19], v[112:115], v[104:107]\n"
"v_max_f32_e32 v247, v172, v173\n"
"v_max3_f32 v247, v247, v174, v175\n"
"v_mfma_f32_16x16x32_bf16 v[104:107], a[20:23], v[120:123], v[104:107]\n"
"ds_write_b128 v3, v[200:203] offset:5120\n"
"s_add_u32 s97, s64, 0x806000\n"
"v_mfma_f32_16x16x32_bf16 v[104:107], a[24:27], v[164:167], v[104:107]\n"
"v_max3_f32 v247, v247, v184, v185\n"
"v_max3_f32 v247, v247, v186, v187\n"
"v_mfma_f32_16x16x32_bf16 v[104:107], a[28:31], v[204:207], v[104:107]\n"
"buffer_load_dwordx4 v[152:155], v0, s[4:7], s98 offen offset:1024 sc1\n"
"v_mfma_f32_16x16x32_bf16 v[160:163], a[32:35], v[176:179], 0\n"
"s_add_u32 s96, s70, 0x204000\n"
"buffer_store_dwordx4 v[92:95], v7, s[12:15], s97 offen offset:1024 sc1\n"
"v_mfma_f32_16x16x32_bf16 v[160:163], a[36:39], v[116:119], v[160:163]\n"
"buffer_load_dwordx4 a[192:195], v0, s[4:7], s96 offen sc1\n"
"v_mfma_f32_16x16x32_bf16 v[160:163], a[40:43], v[144:147], v[160:163]\n"
"v_mov_b32_e32 v246, v247\n"
"v_mfma_f32_16x16x32_bf16 v[160:163], a[44:47], v[180:183], v[160:163]\n"
"s_nop 0\n"
"v_permlane16_swap_b32_e32 v247, v246\n"
"v_max_f32_e32 v247, v247, v246\n"
"v_mfma_f32_16x16x32_bf16 v[160:163], a[48:51], v[112:115], v[160:163]\n"
"buffer_load_dwordx4 v[92:95], v0, s[4:7], s98 offen offset:2048 sc1\n"
"v_mfma_f32_16x16x32_bf16 v[160:163], a[52:55], v[120:123], v[160:163]\n"
"ds_write_b128 v3, v[104:107] offset:6144\n"
"v_mfma_f32_16x16x32_bf16 v[160:163], a[56:59], v[164:167], v[160:163]\n"
"v_mov_b32_e32 v246, v247\n"
"v_mfma_f32_16x16x32_bf16 v[160:163], a[60:63], v[204:207], v[160:163]\n"
"s_nop 0\n"
"v_permlane32_swap_b32_e32 v247, v246\n"
"v_max_f32_e32 v247, v247, v246\n"
"v_mfma_f32_16x16x32_bf16 v[104:107], a[64:67], v[176:179], 0\n"
"v_sub_f32_e32 v172, v172, v247\n"
"v_sub_f32_e32 v173, v173, v247\n"
"v_mfma_f32_16x16x32_bf16 v[104:107], a[68:71], v[116:119], v[104:107]\n"
"buffer_load_dwordx4 v[168:171], v0, s[4:7], s98 offen offset:3072 sc1\n"
"v_mfma_f32_16x16x32_bf16 v[104:107], a[72:75], v[144:147], v[104:107]\n"
"v_sub_f32_e32 v174, v174, v247\n"
"v_sub_f32_e32 v175, v175, v247\n"
"v_mfma_f32_16x16x32_bf16 v[104:107], a[76:79], v[180:183], v[104:107]\n"
"v_sub_f32_e32 v184, v184, v247\n"
"v_sub_f32_e32 v185, v185, v247\n"
"v_mfma_f32_16x16x32_bf16 v[104:107], a[80:83], v[112:115], v[104:107]\n"
"v_mov_b32_e32 v192, -1\n"
"v_mov_b32_e32 v193, -1\n"
"v_mfma_f32_16x16x32_bf16 v[104:107], a[84:87], v[120:123], v[104:107]\n"
"ds_write_b128 v3, v[160:163] offset:7168\n"
"v_mfma_f32_16x16x32_bf16 v[104:107], a[88:91], v[164:167], v[104:107]\n"
"buffer_load_dwordx4 v[160:163], v1, s[4:7], s98 offen sc1\n"
"v_mfma_f32_16x16x32_bf16 v[104:107], a[92:95], v[204:207], v[104:107]\n"
"v_mov_b32_e32 v194, -1\n"
"v_mov_b32_e32 v195, -1\n"
"v_mfma_f32_16x16x32_bf16 v[196:199], a[96:99], v[176:179], 0\n"
"s_add_u32 s97, s63, 0x84000\n"
"buffer_store_dword v192, v14, s[8:11], s97 offen sc1\n"
"v_mfma_f32_16x16x32_bf16 v[196:199], a[100:103], v[116:119], v[196:199]\n"
"v_sub_f32_e32 v186, v186, v247\n"
"v_sub_f32_e32 v187, v187, v247\n"
"v_mfma_f32_16x16x32_bf16 v[196:199], a[104:107], v[144:147], v[196:199]\n"
"v_exp_f32_e32 v172, v172\n"
"v_mfma_f32_16x16x32_bf16 v[196:199], a[108:111], v[180:183], v[196:199]\n"
"buffer_load_dwordx4 v[200:203], v1, s[4:7], s98 offen offset:1024 sc1\n"
"v_mfma_f32_16x16x32_bf16 v[196:199], a[112:115], v[112:115], v[196:199]\n"
"v_exp_f32_e32 v173, v173\n"
"v_mfma_f32_16x16x32_bf16 v[196:199], a[116:119], v[120:123], v[196:199]\n"
"ds_write_b128 v3, v[104:107] offset:8192\n"
"v_mfma_f32_16x16x32_bf16 v[196:199], a[120:123], v[164:167], v[196:199]\n"
"v_exp_f32_e32 v174, v174\n"
"v_mfma_f32_16x16x32_bf16 v[196:199], a[124:127], v[204:207], v[196:199]\n"
"v_exp_f32_e32 v175, v175\n"
"v_mfma_f32_16x16x32_bf16 v[104:107], v[24:27], v[176:179], 0\n"
"buffer_load_dwordx4 v[208:211], v1, s[4:7], s98 offen offset:2048 sc1\n"
"v_mfma_f32_16x16x32_bf16 v[104:107], v[28:31], v[116:119], v[104:107]\n"
"buffer_load_dwordx4 a[196:199], v0, s[4:7], s96 offen offset:1024 sc1\n"
"v_mfma_f32_16x16x32_bf16 v[104:107], v[32:35], v[144:147], v[104:107]\n"
"v_exp_f32_e32 v184, v184\n"
"v_mfma_f32_16x16x32_bf16 v[104:107], v[36:39], v[180:183], v[104:107]\n"
"v_exp_f32_e32 v185, v185\n"
"v_mfma_f32_16x16x32_bf16 v[104:107], v[40:43], v[112:115], v[104:107]\n"
"s_waitcnt vmcnt(12)\n"
"v_add_f32_e32 v255, v255, v253\n"
"v_add_f32_e32 v254, v254, v250\n"
"v_mfma_f32_16x16x32_bf16 v[104:107], v[44:47], v[120:123], v[104:107]\n"
"buffer_load_dwordx4 v[212:215], v1, s[4:7], s98 offen offset:3072 sc1\n"
"v_mfma_f32_16x16x32_bf16 v[104:107], v[48:51], v[164:167], v[104:107]\n"
"ds_write_b128 v3, v[196:199] offset:9216\n"
"v_mfma_f32_16x16x32_bf16 v[104:107], v[52:55], v[204:207], v[104:107]\n"
"v_add_f32_e32 v252, v252, v249\n"
"v_add_f32_e32 v251, v251, v248\n"
"v_mfma_f32_16x16x32_bf16 v[196:199], v[56:59], v[176:179], 0\n"
"v_add_f32_e32 v255, v255, v252\n"
"v_add_f32_e32 v254, v254, v251\n"
"v_mfma_f32_16x16x32_bf16 v[196:199], v[60:63], v[116:119], v[196:199]\n"
"v_add_f32_e32 v255, v255, v254\n"
"v_mov_b32_e32 v254, v255\n"
"v_mfma_f32_16x16x32_bf16 v[196:199], v[64:67], v[144:147], v[196:199]\n"
"s_nop 0\n"
"v_permlane32_swap_b32_e32 v255, v254\n"
"v_add_f32_e32 v255, v255, v254\n"
"v_mfma_f32_16x16x32_bf16 v[196:199], v[68:71], v[180:183], v[196:199]\n"
"v_mul_f32_e32 v255, 0x3a800000, v255\n"
"v_mov_b32_e32 v254, v255\n"
"v_mfma_f32_16x16x32_bf16 v[196:199], v[72:75], v[112:115], v[196:199]\n"
"s_nop 0\n"
"v_permlane16_swap_b32_e32 v255, v254\n"
"v_cvt_pk_bf16_f32 v253, -v255, 0\n"
"v_mfma_f32_16x16x32_bf16 v[196:199], v[76:79], v[120:123], v[196:199]\n"
"ds_write_b128 v3, v[104:107] offset:10240\n"
"v_mfma_f32_16x16x32_bf16 v[196:199], v[80:83], v[164:167], v[196:199]\n"
"v_fma_f32 v254, -v255, v255, v254\n"
"v_lshlrev_b32_e32 v253, 16, v253\n"
"v_mfma_f32_16x16x32_bf16 v[196:199], v[84:87], v[204:207], v[196:199]\n"
"v_max_f32_e32 v254, 0, v254\n"
"v_cvt_pk_bf16_f32 v254, v254, 0\n"
"; @B G1 2.2\n"
"v_mfma_f32_16x16x32_bf16 v[104:107], a[0:3], v[128:131], 0\n"
"v_lshlrev_b32_e32 v254, 16, v254\n"
"v_add_f32_e32 v254, 0x3727c5ac, v254\n"
"v_mfma_f32_16x16x32_bf16 v[104:107], a[4:7], v[136:139], v[104:107]\n"
"v_cvt_pk_bf16_f32 v254, v254, 0\n"
"v_lshlrev_b32_e32 v254, 16, v254\n"
"v_mfma_f32_16x16x32_bf16 v[104:107], a[8:11], v[148:151], v[104:107]\n"
"v_sqrt_f32_e32 v254, v254\n"
"v_mfma_f32_16x16x32_bf16 v[104:107], a[12:15], v[156:159], v[104:107]\n"
"s_waitcnt vmcnt(12)\n"
"v_cvt_pk_bf16_f32 v254, v254, 0\n"
"v_lshlrev_b32_e32 v254, 16, v254\n"
"v_mfma_f32_16x16x32_bf16 v[104:107], a[16:19], v[140:143], v[104:107]\n"
"v_rcp_f32_e32 v255, v254\n"
"v_mfma_f32_16x16x32_bf16 v[104:107], a[20:23], v[100:103], v[104:107]\n"
"ds_write_b128 v3, v[196:199] offset:11264\n"
"v_mfma_f32_16x16x32_bf16 v[104:107], a[24:27], v[124:127], v[104:107]\n"
"v_mul_f32_e32 v254, 0x3e8293ee, v255\n"
"v_mfma_f32_16x16x32_bf16 v[104:107], a[28:31], v[188:191], v[104:107]\n"
"v_exp_f32_e32 v186, v186\n"
"v_mfma_f32_16x16x32_bf16 v[112:115], a[32:35], v[128:131], 0\n"
"v_exp_f32_e32 v187, v187\n"
"v_mfma_f32_16x16x32_bf16 v[112:115], a[36:39], v[136:139], v[112:115]\n"
"buffer_load_dwordx4 a[200:203], v0, s[4:7], s96 offen offset:2048 sc1\n"
"v_mfma_f32_16x16x32_bf16 v[112:115], a[40:43], v[148:151], v[112:115]\n"
"s_add_u32 s98, s52, 0x400000\n"
"buffer_load_dwordx4 v[116:119], v0, s[4:7], s98 offen sc1\n"
"v_mfma_f32_16x16x32_bf16 v[112:115], a[44:47], v[156:159], v[112:115]\n"
"s_waitcnt lgkmcnt(0)\n"
"s_barrier\n"
"v_mfma_f32_16x16x32_bf16 v[112:115], a[48:51], v[140:143], v[112:115]\n"
"ds_read_b128 v[120:123], v4\n"
"ds_read_b128 v[144:147], v9 offset:640\n"
"v_mfma_f32_16x16x32_bf16 v[112:115], a[52:55], v[100:103], v[112:115]\n"
"ds_write_b128 v3, v[104:107] offset:12288\n"
"v_mfma_f32_16x16x32_bf16 v[112:115], a[56:59], v[124:127], v[112:115]\n"
"ds_read_b128 v[104:107], v5\n"
"v_cvt_pk_bf16_f32 v164, v172, v173\n"
"v_mfma_f32_16x16x32_bf16 v[112:115], a[60:63], v[188:191], v[112:115]\n"
"buffer_load_dwordx4 v[176:179], v0, s[4:7], s98 offen offset:1024 sc1\n"
"v_mfma_f32_16x16x32_bf16 v[180:183], a[64:67], v[128:131], 0\n"
"v_cvt_pk_bf16_f32 v165, v174, v175\n"
"v_cvt_pk_bf16_f32 v166, v184, v185\n"
"v_mfma_f32_16x16x32_bf16 v[180:183], a[68:71], v[136:139], v[180:183]\n"
"v_cvt_pk_bf16_f32 v167, v186, v187\n"
"v_mfma_f32_16x16x32_bf16 v[180:183], a[72:75], v[148:151], v[180:183]\n"
"s_nop 0\n"
"v_mfma_f32_16x16x32_bf16 v[172:175], v[108:111], v[164:167], 0\n"
"v_mfma_f32_16x16x32_bf16 v[108:111], v[88:91], v[164:167], 0\n"
"v_mfma_f32_16x16x32_bf16 v[88:91], v[96:99], v[164:167], 0\n"
"v_mfma_f32_16x16x32_bf16 v[180:183], a[76:79], v[156:159], v[180:183]\n"
"ds_read_b128 v[96:99], v4 offset:1024\n"
"ds_read_b128 v[164:167], v9 offset:704\n"
"v_mfma_f32_16x16x32_bf16 v[180:183], a[80:83], v[140:143], v[180:183]\n"
"buffer_load_dwordx4 v[184:187], v0, s[4:7], s98 offen offset:2048 sc1\n"
"v_mfma_f32_16x16x32_bf16 v[180:183], a[84:87], v[100:103], v[180:183]\n"
"ds_write_b128 v3, v[112:115] offset:13312\n"
"v_mfma_f32_16x16x32_bf16 v[180:183], a[88:91], v[124:127], v[180:183]\n"
"ds_read_b128 v[112:115], v5 offset:1024\n"
"s_waitcnt lgkmcnt(4)\n"
"v_mfma_f32_16x16x32_bf16 v[180:183], a[92:95], v[188:191], v[180:183]\n"
"v_fmac_f32_e32 v120, v253, v144\n"
"v_fmac_f32_e32 v121, v253, v145\n"
"v_mfma_f32_16x16x32_bf16 v[196:199], a[96:99], v[128:131], 0\n"
"v_fmac_f32_e32 v122, v253, v146\n"
"v_fmac_f32_e32 v123, v253, v147\n"
"v_mfma_f32_16x16x32_bf16 v[196:199], a[100:103], v[136:139], v[196:199]\n"
"buffer_load_dwordx4 v[144:147], v0, s[4:7], s98 offen offset:3072 sc1\n"
"v_mfma_f32_16x16x32_bf16 v[196:199], a[104:107], v[148:151], v[196:199]\n"
"ds_read_b128 v[204:207], v4 offset:2048\n"
"ds_read_b128 v[216:219], v9 offset:768\n"
"v_mfma_f32_16x16x32_bf16 v[196:199], a[108:111], v[156:159], v[196:199]\n"
"ds_read_b128 v[220:223], v5 offset:2048\n"
"v_add_f32_e32 v120, v120, v104\n"
"v_mfma_f32_16x16x32_bf16 v[196:199], a[112:115], v[140:143], v[196:199]\n"
"v_add_f32_e32 v121, v121, v105\n"
"v_add_f32_e32 v122, v122, v106\n"
"v_mfma_f32_16x16x32_bf16 v[196:199], a[116:119], v[100:103], v[196:199]\n"
"ds_write_b128 v3, v[180:183] offset:14336\n"
"v_mfma_f32_16x16x32_bf16 v[196:199], a[120:123], v[124:127], v[196:199]\n"
"buffer_load_dwordx4 v[180:183], v1, s[4:7], s98 offen sc1\n"
"v_mfma_f32_16x16x32_bf16 v[196:199], a[124:127], v[188:191], v[196:199]\n"
"s_add_u32 s97, s60, 0x848000\n"
"v_add_f32_e32 v123, v123, v107\n"
"s_waitcnt lgkmcnt(4)\n"
"v_mfma_f32_16x16x32_bf16 v[104:107], v[24:27], v[128:131], 0\n"
"buffer_store_dwordx4 v[192:195], v0, s[8:11], s97 offen sc1\n"
"v_mfma_f32_16x16x32_bf16 v[104:107], v[28:31], v[136:139], v[104:107]\n"
"buffer_load_dwordx4 a[204:207], v0, s[4:7], s96 offen offset:3072 sc1\n"
"v_mfma_f32_16x16x32_bf16 v[104:107], v[32:35], v[148:151], v[104:107]\n"
"s_waitcnt vmcnt(17)\n"
"s_add_u32 s97, s68, 0x11000\n"
"ds_read_b128 v[224:227], v4 offset:3072\n"
"v_mfma_f32_16x16x32_bf16 v[104:107], v[36:39], v[156:159], v[104:107]\n"
"buffer_load_dwordx4 v[228:231], v1, s[4:7], s98 offen offset:1024 sc1\n"
"v_mfma_f32_16x16x32_bf16 v[104:107], v[40:43], v[140:143], v[104:107]\n"
"buffer_store_dwordx2 v[18:19], v11, s[12:15], s97 offen offset:64 sc1\n"
"v_mfma_f32_16x16x32_bf16 v[104:107], v[44:47], v[100:103], v[104:107]\n"
"ds_write_b128 v3, v[196:199] offset:15360\n"
"v_mfma_f32_16x16x32_bf16 v[104:107], v[48:51], v[124:127], v[104:107]\n"
"ds_read_b128 v[196:199], v9 offset:832\n"
"ds_read_b128 v[232:235], v5 offset:3072\n"
"v_mfma_f32_16x16x32_bf16 v[104:107], v[52:55], v[188:191], v[104:107]\n"
"v_fmac_f32_e32 v96, v253, v164\n"
"v_fmac_f32_e32 v97, v253, v165\n"
"v_mfma_f32_16x16x32_bf16 v[236:239], v[56:59], v[128:131], 0\n"
"buffer_load_dwordx4 v[128:131], v1, s[4:7], s98 offen offset:2048 sc1\n"
"v_mfma_f32_16x16x32_bf16 v[236:239], v[60:63], v[136:139], v[236:239]\n"
"ds_read_b128 v[136:139], v4 offset:4096\n"
"ds_read_b128 v[240:243], v9 offset:512\n"
"v_mfma_f32_16x16x32_bf16 v[236:239], v[64:67], v[148:151], v[236:239]\n"
"ds_read_b128 v[148:151], v5 offset:4096\n"
"v_fmac_f32_e32 v98, v253, v166\n"
"v_mfma_f32_16x16x32_bf16 v[236:239], v[68:71], v[156:159], v[236:239]\n"
"v_fmac_f32_e32 v99, v253, v167\n"
"v_add_f32_e32 v96, v96, v112\n"
"v_mfma_f32_16x16x32_bf16 v[236:239], v[72:75], v[140:143], v[236:239]\n"
"v_add_f32_e32 v97, v97, v113\n"
"v_add_f32_e32 v98, v98, v114\n"
"v_mfma_f32_16x16x32_bf16 v[236:239], v[76:79], v[100:103], v[236:239]\n"
"buffer_load_dwordx4 v[100:103], v1, s[4:7], s98 offen offset:3072 sc1\n"
"v_mfma_f32_16x16x32_bf16 v[236:239], v[80:83], v[124:127], v[236:239]\n"
"ds_write_b128 v3, v[104:107] offset:16384\n"
"v_mfma_f32_16x16x32_bf16 v[236:239], v[84:87], v[188:191], v[236:239]\n"
"s_waitcnt vmcnt(18)\n"
"ds_read_b128 v[104:107], v4 offset:5120\n"
"ds_read_b128 v[124:127], v9 offset:576\n"
"; @B G1 2.3\n"
"v_mfma_f32_16x16x32_bf16 v[120:123], a[0:3], v[132:135], v[120:123]\n"
"ds_read_b128 v[140:143], v5 offset:5120\n"
"v_add_f32_e32 v99, v99, v115\n"
"v_mfma_f32_16x16x32_bf16 v[120:123], a[4:7], v[152:155], v[120:123]\n"
"s_waitcnt lgkmcnt(12)\n"
"v_fmac_f32_e32 v204, v253, v216\n"
"v_fmac_f32_e32 v205, v253, v217\n"
"v_mfma_f32_16x16x32_bf16 v[120:123], a[8:11], v[92:95], v[120:123]\n"
"s_add_u32 s98, s53, 0x400000\n"
"buffer_load_dwordx4 v[112:115], v0, s[4:7], s98 offen sc1\n"
"v_mfma_f32_16x16x32_bf16 v[120:123], a[12:15], v[168:171], v[120:123]\n"
"s_waitcnt vmcnt(13)\n"
"v_fmac_f32_e32 v206, v253, v218\n"
"v_fmac_f32_e32 v207, v253, v219\n"
"v_mfma_f32_16x16x32_bf16 v[120:123], a[16:19], v[160:163], v[120:123]\n"
"v_add_f32_e32 v204, v204, v220\n"
"v_add_f32_e32 v205, v205, v221\n"
"v_mfma_f32_16x16x32_bf16 v[120:123], a[20:23], v[200:203], v[120:123]\n"
"ds_write_b128 v3, v[236:239] offset:17408\n"
"v_mfma_f32_16x16x32_bf16 v[120:123], a[24:27], v[208:211], v[120:123]\n"
"s_waitcnt lgkmcnt(8)\n"
"v_add_f32_e32 v206, v206, v222\n"
"v_add_f32_e32 v207, v207, v223\n"
"v_mfma_f32_16x16x32_bf16 v[120:123], a[28:31], v[212:215], v[120:123]\n"
"buffer_load_dwordx4 v[156:159], v0, s[4:7], s98 offen offset:1024 sc1\n"
"v_mfma_f32_16x16x32_bf16 v[96:99], a[32:35], v[132:135], v[96:99]\n"
"v_fmac_f32_e32 v224, v253, v196\n"
"v_fmac_f32_e32 v225, v253, v197\n"
"v_mfma_f32_16x16x32_bf16 v[96:99], a[36:39], v[152:155], v[96:99]\n"
"buffer_load_dwordx4 a[208:211], v1, s[4:7], s96 offen sc1\n"
"v_mfma_f32_16x16x32_bf16 v[96:99], a[40:43], v[92:95], v[96:99]\n"
"s_waitcnt lgkmcnt(0)\n"
"s_barrier\n"
"v_mfma_f32_16x16x32_bf16 v[96:99], a[44:47], v[168:171], v[96:99]\n"
"ds_read_b128 v[164:167], v6\n"
"ds_read_b128 v[188:191], v6 offset:1024\n"
"v_mfma_f32_16x16x32_bf16 v[96:99], a[48:51], v[160:163], v[96:99]\n"
"buffer_load_dwordx4 v[216:219], v0, s[4:7], s98 offen offset:2048 sc1\n"
"v_mfma_f32_16x16x32_bf16 v[96:99], a[52:55], v[200:203], v[96:99]\n"
"v_fmac_f32_e32 v226, v253, v198\n"
"v_fmac_f32_e32 v227, v253, v199\n"
"v_mfma_f32_16x16x32_bf16 v[96:99], a[56:59], v[208:211], v[96:99]\n"
"v_add_f32_e32 v224, v224, v232\n"
"v_add_f32_e32 v225, v225, v233\n"
"v_mfma_f32_16x16x32_bf16 v[96:99], a[60:63], v[212:215], v[96:99]\n"
"v_add_f32_e32 v226, v226, v234\n"
"v_add_f32_e32 v227, v227, v235\n"
"v_mfma_f32_16x16x32_bf16 v[204:207], a[64:67], v[132:135], v[204:207]\n"
"v_fmac_f32_e32 v136, v253, v240\n"
"v_fmac_f32_e32 v137, v253, v241\n"
"v_mfma_f32_16x16x32_bf16 v[204:207], a[68:71], v[152:155], v[204:207]\n"
"buffer_load_dwordx4 v[196:199], v0, s[4:7], s98 offen offset:3072 sc1\n"
"v_mfma_f32_16x16x32_bf16 v[204:207], a[72:75], v[92:95], v[204:207]\n"
"v_fmac_f32_e32 v138, v253, v242\n"
"v_fmac_f32_e32 v139, v253, v243\n"
"v_mfma_f32_16x16x32_bf16 v[204:207], a[76:79], v[168:171], v[204:207]\n"
"v_add_f32_e32 v136, v136, v148\n"
"v_add_f32_e32 v137, v137, v149\n"
"v_mfma_f32_16x16x32_bf16 v[204:207], a[80:83], v[160:163], v[204:207]\n"
"v_add_f32_e32 v138, v138, v150\n"
"v_add_f32_e32 v139, v139, v151\n"
"v_mfma_f32_16x16x32_bf16 v[204:207], a[84:87], v[200:203], v[204:207]\n"
"s_waitcnt lgkmcnt(0)\n"
"v_fmac_f32_e32 v104, v253, v124\n"
"v_fmac_f32_e32 v105, v253, v125\n"
"v_mfma_f32_16x16x32_bf16 v[204:207], a[88:91], v[208:211], v[204:207]\n"
"buffer_load_dwordx4 v[148:151], v1, s[4:7], s98 offen sc1\n"
"v_mfma_f32_16x16x32_bf16 v[204:207], a[92:95], v[212:215], v[204:207]\n"
"ds_read_b128 v[220:223], v6 offset:2048\n"
"ds_read_b128 v[232:235], v6 offset:3072\n"
"v_mfma_f32_16x16x32_bf16 v[224:227], a[96:99], v[132:135], v[224:227]\n"
"v_fmac_f32_e32 v106, v253, v126\n"
"v_fmac_f32_e32 v107, v253, v127\n"
"v_mfma_f32_16x16x32_bf16 v[224:227], a[100:103], v[152:155], v[224:227]\n"
"v_add_f32_e32 v104, v104, v140\n"
"v_add_f32_e32 v105, v105, v141\n"
"v_mfma_f32_16x16x32_bf16 v[224:227], a[104:107], v[92:95], v[224:227]\n"
"v_add_f32_e32 v106, v106, v142\n"
"v_add_f32_e32 v107, v107, v143\n"
"v_mfma_f32_16x16x32_bf16 v[224:227], a[108:111], v[168:171], v[224:227]\n"
"buffer_load_dwordx4 v[124:127], v1, s[4:7], s98 offen offset:1024 sc1\n"
"v_mfma_f32_16x16x32_bf16 v[224:227], a[112:115], v[160:163], v[224:227]\n"
"v_add_f32_e32 v120, v120, v164\n"
"v_add_f32_e32 v121, v121, v165\n"
"v_mfma_f32_16x16x32_bf16 v[224:227], a[116:119], v[200:203], v[224:227]\n"
"v_add_f32_e32 v122, v122, v166\n"
"v_add_f32_e32 v123, v123, v167\n"
"v_mfma_f32_16x16x32_bf16 v[224:227], a[120:123], v[208:211], v[224:227]\n"
"v_add_f32_e32 v96, v96, v188\n"
"v_add_f32_e32 v97, v97, v189\n"
"v_mfma_f32_16x16x32_bf16 v[224:227], a[124:127], v[212:215], v[224:227]\n"
"v_add_f32_e32 v98, v98, v190\n"
"v_add_f32_e32 v99, v99, v191\n"
"v_mfma_f32_16x16x32_bf16 v[136:139], v[24:27], v[132:135], v[136:139]\n"
"buffer_load_dwordx4 v[140:143], v1, s[4:7], s98 offen offset:2048 sc1\n"
"v_mfma_f32_16x16x32_bf16 v[136:139], v[28:31], v[152:155], v[136:139]\n"
"buffer_load_dwordx4 a[212:215], v1, s[4:7], s96 offen offset:1024 sc1\n"
"v_mfma_f32_16x16x32_bf16 v[136:139], v[32:35], v[92:95], v[136:139]\n"
"v_mul_f32_e32 v120, v120, v255\n"
"v_mul_f32_e32 v121, v121, v255\n"
"v_mfma_f32_16x16x32_bf16 v[136:139], v[36:39], v[168:171], v[136:139]\n"
"v_mul_f32_e32 v122, v122, v255\n"
"v_mul_f32_e32 v123, v123, v255\n"
"v_mfma_f32_16x16x32_bf16 v[136:139], v[40:43], v[160:163], v[136:139]\n"
"s_waitcnt lgkmcnt(0)\n"
"v_mul_f32_e32 v96, v96, v255\n"
"v_mul_f32_e32 v97, v97, v255\n"
"v_mfma_f32_16x16x32_bf16 v[136:139], v[44:47], v[200:203], v[136:139]\n"
"buffer_load_dwordx4 v[164:167], v1, s[4:7], s98 offen offset:3072 sc1\n"
"v_mfma_f32_16x16x32_bf16 v[136:139], v[48:51], v[208:211], v[136:139]\n"
"s_add_u32 s98, s60, 0x840000\n"
"ds_read_b128 v[188:191], v6 offset:4096\n"
"ds_read_b128 v[236:239], v6 offset:5120\n"
"v_mfma_f32_16x16x32_bf16 v[136:139], v[52:55], v[212:215], v[136:139]\n"
"v_mul_f32_e32 v98, v98, v255\n"
"v_mul_f32_e32 v99, v99, v255\n"
"v_mfma_f32_16x16x32_bf16 v[104:107], v[56:59], v[132:135], v[104:107]\n"
"buffer_store_dwordx4 v[192:195], v0, s[8:11], s98 offen sc1\n"
"v_mfma_f32_16x16x32_bf16 v[104:107], v[60:63], v[152:155], v[104:107]\n"
"s_add_u32 s98, s68, 0x10000\n"
"v_cvt_pk_bf16_f32 v132, v120, v121\n"
"v_mfma_f32_16x16x32_bf16 v[104:107], v[64:67], v[92:95], v[104:107]\n"
"buffer_store_dwordx2 v[18:19], v11, s[4:7], s98 offen offset:32 sc1\n"
"v_mfma_f32_16x16x32_bf16 v[104:107], v[68:71], v[168:171], v[104:107]\n"
"v_cvt_pk_bf16_f32 v133, v122, v123\n"
"v_cvt_pk_bf16_f32 v134, v96, v97\n"
"v_mfma_f32_16x16x32_bf16 v[104:107], v[72:75], v[160:163], v[104:107]\n"
"v_cvt_pk_bf16_f32 v135, v98, v99\n"
"v_add_f32_e32 v204, v204, v220\n"
"v_mfma_f32_16x16x32_bf16 v[104:107], v[76:79], v[200:203], v[104:107]\n"
"ds_write_b128 v8, v[132:135] offset:17408\n"
"v_mfma_f32_16x16x32_bf16 v[104:107], v[80:83], v[208:211], v[104:107]\n"
"v_add_f32_e32 v205, v205, v221\n"
"v_add_f32_e32 v206, v206, v222\n"
"v_mfma_f32_16x16x32_bf16 v[104:107], v[84:87], v[212:215], v[104:107]\n"
"v_add_f32_e32 v207, v207, v223\n"
"v_add_f32_e32 v224, v224, v232\n"
"v_add_f32_e32 v225, v225, v233\n"
"v_add_f32_e32 v226, v226, v234\n"
"v_add_f32_e32 v227, v227, v235\n"
"v_mul_f32_e32 v204, v204, v255\n"
"v_mul_f32_e32 v205, v205, v255\n"
"v_mul_f32_e32 v206, v206, v255\n"
"v_mul_f32_e32 v207, v207, v255\n"
"v_mul_f32_e32 v224, v224, v255\n"
"v_mul_f32_e32 v225, v225, v255\n"
"v_mul_f32_e32 v226, v226, v255\n"
"v_mul_f32_e32 v227, v227, v255\n"
"v_cvt_pk_bf16_f32 v92, v204, v205\n"
"v_cvt_pk_bf16_f32 v93, v206, v207\n"
"v_cvt_pk_bf16_f32 v94, v224, v225\n"
"v_cvt_pk_bf16_f32 v95, v226, v227\n"
"ds_write_b128 v7, v[92:95] offset:50176\n"
"s_add_u32 s98, s60, 0x448000\n"
"v_rcp_f32_e32 v172, v172\n"
"s_nop 0\n"
"v_mul_f32_e32 v108, v108, v172\n"
"v_mul_f32_e32 v109, v109, v172\n"
"v_mul_f32_e32 v110, v110, v172\n"
"v_mul_f32_e32 v111, v111, v172\n"
"v_mul_f32_e32 v88, v88, v172\n"
"v_mul_f32_e32 v89, v89, v172\n"
"v_mul_f32_e32 v90, v90, v172\n"
"v_mul_f32_e32 v91, v91, v172\n"
"v_cvt_pk_bf16_f32 v92, v108, v109\n"
"v_cvt_pk_bf16_f32 v93, v110, v111\n"
"v_cvt_pk_bf16_f32 v94, v88, v89\n"
"v_cvt_pk_bf16_f32 v95, v90, v91\n"
"buffer_store_dwordx4 v[92:95], v0, s[4:7], s98 offen sc1\n"
".Lmk_b3:\n"
"s_waitcnt vmcnt(20)\n"
"; @B G1 3.0\n"
"v_mfma_f32_16x16x32_bf16 v[88:91], a[0:3], v[116:119], 0\n"
"v_mfma_f32_16x16x32_bf16 v[88:91], a[4:7], v[176:179], v[88:91]\n"
"s_add_u32 s98, s62, 0x40000\n"
"v_mfma_f32_16x16x32_bf16 v[88:91], a[8:11], v[184:187], v[88:91]\n"
"s_add_u32 s97, s54, 0x400000\n"
"buffer_load_dwordx4 v[92:95], v0, s[4:7], s97 offen sc1\n"
"v_mfma_f32_16x16x32_bf16 v[88:91], a[12:15], v[144:147], v[88:91]\n"
"s_waitcnt vmcnt(14)\n"
"v_mfma_f32_16x16x32_bf16 v[88:91], a[16:19], v[180:183], v[88:91]\n"
"buffer_load_dword v255, v10, s[4:7], s98 offen sc1\n"
"v_mfma_f32_16x16x32_bf16 v[88:91], a[20:23], v[228:231], v[88:91]\n"
"s_waitcnt lgkmcnt(2)\n"
"v_add_f32_e32 v136, v136, v188\n"
"v_add_f32_e32 v137, v137, v189\n"
"v_mfma_f32_16x16x32_bf16 v[88:91], a[24:27], v[128:131], v[88:91]\n"
"v_add_f32_e32 v138, v138, v190\n"
"v_add_f32_e32 v139, v139, v191\n"
"v_mfma_f32_16x16x32_bf16 v[88:91], a[28:31], v[100:103], v[88:91]\n"
"buffer_load_dwordx4 v[96:99], v0, s[4:7], s97 offen offset:1024 sc1\n"
"v_mfma_f32_16x16x32_bf16 v[108:111], a[32:35], v[116:119], 0\n"
"s_waitcnt lgkmcnt(0)\n"
"s_barrier\n"
"v_mfma_f32_16x16x32_bf16 v[108:111], a[36:39], v[176:179], v[108:111]\n"
"buffer_load_dwordx4 a[216:219], v1, s[4:7], s96 offen offset:2048 sc1\n"
"v_mfma_f32_16x16x32_bf16 v[108:111], a[40:43], v[184:187], v[108:111]\n"
"buffer_load_dword v253, v10, s[4:7], s98 offen offset:256 sc1\n"
"v_mfma_f32_16x16x32_bf16 v[108:111], a[44:47], v[144:147], v[108:111]\n"
"v_add_f32_e32 v104, v104, v236\n"
"v_add_f32_e32 v105, v105, v237\n"
"v_mfma_f32_16x16x32_bf16 v[108:111], a[48:51], v[180:183], v[108:111]\n"
"buffer_load_dwordx4 v[120:123], v0, s[4:7], s97 offen offset:2048 sc1\n"
"v_mfma_f32_16x16x32_bf16 v[108:111], a[52:55], v[228:231], v[108:111]\n"
"ds_write_b128 v3, v[88:91]\n"
"v_mfma_f32_16x16x32_bf16 v[108:111], a[56:59], v[128:131], v[108:111]\n"
"buffer_load_dword v252, v10, s[4:7], s98 offen offset:512 sc1\n"
"v_mfma_f32_16x16x32_bf16 v[108:111], a[60:63], v[100:103], v[108:111]\n"
"v_add_f32_e32 v106, v106, v238\n"
"v_add_f32_e32 v107, v107, v239\n"
"v_mfma_f32_16x16x32_bf16 v[88:91], a[64:67], v[116:119], 0\n"
"v_mul_f32_e32 v136, v136, v254\n"
"v_mul_f32_e32 v137, v137, v254\n"
"v_mfma_f32_16x16x32_bf16 v[88:91], a[68:71], v[176:179], v[88:91]\n"
"buffer_load_dwordx4 v[132:135], v0, s[4:7], s97 offen offset:3072 sc1\n"
"v_mfma_f32_16x16x32_bf16 v[88:91], a[72:75], v[184:187], v[88:91]\n"
"buffer_load_dword v251, v10, s[4:7], s98 offen offset:768 sc1\n"
"v_mfma_f32_16x16x32_bf16 v[88:91], a[76:79], v[144:147], v[88:91]\n"
"v_mul_f32_e32 v138, v138, v254\n"
"v_mul_f32_e32 v139, v139, v254\n"
"v_mfma_f32_16x16x32_bf16 v[88:91], a[80:83], v[180:183], v[88:91]\n"
"v_mul_f32_e32 v104, v104, v254\n"
"v_mul_f32_e32 v105, v105, v254\n"
"v_mfma_f32_16x16x32_bf16 v[88:91], a[84:87], v[228:231], v[88:91]\n"
"ds_write_b128 v3, v[108:111] offset:1024\n"
"v_mfma_f32_16x16x32_bf16 v[88:91], a[88:91], v[128:131], v[88:91]\n"
"buffer_load_dwordx4 v[108:111], v1, s[4:7], s97 offen sc1\n"
"v_mfma_f32_16x16x32_bf16 v[88:91], a[92:95], v[100:103], v[88:91]\n"
"buffer_load_dword v250, v10, s[4:7], s98 offen offset:1024 sc1\n"
"v_mfma_f32_16x16x32_bf16 v[152:155], a[96:99], v[116:119], 0\n"
"v_mul_f32_e32 v106, v106, v254\n"
"v_mul_f32_e32 v107, v107, v254\n"
"v_mfma_f32_16x16x32_bf16 v[152:155], a[100:103], v[176:179], v[152:155]\n"
"v_cvt_pk_bf16_f32 v160, v136, v137\n"
"v_cvt_pk_bf16_f32 v161, v138, v139\n"
"v_mfma_f32_16x16x32_bf16 v[152:155], a[104:107], v[184:187], v[152:155]\n"
"buffer_load_dword v254, v10, s[4:7], s98 offen offset:1280 sc1\n"
"v_mfma_f32_16x16x32_bf16 v[152:155], a[108:111], v[144:147], v[152:155]\n"
"buffer_load_dwordx4 v[136:139], v1, s[4:7], s97 offen offset:1024 sc1\n"
"v_mfma_f32_16x16x32_bf16 v[152:155], a[112:115], v[180:183], v[152:155]\n"
"v_cvt_pk_bf16_f32 v162, v104, v105\n"
"v_cvt_pk_bf16_f32 v163, v106, v107\n"
"v_mfma_f32_16x16x32_bf16 v[152:155], a[116:119], v[228:231], v[152:155]\n"
"ds_write_b128 v3, v[88:91] offset:2048\n"
"v_mfma_f32_16x16x32_bf16 v[152:155], a[120:123], v[128:131], v[152:155]\n"
"buffer_load_dword v249, v10, s[4:7], s98 offen offset:1536 sc1\n"
"v_mfma_f32_16x16x32_bf16 v[152:155], a[124:127], v[100:103], v[152:155]\n"
"ds_write_b128 v2, v[160:163] offset:17408\n"
"v_mfma_f32_16x16x32_bf16 v[88:91], v[24:27], v[116:119], 0\n"
"buffer_load_dwordx4 v[104:107], v1, s[4:7], s97 offen offset:2048 sc1\n"
"v_mfma_f32_16x16x32_bf16 v[88:91], v[28:31], v[176:179], v[88:91]\n"
"buffer_load_dwordx4 a[220:223], v1, s[4:7], s96 offen offset:3072 sc1\n"
"v_mfma_f32_16x16x32_bf16 v[88:91], v[32:35], v[184:187], v[88:91]\n"
"buffer_load_dword v248, v10, s[4:7], s98 offen offset:1792 sc1\n"
"v_mfma_f32_16x16x32_bf16 v[88:91], v[36:39], v[144:147], v[88:91]\n"
"v_add_u32_e32 v247, 0x26c00, v0\n"
"v_mfma_f32_16x16x32_bf16 v[88:91], v[40:43], v[180:183], v[88:91]\n"
"ds_read_b128 v[160:163], v247\n"
"ds_read_b128 v[168:171], v247 offset:1024\n"
"v_xor_b32_e32 v247, 32, v17\n"
"v_mfma_f32_16x16x32_bf16 v[88:91], v[44:47], v[228:231], v[88:91]\n"
"buffer_load_dwordx4 v[172:175], v1, s[4:7], s97 offen offset:3072 sc1\n"
"v_mfma_f32_16x16x32_bf16 v[88:91], v[48:51], v[128:131], v[88:91]\n"
"ds_write_b128 v3, v[152:155] offset:3072\n"
"v_mfma_f32_16x16x32_bf16 v[88:91], v[52:55], v[100:103], v[88:91]\n"
"ds_read_b128 v[152:155], v2 offset:49152\n"
"ds_read_b128 v[188:191], v17 offset:23552\n"
"v_mfma_f32_16x16x32_bf16 v[192:195], v[56:59], v[116:119], 0\n"
"ds_read_b128 v[116:119], v247 offset:23808\n"
"v_mfma_f32_16x16x32_bf16 v[192:195], v[60:63], v[176:179], v[192:195]\n"
"v_mfma_f32_16x16x32_bf16 v[192:195], v[64:67], v[184:187], v[192:195]\n"
"v_mfma_f32_16x16x32_bf16 v[192:195], v[68:71], v[144:147], v[192:195]\n"
"v_mfma_f32_16x16x32_bf16 v[192:195], v[72:75], v[180:183], v[192:195]\n"
"v_mfma_f32_16x16x32_bf16 v[192:195], v[76:79], v[228:231], v[192:195]\n"
"ds_write_b128 v3, v[88:91] offset:4096\n"
"v_mfma_f32_16x16x32_bf16 v[192:195], v[80:83], v[128:131], v[192:195]\n"
"s_waitcnt lgkmcnt(1)\n"
"v_mfma_f32_16x16x32_bf16 v[160:163], v[188:191], v[152:155], v[160:163]\n"
"v_mfma_f32_16x16x32_bf16 v[168:171], v[116:119], v[152:155], v[168:171]\n"
"ds_read_b64_tr_b16 v[88:89], v16 offset:23552\n"
"v_xor_b32_e32 v247, 32, v16\n"
"v_mfma_f32_16x16x32_bf16 v[192:195], v[84:87], v[100:103], v[192:195]\n"
"s_waitcnt vmcnt(26)\n"
"ds_read_b64_tr_b16 v[90:91], v247 offset:23808\n"
"ds_read_b64_tr_b16 v[100:101], v247 offset:23552\n"
"; @B G1 3.1\n"
"v_mfma_f32_16x16x32_bf16 v[116:119], a[0:3], v[112:115], 0\n"
"ds_read_b64_tr_b16 v[102:103], v16 offset:23808\n"
"v_mov_b32_e32 v128, 0x3f803f80\n"
"v_mfma_f32_16x16x32_bf16 v[116:119], a[4:7], v[156:159], v[116:119]\n"
"v_mov_b32_e32 v129, 0x3f803f80\n"
"v_mov_b32_e32 v130, 0x3f803f80\n"
"v_mfma_f32_16x16x32_bf16 v[116:119], a[8:11], v[216:219], v[116:119]\n"
"s_add_u32 s98, s55, 0x400000\n"
"buffer_load_dwordx4 v[144:147], v0, s[4:7], s98 offen sc1\n"
"v_mfma_f32_16x16x32_bf16 v[116:119], a[12:15], v[196:199], v[116:119]\n"
"s_waitcnt vmcnt(22)\n"
"v_mov_b32_e32 v131, 0x3f803f80\n"
"v_mfma_f32_16x16x32_bf16 v[116:119], a[16:19], v[148:151], v[116:119]\n"
"v_max_f32_e32 v247, v160, v161\n"
"v_max3_f32 v247, v247, v162, v163\n"
"v_mfma_f32_16x16x32_bf16 v[116:119], a[20:23], v[124:127], v[116:119]\n"
"ds_write_b128 v3, v[192:195] offset:5120\n"
"v_mfma_f32_16x16x32_bf16 v[116:119], a[24:27], v[140:143], v[116:119]\n"
"v_max3_f32 v247, v247, v168, v169\n"
"v_max3_f32 v247, v247, v170, v171\n"
"v_mfma_f32_16x16x32_bf16 v[116:119], a[28:31], v[164:167], v[116:119]\n"
"buffer_load_dwordx4 v[152:155], v0, s[4:7], s98 offen offset:1024 sc1\n"
"v_mfma_f32_16x16x32_bf16 v[176:179], a[32:35], v[112:115], 0\n"
"s_add_u32 s97, s70, 0x206000\n"
"v_mov_b32_e32 v246, v247\n"
"v_mfma_f32_16x16x32_bf16 v[176:179], a[36:39], v[156:159], v[176:179]\n"
"buffer_load_dwordx4 a[224:227], v0, s[4:7], s97 offen sc1\n"
"v_mfma_f32_16x16x32_bf16 v[176:179], a[40:43], v[216:219], v[176:179]\n"
"v_permlane16_swap_b32_e32 v247, v246\n"
"v_max_f32_e32 v247, v247, v246\n"
"v_mfma_f32_16x16x32_bf16 v[176:179], a[44:47], v[196:199], v[176:179]\n"
"v_mov_b32_e32 v246, v247\n"
"v_mfma_f32_16x16x32_bf16 v[176:179], a[48:51], v[148:151], v[176:179]\n"
"buffer_load_dwordx4 v[180:183], v0, s[4:7], s98 offen offset:2048 sc1\n"
"v_mfma_f32_16x16x32_bf16 v[176:179], a[52:55], v[124:127], v[176:179]\n"
"ds_write_b128 v3, v[116:119] offset:6144\n"
"v_mfma_f32_16x16x32_bf16 v[176:179], a[56:59], v[140:143], v[176:179]\n"
"s_add_u32 s96, s73, 0\n"
"buffer_load_dwordx2 v[244:245], off, s[4:7], s96 sc1\n"
"v_mfma_f32_16x16x32_bf16 v[176:179], a[60:63], v[164:167], v[176:179]\n"
"v_permlane32_swap_b32_e32 v247, v246\n"
"v_max_f32_e32 v247, v247, v246\n"
"v_mfma_f32_16x16x32_bf16 v[116:119], a[64:67], v[112:115], 0\n"
"v_sub_f32_e32 v160, v160, v247\n"
"v_sub_f32_e32 v161, v161, v247\n"
"v_mfma_f32_16x16x32_bf16 v[116:119], a[68:71], v[156:159], v[116:119]\n"
"buffer_load_dwordx4 v[184:187], v0, s[4:7], s98 offen offset:3072 sc1\n"
"v_mfma_f32_16x16x32_bf16 v[116:119], a[72:75], v[216:219], v[116:119]\n"
"v_sub_f32_e32 v162, v162, v247\n"
"v_sub_f32_e32 v163, v163, v247\n"
"v_mfma_f32_16x16x32_bf16 v[116:119], a[76:79], v[196:199], v[116:119]\n"
"v_sub_f32_e32 v168, v168, v247\n"
"v_sub_f32_e32 v169, v169, v247\n"
"v_mfma_f32_16x16x32_bf16 v[116:119], a[80:83], v[148:151], v[116:119]\n"
"v_mov_b32_e32 v188, -1\n"
"v_mov_b32_e32 v189, -1\n"
"v_mfma_f32_16x16x32_bf16 v[116:119], a[84:87], v[124:127], v[116:119]\n"
"ds_write_b128 v3, v[176:179] offset:7168\n"
"v_mfma_f32_16x16x32_bf16 v[116:119], a[88:91], v[140:143], v[116:119]\n"
"buffer_load_dwordx4 v[176:179], v1, s[4:7], s98 offen sc1\n"
"v_mfma_f32_16x16x32_bf16 v[116:119], a[92:95], v[164:167], v[116:119]\n"
"v_mov_b32_e32 v190, -1\n"
"v_mov_b32_e32 v191, -1\n"
"v_mfma_f32_16x16x32_bf16 v[192:195], a[96:99], v[112:115], 0\n"
"s_add_u32 s95, s60, 0x800000\n"
"buffer_store_dwordx4 v[188:191], v0, s[8:11], s95 offen sc1\n"
"v_mfma_f32_16x16x32_bf16 v[192:195], a[100:103], v[156:159], v[192:195]\n"
"v_sub_f32_e32 v170, v170, v247\n"
"v_sub_f32_e32 v171, v171, v247\n"
"v_mfma_f32_16x16x32_bf16 v[192:195], a[104:107], v[216:219], v[192:195]\n"
"v_exp_f32_e32 v160, v160\n"
"v_mfma_f32_16x16x32_bf16 v[192:195], a[108:111], v[196:199], v[192:195]\n"
"buffer_load_dwordx4 v[200:203], v1, s[4:7], s98 offen offset:1024 sc1\n"
"v_mfma_f32_16x16x32_bf16 v[192:195], a[112:115], v[148:151], v[192:195]\n"
"v_exp_f32_e32 v161, v161\n"
"v_mfma_f32_16x16x32_bf16 v[192:195], a[116:119], v[124:127], v[192:195]\n"
"ds_write_b128 v3, v[116:119] offset:8192\n"
"v_mfma_f32_16x16x32_bf16 v[192:195], a[120:123], v[140:143], v[192:195]\n"
"v_exp_f32_e32 v162, v162\n"
"v_mfma_f32_16x16x32_bf16 v[192:195], a[124:127], v[164:167], v[192:195]\n"
"v_exp_f32_e32 v163, v163\n"
"v_mfma_f32_16x16x32_bf16 v[116:119], v[24:27], v[112:115], 0\n"
"buffer_load_dwordx4 v[204:207], v1, s[4:7], s98 offen offset:2048 sc1\n"
"v_mfma_f32_16x16x32_bf16 v[116:119], v[28:31], v[156:159], v[116:119]\n"
"buffer_load_dwordx4 a[228:231], v0, s[4:7], s97 offen offset:1024 sc1\n"
"v_mfma_f32_16x16x32_bf16 v[116:119], v[32:35], v[216:219], v[116:119]\n"
"v_exp_f32_e32 v168, v168\n"
"v_mfma_f32_16x16x32_bf16 v[116:119], v[36:39], v[196:199], v[116:119]\n"
"v_exp_f32_e32 v169, v169\n"
"v_mfma_f32_16x16x32_bf16 v[116:119], v[40:43], v[148:151], v[116:119]\n"
"s_waitcnt vmcnt(12)\n"
"v_add_f32_e32 v255, v255, v250\n"
"v_add_f32_e32 v253, v253, v254\n"
"v_mfma_f32_16x16x32_bf16 v[116:119], v[44:47], v[124:127], v[116:119]\n"
"buffer_load_dwordx4 v[208:211], v1, s[4:7], s98 offen offset:3072 sc1\n"
"v_mfma_f32_16x16x32_bf16 v[116:119], v[48:51], v[140:143], v[116:119]\n"
"ds_write_b128 v3, v[192:195] offset:9216\n"
"v_mfma_f32_16x16x32_bf16 v[116:119], v[52:55], v[164:167], v[116:119]\n"
"v_add_f32_e32 v252, v252, v249\n"
"v_add_f32_e32 v251, v251, v248\n"
"v_mfma_f32_16x16x32_bf16 v[192:195], v[56:59], v[112:115], 0\n"
"v_add_f32_e32 v255, v255, v252\n"
"v_add_f32_e32 v253, v253, v251\n"
"v_mfma_f32_16x16x32_bf16 v[192:195], v[60:63], v[156:159], v[192:195]\n"
"v_add_f32_e32 v255, v255, v253\n"
"v_mov_b32_e32 v253, v255\n"
"v_mfma_f32_16x16x32_bf16 v[192:195], v[64:67], v[216:219], v[192:195]\n"
"s_nop 0\n"
"v_permlane32_swap_b32_e32 v255, v253\n"
"v_add_f32_e32 v255, v255, v253\n"
"v_mfma_f32_16x16x32_bf16 v[192:195], v[68:71], v[196:199], v[192:195]\n"
"v_mul_f32_e32 v255, 0x3a800000, v255\n"
"v_mov_b32_e32 v253, v255\n"
"v_mfma_f32_16x16x32_bf16 v[192:195], v[72:75], v[148:151], v[192:195]\n"
"s_nop 0\n"
"v_permlane16_swap_b32_e32 v255, v253\n"
"v_cvt_pk_bf16_f32 v254, -v255, 0\n"
"v_mfma_f32_16x16x32_bf16 v[192:195], v[76:79], v[124:127], v[192:195]\n"
"ds_write_b128 v3, v[116:119] offset:10240\n"
"v_mfma_f32_16x16x32_bf16 v[192:195], v[80:83], v[140:143], v[192:195]\n"
"v_fma_f32 v253, -v255, v255, v253\n"
"v_lshlrev_b32_e32 v254, 16, v254\n"
"v_mfma_f32_16x16x32_bf16 v[192:195], v[84:87], v[164:167], v[192:195]\n"
"v_max_f32_e32 v253, 0, v253\n"
"v_cvt_pk_bf16_f32 v253, v253, 0\n"
"; @B G1 3.2\n"
"v_mfma_f32_16x16x32_bf16 v[112:115], a[0:3], v[92:95], 0\n"
"v_lshlrev_b32_e32 v253, 16, v253\n"
"v_add_f32_e32 v253, 0x3727c5ac, v253\n"
"v_mfma_f32_16x16x32_bf16 v[112:115], a[4:7], v[96:99], v[112:115]\n"
"v_cvt_pk_bf16_f32 v253, v253, 0\n"
"v_lshlrev_b32_e32 v253, 16, v253\n"
"v_mfma_f32_16x16x32_bf16 v[112:115], a[8:11], v[120:123], v[112:115]\n"
"s_add_u32 s98, s60, 0x808000\n"
"v_sqrt_f32_e32 v253, v253\n"
"v_mfma_f32_16x16x32_bf16 v[112:115], a[12:15], v[132:135], v[112:115]\n"
"s_waitcnt vmcnt(12)\n"
"v_cvt_pk_bf16_f32 v253, v253, 0\n"
"v_lshlrev_b32_e32 v253, 16, v253\n"
"v_mfma_f32_16x16x32_bf16 v[112:115], a[16:19], v[108:111], v[112:115]\n"
"buffer_store_dwordx4 v[188:191], v0, s[8:11], s98 offen sc1\n"
"v_mfma_f32_16x16x32_bf16 v[112:115], a[20:23], v[136:139], v[112:115]\n"
"ds_write_b128 v3, v[192:195] offset:11264\n"
"v_mfma_f32_16x16x32_bf16 v[112:115], a[24:27], v[104:107], v[112:115]\n"
"buffer_load_dwordx4 a[232:235], v0, s[4:7], s97 offen offset:2048 sc1\n"
"v_mfma_f32_16x16x32_bf16 v[112:115], a[28:31], v[172:175], v[112:115]\n"
"v_rcp_f32_e32 v255, v253\n"
"v_mfma_f32_16x16x32_bf16 v[116:119], a[32:35], v[92:95], 0\n"
"v_mul_f32_e32 v253, 0x3e8293ee, v255\n"
"v_mfma_f32_16x16x32_bf16 v[116:119], a[36:39], v[96:99], v[116:119]\n"
"v_exp_f32_e32 v170, v170\n"
"v_mfma_f32_16x16x32_bf16 v[116:119], a[40:43], v[120:123], v[116:119]\n"
"s_waitcnt lgkmcnt(0)\n"
"s_barrier\n"
"v_mfma_f32_16x16x32_bf16 v[116:119], a[44:47], v[132:135], v[116:119]\n"
"ds_read_b128 v[124:127], v4\n"
"ds_read_b128 v[140:143], v9 offset:640\n"
"v_mfma_f32_16x16x32_bf16 v[116:119], a[48:51], v[108:111], v[116:119]\n"
"buffer_load_dwordx4 a[236:239], v0, s[4:7], s97 offen offset:3072 sc1\n"
"v_mfma_f32_16x16x32_bf16 v[116:119], a[52:55], v[136:139], v[116:119]\n"
"ds_write_b128 v3, v[112:115] offset:12288\n"
"v_mfma_f32_16x16x32_bf16 v[116:119], a[56:59], v[104:107], v[116:119]\n"
"ds_read_b128 v[112:115], v5\n"
"v_mfma_f32_16x16x32_bf16 v[116:119], a[60:63], v[172:175], v[116:119]\n"
"v_exp_f32_e32 v171, v171\n"
"v_mfma_f32_16x16x32_bf16 v[148:151], a[64:67], v[92:95], 0\n"
"v_cvt_pk_bf16_f32 v156, v160, v161\n"
"v_cvt_pk_bf16_f32 v157, v162, v163\n"
"v_mfma_f32_16x16x32_bf16 v[148:151], a[68:71], v[96:99], v[148:151]\n"
"v_cvt_pk_bf16_f32 v158, v168, v169\n"
"v_cvt_pk_bf16_f32 v159, v170, v171\n"
"v_mfma_f32_16x16x32_bf16 v[148:151], a[72:75], v[120:123], v[148:151]\n"
"buffer_load_dwordx4 a[240:243], v1, s[4:7], s97 offen sc1\n"
"v_mfma_f32_16x16x32_bf16 v[148:151], a[76:79], v[132:135], v[148:151]\n"
"ds_read_b128 v[160:163], v4 offset:1024\n"
"ds_read_b128 v[164:167], v9 offset:704\n"
"v_mfma_f32_16x16x32_bf16 v[148:151], a[80:83], v[108:111], v[148:151]\n"
"ds_read_b128 v[168:171], v5 offset:1024\n"
"s_waitcnt lgkmcnt(3)\n"
"v_mfma_f32_16x16x32_bf16 v[148:151], a[84:87], v[136:139], v[148:151]\n"
"ds_write_b128 v3, v[116:119] offset:13312\n"
"v_mfma_f32_16x16x32_bf16 v[148:151], a[88:91], v[104:107], v[148:151]\n"
"v_fmac_f32_e32 v124, v254, v140\n"
"v_fmac_f32_e32 v125, v254, v141\n"
"v_mfma_f32_16x16x32_bf16 v[116:119], v[128:131], v[156:159], 0\n"
"v_mfma_f32_16x16x32_bf16 v[128:131], v[88:91], v[156:159], 0\n"
"v_mfma_f32_16x16x32_bf16 v[88:91], v[100:103], v[156:159], 0\n"
"v_mfma_f32_16x16x32_bf16 v[148:151], a[92:95], v[172:175], v[148:151]\n"
"v_fmac_f32_e32 v126, v254, v142\n"
"v_fmac_f32_e32 v127, v254, v143\n"
"v_mfma_f32_16x16x32_bf16 v[100:103], a[96:99], v[92:95], 0\n"
"buffer_load_dwordx4 a[244:247], v1, s[4:7], s97 offen offset:1024 sc1\n"
"v_mfma_f32_16x16x32_bf16 v[100:103], a[100:103], v[96:99], v[100:103]\n"
"s_waitcnt vmcnt(12)\n"
"v_cmp_ne_u64_e32 vcc, v[244:245], v[18:19]\n"
"v_mfma_f32_16x16x32_bf16 v[100:103], a[104:107], v[120:123], v[100:103]\n"
"s_cbranch_vccnz .Lmk_f45s\n"
".Lmk_f45r:\n"
"s_mov_b32 m0, 0x7c00\n"
"v_mfma_f32_16x16x32_bf16 v[100:103], a[108:111], v[132:135], v[100:103]\n"
"s_add_u32 s98, s66, 0x3c00\n"
"buffer_load_dwordx4 v0, s[4:7], s98 offen sc1 lds\n"
"v_mfma_f32_16x16x32_bf16 v[100:103], a[112:115], v[108:111], v[100:103]\n"
"s_mov_b32 m0, 0xfc00\n"
"s_add_u32 s98, s66, 0x7c00\n"
"ds_read_b128 v[140:143], v4 offset:2048\n"
"v_mfma_f32_16x16x32_bf16 v[100:103], a[116:119], v[136:139], v[100:103]\n"
"ds_write_b128 v3, v[148:151] offset:14336\n"
"v_mfma_f32_16x16x32_bf16 v[100:103], a[120:123], v[104:107], v[100:103]\n"
"s_add_u32 s96, s61, 0xc40000\n"
"buffer_load_dwordx4 a[248:251], v1, s[4:7], s97 offen offset:2048 sc1\n"
"v_mfma_f32_16x16x32_bf16 v[100:103], a[124:127], v[172:175], v[100:103]\n"
"buffer_load_dwordx4 v0, s[4:7], s98 offen sc1 lds\n"
"v_mfma_f32_16x16x32_bf16 v[148:151], v[24:27], v[92:95], 0\n"
"buffer_store_dwordx4 v[188:191], v0, s[8:11], s96 offen sc1\n"
"v_mfma_f32_16x16x32_bf16 v[148:151], v[28:31], v[96:99], v[148:151]\n"
"ds_read_b128 v[156:159], v9 offset:768\n"
"ds_read_b128 v[192:195], v5 offset:2048\n"
"v_mfma_f32_16x16x32_bf16 v[148:151], v[32:35], v[120:123], v[148:151]\n"
"s_add_u32 s98, s67, 0x10020\n"
"buffer_load_dwordx2 v[250:251], v12, s[4:7], s98 offen sc1\n"
"v_mfma_f32_16x16x32_bf16 v[148:151], v[36:39], v[132:135], v[148:151]\n"
"ds_read_b128 v[196:199], v4 offset:3072\n"
"ds_read_b128 v[212:215], v9 offset:832\n"
"v_mfma_f32_16x16x32_bf16 v[148:151], v[40:43], v[108:111], v[148:151]\n"
"buffer_load_dwordx4 a[252:255], v1, s[4:7], s97 offen offset:3072 sc1\n"
"v_mfma_f32_16x16x32_bf16 v[148:151], v[44:47], v[136:139], v[148:151]\n"
"ds_write_b128 v3, v[100:103] offset:15360\n"
"v_mfma_f32_16x16x32_bf16 v[148:151], v[48:51], v[104:107], v[148:151]\n"
"ds_read_b128 v[100:103], v5 offset:3072\n"
"v_add_f32_e32 v124, v124, v112\n"
"v_mfma_f32_16x16x32_bf16 v[148:151], v[52:55], v[172:175], v[148:151]\n"
"ds_read_b128 v[216:219], v4 offset:4096\n"
"ds_read_b128 v[220:223], v9 offset:512\n"
"v_mfma_f32_16x16x32_bf16 v[224:227], v[56:59], v[92:95], 0\n"
"ds_read_b128 v[92:95], v5 offset:4096\n"
"v_add_f32_e32 v125, v125, v113\n"
"v_mfma_f32_16x16x32_bf16 v[224:227], v[60:63], v[96:99], v[224:227]\n"
"s_waitcnt lgkmcnt(12)\n"
"v_add_f32_e32 v126, v126, v114\n"
"v_add_f32_e32 v127, v127, v115\n"
"v_mfma_f32_16x16x32_bf16 v[224:227], v[64:67], v[120:123], v[224:227]\n"
"v_fmac_f32_e32 v160, v254, v164\n"
"v_fmac_f32_e32 v161, v254, v165\n"
"v_mfma_f32_16x16x32_bf16 v[224:227], v[68:71], v[132:135], v[224:227]\n"
"v_fmac_f32_e32 v162, v254, v166\n"
"v_fmac_f32_e32 v163, v254, v167\n"
"v_mfma_f32_16x16x32_bf16 v[224:227], v[72:75], v[108:111], v[224:227]\n"
"v_add_f32_e32 v160, v160, v168\n"
"v_add_f32_e32 v161, v161, v169\n"
"v_mfma_f32_16x16x32_bf16 v[224:227], v[76:79], v[136:139], v[224:227]\n"
"ds_write_b128 v3, v[148:151] offset:16384\n"
"v_mfma_f32_16x16x32_bf16 v[224:227], v[80:83], v[104:107], v[224:227]\n"
"ds_read_b128 v[96:99], v4 offset:5120\n"
"ds_read_b128 v[104:107], v9 offset:576\n"
"v_mfma_f32_16x16x32_bf16 v[224:227], v[84:87], v[172:175], v[224:227]\n"
"s_waitcnt vmcnt(17)\n"
"s_waitcnt lgkmcnt(13)\n"
"ds_read_b128 v[108:111], v5 offset:5120\n"
"v_add_f32_e32 v162, v162, v170\n"
"; @B G1 3.3\n"
"v_mfma_f32_16x16x32_bf16 v[124:127], a[0:3], v[144:147], v[124:127]\n"
"s_waitcnt lgkmcnt(11)\n"
"v_add_f32_e32 v163, v163, v171\n"
"v_fmac_f32_e32 v140, v254, v156\n"
"v_mfma_f32_16x16x32_bf16 v[124:127], a[4:7], v[152:155], v[124:127]\n"
"v_fmac_f32_e32 v141, v254, v157\n"
"v_fmac_f32_e32 v142, v254, v158\n"
"v_mfma_f32_16x16x32_bf16 v[124:127], a[8:11], v[180:183], v[124:127]\n"
"v_fmac_f32_e32 v143, v254, v159\n"
"v_add_f32_e32 v140, v140, v192\n"
"v_mfma_f32_16x16x32_bf16 v[124:127], a[12:15], v[184:187], v[124:127]\n"
"s_waitcnt vmcnt(11)\n"
"v_add_f32_e32 v141, v141, v193\n"
"v_add_f32_e32 v142, v142, v194\n"
"v_mfma_f32_16x16x32_bf16 v[124:127], a[16:19], v[176:179], v[124:127]\n"
"s_waitcnt lgkmcnt(7)\n"
"v_add_f32_e32 v143, v143, v195\n"
"v_fmac_f32_e32 v196, v254, v212\n"
"v_mfma_f32_16x16x32_bf16 v[124:127], a[20:23], v[200:203], v[124:127]\n"
"ds_write_b128 v3, v[224:227] offset:17408\n"
"v_mfma_f32_16x16x32_bf16 v[124:127], a[24:27], v[204:207], v[124:127]\n"
"v_fmac_f32_e32 v197, v254, v213\n"
"v_fmac_f32_e32 v198, v254, v214\n"
"v_mfma_f32_16x16x32_bf16 v[124:127], a[28:31], v[208:211], v[124:127]\n"
"v_fmac_f32_e32 v199, v254, v215\n"
"v_add_f32_e32 v196, v196, v100\n"
"v_mfma_f32_16x16x32_bf16 v[160:163], a[32:35], v[144:147], v[160:163]\n"
"v_add_f32_e32 v197, v197, v101\n"
"v_add_f32_e32 v198, v198, v102\n"
"v_mfma_f32_16x16x32_bf16 v[160:163], a[36:39], v[152:155], v[160:163]\n"
"s_waitcnt lgkmcnt(5)\n"
"v_add_f32_e32 v199, v199, v103\n"
"v_fmac_f32_e32 v216, v254, v220\n"
"v_mfma_f32_16x16x32_bf16 v[160:163], a[40:43], v[180:183], v[160:163]\n"
"s_waitcnt lgkmcnt(0)\n"
"s_barrier\n"
"v_mfma_f32_16x16x32_bf16 v[160:163], a[44:47], v[184:187], v[160:163]\n"
"s_add_u32 s97, s61, 0xc40000\n"
"ds_read_b128 v[100:103], v6\n"
"ds_read_b128 v[112:115], v6 offset:1024\n"
"v_mfma_f32_16x16x32_bf16 v[160:163], a[48:51], v[176:179], v[160:163]\n"
"buffer_store_dwordx4 v[188:191], v0, s[8:11], s97 offen offset:1024 sc1\n"
"v_mfma_f32_16x16x32_bf16 v[160:163], a[52:55], v[200:203], v[160:163]\n"
"v_fmac_f32_e32 v217, v254, v221\n"
"v_fmac_f32_e32 v218, v254, v222\n"
"v_mfma_f32_16x16x32_bf16 v[160:163], a[56:59], v[204:207], v[160:163]\n"
"v_fmac_f32_e32 v219, v254, v223\n"
"v_add_f32_e32 v216, v216, v92\n"
"v_mfma_f32_16x16x32_bf16 v[160:163], a[60:63], v[208:211], v[160:163]\n"
"v_add_f32_e32 v217, v217, v93\n"
"v_add_f32_e32 v218, v218, v94\n"
"v_mfma_f32_16x16x32_bf16 v[140:143], a[64:67], v[144:147], v[140:143]\n"
"v_add_f32_e32 v219, v219, v95\n"
"v_fmac_f32_e32 v96, v254, v104\n"
"v_mfma_f32_16x16x32_bf16 v[140:143], a[68:71], v[152:155], v[140:143]\n"
"v_fmac_f32_e32 v97, v254, v105\n"
"v_fmac_f32_e32 v98, v254, v106\n"
"v_mfma_f32_16x16x32_bf16 v[140:143], a[72:75], v[180:183], v[140:143]\n"
"v_fmac_f32_e32 v99, v254, v107\n"
"v_add_f32_e32 v96, v96, v108\n"
"v_mfma_f32_16x16x32_bf16 v[140:143], a[76:79], v[184:187], v[140:143]\n"
"v_add_f32_e32 v97, v97, v109\n"
"v_add_f32_e32 v98, v98, v110\n"
"v_mfma_f32_16x16x32_bf16 v[140:143], a[80:83], v[176:179], v[140:143]\n"
"s_waitcnt lgkmcnt(0)\n"
"v_add_f32_e32 v99, v99, v111\n"
"v_add_f32_e32 v124, v124, v100\n"
"v_mfma_f32_16x16x32_bf16 v[140:143], a[84:87], v[200:203], v[140:143]\n"
"v_rcp_f32_e32 v116, v116\n"
"v_mfma_f32_16x16x32_bf16 v[140:143], a[88:91], v[204:207], v[140:143]\n"
"ds_read_b128 v[92:95], v6 offset:2048\n"
"ds_read_b128 v[104:107], v6 offset:3072\n"
"v_mfma_f32_16x16x32_bf16 v[140:143], a[92:95], v[208:211], v[140:143]\n"
"v_mul_f32_e32 v128, v128, v116\n"
"v_mul_f32_e32 v129, v129, v116\n"
"v_mfma_f32_16x16x32_bf16 v[196:199], a[96:99], v[144:147], v[196:199]\n"
"v_mul_f32_e32 v130, v130, v116\n"
"v_mul_f32_e32 v131, v131, v116\n"
"v_mfma_f32_16x16x32_bf16 v[196:199], a[100:103], v[152:155], v[196:199]\n"
"v_mul_f32_e32 v88, v88, v116\n"
"v_mul_f32_e32 v89, v89, v116\n"
"v_mfma_f32_16x16x32_bf16 v[196:199], a[104:107], v[180:183], v[196:199]\n"
"v_mul_f32_e32 v90, v90, v116\n"
"v_mul_f32_e32 v91, v91, v116\n"
"v_mfma_f32_16x16x32_bf16 v[196:199], a[108:111], v[184:187], v[196:199]\n"
"v_cvt_pk_bf16_f32 v108, v128, v129\n"
"v_cvt_pk_bf16_f32 v109, v130, v131\n"
"v_mfma_f32_16x16x32_bf16 v[196:199], a[112:115], v[176:179], v[196:199]\n"
"s_add_u32 s97, s60, 0x440000\n"
"v_cvt_pk_bf16_f32 v110, v88, v89\n"
"v_cvt_pk_bf16_f32 v111, v90, v91\n"
"v_mfma_f32_16x16x32_bf16 v[196:199], a[116:119], v[200:203], v[196:199]\n"
"buffer_store_dwordx4 v[108:111], v0, s[4:7], s97 offen sc1\n"
"v_mfma_f32_16x16x32_bf16 v[196:199], a[120:123], v[204:207], v[196:199]\n"
";\n"
"v_add_f32_e32 v125, v125, v101\n"
"v_add_f32_e32 v126, v126, v102\n"
"v_mfma_f32_16x16x32_bf16 v[196:199], a[124:127], v[208:211], v[196:199]\n"
"v_add_f32_e32 v127, v127, v103\n"
"v_add_f32_e32 v160, v160, v112\n"
"v_mfma_f32_16x16x32_bf16 v[216:219], v[24:27], v[144:147], v[216:219]\n"
"v_add_f32_e32 v161, v161, v113\n"
"v_add_f32_e32 v162, v162, v114\n"
"v_mfma_f32_16x16x32_bf16 v[216:219], v[28:31], v[152:155], v[216:219]\n"
"v_add_f32_e32 v163, v163, v115\n"
"v_mul_f32_e32 v124, v124, v255\n"
"v_mfma_f32_16x16x32_bf16 v[216:219], v[32:35], v[180:183], v[216:219]\n"
"v_mul_f32_e32 v125, v125, v255\n"
"v_mul_f32_e32 v126, v126, v255\n"
"v_mfma_f32_16x16x32_bf16 v[216:219], v[36:39], v[184:187], v[216:219]\n"
"s_add_u32 s97, s69, 0\n"
"buffer_load_dwordx4 a[0:3], v0, s[8:11], s97 offen sc1\n"
"v_mfma_f32_16x16x32_bf16 v[216:219], v[40:43], v[176:179], v[216:219]\n"
"s_waitcnt lgkmcnt(0)\n"
"v_mul_f32_e32 v127, v127, v255\n"
"v_mul_f32_e32 v160, v160, v255\n"
"v_mfma_f32_16x16x32_bf16 v[216:219], v[44:47], v[200:203], v[216:219]\n"
"v_mul_f32_e32 v161, v161, v255\n"
"v_mul_f32_e32 v162, v162, v255\n"
"v_mfma_f32_16x16x32_bf16 v[216:219], v[48:51], v[204:207], v[216:219]\n"
"s_add_u32 s96, s63, 0xc4000\n"
"ds_read_b128 v[24:27], v6 offset:4096\n"
"ds_read_b128 v[28:31], v6 offset:5120\n"
"v_mfma_f32_16x16x32_bf16 v[216:219], v[52:55], v[208:211], v[216:219]\n"
"v_mul_f32_e32 v163, v163, v255\n"
"v_cvt_pk_bf16_f32 v32, v124, v125\n"
"v_mfma_f32_16x16x32_bf16 v[96:99], v[56:59], v[144:147], v[96:99]\n"
"buffer_store_dword v188, v14, s[8:11], s96 offen sc1\n"
"v_mfma_f32_16x16x32_bf16 v[96:99], v[60:63], v[152:155], v[96:99]\n"
"buffer_load_dwordx4 a[4:7], v0, s[8:11], s97 offen offset:1024 sc1\n"
"v_mfma_f32_16x16x32_bf16 v[96:99], v[64:67], v[180:183], v[96:99]\n"
"v_cvt_pk_bf16_f32 v33, v126, v127\n"
"v_cvt_pk_bf16_f32 v34, v160, v161\n"
"v_mfma_f32_16x16x32_bf16 v[96:99], v[68:71], v[184:187], v[96:99]\n"
"v_cvt_pk_bf16_f32 v35, v162, v163\n"
"v_add_f32_e32 v140, v140, v92\n"
"v_mfma_f32_16x16x32_bf16 v[96:99], v[72:75], v[176:179], v[96:99]\n"
"ds_write_b128 v7, v[32:35] offset:16384\n"
"v_mfma_f32_16x16x32_bf16 v[96:99], v[76:79], v[200:203], v[96:99]\n"
"v_add_f32_e32 v141, v141, v93\n"
"v_add_f32_e32 v142, v142, v94\n"
"v_mfma_f32_16x16x32_bf16 v[96:99], v[80:83], v[204:207], v[96:99]\n"
"buffer_load_dwordx4 a[8:11], v0, s[8:11], s97 offen offset:2048 sc1\n"
"v_mfma_f32_16x16x32_bf16 v[96:99], v[84:87], v[208:211], v[96:99]\n"
"v_add_f32_e32 v143, v143, v95\n"
"v_add_f32_e32 v196, v196, v104\n"
"v_add_f32_e32 v197, v197, v105\n"
"v_add_f32_e32 v198, v198, v106\n"
"v_add_f32_e32 v199, v199, v107\n"
"v_mul_f32_e32 v140, v140, v255\n"
"v_mul_f32_e32 v141, v141, v255\n"
"v_mul_f32_e32 v142, v142, v255\n"
"v_mul_f32_e32 v143, v143, v255\n"
"v_mul_f32_e32 v196, v196, v255\n"
"v_mul_f32_e32 v197, v197, v255\n"
"v_mul_f32_e32 v198, v198, v255\n"
"v_mul_f32_e32 v199, v199, v255\n"
"v_cvt_pk_bf16_f32 v32, v140, v141\n"
"v_cvt_pk_bf16_f32 v33, v142, v143\n"
"v_cvt_pk_bf16_f32 v34, v196, v197\n"
"v_cvt_pk_bf16_f32 v35, v198, v199\n"
"ds_write_b128 v7, v[32:35] offset:49152\n"
"v_add_u32_e32 v255, 0x26c00, v0\n"
"ds_read_b128 v[32:35], v255\n"
"ds_read_b128 v[36:39], v255 offset:1024\n"
"v_add_u32_e32 v255, 0x26c00, v0\n"
"ds_read_b128 v[40:43], v255\n"
"ds_read_b128 v[44:47], v255 offset:1024\n"
"buffer_load_dwordx4 a[12:15], v0, s[8:11], s97 offen offset:3072 sc1\n"
"s_waitcnt lgkmcnt(0)\n"
"buffer_load_dwordx4 a[16:19], v1, s[8:11], s97 offen sc1\n"
"buffer_load_dwordx4 a[20:23], v1, s[8:11], s97 offen offset:1024 sc1\n"
"buffer_load_dwordx4 a[24:27], v1, s[8:11], s97 offen offset:2048 sc1\n"
"buffer_load_dwordx4 a[28:31], v1, s[8:11], s97 offen offset:3072 sc1\n"
"v_mov_b32_e32 v255, 0xff800000\n"
"v_cndmask_b32_e64 v40, v40, v255, s[82:83]\n"
"v_cndmask_b32_e64 v41, v41, v255, s[82:83]\n"
"v_cndmask_b32_e64 v42, v42, v255, s[82:83]\n"
"v_cndmask_b32_e64 v43, v43, v255, s[82:83]\n"
"v_cndmask_b32_e64 v44, v44, v255, s[82:83]\n"
"v_cndmask_b32_e64 v45, v45, v255, s[82:83]\n"
"v_cndmask_b32_e64 v46, v46, v255, s[82:83]\n"
"v_cndmask_b32_e64 v47, v47, v255, s[82:83]\n"
".Lmk_b4:\n"
"; @B tail\n"
";\n"
"s_add_u32 s97, s69, 0x2000\n"
"buffer_load_dwordx4 a[32:35], v0, s[8:11], s97 offen sc1\n"
"buffer_load_dwordx4 a[36:39], v0, s[8:11], s97 offen offset:1024 sc1\n"
"buffer_load_dwordx4 a[40:43], v0, s[8:11], s97 offen offset:2048 sc1\n"
"buffer_load_dwordx4 a[44:47], v0, s[8:11], s97 offen offset:3072 sc1\n"
"buffer_load_dwordx4 a[48:51], v1, s[8:11], s97 offen sc1\n"
"v_add_f32_e32 v216, v216, v24\n"
"v_add_f32_e32 v217, v217, v25\n"
"v_add_f32_e32 v218, v218, v26\n"
"v_add_f32_e32 v219, v219, v27\n"
"v_add_f32_e32 v96, v96, v28\n"
"v_add_f32_e32 v97, v97, v29\n"
"v_add_f32_e32 v98, v98, v30\n"
"v_add_f32_e32 v99, v99, v31\n"
"v_mul_f32_e32 v216, v216, v253\n"
"v_mul_f32_e32 v217, v217, v253\n"
"v_mul_f32_e32 v218, v218, v253\n"
"v_mul_f32_e32 v219, v219, v253\n"
"v_mul_f32_e32 v96, v96, v253\n"
"v_mul_f32_e32 v97, v97, v253\n"
"v_mul_f32_e32 v98, v98, v253\n"
"v_mul_f32_e32 v99, v99, v253\n"
"v_cvt_pk_bf16_f32 v24, v216, v217\n"
"v_cvt_pk_bf16_f32 v25, v218, v219\n"
"v_cvt_pk_bf16_f32 v26, v96, v97\n"
"v_cvt_pk_bf16_f32 v27, v98, v99\n"
"ds_write_b128 v2, v[24:27] offset:16384\n"
"buffer_load_dwordx4 a[52:55], v1, s[8:11], s97 offen offset:1024 sc1\n"
"s_waitcnt vmcnt(18)\n"
"v_cmp_ne_u64_e32 vcc, v[250:251], v[18:19]\n"
"s_cbranch_vccnz .Lmk_f46s\n"
".Lmk_f46r:\n"
"s_waitcnt lgkmcnt(0)\n"
"s_barrier\n"
";\n"
"; @B AT a\n"
";\n"
"s_cmp_lg_u32 s0, 0\n"
"s_cbranch_scc1 .Lmk_exit\n"
"ds_read_b128 v[24:27], v2 offset:17408\n"
"ds_read_b128 v[28:31], v15 offset:16384\n"
"v_xor_b32_e32 v255, 32, v15\n"
"ds_read_b128 v[48:51], v255 offset:16640\n"
"ds_read_b128 v[52:55], v2 offset:16384\n"
"ds_read_b128 v[56:59], v17 offset:15360\n"
"v_xor_b32_e32 v255, 32, v17\n"
"ds_read_b128 v[60:63], v255 offset:15616\n"
"ds_read_b64_tr_b16 v[64:65], v16 offset:16384\n"
"v_xor_b32_e32 v255, 32, v16\n"
"ds_read_b64_tr_b16 v[66:67], v255 offset:16640\n"
"ds_read_b64_tr_b16 v[68:69], v255 offset:16384\n"
"ds_read_b64_tr_b16 v[70:71], v16 offset:16640\n"
"ds_read_b64_tr_b16 v[76:77], v16 offset:15360\n"
"v_mov_b32_e32 v72, 0x3f803f80\n"
"v_mov_b32_e32 v73, 0x3f803f80\n"
"v_mov_b32_e32 v74, 0x3f803f80\n"
"v_mov_b32_e32 v75, 0x3f803f80\n"
"v_xor_b32_e32 v255, 32, v16\n"
"ds_read_b64_tr_b16 v[78:79], v255 offset:15616\n"
"ds_read_b64_tr_b16 v[80:81], v255 offset:15360\n"
"ds_read_b64_tr_b16 v[82:83], v16 offset:15616\n"
"v_mov_b32_e32 v84, 0x3f803f80\n"
"v_mov_b32_e32 v85, 0x3f803f80\n"
"v_mov_b32_e32 v86, 0x3f803f80\n"
"v_mov_b32_e32 v87, 0x3f803f80\n"
"s_waitcnt lgkmcnt(11)\n"
"v_mfma_f32_16x16x32_bf16 v[32:35], v[28:31], v[24:27], v[32:35]\n"
"v_mfma_f32_16x16x32_bf16 v[36:39], v[48:51], v[24:27], v[36:39]\n"
"s_waitcnt lgkmcnt(8)\n"
"v_mfma_f32_16x16x32_bf16 v[40:43], v[56:59], v[52:55], v[40:43]\n"
"v_mfma_f32_16x16x32_bf16 v[44:47], v[60:63], v[52:55], v[44:47]\n"
"buffer_load_dwordx4 a[56:59], v1, s[8:11], s97 offen offset:2048 sc1\n"
"buffer_load_dwordx4 a[60:63], v1, s[8:11], s97 offen offset:3072 sc1\n"
"s_add_u32 s98, s69, 0x4000\n"
"buffer_load_dwordx4 a[64:67], v0, s[8:11], s98 offen sc1\n"
"buffer_load_dwordx4 a[68:71], v0, s[8:11], s98 offen offset:1024 sc1\n"
"s_add_u32 s97, s56, 0x440000\n"
"buffer_load_dwordx4 v[24:27], v0, s[4:7], s97 offen sc1\n"
"buffer_load_dwordx4 v[28:31], v0, s[4:7], s97 offen offset:1024 sc1\n"
"buffer_load_dwordx4 v[48:51], v0, s[4:7], s97 offen offset:2048 sc1\n"
"buffer_load_dwordx4 v[52:55], v0, s[4:7], s97 offen offset:3072 sc1\n"
"v_max_f32_e32 v255, v32, v33\n"
"v_max_f32_e32 v254, v40, v41\n"
"v_max3_f32 v255, v255, v34, v35\n"
"v_max3_f32 v254, v254, v42, v43\n"
"v_max3_f32 v255, v255, v36, v37\n"
"v_max3_f32 v254, v254, v44, v45\n"
"v_max3_f32 v255, v255, v38, v39\n"
"v_max3_f32 v254, v254, v46, v47\n"
"v_mov_b32_e32 v253, v255\n"
"v_mov_b32_e32 v252, v254\n"
"s_nop 0\n"
"v_permlane16_swap_b32_e32 v255, v253\n"
"v_permlane16_swap_b32_e32 v254, v252\n"
"v_max_f32_e32 v255, v255, v253\n"
"v_max_f32_e32 v254, v254, v252\n"
"v_mov_b32_e32 v253, v255\n"
"v_mov_b32_e32 v252, v254\n"
"s_nop 0\n"
"v_permlane32_swap_b32_e32 v255, v253\n"
"v_permlane32_swap_b32_e32 v254, v252\n"
"v_max_f32_e32 v255, v255, v253\n"
"v_max_f32_e32 v254, v254, v252\n"
"v_sub_f32_e32 v32, v32, v255\n"
"v_sub_f32_e32 v33, v33, v255\n"
"v_sub_f32_e32 v34, v34, v255\n"
"v_sub_f32_e32 v35, v35, v255\n"
"v_sub_f32_e32 v36, v36, v255\n"
"v_sub_f32_e32 v37, v37, v255\n"
"v_sub_f32_e32 v38, v38, v255\n"
"v_sub_f32_e32 v39, v39, v255\n"
"v_exp_f32_e32 v32, v32\n"
"v_exp_f32_e32 v33, v33\n"
"v_exp_f32_e32 v34, v34\n"
"v_exp_f32_e32 v35, v35\n"
"v_exp_f32_e32 v36, v36\n"
"v_exp_f32_e32 v37, v37\n"
"v_exp_f32_e32 v38, v38\n"
"v_exp_f32_e32 v39, v39\n"
"v_cvt_pk_bf16_f32 v56, v32, v33\n"
"v_cvt_pk_bf16_f32 v57, v34, v35\n"
"v_cvt_pk_bf16_f32 v58, v36, v37\n"
"v_cvt_pk_bf16_f32 v59, v38, v39\n"
"s_waitcnt lgkmcnt(4)\n"
"v_sub_f32_e32 v40, v40, v254\n"
"v_sub_f32_e32 v41, v41, v254\n"
"v_sub_f32_e32 v42, v42, v254\n"
"v_sub_f32_e32 v43, v43, v254\n"
"v_sub_f32_e32 v44, v44, v254\n"
"v_sub_f32_e32 v45, v45, v254\n"
"v_sub_f32_e32 v46, v46, v254\n"
"v_mfma_f32_16x16x32_bf16 v[32:35], v[72:75], v[56:59], 0\n"
"v_sub_f32_e32 v47, v47, v254\n"
"v_exp_f32_e32 v40, v40\n"
"v_exp_f32_e32 v41, v41\n"
"v_exp_f32_e32 v42, v42\n"
"v_mfma_f32_16x16x32_bf16 v[36:39], v[64:67], v[56:59], 0\n"
"v_exp_f32_e32 v43, v43\n"
"v_exp_f32_e32 v44, v44\n"
"v_exp_f32_e32 v45, v45\n"
"v_exp_f32_e32 v46, v46\n"
"v_mfma_f32_16x16x32_bf16 v[60:63], v[68:71], v[56:59], 0\n"
"v_exp_f32_e32 v47, v47\n"
"v_cvt_pk_bf16_f32 v56, v40, v41\n"
"v_cvt_pk_bf16_f32 v57, v42, v43\n"
"v_cvt_pk_bf16_f32 v58, v44, v45\n"
"v_cvt_pk_bf16_f32 v59, v46, v47\n"
"buffer_load_dwordx4 v[40:43], v1, s[4:7], s97 offen sc1\n"
"buffer_load_dwordx4 v[44:47], v1, s[4:7], s97 offen offset:1024 sc1\n"
"buffer_load_dwordx4 v[64:67], v1, s[4:7], s97 offen offset:2048 sc1\n"
"buffer_load_dwordx4 v[68:71], v1, s[4:7], s97 offen offset:3072 sc1\n"
"s_waitcnt lgkmcnt(0)\n"
"v_rcp_f32_e32 v32, v32\n"
"s_nop 0\n"
"v_mul_f32_e32 v36, v36, v32\n"
"v_mul_f32_e32 v37, v37, v32\n"
"v_mul_f32_e32 v38, v38, v32\n"
"v_mfma_f32_16x16x32_bf16 v[72:75], v[84:87], v[56:59], 0\n"
"v_mul_f32_e32 v39, v39, v32\n"
"v_mul_f32_e32 v60, v60, v32\n"
"v_mul_f32_e32 v61, v61, v32\n"
"v_mul_f32_e32 v62, v62, v32\n"
"v_mul_f32_e32 v63, v63, v32\n"
"v_mfma_f32_16x16x32_bf16 v[32:35], v[76:79], v[56:59], 0\n"
"s_add_u32 s97, s60, 0x408000\n"
"v_cvt_pk_bf16_f32 v76, v36, v37\n"
"v_cvt_pk_bf16_f32 v77, v38, v39\n"
"v_cvt_pk_bf16_f32 v78, v60, v61\n"
"v_cvt_pk_bf16_f32 v79, v62, v63\n"
"v_mfma_f32_16x16x32_bf16 v[36:39], v[80:83], v[56:59], 0\n"
"buffer_store_dwordx4 v[76:79], v0, s[4:7], s97 offen sc1\n"
"s_add_u32 s97, s60, 0x400000\n"
"v_rcp_f32_e32 v72, v72\n"
"s_nop 0\n"
"v_mul_f32_e32 v32, v32, v72\n"
"v_mul_f32_e32 v33, v33, v72\n"
"v_mul_f32_e32 v34, v34, v72\n"
"v_mul_f32_e32 v35, v35, v72\n"
"v_mul_f32_e32 v36, v36, v72\n"
"v_mul_f32_e32 v37, v37, v72\n"
"v_mul_f32_e32 v38, v38, v72\n"
"v_mul_f32_e32 v39, v39, v72\n"
"v_cvt_pk_bf16_f32 v56, v32, v33\n"
"v_cvt_pk_bf16_f32 v57, v34, v35\n"
"v_cvt_pk_bf16_f32 v58, v36, v37\n"
"v_cvt_pk_bf16_f32 v59, v38, v39\n"
"buffer_store_dwordx4 v[56:59], v0, s[4:7], s97 offen sc1\n"
";\n"
"s_add_u32 s97, s57, 0x440000\n"
"buffer_load_dwordx4 v[32:35], v0, s[4:7], s97 offen sc1\n"
"buffer_load_dwordx4 v[36:39], v0, s[4:7], s97 offen offset:1024 sc1\n"
"buffer_load_dwordx4 v[56:59], v0, s[4:7], s97 offen offset:2048 sc1\n"
"buffer_load_dwordx4 v[60:63], v0, s[4:7], s97 offen offset:3072 sc1\n"
"buffer_load_dwordx4 v[72:75], v1, s[4:7], s97 offen sc1\n"
"buffer_load_dwordx4 v[76:79], v1, s[4:7], s97 offen offset:1024 sc1\n"
"buffer_load_dwordx4 v[80:83], v1, s[4:7], s97 offen offset:2048 sc1\n"
"buffer_load_dwordx4 v[84:87], v1, s[4:7], s97 offen offset:3072 sc1\n"
"s_add_u32 s97, s58, 0x440000\n"
"buffer_load_dwordx4 v[88:91], v0, s[4:7], s97 offen sc1\n"
"buffer_load_dwordx4 v[92:95], v0, s[4:7], s97 offen offset:1024 sc1\n"
"buffer_load_dwordx4 v[96:99], v0, s[4:7], s97 offen offset:2048 sc1\n"
"buffer_load_dwordx4 v[100:103], v0, s[4:7], s97 offen offset:3072 sc1\n"
"buffer_load_dwordx4 v[104:107], v1, s[4:7], s97 offen sc1\n"
"buffer_load_dwordx4 v[108:111], v1, s[4:7], s97 offen offset:1024 sc1\n"
"buffer_load_dwordx4 v[112:115], v1, s[4:7], s97 offen offset:2048 sc1\n"
"buffer_load_dwordx4 v[116:119], v1, s[4:7], s97 offen offset:3072 sc1\n"
"buffer_load_dwordx4 a[72:75], v0, s[8:11], s98 offen offset:2048 sc1\n"
"buffer_load_dwordx4 a[76:79], v0, s[8:11], s98 offen offset:3072 sc1\n"
"buffer_load_dwordx4 a[80:83], v1, s[8:11], s98 offen sc1\n"
"buffer_load_dwordx4 a[84:87], v1, s[8:11], s98 offen offset:1024 sc1\n"
"buffer_load_dwordx4 a[88:91], v1, s[8:11], s98 offen offset:2048 sc1\n"
"buffer_load_dwordx4 a[92:95], v1, s[8:11], s98 offen offset:3072 sc1\n"
"s_add_u32 s98, s69, 0x6000\n"
"buffer_load_dwordx4 a[96:99], v0, s[8:11], s98 offen sc1\n"
"buffer_load_dwordx4 a[100:103], v0, s[8:11], s98 offen offset:1024 sc1\n"
"s_waitcnt vmcnt(32)\n"
"; @B G2 b.0\n"
"v_mfma_f32_16x16x32_bf16 v[120:123], a[128:131], v[24:27], 0\n"
"v_mfma_f32_16x16x32_bf16 v[120:123], a[132:135], v[28:31], v[120:123]\n"
"s_waitcnt vmcnt(30)\n"
"v_mfma_f32_16x16x32_bf16 v[120:123], a[136:139], v[48:51], v[120:123]\n"
"s_add_u32 s97, s59, 0x440000\n"
"buffer_load_dwordx4 v[124:127], v0, s[4:7], s97 offen sc1\n"
"v_mfma_f32_16x16x32_bf16 v[120:123], a[140:143], v[52:55], v[120:123]\n"
"s_waitcnt vmcnt(29)\n"
"v_mfma_f32_16x16x32_bf16 v[120:123], a[144:147], v[40:43], v[120:123]\n"
"v_mfma_f32_16x16x32_bf16 v[120:123], a[148:151], v[44:47], v[120:123]\n"
"s_waitcnt vmcnt(27)\n"
"buffer_load_dwordx4 v[128:131], v0, s[4:7], s97 offen offset:1024 sc1\n"
"v_mfma_f32_16x16x32_bf16 v[120:123], a[152:155], v[64:67], v[120:123]\n"
"v_mfma_f32_16x16x32_bf16 v[120:123], a[156:159], v[68:71], v[120:123]\n"
"v_mfma_f32_16x16x32_bf16 v[132:135], a[160:163], v[24:27], 0\n"
"buffer_load_dwordx4 v[136:139], v0, s[4:7], s97 offen offset:2048 sc1\n"
"v_mfma_f32_16x16x32_bf16 v[132:135], a[164:167], v[28:31], v[132:135]\n"
"v_mfma_f32_16x16x32_bf16 v[132:135], a[168:171], v[48:51], v[132:135]\n"
"v_mfma_f32_16x16x32_bf16 v[132:135], a[172:175], v[52:55], v[132:135]\n"
"buffer_load_dwordx4 v[140:143], v0, s[4:7], s97 offen offset:3072 sc1\n"
"v_mfma_f32_16x16x32_bf16 v[132:135], a[176:179], v[40:43], v[132:135]\n"
"s_nop 0\n"
"v_cmp_u_f32_e32 vcc, v120, v120\n"
"v_mfma_f32_16x16x32_bf16 v[132:135], a[180:183], v[44:47], v[132:135]\n"
"s_cbranch_vccnz .Lmk_p68s\n"
".Lmk_p68r:\n"
"v_mfma_f32_16x16x32_bf16 v[132:135], a[184:187], v[64:67], v[132:135]\n"
"ds_write_b128 v3, v[120:123]\n"
"v_mfma_f32_16x16x32_bf16 v[132:135], a[188:191], v[68:71], v[132:135]\n"
"buffer_load_dwordx4 v[120:123], v1, s[4:7], s97 offen sc1\n"
"v_mfma_f32_16x16x32_bf16 v[144:147], a[192:195], v[24:27], 0\n"
"v_mfma_f32_16x16x32_bf16 v[144:147], a[196:199], v[28:31], v[144:147]\n"
"v_mfma_f32_16x16x32_bf16 v[144:147], a[200:203], v[48:51], v[144:147]\n"
"buffer_load_dwordx4 v[148:151], v1, s[4:7], s97 offen offset:1024 sc1\n"
"v_mfma_f32_16x16x32_bf16 v[144:147], a[204:207], v[52:55], v[144:147]\n"
"v_mfma_f32_16x16x32_bf16 v[144:147], a[208:211], v[40:43], v[144:147]\n"
"v_mfma_f32_16x16x32_bf16 v[144:147], a[212:215], v[44:47], v[144:147]\n"
"ds_write_b128 v3, v[132:135] offset:1024\n"
"v_mfma_f32_16x16x32_bf16 v[144:147], a[216:219], v[64:67], v[144:147]\n"
"buffer_load_dwordx4 v[132:135], v1, s[4:7], s97 offen offset:2048 sc1\n"
"v_mfma_f32_16x16x32_bf16 v[144:147], a[220:223], v[68:71], v[144:147]\n"
"v_mfma_f32_16x16x32_bf16 v[152:155], a[224:227], v[24:27], 0\n"
"v_mfma_f32_16x16x32_bf16 v[152:155], a[228:231], v[28:31], v[152:155]\n"
"buffer_load_dwordx4 v[24:27], v1, s[4:7], s97 offen offset:3072 sc1\n"
"v_mfma_f32_16x16x32_bf16 v[152:155], a[232:235], v[48:51], v[152:155]\n"
"v_mfma_f32_16x16x32_bf16 v[152:155], a[236:239], v[52:55], v[152:155]\n"
"v_mfma_f32_16x16x32_bf16 v[152:155], a[240:243], v[40:43], v[152:155]\n"
"s_mov_b32 s97, .Lmk_a0-.Lmk_g72\n"
"s_getpc_b64 s[84:85]\n"
".Lmk_g72:\n"
"s_add_u32 s84, s84, s97\n"
"s_addc_u32 s85, s85, 0\n"
"s_and_b32 s84, s84, -16\n"
"v_mfma_f32_16x16x32_bf16 v[152:155], a[244:247], v[44:47], v[152:155]\n"
"ds_write_b128 v3, v[144:147] offset:2048\n"
"v_mfma_f32_16x16x32_bf16 v[152:155], a[248:251], v[64:67], v[152:155]\n"
"s_and_b32 s85, s85, 0xffff\n"
"s_sub_u32 s97, .Lmk_end-.Lmk_g72, s97\n"
"s_min_u32 s86, s97, 0x3000\n"
"s_mov_b32 s87, 0x20000\n"
"v_mfma_f32_16x16x32_bf16 v[152:155], a[252:255], v[68:71], v[152:155]\n"
"s_waitcnt vmcnt(28)\n"
"s_mov_b32 m0, 0x27800\n"
"s_nop 0\n"
"buffer_load_dwordx4 v0, s[84:87], s99 offen lds\n"
"; @B G2 b.1\n"
"v_mfma_f32_16x16x32_bf16 v[28:31], a[128:131], v[32:35], 0\n"
"v_mfma_f32_16x16x32_bf16 v[28:31], a[132:135], v[36:39], v[28:31]\n"
"v_mfma_f32_16x16x32_bf16 v[28:31], a[136:139], v[56:59], v[28:31]\n"
"v_mfma_f32_16x16x32_bf16 v[28:31], a[140:143], v[60:63], v[28:31]\n"
"s_waitcnt vmcnt(25)\n"
"v_mfma_f32_16x16x32_bf16 v[28:31], a[144:147], v[72:75], v[28:31]\n"
"v_mfma_f32_16x16x32_bf16 v[28:31], a[148:151], v[76:79], v[28:31]\n"
"ds_write_b128 v3, v[152:155] offset:3072\n"
"v_mfma_f32_16x16x32_bf16 v[28:31], a[152:155], v[80:83], v[28:31]\n"
"v_mfma_f32_16x16x32_bf16 v[28:31], a[156:159], v[84:87], v[28:31]\n"
"v_mfma_f32_16x16x32_bf16 v[40:43], a[160:163], v[32:35], 0\n"
"s_add_u32 s97, s56, 0x400000\n"
"buffer_load_dwordx4 v[44:47], v0, s[4:7], s97 offen sc1\n"
"v_mfma_f32_16x16x32_bf16 v[40:43], a[164:167], v[36:39], v[40:43]\n"
"v_mfma_f32_16x16x32_bf16 v[40:43], a[168:171], v[56:59], v[40:43]\n"
"v_mfma_f32_16x16x32_bf16 v[40:43], a[172:175], v[60:63], v[40:43]\n"
"buffer_load_dwordx4 v[48:51], v0, s[4:7], s97 offen offset:1024 sc1\n"
"v_mfma_f32_16x16x32_bf16 v[40:43], a[176:179], v[72:75], v[40:43]\n"
"v_cmp_u_f32_e32 vcc, v28, v28\n"
"v_mfma_f32_16x16x32_bf16 v[40:43], a[180:183], v[76:79], v[40:43]\n"
"s_cbranch_vccnz .Lmk_p69s\n"
".Lmk_p69r:\n"
"v_mfma_f32_16x16x32_bf16 v[40:43], a[184:187], v[80:83], v[40:43]\n"
"ds_write_b128 v3, v[28:31] offset:6144\n"
"v_mfma_f32_16x16x32_bf16 v[40:43], a[188:191], v[84:87], v[40:43]\n"
"buffer_load_dwordx4 v[28:31], v0, s[4:7], s97 offen offset:2048 sc1\n"
"v_mfma_f32_16x16x32_bf16 v[52:55], a[192:195], v[32:35], 0\n"
"v_mfma_f32_16x16x32_bf16 v[52:55], a[196:199], v[36:39], v[52:55]\n"
"v_mfma_f32_16x16x32_bf16 v[52:55], a[200:203], v[56:59], v[52:55]\n"
"buffer_load_dwordx4 v[64:67], v0, s[4:7], s97 offen offset:3072 sc1\n"
"v_mfma_f32_16x16x32_bf16 v[52:55], a[204:207], v[60:63], v[52:55]\n"
"v_mfma_f32_16x16x32_bf16 v[52:55], a[208:211], v[72:75], v[52:55]\n"
"ds_read_b128 v[68:71], v2 offset:8192\n"
"ds_read_b128 v[144:147], v2 offset:9216\n"
"v_mfma_f32_16x16x32_bf16 v[52:55], a[212:215], v[76:79], v[52:55]\n"
"ds_write_b128 v3, v[40:43] offset:7168\n"
"v_mfma_f32_16x16x32_bf16 v[52:55], a[216:219], v[80:83], v[52:55]\n"
"buffer_load_dwordx4 v[40:43], v1, s[4:7], s97 offen sc1\n"
"v_mfma_f32_16x16x32_bf16 v[52:55], a[220:223], v[84:87], v[52:55]\n"
"v_mfma_f32_16x16x32_bf16 v[152:155], a[224:227], v[32:35], 0\n"
"v_mfma_f32_16x16x32_bf16 v[152:155], a[228:231], v[36:39], v[152:155]\n"
"buffer_load_dwordx4 v[32:35], v1, s[4:7], s97 offen offset:1024 sc1\n"
"s_waitcnt lgkmcnt(1)\n"
"v_mfma_f32_16x16x32_bf16 v[152:155], a[232:235], v[56:59], v[152:155]\n"
"v_lshlrev_b32_e32 v255, 16, v68\n"
"v_and_b32_e32 v254, 0xffff0000, v68\n"
"v_lshlrev_b32_e32 v253, 16, v69\n"
"v_mfma_f32_16x16x32_bf16 v[152:155], a[236:239], v[60:63], v[152:155]\n"
"v_and_b32_e32 v252, 0xffff0000, v69\n"
"v_lshlrev_b32_e32 v251, 16, v70\n"
"v_and_b32_e32 v250, 0xffff0000, v70\n"
"v_mfma_f32_16x16x32_bf16 v[152:155], a[240:243], v[72:75], v[152:155]\n"
"buffer_load_dwordx4 v[36:39], v1, s[4:7], s97 offen offset:2048 sc1\n"
"v_lshlrev_b32_e32 v249, 16, v71\n"
"v_mfma_f32_16x16x32_bf16 v[152:155], a[244:247], v[76:79], v[152:155]\n"
"ds_write_b128 v3, v[52:55] offset:8192\n"
"v_mfma_f32_16x16x32_bf16 v[152:155], a[248:251], v[80:83], v[152:155]\n"
"v_and_b32_e32 v248, 0xffff0000, v71\n"
"v_lshlrev_b32_e32 v247, 16, v144\n"
"v_and_b32_e32 v246, 0xffff0000, v144\n"
"v_mfma_f32_16x16x32_bf16 v[152:155], a[252:255], v[84:87], v[152:155]\n"
"s_waitcnt vmcnt(28)\n"
"buffer_load_dwordx4 v[52:55], v1, s[4:7], s97 offen offset:3072 sc1\n"
"; @B G2 b.2\n"
"v_mfma_f32_16x16x32_bf16 v[56:59], a[128:131], v[88:91], 0\n"
"s_add_u32 s97, s57, 0x400000\n"
"v_lshlrev_b32_e32 v245, 16, v145\n"
"v_and_b32_e32 v244, 0xffff0000, v145\n"
"v_lshlrev_b32_e32 v243, 16, v146\n"
"v_mfma_f32_16x16x32_bf16 v[56:59], a[132:135], v[92:95], v[56:59]\n"
"v_and_b32_e32 v242, 0xffff0000, v146\n"
"v_lshlrev_b32_e32 v241, 16, v147\n"
"v_and_b32_e32 v240, 0xffff0000, v147\n"
"v_mfma_f32_16x16x32_bf16 v[56:59], a[136:139], v[96:99], v[56:59]\n"
"buffer_load_dwordx4 v[60:63], v0, s[4:7], s97 offen sc1\n"
"v_mfma_f32_16x16x32_bf16 v[56:59], a[140:143], v[100:103], v[56:59]\n"
"s_waitcnt vmcnt(26)\n"
"v_mfma_f32_16x16x32_bf16 v[56:59], a[144:147], v[104:107], v[56:59]\n"
"v_mfma_f32_16x16x32_bf16 v[56:59], a[148:151], v[108:111], v[56:59]\n"
"ds_write_b128 v3, v[152:155] offset:9216\n"
"v_mfma_f32_16x16x32_bf16 v[56:59], a[152:155], v[112:115], v[56:59]\n"
"buffer_load_dwordx4 v[68:71], v0, s[4:7], s97 offen offset:1024 sc1\n"
"v_mfma_f32_16x16x32_bf16 v[56:59], a[156:159], v[116:119], v[56:59]\n"
"v_mfma_f32_16x16x32_bf16 v[72:75], a[160:163], v[88:91], 0\n"
"v_mfma_f32_16x16x32_bf16 v[72:75], a[164:167], v[92:95], v[72:75]\n"
"s_waitcnt lgkmcnt(0)\n"
"s_barrier\n"
"v_mfma_f32_16x16x32_bf16 v[72:75], a[168:171], v[96:99], v[72:75]\n"
"buffer_load_dwordx4 v[76:79], v0, s[4:7], s97 offen offset:2048 sc1\n"
"v_mfma_f32_16x16x32_bf16 v[72:75], a[172:175], v[100:103], v[72:75]\n"
"ds_read_b128 v[80:83], v4\n"
"ds_read_b128 v[84:87], v5\n"
"v_mfma_f32_16x16x32_bf16 v[72:75], a[176:179], v[104:107], v[72:75]\n"
"v_cmp_u_f32_e32 vcc, v56, v56\n"
"v_mfma_f32_16x16x32_bf16 v[72:75], a[180:183], v[108:111], v[72:75]\n"
"s_cbranch_vccnz .Lmk_p70s\n"
".Lmk_p70r:\n"
"v_mfma_f32_16x16x32_bf16 v[72:75], a[184:187], v[112:115], v[72:75]\n"
"ds_write_b128 v3, v[56:59] offset:12288\n"
"v_mfma_f32_16x16x32_bf16 v[72:75], a[188:191], v[116:119], v[72:75]\n"
"buffer_load_dwordx4 v[56:59], v0, s[4:7], s97 offen offset:3072 sc1\n"
"v_mfma_f32_16x16x32_bf16 v[144:147], a[192:195], v[88:91], 0\n"
"ds_read_b128 v[152:155], v4 offset:1024\n"
"ds_read_b128 v[156:159], v5 offset:1024\n"
"s_waitcnt lgkmcnt(3)\n"
"v_mfma_f32_16x16x32_bf16 v[144:147], a[196:199], v[92:95], v[144:147]\n"
"v_add_f32_e32 v80, v80, v255\n"
"v_add_f32_e32 v81, v81, v254\n"
"v_add_f32_e32 v82, v82, v253\n"
"v_mfma_f32_16x16x32_bf16 v[144:147], a[200:203], v[96:99], v[144:147]\n"
"buffer_load_dwordx4 v[160:163], v1, s[4:7], s97 offen sc1\n"
"v_add_f32_e32 v83, v83, v252\n"
"v_mfma_f32_16x16x32_bf16 v[144:147], a[204:207], v[100:103], v[144:147]\n"
"v_add_f32_e32 v80, v80, v84\n"
"v_add_f32_e32 v81, v81, v85\n"
"v_add_f32_e32 v82, v82, v86\n"
"v_mfma_f32_16x16x32_bf16 v[144:147], a[208:211], v[104:107], v[144:147]\n"
"v_add_f32_e32 v83, v83, v87\n"
"v_mfma_f32_16x16x32_bf16 v[144:147], a[212:215], v[108:111], v[144:147]\n"
"ds_write_b128 v3, v[72:75] offset:13312\n"
"v_mfma_f32_16x16x32_bf16 v[144:147], a[216:219], v[112:115], v[144:147]\n"
"buffer_load_dwordx4 v[72:75], v1, s[4:7], s97 offen offset:1024 sc1\n"
"s_waitcnt lgkmcnt(1)\n"
"v_mfma_f32_16x16x32_bf16 v[144:147], a[220:223], v[116:119], v[144:147]\n"
"ds_read_b128 v[84:87], v4 offset:2048\n"
"ds_read_b128 v[164:167], v5 offset:2048\n"
"v_add_f32_e32 v152, v152, v251\n"
"v_mfma_f32_16x16x32_bf16 v[168:171], a[224:227], v[88:91], 0\n"
"v_add_f32_e32 v153, v153, v250\n"
"v_add_f32_e32 v154, v154, v249\n"
"v_add_f32_e32 v155, v155, v248\n"
"v_mfma_f32_16x16x32_bf16 v[168:171], a[228:231], v[92:95], v[168:171]\n"
"buffer_load_dwordx4 v[88:91], v1, s[4:7], s97 offen offset:2048 sc1\n"
"v_add_f32_e32 v152, v152, v156\n"
"v_mfma_f32_16x16x32_bf16 v[168:171], a[232:235], v[96:99], v[168:171]\n"
"v_add_f32_e32 v153, v153, v157\n"
"v_add_f32_e32 v154, v154, v158\n"
"v_add_f32_e32 v155, v155, v159\n"
"v_mfma_f32_16x16x32_bf16 v[168:171], a[236:239], v[100:103], v[168:171]\n"
"v_mfma_f32_16x16x32_bf16 v[168:171], a[240:243], v[104:107], v[168:171]\n"
"buffer_load_dwordx4 v[92:95], v1, s[4:7], s97 offen offset:3072 sc1\n"
"s_waitcnt lgkmcnt(0)\n"
"v_mfma_f32_16x16x32_bf16 v[168:171], a[244:247], v[108:111], v[168:171]\n"
"ds_write_b128 v3, v[144:147] offset:14336\n"
"v_mfma_f32_16x16x32_bf16 v[168:171], a[248:251], v[112:115], v[168:171]\n"
"s_add_u32 s97, s58, 0x400000\n"
"ds_read_b128 v[96:99], v4 offset:3072\n"
"ds_read_b128 v[100:103], v5 offset:3072\n"
"v_add_f32_e32 v84, v84, v247\n"
"v_mfma_f32_16x16x32_bf16 v[168:171], a[252:255], v[116:119], v[168:171]\n"
"s_waitcnt vmcnt(21)\n"
"buffer_load_dwordx4 v[104:107], v0, s[4:7], s97 offen sc1\n"
"; @B G2 b.3\n"
"v_mfma_f32_16x16x32_bf16 v[108:111], a[128:131], v[124:127], v[80:83]\n"
"v_add_f32_e32 v85, v85, v246\n"
"v_add_f32_e32 v86, v86, v245\n"
"v_add_f32_e32 v87, v87, v244\n"
"v_mfma_f32_16x16x32_bf16 v[108:111], a[132:135], v[128:131], v[108:111]\n"
"v_add_f32_e32 v84, v84, v164\n"
"v_add_f32_e32 v85, v85, v165\n"
"v_add_f32_e32 v86, v86, v166\n"
"v_mfma_f32_16x16x32_bf16 v[108:111], a[136:139], v[136:139], v[108:111]\n"
"buffer_load_dwordx4 v[112:115], v0, s[4:7], s97 offen offset:1024 sc1\n"
"v_add_f32_e32 v87, v87, v167\n"
"v_mfma_f32_16x16x32_bf16 v[108:111], a[140:143], v[140:143], v[108:111]\n"
"s_waitcnt vmcnt(19)\n"
"s_waitcnt lgkmcnt(0)\n"
"v_add_f32_e32 v96, v96, v243\n"
"v_add_f32_e32 v97, v97, v242\n"
"v_mfma_f32_16x16x32_bf16 v[108:111], a[144:147], v[120:123], v[108:111]\n"
"v_add_f32_e32 v98, v98, v241\n"
"v_add_f32_e32 v99, v99, v240\n"
"v_add_f32_e32 v96, v96, v100\n"
"v_mfma_f32_16x16x32_bf16 v[108:111], a[148:151], v[148:151], v[108:111]\n"
"ds_write_b128 v3, v[168:171] offset:15360\n"
"v_mfma_f32_16x16x32_bf16 v[108:111], a[152:155], v[132:135], v[108:111]\n"
"buffer_load_dwordx4 v[116:119], v0, s[4:7], s97 offen offset:2048 sc1\n"
"v_add_f32_e32 v97, v97, v101\n"
"v_mfma_f32_16x16x32_bf16 v[108:111], a[156:159], v[24:27], v[108:111]\n"
"v_add_f32_e32 v98, v98, v102\n"
"v_add_f32_e32 v99, v99, v103\n"
"v_mfma_f32_16x16x32_bf16 v[100:103], a[160:163], v[124:127], v[152:155]\n"
"v_mfma_f32_16x16x32_bf16 v[100:103], a[164:167], v[128:131], v[100:103]\n"
"s_waitcnt lgkmcnt(0)\n"
"s_barrier\n"
"v_mfma_f32_16x16x32_bf16 v[100:103], a[168:171], v[136:139], v[100:103]\n"
"buffer_load_dwordx4 v[144:147], v0, s[4:7], s97 offen offset:3072 sc1\n"
"v_mfma_f32_16x16x32_bf16 v[100:103], a[172:175], v[140:143], v[100:103]\n"
"ds_read_b128 v[156:159], v6\n"
"ds_read_b128 v[164:167], v6 offset:1024\n"
"v_mfma_f32_16x16x32_bf16 v[100:103], a[176:179], v[120:123], v[100:103]\n"
"ds_read_b128 v[168:171], v6 offset:2048\n"
"v_cmp_u_f32_e32 vcc, v108, v108\n"
"v_mfma_f32_16x16x32_bf16 v[100:103], a[180:183], v[148:151], v[100:103]\n"
"s_cbranch_vccnz .Lmk_p71s\n"
".Lmk_p71r:\n"
"ds_read_b128 v[80:83], v6 offset:3072\n"
"v_mfma_f32_16x16x32_bf16 v[100:103], a[184:187], v[132:135], v[100:103]\n"
"buffer_load_dwordx4 v[152:155], v1, s[4:7], s97 offen sc1\n"
"v_mfma_f32_16x16x32_bf16 v[100:103], a[188:191], v[24:27], v[100:103]\n"
"v_mfma_f32_16x16x32_bf16 v[84:87], a[192:195], v[124:127], v[84:87]\n"
"v_mfma_f32_16x16x32_bf16 v[84:87], a[196:199], v[128:131], v[84:87]\n"
"buffer_load_dwordx4 v[172:175], v1, s[4:7], s97 offen offset:1024 sc1\n"
"v_mfma_f32_16x16x32_bf16 v[84:87], a[200:203], v[136:139], v[84:87]\n"
"v_mfma_f32_16x16x32_bf16 v[84:87], a[204:207], v[140:143], v[84:87]\n"
"v_mfma_f32_16x16x32_bf16 v[84:87], a[208:211], v[120:123], v[84:87]\n"
"buffer_load_dwordx4 v[176:179], v1, s[4:7], s97 offen offset:2048 sc1\n"
"s_waitcnt lgkmcnt(0)\n"
"v_mfma_f32_16x16x32_bf16 v[84:87], a[212:215], v[148:151], v[84:87]\n"
"v_add_f32_e32 v156, v108, v156\n"
"v_add_f32_e32 v157, v109, v157\n"
"v_add_f32_e32 v158, v110, v158\n"
"v_mfma_f32_16x16x32_bf16 v[84:87], a[216:219], v[132:135], v[84:87]\n"
"v_add_f32_e32 v159, v111, v159\n"
"v_add_f32_e32 v164, v100, v164\n"
"v_add_f32_e32 v165, v101, v165\n"
"v_mfma_f32_16x16x32_bf16 v[84:87], a[220:223], v[24:27], v[84:87]\n"
"buffer_load_dwordx4 v[108:111], v1, s[4:7], s97 offen offset:3072 sc1\n"
"v_add_f32_e32 v166, v102, v166\n"
"v_mfma_f32_16x16x32_bf16 v[96:99], a[224:227], v[124:127], v[96:99]\n"
"v_add_f32_e32 v167, v103, v167\n"
"v_cvt_pk_bf16_f32 v100, v156, v157\n"
"v_cvt_pk_bf16_f32 v101, v158, v159\n"
"v_mfma_f32_16x16x32_bf16 v[96:99], a[228:231], v[128:131], v[96:99]\n"
"s_add_u32 s97, s61, 0x840000\n"
"v_cvt_pk_bf16_f32 v102, v164, v165\n"
"v_cvt_pk_bf16_f32 v103, v166, v167\n"
"v_mfma_f32_16x16x32_bf16 v[96:99], a[232:235], v[136:139], v[96:99]\n"
"buffer_store_dwordx4 v[100:103], v0, s[4:7], s97 offen sc1\n"
"v_mfma_f32_16x16x32_bf16 v[96:99], a[236:239], v[140:143], v[96:99]\n"
"ds_write_b128 v2, v[100:103] offset:8192\n"
"v_mfma_f32_16x16x32_bf16 v[96:99], a[240:243], v[120:123], v[96:99]\n"
"v_mfma_f32_16x16x32_bf16 v[96:99], a[244:247], v[148:151], v[96:99]\n"
"v_mfma_f32_16x16x32_bf16 v[96:99], a[248:251], v[132:135], v[96:99]\n"
"v_mfma_f32_16x16x32_bf16 v[96:99], a[252:255], v[24:27], v[96:99]\n"
"; @B fin b\n"
"s_add_u32 s97, s59, 0x400000\n"
"buffer_load_dwordx4 v[24:27], v0, s[4:7], s97 offen sc1\n"
"buffer_load_dwordx4 v[124:127], v0, s[4:7], s97 offen offset:1024 sc1\n"
"v_mov_b32_e32 v120, 0x3f803f80\n"
"v_mov_b32_e32 v121, 0x3f803f80\n"
"v_mov_b32_e32 v122, 0x3f803f80\n"
"v_mov_b32_e32 v123, 0x3f803f80\n"
"v_add_f32_e32 v168, v84, v168\n"
"v_add_f32_e32 v169, v85, v169\n"
"v_add_f32_e32 v170, v86, v170\n"
"v_add_f32_e32 v171, v87, v171\n"
"v_add_f32_e32 v80, v96, v80\n"
"v_add_f32_e32 v81, v97, v81\n"
"v_add_f32_e32 v82, v98, v82\n"
"v_add_f32_e32 v83, v99, v83\n"
"buffer_load_dwordx4 v[84:87], v0, s[4:7], s97 offen offset:2048 sc1\n"
"s_add_u32 s96, s61, 0x840000\n"
"v_cvt_pk_bf16_f32 v96, v168, v169\n"
"v_cvt_pk_bf16_f32 v97, v170, v171\n"
"v_cvt_pk_bf16_f32 v98, v80, v81\n"
"v_cvt_pk_bf16_f32 v99, v82, v83\n"
"buffer_store_dwordx4 v[96:99], v0, s[4:7], s96 offen offset:1024 sc1\n"
"buffer_load_dwordx4 v[80:83], v0, s[4:7], s97 offen offset:3072 sc1\n"
"ds_write_b128 v2, v[96:99] offset:9216\n"
"buffer_load_dwordx4 v[128:131], v1, s[4:7], s97 offen sc1\n"
"v_mfma_f32_16x16x32_bf16 v[132:135], v[120:123], v[100:103], 0\n"
"v_mfma_f32_16x16x32_bf16 v[136:139], v[100:103], v[100:103], 0\n"
"buffer_load_dwordx4 v[100:103], v1, s[4:7], s97 offen offset:1024 sc1\n"
"v_mfma_f32_16x16x32_bf16 v[132:135], v[120:123], v[96:99], v[132:135]\n"
"v_mfma_f32_16x16x32_bf16 v[136:139], v[96:99], v[96:99], v[136:139]\n"
"s_mov_b32 s94, 0xaaaaaaaa\n"
"buffer_load_dwordx4 v[96:99], v1, s[4:7], s97 offen offset:2048 sc1\n"
"s_mov_b32 s95, 0xaaaaaaaa\n"
"s_nop 4\n"
"v_cndmask_b32_e64 v136, v136, v137, s[94:95]\n"
"v_cndmask_b32_e64 v138, v138, v139, s[94:95]\n"
"s_mov_b32 s94, 0xcccccccc\n"
"s_mov_b32 s95, 0xcccccccc\n"
"buffer_load_dwordx4 v[120:123], v1, s[4:7], s97 offen offset:3072 sc1\n"
"v_cndmask_b32_e64 v133, v136, v138, s[94:95]\n"
"s_mov_b32 s94, 0xf0000f\n"
"s_mov_b32 s95, 0xf0000f00\n"
"s_add_u32 s97, s63, 0x84000\n"
"v_cndmask_b32_e64 v132, v132, v133, s[94:95]\n"
"buffer_store_dword v132, v14, s[4:7], s97 offen sc1\n"
"s_waitcnt vmcnt(33)\n"
"; @B G2 a.0\n"
"v_mfma_f32_16x16x32_bf16 v[132:135], a[128:131], v[44:47], 0\n"
"v_mfma_f32_16x16x32_bf16 v[132:135], a[132:135], v[48:51], v[132:135]\n"
"s_waitcnt vmcnt(31)\n"
"v_mfma_f32_16x16x32_bf16 v[132:135], a[136:139], v[28:31], v[132:135]\n"
"buffer_load_dwordx4 a[104:107], v0, s[8:11], s98 offen offset:2048 sc1\n"
"v_mfma_f32_16x16x32_bf16 v[132:135], a[140:143], v[64:67], v[132:135]\n"
"s_waitcnt vmcnt(30)\n"
"v_mfma_f32_16x16x32_bf16 v[132:135], a[144:147], v[40:43], v[132:135]\n"
"v_mfma_f32_16x16x32_bf16 v[132:135], a[148:151], v[32:35], v[132:135]\n"
"s_waitcnt vmcnt(28)\n"
"buffer_load_dwordx4 a[108:111], v0, s[8:11], s98 offen offset:3072 sc1\n"
"v_mfma_f32_16x16x32_bf16 v[132:135], a[152:155], v[36:39], v[132:135]\n"
"v_mfma_f32_16x16x32_bf16 v[132:135], a[156:159], v[52:55], v[132:135]\n"
"v_mfma_f32_16x16x32_bf16 v[136:139], a[160:163], v[44:47], 0\n"
"buffer_load_dwordx4 a[112:115], v1, s[8:11], s98 offen sc1\n"
"v_mfma_f32_16x16x32_bf16 v[136:139], a[164:167], v[48:51], v[136:139]\n"
"v_mfma_f32_16x16x32_bf16 v[136:139], a[168:171], v[28:31], v[136:139]\n"
"v_mfma_f32_16x16x32_bf16 v[136:139], a[172:175], v[64:67], v[136:139]\n"
"buffer_load_dwordx4 a[116:119], v1, s[8:11], s98 offen offset:1024 sc1\n"
"v_mfma_f32_16x16x32_bf16 v[136:139], a[176:179], v[40:43], v[136:139]\n"
"s_nop 0\n"
"v_cmp_u_f32_e32 vcc, v132, v132\n"
"v_mfma_f32_16x16x32_bf16 v[136:139], a[180:183], v[32:35], v[136:139]\n"
"s_cbranch_vccnz .Lmk_p73s\n"
".Lmk_p73r:\n"
"v_mfma_f32_16x16x32_bf16 v[136:139], a[184:187], v[36:39], v[136:139]\n"
"ds_write_b128 v3, v[132:135]\n"
"v_mfma_f32_16x16x32_bf16 v[136:139], a[188:191], v[52:55], v[136:139]\n"
"buffer_load_dwordx4 a[120:123], v1, s[8:11], s98 offen offset:2048 sc1\n"
"v_mfma_f32_16x16x32_bf16 v[132:135], a[192:195], v[44:47], 0\n"
"v_mfma_f32_16x16x32_bf16 v[132:135], a[196:199], v[48:51], v[132:135]\n"
"v_mfma_f32_16x16x32_bf16 v[132:135], a[200:203], v[28:31], v[132:135]\n"
"buffer_load_dwordx4 a[124:127], v1, s[8:11], s98 offen offset:3072 sc1\n"
"v_mfma_f32_16x16x32_bf16 v[132:135], a[204:207], v[64:67], v[132:135]\n"
"s_add_u32 s98, s69, 0x8000\n"
"v_mfma_f32_16x16x32_bf16 v[132:135], a[208:211], v[40:43], v[132:135]\n"
"v_mfma_f32_16x16x32_bf16 v[132:135], a[212:215], v[32:35], v[132:135]\n"
"ds_write_b128 v3, v[136:139] offset:1024\n"
"v_mfma_f32_16x16x32_bf16 v[132:135], a[216:219], v[36:39], v[132:135]\n"
"buffer_load_dwordx4 v[136:139], v0, s[8:11], s98 offen sc1\n"
"v_mfma_f32_16x16x32_bf16 v[132:135], a[220:223], v[52:55], v[132:135]\n"
"v_mfma_f32_16x16x32_bf16 v[140:143], a[224:227], v[44:47], 0\n"
"v_mfma_f32_16x16x32_bf16 v[140:143], a[228:231], v[48:51], v[140:143]\n"
"buffer_load_dwordx4 v[44:47], v0, s[8:11], s98 offen offset:1024 sc1\n"
"v_mfma_f32_16x16x32_bf16 v[140:143], a[232:235], v[28:31], v[140:143]\n"
"v_mfma_f32_16x16x32_bf16 v[140:143], a[236:239], v[64:67], v[140:143]\n"
"v_mfma_f32_16x16x32_bf16 v[140:143], a[240:243], v[40:43], v[140:143]\n"
"buffer_load_dwordx4 v[28:31], v0, s[8:11], s98 offen offset:2048 sc1\n"
"v_mfma_f32_16x16x32_bf16 v[140:143], a[244:247], v[32:35], v[140:143]\n"
"ds_write_b128 v3, v[132:135] offset:2048\n"
"v_mfma_f32_16x16x32_bf16 v[140:143], a[248:251], v[36:39], v[140:143]\n"
"v_mfma_f32_16x16x32_bf16 v[140:143], a[252:255], v[52:55], v[140:143]\n"
"s_waitcnt vmcnt(32)\n"
"buffer_load_dwordx4 v[32:35], v0, s[8:11], s98 offen offset:3072 sc1\n"
"; @B G2 a.1\n"
"v_mfma_f32_16x16x32_bf16 v[36:39], a[128:131], v[60:63], 0\n"
"v_mfma_f32_16x16x32_bf16 v[36:39], a[132:135], v[68:71], v[36:39]\n"
"v_mfma_f32_16x16x32_bf16 v[36:39], a[136:139], v[76:79], v[36:39]\n"
"buffer_load_dwordx4 v[40:43], v1, s[8:11], s98 offen sc1\n"
"v_mfma_f32_16x16x32_bf16 v[36:39], a[140:143], v[56:59], v[36:39]\n"
"s_waitcnt vmcnt(30)\n"
"v_mfma_f32_16x16x32_bf16 v[36:39], a[144:147], v[160:163], v[36:39]\n"
"v_mfma_f32_16x16x32_bf16 v[36:39], a[148:151], v[72:75], v[36:39]\n"
"ds_write_b128 v3, v[140:143] offset:3072\n"
"v_mfma_f32_16x16x32_bf16 v[36:39], a[152:155], v[88:91], v[36:39]\n"
"buffer_load_dwordx4 v[48:51], v1, s[8:11], s98 offen offset:1024 sc1\n"
"v_mfma_f32_16x16x32_bf16 v[36:39], a[156:159], v[92:95], v[36:39]\n"
"v_mfma_f32_16x16x32_bf16 v[52:55], a[160:163], v[60:63], 0\n"
"v_mfma_f32_16x16x32_bf16 v[52:55], a[164:167], v[68:71], v[52:55]\n"
"buffer_load_dwordx4 v[64:67], v1, s[8:11], s98 offen offset:2048 sc1\n"
"v_mfma_f32_16x16x32_bf16 v[52:55], a[168:171], v[76:79], v[52:55]\n"
"v_mfma_f32_16x16x32_bf16 v[52:55], a[172:175], v[56:59], v[52:55]\n"
"v_mfma_f32_16x16x32_bf16 v[52:55], a[176:179], v[160:163], v[52:55]\n"
"s_nop 1\n"
"v_cmp_u_f32_e32 vcc, v36, v36\n"
"v_mfma_f32_16x16x32_bf16 v[52:55], a[180:183], v[72:75], v[52:55]\n"
"s_cbranch_vccnz .Lmk_p74s\n"
".Lmk_p74r:\n"
"v_mfma_f32_16x16x32_bf16 v[52:55], a[184:187], v[88:91], v[52:55]\n"
"ds_write_b128 v3, v[36:39] offset:6144\n"
"v_mfma_f32_16x16x32_bf16 v[52:55], a[188:191], v[92:95], v[52:55]\n"
"buffer_load_dwordx4 v[36:39], v1, s[8:11], s98 offen offset:3072 sc1\n"
"v_mfma_f32_16x16x32_bf16 v[132:135], a[192:195], v[60:63], 0\n"
"s_add_u32 s98, s69, 0xa000\n"
"v_mfma_f32_16x16x32_bf16 v[132:135], a[196:199], v[68:71], v[132:135]\n"
"v_mfma_f32_16x16x32_bf16 v[132:135], a[200:203], v[76:79], v[132:135]\n"
"buffer_load_dwordx4 v[140:143], v0, s[8:11], s98 offen sc1\n"
"v_mfma_f32_16x16x32_bf16 v[132:135], a[204:207], v[56:59], v[132:135]\n"
"v_mfma_f32_16x16x32_bf16 v[132:135], a[208:211], v[160:163], v[132:135]\n"
"ds_read_b128 v[148:151], v2\n"
"ds_read_b128 v[156:159], v2 offset:1024\n"
"v_mfma_f32_16x16x32_bf16 v[132:135], a[212:215], v[72:75], v[132:135]\n"
"ds_write_b128 v3, v[52:55] offset:7168\n"
"v_mfma_f32_16x16x32_bf16 v[132:135], a[216:219], v[88:91], v[132:135]\n"
"buffer_load_dwordx4 v[52:55], v0, s[8:11], s98 offen offset:1024 sc1\n"
"v_mfma_f32_16x16x32_bf16 v[132:135], a[220:223], v[92:95], v[132:135]\n"
"v_mfma_f32_16x16x32_bf16 v[164:167], a[224:227], v[60:63], 0\n"
"v_mfma_f32_16x16x32_bf16 v[164:167], a[228:231], v[68:71], v[164:167]\n"
"buffer_load_dwordx4 v[60:63], v0, s[8:11], s98 offen offset:2048 sc1\n"
"s_waitcnt lgkmcnt(1)\n"
"v_mfma_f32_16x16x32_bf16 v[164:167], a[232:235], v[76:79], v[164:167]\n"
"v_lshlrev_b32_e32 v255, 16, v148\n"
"v_and_b32_e32 v254, 0xffff0000, v148\n"
"v_lshlrev_b32_e32 v253, 16, v149\n"
"v_mfma_f32_16x16x32_bf16 v[164:167], a[236:239], v[56:59], v[164:167]\n"
"v_and_b32_e32 v252, 0xffff0000, v149\n"
"v_lshlrev_b32_e32 v251, 16, v150\n"
"v_and_b32_e32 v250, 0xffff0000, v150\n"
"v_mfma_f32_16x16x32_bf16 v[164:167], a[240:243], v[160:163], v[164:167]\n"
"buffer_load_dwordx4 v[56:59], v0, s[8:11], s98 offen offset:3072 sc1\n"
"v_lshlrev_b32_e32 v249, 16, v151\n"
"v_mfma_f32_16x16x32_bf16 v[164:167], a[244:247], v[72:75], v[164:167]\n"
"ds_write_b128 v3, v[132:135] offset:8192\n"
"v_mfma_f32_16x16x32_bf16 v[164:167], a[248:251], v[88:91], v[164:167]\n"
"v_and_b32_e32 v248, 0xffff0000, v151\n"
"v_lshlrev_b32_e32 v247, 16, v156\n"
"v_and_b32_e32 v246, 0xffff0000, v156\n"
"v_mfma_f32_16x16x32_bf16 v[164:167], a[252:255], v[92:95], v[164:167]\n"
"s_waitcnt vmcnt(33)\n"
"buffer_load_dwordx4 v[68:71], v1, s[8:11], s98 offen sc1\n"
"; @B G2 a.2\n"
"v_mfma_f32_16x16x32_bf16 v[72:75], a[128:131], v[104:107], 0\n"
"v_lshlrev_b32_e32 v245, 16, v157\n"
"v_and_b32_e32 v244, 0xffff0000, v157\n"
"v_lshlrev_b32_e32 v243, 16, v158\n"
"v_mfma_f32_16x16x32_bf16 v[72:75], a[132:135], v[112:115], v[72:75]\n"
"v_and_b32_e32 v242, 0xffff0000, v158\n"
"v_lshlrev_b32_e32 v241, 16, v159\n"
"v_and_b32_e32 v240, 0xffff0000, v159\n"
"v_mfma_f32_16x16x32_bf16 v[72:75], a[136:139], v[116:119], v[72:75]\n"
"buffer_load_dwordx4 v[76:79], v1, s[8:11], s98 offen offset:1024 sc1\n"
"v_mfma_f32_16x16x32_bf16 v[72:75], a[140:143], v[144:147], v[72:75]\n"
"s_waitcnt vmcnt(31)\n"
"v_mfma_f32_16x16x32_bf16 v[72:75], a[144:147], v[152:155], v[72:75]\n"
"v_mfma_f32_16x16x32_bf16 v[72:75], a[148:151], v[172:175], v[72:75]\n"
"ds_write_b128 v3, v[164:167] offset:9216\n"
"v_mfma_f32_16x16x32_bf16 v[72:75], a[152:155], v[176:179], v[72:75]\n"
"buffer_load_dwordx4 v[88:91], v1, s[8:11], s98 offen offset:2048 sc1\n"
"v_mfma_f32_16x16x32_bf16 v[72:75], a[156:159], v[108:111], v[72:75]\n"
"v_mfma_f32_16x16x32_bf16 v[92:95], a[160:163], v[104:107], 0\n"
"v_mfma_f32_16x16x32_bf16 v[92:95], a[164:167], v[112:115], v[92:95]\n"
"s_waitcnt lgkmcnt(0)\n"
"s_barrier\n"
"v_mfma_f32_16x16x32_bf16 v[92:95], a[168:171], v[116:119], v[92:95]\n"
"buffer_load_dwordx4 v[132:135], v1, s[8:11], s98 offen offset:3072 sc1\n"
"v_mfma_f32_16x16x32_bf16 v[92:95], a[172:175], v[144:147], v[92:95]\n"
"ds_read_b128 v[148:151], v4\n"
"ds_read_b128 v[156:159], v5\n"
"v_mfma_f32_16x16x32_bf16 v[92:95], a[176:179], v[152:155], v[92:95]\n"
"v_cmp_u_f32_e32 vcc, v72, v72\n"
"v_mfma_f32_16x16x32_bf16 v[92:95], a[180:183], v[172:175], v[92:95]\n"
"s_cbranch_vccnz .Lmk_p75s\n"
".Lmk_p75r:\n"
"v_mfma_f32_16x16x32_bf16 v[92:95], a[184:187], v[176:179], v[92:95]\n"
"ds_write_b128 v3, v[72:75] offset:12288\n"
"v_mfma_f32_16x16x32_bf16 v[92:95], a[188:191], v[108:111], v[92:95]\n"
"v_mfma_f32_16x16x32_bf16 v[72:75], a[192:195], v[104:107], 0\n"
"ds_read_b128 v[160:163], v4 offset:1024\n"
"ds_read_b128 v[164:167], v5 offset:1024\n"
"s_waitcnt lgkmcnt(3)\n"
"v_mfma_f32_16x16x32_bf16 v[72:75], a[196:199], v[112:115], v[72:75]\n"
"v_add_f32_e32 v148, v148, v255\n"
"v_add_f32_e32 v149, v149, v254\n"
"v_add_f32_e32 v150, v150, v253\n"
"v_mfma_f32_16x16x32_bf16 v[72:75], a[200:203], v[116:119], v[72:75]\n"
"v_add_f32_e32 v151, v151, v252\n"
"v_add_f32_e32 v148, v148, v156\n"
"v_add_f32_e32 v149, v149, v157\n"
"v_mfma_f32_16x16x32_bf16 v[72:75], a[204:207], v[144:147], v[72:75]\n"
"v_add_f32_e32 v150, v150, v158\n"
"v_add_f32_e32 v151, v151, v159\n"
"v_mfma_f32_16x16x32_bf16 v[72:75], a[208:211], v[152:155], v[72:75]\n"
"s_add_u32 s98, s52, 0x840000\n"
"buffer_load_dwordx4 v[156:159], v0, s[8:11], s98 offen sc1\n"
"v_mfma_f32_16x16x32_bf16 v[72:75], a[212:215], v[172:175], v[72:75]\n"
"ds_write_b128 v3, v[92:95] offset:13312\n"
"v_mfma_f32_16x16x32_bf16 v[72:75], a[216:219], v[176:179], v[72:75]\n"
"buffer_load_dwordx4 v[92:95], v0, s[8:11], s98 offen offset:1024 sc1\n"
"s_waitcnt lgkmcnt(1)\n"
"v_mfma_f32_16x16x32_bf16 v[72:75], a[220:223], v[108:111], v[72:75]\n"
"ds_read_b128 v[168:171], v4 offset:2048\n"
"ds_read_b128 v[180:183], v5 offset:2048\n"
"v_add_f32_e32 v160, v160, v251\n"
"v_mfma_f32_16x16x32_bf16 v[184:187], a[224:227], v[104:107], 0\n"
"buffer_load_dwordx4 v[104:107], v0, s[8:11], s98 offen offset:2048 sc1\n"
"v_add_f32_e32 v161, v161, v250\n"
"v_mfma_f32_16x16x32_bf16 v[184:187], a[228:231], v[112:115], v[184:187]\n"
"v_add_f32_e32 v162, v162, v249\n"
"v_add_f32_e32 v163, v163, v248\n"
"v_add_f32_e32 v160, v160, v164\n"
"v_mfma_f32_16x16x32_bf16 v[184:187], a[232:235], v[116:119], v[184:187]\n"
"buffer_load_dwordx4 v[112:115], v0, s[8:11], s98 offen offset:3072 sc1\n"
"v_add_f32_e32 v161, v161, v165\n"
"v_mfma_f32_16x16x32_bf16 v[184:187], a[236:239], v[144:147], v[184:187]\n"
"v_add_f32_e32 v162, v162, v166\n"
"v_add_f32_e32 v163, v163, v167\n"
"v_mfma_f32_16x16x32_bf16 v[184:187], a[240:243], v[152:155], v[184:187]\n"
"buffer_load_dwordx4 v[116:119], v1, s[8:11], s98 offen sc1\n"
"s_waitcnt lgkmcnt(0)\n"
"v_mfma_f32_16x16x32_bf16 v[184:187], a[244:247], v[172:175], v[184:187]\n"
"ds_write_b128 v3, v[72:75] offset:14336\n"
"v_mfma_f32_16x16x32_bf16 v[184:187], a[248:251], v[176:179], v[184:187]\n"
"buffer_load_dwordx4 v[72:75], v1, s[8:11], s98 offen offset:1024 sc1\n"
"v_add_f32_e32 v168, v168, v247\n"
"v_mfma_f32_16x16x32_bf16 v[184:187], a[252:255], v[108:111], v[184:187]\n"
"s_waitcnt vmcnt(33)\n"
"ds_read_b128 v[108:111], v4 offset:3072\n"
"ds_read_b128 v[144:147], v5 offset:3072\n"
"v_add_f32_e32 v169, v169, v246\n"
"; @B G2 a.3\n"
"v_mfma_f32_16x16x32_bf16 v[152:155], a[128:131], v[24:27], v[148:151]\n"
"buffer_load_dwordx4 v[164:167], v1, s[8:11], s98 offen offset:2048 sc1\n"
"v_add_f32_e32 v170, v170, v245\n"
"v_mfma_f32_16x16x32_bf16 v[152:155], a[132:135], v[124:127], v[152:155]\n"
"v_add_f32_e32 v171, v171, v244\n"
"v_add_f32_e32 v168, v168, v180\n"
"v_add_f32_e32 v169, v169, v181\n"
"v_mfma_f32_16x16x32_bf16 v[152:155], a[136:139], v[84:87], v[152:155]\n"
"buffer_load_dwordx4 v[172:175], v1, s[8:11], s98 offen offset:3072 sc1\n"
"v_add_f32_e32 v170, v170, v182\n"
"v_mfma_f32_16x16x32_bf16 v[152:155], a[140:143], v[80:83], v[152:155]\n"
"s_waitcnt vmcnt(31)\n"
"s_add_u32 s98, s53, 0x840000\n"
"v_add_f32_e32 v171, v171, v183\n"
"v_mfma_f32_16x16x32_bf16 v[152:155], a[144:147], v[128:131], v[152:155]\n"
"buffer_load_dwordx4 v[176:179], v0, s[8:11], s98 offen sc1\n"
"s_waitcnt lgkmcnt(0)\n"
"v_mfma_f32_16x16x32_bf16 v[152:155], a[148:151], v[100:103], v[152:155]\n"
"ds_write_b128 v3, v[184:187] offset:15360\n"
"v_mfma_f32_16x16x32_bf16 v[152:155], a[152:155], v[96:99], v[152:155]\n"
"buffer_load_dwordx4 v[180:183], v0, s[8:11], s98 offen offset:1024 sc1\n"
"v_add_f32_e32 v108, v108, v243\n"
"v_mfma_f32_16x16x32_bf16 v[152:155], a[156:159], v[120:123], v[152:155]\n"
"v_add_f32_e32 v109, v109, v242\n"
"v_add_f32_e32 v110, v110, v241\n"
"v_add_f32_e32 v111, v111, v240\n"
"v_mfma_f32_16x16x32_bf16 v[184:187], a[160:163], v[24:27], v[160:163]\n"
"buffer_load_dwordx4 v[188:191], v0, s[8:11], s98 offen offset:2048 sc1\n"
"v_add_f32_e32 v108, v108, v144\n"
"v_mfma_f32_16x16x32_bf16 v[184:187], a[164:167], v[124:127], v[184:187]\n"
"s_waitcnt lgkmcnt(0)\n"
"s_barrier\n"
"v_add_f32_e32 v109, v109, v145\n"
"v_mfma_f32_16x16x32_bf16 v[184:187], a[168:171], v[84:87], v[184:187]\n"
"buffer_load_dwordx4 v[192:195], v0, s[8:11], s98 offen offset:3072 sc1\n"
"v_add_f32_e32 v110, v110, v146\n"
"v_mfma_f32_16x16x32_bf16 v[184:187], a[172:175], v[80:83], v[184:187]\n"
"v_add_f32_e32 v111, v111, v147\n"
"v_mfma_f32_16x16x32_bf16 v[184:187], a[176:179], v[128:131], v[184:187]\n"
"ds_read_b128 v[144:147], v6\n"
"ds_read_b128 v[196:199], v6 offset:1024\n"
"v_cmp_u_f32_e32 vcc, v152, v152\n"
"v_mfma_f32_16x16x32_bf16 v[184:187], a[180:183], v[100:103], v[184:187]\n"
"s_cbranch_vccnz .Lmk_p76s\n"
".Lmk_p76r:\n"
"ds_read_b128 v[148:151], v6 offset:2048\n"
"v_mfma_f32_16x16x32_bf16 v[184:187], a[184:187], v[96:99], v[184:187]\n"
"buffer_load_dwordx4 v[160:163], v1, s[8:11], s98 offen sc1\n"
"v_mfma_f32_16x16x32_bf16 v[184:187], a[188:191], v[120:123], v[184:187]\n"
"ds_read_b128 v[200:203], v6 offset:3072\n"
"v_mfma_f32_16x16x32_bf16 v[168:171], a[192:195], v[24:27], v[168:171]\n"
"buffer_load_dwordx4 v[204:207], v1, s[8:11], s98 offen offset:1024 sc1\n"
"v_mfma_f32_16x16x32_bf16 v[168:171], a[196:199], v[124:127], v[168:171]\n"
"v_mfma_f32_16x16x32_bf16 v[168:171], a[200:203], v[84:87], v[168:171]\n"
"buffer_load_dwordx4 v[208:211], v1, s[8:11], s98 offen offset:2048 sc1\n"
"v_mfma_f32_16x16x32_bf16 v[168:171], a[204:207], v[80:83], v[168:171]\n"
"v_mfma_f32_16x16x32_bf16 v[168:171], a[208:211], v[128:131], v[168:171]\n"
"buffer_load_dwordx4 v[212:215], v1, s[8:11], s98 offen offset:3072 sc1\n"
"s_waitcnt lgkmcnt(0)\n"
"v_mfma_f32_16x16x32_bf16 v[168:171], a[212:215], v[100:103], v[168:171]\n"
"v_add_f32_e32 v144, v152, v144\n"
"v_add_f32_e32 v145, v153, v145\n"
"v_add_f32_e32 v146, v154, v146\n"
"v_mfma_f32_16x16x32_bf16 v[168:171], a[216:219], v[96:99], v[168:171]\n"
"v_add_f32_e32 v147, v155, v147\n"
"v_add_f32_e32 v196, v184, v196\n"
"v_add_f32_e32 v197, v185, v197\n"
"v_mfma_f32_16x16x32_bf16 v[168:171], a[220:223], v[120:123], v[168:171]\n"
"v_add_f32_e32 v198, v186, v198\n"
"v_add_f32_e32 v199, v187, v199\n"
"v_cvt_pk_bf16_f32 v152, v144, v145\n"
"v_mfma_f32_16x16x32_bf16 v[108:111], a[224:227], v[24:27], v[108:111]\n"
"s_add_u32 s98, s61, 0x800000\n"
"v_cvt_pk_bf16_f32 v153, v146, v147\n"
"v_cvt_pk_bf16_f32 v154, v196, v197\n"
"v_cvt_pk_bf16_f32 v155, v198, v199\n"
"v_mfma_f32_16x16x32_bf16 v[108:111], a[228:231], v[124:127], v[108:111]\n"
"buffer_store_dwordx4 v[152:155], v0, s[4:7], s98 offen sc1\n"
"v_mfma_f32_16x16x32_bf16 v[108:111], a[232:235], v[84:87], v[108:111]\n"
"ds_write_b128 v2, v[152:155]\n"
"v_mfma_f32_16x16x32_bf16 v[108:111], a[236:239], v[80:83], v[108:111]\n"
"v_mfma_f32_16x16x32_bf16 v[108:111], a[240:243], v[128:131], v[108:111]\n"
"v_mfma_f32_16x16x32_bf16 v[108:111], a[244:247], v[100:103], v[108:111]\n"
"v_mfma_f32_16x16x32_bf16 v[108:111], a[248:251], v[96:99], v[108:111]\n"
"v_mfma_f32_16x16x32_bf16 v[108:111], a[252:255], v[120:123], v[108:111]\n"
"s_lshr_b32 s98, s79, 7\n"
"s_add_u32 s52, s52, 0x800000\n"
"s_add_u32 s53, s53, 0x800000\n"
"s_add_u32 s54, s54, 0x800000\n"
"s_add_u32 s55, s55, 0x800000\n"
"s_add_u32 s56, s56, 0x800000\n"
"s_add_u32 s57, s57, 0x800000\n"
"s_add_u32 s58, s58, 0x800000\n"
"s_add_u32 s59, s59, 0x800000\n"
"s_add_u32 s60, s60, 0x800000\n"
"s_add_u32 s61, s61, 0x800000\n"
"s_add_u32 s62, s62, 0x80000\n"
"s_add_u32 s63, s63, 0x80000\n"
"s_add_u32 s64, s64, 0x1000000\n"
"s_add_u32 s65, s65, s79\n"
"s_add_u32 s66, s66, s79\n"
"s_add_u32 s67, s67, 0x20000\n"
"s_add_u32 s68, s68, 0x20000\n"
"s_add_u32 s71, s71, s98\n"
"s_add_u32 s73, s73, s98\n"
"s_mov_b32 s0, 1\n"
"s_mov_b32 s10, 0\n"
".Lmk_a0:\n"
"s_waitcnt vmcnt(13)\n"
"; @A G1 0.0\n"
"v_mfma_f32_16x16x32_bf16 v[24:27], a[0:3], v[156:159], 0\n"
"v_mfma_f32_16x16x32_bf16 v[24:27], a[4:7], v[92:95], v[24:27]\n"
"s_add_u32 s98, s62, 0x4000\n"
"v_mfma_f32_16x16x32_bf16 v[24:27], a[8:11], v[104:107], v[24:27]\n"
"s_add_u32 s97, s54, 0x40000\n"
"buffer_load_dwordx4 v[80:83], v0, s[4:7], s97 offen sc1\n"
"v_mfma_f32_16x16x32_bf16 v[24:27], a[12:15], v[112:115], v[24:27]\n"
"s_waitcnt vmcnt(10)\n"
"v_mfma_f32_16x16x32_bf16 v[24:27], a[16:19], v[116:119], v[24:27]\n"
"buffer_load_dword v255, v10, s[4:7], s98 offen sc1\n"
"v_mfma_f32_16x16x32_bf16 v[24:27], a[20:23], v[72:75], v[24:27]\n"
"v_mov_b32_e32 v84, 0x3f803f80\n"
"v_mov_b32_e32 v85, 0x3f803f80\n"
"v_mfma_f32_16x16x32_bf16 v[24:27], a[24:27], v[164:167], v[24:27]\n"
"v_mov_b32_e32 v86, 0x3f803f80\n"
"v_mov_b32_e32 v87, 0x3f803f80\n"
"v_mfma_f32_16x16x32_bf16 v[24:27], a[28:31], v[172:175], v[24:27]\n"
"buffer_load_dwordx4 v[96:99], v0, s[4:7], s97 offen offset:1024 sc1\n"
"v_mfma_f32_16x16x32_bf16 v[100:103], a[32:35], v[156:159], 0\n"
"buffer_load_dword v254, v10, s[4:7], s98 offen offset:256 sc1\n"
"v_mfma_f32_16x16x32_bf16 v[100:103], a[36:39], v[92:95], v[100:103]\n"
"v_add_f32_e32 v148, v168, v148\n"
"v_add_f32_e32 v149, v169, v149\n"
"v_mfma_f32_16x16x32_bf16 v[100:103], a[40:43], v[104:107], v[100:103]\n"
"v_add_f32_e32 v150, v170, v150\n"
"v_add_f32_e32 v151, v171, v151\n"
"v_mfma_f32_16x16x32_bf16 v[100:103], a[44:47], v[112:115], v[100:103]\n"
"buffer_load_dword v253, v10, s[4:7], s98 offen offset:512 sc1\n"
"v_mfma_f32_16x16x32_bf16 v[100:103], a[48:51], v[116:119], v[100:103]\n"
"v_cmp_u_f32_e32 vcc, v24, v24\n"
"v_add_f32_e32 v200, v108, v200\n"
"v_mfma_f32_16x16x32_bf16 v[100:103], a[52:55], v[72:75], v[100:103]\n"
"s_cbranch_vccnz .Lmk_p81s\n"
".Lmk_p81r:\n"
"v_add_f32_e32 v201, v109, v201\n"
"v_mfma_f32_16x16x32_bf16 v[100:103], a[56:59], v[164:167], v[100:103]\n"
"buffer_load_dwordx4 v[120:123], v0, s[4:7], s97 offen offset:2048 sc1\n"
"v_mfma_f32_16x16x32_bf16 v[100:103], a[60:63], v[172:175], v[100:103]\n"
"ds_write_b128 v3, v[24:27]\n"
"v_mfma_f32_16x16x32_bf16 v[24:27], a[64:67], v[156:159], 0\n"
"buffer_load_dword v252, v10, s[4:7], s98 offen offset:768 sc1\n"
"v_mfma_f32_16x16x32_bf16 v[24:27], a[68:71], v[92:95], v[24:27]\n"
"buffer_load_dwordx4 v[124:127], v0, s[4:7], s97 offen offset:3072 sc1\n"
"v_mfma_f32_16x16x32_bf16 v[24:27], a[72:75], v[104:107], v[24:27]\n"
"v_add_f32_e32 v202, v110, v202\n"
"v_add_f32_e32 v203, v111, v203\n"
"v_mfma_f32_16x16x32_bf16 v[24:27], a[76:79], v[112:115], v[24:27]\n"
"buffer_load_dword v251, v10, s[4:7], s98 offen offset:1024 sc1\n"
"v_mfma_f32_16x16x32_bf16 v[24:27], a[80:83], v[116:119], v[24:27]\n"
"v_cvt_pk_bf16_f32 v108, v148, v149\n"
"v_cvt_pk_bf16_f32 v109, v150, v151\n"
"v_mfma_f32_16x16x32_bf16 v[24:27], a[84:87], v[72:75], v[24:27]\n"
"ds_write_b128 v3, v[100:103] offset:1024\n"
"v_mfma_f32_16x16x32_bf16 v[24:27], a[88:91], v[164:167], v[24:27]\n"
"buffer_load_dwordx4 v[100:103], v1, s[4:7], s97 offen sc1\n"
"v_mfma_f32_16x16x32_bf16 v[24:27], a[92:95], v[172:175], v[24:27]\n"
"buffer_load_dword v250, v10, s[4:7], s98 offen offset:1280 sc1\n"
"v_mfma_f32_16x16x32_bf16 v[128:131], a[96:99], v[156:159], 0\n"
"v_cvt_pk_bf16_f32 v110, v200, v201\n"
"v_cvt_pk_bf16_f32 v111, v202, v203\n"
"v_mfma_f32_16x16x32_bf16 v[128:131], a[100:103], v[92:95], v[128:131]\n"
"buffer_store_dwordx4 v[108:111], v0, s[4:7], s61 offen offset:1024 sc1\n"
"v_mfma_f32_16x16x32_bf16 v[128:131], a[104:107], v[104:107], v[128:131]\n"
"buffer_load_dword v249, v10, s[4:7], s98 offen offset:1536 sc1\n"
"v_mfma_f32_16x16x32_bf16 v[128:131], a[108:111], v[112:115], v[128:131]\n"
"buffer_load_dwordx4 v[144:147], v1, s[4:7], s97 offen offset:1024 sc1\n"
"v_mfma_f32_16x16x32_bf16 v[128:131], a[112:115], v[116:119], v[128:131]\n"
"ds_write_b128 v2, v[108:111] offset:1024\n"
"v_mfma_f32_16x16x32_bf16 v[148:151], v[84:87], v[152:155], 0\n"
"v_mfma_f32_16x16x32_bf16 v[168:171], v[152:155], v[152:155], 0\n"
"v_mfma_f32_16x16x32_bf16 v[148:151], v[84:87], v[108:111], v[148:151]\n"
"v_mfma_f32_16x16x32_bf16 v[168:171], v[108:111], v[108:111], v[168:171]\n"
"v_mfma_f32_16x16x32_bf16 v[128:131], a[116:119], v[72:75], v[128:131]\n"
"ds_write_b128 v3, v[24:27] offset:2048\n"
"v_mfma_f32_16x16x32_bf16 v[128:131], a[120:123], v[164:167], v[128:131]\n"
"s_mov_b32 s94, 0xaaaaaaaa\n"
"v_mov_b32_e32 v24, -1\n"
"v_mov_b32_e32 v25, -1\n"
"v_mfma_f32_16x16x32_bf16 v[128:131], a[124:127], v[172:175], v[128:131]\n"
"v_mov_b32_e32 v26, -1\n"
"v_mov_b32_e32 v27, -1\n"
"v_mfma_f32_16x16x32_bf16 v[84:87], v[136:139], v[156:159], 0\n"
"buffer_load_dwordx4 v[108:111], v1, s[4:7], s97 offen offset:2048 sc1\n"
"v_mfma_f32_16x16x32_bf16 v[84:87], v[44:47], v[92:95], v[84:87]\n"
"s_add_u32 s96, s64, 0x4000\n"
"buffer_store_dwordx4 v[24:27], v2, s[4:7], s96 offen sc1\n"
"v_mfma_f32_16x16x32_bf16 v[84:87], v[28:31], v[104:107], v[84:87]\n"
"buffer_load_dword v248, v10, s[4:7], s98 offen offset:1792 sc1\n"
"v_mfma_f32_16x16x32_bf16 v[84:87], v[32:35], v[112:115], v[84:87]\n"
"s_mov_b32 s95, 0xaaaaaaaa\n"
"v_mfma_f32_16x16x32_bf16 v[84:87], v[40:43], v[116:119], v[84:87]\n"
"v_mfma_f32_16x16x32_bf16 v[84:87], v[48:51], v[72:75], v[84:87]\n"
"buffer_load_dwordx4 v[152:155], v1, s[4:7], s97 offen offset:3072 sc1\n"
"v_mfma_f32_16x16x32_bf16 v[84:87], v[64:67], v[164:167], v[84:87]\n"
"ds_write_b128 v3, v[128:131] offset:3072\n"
"v_mfma_f32_16x16x32_bf16 v[84:87], v[36:39], v[172:175], v[84:87]\n"
"v_cndmask_b32_e64 v168, v168, v169, s[94:95]\n"
"v_cndmask_b32_e64 v170, v170, v171, s[94:95]\n"
"v_mfma_f32_16x16x32_bf16 v[128:131], v[140:143], v[156:159], 0\n"
"s_mov_b32 s94, 0xcccccccc\n"
"s_mov_b32 s95, 0xcccccccc\n"
"v_mfma_f32_16x16x32_bf16 v[128:131], v[52:55], v[92:95], v[128:131]\n"
"v_cndmask_b32_e64 v149, v168, v170, s[94:95]\n"
"v_mfma_f32_16x16x32_bf16 v[128:131], v[60:63], v[104:107], v[128:131]\n"
"s_mov_b32 s94, 0xf0000f\n"
"s_mov_b32 s95, 0xf0000f00\n"
"v_mfma_f32_16x16x32_bf16 v[128:131], v[56:59], v[112:115], v[128:131]\n"
"v_cndmask_b32_e64 v148, v148, v149, s[94:95]\n"
"v_mfma_f32_16x16x32_bf16 v[128:131], v[68:71], v[116:119], v[128:131]\n"
"buffer_store_dword v148, v14, s[4:7], s63 offen sc1\n"
"v_mfma_f32_16x16x32_bf16 v[128:131], v[76:79], v[72:75], v[128:131]\n"
"ds_write_b128 v3, v[84:87] offset:4096\n"
"v_mfma_f32_16x16x32_bf16 v[128:131], v[88:91], v[164:167], v[128:131]\n"
"v_mfma_f32_16x16x32_bf16 v[128:131], v[132:135], v[172:175], v[128:131]\n"
"s_waitcnt vmcnt(24)\n"
"; @A G1 0.1\n"
"v_mfma_f32_16x16x32_bf16 v[72:75], a[0:3], v[176:179], 0\n"
"v_mfma_f32_16x16x32_bf16 v[72:75], a[4:7], v[180:183], v[72:75]\n"
"v_mfma_f32_16x16x32_bf16 v[72:75], a[8:11], v[188:191], v[72:75]\n"
"s_add_u32 s98, s55, 0x40000\n"
"buffer_load_dwordx4 v[84:87], v0, s[4:7], s98 offen sc1\n"
"v_mfma_f32_16x16x32_bf16 v[72:75], a[12:15], v[192:195], v[72:75]\n"
"s_waitcnt vmcnt(21)\n"
"v_mfma_f32_16x16x32_bf16 v[72:75], a[16:19], v[160:163], v[72:75]\n"
"v_mfma_f32_16x16x32_bf16 v[72:75], a[20:23], v[204:207], v[72:75]\n"
"ds_write_b128 v3, v[128:131] offset:5120\n"
"v_mfma_f32_16x16x32_bf16 v[72:75], a[24:27], v[208:211], v[72:75]\n"
"v_mfma_f32_16x16x32_bf16 v[72:75], a[28:31], v[212:215], v[72:75]\n"
"buffer_load_dwordx4 v[92:95], v0, s[4:7], s98 offen offset:1024 sc1\n"
"v_mfma_f32_16x16x32_bf16 v[104:107], a[32:35], v[176:179], 0\n"
"v_mfma_f32_16x16x32_bf16 v[104:107], a[36:39], v[180:183], v[104:107]\n"
"v_mfma_f32_16x16x32_bf16 v[104:107], a[40:43], v[188:191], v[104:107]\n"
"v_mfma_f32_16x16x32_bf16 v[104:107], a[44:47], v[192:195], v[104:107]\n"
"v_mfma_f32_16x16x32_bf16 v[104:107], a[48:51], v[160:163], v[104:107]\n"
"s_nop 1\n"
"v_cmp_u_f32_e32 vcc, v72, v72\n"
"v_mfma_f32_16x16x32_bf16 v[104:107], a[52:55], v[204:207], v[104:107]\n"
"s_cbranch_vccnz .Lmk_p82s\n"
".Lmk_p82r:\n"
"v_mfma_f32_16x16x32_bf16 v[104:107], a[56:59], v[208:211], v[104:107]\n"
"buffer_load_dwordx4 v[112:115], v0, s[4:7], s98 offen offset:2048 sc1\n"
"v_mfma_f32_16x16x32_bf16 v[104:107], a[60:63], v[212:215], v[104:107]\n"
"ds_write_b128 v3, v[72:75] offset:6144\n"
"v_mfma_f32_16x16x32_bf16 v[72:75], a[64:67], v[176:179], 0\n"
"v_mfma_f32_16x16x32_bf16 v[72:75], a[68:71], v[180:183], v[72:75]\n"
"buffer_load_dwordx4 v[116:119], v0, s[4:7], s98 offen offset:3072 sc1\n"
"v_mfma_f32_16x16x32_bf16 v[72:75], a[72:75], v[188:191], v[72:75]\n"
"v_mfma_f32_16x16x32_bf16 v[72:75], a[76:79], v[192:195], v[72:75]\n"
"v_mfma_f32_16x16x32_bf16 v[72:75], a[80:83], v[160:163], v[72:75]\n"
"v_mfma_f32_16x16x32_bf16 v[72:75], a[84:87], v[204:207], v[72:75]\n"
"ds_write_b128 v3, v[104:107] offset:7168\n"
"v_mfma_f32_16x16x32_bf16 v[72:75], a[88:91], v[208:211], v[72:75]\n"
"buffer_load_dwordx4 v[104:107], v1, s[4:7], s98 offen sc1\n"
"v_mfma_f32_16x16x32_bf16 v[72:75], a[92:95], v[212:215], v[72:75]\n"
"v_mfma_f32_16x16x32_bf16 v[128:131], a[96:99], v[176:179], 0\n"
"v_mfma_f32_16x16x32_bf16 v[128:131], a[100:103], v[180:183], v[128:131]\n"
"v_mfma_f32_16x16x32_bf16 v[128:131], a[104:107], v[188:191], v[128:131]\n"
"v_mfma_f32_16x16x32_bf16 v[128:131], a[108:111], v[192:195], v[128:131]\n"
"buffer_load_dwordx4 v[148:151], v1, s[4:7], s98 offen offset:1024 sc1\n"
"v_mfma_f32_16x16x32_bf16 v[128:131], a[112:115], v[160:163], v[128:131]\n"
"v_mfma_f32_16x16x32_bf16 v[128:131], a[116:119], v[204:207], v[128:131]\n"
"s_nop 0\n"
"ds_write_b128 v3, v[72:75] offset:8192\n"
"v_mfma_f32_16x16x32_bf16 v[128:131], a[120:123], v[208:211], v[128:131]\n"
"v_mfma_f32_16x16x32_bf16 v[128:131], a[124:127], v[212:215], v[128:131]\n"
"v_mfma_f32_16x16x32_bf16 v[72:75], v[136:139], v[176:179], 0\n"
"buffer_load_dwordx4 v[156:159], v1, s[4:7], s98 offen offset:2048 sc1\n"
"v_mfma_f32_16x16x32_bf16 v[72:75], v[44:47], v[180:183], v[72:75]\n"
"s_add_u32 s97, s64, 0x4000\n"
"v_mfma_f32_16x16x32_bf16 v[72:75], v[28:31], v[188:191], v[72:75]\n"
"v_mfma_f32_16x16x32_bf16 v[72:75], v[32:35], v[192:195], v[72:75]\n"
"buffer_store_dwordx4 v[24:27], v2, s[4:7], s97 offen offset:1024 sc1\n"
"v_mfma_f32_16x16x32_bf16 v[72:75], v[40:43], v[160:163], v[72:75]\n"
"s_waitcnt vmcnt(10)\n"
"v_add_f32_e32 v255, v255, v251\n"
"v_add_f32_e32 v254, v254, v250\n"
"v_mfma_f32_16x16x32_bf16 v[72:75], v[48:51], v[204:207], v[72:75]\n"
"buffer_load_dwordx4 v[164:167], v1, s[4:7], s98 offen offset:3072 sc1\n"
"v_mfma_f32_16x16x32_bf16 v[72:75], v[64:67], v[208:211], v[72:75]\n"
"ds_write_b128 v3, v[128:131] offset:9216\n"
"v_mfma_f32_16x16x32_bf16 v[72:75], v[36:39], v[212:215], v[72:75]\n"
"v_add_f32_e32 v253, v253, v249\n"
"v_add_f32_e32 v252, v252, v248\n"
"v_mfma_f32_16x16x32_bf16 v[128:131], v[140:143], v[176:179], 0\n"
"v_add_f32_e32 v255, v255, v253\n"
"v_add_f32_e32 v254, v254, v252\n"
"v_mfma_f32_16x16x32_bf16 v[128:131], v[52:55], v[180:183], v[128:131]\n"
"v_add_f32_e32 v255, v255, v254\n"
"v_cmp_u_f32_e32 vcc, v255, v255\n"
"v_mfma_f32_16x16x32_bf16 v[128:131], v[60:63], v[188:191], v[128:131]\n"
"s_cbranch_vccnz .Lmk_m85s\n"
".Lmk_m85r:\n"
"v_mov_b32_e32 v254, v255\n"
"v_mfma_f32_16x16x32_bf16 v[128:131], v[56:59], v[192:195], v[128:131]\n"
"s_nop 0\n"
"v_permlane32_swap_b32_e32 v255, v254\n"
"v_add_f32_e32 v255, v255, v254\n"
"v_mfma_f32_16x16x32_bf16 v[128:131], v[68:71], v[160:163], v[128:131]\n"
"v_mul_f32_e32 v255, 0x3a800000, v255\n"
"v_mov_b32_e32 v254, v255\n"
"v_mfma_f32_16x16x32_bf16 v[128:131], v[76:79], v[204:207], v[128:131]\n"
"ds_write_b128 v3, v[72:75] offset:10240\n"
"v_mfma_f32_16x16x32_bf16 v[128:131], v[88:91], v[208:211], v[128:131]\n"
"v_permlane16_swap_b32_e32 v255, v254\n"
"v_cvt_pk_bf16_f32 v253, -v255, 0\n"
"v_mfma_f32_16x16x32_bf16 v[128:131], v[132:135], v[212:215], v[128:131]\n"
"v_fma_f32 v254, -v255, v255, v254\n"
"v_lshlrev_b32_e32 v253, 16, v253\n"
"; @A G1 0.2\n"
"v_mfma_f32_16x16x32_bf16 v[72:75], a[0:3], v[80:83], 0\n"
"v_max_f32_e32 v254, 0, v254\n"
"v_cvt_pk_bf16_f32 v254, v254, 0\n"
"v_mfma_f32_16x16x32_bf16 v[72:75], a[4:7], v[96:99], v[72:75]\n"
"v_lshlrev_b32_e32 v254, 16, v254\n"
"v_add_f32_e32 v254, 0x3727c5ac, v254\n"
"v_mfma_f32_16x16x32_bf16 v[72:75], a[8:11], v[120:123], v[72:75]\n"
"v_cvt_pk_bf16_f32 v254, v254, 0\n"
"v_lshlrev_b32_e32 v254, 16, v254\n"
"v_mfma_f32_16x16x32_bf16 v[72:75], a[12:15], v[124:127], v[72:75]\n"
"s_waitcnt vmcnt(10)\n"
"v_sqrt_f32_e32 v254, v254\n"
"v_mfma_f32_16x16x32_bf16 v[72:75], a[16:19], v[100:103], v[72:75]\n"
"s_mov_b32 s98, .Lmk_a2-.Lmk_g80\n"
"s_getpc_b64 s[84:85]\n"
".Lmk_g80:\n"
"s_add_u32 s84, s84, s98\n"
"s_addc_u32 s85, s85, 0\n"
"s_and_b32 s84, s84, -16\n"
"v_mfma_f32_16x16x32_bf16 v[72:75], a[20:23], v[144:147], v[72:75]\n"
"ds_write_b128 v3, v[128:131] offset:11264\n"
"v_mfma_f32_16x16x32_bf16 v[72:75], a[24:27], v[108:111], v[72:75]\n"
"s_and_b32 s85, s85, 0xffff\n"
"s_sub_u32 s98, .Lmk_end-.Lmk_g80, s98\n"
"s_min_u32 s86, s98, 0x3000\n"
"s_mov_b32 s87, 0x20000\n"
"v_mfma_f32_16x16x32_bf16 v[72:75], a[28:31], v[152:155], v[72:75]\n"
"s_mov_b32 m0, 0x27800\n"
"s_nop 0\n"
"buffer_load_dwordx4 v0, s[84:87], s99 offen lds\n"
"v_mfma_f32_16x16x32_bf16 v[128:131], a[32:35], v[80:83], 0\n"
"v_cvt_pk_bf16_f32 v254, v254, 0\n"
"v_lshlrev_b32_e32 v254, 16, v254\n"
"v_mfma_f32_16x16x32_bf16 v[128:131], a[36:39], v[96:99], v[128:131]\n"
"v_rcp_f32_e32 v255, v254\n"
"v_mfma_f32_16x16x32_bf16 v[128:131], a[40:43], v[120:123], v[128:131]\n"
"s_add_u32 s98, s52, 0x48000\n"
"buffer_load_dwordx4 v[160:163], v0, s[4:7], s98 offen sc1\n"
"v_mfma_f32_16x16x32_bf16 v[128:131], a[44:47], v[124:127], v[128:131]\n"
"s_waitcnt lgkmcnt(0)\n"
"s_barrier\n"
"v_mfma_f32_16x16x32_bf16 v[128:131], a[48:51], v[100:103], v[128:131]\n"
"v_cmp_u_f32_e32 vcc, v72, v72\n"
"v_mul_f32_e32 v254, 0x3e8293ee, v255\n"
"v_mfma_f32_16x16x32_bf16 v[128:131], a[52:55], v[144:147], v[128:131]\n"
"s_cbranch_vccnz .Lmk_p83s\n"
".Lmk_p83r:\n"
"ds_read_b128 v[168:171], v4\n"
"v_mfma_f32_16x16x32_bf16 v[128:131], a[56:59], v[108:111], v[128:131]\n"
"ds_write_b128 v3, v[72:75] offset:12288\n"
"v_mfma_f32_16x16x32_bf16 v[128:131], a[60:63], v[152:155], v[128:131]\n"
"buffer_load_dwordx4 v[72:75], v0, s[4:7], s98 offen offset:1024 sc1\n"
"v_mfma_f32_16x16x32_bf16 v[172:175], a[64:67], v[80:83], 0\n"
"ds_read_b128 v[176:179], v9 offset:128\n"
"ds_read_b128 v[180:183], v5\n"
"v_mfma_f32_16x16x32_bf16 v[172:175], a[68:71], v[96:99], v[172:175]\n"
"v_mfma_f32_16x16x32_bf16 v[172:175], a[72:75], v[120:123], v[172:175]\n"
"v_mfma_f32_16x16x32_bf16 v[172:175], a[76:79], v[124:127], v[172:175]\n"
"ds_read_b128 v[184:187], v4 offset:1024\n"
"ds_read_b128 v[188:191], v9 offset:192\n"
"v_mfma_f32_16x16x32_bf16 v[172:175], a[80:83], v[100:103], v[172:175]\n"
"buffer_load_dwordx4 v[192:195], v0, s[4:7], s98 offen offset:2048 sc1\n"
"v_mfma_f32_16x16x32_bf16 v[172:175], a[84:87], v[144:147], v[172:175]\n"
"ds_write_b128 v3, v[128:131] offset:13312\n"
"v_mfma_f32_16x16x32_bf16 v[172:175], a[88:91], v[108:111], v[172:175]\n"
"ds_read_b128 v[128:131], v5 offset:1024\n"
"s_waitcnt lgkmcnt(4)\n"
"v_mfma_f32_16x16x32_bf16 v[172:175], a[92:95], v[152:155], v[172:175]\n"
"v_fmac_f32_e32 v168, v253, v176\n"
"v_fmac_f32_e32 v169, v253, v177\n"
"v_mfma_f32_16x16x32_bf16 v[196:199], a[96:99], v[80:83], 0\n"
"v_fmac_f32_e32 v170, v253, v178\n"
"v_fmac_f32_e32 v171, v253, v179\n"
"v_mfma_f32_16x16x32_bf16 v[196:199], a[100:103], v[96:99], v[196:199]\n"
"buffer_load_dwordx4 v[176:179], v0, s[4:7], s98 offen offset:3072 sc1\n"
"v_mfma_f32_16x16x32_bf16 v[196:199], a[104:107], v[120:123], v[196:199]\n"
"ds_read_b128 v[200:203], v4 offset:2048\n"
"ds_read_b128 v[204:207], v9 offset:256\n"
"v_mfma_f32_16x16x32_bf16 v[196:199], a[108:111], v[124:127], v[196:199]\n"
"ds_read_b128 v[208:211], v5 offset:2048\n"
"v_add_f32_e32 v168, v168, v180\n"
"v_mfma_f32_16x16x32_bf16 v[196:199], a[112:115], v[100:103], v[196:199]\n"
"v_add_f32_e32 v169, v169, v181\n"
"v_add_f32_e32 v170, v170, v182\n"
"v_mfma_f32_16x16x32_bf16 v[196:199], a[116:119], v[144:147], v[196:199]\n"
"ds_write_b128 v3, v[172:175] offset:14336\n"
"v_mfma_f32_16x16x32_bf16 v[196:199], a[120:123], v[108:111], v[196:199]\n"
"buffer_load_dwordx4 v[172:175], v1, s[4:7], s98 offen sc1\n"
"v_mfma_f32_16x16x32_bf16 v[196:199], a[124:127], v[152:155], v[196:199]\n"
"s_waitcnt lgkmcnt(4)\n"
"v_add_f32_e32 v171, v171, v183\n"
"v_fmac_f32_e32 v184, v253, v188\n"
"v_mfma_f32_16x16x32_bf16 v[180:183], v[136:139], v[80:83], 0\n"
"v_fmac_f32_e32 v185, v253, v189\n"
"v_fmac_f32_e32 v186, v253, v190\n"
"v_mfma_f32_16x16x32_bf16 v[180:183], v[44:47], v[96:99], v[180:183]\n"
"ds_read_b128 v[212:215], v4 offset:3072\n"
"ds_read_b128 v[216:219], v9 offset:320\n"
"v_mfma_f32_16x16x32_bf16 v[180:183], v[28:31], v[120:123], v[180:183]\n"
"ds_read_b128 v[220:223], v5 offset:3072\n"
"v_fmac_f32_e32 v187, v253, v191\n"
"v_mfma_f32_16x16x32_bf16 v[180:183], v[32:35], v[124:127], v[180:183]\n"
"buffer_load_dwordx4 v[188:191], v1, s[4:7], s98 offen offset:1024 sc1\n"
"v_mfma_f32_16x16x32_bf16 v[180:183], v[40:43], v[100:103], v[180:183]\n"
"s_add_u32 s97, s64, 0x8000\n"
"v_add_f32_e32 v184, v184, v128\n"
"v_add_f32_e32 v185, v185, v129\n"
"v_mfma_f32_16x16x32_bf16 v[180:183], v[48:51], v[144:147], v[180:183]\n"
"ds_write_b128 v3, v[196:199] offset:15360\n"
"v_mfma_f32_16x16x32_bf16 v[180:183], v[64:67], v[108:111], v[180:183]\n"
"buffer_store_dwordx4 v[24:27], v2, s[4:7], s97 offen sc1\n"
"v_mfma_f32_16x16x32_bf16 v[180:183], v[36:39], v[152:155], v[180:183]\n"
"s_waitcnt lgkmcnt(5)\n"
"v_add_f32_e32 v186, v186, v130\n"
"v_add_f32_e32 v187, v187, v131\n"
"v_mfma_f32_16x16x32_bf16 v[128:131], v[140:143], v[80:83], 0\n"
"buffer_load_dwordx4 v[80:83], v1, s[4:7], s98 offen offset:2048 sc1\n"
"v_mfma_f32_16x16x32_bf16 v[128:131], v[52:55], v[96:99], v[128:131]\n"
"s_add_u32 s97, s68, s80\n"
"s_waitcnt vmcnt(18)\n"
"ds_read_b128 v[96:99], v4 offset:4096\n"
"v_mfma_f32_16x16x32_bf16 v[128:131], v[60:63], v[120:123], v[128:131]\n"
"buffer_store_dwordx2 v[18:19], v11, s[4:7], s97 offen sc1\n"
"v_mfma_f32_16x16x32_bf16 v[128:131], v[56:59], v[124:127], v[128:131]\n"
"ds_read_b128 v[120:123], v9\n"
"ds_read_b128 v[124:127], v5 offset:4096\n"
"v_mfma_f32_16x16x32_bf16 v[128:131], v[68:71], v[100:103], v[128:131]\n"
"v_fmac_f32_e32 v200, v253, v204\n"
"v_fmac_f32_e32 v201, v253, v205\n"
"v_mfma_f32_16x16x32_bf16 v[128:131], v[76:79], v[144:147], v[128:131]\n"
"buffer_load_dwordx4 v[100:103], v1, s[4:7], s98 offen offset:3072 sc1\n"
"v_mfma_f32_16x16x32_bf16 v[128:131], v[88:91], v[108:111], v[128:131]\n"
"ds_write_b128 v3, v[180:183] offset:16384\n"
"v_mfma_f32_16x16x32_bf16 v[128:131], v[132:135], v[152:155], v[128:131]\n"
"s_waitcnt vmcnt(16)\n"
"ds_read_b128 v[108:111], v4 offset:5120\n"
"ds_read_b128 v[144:147], v9 offset:64\n"
"; @A G1 0.3\n"
"v_mfma_f32_16x16x32_bf16 v[152:155], a[0:3], v[84:87], v[168:171]\n"
"ds_read_b128 v[180:183], v5 offset:5120\n"
"v_fmac_f32_e32 v202, v253, v206\n"
"v_mfma_f32_16x16x32_bf16 v[152:155], a[4:7], v[92:95], v[152:155]\n"
"v_fmac_f32_e32 v203, v253, v207\n"
"v_add_f32_e32 v200, v200, v208\n"
"v_mfma_f32_16x16x32_bf16 v[152:155], a[8:11], v[112:115], v[152:155]\n"
"s_add_u32 s98, s53, 0x48000\n"
"buffer_load_dwordx4 v[196:199], v0, s[4:7], s98 offen sc1\n"
"v_mfma_f32_16x16x32_bf16 v[152:155], a[12:15], v[116:119], v[152:155]\n"
"s_waitcnt vmcnt(12)\n"
"v_add_f32_e32 v201, v201, v209\n"
"v_add_f32_e32 v202, v202, v210\n"
"v_mfma_f32_16x16x32_bf16 v[152:155], a[16:19], v[104:107], v[152:155]\n"
"s_waitcnt lgkmcnt(8)\n"
"v_add_f32_e32 v203, v203, v211\n"
"v_fmac_f32_e32 v212, v253, v216\n"
"v_mfma_f32_16x16x32_bf16 v[152:155], a[20:23], v[148:151], v[152:155]\n"
"ds_write_b128 v3, v[128:131] offset:17408\n"
"v_mfma_f32_16x16x32_bf16 v[152:155], a[24:27], v[156:159], v[152:155]\n"
"v_fmac_f32_e32 v213, v253, v217\n"
"v_fmac_f32_e32 v214, v253, v218\n"
"v_mfma_f32_16x16x32_bf16 v[152:155], a[28:31], v[164:167], v[152:155]\n"
"buffer_load_dwordx4 v[128:131], v0, s[4:7], s98 offen offset:1024 sc1\n"
"v_mfma_f32_16x16x32_bf16 v[204:207], a[32:35], v[84:87], v[184:187]\n"
"v_fmac_f32_e32 v215, v253, v219\n"
"v_add_f32_e32 v212, v212, v220\n"
"v_mfma_f32_16x16x32_bf16 v[204:207], a[36:39], v[92:95], v[204:207]\n"
"v_add_f32_e32 v213, v213, v221\n"
"v_add_f32_e32 v214, v214, v222\n"
"v_mfma_f32_16x16x32_bf16 v[204:207], a[40:43], v[112:115], v[204:207]\n"
"s_waitcnt lgkmcnt(0)\n"
"s_barrier\n"
"v_mfma_f32_16x16x32_bf16 v[204:207], a[44:47], v[116:119], v[204:207]\n"
"ds_read_b128 v[208:211], v6\n"
"ds_read_b128 v[216:219], v6 offset:1024\n"
"v_mfma_f32_16x16x32_bf16 v[204:207], a[48:51], v[104:107], v[204:207]\n"
"v_cmp_u_f32_e32 vcc, v152, v152\n"
"v_add_f32_e32 v215, v215, v223\n"
"v_mfma_f32_16x16x32_bf16 v[204:207], a[52:55], v[148:151], v[204:207]\n"
"s_cbranch_vccnz .Lmk_p84s\n"
".Lmk_p84r:\n"
"v_fmac_f32_e32 v96, v253, v120\n"
"v_mfma_f32_16x16x32_bf16 v[204:207], a[56:59], v[156:159], v[204:207]\n"
"buffer_load_dwordx4 v[168:171], v0, s[4:7], s98 offen offset:2048 sc1\n"
"v_mfma_f32_16x16x32_bf16 v[204:207], a[60:63], v[164:167], v[204:207]\n"
"v_fmac_f32_e32 v97, v253, v121\n"
"v_fmac_f32_e32 v98, v253, v122\n"
"v_mfma_f32_16x16x32_bf16 v[200:203], a[64:67], v[84:87], v[200:203]\n"
"v_fmac_f32_e32 v99, v253, v123\n"
"v_add_f32_e32 v96, v96, v124\n"
"v_mfma_f32_16x16x32_bf16 v[200:203], a[68:71], v[92:95], v[200:203]\n"
"buffer_load_dwordx4 v[120:123], v0, s[4:7], s98 offen offset:3072 sc1\n"
"v_mfma_f32_16x16x32_bf16 v[200:203], a[72:75], v[112:115], v[200:203]\n"
"v_add_f32_e32 v97, v97, v125\n"
"v_add_f32_e32 v98, v98, v126\n"
"v_mfma_f32_16x16x32_bf16 v[200:203], a[76:79], v[116:119], v[200:203]\n"
"v_add_f32_e32 v99, v99, v127\n"
"v_fmac_f32_e32 v108, v253, v144\n"
"v_mfma_f32_16x16x32_bf16 v[200:203], a[80:83], v[104:107], v[200:203]\n"
"s_waitcnt lgkmcnt(0)\n"
"v_fmac_f32_e32 v109, v253, v145\n"
"v_fmac_f32_e32 v110, v253, v146\n"
"v_mfma_f32_16x16x32_bf16 v[200:203], a[84:87], v[148:151], v[200:203]\n"
"v_fmac_f32_e32 v111, v253, v147\n"
"v_add_f32_e32 v108, v108, v180\n"
"v_mfma_f32_16x16x32_bf16 v[200:203], a[88:91], v[156:159], v[200:203]\n"
"buffer_load_dwordx4 v[124:127], v1, s[4:7], s98 offen sc1\n"
"v_mfma_f32_16x16x32_bf16 v[200:203], a[92:95], v[164:167], v[200:203]\n"
"ds_read_b128 v[144:147], v6 offset:2048\n"
"ds_read_b128 v[184:187], v6 offset:3072\n"
"v_mfma_f32_16x16x32_bf16 v[212:215], a[96:99], v[84:87], v[212:215]\n"
"v_add_f32_e32 v109, v109, v181\n"
"v_add_f32_e32 v110, v110, v182\n"
"v_mfma_f32_16x16x32_bf16 v[212:215], a[100:103], v[92:95], v[212:215]\n"
"v_add_f32_e32 v111, v111, v183\n"
"v_add_f32_e32 v152, v152, v208\n"
"v_mfma_f32_16x16x32_bf16 v[212:215], a[104:107], v[112:115], v[212:215]\n"
"v_add_f32_e32 v153, v153, v209\n"
"v_add_f32_e32 v154, v154, v210\n"
"v_mfma_f32_16x16x32_bf16 v[212:215], a[108:111], v[116:119], v[212:215]\n"
"buffer_load_dwordx4 v[180:183], v1, s[4:7], s98 offen offset:1024 sc1\n"
"v_mfma_f32_16x16x32_bf16 v[212:215], a[112:115], v[104:107], v[212:215]\n"
"v_add_f32_e32 v155, v155, v211\n"
"v_add_f32_e32 v204, v204, v216\n"
"v_mfma_f32_16x16x32_bf16 v[212:215], a[116:119], v[148:151], v[212:215]\n"
"v_add_f32_e32 v205, v205, v217\n"
"v_add_f32_e32 v206, v206, v218\n"
"v_mfma_f32_16x16x32_bf16 v[212:215], a[120:123], v[156:159], v[212:215]\n"
"v_add_f32_e32 v207, v207, v219\n"
"v_mul_f32_e32 v152, v152, v255\n"
"v_mfma_f32_16x16x32_bf16 v[212:215], a[124:127], v[164:167], v[212:215]\n"
"v_mul_f32_e32 v153, v153, v255\n"
"v_mul_f32_e32 v154, v154, v255\n"
"v_mfma_f32_16x16x32_bf16 v[96:99], v[136:139], v[84:87], v[96:99]\n"
"buffer_load_dwordx4 v[208:211], v1, s[4:7], s98 offen offset:2048 sc1\n"
"v_mfma_f32_16x16x32_bf16 v[96:99], v[44:47], v[92:95], v[96:99]\n"
"v_mul_f32_e32 v155, v155, v255\n"
"v_mul_f32_e32 v204, v204, v255\n"
"v_mfma_f32_16x16x32_bf16 v[96:99], v[28:31], v[112:115], v[96:99]\n"
"v_mul_f32_e32 v205, v205, v255\n"
"v_mul_f32_e32 v206, v206, v255\n"
"v_mfma_f32_16x16x32_bf16 v[96:99], v[32:35], v[116:119], v[96:99]\n"
"v_mul_f32_e32 v207, v207, v255\n"
"v_cvt_pk_bf16_f32 v216, v152, v153\n"
"v_mfma_f32_16x16x32_bf16 v[96:99], v[40:43], v[104:107], v[96:99]\n"
"s_waitcnt lgkmcnt(0)\n"
"v_cvt_pk_bf16_f32 v217, v154, v155\n"
"v_cvt_pk_bf16_f32 v218, v204, v205\n"
"v_mfma_f32_16x16x32_bf16 v[96:99], v[48:51], v[148:151], v[96:99]\n"
"buffer_load_dwordx4 v[152:155], v1, s[4:7], s98 offen offset:3072 sc1\n"
"v_mfma_f32_16x16x32_bf16 v[96:99], v[64:67], v[156:159], v[96:99]\n"
"s_add_u32 s98, s64, 0x8000\n"
"ds_read_b128 v[220:223], v6 offset:4096\n"
"ds_read_b128 v[224:227], v6 offset:5120\n"
"v_mfma_f32_16x16x32_bf16 v[96:99], v[36:39], v[164:167], v[96:99]\n"
"v_cvt_pk_bf16_f32 v219, v206, v207\n"
"v_add_f32_e32 v200, v200, v144\n"
"v_mfma_f32_16x16x32_bf16 v[108:111], v[140:143], v[84:87], v[108:111]\n"
"buffer_store_dwordx4 v[24:27], v2, s[4:7], s98 offen offset:1024 sc1\n"
"v_mfma_f32_16x16x32_bf16 v[108:111], v[52:55], v[92:95], v[108:111]\n"
"ds_write_b128 v7, v[216:219] offset:24576\n"
"v_mfma_f32_16x16x32_bf16 v[108:111], v[60:63], v[112:115], v[108:111]\n"
"v_add_f32_e32 v201, v201, v145\n"
"v_add_f32_e32 v202, v202, v146\n"
"v_mfma_f32_16x16x32_bf16 v[108:111], v[56:59], v[116:119], v[108:111]\n"
"v_add_f32_e32 v203, v203, v147\n"
"v_add_f32_e32 v212, v212, v184\n"
"v_mfma_f32_16x16x32_bf16 v[108:111], v[68:71], v[104:107], v[108:111]\n"
"v_add_f32_e32 v213, v213, v185\n"
"v_add_f32_e32 v214, v214, v186\n"
"v_mfma_f32_16x16x32_bf16 v[108:111], v[76:79], v[148:151], v[108:111]\n"
"v_add_f32_e32 v215, v215, v187\n"
"v_mul_f32_e32 v200, v200, v255\n"
"v_mfma_f32_16x16x32_bf16 v[108:111], v[88:91], v[156:159], v[108:111]\n"
"v_mul_f32_e32 v201, v201, v255\n"
"v_mul_f32_e32 v202, v202, v255\n"
"v_mfma_f32_16x16x32_bf16 v[108:111], v[132:135], v[164:167], v[108:111]\n"
"v_mul_f32_e32 v203, v203, v255\n"
"v_mul_f32_e32 v212, v212, v255\n"
"v_mul_f32_e32 v213, v213, v255\n"
"v_mul_f32_e32 v214, v214, v255\n"
"v_mul_f32_e32 v215, v215, v255\n"
"v_cvt_pk_bf16_f32 v24, v200, v201\n"
"v_cvt_pk_bf16_f32 v25, v202, v203\n"
"v_cvt_pk_bf16_f32 v26, v212, v213\n"
"v_cvt_pk_bf16_f32 v27, v214, v215\n"
"ds_write_b128 v7, v[24:27] offset:57344\n"
".Lmk_a1:\n"
"s_waitcnt vmcnt(15)\n"
"; @A G1 1.0\n"
"v_mfma_f32_16x16x32_bf16 v[84:87], a[0:3], v[160:163], 0\n"
"v_mfma_f32_16x16x32_bf16 v[84:87], a[4:7], v[72:75], v[84:87]\n"
"s_add_u32 s98, s62, 0x4800\n"
"v_mfma_f32_16x16x32_bf16 v[84:87], a[8:11], v[192:195], v[84:87]\n"
"s_add_u32 s97, s54, 0x48000\n"
"buffer_load_dwordx4 v[92:95], v0, s[4:7], s97 offen sc1\n"
"v_mfma_f32_16x16x32_bf16 v[84:87], a[12:15], v[176:179], v[84:87]\n"
"s_waitcnt vmcnt(10)\n"
"v_mfma_f32_16x16x32_bf16 v[84:87], a[16:19], v[172:175], v[84:87]\n"
"buffer_load_dword v255, v10, s[4:7], s98 offen sc1\n"
"v_mfma_f32_16x16x32_bf16 v[84:87], a[20:23], v[188:191], v[84:87]\n"
"s_waitcnt lgkmcnt(2)\n"
"v_add_f32_e32 v96, v96, v220\n"
"v_add_f32_e32 v97, v97, v221\n"
"v_mfma_f32_16x16x32_bf16 v[84:87], a[24:27], v[80:83], v[84:87]\n"
"v_add_f32_e32 v98, v98, v222\n"
"v_add_f32_e32 v99, v99, v223\n"
"v_mfma_f32_16x16x32_bf16 v[84:87], a[28:31], v[100:103], v[84:87]\n"
"buffer_load_dwordx4 v[104:107], v0, s[4:7], s97 offen offset:1024 sc1\n"
"v_mfma_f32_16x16x32_bf16 v[112:115], a[32:35], v[160:163], 0\n"
"s_add_u32 s96, s70, 0\n"
"buffer_load_dwordx4 a[128:131], v0, s[4:7], s96 offen sc1\n"
"v_mfma_f32_16x16x32_bf16 v[112:115], a[36:39], v[72:75], v[112:115]\n"
"buffer_load_dword v253, v10, s[4:7], s98 offen offset:256 sc1\n"
"v_mfma_f32_16x16x32_bf16 v[112:115], a[40:43], v[192:195], v[112:115]\n"
"v_add_f32_e32 v108, v108, v224\n"
"v_add_f32_e32 v109, v109, v225\n"
"v_mfma_f32_16x16x32_bf16 v[112:115], a[44:47], v[176:179], v[112:115]\n"
"v_add_f32_e32 v110, v110, v226\n"
"v_add_f32_e32 v111, v111, v227\n"
"v_mfma_f32_16x16x32_bf16 v[112:115], a[48:51], v[172:175], v[112:115]\n"
"v_cmp_u_f32_e32 vcc, v84, v84\n"
"v_mul_f32_e32 v96, v96, v254\n"
"v_mfma_f32_16x16x32_bf16 v[112:115], a[52:55], v[188:191], v[112:115]\n"
"s_cbranch_vccnz .Lmk_p86s\n"
".Lmk_p86r:\n"
"v_mul_f32_e32 v97, v97, v254\n"
"v_mfma_f32_16x16x32_bf16 v[112:115], a[56:59], v[80:83], v[112:115]\n"
"buffer_load_dwordx4 v[116:119], v0, s[4:7], s97 offen offset:2048 sc1\n"
"v_mfma_f32_16x16x32_bf16 v[112:115], a[60:63], v[100:103], v[112:115]\n"
"ds_write_b128 v3, v[84:87]\n"
"v_mfma_f32_16x16x32_bf16 v[84:87], a[64:67], v[160:163], 0\n"
"buffer_load_dword v252, v10, s[4:7], s98 offen offset:512 sc1\n"
"v_mfma_f32_16x16x32_bf16 v[84:87], a[68:71], v[72:75], v[84:87]\n"
"buffer_load_dwordx4 v[144:147], v0, s[4:7], s97 offen offset:3072 sc1\n"
"v_mfma_f32_16x16x32_bf16 v[84:87], a[72:75], v[192:195], v[84:87]\n"
"v_mul_f32_e32 v98, v98, v254\n"
"v_mul_f32_e32 v99, v99, v254\n"
"v_mfma_f32_16x16x32_bf16 v[84:87], a[76:79], v[176:179], v[84:87]\n"
"buffer_load_dword v251, v10, s[4:7], s98 offen offset:768 sc1\n"
"v_mfma_f32_16x16x32_bf16 v[84:87], a[80:83], v[172:175], v[84:87]\n"
"v_mul_f32_e32 v108, v108, v254\n"
"v_mul_f32_e32 v109, v109, v254\n"
"v_mfma_f32_16x16x32_bf16 v[84:87], a[84:87], v[188:191], v[84:87]\n"
"ds_write_b128 v3, v[112:115] offset:1024\n"
"v_mfma_f32_16x16x32_bf16 v[84:87], a[88:91], v[80:83], v[84:87]\n"
"buffer_load_dwordx4 v[112:115], v1, s[4:7], s97 offen sc1\n"
"v_mfma_f32_16x16x32_bf16 v[84:87], a[92:95], v[100:103], v[84:87]\n"
"buffer_load_dword v250, v10, s[4:7], s98 offen offset:1024 sc1\n"
"v_mfma_f32_16x16x32_bf16 v[148:151], a[96:99], v[160:163], 0\n"
"v_mul_f32_e32 v110, v110, v254\n"
"v_mul_f32_e32 v111, v111, v254\n"
"v_mfma_f32_16x16x32_bf16 v[148:151], a[100:103], v[72:75], v[148:151]\n"
"v_cvt_pk_bf16_f32 v156, v96, v97\n"
"v_cvt_pk_bf16_f32 v157, v98, v99\n"
"v_mfma_f32_16x16x32_bf16 v[148:151], a[104:107], v[192:195], v[148:151]\n"
"buffer_load_dword v254, v10, s[4:7], s98 offen offset:1280 sc1\n"
"v_mfma_f32_16x16x32_bf16 v[148:151], a[108:111], v[176:179], v[148:151]\n"
"buffer_load_dwordx4 v[96:99], v1, s[4:7], s97 offen offset:1024 sc1\n"
"v_mfma_f32_16x16x32_bf16 v[148:151], a[112:115], v[172:175], v[148:151]\n"
"buffer_load_dwordx4 a[132:135], v0, s[4:7], s96 offen offset:1024 sc1\n"
"v_mfma_f32_16x16x32_bf16 v[148:151], a[116:119], v[188:191], v[148:151]\n"
"ds_write_b128 v3, v[84:87] offset:2048\n"
"v_mfma_f32_16x16x32_bf16 v[148:151], a[120:123], v[80:83], v[148:151]\n"
"buffer_load_dword v249, v10, s[4:7], s98 offen offset:1536 sc1\n"
"v_mfma_f32_16x16x32_bf16 v[148:151], a[124:127], v[100:103], v[148:151]\n"
"v_cvt_pk_bf16_f32 v158, v108, v109\n"
"v_cvt_pk_bf16_f32 v159, v110, v111\n"
"v_mfma_f32_16x16x32_bf16 v[84:87], v[136:139], v[160:163], 0\n"
"buffer_load_dwordx4 v[108:111], v1, s[4:7], s97 offen offset:2048 sc1\n"
"v_mfma_f32_16x16x32_bf16 v[84:87], v[44:47], v[72:75], v[84:87]\n"
"buffer_load_dword v248, v10, s[4:7], s98 offen offset:1792 sc1\n"
"v_mfma_f32_16x16x32_bf16 v[84:87], v[28:31], v[192:195], v[84:87]\n"
"ds_write_b128 v2, v[156:159] offset:49152\n"
"v_mfma_f32_16x16x32_bf16 v[84:87], v[32:35], v[176:179], v[84:87]\n"
"v_mfma_f32_16x16x32_bf16 v[84:87], v[40:43], v[172:175], v[84:87]\n"
"v_mfma_f32_16x16x32_bf16 v[84:87], v[48:51], v[188:191], v[84:87]\n"
"buffer_load_dwordx4 v[156:159], v1, s[4:7], s97 offen offset:3072 sc1\n"
"v_mfma_f32_16x16x32_bf16 v[84:87], v[64:67], v[80:83], v[84:87]\n"
"ds_write_b128 v3, v[148:151] offset:3072\n"
"v_mfma_f32_16x16x32_bf16 v[84:87], v[36:39], v[100:103], v[84:87]\n"
"v_mfma_f32_16x16x32_bf16 v[148:151], v[140:143], v[160:163], 0\n"
"v_mfma_f32_16x16x32_bf16 v[148:151], v[52:55], v[72:75], v[148:151]\n"
"v_mfma_f32_16x16x32_bf16 v[148:151], v[60:63], v[192:195], v[148:151]\n"
"v_mfma_f32_16x16x32_bf16 v[148:151], v[56:59], v[176:179], v[148:151]\n"
"v_mfma_f32_16x16x32_bf16 v[148:151], v[68:71], v[172:175], v[148:151]\n"
"v_mfma_f32_16x16x32_bf16 v[148:151], v[76:79], v[188:191], v[148:151]\n"
"s_nop 1\n"
"ds_write_b128 v3, v[84:87] offset:4096\n"
"v_mfma_f32_16x16x32_bf16 v[148:151], v[88:91], v[80:83], v[148:151]\n"
"v_mfma_f32_16x16x32_bf16 v[148:151], v[132:135], v[100:103], v[148:151]\n"
"s_waitcnt vmcnt(23)\n"
"; @A G1 1.1\n"
"v_mfma_f32_16x16x32_bf16 v[72:75], a[0:3], v[196:199], 0\n"
"buffer_load_dwordx4 a[136:139], v0, s[4:7], s96 offen offset:2048 sc1\n"
"v_mfma_f32_16x16x32_bf16 v[72:75], a[4:7], v[128:131], v[72:75]\n"
"v_mfma_f32_16x16x32_bf16 v[72:75], a[8:11], v[168:171], v[72:75]\n"
"s_add_u32 s98, s55, 0x48000\n"
"buffer_load_dwordx4 v[80:83], v0, s[4:7], s98 offen sc1\n"
"v_mfma_f32_16x16x32_bf16 v[72:75], a[12:15], v[120:123], v[72:75]\n"
"s_waitcnt vmcnt(21)\n"
"v_mfma_f32_16x16x32_bf16 v[72:75], a[16:19], v[124:127], v[72:75]\n"
"v_mfma_f32_16x16x32_bf16 v[72:75], a[20:23], v[180:183], v[72:75]\n"
"ds_write_b128 v3, v[148:151] offset:5120\n"
"v_mfma_f32_16x16x32_bf16 v[72:75], a[24:27], v[208:211], v[72:75]\n"
"v_mfma_f32_16x16x32_bf16 v[72:75], a[28:31], v[152:155], v[72:75]\n"
"buffer_load_dwordx4 v[84:87], v0, s[4:7], s98 offen offset:1024 sc1\n"
"v_mfma_f32_16x16x32_bf16 v[100:103], a[32:35], v[196:199], 0\n"
"v_mfma_f32_16x16x32_bf16 v[100:103], a[36:39], v[128:131], v[100:103]\n"
"v_mfma_f32_16x16x32_bf16 v[100:103], a[40:43], v[168:171], v[100:103]\n"
"v_mfma_f32_16x16x32_bf16 v[100:103], a[44:47], v[120:123], v[100:103]\n"
"v_mfma_f32_16x16x32_bf16 v[100:103], a[48:51], v[124:127], v[100:103]\n"
"s_add_u32 s97, s67, 0\n"
"s_nop 0\n"
"v_cmp_u_f32_e32 vcc, v72, v72\n"
"v_mfma_f32_16x16x32_bf16 v[100:103], a[52:55], v[180:183], v[100:103]\n"
"s_cbranch_vccnz .Lmk_p87s\n"
".Lmk_p87r:\n"
"v_mfma_f32_16x16x32_bf16 v[100:103], a[56:59], v[208:211], v[100:103]\n"
"buffer_load_dwordx4 v[148:151], v0, s[4:7], s98 offen offset:2048 sc1\n"
"s_add_u32 s95, s64, 0x2000\n"
"v_mfma_f32_16x16x32_bf16 v[100:103], a[60:63], v[152:155], v[100:103]\n"
"ds_write_b128 v3, v[72:75] offset:6144\n"
"v_mfma_f32_16x16x32_bf16 v[72:75], a[64:67], v[196:199], 0\n"
"buffer_load_dwordx2 v[246:247], v12, s[4:7], s97 offen sc1\n"
"v_mfma_f32_16x16x32_bf16 v[72:75], a[68:71], v[128:131], v[72:75]\n"
"buffer_load_dwordx4 v[160:163], v0, s[4:7], s98 offen offset:3072 sc1\n"
"v_mfma_f32_16x16x32_bf16 v[72:75], a[72:75], v[168:171], v[72:75]\n"
"buffer_store_dwordx4 v[216:219], v7, s[4:7], s95 offen sc1\n"
"v_mfma_f32_16x16x32_bf16 v[72:75], a[76:79], v[120:123], v[72:75]\n"
"v_mfma_f32_16x16x32_bf16 v[72:75], a[80:83], v[124:127], v[72:75]\n"
"buffer_load_dwordx4 a[140:143], v0, s[4:7], s96 offen offset:3072 sc1\n"
"v_mfma_f32_16x16x32_bf16 v[72:75], a[84:87], v[180:183], v[72:75]\n"
"ds_write_b128 v3, v[100:103] offset:7168\n"
"v_mfma_f32_16x16x32_bf16 v[72:75], a[88:91], v[208:211], v[72:75]\n"
"buffer_load_dwordx4 v[100:103], v1, s[4:7], s98 offen sc1\n"
"v_mfma_f32_16x16x32_bf16 v[72:75], a[92:95], v[152:155], v[72:75]\n"
"v_mfma_f32_16x16x32_bf16 v[164:167], a[96:99], v[196:199], 0\n"
"v_mfma_f32_16x16x32_bf16 v[164:167], a[100:103], v[128:131], v[164:167]\n"
"v_mfma_f32_16x16x32_bf16 v[164:167], a[104:107], v[168:171], v[164:167]\n"
"v_mfma_f32_16x16x32_bf16 v[164:167], a[108:111], v[120:123], v[164:167]\n"
"buffer_load_dwordx4 v[172:175], v1, s[4:7], s98 offen offset:1024 sc1\n"
"v_mfma_f32_16x16x32_bf16 v[164:167], a[112:115], v[124:127], v[164:167]\n"
"v_mfma_f32_16x16x32_bf16 v[164:167], a[116:119], v[180:183], v[164:167]\n"
"s_nop 0\n"
"ds_write_b128 v3, v[72:75] offset:8192\n"
"v_mfma_f32_16x16x32_bf16 v[164:167], a[120:123], v[208:211], v[164:167]\n"
"v_mfma_f32_16x16x32_bf16 v[164:167], a[124:127], v[152:155], v[164:167]\n"
"v_mfma_f32_16x16x32_bf16 v[72:75], v[136:139], v[196:199], 0\n"
"buffer_load_dwordx4 v[176:179], v1, s[4:7], s98 offen offset:2048 sc1\n"
"v_mfma_f32_16x16x32_bf16 v[72:75], v[44:47], v[128:131], v[72:75]\n"
"v_mfma_f32_16x16x32_bf16 v[72:75], v[28:31], v[168:171], v[72:75]\n"
"v_mfma_f32_16x16x32_bf16 v[72:75], v[32:35], v[120:123], v[72:75]\n"
"v_mfma_f32_16x16x32_bf16 v[72:75], v[40:43], v[124:127], v[72:75]\n"
"s_waitcnt vmcnt(12)\n"
"v_add_f32_e32 v255, v255, v250\n"
"v_add_f32_e32 v253, v253, v254\n"
"v_mfma_f32_16x16x32_bf16 v[72:75], v[48:51], v[180:183], v[72:75]\n"
"buffer_load_dwordx4 v[184:187], v1, s[4:7], s98 offen offset:3072 sc1\n"
"v_mfma_f32_16x16x32_bf16 v[72:75], v[64:67], v[208:211], v[72:75]\n"
"ds_write_b128 v3, v[164:167] offset:9216\n"
"v_mfma_f32_16x16x32_bf16 v[72:75], v[36:39], v[152:155], v[72:75]\n"
"v_add_f32_e32 v252, v252, v249\n"
"v_add_f32_e32 v251, v251, v248\n"
"v_mfma_f32_16x16x32_bf16 v[164:167], v[140:143], v[196:199], 0\n"
"buffer_load_dwordx4 a[144:147], v1, s[4:7], s96 offen sc1\n"
"s_add_u32 s98, s64, 0x6000\n"
"v_mfma_f32_16x16x32_bf16 v[164:167], v[52:55], v[128:131], v[164:167]\n"
"v_add_f32_e32 v255, v255, v252\n"
"v_add_f32_e32 v253, v253, v251\n"
"v_mfma_f32_16x16x32_bf16 v[164:167], v[60:63], v[168:171], v[164:167]\n"
"buffer_store_dwordx4 v[24:27], v7, s[4:7], s98 offen sc1\n"
"v_mfma_f32_16x16x32_bf16 v[164:167], v[56:59], v[120:123], v[164:167]\n"
"v_add_f32_e32 v255, v255, v253\n"
"v_cmp_u_f32_e32 vcc, v255, v255\n"
"v_mfma_f32_16x16x32_bf16 v[164:167], v[68:71], v[124:127], v[164:167]\n"
"s_cbranch_vccnz .Lmk_m90s\n"
".Lmk_m90r:\n"
"v_mov_b32_e32 v253, v255\n"
"v_mfma_f32_16x16x32_bf16 v[164:167], v[76:79], v[180:183], v[164:167]\n"
"ds_write_b128 v3, v[72:75] offset:10240\n"
"v_mfma_f32_16x16x32_bf16 v[164:167], v[88:91], v[208:211], v[164:167]\n"
"v_permlane32_swap_b32_e32 v255, v253\n"
"v_add_f32_e32 v255, v255, v253\n"
"v_mfma_f32_16x16x32_bf16 v[164:167], v[132:135], v[152:155], v[164:167]\n"
"v_mul_f32_e32 v255, 0x3a800000, v255\n"
"v_mov_b32_e32 v253, v255\n"
"; @A G1 1.2\n"
"v_mfma_f32_16x16x32_bf16 v[24:27], a[0:3], v[92:95], 0\n"
"s_nop 0\n"
"v_permlane16_swap_b32_e32 v255, v253\n"
"v_cvt_pk_bf16_f32 v254, -v255, 0\n"
"v_mfma_f32_16x16x32_bf16 v[24:27], a[4:7], v[104:107], v[24:27]\n"
"s_waitcnt vmcnt(9)\n"
"v_cmp_ne_u64_e32 vcc, v[246:247], v[18:19]\n"
"v_mfma_f32_16x16x32_bf16 v[24:27], a[8:11], v[116:119], v[24:27]\n"
"s_cbranch_vccnz .Lmk_f77s\n"
".Lmk_f77r:\n"
"v_fma_f32 v253, -v255, v255, v253\n"
"v_mfma_f32_16x16x32_bf16 v[24:27], a[12:15], v[144:147], v[24:27]\n"
"v_lshlrev_b32_e32 v254, 16, v254\n"
"v_max_f32_e32 v253, 0, v253\n"
"v_mfma_f32_16x16x32_bf16 v[24:27], a[16:19], v[112:115], v[24:27]\n"
"v_cvt_pk_bf16_f32 v253, v253, 0\n"
"v_lshlrev_b32_e32 v253, 16, v253\n"
"v_mfma_f32_16x16x32_bf16 v[24:27], a[20:23], v[96:99], v[24:27]\n"
"ds_write_b128 v3, v[164:167] offset:11264\n"
"v_mfma_f32_16x16x32_bf16 v[24:27], a[24:27], v[108:111], v[24:27]\n"
"v_add_f32_e32 v253, 0x3727c5ac, v253\n"
"v_cvt_pk_bf16_f32 v253, v253, 0\n"
"v_mfma_f32_16x16x32_bf16 v[24:27], a[28:31], v[156:159], v[24:27]\n"
"v_lshlrev_b32_e32 v253, 16, v253\n"
"v_mfma_f32_16x16x32_bf16 v[72:75], a[32:35], v[92:95], 0\n"
"v_sqrt_f32_e32 v253, v253\n"
"v_mfma_f32_16x16x32_bf16 v[72:75], a[36:39], v[104:107], v[72:75]\n"
"v_cvt_pk_bf16_f32 v253, v253, 0\n"
"v_lshlrev_b32_e32 v253, 16, v253\n"
"v_mfma_f32_16x16x32_bf16 v[72:75], a[40:43], v[116:119], v[72:75]\n"
"s_add_u32 s98, s52, 0x8000\n"
"buffer_load_dwordx4 v[120:123], v0, s[4:7], s98 offen sc1\n"
"v_mfma_f32_16x16x32_bf16 v[72:75], a[44:47], v[144:147], v[72:75]\n"
"s_waitcnt lgkmcnt(0)\n"
"s_barrier\n"
"v_mfma_f32_16x16x32_bf16 v[72:75], a[48:51], v[112:115], v[72:75]\n"
"ds_read_b128 v[124:127], v4\n"
"v_cmp_u_f32_e32 vcc, v24, v24\n"
"v_mfma_f32_16x16x32_bf16 v[72:75], a[52:55], v[96:99], v[72:75]\n"
"s_cbranch_vccnz .Lmk_p88s\n"
".Lmk_p88r:\n"
"ds_read_b128 v[128:131], v9 offset:128\n"
"v_mfma_f32_16x16x32_bf16 v[72:75], a[56:59], v[108:111], v[72:75]\n"
"ds_write_b128 v3, v[24:27] offset:12288\n"
"v_mfma_f32_16x16x32_bf16 v[72:75], a[60:63], v[156:159], v[72:75]\n"
"buffer_load_dwordx4 v[24:27], v0, s[4:7], s98 offen offset:1024 sc1\n"
"v_mfma_f32_16x16x32_bf16 v[152:155], a[64:67], v[92:95], 0\n"
"buffer_load_dwordx4 a[148:151], v1, s[4:7], s96 offen offset:1024 sc1\n"
"v_mfma_f32_16x16x32_bf16 v[152:155], a[68:71], v[104:107], v[152:155]\n"
"v_rcp_f32_e32 v255, v253\n"
"v_mfma_f32_16x16x32_bf16 v[152:155], a[72:75], v[116:119], v[152:155]\n"
"ds_read_b128 v[164:167], v5\n"
"v_mul_f32_e32 v253, 0x3e8293ee, v255\n"
"v_mfma_f32_16x16x32_bf16 v[152:155], a[76:79], v[144:147], v[152:155]\n"
"ds_read_b128 v[168:171], v4 offset:1024\n"
"ds_read_b128 v[180:183], v9 offset:192\n"
"v_mfma_f32_16x16x32_bf16 v[152:155], a[80:83], v[112:115], v[152:155]\n"
"buffer_load_dwordx4 v[188:191], v0, s[4:7], s98 offen offset:2048 sc1\n"
"v_mfma_f32_16x16x32_bf16 v[152:155], a[84:87], v[96:99], v[152:155]\n"
"ds_write_b128 v3, v[72:75] offset:13312\n"
"v_mfma_f32_16x16x32_bf16 v[152:155], a[88:91], v[108:111], v[152:155]\n"
"ds_read_b128 v[72:75], v5 offset:1024\n"
"v_mfma_f32_16x16x32_bf16 v[152:155], a[92:95], v[156:159], v[152:155]\n"
"s_waitcnt lgkmcnt(4)\n"
"v_fmac_f32_e32 v124, v254, v128\n"
"v_fmac_f32_e32 v125, v254, v129\n"
"v_mfma_f32_16x16x32_bf16 v[192:195], a[96:99], v[92:95], 0\n"
"v_fmac_f32_e32 v126, v254, v130\n"
"v_fmac_f32_e32 v127, v254, v131\n"
"v_mfma_f32_16x16x32_bf16 v[192:195], a[100:103], v[104:107], v[192:195]\n"
"buffer_load_dwordx4 v[128:131], v0, s[4:7], s98 offen offset:3072 sc1\n"
"v_mfma_f32_16x16x32_bf16 v[192:195], a[104:107], v[116:119], v[192:195]\n"
"ds_read_b128 v[196:199], v4 offset:2048\n"
"ds_read_b128 v[200:203], v9 offset:256\n"
"v_mfma_f32_16x16x32_bf16 v[192:195], a[108:111], v[144:147], v[192:195]\n"
"ds_read_b128 v[204:207], v5 offset:2048\n"
"v_add_f32_e32 v124, v124, v164\n"
"v_mfma_f32_16x16x32_bf16 v[192:195], a[112:115], v[112:115], v[192:195]\n"
"v_add_f32_e32 v125, v125, v165\n"
"v_add_f32_e32 v126, v126, v166\n"
"v_mfma_f32_16x16x32_bf16 v[192:195], a[116:119], v[96:99], v[192:195]\n"
"ds_write_b128 v3, v[152:155] offset:14336\n"
"v_mfma_f32_16x16x32_bf16 v[192:195], a[120:123], v[108:111], v[192:195]\n"
"buffer_load_dwordx4 v[152:155], v1, s[4:7], s98 offen sc1\n"
"v_mfma_f32_16x16x32_bf16 v[192:195], a[124:127], v[156:159], v[192:195]\n"
"s_waitcnt lgkmcnt(4)\n"
"v_add_f32_e32 v127, v127, v167\n"
"v_fmac_f32_e32 v168, v254, v180\n"
"v_mfma_f32_16x16x32_bf16 v[164:167], v[136:139], v[92:95], 0\n"
"v_fmac_f32_e32 v169, v254, v181\n"
"v_fmac_f32_e32 v170, v254, v182\n"
"v_mfma_f32_16x16x32_bf16 v[164:167], v[44:47], v[104:107], v[164:167]\n"
"ds_read_b128 v[208:211], v4 offset:3072\n"
"ds_read_b128 v[212:215], v9 offset:320\n"
"v_mfma_f32_16x16x32_bf16 v[164:167], v[28:31], v[116:119], v[164:167]\n"
"ds_read_b128 v[216:219], v5 offset:3072\n"
"v_fmac_f32_e32 v171, v254, v183\n"
"v_mfma_f32_16x16x32_bf16 v[164:167], v[32:35], v[144:147], v[164:167]\n"
"buffer_load_dwordx4 v[180:183], v1, s[4:7], s98 offen offset:1024 sc1\n"
"v_mfma_f32_16x16x32_bf16 v[164:167], v[40:43], v[112:115], v[164:167]\n"
"buffer_load_dwordx4 a[152:155], v1, s[4:7], s96 offen offset:2048 sc1\n"
"v_mfma_f32_16x16x32_bf16 v[164:167], v[48:51], v[96:99], v[164:167]\n"
"ds_write_b128 v3, v[192:195] offset:15360\n"
"v_mfma_f32_16x16x32_bf16 v[164:167], v[64:67], v[108:111], v[164:167]\n"
"v_add_f32_e32 v168, v168, v72\n"
"v_add_f32_e32 v169, v169, v73\n"
"v_mfma_f32_16x16x32_bf16 v[164:167], v[36:39], v[156:159], v[164:167]\n"
"s_waitcnt lgkmcnt(5)\n"
"v_add_f32_e32 v170, v170, v74\n"
"v_add_f32_e32 v171, v171, v75\n"
"v_mfma_f32_16x16x32_bf16 v[72:75], v[140:143], v[92:95], 0\n"
"buffer_load_dwordx4 v[92:95], v1, s[4:7], s98 offen offset:2048 sc1\n"
"v_mfma_f32_16x16x32_bf16 v[72:75], v[52:55], v[104:107], v[72:75]\n"
"ds_read_b128 v[104:107], v4 offset:4096\n"
"ds_read_b128 v[192:195], v9\n"
"v_mfma_f32_16x16x32_bf16 v[72:75], v[60:63], v[116:119], v[72:75]\n"
"ds_read_b128 v[116:119], v5 offset:4096\n"
"v_fmac_f32_e32 v196, v254, v200\n"
"v_mfma_f32_16x16x32_bf16 v[72:75], v[56:59], v[144:147], v[72:75]\n"
"v_fmac_f32_e32 v197, v254, v201\n"
"v_fmac_f32_e32 v198, v254, v202\n"
"v_mfma_f32_16x16x32_bf16 v[72:75], v[68:71], v[112:115], v[72:75]\n"
"v_fmac_f32_e32 v199, v254, v203\n"
"v_add_f32_e32 v196, v196, v204\n"
"v_mfma_f32_16x16x32_bf16 v[72:75], v[76:79], v[96:99], v[72:75]\n"
"buffer_load_dwordx4 v[96:99], v1, s[4:7], s98 offen offset:3072 sc1\n"
"v_mfma_f32_16x16x32_bf16 v[72:75], v[88:91], v[108:111], v[72:75]\n"
"ds_write_b128 v3, v[164:167] offset:16384\n"
"v_mfma_f32_16x16x32_bf16 v[72:75], v[132:135], v[156:159], v[72:75]\n"
"s_waitcnt vmcnt(18)\n"
"ds_read_b128 v[108:111], v4 offset:5120\n"
"ds_read_b128 v[112:115], v9 offset:64\n"
"; @A G1 1.3\n"
"v_mfma_f32_16x16x32_bf16 v[144:147], a[0:3], v[80:83], v[124:127]\n"
"ds_read_b128 v[156:159], v5 offset:5120\n"
"v_add_f32_e32 v197, v197, v205\n"
"v_mfma_f32_16x16x32_bf16 v[144:147], a[4:7], v[84:87], v[144:147]\n"
"s_waitcnt lgkmcnt(8)\n"
"v_add_f32_e32 v198, v198, v206\n"
"v_add_f32_e32 v199, v199, v207\n"
"v_mfma_f32_16x16x32_bf16 v[144:147], a[8:11], v[148:151], v[144:147]\n"
"s_add_u32 s98, s53, 0x8000\n"
"buffer_load_dwordx4 v[164:167], v0, s[4:7], s98 offen sc1\n"
"v_mfma_f32_16x16x32_bf16 v[144:147], a[12:15], v[160:163], v[144:147]\n"
"s_waitcnt vmcnt(13)\n"
"v_fmac_f32_e32 v208, v254, v212\n"
"v_fmac_f32_e32 v209, v254, v213\n"
"v_mfma_f32_16x16x32_bf16 v[144:147], a[16:19], v[100:103], v[144:147]\n"
"v_fmac_f32_e32 v210, v254, v214\n"
"v_fmac_f32_e32 v211, v254, v215\n"
"v_mfma_f32_16x16x32_bf16 v[144:147], a[20:23], v[172:175], v[144:147]\n"
"ds_write_b128 v3, v[72:75] offset:17408\n"
"v_mfma_f32_16x16x32_bf16 v[144:147], a[24:27], v[176:179], v[144:147]\n"
"v_add_f32_e32 v208, v208, v216\n"
"v_add_f32_e32 v209, v209, v217\n"
"v_mfma_f32_16x16x32_bf16 v[144:147], a[28:31], v[184:187], v[144:147]\n"
"buffer_load_dwordx4 v[72:75], v0, s[4:7], s98 offen offset:1024 sc1\n"
"v_mfma_f32_16x16x32_bf16 v[200:203], a[32:35], v[80:83], v[168:171]\n"
"buffer_load_dwordx4 a[156:159], v1, s[4:7], s96 offen offset:3072 sc1\n"
"v_mfma_f32_16x16x32_bf16 v[200:203], a[36:39], v[84:87], v[200:203]\n"
"s_waitcnt lgkmcnt(5)\n"
"v_add_f32_e32 v210, v210, v218\n"
"v_add_f32_e32 v211, v211, v219\n"
"v_mfma_f32_16x16x32_bf16 v[200:203], a[40:43], v[148:151], v[200:203]\n"
"s_waitcnt lgkmcnt(0)\n"
"s_barrier\n"
"v_mfma_f32_16x16x32_bf16 v[200:203], a[44:47], v[160:163], v[200:203]\n"
"ds_read_b128 v[204:207], v6\n"
"ds_read_b128 v[212:215], v6 offset:1024\n"
"v_mfma_f32_16x16x32_bf16 v[200:203], a[48:51], v[100:103], v[200:203]\n"
"v_cmp_u_f32_e32 vcc, v144, v144\n"
"v_fmac_f32_e32 v104, v254, v192\n"
"v_mfma_f32_16x16x32_bf16 v[200:203], a[52:55], v[172:175], v[200:203]\n"
"s_cbranch_vccnz .Lmk_p89s\n"
".Lmk_p89r:\n"
"v_fmac_f32_e32 v105, v254, v193\n"
"v_mfma_f32_16x16x32_bf16 v[200:203], a[56:59], v[176:179], v[200:203]\n"
"buffer_load_dwordx4 v[124:127], v0, s[4:7], s98 offen offset:2048 sc1\n"
"v_mfma_f32_16x16x32_bf16 v[200:203], a[60:63], v[184:187], v[200:203]\n"
"v_fmac_f32_e32 v106, v254, v194\n"
"v_fmac_f32_e32 v107, v254, v195\n"
"v_mfma_f32_16x16x32_bf16 v[196:199], a[64:67], v[80:83], v[196:199]\n"
"v_add_f32_e32 v104, v104, v116\n"
"v_add_f32_e32 v105, v105, v117\n"
"v_mfma_f32_16x16x32_bf16 v[196:199], a[68:71], v[84:87], v[196:199]\n"
"buffer_load_dwordx4 v[168:171], v0, s[4:7], s98 offen offset:3072 sc1\n"
"v_mfma_f32_16x16x32_bf16 v[196:199], a[72:75], v[148:151], v[196:199]\n"
"v_add_f32_e32 v106, v106, v118\n"
"v_add_f32_e32 v107, v107, v119\n"
"v_mfma_f32_16x16x32_bf16 v[196:199], a[76:79], v[160:163], v[196:199]\n"
"v_fmac_f32_e32 v108, v254, v112\n"
"v_fmac_f32_e32 v109, v254, v113\n"
"v_mfma_f32_16x16x32_bf16 v[196:199], a[80:83], v[100:103], v[196:199]\n"
"s_waitcnt lgkmcnt(0)\n"
"v_fmac_f32_e32 v110, v254, v114\n"
"v_fmac_f32_e32 v111, v254, v115\n"
"v_mfma_f32_16x16x32_bf16 v[196:199], a[84:87], v[172:175], v[196:199]\n"
"v_add_f32_e32 v108, v108, v156\n"
"v_add_f32_e32 v109, v109, v157\n"
"v_mfma_f32_16x16x32_bf16 v[196:199], a[88:91], v[176:179], v[196:199]\n"
"buffer_load_dwordx4 v[112:115], v1, s[4:7], s98 offen sc1\n"
"v_mfma_f32_16x16x32_bf16 v[196:199], a[92:95], v[184:187], v[196:199]\n"
"ds_read_b128 v[116:119], v6 offset:2048\n"
"ds_read_b128 v[192:195], v6 offset:3072\n"
"v_mfma_f32_16x16x32_bf16 v[208:211], a[96:99], v[80:83], v[208:211]\n"
"v_add_f32_e32 v110, v110, v158\n"
"v_add_f32_e32 v111, v111, v159\n"
"v_mfma_f32_16x16x32_bf16 v[208:211], a[100:103], v[84:87], v[208:211]\n"
"v_add_f32_e32 v144, v144, v204\n"
"v_add_f32_e32 v145, v145, v205\n"
"v_mfma_f32_16x16x32_bf16 v[208:211], a[104:107], v[148:151], v[208:211]\n"
"s_add_u32 s97, s70, 0x2000\n"
"v_add_f32_e32 v146, v146, v206\n"
"v_add_f32_e32 v147, v147, v207\n"
"v_mfma_f32_16x16x32_bf16 v[208:211], a[108:111], v[160:163], v[208:211]\n"
"buffer_load_dwordx4 v[156:159], v1, s[4:7], s98 offen offset:1024 sc1\n"
"v_mfma_f32_16x16x32_bf16 v[208:211], a[112:115], v[100:103], v[208:211]\n"
"buffer_load_dwordx4 a[160:163], v0, s[4:7], s97 offen sc1\n"
"v_mfma_f32_16x16x32_bf16 v[208:211], a[116:119], v[172:175], v[208:211]\n"
"v_add_f32_e32 v200, v200, v212\n"
"v_add_f32_e32 v201, v201, v213\n"
"v_mfma_f32_16x16x32_bf16 v[208:211], a[120:123], v[176:179], v[208:211]\n"
"v_add_f32_e32 v202, v202, v214\n"
"v_add_f32_e32 v203, v203, v215\n"
"v_mfma_f32_16x16x32_bf16 v[208:211], a[124:127], v[184:187], v[208:211]\n"
"v_mul_f32_e32 v144, v144, v255\n"
"v_mul_f32_e32 v145, v145, v255\n"
"v_mfma_f32_16x16x32_bf16 v[104:107], v[136:139], v[80:83], v[104:107]\n"
"buffer_load_dwordx4 v[204:207], v1, s[4:7], s98 offen offset:2048 sc1\n"
"v_mfma_f32_16x16x32_bf16 v[104:107], v[44:47], v[84:87], v[104:107]\n"
"v_mul_f32_e32 v146, v146, v255\n"
"v_mul_f32_e32 v147, v147, v255\n"
"v_mfma_f32_16x16x32_bf16 v[104:107], v[28:31], v[148:151], v[104:107]\n"
"v_mul_f32_e32 v200, v200, v255\n"
"v_mul_f32_e32 v201, v201, v255\n"
"v_mfma_f32_16x16x32_bf16 v[104:107], v[32:35], v[160:163], v[104:107]\n"
"v_mul_f32_e32 v202, v202, v255\n"
"v_mul_f32_e32 v203, v203, v255\n"
"v_mfma_f32_16x16x32_bf16 v[104:107], v[40:43], v[100:103], v[104:107]\n"
"s_waitcnt lgkmcnt(0)\n"
"v_cvt_pk_bf16_f32 v212, v144, v145\n"
"v_cvt_pk_bf16_f32 v213, v146, v147\n"
"v_mfma_f32_16x16x32_bf16 v[104:107], v[48:51], v[172:175], v[104:107]\n"
"buffer_load_dwordx4 v[144:147], v1, s[4:7], s98 offen offset:3072 sc1\n"
"v_mfma_f32_16x16x32_bf16 v[104:107], v[64:67], v[176:179], v[104:107]\n"
"ds_read_b128 v[216:219], v6 offset:4096\n"
"ds_read_b128 v[220:223], v6 offset:5120\n"
"v_mfma_f32_16x16x32_bf16 v[104:107], v[36:39], v[184:187], v[104:107]\n"
"v_cvt_pk_bf16_f32 v214, v200, v201\n"
"v_cvt_pk_bf16_f32 v215, v202, v203\n"
"v_mfma_f32_16x16x32_bf16 v[108:111], v[140:143], v[80:83], v[108:111]\n"
"ds_write_b128 v8, v[212:215] offset:25600\n"
"v_mfma_f32_16x16x32_bf16 v[108:111], v[52:55], v[84:87], v[108:111]\n"
"v_add_f32_e32 v196, v196, v116\n"
"v_add_f32_e32 v197, v197, v117\n"
"v_mfma_f32_16x16x32_bf16 v[108:111], v[60:63], v[148:151], v[108:111]\n"
"v_add_f32_e32 v198, v198, v118\n"
"v_add_f32_e32 v199, v199, v119\n"
"v_mfma_f32_16x16x32_bf16 v[108:111], v[56:59], v[160:163], v[108:111]\n"
"v_add_f32_e32 v208, v208, v192\n"
"v_add_f32_e32 v209, v209, v193\n"
"v_mfma_f32_16x16x32_bf16 v[108:111], v[68:71], v[100:103], v[108:111]\n"
"v_add_f32_e32 v210, v210, v194\n"
"v_add_f32_e32 v211, v211, v195\n"
"v_mfma_f32_16x16x32_bf16 v[108:111], v[76:79], v[172:175], v[108:111]\n"
"v_mul_f32_e32 v196, v196, v255\n"
"v_mul_f32_e32 v197, v197, v255\n"
"v_mfma_f32_16x16x32_bf16 v[108:111], v[88:91], v[176:179], v[108:111]\n"
"v_mul_f32_e32 v198, v198, v255\n"
"v_mul_f32_e32 v199, v199, v255\n"
"v_mfma_f32_16x16x32_bf16 v[108:111], v[132:135], v[184:187], v[108:111]\n"
"v_mul_f32_e32 v208, v208, v255\n"
"v_mul_f32_e32 v209, v209, v255\n"
"v_mul_f32_e32 v210, v210, v255\n"
"v_mul_f32_e32 v211, v211, v255\n"
"v_cvt_pk_bf16_f32 v80, v196, v197\n"
"v_cvt_pk_bf16_f32 v81, v198, v199\n"
"v_cvt_pk_bf16_f32 v82, v208, v209\n"
"v_cvt_pk_bf16_f32 v83, v210, v211\n"
"ds_write_b128 v7, v[80:83] offset:58368\n"
".Lmk_a2:\n"
"s_waitcnt vmcnt(15)\n"
"; @A G1 2.0\n"
"v_mfma_f32_16x16x32_bf16 v[84:87], a[0:3], v[120:123], 0\n"
"v_mfma_f32_16x16x32_bf16 v[84:87], a[4:7], v[24:27], v[84:87]\n"
"s_add_u32 s98, s62, 0x800\n"
"v_mfma_f32_16x16x32_bf16 v[84:87], a[8:11], v[188:191], v[84:87]\n"
"s_add_u32 s96, s54, 0x8000\n"
"buffer_load_dwordx4 v[100:103], v0, s[4:7], s96 offen sc1\n"
"v_mfma_f32_16x16x32_bf16 v[84:87], a[12:15], v[128:131], v[84:87]\n"
"s_waitcnt vmcnt(11)\n"
"v_mfma_f32_16x16x32_bf16 v[84:87], a[16:19], v[152:155], v[84:87]\n"
"buffer_load_dword v255, v10, s[4:7], s98 offen sc1\n"
"v_mfma_f32_16x16x32_bf16 v[84:87], a[20:23], v[180:183], v[84:87]\n"
"s_waitcnt lgkmcnt(2)\n"
"v_add_f32_e32 v104, v104, v216\n"
"v_add_f32_e32 v105, v105, v217\n"
"v_mfma_f32_16x16x32_bf16 v[84:87], a[24:27], v[92:95], v[84:87]\n"
"v_add_f32_e32 v106, v106, v218\n"
"v_add_f32_e32 v107, v107, v219\n"
"v_mfma_f32_16x16x32_bf16 v[84:87], a[28:31], v[96:99], v[84:87]\n"
"buffer_load_dwordx4 v[116:119], v0, s[4:7], s96 offen offset:1024 sc1\n"
"v_mfma_f32_16x16x32_bf16 v[148:151], a[32:35], v[120:123], 0\n"
"buffer_load_dwordx4 a[164:167], v0, s[4:7], s97 offen offset:1024 sc1\n"
"v_mfma_f32_16x16x32_bf16 v[148:151], a[36:39], v[24:27], v[148:151]\n"
"buffer_load_dword v254, v10, s[4:7], s98 offen offset:256 sc1\n"
"v_mfma_f32_16x16x32_bf16 v[148:151], a[40:43], v[188:191], v[148:151]\n"
"v_add_f32_e32 v108, v108, v220\n"
"v_add_f32_e32 v109, v109, v221\n"
"v_mfma_f32_16x16x32_bf16 v[148:151], a[44:47], v[128:131], v[148:151]\n"
"v_add_f32_e32 v110, v110, v222\n"
"v_add_f32_e32 v111, v111, v223\n"
"v_mfma_f32_16x16x32_bf16 v[148:151], a[48:51], v[152:155], v[148:151]\n"
"buffer_load_dwordx4 v[160:163], v0, s[4:7], s96 offen offset:2048 sc1\n"
"v_mfma_f32_16x16x32_bf16 v[148:151], a[52:55], v[180:183], v[148:151]\n"
"ds_write_b128 v3, v[84:87]\n"
"v_mfma_f32_16x16x32_bf16 v[148:151], a[56:59], v[92:95], v[148:151]\n"
"buffer_load_dword v252, v10, s[4:7], s98 offen offset:512 sc1\n"
"v_mfma_f32_16x16x32_bf16 v[148:151], a[60:63], v[96:99], v[148:151]\n"
"v_mul_f32_e32 v104, v104, v253\n"
"v_mul_f32_e32 v105, v105, v253\n"
"v_mfma_f32_16x16x32_bf16 v[84:87], a[64:67], v[120:123], 0\n"
"v_mul_f32_e32 v106, v106, v253\n"
"v_mul_f32_e32 v107, v107, v253\n"
"v_mfma_f32_16x16x32_bf16 v[84:87], a[68:71], v[24:27], v[84:87]\n"
"buffer_load_dwordx4 v[172:175], v0, s[4:7], s96 offen offset:3072 sc1\n"
"v_mfma_f32_16x16x32_bf16 v[84:87], a[72:75], v[188:191], v[84:87]\n"
"buffer_load_dword v251, v10, s[4:7], s98 offen offset:768 sc1\n"
"v_mfma_f32_16x16x32_bf16 v[84:87], a[76:79], v[128:131], v[84:87]\n"
"v_mul_f32_e32 v108, v108, v253\n"
"v_mul_f32_e32 v109, v109, v253\n"
"v_mfma_f32_16x16x32_bf16 v[84:87], a[80:83], v[152:155], v[84:87]\n"
"v_mul_f32_e32 v110, v110, v253\n"
"v_mul_f32_e32 v111, v111, v253\n"
"v_mfma_f32_16x16x32_bf16 v[84:87], a[84:87], v[180:183], v[84:87]\n"
"ds_write_b128 v3, v[148:151] offset:1024\n"
"v_mfma_f32_16x16x32_bf16 v[84:87], a[88:91], v[92:95], v[84:87]\n"
"buffer_load_dwordx4 v[148:151], v1, s[4:7], s96 offen sc1\n"
"v_mfma_f32_16x16x32_bf16 v[84:87], a[92:95], v[96:99], v[84:87]\n"
"buffer_load_dword v253, v10, s[4:7], s98 offen offset:1024 sc1\n"
"v_mfma_f32_16x16x32_bf16 v[176:179], a[96:99], v[120:123], 0\n"
"v_cvt_pk_bf16_f32 v184, v104, v105\n"
"v_cvt_pk_bf16_f32 v185, v106, v107\n"
"v_mfma_f32_16x16x32_bf16 v[176:179], a[100:103], v[24:27], v[176:179]\n"
"v_cvt_pk_bf16_f32 v186, v108, v109\n"
"v_cvt_pk_bf16_f32 v187, v110, v111\n"
"v_mfma_f32_16x16x32_bf16 v[176:179], a[104:107], v[188:191], v[176:179]\n"
"buffer_load_dword v250, v10, s[4:7], s98 offen offset:1280 sc1\n"
"v_mfma_f32_16x16x32_bf16 v[176:179], a[108:111], v[128:131], v[176:179]\n"
"buffer_load_dwordx4 v[104:107], v1, s[4:7], s96 offen offset:1024 sc1\n"
"v_mfma_f32_16x16x32_bf16 v[176:179], a[112:115], v[152:155], v[176:179]\n"
"buffer_load_dwordx4 a[168:171], v0, s[4:7], s97 offen offset:2048 sc1\n"
"v_mfma_f32_16x16x32_bf16 v[176:179], a[116:119], v[180:183], v[176:179]\n"
"ds_write_b128 v3, v[84:87] offset:2048\n"
"v_mfma_f32_16x16x32_bf16 v[176:179], a[120:123], v[92:95], v[176:179]\n"
"buffer_load_dword v249, v10, s[4:7], s98 offen offset:1536 sc1\n"
"v_mfma_f32_16x16x32_bf16 v[176:179], a[124:127], v[96:99], v[176:179]\n"
"ds_write_b128 v2, v[184:187] offset:50176\n"
"v_mfma_f32_16x16x32_bf16 v[84:87], v[136:139], v[120:123], 0\n"
"buffer_load_dwordx4 v[108:111], v1, s[4:7], s96 offen offset:2048 sc1\n"
"s_add_u32 s95, s64, 0x2000\n"
"v_mfma_f32_16x16x32_bf16 v[84:87], v[44:47], v[24:27], v[84:87]\n"
"buffer_load_dword v248, v10, s[4:7], s98 offen offset:1792 sc1\n"
"v_mfma_f32_16x16x32_bf16 v[84:87], v[28:31], v[188:191], v[84:87]\n"
"buffer_store_dwordx4 v[212:215], v8, s[4:7], s95 offen offset:1024 sc1\n"
"v_mfma_f32_16x16x32_bf16 v[84:87], v[32:35], v[128:131], v[84:87]\n"
"v_mfma_f32_16x16x32_bf16 v[84:87], v[40:43], v[152:155], v[84:87]\n"
"v_mfma_f32_16x16x32_bf16 v[84:87], v[48:51], v[180:183], v[84:87]\n"
"buffer_load_dwordx4 v[184:187], v1, s[4:7], s96 offen offset:3072 sc1\n"
"v_mfma_f32_16x16x32_bf16 v[84:87], v[64:67], v[92:95], v[84:87]\n"
"ds_write_b128 v3, v[176:179] offset:3072\n"
"v_mfma_f32_16x16x32_bf16 v[84:87], v[36:39], v[96:99], v[84:87]\n"
"v_mfma_f32_16x16x32_bf16 v[176:179], v[140:143], v[120:123], 0\n"
"v_mfma_f32_16x16x32_bf16 v[176:179], v[52:55], v[24:27], v[176:179]\n"
"v_mfma_f32_16x16x32_bf16 v[176:179], v[60:63], v[188:191], v[176:179]\n"
"v_mfma_f32_16x16x32_bf16 v[176:179], v[56:59], v[128:131], v[176:179]\n"
"v_mfma_f32_16x16x32_bf16 v[176:179], v[68:71], v[152:155], v[176:179]\n"
"v_mfma_f32_16x16x32_bf16 v[176:179], v[76:79], v[180:183], v[176:179]\n"
"s_nop 1\n"
"ds_write_b128 v3, v[84:87] offset:4096\n"
"v_mfma_f32_16x16x32_bf16 v[176:179], v[88:91], v[92:95], v[176:179]\n"
"v_mfma_f32_16x16x32_bf16 v[176:179], v[132:135], v[96:99], v[176:179]\n"
"s_waitcnt vmcnt(24)\n"
"; @A G1 2.1\n"
"v_mfma_f32_16x16x32_bf16 v[24:27], a[0:3], v[164:167], 0\n"
"buffer_load_dwordx4 a[172:175], v0, s[4:7], s97 offen offset:3072 sc1\n"
"v_mfma_f32_16x16x32_bf16 v[24:27], a[4:7], v[72:75], v[24:27]\n"
"v_mfma_f32_16x16x32_bf16 v[24:27], a[8:11], v[124:127], v[24:27]\n"
"s_add_u32 s98, s55, 0x8000\n"
"buffer_load_dwordx4 v[84:87], v0, s[4:7], s98 offen sc1\n"
"v_mfma_f32_16x16x32_bf16 v[24:27], a[12:15], v[168:171], v[24:27]\n"
"s_waitcnt vmcnt(21)\n"
"v_mfma_f32_16x16x32_bf16 v[24:27], a[16:19], v[112:115], v[24:27]\n"
"v_mfma_f32_16x16x32_bf16 v[24:27], a[20:23], v[156:159], v[24:27]\n"
"ds_write_b128 v3, v[176:179] offset:5120\n"
"v_mfma_f32_16x16x32_bf16 v[24:27], a[24:27], v[204:207], v[24:27]\n"
"v_mfma_f32_16x16x32_bf16 v[24:27], a[28:31], v[144:147], v[24:27]\n"
"buffer_load_dwordx4 v[92:95], v0, s[4:7], s98 offen offset:1024 sc1\n"
"v_mfma_f32_16x16x32_bf16 v[96:99], a[32:35], v[164:167], 0\n"
"s_add_u32 s96, s64, 0x6000\n"
"v_mfma_f32_16x16x32_bf16 v[96:99], a[36:39], v[72:75], v[96:99]\n"
"v_mfma_f32_16x16x32_bf16 v[96:99], a[40:43], v[124:127], v[96:99]\n"
"buffer_store_dwordx4 v[80:83], v7, s[4:7], s96 offen offset:1024 sc1\n"
"v_mfma_f32_16x16x32_bf16 v[96:99], a[44:47], v[168:171], v[96:99]\n"
"v_mfma_f32_16x16x32_bf16 v[96:99], a[48:51], v[112:115], v[96:99]\n"
"buffer_load_dwordx4 v[80:83], v0, s[4:7], s98 offen offset:2048 sc1\n"
"v_mfma_f32_16x16x32_bf16 v[96:99], a[52:55], v[156:159], v[96:99]\n"
"ds_write_b128 v3, v[24:27] offset:6144\n"
"v_mfma_f32_16x16x32_bf16 v[96:99], a[56:59], v[204:207], v[96:99]\n"
"v_mfma_f32_16x16x32_bf16 v[96:99], a[60:63], v[144:147], v[96:99]\n"
"v_mfma_f32_16x16x32_bf16 v[24:27], a[64:67], v[164:167], 0\n"
"v_mfma_f32_16x16x32_bf16 v[24:27], a[68:71], v[72:75], v[24:27]\n"
"buffer_load_dwordx4 v[120:123], v0, s[4:7], s98 offen offset:3072 sc1\n"
"v_mfma_f32_16x16x32_bf16 v[24:27], a[72:75], v[124:127], v[24:27]\n"
"v_mfma_f32_16x16x32_bf16 v[24:27], a[76:79], v[168:171], v[24:27]\n"
"v_mfma_f32_16x16x32_bf16 v[24:27], a[80:83], v[112:115], v[24:27]\n"
"buffer_load_dwordx4 a[176:179], v1, s[4:7], s97 offen sc1\n"
"v_mfma_f32_16x16x32_bf16 v[24:27], a[84:87], v[156:159], v[24:27]\n"
"ds_write_b128 v3, v[96:99] offset:7168\n"
"v_mfma_f32_16x16x32_bf16 v[24:27], a[88:91], v[204:207], v[24:27]\n"
"buffer_load_dwordx4 v[96:99], v1, s[4:7], s98 offen sc1\n"
"v_mfma_f32_16x16x32_bf16 v[24:27], a[92:95], v[144:147], v[24:27]\n"
"v_mfma_f32_16x16x32_bf16 v[128:131], a[96:99], v[164:167], 0\n"
"v_mfma_f32_16x16x32_bf16 v[128:131], a[100:103], v[72:75], v[128:131]\n"
"v_mfma_f32_16x16x32_bf16 v[128:131], a[104:107], v[124:127], v[128:131]\n"
"v_mfma_f32_16x16x32_bf16 v[128:131], a[108:111], v[168:171], v[128:131]\n"
"buffer_load_dwordx4 v[152:155], v1, s[4:7], s98 offen offset:1024 sc1\n"
"v_mfma_f32_16x16x32_bf16 v[128:131], a[112:115], v[112:115], v[128:131]\n"
"v_mfma_f32_16x16x32_bf16 v[128:131], a[116:119], v[156:159], v[128:131]\n"
"s_nop 0\n"
"ds_write_b128 v3, v[24:27] offset:8192\n"
"v_mfma_f32_16x16x32_bf16 v[128:131], a[120:123], v[204:207], v[128:131]\n"
"v_mfma_f32_16x16x32_bf16 v[128:131], a[124:127], v[144:147], v[128:131]\n"
"v_mfma_f32_16x16x32_bf16 v[24:27], v[136:139], v[164:167], 0\n"
"buffer_load_dwordx4 v[176:179], v1, s[4:7], s98 offen offset:2048 sc1\n"
"v_mfma_f32_16x16x32_bf16 v[24:27], v[44:47], v[72:75], v[24:27]\n"
"v_mfma_f32_16x16x32_bf16 v[24:27], v[28:31], v[124:127], v[24:27]\n"
"v_mfma_f32_16x16x32_bf16 v[24:27], v[32:35], v[168:171], v[24:27]\n"
"v_mfma_f32_16x16x32_bf16 v[24:27], v[40:43], v[112:115], v[24:27]\n"
"s_waitcnt vmcnt(12)\n"
"v_add_f32_e32 v255, v255, v253\n"
"v_add_f32_e32 v254, v254, v250\n"
"v_mfma_f32_16x16x32_bf16 v[24:27], v[48:51], v[156:159], v[24:27]\n"
"buffer_load_dwordx4 v[180:183], v1, s[4:7], s98 offen offset:3072 sc1\n"
"v_mfma_f32_16x16x32_bf16 v[24:27], v[64:67], v[204:207], v[24:27]\n"
"ds_write_b128 v3, v[128:131] offset:9216\n"
"v_mfma_f32_16x16x32_bf16 v[24:27], v[36:39], v[144:147], v[24:27]\n"
"v_add_f32_e32 v252, v252, v249\n"
"v_add_f32_e32 v251, v251, v248\n"
"v_mfma_f32_16x16x32_bf16 v[128:131], v[140:143], v[164:167], 0\n"
"buffer_load_dwordx4 a[180:183], v1, s[4:7], s97 offen offset:1024 sc1\n"
"v_mfma_f32_16x16x32_bf16 v[128:131], v[52:55], v[72:75], v[128:131]\n"
"v_add_f32_e32 v255, v255, v252\n"
"v_add_f32_e32 v254, v254, v251\n"
"v_mfma_f32_16x16x32_bf16 v[128:131], v[60:63], v[124:127], v[128:131]\n"
"v_add_f32_e32 v255, v255, v254\n"
"v_mov_b32_e32 v254, v255\n"
"v_mfma_f32_16x16x32_bf16 v[128:131], v[56:59], v[168:171], v[128:131]\n"
"s_nop 0\n"
"v_permlane32_swap_b32_e32 v255, v254\n"
"v_add_f32_e32 v255, v255, v254\n"
"v_mfma_f32_16x16x32_bf16 v[128:131], v[68:71], v[112:115], v[128:131]\n"
"v_mul_f32_e32 v255, 0x3a800000, v255\n"
"v_mov_b32_e32 v254, v255\n"
"v_mfma_f32_16x16x32_bf16 v[128:131], v[76:79], v[156:159], v[128:131]\n"
"ds_write_b128 v3, v[24:27] offset:10240\n"
"v_mfma_f32_16x16x32_bf16 v[128:131], v[88:91], v[204:207], v[128:131]\n"
"v_permlane16_swap_b32_e32 v255, v254\n"
"v_cvt_pk_bf16_f32 v253, -v255, 0\n"
"v_mfma_f32_16x16x32_bf16 v[128:131], v[132:135], v[144:147], v[128:131]\n"
"v_fma_f32 v254, -v255, v255, v254\n"
"v_lshlrev_b32_e32 v253, 16, v253\n"
"; @A G1 2.2\n"
"v_mfma_f32_16x16x32_bf16 v[24:27], a[0:3], v[100:103], 0\n"
"v_max_f32_e32 v254, 0, v254\n"
"v_cvt_pk_bf16_f32 v254, v254, 0\n"
"v_mfma_f32_16x16x32_bf16 v[24:27], a[4:7], v[116:119], v[24:27]\n"
"v_lshlrev_b32_e32 v254, 16, v254\n"
"v_add_f32_e32 v254, 0x3727c5ac, v254\n"
"v_mfma_f32_16x16x32_bf16 v[24:27], a[8:11], v[160:163], v[24:27]\n"
"v_cvt_pk_bf16_f32 v254, v254, 0\n"
"v_lshlrev_b32_e32 v254, 16, v254\n"
"v_mfma_f32_16x16x32_bf16 v[24:27], a[12:15], v[172:175], v[24:27]\n"
"s_waitcnt vmcnt(12)\n"
"v_sqrt_f32_e32 v254, v254\n"
"v_mfma_f32_16x16x32_bf16 v[24:27], a[16:19], v[148:151], v[24:27]\n"
"s_mov_b32 s98, .Lmk_a4-.Lmk_g91\n"
"s_getpc_b64 s[84:85]\n"
".Lmk_g91:\n"
"s_add_u32 s84, s84, s98\n"
"s_addc_u32 s85, s85, 0\n"
"s_and_b32 s84, s84, -16\n"
"v_mfma_f32_16x16x32_bf16 v[24:27], a[20:23], v[104:107], v[24:27]\n"
"ds_write_b128 v3, v[128:131] offset:11264\n"
"v_mfma_f32_16x16x32_bf16 v[24:27], a[24:27], v[108:111], v[24:27]\n"
"s_and_b32 s85, s85, 0xffff\n"
"s_sub_u32 s98, .Lmk_end-.Lmk_g91, s98\n"
"s_min_u32 s86, s98, 0x4000\n"
"s_mov_b32 s87, 0x20000\n"
"v_mfma_f32_16x16x32_bf16 v[24:27], a[28:31], v[184:187], v[24:27]\n"
"s_mov_b32 m0, 0x27800\n"
"s_nop 0\n"
"buffer_load_dwordx4 v0, s[84:87], s99 offen lds\n"
"v_mfma_f32_16x16x32_bf16 v[72:75], a[32:35], v[100:103], 0\n"
"v_cvt_pk_bf16_f32 v254, v254, 0\n"
"v_lshlrev_b32_e32 v254, 16, v254\n"
"v_mfma_f32_16x16x32_bf16 v[72:75], a[36:39], v[116:119], v[72:75]\n"
"v_rcp_f32_e32 v255, v254\n"
"v_mfma_f32_16x16x32_bf16 v[72:75], a[40:43], v[160:163], v[72:75]\n"
"s_add_u32 s98, s52, 0\n"
"buffer_load_dwordx4 v[112:115], v0, s[4:7], s98 offen sc1\n"
"v_mfma_f32_16x16x32_bf16 v[72:75], a[44:47], v[172:175], v[72:75]\n"
"s_waitcnt lgkmcnt(0)\n"
"s_barrier\n"
"v_mfma_f32_16x16x32_bf16 v[72:75], a[48:51], v[148:151], v[72:75]\n"
"buffer_load_dwordx4 a[184:187], v1, s[4:7], s97 offen offset:2048 sc1\n"
"v_mfma_f32_16x16x32_bf16 v[72:75], a[52:55], v[104:107], v[72:75]\n"
"ds_write_b128 v3, v[24:27] offset:12288\n"
"v_mfma_f32_16x16x32_bf16 v[72:75], a[56:59], v[108:111], v[72:75]\n"
"ds_read_b128 v[24:27], v4\n"
"v_mul_f32_e32 v254, 0x3e8293ee, v255\n"
"v_mfma_f32_16x16x32_bf16 v[72:75], a[60:63], v[184:187], v[72:75]\n"
"buffer_load_dwordx4 v[124:127], v0, s[4:7], s98 offen offset:1024 sc1\n"
"v_mfma_f32_16x16x32_bf16 v[128:131], a[64:67], v[100:103], 0\n"
"ds_read_b128 v[144:147], v9 offset:128\n"
"ds_read_b128 v[156:159], v5\n"
"v_mfma_f32_16x16x32_bf16 v[128:131], a[68:71], v[116:119], v[128:131]\n"
"v_mfma_f32_16x16x32_bf16 v[128:131], a[72:75], v[160:163], v[128:131]\n"
"v_mfma_f32_16x16x32_bf16 v[128:131], a[76:79], v[172:175], v[128:131]\n"
"ds_read_b128 v[164:167], v4 offset:1024\n"
"ds_read_b128 v[168:171], v9 offset:192\n"
"v_mfma_f32_16x16x32_bf16 v[128:131], a[80:83], v[148:151], v[128:131]\n"
"buffer_load_dwordx4 v[188:191], v0, s[4:7], s98 offen offset:2048 sc1\n"
"v_mfma_f32_16x16x32_bf16 v[128:131], a[84:87], v[104:107], v[128:131]\n"
"ds_write_b128 v3, v[72:75] offset:13312\n"
"v_mfma_f32_16x16x32_bf16 v[128:131], a[88:91], v[108:111], v[128:131]\n"
"ds_read_b128 v[72:75], v5 offset:1024\n"
"s_waitcnt lgkmcnt(4)\n"
"v_mfma_f32_16x16x32_bf16 v[128:131], a[92:95], v[184:187], v[128:131]\n"
"v_fmac_f32_e32 v24, v253, v144\n"
"v_fmac_f32_e32 v25, v253, v145\n"
"v_mfma_f32_16x16x32_bf16 v[192:195], a[96:99], v[100:103], 0\n"
"v_fmac_f32_e32 v26, v253, v146\n"
"v_fmac_f32_e32 v27, v253, v147\n"
"v_mfma_f32_16x16x32_bf16 v[192:195], a[100:103], v[116:119], v[192:195]\n"
"buffer_load_dwordx4 v[144:147], v0, s[4:7], s98 offen offset:3072 sc1\n"
"v_mfma_f32_16x16x32_bf16 v[192:195], a[104:107], v[160:163], v[192:195]\n"
"ds_read_b128 v[196:199], v4 offset:2048\n"
"ds_read_b128 v[200:203], v9 offset:256\n"
"v_mfma_f32_16x16x32_bf16 v[192:195], a[108:111], v[172:175], v[192:195]\n"
"ds_read_b128 v[204:207], v5 offset:2048\n"
"v_add_f32_e32 v24, v24, v156\n"
"v_mfma_f32_16x16x32_bf16 v[192:195], a[112:115], v[148:151], v[192:195]\n"
"v_add_f32_e32 v25, v25, v157\n"
"v_add_f32_e32 v26, v26, v158\n"
"v_mfma_f32_16x16x32_bf16 v[192:195], a[116:119], v[104:107], v[192:195]\n"
"ds_write_b128 v3, v[128:131] offset:14336\n"
"v_mfma_f32_16x16x32_bf16 v[192:195], a[120:123], v[108:111], v[192:195]\n"
"buffer_load_dwordx4 v[128:131], v1, s[4:7], s98 offen sc1\n"
"v_mfma_f32_16x16x32_bf16 v[192:195], a[124:127], v[184:187], v[192:195]\n"
"s_waitcnt lgkmcnt(4)\n"
"v_add_f32_e32 v27, v27, v159\n"
"v_fmac_f32_e32 v164, v253, v168\n"
"v_mfma_f32_16x16x32_bf16 v[156:159], v[136:139], v[100:103], 0\n"
"buffer_load_dwordx4 a[188:191], v1, s[4:7], s97 offen offset:3072 sc1\n"
"v_mfma_f32_16x16x32_bf16 v[156:159], v[44:47], v[116:119], v[156:159]\n"
"ds_read_b128 v[208:211], v4 offset:3072\n"
"ds_read_b128 v[212:215], v9 offset:320\n"
"v_mfma_f32_16x16x32_bf16 v[156:159], v[28:31], v[160:163], v[156:159]\n"
"s_waitcnt vmcnt(16)\n"
"s_add_u32 s97, s68, 0x1000\n"
"ds_read_b128 v[216:219], v5 offset:3072\n"
"v_mfma_f32_16x16x32_bf16 v[156:159], v[32:35], v[172:175], v[156:159]\n"
"buffer_load_dwordx4 v[220:223], v1, s[4:7], s98 offen offset:1024 sc1\n"
"v_mfma_f32_16x16x32_bf16 v[156:159], v[40:43], v[148:151], v[156:159]\n"
"buffer_store_dwordx2 v[18:19], v11, s[4:7], s97 offen offset:64 sc1\n"
"v_mfma_f32_16x16x32_bf16 v[156:159], v[48:51], v[104:107], v[156:159]\n"
"ds_write_b128 v3, v[192:195] offset:15360\n"
"v_mfma_f32_16x16x32_bf16 v[156:159], v[64:67], v[108:111], v[156:159]\n"
"v_fmac_f32_e32 v165, v253, v169\n"
"v_fmac_f32_e32 v166, v253, v170\n"
"v_mfma_f32_16x16x32_bf16 v[156:159], v[36:39], v[184:187], v[156:159]\n"
"v_fmac_f32_e32 v167, v253, v171\n"
"v_add_f32_e32 v164, v164, v72\n"
"v_mfma_f32_16x16x32_bf16 v[168:171], v[140:143], v[100:103], 0\n"
"buffer_load_dwordx4 v[100:103], v1, s[4:7], s98 offen offset:2048 sc1\n"
"v_mfma_f32_16x16x32_bf16 v[168:171], v[52:55], v[116:119], v[168:171]\n"
"ds_read_b128 v[116:119], v4 offset:4096\n"
"ds_read_b128 v[192:195], v9\n"
"v_mfma_f32_16x16x32_bf16 v[168:171], v[60:63], v[160:163], v[168:171]\n"
"ds_read_b128 v[160:163], v5 offset:4096\n"
"v_add_f32_e32 v165, v165, v73\n"
"v_mfma_f32_16x16x32_bf16 v[168:171], v[56:59], v[172:175], v[168:171]\n"
"s_waitcnt lgkmcnt(8)\n"
"v_add_f32_e32 v166, v166, v74\n"
"v_add_f32_e32 v167, v167, v75\n"
"v_mfma_f32_16x16x32_bf16 v[168:171], v[68:71], v[148:151], v[168:171]\n"
"v_fmac_f32_e32 v196, v253, v200\n"
"v_fmac_f32_e32 v197, v253, v201\n"
"v_mfma_f32_16x16x32_bf16 v[168:171], v[76:79], v[104:107], v[168:171]\n"
"buffer_load_dwordx4 v[72:75], v1, s[4:7], s98 offen offset:3072 sc1\n"
"v_mfma_f32_16x16x32_bf16 v[168:171], v[88:91], v[108:111], v[168:171]\n"
"ds_write_b128 v3, v[156:159] offset:16384\n"
"v_mfma_f32_16x16x32_bf16 v[168:171], v[132:135], v[184:187], v[168:171]\n"
"s_waitcnt vmcnt(18)\n"
"ds_read_b128 v[104:107], v4 offset:5120\n"
"ds_read_b128 v[108:111], v9 offset:64\n"
"; @A G1 2.3\n"
"v_mfma_f32_16x16x32_bf16 v[24:27], a[0:3], v[84:87], v[24:27]\n"
"ds_read_b128 v[148:151], v5 offset:5120\n"
"v_fmac_f32_e32 v198, v253, v202\n"
"v_mfma_f32_16x16x32_bf16 v[24:27], a[4:7], v[92:95], v[24:27]\n"
"v_fmac_f32_e32 v199, v253, v203\n"
"v_add_f32_e32 v196, v196, v204\n"
"v_mfma_f32_16x16x32_bf16 v[24:27], a[8:11], v[80:83], v[24:27]\n"
"s_add_u32 s98, s53, 0\n"
"buffer_load_dwordx4 v[156:159], v0, s[4:7], s98 offen sc1\n"
"v_mfma_f32_16x16x32_bf16 v[24:27], a[12:15], v[120:123], v[24:27]\n"
"s_waitcnt vmcnt(14)\n"
"s_add_u32 s97, s70, 0x4000\n"
"v_add_f32_e32 v197, v197, v205\n"
"v_mfma_f32_16x16x32_bf16 v[24:27], a[16:19], v[96:99], v[24:27]\n"
"buffer_load_dwordx4 a[192:195], v0, s[4:7], s97 offen sc1\n"
"v_mfma_f32_16x16x32_bf16 v[24:27], a[20:23], v[152:155], v[24:27]\n"
"ds_write_b128 v3, v[168:171] offset:17408\n"
"v_mfma_f32_16x16x32_bf16 v[24:27], a[24:27], v[176:179], v[24:27]\n"
"s_waitcnt lgkmcnt(9)\n"
"v_add_f32_e32 v198, v198, v206\n"
"v_add_f32_e32 v199, v199, v207\n"
"v_mfma_f32_16x16x32_bf16 v[24:27], a[28:31], v[180:183], v[24:27]\n"
"buffer_load_dwordx4 v[168:171], v0, s[4:7], s98 offen offset:1024 sc1\n"
"v_mfma_f32_16x16x32_bf16 v[164:167], a[32:35], v[84:87], v[164:167]\n"
"v_fmac_f32_e32 v208, v253, v212\n"
"v_fmac_f32_e32 v209, v253, v213\n"
"v_mfma_f32_16x16x32_bf16 v[164:167], a[36:39], v[92:95], v[164:167]\n"
"v_fmac_f32_e32 v210, v253, v214\n"
"v_fmac_f32_e32 v211, v253, v215\n"
"v_mfma_f32_16x16x32_bf16 v[164:167], a[40:43], v[80:83], v[164:167]\n"
"s_waitcnt lgkmcnt(0)\n"
"s_barrier\n"
"v_mfma_f32_16x16x32_bf16 v[164:167], a[44:47], v[120:123], v[164:167]\n"
"ds_read_b128 v[172:175], v6\n"
"ds_read_b128 v[184:187], v6 offset:1024\n"
"v_mfma_f32_16x16x32_bf16 v[164:167], a[48:51], v[96:99], v[164:167]\n"
"buffer_load_dwordx4 v[200:203], v0, s[4:7], s98 offen offset:2048 sc1\n"
"v_mfma_f32_16x16x32_bf16 v[164:167], a[52:55], v[152:155], v[164:167]\n"
"v_add_f32_e32 v208, v208, v216\n"
"v_add_f32_e32 v209, v209, v217\n"
"v_mfma_f32_16x16x32_bf16 v[164:167], a[56:59], v[176:179], v[164:167]\n"
"v_add_f32_e32 v210, v210, v218\n"
"v_add_f32_e32 v211, v211, v219\n"
"v_mfma_f32_16x16x32_bf16 v[164:167], a[60:63], v[180:183], v[164:167]\n"
"v_fmac_f32_e32 v116, v253, v192\n"
"v_fmac_f32_e32 v117, v253, v193\n"
"v_mfma_f32_16x16x32_bf16 v[196:199], a[64:67], v[84:87], v[196:199]\n"
"v_fmac_f32_e32 v118, v253, v194\n"
"v_fmac_f32_e32 v119, v253, v195\n"
"v_mfma_f32_16x16x32_bf16 v[196:199], a[68:71], v[92:95], v[196:199]\n"
"buffer_load_dwordx4 v[192:195], v0, s[4:7], s98 offen offset:3072 sc1\n"
"v_mfma_f32_16x16x32_bf16 v[196:199], a[72:75], v[80:83], v[196:199]\n"
"v_add_f32_e32 v116, v116, v160\n"
"v_add_f32_e32 v117, v117, v161\n"
"v_mfma_f32_16x16x32_bf16 v[196:199], a[76:79], v[120:123], v[196:199]\n"
"v_add_f32_e32 v118, v118, v162\n"
"v_add_f32_e32 v119, v119, v163\n"
"v_mfma_f32_16x16x32_bf16 v[196:199], a[80:83], v[96:99], v[196:199]\n"
"s_waitcnt lgkmcnt(0)\n"
"v_fmac_f32_e32 v104, v253, v108\n"
"v_fmac_f32_e32 v105, v253, v109\n"
"v_mfma_f32_16x16x32_bf16 v[196:199], a[84:87], v[152:155], v[196:199]\n"
"v_fmac_f32_e32 v106, v253, v110\n"
"v_fmac_f32_e32 v107, v253, v111\n"
"v_mfma_f32_16x16x32_bf16 v[196:199], a[88:91], v[176:179], v[196:199]\n"
"buffer_load_dwordx4 v[108:111], v1, s[4:7], s98 offen sc1\n"
"v_mfma_f32_16x16x32_bf16 v[196:199], a[92:95], v[180:183], v[196:199]\n"
"ds_read_b128 v[160:163], v6 offset:2048\n"
"ds_read_b128 v[204:207], v6 offset:3072\n"
"v_mfma_f32_16x16x32_bf16 v[208:211], a[96:99], v[84:87], v[208:211]\n"
"buffer_load_dwordx4 a[196:199], v0, s[4:7], s97 offen offset:1024 sc1\n"
"v_mfma_f32_16x16x32_bf16 v[208:211], a[100:103], v[92:95], v[208:211]\n"
"v_add_f32_e32 v104, v104, v148\n"
"v_add_f32_e32 v105, v105, v149\n"
"v_mfma_f32_16x16x32_bf16 v[208:211], a[104:107], v[80:83], v[208:211]\n"
"v_add_f32_e32 v106, v106, v150\n"
"v_add_f32_e32 v107, v107, v151\n"
"v_mfma_f32_16x16x32_bf16 v[208:211], a[108:111], v[120:123], v[208:211]\n"
"buffer_load_dwordx4 v[148:151], v1, s[4:7], s98 offen offset:1024 sc1\n"
"v_mfma_f32_16x16x32_bf16 v[208:211], a[112:115], v[96:99], v[208:211]\n"
"v_add_f32_e32 v24, v24, v172\n"
"v_add_f32_e32 v25, v25, v173\n"
"v_mfma_f32_16x16x32_bf16 v[208:211], a[116:119], v[152:155], v[208:211]\n"
"v_add_f32_e32 v26, v26, v174\n"
"v_add_f32_e32 v27, v27, v175\n"
"v_mfma_f32_16x16x32_bf16 v[208:211], a[120:123], v[176:179], v[208:211]\n"
"v_add_f32_e32 v164, v164, v184\n"
"v_add_f32_e32 v165, v165, v185\n"
"v_mfma_f32_16x16x32_bf16 v[208:211], a[124:127], v[180:183], v[208:211]\n"
"v_add_f32_e32 v166, v166, v186\n"
"v_add_f32_e32 v167, v167, v187\n"
"v_mfma_f32_16x16x32_bf16 v[116:119], v[136:139], v[84:87], v[116:119]\n"
"buffer_load_dwordx4 v[172:175], v1, s[4:7], s98 offen offset:2048 sc1\n"
"v_mfma_f32_16x16x32_bf16 v[116:119], v[44:47], v[92:95], v[116:119]\n"
"v_mul_f32_e32 v24, v24, v255\n"
"v_mul_f32_e32 v25, v25, v255\n"
"v_mfma_f32_16x16x32_bf16 v[116:119], v[28:31], v[80:83], v[116:119]\n"
"v_mul_f32_e32 v26, v26, v255\n"
"v_mul_f32_e32 v27, v27, v255\n"
"v_mfma_f32_16x16x32_bf16 v[116:119], v[32:35], v[120:123], v[116:119]\n"
"v_mul_f32_e32 v164, v164, v255\n"
"v_mul_f32_e32 v165, v165, v255\n"
"v_mfma_f32_16x16x32_bf16 v[116:119], v[40:43], v[96:99], v[116:119]\n"
"s_waitcnt lgkmcnt(0)\n"
"v_mul_f32_e32 v166, v166, v255\n"
"v_mul_f32_e32 v167, v167, v255\n"
"v_mfma_f32_16x16x32_bf16 v[116:119], v[48:51], v[152:155], v[116:119]\n"
"buffer_load_dwordx4 v[184:187], v1, s[4:7], s98 offen offset:3072 sc1\n"
"v_mfma_f32_16x16x32_bf16 v[116:119], v[64:67], v[176:179], v[116:119]\n"
"ds_read_b128 v[212:215], v6 offset:4096\n"
"ds_read_b128 v[216:219], v6 offset:5120\n"
"v_mfma_f32_16x16x32_bf16 v[116:119], v[36:39], v[180:183], v[116:119]\n"
"v_cvt_pk_bf16_f32 v224, v24, v25\n"
"v_cvt_pk_bf16_f32 v225, v26, v27\n"
"v_mfma_f32_16x16x32_bf16 v[104:107], v[140:143], v[84:87], v[104:107]\n"
"v_cvt_pk_bf16_f32 v226, v164, v165\n"
"v_cvt_pk_bf16_f32 v227, v166, v167\n"
"v_mfma_f32_16x16x32_bf16 v[104:107], v[52:55], v[92:95], v[104:107]\n"
"ds_write_b128 v8, v[224:227] offset:17408\n"
"v_mfma_f32_16x16x32_bf16 v[104:107], v[60:63], v[80:83], v[104:107]\n"
"v_add_f32_e32 v196, v196, v160\n"
"v_add_f32_e32 v197, v197, v161\n"
"v_mfma_f32_16x16x32_bf16 v[104:107], v[56:59], v[120:123], v[104:107]\n"
"v_add_f32_e32 v198, v198, v162\n"
"v_add_f32_e32 v199, v199, v163\n"
"v_mfma_f32_16x16x32_bf16 v[104:107], v[68:71], v[96:99], v[104:107]\n"
"v_add_f32_e32 v208, v208, v204\n"
"v_add_f32_e32 v209, v209, v205\n"
"v_mfma_f32_16x16x32_bf16 v[104:107], v[76:79], v[152:155], v[104:107]\n"
"v_add_f32_e32 v210, v210, v206\n"
"v_add_f32_e32 v211, v211, v207\n"
"v_mfma_f32_16x16x32_bf16 v[104:107], v[88:91], v[176:179], v[104:107]\n"
"v_mul_f32_e32 v196, v196, v255\n"
"v_mul_f32_e32 v197, v197, v255\n"
"v_mfma_f32_16x16x32_bf16 v[104:107], v[132:135], v[180:183], v[104:107]\n"
"v_mul_f32_e32 v198, v198, v255\n"
"v_mul_f32_e32 v199, v199, v255\n"
"v_mul_f32_e32 v208, v208, v255\n"
"v_mul_f32_e32 v209, v209, v255\n"
"v_mul_f32_e32 v210, v210, v255\n"
"v_mul_f32_e32 v211, v211, v255\n"
"v_cvt_pk_bf16_f32 v24, v196, v197\n"
"v_cvt_pk_bf16_f32 v25, v198, v199\n"
"v_cvt_pk_bf16_f32 v26, v208, v209\n"
"v_cvt_pk_bf16_f32 v27, v210, v211\n"
"ds_write_b128 v7, v[24:27] offset:50176\n"
".Lmk_a3:\n"
"s_waitcnt vmcnt(16)\n"
"; @A G1 3.0\n"
"v_mfma_f32_16x16x32_bf16 v[80:83], a[0:3], v[112:115], 0\n"
"v_mfma_f32_16x16x32_bf16 v[80:83], a[4:7], v[124:127], v[80:83]\n"
"s_add_u32 s98, s62, 0\n"
"v_mfma_f32_16x16x32_bf16 v[80:83], a[8:11], v[188:191], v[80:83]\n"
"s_add_u32 s96, s54, 0\n"
"buffer_load_dwordx4 v[84:87], v0, s[4:7], s96 offen sc1\n"
"v_mfma_f32_16x16x32_bf16 v[80:83], a[12:15], v[144:147], v[80:83]\n"
"s_waitcnt vmcnt(11)\n"
"v_mfma_f32_16x16x32_bf16 v[80:83], a[16:19], v[128:131], v[80:83]\n"
"buffer_load_dword v255, v10, s[4:7], s98 offen sc1\n"
"v_mfma_f32_16x16x32_bf16 v[80:83], a[20:23], v[220:223], v[80:83]\n"
"s_waitcnt lgkmcnt(2)\n"
"v_add_f32_e32 v116, v116, v212\n"
"v_add_f32_e32 v117, v117, v213\n"
"v_mfma_f32_16x16x32_bf16 v[80:83], a[24:27], v[100:103], v[80:83]\n"
"v_add_f32_e32 v118, v118, v214\n"
"v_add_f32_e32 v119, v119, v215\n"
"v_mfma_f32_16x16x32_bf16 v[80:83], a[28:31], v[72:75], v[80:83]\n"
"buffer_load_dwordx4 v[92:95], v0, s[4:7], s96 offen offset:1024 sc1\n"
"v_mfma_f32_16x16x32_bf16 v[96:99], a[32:35], v[112:115], 0\n"
"buffer_load_dwordx4 a[200:203], v0, s[4:7], s97 offen offset:2048 sc1\n"
"v_mfma_f32_16x16x32_bf16 v[96:99], a[36:39], v[124:127], v[96:99]\n"
"buffer_load_dword v253, v10, s[4:7], s98 offen offset:256 sc1\n"
"v_mfma_f32_16x16x32_bf16 v[96:99], a[40:43], v[188:191], v[96:99]\n"
"v_add_f32_e32 v104, v104, v216\n"
"v_add_f32_e32 v105, v105, v217\n"
"v_mfma_f32_16x16x32_bf16 v[96:99], a[44:47], v[144:147], v[96:99]\n"
"v_add_f32_e32 v106, v106, v218\n"
"v_add_f32_e32 v107, v107, v219\n"
"v_mfma_f32_16x16x32_bf16 v[96:99], a[48:51], v[128:131], v[96:99]\n"
"buffer_load_dwordx4 v[120:123], v0, s[4:7], s96 offen offset:2048 sc1\n"
"v_mfma_f32_16x16x32_bf16 v[96:99], a[52:55], v[220:223], v[96:99]\n"
"ds_write_b128 v3, v[80:83]\n"
"v_mfma_f32_16x16x32_bf16 v[96:99], a[56:59], v[100:103], v[96:99]\n"
"buffer_load_dword v252, v10, s[4:7], s98 offen offset:512 sc1\n"
"v_mfma_f32_16x16x32_bf16 v[96:99], a[60:63], v[72:75], v[96:99]\n"
"v_mul_f32_e32 v116, v116, v254\n"
"v_mul_f32_e32 v117, v117, v254\n"
"v_mfma_f32_16x16x32_bf16 v[80:83], a[64:67], v[112:115], 0\n"
"v_mul_f32_e32 v118, v118, v254\n"
"v_mul_f32_e32 v119, v119, v254\n"
"v_mfma_f32_16x16x32_bf16 v[80:83], a[68:71], v[124:127], v[80:83]\n"
"buffer_load_dwordx4 v[152:155], v0, s[4:7], s96 offen offset:3072 sc1\n"
"v_mfma_f32_16x16x32_bf16 v[80:83], a[72:75], v[188:191], v[80:83]\n"
"buffer_load_dword v251, v10, s[4:7], s98 offen offset:768 sc1\n"
"v_mfma_f32_16x16x32_bf16 v[80:83], a[76:79], v[144:147], v[80:83]\n"
"v_mul_f32_e32 v104, v104, v254\n"
"v_mul_f32_e32 v105, v105, v254\n"
"v_mfma_f32_16x16x32_bf16 v[80:83], a[80:83], v[128:131], v[80:83]\n"
"v_mul_f32_e32 v106, v106, v254\n"
"v_mul_f32_e32 v107, v107, v254\n"
"v_mfma_f32_16x16x32_bf16 v[80:83], a[84:87], v[220:223], v[80:83]\n"
"ds_write_b128 v3, v[96:99] offset:1024\n"
"v_mfma_f32_16x16x32_bf16 v[80:83], a[88:91], v[100:103], v[80:83]\n"
"buffer_load_dwordx4 v[96:99], v1, s[4:7], s96 offen sc1\n"
"v_mfma_f32_16x16x32_bf16 v[80:83], a[92:95], v[72:75], v[80:83]\n"
"buffer_load_dword v254, v10, s[4:7], s98 offen offset:1024 sc1\n"
"v_mfma_f32_16x16x32_bf16 v[160:163], a[96:99], v[112:115], 0\n"
"v_cvt_pk_bf16_f32 v164, v116, v117\n"
"v_cvt_pk_bf16_f32 v165, v118, v119\n"
"v_mfma_f32_16x16x32_bf16 v[160:163], a[100:103], v[124:127], v[160:163]\n"
"v_cvt_pk_bf16_f32 v166, v104, v105\n"
"v_cvt_pk_bf16_f32 v167, v106, v107\n"
"v_mfma_f32_16x16x32_bf16 v[160:163], a[104:107], v[188:191], v[160:163]\n"
"buffer_load_dwordx4 a[204:207], v0, s[4:7], s97 offen offset:3072 sc1\n"
"v_mfma_f32_16x16x32_bf16 v[160:163], a[108:111], v[144:147], v[160:163]\n"
"buffer_load_dwordx4 v[104:107], v1, s[4:7], s96 offen offset:1024 sc1\n"
"v_mfma_f32_16x16x32_bf16 v[160:163], a[112:115], v[128:131], v[160:163]\n"
"buffer_load_dword v250, v10, s[4:7], s98 offen offset:1280 sc1\n"
"v_mfma_f32_16x16x32_bf16 v[160:163], a[116:119], v[220:223], v[160:163]\n"
"ds_write_b128 v3, v[80:83] offset:2048\n"
"v_mfma_f32_16x16x32_bf16 v[160:163], a[120:123], v[100:103], v[160:163]\n"
"ds_write_b128 v2, v[164:167] offset:17408\n"
"v_mfma_f32_16x16x32_bf16 v[160:163], a[124:127], v[72:75], v[160:163]\n"
"buffer_load_dword v249, v10, s[4:7], s98 offen offset:1536 sc1\n"
"v_mfma_f32_16x16x32_bf16 v[80:83], v[136:139], v[112:115], 0\n"
"buffer_load_dwordx4 v[116:119], v1, s[4:7], s96 offen offset:2048 sc1\n"
"v_mfma_f32_16x16x32_bf16 v[80:83], v[44:47], v[124:127], v[80:83]\n"
"buffer_store_dwordx4 v[224:227], v8, s[4:7], s64 offen offset:1024 sc1\n"
"v_mfma_f32_16x16x32_bf16 v[80:83], v[28:31], v[188:191], v[80:83]\n"
"buffer_load_dword v248, v10, s[4:7], s98 offen offset:1792 sc1\n"
"v_mfma_f32_16x16x32_bf16 v[80:83], v[32:35], v[144:147], v[80:83]\n"
"v_mfma_f32_16x16x32_bf16 v[80:83], v[40:43], v[128:131], v[80:83]\n"
"v_mfma_f32_16x16x32_bf16 v[80:83], v[48:51], v[220:223], v[80:83]\n"
"buffer_load_dwordx4 v[164:167], v1, s[4:7], s96 offen offset:3072 sc1\n"
"v_mfma_f32_16x16x32_bf16 v[80:83], v[64:67], v[100:103], v[80:83]\n"
"ds_write_b128 v3, v[160:163] offset:3072\n"
"v_mfma_f32_16x16x32_bf16 v[80:83], v[36:39], v[72:75], v[80:83]\n"
"v_mfma_f32_16x16x32_bf16 v[160:163], v[140:143], v[112:115], 0\n"
"v_mfma_f32_16x16x32_bf16 v[160:163], v[52:55], v[124:127], v[160:163]\n"
"v_mfma_f32_16x16x32_bf16 v[160:163], v[60:63], v[188:191], v[160:163]\n"
"v_mfma_f32_16x16x32_bf16 v[160:163], v[56:59], v[144:147], v[160:163]\n"
"v_mfma_f32_16x16x32_bf16 v[160:163], v[68:71], v[128:131], v[160:163]\n"
"buffer_load_dwordx4 a[208:211], v1, s[4:7], s97 offen sc1\n"
"v_mfma_f32_16x16x32_bf16 v[160:163], v[76:79], v[220:223], v[160:163]\n"
"s_nop 0\n"
"ds_write_b128 v3, v[80:83] offset:4096\n"
"v_mfma_f32_16x16x32_bf16 v[160:163], v[88:91], v[100:103], v[160:163]\n"
"v_mfma_f32_16x16x32_bf16 v[160:163], v[132:135], v[72:75], v[160:163]\n"
"s_waitcnt vmcnt(25)\n"
"; @A G1 3.1\n"
"v_mfma_f32_16x16x32_bf16 v[72:75], a[0:3], v[156:159], 0\n"
"v_mfma_f32_16x16x32_bf16 v[72:75], a[4:7], v[168:171], v[72:75]\n"
"v_mfma_f32_16x16x32_bf16 v[72:75], a[8:11], v[200:203], v[72:75]\n"
"s_add_u32 s98, s55, 0\n"
"buffer_load_dwordx4 v[80:83], v0, s[4:7], s98 offen sc1\n"
"v_mfma_f32_16x16x32_bf16 v[72:75], a[12:15], v[192:195], v[72:75]\n"
"s_waitcnt vmcnt(21)\n"
"v_mfma_f32_16x16x32_bf16 v[72:75], a[16:19], v[108:111], v[72:75]\n"
"v_mfma_f32_16x16x32_bf16 v[72:75], a[20:23], v[148:151], v[72:75]\n"
"ds_write_b128 v3, v[160:163] offset:5120\n"
"v_mfma_f32_16x16x32_bf16 v[72:75], a[24:27], v[172:175], v[72:75]\n"
"v_mfma_f32_16x16x32_bf16 v[72:75], a[28:31], v[184:187], v[72:75]\n"
"buffer_load_dwordx4 v[100:103], v0, s[4:7], s98 offen offset:1024 sc1\n"
"s_add_u32 s96, s64, 0x4000\n"
"v_mfma_f32_16x16x32_bf16 v[112:115], a[32:35], v[156:159], 0\n"
"v_mfma_f32_16x16x32_bf16 v[112:115], a[36:39], v[168:171], v[112:115]\n"
"buffer_store_dwordx4 v[24:27], v7, s[4:7], s96 offen offset:1024 sc1\n"
"v_mfma_f32_16x16x32_bf16 v[112:115], a[40:43], v[200:203], v[112:115]\n"
"v_mfma_f32_16x16x32_bf16 v[112:115], a[44:47], v[192:195], v[112:115]\n"
"v_mfma_f32_16x16x32_bf16 v[112:115], a[48:51], v[108:111], v[112:115]\n"
"buffer_load_dwordx4 v[24:27], v0, s[4:7], s98 offen offset:2048 sc1\n"
"v_mfma_f32_16x16x32_bf16 v[112:115], a[52:55], v[148:151], v[112:115]\n"
"ds_write_b128 v3, v[72:75] offset:6144\n"
"v_mfma_f32_16x16x32_bf16 v[112:115], a[56:59], v[172:175], v[112:115]\n"
"buffer_load_dwordx4 a[212:215], v1, s[4:7], s97 offen offset:1024 sc1\n"
"v_mfma_f32_16x16x32_bf16 v[112:115], a[60:63], v[184:187], v[112:115]\n"
"v_mfma_f32_16x16x32_bf16 v[72:75], a[64:67], v[156:159], 0\n"
"v_mfma_f32_16x16x32_bf16 v[72:75], a[68:71], v[168:171], v[72:75]\n"
"buffer_load_dwordx4 v[124:127], v0, s[4:7], s98 offen offset:3072 sc1\n"
"v_mfma_f32_16x16x32_bf16 v[72:75], a[72:75], v[200:203], v[72:75]\n"
"v_mfma_f32_16x16x32_bf16 v[72:75], a[76:79], v[192:195], v[72:75]\n"
"v_mfma_f32_16x16x32_bf16 v[72:75], a[80:83], v[108:111], v[72:75]\n"
"v_mfma_f32_16x16x32_bf16 v[72:75], a[84:87], v[148:151], v[72:75]\n"
"s_nop 0\n"
"ds_write_b128 v3, v[112:115] offset:7168\n"
"v_mfma_f32_16x16x32_bf16 v[72:75], a[88:91], v[172:175], v[72:75]\n"
"buffer_load_dwordx4 v[112:115], v1, s[4:7], s98 offen sc1\n"
"v_mfma_f32_16x16x32_bf16 v[72:75], a[92:95], v[184:187], v[72:75]\n"
"v_mfma_f32_16x16x32_bf16 v[128:131], a[96:99], v[156:159], 0\n"
"v_mfma_f32_16x16x32_bf16 v[128:131], a[100:103], v[168:171], v[128:131]\n"
"v_mfma_f32_16x16x32_bf16 v[128:131], a[104:107], v[200:203], v[128:131]\n"
"v_mfma_f32_16x16x32_bf16 v[128:131], a[108:111], v[192:195], v[128:131]\n"
"buffer_load_dwordx4 v[144:147], v1, s[4:7], s98 offen offset:1024 sc1\n"
"v_mfma_f32_16x16x32_bf16 v[128:131], a[112:115], v[108:111], v[128:131]\n"
"v_mfma_f32_16x16x32_bf16 v[128:131], a[116:119], v[148:151], v[128:131]\n"
"s_nop 0\n"
"ds_write_b128 v3, v[72:75] offset:8192\n"
"v_mfma_f32_16x16x32_bf16 v[128:131], a[120:123], v[172:175], v[128:131]\n"
"v_mfma_f32_16x16x32_bf16 v[128:131], a[124:127], v[184:187], v[128:131]\n"
"v_mfma_f32_16x16x32_bf16 v[72:75], v[136:139], v[156:159], 0\n"
"buffer_load_dwordx4 v[160:163], v1, s[4:7], s98 offen offset:2048 sc1\n"
"v_mfma_f32_16x16x32_bf16 v[72:75], v[44:47], v[168:171], v[72:75]\n"
"buffer_load_dwordx4 a[216:219], v1, s[4:7], s97 offen offset:2048 sc1\n"
"v_mfma_f32_16x16x32_bf16 v[72:75], v[28:31], v[200:203], v[72:75]\n"
"v_mfma_f32_16x16x32_bf16 v[72:75], v[32:35], v[192:195], v[72:75]\n"
"v_mfma_f32_16x16x32_bf16 v[72:75], v[40:43], v[108:111], v[72:75]\n"
"s_waitcnt vmcnt(12)\n"
"v_add_f32_e32 v255, v255, v254\n"
"v_add_f32_e32 v253, v253, v250\n"
"v_mfma_f32_16x16x32_bf16 v[72:75], v[48:51], v[148:151], v[72:75]\n"
"buffer_load_dwordx4 v[176:179], v1, s[4:7], s98 offen offset:3072 sc1\n"
"v_mfma_f32_16x16x32_bf16 v[72:75], v[64:67], v[172:175], v[72:75]\n"
"ds_write_b128 v3, v[128:131] offset:9216\n"
"v_mfma_f32_16x16x32_bf16 v[72:75], v[36:39], v[184:187], v[72:75]\n"
"v_add_f32_e32 v252, v252, v249\n"
"v_add_f32_e32 v251, v251, v248\n"
"v_mfma_f32_16x16x32_bf16 v[128:131], v[140:143], v[156:159], 0\n"
"v_add_f32_e32 v255, v255, v252\n"
"v_add_f32_e32 v253, v253, v251\n"
"v_mfma_f32_16x16x32_bf16 v[128:131], v[52:55], v[168:171], v[128:131]\n"
"v_add_f32_e32 v255, v255, v253\n"
"v_mov_b32_e32 v253, v255\n"
"v_mfma_f32_16x16x32_bf16 v[128:131], v[60:63], v[200:203], v[128:131]\n"
"s_add_u32 s98, s71, 0\n"
"buffer_load_dwordx2 v[250:251], off, s[4:7], s98 sc1\n"
"v_mfma_f32_16x16x32_bf16 v[128:131], v[56:59], v[192:195], v[128:131]\n"
"v_permlane32_swap_b32_e32 v255, v253\n"
"v_add_f32_e32 v255, v255, v253\n"
"v_mfma_f32_16x16x32_bf16 v[128:131], v[68:71], v[108:111], v[128:131]\n"
"v_mul_f32_e32 v255, 0x3a800000, v255\n"
"v_mov_b32_e32 v253, v255\n"
"v_mfma_f32_16x16x32_bf16 v[128:131], v[76:79], v[148:151], v[128:131]\n"
"ds_write_b128 v3, v[72:75] offset:10240\n"
"v_mfma_f32_16x16x32_bf16 v[128:131], v[88:91], v[172:175], v[128:131]\n"
"buffer_store_dwordx2 v[18:19], v11, s[4:7], s68 offen offset:32 sc1\n"
"v_mfma_f32_16x16x32_bf16 v[128:131], v[132:135], v[184:187], v[128:131]\n"
"v_permlane16_swap_b32_e32 v255, v253\n"
"v_cvt_pk_bf16_f32 v254, -v255, 0\n"
"; @A G1 3.2\n"
"v_mfma_f32_16x16x32_bf16 v[72:75], a[0:3], v[84:87], 0\n"
"v_fma_f32 v253, -v255, v255, v253\n"
"v_lshlrev_b32_e32 v254, 16, v254\n"
"v_mfma_f32_16x16x32_bf16 v[72:75], a[4:7], v[92:95], v[72:75]\n"
"v_max_f32_e32 v253, 0, v253\n"
"v_cvt_pk_bf16_f32 v253, v253, 0\n"
"v_mfma_f32_16x16x32_bf16 v[72:75], a[8:11], v[120:123], v[72:75]\n"
"buffer_load_dwordx4 a[220:223], v1, s[4:7], s97 offen offset:3072 sc1\n"
"v_mfma_f32_16x16x32_bf16 v[72:75], a[12:15], v[152:155], v[72:75]\n"
"s_waitcnt vmcnt(15)\n"
"v_lshlrev_b32_e32 v253, 16, v253\n"
"v_add_f32_e32 v253, 0x3727c5ac, v253\n"
"v_mfma_f32_16x16x32_bf16 v[72:75], a[16:19], v[96:99], v[72:75]\n"
"s_add_u32 s97, s70, 0x6000\n"
"v_cvt_pk_bf16_f32 v253, v253, 0\n"
"v_lshlrev_b32_e32 v253, 16, v253\n"
"v_mfma_f32_16x16x32_bf16 v[72:75], a[20:23], v[104:107], v[72:75]\n"
"ds_write_b128 v3, v[128:131] offset:11264\n"
"v_mfma_f32_16x16x32_bf16 v[72:75], a[24:27], v[116:119], v[72:75]\n"
"buffer_load_dwordx4 a[224:227], v0, s[4:7], s97 offen sc1\n"
"v_mfma_f32_16x16x32_bf16 v[72:75], a[28:31], v[164:167], v[72:75]\n"
"v_sqrt_f32_e32 v253, v253\n"
"v_mfma_f32_16x16x32_bf16 v[108:111], a[32:35], v[84:87], 0\n"
"v_cvt_pk_bf16_f32 v253, v253, 0\n"
"v_lshlrev_b32_e32 v253, 16, v253\n"
"v_mfma_f32_16x16x32_bf16 v[108:111], a[36:39], v[92:95], v[108:111]\n"
"v_rcp_f32_e32 v255, v253\n"
"v_mfma_f32_16x16x32_bf16 v[108:111], a[40:43], v[120:123], v[108:111]\n"
"s_waitcnt lgkmcnt(0)\n"
"s_barrier\n"
"v_mfma_f32_16x16x32_bf16 v[108:111], a[44:47], v[152:155], v[108:111]\n"
"buffer_load_dwordx4 a[228:231], v0, s[4:7], s97 offen offset:1024 sc1\n"
"v_mfma_f32_16x16x32_bf16 v[108:111], a[48:51], v[96:99], v[108:111]\n"
"ds_read_b128 v[128:131], v4\n"
"v_mul_f32_e32 v253, 0x3e8293ee, v255\n"
"v_mfma_f32_16x16x32_bf16 v[108:111], a[52:55], v[104:107], v[108:111]\n"
"ds_write_b128 v3, v[72:75] offset:12288\n"
"v_mfma_f32_16x16x32_bf16 v[108:111], a[56:59], v[116:119], v[108:111]\n"
"ds_read_b128 v[72:75], v9 offset:128\n"
"ds_read_b128 v[148:151], v5\n"
"v_mfma_f32_16x16x32_bf16 v[108:111], a[60:63], v[164:167], v[108:111]\n"
"buffer_load_dwordx4 a[232:235], v0, s[4:7], s97 offen offset:2048 sc1\n"
"v_mfma_f32_16x16x32_bf16 v[156:159], a[64:67], v[84:87], 0\n"
"v_mfma_f32_16x16x32_bf16 v[156:159], a[68:71], v[92:95], v[156:159]\n"
"v_mfma_f32_16x16x32_bf16 v[156:159], a[72:75], v[120:123], v[156:159]\n"
"ds_read_b128 v[168:171], v4 offset:1024\n"
"ds_read_b128 v[172:175], v9 offset:192\n"
"v_mfma_f32_16x16x32_bf16 v[156:159], a[76:79], v[152:155], v[156:159]\n"
"buffer_load_dwordx4 a[236:239], v0, s[4:7], s97 offen offset:3072 sc1\n"
"v_mfma_f32_16x16x32_bf16 v[156:159], a[80:83], v[96:99], v[156:159]\n"
"ds_read_b128 v[180:183], v5 offset:1024\n"
"s_waitcnt lgkmcnt(3)\n"
"v_mfma_f32_16x16x32_bf16 v[156:159], a[84:87], v[104:107], v[156:159]\n"
"ds_write_b128 v3, v[108:111] offset:13312\n"
"v_mfma_f32_16x16x32_bf16 v[156:159], a[88:91], v[116:119], v[156:159]\n"
"v_fmac_f32_e32 v128, v254, v72\n"
"v_fmac_f32_e32 v129, v254, v73\n"
"v_mfma_f32_16x16x32_bf16 v[156:159], a[92:95], v[164:167], v[156:159]\n"
"buffer_load_dwordx4 a[240:243], v1, s[4:7], s97 offen sc1\n"
"v_mfma_f32_16x16x32_bf16 v[108:111], a[96:99], v[84:87], 0\n"
"v_fmac_f32_e32 v130, v254, v74\n"
"v_fmac_f32_e32 v131, v254, v75\n"
"v_mfma_f32_16x16x32_bf16 v[108:111], a[100:103], v[92:95], v[108:111]\n"
"ds_read_b128 v[72:75], v4 offset:2048\n"
"ds_read_b128 v[184:187], v9 offset:256\n"
"v_mfma_f32_16x16x32_bf16 v[108:111], a[104:107], v[120:123], v[108:111]\n"
"ds_read_b128 v[188:191], v5 offset:2048\n"
"v_add_f32_e32 v128, v128, v148\n"
"v_mfma_f32_16x16x32_bf16 v[108:111], a[108:111], v[152:155], v[108:111]\n"
"buffer_load_dwordx4 a[244:247], v1, s[4:7], s97 offen offset:1024 sc1\n"
"v_mfma_f32_16x16x32_bf16 v[108:111], a[112:115], v[96:99], v[108:111]\n"
"v_add_f32_e32 v129, v129, v149\n"
"v_add_f32_e32 v130, v130, v150\n"
"v_mfma_f32_16x16x32_bf16 v[108:111], a[116:119], v[104:107], v[108:111]\n"
"ds_write_b128 v3, v[156:159] offset:14336\n"
"v_mfma_f32_16x16x32_bf16 v[108:111], a[120:123], v[116:119], v[108:111]\n"
"s_waitcnt lgkmcnt(5)\n"
"v_add_f32_e32 v131, v131, v151\n"
"v_fmac_f32_e32 v168, v254, v172\n"
"v_mfma_f32_16x16x32_bf16 v[108:111], a[124:127], v[164:167], v[108:111]\n"
"buffer_load_dwordx4 a[248:251], v1, s[4:7], s97 offen offset:2048 sc1\n"
"v_mfma_f32_16x16x32_bf16 v[148:151], v[136:139], v[84:87], 0\n"
"ds_read_b128 v[156:159], v4 offset:3072\n"
"ds_read_b128 v[192:195], v9 offset:320\n"
"v_mfma_f32_16x16x32_bf16 v[148:151], v[44:47], v[92:95], v[148:151]\n"
"ds_read_b128 v[196:199], v5 offset:3072\n"
"v_fmac_f32_e32 v169, v254, v173\n"
"v_mfma_f32_16x16x32_bf16 v[148:151], v[28:31], v[120:123], v[148:151]\n"
"v_fmac_f32_e32 v170, v254, v174\n"
"v_fmac_f32_e32 v171, v254, v175\n"
"v_mfma_f32_16x16x32_bf16 v[148:151], v[32:35], v[152:155], v[148:151]\n"
"buffer_load_dwordx4 a[252:255], v1, s[4:7], s97 offen offset:3072 sc1\n"
"v_mfma_f32_16x16x32_bf16 v[148:151], v[40:43], v[96:99], v[148:151]\n"
"v_add_f32_e32 v168, v168, v180\n"
"v_add_f32_e32 v169, v169, v181\n"
"v_mfma_f32_16x16x32_bf16 v[148:151], v[48:51], v[104:107], v[148:151]\n"
"ds_write_b128 v3, v[108:111] offset:15360\n"
"v_mfma_f32_16x16x32_bf16 v[148:151], v[64:67], v[116:119], v[148:151]\n"
"s_waitcnt lgkmcnt(5)\n"
"v_add_f32_e32 v170, v170, v182\n"
"v_add_f32_e32 v171, v171, v183\n"
"v_mfma_f32_16x16x32_bf16 v[148:151], v[36:39], v[164:167], v[148:151]\n"
"ds_read_b128 v[108:111], v4 offset:4096\n"
"ds_read_b128 v[172:175], v9\n"
"v_mfma_f32_16x16x32_bf16 v[180:183], v[140:143], v[84:87], 0\n"
"ds_read_b128 v[84:87], v5 offset:4096\n"
"v_fmac_f32_e32 v72, v254, v184\n"
"v_mfma_f32_16x16x32_bf16 v[180:183], v[52:55], v[92:95], v[180:183]\n"
"v_fmac_f32_e32 v73, v254, v185\n"
"v_fmac_f32_e32 v74, v254, v186\n"
"v_mfma_f32_16x16x32_bf16 v[180:183], v[60:63], v[120:123], v[180:183]\n"
"v_fmac_f32_e32 v75, v254, v187\n"
"v_add_f32_e32 v72, v72, v188\n"
"v_mfma_f32_16x16x32_bf16 v[180:183], v[56:59], v[152:155], v[180:183]\n"
"v_add_f32_e32 v73, v73, v189\n"
"v_add_f32_e32 v74, v74, v190\n"
"v_mfma_f32_16x16x32_bf16 v[180:183], v[68:71], v[96:99], v[180:183]\n"
"s_waitcnt vmcnt(10)\n"
"v_cmp_ne_u64_e32 vcc, v[250:251], v[18:19]\n"
"v_mfma_f32_16x16x32_bf16 v[180:183], v[76:79], v[104:107], v[180:183]\n"
"ds_write_b128 v3, v[148:151] offset:16384\n"
"v_mfma_f32_16x16x32_bf16 v[180:183], v[88:91], v[116:119], v[180:183]\n"
"s_cbranch_vccnz .Lmk_f78s\n"
".Lmk_f78r:\n"
"s_add_u32 m0, s77, 0x6000\n"
"v_mfma_f32_16x16x32_bf16 v[180:183], v[132:135], v[164:167], v[180:183]\n"
"s_add_u32 s98, s65, 0x2000\n"
"ds_read_b128 v[92:95], v4 offset:5120\n"
"; @A G1 3.3\n"
"v_mfma_f32_16x16x32_bf16 v[128:131], a[0:3], v[80:83], v[128:131]\n"
"buffer_load_dwordx4 v2, s[4:7], s98 offen sc1 lds\n"
"v_mfma_f32_16x16x32_bf16 v[128:131], a[4:7], v[100:103], v[128:131]\n"
"buffer_load_dwordx4 v2, s[4:7], s98 offen offset:1024 sc1 lds\n"
"v_mfma_f32_16x16x32_bf16 v[128:131], a[8:11], v[24:27], v[128:131]\n"
"ds_read_b128 v[96:99], v9 offset:64\n"
"ds_read_b128 v[104:107], v5 offset:5120\n"
"v_mfma_f32_16x16x32_bf16 v[128:131], a[12:15], v[124:127], v[128:131]\n"
"s_waitcnt lgkmcnt(8)\n"
"v_add_f32_e32 v75, v75, v191\n"
"v_mfma_f32_16x16x32_bf16 v[128:131], a[16:19], v[112:115], v[128:131]\n"
"v_fmac_f32_e32 v156, v254, v192\n"
"v_fmac_f32_e32 v157, v254, v193\n"
"v_mfma_f32_16x16x32_bf16 v[128:131], a[20:23], v[144:147], v[128:131]\n"
"ds_write_b128 v3, v[180:183] offset:17408\n"
"v_mfma_f32_16x16x32_bf16 v[128:131], a[24:27], v[160:163], v[128:131]\n"
"v_fmac_f32_e32 v158, v254, v194\n"
"v_fmac_f32_e32 v159, v254, v195\n"
"v_mfma_f32_16x16x32_bf16 v[128:131], a[28:31], v[176:179], v[128:131]\n"
"s_add_u32 m0, s77, 0xe000\n"
"s_add_u32 s98, s65, 0x6000\n"
"v_add_f32_e32 v156, v156, v196\n"
"v_mfma_f32_16x16x32_bf16 v[168:171], a[32:35], v[80:83], v[168:171]\n"
"buffer_load_dwordx4 v2, s[4:7], s98 offen sc1 lds\n"
"v_mfma_f32_16x16x32_bf16 v[168:171], a[36:39], v[100:103], v[168:171]\n"
"buffer_load_dwordx4 v2, s[4:7], s98 offen offset:1024 sc1 lds\n"
"v_mfma_f32_16x16x32_bf16 v[168:171], a[40:43], v[24:27], v[168:171]\n"
"s_waitcnt lgkmcnt(0)\n"
"s_barrier\n"
"v_mfma_f32_16x16x32_bf16 v[168:171], a[44:47], v[124:127], v[168:171]\n"
"ds_read_b128 v[116:119], v6\n"
"ds_read_b128 v[120:123], v6 offset:1024\n"
"v_mfma_f32_16x16x32_bf16 v[168:171], a[48:51], v[112:115], v[168:171]\n"
"v_add_f32_e32 v157, v157, v197\n"
"v_add_f32_e32 v158, v158, v198\n"
"v_mfma_f32_16x16x32_bf16 v[168:171], a[52:55], v[144:147], v[168:171]\n"
"v_add_f32_e32 v159, v159, v199\n"
"v_fmac_f32_e32 v108, v254, v172\n"
"v_mfma_f32_16x16x32_bf16 v[168:171], a[56:59], v[160:163], v[168:171]\n"
"s_add_u32 s98, s69, 0x600000\n"
"buffer_load_dwordx4 a[0:3], v0, s[4:7], s98 offen sc1\n"
"v_mfma_f32_16x16x32_bf16 v[168:171], a[60:63], v[176:179], v[168:171]\n"
"v_fmac_f32_e32 v109, v254, v173\n"
"v_fmac_f32_e32 v110, v254, v174\n"
"v_mfma_f32_16x16x32_bf16 v[72:75], a[64:67], v[80:83], v[72:75]\n"
"v_fmac_f32_e32 v111, v254, v175\n"
"v_add_f32_e32 v108, v108, v84\n"
"v_mfma_f32_16x16x32_bf16 v[72:75], a[68:71], v[100:103], v[72:75]\n"
"v_add_f32_e32 v109, v109, v85\n"
"v_add_f32_e32 v110, v110, v86\n"
"v_mfma_f32_16x16x32_bf16 v[72:75], a[72:75], v[24:27], v[72:75]\n"
"v_add_f32_e32 v111, v111, v87\n"
"v_fmac_f32_e32 v92, v254, v96\n"
"v_mfma_f32_16x16x32_bf16 v[72:75], a[76:79], v[124:127], v[72:75]\n"
"buffer_load_dwordx4 a[4:7], v0, s[4:7], s98 offen offset:1024 sc1\n"
"v_mfma_f32_16x16x32_bf16 v[72:75], a[80:83], v[112:115], v[72:75]\n"
"s_waitcnt lgkmcnt(0)\n"
"v_fmac_f32_e32 v93, v254, v97\n"
"v_fmac_f32_e32 v94, v254, v98\n"
"v_mfma_f32_16x16x32_bf16 v[72:75], a[84:87], v[144:147], v[72:75]\n"
"v_fmac_f32_e32 v95, v254, v99\n"
"v_add_f32_e32 v92, v92, v104\n"
"v_mfma_f32_16x16x32_bf16 v[72:75], a[88:91], v[160:163], v[72:75]\n"
"ds_read_b128 v[84:87], v6 offset:2048\n"
"ds_read_b128 v[96:99], v6 offset:3072\n"
"v_mfma_f32_16x16x32_bf16 v[72:75], a[92:95], v[176:179], v[72:75]\n"
"v_add_f32_e32 v93, v93, v105\n"
"v_add_f32_e32 v94, v94, v106\n"
"v_mfma_f32_16x16x32_bf16 v[156:159], a[96:99], v[80:83], v[156:159]\n"
"buffer_load_dwordx4 a[8:11], v0, s[4:7], s98 offen offset:2048 sc1\n"
"v_mfma_f32_16x16x32_bf16 v[156:159], a[100:103], v[100:103], v[156:159]\n"
"v_add_f32_e32 v95, v95, v107\n"
"v_add_f32_e32 v128, v128, v116\n"
"v_mfma_f32_16x16x32_bf16 v[156:159], a[104:107], v[24:27], v[156:159]\n"
"v_add_f32_e32 v129, v129, v117\n"
"v_add_f32_e32 v130, v130, v118\n"
"v_mfma_f32_16x16x32_bf16 v[156:159], a[108:111], v[124:127], v[156:159]\n"
"v_add_f32_e32 v131, v131, v119\n"
"v_add_f32_e32 v168, v168, v120\n"
"v_mfma_f32_16x16x32_bf16 v[156:159], a[112:115], v[112:115], v[156:159]\n"
"v_add_f32_e32 v169, v169, v121\n"
"v_add_f32_e32 v170, v170, v122\n"
"v_mfma_f32_16x16x32_bf16 v[156:159], a[116:119], v[144:147], v[156:159]\n"
"buffer_load_dwordx4 a[12:15], v0, s[4:7], s98 offen offset:3072 sc1\n"
"v_mfma_f32_16x16x32_bf16 v[156:159], a[120:123], v[160:163], v[156:159]\n"
"v_add_f32_e32 v171, v171, v123\n"
"v_mul_f32_e32 v128, v128, v255\n"
"v_mfma_f32_16x16x32_bf16 v[156:159], a[124:127], v[176:179], v[156:159]\n"
"v_mul_f32_e32 v129, v129, v255\n"
"v_mul_f32_e32 v130, v130, v255\n"
"v_mfma_f32_16x16x32_bf16 v[108:111], v[136:139], v[80:83], v[108:111]\n"
"v_mul_f32_e32 v131, v131, v255\n"
"v_mul_f32_e32 v168, v168, v255\n"
"v_mfma_f32_16x16x32_bf16 v[108:111], v[44:47], v[100:103], v[108:111]\n"
"v_mul_f32_e32 v169, v169, v255\n"
"v_mul_f32_e32 v170, v170, v255\n"
"v_mfma_f32_16x16x32_bf16 v[108:111], v[28:31], v[24:27], v[108:111]\n"
"buffer_load_dwordx4 a[16:19], v1, s[4:7], s98 offen sc1\n"
"v_mfma_f32_16x16x32_bf16 v[108:111], v[32:35], v[124:127], v[108:111]\n"
"v_mul_f32_e32 v171, v171, v255\n"
"v_cvt_pk_bf16_f32 v28, v128, v129\n"
"v_mfma_f32_16x16x32_bf16 v[108:111], v[40:43], v[112:115], v[108:111]\n"
"s_waitcnt lgkmcnt(0)\n"
"v_cvt_pk_bf16_f32 v29, v130, v131\n"
"v_cvt_pk_bf16_f32 v30, v168, v169\n"
"v_mfma_f32_16x16x32_bf16 v[108:111], v[48:51], v[144:147], v[108:111]\n"
"v_cvt_pk_bf16_f32 v31, v170, v171\n"
"v_add_f32_e32 v72, v72, v84\n"
"v_mfma_f32_16x16x32_bf16 v[108:111], v[64:67], v[160:163], v[108:111]\n"
"ds_read_b128 v[32:35], v6 offset:4096\n"
"ds_read_b128 v[40:43], v6 offset:5120\n"
"v_mfma_f32_16x16x32_bf16 v[108:111], v[36:39], v[176:179], v[108:111]\n"
"buffer_load_dwordx4 a[20:23], v1, s[4:7], s98 offen offset:1024 sc1\n"
"v_mfma_f32_16x16x32_bf16 v[92:95], v[140:143], v[80:83], v[92:95]\n"
"ds_write_b128 v7, v[28:31] offset:16384\n"
"v_mfma_f32_16x16x32_bf16 v[92:95], v[52:55], v[100:103], v[92:95]\n"
"buffer_store_dwordx4 v[28:31], v7, s[4:7], s64 offen sc1\n"
"v_mfma_f32_16x16x32_bf16 v[92:95], v[60:63], v[24:27], v[92:95]\n"
"v_add_f32_e32 v73, v73, v85\n"
"v_add_f32_e32 v74, v74, v86\n"
"v_mfma_f32_16x16x32_bf16 v[92:95], v[56:59], v[124:127], v[92:95]\n"
"v_add_f32_e32 v75, v75, v87\n"
"v_add_f32_e32 v156, v156, v96\n"
"v_mfma_f32_16x16x32_bf16 v[92:95], v[68:71], v[112:115], v[92:95]\n"
"buffer_load_dwordx4 a[24:27], v1, s[4:7], s98 offen offset:2048 sc1\n"
"v_mfma_f32_16x16x32_bf16 v[92:95], v[76:79], v[144:147], v[92:95]\n"
"v_add_f32_e32 v157, v157, v97\n"
"v_add_f32_e32 v158, v158, v98\n"
"v_mfma_f32_16x16x32_bf16 v[92:95], v[88:91], v[160:163], v[92:95]\n"
"v_add_f32_e32 v159, v159, v99\n"
"v_mul_f32_e32 v72, v72, v255\n"
"v_mfma_f32_16x16x32_bf16 v[92:95], v[132:135], v[176:179], v[92:95]\n"
"v_mul_f32_e32 v73, v73, v255\n"
"v_mul_f32_e32 v74, v74, v255\n"
"v_mul_f32_e32 v75, v75, v255\n"
"v_mul_f32_e32 v156, v156, v255\n"
"v_mul_f32_e32 v157, v157, v255\n"
"v_mul_f32_e32 v158, v158, v255\n"
"v_mul_f32_e32 v159, v159, v255\n"
"v_cvt_pk_bf16_f32 v24, v72, v73\n"
"v_cvt_pk_bf16_f32 v25, v74, v75\n"
"v_cvt_pk_bf16_f32 v26, v156, v157\n"
"v_cvt_pk_bf16_f32 v27, v158, v159\n"
"ds_write_b128 v7, v[24:27] offset:49152\n"
"buffer_load_dwordx4 a[28:31], v1, s[4:7], s98 offen offset:3072 sc1\n"
"v_xor_b32_e32 v255, 32, v16\n"
"v_xor_b32_e32 v254, 32, v15\n"
"v_mov_b32_e32 v88, 0x3f803f80\n"
"v_mov_b32_e32 v89, 0x3f803f80\n"
"v_mov_b32_e32 v90, 0x3f803f80\n"
"v_mov_b32_e32 v91, 0x3f803f80\n"
"v_mov_b32_e32 v252, 0x53800000\n"
".Lmk_a4:\n"
"; @A tail\n"
"s_add_u32 s98, s64, 0x4000\n"
"buffer_store_dwordx4 v[24:27], v7, s[4:7], s98 offen sc1\n"
"s_waitcnt lgkmcnt(2)\n"
"v_add_f32_e32 v108, v108, v32\n"
"v_add_f32_e32 v109, v109, v33\n"
"v_add_f32_e32 v110, v110, v34\n"
"v_add_f32_e32 v111, v111, v35\n"
"v_add_f32_e32 v92, v92, v40\n"
"v_add_f32_e32 v93, v93, v41\n"
"v_add_f32_e32 v94, v94, v42\n"
"v_add_f32_e32 v95, v95, v43\n"
"v_mul_f32_e32 v108, v108, v253\n"
"v_mul_f32_e32 v109, v109, v253\n"
"v_mul_f32_e32 v110, v110, v253\n"
"v_mul_f32_e32 v111, v111, v253\n"
"v_mul_f32_e32 v92, v92, v253\n"
"v_mul_f32_e32 v93, v93, v253\n"
"v_mul_f32_e32 v94, v94, v253\n"
"v_mul_f32_e32 v95, v95, v253\n"
"v_cvt_pk_bf16_f32 v24, v108, v109\n"
"v_cvt_pk_bf16_f32 v25, v110, v111\n"
"v_cvt_pk_bf16_f32 v26, v92, v93\n"
"v_cvt_pk_bf16_f32 v27, v94, v95\n"
"ds_write_b128 v2, v[24:27] offset:16384\n"
"s_waitcnt lgkmcnt(0)\n"
"s_barrier\n"
"ds_read_b128 v[28:31], v2 offset:49152\n"
"ds_read_b128 v[32:35], v2 offset:50176\n"
"ds_read_b128 v[92:95], v2 offset:16384\n"
"ds_read_b128 v[96:99], v2 offset:17408\n"
"; @A AT b\n"
"s_mov_b32 s2, 0\n"
"v_mov_b32_e32 v253, 0xff800000\n"
"v_mov_b32_e32 v251, 0xff800000\n"
"v_mov_b32_e32 v250, 0x7f800000\n"
"v_mov_b32_e32 v249, 0x7f800000\n"
".Lmk_a96:\n"
"ds_read_b128 v[24:27], v15 offset:24576\n"
"ds_read_b128 v[36:39], v254 offset:24832\n"
"ds_read_b128 v[40:43], v15 offset:22528\n"
"ds_read_b128 v[44:47], v254 offset:22784\n"
"s_cmp_gt_i32 s78, 11\n"
"s_cselect_b32 s98, 0x80000000, 0\n"
"s_or_b32 s98, s98, 0x7f800000\n"
"s_waitcnt lgkmcnt(2)\n"
"v_mfma_f32_16x16x32_bf16 v[48:51], v[24:27], v[28:31], 0\n"
"v_mfma_f32_16x16x32_bf16 v[52:55], v[24:27], v[32:35], 0\n"
"v_mfma_f32_16x16x32_bf16 v[24:27], v[36:39], v[28:31], 0\n"
"v_mfma_f32_16x16x32_bf16 v[56:59], v[36:39], v[32:35], 0\n"
"s_waitcnt lgkmcnt(0)\n"
"s_nop 3\n"
"v_max_f32_e32 v248, v48, v49\n"
"v_max_f32_e32 v247, v52, v53\n"
"v_max3_f32 v248, v248, v50, v51\n"
"v_max3_f32 v247, v247, v54, v55\n"
"v_max3_f32 v248, v248, v24, v25\n"
"v_max3_f32 v247, v247, v56, v57\n"
"v_max3_f32 v248, v248, v26, v27\n"
"v_max3_f32 v247, v247, v58, v59\n"
"v_mov_b32_e32 v246, v248\n"
"v_mov_b32_e32 v245, v247\n"
"s_nop 0\n"
"v_permlane16_swap_b32_e32 v248, v246\n"
"v_permlane16_swap_b32_e32 v247, v245\n"
"v_max_f32_e32 v248, v248, v246\n"
"v_max_f32_e32 v247, v247, v245\n"
"v_mov_b32_e32 v246, v248\n"
"v_mov_b32_e32 v245, v247\n"
"s_nop 0\n"
"v_permlane32_swap_b32_e32 v248, v246\n"
"v_permlane32_swap_b32_e32 v247, v245\n"
"v_max_f32_e32 v248, v248, v246\n"
"v_max_f32_e32 v247, v247, v245\n"
"v_med3_f32 v248, v248, v253, v250\n"
"v_med3_f32 v247, v247, v251, v249\n"
"v_min_f32_e64 v36, s98, -v248\n"
"v_min_f32_e64 v37, s98, -v248\n"
"v_min_f32_e64 v38, s98, -v248\n"
"v_min_f32_e64 v39, s98, -v248\n"
"v_min_f32_e64 v60, s98, -v247\n"
"v_min_f32_e64 v61, s98, -v247\n"
"v_min_f32_e64 v62, s98, -v247\n"
"v_min_f32_e64 v63, s98, -v247\n"
"v_mfma_f32_16x16x32_bf16 v[64:67], v[40:43], v[28:31], v[36:39]\n"
"ds_read_b128 v[68:71], v15 offset:20480\n"
"s_add_u32 s98, s69, 0x602000\n"
"buffer_load_dwordx4 a[32:35], v0, s[4:7], s98 offen sc1\n"
"v_sub_f32_e32 v48, v48, v248\n"
"v_sub_f32_e32 v49, v49, v248\n"
"v_sub_f32_e32 v50, v50, v248\n"
"v_mfma_f32_16x16x32_bf16 v[72:75], v[40:43], v[32:35], v[60:63]\n"
"buffer_load_dwordx4 a[36:39], v0, s[4:7], s98 offen offset:1024 sc1\n"
"v_sub_f32_e32 v51, v51, v248\n"
"v_sub_f32_e32 v24, v24, v248\n"
"v_sub_f32_e32 v25, v25, v248\n"
"v_sub_f32_e32 v26, v26, v248\n"
"v_sub_f32_e32 v27, v27, v248\n"
"v_mfma_f32_16x16x32_bf16 v[40:43], v[44:47], v[28:31], v[36:39]\n"
"ds_read_b128 v[76:79], v254 offset:20736\n"
"v_sub_f32_e32 v52, v52, v247\n"
"v_sub_f32_e32 v53, v53, v247\n"
"v_sub_f32_e32 v54, v54, v247\n"
"v_sub_f32_e32 v55, v55, v247\n"
"v_mfma_f32_16x16x32_bf16 v[80:83], v[44:47], v[32:35], v[60:63]\n"
"buffer_load_dwordx4 a[40:43], v0, s[4:7], s98 offen offset:2048 sc1\n"
"v_sub_f32_e32 v56, v56, v247\n"
"v_sub_f32_e32 v57, v57, v247\n"
"v_sub_f32_e32 v58, v58, v247\n"
"v_sub_f32_e32 v59, v59, v247\n"
"s_waitcnt lgkmcnt(0)\n"
"v_mfma_f32_16x16x32_bf16 v[44:47], v[68:71], v[28:31], v[36:39]\n"
"ds_read_b128 v[84:87], v15 offset:18432\n"
"buffer_load_dwordx4 a[44:47], v0, s[4:7], s98 offen offset:3072 sc1\n"
"ds_read_b128 v[100:103], v254 offset:18688\n"
"v_exp_f32_e32 v64, v64\n"
"v_exp_f32_e32 v65, v65\n"
"v_exp_f32_e32 v66, v66\n"
"v_exp_f32_e32 v67, v67\n"
"v_exp_f32_e32 v40, v40\n"
"v_mfma_f32_16x16x32_bf16 v[104:107], v[68:71], v[32:35], v[60:63]\n"
"ds_read_b64_tr_b16 v[68:69], v16 offset:22528\n"
"buffer_load_dwordx4 a[48:51], v1, s[4:7], s98 offen sc1\n"
"v_exp_f32_e32 v41, v41\n"
"v_exp_f32_e32 v42, v42\n"
"v_exp_f32_e32 v43, v43\n"
"v_cvt_pk_bf16_f32 v108, v64, v65\n"
"v_cvt_pk_bf16_f32 v109, v66, v67\n"
"v_cvt_pk_bf16_f32 v110, v40, v41\n"
"v_mfma_f32_16x16x32_bf16 v[64:67], v[76:79], v[28:31], v[36:39]\n"
"ds_read_b64_tr_b16 v[70:71], v255 offset:22784\n"
"ds_read_b64_tr_b16 v[40:41], v255 offset:22528\n"
"buffer_load_dwordx4 a[52:55], v1, s[4:7], s98 offen offset:1024 sc1\n"
"v_cvt_pk_bf16_f32 v111, v42, v43\n"
"v_exp_f32_e32 v72, v72\n"
"v_exp_f32_e32 v73, v73\n"
"v_exp_f32_e32 v74, v74\n"
"v_exp_f32_e32 v75, v75\n"
"v_mfma_f32_16x16x32_bf16 v[112:115], v[76:79], v[32:35], v[60:63]\n"
"ds_read_b64_tr_b16 v[42:43], v16 offset:22784\n"
"v_exp_f32_e32 v80, v80\n"
"v_exp_f32_e32 v81, v81\n"
"v_exp_f32_e32 v82, v82\n"
"v_exp_f32_e32 v83, v83\n"
"v_cvt_pk_bf16_f32 v76, v72, v73\n"
"v_cvt_pk_bf16_f32 v77, v74, v75\n"
"v_cvt_pk_bf16_f32 v78, v80, v81\n"
"v_cvt_pk_bf16_f32 v79, v82, v83\n"
"s_waitcnt lgkmcnt(4)\n"
"v_mfma_f32_16x16x32_bf16 v[72:75], v[84:87], v[28:31], v[36:39]\n"
"ds_read_b128 v[80:83], v15 offset:16384\n"
"v_exp_f32_e32 v44, v44\n"
"v_exp_f32_e32 v45, v45\n"
"v_exp_f32_e32 v46, v46\n"
"v_mfma_f32_16x16x32_bf16 v[116:119], v[84:87], v[32:35], v[60:63]\n"
"buffer_load_dwordx4 a[56:59], v1, s[4:7], s98 offen offset:2048 sc1\n"
"v_exp_f32_e32 v47, v47\n"
"v_mfma_f32_16x16x32_bf16 v[84:87], v[100:103], v[28:31], v[36:39]\n"
"ds_read_b128 v[120:123], v254 offset:16640\n"
"v_exp_f32_e32 v64, v64\n"
"v_exp_f32_e32 v65, v65\n"
"v_mfma_f32_16x16x32_bf16 v[124:127], v[100:103], v[32:35], v[60:63]\n"
"ds_read_b64_tr_b16 v[100:101], v16 offset:20480\n"
"v_exp_f32_e32 v66, v66\n"
"v_exp_f32_e32 v67, v67\n"
"v_cvt_pk_bf16_f32 v128, v44, v45\n"
"s_waitcnt lgkmcnt(3)\n"
"v_mfma_f32_16x16x32_bf16 v[132:135], v[88:91], v[108:111], 0\n"
"buffer_load_dwordx4 a[60:63], v1, s[4:7], s98 offen offset:3072 sc1\n"
"v_cvt_pk_bf16_f32 v129, v46, v47\n"
"v_cvt_pk_bf16_f32 v130, v64, v65\n"
"v_cvt_pk_bf16_f32 v131, v66, v67\n"
"v_mfma_f32_16x16x32_bf16 v[44:47], v[68:71], v[108:111], 0\n"
"ds_read_b64_tr_b16 v[102:103], v255 offset:20736\n"
"v_exp_f32_e32 v104, v104\n"
"v_exp_f32_e32 v105, v105\n"
"v_mfma_f32_16x16x32_bf16 v[64:67], v[40:43], v[108:111], 0\n"
"ds_read_b64_tr_b16 v[108:109], v255 offset:20480\n"
"s_add_u32 s98, s69, 0x604000\n"
"buffer_load_dwordx4 a[64:67], v0, s[4:7], s98 offen sc1\n"
"v_exp_f32_e32 v106, v106\n"
"v_exp_f32_e32 v107, v107\n"
"v_mfma_f32_16x16x32_bf16 v[136:139], v[88:91], v[76:79], 0\n"
"v_exp_f32_e32 v112, v112\n"
"v_exp_f32_e32 v113, v113\n"
"v_mfma_f32_16x16x32_bf16 v[140:143], v[68:71], v[76:79], 0\n"
"ds_read_b64_tr_b16 v[110:111], v16 offset:20736\n"
"v_exp_f32_e32 v114, v114\n"
"v_exp_f32_e32 v115, v115\n"
"v_mfma_f32_16x16x32_bf16 v[68:71], v[40:43], v[76:79], 0\n"
"s_waitcnt vmcnt(19)\n"
"v_cvt_pk_bf16_f32 v40, v104, v105\n"
"v_cvt_pk_bf16_f32 v41, v106, v107\n"
"v_cvt_pk_bf16_f32 v42, v112, v113\n"
"v_cvt_pk_bf16_f32 v43, v114, v115\n"
"s_cmp_lg_u32 s2, 0\n"
"s_cbranch_scc1 .Lmk_s98\n"
"s_barrier\n"
".Lmk_s98:\n"
"s_waitcnt lgkmcnt(4)\n"
"v_mfma_f32_16x16x32_bf16 v[76:79], v[80:83], v[28:31], v[36:39]\n"
"ds_read_b128 v[104:107], v15 offset:14336\n"
"v_exp_f32_e32 v72, v72\n"
"v_exp_f32_e32 v73, v73\n"
"v_exp_f32_e32 v74, v74\n"
"v_mfma_f32_16x16x32_bf16 v[112:115], v[80:83], v[32:35], v[60:63]\n"
"buffer_load_dwordx4 a[68:71], v0, s[4:7], s98 offen offset:1024 sc1\n"
"v_exp_f32_e32 v75, v75\n"
"v_mfma_f32_16x16x32_bf16 v[80:83], v[120:123], v[28:31], v[36:39]\n"
"ds_read_b128 v[144:147], v254 offset:14592\n"
"v_exp_f32_e32 v84, v84\n"
"v_exp_f32_e32 v85, v85\n"
"v_mfma_f32_16x16x32_bf16 v[148:151], v[120:123], v[32:35], v[60:63]\n"
"ds_read_b64_tr_b16 v[120:121], v16 offset:18432\n"
"v_exp_f32_e32 v86, v86\n"
"v_exp_f32_e32 v87, v87\n"
"s_waitcnt lgkmcnt(3)\n"
"v_mfma_f32_16x16x32_bf16 v[132:135], v[88:91], v[128:131], v[132:135]\n"
"buffer_load_dwordx4 a[72:75], v0, s[4:7], s98 offen offset:2048 sc1\n"
"v_cvt_pk_bf16_f32 v152, v72, v73\n"
"v_cvt_pk_bf16_f32 v153, v74, v75\n"
"v_cvt_pk_bf16_f32 v154, v84, v85\n"
"v_mfma_f32_16x16x32_bf16 v[44:47], v[100:103], v[128:131], v[44:47]\n"
"ds_read_b64_tr_b16 v[122:123], v255 offset:18688\n"
"v_cvt_pk_bf16_f32 v155, v86, v87\n"
"v_exp_f32_e32 v116, v116\n"
"v_exp_f32_e32 v117, v117\n"
"v_mfma_f32_16x16x32_bf16 v[64:67], v[108:111], v[128:131], v[64:67]\n"
"ds_read_b64_tr_b16 v[72:73], v255 offset:18432\n"
"v_exp_f32_e32 v118, v118\n"
"v_exp_f32_e32 v119, v119\n"
"v_mfma_f32_16x16x32_bf16 v[136:139], v[88:91], v[40:43], v[136:139]\n"
"buffer_load_dwordx4 a[76:79], v0, s[4:7], s98 offen offset:3072 sc1\n"
"v_exp_f32_e32 v124, v124\n"
"v_exp_f32_e32 v125, v125\n"
"v_mfma_f32_16x16x32_bf16 v[140:143], v[100:103], v[40:43], v[140:143]\n"
"ds_read_b64_tr_b16 v[74:75], v16 offset:18688\n"
"v_exp_f32_e32 v126, v126\n"
"v_exp_f32_e32 v127, v127\n"
"v_mfma_f32_16x16x32_bf16 v[68:71], v[108:111], v[40:43], v[68:71]\n"
"s_waitcnt lgkmcnt(4)\n"
"v_cvt_pk_bf16_f32 v40, v116, v117\n"
"v_cvt_pk_bf16_f32 v41, v118, v119\n"
"v_cvt_pk_bf16_f32 v42, v124, v125\n"
"v_cvt_pk_bf16_f32 v43, v126, v127\n"
"s_cmp_eq_i32 s78, 8\n"
"s_cbranch_scc1 .Lmk_x99s\n"
".Lmk_x99r:\n"
"v_mfma_f32_16x16x32_bf16 v[84:87], v[104:107], v[28:31], v[36:39]\n"
"ds_read_b128 v[100:103], v15 offset:12288\n"
"v_exp_f32_e32 v76, v76\n"
"v_exp_f32_e32 v77, v77\n"
"v_exp_f32_e32 v78, v78\n"
"v_mfma_f32_16x16x32_bf16 v[108:111], v[104:107], v[32:35], v[60:63]\n"
"buffer_load_dwordx4 a[80:83], v1, s[4:7], s98 offen sc1\n"
"v_exp_f32_e32 v79, v79\n"
"v_mfma_f32_16x16x32_bf16 v[104:107], v[144:147], v[28:31], v[36:39]\n"
"ds_read_b128 v[116:119], v254 offset:12544\n"
"v_exp_f32_e32 v80, v80\n"
"v_exp_f32_e32 v81, v81\n"
"v_mfma_f32_16x16x32_bf16 v[124:127], v[144:147], v[32:35], v[60:63]\n"
"ds_read_b64_tr_b16 v[128:129], v16 offset:16384\n"
"v_exp_f32_e32 v82, v82\n"
"v_exp_f32_e32 v83, v83\n"
"s_waitcnt lgkmcnt(3)\n"
"v_mfma_f32_16x16x32_bf16 v[132:135], v[88:91], v[152:155], v[132:135]\n"
"buffer_load_dwordx4 a[84:87], v1, s[4:7], s98 offen offset:1024 sc1\n"
"v_cvt_pk_bf16_f32 v144, v76, v77\n"
"v_cvt_pk_bf16_f32 v145, v78, v79\n"
"v_cvt_pk_bf16_f32 v146, v80, v81\n"
"v_mfma_f32_16x16x32_bf16 v[44:47], v[120:123], v[152:155], v[44:47]\n"
"ds_read_b64_tr_b16 v[130:131], v255 offset:16640\n"
"v_cvt_pk_bf16_f32 v147, v82, v83\n"
"v_exp_f32_e32 v112, v112\n"
"v_exp_f32_e32 v113, v113\n"
"v_mfma_f32_16x16x32_bf16 v[64:67], v[72:75], v[152:155], v[64:67]\n"
"ds_read_b64_tr_b16 v[76:77], v255 offset:16384\n"
"v_exp_f32_e32 v114, v114\n"
"v_exp_f32_e32 v115, v115\n"
"v_mfma_f32_16x16x32_bf16 v[136:139], v[88:91], v[40:43], v[136:139]\n"
"buffer_load_dwordx4 a[88:91], v1, s[4:7], s98 offen offset:2048 sc1\n"
"v_exp_f32_e32 v148, v148\n"
"v_exp_f32_e32 v149, v149\n"
"v_mfma_f32_16x16x32_bf16 v[140:143], v[120:123], v[40:43], v[140:143]\n"
"ds_read_b64_tr_b16 v[78:79], v16 offset:16640\n"
"v_exp_f32_e32 v150, v150\n"
"v_exp_f32_e32 v151, v151\n"
"v_mfma_f32_16x16x32_bf16 v[68:71], v[72:75], v[40:43], v[68:71]\n"
"s_waitcnt lgkmcnt(4)\n"
"v_cvt_pk_bf16_f32 v40, v112, v113\n"
"v_cvt_pk_bf16_f32 v41, v114, v115\n"
"v_cvt_pk_bf16_f32 v42, v148, v149\n"
"v_cvt_pk_bf16_f32 v43, v150, v151\n"
"s_cmp_eq_i32 s78, 7\n"
"s_cbranch_scc1 .Lmk_x100s\n"
".Lmk_x100r:\n"
"v_mfma_f32_16x16x32_bf16 v[72:75], v[100:103], v[28:31], v[36:39]\n"
"ds_read_b128 v[80:83], v15 offset:10240\n"
"v_exp_f32_e32 v84, v84\n"
"v_exp_f32_e32 v85, v85\n"
"v_exp_f32_e32 v86, v86\n"
"v_mfma_f32_16x16x32_bf16 v[112:115], v[100:103], v[32:35], v[60:63]\n"
"buffer_load_dwordx4 a[92:95], v1, s[4:7], s98 offen offset:3072 sc1\n"
"v_exp_f32_e32 v87, v87\n"
"v_mfma_f32_16x16x32_bf16 v[100:103], v[116:119], v[28:31], v[36:39]\n"
"ds_read_b128 v[120:123], v254 offset:10496\n"
"v_exp_f32_e32 v104, v104\n"
"v_exp_f32_e32 v105, v105\n"
"v_mfma_f32_16x16x32_bf16 v[148:151], v[116:119], v[32:35], v[60:63]\n"
"ds_read_b64_tr_b16 v[116:117], v16 offset:14336\n"
"v_exp_f32_e32 v106, v106\n"
"v_exp_f32_e32 v107, v107\n"
"v_cvt_pk_bf16_f32 v152, v84, v85\n"
"s_waitcnt lgkmcnt(3)\n"
"v_mfma_f32_16x16x32_bf16 v[132:135], v[88:91], v[144:147], v[132:135]\n"
"s_add_u32 s98, s69, 0x606000\n"
"buffer_load_dwordx4 a[96:99], v0, s[4:7], s98 offen sc1\n"
"v_cvt_pk_bf16_f32 v153, v86, v87\n"
"v_cvt_pk_bf16_f32 v154, v104, v105\n"
"v_mfma_f32_16x16x32_bf16 v[44:47], v[128:131], v[144:147], v[44:47]\n"
"ds_read_b64_tr_b16 v[118:119], v255 offset:14592\n"
"v_cvt_pk_bf16_f32 v155, v106, v107\n"
"v_exp_f32_e32 v108, v108\n"
"v_exp_f32_e32 v109, v109\n"
"v_mfma_f32_16x16x32_bf16 v[64:67], v[76:79], v[144:147], v[64:67]\n"
"ds_read_b64_tr_b16 v[84:85], v255 offset:14336\n"
"v_exp_f32_e32 v110, v110\n"
"v_exp_f32_e32 v111, v111\n"
"v_mfma_f32_16x16x32_bf16 v[136:139], v[88:91], v[40:43], v[136:139]\n"
"buffer_load_dwordx4 a[100:103], v0, s[4:7], s98 offen offset:1024 sc1\n"
"v_exp_f32_e32 v124, v124\n"
"v_exp_f32_e32 v125, v125\n"
"v_mfma_f32_16x16x32_bf16 v[140:143], v[128:131], v[40:43], v[140:143]\n"
"ds_read_b64_tr_b16 v[86:87], v16 offset:14592\n"
"v_exp_f32_e32 v126, v126\n"
"v_exp_f32_e32 v127, v127\n"
"v_mfma_f32_16x16x32_bf16 v[68:71], v[76:79], v[40:43], v[68:71]\n"
"s_waitcnt lgkmcnt(4)\n"
"v_cvt_pk_bf16_f32 v40, v108, v109\n"
"v_cvt_pk_bf16_f32 v41, v110, v111\n"
"v_cvt_pk_bf16_f32 v42, v124, v125\n"
"v_cvt_pk_bf16_f32 v43, v126, v127\n"
"s_cmp_eq_i32 s78, 6\n"
"s_cbranch_scc1 .Lmk_x101s\n"
".Lmk_x101r:\n"
"v_mfma_f32_16x16x32_bf16 v[76:79], v[80:83], v[28:31], v[36:39]\n"
"ds_read_b128 v[104:107], v15 offset:8192\n"
"v_exp_f32_e32 v72, v72\n"
"v_exp_f32_e32 v73, v73\n"
"v_exp_f32_e32 v74, v74\n"
"v_mfma_f32_16x16x32_bf16 v[108:111], v[80:83], v[32:35], v[60:63]\n"
"buffer_load_dwordx4 a[104:107], v0, s[4:7], s98 offen offset:2048 sc1\n"
"v_exp_f32_e32 v75, v75\n"
"v_mfma_f32_16x16x32_bf16 v[80:83], v[120:123], v[28:31], v[36:39]\n"
"ds_read_b128 v[124:127], v254 offset:8448\n"
"v_exp_f32_e32 v100, v100\n"
"v_exp_f32_e32 v101, v101\n"
"v_mfma_f32_16x16x32_bf16 v[128:131], v[120:123], v[32:35], v[60:63]\n"
"ds_read_b64_tr_b16 v[120:121], v16 offset:12288\n"
"v_exp_f32_e32 v102, v102\n"
"v_exp_f32_e32 v103, v103\n"
"s_waitcnt lgkmcnt(3)\n"
"v_mfma_f32_16x16x32_bf16 v[132:135], v[88:91], v[152:155], v[132:135]\n"
"buffer_load_dwordx4 a[108:111], v0, s[4:7], s98 offen offset:3072 sc1\n"
"v_cvt_pk_bf16_f32 v144, v72, v73\n"
"v_cvt_pk_bf16_f32 v145, v74, v75\n"
"v_cvt_pk_bf16_f32 v146, v100, v101\n"
"v_mfma_f32_16x16x32_bf16 v[44:47], v[116:119], v[152:155], v[44:47]\n"
"ds_read_b64_tr_b16 v[122:123], v255 offset:12544\n"
"v_cvt_pk_bf16_f32 v147, v102, v103\n"
"v_exp_f32_e32 v112, v112\n"
"v_exp_f32_e32 v113, v113\n"
"v_mfma_f32_16x16x32_bf16 v[64:67], v[84:87], v[152:155], v[64:67]\n"
"ds_read_b64_tr_b16 v[72:73], v255 offset:12288\n"
"v_exp_f32_e32 v114, v114\n"
"v_exp_f32_e32 v115, v115\n"
"v_mfma_f32_16x16x32_bf16 v[136:139], v[88:91], v[40:43], v[136:139]\n"
"buffer_load_dwordx4 a[112:115], v1, s[4:7], s98 offen sc1\n"
"v_exp_f32_e32 v148, v148\n"
"v_exp_f32_e32 v149, v149\n"
"v_mfma_f32_16x16x32_bf16 v[140:143], v[116:119], v[40:43], v[140:143]\n"
"ds_read_b64_tr_b16 v[74:75], v16 offset:12544\n"
"v_exp_f32_e32 v150, v150\n"
"v_exp_f32_e32 v151, v151\n"
"v_mfma_f32_16x16x32_bf16 v[68:71], v[84:87], v[40:43], v[68:71]\n"
"s_waitcnt lgkmcnt(4)\n"
"v_cvt_pk_bf16_f32 v40, v112, v113\n"
"v_cvt_pk_bf16_f32 v41, v114, v115\n"
"v_cvt_pk_bf16_f32 v42, v148, v149\n"
"v_cvt_pk_bf16_f32 v43, v150, v151\n"
"s_cmp_eq_i32 s78, 5\n"
"s_cbranch_scc1 .Lmk_x102s\n"
".Lmk_x102r:\n"
"v_mfma_f32_16x16x32_bf16 v[84:87], v[104:107], v[28:31], v[36:39]\n"
"ds_read_b64_tr_b16 v[100:101], v16 offset:10240\n"
"ds_read_b64_tr_b16 v[102:103], v255 offset:10496\n"
"v_exp_f32_e32 v76, v76\n"
"v_exp_f32_e32 v77, v77\n"
"v_mfma_f32_16x16x32_bf16 v[112:115], v[104:107], v[32:35], v[60:63]\n"
"buffer_load_dwordx4 a[116:119], v1, s[4:7], s98 offen offset:1024 sc1\n"
"v_exp_f32_e32 v78, v78\n"
"v_exp_f32_e32 v79, v79\n"
"v_mfma_f32_16x16x32_bf16 v[104:107], v[124:127], v[28:31], v[36:39]\n"
"ds_read_b64_tr_b16 v[36:37], v255 offset:10240\n"
"ds_read_b64_tr_b16 v[38:39], v16 offset:10496\n"
"v_exp_f32_e32 v80, v80\n"
"v_exp_f32_e32 v81, v81\n"
"v_mfma_f32_16x16x32_bf16 v[116:119], v[124:127], v[32:35], v[60:63]\n"
"ds_read_b64_tr_b16 v[60:61], v16 offset:24576\n"
"v_exp_f32_e32 v82, v82\n"
"v_exp_f32_e32 v83, v83\n"
"s_waitcnt lgkmcnt(5)\n"
"v_mfma_f32_16x16x32_bf16 v[132:135], v[88:91], v[144:147], v[132:135]\n"
"buffer_load_dwordx4 a[120:123], v1, s[4:7], s98 offen offset:2048 sc1\n"
"ds_read_b64_tr_b16 v[62:63], v255 offset:24832\n"
"v_cvt_pk_bf16_f32 v124, v76, v77\n"
"v_cvt_pk_bf16_f32 v125, v78, v79\n"
"v_cvt_pk_bf16_f32 v126, v80, v81\n"
"v_mfma_f32_16x16x32_bf16 v[44:47], v[120:123], v[144:147], v[44:47]\n"
"ds_read_b64_tr_b16 v[76:77], v255 offset:24576\n"
"v_cvt_pk_bf16_f32 v127, v82, v83\n"
"v_exp_f32_e32 v108, v108\n"
"v_exp_f32_e32 v109, v109\n"
"v_mfma_f32_16x16x32_bf16 v[64:67], v[72:75], v[144:147], v[64:67]\n"
"ds_read_b64_tr_b16 v[78:79], v16 offset:24832\n"
"v_exp_f32_e32 v110, v110\n"
"v_exp_f32_e32 v111, v111\n"
"v_mfma_f32_16x16x32_bf16 v[136:139], v[88:91], v[40:43], v[136:139]\n"
"buffer_load_dwordx4 a[124:127], v1, s[4:7], s98 offen offset:3072 sc1\n"
"v_exp_f32_e32 v128, v128\n"
"v_add_u32_e32 v248, 0x26400, v0\n"
"v_exp_f32_e32 v129, v129\n"
"v_mfma_f32_16x16x32_bf16 v[140:143], v[120:123], v[40:43], v[140:143]\n"
"ds_read_b128 v[80:83], v248\n"
"v_exp_f32_e32 v130, v130\n"
"v_exp_f32_e32 v131, v131\n"
"v_mfma_f32_16x16x32_bf16 v[68:71], v[72:75], v[40:43], v[68:71]\n"
"ds_read_b128 v[72:75], v248 offset:1024\n"
"v_cvt_pk_bf16_f32 v40, v108, v109\n"
"v_cvt_pk_bf16_f32 v41, v110, v111\n"
"v_cvt_pk_bf16_f32 v42, v128, v129\n"
"v_cvt_pk_bf16_f32 v43, v130, v131\n"
"s_waitcnt lgkmcnt(6)\n"
"v_mfma_f32_16x16x32_bf16 v[132:135], v[88:91], v[124:127], v[132:135]\n"
"ds_read_b64_tr_b16 v[108:109], v16 offset:8192\n"
"v_cndmask_b32_e64 v84, v48, v84, s[20:21]\n"
"v_cndmask_b32_e64 v85, v49, v85, s[22:23]\n"
"v_cndmask_b32_e64 v86, v50, v86, s[24:25]\n"
"s_add_u32 m0, s77, 0x4000\n"
"s_add_u32 s98, s65, 0\n"
"buffer_load_dwordx4 v2, s[4:7], s98 offen sc1 lds\n"
"buffer_load_dwordx4 v2, s[4:7], s98 offen offset:1024 sc1 lds\n"
"v_cndmask_b32_e64 v87, v51, v87, s[26:27]\n"
"v_cndmask_b32_e64 v104, v24, v104, s[28:29]\n"
"v_mfma_f32_16x16x32_bf16 v[44:47], v[100:103], v[124:127], v[44:47]\n"
"ds_read_b64_tr_b16 v[110:111], v255 offset:8448\n"
"v_cndmask_b32_e64 v105, v25, v105, s[30:31]\n"
"v_cndmask_b32_e64 v106, v26, v106, s[32:33]\n"
"v_cndmask_b32_e64 v107, v27, v107, s[34:35]\n"
"v_exp_f32_e32 v84, v84\n"
"v_exp_f32_e32 v85, v85\n"
"v_exp_f32_e32 v86, v86\n"
"v_exp_f32_e32 v87, v87\n"
"v_mfma_f32_16x16x32_bf16 v[64:67], v[36:39], v[124:127], v[64:67]\n"
"v_exp_f32_e32 v104, v104\n"
"v_exp_f32_e32 v105, v105\n"
"v_exp_f32_e32 v106, v106\n"
"v_exp_f32_e32 v107, v107\n"
"v_cvt_pk_bf16_f32 v48, v84, v85\n"
"v_cvt_pk_bf16_f32 v49, v86, v87\n"
"v_cvt_pk_bf16_f32 v50, v104, v105\n"
"v_mfma_f32_16x16x32_bf16 v[136:139], v[88:91], v[40:43], v[136:139]\n"
"ds_read_b64_tr_b16 v[84:85], v255 offset:8192\n"
"s_add_u32 s98, s69, 0x608000\n"
"buffer_load_dwordx4 v[24:27], v0, s[4:7], s98 offen sc1\n"
"v_cvt_pk_bf16_f32 v51, v106, v107\n"
"v_cndmask_b32_e64 v112, v52, v112, s[36:37]\n"
"v_cndmask_b32_e64 v113, v53, v113, s[38:39]\n"
"v_cndmask_b32_e64 v114, v54, v114, s[40:41]\n"
"v_cndmask_b32_e64 v115, v55, v115, s[42:43]\n"
"v_cndmask_b32_e64 v116, v56, v116, s[44:45]\n"
"v_cndmask_b32_e64 v117, v57, v117, s[46:47]\n"
"v_mfma_f32_16x16x32_bf16 v[140:143], v[100:103], v[40:43], v[140:143]\n"
"ds_read_b64_tr_b16 v[86:87], v16 offset:8448\n"
"v_cndmask_b32_e64 v118, v58, v118, s[48:49]\n"
"v_cndmask_b32_e64 v119, v59, v119, s[50:51]\n"
"v_exp_f32_e32 v112, v112\n"
"v_exp_f32_e32 v113, v113\n"
"v_exp_f32_e32 v114, v114\n"
"v_exp_f32_e32 v115, v115\n"
"v_exp_f32_e32 v116, v116\n"
"v_mfma_f32_16x16x32_bf16 v[68:71], v[36:39], v[40:43], v[68:71]\n"
"s_waitcnt lgkmcnt(0)\n"
"v_exp_f32_e32 v117, v117\n"
"v_exp_f32_e32 v118, v118\n"
"v_exp_f32_e32 v119, v119\n"
"v_cvt_pk_bf16_f32 v36, v112, v113\n"
"v_cvt_pk_bf16_f32 v37, v114, v115\n"
"v_cvt_pk_bf16_f32 v38, v116, v117\n"
"v_cvt_pk_bf16_f32 v39, v118, v119\n"
"v_mfma_f32_16x16x32_bf16 v[132:135], v[88:91], v[48:51], v[132:135]\n"
"v_and_b32_e32 v80, v48, v80\n"
"v_and_b32_e32 v81, v49, v81\n"
"v_and_b32_e32 v82, v50, v82\n"
"v_and_b32_e32 v83, v51, v83\n"
"v_mfma_f32_16x16x32_bf16 v[136:139], v[88:91], v[36:39], v[136:139]\n"
"v_xor_b32_e32 v40, v48, v80\n"
"v_xor_b32_e32 v41, v49, v81\n"
"v_xor_b32_e32 v42, v50, v82\n"
"v_xor_b32_e32 v43, v51, v83\n"
"v_mfma_f32_16x16x32_bf16 v[44:47], v[108:111], v[80:83], v[44:47]\n"
"v_and_b32_e32 v72, v36, v72\n"
"v_and_b32_e32 v73, v37, v73\n"
"v_mfma_f32_16x16x32_bf16 v[64:67], v[84:87], v[80:83], v[64:67]\n"
"v_and_b32_e32 v74, v38, v74\n"
"v_and_b32_e32 v75, v39, v75\n"
"v_mfma_f32_16x16x32_bf16 v[44:47], v[60:63], v[40:43], v[44:47]\n"
"v_xor_b32_e32 v48, v36, v72\n"
"v_xor_b32_e32 v49, v37, v73\n"
"v_mfma_f32_16x16x32_bf16 v[64:67], v[76:79], v[40:43], v[64:67]\n"
"v_xor_b32_e32 v50, v38, v74\n"
"v_xor_b32_e32 v51, v39, v75\n"
"v_mfma_f32_16x16x32_bf16 v[140:143], v[108:111], v[72:75], v[140:143]\n"
"s_add_u32 m0, s77, 0xc000\n"
"s_add_u32 s97, s65, 0x4000\n"
"buffer_load_dwordx4 v2, s[4:7], s97 offen sc1 lds\n"
"buffer_load_dwordx4 v2, s[4:7], s97 offen offset:1024 sc1 lds\n"
"v_mfma_f32_16x16x32_bf16 v[68:71], v[84:87], v[72:75], v[68:71]\n"
"v_mfma_f32_16x16x32_bf16 v[140:143], v[60:63], v[48:51], v[140:143]\n"
"v_mfma_f32_16x16x32_bf16 v[68:71], v[76:79], v[48:51], v[68:71]\n"
"v_max_f32_e32 v248, v132, v136\n"
"v_rcp_f32_e32 v132, v132\n"
"v_rcp_f32_e32 v136, v136\n"
"s_nop 0\n"
"v_max3_f32 v248, v248, v132, v136\n"
"v_cmp_nlt_f32_e32 vcc, v248, v252\n"
"s_cbranch_vccnz .Lmk_b97\n"
".Lmk_k103:\n"
"s_add_u32 s97, s60, 0x40000\n"
"v_mul_f32_e32 v44, v44, v132\n"
"v_mul_f32_e32 v45, v45, v132\n"
"v_mul_f32_e32 v46, v46, v132\n"
"v_mul_f32_e32 v47, v47, v132\n"
"v_mul_f32_e32 v64, v64, v132\n"
"v_mul_f32_e32 v65, v65, v132\n"
"v_mul_f32_e32 v66, v66, v132\n"
"v_mul_f32_e32 v67, v67, v132\n"
"v_cvt_pk_bf16_f32 v28, v44, v45\n"
"v_cvt_pk_bf16_f32 v29, v46, v47\n"
"v_cvt_pk_bf16_f32 v30, v64, v65\n"
"v_cvt_pk_bf16_f32 v31, v66, v67\n"
"buffer_store_dwordx4 v[28:31], v0, s[4:7], s97 offen sc1\n"
"s_add_u32 s97, s60, 0x48000\n"
"v_mul_f32_e32 v140, v140, v136\n"
"v_mul_f32_e32 v141, v141, v136\n"
"v_mul_f32_e32 v142, v142, v136\n"
"v_mul_f32_e32 v143, v143, v136\n"
"v_mul_f32_e32 v68, v68, v136\n"
"v_mul_f32_e32 v69, v69, v136\n"
"v_mul_f32_e32 v70, v70, v136\n"
"v_mul_f32_e32 v71, v71, v136\n"
"v_cvt_pk_bf16_f32 v28, v140, v141\n"
"v_cvt_pk_bf16_f32 v29, v142, v143\n"
"v_cvt_pk_bf16_f32 v30, v68, v69\n"
"v_cvt_pk_bf16_f32 v31, v70, v71\n"
"buffer_store_dwordx4 v[28:31], v0, s[4:7], s97 offen sc1\n"
";\n"
"; @A AT a\n"
"s_mov_b32 s2, 0\n"
"v_mov_b32_e32 v253, 0xff800000\n"
"v_mov_b32_e32 v251, 0xff800000\n"
"v_mov_b32_e32 v250, 0x7f800000\n"
"v_mov_b32_e32 v249, 0x7f800000\n"
".Lmk_a104:\n"
"ds_read_b128 v[28:31], v15 offset:16384\n"
"ds_read_b128 v[32:35], v254 offset:16640\n"
"ds_read_b128 v[36:39], v15 offset:14336\n"
"ds_read_b128 v[40:43], v254 offset:14592\n"
"s_cmp_gt_i32 s78, 7\n"
"s_cselect_b32 s97, 0x80000000, 0\n"
"s_or_b32 s97, s97, 0x7f800000\n"
"s_add_u32 s96, s67, 32\n"
"buffer_load_dwordx2 v[246:247], v12, s[4:7], s96 offen sc1\n"
"s_waitcnt lgkmcnt(2)\n"
"v_mfma_f32_16x16x32_bf16 v[100:103], v[28:31], v[92:95], 0\n"
"v_mfma_f32_16x16x32_bf16 v[104:107], v[28:31], v[96:99], 0\n"
"v_mfma_f32_16x16x32_bf16 v[108:111], v[32:35], v[92:95], 0\n"
"v_mfma_f32_16x16x32_bf16 v[112:115], v[32:35], v[96:99], 0\n"
"s_waitcnt lgkmcnt(0)\n"
"s_nop 3\n"
"v_max_f32_e32 v248, v100, v101\n"
"v_max_f32_e32 v245, v104, v105\n"
"v_max3_f32 v248, v248, v102, v103\n"
"v_max3_f32 v245, v245, v106, v107\n"
"v_max3_f32 v248, v248, v108, v109\n"
"v_max3_f32 v245, v245, v112, v113\n"
"v_max3_f32 v248, v248, v110, v111\n"
"v_max3_f32 v245, v245, v114, v115\n"
"v_mov_b32_e32 v244, v248\n"
"v_mov_b32_e32 v243, v245\n"
"s_nop 0\n"
"v_permlane16_swap_b32_e32 v248, v244\n"
"v_permlane16_swap_b32_e32 v245, v243\n"
"v_max_f32_e32 v248, v248, v244\n"
"v_max_f32_e32 v245, v245, v243\n"
"v_mov_b32_e32 v244, v248\n"
"v_mov_b32_e32 v243, v245\n"
"s_nop 0\n"
"v_permlane32_swap_b32_e32 v248, v244\n"
"v_permlane32_swap_b32_e32 v245, v243\n"
"v_max_f32_e32 v248, v248, v244\n"
"v_max_f32_e32 v245, v245, v243\n"
"v_med3_f32 v248, v248, v253, v250\n"
"v_med3_f32 v245, v245, v251, v249\n"
"v_min_f32_e64 v116, s97, -v248\n"
"v_min_f32_e64 v117, s97, -v248\n"
"v_min_f32_e64 v118, s97, -v248\n"
"v_min_f32_e64 v119, s97, -v248\n"
"v_min_f32_e64 v120, s97, -v245\n"
"v_min_f32_e64 v121, s97, -v245\n"
"v_min_f32_e64 v122, s97, -v245\n"
"v_min_f32_e64 v123, s97, -v245\n"
"v_mfma_f32_16x16x32_bf16 v[44:47], v[36:39], v[92:95], v[116:119]\n"
"ds_read_b128 v[32:35], v15 offset:12288\n"
"v_sub_f32_e32 v100, v100, v248\n"
"v_sub_f32_e32 v101, v101, v248\n"
"v_sub_f32_e32 v102, v102, v248\n"
"v_sub_f32_e32 v103, v103, v248\n"
"v_mfma_f32_16x16x32_bf16 v[48:51], v[36:39], v[96:99], v[120:123]\n"
"v_sub_f32_e32 v108, v108, v248\n"
"v_sub_f32_e32 v109, v109, v248\n"
"v_sub_f32_e32 v110, v110, v248\n"
"v_sub_f32_e32 v111, v111, v248\n"
"v_mfma_f32_16x16x32_bf16 v[36:39], v[40:43], v[92:95], v[116:119]\n"
"ds_read_b128 v[52:55], v254 offset:12544\n"
"v_sub_f32_e32 v104, v104, v245\n"
"v_sub_f32_e32 v105, v105, v245\n"
"v_sub_f32_e32 v106, v106, v245\n"
"v_sub_f32_e32 v107, v107, v245\n"
"v_mfma_f32_16x16x32_bf16 v[56:59], v[40:43], v[96:99], v[120:123]\n"
"s_waitcnt lgkmcnt(0)\n"
"v_sub_f32_e32 v112, v112, v245\n"
"v_sub_f32_e32 v113, v113, v245\n"
"v_sub_f32_e32 v114, v114, v245\n"
"v_sub_f32_e32 v115, v115, v245\n"
"s_cmp_eq_i32 s78, 7\n"
"s_cbranch_scc1 .Lmk_x106s\n"
".Lmk_x106r:\n"
"v_mfma_f32_16x16x32_bf16 v[60:63], v[32:35], v[92:95], v[116:119]\n"
"ds_read_b128 v[40:43], v15 offset:10240\n"
"buffer_load_dwordx4 v[28:31], v0, s[4:7], s98 offen offset:1024 sc1\n"
"ds_read_b128 v[64:67], v254 offset:10496\n"
"v_exp_f32_e32 v44, v44\n"
"v_exp_f32_e32 v45, v45\n"
"v_exp_f32_e32 v46, v46\n"
"v_exp_f32_e32 v47, v47\n"
"v_exp_f32_e32 v36, v36\n"
"v_mfma_f32_16x16x32_bf16 v[68:71], v[32:35], v[96:99], v[120:123]\n"
"ds_read_b64_tr_b16 v[72:73], v16 offset:14336\n"
"buffer_load_dwordx4 v[32:35], v0, s[4:7], s98 offen offset:2048 sc1\n"
"v_exp_f32_e32 v37, v37\n"
"v_exp_f32_e32 v38, v38\n"
"v_exp_f32_e32 v39, v39\n"
"v_cvt_pk_bf16_f32 v76, v44, v45\n"
"v_cvt_pk_bf16_f32 v77, v46, v47\n"
"v_cvt_pk_bf16_f32 v78, v36, v37\n"
"v_mfma_f32_16x16x32_bf16 v[80:83], v[52:55], v[92:95], v[116:119]\n"
"ds_read_b64_tr_b16 v[74:75], v255 offset:14592\n"
"ds_read_b64_tr_b16 v[84:85], v255 offset:14336\n"
"v_cvt_pk_bf16_f32 v79, v38, v39\n"
"v_exp_f32_e32 v48, v48\n"
"v_exp_f32_e32 v49, v49\n"
"v_exp_f32_e32 v50, v50\n"
"v_exp_f32_e32 v51, v51\n"
"v_mfma_f32_16x16x32_bf16 v[124:127], v[52:55], v[96:99], v[120:123]\n"
"buffer_load_dwordx4 v[36:39], v0, s[4:7], s98 offen offset:3072 sc1\n"
"ds_read_b64_tr_b16 v[86:87], v16 offset:14592\n"
"v_exp_f32_e32 v56, v56\n"
"v_exp_f32_e32 v57, v57\n"
"v_exp_f32_e32 v58, v58\n"
"v_exp_f32_e32 v59, v59\n"
"v_cvt_pk_bf16_f32 v52, v48, v49\n"
"v_cvt_pk_bf16_f32 v53, v50, v51\n"
"v_cvt_pk_bf16_f32 v54, v56, v57\n"
"v_cvt_pk_bf16_f32 v55, v58, v59\n"
"s_waitcnt lgkmcnt(4)\n"
"s_cmp_eq_i32 s78, 6\n"
"s_cbranch_scc1 .Lmk_x107s\n"
".Lmk_x107r:\n"
"v_mfma_f32_16x16x32_bf16 v[128:131], v[40:43], v[92:95], v[116:119]\n"
"ds_read_b128 v[56:59], v15 offset:8192\n"
"ds_read_b128 v[132:135], v254 offset:8448\n"
"v_exp_f32_e32 v60, v60\n"
"v_exp_f32_e32 v61, v61\n"
"v_mfma_f32_16x16x32_bf16 v[136:139], v[40:43], v[96:99], v[120:123]\n"
"buffer_load_dwordx4 v[40:43], v1, s[4:7], s98 offen sc1\n"
"v_exp_f32_e32 v62, v62\n"
"v_exp_f32_e32 v63, v63\n"
"v_mfma_f32_16x16x32_bf16 v[140:143], v[64:67], v[92:95], v[116:119]\n"
"ds_read_b64_tr_b16 v[144:145], v16 offset:12288\n"
"ds_read_b64_tr_b16 v[146:147], v255 offset:12544\n"
"v_exp_f32_e32 v80, v80\n"
"v_exp_f32_e32 v81, v81\n"
"v_mfma_f32_16x16x32_bf16 v[148:151], v[64:67], v[96:99], v[120:123]\n"
"ds_read_b64_tr_b16 v[64:65], v255 offset:12288\n"
"v_exp_f32_e32 v82, v82\n"
"v_exp_f32_e32 v83, v83\n"
"s_waitcnt lgkmcnt(5)\n"
"v_mfma_f32_16x16x32_bf16 v[152:155], v[88:91], v[76:79], 0\n"
"buffer_load_dwordx4 v[44:47], v1, s[4:7], s98 offen offset:1024 sc1\n"
"v_cvt_pk_bf16_f32 v156, v60, v61\n"
"v_cvt_pk_bf16_f32 v157, v62, v63\n"
"v_cvt_pk_bf16_f32 v158, v80, v81\n"
"v_cvt_pk_bf16_f32 v159, v82, v83\n"
"v_mfma_f32_16x16x32_bf16 v[160:163], v[72:75], v[76:79], 0\n"
"ds_read_b64_tr_b16 v[66:67], v16 offset:12544\n"
"s_waitcnt vmcnt(8)\n"
"ds_read_b128 v[80:83], v2 offset:16384\n"
"v_exp_f32_e32 v68, v68\n"
"v_exp_f32_e32 v69, v69\n"
"v_mfma_f32_16x16x32_bf16 v[164:167], v[84:87], v[76:79], 0\n"
"v_exp_f32_e32 v70, v70\n"
"v_exp_f32_e32 v71, v71\n"
"v_mfma_f32_16x16x32_bf16 v[168:171], v[88:91], v[52:55], 0\n"
"buffer_load_dwordx4 v[48:51], v1, s[4:7], s98 offen offset:2048 sc1\n"
"ds_read_b128 v[76:79], v2 offset:17408\n"
"v_exp_f32_e32 v124, v124\n"
"v_exp_f32_e32 v125, v125\n"
"v_mfma_f32_16x16x32_bf16 v[172:175], v[72:75], v[52:55], 0\n"
"ds_read_b128 v[72:75], v2 offset:49152\n"
"v_exp_f32_e32 v126, v126\n"
"v_exp_f32_e32 v127, v127\n"
"v_mfma_f32_16x16x32_bf16 v[176:179], v[84:87], v[52:55], 0\n"
"ds_read_b128 v[84:87], v2 offset:50176\n"
"v_cvt_pk_bf16_f32 v180, v68, v69\n"
"v_cvt_pk_bf16_f32 v181, v70, v71\n"
"v_cvt_pk_bf16_f32 v182, v124, v125\n"
"v_cvt_pk_bf16_f32 v183, v126, v127\n"
"s_waitcnt lgkmcnt(8)\n"
"s_cmp_eq_i32 s78, 5\n"
"s_cbranch_scc1 .Lmk_x108s\n"
".Lmk_x108r:\n"
"v_mfma_f32_16x16x32_bf16 v[124:127], v[56:59], v[92:95], v[116:119]\n"
"ds_read_b64_tr_b16 v[68:69], v16 offset:10240\n"
"ds_read_b64_tr_b16 v[70:71], v255 offset:10496\n"
"buffer_load_dwordx4 v[52:55], v1, s[4:7], s98 offen offset:3072 sc1\n"
"v_exp_f32_e32 v128, v128\n"
"v_exp_f32_e32 v129, v129\n"
"v_mfma_f32_16x16x32_bf16 v[184:187], v[56:59], v[96:99], v[120:123]\n"
"ds_read_b64_tr_b16 v[188:189], v255 offset:10240\n"
"v_exp_f32_e32 v130, v130\n"
"v_exp_f32_e32 v131, v131\n"
"v_mfma_f32_16x16x32_bf16 v[192:195], v[132:135], v[92:95], v[116:119]\n"
"ds_read_b64_tr_b16 v[190:191], v16 offset:10496\n"
"v_exp_f32_e32 v140, v140\n"
"v_exp_f32_e32 v141, v141\n"
"v_exp_f32_e32 v142, v142\n"
"v_mfma_f32_16x16x32_bf16 v[196:199], v[132:135], v[96:99], v[120:123]\n"
"s_waitcnt lgkmcnt(4)\n"
"s_add_u32 s97, s69, 0x60a000\n"
"buffer_load_dwordx4 v[56:59], v0, s[4:7], s97 offen sc1\n"
"v_max_u32_e32 v248, v80, v81\n"
"v_exp_f32_e32 v143, v143\n"
"v_mfma_f32_16x16x32_bf16 v[152:155], v[88:91], v[156:159], v[152:155]\n"
"v_max3_u32 v248, v248, v82, v83\n"
"v_cvt_pk_bf16_f32 v132, v128, v129\n"
"v_cvt_pk_bf16_f32 v133, v130, v131\n"
"v_cvt_pk_bf16_f32 v134, v140, v141\n"
"v_mfma_f32_16x16x32_bf16 v[160:163], v[144:147], v[156:159], v[160:163]\n"
"v_max3_u32 v248, v248, v76, v77\n"
"v_cvt_pk_bf16_f32 v135, v142, v143\n"
"v_exp_f32_e32 v136, v136\n"
"v_max3_u32 v248, v248, v78, v79\n"
"v_exp_f32_e32 v137, v137\n"
"v_mfma_f32_16x16x32_bf16 v[164:167], v[64:67], v[156:159], v[164:167]\n"
"v_exp_f32_e32 v138, v138\n"
"v_max3_u32 v248, v248, v72, v73\n"
"v_exp_f32_e32 v139, v139\n"
"v_mfma_f32_16x16x32_bf16 v[168:171], v[88:91], v[180:183], v[168:171]\n"
"buffer_load_dwordx4 v[60:63], v0, s[4:7], s97 offen offset:1024 sc1\n"
"v_max3_u32 v248, v248, v74, v75\n"
"v_exp_f32_e32 v148, v148\n"
"v_exp_f32_e32 v149, v149\n"
"v_mfma_f32_16x16x32_bf16 v[172:175], v[144:147], v[180:183], v[172:175]\n"
"v_max3_u32 v248, v248, v84, v85\n"
"v_exp_f32_e32 v150, v150\n"
"v_max3_u32 v248, v248, v86, v87\n"
"v_exp_f32_e32 v151, v151\n"
"v_mfma_f32_16x16x32_bf16 v[176:179], v[64:67], v[180:183], v[176:179]\n"
"v_cvt_pk_bf16_f32 v128, v136, v137\n"
"v_cmp_eq_u32_e32 vcc, -1, v248\n"
"v_cvt_pk_bf16_f32 v129, v138, v139\n"
"v_cvt_pk_bf16_f32 v130, v148, v149\n"
"v_cvt_pk_bf16_f32 v131, v150, v151\n"
"s_cbranch_vccnz .Lmk_h95s\n"
".Lmk_h95r:\n"
"s_cmp_lg_u32 s2, 0\n"
"s_cbranch_scc1 .Lmk_s109\n"
"s_barrier\n"
".Lmk_s109:\n"
"ds_read_b128 v[72:75], v15 offset:6144\n"
"ds_read_b128 v[76:79], v254 offset:6400\n"
"s_waitcnt lgkmcnt(2)\n"
"v_mfma_f32_16x16x32_bf16 v[152:155], v[88:91], v[132:135], v[152:155]\n"
"ds_read_b128 v[80:83], v15 offset:4096\n"
"v_exp_f32_e32 v124, v124\n"
"v_exp_f32_e32 v125, v125\n"
"v_exp_f32_e32 v126, v126\n"
"v_mfma_f32_16x16x32_bf16 v[160:163], v[68:71], v[132:135], v[160:163]\n"
"buffer_load_dwordx4 v[64:67], v0, s[4:7], s97 offen offset:2048 sc1\n"
"ds_read_b128 v[84:87], v254 offset:4352\n"
"v_exp_f32_e32 v127, v127\n"
"v_mfma_f32_16x16x32_bf16 v[164:167], v[188:191], v[132:135], v[164:167]\n"
"ds_read_b64_tr_b16 v[132:133], v16 offset:8192\n"
"v_exp_f32_e32 v192, v192\n"
"v_exp_f32_e32 v193, v193\n"
"v_mfma_f32_16x16x32_bf16 v[168:171], v[88:91], v[128:131], v[168:171]\n"
"ds_read_b64_tr_b16 v[134:135], v255 offset:8448\n"
"v_exp_f32_e32 v194, v194\n"
"v_exp_f32_e32 v195, v195\n"
"v_mfma_f32_16x16x32_bf16 v[172:175], v[68:71], v[128:131], v[172:175]\n"
"buffer_load_dwordx4 v[68:71], v0, s[4:7], s97 offen offset:3072 sc1\n"
"v_cvt_pk_bf16_f32 v136, v124, v125\n"
"v_cvt_pk_bf16_f32 v137, v126, v127\n"
"v_cvt_pk_bf16_f32 v138, v192, v193\n"
"v_cvt_pk_bf16_f32 v139, v194, v195\n"
"v_mfma_f32_16x16x32_bf16 v[176:179], v[188:191], v[128:131], v[176:179]\n"
"ds_read_b64_tr_b16 v[124:125], v255 offset:8192\n"
"v_exp_f32_e32 v184, v184\n"
"v_exp_f32_e32 v185, v185\n"
"s_waitcnt lgkmcnt(5)\n"
"v_mfma_f32_16x16x32_bf16 v[128:131], v[72:75], v[92:95], v[116:119]\n"
"ds_read_b64_tr_b16 v[126:127], v16 offset:8448\n"
"v_exp_f32_e32 v186, v186\n"
"v_exp_f32_e32 v187, v187\n"
"v_exp_f32_e32 v196, v196\n"
"v_mfma_f32_16x16x32_bf16 v[140:143], v[72:75], v[96:99], v[120:123]\n"
"buffer_load_dwordx4 v[72:75], v1, s[4:7], s97 offen sc1\n"
"s_waitcnt vmcnt(12)\n"
"v_cmp_ne_u64_e32 vcc, v[246:247], v[18:19]\n"
"v_mfma_f32_16x16x32_bf16 v[144:147], v[76:79], v[92:95], v[116:119]\n"
"v_exp_f32_e32 v197, v197\n"
"v_exp_f32_e32 v198, v198\n"
"v_exp_f32_e32 v199, v199\n"
"v_mfma_f32_16x16x32_bf16 v[148:151], v[76:79], v[96:99], v[120:123]\n"
"s_cbranch_vccnz .Lmk_f79s\n"
".Lmk_f79r:\n"
"v_cvt_pk_bf16_f32 v156, v184, v185\n"
"v_cvt_pk_bf16_f32 v157, v186, v187\n"
"v_cvt_pk_bf16_f32 v158, v196, v197\n"
"v_cvt_pk_bf16_f32 v159, v198, v199\n"
"s_waitcnt lgkmcnt(4)\n"
"v_mfma_f32_16x16x32_bf16 v[180:183], v[80:83], v[92:95], v[116:119]\n"
"ds_read_b128 v[184:187], v15 offset:2048\n"
"v_exp_f32_e32 v128, v128\n"
"v_exp_f32_e32 v129, v129\n"
"v_exp_f32_e32 v130, v130\n"
"v_mfma_f32_16x16x32_bf16 v[188:191], v[80:83], v[96:99], v[120:123]\n"
"buffer_load_dwordx4 v[76:79], v1, s[4:7], s97 offen offset:1024 sc1\n"
"v_exp_f32_e32 v131, v131\n"
"v_mfma_f32_16x16x32_bf16 v[192:195], v[84:87], v[92:95], v[116:119]\n"
"ds_read_b128 v[196:199], v254 offset:2304\n"
"v_exp_f32_e32 v144, v144\n"
"v_exp_f32_e32 v145, v145\n"
"v_mfma_f32_16x16x32_bf16 v[200:203], v[84:87], v[96:99], v[120:123]\n"
"ds_read_b64_tr_b16 v[204:205], v16 offset:6144\n"
"v_exp_f32_e32 v146, v146\n"
"v_exp_f32_e32 v147, v147\n"
"s_waitcnt lgkmcnt(3)\n"
"v_mfma_f32_16x16x32_bf16 v[152:155], v[88:91], v[136:139], v[152:155]\n"
"buffer_load_dwordx4 v[80:83], v1, s[4:7], s97 offen offset:2048 sc1\n"
"v_cvt_pk_bf16_f32 v208, v128, v129\n"
"v_cvt_pk_bf16_f32 v209, v130, v131\n"
"v_cvt_pk_bf16_f32 v210, v144, v145\n"
"v_mfma_f32_16x16x32_bf16 v[160:163], v[132:135], v[136:139], v[160:163]\n"
"ds_read_b64_tr_b16 v[206:207], v255 offset:6400\n"
"v_cvt_pk_bf16_f32 v211, v146, v147\n"
"v_exp_f32_e32 v140, v140\n"
"v_exp_f32_e32 v141, v141\n"
"v_mfma_f32_16x16x32_bf16 v[164:167], v[124:127], v[136:139], v[164:167]\n"
"ds_read_b64_tr_b16 v[128:129], v255 offset:6144\n"
"v_exp_f32_e32 v142, v142\n"
"v_exp_f32_e32 v143, v143\n"
"v_mfma_f32_16x16x32_bf16 v[168:171], v[88:91], v[156:159], v[168:171]\n"
"buffer_load_dwordx4 v[84:87], v1, s[4:7], s97 offen offset:3072 sc1\n"
"v_exp_f32_e32 v148, v148\n"
"v_exp_f32_e32 v149, v149\n"
"v_mfma_f32_16x16x32_bf16 v[172:175], v[132:135], v[156:159], v[172:175]\n"
"ds_read_b64_tr_b16 v[130:131], v16 offset:6400\n"
"v_exp_f32_e32 v150, v150\n"
"v_exp_f32_e32 v151, v151\n"
"v_mfma_f32_16x16x32_bf16 v[176:179], v[124:127], v[156:159], v[176:179]\n"
"s_waitcnt lgkmcnt(4)\n"
"v_cvt_pk_bf16_f32 v124, v140, v141\n"
"v_cvt_pk_bf16_f32 v125, v142, v143\n"
"v_cvt_pk_bf16_f32 v126, v148, v149\n"
"v_cvt_pk_bf16_f32 v127, v150, v151\n"
"v_mfma_f32_16x16x32_bf16 v[132:135], v[184:187], v[92:95], v[116:119]\n"
"ds_read_b128 v[136:139], v15\n"
"v_exp_f32_e32 v180, v180\n"
"v_exp_f32_e32 v181, v181\n"
"v_exp_f32_e32 v182, v182\n"
"v_mfma_f32_16x16x32_bf16 v[140:143], v[184:187], v[96:99], v[120:123]\n"
"s_add_u32 s97, s56, 0x40000\n"
"buffer_load_dwordx4 v[144:147], v0, s[4:7], s97 offen sc1\n"
"v_exp_f32_e32 v183, v183\n"
"v_mfma_f32_16x16x32_bf16 v[148:151], v[196:199], v[92:95], v[116:119]\n"
"ds_read_b128 v[156:159], v254 offset:256\n"
"v_exp_f32_e32 v192, v192\n"
"v_exp_f32_e32 v193, v193\n"
"v_exp_f32_e32 v194, v194\n"
"v_mfma_f32_16x16x32_bf16 v[184:187], v[196:199], v[96:99], v[120:123]\n"
"buffer_load_dwordx4 v[196:199], v0, s[4:7], s97 offen offset:1024 sc1\n"
"ds_read_b64_tr_b16 v[212:213], v16 offset:4096\n"
"v_exp_f32_e32 v195, v195\n"
"s_waitcnt lgkmcnt(3)\n"
"v_mfma_f32_16x16x32_bf16 v[152:155], v[88:91], v[208:211], v[152:155]\n"
"v_cvt_pk_bf16_f32 v216, v180, v181\n"
"v_cvt_pk_bf16_f32 v217, v182, v183\n"
"v_cvt_pk_bf16_f32 v218, v192, v193\n"
"v_cvt_pk_bf16_f32 v219, v194, v195\n"
"v_mfma_f32_16x16x32_bf16 v[160:163], v[204:207], v[208:211], v[160:163]\n"
"buffer_load_dwordx4 v[180:183], v0, s[4:7], s97 offen offset:2048 sc1\n"
"ds_read_b64_tr_b16 v[214:215], v255 offset:4352\n"
"v_exp_f32_e32 v188, v188\n"
"v_exp_f32_e32 v189, v189\n"
"v_mfma_f32_16x16x32_bf16 v[164:167], v[128:131], v[208:211], v[164:167]\n"
"buffer_load_dwordx4 v[192:195], v0, s[4:7], s97 offen offset:3072 sc1\n"
"v_exp_f32_e32 v190, v190\n"
"v_exp_f32_e32 v191, v191\n"
"v_mfma_f32_16x16x32_bf16 v[168:171], v[88:91], v[124:127], v[168:171]\n"
"ds_read_b64_tr_b16 v[208:209], v255 offset:4096\n"
"v_exp_f32_e32 v200, v200\n"
"v_exp_f32_e32 v201, v201\n"
"v_mfma_f32_16x16x32_bf16 v[172:175], v[204:207], v[124:127], v[172:175]\n"
"buffer_load_dwordx4 v[204:207], v1, s[4:7], s97 offen sc1\n"
"ds_read_b64_tr_b16 v[210:211], v16 offset:4352\n"
"v_exp_f32_e32 v202, v202\n"
"v_exp_f32_e32 v203, v203\n"
"v_mfma_f32_16x16x32_bf16 v[176:179], v[128:131], v[124:127], v[176:179]\n"
"s_waitcnt lgkmcnt(4)\n"
"v_cvt_pk_bf16_f32 v124, v188, v189\n"
"v_cvt_pk_bf16_f32 v125, v190, v191\n"
"v_cvt_pk_bf16_f32 v126, v200, v201\n"
"v_cvt_pk_bf16_f32 v127, v202, v203\n"
"v_mfma_f32_16x16x32_bf16 v[128:131], v[136:139], v[92:95], v[116:119]\n"
"ds_read_b64_tr_b16 v[188:189], v16 offset:2048\n"
"ds_read_b64_tr_b16 v[190:191], v255 offset:2304\n"
"buffer_load_dwordx4 v[200:203], v1, s[4:7], s97 offen offset:1024 sc1\n"
"v_exp_f32_e32 v132, v132\n"
"v_exp_f32_e32 v133, v133\n"
"v_mfma_f32_16x16x32_bf16 v[220:223], v[136:139], v[96:99], v[120:123]\n"
"ds_read_b64_tr_b16 v[136:137], v255 offset:2048\n"
"v_exp_f32_e32 v134, v134\n"
"v_exp_f32_e32 v135, v135\n"
"v_mfma_f32_16x16x32_bf16 v[224:227], v[156:159], v[92:95], v[116:119]\n"
"buffer_load_dwordx4 v[116:119], v1, s[4:7], s97 offen offset:2048 sc1\n"
"v_exp_f32_e32 v148, v148\n"
"v_exp_f32_e32 v149, v149\n"
"v_mfma_f32_16x16x32_bf16 v[228:231], v[156:159], v[96:99], v[120:123]\n"
"ds_read_b64_tr_b16 v[138:139], v16 offset:2304\n"
"ds_read_b64_tr_b16 v[120:121], v16 offset:16384\n"
"v_exp_f32_e32 v150, v150\n"
"v_exp_f32_e32 v151, v151\n"
"s_waitcnt lgkmcnt(5)\n"
"v_mfma_f32_16x16x32_bf16 v[152:155], v[88:91], v[216:219], v[152:155]\n"
"v_cvt_pk_bf16_f32 v156, v132, v133\n"
"v_cvt_pk_bf16_f32 v157, v134, v135\n"
"v_cvt_pk_bf16_f32 v158, v148, v149\n"
"v_mfma_f32_16x16x32_bf16 v[160:163], v[212:215], v[216:219], v[160:163]\n"
"buffer_load_dwordx4 v[132:135], v1, s[4:7], s97 offen offset:3072 sc1\n"
"ds_read_b64_tr_b16 v[122:123], v255 offset:16640\n"
"ds_read_b64_tr_b16 v[148:149], v255 offset:16384\n"
"v_cvt_pk_bf16_f32 v159, v150, v151\n"
"v_exp_f32_e32 v140, v140\n"
"v_exp_f32_e32 v141, v141\n"
"v_exp_f32_e32 v142, v142\n"
"v_mfma_f32_16x16x32_bf16 v[164:167], v[208:211], v[216:219], v[164:167]\n"
"s_add_u32 s97, s57, 0x40000\n"
"buffer_load_dwordx4 v[216:219], v0, s[4:7], s97 offen sc1\n"
"ds_read_b64_tr_b16 v[150:151], v16 offset:16640\n"
"v_exp_f32_e32 v143, v143\n"
"v_mfma_f32_16x16x32_bf16 v[168:171], v[88:91], v[124:127], v[168:171]\n"
"v_exp_f32_e32 v184, v184\n"
"v_add_u32_e32 v248, 0x26400, v0\n"
"v_exp_f32_e32 v185, v185\n"
"v_mfma_f32_16x16x32_bf16 v[172:175], v[212:215], v[124:127], v[172:175]\n"
"buffer_load_dwordx4 v[212:215], v0, s[4:7], s97 offen offset:1024 sc1\n"
"ds_read_b128 v[232:235], v248\n"
"v_exp_f32_e32 v186, v186\n"
"v_exp_f32_e32 v187, v187\n"
"v_mfma_f32_16x16x32_bf16 v[176:179], v[208:211], v[124:127], v[176:179]\n"
"ds_read_b128 v[208:211], v248 offset:1024\n"
"v_cvt_pk_bf16_f32 v124, v140, v141\n"
"v_cvt_pk_bf16_f32 v125, v142, v143\n"
"v_cvt_pk_bf16_f32 v126, v184, v185\n"
"v_cvt_pk_bf16_f32 v127, v186, v187\n"
"s_waitcnt lgkmcnt(6)\n"
"v_mfma_f32_16x16x32_bf16 v[152:155], v[88:91], v[156:159], v[152:155]\n"
"ds_read_b64_tr_b16 v[140:141], v16\n"
"buffer_load_dwordx4 v[184:187], v0, s[4:7], s97 offen offset:2048 sc1\n"
"v_cndmask_b32_e64 v128, v100, v128, s[20:21]\n"
"v_cndmask_b32_e64 v129, v101, v129, s[22:23]\n"
"v_cndmask_b32_e64 v130, v102, v130, s[24:25]\n"
"v_cndmask_b32_e64 v131, v103, v131, s[26:27]\n"
"v_cndmask_b32_e64 v224, v108, v224, s[28:29]\n"
"v_cndmask_b32_e64 v225, v109, v225, s[30:31]\n"
"v_cndmask_b32_e64 v226, v110, v226, s[32:33]\n"
"v_cndmask_b32_e64 v227, v111, v227, s[34:35]\n"
"v_exp_f32_e32 v128, v128\n"
"v_mfma_f32_16x16x32_bf16 v[160:163], v[188:191], v[156:159], v[160:163]\n"
"ds_read_b64_tr_b16 v[142:143], v255 offset:256\n"
"buffer_load_dwordx4 v[100:103], v0, s[4:7], s97 offen offset:3072 sc1\n"
"v_exp_f32_e32 v129, v129\n"
"v_exp_f32_e32 v130, v130\n"
"v_exp_f32_e32 v131, v131\n"
"v_exp_f32_e32 v224, v224\n"
"v_mfma_f32_16x16x32_bf16 v[164:167], v[136:139], v[156:159], v[164:167]\n"
"buffer_load_dwordx4 v[156:159], v1, s[4:7], s97 offen sc1\n"
"v_exp_f32_e32 v225, v225\n"
"v_exp_f32_e32 v226, v226\n"
"v_exp_f32_e32 v227, v227\n"
"v_cvt_pk_bf16_f32 v108, v128, v129\n"
"v_cvt_pk_bf16_f32 v109, v130, v131\n"
"v_cvt_pk_bf16_f32 v110, v224, v225\n"
"v_mfma_f32_16x16x32_bf16 v[168:171], v[88:91], v[124:127], v[168:171]\n"
"ds_read_b64_tr_b16 v[128:129], v255\n"
"v_cvt_pk_bf16_f32 v111, v226, v227\n"
"v_cndmask_b32_e64 v220, v104, v220, s[36:37]\n"
"v_cndmask_b32_e64 v221, v105, v221, s[38:39]\n"
"v_cndmask_b32_e64 v222, v106, v222, s[40:41]\n"
"v_cndmask_b32_e64 v223, v107, v223, s[42:43]\n"
"v_cndmask_b32_e64 v228, v112, v228, s[44:45]\n"
"v_cndmask_b32_e64 v229, v113, v229, s[46:47]\n"
"v_cndmask_b32_e64 v230, v114, v230, s[48:49]\n"
"v_cndmask_b32_e64 v231, v115, v231, s[50:51]\n"
"v_mfma_f32_16x16x32_bf16 v[172:175], v[188:191], v[124:127], v[172:175]\n"
"buffer_load_dwordx4 v[104:107], v1, s[4:7], s97 offen offset:1024 sc1\n"
"ds_read_b64_tr_b16 v[130:131], v16 offset:256\n"
"buffer_load_dwordx4 v[112:115], v1, s[4:7], s97 offen offset:2048 sc1\n"
"v_exp_f32_e32 v220, v220\n"
"v_exp_f32_e32 v221, v221\n"
"v_exp_f32_e32 v222, v222\n"
"v_exp_f32_e32 v223, v223\n"
"v_exp_f32_e32 v228, v228\n"
"v_mfma_f32_16x16x32_bf16 v[176:179], v[136:139], v[124:127], v[176:179]\n"
"s_waitcnt lgkmcnt(0)\n"
"v_exp_f32_e32 v229, v229\n"
"v_exp_f32_e32 v230, v230\n"
"v_exp_f32_e32 v231, v231\n"
"v_cvt_pk_bf16_f32 v124, v220, v221\n"
"v_cvt_pk_bf16_f32 v125, v222, v223\n"
"v_cvt_pk_bf16_f32 v126, v228, v229\n"
"v_cvt_pk_bf16_f32 v127, v230, v231\n"
"v_mfma_f32_16x16x32_bf16 v[152:155], v[88:91], v[108:111], v[152:155]\n"
"v_and_b32_e32 v232, v108, v232\n"
"v_and_b32_e32 v233, v109, v233\n"
"v_and_b32_e32 v234, v110, v234\n"
"v_and_b32_e32 v235, v111, v235\n"
"v_mfma_f32_16x16x32_bf16 v[168:171], v[88:91], v[124:127], v[168:171]\n"
"v_xor_b32_e32 v136, v108, v232\n"
"v_xor_b32_e32 v137, v109, v233\n"
"v_xor_b32_e32 v138, v110, v234\n"
"v_xor_b32_e32 v139, v111, v235\n"
"v_mfma_f32_16x16x32_bf16 v[160:163], v[140:143], v[232:235], v[160:163]\n"
"v_and_b32_e32 v208, v124, v208\n"
"v_and_b32_e32 v209, v125, v209\n"
"v_mfma_f32_16x16x32_bf16 v[164:167], v[128:131], v[232:235], v[164:167]\n"
"v_and_b32_e32 v210, v126, v210\n"
"v_and_b32_e32 v211, v127, v211\n"
"v_mfma_f32_16x16x32_bf16 v[160:163], v[120:123], v[136:139], v[160:163]\n"
"v_xor_b32_e32 v108, v124, v208\n"
"v_xor_b32_e32 v109, v125, v209\n"
"v_mfma_f32_16x16x32_bf16 v[164:167], v[148:151], v[136:139], v[164:167]\n"
"v_xor_b32_e32 v110, v126, v210\n"
"v_xor_b32_e32 v111, v127, v211\n"
"v_mfma_f32_16x16x32_bf16 v[172:175], v[140:143], v[208:211], v[172:175]\n"
"buffer_load_dwordx4 v[124:127], v1, s[4:7], s97 offen offset:3072 sc1\n"
"s_add_u32 s97, s58, 0x40000\n"
"buffer_load_dwordx4 v[136:139], v0, s[4:7], s97 offen sc1\n"
"v_mfma_f32_16x16x32_bf16 v[176:179], v[128:131], v[208:211], v[176:179]\n"
"buffer_load_dwordx4 v[128:131], v0, s[4:7], s97 offen offset:1024 sc1\n"
"v_mfma_f32_16x16x32_bf16 v[172:175], v[120:123], v[108:111], v[172:175]\n"
"buffer_load_dwordx4 v[120:123], v0, s[4:7], s97 offen offset:2048 sc1\n"
"v_mfma_f32_16x16x32_bf16 v[176:179], v[148:151], v[108:111], v[176:179]\n"
"buffer_load_dwordx4 v[108:111], v0, s[4:7], s97 offen offset:3072 sc1\n"
"v_max_f32_e32 v248, v152, v168\n"
"v_rcp_f32_e32 v152, v152\n"
"v_rcp_f32_e32 v168, v168\n"
"s_nop 0\n"
"v_max3_f32 v248, v248, v152, v168\n"
"v_cmp_nlt_f32_e32 vcc, v248, v252\n"
"s_cbranch_vccnz .Lmk_b105\n"
".Lmk_k110:\n"
"v_mul_f32_e32 v160, v160, v152\n"
"v_mul_f32_e32 v161, v161, v152\n"
"v_mul_f32_e32 v162, v162, v152\n"
"v_mul_f32_e32 v163, v163, v152\n"
"v_mul_f32_e32 v164, v164, v152\n"
"v_mul_f32_e32 v165, v165, v152\n"
"v_mul_f32_e32 v166, v166, v152\n"
"v_mul_f32_e32 v167, v167, v152\n"
"v_cvt_pk_bf16_f32 v88, v160, v161\n"
"v_cvt_pk_bf16_f32 v89, v162, v163\n"
"v_cvt_pk_bf16_f32 v90, v164, v165\n"
"v_cvt_pk_bf16_f32 v91, v166, v167\n"
"buffer_store_dwordx4 v[88:91], v0, s[4:7], s60 offen sc1\n"
"s_add_u32 s98, s60, 0x8000\n"
"v_mul_f32_e32 v172, v172, v168\n"
"v_mul_f32_e32 v173, v173, v168\n"
"v_mul_f32_e32 v174, v174, v168\n"
"v_mul_f32_e32 v175, v175, v168\n"
"v_mul_f32_e32 v176, v176, v168\n"
"v_mul_f32_e32 v177, v177, v168\n"
"v_mul_f32_e32 v178, v178, v168\n"
"v_mul_f32_e32 v179, v179, v168\n"
"v_cvt_pk_bf16_f32 v88, v172, v173\n"
"v_cvt_pk_bf16_f32 v89, v174, v175\n"
"v_cvt_pk_bf16_f32 v90, v176, v177\n"
"v_cvt_pk_bf16_f32 v91, v178, v179\n"
"buffer_store_dwordx4 v[88:91], v0, s[4:7], s98 offen sc1\n"
";\n"
"s_waitcnt vmcnt(20)\n"
"; @A G2 b.0\n"
"s_nop 0\n"
"v_mfma_f32_16x16x32_bf16 v[88:91], a[128:131], v[144:147], 0\n"
"v_mfma_f32_16x16x32_bf16 v[88:91], a[132:135], v[196:199], v[88:91]\n"
"s_waitcnt vmcnt(18)\n"
"v_mfma_f32_16x16x32_bf16 v[88:91], a[136:139], v[180:183], v[88:91]\n"
"buffer_load_dwordx4 v[92:95], v1, s[4:7], s97 offen sc1\n"
"v_mfma_f32_16x16x32_bf16 v[88:91], a[140:143], v[192:195], v[88:91]\n"
"s_waitcnt vmcnt(17)\n"
"v_mfma_f32_16x16x32_bf16 v[88:91], a[144:147], v[204:207], v[88:91]\n"
"v_mfma_f32_16x16x32_bf16 v[88:91], a[148:151], v[200:203], v[88:91]\n"
"s_waitcnt vmcnt(15)\n"
"v_mfma_f32_16x16x32_bf16 v[88:91], a[152:155], v[116:119], v[88:91]\n"
"buffer_load_dwordx4 v[96:99], v1, s[4:7], s97 offen offset:1024 sc1\n"
"v_mfma_f32_16x16x32_bf16 v[88:91], a[156:159], v[132:135], v[88:91]\n"
"v_mfma_f32_16x16x32_bf16 v[140:143], a[160:163], v[144:147], 0\n"
"v_mfma_f32_16x16x32_bf16 v[140:143], a[164:167], v[196:199], v[140:143]\n"
"v_mfma_f32_16x16x32_bf16 v[140:143], a[168:171], v[180:183], v[140:143]\n"
"buffer_load_dwordx4 v[148:151], v1, s[4:7], s97 offen offset:2048 sc1\n"
"v_mfma_f32_16x16x32_bf16 v[140:143], a[172:175], v[192:195], v[140:143]\n"
"v_mfma_f32_16x16x32_bf16 v[140:143], a[176:179], v[204:207], v[140:143]\n"
"s_nop 1\n"
"v_cmp_u_f32_e32 vcc, v88, v88\n"
"v_mfma_f32_16x16x32_bf16 v[140:143], a[180:183], v[200:203], v[140:143]\n"
"s_cbranch_vccnz .Lmk_p111s\n"
".Lmk_p111r:\n"
"v_mfma_f32_16x16x32_bf16 v[140:143], a[184:187], v[116:119], v[140:143]\n"
"ds_write_b128 v3, v[88:91]\n"
"v_mfma_f32_16x16x32_bf16 v[140:143], a[188:191], v[132:135], v[140:143]\n"
"buffer_load_dwordx4 v[88:91], v1, s[4:7], s97 offen offset:3072 sc1\n"
"v_mfma_f32_16x16x32_bf16 v[152:155], a[192:195], v[144:147], 0\n"
"v_mfma_f32_16x16x32_bf16 v[152:155], a[196:199], v[196:199], v[152:155]\n"
"s_add_u32 s98, s59, 0x40000\n"
"v_mfma_f32_16x16x32_bf16 v[152:155], a[200:203], v[180:183], v[152:155]\n"
"v_mfma_f32_16x16x32_bf16 v[152:155], a[204:207], v[192:195], v[152:155]\n"
"buffer_load_dwordx4 v[160:163], v0, s[4:7], s98 offen sc1\n"
"v_mfma_f32_16x16x32_bf16 v[152:155], a[208:211], v[204:207], v[152:155]\n"
"v_mfma_f32_16x16x32_bf16 v[152:155], a[212:215], v[200:203], v[152:155]\n"
"ds_write_b128 v3, v[140:143] offset:1024\n"
"v_mfma_f32_16x16x32_bf16 v[152:155], a[216:219], v[116:119], v[152:155]\n"
"v_mfma_f32_16x16x32_bf16 v[152:155], a[220:223], v[132:135], v[152:155]\n"
"buffer_load_dwordx4 v[140:143], v0, s[4:7], s98 offen offset:1024 sc1\n"
"v_mfma_f32_16x16x32_bf16 v[164:167], a[224:227], v[144:147], 0\n"
"v_mfma_f32_16x16x32_bf16 v[164:167], a[228:231], v[196:199], v[164:167]\n"
"v_mfma_f32_16x16x32_bf16 v[164:167], a[232:235], v[180:183], v[164:167]\n"
"v_mfma_f32_16x16x32_bf16 v[164:167], a[236:239], v[192:195], v[164:167]\n"
"buffer_load_dwordx4 v[144:147], v0, s[4:7], s98 offen offset:2048 sc1\n"
"v_mfma_f32_16x16x32_bf16 v[164:167], a[240:243], v[204:207], v[164:167]\n"
"v_mfma_f32_16x16x32_bf16 v[164:167], a[244:247], v[200:203], v[164:167]\n"
"ds_write_b128 v3, v[152:155] offset:2048\n"
"v_mfma_f32_16x16x32_bf16 v[164:167], a[248:251], v[116:119], v[164:167]\n"
"v_mfma_f32_16x16x32_bf16 v[164:167], a[252:255], v[132:135], v[164:167]\n"
"s_waitcnt vmcnt(17)\n"
"buffer_load_dwordx4 v[116:119], v0, s[4:7], s98 offen offset:3072 sc1\n"
"; @A G2 b.1\n"
"v_mfma_f32_16x16x32_bf16 v[132:135], a[128:131], v[216:219], 0\n"
"v_mfma_f32_16x16x32_bf16 v[132:135], a[132:135], v[212:215], v[132:135]\n"
"v_mfma_f32_16x16x32_bf16 v[132:135], a[136:139], v[184:187], v[132:135]\n"
"v_mfma_f32_16x16x32_bf16 v[132:135], a[140:143], v[100:103], v[132:135]\n"
"s_waitcnt vmcnt(14)\n"
"buffer_load_dwordx4 v[152:155], v1, s[4:7], s98 offen sc1\n"
"v_mfma_f32_16x16x32_bf16 v[132:135], a[144:147], v[156:159], v[132:135]\n"
"v_mfma_f32_16x16x32_bf16 v[132:135], a[148:151], v[104:107], v[132:135]\n"
"ds_write_b128 v3, v[164:167] offset:3072\n"
"v_mfma_f32_16x16x32_bf16 v[132:135], a[152:155], v[112:115], v[132:135]\n"
"v_mfma_f32_16x16x32_bf16 v[132:135], a[156:159], v[124:127], v[132:135]\n"
"buffer_load_dwordx4 v[164:167], v1, s[4:7], s98 offen offset:1024 sc1\n"
"v_mfma_f32_16x16x32_bf16 v[168:171], a[160:163], v[216:219], 0\n"
"v_mfma_f32_16x16x32_bf16 v[168:171], a[164:167], v[212:215], v[168:171]\n"
"v_mfma_f32_16x16x32_bf16 v[168:171], a[168:171], v[184:187], v[168:171]\n"
"v_mfma_f32_16x16x32_bf16 v[168:171], a[172:175], v[100:103], v[168:171]\n"
"buffer_load_dwordx4 v[172:175], v1, s[4:7], s98 offen offset:2048 sc1\n"
"v_mfma_f32_16x16x32_bf16 v[168:171], a[176:179], v[156:159], v[168:171]\n"
"s_nop 0\n"
"v_cmp_u_f32_e32 vcc, v132, v132\n"
"v_mfma_f32_16x16x32_bf16 v[168:171], a[180:183], v[104:107], v[168:171]\n"
"s_cbranch_vccnz .Lmk_p112s\n"
".Lmk_p112r:\n"
"v_mfma_f32_16x16x32_bf16 v[168:171], a[184:187], v[112:115], v[168:171]\n"
"ds_write_b128 v3, v[132:135] offset:6144\n"
"v_mfma_f32_16x16x32_bf16 v[168:171], a[188:191], v[124:127], v[168:171]\n"
"buffer_load_dwordx4 v[132:135], v1, s[4:7], s98 offen offset:3072 sc1\n"
"v_mfma_f32_16x16x32_bf16 v[176:179], a[192:195], v[216:219], 0\n"
"v_mfma_f32_16x16x32_bf16 v[176:179], a[196:199], v[212:215], v[176:179]\n"
"v_mfma_f32_16x16x32_bf16 v[176:179], a[200:203], v[184:187], v[176:179]\n"
"v_mfma_f32_16x16x32_bf16 v[176:179], a[204:207], v[100:103], v[176:179]\n"
"v_mfma_f32_16x16x32_bf16 v[176:179], a[208:211], v[156:159], v[176:179]\n"
"ds_read_b128 v[180:183], v2 offset:8192\n"
"ds_read_b128 v[188:191], v2 offset:9216\n"
"v_mfma_f32_16x16x32_bf16 v[176:179], a[212:215], v[104:107], v[176:179]\n"
"ds_write_b128 v3, v[168:171] offset:7168\n"
"v_mfma_f32_16x16x32_bf16 v[176:179], a[216:219], v[112:115], v[176:179]\n"
"v_mfma_f32_16x16x32_bf16 v[176:179], a[220:223], v[124:127], v[176:179]\n"
"v_mfma_f32_16x16x32_bf16 v[168:171], a[224:227], v[216:219], 0\n"
"v_mfma_f32_16x16x32_bf16 v[168:171], a[228:231], v[212:215], v[168:171]\n"
"s_waitcnt lgkmcnt(1)\n"
"v_lshlrev_b32_e32 v255, 16, v180\n"
"v_and_b32_e32 v254, 0xffff0000, v180\n"
"v_mfma_f32_16x16x32_bf16 v[168:171], a[232:235], v[184:187], v[168:171]\n"
"v_lshlrev_b32_e32 v253, 16, v181\n"
"v_and_b32_e32 v252, 0xffff0000, v181\n"
"v_mfma_f32_16x16x32_bf16 v[168:171], a[236:239], v[100:103], v[168:171]\n"
"v_lshlrev_b32_e32 v251, 16, v182\n"
"v_and_b32_e32 v250, 0xffff0000, v182\n"
"v_mfma_f32_16x16x32_bf16 v[168:171], a[240:243], v[156:159], v[168:171]\n"
"v_lshlrev_b32_e32 v249, 16, v183\n"
"v_and_b32_e32 v248, 0xffff0000, v183\n"
"v_mfma_f32_16x16x32_bf16 v[168:171], a[244:247], v[104:107], v[168:171]\n"
"ds_write_b128 v3, v[176:179] offset:8192\n"
"v_mfma_f32_16x16x32_bf16 v[168:171], a[248:251], v[112:115], v[168:171]\n"
"v_lshlrev_b32_e32 v247, 16, v188\n"
"v_and_b32_e32 v246, 0xffff0000, v188\n"
"v_mfma_f32_16x16x32_bf16 v[168:171], a[252:255], v[124:127], v[168:171]\n"
"s_waitcnt vmcnt(14)\n"
"v_lshlrev_b32_e32 v245, 16, v189\n"
"v_and_b32_e32 v244, 0xffff0000, v189\n"
"; @A G2 b.2\n"
"v_mfma_f32_16x16x32_bf16 v[100:103], a[128:131], v[136:139], 0\n"
"v_lshlrev_b32_e32 v243, 16, v190\n"
"v_and_b32_e32 v242, 0xffff0000, v190\n"
"v_mfma_f32_16x16x32_bf16 v[100:103], a[132:135], v[128:131], v[100:103]\n"
"v_lshlrev_b32_e32 v241, 16, v191\n"
"v_and_b32_e32 v240, 0xffff0000, v191\n"
"v_mfma_f32_16x16x32_bf16 v[100:103], a[136:139], v[120:123], v[100:103]\n"
"v_mfma_f32_16x16x32_bf16 v[100:103], a[140:143], v[108:111], v[100:103]\n"
"s_waitcnt vmcnt(8)\n"
"v_mfma_f32_16x16x32_bf16 v[100:103], a[144:147], v[92:95], v[100:103]\n"
"v_mfma_f32_16x16x32_bf16 v[100:103], a[148:151], v[96:99], v[100:103]\n"
"ds_write_b128 v3, v[168:171] offset:9216\n"
"v_mfma_f32_16x16x32_bf16 v[100:103], a[152:155], v[148:151], v[100:103]\n"
"v_mfma_f32_16x16x32_bf16 v[100:103], a[156:159], v[88:91], v[100:103]\n"
"v_mfma_f32_16x16x32_bf16 v[104:107], a[160:163], v[136:139], 0\n"
"v_mfma_f32_16x16x32_bf16 v[104:107], a[164:167], v[128:131], v[104:107]\n"
"s_waitcnt lgkmcnt(0)\n"
"s_barrier\n"
"v_mfma_f32_16x16x32_bf16 v[104:107], a[168:171], v[120:123], v[104:107]\n"
"ds_read_b128 v[112:115], v4\n"
"ds_read_b128 v[124:127], v5\n"
"v_mfma_f32_16x16x32_bf16 v[104:107], a[172:175], v[108:111], v[104:107]\n"
"v_mfma_f32_16x16x32_bf16 v[104:107], a[176:179], v[92:95], v[104:107]\n"
"s_add_u32 s98, s56, 0\n"
"v_cmp_u_f32_e32 vcc, v100, v100\n"
"v_mfma_f32_16x16x32_bf16 v[104:107], a[180:183], v[96:99], v[104:107]\n"
"s_cbranch_vccnz .Lmk_p113s\n"
".Lmk_p113r:\n"
"v_mfma_f32_16x16x32_bf16 v[104:107], a[184:187], v[148:151], v[104:107]\n"
"ds_write_b128 v3, v[100:103] offset:12288\n"
"v_mfma_f32_16x16x32_bf16 v[104:107], a[188:191], v[88:91], v[104:107]\n"
"buffer_load_dwordx4 v[100:103], v0, s[4:7], s98 offen sc1\n"
"v_mfma_f32_16x16x32_bf16 v[156:159], a[192:195], v[136:139], 0\n"
"ds_read_b128 v[168:171], v4 offset:1024\n"
"ds_read_b128 v[176:179], v5 offset:1024\n"
"v_mfma_f32_16x16x32_bf16 v[156:159], a[196:199], v[128:131], v[156:159]\n"
"s_waitcnt lgkmcnt(3)\n"
"v_add_f32_e32 v112, v112, v255\n"
"v_add_f32_e32 v113, v113, v254\n"
"v_mfma_f32_16x16x32_bf16 v[156:159], a[200:203], v[120:123], v[156:159]\n"
"buffer_load_dwordx4 v[180:183], v0, s[4:7], s98 offen offset:1024 sc1\n"
"v_mfma_f32_16x16x32_bf16 v[156:159], a[204:207], v[108:111], v[156:159]\n"
"v_add_f32_e32 v114, v114, v253\n"
"v_add_f32_e32 v115, v115, v252\n"
"v_mfma_f32_16x16x32_bf16 v[156:159], a[208:211], v[92:95], v[156:159]\n"
"v_add_f32_e32 v112, v112, v124\n"
"v_add_f32_e32 v113, v113, v125\n"
"v_mfma_f32_16x16x32_bf16 v[156:159], a[212:215], v[96:99], v[156:159]\n"
"ds_write_b128 v3, v[104:107] offset:13312\n"
"v_mfma_f32_16x16x32_bf16 v[156:159], a[216:219], v[148:151], v[156:159]\n"
"buffer_load_dwordx4 v[104:107], v0, s[4:7], s98 offen offset:2048 sc1\n"
"v_mfma_f32_16x16x32_bf16 v[156:159], a[220:223], v[88:91], v[156:159]\n"
"ds_read_b128 v[184:187], v4 offset:2048\n"
"ds_read_b128 v[188:191], v5 offset:2048\n"
"v_mfma_f32_16x16x32_bf16 v[192:195], a[224:227], v[136:139], 0\n"
"s_waitcnt lgkmcnt(3)\n"
"v_add_f32_e32 v114, v114, v126\n"
"v_add_f32_e32 v115, v115, v127\n"
"v_mfma_f32_16x16x32_bf16 v[192:195], a[228:231], v[128:131], v[192:195]\n"
"buffer_load_dwordx4 v[124:127], v0, s[4:7], s98 offen offset:3072 sc1\n"
"v_mfma_f32_16x16x32_bf16 v[192:195], a[232:235], v[120:123], v[192:195]\n"
"v_add_f32_e32 v168, v168, v251\n"
"v_add_f32_e32 v169, v169, v250\n"
"v_mfma_f32_16x16x32_bf16 v[192:195], a[236:239], v[108:111], v[192:195]\n"
"v_add_f32_e32 v170, v170, v249\n"
"v_add_f32_e32 v171, v171, v248\n"
"v_mfma_f32_16x16x32_bf16 v[192:195], a[240:243], v[92:95], v[192:195]\n"
"buffer_load_dwordx4 v[92:95], v1, s[4:7], s98 offen sc1\n"
"v_mfma_f32_16x16x32_bf16 v[192:195], a[244:247], v[96:99], v[192:195]\n"
"ds_write_b128 v3, v[156:159] offset:14336\n"
"v_mfma_f32_16x16x32_bf16 v[192:195], a[248:251], v[148:151], v[192:195]\n"
"ds_read_b128 v[96:99], v4 offset:3072\n"
"ds_read_b128 v[108:111], v5 offset:3072\n"
"v_mfma_f32_16x16x32_bf16 v[192:195], a[252:255], v[88:91], v[192:195]\n"
"s_waitcnt vmcnt(9)\n"
"buffer_load_dwordx4 v[88:91], v1, s[4:7], s98 offen offset:1024 sc1\n"
"; @A G2 b.3\n"
"v_mfma_f32_16x16x32_bf16 v[120:123], a[128:131], v[160:163], v[112:115]\n"
"v_add_f32_e32 v168, v168, v176\n"
"v_add_f32_e32 v169, v169, v177\n"
"v_mfma_f32_16x16x32_bf16 v[120:123], a[132:135], v[140:143], v[120:123]\n"
"s_waitcnt lgkmcnt(3)\n"
"v_add_f32_e32 v170, v170, v178\n"
"v_add_f32_e32 v171, v171, v179\n"
"v_mfma_f32_16x16x32_bf16 v[120:123], a[136:139], v[144:147], v[120:123]\n"
"buffer_load_dwordx4 v[128:131], v1, s[4:7], s98 offen offset:2048 sc1\n"
"v_mfma_f32_16x16x32_bf16 v[120:123], a[140:143], v[116:119], v[120:123]\n"
"s_waitcnt vmcnt(7)\n"
"v_add_f32_e32 v184, v184, v247\n"
"v_add_f32_e32 v185, v185, v246\n"
"v_mfma_f32_16x16x32_bf16 v[120:123], a[144:147], v[152:155], v[120:123]\n"
"v_add_f32_e32 v186, v186, v245\n"
"v_add_f32_e32 v187, v187, v244\n"
"v_mfma_f32_16x16x32_bf16 v[120:123], a[148:151], v[164:167], v[120:123]\n"
"ds_write_b128 v3, v[192:195] offset:15360\n"
"v_mfma_f32_16x16x32_bf16 v[120:123], a[152:155], v[172:175], v[120:123]\n"
"buffer_load_dwordx4 v[136:139], v1, s[4:7], s98 offen offset:3072 sc1\n"
"v_mfma_f32_16x16x32_bf16 v[120:123], a[156:159], v[132:135], v[120:123]\n"
"s_add_u32 s98, s57, 0\n"
"v_add_f32_e32 v184, v184, v188\n"
"v_add_f32_e32 v185, v185, v189\n"
"v_mfma_f32_16x16x32_bf16 v[148:151], a[160:163], v[160:163], v[168:171]\n"
"s_waitcnt lgkmcnt(1)\n"
"v_add_f32_e32 v186, v186, v190\n"
"v_add_f32_e32 v187, v187, v191\n"
"v_mfma_f32_16x16x32_bf16 v[148:151], a[164:167], v[140:143], v[148:151]\n"
"s_waitcnt lgkmcnt(0)\n"
"s_barrier\n"
"v_mfma_f32_16x16x32_bf16 v[148:151], a[168:171], v[144:147], v[148:151]\n"
"buffer_load_dwordx4 v[156:159], v0, s[4:7], s98 offen sc1\n"
"v_mfma_f32_16x16x32_bf16 v[148:151], a[172:175], v[116:119], v[148:151]\n"
"v_add_f32_e32 v96, v96, v243\n"
"v_add_f32_e32 v97, v97, v242\n"
"v_mfma_f32_16x16x32_bf16 v[148:151], a[176:179], v[152:155], v[148:151]\n"
"v_cmp_u_f32_e32 vcc, v120, v120\n"
"v_add_f32_e32 v98, v98, v241\n"
"v_mfma_f32_16x16x32_bf16 v[148:151], a[180:183], v[164:167], v[148:151]\n"
"s_cbranch_vccnz .Lmk_p114s\n"
".Lmk_p114r:\n"
"v_add_f32_e32 v99, v99, v240\n"
"v_mfma_f32_16x16x32_bf16 v[148:151], a[184:187], v[172:175], v[148:151]\n"
"buffer_load_dwordx4 v[112:115], v0, s[4:7], s98 offen offset:1024 sc1\n"
"v_mfma_f32_16x16x32_bf16 v[148:151], a[188:191], v[132:135], v[148:151]\n"
"v_add_f32_e32 v96, v96, v108\n"
"v_add_f32_e32 v97, v97, v109\n"
"v_mfma_f32_16x16x32_bf16 v[184:187], a[192:195], v[160:163], v[184:187]\n"
"v_add_f32_e32 v98, v98, v110\n"
"v_add_f32_e32 v99, v99, v111\n"
"v_mfma_f32_16x16x32_bf16 v[184:187], a[196:199], v[140:143], v[184:187]\n"
"buffer_load_dwordx4 v[108:111], v0, s[4:7], s98 offen offset:2048 sc1\n"
"v_mfma_f32_16x16x32_bf16 v[184:187], a[200:203], v[144:147], v[184:187]\n"
"ds_read_b128 v[168:171], v6\n"
"ds_read_b128 v[176:179], v6 offset:1024\n"
"v_mfma_f32_16x16x32_bf16 v[184:187], a[204:207], v[116:119], v[184:187]\n"
"ds_read_b128 v[188:191], v6 offset:2048\n"
"ds_read_b128 v[192:195], v6 offset:3072\n"
"v_mfma_f32_16x16x32_bf16 v[184:187], a[208:211], v[152:155], v[184:187]\n"
"buffer_load_dwordx4 v[196:199], v0, s[4:7], s98 offen offset:3072 sc1\n"
"v_mfma_f32_16x16x32_bf16 v[184:187], a[212:215], v[164:167], v[184:187]\n"
"v_mfma_f32_16x16x32_bf16 v[184:187], a[216:219], v[172:175], v[184:187]\n"
"v_mfma_f32_16x16x32_bf16 v[184:187], a[220:223], v[132:135], v[184:187]\n"
"buffer_load_dwordx4 v[200:203], v1, s[4:7], s98 offen sc1\n"
"v_mfma_f32_16x16x32_bf16 v[96:99], a[224:227], v[160:163], v[96:99]\n"
"s_waitcnt lgkmcnt(0)\n"
"v_add_f32_e32 v168, v120, v168\n"
"v_add_f32_e32 v169, v121, v169\n"
"v_mfma_f32_16x16x32_bf16 v[96:99], a[228:231], v[140:143], v[96:99]\n"
"v_add_f32_e32 v170, v122, v170\n"
"v_add_f32_e32 v171, v123, v171\n"
"v_mfma_f32_16x16x32_bf16 v[96:99], a[232:235], v[144:147], v[96:99]\n"
"buffer_load_dwordx4 v[120:123], v1, s[4:7], s98 offen offset:1024 sc1\n"
"v_mfma_f32_16x16x32_bf16 v[96:99], a[236:239], v[116:119], v[96:99]\n"
"v_add_f32_e32 v176, v148, v176\n"
"v_add_f32_e32 v177, v149, v177\n"
"v_mfma_f32_16x16x32_bf16 v[96:99], a[240:243], v[152:155], v[96:99]\n"
"v_add_f32_e32 v178, v150, v178\n"
"v_add_f32_e32 v179, v151, v179\n"
"v_mfma_f32_16x16x32_bf16 v[96:99], a[244:247], v[164:167], v[96:99]\n"
"buffer_load_dwordx4 v[116:119], v1, s[4:7], s98 offen offset:2048 sc1\n"
"v_mfma_f32_16x16x32_bf16 v[96:99], a[248:251], v[172:175], v[96:99]\n"
"v_cvt_pk_bf16_f32 v140, v168, v169\n"
"v_cvt_pk_bf16_f32 v141, v170, v171\n"
"v_mfma_f32_16x16x32_bf16 v[96:99], a[252:255], v[132:135], v[96:99]\n"
"s_add_u32 s97, s61, 0x440000\n"
"v_cvt_pk_bf16_f32 v142, v176, v177\n"
"v_cvt_pk_bf16_f32 v143, v178, v179\n"
"buffer_store_dwordx4 v[140:143], v0, s[4:7], s97 offen sc1\n"
"ds_write_b128 v2, v[140:143] offset:8192\n"
"buffer_load_dwordx4 v[132:135], v1, s[4:7], s98 offen offset:3072 sc1\n"
"; @A fin b\n"
"s_add_u32 s98, s61, 0x440000\n"
"v_mov_b32_e32 v144, 0x3f803f80\n"
"v_mov_b32_e32 v145, 0x3f803f80\n"
"v_mov_b32_e32 v146, 0x3f803f80\n"
"v_mov_b32_e32 v147, 0x3f803f80\n"
"v_add_f32_e32 v188, v184, v188\n"
"v_add_f32_e32 v189, v185, v189\n"
"v_add_f32_e32 v190, v186, v190\n"
"v_add_f32_e32 v191, v187, v191\n"
"v_add_f32_e32 v192, v96, v192\n"
"v_add_f32_e32 v193, v97, v193\n"
"v_add_f32_e32 v194, v98, v194\n"
"v_add_f32_e32 v195, v99, v195\n"
"v_cvt_pk_bf16_f32 v96, v188, v189\n"
"v_cvt_pk_bf16_f32 v97, v190, v191\n"
"v_cvt_pk_bf16_f32 v98, v192, v193\n"
"v_cvt_pk_bf16_f32 v99, v194, v195\n"
"v_mfma_f32_16x16x32_bf16 v[148:151], v[144:147], v[140:143], 0\n"
"buffer_store_dwordx4 v[96:99], v0, s[4:7], s98 offen offset:1024 sc1\n"
"ds_write_b128 v2, v[96:99] offset:9216\n"
"v_mfma_f32_16x16x32_bf16 v[152:155], v[140:143], v[140:143], 0\n"
"v_mfma_f32_16x16x32_bf16 v[148:151], v[144:147], v[96:99], v[148:151]\n"
"v_mfma_f32_16x16x32_bf16 v[152:155], v[96:99], v[96:99], v[152:155]\n"
"s_mov_b32 s96, 0xaaaaaaaa\n"
"s_mov_b32 s97, 0xaaaaaaaa\n"
"s_nop 5\n"
"v_cndmask_b32_e64 v152, v152, v153, s[96:97]\n"
"v_cndmask_b32_e64 v154, v154, v155, s[96:97]\n"
"s_mov_b32 s96, 0xcccccccc\n"
"s_mov_b32 s97, 0xcccccccc\n"
"v_cndmask_b32_e64 v149, v152, v154, s[96:97]\n"
"s_mov_b32 s96, 0xf0000f\n"
"s_mov_b32 s97, 0xf0000f00\n"
"s_add_u32 s98, s63, 0x44000\n"
"v_cndmask_b32_e64 v148, v148, v149, s[96:97]\n"
"buffer_store_dword v148, v14, s[4:7], s98 offen sc1\n"
"s_waitcnt vmcnt(17)\n"
"; @A G2 a.0\n"
"v_mfma_f32_16x16x32_bf16 v[96:99], a[128:131], v[100:103], 0\n"
"s_add_u32 s98, s58, 0\n"
"buffer_load_dwordx4 v[140:143], v0, s[4:7], s98 offen sc1\n"
"v_mfma_f32_16x16x32_bf16 v[96:99], a[132:135], v[180:183], v[96:99]\n"
"s_waitcnt vmcnt(16)\n"
"v_mfma_f32_16x16x32_bf16 v[96:99], a[136:139], v[104:107], v[96:99]\n"
"v_mfma_f32_16x16x32_bf16 v[96:99], a[140:143], v[124:127], v[96:99]\n"
"s_waitcnt vmcnt(14)\n"
"v_mfma_f32_16x16x32_bf16 v[96:99], a[144:147], v[92:95], v[96:99]\n"
"buffer_load_dwordx4 v[144:147], v0, s[4:7], s98 offen offset:1024 sc1\n"
"v_mfma_f32_16x16x32_bf16 v[96:99], a[148:151], v[88:91], v[96:99]\n"
"s_waitcnt vmcnt(13)\n"
"v_mfma_f32_16x16x32_bf16 v[96:99], a[152:155], v[128:131], v[96:99]\n"
"v_mfma_f32_16x16x32_bf16 v[96:99], a[156:159], v[136:139], v[96:99]\n"
"v_mfma_f32_16x16x32_bf16 v[148:151], a[160:163], v[100:103], 0\n"
"buffer_load_dwordx4 v[152:155], v0, s[4:7], s98 offen offset:2048 sc1\n"
"v_mfma_f32_16x16x32_bf16 v[148:151], a[164:167], v[180:183], v[148:151]\n"
"v_mfma_f32_16x16x32_bf16 v[148:151], a[168:171], v[104:107], v[148:151]\n"
"v_mfma_f32_16x16x32_bf16 v[148:151], a[172:175], v[124:127], v[148:151]\n"
"v_mfma_f32_16x16x32_bf16 v[148:151], a[176:179], v[92:95], v[148:151]\n"
"s_nop 1\n"
"v_cmp_u_f32_e32 vcc, v96, v96\n"
"v_mfma_f32_16x16x32_bf16 v[148:151], a[180:183], v[88:91], v[148:151]\n"
"s_cbranch_vccnz .Lmk_p115s\n"
".Lmk_p115r:\n"
"v_mfma_f32_16x16x32_bf16 v[148:151], a[184:187], v[128:131], v[148:151]\n"
"ds_write_b128 v3, v[96:99]\n"
"v_mfma_f32_16x16x32_bf16 v[148:151], a[188:191], v[136:139], v[148:151]\n"
"buffer_load_dwordx4 v[160:163], v0, s[4:7], s98 offen offset:3072 sc1\n"
"v_mfma_f32_16x16x32_bf16 v[96:99], a[192:195], v[100:103], 0\n"
"v_mfma_f32_16x16x32_bf16 v[96:99], a[196:199], v[180:183], v[96:99]\n"
"v_mfma_f32_16x16x32_bf16 v[96:99], a[200:203], v[104:107], v[96:99]\n"
"v_mfma_f32_16x16x32_bf16 v[96:99], a[204:207], v[124:127], v[96:99]\n"
"buffer_load_dwordx4 v[164:167], v1, s[4:7], s98 offen sc1\n"
"v_mfma_f32_16x16x32_bf16 v[96:99], a[208:211], v[92:95], v[96:99]\n"
"v_mfma_f32_16x16x32_bf16 v[96:99], a[212:215], v[88:91], v[96:99]\n"
"ds_write_b128 v3, v[148:151] offset:1024\n"
"v_mfma_f32_16x16x32_bf16 v[96:99], a[216:219], v[128:131], v[96:99]\n"
"v_mfma_f32_16x16x32_bf16 v[96:99], a[220:223], v[136:139], v[96:99]\n"
"buffer_load_dwordx4 v[148:151], v1, s[4:7], s98 offen offset:1024 sc1\n"
"v_mfma_f32_16x16x32_bf16 v[168:171], a[224:227], v[100:103], 0\n"
"v_mfma_f32_16x16x32_bf16 v[168:171], a[228:231], v[180:183], v[168:171]\n"
"v_mfma_f32_16x16x32_bf16 v[168:171], a[232:235], v[104:107], v[168:171]\n"
"v_mfma_f32_16x16x32_bf16 v[168:171], a[236:239], v[124:127], v[168:171]\n"
"buffer_load_dwordx4 v[124:127], v1, s[4:7], s98 offen offset:2048 sc1\n"
"v_mfma_f32_16x16x32_bf16 v[168:171], a[240:243], v[92:95], v[168:171]\n"
"v_mfma_f32_16x16x32_bf16 v[168:171], a[244:247], v[88:91], v[168:171]\n"
"ds_write_b128 v3, v[96:99] offset:2048\n"
"v_mfma_f32_16x16x32_bf16 v[168:171], a[248:251], v[128:131], v[168:171]\n"
"v_mfma_f32_16x16x32_bf16 v[168:171], a[252:255], v[136:139], v[168:171]\n"
"s_waitcnt vmcnt(14)\n"
"buffer_load_dwordx4 v[128:131], v1, s[4:7], s98 offen offset:3072 sc1\n"
"; @A G2 a.1\n"
"v_mfma_f32_16x16x32_bf16 v[88:91], a[128:131], v[156:159], 0\n"
"v_mfma_f32_16x16x32_bf16 v[88:91], a[132:135], v[112:115], v[88:91]\n"
"s_add_u32 s98, s59, 0\n"
"v_mfma_f32_16x16x32_bf16 v[88:91], a[136:139], v[108:111], v[88:91]\n"
"v_mfma_f32_16x16x32_bf16 v[88:91], a[140:143], v[196:199], v[88:91]\n"
"s_waitcnt vmcnt(10)\n"
"buffer_load_dwordx4 v[172:175], v0, s[4:7], s98 offen sc1\n"
"v_mfma_f32_16x16x32_bf16 v[88:91], a[144:147], v[200:203], v[88:91]\n"
"v_mfma_f32_16x16x32_bf16 v[88:91], a[148:151], v[120:123], v[88:91]\n"
"ds_write_b128 v3, v[168:171] offset:3072\n"
"v_mfma_f32_16x16x32_bf16 v[88:91], a[152:155], v[116:119], v[88:91]\n"
"v_mfma_f32_16x16x32_bf16 v[88:91], a[156:159], v[132:135], v[88:91]\n"
"buffer_load_dwordx4 v[168:171], v0, s[4:7], s98 offen offset:1024 sc1\n"
"v_mfma_f32_16x16x32_bf16 v[92:95], a[160:163], v[156:159], 0\n"
"v_mfma_f32_16x16x32_bf16 v[92:95], a[164:167], v[112:115], v[92:95]\n"
"v_mfma_f32_16x16x32_bf16 v[92:95], a[168:171], v[108:111], v[92:95]\n"
"v_mfma_f32_16x16x32_bf16 v[92:95], a[172:175], v[196:199], v[92:95]\n"
"buffer_load_dwordx4 v[176:179], v0, s[4:7], s98 offen offset:2048 sc1\n"
"v_mfma_f32_16x16x32_bf16 v[92:95], a[176:179], v[200:203], v[92:95]\n"
"s_nop 0\n"
"v_cmp_u_f32_e32 vcc, v88, v88\n"
"v_mfma_f32_16x16x32_bf16 v[92:95], a[180:183], v[120:123], v[92:95]\n"
"s_cbranch_vccnz .Lmk_p116s\n"
".Lmk_p116r:\n"
"v_mfma_f32_16x16x32_bf16 v[92:95], a[184:187], v[116:119], v[92:95]\n"
"ds_write_b128 v3, v[88:91] offset:6144\n"
"v_mfma_f32_16x16x32_bf16 v[92:95], a[188:191], v[132:135], v[92:95]\n"
"buffer_load_dwordx4 v[180:183], v0, s[4:7], s98 offen offset:3072 sc1\n"
"v_mfma_f32_16x16x32_bf16 v[88:91], a[192:195], v[156:159], 0\n"
"v_mfma_f32_16x16x32_bf16 v[88:91], a[196:199], v[112:115], v[88:91]\n"
"v_mfma_f32_16x16x32_bf16 v[88:91], a[200:203], v[108:111], v[88:91]\n"
"v_mfma_f32_16x16x32_bf16 v[88:91], a[204:207], v[196:199], v[88:91]\n"
"buffer_load_dwordx4 v[184:187], v1, s[4:7], s98 offen sc1\n"
"v_mfma_f32_16x16x32_bf16 v[88:91], a[208:211], v[200:203], v[88:91]\n"
"ds_read_b128 v[96:99], v2\n"
"ds_read_b128 v[100:103], v2 offset:1024\n"
"v_mfma_f32_16x16x32_bf16 v[88:91], a[212:215], v[120:123], v[88:91]\n"
"ds_write_b128 v3, v[92:95] offset:7168\n"
"v_mfma_f32_16x16x32_bf16 v[88:91], a[216:219], v[116:119], v[88:91]\n"
"v_mfma_f32_16x16x32_bf16 v[88:91], a[220:223], v[132:135], v[88:91]\n"
"buffer_load_dwordx4 v[188:191], v1, s[4:7], s98 offen offset:1024 sc1\n"
"v_mfma_f32_16x16x32_bf16 v[92:95], a[224:227], v[156:159], 0\n"
"v_mfma_f32_16x16x32_bf16 v[92:95], a[228:231], v[112:115], v[92:95]\n"
"s_waitcnt lgkmcnt(1)\n"
"v_lshlrev_b32_e32 v255, 16, v96\n"
"v_and_b32_e32 v254, 0xffff0000, v96\n"
"v_mfma_f32_16x16x32_bf16 v[92:95], a[232:235], v[108:111], v[92:95]\n"
"v_lshlrev_b32_e32 v253, 16, v97\n"
"v_and_b32_e32 v252, 0xffff0000, v97\n"
"v_mfma_f32_16x16x32_bf16 v[92:95], a[236:239], v[196:199], v[92:95]\n"
"buffer_load_dwordx4 v[192:195], v1, s[4:7], s98 offen offset:2048 sc1\n"
"v_mfma_f32_16x16x32_bf16 v[92:95], a[240:243], v[200:203], v[92:95]\n"
"v_lshlrev_b32_e32 v251, 16, v98\n"
"v_and_b32_e32 v250, 0xffff0000, v98\n"
"v_mfma_f32_16x16x32_bf16 v[92:95], a[244:247], v[120:123], v[92:95]\n"
"ds_write_b128 v3, v[88:91] offset:8192\n"
"v_mfma_f32_16x16x32_bf16 v[92:95], a[248:251], v[116:119], v[92:95]\n"
"v_lshlrev_b32_e32 v249, 16, v99\n"
"v_and_b32_e32 v248, 0xffff0000, v99\n"
"v_mfma_f32_16x16x32_bf16 v[92:95], a[252:255], v[132:135], v[92:95]\n"
"s_waitcnt vmcnt(11)\n"
"buffer_load_dwordx4 v[196:199], v1, s[4:7], s98 offen offset:3072 sc1\n"
"; @A G2 a.2\n"
"v_mfma_f32_16x16x32_bf16 v[96:99], a[128:131], v[140:143], 0\n"
"v_lshlrev_b32_e32 v247, 16, v100\n"
"v_and_b32_e32 v246, 0xffff0000, v100\n"
"v_mfma_f32_16x16x32_bf16 v[96:99], a[132:135], v[144:147], v[96:99]\n"
"v_lshlrev_b32_e32 v245, 16, v101\n"
"v_and_b32_e32 v244, 0xffff0000, v101\n"
"v_mfma_f32_16x16x32_bf16 v[96:99], a[136:139], v[152:155], v[96:99]\n"
"v_lshlrev_b32_e32 v243, 16, v102\n"
"v_and_b32_e32 v242, 0xffff0000, v102\n"
"v_mfma_f32_16x16x32_bf16 v[96:99], a[140:143], v[160:163], v[96:99]\n"
"s_waitcnt vmcnt(8)\n"
"v_lshlrev_b32_e32 v241, 16, v103\n"
"v_and_b32_e32 v240, 0xffff0000, v103\n"
"v_mfma_f32_16x16x32_bf16 v[96:99], a[144:147], v[164:167], v[96:99]\n"
"v_mfma_f32_16x16x32_bf16 v[96:99], a[148:151], v[148:151], v[96:99]\n"
"ds_write_b128 v3, v[92:95] offset:9216\n"
"v_mfma_f32_16x16x32_bf16 v[96:99], a[152:155], v[124:127], v[96:99]\n"
"v_mfma_f32_16x16x32_bf16 v[96:99], a[156:159], v[128:131], v[96:99]\n"
"v_mfma_f32_16x16x32_bf16 v[104:107], a[160:163], v[140:143], 0\n"
"s_add_u32 s98, s52, 0x440000\n"
"buffer_load_dwordx4 v[88:91], v0, s[4:7], s98 offen sc1\n"
"v_mfma_f32_16x16x32_bf16 v[104:107], a[164:167], v[144:147], v[104:107]\n"
"s_waitcnt lgkmcnt(0)\n"
"s_barrier\n"
"v_mfma_f32_16x16x32_bf16 v[104:107], a[168:171], v[152:155], v[104:107]\n"
"ds_read_b128 v[156:159], v4\n"
"ds_read_b128 v[112:115], v5\n"
"v_mfma_f32_16x16x32_bf16 v[104:107], a[172:175], v[160:163], v[104:107]\n"
"buffer_load_dwordx4 v[92:95], v0, s[4:7], s98 offen offset:1024 sc1\n"
"v_mfma_f32_16x16x32_bf16 v[104:107], a[176:179], v[164:167], v[104:107]\n"
"v_cmp_u_f32_e32 vcc, v96, v96\n"
"v_mfma_f32_16x16x32_bf16 v[104:107], a[180:183], v[148:151], v[104:107]\n"
"s_cbranch_vccnz .Lmk_p117s\n"
".Lmk_p117r:\n"
"v_mfma_f32_16x16x32_bf16 v[104:107], a[184:187], v[124:127], v[104:107]\n"
"ds_write_b128 v3, v[96:99] offset:12288\n"
"v_mfma_f32_16x16x32_bf16 v[104:107], a[188:191], v[128:131], v[104:107]\n"
"buffer_load_dwordx4 v[96:99], v0, s[4:7], s98 offen offset:2048 sc1\n"
"v_mfma_f32_16x16x32_bf16 v[120:123], a[192:195], v[140:143], 0\n"
"ds_read_b128 v[200:203], v4 offset:1024\n"
"ds_read_b128 v[132:135], v5 offset:1024\n"
"v_mfma_f32_16x16x32_bf16 v[120:123], a[196:199], v[144:147], v[120:123]\n"
"s_waitcnt lgkmcnt(3)\n"
"v_add_f32_e32 v156, v156, v255\n"
"v_add_f32_e32 v157, v157, v254\n"
"v_mfma_f32_16x16x32_bf16 v[120:123], a[200:203], v[152:155], v[120:123]\n"
"buffer_load_dwordx4 v[100:103], v0, s[4:7], s98 offen offset:3072 sc1\n"
"v_mfma_f32_16x16x32_bf16 v[120:123], a[204:207], v[160:163], v[120:123]\n"
"v_add_f32_e32 v158, v158, v253\n"
"v_add_f32_e32 v159, v159, v252\n"
"v_mfma_f32_16x16x32_bf16 v[120:123], a[208:211], v[164:167], v[120:123]\n"
"v_add_f32_e32 v156, v156, v112\n"
"v_add_f32_e32 v157, v157, v113\n"
"v_mfma_f32_16x16x32_bf16 v[120:123], a[212:215], v[148:151], v[120:123]\n"
"ds_write_b128 v3, v[104:107] offset:13312\n"
"v_mfma_f32_16x16x32_bf16 v[120:123], a[216:219], v[124:127], v[120:123]\n"
"buffer_load_dwordx4 v[104:107], v1, s[4:7], s98 offen sc1\n"
"v_mfma_f32_16x16x32_bf16 v[120:123], a[220:223], v[128:131], v[120:123]\n"
"ds_read_b128 v[108:111], v4 offset:2048\n"
"ds_read_b128 v[136:139], v5 offset:2048\n"
"v_mfma_f32_16x16x32_bf16 v[204:207], a[224:227], v[140:143], 0\n"
"s_waitcnt lgkmcnt(3)\n"
"v_add_f32_e32 v158, v158, v114\n"
"v_add_f32_e32 v159, v159, v115\n"
"v_mfma_f32_16x16x32_bf16 v[204:207], a[228:231], v[144:147], v[204:207]\n"
"buffer_load_dwordx4 v[112:115], v1, s[4:7], s98 offen offset:1024 sc1\n"
"v_mfma_f32_16x16x32_bf16 v[204:207], a[232:235], v[152:155], v[204:207]\n"
"v_add_f32_e32 v200, v200, v251\n"
"v_add_f32_e32 v201, v201, v250\n"
"v_mfma_f32_16x16x32_bf16 v[204:207], a[236:239], v[160:163], v[204:207]\n"
"v_add_f32_e32 v202, v202, v249\n"
"v_add_f32_e32 v203, v203, v248\n"
"v_mfma_f32_16x16x32_bf16 v[204:207], a[240:243], v[164:167], v[204:207]\n"
"buffer_load_dwordx4 v[116:119], v1, s[4:7], s98 offen offset:2048 sc1\n"
"v_mfma_f32_16x16x32_bf16 v[204:207], a[244:247], v[148:151], v[204:207]\n"
"ds_write_b128 v3, v[120:123] offset:14336\n"
"v_mfma_f32_16x16x32_bf16 v[204:207], a[248:251], v[124:127], v[204:207]\n"
"ds_read_b128 v[120:123], v4 offset:3072\n"
"ds_read_b128 v[144:147], v5 offset:3072\n"
"v_mfma_f32_16x16x32_bf16 v[204:207], a[252:255], v[128:131], v[204:207]\n"
"s_waitcnt vmcnt(11)\n"
"buffer_load_dwordx4 v[124:127], v1, s[4:7], s98 offen offset:3072 sc1\n"
"; @A G2 a.3\n"
"v_mfma_f32_16x16x32_bf16 v[164:167], a[128:131], v[172:175], v[156:159]\n"
"s_add_u32 s98, s53, 0x440000\n"
"v_add_f32_e32 v200, v200, v132\n"
"v_add_f32_e32 v201, v201, v133\n"
"v_mfma_f32_16x16x32_bf16 v[164:167], a[132:135], v[168:171], v[164:167]\n"
"s_waitcnt lgkmcnt(3)\n"
"v_add_f32_e32 v202, v202, v134\n"
"v_add_f32_e32 v203, v203, v135\n"
"v_mfma_f32_16x16x32_bf16 v[164:167], a[136:139], v[176:179], v[164:167]\n"
"buffer_load_dwordx4 v[128:131], v0, s[4:7], s98 offen sc1\n"
"v_mfma_f32_16x16x32_bf16 v[164:167], a[140:143], v[180:183], v[164:167]\n"
"s_waitcnt vmcnt(9)\n"
"v_add_f32_e32 v108, v108, v247\n"
"v_add_f32_e32 v109, v109, v246\n"
"v_mfma_f32_16x16x32_bf16 v[164:167], a[144:147], v[184:187], v[164:167]\n"
"v_add_f32_e32 v110, v110, v245\n"
"v_add_f32_e32 v111, v111, v244\n"
"v_mfma_f32_16x16x32_bf16 v[164:167], a[148:151], v[188:191], v[164:167]\n"
"ds_write_b128 v3, v[204:207] offset:15360\n"
"v_mfma_f32_16x16x32_bf16 v[164:167], a[152:155], v[192:195], v[164:167]\n"
"buffer_load_dwordx4 v[132:135], v0, s[4:7], s98 offen offset:1024 sc1\n"
"v_mfma_f32_16x16x32_bf16 v[164:167], a[156:159], v[196:199], v[164:167]\n"
"v_add_f32_e32 v108, v108, v136\n"
"v_add_f32_e32 v109, v109, v137\n"
"v_mfma_f32_16x16x32_bf16 v[204:207], a[160:163], v[172:175], v[200:203]\n"
"s_waitcnt lgkmcnt(1)\n"
"v_add_f32_e32 v110, v110, v138\n"
"v_add_f32_e32 v111, v111, v139\n"
"v_mfma_f32_16x16x32_bf16 v[204:207], a[164:167], v[168:171], v[204:207]\n"
"s_waitcnt lgkmcnt(0)\n"
"s_barrier\n"
"v_mfma_f32_16x16x32_bf16 v[204:207], a[168:171], v[176:179], v[204:207]\n"
"buffer_load_dwordx4 v[136:139], v0, s[4:7], s98 offen offset:2048 sc1\n"
"v_mfma_f32_16x16x32_bf16 v[204:207], a[172:175], v[180:183], v[204:207]\n"
"v_add_f32_e32 v120, v120, v243\n"
"v_add_f32_e32 v121, v121, v242\n"
"v_mfma_f32_16x16x32_bf16 v[204:207], a[176:179], v[184:187], v[204:207]\n"
"v_cmp_u_f32_e32 vcc, v164, v164\n"
"v_add_f32_e32 v122, v122, v241\n"
"v_mfma_f32_16x16x32_bf16 v[204:207], a[180:183], v[188:191], v[204:207]\n"
"s_cbranch_vccnz .Lmk_p118s\n"
".Lmk_p118r:\n"
"v_add_f32_e32 v123, v123, v240\n"
"v_mfma_f32_16x16x32_bf16 v[204:207], a[184:187], v[192:195], v[204:207]\n"
"buffer_load_dwordx4 v[140:143], v0, s[4:7], s98 offen offset:3072 sc1\n"
"v_mfma_f32_16x16x32_bf16 v[204:207], a[188:191], v[196:199], v[204:207]\n"
"v_add_f32_e32 v120, v120, v144\n"
"v_add_f32_e32 v121, v121, v145\n"
"v_mfma_f32_16x16x32_bf16 v[108:111], a[192:195], v[172:175], v[108:111]\n"
"v_add_f32_e32 v122, v122, v146\n"
"v_add_f32_e32 v123, v123, v147\n"
"v_mfma_f32_16x16x32_bf16 v[108:111], a[196:199], v[168:171], v[108:111]\n"
"buffer_load_dwordx4 v[144:147], v1, s[4:7], s98 offen sc1\n"
"v_mfma_f32_16x16x32_bf16 v[108:111], a[200:203], v[176:179], v[108:111]\n"
"ds_read_b128 v[200:203], v6\n"
"ds_read_b128 v[208:211], v6 offset:1024\n"
"v_mfma_f32_16x16x32_bf16 v[108:111], a[204:207], v[180:183], v[108:111]\n"
"ds_read_b128 v[152:155], v6 offset:2048\n"
"ds_read_b128 v[156:159], v6 offset:3072\n"
"v_mfma_f32_16x16x32_bf16 v[108:111], a[208:211], v[184:187], v[108:111]\n"
"buffer_load_dwordx4 v[148:151], v1, s[4:7], s98 offen offset:1024 sc1\n"
"v_mfma_f32_16x16x32_bf16 v[108:111], a[212:215], v[188:191], v[108:111]\n"
"v_mfma_f32_16x16x32_bf16 v[108:111], a[216:219], v[192:195], v[108:111]\n"
"v_mfma_f32_16x16x32_bf16 v[108:111], a[220:223], v[196:199], v[108:111]\n"
"buffer_load_dwordx4 v[160:163], v1, s[4:7], s98 offen offset:2048 sc1\n"
"v_mfma_f32_16x16x32_bf16 v[120:123], a[224:227], v[172:175], v[120:123]\n"
"s_waitcnt lgkmcnt(0)\n"
"v_add_f32_e32 v200, v164, v200\n"
"v_add_f32_e32 v201, v165, v201\n"
"v_mfma_f32_16x16x32_bf16 v[120:123], a[228:231], v[168:171], v[120:123]\n"
"v_add_f32_e32 v202, v166, v202\n"
"v_add_f32_e32 v203, v167, v203\n"
"v_mfma_f32_16x16x32_bf16 v[120:123], a[232:235], v[176:179], v[120:123]\n"
"buffer_load_dwordx4 v[164:167], v1, s[4:7], s98 offen offset:3072 sc1\n"
"v_mfma_f32_16x16x32_bf16 v[120:123], a[236:239], v[180:183], v[120:123]\n"
"v_add_f32_e32 v208, v204, v208\n"
"v_add_f32_e32 v209, v205, v209\n"
"v_mfma_f32_16x16x32_bf16 v[120:123], a[240:243], v[184:187], v[120:123]\n"
"v_add_f32_e32 v210, v206, v210\n"
"v_add_f32_e32 v211, v207, v211\n"
"v_mfma_f32_16x16x32_bf16 v[120:123], a[244:247], v[188:191], v[120:123]\n"
"v_cvt_pk_bf16_f32 v168, v200, v201\n"
"v_cvt_pk_bf16_f32 v169, v202, v203\n"
"v_mfma_f32_16x16x32_bf16 v[120:123], a[248:251], v[192:195], v[120:123]\n"
"s_add_u32 s98, s61, 0x400000\n"
"v_cvt_pk_bf16_f32 v170, v208, v209\n"
"v_cvt_pk_bf16_f32 v171, v210, v211\n"
"v_mfma_f32_16x16x32_bf16 v[120:123], a[252:255], v[196:199], v[120:123]\n"
"buffer_store_dwordx4 v[168:171], v0, s[4:7], s98 offen sc1\n"
"ds_write_b128 v2, v[168:171]\n"
"s_branch .Lmk_loop\n"
".Lmk_f1s:\n"
";\n"
"s_mov_b32 s1, 0x10000\n"
".Lmk_f1l:\n"
"buffer_load_dwordx2 v[250:251], v12, s[4:7], s98 offen sc1\n"
"s_waitcnt vmcnt(0)\n"
"v_cmp_ne_u64_e32 vcc, v[250:251], v[18:19]\n"
"s_cbranch_vccz .Lmk_f1r\n"
"s_sub_u32 s1, s1, 1\n"
"s_cbranch_scc0 .Lmk_f1l\n"
"s_branch .Lmk_f1r\n"
".Lmk_f2s:\n"
";\n"
"s_mov_b32 s1, 0x10000\n"
".Lmk_f2l:\n"
"buffer_load_dwordx2 v[246:247], v12, s[4:7], s97 offen sc1\n"
"s_waitcnt vmcnt(0)\n"
"v_cmp_ne_u64_e32 vcc, v[246:247], v[18:19]\n"
"s_cbranch_vccz .Lmk_f2r\n"
"s_sub_u32 s1, s1, 1\n"
"s_cbranch_scc0 .Lmk_f2l\n"
"s_branch .Lmk_f2r\n"
".Lmk_f5s:\n"
";\n"
"s_mov_b32 s1, 0x10000\n"
".Lmk_f5l:\n"
"buffer_load_dwordx2 v[250:251], v41, s[4:7], s97 offen sc1\n"
"s_waitcnt vmcnt(0)\n"
"v_cmp_ne_u64_e32 vcc, v[250:251], v[18:19]\n"
"s_cbranch_vccz .Lmk_f5r\n"
"s_sub_u32 s1, s1, 1\n"
"s_cbranch_scc0 .Lmk_f5l\n"
"s_branch .Lmk_f5r\n"
".Lmk_w7s:\n"
"buffer_store_dwordx4 a[4:7], v0, s[4:7], s97 offen sc1\n"
"s_branch .Lmk_w7r\n"
".Lmk_w8s:\n"
"buffer_store_dwordx4 a[36:39], v0, s[4:7], s97 offen sc1\n"
"s_branch .Lmk_w8r\n"
".Lmk_w9s:\n"
"buffer_store_dwordx4 a[68:71], v0, s[4:7], s96 offen sc1\n"
"s_branch .Lmk_w9r\n"
".Lmk_w10s:\n"
"buffer_store_dwordx4 a[100:103], v0, s[4:7], s96 offen sc1\n"
"s_branch .Lmk_w10r\n"
".Lmk_w11s:\n"
"buffer_store_dwordx4 v[52:55], v0, s[4:7], s96 offen sc1\n"
"s_branch .Lmk_w11r\n"
".Lmk_w12s:\n"
"buffer_store_dwordx4 v[56:59], v0, s[4:7], s96 offen sc1\n"
"s_branch .Lmk_w12r\n"
".Lmk_f6s:\n"
";\n"
"s_mov_b32 s1, 0x10000\n"
".Lmk_f6l:\n"
"buffer_load_dwordx2 v[246:247], v13, s[4:7], s97 offen sc1\n"
"s_waitcnt vmcnt(0)\n"
"v_cmp_ne_u64_e32 vcc, v[246:247], v[18:19]\n"
"s_cbranch_vccz .Lmk_f6r\n"
"s_sub_u32 s1, s1, 1\n"
"s_cbranch_scc0 .Lmk_f6l\n"
"s_branch .Lmk_f6r\n"
".Lmk_f3s:\n"
";\n"
"s_mov_b32 s1, 0x10000\n"
".Lmk_f3l:\n"
"buffer_load_dwordx2 v[250:251], off, s[4:7], s96 sc1\n"
"s_waitcnt vmcnt(0)\n"
"v_cmp_ne_u64_e32 vcc, v[250:251], v[18:19]\n"
"s_cbranch_vccz .Lmk_f3r\n"
"s_sub_u32 s1, s1, 1\n"
"s_cbranch_scc0 .Lmk_f3l\n"
"s_branch .Lmk_f3r\n"
".Lmk_x23s:\n"
"s_nop 3\n"
"v_mov_b32_e32 v24, 0xff800000\n"
"v_mov_b32_e32 v25, 0xff800000\n"
"v_mov_b32_e32 v26, 0xff800000\n"
"v_mov_b32_e32 v27, 0xff800000\n"
"v_mov_b32_e32 v36, 0xff800000\n"
"v_mov_b32_e32 v37, 0xff800000\n"
"v_mov_b32_e32 v38, 0xff800000\n"
"v_mov_b32_e32 v39, 0xff800000\n"
"s_nop 1\n"
"s_branch .Lmk_x23r\n"
".Lmk_x24s:\n"
"s_nop 3\n"
"v_mov_b32_e32 v24, 0xff800000\n"
"v_mov_b32_e32 v25, 0xff800000\n"
"v_mov_b32_e32 v26, 0xff800000\n"
"v_mov_b32_e32 v27, 0xff800000\n"
"v_mov_b32_e32 v36, 0xff800000\n"
"v_mov_b32_e32 v37, 0xff800000\n"
"v_mov_b32_e32 v38, 0xff800000\n"
"v_mov_b32_e32 v39, 0xff800000\n"
"s_nop 1\n"
"s_branch .Lmk_x24r\n"
".Lmk_x25s:\n"
"s_nop 3\n"
"v_mov_b32_e32 v24, 0xff800000\n"
"v_mov_b32_e32 v25, 0xff800000\n"
"v_mov_b32_e32 v26, 0xff800000\n"
"v_mov_b32_e32 v27, 0xff800000\n"
"v_mov_b32_e32 v36, 0xff800000\n"
"v_mov_b32_e32 v37, 0xff800000\n"
"v_mov_b32_e32 v38, 0xff800000\n"
"v_mov_b32_e32 v39, 0xff800000\n"
"s_nop 1\n"
"s_branch .Lmk_x25r\n"
".Lmk_x26s:\n"
"s_nop 3\n"
"v_mov_b32_e32 v24, 0xff800000\n"
"v_mov_b32_e32 v25, 0xff800000\n"
"v_mov_b32_e32 v26, 0xff800000\n"
"v_mov_b32_e32 v27, 0xff800000\n"
"v_mov_b32_e32 v36, 0xff800000\n"
"v_mov_b32_e32 v37, 0xff800000\n"
"v_mov_b32_e32 v38, 0xff800000\n"
"v_mov_b32_e32 v39, 0xff800000\n"
"s_nop 1\n"
"s_branch .Lmk_x26r\n"
".Lmk_f19s:\n"
";\n"
"s_mov_b32 s1, 0x10000\n"
".Lmk_f19l:\n"
"buffer_load_dwordx2 v[238:239], off, s[4:7], s97 sc1\n"
"s_waitcnt vmcnt(0)\n"
"v_cmp_ne_u64_e32 vcc, v[238:239], v[18:19]\n"
"s_cbranch_vccz .Lmk_f19r\n"
"s_sub_u32 s1, s1, 1\n"
"s_cbranch_scc0 .Lmk_f19l\n"
"s_branch .Lmk_f19r\n"
".Lmk_b21:\n"
"s_cmp_lg_u32 s2, 0\n"
"s_cbranch_scc1 .Lmk_k27\n"
"s_mov_b32 s2, 1\n"
"s_waitcnt vmcnt(0) lgkmcnt(0)\n"
"s_nop 7\n"
"s_mov_b32 s97, 11\n"
"v_add_u32_e32 v248, 0x3800, v15\n"
"v_add_u32_e32 v247, 0x3800, v254\n"
"ds_read_b128 v[36:39], v15 offset:24576\n"
"ds_read_b128 v[48:51], v254 offset:24832\n"
"s_waitcnt lgkmcnt(0)\n"
"v_mfma_f32_16x16x32_bf16 v[52:55], v[36:39], v[28:31], 0\n"
"v_mfma_f32_16x16x32_bf16 v[56:59], v[36:39], v[32:35], 0\n"
"v_mfma_f32_16x16x32_bf16 v[36:39], v[48:51], v[28:31], 0\n"
"v_mfma_f32_16x16x32_bf16 v[60:63], v[48:51], v[32:35], 0\n"
".Lmk_b21l:\n"
"ds_read_b128 v[48:51], v248 offset:8192\n"
"ds_read_b128 v[64:67], v247 offset:8448\n"
"s_cmp_gt_i32 s78, s97\n"
"s_cselect_b32 s96, 0xff800000, 0\n"
"v_mov_b32_e32 v72, s96\n"
"v_mov_b32_e32 v73, s96\n"
"v_mov_b32_e32 v74, s96\n"
"v_mov_b32_e32 v75, s96\n"
"v_subrev_u32_e32 v248, 0x800, v248\n"
"v_subrev_u32_e32 v247, 0x800, v247\n"
"s_waitcnt lgkmcnt(0)\n"
"v_mfma_f32_16x16x32_bf16 v[76:79], v[48:51], v[28:31], v[72:75]\n"
"v_mfma_f32_16x16x32_bf16 v[80:83], v[48:51], v[32:35], v[72:75]\n"
"v_mfma_f32_16x16x32_bf16 v[48:51], v[64:67], v[28:31], v[72:75]\n"
"v_mfma_f32_16x16x32_bf16 v[84:87], v[64:67], v[32:35], v[72:75]\n"
"s_nop 7\n"
"s_cmp_lg_u32 s97, 4\n"
"s_cbranch_scc1 .Lmk_b21m\n"
"v_cndmask_b32_e64 v76, v52, v76, s[20:21]\n"
"v_cndmask_b32_e64 v77, v53, v77, s[22:23]\n"
"v_cndmask_b32_e64 v78, v54, v78, s[24:25]\n"
"v_cndmask_b32_e64 v79, v55, v79, s[26:27]\n"
"v_cndmask_b32_e64 v48, v36, v48, s[28:29]\n"
"v_cndmask_b32_e64 v49, v37, v49, s[30:31]\n"
"v_cndmask_b32_e64 v50, v38, v50, s[32:33]\n"
"v_cndmask_b32_e64 v51, v39, v51, s[34:35]\n"
"v_cndmask_b32_e64 v80, v56, v80, s[36:37]\n"
"v_cndmask_b32_e64 v81, v57, v81, s[38:39]\n"
"v_cndmask_b32_e64 v82, v58, v82, s[40:41]\n"
"v_cndmask_b32_e64 v83, v59, v83, s[42:43]\n"
"v_cndmask_b32_e64 v84, v60, v84, s[44:45]\n"
"v_cndmask_b32_e64 v85, v61, v85, s[46:47]\n"
"v_cndmask_b32_e64 v86, v62, v86, s[48:49]\n"
"v_cndmask_b32_e64 v87, v63, v87, s[50:51]\n"
".Lmk_b21m:\n"
"v_max3_f32 v253, v253, v76, v77\n"
"v_max3_f32 v251, v251, v80, v81\n"
"v_max3_f32 v253, v253, v78, v79\n"
"v_max3_f32 v251, v251, v82, v83\n"
"v_max3_f32 v253, v253, v48, v49\n"
"v_max3_f32 v251, v251, v84, v85\n"
"v_max3_f32 v253, v253, v50, v51\n"
"v_max3_f32 v251, v251, v86, v87\n"
"s_sub_u32 s97, s97, 1\n"
"s_cmp_ge_i32 s97, 4\n"
"s_cbranch_scc1 .Lmk_b21l\n"
"v_mov_b32_e32 v248, v253\n"
"v_mov_b32_e32 v247, v251\n"
"s_nop 0\n"
"v_permlane16_swap_b32_e32 v253, v248\n"
"v_permlane16_swap_b32_e32 v251, v247\n"
"v_max_f32_e32 v253, v253, v248\n"
"v_max_f32_e32 v251, v251, v247\n"
"v_mov_b32_e32 v248, v253\n"
"v_mov_b32_e32 v247, v251\n"
"s_nop 0\n"
"v_permlane32_swap_b32_e32 v253, v248\n"
"v_permlane32_swap_b32_e32 v251, v247\n"
"v_max_f32_e32 v253, v253, v248\n"
"v_max_f32_e32 v251, v251, v247\n"
"v_mov_b32_e32 v250, v253\n"
"v_mov_b32_e32 v249, v251\n"
"s_branch .Lmk_a20\n"
".Lmk_x30s:\n"
"s_nop 3\n"
"v_mov_b32_e32 v116, 0xff800000\n"
"v_mov_b32_e32 v117, 0xff800000\n"
"v_mov_b32_e32 v118, 0xff800000\n"
"v_mov_b32_e32 v119, 0xff800000\n"
"v_mov_b32_e32 v120, 0xff800000\n"
"v_mov_b32_e32 v121, 0xff800000\n"
"v_mov_b32_e32 v122, 0xff800000\n"
"v_mov_b32_e32 v123, 0xff800000\n"
"s_nop 1\n"
"s_branch .Lmk_x30r\n"
".Lmk_x31s:\n"
"s_nop 3\n"
"v_mov_b32_e32 v116, 0xff800000\n"
"v_mov_b32_e32 v117, 0xff800000\n"
"v_mov_b32_e32 v118, 0xff800000\n"
"v_mov_b32_e32 v119, 0xff800000\n"
"v_mov_b32_e32 v120, 0xff800000\n"
"v_mov_b32_e32 v121, 0xff800000\n"
"v_mov_b32_e32 v122, 0xff800000\n"
"v_mov_b32_e32 v123, 0xff800000\n"
"s_nop 1\n"
"s_branch .Lmk_x31r\n"
".Lmk_x32s:\n"
"s_nop 3\n"
"v_mov_b32_e32 v116, 0xff800000\n"
"v_mov_b32_e32 v117, 0xff800000\n"
"v_mov_b32_e32 v118, 0xff800000\n"
"v_mov_b32_e32 v119, 0xff800000\n"
"v_mov_b32_e32 v120, 0xff800000\n"
"v_mov_b32_e32 v121, 0xff800000\n"
"v_mov_b32_e32 v122, 0xff800000\n"
"v_mov_b32_e32 v123, 0xff800000\n"
"s_nop 1\n"
"s_branch .Lmk_x32r\n"
".Lmk_f4s:\n"
";\n"
"s_mov_b32 s1, 0x10000\n"
".Lmk_f4l:\n"
"buffer_load_dwordx2 v[246:247], v12, s[4:7], s96 offen sc1\n"
"s_waitcnt vmcnt(0)\n"
"v_cmp_ne_u64_e32 vcc, v[246:247], v[18:19]\n"
"s_cbranch_vccz .Lmk_f4r\n"
"s_sub_u32 s1, s1, 1\n"
"s_cbranch_scc0 .Lmk_f4l\n"
"s_branch .Lmk_f4r\n"
".Lmk_b29:\n"
"s_cmp_lg_u32 s2, 0\n"
"s_cbranch_scc1 .Lmk_k34\n"
"s_mov_b32 s2, 1\n"
"s_waitcnt vmcnt(0) lgkmcnt(0)\n"
"s_nop 7\n"
"s_mov_b32 s96, 7\n"
"v_add_u32_e32 v248, 0x3800, v15\n"
"v_add_u32_e32 v247, 0x3800, v254\n"
"ds_read_b128 v[176:179], v15 offset:16384\n"
"ds_read_b128 v[184:187], v254 offset:16640\n"
"s_waitcnt lgkmcnt(0)\n"
"v_mfma_f32_16x16x32_bf16 v[196:199], v[176:179], v[92:95], 0\n"
"v_mfma_f32_16x16x32_bf16 v[208:211], v[176:179], v[96:99], 0\n"
"v_mfma_f32_16x16x32_bf16 v[176:179], v[184:187], v[92:95], 0\n"
"v_mfma_f32_16x16x32_bf16 v[220:223], v[184:187], v[96:99], 0\n"
".Lmk_b29l:\n"
"ds_read_b128 v[184:187], v248\n"
"ds_read_b128 v[224:227], v247 offset:256\n"
"s_cmp_gt_i32 s78, s96\n"
"s_cselect_b32 s95, 0xff800000, 0\n"
"v_mov_b32_e32 v228, s95\n"
"v_mov_b32_e32 v229, s95\n"
"v_mov_b32_e32 v230, s95\n"
"v_mov_b32_e32 v231, s95\n"
"v_subrev_u32_e32 v248, 0x800, v248\n"
"v_subrev_u32_e32 v247, 0x800, v247\n"
"s_waitcnt lgkmcnt(0)\n"
"v_mfma_f32_16x16x32_bf16 v[232:235], v[184:187], v[92:95], v[228:231]\n"
"v_mfma_f32_16x16x32_bf16 v[236:239], v[184:187], v[96:99], v[228:231]\n"
"v_mfma_f32_16x16x32_bf16 v[184:187], v[224:227], v[92:95], v[228:231]\n"
"v_mfma_f32_16x16x32_bf16 v[240:243], v[224:227], v[96:99], v[228:231]\n"
"s_nop 7\n"
"s_cmp_lg_u32 s96, 0\n"
"s_cbranch_scc1 .Lmk_b29m\n"
"v_cndmask_b32_e64 v232, v196, v232, s[20:21]\n"
"v_cndmask_b32_e64 v233, v197, v233, s[22:23]\n"
"v_cndmask_b32_e64 v234, v198, v234, s[24:25]\n"
"v_cndmask_b32_e64 v235, v199, v235, s[26:27]\n"
"v_cndmask_b32_e64 v184, v176, v184, s[28:29]\n"
"v_cndmask_b32_e64 v185, v177, v185, s[30:31]\n"
"v_cndmask_b32_e64 v186, v178, v186, s[32:33]\n"
"v_cndmask_b32_e64 v187, v179, v187, s[34:35]\n"
"v_cndmask_b32_e64 v236, v208, v236, s[36:37]\n"
"v_cndmask_b32_e64 v237, v209, v237, s[38:39]\n"
"v_cndmask_b32_e64 v238, v210, v238, s[40:41]\n"
"v_cndmask_b32_e64 v239, v211, v239, s[42:43]\n"
"v_cndmask_b32_e64 v240, v220, v240, s[44:45]\n"
"v_cndmask_b32_e64 v241, v221, v241, s[46:47]\n"
"v_cndmask_b32_e64 v242, v222, v242, s[48:49]\n"
"v_cndmask_b32_e64 v243, v223, v243, s[50:51]\n"
".Lmk_b29m:\n"
"v_max3_f32 v253, v253, v232, v233\n"
"v_max3_f32 v251, v251, v236, v237\n"
"v_max3_f32 v253, v253, v234, v235\n"
"v_max3_f32 v251, v251, v238, v239\n"
"v_max3_f32 v253, v253, v184, v185\n"
"v_max3_f32 v251, v251, v240, v241\n"
"v_max3_f32 v253, v253, v186, v187\n"
"v_max3_f32 v251, v251, v242, v243\n"
"s_sub_u32 s96, s96, 1\n"
"s_cmp_ge_i32 s96, 0\n"
"s_cbranch_scc1 .Lmk_b29l\n"
"v_mov_b32_e32 v248, v253\n"
"v_mov_b32_e32 v247, v251\n"
"s_nop 0\n"
"v_permlane16_swap_b32_e32 v253, v248\n"
"v_permlane16_swap_b32_e32 v251, v247\n"
"v_max_f32_e32 v253, v253, v248\n"
"v_max_f32_e32 v251, v251, v247\n"
"v_mov_b32_e32 v248, v253\n"
"v_mov_b32_e32 v247, v251\n"
"s_nop 0\n"
"v_permlane32_swap_b32_e32 v253, v248\n"
"v_permlane32_swap_b32_e32 v251, v247\n"
"v_max_f32_e32 v253, v253, v248\n"
"v_max_f32_e32 v251, v251, v247\n"
"v_mov_b32_e32 v250, v253\n"
"v_mov_b32_e32 v249, v251\n"
"s_branch .Lmk_a28\n"
".Lmk_p35s:\n"
";\n"
"s_mov_b32 s1, 0x10000\n"
".Lmk_p35l:\n"
"s_add_u32 s98, s56, 0x40000\n"
"buffer_load_dwordx4 v[180:183], v0, s[4:7], s98 offen sc1\n"
"buffer_load_dwordx4 v[200:203], v0, s[4:7], s98 offen offset:1024 sc1\n"
"buffer_load_dwordx4 v[128:131], v0, s[4:7], s98 offen offset:2048 sc1\n"
"buffer_load_dwordx4 v[144:147], v0, s[4:7], s98 offen offset:3072 sc1\n"
"buffer_load_dwordx4 v[152:155], v1, s[4:7], s98 offen sc1\n"
"buffer_load_dwordx4 v[204:207], v1, s[4:7], s98 offen offset:1024 sc1\n"
"buffer_load_dwordx4 v[116:119], v1, s[4:7], s98 offen offset:2048 sc1\n"
"buffer_load_dwordx4 v[132:135], v1, s[4:7], s98 offen offset:3072 sc1\n"
"s_add_u32 s98, s57, 0x40000\n"
"buffer_load_dwordx4 v[216:219], v0, s[4:7], s98 offen sc1\n"
"buffer_load_dwordx4 v[212:215], v0, s[4:7], s98 offen offset:1024 sc1\n"
"buffer_load_dwordx4 v[192:195], v0, s[4:7], s98 offen offset:2048 sc1\n"
"buffer_load_dwordx4 v[100:103], v0, s[4:7], s98 offen offset:3072 sc1\n"
"buffer_load_dwordx4 v[188:191], v1, s[4:7], s98 offen sc1\n"
"buffer_load_dwordx4 v[104:107], v1, s[4:7], s98 offen offset:1024 sc1\n"
"buffer_load_dwordx4 v[112:115], v1, s[4:7], s98 offen offset:2048 sc1\n"
"buffer_load_dwordx4 v[124:127], v1, s[4:7], s98 offen offset:3072 sc1\n"
"s_add_u32 s98, s58, 0x40000\n"
"buffer_load_dwordx4 v[140:143], v0, s[4:7], s98 offen sc1\n"
"buffer_load_dwordx4 v[136:139], v0, s[4:7], s98 offen offset:1024 sc1\n"
"buffer_load_dwordx4 v[120:123], v0, s[4:7], s98 offen offset:2048 sc1\n"
"buffer_load_dwordx4 v[108:111], v0, s[4:7], s98 offen offset:3072 sc1\n"
"buffer_load_dwordx4 v[92:95], v1, s[4:7], s98 offen sc1\n"
"buffer_load_dwordx4 v[96:99], v1, s[4:7], s98 offen offset:1024 sc1\n"
"buffer_load_dwordx4 v[156:159], v1, s[4:7], s98 offen offset:2048 sc1\n"
"s_waitcnt vmcnt(0)\n"
"v_max_u32_e32 v255, v180, v181\n"
"v_max3_u32 v255, v255, v182, v183\n"
"v_max3_u32 v255, v255, v200, v201\n"
"v_max3_u32 v255, v255, v202, v203\n"
"v_max3_u32 v255, v255, v128, v129\n"
"v_max3_u32 v255, v255, v130, v131\n"
"v_max3_u32 v255, v255, v144, v145\n"
"v_max3_u32 v255, v255, v146, v147\n"
"v_max3_u32 v255, v255, v152, v153\n"
"v_max3_u32 v255, v255, v154, v155\n"
"v_max3_u32 v255, v255, v204, v205\n"
"v_max3_u32 v255, v255, v206, v207\n"
"v_max3_u32 v255, v255, v116, v117\n"
"v_max3_u32 v255, v255, v118, v119\n"
"v_max3_u32 v255, v255, v132, v133\n"
"v_max3_u32 v255, v255, v134, v135\n"
"v_cmp_eq_u32_e32 vcc, -1, v255\n"
"s_cbranch_vccz .Lmk_p35k\n"
"s_sub_u32 s1, s1, 1\n"
"s_cbranch_scc0 .Lmk_p35l\n"
".Lmk_p35k:\n"
"v_mfma_f32_16x16x32_bf16 v[88:91], a[128:131], v[180:183], 0\n"
"v_mfma_f32_16x16x32_bf16 v[88:91], a[132:135], v[200:203], v[88:91]\n"
"v_mfma_f32_16x16x32_bf16 v[88:91], a[136:139], v[128:131], v[88:91]\n"
"v_mfma_f32_16x16x32_bf16 v[88:91], a[140:143], v[144:147], v[88:91]\n"
"v_mfma_f32_16x16x32_bf16 v[88:91], a[144:147], v[152:155], v[88:91]\n"
"v_mfma_f32_16x16x32_bf16 v[88:91], a[148:151], v[204:207], v[88:91]\n"
"v_mfma_f32_16x16x32_bf16 v[88:91], a[152:155], v[116:119], v[88:91]\n"
"v_mfma_f32_16x16x32_bf16 v[88:91], a[156:159], v[132:135], v[88:91]\n"
"v_mfma_f32_16x16x32_bf16 v[148:151], a[160:163], v[180:183], 0\n"
"v_mfma_f32_16x16x32_bf16 v[148:151], a[164:167], v[200:203], v[148:151]\n"
"v_mfma_f32_16x16x32_bf16 v[148:151], a[168:171], v[128:131], v[148:151]\n"
"v_mfma_f32_16x16x32_bf16 v[148:151], a[172:175], v[144:147], v[148:151]\n"
"v_mfma_f32_16x16x32_bf16 v[148:151], a[176:179], v[152:155], v[148:151]\n"
"v_mfma_f32_16x16x32_bf16 v[148:151], a[180:183], v[204:207], v[148:151]\n"
"s_nop 7\n"
"s_branch .Lmk_p35r\n"
".Lmk_p36s:\n"
";\n"
"s_mov_b32 s1, 0x10000\n"
".Lmk_p36l:\n"
"s_add_u32 s97, s57, 0x40000\n"
"buffer_load_dwordx4 v[216:219], v0, s[4:7], s97 offen sc1\n"
"buffer_load_dwordx4 v[212:215], v0, s[4:7], s97 offen offset:1024 sc1\n"
"buffer_load_dwordx4 v[192:195], v0, s[4:7], s97 offen offset:2048 sc1\n"
"buffer_load_dwordx4 v[100:103], v0, s[4:7], s97 offen offset:3072 sc1\n"
"buffer_load_dwordx4 v[188:191], v1, s[4:7], s97 offen sc1\n"
"buffer_load_dwordx4 v[104:107], v1, s[4:7], s97 offen offset:1024 sc1\n"
"buffer_load_dwordx4 v[112:115], v1, s[4:7], s97 offen offset:2048 sc1\n"
"buffer_load_dwordx4 v[124:127], v1, s[4:7], s97 offen offset:3072 sc1\n"
"s_add_u32 s97, s58, 0x40000\n"
"buffer_load_dwordx4 v[140:143], v0, s[4:7], s97 offen sc1\n"
"buffer_load_dwordx4 v[136:139], v0, s[4:7], s97 offen offset:1024 sc1\n"
"buffer_load_dwordx4 v[120:123], v0, s[4:7], s97 offen offset:2048 sc1\n"
"buffer_load_dwordx4 v[108:111], v0, s[4:7], s97 offen offset:3072 sc1\n"
"buffer_load_dwordx4 v[92:95], v1, s[4:7], s97 offen sc1\n"
"buffer_load_dwordx4 v[96:99], v1, s[4:7], s97 offen offset:1024 sc1\n"
"buffer_load_dwordx4 v[156:159], v1, s[4:7], s97 offen offset:2048 sc1\n"
"buffer_load_dwordx4 v[88:91], v1, s[4:7], s97 offen offset:3072 sc1\n"
"s_add_u32 s97, s59, 0x40000\n"
"buffer_load_dwordx4 v[164:167], v0, s[4:7], s97 offen sc1\n"
"buffer_load_dwordx4 v[148:151], v0, s[4:7], s97 offen offset:1024 sc1\n"
"buffer_load_dwordx4 v[128:131], v0, s[4:7], s97 offen offset:2048 sc1\n"
"buffer_load_dwordx4 v[116:119], v0, s[4:7], s97 offen offset:3072 sc1\n"
"buffer_load_dwordx4 v[144:147], v1, s[4:7], s97 offen sc1\n"
"buffer_load_dwordx4 v[152:155], v1, s[4:7], s97 offen offset:1024 sc1\n"
"buffer_load_dwordx4 v[168:171], v1, s[4:7], s97 offen offset:2048 sc1\n"
"s_waitcnt vmcnt(0)\n"
"v_max_u32_e32 v255, v216, v217\n"
"v_max3_u32 v255, v255, v218, v219\n"
"v_max3_u32 v255, v255, v212, v213\n"
"v_max3_u32 v255, v255, v214, v215\n"
"v_max3_u32 v255, v255, v192, v193\n"
"v_max3_u32 v255, v255, v194, v195\n"
"v_max3_u32 v255, v255, v100, v101\n"
"v_max3_u32 v255, v255, v102, v103\n"
"v_max3_u32 v255, v255, v188, v189\n"
"v_max3_u32 v255, v255, v190, v191\n"
"v_max3_u32 v255, v255, v104, v105\n"
"v_max3_u32 v255, v255, v106, v107\n"
"v_max3_u32 v255, v255, v112, v113\n"
"v_max3_u32 v255, v255, v114, v115\n"
"v_max3_u32 v255, v255, v124, v125\n"
"v_max3_u32 v255, v255, v126, v127\n"
"v_cmp_eq_u32_e32 vcc, -1, v255\n"
"s_cbranch_vccz .Lmk_p36k\n"
"s_sub_u32 s1, s1, 1\n"
"s_cbranch_scc0 .Lmk_p36l\n"
".Lmk_p36k:\n"
"v_mfma_f32_16x16x32_bf16 v[132:135], a[128:131], v[216:219], 0\n"
"v_mfma_f32_16x16x32_bf16 v[132:135], a[132:135], v[212:215], v[132:135]\n"
"v_mfma_f32_16x16x32_bf16 v[132:135], a[136:139], v[192:195], v[132:135]\n"
"v_mfma_f32_16x16x32_bf16 v[132:135], a[140:143], v[100:103], v[132:135]\n"
"v_mfma_f32_16x16x32_bf16 v[132:135], a[144:147], v[188:191], v[132:135]\n"
"v_mfma_f32_16x16x32_bf16 v[132:135], a[148:151], v[104:107], v[132:135]\n"
"v_mfma_f32_16x16x32_bf16 v[132:135], a[152:155], v[112:115], v[132:135]\n"
"v_mfma_f32_16x16x32_bf16 v[132:135], a[156:159], v[124:127], v[132:135]\n"
"v_mfma_f32_16x16x32_bf16 v[160:163], a[160:163], v[216:219], 0\n"
"v_mfma_f32_16x16x32_bf16 v[160:163], a[164:167], v[212:215], v[160:163]\n"
"v_mfma_f32_16x16x32_bf16 v[160:163], a[168:171], v[192:195], v[160:163]\n"
"v_mfma_f32_16x16x32_bf16 v[160:163], a[172:175], v[100:103], v[160:163]\n"
"v_mfma_f32_16x16x32_bf16 v[160:163], a[176:179], v[188:191], v[160:163]\n"
"v_mfma_f32_16x16x32_bf16 v[160:163], a[180:183], v[104:107], v[160:163]\n"
"s_nop 7\n"
"s_branch .Lmk_p36r\n"
".Lmk_p37s:\n"
";\n"
"s_mov_b32 s1, 0x10000\n"
".Lmk_p37l:\n"
"s_add_u32 s97, s58, 0x40000\n"
"buffer_load_dwordx4 v[140:143], v0, s[4:7], s97 offen sc1\n"
"buffer_load_dwordx4 v[136:139], v0, s[4:7], s97 offen offset:1024 sc1\n"
"buffer_load_dwordx4 v[120:123], v0, s[4:7], s97 offen offset:2048 sc1\n"
"buffer_load_dwordx4 v[108:111], v0, s[4:7], s97 offen offset:3072 sc1\n"
"buffer_load_dwordx4 v[92:95], v1, s[4:7], s97 offen sc1\n"
"buffer_load_dwordx4 v[96:99], v1, s[4:7], s97 offen offset:1024 sc1\n"
"buffer_load_dwordx4 v[156:159], v1, s[4:7], s97 offen offset:2048 sc1\n"
"buffer_load_dwordx4 v[88:91], v1, s[4:7], s97 offen offset:3072 sc1\n"
"s_add_u32 s97, s59, 0x40000\n"
"buffer_load_dwordx4 v[164:167], v0, s[4:7], s97 offen sc1\n"
"buffer_load_dwordx4 v[148:151], v0, s[4:7], s97 offen offset:1024 sc1\n"
"buffer_load_dwordx4 v[128:131], v0, s[4:7], s97 offen offset:2048 sc1\n"
"buffer_load_dwordx4 v[116:119], v0, s[4:7], s97 offen offset:3072 sc1\n"
"buffer_load_dwordx4 v[144:147], v1, s[4:7], s97 offen sc1\n"
"buffer_load_dwordx4 v[152:155], v1, s[4:7], s97 offen offset:1024 sc1\n"
"buffer_load_dwordx4 v[168:171], v1, s[4:7], s97 offen offset:2048 sc1\n"
"buffer_load_dwordx4 v[132:135], v1, s[4:7], s97 offen offset:3072 sc1\n"
"s_waitcnt vmcnt(0)\n"
"v_max_u32_e32 v239, v140, v141\n"
"v_max3_u32 v239, v239, v142, v143\n"
"v_max3_u32 v239, v239, v136, v137\n"
"v_max3_u32 v239, v239, v138, v139\n"
"v_max3_u32 v239, v239, v120, v121\n"
"v_max3_u32 v239, v239, v122, v123\n"
"v_max3_u32 v239, v239, v108, v109\n"
"v_max3_u32 v239, v239, v110, v111\n"
"v_max3_u32 v239, v239, v92, v93\n"
"v_max3_u32 v239, v239, v94, v95\n"
"v_max3_u32 v239, v239, v96, v97\n"
"v_max3_u32 v239, v239, v98, v99\n"
"v_max3_u32 v239, v239, v156, v157\n"
"v_max3_u32 v239, v239, v158, v159\n"
"v_max3_u32 v239, v239, v88, v89\n"
"v_max3_u32 v239, v239, v90, v91\n"
"v_cmp_eq_u32_e32 vcc, -1, v239\n"
"s_cbranch_vccz .Lmk_p37k\n"
"s_sub_u32 s1, s1, 1\n"
"s_cbranch_scc0 .Lmk_p37l\n"
".Lmk_p37k:\n"
"v_mfma_f32_16x16x32_bf16 v[100:103], a[128:131], v[140:143], 0\n"
"v_mfma_f32_16x16x32_bf16 v[100:103], a[132:135], v[136:139], v[100:103]\n"
"v_mfma_f32_16x16x32_bf16 v[100:103], a[136:139], v[120:123], v[100:103]\n"
"v_mfma_f32_16x16x32_bf16 v[100:103], a[140:143], v[108:111], v[100:103]\n"
"v_mfma_f32_16x16x32_bf16 v[100:103], a[144:147], v[92:95], v[100:103]\n"
"v_mfma_f32_16x16x32_bf16 v[100:103], a[148:151], v[96:99], v[100:103]\n"
"v_mfma_f32_16x16x32_bf16 v[100:103], a[152:155], v[156:159], v[100:103]\n"
"v_mfma_f32_16x16x32_bf16 v[100:103], a[156:159], v[88:91], v[100:103]\n"
"v_mfma_f32_16x16x32_bf16 v[104:107], a[160:163], v[140:143], 0\n"
"v_mfma_f32_16x16x32_bf16 v[104:107], a[164:167], v[136:139], v[104:107]\n"
"v_mfma_f32_16x16x32_bf16 v[104:107], a[168:171], v[120:123], v[104:107]\n"
"v_mfma_f32_16x16x32_bf16 v[104:107], a[172:175], v[108:111], v[104:107]\n"
"v_mfma_f32_16x16x32_bf16 v[104:107], a[176:179], v[92:95], v[104:107]\n"
"v_mfma_f32_16x16x32_bf16 v[104:107], a[180:183], v[96:99], v[104:107]\n"
"s_nop 7\n"
"s_branch .Lmk_p37r\n"
".Lmk_p38s:\n"
";\n"
"s_mov_b32 s1, 0x10000\n"
".Lmk_p38l:\n"
"s_add_u32 s97, s59, 0x40000\n"
"buffer_load_dwordx4 v[164:167], v0, s[4:7], s97 offen sc1\n"
"buffer_load_dwordx4 v[148:151], v0, s[4:7], s97 offen offset:1024 sc1\n"
"buffer_load_dwordx4 v[128:131], v0, s[4:7], s97 offen offset:2048 sc1\n"
"buffer_load_dwordx4 v[116:119], v0, s[4:7], s97 offen offset:3072 sc1\n"
"buffer_load_dwordx4 v[144:147], v1, s[4:7], s97 offen sc1\n"
"buffer_load_dwordx4 v[152:155], v1, s[4:7], s97 offen offset:1024 sc1\n"
"buffer_load_dwordx4 v[168:171], v1, s[4:7], s97 offen offset:2048 sc1\n"
"buffer_load_dwordx4 v[132:135], v1, s[4:7], s97 offen offset:3072 sc1\n"
"s_add_u32 s97, s56, 0\n"
"buffer_load_dwordx4 v[100:103], v0, s[4:7], s97 offen sc1\n"
"buffer_load_dwordx4 v[180:183], v0, s[4:7], s97 offen offset:1024 sc1\n"
"buffer_load_dwordx4 v[104:107], v0, s[4:7], s97 offen offset:2048 sc1\n"
"buffer_load_dwordx4 v[124:127], v0, s[4:7], s97 offen offset:3072 sc1\n"
"buffer_load_dwordx4 v[92:95], v1, s[4:7], s97 offen sc1\n"
"buffer_load_dwordx4 v[88:91], v1, s[4:7], s97 offen offset:1024 sc1\n"
"buffer_load_dwordx4 v[136:139], v1, s[4:7], s97 offen offset:2048 sc1\n"
"buffer_load_dwordx4 v[140:143], v1, s[4:7], s97 offen offset:3072 sc1\n"
"s_add_u32 s97, s57, 0\n"
"buffer_load_dwordx4 v[160:163], v0, s[4:7], s97 offen sc1\n"
"s_waitcnt vmcnt(0)\n"
"v_max_u32_e32 v255, v164, v165\n"
"v_max3_u32 v255, v255, v166, v167\n"
"v_max3_u32 v255, v255, v148, v149\n"
"v_max3_u32 v255, v255, v150, v151\n"
"v_max3_u32 v255, v255, v128, v129\n"
"v_max3_u32 v255, v255, v130, v131\n"
"v_max3_u32 v255, v255, v116, v117\n"
"v_max3_u32 v255, v255, v118, v119\n"
"v_max3_u32 v255, v255, v144, v145\n"
"v_max3_u32 v255, v255, v146, v147\n"
"v_max3_u32 v255, v255, v152, v153\n"
"v_max3_u32 v255, v255, v154, v155\n"
"v_max3_u32 v255, v255, v168, v169\n"
"v_max3_u32 v255, v255, v170, v171\n"
"v_max3_u32 v255, v255, v132, v133\n"
"v_max3_u32 v255, v255, v134, v135\n"
"v_cmp_eq_u32_e32 vcc, -1, v255\n"
"s_cbranch_vccz .Lmk_p38k\n"
"s_sub_u32 s1, s1, 1\n"
"s_cbranch_scc0 .Lmk_p38l\n"
".Lmk_p38k:\n"
"v_mfma_f32_16x16x32_bf16 v[120:123], a[128:131], v[164:167], v[112:115]\n"
"v_mfma_f32_16x16x32_bf16 v[120:123], a[132:135], v[148:151], v[120:123]\n"
"v_mfma_f32_16x16x32_bf16 v[120:123], a[136:139], v[128:131], v[120:123]\n"
"v_mfma_f32_16x16x32_bf16 v[120:123], a[140:143], v[116:119], v[120:123]\n"
"v_mfma_f32_16x16x32_bf16 v[120:123], a[144:147], v[144:147], v[120:123]\n"
"v_mfma_f32_16x16x32_bf16 v[120:123], a[148:151], v[152:155], v[120:123]\n"
"v_mfma_f32_16x16x32_bf16 v[120:123], a[152:155], v[168:171], v[120:123]\n"
"v_mfma_f32_16x16x32_bf16 v[120:123], a[156:159], v[132:135], v[120:123]\n"
"v_mfma_f32_16x16x32_bf16 v[156:159], a[160:163], v[164:167], v[172:175]\n"
"v_mfma_f32_16x16x32_bf16 v[156:159], a[164:167], v[148:151], v[156:159]\n"
"v_mfma_f32_16x16x32_bf16 v[156:159], a[168:171], v[128:131], v[156:159]\n"
"v_mfma_f32_16x16x32_bf16 v[156:159], a[172:175], v[116:119], v[156:159]\n"
"v_mfma_f32_16x16x32_bf16 v[156:159], a[176:179], v[144:147], v[156:159]\n"
"v_mfma_f32_16x16x32_bf16 v[156:159], a[180:183], v[152:155], v[156:159]\n"
"s_nop 7\n"
"s_branch .Lmk_p38r\n"
".Lmk_p40s:\n"
";\n"
"s_mov_b32 s1, 0x10000\n"
".Lmk_p40l:\n"
"s_add_u32 s97, s56, 0\n"
"buffer_load_dwordx4 v[100:103], v0, s[4:7], s97 offen sc1\n"
"buffer_load_dwordx4 v[180:183], v0, s[4:7], s97 offen offset:1024 sc1\n"
"buffer_load_dwordx4 v[104:107], v0, s[4:7], s97 offen offset:2048 sc1\n"
"buffer_load_dwordx4 v[124:127], v0, s[4:7], s97 offen offset:3072 sc1\n"
"buffer_load_dwordx4 v[92:95], v1, s[4:7], s97 offen sc1\n"
"buffer_load_dwordx4 v[88:91], v1, s[4:7], s97 offen offset:1024 sc1\n"
"buffer_load_dwordx4 v[136:139], v1, s[4:7], s97 offen offset:2048 sc1\n"
"buffer_load_dwordx4 v[140:143], v1, s[4:7], s97 offen offset:3072 sc1\n"
"s_add_u32 s97, s57, 0\n"
"buffer_load_dwordx4 v[160:163], v0, s[4:7], s97 offen sc1\n"
"buffer_load_dwordx4 v[112:115], v0, s[4:7], s97 offen offset:1024 sc1\n"
"buffer_load_dwordx4 v[108:111], v0, s[4:7], s97 offen offset:2048 sc1\n"
"buffer_load_dwordx4 v[196:199], v0, s[4:7], s97 offen offset:3072 sc1\n"
"buffer_load_dwordx4 v[200:203], v1, s[4:7], s97 offen sc1\n"
"buffer_load_dwordx4 v[120:123], v1, s[4:7], s97 offen offset:1024 sc1\n"
"buffer_load_dwordx4 v[116:119], v1, s[4:7], s97 offen offset:2048 sc1\n"
"buffer_load_dwordx4 v[132:135], v1, s[4:7], s97 offen offset:3072 sc1\n"
"s_add_u32 s97, s58, 0\n"
"buffer_load_dwordx4 v[128:131], v0, s[4:7], s97 offen sc1\n"
"buffer_load_dwordx4 v[144:147], v0, s[4:7], s97 offen offset:1024 sc1\n"
"buffer_load_dwordx4 v[152:155], v0, s[4:7], s97 offen offset:2048 sc1\n"
"s_waitcnt vmcnt(0)\n"
"v_max_u32_e32 v255, v100, v101\n"
"v_max3_u32 v255, v255, v102, v103\n"
"v_max3_u32 v255, v255, v180, v181\n"
"v_max3_u32 v255, v255, v182, v183\n"
"v_max3_u32 v255, v255, v104, v105\n"
"v_max3_u32 v255, v255, v106, v107\n"
"v_max3_u32 v255, v255, v124, v125\n"
"v_max3_u32 v255, v255, v126, v127\n"
"v_max3_u32 v255, v255, v92, v93\n"
"v_max3_u32 v255, v255, v94, v95\n"
"v_max3_u32 v255, v255, v88, v89\n"
"v_max3_u32 v255, v255, v90, v91\n"
"v_max3_u32 v255, v255, v136, v137\n"
"v_max3_u32 v255, v255, v138, v139\n"
"v_max3_u32 v255, v255, v140, v141\n"
"v_max3_u32 v255, v255, v142, v143\n"
"v_cmp_eq_u32_e32 vcc, -1, v255\n"
"s_cbranch_vccz .Lmk_p40k\n"
"s_sub_u32 s1, s1, 1\n"
"s_cbranch_scc0 .Lmk_p40l\n"
".Lmk_p40k:\n"
"v_mfma_f32_16x16x32_bf16 v[96:99], a[128:131], v[100:103], 0\n"
"v_mfma_f32_16x16x32_bf16 v[96:99], a[132:135], v[180:183], v[96:99]\n"
"v_mfma_f32_16x16x32_bf16 v[96:99], a[136:139], v[104:107], v[96:99]\n"
"v_mfma_f32_16x16x32_bf16 v[96:99], a[140:143], v[124:127], v[96:99]\n"
"v_mfma_f32_16x16x32_bf16 v[96:99], a[144:147], v[92:95], v[96:99]\n"
"v_mfma_f32_16x16x32_bf16 v[96:99], a[148:151], v[88:91], v[96:99]\n"
"v_mfma_f32_16x16x32_bf16 v[96:99], a[152:155], v[136:139], v[96:99]\n"
"v_mfma_f32_16x16x32_bf16 v[96:99], a[156:159], v[140:143], v[96:99]\n"
"v_mfma_f32_16x16x32_bf16 v[148:151], a[160:163], v[100:103], 0\n"
"v_mfma_f32_16x16x32_bf16 v[148:151], a[164:167], v[180:183], v[148:151]\n"
"v_mfma_f32_16x16x32_bf16 v[148:151], a[168:171], v[104:107], v[148:151]\n"
"v_mfma_f32_16x16x32_bf16 v[148:151], a[172:175], v[124:127], v[148:151]\n"
"v_mfma_f32_16x16x32_bf16 v[148:151], a[176:179], v[92:95], v[148:151]\n"
"v_mfma_f32_16x16x32_bf16 v[148:151], a[180:183], v[88:91], v[148:151]\n"
"s_nop 7\n"
"s_branch .Lmk_p40r\n"
".Lmk_p41s:\n"
";\n"
"s_mov_b32 s1, 0x10000\n"
".Lmk_p41l:\n"
"s_add_u32 s96, s57, 0\n"
"buffer_load_dwordx4 v[160:163], v0, s[4:7], s96 offen sc1\n"
"buffer_load_dwordx4 v[112:115], v0, s[4:7], s96 offen offset:1024 sc1\n"
"buffer_load_dwordx4 v[108:111], v0, s[4:7], s96 offen offset:2048 sc1\n"
"buffer_load_dwordx4 v[196:199], v0, s[4:7], s96 offen offset:3072 sc1\n"
"buffer_load_dwordx4 v[200:203], v1, s[4:7], s96 offen sc1\n"
"buffer_load_dwordx4 v[120:123], v1, s[4:7], s96 offen offset:1024 sc1\n"
"buffer_load_dwordx4 v[116:119], v1, s[4:7], s96 offen offset:2048 sc1\n"
"buffer_load_dwordx4 v[132:135], v1, s[4:7], s96 offen offset:3072 sc1\n"
"s_add_u32 s96, s58, 0\n"
"buffer_load_dwordx4 v[128:131], v0, s[4:7], s96 offen sc1\n"
"buffer_load_dwordx4 v[144:147], v0, s[4:7], s96 offen offset:1024 sc1\n"
"buffer_load_dwordx4 v[152:155], v0, s[4:7], s96 offen offset:2048 sc1\n"
"buffer_load_dwordx4 v[156:159], v0, s[4:7], s96 offen offset:3072 sc1\n"
"buffer_load_dwordx4 v[164:167], v1, s[4:7], s96 offen sc1\n"
"buffer_load_dwordx4 v[148:151], v1, s[4:7], s96 offen offset:1024 sc1\n"
"buffer_load_dwordx4 v[124:127], v1, s[4:7], s96 offen offset:2048 sc1\n"
"buffer_load_dwordx4 v[136:139], v1, s[4:7], s96 offen offset:3072 sc1\n"
"s_add_u32 s96, s59, 0\n"
"buffer_load_dwordx4 v[172:175], v0, s[4:7], s96 offen sc1\n"
"buffer_load_dwordx4 v[168:171], v0, s[4:7], s96 offen offset:1024 sc1\n"
"buffer_load_dwordx4 v[176:179], v0, s[4:7], s96 offen offset:2048 sc1\n"
"s_waitcnt vmcnt(0)\n"
"v_max_u32_e32 v253, v160, v161\n"
"v_max3_u32 v253, v253, v162, v163\n"
"v_max3_u32 v253, v253, v112, v113\n"
"v_max3_u32 v253, v253, v114, v115\n"
"v_max3_u32 v253, v253, v108, v109\n"
"v_max3_u32 v253, v253, v110, v111\n"
"v_max3_u32 v253, v253, v196, v197\n"
"v_max3_u32 v253, v253, v198, v199\n"
"v_max3_u32 v253, v253, v200, v201\n"
"v_max3_u32 v253, v253, v202, v203\n"
"v_max3_u32 v253, v253, v120, v121\n"
"v_max3_u32 v253, v253, v122, v123\n"
"v_max3_u32 v253, v253, v116, v117\n"
"v_max3_u32 v253, v253, v118, v119\n"
"v_max3_u32 v253, v253, v132, v133\n"
"v_max3_u32 v253, v253, v134, v135\n"
"v_cmp_eq_u32_e32 vcc, -1, v253\n"
"s_cbranch_vccz .Lmk_p41k\n"
"s_sub_u32 s1, s1, 1\n"
"s_cbranch_scc0 .Lmk_p41l\n"
".Lmk_p41k:\n"
"v_mfma_f32_16x16x32_bf16 v[88:91], a[128:131], v[160:163], 0\n"
"v_mfma_f32_16x16x32_bf16 v[88:91], a[132:135], v[112:115], v[88:91]\n"
"v_mfma_f32_16x16x32_bf16 v[88:91], a[136:139], v[108:111], v[88:91]\n"
"v_mfma_f32_16x16x32_bf16 v[88:91], a[140:143], v[196:199], v[88:91]\n"
"v_mfma_f32_16x16x32_bf16 v[88:91], a[144:147], v[200:203], v[88:91]\n"
"v_mfma_f32_16x16x32_bf16 v[88:91], a[148:151], v[120:123], v[88:91]\n"
"v_mfma_f32_16x16x32_bf16 v[88:91], a[152:155], v[116:119], v[88:91]\n"
"v_mfma_f32_16x16x32_bf16 v[88:91], a[156:159], v[132:135], v[88:91]\n"
"v_mfma_f32_16x16x32_bf16 v[92:95], a[160:163], v[160:163], 0\n"
"v_mfma_f32_16x16x32_bf16 v[92:95], a[164:167], v[112:115], v[92:95]\n"
"v_mfma_f32_16x16x32_bf16 v[92:95], a[168:171], v[108:111], v[92:95]\n"
"v_mfma_f32_16x16x32_bf16 v[92:95], a[172:175], v[196:199], v[92:95]\n"
"v_mfma_f32_16x16x32_bf16 v[92:95], a[176:179], v[200:203], v[92:95]\n"
"v_mfma_f32_16x16x32_bf16 v[92:95], a[180:183], v[120:123], v[92:95]\n"
"s_nop 7\n"
"s_branch .Lmk_p41r\n"
".Lmk_p42s:\n"
";\n"
"s_mov_b32 s1, 0x10000\n"
".Lmk_p42l:\n"
"s_add_u32 s96, s58, 0\n"
"buffer_load_dwordx4 v[128:131], v0, s[4:7], s96 offen sc1\n"
"buffer_load_dwordx4 v[144:147], v0, s[4:7], s96 offen offset:1024 sc1\n"
"buffer_load_dwordx4 v[152:155], v0, s[4:7], s96 offen offset:2048 sc1\n"
"buffer_load_dwordx4 v[156:159], v0, s[4:7], s96 offen offset:3072 sc1\n"
"buffer_load_dwordx4 v[164:167], v1, s[4:7], s96 offen sc1\n"
"buffer_load_dwordx4 v[148:151], v1, s[4:7], s96 offen offset:1024 sc1\n"
"buffer_load_dwordx4 v[124:127], v1, s[4:7], s96 offen offset:2048 sc1\n"
"buffer_load_dwordx4 v[136:139], v1, s[4:7], s96 offen offset:3072 sc1\n"
"s_add_u32 s96, s59, 0\n"
"buffer_load_dwordx4 v[172:175], v0, s[4:7], s96 offen sc1\n"
"buffer_load_dwordx4 v[168:171], v0, s[4:7], s96 offen offset:1024 sc1\n"
"buffer_load_dwordx4 v[176:179], v0, s[4:7], s96 offen offset:2048 sc1\n"
"buffer_load_dwordx4 v[180:183], v0, s[4:7], s96 offen offset:3072 sc1\n"
"buffer_load_dwordx4 v[184:187], v1, s[4:7], s96 offen sc1\n"
"buffer_load_dwordx4 v[188:191], v1, s[4:7], s96 offen offset:1024 sc1\n"
"buffer_load_dwordx4 v[192:195], v1, s[4:7], s96 offen offset:2048 sc1\n"
"buffer_load_dwordx4 v[196:199], v1, s[4:7], s96 offen offset:3072 sc1\n"
"s_add_u32 s96, s52, 0x440000\n"
"buffer_load_dwordx4 v[88:91], v0, s[4:7], s96 offen sc1\n"
"buffer_load_dwordx4 v[92:95], v0, s[4:7], s96 offen offset:1024 sc1\n"
"s_waitcnt vmcnt(0)\n"
"v_max_u32_e32 v237, v128, v129\n"
"v_max3_u32 v237, v237, v130, v131\n"
"v_max3_u32 v237, v237, v144, v145\n"
"v_max3_u32 v237, v237, v146, v147\n"
"v_max3_u32 v237, v237, v152, v153\n"
"v_max3_u32 v237, v237, v154, v155\n"
"v_max3_u32 v237, v237, v156, v157\n"
"v_max3_u32 v237, v237, v158, v159\n"
"v_max3_u32 v237, v237, v164, v165\n"
"v_max3_u32 v237, v237, v166, v167\n"
"v_max3_u32 v237, v237, v148, v149\n"
"v_max3_u32 v237, v237, v150, v151\n"
"v_max3_u32 v237, v237, v124, v125\n"
"v_max3_u32 v237, v237, v126, v127\n"
"v_max3_u32 v237, v237, v136, v137\n"
"v_max3_u32 v237, v237, v138, v139\n"
"v_cmp_eq_u32_e32 vcc, -1, v237\n"
"s_cbranch_vccz .Lmk_p42k\n"
"s_sub_u32 s1, s1, 1\n"
"s_cbranch_scc0 .Lmk_p42l\n"
".Lmk_p42k:\n"
"v_mfma_f32_16x16x32_bf16 v[96:99], a[128:131], v[128:131], 0\n"
"v_mfma_f32_16x16x32_bf16 v[96:99], a[132:135], v[144:147], v[96:99]\n"
"v_mfma_f32_16x16x32_bf16 v[96:99], a[136:139], v[152:155], v[96:99]\n"
"v_mfma_f32_16x16x32_bf16 v[96:99], a[140:143], v[156:159], v[96:99]\n"
"v_mfma_f32_16x16x32_bf16 v[96:99], a[144:147], v[164:167], v[96:99]\n"
"v_mfma_f32_16x16x32_bf16 v[96:99], a[148:151], v[148:151], v[96:99]\n"
"v_mfma_f32_16x16x32_bf16 v[96:99], a[152:155], v[124:127], v[96:99]\n"
"v_mfma_f32_16x16x32_bf16 v[96:99], a[156:159], v[136:139], v[96:99]\n"
"v_mfma_f32_16x16x32_bf16 v[104:107], a[160:163], v[128:131], 0\n"
"v_mfma_f32_16x16x32_bf16 v[104:107], a[164:167], v[144:147], v[104:107]\n"
"v_mfma_f32_16x16x32_bf16 v[104:107], a[168:171], v[152:155], v[104:107]\n"
"v_mfma_f32_16x16x32_bf16 v[104:107], a[172:175], v[156:159], v[104:107]\n"
"v_mfma_f32_16x16x32_bf16 v[104:107], a[176:179], v[164:167], v[104:107]\n"
"v_mfma_f32_16x16x32_bf16 v[104:107], a[180:183], v[148:151], v[104:107]\n"
"s_nop 7\n"
"s_branch .Lmk_p42r\n"
".Lmk_f13s:\n"
";\n"
"s_mov_b32 s1, 0x10000\n"
".Lmk_f13l:\n"
"buffer_load_dwordx2 v[254:255], v13, s[4:7], s97 offen sc1\n"
"s_waitcnt vmcnt(0)\n"
"v_cmp_ne_u64_e32 vcc, v[254:255], v[18:19]\n"
"s_cbranch_vccz .Lmk_f13r\n"
"s_sub_u32 s1, s1, 1\n"
"s_cbranch_scc0 .Lmk_f13l\n"
"s_branch .Lmk_f13r\n"
".Lmk_p43s:\n"
";\n"
"s_mov_b32 s1, 0x10000\n"
".Lmk_p43l:\n"
"s_add_u32 s97, s59, 0\n"
"buffer_load_dwordx4 v[172:175], v0, s[4:7], s97 offen sc1\n"
"buffer_load_dwordx4 v[168:171], v0, s[4:7], s97 offen offset:1024 sc1\n"
"buffer_load_dwordx4 v[176:179], v0, s[4:7], s97 offen offset:2048 sc1\n"
"buffer_load_dwordx4 v[180:183], v0, s[4:7], s97 offen offset:3072 sc1\n"
"buffer_load_dwordx4 v[184:187], v1, s[4:7], s97 offen sc1\n"
"buffer_load_dwordx4 v[188:191], v1, s[4:7], s97 offen offset:1024 sc1\n"
"buffer_load_dwordx4 v[192:195], v1, s[4:7], s97 offen offset:2048 sc1\n"
"buffer_load_dwordx4 v[196:199], v1, s[4:7], s97 offen offset:3072 sc1\n"
"s_add_u32 s97, s52, 0x440000\n"
"buffer_load_dwordx4 v[88:91], v0, s[4:7], s97 offen sc1\n"
"buffer_load_dwordx4 v[92:95], v0, s[4:7], s97 offen offset:1024 sc1\n"
"buffer_load_dwordx4 v[96:99], v0, s[4:7], s97 offen offset:2048 sc1\n"
"buffer_load_dwordx4 v[100:103], v0, s[4:7], s97 offen offset:3072 sc1\n"
"buffer_load_dwordx4 v[104:107], v1, s[4:7], s97 offen sc1\n"
"buffer_load_dwordx4 v[112:115], v1, s[4:7], s97 offen offset:1024 sc1\n"
"buffer_load_dwordx4 v[116:119], v1, s[4:7], s97 offen offset:2048 sc1\n"
"buffer_load_dwordx4 v[124:127], v1, s[4:7], s97 offen offset:3072 sc1\n"
"s_add_u32 s97, s53, 0x440000\n"
"buffer_load_dwordx4 v[128:131], v0, s[4:7], s97 offen sc1\n"
"buffer_load_dwordx4 v[132:135], v0, s[4:7], s97 offen offset:1024 sc1\n"
"buffer_load_dwordx4 v[136:139], v0, s[4:7], s97 offen offset:2048 sc1\n"
"s_waitcnt vmcnt(0)\n"
"v_max_u32_e32 v255, v172, v173\n"
"v_max3_u32 v255, v255, v174, v175\n"
"v_max3_u32 v255, v255, v168, v169\n"
"v_max3_u32 v255, v255, v170, v171\n"
"v_max3_u32 v255, v255, v176, v177\n"
"v_max3_u32 v255, v255, v178, v179\n"
"v_max3_u32 v255, v255, v180, v181\n"
"v_max3_u32 v255, v255, v182, v183\n"
"v_max3_u32 v255, v255, v184, v185\n"
"v_max3_u32 v255, v255, v186, v187\n"
"v_max3_u32 v255, v255, v188, v189\n"
"v_max3_u32 v255, v255, v190, v191\n"
"v_max3_u32 v255, v255, v192, v193\n"
"v_max3_u32 v255, v255, v194, v195\n"
"v_max3_u32 v255, v255, v196, v197\n"
"v_max3_u32 v255, v255, v198, v199\n"
"v_cmp_eq_u32_e32 vcc, -1, v255\n"
"s_cbranch_vccz .Lmk_p43k\n"
"s_sub_u32 s1, s1, 1\n"
"s_cbranch_scc0 .Lmk_p43l\n"
".Lmk_p43k:\n"
"v_mfma_f32_16x16x32_bf16 v[208:211], a[128:131], v[172:175], v[140:143]\n"
"v_mfma_f32_16x16x32_bf16 v[208:211], a[132:135], v[168:171], v[208:211]\n"
"v_mfma_f32_16x16x32_bf16 v[208:211], a[136:139], v[176:179], v[208:211]\n"
"v_mfma_f32_16x16x32_bf16 v[208:211], a[140:143], v[180:183], v[208:211]\n"
"v_mfma_f32_16x16x32_bf16 v[208:211], a[144:147], v[184:187], v[208:211]\n"
"v_mfma_f32_16x16x32_bf16 v[208:211], a[148:151], v[188:191], v[208:211]\n"
"v_mfma_f32_16x16x32_bf16 v[208:211], a[152:155], v[192:195], v[208:211]\n"
"v_mfma_f32_16x16x32_bf16 v[208:211], a[156:159], v[196:199], v[208:211]\n"
"v_mfma_f32_16x16x32_bf16 v[204:207], a[160:163], v[172:175], v[160:163]\n"
"v_mfma_f32_16x16x32_bf16 v[204:207], a[164:167], v[168:171], v[204:207]\n"
"v_mfma_f32_16x16x32_bf16 v[204:207], a[168:171], v[176:179], v[204:207]\n"
"v_mfma_f32_16x16x32_bf16 v[204:207], a[172:175], v[180:183], v[204:207]\n"
"v_mfma_f32_16x16x32_bf16 v[204:207], a[176:179], v[184:187], v[204:207]\n"
"v_mfma_f32_16x16x32_bf16 v[204:207], a[180:183], v[188:191], v[204:207]\n"
"s_nop 7\n"
"s_branch .Lmk_p43r\n"
".Lmk_p48s:\n"
";\n"
"s_mov_b32 s1, 0x10000\n"
".Lmk_p48l:\n"
"s_add_u32 s96, s52, 0x440000\n"
"buffer_load_dwordx4 v[88:91], v0, s[4:7], s96 offen sc1\n"
"buffer_load_dwordx4 v[92:95], v0, s[4:7], s96 offen offset:1024 sc1\n"
"buffer_load_dwordx4 v[96:99], v0, s[4:7], s96 offen offset:2048 sc1\n"
"buffer_load_dwordx4 v[100:103], v0, s[4:7], s96 offen offset:3072 sc1\n"
"buffer_load_dwordx4 v[104:107], v1, s[4:7], s96 offen sc1\n"
"buffer_load_dwordx4 v[112:115], v1, s[4:7], s96 offen offset:1024 sc1\n"
"buffer_load_dwordx4 v[116:119], v1, s[4:7], s96 offen offset:2048 sc1\n"
"buffer_load_dwordx4 v[124:127], v1, s[4:7], s96 offen offset:3072 sc1\n"
"s_add_u32 s96, s53, 0x440000\n"
"buffer_load_dwordx4 v[128:131], v0, s[4:7], s96 offen sc1\n"
"buffer_load_dwordx4 v[132:135], v0, s[4:7], s96 offen offset:1024 sc1\n"
"buffer_load_dwordx4 v[136:139], v0, s[4:7], s96 offen offset:2048 sc1\n"
"buffer_load_dwordx4 v[140:143], v0, s[4:7], s96 offen offset:3072 sc1\n"
"buffer_load_dwordx4 v[144:147], v1, s[4:7], s96 offen sc1\n"
"buffer_load_dwordx4 v[148:151], v1, s[4:7], s96 offen offset:1024 sc1\n"
"buffer_load_dwordx4 v[160:163], v1, s[4:7], s96 offen offset:2048 sc1\n"
"buffer_load_dwordx4 v[164:167], v1, s[4:7], s96 offen offset:3072 sc1\n"
"s_add_u32 s96, s54, 0x440000\n"
"buffer_load_dwordx4 v[176:179], v0, s[4:7], s96 offen sc1\n"
"buffer_load_dwordx4 v[184:187], v0, s[4:7], s96 offen offset:1024 sc1\n"
"s_waitcnt vmcnt(0)\n"
"v_max_u32_e32 v252, v88, v89\n"
"v_max3_u32 v252, v252, v90, v91\n"
"v_max3_u32 v252, v252, v92, v93\n"
"v_max3_u32 v252, v252, v94, v95\n"
"v_max3_u32 v252, v252, v96, v97\n"
"v_max3_u32 v252, v252, v98, v99\n"
"v_max3_u32 v252, v252, v100, v101\n"
"v_max3_u32 v252, v252, v102, v103\n"
"v_max3_u32 v252, v252, v104, v105\n"
"v_max3_u32 v252, v252, v106, v107\n"
"v_max3_u32 v252, v252, v112, v113\n"
"v_max3_u32 v252, v252, v114, v115\n"
"v_max3_u32 v252, v252, v116, v117\n"
"v_max3_u32 v252, v252, v118, v119\n"
"v_max3_u32 v252, v252, v124, v125\n"
"v_max3_u32 v252, v252, v126, v127\n"
"v_cmp_eq_u32_e32 vcc, -1, v252\n"
"s_cbranch_vccz .Lmk_p48k\n"
"s_sub_u32 s1, s1, 1\n"
"s_cbranch_scc0 .Lmk_p48l\n"
".Lmk_p48k:\n"
"v_mfma_f32_16x16x32_bf16 v[172:175], a[0:3], v[88:91], 0\n"
"v_mfma_f32_16x16x32_bf16 v[172:175], a[4:7], v[92:95], v[172:175]\n"
"v_mfma_f32_16x16x32_bf16 v[172:175], a[8:11], v[96:99], v[172:175]\n"
"v_mfma_f32_16x16x32_bf16 v[172:175], a[12:15], v[100:103], v[172:175]\n"
"v_mfma_f32_16x16x32_bf16 v[172:175], a[16:19], v[104:107], v[172:175]\n"
"v_mfma_f32_16x16x32_bf16 v[172:175], a[20:23], v[112:115], v[172:175]\n"
"v_mfma_f32_16x16x32_bf16 v[172:175], a[24:27], v[116:119], v[172:175]\n"
"v_mfma_f32_16x16x32_bf16 v[172:175], a[28:31], v[124:127], v[172:175]\n"
"v_mfma_f32_16x16x32_bf16 v[188:191], a[32:35], v[88:91], 0\n"
"v_mfma_f32_16x16x32_bf16 v[188:191], a[36:39], v[92:95], v[188:191]\n"
"v_mfma_f32_16x16x32_bf16 v[188:191], a[40:43], v[96:99], v[188:191]\n"
"v_mfma_f32_16x16x32_bf16 v[188:191], a[44:47], v[100:103], v[188:191]\n"
"v_mfma_f32_16x16x32_bf16 v[188:191], a[48:51], v[104:107], v[188:191]\n"
"v_mfma_f32_16x16x32_bf16 v[188:191], a[52:55], v[112:115], v[188:191]\n"
"s_nop 7\n"
"s_branch .Lmk_p48r\n"
".Lmk_p49s:\n"
";\n"
"s_mov_b32 s1, 0x10000\n"
".Lmk_p49l:\n"
"s_add_u32 s96, s53, 0x440000\n"
"buffer_load_dwordx4 v[128:131], v0, s[4:7], s96 offen sc1\n"
"buffer_load_dwordx4 v[132:135], v0, s[4:7], s96 offen offset:1024 sc1\n"
"buffer_load_dwordx4 v[136:139], v0, s[4:7], s96 offen offset:2048 sc1\n"
"buffer_load_dwordx4 v[140:143], v0, s[4:7], s96 offen offset:3072 sc1\n"
"buffer_load_dwordx4 v[144:147], v1, s[4:7], s96 offen sc1\n"
"buffer_load_dwordx4 v[148:151], v1, s[4:7], s96 offen offset:1024 sc1\n"
"buffer_load_dwordx4 v[160:163], v1, s[4:7], s96 offen offset:2048 sc1\n"
"buffer_load_dwordx4 v[164:167], v1, s[4:7], s96 offen offset:3072 sc1\n"
"s_add_u32 s96, s54, 0x440000\n"
"buffer_load_dwordx4 v[176:179], v0, s[4:7], s96 offen sc1\n"
"buffer_load_dwordx4 v[184:187], v0, s[4:7], s96 offen offset:1024 sc1\n"
"buffer_load_dwordx4 v[108:111], v0, s[4:7], s96 offen offset:2048 sc1\n"
"buffer_load_dwordx4 v[192:195], v0, s[4:7], s96 offen offset:3072 sc1\n"
"buffer_load_dwordx4 v[152:155], v1, s[4:7], s96 offen sc1\n"
"buffer_load_dwordx4 v[156:159], v1, s[4:7], s96 offen offset:1024 sc1\n"
"buffer_load_dwordx4 v[172:175], v1, s[4:7], s96 offen offset:2048 sc1\n"
"buffer_load_dwordx4 v[180:183], v1, s[4:7], s96 offen offset:3072 sc1\n"
"s_add_u32 s96, s55, 0x440000\n"
"buffer_load_dwordx4 v[92:95], v0, s[4:7], s96 offen sc1\n"
"buffer_load_dwordx4 v[96:99], v0, s[4:7], s96 offen offset:1024 sc1\n"
"s_waitcnt vmcnt(0)\n"
"v_max_u32_e32 v247, v128, v129\n"
"v_max3_u32 v247, v247, v130, v131\n"
"v_max3_u32 v247, v247, v132, v133\n"
"v_max3_u32 v247, v247, v134, v135\n"
"v_max3_u32 v247, v247, v136, v137\n"
"v_max3_u32 v247, v247, v138, v139\n"
"v_max3_u32 v247, v247, v140, v141\n"
"v_max3_u32 v247, v247, v142, v143\n"
"v_max3_u32 v247, v247, v144, v145\n"
"v_max3_u32 v247, v247, v146, v147\n"
"v_max3_u32 v247, v247, v148, v149\n"
"v_max3_u32 v247, v247, v150, v151\n"
"v_max3_u32 v247, v247, v160, v161\n"
"v_max3_u32 v247, v247, v162, v163\n"
"v_max3_u32 v247, v247, v164, v165\n"
"v_max3_u32 v247, v247, v166, v167\n"
"v_cmp_eq_u32_e32 vcc, -1, v247\n"
"s_cbranch_vccz .Lmk_p49k\n"
"s_sub_u32 s1, s1, 1\n"
"s_cbranch_scc0 .Lmk_p49l\n"
".Lmk_p49k:\n"
"v_mfma_f32_16x16x32_bf16 v[88:91], a[0:3], v[128:131], 0\n"
"v_mfma_f32_16x16x32_bf16 v[88:91], a[4:7], v[132:135], v[88:91]\n"
"v_mfma_f32_16x16x32_bf16 v[88:91], a[8:11], v[136:139], v[88:91]\n"
"v_mfma_f32_16x16x32_bf16 v[88:91], a[12:15], v[140:143], v[88:91]\n"
"v_mfma_f32_16x16x32_bf16 v[88:91], a[16:19], v[144:147], v[88:91]\n"
"v_mfma_f32_16x16x32_bf16 v[88:91], a[20:23], v[148:151], v[88:91]\n"
"v_mfma_f32_16x16x32_bf16 v[88:91], a[24:27], v[160:163], v[88:91]\n"
"v_mfma_f32_16x16x32_bf16 v[88:91], a[28:31], v[164:167], v[88:91]\n"
"v_mfma_f32_16x16x32_bf16 v[100:103], a[32:35], v[128:131], 0\n"
"v_mfma_f32_16x16x32_bf16 v[100:103], a[36:39], v[132:135], v[100:103]\n"
"v_mfma_f32_16x16x32_bf16 v[100:103], a[40:43], v[136:139], v[100:103]\n"
"v_mfma_f32_16x16x32_bf16 v[100:103], a[44:47], v[140:143], v[100:103]\n"
"v_mfma_f32_16x16x32_bf16 v[100:103], a[48:51], v[144:147], v[100:103]\n"
"v_mfma_f32_16x16x32_bf16 v[100:103], a[52:55], v[148:151], v[100:103]\n"
"s_nop 7\n"
"s_branch .Lmk_p49r\n"
".Lmk_m52s:\n"
";\n"
"s_mov_b32 s1, 0x10000\n"
".Lmk_m52l:\n"
"s_add_u32 s98, s62, 0x44000\n"
"buffer_load_dword v255, v10, s[4:7], s98 offen sc1\n"
"buffer_load_dword v254, v10, s[4:7], s98 offen offset:256 sc1\n"
"buffer_load_dword v253, v10, s[4:7], s98 offen offset:512 sc1\n"
"buffer_load_dword v252, v10, s[4:7], s98 offen offset:768 sc1\n"
"buffer_load_dword v251, v10, s[4:7], s98 offen offset:1024 sc1\n"
"buffer_load_dword v250, v10, s[4:7], s98 offen offset:1280 sc1\n"
"buffer_load_dword v249, v10, s[4:7], s98 offen offset:1536 sc1\n"
"buffer_load_dword v248, v10, s[4:7], s98 offen offset:1792 sc1\n"
"s_waitcnt vmcnt(0)\n"
"v_max_u32_e32 v247, v255, v254\n"
"v_max3_u32 v247, v247, v253, v252\n"
"v_max3_u32 v247, v247, v251, v250\n"
"v_max3_u32 v247, v247, v249, v248\n"
"v_cmp_eq_u32_e32 vcc, -1, v247\n"
"s_cbranch_vccz .Lmk_m52k\n"
"s_sub_u32 s1, s1, 1\n"
"s_cbranch_scc0 .Lmk_m52l\n"
".Lmk_m52k:\n"
"v_add_f32_e32 v255, v255, v251\n"
"v_add_f32_e32 v254, v254, v250\n"
"v_add_f32_e32 v253, v253, v249\n"
"v_add_f32_e32 v252, v252, v248\n"
"v_add_f32_e32 v255, v255, v253\n"
"v_add_f32_e32 v254, v254, v252\n"
"v_add_f32_e32 v255, v255, v254\n"
"s_nop 1\n"
"s_branch .Lmk_m52r\n"
".Lmk_p50s:\n"
";\n"
"s_mov_b32 s1, 0x10000\n"
".Lmk_p50l:\n"
"s_add_u32 s95, s54, 0x440000\n"
"buffer_load_dwordx4 v[176:179], v0, s[4:7], s95 offen sc1\n"
"buffer_load_dwordx4 v[184:187], v0, s[4:7], s95 offen offset:1024 sc1\n"
"buffer_load_dwordx4 v[108:111], v0, s[4:7], s95 offen offset:2048 sc1\n"
"buffer_load_dwordx4 v[192:195], v0, s[4:7], s95 offen offset:3072 sc1\n"
"buffer_load_dwordx4 v[152:155], v1, s[4:7], s95 offen sc1\n"
"buffer_load_dwordx4 v[156:159], v1, s[4:7], s95 offen offset:1024 sc1\n"
"buffer_load_dwordx4 v[172:175], v1, s[4:7], s95 offen offset:2048 sc1\n"
"buffer_load_dwordx4 v[180:183], v1, s[4:7], s95 offen offset:3072 sc1\n"
"s_add_u32 s95, s55, 0x440000\n"
"buffer_load_dwordx4 v[92:95], v0, s[4:7], s95 offen sc1\n"
"buffer_load_dwordx4 v[96:99], v0, s[4:7], s95 offen offset:1024 sc1\n"
"buffer_load_dwordx4 v[104:107], v0, s[4:7], s95 offen offset:2048 sc1\n"
"buffer_load_dwordx4 v[112:115], v0, s[4:7], s95 offen offset:3072 sc1\n"
"buffer_load_dwordx4 v[100:103], v1, s[4:7], s95 offen sc1\n"
"buffer_load_dwordx4 v[124:127], v1, s[4:7], s95 offen offset:1024 sc1\n"
"buffer_load_dwordx4 v[168:171], v1, s[4:7], s95 offen offset:2048 sc1\n"
"buffer_load_dwordx4 v[188:191], v1, s[4:7], s95 offen offset:3072 sc1\n"
"s_add_u32 s95, s52, 0x448000\n"
"buffer_load_dwordx4 v[128:131], v0, s[4:7], s95 offen sc1\n"
"s_waitcnt vmcnt(0)\n"
"v_max_u32_e32 v255, v176, v177\n"
"v_max3_u32 v255, v255, v178, v179\n"
"v_max3_u32 v255, v255, v184, v185\n"
"v_max3_u32 v255, v255, v186, v187\n"
"v_max3_u32 v255, v255, v108, v109\n"
"v_max3_u32 v255, v255, v110, v111\n"
"v_max3_u32 v255, v255, v192, v193\n"
"v_max3_u32 v255, v255, v194, v195\n"
"v_max3_u32 v255, v255, v152, v153\n"
"v_max3_u32 v255, v255, v154, v155\n"
"v_max3_u32 v255, v255, v156, v157\n"
"v_max3_u32 v255, v255, v158, v159\n"
"v_max3_u32 v255, v255, v172, v173\n"
"v_max3_u32 v255, v255, v174, v175\n"
"v_max3_u32 v255, v255, v180, v181\n"
"v_max3_u32 v255, v255, v182, v183\n"
"v_cmp_eq_u32_e32 vcc, -1, v255\n"
"s_cbranch_vccz .Lmk_p50k\n"
"s_sub_u32 s1, s1, 1\n"
"s_cbranch_scc0 .Lmk_p50l\n"
".Lmk_p50k:\n"
"v_mfma_f32_16x16x32_bf16 v[88:91], a[0:3], v[176:179], 0\n"
"v_mfma_f32_16x16x32_bf16 v[88:91], a[4:7], v[184:187], v[88:91]\n"
"v_mfma_f32_16x16x32_bf16 v[88:91], a[8:11], v[108:111], v[88:91]\n"
"v_mfma_f32_16x16x32_bf16 v[88:91], a[12:15], v[192:195], v[88:91]\n"
"v_mfma_f32_16x16x32_bf16 v[88:91], a[16:19], v[152:155], v[88:91]\n"
"v_mfma_f32_16x16x32_bf16 v[88:91], a[20:23], v[156:159], v[88:91]\n"
"v_mfma_f32_16x16x32_bf16 v[88:91], a[24:27], v[172:175], v[88:91]\n"
"v_mfma_f32_16x16x32_bf16 v[88:91], a[28:31], v[180:183], v[88:91]\n"
"v_mfma_f32_16x16x32_bf16 v[116:119], a[32:35], v[176:179], 0\n"
"v_mfma_f32_16x16x32_bf16 v[116:119], a[36:39], v[184:187], v[116:119]\n"
"v_mfma_f32_16x16x32_bf16 v[116:119], a[40:43], v[108:111], v[116:119]\n"
"v_mfma_f32_16x16x32_bf16 v[116:119], a[44:47], v[192:195], v[116:119]\n"
"v_mfma_f32_16x16x32_bf16 v[116:119], a[48:51], v[152:155], v[116:119]\n"
"v_mfma_f32_16x16x32_bf16 v[116:119], a[52:55], v[156:159], v[116:119]\n"
"s_nop 7\n"
"s_branch .Lmk_p50r\n"
".Lmk_p51s:\n"
";\n"
"s_mov_b32 s1, 0x10000\n"
".Lmk_p51l:\n"
"s_add_u32 s96, s55, 0x440000\n"
"buffer_load_dwordx4 v[92:95], v0, s[4:7], s96 offen sc1\n"
"buffer_load_dwordx4 v[96:99], v0, s[4:7], s96 offen offset:1024 sc1\n"
"buffer_load_dwordx4 v[104:107], v0, s[4:7], s96 offen offset:2048 sc1\n"
"buffer_load_dwordx4 v[112:115], v0, s[4:7], s96 offen offset:3072 sc1\n"
"buffer_load_dwordx4 v[100:103], v1, s[4:7], s96 offen sc1\n"
"buffer_load_dwordx4 v[124:127], v1, s[4:7], s96 offen offset:1024 sc1\n"
"buffer_load_dwordx4 v[168:171], v1, s[4:7], s96 offen offset:2048 sc1\n"
"buffer_load_dwordx4 v[188:191], v1, s[4:7], s96 offen offset:3072 sc1\n"
"s_add_u32 s96, s52, 0x448000\n"
"buffer_load_dwordx4 v[128:131], v0, s[4:7], s96 offen sc1\n"
"buffer_load_dwordx4 v[88:91], v0, s[4:7], s96 offen offset:1024 sc1\n"
"buffer_load_dwordx4 v[160:163], v0, s[4:7], s96 offen offset:2048 sc1\n"
"buffer_load_dwordx4 v[196:199], v0, s[4:7], s96 offen offset:3072 sc1\n"
"buffer_load_dwordx4 v[136:139], v1, s[4:7], s96 offen sc1\n"
"buffer_load_dwordx4 v[220:223], v1, s[4:7], s96 offen offset:1024 sc1\n"
"buffer_load_dwordx4 v[148:151], v1, s[4:7], s96 offen offset:2048 sc1\n"
"buffer_load_dwordx4 v[156:159], v1, s[4:7], s96 offen offset:3072 sc1\n"
"s_add_u32 s96, s53, 0x448000\n"
"buffer_load_dwordx4 v[184:187], v0, s[4:7], s96 offen sc1\n"
"buffer_load_dwordx4 v[116:119], v0, s[4:7], s96 offen offset:1024 sc1\n"
"s_waitcnt vmcnt(0)\n"
"v_max_u32_e32 v252, v92, v93\n"
"v_max3_u32 v252, v252, v94, v95\n"
"v_max3_u32 v252, v252, v96, v97\n"
"v_max3_u32 v252, v252, v98, v99\n"
"v_max3_u32 v252, v252, v104, v105\n"
"v_max3_u32 v252, v252, v106, v107\n"
"v_max3_u32 v252, v252, v112, v113\n"
"v_max3_u32 v252, v252, v114, v115\n"
"v_max3_u32 v252, v252, v100, v101\n"
"v_max3_u32 v252, v252, v102, v103\n"
"v_max3_u32 v252, v252, v124, v125\n"
"v_max3_u32 v252, v252, v126, v127\n"
"v_max3_u32 v252, v252, v168, v169\n"
"v_max3_u32 v252, v252, v170, v171\n"
"v_max3_u32 v252, v252, v188, v189\n"
"v_max3_u32 v252, v252, v190, v191\n"
"v_cmp_eq_u32_e32 vcc, -1, v252\n"
"s_cbranch_vccz .Lmk_p51k\n"
"s_sub_u32 s1, s1, 1\n"
"s_cbranch_scc0 .Lmk_p51l\n"
".Lmk_p51k:\n"
"v_mfma_f32_16x16x32_bf16 v[140:143], a[0:3], v[92:95], v[132:135]\n"
"v_mfma_f32_16x16x32_bf16 v[140:143], a[4:7], v[96:99], v[140:143]\n"
"v_mfma_f32_16x16x32_bf16 v[140:143], a[8:11], v[104:107], v[140:143]\n"
"v_mfma_f32_16x16x32_bf16 v[140:143], a[12:15], v[112:115], v[140:143]\n"
"v_mfma_f32_16x16x32_bf16 v[140:143], a[16:19], v[100:103], v[140:143]\n"
"v_mfma_f32_16x16x32_bf16 v[140:143], a[20:23], v[124:127], v[140:143]\n"
"v_mfma_f32_16x16x32_bf16 v[140:143], a[24:27], v[168:171], v[140:143]\n"
"v_mfma_f32_16x16x32_bf16 v[140:143], a[28:31], v[188:191], v[140:143]\n"
"v_mfma_f32_16x16x32_bf16 v[192:195], a[32:35], v[92:95], v[144:147]\n"
"v_mfma_f32_16x16x32_bf16 v[192:195], a[36:39], v[96:99], v[192:195]\n"
"v_mfma_f32_16x16x32_bf16 v[192:195], a[40:43], v[104:107], v[192:195]\n"
"v_mfma_f32_16x16x32_bf16 v[192:195], a[44:47], v[112:115], v[192:195]\n"
"v_mfma_f32_16x16x32_bf16 v[192:195], a[48:51], v[100:103], v[192:195]\n"
"v_mfma_f32_16x16x32_bf16 v[192:195], a[52:55], v[124:127], v[192:195]\n"
"s_nop 7\n"
"s_branch .Lmk_p51r\n"
".Lmk_p53s:\n"
";\n"
"s_mov_b32 s1, 0x10000\n"
".Lmk_p53l:\n"
"s_add_u32 s95, s52, 0x448000\n"
"buffer_load_dwordx4 v[128:131], v0, s[4:7], s95 offen sc1\n"
"buffer_load_dwordx4 v[88:91], v0, s[4:7], s95 offen offset:1024 sc1\n"
"buffer_load_dwordx4 v[160:163], v0, s[4:7], s95 offen offset:2048 sc1\n"
"buffer_load_dwordx4 v[196:199], v0, s[4:7], s95 offen offset:3072 sc1\n"
"buffer_load_dwordx4 v[136:139], v1, s[4:7], s95 offen sc1\n"
"buffer_load_dwordx4 v[220:223], v1, s[4:7], s95 offen offset:1024 sc1\n"
"buffer_load_dwordx4 v[148:151], v1, s[4:7], s95 offen offset:2048 sc1\n"
"buffer_load_dwordx4 v[156:159], v1, s[4:7], s95 offen offset:3072 sc1\n"
"s_add_u32 s95, s53, 0x448000\n"
"buffer_load_dwordx4 v[184:187], v0, s[4:7], s95 offen sc1\n"
"buffer_load_dwordx4 v[116:119], v0, s[4:7], s95 offen offset:1024 sc1\n"
"buffer_load_dwordx4 v[132:135], v0, s[4:7], s95 offen offset:2048 sc1\n"
"buffer_load_dwordx4 v[144:147], v0, s[4:7], s95 offen offset:3072 sc1\n"
"buffer_load_dwordx4 v[152:155], v1, s[4:7], s95 offen sc1\n"
"buffer_load_dwordx4 v[176:179], v1, s[4:7], s95 offen offset:1024 sc1\n"
"buffer_load_dwordx4 v[180:183], v1, s[4:7], s95 offen offset:2048 sc1\n"
"buffer_load_dwordx4 v[204:207], v1, s[4:7], s95 offen offset:3072 sc1\n"
"s_add_u32 s95, s54, 0x448000\n"
"buffer_load_dwordx4 v[96:99], v0, s[4:7], s95 offen sc1\n"
"buffer_load_dwordx4 v[100:103], v0, s[4:7], s95 offen offset:1024 sc1\n"
"s_waitcnt vmcnt(0)\n"
"v_max_u32_e32 v252, v128, v129\n"
"v_max3_u32 v252, v252, v130, v131\n"
"v_max3_u32 v252, v252, v88, v89\n"
"v_max3_u32 v252, v252, v90, v91\n"
"v_max3_u32 v252, v252, v160, v161\n"
"v_max3_u32 v252, v252, v162, v163\n"
"v_max3_u32 v252, v252, v196, v197\n"
"v_max3_u32 v252, v252, v198, v199\n"
"v_max3_u32 v252, v252, v136, v137\n"
"v_max3_u32 v252, v252, v138, v139\n"
"v_max3_u32 v252, v252, v220, v221\n"
"v_max3_u32 v252, v252, v222, v223\n"
"v_max3_u32 v252, v252, v148, v149\n"
"v_max3_u32 v252, v252, v150, v151\n"
"v_max3_u32 v252, v252, v156, v157\n"
"v_max3_u32 v252, v252, v158, v159\n"
"v_cmp_eq_u32_e32 vcc, -1, v252\n"
"s_cbranch_vccz .Lmk_p53k\n"
"s_sub_u32 s1, s1, 1\n"
"s_cbranch_scc0 .Lmk_p53l\n"
".Lmk_p53k:\n"
"v_mfma_f32_16x16x32_bf16 v[92:95], a[0:3], v[128:131], 0\n"
"v_mfma_f32_16x16x32_bf16 v[92:95], a[4:7], v[88:91], v[92:95]\n"
"v_mfma_f32_16x16x32_bf16 v[92:95], a[8:11], v[160:163], v[92:95]\n"
"v_mfma_f32_16x16x32_bf16 v[92:95], a[12:15], v[196:199], v[92:95]\n"
"v_mfma_f32_16x16x32_bf16 v[92:95], a[16:19], v[136:139], v[92:95]\n"
"v_mfma_f32_16x16x32_bf16 v[92:95], a[20:23], v[220:223], v[92:95]\n"
"v_mfma_f32_16x16x32_bf16 v[92:95], a[24:27], v[148:151], v[92:95]\n"
"v_mfma_f32_16x16x32_bf16 v[92:95], a[28:31], v[156:159], v[92:95]\n"
"v_mfma_f32_16x16x32_bf16 v[104:107], a[32:35], v[128:131], 0\n"
"v_mfma_f32_16x16x32_bf16 v[104:107], a[36:39], v[88:91], v[104:107]\n"
"v_mfma_f32_16x16x32_bf16 v[104:107], a[40:43], v[160:163], v[104:107]\n"
"v_mfma_f32_16x16x32_bf16 v[104:107], a[44:47], v[196:199], v[104:107]\n"
"v_mfma_f32_16x16x32_bf16 v[104:107], a[48:51], v[136:139], v[104:107]\n"
"v_mfma_f32_16x16x32_bf16 v[104:107], a[52:55], v[220:223], v[104:107]\n"
"s_nop 7\n"
"s_branch .Lmk_p53r\n"
".Lmk_p54s:\n"
";\n"
"s_mov_b32 s1, 0x10000\n"
".Lmk_p54l:\n"
"s_add_u32 s95, s53, 0x448000\n"
"buffer_load_dwordx4 v[184:187], v0, s[4:7], s95 offen sc1\n"
"buffer_load_dwordx4 v[116:119], v0, s[4:7], s95 offen offset:1024 sc1\n"
"buffer_load_dwordx4 v[132:135], v0, s[4:7], s95 offen offset:2048 sc1\n"
"buffer_load_dwordx4 v[144:147], v0, s[4:7], s95 offen offset:3072 sc1\n"
"buffer_load_dwordx4 v[152:155], v1, s[4:7], s95 offen sc1\n"
"buffer_load_dwordx4 v[176:179], v1, s[4:7], s95 offen offset:1024 sc1\n"
"buffer_load_dwordx4 v[180:183], v1, s[4:7], s95 offen offset:2048 sc1\n"
"buffer_load_dwordx4 v[204:207], v1, s[4:7], s95 offen offset:3072 sc1\n"
"s_add_u32 s95, s54, 0x448000\n"
"buffer_load_dwordx4 v[96:99], v0, s[4:7], s95 offen sc1\n"
"buffer_load_dwordx4 v[100:103], v0, s[4:7], s95 offen offset:1024 sc1\n"
"buffer_load_dwordx4 v[112:115], v0, s[4:7], s95 offen offset:2048 sc1\n"
"buffer_load_dwordx4 v[120:123], v0, s[4:7], s95 offen offset:3072 sc1\n"
"buffer_load_dwordx4 v[104:107], v1, s[4:7], s95 offen sc1\n"
"buffer_load_dwordx4 v[108:111], v1, s[4:7], s95 offen offset:1024 sc1\n"
"buffer_load_dwordx4 v[140:143], v1, s[4:7], s95 offen offset:2048 sc1\n"
"buffer_load_dwordx4 v[164:167], v1, s[4:7], s95 offen offset:3072 sc1\n"
"s_add_u32 s95, s55, 0x448000\n"
"buffer_load_dwordx4 v[92:95], v0, s[4:7], s95 offen sc1\n"
"buffer_load_dwordx4 v[124:127], v0, s[4:7], s95 offen offset:1024 sc1\n"
"s_waitcnt vmcnt(0)\n"
"v_max_u32_e32 v247, v184, v185\n"
"v_max3_u32 v247, v247, v186, v187\n"
"v_max3_u32 v247, v247, v116, v117\n"
"v_max3_u32 v247, v247, v118, v119\n"
"v_max3_u32 v247, v247, v132, v133\n"
"v_max3_u32 v247, v247, v134, v135\n"
"v_max3_u32 v247, v247, v144, v145\n"
"v_max3_u32 v247, v247, v146, v147\n"
"v_max3_u32 v247, v247, v152, v153\n"
"v_max3_u32 v247, v247, v154, v155\n"
"v_max3_u32 v247, v247, v176, v177\n"
"v_max3_u32 v247, v247, v178, v179\n"
"v_max3_u32 v247, v247, v180, v181\n"
"v_max3_u32 v247, v247, v182, v183\n"
"v_max3_u32 v247, v247, v204, v205\n"
"v_max3_u32 v247, v247, v206, v207\n"
"v_cmp_eq_u32_e32 vcc, -1, v247\n"
"s_cbranch_vccz .Lmk_p54k\n"
"s_sub_u32 s1, s1, 1\n"
"s_cbranch_scc0 .Lmk_p54l\n"
".Lmk_p54k:\n"
"v_mfma_f32_16x16x32_bf16 v[88:91], a[0:3], v[184:187], 0\n"
"v_mfma_f32_16x16x32_bf16 v[88:91], a[4:7], v[116:119], v[88:91]\n"
"v_mfma_f32_16x16x32_bf16 v[88:91], a[8:11], v[132:135], v[88:91]\n"
"v_mfma_f32_16x16x32_bf16 v[88:91], a[12:15], v[144:147], v[88:91]\n"
"v_mfma_f32_16x16x32_bf16 v[88:91], a[16:19], v[152:155], v[88:91]\n"
"v_mfma_f32_16x16x32_bf16 v[88:91], a[20:23], v[176:179], v[88:91]\n"
"v_mfma_f32_16x16x32_bf16 v[88:91], a[24:27], v[180:183], v[88:91]\n"
"v_mfma_f32_16x16x32_bf16 v[88:91], a[28:31], v[204:207], v[88:91]\n"
"v_mfma_f32_16x16x32_bf16 v[128:131], a[32:35], v[184:187], 0\n"
"v_mfma_f32_16x16x32_bf16 v[128:131], a[36:39], v[116:119], v[128:131]\n"
"v_mfma_f32_16x16x32_bf16 v[128:131], a[40:43], v[132:135], v[128:131]\n"
"v_mfma_f32_16x16x32_bf16 v[128:131], a[44:47], v[144:147], v[128:131]\n"
"v_mfma_f32_16x16x32_bf16 v[128:131], a[48:51], v[152:155], v[128:131]\n"
"v_mfma_f32_16x16x32_bf16 v[128:131], a[52:55], v[176:179], v[128:131]\n"
"s_nop 7\n"
"s_branch .Lmk_p54r\n"
".Lmk_m57s:\n"
";\n"
"s_mov_b32 s1, 0x10000\n"
".Lmk_m57l:\n"
"s_add_u32 s98, s62, 0x44800\n"
"buffer_load_dword v255, v10, s[4:7], s98 offen sc1\n"
"buffer_load_dword v253, v10, s[4:7], s98 offen offset:256 sc1\n"
"buffer_load_dword v252, v10, s[4:7], s98 offen offset:512 sc1\n"
"buffer_load_dword v251, v10, s[4:7], s98 offen offset:768 sc1\n"
"buffer_load_dword v250, v10, s[4:7], s98 offen offset:1024 sc1\n"
"buffer_load_dword v254, v10, s[4:7], s98 offen offset:1280 sc1\n"
"buffer_load_dword v249, v10, s[4:7], s98 offen offset:1536 sc1\n"
"buffer_load_dword v248, v10, s[4:7], s98 offen offset:1792 sc1\n"
"s_waitcnt vmcnt(0)\n"
"v_max_u32_e32 v245, v255, v253\n"
"v_max3_u32 v245, v245, v252, v251\n"
"v_max3_u32 v245, v245, v250, v254\n"
"v_max3_u32 v245, v245, v249, v248\n"
"v_cmp_eq_u32_e32 vcc, -1, v245\n"
"s_cbranch_vccz .Lmk_m57k\n"
"s_sub_u32 s1, s1, 1\n"
"s_cbranch_scc0 .Lmk_m57l\n"
".Lmk_m57k:\n"
"v_add_f32_e32 v255, v255, v250\n"
"v_add_f32_e32 v253, v253, v254\n"
"v_add_f32_e32 v252, v252, v249\n"
"v_add_f32_e32 v251, v251, v248\n"
"v_add_f32_e32 v255, v255, v252\n"
"v_add_f32_e32 v253, v253, v251\n"
"v_add_f32_e32 v255, v255, v253\n"
"s_nop 1\n"
"s_branch .Lmk_m57r\n"
".Lmk_f44s:\n"
";\n"
"s_mov_b32 s1, 0x10000\n"
".Lmk_f44l:\n"
"buffer_load_dwordx2 v[246:247], v12, s[4:7], s96 offen sc1\n"
"s_waitcnt vmcnt(0)\n"
"v_cmp_ne_u64_e32 vcc, v[246:247], v[18:19]\n"
"s_cbranch_vccz .Lmk_f44r\n"
"s_sub_u32 s1, s1, 1\n"
"s_cbranch_scc0 .Lmk_f44l\n"
"s_branch .Lmk_f44r\n"
".Lmk_p55s:\n"
";\n"
"s_mov_b32 s1, 0x10000\n"
".Lmk_p55l:\n"
"s_add_u32 s95, s54, 0x448000\n"
"buffer_load_dwordx4 v[96:99], v0, s[4:7], s95 offen sc1\n"
"buffer_load_dwordx4 v[100:103], v0, s[4:7], s95 offen offset:1024 sc1\n"
"buffer_load_dwordx4 v[112:115], v0, s[4:7], s95 offen offset:2048 sc1\n"
"buffer_load_dwordx4 v[120:123], v0, s[4:7], s95 offen offset:3072 sc1\n"
"buffer_load_dwordx4 v[104:107], v1, s[4:7], s95 offen sc1\n"
"buffer_load_dwordx4 v[108:111], v1, s[4:7], s95 offen offset:1024 sc1\n"
"buffer_load_dwordx4 v[140:143], v1, s[4:7], s95 offen offset:2048 sc1\n"
"buffer_load_dwordx4 v[164:167], v1, s[4:7], s95 offen offset:3072 sc1\n"
"s_add_u32 s95, s55, 0x448000\n"
"buffer_load_dwordx4 v[92:95], v0, s[4:7], s95 offen sc1\n"
"buffer_load_dwordx4 v[124:127], v0, s[4:7], s95 offen offset:1024 sc1\n"
"buffer_load_dwordx4 v[136:139], v0, s[4:7], s95 offen offset:2048 sc1\n"
"buffer_load_dwordx4 v[148:151], v0, s[4:7], s95 offen offset:3072 sc1\n"
"buffer_load_dwordx4 v[128:131], v1, s[4:7], s95 offen sc1\n"
"buffer_load_dwordx4 v[168:171], v1, s[4:7], s95 offen offset:1024 sc1\n"
"buffer_load_dwordx4 v[172:175], v1, s[4:7], s95 offen offset:2048 sc1\n"
"buffer_load_dwordx4 v[188:191], v1, s[4:7], s95 offen offset:3072 sc1\n"
"s_waitcnt vmcnt(0)\n"
"v_max_u32_e32 v252, v96, v97\n"
"v_max3_u32 v252, v252, v98, v99\n"
"v_max3_u32 v252, v252, v100, v101\n"
"v_max3_u32 v252, v252, v102, v103\n"
"v_max3_u32 v252, v252, v112, v113\n"
"v_max3_u32 v252, v252, v114, v115\n"
"v_max3_u32 v252, v252, v120, v121\n"
"v_max3_u32 v252, v252, v122, v123\n"
"v_max3_u32 v252, v252, v104, v105\n"
"v_max3_u32 v252, v252, v106, v107\n"
"v_max3_u32 v252, v252, v108, v109\n"
"v_max3_u32 v252, v252, v110, v111\n"
"v_max3_u32 v252, v252, v140, v141\n"
"v_max3_u32 v252, v252, v142, v143\n"
"v_max3_u32 v252, v252, v164, v165\n"
"v_max3_u32 v252, v252, v166, v167\n"
"v_cmp_eq_u32_e32 vcc, -1, v252\n"
"s_cbranch_vccz .Lmk_p55k\n"
"s_sub_u32 s1, s1, 1\n"
"s_cbranch_scc0 .Lmk_p55l\n"
".Lmk_p55k:\n"
"v_mfma_f32_16x16x32_bf16 v[88:91], a[0:3], v[96:99], 0\n"
"v_mfma_f32_16x16x32_bf16 v[88:91], a[4:7], v[100:103], v[88:91]\n"
"v_mfma_f32_16x16x32_bf16 v[88:91], a[8:11], v[112:115], v[88:91]\n"
"v_mfma_f32_16x16x32_bf16 v[88:91], a[12:15], v[120:123], v[88:91]\n"
"v_mfma_f32_16x16x32_bf16 v[88:91], a[16:19], v[104:107], v[88:91]\n"
"v_mfma_f32_16x16x32_bf16 v[88:91], a[20:23], v[108:111], v[88:91]\n"
"v_mfma_f32_16x16x32_bf16 v[88:91], a[24:27], v[140:143], v[88:91]\n"
"v_mfma_f32_16x16x32_bf16 v[88:91], a[28:31], v[164:167], v[88:91]\n"
"v_mfma_f32_16x16x32_bf16 v[116:119], a[32:35], v[96:99], 0\n"
"v_mfma_f32_16x16x32_bf16 v[116:119], a[36:39], v[100:103], v[116:119]\n"
"v_mfma_f32_16x16x32_bf16 v[116:119], a[40:43], v[112:115], v[116:119]\n"
"v_mfma_f32_16x16x32_bf16 v[116:119], a[44:47], v[120:123], v[116:119]\n"
"v_mfma_f32_16x16x32_bf16 v[116:119], a[48:51], v[104:107], v[116:119]\n"
"v_mfma_f32_16x16x32_bf16 v[116:119], a[52:55], v[108:111], v[116:119]\n"
"s_nop 7\n"
"s_branch .Lmk_p55r\n"
".Lmk_p56s:\n"
";\n"
"s_mov_b32 s1, 0x10000\n"
".Lmk_p56l:\n"
"s_add_u32 s96, s55, 0x448000\n"
"buffer_load_dwordx4 v[92:95], v0, s[4:7], s96 offen sc1\n"
"buffer_load_dwordx4 v[124:127], v0, s[4:7], s96 offen offset:1024 sc1\n"
"buffer_load_dwordx4 v[136:139], v0, s[4:7], s96 offen offset:2048 sc1\n"
"buffer_load_dwordx4 v[148:151], v0, s[4:7], s96 offen offset:3072 sc1\n"
"buffer_load_dwordx4 v[128:131], v1, s[4:7], s96 offen sc1\n"
"buffer_load_dwordx4 v[168:171], v1, s[4:7], s96 offen offset:1024 sc1\n"
"buffer_load_dwordx4 v[172:175], v1, s[4:7], s96 offen offset:2048 sc1\n"
"buffer_load_dwordx4 v[188:191], v1, s[4:7], s96 offen offset:3072 sc1\n"
"s_waitcnt vmcnt(0)\n"
"v_max_u32_e32 v252, v92, v93\n"
"v_max3_u32 v252, v252, v94, v95\n"
"v_max3_u32 v252, v252, v124, v125\n"
"v_max3_u32 v252, v252, v126, v127\n"
"v_max3_u32 v252, v252, v136, v137\n"
"v_max3_u32 v252, v252, v138, v139\n"
"v_max3_u32 v252, v252, v148, v149\n"
"v_max3_u32 v252, v252, v150, v151\n"
"v_max3_u32 v252, v252, v128, v129\n"
"v_max3_u32 v252, v252, v130, v131\n"
"v_max3_u32 v252, v252, v168, v169\n"
"v_max3_u32 v252, v252, v170, v171\n"
"v_max3_u32 v252, v252, v172, v173\n"
"v_max3_u32 v252, v252, v174, v175\n"
"v_max3_u32 v252, v252, v188, v189\n"
"v_max3_u32 v252, v252, v190, v191\n"
"v_cmp_eq_u32_e32 vcc, -1, v252\n"
"s_cbranch_vccz .Lmk_p56k\n"
"s_sub_u32 s1, s1, 1\n"
"s_cbranch_scc0 .Lmk_p56l\n"
".Lmk_p56k:\n"
"v_mfma_f32_16x16x32_bf16 v[140:143], a[0:3], v[92:95], v[144:147]\n"
"v_mfma_f32_16x16x32_bf16 v[140:143], a[4:7], v[124:127], v[140:143]\n"
"v_mfma_f32_16x16x32_bf16 v[140:143], a[8:11], v[136:139], v[140:143]\n"
"v_mfma_f32_16x16x32_bf16 v[140:143], a[12:15], v[148:151], v[140:143]\n"
"v_mfma_f32_16x16x32_bf16 v[140:143], a[16:19], v[128:131], v[140:143]\n"
"v_mfma_f32_16x16x32_bf16 v[140:143], a[20:23], v[168:171], v[140:143]\n"
"v_mfma_f32_16x16x32_bf16 v[140:143], a[24:27], v[172:175], v[140:143]\n"
"v_mfma_f32_16x16x32_bf16 v[140:143], a[28:31], v[188:191], v[140:143]\n"
"v_mfma_f32_16x16x32_bf16 v[196:199], a[32:35], v[92:95], v[180:183]\n"
"v_mfma_f32_16x16x32_bf16 v[196:199], a[36:39], v[124:127], v[196:199]\n"
"v_mfma_f32_16x16x32_bf16 v[196:199], a[40:43], v[136:139], v[196:199]\n"
"v_mfma_f32_16x16x32_bf16 v[196:199], a[44:47], v[148:151], v[196:199]\n"
"v_mfma_f32_16x16x32_bf16 v[196:199], a[48:51], v[128:131], v[196:199]\n"
"v_mfma_f32_16x16x32_bf16 v[196:199], a[52:55], v[168:171], v[196:199]\n"
"s_nop 7\n"
"s_branch .Lmk_p56r\n"
".Lmk_f45s:\n"
";\n"
"s_mov_b32 s1, 0x10000\n"
".Lmk_f45l:\n"
"buffer_load_dwordx2 v[244:245], off, s[4:7], s96 sc1\n"
"s_waitcnt vmcnt(0)\n"
"v_cmp_ne_u64_e32 vcc, v[244:245], v[18:19]\n"
"s_cbranch_vccz .Lmk_f45r\n"
"s_sub_u32 s1, s1, 1\n"
"s_cbranch_scc0 .Lmk_f45l\n"
"s_branch .Lmk_f45r\n"
".Lmk_f46s:\n"
";\n"
"s_mov_b32 s1, 0x10000\n"
".Lmk_f46l:\n"
"buffer_load_dwordx2 v[250:251], v12, s[4:7], s98 offen sc1\n"
"s_waitcnt vmcnt(0)\n"
"v_cmp_ne_u64_e32 vcc, v[250:251], v[18:19]\n"
"s_cbranch_vccz .Lmk_f46r\n"
"s_sub_u32 s1, s1, 1\n"
"s_cbranch_scc0 .Lmk_f46l\n"
"s_branch .Lmk_f46r\n"
".Lmk_p60s:\n"
";\n"
"s_mov_b32 s1, 0x10000\n"
".Lmk_p60l:\n"
"s_add_u32 s96, s56, 0x440000\n"
"buffer_load_dwordx4 v[24:27], v0, s[4:7], s96 offen sc1\n"
"buffer_load_dwordx4 v[28:31], v0, s[4:7], s96 offen offset:1024 sc1\n"
"buffer_load_dwordx4 v[48:51], v0, s[4:7], s96 offen offset:2048 sc1\n"
"buffer_load_dwordx4 v[52:55], v0, s[4:7], s96 offen offset:3072 sc1\n"
"buffer_load_dwordx4 v[56:59], v1, s[4:7], s96 offen sc1\n"
"buffer_load_dwordx4 v[60:63], v1, s[4:7], s96 offen offset:1024 sc1\n"
"buffer_load_dwordx4 v[88:91], v1, s[4:7], s96 offen offset:2048 sc1\n"
"buffer_load_dwordx4 v[92:95], v1, s[4:7], s96 offen offset:3072 sc1\n"
"s_add_u32 s96, s57, 0x440000\n"
"buffer_load_dwordx4 v[100:103], v0, s[4:7], s96 offen sc1\n"
"buffer_load_dwordx4 v[72:75], v0, s[4:7], s96 offen offset:1024 sc1\n"
"buffer_load_dwordx4 v[64:67], v0, s[4:7], s96 offen offset:2048 sc1\n"
"buffer_load_dwordx4 v[112:115], v0, s[4:7], s96 offen offset:3072 sc1\n"
"buffer_load_dwordx4 v[96:99], v1, s[4:7], s96 offen sc1\n"
"buffer_load_dwordx4 v[84:87], v1, s[4:7], s96 offen offset:1024 sc1\n"
"buffer_load_dwordx4 v[76:79], v1, s[4:7], s96 offen offset:2048 sc1\n"
"buffer_load_dwordx4 v[128:131], v1, s[4:7], s96 offen offset:3072 sc1\n"
"s_add_u32 s96, s58, 0x440000\n"
"buffer_load_dwordx4 v[80:83], v0, s[4:7], s96 offen sc1\n"
"buffer_load_dwordx4 v[104:107], v0, s[4:7], s96 offen offset:1024 sc1\n"
"buffer_load_dwordx4 v[116:119], v0, s[4:7], s96 offen offset:2048 sc1\n"
"s_waitcnt vmcnt(0)\n"
"v_max_u32_e32 v255, v24, v25\n"
"v_max3_u32 v255, v255, v26, v27\n"
"v_max3_u32 v255, v255, v28, v29\n"
"v_max3_u32 v255, v255, v30, v31\n"
"v_max3_u32 v255, v255, v48, v49\n"
"v_max3_u32 v255, v255, v50, v51\n"
"v_max3_u32 v255, v255, v52, v53\n"
"v_max3_u32 v255, v255, v54, v55\n"
"v_max3_u32 v255, v255, v56, v57\n"
"v_max3_u32 v255, v255, v58, v59\n"
"v_max3_u32 v255, v255, v60, v61\n"
"v_max3_u32 v255, v255, v62, v63\n"
"v_max3_u32 v255, v255, v88, v89\n"
"v_max3_u32 v255, v255, v90, v91\n"
"v_max3_u32 v255, v255, v92, v93\n"
"v_max3_u32 v255, v255, v94, v95\n"
"v_cmp_eq_u32_e32 vcc, -1, v255\n"
"s_cbranch_vccz .Lmk_p60k\n"
"s_sub_u32 s1, s1, 1\n"
"s_cbranch_scc0 .Lmk_p60l\n"
".Lmk_p60k:\n"
"v_mfma_f32_16x16x32_bf16 v[68:71], a[128:131], v[24:27], 0\n"
"v_mfma_f32_16x16x32_bf16 v[68:71], a[132:135], v[28:31], v[68:71]\n"
"v_mfma_f32_16x16x32_bf16 v[68:71], a[136:139], v[48:51], v[68:71]\n"
"v_mfma_f32_16x16x32_bf16 v[68:71], a[140:143], v[52:55], v[68:71]\n"
"v_mfma_f32_16x16x32_bf16 v[68:71], a[144:147], v[56:59], v[68:71]\n"
"v_mfma_f32_16x16x32_bf16 v[68:71], a[148:151], v[60:63], v[68:71]\n"
"v_mfma_f32_16x16x32_bf16 v[68:71], a[152:155], v[88:91], v[68:71]\n"
"v_mfma_f32_16x16x32_bf16 v[68:71], a[156:159], v[92:95], v[68:71]\n"
"v_mfma_f32_16x16x32_bf16 v[108:111], a[160:163], v[24:27], 0\n"
"v_mfma_f32_16x16x32_bf16 v[108:111], a[164:167], v[28:31], v[108:111]\n"
"v_mfma_f32_16x16x32_bf16 v[108:111], a[168:171], v[48:51], v[108:111]\n"
"v_mfma_f32_16x16x32_bf16 v[108:111], a[172:175], v[52:55], v[108:111]\n"
"v_mfma_f32_16x16x32_bf16 v[108:111], a[176:179], v[56:59], v[108:111]\n"
"v_mfma_f32_16x16x32_bf16 v[108:111], a[180:183], v[60:63], v[108:111]\n"
"s_nop 7\n"
"s_branch .Lmk_p60r\n"
".Lmk_p61s:\n"
";\n"
"s_mov_b32 s1, 0x10000\n"
".Lmk_p61l:\n"
"s_add_u32 s96, s57, 0x440000\n"
"buffer_load_dwordx4 v[100:103], v0, s[4:7], s96 offen sc1\n"
"buffer_load_dwordx4 v[72:75], v0, s[4:7], s96 offen offset:1024 sc1\n"
"buffer_load_dwordx4 v[64:67], v0, s[4:7], s96 offen offset:2048 sc1\n"
"buffer_load_dwordx4 v[112:115], v0, s[4:7], s96 offen offset:3072 sc1\n"
"buffer_load_dwordx4 v[96:99], v1, s[4:7], s96 offen sc1\n"
"buffer_load_dwordx4 v[84:87], v1, s[4:7], s96 offen offset:1024 sc1\n"
"buffer_load_dwordx4 v[76:79], v1, s[4:7], s96 offen offset:2048 sc1\n"
"buffer_load_dwordx4 v[128:131], v1, s[4:7], s96 offen offset:3072 sc1\n"
"s_add_u32 s96, s58, 0x440000\n"
"buffer_load_dwordx4 v[80:83], v0, s[4:7], s96 offen sc1\n"
"buffer_load_dwordx4 v[104:107], v0, s[4:7], s96 offen offset:1024 sc1\n"
"buffer_load_dwordx4 v[116:119], v0, s[4:7], s96 offen offset:2048 sc1\n"
"buffer_load_dwordx4 v[68:71], v0, s[4:7], s96 offen offset:3072 sc1\n"
"buffer_load_dwordx4 v[124:127], v1, s[4:7], s96 offen sc1\n"
"buffer_load_dwordx4 v[108:111], v1, s[4:7], s96 offen offset:1024 sc1\n"
"buffer_load_dwordx4 v[24:27], v1, s[4:7], s96 offen offset:2048 sc1\n"
"buffer_load_dwordx4 v[28:31], v1, s[4:7], s96 offen offset:3072 sc1\n"
"s_add_u32 s96, s59, 0x440000\n"
"buffer_load_dwordx4 v[52:55], v0, s[4:7], s96 offen sc1\n"
"buffer_load_dwordx4 v[56:59], v0, s[4:7], s96 offen offset:1024 sc1\n"
"buffer_load_dwordx4 v[88:91], v0, s[4:7], s96 offen offset:2048 sc1\n"
"s_waitcnt vmcnt(0)\n"
"v_max_u32_e32 v255, v100, v101\n"
"v_max3_u32 v255, v255, v102, v103\n"
"v_max3_u32 v255, v255, v72, v73\n"
"v_max3_u32 v255, v255, v74, v75\n"
"v_max3_u32 v255, v255, v64, v65\n"
"v_max3_u32 v255, v255, v66, v67\n"
"v_max3_u32 v255, v255, v112, v113\n"
"v_max3_u32 v255, v255, v114, v115\n"
"v_max3_u32 v255, v255, v96, v97\n"
"v_max3_u32 v255, v255, v98, v99\n"
"v_max3_u32 v255, v255, v84, v85\n"
"v_max3_u32 v255, v255, v86, v87\n"
"v_max3_u32 v255, v255, v76, v77\n"
"v_max3_u32 v255, v255, v78, v79\n"
"v_max3_u32 v255, v255, v128, v129\n"
"v_max3_u32 v255, v255, v130, v131\n"
"v_cmp_eq_u32_e32 vcc, -1, v255\n"
"s_cbranch_vccz .Lmk_p61k\n"
"s_sub_u32 s1, s1, 1\n"
"s_cbranch_scc0 .Lmk_p61l\n"
".Lmk_p61k:\n"
"v_mfma_f32_16x16x32_bf16 v[48:51], a[128:131], v[100:103], 0\n"
"v_mfma_f32_16x16x32_bf16 v[48:51], a[132:135], v[72:75], v[48:51]\n"
"v_mfma_f32_16x16x32_bf16 v[48:51], a[136:139], v[64:67], v[48:51]\n"
"v_mfma_f32_16x16x32_bf16 v[48:51], a[140:143], v[112:115], v[48:51]\n"
"v_mfma_f32_16x16x32_bf16 v[48:51], a[144:147], v[96:99], v[48:51]\n"
"v_mfma_f32_16x16x32_bf16 v[48:51], a[148:151], v[84:87], v[48:51]\n"
"v_mfma_f32_16x16x32_bf16 v[48:51], a[152:155], v[76:79], v[48:51]\n"
"v_mfma_f32_16x16x32_bf16 v[48:51], a[156:159], v[128:131], v[48:51]\n"
"v_mfma_f32_16x16x32_bf16 v[60:63], a[160:163], v[100:103], 0\n"
"v_mfma_f32_16x16x32_bf16 v[60:63], a[164:167], v[72:75], v[60:63]\n"
"v_mfma_f32_16x16x32_bf16 v[60:63], a[168:171], v[64:67], v[60:63]\n"
"v_mfma_f32_16x16x32_bf16 v[60:63], a[172:175], v[112:115], v[60:63]\n"
"v_mfma_f32_16x16x32_bf16 v[60:63], a[176:179], v[96:99], v[60:63]\n"
"v_mfma_f32_16x16x32_bf16 v[60:63], a[180:183], v[84:87], v[60:63]\n"
"s_nop 7\n"
"s_branch .Lmk_p61r\n"
".Lmk_p62s:\n"
";\n"
"s_mov_b32 s1, 0x10000\n"
".Lmk_p62l:\n"
"s_add_u32 s96, s58, 0x440000\n"
"buffer_load_dwordx4 v[80:83], v0, s[4:7], s96 offen sc1\n"
"buffer_load_dwordx4 v[104:107], v0, s[4:7], s96 offen offset:1024 sc1\n"
"buffer_load_dwordx4 v[116:119], v0, s[4:7], s96 offen offset:2048 sc1\n"
"buffer_load_dwordx4 v[68:71], v0, s[4:7], s96 offen offset:3072 sc1\n"
"buffer_load_dwordx4 v[124:127], v1, s[4:7], s96 offen sc1\n"
"buffer_load_dwordx4 v[108:111], v1, s[4:7], s96 offen offset:1024 sc1\n"
"buffer_load_dwordx4 v[24:27], v1, s[4:7], s96 offen offset:2048 sc1\n"
"buffer_load_dwordx4 v[28:31], v1, s[4:7], s96 offen offset:3072 sc1\n"
"s_add_u32 s96, s59, 0x440000\n"
"buffer_load_dwordx4 v[52:55], v0, s[4:7], s96 offen sc1\n"
"buffer_load_dwordx4 v[56:59], v0, s[4:7], s96 offen offset:1024 sc1\n"
"buffer_load_dwordx4 v[88:91], v0, s[4:7], s96 offen offset:2048 sc1\n"
"buffer_load_dwordx4 v[48:51], v0, s[4:7], s96 offen offset:3072 sc1\n"
"buffer_load_dwordx4 v[120:123], v1, s[4:7], s96 offen sc1\n"
"buffer_load_dwordx4 v[60:63], v1, s[4:7], s96 offen offset:1024 sc1\n"
"buffer_load_dwordx4 v[64:67], v1, s[4:7], s96 offen offset:2048 sc1\n"
"buffer_load_dwordx4 v[72:75], v1, s[4:7], s96 offen offset:3072 sc1\n"
"s_waitcnt vmcnt(0)\n"
"v_max_u32_e32 v239, v80, v81\n"
"v_max3_u32 v239, v239, v82, v83\n"
"v_max3_u32 v239, v239, v104, v105\n"
"v_max3_u32 v239, v239, v106, v107\n"
"v_max3_u32 v239, v239, v116, v117\n"
"v_max3_u32 v239, v239, v118, v119\n"
"v_max3_u32 v239, v239, v68, v69\n"
"v_max3_u32 v239, v239, v70, v71\n"
"v_max3_u32 v239, v239, v124, v125\n"
"v_max3_u32 v239, v239, v126, v127\n"
"v_max3_u32 v239, v239, v108, v109\n"
"v_max3_u32 v239, v239, v110, v111\n"
"v_max3_u32 v239, v239, v24, v25\n"
"v_max3_u32 v239, v239, v26, v27\n"
"v_max3_u32 v239, v239, v28, v29\n"
"v_max3_u32 v239, v239, v30, v31\n"
"v_cmp_eq_u32_e32 vcc, -1, v239\n"
"s_cbranch_vccz .Lmk_p62k\n"
"s_sub_u32 s1, s1, 1\n"
"s_cbranch_scc0 .Lmk_p62l\n"
".Lmk_p62k:\n"
"v_mfma_f32_16x16x32_bf16 v[76:79], a[128:131], v[80:83], 0\n"
"v_mfma_f32_16x16x32_bf16 v[76:79], a[132:135], v[104:107], v[76:79]\n"
"v_mfma_f32_16x16x32_bf16 v[76:79], a[136:139], v[116:119], v[76:79]\n"
"v_mfma_f32_16x16x32_bf16 v[76:79], a[140:143], v[68:71], v[76:79]\n"
"v_mfma_f32_16x16x32_bf16 v[76:79], a[144:147], v[124:127], v[76:79]\n"
"v_mfma_f32_16x16x32_bf16 v[76:79], a[148:151], v[108:111], v[76:79]\n"
"v_mfma_f32_16x16x32_bf16 v[76:79], a[152:155], v[24:27], v[76:79]\n"
"v_mfma_f32_16x16x32_bf16 v[76:79], a[156:159], v[28:31], v[76:79]\n"
"v_mfma_f32_16x16x32_bf16 v[84:87], a[160:163], v[80:83], 0\n"
"v_mfma_f32_16x16x32_bf16 v[84:87], a[164:167], v[104:107], v[84:87]\n"
"v_mfma_f32_16x16x32_bf16 v[84:87], a[168:171], v[116:119], v[84:87]\n"
"v_mfma_f32_16x16x32_bf16 v[84:87], a[172:175], v[68:71], v[84:87]\n"
"v_mfma_f32_16x16x32_bf16 v[84:87], a[176:179], v[124:127], v[84:87]\n"
"v_mfma_f32_16x16x32_bf16 v[84:87], a[180:183], v[108:111], v[84:87]\n"
"s_nop 7\n"
"s_branch .Lmk_p62r\n"
".Lmk_p63s:\n"
";\n"
"s_mov_b32 s1, 0x10000\n"
".Lmk_p63l:\n"
"s_add_u32 s96, s59, 0x440000\n"
"buffer_load_dwordx4 v[52:55], v0, s[4:7], s96 offen sc1\n"
"buffer_load_dwordx4 v[56:59], v0, s[4:7], s96 offen offset:1024 sc1\n"
"buffer_load_dwordx4 v[88:91], v0, s[4:7], s96 offen offset:2048 sc1\n"
"buffer_load_dwordx4 v[48:51], v0, s[4:7], s96 offen offset:3072 sc1\n"
"buffer_load_dwordx4 v[120:123], v1, s[4:7], s96 offen sc1\n"
"buffer_load_dwordx4 v[60:63], v1, s[4:7], s96 offen offset:1024 sc1\n"
"buffer_load_dwordx4 v[64:67], v1, s[4:7], s96 offen offset:2048 sc1\n"
"buffer_load_dwordx4 v[72:75], v1, s[4:7], s96 offen offset:3072 sc1\n"
"s_add_u32 s96, s56, 0x400000\n"
"buffer_load_dwordx4 v[76:79], v0, s[4:7], s96 offen sc1\n"
"buffer_load_dwordx4 v[132:135], v0, s[4:7], s96 offen offset:1024 sc1\n"
"buffer_load_dwordx4 v[84:87], v0, s[4:7], s96 offen offset:2048 sc1\n"
"buffer_load_dwordx4 v[80:83], v0, s[4:7], s96 offen offset:3072 sc1\n"
"buffer_load_dwordx4 v[68:71], v1, s[4:7], s96 offen sc1\n"
"buffer_load_dwordx4 v[28:31], v1, s[4:7], s96 offen offset:1024 sc1\n"
"buffer_load_dwordx4 v[108:111], v1, s[4:7], s96 offen offset:2048 sc1\n"
"buffer_load_dwordx4 v[116:119], v1, s[4:7], s96 offen offset:3072 sc1\n"
"s_add_u32 s96, s57, 0x400000\n"
"buffer_load_dwordx4 v[124:127], v0, s[4:7], s96 offen sc1\n"
"s_waitcnt vmcnt(0)\n"
"v_max_u32_e32 v255, v52, v53\n"
"v_max3_u32 v255, v255, v54, v55\n"
"v_max3_u32 v255, v255, v56, v57\n"
"v_max3_u32 v255, v255, v58, v59\n"
"v_max3_u32 v255, v255, v88, v89\n"
"v_max3_u32 v255, v255, v90, v91\n"
"v_max3_u32 v255, v255, v48, v49\n"
"v_max3_u32 v255, v255, v50, v51\n"
"v_max3_u32 v255, v255, v120, v121\n"
"v_max3_u32 v255, v255, v122, v123\n"
"v_max3_u32 v255, v255, v60, v61\n"
"v_max3_u32 v255, v255, v62, v63\n"
"v_max3_u32 v255, v255, v64, v65\n"
"v_max3_u32 v255, v255, v66, v67\n"
"v_max3_u32 v255, v255, v72, v73\n"
"v_max3_u32 v255, v255, v74, v75\n"
"v_cmp_eq_u32_e32 vcc, -1, v255\n"
"s_cbranch_vccz .Lmk_p63k\n"
"s_sub_u32 s1, s1, 1\n"
"s_cbranch_scc0 .Lmk_p63l\n"
".Lmk_p63k:\n"
"v_mfma_f32_16x16x32_bf16 v[104:107], a[128:131], v[52:55], v[92:95]\n"
"v_mfma_f32_16x16x32_bf16 v[104:107], a[132:135], v[56:59], v[104:107]\n"
"v_mfma_f32_16x16x32_bf16 v[104:107], a[136:139], v[88:91], v[104:107]\n"
"v_mfma_f32_16x16x32_bf16 v[104:107], a[140:143], v[48:51], v[104:107]\n"
"v_mfma_f32_16x16x32_bf16 v[104:107], a[144:147], v[120:123], v[104:107]\n"
"v_mfma_f32_16x16x32_bf16 v[104:107], a[148:151], v[60:63], v[104:107]\n"
"v_mfma_f32_16x16x32_bf16 v[104:107], a[152:155], v[64:67], v[104:107]\n"
"v_mfma_f32_16x16x32_bf16 v[104:107], a[156:159], v[72:75], v[104:107]\n"
"v_mfma_f32_16x16x32_bf16 v[100:103], a[160:163], v[52:55], v[112:115]\n"
"v_mfma_f32_16x16x32_bf16 v[100:103], a[164:167], v[56:59], v[100:103]\n"
"v_mfma_f32_16x16x32_bf16 v[100:103], a[168:171], v[88:91], v[100:103]\n"
"v_mfma_f32_16x16x32_bf16 v[100:103], a[172:175], v[48:51], v[100:103]\n"
"v_mfma_f32_16x16x32_bf16 v[100:103], a[176:179], v[120:123], v[100:103]\n"
"v_mfma_f32_16x16x32_bf16 v[100:103], a[180:183], v[60:63], v[100:103]\n"
"s_nop 7\n"
"s_branch .Lmk_p63r\n"
".Lmk_p64s:\n"
";\n"
"s_mov_b32 s1, 0x10000\n"
".Lmk_p64l:\n"
"s_add_u32 s96, s56, 0x400000\n"
"buffer_load_dwordx4 v[76:79], v0, s[4:7], s96 offen sc1\n"
"buffer_load_dwordx4 v[132:135], v0, s[4:7], s96 offen offset:1024 sc1\n"
"buffer_load_dwordx4 v[84:87], v0, s[4:7], s96 offen offset:2048 sc1\n"
"buffer_load_dwordx4 v[80:83], v0, s[4:7], s96 offen offset:3072 sc1\n"
"buffer_load_dwordx4 v[68:71], v1, s[4:7], s96 offen sc1\n"
"buffer_load_dwordx4 v[28:31], v1, s[4:7], s96 offen offset:1024 sc1\n"
"buffer_load_dwordx4 v[108:111], v1, s[4:7], s96 offen offset:2048 sc1\n"
"buffer_load_dwordx4 v[116:119], v1, s[4:7], s96 offen offset:3072 sc1\n"
"s_add_u32 s96, s57, 0x400000\n"
"buffer_load_dwordx4 v[124:127], v0, s[4:7], s96 offen sc1\n"
"buffer_load_dwordx4 v[112:115], v0, s[4:7], s96 offen offset:1024 sc1\n"
"buffer_load_dwordx4 v[144:147], v0, s[4:7], s96 offen offset:2048 sc1\n"
"buffer_load_dwordx4 v[148:151], v0, s[4:7], s96 offen offset:3072 sc1\n"
"buffer_load_dwordx4 v[104:107], v1, s[4:7], s96 offen sc1\n"
"buffer_load_dwordx4 v[56:59], v1, s[4:7], s96 offen offset:1024 sc1\n"
"buffer_load_dwordx4 v[48:51], v1, s[4:7], s96 offen offset:2048 sc1\n"
"buffer_load_dwordx4 v[60:63], v1, s[4:7], s96 offen offset:3072 sc1\n"
"s_add_u32 s96, s58, 0x400000\n"
"buffer_load_dwordx4 v[72:75], v0, s[4:7], s96 offen sc1\n"
"buffer_load_dwordx4 v[88:91], v0, s[4:7], s96 offen offset:1024 sc1\n"
"buffer_load_dwordx4 v[100:103], v0, s[4:7], s96 offen offset:2048 sc1\n"
"buffer_load_dwordx4 v[128:131], v0, s[4:7], s96 offen offset:3072 sc1\n"
"s_waitcnt vmcnt(0)\n"
"v_max_u32_e32 v254, v76, v77\n"
"v_max3_u32 v254, v254, v78, v79\n"
"v_max3_u32 v254, v254, v132, v133\n"
"v_max3_u32 v254, v254, v134, v135\n"
"v_max3_u32 v254, v254, v84, v85\n"
"v_max3_u32 v254, v254, v86, v87\n"
"v_max3_u32 v254, v254, v80, v81\n"
"v_max3_u32 v254, v254, v82, v83\n"
"v_max3_u32 v254, v254, v68, v69\n"
"v_max3_u32 v254, v254, v70, v71\n"
"v_max3_u32 v254, v254, v28, v29\n"
"v_max3_u32 v254, v254, v30, v31\n"
"v_max3_u32 v254, v254, v108, v109\n"
"v_max3_u32 v254, v254, v110, v111\n"
"v_max3_u32 v254, v254, v116, v117\n"
"v_max3_u32 v254, v254, v118, v119\n"
"v_cmp_eq_u32_e32 vcc, -1, v254\n"
"s_cbranch_vccz .Lmk_p64k\n"
"s_sub_u32 s1, s1, 1\n"
"s_cbranch_scc0 .Lmk_p64l\n"
".Lmk_p64k:\n"
"v_mfma_f32_16x16x32_bf16 v[64:67], a[128:131], v[76:79], 0\n"
"v_mfma_f32_16x16x32_bf16 v[64:67], a[132:135], v[132:135], v[64:67]\n"
"v_mfma_f32_16x16x32_bf16 v[64:67], a[136:139], v[84:87], v[64:67]\n"
"v_mfma_f32_16x16x32_bf16 v[64:67], a[140:143], v[80:83], v[64:67]\n"
"v_mfma_f32_16x16x32_bf16 v[64:67], a[144:147], v[68:71], v[64:67]\n"
"v_mfma_f32_16x16x32_bf16 v[64:67], a[148:151], v[28:31], v[64:67]\n"
"v_mfma_f32_16x16x32_bf16 v[64:67], a[152:155], v[108:111], v[64:67]\n"
"v_mfma_f32_16x16x32_bf16 v[64:67], a[156:159], v[116:119], v[64:67]\n"
"v_mfma_f32_16x16x32_bf16 v[120:123], a[160:163], v[76:79], 0\n"
"v_mfma_f32_16x16x32_bf16 v[120:123], a[164:167], v[132:135], v[120:123]\n"
"v_mfma_f32_16x16x32_bf16 v[120:123], a[168:171], v[84:87], v[120:123]\n"
"v_mfma_f32_16x16x32_bf16 v[120:123], a[172:175], v[80:83], v[120:123]\n"
"v_mfma_f32_16x16x32_bf16 v[120:123], a[176:179], v[68:71], v[120:123]\n"
"v_mfma_f32_16x16x32_bf16 v[120:123], a[180:183], v[28:31], v[120:123]\n"
"s_nop 7\n"
"s_branch .Lmk_p64r\n"
".Lmk_p65s:\n"
";\n"
"s_mov_b32 s1, 0x10000\n"
".Lmk_p65l:\n"
"s_add_u32 s96, s57, 0x400000\n"
"buffer_load_dwordx4 v[124:127], v0, s[4:7], s96 offen sc1\n"
"buffer_load_dwordx4 v[112:115], v0, s[4:7], s96 offen offset:1024 sc1\n"
"buffer_load_dwordx4 v[144:147], v0, s[4:7], s96 offen offset:2048 sc1\n"
"buffer_load_dwordx4 v[148:151], v0, s[4:7], s96 offen offset:3072 sc1\n"
"buffer_load_dwordx4 v[104:107], v1, s[4:7], s96 offen sc1\n"
"buffer_load_dwordx4 v[56:59], v1, s[4:7], s96 offen offset:1024 sc1\n"
"buffer_load_dwordx4 v[48:51], v1, s[4:7], s96 offen offset:2048 sc1\n"
"buffer_load_dwordx4 v[60:63], v1, s[4:7], s96 offen offset:3072 sc1\n"
"s_add_u32 s96, s58, 0x400000\n"
"buffer_load_dwordx4 v[72:75], v0, s[4:7], s96 offen sc1\n"
"buffer_load_dwordx4 v[88:91], v0, s[4:7], s96 offen offset:1024 sc1\n"
"buffer_load_dwordx4 v[100:103], v0, s[4:7], s96 offen offset:2048 sc1\n"
"buffer_load_dwordx4 v[128:131], v0, s[4:7], s96 offen offset:3072 sc1\n"
"buffer_load_dwordx4 v[64:67], v1, s[4:7], s96 offen sc1\n"
"buffer_load_dwordx4 v[24:27], v1, s[4:7], s96 offen offset:1024 sc1\n"
"buffer_load_dwordx4 v[92:95], v1, s[4:7], s96 offen offset:2048 sc1\n"
"buffer_load_dwordx4 v[76:79], v1, s[4:7], s96 offen offset:3072 sc1\n"
"s_add_u32 s96, s59, 0x400000\n"
"buffer_load_dwordx4 v[52:55], v0, s[4:7], s96 offen sc1\n"
"buffer_load_dwordx4 v[28:31], v0, s[4:7], s96 offen offset:1024 sc1\n"
"buffer_load_dwordx4 v[80:83], v0, s[4:7], s96 offen offset:2048 sc1\n"
"buffer_load_dwordx4 v[108:111], v0, s[4:7], s96 offen offset:3072 sc1\n"
"buffer_load_dwordx4 v[120:123], v1, s[4:7], s96 offen sc1\n"
"s_waitcnt vmcnt(0)\n"
"v_max_u32_e32 v255, v124, v125\n"
"v_max3_u32 v255, v255, v126, v127\n"
"v_max3_u32 v255, v255, v112, v113\n"
"v_max3_u32 v255, v255, v114, v115\n"
"v_max3_u32 v255, v255, v144, v145\n"
"v_max3_u32 v255, v255, v146, v147\n"
"v_max3_u32 v255, v255, v148, v149\n"
"v_max3_u32 v255, v255, v150, v151\n"
"v_max3_u32 v255, v255, v104, v105\n"
"v_max3_u32 v255, v255, v106, v107\n"
"v_max3_u32 v255, v255, v56, v57\n"
"v_max3_u32 v255, v255, v58, v59\n"
"v_max3_u32 v255, v255, v48, v49\n"
"v_max3_u32 v255, v255, v50, v51\n"
"v_max3_u32 v255, v255, v60, v61\n"
"v_max3_u32 v255, v255, v62, v63\n"
"v_cmp_eq_u32_e32 vcc, -1, v255\n"
"s_cbranch_vccz .Lmk_p65k\n"
"s_sub_u32 s1, s1, 1\n"
"s_cbranch_scc0 .Lmk_p65l\n"
".Lmk_p65k:\n"
"v_mfma_f32_16x16x32_bf16 v[68:71], a[128:131], v[124:127], 0\n"
"v_mfma_f32_16x16x32_bf16 v[68:71], a[132:135], v[112:115], v[68:71]\n"
"v_mfma_f32_16x16x32_bf16 v[68:71], a[136:139], v[144:147], v[68:71]\n"
"v_mfma_f32_16x16x32_bf16 v[68:71], a[140:143], v[148:151], v[68:71]\n"
"v_mfma_f32_16x16x32_bf16 v[68:71], a[144:147], v[104:107], v[68:71]\n"
"v_mfma_f32_16x16x32_bf16 v[68:71], a[148:151], v[56:59], v[68:71]\n"
"v_mfma_f32_16x16x32_bf16 v[68:71], a[152:155], v[48:51], v[68:71]\n"
"v_mfma_f32_16x16x32_bf16 v[68:71], a[156:159], v[60:63], v[68:71]\n"
"v_mfma_f32_16x16x32_bf16 v[116:119], a[160:163], v[124:127], 0\n"
"v_mfma_f32_16x16x32_bf16 v[116:119], a[164:167], v[112:115], v[116:119]\n"
"v_mfma_f32_16x16x32_bf16 v[116:119], a[168:171], v[144:147], v[116:119]\n"
"v_mfma_f32_16x16x32_bf16 v[116:119], a[172:175], v[148:151], v[116:119]\n"
"v_mfma_f32_16x16x32_bf16 v[116:119], a[176:179], v[104:107], v[116:119]\n"
"v_mfma_f32_16x16x32_bf16 v[116:119], a[180:183], v[56:59], v[116:119]\n"
"s_nop 7\n"
"s_branch .Lmk_p65r\n"
".Lmk_p66s:\n"
";\n"
"s_mov_b32 s1, 0x10000\n"
".Lmk_p66l:\n"
"s_add_u32 s98, s58, 0x400000\n"
"buffer_load_dwordx4 v[72:75], v0, s[4:7], s98 offen sc1\n"
"buffer_load_dwordx4 v[88:91], v0, s[4:7], s98 offen offset:1024 sc1\n"
"buffer_load_dwordx4 v[100:103], v0, s[4:7], s98 offen offset:2048 sc1\n"
"buffer_load_dwordx4 v[128:131], v0, s[4:7], s98 offen offset:3072 sc1\n"
"buffer_load_dwordx4 v[64:67], v1, s[4:7], s98 offen sc1\n"
"buffer_load_dwordx4 v[24:27], v1, s[4:7], s98 offen offset:1024 sc1\n"
"buffer_load_dwordx4 v[92:95], v1, s[4:7], s98 offen offset:2048 sc1\n"
"buffer_load_dwordx4 v[76:79], v1, s[4:7], s98 offen offset:3072 sc1\n"
"s_add_u32 s98, s59, 0x400000\n"
"buffer_load_dwordx4 v[52:55], v0, s[4:7], s98 offen sc1\n"
"buffer_load_dwordx4 v[28:31], v0, s[4:7], s98 offen offset:1024 sc1\n"
"buffer_load_dwordx4 v[80:83], v0, s[4:7], s98 offen offset:2048 sc1\n"
"buffer_load_dwordx4 v[108:111], v0, s[4:7], s98 offen offset:3072 sc1\n"
"buffer_load_dwordx4 v[120:123], v1, s[4:7], s98 offen sc1\n"
"buffer_load_dwordx4 v[68:71], v1, s[4:7], s98 offen offset:1024 sc1\n"
"buffer_load_dwordx4 v[136:139], v1, s[4:7], s98 offen offset:2048 sc1\n"
"buffer_load_dwordx4 v[116:119], v1, s[4:7], s98 offen offset:3072 sc1\n"
"s_waitcnt vmcnt(0)\n"
"v_max_u32_e32 v253, v72, v73\n"
"v_max3_u32 v253, v253, v74, v75\n"
"v_max3_u32 v253, v253, v88, v89\n"
"v_max3_u32 v253, v253, v90, v91\n"
"v_max3_u32 v253, v253, v100, v101\n"
"v_max3_u32 v253, v253, v102, v103\n"
"v_max3_u32 v253, v253, v128, v129\n"
"v_max3_u32 v253, v253, v130, v131\n"
"v_max3_u32 v253, v253, v64, v65\n"
"v_max3_u32 v253, v253, v66, v67\n"
"v_max3_u32 v253, v253, v24, v25\n"
"v_max3_u32 v253, v253, v26, v27\n"
"v_max3_u32 v253, v253, v92, v93\n"
"v_max3_u32 v253, v253, v94, v95\n"
"v_max3_u32 v253, v253, v76, v77\n"
"v_max3_u32 v253, v253, v78, v79\n"
"v_cmp_eq_u32_e32 vcc, -1, v253\n"
"s_cbranch_vccz .Lmk_p66k\n"
"s_sub_u32 s1, s1, 1\n"
"s_cbranch_scc0 .Lmk_p66l\n"
".Lmk_p66k:\n"
"v_mfma_f32_16x16x32_bf16 v[48:51], a[128:131], v[72:75], 0\n"
"v_mfma_f32_16x16x32_bf16 v[48:51], a[132:135], v[88:91], v[48:51]\n"
"v_mfma_f32_16x16x32_bf16 v[48:51], a[136:139], v[100:103], v[48:51]\n"
"v_mfma_f32_16x16x32_bf16 v[48:51], a[140:143], v[128:131], v[48:51]\n"
"v_mfma_f32_16x16x32_bf16 v[48:51], a[144:147], v[64:67], v[48:51]\n"
"v_mfma_f32_16x16x32_bf16 v[48:51], a[148:151], v[24:27], v[48:51]\n"
"v_mfma_f32_16x16x32_bf16 v[48:51], a[152:155], v[92:95], v[48:51]\n"
"v_mfma_f32_16x16x32_bf16 v[48:51], a[156:159], v[76:79], v[48:51]\n"
"v_mfma_f32_16x16x32_bf16 v[56:59], a[160:163], v[72:75], 0\n"
"v_mfma_f32_16x16x32_bf16 v[56:59], a[164:167], v[88:91], v[56:59]\n"
"v_mfma_f32_16x16x32_bf16 v[56:59], a[168:171], v[100:103], v[56:59]\n"
"v_mfma_f32_16x16x32_bf16 v[56:59], a[172:175], v[128:131], v[56:59]\n"
"v_mfma_f32_16x16x32_bf16 v[56:59], a[176:179], v[64:67], v[56:59]\n"
"v_mfma_f32_16x16x32_bf16 v[56:59], a[180:183], v[24:27], v[56:59]\n"
"s_nop 7\n"
"s_branch .Lmk_p66r\n"
".Lmk_p67s:\n"
";\n"
"s_mov_b32 s1, 0x10000\n"
".Lmk_p67l:\n"
"s_add_u32 s98, s59, 0x400000\n"
"buffer_load_dwordx4 v[52:55], v0, s[4:7], s98 offen sc1\n"
"buffer_load_dwordx4 v[28:31], v0, s[4:7], s98 offen offset:1024 sc1\n"
"buffer_load_dwordx4 v[80:83], v0, s[4:7], s98 offen offset:2048 sc1\n"
"buffer_load_dwordx4 v[108:111], v0, s[4:7], s98 offen offset:3072 sc1\n"
"buffer_load_dwordx4 v[120:123], v1, s[4:7], s98 offen sc1\n"
"buffer_load_dwordx4 v[68:71], v1, s[4:7], s98 offen offset:1024 sc1\n"
"buffer_load_dwordx4 v[136:139], v1, s[4:7], s98 offen offset:2048 sc1\n"
"buffer_load_dwordx4 v[116:119], v1, s[4:7], s98 offen offset:3072 sc1\n"
"s_waitcnt vmcnt(0)\n"
"v_max_u32_e32 v255, v52, v53\n"
"v_max3_u32 v255, v255, v54, v55\n"
"v_max3_u32 v255, v255, v28, v29\n"
"v_max3_u32 v255, v255, v30, v31\n"
"v_max3_u32 v255, v255, v80, v81\n"
"v_max3_u32 v255, v255, v82, v83\n"
"v_max3_u32 v255, v255, v108, v109\n"
"v_max3_u32 v255, v255, v110, v111\n"
"v_max3_u32 v255, v255, v120, v121\n"
"v_max3_u32 v255, v255, v122, v123\n"
"v_max3_u32 v255, v255, v68, v69\n"
"v_max3_u32 v255, v255, v70, v71\n"
"v_max3_u32 v255, v255, v136, v137\n"
"v_max3_u32 v255, v255, v138, v139\n"
"v_max3_u32 v255, v255, v116, v117\n"
"v_max3_u32 v255, v255, v118, v119\n"
"v_cmp_eq_u32_e32 vcc, -1, v255\n"
"s_cbranch_vccz .Lmk_p67k\n"
"s_sub_u32 s1, s1, 1\n"
"s_cbranch_scc0 .Lmk_p67l\n"
".Lmk_p67k:\n"
"v_mfma_f32_16x16x32_bf16 v[24:27], a[128:131], v[52:55], v[60:63]\n"
"v_mfma_f32_16x16x32_bf16 v[24:27], a[132:135], v[28:31], v[24:27]\n"
"v_mfma_f32_16x16x32_bf16 v[24:27], a[136:139], v[80:83], v[24:27]\n"
"v_mfma_f32_16x16x32_bf16 v[24:27], a[140:143], v[108:111], v[24:27]\n"
"v_mfma_f32_16x16x32_bf16 v[24:27], a[144:147], v[120:123], v[24:27]\n"
"v_mfma_f32_16x16x32_bf16 v[24:27], a[148:151], v[68:71], v[24:27]\n"
"v_mfma_f32_16x16x32_bf16 v[24:27], a[152:155], v[136:139], v[24:27]\n"
"v_mfma_f32_16x16x32_bf16 v[24:27], a[156:159], v[116:119], v[24:27]\n"
"v_mfma_f32_16x16x32_bf16 v[48:51], a[160:163], v[52:55], v[96:99]\n"
"v_mfma_f32_16x16x32_bf16 v[48:51], a[164:167], v[28:31], v[48:51]\n"
"v_mfma_f32_16x16x32_bf16 v[48:51], a[168:171], v[80:83], v[48:51]\n"
"v_mfma_f32_16x16x32_bf16 v[48:51], a[172:175], v[108:111], v[48:51]\n"
"v_mfma_f32_16x16x32_bf16 v[48:51], a[176:179], v[120:123], v[48:51]\n"
"v_mfma_f32_16x16x32_bf16 v[48:51], a[180:183], v[68:71], v[48:51]\n"
"s_nop 7\n"
"s_branch .Lmk_p67r\n"
".Lmk_p68s:\n"
";\n"
"s_mov_b32 s1, 0x10000\n"
".Lmk_p68l:\n"
"s_add_u32 s96, s56, 0x440000\n"
"buffer_load_dwordx4 v[24:27], v0, s[4:7], s96 offen sc1\n"
"buffer_load_dwordx4 v[28:31], v0, s[4:7], s96 offen offset:1024 sc1\n"
"buffer_load_dwordx4 v[48:51], v0, s[4:7], s96 offen offset:2048 sc1\n"
"buffer_load_dwordx4 v[52:55], v0, s[4:7], s96 offen offset:3072 sc1\n"
"buffer_load_dwordx4 v[40:43], v1, s[4:7], s96 offen sc1\n"
"buffer_load_dwordx4 v[44:47], v1, s[4:7], s96 offen offset:1024 sc1\n"
"buffer_load_dwordx4 v[64:67], v1, s[4:7], s96 offen offset:2048 sc1\n"
"buffer_load_dwordx4 v[68:71], v1, s[4:7], s96 offen offset:3072 sc1\n"
"s_add_u32 s96, s57, 0x440000\n"
"buffer_load_dwordx4 v[32:35], v0, s[4:7], s96 offen sc1\n"
"buffer_load_dwordx4 v[36:39], v0, s[4:7], s96 offen offset:1024 sc1\n"
"buffer_load_dwordx4 v[56:59], v0, s[4:7], s96 offen offset:2048 sc1\n"
"buffer_load_dwordx4 v[60:63], v0, s[4:7], s96 offen offset:3072 sc1\n"
"buffer_load_dwordx4 v[72:75], v1, s[4:7], s96 offen sc1\n"
"buffer_load_dwordx4 v[76:79], v1, s[4:7], s96 offen offset:1024 sc1\n"
"buffer_load_dwordx4 v[80:83], v1, s[4:7], s96 offen offset:2048 sc1\n"
"buffer_load_dwordx4 v[84:87], v1, s[4:7], s96 offen offset:3072 sc1\n"
"s_add_u32 s96, s58, 0x440000\n"
"buffer_load_dwordx4 v[88:91], v0, s[4:7], s96 offen sc1\n"
"buffer_load_dwordx4 v[92:95], v0, s[4:7], s96 offen offset:1024 sc1\n"
"buffer_load_dwordx4 v[96:99], v0, s[4:7], s96 offen offset:2048 sc1\n"
"buffer_load_dwordx4 v[100:103], v0, s[4:7], s96 offen offset:3072 sc1\n"
"buffer_load_dwordx4 v[104:107], v1, s[4:7], s96 offen sc1\n"
"buffer_load_dwordx4 v[108:111], v1, s[4:7], s96 offen offset:1024 sc1\n"
"buffer_load_dwordx4 v[112:115], v1, s[4:7], s96 offen offset:2048 sc1\n"
"buffer_load_dwordx4 v[116:119], v1, s[4:7], s96 offen offset:3072 sc1\n"
"s_add_u32 s96, s59, 0x440000\n"
"buffer_load_dwordx4 v[124:127], v0, s[4:7], s96 offen sc1\n"
"buffer_load_dwordx4 v[128:131], v0, s[4:7], s96 offen offset:1024 sc1\n"
"buffer_load_dwordx4 v[136:139], v0, s[4:7], s96 offen offset:2048 sc1\n"
"buffer_load_dwordx4 v[140:143], v0, s[4:7], s96 offen offset:3072 sc1\n"
"s_waitcnt vmcnt(0)\n"
"v_max_u32_e32 v255, v24, v25\n"
"v_max3_u32 v255, v255, v26, v27\n"
"v_max3_u32 v255, v255, v28, v29\n"
"v_max3_u32 v255, v255, v30, v31\n"
"v_max3_u32 v255, v255, v48, v49\n"
"v_max3_u32 v255, v255, v50, v51\n"
"v_max3_u32 v255, v255, v52, v53\n"
"v_max3_u32 v255, v255, v54, v55\n"
"v_max3_u32 v255, v255, v40, v41\n"
"v_max3_u32 v255, v255, v42, v43\n"
"v_max3_u32 v255, v255, v44, v45\n"
"v_max3_u32 v255, v255, v46, v47\n"
"v_max3_u32 v255, v255, v64, v65\n"
"v_max3_u32 v255, v255, v66, v67\n"
"v_max3_u32 v255, v255, v68, v69\n"
"v_max3_u32 v255, v255, v70, v71\n"
"v_cmp_eq_u32_e32 vcc, -1, v255\n"
"s_cbranch_vccz .Lmk_p68k\n"
"s_sub_u32 s1, s1, 1\n"
"s_cbranch_scc0 .Lmk_p68l\n"
".Lmk_p68k:\n"
"v_mfma_f32_16x16x32_bf16 v[120:123], a[128:131], v[24:27], 0\n"
"v_mfma_f32_16x16x32_bf16 v[120:123], a[132:135], v[28:31], v[120:123]\n"
"v_mfma_f32_16x16x32_bf16 v[120:123], a[136:139], v[48:51], v[120:123]\n"
"v_mfma_f32_16x16x32_bf16 v[120:123], a[140:143], v[52:55], v[120:123]\n"
"v_mfma_f32_16x16x32_bf16 v[120:123], a[144:147], v[40:43], v[120:123]\n"
"v_mfma_f32_16x16x32_bf16 v[120:123], a[148:151], v[44:47], v[120:123]\n"
"v_mfma_f32_16x16x32_bf16 v[120:123], a[152:155], v[64:67], v[120:123]\n"
"v_mfma_f32_16x16x32_bf16 v[120:123], a[156:159], v[68:71], v[120:123]\n"
"v_mfma_f32_16x16x32_bf16 v[132:135], a[160:163], v[24:27], 0\n"
"v_mfma_f32_16x16x32_bf16 v[132:135], a[164:167], v[28:31], v[132:135]\n"
"v_mfma_f32_16x16x32_bf16 v[132:135], a[168:171], v[48:51], v[132:135]\n"
"v_mfma_f32_16x16x32_bf16 v[132:135], a[172:175], v[52:55], v[132:135]\n"
"v_mfma_f32_16x16x32_bf16 v[132:135], a[176:179], v[40:43], v[132:135]\n"
"v_mfma_f32_16x16x32_bf16 v[132:135], a[180:183], v[44:47], v[132:135]\n"
"s_nop 7\n"
"s_branch .Lmk_p68r\n"
".Lmk_p69s:\n"
";\n"
"s_mov_b32 s1, 0x10000\n"
".Lmk_p69l:\n"
"s_add_u32 s96, s57, 0x440000\n"
"buffer_load_dwordx4 v[32:35], v0, s[4:7], s96 offen sc1\n"
"buffer_load_dwordx4 v[36:39], v0, s[4:7], s96 offen offset:1024 sc1\n"
"buffer_load_dwordx4 v[56:59], v0, s[4:7], s96 offen offset:2048 sc1\n"
"buffer_load_dwordx4 v[60:63], v0, s[4:7], s96 offen offset:3072 sc1\n"
"buffer_load_dwordx4 v[72:75], v1, s[4:7], s96 offen sc1\n"
"buffer_load_dwordx4 v[76:79], v1, s[4:7], s96 offen offset:1024 sc1\n"
"buffer_load_dwordx4 v[80:83], v1, s[4:7], s96 offen offset:2048 sc1\n"
"buffer_load_dwordx4 v[84:87], v1, s[4:7], s96 offen offset:3072 sc1\n"
"s_add_u32 s96, s58, 0x440000\n"
"buffer_load_dwordx4 v[88:91], v0, s[4:7], s96 offen sc1\n"
"buffer_load_dwordx4 v[92:95], v0, s[4:7], s96 offen offset:1024 sc1\n"
"buffer_load_dwordx4 v[96:99], v0, s[4:7], s96 offen offset:2048 sc1\n"
"buffer_load_dwordx4 v[100:103], v0, s[4:7], s96 offen offset:3072 sc1\n"
"buffer_load_dwordx4 v[104:107], v1, s[4:7], s96 offen sc1\n"
"buffer_load_dwordx4 v[108:111], v1, s[4:7], s96 offen offset:1024 sc1\n"
"buffer_load_dwordx4 v[112:115], v1, s[4:7], s96 offen offset:2048 sc1\n"
"buffer_load_dwordx4 v[116:119], v1, s[4:7], s96 offen offset:3072 sc1\n"
"s_add_u32 s96, s59, 0x440000\n"
"buffer_load_dwordx4 v[124:127], v0, s[4:7], s96 offen sc1\n"
"buffer_load_dwordx4 v[128:131], v0, s[4:7], s96 offen offset:1024 sc1\n"
"buffer_load_dwordx4 v[136:139], v0, s[4:7], s96 offen offset:2048 sc1\n"
"buffer_load_dwordx4 v[140:143], v0, s[4:7], s96 offen offset:3072 sc1\n"
"buffer_load_dwordx4 v[120:123], v1, s[4:7], s96 offen sc1\n"
"buffer_load_dwordx4 v[148:151], v1, s[4:7], s96 offen offset:1024 sc1\n"
"buffer_load_dwordx4 v[132:135], v1, s[4:7], s96 offen offset:2048 sc1\n"
"buffer_load_dwordx4 v[24:27], v1, s[4:7], s96 offen offset:3072 sc1\n"
"s_add_u32 s96, s56, 0x400000\n"
"buffer_load_dwordx4 v[44:47], v0, s[4:7], s96 offen sc1\n"
"buffer_load_dwordx4 v[48:51], v0, s[4:7], s96 offen offset:1024 sc1\n"
"s_waitcnt vmcnt(0)\n"
"v_max_u32_e32 v255, v32, v33\n"
"v_max3_u32 v255, v255, v34, v35\n"
"v_max3_u32 v255, v255, v36, v37\n"
"v_max3_u32 v255, v255, v38, v39\n"
"v_max3_u32 v255, v255, v56, v57\n"
"v_max3_u32 v255, v255, v58, v59\n"
"v_max3_u32 v255, v255, v60, v61\n"
"v_max3_u32 v255, v255, v62, v63\n"
"v_max3_u32 v255, v255, v72, v73\n"
"v_max3_u32 v255, v255, v74, v75\n"
"v_max3_u32 v255, v255, v76, v77\n"
"v_max3_u32 v255, v255, v78, v79\n"
"v_max3_u32 v255, v255, v80, v81\n"
"v_max3_u32 v255, v255, v82, v83\n"
"v_max3_u32 v255, v255, v84, v85\n"
"v_max3_u32 v255, v255, v86, v87\n"
"v_cmp_eq_u32_e32 vcc, -1, v255\n"
"s_cbranch_vccz .Lmk_p69k\n"
"s_sub_u32 s1, s1, 1\n"
"s_cbranch_scc0 .Lmk_p69l\n"
".Lmk_p69k:\n"
"v_mfma_f32_16x16x32_bf16 v[28:31], a[128:131], v[32:35], 0\n"
"v_mfma_f32_16x16x32_bf16 v[28:31], a[132:135], v[36:39], v[28:31]\n"
"v_mfma_f32_16x16x32_bf16 v[28:31], a[136:139], v[56:59], v[28:31]\n"
"v_mfma_f32_16x16x32_bf16 v[28:31], a[140:143], v[60:63], v[28:31]\n"
"v_mfma_f32_16x16x32_bf16 v[28:31], a[144:147], v[72:75], v[28:31]\n"
"v_mfma_f32_16x16x32_bf16 v[28:31], a[148:151], v[76:79], v[28:31]\n"
"v_mfma_f32_16x16x32_bf16 v[28:31], a[152:155], v[80:83], v[28:31]\n"
"v_mfma_f32_16x16x32_bf16 v[28:31], a[156:159], v[84:87], v[28:31]\n"
"v_mfma_f32_16x16x32_bf16 v[40:43], a[160:163], v[32:35], 0\n"
"v_mfma_f32_16x16x32_bf16 v[40:43], a[164:167], v[36:39], v[40:43]\n"
"v_mfma_f32_16x16x32_bf16 v[40:43], a[168:171], v[56:59], v[40:43]\n"
"v_mfma_f32_16x16x32_bf16 v[40:43], a[172:175], v[60:63], v[40:43]\n"
"v_mfma_f32_16x16x32_bf16 v[40:43], a[176:179], v[72:75], v[40:43]\n"
"v_mfma_f32_16x16x32_bf16 v[40:43], a[180:183], v[76:79], v[40:43]\n"
"s_nop 7\n"
"s_branch .Lmk_p69r\n"
".Lmk_p70s:\n"
";\n"
"s_mov_b32 s1, 0x10000\n"
".Lmk_p70l:\n"
"s_add_u32 s96, s58, 0x440000\n"
"buffer_load_dwordx4 v[88:91], v0, s[4:7], s96 offen sc1\n"
"buffer_load_dwordx4 v[92:95], v0, s[4:7], s96 offen offset:1024 sc1\n"
"buffer_load_dwordx4 v[96:99], v0, s[4:7], s96 offen offset:2048 sc1\n"
"buffer_load_dwordx4 v[100:103], v0, s[4:7], s96 offen offset:3072 sc1\n"
"buffer_load_dwordx4 v[104:107], v1, s[4:7], s96 offen sc1\n"
"buffer_load_dwordx4 v[108:111], v1, s[4:7], s96 offen offset:1024 sc1\n"
"buffer_load_dwordx4 v[112:115], v1, s[4:7], s96 offen offset:2048 sc1\n"
"buffer_load_dwordx4 v[116:119], v1, s[4:7], s96 offen offset:3072 sc1\n"
"s_add_u32 s96, s59, 0x440000\n"
"buffer_load_dwordx4 v[124:127], v0, s[4:7], s96 offen sc1\n"
"buffer_load_dwordx4 v[128:131], v0, s[4:7], s96 offen offset:1024 sc1\n"
"buffer_load_dwordx4 v[136:139], v0, s[4:7], s96 offen offset:2048 sc1\n"
"buffer_load_dwordx4 v[140:143], v0, s[4:7], s96 offen offset:3072 sc1\n"
"buffer_load_dwordx4 v[120:123], v1, s[4:7], s96 offen sc1\n"
"buffer_load_dwordx4 v[148:151], v1, s[4:7], s96 offen offset:1024 sc1\n"
"buffer_load_dwordx4 v[132:135], v1, s[4:7], s96 offen offset:2048 sc1\n"
"buffer_load_dwordx4 v[24:27], v1, s[4:7], s96 offen offset:3072 sc1\n"
"s_add_u32 s96, s56, 0x400000\n"
"buffer_load_dwordx4 v[44:47], v0, s[4:7], s96 offen sc1\n"
"buffer_load_dwordx4 v[48:51], v0, s[4:7], s96 offen offset:1024 sc1\n"
"buffer_load_dwordx4 v[28:31], v0, s[4:7], s96 offen offset:2048 sc1\n"
"buffer_load_dwordx4 v[64:67], v0, s[4:7], s96 offen offset:3072 sc1\n"
"buffer_load_dwordx4 v[40:43], v1, s[4:7], s96 offen sc1\n"
"buffer_load_dwordx4 v[32:35], v1, s[4:7], s96 offen offset:1024 sc1\n"
"buffer_load_dwordx4 v[36:39], v1, s[4:7], s96 offen offset:2048 sc1\n"
"buffer_load_dwordx4 v[52:55], v1, s[4:7], s96 offen offset:3072 sc1\n"
"s_add_u32 s96, s57, 0x400000\n"
"buffer_load_dwordx4 v[60:63], v0, s[4:7], s96 offen sc1\n"
"buffer_load_dwordx4 v[68:71], v0, s[4:7], s96 offen offset:1024 sc1\n"
"buffer_load_dwordx4 v[76:79], v0, s[4:7], s96 offen offset:2048 sc1\n"
"s_waitcnt vmcnt(0)\n"
"v_max_u32_e32 v239, v88, v89\n"
"v_max3_u32 v239, v239, v90, v91\n"
"v_max3_u32 v239, v239, v92, v93\n"
"v_max3_u32 v239, v239, v94, v95\n"
"v_max3_u32 v239, v239, v96, v97\n"
"v_max3_u32 v239, v239, v98, v99\n"
"v_max3_u32 v239, v239, v100, v101\n"
"v_max3_u32 v239, v239, v102, v103\n"
"v_max3_u32 v239, v239, v104, v105\n"
"v_max3_u32 v239, v239, v106, v107\n"
"v_max3_u32 v239, v239, v108, v109\n"
"v_max3_u32 v239, v239, v110, v111\n"
"v_max3_u32 v239, v239, v112, v113\n"
"v_max3_u32 v239, v239, v114, v115\n"
"v_max3_u32 v239, v239, v116, v117\n"
"v_max3_u32 v239, v239, v118, v119\n"
"v_cmp_eq_u32_e32 vcc, -1, v239\n"
"s_cbranch_vccz .Lmk_p70k\n"
"s_sub_u32 s1, s1, 1\n"
"s_cbranch_scc0 .Lmk_p70l\n"
".Lmk_p70k:\n"
"v_mfma_f32_16x16x32_bf16 v[56:59], a[128:131], v[88:91], 0\n"
"v_mfma_f32_16x16x32_bf16 v[56:59], a[132:135], v[92:95], v[56:59]\n"
"v_mfma_f32_16x16x32_bf16 v[56:59], a[136:139], v[96:99], v[56:59]\n"
"v_mfma_f32_16x16x32_bf16 v[56:59], a[140:143], v[100:103], v[56:59]\n"
"v_mfma_f32_16x16x32_bf16 v[56:59], a[144:147], v[104:107], v[56:59]\n"
"v_mfma_f32_16x16x32_bf16 v[56:59], a[148:151], v[108:111], v[56:59]\n"
"v_mfma_f32_16x16x32_bf16 v[56:59], a[152:155], v[112:115], v[56:59]\n"
"v_mfma_f32_16x16x32_bf16 v[56:59], a[156:159], v[116:119], v[56:59]\n"
"v_mfma_f32_16x16x32_bf16 v[72:75], a[160:163], v[88:91], 0\n"
"v_mfma_f32_16x16x32_bf16 v[72:75], a[164:167], v[92:95], v[72:75]\n"
"v_mfma_f32_16x16x32_bf16 v[72:75], a[168:171], v[96:99], v[72:75]\n"
"v_mfma_f32_16x16x32_bf16 v[72:75], a[172:175], v[100:103], v[72:75]\n"
"v_mfma_f32_16x16x32_bf16 v[72:75], a[176:179], v[104:107], v[72:75]\n"
"v_mfma_f32_16x16x32_bf16 v[72:75], a[180:183], v[108:111], v[72:75]\n"
"s_nop 7\n"
"s_branch .Lmk_p70r\n"
".Lmk_p71s:\n"
";\n"
"s_mov_b32 s1, 0x10000\n"
".Lmk_p71l:\n"
"s_add_u32 s96, s59, 0x440000\n"
"buffer_load_dwordx4 v[124:127], v0, s[4:7], s96 offen sc1\n"
"buffer_load_dwordx4 v[128:131], v0, s[4:7], s96 offen offset:1024 sc1\n"
"buffer_load_dwordx4 v[136:139], v0, s[4:7], s96 offen offset:2048 sc1\n"
"buffer_load_dwordx4 v[140:143], v0, s[4:7], s96 offen offset:3072 sc1\n"
"buffer_load_dwordx4 v[120:123], v1, s[4:7], s96 offen sc1\n"
"buffer_load_dwordx4 v[148:151], v1, s[4:7], s96 offen offset:1024 sc1\n"
"buffer_load_dwordx4 v[132:135], v1, s[4:7], s96 offen offset:2048 sc1\n"
"buffer_load_dwordx4 v[24:27], v1, s[4:7], s96 offen offset:3072 sc1\n"
"s_add_u32 s96, s56, 0x400000\n"
"buffer_load_dwordx4 v[44:47], v0, s[4:7], s96 offen sc1\n"
"buffer_load_dwordx4 v[48:51], v0, s[4:7], s96 offen offset:1024 sc1\n"
"buffer_load_dwordx4 v[28:31], v0, s[4:7], s96 offen offset:2048 sc1\n"
"buffer_load_dwordx4 v[64:67], v0, s[4:7], s96 offen offset:3072 sc1\n"
"buffer_load_dwordx4 v[40:43], v1, s[4:7], s96 offen sc1\n"
"buffer_load_dwordx4 v[32:35], v1, s[4:7], s96 offen offset:1024 sc1\n"
"buffer_load_dwordx4 v[36:39], v1, s[4:7], s96 offen offset:2048 sc1\n"
"buffer_load_dwordx4 v[52:55], v1, s[4:7], s96 offen offset:3072 sc1\n"
"s_add_u32 s96, s57, 0x400000\n"
"buffer_load_dwordx4 v[60:63], v0, s[4:7], s96 offen sc1\n"
"buffer_load_dwordx4 v[68:71], v0, s[4:7], s96 offen offset:1024 sc1\n"
"buffer_load_dwordx4 v[76:79], v0, s[4:7], s96 offen offset:2048 sc1\n"
"buffer_load_dwordx4 v[56:59], v0, s[4:7], s96 offen offset:3072 sc1\n"
"buffer_load_dwordx4 v[160:163], v1, s[4:7], s96 offen sc1\n"
"buffer_load_dwordx4 v[72:75], v1, s[4:7], s96 offen offset:1024 sc1\n"
"buffer_load_dwordx4 v[88:91], v1, s[4:7], s96 offen offset:2048 sc1\n"
"buffer_load_dwordx4 v[92:95], v1, s[4:7], s96 offen offset:3072 sc1\n"
"s_add_u32 s96, s58, 0x400000\n"
"buffer_load_dwordx4 v[104:107], v0, s[4:7], s96 offen sc1\n"
"buffer_load_dwordx4 v[112:115], v0, s[4:7], s96 offen offset:1024 sc1\n"
"buffer_load_dwordx4 v[116:119], v0, s[4:7], s96 offen offset:2048 sc1\n"
"buffer_load_dwordx4 v[144:147], v0, s[4:7], s96 offen offset:3072 sc1\n"
"s_waitcnt vmcnt(0)\n"
"v_max_u32_e32 v255, v124, v125\n"
"v_max3_u32 v255, v255, v126, v127\n"
"v_max3_u32 v255, v255, v128, v129\n"
"v_max3_u32 v255, v255, v130, v131\n"
"v_max3_u32 v255, v255, v136, v137\n"
"v_max3_u32 v255, v255, v138, v139\n"
"v_max3_u32 v255, v255, v140, v141\n"
"v_max3_u32 v255, v255, v142, v143\n"
"v_max3_u32 v255, v255, v120, v121\n"
"v_max3_u32 v255, v255, v122, v123\n"
"v_max3_u32 v255, v255, v148, v149\n"
"v_max3_u32 v255, v255, v150, v151\n"
"v_max3_u32 v255, v255, v132, v133\n"
"v_max3_u32 v255, v255, v134, v135\n"
"v_max3_u32 v255, v255, v24, v25\n"
"v_max3_u32 v255, v255, v26, v27\n"
"v_cmp_eq_u32_e32 vcc, -1, v255\n"
"s_cbranch_vccz .Lmk_p71k\n"
"s_sub_u32 s1, s1, 1\n"
"s_cbranch_scc0 .Lmk_p71l\n"
".Lmk_p71k:\n"
"v_mfma_f32_16x16x32_bf16 v[108:111], a[128:131], v[124:127], v[80:83]\n"
"v_mfma_f32_16x16x32_bf16 v[108:111], a[132:135], v[128:131], v[108:111]\n"
"v_mfma_f32_16x16x32_bf16 v[108:111], a[136:139], v[136:139], v[108:111]\n"
"v_mfma_f32_16x16x32_bf16 v[108:111], a[140:143], v[140:143], v[108:111]\n"
"v_mfma_f32_16x16x32_bf16 v[108:111], a[144:147], v[120:123], v[108:111]\n"
"v_mfma_f32_16x16x32_bf16 v[108:111], a[148:151], v[148:151], v[108:111]\n"
"v_mfma_f32_16x16x32_bf16 v[108:111], a[152:155], v[132:135], v[108:111]\n"
"v_mfma_f32_16x16x32_bf16 v[108:111], a[156:159], v[24:27], v[108:111]\n"
"v_mfma_f32_16x16x32_bf16 v[100:103], a[160:163], v[124:127], v[152:155]\n"
"v_mfma_f32_16x16x32_bf16 v[100:103], a[164:167], v[128:131], v[100:103]\n"
"v_mfma_f32_16x16x32_bf16 v[100:103], a[168:171], v[136:139], v[100:103]\n"
"v_mfma_f32_16x16x32_bf16 v[100:103], a[172:175], v[140:143], v[100:103]\n"
"v_mfma_f32_16x16x32_bf16 v[100:103], a[176:179], v[120:123], v[100:103]\n"
"v_mfma_f32_16x16x32_bf16 v[100:103], a[180:183], v[148:151], v[100:103]\n"
"s_nop 7\n"
"s_branch .Lmk_p71r\n"
".Lmk_p73s:\n"
";\n"
"s_mov_b32 s1, 0x10000\n"
".Lmk_p73l:\n"
"s_add_u32 s97, s56, 0x400000\n"
"buffer_load_dwordx4 v[44:47], v0, s[4:7], s97 offen sc1\n"
"buffer_load_dwordx4 v[48:51], v0, s[4:7], s97 offen offset:1024 sc1\n"
"buffer_load_dwordx4 v[28:31], v0, s[4:7], s97 offen offset:2048 sc1\n"
"buffer_load_dwordx4 v[64:67], v0, s[4:7], s97 offen offset:3072 sc1\n"
"buffer_load_dwordx4 v[40:43], v1, s[4:7], s97 offen sc1\n"
"buffer_load_dwordx4 v[32:35], v1, s[4:7], s97 offen offset:1024 sc1\n"
"buffer_load_dwordx4 v[36:39], v1, s[4:7], s97 offen offset:2048 sc1\n"
"buffer_load_dwordx4 v[52:55], v1, s[4:7], s97 offen offset:3072 sc1\n"
"s_add_u32 s97, s57, 0x400000\n"
"buffer_load_dwordx4 v[60:63], v0, s[4:7], s97 offen sc1\n"
"buffer_load_dwordx4 v[68:71], v0, s[4:7], s97 offen offset:1024 sc1\n"
"buffer_load_dwordx4 v[76:79], v0, s[4:7], s97 offen offset:2048 sc1\n"
"buffer_load_dwordx4 v[56:59], v0, s[4:7], s97 offen offset:3072 sc1\n"
"buffer_load_dwordx4 v[160:163], v1, s[4:7], s97 offen sc1\n"
"buffer_load_dwordx4 v[72:75], v1, s[4:7], s97 offen offset:1024 sc1\n"
"buffer_load_dwordx4 v[88:91], v1, s[4:7], s97 offen offset:2048 sc1\n"
"buffer_load_dwordx4 v[92:95], v1, s[4:7], s97 offen offset:3072 sc1\n"
"s_add_u32 s97, s58, 0x400000\n"
"buffer_load_dwordx4 v[104:107], v0, s[4:7], s97 offen sc1\n"
"buffer_load_dwordx4 v[112:115], v0, s[4:7], s97 offen offset:1024 sc1\n"
"buffer_load_dwordx4 v[116:119], v0, s[4:7], s97 offen offset:2048 sc1\n"
"buffer_load_dwordx4 v[144:147], v0, s[4:7], s97 offen offset:3072 sc1\n"
"buffer_load_dwordx4 v[152:155], v1, s[4:7], s97 offen sc1\n"
"buffer_load_dwordx4 v[172:175], v1, s[4:7], s97 offen offset:1024 sc1\n"
"buffer_load_dwordx4 v[176:179], v1, s[4:7], s97 offen offset:2048 sc1\n"
"buffer_load_dwordx4 v[108:111], v1, s[4:7], s97 offen offset:3072 sc1\n"
"s_add_u32 s97, s59, 0x400000\n"
"buffer_load_dwordx4 v[24:27], v0, s[4:7], s97 offen sc1\n"
"buffer_load_dwordx4 v[124:127], v0, s[4:7], s97 offen offset:1024 sc1\n"
"buffer_load_dwordx4 v[84:87], v0, s[4:7], s97 offen offset:2048 sc1\n"
"buffer_load_dwordx4 v[80:83], v0, s[4:7], s97 offen offset:3072 sc1\n"
"buffer_load_dwordx4 v[128:131], v1, s[4:7], s97 offen sc1\n"
"buffer_load_dwordx4 v[100:103], v1, s[4:7], s97 offen offset:1024 sc1\n"
"buffer_load_dwordx4 v[96:99], v1, s[4:7], s97 offen offset:2048 sc1\n"
"buffer_load_dwordx4 v[120:123], v1, s[4:7], s97 offen offset:3072 sc1\n"
"s_waitcnt vmcnt(0)\n"
"v_max_u32_e32 v255, v44, v45\n"
"v_max3_u32 v255, v255, v46, v47\n"
"v_max3_u32 v255, v255, v48, v49\n"
"v_max3_u32 v255, v255, v50, v51\n"
"v_max3_u32 v255, v255, v28, v29\n"
"v_max3_u32 v255, v255, v30, v31\n"
"v_max3_u32 v255, v255, v64, v65\n"
"v_max3_u32 v255, v255, v66, v67\n"
"v_max3_u32 v255, v255, v40, v41\n"
"v_max3_u32 v255, v255, v42, v43\n"
"v_max3_u32 v255, v255, v32, v33\n"
"v_max3_u32 v255, v255, v34, v35\n"
"v_max3_u32 v255, v255, v36, v37\n"
"v_max3_u32 v255, v255, v38, v39\n"
"v_max3_u32 v255, v255, v52, v53\n"
"v_max3_u32 v255, v255, v54, v55\n"
"v_cmp_eq_u32_e32 vcc, -1, v255\n"
"s_cbranch_vccz .Lmk_p73k\n"
"s_sub_u32 s1, s1, 1\n"
"s_cbranch_scc0 .Lmk_p73l\n"
".Lmk_p73k:\n"
"v_mfma_f32_16x16x32_bf16 v[132:135], a[128:131], v[44:47], 0\n"
"v_mfma_f32_16x16x32_bf16 v[132:135], a[132:135], v[48:51], v[132:135]\n"
"v_mfma_f32_16x16x32_bf16 v[132:135], a[136:139], v[28:31], v[132:135]\n"
"v_mfma_f32_16x16x32_bf16 v[132:135], a[140:143], v[64:67], v[132:135]\n"
"v_mfma_f32_16x16x32_bf16 v[132:135], a[144:147], v[40:43], v[132:135]\n"
"v_mfma_f32_16x16x32_bf16 v[132:135], a[148:151], v[32:35], v[132:135]\n"
"v_mfma_f32_16x16x32_bf16 v[132:135], a[152:155], v[36:39], v[132:135]\n"
"v_mfma_f32_16x16x32_bf16 v[132:135], a[156:159], v[52:55], v[132:135]\n"
"v_mfma_f32_16x16x32_bf16 v[136:139], a[160:163], v[44:47], 0\n"
"v_mfma_f32_16x16x32_bf16 v[136:139], a[164:167], v[48:51], v[136:139]\n"
"v_mfma_f32_16x16x32_bf16 v[136:139], a[168:171], v[28:31], v[136:139]\n"
"v_mfma_f32_16x16x32_bf16 v[136:139], a[172:175], v[64:67], v[136:139]\n"
"v_mfma_f32_16x16x32_bf16 v[136:139], a[176:179], v[40:43], v[136:139]\n"
"v_mfma_f32_16x16x32_bf16 v[136:139], a[180:183], v[32:35], v[136:139]\n"
"s_nop 7\n"
"s_branch .Lmk_p73r\n"
".Lmk_p74s:\n"
";\n"
"s_mov_b32 s1, 0x10000\n"
".Lmk_p74l:\n"
"s_add_u32 s97, s57, 0x400000\n"
"buffer_load_dwordx4 v[60:63], v0, s[4:7], s97 offen sc1\n"
"buffer_load_dwordx4 v[68:71], v0, s[4:7], s97 offen offset:1024 sc1\n"
"buffer_load_dwordx4 v[76:79], v0, s[4:7], s97 offen offset:2048 sc1\n"
"buffer_load_dwordx4 v[56:59], v0, s[4:7], s97 offen offset:3072 sc1\n"
"buffer_load_dwordx4 v[160:163], v1, s[4:7], s97 offen sc1\n"
"buffer_load_dwordx4 v[72:75], v1, s[4:7], s97 offen offset:1024 sc1\n"
"buffer_load_dwordx4 v[88:91], v1, s[4:7], s97 offen offset:2048 sc1\n"
"buffer_load_dwordx4 v[92:95], v1, s[4:7], s97 offen offset:3072 sc1\n"
"s_add_u32 s97, s58, 0x400000\n"
"buffer_load_dwordx4 v[104:107], v0, s[4:7], s97 offen sc1\n"
"buffer_load_dwordx4 v[112:115], v0, s[4:7], s97 offen offset:1024 sc1\n"
"buffer_load_dwordx4 v[116:119], v0, s[4:7], s97 offen offset:2048 sc1\n"
"buffer_load_dwordx4 v[144:147], v0, s[4:7], s97 offen offset:3072 sc1\n"
"buffer_load_dwordx4 v[152:155], v1, s[4:7], s97 offen sc1\n"
"buffer_load_dwordx4 v[172:175], v1, s[4:7], s97 offen offset:1024 sc1\n"
"buffer_load_dwordx4 v[176:179], v1, s[4:7], s97 offen offset:2048 sc1\n"
"buffer_load_dwordx4 v[108:111], v1, s[4:7], s97 offen offset:3072 sc1\n"
"s_add_u32 s97, s59, 0x400000\n"
"buffer_load_dwordx4 v[24:27], v0, s[4:7], s97 offen sc1\n"
"buffer_load_dwordx4 v[124:127], v0, s[4:7], s97 offen offset:1024 sc1\n"
"buffer_load_dwordx4 v[84:87], v0, s[4:7], s97 offen offset:2048 sc1\n"
"buffer_load_dwordx4 v[80:83], v0, s[4:7], s97 offen offset:3072 sc1\n"
"buffer_load_dwordx4 v[128:131], v1, s[4:7], s97 offen sc1\n"
"buffer_load_dwordx4 v[100:103], v1, s[4:7], s97 offen offset:1024 sc1\n"
"buffer_load_dwordx4 v[96:99], v1, s[4:7], s97 offen offset:2048 sc1\n"
"buffer_load_dwordx4 v[120:123], v1, s[4:7], s97 offen offset:3072 sc1\n"
"s_waitcnt vmcnt(0)\n"
"v_max_u32_e32 v255, v60, v61\n"
"v_max3_u32 v255, v255, v62, v63\n"
"v_max3_u32 v255, v255, v68, v69\n"
"v_max3_u32 v255, v255, v70, v71\n"
"v_max3_u32 v255, v255, v76, v77\n"
"v_max3_u32 v255, v255, v78, v79\n"
"v_max3_u32 v255, v255, v56, v57\n"
"v_max3_u32 v255, v255, v58, v59\n"
"v_max3_u32 v255, v255, v160, v161\n"
"v_max3_u32 v255, v255, v162, v163\n"
"v_max3_u32 v255, v255, v72, v73\n"
"v_max3_u32 v255, v255, v74, v75\n"
"v_max3_u32 v255, v255, v88, v89\n"
"v_max3_u32 v255, v255, v90, v91\n"
"v_max3_u32 v255, v255, v92, v93\n"
"v_max3_u32 v255, v255, v94, v95\n"
"v_cmp_eq_u32_e32 vcc, -1, v255\n"
"s_cbranch_vccz .Lmk_p74k\n"
"s_sub_u32 s1, s1, 1\n"
"s_cbranch_scc0 .Lmk_p74l\n"
".Lmk_p74k:\n"
"v_mfma_f32_16x16x32_bf16 v[36:39], a[128:131], v[60:63], 0\n"
"v_mfma_f32_16x16x32_bf16 v[36:39], a[132:135], v[68:71], v[36:39]\n"
"v_mfma_f32_16x16x32_bf16 v[36:39], a[136:139], v[76:79], v[36:39]\n"
"v_mfma_f32_16x16x32_bf16 v[36:39], a[140:143], v[56:59], v[36:39]\n"
"v_mfma_f32_16x16x32_bf16 v[36:39], a[144:147], v[160:163], v[36:39]\n"
"v_mfma_f32_16x16x32_bf16 v[36:39], a[148:151], v[72:75], v[36:39]\n"
"v_mfma_f32_16x16x32_bf16 v[36:39], a[152:155], v[88:91], v[36:39]\n"
"v_mfma_f32_16x16x32_bf16 v[36:39], a[156:159], v[92:95], v[36:39]\n"
"v_mfma_f32_16x16x32_bf16 v[52:55], a[160:163], v[60:63], 0\n"
"v_mfma_f32_16x16x32_bf16 v[52:55], a[164:167], v[68:71], v[52:55]\n"
"v_mfma_f32_16x16x32_bf16 v[52:55], a[168:171], v[76:79], v[52:55]\n"
"v_mfma_f32_16x16x32_bf16 v[52:55], a[172:175], v[56:59], v[52:55]\n"
"v_mfma_f32_16x16x32_bf16 v[52:55], a[176:179], v[160:163], v[52:55]\n"
"v_mfma_f32_16x16x32_bf16 v[52:55], a[180:183], v[72:75], v[52:55]\n"
"s_nop 7\n"
"s_branch .Lmk_p74r\n"
".Lmk_p75s:\n"
";\n"
"s_mov_b32 s1, 0x10000\n"
".Lmk_p75l:\n"
"s_add_u32 s98, s58, 0x400000\n"
"buffer_load_dwordx4 v[104:107], v0, s[4:7], s98 offen sc1\n"
"buffer_load_dwordx4 v[112:115], v0, s[4:7], s98 offen offset:1024 sc1\n"
"buffer_load_dwordx4 v[116:119], v0, s[4:7], s98 offen offset:2048 sc1\n"
"buffer_load_dwordx4 v[144:147], v0, s[4:7], s98 offen offset:3072 sc1\n"
"buffer_load_dwordx4 v[152:155], v1, s[4:7], s98 offen sc1\n"
"buffer_load_dwordx4 v[172:175], v1, s[4:7], s98 offen offset:1024 sc1\n"
"buffer_load_dwordx4 v[176:179], v1, s[4:7], s98 offen offset:2048 sc1\n"
"buffer_load_dwordx4 v[108:111], v1, s[4:7], s98 offen offset:3072 sc1\n"
"s_add_u32 s98, s59, 0x400000\n"
"buffer_load_dwordx4 v[24:27], v0, s[4:7], s98 offen sc1\n"
"buffer_load_dwordx4 v[124:127], v0, s[4:7], s98 offen offset:1024 sc1\n"
"buffer_load_dwordx4 v[84:87], v0, s[4:7], s98 offen offset:2048 sc1\n"
"buffer_load_dwordx4 v[80:83], v0, s[4:7], s98 offen offset:3072 sc1\n"
"buffer_load_dwordx4 v[128:131], v1, s[4:7], s98 offen sc1\n"
"buffer_load_dwordx4 v[100:103], v1, s[4:7], s98 offen offset:1024 sc1\n"
"buffer_load_dwordx4 v[96:99], v1, s[4:7], s98 offen offset:2048 sc1\n"
"buffer_load_dwordx4 v[120:123], v1, s[4:7], s98 offen offset:3072 sc1\n"
"s_waitcnt vmcnt(0)\n"
"v_max_u32_e32 v239, v104, v105\n"
"v_max3_u32 v239, v239, v106, v107\n"
"v_max3_u32 v239, v239, v112, v113\n"
"v_max3_u32 v239, v239, v114, v115\n"
"v_max3_u32 v239, v239, v116, v117\n"
"v_max3_u32 v239, v239, v118, v119\n"
"v_max3_u32 v239, v239, v144, v145\n"
"v_max3_u32 v239, v239, v146, v147\n"
"v_max3_u32 v239, v239, v152, v153\n"
"v_max3_u32 v239, v239, v154, v155\n"
"v_max3_u32 v239, v239, v172, v173\n"
"v_max3_u32 v239, v239, v174, v175\n"
"v_max3_u32 v239, v239, v176, v177\n"
"v_max3_u32 v239, v239, v178, v179\n"
"v_max3_u32 v239, v239, v108, v109\n"
"v_max3_u32 v239, v239, v110, v111\n"
"v_cmp_eq_u32_e32 vcc, -1, v239\n"
"s_cbranch_vccz .Lmk_p75k\n"
"s_sub_u32 s1, s1, 1\n"
"s_cbranch_scc0 .Lmk_p75l\n"
".Lmk_p75k:\n"
"v_mfma_f32_16x16x32_bf16 v[72:75], a[128:131], v[104:107], 0\n"
"v_mfma_f32_16x16x32_bf16 v[72:75], a[132:135], v[112:115], v[72:75]\n"
"v_mfma_f32_16x16x32_bf16 v[72:75], a[136:139], v[116:119], v[72:75]\n"
"v_mfma_f32_16x16x32_bf16 v[72:75], a[140:143], v[144:147], v[72:75]\n"
"v_mfma_f32_16x16x32_bf16 v[72:75], a[144:147], v[152:155], v[72:75]\n"
"v_mfma_f32_16x16x32_bf16 v[72:75], a[148:151], v[172:175], v[72:75]\n"
"v_mfma_f32_16x16x32_bf16 v[72:75], a[152:155], v[176:179], v[72:75]\n"
"v_mfma_f32_16x16x32_bf16 v[72:75], a[156:159], v[108:111], v[72:75]\n"
"v_mfma_f32_16x16x32_bf16 v[92:95], a[160:163], v[104:107], 0\n"
"v_mfma_f32_16x16x32_bf16 v[92:95], a[164:167], v[112:115], v[92:95]\n"
"v_mfma_f32_16x16x32_bf16 v[92:95], a[168:171], v[116:119], v[92:95]\n"
"v_mfma_f32_16x16x32_bf16 v[92:95], a[172:175], v[144:147], v[92:95]\n"
"v_mfma_f32_16x16x32_bf16 v[92:95], a[176:179], v[152:155], v[92:95]\n"
"v_mfma_f32_16x16x32_bf16 v[92:95], a[180:183], v[172:175], v[92:95]\n"
"s_nop 7\n"
"s_branch .Lmk_p75r\n"
".Lmk_p76s:\n"
";\n"
"s_mov_b32 s1, 0x10000\n"
".Lmk_p76l:\n"
"s_add_u32 s97, s59, 0x400000\n"
"buffer_load_dwordx4 v[24:27], v0, s[4:7], s97 offen sc1\n"
"buffer_load_dwordx4 v[124:127], v0, s[4:7], s97 offen offset:1024 sc1\n"
"buffer_load_dwordx4 v[84:87], v0, s[4:7], s97 offen offset:2048 sc1\n"
"buffer_load_dwordx4 v[80:83], v0, s[4:7], s97 offen offset:3072 sc1\n"
"buffer_load_dwordx4 v[128:131], v1, s[4:7], s97 offen sc1\n"
"buffer_load_dwordx4 v[100:103], v1, s[4:7], s97 offen offset:1024 sc1\n"
"buffer_load_dwordx4 v[96:99], v1, s[4:7], s97 offen offset:2048 sc1\n"
"buffer_load_dwordx4 v[120:123], v1, s[4:7], s97 offen offset:3072 sc1\n"
"s_add_u32 s97, s52, 0x840000\n"
"buffer_load_dwordx4 v[156:159], v0, s[8:11], s97 offen sc1\n"
"buffer_load_dwordx4 v[92:95], v0, s[8:11], s97 offen offset:1024 sc1\n"
"buffer_load_dwordx4 v[104:107], v0, s[8:11], s97 offen offset:2048 sc1\n"
"buffer_load_dwordx4 v[112:115], v0, s[8:11], s97 offen offset:3072 sc1\n"
"buffer_load_dwordx4 v[116:119], v1, s[8:11], s97 offen sc1\n"
"buffer_load_dwordx4 v[72:75], v1, s[8:11], s97 offen offset:1024 sc1\n"
"buffer_load_dwordx4 v[164:167], v1, s[8:11], s97 offen offset:2048 sc1\n"
"buffer_load_dwordx4 v[172:175], v1, s[8:11], s97 offen offset:3072 sc1\n"
"s_add_u32 s97, s53, 0x840000\n"
"buffer_load_dwordx4 v[176:179], v0, s[8:11], s97 offen sc1\n"
"buffer_load_dwordx4 v[180:183], v0, s[8:11], s97 offen offset:1024 sc1\n"
"buffer_load_dwordx4 v[188:191], v0, s[8:11], s97 offen offset:2048 sc1\n"
"buffer_load_dwordx4 v[192:195], v0, s[8:11], s97 offen offset:3072 sc1\n"
"s_waitcnt vmcnt(0)\n"
"v_max_u32_e32 v255, v24, v25\n"
"v_max3_u32 v255, v255, v26, v27\n"
"v_max3_u32 v255, v255, v124, v125\n"
"v_max3_u32 v255, v255, v126, v127\n"
"v_max3_u32 v255, v255, v84, v85\n"
"v_max3_u32 v255, v255, v86, v87\n"
"v_max3_u32 v255, v255, v80, v81\n"
"v_max3_u32 v255, v255, v82, v83\n"
"v_max3_u32 v255, v255, v128, v129\n"
"v_max3_u32 v255, v255, v130, v131\n"
"v_max3_u32 v255, v255, v100, v101\n"
"v_max3_u32 v255, v255, v102, v103\n"
"v_max3_u32 v255, v255, v96, v97\n"
"v_max3_u32 v255, v255, v98, v99\n"
"v_max3_u32 v255, v255, v120, v121\n"
"v_max3_u32 v255, v255, v122, v123\n"
"v_cmp_eq_u32_e32 vcc, -1, v255\n"
"s_cbranch_vccz .Lmk_p76k\n"
"s_sub_u32 s1, s1, 1\n"
"s_cbranch_scc0 .Lmk_p76l\n"
".Lmk_p76k:\n"
"v_mfma_f32_16x16x32_bf16 v[152:155], a[128:131], v[24:27], v[148:151]\n"
"v_mfma_f32_16x16x32_bf16 v[152:155], a[132:135], v[124:127], v[152:155]\n"
"v_mfma_f32_16x16x32_bf16 v[152:155], a[136:139], v[84:87], v[152:155]\n"
"v_mfma_f32_16x16x32_bf16 v[152:155], a[140:143], v[80:83], v[152:155]\n"
"v_mfma_f32_16x16x32_bf16 v[152:155], a[144:147], v[128:131], v[152:155]\n"
"v_mfma_f32_16x16x32_bf16 v[152:155], a[148:151], v[100:103], v[152:155]\n"
"v_mfma_f32_16x16x32_bf16 v[152:155], a[152:155], v[96:99], v[152:155]\n"
"v_mfma_f32_16x16x32_bf16 v[152:155], a[156:159], v[120:123], v[152:155]\n"
"v_mfma_f32_16x16x32_bf16 v[184:187], a[160:163], v[24:27], v[160:163]\n"
"v_mfma_f32_16x16x32_bf16 v[184:187], a[164:167], v[124:127], v[184:187]\n"
"v_mfma_f32_16x16x32_bf16 v[184:187], a[168:171], v[84:87], v[184:187]\n"
"v_mfma_f32_16x16x32_bf16 v[184:187], a[172:175], v[80:83], v[184:187]\n"
"v_mfma_f32_16x16x32_bf16 v[184:187], a[176:179], v[128:131], v[184:187]\n"
"v_mfma_f32_16x16x32_bf16 v[184:187], a[180:183], v[100:103], v[184:187]\n"
"s_nop 7\n"
"s_branch .Lmk_p76r\n"
".Lmk_p81s:\n"
";\n"
"s_mov_b32 s1, 0x10000\n"
".Lmk_p81l:\n"
"s_add_u32 s96, s52, 0x40000\n"
"buffer_load_dwordx4 v[156:159], v0, s[4:7], s96 offen sc1\n"
"buffer_load_dwordx4 v[92:95], v0, s[4:7], s96 offen offset:1024 sc1\n"
"buffer_load_dwordx4 v[104:107], v0, s[4:7], s96 offen offset:2048 sc1\n"
"buffer_load_dwordx4 v[112:115], v0, s[4:7], s96 offen offset:3072 sc1\n"
"buffer_load_dwordx4 v[116:119], v1, s[4:7], s96 offen sc1\n"
"buffer_load_dwordx4 v[72:75], v1, s[4:7], s96 offen offset:1024 sc1\n"
"buffer_load_dwordx4 v[164:167], v1, s[4:7], s96 offen offset:2048 sc1\n"
"buffer_load_dwordx4 v[172:175], v1, s[4:7], s96 offen offset:3072 sc1\n"
"s_add_u32 s96, s53, 0x40000\n"
"buffer_load_dwordx4 v[176:179], v0, s[4:7], s96 offen sc1\n"
"buffer_load_dwordx4 v[180:183], v0, s[4:7], s96 offen offset:1024 sc1\n"
"buffer_load_dwordx4 v[188:191], v0, s[4:7], s96 offen offset:2048 sc1\n"
"buffer_load_dwordx4 v[192:195], v0, s[4:7], s96 offen offset:3072 sc1\n"
"buffer_load_dwordx4 v[160:163], v1, s[4:7], s96 offen sc1\n"
"buffer_load_dwordx4 v[204:207], v1, s[4:7], s96 offen offset:1024 sc1\n"
"buffer_load_dwordx4 v[208:211], v1, s[4:7], s96 offen offset:2048 sc1\n"
"buffer_load_dwordx4 v[212:215], v1, s[4:7], s96 offen offset:3072 sc1\n"
"s_add_u32 s96, s54, 0x40000\n"
"buffer_load_dwordx4 v[80:83], v0, s[4:7], s96 offen sc1\n"
"buffer_load_dwordx4 v[96:99], v0, s[4:7], s96 offen offset:1024 sc1\n"
"s_waitcnt vmcnt(0)\n"
"v_max_u32_e32 v252, v156, v157\n"
"v_max3_u32 v252, v252, v158, v159\n"
"v_max3_u32 v252, v252, v92, v93\n"
"v_max3_u32 v252, v252, v94, v95\n"
"v_max3_u32 v252, v252, v104, v105\n"
"v_max3_u32 v252, v252, v106, v107\n"
"v_max3_u32 v252, v252, v112, v113\n"
"v_max3_u32 v252, v252, v114, v115\n"
"v_max3_u32 v252, v252, v116, v117\n"
"v_max3_u32 v252, v252, v118, v119\n"
"v_max3_u32 v252, v252, v72, v73\n"
"v_max3_u32 v252, v252, v74, v75\n"
"v_max3_u32 v252, v252, v164, v165\n"
"v_max3_u32 v252, v252, v166, v167\n"
"v_max3_u32 v252, v252, v172, v173\n"
"v_max3_u32 v252, v252, v174, v175\n"
"v_cmp_eq_u32_e32 vcc, -1, v252\n"
"s_cbranch_vccz .Lmk_p81k\n"
"s_sub_u32 s1, s1, 1\n"
"s_cbranch_scc0 .Lmk_p81l\n"
".Lmk_p81k:\n"
"v_mfma_f32_16x16x32_bf16 v[24:27], a[0:3], v[156:159], 0\n"
"v_mfma_f32_16x16x32_bf16 v[24:27], a[4:7], v[92:95], v[24:27]\n"
"v_mfma_f32_16x16x32_bf16 v[24:27], a[8:11], v[104:107], v[24:27]\n"
"v_mfma_f32_16x16x32_bf16 v[24:27], a[12:15], v[112:115], v[24:27]\n"
"v_mfma_f32_16x16x32_bf16 v[24:27], a[16:19], v[116:119], v[24:27]\n"
"v_mfma_f32_16x16x32_bf16 v[24:27], a[20:23], v[72:75], v[24:27]\n"
"v_mfma_f32_16x16x32_bf16 v[24:27], a[24:27], v[164:167], v[24:27]\n"
"v_mfma_f32_16x16x32_bf16 v[24:27], a[28:31], v[172:175], v[24:27]\n"
"v_mfma_f32_16x16x32_bf16 v[100:103], a[32:35], v[156:159], 0\n"
"v_mfma_f32_16x16x32_bf16 v[100:103], a[36:39], v[92:95], v[100:103]\n"
"v_mfma_f32_16x16x32_bf16 v[100:103], a[40:43], v[104:107], v[100:103]\n"
"v_mfma_f32_16x16x32_bf16 v[100:103], a[44:47], v[112:115], v[100:103]\n"
"v_mfma_f32_16x16x32_bf16 v[100:103], a[48:51], v[116:119], v[100:103]\n"
"v_mfma_f32_16x16x32_bf16 v[100:103], a[52:55], v[72:75], v[100:103]\n"
"s_nop 7\n"
"s_branch .Lmk_p81r\n"
".Lmk_p82s:\n"
";\n"
"s_mov_b32 s1, 0x10000\n"
".Lmk_p82l:\n"
"s_add_u32 s97, s53, 0x40000\n"
"buffer_load_dwordx4 v[176:179], v0, s[4:7], s97 offen sc1\n"
"buffer_load_dwordx4 v[180:183], v0, s[4:7], s97 offen offset:1024 sc1\n"
"buffer_load_dwordx4 v[188:191], v0, s[4:7], s97 offen offset:2048 sc1\n"
"buffer_load_dwordx4 v[192:195], v0, s[4:7], s97 offen offset:3072 sc1\n"
"buffer_load_dwordx4 v[160:163], v1, s[4:7], s97 offen sc1\n"
"buffer_load_dwordx4 v[204:207], v1, s[4:7], s97 offen offset:1024 sc1\n"
"buffer_load_dwordx4 v[208:211], v1, s[4:7], s97 offen offset:2048 sc1\n"
"buffer_load_dwordx4 v[212:215], v1, s[4:7], s97 offen offset:3072 sc1\n"
"s_add_u32 s97, s54, 0x40000\n"
"buffer_load_dwordx4 v[80:83], v0, s[4:7], s97 offen sc1\n"
"buffer_load_dwordx4 v[96:99], v0, s[4:7], s97 offen offset:1024 sc1\n"
"buffer_load_dwordx4 v[120:123], v0, s[4:7], s97 offen offset:2048 sc1\n"
"buffer_load_dwordx4 v[124:127], v0, s[4:7], s97 offen offset:3072 sc1\n"
"buffer_load_dwordx4 v[100:103], v1, s[4:7], s97 offen sc1\n"
"buffer_load_dwordx4 v[144:147], v1, s[4:7], s97 offen offset:1024 sc1\n"
"buffer_load_dwordx4 v[108:111], v1, s[4:7], s97 offen offset:2048 sc1\n"
"buffer_load_dwordx4 v[152:155], v1, s[4:7], s97 offen offset:3072 sc1\n"
"s_add_u32 s97, s55, 0x40000\n"
"buffer_load_dwordx4 v[84:87], v0, s[4:7], s97 offen sc1\n"
"buffer_load_dwordx4 v[92:95], v0, s[4:7], s97 offen offset:1024 sc1\n"
"s_waitcnt vmcnt(0)\n"
"v_max_u32_e32 v247, v176, v177\n"
"v_max3_u32 v247, v247, v178, v179\n"
"v_max3_u32 v247, v247, v180, v181\n"
"v_max3_u32 v247, v247, v182, v183\n"
"v_max3_u32 v247, v247, v188, v189\n"
"v_max3_u32 v247, v247, v190, v191\n"
"v_max3_u32 v247, v247, v192, v193\n"
"v_max3_u32 v247, v247, v194, v195\n"
"v_max3_u32 v247, v247, v160, v161\n"
"v_max3_u32 v247, v247, v162, v163\n"
"v_max3_u32 v247, v247, v204, v205\n"
"v_max3_u32 v247, v247, v206, v207\n"
"v_max3_u32 v247, v247, v208, v209\n"
"v_max3_u32 v247, v247, v210, v211\n"
"v_max3_u32 v247, v247, v212, v213\n"
"v_max3_u32 v247, v247, v214, v215\n"
"v_cmp_eq_u32_e32 vcc, -1, v247\n"
"s_cbranch_vccz .Lmk_p82k\n"
"s_sub_u32 s1, s1, 1\n"
"s_cbranch_scc0 .Lmk_p82l\n"
".Lmk_p82k:\n"
"v_mfma_f32_16x16x32_bf16 v[72:75], a[0:3], v[176:179], 0\n"
"v_mfma_f32_16x16x32_bf16 v[72:75], a[4:7], v[180:183], v[72:75]\n"
"v_mfma_f32_16x16x32_bf16 v[72:75], a[8:11], v[188:191], v[72:75]\n"
"v_mfma_f32_16x16x32_bf16 v[72:75], a[12:15], v[192:195], v[72:75]\n"
"v_mfma_f32_16x16x32_bf16 v[72:75], a[16:19], v[160:163], v[72:75]\n"
"v_mfma_f32_16x16x32_bf16 v[72:75], a[20:23], v[204:207], v[72:75]\n"
"v_mfma_f32_16x16x32_bf16 v[72:75], a[24:27], v[208:211], v[72:75]\n"
"v_mfma_f32_16x16x32_bf16 v[72:75], a[28:31], v[212:215], v[72:75]\n"
"v_mfma_f32_16x16x32_bf16 v[104:107], a[32:35], v[176:179], 0\n"
"v_mfma_f32_16x16x32_bf16 v[104:107], a[36:39], v[180:183], v[104:107]\n"
"v_mfma_f32_16x16x32_bf16 v[104:107], a[40:43], v[188:191], v[104:107]\n"
"v_mfma_f32_16x16x32_bf16 v[104:107], a[44:47], v[192:195], v[104:107]\n"
"v_mfma_f32_16x16x32_bf16 v[104:107], a[48:51], v[160:163], v[104:107]\n"
"v_mfma_f32_16x16x32_bf16 v[104:107], a[52:55], v[204:207], v[104:107]\n"
"s_nop 7\n"
"s_branch .Lmk_p82r\n"
".Lmk_m85s:\n"
";\n"
"s_mov_b32 s1, 0x10000\n"
".Lmk_m85l:\n"
"s_add_u32 s98, s62, 0x4000\n"
"buffer_load_dword v255, v10, s[4:7], s98 offen sc1\n"
"buffer_load_dword v254, v10, s[4:7], s98 offen offset:256 sc1\n"
"buffer_load_dword v253, v10, s[4:7], s98 offen offset:512 sc1\n"
"buffer_load_dword v252, v10, s[4:7], s98 offen offset:768 sc1\n"
"buffer_load_dword v251, v10, s[4:7], s98 offen offset:1024 sc1\n"
"buffer_load_dword v250, v10, s[4:7], s98 offen offset:1280 sc1\n"
"buffer_load_dword v249, v10, s[4:7], s98 offen offset:1536 sc1\n"
"buffer_load_dword v248, v10, s[4:7], s98 offen offset:1792 sc1\n"
"s_waitcnt vmcnt(0)\n"
"v_max_u32_e32 v247, v255, v254\n"
"v_max3_u32 v247, v247, v253, v252\n"
"v_max3_u32 v247, v247, v251, v250\n"
"v_max3_u32 v247, v247, v249, v248\n"
"v_cmp_eq_u32_e32 vcc, -1, v247\n"
"s_cbranch_vccz .Lmk_m85k\n"
"s_sub_u32 s1, s1, 1\n"
"s_cbranch_scc0 .Lmk_m85l\n"
".Lmk_m85k:\n"
"v_add_f32_e32 v255, v255, v251\n"
"v_add_f32_e32 v254, v254, v250\n"
"v_add_f32_e32 v253, v253, v249\n"
"v_add_f32_e32 v252, v252, v248\n"
"v_add_f32_e32 v255, v255, v253\n"
"v_add_f32_e32 v254, v254, v252\n"
"v_add_f32_e32 v255, v255, v254\n"
"s_nop 1\n"
"s_branch .Lmk_m85r\n"
".Lmk_p83s:\n"
";\n"
"s_mov_b32 s1, 0x10000\n"
".Lmk_p83l:\n"
"s_add_u32 s97, s54, 0x40000\n"
"buffer_load_dwordx4 v[80:83], v0, s[4:7], s97 offen sc1\n"
"buffer_load_dwordx4 v[96:99], v0, s[4:7], s97 offen offset:1024 sc1\n"
"buffer_load_dwordx4 v[120:123], v0, s[4:7], s97 offen offset:2048 sc1\n"
"buffer_load_dwordx4 v[124:127], v0, s[4:7], s97 offen offset:3072 sc1\n"
"buffer_load_dwordx4 v[100:103], v1, s[4:7], s97 offen sc1\n"
"buffer_load_dwordx4 v[144:147], v1, s[4:7], s97 offen offset:1024 sc1\n"
"buffer_load_dwordx4 v[108:111], v1, s[4:7], s97 offen offset:2048 sc1\n"
"buffer_load_dwordx4 v[152:155], v1, s[4:7], s97 offen offset:3072 sc1\n"
"s_add_u32 s97, s55, 0x40000\n"
"buffer_load_dwordx4 v[84:87], v0, s[4:7], s97 offen sc1\n"
"buffer_load_dwordx4 v[92:95], v0, s[4:7], s97 offen offset:1024 sc1\n"
"buffer_load_dwordx4 v[112:115], v0, s[4:7], s97 offen offset:2048 sc1\n"
"buffer_load_dwordx4 v[116:119], v0, s[4:7], s97 offen offset:3072 sc1\n"
"buffer_load_dwordx4 v[104:107], v1, s[4:7], s97 offen sc1\n"
"buffer_load_dwordx4 v[148:151], v1, s[4:7], s97 offen offset:1024 sc1\n"
"buffer_load_dwordx4 v[156:159], v1, s[4:7], s97 offen offset:2048 sc1\n"
"buffer_load_dwordx4 v[164:167], v1, s[4:7], s97 offen offset:3072 sc1\n"
"s_add_u32 s97, s52, 0x48000\n"
"buffer_load_dwordx4 v[160:163], v0, s[4:7], s97 offen sc1\n"
"s_waitcnt vmcnt(0)\n"
"v_max_u32_e32 v252, v80, v81\n"
"v_max3_u32 v252, v252, v82, v83\n"
"v_max3_u32 v252, v252, v96, v97\n"
"v_max3_u32 v252, v252, v98, v99\n"
"v_max3_u32 v252, v252, v120, v121\n"
"v_max3_u32 v252, v252, v122, v123\n"
"v_max3_u32 v252, v252, v124, v125\n"
"v_max3_u32 v252, v252, v126, v127\n"
"v_max3_u32 v252, v252, v100, v101\n"
"v_max3_u32 v252, v252, v102, v103\n"
"v_max3_u32 v252, v252, v144, v145\n"
"v_max3_u32 v252, v252, v146, v147\n"
"v_max3_u32 v252, v252, v108, v109\n"
"v_max3_u32 v252, v252, v110, v111\n"
"v_max3_u32 v252, v252, v152, v153\n"
"v_max3_u32 v252, v252, v154, v155\n"
"v_cmp_eq_u32_e32 vcc, -1, v252\n"
"s_cbranch_vccz .Lmk_p83k\n"
"s_sub_u32 s1, s1, 1\n"
"s_cbranch_scc0 .Lmk_p83l\n"
".Lmk_p83k:\n"
"v_mfma_f32_16x16x32_bf16 v[72:75], a[0:3], v[80:83], 0\n"
"v_mfma_f32_16x16x32_bf16 v[72:75], a[4:7], v[96:99], v[72:75]\n"
"v_mfma_f32_16x16x32_bf16 v[72:75], a[8:11], v[120:123], v[72:75]\n"
"v_mfma_f32_16x16x32_bf16 v[72:75], a[12:15], v[124:127], v[72:75]\n"
"v_mfma_f32_16x16x32_bf16 v[72:75], a[16:19], v[100:103], v[72:75]\n"
"v_mfma_f32_16x16x32_bf16 v[72:75], a[20:23], v[144:147], v[72:75]\n"
"v_mfma_f32_16x16x32_bf16 v[72:75], a[24:27], v[108:111], v[72:75]\n"
"v_mfma_f32_16x16x32_bf16 v[72:75], a[28:31], v[152:155], v[72:75]\n"
"v_mfma_f32_16x16x32_bf16 v[128:131], a[32:35], v[80:83], 0\n"
"v_mfma_f32_16x16x32_bf16 v[128:131], a[36:39], v[96:99], v[128:131]\n"
"v_mfma_f32_16x16x32_bf16 v[128:131], a[40:43], v[120:123], v[128:131]\n"
"v_mfma_f32_16x16x32_bf16 v[128:131], a[44:47], v[124:127], v[128:131]\n"
"v_mfma_f32_16x16x32_bf16 v[128:131], a[48:51], v[100:103], v[128:131]\n"
"v_mfma_f32_16x16x32_bf16 v[128:131], a[52:55], v[144:147], v[128:131]\n"
"s_nop 7\n"
"s_branch .Lmk_p83r\n"
".Lmk_p84s:\n"
";\n"
"s_mov_b32 s1, 0x10000\n"
".Lmk_p84l:\n"
"s_add_u32 s97, s55, 0x40000\n"
"buffer_load_dwordx4 v[84:87], v0, s[4:7], s97 offen sc1\n"
"buffer_load_dwordx4 v[92:95], v0, s[4:7], s97 offen offset:1024 sc1\n"
"buffer_load_dwordx4 v[112:115], v0, s[4:7], s97 offen offset:2048 sc1\n"
"buffer_load_dwordx4 v[116:119], v0, s[4:7], s97 offen offset:3072 sc1\n"
"buffer_load_dwordx4 v[104:107], v1, s[4:7], s97 offen sc1\n"
"buffer_load_dwordx4 v[148:151], v1, s[4:7], s97 offen offset:1024 sc1\n"
"buffer_load_dwordx4 v[156:159], v1, s[4:7], s97 offen offset:2048 sc1\n"
"buffer_load_dwordx4 v[164:167], v1, s[4:7], s97 offen offset:3072 sc1\n"
"s_add_u32 s97, s52, 0x48000\n"
"buffer_load_dwordx4 v[160:163], v0, s[4:7], s97 offen sc1\n"
"buffer_load_dwordx4 v[72:75], v0, s[4:7], s97 offen offset:1024 sc1\n"
"buffer_load_dwordx4 v[192:195], v0, s[4:7], s97 offen offset:2048 sc1\n"
"buffer_load_dwordx4 v[176:179], v0, s[4:7], s97 offen offset:3072 sc1\n"
"buffer_load_dwordx4 v[172:175], v1, s[4:7], s97 offen sc1\n"
"buffer_load_dwordx4 v[188:191], v1, s[4:7], s97 offen offset:1024 sc1\n"
"buffer_load_dwordx4 v[80:83], v1, s[4:7], s97 offen offset:2048 sc1\n"
"buffer_load_dwordx4 v[100:103], v1, s[4:7], s97 offen offset:3072 sc1\n"
"s_add_u32 s97, s53, 0x48000\n"
"buffer_load_dwordx4 v[196:199], v0, s[4:7], s97 offen sc1\n"
"buffer_load_dwordx4 v[128:131], v0, s[4:7], s97 offen offset:1024 sc1\n"
"s_waitcnt vmcnt(0)\n"
"v_max_u32_e32 v252, v84, v85\n"
"v_max3_u32 v252, v252, v86, v87\n"
"v_max3_u32 v252, v252, v92, v93\n"
"v_max3_u32 v252, v252, v94, v95\n"
"v_max3_u32 v252, v252, v112, v113\n"
"v_max3_u32 v252, v252, v114, v115\n"
"v_max3_u32 v252, v252, v116, v117\n"
"v_max3_u32 v252, v252, v118, v119\n"
"v_max3_u32 v252, v252, v104, v105\n"
"v_max3_u32 v252, v252, v106, v107\n"
"v_max3_u32 v252, v252, v148, v149\n"
"v_max3_u32 v252, v252, v150, v151\n"
"v_max3_u32 v252, v252, v156, v157\n"
"v_max3_u32 v252, v252, v158, v159\n"
"v_max3_u32 v252, v252, v164, v165\n"
"v_max3_u32 v252, v252, v166, v167\n"
"v_cmp_eq_u32_e32 vcc, -1, v252\n"
"s_cbranch_vccz .Lmk_p84k\n"
"s_sub_u32 s1, s1, 1\n"
"s_cbranch_scc0 .Lmk_p84l\n"
".Lmk_p84k:\n"
"v_mfma_f32_16x16x32_bf16 v[152:155], a[0:3], v[84:87], v[168:171]\n"
"v_mfma_f32_16x16x32_bf16 v[152:155], a[4:7], v[92:95], v[152:155]\n"
"v_mfma_f32_16x16x32_bf16 v[152:155], a[8:11], v[112:115], v[152:155]\n"
"v_mfma_f32_16x16x32_bf16 v[152:155], a[12:15], v[116:119], v[152:155]\n"
"v_mfma_f32_16x16x32_bf16 v[152:155], a[16:19], v[104:107], v[152:155]\n"
"v_mfma_f32_16x16x32_bf16 v[152:155], a[20:23], v[148:151], v[152:155]\n"
"v_mfma_f32_16x16x32_bf16 v[152:155], a[24:27], v[156:159], v[152:155]\n"
"v_mfma_f32_16x16x32_bf16 v[152:155], a[28:31], v[164:167], v[152:155]\n"
"v_mfma_f32_16x16x32_bf16 v[204:207], a[32:35], v[84:87], v[184:187]\n"
"v_mfma_f32_16x16x32_bf16 v[204:207], a[36:39], v[92:95], v[204:207]\n"
"v_mfma_f32_16x16x32_bf16 v[204:207], a[40:43], v[112:115], v[204:207]\n"
"v_mfma_f32_16x16x32_bf16 v[204:207], a[44:47], v[116:119], v[204:207]\n"
"v_mfma_f32_16x16x32_bf16 v[204:207], a[48:51], v[104:107], v[204:207]\n"
"v_mfma_f32_16x16x32_bf16 v[204:207], a[52:55], v[148:151], v[204:207]\n"
"s_nop 7\n"
"s_branch .Lmk_p84r\n"
".Lmk_p86s:\n"
";\n"
"s_mov_b32 s1, 0x10000\n"
".Lmk_p86l:\n"
"s_add_u32 s95, s52, 0x48000\n"
"buffer_load_dwordx4 v[160:163], v0, s[4:7], s95 offen sc1\n"
"buffer_load_dwordx4 v[72:75], v0, s[4:7], s95 offen offset:1024 sc1\n"
"buffer_load_dwordx4 v[192:195], v0, s[4:7], s95 offen offset:2048 sc1\n"
"buffer_load_dwordx4 v[176:179], v0, s[4:7], s95 offen offset:3072 sc1\n"
"buffer_load_dwordx4 v[172:175], v1, s[4:7], s95 offen sc1\n"
"buffer_load_dwordx4 v[188:191], v1, s[4:7], s95 offen offset:1024 sc1\n"
"buffer_load_dwordx4 v[80:83], v1, s[4:7], s95 offen offset:2048 sc1\n"
"buffer_load_dwordx4 v[100:103], v1, s[4:7], s95 offen offset:3072 sc1\n"
"s_add_u32 s95, s53, 0x48000\n"
"buffer_load_dwordx4 v[196:199], v0, s[4:7], s95 offen sc1\n"
"buffer_load_dwordx4 v[128:131], v0, s[4:7], s95 offen offset:1024 sc1\n"
"buffer_load_dwordx4 v[168:171], v0, s[4:7], s95 offen offset:2048 sc1\n"
"buffer_load_dwordx4 v[120:123], v0, s[4:7], s95 offen offset:3072 sc1\n"
"buffer_load_dwordx4 v[124:127], v1, s[4:7], s95 offen sc1\n"
"buffer_load_dwordx4 v[180:183], v1, s[4:7], s95 offen offset:1024 sc1\n"
"buffer_load_dwordx4 v[208:211], v1, s[4:7], s95 offen offset:2048 sc1\n"
"buffer_load_dwordx4 v[152:155], v1, s[4:7], s95 offen offset:3072 sc1\n"
"s_add_u32 s95, s54, 0x48000\n"
"buffer_load_dwordx4 v[92:95], v0, s[4:7], s95 offen sc1\n"
"buffer_load_dwordx4 v[104:107], v0, s[4:7], s95 offen offset:1024 sc1\n"
"s_waitcnt vmcnt(0)\n"
"v_max_u32_e32 v252, v160, v161\n"
"v_max3_u32 v252, v252, v162, v163\n"
"v_max3_u32 v252, v252, v72, v73\n"
"v_max3_u32 v252, v252, v74, v75\n"
"v_max3_u32 v252, v252, v192, v193\n"
"v_max3_u32 v252, v252, v194, v195\n"
"v_max3_u32 v252, v252, v176, v177\n"
"v_max3_u32 v252, v252, v178, v179\n"
"v_max3_u32 v252, v252, v172, v173\n"
"v_max3_u32 v252, v252, v174, v175\n"
"v_max3_u32 v252, v252, v188, v189\n"
"v_max3_u32 v252, v252, v190, v191\n"
"v_max3_u32 v252, v252, v80, v81\n"
"v_max3_u32 v252, v252, v82, v83\n"
"v_max3_u32 v252, v252, v100, v101\n"
"v_max3_u32 v252, v252, v102, v103\n"
"v_cmp_eq_u32_e32 vcc, -1, v252\n"
"s_cbranch_vccz .Lmk_p86k\n"
"s_sub_u32 s1, s1, 1\n"
"s_cbranch_scc0 .Lmk_p86l\n"
".Lmk_p86k:\n"
"v_mfma_f32_16x16x32_bf16 v[84:87], a[0:3], v[160:163], 0\n"
"v_mfma_f32_16x16x32_bf16 v[84:87], a[4:7], v[72:75], v[84:87]\n"
"v_mfma_f32_16x16x32_bf16 v[84:87], a[8:11], v[192:195], v[84:87]\n"
"v_mfma_f32_16x16x32_bf16 v[84:87], a[12:15], v[176:179], v[84:87]\n"
"v_mfma_f32_16x16x32_bf16 v[84:87], a[16:19], v[172:175], v[84:87]\n"
"v_mfma_f32_16x16x32_bf16 v[84:87], a[20:23], v[188:191], v[84:87]\n"
"v_mfma_f32_16x16x32_bf16 v[84:87], a[24:27], v[80:83], v[84:87]\n"
"v_mfma_f32_16x16x32_bf16 v[84:87], a[28:31], v[100:103], v[84:87]\n"
"v_mfma_f32_16x16x32_bf16 v[112:115], a[32:35], v[160:163], 0\n"
"v_mfma_f32_16x16x32_bf16 v[112:115], a[36:39], v[72:75], v[112:115]\n"
"v_mfma_f32_16x16x32_bf16 v[112:115], a[40:43], v[192:195], v[112:115]\n"
"v_mfma_f32_16x16x32_bf16 v[112:115], a[44:47], v[176:179], v[112:115]\n"
"v_mfma_f32_16x16x32_bf16 v[112:115], a[48:51], v[172:175], v[112:115]\n"
"v_mfma_f32_16x16x32_bf16 v[112:115], a[52:55], v[188:191], v[112:115]\n"
"s_nop 7\n"
"s_branch .Lmk_p86r\n"
".Lmk_p87s:\n"
";\n"
"s_mov_b32 s1, 0x10000\n"
".Lmk_p87l:\n"
"s_add_u32 s95, s53, 0x48000\n"
"buffer_load_dwordx4 v[196:199], v0, s[4:7], s95 offen sc1\n"
"buffer_load_dwordx4 v[128:131], v0, s[4:7], s95 offen offset:1024 sc1\n"
"buffer_load_dwordx4 v[168:171], v0, s[4:7], s95 offen offset:2048 sc1\n"
"buffer_load_dwordx4 v[120:123], v0, s[4:7], s95 offen offset:3072 sc1\n"
"buffer_load_dwordx4 v[124:127], v1, s[4:7], s95 offen sc1\n"
"buffer_load_dwordx4 v[180:183], v1, s[4:7], s95 offen offset:1024 sc1\n"
"buffer_load_dwordx4 v[208:211], v1, s[4:7], s95 offen offset:2048 sc1\n"
"buffer_load_dwordx4 v[152:155], v1, s[4:7], s95 offen offset:3072 sc1\n"
"s_add_u32 s95, s54, 0x48000\n"
"buffer_load_dwordx4 v[92:95], v0, s[4:7], s95 offen sc1\n"
"buffer_load_dwordx4 v[104:107], v0, s[4:7], s95 offen offset:1024 sc1\n"
"buffer_load_dwordx4 v[116:119], v0, s[4:7], s95 offen offset:2048 sc1\n"
"buffer_load_dwordx4 v[144:147], v0, s[4:7], s95 offen offset:3072 sc1\n"
"buffer_load_dwordx4 v[112:115], v1, s[4:7], s95 offen sc1\n"
"buffer_load_dwordx4 v[96:99], v1, s[4:7], s95 offen offset:1024 sc1\n"
"buffer_load_dwordx4 v[108:111], v1, s[4:7], s95 offen offset:2048 sc1\n"
"buffer_load_dwordx4 v[156:159], v1, s[4:7], s95 offen offset:3072 sc1\n"
"s_add_u32 s95, s55, 0x48000\n"
"buffer_load_dwordx4 v[80:83], v0, s[4:7], s95 offen sc1\n"
"buffer_load_dwordx4 v[84:87], v0, s[4:7], s95 offen offset:1024 sc1\n"
"s_waitcnt vmcnt(0)\n"
"v_max_u32_e32 v247, v196, v197\n"
"v_max3_u32 v247, v247, v198, v199\n"
"v_max3_u32 v247, v247, v128, v129\n"
"v_max3_u32 v247, v247, v130, v131\n"
"v_max3_u32 v247, v247, v168, v169\n"
"v_max3_u32 v247, v247, v170, v171\n"
"v_max3_u32 v247, v247, v120, v121\n"
"v_max3_u32 v247, v247, v122, v123\n"
"v_max3_u32 v247, v247, v124, v125\n"
"v_max3_u32 v247, v247, v126, v127\n"
"v_max3_u32 v247, v247, v180, v181\n"
"v_max3_u32 v247, v247, v182, v183\n"
"v_max3_u32 v247, v247, v208, v209\n"
"v_max3_u32 v247, v247, v210, v211\n"
"v_max3_u32 v247, v247, v152, v153\n"
"v_max3_u32 v247, v247, v154, v155\n"
"v_cmp_eq_u32_e32 vcc, -1, v247\n"
"s_cbranch_vccz .Lmk_p87k\n"
"s_sub_u32 s1, s1, 1\n"
"s_cbranch_scc0 .Lmk_p87l\n"
".Lmk_p87k:\n"
"v_mfma_f32_16x16x32_bf16 v[72:75], a[0:3], v[196:199], 0\n"
"v_mfma_f32_16x16x32_bf16 v[72:75], a[4:7], v[128:131], v[72:75]\n"
"v_mfma_f32_16x16x32_bf16 v[72:75], a[8:11], v[168:171], v[72:75]\n"
"v_mfma_f32_16x16x32_bf16 v[72:75], a[12:15], v[120:123], v[72:75]\n"
"v_mfma_f32_16x16x32_bf16 v[72:75], a[16:19], v[124:127], v[72:75]\n"
"v_mfma_f32_16x16x32_bf16 v[72:75], a[20:23], v[180:183], v[72:75]\n"
"v_mfma_f32_16x16x32_bf16 v[72:75], a[24:27], v[208:211], v[72:75]\n"
"v_mfma_f32_16x16x32_bf16 v[72:75], a[28:31], v[152:155], v[72:75]\n"
"v_mfma_f32_16x16x32_bf16 v[100:103], a[32:35], v[196:199], 0\n"
"v_mfma_f32_16x16x32_bf16 v[100:103], a[36:39], v[128:131], v[100:103]\n"
"v_mfma_f32_16x16x32_bf16 v[100:103], a[40:43], v[168:171], v[100:103]\n"
"v_mfma_f32_16x16x32_bf16 v[100:103], a[44:47], v[120:123], v[100:103]\n"
"v_mfma_f32_16x16x32_bf16 v[100:103], a[48:51], v[124:127], v[100:103]\n"
"v_mfma_f32_16x16x32_bf16 v[100:103], a[52:55], v[180:183], v[100:103]\n"
"s_nop 7\n"
"s_branch .Lmk_p87r\n"
".Lmk_m90s:\n"
";\n"
"s_mov_b32 s1, 0x10000\n"
".Lmk_m90l:\n"
"s_add_u32 s98, s62, 0x4800\n"
"buffer_load_dword v255, v10, s[4:7], s98 offen sc1\n"
"buffer_load_dword v253, v10, s[4:7], s98 offen offset:256 sc1\n"
"buffer_load_dword v252, v10, s[4:7], s98 offen offset:512 sc1\n"
"buffer_load_dword v251, v10, s[4:7], s98 offen offset:768 sc1\n"
"buffer_load_dword v250, v10, s[4:7], s98 offen offset:1024 sc1\n"
"buffer_load_dword v254, v10, s[4:7], s98 offen offset:1280 sc1\n"
"buffer_load_dword v249, v10, s[4:7], s98 offen offset:1536 sc1\n"
"buffer_load_dword v248, v10, s[4:7], s98 offen offset:1792 sc1\n"
"s_waitcnt vmcnt(0)\n"
"v_max_u32_e32 v245, v255, v253\n"
"v_max3_u32 v245, v245, v252, v251\n"
"v_max3_u32 v245, v245, v250, v254\n"
"v_max3_u32 v245, v245, v249, v248\n"
"v_cmp_eq_u32_e32 vcc, -1, v245\n"
"s_cbranch_vccz .Lmk_m90k\n"
"s_sub_u32 s1, s1, 1\n"
"s_cbranch_scc0 .Lmk_m90l\n"
".Lmk_m90k:\n"
"v_add_f32_e32 v255, v255, v250\n"
"v_add_f32_e32 v253, v253, v254\n"
"v_add_f32_e32 v252, v252, v249\n"
"v_add_f32_e32 v251, v251, v248\n"
"v_add_f32_e32 v255, v255, v252\n"
"v_add_f32_e32 v253, v253, v251\n"
"v_add_f32_e32 v255, v255, v253\n"
"s_nop 1\n"
"s_branch .Lmk_m90r\n"
".Lmk_f77s:\n"
";\n"
"s_mov_b32 s1, 0x10000\n"
".Lmk_f77l:\n"
"buffer_load_dwordx2 v[246:247], v12, s[4:7], s97 offen sc1\n"
"s_waitcnt vmcnt(0)\n"
"v_cmp_ne_u64_e32 vcc, v[246:247], v[18:19]\n"
"s_cbranch_vccz .Lmk_f77r\n"
"s_sub_u32 s1, s1, 1\n"
"s_cbranch_scc0 .Lmk_f77l\n"
"s_branch .Lmk_f77r\n"
".Lmk_p88s:\n"
";\n"
"s_mov_b32 s1, 0x10000\n"
".Lmk_p88l:\n"
"s_add_u32 s97, s54, 0x48000\n"
"buffer_load_dwordx4 v[92:95], v0, s[4:7], s97 offen sc1\n"
"buffer_load_dwordx4 v[104:107], v0, s[4:7], s97 offen offset:1024 sc1\n"
"buffer_load_dwordx4 v[116:119], v0, s[4:7], s97 offen offset:2048 sc1\n"
"buffer_load_dwordx4 v[144:147], v0, s[4:7], s97 offen offset:3072 sc1\n"
"buffer_load_dwordx4 v[112:115], v1, s[4:7], s97 offen sc1\n"
"buffer_load_dwordx4 v[96:99], v1, s[4:7], s97 offen offset:1024 sc1\n"
"buffer_load_dwordx4 v[108:111], v1, s[4:7], s97 offen offset:2048 sc1\n"
"buffer_load_dwordx4 v[156:159], v1, s[4:7], s97 offen offset:3072 sc1\n"
"s_add_u32 s97, s55, 0x48000\n"
"buffer_load_dwordx4 v[80:83], v0, s[4:7], s97 offen sc1\n"
"buffer_load_dwordx4 v[84:87], v0, s[4:7], s97 offen offset:1024 sc1\n"
"buffer_load_dwordx4 v[148:151], v0, s[4:7], s97 offen offset:2048 sc1\n"
"buffer_load_dwordx4 v[160:163], v0, s[4:7], s97 offen offset:3072 sc1\n"
"buffer_load_dwordx4 v[100:103], v1, s[4:7], s97 offen sc1\n"
"buffer_load_dwordx4 v[172:175], v1, s[4:7], s97 offen offset:1024 sc1\n"
"buffer_load_dwordx4 v[176:179], v1, s[4:7], s97 offen offset:2048 sc1\n"
"buffer_load_dwordx4 v[184:187], v1, s[4:7], s97 offen offset:3072 sc1\n"
"s_waitcnt vmcnt(0)\n"
"v_max_u32_e32 v255, v92, v93\n"
"v_max3_u32 v255, v255, v94, v95\n"
"v_max3_u32 v255, v255, v104, v105\n"
"v_max3_u32 v255, v255, v106, v107\n"
"v_max3_u32 v255, v255, v116, v117\n"
"v_max3_u32 v255, v255, v118, v119\n"
"v_max3_u32 v255, v255, v144, v145\n"
"v_max3_u32 v255, v255, v146, v147\n"
"v_max3_u32 v255, v255, v112, v113\n"
"v_max3_u32 v255, v255, v114, v115\n"
"v_max3_u32 v255, v255, v96, v97\n"
"v_max3_u32 v255, v255, v98, v99\n"
"v_max3_u32 v255, v255, v108, v109\n"
"v_max3_u32 v255, v255, v110, v111\n"
"v_max3_u32 v255, v255, v156, v157\n"
"v_max3_u32 v255, v255, v158, v159\n"
"v_cmp_eq_u32_e32 vcc, -1, v255\n"
"s_cbranch_vccz .Lmk_p88k\n"
"s_sub_u32 s1, s1, 1\n"
"s_cbranch_scc0 .Lmk_p88l\n"
".Lmk_p88k:\n"
"v_mfma_f32_16x16x32_bf16 v[24:27], a[0:3], v[92:95], 0\n"
"v_mfma_f32_16x16x32_bf16 v[24:27], a[4:7], v[104:107], v[24:27]\n"
"v_mfma_f32_16x16x32_bf16 v[24:27], a[8:11], v[116:119], v[24:27]\n"
"v_mfma_f32_16x16x32_bf16 v[24:27], a[12:15], v[144:147], v[24:27]\n"
"v_mfma_f32_16x16x32_bf16 v[24:27], a[16:19], v[112:115], v[24:27]\n"
"v_mfma_f32_16x16x32_bf16 v[24:27], a[20:23], v[96:99], v[24:27]\n"
"v_mfma_f32_16x16x32_bf16 v[24:27], a[24:27], v[108:111], v[24:27]\n"
"v_mfma_f32_16x16x32_bf16 v[24:27], a[28:31], v[156:159], v[24:27]\n"
"v_mfma_f32_16x16x32_bf16 v[72:75], a[32:35], v[92:95], 0\n"
"v_mfma_f32_16x16x32_bf16 v[72:75], a[36:39], v[104:107], v[72:75]\n"
"v_mfma_f32_16x16x32_bf16 v[72:75], a[40:43], v[116:119], v[72:75]\n"
"v_mfma_f32_16x16x32_bf16 v[72:75], a[44:47], v[144:147], v[72:75]\n"
"v_mfma_f32_16x16x32_bf16 v[72:75], a[48:51], v[112:115], v[72:75]\n"
"v_mfma_f32_16x16x32_bf16 v[72:75], a[52:55], v[96:99], v[72:75]\n"
"s_nop 7\n"
"s_branch .Lmk_p88r\n"
".Lmk_p89s:\n"
";\n"
"s_mov_b32 s1, 0x10000\n"
".Lmk_p89l:\n"
"s_add_u32 s97, s55, 0x48000\n"
"buffer_load_dwordx4 v[80:83], v0, s[4:7], s97 offen sc1\n"
"buffer_load_dwordx4 v[84:87], v0, s[4:7], s97 offen offset:1024 sc1\n"
"buffer_load_dwordx4 v[148:151], v0, s[4:7], s97 offen offset:2048 sc1\n"
"buffer_load_dwordx4 v[160:163], v0, s[4:7], s97 offen offset:3072 sc1\n"
"buffer_load_dwordx4 v[100:103], v1, s[4:7], s97 offen sc1\n"
"buffer_load_dwordx4 v[172:175], v1, s[4:7], s97 offen offset:1024 sc1\n"
"buffer_load_dwordx4 v[176:179], v1, s[4:7], s97 offen offset:2048 sc1\n"
"buffer_load_dwordx4 v[184:187], v1, s[4:7], s97 offen offset:3072 sc1\n"
"s_waitcnt vmcnt(0)\n"
"v_max_u32_e32 v252, v80, v81\n"
"v_max3_u32 v252, v252, v82, v83\n"
"v_max3_u32 v252, v252, v84, v85\n"
"v_max3_u32 v252, v252, v86, v87\n"
"v_max3_u32 v252, v252, v148, v149\n"
"v_max3_u32 v252, v252, v150, v151\n"
"v_max3_u32 v252, v252, v160, v161\n"
"v_max3_u32 v252, v252, v162, v163\n"
"v_max3_u32 v252, v252, v100, v101\n"
"v_max3_u32 v252, v252, v102, v103\n"
"v_max3_u32 v252, v252, v172, v173\n"
"v_max3_u32 v252, v252, v174, v175\n"
"v_max3_u32 v252, v252, v176, v177\n"
"v_max3_u32 v252, v252, v178, v179\n"
"v_max3_u32 v252, v252, v184, v185\n"
"v_max3_u32 v252, v252, v186, v187\n"
"v_cmp_eq_u32_e32 vcc, -1, v252\n"
"s_cbranch_vccz .Lmk_p89k\n"
"s_sub_u32 s1, s1, 1\n"
"s_cbranch_scc0 .Lmk_p89l\n"
".Lmk_p89k:\n"
"v_mfma_f32_16x16x32_bf16 v[144:147], a[0:3], v[80:83], v[124:127]\n"
"v_mfma_f32_16x16x32_bf16 v[144:147], a[4:7], v[84:87], v[144:147]\n"
"v_mfma_f32_16x16x32_bf16 v[144:147], a[8:11], v[148:151], v[144:147]\n"
"v_mfma_f32_16x16x32_bf16 v[144:147], a[12:15], v[160:163], v[144:147]\n"
"v_mfma_f32_16x16x32_bf16 v[144:147], a[16:19], v[100:103], v[144:147]\n"
"v_mfma_f32_16x16x32_bf16 v[144:147], a[20:23], v[172:175], v[144:147]\n"
"v_mfma_f32_16x16x32_bf16 v[144:147], a[24:27], v[176:179], v[144:147]\n"
"v_mfma_f32_16x16x32_bf16 v[144:147], a[28:31], v[184:187], v[144:147]\n"
"v_mfma_f32_16x16x32_bf16 v[200:203], a[32:35], v[80:83], v[168:171]\n"
"v_mfma_f32_16x16x32_bf16 v[200:203], a[36:39], v[84:87], v[200:203]\n"
"v_mfma_f32_16x16x32_bf16 v[200:203], a[40:43], v[148:151], v[200:203]\n"
"v_mfma_f32_16x16x32_bf16 v[200:203], a[44:47], v[160:163], v[200:203]\n"
"v_mfma_f32_16x16x32_bf16 v[200:203], a[48:51], v[100:103], v[200:203]\n"
"v_mfma_f32_16x16x32_bf16 v[200:203], a[52:55], v[172:175], v[200:203]\n"
"s_nop 7\n"
"s_branch .Lmk_p89r\n"
".Lmk_f78s:\n"
";\n"
"s_mov_b32 s1, 0x10000\n"
".Lmk_f78l:\n"
"buffer_load_dwordx2 v[250:251], off, s[4:7], s98 sc1\n"
"s_waitcnt vmcnt(0)\n"
"v_cmp_ne_u64_e32 vcc, v[250:251], v[18:19]\n"
"s_cbranch_vccz .Lmk_f78r\n"
"s_sub_u32 s1, s1, 1\n"
"s_cbranch_scc0 .Lmk_f78l\n"
"s_branch .Lmk_f78r\n"
".Lmk_x99s:\n"
"s_nop 3\n"
"v_mov_b32_e32 v36, 0xff800000\n"
"v_mov_b32_e32 v37, 0xff800000\n"
"v_mov_b32_e32 v38, 0xff800000\n"
"v_mov_b32_e32 v39, 0xff800000\n"
"v_mov_b32_e32 v60, 0xff800000\n"
"v_mov_b32_e32 v61, 0xff800000\n"
"v_mov_b32_e32 v62, 0xff800000\n"
"v_mov_b32_e32 v63, 0xff800000\n"
"s_nop 1\n"
"s_branch .Lmk_x99r\n"
".Lmk_x100s:\n"
"s_nop 3\n"
"v_mov_b32_e32 v36, 0xff800000\n"
"v_mov_b32_e32 v37, 0xff800000\n"
"v_mov_b32_e32 v38, 0xff800000\n"
"v_mov_b32_e32 v39, 0xff800000\n"
"v_mov_b32_e32 v60, 0xff800000\n"
"v_mov_b32_e32 v61, 0xff800000\n"
"v_mov_b32_e32 v62, 0xff800000\n"
"v_mov_b32_e32 v63, 0xff800000\n"
"s_nop 1\n"
"s_branch .Lmk_x100r\n"
".Lmk_x101s:\n"
"s_nop 3\n"
"v_mov_b32_e32 v36, 0xff800000\n"
"v_mov_b32_e32 v37, 0xff800000\n"
"v_mov_b32_e32 v38, 0xff800000\n"
"v_mov_b32_e32 v39, 0xff800000\n"
"v_mov_b32_e32 v60, 0xff800000\n"
"v_mov_b32_e32 v61, 0xff800000\n"
"v_mov_b32_e32 v62, 0xff800000\n"
"v_mov_b32_e32 v63, 0xff800000\n"
"s_nop 1\n"
"s_branch .Lmk_x101r\n"
".Lmk_x102s:\n"
"s_nop 3\n"
"v_mov_b32_e32 v36, 0xff800000\n"
"v_mov_b32_e32 v37, 0xff800000\n"
"v_mov_b32_e32 v38, 0xff800000\n"
"v_mov_b32_e32 v39, 0xff800000\n"
"v_mov_b32_e32 v60, 0xff800000\n"
"v_mov_b32_e32 v61, 0xff800000\n"
"v_mov_b32_e32 v62, 0xff800000\n"
"v_mov_b32_e32 v63, 0xff800000\n"
"s_nop 1\n"
"s_branch .Lmk_x102r\n"
".Lmk_b97:\n"
"s_cmp_lg_u32 s2, 0\n"
"s_cbranch_scc1 .Lmk_k103\n"
"s_mov_b32 s2, 1\n"
"s_waitcnt vmcnt(0) lgkmcnt(0)\n"
"s_nop 7\n"
"s_mov_b32 s97, 11\n"
"v_add_u32_e32 v248, 0x3800, v15\n"
"v_add_u32_e32 v247, 0x3800, v254\n"
"ds_read_b128 v[36:39], v15 offset:24576\n"
"ds_read_b128 v[40:43], v254 offset:24832\n"
"s_waitcnt lgkmcnt(0)\n"
"v_mfma_f32_16x16x32_bf16 v[48:51], v[36:39], v[28:31], 0\n"
"v_mfma_f32_16x16x32_bf16 v[52:55], v[36:39], v[32:35], 0\n"
"v_mfma_f32_16x16x32_bf16 v[36:39], v[40:43], v[28:31], 0\n"
"v_mfma_f32_16x16x32_bf16 v[56:59], v[40:43], v[32:35], 0\n"
".Lmk_b97l:\n"
"ds_read_b128 v[40:43], v248 offset:8192\n"
"ds_read_b128 v[60:63], v247 offset:8448\n"
"s_cmp_gt_i32 s78, s97\n"
"s_cselect_b32 s96, 0xff800000, 0\n"
"v_mov_b32_e32 v72, s96\n"
"v_mov_b32_e32 v73, s96\n"
"v_mov_b32_e32 v74, s96\n"
"v_mov_b32_e32 v75, s96\n"
"v_subrev_u32_e32 v248, 0x800, v248\n"
"v_subrev_u32_e32 v247, 0x800, v247\n"
"s_waitcnt lgkmcnt(0)\n"
"v_mfma_f32_16x16x32_bf16 v[76:79], v[40:43], v[28:31], v[72:75]\n"
"v_mfma_f32_16x16x32_bf16 v[80:83], v[40:43], v[32:35], v[72:75]\n"
"v_mfma_f32_16x16x32_bf16 v[40:43], v[60:63], v[28:31], v[72:75]\n"
"v_mfma_f32_16x16x32_bf16 v[84:87], v[60:63], v[32:35], v[72:75]\n"
"s_nop 7\n"
"s_cmp_lg_u32 s97, 4\n"
"s_cbranch_scc1 .Lmk_b97m\n"
"v_cndmask_b32_e64 v76, v48, v76, s[20:21]\n"
"v_cndmask_b32_e64 v77, v49, v77, s[22:23]\n"
"v_cndmask_b32_e64 v78, v50, v78, s[24:25]\n"
"v_cndmask_b32_e64 v79, v51, v79, s[26:27]\n"
"v_cndmask_b32_e64 v40, v36, v40, s[28:29]\n"
"v_cndmask_b32_e64 v41, v37, v41, s[30:31]\n"
"v_cndmask_b32_e64 v42, v38, v42, s[32:33]\n"
"v_cndmask_b32_e64 v43, v39, v43, s[34:35]\n"
"v_cndmask_b32_e64 v80, v52, v80, s[36:37]\n"
"v_cndmask_b32_e64 v81, v53, v81, s[38:39]\n"
"v_cndmask_b32_e64 v82, v54, v82, s[40:41]\n"
"v_cndmask_b32_e64 v83, v55, v83, s[42:43]\n"
"v_cndmask_b32_e64 v84, v56, v84, s[44:45]\n"
"v_cndmask_b32_e64 v85, v57, v85, s[46:47]\n"
"v_cndmask_b32_e64 v86, v58, v86, s[48:49]\n"
"v_cndmask_b32_e64 v87, v59, v87, s[50:51]\n"
".Lmk_b97m:\n"
"v_max3_f32 v253, v253, v76, v77\n"
"v_max3_f32 v251, v251, v80, v81\n"
"v_max3_f32 v253, v253, v78, v79\n"
"v_max3_f32 v251, v251, v82, v83\n"
"v_max3_f32 v253, v253, v40, v41\n"
"v_max3_f32 v251, v251, v84, v85\n"
"v_max3_f32 v253, v253, v42, v43\n"
"v_max3_f32 v251, v251, v86, v87\n"
"s_sub_u32 s97, s97, 1\n"
"s_cmp_ge_i32 s97, 4\n"
"s_cbranch_scc1 .Lmk_b97l\n"
"v_mov_b32_e32 v248, v253\n"
"v_mov_b32_e32 v247, v251\n"
"s_nop 0\n"
"v_permlane16_swap_b32_e32 v253, v248\n"
"v_permlane16_swap_b32_e32 v251, v247\n"
"v_max_f32_e32 v253, v253, v248\n"
"v_max_f32_e32 v251, v251, v247\n"
"v_mov_b32_e32 v248, v253\n"
"v_mov_b32_e32 v247, v251\n"
"s_nop 0\n"
"v_permlane32_swap_b32_e32 v253, v248\n"
"v_permlane32_swap_b32_e32 v251, v247\n"
"v_max_f32_e32 v253, v253, v248\n"
"v_max_f32_e32 v251, v251, v247\n"
"v_mov_b32_e32 v250, v253\n"
"v_mov_b32_e32 v249, v251\n"
"s_branch .Lmk_a96\n"
".Lmk_x106s:\n"
"s_nop 3\n"
"v_mov_b32_e32 v116, 0xff800000\n"
"v_mov_b32_e32 v117, 0xff800000\n"
"v_mov_b32_e32 v118, 0xff800000\n"
"v_mov_b32_e32 v119, 0xff800000\n"
"v_mov_b32_e32 v120, 0xff800000\n"
"v_mov_b32_e32 v121, 0xff800000\n"
"v_mov_b32_e32 v122, 0xff800000\n"
"v_mov_b32_e32 v123, 0xff800000\n"
"s_nop 1\n"
"s_branch .Lmk_x106r\n"
".Lmk_x107s:\n"
"s_nop 3\n"
"v_mov_b32_e32 v116, 0xff800000\n"
"v_mov_b32_e32 v117, 0xff800000\n"
"v_mov_b32_e32 v118, 0xff800000\n"
"v_mov_b32_e32 v119, 0xff800000\n"
"v_mov_b32_e32 v120, 0xff800000\n"
"v_mov_b32_e32 v121, 0xff800000\n"
"v_mov_b32_e32 v122, 0xff800000\n"
"v_mov_b32_e32 v123, 0xff800000\n"
"s_nop 1\n"
"s_branch .Lmk_x107r\n"
".Lmk_x108s:\n"
"s_nop 3\n"
"v_mov_b32_e32 v116, 0xff800000\n"
"v_mov_b32_e32 v117, 0xff800000\n"
"v_mov_b32_e32 v118, 0xff800000\n"
"v_mov_b32_e32 v119, 0xff800000\n"
"v_mov_b32_e32 v120, 0xff800000\n"
"v_mov_b32_e32 v121, 0xff800000\n"
"v_mov_b32_e32 v122, 0xff800000\n"
"v_mov_b32_e32 v123, 0xff800000\n"
"s_nop 1\n"
"s_branch .Lmk_x108r\n"
".Lmk_h95s:\n"
";\n"
"s_mov_b32 s1, 0x10000\n"
".Lmk_h95l:\n"
"s_add_u32 m0, s77, 0x4000\n"
"s_add_u32 s95, s65, 0\n"
"buffer_load_dwordx4 v2, s[4:7], s95 offen sc1 lds\n"
"buffer_load_dwordx4 v2, s[4:7], s95 offen offset:1024 sc1 lds\n"
"s_add_u32 m0, s77, 0xc000\n"
"s_add_u32 s95, s65, 0x4000\n"
"buffer_load_dwordx4 v2, s[4:7], s95 offen sc1 lds\n"
"buffer_load_dwordx4 v2, s[4:7], s95 offen offset:1024 sc1 lds\n"
"s_waitcnt vmcnt(0)\n"
"ds_read_b128 v[80:83], v2 offset:16384\n"
"ds_read_b128 v[76:79], v2 offset:17408\n"
"ds_read_b128 v[72:75], v2 offset:49152\n"
"ds_read_b128 v[84:87], v2 offset:50176\n"
"s_waitcnt lgkmcnt(0)\n"
"s_waitcnt vmcnt(0)\n"
"v_max_u32_e32 v248, v80, v81\n"
"v_max3_u32 v248, v248, v82, v83\n"
"v_max3_u32 v248, v248, v76, v77\n"
"v_max3_u32 v248, v248, v78, v79\n"
"v_max3_u32 v248, v248, v72, v73\n"
"v_max3_u32 v248, v248, v74, v75\n"
"v_max3_u32 v248, v248, v84, v85\n"
"v_max3_u32 v248, v248, v86, v87\n"
"v_cmp_eq_u32_e32 vcc, -1, v248\n"
"s_cbranch_vccz .Lmk_h95k\n"
"s_sub_u32 s1, s1, 1\n"
"s_cbranch_scc0 .Lmk_h95l\n"
".Lmk_h95k:\n"
"s_branch .Lmk_h95r\n"
".Lmk_f79s:\n"
";\n"
"s_mov_b32 s1, 0x10000\n"
".Lmk_f79l:\n"
"buffer_load_dwordx2 v[246:247], v12, s[4:7], s96 offen sc1\n"
"s_waitcnt vmcnt(0)\n"
"v_cmp_ne_u64_e32 vcc, v[246:247], v[18:19]\n"
"s_cbranch_vccz .Lmk_f79r\n"
"s_sub_u32 s1, s1, 1\n"
"s_cbranch_scc0 .Lmk_f79l\n"
"s_branch .Lmk_f79r\n"
".Lmk_b105:\n"
"s_cmp_lg_u32 s2, 0\n"
"s_cbranch_scc1 .Lmk_k110\n"
"s_mov_b32 s2, 1\n"
"s_waitcnt vmcnt(0) lgkmcnt(0)\n"
"s_nop 7\n"
"s_mov_b32 s96, 7\n"
"v_add_u32_e32 v248, 0x3800, v15\n"
"v_add_u32_e32 v247, 0x3800, v254\n"
"ds_read_b128 v[140:143], v15 offset:16384\n"
"ds_read_b128 v[148:151], v254 offset:16640\n"
"s_waitcnt lgkmcnt(0)\n"
"v_mfma_f32_16x16x32_bf16 v[188:191], v[140:143], v[92:95], 0\n"
"v_mfma_f32_16x16x32_bf16 v[208:211], v[140:143], v[96:99], 0\n"
"v_mfma_f32_16x16x32_bf16 v[140:143], v[148:151], v[92:95], 0\n"
"v_mfma_f32_16x16x32_bf16 v[220:223], v[148:151], v[96:99], 0\n"
".Lmk_b105l:\n"
"ds_read_b128 v[148:151], v248\n"
"ds_read_b128 v[224:227], v247 offset:256\n"
"s_cmp_gt_i32 s78, s96\n"
"s_cselect_b32 s95, 0xff800000, 0\n"
"v_mov_b32_e32 v228, s95\n"
"v_mov_b32_e32 v229, s95\n"
"v_mov_b32_e32 v230, s95\n"
"v_mov_b32_e32 v231, s95\n"
"v_subrev_u32_e32 v248, 0x800, v248\n"
"v_subrev_u32_e32 v247, 0x800, v247\n"
"s_waitcnt lgkmcnt(0)\n"
"v_mfma_f32_16x16x32_bf16 v[232:235], v[148:151], v[92:95], v[228:231]\n"
"v_mfma_f32_16x16x32_bf16 v[236:239], v[148:151], v[96:99], v[228:231]\n"
"v_mfma_f32_16x16x32_bf16 v[148:151], v[224:227], v[92:95], v[228:231]\n"
"v_mfma_f32_16x16x32_bf16 v[240:243], v[224:227], v[96:99], v[228:231]\n"
"s_nop 7\n"
"s_cmp_lg_u32 s96, 0\n"
"s_cbranch_scc1 .Lmk_b105m\n"
"v_cndmask_b32_e64 v232, v188, v232, s[20:21]\n"
"v_cndmask_b32_e64 v233, v189, v233, s[22:23]\n"
"v_cndmask_b32_e64 v234, v190, v234, s[24:25]\n"
"v_cndmask_b32_e64 v235, v191, v235, s[26:27]\n"
"v_cndmask_b32_e64 v148, v140, v148, s[28:29]\n"
"v_cndmask_b32_e64 v149, v141, v149, s[30:31]\n"
"v_cndmask_b32_e64 v150, v142, v150, s[32:33]\n"
"v_cndmask_b32_e64 v151, v143, v151, s[34:35]\n"
"v_cndmask_b32_e64 v236, v208, v236, s[36:37]\n"
"v_cndmask_b32_e64 v237, v209, v237, s[38:39]\n"
"v_cndmask_b32_e64 v238, v210, v238, s[40:41]\n"
"v_cndmask_b32_e64 v239, v211, v239, s[42:43]\n"
"v_cndmask_b32_e64 v240, v220, v240, s[44:45]\n"
"v_cndmask_b32_e64 v241, v221, v241, s[46:47]\n"
"v_cndmask_b32_e64 v242, v222, v242, s[48:49]\n"
"v_cndmask_b32_e64 v243, v223, v243, s[50:51]\n"
".Lmk_b105m:\n"
"v_max3_f32 v253, v253, v232, v233\n"
"v_max3_f32 v251, v251, v236, v237\n"
"v_max3_f32 v253, v253, v234, v235\n"
"v_max3_f32 v251, v251, v238, v239\n"
"v_max3_f32 v253, v253, v148, v149\n"
"v_max3_f32 v251, v251, v240, v241\n"
"v_max3_f32 v253, v253, v150, v151\n"
"v_max3_f32 v251, v251, v242, v243\n"
"s_sub_u32 s96, s96, 1\n"
"s_cmp_ge_i32 s96, 0\n"
"s_cbranch_scc1 .Lmk_b105l\n"
"v_mov_b32_e32 v248, v253\n"
"v_mov_b32_e32 v247, v251\n"
"s_nop 0\n"
"v_permlane16_swap_b32_e32 v253, v248\n"
"v_permlane16_swap_b32_e32 v251, v247\n"
"v_max_f32_e32 v253, v253, v248\n"
"v_max_f32_e32 v251, v251, v247\n"
"v_mov_b32_e32 v248, v253\n"
"v_mov_b32_e32 v247, v251\n"
"s_nop 0\n"
"v_permlane32_swap_b32_e32 v253, v248\n"
"v_permlane32_swap_b32_e32 v251, v247\n"
"v_max_f32_e32 v253, v253, v248\n"
"v_max_f32_e32 v251, v251, v247\n"
"v_mov_b32_e32 v250, v253\n"
"v_mov_b32_e32 v249, v251\n"
"s_branch .Lmk_a104\n"
".Lmk_p111s:\n"
";\n"
"s_mov_b32 s1, 0x10000\n"
".Lmk_p111l:\n"
"s_add_u32 s98, s56, 0x40000\n"
"buffer_load_dwordx4 v[144:147], v0, s[4:7], s98 offen sc1\n"
"buffer_load_dwordx4 v[196:199], v0, s[4:7], s98 offen offset:1024 sc1\n"
"buffer_load_dwordx4 v[180:183], v0, s[4:7], s98 offen offset:2048 sc1\n"
"buffer_load_dwordx4 v[192:195], v0, s[4:7], s98 offen offset:3072 sc1\n"
"buffer_load_dwordx4 v[204:207], v1, s[4:7], s98 offen sc1\n"
"buffer_load_dwordx4 v[200:203], v1, s[4:7], s98 offen offset:1024 sc1\n"
"buffer_load_dwordx4 v[116:119], v1, s[4:7], s98 offen offset:2048 sc1\n"
"buffer_load_dwordx4 v[132:135], v1, s[4:7], s98 offen offset:3072 sc1\n"
"s_add_u32 s98, s57, 0x40000\n"
"buffer_load_dwordx4 v[216:219], v0, s[4:7], s98 offen sc1\n"
"buffer_load_dwordx4 v[212:215], v0, s[4:7], s98 offen offset:1024 sc1\n"
"buffer_load_dwordx4 v[184:187], v0, s[4:7], s98 offen offset:2048 sc1\n"
"buffer_load_dwordx4 v[100:103], v0, s[4:7], s98 offen offset:3072 sc1\n"
"buffer_load_dwordx4 v[156:159], v1, s[4:7], s98 offen sc1\n"
"buffer_load_dwordx4 v[104:107], v1, s[4:7], s98 offen offset:1024 sc1\n"
"buffer_load_dwordx4 v[112:115], v1, s[4:7], s98 offen offset:2048 sc1\n"
"buffer_load_dwordx4 v[124:127], v1, s[4:7], s98 offen offset:3072 sc1\n"
"s_add_u32 s98, s58, 0x40000\n"
"buffer_load_dwordx4 v[136:139], v0, s[4:7], s98 offen sc1\n"
"buffer_load_dwordx4 v[128:131], v0, s[4:7], s98 offen offset:1024 sc1\n"
"buffer_load_dwordx4 v[120:123], v0, s[4:7], s98 offen offset:2048 sc1\n"
"buffer_load_dwordx4 v[108:111], v0, s[4:7], s98 offen offset:3072 sc1\n"
"buffer_load_dwordx4 v[92:95], v1, s[4:7], s98 offen sc1\n"
"buffer_load_dwordx4 v[96:99], v1, s[4:7], s98 offen offset:1024 sc1\n"
"buffer_load_dwordx4 v[148:151], v1, s[4:7], s98 offen offset:2048 sc1\n"
"s_waitcnt vmcnt(0)\n"
"v_max_u32_e32 v255, v144, v145\n"
"v_max3_u32 v255, v255, v146, v147\n"
"v_max3_u32 v255, v255, v196, v197\n"
"v_max3_u32 v255, v255, v198, v199\n"
"v_max3_u32 v255, v255, v180, v181\n"
"v_max3_u32 v255, v255, v182, v183\n"
"v_max3_u32 v255, v255, v192, v193\n"
"v_max3_u32 v255, v255, v194, v195\n"
"v_max3_u32 v255, v255, v204, v205\n"
"v_max3_u32 v255, v255, v206, v207\n"
"v_max3_u32 v255, v255, v200, v201\n"
"v_max3_u32 v255, v255, v202, v203\n"
"v_max3_u32 v255, v255, v116, v117\n"
"v_max3_u32 v255, v255, v118, v119\n"
"v_max3_u32 v255, v255, v132, v133\n"
"v_max3_u32 v255, v255, v134, v135\n"
"v_cmp_eq_u32_e32 vcc, -1, v255\n"
"s_cbranch_vccz .Lmk_p111k\n"
"s_sub_u32 s1, s1, 1\n"
"s_cbranch_scc0 .Lmk_p111l\n"
".Lmk_p111k:\n"
"v_mfma_f32_16x16x32_bf16 v[88:91], a[128:131], v[144:147], 0\n"
"v_mfma_f32_16x16x32_bf16 v[88:91], a[132:135], v[196:199], v[88:91]\n"
"v_mfma_f32_16x16x32_bf16 v[88:91], a[136:139], v[180:183], v[88:91]\n"
"v_mfma_f32_16x16x32_bf16 v[88:91], a[140:143], v[192:195], v[88:91]\n"
"v_mfma_f32_16x16x32_bf16 v[88:91], a[144:147], v[204:207], v[88:91]\n"
"v_mfma_f32_16x16x32_bf16 v[88:91], a[148:151], v[200:203], v[88:91]\n"
"v_mfma_f32_16x16x32_bf16 v[88:91], a[152:155], v[116:119], v[88:91]\n"
"v_mfma_f32_16x16x32_bf16 v[88:91], a[156:159], v[132:135], v[88:91]\n"
"v_mfma_f32_16x16x32_bf16 v[140:143], a[160:163], v[144:147], 0\n"
"v_mfma_f32_16x16x32_bf16 v[140:143], a[164:167], v[196:199], v[140:143]\n"
"v_mfma_f32_16x16x32_bf16 v[140:143], a[168:171], v[180:183], v[140:143]\n"
"v_mfma_f32_16x16x32_bf16 v[140:143], a[172:175], v[192:195], v[140:143]\n"
"v_mfma_f32_16x16x32_bf16 v[140:143], a[176:179], v[204:207], v[140:143]\n"
"v_mfma_f32_16x16x32_bf16 v[140:143], a[180:183], v[200:203], v[140:143]\n"
"s_nop 7\n"
"s_branch .Lmk_p111r\n"
".Lmk_p112s:\n"
";\n"
"s_mov_b32 s1, 0x10000\n"
".Lmk_p112l:\n"
"s_add_u32 s97, s57, 0x40000\n"
"buffer_load_dwordx4 v[216:219], v0, s[4:7], s97 offen sc1\n"
"buffer_load_dwordx4 v[212:215], v0, s[4:7], s97 offen offset:1024 sc1\n"
"buffer_load_dwordx4 v[184:187], v0, s[4:7], s97 offen offset:2048 sc1\n"
"buffer_load_dwordx4 v[100:103], v0, s[4:7], s97 offen offset:3072 sc1\n"
"buffer_load_dwordx4 v[156:159], v1, s[4:7], s97 offen sc1\n"
"buffer_load_dwordx4 v[104:107], v1, s[4:7], s97 offen offset:1024 sc1\n"
"buffer_load_dwordx4 v[112:115], v1, s[4:7], s97 offen offset:2048 sc1\n"
"buffer_load_dwordx4 v[124:127], v1, s[4:7], s97 offen offset:3072 sc1\n"
"s_add_u32 s97, s58, 0x40000\n"
"buffer_load_dwordx4 v[136:139], v0, s[4:7], s97 offen sc1\n"
"buffer_load_dwordx4 v[128:131], v0, s[4:7], s97 offen offset:1024 sc1\n"
"buffer_load_dwordx4 v[120:123], v0, s[4:7], s97 offen offset:2048 sc1\n"
"buffer_load_dwordx4 v[108:111], v0, s[4:7], s97 offen offset:3072 sc1\n"
"buffer_load_dwordx4 v[92:95], v1, s[4:7], s97 offen sc1\n"
"buffer_load_dwordx4 v[96:99], v1, s[4:7], s97 offen offset:1024 sc1\n"
"buffer_load_dwordx4 v[148:151], v1, s[4:7], s97 offen offset:2048 sc1\n"
"buffer_load_dwordx4 v[88:91], v1, s[4:7], s97 offen offset:3072 sc1\n"
"s_add_u32 s97, s59, 0x40000\n"
"buffer_load_dwordx4 v[160:163], v0, s[4:7], s97 offen sc1\n"
"buffer_load_dwordx4 v[140:143], v0, s[4:7], s97 offen offset:1024 sc1\n"
"buffer_load_dwordx4 v[144:147], v0, s[4:7], s97 offen offset:2048 sc1\n"
"buffer_load_dwordx4 v[116:119], v0, s[4:7], s97 offen offset:3072 sc1\n"
"buffer_load_dwordx4 v[152:155], v1, s[4:7], s97 offen sc1\n"
"buffer_load_dwordx4 v[164:167], v1, s[4:7], s97 offen offset:1024 sc1\n"
"buffer_load_dwordx4 v[172:175], v1, s[4:7], s97 offen offset:2048 sc1\n"
"s_waitcnt vmcnt(0)\n"
"v_max_u32_e32 v255, v216, v217\n"
"v_max3_u32 v255, v255, v218, v219\n"
"v_max3_u32 v255, v255, v212, v213\n"
"v_max3_u32 v255, v255, v214, v215\n"
"v_max3_u32 v255, v255, v184, v185\n"
"v_max3_u32 v255, v255, v186, v187\n"
"v_max3_u32 v255, v255, v100, v101\n"
"v_max3_u32 v255, v255, v102, v103\n"
"v_max3_u32 v255, v255, v156, v157\n"
"v_max3_u32 v255, v255, v158, v159\n"
"v_max3_u32 v255, v255, v104, v105\n"
"v_max3_u32 v255, v255, v106, v107\n"
"v_max3_u32 v255, v255, v112, v113\n"
"v_max3_u32 v255, v255, v114, v115\n"
"v_max3_u32 v255, v255, v124, v125\n"
"v_max3_u32 v255, v255, v126, v127\n"
"v_cmp_eq_u32_e32 vcc, -1, v255\n"
"s_cbranch_vccz .Lmk_p112k\n"
"s_sub_u32 s1, s1, 1\n"
"s_cbranch_scc0 .Lmk_p112l\n"
".Lmk_p112k:\n"
"v_mfma_f32_16x16x32_bf16 v[132:135], a[128:131], v[216:219], 0\n"
"v_mfma_f32_16x16x32_bf16 v[132:135], a[132:135], v[212:215], v[132:135]\n"
"v_mfma_f32_16x16x32_bf16 v[132:135], a[136:139], v[184:187], v[132:135]\n"
"v_mfma_f32_16x16x32_bf16 v[132:135], a[140:143], v[100:103], v[132:135]\n"
"v_mfma_f32_16x16x32_bf16 v[132:135], a[144:147], v[156:159], v[132:135]\n"
"v_mfma_f32_16x16x32_bf16 v[132:135], a[148:151], v[104:107], v[132:135]\n"
"v_mfma_f32_16x16x32_bf16 v[132:135], a[152:155], v[112:115], v[132:135]\n"
"v_mfma_f32_16x16x32_bf16 v[132:135], a[156:159], v[124:127], v[132:135]\n"
"v_mfma_f32_16x16x32_bf16 v[168:171], a[160:163], v[216:219], 0\n"
"v_mfma_f32_16x16x32_bf16 v[168:171], a[164:167], v[212:215], v[168:171]\n"
"v_mfma_f32_16x16x32_bf16 v[168:171], a[168:171], v[184:187], v[168:171]\n"
"v_mfma_f32_16x16x32_bf16 v[168:171], a[172:175], v[100:103], v[168:171]\n"
"v_mfma_f32_16x16x32_bf16 v[168:171], a[176:179], v[156:159], v[168:171]\n"
"v_mfma_f32_16x16x32_bf16 v[168:171], a[180:183], v[104:107], v[168:171]\n"
"s_nop 7\n"
"s_branch .Lmk_p112r\n"
".Lmk_p113s:\n"
";\n"
"s_mov_b32 s1, 0x10000\n"
".Lmk_p113l:\n"
"s_add_u32 s97, s58, 0x40000\n"
"buffer_load_dwordx4 v[136:139], v0, s[4:7], s97 offen sc1\n"
"buffer_load_dwordx4 v[128:131], v0, s[4:7], s97 offen offset:1024 sc1\n"
"buffer_load_dwordx4 v[120:123], v0, s[4:7], s97 offen offset:2048 sc1\n"
"buffer_load_dwordx4 v[108:111], v0, s[4:7], s97 offen offset:3072 sc1\n"
"buffer_load_dwordx4 v[92:95], v1, s[4:7], s97 offen sc1\n"
"buffer_load_dwordx4 v[96:99], v1, s[4:7], s97 offen offset:1024 sc1\n"
"buffer_load_dwordx4 v[148:151], v1, s[4:7], s97 offen offset:2048 sc1\n"
"buffer_load_dwordx4 v[88:91], v1, s[4:7], s97 offen offset:3072 sc1\n"
"s_add_u32 s97, s59, 0x40000\n"
"buffer_load_dwordx4 v[160:163], v0, s[4:7], s97 offen sc1\n"
"buffer_load_dwordx4 v[140:143], v0, s[4:7], s97 offen offset:1024 sc1\n"
"buffer_load_dwordx4 v[144:147], v0, s[4:7], s97 offen offset:2048 sc1\n"
"buffer_load_dwordx4 v[116:119], v0, s[4:7], s97 offen offset:3072 sc1\n"
"buffer_load_dwordx4 v[152:155], v1, s[4:7], s97 offen sc1\n"
"buffer_load_dwordx4 v[164:167], v1, s[4:7], s97 offen offset:1024 sc1\n"
"buffer_load_dwordx4 v[172:175], v1, s[4:7], s97 offen offset:2048 sc1\n"
"buffer_load_dwordx4 v[132:135], v1, s[4:7], s97 offen offset:3072 sc1\n"
"s_waitcnt vmcnt(0)\n"
"v_max_u32_e32 v239, v136, v137\n"
"v_max3_u32 v239, v239, v138, v139\n"
"v_max3_u32 v239, v239, v128, v129\n"
"v_max3_u32 v239, v239, v130, v131\n"
"v_max3_u32 v239, v239, v120, v121\n"
"v_max3_u32 v239, v239, v122, v123\n"
"v_max3_u32 v239, v239, v108, v109\n"
"v_max3_u32 v239, v239, v110, v111\n"
"v_max3_u32 v239, v239, v92, v93\n"
"v_max3_u32 v239, v239, v94, v95\n"
"v_max3_u32 v239, v239, v96, v97\n"
"v_max3_u32 v239, v239, v98, v99\n"
"v_max3_u32 v239, v239, v148, v149\n"
"v_max3_u32 v239, v239, v150, v151\n"
"v_max3_u32 v239, v239, v88, v89\n"
"v_max3_u32 v239, v239, v90, v91\n"
"v_cmp_eq_u32_e32 vcc, -1, v239\n"
"s_cbranch_vccz .Lmk_p113k\n"
"s_sub_u32 s1, s1, 1\n"
"s_cbranch_scc0 .Lmk_p113l\n"
".Lmk_p113k:\n"
"v_mfma_f32_16x16x32_bf16 v[100:103], a[128:131], v[136:139], 0\n"
"v_mfma_f32_16x16x32_bf16 v[100:103], a[132:135], v[128:131], v[100:103]\n"
"v_mfma_f32_16x16x32_bf16 v[100:103], a[136:139], v[120:123], v[100:103]\n"
"v_mfma_f32_16x16x32_bf16 v[100:103], a[140:143], v[108:111], v[100:103]\n"
"v_mfma_f32_16x16x32_bf16 v[100:103], a[144:147], v[92:95], v[100:103]\n"
"v_mfma_f32_16x16x32_bf16 v[100:103], a[148:151], v[96:99], v[100:103]\n"
"v_mfma_f32_16x16x32_bf16 v[100:103], a[152:155], v[148:151], v[100:103]\n"
"v_mfma_f32_16x16x32_bf16 v[100:103], a[156:159], v[88:91], v[100:103]\n"
"v_mfma_f32_16x16x32_bf16 v[104:107], a[160:163], v[136:139], 0\n"
"v_mfma_f32_16x16x32_bf16 v[104:107], a[164:167], v[128:131], v[104:107]\n"
"v_mfma_f32_16x16x32_bf16 v[104:107], a[168:171], v[120:123], v[104:107]\n"
"v_mfma_f32_16x16x32_bf16 v[104:107], a[172:175], v[108:111], v[104:107]\n"
"v_mfma_f32_16x16x32_bf16 v[104:107], a[176:179], v[92:95], v[104:107]\n"
"v_mfma_f32_16x16x32_bf16 v[104:107], a[180:183], v[96:99], v[104:107]\n"
"s_nop 7\n"
"s_branch .Lmk_p113r\n"
".Lmk_p114s:\n"
";\n"
"s_mov_b32 s1, 0x10000\n"
".Lmk_p114l:\n"
"s_add_u32 s97, s59, 0x40000\n"
"buffer_load_dwordx4 v[160:163], v0, s[4:7], s97 offen sc1\n"
"buffer_load_dwordx4 v[140:143], v0, s[4:7], s97 offen offset:1024 sc1\n"
"buffer_load_dwordx4 v[144:147], v0, s[4:7], s97 offen offset:2048 sc1\n"
"buffer_load_dwordx4 v[116:119], v0, s[4:7], s97 offen offset:3072 sc1\n"
"buffer_load_dwordx4 v[152:155], v1, s[4:7], s97 offen sc1\n"
"buffer_load_dwordx4 v[164:167], v1, s[4:7], s97 offen offset:1024 sc1\n"
"buffer_load_dwordx4 v[172:175], v1, s[4:7], s97 offen offset:2048 sc1\n"
"buffer_load_dwordx4 v[132:135], v1, s[4:7], s97 offen offset:3072 sc1\n"
"s_add_u32 s97, s56, 0\n"
"buffer_load_dwordx4 v[100:103], v0, s[4:7], s97 offen sc1\n"
"buffer_load_dwordx4 v[180:183], v0, s[4:7], s97 offen offset:1024 sc1\n"
"buffer_load_dwordx4 v[104:107], v0, s[4:7], s97 offen offset:2048 sc1\n"
"buffer_load_dwordx4 v[124:127], v0, s[4:7], s97 offen offset:3072 sc1\n"
"buffer_load_dwordx4 v[92:95], v1, s[4:7], s97 offen sc1\n"
"buffer_load_dwordx4 v[88:91], v1, s[4:7], s97 offen offset:1024 sc1\n"
"buffer_load_dwordx4 v[128:131], v1, s[4:7], s97 offen offset:2048 sc1\n"
"buffer_load_dwordx4 v[136:139], v1, s[4:7], s97 offen offset:3072 sc1\n"
"s_add_u32 s97, s57, 0\n"
"buffer_load_dwordx4 v[156:159], v0, s[4:7], s97 offen sc1\n"
"s_waitcnt vmcnt(0)\n"
"v_max_u32_e32 v255, v160, v161\n"
"v_max3_u32 v255, v255, v162, v163\n"
"v_max3_u32 v255, v255, v140, v141\n"
"v_max3_u32 v255, v255, v142, v143\n"
"v_max3_u32 v255, v255, v144, v145\n"
"v_max3_u32 v255, v255, v146, v147\n"
"v_max3_u32 v255, v255, v116, v117\n"
"v_max3_u32 v255, v255, v118, v119\n"
"v_max3_u32 v255, v255, v152, v153\n"
"v_max3_u32 v255, v255, v154, v155\n"
"v_max3_u32 v255, v255, v164, v165\n"
"v_max3_u32 v255, v255, v166, v167\n"
"v_max3_u32 v255, v255, v172, v173\n"
"v_max3_u32 v255, v255, v174, v175\n"
"v_max3_u32 v255, v255, v132, v133\n"
"v_max3_u32 v255, v255, v134, v135\n"
"v_cmp_eq_u32_e32 vcc, -1, v255\n"
"s_cbranch_vccz .Lmk_p114k\n"
"s_sub_u32 s1, s1, 1\n"
"s_cbranch_scc0 .Lmk_p114l\n"
".Lmk_p114k:\n"
"v_mfma_f32_16x16x32_bf16 v[120:123], a[128:131], v[160:163], v[112:115]\n"
"v_mfma_f32_16x16x32_bf16 v[120:123], a[132:135], v[140:143], v[120:123]\n"
"v_mfma_f32_16x16x32_bf16 v[120:123], a[136:139], v[144:147], v[120:123]\n"
"v_mfma_f32_16x16x32_bf16 v[120:123], a[140:143], v[116:119], v[120:123]\n"
"v_mfma_f32_16x16x32_bf16 v[120:123], a[144:147], v[152:155], v[120:123]\n"
"v_mfma_f32_16x16x32_bf16 v[120:123], a[148:151], v[164:167], v[120:123]\n"
"v_mfma_f32_16x16x32_bf16 v[120:123], a[152:155], v[172:175], v[120:123]\n"
"v_mfma_f32_16x16x32_bf16 v[120:123], a[156:159], v[132:135], v[120:123]\n"
"v_mfma_f32_16x16x32_bf16 v[148:151], a[160:163], v[160:163], v[168:171]\n"
"v_mfma_f32_16x16x32_bf16 v[148:151], a[164:167], v[140:143], v[148:151]\n"
"v_mfma_f32_16x16x32_bf16 v[148:151], a[168:171], v[144:147], v[148:151]\n"
"v_mfma_f32_16x16x32_bf16 v[148:151], a[172:175], v[116:119], v[148:151]\n"
"v_mfma_f32_16x16x32_bf16 v[148:151], a[176:179], v[152:155], v[148:151]\n"
"v_mfma_f32_16x16x32_bf16 v[148:151], a[180:183], v[164:167], v[148:151]\n"
"s_nop 7\n"
"s_branch .Lmk_p114r\n"
".Lmk_p115s:\n"
";\n"
"s_mov_b32 s1, 0x10000\n"
".Lmk_p115l:\n"
"s_add_u32 s97, s56, 0\n"
"buffer_load_dwordx4 v[100:103], v0, s[4:7], s97 offen sc1\n"
"buffer_load_dwordx4 v[180:183], v0, s[4:7], s97 offen offset:1024 sc1\n"
"buffer_load_dwordx4 v[104:107], v0, s[4:7], s97 offen offset:2048 sc1\n"
"buffer_load_dwordx4 v[124:127], v0, s[4:7], s97 offen offset:3072 sc1\n"
"buffer_load_dwordx4 v[92:95], v1, s[4:7], s97 offen sc1\n"
"buffer_load_dwordx4 v[88:91], v1, s[4:7], s97 offen offset:1024 sc1\n"
"buffer_load_dwordx4 v[128:131], v1, s[4:7], s97 offen offset:2048 sc1\n"
"buffer_load_dwordx4 v[136:139], v1, s[4:7], s97 offen offset:3072 sc1\n"
"s_add_u32 s97, s57, 0\n"
"buffer_load_dwordx4 v[156:159], v0, s[4:7], s97 offen sc1\n"
"buffer_load_dwordx4 v[112:115], v0, s[4:7], s97 offen offset:1024 sc1\n"
"buffer_load_dwordx4 v[108:111], v0, s[4:7], s97 offen offset:2048 sc1\n"
"buffer_load_dwordx4 v[196:199], v0, s[4:7], s97 offen offset:3072 sc1\n"
"buffer_load_dwordx4 v[200:203], v1, s[4:7], s97 offen sc1\n"
"buffer_load_dwordx4 v[120:123], v1, s[4:7], s97 offen offset:1024 sc1\n"
"buffer_load_dwordx4 v[116:119], v1, s[4:7], s97 offen offset:2048 sc1\n"
"buffer_load_dwordx4 v[132:135], v1, s[4:7], s97 offen offset:3072 sc1\n"
"s_add_u32 s97, s58, 0\n"
"buffer_load_dwordx4 v[140:143], v0, s[4:7], s97 offen sc1\n"
"buffer_load_dwordx4 v[144:147], v0, s[4:7], s97 offen offset:1024 sc1\n"
"buffer_load_dwordx4 v[152:155], v0, s[4:7], s97 offen offset:2048 sc1\n"
"s_waitcnt vmcnt(0)\n"
"v_max_u32_e32 v255, v100, v101\n"
"v_max3_u32 v255, v255, v102, v103\n"
"v_max3_u32 v255, v255, v180, v181\n"
"v_max3_u32 v255, v255, v182, v183\n"
"v_max3_u32 v255, v255, v104, v105\n"
"v_max3_u32 v255, v255, v106, v107\n"
"v_max3_u32 v255, v255, v124, v125\n"
"v_max3_u32 v255, v255, v126, v127\n"
"v_max3_u32 v255, v255, v92, v93\n"
"v_max3_u32 v255, v255, v94, v95\n"
"v_max3_u32 v255, v255, v88, v89\n"
"v_max3_u32 v255, v255, v90, v91\n"
"v_max3_u32 v255, v255, v128, v129\n"
"v_max3_u32 v255, v255, v130, v131\n"
"v_max3_u32 v255, v255, v136, v137\n"
"v_max3_u32 v255, v255, v138, v139\n"
"v_cmp_eq_u32_e32 vcc, -1, v255\n"
"s_cbranch_vccz .Lmk_p115k\n"
"s_sub_u32 s1, s1, 1\n"
"s_cbranch_scc0 .Lmk_p115l\n"
".Lmk_p115k:\n"
"v_mfma_f32_16x16x32_bf16 v[96:99], a[128:131], v[100:103], 0\n"
"v_mfma_f32_16x16x32_bf16 v[96:99], a[132:135], v[180:183], v[96:99]\n"
"v_mfma_f32_16x16x32_bf16 v[96:99], a[136:139], v[104:107], v[96:99]\n"
"v_mfma_f32_16x16x32_bf16 v[96:99], a[140:143], v[124:127], v[96:99]\n"
"v_mfma_f32_16x16x32_bf16 v[96:99], a[144:147], v[92:95], v[96:99]\n"
"v_mfma_f32_16x16x32_bf16 v[96:99], a[148:151], v[88:91], v[96:99]\n"
"v_mfma_f32_16x16x32_bf16 v[96:99], a[152:155], v[128:131], v[96:99]\n"
"v_mfma_f32_16x16x32_bf16 v[96:99], a[156:159], v[136:139], v[96:99]\n"
"v_mfma_f32_16x16x32_bf16 v[148:151], a[160:163], v[100:103], 0\n"
"v_mfma_f32_16x16x32_bf16 v[148:151], a[164:167], v[180:183], v[148:151]\n"
"v_mfma_f32_16x16x32_bf16 v[148:151], a[168:171], v[104:107], v[148:151]\n"
"v_mfma_f32_16x16x32_bf16 v[148:151], a[172:175], v[124:127], v[148:151]\n"
"v_mfma_f32_16x16x32_bf16 v[148:151], a[176:179], v[92:95], v[148:151]\n"
"v_mfma_f32_16x16x32_bf16 v[148:151], a[180:183], v[88:91], v[148:151]\n"
"s_nop 7\n"
"s_branch .Lmk_p115r\n"
".Lmk_p116s:\n"
";\n"
"s_mov_b32 s1, 0x10000\n"
".Lmk_p116l:\n"
"s_add_u32 s97, s57, 0\n"
"buffer_load_dwordx4 v[156:159], v0, s[4:7], s97 offen sc1\n"
"buffer_load_dwordx4 v[112:115], v0, s[4:7], s97 offen offset:1024 sc1\n"
"buffer_load_dwordx4 v[108:111], v0, s[4:7], s97 offen offset:2048 sc1\n"
"buffer_load_dwordx4 v[196:199], v0, s[4:7], s97 offen offset:3072 sc1\n"
"buffer_load_dwordx4 v[200:203], v1, s[4:7], s97 offen sc1\n"
"buffer_load_dwordx4 v[120:123], v1, s[4:7], s97 offen offset:1024 sc1\n"
"buffer_load_dwordx4 v[116:119], v1, s[4:7], s97 offen offset:2048 sc1\n"
"buffer_load_dwordx4 v[132:135], v1, s[4:7], s97 offen offset:3072 sc1\n"
"s_add_u32 s97, s58, 0\n"
"buffer_load_dwordx4 v[140:143], v0, s[4:7], s97 offen sc1\n"
"buffer_load_dwordx4 v[144:147], v0, s[4:7], s97 offen offset:1024 sc1\n"
"buffer_load_dwordx4 v[152:155], v0, s[4:7], s97 offen offset:2048 sc1\n"
"buffer_load_dwordx4 v[160:163], v0, s[4:7], s97 offen offset:3072 sc1\n"
"buffer_load_dwordx4 v[164:167], v1, s[4:7], s97 offen sc1\n"
"buffer_load_dwordx4 v[148:151], v1, s[4:7], s97 offen offset:1024 sc1\n"
"buffer_load_dwordx4 v[124:127], v1, s[4:7], s97 offen offset:2048 sc1\n"
"buffer_load_dwordx4 v[128:131], v1, s[4:7], s97 offen offset:3072 sc1\n"
"s_add_u32 s97, s59, 0\n"
"buffer_load_dwordx4 v[172:175], v0, s[4:7], s97 offen sc1\n"
"buffer_load_dwordx4 v[168:171], v0, s[4:7], s97 offen offset:1024 sc1\n"
"buffer_load_dwordx4 v[176:179], v0, s[4:7], s97 offen offset:2048 sc1\n"
"s_waitcnt vmcnt(0)\n"
"v_max_u32_e32 v255, v156, v157\n"
"v_max3_u32 v255, v255, v158, v159\n"
"v_max3_u32 v255, v255, v112, v113\n"
"v_max3_u32 v255, v255, v114, v115\n"
"v_max3_u32 v255, v255, v108, v109\n"
"v_max3_u32 v255, v255, v110, v111\n"
"v_max3_u32 v255, v255, v196, v197\n"
"v_max3_u32 v255, v255, v198, v199\n"
"v_max3_u32 v255, v255, v200, v201\n"
"v_max3_u32 v255, v255, v202, v203\n"
"v_max3_u32 v255, v255, v120, v121\n"
"v_max3_u32 v255, v255, v122, v123\n"
"v_max3_u32 v255, v255, v116, v117\n"
"v_max3_u32 v255, v255, v118, v119\n"
"v_max3_u32 v255, v255, v132, v133\n"
"v_max3_u32 v255, v255, v134, v135\n"
"v_cmp_eq_u32_e32 vcc, -1, v255\n"
"s_cbranch_vccz .Lmk_p116k\n"
"s_sub_u32 s1, s1, 1\n"
"s_cbranch_scc0 .Lmk_p116l\n"
".Lmk_p116k:\n"
"v_mfma_f32_16x16x32_bf16 v[88:91], a[128:131], v[156:159], 0\n"
"v_mfma_f32_16x16x32_bf16 v[88:91], a[132:135], v[112:115], v[88:91]\n"
"v_mfma_f32_16x16x32_bf16 v[88:91], a[136:139], v[108:111], v[88:91]\n"
"v_mfma_f32_16x16x32_bf16 v[88:91], a[140:143], v[196:199], v[88:91]\n"
"v_mfma_f32_16x16x32_bf16 v[88:91], a[144:147], v[200:203], v[88:91]\n"
"v_mfma_f32_16x16x32_bf16 v[88:91], a[148:151], v[120:123], v[88:91]\n"
"v_mfma_f32_16x16x32_bf16 v[88:91], a[152:155], v[116:119], v[88:91]\n"
"v_mfma_f32_16x16x32_bf16 v[88:91], a[156:159], v[132:135], v[88:91]\n"
"v_mfma_f32_16x16x32_bf16 v[92:95], a[160:163], v[156:159], 0\n"
"v_mfma_f32_16x16x32_bf16 v[92:95], a[164:167], v[112:115], v[92:95]\n"
"v_mfma_f32_16x16x32_bf16 v[92:95], a[168:171], v[108:111], v[92:95]\n"
"v_mfma_f32_16x16x32_bf16 v[92:95], a[172:175], v[196:199], v[92:95]\n"
"v_mfma_f32_16x16x32_bf16 v[92:95], a[176:179], v[200:203], v[92:95]\n"
"v_mfma_f32_16x16x32_bf16 v[92:95], a[180:183], v[120:123], v[92:95]\n"
"s_nop 7\n"
"s_branch .Lmk_p116r\n"
".Lmk_p117s:\n"
";\n"
"s_mov_b32 s1, 0x10000\n"
".Lmk_p117l:\n"
"s_add_u32 s97, s58, 0\n"
"buffer_load_dwordx4 v[140:143], v0, s[4:7], s97 offen sc1\n"
"buffer_load_dwordx4 v[144:147], v0, s[4:7], s97 offen offset:1024 sc1\n"
"buffer_load_dwordx4 v[152:155], v0, s[4:7], s97 offen offset:2048 sc1\n"
"buffer_load_dwordx4 v[160:163], v0, s[4:7], s97 offen offset:3072 sc1\n"
"buffer_load_dwordx4 v[164:167], v1, s[4:7], s97 offen sc1\n"
"buffer_load_dwordx4 v[148:151], v1, s[4:7], s97 offen offset:1024 sc1\n"
"buffer_load_dwordx4 v[124:127], v1, s[4:7], s97 offen offset:2048 sc1\n"
"buffer_load_dwordx4 v[128:131], v1, s[4:7], s97 offen offset:3072 sc1\n"
"s_add_u32 s97, s59, 0\n"
"buffer_load_dwordx4 v[172:175], v0, s[4:7], s97 offen sc1\n"
"buffer_load_dwordx4 v[168:171], v0, s[4:7], s97 offen offset:1024 sc1\n"
"buffer_load_dwordx4 v[176:179], v0, s[4:7], s97 offen offset:2048 sc1\n"
"buffer_load_dwordx4 v[180:183], v0, s[4:7], s97 offen offset:3072 sc1\n"
"buffer_load_dwordx4 v[184:187], v1, s[4:7], s97 offen sc1\n"
"buffer_load_dwordx4 v[188:191], v1, s[4:7], s97 offen offset:1024 sc1\n"
"buffer_load_dwordx4 v[192:195], v1, s[4:7], s97 offen offset:2048 sc1\n"
"buffer_load_dwordx4 v[196:199], v1, s[4:7], s97 offen offset:3072 sc1\n"
"s_add_u32 s97, s52, 0x440000\n"
"buffer_load_dwordx4 v[88:91], v0, s[4:7], s97 offen sc1\n"
"buffer_load_dwordx4 v[92:95], v0, s[4:7], s97 offen offset:1024 sc1\n"
"s_waitcnt vmcnt(0)\n"
"v_max_u32_e32 v239, v140, v141\n"
"v_max3_u32 v239, v239, v142, v143\n"
"v_max3_u32 v239, v239, v144, v145\n"
"v_max3_u32 v239, v239, v146, v147\n"
"v_max3_u32 v239, v239, v152, v153\n"
"v_max3_u32 v239, v239, v154, v155\n"
"v_max3_u32 v239, v239, v160, v161\n"
"v_max3_u32 v239, v239, v162, v163\n"
"v_max3_u32 v239, v239, v164, v165\n"
"v_max3_u32 v239, v239, v166, v167\n"
"v_max3_u32 v239, v239, v148, v149\n"
"v_max3_u32 v239, v239, v150, v151\n"
"v_max3_u32 v239, v239, v124, v125\n"
"v_max3_u32 v239, v239, v126, v127\n"
"v_max3_u32 v239, v239, v128, v129\n"
"v_max3_u32 v239, v239, v130, v131\n"
"v_cmp_eq_u32_e32 vcc, -1, v239\n"
"s_cbranch_vccz .Lmk_p117k\n"
"s_sub_u32 s1, s1, 1\n"
"s_cbranch_scc0 .Lmk_p117l\n"
".Lmk_p117k:\n"
"v_mfma_f32_16x16x32_bf16 v[96:99], a[128:131], v[140:143], 0\n"
"v_mfma_f32_16x16x32_bf16 v[96:99], a[132:135], v[144:147], v[96:99]\n"
"v_mfma_f32_16x16x32_bf16 v[96:99], a[136:139], v[152:155], v[96:99]\n"
"v_mfma_f32_16x16x32_bf16 v[96:99], a[140:143], v[160:163], v[96:99]\n"
"v_mfma_f32_16x16x32_bf16 v[96:99], a[144:147], v[164:167], v[96:99]\n"
"v_mfma_f32_16x16x32_bf16 v[96:99], a[148:151], v[148:151], v[96:99]\n"
"v_mfma_f32_16x16x32_bf16 v[96:99], a[152:155], v[124:127], v[96:99]\n"
"v_mfma_f32_16x16x32_bf16 v[96:99], a[156:159], v[128:131], v[96:99]\n"
"v_mfma_f32_16x16x32_bf16 v[104:107], a[160:163], v[140:143], 0\n"
"v_mfma_f32_16x16x32_bf16 v[104:107], a[164:167], v[144:147], v[104:107]\n"
"v_mfma_f32_16x16x32_bf16 v[104:107], a[168:171], v[152:155], v[104:107]\n"
"v_mfma_f32_16x16x32_bf16 v[104:107], a[172:175], v[160:163], v[104:107]\n"
"v_mfma_f32_16x16x32_bf16 v[104:107], a[176:179], v[164:167], v[104:107]\n"
"v_mfma_f32_16x16x32_bf16 v[104:107], a[180:183], v[148:151], v[104:107]\n"
"s_nop 7\n"
"s_branch .Lmk_p117r\n"
".Lmk_p118s:\n"
";\n"
"s_mov_b32 s1, 0x10000\n"
".Lmk_p118l:\n"
"s_add_u32 s97, s59, 0\n"
"buffer_load_dwordx4 v[172:175], v0, s[4:7], s97 offen sc1\n"
"buffer_load_dwordx4 v[168:171], v0, s[4:7], s97 offen offset:1024 sc1\n"
"buffer_load_dwordx4 v[176:179], v0, s[4:7], s97 offen offset:2048 sc1\n"
"buffer_load_dwordx4 v[180:183], v0, s[4:7], s97 offen offset:3072 sc1\n"
"buffer_load_dwordx4 v[184:187], v1, s[4:7], s97 offen sc1\n"
"buffer_load_dwordx4 v[188:191], v1, s[4:7], s97 offen offset:1024 sc1\n"
"buffer_load_dwordx4 v[192:195], v1, s[4:7], s97 offen offset:2048 sc1\n"
"buffer_load_dwordx4 v[196:199], v1, s[4:7], s97 offen offset:3072 sc1\n"
"s_add_u32 s97, s52, 0x440000\n"
"buffer_load_dwordx4 v[88:91], v0, s[4:7], s97 offen sc1\n"
"buffer_load_dwordx4 v[92:95], v0, s[4:7], s97 offen offset:1024 sc1\n"
"buffer_load_dwordx4 v[96:99], v0, s[4:7], s97 offen offset:2048 sc1\n"
"buffer_load_dwordx4 v[100:103], v0, s[4:7], s97 offen offset:3072 sc1\n"
"buffer_load_dwordx4 v[104:107], v1, s[4:7], s97 offen sc1\n"
"buffer_load_dwordx4 v[112:115], v1, s[4:7], s97 offen offset:1024 sc1\n"
"buffer_load_dwordx4 v[116:119], v1, s[4:7], s97 offen offset:2048 sc1\n"
"buffer_load_dwordx4 v[124:127], v1, s[4:7], s97 offen offset:3072 sc1\n"
"s_add_u32 s97, s53, 0x440000\n"
"buffer_load_dwordx4 v[128:131], v0, s[4:7], s97 offen sc1\n"
"buffer_load_dwordx4 v[132:135], v0, s[4:7], s97 offen offset:1024 sc1\n"
"buffer_load_dwordx4 v[136:139], v0, s[4:7], s97 offen offset:2048 sc1\n"
"s_waitcnt vmcnt(0)\n"
"v_max_u32_e32 v255, v172, v173\n"
"v_max3_u32 v255, v255, v174, v175\n"
"v_max3_u32 v255, v255, v168, v169\n"
"v_max3_u32 v255, v255, v170, v171\n"
"v_max3_u32 v255, v255, v176, v177\n"
"v_max3_u32 v255, v255, v178, v179\n"
"v_max3_u32 v255, v255, v180, v181\n"
"v_max3_u32 v255, v255, v182, v183\n"
"v_max3_u32 v255, v255, v184, v185\n"
"v_max3_u32 v255, v255, v186, v187\n"
"v_max3_u32 v255, v255, v188, v189\n"
"v_max3_u32 v255, v255, v190, v191\n"
"v_max3_u32 v255, v255, v192, v193\n"
"v_max3_u32 v255, v255, v194, v195\n"
"v_max3_u32 v255, v255, v196, v197\n"
"v_max3_u32 v255, v255, v198, v199\n"
"v_cmp_eq_u32_e32 vcc, -1, v255\n"
"s_cbranch_vccz .Lmk_p118k\n"
"s_sub_u32 s1, s1, 1\n"
"s_cbranch_scc0 .Lmk_p118l\n"
".Lmk_p118k:\n"
"v_mfma_f32_16x16x32_bf16 v[164:167], a[128:131], v[172:175], v[156:159]\n"
"v_mfma_f32_16x16x32_bf16 v[164:167], a[132:135], v[168:171], v[164:167]\n"
"v_mfma_f32_16x16x32_bf16 v[164:167], a[136:139], v[176:179], v[164:167]\n"
"v_mfma_f32_16x16x32_bf16 v[164:167], a[140:143], v[180:183], v[164:167]\n"
"v_mfma_f32_16x16x32_bf16 v[164:167], a[144:147], v[184:187], v[164:167]\n"
"v_mfma_f32_16x16x32_bf16 v[164:167], a[148:151], v[188:191], v[164:167]\n"
"v_mfma_f32_16x16x32_bf16 v[164:167], a[152:155], v[192:195], v[164:167]\n"
"v_mfma_f32_16x16x32_bf16 v[164:167], a[156:159], v[196:199], v[164:167]\n"
"v_mfma_f32_16x16x32_bf16 v[204:207], a[160:163], v[172:175], v[200:203]\n"
"v_mfma_f32_16x16x32_bf16 v[204:207], a[164:167], v[168:171], v[204:207]\n"
"v_mfma_f32_16x16x32_bf16 v[204:207], a[168:171], v[176:179], v[204:207]\n"
"v_mfma_f32_16x16x32_bf16 v[204:207], a[172:175], v[180:183], v[204:207]\n"
"v_mfma_f32_16x16x32_bf16 v[204:207], a[176:179], v[184:187], v[204:207]\n"
"v_mfma_f32_16x16x32_bf16 v[204:207], a[180:183], v[188:191], v[204:207]\n"
"s_nop 7\n"
"s_branch .Lmk_p118r\n"
".Lmk_exit:\n"
"v_xor_b32_e32 v255, 32, v15\n"
"ds_read_b128 v[24:27], v2 offset:17408\n"
"ds_read_b128 v[28:31], v15 offset:16384\n"
"ds_read_b128 v[48:51], v255 offset:16640\n"
"v_xor_b32_e32 v255, 32, v17\n"
"ds_read_b128 v[52:55], v2 offset:16384\n"
"ds_read_b128 v[56:59], v17 offset:15360\n"
"ds_read_b128 v[60:63], v255 offset:15616\n"
"v_xor_b32_e32 v255, 32, v16\n"
"ds_read_b64_tr_b16 v[64:65], v16 offset:16384\n"
"ds_read_b64_tr_b16 v[66:67], v255 offset:16640\n"
"ds_read_b64_tr_b16 v[68:69], v255 offset:16384\n"
"ds_read_b64_tr_b16 v[70:71], v16 offset:16640\n"
"v_mov_b32_e32 v72, 0x3f803f80\n"
"v_mov_b32_e32 v73, 0x3f803f80\n"
"v_mov_b32_e32 v74, 0x3f803f80\n"
"v_mov_b32_e32 v75, 0x3f803f80\n"
"v_xor_b32_e32 v255, 32, v16\n"
"ds_read_b64_tr_b16 v[76:77], v16 offset:15360\n"
"ds_read_b64_tr_b16 v[78:79], v255 offset:15616\n"
"ds_read_b64_tr_b16 v[80:81], v255 offset:15360\n"
"ds_read_b64_tr_b16 v[82:83], v16 offset:15616\n"
"v_mov_b32_e32 v84, 0x3f803f80\n"
"v_mov_b32_e32 v85, 0x3f803f80\n"
"v_mov_b32_e32 v86, 0x3f803f80\n"
"v_mov_b32_e32 v87, 0x3f803f80\n"
"s_waitcnt lgkmcnt(11)\n"
"v_mfma_f32_16x16x32_bf16 v[32:35], v[28:31], v[24:27], v[32:35]\n"
"v_mfma_f32_16x16x32_bf16 v[36:39], v[48:51], v[24:27], v[36:39]\n"
"s_waitcnt lgkmcnt(8)\n"
"v_mfma_f32_16x16x32_bf16 v[40:43], v[56:59], v[52:55], v[40:43]\n"
"v_mfma_f32_16x16x32_bf16 v[44:47], v[60:63], v[52:55], v[44:47]\n"
"s_add_u32 s98, s56, 0x440000\n"
"buffer_load_dwordx4 v[24:27], v0, s[4:7], s98 offen sc1\n"
"buffer_load_dwordx4 v[28:31], v0, s[4:7], s98 offen offset:1024 sc1\n"
"buffer_load_dwordx4 v[48:51], v0, s[4:7], s98 offen offset:2048 sc1\n"
"buffer_load_dwordx4 v[52:55], v0, s[4:7], s98 offen offset:3072 sc1\n"
"v_max_f32_e32 v255, v32, v33\n"
"s_nop 0\n"
"v_max_f32_e32 v254, v40, v41\n"
"v_max3_f32 v255, v255, v34, v35\n"
"v_max3_f32 v254, v254, v42, v43\n"
"v_max3_f32 v255, v255, v36, v37\n"
"v_max3_f32 v254, v254, v44, v45\n"
"v_max3_f32 v255, v255, v38, v39\n"
"v_max3_f32 v254, v254, v46, v47\n"
"v_mov_b32_e32 v253, v255\n"
"v_mov_b32_e32 v252, v254\n"
"s_nop 0\n"
"v_permlane16_swap_b32_e32 v255, v253\n"
"v_permlane16_swap_b32_e32 v254, v252\n"
"v_max_f32_e32 v255, v255, v253\n"
"v_max_f32_e32 v254, v254, v252\n"
"v_mov_b32_e32 v253, v255\n"
"v_mov_b32_e32 v252, v254\n"
"s_nop 0\n"
"v_permlane32_swap_b32_e32 v255, v253\n"
"v_permlane32_swap_b32_e32 v254, v252\n"
"v_max_f32_e32 v255, v255, v253\n"
"v_max_f32_e32 v254, v254, v252\n"
"buffer_load_dwordx4 v[56:59], v1, s[4:7], s98 offen sc1\n"
"v_sub_f32_e32 v32, v32, v255\n"
"v_sub_f32_e32 v33, v33, v255\n"
"v_sub_f32_e32 v34, v34, v255\n"
"v_sub_f32_e32 v35, v35, v255\n"
"v_sub_f32_e32 v36, v36, v255\n"
"v_sub_f32_e32 v37, v37, v255\n"
"v_sub_f32_e32 v38, v38, v255\n"
"buffer_load_dwordx4 v[60:63], v1, s[4:7], s98 offen offset:1024 sc1\n"
"v_sub_f32_e32 v39, v39, v255\n"
"v_exp_f32_e32 v32, v32\n"
"v_exp_f32_e32 v33, v33\n"
"v_exp_f32_e32 v34, v34\n"
"buffer_load_dwordx4 v[88:91], v1, s[4:7], s98 offen offset:2048 sc1\n"
"v_exp_f32_e32 v35, v35\n"
"v_exp_f32_e32 v36, v36\n"
"v_exp_f32_e32 v37, v37\n"
"v_exp_f32_e32 v38, v38\n"
"buffer_load_dwordx4 v[92:95], v1, s[4:7], s98 offen offset:3072 sc1\n"
"v_exp_f32_e32 v39, v39\n"
"v_cvt_pk_bf16_f32 v96, v32, v33\n"
"v_cvt_pk_bf16_f32 v97, v34, v35\n"
"v_cvt_pk_bf16_f32 v98, v36, v37\n"
"v_cvt_pk_bf16_f32 v99, v38, v39\n"
"s_add_u32 s98, s57, 0x440000\n"
"buffer_load_dwordx4 v[100:103], v0, s[4:7], s98 offen sc1\n"
"s_waitcnt lgkmcnt(4)\n"
"v_sub_f32_e32 v40, v40, v254\n"
"v_sub_f32_e32 v41, v41, v254\n"
"v_sub_f32_e32 v42, v42, v254\n"
"v_sub_f32_e32 v43, v43, v254\n"
"v_sub_f32_e32 v44, v44, v254\n"
"v_sub_f32_e32 v45, v45, v254\n"
"v_sub_f32_e32 v46, v46, v254\n"
"v_mfma_f32_16x16x32_bf16 v[104:107], v[72:75], v[96:99], 0\n"
"buffer_load_dwordx4 v[72:75], v0, s[4:7], s98 offen offset:1024 sc1\n"
"v_sub_f32_e32 v47, v47, v254\n"
"v_exp_f32_e32 v40, v40\n"
"v_exp_f32_e32 v41, v41\n"
"v_exp_f32_e32 v42, v42\n"
"v_mfma_f32_16x16x32_bf16 v[108:111], v[64:67], v[96:99], 0\n"
"buffer_load_dwordx4 v[64:67], v0, s[4:7], s98 offen offset:2048 sc1\n"
"v_exp_f32_e32 v43, v43\n"
"v_exp_f32_e32 v44, v44\n"
"v_exp_f32_e32 v45, v45\n"
"v_exp_f32_e32 v46, v46\n"
"buffer_load_dwordx4 v[112:115], v0, s[4:7], s98 offen offset:3072 sc1\n"
"v_mfma_f32_16x16x32_bf16 v[116:119], v[68:71], v[96:99], 0\n"
"v_exp_f32_e32 v47, v47\n"
"v_cvt_pk_bf16_f32 v68, v40, v41\n"
"v_cvt_pk_bf16_f32 v69, v42, v43\n"
"v_cvt_pk_bf16_f32 v70, v44, v45\n"
"v_cvt_pk_bf16_f32 v71, v46, v47\n"
"buffer_load_dwordx4 v[96:99], v1, s[4:7], s98 offen sc1\n"
"s_waitcnt lgkmcnt(0)\n"
"v_rcp_f32_e32 v104, v104\n"
"s_nop 0\n"
"v_mul_f32_e32 v108, v108, v104\n"
"v_mul_f32_e32 v109, v109, v104\n"
"v_mfma_f32_16x16x32_bf16 v[120:123], v[84:87], v[68:71], 0\n"
"buffer_load_dwordx4 v[84:87], v1, s[4:7], s98 offen offset:1024 sc1\n"
"v_mul_f32_e32 v110, v110, v104\n"
"v_mul_f32_e32 v111, v111, v104\n"
"v_mul_f32_e32 v116, v116, v104\n"
"v_mfma_f32_16x16x32_bf16 v[124:127], v[76:79], v[68:71], 0\n"
"buffer_load_dwordx4 v[76:79], v1, s[4:7], s98 offen offset:2048 sc1\n"
"v_mul_f32_e32 v117, v117, v104\n"
"v_mul_f32_e32 v118, v118, v104\n"
"v_mul_f32_e32 v119, v119, v104\n"
"v_cvt_pk_bf16_f32 v104, v108, v109\n"
"buffer_load_dwordx4 v[128:131], v1, s[4:7], s98 offen offset:3072 sc1\n"
"v_mfma_f32_16x16x32_bf16 v[132:135], v[80:83], v[68:71], 0\n"
"v_cvt_pk_bf16_f32 v105, v110, v111\n"
"v_cvt_pk_bf16_f32 v106, v116, v117\n"
"v_cvt_pk_bf16_f32 v107, v118, v119\n"
"s_add_u32 s98, s60, 0x408000\n"
"buffer_store_dwordx4 v[104:107], v0, s[4:7], s98 offen sc1\n"
"v_rcp_f32_e32 v120, v120\n"
"s_nop 0\n"
"v_mul_f32_e32 v124, v124, v120\n"
"v_mul_f32_e32 v125, v125, v120\n"
"v_mul_f32_e32 v126, v126, v120\n"
"v_mul_f32_e32 v127, v127, v120\n"
"v_mul_f32_e32 v132, v132, v120\n"
"v_mul_f32_e32 v133, v133, v120\n"
"v_mul_f32_e32 v134, v134, v120\n"
"v_mul_f32_e32 v135, v135, v120\n"
"v_cvt_pk_bf16_f32 v68, v124, v125\n"
"v_cvt_pk_bf16_f32 v69, v126, v127\n"
"v_cvt_pk_bf16_f32 v70, v132, v133\n"
"v_cvt_pk_bf16_f32 v71, v134, v135\n"
"s_add_u32 s98, s60, 0x400000\n"
"buffer_store_dwordx4 v[68:71], v0, s[4:7], s98 offen sc1\n"
";\n"
"s_waitcnt vmcnt(16)\n"
"; @B G2 b.0\n"
"s_nop 0\n"
"v_mfma_f32_16x16x32_bf16 v[68:71], a[128:131], v[24:27], 0\n"
"s_add_u32 s98, s58, 0x440000\n"
"buffer_load_dwordx4 v[80:83], v0, s[4:7], s98 offen sc1\n"
"v_mfma_f32_16x16x32_bf16 v[68:71], a[132:135], v[28:31], v[68:71]\n"
"s_waitcnt vmcnt(15)\n"
"v_mfma_f32_16x16x32_bf16 v[68:71], a[136:139], v[48:51], v[68:71]\n"
"v_mfma_f32_16x16x32_bf16 v[68:71], a[140:143], v[52:55], v[68:71]\n"
"s_waitcnt vmcnt(13)\n"
"v_mfma_f32_16x16x32_bf16 v[68:71], a[144:147], v[56:59], v[68:71]\n"
"buffer_load_dwordx4 v[104:107], v0, s[4:7], s98 offen offset:1024 sc1\n"
"v_mfma_f32_16x16x32_bf16 v[68:71], a[148:151], v[60:63], v[68:71]\n"
"s_waitcnt vmcnt(12)\n"
"v_mfma_f32_16x16x32_bf16 v[68:71], a[152:155], v[88:91], v[68:71]\n"
"v_mfma_f32_16x16x32_bf16 v[68:71], a[156:159], v[92:95], v[68:71]\n"
"v_mfma_f32_16x16x32_bf16 v[108:111], a[160:163], v[24:27], 0\n"
"buffer_load_dwordx4 v[116:119], v0, s[4:7], s98 offen offset:2048 sc1\n"
"v_mfma_f32_16x16x32_bf16 v[108:111], a[164:167], v[28:31], v[108:111]\n"
"v_mfma_f32_16x16x32_bf16 v[108:111], a[168:171], v[48:51], v[108:111]\n"
"v_mfma_f32_16x16x32_bf16 v[108:111], a[172:175], v[52:55], v[108:111]\n"
"v_mfma_f32_16x16x32_bf16 v[108:111], a[176:179], v[56:59], v[108:111]\n"
"s_nop 1\n"
"v_cmp_u_f32_e32 vcc, v68, v68\n"
"v_mfma_f32_16x16x32_bf16 v[108:111], a[180:183], v[60:63], v[108:111]\n"
"s_cbranch_vccnz .Lmk_p60s\n"
".Lmk_p60r:\n"
"v_mfma_f32_16x16x32_bf16 v[108:111], a[184:187], v[88:91], v[108:111]\n"
"ds_write_b128 v3, v[68:71]\n"
"v_mfma_f32_16x16x32_bf16 v[108:111], a[188:191], v[92:95], v[108:111]\n"
"buffer_load_dwordx4 v[68:71], v0, s[4:7], s98 offen offset:3072 sc1\n"
"v_mfma_f32_16x16x32_bf16 v[120:123], a[192:195], v[24:27], 0\n"
"v_mfma_f32_16x16x32_bf16 v[120:123], a[196:199], v[28:31], v[120:123]\n"
"v_mfma_f32_16x16x32_bf16 v[120:123], a[200:203], v[48:51], v[120:123]\n"
"v_mfma_f32_16x16x32_bf16 v[120:123], a[204:207], v[52:55], v[120:123]\n"
"buffer_load_dwordx4 v[124:127], v1, s[4:7], s98 offen sc1\n"
"v_mfma_f32_16x16x32_bf16 v[120:123], a[208:211], v[56:59], v[120:123]\n"
"v_mfma_f32_16x16x32_bf16 v[120:123], a[212:215], v[60:63], v[120:123]\n"
"ds_write_b128 v3, v[108:111] offset:1024\n"
"v_mfma_f32_16x16x32_bf16 v[120:123], a[216:219], v[88:91], v[120:123]\n"
"v_mfma_f32_16x16x32_bf16 v[120:123], a[220:223], v[92:95], v[120:123]\n"
"buffer_load_dwordx4 v[108:111], v1, s[4:7], s98 offen offset:1024 sc1\n"
"v_mfma_f32_16x16x32_bf16 v[132:135], a[224:227], v[24:27], 0\n"
"v_mfma_f32_16x16x32_bf16 v[132:135], a[228:231], v[28:31], v[132:135]\n"
"v_mfma_f32_16x16x32_bf16 v[132:135], a[232:235], v[48:51], v[132:135]\n"
"v_mfma_f32_16x16x32_bf16 v[132:135], a[236:239], v[52:55], v[132:135]\n"
"buffer_load_dwordx4 v[24:27], v1, s[4:7], s98 offen offset:2048 sc1\n"
"v_mfma_f32_16x16x32_bf16 v[132:135], a[240:243], v[56:59], v[132:135]\n"
"v_mfma_f32_16x16x32_bf16 v[132:135], a[244:247], v[60:63], v[132:135]\n"
"ds_write_b128 v3, v[120:123] offset:2048\n"
"v_mfma_f32_16x16x32_bf16 v[132:135], a[248:251], v[88:91], v[132:135]\n"
"v_mfma_f32_16x16x32_bf16 v[132:135], a[252:255], v[92:95], v[132:135]\n"
"s_waitcnt vmcnt(13)\n"
"buffer_load_dwordx4 v[28:31], v1, s[4:7], s98 offen offset:3072 sc1\n"
"; @B G2 b.1\n"
"v_mfma_f32_16x16x32_bf16 v[48:51], a[128:131], v[100:103], 0\n"
"v_mfma_f32_16x16x32_bf16 v[48:51], a[132:135], v[72:75], v[48:51]\n"
"s_add_u32 s98, s59, 0x440000\n"
"v_mfma_f32_16x16x32_bf16 v[48:51], a[136:139], v[64:67], v[48:51]\n"
"v_mfma_f32_16x16x32_bf16 v[48:51], a[140:143], v[112:115], v[48:51]\n"
"s_waitcnt vmcnt(10)\n"
"buffer_load_dwordx4 v[52:55], v0, s[4:7], s98 offen sc1\n"
"v_mfma_f32_16x16x32_bf16 v[48:51], a[144:147], v[96:99], v[48:51]\n"
"v_mfma_f32_16x16x32_bf16 v[48:51], a[148:151], v[84:87], v[48:51]\n"
"ds_write_b128 v3, v[132:135] offset:3072\n"
"v_mfma_f32_16x16x32_bf16 v[48:51], a[152:155], v[76:79], v[48:51]\n"
"v_mfma_f32_16x16x32_bf16 v[48:51], a[156:159], v[128:131], v[48:51]\n"
"buffer_load_dwordx4 v[56:59], v0, s[4:7], s98 offen offset:1024 sc1\n"
"v_mfma_f32_16x16x32_bf16 v[60:63], a[160:163], v[100:103], 0\n"
"v_mfma_f32_16x16x32_bf16 v[60:63], a[164:167], v[72:75], v[60:63]\n"
"v_mfma_f32_16x16x32_bf16 v[60:63], a[168:171], v[64:67], v[60:63]\n"
"v_mfma_f32_16x16x32_bf16 v[60:63], a[172:175], v[112:115], v[60:63]\n"
"buffer_load_dwordx4 v[88:91], v0, s[4:7], s98 offen offset:2048 sc1\n"
"v_mfma_f32_16x16x32_bf16 v[60:63], a[176:179], v[96:99], v[60:63]\n"
"s_nop 0\n"
"v_cmp_u_f32_e32 vcc, v48, v48\n"
"v_mfma_f32_16x16x32_bf16 v[60:63], a[180:183], v[84:87], v[60:63]\n"
"s_cbranch_vccnz .Lmk_p61s\n"
".Lmk_p61r:\n"
"v_mfma_f32_16x16x32_bf16 v[60:63], a[184:187], v[76:79], v[60:63]\n"
"ds_write_b128 v3, v[48:51] offset:6144\n"
"v_mfma_f32_16x16x32_bf16 v[60:63], a[188:191], v[128:131], v[60:63]\n"
"buffer_load_dwordx4 v[48:51], v0, s[4:7], s98 offen offset:3072 sc1\n"
"v_mfma_f32_16x16x32_bf16 v[92:95], a[192:195], v[100:103], 0\n"
"v_mfma_f32_16x16x32_bf16 v[92:95], a[196:199], v[72:75], v[92:95]\n"
"v_mfma_f32_16x16x32_bf16 v[92:95], a[200:203], v[64:67], v[92:95]\n"
"v_mfma_f32_16x16x32_bf16 v[92:95], a[204:207], v[112:115], v[92:95]\n"
"buffer_load_dwordx4 v[120:123], v1, s[4:7], s98 offen sc1\n"
"v_mfma_f32_16x16x32_bf16 v[92:95], a[208:211], v[96:99], v[92:95]\n"
"ds_read_b128 v[132:135], v2 offset:8192\n"
"ds_read_b128 v[136:139], v2 offset:9216\n"
"v_mfma_f32_16x16x32_bf16 v[92:95], a[212:215], v[84:87], v[92:95]\n"
"ds_write_b128 v3, v[60:63] offset:7168\n"
"v_mfma_f32_16x16x32_bf16 v[92:95], a[216:219], v[76:79], v[92:95]\n"
"v_mfma_f32_16x16x32_bf16 v[92:95], a[220:223], v[128:131], v[92:95]\n"
"buffer_load_dwordx4 v[60:63], v1, s[4:7], s98 offen offset:1024 sc1\n"
"v_mfma_f32_16x16x32_bf16 v[140:143], a[224:227], v[100:103], 0\n"
"v_mfma_f32_16x16x32_bf16 v[140:143], a[228:231], v[72:75], v[140:143]\n"
"s_waitcnt lgkmcnt(1)\n"
"v_lshlrev_b32_e32 v255, 16, v132\n"
"v_and_b32_e32 v254, 0xffff0000, v132\n"
"v_lshlrev_b32_e32 v253, 16, v133\n"
"v_mfma_f32_16x16x32_bf16 v[140:143], a[232:235], v[64:67], v[140:143]\n"
"v_and_b32_e32 v252, 0xffff0000, v133\n"
"v_lshlrev_b32_e32 v251, 16, v134\n"
"v_and_b32_e32 v250, 0xffff0000, v134\n"
"v_mfma_f32_16x16x32_bf16 v[140:143], a[236:239], v[112:115], v[140:143]\n"
"buffer_load_dwordx4 v[64:67], v1, s[4:7], s98 offen offset:2048 sc1\n"
"v_lshlrev_b32_e32 v249, 16, v135\n"
"v_mfma_f32_16x16x32_bf16 v[140:143], a[240:243], v[96:99], v[140:143]\n"
"v_and_b32_e32 v248, 0xffff0000, v135\n"
"v_lshlrev_b32_e32 v247, 16, v136\n"
"v_and_b32_e32 v246, 0xffff0000, v136\n"
"v_mfma_f32_16x16x32_bf16 v[140:143], a[244:247], v[84:87], v[140:143]\n"
"ds_write_b128 v3, v[92:95] offset:8192\n"
"v_mfma_f32_16x16x32_bf16 v[140:143], a[248:251], v[76:79], v[140:143]\n"
"v_lshlrev_b32_e32 v245, 16, v137\n"
"v_and_b32_e32 v244, 0xffff0000, v137\n"
"v_lshlrev_b32_e32 v243, 16, v138\n"
"v_mfma_f32_16x16x32_bf16 v[140:143], a[252:255], v[128:131], v[140:143]\n"
"s_waitcnt vmcnt(11)\n"
"buffer_load_dwordx4 v[72:75], v1, s[4:7], s98 offen offset:3072 sc1\n"
"; @B G2 b.2\n"
"v_mfma_f32_16x16x32_bf16 v[76:79], a[128:131], v[80:83], 0\n"
"v_and_b32_e32 v242, 0xffff0000, v138\n"
"v_lshlrev_b32_e32 v241, 16, v139\n"
"v_and_b32_e32 v240, 0xffff0000, v139\n"
"v_mfma_f32_16x16x32_bf16 v[76:79], a[132:135], v[104:107], v[76:79]\n"
"v_mfma_f32_16x16x32_bf16 v[76:79], a[136:139], v[116:119], v[76:79]\n"
"v_mfma_f32_16x16x32_bf16 v[76:79], a[140:143], v[68:71], v[76:79]\n"
"s_waitcnt vmcnt(8)\n"
"v_mfma_f32_16x16x32_bf16 v[76:79], a[144:147], v[124:127], v[76:79]\n"
"v_mfma_f32_16x16x32_bf16 v[76:79], a[148:151], v[108:111], v[76:79]\n"
"ds_write_b128 v3, v[140:143] offset:9216\n"
"v_mfma_f32_16x16x32_bf16 v[76:79], a[152:155], v[24:27], v[76:79]\n"
"v_mfma_f32_16x16x32_bf16 v[76:79], a[156:159], v[28:31], v[76:79]\n"
"v_mfma_f32_16x16x32_bf16 v[84:87], a[160:163], v[80:83], 0\n"
"v_mfma_f32_16x16x32_bf16 v[84:87], a[164:167], v[104:107], v[84:87]\n"
"s_waitcnt lgkmcnt(0)\n"
"s_barrier\n"
"v_mfma_f32_16x16x32_bf16 v[84:87], a[168:171], v[116:119], v[84:87]\n"
"ds_read_b128 v[92:95], v4\n"
"ds_read_b128 v[96:99], v5\n"
"v_mfma_f32_16x16x32_bf16 v[84:87], a[172:175], v[68:71], v[84:87]\n"
"v_mfma_f32_16x16x32_bf16 v[84:87], a[176:179], v[124:127], v[84:87]\n"
"s_add_u32 s98, s56, 0x400000\n"
"v_cmp_u_f32_e32 vcc, v76, v76\n"
"v_mfma_f32_16x16x32_bf16 v[84:87], a[180:183], v[108:111], v[84:87]\n"
"s_cbranch_vccnz .Lmk_p62s\n"
".Lmk_p62r:\n"
"v_mfma_f32_16x16x32_bf16 v[84:87], a[184:187], v[24:27], v[84:87]\n"
"ds_write_b128 v3, v[76:79] offset:12288\n"
"v_mfma_f32_16x16x32_bf16 v[84:87], a[188:191], v[28:31], v[84:87]\n"
"buffer_load_dwordx4 v[76:79], v0, s[4:7], s98 offen sc1\n"
"s_waitcnt lgkmcnt(1)\n"
"v_mfma_f32_16x16x32_bf16 v[100:103], a[192:195], v[80:83], 0\n"
"ds_read_b128 v[112:115], v4 offset:1024\n"
"ds_read_b128 v[128:131], v5 offset:1024\n"
"v_add_f32_e32 v92, v92, v255\n"
"v_mfma_f32_16x16x32_bf16 v[100:103], a[196:199], v[104:107], v[100:103]\n"
"v_add_f32_e32 v93, v93, v254\n"
"v_add_f32_e32 v94, v94, v253\n"
"v_add_f32_e32 v95, v95, v252\n"
"v_mfma_f32_16x16x32_bf16 v[100:103], a[200:203], v[116:119], v[100:103]\n"
"buffer_load_dwordx4 v[132:135], v0, s[4:7], s98 offen offset:1024 sc1\n"
"v_add_f32_e32 v92, v92, v96\n"
"v_mfma_f32_16x16x32_bf16 v[100:103], a[204:207], v[68:71], v[100:103]\n"
"v_add_f32_e32 v93, v93, v97\n"
"v_add_f32_e32 v94, v94, v98\n"
"v_add_f32_e32 v95, v95, v99\n"
"v_mfma_f32_16x16x32_bf16 v[100:103], a[208:211], v[124:127], v[100:103]\n"
"v_mfma_f32_16x16x32_bf16 v[100:103], a[212:215], v[108:111], v[100:103]\n"
"ds_write_b128 v3, v[84:87] offset:13312\n"
"v_mfma_f32_16x16x32_bf16 v[100:103], a[216:219], v[24:27], v[100:103]\n"
"buffer_load_dwordx4 v[84:87], v0, s[4:7], s98 offen offset:2048 sc1\n"
"s_waitcnt lgkmcnt(1)\n"
"v_mfma_f32_16x16x32_bf16 v[100:103], a[220:223], v[28:31], v[100:103]\n"
"ds_read_b128 v[96:99], v4 offset:2048\n"
"ds_read_b128 v[136:139], v5 offset:2048\n"
"v_add_f32_e32 v112, v112, v251\n"
"v_mfma_f32_16x16x32_bf16 v[140:143], a[224:227], v[80:83], 0\n"
"v_add_f32_e32 v113, v113, v250\n"
"v_add_f32_e32 v114, v114, v249\n"
"v_add_f32_e32 v115, v115, v248\n"
"v_mfma_f32_16x16x32_bf16 v[140:143], a[228:231], v[104:107], v[140:143]\n"
"buffer_load_dwordx4 v[80:83], v0, s[4:7], s98 offen offset:3072 sc1\n"
"v_add_f32_e32 v112, v112, v128\n"
"v_mfma_f32_16x16x32_bf16 v[140:143], a[232:235], v[116:119], v[140:143]\n"
"v_add_f32_e32 v113, v113, v129\n"
"v_add_f32_e32 v114, v114, v130\n"
"v_add_f32_e32 v115, v115, v131\n"
"v_mfma_f32_16x16x32_bf16 v[140:143], a[236:239], v[68:71], v[140:143]\n"
"v_mfma_f32_16x16x32_bf16 v[140:143], a[240:243], v[124:127], v[140:143]\n"
"buffer_load_dwordx4 v[68:71], v1, s[4:7], s98 offen sc1\n"
"s_waitcnt lgkmcnt(0)\n"
"v_mfma_f32_16x16x32_bf16 v[140:143], a[244:247], v[108:111], v[140:143]\n"
"ds_write_b128 v3, v[100:103] offset:14336\n"
"v_mfma_f32_16x16x32_bf16 v[140:143], a[248:251], v[24:27], v[140:143]\n"
"ds_read_b128 v[24:27], v4 offset:3072\n"
"ds_read_b128 v[100:103], v5 offset:3072\n"
"v_add_f32_e32 v96, v96, v247\n"
"v_mfma_f32_16x16x32_bf16 v[140:143], a[252:255], v[28:31], v[140:143]\n"
"s_waitcnt vmcnt(9)\n"
"buffer_load_dwordx4 v[28:31], v1, s[4:7], s98 offen offset:1024 sc1\n"
"; @B G2 b.3\n"
"v_mfma_f32_16x16x32_bf16 v[104:107], a[128:131], v[52:55], v[92:95]\n"
"v_add_f32_e32 v97, v97, v246\n"
"v_add_f32_e32 v98, v98, v245\n"
"v_add_f32_e32 v99, v99, v244\n"
"v_mfma_f32_16x16x32_bf16 v[104:107], a[132:135], v[56:59], v[104:107]\n"
"v_add_f32_e32 v96, v96, v136\n"
"v_add_f32_e32 v97, v97, v137\n"
"v_add_f32_e32 v98, v98, v138\n"
"v_mfma_f32_16x16x32_bf16 v[104:107], a[136:139], v[88:91], v[104:107]\n"
"buffer_load_dwordx4 v[108:111], v1, s[4:7], s98 offen offset:2048 sc1\n"
"v_add_f32_e32 v99, v99, v139\n"
"v_mfma_f32_16x16x32_bf16 v[104:107], a[140:143], v[48:51], v[104:107]\n"
"s_waitcnt vmcnt(7)\n"
"s_waitcnt lgkmcnt(0)\n"
"v_add_f32_e32 v24, v24, v243\n"
"v_add_f32_e32 v25, v25, v242\n"
"v_mfma_f32_16x16x32_bf16 v[104:107], a[144:147], v[120:123], v[104:107]\n"
"v_add_f32_e32 v26, v26, v241\n"
"v_add_f32_e32 v27, v27, v240\n"
"v_add_f32_e32 v24, v24, v100\n"
"v_mfma_f32_16x16x32_bf16 v[104:107], a[148:151], v[60:63], v[104:107]\n"
"ds_write_b128 v3, v[140:143] offset:15360\n"
"v_mfma_f32_16x16x32_bf16 v[104:107], a[152:155], v[64:67], v[104:107]\n"
"buffer_load_dwordx4 v[116:119], v1, s[4:7], s98 offen offset:3072 sc1\n"
"v_add_f32_e32 v25, v25, v101\n"
"v_mfma_f32_16x16x32_bf16 v[104:107], a[156:159], v[72:75], v[104:107]\n"
"s_add_u32 s98, s57, 0x400000\n"
"v_add_f32_e32 v26, v26, v102\n"
"v_add_f32_e32 v27, v27, v103\n"
"v_mfma_f32_16x16x32_bf16 v[100:103], a[160:163], v[52:55], v[112:115]\n"
"v_mfma_f32_16x16x32_bf16 v[100:103], a[164:167], v[56:59], v[100:103]\n"
"s_waitcnt lgkmcnt(0)\n"
"s_barrier\n"
"v_mfma_f32_16x16x32_bf16 v[100:103], a[168:171], v[88:91], v[100:103]\n"
"buffer_load_dwordx4 v[124:127], v0, s[4:7], s98 offen sc1\n"
"v_mfma_f32_16x16x32_bf16 v[100:103], a[172:175], v[48:51], v[100:103]\n"
"ds_read_b128 v[128:131], v6\n"
"ds_read_b128 v[136:139], v6 offset:1024\n"
"v_mfma_f32_16x16x32_bf16 v[100:103], a[176:179], v[120:123], v[100:103]\n"
"ds_read_b128 v[140:143], v6 offset:2048\n"
"v_cmp_u_f32_e32 vcc, v104, v104\n"
"v_mfma_f32_16x16x32_bf16 v[100:103], a[180:183], v[60:63], v[100:103]\n"
"s_cbranch_vccnz .Lmk_p63s\n"
".Lmk_p63r:\n"
"ds_read_b128 v[92:95], v6 offset:3072\n"
"v_mfma_f32_16x16x32_bf16 v[100:103], a[184:187], v[64:67], v[100:103]\n"
"buffer_load_dwordx4 v[112:115], v0, s[4:7], s98 offen offset:1024 sc1\n"
"v_mfma_f32_16x16x32_bf16 v[100:103], a[188:191], v[72:75], v[100:103]\n"
"v_mfma_f32_16x16x32_bf16 v[96:99], a[192:195], v[52:55], v[96:99]\n"
"v_mfma_f32_16x16x32_bf16 v[96:99], a[196:199], v[56:59], v[96:99]\n"
"buffer_load_dwordx4 v[144:147], v0, s[4:7], s98 offen offset:2048 sc1\n"
"v_mfma_f32_16x16x32_bf16 v[96:99], a[200:203], v[88:91], v[96:99]\n"
"v_mfma_f32_16x16x32_bf16 v[96:99], a[204:207], v[48:51], v[96:99]\n"
"v_mfma_f32_16x16x32_bf16 v[96:99], a[208:211], v[120:123], v[96:99]\n"
"buffer_load_dwordx4 v[148:151], v0, s[4:7], s98 offen offset:3072 sc1\n"
"s_waitcnt lgkmcnt(0)\n"
"v_mfma_f32_16x16x32_bf16 v[96:99], a[212:215], v[60:63], v[96:99]\n"
"v_add_f32_e32 v128, v104, v128\n"
"v_add_f32_e32 v129, v105, v129\n"
"v_add_f32_e32 v130, v106, v130\n"
"v_mfma_f32_16x16x32_bf16 v[96:99], a[216:219], v[64:67], v[96:99]\n"
"v_add_f32_e32 v131, v107, v131\n"
"v_add_f32_e32 v136, v100, v136\n"
"v_add_f32_e32 v137, v101, v137\n"
"v_mfma_f32_16x16x32_bf16 v[96:99], a[220:223], v[72:75], v[96:99]\n"
"buffer_load_dwordx4 v[104:107], v1, s[4:7], s98 offen sc1\n"
"v_add_f32_e32 v138, v102, v138\n"
"v_mfma_f32_16x16x32_bf16 v[24:27], a[224:227], v[52:55], v[24:27]\n"
"v_add_f32_e32 v139, v103, v139\n"
"v_cvt_pk_bf16_f32 v52, v128, v129\n"
"v_cvt_pk_bf16_f32 v53, v130, v131\n"
"v_mfma_f32_16x16x32_bf16 v[24:27], a[228:231], v[56:59], v[24:27]\n"
"v_cvt_pk_bf16_f32 v54, v136, v137\n"
"v_cvt_pk_bf16_f32 v55, v138, v139\n"
"v_mfma_f32_16x16x32_bf16 v[24:27], a[232:235], v[88:91], v[24:27]\n"
"buffer_load_dwordx4 v[56:59], v1, s[4:7], s98 offen offset:1024 sc1\n"
"v_mfma_f32_16x16x32_bf16 v[24:27], a[236:239], v[48:51], v[24:27]\n"
"v_mfma_f32_16x16x32_bf16 v[24:27], a[240:243], v[120:123], v[24:27]\n"
"v_mfma_f32_16x16x32_bf16 v[24:27], a[244:247], v[60:63], v[24:27]\n"
"buffer_load_dwordx4 v[48:51], v1, s[4:7], s98 offen offset:2048 sc1\n"
"v_mfma_f32_16x16x32_bf16 v[24:27], a[248:251], v[64:67], v[24:27]\n"
"v_mfma_f32_16x16x32_bf16 v[24:27], a[252:255], v[72:75], v[24:27]\n"
"buffer_load_dwordx4 v[60:63], v1, s[4:7], s98 offen offset:3072 sc1\n"
"s_waitcnt vmcnt(14)\n"
"; @B G2 a.0\n"
"v_mfma_f32_16x16x32_bf16 v[64:67], a[128:131], v[76:79], 0\n"
"s_add_u32 s98, s58, 0x400000\n"
"buffer_load_dwordx4 v[72:75], v0, s[4:7], s98 offen sc1\n"
"v_mfma_f32_16x16x32_bf16 v[64:67], a[132:135], v[132:135], v[64:67]\n"
"s_waitcnt vmcnt(13)\n"
"v_bfe_u32 v255, v0, 4, 4\n"
"v_bfe_u32 v254, v0, 9, 1\n"
"v_mfma_f32_16x16x32_bf16 v[64:67], a[136:139], v[84:87], v[64:67]\n"
"v_and_b32_e32 v253, 7, v255\n"
"v_xor_b32_e32 v254, v254, v253\n"
"v_mfma_f32_16x16x32_bf16 v[64:67], a[140:143], v[80:83], v[64:67]\n"
"s_waitcnt vmcnt(11)\n"
"buffer_load_dwordx4 v[88:91], v0, s[4:7], s98 offen offset:1024 sc1\n"
"v_mfma_f32_16x16x32_bf16 v[64:67], a[144:147], v[68:71], v[64:67]\n"
"v_lshlrev_b32_e32 v255, 7, v255\n"
"v_lshl_add_u32 v255, v254, 4, v255\n"
"v_mfma_f32_16x16x32_bf16 v[64:67], a[148:151], v[28:31], v[64:67]\n"
"s_waitcnt vmcnt(10)\n"
"v_bfe_u32 v254, v0, 8, 1\n"
"v_lshl_add_u32 v255, v254, 3, v255\n"
"v_mfma_f32_16x16x32_bf16 v[64:67], a[152:155], v[108:111], v[64:67]\n"
"buffer_load_dwordx4 v[100:103], v0, s[4:7], s98 offen offset:2048 sc1\n"
"v_mfma_f32_16x16x32_bf16 v[64:67], a[156:159], v[116:119], v[64:67]\n"
"v_add_u32_e32 v255, s77, v255\n"
"v_bfe_u32 v253, v0, 7, 3\n"
"v_mfma_f32_16x16x32_bf16 v[120:123], a[160:163], v[76:79], 0\n"
"v_bfe_u32 v254, v0, 4, 3\n"
"v_xor_b32_e32 v254, v254, v253\n"
"v_mfma_f32_16x16x32_bf16 v[120:123], a[164:167], v[132:135], v[120:123]\n"
"buffer_load_dwordx4 v[128:131], v0, s[4:7], s98 offen offset:3072 sc1\n"
"v_mfma_f32_16x16x32_bf16 v[120:123], a[168:171], v[84:87], v[120:123]\n"
"v_lshlrev_b32_e32 v253, 11, v253\n"
"v_lshl_add_u32 v253, v254, 4, v253\n"
"v_mfma_f32_16x16x32_bf16 v[120:123], a[172:175], v[80:83], v[120:123]\n"
"v_add_f32_e32 v140, v96, v140\n"
"v_add_f32_e32 v141, v97, v141\n"
"v_mfma_f32_16x16x32_bf16 v[120:123], a[176:179], v[68:71], v[120:123]\n"
"v_cmp_u_f32_e32 vcc, v64, v64\n"
"v_add_f32_e32 v142, v98, v142\n"
"v_mfma_f32_16x16x32_bf16 v[120:123], a[180:183], v[28:31], v[120:123]\n"
"s_cbranch_vccnz .Lmk_p64s\n"
".Lmk_p64r:\n"
"v_add_f32_e32 v143, v99, v143\n"
"v_mfma_f32_16x16x32_bf16 v[120:123], a[184:187], v[108:111], v[120:123]\n"
"ds_write_b128 v3, v[64:67]\n"
"v_mfma_f32_16x16x32_bf16 v[120:123], a[188:191], v[116:119], v[120:123]\n"
"buffer_load_dwordx4 v[64:67], v1, s[4:7], s98 offen sc1\n"
"v_mfma_f32_16x16x32_bf16 v[96:99], a[192:195], v[76:79], 0\n"
"v_add_f32_e32 v92, v24, v92\n"
"v_add_f32_e32 v93, v25, v93\n"
"v_mfma_f32_16x16x32_bf16 v[96:99], a[196:199], v[132:135], v[96:99]\n"
"v_add_f32_e32 v94, v26, v94\n"
"v_add_f32_e32 v95, v27, v95\n"
"v_mfma_f32_16x16x32_bf16 v[96:99], a[200:203], v[84:87], v[96:99]\n"
"buffer_load_dwordx4 v[24:27], v1, s[4:7], s98 offen offset:1024 sc1\n"
"v_mfma_f32_16x16x32_bf16 v[96:99], a[204:207], v[80:83], v[96:99]\n"
"v_cvt_pk_bf16_f32 v136, v140, v141\n"
"v_cvt_pk_bf16_f32 v137, v142, v143\n"
"v_mfma_f32_16x16x32_bf16 v[96:99], a[208:211], v[68:71], v[96:99]\n"
"v_cvt_pk_bf16_f32 v138, v92, v93\n"
"v_cvt_pk_bf16_f32 v139, v94, v95\n"
"v_mfma_f32_16x16x32_bf16 v[96:99], a[212:215], v[28:31], v[96:99]\n"
"ds_write_b128 v3, v[120:123] offset:1024\n"
"v_mfma_f32_16x16x32_bf16 v[96:99], a[216:219], v[108:111], v[96:99]\n"
"buffer_load_dwordx4 v[92:95], v1, s[4:7], s98 offen offset:2048 sc1\n"
"v_mfma_f32_16x16x32_bf16 v[96:99], a[220:223], v[116:119], v[96:99]\n"
"v_mov_b32_e32 v254, v255\n"
"v_mfma_f32_16x16x32_bf16 v[120:123], a[224:227], v[76:79], 0\n"
"ds_write_b64 v254, v[52:53] offset:8192\n"
"v_mfma_f32_16x16x32_bf16 v[120:123], a[228:231], v[132:135], v[120:123]\n"
"buffer_load_dwordx4 v[76:79], v1, s[4:7], s98 offen offset:3072 sc1\n"
"v_mfma_f32_16x16x32_bf16 v[120:123], a[232:235], v[84:87], v[120:123]\n"
"s_add_u32 s98, s59, 0x400000\n"
"v_xor_b32_e32 v254, 32, v255\n"
"v_mfma_f32_16x16x32_bf16 v[120:123], a[236:239], v[80:83], v[120:123]\n"
"ds_write_b64 v254, v[54:55] offset:8192\n"
"v_mfma_f32_16x16x32_bf16 v[120:123], a[240:243], v[68:71], v[120:123]\n"
"buffer_load_dwordx4 v[52:55], v0, s[4:7], s98 offen sc1\n"
"v_mfma_f32_16x16x32_bf16 v[120:123], a[244:247], v[28:31], v[120:123]\n"
"ds_write_b128 v3, v[96:99] offset:2048\n"
"v_mfma_f32_16x16x32_bf16 v[120:123], a[248:251], v[108:111], v[120:123]\n"
"v_xor_b32_e32 v254, 64, v255\n"
"v_mfma_f32_16x16x32_bf16 v[120:123], a[252:255], v[116:119], v[120:123]\n"
"s_waitcnt vmcnt(13)\n"
"buffer_load_dwordx4 v[28:31], v0, s[4:7], s98 offen offset:1024 sc1\n"
"; @B G2 a.1\n"
"v_mfma_f32_16x16x32_bf16 v[68:71], a[128:131], v[124:127], 0\n"
"ds_write_b64 v254, v[136:137] offset:8192\n"
"v_mfma_f32_16x16x32_bf16 v[68:71], a[132:135], v[112:115], v[68:71]\n"
"v_xor_b32_e32 v254, 0x60, v255\n"
"v_mfma_f32_16x16x32_bf16 v[68:71], a[136:139], v[144:147], v[68:71]\n"
"buffer_load_dwordx4 v[80:83], v0, s[4:7], s98 offen offset:2048 sc1\n"
"v_mfma_f32_16x16x32_bf16 v[68:71], a[140:143], v[148:151], v[68:71]\n"
"s_waitcnt vmcnt(11)\n"
"ds_write_b64 v254, v[138:139] offset:8192\n"
"v_mfma_f32_16x16x32_bf16 v[68:71], a[144:147], v[104:107], v[68:71]\n"
"ds_read_b128 v[84:87], v2 offset:8192\n"
"ds_read_b128 v[96:99], v2 offset:9216\n"
"v_mfma_f32_16x16x32_bf16 v[68:71], a[148:151], v[56:59], v[68:71]\n"
"ds_write_b128 v3, v[120:123] offset:3072\n"
"v_mfma_f32_16x16x32_bf16 v[68:71], a[152:155], v[48:51], v[68:71]\n"
"buffer_load_dwordx4 v[108:111], v0, s[4:7], s98 offen offset:3072 sc1\n"
"v_mfma_f32_16x16x32_bf16 v[68:71], a[156:159], v[60:63], v[68:71]\n"
"v_mfma_f32_16x16x32_bf16 v[116:119], a[160:163], v[124:127], 0\n"
"v_mfma_f32_16x16x32_bf16 v[116:119], a[164:167], v[112:115], v[116:119]\n"
"buffer_load_dwordx4 v[120:123], v1, s[4:7], s98 offen sc1\n"
"v_mfma_f32_16x16x32_bf16 v[116:119], a[168:171], v[144:147], v[116:119]\n"
"s_waitcnt lgkmcnt(1)\n"
"v_mfma_f32_16x16x32_bf16 v[116:119], a[172:175], v[148:151], v[116:119]\n"
"v_mfma_f32_16x16x32_bf16 v[116:119], a[176:179], v[104:107], v[116:119]\n"
"s_nop 0\n"
"v_cmp_u_f32_e32 vcc, v68, v68\n"
"v_mfma_f32_16x16x32_bf16 v[116:119], a[180:183], v[56:59], v[116:119]\n"
"s_cbranch_vccnz .Lmk_p65s\n"
".Lmk_p65r:\n"
"v_mfma_f32_16x16x32_bf16 v[116:119], a[184:187], v[48:51], v[116:119]\n"
"ds_write_b128 v3, v[68:71] offset:6144\n"
"v_mfma_f32_16x16x32_bf16 v[116:119], a[188:191], v[60:63], v[116:119]\n"
"buffer_load_dwordx4 v[68:71], v1, s[4:7], s98 offen offset:1024 sc1\n"
"v_mfma_f32_16x16x32_bf16 v[132:135], a[192:195], v[124:127], 0\n"
"v_mfma_f32_16x16x32_bf16 v[132:135], a[196:199], v[112:115], v[132:135]\n"
"v_mfma_f32_16x16x32_bf16 v[132:135], a[200:203], v[144:147], v[132:135]\n"
"buffer_load_dwordx4 v[136:139], v1, s[4:7], s98 offen offset:2048 sc1\n"
"v_mfma_f32_16x16x32_bf16 v[132:135], a[204:207], v[148:151], v[132:135]\n"
"v_mfma_f32_16x16x32_bf16 v[132:135], a[208:211], v[104:107], v[132:135]\n"
"ds_read_b128 v[140:143], v2\n"
"ds_read_b128 v[152:155], v2 offset:1024\n"
"v_mfma_f32_16x16x32_bf16 v[132:135], a[212:215], v[56:59], v[132:135]\n"
"ds_write_b128 v3, v[116:119] offset:7168\n"
"v_mfma_f32_16x16x32_bf16 v[132:135], a[216:219], v[48:51], v[132:135]\n"
"buffer_load_dwordx4 v[116:119], v1, s[4:7], s98 offen offset:3072 sc1\n"
"v_mfma_f32_16x16x32_bf16 v[132:135], a[220:223], v[60:63], v[132:135]\n"
"v_mfma_f32_16x16x32_bf16 v[156:159], a[224:227], v[124:127], 0\n"
"v_mfma_f32_16x16x32_bf16 v[156:159], a[228:231], v[112:115], v[156:159]\n"
"s_waitcnt lgkmcnt(1)\n"
"v_lshlrev_b32_e32 v255, 16, v140\n"
"v_and_b32_e32 v254, 0xffff0000, v140\n"
"v_lshlrev_b32_e32 v252, 16, v141\n"
"v_mfma_f32_16x16x32_bf16 v[156:159], a[232:235], v[144:147], v[156:159]\n"
"v_and_b32_e32 v251, 0xffff0000, v141\n"
"v_lshlrev_b32_e32 v250, 16, v142\n"
"v_and_b32_e32 v249, 0xffff0000, v142\n"
"v_mfma_f32_16x16x32_bf16 v[156:159], a[236:239], v[148:151], v[156:159]\n"
"v_lshlrev_b32_e32 v248, 16, v143\n"
"v_and_b32_e32 v247, 0xffff0000, v143\n"
"v_lshlrev_b32_e32 v246, 16, v152\n"
"v_mfma_f32_16x16x32_bf16 v[156:159], a[240:243], v[104:107], v[156:159]\n"
"s_add_u32 s98, s74, 0x40000\n"
"buffer_store_dwordx4 v[84:87], v253, s[16:19], s98 offen sc1\n"
"v_mfma_f32_16x16x32_bf16 v[156:159], a[244:247], v[56:59], v[156:159]\n"
"ds_write_b128 v3, v[132:135] offset:8192\n"
"v_mfma_f32_16x16x32_bf16 v[156:159], a[248:251], v[48:51], v[156:159]\n"
"v_and_b32_e32 v245, 0xffff0000, v152\n"
"v_lshlrev_b32_e32 v244, 16, v153\n"
"v_and_b32_e32 v243, 0xffff0000, v153\n"
"v_mfma_f32_16x16x32_bf16 v[156:159], a[252:255], v[60:63], v[156:159]\n"
"s_waitcnt vmcnt(13)\n"
"v_lshlrev_b32_e32 v242, 16, v154\n"
"v_and_b32_e32 v241, 0xffff0000, v154\n"
"v_lshlrev_b32_e32 v240, 16, v155\n"
"; @B G2 a.2\n"
"v_mfma_f32_16x16x32_bf16 v[48:51], a[128:131], v[72:75], 0\n"
"v_and_b32_e32 v239, 0xffff0000, v155\n"
"v_mfma_f32_16x16x32_bf16 v[48:51], a[132:135], v[88:91], v[48:51]\n"
"v_mfma_f32_16x16x32_bf16 v[48:51], a[136:139], v[100:103], v[48:51]\n"
"v_mfma_f32_16x16x32_bf16 v[48:51], a[140:143], v[128:131], v[48:51]\n"
"s_waitcnt vmcnt(9)\n"
"v_mfma_f32_16x16x32_bf16 v[48:51], a[144:147], v[64:67], v[48:51]\n"
"v_mfma_f32_16x16x32_bf16 v[48:51], a[148:151], v[24:27], v[48:51]\n"
"ds_write_b128 v3, v[156:159] offset:9216\n"
"v_mfma_f32_16x16x32_bf16 v[48:51], a[152:155], v[92:95], v[48:51]\n"
"s_add_u32 s98, s74, 0x44000\n"
"v_mfma_f32_16x16x32_bf16 v[48:51], a[156:159], v[76:79], v[48:51]\n"
"v_mfma_f32_16x16x32_bf16 v[56:59], a[160:163], v[72:75], 0\n"
"buffer_store_dwordx4 v[96:99], v253, s[16:19], s98 offen sc1\n"
"v_mfma_f32_16x16x32_bf16 v[56:59], a[164:167], v[88:91], v[56:59]\n"
"s_waitcnt lgkmcnt(0)\n"
"s_barrier\n"
"v_mfma_f32_16x16x32_bf16 v[56:59], a[168:171], v[100:103], v[56:59]\n"
"ds_read_b128 v[60:63], v4\n"
"ds_read_b128 v[84:87], v5\n"
"v_mfma_f32_16x16x32_bf16 v[56:59], a[172:175], v[128:131], v[56:59]\n"
"v_mfma_f32_16x16x32_bf16 v[56:59], a[176:179], v[64:67], v[56:59]\n"
"v_cmp_u_f32_e32 vcc, v48, v48\n"
"v_mfma_f32_16x16x32_bf16 v[56:59], a[180:183], v[24:27], v[56:59]\n"
"s_cbranch_vccnz .Lmk_p66s\n"
".Lmk_p66r:\n"
"v_mfma_f32_16x16x32_bf16 v[56:59], a[184:187], v[92:95], v[56:59]\n"
"ds_write_b128 v3, v[48:51] offset:12288\n"
"v_mfma_f32_16x16x32_bf16 v[56:59], a[188:191], v[76:79], v[56:59]\n"
"s_waitcnt lgkmcnt(1)\n"
"v_add_f32_e32 v60, v60, v255\n"
"v_add_f32_e32 v61, v61, v254\n"
"v_add_f32_e32 v62, v62, v252\n"
"v_mfma_f32_16x16x32_bf16 v[48:51], a[192:195], v[72:75], 0\n"
"ds_read_b128 v[96:99], v4 offset:1024\n"
"ds_read_b128 v[104:107], v5 offset:1024\n"
"v_add_f32_e32 v63, v63, v251\n"
"v_mfma_f32_16x16x32_bf16 v[48:51], a[196:199], v[88:91], v[48:51]\n"
"v_add_f32_e32 v60, v60, v84\n"
"v_add_f32_e32 v61, v61, v85\n"
"v_add_f32_e32 v62, v62, v86\n"
"v_mfma_f32_16x16x32_bf16 v[48:51], a[200:203], v[100:103], v[48:51]\n"
"v_add_f32_e32 v63, v63, v87\n"
"v_mfma_f32_16x16x32_bf16 v[48:51], a[204:207], v[128:131], v[48:51]\n"
"v_mfma_f32_16x16x32_bf16 v[48:51], a[208:211], v[64:67], v[48:51]\n"
"v_mfma_f32_16x16x32_bf16 v[48:51], a[212:215], v[24:27], v[48:51]\n"
"ds_write_b128 v3, v[56:59] offset:13312\n"
"v_mfma_f32_16x16x32_bf16 v[48:51], a[216:219], v[92:95], v[48:51]\n"
"ds_read_b128 v[56:59], v4 offset:2048\n"
"ds_read_b128 v[84:87], v5 offset:2048\n"
"s_waitcnt lgkmcnt(3)\n"
"v_mfma_f32_16x16x32_bf16 v[48:51], a[220:223], v[76:79], v[48:51]\n"
"v_add_f32_e32 v96, v96, v250\n"
"v_add_f32_e32 v97, v97, v249\n"
"v_add_f32_e32 v98, v98, v248\n"
"v_mfma_f32_16x16x32_bf16 v[112:115], a[224:227], v[72:75], 0\n"
"v_add_f32_e32 v99, v99, v247\n"
"v_add_f32_e32 v96, v96, v104\n"
"v_add_f32_e32 v97, v97, v105\n"
"v_mfma_f32_16x16x32_bf16 v[112:115], a[228:231], v[88:91], v[112:115]\n"
"v_add_f32_e32 v98, v98, v106\n"
"v_add_f32_e32 v99, v99, v107\n"
"v_mfma_f32_16x16x32_bf16 v[112:115], a[232:235], v[100:103], v[112:115]\n"
"v_mfma_f32_16x16x32_bf16 v[112:115], a[236:239], v[128:131], v[112:115]\n"
"s_waitcnt lgkmcnt(0)\n"
"v_add_f32_e32 v56, v56, v246\n"
"v_add_f32_e32 v57, v57, v245\n"
"v_add_f32_e32 v58, v58, v244\n"
"v_mfma_f32_16x16x32_bf16 v[112:115], a[240:243], v[64:67], v[112:115]\n"
"ds_read_b128 v[64:67], v4 offset:3072\n"
"ds_read_b128 v[72:75], v5 offset:3072\n"
"v_add_f32_e32 v59, v59, v243\n"
"v_mfma_f32_16x16x32_bf16 v[112:115], a[244:247], v[24:27], v[112:115]\n"
"ds_write_b128 v3, v[48:51] offset:14336\n"
"v_mfma_f32_16x16x32_bf16 v[112:115], a[248:251], v[92:95], v[112:115]\n"
"v_add_f32_e32 v56, v56, v84\n"
"v_add_f32_e32 v57, v57, v85\n"
"v_add_f32_e32 v58, v58, v86\n"
"v_mfma_f32_16x16x32_bf16 v[112:115], a[252:255], v[76:79], v[112:115]\n"
"s_waitcnt vmcnt(6)\n"
"v_add_f32_e32 v59, v59, v87\n"
"; @B G2 a.3\n"
"v_mfma_f32_16x16x32_bf16 v[24:27], a[128:131], v[52:55], v[60:63]\n"
"v_mfma_f32_16x16x32_bf16 v[24:27], a[132:135], v[28:31], v[24:27]\n"
"s_waitcnt lgkmcnt(1)\n"
"v_add_f32_e32 v64, v64, v242\n"
"v_add_f32_e32 v65, v65, v241\n"
"v_add_f32_e32 v66, v66, v240\n"
"v_mfma_f32_16x16x32_bf16 v[24:27], a[136:139], v[80:83], v[24:27]\n"
"v_add_f32_e32 v67, v67, v239\n"
"v_add_f32_e32 v64, v64, v72\n"
"v_add_f32_e32 v65, v65, v73\n"
"v_mfma_f32_16x16x32_bf16 v[24:27], a[140:143], v[108:111], v[24:27]\n"
"s_waitcnt vmcnt(2)\n"
"v_add_f32_e32 v66, v66, v74\n"
"v_add_f32_e32 v67, v67, v75\n"
"v_mfma_f32_16x16x32_bf16 v[24:27], a[144:147], v[120:123], v[24:27]\n"
"v_mfma_f32_16x16x32_bf16 v[24:27], a[148:151], v[68:71], v[24:27]\n"
"ds_write_b128 v3, v[112:115] offset:15360\n"
"v_mfma_f32_16x16x32_bf16 v[24:27], a[152:155], v[136:139], v[24:27]\n"
"v_mfma_f32_16x16x32_bf16 v[24:27], a[156:159], v[116:119], v[24:27]\n"
"v_mfma_f32_16x16x32_bf16 v[48:51], a[160:163], v[52:55], v[96:99]\n"
"v_mfma_f32_16x16x32_bf16 v[48:51], a[164:167], v[28:31], v[48:51]\n"
"s_waitcnt lgkmcnt(0)\n"
"s_barrier\n"
"v_mfma_f32_16x16x32_bf16 v[48:51], a[168:171], v[80:83], v[48:51]\n"
"ds_read_b128 v[72:75], v6\n"
"ds_read_b128 v[76:79], v6 offset:1024\n"
"v_mfma_f32_16x16x32_bf16 v[48:51], a[172:175], v[108:111], v[48:51]\n"
"ds_read_b128 v[84:87], v6 offset:2048\n"
"ds_read_b128 v[88:91], v6 offset:3072\n"
"v_mfma_f32_16x16x32_bf16 v[48:51], a[176:179], v[120:123], v[48:51]\n"
"v_cmp_u_f32_e32 vcc, v24, v24\n"
"v_mfma_f32_16x16x32_bf16 v[48:51], a[180:183], v[68:71], v[48:51]\n"
"s_cbranch_vccnz .Lmk_p67s\n"
".Lmk_p67r:\n"
"v_mfma_f32_16x16x32_bf16 v[48:51], a[184:187], v[136:139], v[48:51]\n"
"v_mfma_f32_16x16x32_bf16 v[48:51], a[188:191], v[116:119], v[48:51]\n"
"v_mfma_f32_16x16x32_bf16 v[56:59], a[192:195], v[52:55], v[56:59]\n"
"v_mfma_f32_16x16x32_bf16 v[56:59], a[196:199], v[28:31], v[56:59]\n"
"v_mfma_f32_16x16x32_bf16 v[56:59], a[200:203], v[80:83], v[56:59]\n"
"v_mfma_f32_16x16x32_bf16 v[56:59], a[204:207], v[108:111], v[56:59]\n"
"v_mfma_f32_16x16x32_bf16 v[56:59], a[208:211], v[120:123], v[56:59]\n"
"s_waitcnt lgkmcnt(0)\n"
"v_add_f32_e32 v72, v24, v72\n"
"v_add_f32_e32 v73, v25, v73\n"
"v_add_f32_e32 v74, v26, v74\n"
"v_mfma_f32_16x16x32_bf16 v[56:59], a[212:215], v[68:71], v[56:59]\n"
"v_add_f32_e32 v75, v27, v75\n"
"v_add_f32_e32 v76, v48, v76\n"
"v_add_f32_e32 v77, v49, v77\n"
"v_mfma_f32_16x16x32_bf16 v[56:59], a[216:219], v[136:139], v[56:59]\n"
"v_add_f32_e32 v78, v50, v78\n"
"v_add_f32_e32 v79, v51, v79\n"
"v_cvt_pk_bf16_f32 v24, v72, v73\n"
"v_mfma_f32_16x16x32_bf16 v[56:59], a[220:223], v[116:119], v[56:59]\n"
"v_cvt_pk_bf16_f32 v25, v74, v75\n"
"v_cvt_pk_bf16_f32 v26, v76, v77\n"
"v_cvt_pk_bf16_f32 v27, v78, v79\n"
"v_mfma_f32_16x16x32_bf16 v[64:67], a[224:227], v[52:55], v[64:67]\n"
"v_mfma_f32_16x16x32_bf16 v[64:67], a[228:231], v[28:31], v[64:67]\n"
"v_mfma_f32_16x16x32_bf16 v[64:67], a[232:235], v[80:83], v[64:67]\n"
"v_mfma_f32_16x16x32_bf16 v[64:67], a[236:239], v[108:111], v[64:67]\n"
"v_mfma_f32_16x16x32_bf16 v[64:67], a[240:243], v[120:123], v[64:67]\n"
"v_mfma_f32_16x16x32_bf16 v[64:67], a[244:247], v[68:71], v[64:67]\n"
"v_mfma_f32_16x16x32_bf16 v[64:67], a[248:251], v[136:139], v[64:67]\n"
"v_mfma_f32_16x16x32_bf16 v[64:67], a[252:255], v[116:119], v[64:67]\n"
"v_bfe_u32 v255, v0, 4, 4\n"
"v_bfe_u32 v254, v0, 9, 1\n"
"v_and_b32_e32 v253, 7, v255\n"
"v_xor_b32_e32 v254, v254, v253\n"
"v_lshlrev_b32_e32 v255, 7, v255\n"
"v_lshl_add_u32 v255, v254, 4, v255\n"
"v_bfe_u32 v254, v0, 8, 1\n"
"v_lshl_add_u32 v255, v254, 3, v255\n"
"v_add_u32_e32 v255, s77, v255\n"
"v_bfe_u32 v253, v0, 7, 3\n"
"v_bfe_u32 v254, v0, 4, 3\n"
"v_xor_b32_e32 v254, v254, v253\n"
"v_lshlrev_b32_e32 v253, 11, v253\n"
"v_lshl_add_u32 v253, v254, 4, v253\n"
"v_add_f32_e32 v84, v56, v84\n"
"v_add_f32_e32 v85, v57, v85\n"
"v_add_f32_e32 v86, v58, v86\n"
"v_add_f32_e32 v87, v59, v87\n"
"v_add_f32_e32 v88, v64, v88\n"
"v_add_f32_e32 v89, v65, v89\n"
"v_add_f32_e32 v90, v66, v90\n"
"v_add_f32_e32 v91, v67, v91\n"
"v_cvt_pk_bf16_f32 v28, v84, v85\n"
"v_cvt_pk_bf16_f32 v29, v86, v87\n"
"v_cvt_pk_bf16_f32 v30, v88, v89\n"
"v_cvt_pk_bf16_f32 v31, v90, v91\n"
"v_mov_b32_e32 v254, v255\n"
"ds_write_b64 v254, v[24:25] offset:0\n"
"v_xor_b32_e32 v254, 32, v255\n"
"ds_write_b64 v254, v[26:27] offset:0\n"
"v_xor_b32_e32 v254, 64, v255\n"
"ds_write_b64 v254, v[28:29] offset:0\n"
"v_xor_b32_e32 v254, 0x60, v255\n"
"ds_write_b64 v254, v[30:31] offset:0\n"
"ds_read_b128 v[24:27], v2\n"
"ds_read_b128 v[28:31], v2 offset:1024\n"
"s_waitcnt lgkmcnt(0)\n"
"s_nop 0\n"
"buffer_store_dwordx4 v[24:27], v253, s[16:19], s74 offen sc1\n"
"s_add_u32 s98, s74, 0x4000\n"
"buffer_store_dwordx4 v[28:31], v253, s[16:19], s98 offen sc1\n"
"s_cmp_eq_u32 s81, 0\n"
"s_cbranch_scc1 .Lmk_fin\n"
"buffer_store_dwordx2 v[18:19], v11, s[4:7], s81 offen sc1\n"
".Lmk_fin:\n"
"s_endpgm\n"
".Lmk_end:\n"
    :: "{s[4:7]}"(wsr), "{s[8:11]}"(wsr), "{s[12:15]}"(wsk), "{s[16:19]}"(outr), "{s[52:67]}"(sa), "{s[68:83]}"(sb),
       "{s[20:27]}"(pa), "{s[28:31]}"(wa), "{s[32:35]}"(wb), "{s[36:39]}"(oa), "{s[40:43]}"(ob), "{s[44:51]}"(pc), "{s99}"(uni((bix * 4 + w) * 1024)),
       "{v[0:15]}"(va), "{v[16:23]}"(vb), "{v[32:47]}"(pz), "{v48}"(sta), "{v50}"(pf)
    : "memory", "v255", "a255", "s98", "vcc");
  __builtin_unreachable();
}

}

extern "C" void kernel_launch(void* const* d_in, const int* in_sizes, int n_in, void* d_out, int out_size,
                              void* d_ws, size_t ws_size, hipStream_t stream) {
  (void)hipFuncSetAttribute((const void*)mega_kernel, hipFuncAttributeMaxDynamicSharedMemorySize, LDS_BYTES);
  mega_kernel<<<256, 256, LDS_BYTES, stream>>>((const u16*)d_in[0], (const u16*)d_in[1], (const u16*)d_in[2],
                                               (const u16*)d_in[4], (const u16*)d_in[5], (u16*)d_out, (char*)d_ws);
}
```

```cpp
#include <hip/hip_runtime.h>
#include <stdint.h>

namespace {

typedef unsigned short u16;
typedef unsigned long long u64;
typedef short s16x4 __attribute__((ext_vector_type(4)));
typedef float f32x4 __attribute__((ext_vector_type(4)));
typedef unsigned u32x4 __attribute__((ext_vector_type(4)));
typedef unsigned u32x2 __attribute__((ext_vector_type(2)));
typedef unsigned u32x8 __attribute__((ext_vector_type(8)));
typedef unsigned u32x16 __attribute__((ext_vector_type(16)));
typedef __amdgpu_buffer_rsrc_t rsrc_t;
#define LAS __attribute__((address_space(3)))
#define DI __device__ __forceinline__
#define LDSP(a) ((LAS char*)(size_t)(a))

constexpr unsigned MB = 1u << 20;
constexpr unsigned WS_XF = 0;
constexpr unsigned WS_OF = 16 * MB;
constexpr unsigned WS_KV = 32 * MB;
constexpr unsigned WS_WF = 64 * MB;
constexpr unsigned WS_WO = 76 * MB;
constexpr unsigned WS_ST = 80 * MB;
constexpr unsigned WS_CS = 81 * MB;
constexpr unsigned WS_FL = 82 * MB;
constexpr unsigned WS_EP = WS_FL + 2048 * 128;
constexpr unsigned WS_DBG = 96 * MB;
constexpr unsigned OOB = 0x80000000u;
constexpr int L_RES = 0, L_K = 16384, L_V = 49152, L_INB = 81920, L_CS = 155648, L_PM = L_CS + 1024, L_MB = L_PM + 2048, L_SCR = L_MB + 2048;
constexpr int L_WS = 57344;
constexpr int LDS_BYTES = 163840;

template <int N> DI void wait_vm() { asm volatile("s_waitcnt vmcnt(%0)" ::"n"(N) : "memory"); }
DI void lds_barrier() { asm volatile("s_waitcnt lgkmcnt(0)\n\ts_barrier" ::: "memory"); }
template <int N> DI void nops() { asm volatile("s_nop %0" ::"n"(N - 1)); }
template <int OFF> DI void ldv(u32x4& d, rsrc_t r, unsigned v, unsigned s) {
  asm volatile("buffer_load_dwordx4 %0, %1, %2, %3 offen offset:%4 sc1" : "=v"(d) : "v"(v), "s"(r), "s"(s), "i"(OFF) : "memory");
}
template <int OFF> DI void ld8(u32x2& d, rsrc_t r, unsigned v, unsigned s) {
  asm volatile("buffer_load_dwordx2 %0, %1, %2, %3 offen offset:%4 sc1" : "=v"(d) : "v"(v), "s"(r), "s"(s), "i"(OFF) : "memory");
}
template <int OFF> DI void st16(const u32x4& d, rsrc_t r, unsigned v, unsigned s) {
  asm volatile("buffer_store_dwordx4 %0, %1, %2, %3 offen offset:%4 sc1\n\ts_nop 1" :: "v"(d), "v"(v), "s"(r), "s"(s), "i"(OFF) : "memory");
}
template <int OFF> DI void st8(const u32x2& d, rsrc_t r, unsigned v, unsigned s) {
  asm volatile("buffer_store_dwordx2 %0, %1, %2, %3 offen offset:%4 sc1" :: "v"(d), "v"(v), "s"(r), "s"(s), "i"(OFF) : "memory");
}
DI void st4(unsigned d, rsrc_t r, unsigned v, unsigned s) {
  asm volatile("buffer_store_dword %0, %1, %2, %3 offen sc1" :: "v"(d), "v"(v), "s"(r), "s"(s) : "memory");
}
DI void dma(rsrc_t r, unsigned v, unsigned s, unsigned lm) {
  asm volatile("s_mov_b32 m0, %3\n\ts_nop 0\n\tbuffer_load_dwordx4 %0, %1, %2 offen lds" :: "v"(v), "s"(r), "s"(s), "s"(lm) : "memory");
}
DI void touch(rsrc_t r, unsigned v) {
  asm volatile("s_mov_b32 m0, %2\n\ts_nop 0\n\tbuffer_load_dword %0, %1, 0 offen lds" :: "v"(v), "s"(r), "s"((unsigned)L_SCR) : "memory");
}
DI void mma_v(f32x4& c, const u32x4& a, const u32x4& b) { asm volatile("v_mfma_f32_16x16x32_bf16 %0, %1, %2, %0" : "+v"(c) : "v"(a), "v"(b)); }
DI void mm0_v(f32x4& c, const u32x4& a, const u32x4& b) { asm volatile("v_mfma_f32_16x16x32_bf16 %0, %1, %2, 0" : "=v"(c) : "v"(a), "v"(b)); }
DI unsigned uni(unsigned v) { v = __builtin_amdgcn_readfirstlane(v); asm volatile("" : "+s"(v)); return v; }
template <class T> DI void pin(T& v) { asm volatile("" : "+v"(v)); }
DI float sum_x16(float v) {
  auto r = __builtin_amdgcn_permlane16_swap(__float_as_uint(v), __float_as_uint(v), false, false);
  return __uint_as_float(r[0]) + __uint_as_float(r[1]);
}
DI float sum_x32(float v) {
  auto r = __builtin_amdgcn_permlane32_swap(__float_as_uint(v), __float_as_uint(v), false, false);
  return __uint_as_float(r[0]) + __uint_as_float(r[1]);
}
DI u32x4 swap16(const u32x4& x) {
  auto r0 = __builtin_amdgcn_permlane16_swap(x[0], x[2], false, false);
  auto r1 = __builtin_amdgcn_permlane16_swap(x[1], x[3], false, false);
  return (u32x4){(unsigned)r0[0], (unsigned)r1[0], (unsigned)r0[1], (unsigned)r1[1]};
}
DI u32x2 lds_tr(unsigned a) { return __builtin_bit_cast(u32x2, __builtin_amdgcn_ds_read_tr16_b64_v4i16((LAS s16x4*)LDSP(a))); }
DI u32x4 cat(const u32x2& a, const u32x2& b) { return __builtin_shufflevector(a, b, 0, 1, 2, 3); }
DI u32x2 lds_r2(unsigned a) { return *(LAS u32x2*)LDSP(a); }
DI void lds_w4(unsigned a, const u32x4& v) { *(LAS u32x4*)LDSP(a) = v; }
DI unsigned rot(unsigned g, unsigned sl) { return (g + (0x641u >> 4 * sl)) & 15; }
DI int swzK(int u) { return ((((u >> 1) ^ u) & 1) << 1) | (u >> 1); }

#define STAMP(i)

__global__ __launch_bounds__(256, 1) void mega_kernel(const u16* __restrict__ x0, const u16* __restrict__ WqA,
                                                      const u16* __restrict__ WoA, const u16* __restrict__ WqB,
                                                      const u16* __restrict__ WoB, u16* __restrict__ out, char* ws) {
  const unsigned bid = blockIdx.x, xcd = bid & 7, bix = bid >> 3;
  const unsigned w = __builtin_amdgcn_readfirstlane(threadIdx.x >> 6), lane = threadIdx.x & 63;
  const unsigned c = (xcd >> 2) * 4 + (bix >> 3), h = (xcd & 3) * 8 + (bix & 7), par = h & 1, g = h >> 1;
  const unsigned p2 = h >> 3 & 1, g2 = (h >> 4) * 8 + (h & 7);
  const unsigned fr = lane & 15, fq = lane >> 4;
  const unsigned dl = 0, wr = (w + dl) & 3;
  const rsrc_t wsr = __builtin_amdgcn_make_buffer_rsrc(ws, 0, 128 * MB, 0x00020000);
  unsigned stw = 0;
  STAMP(0);
  const unsigned l16 = lane * 16, lw = w * 2048 + l16;
  const unsigned fl0 = lane == 0 ? 0u : OOB;
  const unsigned myfl = WS_FL + (c * 64 + h) * 128 + w * 8;
  const unsigned xo = (fr * 1024 + (((fq & 1) << 4) | ((fq >> 1) << 3))) * 2;
  u64 pc_; unsigned len_;
  asm volatile("s_getpc_b64 %0\n.Lmk_here:\n\ts_mov_b32 %1, .Lmk_end-.Lmk_here" : "=s"(pc_), "=s"(len_));
  const rsrc_t cr = __builtin_amdgcn_make_buffer_rsrc((void*)(pc_ & ~15ull), 0, len_ & ~15u, 0x00020000);
  const rsrc_t xr = __builtin_amdgcn_make_buffer_rsrc((void*)x0, 0, 4 * MB, 0x00020000);
  const unsigned osrc0 = (lane >> 3) * 2048 + (lane & 7) * 16;
  auto get_x = [&](int s) __attribute__((always_inline)) {
#pragma unroll
    for (int i = 0; i < 2; ++i) dma(xr, osrc0, ((c * 16 + s * 8 + 2 * wr + p2) * 16 + i * 8) * 2048 + g2 * 128, L_RES + s * 8192 + w * 2048 + i * 1024);
  };
  u32x2 tag;
  const rsrc_t wa = __builtin_amdgcn_make_buffer_rsrc((void*)WqA, 0, 6 * MB, 0x00020000);
  const unsigned rho = 2 * (c & 3), wbase = w * 256 * 6144 + h * 64;
  const unsigned wv = (lane >> 2) * 6144 + ((lane & 3) ^ ((lane >> 3) & 2)) * 16;
  auto piece = [&](int q, int j) __attribute__((always_inline)) {
    const unsigned k = j ^ par, src = uni(wbase + (((rho + 2 * q) & 7) + k) * 196608), dst = uni(L_WS + ((q & 1) * 8 + k * 4 + w) * 6144);
#pragma unroll
    for (int i = 0; i < 6; ++i) dma(wa, wv, src + (i & 1) * 98304 + (i >> 1) * 2048, dst + i * 1024);
  };
  const unsigned kb = 2 * c + par;
  const rsrc_t oa = __builtin_amdgcn_make_buffer_rsrc((void*)WoA, 0, 2 * MB, 0x00020000);
  const rsrc_t wb = __builtin_amdgcn_make_buffer_rsrc((void*)WqB, 0, 6 * MB, 0x00020000);
  const rsrc_t ob = __builtin_amdgcn_make_buffer_rsrc((void*)WoB, 0, 2 * MB, 0x00020000);
  const unsigned wsrc = (w * 16 + (lane >> 3)) * 6144 + (lane & 7) * 16, osrc = w * 16 * 2048 + osrc0;
  auto get_o = [&](int t) __attribute__((always_inline)) {
#pragma unroll
    for (int i = 0; i < 2; ++i) dma(t ? ob : oa, osrc, (kb * 64 + i * 8) * 2048 + rot(g, 1 + t) * 128, L_K + 24576 + t * 8192 + w * 2048 + i * 1024);
  };
  auto get_q = [&](int t) __attribute__((always_inline)) {
#pragma unroll
    for (int i = 0; i < 6; ++i)
      dma(t ? wb : wa, wsrc, (kb * 64 + (i & 1) * 8) * 6144 + (i >> 1) * 2048 + rot(g, i >> 1) * 128, (t ? L_K : L_INB) + (i >> 1) * 8192 + w * 2048 + (i & 1) * 1024);
  };
  const unsigned fr8 = fq == fr >> 2 ? 64 + fr * 4 : fq == ((fr >> 2) + 1 & 3) ? fr * 4 : OOB;
  u32x4 ones = {0x3F803F80u, 0x3F803F80u, 0x3F803F80u, 0x3F803F80u};
  unsigned sta = 0;
  auto publish = [&](int s, bool now) __attribute__((always_inline)) {
    const unsigned a = L_RES + s * 8192 + w * 2048 + fr * 128 + fq * 8, T = c * 16 + 8 * s + 2 * wr + p2;
    u32x4 y0 = cat(lds_r2(a), lds_r2(a + 32)), y1 = cat(lds_r2(a + 64), lds_r2(a + 96));
    f32x4 sm, gm;
    pin(y0); pin(y1); pin(ones);
    nops<2>();
    mm0_v(sm, ones, y0); mm0_v(gm, y0, y0); mma_v(sm, ones, y1); mma_v(gm, y1, y1);
    if (now) { st16<0>(y0, wsr, l16, WS_XF + T * 32768 + g2 * 2048); st16<1024>(y1, wsr, l16, WS_XF + T * 32768 + g2 * 2048); }
    lds_w4(lw + L_RES + s * 8192, y0); lds_w4(lw + L_RES + s * 8192 + 1024, y1);
    nops<8>();
    pin(sm); pin(gm);
    const unsigned st = __float_as_uint(fq != fr >> 2 ? sm[0] : fr & 2 ? (fr & 1 ? gm[3] : gm[2]) : (fr & 1 ? gm[1] : gm[0]));
    if (now) st4(st, wsr, fr8, WS_ST + T * 2048 + g2 * 128); else sta = st;
  };
  auto next_tag = [&]() __attribute__((always_inline)) {
    pin(tag);
    const u64 t = (((u64)tag[1] << 32) | tag[0]) + 1; tag = (u32x2){(unsigned)t, (unsigned)(t >> 32)};
  };
  const unsigned cb = c * 16 * 32768, skv = WS_KV + (c * 32 + h) * 32768;
  const unsigned soo = WS_OF + cb + wr * 65536 + h * 1024, sxo = WS_XF + cb + (2 * wr + p2) * 32768 + g2 * 2048, sso = WS_ST + (c * 16 + 2 * wr + p2) * 2048 + g2 * 128;
  auto clear = [&](int from, int to) __attribute__((always_inline)) {
    u32x4 z = {~0u, ~0u, ~0u, ~0u};
    pin(z);
#pragma unroll
    for (int k = from; k < to; ++k) {
      if (k < 8) st16<0>(z, wsr, l16, soo + (k == 4 ? 9 : k == 5 ? 8 : k - 6) * 32768);
      else if (k < 10) st16<0>(z, wsr, l16, sxo + 4 * MB + 8 * 32768 + (k - 8) * 1024);
      else st4(z[0], wsr, fr8, sso + 136 * 2048);
    }
  };
  dma(cr, l16, (bix * 4 + w) * 1024, L_SCR + 1024);
  get_x(1);
  get_x(0);
  ld8<0>(tag, wsr, 0u, WS_EP);
  piece(0, 0);
  touch(wsr, lane < 4 ? WS_FL + (lane >> 1) * 524288 + (w * 512 + c * 64 + (lane & 1) * 32 + h) * 128 : OOB);
  STAMP(29);

  unsigned ps[16];
  u32x8 pv = {};
#pragma unroll
  for (int j = 0; j < 3; ++j) {
    const unsigned ct = (w & 1) * 3 + j;
    pv[1 + j] = L_K + (ct >> 1) * 8192 + (4 * fq + (fr >> 2)) * 128 + (w >> 1) * 64 + (ct & 1) * 32 + (lane & 3) * 8;
    ps[j] = WS_WF + 6 * MB + ((2 * rot(g, ct >> 1) + (w >> 1)) * 192 + (kb >> 2) * 48 + (ct + 4) % 6 * 8 + (kb & 3) * 2) * 1024;
    ps[10 + j] = kb * 64 * 6144 + j * 2048 + rot(g, j) * 128;
  }
  { const unsigned ct = (w & 1) * 3 + (fr >> 1);
    pv[4] = ((2 * rot(g, ct >> 1) + (w >> 1)) * 16384 + ct * 128 + (fr & 1) * 64 + fq * 16) | (fr < 6 ? 0u : OOB); }
  ps[3] = WS_CS + 524288 + kb * 1024;
  ps[6] = (c >> 2) | par << 1 | (bix * 4 + w) << 10;                                            ps[7] = L_WS + w * 6144;
  ps[8] = wbase;                                             ps[9] = rho;
  ps[13] = kb * 64 * 2048 + rot(g, 1) * 128;                 ps[14] = kb * 64 * 2048 + rot(g, 2) * 128;
  ps[15] = WS_WF + (h * 4 + w) * 49152 + (rho + (c >> 2)) * 1024;
  pv[5] = wsrc;                                              pv[6] = osrc;
  u32x16 pw = {wv, L_WS + w * 6144 + (4 * fq + (fr >> 2)) * 64 + (fq & 1) * 32 + (lane & 3) * 8,
                    l16 + rho * 1024, l16 + ((rho + 2) & 7) * 1024, l16 + ((rho + 4) & 7) * 1024, l16 + ((rho + 6) & 7) * 1024,
                    L_WS + w * 6144 + (fr == 0 ? fq * 16 : 1024 + l16), L_WS + (w * 24 + (lane < 24 ? lane : 0)) * 4,
                    (w * 128 + (lane >> 3 & 1) * 64 + 2 * ((g2 - rot(0, 1 + (fq & 1))) & 15) + (lane >> 2 & 1)) * 128 + (lane & 3) * 8};
  if (w < 2) {
    u32x4 pm;
#pragma unroll
    for (int k = 0; k < 4; ++k)
      pm[k] = (8 * fq + 2 * k > 16 * w + fr ? 0xFFFFu : 0u) | (8 * fq + 2 * k + 1 > 16 * w + fr ? 0xFFFF0000u : 0u);
    lds_w4(L_PM + w * 1024 + l16, pm);
  } else {
    const unsigned want = (unsigned)((0x5555ull << 16) >> (fr + 25 - 8 * fq));
    u32x4 mb;
#pragma unroll
    for (int k = 0; k < 4; ++k) mb[k] = (want >> (15 - 4 * w - k)) & 1 ? 0u : 0xFF800000u;
    lds_w4(L_MB + (w - 2) * 1024 + l16, mb);
  }
  pv[0] = L_K + 24576 + (16 * (fq & 1) + 4 * (fq >> 1) + (fr >> 2)) * 128 + w * 32 + (lane & 3) * 8;
  pv[7] = lane < 2 ? lane * 32 * 128 : OOB;
  ps[4] = WS_WO + (rot(g, 1) * 128 + (kb >> 2) * 32 + w * 8 + (kb & 3) * 2) * 1024;
  ps[5] = WS_WO + 2 * MB + (rot(g, 2) * 128 + (kb >> 2) * 32 + w * 8 + (kb & 3) * 2) * 1024;
  const unsigned krow = (8 * (fr >> 2) + (fr & 3)) * 64 + wr * 2048 + L_K;
  const unsigned kv0 = 16384 + wr * 2048 + fr * 64 + ((fq ^ ((fr >> 3) << 1) ^ ((fr >> 1) & 3)) * 16);
  u32x16 va = {l16, l16 + 4096, lw, L_INB + w * 18432 + l16, L_INB + ((w + 3) & 3) * 18432 + l16, L_INB + ((w + 2) & 3) * 18432 + 6144 + l16,
                     L_INB + ((w + 1) & 3) * 18432 + 12288 + l16, kv0, kv0 ^ 48, L_CS + fq * 16, lane * 4, fl0,
                     ((lane & 31) >> 2) * 128 + (lane & 3) * 8,
                     fq < 3 ? (w * 128 + (lane >> 3 & 1) * 64 + 2 * ((2 * g - rot(g, fq % 3)) & 15) + (lane >> 2 & 1)) * 128 + (lane & 3) * 8 : ((lane & 7) * 64 + h) * 128 + w * 8,
                     fr8, krow + (fq ^ swzK(fr >> 2) ^ ((fr >> 1) & 1)) * 16};
  u32x8 vb = {L_V + wr * 2048 + (8 * fq + (fr >> 2)) * 64 + 8 * (lane & 1) + (((lane >> 1) & 1) ^ ((fq & 1) << 1) ^ (fr >> 3)) * 16,
                    krow + (fq ^ swzK(((fr >> 2) + 2) & 3) ^ ((fr >> 1) & 1)) * 16, 0, 0, xo,
                    lane < 24 ? L_CS + w * 96 + lane * 4 : L_SCR, w == 0 ? bid * 1024 + lane * 4 : OOB, 0};
  u32x16 pz = {pw[2], pw[3], pw[4], pw[5], pv[1], pv[2], pv[3], pv[4], pv[0], pw[8], pw[0], pw[1], pw[6], pw[7], pv[5], pv[6]};
  unsigned pf = pv[7];
  pin(va); pin(vb); pin(pz); pin(pf);
  piece(0, 1);
  wait_vm<13>();
  next_tag();
  STAMP(18);
  publish(1, true);
  publish(0, true);
  STAMP(1);
  piece(1, 0);
  wait_vm<6>();
  st8<0>(tag, wsr, lane < 2 ? lane * 32 * 128 : OOB, myfl + (2 * g2 + p2 - h) * 128);
  piece(1, 1);
  STAMP(15);

  u32x16 sa, sb;
#pragma unroll
  for (int i = 0; i < 4; ++i) {
    sa[i] = WS_XF + cb + w * 8192 + ((wr + 1 + i) & 3) * 65536;
    sa[4 + i] = sa[i] + WS_OF + p2 * 32768;
  }
  sa[8] = soo;                                              sa[9] = sxo;
  sa[10] = WS_ST + (c * 16 + 2 * wr) * 2048;                sa[11] = sso;
  sa[12] = skv - 16384;                                     sa[13] = c ? skv - 32 * 32768 : WS_XF + (h & 15) * 32768;
  sa[14] = c ? skv - 32 * 32768 + 8 * MB : sa[13];          sa[15] = WS_FL + (c * 64 + 8 * w) * 128;
  sb[0] = myfl;                                             sb[1] = WS_WF + (h * 4 + w) * 49152;
  sb[2] = WS_WO + (g2 * 4 + w) * 32768;                     sb[3] = c ? myfl - w * 8 + ((w - dl) & 3) * 8 - 32 * 128 + 64 : myfl + 4096;
  sb[4] = w * 96;                                           sb[5] = c ? myfl + 65536 - w * 8 + ((3 - dl) & 3) * 8 - 32 * 128 + 64 : myfl + 4096;
  sb[6] = ((c * 16 + 2 * wr + p2) * 16 * 1024 + g2 * 64) * 2;  sb[7] = WS_FL + 96;
  sb[8] = WS_CS + h * 16384;                                sb[9] = w * 2048;
  sb[10] = c ? -8 : 8 - wr;                                  sb[11] = c ? 16 * MB : 0;
  sb[12] = (2 * g2 + p2 - h) * 128;                                sb[13] = bid == 0 && w == 0 ? WS_EP : 0;
  sb[14] = c == 0 && wr == 0 ? ~0u : 0u;                     sb[15] = 0;
#pragma unroll
  for (int i = 0; i < 16; ++i) { sa[i] = uni(sa[i]); sb[i] = uni(sb[i]); }
  const rsrc_t wsk = __builtin_amdgcn_make_buffer_rsrc(ws, 0, wr == 3 ? 128 * MB : 0u, 0x00020000);
  const rsrc_t outr = __builtin_amdgcn_make_buffer_rsrc(out, 0, 4 * MB, 0x00020000);
  u32x8 pa, pc;
#pragma unroll
  for (int i = 0; i < 8; ++i) { pa[i] = uni(ps[i]); pc[i] = uni(ps[8 + i]); }
  STAMP(17);

  vb[2] = tag[0]; vb[3] = tag[1];
  STAMP(19);
  vb[7] = stw;
  asm volatile(
"; @entry\n"
"s_mov_b32 s0, 0\n"
"s_mov_b32 s3, 0\n"
"v_xor_b32_e32 v255, 32, v43\n"
"v_add_u32_e32 v254, 0xc000, v43\n"
"v_add_u32_e32 v253, 0xc000, v255\n"
"v_mov_b32_e32 v24, 0x3f803f80\n"
"v_mov_b32_e32 v25, 0x3f803f80\n"
"v_mov_b32_e32 v26, 0x3f803f80\n"
"v_mov_b32_e32 v27, 0x3f803f80\n"
"s_waitcnt lgkmcnt(0)\n"
"s_waitcnt vmcnt(19)\n"
"ds_read_b64_tr_b16 v[28:29], v43\n"
"ds_read_b64_tr_b16 v[30:31], v43 offset:1024\n"
"ds_read_b64_tr_b16 v[48:49], v255\n"
"ds_read_b64_tr_b16 v[50:51], v255 offset:1024\n"
"ds_read_b64_tr_b16 a[0:1], v43 offset:2048\n"
"ds_read_b64_tr_b16 a[2:3], v43 offset:3072\n"
"ds_read_b64_tr_b16 a[32:33], v255 offset:2048\n"
"ds_read_b64_tr_b16 a[34:35], v255 offset:3072\n"
"ds_read_b64_tr_b16 a[64:65], v43 offset:4096\n"
"ds_read_b64_tr_b16 a[66:67], v43 offset:5120\n"
"ds_read_b64_tr_b16 a[96:97], v255 offset:4096\n"
"ds_read_b64_tr_b16 a[98:99], v255 offset:5120\n"
"ds_read_b64_tr_b16 v[52:53], v43 offset:24576\n"
"ds_read_b64_tr_b16 v[54:55], v43 offset:25600\n"
"ds_read_b64_tr_b16 v[56:57], v255 offset:24576\n"
"s_waitcnt lgkmcnt(13)\n"
"ds_read_b64_tr_b16 v[58:59], v255 offset:25600\n"
"ds_read_b64_tr_b16 a[4:5], v43 offset:26624\n"
"s_waitcnt lgkmcnt(13)\n"
"ds_read_b64_tr_b16 a[6:7], v43 offset:27648\n"
"ds_read_b64_tr_b16 a[36:37], v255 offset:26624\n"
"s_waitcnt lgkmcnt(13)\n"
"ds_read_b64_tr_b16 a[38:39], v255 offset:27648\n"
"ds_read_b64_tr_b16 a[68:69], v43 offset:28672\n"
"s_waitcnt lgkmcnt(13)\n"
"ds_read_b64_tr_b16 a[70:71], v43 offset:29696\n"
"ds_read_b64_tr_b16 a[100:101], v255 offset:28672\n"
"s_waitcnt lgkmcnt(13)\n"
"ds_read_b64_tr_b16 a[102:103], v255 offset:29696\n"
"s_waitcnt lgkmcnt(12)\n"
"v_mfma_f32_16x16x32_bf16 v[60:63], v[28:31], v[24:27], 0\n"
"v_mfma_f32_16x16x32_bf16 v[64:67], v[48:51], v[24:27], 0\n"
"v_mfma_f32_16x16x32_bf16 v[68:71], a[0:3], v[24:27], 0\n"
"v_mfma_f32_16x16x32_bf16 v[72:75], a[32:35], v[24:27], 0\n"
"v_mfma_f32_16x16x32_bf16 v[76:79], a[64:67], v[24:27], 0\n"
"v_mfma_f32_16x16x32_bf16 v[80:83], a[96:99], v[24:27], 0\n"
"s_waitcnt lgkmcnt(0)\n"
"v_mfma_f32_16x16x32_bf16 v[60:63], v[52:55], v[24:27], v[60:63]\n"
"v_mfma_f32_16x16x32_bf16 v[64:67], v[56:59], v[24:27], v[64:67]\n"
"v_mfma_f32_16x16x32_bf16 v[68:71], a[4:7], v[24:27], v[68:71]\n"
"v_mfma_f32_16x16x32_bf16 v[72:75], a[36:39], v[24:27], v[72:75]\n"
"v_mfma_f32_16x16x32_bf16 v[76:79], a[68:71], v[24:27], v[76:79]\n"
"v_mfma_f32_16x16x32_bf16 v[80:83], a[100:103], v[24:27], v[80:83]\n"
"s_bfe_u32 s98, s26, 0x10001\n"
"s_xor_b32 s98, s98, 0\n"
"s_mul_i32 s97, s98, 0x6000\n"
"s_add_u32 s97, s97, s27\n"
"s_add_u32 s96, s45, 4\n"
"s_and_b32 s96, s96, 7\n"
"s_add_u32 s96, s96, s98\n"
"s_mul_i32 s96, s96, 0x30000\n"
"s_add_u32 s96, s96, s44\n"
"s_add_u32 m0, s97, 0\n"
"s_add_u32 s98, s96, 0\n"
"buffer_load_dwordx4 v42, s[28:31], s98 offen lds\n"
"s_add_u32 m0, s97, 0x400\n"
"s_add_u32 s98, s96, 0x18000\n"
"buffer_load_dwordx4 v42, s[28:31], s98 offen lds\n"
"s_add_u32 m0, s97, 0x800\n"
"s_add_u32 s98, s96, 0x800\n"
"buffer_load_dwordx4 v42, s[28:31], s98 offen lds\n"
"s_add_u32 m0, s97, 0xc00\n"
"s_add_u32 s98, s96, 0x18800\n"
"buffer_load_dwordx4 v42, s[28:31], s98 offen lds\n"
"s_add_u32 m0, s97, 0x1000\n"
"s_add_u32 s98, s96, 0x1000\n"
"buffer_load_dwordx4 v42, s[28:31], s98 offen lds\n"
"s_add_u32 m0, s97, 0x1400\n"
"s_add_u32 s98, s96, 0x19000\n"
"buffer_load_dwordx4 v42, s[28:31], s98 offen lds\n"
"s_bfe_u32 s98, s26, 0x10001\n"
"s_xor_b32 s98, s98, 1\n"
"s_mul_i32 s97, s98, 0x6000\n"
"s_add_u32 s97, s97, s27\n"
"s_add_u32 s96, s45, 4\n"
"s_and_b32 s96, s96, 7\n"
"s_add_u32 s96, s96, s98\n"
"s_mul_i32 s96, s96, 0x30000\n"
"s_add_u32 s96, s96, s44\n"
"s_add_u32 m0, s97, 0\n"
"s_add_u32 s98, s96, 0\n"
"buffer_load_dwordx4 v42, s[28:31], s98 offen lds\n"
"s_add_u32 m0, s97, 0x400\n"
"s_add_u32 s98, s96, 0x18000\n"
"buffer_load_dwordx4 v42, s[28:31], s98 offen lds\n"
"s_add_u32 m0, s97, 0x800\n"
"s_add_u32 s98, s96, 0x800\n"
"buffer_load_dwordx4 v42, s[28:31], s98 offen lds\n"
"s_add_u32 m0, s97, 0xc00\n"
"s_add_u32 s98, s96, 0x18800\n"
"buffer_load_dwordx4 v42, s[28:31], s98 offen lds\n"
"s_add_u32 m0, s97, 0x1000\n"
"s_add_u32 s98, s96, 0x1000\n"
"buffer_load_dwordx4 v42, s[28:31], s98 offen lds\n"
"s_add_u32 m0, s97, 0x1400\n"
"s_add_u32 s98, s96, 0x19000\n"
"buffer_load_dwordx4 v42, s[28:31], s98 offen lds\n"
"s_waitcnt vmcnt(12)\n"
"ds_read_b64_tr_b16 v[84:85], v254\n"
"ds_read_b64_tr_b16 v[86:87], v254 offset:1024\n"
"ds_read_b64_tr_b16 v[88:89], v253\n"
"ds_read_b64_tr_b16 v[90:91], v253 offset:1024\n"
"ds_read_b64_tr_b16 a[8:9], v254 offset:2048\n"
"ds_read_b64_tr_b16 a[10:11], v254 offset:3072\n"
"ds_read_b64_tr_b16 a[40:41], v253 offset:2048\n"
"ds_read_b64_tr_b16 a[42:43], v253 offset:3072\n"
"ds_read_b64_tr_b16 a[72:73], v254 offset:4096\n"
"ds_read_b64_tr_b16 a[74:75], v254 offset:5120\n"
"ds_read_b64_tr_b16 a[104:105], v253 offset:4096\n"
"ds_read_b64_tr_b16 a[106:107], v253 offset:5120\n"
"ds_read_b64_tr_b16 v[92:93], v254 offset:24576\n"
"ds_read_b64_tr_b16 v[94:95], v254 offset:25600\n"
"ds_read_b64_tr_b16 v[96:97], v253 offset:24576\n"
"s_waitcnt lgkmcnt(13)\n"
"ds_read_b64_tr_b16 v[98:99], v253 offset:25600\n"
"ds_read_b64_tr_b16 a[12:13], v254 offset:26624\n"
"s_waitcnt lgkmcnt(13)\n"
"ds_read_b64_tr_b16 a[14:15], v254 offset:27648\n"
"ds_read_b64_tr_b16 a[44:45], v253 offset:26624\n"
"s_waitcnt lgkmcnt(13)\n"
"ds_read_b64_tr_b16 a[46:47], v253 offset:27648\n"
"ds_read_b64_tr_b16 a[76:77], v254 offset:28672\n"
"s_waitcnt lgkmcnt(13)\n"
"ds_read_b64_tr_b16 a[78:79], v254 offset:29696\n"
"ds_read_b64_tr_b16 a[108:109], v253 offset:28672\n"
"s_waitcnt lgkmcnt(13)\n"
"ds_read_b64_tr_b16 a[110:111], v253 offset:29696\n"
"s_waitcnt lgkmcnt(12)\n"
"v_mfma_f32_16x16x32_bf16 v[60:63], v[84:87], v[24:27], v[60:63]\n"
"v_mfma_f32_16x16x32_bf16 v[64:67], v[88:91], v[24:27], v[64:67]\n"
"v_mfma_f32_16x16x32_bf16 v[68:71], a[8:11], v[24:27], v[68:71]\n"
"v_mfma_f32_16x16x32_bf16 v[72:75], a[40:43], v[24:27], v[72:75]\n"
"v_mfma_f32_16x16x32_bf16 v[76:79], a[72:75], v[24:27], v[76:79]\n"
"v_mfma_f32_16x16x32_bf16 v[80:83], a[104:107], v[24:27], v[80:83]\n"
"s_waitcnt lgkmcnt(0)\n"
"v_mfma_f32_16x16x32_bf16 v[60:63], v[92:95], v[24:27], v[60:63]\n"
"v_mfma_f32_16x16x32_bf16 v[64:67], v[96:99], v[24:27], v[64:67]\n"
"v_mfma_f32_16x16x32_bf16 v[68:71], a[12:15], v[24:27], v[68:71]\n"
"v_mfma_f32_16x16x32_bf16 v[72:75], a[44:47], v[24:27], v[72:75]\n"
"v_mfma_f32_16x16x32_bf16 v[76:79], a[76:79], v[24:27], v[76:79]\n"
"v_mfma_f32_16x16x32_bf16 v[80:83], a[108:111], v[24:27], v[80:83]\n"
"s_bfe_u32 s98, s26, 0x10001\n"
"s_xor_b32 s98, s98, 0\n"
"s_mul_i32 s97, s98, 0x6000\n"
"s_add_u32 s97, s97, s27\n"
"s_add_u32 s96, s45, 6\n"
"s_and_b32 s96, s96, 7\n"
"s_add_u32 s96, s96, s98\n"
"s_mul_i32 s96, s96, 0x30000\n"
"s_add_u32 s96, s96, s44\n"
"s_add_u32 m0, s97, 0xc000\n"
"s_add_u32 s98, s96, 0\n"
"buffer_load_dwordx4 v42, s[28:31], s98 offen lds\n"
"s_add_u32 m0, s97, 0xc400\n"
"s_add_u32 s98, s96, 0x18000\n"
"buffer_load_dwordx4 v42, s[28:31], s98 offen lds\n"
"s_add_u32 m0, s97, 0xc800\n"
"s_add_u32 s98, s96, 0x800\n"
"buffer_load_dwordx4 v42, s[28:31], s98 offen lds\n"
"s_add_u32 m0, s97, 0xcc00\n"
"s_add_u32 s98, s96, 0x18800\n"
"buffer_load_dwordx4 v42, s[28:31], s98 offen lds\n"
"s_add_u32 m0, s97, 0xd000\n"
"s_add_u32 s98, s96, 0x1000\n"
"buffer_load_dwordx4 v42, s[28:31], s98 offen lds\n"
"s_add_u32 m0, s97, 0xd400\n"
"s_add_u32 s98, s96, 0x19000\n"
"buffer_load_dwordx4 v42, s[28:31], s98 offen lds\n"
"s_bfe_u32 s98, s26, 0x10001\n"
"s_xor_b32 s98, s98, 1\n"
"s_mul_i32 s97, s98, 0x6000\n"
"s_add_u32 s97, s97, s27\n"
"s_add_u32 s96, s45, 6\n"
"s_and_b32 s96, s96, 7\n"
"s_add_u32 s96, s96, s98\n"
"s_mul_i32 s96, s96, 0x30000\n"
"s_add_u32 s96, s96, s44\n"
"s_add_u32 m0, s97, 0xc000\n"
"s_add_u32 s98, s96, 0\n"
"buffer_load_dwordx4 v42, s[28:31], s98 offen lds\n"
"s_add_u32 m0, s97, 0xc400\n"
"s_add_u32 s98, s96, 0x18000\n"
"buffer_load_dwordx4 v42, s[28:31], s98 offen lds\n"
"s_add_u32 m0, s97, 0xc800\n"
"s_add_u32 s98, s96, 0x800\n"
"buffer_load_dwordx4 v42, s[28:31], s98 offen lds\n"
"s_add_u32 m0, s97, 0xcc00\n"
"s_add_u32 s98, s96, 0x18800\n"
"buffer_load_dwordx4 v42, s[28:31], s98 offen lds\n"
"s_add_u32 m0, s97, 0xd000\n"
"s_add_u32 s98, s96, 0x1000\n"
"buffer_load_dwordx4 v42, s[28:31], s98 offen lds\n"
"s_add_u32 m0, s97, 0xd400\n"
"s_add_u32 s98, s96, 0x19000\n"
"buffer_load_dwordx4 v42, s[28:31], s98 offen lds\n"
"s_add_u32 s98, s67, 0x1000\n"
"buffer_load_dwordx2 v[250:251], v12, s[4:7], s98 offen sc1\n"
"s_waitcnt vmcnt(13)\n"
"ds_read_b64_tr_b16 v[100:101], v43\n"
"ds_read_b64_tr_b16 v[102:103], v43 offset:1024\n"
"ds_read_b64_tr_b16 v[104:105], v255\n"
"ds_read_b64_tr_b16 v[106:107], v255 offset:1024\n"
"ds_read_b64_tr_b16 a[16:17], v43 offset:2048\n"
"ds_read_b64_tr_b16 a[18:19], v43 offset:3072\n"
"ds_read_b64_tr_b16 a[48:49], v255 offset:2048\n"
"ds_read_b64_tr_b16 a[50:51], v255 offset:3072\n"
"ds_read_b64_tr_b16 a[80:81], v43 offset:4096\n"
"ds_read_b64_tr_b16 a[82:83], v43 offset:5120\n"
"ds_read_b64_tr_b16 a[112:113], v255 offset:4096\n"
"ds_read_b64_tr_b16 a[114:115], v255 offset:5120\n"
"ds_read_b64_tr_b16 v[108:109], v43 offset:24576\n"
"ds_read_b64_tr_b16 v[110:111], v43 offset:25600\n"
"ds_read_b64_tr_b16 v[112:113], v255 offset:24576\n"
"s_waitcnt lgkmcnt(13)\n"
"ds_read_b64_tr_b16 v[114:115], v255 offset:25600\n"
"ds_read_b64_tr_b16 a[20:21], v43 offset:26624\n"
"s_waitcnt lgkmcnt(13)\n"
"ds_read_b64_tr_b16 a[22:23], v43 offset:27648\n"
"ds_read_b64_tr_b16 a[52:53], v255 offset:26624\n"
"s_waitcnt lgkmcnt(13)\n"
"ds_read_b64_tr_b16 a[54:55], v255 offset:27648\n"
"ds_read_b64_tr_b16 a[84:85], v43 offset:28672\n"
"s_waitcnt lgkmcnt(13)\n"
"ds_read_b64_tr_b16 a[86:87], v43 offset:29696\n"
"ds_read_b64_tr_b16 a[116:117], v255 offset:28672\n"
"s_waitcnt lgkmcnt(13)\n"
"ds_read_b64_tr_b16 a[118:119], v255 offset:29696\n"
"s_waitcnt lgkmcnt(12)\n"
"v_mfma_f32_16x16x32_bf16 v[60:63], v[100:103], v[24:27], v[60:63]\n"
"v_mfma_f32_16x16x32_bf16 v[64:67], v[104:107], v[24:27], v[64:67]\n"
"v_mfma_f32_16x16x32_bf16 v[68:71], a[16:19], v[24:27], v[68:71]\n"
"v_mfma_f32_16x16x32_bf16 v[72:75], a[48:51], v[24:27], v[72:75]\n"
"v_mfma_f32_16x16x32_bf16 v[76:79], a[80:83], v[24:27], v[76:79]\n"
"v_mfma_f32_16x16x32_bf16 v[80:83], a[112:115], v[24:27], v[80:83]\n"
"s_waitcnt lgkmcnt(0)\n"
"v_mfma_f32_16x16x32_bf16 v[60:63], v[108:111], v[24:27], v[60:63]\n"
"v_mfma_f32_16x16x32_bf16 v[64:67], v[112:115], v[24:27], v[64:67]\n"
"v_mfma_f32_16x16x32_bf16 v[68:71], a[20:23], v[24:27], v[68:71]\n"
"v_mfma_f32_16x16x32_bf16 v[72:75], a[52:55], v[24:27], v[72:75]\n"
"v_mfma_f32_16x16x32_bf16 v[76:79], a[84:87], v[24:27], v[76:79]\n"
"v_mfma_f32_16x16x32_bf16 v[80:83], a[116:119], v[24:27], v[80:83]\n"
"s_waitcnt vmcnt(0)\n"
"v_cmp_ne_u64_e32 vcc, v[250:251], v[18:19]\n"
"s_cbranch_vccnz .Lmk_f1s\n"
".Lmk_f1r:\n"
"s_add_u32 s98, s52, 0x40000\n"
"buffer_load_dwordx4 v[116:119], v32, s[4:7], s98 offen sc1\n"
"buffer_load_dwordx4 v[120:123], v32, s[4:7], s98 offen offset:1024 sc1\n"
"buffer_load_dwordx4 v[124:127], v33, s[4:7], s98 offen sc1\n"
"buffer_load_dwordx4 v[128:131], v33, s[4:7], s98 offen offset:1024 sc1\n"
"buffer_load_dwordx4 v[132:135], v34, s[4:7], s98 offen sc1\n"
"buffer_load_dwordx4 v[136:139], v34, s[4:7], s98 offen offset:1024 sc1\n"
"buffer_load_dwordx4 v[140:143], v35, s[4:7], s98 offen sc1\n"
"buffer_load_dwordx4 v[144:147], v35, s[4:7], s98 offen offset:1024 sc1\n"
"s_add_u32 s98, s53, 0x40000\n"
"buffer_load_dwordx4 v[148:151], v32, s[4:7], s98 offen sc1\n"
"buffer_load_dwordx4 v[152:155], v32, s[4:7], s98 offen offset:1024 sc1\n"
"buffer_load_dwordx4 v[156:159], v33, s[4:7], s98 offen sc1\n"
"buffer_load_dwordx4 v[160:163], v33, s[4:7], s98 offen offset:1024 sc1\n"
"buffer_load_dwordx4 v[164:167], v34, s[4:7], s98 offen sc1\n"
"buffer_load_dwordx4 v[168:171], v34, s[4:7], s98 offen offset:1024 sc1\n"
"buffer_load_dwordx4 v[172:175], v35, s[4:7], s98 offen sc1\n"
"buffer_load_dwordx4 v[176:179], v35, s[4:7], s98 offen offset:1024 sc1\n"
"s_getpc_b64 s[84:85]\n"
".Lmk_h2:\n"
"s_and_b32 s84, s84, -16\n"
"s_and_b32 s85, s85, 0xffff\n"
"s_mov_b32 s86, (.Lmk_end-.Lmk_h2)&0xFFFFFFF0\n"
"s_mov_b32 s87, 0x20000\n"
"s_and_b32 s98, s26, 0xfffffc00\n"
"s_mov_b32 m0, 0x27800\n"
"s_add_u32 s98, s98, 0x1e000\n"
"buffer_load_dwordx4 v0, s[84:87], s98 offen lds\n"
"ds_read_b64_tr_b16 v[180:181], v254\n"
"ds_read_b64_tr_b16 v[182:183], v254 offset:1024\n"
"ds_read_b64_tr_b16 v[184:185], v253\n"
"ds_read_b64_tr_b16 v[186:187], v253 offset:1024\n"
"ds_read_b64_tr_b16 a[24:25], v254 offset:2048\n"
"ds_read_b64_tr_b16 a[26:27], v254 offset:3072\n"
"ds_read_b64_tr_b16 a[56:57], v253 offset:2048\n"
"ds_read_b64_tr_b16 a[58:59], v253 offset:3072\n"
"ds_read_b64_tr_b16 a[88:89], v254 offset:4096\n"
"ds_read_b64_tr_b16 a[90:91], v254 offset:5120\n"
"ds_read_b64_tr_b16 a[120:121], v253 offset:4096\n"
"ds_read_b64_tr_b16 a[122:123], v253 offset:5120\n"
"ds_read_b64_tr_b16 v[188:189], v254 offset:24576\n"
"ds_read_b64_tr_b16 v[190:191], v254 offset:25600\n"
"ds_read_b64_tr_b16 v[192:193], v253 offset:24576\n"
"s_waitcnt lgkmcnt(13)\n"
"ds_read_b64_tr_b16 v[194:195], v253 offset:25600\n"
"ds_read_b64_tr_b16 a[28:29], v254 offset:26624\n"
"s_waitcnt lgkmcnt(13)\n"
"ds_read_b64_tr_b16 a[30:31], v254 offset:27648\n"
"ds_read_b64_tr_b16 a[60:61], v253 offset:26624\n"
"s_waitcnt lgkmcnt(13)\n"
"ds_read_b64_tr_b16 a[62:63], v253 offset:27648\n"
"ds_read_b64_tr_b16 a[92:93], v254 offset:28672\n"
"s_waitcnt lgkmcnt(13)\n"
"ds_read_b64_tr_b16 a[94:95], v254 offset:29696\n"
"ds_read_b64_tr_b16 a[124:125], v253 offset:28672\n"
"s_waitcnt lgkmcnt(13)\n"
"ds_read_b64_tr_b16 a[126:127], v253 offset:29696\n"
"s_waitcnt lgkmcnt(12)\n"
"v_mfma_f32_16x16x32_bf16 v[60:63], v[180:183], v[24:27], v[60:63]\n"
"v_mfma_f32_16x16x32_bf16 v[64:67], v[184:187], v[24:27], v[64:67]\n"
"v_mfma_f32_16x16x32_bf16 v[68:71], a[24:27], v[24:27], v[68:71]\n"
"v_mfma_f32_16x16x32_bf16 v[72:75], a[56:59], v[24:27], v[72:75]\n"
"v_mfma_f32_16x16x32_bf16 v[76:79], a[88:91], v[24:27], v[76:79]\n"
"v_mfma_f32_16x16x32_bf16 v[80:83], a[120:123], v[24:27], v[80:83]\n"
"s_waitcnt lgkmcnt(0)\n"
"v_mfma_f32_16x16x32_bf16 v[60:63], v[188:191], v[24:27], v[60:63]\n"
"v_mfma_f32_16x16x32_bf16 v[64:67], v[192:195], v[24:27], v[64:67]\n"
"v_mfma_f32_16x16x32_bf16 v[68:71], a[28:31], v[24:27], v[68:71]\n"
"v_mfma_f32_16x16x32_bf16 v[72:75], a[60:63], v[24:27], v[72:75]\n"
"v_mfma_f32_16x16x32_bf16 v[76:79], a[92:95], v[24:27], v[76:79]\n"
"v_mfma_f32_16x16x32_bf16 v[80:83], a[124:127], v[24:27], v[80:83]\n"
"s_nop 7\n"
"ds_write_b128 v44, v[60:63]\n"
"ds_write_b128 v44, v[64:67] offset:64\n"
"ds_write_b128 v44, v[68:71] offset:128\n"
"ds_write_b128 v44, v[72:75] offset:192\n"
"ds_write_b128 v44, v[76:79] offset:256\n"
"ds_write_b128 v44, v[80:83] offset:320\n"
"s_waitcnt lgkmcnt(0)\n"
"s_barrier\n"
"ds_read_b32 v255, v45\n"
"ds_read_b32 v254, v45 offset:6144\n"
"ds_read_b32 v253, v45 offset:12288\n"
"ds_read_b32 v252, v45 offset:18432\n"
"s_waitcnt lgkmcnt(0)\n"
"v_add_f32_e32 v255, v255, v254\n"
"v_add_f32_e32 v253, v253, v252\n"
"v_add_f32_e32 v255, v255, v253\n"
"ds_write_b32 v21, v255\n"
".Lmk_z0:\n"
"s_waitcnt vmcnt(13)\n"
"; @A G1 0.0\n"
"v_mfma_f32_16x16x32_bf16 v[24:27], a[0:3], v[116:119], 0\n"
"v_mfma_f32_16x16x32_bf16 v[24:27], a[4:7], v[120:123], v[24:27]\n"
"s_add_u32 m0, s77, 0xa000\n"
"s_add_u32 s98, s49, 0\n"
"buffer_load_dwordx4 v47, s[36:39], s98 offen lds\n"
"v_mfma_f32_16x16x32_bf16 v[24:27], a[8:11], v[124:127], v[24:27]\n"
"s_add_u32 s98, s54, 0x40000\n"
"buffer_load_dwordx4 v[60:63], v32, s[4:7], s98 offen sc1\n"
"s_add_u32 m0, s77, 0xa400\n"
"v_mfma_f32_16x16x32_bf16 v[24:27], a[12:15], v[128:131], v[24:27]\n"
"s_waitcnt vmcnt(11)\n"
"s_add_u32 s97, s49, 0x4000\n"
"s_add_u32 s96, s62, 0x4000\n"
"v_mfma_f32_16x16x32_bf16 v[24:27], a[16:19], v[132:135], v[24:27]\n"
"buffer_load_dwordx4 v47, s[36:39], s97 offen lds\n"
"v_mfma_f32_16x16x32_bf16 v[24:27], a[20:23], v[136:139], v[24:27]\n"
"s_add_u32 m0, s77, 0xc000\n"
"s_add_u32 s97, s50, 0\n"
"v_mfma_f32_16x16x32_bf16 v[24:27], a[24:27], v[140:143], v[24:27]\n"
"buffer_load_dword v255, v10, s[4:7], s96 offen sc1\n"
"v_mfma_f32_16x16x32_bf16 v[24:27], a[28:31], v[144:147], v[24:27]\n"
"buffer_load_dwordx4 v[64:67], v32, s[4:7], s98 offen offset:1024 sc1\n"
"buffer_load_dwordx4 v47, s[40:43], s97 offen lds\n"
"v_mfma_f32_16x16x32_bf16 v[68:71], a[60:63], v[144:147], 0\n"
"s_add_u32 m0, s77, 0xc400\n"
"s_add_u32 s97, s50, 0x4000\n"
"v_mfma_f32_16x16x32_bf16 v[68:71], a[56:59], v[140:143], v[68:71]\n"
"buffer_load_dword v254, v10, s[4:7], s96 offen offset:256 sc1\n"
"v_mfma_f32_16x16x32_bf16 v[68:71], a[52:55], v[136:139], v[68:71]\n"
"buffer_load_dwordx4 v47, s[40:43], s97 offen lds\n"
"v_mfma_f32_16x16x32_bf16 v[68:71], a[48:51], v[132:135], v[68:71]\n"
"s_add_u32 m0, s77, 0x4000\n"
"s_add_u32 s97, s46, 0\n"
"v_mfma_f32_16x16x32_bf16 v[68:71], a[44:47], v[128:131], v[68:71]\n"
"buffer_load_dwordx4 v[72:75], v33, s[4:7], s98 offen sc1\n"
"v_mfma_f32_16x16x32_bf16 v[68:71], a[40:43], v[124:127], v[68:71]\n"
"ds_write_b128 v3, v[24:27]\n"
"v_mfma_f32_16x16x32_bf16 v[68:71], a[36:39], v[120:123], v[68:71]\n"
"buffer_load_dwordx4 v46, s[32:35], s97 offen lds\n"
"v_mfma_f32_16x16x32_bf16 v[68:71], a[32:35], v[116:119], v[68:71]\n"
"s_add_u32 m0, s77, 0x4400\n"
"s_add_u32 s97, s46, 0xc000\n"
"v_mfma_f32_16x16x32_bf16 v[24:27], a[64:67], v[116:119], 0\n"
"buffer_load_dword v253, v10, s[4:7], s96 offen offset:512 sc1\n"
"v_mfma_f32_16x16x32_bf16 v[24:27], a[68:71], v[120:123], v[24:27]\n"
"buffer_load_dwordx4 v[76:79], v33, s[4:7], s98 offen offset:1024 sc1\n"
"buffer_load_dwordx4 v46, s[32:35], s97 offen lds\n"
"v_mfma_f32_16x16x32_bf16 v[24:27], a[72:75], v[124:127], v[24:27]\n"
"s_add_u32 m0, s77, 0x6000\n"
"s_add_u32 s97, s47, 0\n"
"v_mfma_f32_16x16x32_bf16 v[24:27], a[76:79], v[128:131], v[24:27]\n"
"buffer_load_dword v252, v10, s[4:7], s96 offen offset:768 sc1\n"
"v_mfma_f32_16x16x32_bf16 v[24:27], a[80:83], v[132:135], v[24:27]\n"
"buffer_load_dwordx4 v46, s[32:35], s97 offen lds\n"
"v_mfma_f32_16x16x32_bf16 v[24:27], a[84:87], v[136:139], v[24:27]\n"
"ds_write_b128 v3, v[68:71] offset:1024\n"
"v_mfma_f32_16x16x32_bf16 v[24:27], a[88:91], v[140:143], v[24:27]\n"
"buffer_load_dwordx4 v[68:71], v34, s[4:7], s98 offen sc1\n"
"s_add_u32 m0, s77, 0x6400\n"
"s_add_u32 s97, s47, 0xc000\n"
"v_mfma_f32_16x16x32_bf16 v[24:27], a[92:95], v[144:147], v[24:27]\n"
"buffer_load_dwordx4 v46, s[32:35], s97 offen lds\n"
"v_mfma_f32_16x16x32_bf16 v[80:83], a[124:127], v[144:147], 0\n"
"s_add_u32 m0, s77, 0x8000\n"
"s_add_u32 s97, s48, 0\n"
"v_mfma_f32_16x16x32_bf16 v[80:83], a[120:123], v[140:143], v[80:83]\n"
"buffer_load_dword v251, v10, s[4:7], s96 offen offset:1024 sc1\n"
"v_mfma_f32_16x16x32_bf16 v[80:83], a[116:119], v[136:139], v[80:83]\n"
"buffer_load_dwordx4 v46, s[32:35], s97 offen lds\n"
"v_mfma_f32_16x16x32_bf16 v[80:83], a[112:115], v[132:135], v[80:83]\n"
"buffer_load_dwordx4 v[196:199], v34, s[4:7], s98 offen offset:1024 sc1\n"
"s_add_u32 m0, s77, 0x8400\n"
"s_add_u32 s97, s48, 0xc000\n"
"v_mfma_f32_16x16x32_bf16 v[80:83], a[108:111], v[128:131], v[80:83]\n"
"buffer_load_dword v250, v10, s[4:7], s96 offen offset:1280 sc1\n"
"v_mfma_f32_16x16x32_bf16 v[80:83], a[104:107], v[124:127], v[80:83]\n"
"ds_write_b128 v3, v[24:27] offset:2048\n"
"v_mfma_f32_16x16x32_bf16 v[80:83], a[100:103], v[120:123], v[80:83]\n"
"buffer_load_dwordx4 v46, s[32:35], s97 offen lds\n"
"v_mfma_f32_16x16x32_bf16 v[80:83], a[96:99], v[116:119], v[80:83]\n"
"buffer_load_dword v249, v10, s[4:7], s96 offen offset:1536 sc1\n"
"v_mfma_f32_16x16x32_bf16 v[24:27], v[28:31], v[116:119], 0\n"
"buffer_load_dwordx4 v[44:47], v35, s[4:7], s98 offen sc1\n"
"v_mfma_f32_16x16x32_bf16 v[24:27], v[52:55], v[120:123], v[24:27]\n"
"v_mfma_f32_16x16x32_bf16 v[24:27], v[84:87], v[124:127], v[24:27]\n"
"buffer_load_dword v248, v10, s[4:7], s96 offen offset:1792 sc1\n"
"v_mfma_f32_16x16x32_bf16 v[24:27], v[92:95], v[128:131], v[24:27]\n"
"v_mfma_f32_16x16x32_bf16 v[24:27], v[100:103], v[132:135], v[24:27]\n"
"v_mfma_f32_16x16x32_bf16 v[24:27], v[108:111], v[136:139], v[24:27]\n"
"buffer_load_dwordx4 v[200:203], v35, s[4:7], s98 offen offset:1024 sc1\n"
"v_mfma_f32_16x16x32_bf16 v[24:27], v[180:183], v[140:143], v[24:27]\n"
"ds_write_b128 v3, v[80:83] offset:3072\n"
"v_mfma_f32_16x16x32_bf16 v[24:27], v[188:191], v[144:147], v[24:27]\n"
"v_mfma_f32_16x16x32_bf16 v[80:83], v[192:195], v[144:147], 0\n"
"v_mfma_f32_16x16x32_bf16 v[80:83], v[184:187], v[140:143], v[80:83]\n"
"v_mfma_f32_16x16x32_bf16 v[80:83], v[112:115], v[136:139], v[80:83]\n"
"v_mfma_f32_16x16x32_bf16 v[80:83], v[104:107], v[132:135], v[80:83]\n"
"v_mfma_f32_16x16x32_bf16 v[80:83], v[96:99], v[128:131], v[80:83]\n"
"v_mfma_f32_16x16x32_bf16 v[80:83], v[88:91], v[124:127], v[80:83]\n"
"s_nop 1\n"
"ds_write_b128 v3, v[24:27] offset:4096\n"
"v_mfma_f32_16x16x32_bf16 v[80:83], v[56:59], v[120:123], v[80:83]\n"
"v_mfma_f32_16x16x32_bf16 v[80:83], v[48:51], v[116:119], v[80:83]\n"
"s_waitcnt vmcnt(31)\n"
"; @A G1 0.1\n"
"v_mfma_f32_16x16x32_bf16 v[24:27], a[0:3], v[148:151], 0\n"
"v_mfma_f32_16x16x32_bf16 v[24:27], a[4:7], v[152:155], v[24:27]\n"
"v_mfma_f32_16x16x32_bf16 v[24:27], a[8:11], v[156:159], v[24:27]\n"
"s_add_u32 s98, s55, 0x40000\n"
"buffer_load_dwordx4 v[116:119], v32, s[4:7], s98 offen sc1\n"
"v_mfma_f32_16x16x32_bf16 v[24:27], a[12:15], v[160:163], v[24:27]\n"
"s_waitcnt vmcnt(28)\n"
"v_mfma_f32_16x16x32_bf16 v[24:27], a[16:19], v[164:167], v[24:27]\n"
"v_mfma_f32_16x16x32_bf16 v[24:27], a[20:23], v[168:171], v[24:27]\n"
"ds_write_b128 v3, v[80:83] offset:5120\n"
"v_mfma_f32_16x16x32_bf16 v[24:27], a[24:27], v[172:175], v[24:27]\n"
"v_mfma_f32_16x16x32_bf16 v[24:27], a[28:31], v[176:179], v[24:27]\n"
"buffer_load_dwordx4 v[80:83], v32, s[4:7], s98 offen offset:1024 sc1\n"
"v_mfma_f32_16x16x32_bf16 v[120:123], a[60:63], v[176:179], 0\n"
"v_mfma_f32_16x16x32_bf16 v[120:123], a[56:59], v[172:175], v[120:123]\n"
"v_mfma_f32_16x16x32_bf16 v[120:123], a[52:55], v[168:171], v[120:123]\n"
"v_mfma_f32_16x16x32_bf16 v[120:123], a[48:51], v[164:167], v[120:123]\n"
"v_mfma_f32_16x16x32_bf16 v[120:123], a[44:47], v[160:163], v[120:123]\n"
"buffer_load_dwordx4 v[124:127], v33, s[4:7], s98 offen sc1\n"
"v_mfma_f32_16x16x32_bf16 v[120:123], a[40:43], v[156:159], v[120:123]\n"
"ds_write_b128 v3, v[24:27] offset:6144\n"
"v_mfma_f32_16x16x32_bf16 v[120:123], a[36:39], v[152:155], v[120:123]\n"
"v_mfma_f32_16x16x32_bf16 v[120:123], a[32:35], v[148:151], v[120:123]\n"
"v_mfma_f32_16x16x32_bf16 v[24:27], a[64:67], v[148:151], 0\n"
"v_mfma_f32_16x16x32_bf16 v[24:27], a[68:71], v[152:155], v[24:27]\n"
"buffer_load_dwordx4 v[128:131], v33, s[4:7], s98 offen offset:1024 sc1\n"
"v_mfma_f32_16x16x32_bf16 v[24:27], a[72:75], v[156:159], v[24:27]\n"
"v_mfma_f32_16x16x32_bf16 v[24:27], a[76:79], v[160:163], v[24:27]\n"
"v_mfma_f32_16x16x32_bf16 v[24:27], a[80:83], v[164:167], v[24:27]\n"
"v_mfma_f32_16x16x32_bf16 v[24:27], a[84:87], v[168:171], v[24:27]\n"
"s_nop 0\n"
"ds_write_b128 v3, v[120:123] offset:7168\n"
"v_mfma_f32_16x16x32_bf16 v[24:27], a[88:91], v[172:175], v[24:27]\n"
"buffer_load_dwordx4 v[120:123], v34, s[4:7], s98 offen sc1\n"
"v_mfma_f32_16x16x32_bf16 v[24:27], a[92:95], v[176:179], v[24:27]\n"
"v_mfma_f32_16x16x32_bf16 v[132:135], a[124:127], v[176:179], 0\n"
"v_mfma_f32_16x16x32_bf16 v[132:135], a[120:123], v[172:175], v[132:135]\n"
"v_mfma_f32_16x16x32_bf16 v[132:135], a[116:119], v[168:171], v[132:135]\n"
"v_mfma_f32_16x16x32_bf16 v[132:135], a[112:115], v[164:167], v[132:135]\n"
"buffer_load_dwordx4 v[136:139], v34, s[4:7], s98 offen offset:1024 sc1\n"
"v_mfma_f32_16x16x32_bf16 v[132:135], a[108:111], v[160:163], v[132:135]\n"
"v_mfma_f32_16x16x32_bf16 v[132:135], a[104:107], v[156:159], v[132:135]\n"
"s_nop 0\n"
"ds_write_b128 v3, v[24:27] offset:8192\n"
"v_mfma_f32_16x16x32_bf16 v[132:135], a[100:103], v[152:155], v[132:135]\n"
"v_mfma_f32_16x16x32_bf16 v[132:135], a[96:99], v[148:151], v[132:135]\n"
"v_mfma_f32_16x16x32_bf16 v[24:27], v[28:31], v[148:151], 0\n"
"buffer_load_dwordx4 v[140:143], v35, s[4:7], s98 offen sc1\n"
"v_mfma_f32_16x16x32_bf16 v[24:27], v[52:55], v[152:155], v[24:27]\n"
"v_mfma_f32_16x16x32_bf16 v[24:27], v[84:87], v[156:159], v[24:27]\n"
"v_mfma_f32_16x16x32_bf16 v[24:27], v[92:95], v[160:163], v[24:27]\n"
"v_mfma_f32_16x16x32_bf16 v[24:27], v[100:103], v[164:167], v[24:27]\n"
"s_waitcnt vmcnt(8)\n"
"v_add_f32_e32 v255, v255, v251\n"
"v_add_f32_e32 v254, v254, v250\n"
"v_mfma_f32_16x16x32_bf16 v[24:27], v[108:111], v[168:171], v[24:27]\n"
"buffer_load_dwordx4 v[144:147], v35, s[4:7], s98 offen offset:1024 sc1\n"
"v_mfma_f32_16x16x32_bf16 v[24:27], v[180:183], v[172:175], v[24:27]\n"
"ds_write_b128 v3, v[132:135] offset:9216\n"
"v_mfma_f32_16x16x32_bf16 v[24:27], v[188:191], v[176:179], v[24:27]\n"
"v_add_f32_e32 v253, v253, v249\n"
"v_add_f32_e32 v252, v252, v248\n"
"v_mfma_f32_16x16x32_bf16 v[132:135], v[192:195], v[176:179], 0\n"
"v_add_f32_e32 v255, v255, v253\n"
"v_add_f32_e32 v254, v254, v252\n"
"v_mfma_f32_16x16x32_bf16 v[132:135], v[184:187], v[172:175], v[132:135]\n"
"v_add_f32_e32 v255, v255, v254\n"
"v_mov_b32_e32 v254, v255\n"
"v_mfma_f32_16x16x32_bf16 v[132:135], v[112:115], v[168:171], v[132:135]\n"
"s_nop 0\n"
"v_permlane32_swap_b32_e32 v255, v254\n"
"v_add_f32_e32 v255, v255, v254\n"
"v_mfma_f32_16x16x32_bf16 v[132:135], v[104:107], v[164:167], v[132:135]\n"
"v_mul_f32_e32 v255, 0x3a800000, v255\n"
"v_mov_b32_e32 v254, v255\n"
"v_mfma_f32_16x16x32_bf16 v[132:135], v[96:99], v[160:163], v[132:135]\n"
"s_nop 0\n"
"v_permlane16_swap_b32_e32 v255, v254\n"
"v_cvt_pk_bf16_f32 v253, -v255, 0\n"
"v_mfma_f32_16x16x32_bf16 v[132:135], v[88:91], v[156:159], v[132:135]\n"
"ds_write_b128 v3, v[24:27] offset:10240\n"
"v_mfma_f32_16x16x32_bf16 v[132:135], v[56:59], v[152:155], v[132:135]\n"
"v_fma_f32 v254, -v255, v255, v254\n"
"v_lshlrev_b32_e32 v253, 16, v253\n"
"v_mfma_f32_16x16x32_bf16 v[132:135], v[48:51], v[148:151], v[132:135]\n"
"v_max_f32_e32 v254, 0, v254\n"
"v_cvt_pk_bf16_f32 v254, v254, 0\n"
"; @A G1 0.2\n"
"v_mfma_f32_16x16x32_bf16 v[24:27], a[0:3], v[60:63], 0\n"
"v_lshlrev_b32_e32 v254, 16, v254\n"
"v_add_f32_e32 v254, 0x3727c5ac, v254\n"
"v_mfma_f32_16x16x32_bf16 v[24:27], a[4:7], v[64:67], v[24:27]\n"
"v_cvt_pk_bf16_f32 v254, v254, 0\n"
"v_lshlrev_b32_e32 v254, 16, v254\n"
"v_mfma_f32_16x16x32_bf16 v[24:27], a[8:11], v[72:75], v[24:27]\n"
"v_sqrt_f32_e32 v254, v254\n"
"v_mfma_f32_16x16x32_bf16 v[24:27], a[12:15], v[76:79], v[24:27]\n"
"s_waitcnt vmcnt(8)\n"
"v_cvt_pk_bf16_f32 v254, v254, 0\n"
"v_lshlrev_b32_e32 v254, 16, v254\n"
"v_mfma_f32_16x16x32_bf16 v[24:27], a[16:19], v[68:71], v[24:27]\n"
"v_rcp_f32_e32 v255, v254\n"
"v_mfma_f32_16x16x32_bf16 v[24:27], a[20:23], v[196:199], v[24:27]\n"
"ds_write_b128 v3, v[132:135] offset:11264\n"
"v_mfma_f32_16x16x32_bf16 v[24:27], a[24:27], v[44:47], v[24:27]\n"
"v_mul_f32_e32 v254, 0x3e8293ee, v255\n"
"v_mfma_f32_16x16x32_bf16 v[24:27], a[28:31], v[200:203], v[24:27]\n"
"v_mfma_f32_16x16x32_bf16 v[132:135], a[60:63], v[200:203], 0\n"
"v_mfma_f32_16x16x32_bf16 v[132:135], a[56:59], v[44:47], v[132:135]\n"
"v_mfma_f32_16x16x32_bf16 v[132:135], a[52:55], v[196:199], v[132:135]\n"
"s_add_u32 s98, s52, 0x48000\n"
"buffer_load_dwordx4 v[148:151], v32, s[4:7], s98 offen sc1\n"
"v_mfma_f32_16x16x32_bf16 v[132:135], a[48:51], v[68:71], v[132:135]\n"
"s_waitcnt lgkmcnt(0)\n"
"s_barrier\n"
"v_mfma_f32_16x16x32_bf16 v[132:135], a[44:47], v[76:79], v[132:135]\n"
"ds_read_b128 v[152:155], v4\n"
"ds_read_b128 v[156:159], v9 offset:128\n"
"v_mfma_f32_16x16x32_bf16 v[132:135], a[40:43], v[72:75], v[132:135]\n"
"ds_write_b128 v3, v[24:27] offset:12288\n"
"v_mfma_f32_16x16x32_bf16 v[132:135], a[36:39], v[64:67], v[132:135]\n"
"ds_read_b128 v[24:27], v5\n"
"ds_read_b64_tr_b16 v[160:161], v40\n"
"ds_read_b64_tr_b16 v[162:163], v40 offset:1024\n"
"v_mfma_f32_16x16x32_bf16 v[132:135], a[32:35], v[60:63], v[132:135]\n"
"buffer_load_dwordx4 v[164:167], v32, s[4:7], s98 offen offset:1024 sc1\n"
"v_mfma_f32_16x16x32_bf16 v[168:171], a[64:67], v[60:63], 0\n"
"v_mfma_f32_16x16x32_bf16 v[168:171], a[68:71], v[64:67], v[168:171]\n"
"v_mfma_f32_16x16x32_bf16 v[168:171], a[72:75], v[72:75], v[168:171]\n"
"v_mfma_f32_16x16x32_bf16 v[168:171], a[76:79], v[76:79], v[168:171]\n"
"ds_read_b128 v[172:175], v4 offset:1024\n"
"ds_read_b128 v[176:179], v9 offset:192\n"
"s_waitcnt lgkmcnt(2)\n"
"buffer_store_dwordx4 v[160:163], v0, s[4:7], s24 offen sc1\n"
"v_mfma_f32_16x16x32_bf16 v[168:171], a[80:83], v[68:71], v[168:171]\n"
"buffer_load_dwordx4 v[160:163], v33, s[4:7], s98 offen sc1\n"
"ds_read_b64_tr_b16 v[204:205], v40 offset:4096\n"
"v_mfma_f32_16x16x32_bf16 v[168:171], a[84:87], v[196:199], v[168:171]\n"
"ds_write_b128 v3, v[132:135] offset:13312\n"
"v_mfma_f32_16x16x32_bf16 v[168:171], a[88:91], v[44:47], v[168:171]\n"
"ds_read_b128 v[132:135], v5 offset:1024\n"
"ds_read_b64_tr_b16 v[206:207], v40 offset:5120\n"
"v_mfma_f32_16x16x32_bf16 v[168:171], a[92:95], v[200:203], v[168:171]\n"
"v_fmac_f32_e32 v152, v253, v156\n"
"v_fmac_f32_e32 v153, v253, v157\n"
"v_mfma_f32_16x16x32_bf16 v[208:211], a[124:127], v[200:203], 0\n"
"v_fmac_f32_e32 v154, v253, v158\n"
"v_fmac_f32_e32 v155, v253, v159\n"
"v_mfma_f32_16x16x32_bf16 v[208:211], a[120:123], v[44:47], v[208:211]\n"
"buffer_load_dwordx4 v[156:159], v33, s[4:7], s98 offen offset:1024 sc1\n"
"v_mfma_f32_16x16x32_bf16 v[208:211], a[116:119], v[196:199], v[208:211]\n"
"ds_read_b128 v[212:215], v4 offset:2048\n"
"ds_read_b128 v[216:219], v9 offset:256\n"
"v_mfma_f32_16x16x32_bf16 v[208:211], a[112:115], v[68:71], v[208:211]\n"
"ds_read_b128 v[220:223], v5 offset:2048\n"
"s_waitcnt lgkmcnt(3)\n"
"buffer_store_dwordx4 v[204:207], v0, s[4:7], s24 offen offset:1024 sc1\n"
"v_add_f32_e32 v152, v152, v24\n"
"v_mfma_f32_16x16x32_bf16 v[208:211], a[108:111], v[76:79], v[208:211]\n"
"v_add_f32_e32 v153, v153, v25\n"
"v_add_f32_e32 v154, v154, v26\n"
"v_mfma_f32_16x16x32_bf16 v[208:211], a[104:107], v[72:75], v[208:211]\n"
"ds_write_b128 v3, v[168:171] offset:14336\n"
"v_mfma_f32_16x16x32_bf16 v[208:211], a[100:103], v[64:67], v[208:211]\n"
"buffer_load_dwordx4 v[168:171], v34, s[4:7], s98 offen sc1\n"
"v_mfma_f32_16x16x32_bf16 v[208:211], a[96:99], v[60:63], v[208:211]\n"
"v_add_f32_e32 v155, v155, v27\n"
"v_fmac_f32_e32 v172, v253, v176\n"
"v_mfma_f32_16x16x32_bf16 v[24:27], v[28:31], v[60:63], 0\n"
"ds_read_b64_tr_b16 v[204:205], v40 offset:8192\n"
"v_fmac_f32_e32 v173, v253, v177\n"
"v_fmac_f32_e32 v174, v253, v178\n"
"v_mfma_f32_16x16x32_bf16 v[24:27], v[52:55], v[64:67], v[24:27]\n"
"ds_read_b128 v[224:227], v4 offset:3072\n"
"ds_read_b128 v[228:231], v9 offset:320\n"
"ds_read_b64_tr_b16 v[206:207], v40 offset:9216\n"
"v_mfma_f32_16x16x32_bf16 v[24:27], v[84:87], v[72:75], v[24:27]\n"
"ds_read_b128 v[232:235], v5 offset:3072\n"
"v_fmac_f32_e32 v175, v253, v179\n"
"v_mfma_f32_16x16x32_bf16 v[24:27], v[92:95], v[76:79], v[24:27]\n"
"buffer_load_dwordx4 v[176:179], v34, s[4:7], s98 offen offset:1024 sc1\n"
"v_mfma_f32_16x16x32_bf16 v[24:27], v[100:103], v[68:71], v[24:27]\n"
"v_add_f32_e32 v172, v172, v132\n"
"v_add_f32_e32 v173, v173, v133\n"
"v_mfma_f32_16x16x32_bf16 v[24:27], v[108:111], v[196:199], v[24:27]\n"
"ds_write_b128 v3, v[208:211] offset:15360\n"
"v_mfma_f32_16x16x32_bf16 v[24:27], v[180:183], v[44:47], v[24:27]\n"
"s_waitcnt lgkmcnt(7)\n"
"s_waitcnt lgkmcnt(2)\n"
"buffer_store_dwordx4 v[204:207], v0, s[4:7], s25 offen sc1\n"
"v_add_f32_e32 v174, v174, v134\n"
"v_add_f32_e32 v175, v175, v135\n"
"v_mfma_f32_16x16x32_bf16 v[24:27], v[188:191], v[200:203], v[24:27]\n"
"v_fmac_f32_e32 v212, v253, v216\n"
"v_fmac_f32_e32 v213, v253, v217\n"
"v_mfma_f32_16x16x32_bf16 v[132:135], v[192:195], v[200:203], 0\n"
"buffer_load_dwordx4 v[200:203], v35, s[4:7], s98 offen sc1\n"
"v_mfma_f32_16x16x32_bf16 v[132:135], v[184:187], v[44:47], v[132:135]\n"
"ds_read_b128 v[44:47], v4 offset:4096\n"
"ds_read_b128 v[204:207], v9\n"
"v_mfma_f32_16x16x32_bf16 v[132:135], v[112:115], v[196:199], v[132:135]\n"
"ds_read_b128 v[196:199], v5 offset:4096\n"
"v_fmac_f32_e32 v214, v253, v218\n"
"v_mfma_f32_16x16x32_bf16 v[132:135], v[104:107], v[68:71], v[132:135]\n"
"v_fmac_f32_e32 v215, v253, v219\n"
"v_add_f32_e32 v212, v212, v220\n"
"v_mfma_f32_16x16x32_bf16 v[132:135], v[96:99], v[76:79], v[132:135]\n"
"v_add_f32_e32 v213, v213, v221\n"
"v_add_f32_e32 v214, v214, v222\n"
"v_mfma_f32_16x16x32_bf16 v[132:135], v[88:91], v[72:75], v[132:135]\n"
"buffer_load_dwordx4 v[68:71], v35, s[4:7], s98 offen offset:1024 sc1\n"
"v_mfma_f32_16x16x32_bf16 v[132:135], v[56:59], v[64:67], v[132:135]\n"
"ds_write_b128 v3, v[24:27] offset:16384\n"
"v_mfma_f32_16x16x32_bf16 v[132:135], v[48:51], v[60:63], v[132:135]\n"
"s_waitcnt vmcnt(15)\n"
"ds_read_b128 v[24:27], v4 offset:5120\n"
"ds_read_b128 v[60:63], v9 offset:64\n"
"ds_read_b64_tr_b16 v[64:65], v40 offset:12288\n"
"; @A G1 0.3\n"
"v_mfma_f32_16x16x32_bf16 v[152:155], a[0:3], v[116:119], v[152:155]\n"
"ds_read_b128 v[72:75], v5 offset:5120\n"
"ds_read_b64_tr_b16 v[66:67], v40 offset:13312\n"
"v_add_f32_e32 v215, v215, v223\n"
"v_mfma_f32_16x16x32_bf16 v[152:155], a[4:7], v[80:83], v[152:155]\n"
"s_waitcnt lgkmcnt(10)\n"
"v_fmac_f32_e32 v224, v253, v228\n"
"v_fmac_f32_e32 v225, v253, v229\n"
"v_mfma_f32_16x16x32_bf16 v[152:155], a[8:11], v[124:127], v[152:155]\n"
"s_add_u32 s98, s53, 0x48000\n"
"buffer_load_dwordx4 v[76:79], v32, s[4:7], s98 offen sc1\n"
"v_mfma_f32_16x16x32_bf16 v[152:155], a[12:15], v[128:131], v[152:155]\n"
"s_waitcnt vmcnt(12)\n"
"v_fmac_f32_e32 v226, v253, v230\n"
"v_fmac_f32_e32 v227, v253, v231\n"
"v_mfma_f32_16x16x32_bf16 v[152:155], a[16:19], v[120:123], v[152:155]\n"
"v_add_f32_e32 v224, v224, v232\n"
"v_add_f32_e32 v225, v225, v233\n"
"v_mfma_f32_16x16x32_bf16 v[152:155], a[20:23], v[136:139], v[152:155]\n"
"ds_write_b128 v3, v[132:135] offset:17408\n"
"v_mfma_f32_16x16x32_bf16 v[152:155], a[24:27], v[140:143], v[152:155]\n"
"s_waitcnt lgkmcnt(7)\n"
"s_waitcnt lgkmcnt(1)\n"
"buffer_store_dwordx4 v[64:67], v0, s[4:7], s25 offen offset:1024 sc1\n"
"v_add_f32_e32 v226, v226, v234\n"
"v_add_f32_e32 v227, v227, v235\n"
"v_mfma_f32_16x16x32_bf16 v[152:155], a[28:31], v[144:147], v[152:155]\n"
"buffer_load_dwordx4 v[64:67], v32, s[4:7], s98 offen offset:1024 sc1\n"
"v_mfma_f32_16x16x32_bf16 v[172:175], a[60:63], v[144:147], v[172:175]\n"
"v_fmac_f32_e32 v44, v253, v204\n"
"v_fmac_f32_e32 v45, v253, v205\n"
"v_mfma_f32_16x16x32_bf16 v[172:175], a[56:59], v[140:143], v[172:175]\n"
"v_fmac_f32_e32 v46, v253, v206\n"
"v_fmac_f32_e32 v47, v253, v207\n"
"v_mfma_f32_16x16x32_bf16 v[172:175], a[52:55], v[136:139], v[172:175]\n"
"s_waitcnt lgkmcnt(0)\n"
"s_barrier\n"
"v_mfma_f32_16x16x32_bf16 v[172:175], a[48:51], v[120:123], v[172:175]\n"
"ds_read_b128 v[132:135], v6\n"
"ds_read_b128 v[204:207], v6 offset:1024\n"
"v_mfma_f32_16x16x32_bf16 v[172:175], a[44:47], v[128:131], v[172:175]\n"
"buffer_load_dwordx4 v[208:211], v33, s[4:7], s98 offen sc1\n"
"v_mfma_f32_16x16x32_bf16 v[172:175], a[40:43], v[124:127], v[172:175]\n"
"v_add_f32_e32 v44, v44, v196\n"
"v_add_f32_e32 v45, v45, v197\n"
"v_mfma_f32_16x16x32_bf16 v[172:175], a[36:39], v[80:83], v[172:175]\n"
"v_add_f32_e32 v46, v46, v198\n"
"v_add_f32_e32 v47, v47, v199\n"
"v_mfma_f32_16x16x32_bf16 v[172:175], a[32:35], v[116:119], v[172:175]\n"
"v_fmac_f32_e32 v24, v253, v60\n"
"v_fmac_f32_e32 v25, v253, v61\n"
"v_mfma_f32_16x16x32_bf16 v[212:215], a[64:67], v[116:119], v[212:215]\n"
"v_fmac_f32_e32 v26, v253, v62\n"
"v_fmac_f32_e32 v27, v253, v63\n"
"v_mfma_f32_16x16x32_bf16 v[212:215], a[68:71], v[80:83], v[212:215]\n"
"buffer_load_dwordx4 v[60:63], v33, s[4:7], s98 offen offset:1024 sc1\n"
"v_mfma_f32_16x16x32_bf16 v[212:215], a[72:75], v[124:127], v[212:215]\n"
"v_add_f32_e32 v24, v24, v72\n"
"v_add_f32_e32 v25, v25, v73\n"
"v_mfma_f32_16x16x32_bf16 v[212:215], a[76:79], v[128:131], v[212:215]\n"
"v_add_f32_e32 v26, v26, v74\n"
"v_add_f32_e32 v27, v27, v75\n"
"v_mfma_f32_16x16x32_bf16 v[212:215], a[80:83], v[120:123], v[212:215]\n"
"s_waitcnt lgkmcnt(0)\n"
"v_add_f32_e32 v152, v152, v132\n"
"v_add_f32_e32 v153, v153, v133\n"
"v_mfma_f32_16x16x32_bf16 v[212:215], a[84:87], v[136:139], v[212:215]\n"
"v_add_f32_e32 v154, v154, v134\n"
"v_add_f32_e32 v155, v155, v135\n"
"v_mfma_f32_16x16x32_bf16 v[212:215], a[88:91], v[140:143], v[212:215]\n"
"buffer_load_dwordx4 v[72:75], v34, s[4:7], s98 offen sc1\n"
"v_mfma_f32_16x16x32_bf16 v[212:215], a[92:95], v[144:147], v[212:215]\n"
"ds_read_b128 v[132:135], v6 offset:2048\n"
"ds_read_b128 v[196:199], v6 offset:3072\n"
"v_mfma_f32_16x16x32_bf16 v[224:227], a[124:127], v[144:147], v[224:227]\n"
"v_add_f32_e32 v172, v172, v204\n"
"v_add_f32_e32 v173, v173, v205\n"
"v_mfma_f32_16x16x32_bf16 v[224:227], a[120:123], v[140:143], v[224:227]\n"
"v_add_f32_e32 v174, v174, v206\n"
"v_add_f32_e32 v175, v175, v207\n"
"v_mfma_f32_16x16x32_bf16 v[224:227], a[116:119], v[136:139], v[224:227]\n"
"v_mul_f32_e32 v152, v152, v255\n"
"v_mul_f32_e32 v153, v153, v255\n"
"v_mfma_f32_16x16x32_bf16 v[224:227], a[112:115], v[120:123], v[224:227]\n"
"buffer_load_dwordx4 v[204:207], v34, s[4:7], s98 offen offset:1024 sc1\n"
"v_mfma_f32_16x16x32_bf16 v[224:227], a[108:111], v[128:131], v[224:227]\n"
"v_mul_f32_e32 v154, v154, v255\n"
"v_mul_f32_e32 v155, v155, v255\n"
"v_mfma_f32_16x16x32_bf16 v[224:227], a[104:107], v[124:127], v[224:227]\n"
"v_mul_f32_e32 v172, v172, v255\n"
"v_mul_f32_e32 v173, v173, v255\n"
"v_mfma_f32_16x16x32_bf16 v[224:227], a[100:103], v[80:83], v[224:227]\n"
"v_mul_f32_e32 v174, v174, v255\n"
"v_mul_f32_e32 v175, v175, v255\n"
"v_mfma_f32_16x16x32_bf16 v[224:227], a[96:99], v[116:119], v[224:227]\n"
"v_cvt_pk_bf16_f32 v216, v152, v153\n"
"v_cvt_pk_bf16_f32 v217, v154, v155\n"
"v_mfma_f32_16x16x32_bf16 v[44:47], v[28:31], v[116:119], v[44:47]\n"
"buffer_load_dwordx4 v[152:155], v35, s[4:7], s98 offen sc1\n"
"v_mfma_f32_16x16x32_bf16 v[44:47], v[52:55], v[80:83], v[44:47]\n"
"v_cvt_pk_bf16_f32 v218, v172, v173\n"
"v_cvt_pk_bf16_f32 v219, v174, v175\n"
"v_mfma_f32_16x16x32_bf16 v[44:47], v[84:87], v[124:127], v[44:47]\n"
"ds_write_b128 v7, v[216:219] offset:24576\n"
"v_mfma_f32_16x16x32_bf16 v[44:47], v[92:95], v[128:131], v[44:47]\n"
"v_mfma_f32_16x16x32_bf16 v[44:47], v[100:103], v[120:123], v[44:47]\n"
"s_waitcnt lgkmcnt(1)\n"
"v_add_f32_e32 v212, v212, v132\n"
"v_add_f32_e32 v213, v213, v133\n"
"v_mfma_f32_16x16x32_bf16 v[44:47], v[108:111], v[136:139], v[44:47]\n"
"buffer_load_dwordx4 v[172:175], v35, s[4:7], s98 offen offset:1024 sc1\n"
"v_mfma_f32_16x16x32_bf16 v[44:47], v[180:183], v[140:143], v[44:47]\n"
"ds_read_b128 v[220:223], v6 offset:4096\n"
"ds_read_b128 v[228:231], v6 offset:5120\n"
"v_mfma_f32_16x16x32_bf16 v[44:47], v[188:191], v[144:147], v[44:47]\n"
"v_add_f32_e32 v214, v214, v134\n"
"v_add_f32_e32 v215, v215, v135\n"
"v_mfma_f32_16x16x32_bf16 v[24:27], v[192:195], v[144:147], v[24:27]\n"
"v_add_f32_e32 v224, v224, v196\n"
"v_add_f32_e32 v225, v225, v197\n"
"v_mfma_f32_16x16x32_bf16 v[24:27], v[184:187], v[140:143], v[24:27]\n"
"v_add_f32_e32 v226, v226, v198\n"
"v_add_f32_e32 v227, v227, v199\n"
"v_mfma_f32_16x16x32_bf16 v[24:27], v[112:115], v[136:139], v[24:27]\n"
"v_mul_f32_e32 v212, v212, v255\n"
"v_mul_f32_e32 v213, v213, v255\n"
"v_mfma_f32_16x16x32_bf16 v[24:27], v[104:107], v[120:123], v[24:27]\n"
"v_mul_f32_e32 v214, v214, v255\n"
"v_mul_f32_e32 v215, v215, v255\n"
"v_mfma_f32_16x16x32_bf16 v[24:27], v[96:99], v[128:131], v[24:27]\n"
"v_mul_f32_e32 v224, v224, v255\n"
"v_mul_f32_e32 v225, v225, v255\n"
"v_mfma_f32_16x16x32_bf16 v[24:27], v[88:91], v[124:127], v[24:27]\n"
"v_mul_f32_e32 v226, v226, v255\n"
"v_mul_f32_e32 v227, v227, v255\n"
"v_mfma_f32_16x16x32_bf16 v[24:27], v[56:59], v[80:83], v[24:27]\n"
"v_cvt_pk_bf16_f32 v80, v212, v213\n"
"v_cvt_pk_bf16_f32 v81, v214, v215\n"
"v_mfma_f32_16x16x32_bf16 v[24:27], v[48:51], v[116:119], v[24:27]\n"
"v_cvt_pk_bf16_f32 v82, v224, v225\n"
"v_cvt_pk_bf16_f32 v83, v226, v227\n"
"ds_write_b128 v7, v[80:83] offset:57344\n"
".Lmk_z1:\n"
"s_waitcnt vmcnt(15)\n"
"; @A G1 1.0\n"
"v_mfma_f32_16x16x32_bf16 v[116:119], a[0:3], v[148:151], 0\n"
"v_mfma_f32_16x16x32_bf16 v[116:119], a[4:7], v[164:167], v[116:119]\n"
"s_add_u32 s98, s62, 0x4800\n"
"v_mfma_f32_16x16x32_bf16 v[116:119], a[8:11], v[160:163], v[116:119]\n"
"s_add_u32 s97, s54, 0x48000\n"
"buffer_load_dwordx4 v[120:123], v32, s[4:7], s97 offen sc1\n"
"v_mfma_f32_16x16x32_bf16 v[116:119], a[12:15], v[156:159], v[116:119]\n"
"s_waitcnt vmcnt(10)\n"
"v_mfma_f32_16x16x32_bf16 v[116:119], a[16:19], v[168:171], v[116:119]\n"
"buffer_load_dword v255, v10, s[4:7], s98 offen sc1\n"
"v_mfma_f32_16x16x32_bf16 v[116:119], a[20:23], v[176:179], v[116:119]\n"
"s_waitcnt lgkmcnt(1)\n"
"v_add_f32_e32 v44, v44, v220\n"
"v_add_f32_e32 v45, v45, v221\n"
"v_mfma_f32_16x16x32_bf16 v[116:119], a[24:27], v[200:203], v[116:119]\n"
"v_add_f32_e32 v46, v46, v222\n"
"v_add_f32_e32 v47, v47, v223\n"
"v_mfma_f32_16x16x32_bf16 v[116:119], a[28:31], v[68:71], v[116:119]\n"
"buffer_load_dwordx4 v[124:127], v32, s[4:7], s97 offen offset:1024 sc1\n"
"v_mfma_f32_16x16x32_bf16 v[128:131], a[60:63], v[68:71], 0\n"
"buffer_load_dword v253, v10, s[4:7], s98 offen offset:256 sc1\n"
"v_mfma_f32_16x16x32_bf16 v[128:131], a[56:59], v[200:203], v[128:131]\n"
"v_add_f32_e32 v24, v24, v228\n"
"v_add_f32_e32 v25, v25, v229\n"
"v_mfma_f32_16x16x32_bf16 v[128:131], a[52:55], v[176:179], v[128:131]\n"
"s_waitcnt vmcnt(11)\n"
"buffer_store_dwordx2 v[18:19], v11, s[4:7], s68 offen offset:96 sc1\n"
"v_mfma_f32_16x16x32_bf16 v[128:131], a[48:51], v[168:171], v[128:131]\n"
"buffer_load_dword v252, v10, s[4:7], s98 offen offset:512 sc1\n"
"v_mfma_f32_16x16x32_bf16 v[128:131], a[44:47], v[156:159], v[128:131]\n"
"buffer_load_dwordx4 v[132:135], v33, s[4:7], s97 offen sc1\n"
"v_mfma_f32_16x16x32_bf16 v[128:131], a[40:43], v[160:163], v[128:131]\n"
"ds_write_b128 v3, v[116:119]\n"
"v_mfma_f32_16x16x32_bf16 v[128:131], a[36:39], v[164:167], v[128:131]\n"
"buffer_load_dword v251, v10, s[4:7], s98 offen offset:768 sc1\n"
"v_mfma_f32_16x16x32_bf16 v[128:131], a[32:35], v[148:151], v[128:131]\n"
"v_add_f32_e32 v26, v26, v230\n"
"v_add_f32_e32 v27, v27, v231\n"
"v_mfma_f32_16x16x32_bf16 v[116:119], a[64:67], v[148:151], 0\n"
"v_mul_f32_e32 v44, v44, v254\n"
"v_mul_f32_e32 v45, v45, v254\n"
"v_mfma_f32_16x16x32_bf16 v[116:119], a[68:71], v[164:167], v[116:119]\n"
"buffer_load_dwordx4 v[136:139], v33, s[4:7], s97 offen offset:1024 sc1\n"
"v_mfma_f32_16x16x32_bf16 v[116:119], a[72:75], v[160:163], v[116:119]\n"
"buffer_load_dword v250, v10, s[4:7], s98 offen offset:1024 sc1\n"
"v_mfma_f32_16x16x32_bf16 v[116:119], a[76:79], v[156:159], v[116:119]\n"
"v_mul_f32_e32 v46, v46, v254\n"
"v_mul_f32_e32 v47, v47, v254\n"
"v_mfma_f32_16x16x32_bf16 v[116:119], a[80:83], v[168:171], v[116:119]\n"
"v_mul_f32_e32 v24, v24, v254\n"
"v_mul_f32_e32 v25, v25, v254\n"
"v_mfma_f32_16x16x32_bf16 v[116:119], a[84:87], v[176:179], v[116:119]\n"
"ds_write_b128 v3, v[128:131] offset:1024\n"
"v_mfma_f32_16x16x32_bf16 v[116:119], a[88:91], v[200:203], v[116:119]\n"
"buffer_load_dwordx4 v[128:131], v34, s[4:7], s97 offen sc1\n"
"v_mfma_f32_16x16x32_bf16 v[116:119], a[92:95], v[68:71], v[116:119]\n"
"buffer_load_dword v249, v10, s[4:7], s98 offen offset:1280 sc1\n"
"v_mfma_f32_16x16x32_bf16 v[140:143], a[124:127], v[68:71], 0\n"
"v_mul_f32_e32 v26, v26, v254\n"
"v_mul_f32_e32 v27, v27, v254\n"
"v_mfma_f32_16x16x32_bf16 v[140:143], a[120:123], v[200:203], v[140:143]\n"
"v_cvt_pk_bf16_f32 v144, v44, v45\n"
"v_cvt_pk_bf16_f32 v145, v46, v47\n"
"v_mfma_f32_16x16x32_bf16 v[140:143], a[116:119], v[176:179], v[140:143]\n"
"buffer_load_dword v254, v10, s[4:7], s98 offen offset:1536 sc1\n"
"v_mfma_f32_16x16x32_bf16 v[140:143], a[112:115], v[168:171], v[140:143]\n"
"buffer_load_dwordx4 v[44:47], v34, s[4:7], s97 offen offset:1024 sc1\n"
"v_mfma_f32_16x16x32_bf16 v[140:143], a[108:111], v[156:159], v[140:143]\n"
"v_cvt_pk_bf16_f32 v146, v24, v25\n"
"v_cvt_pk_bf16_f32 v147, v26, v27\n"
"v_mfma_f32_16x16x32_bf16 v[140:143], a[104:107], v[160:163], v[140:143]\n"
"ds_write_b128 v3, v[116:119] offset:2048\n"
"v_mfma_f32_16x16x32_bf16 v[140:143], a[100:103], v[164:167], v[140:143]\n"
"buffer_load_dword v248, v10, s[4:7], s98 offen offset:1792 sc1\n"
"v_mfma_f32_16x16x32_bf16 v[140:143], a[96:99], v[148:151], v[140:143]\n"
"ds_write_b128 v2, v[144:147] offset:49152\n"
"v_mfma_f32_16x16x32_bf16 v[24:27], v[28:31], v[148:151], 0\n"
"buffer_load_dwordx4 v[116:119], v35, s[4:7], s97 offen sc1\n"
"v_mfma_f32_16x16x32_bf16 v[24:27], v[52:55], v[164:167], v[24:27]\n"
"v_mfma_f32_16x16x32_bf16 v[24:27], v[84:87], v[160:163], v[24:27]\n"
"v_mfma_f32_16x16x32_bf16 v[24:27], v[92:95], v[156:159], v[24:27]\n"
"v_mfma_f32_16x16x32_bf16 v[24:27], v[100:103], v[168:171], v[24:27]\n"
"v_mfma_f32_16x16x32_bf16 v[24:27], v[108:111], v[176:179], v[24:27]\n"
"buffer_load_dwordx4 v[144:147], v35, s[4:7], s97 offen offset:1024 sc1\n"
"v_mfma_f32_16x16x32_bf16 v[24:27], v[180:183], v[200:203], v[24:27]\n"
"ds_write_b128 v3, v[140:143] offset:3072\n"
"v_mfma_f32_16x16x32_bf16 v[24:27], v[188:191], v[68:71], v[24:27]\n"
"v_mfma_f32_16x16x32_bf16 v[140:143], v[192:195], v[68:71], 0\n"
"v_mfma_f32_16x16x32_bf16 v[140:143], v[184:187], v[200:203], v[140:143]\n"
"v_mfma_f32_16x16x32_bf16 v[140:143], v[112:115], v[176:179], v[140:143]\n"
"v_mfma_f32_16x16x32_bf16 v[140:143], v[104:107], v[168:171], v[140:143]\n"
"v_mfma_f32_16x16x32_bf16 v[140:143], v[96:99], v[156:159], v[140:143]\n"
"v_mfma_f32_16x16x32_bf16 v[140:143], v[88:91], v[160:163], v[140:143]\n"
"s_nop 1\n"
"ds_write_b128 v3, v[24:27] offset:4096\n"
"v_mfma_f32_16x16x32_bf16 v[140:143], v[56:59], v[164:167], v[140:143]\n"
"v_mfma_f32_16x16x32_bf16 v[140:143], v[48:51], v[148:151], v[140:143]\n"
"s_waitcnt vmcnt(21)\n"
"; @A G1 1.1\n"
"v_mfma_f32_16x16x32_bf16 v[24:27], a[0:3], v[76:79], 0\n"
"v_mov_b32_e32 v68, -1\n"
"v_mov_b32_e32 v69, -1\n"
"v_mfma_f32_16x16x32_bf16 v[24:27], a[4:7], v[64:67], v[24:27]\n"
"v_mov_b32_e32 v70, -1\n"
"v_mov_b32_e32 v71, -1\n"
"v_mfma_f32_16x16x32_bf16 v[24:27], a[8:11], v[208:211], v[24:27]\n"
"s_add_u32 s98, s55, 0x48000\n"
"buffer_load_dwordx4 v[148:151], v32, s[4:7], s98 offen sc1\n"
"v_mfma_f32_16x16x32_bf16 v[24:27], a[12:15], v[60:63], v[24:27]\n"
"s_waitcnt vmcnt(18)\n"
"ds_read_b64_tr_b16 v[156:157], v36\n"
"ds_read_b64_tr_b16 v[158:159], v36 offset:2048\n"
"s_mov_b32 s96, 0x30003\n"
"v_mfma_f32_16x16x32_bf16 v[24:27], a[16:19], v[72:75], v[24:27]\n"
"s_mov_b32 s97, 0x30003\n"
"v_mov_b32_e32 v163, 0x3f803f80\n"
"v_cndmask_b32_e64 v160, 0, v163, s[96:97]\n"
"v_cndmask_b32_e64 v161, 0, v163, s[96:97]\n"
"v_mfma_f32_16x16x32_bf16 v[24:27], a[20:23], v[204:207], v[24:27]\n"
"ds_write_b128 v3, v[140:143] offset:5120\n"
"v_mfma_f32_16x16x32_bf16 v[24:27], a[24:27], v[152:155], v[24:27]\n"
"v_cndmask_b32_e64 v162, 0, v163, s[96:97]\n"
"v_cndmask_b32_e64 v163, 0, v163, s[96:97]\n"
"v_mfma_f32_16x16x32_bf16 v[24:27], a[28:31], v[172:175], v[24:27]\n"
"buffer_load_dwordx4 v[140:143], v32, s[4:7], s98 offen offset:1024 sc1\n"
"s_waitcnt lgkmcnt(1)\n"
"v_mfma_f32_16x16x32_bf16 v[164:167], v[156:159], v[160:163], 0\n"
"buffer_store_dwordx4 v[156:159], v0, s[4:7], s20 offen sc1\n"
"s_nop 1\n"
"v_mfma_f32_16x16x32_bf16 v[156:159], a[60:63], v[172:175], 0\n"
"v_mfma_f32_16x16x32_bf16 v[156:159], a[56:59], v[152:155], v[156:159]\n"
"v_mfma_f32_16x16x32_bf16 v[156:159], a[52:55], v[204:207], v[156:159]\n"
"v_mfma_f32_16x16x32_bf16 v[156:159], a[48:51], v[72:75], v[156:159]\n"
"v_mfma_f32_16x16x32_bf16 v[156:159], a[44:47], v[60:63], v[156:159]\n"
"buffer_load_dwordx4 v[160:163], v33, s[4:7], s98 offen sc1\n"
"v_mfma_f32_16x16x32_bf16 v[156:159], a[40:43], v[208:211], v[156:159]\n"
"ds_write_b128 v3, v[24:27] offset:6144\n"
"v_mfma_f32_16x16x32_bf16 v[156:159], a[36:39], v[64:67], v[156:159]\n"
"s_add_u32 s97, s67, 0\n"
"buffer_load_dwordx2 v[246:247], v12, s[4:7], s97 offen sc1\n"
"s_add_u32 s96, s64, 0x2000\n"
"v_mfma_f32_16x16x32_bf16 v[156:159], a[32:35], v[76:79], v[156:159]\n"
"buffer_store_dwordx4 v[216:219], v7, s[4:7], s96 offen sc1\n"
"v_mfma_f32_16x16x32_bf16 v[24:27], a[64:67], v[76:79], 0\n"
"ds_read_b64_tr_b16 v[168:169], v36 offset:4096\n"
"ds_read_b64_tr_b16 v[170:171], v36 offset:6144\n"
"s_mov_b32 s94, 0x30003\n"
"v_mfma_f32_16x16x32_bf16 v[24:27], a[68:71], v[64:67], v[24:27]\n"
"buffer_load_dwordx4 v[176:179], v33, s[4:7], s98 offen offset:1024 sc1\n"
"s_mov_b32 s95, 0x30003\n"
"v_mfma_f32_16x16x32_bf16 v[24:27], a[72:75], v[208:211], v[24:27]\n"
"v_mov_b32_e32 v199, 0x3f803f80\n"
"v_cndmask_b32_e64 v196, 0, v199, s[94:95]\n"
"v_cndmask_b32_e64 v197, 0, v199, s[94:95]\n"
"v_mfma_f32_16x16x32_bf16 v[24:27], a[76:79], v[60:63], v[24:27]\n"
"v_cndmask_b32_e64 v198, 0, v199, s[94:95]\n"
"v_cndmask_b32_e64 v199, 0, v199, s[94:95]\n"
"v_mfma_f32_16x16x32_bf16 v[24:27], a[80:83], v[72:75], v[24:27]\n"
"s_waitcnt lgkmcnt(0)\n"
"v_mfma_f32_16x16x32_bf16 v[164:167], v[168:171], v[196:199], v[164:167]\n"
"buffer_store_dwordx4 v[168:171], v0, s[4:7], s20 offen offset:1024 sc1\n"
"v_mfma_f32_16x16x32_bf16 v[24:27], a[84:87], v[204:207], v[24:27]\n"
"ds_write_b128 v3, v[156:159] offset:7168\n"
"v_mfma_f32_16x16x32_bf16 v[24:27], a[88:91], v[152:155], v[24:27]\n"
"buffer_load_dwordx4 v[156:159], v34, s[4:7], s98 offen sc1\n"
"v_mfma_f32_16x16x32_bf16 v[24:27], a[92:95], v[172:175], v[24:27]\n"
"v_mfma_f32_16x16x32_bf16 v[168:171], a[124:127], v[172:175], 0\n"
"v_mfma_f32_16x16x32_bf16 v[168:171], a[120:123], v[152:155], v[168:171]\n"
"v_mfma_f32_16x16x32_bf16 v[168:171], a[116:119], v[204:207], v[168:171]\n"
"v_mfma_f32_16x16x32_bf16 v[168:171], a[112:115], v[72:75], v[168:171]\n"
"buffer_load_dwordx4 v[196:199], v34, s[4:7], s98 offen offset:1024 sc1\n"
"v_mfma_f32_16x16x32_bf16 v[168:171], a[108:111], v[60:63], v[168:171]\n"
"v_mfma_f32_16x16x32_bf16 v[168:171], a[104:107], v[208:211], v[168:171]\n"
"s_nop 0\n"
"ds_write_b128 v3, v[24:27] offset:8192\n"
"v_mfma_f32_16x16x32_bf16 v[168:171], a[100:103], v[64:67], v[168:171]\n"
"v_mfma_f32_16x16x32_bf16 v[168:171], a[96:99], v[76:79], v[168:171]\n"
"v_mfma_f32_16x16x32_bf16 v[24:27], v[28:31], v[76:79], 0\n"
"buffer_load_dwordx4 v[200:203], v35, s[4:7], s98 offen sc1\n"
"v_mfma_f32_16x16x32_bf16 v[24:27], v[52:55], v[64:67], v[24:27]\n"
"v_mfma_f32_16x16x32_bf16 v[24:27], v[84:87], v[208:211], v[24:27]\n"
"v_mfma_f32_16x16x32_bf16 v[24:27], v[92:95], v[60:63], v[24:27]\n"
"v_mfma_f32_16x16x32_bf16 v[24:27], v[100:103], v[72:75], v[24:27]\n"
"s_waitcnt vmcnt(13)\n"
"ds_read_b64_tr_b16 v[212:213], v37\n"
"v_add_f32_e32 v255, v255, v250\n"
"v_add_f32_e32 v253, v253, v249\n"
"v_mfma_f32_16x16x32_bf16 v[24:27], v[108:111], v[204:207], v[24:27]\n"
"buffer_load_dwordx4 v[216:219], v35, s[4:7], s98 offen offset:1024 sc1\n"
"s_add_u32 s98, s64, 0x6000\n"
"v_mfma_f32_16x16x32_bf16 v[24:27], v[180:183], v[152:155], v[24:27]\n"
"ds_write_b128 v3, v[168:171] offset:9216\n"
"v_mfma_f32_16x16x32_bf16 v[24:27], v[188:191], v[172:175], v[24:27]\n"
"buffer_store_dwordx4 v[80:83], v7, s[4:7], s98 offen sc1\n"
"s_nop 1\n"
"v_mfma_f32_16x16x32_bf16 v[80:83], v[192:195], v[172:175], 0\n"
"ds_read_b64_tr_b16 v[214:215], v37 offset:2048\n"
"v_add_f32_e32 v252, v252, v254\n"
"v_add_f32_e32 v251, v251, v248\n"
"v_mfma_f32_16x16x32_bf16 v[80:83], v[184:187], v[152:155], v[80:83]\n"
"s_mov_b32 s94, 0xc000c\n"
"v_add_f32_e32 v255, v255, v252\n"
"v_add_f32_e32 v253, v253, v251\n"
"s_mov_b32 s95, 0xc000c\n"
"v_mfma_f32_16x16x32_bf16 v[80:83], v[112:115], v[204:207], v[80:83]\n"
"v_add_f32_e32 v255, v255, v253\n"
"v_mov_b32_e32 v253, v255\n"
"v_mov_b32_e32 v155, 0x3f803f80\n"
"v_mfma_f32_16x16x32_bf16 v[80:83], v[104:107], v[72:75], v[80:83]\n"
"v_permlane32_swap_b32_e32 v255, v253\n"
"v_add_f32_e32 v255, v255, v253\n"
"v_cndmask_b32_e64 v152, 0, v155, s[94:95]\n"
"v_mfma_f32_16x16x32_bf16 v[80:83], v[96:99], v[60:63], v[80:83]\n"
"v_mul_f32_e32 v255, 0x3a800000, v255\n"
"v_mov_b32_e32 v253, v255\n"
"v_cndmask_b32_e64 v153, 0, v155, s[94:95]\n"
"v_mfma_f32_16x16x32_bf16 v[80:83], v[88:91], v[208:211], v[80:83]\n"
"ds_write_b128 v3, v[24:27] offset:10240\n"
"v_mfma_f32_16x16x32_bf16 v[80:83], v[56:59], v[64:67], v[80:83]\n"
"v_permlane16_swap_b32_e32 v255, v253\n"
"v_cvt_pk_bf16_f32 v254, -v255, 0\n"
"v_cndmask_b32_e64 v154, 0, v155, s[94:95]\n"
"v_mfma_f32_16x16x32_bf16 v[80:83], v[48:51], v[76:79], v[80:83]\n"
"v_fma_f32 v253, -v255, v255, v253\n"
"v_lshlrev_b32_e32 v254, 16, v254\n"
"v_cndmask_b32_e64 v155, 0, v155, s[94:95]\n"
"; @A G1 1.2\n"
"v_mfma_f32_16x16x32_bf16 v[24:27], a[0:3], v[120:123], 0\n"
"s_waitcnt lgkmcnt(1)\n"
"v_max_f32_e32 v253, 0, v253\n"
"v_cvt_pk_bf16_f32 v253, v253, 0\n"
"v_mfma_f32_16x16x32_bf16 v[164:167], v[212:215], v[152:155], v[164:167]\n"
"buffer_store_dwordx4 v[212:215], v0, s[4:7], s21 offen sc1\n"
"v_mfma_f32_16x16x32_bf16 v[24:27], a[4:7], v[124:127], v[24:27]\n"
"s_waitcnt vmcnt(9)\n"
"v_cmp_ne_u64_e32 vcc, v[246:247], v[18:19]\n"
"v_mfma_f32_16x16x32_bf16 v[24:27], a[8:11], v[132:135], v[24:27]\n"
"s_cbranch_vccnz .Lmk_f2s\n"
".Lmk_f2r:\n"
"v_lshlrev_b32_e32 v253, 16, v253\n"
"v_mfma_f32_16x16x32_bf16 v[24:27], a[12:15], v[136:139], v[24:27]\n"
"v_add_f32_e32 v253, 0x3727c5ac, v253\n"
"v_cvt_pk_bf16_f32 v253, v253, 0\n"
"v_mfma_f32_16x16x32_bf16 v[24:27], a[16:19], v[128:131], v[24:27]\n"
"v_lshlrev_b32_e32 v253, 16, v253\n"
"v_mfma_f32_16x16x32_bf16 v[24:27], a[20:23], v[44:47], v[24:27]\n"
"ds_write_b128 v3, v[80:83] offset:11264\n"
"v_mfma_f32_16x16x32_bf16 v[24:27], a[24:27], v[116:119], v[24:27]\n"
"v_sqrt_f32_e32 v253, v253\n"
"v_mfma_f32_16x16x32_bf16 v[24:27], a[28:31], v[144:147], v[24:27]\n"
"v_cvt_pk_bf16_f32 v253, v253, 0\n"
"v_lshlrev_b32_e32 v253, 16, v253\n"
"v_mfma_f32_16x16x32_bf16 v[60:63], a[60:63], v[144:147], 0\n"
"v_rcp_f32_e32 v255, v253\n"
"v_mfma_f32_16x16x32_bf16 v[60:63], a[56:59], v[116:119], v[60:63]\n"
"v_mul_f32_e32 v253, 0x3e8293ee, v255\n"
"v_mfma_f32_16x16x32_bf16 v[60:63], a[52:55], v[44:47], v[60:63]\n"
"s_add_u32 s98, s52, 0x8000\n"
"buffer_load_dwordx4 v[64:67], v32, s[4:7], s98 offen sc1\n"
"v_mfma_f32_16x16x32_bf16 v[60:63], a[48:51], v[128:131], v[60:63]\n"
"s_waitcnt lgkmcnt(0)\n"
"s_barrier\n"
"ds_read_b64_tr_b16 v[72:73], v37 offset:4096\n"
"v_mfma_f32_16x16x32_bf16 v[60:63], a[44:47], v[136:139], v[60:63]\n"
"ds_read_b128 v[76:79], v4\n"
"ds_read_b128 v[80:83], v9 offset:128\n"
"ds_read_b64_tr_b16 v[74:75], v37 offset:6144\n"
"v_mfma_f32_16x16x32_bf16 v[60:63], a[40:43], v[132:135], v[60:63]\n"
"ds_write_b128 v3, v[24:27] offset:12288\n"
"v_mfma_f32_16x16x32_bf16 v[60:63], a[36:39], v[124:127], v[60:63]\n"
"ds_read_b128 v[24:27], v5\n"
"s_mov_b32 s96, 0xc000c\n"
"s_mov_b32 s97, 0xc000c\n"
"v_mfma_f32_16x16x32_bf16 v[60:63], a[32:35], v[120:123], v[60:63]\n"
"buffer_load_dwordx4 v[152:155], v32, s[4:7], s98 offen offset:1024 sc1\n"
"v_mov_b32_e32 v171, 0x3f803f80\n"
"v_mfma_f32_16x16x32_bf16 v[172:175], a[64:67], v[120:123], 0\n"
"v_cndmask_b32_e64 v168, 0, v171, s[96:97]\n"
"v_cndmask_b32_e64 v169, 0, v171, s[96:97]\n"
"v_cndmask_b32_e64 v170, 0, v171, s[96:97]\n"
"v_mfma_f32_16x16x32_bf16 v[172:175], a[68:71], v[124:127], v[172:175]\n"
"v_cndmask_b32_e64 v171, 0, v171, s[96:97]\n"
"v_mfma_f32_16x16x32_bf16 v[172:175], a[72:75], v[132:135], v[172:175]\n"
"s_waitcnt lgkmcnt(2)\n"
"v_mfma_f32_16x16x32_bf16 v[164:167], v[72:75], v[168:171], v[164:167]\n"
"buffer_store_dwordx4 v[72:75], v0, s[4:7], s21 offen offset:1024 sc1\n"
"v_mfma_f32_16x16x32_bf16 v[172:175], a[76:79], v[136:139], v[172:175]\n"
"ds_read_b128 v[72:75], v4 offset:1024\n"
"ds_read_b128 v[168:171], v9 offset:192\n"
"v_mfma_f32_16x16x32_bf16 v[172:175], a[80:83], v[128:131], v[172:175]\n"
"buffer_load_dwordx4 v[204:207], v33, s[4:7], s98 offen sc1\n"
"v_mfma_f32_16x16x32_bf16 v[172:175], a[84:87], v[44:47], v[172:175]\n"
"ds_write_b128 v3, v[60:63] offset:13312\n"
"v_mfma_f32_16x16x32_bf16 v[172:175], a[88:91], v[116:119], v[172:175]\n"
"ds_read_b128 v[60:63], v5 offset:1024\n"
"s_waitcnt lgkmcnt(4)\n"
"v_mfma_f32_16x16x32_bf16 v[172:175], a[92:95], v[144:147], v[172:175]\n"
"v_fmac_f32_e32 v76, v254, v80\n"
"v_fmac_f32_e32 v77, v254, v81\n"
"v_mfma_f32_16x16x32_bf16 v[208:211], a[124:127], v[144:147], 0\n"
"v_fmac_f32_e32 v78, v254, v82\n"
"v_fmac_f32_e32 v79, v254, v83\n"
"v_mfma_f32_16x16x32_bf16 v[208:211], a[120:123], v[116:119], v[208:211]\n"
"buffer_load_dwordx4 v[80:83], v33, s[4:7], s98 offen offset:1024 sc1\n"
"v_mfma_f32_16x16x32_bf16 v[208:211], a[116:119], v[44:47], v[208:211]\n"
"ds_read_b128 v[212:215], v4 offset:2048\n"
"ds_read_b128 v[220:223], v9 offset:256\n"
"v_mfma_f32_16x16x32_bf16 v[208:211], a[112:115], v[128:131], v[208:211]\n"
"ds_read_b128 v[224:227], v5 offset:2048\n"
"v_add_f32_e32 v76, v76, v24\n"
"v_mfma_f32_16x16x32_bf16 v[208:211], a[108:111], v[136:139], v[208:211]\n"
"v_add_f32_e32 v77, v77, v25\n"
"v_add_f32_e32 v78, v78, v26\n"
"v_mfma_f32_16x16x32_bf16 v[208:211], a[104:107], v[132:135], v[208:211]\n"
"ds_write_b128 v3, v[172:175] offset:14336\n"
"v_mfma_f32_16x16x32_bf16 v[208:211], a[100:103], v[124:127], v[208:211]\n"
"buffer_load_dwordx4 v[172:175], v34, s[4:7], s98 offen sc1\n"
"v_mfma_f32_16x16x32_bf16 v[208:211], a[96:99], v[120:123], v[208:211]\n"
"s_waitcnt lgkmcnt(4)\n"
"v_add_f32_e32 v79, v79, v27\n"
"v_fmac_f32_e32 v72, v254, v168\n"
"v_mfma_f32_16x16x32_bf16 v[24:27], v[28:31], v[120:123], 0\n"
"ds_read_b64_tr_b16 v[228:229], v38\n"
"v_fmac_f32_e32 v73, v254, v169\n"
"v_fmac_f32_e32 v74, v254, v170\n"
"v_mfma_f32_16x16x32_bf16 v[24:27], v[52:55], v[124:127], v[24:27]\n"
"ds_read_b128 v[232:235], v4 offset:3072\n"
"ds_read_b128 v[236:239], v9 offset:320\n"
"ds_read_b64_tr_b16 v[230:231], v38 offset:2048\n"
"v_mfma_f32_16x16x32_bf16 v[24:27], v[84:87], v[132:135], v[24:27]\n"
"ds_read_b128 v[240:243], v5 offset:3072\n"
"v_fmac_f32_e32 v75, v254, v171\n"
"s_mov_b32 s96, 0x300030\n"
"v_mfma_f32_16x16x32_bf16 v[24:27], v[92:95], v[136:139], v[24:27]\n"
"buffer_load_dwordx4 v[168:171], v34, s[4:7], s98 offen offset:1024 sc1\n"
"s_mov_b32 s97, 0x300030\n"
"v_mfma_f32_16x16x32_bf16 v[24:27], v[100:103], v[128:131], v[24:27]\n"
"v_add_f32_e32 v72, v72, v60\n"
"v_add_f32_e32 v73, v73, v61\n"
"v_mov_b32_e32 v247, 0x3f803f80\n"
"v_mfma_f32_16x16x32_bf16 v[24:27], v[108:111], v[44:47], v[24:27]\n"
"ds_write_b128 v3, v[208:211] offset:15360\n"
"v_mfma_f32_16x16x32_bf16 v[24:27], v[180:183], v[116:119], v[24:27]\n"
"s_waitcnt lgkmcnt(7)\n"
"v_add_f32_e32 v74, v74, v62\n"
"v_add_f32_e32 v75, v75, v63\n"
"v_cndmask_b32_e64 v244, 0, v247, s[96:97]\n"
"v_mfma_f32_16x16x32_bf16 v[24:27], v[188:191], v[144:147], v[24:27]\n"
"v_fmac_f32_e32 v212, v254, v220\n"
"v_fmac_f32_e32 v213, v254, v221\n"
"v_cndmask_b32_e64 v245, 0, v247, s[96:97]\n"
"v_mfma_f32_16x16x32_bf16 v[60:63], v[192:195], v[144:147], 0\n"
"buffer_load_dwordx4 v[144:147], v35, s[4:7], s98 offen sc1\n"
"v_cndmask_b32_e64 v246, 0, v247, s[96:97]\n"
"v_mfma_f32_16x16x32_bf16 v[60:63], v[184:187], v[116:119], v[60:63]\n"
"ds_read_b128 v[116:119], v4 offset:4096\n"
"ds_read_b128 v[208:211], v9\n"
"v_cndmask_b32_e64 v247, 0, v247, s[96:97]\n"
"v_mfma_f32_16x16x32_bf16 v[60:63], v[112:115], v[44:47], v[60:63]\n"
"ds_read_b128 v[44:47], v5 offset:4096\n"
"v_fmac_f32_e32 v214, v254, v222\n"
"s_waitcnt lgkmcnt(5)\n"
"v_mfma_f32_16x16x32_bf16 v[164:167], v[228:231], v[244:247], v[164:167]\n"
"buffer_store_dwordx4 v[228:231], v0, s[4:7], s22 offen sc1\n"
"v_mfma_f32_16x16x32_bf16 v[60:63], v[104:107], v[128:131], v[60:63]\n"
"v_fmac_f32_e32 v215, v254, v223\n"
"v_add_f32_e32 v212, v212, v224\n"
"v_mfma_f32_16x16x32_bf16 v[60:63], v[96:99], v[136:139], v[60:63]\n"
"s_add_u32 s97, s75, 0\n"
"buffer_load_dwordx2 v[250:251], v41, s[4:7], s97 offen sc1\n"
"v_mfma_f32_16x16x32_bf16 v[60:63], v[88:91], v[132:135], v[60:63]\n"
"buffer_load_dwordx4 v[128:131], v35, s[4:7], s98 offen offset:1024 sc1\n"
"v_mfma_f32_16x16x32_bf16 v[60:63], v[56:59], v[124:127], v[60:63]\n"
"ds_write_b128 v3, v[24:27] offset:16384\n"
"v_mfma_f32_16x16x32_bf16 v[60:63], v[48:51], v[120:123], v[60:63]\n"
"s_waitcnt vmcnt(18)\n"
"ds_read_b128 v[24:27], v4 offset:5120\n"
"ds_read_b128 v[120:123], v9 offset:64\n"
"; @A G1 1.3\n"
"v_mfma_f32_16x16x32_bf16 v[76:79], a[0:3], v[148:151], v[76:79]\n"
"ds_read_b128 v[124:127], v5 offset:5120\n"
"v_add_f32_e32 v213, v213, v225\n"
"v_mfma_f32_16x16x32_bf16 v[76:79], a[4:7], v[140:143], v[76:79]\n"
"s_waitcnt lgkmcnt(8)\n"
"v_add_f32_e32 v214, v214, v226\n"
"v_add_f32_e32 v215, v215, v227\n"
"v_mfma_f32_16x16x32_bf16 v[76:79], a[8:11], v[160:163], v[76:79]\n"
"s_add_u32 s98, s53, 0x8000\n"
"buffer_load_dwordx4 v[132:135], v32, s[4:7], s98 offen sc1\n"
"v_mfma_f32_16x16x32_bf16 v[76:79], a[12:15], v[176:179], v[76:79]\n"
"s_waitcnt vmcnt(14)\n"
"v_fmac_f32_e32 v232, v254, v236\n"
"v_fmac_f32_e32 v233, v254, v237\n"
"v_mfma_f32_16x16x32_bf16 v[76:79], a[16:19], v[156:159], v[76:79]\n"
"v_fmac_f32_e32 v234, v254, v238\n"
"v_fmac_f32_e32 v235, v254, v239\n"
"v_mfma_f32_16x16x32_bf16 v[76:79], a[20:23], v[196:199], v[76:79]\n"
"ds_write_b128 v3, v[60:63] offset:17408\n"
"v_mfma_f32_16x16x32_bf16 v[76:79], a[24:27], v[200:203], v[76:79]\n"
"v_add_f32_e32 v232, v232, v240\n"
"v_add_f32_e32 v233, v233, v241\n"
"v_mfma_f32_16x16x32_bf16 v[76:79], a[28:31], v[216:219], v[76:79]\n"
"buffer_load_dwordx4 v[60:63], v32, s[4:7], s98 offen offset:1024 sc1\n"
"v_mfma_f32_16x16x32_bf16 v[72:75], a[60:63], v[216:219], v[72:75]\n"
"s_waitcnt lgkmcnt(5)\n"
"v_add_f32_e32 v234, v234, v242\n"
"v_add_f32_e32 v235, v235, v243\n"
"v_mfma_f32_16x16x32_bf16 v[72:75], a[56:59], v[200:203], v[72:75]\n"
"v_fmac_f32_e32 v116, v254, v208\n"
"v_fmac_f32_e32 v117, v254, v209\n"
"v_mfma_f32_16x16x32_bf16 v[72:75], a[52:55], v[196:199], v[72:75]\n"
"s_waitcnt lgkmcnt(0)\n"
"s_barrier\n"
"v_mfma_f32_16x16x32_bf16 v[72:75], a[48:51], v[156:159], v[72:75]\n"
"ds_read_b128 v[136:139], v6\n"
"ds_read_b128 v[220:223], v6 offset:1024\n"
"v_mfma_f32_16x16x32_bf16 v[72:75], a[44:47], v[176:179], v[72:75]\n"
"buffer_load_dwordx4 v[224:227], v33, s[4:7], s98 offen sc1\n"
"v_mfma_f32_16x16x32_bf16 v[72:75], a[40:43], v[160:163], v[72:75]\n"
"v_fmac_f32_e32 v118, v254, v210\n"
"v_fmac_f32_e32 v119, v254, v211\n"
"v_mfma_f32_16x16x32_bf16 v[72:75], a[36:39], v[140:143], v[72:75]\n"
"s_add_u32 s96, s60, 0x48000\n"
"buffer_store_dwordx4 v[68:71], v0, s[4:7], s96 offen sc1\n"
"v_add_f32_e32 v116, v116, v44\n"
"v_add_f32_e32 v117, v117, v45\n"
"v_mfma_f32_16x16x32_bf16 v[72:75], a[32:35], v[148:151], v[72:75]\n"
"v_add_f32_e32 v118, v118, v46\n"
"v_add_f32_e32 v119, v119, v47\n"
"v_mfma_f32_16x16x32_bf16 v[212:215], a[64:67], v[148:151], v[212:215]\n"
"v_fmac_f32_e32 v24, v254, v120\n"
"v_fmac_f32_e32 v25, v254, v121\n"
"v_mfma_f32_16x16x32_bf16 v[212:215], a[68:71], v[140:143], v[212:215]\n"
"buffer_load_dwordx4 v[44:47], v33, s[4:7], s98 offen offset:1024 sc1\n"
"v_mfma_f32_16x16x32_bf16 v[212:215], a[72:75], v[160:163], v[212:215]\n"
"v_fmac_f32_e32 v26, v254, v122\n"
"v_fmac_f32_e32 v27, v254, v123\n"
"v_mfma_f32_16x16x32_bf16 v[212:215], a[76:79], v[176:179], v[212:215]\n"
"v_add_f32_e32 v24, v24, v124\n"
"v_add_f32_e32 v25, v25, v125\n"
"v_mfma_f32_16x16x32_bf16 v[212:215], a[80:83], v[156:159], v[212:215]\n"
"s_waitcnt lgkmcnt(0)\n"
"v_add_f32_e32 v26, v26, v126\n"
"v_add_f32_e32 v27, v27, v127\n"
"v_mfma_f32_16x16x32_bf16 v[212:215], a[84:87], v[196:199], v[212:215]\n"
"v_add_f32_e32 v76, v76, v136\n"
"v_add_f32_e32 v77, v77, v137\n"
"v_mfma_f32_16x16x32_bf16 v[212:215], a[88:91], v[200:203], v[212:215]\n"
"buffer_load_dwordx4 v[120:123], v34, s[4:7], s98 offen sc1\n"
"v_mfma_f32_16x16x32_bf16 v[212:215], a[92:95], v[216:219], v[212:215]\n"
"ds_read_b128 v[124:127], v6 offset:2048\n"
"ds_read_b128 v[208:211], v6 offset:3072\n"
"v_mfma_f32_16x16x32_bf16 v[232:235], a[124:127], v[216:219], v[232:235]\n"
"v_add_f32_e32 v78, v78, v138\n"
"v_add_f32_e32 v79, v79, v139\n"
"v_mfma_f32_16x16x32_bf16 v[232:235], a[120:123], v[200:203], v[232:235]\n"
"v_add_f32_e32 v72, v72, v220\n"
"v_add_f32_e32 v73, v73, v221\n"
"v_mfma_f32_16x16x32_bf16 v[232:235], a[116:119], v[196:199], v[232:235]\n"
"v_add_f32_e32 v74, v74, v222\n"
"v_add_f32_e32 v75, v75, v223\n"
"v_mfma_f32_16x16x32_bf16 v[232:235], a[112:115], v[156:159], v[232:235]\n"
"buffer_load_dwordx4 v[136:139], v34, s[4:7], s98 offen offset:1024 sc1\n"
"v_mfma_f32_16x16x32_bf16 v[232:235], a[108:111], v[176:179], v[232:235]\n"
"v_mul_f32_e32 v76, v76, v255\n"
"v_mul_f32_e32 v77, v77, v255\n"
"v_mfma_f32_16x16x32_bf16 v[232:235], a[104:107], v[160:163], v[232:235]\n"
"v_mul_f32_e32 v78, v78, v255\n"
"v_mul_f32_e32 v79, v79, v255\n"
"v_mfma_f32_16x16x32_bf16 v[232:235], a[100:103], v[140:143], v[232:235]\n"
"v_mul_f32_e32 v72, v72, v255\n"
"v_mul_f32_e32 v73, v73, v255\n"
"v_mfma_f32_16x16x32_bf16 v[232:235], a[96:99], v[148:151], v[232:235]\n"
"v_mul_f32_e32 v74, v74, v255\n"
"v_mul_f32_e32 v75, v75, v255\n"
"v_mfma_f32_16x16x32_bf16 v[116:119], v[28:31], v[148:151], v[116:119]\n"
"buffer_load_dwordx4 v[220:223], v35, s[4:7], s98 offen sc1\n"
"v_mfma_f32_16x16x32_bf16 v[116:119], v[52:55], v[140:143], v[116:119]\n"
"v_cvt_pk_bf16_f32 v228, v76, v77\n"
"v_cvt_pk_bf16_f32 v229, v78, v79\n"
"v_mfma_f32_16x16x32_bf16 v[116:119], v[84:87], v[160:163], v[116:119]\n"
"v_cvt_pk_bf16_f32 v230, v72, v73\n"
"v_cvt_pk_bf16_f32 v231, v74, v75\n"
"v_mfma_f32_16x16x32_bf16 v[116:119], v[92:95], v[176:179], v[116:119]\n"
"ds_write_b128 v8, v[228:231] offset:25600\n"
"v_mfma_f32_16x16x32_bf16 v[116:119], v[100:103], v[156:159], v[116:119]\n"
"s_waitcnt lgkmcnt(1)\n"
"s_add_u32 s96, s60, 0x40000\n"
"buffer_store_dwordx4 v[68:71], v0, s[4:7], s96 offen sc1\n"
"v_add_f32_e32 v212, v212, v124\n"
"v_add_f32_e32 v213, v213, v125\n"
"v_mfma_f32_16x16x32_bf16 v[116:119], v[108:111], v[196:199], v[116:119]\n"
"buffer_load_dwordx4 v[68:71], v35, s[4:7], s98 offen offset:1024 sc1\n"
"v_mfma_f32_16x16x32_bf16 v[116:119], v[180:183], v[200:203], v[116:119]\n"
"ds_read_b128 v[72:75], v6 offset:4096\n"
"ds_read_b128 v[76:79], v6 offset:5120\n"
"v_mfma_f32_16x16x32_bf16 v[116:119], v[188:191], v[216:219], v[116:119]\n"
"v_add_f32_e32 v214, v214, v126\n"
"v_add_f32_e32 v215, v215, v127\n"
"v_mfma_f32_16x16x32_bf16 v[24:27], v[192:195], v[216:219], v[24:27]\n"
"s_waitcnt vmcnt(11)\n"
"v_cmp_ne_u64_e32 vcc, v[250:251], v[18:19]\n"
"v_mfma_f32_16x16x32_bf16 v[24:27], v[184:187], v[200:203], v[24:27]\n"
"s_cbranch_vccnz .Lmk_f5s\n"
".Lmk_f5r:\n"
"v_add_f32_e32 v232, v232, v208\n"
"v_mfma_f32_16x16x32_bf16 v[24:27], v[112:115], v[196:199], v[24:27]\n"
"v_add_f32_e32 v233, v233, v209\n"
"v_add_f32_e32 v234, v234, v210\n"
"v_mfma_f32_16x16x32_bf16 v[24:27], v[104:107], v[156:159], v[24:27]\n"
"v_add_f32_e32 v235, v235, v211\n"
"v_mul_f32_e32 v212, v212, v255\n"
"v_mfma_f32_16x16x32_bf16 v[24:27], v[96:99], v[176:179], v[24:27]\n"
"v_mul_f32_e32 v213, v213, v255\n"
"v_mul_f32_e32 v214, v214, v255\n"
"v_mfma_f32_16x16x32_bf16 v[24:27], v[88:91], v[160:163], v[24:27]\n"
"v_mul_f32_e32 v215, v215, v255\n"
"v_mul_f32_e32 v232, v232, v255\n"
"v_mfma_f32_16x16x32_bf16 v[24:27], v[56:59], v[140:143], v[24:27]\n"
"v_mul_f32_e32 v233, v233, v255\n"
"v_mul_f32_e32 v234, v234, v255\n"
"v_mfma_f32_16x16x32_bf16 v[24:27], v[48:51], v[148:151], v[24:27]\n"
"v_mul_f32_e32 v235, v235, v255\n"
"v_cvt_pk_bf16_f32 v40, v212, v213\n"
"v_cvt_pk_bf16_f32 v41, v214, v215\n"
"v_cvt_pk_bf16_f32 v42, v232, v233\n"
"v_cvt_pk_bf16_f32 v43, v234, v235\n"
"ds_write_b128 v7, v[40:43] offset:58368\n"
".Lmk_z2:\n"
"; @A G1 2.0\n"
"v_mfma_f32_16x16x32_bf16 v[124:127], a[0:3], v[64:67], 0\n"
"v_mfma_f32_16x16x32_bf16 v[124:127], a[4:7], v[152:155], v[124:127]\n"
"s_add_u32 s98, s62, 0x800\n"
"v_mfma_f32_16x16x32_bf16 v[124:127], a[8:11], v[204:207], v[124:127]\n"
"s_add_u32 s97, s54, 0x8000\n"
"buffer_load_dwordx4 v[140:143], v32, s[4:7], s97 offen sc1\n"
"v_mfma_f32_16x16x32_bf16 v[124:127], a[12:15], v[80:83], v[124:127]\n"
"s_waitcnt vmcnt(11)\n"
"v_mfma_f32_16x16x32_bf16 v[124:127], a[16:19], v[172:175], v[124:127]\n"
"buffer_load_dword v255, v10, s[4:7], s98 offen sc1\n"
"v_mfma_f32_16x16x32_bf16 v[124:127], a[20:23], v[168:171], v[124:127]\n"
"s_waitcnt lgkmcnt(1)\n"
"v_add_f32_e32 v116, v116, v72\n"
"v_add_f32_e32 v117, v117, v73\n"
"v_mfma_f32_16x16x32_bf16 v[124:127], a[24:27], v[144:147], v[124:127]\n"
"v_add_f32_e32 v118, v118, v74\n"
"v_add_f32_e32 v119, v119, v75\n"
"v_mfma_f32_16x16x32_bf16 v[124:127], a[28:31], v[128:131], v[124:127]\n"
"buffer_load_dwordx4 v[72:75], v32, s[4:7], s97 offen offset:1024 sc1\n"
"v_mfma_f32_16x16x32_bf16 v[148:151], a[60:63], v[128:131], 0\n"
"s_add_u32 s96, s70, 0\n"
"buffer_load_dwordx4 a[128:131], v0, s[4:7], s96 offen sc1\n"
"v_mfma_f32_16x16x32_bf16 v[148:151], a[56:59], v[144:147], v[148:151]\n"
"buffer_load_dword v254, v10, s[4:7], s98 offen offset:256 sc1\n"
"v_mfma_f32_16x16x32_bf16 v[148:151], a[52:55], v[168:171], v[148:151]\n"
"v_add_f32_e32 v24, v24, v76\n"
"v_add_f32_e32 v25, v25, v77\n"
"v_mfma_f32_16x16x32_bf16 v[148:151], a[48:51], v[172:175], v[148:151]\n"
"v_add_f32_e32 v26, v26, v78\n"
"v_add_f32_e32 v27, v27, v79\n"
"v_mfma_f32_16x16x32_bf16 v[148:151], a[44:47], v[80:83], v[148:151]\n"
"buffer_load_dwordx4 v[76:79], v33, s[4:7], s97 offen sc1\n"
"v_mfma_f32_16x16x32_bf16 v[148:151], a[40:43], v[204:207], v[148:151]\n"
"ds_write_b128 v3, v[124:127]\n"
"v_mfma_f32_16x16x32_bf16 v[148:151], a[36:39], v[152:155], v[148:151]\n"
"buffer_load_dword v252, v10, s[4:7], s98 offen offset:512 sc1\n"
"v_mfma_f32_16x16x32_bf16 v[148:151], a[32:35], v[64:67], v[148:151]\n"
"v_mul_f32_e32 v116, v116, v253\n"
"v_mul_f32_e32 v117, v117, v253\n"
"v_mfma_f32_16x16x32_bf16 v[124:127], a[64:67], v[64:67], 0\n"
"v_mul_f32_e32 v118, v118, v253\n"
"v_mul_f32_e32 v119, v119, v253\n"
"v_mfma_f32_16x16x32_bf16 v[124:127], a[68:71], v[152:155], v[124:127]\n"
"buffer_load_dwordx4 v[156:159], v33, s[4:7], s97 offen offset:1024 sc1\n"
"v_mfma_f32_16x16x32_bf16 v[124:127], a[72:75], v[204:207], v[124:127]\n"
"buffer_load_dwordx4 a[132:135], v0, s[4:7], s96 offen offset:1024 sc1\n"
"v_mfma_f32_16x16x32_bf16 v[124:127], a[76:79], v[80:83], v[124:127]\n"
"buffer_load_dword v251, v10, s[4:7], s98 offen offset:768 sc1\n"
"v_mfma_f32_16x16x32_bf16 v[124:127], a[80:83], v[172:175], v[124:127]\n"
"v_mul_f32_e32 v24, v24, v253\n"
"v_mul_f32_e32 v25, v25, v253\n"
"v_mfma_f32_16x16x32_bf16 v[124:127], a[84:87], v[168:171], v[124:127]\n"
"ds_write_b128 v3, v[148:151] offset:1024\n"
"v_mfma_f32_16x16x32_bf16 v[124:127], a[88:91], v[144:147], v[124:127]\n"
"buffer_load_dwordx4 v[148:151], v34, s[4:7], s97 offen sc1\n"
"v_mfma_f32_16x16x32_bf16 v[124:127], a[92:95], v[128:131], v[124:127]\n"
"buffer_load_dword v250, v10, s[4:7], s98 offen offset:1024 sc1\n"
"v_mfma_f32_16x16x32_bf16 v[160:163], a[124:127], v[128:131], 0\n"
"v_mul_f32_e32 v26, v26, v253\n"
"v_mul_f32_e32 v27, v27, v253\n"
"v_mfma_f32_16x16x32_bf16 v[160:163], a[120:123], v[144:147], v[160:163]\n"
"v_cvt_pk_bf16_f32 v176, v116, v117\n"
"v_cvt_pk_bf16_f32 v177, v118, v119\n"
"v_mfma_f32_16x16x32_bf16 v[160:163], a[116:119], v[168:171], v[160:163]\n"
"buffer_load_dword v253, v10, s[4:7], s98 offen offset:1280 sc1\n"
"v_mfma_f32_16x16x32_bf16 v[160:163], a[112:115], v[172:175], v[160:163]\n"
"buffer_load_dwordx4 v[116:119], v34, s[4:7], s97 offen offset:1024 sc1\n"
"v_mfma_f32_16x16x32_bf16 v[160:163], a[108:111], v[80:83], v[160:163]\n"
"buffer_load_dwordx4 a[136:139], v0, s[4:7], s96 offen offset:2048 sc1\n"
"v_mfma_f32_16x16x32_bf16 v[160:163], a[104:107], v[204:207], v[160:163]\n"
"ds_write_b128 v3, v[124:127] offset:2048\n"
"v_mfma_f32_16x16x32_bf16 v[160:163], a[100:103], v[152:155], v[160:163]\n"
"buffer_load_dword v249, v10, s[4:7], s98 offen offset:1536 sc1\n"
"v_mfma_f32_16x16x32_bf16 v[160:163], a[96:99], v[64:67], v[160:163]\n"
"v_cvt_pk_bf16_f32 v178, v24, v25\n"
"v_cvt_pk_bf16_f32 v179, v26, v27\n"
"v_mfma_f32_16x16x32_bf16 v[24:27], v[28:31], v[64:67], 0\n"
"buffer_load_dwordx4 v[124:127], v35, s[4:7], s97 offen sc1\n"
"v_mfma_f32_16x16x32_bf16 v[24:27], v[52:55], v[152:155], v[24:27]\n"
"buffer_load_dword v248, v10, s[4:7], s98 offen offset:1792 sc1\n"
"v_mfma_f32_16x16x32_bf16 v[24:27], v[84:87], v[204:207], v[24:27]\n"
"ds_write_b128 v2, v[176:179] offset:50176\n"
"v_mfma_f32_16x16x32_bf16 v[24:27], v[92:95], v[80:83], v[24:27]\n"
"s_add_u32 s98, s64, 0x2000\n"
"buffer_store_dwordx4 v[228:231], v8, s[4:7], s98 offen offset:1024 sc1\n"
"v_mfma_f32_16x16x32_bf16 v[24:27], v[100:103], v[172:175], v[24:27]\n"
"v_mfma_f32_16x16x32_bf16 v[24:27], v[108:111], v[168:171], v[24:27]\n"
"buffer_load_dwordx4 v[176:179], v35, s[4:7], s97 offen offset:1024 sc1\n"
"v_mfma_f32_16x16x32_bf16 v[24:27], v[180:183], v[144:147], v[24:27]\n"
"ds_write_b128 v3, v[160:163] offset:3072\n"
"v_mfma_f32_16x16x32_bf16 v[24:27], v[188:191], v[128:131], v[24:27]\n"
"buffer_load_dwordx4 a[140:143], v0, s[4:7], s96 offen offset:3072 sc1\n"
"v_mfma_f32_16x16x32_bf16 v[160:163], v[192:195], v[128:131], 0\n"
"v_mfma_f32_16x16x32_bf16 v[160:163], v[184:187], v[144:147], v[160:163]\n"
"v_mfma_f32_16x16x32_bf16 v[160:163], v[112:115], v[168:171], v[160:163]\n"
"v_mfma_f32_16x16x32_bf16 v[160:163], v[104:107], v[172:175], v[160:163]\n"
"v_mfma_f32_16x16x32_bf16 v[160:163], v[96:99], v[80:83], v[160:163]\n"
"v_mfma_f32_16x16x32_bf16 v[160:163], v[88:91], v[204:207], v[160:163]\n"
"s_nop 0\n"
"ds_write_b128 v3, v[24:27] offset:4096\n"
"v_mfma_f32_16x16x32_bf16 v[160:163], v[56:59], v[152:155], v[160:163]\n"
"v_mfma_f32_16x16x32_bf16 v[160:163], v[48:51], v[64:67], v[160:163]\n"
"s_waitcnt vmcnt(26)\n"
"; @A G1 2.1\n"
"v_mfma_f32_16x16x32_bf16 v[24:27], a[0:3], v[132:135], 0\n"
"v_mov_b32_e32 v64, -1\n"
"v_mov_b32_e32 v65, -1\n"
"v_mfma_f32_16x16x32_bf16 v[24:27], a[4:7], v[60:63], v[24:27]\n"
"buffer_load_dwordx4 a[144:147], v1, s[4:7], s96 offen sc1\n"
"v_mfma_f32_16x16x32_bf16 v[24:27], a[8:11], v[224:227], v[24:27]\n"
"s_add_u32 s98, s55, 0x8000\n"
"buffer_load_dwordx4 v[80:83], v32, s[4:7], s98 offen sc1\n"
"v_mfma_f32_16x16x32_bf16 v[24:27], a[12:15], v[44:47], v[24:27]\n"
"s_waitcnt vmcnt(23)\n"
"v_mov_b32_e32 v66, -1\n"
"v_mov_b32_e32 v67, -1\n"
"v_mfma_f32_16x16x32_bf16 v[24:27], a[16:19], v[120:123], v[24:27]\n"
"ds_read_b64_tr_b16 v[128:129], v38 offset:4096\n"
"ds_read_b64_tr_b16 v[130:131], v38 offset:6144\n"
"s_mov_b32 s94, 0x300030\n"
"v_mfma_f32_16x16x32_bf16 v[24:27], a[20:23], v[136:139], v[24:27]\n"
"ds_write_b128 v3, v[160:163] offset:5120\n"
"v_mfma_f32_16x16x32_bf16 v[24:27], a[24:27], v[220:223], v[24:27]\n"
"s_mov_b32 s95, 0x300030\n"
"v_mov_b32_e32 v147, 0x3f803f80\n"
"v_cndmask_b32_e64 v144, 0, v147, s[94:95]\n"
"v_cndmask_b32_e64 v145, 0, v147, s[94:95]\n"
"v_mfma_f32_16x16x32_bf16 v[24:27], a[28:31], v[68:71], v[24:27]\n"
"buffer_load_dwordx4 v[152:155], v32, s[4:7], s98 offen offset:1024 sc1\n"
"v_cndmask_b32_e64 v146, 0, v147, s[94:95]\n"
"v_mfma_f32_16x16x32_bf16 v[160:163], a[60:63], v[68:71], 0\n"
"v_cndmask_b32_e64 v147, 0, v147, s[94:95]\n"
"v_mfma_f32_16x16x32_bf16 v[160:163], a[56:59], v[220:223], v[160:163]\n"
"s_add_u32 s97, s64, 0x6000\n"
"s_waitcnt lgkmcnt(1)\n"
"v_mfma_f32_16x16x32_bf16 v[164:167], v[128:131], v[144:147], v[164:167]\n"
"buffer_store_dwordx4 v[128:131], v0, s[4:7], s22 offen offset:1024 sc1\n"
"v_mfma_f32_16x16x32_bf16 v[160:163], a[52:55], v[136:139], v[160:163]\n"
"v_mfma_f32_16x16x32_bf16 v[160:163], a[48:51], v[120:123], v[160:163]\n"
"buffer_load_dwordx4 a[148:151], v1, s[4:7], s96 offen offset:1024 sc1\n"
"v_mfma_f32_16x16x32_bf16 v[160:163], a[44:47], v[44:47], v[160:163]\n"
"buffer_load_dwordx4 v[128:131], v33, s[4:7], s98 offen sc1\n"
"v_mfma_f32_16x16x32_bf16 v[160:163], a[40:43], v[224:227], v[160:163]\n"
"ds_write_b128 v3, v[24:27] offset:6144\n"
"v_mfma_f32_16x16x32_bf16 v[160:163], a[36:39], v[60:63], v[160:163]\n"
"buffer_store_dwordx4 v[40:43], v7, s[4:7], s97 offen offset:1024 sc1\n"
"v_mfma_f32_16x16x32_bf16 v[160:163], a[32:35], v[132:135], v[160:163]\n"
"buffer_store_dwordx4 v[164:167], v39, s[4:7], s23 offen sc1\n"
"v_mfma_f32_16x16x32_bf16 v[24:27], a[64:67], v[132:135], 0\n"
"v_mfma_f32_16x16x32_bf16 v[24:27], a[68:71], v[60:63], v[24:27]\n"
"buffer_load_dwordx4 v[36:39], v33, s[4:7], s98 offen offset:1024 sc1\n"
"v_mfma_f32_16x16x32_bf16 v[24:27], a[72:75], v[224:227], v[24:27]\n"
"v_mfma_f32_16x16x32_bf16 v[24:27], a[76:79], v[44:47], v[24:27]\n"
"v_mfma_f32_16x16x32_bf16 v[24:27], a[80:83], v[120:123], v[24:27]\n"
"v_mfma_f32_16x16x32_bf16 v[24:27], a[84:87], v[136:139], v[24:27]\n"
"ds_write_b128 v3, v[160:163] offset:7168\n"
"v_mfma_f32_16x16x32_bf16 v[24:27], a[88:91], v[220:223], v[24:27]\n"
"buffer_load_dwordx4 v[40:43], v34, s[4:7], s98 offen sc1\n"
"v_mfma_f32_16x16x32_bf16 v[24:27], a[92:95], v[68:71], v[24:27]\n"
"buffer_load_dwordx4 a[152:155], v1, s[4:7], s96 offen offset:2048 sc1\n"
"v_mfma_f32_16x16x32_bf16 v[144:147], a[124:127], v[68:71], 0\n"
"v_mfma_f32_16x16x32_bf16 v[144:147], a[120:123], v[220:223], v[144:147]\n"
"v_mfma_f32_16x16x32_bf16 v[144:147], a[116:119], v[136:139], v[144:147]\n"
"v_mfma_f32_16x16x32_bf16 v[144:147], a[112:115], v[120:123], v[144:147]\n"
"buffer_load_dwordx4 v[160:163], v34, s[4:7], s98 offen offset:1024 sc1\n"
"v_mfma_f32_16x16x32_bf16 v[144:147], a[108:111], v[44:47], v[144:147]\n"
"s_add_u32 s97, s51, 0\n"
"s_bitcmp1_b32 s26, 0\n"
"s_cbranch_scc1 .Lmk_w7s\n"
"buffer_store_dwordx4 a[0:3], v0, s[4:7], s97 offen sc1\n"
".Lmk_w7r:\n"
"v_mfma_f32_16x16x32_bf16 v[144:147], a[104:107], v[224:227], v[144:147]\n"
"ds_write_b128 v3, v[24:27] offset:8192\n"
"v_mfma_f32_16x16x32_bf16 v[144:147], a[100:103], v[60:63], v[144:147]\n"
"v_mfma_f32_16x16x32_bf16 v[144:147], a[96:99], v[132:135], v[144:147]\n"
"v_mfma_f32_16x16x32_bf16 v[24:27], v[28:31], v[132:135], 0\n"
"buffer_load_dwordx4 v[164:167], v35, s[4:7], s98 offen sc1\n"
"v_mfma_f32_16x16x32_bf16 v[24:27], v[52:55], v[60:63], v[24:27]\n"
"buffer_load_dwordx4 a[156:159], v1, s[4:7], s96 offen offset:3072 sc1\n"
"v_mfma_f32_16x16x32_bf16 v[24:27], v[84:87], v[224:227], v[24:27]\n"
"v_mfma_f32_16x16x32_bf16 v[24:27], v[92:95], v[44:47], v[24:27]\n"
"v_mfma_f32_16x16x32_bf16 v[24:27], v[100:103], v[120:123], v[24:27]\n"
"s_waitcnt vmcnt(18)\n"
"v_add_f32_e32 v255, v255, v250\n"
"v_add_f32_e32 v254, v254, v253\n"
"v_mfma_f32_16x16x32_bf16 v[24:27], v[108:111], v[136:139], v[24:27]\n"
"buffer_load_dwordx4 v[168:171], v35, s[4:7], s98 offen offset:1024 sc1\n"
"v_mfma_f32_16x16x32_bf16 v[24:27], v[180:183], v[220:223], v[24:27]\n"
"ds_write_b128 v3, v[144:147] offset:9216\n"
"v_mfma_f32_16x16x32_bf16 v[24:27], v[188:191], v[68:71], v[24:27]\n"
"v_add_f32_e32 v252, v252, v249\n"
"v_add_f32_e32 v251, v251, v248\n"
"v_mfma_f32_16x16x32_bf16 v[144:147], v[192:195], v[68:71], 0\n"
"v_add_f32_e32 v255, v255, v252\n"
"v_add_f32_e32 v254, v254, v251\n"
"v_mfma_f32_16x16x32_bf16 v[144:147], v[184:187], v[220:223], v[144:147]\n"
"s_add_u32 s98, s70, 0x2000\n"
"v_add_f32_e32 v255, v255, v254\n"
"v_mov_b32_e32 v254, v255\n"
"v_mfma_f32_16x16x32_bf16 v[144:147], v[112:115], v[136:139], v[144:147]\n"
"s_nop 0\n"
"v_permlane32_swap_b32_e32 v255, v254\n"
"v_add_f32_e32 v255, v255, v254\n"
"v_mfma_f32_16x16x32_bf16 v[144:147], v[104:107], v[120:123], v[144:147]\n"
"buffer_load_dwordx4 a[160:163], v0, s[4:7], s98 offen sc1\n"
"s_add_u32 s97, s51, 0x2000\n"
"v_mfma_f32_16x16x32_bf16 v[144:147], v[96:99], v[44:47], v[144:147]\n"
"s_bitcmp1_b32 s26, 0\n"
"v_mul_f32_e32 v255, 0x3a800000, v255\n"
"v_mov_b32_e32 v254, v255\n"
"s_cbranch_scc1 .Lmk_w8s\n"
"buffer_store_dwordx4 a[32:35], v0, s[4:7], s97 offen sc1\n"
".Lmk_w8r:\n"
"v_mfma_f32_16x16x32_bf16 v[144:147], v[88:91], v[224:227], v[144:147]\n"
"ds_write_b128 v3, v[24:27] offset:10240\n"
"v_mfma_f32_16x16x32_bf16 v[144:147], v[56:59], v[60:63], v[144:147]\n"
"v_permlane16_swap_b32_e32 v255, v254\n"
"v_cvt_pk_bf16_f32 v253, -v255, 0\n"
"v_mfma_f32_16x16x32_bf16 v[144:147], v[48:51], v[132:135], v[144:147]\n"
"v_fma_f32 v254, -v255, v255, v254\n"
"v_lshlrev_b32_e32 v253, 16, v253\n"
"; @A G1 2.2\n"
"v_mfma_f32_16x16x32_bf16 v[24:27], a[0:3], v[140:143], 0\n"
"v_max_f32_e32 v254, 0, v254\n"
"v_cvt_pk_bf16_f32 v254, v254, 0\n"
"v_mfma_f32_16x16x32_bf16 v[24:27], a[4:7], v[72:75], v[24:27]\n"
"v_lshlrev_b32_e32 v254, 16, v254\n"
"v_add_f32_e32 v254, 0x3727c5ac, v254\n"
"v_mfma_f32_16x16x32_bf16 v[24:27], a[8:11], v[76:79], v[24:27]\n"
"v_cvt_pk_bf16_f32 v254, v254, 0\n"
"v_lshlrev_b32_e32 v254, 16, v254\n"
"v_mfma_f32_16x16x32_bf16 v[24:27], a[12:15], v[156:159], v[24:27]\n"
"s_waitcnt vmcnt(19)\n"
"v_sqrt_f32_e32 v254, v254\n"
"v_mfma_f32_16x16x32_bf16 v[24:27], a[16:19], v[148:151], v[24:27]\n"
"s_mov_b32 s97, .Lmk_z4-.Lmk_g16\n"
"s_getpc_b64 s[84:85]\n"
".Lmk_g16:\n"
"s_add_u32 s84, s84, s97\n"
"s_addc_u32 s85, s85, 0\n"
"s_and_b32 s84, s84, -16\n"
"v_mfma_f32_16x16x32_bf16 v[24:27], a[20:23], v[116:119], v[24:27]\n"
"ds_write_b128 v3, v[144:147] offset:11264\n"
"v_mfma_f32_16x16x32_bf16 v[24:27], a[24:27], v[124:127], v[24:27]\n"
"buffer_load_dwordx4 a[164:167], v0, s[4:7], s98 offen offset:1024 sc1\n"
"s_and_b32 s85, s85, 0xffff\n"
"s_sub_u32 s97, .Lmk_end-.Lmk_g16, s97\n"
"v_mfma_f32_16x16x32_bf16 v[24:27], a[28:31], v[176:179], v[24:27]\n"
"s_min_u32 s86, s97, 0x4000\n"
"s_mov_b32 s87, 0x20000\n"
"v_cvt_pk_bf16_f32 v254, v254, 0\n"
"v_mfma_f32_16x16x32_bf16 v[44:47], a[60:63], v[176:179], 0\n"
"s_mov_b32 m0, 0x27800\n"
"s_nop 0\n"
"buffer_load_dwordx4 v0, s[84:87], s99 offen lds\n"
"v_mfma_f32_16x16x32_bf16 v[44:47], a[56:59], v[124:127], v[44:47]\n"
"buffer_store_dwordx4 v[64:67], v0, s[4:7], s60 offen sc1\n"
"v_lshlrev_b32_e32 v254, 16, v254\n"
"v_mfma_f32_16x16x32_bf16 v[44:47], a[52:55], v[116:119], v[44:47]\n"
"s_add_u32 s97, s52, 0\n"
"buffer_load_dwordx4 v[60:63], v32, s[4:7], s97 offen sc1\n"
"v_mfma_f32_16x16x32_bf16 v[44:47], a[48:51], v[148:151], v[44:47]\n"
"s_waitcnt lgkmcnt(0)\n"
"s_barrier\n"
"v_mfma_f32_16x16x32_bf16 v[44:47], a[44:47], v[156:159], v[44:47]\n"
"v_rcp_f32_e32 v255, v254\n"
"v_mfma_f32_16x16x32_bf16 v[44:47], a[40:43], v[76:79], v[44:47]\n"
"ds_write_b128 v3, v[24:27] offset:12288\n"
"v_mfma_f32_16x16x32_bf16 v[44:47], a[36:39], v[72:75], v[44:47]\n"
"ds_read_b128 v[24:27], v4\n"
"v_mul_f32_e32 v254, 0x3e8293ee, v255\n"
"v_mfma_f32_16x16x32_bf16 v[44:47], a[32:35], v[140:143], v[44:47]\n"
"buffer_load_dwordx4 v[68:71], v32, s[4:7], s97 offen offset:1024 sc1\n"
"v_mfma_f32_16x16x32_bf16 v[120:123], a[64:67], v[140:143], 0\n"
"buffer_load_dwordx4 a[168:171], v0, s[4:7], s98 offen offset:2048 sc1\n"
"v_mfma_f32_16x16x32_bf16 v[120:123], a[68:71], v[72:75], v[120:123]\n"
"ds_read_b128 v[132:135], v9 offset:128\n"
"ds_read_b128 v[136:139], v5\n"
"v_mfma_f32_16x16x32_bf16 v[120:123], a[72:75], v[76:79], v[120:123]\n"
"v_mfma_f32_16x16x32_bf16 v[120:123], a[76:79], v[156:159], v[120:123]\n"
"ds_read_b128 v[144:147], v4 offset:1024\n"
"ds_read_b128 v[172:175], v9 offset:192\n"
"v_mfma_f32_16x16x32_bf16 v[120:123], a[80:83], v[148:151], v[120:123]\n"
"buffer_load_dwordx4 v[196:199], v33, s[4:7], s97 offen sc1\n"
"v_mfma_f32_16x16x32_bf16 v[120:123], a[84:87], v[116:119], v[120:123]\n"
"ds_write_b128 v3, v[44:47] offset:13312\n"
"v_mfma_f32_16x16x32_bf16 v[120:123], a[88:91], v[124:127], v[120:123]\n"
"ds_read_b128 v[44:47], v5 offset:1024\n"
"s_waitcnt lgkmcnt(4)\n"
"v_mfma_f32_16x16x32_bf16 v[120:123], a[92:95], v[176:179], v[120:123]\n"
"v_fmac_f32_e32 v24, v253, v132\n"
"v_fmac_f32_e32 v25, v253, v133\n"
"v_mfma_f32_16x16x32_bf16 v[200:203], a[124:127], v[176:179], 0\n"
"s_add_u32 s96, s60, 0x8000\n"
"buffer_store_dwordx4 v[64:67], v0, s[4:7], s96 offen sc1\n"
"v_fmac_f32_e32 v26, v253, v134\n"
"v_fmac_f32_e32 v27, v253, v135\n"
"v_mfma_f32_16x16x32_bf16 v[200:203], a[120:123], v[124:127], v[200:203]\n"
"buffer_load_dwordx4 v[64:67], v33, s[4:7], s97 offen offset:1024 sc1\n"
"v_mfma_f32_16x16x32_bf16 v[200:203], a[116:119], v[116:119], v[200:203]\n"
"buffer_load_dwordx4 a[172:175], v0, s[4:7], s98 offen offset:3072 sc1\n"
"v_mfma_f32_16x16x32_bf16 v[200:203], a[112:115], v[148:151], v[200:203]\n"
"ds_read_b128 v[132:135], v4 offset:2048\n"
"ds_read_b128 v[204:207], v9 offset:256\n"
"v_mfma_f32_16x16x32_bf16 v[200:203], a[108:111], v[156:159], v[200:203]\n"
"ds_read_b128 v[208:211], v5 offset:2048\n"
"v_add_f32_e32 v24, v24, v136\n"
"v_mfma_f32_16x16x32_bf16 v[200:203], a[104:107], v[76:79], v[200:203]\n"
"ds_write_b128 v3, v[120:123] offset:14336\n"
"v_mfma_f32_16x16x32_bf16 v[200:203], a[100:103], v[72:75], v[200:203]\n"
"buffer_load_dwordx4 v[120:123], v34, s[4:7], s97 offen sc1\n"
"v_mfma_f32_16x16x32_bf16 v[200:203], a[96:99], v[140:143], v[200:203]\n"
"v_add_f32_e32 v25, v25, v137\n"
"v_add_f32_e32 v26, v26, v138\n"
"v_mfma_f32_16x16x32_bf16 v[212:215], v[28:31], v[140:143], 0\n"
"s_waitcnt lgkmcnt(4)\n"
"v_add_f32_e32 v27, v27, v139\n"
"v_fmac_f32_e32 v144, v253, v172\n"
"v_mfma_f32_16x16x32_bf16 v[212:215], v[52:55], v[72:75], v[212:215]\n"
"ds_read_b128 v[136:139], v4 offset:3072\n"
"ds_read_b128 v[216:219], v9 offset:320\n"
"v_mfma_f32_16x16x32_bf16 v[212:215], v[84:87], v[76:79], v[212:215]\n"
"s_waitcnt vmcnt(22)\n"
"s_add_u32 s96, s68, 0x1000\n"
"ds_read_b128 v[220:223], v5 offset:3072\n"
"v_mfma_f32_16x16x32_bf16 v[212:215], v[92:95], v[156:159], v[212:215]\n"
"buffer_load_dwordx4 v[224:227], v34, s[4:7], s97 offen offset:1024 sc1\n"
"v_mfma_f32_16x16x32_bf16 v[212:215], v[100:103], v[148:151], v[212:215]\n"
"buffer_store_dwordx2 v[18:19], v11, s[4:7], s96 offen offset:64 sc1\n"
"v_mfma_f32_16x16x32_bf16 v[212:215], v[108:111], v[116:119], v[212:215]\n"
"ds_write_b128 v3, v[200:203] offset:15360\n"
"v_mfma_f32_16x16x32_bf16 v[212:215], v[180:183], v[124:127], v[212:215]\n"
"buffer_load_dwordx4 a[176:179], v1, s[4:7], s98 offen sc1\n"
"v_mfma_f32_16x16x32_bf16 v[212:215], v[188:191], v[176:179], v[212:215]\n"
"v_fmac_f32_e32 v145, v253, v173\n"
"v_fmac_f32_e32 v146, v253, v174\n"
"v_mfma_f32_16x16x32_bf16 v[200:203], v[192:195], v[176:179], 0\n"
"buffer_load_dwordx4 v[176:179], v35, s[4:7], s97 offen sc1\n"
"v_mfma_f32_16x16x32_bf16 v[200:203], v[184:187], v[124:127], v[200:203]\n"
"ds_read_b128 v[124:127], v4 offset:4096\n"
"ds_read_b128 v[228:231], v9\n"
"v_mfma_f32_16x16x32_bf16 v[200:203], v[112:115], v[116:119], v[200:203]\n"
"ds_read_b128 v[116:119], v5 offset:4096\n"
"v_fmac_f32_e32 v147, v253, v175\n"
"v_mfma_f32_16x16x32_bf16 v[200:203], v[104:107], v[148:151], v[200:203]\n"
"v_add_f32_e32 v144, v144, v44\n"
"v_add_f32_e32 v145, v145, v45\n"
"v_mfma_f32_16x16x32_bf16 v[200:203], v[96:99], v[156:159], v[200:203]\n"
"s_waitcnt lgkmcnt(8)\n"
"v_add_f32_e32 v146, v146, v46\n"
"v_add_f32_e32 v147, v147, v47\n"
"v_mfma_f32_16x16x32_bf16 v[200:203], v[88:91], v[76:79], v[200:203]\n"
"buffer_load_dwordx4 v[44:47], v35, s[4:7], s97 offen offset:1024 sc1\n"
"v_mfma_f32_16x16x32_bf16 v[200:203], v[56:59], v[72:75], v[200:203]\n"
"ds_write_b128 v3, v[212:215] offset:16384\n"
"v_mfma_f32_16x16x32_bf16 v[200:203], v[48:51], v[140:143], v[200:203]\n"
"s_waitcnt vmcnt(25)\n"
"ds_read_b128 v[72:75], v4 offset:5120\n"
"ds_read_b128 v[76:79], v9 offset:64\n"
"; @A G1 2.3\n"
"v_mfma_f32_16x16x32_bf16 v[24:27], a[0:3], v[80:83], v[24:27]\n"
"buffer_load_dwordx4 a[180:183], v1, s[4:7], s98 offen offset:1024 sc1\n"
"v_mfma_f32_16x16x32_bf16 v[24:27], a[4:7], v[152:155], v[24:27]\n"
"ds_read_b128 v[140:143], v5 offset:5120\n"
"v_fmac_f32_e32 v132, v253, v204\n"
"v_mfma_f32_16x16x32_bf16 v[24:27], a[8:11], v[128:131], v[24:27]\n"
"s_add_u32 s97, s53, 0\n"
"buffer_load_dwordx4 v[148:151], v32, s[4:7], s97 offen sc1\n"
"v_mfma_f32_16x16x32_bf16 v[24:27], a[12:15], v[36:39], v[24:27]\n"
"s_waitcnt vmcnt(20)\n"
"v_fmac_f32_e32 v133, v253, v205\n"
"v_fmac_f32_e32 v134, v253, v206\n"
"v_mfma_f32_16x16x32_bf16 v[24:27], a[16:19], v[40:43], v[24:27]\n"
"v_fmac_f32_e32 v135, v253, v207\n"
"v_add_f32_e32 v132, v132, v208\n"
"v_mfma_f32_16x16x32_bf16 v[24:27], a[20:23], v[160:163], v[24:27]\n"
"ds_write_b128 v3, v[200:203] offset:17408\n"
"v_mfma_f32_16x16x32_bf16 v[24:27], a[24:27], v[164:167], v[24:27]\n"
"v_add_f32_e32 v133, v133, v209\n"
"v_add_f32_e32 v134, v134, v210\n"
"v_mfma_f32_16x16x32_bf16 v[24:27], a[28:31], v[168:171], v[24:27]\n"
"buffer_load_dwordx4 v[156:159], v32, s[4:7], s97 offen offset:1024 sc1\n"
"v_mfma_f32_16x16x32_bf16 v[144:147], a[60:63], v[168:171], v[144:147]\n"
"s_waitcnt lgkmcnt(9)\n"
"v_add_f32_e32 v135, v135, v211\n"
"v_fmac_f32_e32 v136, v253, v216\n"
"v_mfma_f32_16x16x32_bf16 v[144:147], a[56:59], v[164:167], v[144:147]\n"
"v_fmac_f32_e32 v137, v253, v217\n"
"v_fmac_f32_e32 v138, v253, v218\n"
"v_mfma_f32_16x16x32_bf16 v[144:147], a[52:55], v[160:163], v[144:147]\n"
"s_waitcnt lgkmcnt(0)\n"
"s_barrier\n"
"v_mfma_f32_16x16x32_bf16 v[144:147], a[48:51], v[40:43], v[144:147]\n"
"buffer_load_dwordx4 a[184:187], v1, s[4:7], s98 offen offset:2048 sc1\n"
"v_mfma_f32_16x16x32_bf16 v[144:147], a[44:47], v[36:39], v[144:147]\n"
"buffer_load_dwordx4 v[172:175], v33, s[4:7], s97 offen sc1\n"
"v_mfma_f32_16x16x32_bf16 v[144:147], a[40:43], v[128:131], v[144:147]\n"
"ds_read_b128 v[200:203], v6\n"
"ds_read_b128 v[204:207], v6 offset:1024\n"
"v_mfma_f32_16x16x32_bf16 v[144:147], a[36:39], v[152:155], v[144:147]\n"
"v_fmac_f32_e32 v139, v253, v219\n"
"v_add_f32_e32 v136, v136, v220\n"
"v_mfma_f32_16x16x32_bf16 v[144:147], a[32:35], v[80:83], v[144:147]\n"
"v_add_f32_e32 v137, v137, v221\n"
"v_add_f32_e32 v138, v138, v222\n"
"v_mfma_f32_16x16x32_bf16 v[132:135], a[64:67], v[80:83], v[132:135]\n"
"v_add_f32_e32 v139, v139, v223\n"
"v_fmac_f32_e32 v124, v253, v228\n"
"v_mfma_f32_16x16x32_bf16 v[132:135], a[68:71], v[152:155], v[132:135]\n"
"buffer_load_dwordx4 v[208:211], v33, s[4:7], s97 offen offset:1024 sc1\n"
"v_mfma_f32_16x16x32_bf16 v[132:135], a[72:75], v[128:131], v[132:135]\n"
"v_fmac_f32_e32 v125, v253, v229\n"
"v_fmac_f32_e32 v126, v253, v230\n"
"v_mfma_f32_16x16x32_bf16 v[132:135], a[76:79], v[36:39], v[132:135]\n"
"v_fmac_f32_e32 v127, v253, v231\n"
"v_add_f32_e32 v124, v124, v116\n"
"v_mfma_f32_16x16x32_bf16 v[132:135], a[80:83], v[40:43], v[132:135]\n"
"s_waitcnt lgkmcnt(0)\n"
"v_add_f32_e32 v125, v125, v117\n"
"v_add_f32_e32 v126, v126, v118\n"
"v_mfma_f32_16x16x32_bf16 v[132:135], a[84:87], v[160:163], v[132:135]\n"
"buffer_load_dwordx4 a[188:191], v1, s[4:7], s98 offen offset:3072 sc1\n"
"v_mfma_f32_16x16x32_bf16 v[132:135], a[88:91], v[164:167], v[132:135]\n"
"buffer_load_dwordx4 v[212:215], v34, s[4:7], s97 offen sc1\n"
"v_mfma_f32_16x16x32_bf16 v[132:135], a[92:95], v[168:171], v[132:135]\n"
"ds_read_b128 v[216:219], v6 offset:2048\n"
"ds_read_b128 v[220:223], v6 offset:3072\n"
"v_mfma_f32_16x16x32_bf16 v[136:139], a[124:127], v[168:171], v[136:139]\n"
"v_add_f32_e32 v127, v127, v119\n"
"v_fmac_f32_e32 v72, v253, v76\n"
"v_mfma_f32_16x16x32_bf16 v[136:139], a[120:123], v[164:167], v[136:139]\n"
"v_fmac_f32_e32 v73, v253, v77\n"
"v_fmac_f32_e32 v74, v253, v78\n"
"v_mfma_f32_16x16x32_bf16 v[136:139], a[116:119], v[160:163], v[136:139]\n"
"s_waitcnt vmcnt(24)\n"
"s_add_u32 s98, s68, 0x1000\n"
"buffer_store_dwordx2 v[18:19], v11, s[4:7], s98 offen offset:96 sc1\n"
"v_fmac_f32_e32 v75, v253, v79\n"
"v_add_f32_e32 v72, v72, v140\n"
"v_mfma_f32_16x16x32_bf16 v[136:139], a[112:115], v[40:43], v[136:139]\n"
"buffer_load_dwordx4 v[76:79], v34, s[4:7], s97 offen offset:1024 sc1\n"
"v_mfma_f32_16x16x32_bf16 v[136:139], a[108:111], v[36:39], v[136:139]\n"
"v_add_f32_e32 v73, v73, v141\n"
"v_add_f32_e32 v74, v74, v142\n"
"v_mfma_f32_16x16x32_bf16 v[136:139], a[104:107], v[128:131], v[136:139]\n"
"s_add_u32 s98, s70, 0x4000\n"
"v_add_f32_e32 v75, v75, v143\n"
"v_add_f32_e32 v24, v24, v200\n"
"v_mfma_f32_16x16x32_bf16 v[136:139], a[100:103], v[152:155], v[136:139]\n"
"v_add_f32_e32 v25, v25, v201\n"
"v_add_f32_e32 v26, v26, v202\n"
"v_mfma_f32_16x16x32_bf16 v[136:139], a[96:99], v[80:83], v[136:139]\n"
"buffer_load_dwordx4 a[192:195], v0, s[4:7], s98 offen sc1\n"
"v_mfma_f32_16x16x32_bf16 v[124:127], v[28:31], v[80:83], v[124:127]\n"
"buffer_load_dwordx4 v[116:119], v35, s[4:7], s97 offen sc1\n"
"v_mfma_f32_16x16x32_bf16 v[124:127], v[52:55], v[152:155], v[124:127]\n"
"v_add_f32_e32 v27, v27, v203\n"
"v_add_f32_e32 v144, v144, v204\n"
"v_mfma_f32_16x16x32_bf16 v[124:127], v[84:87], v[128:131], v[124:127]\n"
"v_add_f32_e32 v145, v145, v205\n"
"v_add_f32_e32 v146, v146, v206\n"
"v_mfma_f32_16x16x32_bf16 v[124:127], v[92:95], v[36:39], v[124:127]\n"
"v_add_f32_e32 v147, v147, v207\n"
"v_mul_f32_e32 v24, v24, v255\n"
"v_mfma_f32_16x16x32_bf16 v[124:127], v[100:103], v[40:43], v[124:127]\n"
"s_waitcnt lgkmcnt(0)\n"
"v_mul_f32_e32 v25, v25, v255\n"
"v_mul_f32_e32 v26, v26, v255\n"
"v_mfma_f32_16x16x32_bf16 v[124:127], v[108:111], v[160:163], v[124:127]\n"
"buffer_load_dwordx4 v[140:143], v35, s[4:7], s97 offen offset:1024 sc1\n"
"v_mfma_f32_16x16x32_bf16 v[124:127], v[180:183], v[164:167], v[124:127]\n"
"ds_read_b128 v[200:203], v6 offset:4096\n"
"ds_read_b128 v[204:207], v6 offset:5120\n"
"v_mfma_f32_16x16x32_bf16 v[124:127], v[188:191], v[168:171], v[124:127]\n"
"v_mul_f32_e32 v27, v27, v255\n"
"v_mul_f32_e32 v144, v144, v255\n"
"v_mfma_f32_16x16x32_bf16 v[72:75], v[192:195], v[168:171], v[72:75]\n"
"v_mul_f32_e32 v145, v145, v255\n"
"v_mul_f32_e32 v146, v146, v255\n"
"v_mfma_f32_16x16x32_bf16 v[72:75], v[184:187], v[164:167], v[72:75]\n"
"buffer_load_dwordx4 a[196:199], v0, s[4:7], s98 offen offset:1024 sc1\n"
"v_mfma_f32_16x16x32_bf16 v[72:75], v[112:115], v[160:163], v[72:75]\n"
"v_mul_f32_e32 v147, v147, v255\n"
"v_cvt_pk_bf16_f32 v160, v24, v25\n"
"v_mfma_f32_16x16x32_bf16 v[72:75], v[104:107], v[40:43], v[72:75]\n"
"v_cvt_pk_bf16_f32 v161, v26, v27\n"
"v_cvt_pk_bf16_f32 v162, v144, v145\n"
"v_mfma_f32_16x16x32_bf16 v[72:75], v[96:99], v[36:39], v[72:75]\n"
"v_cvt_pk_bf16_f32 v163, v146, v147\n"
"v_add_f32_e32 v132, v132, v216\n"
"v_mfma_f32_16x16x32_bf16 v[72:75], v[88:91], v[128:131], v[72:75]\n"
"ds_write_b128 v8, v[160:163] offset:17408\n"
"v_mfma_f32_16x16x32_bf16 v[72:75], v[56:59], v[152:155], v[72:75]\n"
"v_add_f32_e32 v133, v133, v217\n"
"v_add_f32_e32 v134, v134, v218\n"
"v_mfma_f32_16x16x32_bf16 v[72:75], v[48:51], v[80:83], v[72:75]\n"
"v_add_f32_e32 v135, v135, v219\n"
"v_add_f32_e32 v136, v136, v220\n"
"v_add_f32_e32 v137, v137, v221\n"
"v_add_f32_e32 v138, v138, v222\n"
"v_add_f32_e32 v139, v139, v223\n"
"v_mul_f32_e32 v132, v132, v255\n"
"v_mul_f32_e32 v133, v133, v255\n"
"v_mul_f32_e32 v134, v134, v255\n"
"v_mul_f32_e32 v135, v135, v255\n"
"v_mul_f32_e32 v136, v136, v255\n"
"v_mul_f32_e32 v137, v137, v255\n"
"v_mul_f32_e32 v138, v138, v255\n"
"v_mul_f32_e32 v139, v139, v255\n"
"v_cvt_pk_bf16_f32 v24, v132, v133\n"
"v_cvt_pk_bf16_f32 v25, v134, v135\n"
"v_cvt_pk_bf16_f32 v26, v136, v137\n"
"v_cvt_pk_bf16_f32 v27, v138, v139\n"
"ds_write_b128 v7, v[24:27] offset:50176\n"
".Lmk_z3:\n"
"s_waitcnt vmcnt(21)\n"
"; @A G1 3.0\n"
"v_mfma_f32_16x16x32_bf16 v[36:39], a[0:3], v[60:63], 0\n"
"v_mfma_f32_16x16x32_bf16 v[36:39], a[4:7], v[68:71], v[36:39]\n"
"s_add_u32 s97, s62, 0\n"
"v_mfma_f32_16x16x32_bf16 v[36:39], a[8:11], v[196:199], v[36:39]\n"
"s_add_u32 s96, s54, 0\n"
"buffer_load_dwordx4 v[40:43], v32, s[4:7], s96 offen sc1\n"
"v_mfma_f32_16x16x32_bf16 v[36:39], a[12:15], v[64:67], v[36:39]\n"
"s_waitcnt vmcnt(15)\n"
"v_mfma_f32_16x16x32_bf16 v[36:39], a[16:19], v[120:123], v[36:39]\n"
"buffer_load_dword v255, v10, s[4:7], s97 offen sc1\n"
"v_mfma_f32_16x16x32_bf16 v[36:39], a[20:23], v[224:227], v[36:39]\n"
"s_waitcnt lgkmcnt(2)\n"
"v_add_f32_e32 v124, v124, v200\n"
"v_add_f32_e32 v125, v125, v201\n"
"v_mfma_f32_16x16x32_bf16 v[36:39], a[24:27], v[176:179], v[36:39]\n"
"v_add_f32_e32 v126, v126, v202\n"
"v_add_f32_e32 v127, v127, v203\n"
"v_mfma_f32_16x16x32_bf16 v[36:39], a[28:31], v[44:47], v[36:39]\n"
"buffer_load_dwordx4 v[80:83], v32, s[4:7], s96 offen offset:1024 sc1\n"
"v_mfma_f32_16x16x32_bf16 v[128:131], a[60:63], v[44:47], 0\n"
"buffer_load_dwordx4 a[200:203], v0, s[4:7], s98 offen offset:2048 sc1\n"
"v_mfma_f32_16x16x32_bf16 v[128:131], a[56:59], v[176:179], v[128:131]\n"
"buffer_load_dword v253, v10, s[4:7], s97 offen offset:256 sc1\n"
"v_mfma_f32_16x16x32_bf16 v[128:131], a[52:55], v[224:227], v[128:131]\n"
"v_add_f32_e32 v72, v72, v204\n"
"v_add_f32_e32 v73, v73, v205\n"
"v_mfma_f32_16x16x32_bf16 v[128:131], a[48:51], v[120:123], v[128:131]\n"
"v_add_f32_e32 v74, v74, v206\n"
"v_add_f32_e32 v75, v75, v207\n"
"v_mfma_f32_16x16x32_bf16 v[128:131], a[44:47], v[64:67], v[128:131]\n"
"buffer_load_dwordx4 v[132:135], v33, s[4:7], s96 offen sc1\n"
"v_mfma_f32_16x16x32_bf16 v[128:131], a[40:43], v[196:199], v[128:131]\n"
"ds_write_b128 v3, v[36:39]\n"
"v_mfma_f32_16x16x32_bf16 v[128:131], a[36:39], v[68:71], v[128:131]\n"
"buffer_load_dword v252, v10, s[4:7], s97 offen offset:512 sc1\n"
"v_mfma_f32_16x16x32_bf16 v[128:131], a[32:35], v[60:63], v[128:131]\n"
"v_mul_f32_e32 v124, v124, v254\n"
"v_mul_f32_e32 v125, v125, v254\n"
"v_mfma_f32_16x16x32_bf16 v[36:39], a[64:67], v[60:63], 0\n"
"v_mul_f32_e32 v126, v126, v254\n"
"v_mul_f32_e32 v127, v127, v254\n"
"v_mfma_f32_16x16x32_bf16 v[36:39], a[68:71], v[68:71], v[36:39]\n"
"buffer_load_dwordx4 v[136:139], v33, s[4:7], s96 offen offset:1024 sc1\n"
"v_mfma_f32_16x16x32_bf16 v[36:39], a[72:75], v[196:199], v[36:39]\n"
"buffer_load_dword v251, v10, s[4:7], s97 offen offset:768 sc1\n"
"v_mfma_f32_16x16x32_bf16 v[36:39], a[76:79], v[64:67], v[36:39]\n"
"v_mul_f32_e32 v72, v72, v254\n"
"v_mul_f32_e32 v73, v73, v254\n"
"v_mfma_f32_16x16x32_bf16 v[36:39], a[80:83], v[120:123], v[36:39]\n"
"v_mul_f32_e32 v74, v74, v254\n"
"v_mul_f32_e32 v75, v75, v254\n"
"v_mfma_f32_16x16x32_bf16 v[36:39], a[84:87], v[224:227], v[36:39]\n"
"ds_write_b128 v3, v[128:131] offset:1024\n"
"v_mfma_f32_16x16x32_bf16 v[36:39], a[88:91], v[176:179], v[36:39]\n"
"buffer_load_dwordx4 v[128:131], v34, s[4:7], s96 offen sc1\n"
"v_mfma_f32_16x16x32_bf16 v[36:39], a[92:95], v[44:47], v[36:39]\n"
"buffer_load_dword v254, v10, s[4:7], s97 offen offset:1024 sc1\n"
"v_mfma_f32_16x16x32_bf16 v[144:147], a[124:127], v[44:47], 0\n"
"v_cvt_pk_bf16_f32 v152, v124, v125\n"
"v_cvt_pk_bf16_f32 v153, v126, v127\n"
"v_mfma_f32_16x16x32_bf16 v[144:147], a[120:123], v[176:179], v[144:147]\n"
"v_cvt_pk_bf16_f32 v154, v72, v73\n"
"v_cvt_pk_bf16_f32 v155, v74, v75\n"
"v_mfma_f32_16x16x32_bf16 v[144:147], a[116:119], v[224:227], v[144:147]\n"
"buffer_load_dwordx4 a[204:207], v0, s[4:7], s98 offen offset:3072 sc1\n"
"v_mfma_f32_16x16x32_bf16 v[144:147], a[112:115], v[120:123], v[144:147]\n"
"buffer_load_dwordx4 v[72:75], v34, s[4:7], s96 offen offset:1024 sc1\n"
"v_mfma_f32_16x16x32_bf16 v[144:147], a[108:111], v[64:67], v[144:147]\n"
"buffer_load_dword v250, v10, s[4:7], s97 offen offset:1280 sc1\n"
"v_mfma_f32_16x16x32_bf16 v[144:147], a[104:107], v[196:199], v[144:147]\n"
"ds_write_b128 v3, v[36:39] offset:2048\n"
"v_mfma_f32_16x16x32_bf16 v[144:147], a[100:103], v[68:71], v[144:147]\n"
"ds_write_b128 v2, v[152:155] offset:17408\n"
"v_mfma_f32_16x16x32_bf16 v[144:147], a[96:99], v[60:63], v[144:147]\n"
"buffer_load_dword v249, v10, s[4:7], s97 offen offset:1536 sc1\n"
"v_mfma_f32_16x16x32_bf16 v[36:39], v[28:31], v[60:63], 0\n"
"buffer_load_dwordx4 v[124:127], v35, s[4:7], s96 offen sc1\n"
"v_mfma_f32_16x16x32_bf16 v[36:39], v[52:55], v[68:71], v[36:39]\n"
"buffer_store_dwordx4 v[160:163], v8, s[4:7], s64 offen offset:1024 sc1\n"
"v_mfma_f32_16x16x32_bf16 v[36:39], v[84:87], v[196:199], v[36:39]\n"
"buffer_load_dword v248, v10, s[4:7], s97 offen offset:1792 sc1\n"
"v_mfma_f32_16x16x32_bf16 v[36:39], v[92:95], v[64:67], v[36:39]\n"
"v_mfma_f32_16x16x32_bf16 v[36:39], v[100:103], v[120:123], v[36:39]\n"
"v_mfma_f32_16x16x32_bf16 v[36:39], v[108:111], v[224:227], v[36:39]\n"
"buffer_load_dwordx4 v[152:155], v35, s[4:7], s96 offen offset:1024 sc1\n"
"v_mfma_f32_16x16x32_bf16 v[36:39], v[180:183], v[176:179], v[36:39]\n"
"ds_write_b128 v3, v[144:147] offset:3072\n"
"v_mfma_f32_16x16x32_bf16 v[36:39], v[188:191], v[44:47], v[36:39]\n"
"v_mfma_f32_16x16x32_bf16 v[144:147], v[192:195], v[44:47], 0\n"
"v_mfma_f32_16x16x32_bf16 v[144:147], v[184:187], v[176:179], v[144:147]\n"
"v_mfma_f32_16x16x32_bf16 v[144:147], v[112:115], v[224:227], v[144:147]\n"
"v_mfma_f32_16x16x32_bf16 v[144:147], v[104:107], v[120:123], v[144:147]\n"
"v_mfma_f32_16x16x32_bf16 v[144:147], v[96:99], v[64:67], v[144:147]\n"
"buffer_load_dwordx4 a[208:211], v1, s[4:7], s98 offen sc1\n"
"v_mfma_f32_16x16x32_bf16 v[144:147], v[88:91], v[196:199], v[144:147]\n"
"s_nop 0\n"
"ds_write_b128 v3, v[36:39] offset:4096\n"
"v_mfma_f32_16x16x32_bf16 v[144:147], v[56:59], v[68:71], v[144:147]\n"
"v_mfma_f32_16x16x32_bf16 v[144:147], v[48:51], v[60:63], v[144:147]\n"
"s_waitcnt vmcnt(28)\n"
"; @A G1 3.1\n"
"v_mfma_f32_16x16x32_bf16 v[36:39], a[0:3], v[148:151], 0\n"
"v_mov_b32_e32 v44, -1\n"
"v_mov_b32_e32 v45, -1\n"
"v_mfma_f32_16x16x32_bf16 v[36:39], a[4:7], v[156:159], v[36:39]\n"
"v_mov_b32_e32 v46, -1\n"
"v_mov_b32_e32 v47, -1\n"
"v_mfma_f32_16x16x32_bf16 v[36:39], a[8:11], v[172:175], v[36:39]\n"
"s_add_u32 s97, s55, 0\n"
"buffer_load_dwordx4 v[60:63], v32, s[4:7], s97 offen sc1\n"
"v_mfma_f32_16x16x32_bf16 v[36:39], a[12:15], v[208:211], v[36:39]\n"
"s_waitcnt vmcnt(22)\n"
"s_add_u32 s96, s51, 0x4000\n"
"s_bitcmp1_b32 s26, 0\n"
"s_cbranch_scc1 .Lmk_w9s\n"
"buffer_store_dwordx4 a[64:67], v0, s[4:7], s96 offen sc1\n"
".Lmk_w9r:\n"
"v_mfma_f32_16x16x32_bf16 v[36:39], a[16:19], v[212:215], v[36:39]\n"
"v_mfma_f32_16x16x32_bf16 v[36:39], a[20:23], v[76:79], v[36:39]\n"
"ds_write_b128 v3, v[144:147] offset:5120\n"
"v_mfma_f32_16x16x32_bf16 v[36:39], a[24:27], v[116:119], v[36:39]\n"
"v_mfma_f32_16x16x32_bf16 v[36:39], a[28:31], v[140:143], v[36:39]\n"
"buffer_load_dwordx4 v[64:67], v32, s[4:7], s97 offen offset:1024 sc1\n"
"s_add_u32 s96, s64, 0x4000\n"
"v_mfma_f32_16x16x32_bf16 v[68:71], a[60:63], v[140:143], 0\n"
"v_mfma_f32_16x16x32_bf16 v[68:71], a[56:59], v[116:119], v[68:71]\n"
"buffer_store_dwordx4 v[24:27], v7, s[4:7], s96 offen offset:1024 sc1\n"
"v_mfma_f32_16x16x32_bf16 v[68:71], a[52:55], v[76:79], v[68:71]\n"
"v_mfma_f32_16x16x32_bf16 v[68:71], a[48:51], v[212:215], v[68:71]\n"
"v_mfma_f32_16x16x32_bf16 v[68:71], a[44:47], v[208:211], v[68:71]\n"
"buffer_load_dwordx4 v[24:27], v33, s[4:7], s97 offen sc1\n"
"s_add_u32 s96, s51, 0x6000\n"
"v_mfma_f32_16x16x32_bf16 v[68:71], a[40:43], v[172:175], v[68:71]\n"
"ds_write_b128 v3, v[36:39] offset:6144\n"
"v_mfma_f32_16x16x32_bf16 v[68:71], a[36:39], v[156:159], v[68:71]\n"
"buffer_load_dwordx4 a[212:215], v1, s[4:7], s98 offen offset:1024 sc1\n"
"s_bitcmp1_b32 s26, 0\n"
"s_cbranch_scc1 .Lmk_w10s\n"
"buffer_store_dwordx4 a[96:99], v0, s[4:7], s96 offen sc1\n"
".Lmk_w10r:\n"
"v_mfma_f32_16x16x32_bf16 v[68:71], a[32:35], v[148:151], v[68:71]\n"
"v_mfma_f32_16x16x32_bf16 v[36:39], a[64:67], v[148:151], 0\n"
"v_mfma_f32_16x16x32_bf16 v[36:39], a[68:71], v[156:159], v[36:39]\n"
"buffer_load_dwordx4 v[120:123], v33, s[4:7], s97 offen offset:1024 sc1\n"
"v_mfma_f32_16x16x32_bf16 v[36:39], a[72:75], v[172:175], v[36:39]\n"
"v_mfma_f32_16x16x32_bf16 v[36:39], a[76:79], v[208:211], v[36:39]\n"
"v_mfma_f32_16x16x32_bf16 v[36:39], a[80:83], v[212:215], v[36:39]\n"
"v_mfma_f32_16x16x32_bf16 v[36:39], a[84:87], v[76:79], v[36:39]\n"
"s_nop 0\n"
"ds_write_b128 v3, v[68:71] offset:7168\n"
"v_mfma_f32_16x16x32_bf16 v[36:39], a[88:91], v[116:119], v[36:39]\n"
"buffer_load_dwordx4 v[68:71], v34, s[4:7], s97 offen sc1\n"
"v_mfma_f32_16x16x32_bf16 v[36:39], a[92:95], v[140:143], v[36:39]\n"
"v_mfma_f32_16x16x32_bf16 v[144:147], a[124:127], v[140:143], 0\n"
"s_add_u32 s96, s51, 0x8000\n"
"s_bitcmp1_b32 s26, 0\n"
"s_cbranch_scc1 .Lmk_w11s\n"
"buffer_store_dwordx4 v[28:31], v0, s[4:7], s96 offen sc1\n"
".Lmk_w11r:\n"
"v_mfma_f32_16x16x32_bf16 v[144:147], a[120:123], v[116:119], v[144:147]\n"
"v_mfma_f32_16x16x32_bf16 v[144:147], a[116:119], v[76:79], v[144:147]\n"
"v_mfma_f32_16x16x32_bf16 v[144:147], a[112:115], v[212:215], v[144:147]\n"
"buffer_load_dwordx4 v[160:163], v34, s[4:7], s97 offen offset:1024 sc1\n"
"v_mfma_f32_16x16x32_bf16 v[144:147], a[108:111], v[208:211], v[144:147]\n"
"v_mfma_f32_16x16x32_bf16 v[144:147], a[104:107], v[172:175], v[144:147]\n"
"ds_write_b128 v3, v[36:39] offset:8192\n"
"v_mfma_f32_16x16x32_bf16 v[144:147], a[100:103], v[156:159], v[144:147]\n"
"v_mfma_f32_16x16x32_bf16 v[144:147], a[96:99], v[148:151], v[144:147]\n"
"v_mfma_f32_16x16x32_bf16 v[36:39], v[28:31], v[148:151], 0\n"
"buffer_load_dwordx4 v[164:167], v35, s[4:7], s97 offen sc1\n"
"v_mfma_f32_16x16x32_bf16 v[36:39], v[52:55], v[156:159], v[36:39]\n"
"buffer_load_dwordx4 a[216:219], v1, s[4:7], s98 offen offset:2048 sc1\n"
"v_mfma_f32_16x16x32_bf16 v[36:39], v[84:87], v[172:175], v[36:39]\n"
"v_mfma_f32_16x16x32_bf16 v[36:39], v[92:95], v[208:211], v[36:39]\n"
"v_mfma_f32_16x16x32_bf16 v[36:39], v[100:103], v[212:215], v[36:39]\n"
"s_waitcnt vmcnt(15)\n"
"v_add_f32_e32 v255, v255, v254\n"
"v_add_f32_e32 v253, v253, v250\n"
"s_add_u32 s96, s51, 0xa000\n"
"v_mfma_f32_16x16x32_bf16 v[36:39], v[108:111], v[76:79], v[36:39]\n"
"buffer_load_dwordx4 v[168:171], v35, s[4:7], s97 offen offset:1024 sc1\n"
"s_bitcmp1_b32 s26, 0\n"
"s_cbranch_scc1 .Lmk_w12s\n"
"buffer_store_dwordx4 v[48:51], v0, s[4:7], s96 offen sc1\n"
".Lmk_w12r:\n"
"v_mfma_f32_16x16x32_bf16 v[36:39], v[180:183], v[116:119], v[36:39]\n"
"ds_write_b128 v3, v[144:147] offset:9216\n"
"v_mfma_f32_16x16x32_bf16 v[36:39], v[188:191], v[140:143], v[36:39]\n"
"s_add_u32 s97, s75, 0x1000\n"
"buffer_load_dwordx2 v[246:247], v13, s[4:7], s97 offen sc1\n"
"v_mfma_f32_16x16x32_bf16 v[32:35], v[192:195], v[140:143], 0\n"
"v_add_f32_e32 v252, v252, v249\n"
"v_add_f32_e32 v251, v251, v248\n"
"v_mfma_f32_16x16x32_bf16 v[32:35], v[184:187], v[116:119], v[32:35]\n"
"v_add_f32_e32 v255, v255, v252\n"
"v_add_f32_e32 v253, v253, v251\n"
"v_mfma_f32_16x16x32_bf16 v[32:35], v[112:115], v[76:79], v[32:35]\n"
"s_add_u32 s96, s71, 0\n"
"buffer_load_dwordx2 v[250:251], off, s[4:7], s96 sc1\n"
"v_mfma_f32_16x16x32_bf16 v[32:35], v[104:107], v[212:215], v[32:35]\n"
"v_add_f32_e32 v255, v255, v253\n"
"v_mov_b32_e32 v253, v255\n"
"v_mfma_f32_16x16x32_bf16 v[32:35], v[96:99], v[208:211], v[32:35]\n"
"s_nop 0\n"
"v_permlane32_swap_b32_e32 v255, v253\n"
"v_add_f32_e32 v255, v255, v253\n"
"v_mfma_f32_16x16x32_bf16 v[32:35], v[88:91], v[172:175], v[32:35]\n"
"ds_write_b128 v3, v[36:39] offset:10240\n"
"v_mfma_f32_16x16x32_bf16 v[32:35], v[56:59], v[156:159], v[32:35]\n"
"v_mul_f32_e32 v255, 0x3a800000, v255\n"
"v_mov_b32_e32 v253, v255\n"
"v_mfma_f32_16x16x32_bf16 v[32:35], v[48:51], v[148:151], v[32:35]\n"
"s_nop 0\n"
"v_permlane16_swap_b32_e32 v255, v253\n"
"v_cvt_pk_bf16_f32 v254, -v255, 0\n"
"; @A G1 3.2\n"
"v_mfma_f32_16x16x32_bf16 v[36:39], a[0:3], v[40:43], 0\n"
"s_add_u32 s95, s61, 0x440000\n"
"buffer_store_dwordx4 v[44:47], v0, s[4:7], s95 offen sc1\n"
"v_fma_f32 v253, -v255, v255, v253\n"
"v_lshlrev_b32_e32 v254, 16, v254\n"
"v_mfma_f32_16x16x32_bf16 v[36:39], a[4:7], v[80:83], v[36:39]\n"
"v_max_f32_e32 v253, 0, v253\n"
"v_cvt_pk_bf16_f32 v253, v253, 0\n"
"v_mfma_f32_16x16x32_bf16 v[36:39], a[8:11], v[132:135], v[36:39]\n"
"buffer_load_dwordx4 a[220:223], v1, s[4:7], s98 offen offset:3072 sc1\n"
"v_mfma_f32_16x16x32_bf16 v[36:39], a[12:15], v[136:139], v[36:39]\n"
"s_waitcnt vmcnt(20)\n"
"v_lshlrev_b32_e32 v253, 16, v253\n"
"v_add_f32_e32 v253, 0x3727c5ac, v253\n"
"v_mfma_f32_16x16x32_bf16 v[36:39], a[16:19], v[128:131], v[36:39]\n"
"s_add_u32 s98, s70, 0x6000\n"
"v_cvt_pk_bf16_f32 v253, v253, 0\n"
"v_lshlrev_b32_e32 v253, 16, v253\n"
"v_mfma_f32_16x16x32_bf16 v[36:39], a[20:23], v[72:75], v[36:39]\n"
"ds_write_b128 v3, v[32:35] offset:11264\n"
"v_mfma_f32_16x16x32_bf16 v[36:39], a[24:27], v[124:127], v[36:39]\n"
"buffer_load_dwordx4 a[224:227], v0, s[4:7], s98 offen sc1\n"
"v_mfma_f32_16x16x32_bf16 v[36:39], a[28:31], v[152:155], v[36:39]\n"
"v_sqrt_f32_e32 v253, v253\n"
"v_mfma_f32_16x16x32_bf16 v[32:35], a[60:63], v[152:155], 0\n"
"v_cvt_pk_bf16_f32 v253, v253, 0\n"
"v_lshlrev_b32_e32 v253, 16, v253\n"
"v_mfma_f32_16x16x32_bf16 v[32:35], a[56:59], v[124:127], v[32:35]\n"
"v_rcp_f32_e32 v255, v253\n"
"v_mfma_f32_16x16x32_bf16 v[32:35], a[52:55], v[72:75], v[32:35]\n"
"s_waitcnt lgkmcnt(0)\n"
"s_barrier\n"
"v_mfma_f32_16x16x32_bf16 v[32:35], a[48:51], v[128:131], v[32:35]\n"
"buffer_load_dwordx4 a[228:231], v0, s[4:7], s98 offen offset:1024 sc1\n"
"v_mfma_f32_16x16x32_bf16 v[32:35], a[44:47], v[136:139], v[32:35]\n"
"ds_read_b128 v[76:79], v4\n"
"s_add_u32 s95, s61, 0x440000\n"
"buffer_store_dwordx4 v[44:47], v0, s[4:7], s95 offen offset:1024 sc1\n"
"v_mul_f32_e32 v253, 0x3e8293ee, v255\n"
"v_mfma_f32_16x16x32_bf16 v[32:35], a[40:43], v[132:135], v[32:35]\n"
"ds_write_b128 v3, v[36:39] offset:12288\n"
"v_mfma_f32_16x16x32_bf16 v[32:35], a[36:39], v[80:83], v[32:35]\n"
"ds_read_b128 v[36:39], v9 offset:128\n"
"ds_read_b128 v[116:119], v5\n"
"v_mfma_f32_16x16x32_bf16 v[32:35], a[32:35], v[40:43], v[32:35]\n"
"buffer_load_dwordx4 a[232:235], v0, s[4:7], s98 offen offset:2048 sc1\n"
"v_mfma_f32_16x16x32_bf16 v[140:143], a[64:67], v[40:43], 0\n"
"v_mfma_f32_16x16x32_bf16 v[140:143], a[68:71], v[80:83], v[140:143]\n"
"v_mfma_f32_16x16x32_bf16 v[140:143], a[72:75], v[132:135], v[140:143]\n"
"ds_read_b128 v[144:147], v4 offset:1024\n"
"ds_read_b128 v[148:151], v9 offset:192\n"
"v_mfma_f32_16x16x32_bf16 v[140:143], a[76:79], v[136:139], v[140:143]\n"
"buffer_load_dwordx4 a[236:239], v0, s[4:7], s98 offen offset:3072 sc1\n"
"v_mfma_f32_16x16x32_bf16 v[140:143], a[80:83], v[128:131], v[140:143]\n"
"ds_read_b128 v[156:159], v5 offset:1024\n"
"s_waitcnt lgkmcnt(3)\n"
"v_mfma_f32_16x16x32_bf16 v[140:143], a[84:87], v[72:75], v[140:143]\n"
"ds_write_b128 v3, v[32:35] offset:13312\n"
"v_mfma_f32_16x16x32_bf16 v[140:143], a[88:91], v[124:127], v[140:143]\n"
"v_fmac_f32_e32 v76, v254, v36\n"
"v_fmac_f32_e32 v77, v254, v37\n"
"v_mfma_f32_16x16x32_bf16 v[140:143], a[92:95], v[152:155], v[140:143]\n"
"buffer_load_dwordx4 a[240:243], v1, s[4:7], s98 offen sc1\n"
"v_mfma_f32_16x16x32_bf16 v[32:35], a[124:127], v[152:155], 0\n"
"s_add_u32 s95, s63, 0x44000\n"
"buffer_store_dword v44, v14, s[4:7], s95 offen sc1\n"
"v_fmac_f32_e32 v78, v254, v38\n"
"v_fmac_f32_e32 v79, v254, v39\n"
"v_mfma_f32_16x16x32_bf16 v[32:35], a[120:123], v[124:127], v[32:35]\n"
"ds_read_b128 v[36:39], v4 offset:2048\n"
"ds_read_b128 v[44:47], v9 offset:256\n"
"v_mfma_f32_16x16x32_bf16 v[32:35], a[116:119], v[72:75], v[32:35]\n"
"ds_read_b128 v[172:175], v5 offset:2048\n"
"v_add_f32_e32 v76, v76, v116\n"
"v_mfma_f32_16x16x32_bf16 v[32:35], a[112:115], v[128:131], v[32:35]\n"
"buffer_load_dwordx4 a[244:247], v1, s[4:7], s98 offen offset:1024 sc1\n"
"v_mfma_f32_16x16x32_bf16 v[32:35], a[108:111], v[136:139], v[32:35]\n"
"s_mov_b32 s20, 0xff0000\n"
"v_add_f32_e32 v77, v77, v117\n"
"v_add_f32_e32 v78, v78, v118\n"
"v_mfma_f32_16x16x32_bf16 v[32:35], a[104:107], v[132:135], v[32:35]\n"
"ds_write_b128 v3, v[140:143] offset:14336\n"
"v_mfma_f32_16x16x32_bf16 v[32:35], a[100:103], v[80:83], v[32:35]\n"
"s_waitcnt lgkmcnt(5)\n"
"v_add_f32_e32 v79, v79, v119\n"
"v_fmac_f32_e32 v144, v254, v148\n"
"v_mfma_f32_16x16x32_bf16 v[32:35], a[96:99], v[40:43], v[32:35]\n"
"buffer_load_dwordx4 a[248:251], v1, s[4:7], s98 offen offset:2048 sc1\n"
"v_mfma_f32_16x16x32_bf16 v[116:119], v[28:31], v[40:43], 0\n"
"ds_read_b128 v[140:143], v4 offset:3072\n"
"ds_read_b128 v[176:179], v9 offset:320\n"
"v_mfma_f32_16x16x32_bf16 v[116:119], v[52:55], v[80:83], v[116:119]\n"
"ds_read_b128 v[196:199], v5 offset:3072\n"
"v_fmac_f32_e32 v145, v254, v149\n"
"v_mfma_f32_16x16x32_bf16 v[116:119], v[84:87], v[132:135], v[116:119]\n"
"s_mov_b32 s21, 0xffffffff\n"
"v_fmac_f32_e32 v146, v254, v150\n"
"v_fmac_f32_e32 v147, v254, v151\n"
"v_mfma_f32_16x16x32_bf16 v[116:119], v[92:95], v[136:139], v[116:119]\n"
"buffer_load_dwordx4 a[252:255], v1, s[4:7], s98 offen offset:3072 sc1\n"
"v_mfma_f32_16x16x32_bf16 v[116:119], v[100:103], v[128:131], v[116:119]\n"
"s_waitcnt vmcnt(13)\n"
"v_cmp_ne_u64_e32 vcc, v[246:247], v[18:19]\n"
"v_mfma_f32_16x16x32_bf16 v[116:119], v[108:111], v[72:75], v[116:119]\n"
"ds_write_b128 v3, v[32:35] offset:15360\n"
"v_mfma_f32_16x16x32_bf16 v[116:119], v[180:183], v[124:127], v[116:119]\n"
"s_cbranch_vccnz .Lmk_f6s\n"
".Lmk_f6r:\n"
"v_add_f32_e32 v144, v144, v156\n"
"v_mfma_f32_16x16x32_bf16 v[116:119], v[188:191], v[152:155], v[116:119]\n"
"ds_read_b128 v[32:35], v4 offset:4096\n"
"ds_read_b128 v[148:151], v9\n"
"v_mfma_f32_16x16x32_bf16 v[200:203], v[192:195], v[152:155], 0\n"
"ds_read_b128 v[152:155], v5 offset:4096\n"
"v_add_f32_e32 v145, v145, v157\n"
"v_mfma_f32_16x16x32_bf16 v[200:203], v[184:187], v[124:127], v[200:203]\n"
"s_waitcnt lgkmcnt(8)\n"
"v_add_f32_e32 v146, v146, v158\n"
"v_add_f32_e32 v147, v147, v159\n"
"v_mfma_f32_16x16x32_bf16 v[200:203], v[112:115], v[72:75], v[200:203]\n"
"s_mov_b32 s22, 0x1ff0001\n"
"v_fmac_f32_e32 v36, v254, v44\n"
"v_fmac_f32_e32 v37, v254, v45\n"
"v_mfma_f32_16x16x32_bf16 v[200:203], v[104:107], v[128:131], v[200:203]\n"
"s_mov_b32 s23, 0xffffffff\n"
"v_fmac_f32_e32 v38, v254, v46\n"
"v_fmac_f32_e32 v39, v254, v47\n"
"v_mfma_f32_16x16x32_bf16 v[200:203], v[96:99], v[136:139], v[200:203]\n"
"s_waitcnt vmcnt(12)\n"
"v_cmp_ne_u64_e32 vcc, v[250:251], v[18:19]\n"
"v_mfma_f32_16x16x32_bf16 v[200:203], v[88:91], v[132:135], v[200:203]\n"
"ds_write_b128 v3, v[116:119] offset:16384\n"
"v_mfma_f32_16x16x32_bf16 v[200:203], v[56:59], v[80:83], v[200:203]\n"
"s_cbranch_vccnz .Lmk_f3s\n"
".Lmk_f3r:\n"
"s_add_u32 m0, s77, 0x6000\n"
"v_mfma_f32_16x16x32_bf16 v[200:203], v[48:51], v[40:43], v[200:203]\n"
"s_add_u32 s98, s65, 0x2000\n"
"ds_read_b128 v[40:43], v4 offset:5120\n"
"; @A G1 3.3\n"
"v_mfma_f32_16x16x32_bf16 v[76:79], a[0:3], v[60:63], v[76:79]\n"
"buffer_load_dwordx4 v2, s[4:7], s98 offen sc1 lds\n"
"v_mfma_f32_16x16x32_bf16 v[76:79], a[4:7], v[64:67], v[76:79]\n"
"buffer_load_dwordx4 v2, s[4:7], s98 offen offset:1024 sc1 lds\n"
"v_mfma_f32_16x16x32_bf16 v[76:79], a[8:11], v[24:27], v[76:79]\n"
"ds_read_b128 v[44:47], v9 offset:64\n"
"ds_read_b128 v[72:75], v5 offset:5120\n"
"v_mfma_f32_16x16x32_bf16 v[76:79], a[12:15], v[120:123], v[76:79]\n"
"v_add_f32_e32 v36, v36, v172\n"
"v_add_f32_e32 v37, v37, v173\n"
"v_mfma_f32_16x16x32_bf16 v[76:79], a[16:19], v[68:71], v[76:79]\n"
"s_waitcnt lgkmcnt(8)\n"
"v_add_f32_e32 v38, v38, v174\n"
"v_add_f32_e32 v39, v39, v175\n"
"v_mfma_f32_16x16x32_bf16 v[76:79], a[20:23], v[160:163], v[76:79]\n"
"ds_write_b128 v3, v[200:203] offset:17408\n"
"v_mfma_f32_16x16x32_bf16 v[76:79], a[24:27], v[164:167], v[76:79]\n"
"s_mov_b32 s24, 0x3ff0003\n"
"v_fmac_f32_e32 v140, v254, v176\n"
"v_fmac_f32_e32 v141, v254, v177\n"
"v_mfma_f32_16x16x32_bf16 v[76:79], a[28:31], v[168:171], v[76:79]\n"
"s_add_u32 m0, s77, 0xe000\n"
"s_add_u32 s98, s65, 0x6000\n"
"v_fmac_f32_e32 v142, v254, v178\n"
"v_mfma_f32_16x16x32_bf16 v[144:147], a[60:63], v[168:171], v[144:147]\n"
"buffer_load_dwordx4 v2, s[4:7], s98 offen sc1 lds\n"
"v_mfma_f32_16x16x32_bf16 v[144:147], a[56:59], v[164:167], v[144:147]\n"
"buffer_load_dwordx4 v2, s[4:7], s98 offen offset:1024 sc1 lds\n"
"v_mfma_f32_16x16x32_bf16 v[144:147], a[52:55], v[160:163], v[144:147]\n"
"s_waitcnt lgkmcnt(0)\n"
"s_barrier\n"
"v_mfma_f32_16x16x32_bf16 v[144:147], a[48:51], v[68:71], v[144:147]\n"
"ds_read_b128 v[80:83], v6\n"
"ds_read_b128 v[116:119], v6 offset:1024\n"
"v_mfma_f32_16x16x32_bf16 v[144:147], a[44:47], v[120:123], v[144:147]\n"
"s_add_u32 s98, s69, 0x600000\n"
"buffer_load_dwordx4 a[0:3], v0, s[4:7], s98 offen sc1\n"
"v_mfma_f32_16x16x32_bf16 v[144:147], a[40:43], v[24:27], v[144:147]\n"
"s_mov_b32 s25, 0xffffffff\n"
"v_fmac_f32_e32 v143, v254, v179\n"
"v_add_f32_e32 v140, v140, v196\n"
"v_mfma_f32_16x16x32_bf16 v[144:147], a[36:39], v[64:67], v[144:147]\n"
"s_mov_b32 s26, 0x7ff0007\n"
"v_add_f32_e32 v141, v141, v197\n"
"v_add_f32_e32 v142, v142, v198\n"
"v_mfma_f32_16x16x32_bf16 v[144:147], a[32:35], v[60:63], v[144:147]\n"
"buffer_load_dwordx4 a[4:7], v0, s[4:7], s98 offen offset:1024 sc1\n"
"v_mfma_f32_16x16x32_bf16 v[36:39], a[64:67], v[60:63], v[36:39]\n"
"v_add_f32_e32 v143, v143, v199\n"
"v_fmac_f32_e32 v32, v254, v148\n"
"v_mfma_f32_16x16x32_bf16 v[36:39], a[68:71], v[64:67], v[36:39]\n"
"s_mov_b32 s27, 0xffffffff\n"
"v_fmac_f32_e32 v33, v254, v149\n"
"v_fmac_f32_e32 v34, v254, v150\n"
"v_mfma_f32_16x16x32_bf16 v[36:39], a[72:75], v[24:27], v[36:39]\n"
"buffer_load_dwordx4 a[8:11], v0, s[4:7], s98 offen offset:2048 sc1\n"
"v_mfma_f32_16x16x32_bf16 v[36:39], a[76:79], v[120:123], v[36:39]\n"
"s_mov_b32 s28, 0xfff000f\n"
"v_fmac_f32_e32 v35, v254, v151\n"
"v_add_f32_e32 v32, v32, v152\n"
"v_mfma_f32_16x16x32_bf16 v[36:39], a[80:83], v[68:71], v[36:39]\n"
"s_waitcnt lgkmcnt(0)\n"
"v_add_f32_e32 v33, v33, v153\n"
"v_add_f32_e32 v34, v34, v154\n"
"v_mfma_f32_16x16x32_bf16 v[36:39], a[84:87], v[160:163], v[36:39]\n"
"buffer_load_dwordx4 a[12:15], v0, s[4:7], s98 offen offset:3072 sc1\n"
"v_mfma_f32_16x16x32_bf16 v[36:39], a[88:91], v[164:167], v[36:39]\n"
"ds_read_b128 v[124:127], v6 offset:2048\n"
"ds_read_b128 v[128:131], v6 offset:3072\n"
"v_mfma_f32_16x16x32_bf16 v[36:39], a[92:95], v[168:171], v[36:39]\n"
"v_add_f32_e32 v35, v35, v155\n"
"v_fmac_f32_e32 v40, v254, v44\n"
"v_mfma_f32_16x16x32_bf16 v[140:143], a[124:127], v[168:171], v[140:143]\n"
"buffer_load_dwordx4 a[16:19], v1, s[4:7], s98 offen sc1\n"
"v_mfma_f32_16x16x32_bf16 v[140:143], a[120:123], v[164:167], v[140:143]\n"
"s_mov_b32 s29, 0xffffffff\n"
"v_fmac_f32_e32 v41, v254, v45\n"
"v_fmac_f32_e32 v42, v254, v46\n"
"v_mfma_f32_16x16x32_bf16 v[140:143], a[116:119], v[160:163], v[140:143]\n"
"s_mov_b32 s30, 0x1fff001f\n"
"v_fmac_f32_e32 v43, v254, v47\n"
"v_add_f32_e32 v40, v40, v72\n"
"v_mfma_f32_16x16x32_bf16 v[140:143], a[112:115], v[68:71], v[140:143]\n"
"buffer_load_dwordx4 a[20:23], v1, s[4:7], s98 offen offset:1024 sc1\n"
"v_mfma_f32_16x16x32_bf16 v[140:143], a[108:111], v[120:123], v[140:143]\n"
"s_mov_b32 s31, 0xffffffff\n"
"v_add_f32_e32 v41, v41, v73\n"
"v_add_f32_e32 v42, v42, v74\n"
"v_mfma_f32_16x16x32_bf16 v[140:143], a[104:107], v[24:27], v[140:143]\n"
"s_mov_b32 s32, 0x3fff003f\n"
"v_add_f32_e32 v43, v43, v75\n"
"v_add_f32_e32 v76, v76, v80\n"
"v_mfma_f32_16x16x32_bf16 v[140:143], a[100:103], v[64:67], v[140:143]\n"
"buffer_load_dwordx4 a[24:27], v1, s[4:7], s98 offen offset:2048 sc1\n"
"v_mfma_f32_16x16x32_bf16 v[140:143], a[96:99], v[60:63], v[140:143]\n"
"s_mov_b32 s33, 0xffffffff\n"
"v_add_f32_e32 v77, v77, v81\n"
"v_add_f32_e32 v78, v78, v82\n"
"v_mfma_f32_16x16x32_bf16 v[32:35], v[28:31], v[60:63], v[32:35]\n"
"s_mov_b32 s34, 0x7fff007f\n"
"v_add_f32_e32 v79, v79, v83\n"
"v_add_f32_e32 v144, v144, v116\n"
"v_mfma_f32_16x16x32_bf16 v[32:35], v[52:55], v[64:67], v[32:35]\n"
"buffer_load_dwordx4 a[28:31], v1, s[4:7], s98 offen offset:3072 sc1\n"
"v_mfma_f32_16x16x32_bf16 v[32:35], v[84:87], v[24:27], v[32:35]\n"
"s_mov_b32 s35, 0xffffffff\n"
"v_add_f32_e32 v145, v145, v117\n"
"v_add_f32_e32 v146, v146, v118\n"
"v_mfma_f32_16x16x32_bf16 v[32:35], v[92:95], v[120:123], v[32:35]\n"
"s_mov_b32 s36, 0\n"
"v_add_f32_e32 v147, v147, v119\n"
"v_mul_f32_e32 v76, v76, v255\n"
"v_mfma_f32_16x16x32_bf16 v[32:35], v[100:103], v[68:71], v[32:35]\n"
"s_waitcnt lgkmcnt(0)\n"
"s_waitcnt vmcnt(15)\n"
"buffer_store_dwordx2 v[18:19], v11, s[4:7], s68 offen offset:32 sc1\n"
"v_mul_f32_e32 v77, v77, v255\n"
"v_mul_f32_e32 v78, v78, v255\n"
"v_mfma_f32_16x16x32_bf16 v[32:35], v[108:111], v[160:163], v[32:35]\n"
"s_mov_b32 s37, 0xff0000\n"
"v_mul_f32_e32 v79, v79, v255\n"
"v_mul_f32_e32 v144, v144, v255\n"
"v_mfma_f32_16x16x32_bf16 v[32:35], v[180:183], v[164:167], v[32:35]\n"
"ds_read_b128 v[28:31], v6 offset:4096\n"
"ds_read_b128 v[44:47], v6 offset:5120\n"
"v_mfma_f32_16x16x32_bf16 v[32:35], v[188:191], v[168:171], v[32:35]\n"
"s_mov_b32 s38, 0\n"
"v_mul_f32_e32 v145, v145, v255\n"
"v_mul_f32_e32 v146, v146, v255\n"
"v_mfma_f32_16x16x32_bf16 v[40:43], v[192:195], v[168:171], v[40:43]\n"
"s_mov_b32 s39, 0x1ff0001\n"
"v_mul_f32_e32 v147, v147, v255\n"
"v_cvt_pk_bf16_f32 v52, v76, v77\n"
"v_mfma_f32_16x16x32_bf16 v[40:43], v[184:187], v[164:167], v[40:43]\n"
"s_mov_b32 s40, 0\n"
"v_cvt_pk_bf16_f32 v53, v78, v79\n"
"v_cvt_pk_bf16_f32 v54, v144, v145\n"
"v_mfma_f32_16x16x32_bf16 v[40:43], v[112:115], v[160:163], v[40:43]\n"
"s_mov_b32 s41, 0x3ff0003\n"
"v_cvt_pk_bf16_f32 v55, v146, v147\n"
"v_add_f32_e32 v36, v36, v124\n"
"v_mfma_f32_16x16x32_bf16 v[40:43], v[104:107], v[68:71], v[40:43]\n"
"ds_write_b128 v7, v[52:55] offset:16384\n"
"v_mfma_f32_16x16x32_bf16 v[40:43], v[96:99], v[120:123], v[40:43]\n"
"buffer_store_dwordx4 v[52:55], v7, s[4:7], s64 offen sc1\n"
"v_mfma_f32_16x16x32_bf16 v[40:43], v[88:91], v[24:27], v[40:43]\n"
"s_mov_b32 s42, 0\n"
"v_add_f32_e32 v37, v37, v125\n"
"v_add_f32_e32 v38, v38, v126\n"
"v_mfma_f32_16x16x32_bf16 v[40:43], v[56:59], v[64:67], v[40:43]\n"
"s_mov_b32 s43, 0x7ff0007\n"
"v_add_f32_e32 v39, v39, v127\n"
"v_add_f32_e32 v140, v140, v128\n"
"v_mfma_f32_16x16x32_bf16 v[40:43], v[48:51], v[60:63], v[40:43]\n"
"s_mov_b32 s44, 0\n"
"v_add_f32_e32 v141, v141, v129\n"
"v_add_f32_e32 v142, v142, v130\n"
"v_add_f32_e32 v143, v143, v131\n"
"v_mul_f32_e32 v36, v36, v255\n"
"v_mul_f32_e32 v37, v37, v255\n"
"v_mul_f32_e32 v38, v38, v255\n"
"v_mul_f32_e32 v39, v39, v255\n"
"v_mul_f32_e32 v140, v140, v255\n"
"v_mul_f32_e32 v141, v141, v255\n"
"v_mul_f32_e32 v142, v142, v255\n"
"v_mul_f32_e32 v143, v143, v255\n"
"v_cvt_pk_bf16_f32 v24, v36, v37\n"
"v_cvt_pk_bf16_f32 v25, v38, v39\n"
"v_cvt_pk_bf16_f32 v26, v140, v141\n"
"v_cvt_pk_bf16_f32 v27, v142, v143\n"
"s_mov_b32 s45, 0xfff000f\n"
"s_mov_b32 s46, 0\n"
"s_mov_b32 s47, 0x1fff001f\n"
"s_mov_b32 s48, 0\n"
"s_mov_b32 s49, 0x3fff003f\n"
"s_mov_b32 s50, 0\n"
"s_mov_b32 s51, 0x7fff007f\n"
"ds_write_b128 v7, v[24:27] offset:49152\n"
"v_xor_b32_e32 v255, 32, v16\n"
"v_xor_b32_e32 v254, 32, v15\n"
"v_mov_b32_e32 v88, 0x3f803f80\n"
"v_mov_b32_e32 v89, 0x3f803f80\n"
"v_mov_b32_e32 v90, 0x3f803f80\n"
"v_mov_b32_e32 v91, 0x3f803f80\n"
"v_mov_b32_e32 v252, 0x53800000\n"
".Lmk_z4:\n"
"; @A tail\n"
"s_add_u32 s98, s64, 0x4000\n"
"buffer_store_dwordx4 v[24:27], v7, s[4:7], s98 offen sc1\n"
"s_waitcnt lgkmcnt(2)\n"
"v_add_f32_e32 v32, v32, v28\n"
"v_add_f32_e32 v33, v33, v29\n"
"v_add_f32_e32 v34, v34, v30\n"
"v_add_f32_e32 v35, v35, v31\n"
"v_add_f32_e32 v40, v40, v44\n"
"v_add_f32_e32 v41, v41, v45\n"
"v_add_f32_e32 v42, v42, v46\n"
"v_add_f32_e32 v43, v43, v47\n"
"v_mul_f32_e32 v32, v32, v253\n"
"v_mul_f32_e32 v33, v33, v253\n"
"v_mul_f32_e32 v34, v34, v253\n"
"v_mul_f32_e32 v35, v35, v253\n"
"v_mul_f32_e32 v40, v40, v253\n"
"v_mul_f32_e32 v41, v41, v253\n"
"v_mul_f32_e32 v42, v42, v253\n"
"v_mul_f32_e32 v43, v43, v253\n"
"v_cvt_pk_bf16_f32 v24, v32, v33\n"
"v_cvt_pk_bf16_f32 v25, v34, v35\n"
"v_cvt_pk_bf16_f32 v26, v40, v41\n"
"v_cvt_pk_bf16_f32 v27, v42, v43\n"
"ds_write_b128 v2, v[24:27] offset:16384\n"
"s_waitcnt lgkmcnt(0)\n"
"s_barrier\n"
"ds_read_b128 v[28:31], v2 offset:49152\n"
"ds_read_b128 v[32:35], v2 offset:50176\n"
"ds_read_b128 v[92:95], v2 offset:16384\n"
"ds_read_b128 v[96:99], v2 offset:17408\n"
"; @A AT b\n"
"s_mov_b32 s2, 0\n"
"v_mov_b32_e32 v253, 0xff800000\n"
"v_mov_b32_e32 v251, 0xff800000\n"
"v_mov_b32_e32 v250, 0x7f800000\n"
"v_mov_b32_e32 v249, 0x7f800000\n"
".Lmk_a20:\n"
"ds_read_b128 v[24:27], v15 offset:24576\n"
"ds_read_b128 v[36:39], v254 offset:24832\n"
"ds_read_b128 v[40:43], v15 offset:22528\n"
"ds_read_b128 v[44:47], v254 offset:22784\n"
"s_cmp_gt_i32 s78, 11\n"
"s_cselect_b32 s98, 0x80000000, 0\n"
"s_or_b32 s98, s98, 0x7f800000\n"
"s_waitcnt lgkmcnt(2)\n"
"v_mfma_f32_16x16x32_bf16 v[48:51], v[24:27], v[28:31], 0\n"
"v_mfma_f32_16x16x32_bf16 v[52:55], v[24:27], v[32:35], 0\n"
"v_mfma_f32_16x16x32_bf16 v[56:59], v[36:39], v[32:35], 0\n"
"v_mfma_f32_16x16x32_bf16 v[60:63], v[36:39], v[28:31], 0\n"
"s_waitcnt lgkmcnt(0)\n"
"s_nop 3\n"
"v_max_f32_e32 v248, v48, v49\n"
"v_max_f32_e32 v247, v52, v53\n"
"v_max3_f32 v248, v248, v50, v51\n"
"v_max3_f32 v247, v247, v54, v55\n"
"v_max3_f32 v248, v248, v60, v61\n"
"v_max3_f32 v247, v247, v56, v57\n"
"v_max3_f32 v248, v248, v62, v63\n"
"v_max3_f32 v247, v247, v58, v59\n"
"v_mov_b32_e32 v246, v248\n"
"v_mov_b32_e32 v245, v247\n"
"s_nop 0\n"
"v_permlane16_swap_b32_e32 v248, v246\n"
"v_permlane16_swap_b32_e32 v247, v245\n"
"v_max_f32_e32 v248, v248, v246\n"
"v_max_f32_e32 v247, v247, v245\n"
"v_mov_b32_e32 v246, v248\n"
"v_mov_b32_e32 v245, v247\n"
"s_nop 0\n"
"v_permlane32_swap_b32_e32 v248, v246\n"
"v_permlane32_swap_b32_e32 v247, v245\n"
"v_max_f32_e32 v248, v248, v246\n"
"v_max_f32_e32 v247, v247, v245\n"
"v_med3_f32 v248, v248, v253, v250\n"
"v_med3_f32 v247, v247, v251, v249\n"
"v_min_f32_e64 v24, s98, -v248\n"
"v_min_f32_e64 v25, s98, -v248\n"
"v_min_f32_e64 v26, s98, -v248\n"
"v_min_f32_e64 v27, s98, -v248\n"
"v_min_f32_e64 v36, s98, -v247\n"
"v_min_f32_e64 v37, s98, -v247\n"
"v_min_f32_e64 v38, s98, -v247\n"
"v_min_f32_e64 v39, s98, -v247\n"
"v_mfma_f32_16x16x32_bf16 v[64:67], v[40:43], v[28:31], v[24:27]\n"
"ds_read_b128 v[68:71], v15 offset:20480\n"
"s_add_u32 s98, s69, 0x602000\n"
"buffer_load_dwordx4 a[32:35], v0, s[4:7], s98 offen sc1\n"
"v_sub_f32_e32 v48, v48, v248\n"
"v_sub_f32_e32 v49, v49, v248\n"
"v_sub_f32_e32 v50, v50, v248\n"
"v_mfma_f32_16x16x32_bf16 v[72:75], v[40:43], v[32:35], v[36:39]\n"
"buffer_load_dwordx4 a[36:39], v0, s[4:7], s98 offen offset:1024 sc1\n"
"v_sub_f32_e32 v51, v51, v248\n"
"v_sub_f32_e32 v60, v60, v248\n"
"v_sub_f32_e32 v61, v61, v248\n"
"v_sub_f32_e32 v62, v62, v248\n"
"v_sub_f32_e32 v63, v63, v248\n"
"v_mfma_f32_16x16x32_bf16 v[40:43], v[44:47], v[32:35], v[36:39]\n"
"ds_read_b128 v[76:79], v254 offset:20736\n"
"v_sub_f32_e32 v52, v52, v247\n"
"v_sub_f32_e32 v53, v53, v247\n"
"v_sub_f32_e32 v54, v54, v247\n"
"v_sub_f32_e32 v55, v55, v247\n"
"v_mfma_f32_16x16x32_bf16 v[80:83], v[44:47], v[28:31], v[24:27]\n"
"buffer_load_dwordx4 a[40:43], v0, s[4:7], s98 offen offset:2048 sc1\n"
"v_sub_f32_e32 v56, v56, v247\n"
"v_sub_f32_e32 v57, v57, v247\n"
"v_sub_f32_e32 v58, v58, v247\n"
"v_sub_f32_e32 v59, v59, v247\n"
"s_waitcnt lgkmcnt(0)\n"
"v_mfma_f32_16x16x32_bf16 v[44:47], v[68:71], v[28:31], v[24:27]\n"
"ds_read_b128 v[84:87], v15 offset:18432\n"
"buffer_load_dwordx4 a[44:47], v0, s[4:7], s98 offen offset:3072 sc1\n"
"ds_read_b128 v[100:103], v254 offset:18688\n"
"ds_read_b64_tr_b16 v[104:105], v16 offset:22528\n"
"v_exp_f32_e32 v64, v64\n"
"v_exp_f32_e32 v65, v65\n"
"v_exp_f32_e32 v66, v66\n"
"v_exp_f32_e32 v67, v67\n"
"v_exp_f32_e32 v80, v80\n"
"v_exp_f32_e32 v81, v81\n"
"v_mfma_f32_16x16x32_bf16 v[108:111], v[68:71], v[32:35], v[36:39]\n"
"buffer_load_dwordx4 a[48:51], v1, s[4:7], s98 offen sc1\n"
"ds_read_b64_tr_b16 v[106:107], v255 offset:22784\n"
"buffer_load_dwordx4 a[52:55], v1, s[4:7], s98 offen offset:1024 sc1\n"
"v_exp_f32_e32 v82, v82\n"
"v_exp_f32_e32 v83, v83\n"
"v_cvt_pk_bf16_f32 v68, v64, v65\n"
"v_cvt_pk_bf16_f32 v69, v66, v67\n"
"v_cvt_pk_bf16_f32 v70, v80, v81\n"
"v_cvt_pk_bf16_f32 v71, v82, v83\n"
"v_exp_f32_e32 v72, v72\n"
"v_exp_f32_e32 v73, v73\n"
"v_mfma_f32_16x16x32_bf16 v[64:67], v[76:79], v[32:35], v[36:39]\n"
"ds_read_b64_tr_b16 v[80:81], v255 offset:22528\n"
"s_add_u32 s97, s76, 0x80000\n"
"v_exp_f32_e32 v74, v74\n"
"v_exp_f32_e32 v75, v75\n"
"v_exp_f32_e32 v40, v40\n"
"v_bfe_u32 v248, v0, 2, 7\n"
"v_add_u32_e32 v248, s72, v248\n"
"v_and_b32_e32 v247, 0xffffffc0, v248\n"
"v_add_u32_e32 v248, v248, v247\n"
"v_and_b32_e32 v247, 0x200, v0\n"
"v_lshl_add_u32 v248, v247, 4, v248\n"
"v_mfma_f32_16x16x32_bf16 v[112:115], v[76:79], v[28:31], v[24:27]\n"
"buffer_load_dword v247, v248, s[4:7], s97 offen sc1\n"
"ds_read_b64_tr_b16 v[82:83], v16 offset:22784\n"
"s_add_u32 s96, s68, 0x10000\n"
"buffer_store_dwordx2 v[18:19], v11, s[4:7], s96 offen offset:96 sc1\n"
"buffer_load_dword v246, v248, s[4:7], s97 offen offset:1024 sc1\n"
"v_exp_f32_e32 v41, v41\n"
"v_exp_f32_e32 v42, v42\n"
"v_exp_f32_e32 v43, v43\n"
"v_cvt_pk_bf16_f32 v76, v72, v73\n"
"v_cvt_pk_bf16_f32 v77, v74, v75\n"
"v_cvt_pk_bf16_f32 v78, v40, v41\n"
"v_cvt_pk_bf16_f32 v79, v42, v43\n"
"s_waitcnt lgkmcnt(4)\n"
"v_mfma_f32_16x16x32_bf16 v[40:43], v[84:87], v[28:31], v[24:27]\n"
"ds_read_b128 v[72:75], v15 offset:16384\n"
"v_exp_f32_e32 v44, v44\n"
"v_exp_f32_e32 v45, v45\n"
"v_exp_f32_e32 v46, v46\n"
"v_mfma_f32_16x16x32_bf16 v[116:119], v[84:87], v[32:35], v[36:39]\n"
"buffer_load_dwordx4 a[56:59], v1, s[4:7], s98 offen offset:2048 sc1\n"
"ds_read_b128 v[84:87], v254 offset:16640\n"
"v_exp_f32_e32 v47, v47\n"
"v_exp_f32_e32 v112, v112\n"
"v_mfma_f32_16x16x32_bf16 v[120:123], v[100:103], v[32:35], v[36:39]\n"
"ds_read_b64_tr_b16 v[124:125], v16 offset:20480\n"
"v_exp_f32_e32 v113, v113\n"
"v_exp_f32_e32 v114, v114\n"
"v_mfma_f32_16x16x32_bf16 v[128:131], v[100:103], v[28:31], v[24:27]\n"
"buffer_load_dwordx4 a[60:63], v1, s[4:7], s98 offen offset:3072 sc1\n"
"v_exp_f32_e32 v115, v115\n"
"v_cvt_pk_bf16_f32 v100, v44, v45\n"
"v_cvt_pk_bf16_f32 v101, v46, v47\n"
"s_waitcnt lgkmcnt(3)\n"
"v_mfma_f32_16x16x32_bf16 v[44:47], v[88:91], v[68:71], 0\n"
"ds_read_b64_tr_b16 v[126:127], v255 offset:20736\n"
"s_add_u32 s98, s69, 0x604000\n"
"buffer_load_dwordx4 a[64:67], v0, s[4:7], s98 offen sc1\n"
"v_cvt_pk_bf16_f32 v102, v112, v113\n"
"v_cvt_pk_bf16_f32 v103, v114, v115\n"
"v_exp_f32_e32 v108, v108\n"
"v_mfma_f32_16x16x32_bf16 v[112:115], v[104:107], v[68:71], 0\n"
"ds_read_b64_tr_b16 v[132:133], v255 offset:20480\n"
"v_exp_f32_e32 v109, v109\n"
"v_exp_f32_e32 v110, v110\n"
"v_mfma_f32_16x16x32_bf16 v[136:139], v[80:83], v[68:71], 0\n"
"buffer_load_dword v245, v248, s[4:7], s97 offen offset:2048 sc1\n"
"v_exp_f32_e32 v111, v111\n"
"v_exp_f32_e32 v64, v64\n"
"v_mfma_f32_16x16x32_bf16 v[68:71], v[80:83], v[76:79], 0\n"
"ds_read_b64_tr_b16 v[134:135], v16 offset:20736\n"
"v_exp_f32_e32 v65, v65\n"
"v_exp_f32_e32 v66, v66\n"
"v_mfma_f32_16x16x32_bf16 v[80:83], v[104:107], v[76:79], 0\n"
"buffer_load_dword v244, v248, s[4:7], s97 offen offset:3072 sc1\n"
"s_waitcnt vmcnt(14)\n"
"buffer_store_dwordx2 v[18:19], v11, s[4:7], s68 offen offset:64 sc1\n"
"v_exp_f32_e32 v67, v67\n"
"v_mfma_f32_16x16x32_bf16 v[104:107], v[88:91], v[76:79], 0\n"
"s_cmp_lg_u32 s2, 0\n"
"v_cvt_pk_bf16_f32 v76, v108, v109\n"
"v_cvt_pk_bf16_f32 v77, v110, v111\n"
"v_cvt_pk_bf16_f32 v78, v64, v65\n"
"v_cvt_pk_bf16_f32 v79, v66, v67\n"
"s_cbranch_scc1 .Lmk_s22\n"
"s_barrier\n"
".Lmk_s22:\n"
"s_waitcnt lgkmcnt(4)\n"
"v_mfma_f32_16x16x32_bf16 v[64:67], v[72:75], v[28:31], v[24:27]\n"
"ds_read_b128 v[108:111], v15 offset:14336\n"
"v_exp_f32_e32 v40, v40\n"
"v_exp_f32_e32 v41, v41\n"
"v_exp_f32_e32 v42, v42\n"
"v_mfma_f32_16x16x32_bf16 v[140:143], v[72:75], v[32:35], v[36:39]\n"
"buffer_load_dwordx4 a[68:71], v0, s[4:7], s98 offen offset:1024 sc1\n"
"ds_read_b128 v[72:75], v254 offset:14592\n"
"v_exp_f32_e32 v43, v43\n"
"v_mfma_f32_16x16x32_bf16 v[144:147], v[84:87], v[32:35], v[36:39]\n"
"v_exp_f32_e32 v128, v128\n"
"v_exp_f32_e32 v129, v129\n"
"v_exp_f32_e32 v130, v130\n"
"v_mfma_f32_16x16x32_bf16 v[148:151], v[84:87], v[28:31], v[24:27]\n"
"buffer_load_dwordx4 a[72:75], v0, s[4:7], s98 offen offset:2048 sc1\n"
"ds_read_b64_tr_b16 v[84:85], v16 offset:18432\n"
"v_exp_f32_e32 v131, v131\n"
"s_waitcnt lgkmcnt(3)\n"
"v_mfma_f32_16x16x32_bf16 v[44:47], v[88:91], v[100:103], v[44:47]\n"
"buffer_load_dwordx4 a[76:79], v0, s[4:7], s98 offen offset:3072 sc1\n"
"v_cvt_pk_bf16_f32 v152, v40, v41\n"
"v_cvt_pk_bf16_f32 v153, v42, v43\n"
"v_cvt_pk_bf16_f32 v154, v128, v129\n"
"v_cvt_pk_bf16_f32 v155, v130, v131\n"
"v_mfma_f32_16x16x32_bf16 v[112:115], v[124:127], v[100:103], v[112:115]\n"
"ds_read_b64_tr_b16 v[86:87], v255 offset:18688\n"
"v_exp_f32_e32 v116, v116\n"
"v_exp_f32_e32 v117, v117\n"
"v_exp_f32_e32 v118, v118\n"
"v_mfma_f32_16x16x32_bf16 v[136:139], v[132:135], v[100:103], v[136:139]\n"
"s_add_u32 s97, s76, 0x81000\n"
"buffer_load_dword v243, v248, s[4:7], s97 offen sc1\n"
"v_exp_f32_e32 v119, v119\n"
"v_mfma_f32_16x16x32_bf16 v[68:71], v[132:135], v[76:79], v[68:71]\n"
"ds_read_b64_tr_b16 v[40:41], v255 offset:18432\n"
"v_exp_f32_e32 v120, v120\n"
"v_exp_f32_e32 v121, v121\n"
"v_mfma_f32_16x16x32_bf16 v[80:83], v[124:127], v[76:79], v[80:83]\n"
"buffer_load_dword v242, v248, s[4:7], s97 offen offset:1024 sc1\n"
"ds_read_b64_tr_b16 v[42:43], v16 offset:18688\n"
"v_exp_f32_e32 v122, v122\n"
"v_exp_f32_e32 v123, v123\n"
"v_mfma_f32_16x16x32_bf16 v[104:107], v[88:91], v[76:79], v[104:107]\n"
"s_waitcnt lgkmcnt(4)\n"
"v_cvt_pk_bf16_f32 v76, v116, v117\n"
"v_cvt_pk_bf16_f32 v77, v118, v119\n"
"v_cvt_pk_bf16_f32 v78, v120, v121\n"
"v_cvt_pk_bf16_f32 v79, v122, v123\n"
"s_cmp_eq_i32 s78, 8\n"
"s_cbranch_scc1 .Lmk_x23s\n"
".Lmk_x23r:\n"
"v_mfma_f32_16x16x32_bf16 v[100:103], v[108:111], v[28:31], v[24:27]\n"
"ds_read_b128 v[116:119], v15 offset:12288\n"
"v_exp_f32_e32 v64, v64\n"
"v_exp_f32_e32 v65, v65\n"
"v_exp_f32_e32 v66, v66\n"
"v_mfma_f32_16x16x32_bf16 v[120:123], v[108:111], v[32:35], v[36:39]\n"
"buffer_load_dwordx4 a[80:83], v1, s[4:7], s98 offen sc1\n"
"ds_read_b128 v[108:111], v254 offset:12544\n"
"v_exp_f32_e32 v67, v67\n"
"v_mfma_f32_16x16x32_bf16 v[124:127], v[72:75], v[32:35], v[36:39]\n"
"v_exp_f32_e32 v148, v148\n"
"v_exp_f32_e32 v149, v149\n"
"v_exp_f32_e32 v150, v150\n"
"v_mfma_f32_16x16x32_bf16 v[128:131], v[72:75], v[28:31], v[24:27]\n"
"buffer_load_dwordx4 a[84:87], v1, s[4:7], s98 offen offset:1024 sc1\n"
"ds_read_b64_tr_b16 v[72:73], v16 offset:16384\n"
"v_exp_f32_e32 v151, v151\n"
"s_waitcnt lgkmcnt(3)\n"
"v_mfma_f32_16x16x32_bf16 v[44:47], v[88:91], v[152:155], v[44:47]\n"
"buffer_load_dwordx4 a[88:91], v1, s[4:7], s98 offen offset:2048 sc1\n"
"v_cvt_pk_bf16_f32 v132, v64, v65\n"
"v_cvt_pk_bf16_f32 v133, v66, v67\n"
"v_cvt_pk_bf16_f32 v134, v148, v149\n"
"v_cvt_pk_bf16_f32 v135, v150, v151\n"
"v_mfma_f32_16x16x32_bf16 v[112:115], v[84:87], v[152:155], v[112:115]\n"
"ds_read_b64_tr_b16 v[74:75], v255 offset:16640\n"
"v_exp_f32_e32 v140, v140\n"
"v_exp_f32_e32 v141, v141\n"
"v_mfma_f32_16x16x32_bf16 v[136:139], v[40:43], v[152:155], v[136:139]\n"
"buffer_load_dword v241, v248, s[4:7], s97 offen offset:2048 sc1\n"
"v_exp_f32_e32 v142, v142\n"
"v_exp_f32_e32 v143, v143\n"
"v_mfma_f32_16x16x32_bf16 v[68:71], v[40:43], v[76:79], v[68:71]\n"
"ds_read_b64_tr_b16 v[40:41], v255 offset:16384\n"
"v_exp_f32_e32 v144, v144\n"
"v_exp_f32_e32 v145, v145\n"
"v_mfma_f32_16x16x32_bf16 v[80:83], v[84:87], v[76:79], v[80:83]\n"
"buffer_load_dword v240, v248, s[4:7], s97 offen offset:3072 sc1\n"
"ds_read_b64_tr_b16 v[42:43], v16 offset:16640\n"
"v_exp_f32_e32 v146, v146\n"
"v_exp_f32_e32 v147, v147\n"
"v_mfma_f32_16x16x32_bf16 v[104:107], v[88:91], v[76:79], v[104:107]\n"
"s_waitcnt lgkmcnt(4)\n"
"v_cvt_pk_bf16_f32 v64, v140, v141\n"
"v_cvt_pk_bf16_f32 v65, v142, v143\n"
"v_cvt_pk_bf16_f32 v66, v144, v145\n"
"v_cvt_pk_bf16_f32 v67, v146, v147\n"
"s_cmp_eq_i32 s78, 7\n"
"s_cbranch_scc1 .Lmk_x24s\n"
".Lmk_x24r:\n"
"v_mfma_f32_16x16x32_bf16 v[76:79], v[116:119], v[28:31], v[24:27]\n"
"ds_read_b128 v[84:87], v15 offset:10240\n"
"v_exp_f32_e32 v100, v100\n"
"v_exp_f32_e32 v101, v101\n"
"v_exp_f32_e32 v102, v102\n"
"v_mfma_f32_16x16x32_bf16 v[140:143], v[116:119], v[32:35], v[36:39]\n"
"buffer_load_dwordx4 a[92:95], v1, s[4:7], s98 offen offset:3072 sc1\n"
"v_exp_f32_e32 v103, v103\n"
"v_mfma_f32_16x16x32_bf16 v[116:119], v[108:111], v[32:35], v[36:39]\n"
"ds_read_b128 v[144:147], v254 offset:10496\n"
"v_exp_f32_e32 v128, v128\n"
"v_exp_f32_e32 v129, v129\n"
"v_mfma_f32_16x16x32_bf16 v[148:151], v[108:111], v[28:31], v[24:27]\n"
"ds_read_b64_tr_b16 v[108:109], v16 offset:14336\n"
"v_exp_f32_e32 v130, v130\n"
"v_exp_f32_e32 v131, v131\n"
"v_cvt_pk_bf16_f32 v152, v100, v101\n"
"s_waitcnt lgkmcnt(3)\n"
"v_mfma_f32_16x16x32_bf16 v[44:47], v[88:91], v[132:135], v[44:47]\n"
"s_add_u32 s98, s69, 0x606000\n"
"buffer_load_dwordx4 a[96:99], v0, s[4:7], s98 offen sc1\n"
"v_cvt_pk_bf16_f32 v153, v102, v103\n"
"v_cvt_pk_bf16_f32 v154, v128, v129\n"
"v_mfma_f32_16x16x32_bf16 v[112:115], v[72:75], v[132:135], v[112:115]\n"
"ds_read_b64_tr_b16 v[110:111], v255 offset:14592\n"
"v_cvt_pk_bf16_f32 v155, v130, v131\n"
"v_exp_f32_e32 v120, v120\n"
"v_exp_f32_e32 v121, v121\n"
"v_mfma_f32_16x16x32_bf16 v[136:139], v[40:43], v[132:135], v[136:139]\n"
"ds_read_b64_tr_b16 v[100:101], v255 offset:14336\n"
"v_exp_f32_e32 v122, v122\n"
"v_exp_f32_e32 v123, v123\n"
"v_mfma_f32_16x16x32_bf16 v[68:71], v[40:43], v[64:67], v[68:71]\n"
"buffer_load_dwordx4 a[100:103], v0, s[4:7], s98 offen offset:1024 sc1\n"
"v_exp_f32_e32 v124, v124\n"
"v_exp_f32_e32 v125, v125\n"
"v_mfma_f32_16x16x32_bf16 v[80:83], v[72:75], v[64:67], v[80:83]\n"
"ds_read_b64_tr_b16 v[102:103], v16 offset:14592\n"
"v_exp_f32_e32 v126, v126\n"
"v_exp_f32_e32 v127, v127\n"
"v_mfma_f32_16x16x32_bf16 v[104:107], v[88:91], v[64:67], v[104:107]\n"
"s_waitcnt lgkmcnt(4)\n"
"v_cvt_pk_bf16_f32 v40, v120, v121\n"
"v_cvt_pk_bf16_f32 v41, v122, v123\n"
"v_cvt_pk_bf16_f32 v42, v124, v125\n"
"v_cvt_pk_bf16_f32 v43, v126, v127\n"
"s_cmp_eq_i32 s78, 6\n"
"s_cbranch_scc1 .Lmk_x25s\n"
".Lmk_x25r:\n"
"v_mfma_f32_16x16x32_bf16 v[64:67], v[84:87], v[28:31], v[24:27]\n"
"ds_read_b128 v[72:75], v15 offset:8192\n"
"v_exp_f32_e32 v76, v76\n"
"v_exp_f32_e32 v77, v77\n"
"v_exp_f32_e32 v78, v78\n"
"v_mfma_f32_16x16x32_bf16 v[120:123], v[84:87], v[32:35], v[36:39]\n"
"buffer_load_dwordx4 a[104:107], v0, s[4:7], s98 offen offset:2048 sc1\n"
"v_exp_f32_e32 v79, v79\n"
"v_mfma_f32_16x16x32_bf16 v[84:87], v[144:147], v[32:35], v[36:39]\n"
"ds_read_b128 v[124:127], v254 offset:8448\n"
"v_exp_f32_e32 v148, v148\n"
"v_exp_f32_e32 v149, v149\n"
"v_mfma_f32_16x16x32_bf16 v[128:131], v[144:147], v[28:31], v[24:27]\n"
"ds_read_b64_tr_b16 v[132:133], v16 offset:12288\n"
"v_exp_f32_e32 v150, v150\n"
"v_exp_f32_e32 v151, v151\n"
"s_waitcnt lgkmcnt(3)\n"
"v_mfma_f32_16x16x32_bf16 v[44:47], v[88:91], v[152:155], v[44:47]\n"
"buffer_load_dwordx4 a[108:111], v0, s[4:7], s98 offen offset:3072 sc1\n"
"v_cvt_pk_bf16_f32 v144, v76, v77\n"
"v_cvt_pk_bf16_f32 v145, v78, v79\n"
"v_cvt_pk_bf16_f32 v146, v148, v149\n"
"v_mfma_f32_16x16x32_bf16 v[112:115], v[108:111], v[152:155], v[112:115]\n"
"ds_read_b64_tr_b16 v[134:135], v255 offset:12544\n"
"v_cvt_pk_bf16_f32 v147, v150, v151\n"
"v_exp_f32_e32 v140, v140\n"
"v_exp_f32_e32 v141, v141\n"
"v_mfma_f32_16x16x32_bf16 v[136:139], v[100:103], v[152:155], v[136:139]\n"
"ds_read_b64_tr_b16 v[76:77], v255 offset:12288\n"
"v_exp_f32_e32 v142, v142\n"
"v_exp_f32_e32 v143, v143\n"
"v_mfma_f32_16x16x32_bf16 v[68:71], v[100:103], v[40:43], v[68:71]\n"
"buffer_load_dwordx4 a[112:115], v1, s[4:7], s98 offen sc1\n"
"v_exp_f32_e32 v116, v116\n"
"v_exp_f32_e32 v117, v117\n"
"v_mfma_f32_16x16x32_bf16 v[80:83], v[108:111], v[40:43], v[80:83]\n"
"ds_read_b64_tr_b16 v[78:79], v16 offset:12544\n"
"v_exp_f32_e32 v118, v118\n"
"v_exp_f32_e32 v119, v119\n"
"v_mfma_f32_16x16x32_bf16 v[104:107], v[88:91], v[40:43], v[104:107]\n"
"s_waitcnt lgkmcnt(4)\n"
"v_cvt_pk_bf16_f32 v40, v140, v141\n"
"v_cvt_pk_bf16_f32 v41, v142, v143\n"
"v_cvt_pk_bf16_f32 v42, v116, v117\n"
"v_cvt_pk_bf16_f32 v43, v118, v119\n"
"s_cmp_eq_i32 s78, 5\n"
"s_cbranch_scc1 .Lmk_x26s\n"
".Lmk_x26r:\n"
"v_mfma_f32_16x16x32_bf16 v[100:103], v[72:75], v[28:31], v[24:27]\n"
"ds_read_b64_tr_b16 v[108:109], v16 offset:10240\n"
"ds_read_b64_tr_b16 v[110:111], v255 offset:10496\n"
"v_exp_f32_e32 v64, v64\n"
"v_exp_f32_e32 v65, v65\n"
"v_mfma_f32_16x16x32_bf16 v[116:119], v[72:75], v[32:35], v[36:39]\n"
"s_add_u32 s97, s71, 0xfffff000\n"
"buffer_load_dwordx2 v[238:239], off, s[4:7], s97 sc1\n"
"v_exp_f32_e32 v66, v66\n"
"v_exp_f32_e32 v67, v67\n"
"v_mfma_f32_16x16x32_bf16 v[72:75], v[124:127], v[32:35], v[36:39]\n"
"ds_read_b64_tr_b16 v[36:37], v255 offset:10240\n"
"v_exp_f32_e32 v128, v128\n"
"v_exp_f32_e32 v129, v129\n"
"v_mfma_f32_16x16x32_bf16 v[140:143], v[124:127], v[28:31], v[24:27]\n"
"ds_read_b64_tr_b16 v[38:39], v16 offset:10496\n"
"buffer_load_dwordx4 a[116:119], v1, s[4:7], s98 offen offset:1024 sc1\n"
"v_exp_f32_e32 v130, v130\n"
"v_exp_f32_e32 v131, v131\n"
"s_waitcnt lgkmcnt(4)\n"
"v_mfma_f32_16x16x32_bf16 v[44:47], v[88:91], v[144:147], v[44:47]\n"
"ds_read_b64_tr_b16 v[124:125], v16 offset:24576\n"
"ds_read_b64_tr_b16 v[126:127], v255 offset:24832\n"
"v_cvt_pk_bf16_f32 v148, v64, v65\n"
"v_cvt_pk_bf16_f32 v149, v66, v67\n"
"v_cvt_pk_bf16_f32 v150, v128, v129\n"
"v_mfma_f32_16x16x32_bf16 v[112:115], v[132:135], v[144:147], v[112:115]\n"
"ds_read_b64_tr_b16 v[64:65], v255 offset:24576\n"
"buffer_load_dwordx4 a[120:123], v1, s[4:7], s98 offen offset:2048 sc1\n"
"v_cvt_pk_bf16_f32 v151, v130, v131\n"
"v_exp_f32_e32 v120, v120\n"
"v_mfma_f32_16x16x32_bf16 v[136:139], v[76:79], v[144:147], v[136:139]\n"
"ds_read_b64_tr_b16 v[66:67], v16 offset:24832\n"
"v_exp_f32_e32 v121, v121\n"
"v_exp_f32_e32 v122, v122\n"
"v_exp_f32_e32 v123, v123\n"
"v_mfma_f32_16x16x32_bf16 v[68:71], v[76:79], v[40:43], v[68:71]\n"
"v_exp_f32_e32 v84, v84\n"
"v_add_u32_e32 v248, 0x26400, v0\n"
"v_exp_f32_e32 v85, v85\n"
"v_mfma_f32_16x16x32_bf16 v[80:83], v[132:135], v[40:43], v[80:83]\n"
"buffer_load_dwordx4 a[124:127], v1, s[4:7], s98 offen offset:3072 sc1\n"
"ds_read_b128 v[76:79], v248\n"
"v_exp_f32_e32 v86, v86\n"
"v_exp_f32_e32 v87, v87\n"
"v_mfma_f32_16x16x32_bf16 v[104:107], v[88:91], v[40:43], v[104:107]\n"
"ds_read_b128 v[128:131], v248 offset:1024\n"
"v_cvt_pk_bf16_f32 v40, v120, v121\n"
"v_cvt_pk_bf16_f32 v41, v122, v123\n"
"v_cvt_pk_bf16_f32 v42, v84, v85\n"
"v_cvt_pk_bf16_f32 v43, v86, v87\n"
"s_waitcnt lgkmcnt(6)\n"
"v_mfma_f32_16x16x32_bf16 v[44:47], v[88:91], v[148:151], v[44:47]\n"
"ds_read_b64_tr_b16 v[84:85], v16 offset:8192\n"
"s_add_u32 s98, s69, 0x608000\n"
"buffer_load_dwordx4 v[24:27], v0, s[4:7], s98 offen sc1\n"
"ds_read_b64_tr_b16 v[86:87], v255 offset:8448\n"
"v_cndmask_b32_e64 v100, v48, v100, s[20:21]\n"
"v_cndmask_b32_e64 v101, v49, v101, s[22:23]\n"
"v_cndmask_b32_e64 v102, v50, v102, s[24:25]\n"
"v_cndmask_b32_e64 v103, v51, v103, s[26:27]\n"
"v_cndmask_b32_e64 v140, v60, v140, s[28:29]\n"
"v_cndmask_b32_e64 v141, v61, v141, s[30:31]\n"
"v_cndmask_b32_e64 v142, v62, v142, s[32:33]\n"
"v_mfma_f32_16x16x32_bf16 v[112:115], v[108:111], v[148:151], v[112:115]\n"
"ds_read_b64_tr_b16 v[48:49], v255 offset:8192\n"
"ds_read_b64_tr_b16 v[50:51], v16 offset:8448\n"
"v_cndmask_b32_e64 v143, v63, v143, s[34:35]\n"
"v_exp_f32_e32 v100, v100\n"
"v_exp_f32_e32 v101, v101\n"
"v_exp_f32_e32 v102, v102\n"
"s_waitcnt vmcnt(11)\n"
"v_exp_f32_e32 v103, v103\n"
"v_exp_f32_e32 v140, v140\n"
"v_mfma_f32_16x16x32_bf16 v[136:139], v[36:39], v[148:151], v[136:139]\n"
"v_add_f32_e32 v247, v247, v246\n"
"v_exp_f32_e32 v141, v141\n"
"v_exp_f32_e32 v142, v142\n"
"v_add_f32_e32 v247, v247, v245\n"
"v_exp_f32_e32 v143, v143\n"
"v_cvt_pk_bf16_f32 v60, v100, v101\n"
"v_add_f32_e32 v247, v247, v244\n"
"v_cvt_pk_bf16_f32 v61, v102, v103\n"
"v_cvt_pk_bf16_f32 v62, v140, v141\n"
"v_mfma_f32_16x16x32_bf16 v[68:71], v[36:39], v[40:43], v[68:71]\n"
"v_cvt_pk_bf16_f32 v63, v142, v143\n"
"v_add_f32_e32 v247, v247, v243\n"
"v_cndmask_b32_e64 v116, v52, v116, s[36:37]\n"
"v_cndmask_b32_e64 v117, v53, v117, s[38:39]\n"
"v_cndmask_b32_e64 v118, v54, v118, s[40:41]\n"
"v_cndmask_b32_e64 v119, v55, v119, s[42:43]\n"
"v_add_f32_e32 v247, v247, v242\n"
"v_cndmask_b32_e64 v72, v56, v72, s[44:45]\n"
"v_cndmask_b32_e64 v73, v57, v73, s[46:47]\n"
"v_cndmask_b32_e64 v74, v58, v74, s[48:49]\n"
"v_add_f32_e32 v247, v247, v241\n"
"v_cndmask_b32_e64 v75, v59, v75, s[50:51]\n"
"v_exp_f32_e32 v116, v116\n"
"v_mfma_f32_16x16x32_bf16 v[80:83], v[108:111], v[40:43], v[80:83]\n"
"v_exp_f32_e32 v117, v117\n"
"v_add_f32_e32 v247, v247, v240\n"
"v_exp_f32_e32 v118, v118\n"
"v_mov_b32_e32 v246, v247\n"
"v_exp_f32_e32 v119, v119\n"
"v_exp_f32_e32 v72, v72\n"
"v_permlane32_swap_b32_e32 v247, v246\n"
"v_exp_f32_e32 v73, v73\n"
"v_mfma_f32_16x16x32_bf16 v[104:107], v[88:91], v[40:43], v[104:107]\n"
"v_exp_f32_e32 v74, v74\n"
"v_add_f32_e32 v247, v247, v246\n"
"v_exp_f32_e32 v75, v75\n"
"v_cvt_pk_bf16_f32 v36, v116, v117\n"
"ds_write_b32 v21, v247 offset:512\n"
"v_cvt_pk_bf16_f32 v37, v118, v119\n"
"v_cvt_pk_bf16_f32 v38, v72, v73\n"
"v_cvt_pk_bf16_f32 v39, v74, v75\n"
"s_waitcnt lgkmcnt(1)\n"
"v_mfma_f32_16x16x32_bf16 v[44:47], v[88:91], v[60:63], v[44:47]\n"
"v_and_b32_e32 v76, v60, v76\n"
"v_and_b32_e32 v77, v61, v77\n"
"v_and_b32_e32 v78, v62, v78\n"
"v_and_b32_e32 v79, v63, v79\n"
"v_mfma_f32_16x16x32_bf16 v[104:107], v[88:91], v[36:39], v[104:107]\n"
"v_xor_b32_e32 v40, v60, v76\n"
"v_xor_b32_e32 v41, v61, v77\n"
"v_xor_b32_e32 v42, v62, v78\n"
"v_xor_b32_e32 v43, v63, v79\n"
"v_mfma_f32_16x16x32_bf16 v[112:115], v[84:87], v[76:79], v[112:115]\n"
"v_and_b32_e32 v128, v36, v128\n"
"v_and_b32_e32 v129, v37, v129\n"
"v_mfma_f32_16x16x32_bf16 v[136:139], v[48:51], v[76:79], v[136:139]\n"
"v_and_b32_e32 v130, v38, v130\n"
"v_and_b32_e32 v131, v39, v131\n"
"v_mfma_f32_16x16x32_bf16 v[112:115], v[124:127], v[40:43], v[112:115]\n"
"v_xor_b32_e32 v52, v36, v128\n"
"v_xor_b32_e32 v53, v37, v129\n"
"v_mfma_f32_16x16x32_bf16 v[136:139], v[64:67], v[40:43], v[136:139]\n"
"v_xor_b32_e32 v54, v38, v130\n"
"v_xor_b32_e32 v55, v39, v131\n"
"v_mfma_f32_16x16x32_bf16 v[80:83], v[84:87], v[128:131], v[80:83]\n"
"s_waitcnt vmcnt(4)\n"
"v_cmp_ne_u64_e32 vcc, v[238:239], v[18:19]\n"
"s_nop 0\n"
"s_cbranch_vccnz .Lmk_f19s\n"
".Lmk_f19r:\n"
"v_mfma_f32_16x16x32_bf16 v[68:71], v[48:51], v[128:131], v[68:71]\n"
"s_add_u32 m0, s77, 0x4000\n"
"s_add_u32 s97, s65, 0\n"
"buffer_load_dwordx4 v2, s[4:7], s97 offen sc1 lds\n"
"buffer_load_dwordx4 v2, s[4:7], s97 offen offset:1024 sc1 lds\n"
"v_mfma_f32_16x16x32_bf16 v[80:83], v[124:127], v[52:55], v[80:83]\n"
"s_add_u32 m0, s77, 0xc000\n"
"s_add_u32 s97, s65, 0x4000\n"
"buffer_load_dwordx4 v2, s[4:7], s97 offen sc1 lds\n"
"buffer_load_dwordx4 v2, s[4:7], s97 offen offset:1024 sc1 lds\n"
"v_mfma_f32_16x16x32_bf16 v[68:71], v[64:67], v[52:55], v[68:71]\n"
"v_max_f32_e32 v248, v44, v104\n"
"v_rcp_f32_e32 v44, v44\n"
"v_rcp_f32_e32 v104, v104\n"
"s_nop 0\n"
"v_max3_f32 v248, v248, v44, v104\n"
"v_cmp_nlt_f32_e32 vcc, v248, v252\n"
"s_cbranch_vccnz .Lmk_b21\n"
".Lmk_k27:\n"
"s_add_u32 s97, s60, 0x40000\n"
"v_mul_f32_e32 v112, v112, v44\n"
"v_mul_f32_e32 v113, v113, v44\n"
"v_mul_f32_e32 v114, v114, v44\n"
"v_mul_f32_e32 v115, v115, v44\n"
"v_mul_f32_e32 v136, v136, v44\n"
"v_mul_f32_e32 v137, v137, v44\n"
"v_mul_f32_e32 v138, v138, v44\n"
"v_mul_f32_e32 v139, v139, v44\n"
"v_cvt_pk_bf16_f32 v28, v112, v113\n"
"v_cvt_pk_bf16_f32 v29, v114, v115\n"
"v_cvt_pk_bf16_f32 v30, v136, v137\n"
"v_cvt_pk_bf16_f32 v31, v138, v139\n"
"buffer_store_dwordx4 v[28:31], v0, s[4:7], s97 offen sc1\n"
"s_add_u32 s97, s60, 0x48000\n"
"v_mul_f32_e32 v80, v80, v104\n"
"v_mul_f32_e32 v81, v81, v104\n"
"v_mul_f32_e32 v82, v82, v104\n"
"v_mul_f32_e32 v83, v83, v104\n"
"v_mul_f32_e32 v68, v68, v104\n"
"v_mul_f32_e32 v69, v69, v104\n"
"v_mul_f32_e32 v70, v70, v104\n"
"v_mul_f32_e32 v71, v71, v104\n"
"v_cvt_pk_bf16_f32 v28, v80, v81\n"
"v_cvt_pk_bf16_f32 v29, v82, v83\n"
"v_cvt_pk_bf16_f32 v30, v68, v69\n"
"v_cvt_pk_bf16_f32 v31, v70, v71\n"
"buffer_store_dwordx4 v[28:31], v0, s[4:7], s97 offen sc1\n"
";\n"
"; @A AT a\n"
"s_mov_b32 s2, 0\n"
"v_mov_b32_e32 v253, 0xff800000\n"
"v_mov_b32_e32 v251, 0xff800000\n"
"v_mov_b32_e32 v250, 0x7f800000\n"
"v_mov_b32_e32 v249, 0x7f800000\n"
".Lmk_a28:\n"
"ds_read_b128 v[28:31], v15 offset:16384\n"
"ds_read_b128 v[32:35], v254 offset:16640\n"
"ds_read_b128 v[36:39], v15 offset:14336\n"
"ds_read_b128 v[40:43], v254 offset:14592\n"
"s_cmp_gt_i32 s78, 7\n"
"s_cselect_b32 s97, 0x80000000, 0\n"
"s_or_b32 s97, s97, 0x7f800000\n"
"s_add_u32 s96, s67, 32\n"
"buffer_load_dwordx2 v[246:247], v12, s[4:7], s96 offen sc1\n"
"s_waitcnt lgkmcnt(2)\n"
"v_mfma_f32_16x16x32_bf16 v[100:103], v[28:31], v[92:95], 0\n"
"v_mfma_f32_16x16x32_bf16 v[104:107], v[28:31], v[96:99], 0\n"
"v_mfma_f32_16x16x32_bf16 v[108:111], v[32:35], v[96:99], 0\n"
"v_mfma_f32_16x16x32_bf16 v[112:115], v[32:35], v[92:95], 0\n"
"s_waitcnt lgkmcnt(0)\n"
"s_nop 3\n"
"v_max_f32_e32 v248, v100, v101\n"
"v_max_f32_e32 v245, v104, v105\n"
"v_max3_f32 v248, v248, v102, v103\n"
"v_max3_f32 v245, v245, v106, v107\n"
"v_max3_f32 v248, v248, v112, v113\n"
"v_max3_f32 v245, v245, v108, v109\n"
"v_max3_f32 v248, v248, v114, v115\n"
"v_max3_f32 v245, v245, v110, v111\n"
"v_mov_b32_e32 v244, v248\n"
"v_mov_b32_e32 v243, v245\n"
"s_nop 0\n"
"v_permlane16_swap_b32_e32 v248, v244\n"
"v_permlane16_swap_b32_e32 v245, v243\n"
"v_max_f32_e32 v248, v248, v244\n"
"v_max_f32_e32 v245, v245, v243\n"
"v_mov_b32_e32 v244, v248\n"
"v_mov_b32_e32 v243, v245\n"
"s_nop 0\n"
"v_permlane32_swap_b32_e32 v248, v244\n"
"v_permlane32_swap_b32_e32 v245, v243\n"
"v_max_f32_e32 v248, v248, v244\n"
"v_max_f32_e32 v245, v245, v243\n"
"v_med3_f32 v248, v248, v253, v250\n"
"v_med3_f32 v245, v245, v251, v249\n"
"v_min_f32_e64 v116, s97, -v248\n"
"v_min_f32_e64 v117, s97, -v248\n"
"v_min_f32_e64 v118, s97, -v248\n"
"v_min_f32_e64 v119, s97, -v248\n"
"v_min_f32_e64 v120, s97, -v245\n"
"v_min_f32_e64 v121, s97, -v245\n"
"v_min_f32_e64 v122, s97, -v245\n"
"v_min_f32_e64 v123, s97, -v245\n"
"v_mfma_f32_16x16x32_bf16 v[44:47], v[36:39], v[92:95], v[116:119]\n"
"ds_read_b128 v[32:35], v15 offset:12288\n"
"v_sub_f32_e32 v100, v100, v248\n"
"v_sub_f32_e32 v101, v101, v248\n"
"v_sub_f32_e32 v102, v102, v248\n"
"v_sub_f32_e32 v103, v103, v248\n"
"v_mfma_f32_16x16x32_bf16 v[48:51], v[36:39], v[96:99], v[120:123]\n"
"v_sub_f32_e32 v112, v112, v248\n"
"v_sub_f32_e32 v113, v113, v248\n"
"v_sub_f32_e32 v114, v114, v248\n"
"v_sub_f32_e32 v115, v115, v248\n"
"v_mfma_f32_16x16x32_bf16 v[52:55], v[40:43], v[96:99], v[120:123]\n"
"ds_read_b128 v[56:59], v254 offset:12544\n"
"v_sub_f32_e32 v104, v104, v245\n"
"v_sub_f32_e32 v105, v105, v245\n"
"v_sub_f32_e32 v106, v106, v245\n"
"v_sub_f32_e32 v107, v107, v245\n"
"v_mfma_f32_16x16x32_bf16 v[36:39], v[40:43], v[92:95], v[116:119]\n"
"s_waitcnt lgkmcnt(0)\n"
"v_sub_f32_e32 v108, v108, v245\n"
"v_sub_f32_e32 v109, v109, v245\n"
"v_sub_f32_e32 v110, v110, v245\n"
"v_sub_f32_e32 v111, v111, v245\n"
"s_cmp_eq_i32 s78, 7\n"
"s_cbranch_scc1 .Lmk_x30s\n"
".Lmk_x30r:\n"
"v_mfma_f32_16x16x32_bf16 v[60:63], v[32:35], v[92:95], v[116:119]\n"
"ds_read_b128 v[40:43], v15 offset:10240\n"
"buffer_load_dwordx4 v[28:31], v0, s[4:7], s98 offen offset:1024 sc1\n"
"ds_read_b128 v[64:67], v254 offset:10496\n"
"v_exp_f32_e32 v44, v44\n"
"v_exp_f32_e32 v45, v45\n"
"v_exp_f32_e32 v46, v46\n"
"v_exp_f32_e32 v47, v47\n"
"v_exp_f32_e32 v36, v36\n"
"v_mfma_f32_16x16x32_bf16 v[68:71], v[32:35], v[96:99], v[120:123]\n"
"ds_read_b64_tr_b16 v[72:73], v16 offset:14336\n"
"buffer_load_dwordx4 v[32:35], v0, s[4:7], s98 offen offset:2048 sc1\n"
"v_exp_f32_e32 v37, v37\n"
"v_exp_f32_e32 v38, v38\n"
"v_exp_f32_e32 v39, v39\n"
"v_cvt_pk_bf16_f32 v76, v44, v45\n"
"v_cvt_pk_bf16_f32 v77, v46, v47\n"
"v_cvt_pk_bf16_f32 v78, v36, v37\n"
"v_mfma_f32_16x16x32_bf16 v[80:83], v[56:59], v[96:99], v[120:123]\n"
"ds_read_b64_tr_b16 v[74:75], v255 offset:14592\n"
"ds_read_b64_tr_b16 v[84:85], v255 offset:14336\n"
"v_cvt_pk_bf16_f32 v79, v38, v39\n"
"v_exp_f32_e32 v48, v48\n"
"v_exp_f32_e32 v49, v49\n"
"v_exp_f32_e32 v50, v50\n"
"v_exp_f32_e32 v51, v51\n"
"v_mfma_f32_16x16x32_bf16 v[124:127], v[56:59], v[92:95], v[116:119]\n"
"buffer_load_dwordx4 v[36:39], v0, s[4:7], s98 offen offset:3072 sc1\n"
"ds_read_b64_tr_b16 v[86:87], v16 offset:14592\n"
"v_exp_f32_e32 v52, v52\n"
"v_exp_f32_e32 v53, v53\n"
"v_exp_f32_e32 v54, v54\n"
"v_exp_f32_e32 v55, v55\n"
"v_cvt_pk_bf16_f32 v56, v48, v49\n"
"v_cvt_pk_bf16_f32 v57, v50, v51\n"
"v_cvt_pk_bf16_f32 v58, v52, v53\n"
"v_cvt_pk_bf16_f32 v59, v54, v55\n"
"s_waitcnt lgkmcnt(4)\n"
"s_cmp_eq_i32 s78, 6\n"
"s_cbranch_scc1 .Lmk_x31s\n"
".Lmk_x31r:\n"
"v_mfma_f32_16x16x32_bf16 v[128:131], v[40:43], v[92:95], v[116:119]\n"
"ds_read_b128 v[52:55], v15 offset:8192\n"
"v_exp_f32_e32 v60, v60\n"
"v_exp_f32_e32 v61, v61\n"
"v_exp_f32_e32 v62, v62\n"
"v_mfma_f32_16x16x32_bf16 v[132:135], v[40:43], v[96:99], v[120:123]\n"
"buffer_load_dwordx4 v[40:43], v1, s[4:7], s98 offen sc1\n"
"v_exp_f32_e32 v63, v63\n"
"v_mfma_f32_16x16x32_bf16 v[136:139], v[64:67], v[96:99], v[120:123]\n"
"ds_read_b128 v[140:143], v254 offset:8448\n"
"v_exp_f32_e32 v124, v124\n"
"v_exp_f32_e32 v125, v125\n"
"v_mfma_f32_16x16x32_bf16 v[144:147], v[64:67], v[92:95], v[116:119]\n"
"ds_read_b64_tr_b16 v[64:65], v16 offset:12288\n"
"v_exp_f32_e32 v126, v126\n"
"v_exp_f32_e32 v127, v127\n"
"s_waitcnt lgkmcnt(3)\n"
"v_mfma_f32_16x16x32_bf16 v[148:151], v[88:91], v[76:79], 0\n"
"buffer_load_dwordx4 v[44:47], v1, s[4:7], s98 offen offset:1024 sc1\n"
"v_cvt_pk_bf16_f32 v152, v60, v61\n"
"v_cvt_pk_bf16_f32 v153, v62, v63\n"
"v_cvt_pk_bf16_f32 v154, v124, v125\n"
"v_mfma_f32_16x16x32_bf16 v[156:159], v[72:75], v[76:79], 0\n"
"ds_read_b64_tr_b16 v[66:67], v255 offset:12544\n"
"v_cvt_pk_bf16_f32 v155, v126, v127\n"
"v_exp_f32_e32 v68, v68\n"
"v_exp_f32_e32 v69, v69\n"
"v_mfma_f32_16x16x32_bf16 v[124:127], v[84:87], v[76:79], 0\n"
"ds_read_b64_tr_b16 v[60:61], v255 offset:12288\n"
"v_exp_f32_e32 v70, v70\n"
"v_exp_f32_e32 v71, v71\n"
"v_mfma_f32_16x16x32_bf16 v[160:163], v[84:87], v[56:59], 0\n"
"buffer_load_dwordx4 v[48:51], v1, s[4:7], s98 offen offset:2048 sc1\n"
"v_exp_f32_e32 v80, v80\n"
"v_exp_f32_e32 v81, v81\n"
"v_mfma_f32_16x16x32_bf16 v[164:167], v[72:75], v[56:59], 0\n"
"ds_read_b64_tr_b16 v[62:63], v16 offset:12544\n"
"v_exp_f32_e32 v82, v82\n"
"v_exp_f32_e32 v83, v83\n"
"v_mfma_f32_16x16x32_bf16 v[168:171], v[88:91], v[56:59], 0\n"
"s_waitcnt lgkmcnt(4)\n"
"v_cvt_pk_bf16_f32 v72, v68, v69\n"
"v_cvt_pk_bf16_f32 v73, v70, v71\n"
"v_cvt_pk_bf16_f32 v74, v80, v81\n"
"v_cvt_pk_bf16_f32 v75, v82, v83\n"
"s_cmp_eq_i32 s78, 5\n"
"s_cbranch_scc1 .Lmk_x32s\n"
".Lmk_x32r:\n"
"v_mfma_f32_16x16x32_bf16 v[76:79], v[52:55], v[92:95], v[116:119]\n"
"ds_read_b64_tr_b16 v[68:69], v16 offset:10240\n"
"v_exp_f32_e32 v128, v128\n"
"v_exp_f32_e32 v129, v129\n"
"v_exp_f32_e32 v130, v130\n"
"v_mfma_f32_16x16x32_bf16 v[80:83], v[52:55], v[96:99], v[120:123]\n"
"buffer_load_dwordx4 v[52:55], v1, s[4:7], s98 offen offset:3072 sc1\n"
"v_exp_f32_e32 v131, v131\n"
"v_mfma_f32_16x16x32_bf16 v[84:87], v[140:143], v[96:99], v[120:123]\n"
"ds_read_b64_tr_b16 v[70:71], v255 offset:10496\n"
"v_exp_f32_e32 v144, v144\n"
"v_exp_f32_e32 v145, v145\n"
"v_mfma_f32_16x16x32_bf16 v[172:175], v[140:143], v[92:95], v[116:119]\n"
"s_waitcnt lgkmcnt(2)\n"
"v_exp_f32_e32 v146, v146\n"
"v_exp_f32_e32 v147, v147\n"
"v_cvt_pk_bf16_f32 v140, v128, v129\n"
"v_mfma_f32_16x16x32_bf16 v[148:151], v[88:91], v[152:155], v[148:151]\n"
"s_add_u32 s97, s69, 0x60a000\n"
"buffer_load_dwordx4 v[56:59], v0, s[4:7], s97 offen sc1\n"
"v_cvt_pk_bf16_f32 v141, v130, v131\n"
"v_cvt_pk_bf16_f32 v142, v144, v145\n"
"v_mfma_f32_16x16x32_bf16 v[156:159], v[64:67], v[152:155], v[156:159]\n"
"ds_read_b64_tr_b16 v[128:129], v255 offset:10240\n"
"v_cvt_pk_bf16_f32 v143, v146, v147\n"
"v_exp_f32_e32 v132, v132\n"
"v_exp_f32_e32 v133, v133\n"
"v_mfma_f32_16x16x32_bf16 v[124:127], v[60:63], v[152:155], v[124:127]\n"
"v_exp_f32_e32 v134, v134\n"
"v_exp_f32_e32 v135, v135\n"
"v_mfma_f32_16x16x32_bf16 v[160:163], v[60:63], v[72:75], v[160:163]\n"
"buffer_load_dwordx4 v[60:63], v0, s[4:7], s97 offen offset:1024 sc1\n"
"ds_read_b64_tr_b16 v[130:131], v16 offset:10496\n"
"v_exp_f32_e32 v136, v136\n"
"v_mfma_f32_16x16x32_bf16 v[164:167], v[64:67], v[72:75], v[164:167]\n"
"v_exp_f32_e32 v137, v137\n"
"v_exp_f32_e32 v138, v138\n"
"v_exp_f32_e32 v139, v139\n"
"v_mfma_f32_16x16x32_bf16 v[168:171], v[88:91], v[72:75], v[168:171]\n"
"s_waitcnt vmcnt(12)\n"
"v_cvt_pk_bf16_f32 v72, v132, v133\n"
"v_cvt_pk_bf16_f32 v73, v134, v135\n"
"v_cvt_pk_bf16_f32 v74, v136, v137\n"
"v_cvt_pk_bf16_f32 v75, v138, v139\n"
"s_cmp_lg_u32 s2, 0\n"
"s_cbranch_scc1 .Lmk_s33\n"
"s_barrier\n"
".Lmk_s33:\n"
"ds_read_b128 v[132:135], v15 offset:6144\n"
"ds_read_b128 v[136:139], v254 offset:6400\n"
"s_waitcnt lgkmcnt(2)\n"
"v_mfma_f32_16x16x32_bf16 v[148:151], v[88:91], v[140:143], v[148:151]\n"
"ds_read_b128 v[144:147], v15 offset:4096\n"
"v_exp_f32_e32 v76, v76\n"
"v_exp_f32_e32 v77, v77\n"
"v_exp_f32_e32 v78, v78\n"
"v_mfma_f32_16x16x32_bf16 v[156:159], v[68:71], v[140:143], v[156:159]\n"
"buffer_load_dwordx4 v[64:67], v0, s[4:7], s97 offen offset:2048 sc1\n"
"ds_read_b128 v[152:155], v254 offset:4352\n"
"v_exp_f32_e32 v79, v79\n"
"v_mfma_f32_16x16x32_bf16 v[124:127], v[128:131], v[140:143], v[124:127]\n"
"ds_read_b64_tr_b16 v[140:141], v16 offset:8192\n"
"v_exp_f32_e32 v172, v172\n"
"v_exp_f32_e32 v173, v173\n"
"v_mfma_f32_16x16x32_bf16 v[160:163], v[128:131], v[72:75], v[160:163]\n"
"ds_read_b64_tr_b16 v[142:143], v255 offset:8448\n"
"v_exp_f32_e32 v174, v174\n"
"v_exp_f32_e32 v175, v175\n"
"v_mfma_f32_16x16x32_bf16 v[164:167], v[68:71], v[72:75], v[164:167]\n"
"buffer_load_dwordx4 v[68:71], v0, s[4:7], s97 offen offset:3072 sc1\n"
"v_cvt_pk_bf16_f32 v128, v76, v77\n"
"v_cvt_pk_bf16_f32 v129, v78, v79\n"
"v_cvt_pk_bf16_f32 v130, v172, v173\n"
"v_cvt_pk_bf16_f32 v131, v174, v175\n"
"v_mfma_f32_16x16x32_bf16 v[168:171], v[88:91], v[72:75], v[168:171]\n"
"ds_read_b64_tr_b16 v[172:173], v255 offset:8192\n"
"v_exp_f32_e32 v80, v80\n"
"v_exp_f32_e32 v81, v81\n"
"s_waitcnt lgkmcnt(5)\n"
"v_mfma_f32_16x16x32_bf16 v[176:179], v[132:135], v[92:95], v[116:119]\n"
"ds_read_b64_tr_b16 v[174:175], v16 offset:8448\n"
"v_exp_f32_e32 v82, v82\n"
"v_exp_f32_e32 v83, v83\n"
"v_exp_f32_e32 v84, v84\n"
"v_mfma_f32_16x16x32_bf16 v[180:183], v[132:135], v[96:99], v[120:123]\n"
"buffer_load_dwordx4 v[72:75], v1, s[4:7], s97 offen sc1\n"
"s_waitcnt vmcnt(12)\n"
"v_cmp_ne_u64_e32 vcc, v[246:247], v[18:19]\n"
"v_mfma_f32_16x16x32_bf16 v[132:135], v[136:139], v[96:99], v[120:123]\n"
"v_exp_f32_e32 v85, v85\n"
"v_exp_f32_e32 v86, v86\n"
"v_exp_f32_e32 v87, v87\n"
"v_mfma_f32_16x16x32_bf16 v[184:187], v[136:139], v[92:95], v[116:119]\n"
"s_cbranch_vccnz .Lmk_f4s\n"
".Lmk_f4r:\n"
"v_cvt_pk_bf16_f32 v136, v80, v81\n"
"v_cvt_pk_bf16_f32 v137, v82, v83\n"
"v_cvt_pk_bf16_f32 v138, v84, v85\n"
"v_cvt_pk_bf16_f32 v139, v86, v87\n"
"s_waitcnt lgkmcnt(4)\n"
"v_mfma_f32_16x16x32_bf16 v[188:191], v[144:147], v[92:95], v[116:119]\n"
"ds_read_b128 v[192:195], v15 offset:2048\n"
"v_exp_f32_e32 v176, v176\n"
"v_exp_f32_e32 v177, v177\n"
"v_exp_f32_e32 v178, v178\n"
"v_mfma_f32_16x16x32_bf16 v[196:199], v[144:147], v[96:99], v[120:123]\n"
"buffer_load_dwordx4 v[76:79], v1, s[4:7], s97 offen offset:1024 sc1\n"
"v_exp_f32_e32 v179, v179\n"
"v_mfma_f32_16x16x32_bf16 v[144:147], v[152:155], v[96:99], v[120:123]\n"
"ds_read_b128 v[200:203], v254 offset:2304\n"
"v_exp_f32_e32 v184, v184\n"
"v_exp_f32_e32 v185, v185\n"
"v_mfma_f32_16x16x32_bf16 v[204:207], v[152:155], v[92:95], v[116:119]\n"
"ds_read_b64_tr_b16 v[152:153], v16 offset:6144\n"
"v_exp_f32_e32 v186, v186\n"
"v_exp_f32_e32 v187, v187\n"
"s_waitcnt lgkmcnt(3)\n"
"v_mfma_f32_16x16x32_bf16 v[148:151], v[88:91], v[128:131], v[148:151]\n"
"buffer_load_dwordx4 v[80:83], v1, s[4:7], s97 offen offset:2048 sc1\n"
"v_cvt_pk_bf16_f32 v208, v176, v177\n"
"v_cvt_pk_bf16_f32 v209, v178, v179\n"
"v_cvt_pk_bf16_f32 v210, v184, v185\n"
"v_mfma_f32_16x16x32_bf16 v[156:159], v[140:143], v[128:131], v[156:159]\n"
"ds_read_b64_tr_b16 v[154:155], v255 offset:6400\n"
"v_cvt_pk_bf16_f32 v211, v186, v187\n"
"v_exp_f32_e32 v180, v180\n"
"v_exp_f32_e32 v181, v181\n"
"v_mfma_f32_16x16x32_bf16 v[124:127], v[172:175], v[128:131], v[124:127]\n"
"ds_read_b64_tr_b16 v[128:129], v255 offset:6144\n"
"v_exp_f32_e32 v182, v182\n"
"v_exp_f32_e32 v183, v183\n"
"v_mfma_f32_16x16x32_bf16 v[160:163], v[172:175], v[136:139], v[160:163]\n"
"buffer_load_dwordx4 v[84:87], v1, s[4:7], s97 offen offset:3072 sc1\n"
"v_exp_f32_e32 v132, v132\n"
"v_exp_f32_e32 v133, v133\n"
"v_mfma_f32_16x16x32_bf16 v[164:167], v[140:143], v[136:139], v[164:167]\n"
"ds_read_b64_tr_b16 v[130:131], v16 offset:6400\n"
"v_exp_f32_e32 v134, v134\n"
"v_exp_f32_e32 v135, v135\n"
"v_mfma_f32_16x16x32_bf16 v[168:171], v[88:91], v[136:139], v[168:171]\n"
"s_waitcnt lgkmcnt(4)\n"
"v_cvt_pk_bf16_f32 v136, v180, v181\n"
"v_cvt_pk_bf16_f32 v137, v182, v183\n"
"v_cvt_pk_bf16_f32 v138, v132, v133\n"
"v_cvt_pk_bf16_f32 v139, v134, v135\n"
"v_mfma_f32_16x16x32_bf16 v[132:135], v[192:195], v[92:95], v[116:119]\n"
"ds_read_b128 v[140:143], v15\n"
"v_exp_f32_e32 v188, v188\n"
"v_exp_f32_e32 v189, v189\n"
"v_exp_f32_e32 v190, v190\n"
"v_mfma_f32_16x16x32_bf16 v[172:175], v[192:195], v[96:99], v[120:123]\n"
"s_add_u32 s97, s56, 0x40000\n"
"buffer_load_dwordx4 v[176:179], v0, s[4:7], s97 offen sc1\n"
"v_exp_f32_e32 v191, v191\n"
"v_mfma_f32_16x16x32_bf16 v[180:183], v[200:203], v[96:99], v[120:123]\n"
"ds_read_b128 v[184:187], v254 offset:256\n"
"v_exp_f32_e32 v204, v204\n"
"v_exp_f32_e32 v205, v205\n"
"v_exp_f32_e32 v206, v206\n"
"v_mfma_f32_16x16x32_bf16 v[192:195], v[200:203], v[92:95], v[116:119]\n"
"buffer_load_dwordx4 v[200:203], v0, s[4:7], s97 offen offset:1024 sc1\n"
"ds_read_b64_tr_b16 v[212:213], v16 offset:4096\n"
"v_exp_f32_e32 v207, v207\n"
"s_waitcnt lgkmcnt(3)\n"
"v_mfma_f32_16x16x32_bf16 v[148:151], v[88:91], v[208:211], v[148:151]\n"
"v_cvt_pk_bf16_f32 v216, v188, v189\n"
"v_cvt_pk_bf16_f32 v217, v190, v191\n"
"v_cvt_pk_bf16_f32 v218, v204, v205\n"
"v_cvt_pk_bf16_f32 v219, v206, v207\n"
"v_mfma_f32_16x16x32_bf16 v[156:159], v[152:155], v[208:211], v[156:159]\n"
"buffer_load_dwordx4 v[188:191], v0, s[4:7], s97 offen offset:2048 sc1\n"
"ds_read_b64_tr_b16 v[214:215], v255 offset:4352\n"
"v_exp_f32_e32 v196, v196\n"
"v_exp_f32_e32 v197, v197\n"
"v_mfma_f32_16x16x32_bf16 v[124:127], v[128:131], v[208:211], v[124:127]\n"
"buffer_load_dwordx4 v[204:207], v0, s[4:7], s97 offen offset:3072 sc1\n"
"v_exp_f32_e32 v198, v198\n"
"v_exp_f32_e32 v199, v199\n"
"v_mfma_f32_16x16x32_bf16 v[160:163], v[128:131], v[136:139], v[160:163]\n"
"ds_read_b64_tr_b16 v[128:129], v255 offset:4096\n"
"v_exp_f32_e32 v144, v144\n"
"v_exp_f32_e32 v145, v145\n"
"v_mfma_f32_16x16x32_bf16 v[164:167], v[152:155], v[136:139], v[164:167]\n"
"buffer_load_dwordx4 v[152:155], v1, s[4:7], s97 offen sc1\n"
"ds_read_b64_tr_b16 v[130:131], v16 offset:4352\n"
"v_exp_f32_e32 v146, v146\n"
"v_exp_f32_e32 v147, v147\n"
"v_mfma_f32_16x16x32_bf16 v[168:171], v[88:91], v[136:139], v[168:171]\n"
"s_waitcnt lgkmcnt(4)\n"
"v_cvt_pk_bf16_f32 v136, v196, v197\n"
"v_cvt_pk_bf16_f32 v137, v198, v199\n"
"v_cvt_pk_bf16_f32 v138, v144, v145\n"
"v_cvt_pk_bf16_f32 v139, v146, v147\n"
"v_mfma_f32_16x16x32_bf16 v[144:147], v[140:143], v[92:95], v[116:119]\n"
"ds_read_b64_tr_b16 v[196:197], v16 offset:2048\n"
"ds_read_b64_tr_b16 v[198:199], v255 offset:2304\n"
"buffer_load_dwordx4 v[208:211], v1, s[4:7], s97 offen offset:1024 sc1\n"
"v_exp_f32_e32 v132, v132\n"
"v_exp_f32_e32 v133, v133\n"
"v_mfma_f32_16x16x32_bf16 v[220:223], v[140:143], v[96:99], v[120:123]\n"
"ds_read_b64_tr_b16 v[140:141], v255 offset:2048\n"
"v_exp_f32_e32 v134, v134\n"
"v_exp_f32_e32 v135, v135\n"
"v_mfma_f32_16x16x32_bf16 v[224:227], v[184:187], v[96:99], v[120:123]\n"
"buffer_load_dwordx4 v[120:123], v1, s[4:7], s97 offen offset:2048 sc1\n"
"v_exp_f32_e32 v192, v192\n"
"v_exp_f32_e32 v193, v193\n"
"v_mfma_f32_16x16x32_bf16 v[228:231], v[184:187], v[92:95], v[116:119]\n"
"ds_read_b64_tr_b16 v[142:143], v16 offset:2304\n"
"ds_read_b64_tr_b16 v[116:117], v16 offset:16384\n"
"v_exp_f32_e32 v194, v194\n"
"v_exp_f32_e32 v195, v195\n"
"s_waitcnt lgkmcnt(5)\n"
"v_mfma_f32_16x16x32_bf16 v[148:151], v[88:91], v[216:219], v[148:151]\n"
"v_cvt_pk_bf16_f32 v184, v132, v133\n"
"v_cvt_pk_bf16_f32 v185, v134, v135\n"
"v_cvt_pk_bf16_f32 v186, v192, v193\n"
"v_mfma_f32_16x16x32_bf16 v[156:159], v[212:215], v[216:219], v[156:159]\n"
"buffer_load_dwordx4 v[132:135], v1, s[4:7], s97 offen offset:3072 sc1\n"
"ds_read_b64_tr_b16 v[118:119], v255 offset:16640\n"
"ds_read_b64_tr_b16 v[192:193], v255 offset:16384\n"
"v_cvt_pk_bf16_f32 v187, v194, v195\n"
"v_exp_f32_e32 v172, v172\n"
"v_exp_f32_e32 v173, v173\n"
"v_exp_f32_e32 v174, v174\n"
"v_mfma_f32_16x16x32_bf16 v[124:127], v[128:131], v[216:219], v[124:127]\n"
"s_add_u32 s97, s57, 0x40000\n"
"buffer_load_dwordx4 v[216:219], v0, s[4:7], s97 offen sc1\n"
"ds_read_b64_tr_b16 v[194:195], v16 offset:16640\n"
"v_exp_f32_e32 v175, v175\n"
"v_mfma_f32_16x16x32_bf16 v[160:163], v[128:131], v[136:139], v[160:163]\n"
"v_exp_f32_e32 v180, v180\n"
"v_add_u32_e32 v248, 0x26400, v0\n"
"v_exp_f32_e32 v181, v181\n"
"v_mfma_f32_16x16x32_bf16 v[164:167], v[212:215], v[136:139], v[164:167]\n"
"buffer_load_dwordx4 v[128:131], v0, s[4:7], s97 offen offset:1024 sc1\n"
"ds_read_b128 v[212:215], v248\n"
"v_exp_f32_e32 v182, v182\n"
"v_exp_f32_e32 v183, v183\n"
"v_mfma_f32_16x16x32_bf16 v[168:171], v[88:91], v[136:139], v[168:171]\n"
"ds_read_b128 v[232:235], v248 offset:1024\n"
"v_cvt_pk_bf16_f32 v136, v172, v173\n"
"v_cvt_pk_bf16_f32 v137, v174, v175\n"
"v_cvt_pk_bf16_f32 v138, v180, v181\n"
"v_cvt_pk_bf16_f32 v139, v182, v183\n"
"s_waitcnt lgkmcnt(6)\n"
"v_mfma_f32_16x16x32_bf16 v[148:151], v[88:91], v[184:187], v[148:151]\n"
"ds_read_b64_tr_b16 v[172:173], v16\n"
"buffer_load_dwordx4 v[180:183], v0, s[4:7], s97 offen offset:2048 sc1\n"
"v_cndmask_b32_e64 v144, v100, v144, s[20:21]\n"
"v_cndmask_b32_e64 v145, v101, v145, s[22:23]\n"
"v_cndmask_b32_e64 v146, v102, v146, s[24:25]\n"
"v_cndmask_b32_e64 v147, v103, v147, s[26:27]\n"
"v_cndmask_b32_e64 v228, v112, v228, s[28:29]\n"
"v_cndmask_b32_e64 v229, v113, v229, s[30:31]\n"
"v_cndmask_b32_e64 v230, v114, v230, s[32:33]\n"
"v_cndmask_b32_e64 v231, v115, v231, s[34:35]\n"
"v_exp_f32_e32 v144, v144\n"
"v_mfma_f32_16x16x32_bf16 v[156:159], v[196:199], v[184:187], v[156:159]\n"
"ds_read_b64_tr_b16 v[174:175], v255 offset:256\n"
"buffer_load_dwordx4 v[100:103], v0, s[4:7], s97 offen offset:3072 sc1\n"
"v_exp_f32_e32 v145, v145\n"
"v_exp_f32_e32 v146, v146\n"
"v_exp_f32_e32 v147, v147\n"
"v_exp_f32_e32 v228, v228\n"
"v_mfma_f32_16x16x32_bf16 v[124:127], v[140:143], v[184:187], v[124:127]\n"
"buffer_load_dwordx4 v[184:187], v1, s[4:7], s97 offen sc1\n"
"v_exp_f32_e32 v229, v229\n"
"v_exp_f32_e32 v230, v230\n"
"v_exp_f32_e32 v231, v231\n"
"v_cvt_pk_bf16_f32 v112, v144, v145\n"
"v_cvt_pk_bf16_f32 v113, v146, v147\n"
"v_cvt_pk_bf16_f32 v114, v228, v229\n"
"v_mfma_f32_16x16x32_bf16 v[160:163], v[140:143], v[136:139], v[160:163]\n"
"ds_read_b64_tr_b16 v[140:141], v255\n"
"v_cvt_pk_bf16_f32 v115, v230, v231\n"
"v_cndmask_b32_e64 v220, v104, v220, s[36:37]\n"
"v_cndmask_b32_e64 v221, v105, v221, s[38:39]\n"
"v_cndmask_b32_e64 v222, v106, v222, s[40:41]\n"
"v_cndmask_b32_e64 v223, v107, v223, s[42:43]\n"
"v_cndmask_b32_e64 v224, v108, v224, s[44:45]\n"
"v_cndmask_b32_e64 v225, v109, v225, s[46:47]\n"
"v_cndmask_b32_e64 v226, v110, v226, s[48:49]\n"
"v_cndmask_b32_e64 v227, v111, v227, s[50:51]\n"
"v_mfma_f32_16x16x32_bf16 v[164:167], v[196:199], v[136:139], v[164:167]\n"
"buffer_load_dwordx4 v[104:107], v1, s[4:7], s97 offen offset:1024 sc1\n"
"ds_read_b64_tr_b16 v[142:143], v16 offset:256\n"
"buffer_load_dwordx4 v[108:111], v1, s[4:7], s97 offen offset:2048 sc1\n"
"v_exp_f32_e32 v220, v220\n"
"v_exp_f32_e32 v221, v221\n"
"v_exp_f32_e32 v222, v222\n"
"v_exp_f32_e32 v223, v223\n"
"v_exp_f32_e32 v224, v224\n"
"v_mfma_f32_16x16x32_bf16 v[168:171], v[88:91], v[136:139], v[168:171]\n"
"s_waitcnt lgkmcnt(0)\n"
"v_exp_f32_e32 v225, v225\n"
"v_exp_f32_e32 v226, v226\n"
"v_exp_f32_e32 v227, v227\n"
"v_cvt_pk_bf16_f32 v136, v220, v221\n"
"v_cvt_pk_bf16_f32 v137, v222, v223\n"
"v_cvt_pk_bf16_f32 v138, v224, v225\n"
"v_cvt_pk_bf16_f32 v139, v226, v227\n"
"v_mfma_f32_16x16x32_bf16 v[148:151], v[88:91], v[112:115], v[148:151]\n"
"v_and_b32_e32 v212, v112, v212\n"
"v_and_b32_e32 v213, v113, v213\n"
"v_and_b32_e32 v214, v114, v214\n"
"v_and_b32_e32 v215, v115, v215\n"
"v_mfma_f32_16x16x32_bf16 v[168:171], v[88:91], v[136:139], v[168:171]\n"
"v_xor_b32_e32 v144, v112, v212\n"
"v_xor_b32_e32 v145, v113, v213\n"
"v_xor_b32_e32 v146, v114, v214\n"
"v_xor_b32_e32 v147, v115, v215\n"
"v_mfma_f32_16x16x32_bf16 v[156:159], v[172:175], v[212:215], v[156:159]\n"
"v_and_b32_e32 v232, v136, v232\n"
"v_and_b32_e32 v233, v137, v233\n"
"v_mfma_f32_16x16x32_bf16 v[124:127], v[140:143], v[212:215], v[124:127]\n"
"v_and_b32_e32 v234, v138, v234\n"
"v_and_b32_e32 v235, v139, v235\n"
"v_mfma_f32_16x16x32_bf16 v[156:159], v[116:119], v[144:147], v[156:159]\n"
"v_xor_b32_e32 v112, v136, v232\n"
"v_xor_b32_e32 v113, v137, v233\n"
"v_mfma_f32_16x16x32_bf16 v[124:127], v[192:195], v[144:147], v[124:127]\n"
"v_xor_b32_e32 v114, v138, v234\n"
"v_xor_b32_e32 v115, v139, v235\n"
"v_mfma_f32_16x16x32_bf16 v[164:167], v[172:175], v[232:235], v[164:167]\n"
"buffer_load_dwordx4 v[136:139], v1, s[4:7], s97 offen offset:3072 sc1\n"
"s_add_u32 s97, s58, 0x40000\n"
"buffer_load_dwordx4 v[144:147], v0, s[4:7], s97 offen sc1\n"
"v_mfma_f32_16x16x32_bf16 v[160:163], v[140:143], v[232:235], v[160:163]\n"
"buffer_load_dwordx4 v[140:143], v0, s[4:7], s97 offen offset:1024 sc1\n"
"v_mfma_f32_16x16x32_bf16 v[164:167], v[116:119], v[112:115], v[164:167]\n"
"buffer_load_dwordx4 v[116:119], v0, s[4:7], s97 offen offset:2048 sc1\n"
"v_mfma_f32_16x16x32_bf16 v[160:163], v[192:195], v[112:115], v[160:163]\n"
"buffer_load_dwordx4 v[112:115], v0, s[4:7], s97 offen offset:3072 sc1\n"
"v_max_f32_e32 v248, v148, v168\n"
"v_rcp_f32_e32 v148, v148\n"
"v_rcp_f32_e32 v168, v168\n"
"s_nop 0\n"
"v_max3_f32 v248, v248, v148, v168\n"
"v_cmp_nlt_f32_e32 vcc, v248, v252\n"
"s_cbranch_vccnz .Lmk_b29\n"
".Lmk_k34:\n"
"v_mul_f32_e32 v156, v156, v148\n"
"v_mul_f32_e32 v157, v157, v148\n"
"v_mul_f32_e32 v158, v158, v148\n"
"v_mul_f32_e32 v159, v159, v148\n"
"v_mul_f32_e32 v124, v124, v148\n"
"v_mul_f32_e32 v125, v125, v148\n"
"v_mul_f32_e32 v126, v126, v148\n"
"v_mul_f32_e32 v127, v127, v148\n"
"v_cvt_pk_bf16_f32 v88, v156, v157\n"
"v_cvt_pk_bf16_f32 v89, v158, v159\n"
"v_cvt_pk_bf16_f32 v90, v124, v125\n"
"v_cvt_pk_bf16_f32 v91, v126, v127\n"
"buffer_store_dwordx4 v[88:91], v0, s[4:7], s60 offen sc1\n"
"s_add_u32 s98, s60, 0x8000\n"
"v_mul_f32_e32 v164, v164, v168\n"
"v_mul_f32_e32 v165, v165, v168\n"
"v_mul_f32_e32 v166, v166, v168\n"
"v_mul_f32_e32 v167, v167, v168\n"
"v_mul_f32_e32 v160, v160, v168\n"
"v_mul_f32_e32 v161, v161, v168\n"
"v_mul_f32_e32 v162, v162, v168\n"
"v_mul_f32_e32 v163, v163, v168\n"
"v_cvt_pk_bf16_f32 v88, v164, v165\n"
"v_cvt_pk_bf16_f32 v89, v166, v167\n"
"v_cvt_pk_bf16_f32 v90, v160, v161\n"
"v_cvt_pk_bf16_f32 v91, v162, v163\n"
"buffer_store_dwordx4 v[88:91], v0, s[4:7], s98 offen sc1\n"
";\n"
"s_waitcnt vmcnt(20)\n"
"; @A G2 b.0\n"
"s_nop 0\n"
"v_mfma_f32_16x16x32_bf16 v[88:91], a[128:131], v[176:179], 0\n"
"v_mfma_f32_16x16x32_bf16 v[88:91], a[132:135], v[200:203], v[88:91]\n"
"s_waitcnt vmcnt(18)\n"
"v_mfma_f32_16x16x32_bf16 v[88:91], a[136:139], v[188:191], v[88:91]\n"
"buffer_load_dwordx4 v[92:95], v1, s[4:7], s97 offen sc1\n"
"v_mfma_f32_16x16x32_bf16 v[88:91], a[140:143], v[204:207], v[88:91]\n"
"s_waitcnt vmcnt(17)\n"
"v_mfma_f32_16x16x32_bf16 v[88:91], a[144:147], v[152:155], v[88:91]\n"
"v_mfma_f32_16x16x32_bf16 v[88:91], a[148:151], v[208:211], v[88:91]\n"
"s_waitcnt vmcnt(15)\n"
"v_mfma_f32_16x16x32_bf16 v[88:91], a[152:155], v[120:123], v[88:91]\n"
"buffer_load_dwordx4 v[96:99], v1, s[4:7], s97 offen offset:1024 sc1\n"
"v_mfma_f32_16x16x32_bf16 v[88:91], a[156:159], v[132:135], v[88:91]\n"
"v_mfma_f32_16x16x32_bf16 v[124:127], a[188:191], v[132:135], 0\n"
"v_mfma_f32_16x16x32_bf16 v[124:127], a[184:187], v[120:123], v[124:127]\n"
"v_mfma_f32_16x16x32_bf16 v[124:127], a[180:183], v[208:211], v[124:127]\n"
"buffer_load_dwordx4 v[148:151], v1, s[4:7], s97 offen offset:2048 sc1\n"
"v_mfma_f32_16x16x32_bf16 v[124:127], a[176:179], v[152:155], v[124:127]\n"
"v_mfma_f32_16x16x32_bf16 v[124:127], a[172:175], v[204:207], v[124:127]\n"
"s_nop 1\n"
"v_cmp_u_f32_e32 vcc, v88, v88\n"
"v_mfma_f32_16x16x32_bf16 v[124:127], a[168:171], v[188:191], v[124:127]\n"
"s_cbranch_vccnz .Lmk_p35s\n"
".Lmk_p35r:\n"
"v_mfma_f32_16x16x32_bf16 v[124:127], a[164:167], v[200:203], v[124:127]\n"
"ds_write_b128 v3, v[88:91]\n"
"v_mfma_f32_16x16x32_bf16 v[124:127], a[160:163], v[176:179], v[124:127]\n"
"buffer_load_dwordx4 v[88:91], v1, s[4:7], s97 offen offset:3072 sc1\n"
"v_mfma_f32_16x16x32_bf16 v[156:159], a[192:195], v[176:179], 0\n"
"v_mfma_f32_16x16x32_bf16 v[156:159], a[196:199], v[200:203], v[156:159]\n"
"s_add_u32 s98, s59, 0x40000\n"
"v_mfma_f32_16x16x32_bf16 v[156:159], a[200:203], v[188:191], v[156:159]\n"
"v_mfma_f32_16x16x32_bf16 v[156:159], a[204:207], v[204:207], v[156:159]\n"
"buffer_load_dwordx4 v[160:163], v0, s[4:7], s98 offen sc1\n"
"v_mfma_f32_16x16x32_bf16 v[156:159], a[208:211], v[152:155], v[156:159]\n"
"v_mfma_f32_16x16x32_bf16 v[156:159], a[212:215], v[208:211], v[156:159]\n"
"ds_write_b128 v3, v[124:127] offset:1024\n"
"v_mfma_f32_16x16x32_bf16 v[156:159], a[216:219], v[120:123], v[156:159]\n"
"v_mfma_f32_16x16x32_bf16 v[156:159], a[220:223], v[132:135], v[156:159]\n"
"buffer_load_dwordx4 v[124:127], v0, s[4:7], s98 offen offset:1024 sc1\n"
"v_mfma_f32_16x16x32_bf16 v[164:167], a[252:255], v[132:135], 0\n"
"v_mfma_f32_16x16x32_bf16 v[164:167], a[248:251], v[120:123], v[164:167]\n"
"v_mfma_f32_16x16x32_bf16 v[164:167], a[244:247], v[208:211], v[164:167]\n"
"v_mfma_f32_16x16x32_bf16 v[164:167], a[240:243], v[152:155], v[164:167]\n"
"buffer_load_dwordx4 v[120:123], v0, s[4:7], s98 offen offset:2048 sc1\n"
"v_mfma_f32_16x16x32_bf16 v[164:167], a[236:239], v[204:207], v[164:167]\n"
"v_mfma_f32_16x16x32_bf16 v[164:167], a[232:235], v[188:191], v[164:167]\n"
"ds_write_b128 v3, v[156:159] offset:2048\n"
"v_mfma_f32_16x16x32_bf16 v[164:167], a[228:231], v[200:203], v[164:167]\n"
"v_mfma_f32_16x16x32_bf16 v[164:167], a[224:227], v[176:179], v[164:167]\n"
"s_waitcnt vmcnt(17)\n"
"buffer_load_dwordx4 v[132:135], v0, s[4:7], s98 offen offset:3072 sc1\n"
"; @A G2 b.1\n"
"v_mfma_f32_16x16x32_bf16 v[152:155], a[128:131], v[216:219], 0\n"
"v_mfma_f32_16x16x32_bf16 v[152:155], a[132:135], v[128:131], v[152:155]\n"
"v_mfma_f32_16x16x32_bf16 v[152:155], a[136:139], v[180:183], v[152:155]\n"
"v_mfma_f32_16x16x32_bf16 v[152:155], a[140:143], v[100:103], v[152:155]\n"
"s_waitcnt vmcnt(14)\n"
"buffer_load_dwordx4 v[156:159], v1, s[4:7], s98 offen sc1\n"
"v_mfma_f32_16x16x32_bf16 v[152:155], a[144:147], v[184:187], v[152:155]\n"
"v_mfma_f32_16x16x32_bf16 v[152:155], a[148:151], v[104:107], v[152:155]\n"
"ds_write_b128 v3, v[164:167] offset:3072\n"
"v_mfma_f32_16x16x32_bf16 v[152:155], a[152:155], v[108:111], v[152:155]\n"
"v_mfma_f32_16x16x32_bf16 v[152:155], a[156:159], v[136:139], v[152:155]\n"
"buffer_load_dwordx4 v[164:167], v1, s[4:7], s98 offen offset:1024 sc1\n"
"v_mfma_f32_16x16x32_bf16 v[168:171], a[188:191], v[136:139], 0\n"
"v_mfma_f32_16x16x32_bf16 v[168:171], a[184:187], v[108:111], v[168:171]\n"
"v_mfma_f32_16x16x32_bf16 v[168:171], a[180:183], v[104:107], v[168:171]\n"
"v_mfma_f32_16x16x32_bf16 v[168:171], a[176:179], v[184:187], v[168:171]\n"
"buffer_load_dwordx4 v[172:175], v1, s[4:7], s98 offen offset:2048 sc1\n"
"v_mfma_f32_16x16x32_bf16 v[168:171], a[172:175], v[100:103], v[168:171]\n"
"s_nop 0\n"
"v_cmp_u_f32_e32 vcc, v152, v152\n"
"v_mfma_f32_16x16x32_bf16 v[168:171], a[168:171], v[180:183], v[168:171]\n"
"s_cbranch_vccnz .Lmk_p36s\n"
".Lmk_p36r:\n"
"v_mfma_f32_16x16x32_bf16 v[168:171], a[164:167], v[128:131], v[168:171]\n"
"ds_write_b128 v3, v[152:155] offset:6144\n"
"v_mfma_f32_16x16x32_bf16 v[168:171], a[160:163], v[216:219], v[168:171]\n"
"buffer_load_dwordx4 v[152:155], v1, s[4:7], s98 offen offset:3072 sc1\n"
"v_mfma_f32_16x16x32_bf16 v[176:179], a[192:195], v[216:219], 0\n"
"v_mfma_f32_16x16x32_bf16 v[176:179], a[196:199], v[128:131], v[176:179]\n"
"v_mfma_f32_16x16x32_bf16 v[176:179], a[200:203], v[180:183], v[176:179]\n"
"v_mfma_f32_16x16x32_bf16 v[176:179], a[204:207], v[100:103], v[176:179]\n"
"v_mfma_f32_16x16x32_bf16 v[176:179], a[208:211], v[184:187], v[176:179]\n"
"ds_read_b128 v[188:191], v2 offset:8192\n"
"ds_read_b128 v[192:195], v2 offset:9216\n"
"v_mfma_f32_16x16x32_bf16 v[176:179], a[212:215], v[104:107], v[176:179]\n"
"ds_write_b128 v3, v[168:171] offset:7168\n"
"v_mfma_f32_16x16x32_bf16 v[176:179], a[216:219], v[108:111], v[176:179]\n"
"v_mfma_f32_16x16x32_bf16 v[176:179], a[220:223], v[136:139], v[176:179]\n"
"v_mfma_f32_16x16x32_bf16 v[168:171], a[252:255], v[136:139], 0\n"
"s_mov_b32 s98, .Lmk_b0-.Lmk_g39\n"
"s_getpc_b64 s[84:85]\n"
".Lmk_g39:\n"
"s_add_u32 s84, s84, s98\n"
"s_addc_u32 s85, s85, 0\n"
"s_and_b32 s84, s84, -16\n"
"v_mfma_f32_16x16x32_bf16 v[168:171], a[248:251], v[108:111], v[168:171]\n"
"s_and_b32 s85, s85, 0xffff\n"
"s_sub_u32 s98, .Lmk_end-.Lmk_g39, s98\n"
"s_min_u32 s86, s98, 0x3000\n"
"s_mov_b32 s87, 0x20000\n"
"v_mfma_f32_16x16x32_bf16 v[168:171], a[244:247], v[104:107], v[168:171]\n"
"s_mov_b32 m0, 0x27800\n"
"s_nop 0\n"
"buffer_load_dwordx4 v0, s[84:87], s99 offen lds\n"
"v_mfma_f32_16x16x32_bf16 v[168:171], a[240:243], v[184:187], v[168:171]\n"
"s_waitcnt lgkmcnt(1)\n"
"v_lshlrev_b32_e32 v255, 16, v188\n"
"v_and_b32_e32 v254, 0xffff0000, v188\n"
"v_mfma_f32_16x16x32_bf16 v[168:171], a[236:239], v[100:103], v[168:171]\n"
"v_lshlrev_b32_e32 v253, 16, v189\n"
"v_and_b32_e32 v252, 0xffff0000, v189\n"
"v_mfma_f32_16x16x32_bf16 v[168:171], a[232:235], v[180:183], v[168:171]\n"
"ds_write_b128 v3, v[176:179] offset:8192\n"
"v_mfma_f32_16x16x32_bf16 v[168:171], a[228:231], v[128:131], v[168:171]\n"
"v_lshlrev_b32_e32 v251, 16, v190\n"
"v_and_b32_e32 v250, 0xffff0000, v190\n"
"v_mfma_f32_16x16x32_bf16 v[168:171], a[224:227], v[216:219], v[168:171]\n"
"s_waitcnt vmcnt(15)\n"
"v_lshlrev_b32_e32 v249, 16, v191\n"
"v_and_b32_e32 v248, 0xffff0000, v191\n"
"; @A G2 b.2\n"
"v_mfma_f32_16x16x32_bf16 v[100:103], a[128:131], v[144:147], 0\n"
"v_lshlrev_b32_e32 v247, 16, v192\n"
"v_and_b32_e32 v246, 0xffff0000, v192\n"
"v_mfma_f32_16x16x32_bf16 v[100:103], a[132:135], v[140:143], v[100:103]\n"
"v_lshlrev_b32_e32 v245, 16, v193\n"
"v_and_b32_e32 v244, 0xffff0000, v193\n"
"v_mfma_f32_16x16x32_bf16 v[100:103], a[136:139], v[116:119], v[100:103]\n"
"v_lshlrev_b32_e32 v243, 16, v194\n"
"v_and_b32_e32 v242, 0xffff0000, v194\n"
"v_mfma_f32_16x16x32_bf16 v[100:103], a[140:143], v[112:115], v[100:103]\n"
"s_waitcnt vmcnt(9)\n"
"v_lshlrev_b32_e32 v241, 16, v195\n"
"v_and_b32_e32 v240, 0xffff0000, v195\n"
"v_mfma_f32_16x16x32_bf16 v[100:103], a[144:147], v[92:95], v[100:103]\n"
"v_mfma_f32_16x16x32_bf16 v[100:103], a[148:151], v[96:99], v[100:103]\n"
"ds_write_b128 v3, v[168:171] offset:9216\n"
"v_mfma_f32_16x16x32_bf16 v[100:103], a[152:155], v[148:151], v[100:103]\n"
"v_mfma_f32_16x16x32_bf16 v[100:103], a[156:159], v[88:91], v[100:103]\n"
"v_mfma_f32_16x16x32_bf16 v[104:107], a[188:191], v[88:91], 0\n"
"v_mfma_f32_16x16x32_bf16 v[104:107], a[184:187], v[148:151], v[104:107]\n"
"s_waitcnt lgkmcnt(0)\n"
"s_barrier\n"
"v_mfma_f32_16x16x32_bf16 v[104:107], a[180:183], v[96:99], v[104:107]\n"
"ds_read_b128 v[108:111], v4\n"
"ds_read_b128 v[128:131], v5\n"
"v_mfma_f32_16x16x32_bf16 v[104:107], a[176:179], v[92:95], v[104:107]\n"
"v_mfma_f32_16x16x32_bf16 v[104:107], a[172:175], v[112:115], v[104:107]\n"
"s_add_u32 s98, s56, 0\n"
"v_cmp_u_f32_e32 vcc, v100, v100\n"
"v_mfma_f32_16x16x32_bf16 v[104:107], a[168:171], v[116:119], v[104:107]\n"
"s_cbranch_vccnz .Lmk_p37s\n"
".Lmk_p37r:\n"
"v_mfma_f32_16x16x32_bf16 v[104:107], a[164:167], v[140:143], v[104:107]\n"
"ds_write_b128 v3, v[100:103] offset:12288\n"
"v_mfma_f32_16x16x32_bf16 v[104:107], a[160:163], v[144:147], v[104:107]\n"
"buffer_load_dwordx4 v[100:103], v0, s[4:7], s98 offen sc1\n"
"v_mfma_f32_16x16x32_bf16 v[136:139], a[192:195], v[144:147], 0\n"
"ds_read_b128 v[168:171], v4 offset:1024\n"
"ds_read_b128 v[176:179], v5 offset:1024\n"
"v_mfma_f32_16x16x32_bf16 v[136:139], a[196:199], v[140:143], v[136:139]\n"
"s_waitcnt lgkmcnt(3)\n"
"v_add_f32_e32 v108, v108, v255\n"
"v_add_f32_e32 v109, v109, v254\n"
"v_mfma_f32_16x16x32_bf16 v[136:139], a[200:203], v[116:119], v[136:139]\n"
"buffer_load_dwordx4 v[180:183], v0, s[4:7], s98 offen offset:1024 sc1\n"
"v_mfma_f32_16x16x32_bf16 v[136:139], a[204:207], v[112:115], v[136:139]\n"
"v_add_f32_e32 v110, v110, v253\n"
"v_add_f32_e32 v111, v111, v252\n"
"v_mfma_f32_16x16x32_bf16 v[136:139], a[208:211], v[92:95], v[136:139]\n"
"v_add_f32_e32 v108, v108, v128\n"
"v_add_f32_e32 v109, v109, v129\n"
"v_mfma_f32_16x16x32_bf16 v[136:139], a[212:215], v[96:99], v[136:139]\n"
"ds_write_b128 v3, v[104:107] offset:13312\n"
"v_mfma_f32_16x16x32_bf16 v[136:139], a[216:219], v[148:151], v[136:139]\n"
"buffer_load_dwordx4 v[104:107], v0, s[4:7], s98 offen offset:2048 sc1\n"
"v_mfma_f32_16x16x32_bf16 v[136:139], a[220:223], v[88:91], v[136:139]\n"
"ds_read_b128 v[184:187], v4 offset:2048\n"
"ds_read_b128 v[188:191], v5 offset:2048\n"
"v_mfma_f32_16x16x32_bf16 v[192:195], a[252:255], v[88:91], 0\n"
"s_waitcnt lgkmcnt(3)\n"
"v_add_f32_e32 v110, v110, v130\n"
"v_add_f32_e32 v111, v111, v131\n"
"v_mfma_f32_16x16x32_bf16 v[192:195], a[248:251], v[148:151], v[192:195]\n"
"buffer_load_dwordx4 v[88:91], v0, s[4:7], s98 offen offset:3072 sc1\n"
"v_mfma_f32_16x16x32_bf16 v[192:195], a[244:247], v[96:99], v[192:195]\n"
"v_add_f32_e32 v168, v168, v251\n"
"v_add_f32_e32 v169, v169, v250\n"
"v_mfma_f32_16x16x32_bf16 v[192:195], a[240:243], v[92:95], v[192:195]\n"
"v_add_f32_e32 v170, v170, v249\n"
"v_add_f32_e32 v171, v171, v248\n"
"v_mfma_f32_16x16x32_bf16 v[192:195], a[236:239], v[112:115], v[192:195]\n"
"buffer_load_dwordx4 v[92:95], v1, s[4:7], s98 offen sc1\n"
"v_mfma_f32_16x16x32_bf16 v[192:195], a[232:235], v[116:119], v[192:195]\n"
"ds_write_b128 v3, v[136:139] offset:14336\n"
"v_mfma_f32_16x16x32_bf16 v[192:195], a[228:231], v[140:143], v[192:195]\n"
"ds_read_b128 v[96:99], v4 offset:3072\n"
"ds_read_b128 v[112:115], v5 offset:3072\n"
"v_mfma_f32_16x16x32_bf16 v[192:195], a[224:227], v[144:147], v[192:195]\n"
"s_waitcnt vmcnt(10)\n"
"buffer_load_dwordx4 v[116:119], v1, s[4:7], s98 offen offset:1024 sc1\n"
"; @A G2 b.3\n"
"v_mfma_f32_16x16x32_bf16 v[128:131], a[128:131], v[160:163], v[108:111]\n"
"v_add_f32_e32 v168, v168, v176\n"
"v_add_f32_e32 v169, v169, v177\n"
"v_mfma_f32_16x16x32_bf16 v[128:131], a[132:135], v[124:127], v[128:131]\n"
"s_waitcnt lgkmcnt(3)\n"
"v_add_f32_e32 v170, v170, v178\n"
"v_add_f32_e32 v171, v171, v179\n"
"v_mfma_f32_16x16x32_bf16 v[128:131], a[136:139], v[120:123], v[128:131]\n"
"buffer_load_dwordx4 v[136:139], v1, s[4:7], s98 offen offset:2048 sc1\n"
"v_mfma_f32_16x16x32_bf16 v[128:131], a[140:143], v[132:135], v[128:131]\n"
"s_waitcnt vmcnt(8)\n"
"v_add_f32_e32 v184, v184, v247\n"
"v_add_f32_e32 v185, v185, v246\n"
"v_mfma_f32_16x16x32_bf16 v[128:131], a[144:147], v[156:159], v[128:131]\n"
"v_add_f32_e32 v186, v186, v245\n"
"v_add_f32_e32 v187, v187, v244\n"
"v_mfma_f32_16x16x32_bf16 v[128:131], a[148:151], v[164:167], v[128:131]\n"
"ds_write_b128 v3, v[192:195] offset:15360\n"
"v_mfma_f32_16x16x32_bf16 v[128:131], a[152:155], v[172:175], v[128:131]\n"
"buffer_load_dwordx4 v[140:143], v1, s[4:7], s98 offen offset:3072 sc1\n"
"v_mfma_f32_16x16x32_bf16 v[128:131], a[156:159], v[152:155], v[128:131]\n"
"s_add_u32 s98, s57, 0\n"
"v_add_f32_e32 v184, v184, v188\n"
"v_add_f32_e32 v185, v185, v189\n"
"v_mfma_f32_16x16x32_bf16 v[144:147], a[188:191], v[152:155], v[168:171]\n"
"s_waitcnt lgkmcnt(1)\n"
"v_add_f32_e32 v186, v186, v190\n"
"v_add_f32_e32 v187, v187, v191\n"
"v_mfma_f32_16x16x32_bf16 v[144:147], a[184:187], v[172:175], v[144:147]\n"
"s_waitcnt lgkmcnt(0)\n"
"s_barrier\n"
"v_mfma_f32_16x16x32_bf16 v[144:147], a[180:183], v[164:167], v[144:147]\n"
"buffer_load_dwordx4 v[148:151], v0, s[4:7], s98 offen sc1\n"
"v_mfma_f32_16x16x32_bf16 v[144:147], a[176:179], v[156:159], v[144:147]\n"
"v_add_f32_e32 v96, v96, v243\n"
"v_add_f32_e32 v97, v97, v242\n"
"v_mfma_f32_16x16x32_bf16 v[144:147], a[172:175], v[132:135], v[144:147]\n"
"v_cmp_u_f32_e32 vcc, v128, v128\n"
"v_add_f32_e32 v98, v98, v241\n"
"v_mfma_f32_16x16x32_bf16 v[144:147], a[168:171], v[120:123], v[144:147]\n"
"s_cbranch_vccnz .Lmk_p38s\n"
".Lmk_p38r:\n"
"v_add_f32_e32 v99, v99, v240\n"
"v_mfma_f32_16x16x32_bf16 v[144:147], a[164:167], v[124:127], v[144:147]\n"
"buffer_load_dwordx4 v[108:111], v0, s[4:7], s98 offen offset:1024 sc1\n"
"v_mfma_f32_16x16x32_bf16 v[144:147], a[160:163], v[160:163], v[144:147]\n"
"v_add_f32_e32 v96, v96, v112\n"
"v_add_f32_e32 v97, v97, v113\n"
"v_mfma_f32_16x16x32_bf16 v[184:187], a[192:195], v[160:163], v[184:187]\n"
"v_add_f32_e32 v98, v98, v114\n"
"v_add_f32_e32 v99, v99, v115\n"
"v_mfma_f32_16x16x32_bf16 v[184:187], a[196:199], v[124:127], v[184:187]\n"
"buffer_load_dwordx4 v[112:115], v0, s[4:7], s98 offen offset:2048 sc1\n"
"v_mfma_f32_16x16x32_bf16 v[184:187], a[200:203], v[120:123], v[184:187]\n"
"ds_read_b128 v[168:171], v6\n"
"ds_read_b128 v[176:179], v6 offset:1024\n"
"v_mfma_f32_16x16x32_bf16 v[184:187], a[204:207], v[132:135], v[184:187]\n"
"ds_read_b128 v[188:191], v6 offset:2048\n"
"ds_read_b128 v[192:195], v6 offset:3072\n"
"v_mfma_f32_16x16x32_bf16 v[184:187], a[208:211], v[156:159], v[184:187]\n"
"buffer_load_dwordx4 v[196:199], v0, s[4:7], s98 offen offset:3072 sc1\n"
"v_mfma_f32_16x16x32_bf16 v[184:187], a[212:215], v[164:167], v[184:187]\n"
"v_mfma_f32_16x16x32_bf16 v[184:187], a[216:219], v[172:175], v[184:187]\n"
"v_mfma_f32_16x16x32_bf16 v[184:187], a[220:223], v[152:155], v[184:187]\n"
"buffer_load_dwordx4 v[200:203], v1, s[4:7], s98 offen sc1\n"
"v_mfma_f32_16x16x32_bf16 v[96:99], a[252:255], v[152:155], v[96:99]\n"
"s_waitcnt lgkmcnt(0)\n"
"v_add_f32_e32 v168, v128, v168\n"
"v_add_f32_e32 v169, v129, v169\n"
"v_mfma_f32_16x16x32_bf16 v[96:99], a[248:251], v[172:175], v[96:99]\n"
"v_add_f32_e32 v170, v130, v170\n"
"v_add_f32_e32 v171, v131, v171\n"
"v_mfma_f32_16x16x32_bf16 v[96:99], a[244:247], v[164:167], v[96:99]\n"
"buffer_load_dwordx4 v[128:131], v1, s[4:7], s98 offen offset:1024 sc1\n"
"v_mfma_f32_16x16x32_bf16 v[96:99], a[240:243], v[156:159], v[96:99]\n"
"v_add_f32_e32 v176, v144, v176\n"
"v_add_f32_e32 v177, v145, v177\n"
"v_mfma_f32_16x16x32_bf16 v[96:99], a[236:239], v[132:135], v[96:99]\n"
"v_add_f32_e32 v178, v146, v178\n"
"v_add_f32_e32 v179, v147, v179\n"
"v_mfma_f32_16x16x32_bf16 v[96:99], a[232:235], v[120:123], v[96:99]\n"
"buffer_load_dwordx4 v[120:123], v1, s[4:7], s98 offen offset:2048 sc1\n"
"v_mfma_f32_16x16x32_bf16 v[96:99], a[228:231], v[124:127], v[96:99]\n"
"v_cvt_pk_bf16_f32 v124, v168, v169\n"
"v_cvt_pk_bf16_f32 v125, v170, v171\n"
"v_mfma_f32_16x16x32_bf16 v[96:99], a[224:227], v[160:163], v[96:99]\n"
"s_add_u32 s97, s61, 0x440000\n"
"v_cvt_pk_bf16_f32 v126, v176, v177\n"
"v_cvt_pk_bf16_f32 v127, v178, v179\n"
"buffer_store_dwordx4 v[124:127], v0, s[4:7], s97 offen sc1\n"
"ds_write_b128 v2, v[124:127] offset:8192\n"
"buffer_load_dwordx4 v[132:135], v1, s[4:7], s98 offen offset:3072 sc1\n"
"; @A fin b\n"
"s_add_u32 s98, s61, 0x440000\n"
"v_mov_b32_e32 v144, 0x3f803f80\n"
"v_mov_b32_e32 v145, 0x3f803f80\n"
"v_mov_b32_e32 v146, 0x3f803f80\n"
"v_mov_b32_e32 v147, 0x3f803f80\n"
"v_add_f32_e32 v188, v184, v188\n"
"v_add_f32_e32 v189, v185, v189\n"
"v_add_f32_e32 v190, v186, v190\n"
"v_add_f32_e32 v191, v187, v191\n"
"v_add_f32_e32 v192, v96, v192\n"
"v_add_f32_e32 v193, v97, v193\n"
"v_add_f32_e32 v194, v98, v194\n"
"v_add_f32_e32 v195, v99, v195\n"
"v_cvt_pk_bf16_f32 v96, v188, v189\n"
"v_cvt_pk_bf16_f32 v97, v190, v191\n"
"v_cvt_pk_bf16_f32 v98, v192, v193\n"
"v_cvt_pk_bf16_f32 v99, v194, v195\n"
"v_mfma_f32_16x16x32_bf16 v[152:155], v[144:147], v[124:127], 0\n"
"buffer_store_dwordx4 v[96:99], v0, s[4:7], s98 offen offset:1024 sc1\n"
"ds_write_b128 v2, v[96:99] offset:9216\n"
"v_mfma_f32_16x16x32_bf16 v[156:159], v[124:127], v[124:127], 0\n"
"v_mfma_f32_16x16x32_bf16 v[152:155], v[144:147], v[96:99], v[152:155]\n"
"v_mfma_f32_16x16x32_bf16 v[156:159], v[96:99], v[96:99], v[156:159]\n"
"s_mov_b32 s96, 0xaaaaaaaa\n"
"s_mov_b32 s97, 0xaaaaaaaa\n"
"s_nop 5\n"
"v_cndmask_b32_e64 v156, v156, v157, s[96:97]\n"
"v_cndmask_b32_e64 v158, v158, v159, s[96:97]\n"
"s_mov_b32 s96, 0xcccccccc\n"
"s_mov_b32 s97, 0xcccccccc\n"
"v_cndmask_b32_e64 v153, v156, v158, s[96:97]\n"
"s_mov_b32 s96, 0xf0000f\n"
"s_mov_b32 s97, 0xf0000f00\n"
"s_add_u32 s98, s63, 0x44000\n"
"v_cndmask_b32_e64 v152, v152, v153, s[96:97]\n"
"buffer_store_dword v152, v14, s[4:7], s98 offen sc1\n"
"s_waitcnt vmcnt(17)\n"
"; @A G2 a.0\n"
"v_mfma_f32_16x16x32_bf16 v[96:99], a[128:131], v[100:103], 0\n"
"s_add_u32 s98, s58, 0\n"
"buffer_load_dwordx4 v[124:127], v0, s[4:7], s98 offen sc1\n"
"v_mfma_f32_16x16x32_bf16 v[96:99], a[132:135], v[180:183], v[96:99]\n"
"s_waitcnt vmcnt(16)\n"
"v_mfma_f32_16x16x32_bf16 v[96:99], a[136:139], v[104:107], v[96:99]\n"
"v_mfma_f32_16x16x32_bf16 v[96:99], a[140:143], v[88:91], v[96:99]\n"
"s_waitcnt vmcnt(14)\n"
"v_mfma_f32_16x16x32_bf16 v[96:99], a[144:147], v[92:95], v[96:99]\n"
"buffer_load_dwordx4 v[144:147], v0, s[4:7], s98 offen offset:1024 sc1\n"
"v_mfma_f32_16x16x32_bf16 v[96:99], a[148:151], v[116:119], v[96:99]\n"
"s_waitcnt vmcnt(13)\n"
"v_mfma_f32_16x16x32_bf16 v[96:99], a[152:155], v[136:139], v[96:99]\n"
"v_mfma_f32_16x16x32_bf16 v[96:99], a[156:159], v[140:143], v[96:99]\n"
"v_mfma_f32_16x16x32_bf16 v[152:155], a[188:191], v[140:143], 0\n"
"buffer_load_dwordx4 v[156:159], v0, s[4:7], s98 offen offset:2048 sc1\n"
"v_mfma_f32_16x16x32_bf16 v[152:155], a[184:187], v[136:139], v[152:155]\n"
"v_mfma_f32_16x16x32_bf16 v[152:155], a[180:183], v[116:119], v[152:155]\n"
"v_mfma_f32_16x16x32_bf16 v[152:155], a[176:179], v[92:95], v[152:155]\n"
"v_mfma_f32_16x16x32_bf16 v[152:155], a[172:175], v[88:91], v[152:155]\n"
"s_nop 1\n"
"v_cmp_u_f32_e32 vcc, v96, v96\n"
"v_mfma_f32_16x16x32_bf16 v[152:155], a[168:171], v[104:107], v[152:155]\n"
"s_cbranch_vccnz .Lmk_p40s\n"
".Lmk_p40r:\n"
"v_mfma_f32_16x16x32_bf16 v[152:155], a[164:167], v[180:183], v[152:155]\n"
"ds_write_b128 v3, v[96:99]\n"
"v_mfma_f32_16x16x32_bf16 v[152:155], a[160:163], v[100:103], v[152:155]\n"
"buffer_load_dwordx4 v[160:163], v0, s[4:7], s98 offen offset:3072 sc1\n"
"v_mfma_f32_16x16x32_bf16 v[96:99], a[192:195], v[100:103], 0\n"
"v_mfma_f32_16x16x32_bf16 v[96:99], a[196:199], v[180:183], v[96:99]\n"
"v_mfma_f32_16x16x32_bf16 v[96:99], a[200:203], v[104:107], v[96:99]\n"
"v_mfma_f32_16x16x32_bf16 v[96:99], a[204:207], v[88:91], v[96:99]\n"
"buffer_load_dwordx4 v[164:167], v1, s[4:7], s98 offen sc1\n"
"v_mfma_f32_16x16x32_bf16 v[96:99], a[208:211], v[92:95], v[96:99]\n"
"v_mfma_f32_16x16x32_bf16 v[96:99], a[212:215], v[116:119], v[96:99]\n"
"ds_write_b128 v3, v[152:155] offset:1024\n"
"v_mfma_f32_16x16x32_bf16 v[96:99], a[216:219], v[136:139], v[96:99]\n"
"v_mfma_f32_16x16x32_bf16 v[96:99], a[220:223], v[140:143], v[96:99]\n"
"buffer_load_dwordx4 v[152:155], v1, s[4:7], s98 offen offset:1024 sc1\n"
"v_mfma_f32_16x16x32_bf16 v[168:171], a[252:255], v[140:143], 0\n"
"v_mfma_f32_16x16x32_bf16 v[168:171], a[248:251], v[136:139], v[168:171]\n"
"v_mfma_f32_16x16x32_bf16 v[168:171], a[244:247], v[116:119], v[168:171]\n"
"v_mfma_f32_16x16x32_bf16 v[168:171], a[240:243], v[92:95], v[168:171]\n"
"buffer_load_dwordx4 v[116:119], v1, s[4:7], s98 offen offset:2048 sc1\n"
"v_mfma_f32_16x16x32_bf16 v[168:171], a[236:239], v[88:91], v[168:171]\n"
"v_mfma_f32_16x16x32_bf16 v[168:171], a[232:235], v[104:107], v[168:171]\n"
"ds_write_b128 v3, v[96:99] offset:2048\n"
"v_mfma_f32_16x16x32_bf16 v[168:171], a[228:231], v[180:183], v[168:171]\n"
"v_mfma_f32_16x16x32_bf16 v[168:171], a[224:227], v[100:103], v[168:171]\n"
"s_waitcnt vmcnt(14)\n"
"buffer_load_dwordx4 v[136:139], v1, s[4:7], s98 offen offset:3072 sc1\n"
"; @A G2 a.1\n"
"v_mfma_f32_16x16x32_bf16 v[88:91], a[128:131], v[148:151], 0\n"
"v_mfma_f32_16x16x32_bf16 v[88:91], a[132:135], v[108:111], v[88:91]\n"
"s_add_u32 s98, s59, 0\n"
"v_mfma_f32_16x16x32_bf16 v[88:91], a[136:139], v[112:115], v[88:91]\n"
"v_mfma_f32_16x16x32_bf16 v[88:91], a[140:143], v[196:199], v[88:91]\n"
"s_waitcnt vmcnt(10)\n"
"buffer_load_dwordx4 v[172:175], v0, s[4:7], s98 offen sc1\n"
"v_mfma_f32_16x16x32_bf16 v[88:91], a[144:147], v[200:203], v[88:91]\n"
"v_mfma_f32_16x16x32_bf16 v[88:91], a[148:151], v[128:131], v[88:91]\n"
"ds_write_b128 v3, v[168:171] offset:3072\n"
"v_mfma_f32_16x16x32_bf16 v[88:91], a[152:155], v[120:123], v[88:91]\n"
"v_mfma_f32_16x16x32_bf16 v[88:91], a[156:159], v[132:135], v[88:91]\n"
"buffer_load_dwordx4 v[168:171], v0, s[4:7], s98 offen offset:1024 sc1\n"
"v_mfma_f32_16x16x32_bf16 v[92:95], a[188:191], v[132:135], 0\n"
"s_add_u32 s97, s75, 0x10000\n"
"buffer_load_dwordx2 v[254:255], v13, s[4:7], s97 offen sc1\n"
"v_mfma_f32_16x16x32_bf16 v[92:95], a[184:187], v[120:123], v[92:95]\n"
"v_mfma_f32_16x16x32_bf16 v[92:95], a[180:183], v[128:131], v[92:95]\n"
"v_mfma_f32_16x16x32_bf16 v[92:95], a[176:179], v[200:203], v[92:95]\n"
"buffer_load_dwordx4 v[176:179], v0, s[4:7], s98 offen offset:2048 sc1\n"
"v_mfma_f32_16x16x32_bf16 v[92:95], a[172:175], v[196:199], v[92:95]\n"
"v_cmp_u_f32_e32 vcc, v88, v88\n"
"v_mfma_f32_16x16x32_bf16 v[92:95], a[168:171], v[112:115], v[92:95]\n"
"s_cbranch_vccnz .Lmk_p41s\n"
".Lmk_p41r:\n"
"v_mfma_f32_16x16x32_bf16 v[92:95], a[164:167], v[108:111], v[92:95]\n"
"ds_write_b128 v3, v[88:91] offset:6144\n"
"v_mfma_f32_16x16x32_bf16 v[92:95], a[160:163], v[148:151], v[92:95]\n"
"buffer_load_dwordx4 v[180:183], v0, s[4:7], s98 offen offset:3072 sc1\n"
"v_mfma_f32_16x16x32_bf16 v[88:91], a[192:195], v[148:151], 0\n"
"v_mfma_f32_16x16x32_bf16 v[88:91], a[196:199], v[108:111], v[88:91]\n"
"v_mfma_f32_16x16x32_bf16 v[88:91], a[200:203], v[112:115], v[88:91]\n"
"v_mfma_f32_16x16x32_bf16 v[88:91], a[204:207], v[196:199], v[88:91]\n"
"buffer_load_dwordx4 v[184:187], v1, s[4:7], s98 offen sc1\n"
"v_mfma_f32_16x16x32_bf16 v[88:91], a[208:211], v[200:203], v[88:91]\n"
"ds_read_b128 v[96:99], v2\n"
"ds_read_b128 v[100:103], v2 offset:1024\n"
"v_mfma_f32_16x16x32_bf16 v[88:91], a[212:215], v[128:131], v[88:91]\n"
"ds_write_b128 v3, v[92:95] offset:7168\n"
"v_mfma_f32_16x16x32_bf16 v[88:91], a[216:219], v[120:123], v[88:91]\n"
"v_mfma_f32_16x16x32_bf16 v[88:91], a[220:223], v[132:135], v[88:91]\n"
"buffer_load_dwordx4 v[188:191], v1, s[4:7], s98 offen offset:1024 sc1\n"
"v_mfma_f32_16x16x32_bf16 v[92:95], a[252:255], v[132:135], 0\n"
"v_mfma_f32_16x16x32_bf16 v[92:95], a[248:251], v[120:123], v[92:95]\n"
"s_waitcnt lgkmcnt(1)\n"
"v_lshlrev_b32_e32 v253, 16, v96\n"
"v_and_b32_e32 v252, 0xffff0000, v96\n"
"v_mfma_f32_16x16x32_bf16 v[92:95], a[244:247], v[128:131], v[92:95]\n"
"v_lshlrev_b32_e32 v251, 16, v97\n"
"v_and_b32_e32 v250, 0xffff0000, v97\n"
"v_mfma_f32_16x16x32_bf16 v[92:95], a[240:243], v[200:203], v[92:95]\n"
"buffer_load_dwordx4 v[192:195], v1, s[4:7], s98 offen offset:2048 sc1\n"
"v_mfma_f32_16x16x32_bf16 v[92:95], a[236:239], v[196:199], v[92:95]\n"
"v_lshlrev_b32_e32 v249, 16, v98\n"
"v_and_b32_e32 v248, 0xffff0000, v98\n"
"v_mfma_f32_16x16x32_bf16 v[92:95], a[232:235], v[112:115], v[92:95]\n"
"ds_write_b128 v3, v[88:91] offset:8192\n"
"v_mfma_f32_16x16x32_bf16 v[92:95], a[228:231], v[108:111], v[92:95]\n"
"v_lshlrev_b32_e32 v247, 16, v99\n"
"v_and_b32_e32 v246, 0xffff0000, v99\n"
"v_mfma_f32_16x16x32_bf16 v[92:95], a[224:227], v[148:151], v[92:95]\n"
"s_waitcnt vmcnt(12)\n"
"buffer_load_dwordx4 v[196:199], v1, s[4:7], s98 offen offset:3072 sc1\n"
"; @A G2 a.2\n"
"v_mfma_f32_16x16x32_bf16 v[96:99], a[128:131], v[124:127], 0\n"
"v_lshlrev_b32_e32 v245, 16, v100\n"
"v_and_b32_e32 v244, 0xffff0000, v100\n"
"v_mfma_f32_16x16x32_bf16 v[96:99], a[132:135], v[144:147], v[96:99]\n"
"v_lshlrev_b32_e32 v243, 16, v101\n"
"v_and_b32_e32 v242, 0xffff0000, v101\n"
"v_mfma_f32_16x16x32_bf16 v[96:99], a[136:139], v[156:159], v[96:99]\n"
"v_lshlrev_b32_e32 v241, 16, v102\n"
"v_and_b32_e32 v240, 0xffff0000, v102\n"
"v_mfma_f32_16x16x32_bf16 v[96:99], a[140:143], v[160:163], v[96:99]\n"
"s_waitcnt vmcnt(9)\n"
"v_lshlrev_b32_e32 v239, 16, v103\n"
"v_and_b32_e32 v238, 0xffff0000, v103\n"
"v_mfma_f32_16x16x32_bf16 v[96:99], a[144:147], v[164:167], v[96:99]\n"
"v_mfma_f32_16x16x32_bf16 v[96:99], a[148:151], v[152:155], v[96:99]\n"
"ds_write_b128 v3, v[92:95] offset:9216\n"
"v_mfma_f32_16x16x32_bf16 v[96:99], a[152:155], v[116:119], v[96:99]\n"
"v_mfma_f32_16x16x32_bf16 v[96:99], a[156:159], v[136:139], v[96:99]\n"
"v_mfma_f32_16x16x32_bf16 v[104:107], a[188:191], v[136:139], 0\n"
"s_add_u32 s98, s52, 0x440000\n"
"buffer_load_dwordx4 v[88:91], v0, s[4:7], s98 offen sc1\n"
"v_mfma_f32_16x16x32_bf16 v[104:107], a[184:187], v[116:119], v[104:107]\n"
"s_waitcnt lgkmcnt(0)\n"
"s_barrier\n"
"v_mfma_f32_16x16x32_bf16 v[104:107], a[180:183], v[152:155], v[104:107]\n"
"ds_read_b128 v[140:143], v4\n"
"ds_read_b128 v[112:115], v5\n"
"v_mfma_f32_16x16x32_bf16 v[104:107], a[176:179], v[164:167], v[104:107]\n"
"buffer_load_dwordx4 v[92:95], v0, s[4:7], s98 offen offset:1024 sc1\n"
"v_mfma_f32_16x16x32_bf16 v[104:107], a[172:175], v[160:163], v[104:107]\n"
"v_cmp_u_f32_e32 vcc, v96, v96\n"
"v_mfma_f32_16x16x32_bf16 v[104:107], a[168:171], v[156:159], v[104:107]\n"
"s_cbranch_vccnz .Lmk_p42s\n"
".Lmk_p42r:\n"
"v_mfma_f32_16x16x32_bf16 v[104:107], a[164:167], v[144:147], v[104:107]\n"
"ds_write_b128 v3, v[96:99] offset:12288\n"
"v_mfma_f32_16x16x32_bf16 v[104:107], a[160:163], v[124:127], v[104:107]\n"
"buffer_load_dwordx4 v[96:99], v0, s[4:7], s98 offen offset:2048 sc1\n"
"v_mfma_f32_16x16x32_bf16 v[120:123], a[192:195], v[124:127], 0\n"
"ds_read_b128 v[148:151], v4 offset:1024\n"
"ds_read_b128 v[128:131], v5 offset:1024\n"
"v_mfma_f32_16x16x32_bf16 v[120:123], a[196:199], v[144:147], v[120:123]\n"
"s_waitcnt lgkmcnt(3)\n"
"v_add_f32_e32 v140, v140, v253\n"
"v_add_f32_e32 v141, v141, v252\n"
"v_mfma_f32_16x16x32_bf16 v[120:123], a[200:203], v[156:159], v[120:123]\n"
"buffer_load_dwordx4 v[100:103], v0, s[4:7], s98 offen offset:3072 sc1\n"
"v_mfma_f32_16x16x32_bf16 v[120:123], a[204:207], v[160:163], v[120:123]\n"
"v_add_f32_e32 v142, v142, v251\n"
"v_add_f32_e32 v143, v143, v250\n"
"v_mfma_f32_16x16x32_bf16 v[120:123], a[208:211], v[164:167], v[120:123]\n"
"v_add_f32_e32 v140, v140, v112\n"
"v_add_f32_e32 v141, v141, v113\n"
"v_mfma_f32_16x16x32_bf16 v[120:123], a[212:215], v[152:155], v[120:123]\n"
"ds_write_b128 v3, v[104:107] offset:13312\n"
"v_mfma_f32_16x16x32_bf16 v[120:123], a[216:219], v[116:119], v[120:123]\n"
"buffer_load_dwordx4 v[104:107], v1, s[4:7], s98 offen sc1\n"
"v_mfma_f32_16x16x32_bf16 v[120:123], a[220:223], v[136:139], v[120:123]\n"
"ds_read_b128 v[108:111], v4 offset:2048\n"
"ds_read_b128 v[200:203], v5 offset:2048\n"
"v_mfma_f32_16x16x32_bf16 v[132:135], a[252:255], v[136:139], 0\n"
"s_waitcnt lgkmcnt(3)\n"
"v_add_f32_e32 v142, v142, v114\n"
"v_add_f32_e32 v143, v143, v115\n"
"v_mfma_f32_16x16x32_bf16 v[132:135], a[248:251], v[116:119], v[132:135]\n"
"buffer_load_dwordx4 v[112:115], v1, s[4:7], s98 offen offset:1024 sc1\n"
"v_mfma_f32_16x16x32_bf16 v[132:135], a[244:247], v[152:155], v[132:135]\n"
"v_add_f32_e32 v148, v148, v249\n"
"v_add_f32_e32 v149, v149, v248\n"
"v_mfma_f32_16x16x32_bf16 v[132:135], a[240:243], v[164:167], v[132:135]\n"
"v_add_f32_e32 v150, v150, v247\n"
"v_add_f32_e32 v151, v151, v246\n"
"v_mfma_f32_16x16x32_bf16 v[132:135], a[236:239], v[160:163], v[132:135]\n"
"buffer_load_dwordx4 v[116:119], v1, s[4:7], s98 offen offset:2048 sc1\n"
"v_mfma_f32_16x16x32_bf16 v[132:135], a[232:235], v[156:159], v[132:135]\n"
"ds_write_b128 v3, v[120:123] offset:14336\n"
"v_mfma_f32_16x16x32_bf16 v[132:135], a[228:231], v[144:147], v[132:135]\n"
"ds_read_b128 v[120:123], v4 offset:3072\n"
"ds_read_b128 v[152:155], v5 offset:3072\n"
"v_mfma_f32_16x16x32_bf16 v[132:135], a[224:227], v[124:127], v[132:135]\n"
"s_waitcnt vmcnt(11)\n"
"buffer_load_dwordx4 v[124:127], v1, s[4:7], s98 offen offset:3072 sc1\n"
"; @A G2 a.3\n"
"v_mfma_f32_16x16x32_bf16 v[204:207], a[128:131], v[172:175], v[140:143]\n"
"s_add_u32 s98, s53, 0x440000\n"
"v_add_f32_e32 v148, v148, v128\n"
"v_add_f32_e32 v149, v149, v129\n"
"v_mfma_f32_16x16x32_bf16 v[204:207], a[132:135], v[168:171], v[204:207]\n"
"s_waitcnt lgkmcnt(3)\n"
"v_add_f32_e32 v150, v150, v130\n"
"v_add_f32_e32 v151, v151, v131\n"
"v_mfma_f32_16x16x32_bf16 v[204:207], a[136:139], v[176:179], v[204:207]\n"
"buffer_load_dwordx4 v[128:131], v0, s[4:7], s98 offen sc1\n"
"v_mfma_f32_16x16x32_bf16 v[204:207], a[140:143], v[180:183], v[204:207]\n"
"s_waitcnt vmcnt(9)\n"
"v_add_f32_e32 v108, v108, v245\n"
"v_add_f32_e32 v109, v109, v244\n"
"v_mfma_f32_16x16x32_bf16 v[204:207], a[144:147], v[184:187], v[204:207]\n"
"v_cmp_ne_u64_e32 vcc, v[254:255], v[18:19]\n"
"v_mfma_f32_16x16x32_bf16 v[204:207], a[148:151], v[188:191], v[204:207]\n"
"ds_write_b128 v3, v[132:135] offset:15360\n"
"v_mfma_f32_16x16x32_bf16 v[204:207], a[152:155], v[192:195], v[204:207]\n"
"s_cbranch_vccnz .Lmk_f13s\n"
".Lmk_f13r:\n"
"v_add_f32_e32 v110, v110, v243\n"
"v_mfma_f32_16x16x32_bf16 v[204:207], a[156:159], v[196:199], v[204:207]\n"
"buffer_load_dwordx4 v[132:135], v0, s[4:7], s98 offen offset:1024 sc1\n"
"v_mfma_f32_16x16x32_bf16 v[208:211], a[188:191], v[196:199], v[148:151]\n"
"v_add_f32_e32 v111, v111, v242\n"
"v_add_f32_e32 v108, v108, v200\n"
"v_mfma_f32_16x16x32_bf16 v[208:211], a[184:187], v[192:195], v[208:211]\n"
"s_waitcnt lgkmcnt(0)\n"
"s_barrier\n"
"v_mfma_f32_16x16x32_bf16 v[208:211], a[180:183], v[188:191], v[208:211]\n"
"buffer_load_dwordx4 v[136:139], v0, s[4:7], s98 offen offset:2048 sc1\n"
"v_mfma_f32_16x16x32_bf16 v[208:211], a[176:179], v[184:187], v[208:211]\n"
"v_add_f32_e32 v109, v109, v201\n"
"v_add_f32_e32 v110, v110, v202\n"
"v_mfma_f32_16x16x32_bf16 v[208:211], a[172:175], v[180:183], v[208:211]\n"
"v_cmp_u_f32_e32 vcc, v204, v204\n"
"v_add_f32_e32 v111, v111, v203\n"
"v_mfma_f32_16x16x32_bf16 v[208:211], a[168:171], v[176:179], v[208:211]\n"
"s_cbranch_vccnz .Lmk_p43s\n"
".Lmk_p43r:\n"
"v_add_f32_e32 v120, v120, v241\n"
"v_mfma_f32_16x16x32_bf16 v[208:211], a[164:167], v[168:171], v[208:211]\n"
"buffer_load_dwordx4 v[140:143], v0, s[4:7], s98 offen offset:3072 sc1\n"
"v_mfma_f32_16x16x32_bf16 v[208:211], a[160:163], v[172:175], v[208:211]\n"
"v_add_f32_e32 v121, v121, v240\n"
"v_add_f32_e32 v122, v122, v239\n"
"v_mfma_f32_16x16x32_bf16 v[108:111], a[192:195], v[172:175], v[108:111]\n"
"v_add_f32_e32 v123, v123, v238\n"
"v_add_f32_e32 v120, v120, v152\n"
"v_mfma_f32_16x16x32_bf16 v[108:111], a[196:199], v[168:171], v[108:111]\n"
"buffer_load_dwordx4 v[144:147], v1, s[4:7], s98 offen sc1\n"
"v_mfma_f32_16x16x32_bf16 v[108:111], a[200:203], v[176:179], v[108:111]\n"
"v_add_f32_e32 v121, v121, v153\n"
"v_add_f32_e32 v122, v122, v154\n"
"v_mfma_f32_16x16x32_bf16 v[108:111], a[204:207], v[180:183], v[108:111]\n"
"ds_read_b128 v[200:203], v6\n"
"v_add_f32_e32 v123, v123, v155\n"
"v_mfma_f32_16x16x32_bf16 v[108:111], a[208:211], v[184:187], v[108:111]\n"
"buffer_load_dwordx4 v[148:151], v1, s[4:7], s98 offen offset:1024 sc1\n"
"v_mfma_f32_16x16x32_bf16 v[108:111], a[212:215], v[188:191], v[108:111]\n"
"ds_read_b128 v[212:215], v6 offset:1024\n"
"ds_read_b128 v[152:155], v6 offset:2048\n"
"v_mfma_f32_16x16x32_bf16 v[108:111], a[216:219], v[192:195], v[108:111]\n"
"ds_read_b128 v[156:159], v6 offset:3072\n"
"v_mfma_f32_16x16x32_bf16 v[108:111], a[220:223], v[196:199], v[108:111]\n"
"buffer_load_dwordx4 v[160:163], v1, s[4:7], s98 offen offset:2048 sc1\n"
"v_mfma_f32_16x16x32_bf16 v[120:123], a[252:255], v[196:199], v[120:123]\n"
"v_mfma_f32_16x16x32_bf16 v[120:123], a[248:251], v[192:195], v[120:123]\n"
"v_mfma_f32_16x16x32_bf16 v[120:123], a[244:247], v[188:191], v[120:123]\n"
"buffer_load_dwordx4 v[164:167], v1, s[4:7], s98 offen offset:3072 sc1\n"
"v_mfma_f32_16x16x32_bf16 v[120:123], a[240:243], v[184:187], v[120:123]\n"
"s_waitcnt lgkmcnt(0)\n"
"v_add_f32_e32 v200, v204, v200\n"
"v_add_f32_e32 v201, v205, v201\n"
"v_mfma_f32_16x16x32_bf16 v[120:123], a[236:239], v[180:183], v[120:123]\n"
"v_add_f32_e32 v202, v206, v202\n"
"v_add_f32_e32 v203, v207, v203\n"
"v_mfma_f32_16x16x32_bf16 v[120:123], a[232:235], v[176:179], v[120:123]\n"
"v_add_f32_e32 v212, v208, v212\n"
"v_add_f32_e32 v213, v209, v213\n"
"v_mfma_f32_16x16x32_bf16 v[120:123], a[228:231], v[168:171], v[120:123]\n"
"v_add_f32_e32 v214, v210, v214\n"
"v_add_f32_e32 v215, v211, v215\n"
"v_mfma_f32_16x16x32_bf16 v[120:123], a[224:227], v[172:175], v[120:123]\n"
"s_add_u32 s98, s61, 0x400000\n"
"v_cvt_pk_bf16_f32 v168, v200, v201\n"
"v_cvt_pk_bf16_f32 v169, v202, v203\n"
"v_cvt_pk_bf16_f32 v170, v212, v213\n"
"v_cvt_pk_bf16_f32 v171, v214, v215\n"
"buffer_store_dwordx4 v[168:171], v0, s[4:7], s98 offen sc1\n"
"ds_write_b128 v2, v[168:171]\n"
".Lmk_loop:\n"
".Lmk_b0:\n"
"s_waitcnt vmcnt(13)\n"
"; @B G1 0.0\n"
"v_mfma_f32_16x16x32_bf16 v[172:175], a[0:3], v[88:91], 0\n"
"v_mfma_f32_16x16x32_bf16 v[172:175], a[4:7], v[92:95], v[172:175]\n"
"s_add_u32 s98, s62, 0x44000\n"
"v_mfma_f32_16x16x32_bf16 v[172:175], a[8:11], v[96:99], v[172:175]\n"
"s_add_u32 s97, s54, 0x440000\n"
"buffer_load_dwordx4 v[176:179], v0, s[4:7], s97 offen sc1\n"
"v_mfma_f32_16x16x32_bf16 v[172:175], a[12:15], v[100:103], v[172:175]\n"
"s_waitcnt vmcnt(10)\n"
"v_mfma_f32_16x16x32_bf16 v[172:175], a[16:19], v[104:107], v[172:175]\n"
"buffer_load_dword v255, v10, s[4:7], s98 offen sc1\n"
"v_mfma_f32_16x16x32_bf16 v[172:175], a[20:23], v[112:115], v[172:175]\n"
"v_mov_b32_e32 v180, 0x3f803f80\n"
"v_mov_b32_e32 v181, 0x3f803f80\n"
"v_mfma_f32_16x16x32_bf16 v[172:175], a[24:27], v[116:119], v[172:175]\n"
"v_mov_b32_e32 v182, 0x3f803f80\n"
"v_mov_b32_e32 v183, 0x3f803f80\n"
"v_mfma_f32_16x16x32_bf16 v[172:175], a[28:31], v[124:127], v[172:175]\n"
"buffer_load_dwordx4 v[184:187], v0, s[4:7], s97 offen offset:1024 sc1\n"
"v_mfma_f32_16x16x32_bf16 v[188:191], a[60:63], v[124:127], 0\n"
"buffer_load_dword v254, v10, s[4:7], s98 offen offset:256 sc1\n"
"v_mfma_f32_16x16x32_bf16 v[188:191], a[56:59], v[116:119], v[188:191]\n"
"v_add_f32_e32 v152, v108, v152\n"
"v_add_f32_e32 v153, v109, v153\n"
"v_mfma_f32_16x16x32_bf16 v[188:191], a[52:55], v[112:115], v[188:191]\n"
"v_add_f32_e32 v154, v110, v154\n"
"v_add_f32_e32 v155, v111, v155\n"
"v_mfma_f32_16x16x32_bf16 v[188:191], a[48:51], v[104:107], v[188:191]\n"
"buffer_load_dword v253, v10, s[4:7], s98 offen offset:512 sc1\n"
"v_mfma_f32_16x16x32_bf16 v[188:191], a[44:47], v[100:103], v[188:191]\n"
"v_cmp_u_f32_e32 vcc, v172, v172\n"
"v_add_f32_e32 v156, v120, v156\n"
"v_mfma_f32_16x16x32_bf16 v[188:191], a[40:43], v[96:99], v[188:191]\n"
"s_cbranch_vccnz .Lmk_p48s\n"
".Lmk_p48r:\n"
"v_add_f32_e32 v157, v121, v157\n"
"v_mfma_f32_16x16x32_bf16 v[188:191], a[36:39], v[92:95], v[188:191]\n"
"buffer_load_dwordx4 v[108:111], v0, s[4:7], s97 offen offset:2048 sc1\n"
"v_mfma_f32_16x16x32_bf16 v[188:191], a[32:35], v[88:91], v[188:191]\n"
"ds_write_b128 v3, v[172:175]\n"
"v_mfma_f32_16x16x32_bf16 v[172:175], a[64:67], v[88:91], 0\n"
"buffer_load_dword v252, v10, s[4:7], s98 offen offset:768 sc1\n"
"v_mfma_f32_16x16x32_bf16 v[172:175], a[68:71], v[92:95], v[172:175]\n"
"buffer_load_dwordx4 v[192:195], v0, s[4:7], s97 offen offset:3072 sc1\n"
"v_mfma_f32_16x16x32_bf16 v[172:175], a[72:75], v[96:99], v[172:175]\n"
"v_add_f32_e32 v158, v122, v158\n"
"v_add_f32_e32 v159, v123, v159\n"
"v_mfma_f32_16x16x32_bf16 v[172:175], a[76:79], v[100:103], v[172:175]\n"
"buffer_load_dword v251, v10, s[4:7], s98 offen offset:1024 sc1\n"
"v_mfma_f32_16x16x32_bf16 v[172:175], a[80:83], v[104:107], v[172:175]\n"
"v_cvt_pk_bf16_f32 v120, v152, v153\n"
"v_cvt_pk_bf16_f32 v121, v154, v155\n"
"v_mfma_f32_16x16x32_bf16 v[172:175], a[84:87], v[112:115], v[172:175]\n"
"ds_write_b128 v3, v[188:191] offset:1024\n"
"v_mfma_f32_16x16x32_bf16 v[172:175], a[88:91], v[116:119], v[172:175]\n"
"buffer_load_dwordx4 v[152:155], v1, s[4:7], s97 offen sc1\n"
"v_mfma_f32_16x16x32_bf16 v[172:175], a[92:95], v[124:127], v[172:175]\n"
"buffer_load_dword v250, v10, s[4:7], s98 offen offset:1280 sc1\n"
"v_mfma_f32_16x16x32_bf16 v[188:191], a[124:127], v[124:127], 0\n"
"s_add_u32 s96, s61, 0x400000\n"
"v_cvt_pk_bf16_f32 v122, v156, v157\n"
"v_cvt_pk_bf16_f32 v123, v158, v159\n"
"v_mfma_f32_16x16x32_bf16 v[188:191], a[120:123], v[116:119], v[188:191]\n"
"buffer_store_dwordx4 v[120:123], v0, s[4:7], s96 offen offset:1024 sc1\n"
"v_mfma_f32_16x16x32_bf16 v[188:191], a[116:119], v[112:115], v[188:191]\n"
"buffer_load_dword v249, v10, s[4:7], s98 offen offset:1536 sc1\n"
"v_mfma_f32_16x16x32_bf16 v[188:191], a[112:115], v[104:107], v[188:191]\n"
"buffer_load_dwordx4 v[156:159], v1, s[4:7], s97 offen offset:1024 sc1\n"
"v_mfma_f32_16x16x32_bf16 v[188:191], a[108:111], v[100:103], v[188:191]\n"
"ds_write_b128 v2, v[120:123] offset:1024\n"
"v_mfma_f32_16x16x32_bf16 v[196:199], v[180:183], v[168:171], 0\n"
"v_mfma_f32_16x16x32_bf16 v[200:203], v[168:171], v[168:171], 0\n"
"v_mfma_f32_16x16x32_bf16 v[196:199], v[180:183], v[120:123], v[196:199]\n"
"v_mfma_f32_16x16x32_bf16 v[200:203], v[120:123], v[120:123], v[200:203]\n"
"v_mfma_f32_16x16x32_bf16 v[188:191], a[104:107], v[96:99], v[188:191]\n"
"ds_write_b128 v3, v[172:175] offset:2048\n"
"v_mfma_f32_16x16x32_bf16 v[188:191], a[100:103], v[92:95], v[188:191]\n"
"s_mov_b32 s94, 0xaaaaaaaa\n"
"v_mov_b32_e32 v120, -1\n"
"v_mov_b32_e32 v121, -1\n"
"v_mfma_f32_16x16x32_bf16 v[188:191], a[96:99], v[88:91], v[188:191]\n"
"v_mov_b32_e32 v122, -1\n"
"v_mov_b32_e32 v123, -1\n"
"v_mfma_f32_16x16x32_bf16 v[168:171], v[24:27], v[88:91], 0\n"
"buffer_load_dwordx4 v[172:175], v1, s[4:7], s97 offen offset:2048 sc1\n"
"v_mfma_f32_16x16x32_bf16 v[168:171], v[28:31], v[92:95], v[168:171]\n"
"s_add_u32 s96, s60, 0x448000\n"
"buffer_store_dwordx4 v[120:123], v0, s[4:7], s96 offen sc1\n"
"v_mfma_f32_16x16x32_bf16 v[168:171], v[32:35], v[96:99], v[168:171]\n"
"buffer_load_dword v248, v10, s[4:7], s98 offen offset:1792 sc1\n"
"v_mfma_f32_16x16x32_bf16 v[168:171], v[36:39], v[100:103], v[168:171]\n"
"s_mov_b32 s95, 0xaaaaaaaa\n"
"v_mfma_f32_16x16x32_bf16 v[168:171], v[40:43], v[104:107], v[168:171]\n"
"v_mfma_f32_16x16x32_bf16 v[168:171], v[44:47], v[112:115], v[168:171]\n"
"buffer_load_dwordx4 v[180:183], v1, s[4:7], s97 offen offset:3072 sc1\n"
"v_mfma_f32_16x16x32_bf16 v[168:171], v[48:51], v[116:119], v[168:171]\n"
"ds_write_b128 v3, v[188:191] offset:3072\n"
"v_mfma_f32_16x16x32_bf16 v[168:171], v[52:55], v[124:127], v[168:171]\n"
"v_cndmask_b32_e64 v200, v200, v201, s[94:95]\n"
"v_cndmask_b32_e64 v202, v202, v203, s[94:95]\n"
"v_mfma_f32_16x16x32_bf16 v[188:191], v[84:87], v[124:127], 0\n"
"s_mov_b32 s94, 0xcccccccc\n"
"s_mov_b32 s95, 0xcccccccc\n"
"v_mfma_f32_16x16x32_bf16 v[188:191], v[80:83], v[116:119], v[188:191]\n"
"v_cndmask_b32_e64 v197, v200, v202, s[94:95]\n"
"v_mfma_f32_16x16x32_bf16 v[188:191], v[76:79], v[112:115], v[188:191]\n"
"s_mov_b32 s94, 0xf0000f\n"
"s_mov_b32 s95, 0xf0000f00\n"
"v_mfma_f32_16x16x32_bf16 v[188:191], v[72:75], v[104:107], v[188:191]\n"
"s_add_u32 s98, s63, 0x40000\n"
"v_cndmask_b32_e64 v196, v196, v197, s[94:95]\n"
"v_mfma_f32_16x16x32_bf16 v[188:191], v[68:71], v[100:103], v[188:191]\n"
"s_add_u32 s97, s70, 0x200000\n"
"buffer_load_dwordx4 a[128:131], v0, s[4:7], s97 offen sc1\n"
"v_mfma_f32_16x16x32_bf16 v[188:191], v[64:67], v[96:99], v[188:191]\n"
"ds_write_b128 v3, v[168:171] offset:4096\n"
"v_mfma_f32_16x16x32_bf16 v[188:191], v[60:63], v[92:95], v[188:191]\n"
"buffer_store_dword v196, v14, s[4:7], s98 offen sc1\n"
"v_mfma_f32_16x16x32_bf16 v[188:191], v[56:59], v[88:91], v[188:191]\n"
"s_waitcnt vmcnt(25)\n"
"; @B G1 0.1\n"
"v_mfma_f32_16x16x32_bf16 v[88:91], a[0:3], v[128:131], 0\n"
"v_mfma_f32_16x16x32_bf16 v[88:91], a[4:7], v[132:135], v[88:91]\n"
"v_mfma_f32_16x16x32_bf16 v[88:91], a[8:11], v[136:139], v[88:91]\n"
"s_add_u32 s98, s55, 0x440000\n"
"buffer_load_dwordx4 v[92:95], v0, s[4:7], s98 offen sc1\n"
"v_mfma_f32_16x16x32_bf16 v[88:91], a[12:15], v[140:143], v[88:91]\n"
"s_waitcnt vmcnt(22)\n"
"v_mfma_f32_16x16x32_bf16 v[88:91], a[16:19], v[144:147], v[88:91]\n"
"v_mfma_f32_16x16x32_bf16 v[88:91], a[20:23], v[148:151], v[88:91]\n"
"ds_write_b128 v3, v[188:191] offset:5120\n"
"v_mfma_f32_16x16x32_bf16 v[88:91], a[24:27], v[160:163], v[88:91]\n"
"v_mfma_f32_16x16x32_bf16 v[88:91], a[28:31], v[164:167], v[88:91]\n"
"buffer_load_dwordx4 v[96:99], v0, s[4:7], s98 offen offset:1024 sc1\n"
"v_mfma_f32_16x16x32_bf16 v[100:103], a[60:63], v[164:167], 0\n"
"v_mfma_f32_16x16x32_bf16 v[100:103], a[56:59], v[160:163], v[100:103]\n"
"v_mfma_f32_16x16x32_bf16 v[100:103], a[52:55], v[148:151], v[100:103]\n"
"v_mfma_f32_16x16x32_bf16 v[100:103], a[48:51], v[144:147], v[100:103]\n"
"v_mfma_f32_16x16x32_bf16 v[100:103], a[44:47], v[140:143], v[100:103]\n"
"s_nop 1\n"
"v_cmp_u_f32_e32 vcc, v88, v88\n"
"v_mfma_f32_16x16x32_bf16 v[100:103], a[40:43], v[136:139], v[100:103]\n"
"s_cbranch_vccnz .Lmk_p49s\n"
".Lmk_p49r:\n"
"v_mfma_f32_16x16x32_bf16 v[100:103], a[36:39], v[132:135], v[100:103]\n"
"buffer_load_dwordx4 v[104:107], v0, s[4:7], s98 offen offset:2048 sc1\n"
"v_mfma_f32_16x16x32_bf16 v[100:103], a[32:35], v[128:131], v[100:103]\n"
"ds_write_b128 v3, v[88:91] offset:6144\n"
"v_mfma_f32_16x16x32_bf16 v[88:91], a[64:67], v[128:131], 0\n"
"buffer_load_dwordx4 a[132:135], v0, s[4:7], s97 offen offset:1024 sc1\n"
"v_mfma_f32_16x16x32_bf16 v[88:91], a[68:71], v[132:135], v[88:91]\n"
"buffer_load_dwordx4 v[112:115], v0, s[4:7], s98 offen offset:3072 sc1\n"
"v_mfma_f32_16x16x32_bf16 v[88:91], a[72:75], v[136:139], v[88:91]\n"
"v_mfma_f32_16x16x32_bf16 v[88:91], a[76:79], v[140:143], v[88:91]\n"
"v_mfma_f32_16x16x32_bf16 v[88:91], a[80:83], v[144:147], v[88:91]\n"
"v_mfma_f32_16x16x32_bf16 v[88:91], a[84:87], v[148:151], v[88:91]\n"
"ds_write_b128 v3, v[100:103] offset:7168\n"
"v_mfma_f32_16x16x32_bf16 v[88:91], a[88:91], v[160:163], v[88:91]\n"
"buffer_load_dwordx4 v[100:103], v1, s[4:7], s98 offen sc1\n"
"v_mfma_f32_16x16x32_bf16 v[88:91], a[92:95], v[164:167], v[88:91]\n"
"v_mfma_f32_16x16x32_bf16 v[116:119], a[124:127], v[164:167], 0\n"
"v_mfma_f32_16x16x32_bf16 v[116:119], a[120:123], v[160:163], v[116:119]\n"
"v_mfma_f32_16x16x32_bf16 v[116:119], a[116:119], v[148:151], v[116:119]\n"
"v_mfma_f32_16x16x32_bf16 v[116:119], a[112:115], v[144:147], v[116:119]\n"
"buffer_load_dwordx4 v[124:127], v1, s[4:7], s98 offen offset:1024 sc1\n"
"v_mfma_f32_16x16x32_bf16 v[116:119], a[108:111], v[140:143], v[116:119]\n"
"v_mfma_f32_16x16x32_bf16 v[116:119], a[104:107], v[136:139], v[116:119]\n"
"s_nop 0\n"
"ds_write_b128 v3, v[88:91] offset:8192\n"
"v_mfma_f32_16x16x32_bf16 v[116:119], a[100:103], v[132:135], v[116:119]\n"
"v_mfma_f32_16x16x32_bf16 v[116:119], a[96:99], v[128:131], v[116:119]\n"
"v_mfma_f32_16x16x32_bf16 v[88:91], v[24:27], v[128:131], 0\n"
"buffer_load_dwordx4 v[168:171], v1, s[4:7], s98 offen offset:2048 sc1\n"
"v_mfma_f32_16x16x32_bf16 v[88:91], v[28:31], v[132:135], v[88:91]\n"
"s_add_u32 s96, s60, 0x440000\n"
"v_mfma_f32_16x16x32_bf16 v[88:91], v[32:35], v[136:139], v[88:91]\n"
"v_mfma_f32_16x16x32_bf16 v[88:91], v[36:39], v[140:143], v[88:91]\n"
"buffer_store_dwordx4 v[120:123], v0, s[4:7], s96 offen sc1\n"
"v_mfma_f32_16x16x32_bf16 v[88:91], v[40:43], v[144:147], v[88:91]\n"
"buffer_load_dwordx4 a[136:139], v0, s[4:7], s97 offen offset:2048 sc1\n"
"v_mfma_f32_16x16x32_bf16 v[88:91], v[44:47], v[148:151], v[88:91]\n"
"buffer_load_dwordx4 v[188:191], v1, s[4:7], s98 offen offset:3072 sc1\n"
"v_mfma_f32_16x16x32_bf16 v[88:91], v[48:51], v[160:163], v[88:91]\n"
"ds_write_b128 v3, v[116:119] offset:9216\n"
"v_mfma_f32_16x16x32_bf16 v[88:91], v[52:55], v[164:167], v[88:91]\n"
"s_waitcnt vmcnt(14)\n"
"v_add_f32_e32 v255, v255, v251\n"
"v_add_f32_e32 v254, v254, v250\n"
"v_mfma_f32_16x16x32_bf16 v[116:119], v[84:87], v[164:167], 0\n"
"v_add_f32_e32 v253, v253, v249\n"
"v_add_f32_e32 v252, v252, v248\n"
"v_mfma_f32_16x16x32_bf16 v[116:119], v[80:83], v[160:163], v[116:119]\n"
"v_add_f32_e32 v255, v255, v253\n"
"v_add_f32_e32 v254, v254, v252\n"
"v_mfma_f32_16x16x32_bf16 v[116:119], v[76:79], v[148:151], v[116:119]\n"
"v_add_f32_e32 v255, v255, v254\n"
"v_cmp_u_f32_e32 vcc, v255, v255\n"
"v_mfma_f32_16x16x32_bf16 v[116:119], v[72:75], v[144:147], v[116:119]\n"
"s_cbranch_vccnz .Lmk_m52s\n"
".Lmk_m52r:\n"
"v_mov_b32_e32 v254, v255\n"
"v_mfma_f32_16x16x32_bf16 v[116:119], v[68:71], v[140:143], v[116:119]\n"
"s_nop 0\n"
"v_permlane32_swap_b32_e32 v255, v254\n"
"v_add_f32_e32 v255, v255, v254\n"
"v_mfma_f32_16x16x32_bf16 v[116:119], v[64:67], v[136:139], v[116:119]\n"
"ds_write_b128 v3, v[88:91] offset:10240\n"
"v_mfma_f32_16x16x32_bf16 v[116:119], v[60:63], v[132:135], v[116:119]\n"
"v_mul_f32_e32 v255, 0x3a800000, v255\n"
"v_mov_b32_e32 v254, v255\n"
"v_mfma_f32_16x16x32_bf16 v[116:119], v[56:59], v[128:131], v[116:119]\n"
"s_nop 0\n"
"v_permlane16_swap_b32_e32 v255, v254\n"
"v_cvt_pk_bf16_f32 v253, -v255, 0\n"
"; @B G1 0.2\n"
"v_mfma_f32_16x16x32_bf16 v[88:91], a[0:3], v[176:179], 0\n"
"v_fma_f32 v254, -v255, v255, v254\n"
"v_lshlrev_b32_e32 v253, 16, v253\n"
"v_mfma_f32_16x16x32_bf16 v[88:91], a[4:7], v[184:187], v[88:91]\n"
"v_max_f32_e32 v254, 0, v254\n"
"v_cvt_pk_bf16_f32 v254, v254, 0\n"
"v_mfma_f32_16x16x32_bf16 v[88:91], a[8:11], v[108:111], v[88:91]\n"
"v_lshlrev_b32_e32 v254, 16, v254\n"
"v_add_f32_e32 v254, 0x3727c5ac, v254\n"
"v_mfma_f32_16x16x32_bf16 v[88:91], a[12:15], v[192:195], v[88:91]\n"
"s_waitcnt vmcnt(13)\n"
"v_cvt_pk_bf16_f32 v254, v254, 0\n"
"v_lshlrev_b32_e32 v254, 16, v254\n"
"v_mfma_f32_16x16x32_bf16 v[88:91], a[16:19], v[152:155], v[88:91]\n"
"s_mul_i32 s98, s0, .Lmk_exit-.Lmk_b2\n"
"s_add_u32 s98, s98, .Lmk_b2-.Lmk_g47\n"
"s_getpc_b64 s[84:85]\n"
".Lmk_g47:\n"
"s_add_u32 s84, s84, s98\n"
"s_addc_u32 s85, s85, 0\n"
"v_mfma_f32_16x16x32_bf16 v[88:91], a[20:23], v[156:159], v[88:91]\n"
"ds_write_b128 v3, v[116:119] offset:11264\n"
"v_mfma_f32_16x16x32_bf16 v[88:91], a[24:27], v[172:175], v[88:91]\n"
"s_and_b32 s84, s84, -16\n"
"s_and_b32 s85, s85, 0xffff\n"
"s_sub_u32 s98, .Lmk_end-.Lmk_g47, s98\n"
"s_min_u32 s86, s98, 0x4000\n"
"s_mov_b32 s87, 0x20000\n"
"v_mfma_f32_16x16x32_bf16 v[88:91], a[28:31], v[180:183], v[88:91]\n"
"s_mov_b32 m0, 0x27800\n"
"s_nop 0\n"
"buffer_load_dwordx4 v0, s[84:87], s99 offen lds\n"
"v_mfma_f32_16x16x32_bf16 v[116:119], a[60:63], v[180:183], 0\n"
"buffer_load_dwordx4 a[140:143], v0, s[4:7], s97 offen offset:3072 sc1\n"
"v_mfma_f32_16x16x32_bf16 v[116:119], a[56:59], v[172:175], v[116:119]\n"
"v_sqrt_f32_e32 v254, v254\n"
"v_mfma_f32_16x16x32_bf16 v[116:119], a[52:55], v[156:159], v[116:119]\n"
"s_add_u32 s98, s52, 0x448000\n"
"buffer_load_dwordx4 v[128:131], v0, s[4:7], s98 offen sc1\n"
"v_mfma_f32_16x16x32_bf16 v[116:119], a[48:51], v[152:155], v[116:119]\n"
"s_waitcnt lgkmcnt(0)\n"
"s_barrier\n"
"v_mfma_f32_16x16x32_bf16 v[116:119], a[44:47], v[192:195], v[116:119]\n"
"v_cmp_u_f32_e32 vcc, v88, v88\n"
"v_cvt_pk_bf16_f32 v254, v254, 0\n"
"v_mfma_f32_16x16x32_bf16 v[116:119], a[40:43], v[108:111], v[116:119]\n"
"s_cbranch_vccnz .Lmk_p50s\n"
".Lmk_p50r:\n"
"v_lshlrev_b32_e32 v254, 16, v254\n"
"v_mfma_f32_16x16x32_bf16 v[116:119], a[36:39], v[184:187], v[116:119]\n"
"ds_write_b128 v3, v[88:91] offset:12288\n"
"v_mfma_f32_16x16x32_bf16 v[116:119], a[32:35], v[176:179], v[116:119]\n"
"buffer_load_dwordx4 v[88:91], v0, s[4:7], s98 offen offset:1024 sc1\n"
"v_mfma_f32_16x16x32_bf16 v[132:135], a[64:67], v[176:179], 0\n"
"v_rcp_f32_e32 v255, v254\n"
"v_mfma_f32_16x16x32_bf16 v[132:135], a[68:71], v[184:187], v[132:135]\n"
"ds_read_b128 v[136:139], v4\n"
"v_mul_f32_e32 v254, 0x3e8293ee, v255\n"
"v_mfma_f32_16x16x32_bf16 v[132:135], a[72:75], v[108:111], v[132:135]\n"
"ds_read_b128 v[140:143], v9 offset:640\n"
"ds_read_b128 v[144:147], v5\n"
"v_mfma_f32_16x16x32_bf16 v[132:135], a[76:79], v[192:195], v[132:135]\n"
"ds_read_b128 v[148:151], v4 offset:1024\n"
"ds_read_b128 v[160:163], v9 offset:704\n"
"v_mfma_f32_16x16x32_bf16 v[132:135], a[80:83], v[152:155], v[132:135]\n"
"buffer_load_dwordx4 v[164:167], v0, s[4:7], s98 offen offset:2048 sc1\n"
"v_mfma_f32_16x16x32_bf16 v[132:135], a[84:87], v[156:159], v[132:135]\n"
"ds_write_b128 v3, v[116:119] offset:13312\n"
"v_mfma_f32_16x16x32_bf16 v[132:135], a[88:91], v[172:175], v[132:135]\n"
"ds_read_b128 v[116:119], v5 offset:1024\n"
"v_mfma_f32_16x16x32_bf16 v[132:135], a[92:95], v[180:183], v[132:135]\n"
"s_waitcnt lgkmcnt(4)\n"
"v_fmac_f32_e32 v136, v253, v140\n"
"v_fmac_f32_e32 v137, v253, v141\n"
"v_mfma_f32_16x16x32_bf16 v[196:199], a[124:127], v[180:183], 0\n"
"v_fmac_f32_e32 v138, v253, v142\n"
"v_fmac_f32_e32 v139, v253, v143\n"
"v_mfma_f32_16x16x32_bf16 v[196:199], a[120:123], v[172:175], v[196:199]\n"
"buffer_load_dwordx4 v[140:143], v0, s[4:7], s98 offen offset:3072 sc1\n"
"v_mfma_f32_16x16x32_bf16 v[196:199], a[116:119], v[156:159], v[196:199]\n"
"ds_read_b128 v[200:203], v4 offset:2048\n"
"ds_read_b128 v[204:207], v9 offset:768\n"
"v_mfma_f32_16x16x32_bf16 v[196:199], a[112:115], v[152:155], v[196:199]\n"
"ds_read_b128 v[208:211], v5 offset:2048\n"
"v_add_f32_e32 v136, v136, v144\n"
"v_mfma_f32_16x16x32_bf16 v[196:199], a[108:111], v[192:195], v[196:199]\n"
"buffer_load_dwordx4 a[144:147], v1, s[4:7], s97 offen sc1\n"
"v_mfma_f32_16x16x32_bf16 v[196:199], a[104:107], v[108:111], v[196:199]\n"
"ds_write_b128 v3, v[132:135] offset:14336\n"
"v_mfma_f32_16x16x32_bf16 v[196:199], a[100:103], v[184:187], v[196:199]\n"
"buffer_load_dwordx4 v[132:135], v1, s[4:7], s98 offen sc1\n"
"v_mfma_f32_16x16x32_bf16 v[196:199], a[96:99], v[176:179], v[196:199]\n"
"v_add_f32_e32 v137, v137, v145\n"
"v_add_f32_e32 v138, v138, v146\n"
"v_mfma_f32_16x16x32_bf16 v[212:215], v[24:27], v[176:179], 0\n"
"s_waitcnt lgkmcnt(4)\n"
"v_add_f32_e32 v139, v139, v147\n"
"v_fmac_f32_e32 v148, v253, v160\n"
"v_mfma_f32_16x16x32_bf16 v[212:215], v[28:31], v[184:187], v[212:215]\n"
"ds_read_b128 v[144:147], v4 offset:3072\n"
"ds_read_b128 v[216:219], v9 offset:832\n"
"v_mfma_f32_16x16x32_bf16 v[212:215], v[32:35], v[108:111], v[212:215]\n"
"ds_read_b128 v[220:223], v5 offset:3072\n"
"v_fmac_f32_e32 v149, v253, v161\n"
"v_mfma_f32_16x16x32_bf16 v[212:215], v[36:39], v[192:195], v[212:215]\n"
"buffer_load_dwordx4 v[224:227], v1, s[4:7], s98 offen offset:1024 sc1\n"
"v_mfma_f32_16x16x32_bf16 v[212:215], v[40:43], v[152:155], v[212:215]\n"
"s_add_u32 s96, s60, 0x400000\n"
"v_fmac_f32_e32 v150, v253, v162\n"
"v_fmac_f32_e32 v151, v253, v163\n"
"v_mfma_f32_16x16x32_bf16 v[212:215], v[44:47], v[156:159], v[212:215]\n"
"ds_write_b128 v3, v[196:199] offset:15360\n"
"v_mfma_f32_16x16x32_bf16 v[212:215], v[48:51], v[172:175], v[212:215]\n"
"buffer_store_dwordx4 v[120:123], v0, s[4:7], s96 offen sc1\n"
"v_mfma_f32_16x16x32_bf16 v[212:215], v[52:55], v[180:183], v[212:215]\n"
"v_add_f32_e32 v148, v148, v116\n"
"v_add_f32_e32 v149, v149, v117\n"
"v_mfma_f32_16x16x32_bf16 v[160:163], v[84:87], v[180:183], 0\n"
"buffer_load_dwordx4 v[180:183], v1, s[4:7], s98 offen offset:2048 sc1\n"
"v_mfma_f32_16x16x32_bf16 v[160:163], v[80:83], v[172:175], v[160:163]\n"
"s_add_u32 s96, s68, s80\n"
"s_add_u32 s96, s96, 0x10000\n"
"s_waitcnt vmcnt(22)\n"
"v_mfma_f32_16x16x32_bf16 v[160:163], v[76:79], v[156:159], v[160:163]\n"
"buffer_store_dwordx2 v[18:19], v11, s[4:7], s96 offen sc1\n"
"v_mfma_f32_16x16x32_bf16 v[160:163], v[72:75], v[152:155], v[160:163]\n"
"ds_read_b128 v[152:155], v4 offset:4096\n"
"ds_read_b128 v[156:159], v9 offset:512\n"
"v_mfma_f32_16x16x32_bf16 v[160:163], v[68:71], v[192:195], v[160:163]\n"
"ds_read_b128 v[172:175], v5 offset:4096\n"
"v_add_f32_e32 v150, v150, v118\n"
"v_mfma_f32_16x16x32_bf16 v[160:163], v[64:67], v[108:111], v[160:163]\n"
"buffer_load_dwordx4 v[108:111], v1, s[4:7], s98 offen offset:3072 sc1\n"
"v_mfma_f32_16x16x32_bf16 v[160:163], v[60:63], v[184:187], v[160:163]\n"
"ds_write_b128 v3, v[212:215] offset:16384\n"
"v_mfma_f32_16x16x32_bf16 v[160:163], v[56:59], v[176:179], v[160:163]\n"
"s_waitcnt vmcnt(19)\n"
"ds_read_b128 v[176:179], v4 offset:5120\n"
"ds_read_b128 v[184:187], v9 offset:576\n"
"; @B G1 0.3\n"
"v_mfma_f32_16x16x32_bf16 v[192:195], a[0:3], v[92:95], v[136:139]\n"
"buffer_load_dwordx4 a[148:151], v1, s[4:7], s97 offen offset:1024 sc1\n"
"v_mfma_f32_16x16x32_bf16 v[192:195], a[4:7], v[96:99], v[192:195]\n"
"ds_read_b128 v[196:199], v5 offset:5120\n"
"v_add_f32_e32 v151, v151, v119\n"
"v_mfma_f32_16x16x32_bf16 v[192:195], a[8:11], v[104:107], v[192:195]\n"
"s_add_u32 s98, s53, 0x448000\n"
"buffer_load_dwordx4 v[116:119], v0, s[4:7], s98 offen sc1\n"
"v_mfma_f32_16x16x32_bf16 v[192:195], a[12:15], v[112:115], v[192:195]\n"
"s_waitcnt vmcnt(15)\n"
"s_waitcnt lgkmcnt(12)\n"
"v_fmac_f32_e32 v200, v253, v204\n"
"v_mfma_f32_16x16x32_bf16 v[192:195], a[16:19], v[100:103], v[192:195]\n"
"v_fmac_f32_e32 v201, v253, v205\n"
"v_fmac_f32_e32 v202, v253, v206\n"
"v_mfma_f32_16x16x32_bf16 v[192:195], a[20:23], v[124:127], v[192:195]\n"
"ds_write_b128 v3, v[160:163] offset:17408\n"
"v_mfma_f32_16x16x32_bf16 v[192:195], a[24:27], v[168:171], v[192:195]\n"
"v_fmac_f32_e32 v203, v253, v207\n"
"v_add_f32_e32 v200, v200, v208\n"
"v_mfma_f32_16x16x32_bf16 v[192:195], a[28:31], v[188:191], v[192:195]\n"
"buffer_load_dwordx4 v[160:163], v0, s[4:7], s98 offen offset:1024 sc1\n"
"v_mfma_f32_16x16x32_bf16 v[204:207], a[60:63], v[188:191], v[148:151]\n"
"v_add_f32_e32 v201, v201, v209\n"
"v_add_f32_e32 v202, v202, v210\n"
"v_mfma_f32_16x16x32_bf16 v[204:207], a[56:59], v[168:171], v[204:207]\n"
"s_waitcnt lgkmcnt(9)\n"
"v_add_f32_e32 v203, v203, v211\n"
"v_fmac_f32_e32 v144, v253, v216\n"
"v_mfma_f32_16x16x32_bf16 v[204:207], a[52:55], v[124:127], v[204:207]\n"
"s_waitcnt lgkmcnt(0)\n"
"s_barrier\n"
"v_mfma_f32_16x16x32_bf16 v[204:207], a[48:51], v[100:103], v[204:207]\n"
"ds_read_b128 v[208:211], v6\n"
"ds_read_b128 v[212:215], v6 offset:1024\n"
"v_mfma_f32_16x16x32_bf16 v[204:207], a[44:47], v[112:115], v[204:207]\n"
"v_cmp_u_f32_e32 vcc, v192, v192\n"
"v_fmac_f32_e32 v145, v253, v217\n"
"v_mfma_f32_16x16x32_bf16 v[204:207], a[40:43], v[104:107], v[204:207]\n"
"s_cbranch_vccnz .Lmk_p51s\n"
".Lmk_p51r:\n"
"v_fmac_f32_e32 v146, v253, v218\n"
"v_mfma_f32_16x16x32_bf16 v[204:207], a[36:39], v[96:99], v[204:207]\n"
"buffer_load_dwordx4 v[136:139], v0, s[4:7], s98 offen offset:2048 sc1\n"
"v_mfma_f32_16x16x32_bf16 v[204:207], a[32:35], v[92:95], v[204:207]\n"
"v_fmac_f32_e32 v147, v253, v219\n"
"v_add_f32_e32 v144, v144, v220\n"
"v_mfma_f32_16x16x32_bf16 v[200:203], a[64:67], v[92:95], v[200:203]\n"
"v_add_f32_e32 v145, v145, v221\n"
"v_add_f32_e32 v146, v146, v222\n"
"v_mfma_f32_16x16x32_bf16 v[200:203], a[68:71], v[96:99], v[200:203]\n"
"buffer_load_dwordx4 v[148:151], v0, s[4:7], s98 offen offset:3072 sc1\n"
"v_mfma_f32_16x16x32_bf16 v[200:203], a[72:75], v[104:107], v[200:203]\n"
"v_add_f32_e32 v147, v147, v223\n"
"v_fmac_f32_e32 v152, v253, v156\n"
"v_mfma_f32_16x16x32_bf16 v[200:203], a[76:79], v[112:115], v[200:203]\n"
"v_fmac_f32_e32 v153, v253, v157\n"
"v_fmac_f32_e32 v154, v253, v158\n"
"v_mfma_f32_16x16x32_bf16 v[200:203], a[80:83], v[100:103], v[200:203]\n"
"buffer_load_dwordx4 a[152:155], v1, s[4:7], s97 offen offset:2048 sc1\n"
"v_mfma_f32_16x16x32_bf16 v[200:203], a[84:87], v[124:127], v[200:203]\n"
"s_waitcnt lgkmcnt(0)\n"
"v_fmac_f32_e32 v155, v253, v159\n"
"v_add_f32_e32 v152, v152, v172\n"
"v_mfma_f32_16x16x32_bf16 v[200:203], a[88:91], v[168:171], v[200:203]\n"
"buffer_load_dwordx4 v[156:159], v1, s[4:7], s98 offen sc1\n"
"v_mfma_f32_16x16x32_bf16 v[200:203], a[92:95], v[188:191], v[200:203]\n"
"ds_read_b128 v[216:219], v6 offset:2048\n"
"ds_read_b128 v[220:223], v6 offset:3072\n"
"v_mfma_f32_16x16x32_bf16 v[144:147], a[124:127], v[188:191], v[144:147]\n"
"v_add_f32_e32 v153, v153, v173\n"
"v_add_f32_e32 v154, v154, v174\n"
"v_mfma_f32_16x16x32_bf16 v[144:147], a[120:123], v[168:171], v[144:147]\n"
"v_add_f32_e32 v155, v155, v175\n"
"v_fmac_f32_e32 v176, v253, v184\n"
"v_mfma_f32_16x16x32_bf16 v[144:147], a[116:119], v[124:127], v[144:147]\n"
"v_fmac_f32_e32 v177, v253, v185\n"
"v_fmac_f32_e32 v178, v253, v186\n"
"v_mfma_f32_16x16x32_bf16 v[144:147], a[112:115], v[100:103], v[144:147]\n"
"buffer_load_dwordx4 v[172:175], v1, s[4:7], s98 offen offset:1024 sc1\n"
"v_mfma_f32_16x16x32_bf16 v[144:147], a[108:111], v[112:115], v[144:147]\n"
"v_fmac_f32_e32 v179, v253, v187\n"
"v_add_f32_e32 v176, v176, v196\n"
"v_mfma_f32_16x16x32_bf16 v[144:147], a[104:107], v[104:107], v[144:147]\n"
"v_add_f32_e32 v177, v177, v197\n"
"v_add_f32_e32 v178, v178, v198\n"
"v_mfma_f32_16x16x32_bf16 v[144:147], a[100:103], v[96:99], v[144:147]\n"
"v_add_f32_e32 v179, v179, v199\n"
"v_add_f32_e32 v192, v192, v208\n"
"v_mfma_f32_16x16x32_bf16 v[144:147], a[96:99], v[92:95], v[144:147]\n"
"v_add_f32_e32 v193, v193, v209\n"
"v_add_f32_e32 v194, v194, v210\n"
"v_mfma_f32_16x16x32_bf16 v[152:155], v[24:27], v[92:95], v[152:155]\n"
"buffer_load_dwordx4 v[184:187], v1, s[4:7], s98 offen offset:2048 sc1\n"
"v_mfma_f32_16x16x32_bf16 v[152:155], v[28:31], v[96:99], v[152:155]\n"
"v_add_f32_e32 v195, v195, v211\n"
"v_add_f32_e32 v204, v204, v212\n"
"v_mfma_f32_16x16x32_bf16 v[152:155], v[32:35], v[104:107], v[152:155]\n"
"v_add_f32_e32 v205, v205, v213\n"
"v_add_f32_e32 v206, v206, v214\n"
"v_mfma_f32_16x16x32_bf16 v[152:155], v[36:39], v[112:115], v[152:155]\n"
"v_add_f32_e32 v207, v207, v215\n"
"v_mul_f32_e32 v192, v192, v255\n"
"v_mfma_f32_16x16x32_bf16 v[152:155], v[40:43], v[100:103], v[152:155]\n"
"s_waitcnt lgkmcnt(0)\n"
"v_mul_f32_e32 v193, v193, v255\n"
"v_mul_f32_e32 v194, v194, v255\n"
"v_mfma_f32_16x16x32_bf16 v[152:155], v[44:47], v[124:127], v[152:155]\n"
"buffer_load_dwordx4 v[196:199], v1, s[4:7], s98 offen offset:3072 sc1\n"
"v_mfma_f32_16x16x32_bf16 v[152:155], v[48:51], v[168:171], v[152:155]\n"
"s_add_u32 s98, s60, 0x408000\n"
"ds_read_b128 v[208:211], v6 offset:4096\n"
"ds_read_b128 v[212:215], v6 offset:5120\n"
"v_mfma_f32_16x16x32_bf16 v[152:155], v[52:55], v[188:191], v[152:155]\n"
"v_mul_f32_e32 v195, v195, v255\n"
"v_mul_f32_e32 v204, v204, v255\n"
"v_mfma_f32_16x16x32_bf16 v[176:179], v[84:87], v[188:191], v[176:179]\n"
"buffer_store_dwordx4 v[120:123], v0, s[4:7], s98 offen sc1\n"
"v_mfma_f32_16x16x32_bf16 v[176:179], v[80:83], v[168:171], v[176:179]\n"
"v_mul_f32_e32 v205, v205, v255\n"
"v_mul_f32_e32 v206, v206, v255\n"
"v_mfma_f32_16x16x32_bf16 v[176:179], v[76:79], v[124:127], v[176:179]\n"
"v_mul_f32_e32 v207, v207, v255\n"
"v_cvt_pk_bf16_f32 v120, v192, v193\n"
"v_mfma_f32_16x16x32_bf16 v[176:179], v[72:75], v[100:103], v[176:179]\n"
"v_cvt_pk_bf16_f32 v121, v194, v195\n"
"v_cvt_pk_bf16_f32 v122, v204, v205\n"
"v_mfma_f32_16x16x32_bf16 v[176:179], v[68:71], v[112:115], v[176:179]\n"
"v_cvt_pk_bf16_f32 v123, v206, v207\n"
"v_add_f32_e32 v200, v200, v216\n"
"v_mfma_f32_16x16x32_bf16 v[176:179], v[64:67], v[104:107], v[176:179]\n"
"ds_write_b128 v7, v[120:123] offset:24576\n"
"v_mfma_f32_16x16x32_bf16 v[176:179], v[60:63], v[96:99], v[176:179]\n"
"v_add_f32_e32 v201, v201, v217\n"
"v_add_f32_e32 v202, v202, v218\n"
"v_mfma_f32_16x16x32_bf16 v[176:179], v[56:59], v[92:95], v[176:179]\n"
"v_add_f32_e32 v203, v203, v219\n"
"v_add_f32_e32 v144, v144, v220\n"
"v_add_f32_e32 v145, v145, v221\n"
"v_add_f32_e32 v146, v146, v222\n"
"v_add_f32_e32 v147, v147, v223\n"
"v_mul_f32_e32 v200, v200, v255\n"
"v_mul_f32_e32 v201, v201, v255\n"
"v_mul_f32_e32 v202, v202, v255\n"
"v_mul_f32_e32 v203, v203, v255\n"
"v_mul_f32_e32 v144, v144, v255\n"
"v_mul_f32_e32 v145, v145, v255\n"
"v_mul_f32_e32 v146, v146, v255\n"
"v_mul_f32_e32 v147, v147, v255\n"
"v_cvt_pk_bf16_f32 v92, v200, v201\n"
"v_cvt_pk_bf16_f32 v93, v202, v203\n"
"v_cvt_pk_bf16_f32 v94, v144, v145\n"
"v_cvt_pk_bf16_f32 v95, v146, v147\n"
"ds_write_b128 v7, v[92:95] offset:57344\n"
".Lmk_b1:\n"
"s_waitcnt vmcnt(18)\n"
"; @B G1 1.0\n"
"v_mfma_f32_16x16x32_bf16 v[92:95], a[0:3], v[128:131], 0\n"
"v_mfma_f32_16x16x32_bf16 v[92:95], a[4:7], v[88:91], v[92:95]\n"
"s_add_u32 s98, s62, 0x44800\n"
"v_mfma_f32_16x16x32_bf16 v[92:95], a[8:11], v[164:167], v[92:95]\n"
"s_add_u32 s96, s54, 0x448000\n"
"buffer_load_dwordx4 v[96:99], v0, s[4:7], s96 offen sc1\n"
"v_mfma_f32_16x16x32_bf16 v[92:95], a[12:15], v[140:143], v[92:95]\n"
"s_waitcnt vmcnt(12)\n"
"v_mfma_f32_16x16x32_bf16 v[92:95], a[16:19], v[132:135], v[92:95]\n"
"buffer_load_dword v255, v10, s[4:7], s98 offen sc1\n"
"v_mfma_f32_16x16x32_bf16 v[92:95], a[20:23], v[224:227], v[92:95]\n"
"s_waitcnt lgkmcnt(2)\n"
"v_add_f32_e32 v152, v152, v208\n"
"v_add_f32_e32 v153, v153, v209\n"
"v_mfma_f32_16x16x32_bf16 v[92:95], a[24:27], v[180:183], v[92:95]\n"
"v_add_f32_e32 v154, v154, v210\n"
"v_add_f32_e32 v155, v155, v211\n"
"v_mfma_f32_16x16x32_bf16 v[92:95], a[28:31], v[108:111], v[92:95]\n"
"buffer_load_dwordx4 v[100:103], v0, s[4:7], s96 offen offset:1024 sc1\n"
"v_mfma_f32_16x16x32_bf16 v[104:107], a[60:63], v[108:111], 0\n"
"buffer_load_dwordx4 a[156:159], v1, s[4:7], s97 offen offset:3072 sc1\n"
"v_mfma_f32_16x16x32_bf16 v[104:107], a[56:59], v[180:183], v[104:107]\n"
"buffer_load_dword v253, v10, s[4:7], s98 offen offset:256 sc1\n"
"v_mfma_f32_16x16x32_bf16 v[104:107], a[52:55], v[224:227], v[104:107]\n"
"v_add_f32_e32 v176, v176, v212\n"
"v_add_f32_e32 v177, v177, v213\n"
"v_mfma_f32_16x16x32_bf16 v[104:107], a[48:51], v[132:135], v[104:107]\n"
"v_add_f32_e32 v178, v178, v214\n"
"v_add_f32_e32 v179, v179, v215\n"
"v_mfma_f32_16x16x32_bf16 v[104:107], a[44:47], v[140:143], v[104:107]\n"
"v_cmp_u_f32_e32 vcc, v92, v92\n"
"v_mul_f32_e32 v152, v152, v254\n"
"v_mfma_f32_16x16x32_bf16 v[104:107], a[40:43], v[164:167], v[104:107]\n"
"s_cbranch_vccnz .Lmk_p53s\n"
".Lmk_p53r:\n"
"v_mul_f32_e32 v153, v153, v254\n"
"v_mfma_f32_16x16x32_bf16 v[104:107], a[36:39], v[88:91], v[104:107]\n"
"buffer_load_dwordx4 v[112:115], v0, s[4:7], s96 offen offset:2048 sc1\n"
"v_mfma_f32_16x16x32_bf16 v[104:107], a[32:35], v[128:131], v[104:107]\n"
"ds_write_b128 v3, v[92:95]\n"
"v_mfma_f32_16x16x32_bf16 v[92:95], a[64:67], v[128:131], 0\n"
"buffer_load_dword v252, v10, s[4:7], s98 offen offset:512 sc1\n"
"v_mfma_f32_16x16x32_bf16 v[92:95], a[68:71], v[88:91], v[92:95]\n"
"buffer_load_dwordx4 v[120:123], v0, s[4:7], s96 offen offset:3072 sc1\n"
"v_mfma_f32_16x16x32_bf16 v[92:95], a[72:75], v[164:167], v[92:95]\n"
"v_mul_f32_e32 v154, v154, v254\n"
"v_mul_f32_e32 v155, v155, v254\n"
"v_mfma_f32_16x16x32_bf16 v[92:95], a[76:79], v[140:143], v[92:95]\n"
"buffer_load_dword v251, v10, s[4:7], s98 offen offset:768 sc1\n"
"v_mfma_f32_16x16x32_bf16 v[92:95], a[80:83], v[132:135], v[92:95]\n"
"v_mul_f32_e32 v176, v176, v254\n"
"v_mul_f32_e32 v177, v177, v254\n"
"v_mfma_f32_16x16x32_bf16 v[92:95], a[84:87], v[224:227], v[92:95]\n"
"ds_write_b128 v3, v[104:107] offset:1024\n"
"v_mfma_f32_16x16x32_bf16 v[92:95], a[88:91], v[180:183], v[92:95]\n"
"buffer_load_dwordx4 v[104:107], v1, s[4:7], s96 offen sc1\n"
"v_mfma_f32_16x16x32_bf16 v[92:95], a[92:95], v[108:111], v[92:95]\n"
"buffer_load_dword v250, v10, s[4:7], s98 offen offset:1024 sc1\n"
"v_mfma_f32_16x16x32_bf16 v[124:127], a[124:127], v[108:111], 0\n"
"v_mul_f32_e32 v178, v178, v254\n"
"v_mul_f32_e32 v179, v179, v254\n"
"v_mfma_f32_16x16x32_bf16 v[124:127], a[120:123], v[180:183], v[124:127]\n"
"v_cvt_pk_bf16_f32 v144, v152, v153\n"
"v_cvt_pk_bf16_f32 v145, v154, v155\n"
"v_mfma_f32_16x16x32_bf16 v[124:127], a[116:119], v[224:227], v[124:127]\n"
"s_add_u32 s97, s70, 0x202000\n"
"buffer_load_dword v254, v10, s[4:7], s98 offen offset:1280 sc1\n"
"v_mfma_f32_16x16x32_bf16 v[124:127], a[112:115], v[132:135], v[124:127]\n"
"buffer_load_dwordx4 v[152:155], v1, s[4:7], s96 offen offset:1024 sc1\n"
"v_mfma_f32_16x16x32_bf16 v[124:127], a[108:111], v[140:143], v[124:127]\n"
"buffer_load_dwordx4 a[160:163], v0, s[4:7], s97 offen sc1\n"
"v_mfma_f32_16x16x32_bf16 v[124:127], a[104:107], v[164:167], v[124:127]\n"
"ds_write_b128 v3, v[92:95] offset:2048\n"
"v_mfma_f32_16x16x32_bf16 v[124:127], a[100:103], v[88:91], v[124:127]\n"
"buffer_load_dword v249, v10, s[4:7], s98 offen offset:1536 sc1\n"
"v_mfma_f32_16x16x32_bf16 v[124:127], a[96:99], v[128:131], v[124:127]\n"
"v_cvt_pk_bf16_f32 v146, v176, v177\n"
"v_cvt_pk_bf16_f32 v147, v178, v179\n"
"v_mfma_f32_16x16x32_bf16 v[92:95], v[24:27], v[128:131], 0\n"
"buffer_load_dwordx4 v[168:171], v1, s[4:7], s96 offen offset:2048 sc1\n"
"v_mfma_f32_16x16x32_bf16 v[92:95], v[28:31], v[88:91], v[92:95]\n"
"buffer_load_dword v248, v10, s[4:7], s98 offen offset:1792 sc1\n"
"v_mfma_f32_16x16x32_bf16 v[92:95], v[32:35], v[164:167], v[92:95]\n"
"ds_write_b128 v2, v[144:147] offset:49152\n"
"v_mfma_f32_16x16x32_bf16 v[92:95], v[36:39], v[140:143], v[92:95]\n"
"v_mfma_f32_16x16x32_bf16 v[92:95], v[40:43], v[132:135], v[92:95]\n"
"v_mfma_f32_16x16x32_bf16 v[92:95], v[44:47], v[224:227], v[92:95]\n"
"buffer_load_dwordx4 v[144:147], v1, s[4:7], s96 offen offset:3072 sc1\n"
"v_mfma_f32_16x16x32_bf16 v[92:95], v[48:51], v[180:183], v[92:95]\n"
"ds_write_b128 v3, v[124:127] offset:3072\n"
"v_mfma_f32_16x16x32_bf16 v[92:95], v[52:55], v[108:111], v[92:95]\n"
"v_mfma_f32_16x16x32_bf16 v[124:127], v[84:87], v[108:111], 0\n"
"v_mfma_f32_16x16x32_bf16 v[124:127], v[80:83], v[180:183], v[124:127]\n"
"v_mfma_f32_16x16x32_bf16 v[124:127], v[76:79], v[224:227], v[124:127]\n"
"v_mfma_f32_16x16x32_bf16 v[124:127], v[72:75], v[132:135], v[124:127]\n"
"v_mfma_f32_16x16x32_bf16 v[124:127], v[68:71], v[140:143], v[124:127]\n"
"v_mfma_f32_16x16x32_bf16 v[124:127], v[64:67], v[164:167], v[124:127]\n"
"s_nop 1\n"
"ds_write_b128 v3, v[92:95] offset:4096\n"
"v_mfma_f32_16x16x32_bf16 v[124:127], v[60:63], v[88:91], v[124:127]\n"
"v_mfma_f32_16x16x32_bf16 v[124:127], v[56:59], v[128:131], v[124:127]\n"
"s_waitcnt vmcnt(24)\n"
"; @B G1 1.1\n"
"v_mfma_f32_16x16x32_bf16 v[88:91], a[0:3], v[116:119], 0\n"
"buffer_load_dwordx4 a[164:167], v0, s[4:7], s97 offen offset:1024 sc1\n"
"v_mfma_f32_16x16x32_bf16 v[88:91], a[4:7], v[160:163], v[88:91]\n"
"v_mfma_f32_16x16x32_bf16 v[88:91], a[8:11], v[136:139], v[88:91]\n"
"s_add_u32 s98, s55, 0x448000\n"
"buffer_load_dwordx4 v[92:95], v0, s[4:7], s98 offen sc1\n"
"v_mfma_f32_16x16x32_bf16 v[88:91], a[12:15], v[148:151], v[88:91]\n"
"s_waitcnt vmcnt(21)\n"
"v_mfma_f32_16x16x32_bf16 v[88:91], a[16:19], v[156:159], v[88:91]\n"
"v_mfma_f32_16x16x32_bf16 v[88:91], a[20:23], v[172:175], v[88:91]\n"
"ds_write_b128 v3, v[124:127] offset:5120\n"
"v_mfma_f32_16x16x32_bf16 v[88:91], a[24:27], v[184:187], v[88:91]\n"
"v_mfma_f32_16x16x32_bf16 v[88:91], a[28:31], v[196:199], v[88:91]\n"
"buffer_load_dwordx4 v[108:111], v0, s[4:7], s98 offen offset:1024 sc1\n"
"v_mfma_f32_16x16x32_bf16 v[124:127], a[60:63], v[196:199], 0\n"
"v_mfma_f32_16x16x32_bf16 v[124:127], a[56:59], v[184:187], v[124:127]\n"
"v_mfma_f32_16x16x32_bf16 v[124:127], a[52:55], v[172:175], v[124:127]\n"
"v_mfma_f32_16x16x32_bf16 v[124:127], a[48:51], v[156:159], v[124:127]\n"
"v_mfma_f32_16x16x32_bf16 v[124:127], a[44:47], v[148:151], v[124:127]\n"
"s_add_u32 s96, s67, 0x10000\n"
"s_nop 0\n"
"v_cmp_u_f32_e32 vcc, v88, v88\n"
"v_mfma_f32_16x16x32_bf16 v[124:127], a[40:43], v[136:139], v[124:127]\n"
"s_cbranch_vccnz .Lmk_p54s\n"
".Lmk_p54r:\n"
"v_mfma_f32_16x16x32_bf16 v[124:127], a[36:39], v[160:163], v[124:127]\n"
"buffer_load_dwordx4 v[128:131], v0, s[4:7], s98 offen offset:2048 sc1\n"
"v_mfma_f32_16x16x32_bf16 v[124:127], a[32:35], v[116:119], v[124:127]\n"
"ds_write_b128 v3, v[88:91] offset:6144\n"
"v_mfma_f32_16x16x32_bf16 v[88:91], a[64:67], v[116:119], 0\n"
"buffer_load_dwordx2 v[246:247], v12, s[4:7], s96 offen sc1\n"
"v_mfma_f32_16x16x32_bf16 v[88:91], a[68:71], v[160:163], v[88:91]\n"
"buffer_load_dwordx4 v[132:135], v0, s[4:7], s98 offen offset:3072 sc1\n"
"v_mfma_f32_16x16x32_bf16 v[88:91], a[72:75], v[136:139], v[88:91]\n"
"v_mfma_f32_16x16x32_bf16 v[88:91], a[76:79], v[148:151], v[88:91]\n"
"v_mfma_f32_16x16x32_bf16 v[88:91], a[80:83], v[156:159], v[88:91]\n"
"v_mov_b32_e32 v140, -1\n"
"v_mov_b32_e32 v141, -1\n"
"v_mfma_f32_16x16x32_bf16 v[88:91], a[84:87], v[172:175], v[88:91]\n"
"ds_write_b128 v3, v[124:127] offset:7168\n"
"v_mfma_f32_16x16x32_bf16 v[88:91], a[88:91], v[184:187], v[88:91]\n"
"buffer_load_dwordx4 v[124:127], v1, s[4:7], s98 offen sc1\n"
"v_mfma_f32_16x16x32_bf16 v[88:91], a[92:95], v[196:199], v[88:91]\n"
"v_mov_b32_e32 v142, -1\n"
"v_mov_b32_e32 v143, -1\n"
"v_mfma_f32_16x16x32_bf16 v[164:167], a[124:127], v[196:199], 0\n"
"s_add_u32 s95, s61, 0x840000\n"
"buffer_store_dwordx4 v[140:143], v0, s[8:11], s95 offen sc1\n"
"v_mfma_f32_16x16x32_bf16 v[164:167], a[120:123], v[184:187], v[164:167]\n"
"buffer_load_dwordx4 a[168:171], v0, s[4:7], s97 offen offset:2048 sc1\n"
"v_mfma_f32_16x16x32_bf16 v[164:167], a[116:119], v[172:175], v[164:167]\n"
"v_mfma_f32_16x16x32_bf16 v[164:167], a[112:115], v[156:159], v[164:167]\n"
"buffer_load_dwordx4 v[176:179], v1, s[4:7], s98 offen offset:1024 sc1\n"
"v_mfma_f32_16x16x32_bf16 v[164:167], a[108:111], v[148:151], v[164:167]\n"
"v_mfma_f32_16x16x32_bf16 v[164:167], a[104:107], v[136:139], v[164:167]\n"
"ds_write_b128 v3, v[88:91] offset:8192\n"
"v_mfma_f32_16x16x32_bf16 v[164:167], a[100:103], v[160:163], v[164:167]\n"
"v_mfma_f32_16x16x32_bf16 v[164:167], a[96:99], v[116:119], v[164:167]\n"
"v_mfma_f32_16x16x32_bf16 v[88:91], v[24:27], v[116:119], 0\n"
"buffer_load_dwordx4 v[180:183], v1, s[4:7], s98 offen offset:2048 sc1\n"
"v_mfma_f32_16x16x32_bf16 v[88:91], v[28:31], v[160:163], v[88:91]\n"
"v_mfma_f32_16x16x32_bf16 v[88:91], v[32:35], v[136:139], v[88:91]\n"
"v_mfma_f32_16x16x32_bf16 v[88:91], v[36:39], v[148:151], v[88:91]\n"
"v_mfma_f32_16x16x32_bf16 v[88:91], v[40:43], v[156:159], v[88:91]\n"
"s_waitcnt vmcnt(12)\n"
"v_add_f32_e32 v255, v255, v250\n"
"v_add_f32_e32 v253, v253, v254\n"
"v_mfma_f32_16x16x32_bf16 v[88:91], v[44:47], v[172:175], v[88:91]\n"
"buffer_load_dwordx4 v[188:191], v1, s[4:7], s98 offen offset:3072 sc1\n"
"v_mfma_f32_16x16x32_bf16 v[88:91], v[48:51], v[184:187], v[88:91]\n"
"ds_write_b128 v3, v[164:167] offset:9216\n"
"v_mfma_f32_16x16x32_bf16 v[88:91], v[52:55], v[196:199], v[88:91]\n"
"v_add_f32_e32 v252, v252, v249\n"
"v_add_f32_e32 v251, v251, v248\n"
"v_mfma_f32_16x16x32_bf16 v[164:167], v[84:87], v[196:199], 0\n"
"v_add_f32_e32 v255, v255, v252\n"
"v_add_f32_e32 v253, v253, v251\n"
"v_mfma_f32_16x16x32_bf16 v[164:167], v[80:83], v[184:187], v[164:167]\n"
"v_add_f32_e32 v255, v255, v253\n"
"v_cmp_u_f32_e32 vcc, v255, v255\n"
"v_mfma_f32_16x16x32_bf16 v[164:167], v[76:79], v[172:175], v[164:167]\n"
"s_cbranch_vccnz .Lmk_m57s\n"
".Lmk_m57r:\n"
"v_mov_b32_e32 v253, v255\n"
"v_mfma_f32_16x16x32_bf16 v[164:167], v[72:75], v[156:159], v[164:167]\n"
"s_nop 0\n"
"v_permlane32_swap_b32_e32 v255, v253\n"
"v_add_f32_e32 v255, v255, v253\n"
"v_mfma_f32_16x16x32_bf16 v[164:167], v[68:71], v[148:151], v[164:167]\n"
"v_mul_f32_e32 v255, 0x3a800000, v255\n"
"v_mov_b32_e32 v253, v255\n"
"v_mfma_f32_16x16x32_bf16 v[164:167], v[64:67], v[136:139], v[164:167]\n"
"ds_write_b128 v3, v[88:91] offset:10240\n"
"v_mfma_f32_16x16x32_bf16 v[164:167], v[60:63], v[160:163], v[164:167]\n"
"buffer_load_dwordx4 a[172:175], v0, s[4:7], s97 offen offset:3072 sc1\n"
"v_mfma_f32_16x16x32_bf16 v[164:167], v[56:59], v[116:119], v[164:167]\n"
"v_permlane16_swap_b32_e32 v255, v253\n"
"v_cvt_pk_bf16_f32 v254, -v255, 0\n"
"; @B G1 1.2\n"
"v_mfma_f32_16x16x32_bf16 v[88:91], a[0:3], v[96:99], 0\n"
"v_fma_f32 v253, -v255, v255, v253\n"
"v_lshlrev_b32_e32 v254, 16, v254\n"
"v_mfma_f32_16x16x32_bf16 v[88:91], a[4:7], v[100:103], v[88:91]\n"
"s_waitcnt vmcnt(8)\n"
"v_cmp_ne_u64_e32 vcc, v[246:247], v[18:19]\n"
"v_mfma_f32_16x16x32_bf16 v[88:91], a[8:11], v[112:115], v[88:91]\n"
"s_cbranch_vccnz .Lmk_f44s\n"
".Lmk_f44r:\n"
"v_max_f32_e32 v253, 0, v253\n"
"v_mfma_f32_16x16x32_bf16 v[88:91], a[12:15], v[120:123], v[88:91]\n"
"v_cvt_pk_bf16_f32 v253, v253, 0\n"
"v_lshlrev_b32_e32 v253, 16, v253\n"
"v_mfma_f32_16x16x32_bf16 v[88:91], a[16:19], v[104:107], v[88:91]\n"
"v_add_f32_e32 v253, 0x3727c5ac, v253\n"
"v_cvt_pk_bf16_f32 v253, v253, 0\n"
"v_mfma_f32_16x16x32_bf16 v[88:91], a[20:23], v[152:155], v[88:91]\n"
"ds_write_b128 v3, v[164:167] offset:11264\n"
"v_mfma_f32_16x16x32_bf16 v[88:91], a[24:27], v[168:171], v[88:91]\n"
"v_lshlrev_b32_e32 v253, 16, v253\n"
"v_mfma_f32_16x16x32_bf16 v[88:91], a[28:31], v[144:147], v[88:91]\n"
"v_sqrt_f32_e32 v253, v253\n"
"v_mfma_f32_16x16x32_bf16 v[116:119], a[60:63], v[144:147], 0\n"
"v_cvt_pk_bf16_f32 v253, v253, 0\n"
"v_lshlrev_b32_e32 v253, 16, v253\n"
"v_mfma_f32_16x16x32_bf16 v[116:119], a[56:59], v[168:171], v[116:119]\n"
"v_rcp_f32_e32 v255, v253\n"
"v_mfma_f32_16x16x32_bf16 v[116:119], a[52:55], v[152:155], v[116:119]\n"
"s_add_u32 s98, s52, 0x408000\n"
"buffer_load_dwordx4 v[136:139], v0, s[4:7], s98 offen sc1\n"
"v_mfma_f32_16x16x32_bf16 v[116:119], a[48:51], v[104:107], v[116:119]\n"
"s_waitcnt lgkmcnt(0)\n"
"s_barrier\n"
"v_mfma_f32_16x16x32_bf16 v[116:119], a[44:47], v[120:123], v[116:119]\n"
"v_cmp_u_f32_e32 vcc, v88, v88\n"
"v_mul_f32_e32 v253, 0x3e8293ee, v255\n"
"v_mfma_f32_16x16x32_bf16 v[116:119], a[40:43], v[112:115], v[116:119]\n"
"s_cbranch_vccnz .Lmk_p55s\n"
".Lmk_p55r:\n"
"ds_read_b128 v[148:151], v4\n"
"v_mfma_f32_16x16x32_bf16 v[116:119], a[36:39], v[100:103], v[116:119]\n"
"ds_write_b128 v3, v[88:91] offset:12288\n"
"v_mfma_f32_16x16x32_bf16 v[116:119], a[32:35], v[96:99], v[116:119]\n"
"buffer_load_dwordx4 v[88:91], v0, s[4:7], s98 offen offset:1024 sc1\n"
"v_mfma_f32_16x16x32_bf16 v[156:159], a[64:67], v[96:99], 0\n"
"ds_read_b128 v[160:163], v9 offset:640\n"
"ds_read_b128 v[164:167], v5\n"
"v_mfma_f32_16x16x32_bf16 v[156:159], a[68:71], v[100:103], v[156:159]\n"
"v_mfma_f32_16x16x32_bf16 v[156:159], a[72:75], v[112:115], v[156:159]\n"
"buffer_load_dwordx4 a[176:179], v1, s[4:7], s97 offen sc1\n"
"v_mfma_f32_16x16x32_bf16 v[156:159], a[76:79], v[120:123], v[156:159]\n"
"ds_read_b128 v[172:175], v4 offset:1024\n"
"ds_read_b128 v[184:187], v9 offset:704\n"
"v_mfma_f32_16x16x32_bf16 v[156:159], a[80:83], v[104:107], v[156:159]\n"
"buffer_load_dwordx4 v[192:195], v0, s[4:7], s98 offen offset:2048 sc1\n"
"v_mfma_f32_16x16x32_bf16 v[156:159], a[84:87], v[152:155], v[156:159]\n"
"ds_write_b128 v3, v[116:119] offset:13312\n"
"v_mfma_f32_16x16x32_bf16 v[156:159], a[88:91], v[168:171], v[156:159]\n"
"ds_read_b128 v[116:119], v5 offset:1024\n"
"s_waitcnt lgkmcnt(4)\n"
"v_mfma_f32_16x16x32_bf16 v[156:159], a[92:95], v[144:147], v[156:159]\n"
"v_fmac_f32_e32 v148, v254, v160\n"
"v_fmac_f32_e32 v149, v254, v161\n"
"v_mfma_f32_16x16x32_bf16 v[196:199], a[124:127], v[144:147], 0\n"
"v_fmac_f32_e32 v150, v254, v162\n"
"v_fmac_f32_e32 v151, v254, v163\n"
"v_mfma_f32_16x16x32_bf16 v[196:199], a[120:123], v[168:171], v[196:199]\n"
"buffer_load_dwordx4 v[160:163], v0, s[4:7], s98 offen offset:3072 sc1\n"
"v_mfma_f32_16x16x32_bf16 v[196:199], a[116:119], v[152:155], v[196:199]\n"
"ds_read_b128 v[200:203], v4 offset:2048\n"
"ds_read_b128 v[204:207], v9 offset:768\n"
"v_mfma_f32_16x16x32_bf16 v[196:199], a[112:115], v[104:107], v[196:199]\n"
"ds_read_b128 v[208:211], v5 offset:2048\n"
"v_add_f32_e32 v148, v148, v164\n"
"v_mfma_f32_16x16x32_bf16 v[196:199], a[108:111], v[120:123], v[196:199]\n"
"v_add_f32_e32 v149, v149, v165\n"
"v_add_f32_e32 v150, v150, v166\n"
"v_mfma_f32_16x16x32_bf16 v[196:199], a[104:107], v[112:115], v[196:199]\n"
"ds_write_b128 v3, v[156:159] offset:14336\n"
"v_mfma_f32_16x16x32_bf16 v[196:199], a[100:103], v[100:103], v[196:199]\n"
"buffer_load_dwordx4 v[156:159], v1, s[4:7], s98 offen sc1\n"
"v_mfma_f32_16x16x32_bf16 v[196:199], a[96:99], v[96:99], v[196:199]\n"
"s_add_u32 s96, s61, 0x840000\n"
"v_add_f32_e32 v151, v151, v167\n"
"s_waitcnt lgkmcnt(4)\n"
"v_mfma_f32_16x16x32_bf16 v[164:167], v[24:27], v[96:99], 0\n"
"buffer_store_dwordx4 v[140:143], v0, s[8:11], s96 offen offset:1024 sc1\n"
"v_mfma_f32_16x16x32_bf16 v[164:167], v[28:31], v[100:103], v[164:167]\n"
"ds_read_b128 v[212:215], v4 offset:3072\n"
"ds_read_b128 v[216:219], v9 offset:832\n"
"v_mfma_f32_16x16x32_bf16 v[164:167], v[32:35], v[112:115], v[164:167]\n"
"ds_read_b128 v[220:223], v5 offset:3072\n"
"v_fmac_f32_e32 v172, v254, v184\n"
"v_mfma_f32_16x16x32_bf16 v[164:167], v[36:39], v[120:123], v[164:167]\n"
"buffer_load_dwordx4 v[224:227], v1, s[4:7], s98 offen offset:1024 sc1\n"
"v_mfma_f32_16x16x32_bf16 v[164:167], v[40:43], v[104:107], v[164:167]\n"
"v_fmac_f32_e32 v173, v254, v185\n"
"v_fmac_f32_e32 v174, v254, v186\n"
"v_mfma_f32_16x16x32_bf16 v[164:167], v[44:47], v[152:155], v[164:167]\n"
"ds_write_b128 v3, v[196:199] offset:15360\n"
"v_mfma_f32_16x16x32_bf16 v[164:167], v[48:51], v[168:171], v[164:167]\n"
"buffer_load_dwordx4 a[180:183], v1, s[4:7], s97 offen offset:1024 sc1\n"
"v_mfma_f32_16x16x32_bf16 v[164:167], v[52:55], v[144:147], v[164:167]\n"
"v_fmac_f32_e32 v175, v254, v187\n"
"v_add_f32_e32 v172, v172, v116\n"
"v_mfma_f32_16x16x32_bf16 v[184:187], v[84:87], v[144:147], 0\n"
"buffer_load_dwordx4 v[144:147], v1, s[4:7], s98 offen offset:2048 sc1\n"
"v_mfma_f32_16x16x32_bf16 v[184:187], v[80:83], v[168:171], v[184:187]\n"
"ds_read_b128 v[168:171], v4 offset:4096\n"
"ds_read_b128 v[196:199], v9 offset:512\n"
"v_mfma_f32_16x16x32_bf16 v[184:187], v[76:79], v[152:155], v[184:187]\n"
"ds_read_b128 v[152:155], v5 offset:4096\n"
"v_add_f32_e32 v173, v173, v117\n"
"v_mfma_f32_16x16x32_bf16 v[184:187], v[72:75], v[104:107], v[184:187]\n"
"s_waitcnt lgkmcnt(8)\n"
"v_add_f32_e32 v174, v174, v118\n"
"v_add_f32_e32 v175, v175, v119\n"
"v_mfma_f32_16x16x32_bf16 v[184:187], v[68:71], v[120:123], v[184:187]\n"
"v_fmac_f32_e32 v200, v254, v204\n"
"v_fmac_f32_e32 v201, v254, v205\n"
"v_mfma_f32_16x16x32_bf16 v[184:187], v[64:67], v[112:115], v[184:187]\n"
"buffer_load_dwordx4 v[104:107], v1, s[4:7], s98 offen offset:3072 sc1\n"
"v_mfma_f32_16x16x32_bf16 v[184:187], v[60:63], v[100:103], v[184:187]\n"
"ds_write_b128 v3, v[164:167] offset:16384\n"
"v_mfma_f32_16x16x32_bf16 v[184:187], v[56:59], v[96:99], v[184:187]\n"
"s_waitcnt vmcnt(18)\n"
"ds_read_b128 v[96:99], v4 offset:5120\n"
"ds_read_b128 v[100:103], v9 offset:576\n"
"; @B G1 1.3\n"
"v_mfma_f32_16x16x32_bf16 v[112:115], a[0:3], v[92:95], v[148:151]\n"
"ds_read_b128 v[116:119], v5 offset:5120\n"
"v_fmac_f32_e32 v202, v254, v206\n"
"v_mfma_f32_16x16x32_bf16 v[112:115], a[4:7], v[108:111], v[112:115]\n"
"v_fmac_f32_e32 v203, v254, v207\n"
"v_add_f32_e32 v200, v200, v208\n"
"v_mfma_f32_16x16x32_bf16 v[112:115], a[8:11], v[128:131], v[112:115]\n"
"s_add_u32 s98, s53, 0x408000\n"
"buffer_load_dwordx4 v[120:123], v0, s[4:7], s98 offen sc1\n"
"v_mfma_f32_16x16x32_bf16 v[112:115], a[12:15], v[132:135], v[112:115]\n"
"s_waitcnt vmcnt(13)\n"
"v_add_f32_e32 v201, v201, v209\n"
"v_add_f32_e32 v202, v202, v210\n"
"v_mfma_f32_16x16x32_bf16 v[112:115], a[16:19], v[124:127], v[112:115]\n"
"s_waitcnt lgkmcnt(8)\n"
"v_add_f32_e32 v203, v203, v211\n"
"v_fmac_f32_e32 v212, v254, v216\n"
"v_mfma_f32_16x16x32_bf16 v[112:115], a[20:23], v[176:179], v[112:115]\n"
"ds_write_b128 v3, v[184:187] offset:17408\n"
"v_mfma_f32_16x16x32_bf16 v[112:115], a[24:27], v[180:183], v[112:115]\n"
"v_fmac_f32_e32 v213, v254, v217\n"
"v_fmac_f32_e32 v214, v254, v218\n"
"v_mfma_f32_16x16x32_bf16 v[112:115], a[28:31], v[188:191], v[112:115]\n"
"buffer_load_dwordx4 v[164:167], v0, s[4:7], s98 offen offset:1024 sc1\n"
"v_mfma_f32_16x16x32_bf16 v[184:187], a[60:63], v[188:191], v[172:175]\n"
"v_fmac_f32_e32 v215, v254, v219\n"
"v_add_f32_e32 v212, v212, v220\n"
"v_mfma_f32_16x16x32_bf16 v[184:187], a[56:59], v[180:183], v[184:187]\n"
"v_add_f32_e32 v213, v213, v221\n"
"v_add_f32_e32 v214, v214, v222\n"
"v_mfma_f32_16x16x32_bf16 v[184:187], a[52:55], v[176:179], v[184:187]\n"
"s_waitcnt lgkmcnt(0)\n"
"s_barrier\n"
"v_mfma_f32_16x16x32_bf16 v[184:187], a[48:51], v[124:127], v[184:187]\n"
"buffer_load_dwordx4 a[184:187], v1, s[4:7], s97 offen offset:2048 sc1\n"
"v_mfma_f32_16x16x32_bf16 v[184:187], a[44:47], v[132:135], v[184:187]\n"
"ds_read_b128 v[204:207], v6\n"
"v_cmp_u_f32_e32 vcc, v112, v112\n"
"v_mfma_f32_16x16x32_bf16 v[184:187], a[40:43], v[128:131], v[184:187]\n"
"s_cbranch_vccnz .Lmk_p56s\n"
".Lmk_p56r:\n"
"ds_read_b128 v[148:151], v6 offset:1024\n"
"v_mfma_f32_16x16x32_bf16 v[184:187], a[36:39], v[108:111], v[184:187]\n"
"buffer_load_dwordx4 v[172:175], v0, s[4:7], s98 offen offset:2048 sc1\n"
"v_mfma_f32_16x16x32_bf16 v[184:187], a[32:35], v[92:95], v[184:187]\n"
"v_add_f32_e32 v215, v215, v223\n"
"v_fmac_f32_e32 v168, v254, v196\n"
"v_mfma_f32_16x16x32_bf16 v[200:203], a[64:67], v[92:95], v[200:203]\n"
"v_fmac_f32_e32 v169, v254, v197\n"
"v_fmac_f32_e32 v170, v254, v198\n"
"v_mfma_f32_16x16x32_bf16 v[200:203], a[68:71], v[108:111], v[200:203]\n"
"buffer_load_dwordx4 v[208:211], v0, s[4:7], s98 offen offset:3072 sc1\n"
"v_mfma_f32_16x16x32_bf16 v[200:203], a[72:75], v[128:131], v[200:203]\n"
"v_fmac_f32_e32 v171, v254, v199\n"
"v_add_f32_e32 v168, v168, v152\n"
"v_mfma_f32_16x16x32_bf16 v[200:203], a[76:79], v[132:135], v[200:203]\n"
"v_add_f32_e32 v169, v169, v153\n"
"v_add_f32_e32 v170, v170, v154\n"
"v_mfma_f32_16x16x32_bf16 v[200:203], a[80:83], v[124:127], v[200:203]\n"
"v_add_f32_e32 v171, v171, v155\n"
"v_fmac_f32_e32 v96, v254, v100\n"
"v_mfma_f32_16x16x32_bf16 v[200:203], a[84:87], v[176:179], v[200:203]\n"
"s_waitcnt lgkmcnt(0)\n"
"v_fmac_f32_e32 v97, v254, v101\n"
"v_fmac_f32_e32 v98, v254, v102\n"
"v_mfma_f32_16x16x32_bf16 v[200:203], a[88:91], v[180:183], v[200:203]\n"
"buffer_load_dwordx4 v[152:155], v1, s[4:7], s98 offen sc1\n"
"v_mfma_f32_16x16x32_bf16 v[200:203], a[92:95], v[188:191], v[200:203]\n"
"ds_read_b128 v[196:199], v6 offset:2048\n"
"ds_read_b128 v[216:219], v6 offset:3072\n"
"v_mfma_f32_16x16x32_bf16 v[212:215], a[124:127], v[188:191], v[212:215]\n"
"v_fmac_f32_e32 v99, v254, v103\n"
"v_add_f32_e32 v96, v96, v116\n"
"v_mfma_f32_16x16x32_bf16 v[212:215], a[120:123], v[180:183], v[212:215]\n"
"v_add_f32_e32 v97, v97, v117\n"
"v_add_f32_e32 v98, v98, v118\n"
"v_mfma_f32_16x16x32_bf16 v[212:215], a[116:119], v[176:179], v[212:215]\n"
"v_add_f32_e32 v99, v99, v119\n"
"v_add_f32_e32 v112, v112, v204\n"
"v_mfma_f32_16x16x32_bf16 v[212:215], a[112:115], v[124:127], v[212:215]\n"
"buffer_load_dwordx4 v[100:103], v1, s[4:7], s98 offen offset:1024 sc1\n"
"v_mfma_f32_16x16x32_bf16 v[212:215], a[108:111], v[132:135], v[212:215]\n"
"v_add_f32_e32 v113, v113, v205\n"
"v_add_f32_e32 v114, v114, v206\n"
"v_mfma_f32_16x16x32_bf16 v[212:215], a[104:107], v[128:131], v[212:215]\n"
"v_add_f32_e32 v115, v115, v207\n"
"v_add_f32_e32 v184, v184, v148\n"
"v_mfma_f32_16x16x32_bf16 v[212:215], a[100:103], v[108:111], v[212:215]\n"
"v_add_f32_e32 v185, v185, v149\n"
"v_add_f32_e32 v186, v186, v150\n"
"v_mfma_f32_16x16x32_bf16 v[212:215], a[96:99], v[92:95], v[212:215]\n"
"buffer_load_dwordx4 a[188:191], v1, s[4:7], s97 offen offset:3072 sc1\n"
"v_mfma_f32_16x16x32_bf16 v[168:171], v[24:27], v[92:95], v[168:171]\n"
"buffer_load_dwordx4 v[116:119], v1, s[4:7], s98 offen offset:2048 sc1\n"
"v_mfma_f32_16x16x32_bf16 v[168:171], v[28:31], v[108:111], v[168:171]\n"
"v_add_f32_e32 v187, v187, v151\n"
"v_mul_f32_e32 v112, v112, v255\n"
"v_mfma_f32_16x16x32_bf16 v[168:171], v[32:35], v[128:131], v[168:171]\n"
"v_mul_f32_e32 v113, v113, v255\n"
"v_mul_f32_e32 v114, v114, v255\n"
"v_mfma_f32_16x16x32_bf16 v[168:171], v[36:39], v[132:135], v[168:171]\n"
"v_mul_f32_e32 v115, v115, v255\n"
"v_mul_f32_e32 v184, v184, v255\n"
"v_mfma_f32_16x16x32_bf16 v[168:171], v[40:43], v[124:127], v[168:171]\n"
"s_waitcnt lgkmcnt(0)\n"
"v_mul_f32_e32 v185, v185, v255\n"
"v_mul_f32_e32 v186, v186, v255\n"
"v_mfma_f32_16x16x32_bf16 v[168:171], v[44:47], v[176:179], v[168:171]\n"
"buffer_load_dwordx4 v[148:151], v1, s[4:7], s98 offen offset:3072 sc1\n"
"v_mfma_f32_16x16x32_bf16 v[168:171], v[48:51], v[180:183], v[168:171]\n"
"s_add_u32 s98, s63, 0x84000\n"
"ds_read_b128 v[204:207], v6 offset:4096\n"
"ds_read_b128 v[220:223], v6 offset:5120\n"
"v_mfma_f32_16x16x32_bf16 v[168:171], v[52:55], v[188:191], v[168:171]\n"
"v_mul_f32_e32 v187, v187, v255\n"
"v_cvt_pk_bf16_f32 v228, v112, v113\n"
"v_mfma_f32_16x16x32_bf16 v[96:99], v[84:87], v[188:191], v[96:99]\n"
"buffer_store_dword v140, v14, s[8:11], s98 offen sc1\n"
"v_mfma_f32_16x16x32_bf16 v[96:99], v[80:83], v[180:183], v[96:99]\n"
"v_cvt_pk_bf16_f32 v229, v114, v115\n"
"v_cvt_pk_bf16_f32 v230, v184, v185\n"
"v_mfma_f32_16x16x32_bf16 v[96:99], v[76:79], v[176:179], v[96:99]\n"
"v_cvt_pk_bf16_f32 v231, v186, v187\n"
"v_add_f32_e32 v200, v200, v196\n"
"v_mfma_f32_16x16x32_bf16 v[96:99], v[72:75], v[124:127], v[96:99]\n"
"ds_write_b128 v8, v[228:231] offset:25600\n"
"v_mfma_f32_16x16x32_bf16 v[96:99], v[68:71], v[132:135], v[96:99]\n"
"v_add_f32_e32 v201, v201, v197\n"
"v_add_f32_e32 v202, v202, v198\n"
"v_mfma_f32_16x16x32_bf16 v[96:99], v[64:67], v[128:131], v[96:99]\n"
"v_add_f32_e32 v203, v203, v199\n"
"v_add_f32_e32 v212, v212, v216\n"
"v_mfma_f32_16x16x32_bf16 v[96:99], v[60:63], v[108:111], v[96:99]\n"
"v_add_f32_e32 v213, v213, v217\n"
"v_add_f32_e32 v214, v214, v218\n"
"v_mfma_f32_16x16x32_bf16 v[96:99], v[56:59], v[92:95], v[96:99]\n"
"v_add_f32_e32 v215, v215, v219\n"
"v_mul_f32_e32 v200, v200, v255\n"
"v_mul_f32_e32 v201, v201, v255\n"
"v_mul_f32_e32 v202, v202, v255\n"
"v_mul_f32_e32 v203, v203, v255\n"
"v_mul_f32_e32 v212, v212, v255\n"
"v_mul_f32_e32 v213, v213, v255\n"
"v_mul_f32_e32 v214, v214, v255\n"
"v_mul_f32_e32 v215, v215, v255\n"
"v_cvt_pk_bf16_f32 v92, v200, v201\n"
"v_cvt_pk_bf16_f32 v93, v202, v203\n"
"v_cvt_pk_bf16_f32 v94, v212, v213\n"
"v_cvt_pk_bf16_f32 v95, v214, v215\n"
"ds_write_b128 v7, v[92:95] offset:58368\n"
".Lmk_b2:\n"
"s_waitcnt vmcnt(17)\n"
"; @B G1 2.0\n"
"v_mfma_f32_16x16x32_bf16 v[108:111], a[0:3], v[136:139], 0\n"
"v_mfma_f32_16x16x32_bf16 v[108:111], a[4:7], v[88:91], v[108:111]\n"
"s_add_u32 s98, s62, 0x40800\n"
"v_mfma_f32_16x16x32_bf16 v[108:111], a[8:11], v[192:195], v[108:111]\n"
"s_add_u32 s97, s54, 0x408000\n"
"buffer_load_dwordx4 v[112:115], v0, s[4:7], s97 offen sc1\n"
"v_mfma_f32_16x16x32_bf16 v[108:111], a[12:15], v[160:163], v[108:111]\n"
"s_waitcnt vmcnt(12)\n"
"v_mfma_f32_16x16x32_bf16 v[108:111], a[16:19], v[156:159], v[108:111]\n"
"buffer_load_dword v255, v10, s[4:7], s98 offen sc1\n"
"v_mfma_f32_16x16x32_bf16 v[108:111], a[20:23], v[224:227], v[108:111]\n"
"s_waitcnt lgkmcnt(2)\n"
"v_add_f32_e32 v168, v168, v204\n"
"v_add_f32_e32 v169, v169, v205\n"
"v_mfma_f32_16x16x32_bf16 v[108:111], a[24:27], v[144:147], v[108:111]\n"
"v_add_f32_e32 v170, v170, v206\n"
"v_add_f32_e32 v171, v171, v207\n"
"v_mfma_f32_16x16x32_bf16 v[108:111], a[28:31], v[104:107], v[108:111]\n"
"buffer_load_dwordx4 v[124:127], v0, s[4:7], s97 offen offset:1024 sc1\n"
"v_mfma_f32_16x16x32_bf16 v[128:131], a[60:63], v[104:107], 0\n"
"s_add_u32 s96, s70, 0x204000\n"
"buffer_load_dwordx4 a[192:195], v0, s[4:7], s96 offen sc1\n"
"v_mfma_f32_16x16x32_bf16 v[128:131], a[56:59], v[144:147], v[128:131]\n"
"buffer_load_dword v254, v10, s[4:7], s98 offen offset:256 sc1\n"
"v_mfma_f32_16x16x32_bf16 v[128:131], a[52:55], v[224:227], v[128:131]\n"
"v_add_f32_e32 v96, v96, v220\n"
"v_add_f32_e32 v97, v97, v221\n"
"v_mfma_f32_16x16x32_bf16 v[128:131], a[48:51], v[156:159], v[128:131]\n"
"v_add_f32_e32 v98, v98, v222\n"
"v_add_f32_e32 v99, v99, v223\n"
"v_mfma_f32_16x16x32_bf16 v[128:131], a[44:47], v[160:163], v[128:131]\n"
"buffer_load_dwordx4 v[132:135], v0, s[4:7], s97 offen offset:2048 sc1\n"
"v_mfma_f32_16x16x32_bf16 v[128:131], a[40:43], v[192:195], v[128:131]\n"
"ds_write_b128 v3, v[108:111]\n"
"v_mfma_f32_16x16x32_bf16 v[128:131], a[36:39], v[88:91], v[128:131]\n"
"buffer_load_dword v252, v10, s[4:7], s98 offen offset:512 sc1\n"
"v_mfma_f32_16x16x32_bf16 v[128:131], a[32:35], v[136:139], v[128:131]\n"
"v_mul_f32_e32 v168, v168, v253\n"
"v_mul_f32_e32 v169, v169, v253\n"
"v_mfma_f32_16x16x32_bf16 v[108:111], a[64:67], v[136:139], 0\n"
"v_mul_f32_e32 v170, v170, v253\n"
"v_mul_f32_e32 v171, v171, v253\n"
"v_mfma_f32_16x16x32_bf16 v[108:111], a[68:71], v[88:91], v[108:111]\n"
"buffer_load_dwordx4 v[140:143], v0, s[4:7], s97 offen offset:3072 sc1\n"
"v_mfma_f32_16x16x32_bf16 v[108:111], a[72:75], v[192:195], v[108:111]\n"
"buffer_load_dword v251, v10, s[4:7], s98 offen offset:768 sc1\n"
"v_mfma_f32_16x16x32_bf16 v[108:111], a[76:79], v[160:163], v[108:111]\n"
"v_mul_f32_e32 v96, v96, v253\n"
"v_mul_f32_e32 v97, v97, v253\n"
"v_mfma_f32_16x16x32_bf16 v[108:111], a[80:83], v[156:159], v[108:111]\n"
"v_mul_f32_e32 v98, v98, v253\n"
"v_mul_f32_e32 v99, v99, v253\n"
"v_mfma_f32_16x16x32_bf16 v[108:111], a[84:87], v[224:227], v[108:111]\n"
"ds_write_b128 v3, v[128:131] offset:1024\n"
"v_mfma_f32_16x16x32_bf16 v[108:111], a[88:91], v[144:147], v[108:111]\n"
"buffer_load_dwordx4 v[128:131], v1, s[4:7], s97 offen sc1\n"
"v_mfma_f32_16x16x32_bf16 v[108:111], a[92:95], v[104:107], v[108:111]\n"
"buffer_load_dword v253, v10, s[4:7], s98 offen offset:1024 sc1\n"
"v_mfma_f32_16x16x32_bf16 v[176:179], a[124:127], v[104:107], 0\n"
"v_cvt_pk_bf16_f32 v180, v168, v169\n"
"v_cvt_pk_bf16_f32 v181, v170, v171\n"
"v_mfma_f32_16x16x32_bf16 v[176:179], a[120:123], v[144:147], v[176:179]\n"
"v_cvt_pk_bf16_f32 v182, v96, v97\n"
"v_cvt_pk_bf16_f32 v183, v98, v99\n"
"v_mfma_f32_16x16x32_bf16 v[176:179], a[116:119], v[224:227], v[176:179]\n"
"buffer_load_dword v250, v10, s[4:7], s98 offen offset:1280 sc1\n"
"v_mfma_f32_16x16x32_bf16 v[176:179], a[112:115], v[156:159], v[176:179]\n"
"buffer_load_dwordx4 v[96:99], v1, s[4:7], s97 offen offset:1024 sc1\n"
"v_mfma_f32_16x16x32_bf16 v[176:179], a[108:111], v[160:163], v[176:179]\n"
"buffer_load_dwordx4 a[196:199], v0, s[4:7], s96 offen offset:1024 sc1\n"
"v_mfma_f32_16x16x32_bf16 v[176:179], a[104:107], v[192:195], v[176:179]\n"
"ds_write_b128 v3, v[108:111] offset:2048\n"
"v_mfma_f32_16x16x32_bf16 v[176:179], a[100:103], v[88:91], v[176:179]\n"
"buffer_load_dword v249, v10, s[4:7], s98 offen offset:1536 sc1\n"
"v_mfma_f32_16x16x32_bf16 v[176:179], a[96:99], v[136:139], v[176:179]\n"
"ds_write_b128 v2, v[180:183] offset:50176\n"
"v_mfma_f32_16x16x32_bf16 v[108:111], v[24:27], v[136:139], 0\n"
"buffer_load_dwordx4 v[168:171], v1, s[4:7], s97 offen offset:2048 sc1\n"
"s_add_u32 s95, s64, 0x802000\n"
"v_mfma_f32_16x16x32_bf16 v[108:111], v[28:31], v[88:91], v[108:111]\n"
"buffer_load_dword v248, v10, s[4:7], s98 offen offset:1792 sc1\n"
"v_add_u32_e32 v247, 0x26c00, v0\n"
"v_mfma_f32_16x16x32_bf16 v[108:111], v[32:35], v[192:195], v[108:111]\n"
"buffer_store_dwordx4 v[228:231], v8, s[12:15], s95 offen offset:1024 sc1\n"
"v_mfma_f32_16x16x32_bf16 v[108:111], v[36:39], v[160:163], v[108:111]\n"
"ds_read_b128 v[180:183], v247\n"
"ds_read_b128 v[184:187], v247 offset:1024\n"
"v_xor_b32_e32 v247, 32, v15\n"
"v_mfma_f32_16x16x32_bf16 v[108:111], v[40:43], v[156:159], v[108:111]\n"
"ds_read_b128 v[188:191], v2 offset:50176\n"
"ds_read_b128 v[196:199], v15 offset:24576\n"
"ds_read_b128 v[200:203], v247 offset:24832\n"
"v_mfma_f32_16x16x32_bf16 v[108:111], v[44:47], v[224:227], v[108:111]\n"
"buffer_load_dwordx4 v[204:207], v1, s[4:7], s97 offen offset:3072 sc1\n"
"v_mfma_f32_16x16x32_bf16 v[108:111], v[48:51], v[144:147], v[108:111]\n"
"ds_write_b128 v3, v[176:179] offset:3072\n"
"v_mfma_f32_16x16x32_bf16 v[108:111], v[52:55], v[104:107], v[108:111]\n"
"v_mfma_f32_16x16x32_bf16 v[176:179], v[84:87], v[104:107], 0\n"
"v_mfma_f32_16x16x32_bf16 v[176:179], v[80:83], v[144:147], v[176:179]\n"
"s_waitcnt lgkmcnt(1)\n"
"v_mfma_f32_16x16x32_bf16 v[180:183], v[196:199], v[188:191], v[180:183]\n"
"v_mfma_f32_16x16x32_bf16 v[184:187], v[200:203], v[188:191], v[184:187]\n"
"ds_read_b64_tr_b16 v[104:105], v16 offset:24576\n"
"v_xor_b32_e32 v247, 32, v16\n"
"v_mfma_f32_16x16x32_bf16 v[176:179], v[76:79], v[224:227], v[176:179]\n"
"ds_read_b64_tr_b16 v[106:107], v247 offset:24832\n"
"ds_read_b64_tr_b16 v[144:145], v247 offset:24576\n"
"ds_read_b64_tr_b16 v[146:147], v16 offset:24832\n"
"v_mov_b32_e32 v188, 0x3f803f80\n"
"v_mfma_f32_16x16x32_bf16 v[176:179], v[72:75], v[156:159], v[176:179]\n"
"v_mov_b32_e32 v189, 0x3f803f80\n"
"v_mov_b32_e32 v190, 0x3f803f80\n"
"v_mov_b32_e32 v191, 0x3f803f80\n"
"v_mfma_f32_16x16x32_bf16 v[176:179], v[68:71], v[160:163], v[176:179]\n"
"v_max_f32_e32 v247, v180, v181\n"
"v_max3_f32 v247, v247, v182, v183\n"
"v_max3_f32 v247, v247, v184, v185\n"
"v_mfma_f32_16x16x32_bf16 v[176:179], v[64:67], v[192:195], v[176:179]\n"
"ds_write_b128 v3, v[108:111] offset:4096\n"
"v_mfma_f32_16x16x32_bf16 v[176:179], v[60:63], v[88:91], v[176:179]\n"
"v_max3_f32 v247, v247, v186, v187\n"
"v_mov_b32_e32 v246, v247\n"
"v_mfma_f32_16x16x32_bf16 v[176:179], v[56:59], v[136:139], v[176:179]\n"
"s_waitcnt vmcnt(25)\n"
"v_permlane16_swap_b32_e32 v247, v246\n"
"v_max_f32_e32 v247, v247, v246\n"
"v_mov_b32_e32 v246, v247\n"
"; @B G1 2.1\n"
"v_mfma_f32_16x16x32_bf16 v[88:91], a[0:3], v[120:123], 0\n"
"buffer_load_dwordx4 a[200:203], v0, s[4:7], s96 offen offset:2048 sc1\n"
"v_permlane32_swap_b32_e32 v247, v246\n"
"v_mfma_f32_16x16x32_bf16 v[88:91], a[4:7], v[164:167], v[88:91]\n"
"v_max_f32_e32 v247, v247, v246\n"
"v_sub_f32_e32 v180, v180, v247\n"
"v_sub_f32_e32 v181, v181, v247\n"
"v_mfma_f32_16x16x32_bf16 v[88:91], a[8:11], v[172:175], v[88:91]\n"
"s_add_u32 s98, s55, 0x408000\n"
"buffer_load_dwordx4 v[108:111], v0, s[4:7], s98 offen sc1\n"
"v_mfma_f32_16x16x32_bf16 v[88:91], a[12:15], v[208:211], v[88:91]\n"
"s_waitcnt vmcnt(22)\n"
"v_sub_f32_e32 v182, v182, v247\n"
"v_sub_f32_e32 v183, v183, v247\n"
"v_sub_f32_e32 v184, v184, v247\n"
"v_mfma_f32_16x16x32_bf16 v[88:91], a[16:19], v[152:155], v[88:91]\n"
"v_sub_f32_e32 v185, v185, v247\n"
"v_sub_f32_e32 v186, v186, v247\n"
"v_sub_f32_e32 v187, v187, v247\n"
"v_mfma_f32_16x16x32_bf16 v[88:91], a[20:23], v[100:103], v[88:91]\n"
"ds_write_b128 v3, v[176:179] offset:5120\n"
"v_mfma_f32_16x16x32_bf16 v[88:91], a[24:27], v[116:119], v[88:91]\n"
"v_exp_f32_e32 v180, v180\n"
"v_mfma_f32_16x16x32_bf16 v[88:91], a[28:31], v[148:151], v[88:91]\n"
"buffer_load_dwordx4 v[136:139], v0, s[4:7], s98 offen offset:1024 sc1\n"
"v_mfma_f32_16x16x32_bf16 v[156:159], a[60:63], v[148:151], 0\n"
"s_add_u32 s97, s64, 0x806000\n"
"v_exp_f32_e32 v181, v181\n"
"v_mfma_f32_16x16x32_bf16 v[156:159], a[56:59], v[116:119], v[156:159]\n"
"v_exp_f32_e32 v182, v182\n"
"v_mfma_f32_16x16x32_bf16 v[156:159], a[52:55], v[100:103], v[156:159]\n"
"buffer_store_dwordx4 v[92:95], v7, s[12:15], s97 offen offset:1024 sc1\n"
"v_mfma_f32_16x16x32_bf16 v[156:159], a[48:51], v[152:155], v[156:159]\n"
"v_exp_f32_e32 v183, v183\n"
"v_mfma_f32_16x16x32_bf16 v[156:159], a[44:47], v[208:211], v[156:159]\n"
"buffer_load_dwordx4 v[92:95], v0, s[4:7], s98 offen offset:2048 sc1\n"
"v_mfma_f32_16x16x32_bf16 v[156:159], a[40:43], v[172:175], v[156:159]\n"
"ds_write_b128 v3, v[88:91] offset:6144\n"
"v_mfma_f32_16x16x32_bf16 v[156:159], a[36:39], v[164:167], v[156:159]\n"
"v_exp_f32_e32 v184, v184\n"
"v_mfma_f32_16x16x32_bf16 v[156:159], a[32:35], v[120:123], v[156:159]\n"
"v_exp_f32_e32 v185, v185\n"
"v_mfma_f32_16x16x32_bf16 v[88:91], a[64:67], v[120:123], 0\n"
"v_exp_f32_e32 v186, v186\n"
"v_mfma_f32_16x16x32_bf16 v[88:91], a[68:71], v[164:167], v[88:91]\n"
"buffer_load_dwordx4 v[160:163], v0, s[4:7], s98 offen offset:3072 sc1\n"
"v_mfma_f32_16x16x32_bf16 v[88:91], a[72:75], v[172:175], v[88:91]\n"
"v_exp_f32_e32 v187, v187\n"
"v_cvt_pk_bf16_f32 v176, v180, v181\n"
"v_mfma_f32_16x16x32_bf16 v[88:91], a[76:79], v[208:211], v[88:91]\n"
"v_cvt_pk_bf16_f32 v177, v182, v183\n"
"v_cvt_pk_bf16_f32 v178, v184, v185\n"
"v_cvt_pk_bf16_f32 v179, v186, v187\n"
"v_mfma_f32_16x16x32_bf16 v[88:91], a[80:83], v[152:155], v[88:91]\n"
"s_waitcnt lgkmcnt(3)\n"
"v_mov_b32_e32 v180, -1\n"
"v_mov_b32_e32 v181, -1\n"
"v_mfma_f32_16x16x32_bf16 v[184:187], v[188:191], v[176:179], 0\n"
"v_mfma_f32_16x16x32_bf16 v[188:191], v[104:107], v[176:179], 0\n"
"v_mfma_f32_16x16x32_bf16 v[104:107], v[144:147], v[176:179], 0\n"
"v_mfma_f32_16x16x32_bf16 v[88:91], a[84:87], v[100:103], v[88:91]\n"
"ds_write_b128 v3, v[156:159] offset:7168\n"
"v_mfma_f32_16x16x32_bf16 v[88:91], a[88:91], v[116:119], v[88:91]\n"
"buffer_load_dwordx4 v[144:147], v1, s[4:7], s98 offen sc1\n"
"v_mfma_f32_16x16x32_bf16 v[88:91], a[92:95], v[148:151], v[88:91]\n"
"v_mov_b32_e32 v182, -1\n"
"v_mov_b32_e32 v183, -1\n"
"v_mfma_f32_16x16x32_bf16 v[156:159], a[124:127], v[148:151], 0\n"
"s_add_u32 s97, s60, 0x848000\n"
"buffer_store_dwordx4 v[180:183], v0, s[8:11], s97 offen sc1\n"
"v_mfma_f32_16x16x32_bf16 v[156:159], a[120:123], v[116:119], v[156:159]\n"
"buffer_load_dwordx4 a[204:207], v0, s[4:7], s96 offen offset:3072 sc1\n"
"v_mfma_f32_16x16x32_bf16 v[156:159], a[116:119], v[100:103], v[156:159]\n"
"v_rcp_f32_e32 v184, v184\n"
"v_mfma_f32_16x16x32_bf16 v[156:159], a[112:115], v[152:155], v[156:159]\n"
"buffer_load_dwordx4 v[176:179], v1, s[4:7], s98 offen offset:1024 sc1\n"
"v_mul_f32_e32 v188, v188, v184\n"
"v_mfma_f32_16x16x32_bf16 v[156:159], a[108:111], v[208:211], v[156:159]\n"
"v_mul_f32_e32 v189, v189, v184\n"
"v_mul_f32_e32 v190, v190, v184\n"
"v_mul_f32_e32 v191, v191, v184\n"
"v_mfma_f32_16x16x32_bf16 v[156:159], a[104:107], v[172:175], v[156:159]\n"
"ds_write_b128 v3, v[88:91] offset:8192\n"
"v_mfma_f32_16x16x32_bf16 v[156:159], a[100:103], v[164:167], v[156:159]\n"
"v_mul_f32_e32 v104, v104, v184\n"
"v_mul_f32_e32 v105, v105, v184\n"
"v_mul_f32_e32 v106, v106, v184\n"
"v_mfma_f32_16x16x32_bf16 v[156:159], a[96:99], v[120:123], v[156:159]\n"
"v_mul_f32_e32 v107, v107, v184\n"
"v_cvt_pk_bf16_f32 v88, v188, v189\n"
"v_cvt_pk_bf16_f32 v89, v190, v191\n"
"v_mfma_f32_16x16x32_bf16 v[184:187], v[24:27], v[120:123], 0\n"
"buffer_load_dwordx4 v[188:191], v1, s[4:7], s98 offen offset:2048 sc1\n"
"v_cvt_pk_bf16_f32 v90, v104, v105\n"
"v_mfma_f32_16x16x32_bf16 v[184:187], v[28:31], v[164:167], v[184:187]\n"
"s_add_u32 s97, s60, 0x448000\n"
"v_cvt_pk_bf16_f32 v91, v106, v107\n"
"v_mfma_f32_16x16x32_bf16 v[184:187], v[32:35], v[172:175], v[184:187]\n"
"buffer_store_dwordx4 v[88:91], v0, s[4:7], s97 offen sc1\n"
"v_mfma_f32_16x16x32_bf16 v[184:187], v[36:39], v[208:211], v[184:187]\n"
"v_mfma_f32_16x16x32_bf16 v[184:187], v[40:43], v[152:155], v[184:187]\n"
"s_waitcnt vmcnt(14)\n"
"v_add_f32_e32 v255, v255, v253\n"
"v_add_f32_e32 v254, v254, v250\n"
"v_mfma_f32_16x16x32_bf16 v[184:187], v[44:47], v[100:103], v[184:187]\n"
"buffer_load_dwordx4 v[88:91], v1, s[4:7], s98 offen offset:3072 sc1\n"
"v_mfma_f32_16x16x32_bf16 v[184:187], v[48:51], v[116:119], v[184:187]\n"
"ds_write_b128 v3, v[156:159] offset:9216\n"
"v_mfma_f32_16x16x32_bf16 v[184:187], v[52:55], v[148:151], v[184:187]\n"
"v_add_f32_e32 v252, v252, v249\n"
"v_add_f32_e32 v251, v251, v248\n"
"v_mfma_f32_16x16x32_bf16 v[104:107], v[84:87], v[148:151], 0\n"
"v_add_f32_e32 v255, v255, v252\n"
"v_add_f32_e32 v254, v254, v251\n"
"v_mfma_f32_16x16x32_bf16 v[104:107], v[80:83], v[116:119], v[104:107]\n"
"v_add_f32_e32 v255, v255, v254\n"
"v_mov_b32_e32 v254, v255\n"
"v_mfma_f32_16x16x32_bf16 v[104:107], v[76:79], v[100:103], v[104:107]\n"
"s_nop 0\n"
"v_permlane32_swap_b32_e32 v255, v254\n"
"v_add_f32_e32 v255, v255, v254\n"
"v_mfma_f32_16x16x32_bf16 v[104:107], v[72:75], v[152:155], v[104:107]\n"
"v_mul_f32_e32 v255, 0x3a800000, v255\n"
"v_mov_b32_e32 v254, v255\n"
"v_mfma_f32_16x16x32_bf16 v[104:107], v[68:71], v[208:211], v[104:107]\n"
"s_nop 0\n"
"v_permlane16_swap_b32_e32 v255, v254\n"
"v_cvt_pk_bf16_f32 v253, -v255, 0\n"
"v_mfma_f32_16x16x32_bf16 v[104:107], v[64:67], v[172:175], v[104:107]\n"
"ds_write_b128 v3, v[184:187] offset:10240\n"
"v_mfma_f32_16x16x32_bf16 v[104:107], v[60:63], v[164:167], v[104:107]\n"
"buffer_load_dwordx4 a[208:211], v1, s[4:7], s96 offen sc1\n"
"v_mfma_f32_16x16x32_bf16 v[104:107], v[56:59], v[120:123], v[104:107]\n"
"v_fma_f32 v254, -v255, v255, v254\n"
"v_lshlrev_b32_e32 v253, 16, v253\n"
"; @B G1 2.2\n"
"v_mfma_f32_16x16x32_bf16 v[100:103], a[0:3], v[112:115], 0\n"
"v_max_f32_e32 v254, 0, v254\n"
"v_cvt_pk_bf16_f32 v254, v254, 0\n"
"v_mfma_f32_16x16x32_bf16 v[100:103], a[4:7], v[124:127], v[100:103]\n"
"v_lshlrev_b32_e32 v254, 16, v254\n"
"v_add_f32_e32 v254, 0x3727c5ac, v254\n"
"v_mfma_f32_16x16x32_bf16 v[100:103], a[8:11], v[132:135], v[100:103]\n"
"v_cvt_pk_bf16_f32 v254, v254, 0\n"
"v_lshlrev_b32_e32 v254, 16, v254\n"
"v_mfma_f32_16x16x32_bf16 v[100:103], a[12:15], v[140:143], v[100:103]\n"
"s_waitcnt vmcnt(14)\n"
"v_sqrt_f32_e32 v254, v254\n"
"v_mfma_f32_16x16x32_bf16 v[100:103], a[16:19], v[128:131], v[100:103]\n"
"s_add_u32 s98, s68, 0x10000\n"
"v_cvt_pk_bf16_f32 v254, v254, 0\n"
"v_mfma_f32_16x16x32_bf16 v[100:103], a[20:23], v[96:99], v[100:103]\n"
"ds_write_b128 v3, v[104:107] offset:11264\n"
"v_mfma_f32_16x16x32_bf16 v[100:103], a[24:27], v[168:171], v[100:103]\n"
"buffer_store_dwordx2 v[18:19], v11, s[4:7], s98 offen offset:32 sc1\n"
"v_mfma_f32_16x16x32_bf16 v[100:103], a[28:31], v[204:207], v[100:103]\n"
"v_lshlrev_b32_e32 v254, 16, v254\n"
"v_mfma_f32_16x16x32_bf16 v[104:107], a[60:63], v[204:207], 0\n"
"v_rcp_f32_e32 v255, v254\n"
"v_mfma_f32_16x16x32_bf16 v[104:107], a[56:59], v[168:171], v[104:107]\n"
"v_mul_f32_e32 v254, 0x3e8293ee, v255\n"
"v_mfma_f32_16x16x32_bf16 v[104:107], a[52:55], v[96:99], v[104:107]\n"
"s_add_u32 s98, s52, 0x400000\n"
"buffer_load_dwordx4 v[116:119], v0, s[4:7], s98 offen sc1\n"
"v_mfma_f32_16x16x32_bf16 v[104:107], a[48:51], v[128:131], v[104:107]\n"
"s_waitcnt lgkmcnt(0)\n"
"s_barrier\n"
"v_mfma_f32_16x16x32_bf16 v[104:107], a[44:47], v[140:143], v[104:107]\n"
"ds_read_b128 v[120:123], v4\n"
"ds_read_b128 v[148:151], v9 offset:640\n"
"v_mfma_f32_16x16x32_bf16 v[104:107], a[40:43], v[132:135], v[104:107]\n"
"ds_write_b128 v3, v[100:103] offset:12288\n"
"v_mfma_f32_16x16x32_bf16 v[104:107], a[36:39], v[124:127], v[104:107]\n"
"ds_read_b128 v[100:103], v5\n"
"v_mfma_f32_16x16x32_bf16 v[104:107], a[32:35], v[112:115], v[104:107]\n"
"buffer_load_dwordx4 v[152:155], v0, s[4:7], s98 offen offset:1024 sc1\n"
"v_mfma_f32_16x16x32_bf16 v[156:159], a[64:67], v[112:115], 0\n"
"v_mfma_f32_16x16x32_bf16 v[156:159], a[68:71], v[124:127], v[156:159]\n"
"v_mfma_f32_16x16x32_bf16 v[156:159], a[72:75], v[132:135], v[156:159]\n"
"buffer_load_dwordx4 a[212:215], v1, s[4:7], s96 offen offset:1024 sc1\n"
"v_mfma_f32_16x16x32_bf16 v[156:159], a[76:79], v[140:143], v[156:159]\n"
"ds_read_b128 v[164:167], v4 offset:1024\n"
"ds_read_b128 v[172:175], v9 offset:704\n"
"v_mfma_f32_16x16x32_bf16 v[156:159], a[80:83], v[128:131], v[156:159]\n"
"buffer_load_dwordx4 v[184:187], v0, s[4:7], s98 offen offset:2048 sc1\n"
"v_mfma_f32_16x16x32_bf16 v[156:159], a[84:87], v[96:99], v[156:159]\n"
"ds_write_b128 v3, v[104:107] offset:13312\n"
"v_mfma_f32_16x16x32_bf16 v[156:159], a[88:91], v[168:171], v[156:159]\n"
"ds_read_b128 v[104:107], v5 offset:1024\n"
"s_waitcnt lgkmcnt(4)\n"
"v_mfma_f32_16x16x32_bf16 v[156:159], a[92:95], v[204:207], v[156:159]\n"
"v_fmac_f32_e32 v120, v253, v148\n"
"v_fmac_f32_e32 v121, v253, v149\n"
"v_mfma_f32_16x16x32_bf16 v[192:195], a[124:127], v[204:207], 0\n"
"v_fmac_f32_e32 v122, v253, v150\n"
"v_fmac_f32_e32 v123, v253, v151\n"
"v_mfma_f32_16x16x32_bf16 v[192:195], a[120:123], v[168:171], v[192:195]\n"
"buffer_load_dwordx4 v[148:151], v0, s[4:7], s98 offen offset:3072 sc1\n"
"v_mfma_f32_16x16x32_bf16 v[192:195], a[116:119], v[96:99], v[192:195]\n"
"ds_read_b128 v[196:199], v4 offset:2048\n"
"ds_read_b128 v[200:203], v9 offset:768\n"
"v_mfma_f32_16x16x32_bf16 v[192:195], a[112:115], v[128:131], v[192:195]\n"
"ds_read_b128 v[208:211], v5 offset:2048\n"
"v_add_f32_e32 v120, v120, v100\n"
"v_mfma_f32_16x16x32_bf16 v[192:195], a[108:111], v[140:143], v[192:195]\n"
"v_add_f32_e32 v121, v121, v101\n"
"v_add_f32_e32 v122, v122, v102\n"
"v_mfma_f32_16x16x32_bf16 v[192:195], a[104:107], v[132:135], v[192:195]\n"
"ds_write_b128 v3, v[156:159] offset:14336\n"
"v_mfma_f32_16x16x32_bf16 v[192:195], a[100:103], v[124:127], v[192:195]\n"
"buffer_load_dwordx4 v[156:159], v1, s[4:7], s98 offen sc1\n"
"v_mfma_f32_16x16x32_bf16 v[192:195], a[96:99], v[112:115], v[192:195]\n"
"s_add_u32 s97, s60, 0x840000\n"
"v_add_f32_e32 v123, v123, v103\n"
"s_waitcnt lgkmcnt(4)\n"
"v_mfma_f32_16x16x32_bf16 v[100:103], v[24:27], v[112:115], 0\n"
"buffer_store_dwordx4 v[180:183], v0, s[8:11], s97 offen sc1\n"
"v_mfma_f32_16x16x32_bf16 v[100:103], v[28:31], v[124:127], v[100:103]\n"
"ds_read_b128 v[212:215], v4 offset:3072\n"
"ds_read_b128 v[216:219], v9 offset:832\n"
"v_mfma_f32_16x16x32_bf16 v[100:103], v[32:35], v[132:135], v[100:103]\n"
"s_waitcnt vmcnt(18)\n"
"s_add_u32 s97, s68, 0x11000\n"
"ds_read_b128 v[220:223], v5 offset:3072\n"
"v_mfma_f32_16x16x32_bf16 v[100:103], v[36:39], v[140:143], v[100:103]\n"
"buffer_load_dwordx4 v[224:227], v1, s[4:7], s98 offen offset:1024 sc1\n"
"v_mfma_f32_16x16x32_bf16 v[100:103], v[40:43], v[128:131], v[100:103]\n"
"buffer_store_dwordx2 v[18:19], v11, s[12:15], s97 offen offset:64 sc1\n"
"v_mfma_f32_16x16x32_bf16 v[100:103], v[44:47], v[96:99], v[100:103]\n"
"ds_write_b128 v3, v[192:195] offset:15360\n"
"v_mfma_f32_16x16x32_bf16 v[100:103], v[48:51], v[168:171], v[100:103]\n"
"buffer_load_dwordx4 a[216:219], v1, s[4:7], s96 offen offset:2048 sc1\n"
"v_mfma_f32_16x16x32_bf16 v[100:103], v[52:55], v[204:207], v[100:103]\n"
"v_fmac_f32_e32 v164, v253, v172\n"
"v_fmac_f32_e32 v165, v253, v173\n"
"v_mfma_f32_16x16x32_bf16 v[192:195], v[84:87], v[204:207], 0\n"
"buffer_load_dwordx4 v[204:207], v1, s[4:7], s98 offen offset:2048 sc1\n"
"v_mfma_f32_16x16x32_bf16 v[192:195], v[80:83], v[168:171], v[192:195]\n"
"ds_read_b128 v[168:171], v4 offset:4096\n"
"ds_read_b128 v[228:231], v9 offset:512\n"
"v_mfma_f32_16x16x32_bf16 v[192:195], v[76:79], v[96:99], v[192:195]\n"
"ds_read_b128 v[96:99], v5 offset:4096\n"
"v_fmac_f32_e32 v166, v253, v174\n"
"v_mfma_f32_16x16x32_bf16 v[192:195], v[72:75], v[128:131], v[192:195]\n"
"v_fmac_f32_e32 v167, v253, v175\n"
"v_add_f32_e32 v164, v164, v104\n"
"v_mfma_f32_16x16x32_bf16 v[192:195], v[68:71], v[140:143], v[192:195]\n"
"v_add_f32_e32 v165, v165, v105\n"
"v_add_f32_e32 v166, v166, v106\n"
"v_mfma_f32_16x16x32_bf16 v[192:195], v[64:67], v[132:135], v[192:195]\n"
"buffer_load_dwordx4 v[128:131], v1, s[4:7], s98 offen offset:3072 sc1\n"
"v_mfma_f32_16x16x32_bf16 v[192:195], v[60:63], v[124:127], v[192:195]\n"
"ds_write_b128 v3, v[100:103] offset:16384\n"
"v_mfma_f32_16x16x32_bf16 v[192:195], v[56:59], v[112:115], v[192:195]\n"
"s_waitcnt vmcnt(21)\n"
"ds_read_b128 v[100:103], v4 offset:5120\n"
"ds_read_b128 v[112:115], v9 offset:576\n"
"; @B G1 2.3\n"
"v_mfma_f32_16x16x32_bf16 v[120:123], a[0:3], v[108:111], v[120:123]\n"
"ds_read_b128 v[124:127], v5 offset:5120\n"
"v_add_f32_e32 v167, v167, v107\n"
"v_mfma_f32_16x16x32_bf16 v[120:123], a[4:7], v[136:139], v[120:123]\n"
"s_waitcnt lgkmcnt(12)\n"
"v_fmac_f32_e32 v196, v253, v200\n"
"v_fmac_f32_e32 v197, v253, v201\n"
"v_mfma_f32_16x16x32_bf16 v[120:123], a[8:11], v[92:95], v[120:123]\n"
"s_add_u32 s98, s53, 0x400000\n"
"buffer_load_dwordx4 v[104:107], v0, s[4:7], s98 offen sc1\n"
"v_mfma_f32_16x16x32_bf16 v[120:123], a[12:15], v[160:163], v[120:123]\n"
"s_waitcnt vmcnt(15)\n"
"v_fmac_f32_e32 v198, v253, v202\n"
"v_fmac_f32_e32 v199, v253, v203\n"
"v_mfma_f32_16x16x32_bf16 v[120:123], a[16:19], v[144:147], v[120:123]\n"
"v_add_f32_e32 v196, v196, v208\n"
"v_add_f32_e32 v197, v197, v209\n"
"v_mfma_f32_16x16x32_bf16 v[120:123], a[20:23], v[176:179], v[120:123]\n"
"ds_write_b128 v3, v[192:195] offset:17408\n"
"v_mfma_f32_16x16x32_bf16 v[120:123], a[24:27], v[188:191], v[120:123]\n"
"s_waitcnt lgkmcnt(9)\n"
"v_add_f32_e32 v198, v198, v210\n"
"v_add_f32_e32 v199, v199, v211\n"
"v_mfma_f32_16x16x32_bf16 v[120:123], a[28:31], v[88:91], v[120:123]\n"
"buffer_load_dwordx4 v[132:135], v0, s[4:7], s98 offen offset:1024 sc1\n"
"v_mfma_f32_16x16x32_bf16 v[164:167], a[60:63], v[88:91], v[164:167]\n"
"v_fmac_f32_e32 v212, v253, v216\n"
"v_fmac_f32_e32 v213, v253, v217\n"
"v_mfma_f32_16x16x32_bf16 v[164:167], a[56:59], v[188:191], v[164:167]\n"
"v_fmac_f32_e32 v214, v253, v218\n"
"v_fmac_f32_e32 v215, v253, v219\n"
"v_mfma_f32_16x16x32_bf16 v[164:167], a[52:55], v[176:179], v[164:167]\n"
"s_waitcnt lgkmcnt(0)\n"
"s_barrier\n"
"v_mfma_f32_16x16x32_bf16 v[164:167], a[48:51], v[144:147], v[164:167]\n"
"buffer_load_dwordx4 a[220:223], v1, s[4:7], s96 offen offset:3072 sc1\n"
"v_mfma_f32_16x16x32_bf16 v[164:167], a[44:47], v[160:163], v[164:167]\n"
"buffer_load_dwordx4 v[140:143], v0, s[4:7], s98 offen offset:2048 sc1\n"
"v_mfma_f32_16x16x32_bf16 v[164:167], a[40:43], v[92:95], v[164:167]\n"
"ds_read_b128 v[172:175], v6\n"
"ds_read_b128 v[192:195], v6 offset:1024\n"
"v_mfma_f32_16x16x32_bf16 v[164:167], a[36:39], v[136:139], v[164:167]\n"
"v_add_f32_e32 v212, v212, v220\n"
"v_add_f32_e32 v213, v213, v221\n"
"v_mfma_f32_16x16x32_bf16 v[164:167], a[32:35], v[108:111], v[164:167]\n"
"v_add_f32_e32 v214, v214, v222\n"
"v_add_f32_e32 v215, v215, v223\n"
"v_mfma_f32_16x16x32_bf16 v[196:199], a[64:67], v[108:111], v[196:199]\n"
"v_fmac_f32_e32 v168, v253, v228\n"
"v_fmac_f32_e32 v169, v253, v229\n"
"v_mfma_f32_16x16x32_bf16 v[196:199], a[68:71], v[136:139], v[196:199]\n"
"buffer_load_dwordx4 v[200:203], v0, s[4:7], s98 offen offset:3072 sc1\n"
"v_mfma_f32_16x16x32_bf16 v[196:199], a[72:75], v[92:95], v[196:199]\n"
"v_fmac_f32_e32 v170, v253, v230\n"
"v_fmac_f32_e32 v171, v253, v231\n"
"v_mfma_f32_16x16x32_bf16 v[196:199], a[76:79], v[160:163], v[196:199]\n"
"v_add_f32_e32 v168, v168, v96\n"
"v_add_f32_e32 v169, v169, v97\n"
"v_mfma_f32_16x16x32_bf16 v[196:199], a[80:83], v[144:147], v[196:199]\n"
"v_add_f32_e32 v170, v170, v98\n"
"v_add_f32_e32 v171, v171, v99\n"
"v_mfma_f32_16x16x32_bf16 v[196:199], a[84:87], v[176:179], v[196:199]\n"
"s_waitcnt lgkmcnt(0)\n"
"v_fmac_f32_e32 v100, v253, v112\n"
"v_fmac_f32_e32 v101, v253, v113\n"
"v_mfma_f32_16x16x32_bf16 v[196:199], a[88:91], v[188:191], v[196:199]\n"
"buffer_load_dwordx4 v[96:99], v1, s[4:7], s98 offen sc1\n"
"v_mfma_f32_16x16x32_bf16 v[196:199], a[92:95], v[88:91], v[196:199]\n"
"ds_read_b128 v[208:211], v6 offset:2048\n"
"ds_read_b128 v[216:219], v6 offset:3072\n"
"v_mfma_f32_16x16x32_bf16 v[212:215], a[124:127], v[88:91], v[212:215]\n"
"v_fmac_f32_e32 v102, v253, v114\n"
"v_fmac_f32_e32 v103, v253, v115\n"
"v_mfma_f32_16x16x32_bf16 v[212:215], a[120:123], v[188:191], v[212:215]\n"
"v_add_f32_e32 v100, v100, v124\n"
"v_add_f32_e32 v101, v101, v125\n"
"v_mfma_f32_16x16x32_bf16 v[212:215], a[116:119], v[176:179], v[212:215]\n"
"v_add_f32_e32 v102, v102, v126\n"
"v_add_f32_e32 v103, v103, v127\n"
"v_mfma_f32_16x16x32_bf16 v[212:215], a[112:115], v[144:147], v[212:215]\n"
"buffer_load_dwordx4 v[112:115], v1, s[4:7], s98 offen offset:1024 sc1\n"
"v_mfma_f32_16x16x32_bf16 v[212:215], a[108:111], v[160:163], v[212:215]\n"
"v_add_f32_e32 v120, v120, v172\n"
"v_add_f32_e32 v121, v121, v173\n"
"v_mfma_f32_16x16x32_bf16 v[212:215], a[104:107], v[92:95], v[212:215]\n"
"s_add_u32 s97, s70, 0x206000\n"
"v_add_f32_e32 v122, v122, v174\n"
"v_add_f32_e32 v123, v123, v175\n"
"v_mfma_f32_16x16x32_bf16 v[212:215], a[100:103], v[136:139], v[212:215]\n"
"v_add_f32_e32 v164, v164, v192\n"
"v_add_f32_e32 v165, v165, v193\n"
"v_mfma_f32_16x16x32_bf16 v[212:215], a[96:99], v[108:111], v[212:215]\n"
"buffer_load_dwordx4 a[224:227], v0, s[4:7], s97 offen sc1\n"
"v_mfma_f32_16x16x32_bf16 v[168:171], v[24:27], v[108:111], v[168:171]\n"
"buffer_load_dwordx4 v[124:127], v1, s[4:7], s98 offen offset:2048 sc1\n"
"v_mfma_f32_16x16x32_bf16 v[168:171], v[28:31], v[136:139], v[168:171]\n"
"v_add_f32_e32 v166, v166, v194\n"
"v_add_f32_e32 v167, v167, v195\n"
"v_mfma_f32_16x16x32_bf16 v[168:171], v[32:35], v[92:95], v[168:171]\n"
"v_mul_f32_e32 v120, v120, v255\n"
"v_mul_f32_e32 v121, v121, v255\n"
"v_mfma_f32_16x16x32_bf16 v[168:171], v[36:39], v[160:163], v[168:171]\n"
"v_mul_f32_e32 v122, v122, v255\n"
"v_mul_f32_e32 v123, v123, v255\n"
"v_mfma_f32_16x16x32_bf16 v[168:171], v[40:43], v[144:147], v[168:171]\n"
"s_waitcnt lgkmcnt(0)\n"
"v_mul_f32_e32 v164, v164, v255\n"
"v_mul_f32_e32 v165, v165, v255\n"
"v_mfma_f32_16x16x32_bf16 v[168:171], v[44:47], v[176:179], v[168:171]\n"
"buffer_load_dwordx4 v[172:175], v1, s[4:7], s98 offen offset:3072 sc1\n"
"v_mfma_f32_16x16x32_bf16 v[168:171], v[48:51], v[188:191], v[168:171]\n"
"s_add_u32 s98, s60, 0x800000\n"
"ds_read_b128 v[192:195], v6 offset:4096\n"
"ds_read_b128 v[220:223], v6 offset:5120\n"
"v_mfma_f32_16x16x32_bf16 v[168:171], v[52:55], v[88:91], v[168:171]\n"
"v_mul_f32_e32 v166, v166, v255\n"
"v_mul_f32_e32 v167, v167, v255\n"
"v_mfma_f32_16x16x32_bf16 v[100:103], v[84:87], v[88:91], v[100:103]\n"
"buffer_store_dwordx4 v[180:183], v0, s[8:11], s98 offen sc1\n"
"v_mfma_f32_16x16x32_bf16 v[100:103], v[80:83], v[188:191], v[100:103]\n"
"v_cvt_pk_bf16_f32 v88, v120, v121\n"
"v_cvt_pk_bf16_f32 v89, v122, v123\n"
"v_mfma_f32_16x16x32_bf16 v[100:103], v[76:79], v[176:179], v[100:103]\n"
"v_cvt_pk_bf16_f32 v90, v164, v165\n"
"v_cvt_pk_bf16_f32 v91, v166, v167\n"
"v_mfma_f32_16x16x32_bf16 v[100:103], v[72:75], v[144:147], v[100:103]\n"
"ds_write_b128 v8, v[88:91] offset:17408\n"
"v_mfma_f32_16x16x32_bf16 v[100:103], v[68:71], v[160:163], v[100:103]\n"
"v_add_f32_e32 v196, v196, v208\n"
"v_add_f32_e32 v197, v197, v209\n"
"v_mfma_f32_16x16x32_bf16 v[100:103], v[64:67], v[92:95], v[100:103]\n"
"v_add_f32_e32 v198, v198, v210\n"
"v_add_f32_e32 v199, v199, v211\n"
"v_mfma_f32_16x16x32_bf16 v[100:103], v[60:63], v[136:139], v[100:103]\n"
"v_add_f32_e32 v212, v212, v216\n"
"v_add_f32_e32 v213, v213, v217\n"
"v_mfma_f32_16x16x32_bf16 v[100:103], v[56:59], v[108:111], v[100:103]\n"
"v_add_f32_e32 v214, v214, v218\n"
"v_add_f32_e32 v215, v215, v219\n"
"v_mul_f32_e32 v196, v196, v255\n"
"v_mul_f32_e32 v197, v197, v255\n"
"v_mul_f32_e32 v198, v198, v255\n"
"v_mul_f32_e32 v199, v199, v255\n"
"v_mul_f32_e32 v212, v212, v255\n"
"v_mul_f32_e32 v213, v213, v255\n"
"v_mul_f32_e32 v214, v214, v255\n"
"v_mul_f32_e32 v215, v215, v255\n"
"v_cvt_pk_bf16_f32 v88, v196, v197\n"
"v_cvt_pk_bf16_f32 v89, v198, v199\n"
"v_cvt_pk_bf16_f32 v90, v212, v213\n"
"v_cvt_pk_bf16_f32 v91, v214, v215\n"
"ds_write_b128 v7, v[88:91] offset:50176\n"
".Lmk_b3:\n"
"s_waitcnt vmcnt(18)\n"
"; @B G1 3.0\n"
"v_mfma_f32_16x16x32_bf16 v[88:91], a[0:3], v[116:119], 0\n"
"v_mfma_f32_16x16x32_bf16 v[88:91], a[4:7], v[152:155], v[88:91]\n"
"s_add_u32 s98, s62, 0x40000\n"
"v_mfma_f32_16x16x32_bf16 v[88:91], a[8:11], v[184:187], v[88:91]\n"
"s_add_u32 s96, s54, 0x400000\n"
"buffer_load_dwordx4 v[92:95], v0, s[4:7], s96 offen sc1\n"
"v_mfma_f32_16x16x32_bf16 v[88:91], a[12:15], v[148:151], v[88:91]\n"
"s_waitcnt vmcnt(12)\n"
"v_mfma_f32_16x16x32_bf16 v[88:91], a[16:19], v[156:159], v[88:91]\n"
"buffer_load_dword v255, v10, s[4:7], s98 offen sc1\n"
"v_mfma_f32_16x16x32_bf16 v[88:91], a[20:23], v[224:227], v[88:91]\n"
"s_waitcnt lgkmcnt(2)\n"
"v_add_f32_e32 v168, v168, v192\n"
"v_add_f32_e32 v169, v169, v193\n"
"v_mfma_f32_16x16x32_bf16 v[88:91], a[24:27], v[204:207], v[88:91]\n"
"v_add_f32_e32 v170, v170, v194\n"
"v_add_f32_e32 v171, v171, v195\n"
"v_mfma_f32_16x16x32_bf16 v[88:91], a[28:31], v[128:131], v[88:91]\n"
"buffer_load_dwordx4 v[108:111], v0, s[4:7], s96 offen offset:1024 sc1\n"
"v_mfma_f32_16x16x32_bf16 v[120:123], a[60:63], v[128:131], 0\n"
"s_waitcnt lgkmcnt(0)\n"
"s_barrier\n"
"v_mfma_f32_16x16x32_bf16 v[120:123], a[56:59], v[204:207], v[120:123]\n"
"buffer_load_dwordx4 a[228:231], v0, s[4:7], s97 offen offset:1024 sc1\n"
"v_mfma_f32_16x16x32_bf16 v[120:123], a[52:55], v[224:227], v[120:123]\n"
"buffer_load_dword v253, v10, s[4:7], s98 offen offset:256 sc1\n"
"v_mfma_f32_16x16x32_bf16 v[120:123], a[48:51], v[156:159], v[120:123]\n"
"v_add_f32_e32 v100, v100, v220\n"
"v_add_f32_e32 v101, v101, v221\n"
"v_mfma_f32_16x16x32_bf16 v[120:123], a[44:47], v[148:151], v[120:123]\n"
"buffer_load_dwordx4 v[136:139], v0, s[4:7], s96 offen offset:2048 sc1\n"
"v_mfma_f32_16x16x32_bf16 v[120:123], a[40:43], v[184:187], v[120:123]\n"
"ds_write_b128 v3, v[88:91]\n"
"v_mfma_f32_16x16x32_bf16 v[120:123], a[36:39], v[152:155], v[120:123]\n"
"buffer_load_dword v252, v10, s[4:7], s98 offen offset:512 sc1\n"
"v_mfma_f32_16x16x32_bf16 v[120:123], a[32:35], v[116:119], v[120:123]\n"
"v_add_f32_e32 v102, v102, v222\n"
"v_add_f32_e32 v103, v103, v223\n"
"v_mfma_f32_16x16x32_bf16 v[88:91], a[64:67], v[116:119], 0\n"
"v_mul_f32_e32 v168, v168, v254\n"
"v_mul_f32_e32 v169, v169, v254\n"
"v_mfma_f32_16x16x32_bf16 v[88:91], a[68:71], v[152:155], v[88:91]\n"
"buffer_load_dwordx4 v[144:147], v0, s[4:7], s96 offen offset:3072 sc1\n"
"v_mfma_f32_16x16x32_bf16 v[88:91], a[72:75], v[184:187], v[88:91]\n"
"buffer_load_dword v251, v10, s[4:7], s98 offen offset:768 sc1\n"
"v_mfma_f32_16x16x32_bf16 v[88:91], a[76:79], v[148:151], v[88:91]\n"
"v_mul_f32_e32 v170, v170, v254\n"
"v_mul_f32_e32 v171, v171, v254\n"
"v_mfma_f32_16x16x32_bf16 v[88:91], a[80:83], v[156:159], v[88:91]\n"
"v_mul_f32_e32 v100, v100, v254\n"
"v_mul_f32_e32 v101, v101, v254\n"
"v_add_u32_e32 v250, 0x26c00, v0\n"
"v_mfma_f32_16x16x32_bf16 v[88:91], a[84:87], v[224:227], v[88:91]\n"
"ds_write_b128 v3, v[120:123] offset:1024\n"
"v_mfma_f32_16x16x32_bf16 v[88:91], a[88:91], v[204:207], v[88:91]\n"
"buffer_load_dwordx4 v[120:123], v1, s[4:7], s96 offen sc1\n"
"ds_read_b128 v[160:163], v250\n"
"v_mfma_f32_16x16x32_bf16 v[88:91], a[92:95], v[128:131], v[88:91]\n"
"buffer_load_dwordx4 a[232:235], v0, s[4:7], s97 offen offset:2048 sc1\n"
"ds_read_b128 v[164:167], v250 offset:1024\n"
"v_mfma_f32_16x16x32_bf16 v[176:179], a[124:127], v[128:131], 0\n"
"buffer_load_dword v250, v10, s[4:7], s98 offen offset:1024 sc1\n"
"v_xor_b32_e32 v249, 32, v17\n"
"v_mfma_f32_16x16x32_bf16 v[176:179], a[120:123], v[204:207], v[176:179]\n"
"ds_read_b128 v[180:183], v2 offset:49152\n"
"v_mul_f32_e32 v102, v102, v254\n"
"v_mul_f32_e32 v103, v103, v254\n"
"v_mfma_f32_16x16x32_bf16 v[176:179], a[116:119], v[224:227], v[176:179]\n"
"v_cvt_pk_bf16_f32 v188, v168, v169\n"
"v_cvt_pk_bf16_f32 v189, v170, v171\n"
"v_mfma_f32_16x16x32_bf16 v[176:179], a[112:115], v[156:159], v[176:179]\n"
"ds_read_b128 v[168:171], v17 offset:23552\n"
"buffer_load_dwordx4 v[192:195], v1, s[4:7], s96 offen offset:1024 sc1\n"
"ds_read_b128 v[196:199], v249 offset:23808\n"
"v_mfma_f32_16x16x32_bf16 v[176:179], a[108:111], v[148:151], v[176:179]\n"
"buffer_load_dword v254, v10, s[4:7], s98 offen offset:1280 sc1\n"
"v_mfma_f32_16x16x32_bf16 v[176:179], a[104:107], v[184:187], v[176:179]\n"
"ds_write_b128 v3, v[88:91] offset:2048\n"
"v_mfma_f32_16x16x32_bf16 v[176:179], a[100:103], v[152:155], v[176:179]\n"
"v_cvt_pk_bf16_f32 v190, v100, v101\n"
"v_cvt_pk_bf16_f32 v191, v102, v103\n"
"v_mfma_f32_16x16x32_bf16 v[176:179], a[96:99], v[116:119], v[176:179]\n"
"buffer_load_dword v249, v10, s[4:7], s98 offen offset:1536 sc1\n"
"v_mfma_f32_16x16x32_bf16 v[88:91], v[24:27], v[116:119], 0\n"
"buffer_load_dwordx4 v[100:103], v1, s[4:7], s96 offen offset:2048 sc1\n"
"s_waitcnt lgkmcnt(1)\n"
"v_mfma_f32_16x16x32_bf16 v[160:163], v[168:171], v[180:183], v[160:163]\n"
"v_mfma_f32_16x16x32_bf16 v[164:167], v[196:199], v[180:183], v[164:167]\n"
"v_mfma_f32_16x16x32_bf16 v[88:91], v[28:31], v[152:155], v[88:91]\n"
"ds_write_b128 v2, v[188:191] offset:17408\n"
"v_mfma_f32_16x16x32_bf16 v[88:91], v[32:35], v[184:187], v[88:91]\n"
"buffer_load_dword v248, v10, s[4:7], s98 offen offset:1792 sc1\n"
"v_xor_b32_e32 v247, 32, v16\n"
"v_mfma_f32_16x16x32_bf16 v[88:91], v[36:39], v[148:151], v[88:91]\n"
"ds_read_b64_tr_b16 v[168:169], v16 offset:23552\n"
"ds_read_b64_tr_b16 v[170:171], v247 offset:23808\n"
"ds_read_b64_tr_b16 v[180:181], v247 offset:23552\n"
"v_mfma_f32_16x16x32_bf16 v[88:91], v[40:43], v[156:159], v[88:91]\n"
"buffer_load_dwordx4 a[236:239], v0, s[4:7], s97 offen offset:3072 sc1\n"
"ds_read_b64_tr_b16 v[182:183], v16 offset:23808\n"
"v_mfma_f32_16x16x32_bf16 v[88:91], v[44:47], v[224:227], v[88:91]\n"
"buffer_load_dwordx4 v[188:191], v1, s[4:7], s96 offen offset:3072 sc1\n"
"v_mov_b32_e32 v196, 0x3f803f80\n"
"v_mfma_f32_16x16x32_bf16 v[88:91], v[48:51], v[204:207], v[88:91]\n"
"ds_write_b128 v3, v[176:179] offset:3072\n"
"v_mfma_f32_16x16x32_bf16 v[88:91], v[52:55], v[128:131], v[88:91]\n"
"v_mov_b32_e32 v197, 0x3f803f80\n"
"v_mov_b32_e32 v198, 0x3f803f80\n"
"v_mov_b32_e32 v199, 0x3f803f80\n"
"v_mfma_f32_16x16x32_bf16 v[176:179], v[84:87], v[128:131], 0\n"
"v_max_f32_e32 v247, v160, v161\n"
"v_max3_f32 v247, v247, v162, v163\n"
"v_max3_f32 v247, v247, v164, v165\n"
"v_mfma_f32_16x16x32_bf16 v[176:179], v[80:83], v[204:207], v[176:179]\n"
"v_max3_f32 v247, v247, v166, v167\n"
"v_mov_b32_e32 v246, v247\n"
"v_mfma_f32_16x16x32_bf16 v[176:179], v[76:79], v[224:227], v[176:179]\n"
"s_nop 0\n"
"v_permlane16_swap_b32_e32 v247, v246\n"
"v_max_f32_e32 v247, v247, v246\n"
"v_mov_b32_e32 v246, v247\n"
"v_mfma_f32_16x16x32_bf16 v[176:179], v[72:75], v[156:159], v[176:179]\n"
"s_nop 0\n"
"v_permlane32_swap_b32_e32 v247, v246\n"
"v_max_f32_e32 v247, v247, v246\n"
"v_sub_f32_e32 v160, v160, v247\n"
"v_mfma_f32_16x16x32_bf16 v[176:179], v[68:71], v[148:151], v[176:179]\n"
"v_sub_f32_e32 v161, v161, v247\n"
"v_sub_f32_e32 v162, v162, v247\n"
"v_sub_f32_e32 v163, v163, v247\n"
"v_mfma_f32_16x16x32_bf16 v[176:179], v[64:67], v[184:187], v[176:179]\n"
"ds_write_b128 v3, v[88:91] offset:4096\n"
"v_mfma_f32_16x16x32_bf16 v[176:179], v[60:63], v[152:155], v[176:179]\n"
"v_sub_f32_e32 v164, v164, v247\n"
"v_sub_f32_e32 v165, v165, v247\n"
"v_sub_f32_e32 v166, v166, v247\n"
"v_mfma_f32_16x16x32_bf16 v[176:179], v[56:59], v[116:119], v[176:179]\n"
"s_waitcnt vmcnt(25)\n"
"v_sub_f32_e32 v167, v167, v247\n"
"v_exp_f32_e32 v160, v160\n"
"; @B G1 3.1\n"
"v_mfma_f32_16x16x32_bf16 v[88:91], a[0:3], v[104:107], 0\n"
"v_exp_f32_e32 v161, v161\n"
"v_mfma_f32_16x16x32_bf16 v[88:91], a[4:7], v[132:135], v[88:91]\n"
"buffer_load_dwordx4 a[240:243], v1, s[4:7], s97 offen sc1\n"
"v_mfma_f32_16x16x32_bf16 v[88:91], a[8:11], v[140:143], v[88:91]\n"
"s_add_u32 s98, s55, 0x400000\n"
"buffer_load_dwordx4 v[116:119], v0, s[4:7], s98 offen sc1\n"
"v_mfma_f32_16x16x32_bf16 v[88:91], a[12:15], v[200:203], v[88:91]\n"
"s_waitcnt vmcnt(22)\n"
"v_exp_f32_e32 v162, v162\n"
"v_mfma_f32_16x16x32_bf16 v[88:91], a[16:19], v[96:99], v[88:91]\n"
"v_exp_f32_e32 v163, v163\n"
"v_mfma_f32_16x16x32_bf16 v[88:91], a[20:23], v[112:115], v[88:91]\n"
"ds_write_b128 v3, v[176:179] offset:5120\n"
"v_mfma_f32_16x16x32_bf16 v[88:91], a[24:27], v[124:127], v[88:91]\n"
"v_exp_f32_e32 v164, v164\n"
"v_mfma_f32_16x16x32_bf16 v[88:91], a[28:31], v[172:175], v[88:91]\n"
"buffer_load_dwordx4 v[128:131], v0, s[4:7], s98 offen offset:1024 sc1\n"
"v_mfma_f32_16x16x32_bf16 v[148:151], a[60:63], v[172:175], 0\n"
"v_exp_f32_e32 v165, v165\n"
"v_mfma_f32_16x16x32_bf16 v[148:151], a[56:59], v[124:127], v[148:151]\n"
"v_exp_f32_e32 v166, v166\n"
"v_mfma_f32_16x16x32_bf16 v[148:151], a[52:55], v[112:115], v[148:151]\n"
"v_exp_f32_e32 v167, v167\n"
"v_cvt_pk_bf16_f32 v152, v160, v161\n"
"v_mfma_f32_16x16x32_bf16 v[148:151], a[48:51], v[96:99], v[148:151]\n"
"v_cvt_pk_bf16_f32 v153, v162, v163\n"
"v_cvt_pk_bf16_f32 v154, v164, v165\n"
"v_cvt_pk_bf16_f32 v155, v166, v167\n"
"v_mfma_f32_16x16x32_bf16 v[148:151], a[44:47], v[200:203], v[148:151]\n"
"buffer_load_dwordx4 v[156:159], v0, s[4:7], s98 offen offset:2048 sc1\n"
"s_waitcnt lgkmcnt(3)\n"
"v_mfma_f32_16x16x32_bf16 v[160:163], v[196:199], v[152:155], 0\n"
"v_mfma_f32_16x16x32_bf16 v[164:167], v[168:171], v[152:155], 0\n"
"v_mfma_f32_16x16x32_bf16 v[168:171], v[180:183], v[152:155], 0\n"
"v_mfma_f32_16x16x32_bf16 v[148:151], a[40:43], v[140:143], v[148:151]\n"
"ds_write_b128 v3, v[88:91] offset:6144\n"
"v_mfma_f32_16x16x32_bf16 v[148:151], a[36:39], v[132:135], v[148:151]\n"
"s_add_u32 s96, s73, 0\n"
"buffer_load_dwordx2 v[246:247], off, s[4:7], s96 sc1\n"
"v_mfma_f32_16x16x32_bf16 v[148:151], a[32:35], v[104:107], v[148:151]\n"
"buffer_load_dwordx4 a[244:247], v1, s[4:7], s97 offen offset:1024 sc1\n"
"v_mfma_f32_16x16x32_bf16 v[88:91], a[64:67], v[104:107], 0\n"
"v_mfma_f32_16x16x32_bf16 v[88:91], a[68:71], v[132:135], v[88:91]\n"
"buffer_load_dwordx4 v[152:155], v0, s[4:7], s98 offen offset:3072 sc1\n"
"v_mfma_f32_16x16x32_bf16 v[88:91], a[72:75], v[140:143], v[88:91]\n"
"v_rcp_f32_e32 v160, v160\n"
"v_mfma_f32_16x16x32_bf16 v[88:91], a[76:79], v[200:203], v[88:91]\n"
"v_mul_f32_e32 v164, v164, v160\n"
"v_mul_f32_e32 v165, v165, v160\n"
"v_mul_f32_e32 v166, v166, v160\n"
"v_mfma_f32_16x16x32_bf16 v[88:91], a[80:83], v[96:99], v[88:91]\n"
"v_mov_b32_e32 v176, -1\n"
"v_mov_b32_e32 v177, -1\n"
"v_mul_f32_e32 v167, v167, v160\n"
"v_mfma_f32_16x16x32_bf16 v[88:91], a[84:87], v[112:115], v[88:91]\n"
"ds_write_b128 v3, v[148:151] offset:7168\n"
"v_mfma_f32_16x16x32_bf16 v[88:91], a[88:91], v[124:127], v[88:91]\n"
"buffer_load_dwordx4 v[148:151], v1, s[4:7], s98 offen sc1\n"
"v_mul_f32_e32 v168, v168, v160\n"
"v_mfma_f32_16x16x32_bf16 v[88:91], a[92:95], v[172:175], v[88:91]\n"
"v_mov_b32_e32 v178, -1\n"
"v_mov_b32_e32 v179, -1\n"
"v_mul_f32_e32 v169, v169, v160\n"
"v_mfma_f32_16x16x32_bf16 v[180:183], a[124:127], v[172:175], 0\n"
"s_add_u32 s95, s60, 0x808000\n"
"buffer_store_dwordx4 v[176:179], v0, s[8:11], s95 offen sc1\n"
"v_mfma_f32_16x16x32_bf16 v[180:183], a[120:123], v[124:127], v[180:183]\n"
"v_mul_f32_e32 v170, v170, v160\n"
"v_mul_f32_e32 v171, v171, v160\n"
"v_cvt_pk_bf16_f32 v160, v164, v165\n"
"v_mfma_f32_16x16x32_bf16 v[180:183], a[116:119], v[112:115], v[180:183]\n"
"s_add_u32 s95, s60, 0x440000\n"
"v_cvt_pk_bf16_f32 v161, v166, v167\n"
"v_cvt_pk_bf16_f32 v162, v168, v169\n"
"v_cvt_pk_bf16_f32 v163, v170, v171\n"
"v_mfma_f32_16x16x32_bf16 v[180:183], a[112:115], v[96:99], v[180:183]\n"
"buffer_load_dwordx4 v[164:167], v1, s[4:7], s98 offen offset:1024 sc1\n"
"v_mfma_f32_16x16x32_bf16 v[180:183], a[108:111], v[200:203], v[180:183]\n"
"buffer_load_dwordx4 a[248:251], v1, s[4:7], s97 offen offset:2048 sc1\n"
"v_mfma_f32_16x16x32_bf16 v[180:183], a[104:107], v[140:143], v[180:183]\n"
"ds_write_b128 v3, v[88:91] offset:8192\n"
"v_mfma_f32_16x16x32_bf16 v[180:183], a[100:103], v[132:135], v[180:183]\n"
"buffer_store_dwordx4 v[160:163], v0, s[4:7], s95 offen sc1\n"
"v_mfma_f32_16x16x32_bf16 v[180:183], a[96:99], v[104:107], v[180:183]\n"
";\n"
"v_mfma_f32_16x16x32_bf16 v[88:91], v[24:27], v[104:107], 0\n"
"buffer_load_dwordx4 v[160:163], v1, s[4:7], s98 offen offset:2048 sc1\n"
"v_mfma_f32_16x16x32_bf16 v[88:91], v[28:31], v[132:135], v[88:91]\n"
"v_mfma_f32_16x16x32_bf16 v[88:91], v[32:35], v[140:143], v[88:91]\n"
"v_mfma_f32_16x16x32_bf16 v[88:91], v[36:39], v[200:203], v[88:91]\n"
"v_mfma_f32_16x16x32_bf16 v[88:91], v[40:43], v[96:99], v[88:91]\n"
"s_waitcnt vmcnt(15)\n"
"v_add_f32_e32 v255, v255, v250\n"
"v_add_f32_e32 v253, v253, v254\n"
"v_mfma_f32_16x16x32_bf16 v[88:91], v[44:47], v[112:115], v[88:91]\n"
"buffer_load_dwordx4 v[168:171], v1, s[4:7], s98 offen offset:3072 sc1\n"
"v_mfma_f32_16x16x32_bf16 v[88:91], v[48:51], v[124:127], v[88:91]\n"
"ds_write_b128 v3, v[180:183] offset:9216\n"
"v_mfma_f32_16x16x32_bf16 v[88:91], v[52:55], v[172:175], v[88:91]\n"
"v_add_f32_e32 v252, v252, v249\n"
"v_add_f32_e32 v251, v251, v248\n"
"v_mfma_f32_16x16x32_bf16 v[180:183], v[84:87], v[172:175], 0\n"
"v_add_f32_e32 v255, v255, v252\n"
"v_add_f32_e32 v253, v253, v251\n"
"v_mfma_f32_16x16x32_bf16 v[180:183], v[80:83], v[124:127], v[180:183]\n"
"buffer_load_dwordx4 a[252:255], v1, s[4:7], s97 offen offset:3072 sc1\n"
"v_mfma_f32_16x16x32_bf16 v[180:183], v[76:79], v[112:115], v[180:183]\n"
"v_add_f32_e32 v255, v255, v253\n"
"v_mov_b32_e32 v253, v255\n"
"v_mfma_f32_16x16x32_bf16 v[180:183], v[72:75], v[96:99], v[180:183]\n"
"s_nop 0\n"
"v_permlane32_swap_b32_e32 v255, v253\n"
"v_add_f32_e32 v255, v255, v253\n"
"v_mfma_f32_16x16x32_bf16 v[180:183], v[68:71], v[200:203], v[180:183]\n"
"v_mul_f32_e32 v255, 0x3a800000, v255\n"
"v_mov_b32_e32 v253, v255\n"
"v_mfma_f32_16x16x32_bf16 v[180:183], v[64:67], v[140:143], v[180:183]\n"
"ds_write_b128 v3, v[88:91] offset:10240\n"
"v_mfma_f32_16x16x32_bf16 v[180:183], v[60:63], v[132:135], v[180:183]\n"
"v_permlane16_swap_b32_e32 v255, v253\n"
"v_cvt_pk_bf16_f32 v254, -v255, 0\n"
"v_mfma_f32_16x16x32_bf16 v[180:183], v[56:59], v[104:107], v[180:183]\n"
"v_fma_f32 v253, -v255, v255, v253\n"
"v_lshlrev_b32_e32 v254, 16, v254\n"
"; @B G1 3.2\n"
"v_mfma_f32_16x16x32_bf16 v[88:91], a[0:3], v[92:95], 0\n"
"s_add_u32 s98, s67, 0x10020\n"
"buffer_load_dwordx2 v[250:251], v12, s[4:7], s98 offen sc1\n"
"v_mfma_f32_16x16x32_bf16 v[88:91], a[4:7], v[108:111], v[88:91]\n"
"v_max_f32_e32 v253, 0, v253\n"
"v_cvt_pk_bf16_f32 v253, v253, 0\n"
"v_mfma_f32_16x16x32_bf16 v[88:91], a[8:11], v[136:139], v[88:91]\n"
"v_lshlrev_b32_e32 v253, 16, v253\n"
"v_add_f32_e32 v253, 0x3727c5ac, v253\n"
"v_mfma_f32_16x16x32_bf16 v[88:91], a[12:15], v[144:147], v[88:91]\n"
"s_waitcnt vmcnt(16)\n"
"v_cvt_pk_bf16_f32 v253, v253, 0\n"
"v_lshlrev_b32_e32 v253, 16, v253\n"
"v_mfma_f32_16x16x32_bf16 v[88:91], a[16:19], v[120:123], v[88:91]\n"
"v_sqrt_f32_e32 v253, v253\n"
"v_mfma_f32_16x16x32_bf16 v[88:91], a[20:23], v[192:195], v[88:91]\n"
"ds_write_b128 v3, v[180:183] offset:11264\n"
"v_mfma_f32_16x16x32_bf16 v[88:91], a[24:27], v[100:103], v[88:91]\n"
"v_cvt_pk_bf16_f32 v253, v253, 0\n"
"v_lshlrev_b32_e32 v253, 16, v253\n"
"v_mfma_f32_16x16x32_bf16 v[88:91], a[28:31], v[188:191], v[88:91]\n"
"v_rcp_f32_e32 v255, v253\n"
"v_mfma_f32_16x16x32_bf16 v[96:99], a[60:63], v[188:191], 0\n"
"v_mul_f32_e32 v253, 0x3e8293ee, v255\n"
"v_add_u32_e32 v252, 0x26c00, v0\n"
"v_mfma_f32_16x16x32_bf16 v[96:99], a[56:59], v[100:103], v[96:99]\n"
"ds_read_b128 v[104:107], v252\n"
"ds_read_b128 v[112:115], v252 offset:1024\n"
"v_mfma_f32_16x16x32_bf16 v[96:99], a[52:55], v[192:195], v[96:99]\n"
"s_waitcnt lgkmcnt(0)\n"
"s_barrier\n"
"v_mfma_f32_16x16x32_bf16 v[96:99], a[48:51], v[120:123], v[96:99]\n"
"s_add_u32 s97, s61, 0xc40000\n"
"ds_read_b128 v[124:127], v4\n"
"ds_read_b128 v[132:135], v9 offset:640\n"
"v_mfma_f32_16x16x32_bf16 v[96:99], a[44:47], v[144:147], v[96:99]\n"
"ds_read_b128 v[140:143], v5\n"
"v_add_u32_e32 v252, 0x26c00, v0\n"
"v_mfma_f32_16x16x32_bf16 v[96:99], a[40:43], v[136:139], v[96:99]\n"
"ds_write_b128 v3, v[88:91] offset:12288\n"
"v_mfma_f32_16x16x32_bf16 v[96:99], a[36:39], v[108:111], v[96:99]\n"
"buffer_store_dwordx4 v[176:179], v0, s[8:11], s97 offen sc1\n"
"v_mfma_f32_16x16x32_bf16 v[96:99], a[32:35], v[92:95], v[96:99]\n"
"ds_read_b128 v[88:91], v252\n"
"ds_read_b128 v[172:175], v252 offset:1024\n"
"v_mfma_f32_16x16x32_bf16 v[180:183], a[64:67], v[92:95], 0\n"
"v_mov_b32_e32 v252, 0xff800000\n"
"v_mfma_f32_16x16x32_bf16 v[180:183], a[68:71], v[108:111], v[180:183]\n"
"s_waitcnt lgkmcnt(3)\n"
"v_fmac_f32_e32 v124, v254, v132\n"
"v_fmac_f32_e32 v125, v254, v133\n"
"v_mfma_f32_16x16x32_bf16 v[180:183], a[72:75], v[136:139], v[180:183]\n"
"ds_read_b128 v[184:187], v4 offset:1024\n"
"ds_read_b128 v[196:199], v9 offset:704\n"
"v_mfma_f32_16x16x32_bf16 v[180:183], a[76:79], v[144:147], v[180:183]\n"
"ds_read_b128 v[200:203], v5 offset:1024\n"
"v_fmac_f32_e32 v126, v254, v134\n"
"v_mfma_f32_16x16x32_bf16 v[180:183], a[80:83], v[120:123], v[180:183]\n"
"v_fmac_f32_e32 v127, v254, v135\n"
"v_add_f32_e32 v124, v124, v140\n"
"v_mfma_f32_16x16x32_bf16 v[180:183], a[84:87], v[192:195], v[180:183]\n"
"ds_write_b128 v3, v[96:99] offset:13312\n"
"v_mfma_f32_16x16x32_bf16 v[180:183], a[88:91], v[100:103], v[180:183]\n"
"s_waitcnt lgkmcnt(4)\n"
"v_add_f32_e32 v125, v125, v141\n"
"v_add_f32_e32 v126, v126, v142\n"
"v_mfma_f32_16x16x32_bf16 v[180:183], a[92:95], v[188:191], v[180:183]\n"
"v_add_f32_e32 v127, v127, v143\n"
"v_cndmask_b32_e64 v88, v88, v252, s[82:83]\n"
"v_mfma_f32_16x16x32_bf16 v[96:99], a[124:127], v[188:191], 0\n"
"s_waitcnt vmcnt(12)\n"
"v_cmp_ne_u64_e32 vcc, v[246:247], v[18:19]\n"
"v_mfma_f32_16x16x32_bf16 v[96:99], a[120:123], v[100:103], v[96:99]\n"
"s_cbranch_vccnz .Lmk_f45s\n"
".Lmk_f45r:\n"
"s_mov_b32 m0, 0x7c00\n"
"v_mfma_f32_16x16x32_bf16 v[96:99], a[116:119], v[192:195], v[96:99]\n"
"s_add_u32 s97, s66, 0x3c00\n"
"buffer_load_dwordx4 v0, s[4:7], s97 offen sc1 lds\n"
"v_mfma_f32_16x16x32_bf16 v[96:99], a[112:115], v[120:123], v[96:99]\n"
"s_mov_b32 m0, 0xfc00\n"
"s_add_u32 s97, s66, 0x7c00\n"
"ds_read_b128 v[132:135], v4 offset:2048\n"
"v_mfma_f32_16x16x32_bf16 v[96:99], a[108:111], v[144:147], v[96:99]\n"
"buffer_load_dwordx4 v0, s[4:7], s97 offen sc1 lds\n"
"v_mfma_f32_16x16x32_bf16 v[96:99], a[104:107], v[136:139], v[96:99]\n"
"ds_write_b128 v3, v[180:183] offset:14336\n"
"v_mfma_f32_16x16x32_bf16 v[96:99], a[100:103], v[108:111], v[96:99]\n"
"ds_read_b128 v[140:143], v9 offset:768\n"
"ds_read_b128 v[180:183], v5 offset:2048\n"
"v_mfma_f32_16x16x32_bf16 v[96:99], a[96:99], v[92:95], v[96:99]\n"
"s_waitcnt lgkmcnt(5)\n"
"v_fmac_f32_e32 v184, v254, v196\n"
"v_fmac_f32_e32 v185, v254, v197\n"
"v_mfma_f32_16x16x32_bf16 v[204:207], v[24:27], v[92:95], 0\n"
"ds_read_b128 v[208:211], v4 offset:3072\n"
"ds_read_b128 v[212:215], v9 offset:832\n"
"v_mfma_f32_16x16x32_bf16 v[204:207], v[28:31], v[108:111], v[204:207]\n"
"ds_read_b128 v[216:219], v5 offset:3072\n"
"v_fmac_f32_e32 v186, v254, v198\n"
"v_mfma_f32_16x16x32_bf16 v[204:207], v[32:35], v[136:139], v[204:207]\n"
"v_fmac_f32_e32 v187, v254, v199\n"
"v_add_f32_e32 v184, v184, v200\n"
"v_mfma_f32_16x16x32_bf16 v[204:207], v[36:39], v[144:147], v[204:207]\n"
"v_add_f32_e32 v185, v185, v201\n"
"v_add_f32_e32 v186, v186, v202\n"
"v_mfma_f32_16x16x32_bf16 v[204:207], v[40:43], v[120:123], v[204:207]\n"
"s_waitcnt lgkmcnt(3)\n"
"v_add_f32_e32 v187, v187, v203\n"
"v_fmac_f32_e32 v132, v254, v140\n"
"v_mfma_f32_16x16x32_bf16 v[204:207], v[44:47], v[192:195], v[204:207]\n"
"ds_write_b128 v3, v[96:99] offset:15360\n"
"v_mfma_f32_16x16x32_bf16 v[204:207], v[48:51], v[100:103], v[204:207]\n"
"v_fmac_f32_e32 v133, v254, v141\n"
"v_fmac_f32_e32 v134, v254, v142\n"
"v_mfma_f32_16x16x32_bf16 v[204:207], v[52:55], v[188:191], v[204:207]\n"
"ds_read_b128 v[96:99], v4 offset:4096\n"
"ds_read_b128 v[196:199], v9 offset:512\n"
"v_mfma_f32_16x16x32_bf16 v[200:203], v[84:87], v[188:191], 0\n"
"ds_read_b128 v[188:191], v5 offset:4096\n"
"v_fmac_f32_e32 v135, v254, v143\n"
"v_mfma_f32_16x16x32_bf16 v[200:203], v[80:83], v[100:103], v[200:203]\n"
"v_add_f32_e32 v132, v132, v180\n"
"v_add_f32_e32 v133, v133, v181\n"
"v_mfma_f32_16x16x32_bf16 v[200:203], v[76:79], v[192:195], v[200:203]\n"
"s_waitcnt lgkmcnt(4)\n"
"v_add_f32_e32 v134, v134, v182\n"
"v_add_f32_e32 v135, v135, v183\n"
"v_mfma_f32_16x16x32_bf16 v[200:203], v[72:75], v[120:123], v[200:203]\n"
"v_fmac_f32_e32 v208, v254, v212\n"
"v_fmac_f32_e32 v209, v254, v213\n"
"v_mfma_f32_16x16x32_bf16 v[200:203], v[68:71], v[144:147], v[200:203]\n"
"v_fmac_f32_e32 v210, v254, v214\n"
"v_fmac_f32_e32 v211, v254, v215\n"
"v_mfma_f32_16x16x32_bf16 v[200:203], v[64:67], v[136:139], v[200:203]\n"
"ds_write_b128 v3, v[204:207] offset:16384\n"
"v_mfma_f32_16x16x32_bf16 v[200:203], v[60:63], v[108:111], v[200:203]\n"
"ds_read_b128 v[100:103], v4 offset:5120\n"
"ds_read_b128 v[108:111], v9 offset:576\n"
"v_mfma_f32_16x16x32_bf16 v[200:203], v[56:59], v[92:95], v[200:203]\n"
"s_waitcnt vmcnt(12)\n"
"s_waitcnt vmcnt(3)\n"
"v_cmp_ne_u64_e32 vcc, v[250:251], v[18:19]\n"
"; @B G1 3.3\n"
"v_mfma_f32_16x16x32_bf16 v[124:127], a[0:3], v[116:119], v[124:127]\n"
"s_cbranch_vccnz .Lmk_f46s\n"
".Lmk_f46r:\n"
"ds_read_b128 v[92:95], v5 offset:5120\n"
"v_mfma_f32_16x16x32_bf16 v[124:127], a[4:7], v[128:131], v[124:127]\n"
"s_add_u32 s98, s61, 0xc40000\n"
"s_add_u32 s97, s56, 0x440000\n"
"v_add_f32_e32 v208, v208, v216\n"
"v_mfma_f32_16x16x32_bf16 v[124:127], a[8:11], v[156:159], v[124:127]\n"
"buffer_load_dwordx4 v[120:123], v0, s[4:7], s97 offen sc1\n"
"v_mfma_f32_16x16x32_bf16 v[124:127], a[12:15], v[152:155], v[124:127]\n"
"buffer_store_dwordx4 v[176:179], v0, s[8:11], s98 offen offset:1024 sc1\n"
"v_mfma_f32_16x16x32_bf16 v[124:127], a[16:19], v[148:151], v[124:127]\n"
"s_add_u32 s98, s69, 0\n"
"v_add_f32_e32 v209, v209, v217\n"
"v_add_f32_e32 v210, v210, v218\n"
"v_mfma_f32_16x16x32_bf16 v[124:127], a[20:23], v[164:167], v[124:127]\n"
"ds_write_b128 v3, v[200:203] offset:17408\n"
"v_mfma_f32_16x16x32_bf16 v[124:127], a[24:27], v[160:163], v[124:127]\n"
"buffer_load_dwordx4 v[136:139], v0, s[4:7], s97 offen offset:1024 sc1\n"
"v_mfma_f32_16x16x32_bf16 v[124:127], a[28:31], v[168:171], v[124:127]\n"
"s_waitcnt lgkmcnt(5)\n"
"v_add_f32_e32 v211, v211, v219\n"
"v_fmac_f32_e32 v96, v254, v196\n"
"v_mfma_f32_16x16x32_bf16 v[184:187], a[60:63], v[168:171], v[184:187]\n"
"v_fmac_f32_e32 v97, v254, v197\n"
"v_fmac_f32_e32 v98, v254, v198\n"
"v_mfma_f32_16x16x32_bf16 v[184:187], a[56:59], v[160:163], v[184:187]\n"
"buffer_load_dwordx4 v[140:143], v0, s[4:7], s97 offen offset:2048 sc1\n"
"v_mfma_f32_16x16x32_bf16 v[184:187], a[52:55], v[164:167], v[184:187]\n"
"s_waitcnt lgkmcnt(0)\n"
"s_barrier\n"
"v_mfma_f32_16x16x32_bf16 v[184:187], a[48:51], v[148:151], v[184:187]\n"
"ds_read_b128 v[144:147], v6\n"
"ds_read_b128 v[180:183], v6 offset:1024\n"
"v_mfma_f32_16x16x32_bf16 v[184:187], a[44:47], v[152:155], v[184:187]\n"
"buffer_load_dwordx4 v[192:195], v0, s[4:7], s97 offen offset:3072 sc1\n"
"v_mfma_f32_16x16x32_bf16 v[184:187], a[40:43], v[156:159], v[184:187]\n"
"v_fmac_f32_e32 v99, v254, v199\n"
"v_add_f32_e32 v96, v96, v188\n"
"v_mfma_f32_16x16x32_bf16 v[184:187], a[36:39], v[128:131], v[184:187]\n"
"v_add_f32_e32 v97, v97, v189\n"
"v_add_f32_e32 v98, v98, v190\n"
"v_mfma_f32_16x16x32_bf16 v[184:187], a[32:35], v[116:119], v[184:187]\n"
"buffer_load_dwordx4 v[196:199], v1, s[4:7], s97 offen sc1\n"
"v_mfma_f32_16x16x32_bf16 v[132:135], a[64:67], v[116:119], v[132:135]\n"
"v_add_f32_e32 v99, v99, v191\n"
"v_fmac_f32_e32 v100, v254, v108\n"
"v_mfma_f32_16x16x32_bf16 v[132:135], a[68:71], v[128:131], v[132:135]\n"
"v_fmac_f32_e32 v101, v254, v109\n"
"v_fmac_f32_e32 v102, v254, v110\n"
"v_mfma_f32_16x16x32_bf16 v[132:135], a[72:75], v[156:159], v[132:135]\n"
"buffer_load_dwordx4 v[188:191], v1, s[4:7], s97 offen offset:1024 sc1\n"
"v_mfma_f32_16x16x32_bf16 v[132:135], a[76:79], v[152:155], v[132:135]\n"
"v_fmac_f32_e32 v103, v254, v111\n"
"v_add_f32_e32 v100, v100, v92\n"
"v_mfma_f32_16x16x32_bf16 v[132:135], a[80:83], v[148:151], v[132:135]\n"
"s_waitcnt lgkmcnt(0)\n"
"v_add_f32_e32 v101, v101, v93\n"
"v_add_f32_e32 v102, v102, v94\n"
"v_mfma_f32_16x16x32_bf16 v[132:135], a[84:87], v[164:167], v[132:135]\n"
"buffer_load_dwordx4 v[108:111], v1, s[4:7], s97 offen offset:2048 sc1\n"
"v_mfma_f32_16x16x32_bf16 v[132:135], a[88:91], v[160:163], v[132:135]\n"
"ds_read_b128 v[200:203], v6 offset:2048\n"
"ds_read_b128 v[204:207], v6 offset:3072\n"
"v_mfma_f32_16x16x32_bf16 v[132:135], a[92:95], v[168:171], v[132:135]\n"
"v_add_f32_e32 v103, v103, v95\n"
"v_add_f32_e32 v124, v124, v144\n"
"v_mfma_f32_16x16x32_bf16 v[208:211], a[124:127], v[168:171], v[208:211]\n"
"buffer_load_dwordx4 v[92:95], v1, s[4:7], s97 offen offset:3072 sc1\n"
"v_mfma_f32_16x16x32_bf16 v[208:211], a[120:123], v[160:163], v[208:211]\n"
"s_add_u32 s97, s57, 0x440000\n"
"v_add_f32_e32 v125, v125, v145\n"
"v_add_f32_e32 v126, v126, v146\n"
"v_mfma_f32_16x16x32_bf16 v[208:211], a[116:119], v[164:167], v[208:211]\n"
"v_add_f32_e32 v127, v127, v147\n"
"v_add_f32_e32 v184, v184, v180\n"
"v_mfma_f32_16x16x32_bf16 v[208:211], a[112:115], v[148:151], v[208:211]\n"
"buffer_load_dwordx4 v[144:147], v0, s[4:7], s97 offen sc1\n"
"v_mfma_f32_16x16x32_bf16 v[208:211], a[108:111], v[152:155], v[208:211]\n"
"v_add_f32_e32 v185, v185, v181\n"
"v_add_f32_e32 v186, v186, v182\n"
"v_mfma_f32_16x16x32_bf16 v[208:211], a[104:107], v[156:159], v[208:211]\n"
"v_add_f32_e32 v187, v187, v183\n"
"v_mul_f32_e32 v124, v124, v255\n"
"v_mfma_f32_16x16x32_bf16 v[208:211], a[100:103], v[128:131], v[208:211]\n"
"buffer_load_dwordx4 v[180:183], v0, s[4:7], s97 offen offset:1024 sc1\n"
"v_mfma_f32_16x16x32_bf16 v[208:211], a[96:99], v[116:119], v[208:211]\n"
"v_mul_f32_e32 v125, v125, v255\n"
"v_mul_f32_e32 v126, v126, v255\n"
"v_mfma_f32_16x16x32_bf16 v[96:99], v[24:27], v[116:119], v[96:99]\n"
"v_mul_f32_e32 v127, v127, v255\n"
"v_mul_f32_e32 v184, v184, v255\n"
"v_mfma_f32_16x16x32_bf16 v[96:99], v[28:31], v[128:131], v[96:99]\n"
"buffer_load_dwordx4 v[24:27], v0, s[4:7], s97 offen offset:2048 sc1\n"
"v_mfma_f32_16x16x32_bf16 v[96:99], v[32:35], v[156:159], v[96:99]\n"
"v_mul_f32_e32 v185, v185, v255\n"
"v_mul_f32_e32 v186, v186, v255\n"
"v_mfma_f32_16x16x32_bf16 v[96:99], v[36:39], v[152:155], v[96:99]\n"
"v_mul_f32_e32 v187, v187, v255\n"
"v_cvt_pk_bf16_f32 v28, v124, v125\n"
"v_mfma_f32_16x16x32_bf16 v[96:99], v[40:43], v[148:151], v[96:99]\n"
"buffer_load_dwordx4 v[32:35], v0, s[4:7], s97 offen offset:3072 sc1\n"
"v_mfma_f32_16x16x32_bf16 v[96:99], v[44:47], v[164:167], v[96:99]\n"
"s_waitcnt lgkmcnt(0)\n"
"v_cvt_pk_bf16_f32 v29, v126, v127\n"
"v_cvt_pk_bf16_f32 v30, v184, v185\n"
"v_mfma_f32_16x16x32_bf16 v[96:99], v[48:51], v[160:163], v[96:99]\n"
"s_add_u32 s96, s63, 0xc4000\n"
"ds_read_b128 v[36:39], v6 offset:4096\n"
"ds_read_b128 v[40:43], v6 offset:5120\n"
"v_mfma_f32_16x16x32_bf16 v[96:99], v[52:55], v[168:171], v[96:99]\n"
"buffer_load_dwordx4 v[44:47], v1, s[4:7], s97 offen sc1\n"
"v_mfma_f32_16x16x32_bf16 v[100:103], v[84:87], v[168:171], v[100:103]\n"
"buffer_store_dword v176, v14, s[8:11], s96 offen sc1\n"
"v_mfma_f32_16x16x32_bf16 v[100:103], v[80:83], v[160:163], v[100:103]\n"
"v_cvt_pk_bf16_f32 v31, v186, v187\n"
"v_add_f32_e32 v132, v132, v200\n"
"v_mfma_f32_16x16x32_bf16 v[100:103], v[76:79], v[164:167], v[100:103]\n"
"buffer_load_dwordx4 v[48:51], v1, s[4:7], s97 offen offset:1024 sc1\n"
"ds_write_b128 v7, v[28:31] offset:16384\n"
"v_mfma_f32_16x16x32_bf16 v[100:103], v[72:75], v[148:151], v[100:103]\n"
"v_add_f32_e32 v133, v133, v201\n"
"v_add_f32_e32 v134, v134, v202\n"
"v_mfma_f32_16x16x32_bf16 v[100:103], v[68:71], v[152:155], v[100:103]\n"
"v_add_f32_e32 v135, v135, v203\n"
"v_add_f32_e32 v208, v208, v204\n"
"v_mfma_f32_16x16x32_bf16 v[100:103], v[64:67], v[156:159], v[100:103]\n"
"buffer_load_dwordx4 v[28:31], v1, s[4:7], s97 offen offset:2048 sc1\n"
"v_mfma_f32_16x16x32_bf16 v[100:103], v[60:63], v[128:131], v[100:103]\n"
"v_add_f32_e32 v209, v209, v205\n"
"v_add_f32_e32 v210, v210, v206\n"
"v_mfma_f32_16x16x32_bf16 v[100:103], v[56:59], v[116:119], v[100:103]\n"
"buffer_load_dwordx4 a[0:3], v0, s[8:11], s98 offen sc1\n"
"buffer_load_dwordx4 v[56:59], v1, s[4:7], s97 offen offset:3072 sc1\n"
"v_add_f32_e32 v211, v211, v207\n"
"v_mul_f32_e32 v132, v132, v255\n"
"v_mul_f32_e32 v133, v133, v255\n"
"v_mul_f32_e32 v134, v134, v255\n"
"v_mul_f32_e32 v135, v135, v255\n"
"v_mul_f32_e32 v208, v208, v255\n"
"v_mul_f32_e32 v209, v209, v255\n"
"v_mul_f32_e32 v210, v210, v255\n"
"v_mul_f32_e32 v211, v211, v255\n"
"v_cvt_pk_bf16_f32 v52, v132, v133\n"
"v_cvt_pk_bf16_f32 v53, v134, v135\n"
"v_cvt_pk_bf16_f32 v54, v208, v209\n"
"v_cvt_pk_bf16_f32 v55, v210, v211\n"
"v_cndmask_b32_e64 v89, v89, v252, s[82:83]\n"
"v_cndmask_b32_e64 v90, v90, v252, s[82:83]\n"
"v_cndmask_b32_e64 v91, v91, v252, s[82:83]\n"
"v_cndmask_b32_e64 v172, v172, v252, s[82:83]\n"
"v_cndmask_b32_e64 v173, v173, v252, s[82:83]\n"
"v_cndmask_b32_e64 v174, v174, v252, s[82:83]\n"
"v_cndmask_b32_e64 v175, v175, v252, s[82:83]\n"
"ds_write_b128 v7, v[52:55] offset:49152\n"
"buffer_load_dwordx4 a[4:7], v0, s[8:11], s98 offen offset:1024 sc1\n"
"buffer_load_dwordx4 a[8:11], v0, s[8:11], s98 offen offset:2048 sc1\n"
"buffer_load_dwordx4 a[12:15], v0, s[8:11], s98 offen offset:3072 sc1\n"
"buffer_load_dwordx4 a[16:19], v1, s[8:11], s98 offen sc1\n"
"buffer_load_dwordx4 a[20:23], v1, s[8:11], s98 offen offset:1024 sc1\n"
".Lmk_b4:\n"
"; @B tail\n"
";\n"
"s_waitcnt lgkmcnt(2)\n"
"v_add_f32_e32 v96, v96, v36\n"
"v_add_f32_e32 v97, v97, v37\n"
"v_add_f32_e32 v98, v98, v38\n"
"v_add_f32_e32 v99, v99, v39\n"
"v_add_f32_e32 v100, v100, v40\n"
"v_add_f32_e32 v101, v101, v41\n"
"v_add_f32_e32 v102, v102, v42\n"
"v_add_f32_e32 v103, v103, v43\n"
"v_mul_f32_e32 v96, v96, v253\n"
"v_mul_f32_e32 v97, v97, v253\n"
"v_mul_f32_e32 v98, v98, v253\n"
"v_mul_f32_e32 v99, v99, v253\n"
"v_mul_f32_e32 v100, v100, v253\n"
"v_mul_f32_e32 v101, v101, v253\n"
"v_mul_f32_e32 v102, v102, v253\n"
"v_mul_f32_e32 v103, v103, v253\n"
"v_cvt_pk_bf16_f32 v36, v96, v97\n"
"v_cvt_pk_bf16_f32 v37, v98, v99\n"
"v_cvt_pk_bf16_f32 v38, v100, v101\n"
"v_cvt_pk_bf16_f32 v39, v102, v103\n"
"ds_write_b128 v2, v[36:39] offset:16384\n"
"s_waitcnt vmcnt(24)\n"
";\n"
"; @B AT a\n"
";\n"
"s_cmp_lg_u32 s0, 0\n"
"s_cbranch_scc1 .Lmk_exit\n"
"ds_read_b128 v[36:39], v2 offset:17408\n"
"ds_read_b128 v[40:43], v15 offset:16384\n"
"v_xor_b32_e32 v255, 32, v15\n"
"ds_read_b128 v[52:55], v255 offset:16640\n"
"ds_read_b128 v[60:63], v2 offset:16384\n"
"ds_read_b128 v[64:67], v17 offset:15360\n"
"v_xor_b32_e32 v255, 32, v17\n"
"ds_read_b128 v[68:71], v255 offset:15616\n"
"ds_read_b64_tr_b16 v[72:73], v16 offset:16384\n"
"v_xor_b32_e32 v255, 32, v16\n"
"ds_read_b64_tr_b16 v[74:75], v255 offset:16640\n"
"ds_read_b64_tr_b16 v[76:77], v255 offset:16384\n"
"ds_read_b64_tr_b16 v[78:79], v16 offset:16640\n"
"ds_read_b64_tr_b16 v[84:85], v16 offset:15360\n"
"v_mov_b32_e32 v80, 0x3f803f80\n"
"v_mov_b32_e32 v81, 0x3f803f80\n"
"v_mov_b32_e32 v82, 0x3f803f80\n"
"v_mov_b32_e32 v83, 0x3f803f80\n"
"v_xor_b32_e32 v255, 32, v16\n"
"ds_read_b64_tr_b16 v[86:87], v255 offset:15616\n"
"s_waitcnt lgkmcnt(13)\n"
"ds_read_b64_tr_b16 v[96:97], v255 offset:15360\n"
"ds_read_b64_tr_b16 v[98:99], v16 offset:15616\n"
"v_mov_b32_e32 v100, 0x3f803f80\n"
"v_mov_b32_e32 v101, 0x3f803f80\n"
"v_mov_b32_e32 v102, 0x3f803f80\n"
"v_mov_b32_e32 v103, 0x3f803f80\n"
"s_waitcnt lgkmcnt(11)\n"
"v_mfma_f32_16x16x32_bf16 v[104:107], v[40:43], v[36:39], v[104:107]\n"
"v_mfma_f32_16x16x32_bf16 v[112:115], v[52:55], v[36:39], v[112:115]\n"
"s_waitcnt lgkmcnt(8)\n"
"v_mfma_f32_16x16x32_bf16 v[88:91], v[64:67], v[60:63], v[88:91]\n"
"v_mfma_f32_16x16x32_bf16 v[172:175], v[68:71], v[60:63], v[172:175]\n"
"s_waitcnt lgkmcnt(4)\n"
"s_nop 2\n"
"v_max_f32_e32 v255, v104, v105\n"
"s_nop 1\n"
"v_max_f32_e32 v254, v88, v89\n"
"v_max3_f32 v255, v255, v106, v107\n"
"v_max3_f32 v254, v254, v90, v91\n"
"v_max3_f32 v255, v255, v112, v113\n"
"v_max3_f32 v254, v254, v172, v173\n"
"v_max3_f32 v255, v255, v114, v115\n"
"v_max3_f32 v254, v254, v174, v175\n"
"v_mov_b32_e32 v253, v255\n"
"v_mov_b32_e32 v252, v254\n"
"s_nop 0\n"
"v_permlane16_swap_b32_e32 v255, v253\n"
"v_permlane16_swap_b32_e32 v254, v252\n"
"v_max_f32_e32 v255, v255, v253\n"
"v_max_f32_e32 v254, v254, v252\n"
"v_mov_b32_e32 v253, v255\n"
"v_mov_b32_e32 v252, v254\n"
"s_nop 0\n"
"v_permlane32_swap_b32_e32 v255, v253\n"
"v_permlane32_swap_b32_e32 v254, v252\n"
"v_max_f32_e32 v255, v255, v253\n"
"v_max_f32_e32 v254, v254, v252\n"
"v_sub_f32_e32 v104, v104, v255\n"
"v_sub_f32_e32 v105, v105, v255\n"
"v_sub_f32_e32 v106, v106, v255\n"
"v_sub_f32_e32 v107, v107, v255\n"
"v_sub_f32_e32 v112, v112, v255\n"
"v_sub_f32_e32 v113, v113, v255\n"
"v_sub_f32_e32 v114, v114, v255\n"
"v_sub_f32_e32 v115, v115, v255\n"
"v_exp_f32_e32 v104, v104\n"
"v_exp_f32_e32 v105, v105\n"
"v_exp_f32_e32 v106, v106\n"
"v_exp_f32_e32 v107, v107\n"
"v_exp_f32_e32 v112, v112\n"
"v_exp_f32_e32 v113, v113\n"
"v_exp_f32_e32 v114, v114\n"
"v_exp_f32_e32 v115, v115\n"
"v_cvt_pk_bf16_f32 v36, v104, v105\n"
"v_cvt_pk_bf16_f32 v37, v106, v107\n"
"v_cvt_pk_bf16_f32 v38, v112, v113\n"
"v_cvt_pk_bf16_f32 v39, v114, v115\n"
"v_sub_f32_e32 v88, v88, v254\n"
"v_sub_f32_e32 v89, v89, v254\n"
"v_sub_f32_e32 v90, v90, v254\n"
"v_sub_f32_e32 v91, v91, v254\n"
"v_sub_f32_e32 v172, v172, v254\n"
"v_sub_f32_e32 v173, v173, v254\n"
"v_sub_f32_e32 v174, v174, v254\n"
"v_mfma_f32_16x16x32_bf16 v[40:43], v[80:83], v[36:39], 0\n"
"v_sub_f32_e32 v175, v175, v254\n"
"v_exp_f32_e32 v88, v88\n"
"v_exp_f32_e32 v89, v89\n"
"v_exp_f32_e32 v90, v90\n"
"v_mfma_f32_16x16x32_bf16 v[52:55], v[72:75], v[36:39], 0\n"
"v_exp_f32_e32 v91, v91\n"
"v_exp_f32_e32 v172, v172\n"
"v_exp_f32_e32 v173, v173\n"
"v_exp_f32_e32 v174, v174\n"
"v_mfma_f32_16x16x32_bf16 v[60:63], v[76:79], v[36:39], 0\n"
"s_waitcnt lgkmcnt(0)\n"
"v_exp_f32_e32 v175, v175\n"
"v_cvt_pk_bf16_f32 v36, v88, v89\n"
"v_cvt_pk_bf16_f32 v37, v90, v91\n"
"v_cvt_pk_bf16_f32 v38, v172, v173\n"
"v_cvt_pk_bf16_f32 v39, v174, v175\n"
"v_rcp_f32_e32 v40, v40\n"
"s_nop 0\n"
"v_mul_f32_e32 v52, v52, v40\n"
"v_mul_f32_e32 v53, v53, v40\n"
"v_mul_f32_e32 v54, v54, v40\n"
"v_mfma_f32_16x16x32_bf16 v[64:67], v[100:103], v[36:39], 0\n"
"v_mul_f32_e32 v55, v55, v40\n"
"v_mul_f32_e32 v60, v60, v40\n"
"v_mul_f32_e32 v61, v61, v40\n"
"v_mul_f32_e32 v62, v62, v40\n"
"v_mul_f32_e32 v63, v63, v40\n"
"v_mfma_f32_16x16x32_bf16 v[40:43], v[84:87], v[36:39], 0\n"
"s_add_u32 s97, s60, 0x408000\n"
"v_cvt_pk_bf16_f32 v68, v52, v53\n"
"v_cvt_pk_bf16_f32 v69, v54, v55\n"
"v_cvt_pk_bf16_f32 v70, v60, v61\n"
"v_cvt_pk_bf16_f32 v71, v62, v63\n"
"v_mfma_f32_16x16x32_bf16 v[52:55], v[96:99], v[36:39], 0\n"
"buffer_store_dwordx4 v[68:71], v0, s[4:7], s97 offen sc1\n"
"s_add_u32 s97, s60, 0x400000\n"
"v_rcp_f32_e32 v64, v64\n"
"s_nop 0\n"
"v_mul_f32_e32 v40, v40, v64\n"
"v_mul_f32_e32 v41, v41, v64\n"
"v_mul_f32_e32 v42, v42, v64\n"
"v_mul_f32_e32 v43, v43, v64\n"
"v_mul_f32_e32 v52, v52, v64\n"
"v_mul_f32_e32 v53, v53, v64\n"
"v_mul_f32_e32 v54, v54, v64\n"
"v_mul_f32_e32 v55, v55, v64\n"
"v_cvt_pk_bf16_f32 v36, v40, v41\n"
"v_cvt_pk_bf16_f32 v37, v42, v43\n"
"v_cvt_pk_bf16_f32 v38, v52, v53\n"
"v_cvt_pk_bf16_f32 v39, v54, v55\n"
"buffer_store_dwordx4 v[36:39], v0, s[4:7], s97 offen sc1\n"
";\n"
"s_add_u32 s97, s58, 0x440000\n"
"buffer_load_dwordx4 v[36:39], v0, s[4:7], s97 offen sc1\n"
"buffer_load_dwordx4 v[40:43], v0, s[4:7], s97 offen offset:1024 sc1\n"
"buffer_load_dwordx4 v[52:55], v0, s[4:7], s97 offen offset:2048 sc1\n"
"buffer_load_dwordx4 v[60:63], v0, s[4:7], s97 offen offset:3072 sc1\n"
"buffer_load_dwordx4 v[64:67], v1, s[4:7], s97 offen sc1\n"
"buffer_load_dwordx4 v[68:71], v1, s[4:7], s97 offen offset:1024 sc1\n"
"buffer_load_dwordx4 v[72:75], v1, s[4:7], s97 offen offset:2048 sc1\n"
"buffer_load_dwordx4 v[76:79], v1, s[4:7], s97 offen offset:3072 sc1\n"
"buffer_load_dwordx4 a[24:27], v1, s[8:11], s98 offen offset:2048 sc1\n"
"buffer_load_dwordx4 a[28:31], v1, s[8:11], s98 offen offset:3072 sc1\n"
"s_add_u32 s98, s69, 0x2000\n"
"buffer_load_dwordx4 a[32:35], v0, s[8:11], s98 offen sc1\n"
"buffer_load_dwordx4 a[36:39], v0, s[8:11], s98 offen offset:1024 sc1\n"
"buffer_load_dwordx4 a[40:43], v0, s[8:11], s98 offen offset:2048 sc1\n"
"buffer_load_dwordx4 a[44:47], v0, s[8:11], s98 offen offset:3072 sc1\n"
"buffer_load_dwordx4 a[48:51], v1, s[8:11], s98 offen sc1\n"
"buffer_load_dwordx4 a[52:55], v1, s[8:11], s98 offen offset:1024 sc1\n"
"s_waitcnt vmcnt(39)\n"
"; @B G2 b.0\n"
"v_mfma_f32_16x16x32_bf16 v[80:83], a[128:131], v[120:123], 0\n"
"v_mfma_f32_16x16x32_bf16 v[80:83], a[132:135], v[136:139], v[80:83]\n"
"s_waitcnt vmcnt(37)\n"
"v_mfma_f32_16x16x32_bf16 v[80:83], a[136:139], v[140:143], v[80:83]\n"
"s_add_u32 s97, s59, 0x440000\n"
"buffer_load_dwordx4 v[84:87], v0, s[4:7], s97 offen sc1\n"
"v_mfma_f32_16x16x32_bf16 v[80:83], a[140:143], v[192:195], v[80:83]\n"
"s_waitcnt vmcnt(36)\n"
"v_mfma_f32_16x16x32_bf16 v[80:83], a[144:147], v[196:199], v[80:83]\n"
"v_mfma_f32_16x16x32_bf16 v[80:83], a[148:151], v[188:191], v[80:83]\n"
"s_waitcnt vmcnt(34)\n"
"buffer_load_dwordx4 v[88:91], v0, s[4:7], s97 offen offset:1024 sc1\n"
"v_mfma_f32_16x16x32_bf16 v[80:83], a[152:155], v[108:111], v[80:83]\n"
"v_mfma_f32_16x16x32_bf16 v[80:83], a[156:159], v[92:95], v[80:83]\n"
"v_mfma_f32_16x16x32_bf16 v[96:99], a[188:191], v[92:95], 0\n"
"buffer_load_dwordx4 v[100:103], v0, s[4:7], s97 offen offset:2048 sc1\n"
"v_mfma_f32_16x16x32_bf16 v[96:99], a[184:187], v[108:111], v[96:99]\n"
"v_mfma_f32_16x16x32_bf16 v[96:99], a[180:183], v[188:191], v[96:99]\n"
"v_mfma_f32_16x16x32_bf16 v[96:99], a[176:179], v[196:199], v[96:99]\n"
"buffer_load_dwordx4 v[104:107], v0, s[4:7], s97 offen offset:3072 sc1\n"
"v_mfma_f32_16x16x32_bf16 v[96:99], a[172:175], v[192:195], v[96:99]\n"
"s_nop 0\n"
"v_cmp_u_f32_e32 vcc, v80, v80\n"
"v_mfma_f32_16x16x32_bf16 v[96:99], a[168:171], v[140:143], v[96:99]\n"
"s_cbranch_vccnz .Lmk_p68s\n"
".Lmk_p68r:\n"
"v_mfma_f32_16x16x32_bf16 v[96:99], a[164:167], v[136:139], v[96:99]\n"
"ds_write_b128 v3, v[80:83]\n"
"v_mfma_f32_16x16x32_bf16 v[96:99], a[160:163], v[120:123], v[96:99]\n"
"buffer_load_dwordx4 v[80:83], v1, s[4:7], s97 offen sc1\n"
"v_mfma_f32_16x16x32_bf16 v[112:115], a[192:195], v[120:123], 0\n"
"v_mfma_f32_16x16x32_bf16 v[112:115], a[196:199], v[136:139], v[112:115]\n"
"v_mfma_f32_16x16x32_bf16 v[112:115], a[200:203], v[140:143], v[112:115]\n"
"buffer_load_dwordx4 v[116:119], v1, s[4:7], s97 offen offset:1024 sc1\n"
"v_mfma_f32_16x16x32_bf16 v[112:115], a[204:207], v[192:195], v[112:115]\n"
"v_mfma_f32_16x16x32_bf16 v[112:115], a[208:211], v[196:199], v[112:115]\n"
"v_mfma_f32_16x16x32_bf16 v[112:115], a[212:215], v[188:191], v[112:115]\n"
"ds_write_b128 v3, v[96:99] offset:1024\n"
"v_mfma_f32_16x16x32_bf16 v[112:115], a[216:219], v[108:111], v[112:115]\n"
"buffer_load_dwordx4 v[96:99], v1, s[4:7], s97 offen offset:2048 sc1\n"
"v_mfma_f32_16x16x32_bf16 v[112:115], a[220:223], v[92:95], v[112:115]\n"
"v_mfma_f32_16x16x32_bf16 v[124:127], a[252:255], v[92:95], 0\n"
"v_mfma_f32_16x16x32_bf16 v[124:127], a[248:251], v[108:111], v[124:127]\n"
"buffer_load_dwordx4 v[92:95], v1, s[4:7], s97 offen offset:3072 sc1\n"
"v_mfma_f32_16x16x32_bf16 v[124:127], a[244:247], v[188:191], v[124:127]\n"
"v_mfma_f32_16x16x32_bf16 v[124:127], a[240:243], v[196:199], v[124:127]\n"
"v_mfma_f32_16x16x32_bf16 v[124:127], a[236:239], v[192:195], v[124:127]\n"
"s_mov_b32 s97, .Lmk_a0-.Lmk_g72\n"
"s_getpc_b64 s[84:85]\n"
".Lmk_g72:\n"
"s_add_u32 s84, s84, s97\n"
"s_addc_u32 s85, s85, 0\n"
"s_and_b32 s84, s84, -16\n"
"v_mfma_f32_16x16x32_bf16 v[124:127], a[232:235], v[140:143], v[124:127]\n"
"ds_write_b128 v3, v[112:115] offset:2048\n"
"v_mfma_f32_16x16x32_bf16 v[124:127], a[228:231], v[136:139], v[124:127]\n"
"s_and_b32 s85, s85, 0xffff\n"
"s_sub_u32 s97, .Lmk_end-.Lmk_g72, s97\n"
"s_min_u32 s86, s97, 0x3000\n"
"s_mov_b32 s87, 0x20000\n"
"v_mfma_f32_16x16x32_bf16 v[124:127], a[224:227], v[120:123], v[124:127]\n"
"s_waitcnt vmcnt(37)\n"
"s_mov_b32 m0, 0x27800\n"
"s_nop 0\n"
"buffer_load_dwordx4 v0, s[84:87], s99 offen lds\n"
"; @B G2 b.1\n"
"v_mfma_f32_16x16x32_bf16 v[108:111], a[128:131], v[144:147], 0\n"
"v_mfma_f32_16x16x32_bf16 v[108:111], a[132:135], v[180:183], v[108:111]\n"
"v_mfma_f32_16x16x32_bf16 v[108:111], a[136:139], v[24:27], v[108:111]\n"
"v_mfma_f32_16x16x32_bf16 v[108:111], a[140:143], v[32:35], v[108:111]\n"
"s_waitcnt vmcnt(32)\n"
"v_mfma_f32_16x16x32_bf16 v[108:111], a[144:147], v[44:47], v[108:111]\n"
"v_mfma_f32_16x16x32_bf16 v[108:111], a[148:151], v[48:51], v[108:111]\n"
"ds_write_b128 v3, v[124:127] offset:3072\n"
"v_mfma_f32_16x16x32_bf16 v[108:111], a[152:155], v[28:31], v[108:111]\n"
"v_mfma_f32_16x16x32_bf16 v[108:111], a[156:159], v[56:59], v[108:111]\n"
"v_mfma_f32_16x16x32_bf16 v[112:115], a[188:191], v[56:59], 0\n"
"s_add_u32 s97, s56, 0x400000\n"
"buffer_load_dwordx4 v[120:123], v0, s[4:7], s97 offen sc1\n"
"v_mfma_f32_16x16x32_bf16 v[112:115], a[184:187], v[28:31], v[112:115]\n"
"v_mfma_f32_16x16x32_bf16 v[112:115], a[180:183], v[48:51], v[112:115]\n"
"v_mfma_f32_16x16x32_bf16 v[112:115], a[176:179], v[44:47], v[112:115]\n"
"buffer_load_dwordx4 v[124:127], v0, s[4:7], s97 offen offset:1024 sc1\n"
"v_mfma_f32_16x16x32_bf16 v[112:115], a[172:175], v[32:35], v[112:115]\n"
"v_cmp_u_f32_e32 vcc, v108, v108\n"
"v_mfma_f32_16x16x32_bf16 v[112:115], a[168:171], v[24:27], v[112:115]\n"
"s_cbranch_vccnz .Lmk_p69s\n"
".Lmk_p69r:\n"
"v_mfma_f32_16x16x32_bf16 v[112:115], a[164:167], v[180:183], v[112:115]\n"
"ds_write_b128 v3, v[108:111] offset:6144\n"
"v_mfma_f32_16x16x32_bf16 v[112:115], a[160:163], v[144:147], v[112:115]\n"
"buffer_load_dwordx4 v[108:111], v0, s[4:7], s97 offen offset:2048 sc1\n"
"v_mfma_f32_16x16x32_bf16 v[128:131], a[192:195], v[144:147], 0\n"
"v_mfma_f32_16x16x32_bf16 v[128:131], a[196:199], v[180:183], v[128:131]\n"
"v_mfma_f32_16x16x32_bf16 v[128:131], a[200:203], v[24:27], v[128:131]\n"
"buffer_load_dwordx4 v[132:135], v0, s[4:7], s97 offen offset:3072 sc1\n"
"v_mfma_f32_16x16x32_bf16 v[128:131], a[204:207], v[32:35], v[128:131]\n"
"v_mfma_f32_16x16x32_bf16 v[128:131], a[208:211], v[44:47], v[128:131]\n"
"ds_read_b128 v[136:139], v2 offset:8192\n"
"ds_read_b128 v[140:143], v2 offset:9216\n"
"v_mfma_f32_16x16x32_bf16 v[128:131], a[212:215], v[48:51], v[128:131]\n"
"ds_write_b128 v3, v[112:115] offset:7168\n"
"v_mfma_f32_16x16x32_bf16 v[128:131], a[216:219], v[28:31], v[128:131]\n"
"buffer_load_dwordx4 v[112:115], v1, s[4:7], s97 offen sc1\n"
"v_mfma_f32_16x16x32_bf16 v[128:131], a[220:223], v[56:59], v[128:131]\n"
"v_mfma_f32_16x16x32_bf16 v[148:151], a[252:255], v[56:59], 0\n"
"v_mfma_f32_16x16x32_bf16 v[148:151], a[248:251], v[28:31], v[148:151]\n"
"buffer_load_dwordx4 v[28:31], v1, s[4:7], s97 offen offset:1024 sc1\n"
"s_waitcnt lgkmcnt(1)\n"
"v_mfma_f32_16x16x32_bf16 v[148:151], a[244:247], v[48:51], v[148:151]\n"
"v_lshlrev_b32_e32 v255, 16, v136\n"
"v_and_b32_e32 v254, 0xffff0000, v136\n"
"v_lshlrev_b32_e32 v253, 16, v137\n"
"v_mfma_f32_16x16x32_bf16 v[148:151], a[240:243], v[44:47], v[148:151]\n"
"v_and_b32_e32 v252, 0xffff0000, v137\n"
"v_lshlrev_b32_e32 v251, 16, v138\n"
"v_and_b32_e32 v250, 0xffff0000, v138\n"
"v_mfma_f32_16x16x32_bf16 v[148:151], a[236:239], v[32:35], v[148:151]\n"
"buffer_load_dwordx4 v[32:35], v1, s[4:7], s97 offen offset:2048 sc1\n"
"v_lshlrev_b32_e32 v249, 16, v139\n"
"v_mfma_f32_16x16x32_bf16 v[148:151], a[232:235], v[24:27], v[148:151]\n"
"ds_write_b128 v3, v[128:131] offset:8192\n"
"v_mfma_f32_16x16x32_bf16 v[148:151], a[228:231], v[180:183], v[148:151]\n"
"v_and_b32_e32 v248, 0xffff0000, v139\n"
"v_lshlrev_b32_e32 v247, 16, v140\n"
"v_and_b32_e32 v246, 0xffff0000, v140\n"
"v_mfma_f32_16x16x32_bf16 v[148:151], a[224:227], v[144:147], v[148:151]\n"
"s_waitcnt vmcnt(28)\n"
"buffer_load_dwordx4 v[24:27], v1, s[4:7], s97 offen offset:3072 sc1\n"
"; @B G2 b.2\n"
"v_mfma_f32_16x16x32_bf16 v[44:47], a[128:131], v[36:39], 0\n"
"s_add_u32 s97, s57, 0x400000\n"
"v_lshlrev_b32_e32 v245, 16, v141\n"
"v_and_b32_e32 v244, 0xffff0000, v141\n"
"v_lshlrev_b32_e32 v243, 16, v142\n"
"v_mfma_f32_16x16x32_bf16 v[44:47], a[132:135], v[40:43], v[44:47]\n"
"v_and_b32_e32 v242, 0xffff0000, v142\n"
"v_lshlrev_b32_e32 v241, 16, v143\n"
"v_and_b32_e32 v240, 0xffff0000, v143\n"
"v_mfma_f32_16x16x32_bf16 v[44:47], a[136:139], v[52:55], v[44:47]\n"
"buffer_load_dwordx4 v[48:51], v0, s[4:7], s97 offen sc1\n"
"v_mfma_f32_16x16x32_bf16 v[44:47], a[140:143], v[60:63], v[44:47]\n"
"s_waitcnt vmcnt(26)\n"
"v_mfma_f32_16x16x32_bf16 v[44:47], a[144:147], v[64:67], v[44:47]\n"
"v_mfma_f32_16x16x32_bf16 v[44:47], a[148:151], v[68:71], v[44:47]\n"
"ds_write_b128 v3, v[148:151] offset:9216\n"
"v_mfma_f32_16x16x32_bf16 v[44:47], a[152:155], v[72:75], v[44:47]\n"
"buffer_load_dwordx4 v[56:59], v0, s[4:7], s97 offen offset:1024 sc1\n"
"v_mfma_f32_16x16x32_bf16 v[44:47], a[156:159], v[76:79], v[44:47]\n"
"v_mfma_f32_16x16x32_bf16 v[128:131], a[188:191], v[76:79], 0\n"
"v_mfma_f32_16x16x32_bf16 v[128:131], a[184:187], v[72:75], v[128:131]\n"
"s_waitcnt lgkmcnt(0)\n"
"s_barrier\n"
"v_mfma_f32_16x16x32_bf16 v[128:131], a[180:183], v[68:71], v[128:131]\n"
"buffer_load_dwordx4 v[136:139], v0, s[4:7], s97 offen offset:2048 sc1\n"
"v_mfma_f32_16x16x32_bf16 v[128:131], a[176:179], v[64:67], v[128:131]\n"
"ds_read_b128 v[140:143], v4\n"
"ds_read_b128 v[144:147], v5\n"
"v_mfma_f32_16x16x32_bf16 v[128:131], a[172:175], v[60:63], v[128:131]\n"
"v_cmp_u_f32_e32 vcc, v44, v44\n"
"v_mfma_f32_16x16x32_bf16 v[128:131], a[168:171], v[52:55], v[128:131]\n"
"s_cbranch_vccnz .Lmk_p70s\n"
".Lmk_p70r:\n"
"v_mfma_f32_16x16x32_bf16 v[128:131], a[164:167], v[40:43], v[128:131]\n"
"ds_write_b128 v3, v[44:47] offset:12288\n"
"v_mfma_f32_16x16x32_bf16 v[128:131], a[160:163], v[36:39], v[128:131]\n"
"buffer_load_dwordx4 v[44:47], v0, s[4:7], s97 offen offset:3072 sc1\n"
"v_mfma_f32_16x16x32_bf16 v[148:151], a[192:195], v[36:39], 0\n"
"ds_read_b128 v[152:155], v4 offset:1024\n"
"ds_read_b128 v[156:159], v5 offset:1024\n"
"s_waitcnt lgkmcnt(3)\n"
"v_mfma_f32_16x16x32_bf16 v[148:151], a[196:199], v[40:43], v[148:151]\n"
"v_add_f32_e32 v140, v140, v255\n"
"v_add_f32_e32 v141, v141, v254\n"
"v_add_f32_e32 v142, v142, v253\n"
"v_mfma_f32_16x16x32_bf16 v[148:151], a[200:203], v[52:55], v[148:151]\n"
"buffer_load_dwordx4 v[160:163], v1, s[4:7], s97 offen sc1\n"
"v_add_f32_e32 v143, v143, v252\n"
"v_mfma_f32_16x16x32_bf16 v[148:151], a[204:207], v[60:63], v[148:151]\n"
"v_add_f32_e32 v140, v140, v144\n"
"v_add_f32_e32 v141, v141, v145\n"
"v_add_f32_e32 v142, v142, v146\n"
"v_mfma_f32_16x16x32_bf16 v[148:151], a[208:211], v[64:67], v[148:151]\n"
"v_add_f32_e32 v143, v143, v147\n"
"v_mfma_f32_16x16x32_bf16 v[148:151], a[212:215], v[68:71], v[148:151]\n"
"ds_write_b128 v3, v[128:131] offset:13312\n"
"v_mfma_f32_16x16x32_bf16 v[148:151], a[216:219], v[72:75], v[148:151]\n"
"buffer_load_dwordx4 v[128:131], v1, s[4:7], s97 offen offset:1024 sc1\n"
"s_waitcnt lgkmcnt(1)\n"
"v_mfma_f32_16x16x32_bf16 v[148:151], a[220:223], v[76:79], v[148:151]\n"
"ds_read_b128 v[144:147], v4 offset:2048\n"
"ds_read_b128 v[164:167], v5 offset:2048\n"
"v_add_f32_e32 v152, v152, v251\n"
"v_mfma_f32_16x16x32_bf16 v[168:171], a[252:255], v[76:79], 0\n"
"v_add_f32_e32 v153, v153, v250\n"
"v_add_f32_e32 v154, v154, v249\n"
"v_add_f32_e32 v155, v155, v248\n"
"v_mfma_f32_16x16x32_bf16 v[168:171], a[248:251], v[72:75], v[168:171]\n"
"buffer_load_dwordx4 v[72:75], v1, s[4:7], s97 offen offset:2048 sc1\n"
"v_add_f32_e32 v152, v152, v156\n"
"v_mfma_f32_16x16x32_bf16 v[168:171], a[244:247], v[68:71], v[168:171]\n"
"v_add_f32_e32 v153, v153, v157\n"
"v_add_f32_e32 v154, v154, v158\n"
"v_add_f32_e32 v155, v155, v159\n"
"v_mfma_f32_16x16x32_bf16 v[168:171], a[240:243], v[64:67], v[168:171]\n"
"v_mfma_f32_16x16x32_bf16 v[168:171], a[236:239], v[60:63], v[168:171]\n"
"buffer_load_dwordx4 v[60:63], v1, s[4:7], s97 offen offset:3072 sc1\n"
"s_waitcnt lgkmcnt(0)\n"
"v_mfma_f32_16x16x32_bf16 v[168:171], a[232:235], v[52:55], v[168:171]\n"
"ds_write_b128 v3, v[148:151] offset:14336\n"
"v_mfma_f32_16x16x32_bf16 v[168:171], a[228:231], v[40:43], v[168:171]\n"
"s_add_u32 s97, s58, 0x400000\n"
"ds_read_b128 v[40:43], v4 offset:3072\n"
"ds_read_b128 v[52:55], v5 offset:3072\n"
"v_add_f32_e32 v144, v144, v247\n"
"v_mfma_f32_16x16x32_bf16 v[168:171], a[224:227], v[36:39], v[168:171]\n"
"s_waitcnt vmcnt(21)\n"
"buffer_load_dwordx4 v[36:39], v0, s[4:7], s97 offen sc1\n"
"; @B G2 b.3\n"
"v_mfma_f32_16x16x32_bf16 v[64:67], a[128:131], v[84:87], v[140:143]\n"
"v_add_f32_e32 v145, v145, v246\n"
"v_add_f32_e32 v146, v146, v245\n"
"v_add_f32_e32 v147, v147, v244\n"
"v_mfma_f32_16x16x32_bf16 v[64:67], a[132:135], v[88:91], v[64:67]\n"
"v_add_f32_e32 v144, v144, v164\n"
"v_add_f32_e32 v145, v145, v165\n"
"v_add_f32_e32 v146, v146, v166\n"
"v_mfma_f32_16x16x32_bf16 v[64:67], a[136:139], v[100:103], v[64:67]\n"
"buffer_load_dwordx4 v[68:71], v0, s[4:7], s97 offen offset:1024 sc1\n"
"v_add_f32_e32 v147, v147, v167\n"
"v_mfma_f32_16x16x32_bf16 v[64:67], a[140:143], v[104:107], v[64:67]\n"
"s_waitcnt vmcnt(19)\n"
"s_waitcnt lgkmcnt(0)\n"
"v_add_f32_e32 v40, v40, v243\n"
"v_add_f32_e32 v41, v41, v242\n"
"v_mfma_f32_16x16x32_bf16 v[64:67], a[144:147], v[80:83], v[64:67]\n"
"v_add_f32_e32 v42, v42, v241\n"
"v_add_f32_e32 v43, v43, v240\n"
"v_add_f32_e32 v40, v40, v52\n"
"v_mfma_f32_16x16x32_bf16 v[64:67], a[148:151], v[116:119], v[64:67]\n"
"ds_write_b128 v3, v[168:171] offset:15360\n"
"v_mfma_f32_16x16x32_bf16 v[64:67], a[152:155], v[96:99], v[64:67]\n"
"buffer_load_dwordx4 v[76:79], v0, s[4:7], s97 offen offset:2048 sc1\n"
"v_add_f32_e32 v41, v41, v53\n"
"v_mfma_f32_16x16x32_bf16 v[64:67], a[156:159], v[92:95], v[64:67]\n"
"v_add_f32_e32 v42, v42, v54\n"
"v_add_f32_e32 v43, v43, v55\n"
"v_mfma_f32_16x16x32_bf16 v[52:55], a[188:191], v[92:95], v[152:155]\n"
"v_mfma_f32_16x16x32_bf16 v[52:55], a[184:187], v[96:99], v[52:55]\n"
"s_waitcnt lgkmcnt(0)\n"
"s_barrier\n"
"v_mfma_f32_16x16x32_bf16 v[52:55], a[180:183], v[116:119], v[52:55]\n"
"buffer_load_dwordx4 v[148:151], v0, s[4:7], s97 offen offset:3072 sc1\n"
"v_mfma_f32_16x16x32_bf16 v[52:55], a[176:179], v[80:83], v[52:55]\n"
"ds_read_b128 v[156:159], v6\n"
"ds_read_b128 v[164:167], v6 offset:1024\n"
"v_mfma_f32_16x16x32_bf16 v[52:55], a[172:175], v[104:107], v[52:55]\n"
"ds_read_b128 v[168:171], v6 offset:2048\n"
"v_cmp_u_f32_e32 vcc, v64, v64\n"
"v_mfma_f32_16x16x32_bf16 v[52:55], a[168:171], v[100:103], v[52:55]\n"
"s_cbranch_vccnz .Lmk_p71s\n"
".Lmk_p71r:\n"
"ds_read_b128 v[140:143], v6 offset:3072\n"
"v_mfma_f32_16x16x32_bf16 v[52:55], a[164:167], v[88:91], v[52:55]\n"
"buffer_load_dwordx4 v[152:155], v1, s[4:7], s97 offen sc1\n"
"v_mfma_f32_16x16x32_bf16 v[52:55], a[160:163], v[84:87], v[52:55]\n"
"v_mfma_f32_16x16x32_bf16 v[144:147], a[192:195], v[84:87], v[144:147]\n"
"v_mfma_f32_16x16x32_bf16 v[144:147], a[196:199], v[88:91], v[144:147]\n"
"buffer_load_dwordx4 v[172:175], v1, s[4:7], s97 offen offset:1024 sc1\n"
"v_mfma_f32_16x16x32_bf16 v[144:147], a[200:203], v[100:103], v[144:147]\n"
"v_mfma_f32_16x16x32_bf16 v[144:147], a[204:207], v[104:107], v[144:147]\n"
"v_mfma_f32_16x16x32_bf16 v[144:147], a[208:211], v[80:83], v[144:147]\n"
"buffer_load_dwordx4 v[176:179], v1, s[4:7], s97 offen offset:2048 sc1\n"
"s_waitcnt lgkmcnt(0)\n"
"v_mfma_f32_16x16x32_bf16 v[144:147], a[212:215], v[116:119], v[144:147]\n"
"v_add_f32_e32 v156, v64, v156\n"
"v_add_f32_e32 v157, v65, v157\n"
"v_add_f32_e32 v158, v66, v158\n"
"v_mfma_f32_16x16x32_bf16 v[144:147], a[216:219], v[96:99], v[144:147]\n"
"v_add_f32_e32 v159, v67, v159\n"
"v_add_f32_e32 v164, v52, v164\n"
"v_add_f32_e32 v165, v53, v165\n"
"v_mfma_f32_16x16x32_bf16 v[144:147], a[220:223], v[92:95], v[144:147]\n"
"buffer_load_dwordx4 v[64:67], v1, s[4:7], s97 offen offset:3072 sc1\n"
"v_add_f32_e32 v166, v54, v166\n"
"v_mfma_f32_16x16x32_bf16 v[40:43], a[252:255], v[92:95], v[40:43]\n"
"v_add_f32_e32 v167, v55, v167\n"
"v_cvt_pk_bf16_f32 v52, v156, v157\n"
"v_cvt_pk_bf16_f32 v53, v158, v159\n"
"v_mfma_f32_16x16x32_bf16 v[40:43], a[248:251], v[96:99], v[40:43]\n"
"s_add_u32 s97, s61, 0x840000\n"
"v_cvt_pk_bf16_f32 v54, v164, v165\n"
"v_cvt_pk_bf16_f32 v55, v166, v167\n"
"v_mfma_f32_16x16x32_bf16 v[40:43], a[244:247], v[116:119], v[40:43]\n"
"buffer_store_dwordx4 v[52:55], v0, s[4:7], s97 offen sc1\n"
"v_mfma_f32_16x16x32_bf16 v[40:43], a[240:243], v[80:83], v[40:43]\n"
"ds_write_b128 v2, v[52:55] offset:8192\n"
"v_mfma_f32_16x16x32_bf16 v[40:43], a[236:239], v[104:107], v[40:43]\n"
"v_mfma_f32_16x16x32_bf16 v[40:43], a[232:235], v[100:103], v[40:43]\n"
"v_mfma_f32_16x16x32_bf16 v[40:43], a[228:231], v[88:91], v[40:43]\n"
"v_mfma_f32_16x16x32_bf16 v[40:43], a[224:227], v[84:87], v[40:43]\n"
"; @B fin b\n"
"s_add_u32 s97, s59, 0x400000\n"
"buffer_load_dwordx4 v[80:83], v0, s[4:7], s97 offen sc1\n"
"buffer_load_dwordx4 v[88:91], v0, s[4:7], s97 offen offset:1024 sc1\n"
"v_mov_b32_e32 v84, 0x3f803f80\n"
"v_mov_b32_e32 v85, 0x3f803f80\n"
"v_mov_b32_e32 v86, 0x3f803f80\n"
"v_mov_b32_e32 v87, 0x3f803f80\n"
"v_add_f32_e32 v168, v144, v168\n"
"v_add_f32_e32 v169, v145, v169\n"
"v_add_f32_e32 v170, v146, v170\n"
"v_add_f32_e32 v171, v147, v171\n"
"v_add_f32_e32 v140, v40, v140\n"
"v_add_f32_e32 v141, v41, v141\n"
"v_add_f32_e32 v142, v42, v142\n"
"v_add_f32_e32 v143, v43, v143\n"
"buffer_load_dwordx4 v[40:43], v0, s[4:7], s97 offen offset:2048 sc1\n"
"s_add_u32 s96, s61, 0x840000\n"
"v_cvt_pk_bf16_f32 v92, v168, v169\n"
"v_cvt_pk_bf16_f32 v93, v170, v171\n"
"v_cvt_pk_bf16_f32 v94, v140, v141\n"
"v_cvt_pk_bf16_f32 v95, v142, v143\n"
"buffer_store_dwordx4 v[92:95], v0, s[4:7], s96 offen offset:1024 sc1\n"
"buffer_load_dwordx4 v[96:99], v0, s[4:7], s97 offen offset:3072 sc1\n"
"ds_write_b128 v2, v[92:95] offset:9216\n"
"buffer_load_dwordx4 v[100:103], v1, s[4:7], s97 offen sc1\n"
"v_mfma_f32_16x16x32_bf16 v[104:107], v[84:87], v[52:55], 0\n"
"v_mfma_f32_16x16x32_bf16 v[116:119], v[52:55], v[52:55], 0\n"
"buffer_load_dwordx4 v[52:55], v1, s[4:7], s97 offen offset:1024 sc1\n"
"v_mfma_f32_16x16x32_bf16 v[104:107], v[84:87], v[92:95], v[104:107]\n"
"v_mfma_f32_16x16x32_bf16 v[116:119], v[92:95], v[92:95], v[116:119]\n"
"s_mov_b32 s94, 0xaaaaaaaa\n"
"buffer_load_dwordx4 v[84:87], v1, s[4:7], s97 offen offset:2048 sc1\n"
"s_mov_b32 s95, 0xaaaaaaaa\n"
"s_nop 4\n"
"v_cndmask_b32_e64 v116, v116, v117, s[94:95]\n"
"v_cndmask_b32_e64 v118, v118, v119, s[94:95]\n"
"s_mov_b32 s94, 0xcccccccc\n"
"s_mov_b32 s95, 0xcccccccc\n"
"buffer_load_dwordx4 v[92:95], v1, s[4:7], s97 offen offset:3072 sc1\n"
"v_cndmask_b32_e64 v105, v116, v118, s[94:95]\n"
"s_mov_b32 s94, 0xf0000f\n"
"s_mov_b32 s95, 0xf0000f00\n"
"s_add_u32 s97, s63, 0x84000\n"
"v_cndmask_b32_e64 v104, v104, v105, s[94:95]\n"
"buffer_store_dword v104, v14, s[4:7], s97 offen sc1\n"
"s_waitcnt vmcnt(33)\n"
"; @B G2 a.0\n"
"v_mfma_f32_16x16x32_bf16 v[104:107], a[128:131], v[120:123], 0\n"
"v_mfma_f32_16x16x32_bf16 v[104:107], a[132:135], v[124:127], v[104:107]\n"
"s_waitcnt vmcnt(31)\n"
"v_mfma_f32_16x16x32_bf16 v[104:107], a[136:139], v[108:111], v[104:107]\n"
"buffer_load_dwordx4 a[56:59], v1, s[8:11], s98 offen offset:2048 sc1\n"
"v_mfma_f32_16x16x32_bf16 v[104:107], a[140:143], v[132:135], v[104:107]\n"
"s_waitcnt vmcnt(30)\n"
"v_mfma_f32_16x16x32_bf16 v[104:107], a[144:147], v[112:115], v[104:107]\n"
"v_mfma_f32_16x16x32_bf16 v[104:107], a[148:151], v[28:31], v[104:107]\n"
"s_waitcnt vmcnt(28)\n"
"buffer_load_dwordx4 a[60:63], v1, s[8:11], s98 offen offset:3072 sc1\n"
"v_mfma_f32_16x16x32_bf16 v[104:107], a[152:155], v[32:35], v[104:107]\n"
"s_add_u32 s98, s69, 0x4000\n"
"v_mfma_f32_16x16x32_bf16 v[104:107], a[156:159], v[24:27], v[104:107]\n"
"v_mfma_f32_16x16x32_bf16 v[116:119], a[188:191], v[24:27], 0\n"
"buffer_load_dwordx4 a[64:67], v0, s[8:11], s98 offen sc1\n"
"v_mfma_f32_16x16x32_bf16 v[116:119], a[184:187], v[32:35], v[116:119]\n"
"v_mfma_f32_16x16x32_bf16 v[116:119], a[180:183], v[28:31], v[116:119]\n"
"v_mfma_f32_16x16x32_bf16 v[116:119], a[176:179], v[112:115], v[116:119]\n"
"buffer_load_dwordx4 a[68:71], v0, s[8:11], s98 offen offset:1024 sc1\n"
"v_mfma_f32_16x16x32_bf16 v[116:119], a[172:175], v[132:135], v[116:119]\n"
"s_nop 0\n"
"v_cmp_u_f32_e32 vcc, v104, v104\n"
"v_mfma_f32_16x16x32_bf16 v[116:119], a[168:171], v[108:111], v[116:119]\n"
"s_cbranch_vccnz .Lmk_p73s\n"
".Lmk_p73r:\n"
"v_mfma_f32_16x16x32_bf16 v[116:119], a[164:167], v[124:127], v[116:119]\n"
"ds_write_b128 v3, v[104:107]\n"
"v_mfma_f32_16x16x32_bf16 v[116:119], a[160:163], v[120:123], v[116:119]\n"
"buffer_load_dwordx4 a[72:75], v0, s[8:11], s98 offen offset:2048 sc1\n"
"v_mfma_f32_16x16x32_bf16 v[104:107], a[192:195], v[120:123], 0\n"
"v_mfma_f32_16x16x32_bf16 v[104:107], a[196:199], v[124:127], v[104:107]\n"
"v_mfma_f32_16x16x32_bf16 v[104:107], a[200:203], v[108:111], v[104:107]\n"
"buffer_load_dwordx4 a[76:79], v0, s[8:11], s98 offen offset:3072 sc1\n"
"v_mfma_f32_16x16x32_bf16 v[104:107], a[204:207], v[132:135], v[104:107]\n"
"v_mfma_f32_16x16x32_bf16 v[104:107], a[208:211], v[112:115], v[104:107]\n"
"v_mfma_f32_16x16x32_bf16 v[104:107], a[212:215], v[28:31], v[104:107]\n"
"ds_write_b128 v3, v[116:119] offset:1024\n"
"v_mfma_f32_16x16x32_bf16 v[104:107], a[216:219], v[32:35], v[104:107]\n"
"buffer_load_dwordx4 a[80:83], v1, s[8:11], s98 offen sc1\n"
"v_mfma_f32_16x16x32_bf16 v[104:107], a[220:223], v[24:27], v[104:107]\n"
"v_mfma_f32_16x16x32_bf16 v[116:119], a[252:255], v[24:27], 0\n"
"v_mfma_f32_16x16x32_bf16 v[116:119], a[248:251], v[32:35], v[116:119]\n"
"buffer_load_dwordx4 a[84:87], v1, s[8:11], s98 offen offset:1024 sc1\n"
"v_mfma_f32_16x16x32_bf16 v[116:119], a[244:247], v[28:31], v[116:119]\n"
"v_mfma_f32_16x16x32_bf16 v[116:119], a[240:243], v[112:115], v[116:119]\n"
"v_mfma_f32_16x16x32_bf16 v[116:119], a[236:239], v[132:135], v[116:119]\n"
"buffer_load_dwordx4 a[88:91], v1, s[8:11], s98 offen offset:2048 sc1\n"
"v_mfma_f32_16x16x32_bf16 v[116:119], a[232:235], v[108:111], v[116:119]\n"
"ds_write_b128 v3, v[104:107] offset:2048\n"
"v_mfma_f32_16x16x32_bf16 v[116:119], a[228:231], v[124:127], v[116:119]\n"
"v_mfma_f32_16x16x32_bf16 v[116:119], a[224:227], v[120:123], v[116:119]\n"
"s_waitcnt vmcnt(32)\n"
"buffer_load_dwordx4 a[92:95], v1, s[8:11], s98 offen offset:3072 sc1\n"
"; @B G2 a.1\n"
"v_mfma_f32_16x16x32_bf16 v[24:27], a[128:131], v[48:51], 0\n"
"s_add_u32 s98, s69, 0x6000\n"
"v_mfma_f32_16x16x32_bf16 v[24:27], a[132:135], v[56:59], v[24:27]\n"
"v_mfma_f32_16x16x32_bf16 v[24:27], a[136:139], v[136:139], v[24:27]\n"
"buffer_load_dwordx4 a[96:99], v0, s[8:11], s98 offen sc1\n"
"v_mfma_f32_16x16x32_bf16 v[24:27], a[140:143], v[44:47], v[24:27]\n"
"s_waitcnt vmcnt(30)\n"
"v_mfma_f32_16x16x32_bf16 v[24:27], a[144:147], v[160:163], v[24:27]\n"
"v_mfma_f32_16x16x32_bf16 v[24:27], a[148:151], v[128:131], v[24:27]\n"
"ds_write_b128 v3, v[116:119] offset:3072\n"
"v_mfma_f32_16x16x32_bf16 v[24:27], a[152:155], v[72:75], v[24:27]\n"
"buffer_load_dwordx4 a[100:103], v0, s[8:11], s98 offen offset:1024 sc1\n"
"v_mfma_f32_16x16x32_bf16 v[24:27], a[156:159], v[60:63], v[24:27]\n"
"v_mfma_f32_16x16x32_bf16 v[28:31], a[188:191], v[60:63], 0\n"
"v_mfma_f32_16x16x32_bf16 v[28:31], a[184:187], v[72:75], v[28:31]\n"
"buffer_load_dwordx4 a[104:107], v0, s[8:11], s98 offen offset:2048 sc1\n"
"v_mfma_f32_16x16x32_bf16 v[28:31], a[180:183], v[128:131], v[28:31]\n"
"v_mfma_f32_16x16x32_bf16 v[28:31], a[176:179], v[160:163], v[28:31]\n"
"v_mfma_f32_16x16x32_bf16 v[28:31], a[172:175], v[44:47], v[28:31]\n"
"s_nop 1\n"
"v_cmp_u_f32_e32 vcc, v24, v24\n"
"v_mfma_f32_16x16x32_bf16 v[28:31], a[168:171], v[136:139], v[28:31]\n"
"s_cbranch_vccnz .Lmk_p74s\n"
".Lmk_p74r:\n"
"v_mfma_f32_16x16x32_bf16 v[28:31], a[164:167], v[56:59], v[28:31]\n"
"ds_write_b128 v3, v[24:27] offset:6144\n"
"v_mfma_f32_16x16x32_bf16 v[28:31], a[160:163], v[48:51], v[28:31]\n"
"buffer_load_dwordx4 a[108:111], v0, s[8:11], s98 offen offset:3072 sc1\n"
"v_mfma_f32_16x16x32_bf16 v[24:27], a[192:195], v[48:51], 0\n"
"v_mfma_f32_16x16x32_bf16 v[24:27], a[196:199], v[56:59], v[24:27]\n"
"v_mfma_f32_16x16x32_bf16 v[24:27], a[200:203], v[136:139], v[24:27]\n"
"buffer_load_dwordx4 a[112:115], v1, s[8:11], s98 offen sc1\n"
"v_mfma_f32_16x16x32_bf16 v[24:27], a[204:207], v[44:47], v[24:27]\n"
"v_mfma_f32_16x16x32_bf16 v[24:27], a[208:211], v[160:163], v[24:27]\n"
"ds_read_b128 v[32:35], v2\n"
"ds_read_b128 v[104:107], v2 offset:1024\n"
"v_mfma_f32_16x16x32_bf16 v[24:27], a[212:215], v[128:131], v[24:27]\n"
"ds_write_b128 v3, v[28:31] offset:7168\n"
"v_mfma_f32_16x16x32_bf16 v[24:27], a[216:219], v[72:75], v[24:27]\n"
"buffer_load_dwordx4 a[116:119], v1, s[8:11], s98 offen offset:1024 sc1\n"
"v_mfma_f32_16x16x32_bf16 v[24:27], a[220:223], v[60:63], v[24:27]\n"
"v_mfma_f32_16x16x32_bf16 v[28:31], a[252:255], v[60:63], 0\n"
"v_mfma_f32_16x16x32_bf16 v[28:31], a[248:251], v[72:75], v[28:31]\n"
"buffer_load_dwordx4 a[120:123], v1, s[8:11], s98 offen offset:2048 sc1\n"
"s_waitcnt lgkmcnt(1)\n"
"v_mfma_f32_16x16x32_bf16 v[28:31], a[244:247], v[128:131], v[28:31]\n"
"v_lshlrev_b32_e32 v255, 16, v32\n"
"v_and_b32_e32 v254, 0xffff0000, v32\n"
"v_lshlrev_b32_e32 v253, 16, v33\n"
"v_mfma_f32_16x16x32_bf16 v[28:31], a[240:243], v[160:163], v[28:31]\n"
"v_and_b32_e32 v252, 0xffff0000, v33\n"
"v_lshlrev_b32_e32 v251, 16, v34\n"
"v_and_b32_e32 v250, 0xffff0000, v34\n"
"v_mfma_f32_16x16x32_bf16 v[28:31], a[236:239], v[44:47], v[28:31]\n"
"buffer_load_dwordx4 a[124:127], v1, s[8:11], s98 offen offset:3072 sc1\n"
"v_lshlrev_b32_e32 v249, 16, v35\n"
"v_mfma_f32_16x16x32_bf16 v[28:31], a[232:235], v[136:139], v[28:31]\n"
"ds_write_b128 v3, v[24:27] offset:8192\n"
"v_mfma_f32_16x16x32_bf16 v[28:31], a[228:231], v[56:59], v[28:31]\n"
"s_add_u32 s98, s69, 0x8000\n"
"v_and_b32_e32 v248, 0xffff0000, v35\n"
"v_lshlrev_b32_e32 v247, 16, v104\n"
"v_and_b32_e32 v246, 0xffff0000, v104\n"
"v_mfma_f32_16x16x32_bf16 v[28:31], a[224:227], v[48:51], v[28:31]\n"
"s_waitcnt vmcnt(33)\n"
"buffer_load_dwordx4 v[24:27], v0, s[8:11], s98 offen sc1\n"
"; @B G2 a.2\n"
"v_mfma_f32_16x16x32_bf16 v[32:35], a[128:131], v[36:39], 0\n"
"v_lshlrev_b32_e32 v245, 16, v105\n"
"v_and_b32_e32 v244, 0xffff0000, v105\n"
"v_lshlrev_b32_e32 v243, 16, v106\n"
"v_mfma_f32_16x16x32_bf16 v[32:35], a[132:135], v[68:71], v[32:35]\n"
"v_and_b32_e32 v242, 0xffff0000, v106\n"
"v_lshlrev_b32_e32 v241, 16, v107\n"
"v_and_b32_e32 v240, 0xffff0000, v107\n"
"v_mfma_f32_16x16x32_bf16 v[32:35], a[136:139], v[76:79], v[32:35]\n"
"buffer_load_dwordx4 v[44:47], v0, s[8:11], s98 offen offset:1024 sc1\n"
"v_mfma_f32_16x16x32_bf16 v[32:35], a[140:143], v[148:151], v[32:35]\n"
"s_waitcnt vmcnt(31)\n"
"v_mfma_f32_16x16x32_bf16 v[32:35], a[144:147], v[152:155], v[32:35]\n"
"v_mfma_f32_16x16x32_bf16 v[32:35], a[148:151], v[172:175], v[32:35]\n"
"ds_write_b128 v3, v[28:31] offset:9216\n"
"v_mfma_f32_16x16x32_bf16 v[32:35], a[152:155], v[176:179], v[32:35]\n"
"buffer_load_dwordx4 v[28:31], v0, s[8:11], s98 offen offset:2048 sc1\n"
"v_mfma_f32_16x16x32_bf16 v[32:35], a[156:159], v[64:67], v[32:35]\n"
"v_mfma_f32_16x16x32_bf16 v[48:51], a[188:191], v[64:67], 0\n"
"v_mfma_f32_16x16x32_bf16 v[48:51], a[184:187], v[176:179], v[48:51]\n"
"s_waitcnt lgkmcnt(0)\n"
"s_barrier\n"
"v_mfma_f32_16x16x32_bf16 v[48:51], a[180:183], v[172:175], v[48:51]\n"
"buffer_load_dwordx4 v[56:59], v0, s[8:11], s98 offen offset:3072 sc1\n"
"v_mfma_f32_16x16x32_bf16 v[48:51], a[176:179], v[152:155], v[48:51]\n"
"ds_read_b128 v[60:63], v4\n"
"ds_read_b128 v[72:75], v5\n"
"v_mfma_f32_16x16x32_bf16 v[48:51], a[172:175], v[148:151], v[48:51]\n"
"v_cmp_u_f32_e32 vcc, v32, v32\n"
"v_mfma_f32_16x16x32_bf16 v[48:51], a[168:171], v[76:79], v[48:51]\n"
"s_cbranch_vccnz .Lmk_p75s\n"
".Lmk_p75r:\n"
"v_mfma_f32_16x16x32_bf16 v[48:51], a[164:167], v[68:71], v[48:51]\n"
"ds_write_b128 v3, v[32:35] offset:12288\n"
"v_mfma_f32_16x16x32_bf16 v[48:51], a[160:163], v[36:39], v[48:51]\n"
"buffer_load_dwordx4 v[32:35], v1, s[8:11], s98 offen sc1\n"
"v_mfma_f32_16x16x32_bf16 v[104:107], a[192:195], v[36:39], 0\n"
"ds_read_b128 v[108:111], v4 offset:1024\n"
"ds_read_b128 v[112:115], v5 offset:1024\n"
"s_waitcnt lgkmcnt(3)\n"
"v_mfma_f32_16x16x32_bf16 v[104:107], a[196:199], v[68:71], v[104:107]\n"
"v_add_f32_e32 v60, v60, v255\n"
"v_add_f32_e32 v61, v61, v254\n"
"v_add_f32_e32 v62, v62, v253\n"
"v_mfma_f32_16x16x32_bf16 v[104:107], a[200:203], v[76:79], v[104:107]\n"
"buffer_load_dwordx4 v[116:119], v1, s[8:11], s98 offen offset:1024 sc1\n"
"v_add_f32_e32 v63, v63, v252\n"
"v_mfma_f32_16x16x32_bf16 v[104:107], a[204:207], v[148:151], v[104:107]\n"
"v_add_f32_e32 v60, v60, v72\n"
"v_add_f32_e32 v61, v61, v73\n"
"v_add_f32_e32 v62, v62, v74\n"
"v_mfma_f32_16x16x32_bf16 v[104:107], a[208:211], v[152:155], v[104:107]\n"
"v_add_f32_e32 v63, v63, v75\n"
"v_mfma_f32_16x16x32_bf16 v[104:107], a[212:215], v[172:175], v[104:107]\n"
"ds_write_b128 v3, v[48:51] offset:13312\n"
"v_mfma_f32_16x16x32_bf16 v[104:107], a[216:219], v[176:179], v[104:107]\n"
"buffer_load_dwordx4 v[48:51], v1, s[8:11], s98 offen offset:2048 sc1\n"
"s_waitcnt lgkmcnt(1)\n"
"v_mfma_f32_16x16x32_bf16 v[104:107], a[220:223], v[64:67], v[104:107]\n"
"ds_read_b128 v[72:75], v4 offset:2048\n"
"ds_read_b128 v[120:123], v5 offset:2048\n"
"v_add_f32_e32 v108, v108, v251\n"
"v_mfma_f32_16x16x32_bf16 v[124:127], a[252:255], v[64:67], 0\n"
"v_add_f32_e32 v109, v109, v250\n"
"v_add_f32_e32 v110, v110, v249\n"
"v_add_f32_e32 v111, v111, v248\n"
"v_mfma_f32_16x16x32_bf16 v[124:127], a[248:251], v[176:179], v[124:127]\n"
"buffer_load_dwordx4 v[64:67], v1, s[8:11], s98 offen offset:3072 sc1\n"
"v_add_f32_e32 v108, v108, v112\n"
"v_mfma_f32_16x16x32_bf16 v[124:127], a[244:247], v[172:175], v[124:127]\n"
"s_add_u32 s98, s69, 0xa000\n"
"v_add_f32_e32 v109, v109, v113\n"
"v_add_f32_e32 v110, v110, v114\n"
"v_add_f32_e32 v111, v111, v115\n"
"v_mfma_f32_16x16x32_bf16 v[124:127], a[240:243], v[152:155], v[124:127]\n"
"v_mfma_f32_16x16x32_bf16 v[124:127], a[236:239], v[148:151], v[124:127]\n"
"buffer_load_dwordx4 v[112:115], v0, s[8:11], s98 offen sc1\n"
"s_waitcnt lgkmcnt(0)\n"
"v_mfma_f32_16x16x32_bf16 v[124:127], a[232:235], v[76:79], v[124:127]\n"
"ds_write_b128 v3, v[104:107] offset:14336\n"
"v_mfma_f32_16x16x32_bf16 v[124:127], a[228:231], v[68:71], v[124:127]\n"
"ds_read_b128 v[68:71], v4 offset:3072\n"
"ds_read_b128 v[76:79], v5 offset:3072\n"
"v_add_f32_e32 v72, v72, v247\n"
"v_mfma_f32_16x16x32_bf16 v[124:127], a[224:227], v[36:39], v[124:127]\n"
"s_waitcnt vmcnt(32)\n"
"buffer_load_dwordx4 v[36:39], v0, s[8:11], s98 offen offset:1024 sc1\n"
"; @B G2 a.3\n"
"v_mfma_f32_16x16x32_bf16 v[104:107], a[128:131], v[80:83], v[60:63]\n"
"v_add_f32_e32 v73, v73, v246\n"
"v_add_f32_e32 v74, v74, v245\n"
"v_add_f32_e32 v75, v75, v244\n"
"v_mfma_f32_16x16x32_bf16 v[104:107], a[132:135], v[88:91], v[104:107]\n"
"v_add_f32_e32 v72, v72, v120\n"
"v_add_f32_e32 v73, v73, v121\n"
"v_add_f32_e32 v74, v74, v122\n"
"v_mfma_f32_16x16x32_bf16 v[104:107], a[136:139], v[40:43], v[104:107]\n"
"buffer_load_dwordx4 v[128:131], v0, s[8:11], s98 offen offset:2048 sc1\n"
"v_add_f32_e32 v75, v75, v123\n"
"v_mfma_f32_16x16x32_bf16 v[104:107], a[140:143], v[96:99], v[104:107]\n"
"s_waitcnt vmcnt(30)\n"
"s_waitcnt lgkmcnt(0)\n"
"v_add_f32_e32 v68, v68, v243\n"
"v_add_f32_e32 v69, v69, v242\n"
"v_mfma_f32_16x16x32_bf16 v[104:107], a[144:147], v[100:103], v[104:107]\n"
"v_add_f32_e32 v70, v70, v241\n"
"v_add_f32_e32 v71, v71, v240\n"
"v_add_f32_e32 v68, v68, v76\n"
"v_mfma_f32_16x16x32_bf16 v[104:107], a[148:151], v[52:55], v[104:107]\n"
"ds_write_b128 v3, v[124:127] offset:15360\n"
"v_mfma_f32_16x16x32_bf16 v[104:107], a[152:155], v[84:87], v[104:107]\n"
"buffer_load_dwordx4 v[120:123], v0, s[8:11], s98 offen offset:3072 sc1\n"
"v_add_f32_e32 v69, v69, v77\n"
"v_mfma_f32_16x16x32_bf16 v[104:107], a[156:159], v[92:95], v[104:107]\n"
"v_add_f32_e32 v70, v70, v78\n"
"v_add_f32_e32 v71, v71, v79\n"
"v_mfma_f32_16x16x32_bf16 v[76:79], a[188:191], v[92:95], v[108:111]\n"
"v_mfma_f32_16x16x32_bf16 v[76:79], a[184:187], v[84:87], v[76:79]\n"
"s_waitcnt lgkmcnt(0)\n"
"s_barrier\n"
"v_mfma_f32_16x16x32_bf16 v[76:79], a[180:183], v[52:55], v[76:79]\n"
"buffer_load_dwordx4 v[124:127], v1, s[8:11], s98 offen sc1\n"
"v_mfma_f32_16x16x32_bf16 v[76:79], a[176:179], v[100:103], v[76:79]\n"
"ds_read_b128 v[132:135], v6\n"
"ds_read_b128 v[136:139], v6 offset:1024\n"
"v_mfma_f32_16x16x32_bf16 v[76:79], a[172:175], v[96:99], v[76:79]\n"
"ds_read_b128 v[140:143], v6 offset:2048\n"
"v_cmp_u_f32_e32 vcc, v104, v104\n"
"v_mfma_f32_16x16x32_bf16 v[76:79], a[168:171], v[40:43], v[76:79]\n"
"s_cbranch_vccnz .Lmk_p76s\n"
".Lmk_p76r:\n"
"ds_read_b128 v[60:63], v6 offset:3072\n"
"v_mfma_f32_16x16x32_bf16 v[76:79], a[164:167], v[88:91], v[76:79]\n"
"buffer_load_dwordx4 v[108:111], v1, s[8:11], s98 offen offset:1024 sc1\n"
"v_mfma_f32_16x16x32_bf16 v[76:79], a[160:163], v[80:83], v[76:79]\n"
"v_mfma_f32_16x16x32_bf16 v[72:75], a[192:195], v[80:83], v[72:75]\n"
"v_mfma_f32_16x16x32_bf16 v[72:75], a[196:199], v[88:91], v[72:75]\n"
"buffer_load_dwordx4 v[144:147], v1, s[8:11], s98 offen offset:2048 sc1\n"
"v_mfma_f32_16x16x32_bf16 v[72:75], a[200:203], v[40:43], v[72:75]\n"
"v_mfma_f32_16x16x32_bf16 v[72:75], a[204:207], v[96:99], v[72:75]\n"
"v_mfma_f32_16x16x32_bf16 v[72:75], a[208:211], v[100:103], v[72:75]\n"
"buffer_load_dwordx4 v[148:151], v1, s[8:11], s98 offen offset:3072 sc1\n"
"s_waitcnt lgkmcnt(0)\n"
"v_mfma_f32_16x16x32_bf16 v[72:75], a[212:215], v[52:55], v[72:75]\n"
"s_add_u32 s98, s52, 0x840000\n"
"buffer_load_dwordx4 v[152:155], v0, s[8:11], s98 offen sc1\n"
"v_mfma_f32_16x16x32_bf16 v[72:75], a[216:219], v[84:87], v[72:75]\n"
"v_add_f32_e32 v132, v104, v132\n"
"v_add_f32_e32 v133, v105, v133\n"
"v_add_f32_e32 v134, v106, v134\n"
"v_mfma_f32_16x16x32_bf16 v[72:75], a[220:223], v[92:95], v[72:75]\n"
"buffer_load_dwordx4 v[156:159], v0, s[8:11], s98 offen offset:1024 sc1\n"
"v_add_f32_e32 v135, v107, v135\n"
"v_mfma_f32_16x16x32_bf16 v[68:71], a[252:255], v[92:95], v[68:71]\n"
"v_add_f32_e32 v136, v76, v136\n"
"v_add_f32_e32 v137, v77, v137\n"
"v_add_f32_e32 v138, v78, v138\n"
"v_mfma_f32_16x16x32_bf16 v[68:71], a[248:251], v[84:87], v[68:71]\n"
"buffer_load_dwordx4 v[84:87], v0, s[8:11], s98 offen offset:2048 sc1\n"
"v_add_f32_e32 v139, v79, v139\n"
"v_mfma_f32_16x16x32_bf16 v[68:71], a[244:247], v[52:55], v[68:71]\n"
"v_cvt_pk_bf16_f32 v52, v132, v133\n"
"v_cvt_pk_bf16_f32 v53, v134, v135\n"
"v_cvt_pk_bf16_f32 v54, v136, v137\n"
"v_mfma_f32_16x16x32_bf16 v[68:71], a[240:243], v[100:103], v[68:71]\n"
"buffer_load_dwordx4 v[76:79], v0, s[8:11], s98 offen offset:3072 sc1\n"
"v_cvt_pk_bf16_f32 v55, v138, v139\n"
"v_mfma_f32_16x16x32_bf16 v[68:71], a[236:239], v[96:99], v[68:71]\n"
"s_add_u32 s97, s61, 0x800000\n"
"buffer_store_dwordx4 v[52:55], v0, s[4:7], s97 offen sc1\n"
"v_mfma_f32_16x16x32_bf16 v[68:71], a[232:235], v[40:43], v[68:71]\n"
"buffer_load_dwordx4 v[40:43], v1, s[8:11], s98 offen sc1\n"
"v_mfma_f32_16x16x32_bf16 v[68:71], a[228:231], v[88:91], v[68:71]\n"
"ds_write_b128 v2, v[52:55]\n"
"v_mfma_f32_16x16x32_bf16 v[68:71], a[224:227], v[80:83], v[68:71]\n"
"buffer_load_dwordx4 v[80:83], v1, s[8:11], s98 offen offset:1024 sc1\n"
"buffer_load_dwordx4 v[88:91], v1, s[8:11], s98 offen offset:2048 sc1\n"
"buffer_load_dwordx4 v[92:95], v1, s[8:11], s98 offen offset:3072 sc1\n"
"s_add_u32 s98, s53, 0x840000\n"
"buffer_load_dwordx4 v[96:99], v0, s[8:11], s98 offen sc1\n"
"buffer_load_dwordx4 v[100:103], v0, s[8:11], s98 offen offset:1024 sc1\n"
"buffer_load_dwordx4 v[104:107], v0, s[8:11], s98 offen offset:2048 sc1\n"
"buffer_load_dwordx4 v[132:135], v0, s[8:11], s98 offen offset:3072 sc1\n"
"buffer_load_dwordx4 v[136:139], v1, s[8:11], s98 offen sc1\n"
"buffer_load_dwordx4 v[160:163], v1, s[8:11], s98 offen offset:1024 sc1\n"
"buffer_load_dwordx4 v[164:167], v1, s[8:11], s98 offen offset:2048 sc1\n"
"buffer_load_dwordx4 v[168:171], v1, s[8:11], s98 offen offset:3072 sc1\n"
"s_lshr_b32 s98, s79, 7\n"
"s_add_u32 s52, s52, 0x800000\n"
"s_add_u32 s53, s53, 0x800000\n"
"s_add_u32 s54, s54, 0x800000\n"
"s_add_u32 s55, s55, 0x800000\n"
"s_add_u32 s56, s56, 0x800000\n"
"s_add_u32 s57, s57, 0x800000\n"
"s_add_u32 s58, s58, 0x800000\n"
"s_add_u32 s59, s59, 0x800000\n"
"s_add_u32 s60, s60, 0x800000\n"
"s_add_u32 s61, s61, 0x800000\n"
"s_add_u32 s62, s62, 0x80000\n"
"s_add_u32 s63, s63, 0x80000\n"
"s_add_u32 s64, s64, 0x1000000\n"
"s_add_u32 s65, s65, s79\n"
"s_add_u32 s66, s66, s79\n"
"s_add_u32 s67, s67, 0x20000\n"
"s_add_u32 s68, s68, 0x20000\n"
"s_add_u32 s71, s71, s98\n"
"s_add_u32 s73, s73, s98\n"
"s_mov_b32 s0, 1\n"
"s_mov_b32 s10, 0\n"
".Lmk_a0:\n"
"s_waitcnt vmcnt(13)\n"
"; @A G1 0.0\n"
"v_mfma_f32_16x16x32_bf16 v[172:175], a[0:3], v[152:155], 0\n"
"v_mfma_f32_16x16x32_bf16 v[172:175], a[4:7], v[156:159], v[172:175]\n"
"s_add_u32 s98, s62, 0x4000\n"
"v_mfma_f32_16x16x32_bf16 v[172:175], a[8:11], v[84:87], v[172:175]\n"
"s_add_u32 s97, s54, 0x40000\n"
"buffer_load_dwordx4 v[176:179], v0, s[4:7], s97 offen sc1\n"
"v_mfma_f32_16x16x32_bf16 v[172:175], a[12:15], v[76:79], v[172:175]\n"
"s_waitcnt vmcnt(9)\n"
"v_mfma_f32_16x16x32_bf16 v[172:175], a[16:19], v[40:43], v[172:175]\n"
"buffer_load_dword v255, v10, s[4:7], s98 offen sc1\n"
"v_mfma_f32_16x16x32_bf16 v[172:175], a[20:23], v[80:83], v[172:175]\n"
"v_mov_b32_e32 v180, 0x3f803f80\n"
"v_mov_b32_e32 v181, 0x3f803f80\n"
"v_mfma_f32_16x16x32_bf16 v[172:175], a[24:27], v[88:91], v[172:175]\n"
"v_mov_b32_e32 v182, 0x3f803f80\n"
"v_mov_b32_e32 v183, 0x3f803f80\n"
"v_mfma_f32_16x16x32_bf16 v[172:175], a[28:31], v[92:95], v[172:175]\n"
"buffer_load_dwordx4 v[184:187], v0, s[4:7], s97 offen offset:1024 sc1\n"
"v_mfma_f32_16x16x32_bf16 v[188:191], a[60:63], v[92:95], 0\n"
"buffer_load_dword v254, v10, s[4:7], s98 offen offset:256 sc1\n"
"v_mfma_f32_16x16x32_bf16 v[188:191], a[56:59], v[88:91], v[188:191]\n"
"v_add_f32_e32 v140, v72, v140\n"
"v_add_f32_e32 v141, v73, v141\n"
"v_mfma_f32_16x16x32_bf16 v[188:191], a[52:55], v[80:83], v[188:191]\n"
"v_add_f32_e32 v142, v74, v142\n"
"v_add_f32_e32 v143, v75, v143\n"
"v_mfma_f32_16x16x32_bf16 v[188:191], a[48:51], v[40:43], v[188:191]\n"
"buffer_load_dword v253, v10, s[4:7], s98 offen offset:512 sc1\n"
"v_mfma_f32_16x16x32_bf16 v[188:191], a[44:47], v[76:79], v[188:191]\n"
"v_cmp_u_f32_e32 vcc, v172, v172\n"
"v_add_f32_e32 v60, v68, v60\n"
"v_mfma_f32_16x16x32_bf16 v[188:191], a[40:43], v[84:87], v[188:191]\n"
"s_cbranch_vccnz .Lmk_p81s\n"
".Lmk_p81r:\n"
"v_add_f32_e32 v61, v69, v61\n"
"v_mfma_f32_16x16x32_bf16 v[188:191], a[36:39], v[156:159], v[188:191]\n"
"buffer_load_dwordx4 v[72:75], v0, s[4:7], s97 offen offset:2048 sc1\n"
"v_mfma_f32_16x16x32_bf16 v[188:191], a[32:35], v[152:155], v[188:191]\n"
"ds_write_b128 v3, v[172:175]\n"
"v_mfma_f32_16x16x32_bf16 v[172:175], a[64:67], v[152:155], 0\n"
"buffer_load_dword v252, v10, s[4:7], s98 offen offset:768 sc1\n"
"v_mfma_f32_16x16x32_bf16 v[172:175], a[68:71], v[156:159], v[172:175]\n"
"buffer_load_dwordx4 v[192:195], v0, s[4:7], s97 offen offset:3072 sc1\n"
"v_mfma_f32_16x16x32_bf16 v[172:175], a[72:75], v[84:87], v[172:175]\n"
"v_add_f32_e32 v62, v70, v62\n"
"v_add_f32_e32 v63, v71, v63\n"
"v_mfma_f32_16x16x32_bf16 v[172:175], a[76:79], v[76:79], v[172:175]\n"
"buffer_load_dword v251, v10, s[4:7], s98 offen offset:1024 sc1\n"
"v_mfma_f32_16x16x32_bf16 v[172:175], a[80:83], v[40:43], v[172:175]\n"
"v_cvt_pk_bf16_f32 v68, v140, v141\n"
"v_cvt_pk_bf16_f32 v69, v142, v143\n"
"v_mfma_f32_16x16x32_bf16 v[172:175], a[84:87], v[80:83], v[172:175]\n"
"ds_write_b128 v3, v[188:191] offset:1024\n"
"v_mfma_f32_16x16x32_bf16 v[172:175], a[88:91], v[88:91], v[172:175]\n"
"buffer_load_dwordx4 v[140:143], v1, s[4:7], s97 offen sc1\n"
"v_mfma_f32_16x16x32_bf16 v[172:175], a[92:95], v[92:95], v[172:175]\n"
"buffer_load_dword v250, v10, s[4:7], s98 offen offset:1280 sc1\n"
"v_mfma_f32_16x16x32_bf16 v[188:191], a[124:127], v[92:95], 0\n"
"v_cvt_pk_bf16_f32 v70, v60, v61\n"
"v_cvt_pk_bf16_f32 v71, v62, v63\n"
"v_mfma_f32_16x16x32_bf16 v[188:191], a[120:123], v[88:91], v[188:191]\n"
"buffer_store_dwordx4 v[68:71], v0, s[4:7], s61 offen offset:1024 sc1\n"
"v_mfma_f32_16x16x32_bf16 v[188:191], a[116:119], v[80:83], v[188:191]\n"
"buffer_load_dword v249, v10, s[4:7], s98 offen offset:1536 sc1\n"
"v_mfma_f32_16x16x32_bf16 v[188:191], a[112:115], v[40:43], v[188:191]\n"
"buffer_load_dwordx4 v[60:63], v1, s[4:7], s97 offen offset:1024 sc1\n"
"v_mfma_f32_16x16x32_bf16 v[188:191], a[108:111], v[76:79], v[188:191]\n"
"ds_write_b128 v2, v[68:71] offset:1024\n"
"v_mfma_f32_16x16x32_bf16 v[196:199], v[180:183], v[52:55], 0\n"
"v_mfma_f32_16x16x32_bf16 v[200:203], v[52:55], v[52:55], 0\n"
"v_mfma_f32_16x16x32_bf16 v[196:199], v[180:183], v[68:71], v[196:199]\n"
"v_mfma_f32_16x16x32_bf16 v[200:203], v[68:71], v[68:71], v[200:203]\n"
"v_mfma_f32_16x16x32_bf16 v[188:191], a[104:107], v[84:87], v[188:191]\n"
"ds_write_b128 v3, v[172:175] offset:2048\n"
"v_mfma_f32_16x16x32_bf16 v[188:191], a[100:103], v[156:159], v[188:191]\n"
"s_mov_b32 s94, 0xaaaaaaaa\n"
"v_mov_b32_e32 v52, -1\n"
"v_mov_b32_e32 v53, -1\n"
"v_mfma_f32_16x16x32_bf16 v[188:191], a[96:99], v[152:155], v[188:191]\n"
"v_mov_b32_e32 v54, -1\n"
"v_mov_b32_e32 v55, -1\n"
"v_mfma_f32_16x16x32_bf16 v[68:71], v[24:27], v[152:155], 0\n"
"buffer_load_dwordx4 v[172:175], v1, s[4:7], s97 offen offset:2048 sc1\n"
"v_mfma_f32_16x16x32_bf16 v[68:71], v[44:47], v[156:159], v[68:71]\n"
"s_add_u32 s96, s64, 0x4000\n"
"buffer_store_dwordx4 v[52:55], v2, s[4:7], s96 offen sc1\n"
"v_mfma_f32_16x16x32_bf16 v[68:71], v[28:31], v[84:87], v[68:71]\n"
"buffer_load_dword v248, v10, s[4:7], s98 offen offset:1792 sc1\n"
"v_mfma_f32_16x16x32_bf16 v[68:71], v[56:59], v[76:79], v[68:71]\n"
"s_mov_b32 s95, 0xaaaaaaaa\n"
"v_mfma_f32_16x16x32_bf16 v[68:71], v[32:35], v[40:43], v[68:71]\n"
"v_mfma_f32_16x16x32_bf16 v[68:71], v[116:119], v[80:83], v[68:71]\n"
"buffer_load_dwordx4 v[180:183], v1, s[4:7], s97 offen offset:3072 sc1\n"
"v_mfma_f32_16x16x32_bf16 v[68:71], v[48:51], v[88:91], v[68:71]\n"
"ds_write_b128 v3, v[188:191] offset:3072\n"
"v_mfma_f32_16x16x32_bf16 v[68:71], v[64:67], v[92:95], v[68:71]\n"
"v_cndmask_b32_e64 v200, v200, v201, s[94:95]\n"
"v_cndmask_b32_e64 v202, v202, v203, s[94:95]\n"
"v_mfma_f32_16x16x32_bf16 v[188:191], v[148:151], v[92:95], 0\n"
"s_mov_b32 s94, 0xcccccccc\n"
"s_mov_b32 s95, 0xcccccccc\n"
"v_mfma_f32_16x16x32_bf16 v[188:191], v[144:147], v[88:91], v[188:191]\n"
"v_cndmask_b32_e64 v197, v200, v202, s[94:95]\n"
"v_mfma_f32_16x16x32_bf16 v[188:191], v[108:111], v[80:83], v[188:191]\n"
"s_mov_b32 s94, 0xf0000f\n"
"s_mov_b32 s95, 0xf0000f00\n"
"v_mfma_f32_16x16x32_bf16 v[188:191], v[124:127], v[40:43], v[188:191]\n"
"v_cndmask_b32_e64 v196, v196, v197, s[94:95]\n"
"v_mfma_f32_16x16x32_bf16 v[188:191], v[120:123], v[76:79], v[188:191]\n"
"buffer_store_dword v196, v14, s[4:7], s63 offen sc1\n"
"v_mfma_f32_16x16x32_bf16 v[188:191], v[128:131], v[84:87], v[188:191]\n"
"ds_write_b128 v3, v[68:71] offset:4096\n"
"v_mfma_f32_16x16x32_bf16 v[188:191], v[36:39], v[156:159], v[188:191]\n"
"v_mfma_f32_16x16x32_bf16 v[188:191], v[112:115], v[152:155], v[188:191]\n"
"s_waitcnt vmcnt(23)\n"
"; @A G1 0.1\n"
"v_mfma_f32_16x16x32_bf16 v[40:43], a[0:3], v[96:99], 0\n"
"v_mfma_f32_16x16x32_bf16 v[40:43], a[4:7], v[100:103], v[40:43]\n"
"v_mfma_f32_16x16x32_bf16 v[40:43], a[8:11], v[104:107], v[40:43]\n"
"s_add_u32 s98, s55, 0x40000\n"
"buffer_load_dwordx4 v[68:71], v0, s[4:7], s98 offen sc1\n"
"v_mfma_f32_16x16x32_bf16 v[40:43], a[12:15], v[132:135], v[40:43]\n"
"s_waitcnt vmcnt(20)\n"
"v_mfma_f32_16x16x32_bf16 v[40:43], a[16:19], v[136:139], v[40:43]\n"
"v_mfma_f32_16x16x32_bf16 v[40:43], a[20:23], v[160:163], v[40:43]\n"
"ds_write_b128 v3, v[188:191] offset:5120\n"
"v_mfma_f32_16x16x32_bf16 v[40:43], a[24:27], v[164:167], v[40:43]\n"
"v_mfma_f32_16x16x32_bf16 v[40:43], a[28:31], v[168:171], v[40:43]\n"
"buffer_load_dwordx4 v[76:79], v0, s[4:7], s98 offen offset:1024 sc1\n"
"v_mfma_f32_16x16x32_bf16 v[80:83], a[60:63], v[168:171], 0\n"
"v_mfma_f32_16x16x32_bf16 v[80:83], a[56:59], v[164:167], v[80:83]\n"
"v_mfma_f32_16x16x32_bf16 v[80:83], a[52:55], v[160:163], v[80:83]\n"
"v_mfma_f32_16x16x32_bf16 v[80:83], a[48:51], v[136:139], v[80:83]\n"
"v_mfma_f32_16x16x32_bf16 v[80:83], a[44:47], v[132:135], v[80:83]\n"
"s_nop 1\n"
"v_cmp_u_f32_e32 vcc, v40, v40\n"
"v_mfma_f32_16x16x32_bf16 v[80:83], a[40:43], v[104:107], v[80:83]\n"
"s_cbranch_vccnz .Lmk_p82s\n"
".Lmk_p82r:\n"
"v_mfma_f32_16x16x32_bf16 v[80:83], a[36:39], v[100:103], v[80:83]\n"
"buffer_load_dwordx4 v[84:87], v0, s[4:7], s98 offen offset:2048 sc1\n"
"v_mfma_f32_16x16x32_bf16 v[80:83], a[32:35], v[96:99], v[80:83]\n"
"ds_write_b128 v3, v[40:43] offset:6144\n"
"v_mfma_f32_16x16x32_bf16 v[40:43], a[64:67], v[96:99], 0\n"
"v_mfma_f32_16x16x32_bf16 v[40:43], a[68:71], v[100:103], v[40:43]\n"
"buffer_load_dwordx4 v[88:91], v0, s[4:7], s98 offen offset:3072 sc1\n"
"v_mfma_f32_16x16x32_bf16 v[40:43], a[72:75], v[104:107], v[40:43]\n"
"v_mfma_f32_16x16x32_bf16 v[40:43], a[76:79], v[132:135], v[40:43]\n"
"v_mfma_f32_16x16x32_bf16 v[40:43], a[80:83], v[136:139], v[40:43]\n"
"v_mfma_f32_16x16x32_bf16 v[40:43], a[84:87], v[160:163], v[40:43]\n"
"ds_write_b128 v3, v[80:83] offset:7168\n"
"v_mfma_f32_16x16x32_bf16 v[40:43], a[88:91], v[164:167], v[40:43]\n"
"buffer_load_dwordx4 v[80:83], v1, s[4:7], s98 offen sc1\n"
"v_mfma_f32_16x16x32_bf16 v[40:43], a[92:95], v[168:171], v[40:43]\n"
"v_mfma_f32_16x16x32_bf16 v[92:95], a[124:127], v[168:171], 0\n"
"v_mfma_f32_16x16x32_bf16 v[92:95], a[120:123], v[164:167], v[92:95]\n"
"v_mfma_f32_16x16x32_bf16 v[92:95], a[116:119], v[160:163], v[92:95]\n"
"v_mfma_f32_16x16x32_bf16 v[92:95], a[112:115], v[136:139], v[92:95]\n"
"buffer_load_dwordx4 v[152:155], v1, s[4:7], s98 offen offset:1024 sc1\n"
"v_mfma_f32_16x16x32_bf16 v[92:95], a[108:111], v[132:135], v[92:95]\n"
"v_mfma_f32_16x16x32_bf16 v[92:95], a[104:107], v[104:107], v[92:95]\n"
"s_nop 0\n"
"ds_write_b128 v3, v[40:43] offset:8192\n"
"v_mfma_f32_16x16x32_bf16 v[92:95], a[100:103], v[100:103], v[92:95]\n"
"v_mfma_f32_16x16x32_bf16 v[92:95], a[96:99], v[96:99], v[92:95]\n"
"v_mfma_f32_16x16x32_bf16 v[40:43], v[24:27], v[96:99], 0\n"
"buffer_load_dwordx4 v[156:159], v1, s[4:7], s98 offen offset:2048 sc1\n"
"v_mfma_f32_16x16x32_bf16 v[40:43], v[44:47], v[100:103], v[40:43]\n"
"s_add_u32 s97, s64, 0x4000\n"
"v_mfma_f32_16x16x32_bf16 v[40:43], v[28:31], v[104:107], v[40:43]\n"
"v_mfma_f32_16x16x32_bf16 v[40:43], v[56:59], v[132:135], v[40:43]\n"
"buffer_store_dwordx4 v[52:55], v2, s[4:7], s97 offen offset:1024 sc1\n"
"v_mfma_f32_16x16x32_bf16 v[40:43], v[32:35], v[136:139], v[40:43]\n"
"s_waitcnt vmcnt(10)\n"
"v_add_f32_e32 v255, v255, v251\n"
"v_add_f32_e32 v254, v254, v250\n"
"v_mfma_f32_16x16x32_bf16 v[40:43], v[116:119], v[160:163], v[40:43]\n"
"buffer_load_dwordx4 v[188:191], v1, s[4:7], s98 offen offset:3072 sc1\n"
"v_mfma_f32_16x16x32_bf16 v[40:43], v[48:51], v[164:167], v[40:43]\n"
"ds_write_b128 v3, v[92:95] offset:9216\n"
"v_mfma_f32_16x16x32_bf16 v[40:43], v[64:67], v[168:171], v[40:43]\n"
"v_add_f32_e32 v253, v253, v249\n"
"v_add_f32_e32 v252, v252, v248\n"
"v_mfma_f32_16x16x32_bf16 v[92:95], v[148:151], v[168:171], 0\n"
"v_add_f32_e32 v255, v255, v253\n"
"v_add_f32_e32 v254, v254, v252\n"
"v_mfma_f32_16x16x32_bf16 v[92:95], v[144:147], v[164:167], v[92:95]\n"
"v_add_f32_e32 v255, v255, v254\n"
"v_cmp_u_f32_e32 vcc, v255, v255\n"
"v_mfma_f32_16x16x32_bf16 v[92:95], v[108:111], v[160:163], v[92:95]\n"
"s_cbranch_vccnz .Lmk_m85s\n"
".Lmk_m85r:\n"
"v_mov_b32_e32 v254, v255\n"
"v_mfma_f32_16x16x32_bf16 v[92:95], v[124:127], v[136:139], v[92:95]\n"
"s_nop 0\n"
"v_permlane32_swap_b32_e32 v255, v254\n"
"v_add_f32_e32 v255, v255, v254\n"
"v_mfma_f32_16x16x32_bf16 v[92:95], v[120:123], v[132:135], v[92:95]\n"
"v_mul_f32_e32 v255, 0x3a800000, v255\n"
"v_mov_b32_e32 v254, v255\n"
"v_mfma_f32_16x16x32_bf16 v[92:95], v[128:131], v[104:107], v[92:95]\n"
"ds_write_b128 v3, v[40:43] offset:10240\n"
"v_mfma_f32_16x16x32_bf16 v[92:95], v[36:39], v[100:103], v[92:95]\n"
"v_permlane16_swap_b32_e32 v255, v254\n"
"v_cvt_pk_bf16_f32 v253, -v255, 0\n"
"v_mfma_f32_16x16x32_bf16 v[92:95], v[112:115], v[96:99], v[92:95]\n"
"v_fma_f32 v254, -v255, v255, v254\n"
"v_lshlrev_b32_e32 v253, 16, v253\n"
"; @A G1 0.2\n"
"v_mfma_f32_16x16x32_bf16 v[40:43], a[0:3], v[176:179], 0\n"
"v_max_f32_e32 v254, 0, v254\n"
"v_cvt_pk_bf16_f32 v254, v254, 0\n"
"v_mfma_f32_16x16x32_bf16 v[40:43], a[4:7], v[184:187], v[40:43]\n"
"v_lshlrev_b32_e32 v254, 16, v254\n"
"v_add_f32_e32 v254, 0x3727c5ac, v254\n"
"v_mfma_f32_16x16x32_bf16 v[40:43], a[8:11], v[72:75], v[40:43]\n"
"v_cvt_pk_bf16_f32 v254, v254, 0\n"
"v_lshlrev_b32_e32 v254, 16, v254\n"
"v_mfma_f32_16x16x32_bf16 v[40:43], a[12:15], v[192:195], v[40:43]\n"
"s_waitcnt vmcnt(10)\n"
"v_sqrt_f32_e32 v254, v254\n"
"v_mfma_f32_16x16x32_bf16 v[40:43], a[16:19], v[140:143], v[40:43]\n"
"s_mov_b32 s98, .Lmk_a2-.Lmk_g80\n"
"s_getpc_b64 s[84:85]\n"
".Lmk_g80:\n"
"s_add_u32 s84, s84, s98\n"
"s_addc_u32 s85, s85, 0\n"
"s_and_b32 s84, s84, -16\n"
"v_mfma_f32_16x16x32_bf16 v[40:43], a[20:23], v[60:63], v[40:43]\n"
"ds_write_b128 v3, v[92:95] offset:11264\n"
"v_mfma_f32_16x16x32_bf16 v[40:43], a[24:27], v[172:175], v[40:43]\n"
"s_and_b32 s85, s85, 0xffff\n"
"s_sub_u32 s98, .Lmk_end-.Lmk_g80, s98\n"
"s_min_u32 s86, s98, 0x3000\n"
"s_mov_b32 s87, 0x20000\n"
"v_mfma_f32_16x16x32_bf16 v[40:43], a[28:31], v[180:183], v[40:43]\n"
"s_mov_b32 m0, 0x27800\n"
"s_nop 0\n"
"buffer_load_dwordx4 v0, s[84:87], s99 offen lds\n"
"v_mfma_f32_16x16x32_bf16 v[92:95], a[60:63], v[180:183], 0\n"
"v_cvt_pk_bf16_f32 v254, v254, 0\n"
"v_lshlrev_b32_e32 v254, 16, v254\n"
"v_mfma_f32_16x16x32_bf16 v[92:95], a[56:59], v[172:175], v[92:95]\n"
"v_rcp_f32_e32 v255, v254\n"
"v_mfma_f32_16x16x32_bf16 v[92:95], a[52:55], v[60:63], v[92:95]\n"
"s_add_u32 s98, s52, 0x48000\n"
"buffer_load_dwordx4 v[96:99], v0, s[4:7], s98 offen sc1\n"
"v_mfma_f32_16x16x32_bf16 v[92:95], a[48:51], v[140:143], v[92:95]\n"
"s_waitcnt lgkmcnt(0)\n"
"s_barrier\n"
"v_mfma_f32_16x16x32_bf16 v[92:95], a[44:47], v[192:195], v[92:95]\n"
"v_cmp_u_f32_e32 vcc, v40, v40\n"
"v_mul_f32_e32 v254, 0x3e8293ee, v255\n"
"v_mfma_f32_16x16x32_bf16 v[92:95], a[40:43], v[72:75], v[92:95]\n"
"s_cbranch_vccnz .Lmk_p83s\n"
".Lmk_p83r:\n"
"ds_read_b128 v[100:103], v4\n"
"v_mfma_f32_16x16x32_bf16 v[92:95], a[36:39], v[184:187], v[92:95]\n"
"ds_write_b128 v3, v[40:43] offset:12288\n"
"v_mfma_f32_16x16x32_bf16 v[92:95], a[32:35], v[176:179], v[92:95]\n"
"buffer_load_dwordx4 v[40:43], v0, s[4:7], s98 offen offset:1024 sc1\n"
"v_mfma_f32_16x16x32_bf16 v[104:107], a[64:67], v[176:179], 0\n"
"ds_read_b128 v[132:135], v9 offset:128\n"
"ds_read_b128 v[136:139], v5\n"
"v_mfma_f32_16x16x32_bf16 v[104:107], a[68:71], v[184:187], v[104:107]\n"
"v_mfma_f32_16x16x32_bf16 v[104:107], a[72:75], v[72:75], v[104:107]\n"
"v_mfma_f32_16x16x32_bf16 v[104:107], a[76:79], v[192:195], v[104:107]\n"
"ds_read_b128 v[160:163], v4 offset:1024\n"
"ds_read_b128 v[164:167], v9 offset:192\n"
"v_mfma_f32_16x16x32_bf16 v[104:107], a[80:83], v[140:143], v[104:107]\n"
"buffer_load_dwordx4 v[168:171], v0, s[4:7], s98 offen offset:2048 sc1\n"
"v_mfma_f32_16x16x32_bf16 v[104:107], a[84:87], v[60:63], v[104:107]\n"
"ds_write_b128 v3, v[92:95] offset:13312\n"
"v_mfma_f32_16x16x32_bf16 v[104:107], a[88:91], v[172:175], v[104:107]\n"
"ds_read_b128 v[92:95], v5 offset:1024\n"
"s_waitcnt lgkmcnt(4)\n"
"v_mfma_f32_16x16x32_bf16 v[104:107], a[92:95], v[180:183], v[104:107]\n"
"v_fmac_f32_e32 v100, v253, v132\n"
"v_fmac_f32_e32 v101, v253, v133\n"
"v_mfma_f32_16x16x32_bf16 v[196:199], a[124:127], v[180:183], 0\n"
"v_fmac_f32_e32 v102, v253, v134\n"
"v_fmac_f32_e32 v103, v253, v135\n"
"v_mfma_f32_16x16x32_bf16 v[196:199], a[120:123], v[172:175], v[196:199]\n"
"buffer_load_dwordx4 v[132:135], v0, s[4:7], s98 offen offset:3072 sc1\n"
"v_mfma_f32_16x16x32_bf16 v[196:199], a[116:119], v[60:63], v[196:199]\n"
"ds_read_b128 v[200:203], v4 offset:2048\n"
"ds_read_b128 v[204:207], v9 offset:256\n"
"v_mfma_f32_16x16x32_bf16 v[196:199], a[112:115], v[140:143], v[196:199]\n"
"ds_read_b128 v[208:211], v5 offset:2048\n"
"v_add_f32_e32 v100, v100, v136\n"
"v_mfma_f32_16x16x32_bf16 v[196:199], a[108:111], v[192:195], v[196:199]\n"
"v_add_f32_e32 v101, v101, v137\n"
"v_add_f32_e32 v102, v102, v138\n"
"v_mfma_f32_16x16x32_bf16 v[196:199], a[104:107], v[72:75], v[196:199]\n"
"ds_write_b128 v3, v[104:107] offset:14336\n"
"v_mfma_f32_16x16x32_bf16 v[196:199], a[100:103], v[184:187], v[196:199]\n"
"buffer_load_dwordx4 v[104:107], v1, s[4:7], s98 offen sc1\n"
"v_mfma_f32_16x16x32_bf16 v[196:199], a[96:99], v[176:179], v[196:199]\n"
"s_waitcnt lgkmcnt(4)\n"
"v_add_f32_e32 v103, v103, v139\n"
"v_fmac_f32_e32 v160, v253, v164\n"
"v_mfma_f32_16x16x32_bf16 v[136:139], v[24:27], v[176:179], 0\n"
"v_fmac_f32_e32 v161, v253, v165\n"
"v_fmac_f32_e32 v162, v253, v166\n"
"v_mfma_f32_16x16x32_bf16 v[136:139], v[44:47], v[184:187], v[136:139]\n"
"ds_read_b128 v[212:215], v4 offset:3072\n"
"ds_read_b128 v[216:219], v9 offset:320\n"
"v_mfma_f32_16x16x32_bf16 v[136:139], v[28:31], v[72:75], v[136:139]\n"
"ds_read_b128 v[220:223], v5 offset:3072\n"
"v_fmac_f32_e32 v163, v253, v167\n"
"v_mfma_f32_16x16x32_bf16 v[136:139], v[56:59], v[192:195], v[136:139]\n"
"buffer_load_dwordx4 v[164:167], v1, s[4:7], s98 offen offset:1024 sc1\n"
"v_mfma_f32_16x16x32_bf16 v[136:139], v[32:35], v[140:143], v[136:139]\n"
"s_add_u32 s97, s64, 0x8000\n"
"v_add_f32_e32 v160, v160, v92\n"
"v_add_f32_e32 v161, v161, v93\n"
"v_mfma_f32_16x16x32_bf16 v[136:139], v[116:119], v[60:63], v[136:139]\n"
"ds_write_b128 v3, v[196:199] offset:15360\n"
"v_mfma_f32_16x16x32_bf16 v[136:139], v[48:51], v[172:175], v[136:139]\n"
"buffer_store_dwordx4 v[52:55], v2, s[4:7], s97 offen sc1\n"
"v_mfma_f32_16x16x32_bf16 v[136:139], v[64:67], v[180:183], v[136:139]\n"
"s_waitcnt lgkmcnt(5)\n"
"v_add_f32_e32 v162, v162, v94\n"
"v_add_f32_e32 v163, v163, v95\n"
"v_mfma_f32_16x16x32_bf16 v[92:95], v[148:151], v[180:183], 0\n"
"buffer_load_dwordx4 v[180:183], v1, s[4:7], s98 offen offset:2048 sc1\n"
"v_mfma_f32_16x16x32_bf16 v[92:95], v[144:147], v[172:175], v[92:95]\n"
"s_add_u32 s97, s68, s80\n"
"s_waitcnt vmcnt(18)\n"
"ds_read_b128 v[172:175], v4 offset:4096\n"
"v_mfma_f32_16x16x32_bf16 v[92:95], v[108:111], v[60:63], v[92:95]\n"
"buffer_store_dwordx2 v[18:19], v11, s[4:7], s97 offen sc1\n"
"v_mfma_f32_16x16x32_bf16 v[92:95], v[124:127], v[140:143], v[92:95]\n"
"ds_read_b128 v[60:63], v9\n"
"ds_read_b128 v[140:143], v5 offset:4096\n"
"v_mfma_f32_16x16x32_bf16 v[92:95], v[120:123], v[192:195], v[92:95]\n"
"v_fmac_f32_e32 v200, v253, v204\n"
"v_fmac_f32_e32 v201, v253, v205\n"
"v_mfma_f32_16x16x32_bf16 v[92:95], v[128:131], v[72:75], v[92:95]\n"
"buffer_load_dwordx4 v[72:75], v1, s[4:7], s98 offen offset:3072 sc1\n"
"v_mfma_f32_16x16x32_bf16 v[92:95], v[36:39], v[184:187], v[92:95]\n"
"ds_write_b128 v3, v[136:139] offset:16384\n"
"v_mfma_f32_16x16x32_bf16 v[92:95], v[112:115], v[176:179], v[92:95]\n"
"s_waitcnt vmcnt(16)\n"
"ds_read_b128 v[136:139], v4 offset:5120\n"
"ds_read_b128 v[176:179], v9 offset:64\n"
"; @A G1 0.3\n"
"v_mfma_f32_16x16x32_bf16 v[184:187], a[0:3], v[68:71], v[100:103]\n"
"ds_read_b128 v[192:195], v5 offset:5120\n"
"v_fmac_f32_e32 v202, v253, v206\n"
"v_mfma_f32_16x16x32_bf16 v[184:187], a[4:7], v[76:79], v[184:187]\n"
"v_fmac_f32_e32 v203, v253, v207\n"
"v_add_f32_e32 v200, v200, v208\n"
"v_mfma_f32_16x16x32_bf16 v[184:187], a[8:11], v[84:87], v[184:187]\n"
"s_add_u32 s98, s53, 0x48000\n"
"buffer_load_dwordx4 v[196:199], v0, s[4:7], s98 offen sc1\n"
"v_mfma_f32_16x16x32_bf16 v[184:187], a[12:15], v[88:91], v[184:187]\n"
"s_waitcnt vmcnt(12)\n"
"v_add_f32_e32 v201, v201, v209\n"
"v_add_f32_e32 v202, v202, v210\n"
"v_mfma_f32_16x16x32_bf16 v[184:187], a[16:19], v[80:83], v[184:187]\n"
"s_waitcnt lgkmcnt(8)\n"
"v_add_f32_e32 v203, v203, v211\n"
"v_fmac_f32_e32 v212, v253, v216\n"
"v_mfma_f32_16x16x32_bf16 v[184:187], a[20:23], v[152:155], v[184:187]\n"
"ds_write_b128 v3, v[92:95] offset:17408\n"
"v_mfma_f32_16x16x32_bf16 v[184:187], a[24:27], v[156:159], v[184:187]\n"
"v_fmac_f32_e32 v213, v253, v217\n"
"v_fmac_f32_e32 v214, v253, v218\n"
"v_mfma_f32_16x16x32_bf16 v[184:187], a[28:31], v[188:191], v[184:187]\n"
"buffer_load_dwordx4 v[92:95], v0, s[4:7], s98 offen offset:1024 sc1\n"
"v_mfma_f32_16x16x32_bf16 v[204:207], a[60:63], v[188:191], v[160:163]\n"
"v_fmac_f32_e32 v215, v253, v219\n"
"v_add_f32_e32 v212, v212, v220\n"
"v_mfma_f32_16x16x32_bf16 v[204:207], a[56:59], v[156:159], v[204:207]\n"
"v_add_f32_e32 v213, v213, v221\n"
"v_add_f32_e32 v214, v214, v222\n"
"v_mfma_f32_16x16x32_bf16 v[204:207], a[52:55], v[152:155], v[204:207]\n"
"s_waitcnt lgkmcnt(0)\n"
"s_barrier\n"
"v_mfma_f32_16x16x32_bf16 v[204:207], a[48:51], v[80:83], v[204:207]\n"
"ds_read_b128 v[208:211], v6\n"
"ds_read_b128 v[216:219], v6 offset:1024\n"
"v_mfma_f32_16x16x32_bf16 v[204:207], a[44:47], v[88:91], v[204:207]\n"
"v_cmp_u_f32_e32 vcc, v184, v184\n"
"v_add_f32_e32 v215, v215, v223\n"
"v_mfma_f32_16x16x32_bf16 v[204:207], a[40:43], v[84:87], v[204:207]\n"
"s_cbranch_vccnz .Lmk_p84s\n"
".Lmk_p84r:\n"
"v_fmac_f32_e32 v172, v253, v60\n"
"v_mfma_f32_16x16x32_bf16 v[204:207], a[36:39], v[76:79], v[204:207]\n"
"buffer_load_dwordx4 v[100:103], v0, s[4:7], s98 offen offset:2048 sc1\n"
"v_mfma_f32_16x16x32_bf16 v[204:207], a[32:35], v[68:71], v[204:207]\n"
"v_fmac_f32_e32 v173, v253, v61\n"
"v_fmac_f32_e32 v174, v253, v62\n"
"v_mfma_f32_16x16x32_bf16 v[200:203], a[64:67], v[68:71], v[200:203]\n"
"v_fmac_f32_e32 v175, v253, v63\n"
"v_add_f32_e32 v172, v172, v140\n"
"v_mfma_f32_16x16x32_bf16 v[200:203], a[68:71], v[76:79], v[200:203]\n"
"buffer_load_dwordx4 v[60:63], v0, s[4:7], s98 offen offset:3072 sc1\n"
"v_mfma_f32_16x16x32_bf16 v[200:203], a[72:75], v[84:87], v[200:203]\n"
"v_add_f32_e32 v173, v173, v141\n"
"v_add_f32_e32 v174, v174, v142\n"
"v_mfma_f32_16x16x32_bf16 v[200:203], a[76:79], v[88:91], v[200:203]\n"
"v_add_f32_e32 v175, v175, v143\n"
"v_fmac_f32_e32 v136, v253, v176\n"
"v_mfma_f32_16x16x32_bf16 v[200:203], a[80:83], v[80:83], v[200:203]\n"
"s_waitcnt lgkmcnt(0)\n"
"v_fmac_f32_e32 v137, v253, v177\n"
"v_fmac_f32_e32 v138, v253, v178\n"
"v_mfma_f32_16x16x32_bf16 v[200:203], a[84:87], v[152:155], v[200:203]\n"
"v_fmac_f32_e32 v139, v253, v179\n"
"v_add_f32_e32 v136, v136, v192\n"
"v_mfma_f32_16x16x32_bf16 v[200:203], a[88:91], v[156:159], v[200:203]\n"
"buffer_load_dwordx4 v[140:143], v1, s[4:7], s98 offen sc1\n"
"v_mfma_f32_16x16x32_bf16 v[200:203], a[92:95], v[188:191], v[200:203]\n"
"ds_read_b128 v[160:163], v6 offset:2048\n"
"ds_read_b128 v[176:179], v6 offset:3072\n"
"v_mfma_f32_16x16x32_bf16 v[212:215], a[124:127], v[188:191], v[212:215]\n"
"v_add_f32_e32 v137, v137, v193\n"
"v_add_f32_e32 v138, v138, v194\n"
"v_mfma_f32_16x16x32_bf16 v[212:215], a[120:123], v[156:159], v[212:215]\n"
"v_add_f32_e32 v139, v139, v195\n"
"v_add_f32_e32 v184, v184, v208\n"
"v_mfma_f32_16x16x32_bf16 v[212:215], a[116:119], v[152:155], v[212:215]\n"
"v_add_f32_e32 v185, v185, v209\n"
"v_add_f32_e32 v186, v186, v210\n"
"v_mfma_f32_16x16x32_bf16 v[212:215], a[112:115], v[80:83], v[212:215]\n"
"buffer_load_dwordx4 v[192:195], v1, s[4:7], s98 offen offset:1024 sc1\n"
"v_mfma_f32_16x16x32_bf16 v[212:215], a[108:111], v[88:91], v[212:215]\n"
"v_add_f32_e32 v187, v187, v211\n"
"v_add_f32_e32 v204, v204, v216\n"
"v_mfma_f32_16x16x32_bf16 v[212:215], a[104:107], v[84:87], v[212:215]\n"
"v_add_f32_e32 v205, v205, v217\n"
"v_add_f32_e32 v206, v206, v218\n"
"v_mfma_f32_16x16x32_bf16 v[212:215], a[100:103], v[76:79], v[212:215]\n"
"v_add_f32_e32 v207, v207, v219\n"
"v_mul_f32_e32 v184, v184, v255\n"
"v_mfma_f32_16x16x32_bf16 v[212:215], a[96:99], v[68:71], v[212:215]\n"
"v_mul_f32_e32 v185, v185, v255\n"
"v_mul_f32_e32 v186, v186, v255\n"
"v_mfma_f32_16x16x32_bf16 v[172:175], v[24:27], v[68:71], v[172:175]\n"
"buffer_load_dwordx4 v[208:211], v1, s[4:7], s98 offen offset:2048 sc1\n"
"v_mfma_f32_16x16x32_bf16 v[172:175], v[44:47], v[76:79], v[172:175]\n"
"v_mul_f32_e32 v187, v187, v255\n"
"v_mul_f32_e32 v204, v204, v255\n"
"v_mfma_f32_16x16x32_bf16 v[172:175], v[28:31], v[84:87], v[172:175]\n"
"v_mul_f32_e32 v205, v205, v255\n"
"v_mul_f32_e32 v206, v206, v255\n"
"v_mfma_f32_16x16x32_bf16 v[172:175], v[56:59], v[88:91], v[172:175]\n"
"v_mul_f32_e32 v207, v207, v255\n"
"v_cvt_pk_bf16_f32 v216, v184, v185\n"
"v_mfma_f32_16x16x32_bf16 v[172:175], v[32:35], v[80:83], v[172:175]\n"
"s_waitcnt lgkmcnt(0)\n"
"v_cvt_pk_bf16_f32 v217, v186, v187\n"
"v_cvt_pk_bf16_f32 v218, v204, v205\n"
"v_mfma_f32_16x16x32_bf16 v[172:175], v[116:119], v[152:155], v[172:175]\n"
"buffer_load_dwordx4 v[184:187], v1, s[4:7], s98 offen offset:3072 sc1\n"
"v_mfma_f32_16x16x32_bf16 v[172:175], v[48:51], v[156:159], v[172:175]\n"
"s_add_u32 s98, s64, 0x8000\n"
"ds_read_b128 v[220:223], v6 offset:4096\n"
"ds_read_b128 v[224:227], v6 offset:5120\n"
"v_mfma_f32_16x16x32_bf16 v[172:175], v[64:67], v[188:191], v[172:175]\n"
"v_cvt_pk_bf16_f32 v219, v206, v207\n"
"v_add_f32_e32 v200, v200, v160\n"
"v_mfma_f32_16x16x32_bf16 v[136:139], v[148:151], v[188:191], v[136:139]\n"
"buffer_store_dwordx4 v[52:55], v2, s[4:7], s98 offen offset:1024 sc1\n"
"v_mfma_f32_16x16x32_bf16 v[136:139], v[144:147], v[156:159], v[136:139]\n"
"ds_write_b128 v7, v[216:219] offset:24576\n"
"v_mfma_f32_16x16x32_bf16 v[136:139], v[108:111], v[152:155], v[136:139]\n"
"v_add_f32_e32 v201, v201, v161\n"
"v_add_f32_e32 v202, v202, v162\n"
"v_mfma_f32_16x16x32_bf16 v[136:139], v[124:127], v[80:83], v[136:139]\n"
"v_add_f32_e32 v203, v203, v163\n"
"v_add_f32_e32 v212, v212, v176\n"
"v_mfma_f32_16x16x32_bf16 v[136:139], v[120:123], v[88:91], v[136:139]\n"
"v_add_f32_e32 v213, v213, v177\n"
"v_add_f32_e32 v214, v214, v178\n"
"v_mfma_f32_16x16x32_bf16 v[136:139], v[128:131], v[84:87], v[136:139]\n"
"v_add_f32_e32 v215, v215, v179\n"
"v_mul_f32_e32 v200, v200, v255\n"
"v_mfma_f32_16x16x32_bf16 v[136:139], v[36:39], v[76:79], v[136:139]\n"
"v_mul_f32_e32 v201, v201, v255\n"
"v_mul_f32_e32 v202, v202, v255\n"
"v_mfma_f32_16x16x32_bf16 v[136:139], v[112:115], v[68:71], v[136:139]\n"
"v_mul_f32_e32 v203, v203, v255\n"
"v_mul_f32_e32 v212, v212, v255\n"
"v_mul_f32_e32 v213, v213, v255\n"
"v_mul_f32_e32 v214, v214, v255\n"
"v_mul_f32_e32 v215, v215, v255\n"
"v_cvt_pk_bf16_f32 v52, v200, v201\n"
"v_cvt_pk_bf16_f32 v53, v202, v203\n"
"v_cvt_pk_bf16_f32 v54, v212, v213\n"
"v_cvt_pk_bf16_f32 v55, v214, v215\n"
"ds_write_b128 v7, v[52:55] offset:57344\n"
".Lmk_a1:\n"
"s_waitcnt vmcnt(15)\n"
"; @A G1 1.0\n"
"v_mfma_f32_16x16x32_bf16 v[68:71], a[0:3], v[96:99], 0\n"
"v_mfma_f32_16x16x32_bf16 v[68:71], a[4:7], v[40:43], v[68:71]\n"
"s_add_u32 s98, s62, 0x4800\n"
"v_mfma_f32_16x16x32_bf16 v[68:71], a[8:11], v[168:171], v[68:71]\n"
"s_add_u32 s97, s54, 0x48000\n"
"buffer_load_dwordx4 v[76:79], v0, s[4:7], s97 offen sc1\n"
"v_mfma_f32_16x16x32_bf16 v[68:71], a[12:15], v[132:135], v[68:71]\n"
"s_waitcnt vmcnt(10)\n"
"v_mfma_f32_16x16x32_bf16 v[68:71], a[16:19], v[104:107], v[68:71]\n"
"buffer_load_dword v255, v10, s[4:7], s98 offen sc1\n"
"v_mfma_f32_16x16x32_bf16 v[68:71], a[20:23], v[164:167], v[68:71]\n"
"s_waitcnt lgkmcnt(2)\n"
"v_add_f32_e32 v172, v172, v220\n"
"v_add_f32_e32 v173, v173, v221\n"
"v_mfma_f32_16x16x32_bf16 v[68:71], a[24:27], v[180:183], v[68:71]\n"
"v_add_f32_e32 v174, v174, v222\n"
"v_add_f32_e32 v175, v175, v223\n"
"v_mfma_f32_16x16x32_bf16 v[68:71], a[28:31], v[72:75], v[68:71]\n"
"buffer_load_dwordx4 v[80:83], v0, s[4:7], s97 offen offset:1024 sc1\n"
"v_mfma_f32_16x16x32_bf16 v[84:87], a[60:63], v[72:75], 0\n"
"s_add_u32 s96, s70, 0\n"
"buffer_load_dwordx4 a[128:131], v0, s[4:7], s96 offen sc1\n"
"v_mfma_f32_16x16x32_bf16 v[84:87], a[56:59], v[180:183], v[84:87]\n"
"buffer_load_dword v253, v10, s[4:7], s98 offen offset:256 sc1\n"
"v_mfma_f32_16x16x32_bf16 v[84:87], a[52:55], v[164:167], v[84:87]\n"
"v_add_f32_e32 v136, v136, v224\n"
"v_add_f32_e32 v137, v137, v225\n"
"v_mfma_f32_16x16x32_bf16 v[84:87], a[48:51], v[104:107], v[84:87]\n"
"v_add_f32_e32 v138, v138, v226\n"
"v_add_f32_e32 v139, v139, v227\n"
"v_mfma_f32_16x16x32_bf16 v[84:87], a[44:47], v[132:135], v[84:87]\n"
"v_cmp_u_f32_e32 vcc, v68, v68\n"
"v_mul_f32_e32 v172, v172, v254\n"
"v_mfma_f32_16x16x32_bf16 v[84:87], a[40:43], v[168:171], v[84:87]\n"
"s_cbranch_vccnz .Lmk_p86s\n"
".Lmk_p86r:\n"
"v_mul_f32_e32 v173, v173, v254\n"
"v_mfma_f32_16x16x32_bf16 v[84:87], a[36:39], v[40:43], v[84:87]\n"
"buffer_load_dwordx4 v[88:91], v0, s[4:7], s97 offen offset:2048 sc1\n"
"v_mfma_f32_16x16x32_bf16 v[84:87], a[32:35], v[96:99], v[84:87]\n"
"ds_write_b128 v3, v[68:71]\n"
"v_mfma_f32_16x16x32_bf16 v[68:71], a[64:67], v[96:99], 0\n"
"buffer_load_dword v252, v10, s[4:7], s98 offen offset:512 sc1\n"
"v_mfma_f32_16x16x32_bf16 v[68:71], a[68:71], v[40:43], v[68:71]\n"
"buffer_load_dwordx4 v[152:155], v0, s[4:7], s97 offen offset:3072 sc1\n"
"v_mfma_f32_16x16x32_bf16 v[68:71], a[72:75], v[168:171], v[68:71]\n"
"v_mul_f32_e32 v174, v174, v254\n"
"v_mul_f32_e32 v175, v175, v254\n"
"v_mfma_f32_16x16x32_bf16 v[68:71], a[76:79], v[132:135], v[68:71]\n"
"buffer_load_dword v251, v10, s[4:7], s98 offen offset:768 sc1\n"
"v_mfma_f32_16x16x32_bf16 v[68:71], a[80:83], v[104:107], v[68:71]\n"
"v_mul_f32_e32 v136, v136, v254\n"
"v_mul_f32_e32 v137, v137, v254\n"
"v_mfma_f32_16x16x32_bf16 v[68:71], a[84:87], v[164:167], v[68:71]\n"
"ds_write_b128 v3, v[84:87] offset:1024\n"
"v_mfma_f32_16x16x32_bf16 v[68:71], a[88:91], v[180:183], v[68:71]\n"
"buffer_load_dwordx4 v[84:87], v1, s[4:7], s97 offen sc1\n"
"v_mfma_f32_16x16x32_bf16 v[68:71], a[92:95], v[72:75], v[68:71]\n"
"buffer_load_dword v250, v10, s[4:7], s98 offen offset:1024 sc1\n"
"v_mfma_f32_16x16x32_bf16 v[156:159], a[124:127], v[72:75], 0\n"
"v_mul_f32_e32 v138, v138, v254\n"
"v_mul_f32_e32 v139, v139, v254\n"
"v_mfma_f32_16x16x32_bf16 v[156:159], a[120:123], v[180:183], v[156:159]\n"
"v_cvt_pk_bf16_f32 v160, v172, v173\n"
"v_cvt_pk_bf16_f32 v161, v174, v175\n"
"v_mfma_f32_16x16x32_bf16 v[156:159], a[116:119], v[164:167], v[156:159]\n"
"buffer_load_dword v254, v10, s[4:7], s98 offen offset:1280 sc1\n"
"v_mfma_f32_16x16x32_bf16 v[156:159], a[112:115], v[104:107], v[156:159]\n"
"buffer_load_dwordx4 v[172:175], v1, s[4:7], s97 offen offset:1024 sc1\n"
"v_mfma_f32_16x16x32_bf16 v[156:159], a[108:111], v[132:135], v[156:159]\n"
"buffer_load_dwordx4 a[132:135], v0, s[4:7], s96 offen offset:1024 sc1\n"
"v_mfma_f32_16x16x32_bf16 v[156:159], a[104:107], v[168:171], v[156:159]\n"
"ds_write_b128 v3, v[68:71] offset:2048\n"
"v_mfma_f32_16x16x32_bf16 v[156:159], a[100:103], v[40:43], v[156:159]\n"
"buffer_load_dword v249, v10, s[4:7], s98 offen offset:1536 sc1\n"
"v_mfma_f32_16x16x32_bf16 v[156:159], a[96:99], v[96:99], v[156:159]\n"
"v_cvt_pk_bf16_f32 v162, v136, v137\n"
"v_cvt_pk_bf16_f32 v163, v138, v139\n"
"v_mfma_f32_16x16x32_bf16 v[68:71], v[24:27], v[96:99], 0\n"
"buffer_load_dwordx4 v[136:139], v1, s[4:7], s97 offen offset:2048 sc1\n"
"v_mfma_f32_16x16x32_bf16 v[68:71], v[44:47], v[40:43], v[68:71]\n"
"buffer_load_dword v248, v10, s[4:7], s98 offen offset:1792 sc1\n"
"v_mfma_f32_16x16x32_bf16 v[68:71], v[28:31], v[168:171], v[68:71]\n"
"ds_write_b128 v2, v[160:163] offset:49152\n"
"v_mfma_f32_16x16x32_bf16 v[68:71], v[56:59], v[132:135], v[68:71]\n"
"v_mfma_f32_16x16x32_bf16 v[68:71], v[32:35], v[104:107], v[68:71]\n"
"v_mfma_f32_16x16x32_bf16 v[68:71], v[116:119], v[164:167], v[68:71]\n"
"buffer_load_dwordx4 v[160:163], v1, s[4:7], s97 offen offset:3072 sc1\n"
"v_mfma_f32_16x16x32_bf16 v[68:71], v[48:51], v[180:183], v[68:71]\n"
"ds_write_b128 v3, v[156:159] offset:3072\n"
"v_mfma_f32_16x16x32_bf16 v[68:71], v[64:67], v[72:75], v[68:71]\n"
"v_mfma_f32_16x16x32_bf16 v[156:159], v[148:151], v[72:75], 0\n"
"v_mfma_f32_16x16x32_bf16 v[156:159], v[144:147], v[180:183], v[156:159]\n"
"v_mfma_f32_16x16x32_bf16 v[156:159], v[108:111], v[164:167], v[156:159]\n"
"v_mfma_f32_16x16x32_bf16 v[156:159], v[124:127], v[104:107], v[156:159]\n"
"v_mfma_f32_16x16x32_bf16 v[156:159], v[120:123], v[132:135], v[156:159]\n"
"v_mfma_f32_16x16x32_bf16 v[156:159], v[128:131], v[168:171], v[156:159]\n"
"s_nop 1\n"
"ds_write_b128 v3, v[68:71] offset:4096\n"
"v_mfma_f32_16x16x32_bf16 v[156:159], v[36:39], v[40:43], v[156:159]\n"
"v_mfma_f32_16x16x32_bf16 v[156:159], v[112:115], v[96:99], v[156:159]\n"
"s_waitcnt vmcnt(23)\n"
"; @A G1 1.1\n"
"v_mfma_f32_16x16x32_bf16 v[40:43], a[0:3], v[196:199], 0\n"
"buffer_load_dwordx4 a[136:139], v0, s[4:7], s96 offen offset:2048 sc1\n"
"v_mfma_f32_16x16x32_bf16 v[40:43], a[4:7], v[92:95], v[40:43]\n"
"v_mfma_f32_16x16x32_bf16 v[40:43], a[8:11], v[100:103], v[40:43]\n"
"s_add_u32 s98, s55, 0x48000\n"
"buffer_load_dwordx4 v[68:71], v0, s[4:7], s98 offen sc1\n"
"v_mfma_f32_16x16x32_bf16 v[40:43], a[12:15], v[60:63], v[40:43]\n"
"s_waitcnt vmcnt(21)\n"
"v_mfma_f32_16x16x32_bf16 v[40:43], a[16:19], v[140:143], v[40:43]\n"
"v_mfma_f32_16x16x32_bf16 v[40:43], a[20:23], v[192:195], v[40:43]\n"
"ds_write_b128 v3, v[156:159] offset:5120\n"
"v_mfma_f32_16x16x32_bf16 v[40:43], a[24:27], v[208:211], v[40:43]\n"
"v_mfma_f32_16x16x32_bf16 v[40:43], a[28:31], v[184:187], v[40:43]\n"
"buffer_load_dwordx4 v[72:75], v0, s[4:7], s98 offen offset:1024 sc1\n"
"v_mfma_f32_16x16x32_bf16 v[96:99], a[60:63], v[184:187], 0\n"
"v_mfma_f32_16x16x32_bf16 v[96:99], a[56:59], v[208:211], v[96:99]\n"
"v_mfma_f32_16x16x32_bf16 v[96:99], a[52:55], v[192:195], v[96:99]\n"
"v_mfma_f32_16x16x32_bf16 v[96:99], a[48:51], v[140:143], v[96:99]\n"
"v_mfma_f32_16x16x32_bf16 v[96:99], a[44:47], v[60:63], v[96:99]\n"
"s_add_u32 s97, s67, 0\n"
"s_nop 0\n"
"v_cmp_u_f32_e32 vcc, v40, v40\n"
"v_mfma_f32_16x16x32_bf16 v[96:99], a[40:43], v[100:103], v[96:99]\n"
"s_cbranch_vccnz .Lmk_p87s\n"
".Lmk_p87r:\n"
"v_mfma_f32_16x16x32_bf16 v[96:99], a[36:39], v[92:95], v[96:99]\n"
"buffer_load_dwordx4 v[104:107], v0, s[4:7], s98 offen offset:2048 sc1\n"
"s_add_u32 s95, s64, 0x2000\n"
"v_mfma_f32_16x16x32_bf16 v[96:99], a[32:35], v[196:199], v[96:99]\n"
"ds_write_b128 v3, v[40:43] offset:6144\n"
"v_mfma_f32_16x16x32_bf16 v[40:43], a[64:67], v[196:199], 0\n"
"buffer_load_dwordx2 v[246:247], v12, s[4:7], s97 offen sc1\n"
"v_mfma_f32_16x16x32_bf16 v[40:43], a[68:71], v[92:95], v[40:43]\n"
"buffer_load_dwordx4 v[132:135], v0, s[4:7], s98 offen offset:3072 sc1\n"
"v_mfma_f32_16x16x32_bf16 v[40:43], a[72:75], v[100:103], v[40:43]\n"
"buffer_store_dwordx4 v[216:219], v7, s[4:7], s95 offen sc1\n"
"v_mfma_f32_16x16x32_bf16 v[40:43], a[76:79], v[60:63], v[40:43]\n"
"v_mfma_f32_16x16x32_bf16 v[40:43], a[80:83], v[140:143], v[40:43]\n"
"buffer_load_dwordx4 a[140:143], v0, s[4:7], s96 offen offset:3072 sc1\n"
"v_mfma_f32_16x16x32_bf16 v[40:43], a[84:87], v[192:195], v[40:43]\n"
"ds_write_b128 v3, v[96:99] offset:7168\n"
"v_mfma_f32_16x16x32_bf16 v[40:43], a[88:91], v[208:211], v[40:43]\n"
"buffer_load_dwordx4 v[96:99], v1, s[4:7], s98 offen sc1\n"
"v_mfma_f32_16x16x32_bf16 v[40:43], a[92:95], v[184:187], v[40:43]\n"
"v_mfma_f32_16x16x32_bf16 v[156:159], a[124:127], v[184:187], 0\n"
"v_mfma_f32_16x16x32_bf16 v[156:159], a[120:123], v[208:211], v[156:159]\n"
"v_mfma_f32_16x16x32_bf16 v[156:159], a[116:119], v[192:195], v[156:159]\n"
"v_mfma_f32_16x16x32_bf16 v[156:159], a[112:115], v[140:143], v[156:159]\n"
"buffer_load_dwordx4 v[164:167], v1, s[4:7], s98 offen offset:1024 sc1\n"
"v_mfma_f32_16x16x32_bf16 v[156:159], a[108:111], v[60:63], v[156:159]\n"
"v_mfma_f32_16x16x32_bf16 v[156:159], a[104:107], v[100:103], v[156:159]\n"
"s_nop 0\n"
"ds_write_b128 v3, v[40:43] offset:8192\n"
"v_mfma_f32_16x16x32_bf16 v[156:159], a[100:103], v[92:95], v[156:159]\n"
"v_mfma_f32_16x16x32_bf16 v[156:159], a[96:99], v[196:199], v[156:159]\n"
"v_mfma_f32_16x16x32_bf16 v[40:43], v[24:27], v[196:199], 0\n"
"buffer_load_dwordx4 v[168:171], v1, s[4:7], s98 offen offset:2048 sc1\n"
"v_mfma_f32_16x16x32_bf16 v[40:43], v[44:47], v[92:95], v[40:43]\n"
"v_mfma_f32_16x16x32_bf16 v[40:43], v[28:31], v[100:103], v[40:43]\n"
"v_mfma_f32_16x16x32_bf16 v[40:43], v[56:59], v[60:63], v[40:43]\n"
"v_mfma_f32_16x16x32_bf16 v[40:43], v[32:35], v[140:143], v[40:43]\n"
"s_waitcnt vmcnt(12)\n"
"v_add_f32_e32 v255, v255, v250\n"
"v_add_f32_e32 v253, v253, v254\n"
"v_mfma_f32_16x16x32_bf16 v[40:43], v[116:119], v[192:195], v[40:43]\n"
"buffer_load_dwordx4 v[176:179], v1, s[4:7], s98 offen offset:3072 sc1\n"
"v_mfma_f32_16x16x32_bf16 v[40:43], v[48:51], v[208:211], v[40:43]\n"
"ds_write_b128 v3, v[156:159] offset:9216\n"
"v_mfma_f32_16x16x32_bf16 v[40:43], v[64:67], v[184:187], v[40:43]\n"
"v_add_f32_e32 v252, v252, v249\n"
"v_add_f32_e32 v251, v251, v248\n"
"v_mfma_f32_16x16x32_bf16 v[156:159], v[148:151], v[184:187], 0\n"
"buffer_load_dwordx4 a[144:147], v1, s[4:7], s96 offen sc1\n"
"s_add_u32 s98, s64, 0x6000\n"
"v_mfma_f32_16x16x32_bf16 v[156:159], v[144:147], v[208:211], v[156:159]\n"
"v_add_f32_e32 v255, v255, v252\n"
"v_add_f32_e32 v253, v253, v251\n"
"v_mfma_f32_16x16x32_bf16 v[156:159], v[108:111], v[192:195], v[156:159]\n"
"buffer_store_dwordx4 v[52:55], v7, s[4:7], s98 offen sc1\n"
"v_mfma_f32_16x16x32_bf16 v[156:159], v[124:127], v[140:143], v[156:159]\n"
"v_add_f32_e32 v255, v255, v253\n"
"v_cmp_u_f32_e32 vcc, v255, v255\n"
"v_mfma_f32_16x16x32_bf16 v[156:159], v[120:123], v[60:63], v[156:159]\n"
"s_cbranch_vccnz .Lmk_m90s\n"
".Lmk_m90r:\n"
"v_mov_b32_e32 v253, v255\n"
"v_mfma_f32_16x16x32_bf16 v[156:159], v[128:131], v[100:103], v[156:159]\n"
"ds_write_b128 v3, v[40:43] offset:10240\n"
"v_mfma_f32_16x16x32_bf16 v[156:159], v[36:39], v[92:95], v[156:159]\n"
"v_permlane32_swap_b32_e32 v255, v253\n"
"v_add_f32_e32 v255, v255, v253\n"
"v_mfma_f32_16x16x32_bf16 v[156:159], v[112:115], v[196:199], v[156:159]\n"
"v_mul_f32_e32 v255, 0x3a800000, v255\n"
"v_mov_b32_e32 v253, v255\n"
"; @A G1 1.2\n"
"v_mfma_f32_16x16x32_bf16 v[40:43], a[0:3], v[76:79], 0\n"
"s_nop 0\n"
"v_permlane16_swap_b32_e32 v255, v253\n"
"v_cvt_pk_bf16_f32 v254, -v255, 0\n"
"v_mfma_f32_16x16x32_bf16 v[40:43], a[4:7], v[80:83], v[40:43]\n"
"s_waitcnt vmcnt(9)\n"
"v_cmp_ne_u64_e32 vcc, v[246:247], v[18:19]\n"
"v_mfma_f32_16x16x32_bf16 v[40:43], a[8:11], v[88:91], v[40:43]\n"
"s_cbranch_vccnz .Lmk_f77s\n"
".Lmk_f77r:\n"
"v_fma_f32 v253, -v255, v255, v253\n"
"v_mfma_f32_16x16x32_bf16 v[40:43], a[12:15], v[152:155], v[40:43]\n"
"v_lshlrev_b32_e32 v254, 16, v254\n"
"v_max_f32_e32 v253, 0, v253\n"
"v_mfma_f32_16x16x32_bf16 v[40:43], a[16:19], v[84:87], v[40:43]\n"
"v_cvt_pk_bf16_f32 v253, v253, 0\n"
"v_lshlrev_b32_e32 v253, 16, v253\n"
"v_mfma_f32_16x16x32_bf16 v[40:43], a[20:23], v[172:175], v[40:43]\n"
"ds_write_b128 v3, v[156:159] offset:11264\n"
"v_mfma_f32_16x16x32_bf16 v[40:43], a[24:27], v[136:139], v[40:43]\n"
"v_add_f32_e32 v253, 0x3727c5ac, v253\n"
"v_cvt_pk_bf16_f32 v253, v253, 0\n"
"v_mfma_f32_16x16x32_bf16 v[40:43], a[28:31], v[160:163], v[40:43]\n"
"v_lshlrev_b32_e32 v253, 16, v253\n"
"v_mfma_f32_16x16x32_bf16 v[52:55], a[60:63], v[160:163], 0\n"
"v_sqrt_f32_e32 v253, v253\n"
"v_mfma_f32_16x16x32_bf16 v[52:55], a[56:59], v[136:139], v[52:55]\n"
"v_cvt_pk_bf16_f32 v253, v253, 0\n"
"v_lshlrev_b32_e32 v253, 16, v253\n"
"v_mfma_f32_16x16x32_bf16 v[52:55], a[52:55], v[172:175], v[52:55]\n"
"s_add_u32 s98, s52, 0x8000\n"
"buffer_load_dwordx4 v[60:63], v0, s[4:7], s98 offen sc1\n"
"v_mfma_f32_16x16x32_bf16 v[52:55], a[48:51], v[84:87], v[52:55]\n"
"s_waitcnt lgkmcnt(0)\n"
"s_barrier\n"
"v_mfma_f32_16x16x32_bf16 v[52:55], a[44:47], v[152:155], v[52:55]\n"
"ds_read_b128 v[92:95], v4\n"
"v_cmp_u_f32_e32 vcc, v40, v40\n"
"v_mfma_f32_16x16x32_bf16 v[52:55], a[40:43], v[88:91], v[52:55]\n"
"s_cbranch_vccnz .Lmk_p88s\n"
".Lmk_p88r:\n"
"ds_read_b128 v[100:103], v9 offset:128\n"
"v_mfma_f32_16x16x32_bf16 v[52:55], a[36:39], v[80:83], v[52:55]\n"
"ds_write_b128 v3, v[40:43] offset:12288\n"
"v_mfma_f32_16x16x32_bf16 v[52:55], a[32:35], v[76:79], v[52:55]\n"
"buffer_load_dwordx4 v[40:43], v0, s[4:7], s98 offen offset:1024 sc1\n"
"v_mfma_f32_16x16x32_bf16 v[140:143], a[64:67], v[76:79], 0\n"
"buffer_load_dwordx4 a[148:151], v1, s[4:7], s96 offen offset:1024 sc1\n"
"v_mfma_f32_16x16x32_bf16 v[140:143], a[68:71], v[80:83], v[140:143]\n"
"v_rcp_f32_e32 v255, v253\n"
"v_mfma_f32_16x16x32_bf16 v[140:143], a[72:75], v[88:91], v[140:143]\n"
"ds_read_b128 v[156:159], v5\n"
"v_mul_f32_e32 v253, 0x3e8293ee, v255\n"
"v_mfma_f32_16x16x32_bf16 v[140:143], a[76:79], v[152:155], v[140:143]\n"
"ds_read_b128 v[180:183], v4 offset:1024\n"
"ds_read_b128 v[184:187], v9 offset:192\n"
"v_mfma_f32_16x16x32_bf16 v[140:143], a[80:83], v[84:87], v[140:143]\n"
"buffer_load_dwordx4 v[188:191], v0, s[4:7], s98 offen offset:2048 sc1\n"
"v_mfma_f32_16x16x32_bf16 v[140:143], a[84:87], v[172:175], v[140:143]\n"
"ds_write_b128 v3, v[52:55] offset:13312\n"
"v_mfma_f32_16x16x32_bf16 v[140:143], a[88:91], v[136:139], v[140:143]\n"
"ds_read_b128 v[52:55], v5 offset:1024\n"
"v_mfma_f32_16x16x32_bf16 v[140:143], a[92:95], v[160:163], v[140:143]\n"
"s_waitcnt lgkmcnt(4)\n"
"v_fmac_f32_e32 v92, v254, v100\n"
"v_fmac_f32_e32 v93, v254, v101\n"
"v_mfma_f32_16x16x32_bf16 v[192:195], a[124:127], v[160:163], 0\n"
"v_fmac_f32_e32 v94, v254, v102\n"
"v_fmac_f32_e32 v95, v254, v103\n"
"v_mfma_f32_16x16x32_bf16 v[192:195], a[120:123], v[136:139], v[192:195]\n"
"buffer_load_dwordx4 v[100:103], v0, s[4:7], s98 offen offset:3072 sc1\n"
"v_mfma_f32_16x16x32_bf16 v[192:195], a[116:119], v[172:175], v[192:195]\n"
"ds_read_b128 v[196:199], v4 offset:2048\n"
"ds_read_b128 v[200:203], v9 offset:256\n"
"v_mfma_f32_16x16x32_bf16 v[192:195], a[112:115], v[84:87], v[192:195]\n"
"ds_read_b128 v[204:207], v5 offset:2048\n"
"v_add_f32_e32 v92, v92, v156\n"
"v_mfma_f32_16x16x32_bf16 v[192:195], a[108:111], v[152:155], v[192:195]\n"
"v_add_f32_e32 v93, v93, v157\n"
"v_add_f32_e32 v94, v94, v158\n"
"v_mfma_f32_16x16x32_bf16 v[192:195], a[104:107], v[88:91], v[192:195]\n"
"ds_write_b128 v3, v[140:143] offset:14336\n"
"v_mfma_f32_16x16x32_bf16 v[192:195], a[100:103], v[80:83], v[192:195]\n"
"buffer_load_dwordx4 v[140:143], v1, s[4:7], s98 offen sc1\n"
"v_mfma_f32_16x16x32_bf16 v[192:195], a[96:99], v[76:79], v[192:195]\n"
"s_waitcnt lgkmcnt(4)\n"
"v_add_f32_e32 v95, v95, v159\n"
"v_fmac_f32_e32 v180, v254, v184\n"
"v_mfma_f32_16x16x32_bf16 v[156:159], v[24:27], v[76:79], 0\n"
"v_fmac_f32_e32 v181, v254, v185\n"
"v_fmac_f32_e32 v182, v254, v186\n"
"v_mfma_f32_16x16x32_bf16 v[156:159], v[44:47], v[80:83], v[156:159]\n"
"ds_read_b128 v[208:211], v4 offset:3072\n"
"ds_read_b128 v[212:215], v9 offset:320\n"
"v_mfma_f32_16x16x32_bf16 v[156:159], v[28:31], v[88:91], v[156:159]\n"
"ds_read_b128 v[216:219], v5 offset:3072\n"
"v_fmac_f32_e32 v183, v254, v187\n"
"v_mfma_f32_16x16x32_bf16 v[156:159], v[56:59], v[152:155], v[156:159]\n"
"buffer_load_dwordx4 v[184:187], v1, s[4:7], s98 offen offset:1024 sc1\n"
"v_mfma_f32_16x16x32_bf16 v[156:159], v[32:35], v[84:87], v[156:159]\n"
"buffer_load_dwordx4 a[152:155], v1, s[4:7], s96 offen offset:2048 sc1\n"
"v_mfma_f32_16x16x32_bf16 v[156:159], v[116:119], v[172:175], v[156:159]\n"
"ds_write_b128 v3, v[192:195] offset:15360\n"
"v_mfma_f32_16x16x32_bf16 v[156:159], v[48:51], v[136:139], v[156:159]\n"
"v_add_f32_e32 v180, v180, v52\n"
"v_add_f32_e32 v181, v181, v53\n"
"v_mfma_f32_16x16x32_bf16 v[156:159], v[64:67], v[160:163], v[156:159]\n"
"s_waitcnt lgkmcnt(5)\n"
"v_add_f32_e32 v182, v182, v54\n"
"v_add_f32_e32 v183, v183, v55\n"
"v_mfma_f32_16x16x32_bf16 v[52:55], v[148:151], v[160:163], 0\n"
"buffer_load_dwordx4 v[160:163], v1, s[4:7], s98 offen offset:2048 sc1\n"
"v_mfma_f32_16x16x32_bf16 v[52:55], v[144:147], v[136:139], v[52:55]\n"
"ds_read_b128 v[136:139], v4 offset:4096\n"
"ds_read_b128 v[192:195], v9\n"
"v_mfma_f32_16x16x32_bf16 v[52:55], v[108:111], v[172:175], v[52:55]\n"
"ds_read_b128 v[172:175], v5 offset:4096\n"
"v_fmac_f32_e32 v196, v254, v200\n"
"v_mfma_f32_16x16x32_bf16 v[52:55], v[124:127], v[84:87], v[52:55]\n"
"v_fmac_f32_e32 v197, v254, v201\n"
"v_fmac_f32_e32 v198, v254, v202\n"
"v_mfma_f32_16x16x32_bf16 v[52:55], v[120:123], v[152:155], v[52:55]\n"
"v_fmac_f32_e32 v199, v254, v203\n"
"v_add_f32_e32 v196, v196, v204\n"
"v_mfma_f32_16x16x32_bf16 v[52:55], v[128:131], v[88:91], v[52:55]\n"
"buffer_load_dwordx4 v[84:87], v1, s[4:7], s98 offen offset:3072 sc1\n"
"v_mfma_f32_16x16x32_bf16 v[52:55], v[36:39], v[80:83], v[52:55]\n"
"ds_write_b128 v3, v[156:159] offset:16384\n"
"v_mfma_f32_16x16x32_bf16 v[52:55], v[112:115], v[76:79], v[52:55]\n"
"s_waitcnt vmcnt(18)\n"
"ds_read_b128 v[76:79], v4 offset:5120\n"
"ds_read_b128 v[80:83], v9 offset:64\n"
"; @A G1 1.3\n"
"v_mfma_f32_16x16x32_bf16 v[88:91], a[0:3], v[68:71], v[92:95]\n"
"ds_read_b128 v[152:155], v5 offset:5120\n"
"v_add_f32_e32 v197, v197, v205\n"
"v_mfma_f32_16x16x32_bf16 v[88:91], a[4:7], v[72:75], v[88:91]\n"
"s_waitcnt lgkmcnt(8)\n"
"v_add_f32_e32 v198, v198, v206\n"
"v_add_f32_e32 v199, v199, v207\n"
"v_mfma_f32_16x16x32_bf16 v[88:91], a[8:11], v[104:107], v[88:91]\n"
"s_add_u32 s98, s53, 0x8000\n"
"buffer_load_dwordx4 v[156:159], v0, s[4:7], s98 offen sc1\n"
"v_mfma_f32_16x16x32_bf16 v[88:91], a[12:15], v[132:135], v[88:91]\n"
"s_waitcnt vmcnt(13)\n"
"v_fmac_f32_e32 v208, v254, v212\n"
"v_fmac_f32_e32 v209, v254, v213\n"
"v_mfma_f32_16x16x32_bf16 v[88:91], a[16:19], v[96:99], v[88:91]\n"
"v_fmac_f32_e32 v210, v254, v214\n"
"v_fmac_f32_e32 v211, v254, v215\n"
"v_mfma_f32_16x16x32_bf16 v[88:91], a[20:23], v[164:167], v[88:91]\n"
"ds_write_b128 v3, v[52:55] offset:17408\n"
"v_mfma_f32_16x16x32_bf16 v[88:91], a[24:27], v[168:171], v[88:91]\n"
"v_add_f32_e32 v208, v208, v216\n"
"v_add_f32_e32 v209, v209, v217\n"
"v_mfma_f32_16x16x32_bf16 v[88:91], a[28:31], v[176:179], v[88:91]\n"
"buffer_load_dwordx4 v[52:55], v0, s[4:7], s98 offen offset:1024 sc1\n"
"v_mfma_f32_16x16x32_bf16 v[200:203], a[60:63], v[176:179], v[180:183]\n"
"buffer_load_dwordx4 a[156:159], v1, s[4:7], s96 offen offset:3072 sc1\n"
"v_mfma_f32_16x16x32_bf16 v[200:203], a[56:59], v[168:171], v[200:203]\n"
"s_waitcnt lgkmcnt(5)\n"
"v_add_f32_e32 v210, v210, v218\n"
"v_add_f32_e32 v211, v211, v219\n"
"v_mfma_f32_16x16x32_bf16 v[200:203], a[52:55], v[164:167], v[200:203]\n"
"s_waitcnt lgkmcnt(0)\n"
"s_barrier\n"
"v_mfma_f32_16x16x32_bf16 v[200:203], a[48:51], v[96:99], v[200:203]\n"
"ds_read_b128 v[204:207], v6\n"
"ds_read_b128 v[212:215], v6 offset:1024\n"
"v_mfma_f32_16x16x32_bf16 v[200:203], a[44:47], v[132:135], v[200:203]\n"
"v_cmp_u_f32_e32 vcc, v88, v88\n"
"v_fmac_f32_e32 v136, v254, v192\n"
"v_mfma_f32_16x16x32_bf16 v[200:203], a[40:43], v[104:107], v[200:203]\n"
"s_cbranch_vccnz .Lmk_p89s\n"
".Lmk_p89r:\n"
"v_fmac_f32_e32 v137, v254, v193\n"
"v_mfma_f32_16x16x32_bf16 v[200:203], a[36:39], v[72:75], v[200:203]\n"
"buffer_load_dwordx4 v[92:95], v0, s[4:7], s98 offen offset:2048 sc1\n"
"v_mfma_f32_16x16x32_bf16 v[200:203], a[32:35], v[68:71], v[200:203]\n"
"v_fmac_f32_e32 v138, v254, v194\n"
"v_fmac_f32_e32 v139, v254, v195\n"
"v_mfma_f32_16x16x32_bf16 v[196:199], a[64:67], v[68:71], v[196:199]\n"
"v_add_f32_e32 v136, v136, v172\n"
"v_add_f32_e32 v137, v137, v173\n"
"v_mfma_f32_16x16x32_bf16 v[196:199], a[68:71], v[72:75], v[196:199]\n"
"buffer_load_dwordx4 v[180:183], v0, s[4:7], s98 offen offset:3072 sc1\n"
"v_mfma_f32_16x16x32_bf16 v[196:199], a[72:75], v[104:107], v[196:199]\n"
"v_add_f32_e32 v138, v138, v174\n"
"v_add_f32_e32 v139, v139, v175\n"
"v_mfma_f32_16x16x32_bf16 v[196:199], a[76:79], v[132:135], v[196:199]\n"
"v_fmac_f32_e32 v76, v254, v80\n"
"v_fmac_f32_e32 v77, v254, v81\n"
"v_mfma_f32_16x16x32_bf16 v[196:199], a[80:83], v[96:99], v[196:199]\n"
"s_waitcnt lgkmcnt(0)\n"
"v_fmac_f32_e32 v78, v254, v82\n"
"v_fmac_f32_e32 v79, v254, v83\n"
"v_mfma_f32_16x16x32_bf16 v[196:199], a[84:87], v[164:167], v[196:199]\n"
"v_add_f32_e32 v76, v76, v152\n"
"v_add_f32_e32 v77, v77, v153\n"
"v_mfma_f32_16x16x32_bf16 v[196:199], a[88:91], v[168:171], v[196:199]\n"
"buffer_load_dwordx4 v[80:83], v1, s[4:7], s98 offen sc1\n"
"v_mfma_f32_16x16x32_bf16 v[196:199], a[92:95], v[176:179], v[196:199]\n"
"ds_read_b128 v[172:175], v6 offset:2048\n"
"ds_read_b128 v[192:195], v6 offset:3072\n"
"v_mfma_f32_16x16x32_bf16 v[208:211], a[124:127], v[176:179], v[208:211]\n"
"v_add_f32_e32 v78, v78, v154\n"
"v_add_f32_e32 v79, v79, v155\n"
"v_mfma_f32_16x16x32_bf16 v[208:211], a[120:123], v[168:171], v[208:211]\n"
"v_add_f32_e32 v88, v88, v204\n"
"v_add_f32_e32 v89, v89, v205\n"
"v_mfma_f32_16x16x32_bf16 v[208:211], a[116:119], v[164:167], v[208:211]\n"
"s_add_u32 s97, s70, 0x2000\n"
"v_add_f32_e32 v90, v90, v206\n"
"v_add_f32_e32 v91, v91, v207\n"
"v_mfma_f32_16x16x32_bf16 v[208:211], a[112:115], v[96:99], v[208:211]\n"
"buffer_load_dwordx4 v[152:155], v1, s[4:7], s98 offen offset:1024 sc1\n"
"v_mfma_f32_16x16x32_bf16 v[208:211], a[108:111], v[132:135], v[208:211]\n"
"buffer_load_dwordx4 a[160:163], v0, s[4:7], s97 offen sc1\n"
"v_mfma_f32_16x16x32_bf16 v[208:211], a[104:107], v[104:107], v[208:211]\n"
"v_add_f32_e32 v200, v200, v212\n"
"v_add_f32_e32 v201, v201, v213\n"
"v_mfma_f32_16x16x32_bf16 v[208:211], a[100:103], v[72:75], v[208:211]\n"
"v_add_f32_e32 v202, v202, v214\n"
"v_add_f32_e32 v203, v203, v215\n"
"v_mfma_f32_16x16x32_bf16 v[208:211], a[96:99], v[68:71], v[208:211]\n"
"v_mul_f32_e32 v88, v88, v255\n"
"v_mul_f32_e32 v89, v89, v255\n"
"v_mfma_f32_16x16x32_bf16 v[136:139], v[24:27], v[68:71], v[136:139]\n"
"buffer_load_dwordx4 v[204:207], v1, s[4:7], s98 offen offset:2048 sc1\n"
"v_mfma_f32_16x16x32_bf16 v[136:139], v[44:47], v[72:75], v[136:139]\n"
"v_mul_f32_e32 v90, v90, v255\n"
"v_mul_f32_e32 v91, v91, v255\n"
"v_mfma_f32_16x16x32_bf16 v[136:139], v[28:31], v[104:107], v[136:139]\n"
"v_mul_f32_e32 v200, v200, v255\n"
"v_mul_f32_e32 v201, v201, v255\n"
"v_mfma_f32_16x16x32_bf16 v[136:139], v[56:59], v[132:135], v[136:139]\n"
"v_mul_f32_e32 v202, v202, v255\n"
"v_mul_f32_e32 v203, v203, v255\n"
"v_mfma_f32_16x16x32_bf16 v[136:139], v[32:35], v[96:99], v[136:139]\n"
"s_waitcnt lgkmcnt(0)\n"
"v_cvt_pk_bf16_f32 v212, v88, v89\n"
"v_cvt_pk_bf16_f32 v213, v90, v91\n"
"v_mfma_f32_16x16x32_bf16 v[136:139], v[116:119], v[164:167], v[136:139]\n"
"buffer_load_dwordx4 v[88:91], v1, s[4:7], s98 offen offset:3072 sc1\n"
"v_mfma_f32_16x16x32_bf16 v[136:139], v[48:51], v[168:171], v[136:139]\n"
"ds_read_b128 v[216:219], v6 offset:4096\n"
"ds_read_b128 v[220:223], v6 offset:5120\n"
"v_mfma_f32_16x16x32_bf16 v[136:139], v[64:67], v[176:179], v[136:139]\n"
"v_cvt_pk_bf16_f32 v214, v200, v201\n"
"v_cvt_pk_bf16_f32 v215, v202, v203\n"
"v_mfma_f32_16x16x32_bf16 v[76:79], v[148:151], v[176:179], v[76:79]\n"
"ds_write_b128 v8, v[212:215] offset:25600\n"
"v_mfma_f32_16x16x32_bf16 v[76:79], v[144:147], v[168:171], v[76:79]\n"
"v_add_f32_e32 v196, v196, v172\n"
"v_add_f32_e32 v197, v197, v173\n"
"v_mfma_f32_16x16x32_bf16 v[76:79], v[108:111], v[164:167], v[76:79]\n"
"v_add_f32_e32 v198, v198, v174\n"
"v_add_f32_e32 v199, v199, v175\n"
"v_mfma_f32_16x16x32_bf16 v[76:79], v[124:127], v[96:99], v[76:79]\n"
"v_add_f32_e32 v208, v208, v192\n"
"v_add_f32_e32 v209, v209, v193\n"
"v_mfma_f32_16x16x32_bf16 v[76:79], v[120:123], v[132:135], v[76:79]\n"
"v_add_f32_e32 v210, v210, v194\n"
"v_add_f32_e32 v211, v211, v195\n"
"v_mfma_f32_16x16x32_bf16 v[76:79], v[128:131], v[104:107], v[76:79]\n"
"v_mul_f32_e32 v196, v196, v255\n"
"v_mul_f32_e32 v197, v197, v255\n"
"v_mfma_f32_16x16x32_bf16 v[76:79], v[36:39], v[72:75], v[76:79]\n"
"v_mul_f32_e32 v198, v198, v255\n"
"v_mul_f32_e32 v199, v199, v255\n"
"v_mfma_f32_16x16x32_bf16 v[76:79], v[112:115], v[68:71], v[76:79]\n"
"v_mul_f32_e32 v208, v208, v255\n"
"v_mul_f32_e32 v209, v209, v255\n"
"v_mul_f32_e32 v210, v210, v255\n"
"v_mul_f32_e32 v211, v211, v255\n"
"v_cvt_pk_bf16_f32 v68, v196, v197\n"
"v_cvt_pk_bf16_f32 v69, v198, v199\n"
"v_cvt_pk_bf16_f32 v70, v208, v209\n"
"v_cvt_pk_bf16_f32 v71, v210, v211\n"
"ds_write_b128 v7, v[68:71] offset:58368\n"
".Lmk_a2:\n"
"s_waitcnt vmcnt(15)\n"
"; @A G1 2.0\n"
"v_mfma_f32_16x16x32_bf16 v[72:75], a[0:3], v[60:63], 0\n"
"v_mfma_f32_16x16x32_bf16 v[72:75], a[4:7], v[40:43], v[72:75]\n"
"s_add_u32 s98, s62, 0x800\n"
"v_mfma_f32_16x16x32_bf16 v[72:75], a[8:11], v[188:191], v[72:75]\n"
"s_add_u32 s96, s54, 0x8000\n"
"buffer_load_dwordx4 v[96:99], v0, s[4:7], s96 offen sc1\n"
"v_mfma_f32_16x16x32_bf16 v[72:75], a[12:15], v[100:103], v[72:75]\n"
"s_waitcnt vmcnt(11)\n"
"v_mfma_f32_16x16x32_bf16 v[72:75], a[16:19], v[140:143], v[72:75]\n"
"buffer_load_dword v255, v10, s[4:7], s98 offen sc1\n"
"v_mfma_f32_16x16x32_bf16 v[72:75], a[20:23], v[184:187], v[72:75]\n"
"s_waitcnt lgkmcnt(2)\n"
"v_add_f32_e32 v136, v136, v216\n"
"v_add_f32_e32 v137, v137, v217\n"
"v_mfma_f32_16x16x32_bf16 v[72:75], a[24:27], v[160:163], v[72:75]\n"
"v_add_f32_e32 v138, v138, v218\n"
"v_add_f32_e32 v139, v139, v219\n"
"v_mfma_f32_16x16x32_bf16 v[72:75], a[28:31], v[84:87], v[72:75]\n"
"buffer_load_dwordx4 v[104:107], v0, s[4:7], s96 offen offset:1024 sc1\n"
"v_mfma_f32_16x16x32_bf16 v[132:135], a[60:63], v[84:87], 0\n"
"buffer_load_dwordx4 a[164:167], v0, s[4:7], s97 offen offset:1024 sc1\n"
"v_mfma_f32_16x16x32_bf16 v[132:135], a[56:59], v[160:163], v[132:135]\n"
"buffer_load_dword v254, v10, s[4:7], s98 offen offset:256 sc1\n"
"v_mfma_f32_16x16x32_bf16 v[132:135], a[52:55], v[184:187], v[132:135]\n"
"v_add_f32_e32 v76, v76, v220\n"
"v_add_f32_e32 v77, v77, v221\n"
"v_mfma_f32_16x16x32_bf16 v[132:135], a[48:51], v[140:143], v[132:135]\n"
"v_add_f32_e32 v78, v78, v222\n"
"v_add_f32_e32 v79, v79, v223\n"
"v_mfma_f32_16x16x32_bf16 v[132:135], a[44:47], v[100:103], v[132:135]\n"
"buffer_load_dwordx4 v[164:167], v0, s[4:7], s96 offen offset:2048 sc1\n"
"v_mfma_f32_16x16x32_bf16 v[132:135], a[40:43], v[188:191], v[132:135]\n"
"ds_write_b128 v3, v[72:75]\n"
"v_mfma_f32_16x16x32_bf16 v[132:135], a[36:39], v[40:43], v[132:135]\n"
"buffer_load_dword v252, v10, s[4:7], s98 offen offset:512 sc1\n"
"v_mfma_f32_16x16x32_bf16 v[132:135], a[32:35], v[60:63], v[132:135]\n"
"v_mul_f32_e32 v136, v136, v253\n"
"v_mul_f32_e32 v137, v137, v253\n"
"v_mfma_f32_16x16x32_bf16 v[72:75], a[64:67], v[60:63], 0\n"
"v_mul_f32_e32 v138, v138, v253\n"
"v_mul_f32_e32 v139, v139, v253\n"
"v_mfma_f32_16x16x32_bf16 v[72:75], a[68:71], v[40:43], v[72:75]\n"
"buffer_load_dwordx4 v[168:171], v0, s[4:7], s96 offen offset:3072 sc1\n"
"v_mfma_f32_16x16x32_bf16 v[72:75], a[72:75], v[188:191], v[72:75]\n"
"buffer_load_dword v251, v10, s[4:7], s98 offen offset:768 sc1\n"
"v_mfma_f32_16x16x32_bf16 v[72:75], a[76:79], v[100:103], v[72:75]\n"
"v_mul_f32_e32 v76, v76, v253\n"
"v_mul_f32_e32 v77, v77, v253\n"
"v_mfma_f32_16x16x32_bf16 v[72:75], a[80:83], v[140:143], v[72:75]\n"
"v_mul_f32_e32 v78, v78, v253\n"
"v_mul_f32_e32 v79, v79, v253\n"
"v_mfma_f32_16x16x32_bf16 v[72:75], a[84:87], v[184:187], v[72:75]\n"
"ds_write_b128 v3, v[132:135] offset:1024\n"
"v_mfma_f32_16x16x32_bf16 v[72:75], a[88:91], v[160:163], v[72:75]\n"
"buffer_load_dwordx4 v[132:135], v1, s[4:7], s96 offen sc1\n"
"v_mfma_f32_16x16x32_bf16 v[72:75], a[92:95], v[84:87], v[72:75]\n"
"buffer_load_dword v253, v10, s[4:7], s98 offen offset:1024 sc1\n"
"v_mfma_f32_16x16x32_bf16 v[172:175], a[124:127], v[84:87], 0\n"
"v_cvt_pk_bf16_f32 v176, v136, v137\n"
"v_cvt_pk_bf16_f32 v177, v138, v139\n"
"v_mfma_f32_16x16x32_bf16 v[172:175], a[120:123], v[160:163], v[172:175]\n"
"v_cvt_pk_bf16_f32 v178, v76, v77\n"
"v_cvt_pk_bf16_f32 v179, v78, v79\n"
"v_mfma_f32_16x16x32_bf16 v[172:175], a[116:119], v[184:187], v[172:175]\n"
"buffer_load_dword v250, v10, s[4:7], s98 offen offset:1280 sc1\n"
"v_mfma_f32_16x16x32_bf16 v[172:175], a[112:115], v[140:143], v[172:175]\n"
"buffer_load_dwordx4 v[76:79], v1, s[4:7], s96 offen offset:1024 sc1\n"
"v_mfma_f32_16x16x32_bf16 v[172:175], a[108:111], v[100:103], v[172:175]\n"
"buffer_load_dwordx4 a[168:171], v0, s[4:7], s97 offen offset:2048 sc1\n"
"v_mfma_f32_16x16x32_bf16 v[172:175], a[104:107], v[188:191], v[172:175]\n"
"ds_write_b128 v3, v[72:75] offset:2048\n"
"v_mfma_f32_16x16x32_bf16 v[172:175], a[100:103], v[40:43], v[172:175]\n"
"buffer_load_dword v249, v10, s[4:7], s98 offen offset:1536 sc1\n"
"v_mfma_f32_16x16x32_bf16 v[172:175], a[96:99], v[60:63], v[172:175]\n"
"ds_write_b128 v2, v[176:179] offset:50176\n"
"v_mfma_f32_16x16x32_bf16 v[72:75], v[24:27], v[60:63], 0\n"
"buffer_load_dwordx4 v[136:139], v1, s[4:7], s96 offen offset:2048 sc1\n"
"s_add_u32 s95, s64, 0x2000\n"
"v_mfma_f32_16x16x32_bf16 v[72:75], v[44:47], v[40:43], v[72:75]\n"
"buffer_load_dword v248, v10, s[4:7], s98 offen offset:1792 sc1\n"
"v_mfma_f32_16x16x32_bf16 v[72:75], v[28:31], v[188:191], v[72:75]\n"
"buffer_store_dwordx4 v[212:215], v8, s[4:7], s95 offen offset:1024 sc1\n"
"v_mfma_f32_16x16x32_bf16 v[72:75], v[56:59], v[100:103], v[72:75]\n"
"v_mfma_f32_16x16x32_bf16 v[72:75], v[32:35], v[140:143], v[72:75]\n"
"v_mfma_f32_16x16x32_bf16 v[72:75], v[116:119], v[184:187], v[72:75]\n"
"buffer_load_dwordx4 v[176:179], v1, s[4:7], s96 offen offset:3072 sc1\n"
"v_mfma_f32_16x16x32_bf16 v[72:75], v[48:51], v[160:163], v[72:75]\n"
"ds_write_b128 v3, v[172:175] offset:3072\n"
"v_mfma_f32_16x16x32_bf16 v[72:75], v[64:67], v[84:87], v[72:75]\n"
"v_mfma_f32_16x16x32_bf16 v[172:175], v[148:151], v[84:87], 0\n"
"v_mfma_f32_16x16x32_bf16 v[172:175], v[144:147], v[160:163], v[172:175]\n"
"v_mfma_f32_16x16x32_bf16 v[172:175], v[108:111], v[184:187], v[172:175]\n"
"v_mfma_f32_16x16x32_bf16 v[172:175], v[124:127], v[140:143], v[172:175]\n"
"v_mfma_f32_16x16x32_bf16 v[172:175], v[120:123], v[100:103], v[172:175]\n"
"v_mfma_f32_16x16x32_bf16 v[172:175], v[128:131], v[188:191], v[172:175]\n"
"s_nop 1\n"
"ds_write_b128 v3, v[72:75] offset:4096\n"
"v_mfma_f32_16x16x32_bf16 v[172:175], v[36:39], v[40:43], v[172:175]\n"
"v_mfma_f32_16x16x32_bf16 v[172:175], v[112:115], v[60:63], v[172:175]\n"
"s_waitcnt vmcnt(24)\n"
"; @A G1 2.1\n"
"v_mfma_f32_16x16x32_bf16 v[40:43], a[0:3], v[156:159], 0\n"
"buffer_load_dwordx4 a[172:175], v0, s[4:7], s97 offen offset:3072 sc1\n"
"v_mfma_f32_16x16x32_bf16 v[40:43], a[4:7], v[52:55], v[40:43]\n"
"v_mfma_f32_16x16x32_bf16 v[40:43], a[8:11], v[92:95], v[40:43]\n"
"s_add_u32 s98, s55, 0x8000\n"
"buffer_load_dwordx4 v[60:63], v0, s[4:7], s98 offen sc1\n"
"v_mfma_f32_16x16x32_bf16 v[40:43], a[12:15], v[180:183], v[40:43]\n"
"s_waitcnt vmcnt(21)\n"
"v_mfma_f32_16x16x32_bf16 v[40:43], a[16:19], v[80:83], v[40:43]\n"
"v_mfma_f32_16x16x32_bf16 v[40:43], a[20:23], v[152:155], v[40:43]\n"
"ds_write_b128 v3, v[172:175] offset:5120\n"
"v_mfma_f32_16x16x32_bf16 v[40:43], a[24:27], v[204:207], v[40:43]\n"
"v_mfma_f32_16x16x32_bf16 v[40:43], a[28:31], v[88:91], v[40:43]\n"
"buffer_load_dwordx4 v[72:75], v0, s[4:7], s98 offen offset:1024 sc1\n"
"v_mfma_f32_16x16x32_bf16 v[84:87], a[60:63], v[88:91], 0\n"
"s_add_u32 s96, s64, 0x6000\n"
"v_mfma_f32_16x16x32_bf16 v[84:87], a[56:59], v[204:207], v[84:87]\n"
"v_mfma_f32_16x16x32_bf16 v[84:87], a[52:55], v[152:155], v[84:87]\n"
"buffer_store_dwordx4 v[68:71], v7, s[4:7], s96 offen offset:1024 sc1\n"
"v_mfma_f32_16x16x32_bf16 v[84:87], a[48:51], v[80:83], v[84:87]\n"
"v_mfma_f32_16x16x32_bf16 v[84:87], a[44:47], v[180:183], v[84:87]\n"
"buffer_load_dwordx4 v[68:71], v0, s[4:7], s98 offen offset:2048 sc1\n"
"v_mfma_f32_16x16x32_bf16 v[84:87], a[40:43], v[92:95], v[84:87]\n"
"ds_write_b128 v3, v[40:43] offset:6144\n"
"v_mfma_f32_16x16x32_bf16 v[84:87], a[36:39], v[52:55], v[84:87]\n"
"v_mfma_f32_16x16x32_bf16 v[84:87], a[32:35], v[156:159], v[84:87]\n"
"v_mfma_f32_16x16x32_bf16 v[40:43], a[64:67], v[156:159], 0\n"
"v_mfma_f32_16x16x32_bf16 v[40:43], a[68:71], v[52:55], v[40:43]\n"
"buffer_load_dwordx4 v[100:103], v0, s[4:7], s98 offen offset:3072 sc1\n"
"v_mfma_f32_16x16x32_bf16 v[40:43], a[72:75], v[92:95], v[40:43]\n"
"v_mfma_f32_16x16x32_bf16 v[40:43], a[76:79], v[180:183], v[40:43]\n"
"v_mfma_f32_16x16x32_bf16 v[40:43], a[80:83], v[80:83], v[40:43]\n"
"buffer_load_dwordx4 a[176:179], v1, s[4:7], s97 offen sc1\n"
"v_mfma_f32_16x16x32_bf16 v[40:43], a[84:87], v[152:155], v[40:43]\n"
"ds_write_b128 v3, v[84:87] offset:7168\n"
"v_mfma_f32_16x16x32_bf16 v[40:43], a[88:91], v[204:207], v[40:43]\n"
"buffer_load_dwordx4 v[84:87], v1, s[4:7], s98 offen sc1\n"
"v_mfma_f32_16x16x32_bf16 v[40:43], a[92:95], v[88:91], v[40:43]\n"
"v_mfma_f32_16x16x32_bf16 v[140:143], a[124:127], v[88:91], 0\n"
"v_mfma_f32_16x16x32_bf16 v[140:143], a[120:123], v[204:207], v[140:143]\n"
"v_mfma_f32_16x16x32_bf16 v[140:143], a[116:119], v[152:155], v[140:143]\n"
"v_mfma_f32_16x16x32_bf16 v[140:143], a[112:115], v[80:83], v[140:143]\n"
"buffer_load_dwordx4 v[160:163], v1, s[4:7], s98 offen offset:1024 sc1\n"
"v_mfma_f32_16x16x32_bf16 v[140:143], a[108:111], v[180:183], v[140:143]\n"
"v_mfma_f32_16x16x32_bf16 v[140:143], a[104:107], v[92:95], v[140:143]\n"
"s_nop 0\n"
"ds_write_b128 v3, v[40:43] offset:8192\n"
"v_mfma_f32_16x16x32_bf16 v[140:143], a[100:103], v[52:55], v[140:143]\n"
"v_mfma_f32_16x16x32_bf16 v[140:143], a[96:99], v[156:159], v[140:143]\n"
"v_mfma_f32_16x16x32_bf16 v[40:43], v[24:27], v[156:159], 0\n"
"buffer_load_dwordx4 v[172:175], v1, s[4:7], s98 offen offset:2048 sc1\n"
"v_mfma_f32_16x16x32_bf16 v[40:43], v[44:47], v[52:55], v[40:43]\n"
"v_mfma_f32_16x16x32_bf16 v[40:43], v[28:31], v[92:95], v[40:43]\n"
"v_mfma_f32_16x16x32_bf16 v[40:43], v[56:59], v[180:183], v[40:43]\n"
"v_mfma_f32_16x16x32_bf16 v[40:43], v[32:35], v[80:83], v[40:43]\n"
"s_waitcnt vmcnt(12)\n"
"v_add_f32_e32 v255, v255, v253\n"
"v_add_f32_e32 v254, v254, v250\n"
"v_mfma_f32_16x16x32_bf16 v[40:43], v[116:119], v[152:155], v[40:43]\n"
"buffer_load_dwordx4 v[184:187], v1, s[4:7], s98 offen offset:3072 sc1\n"
"v_mfma_f32_16x16x32_bf16 v[40:43], v[48:51], v[204:207], v[40:43]\n"
"ds_write_b128 v3, v[140:143] offset:9216\n"
"v_mfma_f32_16x16x32_bf16 v[40:43], v[64:67], v[88:91], v[40:43]\n"
"v_add_f32_e32 v252, v252, v249\n"
"v_add_f32_e32 v251, v251, v248\n"
"v_mfma_f32_16x16x32_bf16 v[140:143], v[148:151], v[88:91], 0\n"
"buffer_load_dwordx4 a[180:183], v1, s[4:7], s97 offen offset:1024 sc1\n"
"v_mfma_f32_16x16x32_bf16 v[140:143], v[144:147], v[204:207], v[140:143]\n"
"v_add_f32_e32 v255, v255, v252\n"
"v_add_f32_e32 v254, v254, v251\n"
"v_mfma_f32_16x16x32_bf16 v[140:143], v[108:111], v[152:155], v[140:143]\n"
"v_add_f32_e32 v255, v255, v254\n"
"v_mov_b32_e32 v254, v255\n"
"v_mfma_f32_16x16x32_bf16 v[140:143], v[124:127], v[80:83], v[140:143]\n"
"s_nop 0\n"
"v_permlane32_swap_b32_e32 v255, v254\n"
"v_add_f32_e32 v255, v255, v254\n"
"v_mfma_f32_16x16x32_bf16 v[140:143], v[120:123], v[180:183], v[140:143]\n"
"v_mul_f32_e32 v255, 0x3a800000, v255\n"
"v_mov_b32_e32 v254, v255\n"
"v_mfma_f32_16x16x32_bf16 v[140:143], v[128:131], v[92:95], v[140:143]\n"
"ds_write_b128 v3, v[40:43] offset:10240\n"
"v_mfma_f32_16x16x32_bf16 v[140:143], v[36:39], v[52:55], v[140:143]\n"
"v_permlane16_swap_b32_e32 v255, v254\n"
"v_cvt_pk_bf16_f32 v253, -v255, 0\n"
"v_mfma_f32_16x16x32_bf16 v[140:143], v[112:115], v[156:159], v[140:143]\n"
"v_fma_f32 v254, -v255, v255, v254\n"
"v_lshlrev_b32_e32 v253, 16, v253\n"
"; @A G1 2.2\n"
"v_mfma_f32_16x16x32_bf16 v[40:43], a[0:3], v[96:99], 0\n"
"v_max_f32_e32 v254, 0, v254\n"
"v_cvt_pk_bf16_f32 v254, v254, 0\n"
"v_mfma_f32_16x16x32_bf16 v[40:43], a[4:7], v[104:107], v[40:43]\n"
"v_lshlrev_b32_e32 v254, 16, v254\n"
"v_add_f32_e32 v254, 0x3727c5ac, v254\n"
"v_mfma_f32_16x16x32_bf16 v[40:43], a[8:11], v[164:167], v[40:43]\n"
"v_cvt_pk_bf16_f32 v254, v254, 0\n"
"v_lshlrev_b32_e32 v254, 16, v254\n"
"v_mfma_f32_16x16x32_bf16 v[40:43], a[12:15], v[168:171], v[40:43]\n"
"s_waitcnt vmcnt(12)\n"
"v_sqrt_f32_e32 v254, v254\n"
"v_mfma_f32_16x16x32_bf16 v[40:43], a[16:19], v[132:135], v[40:43]\n"
"s_mov_b32 s98, .Lmk_a4-.Lmk_g91\n"
"s_getpc_b64 s[84:85]\n"
".Lmk_g91:\n"
"s_add_u32 s84, s84, s98\n"
"s_addc_u32 s85, s85, 0\n"
"s_and_b32 s84, s84, -16\n"
"v_mfma_f32_16x16x32_bf16 v[40:43], a[20:23], v[76:79], v[40:43]\n"
"ds_write_b128 v3, v[140:143] offset:11264\n"
"v_mfma_f32_16x16x32_bf16 v[40:43], a[24:27], v[136:139], v[40:43]\n"
"s_and_b32 s85, s85, 0xffff\n"
"s_sub_u32 s98, .Lmk_end-.Lmk_g91, s98\n"
"s_min_u32 s86, s98, 0x4000\n"
"s_mov_b32 s87, 0x20000\n"
"v_mfma_f32_16x16x32_bf16 v[40:43], a[28:31], v[176:179], v[40:43]\n"
"s_mov_b32 m0, 0x27800\n"
"s_nop 0\n"
"buffer_load_dwordx4 v0, s[84:87], s99 offen lds\n"
"v_mfma_f32_16x16x32_bf16 v[52:55], a[60:63], v[176:179], 0\n"
"v_cvt_pk_bf16_f32 v254, v254, 0\n"
"v_lshlrev_b32_e32 v254, 16, v254\n"
"v_mfma_f32_16x16x32_bf16 v[52:55], a[56:59], v[136:139], v[52:55]\n"
"v_rcp_f32_e32 v255, v254\n"
"v_mfma_f32_16x16x32_bf16 v[52:55], a[52:55], v[76:79], v[52:55]\n"
"s_add_u32 s98, s52, 0\n"
"buffer_load_dwordx4 v[80:83], v0, s[4:7], s98 offen sc1\n"
"v_mfma_f32_16x16x32_bf16 v[52:55], a[48:51], v[132:135], v[52:55]\n"
"s_waitcnt lgkmcnt(0)\n"
"s_barrier\n"
"v_mfma_f32_16x16x32_bf16 v[52:55], a[44:47], v[168:171], v[52:55]\n"
"buffer_load_dwordx4 a[184:187], v1, s[4:7], s97 offen offset:2048 sc1\n"
"v_mfma_f32_16x16x32_bf16 v[52:55], a[40:43], v[164:167], v[52:55]\n"
"ds_write_b128 v3, v[40:43] offset:12288\n"
"v_mfma_f32_16x16x32_bf16 v[52:55], a[36:39], v[104:107], v[52:55]\n"
"ds_read_b128 v[40:43], v4\n"
"v_mul_f32_e32 v254, 0x3e8293ee, v255\n"
"v_mfma_f32_16x16x32_bf16 v[52:55], a[32:35], v[96:99], v[52:55]\n"
"buffer_load_dwordx4 v[88:91], v0, s[4:7], s98 offen offset:1024 sc1\n"
"v_mfma_f32_16x16x32_bf16 v[92:95], a[64:67], v[96:99], 0\n"
"ds_read_b128 v[140:143], v9 offset:128\n"
"ds_read_b128 v[152:155], v5\n"
"v_mfma_f32_16x16x32_bf16 v[92:95], a[68:71], v[104:107], v[92:95]\n"
"v_mfma_f32_16x16x32_bf16 v[92:95], a[72:75], v[164:167], v[92:95]\n"
"v_mfma_f32_16x16x32_bf16 v[92:95], a[76:79], v[168:171], v[92:95]\n"
"ds_read_b128 v[156:159], v4 offset:1024\n"
"ds_read_b128 v[180:183], v9 offset:192\n"
"v_mfma_f32_16x16x32_bf16 v[92:95], a[80:83], v[132:135], v[92:95]\n"
"buffer_load_dwordx4 v[188:191], v0, s[4:7], s98 offen offset:2048 sc1\n"
"v_mfma_f32_16x16x32_bf16 v[92:95], a[84:87], v[76:79], v[92:95]\n"
"ds_write_b128 v3, v[52:55] offset:13312\n"
"v_mfma_f32_16x16x32_bf16 v[92:95], a[88:91], v[136:139], v[92:95]\n"
"ds_read_b128 v[52:55], v5 offset:1024\n"
"s_waitcnt lgkmcnt(4)\n"
"v_mfma_f32_16x16x32_bf16 v[92:95], a[92:95], v[176:179], v[92:95]\n"
"v_fmac_f32_e32 v40, v253, v140\n"
"v_fmac_f32_e32 v41, v253, v141\n"
"v_mfma_f32_16x16x32_bf16 v[192:195], a[124:127], v[176:179], 0\n"
"v_fmac_f32_e32 v42, v253, v142\n"
"v_fmac_f32_e32 v43, v253, v143\n"
"v_mfma_f32_16x16x32_bf16 v[192:195], a[120:123], v[136:139], v[192:195]\n"
"buffer_load_dwordx4 v[140:143], v0, s[4:7], s98 offen offset:3072 sc1\n"
"v_mfma_f32_16x16x32_bf16 v[192:195], a[116:119], v[76:79], v[192:195]\n"
"ds_read_b128 v[196:199], v4 offset:2048\n"
"ds_read_b128 v[200:203], v9 offset:256\n"
"v_mfma_f32_16x16x32_bf16 v[192:195], a[112:115], v[132:135], v[192:195]\n"
"ds_read_b128 v[204:207], v5 offset:2048\n"
"v_add_f32_e32 v40, v40, v152\n"
"v_mfma_f32_16x16x32_bf16 v[192:195], a[108:111], v[168:171], v[192:195]\n"
"v_add_f32_e32 v41, v41, v153\n"
"v_add_f32_e32 v42, v42, v154\n"
"v_mfma_f32_16x16x32_bf16 v[192:195], a[104:107], v[164:167], v[192:195]\n"
"ds_write_b128 v3, v[92:95] offset:14336\n"
"v_mfma_f32_16x16x32_bf16 v[192:195], a[100:103], v[104:107], v[192:195]\n"
"buffer_load_dwordx4 v[92:95], v1, s[4:7], s98 offen sc1\n"
"v_mfma_f32_16x16x32_bf16 v[192:195], a[96:99], v[96:99], v[192:195]\n"
"s_waitcnt lgkmcnt(4)\n"
"v_add_f32_e32 v43, v43, v155\n"
"v_fmac_f32_e32 v156, v253, v180\n"
"v_mfma_f32_16x16x32_bf16 v[152:155], v[24:27], v[96:99], 0\n"
"buffer_load_dwordx4 a[188:191], v1, s[4:7], s97 offen offset:3072 sc1\n"
"v_mfma_f32_16x16x32_bf16 v[152:155], v[44:47], v[104:107], v[152:155]\n"
"ds_read_b128 v[208:211], v4 offset:3072\n"
"ds_read_b128 v[212:215], v9 offset:320\n"
"v_mfma_f32_16x16x32_bf16 v[152:155], v[28:31], v[164:167], v[152:155]\n"
"s_waitcnt vmcnt(16)\n"
"s_add_u32 s97, s68, 0x1000\n"
"ds_read_b128 v[216:219], v5 offset:3072\n"
"v_mfma_f32_16x16x32_bf16 v[152:155], v[56:59], v[168:171], v[152:155]\n"
"buffer_load_dwordx4 v[220:223], v1, s[4:7], s98 offen offset:1024 sc1\n"
"v_mfma_f32_16x16x32_bf16 v[152:155], v[32:35], v[132:135], v[152:155]\n"
"buffer_store_dwordx2 v[18:19], v11, s[4:7], s97 offen offset:64 sc1\n"
"v_mfma_f32_16x16x32_bf16 v[152:155], v[116:119], v[76:79], v[152:155]\n"
"ds_write_b128 v3, v[192:195] offset:15360\n"
"v_mfma_f32_16x16x32_bf16 v[152:155], v[48:51], v[136:139], v[152:155]\n"
"v_fmac_f32_e32 v157, v253, v181\n"
"v_fmac_f32_e32 v158, v253, v182\n"
"v_mfma_f32_16x16x32_bf16 v[152:155], v[64:67], v[176:179], v[152:155]\n"
"v_fmac_f32_e32 v159, v253, v183\n"
"v_add_f32_e32 v156, v156, v52\n"
"v_mfma_f32_16x16x32_bf16 v[180:183], v[148:151], v[176:179], 0\n"
"buffer_load_dwordx4 v[176:179], v1, s[4:7], s98 offen offset:2048 sc1\n"
"v_mfma_f32_16x16x32_bf16 v[180:183], v[144:147], v[136:139], v[180:183]\n"
"ds_read_b128 v[136:139], v4 offset:4096\n"
"ds_read_b128 v[192:195], v9\n"
"v_mfma_f32_16x16x32_bf16 v[180:183], v[108:111], v[76:79], v[180:183]\n"
"ds_read_b128 v[76:79], v5 offset:4096\n"
"v_add_f32_e32 v157, v157, v53\n"
"v_mfma_f32_16x16x32_bf16 v[180:183], v[124:127], v[132:135], v[180:183]\n"
"s_waitcnt lgkmcnt(8)\n"
"v_add_f32_e32 v158, v158, v54\n"
"v_add_f32_e32 v159, v159, v55\n"
"v_mfma_f32_16x16x32_bf16 v[180:183], v[120:123], v[168:171], v[180:183]\n"
"v_fmac_f32_e32 v196, v253, v200\n"
"v_fmac_f32_e32 v197, v253, v201\n"
"v_mfma_f32_16x16x32_bf16 v[180:183], v[128:131], v[164:167], v[180:183]\n"
"buffer_load_dwordx4 v[52:55], v1, s[4:7], s98 offen offset:3072 sc1\n"
"v_mfma_f32_16x16x32_bf16 v[180:183], v[36:39], v[104:107], v[180:183]\n"
"ds_write_b128 v3, v[152:155] offset:16384\n"
"v_mfma_f32_16x16x32_bf16 v[180:183], v[112:115], v[96:99], v[180:183]\n"
"s_waitcnt vmcnt(18)\n"
"ds_read_b128 v[96:99], v4 offset:5120\n"
"ds_read_b128 v[104:107], v9 offset:64\n"
"; @A G1 2.3\n"
"v_mfma_f32_16x16x32_bf16 v[40:43], a[0:3], v[60:63], v[40:43]\n"
"ds_read_b128 v[132:135], v5 offset:5120\n"
"v_fmac_f32_e32 v198, v253, v202\n"
"v_mfma_f32_16x16x32_bf16 v[40:43], a[4:7], v[72:75], v[40:43]\n"
"v_fmac_f32_e32 v199, v253, v203\n"
"v_add_f32_e32 v196, v196, v204\n"
"v_mfma_f32_16x16x32_bf16 v[40:43], a[8:11], v[68:71], v[40:43]\n"
"s_add_u32 s98, s53, 0\n"
"buffer_load_dwordx4 v[152:155], v0, s[4:7], s98 offen sc1\n"
"v_mfma_f32_16x16x32_bf16 v[40:43], a[12:15], v[100:103], v[40:43]\n"
"s_waitcnt vmcnt(14)\n"
"s_add_u32 s97, s70, 0x4000\n"
"v_add_f32_e32 v197, v197, v205\n"
"v_mfma_f32_16x16x32_bf16 v[40:43], a[16:19], v[84:87], v[40:43]\n"
"buffer_load_dwordx4 a[192:195], v0, s[4:7], s97 offen sc1\n"
"v_mfma_f32_16x16x32_bf16 v[40:43], a[20:23], v[160:163], v[40:43]\n"
"ds_write_b128 v3, v[180:183] offset:17408\n"
"v_mfma_f32_16x16x32_bf16 v[40:43], a[24:27], v[172:175], v[40:43]\n"
"s_waitcnt lgkmcnt(9)\n"
"v_add_f32_e32 v198, v198, v206\n"
"v_add_f32_e32 v199, v199, v207\n"
"v_mfma_f32_16x16x32_bf16 v[40:43], a[28:31], v[184:187], v[40:43]\n"
"buffer_load_dwordx4 v[164:167], v0, s[4:7], s98 offen offset:1024 sc1\n"
"v_mfma_f32_16x16x32_bf16 v[156:159], a[60:63], v[184:187], v[156:159]\n"
"v_fmac_f32_e32 v208, v253, v212\n"
"v_fmac_f32_e32 v209, v253, v213\n"
"v_mfma_f32_16x16x32_bf16 v[156:159], a[56:59], v[172:175], v[156:159]\n"
"v_fmac_f32_e32 v210, v253, v214\n"
"v_fmac_f32_e32 v211, v253, v215\n"
"v_mfma_f32_16x16x32_bf16 v[156:159], a[52:55], v[160:163], v[156:159]\n"
"s_waitcnt lgkmcnt(0)\n"
"s_barrier\n"
"v_mfma_f32_16x16x32_bf16 v[156:159], a[48:51], v[84:87], v[156:159]\n"
"ds_read_b128 v[168:171], v6\n"
"ds_read_b128 v[180:183], v6 offset:1024\n"
"v_mfma_f32_16x16x32_bf16 v[156:159], a[44:47], v[100:103], v[156:159]\n"
"buffer_load_dwordx4 v[200:203], v0, s[4:7], s98 offen offset:2048 sc1\n"
"v_mfma_f32_16x16x32_bf16 v[156:159], a[40:43], v[68:71], v[156:159]\n"
"v_add_f32_e32 v208, v208, v216\n"
"v_add_f32_e32 v209, v209, v217\n"
"v_mfma_f32_16x16x32_bf16 v[156:159], a[36:39], v[72:75], v[156:159]\n"
"v_add_f32_e32 v210, v210, v218\n"
"v_add_f32_e32 v211, v211, v219\n"
"v_mfma_f32_16x16x32_bf16 v[156:159], a[32:35], v[60:63], v[156:159]\n"
"v_fmac_f32_e32 v136, v253, v192\n"
"v_fmac_f32_e32 v137, v253, v193\n"
"v_mfma_f32_16x16x32_bf16 v[196:199], a[64:67], v[60:63], v[196:199]\n"
"v_fmac_f32_e32 v138, v253, v194\n"
"v_fmac_f32_e32 v139, v253, v195\n"
"v_mfma_f32_16x16x32_bf16 v[196:199], a[68:71], v[72:75], v[196:199]\n"
"buffer_load_dwordx4 v[192:195], v0, s[4:7], s98 offen offset:3072 sc1\n"
"v_mfma_f32_16x16x32_bf16 v[196:199], a[72:75], v[68:71], v[196:199]\n"
"v_add_f32_e32 v136, v136, v76\n"
"v_add_f32_e32 v137, v137, v77\n"
"v_mfma_f32_16x16x32_bf16 v[196:199], a[76:79], v[100:103], v[196:199]\n"
"v_add_f32_e32 v138, v138, v78\n"
"v_add_f32_e32 v139, v139, v79\n"
"v_mfma_f32_16x16x32_bf16 v[196:199], a[80:83], v[84:87], v[196:199]\n"
"s_waitcnt lgkmcnt(0)\n"
"v_fmac_f32_e32 v96, v253, v104\n"
"v_fmac_f32_e32 v97, v253, v105\n"
"v_mfma_f32_16x16x32_bf16 v[196:199], a[84:87], v[160:163], v[196:199]\n"
"v_fmac_f32_e32 v98, v253, v106\n"
"v_fmac_f32_e32 v99, v253, v107\n"
"v_mfma_f32_16x16x32_bf16 v[196:199], a[88:91], v[172:175], v[196:199]\n"
"buffer_load_dwordx4 v[76:79], v1, s[4:7], s98 offen sc1\n"
"v_mfma_f32_16x16x32_bf16 v[196:199], a[92:95], v[184:187], v[196:199]\n"
"ds_read_b128 v[104:107], v6 offset:2048\n"
"ds_read_b128 v[204:207], v6 offset:3072\n"
"v_mfma_f32_16x16x32_bf16 v[208:211], a[124:127], v[184:187], v[208:211]\n"
"buffer_load_dwordx4 a[196:199], v0, s[4:7], s97 offen offset:1024 sc1\n"
"v_mfma_f32_16x16x32_bf16 v[208:211], a[120:123], v[172:175], v[208:211]\n"
"v_add_f32_e32 v96, v96, v132\n"
"v_add_f32_e32 v97, v97, v133\n"
"v_mfma_f32_16x16x32_bf16 v[208:211], a[116:119], v[160:163], v[208:211]\n"
"v_add_f32_e32 v98, v98, v134\n"
"v_add_f32_e32 v99, v99, v135\n"
"v_mfma_f32_16x16x32_bf16 v[208:211], a[112:115], v[84:87], v[208:211]\n"
"buffer_load_dwordx4 v[132:135], v1, s[4:7], s98 offen offset:1024 sc1\n"
"v_mfma_f32_16x16x32_bf16 v[208:211], a[108:111], v[100:103], v[208:211]\n"
"v_add_f32_e32 v40, v40, v168\n"
"v_add_f32_e32 v41, v41, v169\n"
"v_mfma_f32_16x16x32_bf16 v[208:211], a[104:107], v[68:71], v[208:211]\n"
"v_add_f32_e32 v42, v42, v170\n"
"v_add_f32_e32 v43, v43, v171\n"
"v_mfma_f32_16x16x32_bf16 v[208:211], a[100:103], v[72:75], v[208:211]\n"
"v_add_f32_e32 v156, v156, v180\n"
"v_add_f32_e32 v157, v157, v181\n"
"v_mfma_f32_16x16x32_bf16 v[208:211], a[96:99], v[60:63], v[208:211]\n"
"v_add_f32_e32 v158, v158, v182\n"
"v_add_f32_e32 v159, v159, v183\n"
"v_mfma_f32_16x16x32_bf16 v[136:139], v[24:27], v[60:63], v[136:139]\n"
"buffer_load_dwordx4 v[168:171], v1, s[4:7], s98 offen offset:2048 sc1\n"
"v_mfma_f32_16x16x32_bf16 v[136:139], v[44:47], v[72:75], v[136:139]\n"
"v_mul_f32_e32 v40, v40, v255\n"
"v_mul_f32_e32 v41, v41, v255\n"
"v_mfma_f32_16x16x32_bf16 v[136:139], v[28:31], v[68:71], v[136:139]\n"
"v_mul_f32_e32 v42, v42, v255\n"
"v_mul_f32_e32 v43, v43, v255\n"
"v_mfma_f32_16x16x32_bf16 v[136:139], v[56:59], v[100:103], v[136:139]\n"
"v_mul_f32_e32 v156, v156, v255\n"
"v_mul_f32_e32 v157, v157, v255\n"
"v_mfma_f32_16x16x32_bf16 v[136:139], v[32:35], v[84:87], v[136:139]\n"
"s_waitcnt lgkmcnt(0)\n"
"v_mul_f32_e32 v158, v158, v255\n"
"v_mul_f32_e32 v159, v159, v255\n"
"v_mfma_f32_16x16x32_bf16 v[136:139], v[116:119], v[160:163], v[136:139]\n"
"buffer_load_dwordx4 v[180:183], v1, s[4:7], s98 offen offset:3072 sc1\n"
"v_mfma_f32_16x16x32_bf16 v[136:139], v[48:51], v[172:175], v[136:139]\n"
"ds_read_b128 v[212:215], v6 offset:4096\n"
"ds_read_b128 v[216:219], v6 offset:5120\n"
"v_mfma_f32_16x16x32_bf16 v[136:139], v[64:67], v[184:187], v[136:139]\n"
"v_cvt_pk_bf16_f32 v224, v40, v41\n"
"v_cvt_pk_bf16_f32 v225, v42, v43\n"
"v_mfma_f32_16x16x32_bf16 v[96:99], v[148:151], v[184:187], v[96:99]\n"
"v_cvt_pk_bf16_f32 v226, v156, v157\n"
"v_cvt_pk_bf16_f32 v227, v158, v159\n"
"v_mfma_f32_16x16x32_bf16 v[96:99], v[144:147], v[172:175], v[96:99]\n"
"ds_write_b128 v8, v[224:227] offset:17408\n"
"v_mfma_f32_16x16x32_bf16 v[96:99], v[108:111], v[160:163], v[96:99]\n"
"v_add_f32_e32 v196, v196, v104\n"
"v_add_f32_e32 v197, v197, v105\n"
"v_mfma_f32_16x16x32_bf16 v[96:99], v[124:127], v[84:87], v[96:99]\n"
"v_add_f32_e32 v198, v198, v106\n"
"v_add_f32_e32 v199, v199, v107\n"
"v_mfma_f32_16x16x32_bf16 v[96:99], v[120:123], v[100:103], v[96:99]\n"
"v_add_f32_e32 v208, v208, v204\n"
"v_add_f32_e32 v209, v209, v205\n"
"v_mfma_f32_16x16x32_bf16 v[96:99], v[128:131], v[68:71], v[96:99]\n"
"v_add_f32_e32 v210, v210, v206\n"
"v_add_f32_e32 v211, v211, v207\n"
"v_mfma_f32_16x16x32_bf16 v[96:99], v[36:39], v[72:75], v[96:99]\n"
"v_mul_f32_e32 v196, v196, v255\n"
"v_mul_f32_e32 v197, v197, v255\n"
"v_mfma_f32_16x16x32_bf16 v[96:99], v[112:115], v[60:63], v[96:99]\n"
"v_mul_f32_e32 v198, v198, v255\n"
"v_mul_f32_e32 v199, v199, v255\n"
"v_mul_f32_e32 v208, v208, v255\n"
"v_mul_f32_e32 v209, v209, v255\n"
"v_mul_f32_e32 v210, v210, v255\n"
"v_mul_f32_e32 v211, v211, v255\n"
"v_cvt_pk_bf16_f32 v40, v196, v197\n"
"v_cvt_pk_bf16_f32 v41, v198, v199\n"
"v_cvt_pk_bf16_f32 v42, v208, v209\n"
"v_cvt_pk_bf16_f32 v43, v210, v211\n"
"ds_write_b128 v7, v[40:43] offset:50176\n"
".Lmk_a3:\n"
"s_waitcnt vmcnt(16)\n"
"; @A G1 3.0\n"
"v_mfma_f32_16x16x32_bf16 v[60:63], a[0:3], v[80:83], 0\n"
"v_mfma_f32_16x16x32_bf16 v[60:63], a[4:7], v[88:91], v[60:63]\n"
"s_add_u32 s98, s62, 0\n"
"v_mfma_f32_16x16x32_bf16 v[60:63], a[8:11], v[188:191], v[60:63]\n"
"s_add_u32 s96, s54, 0\n"
"buffer_load_dwordx4 v[68:71], v0, s[4:7], s96 offen sc1\n"
"v_mfma_f32_16x16x32_bf16 v[60:63], a[12:15], v[140:143], v[60:63]\n"
"s_waitcnt vmcnt(11)\n"
"v_mfma_f32_16x16x32_bf16 v[60:63], a[16:19], v[92:95], v[60:63]\n"
"buffer_load_dword v255, v10, s[4:7], s98 offen sc1\n"
"v_mfma_f32_16x16x32_bf16 v[60:63], a[20:23], v[220:223], v[60:63]\n"
"s_waitcnt lgkmcnt(2)\n"
"v_add_f32_e32 v136, v136, v212\n"
"v_add_f32_e32 v137, v137, v213\n"
"v_mfma_f32_16x16x32_bf16 v[60:63], a[24:27], v[176:179], v[60:63]\n"
"v_add_f32_e32 v138, v138, v214\n"
"v_add_f32_e32 v139, v139, v215\n"
"v_mfma_f32_16x16x32_bf16 v[60:63], a[28:31], v[52:55], v[60:63]\n"
"buffer_load_dwordx4 v[72:75], v0, s[4:7], s96 offen offset:1024 sc1\n"
"v_mfma_f32_16x16x32_bf16 v[84:87], a[60:63], v[52:55], 0\n"
"buffer_load_dwordx4 a[200:203], v0, s[4:7], s97 offen offset:2048 sc1\n"
"v_mfma_f32_16x16x32_bf16 v[84:87], a[56:59], v[176:179], v[84:87]\n"
"buffer_load_dword v253, v10, s[4:7], s98 offen offset:256 sc1\n"
"v_mfma_f32_16x16x32_bf16 v[84:87], a[52:55], v[220:223], v[84:87]\n"
"v_add_f32_e32 v96, v96, v216\n"
"v_add_f32_e32 v97, v97, v217\n"
"v_mfma_f32_16x16x32_bf16 v[84:87], a[48:51], v[92:95], v[84:87]\n"
"v_add_f32_e32 v98, v98, v218\n"
"v_add_f32_e32 v99, v99, v219\n"
"v_mfma_f32_16x16x32_bf16 v[84:87], a[44:47], v[140:143], v[84:87]\n"
"buffer_load_dwordx4 v[100:103], v0, s[4:7], s96 offen offset:2048 sc1\n"
"v_mfma_f32_16x16x32_bf16 v[84:87], a[40:43], v[188:191], v[84:87]\n"
"ds_write_b128 v3, v[60:63]\n"
"v_mfma_f32_16x16x32_bf16 v[84:87], a[36:39], v[88:91], v[84:87]\n"
"buffer_load_dword v252, v10, s[4:7], s98 offen offset:512 sc1\n"
"v_mfma_f32_16x16x32_bf16 v[84:87], a[32:35], v[80:83], v[84:87]\n"
"v_mul_f32_e32 v136, v136, v254\n"
"v_mul_f32_e32 v137, v137, v254\n"
"v_mfma_f32_16x16x32_bf16 v[60:63], a[64:67], v[80:83], 0\n"
"v_mul_f32_e32 v138, v138, v254\n"
"v_mul_f32_e32 v139, v139, v254\n"
"v_mfma_f32_16x16x32_bf16 v[60:63], a[68:71], v[88:91], v[60:63]\n"
"buffer_load_dwordx4 v[104:107], v0, s[4:7], s96 offen offset:3072 sc1\n"
"v_mfma_f32_16x16x32_bf16 v[60:63], a[72:75], v[188:191], v[60:63]\n"
"buffer_load_dword v251, v10, s[4:7], s98 offen offset:768 sc1\n"
"v_mfma_f32_16x16x32_bf16 v[60:63], a[76:79], v[140:143], v[60:63]\n"
"v_mul_f32_e32 v96, v96, v254\n"
"v_mul_f32_e32 v97, v97, v254\n"
"v_mfma_f32_16x16x32_bf16 v[60:63], a[80:83], v[92:95], v[60:63]\n"
"v_mul_f32_e32 v98, v98, v254\n"
"v_mul_f32_e32 v99, v99, v254\n"
"v_mfma_f32_16x16x32_bf16 v[60:63], a[84:87], v[220:223], v[60:63]\n"
"ds_write_b128 v3, v[84:87] offset:1024\n"
"v_mfma_f32_16x16x32_bf16 v[60:63], a[88:91], v[176:179], v[60:63]\n"
"buffer_load_dwordx4 v[84:87], v1, s[4:7], s96 offen sc1\n"
"v_mfma_f32_16x16x32_bf16 v[60:63], a[92:95], v[52:55], v[60:63]\n"
"buffer_load_dword v254, v10, s[4:7], s98 offen offset:1024 sc1\n"
"v_mfma_f32_16x16x32_bf16 v[156:159], a[124:127], v[52:55], 0\n"
"v_cvt_pk_bf16_f32 v160, v136, v137\n"
"v_cvt_pk_bf16_f32 v161, v138, v139\n"
"v_mfma_f32_16x16x32_bf16 v[156:159], a[120:123], v[176:179], v[156:159]\n"
"v_cvt_pk_bf16_f32 v162, v96, v97\n"
"v_cvt_pk_bf16_f32 v163, v98, v99\n"
"v_mfma_f32_16x16x32_bf16 v[156:159], a[116:119], v[220:223], v[156:159]\n"
"buffer_load_dwordx4 a[204:207], v0, s[4:7], s97 offen offset:3072 sc1\n"
"v_mfma_f32_16x16x32_bf16 v[156:159], a[112:115], v[92:95], v[156:159]\n"
"buffer_load_dwordx4 v[96:99], v1, s[4:7], s96 offen offset:1024 sc1\n"
"v_mfma_f32_16x16x32_bf16 v[156:159], a[108:111], v[140:143], v[156:159]\n"
"buffer_load_dword v250, v10, s[4:7], s98 offen offset:1280 sc1\n"
"v_mfma_f32_16x16x32_bf16 v[156:159], a[104:107], v[188:191], v[156:159]\n"
"ds_write_b128 v3, v[60:63] offset:2048\n"
"v_mfma_f32_16x16x32_bf16 v[156:159], a[100:103], v[88:91], v[156:159]\n"
"ds_write_b128 v2, v[160:163] offset:17408\n"
"v_mfma_f32_16x16x32_bf16 v[156:159], a[96:99], v[80:83], v[156:159]\n"
"buffer_load_dword v249, v10, s[4:7], s98 offen offset:1536 sc1\n"
"v_mfma_f32_16x16x32_bf16 v[60:63], v[24:27], v[80:83], 0\n"
"buffer_load_dwordx4 v[136:139], v1, s[4:7], s96 offen offset:2048 sc1\n"
"v_mfma_f32_16x16x32_bf16 v[60:63], v[44:47], v[88:91], v[60:63]\n"
"buffer_store_dwordx4 v[224:227], v8, s[4:7], s64 offen offset:1024 sc1\n"
"v_mfma_f32_16x16x32_bf16 v[60:63], v[28:31], v[188:191], v[60:63]\n"
"buffer_load_dword v248, v10, s[4:7], s98 offen offset:1792 sc1\n"
"v_mfma_f32_16x16x32_bf16 v[60:63], v[56:59], v[140:143], v[60:63]\n"
"v_mfma_f32_16x16x32_bf16 v[60:63], v[32:35], v[92:95], v[60:63]\n"
"v_mfma_f32_16x16x32_bf16 v[60:63], v[116:119], v[220:223], v[60:63]\n"
"buffer_load_dwordx4 v[160:163], v1, s[4:7], s96 offen offset:3072 sc1\n"
"v_mfma_f32_16x16x32_bf16 v[60:63], v[48:51], v[176:179], v[60:63]\n"
"ds_write_b128 v3, v[156:159] offset:3072\n"
"v_mfma_f32_16x16x32_bf16 v[60:63], v[64:67], v[52:55], v[60:63]\n"
"v_mfma_f32_16x16x32_bf16 v[156:159], v[148:151], v[52:55], 0\n"
"v_mfma_f32_16x16x32_bf16 v[156:159], v[144:147], v[176:179], v[156:159]\n"
"v_mfma_f32_16x16x32_bf16 v[156:159], v[108:111], v[220:223], v[156:159]\n"
"v_mfma_f32_16x16x32_bf16 v[156:159], v[124:127], v[92:95], v[156:159]\n"
"v_mfma_f32_16x16x32_bf16 v[156:159], v[120:123], v[140:143], v[156:159]\n"
"buffer_load_dwordx4 a[208:211], v1, s[4:7], s97 offen sc1\n"
"v_mfma_f32_16x16x32_bf16 v[156:159], v[128:131], v[188:191], v[156:159]\n"
"s_nop 0\n"
"ds_write_b128 v3, v[60:63] offset:4096\n"
"v_mfma_f32_16x16x32_bf16 v[156:159], v[36:39], v[88:91], v[156:159]\n"
"v_mfma_f32_16x16x32_bf16 v[156:159], v[112:115], v[80:83], v[156:159]\n"
"s_waitcnt vmcnt(25)\n"
"; @A G1 3.1\n"
"v_mfma_f32_16x16x32_bf16 v[52:55], a[0:3], v[152:155], 0\n"
"v_mfma_f32_16x16x32_bf16 v[52:55], a[4:7], v[164:167], v[52:55]\n"
"v_mfma_f32_16x16x32_bf16 v[52:55], a[8:11], v[200:203], v[52:55]\n"
"s_add_u32 s98, s55, 0\n"
"buffer_load_dwordx4 v[60:63], v0, s[4:7], s98 offen sc1\n"
"v_mfma_f32_16x16x32_bf16 v[52:55], a[12:15], v[192:195], v[52:55]\n"
"s_waitcnt vmcnt(21)\n"
"v_mfma_f32_16x16x32_bf16 v[52:55], a[16:19], v[76:79], v[52:55]\n"
"v_mfma_f32_16x16x32_bf16 v[52:55], a[20:23], v[132:135], v[52:55]\n"
"ds_write_b128 v3, v[156:159] offset:5120\n"
"v_mfma_f32_16x16x32_bf16 v[52:55], a[24:27], v[168:171], v[52:55]\n"
"v_mfma_f32_16x16x32_bf16 v[52:55], a[28:31], v[180:183], v[52:55]\n"
"buffer_load_dwordx4 v[80:83], v0, s[4:7], s98 offen offset:1024 sc1\n"
"s_add_u32 s96, s64, 0x4000\n"
"v_mfma_f32_16x16x32_bf16 v[88:91], a[60:63], v[180:183], 0\n"
"v_mfma_f32_16x16x32_bf16 v[88:91], a[56:59], v[168:171], v[88:91]\n"
"buffer_store_dwordx4 v[40:43], v7, s[4:7], s96 offen offset:1024 sc1\n"
"v_mfma_f32_16x16x32_bf16 v[88:91], a[52:55], v[132:135], v[88:91]\n"
"v_mfma_f32_16x16x32_bf16 v[88:91], a[48:51], v[76:79], v[88:91]\n"
"v_mfma_f32_16x16x32_bf16 v[88:91], a[44:47], v[192:195], v[88:91]\n"
"buffer_load_dwordx4 v[40:43], v0, s[4:7], s98 offen offset:2048 sc1\n"
"v_mfma_f32_16x16x32_bf16 v[88:91], a[40:43], v[200:203], v[88:91]\n"
"ds_write_b128 v3, v[52:55] offset:6144\n"
"v_mfma_f32_16x16x32_bf16 v[88:91], a[36:39], v[164:167], v[88:91]\n"
"buffer_load_dwordx4 a[212:215], v1, s[4:7], s97 offen offset:1024 sc1\n"
"v_mfma_f32_16x16x32_bf16 v[88:91], a[32:35], v[152:155], v[88:91]\n"
"v_mfma_f32_16x16x32_bf16 v[52:55], a[64:67], v[152:155], 0\n"
"v_mfma_f32_16x16x32_bf16 v[52:55], a[68:71], v[164:167], v[52:55]\n"
"buffer_load_dwordx4 v[92:95], v0, s[4:7], s98 offen offset:3072 sc1\n"
"v_mfma_f32_16x16x32_bf16 v[52:55], a[72:75], v[200:203], v[52:55]\n"
"v_mfma_f32_16x16x32_bf16 v[52:55], a[76:79], v[192:195], v[52:55]\n"
"v_mfma_f32_16x16x32_bf16 v[52:55], a[80:83], v[76:79], v[52:55]\n"
"v_mfma_f32_16x16x32_bf16 v[52:55], a[84:87], v[132:135], v[52:55]\n"
"s_nop 0\n"
"ds_write_b128 v3, v[88:91] offset:7168\n"
"v_mfma_f32_16x16x32_bf16 v[52:55], a[88:91], v[168:171], v[52:55]\n"
"buffer_load_dwordx4 v[88:91], v1, s[4:7], s98 offen sc1\n"
"v_mfma_f32_16x16x32_bf16 v[52:55], a[92:95], v[180:183], v[52:55]\n"
"v_mfma_f32_16x16x32_bf16 v[140:143], a[124:127], v[180:183], 0\n"
"v_mfma_f32_16x16x32_bf16 v[140:143], a[120:123], v[168:171], v[140:143]\n"
"v_mfma_f32_16x16x32_bf16 v[140:143], a[116:119], v[132:135], v[140:143]\n"
"v_mfma_f32_16x16x32_bf16 v[140:143], a[112:115], v[76:79], v[140:143]\n"
"buffer_load_dwordx4 v[156:159], v1, s[4:7], s98 offen offset:1024 sc1\n"
"v_mfma_f32_16x16x32_bf16 v[140:143], a[108:111], v[192:195], v[140:143]\n"
"v_mfma_f32_16x16x32_bf16 v[140:143], a[104:107], v[200:203], v[140:143]\n"
"s_nop 0\n"
"ds_write_b128 v3, v[52:55] offset:8192\n"
"v_mfma_f32_16x16x32_bf16 v[140:143], a[100:103], v[164:167], v[140:143]\n"
"v_mfma_f32_16x16x32_bf16 v[140:143], a[96:99], v[152:155], v[140:143]\n"
"v_mfma_f32_16x16x32_bf16 v[52:55], v[24:27], v[152:155], 0\n"
"buffer_load_dwordx4 v[172:175], v1, s[4:7], s98 offen offset:2048 sc1\n"
"v_mfma_f32_16x16x32_bf16 v[52:55], v[44:47], v[164:167], v[52:55]\n"
"buffer_load_dwordx4 a[216:219], v1, s[4:7], s97 offen offset:2048 sc1\n"
"v_mfma_f32_16x16x32_bf16 v[52:55], v[28:31], v[200:203], v[52:55]\n"
"v_mfma_f32_16x16x32_bf16 v[52:55], v[56:59], v[192:195], v[52:55]\n"
"v_mfma_f32_16x16x32_bf16 v[52:55], v[32:35], v[76:79], v[52:55]\n"
"s_waitcnt vmcnt(12)\n"
"v_add_f32_e32 v255, v255, v254\n"
"v_add_f32_e32 v253, v253, v250\n"
"v_mfma_f32_16x16x32_bf16 v[52:55], v[116:119], v[132:135], v[52:55]\n"
"buffer_load_dwordx4 v[176:179], v1, s[4:7], s98 offen offset:3072 sc1\n"
"v_mfma_f32_16x16x32_bf16 v[52:55], v[48:51], v[168:171], v[52:55]\n"
"ds_write_b128 v3, v[140:143] offset:9216\n"
"v_mfma_f32_16x16x32_bf16 v[52:55], v[64:67], v[180:183], v[52:55]\n"
"v_add_f32_e32 v252, v252, v249\n"
"v_add_f32_e32 v251, v251, v248\n"
"v_mfma_f32_16x16x32_bf16 v[140:143], v[148:151], v[180:183], 0\n"
"v_add_f32_e32 v255, v255, v252\n"
"v_add_f32_e32 v253, v253, v251\n"
"v_mfma_f32_16x16x32_bf16 v[140:143], v[144:147], v[168:171], v[140:143]\n"
"v_add_f32_e32 v255, v255, v253\n"
"v_mov_b32_e32 v253, v255\n"
"v_mfma_f32_16x16x32_bf16 v[140:143], v[108:111], v[132:135], v[140:143]\n"
"s_add_u32 s98, s71, 0\n"
"buffer_load_dwordx2 v[250:251], off, s[4:7], s98 sc1\n"
"v_mfma_f32_16x16x32_bf16 v[140:143], v[124:127], v[76:79], v[140:143]\n"
"v_permlane32_swap_b32_e32 v255, v253\n"
"v_add_f32_e32 v255, v255, v253\n"
"v_mfma_f32_16x16x32_bf16 v[140:143], v[120:123], v[192:195], v[140:143]\n"
"v_mul_f32_e32 v255, 0x3a800000, v255\n"
"v_mov_b32_e32 v253, v255\n"
"v_mfma_f32_16x16x32_bf16 v[140:143], v[128:131], v[200:203], v[140:143]\n"
"ds_write_b128 v3, v[52:55] offset:10240\n"
"v_mfma_f32_16x16x32_bf16 v[140:143], v[36:39], v[164:167], v[140:143]\n"
"buffer_store_dwordx2 v[18:19], v11, s[4:7], s68 offen offset:32 sc1\n"
"v_mfma_f32_16x16x32_bf16 v[140:143], v[112:115], v[152:155], v[140:143]\n"
"v_permlane16_swap_b32_e32 v255, v253\n"
"v_cvt_pk_bf16_f32 v254, -v255, 0\n"
"; @A G1 3.2\n"
"v_mfma_f32_16x16x32_bf16 v[52:55], a[0:3], v[68:71], 0\n"
"v_fma_f32 v253, -v255, v255, v253\n"
"v_lshlrev_b32_e32 v254, 16, v254\n"
"v_mfma_f32_16x16x32_bf16 v[52:55], a[4:7], v[72:75], v[52:55]\n"
"v_max_f32_e32 v253, 0, v253\n"
"v_cvt_pk_bf16_f32 v253, v253, 0\n"
"v_mfma_f32_16x16x32_bf16 v[52:55], a[8:11], v[100:103], v[52:55]\n"
"buffer_load_dwordx4 a[220:223], v1, s[4:7], s97 offen offset:3072 sc1\n"
"v_mfma_f32_16x16x32_bf16 v[52:55], a[12:15], v[104:107], v[52:55]\n"
"s_waitcnt vmcnt(15)\n"
"v_lshlrev_b32_e32 v253, 16, v253\n"
"v_add_f32_e32 v253, 0x3727c5ac, v253\n"
"v_mfma_f32_16x16x32_bf16 v[52:55], a[16:19], v[84:87], v[52:55]\n"
"s_add_u32 s97, s70, 0x6000\n"
"v_cvt_pk_bf16_f32 v253, v253, 0\n"
"v_lshlrev_b32_e32 v253, 16, v253\n"
"v_mfma_f32_16x16x32_bf16 v[52:55], a[20:23], v[96:99], v[52:55]\n"
"ds_write_b128 v3, v[140:143] offset:11264\n"
"v_mfma_f32_16x16x32_bf16 v[52:55], a[24:27], v[136:139], v[52:55]\n"
"buffer_load_dwordx4 a[224:227], v0, s[4:7], s97 offen sc1\n"
"v_mfma_f32_16x16x32_bf16 v[52:55], a[28:31], v[160:163], v[52:55]\n"
"v_sqrt_f32_e32 v253, v253\n"
"v_mfma_f32_16x16x32_bf16 v[76:79], a[60:63], v[160:163], 0\n"
"v_cvt_pk_bf16_f32 v253, v253, 0\n"
"v_lshlrev_b32_e32 v253, 16, v253\n"
"v_mfma_f32_16x16x32_bf16 v[76:79], a[56:59], v[136:139], v[76:79]\n"
"v_rcp_f32_e32 v255, v253\n"
"v_mfma_f32_16x16x32_bf16 v[76:79], a[52:55], v[96:99], v[76:79]\n"
"s_waitcnt lgkmcnt(0)\n"
"s_barrier\n"
"v_mfma_f32_16x16x32_bf16 v[76:79], a[48:51], v[84:87], v[76:79]\n"
"buffer_load_dwordx4 a[228:231], v0, s[4:7], s97 offen offset:1024 sc1\n"
"v_mfma_f32_16x16x32_bf16 v[76:79], a[44:47], v[104:107], v[76:79]\n"
"ds_read_b128 v[132:135], v4\n"
"v_mul_f32_e32 v253, 0x3e8293ee, v255\n"
"v_mfma_f32_16x16x32_bf16 v[76:79], a[40:43], v[100:103], v[76:79]\n"
"ds_write_b128 v3, v[52:55] offset:12288\n"
"v_mfma_f32_16x16x32_bf16 v[76:79], a[36:39], v[72:75], v[76:79]\n"
"ds_read_b128 v[52:55], v9 offset:128\n"
"ds_read_b128 v[140:143], v5\n"
"v_mfma_f32_16x16x32_bf16 v[76:79], a[32:35], v[68:71], v[76:79]\n"
"buffer_load_dwordx4 a[232:235], v0, s[4:7], s97 offen offset:2048 sc1\n"
"v_mfma_f32_16x16x32_bf16 v[152:155], a[64:67], v[68:71], 0\n"
"v_mfma_f32_16x16x32_bf16 v[152:155], a[68:71], v[72:75], v[152:155]\n"
"v_mfma_f32_16x16x32_bf16 v[152:155], a[72:75], v[100:103], v[152:155]\n"
"ds_read_b128 v[164:167], v4 offset:1024\n"
"ds_read_b128 v[168:171], v9 offset:192\n"
"v_mfma_f32_16x16x32_bf16 v[152:155], a[76:79], v[104:107], v[152:155]\n"
"buffer_load_dwordx4 a[236:239], v0, s[4:7], s97 offen offset:3072 sc1\n"
"v_mfma_f32_16x16x32_bf16 v[152:155], a[80:83], v[84:87], v[152:155]\n"
"ds_read_b128 v[180:183], v5 offset:1024\n"
"s_waitcnt lgkmcnt(3)\n"
"v_mfma_f32_16x16x32_bf16 v[152:155], a[84:87], v[96:99], v[152:155]\n"
"ds_write_b128 v3, v[76:79] offset:13312\n"
"v_mfma_f32_16x16x32_bf16 v[152:155], a[88:91], v[136:139], v[152:155]\n"
"v_fmac_f32_e32 v132, v254, v52\n"
"v_fmac_f32_e32 v133, v254, v53\n"
"v_mfma_f32_16x16x32_bf16 v[152:155], a[92:95], v[160:163], v[152:155]\n"
"buffer_load_dwordx4 a[240:243], v1, s[4:7], s97 offen sc1\n"
"v_mfma_f32_16x16x32_bf16 v[76:79], a[124:127], v[160:163], 0\n"
"v_fmac_f32_e32 v134, v254, v54\n"
"v_fmac_f32_e32 v135, v254, v55\n"
"v_mfma_f32_16x16x32_bf16 v[76:79], a[120:123], v[136:139], v[76:79]\n"
"ds_read_b128 v[52:55], v4 offset:2048\n"
"ds_read_b128 v[184:187], v9 offset:256\n"
"v_mfma_f32_16x16x32_bf16 v[76:79], a[116:119], v[96:99], v[76:79]\n"
"ds_read_b128 v[188:191], v5 offset:2048\n"
"v_add_f32_e32 v132, v132, v140\n"
"v_mfma_f32_16x16x32_bf16 v[76:79], a[112:115], v[84:87], v[76:79]\n"
"buffer_load_dwordx4 a[244:247], v1, s[4:7], s97 offen offset:1024 sc1\n"
"v_mfma_f32_16x16x32_bf16 v[76:79], a[108:111], v[104:107], v[76:79]\n"
"v_add_f32_e32 v133, v133, v141\n"
"v_add_f32_e32 v134, v134, v142\n"
"v_mfma_f32_16x16x32_bf16 v[76:79], a[104:107], v[100:103], v[76:79]\n"
"ds_write_b128 v3, v[152:155] offset:14336\n"
"v_mfma_f32_16x16x32_bf16 v[76:79], a[100:103], v[72:75], v[76:79]\n"
"s_waitcnt lgkmcnt(5)\n"
"v_add_f32_e32 v135, v135, v143\n"
"v_fmac_f32_e32 v164, v254, v168\n"
"v_mfma_f32_16x16x32_bf16 v[76:79], a[96:99], v[68:71], v[76:79]\n"
"buffer_load_dwordx4 a[248:251], v1, s[4:7], s97 offen offset:2048 sc1\n"
"v_mfma_f32_16x16x32_bf16 v[140:143], v[24:27], v[68:71], 0\n"
"ds_read_b128 v[152:155], v4 offset:3072\n"
"ds_read_b128 v[192:195], v9 offset:320\n"
"v_mfma_f32_16x16x32_bf16 v[140:143], v[44:47], v[72:75], v[140:143]\n"
"ds_read_b128 v[196:199], v5 offset:3072\n"
"v_fmac_f32_e32 v165, v254, v169\n"
"v_mfma_f32_16x16x32_bf16 v[140:143], v[28:31], v[100:103], v[140:143]\n"
"v_fmac_f32_e32 v166, v254, v170\n"
"v_fmac_f32_e32 v167, v254, v171\n"
"v_mfma_f32_16x16x32_bf16 v[140:143], v[56:59], v[104:107], v[140:143]\n"
"buffer_load_dwordx4 a[252:255], v1, s[4:7], s97 offen offset:3072 sc1\n"
"v_mfma_f32_16x16x32_bf16 v[140:143], v[32:35], v[84:87], v[140:143]\n"
"v_add_f32_e32 v164, v164, v180\n"
"v_add_f32_e32 v165, v165, v181\n"
"v_mfma_f32_16x16x32_bf16 v[140:143], v[116:119], v[96:99], v[140:143]\n"
"ds_write_b128 v3, v[76:79] offset:15360\n"
"v_mfma_f32_16x16x32_bf16 v[140:143], v[48:51], v[136:139], v[140:143]\n"
"s_waitcnt lgkmcnt(5)\n"
"v_add_f32_e32 v166, v166, v182\n"
"v_add_f32_e32 v167, v167, v183\n"
"v_mfma_f32_16x16x32_bf16 v[140:143], v[64:67], v[160:163], v[140:143]\n"
"ds_read_b128 v[76:79], v4 offset:4096\n"
"ds_read_b128 v[168:171], v9\n"
"v_mfma_f32_16x16x32_bf16 v[180:183], v[148:151], v[160:163], 0\n"
"ds_read_b128 v[160:163], v5 offset:4096\n"
"v_fmac_f32_e32 v52, v254, v184\n"
"v_mfma_f32_16x16x32_bf16 v[180:183], v[144:147], v[136:139], v[180:183]\n"
"v_fmac_f32_e32 v53, v254, v185\n"
"v_fmac_f32_e32 v54, v254, v186\n"
"v_mfma_f32_16x16x32_bf16 v[180:183], v[108:111], v[96:99], v[180:183]\n"
"v_fmac_f32_e32 v55, v254, v187\n"
"v_add_f32_e32 v52, v52, v188\n"
"v_mfma_f32_16x16x32_bf16 v[180:183], v[124:127], v[84:87], v[180:183]\n"
"v_add_f32_e32 v53, v53, v189\n"
"v_add_f32_e32 v54, v54, v190\n"
"v_mfma_f32_16x16x32_bf16 v[180:183], v[120:123], v[104:107], v[180:183]\n"
"s_waitcnt vmcnt(10)\n"
"v_cmp_ne_u64_e32 vcc, v[250:251], v[18:19]\n"
"v_mfma_f32_16x16x32_bf16 v[180:183], v[128:131], v[100:103], v[180:183]\n"
"ds_write_b128 v3, v[140:143] offset:16384\n"
"v_mfma_f32_16x16x32_bf16 v[180:183], v[36:39], v[72:75], v[180:183]\n"
"s_cbranch_vccnz .Lmk_f78s\n"
".Lmk_f78r:\n"
"s_add_u32 m0, s77, 0x6000\n"
"v_mfma_f32_16x16x32_bf16 v[180:183], v[112:115], v[68:71], v[180:183]\n"
"s_add_u32 s98, s65, 0x2000\n"
"ds_read_b128 v[68:71], v4 offset:5120\n"
"; @A G1 3.3\n"
"v_mfma_f32_16x16x32_bf16 v[132:135], a[0:3], v[60:63], v[132:135]\n"
"buffer_load_dwordx4 v2, s[4:7], s98 offen sc1 lds\n"
"v_mfma_f32_16x16x32_bf16 v[132:135], a[4:7], v[80:83], v[132:135]\n"
"buffer_load_dwordx4 v2, s[4:7], s98 offen offset:1024 sc1 lds\n"
"v_mfma_f32_16x16x32_bf16 v[132:135], a[8:11], v[40:43], v[132:135]\n"
"ds_read_b128 v[72:75], v9 offset:64\n"
"ds_read_b128 v[84:87], v5 offset:5120\n"
"v_mfma_f32_16x16x32_bf16 v[132:135], a[12:15], v[92:95], v[132:135]\n"
"s_waitcnt lgkmcnt(8)\n"
"v_add_f32_e32 v55, v55, v191\n"
"v_mfma_f32_16x16x32_bf16 v[132:135], a[16:19], v[88:91], v[132:135]\n"
"v_fmac_f32_e32 v152, v254, v192\n"
"v_fmac_f32_e32 v153, v254, v193\n"
"v_mfma_f32_16x16x32_bf16 v[132:135], a[20:23], v[156:159], v[132:135]\n"
"ds_write_b128 v3, v[180:183] offset:17408\n"
"v_mfma_f32_16x16x32_bf16 v[132:135], a[24:27], v[172:175], v[132:135]\n"
"v_fmac_f32_e32 v154, v254, v194\n"
"v_fmac_f32_e32 v155, v254, v195\n"
"v_mfma_f32_16x16x32_bf16 v[132:135], a[28:31], v[176:179], v[132:135]\n"
"s_add_u32 m0, s77, 0xe000\n"
"s_add_u32 s98, s65, 0x6000\n"
"v_add_f32_e32 v152, v152, v196\n"
"v_mfma_f32_16x16x32_bf16 v[164:167], a[60:63], v[176:179], v[164:167]\n"
"buffer_load_dwordx4 v2, s[4:7], s98 offen sc1 lds\n"
"v_mfma_f32_16x16x32_bf16 v[164:167], a[56:59], v[172:175], v[164:167]\n"
"buffer_load_dwordx4 v2, s[4:7], s98 offen offset:1024 sc1 lds\n"
"v_mfma_f32_16x16x32_bf16 v[164:167], a[52:55], v[156:159], v[164:167]\n"
"s_waitcnt lgkmcnt(0)\n"
"s_barrier\n"
"v_mfma_f32_16x16x32_bf16 v[164:167], a[48:51], v[88:91], v[164:167]\n"
"ds_read_b128 v[96:99], v6\n"
"ds_read_b128 v[100:103], v6 offset:1024\n"
"v_mfma_f32_16x16x32_bf16 v[164:167], a[44:47], v[92:95], v[164:167]\n"
"s_add_u32 s98, s69, 0x600000\n"
"buffer_load_dwordx4 a[0:3], v0, s[4:7], s98 offen sc1\n"
"v_mfma_f32_16x16x32_bf16 v[164:167], a[40:43], v[40:43], v[164:167]\n"
"v_add_f32_e32 v153, v153, v197\n"
"v_add_f32_e32 v154, v154, v198\n"
"v_mfma_f32_16x16x32_bf16 v[164:167], a[36:39], v[80:83], v[164:167]\n"
"v_add_f32_e32 v155, v155, v199\n"
"v_fmac_f32_e32 v76, v254, v168\n"
"v_mfma_f32_16x16x32_bf16 v[164:167], a[32:35], v[60:63], v[164:167]\n"
"buffer_load_dwordx4 a[4:7], v0, s[4:7], s98 offen offset:1024 sc1\n"
"v_mfma_f32_16x16x32_bf16 v[52:55], a[64:67], v[60:63], v[52:55]\n"
"v_fmac_f32_e32 v77, v254, v169\n"
"v_fmac_f32_e32 v78, v254, v170\n"
"v_mfma_f32_16x16x32_bf16 v[52:55], a[68:71], v[80:83], v[52:55]\n"
"v_fmac_f32_e32 v79, v254, v171\n"
"v_add_f32_e32 v76, v76, v160\n"
"v_mfma_f32_16x16x32_bf16 v[52:55], a[72:75], v[40:43], v[52:55]\n"
"buffer_load_dwordx4 a[8:11], v0, s[4:7], s98 offen offset:2048 sc1\n"
"v_mfma_f32_16x16x32_bf16 v[52:55], a[76:79], v[92:95], v[52:55]\n"
"v_add_f32_e32 v77, v77, v161\n"
"v_add_f32_e32 v78, v78, v162\n"
"v_mfma_f32_16x16x32_bf16 v[52:55], a[80:83], v[88:91], v[52:55]\n"
"v_add_f32_e32 v79, v79, v163\n"
"v_fmac_f32_e32 v68, v254, v72\n"
"v_mfma_f32_16x16x32_bf16 v[52:55], a[84:87], v[156:159], v[52:55]\n"
"buffer_load_dwordx4 a[12:15], v0, s[4:7], s98 offen offset:3072 sc1\n"
"v_mfma_f32_16x16x32_bf16 v[52:55], a[88:91], v[172:175], v[52:55]\n"
"ds_read_b128 v[104:107], v6 offset:2048\n"
"ds_read_b128 v[136:139], v6 offset:3072\n"
"v_mfma_f32_16x16x32_bf16 v[52:55], a[92:95], v[176:179], v[52:55]\n"
"s_waitcnt lgkmcnt(2)\n"
"v_fmac_f32_e32 v69, v254, v73\n"
"v_fmac_f32_e32 v70, v254, v74\n"
"v_mfma_f32_16x16x32_bf16 v[152:155], a[124:127], v[176:179], v[152:155]\n"
"buffer_load_dwordx4 a[16:19], v1, s[4:7], s98 offen sc1\n"
"v_mfma_f32_16x16x32_bf16 v[152:155], a[120:123], v[172:175], v[152:155]\n"
"v_fmac_f32_e32 v71, v254, v75\n"
"v_add_f32_e32 v68, v68, v84\n"
"v_mfma_f32_16x16x32_bf16 v[152:155], a[116:119], v[156:159], v[152:155]\n"
"v_add_f32_e32 v69, v69, v85\n"
"v_add_f32_e32 v70, v70, v86\n"
"v_mfma_f32_16x16x32_bf16 v[152:155], a[112:115], v[88:91], v[152:155]\n"
"buffer_load_dwordx4 a[20:23], v1, s[4:7], s98 offen offset:1024 sc1\n"
"v_mfma_f32_16x16x32_bf16 v[152:155], a[108:111], v[92:95], v[152:155]\n"
"v_add_f32_e32 v71, v71, v87\n"
"v_add_f32_e32 v132, v132, v96\n"
"v_mfma_f32_16x16x32_bf16 v[152:155], a[104:107], v[40:43], v[152:155]\n"
"v_add_f32_e32 v133, v133, v97\n"
"v_add_f32_e32 v134, v134, v98\n"
"v_mfma_f32_16x16x32_bf16 v[152:155], a[100:103], v[80:83], v[152:155]\n"
"buffer_load_dwordx4 a[24:27], v1, s[4:7], s98 offen offset:2048 sc1\n"
"v_mfma_f32_16x16x32_bf16 v[152:155], a[96:99], v[60:63], v[152:155]\n"
"v_add_f32_e32 v135, v135, v99\n"
"v_add_f32_e32 v164, v164, v100\n"
"v_mfma_f32_16x16x32_bf16 v[76:79], v[24:27], v[60:63], v[76:79]\n"
"v_add_f32_e32 v165, v165, v101\n"
"v_add_f32_e32 v166, v166, v102\n"
"v_mfma_f32_16x16x32_bf16 v[76:79], v[44:47], v[80:83], v[76:79]\n"
"buffer_load_dwordx4 a[28:31], v1, s[4:7], s98 offen offset:3072 sc1\n"
"v_mfma_f32_16x16x32_bf16 v[76:79], v[28:31], v[40:43], v[76:79]\n"
"v_add_f32_e32 v167, v167, v103\n"
"v_mul_f32_e32 v132, v132, v255\n"
"v_mfma_f32_16x16x32_bf16 v[76:79], v[56:59], v[92:95], v[76:79]\n"
"v_mul_f32_e32 v133, v133, v255\n"
"v_mul_f32_e32 v134, v134, v255\n"
"v_mfma_f32_16x16x32_bf16 v[76:79], v[32:35], v[88:91], v[76:79]\n"
"s_waitcnt lgkmcnt(0)\n"
"v_mul_f32_e32 v135, v135, v255\n"
"v_mul_f32_e32 v164, v164, v255\n"
"v_mfma_f32_16x16x32_bf16 v[76:79], v[116:119], v[156:159], v[76:79]\n"
"v_mul_f32_e32 v165, v165, v255\n"
"v_mul_f32_e32 v166, v166, v255\n"
"v_mfma_f32_16x16x32_bf16 v[76:79], v[48:51], v[172:175], v[76:79]\n"
"ds_read_b128 v[24:27], v6 offset:4096\n"
"ds_read_b128 v[28:31], v6 offset:5120\n"
"v_mfma_f32_16x16x32_bf16 v[76:79], v[64:67], v[176:179], v[76:79]\n"
"v_mul_f32_e32 v167, v167, v255\n"
"v_cvt_pk_bf16_f32 v32, v132, v133\n"
"v_mfma_f32_16x16x32_bf16 v[68:71], v[148:151], v[176:179], v[68:71]\n"
"v_cvt_pk_bf16_f32 v33, v134, v135\n"
"v_cvt_pk_bf16_f32 v34, v164, v165\n"
"v_mfma_f32_16x16x32_bf16 v[68:71], v[144:147], v[172:175], v[68:71]\n"
"v_cvt_pk_bf16_f32 v35, v166, v167\n"
"v_add_f32_e32 v52, v52, v104\n"
"v_mfma_f32_16x16x32_bf16 v[68:71], v[108:111], v[156:159], v[68:71]\n"
"ds_write_b128 v7, v[32:35] offset:16384\n"
"v_mfma_f32_16x16x32_bf16 v[68:71], v[124:127], v[88:91], v[68:71]\n"
"buffer_store_dwordx4 v[32:35], v7, s[4:7], s64 offen sc1\n"
"v_mfma_f32_16x16x32_bf16 v[68:71], v[120:123], v[92:95], v[68:71]\n"
"v_add_f32_e32 v53, v53, v105\n"
"v_add_f32_e32 v54, v54, v106\n"
"v_mfma_f32_16x16x32_bf16 v[68:71], v[128:131], v[40:43], v[68:71]\n"
"v_add_f32_e32 v55, v55, v107\n"
"v_add_f32_e32 v152, v152, v136\n"
"v_mfma_f32_16x16x32_bf16 v[68:71], v[36:39], v[80:83], v[68:71]\n"
"v_add_f32_e32 v153, v153, v137\n"
"v_add_f32_e32 v154, v154, v138\n"
"v_mfma_f32_16x16x32_bf16 v[68:71], v[112:115], v[60:63], v[68:71]\n"
"v_add_f32_e32 v155, v155, v139\n"
"v_mul_f32_e32 v52, v52, v255\n"
"v_mul_f32_e32 v53, v53, v255\n"
"v_mul_f32_e32 v54, v54, v255\n"
"v_mul_f32_e32 v55, v55, v255\n"
"v_mul_f32_e32 v152, v152, v255\n"
"v_mul_f32_e32 v153, v153, v255\n"
"v_mul_f32_e32 v154, v154, v255\n"
"v_mul_f32_e32 v155, v155, v255\n"
"v_cvt_pk_bf16_f32 v32, v52, v53\n"
"v_cvt_pk_bf16_f32 v33, v54, v55\n"
"v_cvt_pk_bf16_f32 v34, v152, v153\n"
"v_cvt_pk_bf16_f32 v35, v154, v155\n"
"v_xor_b32_e32 v255, 32, v16\n"
"v_xor_b32_e32 v254, 32, v15\n"
"v_mov_b32_e32 v88, 0x3f803f80\n"
"v_mov_b32_e32 v89, 0x3f803f80\n"
"v_mov_b32_e32 v90, 0x3f803f80\n"
"v_mov_b32_e32 v91, 0x3f803f80\n"
"v_mov_b32_e32 v252, 0x53800000\n"
"ds_write_b128 v7, v[32:35] offset:49152\n"
".Lmk_a4:\n"
"; @A tail\n"
"s_add_u32 s98, s64, 0x4000\n"
"buffer_store_dwordx4 v[32:35], v7, s[4:7], s98 offen sc1\n"
"s_waitcnt lgkmcnt(2)\n"
"v_add_f32_e32 v76, v76, v24\n"
"v_add_f32_e32 v77, v77, v25\n"
"v_add_f32_e32 v78, v78, v26\n"
"v_add_f32_e32 v79, v79, v27\n"
"v_add_f32_e32 v68, v68, v28\n"
"v_add_f32_e32 v69, v69, v29\n"
"v_add_f32_e32 v70, v70, v30\n"
"v_add_f32_e32 v71, v71, v31\n"
"v_mul_f32_e32 v76, v76, v253\n"
"v_mul_f32_e32 v77, v77, v253\n"
"v_mul_f32_e32 v78, v78, v253\n"
"v_mul_f32_e32 v79, v79, v253\n"
"v_mul_f32_e32 v68, v68, v253\n"
"v_mul_f32_e32 v69, v69, v253\n"
"v_mul_f32_e32 v70, v70, v253\n"
"v_mul_f32_e32 v71, v71, v253\n"
"v_cvt_pk_bf16_f32 v24, v76, v77\n"
"v_cvt_pk_bf16_f32 v25, v78, v79\n"
"v_cvt_pk_bf16_f32 v26, v68, v69\n"
"v_cvt_pk_bf16_f32 v27, v70, v71\n"
"ds_write_b128 v2, v[24:27] offset:16384\n"
"s_waitcnt lgkmcnt(0)\n"
"s_barrier\n"
"ds_read_b128 v[28:31], v2 offset:49152\n"
"ds_read_b128 v[32:35], v2 offset:50176\n"
"ds_read_b128 v[92:95], v2 offset:16384\n"
"ds_read_b128 v[96:99], v2 offset:17408\n"
"; @A AT b\n"
"s_mov_b32 s2, 0\n"
"v_mov_b32_e32 v253, 0xff800000\n"
"v_mov_b32_e32 v251, 0xff800000\n"
"v_mov_b32_e32 v250, 0x7f800000\n"
"v_mov_b32_e32 v249, 0x7f800000\n"
".Lmk_a96:\n"
"ds_read_b128 v[24:27], v15 offset:24576\n"
"ds_read_b128 v[36:39], v254 offset:24832\n"
"ds_read_b128 v[40:43], v15 offset:22528\n"
"ds_read_b128 v[44:47], v254 offset:22784\n"
"s_cmp_gt_i32 s78, 11\n"
"s_cselect_b32 s98, 0x80000000, 0\n"
"s_or_b32 s98, s98, 0x7f800000\n"
"s_waitcnt lgkmcnt(2)\n"
"v_mfma_f32_16x16x32_bf16 v[48:51], v[24:27], v[28:31], 0\n"
"v_mfma_f32_16x16x32_bf16 v[52:55], v[24:27], v[32:35], 0\n"
"v_mfma_f32_16x16x32_bf16 v[56:59], v[36:39], v[32:35], 0\n"
"v_mfma_f32_16x16x32_bf16 v[24:27], v[36:39], v[28:31], 0\n"
"s_waitcnt lgkmcnt(0)\n"
"s_nop 3\n"
"v_max_f32_e32 v248, v48, v49\n"
"v_max_f32_e32 v247, v52, v53\n"
"v_max3_f32 v248, v248, v50, v51\n"
"v_max3_f32 v247, v247, v54, v55\n"
"v_max3_f32 v248, v248, v24, v25\n"
"v_max3_f32 v247, v247, v56, v57\n"
"v_max3_f32 v248, v248, v26, v27\n"
"v_max3_f32 v247, v247, v58, v59\n"
"v_mov_b32_e32 v246, v248\n"
"v_mov_b32_e32 v245, v247\n"
"s_nop 0\n"
"v_permlane16_swap_b32_e32 v248, v246\n"
"v_permlane16_swap_b32_e32 v247, v245\n"
"v_max_f32_e32 v248, v248, v246\n"
"v_max_f32_e32 v247, v247, v245\n"
"v_mov_b32_e32 v246, v248\n"
"v_mov_b32_e32 v245, v247\n"
"s_nop 0\n"
"v_permlane32_swap_b32_e32 v248, v246\n"
"v_permlane32_swap_b32_e32 v247, v245\n"
"v_max_f32_e32 v248, v248, v246\n"
"v_max_f32_e32 v247, v247, v245\n"
"v_med3_f32 v248, v248, v253, v250\n"
"v_med3_f32 v247, v247, v251, v249\n"
"v_min_f32_e64 v36, s98, -v248\n"
"v_min_f32_e64 v37, s98, -v248\n"
"v_min_f32_e64 v38, s98, -v248\n"
"v_min_f32_e64 v39, s98, -v248\n"
"v_min_f32_e64 v60, s98, -v247\n"
"v_min_f32_e64 v61, s98, -v247\n"
"v_min_f32_e64 v62, s98, -v247\n"
"v_min_f32_e64 v63, s98, -v247\n"
"v_mfma_f32_16x16x32_bf16 v[64:67], v[40:43], v[28:31], v[36:39]\n"
"ds_read_b128 v[68:71], v15 offset:20480\n"
"s_add_u32 s98, s69, 0x602000\n"
"buffer_load_dwordx4 a[32:35], v0, s[4:7], s98 offen sc1\n"
"v_sub_f32_e32 v48, v48, v248\n"
"v_sub_f32_e32 v49, v49, v248\n"
"v_sub_f32_e32 v50, v50, v248\n"
"v_mfma_f32_16x16x32_bf16 v[72:75], v[40:43], v[32:35], v[60:63]\n"
"buffer_load_dwordx4 a[36:39], v0, s[4:7], s98 offen offset:1024 sc1\n"
"v_sub_f32_e32 v51, v51, v248\n"
"v_sub_f32_e32 v24, v24, v248\n"
"v_sub_f32_e32 v25, v25, v248\n"
"v_sub_f32_e32 v26, v26, v248\n"
"v_sub_f32_e32 v27, v27, v248\n"
"v_mfma_f32_16x16x32_bf16 v[40:43], v[44:47], v[32:35], v[60:63]\n"
"ds_read_b128 v[76:79], v254 offset:20736\n"
"v_sub_f32_e32 v52, v52, v247\n"
"v_sub_f32_e32 v53, v53, v247\n"
"v_sub_f32_e32 v54, v54, v247\n"
"v_sub_f32_e32 v55, v55, v247\n"
"v_mfma_f32_16x16x32_bf16 v[80:83], v[44:47], v[28:31], v[36:39]\n"
"buffer_load_dwordx4 a[40:43], v0, s[4:7], s98 offen offset:2048 sc1\n"
"v_sub_f32_e32 v56, v56, v247\n"
"v_sub_f32_e32 v57, v57, v247\n"
"v_sub_f32_e32 v58, v58, v247\n"
"v_sub_f32_e32 v59, v59, v247\n"
"s_waitcnt lgkmcnt(0)\n"
"v_mfma_f32_16x16x32_bf16 v[44:47], v[68:71], v[28:31], v[36:39]\n"
"ds_read_b128 v[84:87], v15 offset:18432\n"
"buffer_load_dwordx4 a[44:47], v0, s[4:7], s98 offen offset:3072 sc1\n"
"ds_read_b128 v[100:103], v254 offset:18688\n"
"v_exp_f32_e32 v64, v64\n"
"v_exp_f32_e32 v65, v65\n"
"v_exp_f32_e32 v66, v66\n"
"v_exp_f32_e32 v67, v67\n"
"v_exp_f32_e32 v80, v80\n"
"v_mfma_f32_16x16x32_bf16 v[104:107], v[68:71], v[32:35], v[60:63]\n"
"ds_read_b64_tr_b16 v[68:69], v16 offset:22528\n"
"buffer_load_dwordx4 a[48:51], v1, s[4:7], s98 offen sc1\n"
"v_exp_f32_e32 v81, v81\n"
"v_exp_f32_e32 v82, v82\n"
"v_exp_f32_e32 v83, v83\n"
"v_cvt_pk_bf16_f32 v108, v64, v65\n"
"v_cvt_pk_bf16_f32 v109, v66, v67\n"
"v_cvt_pk_bf16_f32 v110, v80, v81\n"
"v_mfma_f32_16x16x32_bf16 v[64:67], v[76:79], v[32:35], v[60:63]\n"
"ds_read_b64_tr_b16 v[70:71], v255 offset:22784\n"
"ds_read_b64_tr_b16 v[80:81], v255 offset:22528\n"
"buffer_load_dwordx4 a[52:55], v1, s[4:7], s98 offen offset:1024 sc1\n"
"v_cvt_pk_bf16_f32 v111, v82, v83\n"
"v_exp_f32_e32 v72, v72\n"
"v_exp_f32_e32 v73, v73\n"
"v_exp_f32_e32 v74, v74\n"
"v_exp_f32_e32 v75, v75\n"
"v_mfma_f32_16x16x32_bf16 v[112:115], v[76:79], v[28:31], v[36:39]\n"
"ds_read_b64_tr_b16 v[82:83], v16 offset:22784\n"
"v_exp_f32_e32 v40, v40\n"
"v_exp_f32_e32 v41, v41\n"
"v_exp_f32_e32 v42, v42\n"
"v_exp_f32_e32 v43, v43\n"
"v_cvt_pk_bf16_f32 v76, v72, v73\n"
"v_cvt_pk_bf16_f32 v77, v74, v75\n"
"v_cvt_pk_bf16_f32 v78, v40, v41\n"
"v_cvt_pk_bf16_f32 v79, v42, v43\n"
"s_waitcnt lgkmcnt(4)\n"
"v_mfma_f32_16x16x32_bf16 v[40:43], v[84:87], v[28:31], v[36:39]\n"
"ds_read_b128 v[72:75], v15 offset:16384\n"
"v_exp_f32_e32 v44, v44\n"
"v_exp_f32_e32 v45, v45\n"
"v_exp_f32_e32 v46, v46\n"
"v_mfma_f32_16x16x32_bf16 v[116:119], v[84:87], v[32:35], v[60:63]\n"
"buffer_load_dwordx4 a[56:59], v1, s[4:7], s98 offen offset:2048 sc1\n"
"v_exp_f32_e32 v47, v47\n"
"v_mfma_f32_16x16x32_bf16 v[84:87], v[100:103], v[32:35], v[60:63]\n"
"ds_read_b128 v[120:123], v254 offset:16640\n"
"v_exp_f32_e32 v112, v112\n"
"v_exp_f32_e32 v113, v113\n"
"v_mfma_f32_16x16x32_bf16 v[124:127], v[100:103], v[28:31], v[36:39]\n"
"ds_read_b64_tr_b16 v[100:101], v16 offset:20480\n"
"v_exp_f32_e32 v114, v114\n"
"v_exp_f32_e32 v115, v115\n"
"v_cvt_pk_bf16_f32 v128, v44, v45\n"
"s_waitcnt lgkmcnt(3)\n"
"v_mfma_f32_16x16x32_bf16 v[132:135], v[88:91], v[108:111], 0\n"
"buffer_load_dwordx4 a[60:63], v1, s[4:7], s98 offen offset:3072 sc1\n"
"v_cvt_pk_bf16_f32 v129, v46, v47\n"
"v_cvt_pk_bf16_f32 v130, v112, v113\n"
"v_cvt_pk_bf16_f32 v131, v114, v115\n"
"v_mfma_f32_16x16x32_bf16 v[44:47], v[68:71], v[108:111], 0\n"
"ds_read_b64_tr_b16 v[102:103], v255 offset:20736\n"
"v_exp_f32_e32 v104, v104\n"
"v_exp_f32_e32 v105, v105\n"
"v_mfma_f32_16x16x32_bf16 v[112:115], v[80:83], v[108:111], 0\n"
"ds_read_b64_tr_b16 v[108:109], v255 offset:20480\n"
"s_add_u32 s98, s69, 0x604000\n"
"buffer_load_dwordx4 a[64:67], v0, s[4:7], s98 offen sc1\n"
"v_exp_f32_e32 v106, v106\n"
"v_exp_f32_e32 v107, v107\n"
"v_mfma_f32_16x16x32_bf16 v[136:139], v[80:83], v[76:79], 0\n"
"v_exp_f32_e32 v64, v64\n"
"v_exp_f32_e32 v65, v65\n"
"v_mfma_f32_16x16x32_bf16 v[80:83], v[68:71], v[76:79], 0\n"
"ds_read_b64_tr_b16 v[110:111], v16 offset:20736\n"
"v_exp_f32_e32 v66, v66\n"
"v_exp_f32_e32 v67, v67\n"
"v_mfma_f32_16x16x32_bf16 v[68:71], v[88:91], v[76:79], 0\n"
"s_waitcnt vmcnt(19)\n"
"v_cvt_pk_bf16_f32 v76, v104, v105\n"
"v_cvt_pk_bf16_f32 v77, v106, v107\n"
"v_cvt_pk_bf16_f32 v78, v64, v65\n"
"v_cvt_pk_bf16_f32 v79, v66, v67\n"
"s_cmp_lg_u32 s2, 0\n"
"s_cbranch_scc1 .Lmk_s98\n"
"s_barrier\n"
".Lmk_s98:\n"
"s_waitcnt lgkmcnt(4)\n"
"v_mfma_f32_16x16x32_bf16 v[64:67], v[72:75], v[28:31], v[36:39]\n"
"ds_read_b128 v[104:107], v15 offset:14336\n"
"v_exp_f32_e32 v40, v40\n"
"v_exp_f32_e32 v41, v41\n"
"v_exp_f32_e32 v42, v42\n"
"v_mfma_f32_16x16x32_bf16 v[140:143], v[72:75], v[32:35], v[60:63]\n"
"buffer_load_dwordx4 a[68:71], v0, s[4:7], s98 offen offset:1024 sc1\n"
"v_exp_f32_e32 v43, v43\n"
"v_mfma_f32_16x16x32_bf16 v[72:75], v[120:123], v[32:35], v[60:63]\n"
"ds_read_b128 v[144:147], v254 offset:14592\n"
"v_exp_f32_e32 v124, v124\n"
"v_exp_f32_e32 v125, v125\n"
"v_mfma_f32_16x16x32_bf16 v[148:151], v[120:123], v[28:31], v[36:39]\n"
"ds_read_b64_tr_b16 v[120:121], v16 offset:18432\n"
"v_exp_f32_e32 v126, v126\n"
"v_exp_f32_e32 v127, v127\n"
"s_waitcnt lgkmcnt(3)\n"
"v_mfma_f32_16x16x32_bf16 v[132:135], v[88:91], v[128:131], v[132:135]\n"
"buffer_load_dwordx4 a[72:75], v0, s[4:7], s98 offen offset:2048 sc1\n"
"v_cvt_pk_bf16_f32 v152, v40, v41\n"
"v_cvt_pk_bf16_f32 v153, v42, v43\n"
"v_cvt_pk_bf16_f32 v154, v124, v125\n"
"v_mfma_f32_16x16x32_bf16 v[44:47], v[100:103], v[128:131], v[44:47]\n"
"ds_read_b64_tr_b16 v[122:123], v255 offset:18688\n"
"v_cvt_pk_bf16_f32 v155, v126, v127\n"
"v_exp_f32_e32 v116, v116\n"
"v_exp_f32_e32 v117, v117\n"
"v_mfma_f32_16x16x32_bf16 v[112:115], v[108:111], v[128:131], v[112:115]\n"
"ds_read_b64_tr_b16 v[40:41], v255 offset:18432\n"
"v_exp_f32_e32 v118, v118\n"
"v_exp_f32_e32 v119, v119\n"
"v_mfma_f32_16x16x32_bf16 v[136:139], v[108:111], v[76:79], v[136:139]\n"
"buffer_load_dwordx4 a[76:79], v0, s[4:7], s98 offen offset:3072 sc1\n"
"v_exp_f32_e32 v84, v84\n"
"v_exp_f32_e32 v85, v85\n"
"v_mfma_f32_16x16x32_bf16 v[80:83], v[100:103], v[76:79], v[80:83]\n"
"ds_read_b64_tr_b16 v[42:43], v16 offset:18688\n"
"v_exp_f32_e32 v86, v86\n"
"v_exp_f32_e32 v87, v87\n"
"v_mfma_f32_16x16x32_bf16 v[68:71], v[88:91], v[76:79], v[68:71]\n"
"s_waitcnt lgkmcnt(4)\n"
"v_cvt_pk_bf16_f32 v76, v116, v117\n"
"v_cvt_pk_bf16_f32 v77, v118, v119\n"
"v_cvt_pk_bf16_f32 v78, v84, v85\n"
"v_cvt_pk_bf16_f32 v79, v86, v87\n"
"s_cmp_eq_i32 s78, 8\n"
"s_cbranch_scc1 .Lmk_x99s\n"
".Lmk_x99r:\n"
"v_mfma_f32_16x16x32_bf16 v[84:87], v[104:107], v[28:31], v[36:39]\n"
"ds_read_b128 v[100:103], v15 offset:12288\n"
"v_exp_f32_e32 v64, v64\n"
"v_exp_f32_e32 v65, v65\n"
"v_exp_f32_e32 v66, v66\n"
"v_mfma_f32_16x16x32_bf16 v[108:111], v[104:107], v[32:35], v[60:63]\n"
"buffer_load_dwordx4 a[80:83], v1, s[4:7], s98 offen sc1\n"
"v_exp_f32_e32 v67, v67\n"
"v_mfma_f32_16x16x32_bf16 v[104:107], v[144:147], v[32:35], v[60:63]\n"
"ds_read_b128 v[116:119], v254 offset:12544\n"
"v_exp_f32_e32 v148, v148\n"
"v_exp_f32_e32 v149, v149\n"
"v_mfma_f32_16x16x32_bf16 v[124:127], v[144:147], v[28:31], v[36:39]\n"
"ds_read_b64_tr_b16 v[128:129], v16 offset:16384\n"
"v_exp_f32_e32 v150, v150\n"
"v_exp_f32_e32 v151, v151\n"
"s_waitcnt lgkmcnt(3)\n"
"v_mfma_f32_16x16x32_bf16 v[132:135], v[88:91], v[152:155], v[132:135]\n"
"buffer_load_dwordx4 a[84:87], v1, s[4:7], s98 offen offset:1024 sc1\n"
"v_cvt_pk_bf16_f32 v144, v64, v65\n"
"v_cvt_pk_bf16_f32 v145, v66, v67\n"
"v_cvt_pk_bf16_f32 v146, v148, v149\n"
"v_mfma_f32_16x16x32_bf16 v[44:47], v[120:123], v[152:155], v[44:47]\n"
"ds_read_b64_tr_b16 v[130:131], v255 offset:16640\n"
"v_cvt_pk_bf16_f32 v147, v150, v151\n"
"v_exp_f32_e32 v140, v140\n"
"v_exp_f32_e32 v141, v141\n"
"v_mfma_f32_16x16x32_bf16 v[112:115], v[40:43], v[152:155], v[112:115]\n"
"ds_read_b64_tr_b16 v[64:65], v255 offset:16384\n"
"v_exp_f32_e32 v142, v142\n"
"v_exp_f32_e32 v143, v143\n"
"v_mfma_f32_16x16x32_bf16 v[136:139], v[40:43], v[76:79], v[136:139]\n"
"buffer_load_dwordx4 a[88:91], v1, s[4:7], s98 offen offset:2048 sc1\n"
"v_exp_f32_e32 v72, v72\n"
"v_exp_f32_e32 v73, v73\n"
"v_mfma_f32_16x16x32_bf16 v[80:83], v[120:123], v[76:79], v[80:83]\n"
"ds_read_b64_tr_b16 v[66:67], v16 offset:16640\n"
"v_exp_f32_e32 v74, v74\n"
"v_exp_f32_e32 v75, v75\n"
"v_mfma_f32_16x16x32_bf16 v[68:71], v[88:91], v[76:79], v[68:71]\n"
"s_waitcnt lgkmcnt(4)\n"
"v_cvt_pk_bf16_f32 v40, v140, v141\n"
"v_cvt_pk_bf16_f32 v41, v142, v143\n"
"v_cvt_pk_bf16_f32 v42, v72, v73\n"
"v_cvt_pk_bf16_f32 v43, v74, v75\n"
"s_cmp_eq_i32 s78, 7\n"
"s_cbranch_scc1 .Lmk_x100s\n"
".Lmk_x100r:\n"
"v_mfma_f32_16x16x32_bf16 v[72:75], v[100:103], v[28:31], v[36:39]\n"
"ds_read_b128 v[76:79], v15 offset:10240\n"
"v_exp_f32_e32 v84, v84\n"
"v_exp_f32_e32 v85, v85\n"
"v_exp_f32_e32 v86, v86\n"
"v_mfma_f32_16x16x32_bf16 v[120:123], v[100:103], v[32:35], v[60:63]\n"
"buffer_load_dwordx4 a[92:95], v1, s[4:7], s98 offen offset:3072 sc1\n"
"v_exp_f32_e32 v87, v87\n"
"v_mfma_f32_16x16x32_bf16 v[100:103], v[116:119], v[32:35], v[60:63]\n"
"ds_read_b128 v[140:143], v254 offset:10496\n"
"v_exp_f32_e32 v124, v124\n"
"v_exp_f32_e32 v125, v125\n"
"v_mfma_f32_16x16x32_bf16 v[148:151], v[116:119], v[28:31], v[36:39]\n"
"ds_read_b64_tr_b16 v[116:117], v16 offset:14336\n"
"v_exp_f32_e32 v126, v126\n"
"v_exp_f32_e32 v127, v127\n"
"v_cvt_pk_bf16_f32 v152, v84, v85\n"
"s_waitcnt lgkmcnt(3)\n"
"v_mfma_f32_16x16x32_bf16 v[132:135], v[88:91], v[144:147], v[132:135]\n"
"s_add_u32 s98, s69, 0x606000\n"
"buffer_load_dwordx4 a[96:99], v0, s[4:7], s98 offen sc1\n"
"v_cvt_pk_bf16_f32 v153, v86, v87\n"
"v_cvt_pk_bf16_f32 v154, v124, v125\n"
"v_mfma_f32_16x16x32_bf16 v[44:47], v[128:131], v[144:147], v[44:47]\n"
"ds_read_b64_tr_b16 v[118:119], v255 offset:14592\n"
"v_cvt_pk_bf16_f32 v155, v126, v127\n"
"v_exp_f32_e32 v108, v108\n"
"v_exp_f32_e32 v109, v109\n"
"v_mfma_f32_16x16x32_bf16 v[112:115], v[64:67], v[144:147], v[112:115]\n"
"ds_read_b64_tr_b16 v[84:85], v255 offset:14336\n"
"v_exp_f32_e32 v110, v110\n"
"v_exp_f32_e32 v111, v111\n"
"v_mfma_f32_16x16x32_bf16 v[136:139], v[64:67], v[40:43], v[136:139]\n"
"buffer_load_dwordx4 a[100:103], v0, s[4:7], s98 offen offset:1024 sc1\n"
"v_exp_f32_e32 v104, v104\n"
"v_exp_f32_e32 v105, v105\n"
"v_mfma_f32_16x16x32_bf16 v[80:83], v[128:131], v[40:43], v[80:83]\n"
"ds_read_b64_tr_b16 v[86:87], v16 offset:14592\n"
"v_exp_f32_e32 v106, v106\n"
"v_exp_f32_e32 v107, v107\n"
"v_mfma_f32_16x16x32_bf16 v[68:71], v[88:91], v[40:43], v[68:71]\n"
"s_waitcnt lgkmcnt(4)\n"
"v_cvt_pk_bf16_f32 v40, v108, v109\n"
"v_cvt_pk_bf16_f32 v41, v110, v111\n"
"v_cvt_pk_bf16_f32 v42, v104, v105\n"
"v_cvt_pk_bf16_f32 v43, v106, v107\n"
"s_cmp_eq_i32 s78, 6\n"
"s_cbranch_scc1 .Lmk_x101s\n"
".Lmk_x101r:\n"
"v_mfma_f32_16x16x32_bf16 v[64:67], v[76:79], v[28:31], v[36:39]\n"
"ds_read_b128 v[104:107], v15 offset:8192\n"
"v_exp_f32_e32 v72, v72\n"
"v_exp_f32_e32 v73, v73\n"
"v_exp_f32_e32 v74, v74\n"
"v_mfma_f32_16x16x32_bf16 v[108:111], v[76:79], v[32:35], v[60:63]\n"
"buffer_load_dwordx4 a[104:107], v0, s[4:7], s98 offen offset:2048 sc1\n"
"v_exp_f32_e32 v75, v75\n"
"v_mfma_f32_16x16x32_bf16 v[76:79], v[140:143], v[32:35], v[60:63]\n"
"ds_read_b128 v[124:127], v254 offset:8448\n"
"v_exp_f32_e32 v148, v148\n"
"v_exp_f32_e32 v149, v149\n"
"v_mfma_f32_16x16x32_bf16 v[128:131], v[140:143], v[28:31], v[36:39]\n"
"ds_read_b64_tr_b16 v[140:141], v16 offset:12288\n"
"v_exp_f32_e32 v150, v150\n"
"v_exp_f32_e32 v151, v151\n"
"s_waitcnt lgkmcnt(3)\n"
"v_mfma_f32_16x16x32_bf16 v[132:135], v[88:91], v[152:155], v[132:135]\n"
"buffer_load_dwordx4 a[108:111], v0, s[4:7], s98 offen offset:3072 sc1\n"
"v_cvt_pk_bf16_f32 v144, v72, v73\n"
"v_cvt_pk_bf16_f32 v145, v74, v75\n"
"v_cvt_pk_bf16_f32 v146, v148, v149\n"
"v_mfma_f32_16x16x32_bf16 v[44:47], v[116:119], v[152:155], v[44:47]\n"
"ds_read_b64_tr_b16 v[142:143], v255 offset:12544\n"
"v_cvt_pk_bf16_f32 v147, v150, v151\n"
"v_exp_f32_e32 v120, v120\n"
"v_exp_f32_e32 v121, v121\n"
"v_mfma_f32_16x16x32_bf16 v[112:115], v[84:87], v[152:155], v[112:115]\n"
"ds_read_b64_tr_b16 v[72:73], v255 offset:12288\n"
"v_exp_f32_e32 v122, v122\n"
"v_exp_f32_e32 v123, v123\n"
"v_mfma_f32_16x16x32_bf16 v[136:139], v[84:87], v[40:43], v[136:139]\n"
"buffer_load_dwordx4 a[112:115], v1, s[4:7], s98 offen sc1\n"
"v_exp_f32_e32 v100, v100\n"
"v_exp_f32_e32 v101, v101\n"
"v_mfma_f32_16x16x32_bf16 v[80:83], v[116:119], v[40:43], v[80:83]\n"
"ds_read_b64_tr_b16 v[74:75], v16 offset:12544\n"
"v_exp_f32_e32 v102, v102\n"
"v_exp_f32_e32 v103, v103\n"
"v_mfma_f32_16x16x32_bf16 v[68:71], v[88:91], v[40:43], v[68:71]\n"
"s_waitcnt lgkmcnt(4)\n"
"v_cvt_pk_bf16_f32 v40, v120, v121\n"
"v_cvt_pk_bf16_f32 v41, v122, v123\n"
"v_cvt_pk_bf16_f32 v42, v100, v101\n"
"v_cvt_pk_bf16_f32 v43, v102, v103\n"
"s_cmp_eq_i32 s78, 5\n"
"s_cbranch_scc1 .Lmk_x102s\n"
".Lmk_x102r:\n"
"v_mfma_f32_16x16x32_bf16 v[84:87], v[104:107], v[28:31], v[36:39]\n"
"ds_read_b64_tr_b16 v[100:101], v16 offset:10240\n"
"ds_read_b64_tr_b16 v[102:103], v255 offset:10496\n"
"v_exp_f32_e32 v64, v64\n"
"v_exp_f32_e32 v65, v65\n"
"v_mfma_f32_16x16x32_bf16 v[116:119], v[104:107], v[32:35], v[60:63]\n"
"buffer_load_dwordx4 a[116:119], v1, s[4:7], s98 offen offset:1024 sc1\n"
"v_exp_f32_e32 v66, v66\n"
"v_exp_f32_e32 v67, v67\n"
"v_mfma_f32_16x16x32_bf16 v[104:107], v[124:127], v[32:35], v[60:63]\n"
"ds_read_b64_tr_b16 v[60:61], v255 offset:10240\n"
"ds_read_b64_tr_b16 v[62:63], v16 offset:10496\n"
"v_exp_f32_e32 v128, v128\n"
"v_exp_f32_e32 v129, v129\n"
"v_mfma_f32_16x16x32_bf16 v[120:123], v[124:127], v[28:31], v[36:39]\n"
"ds_read_b64_tr_b16 v[36:37], v16 offset:24576\n"
"v_exp_f32_e32 v130, v130\n"
"v_exp_f32_e32 v131, v131\n"
"s_waitcnt lgkmcnt(5)\n"
"v_mfma_f32_16x16x32_bf16 v[132:135], v[88:91], v[144:147], v[132:135]\n"
"buffer_load_dwordx4 a[120:123], v1, s[4:7], s98 offen offset:2048 sc1\n"
"ds_read_b64_tr_b16 v[38:39], v255 offset:24832\n"
"v_cvt_pk_bf16_f32 v124, v64, v65\n"
"v_cvt_pk_bf16_f32 v125, v66, v67\n"
"v_cvt_pk_bf16_f32 v126, v128, v129\n"
"v_mfma_f32_16x16x32_bf16 v[44:47], v[140:143], v[144:147], v[44:47]\n"
"ds_read_b64_tr_b16 v[64:65], v255 offset:24576\n"
"v_cvt_pk_bf16_f32 v127, v130, v131\n"
"v_exp_f32_e32 v108, v108\n"
"v_exp_f32_e32 v109, v109\n"
"v_mfma_f32_16x16x32_bf16 v[112:115], v[72:75], v[144:147], v[112:115]\n"
"ds_read_b64_tr_b16 v[66:67], v16 offset:24832\n"
"v_exp_f32_e32 v110, v110\n"
"v_exp_f32_e32 v111, v111\n"
"v_mfma_f32_16x16x32_bf16 v[136:139], v[72:75], v[40:43], v[136:139]\n"
"buffer_load_dwordx4 a[124:127], v1, s[4:7], s98 offen offset:3072 sc1\n"
"v_exp_f32_e32 v76, v76\n"
"v_add_u32_e32 v248, 0x26400, v0\n"
"v_exp_f32_e32 v77, v77\n"
"v_mfma_f32_16x16x32_bf16 v[80:83], v[140:143], v[40:43], v[80:83]\n"
"ds_read_b128 v[72:75], v248\n"
"v_exp_f32_e32 v78, v78\n"
"v_exp_f32_e32 v79, v79\n"
"v_mfma_f32_16x16x32_bf16 v[68:71], v[88:91], v[40:43], v[68:71]\n"
"ds_read_b128 v[128:131], v248 offset:1024\n"
"v_cvt_pk_bf16_f32 v40, v108, v109\n"
"v_cvt_pk_bf16_f32 v41, v110, v111\n"
"v_cvt_pk_bf16_f32 v42, v76, v77\n"
"v_cvt_pk_bf16_f32 v43, v78, v79\n"
"s_waitcnt lgkmcnt(6)\n"
"v_mfma_f32_16x16x32_bf16 v[132:135], v[88:91], v[124:127], v[132:135]\n"
"ds_read_b64_tr_b16 v[76:77], v16 offset:8192\n"
"v_cndmask_b32_e64 v84, v48, v84, s[20:21]\n"
"v_cndmask_b32_e64 v85, v49, v85, s[22:23]\n"
"v_cndmask_b32_e64 v86, v50, v86, s[24:25]\n"
"s_add_u32 m0, s77, 0x4000\n"
"s_add_u32 s98, s65, 0\n"
"buffer_load_dwordx4 v2, s[4:7], s98 offen sc1 lds\n"
"buffer_load_dwordx4 v2, s[4:7], s98 offen offset:1024 sc1 lds\n"
"v_cndmask_b32_e64 v87, v51, v87, s[26:27]\n"
"v_cndmask_b32_e64 v120, v24, v120, s[28:29]\n"
"v_mfma_f32_16x16x32_bf16 v[44:47], v[100:103], v[124:127], v[44:47]\n"
"ds_read_b64_tr_b16 v[78:79], v255 offset:8448\n"
"v_cndmask_b32_e64 v121, v25, v121, s[30:31]\n"
"v_cndmask_b32_e64 v122, v26, v122, s[32:33]\n"
"v_cndmask_b32_e64 v123, v27, v123, s[34:35]\n"
"v_exp_f32_e32 v84, v84\n"
"v_exp_f32_e32 v85, v85\n"
"v_exp_f32_e32 v86, v86\n"
"v_exp_f32_e32 v87, v87\n"
"v_mfma_f32_16x16x32_bf16 v[112:115], v[60:63], v[124:127], v[112:115]\n"
"v_exp_f32_e32 v120, v120\n"
"v_exp_f32_e32 v121, v121\n"
"v_exp_f32_e32 v122, v122\n"
"v_exp_f32_e32 v123, v123\n"
"v_cvt_pk_bf16_f32 v48, v84, v85\n"
"v_cvt_pk_bf16_f32 v49, v86, v87\n"
"v_cvt_pk_bf16_f32 v50, v120, v121\n"
"v_mfma_f32_16x16x32_bf16 v[136:139], v[60:63], v[40:43], v[136:139]\n"
"ds_read_b64_tr_b16 v[60:61], v255 offset:8192\n"
"s_add_u32 s98, s69, 0x608000\n"
"buffer_load_dwordx4 v[24:27], v0, s[4:7], s98 offen sc1\n"
"v_cvt_pk_bf16_f32 v51, v122, v123\n"
"v_cndmask_b32_e64 v116, v52, v116, s[36:37]\n"
"v_cndmask_b32_e64 v117, v53, v117, s[38:39]\n"
"v_cndmask_b32_e64 v118, v54, v118, s[40:41]\n"
"v_cndmask_b32_e64 v119, v55, v119, s[42:43]\n"
"v_cndmask_b32_e64 v104, v56, v104, s[44:45]\n"
"v_cndmask_b32_e64 v105, v57, v105, s[46:47]\n"
"v_mfma_f32_16x16x32_bf16 v[80:83], v[100:103], v[40:43], v[80:83]\n"
"ds_read_b64_tr_b16 v[62:63], v16 offset:8448\n"
"v_cndmask_b32_e64 v106, v58, v106, s[48:49]\n"
"v_cndmask_b32_e64 v107, v59, v107, s[50:51]\n"
"v_exp_f32_e32 v116, v116\n"
"v_exp_f32_e32 v117, v117\n"
"v_exp_f32_e32 v118, v118\n"
"v_exp_f32_e32 v119, v119\n"
"v_exp_f32_e32 v104, v104\n"
"v_mfma_f32_16x16x32_bf16 v[68:71], v[88:91], v[40:43], v[68:71]\n"
"s_waitcnt lgkmcnt(0)\n"
"v_exp_f32_e32 v105, v105\n"
"v_exp_f32_e32 v106, v106\n"
"v_exp_f32_e32 v107, v107\n"
"v_cvt_pk_bf16_f32 v40, v116, v117\n"
"v_cvt_pk_bf16_f32 v41, v118, v119\n"
"v_cvt_pk_bf16_f32 v42, v104, v105\n"
"v_cvt_pk_bf16_f32 v43, v106, v107\n"
"v_mfma_f32_16x16x32_bf16 v[132:135], v[88:91], v[48:51], v[132:135]\n"
"v_and_b32_e32 v72, v48, v72\n"
"v_and_b32_e32 v73, v49, v73\n"
"v_and_b32_e32 v74, v50, v74\n"
"v_and_b32_e32 v75, v51, v75\n"
"v_mfma_f32_16x16x32_bf16 v[68:71], v[88:91], v[40:43], v[68:71]\n"
"v_xor_b32_e32 v52, v48, v72\n"
"v_xor_b32_e32 v53, v49, v73\n"
"v_xor_b32_e32 v54, v50, v74\n"
"v_xor_b32_e32 v55, v51, v75\n"
"v_mfma_f32_16x16x32_bf16 v[44:47], v[76:79], v[72:75], v[44:47]\n"
"v_and_b32_e32 v128, v40, v128\n"
"v_and_b32_e32 v129, v41, v129\n"
"v_mfma_f32_16x16x32_bf16 v[112:115], v[60:63], v[72:75], v[112:115]\n"
"v_and_b32_e32 v130, v42, v130\n"
"v_and_b32_e32 v131, v43, v131\n"
"v_mfma_f32_16x16x32_bf16 v[44:47], v[36:39], v[52:55], v[44:47]\n"
"v_xor_b32_e32 v48, v40, v128\n"
"v_xor_b32_e32 v49, v41, v129\n"
"v_mfma_f32_16x16x32_bf16 v[112:115], v[64:67], v[52:55], v[112:115]\n"
"v_xor_b32_e32 v50, v42, v130\n"
"v_xor_b32_e32 v51, v43, v131\n"
"v_mfma_f32_16x16x32_bf16 v[80:83], v[76:79], v[128:131], v[80:83]\n"
"s_add_u32 m0, s77, 0xc000\n"
"s_add_u32 s97, s65, 0x4000\n"
"buffer_load_dwordx4 v2, s[4:7], s97 offen sc1 lds\n"
"buffer_load_dwordx4 v2, s[4:7], s97 offen offset:1024 sc1 lds\n"
"v_mfma_f32_16x16x32_bf16 v[136:139], v[60:63], v[128:131], v[136:139]\n"
"v_mfma_f32_16x16x32_bf16 v[80:83], v[36:39], v[48:51], v[80:83]\n"
"v_mfma_f32_16x16x32_bf16 v[136:139], v[64:67], v[48:51], v[136:139]\n"
"v_max_f32_e32 v248, v132, v68\n"
"v_rcp_f32_e32 v132, v132\n"
"v_rcp_f32_e32 v68, v68\n"
"s_nop 0\n"
"v_max3_f32 v248, v248, v132, v68\n"
"v_cmp_nlt_f32_e32 vcc, v248, v252\n"
"s_cbranch_vccnz .Lmk_b97\n"
".Lmk_k103:\n"
"s_add_u32 s97, s60, 0x40000\n"
"v_mul_f32_e32 v44, v44, v132\n"
"v_mul_f32_e32 v45, v45, v132\n"
"v_mul_f32_e32 v46, v46, v132\n"
"v_mul_f32_e32 v47, v47, v132\n"
"v_mul_f32_e32 v112, v112, v132\n"
"v_mul_f32_e32 v113, v113, v132\n"
"v_mul_f32_e32 v114, v114, v132\n"
"v_mul_f32_e32 v115, v115, v132\n"
"v_cvt_pk_bf16_f32 v28, v44, v45\n"
"v_cvt_pk_bf16_f32 v29, v46, v47\n"
"v_cvt_pk_bf16_f32 v30, v112, v113\n"
"v_cvt_pk_bf16_f32 v31, v114, v115\n"
"buffer_store_dwordx4 v[28:31], v0, s[4:7], s97 offen sc1\n"
"s_add_u32 s97, s60, 0x48000\n"
"v_mul_f32_e32 v80, v80, v68\n"
"v_mul_f32_e32 v81, v81, v68\n"
"v_mul_f32_e32 v82, v82, v68\n"
"v_mul_f32_e32 v83, v83, v68\n"
"v_mul_f32_e32 v136, v136, v68\n"
"v_mul_f32_e32 v137, v137, v68\n"
"v_mul_f32_e32 v138, v138, v68\n"
"v_mul_f32_e32 v139, v139, v68\n"
"v_cvt_pk_bf16_f32 v28, v80, v81\n"
"v_cvt_pk_bf16_f32 v29, v82, v83\n"
"v_cvt_pk_bf16_f32 v30, v136, v137\n"
"v_cvt_pk_bf16_f32 v31, v138, v139\n"
"buffer_store_dwordx4 v[28:31], v0, s[4:7], s97 offen sc1\n"
";\n"
"; @A AT a\n"
"s_mov_b32 s2, 0\n"
"v_mov_b32_e32 v253, 0xff800000\n"
"v_mov_b32_e32 v251, 0xff800000\n"
"v_mov_b32_e32 v250, 0x7f800000\n"
"v_mov_b32_e32 v249, 0x7f800000\n"
".Lmk_a104:\n"
"ds_read_b128 v[28:31], v15 offset:16384\n"
"ds_read_b128 v[32:35], v254 offset:16640\n"
"ds_read_b128 v[36:39], v15 offset:14336\n"
"ds_read_b128 v[40:43], v254 offset:14592\n"
"s_cmp_gt_i32 s78, 7\n"
"s_cselect_b32 s97, 0x80000000, 0\n"
"s_or_b32 s97, s97, 0x7f800000\n"
"s_add_u32 s96, s67, 32\n"
"buffer_load_dwordx2 v[246:247], v12, s[4:7], s96 offen sc1\n"
"s_waitcnt lgkmcnt(2)\n"
"v_mfma_f32_16x16x32_bf16 v[100:103], v[28:31], v[92:95], 0\n"
"v_mfma_f32_16x16x32_bf16 v[104:107], v[28:31], v[96:99], 0\n"
"v_mfma_f32_16x16x32_bf16 v[108:111], v[32:35], v[96:99], 0\n"
"v_mfma_f32_16x16x32_bf16 v[112:115], v[32:35], v[92:95], 0\n"
"s_waitcnt lgkmcnt(0)\n"
"s_nop 3\n"
"v_max_f32_e32 v248, v100, v101\n"
"v_max_f32_e32 v245, v104, v105\n"
"v_max3_f32 v248, v248, v102, v103\n"
"v_max3_f32 v245, v245, v106, v107\n"
"v_max3_f32 v248, v248, v112, v113\n"
"v_max3_f32 v245, v245, v108, v109\n"
"v_max3_f32 v248, v248, v114, v115\n"
"v_max3_f32 v245, v245, v110, v111\n"
"v_mov_b32_e32 v244, v248\n"
"v_mov_b32_e32 v243, v245\n"
"s_nop 0\n"
"v_permlane16_swap_b32_e32 v248, v244\n"
"v_permlane16_swap_b32_e32 v245, v243\n"
"v_max_f32_e32 v248, v248, v244\n"
"v_max_f32_e32 v245, v245, v243\n"
"v_mov_b32_e32 v244, v248\n"
"v_mov_b32_e32 v243, v245\n"
"s_nop 0\n"
"v_permlane32_swap_b32_e32 v248, v244\n"
"v_permlane32_swap_b32_e32 v245, v243\n"
"v_max_f32_e32 v248, v248, v244\n"
"v_max_f32_e32 v245, v245, v243\n"
"v_med3_f32 v248, v248, v253, v250\n"
"v_med3_f32 v245, v245, v251, v249\n"
"v_min_f32_e64 v116, s97, -v248\n"
"v_min_f32_e64 v117, s97, -v248\n"
"v_min_f32_e64 v118, s97, -v248\n"
"v_min_f32_e64 v119, s97, -v248\n"
"v_min_f32_e64 v120, s97, -v245\n"
"v_min_f32_e64 v121, s97, -v245\n"
"v_min_f32_e64 v122, s97, -v245\n"
"v_min_f32_e64 v123, s97, -v245\n"
"v_mfma_f32_16x16x32_bf16 v[44:47], v[36:39], v[92:95], v[116:119]\n"
"ds_read_b128 v[32:35], v15 offset:12288\n"
"v_sub_f32_e32 v100, v100, v248\n"
"v_sub_f32_e32 v101, v101, v248\n"
"v_sub_f32_e32 v102, v102, v248\n"
"v_sub_f32_e32 v103, v103, v248\n"
"v_mfma_f32_16x16x32_bf16 v[48:51], v[36:39], v[96:99], v[120:123]\n"
"v_sub_f32_e32 v112, v112, v248\n"
"v_sub_f32_e32 v113, v113, v248\n"
"v_sub_f32_e32 v114, v114, v248\n"
"v_sub_f32_e32 v115, v115, v248\n"
"v_mfma_f32_16x16x32_bf16 v[52:55], v[40:43], v[96:99], v[120:123]\n"
"ds_read_b128 v[56:59], v254 offset:12544\n"
"v_sub_f32_e32 v104, v104, v245\n"
"v_sub_f32_e32 v105, v105, v245\n"
"v_sub_f32_e32 v106, v106, v245\n"
"v_sub_f32_e32 v107, v107, v245\n"
"v_mfma_f32_16x16x32_bf16 v[36:39], v[40:43], v[92:95], v[116:119]\n"
"s_waitcnt lgkmcnt(0)\n"
"v_sub_f32_e32 v108, v108, v245\n"
"v_sub_f32_e32 v109, v109, v245\n"
"v_sub_f32_e32 v110, v110, v245\n"
"v_sub_f32_e32 v111, v111, v245\n"
"s_cmp_eq_i32 s78, 7\n"
"s_cbranch_scc1 .Lmk_x106s\n"
".Lmk_x106r:\n"
"v_mfma_f32_16x16x32_bf16 v[60:63], v[32:35], v[92:95], v[116:119]\n"
"ds_read_b128 v[40:43], v15 offset:10240\n"
"buffer_load_dwordx4 v[28:31], v0, s[4:7], s98 offen offset:1024 sc1\n"
"ds_read_b128 v[64:67], v254 offset:10496\n"
"v_exp_f32_e32 v44, v44\n"
"v_exp_f32_e32 v45, v45\n"
"v_exp_f32_e32 v46, v46\n"
"v_exp_f32_e32 v47, v47\n"
"v_exp_f32_e32 v36, v36\n"
"v_mfma_f32_16x16x32_bf16 v[68:71], v[32:35], v[96:99], v[120:123]\n"
"ds_read_b64_tr_b16 v[72:73], v16 offset:14336\n"
"buffer_load_dwordx4 v[32:35], v0, s[4:7], s98 offen offset:2048 sc1\n"
"v_exp_f32_e32 v37, v37\n"
"v_exp_f32_e32 v38, v38\n"
"v_exp_f32_e32 v39, v39\n"
"v_cvt_pk_bf16_f32 v76, v44, v45\n"
"v_cvt_pk_bf16_f32 v77, v46, v47\n"
"v_cvt_pk_bf16_f32 v78, v36, v37\n"
"v_mfma_f32_16x16x32_bf16 v[80:83], v[56:59], v[96:99], v[120:123]\n"
"ds_read_b64_tr_b16 v[74:75], v255 offset:14592\n"
"ds_read_b64_tr_b16 v[84:85], v255 offset:14336\n"
"v_cvt_pk_bf16_f32 v79, v38, v39\n"
"v_exp_f32_e32 v48, v48\n"
"v_exp_f32_e32 v49, v49\n"
"v_exp_f32_e32 v50, v50\n"
"v_exp_f32_e32 v51, v51\n"
"v_mfma_f32_16x16x32_bf16 v[124:127], v[56:59], v[92:95], v[116:119]\n"
"buffer_load_dwordx4 v[36:39], v0, s[4:7], s98 offen offset:3072 sc1\n"
"ds_read_b64_tr_b16 v[86:87], v16 offset:14592\n"
"v_exp_f32_e32 v52, v52\n"
"v_exp_f32_e32 v53, v53\n"
"v_exp_f32_e32 v54, v54\n"
"v_exp_f32_e32 v55, v55\n"
"v_cvt_pk_bf16_f32 v56, v48, v49\n"
"v_cvt_pk_bf16_f32 v57, v50, v51\n"
"v_cvt_pk_bf16_f32 v58, v52, v53\n"
"v_cvt_pk_bf16_f32 v59, v54, v55\n"
"s_waitcnt lgkmcnt(4)\n"
"s_cmp_eq_i32 s78, 6\n"
"s_cbranch_scc1 .Lmk_x107s\n"
".Lmk_x107r:\n"
"v_mfma_f32_16x16x32_bf16 v[128:131], v[40:43], v[92:95], v[116:119]\n"
"ds_read_b128 v[132:135], v15 offset:8192\n"
"ds_read_b128 v[136:139], v254 offset:8448\n"
"v_exp_f32_e32 v60, v60\n"
"v_exp_f32_e32 v61, v61\n"
"v_mfma_f32_16x16x32_bf16 v[140:143], v[40:43], v[96:99], v[120:123]\n"
"buffer_load_dwordx4 v[40:43], v1, s[4:7], s98 offen sc1\n"
"v_exp_f32_e32 v62, v62\n"
"v_exp_f32_e32 v63, v63\n"
"v_mfma_f32_16x16x32_bf16 v[144:147], v[64:67], v[96:99], v[120:123]\n"
"ds_read_b64_tr_b16 v[148:149], v16 offset:12288\n"
"ds_read_b64_tr_b16 v[150:151], v255 offset:12544\n"
"v_exp_f32_e32 v124, v124\n"
"v_exp_f32_e32 v125, v125\n"
"v_mfma_f32_16x16x32_bf16 v[152:155], v[64:67], v[92:95], v[116:119]\n"
"ds_read_b64_tr_b16 v[64:65], v255 offset:12288\n"
"v_exp_f32_e32 v126, v126\n"
"v_exp_f32_e32 v127, v127\n"
"s_waitcnt lgkmcnt(5)\n"
"v_mfma_f32_16x16x32_bf16 v[156:159], v[88:91], v[76:79], 0\n"
"buffer_load_dwordx4 v[44:47], v1, s[4:7], s98 offen offset:1024 sc1\n"
"v_cvt_pk_bf16_f32 v160, v60, v61\n"
"v_cvt_pk_bf16_f32 v161, v62, v63\n"
"v_cvt_pk_bf16_f32 v162, v124, v125\n"
"v_cvt_pk_bf16_f32 v163, v126, v127\n"
"v_mfma_f32_16x16x32_bf16 v[124:127], v[72:75], v[76:79], 0\n"
"ds_read_b64_tr_b16 v[66:67], v16 offset:12544\n"
"s_waitcnt vmcnt(8)\n"
"ds_read_b128 v[164:167], v2 offset:16384\n"
"v_exp_f32_e32 v68, v68\n"
"v_exp_f32_e32 v69, v69\n"
"v_mfma_f32_16x16x32_bf16 v[168:171], v[84:87], v[76:79], 0\n"
"v_exp_f32_e32 v70, v70\n"
"v_exp_f32_e32 v71, v71\n"
"v_mfma_f32_16x16x32_bf16 v[172:175], v[84:87], v[56:59], 0\n"
"buffer_load_dwordx4 v[48:51], v1, s[4:7], s98 offen offset:2048 sc1\n"
"ds_read_b128 v[76:79], v2 offset:17408\n"
"v_exp_f32_e32 v80, v80\n"
"v_exp_f32_e32 v81, v81\n"
"v_mfma_f32_16x16x32_bf16 v[176:179], v[72:75], v[56:59], 0\n"
"ds_read_b128 v[72:75], v2 offset:49152\n"
"v_exp_f32_e32 v82, v82\n"
"v_exp_f32_e32 v83, v83\n"
"v_mfma_f32_16x16x32_bf16 v[180:183], v[88:91], v[56:59], 0\n"
"ds_read_b128 v[84:87], v2 offset:50176\n"
"v_cvt_pk_bf16_f32 v184, v68, v69\n"
"v_cvt_pk_bf16_f32 v185, v70, v71\n"
"v_cvt_pk_bf16_f32 v186, v80, v81\n"
"v_cvt_pk_bf16_f32 v187, v82, v83\n"
"s_waitcnt lgkmcnt(8)\n"
"s_cmp_eq_i32 s78, 5\n"
"s_cbranch_scc1 .Lmk_x108s\n"
".Lmk_x108r:\n"
"v_mfma_f32_16x16x32_bf16 v[80:83], v[132:135], v[92:95], v[116:119]\n"
"ds_read_b64_tr_b16 v[68:69], v16 offset:10240\n"
"ds_read_b64_tr_b16 v[70:71], v255 offset:10496\n"
"buffer_load_dwordx4 v[52:55], v1, s[4:7], s98 offen offset:3072 sc1\n"
"v_exp_f32_e32 v128, v128\n"
"v_exp_f32_e32 v129, v129\n"
"v_mfma_f32_16x16x32_bf16 v[188:191], v[132:135], v[96:99], v[120:123]\n"
"ds_read_b64_tr_b16 v[132:133], v255 offset:10240\n"
"v_exp_f32_e32 v130, v130\n"
"v_exp_f32_e32 v131, v131\n"
"v_mfma_f32_16x16x32_bf16 v[192:195], v[136:139], v[96:99], v[120:123]\n"
"ds_read_b64_tr_b16 v[134:135], v16 offset:10496\n"
"v_exp_f32_e32 v152, v152\n"
"v_exp_f32_e32 v153, v153\n"
"v_exp_f32_e32 v154, v154\n"
"v_mfma_f32_16x16x32_bf16 v[196:199], v[136:139], v[92:95], v[116:119]\n"
"s_waitcnt lgkmcnt(4)\n"
"s_add_u32 s97, s69, 0x60a000\n"
"buffer_load_dwordx4 v[56:59], v0, s[4:7], s97 offen sc1\n"
"v_max_u32_e32 v248, v164, v165\n"
"v_exp_f32_e32 v155, v155\n"
"v_mfma_f32_16x16x32_bf16 v[156:159], v[88:91], v[160:163], v[156:159]\n"
"v_max3_u32 v248, v248, v166, v167\n"
"v_cvt_pk_bf16_f32 v136, v128, v129\n"
"v_cvt_pk_bf16_f32 v137, v130, v131\n"
"v_cvt_pk_bf16_f32 v138, v152, v153\n"
"v_mfma_f32_16x16x32_bf16 v[124:127], v[148:151], v[160:163], v[124:127]\n"
"v_max3_u32 v248, v248, v76, v77\n"
"v_cvt_pk_bf16_f32 v139, v154, v155\n"
"v_exp_f32_e32 v140, v140\n"
"v_max3_u32 v248, v248, v78, v79\n"
"v_exp_f32_e32 v141, v141\n"
"v_mfma_f32_16x16x32_bf16 v[168:171], v[64:67], v[160:163], v[168:171]\n"
"v_exp_f32_e32 v142, v142\n"
"v_max3_u32 v248, v248, v72, v73\n"
"v_exp_f32_e32 v143, v143\n"
"v_mfma_f32_16x16x32_bf16 v[172:175], v[64:67], v[184:187], v[172:175]\n"
"buffer_load_dwordx4 v[60:63], v0, s[4:7], s97 offen offset:1024 sc1\n"
"v_max3_u32 v248, v248, v74, v75\n"
"v_exp_f32_e32 v144, v144\n"
"v_exp_f32_e32 v145, v145\n"
"v_mfma_f32_16x16x32_bf16 v[176:179], v[148:151], v[184:187], v[176:179]\n"
"v_max3_u32 v248, v248, v84, v85\n"
"v_exp_f32_e32 v146, v146\n"
"v_max3_u32 v248, v248, v86, v87\n"
"v_exp_f32_e32 v147, v147\n"
"v_mfma_f32_16x16x32_bf16 v[180:183], v[88:91], v[184:187], v[180:183]\n"
"v_cvt_pk_bf16_f32 v128, v140, v141\n"
"v_cmp_eq_u32_e32 vcc, -1, v248\n"
"v_cvt_pk_bf16_f32 v129, v142, v143\n"
"v_cvt_pk_bf16_f32 v130, v144, v145\n"
"v_cvt_pk_bf16_f32 v131, v146, v147\n"
"s_cbranch_vccnz .Lmk_h95s\n"
".Lmk_h95r:\n"
"s_cmp_lg_u32 s2, 0\n"
"s_cbranch_scc1 .Lmk_s109\n"
"s_barrier\n"
".Lmk_s109:\n"
"ds_read_b128 v[72:75], v15 offset:6144\n"
"ds_read_b128 v[76:79], v254 offset:6400\n"
"s_waitcnt lgkmcnt(2)\n"
"v_mfma_f32_16x16x32_bf16 v[156:159], v[88:91], v[136:139], v[156:159]\n"
"ds_read_b128 v[84:87], v15 offset:4096\n"
"v_exp_f32_e32 v80, v80\n"
"v_exp_f32_e32 v81, v81\n"
"v_exp_f32_e32 v82, v82\n"
"v_mfma_f32_16x16x32_bf16 v[124:127], v[68:71], v[136:139], v[124:127]\n"
"buffer_load_dwordx4 v[64:67], v0, s[4:7], s97 offen offset:2048 sc1\n"
"ds_read_b128 v[140:143], v254 offset:4352\n"
"v_exp_f32_e32 v83, v83\n"
"v_mfma_f32_16x16x32_bf16 v[168:171], v[132:135], v[136:139], v[168:171]\n"
"ds_read_b64_tr_b16 v[136:137], v16 offset:8192\n"
"v_exp_f32_e32 v196, v196\n"
"v_exp_f32_e32 v197, v197\n"
"v_mfma_f32_16x16x32_bf16 v[172:175], v[132:135], v[128:131], v[172:175]\n"
"ds_read_b64_tr_b16 v[138:139], v255 offset:8448\n"
"v_exp_f32_e32 v198, v198\n"
"v_exp_f32_e32 v199, v199\n"
"v_mfma_f32_16x16x32_bf16 v[176:179], v[68:71], v[128:131], v[176:179]\n"
"buffer_load_dwordx4 v[68:71], v0, s[4:7], s97 offen offset:3072 sc1\n"
"v_cvt_pk_bf16_f32 v132, v80, v81\n"
"v_cvt_pk_bf16_f32 v133, v82, v83\n"
"v_cvt_pk_bf16_f32 v134, v196, v197\n"
"v_cvt_pk_bf16_f32 v135, v198, v199\n"
"v_mfma_f32_16x16x32_bf16 v[180:183], v[88:91], v[128:131], v[180:183]\n"
"ds_read_b64_tr_b16 v[128:129], v255 offset:8192\n"
"v_exp_f32_e32 v188, v188\n"
"v_exp_f32_e32 v189, v189\n"
"s_waitcnt lgkmcnt(5)\n"
"v_mfma_f32_16x16x32_bf16 v[144:147], v[72:75], v[92:95], v[116:119]\n"
"ds_read_b64_tr_b16 v[130:131], v16 offset:8448\n"
"v_exp_f32_e32 v190, v190\n"
"v_exp_f32_e32 v191, v191\n"
"v_exp_f32_e32 v192, v192\n"
"v_mfma_f32_16x16x32_bf16 v[148:151], v[72:75], v[96:99], v[120:123]\n"
"buffer_load_dwordx4 v[72:75], v1, s[4:7], s97 offen sc1\n"
"s_waitcnt vmcnt(12)\n"
"v_cmp_ne_u64_e32 vcc, v[246:247], v[18:19]\n"
"v_mfma_f32_16x16x32_bf16 v[152:155], v[76:79], v[96:99], v[120:123]\n"
"v_exp_f32_e32 v193, v193\n"
"v_exp_f32_e32 v194, v194\n"
"v_exp_f32_e32 v195, v195\n"
"v_mfma_f32_16x16x32_bf16 v[160:163], v[76:79], v[92:95], v[116:119]\n"
"s_cbranch_vccnz .Lmk_f79s\n"
".Lmk_f79r:\n"
"v_cvt_pk_bf16_f32 v164, v188, v189\n"
"v_cvt_pk_bf16_f32 v165, v190, v191\n"
"v_cvt_pk_bf16_f32 v166, v192, v193\n"
"v_cvt_pk_bf16_f32 v167, v194, v195\n"
"s_waitcnt lgkmcnt(4)\n"
"v_mfma_f32_16x16x32_bf16 v[184:187], v[84:87], v[92:95], v[116:119]\n"
"ds_read_b128 v[188:191], v15 offset:2048\n"
"v_exp_f32_e32 v144, v144\n"
"v_exp_f32_e32 v145, v145\n"
"v_exp_f32_e32 v146, v146\n"
"v_mfma_f32_16x16x32_bf16 v[192:195], v[84:87], v[96:99], v[120:123]\n"
"buffer_load_dwordx4 v[76:79], v1, s[4:7], s97 offen offset:1024 sc1\n"
"v_exp_f32_e32 v147, v147\n"
"v_mfma_f32_16x16x32_bf16 v[196:199], v[140:143], v[96:99], v[120:123]\n"
"ds_read_b128 v[200:203], v254 offset:2304\n"
"v_exp_f32_e32 v160, v160\n"
"v_exp_f32_e32 v161, v161\n"
"v_mfma_f32_16x16x32_bf16 v[204:207], v[140:143], v[92:95], v[116:119]\n"
"ds_read_b64_tr_b16 v[140:141], v16 offset:6144\n"
"v_exp_f32_e32 v162, v162\n"
"v_exp_f32_e32 v163, v163\n"
"s_waitcnt lgkmcnt(3)\n"
"v_mfma_f32_16x16x32_bf16 v[156:159], v[88:91], v[132:135], v[156:159]\n"
"buffer_load_dwordx4 v[80:83], v1, s[4:7], s97 offen offset:2048 sc1\n"
"v_cvt_pk_bf16_f32 v208, v144, v145\n"
"v_cvt_pk_bf16_f32 v209, v146, v147\n"
"v_cvt_pk_bf16_f32 v210, v160, v161\n"
"v_mfma_f32_16x16x32_bf16 v[124:127], v[136:139], v[132:135], v[124:127]\n"
"ds_read_b64_tr_b16 v[142:143], v255 offset:6400\n"
"v_cvt_pk_bf16_f32 v211, v162, v163\n"
"v_exp_f32_e32 v148, v148\n"
"v_exp_f32_e32 v149, v149\n"
"v_mfma_f32_16x16x32_bf16 v[168:171], v[128:131], v[132:135], v[168:171]\n"
"ds_read_b64_tr_b16 v[132:133], v255 offset:6144\n"
"v_exp_f32_e32 v150, v150\n"
"v_exp_f32_e32 v151, v151\n"
"v_mfma_f32_16x16x32_bf16 v[172:175], v[128:131], v[164:167], v[172:175]\n"
"buffer_load_dwordx4 v[84:87], v1, s[4:7], s97 offen offset:3072 sc1\n"
"v_exp_f32_e32 v152, v152\n"
"v_exp_f32_e32 v153, v153\n"
"v_mfma_f32_16x16x32_bf16 v[176:179], v[136:139], v[164:167], v[176:179]\n"
"ds_read_b64_tr_b16 v[134:135], v16 offset:6400\n"
"v_exp_f32_e32 v154, v154\n"
"v_exp_f32_e32 v155, v155\n"
"v_mfma_f32_16x16x32_bf16 v[180:183], v[88:91], v[164:167], v[180:183]\n"
"s_waitcnt lgkmcnt(4)\n"
"v_cvt_pk_bf16_f32 v128, v148, v149\n"
"v_cvt_pk_bf16_f32 v129, v150, v151\n"
"v_cvt_pk_bf16_f32 v130, v152, v153\n"
"v_cvt_pk_bf16_f32 v131, v154, v155\n"
"v_mfma_f32_16x16x32_bf16 v[136:139], v[188:191], v[92:95], v[116:119]\n"
"ds_read_b128 v[144:147], v15\n"
"v_exp_f32_e32 v184, v184\n"
"v_exp_f32_e32 v185, v185\n"
"v_exp_f32_e32 v186, v186\n"
"v_mfma_f32_16x16x32_bf16 v[148:151], v[188:191], v[96:99], v[120:123]\n"
"s_add_u32 s97, s56, 0x40000\n"
"buffer_load_dwordx4 v[152:155], v0, s[4:7], s97 offen sc1\n"
"v_exp_f32_e32 v187, v187\n"
"v_mfma_f32_16x16x32_bf16 v[160:163], v[200:203], v[96:99], v[120:123]\n"
"ds_read_b128 v[164:167], v254 offset:256\n"
"v_exp_f32_e32 v204, v204\n"
"v_exp_f32_e32 v205, v205\n"
"v_exp_f32_e32 v206, v206\n"
"v_mfma_f32_16x16x32_bf16 v[188:191], v[200:203], v[92:95], v[116:119]\n"
"buffer_load_dwordx4 v[200:203], v0, s[4:7], s97 offen offset:1024 sc1\n"
"ds_read_b64_tr_b16 v[212:213], v16 offset:4096\n"
"v_exp_f32_e32 v207, v207\n"
"s_waitcnt lgkmcnt(3)\n"
"v_mfma_f32_16x16x32_bf16 v[156:159], v[88:91], v[208:211], v[156:159]\n"
"v_cvt_pk_bf16_f32 v216, v184, v185\n"
"v_cvt_pk_bf16_f32 v217, v186, v187\n"
"v_cvt_pk_bf16_f32 v218, v204, v205\n"
"v_cvt_pk_bf16_f32 v219, v206, v207\n"
"v_mfma_f32_16x16x32_bf16 v[124:127], v[140:143], v[208:211], v[124:127]\n"
"buffer_load_dwordx4 v[184:187], v0, s[4:7], s97 offen offset:2048 sc1\n"
"ds_read_b64_tr_b16 v[214:215], v255 offset:4352\n"
"v_exp_f32_e32 v192, v192\n"
"v_exp_f32_e32 v193, v193\n"
"v_mfma_f32_16x16x32_bf16 v[168:171], v[132:135], v[208:211], v[168:171]\n"
"buffer_load_dwordx4 v[204:207], v0, s[4:7], s97 offen offset:3072 sc1\n"
"v_exp_f32_e32 v194, v194\n"
"v_exp_f32_e32 v195, v195\n"
"v_mfma_f32_16x16x32_bf16 v[172:175], v[132:135], v[128:131], v[172:175]\n"
"ds_read_b64_tr_b16 v[132:133], v255 offset:4096\n"
"v_exp_f32_e32 v196, v196\n"
"v_exp_f32_e32 v197, v197\n"
"v_mfma_f32_16x16x32_bf16 v[176:179], v[140:143], v[128:131], v[176:179]\n"
"buffer_load_dwordx4 v[140:143], v1, s[4:7], s97 offen sc1\n"
"ds_read_b64_tr_b16 v[134:135], v16 offset:4352\n"
"v_exp_f32_e32 v198, v198\n"
"v_exp_f32_e32 v199, v199\n"
"v_mfma_f32_16x16x32_bf16 v[180:183], v[88:91], v[128:131], v[180:183]\n"
"s_waitcnt lgkmcnt(4)\n"
"v_cvt_pk_bf16_f32 v128, v192, v193\n"
"v_cvt_pk_bf16_f32 v129, v194, v195\n"
"v_cvt_pk_bf16_f32 v130, v196, v197\n"
"v_cvt_pk_bf16_f32 v131, v198, v199\n"
"v_mfma_f32_16x16x32_bf16 v[192:195], v[144:147], v[92:95], v[116:119]\n"
"ds_read_b64_tr_b16 v[196:197], v16 offset:2048\n"
"ds_read_b64_tr_b16 v[198:199], v255 offset:2304\n"
"buffer_load_dwordx4 v[208:211], v1, s[4:7], s97 offen offset:1024 sc1\n"
"v_exp_f32_e32 v136, v136\n"
"v_exp_f32_e32 v137, v137\n"
"v_mfma_f32_16x16x32_bf16 v[220:223], v[144:147], v[96:99], v[120:123]\n"
"ds_read_b64_tr_b16 v[144:145], v255 offset:2048\n"
"v_exp_f32_e32 v138, v138\n"
"v_exp_f32_e32 v139, v139\n"
"v_mfma_f32_16x16x32_bf16 v[224:227], v[164:167], v[96:99], v[120:123]\n"
"buffer_load_dwordx4 v[120:123], v1, s[4:7], s97 offen offset:2048 sc1\n"
"v_exp_f32_e32 v188, v188\n"
"v_exp_f32_e32 v189, v189\n"
"v_mfma_f32_16x16x32_bf16 v[228:231], v[164:167], v[92:95], v[116:119]\n"
"ds_read_b64_tr_b16 v[146:147], v16 offset:2304\n"
"ds_read_b64_tr_b16 v[116:117], v16 offset:16384\n"
"v_exp_f32_e32 v190, v190\n"
"v_exp_f32_e32 v191, v191\n"
"s_waitcnt lgkmcnt(5)\n"
"v_mfma_f32_16x16x32_bf16 v[156:159], v[88:91], v[216:219], v[156:159]\n"
"v_cvt_pk_bf16_f32 v164, v136, v137\n"
"v_cvt_pk_bf16_f32 v165, v138, v139\n"
"v_cvt_pk_bf16_f32 v166, v188, v189\n"
"v_mfma_f32_16x16x32_bf16 v[124:127], v[212:215], v[216:219], v[124:127]\n"
"buffer_load_dwordx4 v[136:139], v1, s[4:7], s97 offen offset:3072 sc1\n"
"ds_read_b64_tr_b16 v[118:119], v255 offset:16640\n"
"ds_read_b64_tr_b16 v[188:189], v255 offset:16384\n"
"v_cvt_pk_bf16_f32 v167, v190, v191\n"
"v_exp_f32_e32 v148, v148\n"
"v_exp_f32_e32 v149, v149\n"
"v_exp_f32_e32 v150, v150\n"
"v_mfma_f32_16x16x32_bf16 v[168:171], v[132:135], v[216:219], v[168:171]\n"
"s_add_u32 s97, s57, 0x40000\n"
"buffer_load_dwordx4 v[216:219], v0, s[4:7], s97 offen sc1\n"
"ds_read_b64_tr_b16 v[190:191], v16 offset:16640\n"
"v_exp_f32_e32 v151, v151\n"
"v_mfma_f32_16x16x32_bf16 v[172:175], v[132:135], v[128:131], v[172:175]\n"
"v_exp_f32_e32 v160, v160\n"
"v_add_u32_e32 v248, 0x26400, v0\n"
"v_exp_f32_e32 v161, v161\n"
"v_mfma_f32_16x16x32_bf16 v[176:179], v[212:215], v[128:131], v[176:179]\n"
"buffer_load_dwordx4 v[132:135], v0, s[4:7], s97 offen offset:1024 sc1\n"
"ds_read_b128 v[212:215], v248\n"
"v_exp_f32_e32 v162, v162\n"
"v_exp_f32_e32 v163, v163\n"
"v_mfma_f32_16x16x32_bf16 v[180:183], v[88:91], v[128:131], v[180:183]\n"
"ds_read_b128 v[232:235], v248 offset:1024\n"
"v_cvt_pk_bf16_f32 v128, v148, v149\n"
"v_cvt_pk_bf16_f32 v129, v150, v151\n"
"v_cvt_pk_bf16_f32 v130, v160, v161\n"
"v_cvt_pk_bf16_f32 v131, v162, v163\n"
"s_waitcnt lgkmcnt(6)\n"
"v_mfma_f32_16x16x32_bf16 v[156:159], v[88:91], v[164:167], v[156:159]\n"
"ds_read_b64_tr_b16 v[148:149], v16\n"
"buffer_load_dwordx4 v[160:163], v0, s[4:7], s97 offen offset:2048 sc1\n"
"v_cndmask_b32_e64 v192, v100, v192, s[20:21]\n"
"v_cndmask_b32_e64 v193, v101, v193, s[22:23]\n"
"v_cndmask_b32_e64 v194, v102, v194, s[24:25]\n"
"v_cndmask_b32_e64 v195, v103, v195, s[26:27]\n"
"v_cndmask_b32_e64 v228, v112, v228, s[28:29]\n"
"v_cndmask_b32_e64 v229, v113, v229, s[30:31]\n"
"v_cndmask_b32_e64 v230, v114, v230, s[32:33]\n"
"v_cndmask_b32_e64 v231, v115, v231, s[34:35]\n"
"v_exp_f32_e32 v192, v192\n"
"v_mfma_f32_16x16x32_bf16 v[124:127], v[196:199], v[164:167], v[124:127]\n"
"ds_read_b64_tr_b16 v[150:151], v255 offset:256\n"
"buffer_load_dwordx4 v[100:103], v0, s[4:7], s97 offen offset:3072 sc1\n"
"v_exp_f32_e32 v193, v193\n"
"v_exp_f32_e32 v194, v194\n"
"v_exp_f32_e32 v195, v195\n"
"v_exp_f32_e32 v228, v228\n"
"v_mfma_f32_16x16x32_bf16 v[168:171], v[144:147], v[164:167], v[168:171]\n"
"buffer_load_dwordx4 v[164:167], v1, s[4:7], s97 offen sc1\n"
"v_exp_f32_e32 v229, v229\n"
"v_exp_f32_e32 v230, v230\n"
"v_exp_f32_e32 v231, v231\n"
"v_cvt_pk_bf16_f32 v112, v192, v193\n"
"v_cvt_pk_bf16_f32 v113, v194, v195\n"
"v_cvt_pk_bf16_f32 v114, v228, v229\n"
"v_mfma_f32_16x16x32_bf16 v[172:175], v[144:147], v[128:131], v[172:175]\n"
"ds_read_b64_tr_b16 v[144:145], v255\n"
"v_cvt_pk_bf16_f32 v115, v230, v231\n"
"v_cndmask_b32_e64 v220, v104, v220, s[36:37]\n"
"v_cndmask_b32_e64 v221, v105, v221, s[38:39]\n"
"v_cndmask_b32_e64 v222, v106, v222, s[40:41]\n"
"v_cndmask_b32_e64 v223, v107, v223, s[42:43]\n"
"v_cndmask_b32_e64 v224, v108, v224, s[44:45]\n"
"v_cndmask_b32_e64 v225, v109, v225, s[46:47]\n"
"v_cndmask_b32_e64 v226, v110, v226, s[48:49]\n"
"v_cndmask_b32_e64 v227, v111, v227, s[50:51]\n"
"v_mfma_f32_16x16x32_bf16 v[176:179], v[196:199], v[128:131], v[176:179]\n"
"buffer_load_dwordx4 v[104:107], v1, s[4:7], s97 offen offset:1024 sc1\n"
"ds_read_b64_tr_b16 v[146:147], v16 offset:256\n"
"buffer_load_dwordx4 v[108:111], v1, s[4:7], s97 offen offset:2048 sc1\n"
"v_exp_f32_e32 v220, v220\n"
"v_exp_f32_e32 v221, v221\n"
"v_exp_f32_e32 v222, v222\n"
"v_exp_f32_e32 v223, v223\n"
"v_exp_f32_e32 v224, v224\n"
"v_mfma_f32_16x16x32_bf16 v[180:183], v[88:91], v[128:131], v[180:183]\n"
"s_waitcnt lgkmcnt(0)\n"
"v_exp_f32_e32 v225, v225\n"
"v_exp_f32_e32 v226, v226\n"
"v_exp_f32_e32 v227, v227\n"
"v_cvt_pk_bf16_f32 v128, v220, v221\n"
"v_cvt_pk_bf16_f32 v129, v222, v223\n"
"v_cvt_pk_bf16_f32 v130, v224, v225\n"
"v_cvt_pk_bf16_f32 v131, v226, v227\n"
"v_mfma_f32_16x16x32_bf16 v[156:159], v[88:91], v[112:115], v[156:159]\n"
"v_and_b32_e32 v212, v112, v212\n"
"v_and_b32_e32 v213, v113, v213\n"
"v_and_b32_e32 v214, v114, v214\n"
"v_and_b32_e32 v215, v115, v215\n"
"v_mfma_f32_16x16x32_bf16 v[180:183], v[88:91], v[128:131], v[180:183]\n"
"v_xor_b32_e32 v192, v112, v212\n"
"v_xor_b32_e32 v193, v113, v213\n"
"v_xor_b32_e32 v194, v114, v214\n"
"v_xor_b32_e32 v195, v115, v215\n"
"v_mfma_f32_16x16x32_bf16 v[124:127], v[148:151], v[212:215], v[124:127]\n"
"v_and_b32_e32 v232, v128, v232\n"
"v_and_b32_e32 v233, v129, v233\n"
"v_mfma_f32_16x16x32_bf16 v[168:171], v[144:147], v[212:215], v[168:171]\n"
"v_and_b32_e32 v234, v130, v234\n"
"v_and_b32_e32 v235, v131, v235\n"
"v_mfma_f32_16x16x32_bf16 v[124:127], v[116:119], v[192:195], v[124:127]\n"
"v_xor_b32_e32 v112, v128, v232\n"
"v_xor_b32_e32 v113, v129, v233\n"
"v_mfma_f32_16x16x32_bf16 v[168:171], v[188:191], v[192:195], v[168:171]\n"
"v_xor_b32_e32 v114, v130, v234\n"
"v_xor_b32_e32 v115, v131, v235\n"
"v_mfma_f32_16x16x32_bf16 v[176:179], v[148:151], v[232:235], v[176:179]\n"
"buffer_load_dwordx4 v[128:131], v1, s[4:7], s97 offen offset:3072 sc1\n"
"s_add_u32 s97, s58, 0x40000\n"
"buffer_load_dwordx4 v[148:151], v0, s[4:7], s97 offen sc1\n"
"v_mfma_f32_16x16x32_bf16 v[172:175], v[144:147], v[232:235], v[172:175]\n"
"buffer_load_dwordx4 v[144:147], v0, s[4:7], s97 offen offset:1024 sc1\n"
"v_mfma_f32_16x16x32_bf16 v[176:179], v[116:119], v[112:115], v[176:179]\n"
"buffer_load_dwordx4 v[116:119], v0, s[4:7], s97 offen offset:2048 sc1\n"
"v_mfma_f32_16x16x32_bf16 v[172:175], v[188:191], v[112:115], v[172:175]\n"
"buffer_load_dwordx4 v[112:115], v0, s[4:7], s97 offen offset:3072 sc1\n"
"v_max_f32_e32 v248, v156, v180\n"
"v_rcp_f32_e32 v156, v156\n"
"v_rcp_f32_e32 v180, v180\n"
"s_nop 0\n"
"v_max3_f32 v248, v248, v156, v180\n"
"v_cmp_nlt_f32_e32 vcc, v248, v252\n"
"s_cbranch_vccnz .Lmk_b105\n"
".Lmk_k110:\n"
"v_mul_f32_e32 v124, v124, v156\n"
"v_mul_f32_e32 v125, v125, v156\n"
"v_mul_f32_e32 v126, v126, v156\n"
"v_mul_f32_e32 v127, v127, v156\n"
"v_mul_f32_e32 v168, v168, v156\n"
"v_mul_f32_e32 v169, v169, v156\n"
"v_mul_f32_e32 v170, v170, v156\n"
"v_mul_f32_e32 v171, v171, v156\n"
"v_cvt_pk_bf16_f32 v88, v124, v125\n"
"v_cvt_pk_bf16_f32 v89, v126, v127\n"
"v_cvt_pk_bf16_f32 v90, v168, v169\n"
"v_cvt_pk_bf16_f32 v91, v170, v171\n"
"buffer_store_dwordx4 v[88:91], v0, s[4:7], s60 offen sc1\n"
"s_add_u32 s98, s60, 0x8000\n"
"v_mul_f32_e32 v176, v176, v180\n"
"v_mul_f32_e32 v177, v177, v180\n"
"v_mul_f32_e32 v178, v178, v180\n"
"v_mul_f32_e32 v179, v179, v180\n"
"v_mul_f32_e32 v172, v172, v180\n"
"v_mul_f32_e32 v173, v173, v180\n"
"v_mul_f32_e32 v174, v174, v180\n"
"v_mul_f32_e32 v175, v175, v180\n"
"v_cvt_pk_bf16_f32 v88, v176, v177\n"
"v_cvt_pk_bf16_f32 v89, v178, v179\n"
"v_cvt_pk_bf16_f32 v90, v172, v173\n"
"v_cvt_pk_bf16_f32 v91, v174, v175\n"
"buffer_store_dwordx4 v[88:91], v0, s[4:7], s98 offen sc1\n"
";\n"
"s_waitcnt vmcnt(20)\n"
"; @A G2 b.0\n"
"s_nop 0\n"
"v_mfma_f32_16x16x32_bf16 v[88:91], a[128:131], v[152:155], 0\n"
"v_mfma_f32_16x16x32_bf16 v[88:91], a[132:135], v[200:203], v[88:91]\n"
"s_waitcnt vmcnt(18)\n"
"v_mfma_f32_16x16x32_bf16 v[88:91], a[136:139], v[184:187], v[88:91]\n"
"buffer_load_dwordx4 v[92:95], v1, s[4:7], s97 offen sc1\n"
"v_mfma_f32_16x16x32_bf16 v[88:91], a[140:143], v[204:207], v[88:91]\n"
"s_waitcnt vmcnt(17)\n"
"v_mfma_f32_16x16x32_bf16 v[88:91], a[144:147], v[140:143], v[88:91]\n"
"v_mfma_f32_16x16x32_bf16 v[88:91], a[148:151], v[208:211], v[88:91]\n"
"s_waitcnt vmcnt(15)\n"
"v_mfma_f32_16x16x32_bf16 v[88:91], a[152:155], v[120:123], v[88:91]\n"
"buffer_load_dwordx4 v[96:99], v1, s[4:7], s97 offen offset:1024 sc1\n"
"v_mfma_f32_16x16x32_bf16 v[88:91], a[156:159], v[136:139], v[88:91]\n"
"v_mfma_f32_16x16x32_bf16 v[124:127], a[188:191], v[136:139], 0\n"
"v_mfma_f32_16x16x32_bf16 v[124:127], a[184:187], v[120:123], v[124:127]\n"
"v_mfma_f32_16x16x32_bf16 v[124:127], a[180:183], v[208:211], v[124:127]\n"
"buffer_load_dwordx4 v[156:159], v1, s[4:7], s97 offen offset:2048 sc1\n"
"v_mfma_f32_16x16x32_bf16 v[124:127], a[176:179], v[140:143], v[124:127]\n"
"v_mfma_f32_16x16x32_bf16 v[124:127], a[172:175], v[204:207], v[124:127]\n"
"s_nop 1\n"
"v_cmp_u_f32_e32 vcc, v88, v88\n"
"v_mfma_f32_16x16x32_bf16 v[124:127], a[168:171], v[184:187], v[124:127]\n"
"s_cbranch_vccnz .Lmk_p111s\n"
".Lmk_p111r:\n"
"v_mfma_f32_16x16x32_bf16 v[124:127], a[164:167], v[200:203], v[124:127]\n"
"ds_write_b128 v3, v[88:91]\n"
"v_mfma_f32_16x16x32_bf16 v[124:127], a[160:163], v[152:155], v[124:127]\n"
"buffer_load_dwordx4 v[88:91], v1, s[4:7], s97 offen offset:3072 sc1\n"
"v_mfma_f32_16x16x32_bf16 v[168:171], a[192:195], v[152:155], 0\n"
"v_mfma_f32_16x16x32_bf16 v[168:171], a[196:199], v[200:203], v[168:171]\n"
"s_add_u32 s98, s59, 0x40000\n"
"v_mfma_f32_16x16x32_bf16 v[168:171], a[200:203], v[184:187], v[168:171]\n"
"v_mfma_f32_16x16x32_bf16 v[168:171], a[204:207], v[204:207], v[168:171]\n"
"buffer_load_dwordx4 v[172:175], v0, s[4:7], s98 offen sc1\n"
"v_mfma_f32_16x16x32_bf16 v[168:171], a[208:211], v[140:143], v[168:171]\n"
"v_mfma_f32_16x16x32_bf16 v[168:171], a[212:215], v[208:211], v[168:171]\n"
"ds_write_b128 v3, v[124:127] offset:1024\n"
"v_mfma_f32_16x16x32_bf16 v[168:171], a[216:219], v[120:123], v[168:171]\n"
"v_mfma_f32_16x16x32_bf16 v[168:171], a[220:223], v[136:139], v[168:171]\n"
"buffer_load_dwordx4 v[124:127], v0, s[4:7], s98 offen offset:1024 sc1\n"
"v_mfma_f32_16x16x32_bf16 v[176:179], a[252:255], v[136:139], 0\n"
"v_mfma_f32_16x16x32_bf16 v[176:179], a[248:251], v[120:123], v[176:179]\n"
"v_mfma_f32_16x16x32_bf16 v[176:179], a[244:247], v[208:211], v[176:179]\n"
"v_mfma_f32_16x16x32_bf16 v[176:179], a[240:243], v[140:143], v[176:179]\n"
"buffer_load_dwordx4 v[120:123], v0, s[4:7], s98 offen offset:2048 sc1\n"
"v_mfma_f32_16x16x32_bf16 v[176:179], a[236:239], v[204:207], v[176:179]\n"
"v_mfma_f32_16x16x32_bf16 v[176:179], a[232:235], v[184:187], v[176:179]\n"
"ds_write_b128 v3, v[168:171] offset:2048\n"
"v_mfma_f32_16x16x32_bf16 v[176:179], a[228:231], v[200:203], v[176:179]\n"
"v_mfma_f32_16x16x32_bf16 v[176:179], a[224:227], v[152:155], v[176:179]\n"
"s_waitcnt vmcnt(17)\n"
"buffer_load_dwordx4 v[136:139], v0, s[4:7], s98 offen offset:3072 sc1\n"
"; @A G2 b.1\n"
"v_mfma_f32_16x16x32_bf16 v[140:143], a[128:131], v[216:219], 0\n"
"v_mfma_f32_16x16x32_bf16 v[140:143], a[132:135], v[132:135], v[140:143]\n"
"v_mfma_f32_16x16x32_bf16 v[140:143], a[136:139], v[160:163], v[140:143]\n"
"v_mfma_f32_16x16x32_bf16 v[140:143], a[140:143], v[100:103], v[140:143]\n"
"s_waitcnt vmcnt(14)\n"
"buffer_load_dwordx4 v[152:155], v1, s[4:7], s98 offen sc1\n"
"v_mfma_f32_16x16x32_bf16 v[140:143], a[144:147], v[164:167], v[140:143]\n"
"v_mfma_f32_16x16x32_bf16 v[140:143], a[148:151], v[104:107], v[140:143]\n"
"ds_write_b128 v3, v[176:179] offset:3072\n"
"v_mfma_f32_16x16x32_bf16 v[140:143], a[152:155], v[108:111], v[140:143]\n"
"v_mfma_f32_16x16x32_bf16 v[140:143], a[156:159], v[128:131], v[140:143]\n"
"buffer_load_dwordx4 v[168:171], v1, s[4:7], s98 offen offset:1024 sc1\n"
"v_mfma_f32_16x16x32_bf16 v[176:179], a[188:191], v[128:131], 0\n"
"v_mfma_f32_16x16x32_bf16 v[176:179], a[184:187], v[108:111], v[176:179]\n"
"v_mfma_f32_16x16x32_bf16 v[176:179], a[180:183], v[104:107], v[176:179]\n"
"v_mfma_f32_16x16x32_bf16 v[176:179], a[176:179], v[164:167], v[176:179]\n"
"buffer_load_dwordx4 v[180:183], v1, s[4:7], s98 offen offset:2048 sc1\n"
"v_mfma_f32_16x16x32_bf16 v[176:179], a[172:175], v[100:103], v[176:179]\n"
"s_nop 0\n"
"v_cmp_u_f32_e32 vcc, v140, v140\n"
"v_mfma_f32_16x16x32_bf16 v[176:179], a[168:171], v[160:163], v[176:179]\n"
"s_cbranch_vccnz .Lmk_p112s\n"
".Lmk_p112r:\n"
"v_mfma_f32_16x16x32_bf16 v[176:179], a[164:167], v[132:135], v[176:179]\n"
"ds_write_b128 v3, v[140:143] offset:6144\n"
"v_mfma_f32_16x16x32_bf16 v[176:179], a[160:163], v[216:219], v[176:179]\n"
"buffer_load_dwordx4 v[140:143], v1, s[4:7], s98 offen offset:3072 sc1\n"
"v_mfma_f32_16x16x32_bf16 v[184:187], a[192:195], v[216:219], 0\n"
"v_mfma_f32_16x16x32_bf16 v[184:187], a[196:199], v[132:135], v[184:187]\n"
"v_mfma_f32_16x16x32_bf16 v[184:187], a[200:203], v[160:163], v[184:187]\n"
"v_mfma_f32_16x16x32_bf16 v[184:187], a[204:207], v[100:103], v[184:187]\n"
"v_mfma_f32_16x16x32_bf16 v[184:187], a[208:211], v[164:167], v[184:187]\n"
"ds_read_b128 v[188:191], v2 offset:8192\n"
"ds_read_b128 v[192:195], v2 offset:9216\n"
"v_mfma_f32_16x16x32_bf16 v[184:187], a[212:215], v[104:107], v[184:187]\n"
"ds_write_b128 v3, v[176:179] offset:7168\n"
"v_mfma_f32_16x16x32_bf16 v[184:187], a[216:219], v[108:111], v[184:187]\n"
"v_mfma_f32_16x16x32_bf16 v[184:187], a[220:223], v[128:131], v[184:187]\n"
"v_mfma_f32_16x16x32_bf16 v[176:179], a[252:255], v[128:131], 0\n"
"v_mfma_f32_16x16x32_bf16 v[176:179], a[248:251], v[108:111], v[176:179]\n"
"s_waitcnt lgkmcnt(1)\n"
"v_lshlrev_b32_e32 v255, 16, v188\n"
"v_and_b32_e32 v254, 0xffff0000, v188\n"
"v_mfma_f32_16x16x32_bf16 v[176:179], a[244:247], v[104:107], v[176:179]\n"
"v_lshlrev_b32_e32 v253, 16, v189\n"
"v_and_b32_e32 v252, 0xffff0000, v189\n"
"v_mfma_f32_16x16x32_bf16 v[176:179], a[240:243], v[164:167], v[176:179]\n"
"v_lshlrev_b32_e32 v251, 16, v190\n"
"v_and_b32_e32 v250, 0xffff0000, v190\n"
"v_mfma_f32_16x16x32_bf16 v[176:179], a[236:239], v[100:103], v[176:179]\n"
"v_lshlrev_b32_e32 v249, 16, v191\n"
"v_and_b32_e32 v248, 0xffff0000, v191\n"
"v_mfma_f32_16x16x32_bf16 v[176:179], a[232:235], v[160:163], v[176:179]\n"
"ds_write_b128 v3, v[184:187] offset:8192\n"
"v_mfma_f32_16x16x32_bf16 v[176:179], a[228:231], v[132:135], v[176:179]\n"
"v_lshlrev_b32_e32 v247, 16, v192\n"
"v_and_b32_e32 v246, 0xffff0000, v192\n"
"v_mfma_f32_16x16x32_bf16 v[176:179], a[224:227], v[216:219], v[176:179]\n"
"s_waitcnt vmcnt(14)\n"
"v_lshlrev_b32_e32 v245, 16, v193\n"
"v_and_b32_e32 v244, 0xffff0000, v193\n"
"; @A G2 b.2\n"
"v_mfma_f32_16x16x32_bf16 v[100:103], a[128:131], v[148:151], 0\n"
"v_lshlrev_b32_e32 v243, 16, v194\n"
"v_and_b32_e32 v242, 0xffff0000, v194\n"
"v_mfma_f32_16x16x32_bf16 v[100:103], a[132:135], v[144:147], v[100:103]\n"
"v_lshlrev_b32_e32 v241, 16, v195\n"
"v_and_b32_e32 v240, 0xffff0000, v195\n"
"v_mfma_f32_16x16x32_bf16 v[100:103], a[136:139], v[116:119], v[100:103]\n"
"v_mfma_f32_16x16x32_bf16 v[100:103], a[140:143], v[112:115], v[100:103]\n"
"s_waitcnt vmcnt(8)\n"
"v_mfma_f32_16x16x32_bf16 v[100:103], a[144:147], v[92:95], v[100:103]\n"
"v_mfma_f32_16x16x32_bf16 v[100:103], a[148:151], v[96:99], v[100:103]\n"
"ds_write_b128 v3, v[176:179] offset:9216\n"
"v_mfma_f32_16x16x32_bf16 v[100:103], a[152:155], v[156:159], v[100:103]\n"
"v_mfma_f32_16x16x32_bf16 v[100:103], a[156:159], v[88:91], v[100:103]\n"
"v_mfma_f32_16x16x32_bf16 v[104:107], a[188:191], v[88:91], 0\n"
"v_mfma_f32_16x16x32_bf16 v[104:107], a[184:187], v[156:159], v[104:107]\n"
"s_waitcnt lgkmcnt(0)\n"
"s_barrier\n"
"v_mfma_f32_16x16x32_bf16 v[104:107], a[180:183], v[96:99], v[104:107]\n"
"ds_read_b128 v[108:111], v4\n"
"ds_read_b128 v[128:131], v5\n"
"v_mfma_f32_16x16x32_bf16 v[104:107], a[176:179], v[92:95], v[104:107]\n"
"v_mfma_f32_16x16x32_bf16 v[104:107], a[172:175], v[112:115], v[104:107]\n"
"s_add_u32 s98, s56, 0\n"
"v_cmp_u_f32_e32 vcc, v100, v100\n"
"v_mfma_f32_16x16x32_bf16 v[104:107], a[168:171], v[116:119], v[104:107]\n"
"s_cbranch_vccnz .Lmk_p113s\n"
".Lmk_p113r:\n"
"v_mfma_f32_16x16x32_bf16 v[104:107], a[164:167], v[144:147], v[104:107]\n"
"ds_write_b128 v3, v[100:103] offset:12288\n"
"v_mfma_f32_16x16x32_bf16 v[104:107], a[160:163], v[148:151], v[104:107]\n"
"buffer_load_dwordx4 v[100:103], v0, s[4:7], s98 offen sc1\n"
"v_mfma_f32_16x16x32_bf16 v[132:135], a[192:195], v[148:151], 0\n"
"ds_read_b128 v[160:163], v4 offset:1024\n"
"ds_read_b128 v[164:167], v5 offset:1024\n"
"v_mfma_f32_16x16x32_bf16 v[132:135], a[196:199], v[144:147], v[132:135]\n"
"s_waitcnt lgkmcnt(3)\n"
"v_add_f32_e32 v108, v108, v255\n"
"v_add_f32_e32 v109, v109, v254\n"
"v_mfma_f32_16x16x32_bf16 v[132:135], a[200:203], v[116:119], v[132:135]\n"
"buffer_load_dwordx4 v[176:179], v0, s[4:7], s98 offen offset:1024 sc1\n"
"v_mfma_f32_16x16x32_bf16 v[132:135], a[204:207], v[112:115], v[132:135]\n"
"v_add_f32_e32 v110, v110, v253\n"
"v_add_f32_e32 v111, v111, v252\n"
"v_mfma_f32_16x16x32_bf16 v[132:135], a[208:211], v[92:95], v[132:135]\n"
"v_add_f32_e32 v108, v108, v128\n"
"v_add_f32_e32 v109, v109, v129\n"
"v_mfma_f32_16x16x32_bf16 v[132:135], a[212:215], v[96:99], v[132:135]\n"
"ds_write_b128 v3, v[104:107] offset:13312\n"
"v_mfma_f32_16x16x32_bf16 v[132:135], a[216:219], v[156:159], v[132:135]\n"
"buffer_load_dwordx4 v[104:107], v0, s[4:7], s98 offen offset:2048 sc1\n"
"v_mfma_f32_16x16x32_bf16 v[132:135], a[220:223], v[88:91], v[132:135]\n"
"ds_read_b128 v[184:187], v4 offset:2048\n"
"ds_read_b128 v[188:191], v5 offset:2048\n"
"v_mfma_f32_16x16x32_bf16 v[192:195], a[252:255], v[88:91], 0\n"
"s_waitcnt lgkmcnt(3)\n"
"v_add_f32_e32 v110, v110, v130\n"
"v_add_f32_e32 v111, v111, v131\n"
"v_mfma_f32_16x16x32_bf16 v[192:195], a[248:251], v[156:159], v[192:195]\n"
"buffer_load_dwordx4 v[88:91], v0, s[4:7], s98 offen offset:3072 sc1\n"
"v_mfma_f32_16x16x32_bf16 v[192:195], a[244:247], v[96:99], v[192:195]\n"
"v_add_f32_e32 v160, v160, v251\n"
"v_add_f32_e32 v161, v161, v250\n"
"v_mfma_f32_16x16x32_bf16 v[192:195], a[240:243], v[92:95], v[192:195]\n"
"v_add_f32_e32 v162, v162, v249\n"
"v_add_f32_e32 v163, v163, v248\n"
"v_mfma_f32_16x16x32_bf16 v[192:195], a[236:239], v[112:115], v[192:195]\n"
"buffer_load_dwordx4 v[92:95], v1, s[4:7], s98 offen sc1\n"
"v_mfma_f32_16x16x32_bf16 v[192:195], a[232:235], v[116:119], v[192:195]\n"
"ds_write_b128 v3, v[132:135] offset:14336\n"
"v_mfma_f32_16x16x32_bf16 v[192:195], a[228:231], v[144:147], v[192:195]\n"
"ds_read_b128 v[96:99], v4 offset:3072\n"
"ds_read_b128 v[112:115], v5 offset:3072\n"
"v_mfma_f32_16x16x32_bf16 v[192:195], a[224:227], v[148:151], v[192:195]\n"
"s_waitcnt vmcnt(9)\n"
"buffer_load_dwordx4 v[116:119], v1, s[4:7], s98 offen offset:1024 sc1\n"
"; @A G2 b.3\n"
"v_mfma_f32_16x16x32_bf16 v[128:131], a[128:131], v[172:175], v[108:111]\n"
"v_add_f32_e32 v160, v160, v164\n"
"v_add_f32_e32 v161, v161, v165\n"
"v_mfma_f32_16x16x32_bf16 v[128:131], a[132:135], v[124:127], v[128:131]\n"
"s_waitcnt lgkmcnt(3)\n"
"v_add_f32_e32 v162, v162, v166\n"
"v_add_f32_e32 v163, v163, v167\n"
"v_mfma_f32_16x16x32_bf16 v[128:131], a[136:139], v[120:123], v[128:131]\n"
"buffer_load_dwordx4 v[132:135], v1, s[4:7], s98 offen offset:2048 sc1\n"
"v_mfma_f32_16x16x32_bf16 v[128:131], a[140:143], v[136:139], v[128:131]\n"
"s_waitcnt vmcnt(7)\n"
"v_add_f32_e32 v184, v184, v247\n"
"v_add_f32_e32 v185, v185, v246\n"
"v_mfma_f32_16x16x32_bf16 v[128:131], a[144:147], v[152:155], v[128:131]\n"
"v_add_f32_e32 v186, v186, v245\n"
"v_add_f32_e32 v187, v187, v244\n"
"v_mfma_f32_16x16x32_bf16 v[128:131], a[148:151], v[168:171], v[128:131]\n"
"ds_write_b128 v3, v[192:195] offset:15360\n"
"v_mfma_f32_16x16x32_bf16 v[128:131], a[152:155], v[180:183], v[128:131]\n"
"buffer_load_dwordx4 v[144:147], v1, s[4:7], s98 offen offset:3072 sc1\n"
"v_mfma_f32_16x16x32_bf16 v[128:131], a[156:159], v[140:143], v[128:131]\n"
"s_add_u32 s98, s57, 0\n"
"v_add_f32_e32 v184, v184, v188\n"
"v_add_f32_e32 v185, v185, v189\n"
"v_mfma_f32_16x16x32_bf16 v[148:151], a[188:191], v[140:143], v[160:163]\n"
"s_waitcnt lgkmcnt(1)\n"
"v_add_f32_e32 v186, v186, v190\n"
"v_add_f32_e32 v187, v187, v191\n"
"v_mfma_f32_16x16x32_bf16 v[148:151], a[184:187], v[180:183], v[148:151]\n"
"s_waitcnt lgkmcnt(0)\n"
"s_barrier\n"
"v_mfma_f32_16x16x32_bf16 v[148:151], a[180:183], v[168:171], v[148:151]\n"
"buffer_load_dwordx4 v[156:159], v0, s[4:7], s98 offen sc1\n"
"v_mfma_f32_16x16x32_bf16 v[148:151], a[176:179], v[152:155], v[148:151]\n"
"v_add_f32_e32 v96, v96, v243\n"
"v_add_f32_e32 v97, v97, v242\n"
"v_mfma_f32_16x16x32_bf16 v[148:151], a[172:175], v[136:139], v[148:151]\n"
"v_cmp_u_f32_e32 vcc, v128, v128\n"
"v_add_f32_e32 v98, v98, v241\n"
"v_mfma_f32_16x16x32_bf16 v[148:151], a[168:171], v[120:123], v[148:151]\n"
"s_cbranch_vccnz .Lmk_p114s\n"
".Lmk_p114r:\n"
"v_add_f32_e32 v99, v99, v240\n"
"v_mfma_f32_16x16x32_bf16 v[148:151], a[164:167], v[124:127], v[148:151]\n"
"buffer_load_dwordx4 v[108:111], v0, s[4:7], s98 offen offset:1024 sc1\n"
"v_mfma_f32_16x16x32_bf16 v[148:151], a[160:163], v[172:175], v[148:151]\n"
"v_add_f32_e32 v96, v96, v112\n"
"v_add_f32_e32 v97, v97, v113\n"
"v_mfma_f32_16x16x32_bf16 v[184:187], a[192:195], v[172:175], v[184:187]\n"
"v_add_f32_e32 v98, v98, v114\n"
"v_add_f32_e32 v99, v99, v115\n"
"v_mfma_f32_16x16x32_bf16 v[184:187], a[196:199], v[124:127], v[184:187]\n"
"buffer_load_dwordx4 v[112:115], v0, s[4:7], s98 offen offset:2048 sc1\n"
"v_mfma_f32_16x16x32_bf16 v[184:187], a[200:203], v[120:123], v[184:187]\n"
"ds_read_b128 v[160:163], v6\n"
"ds_read_b128 v[164:167], v6 offset:1024\n"
"v_mfma_f32_16x16x32_bf16 v[184:187], a[204:207], v[136:139], v[184:187]\n"
"ds_read_b128 v[188:191], v6 offset:2048\n"
"ds_read_b128 v[192:195], v6 offset:3072\n"
"v_mfma_f32_16x16x32_bf16 v[184:187], a[208:211], v[152:155], v[184:187]\n"
"buffer_load_dwordx4 v[196:199], v0, s[4:7], s98 offen offset:3072 sc1\n"
"v_mfma_f32_16x16x32_bf16 v[184:187], a[212:215], v[168:171], v[184:187]\n"
"v_mfma_f32_16x16x32_bf16 v[184:187], a[216:219], v[180:183], v[184:187]\n"
"v_mfma_f32_16x16x32_bf16 v[184:187], a[220:223], v[140:143], v[184:187]\n"
"buffer_load_dwordx4 v[200:203], v1, s[4:7], s98 offen sc1\n"
"v_mfma_f32_16x16x32_bf16 v[96:99], a[252:255], v[140:143], v[96:99]\n"
"s_waitcnt lgkmcnt(0)\n"
"v_add_f32_e32 v160, v128, v160\n"
"v_add_f32_e32 v161, v129, v161\n"
"v_mfma_f32_16x16x32_bf16 v[96:99], a[248:251], v[180:183], v[96:99]\n"
"v_add_f32_e32 v162, v130, v162\n"
"v_add_f32_e32 v163, v131, v163\n"
"v_mfma_f32_16x16x32_bf16 v[96:99], a[244:247], v[168:171], v[96:99]\n"
"buffer_load_dwordx4 v[128:131], v1, s[4:7], s98 offen offset:1024 sc1\n"
"v_mfma_f32_16x16x32_bf16 v[96:99], a[240:243], v[152:155], v[96:99]\n"
"v_add_f32_e32 v164, v148, v164\n"
"v_add_f32_e32 v165, v149, v165\n"
"v_mfma_f32_16x16x32_bf16 v[96:99], a[236:239], v[136:139], v[96:99]\n"
"v_add_f32_e32 v166, v150, v166\n"
"v_add_f32_e32 v167, v151, v167\n"
"v_mfma_f32_16x16x32_bf16 v[96:99], a[232:235], v[120:123], v[96:99]\n"
"buffer_load_dwordx4 v[120:123], v1, s[4:7], s98 offen offset:2048 sc1\n"
"v_mfma_f32_16x16x32_bf16 v[96:99], a[228:231], v[124:127], v[96:99]\n"
"v_cvt_pk_bf16_f32 v124, v160, v161\n"
"v_cvt_pk_bf16_f32 v125, v162, v163\n"
"v_mfma_f32_16x16x32_bf16 v[96:99], a[224:227], v[172:175], v[96:99]\n"
"s_add_u32 s97, s61, 0x440000\n"
"v_cvt_pk_bf16_f32 v126, v164, v165\n"
"v_cvt_pk_bf16_f32 v127, v166, v167\n"
"buffer_store_dwordx4 v[124:127], v0, s[4:7], s97 offen sc1\n"
"ds_write_b128 v2, v[124:127] offset:8192\n"
"buffer_load_dwordx4 v[136:139], v1, s[4:7], s98 offen offset:3072 sc1\n"
"; @A fin b\n"
"s_add_u32 s98, s61, 0x440000\n"
"v_mov_b32_e32 v140, 0x3f803f80\n"
"v_mov_b32_e32 v141, 0x3f803f80\n"
"v_mov_b32_e32 v142, 0x3f803f80\n"
"v_mov_b32_e32 v143, 0x3f803f80\n"
"v_add_f32_e32 v188, v184, v188\n"
"v_add_f32_e32 v189, v185, v189\n"
"v_add_f32_e32 v190, v186, v190\n"
"v_add_f32_e32 v191, v187, v191\n"
"v_add_f32_e32 v192, v96, v192\n"
"v_add_f32_e32 v193, v97, v193\n"
"v_add_f32_e32 v194, v98, v194\n"
"v_add_f32_e32 v195, v99, v195\n"
"v_cvt_pk_bf16_f32 v96, v188, v189\n"
"v_cvt_pk_bf16_f32 v97, v190, v191\n"
"v_cvt_pk_bf16_f32 v98, v192, v193\n"
"v_cvt_pk_bf16_f32 v99, v194, v195\n"
"v_mfma_f32_16x16x32_bf16 v[148:151], v[140:143], v[124:127], 0\n"
"buffer_store_dwordx4 v[96:99], v0, s[4:7], s98 offen offset:1024 sc1\n"
"ds_write_b128 v2, v[96:99] offset:9216\n"
"v_mfma_f32_16x16x32_bf16 v[152:155], v[124:127], v[124:127], 0\n"
"v_mfma_f32_16x16x32_bf16 v[148:151], v[140:143], v[96:99], v[148:151]\n"
"v_mfma_f32_16x16x32_bf16 v[152:155], v[96:99], v[96:99], v[152:155]\n"
"s_mov_b32 s96, 0xaaaaaaaa\n"
"s_mov_b32 s97, 0xaaaaaaaa\n"
"s_nop 5\n"
"v_cndmask_b32_e64 v152, v152, v153, s[96:97]\n"
"v_cndmask_b32_e64 v154, v154, v155, s[96:97]\n"
"s_mov_b32 s96, 0xcccccccc\n"
"s_mov_b32 s97, 0xcccccccc\n"
"v_cndmask_b32_e64 v149, v152, v154, s[96:97]\n"
"s_mov_b32 s96, 0xf0000f\n"
"s_mov_b32 s97, 0xf0000f00\n"
"s_add_u32 s98, s63, 0x44000\n"
"v_cndmask_b32_e64 v148, v148, v149, s[96:97]\n"
"buffer_store_dword v148, v14, s[4:7], s98 offen sc1\n"
"s_waitcnt vmcnt(17)\n"
"; @A G2 a.0\n"
"v_mfma_f32_16x16x32_bf16 v[96:99], a[128:131], v[100:103], 0\n"
"s_add_u32 s98, s58, 0\n"
"buffer_load_dwordx4 v[124:127], v0, s[4:7], s98 offen sc1\n"
"v_mfma_f32_16x16x32_bf16 v[96:99], a[132:135], v[176:179], v[96:99]\n"
"s_waitcnt vmcnt(16)\n"
"v_mfma_f32_16x16x32_bf16 v[96:99], a[136:139], v[104:107], v[96:99]\n"
"v_mfma_f32_16x16x32_bf16 v[96:99], a[140:143], v[88:91], v[96:99]\n"
"s_waitcnt vmcnt(14)\n"
"v_mfma_f32_16x16x32_bf16 v[96:99], a[144:147], v[92:95], v[96:99]\n"
"buffer_load_dwordx4 v[140:143], v0, s[4:7], s98 offen offset:1024 sc1\n"
"v_mfma_f32_16x16x32_bf16 v[96:99], a[148:151], v[116:119], v[96:99]\n"
"s_waitcnt vmcnt(13)\n"
"v_mfma_f32_16x16x32_bf16 v[96:99], a[152:155], v[132:135], v[96:99]\n"
"v_mfma_f32_16x16x32_bf16 v[96:99], a[156:159], v[144:147], v[96:99]\n"
"v_mfma_f32_16x16x32_bf16 v[148:151], a[188:191], v[144:147], 0\n"
"buffer_load_dwordx4 v[152:155], v0, s[4:7], s98 offen offset:2048 sc1\n"
"v_mfma_f32_16x16x32_bf16 v[148:151], a[184:187], v[132:135], v[148:151]\n"
"v_mfma_f32_16x16x32_bf16 v[148:151], a[180:183], v[116:119], v[148:151]\n"
"v_mfma_f32_16x16x32_bf16 v[148:151], a[176:179], v[92:95], v[148:151]\n"
"v_mfma_f32_16x16x32_bf16 v[148:151], a[172:175], v[88:91], v[148:151]\n"
"s_nop 1\n"
"v_cmp_u_f32_e32 vcc, v96, v96\n"
"v_mfma_f32_16x16x32_bf16 v[148:151], a[168:171], v[104:107], v[148:151]\n"
"s_cbranch_vccnz .Lmk_p115s\n"
".Lmk_p115r:\n"
"v_mfma_f32_16x16x32_bf16 v[148:151], a[164:167], v[176:179], v[148:151]\n"
"ds_write_b128 v3, v[96:99]\n"
"v_mfma_f32_16x16x32_bf16 v[148:151], a[160:163], v[100:103], v[148:151]\n"
"buffer_load_dwordx4 v[160:163], v0, s[4:7], s98 offen offset:3072 sc1\n"
"v_mfma_f32_16x16x32_bf16 v[96:99], a[192:195], v[100:103], 0\n"
"v_mfma_f32_16x16x32_bf16 v[96:99], a[196:199], v[176:179], v[96:99]\n"
"v_mfma_f32_16x16x32_bf16 v[96:99], a[200:203], v[104:107], v[96:99]\n"
"v_mfma_f32_16x16x32_bf16 v[96:99], a[204:207], v[88:91], v[96:99]\n"
"buffer_load_dwordx4 v[164:167], v1, s[4:7], s98 offen sc1\n"
"v_mfma_f32_16x16x32_bf16 v[96:99], a[208:211], v[92:95], v[96:99]\n"
"v_mfma_f32_16x16x32_bf16 v[96:99], a[212:215], v[116:119], v[96:99]\n"
"ds_write_b128 v3, v[148:151] offset:1024\n"
"v_mfma_f32_16x16x32_bf16 v[96:99], a[216:219], v[132:135], v[96:99]\n"
"v_mfma_f32_16x16x32_bf16 v[96:99], a[220:223], v[144:147], v[96:99]\n"
"buffer_load_dwordx4 v[148:151], v1, s[4:7], s98 offen offset:1024 sc1\n"
"v_mfma_f32_16x16x32_bf16 v[168:171], a[252:255], v[144:147], 0\n"
"v_mfma_f32_16x16x32_bf16 v[168:171], a[248:251], v[132:135], v[168:171]\n"
"v_mfma_f32_16x16x32_bf16 v[168:171], a[244:247], v[116:119], v[168:171]\n"
"v_mfma_f32_16x16x32_bf16 v[168:171], a[240:243], v[92:95], v[168:171]\n"
"buffer_load_dwordx4 v[116:119], v1, s[4:7], s98 offen offset:2048 sc1\n"
"v_mfma_f32_16x16x32_bf16 v[168:171], a[236:239], v[88:91], v[168:171]\n"
"v_mfma_f32_16x16x32_bf16 v[168:171], a[232:235], v[104:107], v[168:171]\n"
"ds_write_b128 v3, v[96:99] offset:2048\n"
"v_mfma_f32_16x16x32_bf16 v[168:171], a[228:231], v[176:179], v[168:171]\n"
"v_mfma_f32_16x16x32_bf16 v[168:171], a[224:227], v[100:103], v[168:171]\n"
"s_waitcnt vmcnt(14)\n"
"buffer_load_dwordx4 v[132:135], v1, s[4:7], s98 offen offset:3072 sc1\n"
"; @A G2 a.1\n"
"v_mfma_f32_16x16x32_bf16 v[88:91], a[128:131], v[156:159], 0\n"
"v_mfma_f32_16x16x32_bf16 v[88:91], a[132:135], v[108:111], v[88:91]\n"
"s_add_u32 s98, s59, 0\n"
"v_mfma_f32_16x16x32_bf16 v[88:91], a[136:139], v[112:115], v[88:91]\n"
"v_mfma_f32_16x16x32_bf16 v[88:91], a[140:143], v[196:199], v[88:91]\n"
"s_waitcnt vmcnt(10)\n"
"buffer_load_dwordx4 v[172:175], v0, s[4:7], s98 offen sc1\n"
"v_mfma_f32_16x16x32_bf16 v[88:91], a[144:147], v[200:203], v[88:91]\n"
"v_mfma_f32_16x16x32_bf16 v[88:91], a[148:151], v[128:131], v[88:91]\n"
"ds_write_b128 v3, v[168:171] offset:3072\n"
"v_mfma_f32_16x16x32_bf16 v[88:91], a[152:155], v[120:123], v[88:91]\n"
"v_mfma_f32_16x16x32_bf16 v[88:91], a[156:159], v[136:139], v[88:91]\n"
"buffer_load_dwordx4 v[176:179], v0, s[4:7], s98 offen offset:1024 sc1\n"
"v_mfma_f32_16x16x32_bf16 v[92:95], a[188:191], v[136:139], 0\n"
"v_mfma_f32_16x16x32_bf16 v[92:95], a[184:187], v[120:123], v[92:95]\n"
"v_mfma_f32_16x16x32_bf16 v[92:95], a[180:183], v[128:131], v[92:95]\n"
"v_mfma_f32_16x16x32_bf16 v[92:95], a[176:179], v[200:203], v[92:95]\n"
"buffer_load_dwordx4 v[168:171], v0, s[4:7], s98 offen offset:2048 sc1\n"
"v_mfma_f32_16x16x32_bf16 v[92:95], a[172:175], v[196:199], v[92:95]\n"
"s_nop 0\n"
"v_cmp_u_f32_e32 vcc, v88, v88\n"
"v_mfma_f32_16x16x32_bf16 v[92:95], a[168:171], v[112:115], v[92:95]\n"
"s_cbranch_vccnz .Lmk_p116s\n"
".Lmk_p116r:\n"
"v_mfma_f32_16x16x32_bf16 v[92:95], a[164:167], v[108:111], v[92:95]\n"
"ds_write_b128 v3, v[88:91] offset:6144\n"
"v_mfma_f32_16x16x32_bf16 v[92:95], a[160:163], v[156:159], v[92:95]\n"
"buffer_load_dwordx4 v[180:183], v0, s[4:7], s98 offen offset:3072 sc1\n"
"v_mfma_f32_16x16x32_bf16 v[88:91], a[192:195], v[156:159], 0\n"
"v_mfma_f32_16x16x32_bf16 v[88:91], a[196:199], v[108:111], v[88:91]\n"
"v_mfma_f32_16x16x32_bf16 v[88:91], a[200:203], v[112:115], v[88:91]\n"
"v_mfma_f32_16x16x32_bf16 v[88:91], a[204:207], v[196:199], v[88:91]\n"
"buffer_load_dwordx4 v[184:187], v1, s[4:7], s98 offen sc1\n"
"v_mfma_f32_16x16x32_bf16 v[88:91], a[208:211], v[200:203], v[88:91]\n"
"ds_read_b128 v[96:99], v2\n"
"ds_read_b128 v[100:103], v2 offset:1024\n"
"v_mfma_f32_16x16x32_bf16 v[88:91], a[212:215], v[128:131], v[88:91]\n"
"ds_write_b128 v3, v[92:95] offset:7168\n"
"v_mfma_f32_16x16x32_bf16 v[88:91], a[216:219], v[120:123], v[88:91]\n"
"v_mfma_f32_16x16x32_bf16 v[88:91], a[220:223], v[136:139], v[88:91]\n"
"buffer_load_dwordx4 v[188:191], v1, s[4:7], s98 offen offset:1024 sc1\n"
"v_mfma_f32_16x16x32_bf16 v[92:95], a[252:255], v[136:139], 0\n"
"v_mfma_f32_16x16x32_bf16 v[92:95], a[248:251], v[120:123], v[92:95]\n"
"s_waitcnt lgkmcnt(1)\n"
"v_lshlrev_b32_e32 v255, 16, v96\n"
"v_and_b32_e32 v254, 0xffff0000, v96\n"
"v_mfma_f32_16x16x32_bf16 v[92:95], a[244:247], v[128:131], v[92:95]\n"
"v_lshlrev_b32_e32 v253, 16, v97\n"
"v_and_b32_e32 v252, 0xffff0000, v97\n"
"v_mfma_f32_16x16x32_bf16 v[92:95], a[240:243], v[200:203], v[92:95]\n"
"buffer_load_dwordx4 v[192:195], v1, s[4:7], s98 offen offset:2048 sc1\n"
"v_mfma_f32_16x16x32_bf16 v[92:95], a[236:239], v[196:199], v[92:95]\n"
"v_lshlrev_b32_e32 v251, 16, v98\n"
"v_and_b32_e32 v250, 0xffff0000, v98\n"
"v_mfma_f32_16x16x32_bf16 v[92:95], a[232:235], v[112:115], v[92:95]\n"
"ds_write_b128 v3, v[88:91] offset:8192\n"
"v_mfma_f32_16x16x32_bf16 v[92:95], a[228:231], v[108:111], v[92:95]\n"
"v_lshlrev_b32_e32 v249, 16, v99\n"
"v_and_b32_e32 v248, 0xffff0000, v99\n"
"v_mfma_f32_16x16x32_bf16 v[92:95], a[224:227], v[156:159], v[92:95]\n"
"s_waitcnt vmcnt(11)\n"
"buffer_load_dwordx4 v[196:199], v1, s[4:7], s98 offen offset:3072 sc1\n"
"; @A G2 a.2\n"
"v_mfma_f32_16x16x32_bf16 v[96:99], a[128:131], v[124:127], 0\n"
"v_lshlrev_b32_e32 v247, 16, v100\n"
"v_and_b32_e32 v246, 0xffff0000, v100\n"
"v_mfma_f32_16x16x32_bf16 v[96:99], a[132:135], v[140:143], v[96:99]\n"
"v_lshlrev_b32_e32 v245, 16, v101\n"
"v_and_b32_e32 v244, 0xffff0000, v101\n"
"v_mfma_f32_16x16x32_bf16 v[96:99], a[136:139], v[152:155], v[96:99]\n"
"v_lshlrev_b32_e32 v243, 16, v102\n"
"v_and_b32_e32 v242, 0xffff0000, v102\n"
"v_mfma_f32_16x16x32_bf16 v[96:99], a[140:143], v[160:163], v[96:99]\n"
"s_waitcnt vmcnt(8)\n"
"v_lshlrev_b32_e32 v241, 16, v103\n"
"v_and_b32_e32 v240, 0xffff0000, v103\n"
"v_mfma_f32_16x16x32_bf16 v[96:99], a[144:147], v[164:167], v[96:99]\n"
"v_mfma_f32_16x16x32_bf16 v[96:99], a[148:151], v[148:151], v[96:99]\n"
"ds_write_b128 v3, v[92:95] offset:9216\n"
"v_mfma_f32_16x16x32_bf16 v[96:99], a[152:155], v[116:119], v[96:99]\n"
"v_mfma_f32_16x16x32_bf16 v[96:99], a[156:159], v[132:135], v[96:99]\n"
"v_mfma_f32_16x16x32_bf16 v[104:107], a[188:191], v[132:135], 0\n"
"s_add_u32 s98, s52, 0x440000\n"
"buffer_load_dwordx4 v[88:91], v0, s[4:7], s98 offen sc1\n"
"v_mfma_f32_16x16x32_bf16 v[104:107], a[184:187], v[116:119], v[104:107]\n"
"s_waitcnt lgkmcnt(0)\n"
"s_barrier\n"
"v_mfma_f32_16x16x32_bf16 v[104:107], a[180:183], v[148:151], v[104:107]\n"
"ds_read_b128 v[144:147], v4\n"
"ds_read_b128 v[112:115], v5\n"
"v_mfma_f32_16x16x32_bf16 v[104:107], a[176:179], v[164:167], v[104:107]\n"
"buffer_load_dwordx4 v[92:95], v0, s[4:7], s98 offen offset:1024 sc1\n"
"v_mfma_f32_16x16x32_bf16 v[104:107], a[172:175], v[160:163], v[104:107]\n"
"v_cmp_u_f32_e32 vcc, v96, v96\n"
"v_mfma_f32_16x16x32_bf16 v[104:107], a[168:171], v[152:155], v[104:107]\n"
"s_cbranch_vccnz .Lmk_p117s\n"
".Lmk_p117r:\n"
"v_mfma_f32_16x16x32_bf16 v[104:107], a[164:167], v[140:143], v[104:107]\n"
"ds_write_b128 v3, v[96:99] offset:12288\n"
"v_mfma_f32_16x16x32_bf16 v[104:107], a[160:163], v[124:127], v[104:107]\n"
"buffer_load_dwordx4 v[96:99], v0, s[4:7], s98 offen offset:2048 sc1\n"
"v_mfma_f32_16x16x32_bf16 v[120:123], a[192:195], v[124:127], 0\n"
"ds_read_b128 v[156:159], v4 offset:1024\n"
"ds_read_b128 v[128:131], v5 offset:1024\n"
"v_mfma_f32_16x16x32_bf16 v[120:123], a[196:199], v[140:143], v[120:123]\n"
"s_waitcnt lgkmcnt(3)\n"
"v_add_f32_e32 v144, v144, v255\n"
"v_add_f32_e32 v145, v145, v254\n"
"v_mfma_f32_16x16x32_bf16 v[120:123], a[200:203], v[152:155], v[120:123]\n"
"buffer_load_dwordx4 v[100:103], v0, s[4:7], s98 offen offset:3072 sc1\n"
"v_mfma_f32_16x16x32_bf16 v[120:123], a[204:207], v[160:163], v[120:123]\n"
"v_add_f32_e32 v146, v146, v253\n"
"v_add_f32_e32 v147, v147, v252\n"
"v_mfma_f32_16x16x32_bf16 v[120:123], a[208:211], v[164:167], v[120:123]\n"
"v_add_f32_e32 v144, v144, v112\n"
"v_add_f32_e32 v145, v145, v113\n"
"v_mfma_f32_16x16x32_bf16 v[120:123], a[212:215], v[148:151], v[120:123]\n"
"ds_write_b128 v3, v[104:107] offset:13312\n"
"v_mfma_f32_16x16x32_bf16 v[120:123], a[216:219], v[116:119], v[120:123]\n"
"buffer_load_dwordx4 v[104:107], v1, s[4:7], s98 offen sc1\n"
"v_mfma_f32_16x16x32_bf16 v[120:123], a[220:223], v[132:135], v[120:123]\n"
"ds_read_b128 v[108:111], v4 offset:2048\n"
"ds_read_b128 v[136:139], v5 offset:2048\n"
"v_mfma_f32_16x16x32_bf16 v[200:203], a[252:255], v[132:135], 0\n"
"s_waitcnt lgkmcnt(3)\n"
"v_add_f32_e32 v146, v146, v114\n"
"v_add_f32_e32 v147, v147, v115\n"
"v_mfma_f32_16x16x32_bf16 v[200:203], a[248:251], v[116:119], v[200:203]\n"
"buffer_load_dwordx4 v[112:115], v1, s[4:7], s98 offen offset:1024 sc1\n"
"v_mfma_f32_16x16x32_bf16 v[200:203], a[244:247], v[148:151], v[200:203]\n"
"v_add_f32_e32 v156, v156, v251\n"
"v_add_f32_e32 v157, v157, v250\n"
"v_mfma_f32_16x16x32_bf16 v[200:203], a[240:243], v[164:167], v[200:203]\n"
"v_add_f32_e32 v158, v158, v249\n"
"v_add_f32_e32 v159, v159, v248\n"
"v_mfma_f32_16x16x32_bf16 v[200:203], a[236:239], v[160:163], v[200:203]\n"
"buffer_load_dwordx4 v[116:119], v1, s[4:7], s98 offen offset:2048 sc1\n"
"v_mfma_f32_16x16x32_bf16 v[200:203], a[232:235], v[152:155], v[200:203]\n"
"ds_write_b128 v3, v[120:123] offset:14336\n"
"v_mfma_f32_16x16x32_bf16 v[200:203], a[228:231], v[140:143], v[200:203]\n"
"ds_read_b128 v[120:123], v4 offset:3072\n"
"ds_read_b128 v[148:151], v5 offset:3072\n"
"v_mfma_f32_16x16x32_bf16 v[200:203], a[224:227], v[124:127], v[200:203]\n"
"s_waitcnt vmcnt(11)\n"
"buffer_load_dwordx4 v[124:127], v1, s[4:7], s98 offen offset:3072 sc1\n"
"; @A G2 a.3\n"
"v_mfma_f32_16x16x32_bf16 v[164:167], a[128:131], v[172:175], v[144:147]\n"
"s_add_u32 s98, s53, 0x440000\n"
"v_add_f32_e32 v156, v156, v128\n"
"v_add_f32_e32 v157, v157, v129\n"
"v_mfma_f32_16x16x32_bf16 v[164:167], a[132:135], v[176:179], v[164:167]\n"
"s_waitcnt lgkmcnt(3)\n"
"v_add_f32_e32 v158, v158, v130\n"
"v_add_f32_e32 v159, v159, v131\n"
"v_mfma_f32_16x16x32_bf16 v[164:167], a[136:139], v[168:171], v[164:167]\n"
"buffer_load_dwordx4 v[128:131], v0, s[4:7], s98 offen sc1\n"
"v_mfma_f32_16x16x32_bf16 v[164:167], a[140:143], v[180:183], v[164:167]\n"
"s_waitcnt vmcnt(9)\n"
"v_add_f32_e32 v108, v108, v247\n"
"v_add_f32_e32 v109, v109, v246\n"
"v_mfma_f32_16x16x32_bf16 v[164:167], a[144:147], v[184:187], v[164:167]\n"
"v_add_f32_e32 v110, v110, v245\n"
"v_add_f32_e32 v111, v111, v244\n"
"v_mfma_f32_16x16x32_bf16 v[164:167], a[148:151], v[188:191], v[164:167]\n"
"ds_write_b128 v3, v[200:203] offset:15360\n"
"v_mfma_f32_16x16x32_bf16 v[164:167], a[152:155], v[192:195], v[164:167]\n"
"buffer_load_dwordx4 v[132:135], v0, s[4:7], s98 offen offset:1024 sc1\n"
"v_mfma_f32_16x16x32_bf16 v[164:167], a[156:159], v[196:199], v[164:167]\n"
"v_add_f32_e32 v108, v108, v136\n"
"v_add_f32_e32 v109, v109, v137\n"
"v_mfma_f32_16x16x32_bf16 v[200:203], a[188:191], v[196:199], v[156:159]\n"
"s_waitcnt lgkmcnt(1)\n"
"v_add_f32_e32 v110, v110, v138\n"
"v_add_f32_e32 v111, v111, v139\n"
"v_mfma_f32_16x16x32_bf16 v[200:203], a[184:187], v[192:195], v[200:203]\n"
"s_waitcnt lgkmcnt(0)\n"
"s_barrier\n"
"v_mfma_f32_16x16x32_bf16 v[200:203], a[180:183], v[188:191], v[200:203]\n"
"buffer_load_dwordx4 v[136:139], v0, s[4:7], s98 offen offset:2048 sc1\n"
"v_mfma_f32_16x16x32_bf16 v[200:203], a[176:179], v[184:187], v[200:203]\n"
"v_add_f32_e32 v120, v120, v243\n"
"v_add_f32_e32 v121, v121, v242\n"
"v_mfma_f32_16x16x32_bf16 v[200:203], a[172:175], v[180:183], v[200:203]\n"
"v_cmp_u_f32_e32 vcc, v164, v164\n"
"v_add_f32_e32 v122, v122, v241\n"
"v_mfma_f32_16x16x32_bf16 v[200:203], a[168:171], v[168:171], v[200:203]\n"
"s_cbranch_vccnz .Lmk_p118s\n"
".Lmk_p118r:\n"
"v_add_f32_e32 v123, v123, v240\n"
"v_mfma_f32_16x16x32_bf16 v[200:203], a[164:167], v[176:179], v[200:203]\n"
"buffer_load_dwordx4 v[140:143], v0, s[4:7], s98 offen offset:3072 sc1\n"
"v_mfma_f32_16x16x32_bf16 v[200:203], a[160:163], v[172:175], v[200:203]\n"
"v_add_f32_e32 v120, v120, v148\n"
"v_add_f32_e32 v121, v121, v149\n"
"v_mfma_f32_16x16x32_bf16 v[108:111], a[192:195], v[172:175], v[108:111]\n"
"v_add_f32_e32 v122, v122, v150\n"
"v_add_f32_e32 v123, v123, v151\n"
"v_mfma_f32_16x16x32_bf16 v[108:111], a[196:199], v[176:179], v[108:111]\n"
"buffer_load_dwordx4 v[144:147], v1, s[4:7], s98 offen sc1\n"
"v_mfma_f32_16x16x32_bf16 v[108:111], a[200:203], v[168:171], v[108:111]\n"
"ds_read_b128 v[204:207], v6\n"
"ds_read_b128 v[208:211], v6 offset:1024\n"
"v_mfma_f32_16x16x32_bf16 v[108:111], a[204:207], v[180:183], v[108:111]\n"
"ds_read_b128 v[152:155], v6 offset:2048\n"
"ds_read_b128 v[156:159], v6 offset:3072\n"
"v_mfma_f32_16x16x32_bf16 v[108:111], a[208:211], v[184:187], v[108:111]\n"
"buffer_load_dwordx4 v[148:151], v1, s[4:7], s98 offen offset:1024 sc1\n"
"v_mfma_f32_16x16x32_bf16 v[108:111], a[212:215], v[188:191], v[108:111]\n"
"v_mfma_f32_16x16x32_bf16 v[108:111], a[216:219], v[192:195], v[108:111]\n"
"v_mfma_f32_16x16x32_bf16 v[108:111], a[220:223], v[196:199], v[108:111]\n"
"buffer_load_dwordx4 v[160:163], v1, s[4:7], s98 offen offset:2048 sc1\n"
"v_mfma_f32_16x16x32_bf16 v[120:123], a[252:255], v[196:199], v[120:123]\n"
"s_waitcnt lgkmcnt(0)\n"
"v_add_f32_e32 v204, v164, v204\n"
"v_add_f32_e32 v205, v165, v205\n"
"v_mfma_f32_16x16x32_bf16 v[120:123], a[248:251], v[192:195], v[120:123]\n"
"v_add_f32_e32 v206, v166, v206\n"
"v_add_f32_e32 v207, v167, v207\n"
"v_mfma_f32_16x16x32_bf16 v[120:123], a[244:247], v[188:191], v[120:123]\n"
"buffer_load_dwordx4 v[164:167], v1, s[4:7], s98 offen offset:3072 sc1\n"
"v_mfma_f32_16x16x32_bf16 v[120:123], a[240:243], v[184:187], v[120:123]\n"
"v_add_f32_e32 v208, v200, v208\n"
"v_add_f32_e32 v209, v201, v209\n"
"v_mfma_f32_16x16x32_bf16 v[120:123], a[236:239], v[180:183], v[120:123]\n"
"v_add_f32_e32 v210, v202, v210\n"
"v_add_f32_e32 v211, v203, v211\n"
"v_mfma_f32_16x16x32_bf16 v[120:123], a[232:235], v[168:171], v[120:123]\n"
"v_cvt_pk_bf16_f32 v168, v204, v205\n"
"v_cvt_pk_bf16_f32 v169, v206, v207\n"
"v_mfma_f32_16x16x32_bf16 v[120:123], a[228:231], v[176:179], v[120:123]\n"
"s_add_u32 s98, s61, 0x400000\n"
"v_cvt_pk_bf16_f32 v170, v208, v209\n"
"v_cvt_pk_bf16_f32 v171, v210, v211\n"
"v_mfma_f32_16x16x32_bf16 v[120:123], a[224:227], v[172:175], v[120:123]\n"
"buffer_store_dwordx4 v[168:171], v0, s[4:7], s98 offen sc1\n"
"ds_write_b128 v2, v[168:171]\n"
"s_branch .Lmk_loop\n"
".Lmk_f1s:\n"
";\n"
"s_mov_b32 s1, 0x10000\n"
".Lmk_f1l:\n"
"buffer_load_dwordx2 v[250:251], v12, s[4:7], s98 offen sc1\n"
"s_waitcnt vmcnt(0)\n"
"v_cmp_ne_u64_e32 vcc, v[250:251], v[18:19]\n"
"s_cbranch_vccz .Lmk_f1r\n"
"s_sub_u32 s1, s1, 1\n"
"s_cbranch_scc0 .Lmk_f1l\n"
"s_branch .Lmk_f1r\n"
".Lmk_f2s:\n"
";\n"
"s_mov_b32 s1, 0x10000\n"
".Lmk_f2l:\n"
"buffer_load_dwordx2 v[246:247], v12, s[4:7], s97 offen sc1\n"
"s_waitcnt vmcnt(0)\n"
"v_cmp_ne_u64_e32 vcc, v[246:247], v[18:19]\n"
"s_cbranch_vccz .Lmk_f2r\n"
"s_sub_u32 s1, s1, 1\n"
"s_cbranch_scc0 .Lmk_f2l\n"
"s_branch .Lmk_f2r\n"
".Lmk_f5s:\n"
";\n"
"s_mov_b32 s1, 0x10000\n"
".Lmk_f5l:\n"
"buffer_load_dwordx2 v[250:251], v41, s[4:7], s97 offen sc1\n"
"s_waitcnt vmcnt(0)\n"
"v_cmp_ne_u64_e32 vcc, v[250:251], v[18:19]\n"
"s_cbranch_vccz .Lmk_f5r\n"
"s_sub_u32 s1, s1, 1\n"
"s_cbranch_scc0 .Lmk_f5l\n"
"s_branch .Lmk_f5r\n"
".Lmk_w7s:\n"
"buffer_store_dwordx4 a[4:7], v0, s[4:7], s97 offen sc1\n"
"s_branch .Lmk_w7r\n"
".Lmk_w8s:\n"
"buffer_store_dwordx4 a[36:39], v0, s[4:7], s97 offen sc1\n"
"s_branch .Lmk_w8r\n"
".Lmk_w9s:\n"
"buffer_store_dwordx4 a[68:71], v0, s[4:7], s96 offen sc1\n"
"s_branch .Lmk_w9r\n"
".Lmk_w10s:\n"
"buffer_store_dwordx4 a[100:103], v0, s[4:7], s96 offen sc1\n"
"s_branch .Lmk_w10r\n"
".Lmk_w11s:\n"
"buffer_store_dwordx4 v[52:55], v0, s[4:7], s96 offen sc1\n"
"s_branch .Lmk_w11r\n"
".Lmk_w12s:\n"
"buffer_store_dwordx4 v[56:59], v0, s[4:7], s96 offen sc1\n"
"s_branch .Lmk_w12r\n"
".Lmk_f6s:\n"
";\n"
"s_mov_b32 s1, 0x10000\n"
".Lmk_f6l:\n"
"buffer_load_dwordx2 v[246:247], v13, s[4:7], s97 offen sc1\n"
"s_waitcnt vmcnt(0)\n"
"v_cmp_ne_u64_e32 vcc, v[246:247], v[18:19]\n"
"s_cbranch_vccz .Lmk_f6r\n"
"s_sub_u32 s1, s1, 1\n"
"s_cbranch_scc0 .Lmk_f6l\n"
"s_branch .Lmk_f6r\n"
".Lmk_f3s:\n"
";\n"
"s_mov_b32 s1, 0x10000\n"
".Lmk_f3l:\n"
"buffer_load_dwordx2 v[250:251], off, s[4:7], s96 sc1\n"
"s_waitcnt vmcnt(0)\n"
"v_cmp_ne_u64_e32 vcc, v[250:251], v[18:19]\n"
"s_cbranch_vccz .Lmk_f3r\n"
"s_sub_u32 s1, s1, 1\n"
"s_cbranch_scc0 .Lmk_f3l\n"
"s_branch .Lmk_f3r\n"
".Lmk_x23s:\n"
"s_nop 3\n"
"v_mov_b32_e32 v24, 0xff800000\n"
"v_mov_b32_e32 v25, 0xff800000\n"
"v_mov_b32_e32 v26, 0xff800000\n"
"v_mov_b32_e32 v27, 0xff800000\n"
"v_mov_b32_e32 v36, 0xff800000\n"
"v_mov_b32_e32 v37, 0xff800000\n"
"v_mov_b32_e32 v38, 0xff800000\n"
"v_mov_b32_e32 v39, 0xff800000\n"
"s_nop 1\n"
"s_branch .Lmk_x23r\n"
".Lmk_x24s:\n"
"s_nop 3\n"
"v_mov_b32_e32 v24, 0xff800000\n"
"v_mov_b32_e32 v25, 0xff800000\n"
"v_mov_b32_e32 v26, 0xff800000\n"
"v_mov_b32_e32 v27, 0xff800000\n"
"v_mov_b32_e32 v36, 0xff800000\n"
"v_mov_b32_e32 v37, 0xff800000\n"
"v_mov_b32_e32 v38, 0xff800000\n"
"v_mov_b32_e32 v39, 0xff800000\n"
"s_nop 1\n"
"s_branch .Lmk_x24r\n"
".Lmk_x25s:\n"
"s_nop 3\n"
"v_mov_b32_e32 v24, 0xff800000\n"
"v_mov_b32_e32 v25, 0xff800000\n"
"v_mov_b32_e32 v26, 0xff800000\n"
"v_mov_b32_e32 v27, 0xff800000\n"
"v_mov_b32_e32 v36, 0xff800000\n"
"v_mov_b32_e32 v37, 0xff800000\n"
"v_mov_b32_e32 v38, 0xff800000\n"
"v_mov_b32_e32 v39, 0xff800000\n"
"s_nop 1\n"
"s_branch .Lmk_x25r\n"
".Lmk_x26s:\n"
"s_nop 3\n"
"v_mov_b32_e32 v24, 0xff800000\n"
"v_mov_b32_e32 v25, 0xff800000\n"
"v_mov_b32_e32 v26, 0xff800000\n"
"v_mov_b32_e32 v27, 0xff800000\n"
"v_mov_b32_e32 v36, 0xff800000\n"
"v_mov_b32_e32 v37, 0xff800000\n"
"v_mov_b32_e32 v38, 0xff800000\n"
"v_mov_b32_e32 v39, 0xff800000\n"
"s_nop 1\n"
"s_branch .Lmk_x26r\n"
".Lmk_f19s:\n"
";\n"
"s_mov_b32 s1, 0x10000\n"
".Lmk_f19l:\n"
"buffer_load_dwordx2 v[238:239], off, s[4:7], s97 sc1\n"
"s_waitcnt vmcnt(0)\n"
"v_cmp_ne_u64_e32 vcc, v[238:239], v[18:19]\n"
"s_cbranch_vccz .Lmk_f19r\n"
"s_sub_u32 s1, s1, 1\n"
"s_cbranch_scc0 .Lmk_f19l\n"
"s_branch .Lmk_f19r\n"
".Lmk_b21:\n"
"s_cmp_lg_u32 s2, 0\n"
"s_cbranch_scc1 .Lmk_k27\n"
"s_mov_b32 s2, 1\n"
"s_waitcnt vmcnt(0) lgkmcnt(0)\n"
"s_nop 7\n"
"s_mov_b32 s97, 11\n"
"v_add_u32_e32 v248, 0x3800, v15\n"
"v_add_u32_e32 v247, 0x3800, v254\n"
"ds_read_b128 v[36:39], v15 offset:24576\n"
"ds_read_b128 v[40:43], v254 offset:24832\n"
"s_waitcnt lgkmcnt(0)\n"
"v_mfma_f32_16x16x32_bf16 v[48:51], v[36:39], v[28:31], 0\n"
"v_mfma_f32_16x16x32_bf16 v[52:55], v[36:39], v[32:35], 0\n"
"v_mfma_f32_16x16x32_bf16 v[36:39], v[40:43], v[32:35], 0\n"
"v_mfma_f32_16x16x32_bf16 v[56:59], v[40:43], v[28:31], 0\n"
".Lmk_b21l:\n"
"ds_read_b128 v[40:43], v248 offset:8192\n"
"ds_read_b128 v[60:63], v247 offset:8448\n"
"s_cmp_gt_i32 s78, s97\n"
"s_cselect_b32 s96, 0xff800000, 0\n"
"v_mov_b32_e32 v64, s96\n"
"v_mov_b32_e32 v65, s96\n"
"v_mov_b32_e32 v66, s96\n"
"v_mov_b32_e32 v67, s96\n"
"v_subrev_u32_e32 v248, 0x800, v248\n"
"v_subrev_u32_e32 v247, 0x800, v247\n"
"s_waitcnt lgkmcnt(0)\n"
"v_mfma_f32_16x16x32_bf16 v[72:75], v[40:43], v[28:31], v[64:67]\n"
"v_mfma_f32_16x16x32_bf16 v[76:79], v[40:43], v[32:35], v[64:67]\n"
"v_mfma_f32_16x16x32_bf16 v[40:43], v[60:63], v[32:35], v[64:67]\n"
"v_mfma_f32_16x16x32_bf16 v[84:87], v[60:63], v[28:31], v[64:67]\n"
"s_nop 7\n"
"s_cmp_lg_u32 s97, 4\n"
"s_cbranch_scc1 .Lmk_b21m\n"
"v_cndmask_b32_e64 v72, v48, v72, s[20:21]\n"
"v_cndmask_b32_e64 v73, v49, v73, s[22:23]\n"
"v_cndmask_b32_e64 v74, v50, v74, s[24:25]\n"
"v_cndmask_b32_e64 v75, v51, v75, s[26:27]\n"
"v_cndmask_b32_e64 v84, v56, v84, s[28:29]\n"
"v_cndmask_b32_e64 v85, v57, v85, s[30:31]\n"
"v_cndmask_b32_e64 v86, v58, v86, s[32:33]\n"
"v_cndmask_b32_e64 v87, v59, v87, s[34:35]\n"
"v_cndmask_b32_e64 v76, v52, v76, s[36:37]\n"
"v_cndmask_b32_e64 v77, v53, v77, s[38:39]\n"
"v_cndmask_b32_e64 v78, v54, v78, s[40:41]\n"
"v_cndmask_b32_e64 v79, v55, v79, s[42:43]\n"
"v_cndmask_b32_e64 v40, v36, v40, s[44:45]\n"
"v_cndmask_b32_e64 v41, v37, v41, s[46:47]\n"
"v_cndmask_b32_e64 v42, v38, v42, s[48:49]\n"
"v_cndmask_b32_e64 v43, v39, v43, s[50:51]\n"
".Lmk_b21m:\n"
"v_max3_f32 v253, v253, v72, v73\n"
"v_max3_f32 v251, v251, v76, v77\n"
"v_max3_f32 v253, v253, v74, v75\n"
"v_max3_f32 v251, v251, v78, v79\n"
"v_max3_f32 v253, v253, v84, v85\n"
"v_max3_f32 v251, v251, v40, v41\n"
"v_max3_f32 v253, v253, v86, v87\n"
"v_max3_f32 v251, v251, v42, v43\n"
"s_sub_u32 s97, s97, 1\n"
"s_cmp_ge_i32 s97, 4\n"
"s_cbranch_scc1 .Lmk_b21l\n"
"v_mov_b32_e32 v248, v253\n"
"v_mov_b32_e32 v247, v251\n"
"s_nop 0\n"
"v_permlane16_swap_b32_e32 v253, v248\n"
"v_permlane16_swap_b32_e32 v251, v247\n"
"v_max_f32_e32 v253, v253, v248\n"
"v_max_f32_e32 v251, v251, v247\n"
"v_mov_b32_e32 v248, v253\n"
"v_mov_b32_e32 v247, v251\n"
"s_nop 0\n"
"v_permlane32_swap_b32_e32 v253, v248\n"
"v_permlane32_swap_b32_e32 v251, v247\n"
"v_max_f32_e32 v253, v253, v248\n"
"v_max_f32_e32 v251, v251, v247\n"
"v_mov_b32_e32 v250, v253\n"
"v_mov_b32_e32 v249, v251\n"
"s_branch .Lmk_a20\n"
".Lmk_x30s:\n"
"s_nop 3\n"
"v_mov_b32_e32 v116, 0xff800000\n"
"v_mov_b32_e32 v117, 0xff800000\n"
"v_mov_b32_e32 v118, 0xff800000\n"
"v_mov_b32_e32 v119, 0xff800000\n"
"v_mov_b32_e32 v120, 0xff800000\n"
"v_mov_b32_e32 v121, 0xff800000\n"
"v_mov_b32_e32 v122, 0xff800000\n"
"v_mov_b32_e32 v123, 0xff800000\n"
"s_nop 1\n"
"s_branch .Lmk_x30r\n"
".Lmk_x31s:\n"
"s_nop 3\n"
"v_mov_b32_e32 v116, 0xff800000\n"
"v_mov_b32_e32 v117, 0xff800000\n"
"v_mov_b32_e32 v118, 0xff800000\n"
"v_mov_b32_e32 v119, 0xff800000\n"
"v_mov_b32_e32 v120, 0xff800000\n"
"v_mov_b32_e32 v121, 0xff800000\n"
"v_mov_b32_e32 v122, 0xff800000\n"
"v_mov_b32_e32 v123, 0xff800000\n"
"s_nop 1\n"
"s_branch .Lmk_x31r\n"
".Lmk_x32s:\n"
"s_nop 3\n"
"v_mov_b32_e32 v116, 0xff800000\n"
"v_mov_b32_e32 v117, 0xff800000\n"
"v_mov_b32_e32 v118, 0xff800000\n"
"v_mov_b32_e32 v119, 0xff800000\n"
"v_mov_b32_e32 v120, 0xff800000\n"
"v_mov_b32_e32 v121, 0xff800000\n"
"v_mov_b32_e32 v122, 0xff800000\n"
"v_mov_b32_e32 v123, 0xff800000\n"
"s_nop 1\n"
"s_branch .Lmk_x32r\n"
".Lmk_f4s:\n"
";\n"
"s_mov_b32 s1, 0x10000\n"
".Lmk_f4l:\n"
"buffer_load_dwordx2 v[246:247], v12, s[4:7], s96 offen sc1\n"
"s_waitcnt vmcnt(0)\n"
"v_cmp_ne_u64_e32 vcc, v[246:247], v[18:19]\n"
"s_cbranch_vccz .Lmk_f4r\n"
"s_sub_u32 s1, s1, 1\n"
"s_cbranch_scc0 .Lmk_f4l\n"
"s_branch .Lmk_f4r\n"
".Lmk_b29:\n"
"s_cmp_lg_u32 s2, 0\n"
"s_cbranch_scc1 .Lmk_k34\n"
"s_mov_b32 s2, 1\n"
"s_waitcnt vmcnt(0) lgkmcnt(0)\n"
"s_nop 7\n"
"s_mov_b32 s96, 7\n"
"v_add_u32_e32 v248, 0x3800, v15\n"
"v_add_u32_e32 v247, 0x3800, v254\n"
"ds_read_b128 v[172:175], v15 offset:16384\n"
"ds_read_b128 v[192:195], v254 offset:16640\n"
"s_waitcnt lgkmcnt(0)\n"
"v_mfma_f32_16x16x32_bf16 v[196:199], v[172:175], v[92:95], 0\n"
"v_mfma_f32_16x16x32_bf16 v[212:215], v[172:175], v[96:99], 0\n"
"v_mfma_f32_16x16x32_bf16 v[172:175], v[192:195], v[96:99], 0\n"
"v_mfma_f32_16x16x32_bf16 v[220:223], v[192:195], v[92:95], 0\n"
".Lmk_b29l:\n"
"ds_read_b128 v[192:195], v248\n"
"ds_read_b128 v[224:227], v247 offset:256\n"
"s_cmp_gt_i32 s78, s96\n"
"s_cselect_b32 s95, 0xff800000, 0\n"
"v_mov_b32_e32 v228, s95\n"
"v_mov_b32_e32 v229, s95\n"
"v_mov_b32_e32 v230, s95\n"
"v_mov_b32_e32 v231, s95\n"
"v_subrev_u32_e32 v248, 0x800, v248\n"
"v_subrev_u32_e32 v247, 0x800, v247\n"
"s_waitcnt lgkmcnt(0)\n"
"v_mfma_f32_16x16x32_bf16 v[232:235], v[192:195], v[92:95], v[228:231]\n"
"v_mfma_f32_16x16x32_bf16 v[236:239], v[192:195], v[96:99], v[228:231]\n"
"v_mfma_f32_16x16x32_bf16 v[192:195], v[224:227], v[96:99], v[228:231]\n"
"v_mfma_f32_16x16x32_bf16 v[240:243], v[224:227], v[92:95], v[228:231]\n"
"s_nop 7\n"
"s_cmp_lg_u32 s96, 0\n"
"s_cbranch_scc1 .Lmk_b29m\n"
"v_cndmask_b32_e64 v232, v196, v232, s[20:21]\n"
"v_cndmask_b32_e64 v233, v197, v233, s[22:23]\n"
"v_cndmask_b32_e64 v234, v198, v234, s[24:25]\n"
"v_cndmask_b32_e64 v235, v199, v235, s[26:27]\n"
"v_cndmask_b32_e64 v240, v220, v240, s[28:29]\n"
"v_cndmask_b32_e64 v241, v221, v241, s[30:31]\n"
"v_cndmask_b32_e64 v242, v222, v242, s[32:33]\n"
"v_cndmask_b32_e64 v243, v223, v243, s[34:35]\n"
"v_cndmask_b32_e64 v236, v212, v236, s[36:37]\n"
"v_cndmask_b32_e64 v237, v213, v237, s[38:39]\n"
"v_cndmask_b32_e64 v238, v214, v238, s[40:41]\n"
"v_cndmask_b32_e64 v239, v215, v239, s[42:43]\n"
"v_cndmask_b32_e64 v192, v172, v192, s[44:45]\n"
"v_cndmask_b32_e64 v193, v173, v193, s[46:47]\n"
"v_cndmask_b32_e64 v194, v174, v194, s[48:49]\n"
"v_cndmask_b32_e64 v195, v175, v195, s[50:51]\n"
".Lmk_b29m:\n"
"v_max3_f32 v253, v253, v232, v233\n"
"v_max3_f32 v251, v251, v236, v237\n"
"v_max3_f32 v253, v253, v234, v235\n"
"v_max3_f32 v251, v251, v238, v239\n"
"v_max3_f32 v253, v253, v240, v241\n"
"v_max3_f32 v251, v251, v192, v193\n"
"v_max3_f32 v253, v253, v242, v243\n"
"v_max3_f32 v251, v251, v194, v195\n"
"s_sub_u32 s96, s96, 1\n"
"s_cmp_ge_i32 s96, 0\n"
"s_cbranch_scc1 .Lmk_b29l\n"
"v_mov_b32_e32 v248, v253\n"
"v_mov_b32_e32 v247, v251\n"
"s_nop 0\n"
"v_permlane16_swap_b32_e32 v253, v248\n"
"v_permlane16_swap_b32_e32 v251, v247\n"
"v_max_f32_e32 v253, v253, v248\n"
"v_max_f32_e32 v251, v251, v247\n"
"v_mov_b32_e32 v248, v253\n"
"v_mov_b32_e32 v247, v251\n"
"s_nop 0\n"
"v_permlane32_swap_b32_e32 v253, v248\n"
"v_permlane32_swap_b32_e32 v251, v247\n"
"v_max_f32_e32 v253, v253, v248\n"
"v_max_f32_e32 v251, v251, v247\n"
"v_mov_b32_e32 v250, v253\n"
"v_mov_b32_e32 v249, v251\n"
"s_branch .Lmk_a28\n"
".Lmk_p35s:\n"
";\n"
"s_mov_b32 s1, 0x10000\n"
".Lmk_p35l:\n"
"s_add_u32 s98, s56, 0x40000\n"
"buffer_load_dwordx4 v[176:179], v0, s[4:7], s98 offen sc1\n"
"buffer_load_dwordx4 v[200:203], v0, s[4:7], s98 offen offset:1024 sc1\n"
"buffer_load_dwordx4 v[188:191], v0, s[4:7], s98 offen offset:2048 sc1\n"
"buffer_load_dwordx4 v[204:207], v0, s[4:7], s98 offen offset:3072 sc1\n"
"buffer_load_dwordx4 v[152:155], v1, s[4:7], s98 offen sc1\n"
"buffer_load_dwordx4 v[208:211], v1, s[4:7], s98 offen offset:1024 sc1\n"
"buffer_load_dwordx4 v[120:123], v1, s[4:7], s98 offen offset:2048 sc1\n"
"buffer_load_dwordx4 v[132:135], v1, s[4:7], s98 offen offset:3072 sc1\n"
"s_add_u32 s98, s57, 0x40000\n"
"buffer_load_dwordx4 v[216:219], v0, s[4:7], s98 offen sc1\n"
"buffer_load_dwordx4 v[128:131], v0, s[4:7], s98 offen offset:1024 sc1\n"
"buffer_load_dwordx4 v[180:183], v0, s[4:7], s98 offen offset:2048 sc1\n"
"buffer_load_dwordx4 v[100:103], v0, s[4:7], s98 offen offset:3072 sc1\n"
"buffer_load_dwordx4 v[184:187], v1, s[4:7], s98 offen sc1\n"
"buffer_load_dwordx4 v[104:107], v1, s[4:7], s98 offen offset:1024 sc1\n"
"buffer_load_dwordx4 v[108:111], v1, s[4:7], s98 offen offset:2048 sc1\n"
"buffer_load_dwordx4 v[136:139], v1, s[4:7], s98 offen offset:3072 sc1\n"
"s_add_u32 s98, s58, 0x40000\n"
"buffer_load_dwordx4 v[144:147], v0, s[4:7], s98 offen sc1\n"
"buffer_load_dwordx4 v[140:143], v0, s[4:7], s98 offen offset:1024 sc1\n"
"buffer_load_dwordx4 v[116:119], v0, s[4:7], s98 offen offset:2048 sc1\n"
"buffer_load_dwordx4 v[112:115], v0, s[4:7], s98 offen offset:3072 sc1\n"
"buffer_load_dwordx4 v[92:95], v1, s[4:7], s98 offen sc1\n"
"buffer_load_dwordx4 v[96:99], v1, s[4:7], s98 offen offset:1024 sc1\n"
"buffer_load_dwordx4 v[148:151], v1, s[4:7], s98 offen offset:2048 sc1\n"
"s_waitcnt vmcnt(0)\n"
"v_max_u32_e32 v255, v176, v177\n"
"v_max3_u32 v255, v255, v178, v179\n"
"v_max3_u32 v255, v255, v200, v201\n"
"v_max3_u32 v255, v255, v202, v203\n"
"v_max3_u32 v255, v255, v188, v189\n"
"v_max3_u32 v255, v255, v190, v191\n"
"v_max3_u32 v255, v255, v204, v205\n"
"v_max3_u32 v255, v255, v206, v207\n"
"v_max3_u32 v255, v255, v152, v153\n"
"v_max3_u32 v255, v255, v154, v155\n"
"v_max3_u32 v255, v255, v208, v209\n"
"v_max3_u32 v255, v255, v210, v211\n"
"v_max3_u32 v255, v255, v120, v121\n"
"v_max3_u32 v255, v255, v122, v123\n"
"v_max3_u32 v255, v255, v132, v133\n"
"v_max3_u32 v255, v255, v134, v135\n"
"v_cmp_eq_u32_e32 vcc, -1, v255\n"
"s_cbranch_vccz .Lmk_p35k\n"
"s_sub_u32 s1, s1, 1\n"
"s_cbranch_scc0 .Lmk_p35l\n"
".Lmk_p35k:\n"
"v_mfma_f32_16x16x32_bf16 v[88:91], a[128:131], v[176:179], 0\n"
"v_mfma_f32_16x16x32_bf16 v[88:91], a[132:135], v[200:203], v[88:91]\n"
"v_mfma_f32_16x16x32_bf16 v[88:91], a[136:139], v[188:191], v[88:91]\n"
"v_mfma_f32_16x16x32_bf16 v[88:91], a[140:143], v[204:207], v[88:91]\n"
"v_mfma_f32_16x16x32_bf16 v[88:91], a[144:147], v[152:155], v[88:91]\n"
"v_mfma_f32_16x16x32_bf16 v[88:91], a[148:151], v[208:211], v[88:91]\n"
"v_mfma_f32_16x16x32_bf16 v[88:91], a[152:155], v[120:123], v[88:91]\n"
"v_mfma_f32_16x16x32_bf16 v[88:91], a[156:159], v[132:135], v[88:91]\n"
"v_mfma_f32_16x16x32_bf16 v[124:127], a[188:191], v[132:135], 0\n"
"v_mfma_f32_16x16x32_bf16 v[124:127], a[184:187], v[120:123], v[124:127]\n"
"v_mfma_f32_16x16x32_bf16 v[124:127], a[180:183], v[208:211], v[124:127]\n"
"v_mfma_f32_16x16x32_bf16 v[124:127], a[176:179], v[152:155], v[124:127]\n"
"v_mfma_f32_16x16x32_bf16 v[124:127], a[172:175], v[204:207], v[124:127]\n"
"v_mfma_f32_16x16x32_bf16 v[124:127], a[168:171], v[188:191], v[124:127]\n"
"s_nop 7\n"
"s_branch .Lmk_p35r\n"
".Lmk_p36s:\n"
";\n"
"s_mov_b32 s1, 0x10000\n"
".Lmk_p36l:\n"
"s_add_u32 s97, s57, 0x40000\n"
"buffer_load_dwordx4 v[216:219], v0, s[4:7], s97 offen sc1\n"
"buffer_load_dwordx4 v[128:131], v0, s[4:7], s97 offen offset:1024 sc1\n"
"buffer_load_dwordx4 v[180:183], v0, s[4:7], s97 offen offset:2048 sc1\n"
"buffer_load_dwordx4 v[100:103], v0, s[4:7], s97 offen offset:3072 sc1\n"
"buffer_load_dwordx4 v[184:187], v1, s[4:7], s97 offen sc1\n"
"buffer_load_dwordx4 v[104:107], v1, s[4:7], s97 offen offset:1024 sc1\n"
"buffer_load_dwordx4 v[108:111], v1, s[4:7], s97 offen offset:2048 sc1\n"
"buffer_load_dwordx4 v[136:139], v1, s[4:7], s97 offen offset:3072 sc1\n"
"s_add_u32 s97, s58, 0x40000\n"
"buffer_load_dwordx4 v[144:147], v0, s[4:7], s97 offen sc1\n"
"buffer_load_dwordx4 v[140:143], v0, s[4:7], s97 offen offset:1024 sc1\n"
"buffer_load_dwordx4 v[116:119], v0, s[4:7], s97 offen offset:2048 sc1\n"
"buffer_load_dwordx4 v[112:115], v0, s[4:7], s97 offen offset:3072 sc1\n"
"buffer_load_dwordx4 v[92:95], v1, s[4:7], s97 offen sc1\n"
"buffer_load_dwordx4 v[96:99], v1, s[4:7], s97 offen offset:1024 sc1\n"
"buffer_load_dwordx4 v[148:151], v1, s[4:7], s97 offen offset:2048 sc1\n"
"buffer_load_dwordx4 v[88:91], v1, s[4:7], s97 offen offset:3072 sc1\n"
"s_add_u32 s97, s59, 0x40000\n"
"buffer_load_dwordx4 v[160:163], v0, s[4:7], s97 offen sc1\n"
"buffer_load_dwordx4 v[124:127], v0, s[4:7], s97 offen offset:1024 sc1\n"
"buffer_load_dwordx4 v[120:123], v0, s[4:7], s97 offen offset:2048 sc1\n"
"buffer_load_dwordx4 v[132:135], v0, s[4:7], s97 offen offset:3072 sc1\n"
"buffer_load_dwordx4 v[156:159], v1, s[4:7], s97 offen sc1\n"
"buffer_load_dwordx4 v[164:167], v1, s[4:7], s97 offen offset:1024 sc1\n"
"buffer_load_dwordx4 v[172:175], v1, s[4:7], s97 offen offset:2048 sc1\n"
"s_waitcnt vmcnt(0)\n"
"v_max_u32_e32 v255, v216, v217\n"
"v_max3_u32 v255, v255, v218, v219\n"
"v_max3_u32 v255, v255, v128, v129\n"
"v_max3_u32 v255, v255, v130, v131\n"
"v_max3_u32 v255, v255, v180, v181\n"
"v_max3_u32 v255, v255, v182, v183\n"
"v_max3_u32 v255, v255, v100, v101\n"
"v_max3_u32 v255, v255, v102, v103\n"
"v_max3_u32 v255, v255, v184, v185\n"
"v_max3_u32 v255, v255, v186, v187\n"
"v_max3_u32 v255, v255, v104, v105\n"
"v_max3_u32 v255, v255, v106, v107\n"
"v_max3_u32 v255, v255, v108, v109\n"
"v_max3_u32 v255, v255, v110, v111\n"
"v_max3_u32 v255, v255, v136, v137\n"
"v_max3_u32 v255, v255, v138, v139\n"
"v_cmp_eq_u32_e32 vcc, -1, v255\n"
"s_cbranch_vccz .Lmk_p36k\n"
"s_sub_u32 s1, s1, 1\n"
"s_cbranch_scc0 .Lmk_p36l\n"
".Lmk_p36k:\n"
"v_mfma_f32_16x16x32_bf16 v[152:155], a[128:131], v[216:219], 0\n"
"v_mfma_f32_16x16x32_bf16 v[152:155], a[132:135], v[128:131], v[152:155]\n"
"v_mfma_f32_16x16x32_bf16 v[152:155], a[136:139], v[180:183], v[152:155]\n"
"v_mfma_f32_16x16x32_bf16 v[152:155], a[140:143], v[100:103], v[152:155]\n"
"v_mfma_f32_16x16x32_bf16 v[152:155], a[144:147], v[184:187], v[152:155]\n"
"v_mfma_f32_16x16x32_bf16 v[152:155], a[148:151], v[104:107], v[152:155]\n"
"v_mfma_f32_16x16x32_bf16 v[152:155], a[152:155], v[108:111], v[152:155]\n"
"v_mfma_f32_16x16x32_bf16 v[152:155], a[156:159], v[136:139], v[152:155]\n"
"v_mfma_f32_16x16x32_bf16 v[168:171], a[188:191], v[136:139], 0\n"
"v_mfma_f32_16x16x32_bf16 v[168:171], a[184:187], v[108:111], v[168:171]\n"
"v_mfma_f32_16x16x32_bf16 v[168:171], a[180:183], v[104:107], v[168:171]\n"
"v_mfma_f32_16x16x32_bf16 v[168:171], a[176:179], v[184:187], v[168:171]\n"
"v_mfma_f32_16x16x32_bf16 v[168:171], a[172:175], v[100:103], v[168:171]\n"
"v_mfma_f32_16x16x32_bf16 v[168:171], a[168:171], v[180:183], v[168:171]\n"
"s_nop 7\n"
"s_branch .Lmk_p36r\n"
".Lmk_p37s:\n"
";\n"
"s_mov_b32 s1, 0x10000\n"
".Lmk_p37l:\n"
"s_add_u32 s97, s58, 0x40000\n"
"buffer_load_dwordx4 v[144:147], v0, s[4:7], s97 offen sc1\n"
"buffer_load_dwordx4 v[140:143], v0, s[4:7], s97 offen offset:1024 sc1\n"
"buffer_load_dwordx4 v[116:119], v0, s[4:7], s97 offen offset:2048 sc1\n"
"buffer_load_dwordx4 v[112:115], v0, s[4:7], s97 offen offset:3072 sc1\n"
"buffer_load_dwordx4 v[92:95], v1, s[4:7], s97 offen sc1\n"
"buffer_load_dwordx4 v[96:99], v1, s[4:7], s97 offen offset:1024 sc1\n"
"buffer_load_dwordx4 v[148:151], v1, s[4:7], s97 offen offset:2048 sc1\n"
"buffer_load_dwordx4 v[88:91], v1, s[4:7], s97 offen offset:3072 sc1\n"
"s_add_u32 s97, s59, 0x40000\n"
"buffer_load_dwordx4 v[160:163], v0, s[4:7], s97 offen sc1\n"
"buffer_load_dwordx4 v[124:127], v0, s[4:7], s97 offen offset:1024 sc1\n"
"buffer_load_dwordx4 v[120:123], v0, s[4:7], s97 offen offset:2048 sc1\n"
"buffer_load_dwordx4 v[132:135], v0, s[4:7], s97 offen offset:3072 sc1\n"
"buffer_load_dwordx4 v[156:159], v1, s[4:7], s97 offen sc1\n"
"buffer_load_dwordx4 v[164:167], v1, s[4:7], s97 offen offset:1024 sc1\n"
"buffer_load_dwordx4 v[172:175], v1, s[4:7], s97 offen offset:2048 sc1\n"
"buffer_load_dwordx4 v[152:155], v1, s[4:7], s97 offen offset:3072 sc1\n"
"s_waitcnt vmcnt(0)\n"
"v_max_u32_e32 v239, v144, v145\n"
"v_max3_u32 v239, v239, v146, v147\n"
"v_max3_u32 v239, v239, v140, v141\n"
"v_max3_u32 v239, v239, v142, v143\n"
"v_max3_u32 v239, v239, v116, v117\n"
"v_max3_u32 v239, v239, v118, v119\n"
"v_max3_u32 v239, v239, v112, v113\n"
"v_max3_u32 v239, v239, v114, v115\n"
"v_max3_u32 v239, v239, v92, v93\n"
"v_max3_u32 v239, v239, v94, v95\n"
"v_max3_u32 v239, v239, v96, v97\n"
"v_max3_u32 v239, v239, v98, v99\n"
"v_max3_u32 v239, v239, v148, v149\n"
"v_max3_u32 v239, v239, v150, v151\n"
"v_max3_u32 v239, v239, v88, v89\n"
"v_max3_u32 v239, v239, v90, v91\n"
"v_cmp_eq_u32_e32 vcc, -1, v239\n"
"s_cbranch_vccz .Lmk_p37k\n"
"s_sub_u32 s1, s1, 1\n"
"s_cbranch_scc0 .Lmk_p37l\n"
".Lmk_p37k:\n"
"v_mfma_f32_16x16x32_bf16 v[100:103], a[128:131], v[144:147], 0\n"
"v_mfma_f32_16x16x32_bf16 v[100:103], a[132:135], v[140:143], v[100:103]\n"
"v_mfma_f32_16x16x32_bf16 v[100:103], a[136:139], v[116:119], v[100:103]\n"
"v_mfma_f32_16x16x32_bf16 v[100:103], a[140:143], v[112:115], v[100:103]\n"
"v_mfma_f32_16x16x32_bf16 v[100:103], a[144:147], v[92:95], v[100:103]\n"
"v_mfma_f32_16x16x32_bf16 v[100:103], a[148:151], v[96:99], v[100:103]\n"
"v_mfma_f32_16x16x32_bf16 v[100:103], a[152:155], v[148:151], v[100:103]\n"
"v_mfma_f32_16x16x32_bf16 v[100:103], a[156:159], v[88:91], v[100:103]\n"
"v_mfma_f32_16x16x32_bf16 v[104:107], a[188:191], v[88:91], 0\n"
"v_mfma_f32_16x16x32_bf16 v[104:107], a[184:187], v[148:151], v[104:107]\n"
"v_mfma_f32_16x16x32_bf16 v[104:107], a[180:183], v[96:99], v[104:107]\n"
"v_mfma_f32_16x16x32_bf16 v[104:107], a[176:179], v[92:95], v[104:107]\n"
"v_mfma_f32_16x16x32_bf16 v[104:107], a[172:175], v[112:115], v[104:107]\n"
"v_mfma_f32_16x16x32_bf16 v[104:107], a[168:171], v[116:119], v[104:107]\n"
"s_nop 7\n"
"s_branch .Lmk_p37r\n"
".Lmk_p38s:\n"
";\n"
"s_mov_b32 s1, 0x10000\n"
".Lmk_p38l:\n"
"s_add_u32 s97, s59, 0x40000\n"
"buffer_load_dwordx4 v[160:163], v0, s[4:7], s97 offen sc1\n"
"buffer_load_dwordx4 v[124:127], v0, s[4:7], s97 offen offset:1024 sc1\n"
"buffer_load_dwordx4 v[120:123], v0, s[4:7], s97 offen offset:2048 sc1\n"
"buffer_load_dwordx4 v[132:135], v0, s[4:7], s97 offen offset:3072 sc1\n"
"buffer_load_dwordx4 v[156:159], v1, s[4:7], s97 offen sc1\n"
"buffer_load_dwordx4 v[164:167], v1, s[4:7], s97 offen offset:1024 sc1\n"
"buffer_load_dwordx4 v[172:175], v1, s[4:7], s97 offen offset:2048 sc1\n"
"buffer_load_dwordx4 v[152:155], v1, s[4:7], s97 offen offset:3072 sc1\n"
"s_add_u32 s97, s56, 0\n"
"buffer_load_dwordx4 v[100:103], v0, s[4:7], s97 offen sc1\n"
"buffer_load_dwordx4 v[180:183], v0, s[4:7], s97 offen offset:1024 sc1\n"
"buffer_load_dwordx4 v[104:107], v0, s[4:7], s97 offen offset:2048 sc1\n"
"buffer_load_dwordx4 v[88:91], v0, s[4:7], s97 offen offset:3072 sc1\n"
"buffer_load_dwordx4 v[92:95], v1, s[4:7], s97 offen sc1\n"
"buffer_load_dwordx4 v[116:119], v1, s[4:7], s97 offen offset:1024 sc1\n"
"buffer_load_dwordx4 v[136:139], v1, s[4:7], s97 offen offset:2048 sc1\n"
"buffer_load_dwordx4 v[140:143], v1, s[4:7], s97 offen offset:3072 sc1\n"
"s_add_u32 s97, s57, 0\n"
"buffer_load_dwordx4 v[148:151], v0, s[4:7], s97 offen sc1\n"
"s_waitcnt vmcnt(0)\n"
"v_max_u32_e32 v255, v160, v161\n"
"v_max3_u32 v255, v255, v162, v163\n"
"v_max3_u32 v255, v255, v124, v125\n"
"v_max3_u32 v255, v255, v126, v127\n"
"v_max3_u32 v255, v255, v120, v121\n"
"v_max3_u32 v255, v255, v122, v123\n"
"v_max3_u32 v255, v255, v132, v133\n"
"v_max3_u32 v255, v255, v134, v135\n"
"v_max3_u32 v255, v255, v156, v157\n"
"v_max3_u32 v255, v255, v158, v159\n"
"v_max3_u32 v255, v255, v164, v165\n"
"v_max3_u32 v255, v255, v166, v167\n"
"v_max3_u32 v255, v255, v172, v173\n"
"v_max3_u32 v255, v255, v174, v175\n"
"v_max3_u32 v255, v255, v152, v153\n"
"v_max3_u32 v255, v255, v154, v155\n"
"v_cmp_eq_u32_e32 vcc, -1, v255\n"
"s_cbranch_vccz .Lmk_p38k\n"
"s_sub_u32 s1, s1, 1\n"
"s_cbranch_scc0 .Lmk_p38l\n"
".Lmk_p38k:\n"
"v_mfma_f32_16x16x32_bf16 v[128:131], a[128:131], v[160:163], v[108:111]\n"
"v_mfma_f32_16x16x32_bf16 v[128:131], a[132:135], v[124:127], v[128:131]\n"
"v_mfma_f32_16x16x32_bf16 v[128:131], a[136:139], v[120:123], v[128:131]\n"
"v_mfma_f32_16x16x32_bf16 v[128:131], a[140:143], v[132:135], v[128:131]\n"
"v_mfma_f32_16x16x32_bf16 v[128:131], a[144:147], v[156:159], v[128:131]\n"
"v_mfma_f32_16x16x32_bf16 v[128:131], a[148:151], v[164:167], v[128:131]\n"
"v_mfma_f32_16x16x32_bf16 v[128:131], a[152:155], v[172:175], v[128:131]\n"
"v_mfma_f32_16x16x32_bf16 v[128:131], a[156:159], v[152:155], v[128:131]\n"
"v_mfma_f32_16x16x32_bf16 v[144:147], a[188:191], v[152:155], v[168:171]\n"
"v_mfma_f32_16x16x32_bf16 v[144:147], a[184:187], v[172:175], v[144:147]\n"
"v_mfma_f32_16x16x32_bf16 v[144:147], a[180:183], v[164:167], v[144:147]\n"
"v_mfma_f32_16x16x32_bf16 v[144:147], a[176:179], v[156:159], v[144:147]\n"
"v_mfma_f32_16x16x32_bf16 v[144:147], a[172:175], v[132:135], v[144:147]\n"
"v_mfma_f32_16x16x32_bf16 v[144:147], a[168:171], v[120:123], v[144:147]\n"
"s_nop 7\n"
"s_branch .Lmk_p38r\n"
".Lmk_p40s:\n"
";\n"
"s_mov_b32 s1, 0x10000\n"
".Lmk_p40l:\n"
"s_add_u32 s97, s56, 0\n"
"buffer_load_dwordx4 v[100:103], v0, s[4:7], s97 offen sc1\n"
"buffer_load_dwordx4 v[180:183], v0, s[4:7], s97 offen offset:1024 sc1\n"
"buffer_load_dwordx4 v[104:107], v0, s[4:7], s97 offen offset:2048 sc1\n"
"buffer_load_dwordx4 v[88:91], v0, s[4:7], s97 offen offset:3072 sc1\n"
"buffer_load_dwordx4 v[92:95], v1, s[4:7], s97 offen sc1\n"
"buffer_load_dwordx4 v[116:119], v1, s[4:7], s97 offen offset:1024 sc1\n"
"buffer_load_dwordx4 v[136:139], v1, s[4:7], s97 offen offset:2048 sc1\n"
"buffer_load_dwordx4 v[140:143], v1, s[4:7], s97 offen offset:3072 sc1\n"
"s_add_u32 s97, s57, 0\n"
"buffer_load_dwordx4 v[148:151], v0, s[4:7], s97 offen sc1\n"
"buffer_load_dwordx4 v[108:111], v0, s[4:7], s97 offen offset:1024 sc1\n"
"buffer_load_dwordx4 v[112:115], v0, s[4:7], s97 offen offset:2048 sc1\n"
"buffer_load_dwordx4 v[196:199], v0, s[4:7], s97 offen offset:3072 sc1\n"
"buffer_load_dwordx4 v[200:203], v1, s[4:7], s97 offen sc1\n"
"buffer_load_dwordx4 v[128:131], v1, s[4:7], s97 offen offset:1024 sc1\n"
"buffer_load_dwordx4 v[120:123], v1, s[4:7], s97 offen offset:2048 sc1\n"
"buffer_load_dwordx4 v[132:135], v1, s[4:7], s97 offen offset:3072 sc1\n"
"s_add_u32 s97, s58, 0\n"
"buffer_load_dwordx4 v[124:127], v0, s[4:7], s97 offen sc1\n"
"buffer_load_dwordx4 v[144:147], v0, s[4:7], s97 offen offset:1024 sc1\n"
"buffer_load_dwordx4 v[156:159], v0, s[4:7], s97 offen offset:2048 sc1\n"
"s_waitcnt vmcnt(0)\n"
"v_max_u32_e32 v255, v100, v101\n"
"v_max3_u32 v255, v255, v102, v103\n"
"v_max3_u32 v255, v255, v180, v181\n"
"v_max3_u32 v255, v255, v182, v183\n"
"v_max3_u32 v255, v255, v104, v105\n"
"v_max3_u32 v255, v255, v106, v107\n"
"v_max3_u32 v255, v255, v88, v89\n"
"v_max3_u32 v255, v255, v90, v91\n"
"v_max3_u32 v255, v255, v92, v93\n"
"v_max3_u32 v255, v255, v94, v95\n"
"v_max3_u32 v255, v255, v116, v117\n"
"v_max3_u32 v255, v255, v118, v119\n"
"v_max3_u32 v255, v255, v136, v137\n"
"v_max3_u32 v255, v255, v138, v139\n"
"v_max3_u32 v255, v255, v140, v141\n"
"v_max3_u32 v255, v255, v142, v143\n"
"v_cmp_eq_u32_e32 vcc, -1, v255\n"
"s_cbranch_vccz .Lmk_p40k\n"
"s_sub_u32 s1, s1, 1\n"
"s_cbranch_scc0 .Lmk_p40l\n"
".Lmk_p40k:\n"
"v_mfma_f32_16x16x32_bf16 v[96:99], a[128:131], v[100:103], 0\n"
"v_mfma_f32_16x16x32_bf16 v[96:99], a[132:135], v[180:183], v[96:99]\n"
"v_mfma_f32_16x16x32_bf16 v[96:99], a[136:139], v[104:107], v[96:99]\n"
"v_mfma_f32_16x16x32_bf16 v[96:99], a[140:143], v[88:91], v[96:99]\n"
"v_mfma_f32_16x16x32_bf16 v[96:99], a[144:147], v[92:95], v[96:99]\n"
"v_mfma_f32_16x16x32_bf16 v[96:99], a[148:151], v[116:119], v[96:99]\n"
"v_mfma_f32_16x16x32_bf16 v[96:99], a[152:155], v[136:139], v[96:99]\n"
"v_mfma_f32_16x16x32_bf16 v[96:99], a[156:159], v[140:143], v[96:99]\n"
"v_mfma_f32_16x16x32_bf16 v[152:155], a[188:191], v[140:143], 0\n"
"v_mfma_f32_16x16x32_bf16 v[152:155], a[184:187], v[136:139], v[152:155]\n"
"v_mfma_f32_16x16x32_bf16 v[152:155], a[180:183], v[116:119], v[152:155]\n"
"v_mfma_f32_16x16x32_bf16 v[152:155], a[176:179], v[92:95], v[152:155]\n"
"v_mfma_f32_16x16x32_bf16 v[152:155], a[172:175], v[88:91], v[152:155]\n"
"v_mfma_f32_16x16x32_bf16 v[152:155], a[168:171], v[104:107], v[152:155]\n"
"s_nop 7\n"
"s_branch .Lmk_p40r\n"
".Lmk_p41s:\n"
";\n"
"s_mov_b32 s1, 0x10000\n"
".Lmk_p41l:\n"
"s_add_u32 s96, s57, 0\n"
"buffer_load_dwordx4 v[148:151], v0, s[4:7], s96 offen sc1\n"
"buffer_load_dwordx4 v[108:111], v0, s[4:7], s96 offen offset:1024 sc1\n"
"buffer_load_dwordx4 v[112:115], v0, s[4:7], s96 offen offset:2048 sc1\n"
"buffer_load_dwordx4 v[196:199], v0, s[4:7], s96 offen offset:3072 sc1\n"
"buffer_load_dwordx4 v[200:203], v1, s[4:7], s96 offen sc1\n"
"buffer_load_dwordx4 v[128:131], v1, s[4:7], s96 offen offset:1024 sc1\n"
"buffer_load_dwordx4 v[120:123], v1, s[4:7], s96 offen offset:2048 sc1\n"
"buffer_load_dwordx4 v[132:135], v1, s[4:7], s96 offen offset:3072 sc1\n"
"s_add_u32 s96, s58, 0\n"
"buffer_load_dwordx4 v[124:127], v0, s[4:7], s96 offen sc1\n"
"buffer_load_dwordx4 v[144:147], v0, s[4:7], s96 offen offset:1024 sc1\n"
"buffer_load_dwordx4 v[156:159], v0, s[4:7], s96 offen offset:2048 sc1\n"
"buffer_load_dwordx4 v[160:163], v0, s[4:7], s96 offen offset:3072 sc1\n"
"buffer_load_dwordx4 v[164:167], v1, s[4:7], s96 offen sc1\n"
"buffer_load_dwordx4 v[152:155], v1, s[4:7], s96 offen offset:1024 sc1\n"
"buffer_load_dwordx4 v[116:119], v1, s[4:7], s96 offen offset:2048 sc1\n"
"buffer_load_dwordx4 v[136:139], v1, s[4:7], s96 offen offset:3072 sc1\n"
"s_add_u32 s96, s59, 0\n"
"buffer_load_dwordx4 v[172:175], v0, s[4:7], s96 offen sc1\n"
"buffer_load_dwordx4 v[168:171], v0, s[4:7], s96 offen offset:1024 sc1\n"
"buffer_load_dwordx4 v[176:179], v0, s[4:7], s96 offen offset:2048 sc1\n"
"s_waitcnt vmcnt(0)\n"
"v_max_u32_e32 v253, v148, v149\n"
"v_max3_u32 v253, v253, v150, v151\n"
"v_max3_u32 v253, v253, v108, v109\n"
"v_max3_u32 v253, v253, v110, v111\n"
"v_max3_u32 v253, v253, v112, v113\n"
"v_max3_u32 v253, v253, v114, v115\n"
"v_max3_u32 v253, v253, v196, v197\n"
"v_max3_u32 v253, v253, v198, v199\n"
"v_max3_u32 v253, v253, v200, v201\n"
"v_max3_u32 v253, v253, v202, v203\n"
"v_max3_u32 v253, v253, v128, v129\n"
"v_max3_u32 v253, v253, v130, v131\n"
"v_max3_u32 v253, v253, v120, v121\n"
"v_max3_u32 v253, v253, v122, v123\n"
"v_max3_u32 v253, v253, v132, v133\n"
"v_max3_u32 v253, v253, v134, v135\n"
"v_cmp_eq_u32_e32 vcc, -1, v253\n"
"s_cbranch_vccz .Lmk_p41k\n"
"s_sub_u32 s1, s1, 1\n"
"s_cbranch_scc0 .Lmk_p41l\n"
".Lmk_p41k:\n"
"v_mfma_f32_16x16x32_bf16 v[88:91], a[128:131], v[148:151], 0\n"
"v_mfma_f32_16x16x32_bf16 v[88:91], a[132:135], v[108:111], v[88:91]\n"
"v_mfma_f32_16x16x32_bf16 v[88:91], a[136:139], v[112:115], v[88:91]\n"
"v_mfma_f32_16x16x32_bf16 v[88:91], a[140:143], v[196:199], v[88:91]\n"
"v_mfma_f32_16x16x32_bf16 v[88:91], a[144:147], v[200:203], v[88:91]\n"
"v_mfma_f32_16x16x32_bf16 v[88:91], a[148:151], v[128:131], v[88:91]\n"
"v_mfma_f32_16x16x32_bf16 v[88:91], a[152:155], v[120:123], v[88:91]\n"
"v_mfma_f32_16x16x32_bf16 v[88:91], a[156:159], v[132:135], v[88:91]\n"
"v_mfma_f32_16x16x32_bf16 v[92:95], a[188:191], v[132:135], 0\n"
"v_mfma_f32_16x16x32_bf16 v[92:95], a[184:187], v[120:123], v[92:95]\n"
"v_mfma_f32_16x16x32_bf16 v[92:95], a[180:183], v[128:131], v[92:95]\n"
"v_mfma_f32_16x16x32_bf16 v[92:95], a[176:179], v[200:203], v[92:95]\n"
"v_mfma_f32_16x16x32_bf16 v[92:95], a[172:175], v[196:199], v[92:95]\n"
"v_mfma_f32_16x16x32_bf16 v[92:95], a[168:171], v[112:115], v[92:95]\n"
"s_nop 7\n"
"s_branch .Lmk_p41r\n"
".Lmk_p42s:\n"
";\n"
"s_mov_b32 s1, 0x10000\n"
".Lmk_p42l:\n"
"s_add_u32 s96, s58, 0\n"
"buffer_load_dwordx4 v[124:127], v0, s[4:7], s96 offen sc1\n"
"buffer_load_dwordx4 v[144:147], v0, s[4:7], s96 offen offset:1024 sc1\n"
"buffer_load_dwordx4 v[156:159], v0, s[4:7], s96 offen offset:2048 sc1\n"
"buffer_load_dwordx4 v[160:163], v0, s[4:7], s96 offen offset:3072 sc1\n"
"buffer_load_dwordx4 v[164:167], v1, s[4:7], s96 offen sc1\n"
"buffer_load_dwordx4 v[152:155], v1, s[4:7], s96 offen offset:1024 sc1\n"
"buffer_load_dwordx4 v[116:119], v1, s[4:7], s96 offen offset:2048 sc1\n"
"buffer_load_dwordx4 v[136:139], v1, s[4:7], s96 offen offset:3072 sc1\n"
"s_add_u32 s96, s59, 0\n"
"buffer_load_dwordx4 v[172:175], v0, s[4:7], s96 offen sc1\n"
"buffer_load_dwordx4 v[168:171], v0, s[4:7], s96 offen offset:1024 sc1\n"
"buffer_load_dwordx4 v[176:179], v0, s[4:7], s96 offen offset:2048 sc1\n"
"buffer_load_dwordx4 v[180:183], v0, s[4:7], s96 offen offset:3072 sc1\n"
"buffer_load_dwordx4 v[184:187], v1, s[4:7], s96 offen sc1\n"
"buffer_load_dwordx4 v[188:191], v1, s[4:7], s96 offen offset:1024 sc1\n"
"buffer_load_dwordx4 v[192:195], v1, s[4:7], s96 offen offset:2048 sc1\n"
"buffer_load_dwordx4 v[196:199], v1, s[4:7], s96 offen offset:3072 sc1\n"
"s_add_u32 s96, s52, 0x440000\n"
"buffer_load_dwordx4 v[88:91], v0, s[4:7], s96 offen sc1\n"
"buffer_load_dwordx4 v[92:95], v0, s[4:7], s96 offen offset:1024 sc1\n"
"s_waitcnt vmcnt(0)\n"
"v_max_u32_e32 v237, v124, v125\n"
"v_max3_u32 v237, v237, v126, v127\n"
"v_max3_u32 v237, v237, v144, v145\n"
"v_max3_u32 v237, v237, v146, v147\n"
"v_max3_u32 v237, v237, v156, v157\n"
"v_max3_u32 v237, v237, v158, v159\n"
"v_max3_u32 v237, v237, v160, v161\n"
"v_max3_u32 v237, v237, v162, v163\n"
"v_max3_u32 v237, v237, v164, v165\n"
"v_max3_u32 v237, v237, v166, v167\n"
"v_max3_u32 v237, v237, v152, v153\n"
"v_max3_u32 v237, v237, v154, v155\n"
"v_max3_u32 v237, v237, v116, v117\n"
"v_max3_u32 v237, v237, v118, v119\n"
"v_max3_u32 v237, v237, v136, v137\n"
"v_max3_u32 v237, v237, v138, v139\n"
"v_cmp_eq_u32_e32 vcc, -1, v237\n"
"s_cbranch_vccz .Lmk_p42k\n"
"s_sub_u32 s1, s1, 1\n"
"s_cbranch_scc0 .Lmk_p42l\n"
".Lmk_p42k:\n"
"v_mfma_f32_16x16x32_bf16 v[96:99], a[128:131], v[124:127], 0\n"
"v_mfma_f32_16x16x32_bf16 v[96:99], a[132:135], v[144:147], v[96:99]\n"
"v_mfma_f32_16x16x32_bf16 v[96:99], a[136:139], v[156:159], v[96:99]\n"
"v_mfma_f32_16x16x32_bf16 v[96:99], a[140:143], v[160:163], v[96:99]\n"
"v_mfma_f32_16x16x32_bf16 v[96:99], a[144:147], v[164:167], v[96:99]\n"
"v_mfma_f32_16x16x32_bf16 v[96:99], a[148:151], v[152:155], v[96:99]\n"
"v_mfma_f32_16x16x32_bf16 v[96:99], a[152:155], v[116:119], v[96:99]\n"
"v_mfma_f32_16x16x32_bf16 v[96:99], a[156:159], v[136:139], v[96:99]\n"
"v_mfma_f32_16x16x32_bf16 v[104:107], a[188:191], v[136:139], 0\n"
"v_mfma_f32_16x16x32_bf16 v[104:107], a[184:187], v[116:119], v[104:107]\n"
"v_mfma_f32_16x16x32_bf16 v[104:107], a[180:183], v[152:155], v[104:107]\n"
"v_mfma_f32_16x16x32_bf16 v[104:107], a[176:179], v[164:167], v[104:107]\n"
"v_mfma_f32_16x16x32_bf16 v[104:107], a[172:175], v[160:163], v[104:107]\n"
"v_mfma_f32_16x16x32_bf16 v[104:107], a[168:171], v[156:159], v[104:107]\n"
"s_nop 7\n"
"s_branch .Lmk_p42r\n"
".Lmk_f13s:\n"
";\n"
"s_mov_b32 s1, 0x10000\n"
".Lmk_f13l:\n"
"buffer_load_dwordx2 v[254:255], v13, s[4:7], s97 offen sc1\n"
"s_waitcnt vmcnt(0)\n"
"v_cmp_ne_u64_e32 vcc, v[254:255], v[18:19]\n"
"s_cbranch_vccz .Lmk_f13r\n"
"s_sub_u32 s1, s1, 1\n"
"s_cbranch_scc0 .Lmk_f13l\n"
"s_branch .Lmk_f13r\n"
".Lmk_p43s:\n"
";\n"
"s_mov_b32 s1, 0x10000\n"
".Lmk_p43l:\n"
"s_add_u32 s97, s59, 0\n"
"buffer_load_dwordx4 v[172:175], v0, s[4:7], s97 offen sc1\n"
"buffer_load_dwordx4 v[168:171], v0, s[4:7], s97 offen offset:1024 sc1\n"
"buffer_load_dwordx4 v[176:179], v0, s[4:7], s97 offen offset:2048 sc1\n"
"buffer_load_dwordx4 v[180:183], v0, s[4:7], s97 offen offset:3072 sc1\n"
"buffer_load_dwordx4 v[184:187], v1, s[4:7], s97 offen sc1\n"
"buffer_load_dwordx4 v[188:191], v1, s[4:7], s97 offen offset:1024 sc1\n"
"buffer_load_dwordx4 v[192:195], v1, s[4:7], s97 offen offset:2048 sc1\n"
"buffer_load_dwordx4 v[196:199], v1, s[4:7], s97 offen offset:3072 sc1\n"
"s_add_u32 s97, s52, 0x440000\n"
"buffer_load_dwordx4 v[88:91], v0, s[4:7], s97 offen sc1\n"
"buffer_load_dwordx4 v[92:95], v0, s[4:7], s97 offen offset:1024 sc1\n"
"buffer_load_dwordx4 v[96:99], v0, s[4:7], s97 offen offset:2048 sc1\n"
"buffer_load_dwordx4 v[100:103], v0, s[4:7], s97 offen offset:3072 sc1\n"
"buffer_load_dwordx4 v[104:107], v1, s[4:7], s97 offen sc1\n"
"buffer_load_dwordx4 v[112:115], v1, s[4:7], s97 offen offset:1024 sc1\n"
"buffer_load_dwordx4 v[116:119], v1, s[4:7], s97 offen offset:2048 sc1\n"
"buffer_load_dwordx4 v[124:127], v1, s[4:7], s97 offen offset:3072 sc1\n"
"s_add_u32 s97, s53, 0x440000\n"
"buffer_load_dwordx4 v[128:131], v0, s[4:7], s97 offen sc1\n"
"buffer_load_dwordx4 v[132:135], v0, s[4:7], s97 offen offset:1024 sc1\n"
"buffer_load_dwordx4 v[136:139], v0, s[4:7], s97 offen offset:2048 sc1\n"
"s_waitcnt vmcnt(0)\n"
"v_max_u32_e32 v255, v172, v173\n"
"v_max3_u32 v255, v255, v174, v175\n"
"v_max3_u32 v255, v255, v168, v169\n"
"v_max3_u32 v255, v255, v170, v171\n"
"v_max3_u32 v255, v255, v176, v177\n"
"v_max3_u32 v255, v255, v178, v179\n"
"v_max3_u32 v255, v255, v180, v181\n"
"v_max3_u32 v255, v255, v182, v183\n"
"v_max3_u32 v255, v255, v184, v185\n"
"v_max3_u32 v255, v255, v186, v187\n"
"v_max3_u32 v255, v255, v188, v189\n"
"v_max3_u32 v255, v255, v190, v191\n"
"v_max3_u32 v255, v255, v192, v193\n"
"v_max3_u32 v255, v255, v194, v195\n"
"v_max3_u32 v255, v255, v196, v197\n"
"v_max3_u32 v255, v255, v198, v199\n"
"v_cmp_eq_u32_e32 vcc, -1, v255\n"
"s_cbranch_vccz .Lmk_p43k\n"
"s_sub_u32 s1, s1, 1\n"
"s_cbranch_scc0 .Lmk_p43l\n"
".Lmk_p43k:\n"
"v_mfma_f32_16x16x32_bf16 v[204:207], a[128:131], v[172:175], v[140:143]\n"
"v_mfma_f32_16x16x32_bf16 v[204:207], a[132:135], v[168:171], v[204:207]\n"
"v_mfma_f32_16x16x32_bf16 v[204:207], a[136:139], v[176:179], v[204:207]\n"
"v_mfma_f32_16x16x32_bf16 v[204:207], a[140:143], v[180:183], v[204:207]\n"
"v_mfma_f32_16x16x32_bf16 v[204:207], a[144:147], v[184:187], v[204:207]\n"
"v_mfma_f32_16x16x32_bf16 v[204:207], a[148:151], v[188:191], v[204:207]\n"
"v_mfma_f32_16x16x32_bf16 v[204:207], a[152:155], v[192:195], v[204:207]\n"
"v_mfma_f32_16x16x32_bf16 v[204:207], a[156:159], v[196:199], v[204:207]\n"
"v_mfma_f32_16x16x32_bf16 v[208:211], a[188:191], v[196:199], v[148:151]\n"
"v_mfma_f32_16x16x32_bf16 v[208:211], a[184:187], v[192:195], v[208:211]\n"
"v_mfma_f32_16x16x32_bf16 v[208:211], a[180:183], v[188:191], v[208:211]\n"
"v_mfma_f32_16x16x32_bf16 v[208:211], a[176:179], v[184:187], v[208:211]\n"
"v_mfma_f32_16x16x32_bf16 v[208:211], a[172:175], v[180:183], v[208:211]\n"
"v_mfma_f32_16x16x32_bf16 v[208:211], a[168:171], v[176:179], v[208:211]\n"
"s_nop 7\n"
"s_branch .Lmk_p43r\n"
".Lmk_p48s:\n"
";\n"
"s_mov_b32 s1, 0x10000\n"
".Lmk_p48l:\n"
"s_add_u32 s96, s52, 0x440000\n"
"buffer_load_dwordx4 v[88:91], v0, s[4:7], s96 offen sc1\n"
"buffer_load_dwordx4 v[92:95], v0, s[4:7], s96 offen offset:1024 sc1\n"
"buffer_load_dwordx4 v[96:99], v0, s[4:7], s96 offen offset:2048 sc1\n"
"buffer_load_dwordx4 v[100:103], v0, s[4:7], s96 offen offset:3072 sc1\n"
"buffer_load_dwordx4 v[104:107], v1, s[4:7], s96 offen sc1\n"
"buffer_load_dwordx4 v[112:115], v1, s[4:7], s96 offen offset:1024 sc1\n"
"buffer_load_dwordx4 v[116:119], v1, s[4:7], s96 offen offset:2048 sc1\n"
"buffer_load_dwordx4 v[124:127], v1, s[4:7], s96 offen offset:3072 sc1\n"
"s_add_u32 s96, s53, 0x440000\n"
"buffer_load_dwordx4 v[128:131], v0, s[4:7], s96 offen sc1\n"
"buffer_load_dwordx4 v[132:135], v0, s[4:7], s96 offen offset:1024 sc1\n"
"buffer_load_dwordx4 v[136:139], v0, s[4:7], s96 offen offset:2048 sc1\n"
"buffer_load_dwordx4 v[140:143], v0, s[4:7], s96 offen offset:3072 sc1\n"
"buffer_load_dwordx4 v[144:147], v1, s[4:7], s96 offen sc1\n"
"buffer_load_dwordx4 v[148:151], v1, s[4:7], s96 offen offset:1024 sc1\n"
"buffer_load_dwordx4 v[160:163], v1, s[4:7], s96 offen offset:2048 sc1\n"
"buffer_load_dwordx4 v[164:167], v1, s[4:7], s96 offen offset:3072 sc1\n"
"s_add_u32 s96, s54, 0x440000\n"
"buffer_load_dwordx4 v[176:179], v0, s[4:7], s96 offen sc1\n"
"buffer_load_dwordx4 v[184:187], v0, s[4:7], s96 offen offset:1024 sc1\n"
"s_waitcnt vmcnt(0)\n"
"v_max_u32_e32 v252, v88, v89\n"
"v_max3_u32 v252, v252, v90, v91\n"
"v_max3_u32 v252, v252, v92, v93\n"
"v_max3_u32 v252, v252, v94, v95\n"
"v_max3_u32 v252, v252, v96, v97\n"
"v_max3_u32 v252, v252, v98, v99\n"
"v_max3_u32 v252, v252, v100, v101\n"
"v_max3_u32 v252, v252, v102, v103\n"
"v_max3_u32 v252, v252, v104, v105\n"
"v_max3_u32 v252, v252, v106, v107\n"
"v_max3_u32 v252, v252, v112, v113\n"
"v_max3_u32 v252, v252, v114, v115\n"
"v_max3_u32 v252, v252, v116, v117\n"
"v_max3_u32 v252, v252, v118, v119\n"
"v_max3_u32 v252, v252, v124, v125\n"
"v_max3_u32 v252, v252, v126, v127\n"
"v_cmp_eq_u32_e32 vcc, -1, v252\n"
"s_cbranch_vccz .Lmk_p48k\n"
"s_sub_u32 s1, s1, 1\n"
"s_cbranch_scc0 .Lmk_p48l\n"
".Lmk_p48k:\n"
"v_mfma_f32_16x16x32_bf16 v[172:175], a[0:3], v[88:91], 0\n"
"v_mfma_f32_16x16x32_bf16 v[172:175], a[4:7], v[92:95], v[172:175]\n"
"v_mfma_f32_16x16x32_bf16 v[172:175], a[8:11], v[96:99], v[172:175]\n"
"v_mfma_f32_16x16x32_bf16 v[172:175], a[12:15], v[100:103], v[172:175]\n"
"v_mfma_f32_16x16x32_bf16 v[172:175], a[16:19], v[104:107], v[172:175]\n"
"v_mfma_f32_16x16x32_bf16 v[172:175], a[20:23], v[112:115], v[172:175]\n"
"v_mfma_f32_16x16x32_bf16 v[172:175], a[24:27], v[116:119], v[172:175]\n"
"v_mfma_f32_16x16x32_bf16 v[172:175], a[28:31], v[124:127], v[172:175]\n"
"v_mfma_f32_16x16x32_bf16 v[188:191], a[60:63], v[124:127], 0\n"
"v_mfma_f32_16x16x32_bf16 v[188:191], a[56:59], v[116:119], v[188:191]\n"
"v_mfma_f32_16x16x32_bf16 v[188:191], a[52:55], v[112:115], v[188:191]\n"
"v_mfma_f32_16x16x32_bf16 v[188:191], a[48:51], v[104:107], v[188:191]\n"
"v_mfma_f32_16x16x32_bf16 v[188:191], a[44:47], v[100:103], v[188:191]\n"
"v_mfma_f32_16x16x32_bf16 v[188:191], a[40:43], v[96:99], v[188:191]\n"
"s_nop 7\n"
"s_branch .Lmk_p48r\n"
".Lmk_p49s:\n"
";\n"
"s_mov_b32 s1, 0x10000\n"
".Lmk_p49l:\n"
"s_add_u32 s96, s53, 0x440000\n"
"buffer_load_dwordx4 v[128:131], v0, s[4:7], s96 offen sc1\n"
"buffer_load_dwordx4 v[132:135], v0, s[4:7], s96 offen offset:1024 sc1\n"
"buffer_load_dwordx4 v[136:139], v0, s[4:7], s96 offen offset:2048 sc1\n"
"buffer_load_dwordx4 v[140:143], v0, s[4:7], s96 offen offset:3072 sc1\n"
"buffer_load_dwordx4 v[144:147], v1, s[4:7], s96 offen sc1\n"
"buffer_load_dwordx4 v[148:151], v1, s[4:7], s96 offen offset:1024 sc1\n"
"buffer_load_dwordx4 v[160:163], v1, s[4:7], s96 offen offset:2048 sc1\n"
"buffer_load_dwordx4 v[164:167], v1, s[4:7], s96 offen offset:3072 sc1\n"
"s_add_u32 s96, s54, 0x440000\n"
"buffer_load_dwordx4 v[176:179], v0, s[4:7], s96 offen sc1\n"
"buffer_load_dwordx4 v[184:187], v0, s[4:7], s96 offen offset:1024 sc1\n"
"buffer_load_dwordx4 v[108:111], v0, s[4:7], s96 offen offset:2048 sc1\n"
"buffer_load_dwordx4 v[192:195], v0, s[4:7], s96 offen offset:3072 sc1\n"
"buffer_load_dwordx4 v[152:155], v1, s[4:7], s96 offen sc1\n"
"buffer_load_dwordx4 v[156:159], v1, s[4:7], s96 offen offset:1024 sc1\n"
"buffer_load_dwordx4 v[172:175], v1, s[4:7], s96 offen offset:2048 sc1\n"
"buffer_load_dwordx4 v[180:183], v1, s[4:7], s96 offen offset:3072 sc1\n"
"s_add_u32 s96, s55, 0x440000\n"
"buffer_load_dwordx4 v[92:95], v0, s[4:7], s96 offen sc1\n"
"buffer_load_dwordx4 v[96:99], v0, s[4:7], s96 offen offset:1024 sc1\n"
"s_waitcnt vmcnt(0)\n"
"v_max_u32_e32 v247, v128, v129\n"
"v_max3_u32 v247, v247, v130, v131\n"
"v_max3_u32 v247, v247, v132, v133\n"
"v_max3_u32 v247, v247, v134, v135\n"
"v_max3_u32 v247, v247, v136, v137\n"
"v_max3_u32 v247, v247, v138, v139\n"
"v_max3_u32 v247, v247, v140, v141\n"
"v_max3_u32 v247, v247, v142, v143\n"
"v_max3_u32 v247, v247, v144, v145\n"
"v_max3_u32 v247, v247, v146, v147\n"
"v_max3_u32 v247, v247, v148, v149\n"
"v_max3_u32 v247, v247, v150, v151\n"
"v_max3_u32 v247, v247, v160, v161\n"
"v_max3_u32 v247, v247, v162, v163\n"
"v_max3_u32 v247, v247, v164, v165\n"
"v_max3_u32 v247, v247, v166, v167\n"
"v_cmp_eq_u32_e32 vcc, -1, v247\n"
"s_cbranch_vccz .Lmk_p49k\n"
"s_sub_u32 s1, s1, 1\n"
"s_cbranch_scc0 .Lmk_p49l\n"
".Lmk_p49k:\n"
"v_mfma_f32_16x16x32_bf16 v[88:91], a[0:3], v[128:131], 0\n"
"v_mfma_f32_16x16x32_bf16 v[88:91], a[4:7], v[132:135], v[88:91]\n"
"v_mfma_f32_16x16x32_bf16 v[88:91], a[8:11], v[136:139], v[88:91]\n"
"v_mfma_f32_16x16x32_bf16 v[88:91], a[12:15], v[140:143], v[88:91]\n"
"v_mfma_f32_16x16x32_bf16 v[88:91], a[16:19], v[144:147], v[88:91]\n"
"v_mfma_f32_16x16x32_bf16 v[88:91], a[20:23], v[148:151], v[88:91]\n"
"v_mfma_f32_16x16x32_bf16 v[88:91], a[24:27], v[160:163], v[88:91]\n"
"v_mfma_f32_16x16x32_bf16 v[88:91], a[28:31], v[164:167], v[88:91]\n"
"v_mfma_f32_16x16x32_bf16 v[100:103], a[60:63], v[164:167], 0\n"
"v_mfma_f32_16x16x32_bf16 v[100:103], a[56:59], v[160:163], v[100:103]\n"
"v_mfma_f32_16x16x32_bf16 v[100:103], a[52:55], v[148:151], v[100:103]\n"
"v_mfma_f32_16x16x32_bf16 v[100:103], a[48:51], v[144:147], v[100:103]\n"
"v_mfma_f32_16x16x32_bf16 v[100:103], a[44:47], v[140:143], v[100:103]\n"
"v_mfma_f32_16x16x32_bf16 v[100:103], a[40:43], v[136:139], v[100:103]\n"
"s_nop 7\n"
"s_branch .Lmk_p49r\n"
".Lmk_m52s:\n"
";\n"
"s_mov_b32 s1, 0x10000\n"
".Lmk_m52l:\n"
"s_add_u32 s98, s62, 0x44000\n"
"buffer_load_dword v255, v10, s[4:7], s98 offen sc1\n"
"buffer_load_dword v254, v10, s[4:7], s98 offen offset:256 sc1\n"
"buffer_load_dword v253, v10, s[4:7], s98 offen offset:512 sc1\n"
"buffer_load_dword v252, v10, s[4:7], s98 offen offset:768 sc1\n"
"buffer_load_dword v251, v10, s[4:7], s98 offen offset:1024 sc1\n"
"buffer_load_dword v250, v10, s[4:7], s98 offen offset:1280 sc1\n"
"buffer_load_dword v249, v10, s[4:7], s98 offen offset:1536 sc1\n"
"buffer_load_dword v248, v10, s[4:7], s98 offen offset:1792 sc1\n"
"s_waitcnt vmcnt(0)\n"
"v_max_u32_e32 v247, v255, v254\n"
"v_max3_u32 v247, v247, v253, v252\n"
"v_max3_u32 v247, v247, v251, v250\n"
"v_max3_u32 v247, v247, v249, v248\n"
"v_cmp_eq_u32_e32 vcc, -1, v247\n"
"s_cbranch_vccz .Lmk_m52k\n"
"s_sub_u32 s1, s1, 1\n"
"s_cbranch_scc0 .Lmk_m52l\n"
".Lmk_m52k:\n"
"v_add_f32_e32 v255, v255, v251\n"
"v_add_f32_e32 v254, v254, v250\n"
"v_add_f32_e32 v253, v253, v249\n"
"v_add_f32_e32 v252, v252, v248\n"
"v_add_f32_e32 v255, v255, v253\n"
"v_add_f32_e32 v254, v254, v252\n"
"v_add_f32_e32 v255, v255, v254\n"
"s_nop 1\n"
"s_branch .Lmk_m52r\n"
".Lmk_p50s:\n"
";\n"
"s_mov_b32 s1, 0x10000\n"
".Lmk_p50l:\n"
"s_add_u32 s96, s54, 0x440000\n"
"buffer_load_dwordx4 v[176:179], v0, s[4:7], s96 offen sc1\n"
"buffer_load_dwordx4 v[184:187], v0, s[4:7], s96 offen offset:1024 sc1\n"
"buffer_load_dwordx4 v[108:111], v0, s[4:7], s96 offen offset:2048 sc1\n"
"buffer_load_dwordx4 v[192:195], v0, s[4:7], s96 offen offset:3072 sc1\n"
"buffer_load_dwordx4 v[152:155], v1, s[4:7], s96 offen sc1\n"
"buffer_load_dwordx4 v[156:159], v1, s[4:7], s96 offen offset:1024 sc1\n"
"buffer_load_dwordx4 v[172:175], v1, s[4:7], s96 offen offset:2048 sc1\n"
"buffer_load_dwordx4 v[180:183], v1, s[4:7], s96 offen offset:3072 sc1\n"
"s_add_u32 s96, s55, 0x440000\n"
"buffer_load_dwordx4 v[92:95], v0, s[4:7], s96 offen sc1\n"
"buffer_load_dwordx4 v[96:99], v0, s[4:7], s96 offen offset:1024 sc1\n"
"buffer_load_dwordx4 v[104:107], v0, s[4:7], s96 offen offset:2048 sc1\n"
"buffer_load_dwordx4 v[112:115], v0, s[4:7], s96 offen offset:3072 sc1\n"
"buffer_load_dwordx4 v[100:103], v1, s[4:7], s96 offen sc1\n"
"buffer_load_dwordx4 v[124:127], v1, s[4:7], s96 offen offset:1024 sc1\n"
"buffer_load_dwordx4 v[168:171], v1, s[4:7], s96 offen offset:2048 sc1\n"
"buffer_load_dwordx4 v[188:191], v1, s[4:7], s96 offen offset:3072 sc1\n"
"s_add_u32 s96, s52, 0x448000\n"
"buffer_load_dwordx4 v[128:131], v0, s[4:7], s96 offen sc1\n"
"s_waitcnt vmcnt(0)\n"
"v_max_u32_e32 v255, v176, v177\n"
"v_max3_u32 v255, v255, v178, v179\n"
"v_max3_u32 v255, v255, v184, v185\n"
"v_max3_u32 v255, v255, v186, v187\n"
"v_max3_u32 v255, v255, v108, v109\n"
"v_max3_u32 v255, v255, v110, v111\n"
"v_max3_u32 v255, v255, v192, v193\n"
"v_max3_u32 v255, v255, v194, v195\n"
"v_max3_u32 v255, v255, v152, v153\n"
"v_max3_u32 v255, v255, v154, v155\n"
"v_max3_u32 v255, v255, v156, v157\n"
"v_max3_u32 v255, v255, v158, v159\n"
"v_max3_u32 v255, v255, v172, v173\n"
"v_max3_u32 v255, v255, v174, v175\n"
"v_max3_u32 v255, v255, v180, v181\n"
"v_max3_u32 v255, v255, v182, v183\n"
"v_cmp_eq_u32_e32 vcc, -1, v255\n"
"s_cbranch_vccz .Lmk_p50k\n"
"s_sub_u32 s1, s1, 1\n"
"s_cbranch_scc0 .Lmk_p50l\n"
".Lmk_p50k:\n"
"v_mfma_f32_16x16x32_bf16 v[88:91], a[0:3], v[176:179], 0\n"
"v_mfma_f32_16x16x32_bf16 v[88:91], a[4:7], v[184:187], v[88:91]\n"
"v_mfma_f32_16x16x32_bf16 v[88:91], a[8:11], v[108:111], v[88:91]\n"
"v_mfma_f32_16x16x32_bf16 v[88:91], a[12:15], v[192:195], v[88:91]\n"
"v_mfma_f32_16x16x32_bf16 v[88:91], a[16:19], v[152:155], v[88:91]\n"
"v_mfma_f32_16x16x32_bf16 v[88:91], a[20:23], v[156:159], v[88:91]\n"
"v_mfma_f32_16x16x32_bf16 v[88:91], a[24:27], v[172:175], v[88:91]\n"
"v_mfma_f32_16x16x32_bf16 v[88:91], a[28:31], v[180:183], v[88:91]\n"
"v_mfma_f32_16x16x32_bf16 v[116:119], a[60:63], v[180:183], 0\n"
"v_mfma_f32_16x16x32_bf16 v[116:119], a[56:59], v[172:175], v[116:119]\n"
"v_mfma_f32_16x16x32_bf16 v[116:119], a[52:55], v[156:159], v[116:119]\n"
"v_mfma_f32_16x16x32_bf16 v[116:119], a[48:51], v[152:155], v[116:119]\n"
"v_mfma_f32_16x16x32_bf16 v[116:119], a[44:47], v[192:195], v[116:119]\n"
"v_mfma_f32_16x16x32_bf16 v[116:119], a[40:43], v[108:111], v[116:119]\n"
"s_nop 7\n"
"s_branch .Lmk_p50r\n"
".Lmk_p51s:\n"
";\n"
"s_mov_b32 s1, 0x10000\n"
".Lmk_p51l:\n"
"s_add_u32 s96, s55, 0x440000\n"
"buffer_load_dwordx4 v[92:95], v0, s[4:7], s96 offen sc1\n"
"buffer_load_dwordx4 v[96:99], v0, s[4:7], s96 offen offset:1024 sc1\n"
"buffer_load_dwordx4 v[104:107], v0, s[4:7], s96 offen offset:2048 sc1\n"
"buffer_load_dwordx4 v[112:115], v0, s[4:7], s96 offen offset:3072 sc1\n"
"buffer_load_dwordx4 v[100:103], v1, s[4:7], s96 offen sc1\n"
"buffer_load_dwordx4 v[124:127], v1, s[4:7], s96 offen offset:1024 sc1\n"
"buffer_load_dwordx4 v[168:171], v1, s[4:7], s96 offen offset:2048 sc1\n"
"buffer_load_dwordx4 v[188:191], v1, s[4:7], s96 offen offset:3072 sc1\n"
"s_add_u32 s96, s52, 0x448000\n"
"buffer_load_dwordx4 v[128:131], v0, s[4:7], s96 offen sc1\n"
"buffer_load_dwordx4 v[88:91], v0, s[4:7], s96 offen offset:1024 sc1\n"
"buffer_load_dwordx4 v[164:167], v0, s[4:7], s96 offen offset:2048 sc1\n"
"buffer_load_dwordx4 v[140:143], v0, s[4:7], s96 offen offset:3072 sc1\n"
"buffer_load_dwordx4 v[132:135], v1, s[4:7], s96 offen sc1\n"
"buffer_load_dwordx4 v[224:227], v1, s[4:7], s96 offen offset:1024 sc1\n"
"buffer_load_dwordx4 v[180:183], v1, s[4:7], s96 offen offset:2048 sc1\n"
"buffer_load_dwordx4 v[108:111], v1, s[4:7], s96 offen offset:3072 sc1\n"
"s_add_u32 s96, s53, 0x448000\n"
"buffer_load_dwordx4 v[116:119], v0, s[4:7], s96 offen sc1\n"
"buffer_load_dwordx4 v[160:163], v0, s[4:7], s96 offen offset:1024 sc1\n"
"s_waitcnt vmcnt(0)\n"
"v_max_u32_e32 v252, v92, v93\n"
"v_max3_u32 v252, v252, v94, v95\n"
"v_max3_u32 v252, v252, v96, v97\n"
"v_max3_u32 v252, v252, v98, v99\n"
"v_max3_u32 v252, v252, v104, v105\n"
"v_max3_u32 v252, v252, v106, v107\n"
"v_max3_u32 v252, v252, v112, v113\n"
"v_max3_u32 v252, v252, v114, v115\n"
"v_max3_u32 v252, v252, v100, v101\n"
"v_max3_u32 v252, v252, v102, v103\n"
"v_max3_u32 v252, v252, v124, v125\n"
"v_max3_u32 v252, v252, v126, v127\n"
"v_max3_u32 v252, v252, v168, v169\n"
"v_max3_u32 v252, v252, v170, v171\n"
"v_max3_u32 v252, v252, v188, v189\n"
"v_max3_u32 v252, v252, v190, v191\n"
"v_cmp_eq_u32_e32 vcc, -1, v252\n"
"s_cbranch_vccz .Lmk_p51k\n"
"s_sub_u32 s1, s1, 1\n"
"s_cbranch_scc0 .Lmk_p51l\n"
".Lmk_p51k:\n"
"v_mfma_f32_16x16x32_bf16 v[192:195], a[0:3], v[92:95], v[136:139]\n"
"v_mfma_f32_16x16x32_bf16 v[192:195], a[4:7], v[96:99], v[192:195]\n"
"v_mfma_f32_16x16x32_bf16 v[192:195], a[8:11], v[104:107], v[192:195]\n"
"v_mfma_f32_16x16x32_bf16 v[192:195], a[12:15], v[112:115], v[192:195]\n"
"v_mfma_f32_16x16x32_bf16 v[192:195], a[16:19], v[100:103], v[192:195]\n"
"v_mfma_f32_16x16x32_bf16 v[192:195], a[20:23], v[124:127], v[192:195]\n"
"v_mfma_f32_16x16x32_bf16 v[192:195], a[24:27], v[168:171], v[192:195]\n"
"v_mfma_f32_16x16x32_bf16 v[192:195], a[28:31], v[188:191], v[192:195]\n"
"v_mfma_f32_16x16x32_bf16 v[204:207], a[60:63], v[188:191], v[148:151]\n"
"v_mfma_f32_16x16x32_bf16 v[204:207], a[56:59], v[168:171], v[204:207]\n"
"v_mfma_f32_16x16x32_bf16 v[204:207], a[52:55], v[124:127], v[204:207]\n"
"v_mfma_f32_16x16x32_bf16 v[204:207], a[48:51], v[100:103], v[204:207]\n"
"v_mfma_f32_16x16x32_bf16 v[204:207], a[44:47], v[112:115], v[204:207]\n"
"v_mfma_f32_16x16x32_bf16 v[204:207], a[40:43], v[104:107], v[204:207]\n"
"s_nop 7\n"
"s_branch .Lmk_p51r\n"
".Lmk_p53s:\n"
";\n"
"s_mov_b32 s1, 0x10000\n"
".Lmk_p53l:\n"
"s_add_u32 s97, s52, 0x448000\n"
"buffer_load_dwordx4 v[128:131], v0, s[4:7], s97 offen sc1\n"
"buffer_load_dwordx4 v[88:91], v0, s[4:7], s97 offen offset:1024 sc1\n"
"buffer_load_dwordx4 v[164:167], v0, s[4:7], s97 offen offset:2048 sc1\n"
"buffer_load_dwordx4 v[140:143], v0, s[4:7], s97 offen offset:3072 sc1\n"
"buffer_load_dwordx4 v[132:135], v1, s[4:7], s97 offen sc1\n"
"buffer_load_dwordx4 v[224:227], v1, s[4:7], s97 offen offset:1024 sc1\n"
"buffer_load_dwordx4 v[180:183], v1, s[4:7], s97 offen offset:2048 sc1\n"
"buffer_load_dwordx4 v[108:111], v1, s[4:7], s97 offen offset:3072 sc1\n"
"s_add_u32 s97, s53, 0x448000\n"
"buffer_load_dwordx4 v[116:119], v0, s[4:7], s97 offen sc1\n"
"buffer_load_dwordx4 v[160:163], v0, s[4:7], s97 offen offset:1024 sc1\n"
"buffer_load_dwordx4 v[136:139], v0, s[4:7], s97 offen offset:2048 sc1\n"
"buffer_load_dwordx4 v[148:151], v0, s[4:7], s97 offen offset:3072 sc1\n"
"buffer_load_dwordx4 v[156:159], v1, s[4:7], s97 offen sc1\n"
"buffer_load_dwordx4 v[172:175], v1, s[4:7], s97 offen offset:1024 sc1\n"
"buffer_load_dwordx4 v[184:187], v1, s[4:7], s97 offen offset:2048 sc1\n"
"buffer_load_dwordx4 v[196:199], v1, s[4:7], s97 offen offset:3072 sc1\n"
"s_add_u32 s97, s54, 0x448000\n"
"buffer_load_dwordx4 v[96:99], v0, s[4:7], s97 offen sc1\n"
"buffer_load_dwordx4 v[100:103], v0, s[4:7], s97 offen offset:1024 sc1\n"
"s_waitcnt vmcnt(0)\n"
"v_max_u32_e32 v252, v128, v129\n"
"v_max3_u32 v252, v252, v130, v131\n"
"v_max3_u32 v252, v252, v88, v89\n"
"v_max3_u32 v252, v252, v90, v91\n"
"v_max3_u32 v252, v252, v164, v165\n"
"v_max3_u32 v252, v252, v166, v167\n"
"v_max3_u32 v252, v252, v140, v141\n"
"v_max3_u32 v252, v252, v142, v143\n"
"v_max3_u32 v252, v252, v132, v133\n"
"v_max3_u32 v252, v252, v134, v135\n"
"v_max3_u32 v252, v252, v224, v225\n"
"v_max3_u32 v252, v252, v226, v227\n"
"v_max3_u32 v252, v252, v180, v181\n"
"v_max3_u32 v252, v252, v182, v183\n"
"v_max3_u32 v252, v252, v108, v109\n"
"v_max3_u32 v252, v252, v110, v111\n"
"v_cmp_eq_u32_e32 vcc, -1, v252\n"
"s_cbranch_vccz .Lmk_p53k\n"
"s_sub_u32 s1, s1, 1\n"
"s_cbranch_scc0 .Lmk_p53l\n"
".Lmk_p53k:\n"
"v_mfma_f32_16x16x32_bf16 v[92:95], a[0:3], v[128:131], 0\n"
"v_mfma_f32_16x16x32_bf16 v[92:95], a[4:7], v[88:91], v[92:95]\n"
"v_mfma_f32_16x16x32_bf16 v[92:95], a[8:11], v[164:167], v[92:95]\n"
"v_mfma_f32_16x16x32_bf16 v[92:95], a[12:15], v[140:143], v[92:95]\n"
"v_mfma_f32_16x16x32_bf16 v[92:95], a[16:19], v[132:135], v[92:95]\n"
"v_mfma_f32_16x16x32_bf16 v[92:95], a[20:23], v[224:227], v[92:95]\n"
"v_mfma_f32_16x16x32_bf16 v[92:95], a[24:27], v[180:183], v[92:95]\n"
"v_mfma_f32_16x16x32_bf16 v[92:95], a[28:31], v[108:111], v[92:95]\n"
"v_mfma_f32_16x16x32_bf16 v[104:107], a[60:63], v[108:111], 0\n"
"v_mfma_f32_16x16x32_bf16 v[104:107], a[56:59], v[180:183], v[104:107]\n"
"v_mfma_f32_16x16x32_bf16 v[104:107], a[52:55], v[224:227], v[104:107]\n"
"v_mfma_f32_16x16x32_bf16 v[104:107], a[48:51], v[132:135], v[104:107]\n"
"v_mfma_f32_16x16x32_bf16 v[104:107], a[44:47], v[140:143], v[104:107]\n"
"v_mfma_f32_16x16x32_bf16 v[104:107], a[40:43], v[164:167], v[104:107]\n"
"s_nop 7\n"
"s_branch .Lmk_p53r\n"
".Lmk_p54s:\n"
";\n"
"s_mov_b32 s1, 0x10000\n"
".Lmk_p54l:\n"
"s_add_u32 s95, s53, 0x448000\n"
"buffer_load_dwordx4 v[116:119], v0, s[4:7], s95 offen sc1\n"
"buffer_load_dwordx4 v[160:163], v0, s[4:7], s95 offen offset:1024 sc1\n"
"buffer_load_dwordx4 v[136:139], v0, s[4:7], s95 offen offset:2048 sc1\n"
"buffer_load_dwordx4 v[148:151], v0, s[4:7], s95 offen offset:3072 sc1\n"
"buffer_load_dwordx4 v[156:159], v1, s[4:7], s95 offen sc1\n"
"buffer_load_dwordx4 v[172:175], v1, s[4:7], s95 offen offset:1024 sc1\n"
"buffer_load_dwordx4 v[184:187], v1, s[4:7], s95 offen offset:2048 sc1\n"
"buffer_load_dwordx4 v[196:199], v1, s[4:7], s95 offen offset:3072 sc1\n"
"s_add_u32 s95, s54, 0x448000\n"
"buffer_load_dwordx4 v[96:99], v0, s[4:7], s95 offen sc1\n"
"buffer_load_dwordx4 v[100:103], v0, s[4:7], s95 offen offset:1024 sc1\n"
"buffer_load_dwordx4 v[112:115], v0, s[4:7], s95 offen offset:2048 sc1\n"
"buffer_load_dwordx4 v[120:123], v0, s[4:7], s95 offen offset:3072 sc1\n"
"buffer_load_dwordx4 v[104:107], v1, s[4:7], s95 offen sc1\n"
"buffer_load_dwordx4 v[152:155], v1, s[4:7], s95 offen offset:1024 sc1\n"
"buffer_load_dwordx4 v[168:171], v1, s[4:7], s95 offen offset:2048 sc1\n"
"buffer_load_dwordx4 v[144:147], v1, s[4:7], s95 offen offset:3072 sc1\n"
"s_add_u32 s95, s55, 0x448000\n"
"buffer_load_dwordx4 v[92:95], v0, s[4:7], s95 offen sc1\n"
"buffer_load_dwordx4 v[108:111], v0, s[4:7], s95 offen offset:1024 sc1\n"
"s_waitcnt vmcnt(0)\n"
"v_max_u32_e32 v247, v116, v117\n"
"v_max3_u32 v247, v247, v118, v119\n"
"v_max3_u32 v247, v247, v160, v161\n"
"v_max3_u32 v247, v247, v162, v163\n"
"v_max3_u32 v247, v247, v136, v137\n"
"v_max3_u32 v247, v247, v138, v139\n"
"v_max3_u32 v247, v247, v148, v149\n"
"v_max3_u32 v247, v247, v150, v151\n"
"v_max3_u32 v247, v247, v156, v157\n"
"v_max3_u32 v247, v247, v158, v159\n"
"v_max3_u32 v247, v247, v172, v173\n"
"v_max3_u32 v247, v247, v174, v175\n"
"v_max3_u32 v247, v247, v184, v185\n"
"v_max3_u32 v247, v247, v186, v187\n"
"v_max3_u32 v247, v247, v196, v197\n"
"v_max3_u32 v247, v247, v198, v199\n"
"v_cmp_eq_u32_e32 vcc, -1, v247\n"
"s_cbranch_vccz .Lmk_p54k\n"
"s_sub_u32 s1, s1, 1\n"
"s_cbranch_scc0 .Lmk_p54l\n"
".Lmk_p54k:\n"
"v_mfma_f32_16x16x32_bf16 v[88:91], a[0:3], v[116:119], 0\n"
"v_mfma_f32_16x16x32_bf16 v[88:91], a[4:7], v[160:163], v[88:91]\n"
"v_mfma_f32_16x16x32_bf16 v[88:91], a[8:11], v[136:139], v[88:91]\n"
"v_mfma_f32_16x16x32_bf16 v[88:91], a[12:15], v[148:151], v[88:91]\n"
"v_mfma_f32_16x16x32_bf16 v[88:91], a[16:19], v[156:159], v[88:91]\n"
"v_mfma_f32_16x16x32_bf16 v[88:91], a[20:23], v[172:175], v[88:91]\n"
"v_mfma_f32_16x16x32_bf16 v[88:91], a[24:27], v[184:187], v[88:91]\n"
"v_mfma_f32_16x16x32_bf16 v[88:91], a[28:31], v[196:199], v[88:91]\n"
"v_mfma_f32_16x16x32_bf16 v[124:127], a[60:63], v[196:199], 0\n"
"v_mfma_f32_16x16x32_bf16 v[124:127], a[56:59], v[184:187], v[124:127]\n"
"v_mfma_f32_16x16x32_bf16 v[124:127], a[52:55], v[172:175], v[124:127]\n"
"v_mfma_f32_16x16x32_bf16 v[124:127], a[48:51], v[156:159], v[124:127]\n"
"v_mfma_f32_16x16x32_bf16 v[124:127], a[44:47], v[148:151], v[124:127]\n"
"v_mfma_f32_16x16x32_bf16 v[124:127], a[40:43], v[136:139], v[124:127]\n"
"s_nop 7\n"
"s_branch .Lmk_p54r\n"
".Lmk_m57s:\n"
";\n"
"s_mov_b32 s1, 0x10000\n"
".Lmk_m57l:\n"
"s_add_u32 s98, s62, 0x44800\n"
"buffer_load_dword v255, v10, s[4:7], s98 offen sc1\n"
"buffer_load_dword v253, v10, s[4:7], s98 offen offset:256 sc1\n"
"buffer_load_dword v252, v10, s[4:7], s98 offen offset:512 sc1\n"
"buffer_load_dword v251, v10, s[4:7], s98 offen offset:768 sc1\n"
"buffer_load_dword v250, v10, s[4:7], s98 offen offset:1024 sc1\n"
"buffer_load_dword v254, v10, s[4:7], s98 offen offset:1280 sc1\n"
"buffer_load_dword v249, v10, s[4:7], s98 offen offset:1536 sc1\n"
"buffer_load_dword v248, v10, s[4:7], s98 offen offset:1792 sc1\n"
"s_waitcnt vmcnt(0)\n"
"v_max_u32_e32 v245, v255, v253\n"
"v_max3_u32 v245, v245, v252, v251\n"
"v_max3_u32 v245, v245, v250, v254\n"
"v_max3_u32 v245, v245, v249, v248\n"
"v_cmp_eq_u32_e32 vcc, -1, v245\n"
"s_cbranch_vccz .Lmk_m57k\n"
"s_sub_u32 s1, s1, 1\n"
"s_cbranch_scc0 .Lmk_m57l\n"
".Lmk_m57k:\n"
"v_add_f32_e32 v255, v255, v250\n"
"v_add_f32_e32 v253, v253, v254\n"
"v_add_f32_e32 v252, v252, v249\n"
"v_add_f32_e32 v251, v251, v248\n"
"v_add_f32_e32 v255, v255, v252\n"
"v_add_f32_e32 v253, v253, v251\n"
"v_add_f32_e32 v255, v255, v253\n"
"s_nop 1\n"
"s_branch .Lmk_m57r\n"
".Lmk_f44s:\n"
";\n"
"s_mov_b32 s1, 0x10000\n"
".Lmk_f44l:\n"
"buffer_load_dwordx2 v[246:247], v12, s[4:7], s96 offen sc1\n"
"s_waitcnt vmcnt(0)\n"
"v_cmp_ne_u64_e32 vcc, v[246:247], v[18:19]\n"
"s_cbranch_vccz .Lmk_f44r\n"
"s_sub_u32 s1, s1, 1\n"
"s_cbranch_scc0 .Lmk_f44l\n"
"s_branch .Lmk_f44r\n"
".Lmk_p55s:\n"
";\n"
"s_mov_b32 s1, 0x10000\n"
".Lmk_p55l:\n"
"s_add_u32 s96, s54, 0x448000\n"
"buffer_load_dwordx4 v[96:99], v0, s[4:7], s96 offen sc1\n"
"buffer_load_dwordx4 v[100:103], v0, s[4:7], s96 offen offset:1024 sc1\n"
"buffer_load_dwordx4 v[112:115], v0, s[4:7], s96 offen offset:2048 sc1\n"
"buffer_load_dwordx4 v[120:123], v0, s[4:7], s96 offen offset:3072 sc1\n"
"buffer_load_dwordx4 v[104:107], v1, s[4:7], s96 offen sc1\n"
"buffer_load_dwordx4 v[152:155], v1, s[4:7], s96 offen offset:1024 sc1\n"
"buffer_load_dwordx4 v[168:171], v1, s[4:7], s96 offen offset:2048 sc1\n"
"buffer_load_dwordx4 v[144:147], v1, s[4:7], s96 offen offset:3072 sc1\n"
"s_add_u32 s96, s55, 0x448000\n"
"buffer_load_dwordx4 v[92:95], v0, s[4:7], s96 offen sc1\n"
"buffer_load_dwordx4 v[108:111], v0, s[4:7], s96 offen offset:1024 sc1\n"
"buffer_load_dwordx4 v[128:131], v0, s[4:7], s96 offen offset:2048 sc1\n"
"buffer_load_dwordx4 v[132:135], v0, s[4:7], s96 offen offset:3072 sc1\n"
"buffer_load_dwordx4 v[124:127], v1, s[4:7], s96 offen sc1\n"
"buffer_load_dwordx4 v[176:179], v1, s[4:7], s96 offen offset:1024 sc1\n"
"buffer_load_dwordx4 v[180:183], v1, s[4:7], s96 offen offset:2048 sc1\n"
"buffer_load_dwordx4 v[188:191], v1, s[4:7], s96 offen offset:3072 sc1\n"
"s_waitcnt vmcnt(0)\n"
"v_max_u32_e32 v252, v96, v97\n"
"v_max3_u32 v252, v252, v98, v99\n"
"v_max3_u32 v252, v252, v100, v101\n"
"v_max3_u32 v252, v252, v102, v103\n"
"v_max3_u32 v252, v252, v112, v113\n"
"v_max3_u32 v252, v252, v114, v115\n"
"v_max3_u32 v252, v252, v120, v121\n"
"v_max3_u32 v252, v252, v122, v123\n"
"v_max3_u32 v252, v252, v104, v105\n"
"v_max3_u32 v252, v252, v106, v107\n"
"v_max3_u32 v252, v252, v152, v153\n"
"v_max3_u32 v252, v252, v154, v155\n"
"v_max3_u32 v252, v252, v168, v169\n"
"v_max3_u32 v252, v252, v170, v171\n"
"v_max3_u32 v252, v252, v144, v145\n"
"v_max3_u32 v252, v252, v146, v147\n"
"v_cmp_eq_u32_e32 vcc, -1, v252\n"
"s_cbranch_vccz .Lmk_p55k\n"
"s_sub_u32 s1, s1, 1\n"
"s_cbranch_scc0 .Lmk_p55l\n"
".Lmk_p55k:\n"
"v_mfma_f32_16x16x32_bf16 v[88:91], a[0:3], v[96:99], 0\n"
"v_mfma_f32_16x16x32_bf16 v[88:91], a[4:7], v[100:103], v[88:91]\n"
"v_mfma_f32_16x16x32_bf16 v[88:91], a[8:11], v[112:115], v[88:91]\n"
"v_mfma_f32_16x16x32_bf16 v[88:91], a[12:15], v[120:123], v[88:91]\n"
"v_mfma_f32_16x16x32_bf16 v[88:91], a[16:19], v[104:107], v[88:91]\n"
"v_mfma_f32_16x16x32_bf16 v[88:91], a[20:23], v[152:155], v[88:91]\n"
"v_mfma_f32_16x16x32_bf16 v[88:91], a[24:27], v[168:171], v[88:91]\n"
"v_mfma_f32_16x16x32_bf16 v[88:91], a[28:31], v[144:147], v[88:91]\n"
"v_mfma_f32_16x16x32_bf16 v[116:119], a[60:63], v[144:147], 0\n"
"v_mfma_f32_16x16x32_bf16 v[116:119], a[56:59], v[168:171], v[116:119]\n"
"v_mfma_f32_16x16x32_bf16 v[116:119], a[52:55], v[152:155], v[116:119]\n"
"v_mfma_f32_16x16x32_bf16 v[116:119], a[48:51], v[104:107], v[116:119]\n"
"v_mfma_f32_16x16x32_bf16 v[116:119], a[44:47], v[120:123], v[116:119]\n"
"v_mfma_f32_16x16x32_bf16 v[116:119], a[40:43], v[112:115], v[116:119]\n"
"s_nop 7\n"
"s_branch .Lmk_p55r\n"
".Lmk_p56s:\n"
";\n"
"s_mov_b32 s1, 0x10000\n"
".Lmk_p56l:\n"
"s_add_u32 s96, s55, 0x448000\n"
"buffer_load_dwordx4 v[92:95], v0, s[4:7], s96 offen sc1\n"
"buffer_load_dwordx4 v[108:111], v0, s[4:7], s96 offen offset:1024 sc1\n"
"buffer_load_dwordx4 v[128:131], v0, s[4:7], s96 offen offset:2048 sc1\n"
"buffer_load_dwordx4 v[132:135], v0, s[4:7], s96 offen offset:3072 sc1\n"
"buffer_load_dwordx4 v[124:127], v1, s[4:7], s96 offen sc1\n"
"buffer_load_dwordx4 v[176:179], v1, s[4:7], s96 offen offset:1024 sc1\n"
"buffer_load_dwordx4 v[180:183], v1, s[4:7], s96 offen offset:2048 sc1\n"
"buffer_load_dwordx4 v[188:191], v1, s[4:7], s96 offen offset:3072 sc1\n"
"s_waitcnt vmcnt(0)\n"
"v_max_u32_e32 v252, v92, v93\n"
"v_max3_u32 v252, v252, v94, v95\n"
"v_max3_u32 v252, v252, v108, v109\n"
"v_max3_u32 v252, v252, v110, v111\n"
"v_max3_u32 v252, v252, v128, v129\n"
"v_max3_u32 v252, v252, v130, v131\n"
"v_max3_u32 v252, v252, v132, v133\n"
"v_max3_u32 v252, v252, v134, v135\n"
"v_max3_u32 v252, v252, v124, v125\n"
"v_max3_u32 v252, v252, v126, v127\n"
"v_max3_u32 v252, v252, v176, v177\n"
"v_max3_u32 v252, v252, v178, v179\n"
"v_max3_u32 v252, v252, v180, v181\n"
"v_max3_u32 v252, v252, v182, v183\n"
"v_max3_u32 v252, v252, v188, v189\n"
"v_max3_u32 v252, v252, v190, v191\n"
"v_cmp_eq_u32_e32 vcc, -1, v252\n"
"s_cbranch_vccz .Lmk_p56k\n"
"s_sub_u32 s1, s1, 1\n"
"s_cbranch_scc0 .Lmk_p56l\n"
".Lmk_p56k:\n"
"v_mfma_f32_16x16x32_bf16 v[112:115], a[0:3], v[92:95], v[148:151]\n"
"v_mfma_f32_16x16x32_bf16 v[112:115], a[4:7], v[108:111], v[112:115]\n"
"v_mfma_f32_16x16x32_bf16 v[112:115], a[8:11], v[128:131], v[112:115]\n"
"v_mfma_f32_16x16x32_bf16 v[112:115], a[12:15], v[132:135], v[112:115]\n"
"v_mfma_f32_16x16x32_bf16 v[112:115], a[16:19], v[124:127], v[112:115]\n"
"v_mfma_f32_16x16x32_bf16 v[112:115], a[20:23], v[176:179], v[112:115]\n"
"v_mfma_f32_16x16x32_bf16 v[112:115], a[24:27], v[180:183], v[112:115]\n"
"v_mfma_f32_16x16x32_bf16 v[112:115], a[28:31], v[188:191], v[112:115]\n"
"v_mfma_f32_16x16x32_bf16 v[184:187], a[60:63], v[188:191], v[172:175]\n"
"v_mfma_f32_16x16x32_bf16 v[184:187], a[56:59], v[180:183], v[184:187]\n"
"v_mfma_f32_16x16x32_bf16 v[184:187], a[52:55], v[176:179], v[184:187]\n"
"v_mfma_f32_16x16x32_bf16 v[184:187], a[48:51], v[124:127], v[184:187]\n"
"v_mfma_f32_16x16x32_bf16 v[184:187], a[44:47], v[132:135], v[184:187]\n"
"v_mfma_f32_16x16x32_bf16 v[184:187], a[40:43], v[128:131], v[184:187]\n"
"s_nop 7\n"
"s_branch .Lmk_p56r\n"
".Lmk_f45s:\n"
";\n"
"s_mov_b32 s1, 0x10000\n"
".Lmk_f45l:\n"
"buffer_load_dwordx2 v[246:247], off, s[4:7], s96 sc1\n"
"s_waitcnt vmcnt(0)\n"
"v_cmp_ne_u64_e32 vcc, v[246:247], v[18:19]\n"
"s_cbranch_vccz .Lmk_f45r\n"
"s_sub_u32 s1, s1, 1\n"
"s_cbranch_scc0 .Lmk_f45l\n"
"s_branch .Lmk_f45r\n"
".Lmk_f46s:\n"
";\n"
"s_mov_b32 s1, 0x10000\n"
".Lmk_f46l:\n"
"buffer_load_dwordx2 v[250:251], v12, s[4:7], s98 offen sc1\n"
"s_waitcnt vmcnt(0)\n"
"v_cmp_ne_u64_e32 vcc, v[250:251], v[18:19]\n"
"s_cbranch_vccz .Lmk_f46r\n"
"s_sub_u32 s1, s1, 1\n"
"s_cbranch_scc0 .Lmk_f46l\n"
"s_branch .Lmk_f46r\n"
".Lmk_p60s:\n"
";\n"
"s_mov_b32 s1, 0x10000\n"
".Lmk_p60l:\n"
"s_add_u32 s96, s56, 0x440000\n"
"buffer_load_dwordx4 v[120:123], v0, s[4:7], s96 offen sc1\n"
"buffer_load_dwordx4 v[136:139], v0, s[4:7], s96 offen offset:1024 sc1\n"
"buffer_load_dwordx4 v[140:143], v0, s[4:7], s96 offen offset:2048 sc1\n"
"buffer_load_dwordx4 v[192:195], v0, s[4:7], s96 offen offset:3072 sc1\n"
"buffer_load_dwordx4 v[196:199], v1, s[4:7], s96 offen sc1\n"
"buffer_load_dwordx4 v[188:191], v1, s[4:7], s96 offen offset:1024 sc1\n"
"buffer_load_dwordx4 v[108:111], v1, s[4:7], s96 offen offset:2048 sc1\n"
"buffer_load_dwordx4 v[92:95], v1, s[4:7], s96 offen offset:3072 sc1\n"
"s_add_u32 s96, s57, 0x440000\n"
"buffer_load_dwordx4 v[144:147], v0, s[4:7], s96 offen sc1\n"
"buffer_load_dwordx4 v[180:183], v0, s[4:7], s96 offen offset:1024 sc1\n"
"buffer_load_dwordx4 v[24:27], v0, s[4:7], s96 offen offset:2048 sc1\n"
"buffer_load_dwordx4 v[32:35], v0, s[4:7], s96 offen offset:3072 sc1\n"
"buffer_load_dwordx4 v[44:47], v1, s[4:7], s96 offen sc1\n"
"buffer_load_dwordx4 v[48:51], v1, s[4:7], s96 offen offset:1024 sc1\n"
"buffer_load_dwordx4 v[28:31], v1, s[4:7], s96 offen offset:2048 sc1\n"
"buffer_load_dwordx4 v[56:59], v1, s[4:7], s96 offen offset:3072 sc1\n"
"s_add_u32 s96, s58, 0x440000\n"
"buffer_load_dwordx4 v[40:43], v0, s[4:7], s96 offen sc1\n"
"buffer_load_dwordx4 v[52:55], v0, s[4:7], s96 offen offset:1024 sc1\n"
"buffer_load_dwordx4 v[64:67], v0, s[4:7], s96 offen offset:2048 sc1\n"
"s_waitcnt vmcnt(0)\n"
"v_max_u32_e32 v255, v120, v121\n"
"v_max3_u32 v255, v255, v122, v123\n"
"v_max3_u32 v255, v255, v136, v137\n"
"v_max3_u32 v255, v255, v138, v139\n"
"v_max3_u32 v255, v255, v140, v141\n"
"v_max3_u32 v255, v255, v142, v143\n"
"v_max3_u32 v255, v255, v192, v193\n"
"v_max3_u32 v255, v255, v194, v195\n"
"v_max3_u32 v255, v255, v196, v197\n"
"v_max3_u32 v255, v255, v198, v199\n"
"v_max3_u32 v255, v255, v188, v189\n"
"v_max3_u32 v255, v255, v190, v191\n"
"v_max3_u32 v255, v255, v108, v109\n"
"v_max3_u32 v255, v255, v110, v111\n"
"v_max3_u32 v255, v255, v92, v93\n"
"v_max3_u32 v255, v255, v94, v95\n"
"v_cmp_eq_u32_e32 vcc, -1, v255\n"
"s_cbranch_vccz .Lmk_p60k\n"
"s_sub_u32 s1, s1, 1\n"
"s_cbranch_scc0 .Lmk_p60l\n"
".Lmk_p60k:\n"
"v_mfma_f32_16x16x32_bf16 v[36:39], a[128:131], v[120:123], 0\n"
"v_mfma_f32_16x16x32_bf16 v[36:39], a[132:135], v[136:139], v[36:39]\n"
"v_mfma_f32_16x16x32_bf16 v[36:39], a[136:139], v[140:143], v[36:39]\n"
"v_mfma_f32_16x16x32_bf16 v[36:39], a[140:143], v[192:195], v[36:39]\n"
"v_mfma_f32_16x16x32_bf16 v[36:39], a[144:147], v[196:199], v[36:39]\n"
"v_mfma_f32_16x16x32_bf16 v[36:39], a[148:151], v[188:191], v[36:39]\n"
"v_mfma_f32_16x16x32_bf16 v[36:39], a[152:155], v[108:111], v[36:39]\n"
"v_mfma_f32_16x16x32_bf16 v[36:39], a[156:159], v[92:95], v[36:39]\n"
"v_mfma_f32_16x16x32_bf16 v[60:63], a[188:191], v[92:95], 0\n"
"v_mfma_f32_16x16x32_bf16 v[60:63], a[184:187], v[108:111], v[60:63]\n"
"v_mfma_f32_16x16x32_bf16 v[60:63], a[180:183], v[188:191], v[60:63]\n"
"v_mfma_f32_16x16x32_bf16 v[60:63], a[176:179], v[196:199], v[60:63]\n"
"v_mfma_f32_16x16x32_bf16 v[60:63], a[172:175], v[192:195], v[60:63]\n"
"v_mfma_f32_16x16x32_bf16 v[60:63], a[168:171], v[140:143], v[60:63]\n"
"s_nop 7\n"
"s_branch .Lmk_p60r\n"
".Lmk_p61s:\n"
";\n"
"s_mov_b32 s1, 0x10000\n"
".Lmk_p61l:\n"
"s_add_u32 s96, s57, 0x440000\n"
"buffer_load_dwordx4 v[144:147], v0, s[4:7], s96 offen sc1\n"
"buffer_load_dwordx4 v[180:183], v0, s[4:7], s96 offen offset:1024 sc1\n"
"buffer_load_dwordx4 v[24:27], v0, s[4:7], s96 offen offset:2048 sc1\n"
"buffer_load_dwordx4 v[32:35], v0, s[4:7], s96 offen offset:3072 sc1\n"
"buffer_load_dwordx4 v[44:47], v1, s[4:7], s96 offen sc1\n"
"buffer_load_dwordx4 v[48:51], v1, s[4:7], s96 offen offset:1024 sc1\n"
"buffer_load_dwordx4 v[28:31], v1, s[4:7], s96 offen offset:2048 sc1\n"
"buffer_load_dwordx4 v[56:59], v1, s[4:7], s96 offen offset:3072 sc1\n"
"s_add_u32 s96, s58, 0x440000\n"
"buffer_load_dwordx4 v[40:43], v0, s[4:7], s96 offen sc1\n"
"buffer_load_dwordx4 v[52:55], v0, s[4:7], s96 offen offset:1024 sc1\n"
"buffer_load_dwordx4 v[64:67], v0, s[4:7], s96 offen offset:2048 sc1\n"
"buffer_load_dwordx4 v[36:39], v0, s[4:7], s96 offen offset:3072 sc1\n"
"buffer_load_dwordx4 v[72:75], v1, s[4:7], s96 offen sc1\n"
"buffer_load_dwordx4 v[60:63], v1, s[4:7], s96 offen offset:1024 sc1\n"
"buffer_load_dwordx4 v[80:83], v1, s[4:7], s96 offen offset:2048 sc1\n"
"buffer_load_dwordx4 v[68:71], v1, s[4:7], s96 offen offset:3072 sc1\n"
"s_add_u32 s96, s59, 0x440000\n"
"buffer_load_dwordx4 v[96:99], v0, s[4:7], s96 offen sc1\n"
"buffer_load_dwordx4 v[76:79], v0, s[4:7], s96 offen offset:1024 sc1\n"
"buffer_load_dwordx4 v[116:119], v0, s[4:7], s96 offen offset:2048 sc1\n"
"s_waitcnt vmcnt(0)\n"
"v_max_u32_e32 v255, v144, v145\n"
"v_max3_u32 v255, v255, v146, v147\n"
"v_max3_u32 v255, v255, v180, v181\n"
"v_max3_u32 v255, v255, v182, v183\n"
"v_max3_u32 v255, v255, v24, v25\n"
"v_max3_u32 v255, v255, v26, v27\n"
"v_max3_u32 v255, v255, v32, v33\n"
"v_max3_u32 v255, v255, v34, v35\n"
"v_max3_u32 v255, v255, v44, v45\n"
"v_max3_u32 v255, v255, v46, v47\n"
"v_max3_u32 v255, v255, v48, v49\n"
"v_max3_u32 v255, v255, v50, v51\n"
"v_max3_u32 v255, v255, v28, v29\n"
"v_max3_u32 v255, v255, v30, v31\n"
"v_max3_u32 v255, v255, v56, v57\n"
"v_max3_u32 v255, v255, v58, v59\n"
"v_cmp_eq_u32_e32 vcc, -1, v255\n"
"s_cbranch_vccz .Lmk_p61k\n"
"s_sub_u32 s1, s1, 1\n"
"s_cbranch_scc0 .Lmk_p61l\n"
".Lmk_p61k:\n"
"v_mfma_f32_16x16x32_bf16 v[84:87], a[128:131], v[144:147], 0\n"
"v_mfma_f32_16x16x32_bf16 v[84:87], a[132:135], v[180:183], v[84:87]\n"
"v_mfma_f32_16x16x32_bf16 v[84:87], a[136:139], v[24:27], v[84:87]\n"
"v_mfma_f32_16x16x32_bf16 v[84:87], a[140:143], v[32:35], v[84:87]\n"
"v_mfma_f32_16x16x32_bf16 v[84:87], a[144:147], v[44:47], v[84:87]\n"
"v_mfma_f32_16x16x32_bf16 v[84:87], a[148:151], v[48:51], v[84:87]\n"
"v_mfma_f32_16x16x32_bf16 v[84:87], a[152:155], v[28:31], v[84:87]\n"
"v_mfma_f32_16x16x32_bf16 v[84:87], a[156:159], v[56:59], v[84:87]\n"
"v_mfma_f32_16x16x32_bf16 v[100:103], a[188:191], v[56:59], 0\n"
"v_mfma_f32_16x16x32_bf16 v[100:103], a[184:187], v[28:31], v[100:103]\n"
"v_mfma_f32_16x16x32_bf16 v[100:103], a[180:183], v[48:51], v[100:103]\n"
"v_mfma_f32_16x16x32_bf16 v[100:103], a[176:179], v[44:47], v[100:103]\n"
"v_mfma_f32_16x16x32_bf16 v[100:103], a[172:175], v[32:35], v[100:103]\n"
"v_mfma_f32_16x16x32_bf16 v[100:103], a[168:171], v[24:27], v[100:103]\n"
"s_nop 7\n"
"s_branch .Lmk_p61r\n"
".Lmk_p62s:\n"
";\n"
"s_mov_b32 s1, 0x10000\n"
".Lmk_p62l:\n"
"s_add_u32 s96, s58, 0x440000\n"
"buffer_load_dwordx4 v[40:43], v0, s[4:7], s96 offen sc1\n"
"buffer_load_dwordx4 v[52:55], v0, s[4:7], s96 offen offset:1024 sc1\n"
"buffer_load_dwordx4 v[64:67], v0, s[4:7], s96 offen offset:2048 sc1\n"
"buffer_load_dwordx4 v[36:39], v0, s[4:7], s96 offen offset:3072 sc1\n"
"buffer_load_dwordx4 v[72:75], v1, s[4:7], s96 offen sc1\n"
"buffer_load_dwordx4 v[60:63], v1, s[4:7], s96 offen offset:1024 sc1\n"
"buffer_load_dwordx4 v[80:83], v1, s[4:7], s96 offen offset:2048 sc1\n"
"buffer_load_dwordx4 v[68:71], v1, s[4:7], s96 offen offset:3072 sc1\n"
"s_add_u32 s96, s59, 0x440000\n"
"buffer_load_dwordx4 v[96:99], v0, s[4:7], s96 offen sc1\n"
"buffer_load_dwordx4 v[76:79], v0, s[4:7], s96 offen offset:1024 sc1\n"
"buffer_load_dwordx4 v[116:119], v0, s[4:7], s96 offen offset:2048 sc1\n"
"buffer_load_dwordx4 v[84:87], v0, s[4:7], s96 offen offset:3072 sc1\n"
"buffer_load_dwordx4 v[128:131], v1, s[4:7], s96 offen sc1\n"
"buffer_load_dwordx4 v[100:103], v1, s[4:7], s96 offen offset:1024 sc1\n"
"buffer_load_dwordx4 v[156:159], v1, s[4:7], s96 offen offset:2048 sc1\n"
"buffer_load_dwordx4 v[124:127], v1, s[4:7], s96 offen offset:3072 sc1\n"
"s_waitcnt vmcnt(0)\n"
"v_max_u32_e32 v239, v40, v41\n"
"v_max3_u32 v239, v239, v42, v43\n"
"v_max3_u32 v239, v239, v52, v53\n"
"v_max3_u32 v239, v239, v54, v55\n"
"v_max3_u32 v239, v239, v64, v65\n"
"v_max3_u32 v239, v239, v66, v67\n"
"v_max3_u32 v239, v239, v36, v37\n"
"v_max3_u32 v239, v239, v38, v39\n"
"v_max3_u32 v239, v239, v72, v73\n"
"v_max3_u32 v239, v239, v74, v75\n"
"v_max3_u32 v239, v239, v60, v61\n"
"v_max3_u32 v239, v239, v62, v63\n"
"v_max3_u32 v239, v239, v80, v81\n"
"v_max3_u32 v239, v239, v82, v83\n"
"v_max3_u32 v239, v239, v68, v69\n"
"v_max3_u32 v239, v239, v70, v71\n"
"v_cmp_eq_u32_e32 vcc, -1, v239\n"
"s_cbranch_vccz .Lmk_p62k\n"
"s_sub_u32 s1, s1, 1\n"
"s_cbranch_scc0 .Lmk_p62l\n"
".Lmk_p62k:\n"
"v_mfma_f32_16x16x32_bf16 v[132:135], a[128:131], v[40:43], 0\n"
"v_mfma_f32_16x16x32_bf16 v[132:135], a[132:135], v[52:55], v[132:135]\n"
"v_mfma_f32_16x16x32_bf16 v[132:135], a[136:139], v[64:67], v[132:135]\n"
"v_mfma_f32_16x16x32_bf16 v[132:135], a[140:143], v[36:39], v[132:135]\n"
"v_mfma_f32_16x16x32_bf16 v[132:135], a[144:147], v[72:75], v[132:135]\n"
"v_mfma_f32_16x16x32_bf16 v[132:135], a[148:151], v[60:63], v[132:135]\n"
"v_mfma_f32_16x16x32_bf16 v[132:135], a[152:155], v[80:83], v[132:135]\n"
"v_mfma_f32_16x16x32_bf16 v[132:135], a[156:159], v[68:71], v[132:135]\n"
"v_mfma_f32_16x16x32_bf16 v[148:151], a[188:191], v[68:71], 0\n"
"v_mfma_f32_16x16x32_bf16 v[148:151], a[184:187], v[80:83], v[148:151]\n"
"v_mfma_f32_16x16x32_bf16 v[148:151], a[180:183], v[60:63], v[148:151]\n"
"v_mfma_f32_16x16x32_bf16 v[148:151], a[176:179], v[72:75], v[148:151]\n"
"v_mfma_f32_16x16x32_bf16 v[148:151], a[172:175], v[36:39], v[148:151]\n"
"v_mfma_f32_16x16x32_bf16 v[148:151], a[168:171], v[64:67], v[148:151]\n"
"s_nop 7\n"
"s_branch .Lmk_p62r\n"
".Lmk_p63s:\n"
";\n"
"s_mov_b32 s1, 0x10000\n"
".Lmk_p63l:\n"
"s_add_u32 s96, s59, 0x440000\n"
"buffer_load_dwordx4 v[96:99], v0, s[4:7], s96 offen sc1\n"
"buffer_load_dwordx4 v[76:79], v0, s[4:7], s96 offen offset:1024 sc1\n"
"buffer_load_dwordx4 v[116:119], v0, s[4:7], s96 offen offset:2048 sc1\n"
"buffer_load_dwordx4 v[84:87], v0, s[4:7], s96 offen offset:3072 sc1\n"
"buffer_load_dwordx4 v[128:131], v1, s[4:7], s96 offen sc1\n"
"buffer_load_dwordx4 v[100:103], v1, s[4:7], s96 offen offset:1024 sc1\n"
"buffer_load_dwordx4 v[156:159], v1, s[4:7], s96 offen offset:2048 sc1\n"
"buffer_load_dwordx4 v[124:127], v1, s[4:7], s96 offen offset:3072 sc1\n"
"s_add_u32 s96, s56, 0x400000\n"
"buffer_load_dwordx4 v[132:135], v0, s[4:7], s96 offen sc1\n"
"buffer_load_dwordx4 v[184:187], v0, s[4:7], s96 offen offset:1024 sc1\n"
"buffer_load_dwordx4 v[148:151], v0, s[4:7], s96 offen offset:2048 sc1\n"
"buffer_load_dwordx4 v[68:71], v0, s[4:7], s96 offen offset:3072 sc1\n"
"buffer_load_dwordx4 v[36:39], v1, s[4:7], s96 offen sc1\n"
"buffer_load_dwordx4 v[40:43], v1, s[4:7], s96 offen offset:1024 sc1\n"
"buffer_load_dwordx4 v[72:75], v1, s[4:7], s96 offen offset:2048 sc1\n"
"buffer_load_dwordx4 v[80:83], v1, s[4:7], s96 offen offset:3072 sc1\n"
"s_add_u32 s96, s57, 0x400000\n"
"buffer_load_dwordx4 v[164:167], v0, s[4:7], s96 offen sc1\n"
"s_waitcnt vmcnt(0)\n"
"v_max_u32_e32 v255, v96, v97\n"
"v_max3_u32 v255, v255, v98, v99\n"
"v_max3_u32 v255, v255, v76, v77\n"
"v_max3_u32 v255, v255, v78, v79\n"
"v_max3_u32 v255, v255, v116, v117\n"
"v_max3_u32 v255, v255, v118, v119\n"
"v_max3_u32 v255, v255, v84, v85\n"
"v_max3_u32 v255, v255, v86, v87\n"
"v_max3_u32 v255, v255, v128, v129\n"
"v_max3_u32 v255, v255, v130, v131\n"
"v_max3_u32 v255, v255, v100, v101\n"
"v_max3_u32 v255, v255, v102, v103\n"
"v_max3_u32 v255, v255, v156, v157\n"
"v_max3_u32 v255, v255, v158, v159\n"
"v_max3_u32 v255, v255, v124, v125\n"
"v_max3_u32 v255, v255, v126, v127\n"
"v_cmp_eq_u32_e32 vcc, -1, v255\n"
"s_cbranch_vccz .Lmk_p63k\n"
"s_sub_u32 s1, s1, 1\n"
"s_cbranch_scc0 .Lmk_p63l\n"
".Lmk_p63k:\n"
"v_mfma_f32_16x16x32_bf16 v[64:67], a[128:131], v[96:99], v[152:155]\n"
"v_mfma_f32_16x16x32_bf16 v[64:67], a[132:135], v[76:79], v[64:67]\n"
"v_mfma_f32_16x16x32_bf16 v[64:67], a[136:139], v[116:119], v[64:67]\n"
"v_mfma_f32_16x16x32_bf16 v[64:67], a[140:143], v[84:87], v[64:67]\n"
"v_mfma_f32_16x16x32_bf16 v[64:67], a[144:147], v[128:131], v[64:67]\n"
"v_mfma_f32_16x16x32_bf16 v[64:67], a[148:151], v[100:103], v[64:67]\n"
"v_mfma_f32_16x16x32_bf16 v[64:67], a[152:155], v[156:159], v[64:67]\n"
"v_mfma_f32_16x16x32_bf16 v[64:67], a[156:159], v[124:127], v[64:67]\n"
"v_mfma_f32_16x16x32_bf16 v[60:63], a[188:191], v[124:127], v[168:171]\n"
"v_mfma_f32_16x16x32_bf16 v[60:63], a[184:187], v[156:159], v[60:63]\n"
"v_mfma_f32_16x16x32_bf16 v[60:63], a[180:183], v[100:103], v[60:63]\n"
"v_mfma_f32_16x16x32_bf16 v[60:63], a[176:179], v[128:131], v[60:63]\n"
"v_mfma_f32_16x16x32_bf16 v[60:63], a[172:175], v[84:87], v[60:63]\n"
"v_mfma_f32_16x16x32_bf16 v[60:63], a[168:171], v[116:119], v[60:63]\n"
"s_nop 7\n"
"s_branch .Lmk_p63r\n"
".Lmk_p64s:\n"
";\n"
"s_mov_b32 s1, 0x10000\n"
".Lmk_p64l:\n"
"s_add_u32 s96, s56, 0x400000\n"
"buffer_load_dwordx4 v[132:135], v0, s[4:7], s96 offen sc1\n"
"buffer_load_dwordx4 v[184:187], v0, s[4:7], s96 offen offset:1024 sc1\n"
"buffer_load_dwordx4 v[148:151], v0, s[4:7], s96 offen offset:2048 sc1\n"
"buffer_load_dwordx4 v[68:71], v0, s[4:7], s96 offen offset:3072 sc1\n"
"buffer_load_dwordx4 v[36:39], v1, s[4:7], s96 offen sc1\n"
"buffer_load_dwordx4 v[40:43], v1, s[4:7], s96 offen offset:1024 sc1\n"
"buffer_load_dwordx4 v[72:75], v1, s[4:7], s96 offen offset:2048 sc1\n"
"buffer_load_dwordx4 v[80:83], v1, s[4:7], s96 offen offset:3072 sc1\n"
"s_add_u32 s96, s57, 0x400000\n"
"buffer_load_dwordx4 v[164:167], v0, s[4:7], s96 offen sc1\n"
"buffer_load_dwordx4 v[168:171], v0, s[4:7], s96 offen offset:1024 sc1\n"
"buffer_load_dwordx4 v[208:211], v0, s[4:7], s96 offen offset:2048 sc1\n"
"buffer_load_dwordx4 v[212:215], v0, s[4:7], s96 offen offset:3072 sc1\n"
"buffer_load_dwordx4 v[64:67], v1, s[4:7], s96 offen sc1\n"
"buffer_load_dwordx4 v[100:103], v1, s[4:7], s96 offen offset:1024 sc1\n"
"buffer_load_dwordx4 v[84:87], v1, s[4:7], s96 offen offset:2048 sc1\n"
"buffer_load_dwordx4 v[76:79], v1, s[4:7], s96 offen offset:3072 sc1\n"
"s_add_u32 s96, s58, 0x400000\n"
"buffer_load_dwordx4 v[116:119], v0, s[4:7], s96 offen sc1\n"
"buffer_load_dwordx4 v[124:127], v0, s[4:7], s96 offen offset:1024 sc1\n"
"buffer_load_dwordx4 v[128:131], v0, s[4:7], s96 offen offset:2048 sc1\n"
"buffer_load_dwordx4 v[176:179], v0, s[4:7], s96 offen offset:3072 sc1\n"
"s_waitcnt vmcnt(0)\n"
"v_max_u32_e32 v254, v132, v133\n"
"v_max3_u32 v254, v254, v134, v135\n"
"v_max3_u32 v254, v254, v184, v185\n"
"v_max3_u32 v254, v254, v186, v187\n"
"v_max3_u32 v254, v254, v148, v149\n"
"v_max3_u32 v254, v254, v150, v151\n"
"v_max3_u32 v254, v254, v68, v69\n"
"v_max3_u32 v254, v254, v70, v71\n"
"v_max3_u32 v254, v254, v36, v37\n"
"v_max3_u32 v254, v254, v38, v39\n"
"v_max3_u32 v254, v254, v40, v41\n"
"v_max3_u32 v254, v254, v42, v43\n"
"v_max3_u32 v254, v254, v72, v73\n"
"v_max3_u32 v254, v254, v74, v75\n"
"v_max3_u32 v254, v254, v80, v81\n"
"v_max3_u32 v254, v254, v82, v83\n"
"v_cmp_eq_u32_e32 vcc, -1, v254\n"
"s_cbranch_vccz .Lmk_p64k\n"
"s_sub_u32 s1, s1, 1\n"
"s_cbranch_scc0 .Lmk_p64l\n"
".Lmk_p64k:\n"
"v_mfma_f32_16x16x32_bf16 v[96:99], a[128:131], v[132:135], 0\n"
"v_mfma_f32_16x16x32_bf16 v[96:99], a[132:135], v[184:187], v[96:99]\n"
"v_mfma_f32_16x16x32_bf16 v[96:99], a[136:139], v[148:151], v[96:99]\n"
"v_mfma_f32_16x16x32_bf16 v[96:99], a[140:143], v[68:71], v[96:99]\n"
"v_mfma_f32_16x16x32_bf16 v[96:99], a[144:147], v[36:39], v[96:99]\n"
"v_mfma_f32_16x16x32_bf16 v[96:99], a[148:151], v[40:43], v[96:99]\n"
"v_mfma_f32_16x16x32_bf16 v[96:99], a[152:155], v[72:75], v[96:99]\n"
"v_mfma_f32_16x16x32_bf16 v[96:99], a[156:159], v[80:83], v[96:99]\n"
"v_mfma_f32_16x16x32_bf16 v[156:159], a[188:191], v[80:83], 0\n"
"v_mfma_f32_16x16x32_bf16 v[156:159], a[184:187], v[72:75], v[156:159]\n"
"v_mfma_f32_16x16x32_bf16 v[156:159], a[180:183], v[40:43], v[156:159]\n"
"v_mfma_f32_16x16x32_bf16 v[156:159], a[176:179], v[36:39], v[156:159]\n"
"v_mfma_f32_16x16x32_bf16 v[156:159], a[172:175], v[68:71], v[156:159]\n"
"v_mfma_f32_16x16x32_bf16 v[156:159], a[168:171], v[148:151], v[156:159]\n"
"s_nop 7\n"
"s_branch .Lmk_p64r\n"
".Lmk_p65s:\n"
";\n"
"s_mov_b32 s1, 0x10000\n"
".Lmk_p65l:\n"
"s_add_u32 s96, s57, 0x400000\n"
"buffer_load_dwordx4 v[164:167], v0, s[4:7], s96 offen sc1\n"
"buffer_load_dwordx4 v[168:171], v0, s[4:7], s96 offen offset:1024 sc1\n"
"buffer_load_dwordx4 v[208:211], v0, s[4:7], s96 offen offset:2048 sc1\n"
"buffer_load_dwordx4 v[212:215], v0, s[4:7], s96 offen offset:3072 sc1\n"
"buffer_load_dwordx4 v[64:67], v1, s[4:7], s96 offen sc1\n"
"buffer_load_dwordx4 v[100:103], v1, s[4:7], s96 offen offset:1024 sc1\n"
"buffer_load_dwordx4 v[84:87], v1, s[4:7], s96 offen offset:2048 sc1\n"
"buffer_load_dwordx4 v[76:79], v1, s[4:7], s96 offen offset:3072 sc1\n"
"s_add_u32 s96, s58, 0x400000\n"
"buffer_load_dwordx4 v[116:119], v0, s[4:7], s96 offen sc1\n"
"buffer_load_dwordx4 v[124:127], v0, s[4:7], s96 offen offset:1024 sc1\n"
"buffer_load_dwordx4 v[128:131], v0, s[4:7], s96 offen offset:2048 sc1\n"
"buffer_load_dwordx4 v[176:179], v0, s[4:7], s96 offen offset:3072 sc1\n"
"buffer_load_dwordx4 v[96:99], v1, s[4:7], s96 offen sc1\n"
"buffer_load_dwordx4 v[52:55], v1, s[4:7], s96 offen offset:1024 sc1\n"
"buffer_load_dwordx4 v[152:155], v1, s[4:7], s96 offen offset:2048 sc1\n"
"buffer_load_dwordx4 v[72:75], v1, s[4:7], s96 offen offset:3072 sc1\n"
"s_add_u32 s96, s59, 0x400000\n"
"buffer_load_dwordx4 v[36:39], v0, s[4:7], s96 offen sc1\n"
"buffer_load_dwordx4 v[40:43], v0, s[4:7], s96 offen offset:1024 sc1\n"
"buffer_load_dwordx4 v[68:71], v0, s[4:7], s96 offen offset:2048 sc1\n"
"buffer_load_dwordx4 v[148:151], v0, s[4:7], s96 offen offset:3072 sc1\n"
"buffer_load_dwordx4 v[160:163], v1, s[4:7], s96 offen sc1\n"
"s_waitcnt vmcnt(0)\n"
"v_max_u32_e32 v255, v164, v165\n"
"v_max3_u32 v255, v255, v166, v167\n"
"v_max3_u32 v255, v255, v168, v169\n"
"v_max3_u32 v255, v255, v170, v171\n"
"v_max3_u32 v255, v255, v208, v209\n"
"v_max3_u32 v255, v255, v210, v211\n"
"v_max3_u32 v255, v255, v212, v213\n"
"v_max3_u32 v255, v255, v214, v215\n"
"v_max3_u32 v255, v255, v64, v65\n"
"v_max3_u32 v255, v255, v66, v67\n"
"v_max3_u32 v255, v255, v100, v101\n"
"v_max3_u32 v255, v255, v102, v103\n"
"v_max3_u32 v255, v255, v84, v85\n"
"v_max3_u32 v255, v255, v86, v87\n"
"v_max3_u32 v255, v255, v76, v77\n"
"v_max3_u32 v255, v255, v78, v79\n"
"v_cmp_eq_u32_e32 vcc, -1, v255\n"
"s_cbranch_vccz .Lmk_p65k\n"
"s_sub_u32 s1, s1, 1\n"
"s_cbranch_scc0 .Lmk_p65l\n"
".Lmk_p65k:\n"
"v_mfma_f32_16x16x32_bf16 v[60:63], a[128:131], v[164:167], 0\n"
"v_mfma_f32_16x16x32_bf16 v[60:63], a[132:135], v[168:171], v[60:63]\n"
"v_mfma_f32_16x16x32_bf16 v[60:63], a[136:139], v[208:211], v[60:63]\n"
"v_mfma_f32_16x16x32_bf16 v[60:63], a[140:143], v[212:215], v[60:63]\n"
"v_mfma_f32_16x16x32_bf16 v[60:63], a[144:147], v[64:67], v[60:63]\n"
"v_mfma_f32_16x16x32_bf16 v[60:63], a[148:151], v[100:103], v[60:63]\n"
"v_mfma_f32_16x16x32_bf16 v[60:63], a[152:155], v[84:87], v[60:63]\n"
"v_mfma_f32_16x16x32_bf16 v[60:63], a[156:159], v[76:79], v[60:63]\n"
"v_mfma_f32_16x16x32_bf16 v[156:159], a[188:191], v[76:79], 0\n"
"v_mfma_f32_16x16x32_bf16 v[156:159], a[184:187], v[84:87], v[156:159]\n"
"v_mfma_f32_16x16x32_bf16 v[156:159], a[180:183], v[100:103], v[156:159]\n"
"v_mfma_f32_16x16x32_bf16 v[156:159], a[176:179], v[64:67], v[156:159]\n"
"v_mfma_f32_16x16x32_bf16 v[156:159], a[172:175], v[212:215], v[156:159]\n"
"v_mfma_f32_16x16x32_bf16 v[156:159], a[168:171], v[208:211], v[156:159]\n"
"s_nop 7\n"
"s_branch .Lmk_p65r\n"
".Lmk_p66s:\n"
";\n"
"s_mov_b32 s1, 0x10000\n"
".Lmk_p66l:\n"
"s_add_u32 s97, s58, 0x400000\n"
"buffer_load_dwordx4 v[116:119], v0, s[4:7], s97 offen sc1\n"
"buffer_load_dwordx4 v[124:127], v0, s[4:7], s97 offen offset:1024 sc1\n"
"buffer_load_dwordx4 v[128:131], v0, s[4:7], s97 offen offset:2048 sc1\n"
"buffer_load_dwordx4 v[176:179], v0, s[4:7], s97 offen offset:3072 sc1\n"
"buffer_load_dwordx4 v[96:99], v1, s[4:7], s97 offen sc1\n"
"buffer_load_dwordx4 v[52:55], v1, s[4:7], s97 offen offset:1024 sc1\n"
"buffer_load_dwordx4 v[152:155], v1, s[4:7], s97 offen offset:2048 sc1\n"
"buffer_load_dwordx4 v[72:75], v1, s[4:7], s97 offen offset:3072 sc1\n"
"s_add_u32 s97, s59, 0x400000\n"
"buffer_load_dwordx4 v[36:39], v0, s[4:7], s97 offen sc1\n"
"buffer_load_dwordx4 v[40:43], v0, s[4:7], s97 offen offset:1024 sc1\n"
"buffer_load_dwordx4 v[68:71], v0, s[4:7], s97 offen offset:2048 sc1\n"
"buffer_load_dwordx4 v[148:151], v0, s[4:7], s97 offen offset:3072 sc1\n"
"buffer_load_dwordx4 v[160:163], v1, s[4:7], s97 offen sc1\n"
"buffer_load_dwordx4 v[60:63], v1, s[4:7], s97 offen offset:1024 sc1\n"
"buffer_load_dwordx4 v[200:203], v1, s[4:7], s97 offen offset:2048 sc1\n"
"buffer_load_dwordx4 v[156:159], v1, s[4:7], s97 offen offset:3072 sc1\n"
"s_waitcnt vmcnt(0)\n"
"v_max_u32_e32 v253, v116, v117\n"
"v_max3_u32 v253, v253, v118, v119\n"
"v_max3_u32 v253, v253, v124, v125\n"
"v_max3_u32 v253, v253, v126, v127\n"
"v_max3_u32 v253, v253, v128, v129\n"
"v_max3_u32 v253, v253, v130, v131\n"
"v_max3_u32 v253, v253, v176, v177\n"
"v_max3_u32 v253, v253, v178, v179\n"
"v_max3_u32 v253, v253, v96, v97\n"
"v_max3_u32 v253, v253, v98, v99\n"
"v_max3_u32 v253, v253, v52, v53\n"
"v_max3_u32 v253, v253, v54, v55\n"
"v_max3_u32 v253, v253, v152, v153\n"
"v_max3_u32 v253, v253, v154, v155\n"
"v_max3_u32 v253, v253, v72, v73\n"
"v_max3_u32 v253, v253, v74, v75\n"
"v_cmp_eq_u32_e32 vcc, -1, v253\n"
"s_cbranch_vccz .Lmk_p66k\n"
"s_sub_u32 s1, s1, 1\n"
"s_cbranch_scc0 .Lmk_p66l\n"
".Lmk_p66k:\n"
"v_mfma_f32_16x16x32_bf16 v[64:67], a[128:131], v[116:119], 0\n"
"v_mfma_f32_16x16x32_bf16 v[64:67], a[132:135], v[124:127], v[64:67]\n"
"v_mfma_f32_16x16x32_bf16 v[64:67], a[136:139], v[128:131], v[64:67]\n"
"v_mfma_f32_16x16x32_bf16 v[64:67], a[140:143], v[176:179], v[64:67]\n"
"v_mfma_f32_16x16x32_bf16 v[64:67], a[144:147], v[96:99], v[64:67]\n"
"v_mfma_f32_16x16x32_bf16 v[64:67], a[148:151], v[52:55], v[64:67]\n"
"v_mfma_f32_16x16x32_bf16 v[64:67], a[152:155], v[152:155], v[64:67]\n"
"v_mfma_f32_16x16x32_bf16 v[64:67], a[156:159], v[72:75], v[64:67]\n"
"v_mfma_f32_16x16x32_bf16 v[76:79], a[188:191], v[72:75], 0\n"
"v_mfma_f32_16x16x32_bf16 v[76:79], a[184:187], v[152:155], v[76:79]\n"
"v_mfma_f32_16x16x32_bf16 v[76:79], a[180:183], v[52:55], v[76:79]\n"
"v_mfma_f32_16x16x32_bf16 v[76:79], a[176:179], v[96:99], v[76:79]\n"
"v_mfma_f32_16x16x32_bf16 v[76:79], a[172:175], v[176:179], v[76:79]\n"
"v_mfma_f32_16x16x32_bf16 v[76:79], a[168:171], v[128:131], v[76:79]\n"
"s_nop 7\n"
"s_branch .Lmk_p66r\n"
".Lmk_p67s:\n"
";\n"
"s_mov_b32 s1, 0x10000\n"
".Lmk_p67l:\n"
"s_add_u32 s97, s59, 0x400000\n"
"buffer_load_dwordx4 v[36:39], v0, s[4:7], s97 offen sc1\n"
"buffer_load_dwordx4 v[40:43], v0, s[4:7], s97 offen offset:1024 sc1\n"
"buffer_load_dwordx4 v[68:71], v0, s[4:7], s97 offen offset:2048 sc1\n"
"buffer_load_dwordx4 v[148:151], v0, s[4:7], s97 offen offset:3072 sc1\n"
"buffer_load_dwordx4 v[160:163], v1, s[4:7], s97 offen sc1\n"
"buffer_load_dwordx4 v[60:63], v1, s[4:7], s97 offen offset:1024 sc1\n"
"buffer_load_dwordx4 v[200:203], v1, s[4:7], s97 offen offset:2048 sc1\n"
"buffer_load_dwordx4 v[156:159], v1, s[4:7], s97 offen offset:3072 sc1\n"
"s_waitcnt vmcnt(0)\n"
"v_max_u32_e32 v255, v36, v37\n"
"v_max3_u32 v255, v255, v38, v39\n"
"v_max3_u32 v255, v255, v40, v41\n"
"v_max3_u32 v255, v255, v42, v43\n"
"v_max3_u32 v255, v255, v68, v69\n"
"v_max3_u32 v255, v255, v70, v71\n"
"v_max3_u32 v255, v255, v148, v149\n"
"v_max3_u32 v255, v255, v150, v151\n"
"v_max3_u32 v255, v255, v160, v161\n"
"v_max3_u32 v255, v255, v162, v163\n"
"v_max3_u32 v255, v255, v60, v61\n"
"v_max3_u32 v255, v255, v62, v63\n"
"v_max3_u32 v255, v255, v200, v201\n"
"v_max3_u32 v255, v255, v202, v203\n"
"v_max3_u32 v255, v255, v156, v157\n"
"v_max3_u32 v255, v255, v158, v159\n"
"v_cmp_eq_u32_e32 vcc, -1, v255\n"
"s_cbranch_vccz .Lmk_p67k\n"
"s_sub_u32 s1, s1, 1\n"
"s_cbranch_scc0 .Lmk_p67l\n"
".Lmk_p67k:\n"
"v_mfma_f32_16x16x32_bf16 v[64:67], a[128:131], v[36:39], v[80:83]\n"
"v_mfma_f32_16x16x32_bf16 v[64:67], a[132:135], v[40:43], v[64:67]\n"
"v_mfma_f32_16x16x32_bf16 v[64:67], a[136:139], v[68:71], v[64:67]\n"
"v_mfma_f32_16x16x32_bf16 v[64:67], a[140:143], v[148:151], v[64:67]\n"
"v_mfma_f32_16x16x32_bf16 v[64:67], a[144:147], v[160:163], v[64:67]\n"
"v_mfma_f32_16x16x32_bf16 v[64:67], a[148:151], v[60:63], v[64:67]\n"
"v_mfma_f32_16x16x32_bf16 v[64:67], a[152:155], v[200:203], v[64:67]\n"
"v_mfma_f32_16x16x32_bf16 v[64:67], a[156:159], v[156:159], v[64:67]\n"
"v_mfma_f32_16x16x32_bf16 v[72:75], a[188:191], v[156:159], v[100:103]\n"
"v_mfma_f32_16x16x32_bf16 v[72:75], a[184:187], v[200:203], v[72:75]\n"
"v_mfma_f32_16x16x32_bf16 v[72:75], a[180:183], v[60:63], v[72:75]\n"
"v_mfma_f32_16x16x32_bf16 v[72:75], a[176:179], v[160:163], v[72:75]\n"
"v_mfma_f32_16x16x32_bf16 v[72:75], a[172:175], v[148:151], v[72:75]\n"
"v_mfma_f32_16x16x32_bf16 v[72:75], a[168:171], v[68:71], v[72:75]\n"
"s_nop 7\n"
"s_branch .Lmk_p67r\n"
".Lmk_p68s:\n"
";\n"
"s_mov_b32 s1, 0x10000\n"
".Lmk_p68l:\n"
"s_add_u32 s96, s56, 0x440000\n"
"buffer_load_dwordx4 v[120:123], v0, s[4:7], s96 offen sc1\n"
"buffer_load_dwordx4 v[136:139], v0, s[4:7], s96 offen offset:1024 sc1\n"
"buffer_load_dwordx4 v[140:143], v0, s[4:7], s96 offen offset:2048 sc1\n"
"buffer_load_dwordx4 v[192:195], v0, s[4:7], s96 offen offset:3072 sc1\n"
"buffer_load_dwordx4 v[196:199], v1, s[4:7], s96 offen sc1\n"
"buffer_load_dwordx4 v[188:191], v1, s[4:7], s96 offen offset:1024 sc1\n"
"buffer_load_dwordx4 v[108:111], v1, s[4:7], s96 offen offset:2048 sc1\n"
"buffer_load_dwordx4 v[92:95], v1, s[4:7], s96 offen offset:3072 sc1\n"
"s_add_u32 s96, s57, 0x440000\n"
"buffer_load_dwordx4 v[144:147], v0, s[4:7], s96 offen sc1\n"
"buffer_load_dwordx4 v[180:183], v0, s[4:7], s96 offen offset:1024 sc1\n"
"buffer_load_dwordx4 v[24:27], v0, s[4:7], s96 offen offset:2048 sc1\n"
"buffer_load_dwordx4 v[32:35], v0, s[4:7], s96 offen offset:3072 sc1\n"
"buffer_load_dwordx4 v[44:47], v1, s[4:7], s96 offen sc1\n"
"buffer_load_dwordx4 v[48:51], v1, s[4:7], s96 offen offset:1024 sc1\n"
"buffer_load_dwordx4 v[28:31], v1, s[4:7], s96 offen offset:2048 sc1\n"
"buffer_load_dwordx4 v[56:59], v1, s[4:7], s96 offen offset:3072 sc1\n"
"s_add_u32 s96, s58, 0x440000\n"
"buffer_load_dwordx4 v[36:39], v0, s[4:7], s96 offen sc1\n"
"buffer_load_dwordx4 v[40:43], v0, s[4:7], s96 offen offset:1024 sc1\n"
"buffer_load_dwordx4 v[52:55], v0, s[4:7], s96 offen offset:2048 sc1\n"
"buffer_load_dwordx4 v[60:63], v0, s[4:7], s96 offen offset:3072 sc1\n"
"buffer_load_dwordx4 v[64:67], v1, s[4:7], s96 offen sc1\n"
"buffer_load_dwordx4 v[68:71], v1, s[4:7], s96 offen offset:1024 sc1\n"
"buffer_load_dwordx4 v[72:75], v1, s[4:7], s96 offen offset:2048 sc1\n"
"buffer_load_dwordx4 v[76:79], v1, s[4:7], s96 offen offset:3072 sc1\n"
"s_add_u32 s96, s59, 0x440000\n"
"buffer_load_dwordx4 v[84:87], v0, s[4:7], s96 offen sc1\n"
"buffer_load_dwordx4 v[88:91], v0, s[4:7], s96 offen offset:1024 sc1\n"
"buffer_load_dwordx4 v[100:103], v0, s[4:7], s96 offen offset:2048 sc1\n"
"buffer_load_dwordx4 v[104:107], v0, s[4:7], s96 offen offset:3072 sc1\n"
"s_waitcnt vmcnt(0)\n"
"v_max_u32_e32 v255, v120, v121\n"
"v_max3_u32 v255, v255, v122, v123\n"
"v_max3_u32 v255, v255, v136, v137\n"
"v_max3_u32 v255, v255, v138, v139\n"
"v_max3_u32 v255, v255, v140, v141\n"
"v_max3_u32 v255, v255, v142, v143\n"
"v_max3_u32 v255, v255, v192, v193\n"
"v_max3_u32 v255, v255, v194, v195\n"
"v_max3_u32 v255, v255, v196, v197\n"
"v_max3_u32 v255, v255, v198, v199\n"
"v_max3_u32 v255, v255, v188, v189\n"
"v_max3_u32 v255, v255, v190, v191\n"
"v_max3_u32 v255, v255, v108, v109\n"
"v_max3_u32 v255, v255, v110, v111\n"
"v_max3_u32 v255, v255, v92, v93\n"
"v_max3_u32 v255, v255, v94, v95\n"
"v_cmp_eq_u32_e32 vcc, -1, v255\n"
"s_cbranch_vccz .Lmk_p68k\n"
"s_sub_u32 s1, s1, 1\n"
"s_cbranch_scc0 .Lmk_p68l\n"
".Lmk_p68k:\n"
"v_mfma_f32_16x16x32_bf16 v[80:83], a[128:131], v[120:123], 0\n"
"v_mfma_f32_16x16x32_bf16 v[80:83], a[132:135], v[136:139], v[80:83]\n"
"v_mfma_f32_16x16x32_bf16 v[80:83], a[136:139], v[140:143], v[80:83]\n"
"v_mfma_f32_16x16x32_bf16 v[80:83], a[140:143], v[192:195], v[80:83]\n"
"v_mfma_f32_16x16x32_bf16 v[80:83], a[144:147], v[196:199], v[80:83]\n"
"v_mfma_f32_16x16x32_bf16 v[80:83], a[148:151], v[188:191], v[80:83]\n"
"v_mfma_f32_16x16x32_bf16 v[80:83], a[152:155], v[108:111], v[80:83]\n"
"v_mfma_f32_16x16x32_bf16 v[80:83], a[156:159], v[92:95], v[80:83]\n"
"v_mfma_f32_16x16x32_bf16 v[96:99], a[188:191], v[92:95], 0\n"
"v_mfma_f32_16x16x32_bf16 v[96:99], a[184:187], v[108:111], v[96:99]\n"
"v_mfma_f32_16x16x32_bf16 v[96:99], a[180:183], v[188:191], v[96:99]\n"
"v_mfma_f32_16x16x32_bf16 v[96:99], a[176:179], v[196:199], v[96:99]\n"
"v_mfma_f32_16x16x32_bf16 v[96:99], a[172:175], v[192:195], v[96:99]\n"
"v_mfma_f32_16x16x32_bf16 v[96:99], a[168:171], v[140:143], v[96:99]\n"
"s_nop 7\n"
"s_branch .Lmk_p68r\n"
".Lmk_p69s:\n"
";\n"
"s_mov_b32 s1, 0x10000\n"
".Lmk_p69l:\n"
"s_add_u32 s96, s57, 0x440000\n"
"buffer_load_dwordx4 v[144:147], v0, s[4:7], s96 offen sc1\n"
"buffer_load_dwordx4 v[180:183], v0, s[4:7], s96 offen offset:1024 sc1\n"
"buffer_load_dwordx4 v[24:27], v0, s[4:7], s96 offen offset:2048 sc1\n"
"buffer_load_dwordx4 v[32:35], v0, s[4:7], s96 offen offset:3072 sc1\n"
"buffer_load_dwordx4 v[44:47], v1, s[4:7], s96 offen sc1\n"
"buffer_load_dwordx4 v[48:51], v1, s[4:7], s96 offen offset:1024 sc1\n"
"buffer_load_dwordx4 v[28:31], v1, s[4:7], s96 offen offset:2048 sc1\n"
"buffer_load_dwordx4 v[56:59], v1, s[4:7], s96 offen offset:3072 sc1\n"
"s_add_u32 s96, s58, 0x440000\n"
"buffer_load_dwordx4 v[36:39], v0, s[4:7], s96 offen sc1\n"
"buffer_load_dwordx4 v[40:43], v0, s[4:7], s96 offen offset:1024 sc1\n"
"buffer_load_dwordx4 v[52:55], v0, s[4:7], s96 offen offset:2048 sc1\n"
"buffer_load_dwordx4 v[60:63], v0, s[4:7], s96 offen offset:3072 sc1\n"
"buffer_load_dwordx4 v[64:67], v1, s[4:7], s96 offen sc1\n"
"buffer_load_dwordx4 v[68:71], v1, s[4:7], s96 offen offset:1024 sc1\n"
"buffer_load_dwordx4 v[72:75], v1, s[4:7], s96 offen offset:2048 sc1\n"
"buffer_load_dwordx4 v[76:79], v1, s[4:7], s96 offen offset:3072 sc1\n"
"s_add_u32 s96, s59, 0x440000\n"
"buffer_load_dwordx4 v[84:87], v0, s[4:7], s96 offen sc1\n"
"buffer_load_dwordx4 v[88:91], v0, s[4:7], s96 offen offset:1024 sc1\n"
"buffer_load_dwordx4 v[100:103], v0, s[4:7], s96 offen offset:2048 sc1\n"
"buffer_load_dwordx4 v[104:107], v0, s[4:7], s96 offen offset:3072 sc1\n"
"buffer_load_dwordx4 v[80:83], v1, s[4:7], s96 offen sc1\n"
"buffer_load_dwordx4 v[116:119], v1, s[4:7], s96 offen offset:1024 sc1\n"
"buffer_load_dwordx4 v[96:99], v1, s[4:7], s96 offen offset:2048 sc1\n"
"buffer_load_dwordx4 v[92:95], v1, s[4:7], s96 offen offset:3072 sc1\n"
"s_add_u32 s96, s56, 0x400000\n"
"buffer_load_dwordx4 v[120:123], v0, s[4:7], s96 offen sc1\n"
"buffer_load_dwordx4 v[124:127], v0, s[4:7], s96 offen offset:1024 sc1\n"
"s_waitcnt vmcnt(0)\n"
"v_max_u32_e32 v255, v144, v145\n"
"v_max3_u32 v255, v255, v146, v147\n"
"v_max3_u32 v255, v255, v180, v181\n"
"v_max3_u32 v255, v255, v182, v183\n"
"v_max3_u32 v255, v255, v24, v25\n"
"v_max3_u32 v255, v255, v26, v27\n"
"v_max3_u32 v255, v255, v32, v33\n"
"v_max3_u32 v255, v255, v34, v35\n"
"v_max3_u32 v255, v255, v44, v45\n"
"v_max3_u32 v255, v255, v46, v47\n"
"v_max3_u32 v255, v255, v48, v49\n"
"v_max3_u32 v255, v255, v50, v51\n"
"v_max3_u32 v255, v255, v28, v29\n"
"v_max3_u32 v255, v255, v30, v31\n"
"v_max3_u32 v255, v255, v56, v57\n"
"v_max3_u32 v255, v255, v58, v59\n"
"v_cmp_eq_u32_e32 vcc, -1, v255\n"
"s_cbranch_vccz .Lmk_p69k\n"
"s_sub_u32 s1, s1, 1\n"
"s_cbranch_scc0 .Lmk_p69l\n"
".Lmk_p69k:\n"
"v_mfma_f32_16x16x32_bf16 v[108:111], a[128:131], v[144:147], 0\n"
"v_mfma_f32_16x16x32_bf16 v[108:111], a[132:135], v[180:183], v[108:111]\n"
"v_mfma_f32_16x16x32_bf16 v[108:111], a[136:139], v[24:27], v[108:111]\n"
"v_mfma_f32_16x16x32_bf16 v[108:111], a[140:143], v[32:35], v[108:111]\n"
"v_mfma_f32_16x16x32_bf16 v[108:111], a[144:147], v[44:47], v[108:111]\n"
"v_mfma_f32_16x16x32_bf16 v[108:111], a[148:151], v[48:51], v[108:111]\n"
"v_mfma_f32_16x16x32_bf16 v[108:111], a[152:155], v[28:31], v[108:111]\n"
"v_mfma_f32_16x16x32_bf16 v[108:111], a[156:159], v[56:59], v[108:111]\n"
"v_mfma_f32_16x16x32_bf16 v[112:115], a[188:191], v[56:59], 0\n"
"v_mfma_f32_16x16x32_bf16 v[112:115], a[184:187], v[28:31], v[112:115]\n"
"v_mfma_f32_16x16x32_bf16 v[112:115], a[180:183], v[48:51], v[112:115]\n"
"v_mfma_f32_16x16x32_bf16 v[112:115], a[176:179], v[44:47], v[112:115]\n"
"v_mfma_f32_16x16x32_bf16 v[112:115], a[172:175], v[32:35], v[112:115]\n"
"v_mfma_f32_16x16x32_bf16 v[112:115], a[168:171], v[24:27], v[112:115]\n"
"s_nop 7\n"
"s_branch .Lmk_p69r\n"
".Lmk_p70s:\n"
";\n"
"s_mov_b32 s1, 0x10000\n"
".Lmk_p70l:\n"
"s_add_u32 s96, s58, 0x440000\n"
"buffer_load_dwordx4 v[36:39], v0, s[4:7], s96 offen sc1\n"
"buffer_load_dwordx4 v[40:43], v0, s[4:7], s96 offen offset:1024 sc1\n"
"buffer_load_dwordx4 v[52:55], v0, s[4:7], s96 offen offset:2048 sc1\n"
"buffer_load_dwordx4 v[60:63], v0, s[4:7], s96 offen offset:3072 sc1\n"
"buffer_load_dwordx4 v[64:67], v1, s[4:7], s96 offen sc1\n"
"buffer_load_dwordx4 v[68:71], v1, s[4:7], s96 offen offset:1024 sc1\n"
"buffer_load_dwordx4 v[72:75], v1, s[4:7], s96 offen offset:2048 sc1\n"
"buffer_load_dwordx4 v[76:79], v1, s[4:7], s96 offen offset:3072 sc1\n"
"s_add_u32 s96, s59, 0x440000\n"
"buffer_load_dwordx4 v[84:87], v0, s[4:7], s96 offen sc1\n"
"buffer_load_dwordx4 v[88:91], v0, s[4:7], s96 offen offset:1024 sc1\n"
"buffer_load_dwordx4 v[100:103], v0, s[4:7], s96 offen offset:2048 sc1\n"
"buffer_load_dwordx4 v[104:107], v0, s[4:7], s96 offen offset:3072 sc1\n"
"buffer_load_dwordx4 v[80:83], v1, s[4:7], s96 offen sc1\n"
"buffer_load_dwordx4 v[116:119], v1, s[4:7], s96 offen offset:1024 sc1\n"
"buffer_load_dwordx4 v[96:99], v1, s[4:7], s96 offen offset:2048 sc1\n"
"buffer_load_dwordx4 v[92:95], v1, s[4:7], s96 offen offset:3072 sc1\n"
"s_add_u32 s96, s56, 0x400000\n"
"buffer_load_dwordx4 v[120:123], v0, s[4:7], s96 offen sc1\n"
"buffer_load_dwordx4 v[124:127], v0, s[4:7], s96 offen offset:1024 sc1\n"
"buffer_load_dwordx4 v[108:111], v0, s[4:7], s96 offen offset:2048 sc1\n"
"buffer_load_dwordx4 v[132:135], v0, s[4:7], s96 offen offset:3072 sc1\n"
"buffer_load_dwordx4 v[112:115], v1, s[4:7], s96 offen sc1\n"
"buffer_load_dwordx4 v[28:31], v1, s[4:7], s96 offen offset:1024 sc1\n"
"buffer_load_dwordx4 v[32:35], v1, s[4:7], s96 offen offset:2048 sc1\n"
"buffer_load_dwordx4 v[24:27], v1, s[4:7], s96 offen offset:3072 sc1\n"
"s_add_u32 s96, s57, 0x400000\n"
"buffer_load_dwordx4 v[48:51], v0, s[4:7], s96 offen sc1\n"
"buffer_load_dwordx4 v[56:59], v0, s[4:7], s96 offen offset:1024 sc1\n"
"buffer_load_dwordx4 v[136:139], v0, s[4:7], s96 offen offset:2048 sc1\n"
"s_waitcnt vmcnt(0)\n"
"v_max_u32_e32 v239, v36, v37\n"
"v_max3_u32 v239, v239, v38, v39\n"
"v_max3_u32 v239, v239, v40, v41\n"
"v_max3_u32 v239, v239, v42, v43\n"
"v_max3_u32 v239, v239, v52, v53\n"
"v_max3_u32 v239, v239, v54, v55\n"
"v_max3_u32 v239, v239, v60, v61\n"
"v_max3_u32 v239, v239, v62, v63\n"
"v_max3_u32 v239, v239, v64, v65\n"
"v_max3_u32 v239, v239, v66, v67\n"
"v_max3_u32 v239, v239, v68, v69\n"
"v_max3_u32 v239, v239, v70, v71\n"
"v_max3_u32 v239, v239, v72, v73\n"
"v_max3_u32 v239, v239, v74, v75\n"
"v_max3_u32 v239, v239, v76, v77\n"
"v_max3_u32 v239, v239, v78, v79\n"
"v_cmp_eq_u32_e32 vcc, -1, v239\n"
"s_cbranch_vccz .Lmk_p70k\n"
"s_sub_u32 s1, s1, 1\n"
"s_cbranch_scc0 .Lmk_p70l\n"
".Lmk_p70k:\n"
"v_mfma_f32_16x16x32_bf16 v[44:47], a[128:131], v[36:39], 0\n"
"v_mfma_f32_16x16x32_bf16 v[44:47], a[132:135], v[40:43], v[44:47]\n"
"v_mfma_f32_16x16x32_bf16 v[44:47], a[136:139], v[52:55], v[44:47]\n"
"v_mfma_f32_16x16x32_bf16 v[44:47], a[140:143], v[60:63], v[44:47]\n"
"v_mfma_f32_16x16x32_bf16 v[44:47], a[144:147], v[64:67], v[44:47]\n"
"v_mfma_f32_16x16x32_bf16 v[44:47], a[148:151], v[68:71], v[44:47]\n"
"v_mfma_f32_16x16x32_bf16 v[44:47], a[152:155], v[72:75], v[44:47]\n"
"v_mfma_f32_16x16x32_bf16 v[44:47], a[156:159], v[76:79], v[44:47]\n"
"v_mfma_f32_16x16x32_bf16 v[128:131], a[188:191], v[76:79], 0\n"
"v_mfma_f32_16x16x32_bf16 v[128:131], a[184:187], v[72:75], v[128:131]\n"
"v_mfma_f32_16x16x32_bf16 v[128:131], a[180:183], v[68:71], v[128:131]\n"
"v_mfma_f32_16x16x32_bf16 v[128:131], a[176:179], v[64:67], v[128:131]\n"
"v_mfma_f32_16x16x32_bf16 v[128:131], a[172:175], v[60:63], v[128:131]\n"
"v_mfma_f32_16x16x32_bf16 v[128:131], a[168:171], v[52:55], v[128:131]\n"
"s_nop 7\n"
"s_branch .Lmk_p70r\n"
".Lmk_p71s:\n"
";\n"
"s_mov_b32 s1, 0x10000\n"
".Lmk_p71l:\n"
"s_add_u32 s96, s59, 0x440000\n"
"buffer_load_dwordx4 v[84:87], v0, s[4:7], s96 offen sc1\n"
"buffer_load_dwordx4 v[88:91], v0, s[4:7], s96 offen offset:1024 sc1\n"
"buffer_load_dwordx4 v[100:103], v0, s[4:7], s96 offen offset:2048 sc1\n"
"buffer_load_dwordx4 v[104:107], v0, s[4:7], s96 offen offset:3072 sc1\n"
"buffer_load_dwordx4 v[80:83], v1, s[4:7], s96 offen sc1\n"
"buffer_load_dwordx4 v[116:119], v1, s[4:7], s96 offen offset:1024 sc1\n"
"buffer_load_dwordx4 v[96:99], v1, s[4:7], s96 offen offset:2048 sc1\n"
"buffer_load_dwordx4 v[92:95], v1, s[4:7], s96 offen offset:3072 sc1\n"
"s_add_u32 s96, s56, 0x400000\n"
"buffer_load_dwordx4 v[120:123], v0, s[4:7], s96 offen sc1\n"
"buffer_load_dwordx4 v[124:127], v0, s[4:7], s96 offen offset:1024 sc1\n"
"buffer_load_dwordx4 v[108:111], v0, s[4:7], s96 offen offset:2048 sc1\n"
"buffer_load_dwordx4 v[132:135], v0, s[4:7], s96 offen offset:3072 sc1\n"
"buffer_load_dwordx4 v[112:115], v1, s[4:7], s96 offen sc1\n"
"buffer_load_dwordx4 v[28:31], v1, s[4:7], s96 offen offset:1024 sc1\n"
"buffer_load_dwordx4 v[32:35], v1, s[4:7], s96 offen offset:2048 sc1\n"
"buffer_load_dwordx4 v[24:27], v1, s[4:7], s96 offen offset:3072 sc1\n"
"s_add_u32 s96, s57, 0x400000\n"
"buffer_load_dwordx4 v[48:51], v0, s[4:7], s96 offen sc1\n"
"buffer_load_dwordx4 v[56:59], v0, s[4:7], s96 offen offset:1024 sc1\n"
"buffer_load_dwordx4 v[136:139], v0, s[4:7], s96 offen offset:2048 sc1\n"
"buffer_load_dwordx4 v[44:47], v0, s[4:7], s96 offen offset:3072 sc1\n"
"buffer_load_dwordx4 v[160:163], v1, s[4:7], s96 offen sc1\n"
"buffer_load_dwordx4 v[128:131], v1, s[4:7], s96 offen offset:1024 sc1\n"
"buffer_load_dwordx4 v[72:75], v1, s[4:7], s96 offen offset:2048 sc1\n"
"buffer_load_dwordx4 v[60:63], v1, s[4:7], s96 offen offset:3072 sc1\n"
"s_add_u32 s96, s58, 0x400000\n"
"buffer_load_dwordx4 v[36:39], v0, s[4:7], s96 offen sc1\n"
"buffer_load_dwordx4 v[68:71], v0, s[4:7], s96 offen offset:1024 sc1\n"
"buffer_load_dwordx4 v[76:79], v0, s[4:7], s96 offen offset:2048 sc1\n"
"buffer_load_dwordx4 v[148:151], v0, s[4:7], s96 offen offset:3072 sc1\n"
"s_waitcnt vmcnt(0)\n"
"v_max_u32_e32 v255, v84, v85\n"
"v_max3_u32 v255, v255, v86, v87\n"
"v_max3_u32 v255, v255, v88, v89\n"
"v_max3_u32 v255, v255, v90, v91\n"
"v_max3_u32 v255, v255, v100, v101\n"
"v_max3_u32 v255, v255, v102, v103\n"
"v_max3_u32 v255, v255, v104, v105\n"
"v_max3_u32 v255, v255, v106, v107\n"
"v_max3_u32 v255, v255, v80, v81\n"
"v_max3_u32 v255, v255, v82, v83\n"
"v_max3_u32 v255, v255, v116, v117\n"
"v_max3_u32 v255, v255, v118, v119\n"
"v_max3_u32 v255, v255, v96, v97\n"
"v_max3_u32 v255, v255, v98, v99\n"
"v_max3_u32 v255, v255, v92, v93\n"
"v_max3_u32 v255, v255, v94, v95\n"
"v_cmp_eq_u32_e32 vcc, -1, v255\n"
"s_cbranch_vccz .Lmk_p71k\n"
"s_sub_u32 s1, s1, 1\n"
"s_cbranch_scc0 .Lmk_p71l\n"
".Lmk_p71k:\n"
"v_mfma_f32_16x16x32_bf16 v[64:67], a[128:131], v[84:87], v[140:143]\n"
"v_mfma_f32_16x16x32_bf16 v[64:67], a[132:135], v[88:91], v[64:67]\n"
"v_mfma_f32_16x16x32_bf16 v[64:67], a[136:139], v[100:103], v[64:67]\n"
"v_mfma_f32_16x16x32_bf16 v[64:67], a[140:143], v[104:107], v[64:67]\n"
"v_mfma_f32_16x16x32_bf16 v[64:67], a[144:147], v[80:83], v[64:67]\n"
"v_mfma_f32_16x16x32_bf16 v[64:67], a[148:151], v[116:119], v[64:67]\n"
"v_mfma_f32_16x16x32_bf16 v[64:67], a[152:155], v[96:99], v[64:67]\n"
"v_mfma_f32_16x16x32_bf16 v[64:67], a[156:159], v[92:95], v[64:67]\n"
"v_mfma_f32_16x16x32_bf16 v[52:55], a[188:191], v[92:95], v[152:155]\n"
"v_mfma_f32_16x16x32_bf16 v[52:55], a[184:187], v[96:99], v[52:55]\n"
"v_mfma_f32_16x16x32_bf16 v[52:55], a[180:183], v[116:119], v[52:55]\n"
"v_mfma_f32_16x16x32_bf16 v[52:55], a[176:179], v[80:83], v[52:55]\n"
"v_mfma_f32_16x16x32_bf16 v[52:55], a[172:175], v[104:107], v[52:55]\n"
"v_mfma_f32_16x16x32_bf16 v[52:55], a[168:171], v[100:103], v[52:55]\n"
"s_nop 7\n"
"s_branch .Lmk_p71r\n"
".Lmk_p73s:\n"
";\n"
"s_mov_b32 s1, 0x10000\n"
".Lmk_p73l:\n"
"s_add_u32 s97, s56, 0x400000\n"
"buffer_load_dwordx4 v[120:123], v0, s[4:7], s97 offen sc1\n"
"buffer_load_dwordx4 v[124:127], v0, s[4:7], s97 offen offset:1024 sc1\n"
"buffer_load_dwordx4 v[108:111], v0, s[4:7], s97 offen offset:2048 sc1\n"
"buffer_load_dwordx4 v[132:135], v0, s[4:7], s97 offen offset:3072 sc1\n"
"buffer_load_dwordx4 v[112:115], v1, s[4:7], s97 offen sc1\n"
"buffer_load_dwordx4 v[28:31], v1, s[4:7], s97 offen offset:1024 sc1\n"
"buffer_load_dwordx4 v[32:35], v1, s[4:7], s97 offen offset:2048 sc1\n"
"buffer_load_dwordx4 v[24:27], v1, s[4:7], s97 offen offset:3072 sc1\n"
"s_add_u32 s97, s57, 0x400000\n"
"buffer_load_dwordx4 v[48:51], v0, s[4:7], s97 offen sc1\n"
"buffer_load_dwordx4 v[56:59], v0, s[4:7], s97 offen offset:1024 sc1\n"
"buffer_load_dwordx4 v[136:139], v0, s[4:7], s97 offen offset:2048 sc1\n"
"buffer_load_dwordx4 v[44:47], v0, s[4:7], s97 offen offset:3072 sc1\n"
"buffer_load_dwordx4 v[160:163], v1, s[4:7], s97 offen sc1\n"
"buffer_load_dwordx4 v[128:131], v1, s[4:7], s97 offen offset:1024 sc1\n"
"buffer_load_dwordx4 v[72:75], v1, s[4:7], s97 offen offset:2048 sc1\n"
"buffer_load_dwordx4 v[60:63], v1, s[4:7], s97 offen offset:3072 sc1\n"
"s_add_u32 s97, s58, 0x400000\n"
"buffer_load_dwordx4 v[36:39], v0, s[4:7], s97 offen sc1\n"
"buffer_load_dwordx4 v[68:71], v0, s[4:7], s97 offen offset:1024 sc1\n"
"buffer_load_dwordx4 v[76:79], v0, s[4:7], s97 offen offset:2048 sc1\n"
"buffer_load_dwordx4 v[148:151], v0, s[4:7], s97 offen offset:3072 sc1\n"
"buffer_load_dwordx4 v[152:155], v1, s[4:7], s97 offen sc1\n"
"buffer_load_dwordx4 v[172:175], v1, s[4:7], s97 offen offset:1024 sc1\n"
"buffer_load_dwordx4 v[176:179], v1, s[4:7], s97 offen offset:2048 sc1\n"
"buffer_load_dwordx4 v[64:67], v1, s[4:7], s97 offen offset:3072 sc1\n"
"s_add_u32 s97, s59, 0x400000\n"
"buffer_load_dwordx4 v[80:83], v0, s[4:7], s97 offen sc1\n"
"buffer_load_dwordx4 v[88:91], v0, s[4:7], s97 offen offset:1024 sc1\n"
"buffer_load_dwordx4 v[40:43], v0, s[4:7], s97 offen offset:2048 sc1\n"
"buffer_load_dwordx4 v[96:99], v0, s[4:7], s97 offen offset:3072 sc1\n"
"buffer_load_dwordx4 v[100:103], v1, s[4:7], s97 offen sc1\n"
"buffer_load_dwordx4 v[52:55], v1, s[4:7], s97 offen offset:1024 sc1\n"
"buffer_load_dwordx4 v[84:87], v1, s[4:7], s97 offen offset:2048 sc1\n"
"buffer_load_dwordx4 v[92:95], v1, s[4:7], s97 offen offset:3072 sc1\n"
"s_waitcnt vmcnt(0)\n"
"v_max_u32_e32 v255, v120, v121\n"
"v_max3_u32 v255, v255, v122, v123\n"
"v_max3_u32 v255, v255, v124, v125\n"
"v_max3_u32 v255, v255, v126, v127\n"
"v_max3_u32 v255, v255, v108, v109\n"
"v_max3_u32 v255, v255, v110, v111\n"
"v_max3_u32 v255, v255, v132, v133\n"
"v_max3_u32 v255, v255, v134, v135\n"
"v_max3_u32 v255, v255, v112, v113\n"
"v_max3_u32 v255, v255, v114, v115\n"
"v_max3_u32 v255, v255, v28, v29\n"
"v_max3_u32 v255, v255, v30, v31\n"
"v_max3_u32 v255, v255, v32, v33\n"
"v_max3_u32 v255, v255, v34, v35\n"
"v_max3_u32 v255, v255, v24, v25\n"
"v_max3_u32 v255, v255, v26, v27\n"
"v_cmp_eq_u32_e32 vcc, -1, v255\n"
"s_cbranch_vccz .Lmk_p73k\n"
"s_sub_u32 s1, s1, 1\n"
"s_cbranch_scc0 .Lmk_p73l\n"
".Lmk_p73k:\n"
"v_mfma_f32_16x16x32_bf16 v[104:107], a[128:131], v[120:123], 0\n"
"v_mfma_f32_16x16x32_bf16 v[104:107], a[132:135], v[124:127], v[104:107]\n"
"v_mfma_f32_16x16x32_bf16 v[104:107], a[136:139], v[108:111], v[104:107]\n"
"v_mfma_f32_16x16x32_bf16 v[104:107], a[140:143], v[132:135], v[104:107]\n"
"v_mfma_f32_16x16x32_bf16 v[104:107], a[144:147], v[112:115], v[104:107]\n"
"v_mfma_f32_16x16x32_bf16 v[104:107], a[148:151], v[28:31], v[104:107]\n"
"v_mfma_f32_16x16x32_bf16 v[104:107], a[152:155], v[32:35], v[104:107]\n"
"v_mfma_f32_16x16x32_bf16 v[104:107], a[156:159], v[24:27], v[104:107]\n"
"v_mfma_f32_16x16x32_bf16 v[116:119], a[188:191], v[24:27], 0\n"
"v_mfma_f32_16x16x32_bf16 v[116:119], a[184:187], v[32:35], v[116:119]\n"
"v_mfma_f32_16x16x32_bf16 v[116:119], a[180:183], v[28:31], v[116:119]\n"
"v_mfma_f32_16x16x32_bf16 v[116:119], a[176:179], v[112:115], v[116:119]\n"
"v_mfma_f32_16x16x32_bf16 v[116:119], a[172:175], v[132:135], v[116:119]\n"
"v_mfma_f32_16x16x32_bf16 v[116:119], a[168:171], v[108:111], v[116:119]\n"
"s_nop 7\n"
"s_branch .Lmk_p73r\n"
".Lmk_p74s:\n"
";\n"
"s_mov_b32 s1, 0x10000\n"
".Lmk_p74l:\n"
"s_add_u32 s97, s57, 0x400000\n"
"buffer_load_dwordx4 v[48:51], v0, s[4:7], s97 offen sc1\n"
"buffer_load_dwordx4 v[56:59], v0, s[4:7], s97 offen offset:1024 sc1\n"
"buffer_load_dwordx4 v[136:139], v0, s[4:7], s97 offen offset:2048 sc1\n"
"buffer_load_dwordx4 v[44:47], v0, s[4:7], s97 offen offset:3072 sc1\n"
"buffer_load_dwordx4 v[160:163], v1, s[4:7], s97 offen sc1\n"
"buffer_load_dwordx4 v[128:131], v1, s[4:7], s97 offen offset:1024 sc1\n"
"buffer_load_dwordx4 v[72:75], v1, s[4:7], s97 offen offset:2048 sc1\n"
"buffer_load_dwordx4 v[60:63], v1, s[4:7], s97 offen offset:3072 sc1\n"
"s_add_u32 s97, s58, 0x400000\n"
"buffer_load_dwordx4 v[36:39], v0, s[4:7], s97 offen sc1\n"
"buffer_load_dwordx4 v[68:71], v0, s[4:7], s97 offen offset:1024 sc1\n"
"buffer_load_dwordx4 v[76:79], v0, s[4:7], s97 offen offset:2048 sc1\n"
"buffer_load_dwordx4 v[148:151], v0, s[4:7], s97 offen offset:3072 sc1\n"
"buffer_load_dwordx4 v[152:155], v1, s[4:7], s97 offen sc1\n"
"buffer_load_dwordx4 v[172:175], v1, s[4:7], s97 offen offset:1024 sc1\n"
"buffer_load_dwordx4 v[176:179], v1, s[4:7], s97 offen offset:2048 sc1\n"
"buffer_load_dwordx4 v[64:67], v1, s[4:7], s97 offen offset:3072 sc1\n"
"s_add_u32 s97, s59, 0x400000\n"
"buffer_load_dwordx4 v[80:83], v0, s[4:7], s97 offen sc1\n"
"buffer_load_dwordx4 v[88:91], v0, s[4:7], s97 offen offset:1024 sc1\n"
"buffer_load_dwordx4 v[40:43], v0, s[4:7], s97 offen offset:2048 sc1\n"
"buffer_load_dwordx4 v[96:99], v0, s[4:7], s97 offen offset:3072 sc1\n"
"buffer_load_dwordx4 v[100:103], v1, s[4:7], s97 offen sc1\n"
"buffer_load_dwordx4 v[52:55], v1, s[4:7], s97 offen offset:1024 sc1\n"
"buffer_load_dwordx4 v[84:87], v1, s[4:7], s97 offen offset:2048 sc1\n"
"buffer_load_dwordx4 v[92:95], v1, s[4:7], s97 offen offset:3072 sc1\n"
"s_waitcnt vmcnt(0)\n"
"v_max_u32_e32 v255, v48, v49\n"
"v_max3_u32 v255, v255, v50, v51\n"
"v_max3_u32 v255, v255, v56, v57\n"
"v_max3_u32 v255, v255, v58, v59\n"
"v_max3_u32 v255, v255, v136, v137\n"
"v_max3_u32 v255, v255, v138, v139\n"
"v_max3_u32 v255, v255, v44, v45\n"
"v_max3_u32 v255, v255, v46, v47\n"
"v_max3_u32 v255, v255, v160, v161\n"
"v_max3_u32 v255, v255, v162, v163\n"
"v_max3_u32 v255, v255, v128, v129\n"
"v_max3_u32 v255, v255, v130, v131\n"
"v_max3_u32 v255, v255, v72, v73\n"
"v_max3_u32 v255, v255, v74, v75\n"
"v_max3_u32 v255, v255, v60, v61\n"
"v_max3_u32 v255, v255, v62, v63\n"
"v_cmp_eq_u32_e32 vcc, -1, v255\n"
"s_cbranch_vccz .Lmk_p74k\n"
"s_sub_u32 s1, s1, 1\n"
"s_cbranch_scc0 .Lmk_p74l\n"
".Lmk_p74k:\n"
"v_mfma_f32_16x16x32_bf16 v[24:27], a[128:131], v[48:51], 0\n"
"v_mfma_f32_16x16x32_bf16 v[24:27], a[132:135], v[56:59], v[24:27]\n"
"v_mfma_f32_16x16x32_bf16 v[24:27], a[136:139], v[136:139], v[24:27]\n"
"v_mfma_f32_16x16x32_bf16 v[24:27], a[140:143], v[44:47], v[24:27]\n"
"v_mfma_f32_16x16x32_bf16 v[24:27], a[144:147], v[160:163], v[24:27]\n"
"v_mfma_f32_16x16x32_bf16 v[24:27], a[148:151], v[128:131], v[24:27]\n"
"v_mfma_f32_16x16x32_bf16 v[24:27], a[152:155], v[72:75], v[24:27]\n"
"v_mfma_f32_16x16x32_bf16 v[24:27], a[156:159], v[60:63], v[24:27]\n"
"v_mfma_f32_16x16x32_bf16 v[28:31], a[188:191], v[60:63], 0\n"
"v_mfma_f32_16x16x32_bf16 v[28:31], a[184:187], v[72:75], v[28:31]\n"
"v_mfma_f32_16x16x32_bf16 v[28:31], a[180:183], v[128:131], v[28:31]\n"
"v_mfma_f32_16x16x32_bf16 v[28:31], a[176:179], v[160:163], v[28:31]\n"
"v_mfma_f32_16x16x32_bf16 v[28:31], a[172:175], v[44:47], v[28:31]\n"
"v_mfma_f32_16x16x32_bf16 v[28:31], a[168:171], v[136:139], v[28:31]\n"
"s_nop 7\n"
"s_branch .Lmk_p74r\n"
".Lmk_p75s:\n"
";\n"
"s_mov_b32 s1, 0x10000\n"
".Lmk_p75l:\n"
"s_add_u32 s97, s58, 0x400000\n"
"buffer_load_dwordx4 v[36:39], v0, s[4:7], s97 offen sc1\n"
"buffer_load_dwordx4 v[68:71], v0, s[4:7], s97 offen offset:1024 sc1\n"
"buffer_load_dwordx4 v[76:79], v0, s[4:7], s97 offen offset:2048 sc1\n"
"buffer_load_dwordx4 v[148:151], v0, s[4:7], s97 offen offset:3072 sc1\n"
"buffer_load_dwordx4 v[152:155], v1, s[4:7], s97 offen sc1\n"
"buffer_load_dwordx4 v[172:175], v1, s[4:7], s97 offen offset:1024 sc1\n"
"buffer_load_dwordx4 v[176:179], v1, s[4:7], s97 offen offset:2048 sc1\n"
"buffer_load_dwordx4 v[64:67], v1, s[4:7], s97 offen offset:3072 sc1\n"
"s_add_u32 s97, s59, 0x400000\n"
"buffer_load_dwordx4 v[80:83], v0, s[4:7], s97 offen sc1\n"
"buffer_load_dwordx4 v[88:91], v0, s[4:7], s97 offen offset:1024 sc1\n"
"buffer_load_dwordx4 v[40:43], v0, s[4:7], s97 offen offset:2048 sc1\n"
"buffer_load_dwordx4 v[96:99], v0, s[4:7], s97 offen offset:3072 sc1\n"
"buffer_load_dwordx4 v[100:103], v1, s[4:7], s97 offen sc1\n"
"buffer_load_dwordx4 v[52:55], v1, s[4:7], s97 offen offset:1024 sc1\n"
"buffer_load_dwordx4 v[84:87], v1, s[4:7], s97 offen offset:2048 sc1\n"
"buffer_load_dwordx4 v[92:95], v1, s[4:7], s97 offen offset:3072 sc1\n"
"s_waitcnt vmcnt(0)\n"
"v_max_u32_e32 v239, v36, v37\n"
"v_max3_u32 v239, v239, v38, v39\n"
"v_max3_u32 v239, v239, v68, v69\n"
"v_max3_u32 v239, v239, v70, v71\n"
"v_max3_u32 v239, v239, v76, v77\n"
"v_max3_u32 v239, v239, v78, v79\n"
"v_max3_u32 v239, v239, v148, v149\n"
"v_max3_u32 v239, v239, v150, v151\n"
"v_max3_u32 v239, v239, v152, v153\n"
"v_max3_u32 v239, v239, v154, v155\n"
"v_max3_u32 v239, v239, v172, v173\n"
"v_max3_u32 v239, v239, v174, v175\n"
"v_max3_u32 v239, v239, v176, v177\n"
"v_max3_u32 v239, v239, v178, v179\n"
"v_max3_u32 v239, v239, v64, v65\n"
"v_max3_u32 v239, v239, v66, v67\n"
"v_cmp_eq_u32_e32 vcc, -1, v239\n"
"s_cbranch_vccz .Lmk_p75k\n"
"s_sub_u32 s1, s1, 1\n"
"s_cbranch_scc0 .Lmk_p75l\n"
".Lmk_p75k:\n"
"v_mfma_f32_16x16x32_bf16 v[32:35], a[128:131], v[36:39], 0\n"
"v_mfma_f32_16x16x32_bf16 v[32:35], a[132:135], v[68:71], v[32:35]\n"
"v_mfma_f32_16x16x32_bf16 v[32:35], a[136:139], v[76:79], v[32:35]\n"
"v_mfma_f32_16x16x32_bf16 v[32:35], a[140:143], v[148:151], v[32:35]\n"
"v_mfma_f32_16x16x32_bf16 v[32:35], a[144:147], v[152:155], v[32:35]\n"
"v_mfma_f32_16x16x32_bf16 v[32:35], a[148:151], v[172:175], v[32:35]\n"
"v_mfma_f32_16x16x32_bf16 v[32:35], a[152:155], v[176:179], v[32:35]\n"
"v_mfma_f32_16x16x32_bf16 v[32:35], a[156:159], v[64:67], v[32:35]\n"
"v_mfma_f32_16x16x32_bf16 v[48:51], a[188:191], v[64:67], 0\n"
"v_mfma_f32_16x16x32_bf16 v[48:51], a[184:187], v[176:179], v[48:51]\n"
"v_mfma_f32_16x16x32_bf16 v[48:51], a[180:183], v[172:175], v[48:51]\n"
"v_mfma_f32_16x16x32_bf16 v[48:51], a[176:179], v[152:155], v[48:51]\n"
"v_mfma_f32_16x16x32_bf16 v[48:51], a[172:175], v[148:151], v[48:51]\n"
"v_mfma_f32_16x16x32_bf16 v[48:51], a[168:171], v[76:79], v[48:51]\n"
"s_nop 7\n"
"s_branch .Lmk_p75r\n"
".Lmk_p76s:\n"
";\n"
"s_mov_b32 s1, 0x10000\n"
".Lmk_p76l:\n"
"s_add_u32 s97, s59, 0x400000\n"
"buffer_load_dwordx4 v[80:83], v0, s[4:7], s97 offen sc1\n"
"buffer_load_dwordx4 v[88:91], v0, s[4:7], s97 offen offset:1024 sc1\n"
"buffer_load_dwordx4 v[40:43], v0, s[4:7], s97 offen offset:2048 sc1\n"
"buffer_load_dwordx4 v[96:99], v0, s[4:7], s97 offen offset:3072 sc1\n"
"buffer_load_dwordx4 v[100:103], v1, s[4:7], s97 offen sc1\n"
"buffer_load_dwordx4 v[52:55], v1, s[4:7], s97 offen offset:1024 sc1\n"
"buffer_load_dwordx4 v[84:87], v1, s[4:7], s97 offen offset:2048 sc1\n"
"buffer_load_dwordx4 v[92:95], v1, s[4:7], s97 offen offset:3072 sc1\n"
"s_waitcnt vmcnt(0)\n"
"v_max_u32_e32 v255, v80, v81\n"
"v_max3_u32 v255, v255, v82, v83\n"
"v_max3_u32 v255, v255, v88, v89\n"
"v_max3_u32 v255, v255, v90, v91\n"
"v_max3_u32 v255, v255, v40, v41\n"
"v_max3_u32 v255, v255, v42, v43\n"
"v_max3_u32 v255, v255, v96, v97\n"
"v_max3_u32 v255, v255, v98, v99\n"
"v_max3_u32 v255, v255, v100, v101\n"
"v_max3_u32 v255, v255, v102, v103\n"
"v_max3_u32 v255, v255, v52, v53\n"
"v_max3_u32 v255, v255, v54, v55\n"
"v_max3_u32 v255, v255, v84, v85\n"
"v_max3_u32 v255, v255, v86, v87\n"
"v_max3_u32 v255, v255, v92, v93\n"
"v_max3_u32 v255, v255, v94, v95\n"
"v_cmp_eq_u32_e32 vcc, -1, v255\n"
"s_cbranch_vccz .Lmk_p76k\n"
"s_sub_u32 s1, s1, 1\n"
"s_cbranch_scc0 .Lmk_p76l\n"
".Lmk_p76k:\n"
"v_mfma_f32_16x16x32_bf16 v[104:107], a[128:131], v[80:83], v[60:63]\n"
"v_mfma_f32_16x16x32_bf16 v[104:107], a[132:135], v[88:91], v[104:107]\n"
"v_mfma_f32_16x16x32_bf16 v[104:107], a[136:139], v[40:43], v[104:107]\n"
"v_mfma_f32_16x16x32_bf16 v[104:107], a[140:143], v[96:99], v[104:107]\n"
"v_mfma_f32_16x16x32_bf16 v[104:107], a[144:147], v[100:103], v[104:107]\n"
"v_mfma_f32_16x16x32_bf16 v[104:107], a[148:151], v[52:55], v[104:107]\n"
"v_mfma_f32_16x16x32_bf16 v[104:107], a[152:155], v[84:87], v[104:107]\n"
"v_mfma_f32_16x16x32_bf16 v[104:107], a[156:159], v[92:95], v[104:107]\n"
"v_mfma_f32_16x16x32_bf16 v[76:79], a[188:191], v[92:95], v[108:111]\n"
"v_mfma_f32_16x16x32_bf16 v[76:79], a[184:187], v[84:87], v[76:79]\n"
"v_mfma_f32_16x16x32_bf16 v[76:79], a[180:183], v[52:55], v[76:79]\n"
"v_mfma_f32_16x16x32_bf16 v[76:79], a[176:179], v[100:103], v[76:79]\n"
"v_mfma_f32_16x16x32_bf16 v[76:79], a[172:175], v[96:99], v[76:79]\n"
"v_mfma_f32_16x16x32_bf16 v[76:79], a[168:171], v[40:43], v[76:79]\n"
"s_nop 7\n"
"s_branch .Lmk_p76r\n"
".Lmk_p81s:\n"
";\n"
"s_mov_b32 s1, 0x10000\n"
".Lmk_p81l:\n"
"s_add_u32 s96, s52, 0x40000\n"
"buffer_load_dwordx4 v[152:155], v0, s[4:7], s96 offen sc1\n"
"buffer_load_dwordx4 v[156:159], v0, s[4:7], s96 offen offset:1024 sc1\n"
"buffer_load_dwordx4 v[84:87], v0, s[4:7], s96 offen offset:2048 sc1\n"
"buffer_load_dwordx4 v[76:79], v0, s[4:7], s96 offen offset:3072 sc1\n"
"buffer_load_dwordx4 v[40:43], v1, s[4:7], s96 offen sc1\n"
"buffer_load_dwordx4 v[80:83], v1, s[4:7], s96 offen offset:1024 sc1\n"
"buffer_load_dwordx4 v[88:91], v1, s[4:7], s96 offen offset:2048 sc1\n"
"buffer_load_dwordx4 v[92:95], v1, s[4:7], s96 offen offset:3072 sc1\n"
"s_add_u32 s96, s53, 0x40000\n"
"buffer_load_dwordx4 v[96:99], v0, s[4:7], s96 offen sc1\n"
"buffer_load_dwordx4 v[100:103], v0, s[4:7], s96 offen offset:1024 sc1\n"
"buffer_load_dwordx4 v[104:107], v0, s[4:7], s96 offen offset:2048 sc1\n"
"buffer_load_dwordx4 v[132:135], v0, s[4:7], s96 offen offset:3072 sc1\n"
"buffer_load_dwordx4 v[136:139], v1, s[4:7], s96 offen sc1\n"
"buffer_load_dwordx4 v[160:163], v1, s[4:7], s96 offen offset:1024 sc1\n"
"buffer_load_dwordx4 v[164:167], v1, s[4:7], s96 offen offset:2048 sc1\n"
"buffer_load_dwordx4 v[168:171], v1, s[4:7], s96 offen offset:3072 sc1\n"
"s_add_u32 s96, s54, 0x40000\n"
"buffer_load_dwordx4 v[176:179], v0, s[4:7], s96 offen sc1\n"
"buffer_load_dwordx4 v[184:187], v0, s[4:7], s96 offen offset:1024 sc1\n"
"s_waitcnt vmcnt(0)\n"
"v_max_u32_e32 v252, v152, v153\n"
"v_max3_u32 v252, v252, v154, v155\n"
"v_max3_u32 v252, v252, v156, v157\n"
"v_max3_u32 v252, v252, v158, v159\n"
"v_max3_u32 v252, v252, v84, v85\n"
"v_max3_u32 v252, v252, v86, v87\n"
"v_max3_u32 v252, v252, v76, v77\n"
"v_max3_u32 v252, v252, v78, v79\n"
"v_max3_u32 v252, v252, v40, v41\n"
"v_max3_u32 v252, v252, v42, v43\n"
"v_max3_u32 v252, v252, v80, v81\n"
"v_max3_u32 v252, v252, v82, v83\n"
"v_max3_u32 v252, v252, v88, v89\n"
"v_max3_u32 v252, v252, v90, v91\n"
"v_max3_u32 v252, v252, v92, v93\n"
"v_max3_u32 v252, v252, v94, v95\n"
"v_cmp_eq_u32_e32 vcc, -1, v252\n"
"s_cbranch_vccz .Lmk_p81k\n"
"s_sub_u32 s1, s1, 1\n"
"s_cbranch_scc0 .Lmk_p81l\n"
".Lmk_p81k:\n"
"v_mfma_f32_16x16x32_bf16 v[172:175], a[0:3], v[152:155], 0\n"
"v_mfma_f32_16x16x32_bf16 v[172:175], a[4:7], v[156:159], v[172:175]\n"
"v_mfma_f32_16x16x32_bf16 v[172:175], a[8:11], v[84:87], v[172:175]\n"
"v_mfma_f32_16x16x32_bf16 v[172:175], a[12:15], v[76:79], v[172:175]\n"
"v_mfma_f32_16x16x32_bf16 v[172:175], a[16:19], v[40:43], v[172:175]\n"
"v_mfma_f32_16x16x32_bf16 v[172:175], a[20:23], v[80:83], v[172:175]\n"
"v_mfma_f32_16x16x32_bf16 v[172:175], a[24:27], v[88:91], v[172:175]\n"
"v_mfma_f32_16x16x32_bf16 v[172:175], a[28:31], v[92:95], v[172:175]\n"
"v_mfma_f32_16x16x32_bf16 v[188:191], a[60:63], v[92:95], 0\n"
"v_mfma_f32_16x16x32_bf16 v[188:191], a[56:59], v[88:91], v[188:191]\n"
"v_mfma_f32_16x16x32_bf16 v[188:191], a[52:55], v[80:83], v[188:191]\n"
"v_mfma_f32_16x16x32_bf16 v[188:191], a[48:51], v[40:43], v[188:191]\n"
"v_mfma_f32_16x16x32_bf16 v[188:191], a[44:47], v[76:79], v[188:191]\n"
"v_mfma_f32_16x16x32_bf16 v[188:191], a[40:43], v[84:87], v[188:191]\n"
"s_nop 7\n"
"s_branch .Lmk_p81r\n"
".Lmk_p82s:\n"
";\n"
"s_mov_b32 s1, 0x10000\n"
".Lmk_p82l:\n"
"s_add_u32 s97, s53, 0x40000\n"
"buffer_load_dwordx4 v[96:99], v0, s[4:7], s97 offen sc1\n"
"buffer_load_dwordx4 v[100:103], v0, s[4:7], s97 offen offset:1024 sc1\n"
"buffer_load_dwordx4 v[104:107], v0, s[4:7], s97 offen offset:2048 sc1\n"
"buffer_load_dwordx4 v[132:135], v0, s[4:7], s97 offen offset:3072 sc1\n"
"buffer_load_dwordx4 v[136:139], v1, s[4:7], s97 offen sc1\n"
"buffer_load_dwordx4 v[160:163], v1, s[4:7], s97 offen offset:1024 sc1\n"
"buffer_load_dwordx4 v[164:167], v1, s[4:7], s97 offen offset:2048 sc1\n"
"buffer_load_dwordx4 v[168:171], v1, s[4:7], s97 offen offset:3072 sc1\n"
"s_add_u32 s97, s54, 0x40000\n"
"buffer_load_dwordx4 v[176:179], v0, s[4:7], s97 offen sc1\n"
"buffer_load_dwordx4 v[184:187], v0, s[4:7], s97 offen offset:1024 sc1\n"
"buffer_load_dwordx4 v[72:75], v0, s[4:7], s97 offen offset:2048 sc1\n"
"buffer_load_dwordx4 v[192:195], v0, s[4:7], s97 offen offset:3072 sc1\n"
"buffer_load_dwordx4 v[140:143], v1, s[4:7], s97 offen sc1\n"
"buffer_load_dwordx4 v[60:63], v1, s[4:7], s97 offen offset:1024 sc1\n"
"buffer_load_dwordx4 v[172:175], v1, s[4:7], s97 offen offset:2048 sc1\n"
"buffer_load_dwordx4 v[180:183], v1, s[4:7], s97 offen offset:3072 sc1\n"
"s_add_u32 s97, s55, 0x40000\n"
"buffer_load_dwordx4 v[68:71], v0, s[4:7], s97 offen sc1\n"
"buffer_load_dwordx4 v[76:79], v0, s[4:7], s97 offen offset:1024 sc1\n"
"s_waitcnt vmcnt(0)\n"
"v_max_u32_e32 v247, v96, v97\n"
"v_max3_u32 v247, v247, v98, v99\n"
"v_max3_u32 v247, v247, v100, v101\n"
"v_max3_u32 v247, v247, v102, v103\n"
"v_max3_u32 v247, v247, v104, v105\n"
"v_max3_u32 v247, v247, v106, v107\n"
"v_max3_u32 v247, v247, v132, v133\n"
"v_max3_u32 v247, v247, v134, v135\n"
"v_max3_u32 v247, v247, v136, v137\n"
"v_max3_u32 v247, v247, v138, v139\n"
"v_max3_u32 v247, v247, v160, v161\n"
"v_max3_u32 v247, v247, v162, v163\n"
"v_max3_u32 v247, v247, v164, v165\n"
"v_max3_u32 v247, v247, v166, v167\n"
"v_max3_u32 v247, v247, v168, v169\n"
"v_max3_u32 v247, v247, v170, v171\n"
"v_cmp_eq_u32_e32 vcc, -1, v247\n"
"s_cbranch_vccz .Lmk_p82k\n"
"s_sub_u32 s1, s1, 1\n"
"s_cbranch_scc0 .Lmk_p82l\n"
".Lmk_p82k:\n"
"v_mfma_f32_16x16x32_bf16 v[40:43], a[0:3], v[96:99], 0\n"
"v_mfma_f32_16x16x32_bf16 v[40:43], a[4:7], v[100:103], v[40:43]\n"
"v_mfma_f32_16x16x32_bf16 v[40:43], a[8:11], v[104:107], v[40:43]\n"
"v_mfma_f32_16x16x32_bf16 v[40:43], a[12:15], v[132:135], v[40:43]\n"
"v_mfma_f32_16x16x32_bf16 v[40:43], a[16:19], v[136:139], v[40:43]\n"
"v_mfma_f32_16x16x32_bf16 v[40:43], a[20:23], v[160:163], v[40:43]\n"
"v_mfma_f32_16x16x32_bf16 v[40:43], a[24:27], v[164:167], v[40:43]\n"
"v_mfma_f32_16x16x32_bf16 v[40:43], a[28:31], v[168:171], v[40:43]\n"
"v_mfma_f32_16x16x32_bf16 v[80:83], a[60:63], v[168:171], 0\n"
"v_mfma_f32_16x16x32_bf16 v[80:83], a[56:59], v[164:167], v[80:83]\n"
"v_mfma_f32_16x16x32_bf16 v[80:83], a[52:55], v[160:163], v[80:83]\n"
"v_mfma_f32_16x16x32_bf16 v[80:83], a[48:51], v[136:139], v[80:83]\n"
"v_mfma_f32_16x16x32_bf16 v[80:83], a[44:47], v[132:135], v[80:83]\n"
"v_mfma_f32_16x16x32_bf16 v[80:83], a[40:43], v[104:107], v[80:83]\n"
"s_nop 7\n"
"s_branch .Lmk_p82r\n"
".Lmk_m85s:\n"
";\n"
"s_mov_b32 s1, 0x10000\n"
".Lmk_m85l:\n"
"s_add_u32 s98, s62, 0x4000\n"
"buffer_load_dword v255, v10, s[4:7], s98 offen sc1\n"
"buffer_load_dword v254, v10, s[4:7], s98 offen offset:256 sc1\n"
"buffer_load_dword v253, v10, s[4:7], s98 offen offset:512 sc1\n"
"buffer_load_dword v252, v10, s[4:7], s98 offen offset:768 sc1\n"
"buffer_load_dword v251, v10, s[4:7], s98 offen offset:1024 sc1\n"
"buffer_load_dword v250, v10, s[4:7], s98 offen offset:1280 sc1\n"
"buffer_load_dword v249, v10, s[4:7], s98 offen offset:1536 sc1\n"
"buffer_load_dword v248, v10, s[4:7], s98 offen offset:1792 sc1\n"
"s_waitcnt vmcnt(0)\n"
"v_max_u32_e32 v247, v255, v254\n"
"v_max3_u32 v247, v247, v253, v252\n"
"v_max3_u32 v247, v247, v251, v250\n"
"v_max3_u32 v247, v247, v249, v248\n"
"v_cmp_eq_u32_e32 vcc, -1, v247\n"
"s_cbranch_vccz .Lmk_m85k\n"
"s_sub_u32 s1, s1, 1\n"
"s_cbranch_scc0 .Lmk_m85l\n"
".Lmk_m85k:\n"
"v_add_f32_e32 v255, v255, v251\n"
"v_add_f32_e32 v254, v254, v250\n"
"v_add_f32_e32 v253, v253, v249\n"
"v_add_f32_e32 v252, v252, v248\n"
"v_add_f32_e32 v255, v255, v253\n"
"v_add_f32_e32 v254, v254, v252\n"
"v_add_f32_e32 v255, v255, v254\n"
"s_nop 1\n"
"s_branch .Lmk_m85r\n"
".Lmk_p83s:\n"
";\n"
"s_mov_b32 s1, 0x10000\n"
".Lmk_p83l:\n"
"s_add_u32 s97, s54, 0x40000\n"
"buffer_load_dwordx4 v[176:179], v0, s[4:7], s97 offen sc1\n"
"buffer_load_dwordx4 v[184:187], v0, s[4:7], s97 offen offset:1024 sc1\n"
"buffer_load_dwordx4 v[72:75], v0, s[4:7], s97 offen offset:2048 sc1\n"
"buffer_load_dwordx4 v[192:195], v0, s[4:7], s97 offen offset:3072 sc1\n"
"buffer_load_dwordx4 v[140:143], v1, s[4:7], s97 offen sc1\n"
"buffer_load_dwordx4 v[60:63], v1, s[4:7], s97 offen offset:1024 sc1\n"
"buffer_load_dwordx4 v[172:175], v1, s[4:7], s97 offen offset:2048 sc1\n"
"buffer_load_dwordx4 v[180:183], v1, s[4:7], s97 offen offset:3072 sc1\n"
"s_add_u32 s97, s55, 0x40000\n"
"buffer_load_dwordx4 v[68:71], v0, s[4:7], s97 offen sc1\n"
"buffer_load_dwordx4 v[76:79], v0, s[4:7], s97 offen offset:1024 sc1\n"
"buffer_load_dwordx4 v[84:87], v0, s[4:7], s97 offen offset:2048 sc1\n"
"buffer_load_dwordx4 v[88:91], v0, s[4:7], s97 offen offset:3072 sc1\n"
"buffer_load_dwordx4 v[80:83], v1, s[4:7], s97 offen sc1\n"
"buffer_load_dwordx4 v[152:155], v1, s[4:7], s97 offen offset:1024 sc1\n"
"buffer_load_dwordx4 v[156:159], v1, s[4:7], s97 offen offset:2048 sc1\n"
"buffer_load_dwordx4 v[188:191], v1, s[4:7], s97 offen offset:3072 sc1\n"
"s_add_u32 s97, s52, 0x48000\n"
"buffer_load_dwordx4 v[96:99], v0, s[4:7], s97 offen sc1\n"
"s_waitcnt vmcnt(0)\n"
"v_max_u32_e32 v252, v176, v177\n"
"v_max3_u32 v252, v252, v178, v179\n"
"v_max3_u32 v252, v252, v184, v185\n"
"v_max3_u32 v252, v252, v186, v187\n"
"v_max3_u32 v252, v252, v72, v73\n"
"v_max3_u32 v252, v252, v74, v75\n"
"v_max3_u32 v252, v252, v192, v193\n"
"v_max3_u32 v252, v252, v194, v195\n"
"v_max3_u32 v252, v252, v140, v141\n"
"v_max3_u32 v252, v252, v142, v143\n"
"v_max3_u32 v252, v252, v60, v61\n"
"v_max3_u32 v252, v252, v62, v63\n"
"v_max3_u32 v252, v252, v172, v173\n"
"v_max3_u32 v252, v252, v174, v175\n"
"v_max3_u32 v252, v252, v180, v181\n"
"v_max3_u32 v252, v252, v182, v183\n"
"v_cmp_eq_u32_e32 vcc, -1, v252\n"
"s_cbranch_vccz .Lmk_p83k\n"
"s_sub_u32 s1, s1, 1\n"
"s_cbranch_scc0 .Lmk_p83l\n"
".Lmk_p83k:\n"
"v_mfma_f32_16x16x32_bf16 v[40:43], a[0:3], v[176:179], 0\n"
"v_mfma_f32_16x16x32_bf16 v[40:43], a[4:7], v[184:187], v[40:43]\n"
"v_mfma_f32_16x16x32_bf16 v[40:43], a[8:11], v[72:75], v[40:43]\n"
"v_mfma_f32_16x16x32_bf16 v[40:43], a[12:15], v[192:195], v[40:43]\n"
"v_mfma_f32_16x16x32_bf16 v[40:43], a[16:19], v[140:143], v[40:43]\n"
"v_mfma_f32_16x16x32_bf16 v[40:43], a[20:23], v[60:63], v[40:43]\n"
"v_mfma_f32_16x16x32_bf16 v[40:43], a[24:27], v[172:175], v[40:43]\n"
"v_mfma_f32_16x16x32_bf16 v[40:43], a[28:31], v[180:183], v[40:43]\n"
"v_mfma_f32_16x16x32_bf16 v[92:95], a[60:63], v[180:183], 0\n"
"v_mfma_f32_16x16x32_bf16 v[92:95], a[56:59], v[172:175], v[92:95]\n"
"v_mfma_f32_16x16x32_bf16 v[92:95], a[52:55], v[60:63], v[92:95]\n"
"v_mfma_f32_16x16x32_bf16 v[92:95], a[48:51], v[140:143], v[92:95]\n"
"v_mfma_f32_16x16x32_bf16 v[92:95], a[44:47], v[192:195], v[92:95]\n"
"v_mfma_f32_16x16x32_bf16 v[92:95], a[40:43], v[72:75], v[92:95]\n"
"s_nop 7\n"
"s_branch .Lmk_p83r\n"
".Lmk_p84s:\n"
";\n"
"s_mov_b32 s1, 0x10000\n"
".Lmk_p84l:\n"
"s_add_u32 s97, s55, 0x40000\n"
"buffer_load_dwordx4 v[68:71], v0, s[4:7], s97 offen sc1\n"
"buffer_load_dwordx4 v[76:79], v0, s[4:7], s97 offen offset:1024 sc1\n"
"buffer_load_dwordx4 v[84:87], v0, s[4:7], s97 offen offset:2048 sc1\n"
"buffer_load_dwordx4 v[88:91], v0, s[4:7], s97 offen offset:3072 sc1\n"
"buffer_load_dwordx4 v[80:83], v1, s[4:7], s97 offen sc1\n"
"buffer_load_dwordx4 v[152:155], v1, s[4:7], s97 offen offset:1024 sc1\n"
"buffer_load_dwordx4 v[156:159], v1, s[4:7], s97 offen offset:2048 sc1\n"
"buffer_load_dwordx4 v[188:191], v1, s[4:7], s97 offen offset:3072 sc1\n"
"s_add_u32 s97, s52, 0x48000\n"
"buffer_load_dwordx4 v[96:99], v0, s[4:7], s97 offen sc1\n"
"buffer_load_dwordx4 v[40:43], v0, s[4:7], s97 offen offset:1024 sc1\n"
"buffer_load_dwordx4 v[168:171], v0, s[4:7], s97 offen offset:2048 sc1\n"
"buffer_load_dwordx4 v[132:135], v0, s[4:7], s97 offen offset:3072 sc1\n"
"buffer_load_dwordx4 v[104:107], v1, s[4:7], s97 offen sc1\n"
"buffer_load_dwordx4 v[164:167], v1, s[4:7], s97 offen offset:1024 sc1\n"
"buffer_load_dwordx4 v[180:183], v1, s[4:7], s97 offen offset:2048 sc1\n"
"buffer_load_dwordx4 v[72:75], v1, s[4:7], s97 offen offset:3072 sc1\n"
"s_add_u32 s97, s53, 0x48000\n"
"buffer_load_dwordx4 v[196:199], v0, s[4:7], s97 offen sc1\n"
"buffer_load_dwordx4 v[92:95], v0, s[4:7], s97 offen offset:1024 sc1\n"
"s_waitcnt vmcnt(0)\n"
"v_max_u32_e32 v252, v68, v69\n"
"v_max3_u32 v252, v252, v70, v71\n"
"v_max3_u32 v252, v252, v76, v77\n"
"v_max3_u32 v252, v252, v78, v79\n"
"v_max3_u32 v252, v252, v84, v85\n"
"v_max3_u32 v252, v252, v86, v87\n"
"v_max3_u32 v252, v252, v88, v89\n"
"v_max3_u32 v252, v252, v90, v91\n"
"v_max3_u32 v252, v252, v80, v81\n"
"v_max3_u32 v252, v252, v82, v83\n"
"v_max3_u32 v252, v252, v152, v153\n"
"v_max3_u32 v252, v252, v154, v155\n"
"v_max3_u32 v252, v252, v156, v157\n"
"v_max3_u32 v252, v252, v158, v159\n"
"v_max3_u32 v252, v252, v188, v189\n"
"v_max3_u32 v252, v252, v190, v191\n"
"v_cmp_eq_u32_e32 vcc, -1, v252\n"
"s_cbranch_vccz .Lmk_p84k\n"
"s_sub_u32 s1, s1, 1\n"
"s_cbranch_scc0 .Lmk_p84l\n"
".Lmk_p84k:\n"
"v_mfma_f32_16x16x32_bf16 v[184:187], a[0:3], v[68:71], v[100:103]\n"
"v_mfma_f32_16x16x32_bf16 v[184:187], a[4:7], v[76:79], v[184:187]\n"
"v_mfma_f32_16x16x32_bf16 v[184:187], a[8:11], v[84:87], v[184:187]\n"
"v_mfma_f32_16x16x32_bf16 v[184:187], a[12:15], v[88:91], v[184:187]\n"
"v_mfma_f32_16x16x32_bf16 v[184:187], a[16:19], v[80:83], v[184:187]\n"
"v_mfma_f32_16x16x32_bf16 v[184:187], a[20:23], v[152:155], v[184:187]\n"
"v_mfma_f32_16x16x32_bf16 v[184:187], a[24:27], v[156:159], v[184:187]\n"
"v_mfma_f32_16x16x32_bf16 v[184:187], a[28:31], v[188:191], v[184:187]\n"
"v_mfma_f32_16x16x32_bf16 v[204:207], a[60:63], v[188:191], v[160:163]\n"
"v_mfma_f32_16x16x32_bf16 v[204:207], a[56:59], v[156:159], v[204:207]\n"
"v_mfma_f32_16x16x32_bf16 v[204:207], a[52:55], v[152:155], v[204:207]\n"
"v_mfma_f32_16x16x32_bf16 v[204:207], a[48:51], v[80:83], v[204:207]\n"
"v_mfma_f32_16x16x32_bf16 v[204:207], a[44:47], v[88:91], v[204:207]\n"
"v_mfma_f32_16x16x32_bf16 v[204:207], a[40:43], v[84:87], v[204:207]\n"
"s_nop 7\n"
"s_branch .Lmk_p84r\n"
".Lmk_p86s:\n"
";\n"
"s_mov_b32 s1, 0x10000\n"
".Lmk_p86l:\n"
"s_add_u32 s95, s52, 0x48000\n"
"buffer_load_dwordx4 v[96:99], v0, s[4:7], s95 offen sc1\n"
"buffer_load_dwordx4 v[40:43], v0, s[4:7], s95 offen offset:1024 sc1\n"
"buffer_load_dwordx4 v[168:171], v0, s[4:7], s95 offen offset:2048 sc1\n"
"buffer_load_dwordx4 v[132:135], v0, s[4:7], s95 offen offset:3072 sc1\n"
"buffer_load_dwordx4 v[104:107], v1, s[4:7], s95 offen sc1\n"
"buffer_load_dwordx4 v[164:167], v1, s[4:7], s95 offen offset:1024 sc1\n"
"buffer_load_dwordx4 v[180:183], v1, s[4:7], s95 offen offset:2048 sc1\n"
"buffer_load_dwordx4 v[72:75], v1, s[4:7], s95 offen offset:3072 sc1\n"
"s_add_u32 s95, s53, 0x48000\n"
"buffer_load_dwordx4 v[196:199], v0, s[4:7], s95 offen sc1\n"
"buffer_load_dwordx4 v[92:95], v0, s[4:7], s95 offen offset:1024 sc1\n"
"buffer_load_dwordx4 v[100:103], v0, s[4:7], s95 offen offset:2048 sc1\n"
"buffer_load_dwordx4 v[60:63], v0, s[4:7], s95 offen offset:3072 sc1\n"
"buffer_load_dwordx4 v[140:143], v1, s[4:7], s95 offen sc1\n"
"buffer_load_dwordx4 v[192:195], v1, s[4:7], s95 offen offset:1024 sc1\n"
"buffer_load_dwordx4 v[208:211], v1, s[4:7], s95 offen offset:2048 sc1\n"
"buffer_load_dwordx4 v[184:187], v1, s[4:7], s95 offen offset:3072 sc1\n"
"s_add_u32 s95, s54, 0x48000\n"
"buffer_load_dwordx4 v[76:79], v0, s[4:7], s95 offen sc1\n"
"buffer_load_dwordx4 v[80:83], v0, s[4:7], s95 offen offset:1024 sc1\n"
"s_waitcnt vmcnt(0)\n"
"v_max_u32_e32 v252, v96, v97\n"
"v_max3_u32 v252, v252, v98, v99\n"
"v_max3_u32 v252, v252, v40, v41\n"
"v_max3_u32 v252, v252, v42, v43\n"
"v_max3_u32 v252, v252, v168, v169\n"
"v_max3_u32 v252, v252, v170, v171\n"
"v_max3_u32 v252, v252, v132, v133\n"
"v_max3_u32 v252, v252, v134, v135\n"
"v_max3_u32 v252, v252, v104, v105\n"
"v_max3_u32 v252, v252, v106, v107\n"
"v_max3_u32 v252, v252, v164, v165\n"
"v_max3_u32 v252, v252, v166, v167\n"
"v_max3_u32 v252, v252, v180, v181\n"
"v_max3_u32 v252, v252, v182, v183\n"
"v_max3_u32 v252, v252, v72, v73\n"
"v_max3_u32 v252, v252, v74, v75\n"
"v_cmp_eq_u32_e32 vcc, -1, v252\n"
"s_cbranch_vccz .Lmk_p86k\n"
"s_sub_u32 s1, s1, 1\n"
"s_cbranch_scc0 .Lmk_p86l\n"
".Lmk_p86k:\n"
"v_mfma_f32_16x16x32_bf16 v[68:71], a[0:3], v[96:99], 0\n"
"v_mfma_f32_16x16x32_bf16 v[68:71], a[4:7], v[40:43], v[68:71]\n"
"v_mfma_f32_16x16x32_bf16 v[68:71], a[8:11], v[168:171], v[68:71]\n"
"v_mfma_f32_16x16x32_bf16 v[68:71], a[12:15], v[132:135], v[68:71]\n"
"v_mfma_f32_16x16x32_bf16 v[68:71], a[16:19], v[104:107], v[68:71]\n"
"v_mfma_f32_16x16x32_bf16 v[68:71], a[20:23], v[164:167], v[68:71]\n"
"v_mfma_f32_16x16x32_bf16 v[68:71], a[24:27], v[180:183], v[68:71]\n"
"v_mfma_f32_16x16x32_bf16 v[68:71], a[28:31], v[72:75], v[68:71]\n"
"v_mfma_f32_16x16x32_bf16 v[84:87], a[60:63], v[72:75], 0\n"
"v_mfma_f32_16x16x32_bf16 v[84:87], a[56:59], v[180:183], v[84:87]\n"
"v_mfma_f32_16x16x32_bf16 v[84:87], a[52:55], v[164:167], v[84:87]\n"
"v_mfma_f32_16x16x32_bf16 v[84:87], a[48:51], v[104:107], v[84:87]\n"
"v_mfma_f32_16x16x32_bf16 v[84:87], a[44:47], v[132:135], v[84:87]\n"
"v_mfma_f32_16x16x32_bf16 v[84:87], a[40:43], v[168:171], v[84:87]\n"
"s_nop 7\n"
"s_branch .Lmk_p86r\n"
".Lmk_p87s:\n"
";\n"
"s_mov_b32 s1, 0x10000\n"
".Lmk_p87l:\n"
"s_add_u32 s95, s53, 0x48000\n"
"buffer_load_dwordx4 v[196:199], v0, s[4:7], s95 offen sc1\n"
"buffer_load_dwordx4 v[92:95], v0, s[4:7], s95 offen offset:1024 sc1\n"
"buffer_load_dwordx4 v[100:103], v0, s[4:7], s95 offen offset:2048 sc1\n"
"buffer_load_dwordx4 v[60:63], v0, s[4:7], s95 offen offset:3072 sc1\n"
"buffer_load_dwordx4 v[140:143], v1, s[4:7], s95 offen sc1\n"
"buffer_load_dwordx4 v[192:195], v1, s[4:7], s95 offen offset:1024 sc1\n"
"buffer_load_dwordx4 v[208:211], v1, s[4:7], s95 offen offset:2048 sc1\n"
"buffer_load_dwordx4 v[184:187], v1, s[4:7], s95 offen offset:3072 sc1\n"
"s_add_u32 s95, s54, 0x48000\n"
"buffer_load_dwordx4 v[76:79], v0, s[4:7], s95 offen sc1\n"
"buffer_load_dwordx4 v[80:83], v0, s[4:7], s95 offen offset:1024 sc1\n"
"buffer_load_dwordx4 v[88:91], v0, s[4:7], s95 offen offset:2048 sc1\n"
"buffer_load_dwordx4 v[152:155], v0, s[4:7], s95 offen offset:3072 sc1\n"
"buffer_load_dwordx4 v[84:87], v1, s[4:7], s95 offen sc1\n"
"buffer_load_dwordx4 v[172:175], v1, s[4:7], s95 offen offset:1024 sc1\n"
"buffer_load_dwordx4 v[136:139], v1, s[4:7], s95 offen offset:2048 sc1\n"
"buffer_load_dwordx4 v[160:163], v1, s[4:7], s95 offen offset:3072 sc1\n"
"s_add_u32 s95, s55, 0x48000\n"
"buffer_load_dwordx4 v[68:71], v0, s[4:7], s95 offen sc1\n"
"buffer_load_dwordx4 v[72:75], v0, s[4:7], s95 offen offset:1024 sc1\n"
"s_waitcnt vmcnt(0)\n"
"v_max_u32_e32 v247, v196, v197\n"
"v_max3_u32 v247, v247, v198, v199\n"
"v_max3_u32 v247, v247, v92, v93\n"
"v_max3_u32 v247, v247, v94, v95\n"
"v_max3_u32 v247, v247, v100, v101\n"
"v_max3_u32 v247, v247, v102, v103\n"
"v_max3_u32 v247, v247, v60, v61\n"
"v_max3_u32 v247, v247, v62, v63\n"
"v_max3_u32 v247, v247, v140, v141\n"
"v_max3_u32 v247, v247, v142, v143\n"
"v_max3_u32 v247, v247, v192, v193\n"
"v_max3_u32 v247, v247, v194, v195\n"
"v_max3_u32 v247, v247, v208, v209\n"
"v_max3_u32 v247, v247, v210, v211\n"
"v_max3_u32 v247, v247, v184, v185\n"
"v_max3_u32 v247, v247, v186, v187\n"
"v_cmp_eq_u32_e32 vcc, -1, v247\n"
"s_cbranch_vccz .Lmk_p87k\n"
"s_sub_u32 s1, s1, 1\n"
"s_cbranch_scc0 .Lmk_p87l\n"
".Lmk_p87k:\n"
"v_mfma_f32_16x16x32_bf16 v[40:43], a[0:3], v[196:199], 0\n"
"v_mfma_f32_16x16x32_bf16 v[40:43], a[4:7], v[92:95], v[40:43]\n"
"v_mfma_f32_16x16x32_bf16 v[40:43], a[8:11], v[100:103], v[40:43]\n"
"v_mfma_f32_16x16x32_bf16 v[40:43], a[12:15], v[60:63], v[40:43]\n"
"v_mfma_f32_16x16x32_bf16 v[40:43], a[16:19], v[140:143], v[40:43]\n"
"v_mfma_f32_16x16x32_bf16 v[40:43], a[20:23], v[192:195], v[40:43]\n"
"v_mfma_f32_16x16x32_bf16 v[40:43], a[24:27], v[208:211], v[40:43]\n"
"v_mfma_f32_16x16x32_bf16 v[40:43], a[28:31], v[184:187], v[40:43]\n"
"v_mfma_f32_16x16x32_bf16 v[96:99], a[60:63], v[184:187], 0\n"
"v_mfma_f32_16x16x32_bf16 v[96:99], a[56:59], v[208:211], v[96:99]\n"
"v_mfma_f32_16x16x32_bf16 v[96:99], a[52:55], v[192:195], v[96:99]\n"
"v_mfma_f32_16x16x32_bf16 v[96:99], a[48:51], v[140:143], v[96:99]\n"
"v_mfma_f32_16x16x32_bf16 v[96:99], a[44:47], v[60:63], v[96:99]\n"
"v_mfma_f32_16x16x32_bf16 v[96:99], a[40:43], v[100:103], v[96:99]\n"
"s_nop 7\n"
"s_branch .Lmk_p87r\n"
".Lmk_m90s:\n"
";\n"
"s_mov_b32 s1, 0x10000\n"
".Lmk_m90l:\n"
"s_add_u32 s98, s62, 0x4800\n"
"buffer_load_dword v255, v10, s[4:7], s98 offen sc1\n"
"buffer_load_dword v253, v10, s[4:7], s98 offen offset:256 sc1\n"
"buffer_load_dword v252, v10, s[4:7], s98 offen offset:512 sc1\n"
"buffer_load_dword v251, v10, s[4:7], s98 offen offset:768 sc1\n"
"buffer_load_dword v250, v10, s[4:7], s98 offen offset:1024 sc1\n"
"buffer_load_dword v254, v10, s[4:7], s98 offen offset:1280 sc1\n"
"buffer_load_dword v249, v10, s[4:7], s98 offen offset:1536 sc1\n"
"buffer_load_dword v248, v10, s[4:7], s98 offen offset:1792 sc1\n"
"s_waitcnt vmcnt(0)\n"
"v_max_u32_e32 v245, v255, v253\n"
"v_max3_u32 v245, v245, v252, v251\n"
"v_max3_u32 v245, v245, v250, v254\n"
"v_max3_u32 v245, v245, v249, v248\n"
"v_cmp_eq_u32_e32 vcc, -1, v245\n"
"s_cbranch_vccz .Lmk_m90k\n"
"s_sub_u32 s1, s1, 1\n"
"s_cbranch_scc0 .Lmk_m90l\n"
".Lmk_m90k:\n"
"v_add_f32_e32 v255, v255, v250\n"
"v_add_f32_e32 v253, v253, v254\n"
"v_add_f32_e32 v252, v252, v249\n"
"v_add_f32_e32 v251, v251, v248\n"
"v_add_f32_e32 v255, v255, v252\n"
"v_add_f32_e32 v253, v253, v251\n"
"v_add_f32_e32 v255, v255, v253\n"
"s_nop 1\n"
"s_branch .Lmk_m90r\n"
".Lmk_f77s:\n"
";\n"
"s_mov_b32 s1, 0x10000\n"
".Lmk_f77l:\n"
"buffer_load_dwordx2 v[246:247], v12, s[4:7], s97 offen sc1\n"
"s_waitcnt vmcnt(0)\n"
"v_cmp_ne_u64_e32 vcc, v[246:247], v[18:19]\n"
"s_cbranch_vccz .Lmk_f77r\n"
"s_sub_u32 s1, s1, 1\n"
"s_cbranch_scc0 .Lmk_f77l\n"
"s_branch .Lmk_f77r\n"
".Lmk_p88s:\n"
";\n"
"s_mov_b32 s1, 0x10000\n"
".Lmk_p88l:\n"
"s_add_u32 s97, s54, 0x48000\n"
"buffer_load_dwordx4 v[76:79], v0, s[4:7], s97 offen sc1\n"
"buffer_load_dwordx4 v[80:83], v0, s[4:7], s97 offen offset:1024 sc1\n"
"buffer_load_dwordx4 v[88:91], v0, s[4:7], s97 offen offset:2048 sc1\n"
"buffer_load_dwordx4 v[152:155], v0, s[4:7], s97 offen offset:3072 sc1\n"
"buffer_load_dwordx4 v[84:87], v1, s[4:7], s97 offen sc1\n"
"buffer_load_dwordx4 v[172:175], v1, s[4:7], s97 offen offset:1024 sc1\n"
"buffer_load_dwordx4 v[136:139], v1, s[4:7], s97 offen offset:2048 sc1\n"
"buffer_load_dwordx4 v[160:163], v1, s[4:7], s97 offen offset:3072 sc1\n"
"s_add_u32 s97, s55, 0x48000\n"
"buffer_load_dwordx4 v[68:71], v0, s[4:7], s97 offen sc1\n"
"buffer_load_dwordx4 v[72:75], v0, s[4:7], s97 offen offset:1024 sc1\n"
"buffer_load_dwordx4 v[104:107], v0, s[4:7], s97 offen offset:2048 sc1\n"
"buffer_load_dwordx4 v[132:135], v0, s[4:7], s97 offen offset:3072 sc1\n"
"buffer_load_dwordx4 v[96:99], v1, s[4:7], s97 offen sc1\n"
"buffer_load_dwordx4 v[164:167], v1, s[4:7], s97 offen offset:1024 sc1\n"
"buffer_load_dwordx4 v[168:171], v1, s[4:7], s97 offen offset:2048 sc1\n"
"buffer_load_dwordx4 v[176:179], v1, s[4:7], s97 offen offset:3072 sc1\n"
"s_waitcnt vmcnt(0)\n"
"v_max_u32_e32 v255, v76, v77\n"
"v_max3_u32 v255, v255, v78, v79\n"
"v_max3_u32 v255, v255, v80, v81\n"
"v_max3_u32 v255, v255, v82, v83\n"
"v_max3_u32 v255, v255, v88, v89\n"
"v_max3_u32 v255, v255, v90, v91\n"
"v_max3_u32 v255, v255, v152, v153\n"
"v_max3_u32 v255, v255, v154, v155\n"
"v_max3_u32 v255, v255, v84, v85\n"
"v_max3_u32 v255, v255, v86, v87\n"
"v_max3_u32 v255, v255, v172, v173\n"
"v_max3_u32 v255, v255, v174, v175\n"
"v_max3_u32 v255, v255, v136, v137\n"
"v_max3_u32 v255, v255, v138, v139\n"
"v_max3_u32 v255, v255, v160, v161\n"
"v_max3_u32 v255, v255, v162, v163\n"
"v_cmp_eq_u32_e32 vcc, -1, v255\n"
"s_cbranch_vccz .Lmk_p88k\n"
"s_sub_u32 s1, s1, 1\n"
"s_cbranch_scc0 .Lmk_p88l\n"
".Lmk_p88k:\n"
"v_mfma_f32_16x16x32_bf16 v[40:43], a[0:3], v[76:79], 0\n"
"v_mfma_f32_16x16x32_bf16 v[40:43], a[4:7], v[80:83], v[40:43]\n"
"v_mfma_f32_16x16x32_bf16 v[40:43], a[8:11], v[88:91], v[40:43]\n"
"v_mfma_f32_16x16x32_bf16 v[40:43], a[12:15], v[152:155], v[40:43]\n"
"v_mfma_f32_16x16x32_bf16 v[40:43], a[16:19], v[84:87], v[40:43]\n"
"v_mfma_f32_16x16x32_bf16 v[40:43], a[20:23], v[172:175], v[40:43]\n"
"v_mfma_f32_16x16x32_bf16 v[40:43], a[24:27], v[136:139], v[40:43]\n"
"v_mfma_f32_16x16x32_bf16 v[40:43], a[28:31], v[160:163], v[40:43]\n"
"v_mfma_f32_16x16x32_bf16 v[52:55], a[60:63], v[160:163], 0\n"
"v_mfma_f32_16x16x32_bf16 v[52:55], a[56:59], v[136:139], v[52:55]\n"
"v_mfma_f32_16x16x32_bf16 v[52:55], a[52:55], v[172:175], v[52:55]\n"
"v_mfma_f32_16x16x32_bf16 v[52:55], a[48:51], v[84:87], v[52:55]\n"
"v_mfma_f32_16x16x32_bf16 v[52:55], a[44:47], v[152:155], v[52:55]\n"
"v_mfma_f32_16x16x32_bf16 v[52:55], a[40:43], v[88:91], v[52:55]\n"
"s_nop 7\n"
"s_branch .Lmk_p88r\n"
".Lmk_p89s:\n"
";\n"
"s_mov_b32 s1, 0x10000\n"
".Lmk_p89l:\n"
"s_add_u32 s97, s55, 0x48000\n"
"buffer_load_dwordx4 v[68:71], v0, s[4:7], s97 offen sc1\n"
"buffer_load_dwordx4 v[72:75], v0, s[4:7], s97 offen offset:1024 sc1\n"
"buffer_load_dwordx4 v[104:107], v0, s[4:7], s97 offen offset:2048 sc1\n"
"buffer_load_dwordx4 v[132:135], v0, s[4:7], s97 offen offset:3072 sc1\n"
"buffer_load_dwordx4 v[96:99], v1, s[4:7], s97 offen sc1\n"
"buffer_load_dwordx4 v[164:167], v1, s[4:7], s97 offen offset:1024 sc1\n"
"buffer_load_dwordx4 v[168:171], v1, s[4:7], s97 offen offset:2048 sc1\n"
"buffer_load_dwordx4 v[176:179], v1, s[4:7], s97 offen offset:3072 sc1\n"
"s_waitcnt vmcnt(0)\n"
"v_max_u32_e32 v252, v68, v69\n"
"v_max3_u32 v252, v252, v70, v71\n"
"v_max3_u32 v252, v252, v72, v73\n"
"v_max3_u32 v252, v252, v74, v75\n"
"v_max3_u32 v252, v252, v104, v105\n"
"v_max3_u32 v252, v252, v106, v107\n"
"v_max3_u32 v252, v252, v132, v133\n"
"v_max3_u32 v252, v252, v134, v135\n"
"v_max3_u32 v252, v252, v96, v97\n"
"v_max3_u32 v252, v252, v98, v99\n"
"v_max3_u32 v252, v252, v164, v165\n"
"v_max3_u32 v252, v252, v166, v167\n"
"v_max3_u32 v252, v252, v168, v169\n"
"v_max3_u32 v252, v252, v170, v171\n"
"v_max3_u32 v252, v252, v176, v177\n"
"v_max3_u32 v252, v252, v178, v179\n"
"v_cmp_eq_u32_e32 vcc, -1, v252\n"
"s_cbranch_vccz .Lmk_p89k\n"
"s_sub_u32 s1, s1, 1\n"
"s_cbranch_scc0 .Lmk_p89l\n"
".Lmk_p89k:\n"
"v_mfma_f32_16x16x32_bf16 v[88:91], a[0:3], v[68:71], v[92:95]\n"
"v_mfma_f32_16x16x32_bf16 v[88:91], a[4:7], v[72:75], v[88:91]\n"
"v_mfma_f32_16x16x32_bf16 v[88:91], a[8:11], v[104:107], v[88:91]\n"
"v_mfma_f32_16x16x32_bf16 v[88:91], a[12:15], v[132:135], v[88:91]\n"
"v_mfma_f32_16x16x32_bf16 v[88:91], a[16:19], v[96:99], v[88:91]\n"
"v_mfma_f32_16x16x32_bf16 v[88:91], a[20:23], v[164:167], v[88:91]\n"
"v_mfma_f32_16x16x32_bf16 v[88:91], a[24:27], v[168:171], v[88:91]\n"
"v_mfma_f32_16x16x32_bf16 v[88:91], a[28:31], v[176:179], v[88:91]\n"
"v_mfma_f32_16x16x32_bf16 v[200:203], a[60:63], v[176:179], v[180:183]\n"
"v_mfma_f32_16x16x32_bf16 v[200:203], a[56:59], v[168:171], v[200:203]\n"
"v_mfma_f32_16x16x32_bf16 v[200:203], a[52:55], v[164:167], v[200:203]\n"
"v_mfma_f32_16x16x32_bf16 v[200:203], a[48:51], v[96:99], v[200:203]\n"
"v_mfma_f32_16x16x32_bf16 v[200:203], a[44:47], v[132:135], v[200:203]\n"
"v_mfma_f32_16x16x32_bf16 v[200:203], a[40:43], v[104:107], v[200:203]\n"
"s_nop 7\n"
"s_branch .Lmk_p89r\n"
".Lmk_f78s:\n"
";\n"
"s_mov_b32 s1, 0x10000\n"
".Lmk_f78l:\n"
"buffer_load_dwordx2 v[250:251], off, s[4:7], s98 sc1\n"
"s_waitcnt vmcnt(0)\n"
"v_cmp_ne_u64_e32 vcc, v[250:251], v[18:19]\n"
"s_cbranch_vccz .Lmk_f78r\n"
"s_sub_u32 s1, s1, 1\n"
"s_cbranch_scc0 .Lmk_f78l\n"
"s_branch .Lmk_f78r\n"
".Lmk_x99s:\n"
"s_nop 3\n"
"v_mov_b32_e32 v36, 0xff800000\n"
"v_mov_b32_e32 v37, 0xff800000\n"
"v_mov_b32_e32 v38, 0xff800000\n"
"v_mov_b32_e32 v39, 0xff800000\n"
"v_mov_b32_e32 v60, 0xff800000\n"
"v_mov_b32_e32 v61, 0xff800000\n"
"v_mov_b32_e32 v62, 0xff800000\n"
"v_mov_b32_e32 v63, 0xff800000\n"
"s_nop 1\n"
"s_branch .Lmk_x99r\n"
".Lmk_x100s:\n"
"s_nop 3\n"
"v_mov_b32_e32 v36, 0xff800000\n"
"v_mov_b32_e32 v37, 0xff800000\n"
"v_mov_b32_e32 v38, 0xff800000\n"
"v_mov_b32_e32 v39, 0xff800000\n"
"v_mov_b32_e32 v60, 0xff800000\n"
"v_mov_b32_e32 v61, 0xff800000\n"
"v_mov_b32_e32 v62, 0xff800000\n"
"v_mov_b32_e32 v63, 0xff800000\n"
"s_nop 1\n"
"s_branch .Lmk_x100r\n"
".Lmk_x101s:\n"
"s_nop 3\n"
"v_mov_b32_e32 v36, 0xff800000\n"
"v_mov_b32_e32 v37, 0xff800000\n"
"v_mov_b32_e32 v38, 0xff800000\n"
"v_mov_b32_e32 v39, 0xff800000\n"
"v_mov_b32_e32 v60, 0xff800000\n"
"v_mov_b32_e32 v61, 0xff800000\n"
"v_mov_b32_e32 v62, 0xff800000\n"
"v_mov_b32_e32 v63, 0xff800000\n"
"s_nop 1\n"
"s_branch .Lmk_x101r\n"
".Lmk_x102s:\n"
"s_nop 3\n"
"v_mov_b32_e32 v36, 0xff800000\n"
"v_mov_b32_e32 v37, 0xff800000\n"
"v_mov_b32_e32 v38, 0xff800000\n"
"v_mov_b32_e32 v39, 0xff800000\n"
"v_mov_b32_e32 v60, 0xff800000\n"
"v_mov_b32_e32 v61, 0xff800000\n"
"v_mov_b32_e32 v62, 0xff800000\n"
"v_mov_b32_e32 v63, 0xff800000\n"
"s_nop 1\n"
"s_branch .Lmk_x102r\n"
".Lmk_b97:\n"
"s_cmp_lg_u32 s2, 0\n"
"s_cbranch_scc1 .Lmk_k103\n"
"s_mov_b32 s2, 1\n"
"s_waitcnt vmcnt(0) lgkmcnt(0)\n"
"s_nop 7\n"
"s_mov_b32 s97, 11\n"
"v_add_u32_e32 v248, 0x3800, v15\n"
"v_add_u32_e32 v247, 0x3800, v254\n"
"ds_read_b128 v[36:39], v15 offset:24576\n"
"ds_read_b128 v[40:43], v254 offset:24832\n"
"s_waitcnt lgkmcnt(0)\n"
"v_mfma_f32_16x16x32_bf16 v[48:51], v[36:39], v[28:31], 0\n"
"v_mfma_f32_16x16x32_bf16 v[52:55], v[36:39], v[32:35], 0\n"
"v_mfma_f32_16x16x32_bf16 v[36:39], v[40:43], v[32:35], 0\n"
"v_mfma_f32_16x16x32_bf16 v[56:59], v[40:43], v[28:31], 0\n"
".Lmk_b97l:\n"
"ds_read_b128 v[40:43], v248 offset:8192\n"
"ds_read_b128 v[60:63], v247 offset:8448\n"
"s_cmp_gt_i32 s78, s97\n"
"s_cselect_b32 s96, 0xff800000, 0\n"
"v_mov_b32_e32 v64, s96\n"
"v_mov_b32_e32 v65, s96\n"
"v_mov_b32_e32 v66, s96\n"
"v_mov_b32_e32 v67, s96\n"
"v_subrev_u32_e32 v248, 0x800, v248\n"
"v_subrev_u32_e32 v247, 0x800, v247\n"
"s_waitcnt lgkmcnt(0)\n"
"v_mfma_f32_16x16x32_bf16 v[72:75], v[40:43], v[28:31], v[64:67]\n"
"v_mfma_f32_16x16x32_bf16 v[76:79], v[40:43], v[32:35], v[64:67]\n"
"v_mfma_f32_16x16x32_bf16 v[40:43], v[60:63], v[32:35], v[64:67]\n"
"v_mfma_f32_16x16x32_bf16 v[84:87], v[60:63], v[28:31], v[64:67]\n"
"s_nop 7\n"
"s_cmp_lg_u32 s97, 4\n"
"s_cbranch_scc1 .Lmk_b97m\n"
"v_cndmask_b32_e64 v72, v48, v72, s[20:21]\n"
"v_cndmask_b32_e64 v73, v49, v73, s[22:23]\n"
"v_cndmask_b32_e64 v74, v50, v74, s[24:25]\n"
"v_cndmask_b32_e64 v75, v51, v75, s[26:27]\n"
"v_cndmask_b32_e64 v84, v56, v84, s[28:29]\n"
"v_cndmask_b32_e64 v85, v57, v85, s[30:31]\n"
"v_cndmask_b32_e64 v86, v58, v86, s[32:33]\n"
"v_cndmask_b32_e64 v87, v59, v87, s[34:35]\n"
"v_cndmask_b32_e64 v76, v52, v76, s[36:37]\n"
"v_cndmask_b32_e64 v77, v53, v77, s[38:39]\n"
"v_cndmask_b32_e64 v78, v54, v78, s[40:41]\n"
"v_cndmask_b32_e64 v79, v55, v79, s[42:43]\n"
"v_cndmask_b32_e64 v40, v36, v40, s[44:45]\n"
"v_cndmask_b32_e64 v41, v37, v41, s[46:47]\n"
"v_cndmask_b32_e64 v42, v38, v42, s[48:49]\n"
"v_cndmask_b32_e64 v43, v39, v43, s[50:51]\n"
".Lmk_b97m:\n"
"v_max3_f32 v253, v253, v72, v73\n"
"v_max3_f32 v251, v251, v76, v77\n"
"v_max3_f32 v253, v253, v74, v75\n"
"v_max3_f32 v251, v251, v78, v79\n"
"v_max3_f32 v253, v253, v84, v85\n"
"v_max3_f32 v251, v251, v40, v41\n"
"v_max3_f32 v253, v253, v86, v87\n"
"v_max3_f32 v251, v251, v42, v43\n"
"s_sub_u32 s97, s97, 1\n"
"s_cmp_ge_i32 s97, 4\n"
"s_cbranch_scc1 .Lmk_b97l\n"
"v_mov_b32_e32 v248, v253\n"
"v_mov_b32_e32 v247, v251\n"
"s_nop 0\n"
"v_permlane16_swap_b32_e32 v253, v248\n"
"v_permlane16_swap_b32_e32 v251, v247\n"
"v_max_f32_e32 v253, v253, v248\n"
"v_max_f32_e32 v251, v251, v247\n"
"v_mov_b32_e32 v248, v253\n"
"v_mov_b32_e32 v247, v251\n"
"s_nop 0\n"
"v_permlane32_swap_b32_e32 v253, v248\n"
"v_permlane32_swap_b32_e32 v251, v247\n"
"v_max_f32_e32 v253, v253, v248\n"
"v_max_f32_e32 v251, v251, v247\n"
"v_mov_b32_e32 v250, v253\n"
"v_mov_b32_e32 v249, v251\n"
"s_branch .Lmk_a96\n"
".Lmk_x106s:\n"
"s_nop 3\n"
"v_mov_b32_e32 v116, 0xff800000\n"
"v_mov_b32_e32 v117, 0xff800000\n"
"v_mov_b32_e32 v118, 0xff800000\n"
"v_mov_b32_e32 v119, 0xff800000\n"
"v_mov_b32_e32 v120, 0xff800000\n"
"v_mov_b32_e32 v121, 0xff800000\n"
"v_mov_b32_e32 v122, 0xff800000\n"
"v_mov_b32_e32 v123, 0xff800000\n"
"s_nop 1\n"
"s_branch .Lmk_x106r\n"
".Lmk_x107s:\n"
"s_nop 3\n"
"v_mov_b32_e32 v116, 0xff800000\n"
"v_mov_b32_e32 v117, 0xff800000\n"
"v_mov_b32_e32 v118, 0xff800000\n"
"v_mov_b32_e32 v119, 0xff800000\n"
"v_mov_b32_e32 v120, 0xff800000\n"
"v_mov_b32_e32 v121, 0xff800000\n"
"v_mov_b32_e32 v122, 0xff800000\n"
"v_mov_b32_e32 v123, 0xff800000\n"
"s_nop 1\n"
"s_branch .Lmk_x107r\n"
".Lmk_x108s:\n"
"s_nop 3\n"
"v_mov_b32_e32 v116, 0xff800000\n"
"v_mov_b32_e32 v117, 0xff800000\n"
"v_mov_b32_e32 v118, 0xff800000\n"
"v_mov_b32_e32 v119, 0xff800000\n"
"v_mov_b32_e32 v120, 0xff800000\n"
"v_mov_b32_e32 v121, 0xff800000\n"
"v_mov_b32_e32 v122, 0xff800000\n"
"v_mov_b32_e32 v123, 0xff800000\n"
"s_nop 1\n"
"s_branch .Lmk_x108r\n"
".Lmk_h95s:\n"
";\n"
"s_mov_b32 s1, 0x10000\n"
".Lmk_h95l:\n"
"s_add_u32 m0, s77, 0x4000\n"
"s_add_u32 s95, s65, 0\n"
"buffer_load_dwordx4 v2, s[4:7], s95 offen sc1 lds\n"
"buffer_load_dwordx4 v2, s[4:7], s95 offen offset:1024 sc1 lds\n"
"s_add_u32 m0, s77, 0xc000\n"
"s_add_u32 s95, s65, 0x4000\n"
"buffer_load_dwordx4 v2, s[4:7], s95 offen sc1 lds\n"
"buffer_load_dwordx4 v2, s[4:7], s95 offen offset:1024 sc1 lds\n"
"s_waitcnt vmcnt(0)\n"
"ds_read_b128 v[164:167], v2 offset:16384\n"
"ds_read_b128 v[76:79], v2 offset:17408\n"
"ds_read_b128 v[72:75], v2 offset:49152\n"
"ds_read_b128 v[84:87], v2 offset:50176\n"
"s_waitcnt lgkmcnt(0)\n"
"s_waitcnt vmcnt(0)\n"
"v_max_u32_e32 v248, v164, v165\n"
"v_max3_u32 v248, v248, v166, v167\n"
"v_max3_u32 v248, v248, v76, v77\n"
"v_max3_u32 v248, v248, v78, v79\n"
"v_max3_u32 v248, v248, v72, v73\n"
"v_max3_u32 v248, v248, v74, v75\n"
"v_max3_u32 v248, v248, v84, v85\n"
"v_max3_u32 v248, v248, v86, v87\n"
"v_cmp_eq_u32_e32 vcc, -1, v248\n"
"s_cbranch_vccz .Lmk_h95k\n"
"s_sub_u32 s1, s1, 1\n"
"s_cbranch_scc0 .Lmk_h95l\n"
".Lmk_h95k:\n"
"s_branch .Lmk_h95r\n"
".Lmk_f79s:\n"
";\n"
"s_mov_b32 s1, 0x10000\n"
".Lmk_f79l:\n"
"buffer_load_dwordx2 v[246:247], v12, s[4:7], s96 offen sc1\n"
"s_waitcnt vmcnt(0)\n"
"v_cmp_ne_u64_e32 vcc, v[246:247], v[18:19]\n"
"s_cbranch_vccz .Lmk_f79r\n"
"s_sub_u32 s1, s1, 1\n"
"s_cbranch_scc0 .Lmk_f79l\n"
"s_branch .Lmk_f79r\n"
".Lmk_b105:\n"
"s_cmp_lg_u32 s2, 0\n"
"s_cbranch_scc1 .Lmk_k110\n"
"s_mov_b32 s2, 1\n"
"s_waitcnt vmcnt(0) lgkmcnt(0)\n"
"s_nop 7\n"
"s_mov_b32 s96, 7\n"
"v_add_u32_e32 v248, 0x3800, v15\n"
"v_add_u32_e32 v247, 0x3800, v254\n"
"ds_read_b128 v[188:191], v15 offset:16384\n"
"ds_read_b128 v[192:195], v254 offset:16640\n"
"s_waitcnt lgkmcnt(0)\n"
"v_mfma_f32_16x16x32_bf16 v[196:199], v[188:191], v[92:95], 0\n"
"v_mfma_f32_16x16x32_bf16 v[212:215], v[188:191], v[96:99], 0\n"
"v_mfma_f32_16x16x32_bf16 v[188:191], v[192:195], v[96:99], 0\n"
"v_mfma_f32_16x16x32_bf16 v[220:223], v[192:195], v[92:95], 0\n"
".Lmk_b105l:\n"
"ds_read_b128 v[192:195], v248\n"
"ds_read_b128 v[224:227], v247 offset:256\n"
"s_cmp_gt_i32 s78, s96\n"
"s_cselect_b32 s95, 0xff800000, 0\n"
"v_mov_b32_e32 v228, s95\n"
"v_mov_b32_e32 v229, s95\n"
"v_mov_b32_e32 v230, s95\n"
"v_mov_b32_e32 v231, s95\n"
"v_subrev_u32_e32 v248, 0x800, v248\n"
"v_subrev_u32_e32 v247, 0x800, v247\n"
"s_waitcnt lgkmcnt(0)\n"
"v_mfma_f32_16x16x32_bf16 v[232:235], v[192:195], v[92:95], v[228:231]\n"
"v_mfma_f32_16x16x32_bf16 v[236:239], v[192:195], v[96:99], v[228:231]\n"
"v_mfma_f32_16x16x32_bf16 v[192:195], v[224:227], v[96:99], v[228:231]\n"
"v_mfma_f32_16x16x32_bf16 v[240:243], v[224:227], v[92:95], v[228:231]\n"
"s_nop 7\n"
"s_cmp_lg_u32 s96, 0\n"
"s_cbranch_scc1 .Lmk_b105m\n"
"v_cndmask_b32_e64 v232, v196, v232, s[20:21]\n"
"v_cndmask_b32_e64 v233, v197, v233, s[22:23]\n"
"v_cndmask_b32_e64 v234, v198, v234, s[24:25]\n"
"v_cndmask_b32_e64 v235, v199, v235, s[26:27]\n"
"v_cndmask_b32_e64 v240, v220, v240, s[28:29]\n"
"v_cndmask_b32_e64 v241, v221, v241, s[30:31]\n"
"v_cndmask_b32_e64 v242, v222, v242, s[32:33]\n"
"v_cndmask_b32_e64 v243, v223, v243, s[34:35]\n"
"v_cndmask_b32_e64 v236, v212, v236, s[36:37]\n"
"v_cndmask_b32_e64 v237, v213, v237, s[38:39]\n"
"v_cndmask_b32_e64 v238, v214, v238, s[40:41]\n"
"v_cndmask_b32_e64 v239, v215, v239, s[42:43]\n"
"v_cndmask_b32_e64 v192, v188, v192, s[44:45]\n"
"v_cndmask_b32_e64 v193, v189, v193, s[46:47]\n"
"v_cndmask_b32_e64 v194, v190, v194, s[48:49]\n"
"v_cndmask_b32_e64 v195, v191, v195, s[50:51]\n"
".Lmk_b105m:\n"
"v_max3_f32 v253, v253, v232, v233\n"
"v_max3_f32 v251, v251, v236, v237\n"
"v_max3_f32 v253, v253, v234, v235\n"
"v_max3_f32 v251, v251, v238, v239\n"
"v_max3_f32 v253, v253, v240, v241\n"
"v_max3_f32 v251, v251, v192, v193\n"
"v_max3_f32 v253, v253, v242, v243\n"
"v_max3_f32 v251, v251, v194, v195\n"
"s_sub_u32 s96, s96, 1\n"
"s_cmp_ge_i32 s96, 0\n"
"s_cbranch_scc1 .Lmk_b105l\n"
"v_mov_b32_e32 v248, v253\n"
"v_mov_b32_e32 v247, v251\n"
"s_nop 0\n"
"v_permlane16_swap_b32_e32 v253, v248\n"
"v_permlane16_swap_b32_e32 v251, v247\n"
"v_max_f32_e32 v253, v253, v248\n"
"v_max_f32_e32 v251, v251, v247\n"
"v_mov_b32_e32 v248, v253\n"
"v_mov_b32_e32 v247, v251\n"
"s_nop 0\n"
"v_permlane32_swap_b32_e32 v253, v248\n"
"v_permlane32_swap_b32_e32 v251, v247\n"
"v_max_f32_e32 v253, v253, v248\n"
"v_max_f32_e32 v251, v251, v247\n"
"v_mov_b32_e32 v250, v253\n"
"v_mov_b32_e32 v249, v251\n"
"s_branch .Lmk_a104\n"
".Lmk_p111s:\n"
";\n"
"s_mov_b32 s1, 0x10000\n"
".Lmk_p111l:\n"
"s_add_u32 s98, s56, 0x40000\n"
"buffer_load_dwordx4 v[152:155], v0, s[4:7], s98 offen sc1\n"
"buffer_load_dwordx4 v[200:203], v0, s[4:7], s98 offen offset:1024 sc1\n"
"buffer_load_dwordx4 v[184:187], v0, s[4:7], s98 offen offset:2048 sc1\n"
"buffer_load_dwordx4 v[204:207], v0, s[4:7], s98 offen offset:3072 sc1\n"
"buffer_load_dwordx4 v[140:143], v1, s[4:7], s98 offen sc1\n"
"buffer_load_dwordx4 v[208:211], v1, s[4:7], s98 offen offset:1024 sc1\n"
"buffer_load_dwordx4 v[120:123], v1, s[4:7], s98 offen offset:2048 sc1\n"
"buffer_load_dwordx4 v[136:139], v1, s[4:7], s98 offen offset:3072 sc1\n"
"s_add_u32 s98, s57, 0x40000\n"
"buffer_load_dwordx4 v[216:219], v0, s[4:7], s98 offen sc1\n"
"buffer_load_dwordx4 v[132:135], v0, s[4:7], s98 offen offset:1024 sc1\n"
"buffer_load_dwordx4 v[160:163], v0, s[4:7], s98 offen offset:2048 sc1\n"
"buffer_load_dwordx4 v[100:103], v0, s[4:7], s98 offen offset:3072 sc1\n"
"buffer_load_dwordx4 v[164:167], v1, s[4:7], s98 offen sc1\n"
"buffer_load_dwordx4 v[104:107], v1, s[4:7], s98 offen offset:1024 sc1\n"
"buffer_load_dwordx4 v[108:111], v1, s[4:7], s98 offen offset:2048 sc1\n"
"buffer_load_dwordx4 v[128:131], v1, s[4:7], s98 offen offset:3072 sc1\n"
"s_add_u32 s98, s58, 0x40000\n"
"buffer_load_dwordx4 v[148:151], v0, s[4:7], s98 offen sc1\n"
"buffer_load_dwordx4 v[144:147], v0, s[4:7], s98 offen offset:1024 sc1\n"
"buffer_load_dwordx4 v[116:119], v0, s[4:7], s98 offen offset:2048 sc1\n"
"buffer_load_dwordx4 v[112:115], v0, s[4:7], s98 offen offset:3072 sc1\n"
"buffer_load_dwordx4 v[92:95], v1, s[4:7], s98 offen sc1\n"
"buffer_load_dwordx4 v[96:99], v1, s[4:7], s98 offen offset:1024 sc1\n"
"buffer_load_dwordx4 v[156:159], v1, s[4:7], s98 offen offset:2048 sc1\n"
"s_waitcnt vmcnt(0)\n"
"v_max_u32_e32 v255, v152, v153\n"
"v_max3_u32 v255, v255, v154, v155\n"
"v_max3_u32 v255, v255, v200, v201\n"
"v_max3_u32 v255, v255, v202, v203\n"
"v_max3_u32 v255, v255, v184, v185\n"
"v_max3_u32 v255, v255, v186, v187\n"
"v_max3_u32 v255, v255, v204, v205\n"
"v_max3_u32 v255, v255, v206, v207\n"
"v_max3_u32 v255, v255, v140, v141\n"
"v_max3_u32 v255, v255, v142, v143\n"
"v_max3_u32 v255, v255, v208, v209\n"
"v_max3_u32 v255, v255, v210, v211\n"
"v_max3_u32 v255, v255, v120, v121\n"
"v_max3_u32 v255, v255, v122, v123\n"
"v_max3_u32 v255, v255, v136, v137\n"
"v_max3_u32 v255, v255, v138, v139\n"
"v_cmp_eq_u32_e32 vcc, -1, v255\n"
"s_cbranch_vccz .Lmk_p111k\n"
"s_sub_u32 s1, s1, 1\n"
"s_cbranch_scc0 .Lmk_p111l\n"
".Lmk_p111k:\n"
"v_mfma_f32_16x16x32_bf16 v[88:91], a[128:131], v[152:155], 0\n"
"v_mfma_f32_16x16x32_bf16 v[88:91], a[132:135], v[200:203], v[88:91]\n"
"v_mfma_f32_16x16x32_bf16 v[88:91], a[136:139], v[184:187], v[88:91]\n"
"v_mfma_f32_16x16x32_bf16 v[88:91], a[140:143], v[204:207], v[88:91]\n"
"v_mfma_f32_16x16x32_bf16 v[88:91], a[144:147], v[140:143], v[88:91]\n"
"v_mfma_f32_16x16x32_bf16 v[88:91], a[148:151], v[208:211], v[88:91]\n"
"v_mfma_f32_16x16x32_bf16 v[88:91], a[152:155], v[120:123], v[88:91]\n"
"v_mfma_f32_16x16x32_bf16 v[88:91], a[156:159], v[136:139], v[88:91]\n"
"v_mfma_f32_16x16x32_bf16 v[124:127], a[188:191], v[136:139], 0\n"
"v_mfma_f32_16x16x32_bf16 v[124:127], a[184:187], v[120:123], v[124:127]\n"
"v_mfma_f32_16x16x32_bf16 v[124:127], a[180:183], v[208:211], v[124:127]\n"
"v_mfma_f32_16x16x32_bf16 v[124:127], a[176:179], v[140:143], v[124:127]\n"
"v_mfma_f32_16x16x32_bf16 v[124:127], a[172:175], v[204:207], v[124:127]\n"
"v_mfma_f32_16x16x32_bf16 v[124:127], a[168:171], v[184:187], v[124:127]\n"
"s_nop 7\n"
"s_branch .Lmk_p111r\n"
".Lmk_p112s:\n"
";\n"
"s_mov_b32 s1, 0x10000\n"
".Lmk_p112l:\n"
"s_add_u32 s97, s57, 0x40000\n"
"buffer_load_dwordx4 v[216:219], v0, s[4:7], s97 offen sc1\n"
"buffer_load_dwordx4 v[132:135], v0, s[4:7], s97 offen offset:1024 sc1\n"
"buffer_load_dwordx4 v[160:163], v0, s[4:7], s97 offen offset:2048 sc1\n"
"buffer_load_dwordx4 v[100:103], v0, s[4:7], s97 offen offset:3072 sc1\n"
"buffer_load_dwordx4 v[164:167], v1, s[4:7], s97 offen sc1\n"
"buffer_load_dwordx4 v[104:107], v1, s[4:7], s97 offen offset:1024 sc1\n"
"buffer_load_dwordx4 v[108:111], v1, s[4:7], s97 offen offset:2048 sc1\n"
"buffer_load_dwordx4 v[128:131], v1, s[4:7], s97 offen offset:3072 sc1\n"
"s_add_u32 s97, s58, 0x40000\n"
"buffer_load_dwordx4 v[148:151], v0, s[4:7], s97 offen sc1\n"
"buffer_load_dwordx4 v[144:147], v0, s[4:7], s97 offen offset:1024 sc1\n"
"buffer_load_dwordx4 v[116:119], v0, s[4:7], s97 offen offset:2048 sc1\n"
"buffer_load_dwordx4 v[112:115], v0, s[4:7], s97 offen offset:3072 sc1\n"
"buffer_load_dwordx4 v[92:95], v1, s[4:7], s97 offen sc1\n"
"buffer_load_dwordx4 v[96:99], v1, s[4:7], s97 offen offset:1024 sc1\n"
"buffer_load_dwordx4 v[156:159], v1, s[4:7], s97 offen offset:2048 sc1\n"
"buffer_load_dwordx4 v[88:91], v1, s[4:7], s97 offen offset:3072 sc1\n"
"s_add_u32 s97, s59, 0x40000\n"
"buffer_load_dwordx4 v[172:175], v0, s[4:7], s97 offen sc1\n"
"buffer_load_dwordx4 v[124:127], v0, s[4:7], s97 offen offset:1024 sc1\n"
"buffer_load_dwordx4 v[120:123], v0, s[4:7], s97 offen offset:2048 sc1\n"
"buffer_load_dwordx4 v[136:139], v0, s[4:7], s97 offen offset:3072 sc1\n"
"buffer_load_dwordx4 v[152:155], v1, s[4:7], s97 offen sc1\n"
"buffer_load_dwordx4 v[168:171], v1, s[4:7], s97 offen offset:1024 sc1\n"
"buffer_load_dwordx4 v[180:183], v1, s[4:7], s97 offen offset:2048 sc1\n"
"s_waitcnt vmcnt(0)\n"
"v_max_u32_e32 v255, v216, v217\n"
"v_max3_u32 v255, v255, v218, v219\n"
"v_max3_u32 v255, v255, v132, v133\n"
"v_max3_u32 v255, v255, v134, v135\n"
"v_max3_u32 v255, v255, v160, v161\n"
"v_max3_u32 v255, v255, v162, v163\n"
"v_max3_u32 v255, v255, v100, v101\n"
"v_max3_u32 v255, v255, v102, v103\n"
"v_max3_u32 v255, v255, v164, v165\n"
"v_max3_u32 v255, v255, v166, v167\n"
"v_max3_u32 v255, v255, v104, v105\n"
"v_max3_u32 v255, v255, v106, v107\n"
"v_max3_u32 v255, v255, v108, v109\n"
"v_max3_u32 v255, v255, v110, v111\n"
"v_max3_u32 v255, v255, v128, v129\n"
"v_max3_u32 v255, v255, v130, v131\n"
"v_cmp_eq_u32_e32 vcc, -1, v255\n"
"s_cbranch_vccz .Lmk_p112k\n"
"s_sub_u32 s1, s1, 1\n"
"s_cbranch_scc0 .Lmk_p112l\n"
".Lmk_p112k:\n"
"v_mfma_f32_16x16x32_bf16 v[140:143], a[128:131], v[216:219], 0\n"
"v_mfma_f32_16x16x32_bf16 v[140:143], a[132:135], v[132:135], v[140:143]\n"
"v_mfma_f32_16x16x32_bf16 v[140:143], a[136:139], v[160:163], v[140:143]\n"
"v_mfma_f32_16x16x32_bf16 v[140:143], a[140:143], v[100:103], v[140:143]\n"
"v_mfma_f32_16x16x32_bf16 v[140:143], a[144:147], v[164:167], v[140:143]\n"
"v_mfma_f32_16x16x32_bf16 v[140:143], a[148:151], v[104:107], v[140:143]\n"
"v_mfma_f32_16x16x32_bf16 v[140:143], a[152:155], v[108:111], v[140:143]\n"
"v_mfma_f32_16x16x32_bf16 v[140:143], a[156:159], v[128:131], v[140:143]\n"
"v_mfma_f32_16x16x32_bf16 v[176:179], a[188:191], v[128:131], 0\n"
"v_mfma_f32_16x16x32_bf16 v[176:179], a[184:187], v[108:111], v[176:179]\n"
"v_mfma_f32_16x16x32_bf16 v[176:179], a[180:183], v[104:107], v[176:179]\n"
"v_mfma_f32_16x16x32_bf16 v[176:179], a[176:179], v[164:167], v[176:179]\n"
"v_mfma_f32_16x16x32_bf16 v[176:179], a[172:175], v[100:103], v[176:179]\n"
"v_mfma_f32_16x16x32_bf16 v[176:179], a[168:171], v[160:163], v[176:179]\n"
"s_nop 7\n"
"s_branch .Lmk_p112r\n"
".Lmk_p113s:\n"
";\n"
"s_mov_b32 s1, 0x10000\n"
".Lmk_p113l:\n"
"s_add_u32 s97, s58, 0x40000\n"
"buffer_load_dwordx4 v[148:151], v0, s[4:7], s97 offen sc1\n"
"buffer_load_dwordx4 v[144:147], v0, s[4:7], s97 offen offset:1024 sc1\n"
"buffer_load_dwordx4 v[116:119], v0, s[4:7], s97 offen offset:2048 sc1\n"
"buffer_load_dwordx4 v[112:115], v0, s[4:7], s97 offen offset:3072 sc1\n"
"buffer_load_dwordx4 v[92:95], v1, s[4:7], s97 offen sc1\n"
"buffer_load_dwordx4 v[96:99], v1, s[4:7], s97 offen offset:1024 sc1\n"
"buffer_load_dwordx4 v[156:159], v1, s[4:7], s97 offen offset:2048 sc1\n"
"buffer_load_dwordx4 v[88:91], v1, s[4:7], s97 offen offset:3072 sc1\n"
"s_add_u32 s97, s59, 0x40000\n"
"buffer_load_dwordx4 v[172:175], v0, s[4:7], s97 offen sc1\n"
"buffer_load_dwordx4 v[124:127], v0, s[4:7], s97 offen offset:1024 sc1\n"
"buffer_load_dwordx4 v[120:123], v0, s[4:7], s97 offen offset:2048 sc1\n"
"buffer_load_dwordx4 v[136:139], v0, s[4:7], s97 offen offset:3072 sc1\n"
"buffer_load_dwordx4 v[152:155], v1, s[4:7], s97 offen sc1\n"
"buffer_load_dwordx4 v[168:171], v1, s[4:7], s97 offen offset:1024 sc1\n"
"buffer_load_dwordx4 v[180:183], v1, s[4:7], s97 offen offset:2048 sc1\n"
"buffer_load_dwordx4 v[140:143], v1, s[4:7], s97 offen offset:3072 sc1\n"
"s_waitcnt vmcnt(0)\n"
"v_max_u32_e32 v239, v148, v149\n"
"v_max3_u32 v239, v239, v150, v151\n"
"v_max3_u32 v239, v239, v144, v145\n"
"v_max3_u32 v239, v239, v146, v147\n"
"v_max3_u32 v239, v239, v116, v117\n"
"v_max3_u32 v239, v239, v118, v119\n"
"v_max3_u32 v239, v239, v112, v113\n"
"v_max3_u32 v239, v239, v114, v115\n"
"v_max3_u32 v239, v239, v92, v93\n"
"v_max3_u32 v239, v239, v94, v95\n"
"v_max3_u32 v239, v239, v96, v97\n"
"v_max3_u32 v239, v239, v98, v99\n"
"v_max3_u32 v239, v239, v156, v157\n"
"v_max3_u32 v239, v239, v158, v159\n"
"v_max3_u32 v239, v239, v88, v89\n"
"v_max3_u32 v239, v239, v90, v91\n"
"v_cmp_eq_u32_e32 vcc, -1, v239\n"
"s_cbranch_vccz .Lmk_p113k\n"
"s_sub_u32 s1, s1, 1\n"
"s_cbranch_scc0 .Lmk_p113l\n"
".Lmk_p113k:\n"
"v_mfma_f32_16x16x32_bf16 v[100:103], a[128:131], v[148:151], 0\n"
"v_mfma_f32_16x16x32_bf16 v[100:103], a[132:135], v[144:147], v[100:103]\n"
"v_mfma_f32_16x16x32_bf16 v[100:103], a[136:139], v[116:119], v[100:103]\n"
"v_mfma_f32_16x16x32_bf16 v[100:103], a[140:143], v[112:115], v[100:103]\n"
"v_mfma_f32_16x16x32_bf16 v[100:103], a[144:147], v[92:95], v[100:103]\n"
"v_mfma_f32_16x16x32_bf16 v[100:103], a[148:151], v[96:99], v[100:103]\n"
"v_mfma_f32_16x16x32_bf16 v[100:103], a[152:155], v[156:159], v[100:103]\n"
"v_mfma_f32_16x16x32_bf16 v[100:103], a[156:159], v[88:91], v[100:103]\n"
"v_mfma_f32_16x16x32_bf16 v[104:107], a[188:191], v[88:91], 0\n"
"v_mfma_f32_16x16x32_bf16 v[104:107], a[184:187], v[156:159], v[104:107]\n"
"v_mfma_f32_16x16x32_bf16 v[104:107], a[180:183], v[96:99], v[104:107]\n"
"v_mfma_f32_16x16x32_bf16 v[104:107], a[176:179], v[92:95], v[104:107]\n"
"v_mfma_f32_16x16x32_bf16 v[104:107], a[172:175], v[112:115], v[104:107]\n"
"v_mfma_f32_16x16x32_bf16 v[104:107], a[168:171], v[116:119], v[104:107]\n"
"s_nop 7\n"
"s_branch .Lmk_p113r\n"
".Lmk_p114s:\n"
";\n"
"s_mov_b32 s1, 0x10000\n"
".Lmk_p114l:\n"
"s_add_u32 s97, s59, 0x40000\n"
"buffer_load_dwordx4 v[172:175], v0, s[4:7], s97 offen sc1\n"
"buffer_load_dwordx4 v[124:127], v0, s[4:7], s97 offen offset:1024 sc1\n"
"buffer_load_dwordx4 v[120:123], v0, s[4:7], s97 offen offset:2048 sc1\n"
"buffer_load_dwordx4 v[136:139], v0, s[4:7], s97 offen offset:3072 sc1\n"
"buffer_load_dwordx4 v[152:155], v1, s[4:7], s97 offen sc1\n"
"buffer_load_dwordx4 v[168:171], v1, s[4:7], s97 offen offset:1024 sc1\n"
"buffer_load_dwordx4 v[180:183], v1, s[4:7], s97 offen offset:2048 sc1\n"
"buffer_load_dwordx4 v[140:143], v1, s[4:7], s97 offen offset:3072 sc1\n"
"s_add_u32 s97, s56, 0\n"
"buffer_load_dwordx4 v[100:103], v0, s[4:7], s97 offen sc1\n"
"buffer_load_dwordx4 v[176:179], v0, s[4:7], s97 offen offset:1024 sc1\n"
"buffer_load_dwordx4 v[104:107], v0, s[4:7], s97 offen offset:2048 sc1\n"
"buffer_load_dwordx4 v[88:91], v0, s[4:7], s97 offen offset:3072 sc1\n"
"buffer_load_dwordx4 v[92:95], v1, s[4:7], s97 offen sc1\n"
"buffer_load_dwordx4 v[116:119], v1, s[4:7], s97 offen offset:1024 sc1\n"
"buffer_load_dwordx4 v[132:135], v1, s[4:7], s97 offen offset:2048 sc1\n"
"buffer_load_dwordx4 v[144:147], v1, s[4:7], s97 offen offset:3072 sc1\n"
"s_add_u32 s97, s57, 0\n"
"buffer_load_dwordx4 v[156:159], v0, s[4:7], s97 offen sc1\n"
"s_waitcnt vmcnt(0)\n"
"v_max_u32_e32 v255, v172, v173\n"
"v_max3_u32 v255, v255, v174, v175\n"
"v_max3_u32 v255, v255, v124, v125\n"
"v_max3_u32 v255, v255, v126, v127\n"
"v_max3_u32 v255, v255, v120, v121\n"
"v_max3_u32 v255, v255, v122, v123\n"
"v_max3_u32 v255, v255, v136, v137\n"
"v_max3_u32 v255, v255, v138, v139\n"
"v_max3_u32 v255, v255, v152, v153\n"
"v_max3_u32 v255, v255, v154, v155\n"
"v_max3_u32 v255, v255, v168, v169\n"
"v_max3_u32 v255, v255, v170, v171\n"
"v_max3_u32 v255, v255, v180, v181\n"
"v_max3_u32 v255, v255, v182, v183\n"
"v_max3_u32 v255, v255, v140, v141\n"
"v_max3_u32 v255, v255, v142, v143\n"
"v_cmp_eq_u32_e32 vcc, -1, v255\n"
"s_cbranch_vccz .Lmk_p114k\n"
"s_sub_u32 s1, s1, 1\n"
"s_cbranch_scc0 .Lmk_p114l\n"
".Lmk_p114k:\n"
"v_mfma_f32_16x16x32_bf16 v[128:131], a[128:131], v[172:175], v[108:111]\n"
"v_mfma_f32_16x16x32_bf16 v[128:131], a[132:135], v[124:127], v[128:131]\n"
"v_mfma_f32_16x16x32_bf16 v[128:131], a[136:139], v[120:123], v[128:131]\n"
"v_mfma_f32_16x16x32_bf16 v[128:131], a[140:143], v[136:139], v[128:131]\n"
"v_mfma_f32_16x16x32_bf16 v[128:131], a[144:147], v[152:155], v[128:131]\n"
"v_mfma_f32_16x16x32_bf16 v[128:131], a[148:151], v[168:171], v[128:131]\n"
"v_mfma_f32_16x16x32_bf16 v[128:131], a[152:155], v[180:183], v[128:131]\n"
"v_mfma_f32_16x16x32_bf16 v[128:131], a[156:159], v[140:143], v[128:131]\n"
"v_mfma_f32_16x16x32_bf16 v[148:151], a[188:191], v[140:143], v[160:163]\n"
"v_mfma_f32_16x16x32_bf16 v[148:151], a[184:187], v[180:183], v[148:151]\n"
"v_mfma_f32_16x16x32_bf16 v[148:151], a[180:183], v[168:171], v[148:151]\n"
"v_mfma_f32_16x16x32_bf16 v[148:151], a[176:179], v[152:155], v[148:151]\n"
"v_mfma_f32_16x16x32_bf16 v[148:151], a[172:175], v[136:139], v[148:151]\n"
"v_mfma_f32_16x16x32_bf16 v[148:151], a[168:171], v[120:123], v[148:151]\n"
"s_nop 7\n"
"s_branch .Lmk_p114r\n"
".Lmk_p115s:\n"
";\n"
"s_mov_b32 s1, 0x10000\n"
".Lmk_p115l:\n"
"s_add_u32 s97, s56, 0\n"
"buffer_load_dwordx4 v[100:103], v0, s[4:7], s97 offen sc1\n"
"buffer_load_dwordx4 v[176:179], v0, s[4:7], s97 offen offset:1024 sc1\n"
"buffer_load_dwordx4 v[104:107], v0, s[4:7], s97 offen offset:2048 sc1\n"
"buffer_load_dwordx4 v[88:91], v0, s[4:7], s97 offen offset:3072 sc1\n"
"buffer_load_dwordx4 v[92:95], v1, s[4:7], s97 offen sc1\n"
"buffer_load_dwordx4 v[116:119], v1, s[4:7], s97 offen offset:1024 sc1\n"
"buffer_load_dwordx4 v[132:135], v1, s[4:7], s97 offen offset:2048 sc1\n"
"buffer_load_dwordx4 v[144:147], v1, s[4:7], s97 offen offset:3072 sc1\n"
"s_add_u32 s97, s57, 0\n"
"buffer_load_dwordx4 v[156:159], v0, s[4:7], s97 offen sc1\n"
"buffer_load_dwordx4 v[108:111], v0, s[4:7], s97 offen offset:1024 sc1\n"
"buffer_load_dwordx4 v[112:115], v0, s[4:7], s97 offen offset:2048 sc1\n"
"buffer_load_dwordx4 v[196:199], v0, s[4:7], s97 offen offset:3072 sc1\n"
"buffer_load_dwordx4 v[200:203], v1, s[4:7], s97 offen sc1\n"
"buffer_load_dwordx4 v[128:131], v1, s[4:7], s97 offen offset:1024 sc1\n"
"buffer_load_dwordx4 v[120:123], v1, s[4:7], s97 offen offset:2048 sc1\n"
"buffer_load_dwordx4 v[136:139], v1, s[4:7], s97 offen offset:3072 sc1\n"
"s_add_u32 s97, s58, 0\n"
"buffer_load_dwordx4 v[124:127], v0, s[4:7], s97 offen sc1\n"
"buffer_load_dwordx4 v[140:143], v0, s[4:7], s97 offen offset:1024 sc1\n"
"buffer_load_dwordx4 v[152:155], v0, s[4:7], s97 offen offset:2048 sc1\n"
"s_waitcnt vmcnt(0)\n"
"v_max_u32_e32 v255, v100, v101\n"
"v_max3_u32 v255, v255, v102, v103\n"
"v_max3_u32 v255, v255, v176, v177\n"
"v_max3_u32 v255, v255, v178, v179\n"
"v_max3_u32 v255, v255, v104, v105\n"
"v_max3_u32 v255, v255, v106, v107\n"
"v_max3_u32 v255, v255, v88, v89\n"
"v_max3_u32 v255, v255, v90, v91\n"
"v_max3_u32 v255, v255, v92, v93\n"
"v_max3_u32 v255, v255, v94, v95\n"
"v_max3_u32 v255, v255, v116, v117\n"
"v_max3_u32 v255, v255, v118, v119\n"
"v_max3_u32 v255, v255, v132, v133\n"
"v_max3_u32 v255, v255, v134, v135\n"
"v_max3_u32 v255, v255, v144, v145\n"
"v_max3_u32 v255, v255, v146, v147\n"
"v_cmp_eq_u32_e32 vcc, -1, v255\n"
"s_cbranch_vccz .Lmk_p115k\n"
"s_sub_u32 s1, s1, 1\n"
"s_cbranch_scc0 .Lmk_p115l\n"
".Lmk_p115k:\n"
"v_mfma_f32_16x16x32_bf16 v[96:99], a[128:131], v[100:103], 0\n"
"v_mfma_f32_16x16x32_bf16 v[96:99], a[132:135], v[176:179], v[96:99]\n"
"v_mfma_f32_16x16x32_bf16 v[96:99], a[136:139], v[104:107], v[96:99]\n"
"v_mfma_f32_16x16x32_bf16 v[96:99], a[140:143], v[88:91], v[96:99]\n"
"v_mfma_f32_16x16x32_bf16 v[96:99], a[144:147], v[92:95], v[96:99]\n"
"v_mfma_f32_16x16x32_bf16 v[96:99], a[148:151], v[116:119], v[96:99]\n"
"v_mfma_f32_16x16x32_bf16 v[96:99], a[152:155], v[132:135], v[96:99]\n"
"v_mfma_f32_16x16x32_bf16 v[96:99], a[156:159], v[144:147], v[96:99]\n"
"v_mfma_f32_16x16x32_bf16 v[148:151], a[188:191], v[144:147], 0\n"
"v_mfma_f32_16x16x32_bf16 v[148:151], a[184:187], v[132:135], v[148:151]\n"
"v_mfma_f32_16x16x32_bf16 v[148:151], a[180:183], v[116:119], v[148:151]\n"
"v_mfma_f32_16x16x32_bf16 v[148:151], a[176:179], v[92:95], v[148:151]\n"
"v_mfma_f32_16x16x32_bf16 v[148:151], a[172:175], v[88:91], v[148:151]\n"
"v_mfma_f32_16x16x32_bf16 v[148:151], a[168:171], v[104:107], v[148:151]\n"
"s_nop 7\n"
"s_branch .Lmk_p115r\n"
".Lmk_p116s:\n"
";\n"
"s_mov_b32 s1, 0x10000\n"
".Lmk_p116l:\n"
"s_add_u32 s97, s57, 0\n"
"buffer_load_dwordx4 v[156:159], v0, s[4:7], s97 offen sc1\n"
"buffer_load_dwordx4 v[108:111], v0, s[4:7], s97 offen offset:1024 sc1\n"
"buffer_load_dwordx4 v[112:115], v0, s[4:7], s97 offen offset:2048 sc1\n"
"buffer_load_dwordx4 v[196:199], v0, s[4:7], s97 offen offset:3072 sc1\n"
"buffer_load_dwordx4 v[200:203], v1, s[4:7], s97 offen sc1\n"
"buffer_load_dwordx4 v[128:131], v1, s[4:7], s97 offen offset:1024 sc1\n"
"buffer_load_dwordx4 v[120:123], v1, s[4:7], s97 offen offset:2048 sc1\n"
"buffer_load_dwordx4 v[136:139], v1, s[4:7], s97 offen offset:3072 sc1\n"
"s_add_u32 s97, s58, 0\n"
"buffer_load_dwordx4 v[124:127], v0, s[4:7], s97 offen sc1\n"
"buffer_load_dwordx4 v[140:143], v0, s[4:7], s97 offen offset:1024 sc1\n"
"buffer_load_dwordx4 v[152:155], v0, s[4:7], s97 offen offset:2048 sc1\n"
"buffer_load_dwordx4 v[160:163], v0, s[4:7], s97 offen offset:3072 sc1\n"
"buffer_load_dwordx4 v[164:167], v1, s[4:7], s97 offen sc1\n"
"buffer_load_dwordx4 v[148:151], v1, s[4:7], s97 offen offset:1024 sc1\n"
"buffer_load_dwordx4 v[116:119], v1, s[4:7], s97 offen offset:2048 sc1\n"
"buffer_load_dwordx4 v[132:135], v1, s[4:7], s97 offen offset:3072 sc1\n"
"s_add_u32 s97, s59, 0\n"
"buffer_load_dwordx4 v[172:175], v0, s[4:7], s97 offen sc1\n"
"buffer_load_dwordx4 v[176:179], v0, s[4:7], s97 offen offset:1024 sc1\n"
"buffer_load_dwordx4 v[168:171], v0, s[4:7], s97 offen offset:2048 sc1\n"
"s_waitcnt vmcnt(0)\n"
"v_max_u32_e32 v255, v156, v157\n"
"v_max3_u32 v255, v255, v158, v159\n"
"v_max3_u32 v255, v255, v108, v109\n"
"v_max3_u32 v255, v255, v110, v111\n"
"v_max3_u32 v255, v255, v112, v113\n"
"v_max3_u32 v255, v255, v114, v115\n"
"v_max3_u32 v255, v255, v196, v197\n"
"v_max3_u32 v255, v255, v198, v199\n"
"v_max3_u32 v255, v255, v200, v201\n"
"v_max3_u32 v255, v255, v202, v203\n"
"v_max3_u32 v255, v255, v128, v129\n"
"v_max3_u32 v255, v255, v130, v131\n"
"v_max3_u32 v255, v255, v120, v121\n"
"v_max3_u32 v255, v255, v122, v123\n"
"v_max3_u32 v255, v255, v136, v137\n"
"v_max3_u32 v255, v255, v138, v139\n"
"v_cmp_eq_u32_e32 vcc, -1, v255\n"
"s_cbranch_vccz .Lmk_p116k\n"
"s_sub_u32 s1, s1, 1\n"
"s_cbranch_scc0 .Lmk_p116l\n"
".Lmk_p116k:\n"
"v_mfma_f32_16x16x32_bf16 v[88:91], a[128:131], v[156:159], 0\n"
"v_mfma_f32_16x16x32_bf16 v[88:91], a[132:135], v[108:111], v[88:91]\n"
"v_mfma_f32_16x16x32_bf16 v[88:91], a[136:139], v[112:115], v[88:91]\n"
"v_mfma_f32_16x16x32_bf16 v[88:91], a[140:143], v[196:199], v[88:91]\n"
"v_mfma_f32_16x16x32_bf16 v[88:91], a[144:147], v[200:203], v[88:91]\n"
"v_mfma_f32_16x16x32_bf16 v[88:91], a[148:151], v[128:131], v[88:91]\n"
"v_mfma_f32_16x16x32_bf16 v[88:91], a[152:155], v[120:123], v[88:91]\n"
"v_mfma_f32_16x16x32_bf16 v[88:91], a[156:159], v[136:139], v[88:91]\n"
"v_mfma_f32_16x16x32_bf16 v[92:95], a[188:191], v[136:139], 0\n"
"v_mfma_f32_16x16x32_bf16 v[92:95], a[184:187], v[120:123], v[92:95]\n"
"v_mfma_f32_16x16x32_bf16 v[92:95], a[180:183], v[128:131], v[92:95]\n"
"v_mfma_f32_16x16x32_bf16 v[92:95], a[176:179], v[200:203], v[92:95]\n"
"v_mfma_f32_16x16x32_bf16 v[92:95], a[172:175], v[196:199], v[92:95]\n"
"v_mfma_f32_16x16x32_bf16 v[92:95], a[168:171], v[112:115], v[92:95]\n"
"s_nop 7\n"
"s_branch .Lmk_p116r\n"
".Lmk_p117s:\n"
";\n"
"s_mov_b32 s1, 0x10000\n"
".Lmk_p117l:\n"
"s_add_u32 s97, s58, 0\n"
"buffer_load_dwordx4 v[124:127], v0, s[4:7], s97 offen sc1\n"
"buffer_load_dwordx4 v[140:143], v0, s[4:7], s97 offen offset:1024 sc1\n"
"buffer_load_dwordx4 v[152:155], v0, s[4:7], s97 offen offset:2048 sc1\n"
"buffer_load_dwordx4 v[160:163], v0, s[4:7], s97 offen offset:3072 sc1\n"
"buffer_load_dwordx4 v[164:167], v1, s[4:7], s97 offen sc1\n"
"buffer_load_dwordx4 v[148:151], v1, s[4:7], s97 offen offset:1024 sc1\n"
"buffer_load_dwordx4 v[116:119], v1, s[4:7], s97 offen offset:2048 sc1\n"
"buffer_load_dwordx4 v[132:135], v1, s[4:7], s97 offen offset:3072 sc1\n"
"s_add_u32 s97, s59, 0\n"
"buffer_load_dwordx4 v[172:175], v0, s[4:7], s97 offen sc1\n"
"buffer_load_dwordx4 v[176:179], v0, s[4:7], s97 offen offset:1024 sc1\n"
"buffer_load_dwordx4 v[168:171], v0, s[4:7], s97 offen offset:2048 sc1\n"
"buffer_load_dwordx4 v[180:183], v0, s[4:7], s97 offen offset:3072 sc1\n"
"buffer_load_dwordx4 v[184:187], v1, s[4:7], s97 offen sc1\n"
"buffer_load_dwordx4 v[188:191], v1, s[4:7], s97 offen offset:1024 sc1\n"
"buffer_load_dwordx4 v[192:195], v1, s[4:7], s97 offen offset:2048 sc1\n"
"buffer_load_dwordx4 v[196:199], v1, s[4:7], s97 offen offset:3072 sc1\n"
"s_add_u32 s97, s52, 0x440000\n"
"buffer_load_dwordx4 v[88:91], v0, s[4:7], s97 offen sc1\n"
"buffer_load_dwordx4 v[92:95], v0, s[4:7], s97 offen offset:1024 sc1\n"
"s_waitcnt vmcnt(0)\n"
"v_max_u32_e32 v239, v124, v125\n"
"v_max3_u32 v239, v239, v126, v127\n"
"v_max3_u32 v239, v239, v140, v141\n"
"v_max3_u32 v239, v239, v142, v143\n"
"v_max3_u32 v239, v239, v152, v153\n"
"v_max3_u32 v239, v239, v154, v155\n"
"v_max3_u32 v239, v239, v160, v161\n"
"v_max3_u32 v239, v239, v162, v163\n"
"v_max3_u32 v239, v239, v164, v165\n"
"v_max3_u32 v239, v239, v166, v167\n"
"v_max3_u32 v239, v239, v148, v149\n"
"v_max3_u32 v239, v239, v150, v151\n"
"v_max3_u32 v239, v239, v116, v117\n"
"v_max3_u32 v239, v239, v118, v119\n"
"v_max3_u32 v239, v239, v132, v133\n"
"v_max3_u32 v239, v239, v134, v135\n"
"v_cmp_eq_u32_e32 vcc, -1, v239\n"
"s_cbranch_vccz .Lmk_p117k\n"
"s_sub_u32 s1, s1, 1\n"
"s_cbranch_scc0 .Lmk_p117l\n"
".Lmk_p117k:\n"
"v_mfma_f32_16x16x32_bf16 v[96:99], a[128:131], v[124:127], 0\n"
"v_mfma_f32_16x16x32_bf16 v[96:99], a[132:135], v[140:143], v[96:99]\n"
"v_mfma_f32_16x16x32_bf16 v[96:99], a[136:139], v[152:155], v[96:99]\n"
"v_mfma_f32_16x16x32_bf16 v[96:99], a[140:143], v[160:163], v[96:99]\n"
"v_mfma_f32_16x16x32_bf16 v[96:99], a[144:147], v[164:167], v[96:99]\n"
"v_mfma_f32_16x16x32_bf16 v[96:99], a[148:151], v[148:151], v[96:99]\n"
"v_mfma_f32_16x16x32_bf16 v[96:99], a[152:155], v[116:119], v[96:99]\n"
"v_mfma_f32_16x16x32_bf16 v[96:99], a[156:159], v[132:135], v[96:99]\n"
"v_mfma_f32_16x16x32_bf16 v[104:107], a[188:191], v[132:135], 0\n"
"v_mfma_f32_16x16x32_bf16 v[104:107], a[184:187], v[116:119], v[104:107]\n"
"v_mfma_f32_16x16x32_bf16 v[104:107], a[180:183], v[148:151], v[104:107]\n"
"v_mfma_f32_16x16x32_bf16 v[104:107], a[176:179], v[164:167], v[104:107]\n"
"v_mfma_f32_16x16x32_bf16 v[104:107], a[172:175], v[160:163], v[104:107]\n"
"v_mfma_f32_16x16x32_bf16 v[104:107], a[168:171], v[152:155], v[104:107]\n"
"s_nop 7\n"
"s_branch .Lmk_p117r\n"
".Lmk_p118s:\n"
";\n"
"s_mov_b32 s1, 0x10000\n"
".Lmk_p118l:\n"
"s_add_u32 s97, s59, 0\n"
"buffer_load_dwordx4 v[172:175], v0, s[4:7], s97 offen sc1\n"
"buffer_load_dwordx4 v[176:179], v0, s[4:7], s97 offen offset:1024 sc1\n"
"buffer_load_dwordx4 v[168:171], v0, s[4:7], s97 offen offset:2048 sc1\n"
"buffer_load_dwordx4 v[180:183], v0, s[4:7], s97 offen offset:3072 sc1\n"
"buffer_load_dwordx4 v[184:187], v1, s[4:7], s97 offen sc1\n"
"buffer_load_dwordx4 v[188:191], v1, s[4:7], s97 offen offset:1024 sc1\n"
"buffer_load_dwordx4 v[192:195], v1, s[4:7], s97 offen offset:2048 sc1\n"
"buffer_load_dwordx4 v[196:199], v1, s[4:7], s97 offen offset:3072 sc1\n"
"s_add_u32 s97, s52, 0x440000\n"
"buffer_load_dwordx4 v[88:91], v0, s[4:7], s97 offen sc1\n"
"buffer_load_dwordx4 v[92:95], v0, s[4:7], s97 offen offset:1024 sc1\n"
"buffer_load_dwordx4 v[96:99], v0, s[4:7], s97 offen offset:2048 sc1\n"
"buffer_load_dwordx4 v[100:103], v0, s[4:7], s97 offen offset:3072 sc1\n"
"buffer_load_dwordx4 v[104:107], v1, s[4:7], s97 offen sc1\n"
"buffer_load_dwordx4 v[112:115], v1, s[4:7], s97 offen offset:1024 sc1\n"
"buffer_load_dwordx4 v[116:119], v1, s[4:7], s97 offen offset:2048 sc1\n"
"buffer_load_dwordx4 v[124:127], v1, s[4:7], s97 offen offset:3072 sc1\n"
"s_add_u32 s97, s53, 0x440000\n"
"buffer_load_dwordx4 v[128:131], v0, s[4:7], s97 offen sc1\n"
"buffer_load_dwordx4 v[132:135], v0, s[4:7], s97 offen offset:1024 sc1\n"
"buffer_load_dwordx4 v[136:139], v0, s[4:7], s97 offen offset:2048 sc1\n"
"s_waitcnt vmcnt(0)\n"
"v_max_u32_e32 v255, v172, v173\n"
"v_max3_u32 v255, v255, v174, v175\n"
"v_max3_u32 v255, v255, v176, v177\n"
"v_max3_u32 v255, v255, v178, v179\n"
"v_max3_u32 v255, v255, v168, v169\n"
"v_max3_u32 v255, v255, v170, v171\n"
"v_max3_u32 v255, v255, v180, v181\n"
"v_max3_u32 v255, v255, v182, v183\n"
"v_max3_u32 v255, v255, v184, v185\n"
"v_max3_u32 v255, v255, v186, v187\n"
"v_max3_u32 v255, v255, v188, v189\n"
"v_max3_u32 v255, v255, v190, v191\n"
"v_max3_u32 v255, v255, v192, v193\n"
"v_max3_u32 v255, v255, v194, v195\n"
"v_max3_u32 v255, v255, v196, v197\n"
"v_max3_u32 v255, v255, v198, v199\n"
"v_cmp_eq_u32_e32 vcc, -1, v255\n"
"s_cbranch_vccz .Lmk_p118k\n"
"s_sub_u32 s1, s1, 1\n"
"s_cbranch_scc0 .Lmk_p118l\n"
".Lmk_p118k:\n"
"v_mfma_f32_16x16x32_bf16 v[164:167], a[128:131], v[172:175], v[144:147]\n"
"v_mfma_f32_16x16x32_bf16 v[164:167], a[132:135], v[176:179], v[164:167]\n"
"v_mfma_f32_16x16x32_bf16 v[164:167], a[136:139], v[168:171], v[164:167]\n"
"v_mfma_f32_16x16x32_bf16 v[164:167], a[140:143], v[180:183], v[164:167]\n"
"v_mfma_f32_16x16x32_bf16 v[164:167], a[144:147], v[184:187], v[164:167]\n"
"v_mfma_f32_16x16x32_bf16 v[164:167], a[148:151], v[188:191], v[164:167]\n"
"v_mfma_f32_16x16x32_bf16 v[164:167], a[152:155], v[192:195], v[164:167]\n"
"v_mfma_f32_16x16x32_bf16 v[164:167], a[156:159], v[196:199], v[164:167]\n"
"v_mfma_f32_16x16x32_bf16 v[200:203], a[188:191], v[196:199], v[156:159]\n"
"v_mfma_f32_16x16x32_bf16 v[200:203], a[184:187], v[192:195], v[200:203]\n"
"v_mfma_f32_16x16x32_bf16 v[200:203], a[180:183], v[188:191], v[200:203]\n"
"v_mfma_f32_16x16x32_bf16 v[200:203], a[176:179], v[184:187], v[200:203]\n"
"v_mfma_f32_16x16x32_bf16 v[200:203], a[172:175], v[180:183], v[200:203]\n"
"v_mfma_f32_16x16x32_bf16 v[200:203], a[168:171], v[168:171], v[200:203]\n"
"s_nop 7\n"
"s_branch .Lmk_p118r\n"
".Lmk_exit:\n"
"v_xor_b32_e32 v255, 32, v15\n"
"ds_read_b128 v[36:39], v2 offset:17408\n"
"ds_read_b128 v[40:43], v15 offset:16384\n"
"ds_read_b128 v[52:55], v255 offset:16640\n"
"v_xor_b32_e32 v255, 32, v17\n"
"ds_read_b128 v[60:63], v2 offset:16384\n"
"ds_read_b128 v[64:67], v17 offset:15360\n"
"ds_read_b128 v[68:71], v255 offset:15616\n"
"v_xor_b32_e32 v255, 32, v16\n"
"ds_read_b64_tr_b16 v[72:73], v16 offset:16384\n"
"ds_read_b64_tr_b16 v[74:75], v255 offset:16640\n"
"ds_read_b64_tr_b16 v[76:77], v255 offset:16384\n"
"ds_read_b64_tr_b16 v[78:79], v16 offset:16640\n"
"v_mov_b32_e32 v80, 0x3f803f80\n"
"v_mov_b32_e32 v81, 0x3f803f80\n"
"v_mov_b32_e32 v82, 0x3f803f80\n"
"v_mov_b32_e32 v83, 0x3f803f80\n"
"v_xor_b32_e32 v255, 32, v16\n"
"ds_read_b64_tr_b16 v[84:85], v16 offset:15360\n"
"ds_read_b64_tr_b16 v[86:87], v255 offset:15616\n"
"s_waitcnt lgkmcnt(13)\n"
"ds_read_b64_tr_b16 v[96:97], v255 offset:15360\n"
"ds_read_b64_tr_b16 v[98:99], v16 offset:15616\n"
"v_mov_b32_e32 v100, 0x3f803f80\n"
"v_mov_b32_e32 v101, 0x3f803f80\n"
"v_mov_b32_e32 v102, 0x3f803f80\n"
"v_mov_b32_e32 v103, 0x3f803f80\n"
"s_waitcnt lgkmcnt(11)\n"
"v_mfma_f32_16x16x32_bf16 v[104:107], v[40:43], v[36:39], v[104:107]\n"
"v_mfma_f32_16x16x32_bf16 v[112:115], v[52:55], v[36:39], v[112:115]\n"
"s_waitcnt lgkmcnt(8)\n"
"v_mfma_f32_16x16x32_bf16 v[88:91], v[64:67], v[60:63], v[88:91]\n"
"v_mfma_f32_16x16x32_bf16 v[172:175], v[68:71], v[60:63], v[172:175]\n"
"s_nop 3\n"
"v_max_f32_e32 v255, v104, v105\n"
"s_nop 1\n"
"v_max_f32_e32 v254, v88, v89\n"
"v_max3_f32 v255, v255, v106, v107\n"
"v_max3_f32 v254, v254, v90, v91\n"
"v_max3_f32 v255, v255, v112, v113\n"
"v_max3_f32 v254, v254, v172, v173\n"
"v_max3_f32 v255, v255, v114, v115\n"
"v_max3_f32 v254, v254, v174, v175\n"
"v_mov_b32_e32 v253, v255\n"
"v_mov_b32_e32 v252, v254\n"
"s_nop 0\n"
"v_permlane16_swap_b32_e32 v255, v253\n"
"v_permlane16_swap_b32_e32 v254, v252\n"
"v_max_f32_e32 v255, v255, v253\n"
"v_max_f32_e32 v254, v254, v252\n"
"v_mov_b32_e32 v253, v255\n"
"v_mov_b32_e32 v252, v254\n"
"s_nop 0\n"
"v_permlane32_swap_b32_e32 v255, v253\n"
"v_permlane32_swap_b32_e32 v254, v252\n"
"v_max_f32_e32 v255, v255, v253\n"
"v_max_f32_e32 v254, v254, v252\n"
"v_sub_f32_e32 v104, v104, v255\n"
"v_sub_f32_e32 v105, v105, v255\n"
"v_sub_f32_e32 v106, v106, v255\n"
"v_sub_f32_e32 v107, v107, v255\n"
"v_sub_f32_e32 v112, v112, v255\n"
"v_sub_f32_e32 v113, v113, v255\n"
"v_sub_f32_e32 v114, v114, v255\n"
"v_sub_f32_e32 v115, v115, v255\n"
"v_exp_f32_e32 v104, v104\n"
"v_exp_f32_e32 v105, v105\n"
"v_exp_f32_e32 v106, v106\n"
"v_exp_f32_e32 v107, v107\n"
"v_exp_f32_e32 v112, v112\n"
"v_exp_f32_e32 v113, v113\n"
"v_exp_f32_e32 v114, v114\n"
"v_exp_f32_e32 v115, v115\n"
"v_cvt_pk_bf16_f32 v36, v104, v105\n"
"v_cvt_pk_bf16_f32 v37, v106, v107\n"
"v_cvt_pk_bf16_f32 v38, v112, v113\n"
"v_cvt_pk_bf16_f32 v39, v114, v115\n"
"s_waitcnt lgkmcnt(4)\n"
"v_sub_f32_e32 v88, v88, v254\n"
"v_sub_f32_e32 v89, v89, v254\n"
"v_sub_f32_e32 v90, v90, v254\n"
"v_sub_f32_e32 v91, v91, v254\n"
"v_sub_f32_e32 v172, v172, v254\n"
"v_sub_f32_e32 v173, v173, v254\n"
"v_sub_f32_e32 v174, v174, v254\n"
"v_mfma_f32_16x16x32_bf16 v[40:43], v[80:83], v[36:39], 0\n"
"v_sub_f32_e32 v175, v175, v254\n"
"v_exp_f32_e32 v88, v88\n"
"v_exp_f32_e32 v89, v89\n"
"v_exp_f32_e32 v90, v90\n"
"v_mfma_f32_16x16x32_bf16 v[52:55], v[72:75], v[36:39], 0\n"
"v_exp_f32_e32 v91, v91\n"
"v_exp_f32_e32 v172, v172\n"
"v_exp_f32_e32 v173, v173\n"
"v_exp_f32_e32 v174, v174\n"
"v_mfma_f32_16x16x32_bf16 v[60:63], v[76:79], v[36:39], 0\n"
"v_exp_f32_e32 v175, v175\n"
"v_cvt_pk_bf16_f32 v36, v88, v89\n"
"v_cvt_pk_bf16_f32 v37, v90, v91\n"
"v_cvt_pk_bf16_f32 v38, v172, v173\n"
"v_cvt_pk_bf16_f32 v39, v174, v175\n"
"s_waitcnt lgkmcnt(0)\n"
"v_rcp_f32_e32 v40, v40\n"
"s_nop 0\n"
"v_mul_f32_e32 v52, v52, v40\n"
"v_mul_f32_e32 v53, v53, v40\n"
"v_mfma_f32_16x16x32_bf16 v[64:67], v[100:103], v[36:39], 0\n"
"v_mul_f32_e32 v54, v54, v40\n"
"v_mul_f32_e32 v55, v55, v40\n"
"v_mul_f32_e32 v60, v60, v40\n"
"v_mfma_f32_16x16x32_bf16 v[68:71], v[84:87], v[36:39], 0\n"
"v_mul_f32_e32 v61, v61, v40\n"
"v_mul_f32_e32 v62, v62, v40\n"
"v_mul_f32_e32 v63, v63, v40\n"
"v_cvt_pk_bf16_f32 v40, v52, v53\n"
"v_mfma_f32_16x16x32_bf16 v[72:75], v[96:99], v[36:39], 0\n"
"v_cvt_pk_bf16_f32 v41, v54, v55\n"
"v_cvt_pk_bf16_f32 v42, v60, v61\n"
"v_cvt_pk_bf16_f32 v43, v62, v63\n"
"s_add_u32 s97, s60, 0x408000\n"
"buffer_store_dwordx4 v[40:43], v0, s[4:7], s97 offen sc1\n"
"v_rcp_f32_e32 v64, v64\n"
"s_nop 0\n"
"v_mul_f32_e32 v68, v68, v64\n"
"v_mul_f32_e32 v69, v69, v64\n"
"v_mul_f32_e32 v70, v70, v64\n"
"v_mul_f32_e32 v71, v71, v64\n"
"v_mul_f32_e32 v72, v72, v64\n"
"v_mul_f32_e32 v73, v73, v64\n"
"v_mul_f32_e32 v74, v74, v64\n"
"v_mul_f32_e32 v75, v75, v64\n"
"v_cvt_pk_bf16_f32 v36, v68, v69\n"
"v_cvt_pk_bf16_f32 v37, v70, v71\n"
"v_cvt_pk_bf16_f32 v38, v72, v73\n"
"v_cvt_pk_bf16_f32 v39, v74, v75\n"
"s_add_u32 s97, s60, 0x400000\n"
"buffer_store_dwordx4 v[36:39], v0, s[4:7], s97 offen sc1\n"
";\n"
"s_waitcnt vmcnt(23)\n"
"; @B G2 b.0\n"
"s_nop 0\n"
"v_mfma_f32_16x16x32_bf16 v[36:39], a[128:131], v[120:123], 0\n"
"s_add_u32 s97, s58, 0x440000\n"
"buffer_load_dwordx4 v[40:43], v0, s[4:7], s97 offen sc1\n"
"v_mfma_f32_16x16x32_bf16 v[36:39], a[132:135], v[136:139], v[36:39]\n"
"s_waitcnt vmcnt(22)\n"
"v_mfma_f32_16x16x32_bf16 v[36:39], a[136:139], v[140:143], v[36:39]\n"
"v_mfma_f32_16x16x32_bf16 v[36:39], a[140:143], v[192:195], v[36:39]\n"
"s_waitcnt vmcnt(20)\n"
"v_mfma_f32_16x16x32_bf16 v[36:39], a[144:147], v[196:199], v[36:39]\n"
"buffer_load_dwordx4 v[52:55], v0, s[4:7], s97 offen offset:1024 sc1\n"
"v_mfma_f32_16x16x32_bf16 v[36:39], a[148:151], v[188:191], v[36:39]\n"
"s_waitcnt vmcnt(19)\n"
"v_mfma_f32_16x16x32_bf16 v[36:39], a[152:155], v[108:111], v[36:39]\n"
"v_mfma_f32_16x16x32_bf16 v[36:39], a[156:159], v[92:95], v[36:39]\n"
"v_mfma_f32_16x16x32_bf16 v[60:63], a[188:191], v[92:95], 0\n"
"buffer_load_dwordx4 v[64:67], v0, s[4:7], s97 offen offset:2048 sc1\n"
"v_mfma_f32_16x16x32_bf16 v[60:63], a[184:187], v[108:111], v[60:63]\n"
"v_mfma_f32_16x16x32_bf16 v[60:63], a[180:183], v[188:191], v[60:63]\n"
"v_mfma_f32_16x16x32_bf16 v[60:63], a[176:179], v[196:199], v[60:63]\n"
"v_mfma_f32_16x16x32_bf16 v[60:63], a[172:175], v[192:195], v[60:63]\n"
"s_nop 1\n"
"v_cmp_u_f32_e32 vcc, v36, v36\n"
"v_mfma_f32_16x16x32_bf16 v[60:63], a[168:171], v[140:143], v[60:63]\n"
"s_cbranch_vccnz .Lmk_p60s\n"
".Lmk_p60r:\n"
"v_mfma_f32_16x16x32_bf16 v[60:63], a[164:167], v[136:139], v[60:63]\n"
"ds_write_b128 v3, v[36:39]\n"
"v_mfma_f32_16x16x32_bf16 v[60:63], a[160:163], v[120:123], v[60:63]\n"
"buffer_load_dwordx4 v[36:39], v0, s[4:7], s97 offen offset:3072 sc1\n"
"v_mfma_f32_16x16x32_bf16 v[68:71], a[192:195], v[120:123], 0\n"
"v_mfma_f32_16x16x32_bf16 v[68:71], a[196:199], v[136:139], v[68:71]\n"
"v_mfma_f32_16x16x32_bf16 v[68:71], a[200:203], v[140:143], v[68:71]\n"
"v_mfma_f32_16x16x32_bf16 v[68:71], a[204:207], v[192:195], v[68:71]\n"
"buffer_load_dwordx4 v[72:75], v1, s[4:7], s97 offen sc1\n"
"v_mfma_f32_16x16x32_bf16 v[68:71], a[208:211], v[196:199], v[68:71]\n"
"v_mfma_f32_16x16x32_bf16 v[68:71], a[212:215], v[188:191], v[68:71]\n"
"ds_write_b128 v3, v[60:63] offset:1024\n"
"v_mfma_f32_16x16x32_bf16 v[68:71], a[216:219], v[108:111], v[68:71]\n"
"v_mfma_f32_16x16x32_bf16 v[68:71], a[220:223], v[92:95], v[68:71]\n"
"buffer_load_dwordx4 v[60:63], v1, s[4:7], s97 offen offset:1024 sc1\n"
"v_mfma_f32_16x16x32_bf16 v[76:79], a[252:255], v[92:95], 0\n"
"v_mfma_f32_16x16x32_bf16 v[76:79], a[248:251], v[108:111], v[76:79]\n"
"v_mfma_f32_16x16x32_bf16 v[76:79], a[244:247], v[188:191], v[76:79]\n"
"v_mfma_f32_16x16x32_bf16 v[76:79], a[240:243], v[196:199], v[76:79]\n"
"buffer_load_dwordx4 v[80:83], v1, s[4:7], s97 offen offset:2048 sc1\n"
"v_mfma_f32_16x16x32_bf16 v[76:79], a[236:239], v[192:195], v[76:79]\n"
"v_mfma_f32_16x16x32_bf16 v[76:79], a[232:235], v[140:143], v[76:79]\n"
"ds_write_b128 v3, v[68:71] offset:2048\n"
"v_mfma_f32_16x16x32_bf16 v[76:79], a[228:231], v[136:139], v[76:79]\n"
"v_mfma_f32_16x16x32_bf16 v[76:79], a[224:227], v[120:123], v[76:79]\n"
"s_waitcnt vmcnt(20)\n"
"buffer_load_dwordx4 v[68:71], v1, s[4:7], s97 offen offset:3072 sc1\n"
"; @B G2 b.1\n"
"v_mfma_f32_16x16x32_bf16 v[84:87], a[128:131], v[144:147], 0\n"
"v_mfma_f32_16x16x32_bf16 v[84:87], a[132:135], v[180:183], v[84:87]\n"
"s_add_u32 s97, s59, 0x440000\n"
"v_mfma_f32_16x16x32_bf16 v[84:87], a[136:139], v[24:27], v[84:87]\n"
"v_mfma_f32_16x16x32_bf16 v[84:87], a[140:143], v[32:35], v[84:87]\n"
"s_waitcnt vmcnt(15)\n"
"buffer_load_dwordx4 v[96:99], v0, s[4:7], s97 offen sc1\n"
"v_mfma_f32_16x16x32_bf16 v[84:87], a[144:147], v[44:47], v[84:87]\n"
"v_mfma_f32_16x16x32_bf16 v[84:87], a[148:151], v[48:51], v[84:87]\n"
"ds_write_b128 v3, v[76:79] offset:3072\n"
"v_mfma_f32_16x16x32_bf16 v[84:87], a[152:155], v[28:31], v[84:87]\n"
"v_mfma_f32_16x16x32_bf16 v[84:87], a[156:159], v[56:59], v[84:87]\n"
"buffer_load_dwordx4 v[76:79], v0, s[4:7], s97 offen offset:1024 sc1\n"
"v_mfma_f32_16x16x32_bf16 v[100:103], a[188:191], v[56:59], 0\n"
"v_mfma_f32_16x16x32_bf16 v[100:103], a[184:187], v[28:31], v[100:103]\n"
"v_mfma_f32_16x16x32_bf16 v[100:103], a[180:183], v[48:51], v[100:103]\n"
"v_mfma_f32_16x16x32_bf16 v[100:103], a[176:179], v[44:47], v[100:103]\n"
"buffer_load_dwordx4 v[116:119], v0, s[4:7], s97 offen offset:2048 sc1\n"
"v_mfma_f32_16x16x32_bf16 v[100:103], a[172:175], v[32:35], v[100:103]\n"
"s_nop 0\n"
"v_cmp_u_f32_e32 vcc, v84, v84\n"
"v_mfma_f32_16x16x32_bf16 v[100:103], a[168:171], v[24:27], v[100:103]\n"
"s_cbranch_vccnz .Lmk_p61s\n"
".Lmk_p61r:\n"
"v_mfma_f32_16x16x32_bf16 v[100:103], a[164:167], v[180:183], v[100:103]\n"
"ds_write_b128 v3, v[84:87] offset:6144\n"
"v_mfma_f32_16x16x32_bf16 v[100:103], a[160:163], v[144:147], v[100:103]\n"
"buffer_load_dwordx4 v[84:87], v0, s[4:7], s97 offen offset:3072 sc1\n"
"v_mfma_f32_16x16x32_bf16 v[124:127], a[192:195], v[144:147], 0\n"
"v_mfma_f32_16x16x32_bf16 v[124:127], a[196:199], v[180:183], v[124:127]\n"
"v_mfma_f32_16x16x32_bf16 v[124:127], a[200:203], v[24:27], v[124:127]\n"
"v_mfma_f32_16x16x32_bf16 v[124:127], a[204:207], v[32:35], v[124:127]\n"
"buffer_load_dwordx4 v[128:131], v1, s[4:7], s97 offen sc1\n"
"v_mfma_f32_16x16x32_bf16 v[124:127], a[208:211], v[44:47], v[124:127]\n"
"ds_read_b128 v[132:135], v2 offset:8192\n"
"ds_read_b128 v[148:151], v2 offset:9216\n"
"v_mfma_f32_16x16x32_bf16 v[124:127], a[212:215], v[48:51], v[124:127]\n"
"ds_write_b128 v3, v[100:103] offset:7168\n"
"v_mfma_f32_16x16x32_bf16 v[124:127], a[216:219], v[28:31], v[124:127]\n"
"v_mfma_f32_16x16x32_bf16 v[124:127], a[220:223], v[56:59], v[124:127]\n"
"buffer_load_dwordx4 v[100:103], v1, s[4:7], s97 offen offset:1024 sc1\n"
"v_mfma_f32_16x16x32_bf16 v[152:155], a[252:255], v[56:59], 0\n"
"v_mfma_f32_16x16x32_bf16 v[152:155], a[248:251], v[28:31], v[152:155]\n"
"s_waitcnt lgkmcnt(1)\n"
"v_lshlrev_b32_e32 v255, 16, v132\n"
"v_and_b32_e32 v254, 0xffff0000, v132\n"
"v_lshlrev_b32_e32 v253, 16, v133\n"
"v_mfma_f32_16x16x32_bf16 v[152:155], a[244:247], v[48:51], v[152:155]\n"
"v_and_b32_e32 v252, 0xffff0000, v133\n"
"v_lshlrev_b32_e32 v251, 16, v134\n"
"v_and_b32_e32 v250, 0xffff0000, v134\n"
"v_mfma_f32_16x16x32_bf16 v[152:155], a[240:243], v[44:47], v[152:155]\n"
"buffer_load_dwordx4 v[156:159], v1, s[4:7], s97 offen offset:2048 sc1\n"
"v_lshlrev_b32_e32 v249, 16, v135\n"
"v_mfma_f32_16x16x32_bf16 v[152:155], a[236:239], v[32:35], v[152:155]\n"
"v_and_b32_e32 v248, 0xffff0000, v135\n"
"v_lshlrev_b32_e32 v247, 16, v148\n"
"v_and_b32_e32 v246, 0xffff0000, v148\n"
"v_mfma_f32_16x16x32_bf16 v[152:155], a[232:235], v[24:27], v[152:155]\n"
"ds_write_b128 v3, v[124:127] offset:8192\n"
"v_mfma_f32_16x16x32_bf16 v[152:155], a[228:231], v[180:183], v[152:155]\n"
"v_lshlrev_b32_e32 v245, 16, v149\n"
"v_and_b32_e32 v244, 0xffff0000, v149\n"
"v_lshlrev_b32_e32 v243, 16, v150\n"
"v_mfma_f32_16x16x32_bf16 v[152:155], a[224:227], v[144:147], v[152:155]\n"
"s_waitcnt vmcnt(11)\n"
"buffer_load_dwordx4 v[124:127], v1, s[4:7], s97 offen offset:3072 sc1\n"
"; @B G2 b.2\n"
"v_mfma_f32_16x16x32_bf16 v[132:135], a[128:131], v[40:43], 0\n"
"v_and_b32_e32 v242, 0xffff0000, v150\n"
"v_lshlrev_b32_e32 v241, 16, v151\n"
"v_and_b32_e32 v240, 0xffff0000, v151\n"
"v_mfma_f32_16x16x32_bf16 v[132:135], a[132:135], v[52:55], v[132:135]\n"
"v_mfma_f32_16x16x32_bf16 v[132:135], a[136:139], v[64:67], v[132:135]\n"
"v_mfma_f32_16x16x32_bf16 v[132:135], a[140:143], v[36:39], v[132:135]\n"
"s_waitcnt vmcnt(8)\n"
"v_mfma_f32_16x16x32_bf16 v[132:135], a[144:147], v[72:75], v[132:135]\n"
"v_mfma_f32_16x16x32_bf16 v[132:135], a[148:151], v[60:63], v[132:135]\n"
"ds_write_b128 v3, v[152:155] offset:9216\n"
"v_mfma_f32_16x16x32_bf16 v[132:135], a[152:155], v[80:83], v[132:135]\n"
"v_mfma_f32_16x16x32_bf16 v[132:135], a[156:159], v[68:71], v[132:135]\n"
"v_mfma_f32_16x16x32_bf16 v[148:151], a[188:191], v[68:71], 0\n"
"v_mfma_f32_16x16x32_bf16 v[148:151], a[184:187], v[80:83], v[148:151]\n"
"s_waitcnt lgkmcnt(0)\n"
"s_barrier\n"
"v_mfma_f32_16x16x32_bf16 v[148:151], a[180:183], v[60:63], v[148:151]\n"
"ds_read_b128 v[152:155], v4\n"
"ds_read_b128 v[160:163], v5\n"
"v_mfma_f32_16x16x32_bf16 v[148:151], a[176:179], v[72:75], v[148:151]\n"
"v_mfma_f32_16x16x32_bf16 v[148:151], a[172:175], v[36:39], v[148:151]\n"
"s_add_u32 s97, s56, 0x400000\n"
"v_cmp_u_f32_e32 vcc, v132, v132\n"
"v_mfma_f32_16x16x32_bf16 v[148:151], a[168:171], v[64:67], v[148:151]\n"
"s_cbranch_vccnz .Lmk_p62s\n"
".Lmk_p62r:\n"
"v_mfma_f32_16x16x32_bf16 v[148:151], a[164:167], v[52:55], v[148:151]\n"
"ds_write_b128 v3, v[132:135] offset:12288\n"
"v_mfma_f32_16x16x32_bf16 v[148:151], a[160:163], v[40:43], v[148:151]\n"
"buffer_load_dwordx4 v[132:135], v0, s[4:7], s97 offen sc1\n"
"s_waitcnt lgkmcnt(1)\n"
"v_mfma_f32_16x16x32_bf16 v[164:167], a[192:195], v[40:43], 0\n"
"ds_read_b128 v[168:171], v4 offset:1024\n"
"ds_read_b128 v[176:179], v5 offset:1024\n"
"v_add_f32_e32 v152, v152, v255\n"
"v_mfma_f32_16x16x32_bf16 v[164:167], a[196:199], v[52:55], v[164:167]\n"
"v_add_f32_e32 v153, v153, v254\n"
"v_add_f32_e32 v154, v154, v253\n"
"v_add_f32_e32 v155, v155, v252\n"
"v_mfma_f32_16x16x32_bf16 v[164:167], a[200:203], v[64:67], v[164:167]\n"
"buffer_load_dwordx4 v[184:187], v0, s[4:7], s97 offen offset:1024 sc1\n"
"v_add_f32_e32 v152, v152, v160\n"
"v_mfma_f32_16x16x32_bf16 v[164:167], a[204:207], v[36:39], v[164:167]\n"
"v_add_f32_e32 v153, v153, v161\n"
"v_add_f32_e32 v154, v154, v162\n"
"v_add_f32_e32 v155, v155, v163\n"
"v_mfma_f32_16x16x32_bf16 v[164:167], a[208:211], v[72:75], v[164:167]\n"
"v_mfma_f32_16x16x32_bf16 v[164:167], a[212:215], v[60:63], v[164:167]\n"
"ds_write_b128 v3, v[148:151] offset:13312\n"
"v_mfma_f32_16x16x32_bf16 v[164:167], a[216:219], v[80:83], v[164:167]\n"
"buffer_load_dwordx4 v[148:151], v0, s[4:7], s97 offen offset:2048 sc1\n"
"s_waitcnt lgkmcnt(1)\n"
"v_mfma_f32_16x16x32_bf16 v[164:167], a[220:223], v[68:71], v[164:167]\n"
"ds_read_b128 v[160:163], v4 offset:2048\n"
"ds_read_b128 v[200:203], v5 offset:2048\n"
"v_add_f32_e32 v168, v168, v251\n"
"v_mfma_f32_16x16x32_bf16 v[204:207], a[252:255], v[68:71], 0\n"
"v_add_f32_e32 v169, v169, v250\n"
"v_add_f32_e32 v170, v170, v249\n"
"v_add_f32_e32 v171, v171, v248\n"
"v_mfma_f32_16x16x32_bf16 v[204:207], a[248:251], v[80:83], v[204:207]\n"
"buffer_load_dwordx4 v[68:71], v0, s[4:7], s97 offen offset:3072 sc1\n"
"v_add_f32_e32 v168, v168, v176\n"
"v_mfma_f32_16x16x32_bf16 v[204:207], a[244:247], v[60:63], v[204:207]\n"
"v_add_f32_e32 v169, v169, v177\n"
"v_add_f32_e32 v170, v170, v178\n"
"v_add_f32_e32 v171, v171, v179\n"
"v_mfma_f32_16x16x32_bf16 v[204:207], a[240:243], v[72:75], v[204:207]\n"
"v_mfma_f32_16x16x32_bf16 v[204:207], a[236:239], v[36:39], v[204:207]\n"
"buffer_load_dwordx4 v[36:39], v1, s[4:7], s97 offen sc1\n"
"s_waitcnt lgkmcnt(0)\n"
"v_mfma_f32_16x16x32_bf16 v[204:207], a[232:235], v[64:67], v[204:207]\n"
"ds_write_b128 v3, v[164:167] offset:14336\n"
"v_mfma_f32_16x16x32_bf16 v[204:207], a[228:231], v[52:55], v[204:207]\n"
"ds_read_b128 v[52:55], v4 offset:3072\n"
"ds_read_b128 v[60:63], v5 offset:3072\n"
"v_add_f32_e32 v160, v160, v247\n"
"v_mfma_f32_16x16x32_bf16 v[204:207], a[224:227], v[40:43], v[204:207]\n"
"s_waitcnt vmcnt(9)\n"
"buffer_load_dwordx4 v[40:43], v1, s[4:7], s97 offen offset:1024 sc1\n"
"; @B G2 b.3\n"
"v_mfma_f32_16x16x32_bf16 v[64:67], a[128:131], v[96:99], v[152:155]\n"
"v_add_f32_e32 v161, v161, v246\n"
"v_add_f32_e32 v162, v162, v245\n"
"v_add_f32_e32 v163, v163, v244\n"
"v_mfma_f32_16x16x32_bf16 v[64:67], a[132:135], v[76:79], v[64:67]\n"
"v_add_f32_e32 v160, v160, v200\n"
"v_add_f32_e32 v161, v161, v201\n"
"v_add_f32_e32 v162, v162, v202\n"
"v_mfma_f32_16x16x32_bf16 v[64:67], a[136:139], v[116:119], v[64:67]\n"
"buffer_load_dwordx4 v[72:75], v1, s[4:7], s97 offen offset:2048 sc1\n"
"v_add_f32_e32 v163, v163, v203\n"
"v_mfma_f32_16x16x32_bf16 v[64:67], a[140:143], v[84:87], v[64:67]\n"
"s_waitcnt vmcnt(7)\n"
"s_waitcnt lgkmcnt(0)\n"
"v_add_f32_e32 v52, v52, v243\n"
"v_add_f32_e32 v53, v53, v242\n"
"v_mfma_f32_16x16x32_bf16 v[64:67], a[144:147], v[128:131], v[64:67]\n"
"v_add_f32_e32 v54, v54, v241\n"
"v_add_f32_e32 v55, v55, v240\n"
"v_add_f32_e32 v52, v52, v60\n"
"v_mfma_f32_16x16x32_bf16 v[64:67], a[148:151], v[100:103], v[64:67]\n"
"ds_write_b128 v3, v[204:207] offset:15360\n"
"v_mfma_f32_16x16x32_bf16 v[64:67], a[152:155], v[156:159], v[64:67]\n"
"buffer_load_dwordx4 v[80:83], v1, s[4:7], s97 offen offset:3072 sc1\n"
"v_add_f32_e32 v53, v53, v61\n"
"v_mfma_f32_16x16x32_bf16 v[64:67], a[156:159], v[124:127], v[64:67]\n"
"s_add_u32 s97, s57, 0x400000\n"
"v_add_f32_e32 v54, v54, v62\n"
"v_add_f32_e32 v55, v55, v63\n"
"v_mfma_f32_16x16x32_bf16 v[60:63], a[188:191], v[124:127], v[168:171]\n"
"v_mfma_f32_16x16x32_bf16 v[60:63], a[184:187], v[156:159], v[60:63]\n"
"s_waitcnt lgkmcnt(0)\n"
"s_barrier\n"
"v_mfma_f32_16x16x32_bf16 v[60:63], a[180:183], v[100:103], v[60:63]\n"
"buffer_load_dwordx4 v[164:167], v0, s[4:7], s97 offen sc1\n"
"v_mfma_f32_16x16x32_bf16 v[60:63], a[176:179], v[128:131], v[60:63]\n"
"ds_read_b128 v[176:179], v6\n"
"ds_read_b128 v[200:203], v6 offset:1024\n"
"v_mfma_f32_16x16x32_bf16 v[60:63], a[172:175], v[84:87], v[60:63]\n"
"ds_read_b128 v[204:207], v6 offset:2048\n"
"v_cmp_u_f32_e32 vcc, v64, v64\n"
"v_mfma_f32_16x16x32_bf16 v[60:63], a[168:171], v[116:119], v[60:63]\n"
"s_cbranch_vccnz .Lmk_p63s\n"
".Lmk_p63r:\n"
"ds_read_b128 v[152:155], v6 offset:3072\n"
"v_mfma_f32_16x16x32_bf16 v[60:63], a[164:167], v[76:79], v[60:63]\n"
"buffer_load_dwordx4 v[168:171], v0, s[4:7], s97 offen offset:1024 sc1\n"
"v_mfma_f32_16x16x32_bf16 v[60:63], a[160:163], v[96:99], v[60:63]\n"
"v_mfma_f32_16x16x32_bf16 v[160:163], a[192:195], v[96:99], v[160:163]\n"
"v_mfma_f32_16x16x32_bf16 v[160:163], a[196:199], v[76:79], v[160:163]\n"
"buffer_load_dwordx4 v[208:211], v0, s[4:7], s97 offen offset:2048 sc1\n"
"v_mfma_f32_16x16x32_bf16 v[160:163], a[200:203], v[116:119], v[160:163]\n"
"v_mfma_f32_16x16x32_bf16 v[160:163], a[204:207], v[84:87], v[160:163]\n"
"v_mfma_f32_16x16x32_bf16 v[160:163], a[208:211], v[128:131], v[160:163]\n"
"buffer_load_dwordx4 v[212:215], v0, s[4:7], s97 offen offset:3072 sc1\n"
"s_waitcnt lgkmcnt(0)\n"
"v_mfma_f32_16x16x32_bf16 v[160:163], a[212:215], v[100:103], v[160:163]\n"
"v_add_f32_e32 v176, v64, v176\n"
"v_add_f32_e32 v177, v65, v177\n"
"v_add_f32_e32 v178, v66, v178\n"
"v_mfma_f32_16x16x32_bf16 v[160:163], a[216:219], v[156:159], v[160:163]\n"
"v_add_f32_e32 v179, v67, v179\n"
"v_add_f32_e32 v200, v60, v200\n"
"v_add_f32_e32 v201, v61, v201\n"
"v_mfma_f32_16x16x32_bf16 v[160:163], a[220:223], v[124:127], v[160:163]\n"
"buffer_load_dwordx4 v[64:67], v1, s[4:7], s97 offen sc1\n"
"v_add_f32_e32 v202, v62, v202\n"
"v_mfma_f32_16x16x32_bf16 v[52:55], a[252:255], v[124:127], v[52:55]\n"
"v_add_f32_e32 v203, v63, v203\n"
"v_cvt_pk_bf16_f32 v60, v176, v177\n"
"v_cvt_pk_bf16_f32 v61, v178, v179\n"
"v_mfma_f32_16x16x32_bf16 v[52:55], a[248:251], v[156:159], v[52:55]\n"
"v_cvt_pk_bf16_f32 v62, v200, v201\n"
"v_cvt_pk_bf16_f32 v63, v202, v203\n"
"v_mfma_f32_16x16x32_bf16 v[52:55], a[244:247], v[100:103], v[52:55]\n"
"buffer_load_dwordx4 v[100:103], v1, s[4:7], s97 offen offset:1024 sc1\n"
"v_mfma_f32_16x16x32_bf16 v[52:55], a[240:243], v[128:131], v[52:55]\n"
"v_mfma_f32_16x16x32_bf16 v[52:55], a[236:239], v[84:87], v[52:55]\n"
"v_mfma_f32_16x16x32_bf16 v[52:55], a[232:235], v[116:119], v[52:55]\n"
"buffer_load_dwordx4 v[84:87], v1, s[4:7], s97 offen offset:2048 sc1\n"
"v_mfma_f32_16x16x32_bf16 v[52:55], a[228:231], v[76:79], v[52:55]\n"
"v_mfma_f32_16x16x32_bf16 v[52:55], a[224:227], v[96:99], v[52:55]\n"
"buffer_load_dwordx4 v[76:79], v1, s[4:7], s97 offen offset:3072 sc1\n"
"s_waitcnt vmcnt(14)\n"
"; @B G2 a.0\n"
"v_mfma_f32_16x16x32_bf16 v[96:99], a[128:131], v[132:135], 0\n"
"s_add_u32 s97, s58, 0x400000\n"
"buffer_load_dwordx4 v[116:119], v0, s[4:7], s97 offen sc1\n"
"v_mfma_f32_16x16x32_bf16 v[96:99], a[132:135], v[184:187], v[96:99]\n"
"s_waitcnt vmcnt(13)\n"
"v_bfe_u32 v255, v0, 4, 4\n"
"v_bfe_u32 v254, v0, 9, 1\n"
"v_mfma_f32_16x16x32_bf16 v[96:99], a[136:139], v[148:151], v[96:99]\n"
"v_and_b32_e32 v253, 7, v255\n"
"v_xor_b32_e32 v254, v254, v253\n"
"v_mfma_f32_16x16x32_bf16 v[96:99], a[140:143], v[68:71], v[96:99]\n"
"s_waitcnt vmcnt(11)\n"
"buffer_load_dwordx4 v[124:127], v0, s[4:7], s97 offen offset:1024 sc1\n"
"v_mfma_f32_16x16x32_bf16 v[96:99], a[144:147], v[36:39], v[96:99]\n"
"v_lshlrev_b32_e32 v255, 7, v255\n"
"v_lshl_add_u32 v255, v254, 4, v255\n"
"v_mfma_f32_16x16x32_bf16 v[96:99], a[148:151], v[40:43], v[96:99]\n"
"s_waitcnt vmcnt(10)\n"
"v_bfe_u32 v254, v0, 8, 1\n"
"v_lshl_add_u32 v255, v254, 3, v255\n"
"v_mfma_f32_16x16x32_bf16 v[96:99], a[152:155], v[72:75], v[96:99]\n"
"buffer_load_dwordx4 v[128:131], v0, s[4:7], s97 offen offset:2048 sc1\n"
"v_mfma_f32_16x16x32_bf16 v[96:99], a[156:159], v[80:83], v[96:99]\n"
"v_add_u32_e32 v255, s77, v255\n"
"v_bfe_u32 v253, v0, 7, 3\n"
"v_mfma_f32_16x16x32_bf16 v[156:159], a[188:191], v[80:83], 0\n"
"v_bfe_u32 v254, v0, 4, 3\n"
"v_xor_b32_e32 v254, v254, v253\n"
"v_mfma_f32_16x16x32_bf16 v[156:159], a[184:187], v[72:75], v[156:159]\n"
"buffer_load_dwordx4 v[176:179], v0, s[4:7], s97 offen offset:3072 sc1\n"
"v_mfma_f32_16x16x32_bf16 v[156:159], a[180:183], v[40:43], v[156:159]\n"
"v_lshlrev_b32_e32 v253, 11, v253\n"
"v_lshl_add_u32 v253, v254, 4, v253\n"
"v_mfma_f32_16x16x32_bf16 v[156:159], a[176:179], v[36:39], v[156:159]\n"
"v_add_f32_e32 v204, v160, v204\n"
"v_add_f32_e32 v205, v161, v205\n"
"v_mfma_f32_16x16x32_bf16 v[156:159], a[172:175], v[68:71], v[156:159]\n"
"v_cmp_u_f32_e32 vcc, v96, v96\n"
"v_add_f32_e32 v206, v162, v206\n"
"v_mfma_f32_16x16x32_bf16 v[156:159], a[168:171], v[148:151], v[156:159]\n"
"s_cbranch_vccnz .Lmk_p64s\n"
".Lmk_p64r:\n"
"v_add_f32_e32 v207, v163, v207\n"
"v_mfma_f32_16x16x32_bf16 v[156:159], a[164:167], v[184:187], v[156:159]\n"
"ds_write_b128 v3, v[96:99]\n"
"v_mfma_f32_16x16x32_bf16 v[156:159], a[160:163], v[132:135], v[156:159]\n"
"buffer_load_dwordx4 v[96:99], v1, s[4:7], s97 offen sc1\n"
"v_mfma_f32_16x16x32_bf16 v[160:163], a[192:195], v[132:135], 0\n"
"v_add_f32_e32 v152, v52, v152\n"
"v_add_f32_e32 v153, v53, v153\n"
"v_mfma_f32_16x16x32_bf16 v[160:163], a[196:199], v[184:187], v[160:163]\n"
"v_add_f32_e32 v154, v54, v154\n"
"v_add_f32_e32 v155, v55, v155\n"
"v_mfma_f32_16x16x32_bf16 v[160:163], a[200:203], v[148:151], v[160:163]\n"
"buffer_load_dwordx4 v[52:55], v1, s[4:7], s97 offen offset:1024 sc1\n"
"v_mfma_f32_16x16x32_bf16 v[160:163], a[204:207], v[68:71], v[160:163]\n"
"v_cvt_pk_bf16_f32 v200, v204, v205\n"
"v_cvt_pk_bf16_f32 v201, v206, v207\n"
"v_mfma_f32_16x16x32_bf16 v[160:163], a[208:211], v[36:39], v[160:163]\n"
"v_cvt_pk_bf16_f32 v202, v152, v153\n"
"v_cvt_pk_bf16_f32 v203, v154, v155\n"
"v_mfma_f32_16x16x32_bf16 v[160:163], a[212:215], v[40:43], v[160:163]\n"
"ds_write_b128 v3, v[156:159] offset:1024\n"
"v_mfma_f32_16x16x32_bf16 v[160:163], a[216:219], v[72:75], v[160:163]\n"
"buffer_load_dwordx4 v[152:155], v1, s[4:7], s97 offen offset:2048 sc1\n"
"v_mfma_f32_16x16x32_bf16 v[160:163], a[220:223], v[80:83], v[160:163]\n"
"v_mov_b32_e32 v254, v255\n"
"v_mfma_f32_16x16x32_bf16 v[156:159], a[252:255], v[80:83], 0\n"
"ds_write_b64 v254, v[60:61] offset:8192\n"
"v_mfma_f32_16x16x32_bf16 v[156:159], a[248:251], v[72:75], v[156:159]\n"
"buffer_load_dwordx4 v[72:75], v1, s[4:7], s97 offen offset:3072 sc1\n"
"v_mfma_f32_16x16x32_bf16 v[156:159], a[244:247], v[40:43], v[156:159]\n"
"s_add_u32 s97, s59, 0x400000\n"
"v_xor_b32_e32 v254, 32, v255\n"
"v_mfma_f32_16x16x32_bf16 v[156:159], a[240:243], v[36:39], v[156:159]\n"
"ds_write_b64 v254, v[62:63] offset:8192\n"
"v_mfma_f32_16x16x32_bf16 v[156:159], a[236:239], v[68:71], v[156:159]\n"
"buffer_load_dwordx4 v[36:39], v0, s[4:7], s97 offen sc1\n"
"v_mfma_f32_16x16x32_bf16 v[156:159], a[232:235], v[148:151], v[156:159]\n"
"ds_write_b128 v3, v[160:163] offset:2048\n"
"v_mfma_f32_16x16x32_bf16 v[156:159], a[228:231], v[184:187], v[156:159]\n"
"v_xor_b32_e32 v254, 64, v255\n"
"v_mfma_f32_16x16x32_bf16 v[156:159], a[224:227], v[132:135], v[156:159]\n"
"s_waitcnt vmcnt(13)\n"
"buffer_load_dwordx4 v[40:43], v0, s[4:7], s97 offen offset:1024 sc1\n"
"; @B G2 a.1\n"
"v_mfma_f32_16x16x32_bf16 v[60:63], a[128:131], v[164:167], 0\n"
"ds_write_b64 v254, v[200:201] offset:8192\n"
"v_mfma_f32_16x16x32_bf16 v[60:63], a[132:135], v[168:171], v[60:63]\n"
"v_xor_b32_e32 v254, 0x60, v255\n"
"v_mfma_f32_16x16x32_bf16 v[60:63], a[136:139], v[208:211], v[60:63]\n"
"buffer_load_dwordx4 v[68:71], v0, s[4:7], s97 offen offset:2048 sc1\n"
"v_mfma_f32_16x16x32_bf16 v[60:63], a[140:143], v[212:215], v[60:63]\n"
"s_waitcnt vmcnt(11)\n"
"ds_write_b64 v254, v[202:203] offset:8192\n"
"v_mfma_f32_16x16x32_bf16 v[60:63], a[144:147], v[64:67], v[60:63]\n"
"ds_read_b128 v[80:83], v2 offset:8192\n"
"ds_read_b128 v[132:135], v2 offset:9216\n"
"v_mfma_f32_16x16x32_bf16 v[60:63], a[148:151], v[100:103], v[60:63]\n"
"ds_write_b128 v3, v[156:159] offset:3072\n"
"v_mfma_f32_16x16x32_bf16 v[60:63], a[152:155], v[84:87], v[60:63]\n"
"buffer_load_dwordx4 v[148:151], v0, s[4:7], s97 offen offset:3072 sc1\n"
"v_mfma_f32_16x16x32_bf16 v[60:63], a[156:159], v[76:79], v[60:63]\n"
"v_mfma_f32_16x16x32_bf16 v[156:159], a[188:191], v[76:79], 0\n"
"v_mfma_f32_16x16x32_bf16 v[156:159], a[184:187], v[84:87], v[156:159]\n"
"buffer_load_dwordx4 v[160:163], v1, s[4:7], s97 offen sc1\n"
"v_mfma_f32_16x16x32_bf16 v[156:159], a[180:183], v[100:103], v[156:159]\n"
"s_waitcnt lgkmcnt(1)\n"
"v_mfma_f32_16x16x32_bf16 v[156:159], a[176:179], v[64:67], v[156:159]\n"
"v_mfma_f32_16x16x32_bf16 v[156:159], a[172:175], v[212:215], v[156:159]\n"
"s_nop 0\n"
"v_cmp_u_f32_e32 vcc, v60, v60\n"
"v_mfma_f32_16x16x32_bf16 v[156:159], a[168:171], v[208:211], v[156:159]\n"
"s_cbranch_vccnz .Lmk_p65s\n"
".Lmk_p65r:\n"
"v_mfma_f32_16x16x32_bf16 v[156:159], a[164:167], v[168:171], v[156:159]\n"
"ds_write_b128 v3, v[60:63] offset:6144\n"
"v_mfma_f32_16x16x32_bf16 v[156:159], a[160:163], v[164:167], v[156:159]\n"
"buffer_load_dwordx4 v[60:63], v1, s[4:7], s97 offen offset:1024 sc1\n"
"v_mfma_f32_16x16x32_bf16 v[184:187], a[192:195], v[164:167], 0\n"
"v_mfma_f32_16x16x32_bf16 v[184:187], a[196:199], v[168:171], v[184:187]\n"
"v_mfma_f32_16x16x32_bf16 v[184:187], a[200:203], v[208:211], v[184:187]\n"
"buffer_load_dwordx4 v[200:203], v1, s[4:7], s97 offen offset:2048 sc1\n"
"v_mfma_f32_16x16x32_bf16 v[184:187], a[204:207], v[212:215], v[184:187]\n"
"v_mfma_f32_16x16x32_bf16 v[184:187], a[208:211], v[64:67], v[184:187]\n"
"ds_read_b128 v[204:207], v2\n"
"ds_read_b128 v[216:219], v2 offset:1024\n"
"v_mfma_f32_16x16x32_bf16 v[184:187], a[212:215], v[100:103], v[184:187]\n"
"ds_write_b128 v3, v[156:159] offset:7168\n"
"v_mfma_f32_16x16x32_bf16 v[184:187], a[216:219], v[84:87], v[184:187]\n"
"buffer_load_dwordx4 v[156:159], v1, s[4:7], s97 offen offset:3072 sc1\n"
"v_mfma_f32_16x16x32_bf16 v[184:187], a[220:223], v[76:79], v[184:187]\n"
"v_mfma_f32_16x16x32_bf16 v[220:223], a[252:255], v[76:79], 0\n"
"v_mfma_f32_16x16x32_bf16 v[220:223], a[248:251], v[84:87], v[220:223]\n"
"s_waitcnt lgkmcnt(1)\n"
"v_lshlrev_b32_e32 v255, 16, v204\n"
"v_and_b32_e32 v254, 0xffff0000, v204\n"
"v_lshlrev_b32_e32 v252, 16, v205\n"
"v_mfma_f32_16x16x32_bf16 v[220:223], a[244:247], v[100:103], v[220:223]\n"
"v_and_b32_e32 v251, 0xffff0000, v205\n"
"v_lshlrev_b32_e32 v250, 16, v206\n"
"v_and_b32_e32 v249, 0xffff0000, v206\n"
"v_mfma_f32_16x16x32_bf16 v[220:223], a[240:243], v[64:67], v[220:223]\n"
"v_lshlrev_b32_e32 v248, 16, v207\n"
"v_and_b32_e32 v247, 0xffff0000, v207\n"
"v_lshlrev_b32_e32 v246, 16, v216\n"
"v_mfma_f32_16x16x32_bf16 v[220:223], a[236:239], v[212:215], v[220:223]\n"
"s_add_u32 s97, s74, 0x40000\n"
"buffer_store_dwordx4 v[80:83], v253, s[16:19], s97 offen sc1\n"
"v_mfma_f32_16x16x32_bf16 v[220:223], a[232:235], v[208:211], v[220:223]\n"
"ds_write_b128 v3, v[184:187] offset:8192\n"
"v_mfma_f32_16x16x32_bf16 v[220:223], a[228:231], v[168:171], v[220:223]\n"
"v_and_b32_e32 v245, 0xffff0000, v216\n"
"v_lshlrev_b32_e32 v244, 16, v217\n"
"v_and_b32_e32 v243, 0xffff0000, v217\n"
"v_mfma_f32_16x16x32_bf16 v[220:223], a[224:227], v[164:167], v[220:223]\n"
"s_waitcnt vmcnt(13)\n"
"v_lshlrev_b32_e32 v242, 16, v218\n"
"v_and_b32_e32 v241, 0xffff0000, v218\n"
"v_lshlrev_b32_e32 v240, 16, v219\n"
"; @B G2 a.2\n"
"v_mfma_f32_16x16x32_bf16 v[64:67], a[128:131], v[116:119], 0\n"
"v_and_b32_e32 v239, 0xffff0000, v219\n"
"v_mfma_f32_16x16x32_bf16 v[64:67], a[132:135], v[124:127], v[64:67]\n"
"v_mfma_f32_16x16x32_bf16 v[64:67], a[136:139], v[128:131], v[64:67]\n"
"v_mfma_f32_16x16x32_bf16 v[64:67], a[140:143], v[176:179], v[64:67]\n"
"s_waitcnt vmcnt(9)\n"
"v_mfma_f32_16x16x32_bf16 v[64:67], a[144:147], v[96:99], v[64:67]\n"
"v_mfma_f32_16x16x32_bf16 v[64:67], a[148:151], v[52:55], v[64:67]\n"
"ds_write_b128 v3, v[220:223] offset:9216\n"
"v_mfma_f32_16x16x32_bf16 v[64:67], a[152:155], v[152:155], v[64:67]\n"
"s_add_u32 s97, s74, 0x44000\n"
"v_mfma_f32_16x16x32_bf16 v[64:67], a[156:159], v[72:75], v[64:67]\n"
"v_mfma_f32_16x16x32_bf16 v[76:79], a[188:191], v[72:75], 0\n"
"buffer_store_dwordx4 v[132:135], v253, s[16:19], s97 offen sc1\n"
"v_mfma_f32_16x16x32_bf16 v[76:79], a[184:187], v[152:155], v[76:79]\n"
"s_waitcnt lgkmcnt(0)\n"
"s_barrier\n"
"v_mfma_f32_16x16x32_bf16 v[76:79], a[180:183], v[52:55], v[76:79]\n"
"ds_read_b128 v[80:83], v4\n"
"ds_read_b128 v[84:87], v5\n"
"v_mfma_f32_16x16x32_bf16 v[76:79], a[176:179], v[96:99], v[76:79]\n"
"v_mfma_f32_16x16x32_bf16 v[76:79], a[172:175], v[176:179], v[76:79]\n"
"v_cmp_u_f32_e32 vcc, v64, v64\n"
"v_mfma_f32_16x16x32_bf16 v[76:79], a[168:171], v[128:131], v[76:79]\n"
"s_cbranch_vccnz .Lmk_p66s\n"
".Lmk_p66r:\n"
"v_mfma_f32_16x16x32_bf16 v[76:79], a[164:167], v[124:127], v[76:79]\n"
"ds_write_b128 v3, v[64:67] offset:12288\n"
"v_mfma_f32_16x16x32_bf16 v[76:79], a[160:163], v[116:119], v[76:79]\n"
"s_waitcnt lgkmcnt(1)\n"
"v_add_f32_e32 v80, v80, v255\n"
"v_add_f32_e32 v81, v81, v254\n"
"v_add_f32_e32 v82, v82, v252\n"
"v_mfma_f32_16x16x32_bf16 v[64:67], a[192:195], v[116:119], 0\n"
"ds_read_b128 v[100:103], v4 offset:1024\n"
"ds_read_b128 v[132:135], v5 offset:1024\n"
"v_add_f32_e32 v83, v83, v251\n"
"v_mfma_f32_16x16x32_bf16 v[64:67], a[196:199], v[124:127], v[64:67]\n"
"v_add_f32_e32 v80, v80, v84\n"
"v_add_f32_e32 v81, v81, v85\n"
"v_add_f32_e32 v82, v82, v86\n"
"v_mfma_f32_16x16x32_bf16 v[64:67], a[200:203], v[128:131], v[64:67]\n"
"v_add_f32_e32 v83, v83, v87\n"
"v_mfma_f32_16x16x32_bf16 v[64:67], a[204:207], v[176:179], v[64:67]\n"
"v_mfma_f32_16x16x32_bf16 v[64:67], a[208:211], v[96:99], v[64:67]\n"
"v_mfma_f32_16x16x32_bf16 v[64:67], a[212:215], v[52:55], v[64:67]\n"
"ds_write_b128 v3, v[76:79] offset:13312\n"
"v_mfma_f32_16x16x32_bf16 v[64:67], a[216:219], v[152:155], v[64:67]\n"
"ds_read_b128 v[76:79], v4 offset:2048\n"
"ds_read_b128 v[84:87], v5 offset:2048\n"
"s_waitcnt lgkmcnt(3)\n"
"v_mfma_f32_16x16x32_bf16 v[64:67], a[220:223], v[72:75], v[64:67]\n"
"v_add_f32_e32 v100, v100, v250\n"
"v_add_f32_e32 v101, v101, v249\n"
"v_add_f32_e32 v102, v102, v248\n"
"v_mfma_f32_16x16x32_bf16 v[164:167], a[252:255], v[72:75], 0\n"
"v_add_f32_e32 v103, v103, v247\n"
"v_add_f32_e32 v100, v100, v132\n"
"v_add_f32_e32 v101, v101, v133\n"
"v_mfma_f32_16x16x32_bf16 v[164:167], a[248:251], v[152:155], v[164:167]\n"
"v_add_f32_e32 v102, v102, v134\n"
"v_add_f32_e32 v103, v103, v135\n"
"v_mfma_f32_16x16x32_bf16 v[164:167], a[244:247], v[52:55], v[164:167]\n"
"v_mfma_f32_16x16x32_bf16 v[164:167], a[240:243], v[96:99], v[164:167]\n"
"s_waitcnt lgkmcnt(0)\n"
"v_add_f32_e32 v76, v76, v246\n"
"v_add_f32_e32 v77, v77, v245\n"
"v_add_f32_e32 v78, v78, v244\n"
"v_mfma_f32_16x16x32_bf16 v[164:167], a[236:239], v[176:179], v[164:167]\n"
"ds_read_b128 v[52:55], v4 offset:3072\n"
"ds_read_b128 v[72:75], v5 offset:3072\n"
"v_add_f32_e32 v79, v79, v243\n"
"v_mfma_f32_16x16x32_bf16 v[164:167], a[232:235], v[128:131], v[164:167]\n"
"ds_write_b128 v3, v[64:67] offset:14336\n"
"v_mfma_f32_16x16x32_bf16 v[164:167], a[228:231], v[124:127], v[164:167]\n"
"v_add_f32_e32 v76, v76, v84\n"
"v_add_f32_e32 v77, v77, v85\n"
"v_add_f32_e32 v78, v78, v86\n"
"v_mfma_f32_16x16x32_bf16 v[164:167], a[224:227], v[116:119], v[164:167]\n"
"s_waitcnt vmcnt(6)\n"
"v_add_f32_e32 v79, v79, v87\n"
"; @B G2 a.3\n"
"v_mfma_f32_16x16x32_bf16 v[64:67], a[128:131], v[36:39], v[80:83]\n"
"v_mfma_f32_16x16x32_bf16 v[64:67], a[132:135], v[40:43], v[64:67]\n"
"s_waitcnt lgkmcnt(1)\n"
"v_add_f32_e32 v52, v52, v242\n"
"v_add_f32_e32 v53, v53, v241\n"
"v_add_f32_e32 v54, v54, v240\n"
"v_mfma_f32_16x16x32_bf16 v[64:67], a[136:139], v[68:71], v[64:67]\n"
"v_add_f32_e32 v55, v55, v239\n"
"v_add_f32_e32 v52, v52, v72\n"
"v_add_f32_e32 v53, v53, v73\n"
"v_mfma_f32_16x16x32_bf16 v[64:67], a[140:143], v[148:151], v[64:67]\n"
"s_waitcnt vmcnt(2)\n"
"v_add_f32_e32 v54, v54, v74\n"
"v_add_f32_e32 v55, v55, v75\n"
"v_mfma_f32_16x16x32_bf16 v[64:67], a[144:147], v[160:163], v[64:67]\n"
"v_mfma_f32_16x16x32_bf16 v[64:67], a[148:151], v[60:63], v[64:67]\n"
"ds_write_b128 v3, v[164:167] offset:15360\n"
"v_mfma_f32_16x16x32_bf16 v[64:67], a[152:155], v[200:203], v[64:67]\n"
"v_mfma_f32_16x16x32_bf16 v[64:67], a[156:159], v[156:159], v[64:67]\n"
"v_mfma_f32_16x16x32_bf16 v[72:75], a[188:191], v[156:159], v[100:103]\n"
"v_mfma_f32_16x16x32_bf16 v[72:75], a[184:187], v[200:203], v[72:75]\n"
"s_waitcnt lgkmcnt(0)\n"
"s_barrier\n"
"v_mfma_f32_16x16x32_bf16 v[72:75], a[180:183], v[60:63], v[72:75]\n"
"ds_read_b128 v[84:87], v6\n"
"ds_read_b128 v[96:99], v6 offset:1024\n"
"v_mfma_f32_16x16x32_bf16 v[72:75], a[176:179], v[160:163], v[72:75]\n"
"ds_read_b128 v[116:119], v6 offset:2048\n"
"ds_read_b128 v[124:127], v6 offset:3072\n"
"v_mfma_f32_16x16x32_bf16 v[72:75], a[172:175], v[148:151], v[72:75]\n"
"v_cmp_u_f32_e32 vcc, v64, v64\n"
"v_mfma_f32_16x16x32_bf16 v[72:75], a[168:171], v[68:71], v[72:75]\n"
"s_cbranch_vccnz .Lmk_p67s\n"
".Lmk_p67r:\n"
"v_mfma_f32_16x16x32_bf16 v[72:75], a[164:167], v[40:43], v[72:75]\n"
"v_mfma_f32_16x16x32_bf16 v[72:75], a[160:163], v[36:39], v[72:75]\n"
"v_mfma_f32_16x16x32_bf16 v[76:79], a[192:195], v[36:39], v[76:79]\n"
"v_mfma_f32_16x16x32_bf16 v[76:79], a[196:199], v[40:43], v[76:79]\n"
"v_mfma_f32_16x16x32_bf16 v[76:79], a[200:203], v[68:71], v[76:79]\n"
"v_mfma_f32_16x16x32_bf16 v[76:79], a[204:207], v[148:151], v[76:79]\n"
"v_mfma_f32_16x16x32_bf16 v[76:79], a[208:211], v[160:163], v[76:79]\n"
"s_waitcnt lgkmcnt(0)\n"
"v_add_f32_e32 v84, v64, v84\n"
"v_add_f32_e32 v85, v65, v85\n"
"v_add_f32_e32 v86, v66, v86\n"
"v_mfma_f32_16x16x32_bf16 v[76:79], a[212:215], v[60:63], v[76:79]\n"
"v_add_f32_e32 v87, v67, v87\n"
"v_add_f32_e32 v96, v72, v96\n"
"v_add_f32_e32 v97, v73, v97\n"
"v_mfma_f32_16x16x32_bf16 v[76:79], a[216:219], v[200:203], v[76:79]\n"
"v_add_f32_e32 v98, v74, v98\n"
"v_add_f32_e32 v99, v75, v99\n"
"v_cvt_pk_bf16_f32 v64, v84, v85\n"
"v_mfma_f32_16x16x32_bf16 v[76:79], a[220:223], v[156:159], v[76:79]\n"
"v_cvt_pk_bf16_f32 v65, v86, v87\n"
"v_cvt_pk_bf16_f32 v66, v96, v97\n"
"v_cvt_pk_bf16_f32 v67, v98, v99\n"
"v_mfma_f32_16x16x32_bf16 v[52:55], a[252:255], v[156:159], v[52:55]\n"
"v_mfma_f32_16x16x32_bf16 v[52:55], a[248:251], v[200:203], v[52:55]\n"
"v_mfma_f32_16x16x32_bf16 v[52:55], a[244:247], v[60:63], v[52:55]\n"
"v_mfma_f32_16x16x32_bf16 v[52:55], a[240:243], v[160:163], v[52:55]\n"
"v_mfma_f32_16x16x32_bf16 v[52:55], a[236:239], v[148:151], v[52:55]\n"
"v_mfma_f32_16x16x32_bf16 v[52:55], a[232:235], v[68:71], v[52:55]\n"
"v_mfma_f32_16x16x32_bf16 v[52:55], a[228:231], v[40:43], v[52:55]\n"
"v_mfma_f32_16x16x32_bf16 v[52:55], a[224:227], v[36:39], v[52:55]\n"
"v_bfe_u32 v255, v0, 4, 4\n"
"v_bfe_u32 v254, v0, 9, 1\n"
"v_and_b32_e32 v253, 7, v255\n"
"v_xor_b32_e32 v254, v254, v253\n"
"v_lshlrev_b32_e32 v255, 7, v255\n"
"v_lshl_add_u32 v255, v254, 4, v255\n"
"v_bfe_u32 v254, v0, 8, 1\n"
"v_lshl_add_u32 v255, v254, 3, v255\n"
"v_add_u32_e32 v255, s77, v255\n"
"v_bfe_u32 v253, v0, 7, 3\n"
"v_bfe_u32 v254, v0, 4, 3\n"
"v_xor_b32_e32 v254, v254, v253\n"
"v_lshlrev_b32_e32 v253, 11, v253\n"
"v_lshl_add_u32 v253, v254, 4, v253\n"
"v_add_f32_e32 v116, v76, v116\n"
"v_add_f32_e32 v117, v77, v117\n"
"v_add_f32_e32 v118, v78, v118\n"
"v_add_f32_e32 v119, v79, v119\n"
"v_add_f32_e32 v124, v52, v124\n"
"v_add_f32_e32 v125, v53, v125\n"
"v_add_f32_e32 v126, v54, v126\n"
"v_add_f32_e32 v127, v55, v127\n"
"v_cvt_pk_bf16_f32 v36, v116, v117\n"
"v_cvt_pk_bf16_f32 v37, v118, v119\n"
"v_cvt_pk_bf16_f32 v38, v124, v125\n"
"v_cvt_pk_bf16_f32 v39, v126, v127\n"
"v_mov_b32_e32 v254, v255\n"
"ds_write_b64 v254, v[64:65] offset:0\n"
"v_xor_b32_e32 v254, 32, v255\n"
"ds_write_b64 v254, v[66:67] offset:0\n"
"v_xor_b32_e32 v254, 64, v255\n"
"ds_write_b64 v254, v[36:37] offset:0\n"
"v_xor_b32_e32 v254, 0x60, v255\n"
"ds_write_b64 v254, v[38:39] offset:0\n"
"ds_read_b128 v[36:39], v2\n"
"ds_read_b128 v[40:43], v2 offset:1024\n"
"s_waitcnt lgkmcnt(0)\n"
"s_nop 0\n"
"buffer_store_dwordx4 v[36:39], v253, s[16:19], s74 offen sc1\n"
"s_add_u32 s97, s74, 0x4000\n"
"buffer_store_dwordx4 v[40:43], v253, s[16:19], s97 offen sc1\n"
"s_cmp_eq_u32 s81, 0\n"
"s_cbranch_scc1 .Lmk_fin\n"
"buffer_store_dwordx2 v[18:19], v11, s[4:7], s81 offen sc1\n"
".Lmk_fin:\n"
"s_endpgm\n"
".Lmk_end:\n"
    :: "{s[4:7]}"(wsr), "{s[8:11]}"(wsr), "{s[12:15]}"(wsk), "{s[16:19]}"(outr), "{s[52:67]}"(sa), "{s[68:83]}"(sb),
       "{s[20:27]}"(pa), "{s[28:31]}"(wa), "{s[32:35]}"(wb), "{s[36:39]}"(oa), "{s[40:43]}"(ob), "{s[44:51]}"(pc), "{s99}"(uni((bix * 4 + w) * 1024)),
       "{v[0:15]}"(va), "{v[16:23]}"(vb), "{v[32:47]}"(pz), "{v48}"(sta), "{v50}"(pf)
    : "memory", "v255", "a255", "s98", "vcc");
  __builtin_unreachable();
}

}

extern "C" void kernel_launch(void* const* d_in, const int* in_sizes, int n_in, void* d_out, int out_size,
                              void* d_ws, size_t ws_size, hipStream_t stream) {
  (void)hipFuncSetAttribute((const void*)mega_kernel, hipFuncAttributeMaxDynamicSharedMemorySize, LDS_BYTES);
  mega_kernel<<<256, 256, LDS_BYTES, stream>>>((const u16*)d_in[0], (const u16*)d_in[1], (const u16*)d_in[2],
                                               (const u16*)d_in[4], (const u16*)d_in[5], (u16*)d_out, (char*)d_ws);
}
```

```cpp
#include <hip/hip_runtime.h>
#include <stdint.h>

namespace {

typedef unsigned short u16;
typedef unsigned long long u64;
typedef short s16x4 __attribute__((ext_vector_type(4)));
typedef float f32x4 __attribute__((ext_vector_type(4)));
typedef unsigned u32x4 __attribute__((ext_vector_type(4)));
typedef unsigned u32x2 __attribute__((ext_vector_type(2)));
typedef unsigned u32x8 __attribute__((ext_vector_type(8)));
typedef unsigned u32x16 __attribute__((ext_vector_type(16)));
typedef __amdgpu_buffer_rsrc_t rsrc_t;
#define LAS __attribute__((address_space(3)))
#define DI __device__ __forceinline__
#define LDSP(a) ((LAS char*)(size_t)(a))

constexpr unsigned MB = 1u << 20;
constexpr unsigned WS_XF = 0;
constexpr unsigned WS_OF = 16 * MB;
constexpr unsigned WS_KV = 32 * MB;
constexpr unsigned WS_WF = 64 * MB;
constexpr unsigned WS_WO = 76 * MB;
constexpr unsigned WS_ST = 80 * MB;
constexpr unsigned WS_CS = 81 * MB;
constexpr unsigned WS_FL = 82 * MB;
constexpr unsigned WS_EP = WS_FL + 2048 * 128;
constexpr unsigned WS_DBG = 96 * MB;
constexpr unsigned OOB = 0x80000000u;
constexpr int L_RES = 0, L_K = 16384, L_V = 49152, L_INB = 81920, L_CS = 155648, L_PM = L_CS + 1024, L_MB = L_PM + 2048, L_SCR = L_MB + 2048;
constexpr int L_WS = 57344;
constexpr int LDS_BYTES = 163840;

template <int N> DI void wait_vm() { asm volatile("s_waitcnt vmcnt(%0)" ::"n"(N) : "memory"); }
template <int N> DI void nops() { asm volatile("s_nop %0" ::"n"(N - 1)); }
template <int OFF> DI void ld8(u32x2& d, rsrc_t r, unsigned v, unsigned s) {
  asm volatile("buffer_load_dwordx2 %0, %1, %2, %3 offen offset:%4 sc1" : "=v"(d) : "v"(v), "s"(r), "s"(s), "i"(OFF) : "memory");
}
template <int OFF> DI void st16(const u32x4& d, rsrc_t r, unsigned v, unsigned s) {
  asm volatile("buffer_store_dwordx4 %0, %1, %2, %3 offen offset:%4 sc1\n\ts_nop 1" :: "v"(d), "v"(v), "s"(r), "s"(s), "i"(OFF) : "memory");
}
template <int OFF> DI void st8(const u32x2& d, rsrc_t r, unsigned v, unsigned s) {
  asm volatile("buffer_store_dwordx2 %0, %1, %2, %3 offen offset:%4 sc1" :: "v"(d), "v"(v), "s"(r), "s"(s), "i"(OFF) : "memory");
}
DI void st4(unsigned d, rsrc_t r, unsigned v, unsigned s) {
  asm volatile("buffer_store_dword %0, %1, %2, %3 offen sc1" :: "v"(d), "v"(v), "s"(r), "s"(s) : "memory");
}
DI void dma(rsrc_t r, unsigned v, unsigned s, unsigned lm) {
  asm volatile("s_mov_b32 m0, %3\n\ts_nop 0\n\tbuffer_load_dwordx4 %0, %1, %2 offen lds" :: "v"(v), "s"(r), "s"(s), "s"(lm) : "memory");
}
DI void touch(rsrc_t r, unsigned v) {
  asm volatile("s_mov_b32 m0, %2\n\ts_nop 0\n\tbuffer_load_dword %0, %1, 0 offen lds" :: "v"(v), "s"(r), "s"((unsigned)L_SCR) : "memory");
}
DI void mma_v(f32x4& c, const u32x4& a, const u32x4& b) { asm volatile("v_mfma_f32_16x16x32_bf16 %0, %1, %2, %0" : "+v"(c) : "v"(a), "v"(b)); }
DI void mm0_v(f32x4& c, const u32x4& a, const u32x4& b) { asm volatile("v_mfma_f32_16x16x32_bf16 %0, %1, %2, 0" : "=v"(c) : "v"(a), "v"(b)); }
DI unsigned uni(unsigned v) { v = __builtin_amdgcn_readfirstlane(v); asm volatile("" : "+s"(v)); return v; }
template <class T> DI void pin(T& v) { asm volatile("" : "+v"(v)); }
DI u32x4 cat(const u32x2& a, const u32x2& b) { return __builtin_shufflevector(a, b, 0, 1, 2, 3); }
DI u32x2 lds_r2(unsigned a) { return *(LAS u32x2*)LDSP(a); }
DI void lds_w4(unsigned a, const u32x4& v) { *(LAS u32x4*)LDSP(a) = v; }
DI unsigned rot(unsigned g, unsigned sl) { return (g + (0x641u >> 4 * sl)) & 15; }
DI int swzK(int u) { return ((((u >> 1) ^ u) & 1) << 1) | (u >> 1); }

#define STAMP(i)

__global__ __launch_bounds__(256, 1) void mega_kernel(const u16* __restrict__ x0, const u16* __restrict__ WqA,
                                                      const u16* __restrict__ WoA, const u16* __restrict__ WqB,
                                                      const u16* __restrict__ WoB, u16* __restrict__ out, char* ws) {
  const unsigned bid = blockIdx.x, w = __builtin_amdgcn_readfirstlane(threadIdx.x >> 6);
  unsigned lane = threadIdx.x & 63;
  unsigned stw = 0;
  STAMP(0);
  u32x2 tag;
  {
    u64 t0; unsigned t1, t2, t3, v1, v2;
    asm volatile(
      "s_getpc_b64 %[t0]\n.Lmk_here:\n"
      "s_lshr_b32 %[t1], %[bid], 3\n s_lshl2_add_u32 %[t1], %[t1], %[w]\n s_lshl_b32 %[t1], %[t1], 10\n"
      "v_lshlrev_b32 %[v1], 4, %[lane]\n"
      "s_mov_b64 s[88:89], %[t0]\n s_and_b32 s88, s88, -16\n s_and_b32 s89, s89, 0xffff\n s_mov_b32 s90, (.Lmk_end-.Lmk_here)&0xfffffff0\n s_mov_b32 s91, 0x20000\n"
      "s_mov_b32 m0, %[scr]\n"
      "v_lshrrev_b32 %[v2], 3, %[lane]\n"
      "buffer_load_dwordx4 %[v1], s[88:91], %[t1] offen lds\n"
      "v_and_b32 %[v1], 0x70, %[v1]\n v_lshl_or_b32 %[v1], %[v2], 11, %[v1]\n"
      "s_and_b32 %[t1], %[bid], 4\n s_lshr_b32 %[t2], %[bid], 6\n s_add_u32 %[t1], %[t1], %[t2]\n s_lshl3_add_u32 %[t1], %[t1], %[w]\n"
      "s_and_b32 %[t3], %[bid], 1\n s_lshl1_add_u32 %[t1], %[t1], %[t3]\n s_lshl_b32 %[t1], %[t1], 8\n"
      "s_bfe_u32 %[t3], %[bid], 0x30003\n s_add_u32 %[t1], %[t1], %[t3]\n s_and_b32 %[t3], %[bid], 2\n s_lshl2_add_u32 %[t1], %[t3], %[t1]\n s_lshl_b32 %[t1], %[t1], 7\n"
      "s_lshl_b32 %[t3], %[w], 11\n"
      "v_lshrrev_b32 %[v2], 2, %[lane]\n v_mul_u32_u24 %[v2], 0x1800, %[v2]\n"
      : [t0] "=&s"(t0), [t1] "=&s"(t1), [t2] "=&s"(t2), [t3] "=&s"(t3), [v1] "=&v"(v1), [v2] "=&v"(v2)
      : [bid] "s"(bid), [w] "s"(w), [lane] "v"(lane), [scr] "i"(L_SCR + 1024) : "memory", "s88", "s89", "s90", "s91", "scc");
    v2 += ((lane & 3) ^ ((lane >> 3) & 2)) * 16;
    const unsigned par_ = bid >> 3 & 1;
    const unsigned src = w * 0x180000 + (bid & 3) * 512 + (bid >> 3 & 7) * 64 + (2 * (bid >> 6) + par_) * 196608, dst = par_ ? L_INB + w * 18432 : L_WS + w * 6144;
    asm volatile(
      "s_mov_b64 s[88:89], %[x]\n s_and_b32 s89, s89, 0xffff\n s_mov_b32 s90, 0x400000\n s_mov_b32 s91, 0x20000\n"
      "s_add_u32 m0, %[t3], 0x2000\n s_add_u32 %[t2], %[t1], 0x40000\n buffer_load_dwordx4 %[v1], s[88:91], %[t2] offen lds\n"
      "s_add_u32 m0, %[t3], 0x2400\n s_add_u32 %[t2], %[t1], 0x44000\n buffer_load_dwordx4 %[v1], s[88:91], %[t2] offen lds\n"
      "s_mov_b32 m0, %[t3]\n s_add_u32 %[t2], %[t1], 0x4000\n buffer_load_dwordx4 %[v1], s[88:91], %[t1] offen lds\n"
      "s_add_u32 m0, %[t3], 0x400\n s_mov_b32 %[t1], %[ep]\n buffer_load_dwordx4 %[v1], s[88:91], %[t2] offen lds\n"
      "s_mov_b64 s[88:89], %[ws]\n s_and_b32 s89, s89, 0xffff\n s_mov_b32 s90, 0x8000000\n"
      "buffer_load_dwordx2 %[tag], off, s[88:91], %[t1] sc1\n"
      "s_mov_b64 s[88:89], %[wq]\n s_and_b32 s89, s89, 0xffff\n s_mov_b32 s90, 0x600000\n"
      "s_mov_b32 m0, %[dst]\n s_add_u32 %[t1], %[src], 0x18000\n buffer_load_dwordx4 %[v2], s[88:91], %[src] offen lds\n buffer_load_dwordx4 %[v2], s[88:91], %[src] offen offset:2048 lds\n"
      "s_add_u32 m0, %[dst], 0x400\n s_add_u32 %[t2], %[src], 0x1000\n buffer_load_dwordx4 %[v2], s[88:91], %[t1] offen lds\n buffer_load_dwordx4 %[v2], s[88:91], %[t1] offen offset:2048 lds\n"
      "s_add_u32 m0, %[dst], 0x1000\n s_add_u32 %[t1], %[src], 0x19000\n buffer_load_dwordx4 %[v2], s[88:91], %[t2] offen lds\n"
      "s_add_u32 m0, %[dst], 0x1400\n s_nop 0\n buffer_load_dwordx4 %[v2], s[88:91], %[t1] offen lds\n"
      : [t1] "+s"(t1), [t2] "+s"(t2), [tag] "=&v"(tag), [lane] "+v"(lane)
      : [t3] "s"(t3), [v1] "v"(v1), [v2] "v"(v2), [x] "s"(x0), [ws] "s"(ws), [wq] "s"(WqA), [src] "s"(src), [dst] "s"(dst), [ep] "i"(WS_EP),
        "s"(WoA), "s"(WqB), "s"(WoB), "s"(out) : "memory", "s88", "s89", "s90", "s91", "scc");
  }
  const unsigned xcd = bid & 7, bix = bid >> 3;
  const unsigned c = (xcd >> 2) * 4 + (bix >> 3), h = (xcd & 3) * 8 + (bix & 7), par = h & 1, g = h >> 1;
  const unsigned p2 = h >> 3 & 1, g2 = (h >> 4) * 8 + (h & 7);
  const unsigned fr = lane & 15, fq = lane >> 4;
  const unsigned dl = 0, wr = (w + dl) & 3;
  const rsrc_t wsr = __builtin_amdgcn_make_buffer_rsrc(ws, 0, 128 * MB, 0x00020000);
  const unsigned l16 = lane * 16, lw = w * 2048 + l16;
  const unsigned fl0 = lane == 0 ? 0u : OOB;
  const unsigned myfl = WS_FL + (c * 64 + h) * 128 + w * 8;
  const unsigned osrc0 = (lane >> 3) * 2048 + (lane & 7) * 16;
  const rsrc_t wa = __builtin_amdgcn_make_buffer_rsrc((void*)WqA, 0, 6 * MB, 0x00020000);
  const unsigned rho = 2 * (c & 3), wbase = w * 256 * 6144 + h * 64;
  const unsigned wv = (lane >> 2) * 6144 + ((lane & 3) ^ ((lane >> 3) & 2)) * 16;
  auto piece = [&](int q, int j) __attribute__((always_inline)) {
    const unsigned k = j ^ par, src = uni(wbase + (((rho + 2 * q) & 7) + k) * 196608), dst = uni(q + k ? L_INB + w * 18432 + (2 * q + k - 1) * 6144 : L_WS + w * 6144);
#pragma unroll
    for (int i = 0; i < 6; ++i) dma(wa, wv, src + (i & 1) * 98304 + (i >> 1) * 2048, dst + i * 1024);
  };
  const unsigned kb = 2 * c + par;
  const rsrc_t oa = __builtin_amdgcn_make_buffer_rsrc((void*)WoA, 0, 2 * MB, 0x00020000);
  const rsrc_t wb = __builtin_amdgcn_make_buffer_rsrc((void*)WqB, 0, 6 * MB, 0x00020000);
  const rsrc_t ob = __builtin_amdgcn_make_buffer_rsrc((void*)WoB, 0, 2 * MB, 0x00020000);
  const unsigned wsrc = (w * 16 + (lane >> 3)) * 6144 + (lane & 7) * 16, osrc = w * 16 * 2048 + osrc0;
  auto get_o = [&](int t) __attribute__((always_inline)) {
#pragma unroll
    for (int i = 0; i < 2; ++i) dma(t ? ob : oa, osrc, (kb * 64 + i * 8) * 2048 + rot(g, 1 + t) * 128, L_K + 24576 + t * 8192 + w * 2048 + i * 1024);
  };
  auto get_q = [&](int t) __attribute__((always_inline)) {
#pragma unroll
    for (int i = 0; i < 6; ++i)
      dma(t ? wb : wa, wsrc, (kb * 64 + (i & 1) * 8) * 6144 + (i >> 1) * 2048 + rot(g, i >> 1) * 128, (t ? L_K : L_INB) + (i >> 1) * 8192 + w * 2048 + (i & 1) * 1024);
  };
  const unsigned fr8 = fq == fr >> 2 ? 64 + fr * 4 : fq == ((fr >> 2) + 1 & 3) ? fr * 4 : OOB;
  u32x4 ones = {0x3F803F80u, 0x3F803F80u, 0x3F803F80u, 0x3F803F80u};
  unsigned sta = 0;
  auto publish = [&](int s, bool now) __attribute__((always_inline)) {
    const unsigned a = L_RES + s * 8192 + w * 2048 + fr * 128 + fq * 8, T = c * 16 + 8 * s + 2 * wr + p2;
    u32x4 y0 = cat(lds_r2(a), lds_r2(a + 32)), y1 = cat(lds_r2(a + 64), lds_r2(a + 96));
    f32x4 sm, gm;
    pin(y0); pin(y1); pin(ones);
    nops<2>();
    mm0_v(sm, ones, y0); mm0_v(gm, y0, y0); mma_v(sm, ones, y1); mma_v(gm, y1, y1);
    if (now) { st16<0>(y0, wsr, l16, WS_XF + T * 32768 + g2 * 2048); st16<1024>(y1, wsr, l16, WS_XF + T * 32768 + g2 * 2048); }
    lds_w4(lw + L_RES + s * 8192, y0); lds_w4(lw + L_RES + s * 8192 + 1024, y1);
    nops<8>();
    pin(sm); pin(gm);
    const unsigned st = __float_as_uint(fq != fr >> 2 ? sm[0] : fr & 2 ? (fr & 1 ? gm[3] : gm[2]) : (fr & 1 ? gm[1] : gm[0]));
    if (now) st4(st, wsr, fr8, WS_ST + T * 2048 + g2 * 128); else sta = st;
  };
  auto next_tag = [&]() __attribute__((always_inline)) {
    pin(tag);
    const u64 t = (((u64)tag[1] << 32) | tag[0]) + 1; tag = (u32x2){(unsigned)t, (unsigned)(t >> 32)};
  };
  const unsigned cb = c * 16 * 32768, skv = WS_KV + (c * 32 + h) * 32768;
  const unsigned soo = WS_OF + cb + wr * 65536 + h * 1024, sxo = WS_XF + cb + (2 * wr + p2) * 32768 + g2 * 2048, sso = WS_ST + (c * 16 + 2 * wr + p2) * 2048 + g2 * 128;
  auto clear = [&](int from, int to) __attribute__((always_inline)) {
    u32x4 z = {~0u, ~0u, ~0u, ~0u};
    pin(z);
#pragma unroll
    for (int k = from; k < to; ++k) {
      if (k < 8) st16<0>(z, wsr, l16, soo + (k == 4 ? 9 : k == 5 ? 8 : k - 6) * 32768);
      else if (k < 10) st16<0>(z, wsr, l16, sxo + 4 * MB + 8 * 32768 + (k - 8) * 1024);
      else st4(z[0], wsr, fr8, sso + 136 * 2048);
    }
  };
  ;
  touch(wsr, lane < 4 ? WS_FL + (lane >> 1) * 524288 + (w * 512 + c * 64 + (lane & 1) * 32 + h) * 128 : OOB);
  STAMP(29);

  unsigned ps[16];
  u32x8 pv = {};
#pragma unroll
  for (int j = 0; j < 3; ++j) {
    const unsigned ct = (w & 1) * 3 + j;
    pv[1 + j] = L_K + (ct >> 1) * 8192 + (4 * fq + (fr >> 2)) * 128 + (w >> 1) * 64 + (ct & 1) * 32 + (lane & 3) * 8;
    ps[j] = WS_WF + 6 * MB + ((2 * rot(g, ct >> 1) + (w >> 1)) * 192 + (kb >> 2) * 48 + (ct + 4) % 6 * 8 + (kb & 3) * 2) * 1024;
    ps[10 + j] = kb * 64 * 6144 + j * 2048 + rot(g, j) * 128;
  }
  { const unsigned ct = (w & 1) * 3 + (fr >> 1);
    pv[4] = ((2 * rot(g, ct >> 1) + (w >> 1)) * 16384 + ct * 128 + (fr & 1) * 64 + fq * 16) | (fr < 6 ? 0u : OOB); }
  ps[3] = WS_CS + 524288 + kb * 1024;
  ps[6] = (c >> 2) | par << 1 | (bix * 4 + w) << 10;                                            ps[7] = L_WS + w * 6144;
  ps[8] = wbase;                                             ps[9] = rho;
  ps[13] = kb * 64 * 2048 + rot(g, 1) * 128;                 ps[14] = kb * 64 * 2048 + rot(g, 2) * 128;
  ps[15] = WS_WF + (h * 4 + w) * 49152 + (rho + (c >> 2)) * 1024;
  pv[5] = wsrc;                                              pv[6] = osrc;
  u32x16 pw = {wv, L_WS + w * 6144 + (4 * fq + (fr >> 2)) * 64 + (fq & 1) * 32 + (lane & 3) * 8,
                    l16 + rho * 1024, l16 + ((rho + 2) & 7) * 1024, l16 + ((rho + 4) & 7) * 1024, l16 + ((rho + 6) & 7) * 1024,
                    L_WS + w * 6144 + (fr == 0 ? fq * 16 : 1024 + l16), L_WS + (w * 24 + (lane < 24 ? lane : 0)) * 4,
                    (w * 128 + (lane >> 3 & 1) * 64 + 2 * ((g2 - rot(0, 1 + (fq & 1))) & 15) + (lane >> 2 & 1)) * 128 + (lane & 3) * 8};
  if (w < 2) {
    u32x4 pm;
#pragma unroll
    for (int k = 0; k < 4; ++k)
      pm[k] = (8 * fq + 2 * k > 16 * w + fr ? 0xFFFFu : 0u) | (8 * fq + 2 * k + 1 > 16 * w + fr ? 0xFFFF0000u : 0u);
    lds_w4(L_PM + w * 1024 + l16, pm);
  } else {
    const unsigned want = (unsigned)((0x5555ull << 16) >> (fr + 25 - 8 * fq));
    u32x4 mb;
#pragma unroll
    for (int k = 0; k < 4; ++k) mb[k] = (want >> (15 - 4 * w - k)) & 1 ? 0u : 0xFF800000u;
    lds_w4(L_MB + (w - 2) * 1024 + l16, mb);
  }
  pv[0] = L_K + 24576 + (16 * (fq & 1) + 4 * (fq >> 1) + (fr >> 2)) * 128 + w * 32 + (lane & 3) * 8;
  pv[7] = lane < 2 ? lane * 32 * 128 : OOB;
  ps[4] = WS_WO + (rot(g, 1) * 128 + (kb >> 2) * 32 + w * 8 + (kb & 3) * 2) * 1024;
  ps[5] = WS_WO + 2 * MB + (rot(g, 2) * 128 + (kb >> 2) * 32 + w * 8 + (kb & 3) * 2) * 1024;
  const unsigned krow = (8 * (fr >> 2) + (fr & 3)) * 64 + wr * 2048 + L_K;
  const unsigned kv0 = 16384 + wr * 2048 + fr * 64 + ((fq ^ ((fr >> 3) << 1) ^ ((fr >> 1) & 3)) * 16);
  u32x16 va = {l16, l16 + 4096, lw, L_INB + w * 18432 + l16, L_INB + ((w + 3) & 3) * 18432 + l16, L_INB + ((w + 2) & 3) * 18432 + 6144 + l16,
                     L_INB + ((w + 1) & 3) * 18432 + 12288 + l16, kv0, kv0 ^ 48, L_CS + fq * 16, lane * 4, fl0,
                     ((lane & 31) >> 2) * 128 + (lane & 3) * 8,
                     fq < 3 ? (w * 128 + (lane >> 3 & 1) * 64 + 2 * ((2 * g - rot(g, fq % 3)) & 15) + (lane >> 2 & 1)) * 128 + (lane & 3) * 8 : ((lane & 7) * 64 + h) * 128 + w * 8,
                     fr8, krow + (fq ^ swzK(fr >> 2) ^ ((fr >> 1) & 1)) * 16};
  u32x8 vb = {L_V + wr * 2048 + (8 * fq + (fr >> 2)) * 64 + 8 * (lane & 1) + (((lane >> 1) & 1) ^ ((fq & 1) << 1) ^ (fr >> 3)) * 16,
                    krow + (fq ^ swzK(((fr >> 2) + 2) & 3) ^ ((fr >> 1) & 1)) * 16, 0, 0, 0,
                    lane < 24 ? L_CS + w * 96 + lane * 4 : L_SCR, w == 0 ? bid * 1024 + lane * 4 : OOB, 0};
  u32x16 pz = {pw[2], pw[3], pw[4], pw[5], pv[1], pv[2], pv[3], pv[4], pv[0], pw[8], pw[0], pw[1], pw[6], pw[7], pv[5], pv[6]};
  unsigned pf = pv[7];
  pin(va); pin(vb); pin(pz); pin(pf);
  piece(0, 1);
  wait_vm<13>();
  next_tag();
  STAMP(18);
  publish(1, true);
  publish(0, true);
  STAMP(1);
  piece(1, 0);
  wait_vm<6>();
  st8<0>(tag, wsr, lane < 2 ? lane * 32 * 128 : OOB, myfl + (2 * g2 + p2 - h) * 128);
  piece(1, 1);
  STAMP(15);

  u32x16 sa, sb;
#pragma unroll
  for (int i = 0; i < 4; ++i) {
    sa[i] = WS_XF + cb + w * 8192 + ((wr + 1 + i) & 3) * 65536;
    sa[4 + i] = sa[i] + WS_OF + p2 * 32768;
  }
  sa[8] = soo;                                              sa[9] = sxo;
  sa[10] = WS_ST + (c * 16 + 2 * wr) * 2048;                sa[11] = sso;
  sa[12] = skv - 16384;                                     sa[13] = c ? skv - 32 * 32768 : WS_XF + (h & 15) * 32768;
  sa[14] = c ? skv - 32 * 32768 + 8 * MB : sa[13];          sa[15] = WS_FL + (c * 64 + 8 * w) * 128;
  sb[0] = myfl;                                             sb[1] = WS_WF + (h * 4 + w) * 49152;
  sb[2] = WS_WO + (g2 * 4 + w) * 32768;                     sb[3] = c ? myfl - w * 8 + ((w - dl) & 3) * 8 - 32 * 128 + 64 : myfl + 4096;
  sb[4] = w * 96;                                           sb[5] = c ? myfl + 65536 - w * 8 + ((3 - dl) & 3) * 8 - 32 * 128 + 64 : myfl + 4096;
  sb[6] = ((c * 16 + 2 * wr + p2) * 16 * 1024 + g2 * 64) * 2;  sb[7] = WS_FL + 96;
  sb[8] = WS_CS + h * 16384;                                sb[9] = w * 2048;
  sb[10] = c ? -8 : 8 - wr;                                  sb[11] = c ? 16 * MB : 0;
  sb[12] = (2 * g2 + p2 - h) * 128;                                sb[13] = bid == 0 && w == 0 ? WS_EP : 0;
  sb[14] = c == 0 && wr == 0 ? ~0u : 0u;                     sb[15] = 0;
#pragma unroll
  for (int i = 0; i < 16; ++i) { sa[i] = uni(sa[i]); sb[i] = uni(sb[i]); }
  const rsrc_t wsk = __builtin_amdgcn_make_buffer_rsrc(ws, 0, wr == 3 ? 128 * MB : 0u, 0x00020000);
  const rsrc_t outr = __builtin_amdgcn_make_buffer_rsrc(out, 0, 4 * MB, 0x00020000);
  u32x8 pa, pc;
#pragma unroll
  for (int i = 0; i < 8; ++i) { pa[i] = uni(ps[i]); pc[i] = uni(ps[8 + i]); }
  STAMP(17);

  vb[2] = tag[0]; vb[3] = tag[1];
  STAMP(19);
  vb[7] = stw;
  asm volatile(
"; @entry\n"
"s_mov_b32 s0, 0\n"
"s_mov_b32 s3, 0\n"
"s_mul_i32 s98, s77, 6\n"
"s_add_u32 s98, s98, 0x6000\n"
"v_xor_b32_e32 v255, 32, v43\n"
"v_add_u32_e32 v254, s98, v43\n"
"v_add_u32_e32 v253, s98, v255\n"
"v_mov_b32_e32 v24, 0x3f803f80\n"
"v_mov_b32_e32 v25, 0x3f803f80\n"
"v_mov_b32_e32 v26, 0x3f803f80\n"
"v_mov_b32_e32 v27, 0x3f803f80\n"
"s_waitcnt lgkmcnt(0)\n"
"s_waitcnt vmcnt(19)\n"
"ds_read_b64_tr_b16 v[28:29], v43\n"
"ds_read_b64_tr_b16 v[30:31], v43 offset:1024\n"
"ds_read_b64_tr_b16 v[48:49], v255\n"
"ds_read_b64_tr_b16 v[50:51], v255 offset:1024\n"
"ds_read_b64_tr_b16 a[0:1], v43 offset:2048\n"
"ds_read_b64_tr_b16 a[2:3], v43 offset:3072\n"
"ds_read_b64_tr_b16 a[32:33], v255 offset:2048\n"
"ds_read_b64_tr_b16 a[34:35], v255 offset:3072\n"
"ds_read_b64_tr_b16 a[64:65], v43 offset:4096\n"
"ds_read_b64_tr_b16 a[66:67], v43 offset:5120\n"
"ds_read_b64_tr_b16 a[96:97], v255 offset:4096\n"
"ds_read_b64_tr_b16 a[98:99], v255 offset:5120\n"
"ds_read_b64_tr_b16 v[52:53], v254\n"
"ds_read_b64_tr_b16 v[54:55], v254 offset:1024\n"
"ds_read_b64_tr_b16 v[56:57], v253\n"
"s_waitcnt lgkmcnt(13)\n"
"ds_read_b64_tr_b16 v[58:59], v253 offset:1024\n"
"ds_read_b64_tr_b16 a[4:5], v254 offset:2048\n"
"s_waitcnt lgkmcnt(13)\n"
"ds_read_b64_tr_b16 a[6:7], v254 offset:3072\n"
"ds_read_b64_tr_b16 a[36:37], v253 offset:2048\n"
"s_waitcnt lgkmcnt(13)\n"
"ds_read_b64_tr_b16 a[38:39], v253 offset:3072\n"
"ds_read_b64_tr_b16 a[68:69], v254 offset:4096\n"
"s_waitcnt lgkmcnt(13)\n"
"ds_read_b64_tr_b16 a[70:71], v254 offset:5120\n"
"ds_read_b64_tr_b16 a[100:101], v253 offset:4096\n"
"s_waitcnt lgkmcnt(13)\n"
"ds_read_b64_tr_b16 a[102:103], v253 offset:5120\n"
"s_waitcnt lgkmcnt(12)\n"
"v_mfma_f32_16x16x32_bf16 v[60:63], v[28:31], v[24:27], 0\n"
"v_mfma_f32_16x16x32_bf16 v[64:67], v[48:51], v[24:27], 0\n"
"v_mfma_f32_16x16x32_bf16 v[68:71], a[0:3], v[24:27], 0\n"
"v_mfma_f32_16x16x32_bf16 v[72:75], a[32:35], v[24:27], 0\n"
"v_mfma_f32_16x16x32_bf16 v[76:79], a[64:67], v[24:27], 0\n"
"v_mfma_f32_16x16x32_bf16 v[80:83], a[96:99], v[24:27], 0\n"
"s_waitcnt lgkmcnt(0)\n"
"v_mfma_f32_16x16x32_bf16 v[60:63], v[52:55], v[24:27], v[60:63]\n"
"v_mfma_f32_16x16x32_bf16 v[64:67], v[56:59], v[24:27], v[64:67]\n"
"v_mfma_f32_16x16x32_bf16 v[68:71], a[4:7], v[24:27], v[68:71]\n"
"v_mfma_f32_16x16x32_bf16 v[72:75], a[36:39], v[24:27], v[72:75]\n"
"v_mfma_f32_16x16x32_bf16 v[76:79], a[68:71], v[24:27], v[76:79]\n"
"v_mfma_f32_16x16x32_bf16 v[80:83], a[100:103], v[24:27], v[80:83]\n"
"s_bfe_u32 s98, s26, 0x10001\n"
"s_xor_b32 s98, s98, 0\n"
"s_mul_i32 s97, s77, 9\n"
"s_add_u32 s97, s97, 0x14000\n"
"s_cmp_eq_u32 s98, 0\n"
"s_cselect_b32 s97, s27, s97\n"
"s_add_u32 s96, s45, 4\n"
"s_and_b32 s96, s96, 7\n"
"s_add_u32 s96, s96, s98\n"
"s_mul_i32 s96, s96, 0x30000\n"
"s_add_u32 s96, s96, s44\n"
"s_add_u32 m0, s97, 0\n"
"s_add_u32 s98, s96, 0\n"
"buffer_load_dwordx4 v42, s[28:31], s98 offen lds\n"
"s_add_u32 m0, s97, 0x400\n"
"s_add_u32 s98, s96, 0x18000\n"
"buffer_load_dwordx4 v42, s[28:31], s98 offen lds\n"
"s_add_u32 m0, s97, 0x800\n"
"s_add_u32 s98, s96, 0x800\n"
"buffer_load_dwordx4 v42, s[28:31], s98 offen lds\n"
"s_add_u32 m0, s97, 0xc00\n"
"s_add_u32 s98, s96, 0x18800\n"
"buffer_load_dwordx4 v42, s[28:31], s98 offen lds\n"
"s_add_u32 m0, s97, 0x1000\n"
"s_add_u32 s98, s96, 0x1000\n"
"buffer_load_dwordx4 v42, s[28:31], s98 offen lds\n"
"s_add_u32 m0, s97, 0x1400\n"
"s_add_u32 s98, s96, 0x19000\n"
"buffer_load_dwordx4 v42, s[28:31], s98 offen lds\n"
"s_bfe_u32 s98, s26, 0x10001\n"
"s_xor_b32 s98, s98, 1\n"
"s_mul_i32 s97, s77, 9\n"
"s_add_u32 s97, s97, 0x14000\n"
"s_cmp_eq_u32 s98, 0\n"
"s_cselect_b32 s97, s27, s97\n"
"s_add_u32 s96, s45, 4\n"
"s_and_b32 s96, s96, 7\n"
"s_add_u32 s96, s96, s98\n"
"s_mul_i32 s96, s96, 0x30000\n"
"s_add_u32 s96, s96, s44\n"
"s_add_u32 m0, s97, 0\n"
"s_add_u32 s98, s96, 0\n"
"buffer_load_dwordx4 v42, s[28:31], s98 offen lds\n"
"s_add_u32 m0, s97, 0x400\n"
"s_add_u32 s98, s96, 0x18000\n"
"buffer_load_dwordx4 v42, s[28:31], s98 offen lds\n"
"s_add_u32 m0, s97, 0x800\n"
"s_add_u32 s98, s96, 0x800\n"
"buffer_load_dwordx4 v42, s[28:31], s98 offen lds\n"
"s_add_u32 m0, s97, 0xc00\n"
"s_add_u32 s98, s96, 0x18800\n"
"buffer_load_dwordx4 v42, s[28:31], s98 offen lds\n"
"s_add_u32 m0, s97, 0x1000\n"
"s_add_u32 s98, s96, 0x1000\n"
"buffer_load_dwordx4 v42, s[28:31], s98 offen lds\n"
"s_add_u32 m0, s97, 0x1400\n"
"s_add_u32 s98, s96, 0x19000\n"
"buffer_load_dwordx4 v42, s[28:31], s98 offen lds\n"
"s_waitcnt vmcnt(12)\n"
"ds_read_b64_tr_b16 v[84:85], v254 offset:6144\n"
"ds_read_b64_tr_b16 v[86:87], v254 offset:7168\n"
"ds_read_b64_tr_b16 v[88:89], v253 offset:6144\n"
"ds_read_b64_tr_b16 v[90:91], v253 offset:7168\n"
"ds_read_b64_tr_b16 a[8:9], v254 offset:8192\n"
"ds_read_b64_tr_b16 a[10:11], v254 offset:9216\n"
"ds_read_b64_tr_b16 a[40:41], v253 offset:8192\n"
"ds_read_b64_tr_b16 a[42:43], v253 offset:9216\n"
"ds_read_b64_tr_b16 a[72:73], v254 offset:10240\n"
"ds_read_b64_tr_b16 a[74:75], v254 offset:11264\n"
"ds_read_b64_tr_b16 a[104:105], v253 offset:10240\n"
"ds_read_b64_tr_b16 a[106:107], v253 offset:11264\n"
"ds_read_b64_tr_b16 v[92:93], v254 offset:12288\n"
"ds_read_b64_tr_b16 v[94:95], v254 offset:13312\n"
"ds_read_b64_tr_b16 v[96:97], v253 offset:12288\n"
"s_waitcnt lgkmcnt(13)\n"
"ds_read_b64_tr_b16 v[98:99], v253 offset:13312\n"
"ds_read_b64_tr_b16 a[12:13], v254 offset:14336\n"
"s_waitcnt lgkmcnt(13)\n"
"ds_read_b64_tr_b16 a[14:15], v254 offset:15360\n"
"ds_read_b64_tr_b16 a[44:45], v253 offset:14336\n"
"s_waitcnt lgkmcnt(13)\n"
"ds_read_b64_tr_b16 a[46:47], v253 offset:15360\n"
"ds_read_b64_tr_b16 a[76:77], v254 offset:16384\n"
"s_waitcnt lgkmcnt(13)\n"
"ds_read_b64_tr_b16 a[78:79], v254 offset:17408\n"
"ds_read_b64_tr_b16 a[108:109], v253 offset:16384\n"
"s_waitcnt lgkmcnt(13)\n"
"ds_read_b64_tr_b16 a[110:111], v253 offset:17408\n"
"s_waitcnt lgkmcnt(12)\n"
"v_mfma_f32_16x16x32_bf16 v[60:63], v[84:87], v[24:27], v[60:63]\n"
"v_mfma_f32_16x16x32_bf16 v[64:67], v[88:91], v[24:27], v[64:67]\n"
"v_mfma_f32_16x16x32_bf16 v[68:71], a[8:11], v[24:27], v[68:71]\n"
"v_mfma_f32_16x16x32_bf16 v[72:75], a[40:43], v[24:27], v[72:75]\n"
"v_mfma_f32_16x16x32_bf16 v[76:79], a[72:75], v[24:27], v[76:79]\n"
"v_mfma_f32_16x16x32_bf16 v[80:83], a[104:107], v[24:27], v[80:83]\n"
"s_waitcnt lgkmcnt(0)\n"
"v_mfma_f32_16x16x32_bf16 v[60:63], v[92:95], v[24:27], v[60:63]\n"
"v_mfma_f32_16x16x32_bf16 v[64:67], v[96:99], v[24:27], v[64:67]\n"
"v_mfma_f32_16x16x32_bf16 v[68:71], a[12:15], v[24:27], v[68:71]\n"
"v_mfma_f32_16x16x32_bf16 v[72:75], a[44:47], v[24:27], v[72:75]\n"
"v_mfma_f32_16x16x32_bf16 v[76:79], a[76:79], v[24:27], v[76:79]\n"
"v_mfma_f32_16x16x32_bf16 v[80:83], a[108:111], v[24:27], v[80:83]\n"
"s_bfe_u32 s98, s26, 0x10001\n"
"s_xor_b32 s98, s98, 0\n"
"s_mul_i32 s97, s77, 9\n"
"s_add_u32 s97, s97, 0x15800\n"
"s_mul_i32 s96, s98, 0x1800\n"
"s_add_u32 s97, s97, s96\n"
"s_add_u32 s96, s45, 6\n"
"s_and_b32 s96, s96, 7\n"
"s_add_u32 s96, s96, s98\n"
"s_mul_i32 s96, s96, 0x30000\n"
"s_add_u32 s96, s96, s44\n"
"s_add_u32 m0, s97, 0\n"
"s_add_u32 s98, s96, 0\n"
"buffer_load_dwordx4 v42, s[28:31], s98 offen lds\n"
"s_add_u32 m0, s97, 0x400\n"
"s_add_u32 s98, s96, 0x18000\n"
"buffer_load_dwordx4 v42, s[28:31], s98 offen lds\n"
"s_add_u32 m0, s97, 0x800\n"
"s_add_u32 s98, s96, 0x800\n"
"buffer_load_dwordx4 v42, s[28:31], s98 offen lds\n"
"s_add_u32 m0, s97, 0xc00\n"
"s_add_u32 s98, s96, 0x18800\n"
"buffer_load_dwordx4 v42, s[28:31], s98 offen lds\n"
"s_add_u32 m0, s97, 0x1000\n"
"s_add_u32 s98, s96, 0x1000\n"
"buffer_load_dwordx4 v42, s[28:31], s98 offen lds\n"
"s_add_u32 m0, s97, 0x1400\n"
"s_add_u32 s98, s96, 0x19000\n"
"buffer_load_dwordx4 v42, s[28:31], s98 offen lds\n"
"s_bfe_u32 s98, s26, 0x10001\n"
"s_xor_b32 s98, s98, 1\n"
"s_mul_i32 s97, s77, 9\n"
"s_add_u32 s97, s97, 0x15800\n"
"s_mul_i32 s96, s98, 0x1800\n"
"s_add_u32 s97, s97, s96\n"
"s_add_u32 s96, s45, 6\n"
"s_and_b32 s96, s96, 7\n"
"s_add_u32 s96, s96, s98\n"
"s_mul_i32 s96, s96, 0x30000\n"
"s_add_u32 s96, s96, s44\n"
"s_add_u32 m0, s97, 0\n"
"s_add_u32 s98, s96, 0\n"
"buffer_load_dwordx4 v42, s[28:31], s98 offen lds\n"
"s_add_u32 m0, s97, 0x400\n"
"s_add_u32 s98, s96, 0x18000\n"
"buffer_load_dwordx4 v42, s[28:31], s98 offen lds\n"
"s_add_u32 m0, s97, 0x800\n"
"s_add_u32 s98, s96, 0x800\n"
"buffer_load_dwordx4 v42, s[28:31], s98 offen lds\n"
"s_add_u32 m0, s97, 0xc00\n"
"s_add_u32 s98, s96, 0x18800\n"
"buffer_load_dwordx4 v42, s[28:31], s98 offen lds\n"
"s_add_u32 m0, s97, 0x1000\n"
"s_add_u32 s98, s96, 0x1000\n"
"buffer_load_dwordx4 v42, s[28:31], s98 offen lds\n"
"s_add_u32 m0, s97, 0x1400\n"
"s_add_u32 s98, s96, 0x19000\n"
"buffer_load_dwordx4 v42, s[28:31], s98 offen lds\n"
"s_add_u32 s98, s67, 0x1000\n"
"buffer_load_dwordx2 v[250:251], v12, s[4:7], s98 offen sc1\n"
"s_waitcnt vmcnt(13)\n"
"ds_read_b64_tr_b16 v[100:101], v43\n"
"ds_read_b64_tr_b16 v[102:103], v43 offset:1024\n"
"ds_read_b64_tr_b16 v[104:105], v255\n"
"ds_read_b64_tr_b16 v[106:107], v255 offset:1024\n"
"ds_read_b64_tr_b16 a[16:17], v43 offset:2048\n"
"ds_read_b64_tr_b16 a[18:19], v43 offset:3072\n"
"ds_read_b64_tr_b16 a[48:49], v255 offset:2048\n"
"ds_read_b64_tr_b16 a[50:51], v255 offset:3072\n"
"ds_read_b64_tr_b16 a[80:81], v43 offset:4096\n"
"ds_read_b64_tr_b16 a[82:83], v43 offset:5120\n"
"ds_read_b64_tr_b16 a[112:113], v255 offset:4096\n"
"ds_read_b64_tr_b16 a[114:115], v255 offset:5120\n"
"ds_read_b64_tr_b16 v[108:109], v254\n"
"ds_read_b64_tr_b16 v[110:111], v254 offset:1024\n"
"ds_read_b64_tr_b16 v[112:113], v253\n"
"s_waitcnt lgkmcnt(13)\n"
"ds_read_b64_tr_b16 v[114:115], v253 offset:1024\n"
"ds_read_b64_tr_b16 a[20:21], v254 offset:2048\n"
"s_waitcnt lgkmcnt(13)\n"
"ds_read_b64_tr_b16 a[22:23], v254 offset:3072\n"
"ds_read_b64_tr_b16 a[52:53], v253 offset:2048\n"
"s_waitcnt lgkmcnt(13)\n"
"ds_read_b64_tr_b16 a[54:55], v253 offset:3072\n"
"ds_read_b64_tr_b16 a[84:85], v254 offset:4096\n"
"s_waitcnt lgkmcnt(13)\n"
"ds_read_b64_tr_b16 a[86:87], v254 offset:5120\n"
"ds_read_b64_tr_b16 a[116:117], v253 offset:4096\n"
"s_waitcnt lgkmcnt(13)\n"
"ds_read_b64_tr_b16 a[118:119], v253 offset:5120\n"
"s_waitcnt lgkmcnt(12)\n"
"v_mfma_f32_16x16x32_bf16 v[60:63], v[100:103], v[24:27], v[60:63]\n"
"v_mfma_f32_16x16x32_bf16 v[64:67], v[104:107], v[24:27], v[64:67]\n"
"v_mfma_f32_16x16x32_bf16 v[68:71], a[16:19], v[24:27], v[68:71]\n"
"v_mfma_f32_16x16x32_bf16 v[72:75], a[48:51], v[24:27], v[72:75]\n"
"v_mfma_f32_16x16x32_bf16 v[76:79], a[80:83], v[24:27], v[76:79]\n"
"v_mfma_f32_16x16x32_bf16 v[80:83], a[112:115], v[24:27], v[80:83]\n"
"s_waitcnt lgkmcnt(0)\n"
"v_mfma_f32_16x16x32_bf16 v[60:63], v[108:111], v[24:27], v[60:63]\n"
"v_mfma_f32_16x16x32_bf16 v[64:67], v[112:115], v[24:27], v[64:67]\n"
"v_mfma_f32_16x16x32_bf16 v[68:71], a[20:23], v[24:27], v[68:71]\n"
"v_mfma_f32_16x16x32_bf16 v[72:75], a[52:55], v[24:27], v[72:75]\n"
"v_mfma_f32_16x16x32_bf16 v[76:79], a[84:87], v[24:27], v[76:79]\n"
"v_mfma_f32_16x16x32_bf16 v[80:83], a[116:119], v[24:27], v[80:83]\n"
"s_waitcnt vmcnt(0)\n"
"v_cmp_ne_u64_e32 vcc, v[250:251], v[18:19]\n"
"s_cbranch_vccnz .Lmk_f1s\n"
".Lmk_f1r:\n"
"s_add_u32 s98, s52, 0x40000\n"
"buffer_load_dwordx4 v[116:119], v32, s[4:7], s98 offen sc1\n"
"buffer_load_dwordx4 v[120:123], v32, s[4:7], s98 offen offset:1024 sc1\n"
"buffer_load_dwordx4 v[124:127], v33, s[4:7], s98 offen sc1\n"
"buffer_load_dwordx4 v[128:131], v33, s[4:7], s98 offen offset:1024 sc1\n"
"buffer_load_dwordx4 v[132:135], v34, s[4:7], s98 offen sc1\n"
"buffer_load_dwordx4 v[136:139], v34, s[4:7], s98 offen offset:1024 sc1\n"
"buffer_load_dwordx4 v[140:143], v35, s[4:7], s98 offen sc1\n"
"buffer_load_dwordx4 v[144:147], v35, s[4:7], s98 offen offset:1024 sc1\n"
"s_add_u32 s98, s53, 0x40000\n"
"buffer_load_dwordx4 v[148:151], v32, s[4:7], s98 offen sc1\n"
"buffer_load_dwordx4 v[152:155], v32, s[4:7], s98 offen offset:1024 sc1\n"
"buffer_load_dwordx4 v[156:159], v33, s[4:7], s98 offen sc1\n"
"buffer_load_dwordx4 v[160:163], v33, s[4:7], s98 offen offset:1024 sc1\n"
"buffer_load_dwordx4 v[164:167], v34, s[4:7], s98 offen sc1\n"
"buffer_load_dwordx4 v[168:171], v34, s[4:7], s98 offen offset:1024 sc1\n"
"buffer_load_dwordx4 v[172:175], v35, s[4:7], s98 offen sc1\n"
"buffer_load_dwordx4 v[176:179], v35, s[4:7], s98 offen offset:1024 sc1\n"
"s_getpc_b64 s[84:85]\n"
".Lmk_h2:\n"
"s_and_b32 s84, s84, -16\n"
"s_and_b32 s85, s85, 0xffff\n"
"s_mov_b32 s86, (.Lmk_end-.Lmk_h2)&0xFFFFFFF0\n"
"s_mov_b32 s87, 0x20000\n"
"s_and_b32 s98, s26, 0xfffffc00\n"
"s_mov_b32 m0, 0x27800\n"
"s_add_u32 s98, s98, 0x1e000\n"
"buffer_load_dwordx4 v0, s[84:87], s98 offen lds\n"
"ds_read_b64_tr_b16 v[180:181], v254 offset:6144\n"
"ds_read_b64_tr_b16 v[182:183], v254 offset:7168\n"
"ds_read_b64_tr_b16 v[184:185], v253 offset:6144\n"
"ds_read_b64_tr_b16 v[186:187], v253 offset:7168\n"
"ds_read_b64_tr_b16 a[24:25], v254 offset:8192\n"
"ds_read_b64_tr_b16 a[26:27], v254 offset:9216\n"
"ds_read_b64_tr_b16 a[56:57], v253 offset:8192\n"
"ds_read_b64_tr_b16 a[58:59], v253 offset:9216\n"
"ds_read_b64_tr_b16 a[88:89], v254 offset:10240\n"
"ds_read_b64_tr_b16 a[90:91], v254 offset:11264\n"
"ds_read_b64_tr_b16 a[120:121], v253 offset:10240\n"
"ds_read_b64_tr_b16 a[122:123], v253 offset:11264\n"
"ds_read_b64_tr_b16 v[188:189], v254 offset:12288\n"
"ds_read_b64_tr_b16 v[190:191], v254 offset:13312\n"
"ds_read_b64_tr_b16 v[192:193], v253 offset:12288\n"
"s_waitcnt lgkmcnt(13)\n"
"ds_read_b64_tr_b16 v[194:195], v253 offset:13312\n"
"ds_read_b64_tr_b16 a[28:29], v254 offset:14336\n"
"s_waitcnt lgkmcnt(13)\n"
"ds_read_b64_tr_b16 a[30:31], v254 offset:15360\n"
"ds_read_b64_tr_b16 a[60:61], v253 offset:14336\n"
"s_waitcnt lgkmcnt(13)\n"
"ds_read_b64_tr_b16 a[62:63], v253 offset:15360\n"
"ds_read_b64_tr_b16 a[92:93], v254 offset:16384\n"
"s_waitcnt lgkmcnt(13)\n"
"ds_read_b64_tr_b16 a[94:95], v254 offset:17408\n"
"ds_read_b64_tr_b16 a[124:125], v253 offset:16384\n"
"s_waitcnt lgkmcnt(13)\n"
"ds_read_b64_tr_b16 a[126:127], v253 offset:17408\n"
"s_waitcnt lgkmcnt(12)\n"
"v_mfma_f32_16x16x32_bf16 v[60:63], v[180:183], v[24:27], v[60:63]\n"
"v_mfma_f32_16x16x32_bf16 v[64:67], v[184:187], v[24:27], v[64:67]\n"
"v_mfma_f32_16x16x32_bf16 v[68:71], a[24:27], v[24:27], v[68:71]\n"
"v_mfma_f32_16x16x32_bf16 v[72:75], a[56:59], v[24:27], v[72:75]\n"
"v_mfma_f32_16x16x32_bf16 v[76:79], a[88:91], v[24:27], v[76:79]\n"
"v_mfma_f32_16x16x32_bf16 v[80:83], a[120:123], v[24:27], v[80:83]\n"
"s_waitcnt lgkmcnt(0)\n"
"v_mfma_f32_16x16x32_bf16 v[60:63], v[188:191], v[24:27], v[60:63]\n"
"v_mfma_f32_16x16x32_bf16 v[64:67], v[192:195], v[24:27], v[64:67]\n"
"v_mfma_f32_16x16x32_bf16 v[68:71], a[28:31], v[24:27], v[68:71]\n"
"v_mfma_f32_16x16x32_bf16 v[72:75], a[60:63], v[24:27], v[72:75]\n"
"v_mfma_f32_16x16x32_bf16 v[76:79], a[92:95], v[24:27], v[76:79]\n"
"v_mfma_f32_16x16x32_bf16 v[80:83], a[124:127], v[24:27], v[80:83]\n"
"s_nop 7\n"
"ds_write_b128 v44, v[60:63]\n"
"ds_write_b128 v44, v[64:67] offset:64\n"
"ds_write_b128 v44, v[68:71] offset:128\n"
"ds_write_b128 v44, v[72:75] offset:192\n"
"ds_write_b128 v44, v[76:79] offset:256\n"
"ds_write_b128 v44, v[80:83] offset:320\n"
"s_waitcnt lgkmcnt(0)\n"
"s_barrier\n"
"ds_read_b32 v255, v45\n"
"ds_read_b32 v254, v45 offset:6144\n"
"ds_read_b32 v253, v45 offset:12288\n"
"ds_read_b32 v252, v45 offset:18432\n"
"s_waitcnt lgkmcnt(0)\n"
"v_add_f32_e32 v255, v255, v254\n"
"v_add_f32_e32 v253, v253, v252\n"
"v_add_f32_e32 v255, v255, v253\n"
"ds_write_b32 v21, v255\n"
".Lmk_z0:\n"
"s_waitcnt vmcnt(13)\n"
"; @A G1 0.0\n"
"v_mfma_f32_16x16x32_bf16 v[24:27], a[0:3], v[116:119], 0\n"
"v_mfma_f32_16x16x32_bf16 v[24:27], a[4:7], v[120:123], v[24:27]\n"
"s_add_u32 m0, s77, 0xa000\n"
"s_add_u32 s98, s49, 0\n"
"buffer_load_dwordx4 v47, s[36:39], s98 offen lds\n"
"v_mfma_f32_16x16x32_bf16 v[24:27], a[8:11], v[124:127], v[24:27]\n"
"s_add_u32 s98, s54, 0x40000\n"
"buffer_load_dwordx4 v[60:63], v32, s[4:7], s98 offen sc1\n"
"s_add_u32 m0, s77, 0xa400\n"
"v_mfma_f32_16x16x32_bf16 v[24:27], a[12:15], v[128:131], v[24:27]\n"
"s_waitcnt vmcnt(11)\n"
"s_add_u32 s97, s49, 0x4000\n"
"s_add_u32 s96, s62, 0x4000\n"
"v_mfma_f32_16x16x32_bf16 v[24:27], a[16:19], v[132:135], v[24:27]\n"
"buffer_load_dwordx4 v47, s[36:39], s97 offen lds\n"
"v_mfma_f32_16x16x32_bf16 v[24:27], a[20:23], v[136:139], v[24:27]\n"
"s_add_u32 m0, s77, 0xc000\n"
"s_add_u32 s97, s50, 0\n"
"v_mfma_f32_16x16x32_bf16 v[24:27], a[24:27], v[140:143], v[24:27]\n"
"buffer_load_dword v255, v10, s[4:7], s96 offen sc1\n"
"v_mfma_f32_16x16x32_bf16 v[24:27], a[28:31], v[144:147], v[24:27]\n"
"buffer_load_dwordx4 v[64:67], v32, s[4:7], s98 offen offset:1024 sc1\n"
"buffer_load_dwordx4 v47, s[40:43], s97 offen lds\n"
"v_mfma_f32_16x16x32_bf16 v[68:71], a[60:63], v[144:147], 0\n"
"s_add_u32 m0, s77, 0xc400\n"
"s_add_u32 s97, s50, 0x4000\n"
"v_mfma_f32_16x16x32_bf16 v[68:71], a[56:59], v[140:143], v[68:71]\n"
"buffer_load_dword v254, v10, s[4:7], s96 offen offset:256 sc1\n"
"v_mfma_f32_16x16x32_bf16 v[68:71], a[52:55], v[136:139], v[68:71]\n"
"buffer_load_dwordx4 v47, s[40:43], s97 offen lds\n"
"v_mfma_f32_16x16x32_bf16 v[68:71], a[48:51], v[132:135], v[68:71]\n"
"s_add_u32 m0, s77, 0x4000\n"
"s_add_u32 s97, s46, 0\n"
"v_mfma_f32_16x16x32_bf16 v[68:71], a[44:47], v[128:131], v[68:71]\n"
"buffer_load_dwordx4 v[72:75], v33, s[4:7], s98 offen sc1\n"
"v_mfma_f32_16x16x32_bf16 v[68:71], a[40:43], v[124:127], v[68:71]\n"
"ds_write_b128 v3, v[24:27]\n"
"v_mfma_f32_16x16x32_bf16 v[68:71], a[36:39], v[120:123], v[68:71]\n"
"buffer_load_dwordx4 v46, s[32:35], s97 offen lds\n"
"v_mfma_f32_16x16x32_bf16 v[68:71], a[32:35], v[116:119], v[68:71]\n"
"s_add_u32 m0, s77, 0x4400\n"
"s_add_u32 s97, s46, 0xc000\n"
"v_mfma_f32_16x16x32_bf16 v[24:27], a[64:67], v[116:119], 0\n"
"buffer_load_dword v253, v10, s[4:7], s96 offen offset:512 sc1\n"
"v_mfma_f32_16x16x32_bf16 v[24:27], a[68:71], v[120:123], v[24:27]\n"
"buffer_load_dwordx4 v[76:79], v33, s[4:7], s98 offen offset:1024 sc1\n"
"buffer_load_dwordx4 v46, s[32:35], s97 offen lds\n"
"v_mfma_f32_16x16x32_bf16 v[24:27], a[72:75], v[124:127], v[24:27]\n"
"s_add_u32 m0, s77, 0x6000\n"
"s_add_u32 s97, s47, 0\n"
"v_mfma_f32_16x16x32_bf16 v[24:27], a[76:79], v[128:131], v[24:27]\n"
"buffer_load_dword v252, v10, s[4:7], s96 offen offset:768 sc1\n"
"v_mfma_f32_16x16x32_bf16 v[24:27], a[80:83], v[132:135], v[24:27]\n"
"buffer_load_dwordx4 v46, s[32:35], s97 offen lds\n"
"v_mfma_f32_16x16x32_bf16 v[24:27], a[84:87], v[136:139], v[24:27]\n"
"ds_write_b128 v3, v[68:71] offset:1024\n"
"v_mfma_f32_16x16x32_bf16 v[24:27], a[88:91], v[140:143], v[24:27]\n"
"buffer_load_dwordx4 v[68:71], v34, s[4:7], s98 offen sc1\n"
"s_add_u32 m0, s77, 0x6400\n"
"s_add_u32 s97, s47, 0xc000\n"
"v_mfma_f32_16x16x32_bf16 v[24:27], a[92:95], v[144:147], v[24:27]\n"
"buffer_load_dwordx4 v46, s[32:35], s97 offen lds\n"
"v_mfma_f32_16x16x32_bf16 v[80:83], a[124:127], v[144:147], 0\n"
"s_add_u32 m0, s77, 0x8000\n"
"s_add_u32 s97, s48, 0\n"
"v_mfma_f32_16x16x32_bf16 v[80:83], a[120:123], v[140:143], v[80:83]\n"
"buffer_load_dword v251, v10, s[4:7], s96 offen offset:1024 sc1\n"
"v_mfma_f32_16x16x32_bf16 v[80:83], a[116:119], v[136:139], v[80:83]\n"
"buffer_load_dwordx4 v46, s[32:35], s97 offen lds\n"
"v_mfma_f32_16x16x32_bf16 v[80:83], a[112:115], v[132:135], v[80:83]\n"
"buffer_load_dwordx4 v[196:199], v34, s[4:7], s98 offen offset:1024 sc1\n"
"s_add_u32 m0, s77, 0x8400\n"
"s_add_u32 s97, s48, 0xc000\n"
"v_mfma_f32_16x16x32_bf16 v[80:83], a[108:111], v[128:131], v[80:83]\n"
"buffer_load_dword v250, v10, s[4:7], s96 offen offset:1280 sc1\n"
"v_mfma_f32_16x16x32_bf16 v[80:83], a[104:107], v[124:127], v[80:83]\n"
"ds_write_b128 v3, v[24:27] offset:2048\n"
"v_mfma_f32_16x16x32_bf16 v[80:83], a[100:103], v[120:123], v[80:83]\n"
"buffer_load_dwordx4 v46, s[32:35], s97 offen lds\n"
"v_mfma_f32_16x16x32_bf16 v[80:83], a[96:99], v[116:119], v[80:83]\n"
"buffer_load_dword v249, v10, s[4:7], s96 offen offset:1536 sc1\n"
"v_mfma_f32_16x16x32_bf16 v[24:27], v[28:31], v[116:119], 0\n"
"buffer_load_dwordx4 v[44:47], v35, s[4:7], s98 offen sc1\n"
"v_mfma_f32_16x16x32_bf16 v[24:27], v[52:55], v[120:123], v[24:27]\n"
"v_mfma_f32_16x16x32_bf16 v[24:27], v[84:87], v[124:127], v[24:27]\n"
"buffer_load_dword v248, v10, s[4:7], s96 offen offset:1792 sc1\n"
"v_mfma_f32_16x16x32_bf16 v[24:27], v[92:95], v[128:131], v[24:27]\n"
"v_mfma_f32_16x16x32_bf16 v[24:27], v[100:103], v[132:135], v[24:27]\n"
"v_mfma_f32_16x16x32_bf16 v[24:27], v[108:111], v[136:139], v[24:27]\n"
"buffer_load_dwordx4 v[200:203], v35, s[4:7], s98 offen offset:1024 sc1\n"
"v_mfma_f32_16x16x32_bf16 v[24:27], v[180:183], v[140:143], v[24:27]\n"
"ds_write_b128 v3, v[80:83] offset:3072\n"
"v_mfma_f32_16x16x32_bf16 v[24:27], v[188:191], v[144:147], v[24:27]\n"
"v_mfma_f32_16x16x32_bf16 v[80:83], v[192:195], v[144:147], 0\n"
"v_mfma_f32_16x16x32_bf16 v[80:83], v[184:187], v[140:143], v[80:83]\n"
"v_mfma_f32_16x16x32_bf16 v[80:83], v[112:115], v[136:139], v[80:83]\n"
"v_mfma_f32_16x16x32_bf16 v[80:83], v[104:107], v[132:135], v[80:83]\n"
"v_mfma_f32_16x16x32_bf16 v[80:83], v[96:99], v[128:131], v[80:83]\n"
"v_mfma_f32_16x16x32_bf16 v[80:83], v[88:91], v[124:127], v[80:83]\n"
"s_nop 1\n"
"ds_write_b128 v3, v[24:27] offset:4096\n"
"v_mfma_f32_16x16x32_bf16 v[80:83], v[56:59], v[120:123], v[80:83]\n"
"v_mfma_f32_16x16x32_bf16 v[80:83], v[48:51], v[116:119], v[80:83]\n"
"s_waitcnt vmcnt(31)\n"
"; @A G1 0.1\n"
"v_mfma_f32_16x16x32_bf16 v[24:27], a[0:3], v[148:151], 0\n"
"v_mfma_f32_16x16x32_bf16 v[24:27], a[4:7], v[152:155], v[24:27]\n"
"v_mfma_f32_16x16x32_bf16 v[24:27], a[8:11], v[156:159], v[24:27]\n"
"s_add_u32 s98, s55, 0x40000\n"
"buffer_load_dwordx4 v[116:119], v32, s[4:7], s98 offen sc1\n"
"v_mfma_f32_16x16x32_bf16 v[24:27], a[12:15], v[160:163], v[24:27]\n"
"s_waitcnt vmcnt(28)\n"
"v_mfma_f32_16x16x32_bf16 v[24:27], a[16:19], v[164:167], v[24:27]\n"
"v_mfma_f32_16x16x32_bf16 v[24:27], a[20:23], v[168:171], v[24:27]\n"
"ds_write_b128 v3, v[80:83] offset:5120\n"
"v_mfma_f32_16x16x32_bf16 v[24:27], a[24:27], v[172:175], v[24:27]\n"
"v_mfma_f32_16x16x32_bf16 v[24:27], a[28:31], v[176:179], v[24:27]\n"
"buffer_load_dwordx4 v[80:83], v32, s[4:7], s98 offen offset:1024 sc1\n"
"v_mfma_f32_16x16x32_bf16 v[120:123], a[60:63], v[176:179], 0\n"
"v_mfma_f32_16x16x32_bf16 v[120:123], a[56:59], v[172:175], v[120:123]\n"
"v_mfma_f32_16x16x32_bf16 v[120:123], a[52:55], v[168:171], v[120:123]\n"
"v_mfma_f32_16x16x32_bf16 v[120:123], a[48:51], v[164:167], v[120:123]\n"
"v_mfma_f32_16x16x32_bf16 v[120:123], a[44:47], v[160:163], v[120:123]\n"
"buffer_load_dwordx4 v[124:127], v33, s[4:7], s98 offen sc1\n"
"v_mfma_f32_16x16x32_bf16 v[120:123], a[40:43], v[156:159], v[120:123]\n"
"ds_write_b128 v3, v[24:27] offset:6144\n"
"v_mfma_f32_16x16x32_bf16 v[120:123], a[36:39], v[152:155], v[120:123]\n"
"v_mfma_f32_16x16x32_bf16 v[120:123], a[32:35], v[148:151], v[120:123]\n"
"v_mfma_f32_16x16x32_bf16 v[24:27], a[64:67], v[148:151], 0\n"
"v_mfma_f32_16x16x32_bf16 v[24:27], a[68:71], v[152:155], v[24:27]\n"
"buffer_load_dwordx4 v[128:131], v33, s[4:7], s98 offen offset:1024 sc1\n"
"v_mfma_f32_16x16x32_bf16 v[24:27], a[72:75], v[156:159], v[24:27]\n"
"v_mfma_f32_16x16x32_bf16 v[24:27], a[76:79], v[160:163], v[24:27]\n"
"v_mfma_f32_16x16x32_bf16 v[24:27], a[80:83], v[164:167], v[24:27]\n"
"v_mfma_f32_16x16x32_bf16 v[24:27], a[84:87], v[168:171], v[24:27]\n"
"s_nop 0\n"
"ds_write_b128 v3, v[120:123] offset:7168\n"
"v_mfma_f32_16x16x32_bf16 v[24:27], a[88:91], v[172:175], v[24:27]\n"
"buffer_load_dwordx4 v[120:123], v34, s[4:7], s98 offen sc1\n"
"v_mfma_f32_16x16x32_bf16 v[24:27], a[92:95], v[176:179], v[24:27]\n"
"v_mfma_f32_16x16x32_bf16 v[132:135], a[124:127], v[176:179], 0\n"
"v_mfma_f32_16x16x32_bf16 v[132:135], a[120:123], v[172:175], v[132:135]\n"
"v_mfma_f32_16x16x32_bf16 v[132:135], a[116:119], v[168:171], v[132:135]\n"
"v_mfma_f32_16x16x32_bf16 v[132:135], a[112:115], v[164:167], v[132:135]\n"
"buffer_load_dwordx4 v[136:139], v34, s[4:7], s98 offen offset:1024 sc1\n"
"v_mfma_f32_16x16x32_bf16 v[132:135], a[108:111], v[160:163], v[132:135]\n"
"v_mfma_f32_16x16x32_bf16 v[132:135], a[104:107], v[156:159], v[132:135]\n"
"s_nop 0\n"
"ds_write_b128 v3, v[24:27] offset:8192\n"
"v_mfma_f32_16x16x32_bf16 v[132:135], a[100:103], v[152:155], v[132:135]\n"
"v_mfma_f32_16x16x32_bf16 v[132:135], a[96:99], v[148:151], v[132:135]\n"
"v_mfma_f32_16x16x32_bf16 v[24:27], v[28:31], v[148:151], 0\n"
"buffer_load_dwordx4 v[140:143], v35, s[4:7], s98 offen sc1\n"
"v_mfma_f32_16x16x32_bf16 v[24:27], v[52:55], v[152:155], v[24:27]\n"
"v_mfma_f32_16x16x32_bf16 v[24:27], v[84:87], v[156:159], v[24:27]\n"
"v_mfma_f32_16x16x32_bf16 v[24:27], v[92:95], v[160:163], v[24:27]\n"
"v_mfma_f32_16x16x32_bf16 v[24:27], v[100:103], v[164:167], v[24:27]\n"
"s_waitcnt vmcnt(8)\n"
"v_add_f32_e32 v255, v255, v251\n"
"v_add_f32_e32 v254, v254, v250\n"
"v_mfma_f32_16x16x32_bf16 v[24:27], v[108:111], v[168:171], v[24:27]\n"
"buffer_load_dwordx4 v[144:147], v35, s[4:7], s98 offen offset:1024 sc1\n"
"v_mfma_f32_16x16x32_bf16 v[24:27], v[180:183], v[172:175], v[24:27]\n"
"ds_write_b128 v3, v[132:135] offset:9216\n"
"v_mfma_f32_16x16x32_bf16 v[24:27], v[188:191], v[176:179], v[24:27]\n"
"v_add_f32_e32 v253, v253, v249\n"
"v_add_f32_e32 v252, v252, v248\n"
"v_mfma_f32_16x16x32_bf16 v[132:135], v[192:195], v[176:179], 0\n"
"v_add_f32_e32 v255, v255, v253\n"
"v_add_f32_e32 v254, v254, v252\n"
"v_mfma_f32_16x16x32_bf16 v[132:135], v[184:187], v[172:175], v[132:135]\n"
"v_add_f32_e32 v255, v255, v254\n"
"v_mov_b32_e32 v254, v255\n"
"v_mfma_f32_16x16x32_bf16 v[132:135], v[112:115], v[168:171], v[132:135]\n"
"s_nop 0\n"
"v_permlane32_swap_b32_e32 v255, v254\n"
"v_add_f32_e32 v255, v255, v254\n"
"v_mfma_f32_16x16x32_bf16 v[132:135], v[104:107], v[164:167], v[132:135]\n"
"v_mul_f32_e32 v255, 0x3a800000, v255\n"
"v_mov_b32_e32 v254, v255\n"
"v_mfma_f32_16x16x32_bf16 v[132:135], v[96:99], v[160:163], v[132:135]\n"
"s_nop 0\n"
"v_permlane16_swap_b32_e32 v255, v254\n"
"v_cvt_pk_bf16_f32 v253, -v255, 0\n"
"v_mfma_f32_16x16x32_bf16 v[132:135], v[88:91], v[156:159], v[132:135]\n"
"ds_write_b128 v3, v[24:27] offset:10240\n"
"v_mfma_f32_16x16x32_bf16 v[132:135], v[56:59], v[152:155], v[132:135]\n"
"v_fma_f32 v254, -v255, v255, v254\n"
"v_lshlrev_b32_e32 v253, 16, v253\n"
"v_mfma_f32_16x16x32_bf16 v[132:135], v[48:51], v[148:151], v[132:135]\n"
"v_max_f32_e32 v254, 0, v254\n"
"v_cvt_pk_bf16_f32 v254, v254, 0\n"
"; @A G1 0.2\n"
"v_mfma_f32_16x16x32_bf16 v[24:27], a[0:3], v[60:63], 0\n"
"v_lshlrev_b32_e32 v254, 16, v254\n"
"v_add_f32_e32 v254, 0x3727c5ac, v254\n"
"v_mfma_f32_16x16x32_bf16 v[24:27], a[4:7], v[64:67], v[24:27]\n"
"v_cvt_pk_bf16_f32 v254, v254, 0\n"
"v_lshlrev_b32_e32 v254, 16, v254\n"
"v_mfma_f32_16x16x32_bf16 v[24:27], a[8:11], v[72:75], v[24:27]\n"
"v_sqrt_f32_e32 v254, v254\n"
"v_mfma_f32_16x16x32_bf16 v[24:27], a[12:15], v[76:79], v[24:27]\n"
"s_waitcnt vmcnt(8)\n"
"v_cvt_pk_bf16_f32 v254, v254, 0\n"
"v_lshlrev_b32_e32 v254, 16, v254\n"
"v_mfma_f32_16x16x32_bf16 v[24:27], a[16:19], v[68:71], v[24:27]\n"
"v_rcp_f32_e32 v255, v254\n"
"v_mfma_f32_16x16x32_bf16 v[24:27], a[20:23], v[196:199], v[24:27]\n"
"ds_write_b128 v3, v[132:135] offset:11264\n"
"v_mfma_f32_16x16x32_bf16 v[24:27], a[24:27], v[44:47], v[24:27]\n"
"v_mul_f32_e32 v254, 0x3e8293ee, v255\n"
"v_mfma_f32_16x16x32_bf16 v[24:27], a[28:31], v[200:203], v[24:27]\n"
"v_mfma_f32_16x16x32_bf16 v[132:135], a[60:63], v[200:203], 0\n"
"v_mfma_f32_16x16x32_bf16 v[132:135], a[56:59], v[44:47], v[132:135]\n"
"v_mfma_f32_16x16x32_bf16 v[132:135], a[52:55], v[196:199], v[132:135]\n"
"s_add_u32 s98, s52, 0x48000\n"
"buffer_load_dwordx4 v[148:151], v32, s[4:7], s98 offen sc1\n"
"v_mfma_f32_16x16x32_bf16 v[132:135], a[48:51], v[68:71], v[132:135]\n"
"s_waitcnt lgkmcnt(0)\n"
"s_barrier\n"
"v_mfma_f32_16x16x32_bf16 v[132:135], a[44:47], v[76:79], v[132:135]\n"
"ds_read_b128 v[152:155], v4\n"
"ds_read_b128 v[156:159], v9 offset:128\n"
"v_mfma_f32_16x16x32_bf16 v[132:135], a[40:43], v[72:75], v[132:135]\n"
"ds_write_b128 v3, v[24:27] offset:12288\n"
"v_mfma_f32_16x16x32_bf16 v[132:135], a[36:39], v[64:67], v[132:135]\n"
"ds_read_b128 v[24:27], v5\n"
"ds_read_b64_tr_b16 v[160:161], v40\n"
"ds_read_b64_tr_b16 v[162:163], v40 offset:1024\n"
"v_mfma_f32_16x16x32_bf16 v[132:135], a[32:35], v[60:63], v[132:135]\n"
"buffer_load_dwordx4 v[164:167], v32, s[4:7], s98 offen offset:1024 sc1\n"
"v_mfma_f32_16x16x32_bf16 v[168:171], a[64:67], v[60:63], 0\n"
"v_mfma_f32_16x16x32_bf16 v[168:171], a[68:71], v[64:67], v[168:171]\n"
"v_mfma_f32_16x16x32_bf16 v[168:171], a[72:75], v[72:75], v[168:171]\n"
"v_mfma_f32_16x16x32_bf16 v[168:171], a[76:79], v[76:79], v[168:171]\n"
"ds_read_b128 v[172:175], v4 offset:1024\n"
"ds_read_b128 v[176:179], v9 offset:192\n"
"s_waitcnt lgkmcnt(2)\n"
"buffer_store_dwordx4 v[160:163], v0, s[4:7], s24 offen sc1\n"
"v_mfma_f32_16x16x32_bf16 v[168:171], a[80:83], v[68:71], v[168:171]\n"
"buffer_load_dwordx4 v[160:163], v33, s[4:7], s98 offen sc1\n"
"ds_read_b64_tr_b16 v[204:205], v40 offset:4096\n"
"v_mfma_f32_16x16x32_bf16 v[168:171], a[84:87], v[196:199], v[168:171]\n"
"ds_write_b128 v3, v[132:135] offset:13312\n"
"v_mfma_f32_16x16x32_bf16 v[168:171], a[88:91], v[44:47], v[168:171]\n"
"ds_read_b128 v[132:135], v5 offset:1024\n"
"ds_read_b64_tr_b16 v[206:207], v40 offset:5120\n"
"v_mfma_f32_16x16x32_bf16 v[168:171], a[92:95], v[200:203], v[168:171]\n"
"v_fmac_f32_e32 v152, v253, v156\n"
"v_fmac_f32_e32 v153, v253, v157\n"
"v_mfma_f32_16x16x32_bf16 v[208:211], a[124:127], v[200:203], 0\n"
"v_fmac_f32_e32 v154, v253, v158\n"
"v_fmac_f32_e32 v155, v253, v159\n"
"v_mfma_f32_16x16x32_bf16 v[208:211], a[120:123], v[44:47], v[208:211]\n"
"buffer_load_dwordx4 v[156:159], v33, s[4:7], s98 offen offset:1024 sc1\n"
"v_mfma_f32_16x16x32_bf16 v[208:211], a[116:119], v[196:199], v[208:211]\n"
"ds_read_b128 v[212:215], v4 offset:2048\n"
"ds_read_b128 v[216:219], v9 offset:256\n"
"v_mfma_f32_16x16x32_bf16 v[208:211], a[112:115], v[68:71], v[208:211]\n"
"ds_read_b128 v[220:223], v5 offset:2048\n"
"s_waitcnt lgkmcnt(3)\n"
"buffer_store_dwordx4 v[204:207], v0, s[4:7], s24 offen offset:1024 sc1\n"
"v_add_f32_e32 v152, v152, v24\n"
"v_mfma_f32_16x16x32_bf16 v[208:211], a[108:111], v[76:79], v[208:211]\n"
"v_add_f32_e32 v153, v153, v25\n"
"v_add_f32_e32 v154, v154, v26\n"
"v_mfma_f32_16x16x32_bf16 v[208:211], a[104:107], v[72:75], v[208:211]\n"
"ds_write_b128 v3, v[168:171] offset:14336\n"
"v_mfma_f32_16x16x32_bf16 v[208:211], a[100:103], v[64:67], v[208:211]\n"
"buffer_load_dwordx4 v[168:171], v34, s[4:7], s98 offen sc1\n"
"v_mfma_f32_16x16x32_bf16 v[208:211], a[96:99], v[60:63], v[208:211]\n"
"v_add_f32_e32 v155, v155, v27\n"
"v_fmac_f32_e32 v172, v253, v176\n"
"v_mfma_f32_16x16x32_bf16 v[24:27], v[28:31], v[60:63], 0\n"
"ds_read_b64_tr_b16 v[204:205], v40 offset:8192\n"
"v_fmac_f32_e32 v173, v253, v177\n"
"v_fmac_f32_e32 v174, v253, v178\n"
"v_mfma_f32_16x16x32_bf16 v[24:27], v[52:55], v[64:67], v[24:27]\n"
"ds_read_b128 v[224:227], v4 offset:3072\n"
"ds_read_b128 v[228:231], v9 offset:320\n"
"ds_read_b64_tr_b16 v[206:207], v40 offset:9216\n"
"v_mfma_f32_16x16x32_bf16 v[24:27], v[84:87], v[72:75], v[24:27]\n"
"ds_read_b128 v[232:235], v5 offset:3072\n"
"v_fmac_f32_e32 v175, v253, v179\n"
"v_mfma_f32_16x16x32_bf16 v[24:27], v[92:95], v[76:79], v[24:27]\n"
"buffer_load_dwordx4 v[176:179], v34, s[4:7], s98 offen offset:1024 sc1\n"
"v_mfma_f32_16x16x32_bf16 v[24:27], v[100:103], v[68:71], v[24:27]\n"
"v_add_f32_e32 v172, v172, v132\n"
"v_add_f32_e32 v173, v173, v133\n"
"v_mfma_f32_16x16x32_bf16 v[24:27], v[108:111], v[196:199], v[24:27]\n"
"ds_write_b128 v3, v[208:211] offset:15360\n"
"v_mfma_f32_16x16x32_bf16 v[24:27], v[180:183], v[44:47], v[24:27]\n"
"s_waitcnt lgkmcnt(7)\n"
"s_waitcnt lgkmcnt(2)\n"
"buffer_store_dwordx4 v[204:207], v0, s[4:7], s25 offen sc1\n"
"v_add_f32_e32 v174, v174, v134\n"
"v_add_f32_e32 v175, v175, v135\n"
"v_mfma_f32_16x16x32_bf16 v[24:27], v[188:191], v[200:203], v[24:27]\n"
"v_fmac_f32_e32 v212, v253, v216\n"
"v_fmac_f32_e32 v213, v253, v217\n"
"v_mfma_f32_16x16x32_bf16 v[132:135], v[192:195], v[200:203], 0\n"
"buffer_load_dwordx4 v[200:203], v35, s[4:7], s98 offen sc1\n"
"v_mfma_f32_16x16x32_bf16 v[132:135], v[184:187], v[44:47], v[132:135]\n"
"ds_read_b128 v[44:47], v4 offset:4096\n"
"ds_read_b128 v[204:207], v9\n"
"v_mfma_f32_16x16x32_bf16 v[132:135], v[112:115], v[196:199], v[132:135]\n"
"ds_read_b128 v[196:199], v5 offset:4096\n"
"v_fmac_f32_e32 v214, v253, v218\n"
"v_mfma_f32_16x16x32_bf16 v[132:135], v[104:107], v[68:71], v[132:135]\n"
"v_fmac_f32_e32 v215, v253, v219\n"
"v_add_f32_e32 v212, v212, v220\n"
"v_mfma_f32_16x16x32_bf16 v[132:135], v[96:99], v[76:79], v[132:135]\n"
"v_add_f32_e32 v213, v213, v221\n"
"v_add_f32_e32 v214, v214, v222\n"
"v_mfma_f32_16x16x32_bf16 v[132:135], v[88:91], v[72:75], v[132:135]\n"
"buffer_load_dwordx4 v[68:71], v35, s[4:7], s98 offen offset:1024 sc1\n"
"v_mfma_f32_16x16x32_bf16 v[132:135], v[56:59], v[64:67], v[132:135]\n"
"ds_write_b128 v3, v[24:27] offset:16384\n"
"v_mfma_f32_16x16x32_bf16 v[132:135], v[48:51], v[60:63], v[132:135]\n"
"s_waitcnt vmcnt(15)\n"
"ds_read_b128 v[24:27], v4 offset:5120\n"
"ds_read_b128 v[60:63], v9 offset:64\n"
"ds_read_b64_tr_b16 v[64:65], v40 offset:12288\n"
"; @A G1 0.3\n"
"v_mfma_f32_16x16x32_bf16 v[152:155], a[0:3], v[116:119], v[152:155]\n"
"ds_read_b128 v[72:75], v5 offset:5120\n"
"ds_read_b64_tr_b16 v[66:67], v40 offset:13312\n"
"v_add_f32_e32 v215, v215, v223\n"
"v_mfma_f32_16x16x32_bf16 v[152:155], a[4:7], v[80:83], v[152:155]\n"
"s_waitcnt lgkmcnt(10)\n"
"v_fmac_f32_e32 v224, v253, v228\n"
"v_fmac_f32_e32 v225, v253, v229\n"
"v_mfma_f32_16x16x32_bf16 v[152:155], a[8:11], v[124:127], v[152:155]\n"
"s_add_u32 s98, s53, 0x48000\n"
"buffer_load_dwordx4 v[76:79], v32, s[4:7], s98 offen sc1\n"
"v_mfma_f32_16x16x32_bf16 v[152:155], a[12:15], v[128:131], v[152:155]\n"
"s_waitcnt vmcnt(12)\n"
"v_fmac_f32_e32 v226, v253, v230\n"
"v_fmac_f32_e32 v227, v253, v231\n"
"v_mfma_f32_16x16x32_bf16 v[152:155], a[16:19], v[120:123], v[152:155]\n"
"v_add_f32_e32 v224, v224, v232\n"
"v_add_f32_e32 v225, v225, v233\n"
"v_mfma_f32_16x16x32_bf16 v[152:155], a[20:23], v[136:139], v[152:155]\n"
"ds_write_b128 v3, v[132:135] offset:17408\n"
"v_mfma_f32_16x16x32_bf16 v[152:155], a[24:27], v[140:143], v[152:155]\n"
"s_waitcnt lgkmcnt(7)\n"
"s_waitcnt lgkmcnt(1)\n"
"buffer_store_dwordx4 v[64:67], v0, s[4:7], s25 offen offset:1024 sc1\n"
"v_add_f32_e32 v226, v226, v234\n"
"v_add_f32_e32 v227, v227, v235\n"
"v_mfma_f32_16x16x32_bf16 v[152:155], a[28:31], v[144:147], v[152:155]\n"
"buffer_load_dwordx4 v[64:67], v32, s[4:7], s98 offen offset:1024 sc1\n"
"v_mfma_f32_16x16x32_bf16 v[172:175], a[60:63], v[144:147], v[172:175]\n"
"v_fmac_f32_e32 v44, v253, v204\n"
"v_fmac_f32_e32 v45, v253, v205\n"
"v_mfma_f32_16x16x32_bf16 v[172:175], a[56:59], v[140:143], v[172:175]\n"
"v_fmac_f32_e32 v46, v253, v206\n"
"v_fmac_f32_e32 v47, v253, v207\n"
"v_mfma_f32_16x16x32_bf16 v[172:175], a[52:55], v[136:139], v[172:175]\n"
"s_waitcnt lgkmcnt(0)\n"
"s_barrier\n"
"v_mfma_f32_16x16x32_bf16 v[172:175], a[48:51], v[120:123], v[172:175]\n"
"ds_read_b128 v[132:135], v6\n"
"ds_read_b128 v[204:207], v6 offset:1024\n"
"v_mfma_f32_16x16x32_bf16 v[172:175], a[44:47], v[128:131], v[172:175]\n"
"buffer_load_dwordx4 v[208:211], v33, s[4:7], s98 offen sc1\n"
"v_mfma_f32_16x16x32_bf16 v[172:175], a[40:43], v[124:127], v[172:175]\n"
"v_add_f32_e32 v44, v44, v196\n"
"v_add_f32_e32 v45, v45, v197\n"
"v_mfma_f32_16x16x32_bf16 v[172:175], a[36:39], v[80:83], v[172:175]\n"
"v_add_f32_e32 v46, v46, v198\n"
"v_add_f32_e32 v47, v47, v199\n"
"v_mfma_f32_16x16x32_bf16 v[172:175], a[32:35], v[116:119], v[172:175]\n"
"v_fmac_f32_e32 v24, v253, v60\n"
"v_fmac_f32_e32 v25, v253, v61\n"
"v_mfma_f32_16x16x32_bf16 v[212:215], a[64:67], v[116:119], v[212:215]\n"
"v_fmac_f32_e32 v26, v253, v62\n"
"v_fmac_f32_e32 v27, v253, v63\n"
"v_mfma_f32_16x16x32_bf16 v[212:215], a[68:71], v[80:83], v[212:215]\n"
"buffer_load_dwordx4 v[60:63], v33, s[4:7], s98 offen offset:1024 sc1\n"
"v_mfma_f32_16x16x32_bf16 v[212:215], a[72:75], v[124:127], v[212:215]\n"
"v_add_f32_e32 v24, v24, v72\n"
"v_add_f32_e32 v25, v25, v73\n"
"v_mfma_f32_16x16x32_bf16 v[212:215], a[76:79], v[128:131], v[212:215]\n"
"v_add_f32_e32 v26, v26, v74\n"
"v_add_f32_e32 v27, v27, v75\n"
"v_mfma_f32_16x16x32_bf16 v[212:215], a[80:83], v[120:123], v[212:215]\n"
"s_waitcnt lgkmcnt(0)\n"
"v_add_f32_e32 v152, v152, v132\n"
"v_add_f32_e32 v153, v153, v133\n"
"v_mfma_f32_16x16x32_bf16 v[212:215], a[84:87], v[136:139], v[212:215]\n"
"v_add_f32_e32 v154, v154, v134\n"
"v_add_f32_e32 v155, v155, v135\n"
"v_mfma_f32_16x16x32_bf16 v[212:215], a[88:91], v[140:143], v[212:215]\n"
"buffer_load_dwordx4 v[72:75], v34, s[4:7], s98 offen sc1\n"
"v_mfma_f32_16x16x32_bf16 v[212:215], a[92:95], v[144:147], v[212:215]\n"
"ds_read_b128 v[132:135], v6 offset:2048\n"
"ds_read_b128 v[196:199], v6 offset:3072\n"
"v_mfma_f32_16x16x32_bf16 v[224:227], a[124:127], v[144:147], v[224:227]\n"
"v_add_f32_e32 v172, v172, v204\n"
"v_add_f32_e32 v173, v173, v205\n"
"v_mfma_f32_16x16x32_bf16 v[224:227], a[120:123], v[140:143], v[224:227]\n"
"v_add_f32_e32 v174, v174, v206\n"
"v_add_f32_e32 v175, v175, v207\n"
"v_mfma_f32_16x16x32_bf16 v[224:227], a[116:119], v[136:139], v[224:227]\n"
"v_mul_f32_e32 v152, v152, v255\n"
"v_mul_f32_e32 v153, v153, v255\n"
"v_mfma_f32_16x16x32_bf16 v[224:227], a[112:115], v[120:123], v[224:227]\n"
"buffer_load_dwordx4 v[204:207], v34, s[4:7], s98 offen offset:1024 sc1\n"
"v_mfma_f32_16x16x32_bf16 v[224:227], a[108:111], v[128:131], v[224:227]\n"
"v_mul_f32_e32 v154, v154, v255\n"
"v_mul_f32_e32 v155, v155, v255\n"
"v_mfma_f32_16x16x32_bf16 v[224:227], a[104:107], v[124:127], v[224:227]\n"
"v_mul_f32_e32 v172, v172, v255\n"
"v_mul_f32_e32 v173, v173, v255\n"
"v_mfma_f32_16x16x32_bf16 v[224:227], a[100:103], v[80:83], v[224:227]\n"
"v_mul_f32_e32 v174, v174, v255\n"
"v_mul_f32_e32 v175, v175, v255\n"
"v_mfma_f32_16x16x32_bf16 v[224:227], a[96:99], v[116:119], v[224:227]\n"
"v_cvt_pk_bf16_f32 v216, v152, v153\n"
"v_cvt_pk_bf16_f32 v217, v154, v155\n"
"v_mfma_f32_16x16x32_bf16 v[44:47], v[28:31], v[116:119], v[44:47]\n"
"buffer_load_dwordx4 v[152:155], v35, s[4:7], s98 offen sc1\n"
"v_mfma_f32_16x16x32_bf16 v[44:47], v[52:55], v[80:83], v[44:47]\n"
"v_cvt_pk_bf16_f32 v218, v172, v173\n"
"v_cvt_pk_bf16_f32 v219, v174, v175\n"
"v_mfma_f32_16x16x32_bf16 v[44:47], v[84:87], v[124:127], v[44:47]\n"
"ds_write_b128 v7, v[216:219] offset:24576\n"
"v_mfma_f32_16x16x32_bf16 v[44:47], v[92:95], v[128:131], v[44:47]\n"
"v_mfma_f32_16x16x32_bf16 v[44:47], v[100:103], v[120:123], v[44:47]\n"
"s_waitcnt lgkmcnt(1)\n"
"v_add_f32_e32 v212, v212, v132\n"
"v_add_f32_e32 v213, v213, v133\n"
"v_mfma_f32_16x16x32_bf16 v[44:47], v[108:111], v[136:139], v[44:47]\n"
"buffer_load_dwordx4 v[172:175], v35, s[4:7], s98 offen offset:1024 sc1\n"
"v_mfma_f32_16x16x32_bf16 v[44:47], v[180:183], v[140:143], v[44:47]\n"
"ds_read_b128 v[220:223], v6 offset:4096\n"
"ds_read_b128 v[228:231], v6 offset:5120\n"
"v_mfma_f32_16x16x32_bf16 v[44:47], v[188:191], v[144:147], v[44:47]\n"
"v_add_f32_e32 v214, v214, v134\n"
"v_add_f32_e32 v215, v215, v135\n"
"v_mfma_f32_16x16x32_bf16 v[24:27], v[192:195], v[144:147], v[24:27]\n"
"v_add_f32_e32 v224, v224, v196\n"
"v_add_f32_e32 v225, v225, v197\n"
"v_mfma_f32_16x16x32_bf16 v[24:27], v[184:187], v[140:143], v[24:27]\n"
"v_add_f32_e32 v226, v226, v198\n"
"v_add_f32_e32 v227, v227, v199\n"
"v_mfma_f32_16x16x32_bf16 v[24:27], v[112:115], v[136:139], v[24:27]\n"
"v_mul_f32_e32 v212, v212, v255\n"
"v_mul_f32_e32 v213, v213, v255\n"
"v_mfma_f32_16x16x32_bf16 v[24:27], v[104:107], v[120:123], v[24:27]\n"
"v_mul_f32_e32 v214, v214, v255\n"
"v_mul_f32_e32 v215, v215, v255\n"
"v_mfma_f32_16x16x32_bf16 v[24:27], v[96:99], v[128:131], v[24:27]\n"
"v_mul_f32_e32 v224, v224, v255\n"
"v_mul_f32_e32 v225, v225, v255\n"
"v_mfma_f32_16x16x32_bf16 v[24:27], v[88:91], v[124:127], v[24:27]\n"
"v_mul_f32_e32 v226, v226, v255\n"
"v_mul_f32_e32 v227, v227, v255\n"
"v_mfma_f32_16x16x32_bf16 v[24:27], v[56:59], v[80:83], v[24:27]\n"
"v_cvt_pk_bf16_f32 v80, v212, v213\n"
"v_cvt_pk_bf16_f32 v81, v214, v215\n"
"v_mfma_f32_16x16x32_bf16 v[24:27], v[48:51], v[116:119], v[24:27]\n"
"v_cvt_pk_bf16_f32 v82, v224, v225\n"
"v_cvt_pk_bf16_f32 v83, v226, v227\n"
"ds_write_b128 v7, v[80:83] offset:57344\n"
".Lmk_z1:\n"
"s_waitcnt vmcnt(15)\n"
"; @A G1 1.0\n"
"v_mfma_f32_16x16x32_bf16 v[116:119], a[0:3], v[148:151], 0\n"
"v_mfma_f32_16x16x32_bf16 v[116:119], a[4:7], v[164:167], v[116:119]\n"
"s_add_u32 s98, s62, 0x4800\n"
"v_mfma_f32_16x16x32_bf16 v[116:119], a[8:11], v[160:163], v[116:119]\n"
"s_add_u32 s97, s54, 0x48000\n"
"buffer_load_dwordx4 v[120:123], v32, s[4:7], s97 offen sc1\n"
"v_mfma_f32_16x16x32_bf16 v[116:119], a[12:15], v[156:159], v[116:119]\n"
"s_waitcnt vmcnt(10)\n"
"v_mfma_f32_16x16x32_bf16 v[116:119], a[16:19], v[168:171], v[116:119]\n"
"buffer_load_dword v255, v10, s[4:7], s98 offen sc1\n"
"v_mfma_f32_16x16x32_bf16 v[116:119], a[20:23], v[176:179], v[116:119]\n"
"s_waitcnt lgkmcnt(1)\n"
"v_add_f32_e32 v44, v44, v220\n"
"v_add_f32_e32 v45, v45, v221\n"
"v_mfma_f32_16x16x32_bf16 v[116:119], a[24:27], v[200:203], v[116:119]\n"
"v_add_f32_e32 v46, v46, v222\n"
"v_add_f32_e32 v47, v47, v223\n"
"v_mfma_f32_16x16x32_bf16 v[116:119], a[28:31], v[68:71], v[116:119]\n"
"buffer_load_dwordx4 v[124:127], v32, s[4:7], s97 offen offset:1024 sc1\n"
"v_mfma_f32_16x16x32_bf16 v[128:131], a[60:63], v[68:71], 0\n"
"buffer_load_dword v253, v10, s[4:7], s98 offen offset:256 sc1\n"
"v_mfma_f32_16x16x32_bf16 v[128:131], a[56:59], v[200:203], v[128:131]\n"
"v_add_f32_e32 v24, v24, v228\n"
"v_add_f32_e32 v25, v25, v229\n"
"v_mfma_f32_16x16x32_bf16 v[128:131], a[52:55], v[176:179], v[128:131]\n"
"s_waitcnt vmcnt(11)\n"
"buffer_store_dwordx2 v[18:19], v11, s[4:7], s68 offen offset:96 sc1\n"
"v_mfma_f32_16x16x32_bf16 v[128:131], a[48:51], v[168:171], v[128:131]\n"
"buffer_load_dword v252, v10, s[4:7], s98 offen offset:512 sc1\n"
"v_mfma_f32_16x16x32_bf16 v[128:131], a[44:47], v[156:159], v[128:131]\n"
"buffer_load_dwordx4 v[132:135], v33, s[4:7], s97 offen sc1\n"
"v_mfma_f32_16x16x32_bf16 v[128:131], a[40:43], v[160:163], v[128:131]\n"
"ds_write_b128 v3, v[116:119]\n"
"v_mfma_f32_16x16x32_bf16 v[128:131], a[36:39], v[164:167], v[128:131]\n"
"buffer_load_dword v251, v10, s[4:7], s98 offen offset:768 sc1\n"
"v_mfma_f32_16x16x32_bf16 v[128:131], a[32:35], v[148:151], v[128:131]\n"
"v_add_f32_e32 v26, v26, v230\n"
"v_add_f32_e32 v27, v27, v231\n"
"v_mfma_f32_16x16x32_bf16 v[116:119], a[64:67], v[148:151], 0\n"
"v_mul_f32_e32 v44, v44, v254\n"
"v_mul_f32_e32 v45, v45, v254\n"
"v_mfma_f32_16x16x32_bf16 v[116:119], a[68:71], v[164:167], v[116:119]\n"
"buffer_load_dwordx4 v[136:139], v33, s[4:7], s97 offen offset:1024 sc1\n"
"v_mfma_f32_16x16x32_bf16 v[116:119], a[72:75], v[160:163], v[116:119]\n"
"buffer_load_dword v250, v10, s[4:7], s98 offen offset:1024 sc1\n"
"v_mfma_f32_16x16x32_bf16 v[116:119], a[76:79], v[156:159], v[116:119]\n"
"v_mul_f32_e32 v46, v46, v254\n"
"v_mul_f32_e32 v47, v47, v254\n"
"v_mfma_f32_16x16x32_bf16 v[116:119], a[80:83], v[168:171], v[116:119]\n"
"v_mul_f32_e32 v24, v24, v254\n"
"v_mul_f32_e32 v25, v25, v254\n"
"v_mfma_f32_16x16x32_bf16 v[116:119], a[84:87], v[176:179], v[116:119]\n"
"ds_write_b128 v3, v[128:131] offset:1024\n"
"v_mfma_f32_16x16x32_bf16 v[116:119], a[88:91], v[200:203], v[116:119]\n"
"buffer_load_dwordx4 v[128:131], v34, s[4:7], s97 offen sc1\n"
"v_mfma_f32_16x16x32_bf16 v[116:119], a[92:95], v[68:71], v[116:119]\n"
"buffer_load_dword v249, v10, s[4:7], s98 offen offset:1280 sc1\n"
"v_mfma_f32_16x16x32_bf16 v[140:143], a[124:127], v[68:71], 0\n"
"v_mul_f32_e32 v26, v26, v254\n"
"v_mul_f32_e32 v27, v27, v254\n"
"v_mfma_f32_16x16x32_bf16 v[140:143], a[120:123], v[200:203], v[140:143]\n"
"v_cvt_pk_bf16_f32 v144, v44, v45\n"
"v_cvt_pk_bf16_f32 v145, v46, v47\n"
"v_mfma_f32_16x16x32_bf16 v[140:143], a[116:119], v[176:179], v[140:143]\n"
"buffer_load_dword v254, v10, s[4:7], s98 offen offset:1536 sc1\n"
"v_mfma_f32_16x16x32_bf16 v[140:143], a[112:115], v[168:171], v[140:143]\n"
"buffer_load_dwordx4 v[44:47], v34, s[4:7], s97 offen offset:1024 sc1\n"
"v_mfma_f32_16x16x32_bf16 v[140:143], a[108:111], v[156:159], v[140:143]\n"
"v_cvt_pk_bf16_f32 v146, v24, v25\n"
"v_cvt_pk_bf16_f32 v147, v26, v27\n"
"v_mfma_f32_16x16x32_bf16 v[140:143], a[104:107], v[160:163], v[140:143]\n"
"ds_write_b128 v3, v[116:119] offset:2048\n"
"v_mfma_f32_16x16x32_bf16 v[140:143], a[100:103], v[164:167], v[140:143]\n"
"buffer_load_dword v248, v10, s[4:7], s98 offen offset:1792 sc1\n"
"v_mfma_f32_16x16x32_bf16 v[140:143], a[96:99], v[148:151], v[140:143]\n"
"ds_write_b128 v2, v[144:147] offset:49152\n"
"v_mfma_f32_16x16x32_bf16 v[24:27], v[28:31], v[148:151], 0\n"
"buffer_load_dwordx4 v[116:119], v35, s[4:7], s97 offen sc1\n"
"v_mfma_f32_16x16x32_bf16 v[24:27], v[52:55], v[164:167], v[24:27]\n"
"v_mfma_f32_16x16x32_bf16 v[24:27], v[84:87], v[160:163], v[24:27]\n"
"v_mfma_f32_16x16x32_bf16 v[24:27], v[92:95], v[156:159], v[24:27]\n"
"v_mfma_f32_16x16x32_bf16 v[24:27], v[100:103], v[168:171], v[24:27]\n"
"v_mfma_f32_16x16x32_bf16 v[24:27], v[108:111], v[176:179], v[24:27]\n"
"buffer_load_dwordx4 v[144:147], v35, s[4:7], s97 offen offset:1024 sc1\n"
"v_mfma_f32_16x16x32_bf16 v[24:27], v[180:183], v[200:203], v[24:27]\n"
"ds_write_b128 v3, v[140:143] offset:3072\n"
"v_mfma_f32_16x16x32_bf16 v[24:27], v[188:191], v[68:71], v[24:27]\n"
"v_mfma_f32_16x16x32_bf16 v[140:143], v[192:195], v[68:71], 0\n"
"v_mfma_f32_16x16x32_bf16 v[140:143], v[184:187], v[200:203], v[140:143]\n"
"v_mfma_f32_16x16x32_bf16 v[140:143], v[112:115], v[176:179], v[140:143]\n"
"v_mfma_f32_16x16x32_bf16 v[140:143], v[104:107], v[168:171], v[140:143]\n"
"v_mfma_f32_16x16x32_bf16 v[140:143], v[96:99], v[156:159], v[140:143]\n"
"v_mfma_f32_16x16x32_bf16 v[140:143], v[88:91], v[160:163], v[140:143]\n"
"s_nop 1\n"
"ds_write_b128 v3, v[24:27] offset:4096\n"
"v_mfma_f32_16x16x32_bf16 v[140:143], v[56:59], v[164:167], v[140:143]\n"
"v_mfma_f32_16x16x32_bf16 v[140:143], v[48:51], v[148:151], v[140:143]\n"
"s_waitcnt vmcnt(21)\n"
"; @A G1 1.1\n"
"v_mfma_f32_16x16x32_bf16 v[24:27], a[0:3], v[76:79], 0\n"
"v_mov_b32_e32 v68, -1\n"
"v_mov_b32_e32 v69, -1\n"
"v_mfma_f32_16x16x32_bf16 v[24:27], a[4:7], v[64:67], v[24:27]\n"
"v_mov_b32_e32 v70, -1\n"
"v_mov_b32_e32 v71, -1\n"
"v_mfma_f32_16x16x32_bf16 v[24:27], a[8:11], v[208:211], v[24:27]\n"
"s_add_u32 s98, s55, 0x48000\n"
"buffer_load_dwordx4 v[148:151], v32, s[4:7], s98 offen sc1\n"
"v_mfma_f32_16x16x32_bf16 v[24:27], a[12:15], v[60:63], v[24:27]\n"
"s_waitcnt vmcnt(18)\n"
"ds_read_b64_tr_b16 v[156:157], v36\n"
"ds_read_b64_tr_b16 v[158:159], v36 offset:2048\n"
"s_mov_b32 s96, 0x30003\n"
"v_mfma_f32_16x16x32_bf16 v[24:27], a[16:19], v[72:75], v[24:27]\n"
"s_mov_b32 s97, 0x30003\n"
"v_mov_b32_e32 v163, 0x3f803f80\n"
"v_cndmask_b32_e64 v160, 0, v163, s[96:97]\n"
"v_cndmask_b32_e64 v161, 0, v163, s[96:97]\n"
"v_mfma_f32_16x16x32_bf16 v[24:27], a[20:23], v[204:207], v[24:27]\n"
"ds_write_b128 v3, v[140:143] offset:5120\n"
"v_mfma_f32_16x16x32_bf16 v[24:27], a[24:27], v[152:155], v[24:27]\n"
"v_cndmask_b32_e64 v162, 0, v163, s[96:97]\n"
"v_cndmask_b32_e64 v163, 0, v163, s[96:97]\n"
"v_mfma_f32_16x16x32_bf16 v[24:27], a[28:31], v[172:175], v[24:27]\n"
"buffer_load_dwordx4 v[140:143], v32, s[4:7], s98 offen offset:1024 sc1\n"
"s_waitcnt lgkmcnt(1)\n"
"v_mfma_f32_16x16x32_bf16 v[164:167], v[156:159], v[160:163], 0\n"
"buffer_store_dwordx4 v[156:159], v0, s[4:7], s20 offen sc1\n"
"s_nop 1\n"
"v_mfma_f32_16x16x32_bf16 v[156:159], a[60:63], v[172:175], 0\n"
"v_mfma_f32_16x16x32_bf16 v[156:159], a[56:59], v[152:155], v[156:159]\n"
"v_mfma_f32_16x16x32_bf16 v[156:159], a[52:55], v[204:207], v[156:159]\n"
"v_mfma_f32_16x16x32_bf16 v[156:159], a[48:51], v[72:75], v[156:159]\n"
"v_mfma_f32_16x16x32_bf16 v[156:159], a[44:47], v[60:63], v[156:159]\n"
"buffer_load_dwordx4 v[160:163], v33, s[4:7], s98 offen sc1\n"
"v_mfma_f32_16x16x32_bf16 v[156:159], a[40:43], v[208:211], v[156:159]\n"
"ds_write_b128 v3, v[24:27] offset:6144\n"
"v_mfma_f32_16x16x32_bf16 v[156:159], a[36:39], v[64:67], v[156:159]\n"
"s_add_u32 s97, s67, 0\n"
"buffer_load_dwordx2 v[246:247], v12, s[4:7], s97 offen sc1\n"
"s_add_u32 s96, s64, 0x2000\n"
"v_mfma_f32_16x16x32_bf16 v[156:159], a[32:35], v[76:79], v[156:159]\n"
"buffer_store_dwordx4 v[216:219], v7, s[4:7], s96 offen sc1\n"
"v_mfma_f32_16x16x32_bf16 v[24:27], a[64:67], v[76:79], 0\n"
"ds_read_b64_tr_b16 v[168:169], v36 offset:4096\n"
"ds_read_b64_tr_b16 v[170:171], v36 offset:6144\n"
"s_mov_b32 s94, 0x30003\n"
"v_mfma_f32_16x16x32_bf16 v[24:27], a[68:71], v[64:67], v[24:27]\n"
"buffer_load_dwordx4 v[176:179], v33, s[4:7], s98 offen offset:1024 sc1\n"
"s_mov_b32 s95, 0x30003\n"
"v_mfma_f32_16x16x32_bf16 v[24:27], a[72:75], v[208:211], v[24:27]\n"
"v_mov_b32_e32 v199, 0x3f803f80\n"
"v_cndmask_b32_e64 v196, 0, v199, s[94:95]\n"
"v_cndmask_b32_e64 v197, 0, v199, s[94:95]\n"
"v_mfma_f32_16x16x32_bf16 v[24:27], a[76:79], v[60:63], v[24:27]\n"
"v_cndmask_b32_e64 v198, 0, v199, s[94:95]\n"
"v_cndmask_b32_e64 v199, 0, v199, s[94:95]\n"
"v_mfma_f32_16x16x32_bf16 v[24:27], a[80:83], v[72:75], v[24:27]\n"
"s_waitcnt lgkmcnt(0)\n"
"v_mfma_f32_16x16x32_bf16 v[164:167], v[168:171], v[196:199], v[164:167]\n"
"buffer_store_dwordx4 v[168:171], v0, s[4:7], s20 offen offset:1024 sc1\n"
"v_mfma_f32_16x16x32_bf16 v[24:27], a[84:87], v[204:207], v[24:27]\n"
"ds_write_b128 v3, v[156:159] offset:7168\n"
"v_mfma_f32_16x16x32_bf16 v[24:27], a[88:91], v[152:155], v[24:27]\n"
"buffer_load_dwordx4 v[156:159], v34, s[4:7], s98 offen sc1\n"
"v_mfma_f32_16x16x32_bf16 v[24:27], a[92:95], v[172:175], v[24:27]\n"
"v_mfma_f32_16x16x32_bf16 v[168:171], a[124:127], v[172:175], 0\n"
"v_mfma_f32_16x16x32_bf16 v[168:171], a[120:123], v[152:155], v[168:171]\n"
"v_mfma_f32_16x16x32_bf16 v[168:171], a[116:119], v[204:207], v[168:171]\n"
"v_mfma_f32_16x16x32_bf16 v[168:171], a[112:115], v[72:75], v[168:171]\n"
"buffer_load_dwordx4 v[196:199], v34, s[4:7], s98 offen offset:1024 sc1\n"
"v_mfma_f32_16x16x32_bf16 v[168:171], a[108:111], v[60:63], v[168:171]\n"
"v_mfma_f32_16x16x32_bf16 v[168:171], a[104:107], v[208:211], v[168:171]\n"
"s_nop 0\n"
"ds_write_b128 v3, v[24:27] offset:8192\n"
"v_mfma_f32_16x16x32_bf16 v[168:171], a[100:103], v[64:67], v[168:171]\n"
"v_mfma_f32_16x16x32_bf16 v[168:171], a[96:99], v[76:79], v[168:171]\n"
"v_mfma_f32_16x16x32_bf16 v[24:27], v[28:31], v[76:79], 0\n"
"buffer_load_dwordx4 v[200:203], v35, s[4:7], s98 offen sc1\n"
"v_mfma_f32_16x16x32_bf16 v[24:27], v[52:55], v[64:67], v[24:27]\n"
"v_mfma_f32_16x16x32_bf16 v[24:27], v[84:87], v[208:211], v[24:27]\n"
"v_mfma_f32_16x16x32_bf16 v[24:27], v[92:95], v[60:63], v[24:27]\n"
"v_mfma_f32_16x16x32_bf16 v[24:27], v[100:103], v[72:75], v[24:27]\n"
"s_waitcnt vmcnt(13)\n"
"ds_read_b64_tr_b16 v[212:213], v37\n"
"v_add_f32_e32 v255, v255, v250\n"
"v_add_f32_e32 v253, v253, v249\n"
"v_mfma_f32_16x16x32_bf16 v[24:27], v[108:111], v[204:207], v[24:27]\n"
"buffer_load_dwordx4 v[216:219], v35, s[4:7], s98 offen offset:1024 sc1\n"
"s_add_u32 s98, s64, 0x6000\n"
"v_mfma_f32_16x16x32_bf16 v[24:27], v[180:183], v[152:155], v[24:27]\n"
"ds_write_b128 v3, v[168:171] offset:9216\n"
"v_mfma_f32_16x16x32_bf16 v[24:27], v[188:191], v[172:175], v[24:27]\n"
"buffer_store_dwordx4 v[80:83], v7, s[4:7], s98 offen sc1\n"
"s_nop 1\n"
"v_mfma_f32_16x16x32_bf16 v[80:83], v[192:195], v[172:175], 0\n"
"ds_read_b64_tr_b16 v[214:215], v37 offset:2048\n"
"v_add_f32_e32 v252, v252, v254\n"
"v_add_f32_e32 v251, v251, v248\n"
"v_mfma_f32_16x16x32_bf16 v[80:83], v[184:187], v[152:155], v[80:83]\n"
"s_mov_b32 s94, 0xc000c\n"
"v_add_f32_e32 v255, v255, v252\n"
"v_add_f32_e32 v253, v253, v251\n"
"s_mov_b32 s95, 0xc000c\n"
"v_mfma_f32_16x16x32_bf16 v[80:83], v[112:115], v[204:207], v[80:83]\n"
"v_add_f32_e32 v255, v255, v253\n"
"v_mov_b32_e32 v253, v255\n"
"v_mov_b32_e32 v155, 0x3f803f80\n"
"v_mfma_f32_16x16x32_bf16 v[80:83], v[104:107], v[72:75], v[80:83]\n"
"v_permlane32_swap_b32_e32 v255, v253\n"
"v_add_f32_e32 v255, v255, v253\n"
"v_cndmask_b32_e64 v152, 0, v155, s[94:95]\n"
"v_mfma_f32_16x16x32_bf16 v[80:83], v[96:99], v[60:63], v[80:83]\n"
"v_mul_f32_e32 v255, 0x3a800000, v255\n"
"v_mov_b32_e32 v253, v255\n"
"v_cndmask_b32_e64 v153, 0, v155, s[94:95]\n"
"v_mfma_f32_16x16x32_bf16 v[80:83], v[88:91], v[208:211], v[80:83]\n"
"ds_write_b128 v3, v[24:27] offset:10240\n"
"v_mfma_f32_16x16x32_bf16 v[80:83], v[56:59], v[64:67], v[80:83]\n"
"v_permlane16_swap_b32_e32 v255, v253\n"
"v_cvt_pk_bf16_f32 v254, -v255, 0\n"
"v_cndmask_b32_e64 v154, 0, v155, s[94:95]\n"
"v_mfma_f32_16x16x32_bf16 v[80:83], v[48:51], v[76:79], v[80:83]\n"
"v_fma_f32 v253, -v255, v255, v253\n"
"v_lshlrev_b32_e32 v254, 16, v254\n"
"v_cndmask_b32_e64 v155, 0, v155, s[94:95]\n"
"; @A G1 1.2\n"
"v_mfma_f32_16x16x32_bf16 v[24:27], a[0:3], v[120:123], 0\n"
"s_waitcnt lgkmcnt(1)\n"
"v_max_f32_e32 v253, 0, v253\n"
"v_cvt_pk_bf16_f32 v253, v253, 0\n"
"v_mfma_f32_16x16x32_bf16 v[164:167], v[212:215], v[152:155], v[164:167]\n"
"buffer_store_dwordx4 v[212:215], v0, s[4:7], s21 offen sc1\n"
"v_mfma_f32_16x16x32_bf16 v[24:27], a[4:7], v[124:127], v[24:27]\n"
"s_waitcnt vmcnt(9)\n"
"v_cmp_ne_u64_e32 vcc, v[246:247], v[18:19]\n"
"v_mfma_f32_16x16x32_bf16 v[24:27], a[8:11], v[132:135], v[24:27]\n"
"s_cbranch_vccnz .Lmk_f2s\n"
".Lmk_f2r:\n"
"v_lshlrev_b32_e32 v253, 16, v253\n"
"v_mfma_f32_16x16x32_bf16 v[24:27], a[12:15], v[136:139], v[24:27]\n"
"v_add_f32_e32 v253, 0x3727c5ac, v253\n"
"v_cvt_pk_bf16_f32 v253, v253, 0\n"
"v_mfma_f32_16x16x32_bf16 v[24:27], a[16:19], v[128:131], v[24:27]\n"
"v_lshlrev_b32_e32 v253, 16, v253\n"
"v_mfma_f32_16x16x32_bf16 v[24:27], a[20:23], v[44:47], v[24:27]\n"
"ds_write_b128 v3, v[80:83] offset:11264\n"
"v_mfma_f32_16x16x32_bf16 v[24:27], a[24:27], v[116:119], v[24:27]\n"
"v_sqrt_f32_e32 v253, v253\n"
"v_mfma_f32_16x16x32_bf16 v[24:27], a[28:31], v[144:147], v[24:27]\n"
"v_cvt_pk_bf16_f32 v253, v253, 0\n"
"v_lshlrev_b32_e32 v253, 16, v253\n"
"v_mfma_f32_16x16x32_bf16 v[60:63], a[60:63], v[144:147], 0\n"
"v_rcp_f32_e32 v255, v253\n"
"v_mfma_f32_16x16x32_bf16 v[60:63], a[56:59], v[116:119], v[60:63]\n"
"v_mul_f32_e32 v253, 0x3e8293ee, v255\n"
"v_mfma_f32_16x16x32_bf16 v[60:63], a[52:55], v[44:47], v[60:63]\n"
"s_add_u32 s98, s52, 0x8000\n"
"buffer_load_dwordx4 v[64:67], v32, s[4:7], s98 offen sc1\n"
"v_mfma_f32_16x16x32_bf16 v[60:63], a[48:51], v[128:131], v[60:63]\n"
"s_waitcnt lgkmcnt(0)\n"
"s_barrier\n"
"ds_read_b64_tr_b16 v[72:73], v37 offset:4096\n"
"v_mfma_f32_16x16x32_bf16 v[60:63], a[44:47], v[136:139], v[60:63]\n"
"ds_read_b128 v[76:79], v4\n"
"ds_read_b128 v[80:83], v9 offset:128\n"
"ds_read_b64_tr_b16 v[74:75], v37 offset:6144\n"
"v_mfma_f32_16x16x32_bf16 v[60:63], a[40:43], v[132:135], v[60:63]\n"
"ds_write_b128 v3, v[24:27] offset:12288\n"
"v_mfma_f32_16x16x32_bf16 v[60:63], a[36:39], v[124:127], v[60:63]\n"
"ds_read_b128 v[24:27], v5\n"
"s_mov_b32 s96, 0xc000c\n"
"s_mov_b32 s97, 0xc000c\n"
"v_mfma_f32_16x16x32_bf16 v[60:63], a[32:35], v[120:123], v[60:63]\n"
"buffer_load_dwordx4 v[152:155], v32, s[4:7], s98 offen offset:1024 sc1\n"
"v_mov_b32_e32 v171, 0x3f803f80\n"
"v_mfma_f32_16x16x32_bf16 v[172:175], a[64:67], v[120:123], 0\n"
"v_cndmask_b32_e64 v168, 0, v171, s[96:97]\n"
"v_cndmask_b32_e64 v169, 0, v171, s[96:97]\n"
"v_cndmask_b32_e64 v170, 0, v171, s[96:97]\n"
"v_mfma_f32_16x16x32_bf16 v[172:175], a[68:71], v[124:127], v[172:175]\n"
"v_cndmask_b32_e64 v171, 0, v171, s[96:97]\n"
"v_mfma_f32_16x16x32_bf16 v[172:175], a[72:75], v[132:135], v[172:175]\n"
"s_waitcnt lgkmcnt(2)\n"
"v_mfma_f32_16x16x32_bf16 v[164:167], v[72:75], v[168:171], v[164:167]\n"
"buffer_store_dwordx4 v[72:75], v0, s[4:7], s21 offen offset:1024 sc1\n"
"v_mfma_f32_16x16x32_bf16 v[172:175], a[76:79], v[136:139], v[172:175]\n"
"ds_read_b128 v[72:75], v4 offset:1024\n"
"ds_read_b128 v[168:171], v9 offset:192\n"
"v_mfma_f32_16x16x32_bf16 v[172:175], a[80:83], v[128:131], v[172:175]\n"
"buffer_load_dwordx4 v[204:207], v33, s[4:7], s98 offen sc1\n"
"v_mfma_f32_16x16x32_bf16 v[172:175], a[84:87], v[44:47], v[172:175]\n"
"ds_write_b128 v3, v[60:63] offset:13312\n"
"v_mfma_f32_16x16x32_bf16 v[172:175], a[88:91], v[116:119], v[172:175]\n"
"ds_read_b128 v[60:63], v5 offset:1024\n"
"s_waitcnt lgkmcnt(4)\n"
"v_mfma_f32_16x16x32_bf16 v[172:175], a[92:95], v[144:147], v[172:175]\n"
"v_fmac_f32_e32 v76, v254, v80\n"
"v_fmac_f32_e32 v77, v254, v81\n"
"v_mfma_f32_16x16x32_bf16 v[208:211], a[124:127], v[144:147], 0\n"
"v_fmac_f32_e32 v78, v254, v82\n"
"v_fmac_f32_e32 v79, v254, v83\n"
"v_mfma_f32_16x16x32_bf16 v[208:211], a[120:123], v[116:119], v[208:211]\n"
"buffer_load_dwordx4 v[80:83], v33, s[4:7], s98 offen offset:1024 sc1\n"
"v_mfma_f32_16x16x32_bf16 v[208:211], a[116:119], v[44:47], v[208:211]\n"
"ds_read_b128 v[212:215], v4 offset:2048\n"
"ds_read_b128 v[220:223], v9 offset:256\n"
"v_mfma_f32_16x16x32_bf16 v[208:211], a[112:115], v[128:131], v[208:211]\n"
"ds_read_b128 v[224:227], v5 offset:2048\n"
"v_add_f32_e32 v76, v76, v24\n"
"v_mfma_f32_16x16x32_bf16 v[208:211], a[108:111], v[136:139], v[208:211]\n"
"v_add_f32_e32 v77, v77, v25\n"
"v_add_f32_e32 v78, v78, v26\n"
"v_mfma_f32_16x16x32_bf16 v[208:211], a[104:107], v[132:135], v[208:211]\n"
"ds_write_b128 v3, v[172:175] offset:14336\n"
"v_mfma_f32_16x16x32_bf16 v[208:211], a[100:103], v[124:127], v[208:211]\n"
"buffer_load_dwordx4 v[172:175], v34, s[4:7], s98 offen sc1\n"
"v_mfma_f32_16x16x32_bf16 v[208:211], a[96:99], v[120:123], v[208:211]\n"
"s_waitcnt lgkmcnt(4)\n"
"v_add_f32_e32 v79, v79, v27\n"
"v_fmac_f32_e32 v72, v254, v168\n"
"v_mfma_f32_16x16x32_bf16 v[24:27], v[28:31], v[120:123], 0\n"
"ds_read_b64_tr_b16 v[228:229], v38\n"
"v_fmac_f32_e32 v73, v254, v169\n"
"v_fmac_f32_e32 v74, v254, v170\n"
"v_mfma_f32_16x16x32_bf16 v[24:27], v[52:55], v[124:127], v[24:27]\n"
"ds_read_b128 v[232:235], v4 offset:3072\n"
"ds_read_b128 v[236:239], v9 offset:320\n"
"ds_read_b64_tr_b16 v[230:231], v38 offset:2048\n"
"v_mfma_f32_16x16x32_bf16 v[24:27], v[84:87], v[132:135], v[24:27]\n"
"ds_read_b128 v[240:243], v5 offset:3072\n"
"v_fmac_f32_e32 v75, v254, v171\n"
"s_mov_b32 s96, 0x300030\n"
"v_mfma_f32_16x16x32_bf16 v[24:27], v[92:95], v[136:139], v[24:27]\n"
"buffer_load_dwordx4 v[168:171], v34, s[4:7], s98 offen offset:1024 sc1\n"
"s_mov_b32 s97, 0x300030\n"
"v_mfma_f32_16x16x32_bf16 v[24:27], v[100:103], v[128:131], v[24:27]\n"
"v_add_f32_e32 v72, v72, v60\n"
"v_add_f32_e32 v73, v73, v61\n"
"v_mov_b32_e32 v247, 0x3f803f80\n"
"v_mfma_f32_16x16x32_bf16 v[24:27], v[108:111], v[44:47], v[24:27]\n"
"ds_write_b128 v3, v[208:211] offset:15360\n"
"v_mfma_f32_16x16x32_bf16 v[24:27], v[180:183], v[116:119], v[24:27]\n"
"s_waitcnt lgkmcnt(7)\n"
"v_add_f32_e32 v74, v74, v62\n"
"v_add_f32_e32 v75, v75, v63\n"
"v_cndmask_b32_e64 v244, 0, v247, s[96:97]\n"
"v_mfma_f32_16x16x32_bf16 v[24:27], v[188:191], v[144:147], v[24:27]\n"
"v_fmac_f32_e32 v212, v254, v220\n"
"v_fmac_f32_e32 v213, v254, v221\n"
"v_cndmask_b32_e64 v245, 0, v247, s[96:97]\n"
"v_mfma_f32_16x16x32_bf16 v[60:63], v[192:195], v[144:147], 0\n"
"buffer_load_dwordx4 v[144:147], v35, s[4:7], s98 offen sc1\n"
"v_cndmask_b32_e64 v246, 0, v247, s[96:97]\n"
"v_mfma_f32_16x16x32_bf16 v[60:63], v[184:187], v[116:119], v[60:63]\n"
"ds_read_b128 v[116:119], v4 offset:4096\n"
"ds_read_b128 v[208:211], v9\n"
"v_cndmask_b32_e64 v247, 0, v247, s[96:97]\n"
"v_mfma_f32_16x16x32_bf16 v[60:63], v[112:115], v[44:47], v[60:63]\n"
"ds_read_b128 v[44:47], v5 offset:4096\n"
"v_fmac_f32_e32 v214, v254, v222\n"
"s_waitcnt lgkmcnt(5)\n"
"v_mfma_f32_16x16x32_bf16 v[164:167], v[228:231], v[244:247], v[164:167]\n"
"buffer_store_dwordx4 v[228:231], v0, s[4:7], s22 offen sc1\n"
"v_mfma_f32_16x16x32_bf16 v[60:63], v[104:107], v[128:131], v[60:63]\n"
"v_fmac_f32_e32 v215, v254, v223\n"
"v_add_f32_e32 v212, v212, v224\n"
"v_mfma_f32_16x16x32_bf16 v[60:63], v[96:99], v[136:139], v[60:63]\n"
"s_add_u32 s97, s75, 0\n"
"buffer_load_dwordx2 v[250:251], v41, s[4:7], s97 offen sc1\n"
"v_mfma_f32_16x16x32_bf16 v[60:63], v[88:91], v[132:135], v[60:63]\n"
"buffer_load_dwordx4 v[128:131], v35, s[4:7], s98 offen offset:1024 sc1\n"
"v_mfma_f32_16x16x32_bf16 v[60:63], v[56:59], v[124:127], v[60:63]\n"
"ds_write_b128 v3, v[24:27] offset:16384\n"
"v_mfma_f32_16x16x32_bf16 v[60:63], v[48:51], v[120:123], v[60:63]\n"
"s_waitcnt vmcnt(18)\n"
"ds_read_b128 v[24:27], v4 offset:5120\n"
"ds_read_b128 v[120:123], v9 offset:64\n"
"; @A G1 1.3\n"
"v_mfma_f32_16x16x32_bf16 v[76:79], a[0:3], v[148:151], v[76:79]\n"
"ds_read_b128 v[124:127], v5 offset:5120\n"
"v_add_f32_e32 v213, v213, v225\n"
"v_mfma_f32_16x16x32_bf16 v[76:79], a[4:7], v[140:143], v[76:79]\n"
"s_waitcnt lgkmcnt(8)\n"
"v_add_f32_e32 v214, v214, v226\n"
"v_add_f32_e32 v215, v215, v227\n"
"v_mfma_f32_16x16x32_bf16 v[76:79], a[8:11], v[160:163], v[76:79]\n"
"s_add_u32 s98, s53, 0x8000\n"
"buffer_load_dwordx4 v[132:135], v32, s[4:7], s98 offen sc1\n"
"v_mfma_f32_16x16x32_bf16 v[76:79], a[12:15], v[176:179], v[76:79]\n"
"s_waitcnt vmcnt(14)\n"
"v_fmac_f32_e32 v232, v254, v236\n"
"v_fmac_f32_e32 v233, v254, v237\n"
"v_mfma_f32_16x16x32_bf16 v[76:79], a[16:19], v[156:159], v[76:79]\n"
"v_fmac_f32_e32 v234, v254, v238\n"
"v_fmac_f32_e32 v235, v254, v239\n"
"v_mfma_f32_16x16x32_bf16 v[76:79], a[20:23], v[196:199], v[76:79]\n"
"ds_write_b128 v3, v[60:63] offset:17408\n"
"v_mfma_f32_16x16x32_bf16 v[76:79], a[24:27], v[200:203], v[76:79]\n"
"v_add_f32_e32 v232, v232, v240\n"
"v_add_f32_e32 v233, v233, v241\n"
"v_mfma_f32_16x16x32_bf16 v[76:79], a[28:31], v[216:219], v[76:79]\n"
"buffer_load_dwordx4 v[60:63], v32, s[4:7], s98 offen offset:1024 sc1\n"
"v_mfma_f32_16x16x32_bf16 v[72:75], a[60:63], v[216:219], v[72:75]\n"
"s_waitcnt lgkmcnt(5)\n"
"v_add_f32_e32 v234, v234, v242\n"
"v_add_f32_e32 v235, v235, v243\n"
"v_mfma_f32_16x16x32_bf16 v[72:75], a[56:59], v[200:203], v[72:75]\n"
"v_fmac_f32_e32 v116, v254, v208\n"
"v_fmac_f32_e32 v117, v254, v209\n"
"v_mfma_f32_16x16x32_bf16 v[72:75], a[52:55], v[196:199], v[72:75]\n"
"s_waitcnt lgkmcnt(0)\n"
"s_barrier\n"
"v_mfma_f32_16x16x32_bf16 v[72:75], a[48:51], v[156:159], v[72:75]\n"
"ds_read_b128 v[136:139], v6\n"
"ds_read_b128 v[220:223], v6 offset:1024\n"
"v_mfma_f32_16x16x32_bf16 v[72:75], a[44:47], v[176:179], v[72:75]\n"
"buffer_load_dwordx4 v[224:227], v33, s[4:7], s98 offen sc1\n"
"v_mfma_f32_16x16x32_bf16 v[72:75], a[40:43], v[160:163], v[72:75]\n"
"v_fmac_f32_e32 v118, v254, v210\n"
"v_fmac_f32_e32 v119, v254, v211\n"
"v_mfma_f32_16x16x32_bf16 v[72:75], a[36:39], v[140:143], v[72:75]\n"
"s_add_u32 s96, s60, 0x48000\n"
"buffer_store_dwordx4 v[68:71], v0, s[4:7], s96 offen sc1\n"
"v_add_f32_e32 v116, v116, v44\n"
"v_add_f32_e32 v117, v117, v45\n"
"v_mfma_f32_16x16x32_bf16 v[72:75], a[32:35], v[148:151], v[72:75]\n"
"v_add_f32_e32 v118, v118, v46\n"
"v_add_f32_e32 v119, v119, v47\n"
"v_mfma_f32_16x16x32_bf16 v[212:215], a[64:67], v[148:151], v[212:215]\n"
"v_fmac_f32_e32 v24, v254, v120\n"
"v_fmac_f32_e32 v25, v254, v121\n"
"v_mfma_f32_16x16x32_bf16 v[212:215], a[68:71], v[140:143], v[212:215]\n"
"buffer_load_dwordx4 v[44:47], v33, s[4:7], s98 offen offset:1024 sc1\n"
"v_mfma_f32_16x16x32_bf16 v[212:215], a[72:75], v[160:163], v[212:215]\n"
"v_fmac_f32_e32 v26, v254, v122\n"
"v_fmac_f32_e32 v27, v254, v123\n"
"v_mfma_f32_16x16x32_bf16 v[212:215], a[76:79], v[176:179], v[212:215]\n"
"v_add_f32_e32 v24, v24, v124\n"
"v_add_f32_e32 v25, v25, v125\n"
"v_mfma_f32_16x16x32_bf16 v[212:215], a[80:83], v[156:159], v[212:215]\n"
"s_waitcnt lgkmcnt(0)\n"
"v_add_f32_e32 v26, v26, v126\n"
"v_add_f32_e32 v27, v27, v127\n"
"v_mfma_f32_16x16x32_bf16 v[212:215], a[84:87], v[196:199], v[212:215]\n"
"v_add_f32_e32 v76, v76, v136\n"
"v_add_f32_e32 v77, v77, v137\n"
"v_mfma_f32_16x16x32_bf16 v[212:215], a[88:91], v[200:203], v[212:215]\n"
"buffer_load_dwordx4 v[120:123], v34, s[4:7], s98 offen sc1\n"
"v_mfma_f32_16x16x32_bf16 v[212:215], a[92:95], v[216:219], v[212:215]\n"
"ds_read_b128 v[124:127], v6 offset:2048\n"
"ds_read_b128 v[208:211], v6 offset:3072\n"
"v_mfma_f32_16x16x32_bf16 v[232:235], a[124:127], v[216:219], v[232:235]\n"
"v_add_f32_e32 v78, v78, v138\n"
"v_add_f32_e32 v79, v79, v139\n"
"v_mfma_f32_16x16x32_bf16 v[232:235], a[120:123], v[200:203], v[232:235]\n"
"v_add_f32_e32 v72, v72, v220\n"
"v_add_f32_e32 v73, v73, v221\n"
"v_mfma_f32_16x16x32_bf16 v[232:235], a[116:119], v[196:199], v[232:235]\n"
"v_add_f32_e32 v74, v74, v222\n"
"v_add_f32_e32 v75, v75, v223\n"
"v_mfma_f32_16x16x32_bf16 v[232:235], a[112:115], v[156:159], v[232:235]\n"
"buffer_load_dwordx4 v[136:139], v34, s[4:7], s98 offen offset:1024 sc1\n"
"v_mfma_f32_16x16x32_bf16 v[232:235], a[108:111], v[176:179], v[232:235]\n"
"v_mul_f32_e32 v76, v76, v255\n"
"v_mul_f32_e32 v77, v77, v255\n"
"v_mfma_f32_16x16x32_bf16 v[232:235], a[104:107], v[160:163], v[232:235]\n"
"v_mul_f32_e32 v78, v78, v255\n"
"v_mul_f32_e32 v79, v79, v255\n"
"v_mfma_f32_16x16x32_bf16 v[232:235], a[100:103], v[140:143], v[232:235]\n"
"v_mul_f32_e32 v72, v72, v255\n"
"v_mul_f32_e32 v73, v73, v255\n"
"v_mfma_f32_16x16x32_bf16 v[232:235], a[96:99], v[148:151], v[232:235]\n"
"v_mul_f32_e32 v74, v74, v255\n"
"v_mul_f32_e32 v75, v75, v255\n"
"v_mfma_f32_16x16x32_bf16 v[116:119], v[28:31], v[148:151], v[116:119]\n"
"buffer_load_dwordx4 v[220:223], v35, s[4:7], s98 offen sc1\n"
"v_mfma_f32_16x16x32_bf16 v[116:119], v[52:55], v[140:143], v[116:119]\n"
"v_cvt_pk_bf16_f32 v228, v76, v77\n"
"v_cvt_pk_bf16_f32 v229, v78, v79\n"
"v_mfma_f32_16x16x32_bf16 v[116:119], v[84:87], v[160:163], v[116:119]\n"
"v_cvt_pk_bf16_f32 v230, v72, v73\n"
"v_cvt_pk_bf16_f32 v231, v74, v75\n"
"v_mfma_f32_16x16x32_bf16 v[116:119], v[92:95], v[176:179], v[116:119]\n"
"ds_write_b128 v8, v[228:231] offset:25600\n"
"v_mfma_f32_16x16x32_bf16 v[116:119], v[100:103], v[156:159], v[116:119]\n"
"s_waitcnt lgkmcnt(1)\n"
"s_add_u32 s96, s60, 0x40000\n"
"buffer_store_dwordx4 v[68:71], v0, s[4:7], s96 offen sc1\n"
"v_add_f32_e32 v212, v212, v124\n"
"v_add_f32_e32 v213, v213, v125\n"
"v_mfma_f32_16x16x32_bf16 v[116:119], v[108:111], v[196:199], v[116:119]\n"
"buffer_load_dwordx4 v[68:71], v35, s[4:7], s98 offen offset:1024 sc1\n"
"v_mfma_f32_16x16x32_bf16 v[116:119], v[180:183], v[200:203], v[116:119]\n"
"ds_read_b128 v[72:75], v6 offset:4096\n"
"ds_read_b128 v[76:79], v6 offset:5120\n"
"v_mfma_f32_16x16x32_bf16 v[116:119], v[188:191], v[216:219], v[116:119]\n"
"v_add_f32_e32 v214, v214, v126\n"
"v_add_f32_e32 v215, v215, v127\n"
"v_mfma_f32_16x16x32_bf16 v[24:27], v[192:195], v[216:219], v[24:27]\n"
"s_waitcnt vmcnt(11)\n"
"v_cmp_ne_u64_e32 vcc, v[250:251], v[18:19]\n"
"v_mfma_f32_16x16x32_bf16 v[24:27], v[184:187], v[200:203], v[24:27]\n"
"s_cbranch_vccnz .Lmk_f5s\n"
".Lmk_f5r:\n"
"v_add_f32_e32 v232, v232, v208\n"
"v_mfma_f32_16x16x32_bf16 v[24:27], v[112:115], v[196:199], v[24:27]\n"
"v_add_f32_e32 v233, v233, v209\n"
"v_add_f32_e32 v234, v234, v210\n"
"v_mfma_f32_16x16x32_bf16 v[24:27], v[104:107], v[156:159], v[24:27]\n"
"v_add_f32_e32 v235, v235, v211\n"
"v_mul_f32_e32 v212, v212, v255\n"
"v_mfma_f32_16x16x32_bf16 v[24:27], v[96:99], v[176:179], v[24:27]\n"
"v_mul_f32_e32 v213, v213, v255\n"
"v_mul_f32_e32 v214, v214, v255\n"
"v_mfma_f32_16x16x32_bf16 v[24:27], v[88:91], v[160:163], v[24:27]\n"
"v_mul_f32_e32 v215, v215, v255\n"
"v_mul_f32_e32 v232, v232, v255\n"
"v_mfma_f32_16x16x32_bf16 v[24:27], v[56:59], v[140:143], v[24:27]\n"
"v_mul_f32_e32 v233, v233, v255\n"
"v_mul_f32_e32 v234, v234, v255\n"
"v_mfma_f32_16x16x32_bf16 v[24:27], v[48:51], v[148:151], v[24:27]\n"
"v_mul_f32_e32 v235, v235, v255\n"
"v_cvt_pk_bf16_f32 v40, v212, v213\n"
"v_cvt_pk_bf16_f32 v41, v214, v215\n"
"v_cvt_pk_bf16_f32 v42, v232, v233\n"
"v_cvt_pk_bf16_f32 v43, v234, v235\n"
"ds_write_b128 v7, v[40:43] offset:58368\n"
".Lmk_z2:\n"
"; @A G1 2.0\n"
"v_mfma_f32_16x16x32_bf16 v[124:127], a[0:3], v[64:67], 0\n"
"v_mfma_f32_16x16x32_bf16 v[124:127], a[4:7], v[152:155], v[124:127]\n"
"s_add_u32 s98, s62, 0x800\n"
"v_mfma_f32_16x16x32_bf16 v[124:127], a[8:11], v[204:207], v[124:127]\n"
"s_add_u32 s97, s54, 0x8000\n"
"buffer_load_dwordx4 v[140:143], v32, s[4:7], s97 offen sc1\n"
"v_mfma_f32_16x16x32_bf16 v[124:127], a[12:15], v[80:83], v[124:127]\n"
"s_waitcnt vmcnt(11)\n"
"v_mfma_f32_16x16x32_bf16 v[124:127], a[16:19], v[172:175], v[124:127]\n"
"buffer_load_dword v255, v10, s[4:7], s98 offen sc1\n"
"v_mfma_f32_16x16x32_bf16 v[124:127], a[20:23], v[168:171], v[124:127]\n"
"s_waitcnt lgkmcnt(1)\n"
"v_add_f32_e32 v116, v116, v72\n"
"v_add_f32_e32 v117, v117, v73\n"
"v_mfma_f32_16x16x32_bf16 v[124:127], a[24:27], v[144:147], v[124:127]\n"
"v_add_f32_e32 v118, v118, v74\n"
"v_add_f32_e32 v119, v119, v75\n"
"v_mfma_f32_16x16x32_bf16 v[124:127], a[28:31], v[128:131], v[124:127]\n"
"buffer_load_dwordx4 v[72:75], v32, s[4:7], s97 offen offset:1024 sc1\n"
"v_mfma_f32_16x16x32_bf16 v[148:151], a[60:63], v[128:131], 0\n"
"s_add_u32 s96, s70, 0\n"
"buffer_load_dwordx4 a[128:131], v0, s[4:7], s96 offen sc1\n"
"v_mfma_f32_16x16x32_bf16 v[148:151], a[56:59], v[144:147], v[148:151]\n"
"buffer_load_dword v254, v10, s[4:7], s98 offen offset:256 sc1\n"
"v_mfma_f32_16x16x32_bf16 v[148:151], a[52:55], v[168:171], v[148:151]\n"
"v_add_f32_e32 v24, v24, v76\n"
"v_add_f32_e32 v25, v25, v77\n"
"v_mfma_f32_16x16x32_bf16 v[148:151], a[48:51], v[172:175], v[148:151]\n"
"v_add_f32_e32 v26, v26, v78\n"
"v_add_f32_e32 v27, v27, v79\n"
"v_mfma_f32_16x16x32_bf16 v[148:151], a[44:47], v[80:83], v[148:151]\n"
"buffer_load_dwordx4 v[76:79], v33, s[4:7], s97 offen sc1\n"
"v_mfma_f32_16x16x32_bf16 v[148:151], a[40:43], v[204:207], v[148:151]\n"
"ds_write_b128 v3, v[124:127]\n"
"v_mfma_f32_16x16x32_bf16 v[148:151], a[36:39], v[152:155], v[148:151]\n"
"buffer_load_dword v252, v10, s[4:7], s98 offen offset:512 sc1\n"
"v_mfma_f32_16x16x32_bf16 v[148:151], a[32:35], v[64:67], v[148:151]\n"
"v_mul_f32_e32 v116, v116, v253\n"
"v_mul_f32_e32 v117, v117, v253\n"
"v_mfma_f32_16x16x32_bf16 v[124:127], a[64:67], v[64:67], 0\n"
"v_mul_f32_e32 v118, v118, v253\n"
"v_mul_f32_e32 v119, v119, v253\n"
"v_mfma_f32_16x16x32_bf16 v[124:127], a[68:71], v[152:155], v[124:127]\n"
"buffer_load_dwordx4 v[156:159], v33, s[4:7], s97 offen offset:1024 sc1\n"
"v_mfma_f32_16x16x32_bf16 v[124:127], a[72:75], v[204:207], v[124:127]\n"
"buffer_load_dwordx4 a[132:135], v0, s[4:7], s96 offen offset:1024 sc1\n"
"v_mfma_f32_16x16x32_bf16 v[124:127], a[76:79], v[80:83], v[124:127]\n"
"buffer_load_dword v251, v10, s[4:7], s98 offen offset:768 sc1\n"
"v_mfma_f32_16x16x32_bf16 v[124:127], a[80:83], v[172:175], v[124:127]\n"
"v_mul_f32_e32 v24, v24, v253\n"
"v_mul_f32_e32 v25, v25, v253\n"
"v_mfma_f32_16x16x32_bf16 v[124:127], a[84:87], v[168:171], v[124:127]\n"
"ds_write_b128 v3, v[148:151] offset:1024\n"
"v_mfma_f32_16x16x32_bf16 v[124:127], a[88:91], v[144:147], v[124:127]\n"
"buffer_load_dwordx4 v[148:151], v34, s[4:7], s97 offen sc1\n"
"v_mfma_f32_16x16x32_bf16 v[124:127], a[92:95], v[128:131], v[124:127]\n"
"buffer_load_dword v250, v10, s[4:7], s98 offen offset:1024 sc1\n"
"v_mfma_f32_16x16x32_bf16 v[160:163], a[124:127], v[128:131], 0\n"
"v_mul_f32_e32 v26, v26, v253\n"
"v_mul_f32_e32 v27, v27, v253\n"
"v_mfma_f32_16x16x32_bf16 v[160:163], a[120:123], v[144:147], v[160:163]\n"
"v_cvt_pk_bf16_f32 v176, v116, v117\n"
"v_cvt_pk_bf16_f32 v177, v118, v119\n"
"v_mfma_f32_16x16x32_bf16 v[160:163], a[116:119], v[168:171], v[160:163]\n"
"buffer_load_dword v253, v10, s[4:7], s98 offen offset:1280 sc1\n"
"v_mfma_f32_16x16x32_bf16 v[160:163], a[112:115], v[172:175], v[160:163]\n"
"buffer_load_dwordx4 v[116:119], v34, s[4:7], s97 offen offset:1024 sc1\n"
"v_mfma_f32_16x16x32_bf16 v[160:163], a[108:111], v[80:83], v[160:163]\n"
"buffer_load_dwordx4 a[136:139], v0, s[4:7], s96 offen offset:2048 sc1\n"
"v_mfma_f32_16x16x32_bf16 v[160:163], a[104:107], v[204:207], v[160:163]\n"
"ds_write_b128 v3, v[124:127] offset:2048\n"
"v_mfma_f32_16x16x32_bf16 v[160:163], a[100:103], v[152:155], v[160:163]\n"
"buffer_load_dword v249, v10, s[4:7], s98 offen offset:1536 sc1\n"
"v_mfma_f32_16x16x32_bf16 v[160:163], a[96:99], v[64:67], v[160:163]\n"
"v_cvt_pk_bf16_f32 v178, v24, v25\n"
"v_cvt_pk_bf16_f32 v179, v26, v27\n"
"v_mfma_f32_16x16x32_bf16 v[24:27], v[28:31], v[64:67], 0\n"
"buffer_load_dwordx4 v[124:127], v35, s[4:7], s97 offen sc1\n"
"v_mfma_f32_16x16x32_bf16 v[24:27], v[52:55], v[152:155], v[24:27]\n"
"buffer_load_dword v248, v10, s[4:7], s98 offen offset:1792 sc1\n"
"v_mfma_f32_16x16x32_bf16 v[24:27], v[84:87], v[204:207], v[24:27]\n"
"ds_write_b128 v2, v[176:179] offset:50176\n"
"v_mfma_f32_16x16x32_bf16 v[24:27], v[92:95], v[80:83], v[24:27]\n"
"s_add_u32 s98, s64, 0x2000\n"
"buffer_store_dwordx4 v[228:231], v8, s[4:7], s98 offen offset:1024 sc1\n"
"v_mfma_f32_16x16x32_bf16 v[24:27], v[100:103], v[172:175], v[24:27]\n"
"v_mfma_f32_16x16x32_bf16 v[24:27], v[108:111], v[168:171], v[24:27]\n"
"buffer_load_dwordx4 v[176:179], v35, s[4:7], s97 offen offset:1024 sc1\n"
"v_mfma_f32_16x16x32_bf16 v[24:27], v[180:183], v[144:147], v[24:27]\n"
"ds_write_b128 v3, v[160:163] offset:3072\n"
"v_mfma_f32_16x16x32_bf16 v[24:27], v[188:191], v[128:131], v[24:27]\n"
"buffer_load_dwordx4 a[140:143], v0, s[4:7], s96 offen offset:3072 sc1\n"
"v_mfma_f32_16x16x32_bf16 v[160:163], v[192:195], v[128:131], 0\n"
"v_mfma_f32_16x16x32_bf16 v[160:163], v[184:187], v[144:147], v[160:163]\n"
"v_mfma_f32_16x16x32_bf16 v[160:163], v[112:115], v[168:171], v[160:163]\n"
"v_mfma_f32_16x16x32_bf16 v[160:163], v[104:107], v[172:175], v[160:163]\n"
"v_mfma_f32_16x16x32_bf16 v[160:163], v[96:99], v[80:83], v[160:163]\n"
"v_mfma_f32_16x16x32_bf16 v[160:163], v[88:91], v[204:207], v[160:163]\n"
"s_nop 0\n"
"ds_write_b128 v3, v[24:27] offset:4096\n"
"v_mfma_f32_16x16x32_bf16 v[160:163], v[56:59], v[152:155], v[160:163]\n"
"v_mfma_f32_16x16x32_bf16 v[160:163], v[48:51], v[64:67], v[160:163]\n"
"s_waitcnt vmcnt(26)\n"
"; @A G1 2.1\n"
"v_mfma_f32_16x16x32_bf16 v[24:27], a[0:3], v[132:135], 0\n"
"v_mov_b32_e32 v64, -1\n"
"v_mov_b32_e32 v65, -1\n"
"v_mfma_f32_16x16x32_bf16 v[24:27], a[4:7], v[60:63], v[24:27]\n"
"buffer_load_dwordx4 a[144:147], v1, s[4:7], s96 offen sc1\n"
"v_mfma_f32_16x16x32_bf16 v[24:27], a[8:11], v[224:227], v[24:27]\n"
"s_add_u32 s98, s55, 0x8000\n"
"buffer_load_dwordx4 v[80:83], v32, s[4:7], s98 offen sc1\n"
"v_mfma_f32_16x16x32_bf16 v[24:27], a[12:15], v[44:47], v[24:27]\n"
"s_waitcnt vmcnt(23)\n"
"v_mov_b32_e32 v66, -1\n"
"v_mov_b32_e32 v67, -1\n"
"v_mfma_f32_16x16x32_bf16 v[24:27], a[16:19], v[120:123], v[24:27]\n"
"ds_read_b64_tr_b16 v[128:129], v38 offset:4096\n"
"ds_read_b64_tr_b16 v[130:131], v38 offset:6144\n"
"s_mov_b32 s94, 0x300030\n"
"v_mfma_f32_16x16x32_bf16 v[24:27], a[20:23], v[136:139], v[24:27]\n"
"ds_write_b128 v3, v[160:163] offset:5120\n"
"v_mfma_f32_16x16x32_bf16 v[24:27], a[24:27], v[220:223], v[24:27]\n"
"s_mov_b32 s95, 0x300030\n"
"v_mov_b32_e32 v147, 0x3f803f80\n"
"v_cndmask_b32_e64 v144, 0, v147, s[94:95]\n"
"v_cndmask_b32_e64 v145, 0, v147, s[94:95]\n"
"v_mfma_f32_16x16x32_bf16 v[24:27], a[28:31], v[68:71], v[24:27]\n"
"buffer_load_dwordx4 v[152:155], v32, s[4:7], s98 offen offset:1024 sc1\n"
"v_cndmask_b32_e64 v146, 0, v147, s[94:95]\n"
"v_mfma_f32_16x16x32_bf16 v[160:163], a[60:63], v[68:71], 0\n"
"v_cndmask_b32_e64 v147, 0, v147, s[94:95]\n"
"v_mfma_f32_16x16x32_bf16 v[160:163], a[56:59], v[220:223], v[160:163]\n"
"s_add_u32 s97, s64, 0x6000\n"
"s_waitcnt lgkmcnt(1)\n"
"v_mfma_f32_16x16x32_bf16 v[164:167], v[128:131], v[144:147], v[164:167]\n"
"buffer_store_dwordx4 v[128:131], v0, s[4:7], s22 offen offset:1024 sc1\n"
"v_mfma_f32_16x16x32_bf16 v[160:163], a[52:55], v[136:139], v[160:163]\n"
"v_mfma_f32_16x16x32_bf16 v[160:163], a[48:51], v[120:123], v[160:163]\n"
"buffer_load_dwordx4 a[148:151], v1, s[4:7], s96 offen offset:1024 sc1\n"
"v_mfma_f32_16x16x32_bf16 v[160:163], a[44:47], v[44:47], v[160:163]\n"
"buffer_load_dwordx4 v[128:131], v33, s[4:7], s98 offen sc1\n"
"v_mfma_f32_16x16x32_bf16 v[160:163], a[40:43], v[224:227], v[160:163]\n"
"ds_write_b128 v3, v[24:27] offset:6144\n"
"v_mfma_f32_16x16x32_bf16 v[160:163], a[36:39], v[60:63], v[160:163]\n"
"buffer_store_dwordx4 v[40:43], v7, s[4:7], s97 offen offset:1024 sc1\n"
"v_mfma_f32_16x16x32_bf16 v[160:163], a[32:35], v[132:135], v[160:163]\n"
"buffer_store_dwordx4 v[164:167], v39, s[4:7], s23 offen sc1\n"
"v_mfma_f32_16x16x32_bf16 v[24:27], a[64:67], v[132:135], 0\n"
"v_mfma_f32_16x16x32_bf16 v[24:27], a[68:71], v[60:63], v[24:27]\n"
"buffer_load_dwordx4 v[36:39], v33, s[4:7], s98 offen offset:1024 sc1\n"
"v_mfma_f32_16x16x32_bf16 v[24:27], a[72:75], v[224:227], v[24:27]\n"
"v_mfma_f32_16x16x32_bf16 v[24:27], a[76:79], v[44:47], v[24:27]\n"
"v_mfma_f32_16x16x32_bf16 v[24:27], a[80:83], v[120:123], v[24:27]\n"
"v_mfma_f32_16x16x32_bf16 v[24:27], a[84:87], v[136:139], v[24:27]\n"
"ds_write_b128 v3, v[160:163] offset:7168\n"
"v_mfma_f32_16x16x32_bf16 v[24:27], a[88:91], v[220:223], v[24:27]\n"
"buffer_load_dwordx4 v[40:43], v34, s[4:7], s98 offen sc1\n"
"v_mfma_f32_16x16x32_bf16 v[24:27], a[92:95], v[68:71], v[24:27]\n"
"buffer_load_dwordx4 a[152:155], v1, s[4:7], s96 offen offset:2048 sc1\n"
"v_mfma_f32_16x16x32_bf16 v[144:147], a[124:127], v[68:71], 0\n"
"v_mfma_f32_16x16x32_bf16 v[144:147], a[120:123], v[220:223], v[144:147]\n"
"v_mfma_f32_16x16x32_bf16 v[144:147], a[116:119], v[136:139], v[144:147]\n"
"v_mfma_f32_16x16x32_bf16 v[144:147], a[112:115], v[120:123], v[144:147]\n"
"buffer_load_dwordx4 v[160:163], v34, s[4:7], s98 offen offset:1024 sc1\n"
"v_mfma_f32_16x16x32_bf16 v[144:147], a[108:111], v[44:47], v[144:147]\n"
"s_add_u32 s97, s51, 0\n"
"s_bitcmp1_b32 s26, 0\n"
"s_cbranch_scc1 .Lmk_w7s\n"
"buffer_store_dwordx4 a[0:3], v0, s[4:7], s97 offen sc1\n"
".Lmk_w7r:\n"
"v_mfma_f32_16x16x32_bf16 v[144:147], a[104:107], v[224:227], v[144:147]\n"
"ds_write_b128 v3, v[24:27] offset:8192\n"
"v_mfma_f32_16x16x32_bf16 v[144:147], a[100:103], v[60:63], v[144:147]\n"
"v_mfma_f32_16x16x32_bf16 v[144:147], a[96:99], v[132:135], v[144:147]\n"
"v_mfma_f32_16x16x32_bf16 v[24:27], v[28:31], v[132:135], 0\n"
"buffer_load_dwordx4 v[164:167], v35, s[4:7], s98 offen sc1\n"
"v_mfma_f32_16x16x32_bf16 v[24:27], v[52:55], v[60:63], v[24:27]\n"
"buffer_load_dwordx4 a[156:159], v1, s[4:7], s96 offen offset:3072 sc1\n"
"v_mfma_f32_16x16x32_bf16 v[24:27], v[84:87], v[224:227], v[24:27]\n"
"v_mfma_f32_16x16x32_bf16 v[24:27], v[92:95], v[44:47], v[24:27]\n"
"v_mfma_f32_16x16x32_bf16 v[24:27], v[100:103], v[120:123], v[24:27]\n"
"s_waitcnt vmcnt(18)\n"
"v_add_f32_e32 v255, v255, v250\n"
"v_add_f32_e32 v254, v254, v253\n"
"v_mfma_f32_16x16x32_bf16 v[24:27], v[108:111], v[136:139], v[24:27]\n"
"buffer_load_dwordx4 v[168:171], v35, s[4:7], s98 offen offset:1024 sc1\n"
"v_mfma_f32_16x16x32_bf16 v[24:27], v[180:183], v[220:223], v[24:27]\n"
"ds_write_b128 v3, v[144:147] offset:9216\n"
"v_mfma_f32_16x16x32_bf16 v[24:27], v[188:191], v[68:71], v[24:27]\n"
"v_add_f32_e32 v252, v252, v249\n"
"v_add_f32_e32 v251, v251, v248\n"
"v_mfma_f32_16x16x32_bf16 v[144:147], v[192:195], v[68:71], 0\n"
"v_add_f32_e32 v255, v255, v252\n"
"v_add_f32_e32 v254, v254, v251\n"
"v_mfma_f32_16x16x32_bf16 v[144:147], v[184:187], v[220:223], v[144:147]\n"
"s_add_u32 s98, s70, 0x2000\n"
"v_add_f32_e32 v255, v255, v254\n"
"v_mov_b32_e32 v254, v255\n"
"v_mfma_f32_16x16x32_bf16 v[144:147], v[112:115], v[136:139], v[144:147]\n"
"s_nop 0\n"
"v_permlane32_swap_b32_e32 v255, v254\n"
"v_add_f32_e32 v255, v255, v254\n"
"v_mfma_f32_16x16x32_bf16 v[144:147], v[104:107], v[120:123], v[144:147]\n"
"buffer_load_dwordx4 a[160:163], v0, s[4:7], s98 offen sc1\n"
"s_add_u32 s97, s51, 0x2000\n"
"v_mfma_f32_16x16x32_bf16 v[144:147], v[96:99], v[44:47], v[144:147]\n"
"s_bitcmp1_b32 s26, 0\n"
"v_mul_f32_e32 v255, 0x3a800000, v255\n"
"v_mov_b32_e32 v254, v255\n"
"s_cbranch_scc1 .Lmk_w8s\n"
"buffer_store_dwordx4 a[32:35], v0, s[4:7], s97 offen sc1\n"
".Lmk_w8r:\n"
"v_mfma_f32_16x16x32_bf16 v[144:147], v[88:91], v[224:227], v[144:147]\n"
"ds_write_b128 v3, v[24:27] offset:10240\n"
"v_mfma_f32_16x16x32_bf16 v[144:147], v[56:59], v[60:63], v[144:147]\n"
"v_permlane16_swap_b32_e32 v255, v254\n"
"v_cvt_pk_bf16_f32 v253, -v255, 0\n"
"v_mfma_f32_16x16x32_bf16 v[144:147], v[48:51], v[132:135], v[144:147]\n"
"v_fma_f32 v254, -v255, v255, v254\n"
"v_lshlrev_b32_e32 v253, 16, v253\n"
"; @A G1 2.2\n"
"v_mfma_f32_16x16x32_bf16 v[24:27], a[0:3], v[140:143], 0\n"
"v_max_f32_e32 v254, 0, v254\n"
"v_cvt_pk_bf16_f32 v254, v254, 0\n"
"v_mfma_f32_16x16x32_bf16 v[24:27], a[4:7], v[72:75], v[24:27]\n"
"v_lshlrev_b32_e32 v254, 16, v254\n"
"v_add_f32_e32 v254, 0x3727c5ac, v254\n"
"v_mfma_f32_16x16x32_bf16 v[24:27], a[8:11], v[76:79], v[24:27]\n"
"v_cvt_pk_bf16_f32 v254, v254, 0\n"
"v_lshlrev_b32_e32 v254, 16, v254\n"
"v_mfma_f32_16x16x32_bf16 v[24:27], a[12:15], v[156:159], v[24:27]\n"
"s_waitcnt vmcnt(19)\n"
"v_sqrt_f32_e32 v254, v254\n"
"v_mfma_f32_16x16x32_bf16 v[24:27], a[16:19], v[148:151], v[24:27]\n"
"s_mov_b32 s97, .Lmk_z4-.Lmk_g16\n"
"s_getpc_b64 s[84:85]\n"
".Lmk_g16:\n"
"s_add_u32 s84, s84, s97\n"
"s_addc_u32 s85, s85, 0\n"
"s_and_b32 s84, s84, -16\n"
"v_mfma_f32_16x16x32_bf16 v[24:27], a[20:23], v[116:119], v[24:27]\n"
"ds_write_b128 v3, v[144:147] offset:11264\n"
"v_mfma_f32_16x16x32_bf16 v[24:27], a[24:27], v[124:127], v[24:27]\n"
"buffer_load_dwordx4 a[164:167], v0, s[4:7], s98 offen offset:1024 sc1\n"
"s_and_b32 s85, s85, 0xffff\n"
"s_sub_u32 s97, .Lmk_end-.Lmk_g16, s97\n"
"v_mfma_f32_16x16x32_bf16 v[24:27], a[28:31], v[176:179], v[24:27]\n"
"s_min_u32 s86, s97, 0x4000\n"
"s_mov_b32 s87, 0x20000\n"
"v_cvt_pk_bf16_f32 v254, v254, 0\n"
"v_mfma_f32_16x16x32_bf16 v[44:47], a[60:63], v[176:179], 0\n"
"s_mov_b32 m0, 0x27800\n"
"s_nop 0\n"
"buffer_load_dwordx4 v0, s[84:87], s99 offen lds\n"
"v_mfma_f32_16x16x32_bf16 v[44:47], a[56:59], v[124:127], v[44:47]\n"
"buffer_store_dwordx4 v[64:67], v0, s[4:7], s60 offen sc1\n"
"v_lshlrev_b32_e32 v254, 16, v254\n"
"v_mfma_f32_16x16x32_bf16 v[44:47], a[52:55], v[116:119], v[44:47]\n"
"s_add_u32 s97, s52, 0\n"
"buffer_load_dwordx4 v[60:63], v32, s[4:7], s97 offen sc1\n"
"v_mfma_f32_16x16x32_bf16 v[44:47], a[48:51], v[148:151], v[44:47]\n"
"s_waitcnt lgkmcnt(0)\n"
"s_barrier\n"
"v_mfma_f32_16x16x32_bf16 v[44:47], a[44:47], v[156:159], v[44:47]\n"
"v_rcp_f32_e32 v255, v254\n"
"v_mfma_f32_16x16x32_bf16 v[44:47], a[40:43], v[76:79], v[44:47]\n"
"ds_write_b128 v3, v[24:27] offset:12288\n"
"v_mfma_f32_16x16x32_bf16 v[44:47], a[36:39], v[72:75], v[44:47]\n"
"ds_read_b128 v[24:27], v4\n"
"v_mul_f32_e32 v254, 0x3e8293ee, v255\n"
"v_mfma_f32_16x16x32_bf16 v[44:47], a[32:35], v[140:143], v[44:47]\n"
"buffer_load_dwordx4 v[68:71], v32, s[4:7], s97 offen offset:1024 sc1\n"
"v_mfma_f32_16x16x32_bf16 v[120:123], a[64:67], v[140:143], 0\n"
"buffer_load_dwordx4 a[168:171], v0, s[4:7], s98 offen offset:2048 sc1\n"
"v_mfma_f32_16x16x32_bf16 v[120:123], a[68:71], v[72:75], v[120:123]\n"
"ds_read_b128 v[132:135], v9 offset:128\n"
"ds_read_b128 v[136:139], v5\n"
"v_mfma_f32_16x16x32_bf16 v[120:123], a[72:75], v[76:79], v[120:123]\n"
"v_mfma_f32_16x16x32_bf16 v[120:123], a[76:79], v[156:159], v[120:123]\n"
"ds_read_b128 v[144:147], v4 offset:1024\n"
"ds_read_b128 v[172:175], v9 offset:192\n"
"v_mfma_f32_16x16x32_bf16 v[120:123], a[80:83], v[148:151], v[120:123]\n"
"buffer_load_dwordx4 v[196:199], v33, s[4:7], s97 offen sc1\n"
"v_mfma_f32_16x16x32_bf16 v[120:123], a[84:87], v[116:119], v[120:123]\n"
"ds_write_b128 v3, v[44:47] offset:13312\n"
"v_mfma_f32_16x16x32_bf16 v[120:123], a[88:91], v[124:127], v[120:123]\n"
"ds_read_b128 v[44:47], v5 offset:1024\n"
"s_waitcnt lgkmcnt(4)\n"
"v_mfma_f32_16x16x32_bf16 v[120:123], a[92:95], v[176:179], v[120:123]\n"
"v_fmac_f32_e32 v24, v253, v132\n"
"v_fmac_f32_e32 v25, v253, v133\n"
"v_mfma_f32_16x16x32_bf16 v[200:203], a[124:127], v[176:179], 0\n"
"s_add_u32 s96, s60, 0x8000\n"
"buffer_store_dwordx4 v[64:67], v0, s[4:7], s96 offen sc1\n"
"v_fmac_f32_e32 v26, v253, v134\n"
"v_fmac_f32_e32 v27, v253, v135\n"
"v_mfma_f32_16x16x32_bf16 v[200:203], a[120:123], v[124:127], v[200:203]\n"
"buffer_load_dwordx4 v[64:67], v33, s[4:7], s97 offen offset:1024 sc1\n"
"v_mfma_f32_16x16x32_bf16 v[200:203], a[116:119], v[116:119], v[200:203]\n"
"buffer_load_dwordx4 a[172:175], v0, s[4:7], s98 offen offset:3072 sc1\n"
"v_mfma_f32_16x16x32_bf16 v[200:203], a[112:115], v[148:151], v[200:203]\n"
"ds_read_b128 v[132:135], v4 offset:2048\n"
"ds_read_b128 v[204:207], v9 offset:256\n"
"v_mfma_f32_16x16x32_bf16 v[200:203], a[108:111], v[156:159], v[200:203]\n"
"ds_read_b128 v[208:211], v5 offset:2048\n"
"v_add_f32_e32 v24, v24, v136\n"
"v_mfma_f32_16x16x32_bf16 v[200:203], a[104:107], v[76:79], v[200:203]\n"
"ds_write_b128 v3, v[120:123] offset:14336\n"
"v_mfma_f32_16x16x32_bf16 v[200:203], a[100:103], v[72:75], v[200:203]\n"
"buffer_load_dwordx4 v[120:123], v34, s[4:7], s97 offen sc1\n"
"v_mfma_f32_16x16x32_bf16 v[200:203], a[96:99], v[140:143], v[200:203]\n"
"v_add_f32_e32 v25, v25, v137\n"
"v_add_f32_e32 v26, v26, v138\n"
"v_mfma_f32_16x16x32_bf16 v[212:215], v[28:31], v[140:143], 0\n"
"s_waitcnt lgkmcnt(4)\n"
"v_add_f32_e32 v27, v27, v139\n"
"v_fmac_f32_e32 v144, v253, v172\n"
"v_mfma_f32_16x16x32_bf16 v[212:215], v[52:55], v[72:75], v[212:215]\n"
"ds_read_b128 v[136:139], v4 offset:3072\n"
"ds_read_b128 v[216:219], v9 offset:320\n"
"v_mfma_f32_16x16x32_bf16 v[212:215], v[84:87], v[76:79], v[212:215]\n"
"s_waitcnt vmcnt(22)\n"
"s_add_u32 s96, s68, 0x1000\n"
"ds_read_b128 v[220:223], v5 offset:3072\n"
"v_mfma_f32_16x16x32_bf16 v[212:215], v[92:95], v[156:159], v[212:215]\n"
"buffer_load_dwordx4 v[224:227], v34, s[4:7], s97 offen offset:1024 sc1\n"
"v_mfma_f32_16x16x32_bf16 v[212:215], v[100:103], v[148:151], v[212:215]\n"
"buffer_store_dwordx2 v[18:19], v11, s[4:7], s96 offen offset:64 sc1\n"
"v_mfma_f32_16x16x32_bf16 v[212:215], v[108:111], v[116:119], v[212:215]\n"
"ds_write_b128 v3, v[200:203] offset:15360\n"
"v_mfma_f32_16x16x32_bf16 v[212:215], v[180:183], v[124:127], v[212:215]\n"
"buffer_load_dwordx4 a[176:179], v1, s[4:7], s98 offen sc1\n"
"v_mfma_f32_16x16x32_bf16 v[212:215], v[188:191], v[176:179], v[212:215]\n"
"v_fmac_f32_e32 v145, v253, v173\n"
"v_fmac_f32_e32 v146, v253, v174\n"
"v_mfma_f32_16x16x32_bf16 v[200:203], v[192:195], v[176:179], 0\n"
"buffer_load_dwordx4 v[176:179], v35, s[4:7], s97 offen sc1\n"
"v_mfma_f32_16x16x32_bf16 v[200:203], v[184:187], v[124:127], v[200:203]\n"
"ds_read_b128 v[124:127], v4 offset:4096\n"
"ds_read_b128 v[228:231], v9\n"
"v_mfma_f32_16x16x32_bf16 v[200:203], v[112:115], v[116:119], v[200:203]\n"
"ds_read_b128 v[116:119], v5 offset:4096\n"
"v_fmac_f32_e32 v147, v253, v175\n"
"v_mfma_f32_16x16x32_bf16 v[200:203], v[104:107], v[148:151], v[200:203]\n"
"v_add_f32_e32 v144, v144, v44\n"
"v_add_f32_e32 v145, v145, v45\n"
"v_mfma_f32_16x16x32_bf16 v[200:203], v[96:99], v[156:159], v[200:203]\n"
"s_waitcnt lgkmcnt(8)\n"
"v_add_f32_e32 v146, v146, v46\n"
"v_add_f32_e32 v147, v147, v47\n"
"v_mfma_f32_16x16x32_bf16 v[200:203], v[88:91], v[76:79], v[200:203]\n"
"buffer_load_dwordx4 v[44:47], v35, s[4:7], s97 offen offset:1024 sc1\n"
"v_mfma_f32_16x16x32_bf16 v[200:203], v[56:59], v[72:75], v[200:203]\n"
"ds_write_b128 v3, v[212:215] offset:16384\n"
"v_mfma_f32_16x16x32_bf16 v[200:203], v[48:51], v[140:143], v[200:203]\n"
"s_waitcnt vmcnt(25)\n"
"ds_read_b128 v[72:75], v4 offset:5120\n"
"ds_read_b128 v[76:79], v9 offset:64\n"
"; @A G1 2.3\n"
"v_mfma_f32_16x16x32_bf16 v[24:27], a[0:3], v[80:83], v[24:27]\n"
"buffer_load_dwordx4 a[180:183], v1, s[4:7], s98 offen offset:1024 sc1\n"
"v_mfma_f32_16x16x32_bf16 v[24:27], a[4:7], v[152:155], v[24:27]\n"
"ds_read_b128 v[140:143], v5 offset:5120\n"
"v_fmac_f32_e32 v132, v253, v204\n"
"v_mfma_f32_16x16x32_bf16 v[24:27], a[8:11], v[128:131], v[24:27]\n"
"s_add_u32 s97, s53, 0\n"
"buffer_load_dwordx4 v[148:151], v32, s[4:7], s97 offen sc1\n"
"v_mfma_f32_16x16x32_bf16 v[24:27], a[12:15], v[36:39], v[24:27]\n"
"s_waitcnt vmcnt(20)\n"
"v_fmac_f32_e32 v133, v253, v205\n"
"v_fmac_f32_e32 v134, v253, v206\n"
"v_mfma_f32_16x16x32_bf16 v[24:27], a[16:19], v[40:43], v[24:27]\n"
"v_fmac_f32_e32 v135, v253, v207\n"
"v_add_f32_e32 v132, v132, v208\n"
"v_mfma_f32_16x16x32_bf16 v[24:27], a[20:23], v[160:163], v[24:27]\n"
"ds_write_b128 v3, v[200:203] offset:17408\n"
"v_mfma_f32_16x16x32_bf16 v[24:27], a[24:27], v[164:167], v[24:27]\n"
"v_add_f32_e32 v133, v133, v209\n"
"v_add_f32_e32 v134, v134, v210\n"
"v_mfma_f32_16x16x32_bf16 v[24:27], a[28:31], v[168:171], v[24:27]\n"
"buffer_load_dwordx4 v[156:159], v32, s[4:7], s97 offen offset:1024 sc1\n"
"v_mfma_f32_16x16x32_bf16 v[144:147], a[60:63], v[168:171], v[144:147]\n"
"s_waitcnt lgkmcnt(9)\n"
"v_add_f32_e32 v135, v135, v211\n"
"v_fmac_f32_e32 v136, v253, v216\n"
"v_mfma_f32_16x16x32_bf16 v[144:147], a[56:59], v[164:167], v[144:147]\n"
"v_fmac_f32_e32 v137, v253, v217\n"
"v_fmac_f32_e32 v138, v253, v218\n"
"v_mfma_f32_16x16x32_bf16 v[144:147], a[52:55], v[160:163], v[144:147]\n"
"s_waitcnt lgkmcnt(0)\n"
"s_barrier\n"
"v_mfma_f32_16x16x32_bf16 v[144:147], a[48:51], v[40:43], v[144:147]\n"
"buffer_load_dwordx4 a[184:187], v1, s[4:7], s98 offen offset:2048 sc1\n"
"v_mfma_f32_16x16x32_bf16 v[144:147], a[44:47], v[36:39], v[144:147]\n"
"buffer_load_dwordx4 v[172:175], v33, s[4:7], s97 offen sc1\n"
"v_mfma_f32_16x16x32_bf16 v[144:147], a[40:43], v[128:131], v[144:147]\n"
"ds_read_b128 v[200:203], v6\n"
"ds_read_b128 v[204:207], v6 offset:1024\n"
"v_mfma_f32_16x16x32_bf16 v[144:147], a[36:39], v[152:155], v[144:147]\n"
"v_fmac_f32_e32 v139, v253, v219\n"
"v_add_f32_e32 v136, v136, v220\n"
"v_mfma_f32_16x16x32_bf16 v[144:147], a[32:35], v[80:83], v[144:147]\n"
"v_add_f32_e32 v137, v137, v221\n"
"v_add_f32_e32 v138, v138, v222\n"
"v_mfma_f32_16x16x32_bf16 v[132:135], a[64:67], v[80:83], v[132:135]\n"
"v_add_f32_e32 v139, v139, v223\n"
"v_fmac_f32_e32 v124, v253, v228\n"
"v_mfma_f32_16x16x32_bf16 v[132:135], a[68:71], v[152:155], v[132:135]\n"
"buffer_load_dwordx4 v[208:211], v33, s[4:7], s97 offen offset:1024 sc1\n"
"v_mfma_f32_16x16x32_bf16 v[132:135], a[72:75], v[128:131], v[132:135]\n"
"v_fmac_f32_e32 v125, v253, v229\n"
"v_fmac_f32_e32 v126, v253, v230\n"
"v_mfma_f32_16x16x32_bf16 v[132:135], a[76:79], v[36:39], v[132:135]\n"
"v_fmac_f32_e32 v127, v253, v231\n"
"v_add_f32_e32 v124, v124, v116\n"
"v_mfma_f32_16x16x32_bf16 v[132:135], a[80:83], v[40:43], v[132:135]\n"
"s_waitcnt lgkmcnt(0)\n"
"v_add_f32_e32 v125, v125, v117\n"
"v_add_f32_e32 v126, v126, v118\n"
"v_mfma_f32_16x16x32_bf16 v[132:135], a[84:87], v[160:163], v[132:135]\n"
"buffer_load_dwordx4 a[188:191], v1, s[4:7], s98 offen offset:3072 sc1\n"
"v_mfma_f32_16x16x32_bf16 v[132:135], a[88:91], v[164:167], v[132:135]\n"
"buffer_load_dwordx4 v[212:215], v34, s[4:7], s97 offen sc1\n"
"v_mfma_f32_16x16x32_bf16 v[132:135], a[92:95], v[168:171], v[132:135]\n"
"ds_read_b128 v[216:219], v6 offset:2048\n"
"ds_read_b128 v[220:223], v6 offset:3072\n"
"v_mfma_f32_16x16x32_bf16 v[136:139], a[124:127], v[168:171], v[136:139]\n"
"v_add_f32_e32 v127, v127, v119\n"
"v_fmac_f32_e32 v72, v253, v76\n"
"v_mfma_f32_16x16x32_bf16 v[136:139], a[120:123], v[164:167], v[136:139]\n"
"v_fmac_f32_e32 v73, v253, v77\n"
"v_fmac_f32_e32 v74, v253, v78\n"
"v_mfma_f32_16x16x32_bf16 v[136:139], a[116:119], v[160:163], v[136:139]\n"
"s_waitcnt vmcnt(24)\n"
"s_add_u32 s98, s68, 0x1000\n"
"buffer_store_dwordx2 v[18:19], v11, s[4:7], s98 offen offset:96 sc1\n"
"v_fmac_f32_e32 v75, v253, v79\n"
"v_add_f32_e32 v72, v72, v140\n"
"v_mfma_f32_16x16x32_bf16 v[136:139], a[112:115], v[40:43], v[136:139]\n"
"buffer_load_dwordx4 v[76:79], v34, s[4:7], s97 offen offset:1024 sc1\n"
"v_mfma_f32_16x16x32_bf16 v[136:139], a[108:111], v[36:39], v[136:139]\n"
"v_add_f32_e32 v73, v73, v141\n"
"v_add_f32_e32 v74, v74, v142\n"
"v_mfma_f32_16x16x32_bf16 v[136:139], a[104:107], v[128:131], v[136:139]\n"
"s_add_u32 s98, s70, 0x4000\n"
"v_add_f32_e32 v75, v75, v143\n"
"v_add_f32_e32 v24, v24, v200\n"
"v_mfma_f32_16x16x32_bf16 v[136:139], a[100:103], v[152:155], v[136:139]\n"
"v_add_f32_e32 v25, v25, v201\n"
"v_add_f32_e32 v26, v26, v202\n"
"v_mfma_f32_16x16x32_bf16 v[136:139], a[96:99], v[80:83], v[136:139]\n"
"buffer_load_dwordx4 a[192:195], v0, s[4:7], s98 offen sc1\n"
"v_mfma_f32_16x16x32_bf16 v[124:127], v[28:31], v[80:83], v[124:127]\n"
"buffer_load_dwordx4 v[116:119], v35, s[4:7], s97 offen sc1\n"
"v_mfma_f32_16x16x32_bf16 v[124:127], v[52:55], v[152:155], v[124:127]\n"
"v_add_f32_e32 v27, v27, v203\n"
"v_add_f32_e32 v144, v144, v204\n"
"v_mfma_f32_16x16x32_bf16 v[124:127], v[84:87], v[128:131], v[124:127]\n"
"v_add_f32_e32 v145, v145, v205\n"
"v_add_f32_e32 v146, v146, v206\n"
"v_mfma_f32_16x16x32_bf16 v[124:127], v[92:95], v[36:39], v[124:127]\n"
"v_add_f32_e32 v147, v147, v207\n"
"v_mul_f32_e32 v24, v24, v255\n"
"v_mfma_f32_16x16x32_bf16 v[124:127], v[100:103], v[40:43], v[124:127]\n"
"s_waitcnt lgkmcnt(0)\n"
"v_mul_f32_e32 v25, v25, v255\n"
"v_mul_f32_e32 v26, v26, v255\n"
"v_mfma_f32_16x16x32_bf16 v[124:127], v[108:111], v[160:163], v[124:127]\n"
"buffer_load_dwordx4 v[140:143], v35, s[4:7], s97 offen offset:1024 sc1\n"
"v_mfma_f32_16x16x32_bf16 v[124:127], v[180:183], v[164:167], v[124:127]\n"
"ds_read_b128 v[200:203], v6 offset:4096\n"
"ds_read_b128 v[204:207], v6 offset:5120\n"
"v_mfma_f32_16x16x32_bf16 v[124:127], v[188:191], v[168:171], v[124:127]\n"
"v_mul_f32_e32 v27, v27, v255\n"
"v_mul_f32_e32 v144, v144, v255\n"
"v_mfma_f32_16x16x32_bf16 v[72:75], v[192:195], v[168:171], v[72:75]\n"
"v_mul_f32_e32 v145, v145, v255\n"
"v_mul_f32_e32 v146, v146, v255\n"
"v_mfma_f32_16x16x32_bf16 v[72:75], v[184:187], v[164:167], v[72:75]\n"
"buffer_load_dwordx4 a[196:199], v0, s[4:7], s98 offen offset:1024 sc1\n"
"v_mfma_f32_16x16x32_bf16 v[72:75], v[112:115], v[160:163], v[72:75]\n"
"v_mul_f32_e32 v147, v147, v255\n"
"v_cvt_pk_bf16_f32 v160, v24, v25\n"
"v_mfma_f32_16x16x32_bf16 v[72:75], v[104:107], v[40:43], v[72:75]\n"
"v_cvt_pk_bf16_f32 v161, v26, v27\n"
"v_cvt_pk_bf16_f32 v162, v144, v145\n"
"v_mfma_f32_16x16x32_bf16 v[72:75], v[96:99], v[36:39], v[72:75]\n"
"v_cvt_pk_bf16_f32 v163, v146, v147\n"
"v_add_f32_e32 v132, v132, v216\n"
"v_mfma_f32_16x16x32_bf16 v[72:75], v[88:91], v[128:131], v[72:75]\n"
"ds_write_b128 v8, v[160:163] offset:17408\n"
"v_mfma_f32_16x16x32_bf16 v[72:75], v[56:59], v[152:155], v[72:75]\n"
"v_add_f32_e32 v133, v133, v217\n"
"v_add_f32_e32 v134, v134, v218\n"
"v_mfma_f32_16x16x32_bf16 v[72:75], v[48:51], v[80:83], v[72:75]\n"
"v_add_f32_e32 v135, v135, v219\n"
"v_add_f32_e32 v136, v136, v220\n"
"v_add_f32_e32 v137, v137, v221\n"
"v_add_f32_e32 v138, v138, v222\n"
"v_add_f32_e32 v139, v139, v223\n"
"v_mul_f32_e32 v132, v132, v255\n"
"v_mul_f32_e32 v133, v133, v255\n"
"v_mul_f32_e32 v134, v134, v255\n"
"v_mul_f32_e32 v135, v135, v255\n"
"v_mul_f32_e32 v136, v136, v255\n"
"v_mul_f32_e32 v137, v137, v255\n"
"v_mul_f32_e32 v138, v138, v255\n"
"v_mul_f32_e32 v139, v139, v255\n"
"v_cvt_pk_bf16_f32 v24, v132, v133\n"
"v_cvt_pk_bf16_f32 v25, v134, v135\n"
"v_cvt_pk_bf16_f32 v26, v136, v137\n"
"v_cvt_pk_bf16_f32 v27, v138, v139\n"
"ds_write_b128 v7, v[24:27] offset:50176\n"
".Lmk_z3:\n"
"s_waitcnt vmcnt(21)\n"
"; @A G1 3.0\n"
"v_mfma_f32_16x16x32_bf16 v[36:39], a[0:3], v[60:63], 0\n"
"v_mfma_f32_16x16x32_bf16 v[36:39], a[4:7], v[68:71], v[36:39]\n"
"s_add_u32 s97, s62, 0\n"
"v_mfma_f32_16x16x32_bf16 v[36:39], a[8:11], v[196:199], v[36:39]\n"
"s_add_u32 s96, s54, 0\n"
"buffer_load_dwordx4 v[40:43], v32, s[4:7], s96 offen sc1\n"
"v_mfma_f32_16x16x32_bf16 v[36:39], a[12:15], v[64:67], v[36:39]\n"
"s_waitcnt vmcnt(15)\n"
"v_mfma_f32_16x16x32_bf16 v[36:39], a[16:19], v[120:123], v[36:39]\n"
"buffer_load_dword v255, v10, s[4:7], s97 offen sc1\n"
"v_mfma_f32_16x16x32_bf16 v[36:39], a[20:23], v[224:227], v[36:39]\n"
"s_waitcnt lgkmcnt(2)\n"
"v_add_f32_e32 v124, v124, v200\n"
"v_add_f32_e32 v125, v125, v201\n"
"v_mfma_f32_16x16x32_bf16 v[36:39], a[24:27], v[176:179], v[36:39]\n"
"v_add_f32_e32 v126, v126, v202\n"
"v_add_f32_e32 v127, v127, v203\n"
"v_mfma_f32_16x16x32_bf16 v[36:39], a[28:31], v[44:47], v[36:39]\n"
"buffer_load_dwordx4 v[80:83], v32, s[4:7], s96 offen offset:1024 sc1\n"
"v_mfma_f32_16x16x32_bf16 v[128:131], a[60:63], v[44:47], 0\n"
"buffer_load_dwordx4 a[200:203], v0, s[4:7], s98 offen offset:2048 sc1\n"
"v_mfma_f32_16x16x32_bf16 v[128:131], a[56:59], v[176:179], v[128:131]\n"
"buffer_load_dword v253, v10, s[4:7], s97 offen offset:256 sc1\n"
"v_mfma_f32_16x16x32_bf16 v[128:131], a[52:55], v[224:227], v[128:131]\n"
"v_add_f32_e32 v72, v72, v204\n"
"v_add_f32_e32 v73, v73, v205\n"
"v_mfma_f32_16x16x32_bf16 v[128:131], a[48:51], v[120:123], v[128:131]\n"
"v_add_f32_e32 v74, v74, v206\n"
"v_add_f32_e32 v75, v75, v207\n"
"v_mfma_f32_16x16x32_bf16 v[128:131], a[44:47], v[64:67], v[128:131]\n"
"buffer_load_dwordx4 v[132:135], v33, s[4:7], s96 offen sc1\n"
"v_mfma_f32_16x16x32_bf16 v[128:131], a[40:43], v[196:199], v[128:131]\n"
"ds_write_b128 v3, v[36:39]\n"
"v_mfma_f32_16x16x32_bf16 v[128:131], a[36:39], v[68:71], v[128:131]\n"
"buffer_load_dword v252, v10, s[4:7], s97 offen offset:512 sc1\n"
"v_mfma_f32_16x16x32_bf16 v[128:131], a[32:35], v[60:63], v[128:131]\n"
"v_mul_f32_e32 v124, v124, v254\n"
"v_mul_f32_e32 v125, v125, v254\n"
"v_mfma_f32_16x16x32_bf16 v[36:39], a[64:67], v[60:63], 0\n"
"v_mul_f32_e32 v126, v126, v254\n"
"v_mul_f32_e32 v127, v127, v254\n"
"v_mfma_f32_16x16x32_bf16 v[36:39], a[68:71], v[68:71], v[36:39]\n"
"buffer_load_dwordx4 v[136:139], v33, s[4:7], s96 offen offset:1024 sc1\n"
"v_mfma_f32_16x16x32_bf16 v[36:39], a[72:75], v[196:199], v[36:39]\n"
"buffer_load_dword v251, v10, s[4:7], s97 offen offset:768 sc1\n"
"v_mfma_f32_16x16x32_bf16 v[36:39], a[76:79], v[64:67], v[36:39]\n"
"v_mul_f32_e32 v72, v72, v254\n"
"v_mul_f32_e32 v73, v73, v254\n"
"v_mfma_f32_16x16x32_bf16 v[36:39], a[80:83], v[120:123], v[36:39]\n"
"v_mul_f32_e32 v74, v74, v254\n"
"v_mul_f32_e32 v75, v75, v254\n"
"v_mfma_f32_16x16x32_bf16 v[36:39], a[84:87], v[224:227], v[36:39]\n"
"ds_write_b128 v3, v[128:131] offset:1024\n"
"v_mfma_f32_16x16x32_bf16 v[36:39], a[88:91], v[176:179], v[36:39]\n"
"buffer_load_dwordx4 v[128:131], v34, s[4:7], s96 offen sc1\n"
"v_mfma_f32_16x16x32_bf16 v[36:39], a[92:95], v[44:47], v[36:39]\n"
"buffer_load_dword v254, v10, s[4:7], s97 offen offset:1024 sc1\n"
"v_mfma_f32_16x16x32_bf16 v[144:147], a[124:127], v[44:47], 0\n"
"v_cvt_pk_bf16_f32 v152, v124, v125\n"
"v_cvt_pk_bf16_f32 v153, v126, v127\n"
"v_mfma_f32_16x16x32_bf16 v[144:147], a[120:123], v[176:179], v[144:147]\n"
"v_cvt_pk_bf16_f32 v154, v72, v73\n"
"v_cvt_pk_bf16_f32 v155, v74, v75\n"
"v_mfma_f32_16x16x32_bf16 v[144:147], a[116:119], v[224:227], v[144:147]\n"
"buffer_load_dwordx4 a[204:207], v0, s[4:7], s98 offen offset:3072 sc1\n"
"v_mfma_f32_16x16x32_bf16 v[144:147], a[112:115], v[120:123], v[144:147]\n"
"buffer_load_dwordx4 v[72:75], v34, s[4:7], s96 offen offset:1024 sc1\n"
"v_mfma_f32_16x16x32_bf16 v[144:147], a[108:111], v[64:67], v[144:147]\n"
"buffer_load_dword v250, v10, s[4:7], s97 offen offset:1280 sc1\n"
"v_mfma_f32_16x16x32_bf16 v[144:147], a[104:107], v[196:199], v[144:147]\n"
"ds_write_b128 v3, v[36:39] offset:2048\n"
"v_mfma_f32_16x16x32_bf16 v[144:147], a[100:103], v[68:71], v[144:147]\n"
"ds_write_b128 v2, v[152:155] offset:17408\n"
"v_mfma_f32_16x16x32_bf16 v[144:147], a[96:99], v[60:63], v[144:147]\n"
"buffer_load_dword v249, v10, s[4:7], s97 offen offset:1536 sc1\n"
"v_mfma_f32_16x16x32_bf16 v[36:39], v[28:31], v[60:63], 0\n"
"buffer_load_dwordx4 v[124:127], v35, s[4:7], s96 offen sc1\n"
"v_mfma_f32_16x16x32_bf16 v[36:39], v[52:55], v[68:71], v[36:39]\n"
"buffer_store_dwordx4 v[160:163], v8, s[4:7], s64 offen offset:1024 sc1\n"
"v_mfma_f32_16x16x32_bf16 v[36:39], v[84:87], v[196:199], v[36:39]\n"
"buffer_load_dword v248, v10, s[4:7], s97 offen offset:1792 sc1\n"
"v_mfma_f32_16x16x32_bf16 v[36:39], v[92:95], v[64:67], v[36:39]\n"
"v_mfma_f32_16x16x32_bf16 v[36:39], v[100:103], v[120:123], v[36:39]\n"
"v_mfma_f32_16x16x32_bf16 v[36:39], v[108:111], v[224:227], v[36:39]\n"
"buffer_load_dwordx4 v[152:155], v35, s[4:7], s96 offen offset:1024 sc1\n"
"v_mfma_f32_16x16x32_bf16 v[36:39], v[180:183], v[176:179], v[36:39]\n"
"ds_write_b128 v3, v[144:147] offset:3072\n"
"v_mfma_f32_16x16x32_bf16 v[36:39], v[188:191], v[44:47], v[36:39]\n"
"v_mfma_f32_16x16x32_bf16 v[144:147], v[192:195], v[44:47], 0\n"
"v_mfma_f32_16x16x32_bf16 v[144:147], v[184:187], v[176:179], v[144:147]\n"
"v_mfma_f32_16x16x32_bf16 v[144:147], v[112:115], v[224:227], v[144:147]\n"
"v_mfma_f32_16x16x32_bf16 v[144:147], v[104:107], v[120:123], v[144:147]\n"
"v_mfma_f32_16x16x32_bf16 v[144:147], v[96:99], v[64:67], v[144:147]\n"
"buffer_load_dwordx4 a[208:211], v1, s[4:7], s98 offen sc1\n"
"v_mfma_f32_16x16x32_bf16 v[144:147], v[88:91], v[196:199], v[144:147]\n"
"s_nop 0\n"
"ds_write_b128 v3, v[36:39] offset:4096\n"
"v_mfma_f32_16x16x32_bf16 v[144:147], v[56:59], v[68:71], v[144:147]\n"
"v_mfma_f32_16x16x32_bf16 v[144:147], v[48:51], v[60:63], v[144:147]\n"
"s_waitcnt vmcnt(28)\n"
"; @A G1 3.1\n"
"v_mfma_f32_16x16x32_bf16 v[36:39], a[0:3], v[148:151], 0\n"
"v_mov_b32_e32 v44, -1\n"
"v_mov_b32_e32 v45, -1\n"
"v_mfma_f32_16x16x32_bf16 v[36:39], a[4:7], v[156:159], v[36:39]\n"
"v_mov_b32_e32 v46, -1\n"
"v_mov_b32_e32 v47, -1\n"
"v_mfma_f32_16x16x32_bf16 v[36:39], a[8:11], v[172:175], v[36:39]\n"
"s_add_u32 s97, s55, 0\n"
"buffer_load_dwordx4 v[60:63], v32, s[4:7], s97 offen sc1\n"
"v_mfma_f32_16x16x32_bf16 v[36:39], a[12:15], v[208:211], v[36:39]\n"
"s_waitcnt vmcnt(22)\n"
"s_add_u32 s96, s51, 0x4000\n"
"s_bitcmp1_b32 s26, 0\n"
"s_cbranch_scc1 .Lmk_w9s\n"
"buffer_store_dwordx4 a[64:67], v0, s[4:7], s96 offen sc1\n"
".Lmk_w9r:\n"
"v_mfma_f32_16x16x32_bf16 v[36:39], a[16:19], v[212:215], v[36:39]\n"
"v_mfma_f32_16x16x32_bf16 v[36:39], a[20:23], v[76:79], v[36:39]\n"
"ds_write_b128 v3, v[144:147] offset:5120\n"
"v_mfma_f32_16x16x32_bf16 v[36:39], a[24:27], v[116:119], v[36:39]\n"
"v_mfma_f32_16x16x32_bf16 v[36:39], a[28:31], v[140:143], v[36:39]\n"
"buffer_load_dwordx4 v[64:67], v32, s[4:7], s97 offen offset:1024 sc1\n"
"s_add_u32 s96, s64, 0x4000\n"
"v_mfma_f32_16x16x32_bf16 v[68:71], a[60:63], v[140:143], 0\n"
"v_mfma_f32_16x16x32_bf16 v[68:71], a[56:59], v[116:119], v[68:71]\n"
"buffer_store_dwordx4 v[24:27], v7, s[4:7], s96 offen offset:1024 sc1\n"
"v_mfma_f32_16x16x32_bf16 v[68:71], a[52:55], v[76:79], v[68:71]\n"
"v_mfma_f32_16x16x32_bf16 v[68:71], a[48:51], v[212:215], v[68:71]\n"
"v_mfma_f32_16x16x32_bf16 v[68:71], a[44:47], v[208:211], v[68:71]\n"
"buffer_load_dwordx4 v[24:27], v33, s[4:7], s97 offen sc1\n"
"s_add_u32 s96, s51, 0x6000\n"
"v_mfma_f32_16x16x32_bf16 v[68:71], a[40:43], v[172:175], v[68:71]\n"
"ds_write_b128 v3, v[36:39] offset:6144\n"
"v_mfma_f32_16x16x32_bf16 v[68:71], a[36:39], v[156:159], v[68:71]\n"
"buffer_load_dwordx4 a[212:215], v1, s[4:7], s98 offen offset:1024 sc1\n"
"s_bitcmp1_b32 s26, 0\n"
"s_cbranch_scc1 .Lmk_w10s\n"
"buffer_store_dwordx4 a[96:99], v0, s[4:7], s96 offen sc1\n"
".Lmk_w10r:\n"
"v_mfma_f32_16x16x32_bf16 v[68:71], a[32:35], v[148:151], v[68:71]\n"
"v_mfma_f32_16x16x32_bf16 v[36:39], a[64:67], v[148:151], 0\n"
"v_mfma_f32_16x16x32_bf16 v[36:39], a[68:71], v[156:159], v[36:39]\n"
"buffer_load_dwordx4 v[120:123], v33, s[4:7], s97 offen offset:1024 sc1\n"
"v_mfma_f32_16x16x32_bf16 v[36:39], a[72:75], v[172:175], v[36:39]\n"
"v_mfma_f32_16x16x32_bf16 v[36:39], a[76:79], v[208:211], v[36:39]\n"
"v_mfma_f32_16x16x32_bf16 v[36:39], a[80:83], v[212:215], v[36:39]\n"
"v_mfma_f32_16x16x32_bf16 v[36:39], a[84:87], v[76:79], v[36:39]\n"
"s_nop 0\n"
"ds_write_b128 v3, v[68:71] offset:7168\n"
"v_mfma_f32_16x16x32_bf16 v[36:39], a[88:91], v[116:119], v[36:39]\n"
"buffer_load_dwordx4 v[68:71], v34, s[4:7], s97 offen sc1\n"
"v_mfma_f32_16x16x32_bf16 v[36:39], a[92:95], v[140:143], v[36:39]\n"
"v_mfma_f32_16x16x32_bf16 v[144:147], a[124:127], v[140:143], 0\n"
"s_add_u32 s96, s51, 0x8000\n"
"s_bitcmp1_b32 s26, 0\n"
"s_cbranch_scc1 .Lmk_w11s\n"
"buffer_store_dwordx4 v[28:31], v0, s[4:7], s96 offen sc1\n"
".Lmk_w11r:\n"
"v_mfma_f32_16x16x32_bf16 v[144:147], a[120:123], v[116:119], v[144:147]\n"
"v_mfma_f32_16x16x32_bf16 v[144:147], a[116:119], v[76:79], v[144:147]\n"
"v_mfma_f32_16x16x32_bf16 v[144:147], a[112:115], v[212:215], v[144:147]\n"
"buffer_load_dwordx4 v[160:163], v34, s[4:7], s97 offen offset:1024 sc1\n"
"v_mfma_f32_16x16x32_bf16 v[144:147], a[108:111], v[208:211], v[144:147]\n"
"v_mfma_f32_16x16x32_bf16 v[144:147], a[104:107], v[172:175], v[144:147]\n"
"ds_write_b128 v3, v[36:39] offset:8192\n"
"v_mfma_f32_16x16x32_bf16 v[144:147], a[100:103], v[156:159], v[144:147]\n"
"v_mfma_f32_16x16x32_bf16 v[144:147], a[96:99], v[148:151], v[144:147]\n"
"v_mfma_f32_16x16x32_bf16 v[36:39], v[28:31], v[148:151], 0\n"
"buffer_load_dwordx4 v[164:167], v35, s[4:7], s97 offen sc1\n"
"v_mfma_f32_16x16x32_bf16 v[36:39], v[52:55], v[156:159], v[36:39]\n"
"buffer_load_dwordx4 a[216:219], v1, s[4:7], s98 offen offset:2048 sc1\n"
"v_mfma_f32_16x16x32_bf16 v[36:39], v[84:87], v[172:175], v[36:39]\n"
"v_mfma_f32_16x16x32_bf16 v[36:39], v[92:95], v[208:211], v[36:39]\n"
"v_mfma_f32_16x16x32_bf16 v[36:39], v[100:103], v[212:215], v[36:39]\n"
"s_waitcnt vmcnt(15)\n"
"v_add_f32_e32 v255, v255, v254\n"
"v_add_f32_e32 v253, v253, v250\n"
"s_add_u32 s96, s51, 0xa000\n"
"v_mfma_f32_16x16x32_bf16 v[36:39], v[108:111], v[76:79], v[36:39]\n"
"buffer_load_dwordx4 v[168:171], v35, s[4:7], s97 offen offset:1024 sc1\n"
"s_bitcmp1_b32 s26, 0\n"
"s_cbranch_scc1 .Lmk_w12s\n"
"buffer_store_dwordx4 v[48:51], v0, s[4:7], s96 offen sc1\n"
".Lmk_w12r:\n"
"v_mfma_f32_16x16x32_bf16 v[36:39], v[180:183], v[116:119], v[36:39]\n"
"ds_write_b128 v3, v[144:147] offset:9216\n"
"v_mfma_f32_16x16x32_bf16 v[36:39], v[188:191], v[140:143], v[36:39]\n"
"s_add_u32 s97, s75, 0x1000\n"
"buffer_load_dwordx2 v[246:247], v13, s[4:7], s97 offen sc1\n"
"v_mfma_f32_16x16x32_bf16 v[32:35], v[192:195], v[140:143], 0\n"
"v_add_f32_e32 v252, v252, v249\n"
"v_add_f32_e32 v251, v251, v248\n"
"v_mfma_f32_16x16x32_bf16 v[32:35], v[184:187], v[116:119], v[32:35]\n"
"v_add_f32_e32 v255, v255, v252\n"
"v_add_f32_e32 v253, v253, v251\n"
"v_mfma_f32_16x16x32_bf16 v[32:35], v[112:115], v[76:79], v[32:35]\n"
"s_add_u32 s96, s71, 0\n"
"buffer_load_dwordx2 v[250:251], off, s[4:7], s96 sc1\n"
"v_mfma_f32_16x16x32_bf16 v[32:35], v[104:107], v[212:215], v[32:35]\n"
"v_add_f32_e32 v255, v255, v253\n"
"v_mov_b32_e32 v253, v255\n"
"v_mfma_f32_16x16x32_bf16 v[32:35], v[96:99], v[208:211], v[32:35]\n"
"s_nop 0\n"
"v_permlane32_swap_b32_e32 v255, v253\n"
"v_add_f32_e32 v255, v255, v253\n"
"v_mfma_f32_16x16x32_bf16 v[32:35], v[88:91], v[172:175], v[32:35]\n"
"ds_write_b128 v3, v[36:39] offset:10240\n"
"v_mfma_f32_16x16x32_bf16 v[32:35], v[56:59], v[156:159], v[32:35]\n"
"v_mul_f32_e32 v255, 0x3a800000, v255\n"
"v_mov_b32_e32 v253, v255\n"
"v_mfma_f32_16x16x32_bf16 v[32:35], v[48:51], v[148:151], v[32:35]\n"
"s_nop 0\n"
"v_permlane16_swap_b32_e32 v255, v253\n"
"v_cvt_pk_bf16_f32 v254, -v255, 0\n"
"; @A G1 3.2\n"
"v_mfma_f32_16x16x32_bf16 v[36:39], a[0:3], v[40:43], 0\n"
"s_add_u32 s95, s61, 0x440000\n"
"buffer_store_dwordx4 v[44:47], v0, s[4:7], s95 offen sc1\n"
"v_fma_f32 v253, -v255, v255, v253\n"
"v_lshlrev_b32_e32 v254, 16, v254\n"
"v_mfma_f32_16x16x32_bf16 v[36:39], a[4:7], v[80:83], v[36:39]\n"
"v_max_f32_e32 v253, 0, v253\n"
"v_cvt_pk_bf16_f32 v253, v253, 0\n"
"v_mfma_f32_16x16x32_bf16 v[36:39], a[8:11], v[132:135], v[36:39]\n"
"buffer_load_dwordx4 a[220:223], v1, s[4:7], s98 offen offset:3072 sc1\n"
"v_mfma_f32_16x16x32_bf16 v[36:39], a[12:15], v[136:139], v[36:39]\n"
"s_waitcnt vmcnt(20)\n"
"v_lshlrev_b32_e32 v253, 16, v253\n"
"v_add_f32_e32 v253, 0x3727c5ac, v253\n"
"v_mfma_f32_16x16x32_bf16 v[36:39], a[16:19], v[128:131], v[36:39]\n"
"s_add_u32 s98, s70, 0x6000\n"
"v_cvt_pk_bf16_f32 v253, v253, 0\n"
"v_lshlrev_b32_e32 v253, 16, v253\n"
"v_mfma_f32_16x16x32_bf16 v[36:39], a[20:23], v[72:75], v[36:39]\n"
"ds_write_b128 v3, v[32:35] offset:11264\n"
"v_mfma_f32_16x16x32_bf16 v[36:39], a[24:27], v[124:127], v[36:39]\n"
"buffer_load_dwordx4 a[224:227], v0, s[4:7], s98 offen sc1\n"
"v_mfma_f32_16x16x32_bf16 v[36:39], a[28:31], v[152:155], v[36:39]\n"
"v_sqrt_f32_e32 v253, v253\n"
"v_mfma_f32_16x16x32_bf16 v[32:35], a[60:63], v[152:155], 0\n"
"v_cvt_pk_bf16_f32 v253, v253, 0\n"
"v_lshlrev_b32_e32 v253, 16, v253\n"
"v_mfma_f32_16x16x32_bf16 v[32:35], a[56:59], v[124:127], v[32:35]\n"
"v_rcp_f32_e32 v255, v253\n"
"v_mfma_f32_16x16x32_bf16 v[32:35], a[52:55], v[72:75], v[32:35]\n"
"s_waitcnt lgkmcnt(0)\n"
"s_barrier\n"
"v_mfma_f32_16x16x32_bf16 v[32:35], a[48:51], v[128:131], v[32:35]\n"
"buffer_load_dwordx4 a[228:231], v0, s[4:7], s98 offen offset:1024 sc1\n"
"v_mfma_f32_16x16x32_bf16 v[32:35], a[44:47], v[136:139], v[32:35]\n"
"ds_read_b128 v[76:79], v4\n"
"s_add_u32 s95, s61, 0x440000\n"
"buffer_store_dwordx4 v[44:47], v0, s[4:7], s95 offen offset:1024 sc1\n"
"v_mul_f32_e32 v253, 0x3e8293ee, v255\n"
"v_mfma_f32_16x16x32_bf16 v[32:35], a[40:43], v[132:135], v[32:35]\n"
"ds_write_b128 v3, v[36:39] offset:12288\n"
"v_mfma_f32_16x16x32_bf16 v[32:35], a[36:39], v[80:83], v[32:35]\n"
"ds_read_b128 v[36:39], v9 offset:128\n"
"ds_read_b128 v[116:119], v5\n"
"v_mfma_f32_16x16x32_bf16 v[32:35], a[32:35], v[40:43], v[32:35]\n"
"buffer_load_dwordx4 a[232:235], v0, s[4:7], s98 offen offset:2048 sc1\n"
"v_mfma_f32_16x16x32_bf16 v[140:143], a[64:67], v[40:43], 0\n"
"v_mfma_f32_16x16x32_bf16 v[140:143], a[68:71], v[80:83], v[140:143]\n"
"v_mfma_f32_16x16x32_bf16 v[140:143], a[72:75], v[132:135], v[140:143]\n"
"ds_read_b128 v[144:147], v4 offset:1024\n"
"ds_read_b128 v[148:151], v9 offset:192\n"
"v_mfma_f32_16x16x32_bf16 v[140:143], a[76:79], v[136:139], v[140:143]\n"
"buffer_load_dwordx4 a[236:239], v0, s[4:7], s98 offen offset:3072 sc1\n"
"v_mfma_f32_16x16x32_bf16 v[140:143], a[80:83], v[128:131], v[140:143]\n"
"ds_read_b128 v[156:159], v5 offset:1024\n"
"s_waitcnt lgkmcnt(3)\n"
"v_mfma_f32_16x16x32_bf16 v[140:143], a[84:87], v[72:75], v[140:143]\n"
"ds_write_b128 v3, v[32:35] offset:13312\n"
"v_mfma_f32_16x16x32_bf16 v[140:143], a[88:91], v[124:127], v[140:143]\n"
"v_fmac_f32_e32 v76, v254, v36\n"
"v_fmac_f32_e32 v77, v254, v37\n"
"v_mfma_f32_16x16x32_bf16 v[140:143], a[92:95], v[152:155], v[140:143]\n"
"buffer_load_dwordx4 a[240:243], v1, s[4:7], s98 offen sc1\n"
"v_mfma_f32_16x16x32_bf16 v[32:35], a[124:127], v[152:155], 0\n"
"s_add_u32 s95, s63, 0x44000\n"
"buffer_store_dword v44, v14, s[4:7], s95 offen sc1\n"
"v_fmac_f32_e32 v78, v254, v38\n"
"v_fmac_f32_e32 v79, v254, v39\n"
"v_mfma_f32_16x16x32_bf16 v[32:35], a[120:123], v[124:127], v[32:35]\n"
"ds_read_b128 v[36:39], v4 offset:2048\n"
"ds_read_b128 v[44:47], v9 offset:256\n"
"v_mfma_f32_16x16x32_bf16 v[32:35], a[116:119], v[72:75], v[32:35]\n"
"ds_read_b128 v[172:175], v5 offset:2048\n"
"v_add_f32_e32 v76, v76, v116\n"
"v_mfma_f32_16x16x32_bf16 v[32:35], a[112:115], v[128:131], v[32:35]\n"
"buffer_load_dwordx4 a[244:247], v1, s[4:7], s98 offen offset:1024 sc1\n"
"v_mfma_f32_16x16x32_bf16 v[32:35], a[108:111], v[136:139], v[32:35]\n"
"s_mov_b32 s20, 0xff0000\n"
"v_add_f32_e32 v77, v77, v117\n"
"v_add_f32_e32 v78, v78, v118\n"
"v_mfma_f32_16x16x32_bf16 v[32:35], a[104:107], v[132:135], v[32:35]\n"
"ds_write_b128 v3, v[140:143] offset:14336\n"
"v_mfma_f32_16x16x32_bf16 v[32:35], a[100:103], v[80:83], v[32:35]\n"
"s_waitcnt lgkmcnt(5)\n"
"v_add_f32_e32 v79, v79, v119\n"
"v_fmac_f32_e32 v144, v254, v148\n"
"v_mfma_f32_16x16x32_bf16 v[32:35], a[96:99], v[40:43], v[32:35]\n"
"buffer_load_dwordx4 a[248:251], v1, s[4:7], s98 offen offset:2048 sc1\n"
"v_mfma_f32_16x16x32_bf16 v[116:119], v[28:31], v[40:43], 0\n"
"ds_read_b128 v[140:143], v4 offset:3072\n"
"ds_read_b128 v[176:179], v9 offset:320\n"
"v_mfma_f32_16x16x32_bf16 v[116:119], v[52:55], v[80:83], v[116:119]\n"
"ds_read_b128 v[196:199], v5 offset:3072\n"
"v_fmac_f32_e32 v145, v254, v149\n"
"v_mfma_f32_16x16x32_bf16 v[116:119], v[84:87], v[132:135], v[116:119]\n"
"s_mov_b32 s21, 0xffffffff\n"
"v_fmac_f32_e32 v146, v254, v150\n"
"v_fmac_f32_e32 v147, v254, v151\n"
"v_mfma_f32_16x16x32_bf16 v[116:119], v[92:95], v[136:139], v[116:119]\n"
"buffer_load_dwordx4 a[252:255], v1, s[4:7], s98 offen offset:3072 sc1\n"
"v_mfma_f32_16x16x32_bf16 v[116:119], v[100:103], v[128:131], v[116:119]\n"
"s_waitcnt vmcnt(13)\n"
"v_cmp_ne_u64_e32 vcc, v[246:247], v[18:19]\n"
"v_mfma_f32_16x16x32_bf16 v[116:119], v[108:111], v[72:75], v[116:119]\n"
"ds_write_b128 v3, v[32:35] offset:15360\n"
"v_mfma_f32_16x16x32_bf16 v[116:119], v[180:183], v[124:127], v[116:119]\n"
"s_cbranch_vccnz .Lmk_f6s\n"
".Lmk_f6r:\n"
"v_add_f32_e32 v144, v144, v156\n"
"v_mfma_f32_16x16x32_bf16 v[116:119], v[188:191], v[152:155], v[116:119]\n"
"ds_read_b128 v[32:35], v4 offset:4096\n"
"ds_read_b128 v[148:151], v9\n"
"v_mfma_f32_16x16x32_bf16 v[200:203], v[192:195], v[152:155], 0\n"
"ds_read_b128 v[152:155], v5 offset:4096\n"
"v_add_f32_e32 v145, v145, v157\n"
"v_mfma_f32_16x16x32_bf16 v[200:203], v[184:187], v[124:127], v[200:203]\n"
"s_waitcnt lgkmcnt(8)\n"
"v_add_f32_e32 v146, v146, v158\n"
"v_add_f32_e32 v147, v147, v159\n"
"v_mfma_f32_16x16x32_bf16 v[200:203], v[112:115], v[72:75], v[200:203]\n"
"s_mov_b32 s22, 0x1ff0001\n"
"v_fmac_f32_e32 v36, v254, v44\n"
"v_fmac_f32_e32 v37, v254, v45\n"
"v_mfma_f32_16x16x32_bf16 v[200:203], v[104:107], v[128:131], v[200:203]\n"
"s_mov_b32 s23, 0xffffffff\n"
"v_fmac_f32_e32 v38, v254, v46\n"
"v_fmac_f32_e32 v39, v254, v47\n"
"v_mfma_f32_16x16x32_bf16 v[200:203], v[96:99], v[136:139], v[200:203]\n"
"s_waitcnt vmcnt(12)\n"
"v_cmp_ne_u64_e32 vcc, v[250:251], v[18:19]\n"
"v_mfma_f32_16x16x32_bf16 v[200:203], v[88:91], v[132:135], v[200:203]\n"
"ds_write_b128 v3, v[116:119] offset:16384\n"
"v_mfma_f32_16x16x32_bf16 v[200:203], v[56:59], v[80:83], v[200:203]\n"
"s_cbranch_vccnz .Lmk_f3s\n"
".Lmk_f3r:\n"
"s_add_u32 m0, s77, 0x6000\n"
"v_mfma_f32_16x16x32_bf16 v[200:203], v[48:51], v[40:43], v[200:203]\n"
"s_add_u32 s98, s65, 0x2000\n"
"ds_read_b128 v[40:43], v4 offset:5120\n"
"; @A G1 3.3\n"
"v_mfma_f32_16x16x32_bf16 v[76:79], a[0:3], v[60:63], v[76:79]\n"
"buffer_load_dwordx4 v2, s[4:7], s98 offen sc1 lds\n"
"v_mfma_f32_16x16x32_bf16 v[76:79], a[4:7], v[64:67], v[76:79]\n"
"buffer_load_dwordx4 v2, s[4:7], s98 offen offset:1024 sc1 lds\n"
"v_mfma_f32_16x16x32_bf16 v[76:79], a[8:11], v[24:27], v[76:79]\n"
"ds_read_b128 v[44:47], v9 offset:64\n"
"ds_read_b128 v[72:75], v5 offset:5120\n"
"v_mfma_f32_16x16x32_bf16 v[76:79], a[12:15], v[120:123], v[76:79]\n"
"v_add_f32_e32 v36, v36, v172\n"
"v_add_f32_e32 v37, v37, v173\n"
"v_mfma_f32_16x16x32_bf16 v[76:79], a[16:19], v[68:71], v[76:79]\n"
"s_waitcnt lgkmcnt(8)\n"
"v_add_f32_e32 v38, v38, v174\n"
"v_add_f32_e32 v39, v39, v175\n"
"v_mfma_f32_16x16x32_bf16 v[76:79], a[20:23], v[160:163], v[76:79]\n"
"ds_write_b128 v3, v[200:203] offset:17408\n"
"v_mfma_f32_16x16x32_bf16 v[76:79], a[24:27], v[164:167], v[76:79]\n"
"s_mov_b32 s24, 0x3ff0003\n"
"v_fmac_f32_e32 v140, v254, v176\n"
"v_fmac_f32_e32 v141, v254, v177\n"
"v_mfma_f32_16x16x32_bf16 v[76:79], a[28:31], v[168:171], v[76:79]\n"
"s_add_u32 m0, s77, 0xe000\n"
"s_add_u32 s98, s65, 0x6000\n"
"v_fmac_f32_e32 v142, v254, v178\n"
"v_mfma_f32_16x16x32_bf16 v[144:147], a[60:63], v[168:171], v[144:147]\n"
"buffer_load_dwordx4 v2, s[4:7], s98 offen sc1 lds\n"
"v_mfma_f32_16x16x32_bf16 v[144:147], a[56:59], v[164:167], v[144:147]\n"
"buffer_load_dwordx4 v2, s[4:7], s98 offen offset:1024 sc1 lds\n"
"v_mfma_f32_16x16x32_bf16 v[144:147], a[52:55], v[160:163], v[144:147]\n"
"s_waitcnt lgkmcnt(0)\n"
"s_barrier\n"
"v_mfma_f32_16x16x32_bf16 v[144:147], a[48:51], v[68:71], v[144:147]\n"
"ds_read_b128 v[80:83], v6\n"
"ds_read_b128 v[116:119], v6 offset:1024\n"
"v_mfma_f32_16x16x32_bf16 v[144:147], a[44:47], v[120:123], v[144:147]\n"
"s_add_u32 s98, s69, 0x600000\n"
"buffer_load_dwordx4 a[0:3], v0, s[4:7], s98 offen sc1\n"
"v_mfma_f32_16x16x32_bf16 v[144:147], a[40:43], v[24:27], v[144:147]\n"
"s_mov_b32 s25, 0xffffffff\n"
"v_fmac_f32_e32 v143, v254, v179\n"
"v_add_f32_e32 v140, v140, v196\n"
"v_mfma_f32_16x16x32_bf16 v[144:147], a[36:39], v[64:67], v[144:147]\n"
"s_mov_b32 s26, 0x7ff0007\n"
"v_add_f32_e32 v141, v141, v197\n"
"v_add_f32_e32 v142, v142, v198\n"
"v_mfma_f32_16x16x32_bf16 v[144:147], a[32:35], v[60:63], v[144:147]\n"
"buffer_load_dwordx4 a[4:7], v0, s[4:7], s98 offen offset:1024 sc1\n"
"v_mfma_f32_16x16x32_bf16 v[36:39], a[64:67], v[60:63], v[36:39]\n"
"v_add_f32_e32 v143, v143, v199\n"
"v_fmac_f32_e32 v32, v254, v148\n"
"v_mfma_f32_16x16x32_bf16 v[36:39], a[68:71], v[64:67], v[36:39]\n"
"s_mov_b32 s27, 0xffffffff\n"
"v_fmac_f32_e32 v33, v254, v149\n"
"v_fmac_f32_e32 v34, v254, v150\n"
"v_mfma_f32_16x16x32_bf16 v[36:39], a[72:75], v[24:27], v[36:39]\n"
"buffer_load_dwordx4 a[8:11], v0, s[4:7], s98 offen offset:2048 sc1\n"
"v_mfma_f32_16x16x32_bf16 v[36:39], a[76:79], v[120:123], v[36:39]\n"
"s_mov_b32 s28, 0xfff000f\n"
"v_fmac_f32_e32 v35, v254, v151\n"
"v_add_f32_e32 v32, v32, v152\n"
"v_mfma_f32_16x16x32_bf16 v[36:39], a[80:83], v[68:71], v[36:39]\n"
"s_waitcnt lgkmcnt(0)\n"
"v_add_f32_e32 v33, v33, v153\n"
"v_add_f32_e32 v34, v34, v154\n"
"v_mfma_f32_16x16x32_bf16 v[36:39], a[84:87], v[160:163], v[36:39]\n"
"buffer_load_dwordx4 a[12:15], v0, s[4:7], s98 offen offset:3072 sc1\n"
"v_mfma_f32_16x16x32_bf16 v[36:39], a[88:91], v[164:167], v[36:39]\n"
"ds_read_b128 v[124:127], v6 offset:2048\n"
"ds_read_b128 v[128:131], v6 offset:3072\n"
"v_mfma_f32_16x16x32_bf16 v[36:39], a[92:95], v[168:171], v[36:39]\n"
"v_add_f32_e32 v35, v35, v155\n"
"v_fmac_f32_e32 v40, v254, v44\n"
"v_mfma_f32_16x16x32_bf16 v[140:143], a[124:127], v[168:171], v[140:143]\n"
"buffer_load_dwordx4 a[16:19], v1, s[4:7], s98 offen sc1\n"
"v_mfma_f32_16x16x32_bf16 v[140:143], a[120:123], v[164:167], v[140:143]\n"
"s_mov_b32 s29, 0xffffffff\n"
"v_fmac_f32_e32 v41, v254, v45\n"
"v_fmac_f32_e32 v42, v254, v46\n"
"v_mfma_f32_16x16x32_bf16 v[140:143], a[116:119], v[160:163], v[140:143]\n"
"s_mov_b32 s30, 0x1fff001f\n"
"v_fmac_f32_e32 v43, v254, v47\n"
"v_add_f32_e32 v40, v40, v72\n"
"v_mfma_f32_16x16x32_bf16 v[140:143], a[112:115], v[68:71], v[140:143]\n"
"buffer_load_dwordx4 a[20:23], v1, s[4:7], s98 offen offset:1024 sc1\n"
"v_mfma_f32_16x16x32_bf16 v[140:143], a[108:111], v[120:123], v[140:143]\n"
"s_mov_b32 s31, 0xffffffff\n"
"v_add_f32_e32 v41, v41, v73\n"
"v_add_f32_e32 v42, v42, v74\n"
"v_mfma_f32_16x16x32_bf16 v[140:143], a[104:107], v[24:27], v[140:143]\n"
"s_mov_b32 s32, 0x3fff003f\n"
"v_add_f32_e32 v43, v43, v75\n"
"v_add_f32_e32 v76, v76, v80\n"
"v_mfma_f32_16x16x32_bf16 v[140:143], a[100:103], v[64:67], v[140:143]\n"
"buffer_load_dwordx4 a[24:27], v1, s[4:7], s98 offen offset:2048 sc1\n"
"v_mfma_f32_16x16x32_bf16 v[140:143], a[96:99], v[60:63], v[140:143]\n"
"s_mov_b32 s33, 0xffffffff\n"
"v_add_f32_e32 v77, v77, v81\n"
"v_add_f32_e32 v78, v78, v82\n"
"v_mfma_f32_16x16x32_bf16 v[32:35], v[28:31], v[60:63], v[32:35]\n"
"s_mov_b32 s34, 0x7fff007f\n"
"v_add_f32_e32 v79, v79, v83\n"
"v_add_f32_e32 v144, v144, v116\n"
"v_mfma_f32_16x16x32_bf16 v[32:35], v[52:55], v[64:67], v[32:35]\n"
"buffer_load_dwordx4 a[28:31], v1, s[4:7], s98 offen offset:3072 sc1\n"
"v_mfma_f32_16x16x32_bf16 v[32:35], v[84:87], v[24:27], v[32:35]\n"
"s_mov_b32 s35, 0xffffffff\n"
"v_add_f32_e32 v145, v145, v117\n"
"v_add_f32_e32 v146, v146, v118\n"
"v_mfma_f32_16x16x32_bf16 v[32:35], v[92:95], v[120:123], v[32:35]\n"
"s_mov_b32 s36, 0\n"
"v_add_f32_e32 v147, v147, v119\n"
"v_mul_f32_e32 v76, v76, v255\n"
"v_mfma_f32_16x16x32_bf16 v[32:35], v[100:103], v[68:71], v[32:35]\n"
"s_waitcnt lgkmcnt(0)\n"
"s_waitcnt vmcnt(15)\n"
"buffer_store_dwordx2 v[18:19], v11, s[4:7], s68 offen offset:32 sc1\n"
"v_mul_f32_e32 v77, v77, v255\n"
"v_mul_f32_e32 v78, v78, v255\n"
"v_mfma_f32_16x16x32_bf16 v[32:35], v[108:111], v[160:163], v[32:35]\n"
"s_mov_b32 s37, 0xff0000\n"
"v_mul_f32_e32 v79, v79, v255\n"
"v_mul_f32_e32 v144, v144, v255\n"
"v_mfma_f32_16x16x32_bf16 v[32:35], v[180:183], v[164:167], v[32:35]\n"
"ds_read_b128 v[28:31], v6 offset:4096\n"
"ds_read_b128 v[44:47], v6 offset:5120\n"
"v_mfma_f32_16x16x32_bf16 v[32:35], v[188:191], v[168:171], v[32:35]\n"
"s_mov_b32 s38, 0\n"
"v_mul_f32_e32 v145, v145, v255\n"
"v_mul_f32_e32 v146, v146, v255\n"
"v_mfma_f32_16x16x32_bf16 v[40:43], v[192:195], v[168:171], v[40:43]\n"
"s_mov_b32 s39, 0x1ff0001\n"
"v_mul_f32_e32 v147, v147, v255\n"
"v_cvt_pk_bf16_f32 v52, v76, v77\n"
"v_mfma_f32_16x16x32_bf16 v[40:43], v[184:187], v[164:167], v[40:43]\n"
"s_mov_b32 s40, 0\n"
"v_cvt_pk_bf16_f32 v53, v78, v79\n"
"v_cvt_pk_bf16_f32 v54, v144, v145\n"
"v_mfma_f32_16x16x32_bf16 v[40:43], v[112:115], v[160:163], v[40:43]\n"
"s_mov_b32 s41, 0x3ff0003\n"
"v_cvt_pk_bf16_f32 v55, v146, v147\n"
"v_add_f32_e32 v36, v36, v124\n"
"v_mfma_f32_16x16x32_bf16 v[40:43], v[104:107], v[68:71], v[40:43]\n"
"ds_write_b128 v7, v[52:55] offset:16384\n"
"v_mfma_f32_16x16x32_bf16 v[40:43], v[96:99], v[120:123], v[40:43]\n"
"buffer_store_dwordx4 v[52:55], v7, s[4:7], s64 offen sc1\n"
"v_mfma_f32_16x16x32_bf16 v[40:43], v[88:91], v[24:27], v[40:43]\n"
"s_mov_b32 s42, 0\n"
"v_add_f32_e32 v37, v37, v125\n"
"v_add_f32_e32 v38, v38, v126\n"
"v_mfma_f32_16x16x32_bf16 v[40:43], v[56:59], v[64:67], v[40:43]\n"
"s_mov_b32 s43, 0x7ff0007\n"
"v_add_f32_e32 v39, v39, v127\n"
"v_add_f32_e32 v140, v140, v128\n"
"v_mfma_f32_16x16x32_bf16 v[40:43], v[48:51], v[60:63], v[40:43]\n"
"s_mov_b32 s44, 0\n"
"v_add_f32_e32 v141, v141, v129\n"
"v_add_f32_e32 v142, v142, v130\n"
"v_add_f32_e32 v143, v143, v131\n"
"v_mul_f32_e32 v36, v36, v255\n"
"v_mul_f32_e32 v37, v37, v255\n"
"v_mul_f32_e32 v38, v38, v255\n"
"v_mul_f32_e32 v39, v39, v255\n"
"v_mul_f32_e32 v140, v140, v255\n"
"v_mul_f32_e32 v141, v141, v255\n"
"v_mul_f32_e32 v142, v142, v255\n"
"v_mul_f32_e32 v143, v143, v255\n"
"v_cvt_pk_bf16_f32 v24, v36, v37\n"
"v_cvt_pk_bf16_f32 v25, v38, v39\n"
"v_cvt_pk_bf16_f32 v26, v140, v141\n"
"v_cvt_pk_bf16_f32 v27, v142, v143\n"
"s_mov_b32 s45, 0xfff000f\n"
"s_mov_b32 s46, 0\n"
"s_mov_b32 s47, 0x1fff001f\n"
"s_mov_b32 s48, 0\n"
"s_mov_b32 s49, 0x3fff003f\n"
"s_mov_b32 s50, 0\n"
"s_mov_b32 s51, 0x7fff007f\n"
"ds_write_b128 v7, v[24:27] offset:49152\n"
"v_xor_b32_e32 v255, 32, v16\n"
"v_xor_b32_e32 v254, 32, v15\n"
"v_mov_b32_e32 v88, 0x3f803f80\n"
"v_mov_b32_e32 v89, 0x3f803f80\n"
"v_mov_b32_e32 v90, 0x3f803f80\n"
"v_mov_b32_e32 v91, 0x3f803f80\n"
"v_mov_b32_e32 v252, 0x53800000\n"
".Lmk_z4:\n"
"; @A tail\n"
"s_add_u32 s98, s64, 0x4000\n"
"buffer_store_dwordx4 v[24:27], v7, s[4:7], s98 offen sc1\n"
"s_waitcnt lgkmcnt(2)\n"
"v_add_f32_e32 v32, v32, v28\n"
"v_add_f32_e32 v33, v33, v29\n"
"v_add_f32_e32 v34, v34, v30\n"
"v_add_f32_e32 v35, v35, v31\n"
"v_add_f32_e32 v40, v40, v44\n"
"v_add_f32_e32 v41, v41, v45\n"
"v_add_f32_e32 v42, v42, v46\n"
"v_add_f32_e32 v43, v43, v47\n"
"v_mul_f32_e32 v32, v32, v253\n"
"v_mul_f32_e32 v33, v33, v253\n"
"v_mul_f32_e32 v34, v34, v253\n"
"v_mul_f32_e32 v35, v35, v253\n"
"v_mul_f32_e32 v40, v40, v253\n"
"v_mul_f32_e32 v41, v41, v253\n"
"v_mul_f32_e32 v42, v42, v253\n"
"v_mul_f32_e32 v43, v43, v253\n"
"v_cvt_pk_bf16_f32 v24, v32, v33\n"
"v_cvt_pk_bf16_f32 v25, v34, v35\n"
"v_cvt_pk_bf16_f32 v26, v40, v41\n"
"v_cvt_pk_bf16_f32 v27, v42, v43\n"
"ds_write_b128 v2, v[24:27] offset:16384\n"
"s_waitcnt lgkmcnt(0)\n"
"s_barrier\n"
"ds_read_b128 v[28:31], v2 offset:49152\n"
"ds_read_b128 v[32:35], v2 offset:50176\n"
"ds_read_b128 v[92:95], v2 offset:16384\n"
"ds_read_b128 v[96:99], v2 offset:17408\n"
"; @A AT b\n"
"s_mov_b32 s2, 0\n"
"v_mov_b32_e32 v253, 0xff800000\n"
"v_mov_b32_e32 v251, 0xff800000\n"
"v_mov_b32_e32 v250, 0x7f800000\n"
"v_mov_b32_e32 v249, 0x7f800000\n"
".Lmk_a20:\n"
"ds_read_b128 v[24:27], v15 offset:24576\n"
"ds_read_b128 v[36:39], v254 offset:24832\n"
"ds_read_b128 v[40:43], v15 offset:22528\n"
"ds_read_b128 v[44:47], v254 offset:22784\n"
"s_cmp_gt_i32 s78, 11\n"
"s_cselect_b32 s98, 0x80000000, 0\n"
"s_or_b32 s98, s98, 0x7f800000\n"
"s_waitcnt lgkmcnt(2)\n"
"v_mfma_f32_16x16x32_bf16 v[48:51], v[24:27], v[28:31], 0\n"
"v_mfma_f32_16x16x32_bf16 v[52:55], v[24:27], v[32:35], 0\n"
"v_mfma_f32_16x16x32_bf16 v[56:59], v[36:39], v[32:35], 0\n"
"v_mfma_f32_16x16x32_bf16 v[60:63], v[36:39], v[28:31], 0\n"
"s_waitcnt lgkmcnt(0)\n"
"s_nop 3\n"
"v_max_f32_e32 v248, v48, v49\n"
"v_max_f32_e32 v247, v52, v53\n"
"v_max3_f32 v248, v248, v50, v51\n"
"v_max3_f32 v247, v247, v54, v55\n"
"v_max3_f32 v248, v248, v60, v61\n"
"v_max3_f32 v247, v247, v56, v57\n"
"v_max3_f32 v248, v248, v62, v63\n"
"v_max3_f32 v247, v247, v58, v59\n"
"v_mov_b32_e32 v246, v248\n"
"v_mov_b32_e32 v245, v247\n"
"s_nop 0\n"
"v_permlane16_swap_b32_e32 v248, v246\n"
"v_permlane16_swap_b32_e32 v247, v245\n"
"v_max_f32_e32 v248, v248, v246\n"
"v_max_f32_e32 v247, v247, v245\n"
"v_mov_b32_e32 v246, v248\n"
"v_mov_b32_e32 v245, v247\n"
"s_nop 0\n"
"v_permlane32_swap_b32_e32 v248, v246\n"
"v_permlane32_swap_b32_e32 v247, v245\n"
"v_max_f32_e32 v248, v248, v246\n"
"v_max_f32_e32 v247, v247, v245\n"
"v_med3_f32 v248, v248, v253, v250\n"
"v_med3_f32 v247, v247, v251, v249\n"
"v_min_f32_e64 v24, s98, -v248\n"
"v_min_f32_e64 v25, s98, -v248\n"
"v_min_f32_e64 v26, s98, -v248\n"
"v_min_f32_e64 v27, s98, -v248\n"
"v_min_f32_e64 v36, s98, -v247\n"
"v_min_f32_e64 v37, s98, -v247\n"
"v_min_f32_e64 v38, s98, -v247\n"
"v_min_f32_e64 v39, s98, -v247\n"
"v_mfma_f32_16x16x32_bf16 v[64:67], v[40:43], v[28:31], v[24:27]\n"
"ds_read_b128 v[68:71], v15 offset:20480\n"
"s_add_u32 s98, s69, 0x602000\n"
"buffer_load_dwordx4 a[32:35], v0, s[4:7], s98 offen sc1\n"
"v_sub_f32_e32 v48, v48, v248\n"
"v_sub_f32_e32 v49, v49, v248\n"
"v_sub_f32_e32 v50, v50, v248\n"
"v_mfma_f32_16x16x32_bf16 v[72:75], v[40:43], v[32:35], v[36:39]\n"
"buffer_load_dwordx4 a[36:39], v0, s[4:7], s98 offen offset:1024 sc1\n"
"v_sub_f32_e32 v51, v51, v248\n"
"v_sub_f32_e32 v60, v60, v248\n"
"v_sub_f32_e32 v61, v61, v248\n"
"v_sub_f32_e32 v62, v62, v248\n"
"v_sub_f32_e32 v63, v63, v248\n"
"v_mfma_f32_16x16x32_bf16 v[40:43], v[44:47], v[32:35], v[36:39]\n"
"ds_read_b128 v[76:79], v254 offset:20736\n"
"v_sub_f32_e32 v52, v52, v247\n"
"v_sub_f32_e32 v53, v53, v247\n"
"v_sub_f32_e32 v54, v54, v247\n"
"v_sub_f32_e32 v55, v55, v247\n"
"v_mfma_f32_16x16x32_bf16 v[80:83], v[44:47], v[28:31], v[24:27]\n"
"buffer_load_dwordx4 a[40:43], v0, s[4:7], s98 offen offset:2048 sc1\n"
"v_sub_f32_e32 v56, v56, v247\n"
"v_sub_f32_e32 v57, v57, v247\n"
"v_sub_f32_e32 v58, v58, v247\n"
"v_sub_f32_e32 v59, v59, v247\n"
"s_waitcnt lgkmcnt(0)\n"
"v_mfma_f32_16x16x32_bf16 v[44:47], v[68:71], v[28:31], v[24:27]\n"
"ds_read_b128 v[84:87], v15 offset:18432\n"
"buffer_load_dwordx4 a[44:47], v0, s[4:7], s98 offen offset:3072 sc1\n"
"ds_read_b128 v[100:103], v254 offset:18688\n"
"ds_read_b64_tr_b16 v[104:105], v16 offset:22528\n"
"v_exp_f32_e32 v64, v64\n"
"v_exp_f32_e32 v65, v65\n"
"v_exp_f32_e32 v66, v66\n"
"v_exp_f32_e32 v67, v67\n"
"v_exp_f32_e32 v80, v80\n"
"v_exp_f32_e32 v81, v81\n"
"v_mfma_f32_16x16x32_bf16 v[108:111], v[68:71], v[32:35], v[36:39]\n"
"buffer_load_dwordx4 a[48:51], v1, s[4:7], s98 offen sc1\n"
"ds_read_b64_tr_b16 v[106:107], v255 offset:22784\n"
"buffer_load_dwordx4 a[52:55], v1, s[4:7], s98 offen offset:1024 sc1\n"
"v_exp_f32_e32 v82, v82\n"
"v_exp_f32_e32 v83, v83\n"
"v_cvt_pk_bf16_f32 v68, v64, v65\n"
"v_cvt_pk_bf16_f32 v69, v66, v67\n"
"v_cvt_pk_bf16_f32 v70, v80, v81\n"
"v_cvt_pk_bf16_f32 v71, v82, v83\n"
"v_exp_f32_e32 v72, v72\n"
"v_exp_f32_e32 v73, v73\n"
"v_mfma_f32_16x16x32_bf16 v[64:67], v[76:79], v[32:35], v[36:39]\n"
"ds_read_b64_tr_b16 v[80:81], v255 offset:22528\n"
"s_add_u32 s97, s76, 0x80000\n"
"v_exp_f32_e32 v74, v74\n"
"v_exp_f32_e32 v75, v75\n"
"v_exp_f32_e32 v40, v40\n"
"v_bfe_u32 v248, v0, 2, 7\n"
"v_add_u32_e32 v248, s72, v248\n"
"v_and_b32_e32 v247, 0xffffffc0, v248\n"
"v_add_u32_e32 v248, v248, v247\n"
"v_and_b32_e32 v247, 0x200, v0\n"
"v_lshl_add_u32 v248, v247, 4, v248\n"
"v_mfma_f32_16x16x32_bf16 v[112:115], v[76:79], v[28:31], v[24:27]\n"
"buffer_load_dword v247, v248, s[4:7], s97 offen sc1\n"
"ds_read_b64_tr_b16 v[82:83], v16 offset:22784\n"
"s_add_u32 s96, s68, 0x10000\n"
"buffer_store_dwordx2 v[18:19], v11, s[4:7], s96 offen offset:96 sc1\n"
"buffer_load_dword v246, v248, s[4:7], s97 offen offset:1024 sc1\n"
"v_exp_f32_e32 v41, v41\n"
"v_exp_f32_e32 v42, v42\n"
"v_exp_f32_e32 v43, v43\n"
"v_cvt_pk_bf16_f32 v76, v72, v73\n"
"v_cvt_pk_bf16_f32 v77, v74, v75\n"
"v_cvt_pk_bf16_f32 v78, v40, v41\n"
"v_cvt_pk_bf16_f32 v79, v42, v43\n"
"s_waitcnt lgkmcnt(4)\n"
"v_mfma_f32_16x16x32_bf16 v[40:43], v[84:87], v[28:31], v[24:27]\n"
"ds_read_b128 v[72:75], v15 offset:16384\n"
"v_exp_f32_e32 v44, v44\n"
"v_exp_f32_e32 v45, v45\n"
"v_exp_f32_e32 v46, v46\n"
"v_mfma_f32_16x16x32_bf16 v[116:119], v[84:87], v[32:35], v[36:39]\n"
"buffer_load_dwordx4 a[56:59], v1, s[4:7], s98 offen offset:2048 sc1\n"
"ds_read_b128 v[84:87], v254 offset:16640\n"
"v_exp_f32_e32 v47, v47\n"
"v_exp_f32_e32 v112, v112\n"
"v_mfma_f32_16x16x32_bf16 v[120:123], v[100:103], v[32:35], v[36:39]\n"
"ds_read_b64_tr_b16 v[124:125], v16 offset:20480\n"
"v_exp_f32_e32 v113, v113\n"
"v_exp_f32_e32 v114, v114\n"
"v_mfma_f32_16x16x32_bf16 v[128:131], v[100:103], v[28:31], v[24:27]\n"
"buffer_load_dwordx4 a[60:63], v1, s[4:7], s98 offen offset:3072 sc1\n"
"v_exp_f32_e32 v115, v115\n"
"v_cvt_pk_bf16_f32 v100, v44, v45\n"
"v_cvt_pk_bf16_f32 v101, v46, v47\n"
"s_waitcnt lgkmcnt(3)\n"
"v_mfma_f32_16x16x32_bf16 v[44:47], v[88:91], v[68:71], 0\n"
"ds_read_b64_tr_b16 v[126:127], v255 offset:20736\n"
"s_add_u32 s98, s69, 0x604000\n"
"buffer_load_dwordx4 a[64:67], v0, s[4:7], s98 offen sc1\n"
"v_cvt_pk_bf16_f32 v102, v112, v113\n"
"v_cvt_pk_bf16_f32 v103, v114, v115\n"
"v_exp_f32_e32 v108, v108\n"
"v_mfma_f32_16x16x32_bf16 v[112:115], v[104:107], v[68:71], 0\n"
"ds_read_b64_tr_b16 v[132:133], v255 offset:20480\n"
"v_exp_f32_e32 v109, v109\n"
"v_exp_f32_e32 v110, v110\n"
"v_mfma_f32_16x16x32_bf16 v[136:139], v[80:83], v[68:71], 0\n"
"buffer_load_dword v245, v248, s[4:7], s97 offen offset:2048 sc1\n"
"v_exp_f32_e32 v111, v111\n"
"v_exp_f32_e32 v64, v64\n"
"v_mfma_f32_16x16x32_bf16 v[68:71], v[80:83], v[76:79], 0\n"
"ds_read_b64_tr_b16 v[134:135], v16 offset:20736\n"
"v_exp_f32_e32 v65, v65\n"
"v_exp_f32_e32 v66, v66\n"
"v_mfma_f32_16x16x32_bf16 v[80:83], v[104:107], v[76:79], 0\n"
"buffer_load_dword v244, v248, s[4:7], s97 offen offset:3072 sc1\n"
"s_waitcnt vmcnt(14)\n"
"buffer_store_dwordx2 v[18:19], v11, s[4:7], s68 offen offset:64 sc1\n"
"v_exp_f32_e32 v67, v67\n"
"v_mfma_f32_16x16x32_bf16 v[104:107], v[88:91], v[76:79], 0\n"
"s_cmp_lg_u32 s2, 0\n"
"v_cvt_pk_bf16_f32 v76, v108, v109\n"
"v_cvt_pk_bf16_f32 v77, v110, v111\n"
"v_cvt_pk_bf16_f32 v78, v64, v65\n"
"v_cvt_pk_bf16_f32 v79, v66, v67\n"
"s_cbranch_scc1 .Lmk_s22\n"
"s_barrier\n"
".Lmk_s22:\n"
"s_waitcnt lgkmcnt(4)\n"
"v_mfma_f32_16x16x32_bf16 v[64:67], v[72:75], v[28:31], v[24:27]\n"
"ds_read_b128 v[108:111], v15 offset:14336\n"
"v_exp_f32_e32 v40, v40\n"
"v_exp_f32_e32 v41, v41\n"
"v_exp_f32_e32 v42, v42\n"
"v_mfma_f32_16x16x32_bf16 v[140:143], v[72:75], v[32:35], v[36:39]\n"
"buffer_load_dwordx4 a[68:71], v0, s[4:7], s98 offen offset:1024 sc1\n"
"ds_read_b128 v[72:75], v254 offset:14592\n"
"v_exp_f32_e32 v43, v43\n"
"v_mfma_f32_16x16x32_bf16 v[144:147], v[84:87], v[32:35], v[36:39]\n"
"v_exp_f32_e32 v128, v128\n"
"v_exp_f32_e32 v129, v129\n"
"v_exp_f32_e32 v130, v130\n"
"v_mfma_f32_16x16x32_bf16 v[148:151], v[84:87], v[28:31], v[24:27]\n"
"buffer_load_dwordx4 a[72:75], v0, s[4:7], s98 offen offset:2048 sc1\n"
"ds_read_b64_tr_b16 v[84:85], v16 offset:18432\n"
"v_exp_f32_e32 v131, v131\n"
"s_waitcnt lgkmcnt(3)\n"
"v_mfma_f32_16x16x32_bf16 v[44:47], v[88:91], v[100:103], v[44:47]\n"
"buffer_load_dwordx4 a[76:79], v0, s[4:7], s98 offen offset:3072 sc1\n"
"v_cvt_pk_bf16_f32 v152, v40, v41\n"
"v_cvt_pk_bf16_f32 v153, v42, v43\n"
"v_cvt_pk_bf16_f32 v154, v128, v129\n"
"v_cvt_pk_bf16_f32 v155, v130, v131\n"
"v_mfma_f32_16x16x32_bf16 v[112:115], v[124:127], v[100:103], v[112:115]\n"
"ds_read_b64_tr_b16 v[86:87], v255 offset:18688\n"
"v_exp_f32_e32 v116, v116\n"
"v_exp_f32_e32 v117, v117\n"
"v_exp_f32_e32 v118, v118\n"
"v_mfma_f32_16x16x32_bf16 v[136:139], v[132:135], v[100:103], v[136:139]\n"
"s_add_u32 s97, s76, 0x81000\n"
"buffer_load_dword v243, v248, s[4:7], s97 offen sc1\n"
"v_exp_f32_e32 v119, v119\n"
"v_mfma_f32_16x16x32_bf16 v[68:71], v[132:135], v[76:79], v[68:71]\n"
"ds_read_b64_tr_b16 v[40:41], v255 offset:18432\n"
"v_exp_f32_e32 v120, v120\n"
"v_exp_f32_e32 v121, v121\n"
"v_mfma_f32_16x16x32_bf16 v[80:83], v[124:127], v[76:79], v[80:83]\n"
"buffer_load_dword v242, v248, s[4:7], s97 offen offset:1024 sc1\n"
"ds_read_b64_tr_b16 v[42:43], v16 offset:18688\n"
"v_exp_f32_e32 v122, v122\n"
"v_exp_f32_e32 v123, v123\n"
"v_mfma_f32_16x16x32_bf16 v[104:107], v[88:91], v[76:79], v[104:107]\n"
"s_waitcnt lgkmcnt(4)\n"
"v_cvt_pk_bf16_f32 v76, v116, v117\n"
"v_cvt_pk_bf16_f32 v77, v118, v119\n"
"v_cvt_pk_bf16_f32 v78, v120, v121\n"
"v_cvt_pk_bf16_f32 v79, v122, v123\n"
"s_cmp_eq_i32 s78, 8\n"
"s_cbranch_scc1 .Lmk_x23s\n"
".Lmk_x23r:\n"
"v_mfma_f32_16x16x32_bf16 v[100:103], v[108:111], v[28:31], v[24:27]\n"
"ds_read_b128 v[116:119], v15 offset:12288\n"
"v_exp_f32_e32 v64, v64\n"
"v_exp_f32_e32 v65, v65\n"
"v_exp_f32_e32 v66, v66\n"
"v_mfma_f32_16x16x32_bf16 v[120:123], v[108:111], v[32:35], v[36:39]\n"
"buffer_load_dwordx4 a[80:83], v1, s[4:7], s98 offen sc1\n"
"ds_read_b128 v[108:111], v254 offset:12544\n"
"v_exp_f32_e32 v67, v67\n"
"v_mfma_f32_16x16x32_bf16 v[124:127], v[72:75], v[32:35], v[36:39]\n"
"v_exp_f32_e32 v148, v148\n"
"v_exp_f32_e32 v149, v149\n"
"v_exp_f32_e32 v150, v150\n"
"v_mfma_f32_16x16x32_bf16 v[128:131], v[72:75], v[28:31], v[24:27]\n"
"buffer_load_dwordx4 a[84:87], v1, s[4:7], s98 offen offset:1024 sc1\n"
"ds_read_b64_tr_b16 v[72:73], v16 offset:16384\n"
"v_exp_f32_e32 v151, v151\n"
"s_waitcnt lgkmcnt(3)\n"
"v_mfma_f32_16x16x32_bf16 v[44:47], v[88:91], v[152:155], v[44:47]\n"
"buffer_load_dwordx4 a[88:91], v1, s[4:7], s98 offen offset:2048 sc1\n"
"v_cvt_pk_bf16_f32 v132, v64, v65\n"
"v_cvt_pk_bf16_f32 v133, v66, v67\n"
"v_cvt_pk_bf16_f32 v134, v148, v149\n"
"v_cvt_pk_bf16_f32 v135, v150, v151\n"
"v_mfma_f32_16x16x32_bf16 v[112:115], v[84:87], v[152:155], v[112:115]\n"
"ds_read_b64_tr_b16 v[74:75], v255 offset:16640\n"
"v_exp_f32_e32 v140, v140\n"
"v_exp_f32_e32 v141, v141\n"
"v_mfma_f32_16x16x32_bf16 v[136:139], v[40:43], v[152:155], v[136:139]\n"
"buffer_load_dword v241, v248, s[4:7], s97 offen offset:2048 sc1\n"
"v_exp_f32_e32 v142, v142\n"
"v_exp_f32_e32 v143, v143\n"
"v_mfma_f32_16x16x32_bf16 v[68:71], v[40:43], v[76:79], v[68:71]\n"
"ds_read_b64_tr_b16 v[40:41], v255 offset:16384\n"
"v_exp_f32_e32 v144, v144\n"
"v_exp_f32_e32 v145, v145\n"
"v_mfma_f32_16x16x32_bf16 v[80:83], v[84:87], v[76:79], v[80:83]\n"
"buffer_load_dword v240, v248, s[4:7], s97 offen offset:3072 sc1\n"
"ds_read_b64_tr_b16 v[42:43], v16 offset:16640\n"
"v_exp_f32_e32 v146, v146\n"
"v_exp_f32_e32 v147, v147\n"
"v_mfma_f32_16x16x32_bf16 v[104:107], v[88:91], v[76:79], v[104:107]\n"
"s_waitcnt lgkmcnt(4)\n"
"v_cvt_pk_bf16_f32 v64, v140, v141\n"
"v_cvt_pk_bf16_f32 v65, v142, v143\n"
"v_cvt_pk_bf16_f32 v66, v144, v145\n"
"v_cvt_pk_bf16_f32 v67, v146, v147\n"
"s_cmp_eq_i32 s78, 7\n"
"s_cbranch_scc1 .Lmk_x24s\n"
".Lmk_x24r:\n"
"v_mfma_f32_16x16x32_bf16 v[76:79], v[116:119], v[28:31], v[24:27]\n"
"ds_read_b128 v[84:87], v15 offset:10240\n"
"v_exp_f32_e32 v100, v100\n"
"v_exp_f32_e32 v101, v101\n"
"v_exp_f32_e32 v102, v102\n"
"v_mfma_f32_16x16x32_bf16 v[140:143], v[116:119], v[32:35], v[36:39]\n"
"buffer_load_dwordx4 a[92:95], v1, s[4:7], s98 offen offset:3072 sc1\n"
"v_exp_f32_e32 v103, v103\n"
"v_mfma_f32_16x16x32_bf16 v[116:119], v[108:111], v[32:35], v[36:39]\n"
"ds_read_b128 v[144:147], v254 offset:10496\n"
"v_exp_f32_e32 v128, v128\n"
"v_exp_f32_e32 v129, v129\n"
"v_mfma_f32_16x16x32_bf16 v[148:151], v[108:111], v[28:31], v[24:27]\n"
"ds_read_b64_tr_b16 v[108:109], v16 offset:14336\n"
"v_exp_f32_e32 v130, v130\n"
"v_exp_f32_e32 v131, v131\n"
"v_cvt_pk_bf16_f32 v152, v100, v101\n"
"s_waitcnt lgkmcnt(3)\n"
"v_mfma_f32_16x16x32_bf16 v[44:47], v[88:91], v[132:135], v[44:47]\n"
"s_add_u32 s98, s69, 0x606000\n"
"buffer_load_dwordx4 a[96:99], v0, s[4:7], s98 offen sc1\n"
"v_cvt_pk_bf16_f32 v153, v102, v103\n"
"v_cvt_pk_bf16_f32 v154, v128, v129\n"
"v_mfma_f32_16x16x32_bf16 v[112:115], v[72:75], v[132:135], v[112:115]\n"
"ds_read_b64_tr_b16 v[110:111], v255 offset:14592\n"
"v_cvt_pk_bf16_f32 v155, v130, v131\n"
"v_exp_f32_e32 v120, v120\n"
"v_exp_f32_e32 v121, v121\n"
"v_mfma_f32_16x16x32_bf16 v[136:139], v[40:43], v[132:135], v[136:139]\n"
"ds_read_b64_tr_b16 v[100:101], v255 offset:14336\n"
"v_exp_f32_e32 v122, v122\n"
"v_exp_f32_e32 v123, v123\n"
"v_mfma_f32_16x16x32_bf16 v[68:71], v[40:43], v[64:67], v[68:71]\n"
"buffer_load_dwordx4 a[100:103], v0, s[4:7], s98 offen offset:1024 sc1\n"
"v_exp_f32_e32 v124, v124\n"
"v_exp_f32_e32 v125, v125\n"
"v_mfma_f32_16x16x32_bf16 v[80:83], v[72:75], v[64:67], v[80:83]\n"
"ds_read_b64_tr_b16 v[102:103], v16 offset:14592\n"
"v_exp_f32_e32 v126, v126\n"
"v_exp_f32_e32 v127, v127\n"
"v_mfma_f32_16x16x32_bf16 v[104:107], v[88:91], v[64:67], v[104:107]\n"
"s_waitcnt lgkmcnt(4)\n"
"v_cvt_pk_bf16_f32 v40, v120, v121\n"
"v_cvt_pk_bf16_f32 v41, v122, v123\n"
"v_cvt_pk_bf16_f32 v42, v124, v125\n"
"v_cvt_pk_bf16_f32 v43, v126, v127\n"
"s_cmp_eq_i32 s78, 6\n"
"s_cbranch_scc1 .Lmk_x25s\n"
".Lmk_x25r:\n"
"v_mfma_f32_16x16x32_bf16 v[64:67], v[84:87], v[28:31], v[24:27]\n"
"ds_read_b128 v[72:75], v15 offset:8192\n"
"v_exp_f32_e32 v76, v76\n"
"v_exp_f32_e32 v77, v77\n"
"v_exp_f32_e32 v78, v78\n"
"v_mfma_f32_16x16x32_bf16 v[120:123], v[84:87], v[32:35], v[36:39]\n"
"buffer_load_dwordx4 a[104:107], v0, s[4:7], s98 offen offset:2048 sc1\n"
"v_exp_f32_e32 v79, v79\n"
"v_mfma_f32_16x16x32_bf16 v[84:87], v[144:147], v[32:35], v[36:39]\n"
"ds_read_b128 v[124:127], v254 offset:8448\n"
"v_exp_f32_e32 v148, v148\n"
"v_exp_f32_e32 v149, v149\n"
"v_mfma_f32_16x16x32_bf16 v[128:131], v[144:147], v[28:31], v[24:27]\n"
"ds_read_b64_tr_b16 v[132:133], v16 offset:12288\n"
"v_exp_f32_e32 v150, v150\n"
"v_exp_f32_e32 v151, v151\n"
"s_waitcnt lgkmcnt(3)\n"
"v_mfma_f32_16x16x32_bf16 v[44:47], v[88:91], v[152:155], v[44:47]\n"
"buffer_load_dwordx4 a[108:111], v0, s[4:7], s98 offen offset:3072 sc1\n"
"v_cvt_pk_bf16_f32 v144, v76, v77\n"
"v_cvt_pk_bf16_f32 v145, v78, v79\n"
"v_cvt_pk_bf16_f32 v146, v148, v149\n"
"v_mfma_f32_16x16x32_bf16 v[112:115], v[108:111], v[152:155], v[112:115]\n"
"ds_read_b64_tr_b16 v[134:135], v255 offset:12544\n"
"v_cvt_pk_bf16_f32 v147, v150, v151\n"
"v_exp_f32_e32 v140, v140\n"
"v_exp_f32_e32 v141, v141\n"
"v_mfma_f32_16x16x32_bf16 v[136:139], v[100:103], v[152:155], v[136:139]\n"
"ds_read_b64_tr_b16 v[76:77], v255 offset:12288\n"
"v_exp_f32_e32 v142, v142\n"
"v_exp_f32_e32 v143, v143\n"
"v_mfma_f32_16x16x32_bf16 v[68:71], v[100:103], v[40:43], v[68:71]\n"
"buffer_load_dwordx4 a[112:115], v1, s[4:7], s98 offen sc1\n"
"v_exp_f32_e32 v116, v116\n"
"v_exp_f32_e32 v117, v117\n"
"v_mfma_f32_16x16x32_bf16 v[80:83], v[108:111], v[40:43], v[80:83]\n"
"ds_read_b64_tr_b16 v[78:79], v16 offset:12544\n"
"v_exp_f32_e32 v118, v118\n"
"v_exp_f32_e32 v119, v119\n"
"v_mfma_f32_16x16x32_bf16 v[104:107], v[88:91], v[40:43], v[104:107]\n"
"s_waitcnt lgkmcnt(4)\n"
"v_cvt_pk_bf16_f32 v40, v140, v141\n"
"v_cvt_pk_bf16_f32 v41, v142, v143\n"
"v_cvt_pk_bf16_f32 v42, v116, v117\n"
"v_cvt_pk_bf16_f32 v43, v118, v119\n"
"s_cmp_eq_i32 s78, 5\n"
"s_cbranch_scc1 .Lmk_x26s\n"
".Lmk_x26r:\n"
"v_mfma_f32_16x16x32_bf16 v[100:103], v[72:75], v[28:31], v[24:27]\n"
"ds_read_b64_tr_b16 v[108:109], v16 offset:10240\n"
"ds_read_b64_tr_b16 v[110:111], v255 offset:10496\n"
"v_exp_f32_e32 v64, v64\n"
"v_exp_f32_e32 v65, v65\n"
"v_mfma_f32_16x16x32_bf16 v[116:119], v[72:75], v[32:35], v[36:39]\n"
"s_add_u32 s97, s71, 0xfffff000\n"
"buffer_load_dwordx2 v[238:239], off, s[4:7], s97 sc1\n"
"v_exp_f32_e32 v66, v66\n"
"v_exp_f32_e32 v67, v67\n"
"v_mfma_f32_16x16x32_bf16 v[72:75], v[124:127], v[32:35], v[36:39]\n"
"ds_read_b64_tr_b16 v[36:37], v255 offset:10240\n"
"v_exp_f32_e32 v128, v128\n"
"v_exp_f32_e32 v129, v129\n"
"v_mfma_f32_16x16x32_bf16 v[140:143], v[124:127], v[28:31], v[24:27]\n"
"ds_read_b64_tr_b16 v[38:39], v16 offset:10496\n"
"buffer_load_dwordx4 a[116:119], v1, s[4:7], s98 offen offset:1024 sc1\n"
"v_exp_f32_e32 v130, v130\n"
"v_exp_f32_e32 v131, v131\n"
"s_waitcnt lgkmcnt(4)\n"
"v_mfma_f32_16x16x32_bf16 v[44:47], v[88:91], v[144:147], v[44:47]\n"
"ds_read_b64_tr_b16 v[124:125], v16 offset:24576\n"
"ds_read_b64_tr_b16 v[126:127], v255 offset:24832\n"
"v_cvt_pk_bf16_f32 v148, v64, v65\n"
"v_cvt_pk_bf16_f32 v149, v66, v67\n"
"v_cvt_pk_bf16_f32 v150, v128, v129\n"
"v_mfma_f32_16x16x32_bf16 v[112:115], v[132:135], v[144:147], v[112:115]\n"
"ds_read_b64_tr_b16 v[64:65], v255 offset:24576\n"
"buffer_load_dwordx4 a[120:123], v1, s[4:7], s98 offen offset:2048 sc1\n"
"v_cvt_pk_bf16_f32 v151, v130, v131\n"
"v_exp_f32_e32 v120, v120\n"
"v_mfma_f32_16x16x32_bf16 v[136:139], v[76:79], v[144:147], v[136:139]\n"
"ds_read_b64_tr_b16 v[66:67], v16 offset:24832\n"
"v_exp_f32_e32 v121, v121\n"
"v_exp_f32_e32 v122, v122\n"
"v_exp_f32_e32 v123, v123\n"
"v_mfma_f32_16x16x32_bf16 v[68:71], v[76:79], v[40:43], v[68:71]\n"
"v_exp_f32_e32 v84, v84\n"
"v_add_u32_e32 v248, 0x26400, v0\n"
"v_exp_f32_e32 v85, v85\n"
"v_mfma_f32_16x16x32_bf16 v[80:83], v[132:135], v[40:43], v[80:83]\n"
"buffer_load_dwordx4 a[124:127], v1, s[4:7], s98 offen offset:3072 sc1\n"
"ds_read_b128 v[76:79], v248\n"
"v_exp_f32_e32 v86, v86\n"
"v_exp_f32_e32 v87, v87\n"
"v_mfma_f32_16x16x32_bf16 v[104:107], v[88:91], v[40:43], v[104:107]\n"
"ds_read_b128 v[128:131], v248 offset:1024\n"
"v_cvt_pk_bf16_f32 v40, v120, v121\n"
"v_cvt_pk_bf16_f32 v41, v122, v123\n"
"v_cvt_pk_bf16_f32 v42, v84, v85\n"
"v_cvt_pk_bf16_f32 v43, v86, v87\n"
"s_waitcnt lgkmcnt(6)\n"
"v_mfma_f32_16x16x32_bf16 v[44:47], v[88:91], v[148:151], v[44:47]\n"
"ds_read_b64_tr_b16 v[84:85], v16 offset:8192\n"
"s_add_u32 s98, s69, 0x608000\n"
"buffer_load_dwordx4 v[24:27], v0, s[4:7], s98 offen sc1\n"
"ds_read_b64_tr_b16 v[86:87], v255 offset:8448\n"
"v_cndmask_b32_e64 v100, v48, v100, s[20:21]\n"
"v_cndmask_b32_e64 v101, v49, v101, s[22:23]\n"
"v_cndmask_b32_e64 v102, v50, v102, s[24:25]\n"
"v_cndmask_b32_e64 v103, v51, v103, s[26:27]\n"
"v_cndmask_b32_e64 v140, v60, v140, s[28:29]\n"
"v_cndmask_b32_e64 v141, v61, v141, s[30:31]\n"
"v_cndmask_b32_e64 v142, v62, v142, s[32:33]\n"
"v_mfma_f32_16x16x32_bf16 v[112:115], v[108:111], v[148:151], v[112:115]\n"
"ds_read_b64_tr_b16 v[48:49], v255 offset:8192\n"
"ds_read_b64_tr_b16 v[50:51], v16 offset:8448\n"
"v_cndmask_b32_e64 v143, v63, v143, s[34:35]\n"
"v_exp_f32_e32 v100, v100\n"
"v_exp_f32_e32 v101, v101\n"
"v_exp_f32_e32 v102, v102\n"
"s_waitcnt vmcnt(11)\n"
"v_exp_f32_e32 v103, v103\n"
"v_exp_f32_e32 v140, v140\n"
"v_mfma_f32_16x16x32_bf16 v[136:139], v[36:39], v[148:151], v[136:139]\n"
"v_add_f32_e32 v247, v247, v246\n"
"v_exp_f32_e32 v141, v141\n"
"v_exp_f32_e32 v142, v142\n"
"v_add_f32_e32 v247, v247, v245\n"
"v_exp_f32_e32 v143, v143\n"
"v_cvt_pk_bf16_f32 v60, v100, v101\n"
"v_add_f32_e32 v247, v247, v244\n"
"v_cvt_pk_bf16_f32 v61, v102, v103\n"
"v_cvt_pk_bf16_f32 v62, v140, v141\n"
"v_mfma_f32_16x16x32_bf16 v[68:71], v[36:39], v[40:43], v[68:71]\n"
"v_cvt_pk_bf16_f32 v63, v142, v143\n"
"v_add_f32_e32 v247, v247, v243\n"
"v_cndmask_b32_e64 v116, v52, v116, s[36:37]\n"
"v_cndmask_b32_e64 v117, v53, v117, s[38:39]\n"
"v_cndmask_b32_e64 v118, v54, v118, s[40:41]\n"
"v_cndmask_b32_e64 v119, v55, v119, s[42:43]\n"
"v_add_f32_e32 v247, v247, v242\n"
"v_cndmask_b32_e64 v72, v56, v72, s[44:45]\n"
"v_cndmask_b32_e64 v73, v57, v73, s[46:47]\n"
"v_cndmask_b32_e64 v74, v58, v74, s[48:49]\n"
"v_add_f32_e32 v247, v247, v241\n"
"v_cndmask_b32_e64 v75, v59, v75, s[50:51]\n"
"v_exp_f32_e32 v116, v116\n"
"v_mfma_f32_16x16x32_bf16 v[80:83], v[108:111], v[40:43], v[80:83]\n"
"v_exp_f32_e32 v117, v117\n"
"v_add_f32_e32 v247, v247, v240\n"
"v_exp_f32_e32 v118, v118\n"
"v_mov_b32_e32 v246, v247\n"
"v_exp_f32_e32 v119, v119\n"
"v_exp_f32_e32 v72, v72\n"
"v_permlane32_swap_b32_e32 v247, v246\n"
"v_exp_f32_e32 v73, v73\n"
"v_mfma_f32_16x16x32_bf16 v[104:107], v[88:91], v[40:43], v[104:107]\n"
"v_exp_f32_e32 v74, v74\n"
"v_add_f32_e32 v247, v247, v246\n"
"v_exp_f32_e32 v75, v75\n"
"v_cvt_pk_bf16_f32 v36, v116, v117\n"
"ds_write_b32 v21, v247 offset:512\n"
"v_cvt_pk_bf16_f32 v37, v118, v119\n"
"v_cvt_pk_bf16_f32 v38, v72, v73\n"
"v_cvt_pk_bf16_f32 v39, v74, v75\n"
"s_waitcnt lgkmcnt(1)\n"
"v_mfma_f32_16x16x32_bf16 v[44:47], v[88:91], v[60:63], v[44:47]\n"
"v_and_b32_e32 v76, v60, v76\n"
"v_and_b32_e32 v77, v61, v77\n"
"v_and_b32_e32 v78, v62, v78\n"
"v_and_b32_e32 v79, v63, v79\n"
"v_mfma_f32_16x16x32_bf16 v[104:107], v[88:91], v[36:39], v[104:107]\n"
"v_xor_b32_e32 v40, v60, v76\n"
"v_xor_b32_e32 v41, v61, v77\n"
"v_xor_b32_e32 v42, v62, v78\n"
"v_xor_b32_e32 v43, v63, v79\n"
"v_mfma_f32_16x16x32_bf16 v[112:115], v[84:87], v[76:79], v[112:115]\n"
"v_and_b32_e32 v128, v36, v128\n"
"v_and_b32_e32 v129, v37, v129\n"
"v_mfma_f32_16x16x32_bf16 v[136:139], v[48:51], v[76:79], v[136:139]\n"
"v_and_b32_e32 v130, v38, v130\n"
"v_and_b32_e32 v131, v39, v131\n"
"v_mfma_f32_16x16x32_bf16 v[112:115], v[124:127], v[40:43], v[112:115]\n"
"v_xor_b32_e32 v52, v36, v128\n"
"v_xor_b32_e32 v53, v37, v129\n"
"v_mfma_f32_16x16x32_bf16 v[136:139], v[64:67], v[40:43], v[136:139]\n"
"v_xor_b32_e32 v54, v38, v130\n"
"v_xor_b32_e32 v55, v39, v131\n"
"v_mfma_f32_16x16x32_bf16 v[80:83], v[84:87], v[128:131], v[80:83]\n"
"s_waitcnt vmcnt(4)\n"
"v_cmp_ne_u64_e32 vcc, v[238:239], v[18:19]\n"
"s_nop 0\n"
"s_cbranch_vccnz .Lmk_f19s\n"
".Lmk_f19r:\n"
"v_mfma_f32_16x16x32_bf16 v[68:71], v[48:51], v[128:131], v[68:71]\n"
"s_add_u32 m0, s77, 0x4000\n"
"s_add_u32 s97, s65, 0\n"
"buffer_load_dwordx4 v2, s[4:7], s97 offen sc1 lds\n"
"buffer_load_dwordx4 v2, s[4:7], s97 offen offset:1024 sc1 lds\n"
"v_mfma_f32_16x16x32_bf16 v[80:83], v[124:127], v[52:55], v[80:83]\n"
"s_add_u32 m0, s77, 0xc000\n"
"s_add_u32 s97, s65, 0x4000\n"
"buffer_load_dwordx4 v2, s[4:7], s97 offen sc1 lds\n"
"buffer_load_dwordx4 v2, s[4:7], s97 offen offset:1024 sc1 lds\n"
"v_mfma_f32_16x16x32_bf16 v[68:71], v[64:67], v[52:55], v[68:71]\n"
"v_max_f32_e32 v248, v44, v104\n"
"v_rcp_f32_e32 v44, v44\n"
"v_rcp_f32_e32 v104, v104\n"
"s_nop 0\n"
"v_max3_f32 v248, v248, v44, v104\n"
"v_cmp_nlt_f32_e32 vcc, v248, v252\n"
"s_cbranch_vccnz .Lmk_b21\n"
".Lmk_k27:\n"
"s_add_u32 s97, s60, 0x40000\n"
"v_mul_f32_e32 v112, v112, v44\n"
"v_mul_f32_e32 v113, v113, v44\n"
"v_mul_f32_e32 v114, v114, v44\n"
"v_mul_f32_e32 v115, v115, v44\n"
"v_mul_f32_e32 v136, v136, v44\n"
"v_mul_f32_e32 v137, v137, v44\n"
"v_mul_f32_e32 v138, v138, v44\n"
"v_mul_f32_e32 v139, v139, v44\n"
"v_cvt_pk_bf16_f32 v28, v112, v113\n"
"v_cvt_pk_bf16_f32 v29, v114, v115\n"
"v_cvt_pk_bf16_f32 v30, v136, v137\n"
"v_cvt_pk_bf16_f32 v31, v138, v139\n"
"buffer_store_dwordx4 v[28:31], v0, s[4:7], s97 offen sc1\n"
"s_add_u32 s97, s60, 0x48000\n"
"v_mul_f32_e32 v80, v80, v104\n"
"v_mul_f32_e32 v81, v81, v104\n"
"v_mul_f32_e32 v82, v82, v104\n"
"v_mul_f32_e32 v83, v83, v104\n"
"v_mul_f32_e32 v68, v68, v104\n"
"v_mul_f32_e32 v69, v69, v104\n"
"v_mul_f32_e32 v70, v70, v104\n"
"v_mul_f32_e32 v71, v71, v104\n"
"v_cvt_pk_bf16_f32 v28, v80, v81\n"
"v_cvt_pk_bf16_f32 v29, v82, v83\n"
"v_cvt_pk_bf16_f32 v30, v68, v69\n"
"v_cvt_pk_bf16_f32 v31, v70, v71\n"
"buffer_store_dwordx4 v[28:31], v0, s[4:7], s97 offen sc1\n"
";\n"
"; @A AT a\n"
"s_mov_b32 s2, 0\n"
"v_mov_b32_e32 v253, 0xff800000\n"
"v_mov_b32_e32 v251, 0xff800000\n"
"v_mov_b32_e32 v250, 0x7f800000\n"
"v_mov_b32_e32 v249, 0x7f800000\n"
".Lmk_a28:\n"
"ds_read_b128 v[28:31], v15 offset:16384\n"
"ds_read_b128 v[32:35], v254 offset:16640\n"
"ds_read_b128 v[36:39], v15 offset:14336\n"
"ds_read_b128 v[40:43], v254 offset:14592\n"
"s_cmp_gt_i32 s78, 7\n"
"s_cselect_b32 s97, 0x80000000, 0\n"
"s_or_b32 s97, s97, 0x7f800000\n"
"s_add_u32 s96, s67, 32\n"
"buffer_load_dwordx2 v[246:247], v12, s[4:7], s96 offen sc1\n"
"s_waitcnt lgkmcnt(2)\n"
"v_mfma_f32_16x16x32_bf16 v[100:103], v[28:31], v[92:95], 0\n"
"v_mfma_f32_16x16x32_bf16 v[104:107], v[28:31], v[96:99], 0\n"
"v_mfma_f32_16x16x32_bf16 v[108:111], v[32:35], v[96:99], 0\n"
"v_mfma_f32_16x16x32_bf16 v[112:115], v[32:35], v[92:95], 0\n"
"s_waitcnt lgkmcnt(0)\n"
"s_nop 3\n"
"v_max_f32_e32 v248, v100, v101\n"
"v_max_f32_e32 v245, v104, v105\n"
"v_max3_f32 v248, v248, v102, v103\n"
"v_max3_f32 v245, v245, v106, v107\n"
"v_max3_f32 v248, v248, v112, v113\n"
"v_max3_f32 v245, v245, v108, v109\n"
"v_max3_f32 v248, v248, v114, v115\n"
"v_max3_f32 v245, v245, v110, v111\n"
"v_mov_b32_e32 v244, v248\n"
"v_mov_b32_e32 v243, v245\n"
"s_nop 0\n"
"v_permlane16_swap_b32_e32 v248, v244\n"
"v_permlane16_swap_b32_e32 v245, v243\n"
"v_max_f32_e32 v248, v248, v244\n"
"v_max_f32_e32 v245, v245, v243\n"
"v_mov_b32_e32 v244, v248\n"
"v_mov_b32_e32 v243, v245\n"
"s_nop 0\n"
"v_permlane32_swap_b32_e32 v248, v244\n"
"v_permlane32_swap_b32_e32 v245, v243\n"
"v_max_f32_e32 v248, v248, v244\n"
"v_max_f32_e32 v245, v245, v243\n"
"v_med3_f32 v248, v248, v253, v250\n"
"v_med3_f32 v245, v245, v251, v249\n"
"v_min_f32_e64 v116, s97, -v248\n"
"v_min_f32_e64 v117, s97, -v248\n"
"v_min_f32_e64 v118, s97, -v248\n"
"v_min_f32_e64 v119, s97, -v248\n"
"v_min_f32_e64 v120, s97, -v245\n"
"v_min_f32_e64 v121, s97, -v245\n"
"v_min_f32_e64 v122, s97, -v245\n"
"v_min_f32_e64 v123, s97, -v245\n"
"v_mfma_f32_16x16x32_bf16 v[44:47], v[36:39], v[92:95], v[116:119]\n"
"ds_read_b128 v[32:35], v15 offset:12288\n"
"v_sub_f32_e32 v100, v100, v248\n"
"v_sub_f32_e32 v101, v101, v248\n"
"v_sub_f32_e32 v102, v102, v248\n"
"v_sub_f32_e32 v103, v103, v248\n"
"v_mfma_f32_16x16x32_bf16 v[48:51], v[36:39], v[96:99], v[120:123]\n"
"v_sub_f32_e32 v112, v112, v248\n"
"v_sub_f32_e32 v113, v113, v248\n"
"v_sub_f32_e32 v114, v114, v248\n"
"v_sub_f32_e32 v115, v115, v248\n"
"v_mfma_f32_16x16x32_bf16 v[52:55], v[40:43], v[96:99], v[120:123]\n"
"ds_read_b128 v[56:59], v254 offset:12544\n"
"v_sub_f32_e32 v104, v104, v245\n"
"v_sub_f32_e32 v105, v105, v245\n"
"v_sub_f32_e32 v106, v106, v245\n"
"v_sub_f32_e32 v107, v107, v245\n"
"v_mfma_f32_16x16x32_bf16 v[36:39], v[40:43], v[92:95], v[116:119]\n"
"s_waitcnt lgkmcnt(0)\n"
"v_sub_f32_e32 v108, v108, v245\n"
"v_sub_f32_e32 v109, v109, v245\n"
"v_sub_f32_e32 v110, v110, v245\n"
"v_sub_f32_e32 v111, v111, v245\n"
"s_cmp_eq_i32 s78, 7\n"
"s_cbranch_scc1 .Lmk_x30s\n"
".Lmk_x30r:\n"
"v_mfma_f32_16x16x32_bf16 v[60:63], v[32:35], v[92:95], v[116:119]\n"
"ds_read_b128 v[40:43], v15 offset:10240\n"
"buffer_load_dwordx4 v[28:31], v0, s[4:7], s98 offen offset:1024 sc1\n"
"ds_read_b128 v[64:67], v254 offset:10496\n"
"v_exp_f32_e32 v44, v44\n"
"v_exp_f32_e32 v45, v45\n"
"v_exp_f32_e32 v46, v46\n"
"v_exp_f32_e32 v47, v47\n"
"v_exp_f32_e32 v36, v36\n"
"v_mfma_f32_16x16x32_bf16 v[68:71], v[32:35], v[96:99], v[120:123]\n"
"ds_read_b64_tr_b16 v[72:73], v16 offset:14336\n"
"buffer_load_dwordx4 v[32:35], v0, s[4:7], s98 offen offset:2048 sc1\n"
"v_exp_f32_e32 v37, v37\n"
"v_exp_f32_e32 v38, v38\n"
"v_exp_f32_e32 v39, v39\n"
"v_cvt_pk_bf16_f32 v76, v44, v45\n"
"v_cvt_pk_bf16_f32 v77, v46, v47\n"
"v_cvt_pk_bf16_f32 v78, v36, v37\n"
"v_mfma_f32_16x16x32_bf16 v[80:83], v[56:59], v[96:99], v[120:123]\n"
"ds_read_b64_tr_b16 v[74:75], v255 offset:14592\n"
"ds_read_b64_tr_b16 v[84:85], v255 offset:14336\n"
"v_cvt_pk_bf16_f32 v79, v38, v39\n"
"v_exp_f32_e32 v48, v48\n"
"v_exp_f32_e32 v49, v49\n"
"v_exp_f32_e32 v50, v50\n"
"v_exp_f32_e32 v51, v51\n"
"v_mfma_f32_16x16x32_bf16 v[124:127], v[56:59], v[92:95], v[116:119]\n"
"buffer_load_dwordx4 v[36:39], v0, s[4:7], s98 offen offset:3072 sc1\n"
"ds_read_b64_tr_b16 v[86:87], v16 offset:14592\n"
"v_exp_f32_e32 v52, v52\n"
"v_exp_f32_e32 v53, v53\n"
"v_exp_f32_e32 v54, v54\n"
"v_exp_f32_e32 v55, v55\n"
"v_cvt_pk_bf16_f32 v56, v48, v49\n"
"v_cvt_pk_bf16_f32 v57, v50, v51\n"
"v_cvt_pk_bf16_f32 v58, v52, v53\n"
"v_cvt_pk_bf16_f32 v59, v54, v55\n"
"s_waitcnt lgkmcnt(4)\n"
"s_cmp_eq_i32 s78, 6\n"
"s_cbranch_scc1 .Lmk_x31s\n"
".Lmk_x31r:\n"
"v_mfma_f32_16x16x32_bf16 v[128:131], v[40:43], v[92:95], v[116:119]\n"
"ds_read_b128 v[52:55], v15 offset:8192\n"
"v_exp_f32_e32 v60, v60\n"
"v_exp_f32_e32 v61, v61\n"
"v_exp_f32_e32 v62, v62\n"
"v_mfma_f32_16x16x32_bf16 v[132:135], v[40:43], v[96:99], v[120:123]\n"
"buffer_load_dwordx4 v[40:43], v1, s[4:7], s98 offen sc1\n"
"v_exp_f32_e32 v63, v63\n"
"v_mfma_f32_16x16x32_bf16 v[136:139], v[64:67], v[96:99], v[120:123]\n"
"ds_read_b128 v[140:143], v254 offset:8448\n"
"v_exp_f32_e32 v124, v124\n"
"v_exp_f32_e32 v125, v125\n"
"v_mfma_f32_16x16x32_bf16 v[144:147], v[64:67], v[92:95], v[116:119]\n"
"ds_read_b64_tr_b16 v[64:65], v16 offset:12288\n"
"v_exp_f32_e32 v126, v126\n"
"v_exp_f32_e32 v127, v127\n"
"s_waitcnt lgkmcnt(3)\n"
"v_mfma_f32_16x16x32_bf16 v[148:151], v[88:91], v[76:79], 0\n"
"buffer_load_dwordx4 v[44:47], v1, s[4:7], s98 offen offset:1024 sc1\n"
"v_cvt_pk_bf16_f32 v152, v60, v61\n"
"v_cvt_pk_bf16_f32 v153, v62, v63\n"
"v_cvt_pk_bf16_f32 v154, v124, v125\n"
"v_mfma_f32_16x16x32_bf16 v[156:159], v[72:75], v[76:79], 0\n"
"ds_read_b64_tr_b16 v[66:67], v255 offset:12544\n"
"v_cvt_pk_bf16_f32 v155, v126, v127\n"
"v_exp_f32_e32 v68, v68\n"
"v_exp_f32_e32 v69, v69\n"
"v_mfma_f32_16x16x32_bf16 v[124:127], v[84:87], v[76:79], 0\n"
"ds_read_b64_tr_b16 v[60:61], v255 offset:12288\n"
"v_exp_f32_e32 v70, v70\n"
"v_exp_f32_e32 v71, v71\n"
"v_mfma_f32_16x16x32_bf16 v[160:163], v[84:87], v[56:59], 0\n"
"buffer_load_dwordx4 v[48:51], v1, s[4:7], s98 offen offset:2048 sc1\n"
"v_exp_f32_e32 v80, v80\n"
"v_exp_f32_e32 v81, v81\n"
"v_mfma_f32_16x16x32_bf16 v[164:167], v[72:75], v[56:59], 0\n"
"ds_read_b64_tr_b16 v[62:63], v16 offset:12544\n"
"v_exp_f32_e32 v82, v82\n"
"v_exp_f32_e32 v83, v83\n"
"v_mfma_f32_16x16x32_bf16 v[168:171], v[88:91], v[56:59], 0\n"
"s_waitcnt lgkmcnt(4)\n"
"v_cvt_pk_bf16_f32 v72, v68, v69\n"
"v_cvt_pk_bf16_f32 v73, v70, v71\n"
"v_cvt_pk_bf16_f32 v74, v80, v81\n"
"v_cvt_pk_bf16_f32 v75, v82, v83\n"
"s_cmp_eq_i32 s78, 5\n"
"s_cbranch_scc1 .Lmk_x32s\n"
".Lmk_x32r:\n"
"v_mfma_f32_16x16x32_bf16 v[76:79], v[52:55], v[92:95], v[116:119]\n"
"ds_read_b64_tr_b16 v[68:69], v16 offset:10240\n"
"v_exp_f32_e32 v128, v128\n"
"v_exp_f32_e32 v129, v129\n"
"v_exp_f32_e32 v130, v130\n"
"v_mfma_f32_16x16x32_bf16 v[80:83], v[52:55], v[96:99], v[120:123]\n"
"buffer_load_dwordx4 v[52:55], v1, s[4:7], s98 offen offset:3072 sc1\n"
"v_exp_f32_e32 v131, v131\n"
"v_mfma_f32_16x16x32_bf16 v[84:87], v[140:143], v[96:99], v[120:123]\n"
"ds_read_b64_tr_b16 v[70:71], v255 offset:10496\n"
"v_exp_f32_e32 v144, v144\n"
"v_exp_f32_e32 v145, v145\n"
"v_mfma_f32_16x16x32_bf16 v[172:175], v[140:143], v[92:95], v[116:119]\n"
"s_waitcnt lgkmcnt(2)\n"
"v_exp_f32_e32 v146, v146\n"
"v_exp_f32_e32 v147, v147\n"
"v_cvt_pk_bf16_f32 v140, v128, v129\n"
"v_mfma_f32_16x16x32_bf16 v[148:151], v[88:91], v[152:155], v[148:151]\n"
"s_add_u32 s97, s69, 0x60a000\n"
"buffer_load_dwordx4 v[56:59], v0, s[4:7], s97 offen sc1\n"
"v_cvt_pk_bf16_f32 v141, v130, v131\n"
"v_cvt_pk_bf16_f32 v142, v144, v145\n"
"v_mfma_f32_16x16x32_bf16 v[156:159], v[64:67], v[152:155], v[156:159]\n"
"ds_read_b64_tr_b16 v[128:129], v255 offset:10240\n"
"v_cvt_pk_bf16_f32 v143, v146, v147\n"
"v_exp_f32_e32 v132, v132\n"
"v_exp_f32_e32 v133, v133\n"
"v_mfma_f32_16x16x32_bf16 v[124:127], v[60:63], v[152:155], v[124:127]\n"
"v_exp_f32_e32 v134, v134\n"
"v_exp_f32_e32 v135, v135\n"
"v_mfma_f32_16x16x32_bf16 v[160:163], v[60:63], v[72:75], v[160:163]\n"
"buffer_load_dwordx4 v[60:63], v0, s[4:7], s97 offen offset:1024 sc1\n"
"ds_read_b64_tr_b16 v[130:131], v16 offset:10496\n"
"v_exp_f32_e32 v136, v136\n"
"v_mfma_f32_16x16x32_bf16 v[164:167], v[64:67], v[72:75], v[164:167]\n"
"v_exp_f32_e32 v137, v137\n"
"v_exp_f32_e32 v138, v138\n"
"v_exp_f32_e32 v139, v139\n"
"v_mfma_f32_16x16x32_bf16 v[168:171], v[88:91], v[72:75], v[168:171]\n"
"s_waitcnt vmcnt(12)\n"
"v_cvt_pk_bf16_f32 v72, v132, v133\n"
"v_cvt_pk_bf16_f32 v73, v134, v135\n"
"v_cvt_pk_bf16_f32 v74, v136, v137\n"
"v_cvt_pk_bf16_f32 v75, v138, v139\n"
"s_cmp_lg_u32 s2, 0\n"
"s_cbranch_scc1 .Lmk_s33\n"
"s_barrier\n"
".Lmk_s33:\n"
"ds_read_b128 v[132:135], v15 offset:6144\n"
"ds_read_b128 v[136:139], v254 offset:6400\n"
"s_waitcnt lgkmcnt(2)\n"
"v_mfma_f32_16x16x32_bf16 v[148:151], v[88:91], v[140:143], v[148:151]\n"
"ds_read_b128 v[144:147], v15 offset:4096\n"
"v_exp_f32_e32 v76, v76\n"
"v_exp_f32_e32 v77, v77\n"
"v_exp_f32_e32 v78, v78\n"
"v_mfma_f32_16x16x32_bf16 v[156:159], v[68:71], v[140:143], v[156:159]\n"
"buffer_load_dwordx4 v[64:67], v0, s[4:7], s97 offen offset:2048 sc1\n"
"ds_read_b128 v[152:155], v254 offset:4352\n"
"v_exp_f32_e32 v79, v79\n"
"v_mfma_f32_16x16x32_bf16 v[124:127], v[128:131], v[140:143], v[124:127]\n"
"ds_read_b64_tr_b16 v[140:141], v16 offset:8192\n"
"v_exp_f32_e32 v172, v172\n"
"v_exp_f32_e32 v173, v173\n"
"v_mfma_f32_16x16x32_bf16 v[160:163], v[128:131], v[72:75], v[160:163]\n"
"ds_read_b64_tr_b16 v[142:143], v255 offset:8448\n"
"v_exp_f32_e32 v174, v174\n"
"v_exp_f32_e32 v175, v175\n"
"v_mfma_f32_16x16x32_bf16 v[164:167], v[68:71], v[72:75], v[164:167]\n"
"buffer_load_dwordx4 v[68:71], v0, s[4:7], s97 offen offset:3072 sc1\n"
"v_cvt_pk_bf16_f32 v128, v76, v77\n"
"v_cvt_pk_bf16_f32 v129, v78, v79\n"
"v_cvt_pk_bf16_f32 v130, v172, v173\n"
"v_cvt_pk_bf16_f32 v131, v174, v175\n"
"v_mfma_f32_16x16x32_bf16 v[168:171], v[88:91], v[72:75], v[168:171]\n"
"ds_read_b64_tr_b16 v[172:173], v255 offset:8192\n"
"v_exp_f32_e32 v80, v80\n"
"v_exp_f32_e32 v81, v81\n"
"s_waitcnt lgkmcnt(5)\n"
"v_mfma_f32_16x16x32_bf16 v[176:179], v[132:135], v[92:95], v[116:119]\n"
"ds_read_b64_tr_b16 v[174:175], v16 offset:8448\n"
"v_exp_f32_e32 v82, v82\n"
"v_exp_f32_e32 v83, v83\n"
"v_exp_f32_e32 v84, v84\n"
"v_mfma_f32_16x16x32_bf16 v[180:183], v[132:135], v[96:99], v[120:123]\n"
"buffer_load_dwordx4 v[72:75], v1, s[4:7], s97 offen sc1\n"
"s_waitcnt vmcnt(12)\n"
"v_cmp_ne_u64_e32 vcc, v[246:247], v[18:19]\n"
"v_mfma_f32_16x16x32_bf16 v[132:135], v[136:139], v[96:99], v[120:123]\n"
"v_exp_f32_e32 v85, v85\n"
"v_exp_f32_e32 v86, v86\n"
"v_exp_f32_e32 v87, v87\n"
"v_mfma_f32_16x16x32_bf16 v[184:187], v[136:139], v[92:95], v[116:119]\n"
"s_cbranch_vccnz .Lmk_f4s\n"
".Lmk_f4r:\n"
"v_cvt_pk_bf16_f32 v136, v80, v81\n"
"v_cvt_pk_bf16_f32 v137, v82, v83\n"
"v_cvt_pk_bf16_f32 v138, v84, v85\n"
"v_cvt_pk_bf16_f32 v139, v86, v87\n"
"s_waitcnt lgkmcnt(4)\n"
"v_mfma_f32_16x16x32_bf16 v[188:191], v[144:147], v[92:95], v[116:119]\n"
"ds_read_b128 v[192:195], v15 offset:2048\n"
"v_exp_f32_e32 v176, v176\n"
"v_exp_f32_e32 v177, v177\n"
"v_exp_f32_e32 v178, v178\n"
"v_mfma_f32_16x16x32_bf16 v[196:199], v[144:147], v[96:99], v[120:123]\n"
"buffer_load_dwordx4 v[76:79], v1, s[4:7], s97 offen offset:1024 sc1\n"
"v_exp_f32_e32 v179, v179\n"
"v_mfma_f32_16x16x32_bf16 v[144:147], v[152:155], v[96:99], v[120:123]\n"
"ds_read_b128 v[200:203], v254 offset:2304\n"
"v_exp_f32_e32 v184, v184\n"
"v_exp_f32_e32 v185, v185\n"
"v_mfma_f32_16x16x32_bf16 v[204:207], v[152:155], v[92:95], v[116:119]\n"
"ds_read_b64_tr_b16 v[152:153], v16 offset:6144\n"
"v_exp_f32_e32 v186, v186\n"
"v_exp_f32_e32 v187, v187\n"
"s_waitcnt lgkmcnt(3)\n"
"v_mfma_f32_16x16x32_bf16 v[148:151], v[88:91], v[128:131], v[148:151]\n"
"buffer_load_dwordx4 v[80:83], v1, s[4:7], s97 offen offset:2048 sc1\n"
"v_cvt_pk_bf16_f32 v208, v176, v177\n"
"v_cvt_pk_bf16_f32 v209, v178, v179\n"
"v_cvt_pk_bf16_f32 v210, v184, v185\n"
"v_mfma_f32_16x16x32_bf16 v[156:159], v[140:143], v[128:131], v[156:159]\n"
"ds_read_b64_tr_b16 v[154:155], v255 offset:6400\n"
"v_cvt_pk_bf16_f32 v211, v186, v187\n"
"v_exp_f32_e32 v180, v180\n"
"v_exp_f32_e32 v181, v181\n"
"v_mfma_f32_16x16x32_bf16 v[124:127], v[172:175], v[128:131], v[124:127]\n"
"ds_read_b64_tr_b16 v[128:129], v255 offset:6144\n"
"v_exp_f32_e32 v182, v182\n"
"v_exp_f32_e32 v183, v183\n"
"v_mfma_f32_16x16x32_bf16 v[160:163], v[172:175], v[136:139], v[160:163]\n"
"buffer_load_dwordx4 v[84:87], v1, s[4:7], s97 offen offset:3072 sc1\n"
"v_exp_f32_e32 v132, v132\n"
"v_exp_f32_e32 v133, v133\n"
"v_mfma_f32_16x16x32_bf16 v[164:167], v[140:143], v[136:139], v[164:167]\n"
"ds_read_b64_tr_b16 v[130:131], v16 offset:6400\n"
"v_exp_f32_e32 v134, v134\n"
"v_exp_f32_e32 v135, v135\n"
"v_mfma_f32_16x16x32_bf16 v[168:171], v[88:91], v[136:139], v[168:171]\n"
"s_waitcnt lgkmcnt(4)\n"
"v_cvt_pk_bf16_f32 v136, v180, v181\n"
"v_cvt_pk_bf16_f32 v137, v182, v183\n"
"v_cvt_pk_bf16_f32 v138, v132, v133\n"
"v_cvt_pk_bf16_f32 v139, v134, v135\n"
"v_mfma_f32_16x16x32_bf16 v[132:135], v[192:195], v[92:95], v[116:119]\n"
"ds_read_b128 v[140:143], v15\n"
"v_exp_f32_e32 v188, v188\n"
"v_exp_f32_e32 v189, v189\n"
"v_exp_f32_e32 v190, v190\n"
"v_mfma_f32_16x16x32_bf16 v[172:175], v[192:195], v[96:99], v[120:123]\n"
"s_add_u32 s97, s56, 0x40000\n"
"buffer_load_dwordx4 v[176:179], v0, s[4:7], s97 offen sc1\n"
"v_exp_f32_e32 v191, v191\n"
"v_mfma_f32_16x16x32_bf16 v[180:183], v[200:203], v[96:99], v[120:123]\n"
"ds_read_b128 v[184:187], v254 offset:256\n"
"v_exp_f32_e32 v204, v204\n"
"v_exp_f32_e32 v205, v205\n"
"v_exp_f32_e32 v206, v206\n"
"v_mfma_f32_16x16x32_bf16 v[192:195], v[200:203], v[92:95], v[116:119]\n"
"buffer_load_dwordx4 v[200:203], v0, s[4:7], s97 offen offset:1024 sc1\n"
"ds_read_b64_tr_b16 v[212:213], v16 offset:4096\n"
"v_exp_f32_e32 v207, v207\n"
"s_waitcnt lgkmcnt(3)\n"
"v_mfma_f32_16x16x32_bf16 v[148:151], v[88:91], v[208:211], v[148:151]\n"
"v_cvt_pk_bf16_f32 v216, v188, v189\n"
"v_cvt_pk_bf16_f32 v217, v190, v191\n"
"v_cvt_pk_bf16_f32 v218, v204, v205\n"
"v_cvt_pk_bf16_f32 v219, v206, v207\n"
"v_mfma_f32_16x16x32_bf16 v[156:159], v[152:155], v[208:211], v[156:159]\n"
"buffer_load_dwordx4 v[188:191], v0, s[4:7], s97 offen offset:2048 sc1\n"
"ds_read_b64_tr_b16 v[214:215], v255 offset:4352\n"
"v_exp_f32_e32 v196, v196\n"
"v_exp_f32_e32 v197, v197\n"
"v_mfma_f32_16x16x32_bf16 v[124:127], v[128:131], v[208:211], v[124:127]\n"
"buffer_load_dwordx4 v[204:207], v0, s[4:7], s97 offen offset:3072 sc1\n"
"v_exp_f32_e32 v198, v198\n"
"v_exp_f32_e32 v199, v199\n"
"v_mfma_f32_16x16x32_bf16 v[160:163], v[128:131], v[136:139], v[160:163]\n"
"ds_read_b64_tr_b16 v[128:129], v255 offset:4096\n"
"v_exp_f32_e32 v144, v144\n"
"v_exp_f32_e32 v145, v145\n"
"v_mfma_f32_16x16x32_bf16 v[164:167], v[152:155], v[136:139], v[164:167]\n"
"buffer_load_dwordx4 v[152:155], v1, s[4:7], s97 offen sc1\n"
"ds_read_b64_tr_b16 v[130:131], v16 offset:4352\n"
"v_exp_f32_e32 v146, v146\n"
"v_exp_f32_e32 v147, v147\n"
"v_mfma_f32_16x16x32_bf16 v[168:171], v[88:91], v[136:139], v[168:171]\n"
"s_waitcnt lgkmcnt(4)\n"
"v_cvt_pk_bf16_f32 v136, v196, v197\n"
"v_cvt_pk_bf16_f32 v137, v198, v199\n"
"v_cvt_pk_bf16_f32 v138, v144, v145\n"
"v_cvt_pk_bf16_f32 v139, v146, v147\n"
"v_mfma_f32_16x16x32_bf16 v[144:147], v[140:143], v[92:95], v[116:119]\n"
"ds_read_b64_tr_b16 v[196:197], v16 offset:2048\n"
"ds_read_b64_tr_b16 v[198:199], v255 offset:2304\n"
"buffer_load_dwordx4 v[208:211], v1, s[4:7], s97 offen offset:1024 sc1\n"
"v_exp_f32_e32 v132, v132\n"
"v_exp_f32_e32 v133, v133\n"
"v_mfma_f32_16x16x32_bf16 v[220:223], v[140:143], v[96:99], v[120:123]\n"
"ds_read_b64_tr_b16 v[140:141], v255 offset:2048\n"
"v_exp_f32_e32 v134, v134\n"
"v_exp_f32_e32 v135, v135\n"
"v_mfma_f32_16x16x32_bf16 v[224:227], v[184:187], v[96:99], v[120:123]\n"
"buffer_load_dwordx4 v[120:123], v1, s[4:7], s97 offen offset:2048 sc1\n"
"v_exp_f32_e32 v192, v192\n"
"v_exp_f32_e32 v193, v193\n"
"v_mfma_f32_16x16x32_bf16 v[228:231], v[184:187], v[92:95], v[116:119]\n"
"ds_read_b64_tr_b16 v[142:143], v16 offset:2304\n"
"ds_read_b64_tr_b16 v[116:117], v16 offset:16384\n"
"v_exp_f32_e32 v194, v194\n"
"v_exp_f32_e32 v195, v195\n"
"s_waitcnt lgkmcnt(5)\n"
"v_mfma_f32_16x16x32_bf16 v[148:151], v[88:91], v[216:219], v[148:151]\n"
"v_cvt_pk_bf16_f32 v184, v132, v133\n"
"v_cvt_pk_bf16_f32 v185, v134, v135\n"
"v_cvt_pk_bf16_f32 v186, v192, v193\n"
"v_mfma_f32_16x16x32_bf16 v[156:159], v[212:215], v[216:219], v[156:159]\n"
"buffer_load_dwordx4 v[132:135], v1, s[4:7], s97 offen offset:3072 sc1\n"
"ds_read_b64_tr_b16 v[118:119], v255 offset:16640\n"
"ds_read_b64_tr_b16 v[192:193], v255 offset:16384\n"
"v_cvt_pk_bf16_f32 v187, v194, v195\n"
"v_exp_f32_e32 v172, v172\n"
"v_exp_f32_e32 v173, v173\n"
"v_exp_f32_e32 v174, v174\n"
"v_mfma_f32_16x16x32_bf16 v[124:127], v[128:131], v[216:219], v[124:127]\n"
"s_add_u32 s97, s57, 0x40000\n"
"buffer_load_dwordx4 v[216:219], v0, s[4:7], s97 offen sc1\n"
"ds_read_b64_tr_b16 v[194:195], v16 offset:16640\n"
"v_exp_f32_e32 v175, v175\n"
"v_mfma_f32_16x16x32_bf16 v[160:163], v[128:131], v[136:139], v[160:163]\n"
"v_exp_f32_e32 v180, v180\n"
"v_add_u32_e32 v248, 0x26400, v0\n"
"v_exp_f32_e32 v181, v181\n"
"v_mfma_f32_16x16x32_bf16 v[164:167], v[212:215], v[136:139], v[164:167]\n"
"buffer_load_dwordx4 v[128:131], v0, s[4:7], s97 offen offset:1024 sc1\n"
"ds_read_b128 v[212:215], v248\n"
"v_exp_f32_e32 v182, v182\n"
"v_exp_f32_e32 v183, v183\n"
"v_mfma_f32_16x16x32_bf16 v[168:171], v[88:91], v[136:139], v[168:171]\n"
"ds_read_b128 v[232:235], v248 offset:1024\n"
"v_cvt_pk_bf16_f32 v136, v172, v173\n"
"v_cvt_pk_bf16_f32 v137, v174, v175\n"
"v_cvt_pk_bf16_f32 v138, v180, v181\n"
"v_cvt_pk_bf16_f32 v139, v182, v183\n"
"s_waitcnt lgkmcnt(6)\n"
"v_mfma_f32_16x16x32_bf16 v[148:151], v[88:91], v[184:187], v[148:151]\n"
"ds_read_b64_tr_b16 v[172:173], v16\n"
"buffer_load_dwordx4 v[180:183], v0, s[4:7], s97 offen offset:2048 sc1\n"
"v_cndmask_b32_e64 v144, v100, v144, s[20:21]\n"
"v_cndmask_b32_e64 v145, v101, v145, s[22:23]\n"
"v_cndmask_b32_e64 v146, v102, v146, s[24:25]\n"
"v_cndmask_b32_e64 v147, v103, v147, s[26:27]\n"
"v_cndmask_b32_e64 v228, v112, v228, s[28:29]\n"
"v_cndmask_b32_e64 v229, v113, v229, s[30:31]\n"
"v_cndmask_b32_e64 v230, v114, v230, s[32:33]\n"
"v_cndmask_b32_e64 v231, v115, v231, s[34:35]\n"
"v_exp_f32_e32 v144, v144\n"
"v_mfma_f32_16x16x32_bf16 v[156:159], v[196:199], v[184:187], v[156:159]\n"
"ds_read_b64_tr_b16 v[174:175], v255 offset:256\n"
"buffer_load_dwordx4 v[100:103], v0, s[4:7], s97 offen offset:3072 sc1\n"
"v_exp_f32_e32 v145, v145\n"
"v_exp_f32_e32 v146, v146\n"
"v_exp_f32_e32 v147, v147\n"
"v_exp_f32_e32 v228, v228\n"
"v_mfma_f32_16x16x32_bf16 v[124:127], v[140:143], v[184:187], v[124:127]\n"
"buffer_load_dwordx4 v[184:187], v1, s[4:7], s97 offen sc1\n"
"v_exp_f32_e32 v229, v229\n"
"v_exp_f32_e32 v230, v230\n"
"v_exp_f32_e32 v231, v231\n"
"v_cvt_pk_bf16_f32 v112, v144, v145\n"
"v_cvt_pk_bf16_f32 v113, v146, v147\n"
"v_cvt_pk_bf16_f32 v114, v228, v229\n"
"v_mfma_f32_16x16x32_bf16 v[160:163], v[140:143], v[136:139], v[160:163]\n"
"ds_read_b64_tr_b16 v[140:141], v255\n"
"v_cvt_pk_bf16_f32 v115, v230, v231\n"
"v_cndmask_b32_e64 v220, v104, v220, s[36:37]\n"
"v_cndmask_b32_e64 v221, v105, v221, s[38:39]\n"
"v_cndmask_b32_e64 v222, v106, v222, s[40:41]\n"
"v_cndmask_b32_e64 v223, v107, v223, s[42:43]\n"
"v_cndmask_b32_e64 v224, v108, v224, s[44:45]\n"
"v_cndmask_b32_e64 v225, v109, v225, s[46:47]\n"
"v_cndmask_b32_e64 v226, v110, v226, s[48:49]\n"
"v_cndmask_b32_e64 v227, v111, v227, s[50:51]\n"
"v_mfma_f32_16x16x32_bf16 v[164:167], v[196:199], v[136:139], v[164:167]\n"
"buffer_load_dwordx4 v[104:107], v1, s[4:7], s97 offen offset:1024 sc1\n"
"ds_read_b64_tr_b16 v[142:143], v16 offset:256\n"
"buffer_load_dwordx4 v[108:111], v1, s[4:7], s97 offen offset:2048 sc1\n"
"v_exp_f32_e32 v220, v220\n"
"v_exp_f32_e32 v221, v221\n"
"v_exp_f32_e32 v222, v222\n"
"v_exp_f32_e32 v223, v223\n"
"v_exp_f32_e32 v224, v224\n"
"v_mfma_f32_16x16x32_bf16 v[168:171], v[88:91], v[136:139], v[168:171]\n"
"s_waitcnt lgkmcnt(0)\n"
"v_exp_f32_e32 v225, v225\n"
"v_exp_f32_e32 v226, v226\n"
"v_exp_f32_e32 v227, v227\n"
"v_cvt_pk_bf16_f32 v136, v220, v221\n"
"v_cvt_pk_bf16_f32 v137, v222, v223\n"
"v_cvt_pk_bf16_f32 v138, v224, v225\n"
"v_cvt_pk_bf16_f32 v139, v226, v227\n"
"v_mfma_f32_16x16x32_bf16 v[148:151], v[88:91], v[112:115], v[148:151]\n"
"v_and_b32_e32 v212, v112, v212\n"
"v_and_b32_e32 v213, v113, v213\n"
"v_and_b32_e32 v214, v114, v214\n"
"v_and_b32_e32 v215, v115, v215\n"
"v_mfma_f32_16x16x32_bf16 v[168:171], v[88:91], v[136:139], v[168:171]\n"
"v_xor_b32_e32 v144, v112, v212\n"
"v_xor_b32_e32 v145, v113, v213\n"
"v_xor_b32_e32 v146, v114, v214\n"
"v_xor_b32_e32 v147, v115, v215\n"
"v_mfma_f32_16x16x32_bf16 v[156:159], v[172:175], v[212:215], v[156:159]\n"
"v_and_b32_e32 v232, v136, v232\n"
"v_and_b32_e32 v233, v137, v233\n"
"v_mfma_f32_16x16x32_bf16 v[124:127], v[140:143], v[212:215], v[124:127]\n"
"v_and_b32_e32 v234, v138, v234\n"
"v_and_b32_e32 v235, v139, v235\n"
"v_mfma_f32_16x16x32_bf16 v[156:159], v[116:119], v[144:147], v[156:159]\n"
"v_xor_b32_e32 v112, v136, v232\n"
"v_xor_b32_e32 v113, v137, v233\n"
"v_mfma_f32_16x16x32_bf16 v[124:127], v[192:195], v[144:147], v[124:127]\n"
"v_xor_b32_e32 v114, v138, v234\n"
"v_xor_b32_e32 v115, v139, v235\n"
"v_mfma_f32_16x16x32_bf16 v[164:167], v[172:175], v[232:235], v[164:167]\n"
"buffer_load_dwordx4 v[136:139], v1, s[4:7], s97 offen offset:3072 sc1\n"
"s_add_u32 s97, s58, 0x40000\n"
"buffer_load_dwordx4 v[144:147], v0, s[4:7], s97 offen sc1\n"
"v_mfma_f32_16x16x32_bf16 v[160:163], v[140:143], v[232:235], v[160:163]\n"
"buffer_load_dwordx4 v[140:143], v0, s[4:7], s97 offen offset:1024 sc1\n"
"v_mfma_f32_16x16x32_bf16 v[164:167], v[116:119], v[112:115], v[164:167]\n"
"buffer_load_dwordx4 v[116:119], v0, s[4:7], s97 offen offset:2048 sc1\n"
"v_mfma_f32_16x16x32_bf16 v[160:163], v[192:195], v[112:115], v[160:163]\n"
"buffer_load_dwordx4 v[112:115], v0, s[4:7], s97 offen offset:3072 sc1\n"
"v_max_f32_e32 v248, v148, v168\n"
"v_rcp_f32_e32 v148, v148\n"
"v_rcp_f32_e32 v168, v168\n"
"s_nop 0\n"
"v_max3_f32 v248, v248, v148, v168\n"
"v_cmp_nlt_f32_e32 vcc, v248, v252\n"
"s_cbranch_vccnz .Lmk_b29\n"
".Lmk_k34:\n"
"v_mul_f32_e32 v156, v156, v148\n"
"v_mul_f32_e32 v157, v157, v148\n"
"v_mul_f32_e32 v158, v158, v148\n"
"v_mul_f32_e32 v159, v159, v148\n"
"v_mul_f32_e32 v124, v124, v148\n"
"v_mul_f32_e32 v125, v125, v148\n"
"v_mul_f32_e32 v126, v126, v148\n"
"v_mul_f32_e32 v127, v127, v148\n"
"v_cvt_pk_bf16_f32 v88, v156, v157\n"
"v_cvt_pk_bf16_f32 v89, v158, v159\n"
"v_cvt_pk_bf16_f32 v90, v124, v125\n"
"v_cvt_pk_bf16_f32 v91, v126, v127\n"
"buffer_store_dwordx4 v[88:91], v0, s[4:7], s60 offen sc1\n"
"s_add_u32 s98, s60, 0x8000\n"
"v_mul_f32_e32 v164, v164, v168\n"
"v_mul_f32_e32 v165, v165, v168\n"
"v_mul_f32_e32 v166, v166, v168\n"
"v_mul_f32_e32 v167, v167, v168\n"
"v_mul_f32_e32 v160, v160, v168\n"
"v_mul_f32_e32 v161, v161, v168\n"
"v_mul_f32_e32 v162, v162, v168\n"
"v_mul_f32_e32 v163, v163, v168\n"
"v_cvt_pk_bf16_f32 v88, v164, v165\n"
"v_cvt_pk_bf16_f32 v89, v166, v167\n"
"v_cvt_pk_bf16_f32 v90, v160, v161\n"
"v_cvt_pk_bf16_f32 v91, v162, v163\n"
"buffer_store_dwordx4 v[88:91], v0, s[4:7], s98 offen sc1\n"
";\n"
"s_waitcnt vmcnt(20)\n"
"; @A G2 b.0\n"
"s_nop 0\n"
"v_mfma_f32_16x16x32_bf16 v[88:91], a[128:131], v[176:179], 0\n"
"v_mfma_f32_16x16x32_bf16 v[88:91], a[132:135], v[200:203], v[88:91]\n"
"s_waitcnt vmcnt(18)\n"
"v_mfma_f32_16x16x32_bf16 v[88:91], a[136:139], v[188:191], v[88:91]\n"
"buffer_load_dwordx4 v[92:95], v1, s[4:7], s97 offen sc1\n"
"v_mfma_f32_16x16x32_bf16 v[88:91], a[140:143], v[204:207], v[88:91]\n"
"s_waitcnt vmcnt(17)\n"
"v_mfma_f32_16x16x32_bf16 v[88:91], a[144:147], v[152:155], v[88:91]\n"
"v_mfma_f32_16x16x32_bf16 v[88:91], a[148:151], v[208:211], v[88:91]\n"
"s_waitcnt vmcnt(15)\n"
"v_mfma_f32_16x16x32_bf16 v[88:91], a[152:155], v[120:123], v[88:91]\n"
"buffer_load_dwordx4 v[96:99], v1, s[4:7], s97 offen offset:1024 sc1\n"
"v_mfma_f32_16x16x32_bf16 v[88:91], a[156:159], v[132:135], v[88:91]\n"
"v_mfma_f32_16x16x32_bf16 v[124:127], a[188:191], v[132:135], 0\n"
"v_mfma_f32_16x16x32_bf16 v[124:127], a[184:187], v[120:123], v[124:127]\n"
"v_mfma_f32_16x16x32_bf16 v[124:127], a[180:183], v[208:211], v[124:127]\n"
"buffer_load_dwordx4 v[148:151], v1, s[4:7], s97 offen offset:2048 sc1\n"
"v_mfma_f32_16x16x32_bf16 v[124:127], a[176:179], v[152:155], v[124:127]\n"
"v_mfma_f32_16x16x32_bf16 v[124:127], a[172:175], v[204:207], v[124:127]\n"
"s_nop 1\n"
"v_cmp_u_f32_e32 vcc, v88, v88\n"
"v_mfma_f32_16x16x32_bf16 v[124:127], a[168:171], v[188:191], v[124:127]\n"
"s_cbranch_vccnz .Lmk_p35s\n"
".Lmk_p35r:\n"
"v_mfma_f32_16x16x32_bf16 v[124:127], a[164:167], v[200:203], v[124:127]\n"
"ds_write_b128 v3, v[88:91]\n"
"v_mfma_f32_16x16x32_bf16 v[124:127], a[160:163], v[176:179], v[124:127]\n"
"buffer_load_dwordx4 v[88:91], v1, s[4:7], s97 offen offset:3072 sc1\n"
"v_mfma_f32_16x16x32_bf16 v[156:159], a[192:195], v[176:179], 0\n"
"v_mfma_f32_16x16x32_bf16 v[156:159], a[196:199], v[200:203], v[156:159]\n"
"s_add_u32 s98, s59, 0x40000\n"
"v_mfma_f32_16x16x32_bf16 v[156:159], a[200:203], v[188:191], v[156:159]\n"
"v_mfma_f32_16x16x32_bf16 v[156:159], a[204:207], v[204:207], v[156:159]\n"
"buffer_load_dwordx4 v[160:163], v0, s[4:7], s98 offen sc1\n"
"v_mfma_f32_16x16x32_bf16 v[156:159], a[208:211], v[152:155], v[156:159]\n"
"v_mfma_f32_16x16x32_bf16 v[156:159], a[212:215], v[208:211], v[156:159]\n"
"ds_write_b128 v3, v[124:127] offset:1024\n"
"v_mfma_f32_16x16x32_bf16 v[156:159], a[216:219], v[120:123], v[156:159]\n"
"v_mfma_f32_16x16x32_bf16 v[156:159], a[220:223], v[132:135], v[156:159]\n"
"buffer_load_dwordx4 v[124:127], v0, s[4:7], s98 offen offset:1024 sc1\n"
"v_mfma_f32_16x16x32_bf16 v[164:167], a[252:255], v[132:135], 0\n"
"v_mfma_f32_16x16x32_bf16 v[164:167], a[248:251], v[120:123], v[164:167]\n"
"v_mfma_f32_16x16x32_bf16 v[164:167], a[244:247], v[208:211], v[164:167]\n"
"v_mfma_f32_16x16x32_bf16 v[164:167], a[240:243], v[152:155], v[164:167]\n"
"buffer_load_dwordx4 v[120:123], v0, s[4:7], s98 offen offset:2048 sc1\n"
"v_mfma_f32_16x16x32_bf16 v[164:167], a[236:239], v[204:207], v[164:167]\n"
"v_mfma_f32_16x16x32_bf16 v[164:167], a[232:235], v[188:191], v[164:167]\n"
"ds_write_b128 v3, v[156:159] offset:2048\n"
"v_mfma_f32_16x16x32_bf16 v[164:167], a[228:231], v[200:203], v[164:167]\n"
"v_mfma_f32_16x16x32_bf16 v[164:167], a[224:227], v[176:179], v[164:167]\n"
"s_waitcnt vmcnt(17)\n"
"buffer_load_dwordx4 v[132:135], v0, s[4:7], s98 offen offset:3072 sc1\n"
"; @A G2 b.1\n"
"v_mfma_f32_16x16x32_bf16 v[152:155], a[128:131], v[216:219], 0\n"
"v_mfma_f32_16x16x32_bf16 v[152:155], a[132:135], v[128:131], v[152:155]\n"
"v_mfma_f32_16x16x32_bf16 v[152:155], a[136:139], v[180:183], v[152:155]\n"
"v_mfma_f32_16x16x32_bf16 v[152:155], a[140:143], v[100:103], v[152:155]\n"
"s_waitcnt vmcnt(14)\n"
"buffer_load_dwordx4 v[156:159], v1, s[4:7], s98 offen sc1\n"
"v_mfma_f32_16x16x32_bf16 v[152:155], a[144:147], v[184:187], v[152:155]\n"
"v_mfma_f32_16x16x32_bf16 v[152:155], a[148:151], v[104:107], v[152:155]\n"
"ds_write_b128 v3, v[164:167] offset:3072\n"
"v_mfma_f32_16x16x32_bf16 v[152:155], a[152:155], v[108:111], v[152:155]\n"
"v_mfma_f32_16x16x32_bf16 v[152:155], a[156:159], v[136:139], v[152:155]\n"
"buffer_load_dwordx4 v[164:167], v1, s[4:7], s98 offen offset:1024 sc1\n"
"v_mfma_f32_16x16x32_bf16 v[168:171], a[188:191], v[136:139], 0\n"
"v_mfma_f32_16x16x32_bf16 v[168:171], a[184:187], v[108:111], v[168:171]\n"
"v_mfma_f32_16x16x32_bf16 v[168:171], a[180:183], v[104:107], v[168:171]\n"
"v_mfma_f32_16x16x32_bf16 v[168:171], a[176:179], v[184:187], v[168:171]\n"
"buffer_load_dwordx4 v[172:175], v1, s[4:7], s98 offen offset:2048 sc1\n"
"v_mfma_f32_16x16x32_bf16 v[168:171], a[172:175], v[100:103], v[168:171]\n"
"s_nop 0\n"
"v_cmp_u_f32_e32 vcc, v152, v152\n"
"v_mfma_f32_16x16x32_bf16 v[168:171], a[168:171], v[180:183], v[168:171]\n"
"s_cbranch_vccnz .Lmk_p36s\n"
".Lmk_p36r:\n"
"v_mfma_f32_16x16x32_bf16 v[168:171], a[164:167], v[128:131], v[168:171]\n"
"ds_write_b128 v3, v[152:155] offset:6144\n"
"v_mfma_f32_16x16x32_bf16 v[168:171], a[160:163], v[216:219], v[168:171]\n"
"buffer_load_dwordx4 v[152:155], v1, s[4:7], s98 offen offset:3072 sc1\n"
"v_mfma_f32_16x16x32_bf16 v[176:179], a[192:195], v[216:219], 0\n"
"v_mfma_f32_16x16x32_bf16 v[176:179], a[196:199], v[128:131], v[176:179]\n"
"v_mfma_f32_16x16x32_bf16 v[176:179], a[200:203], v[180:183], v[176:179]\n"
"v_mfma_f32_16x16x32_bf16 v[176:179], a[204:207], v[100:103], v[176:179]\n"
"v_mfma_f32_16x16x32_bf16 v[176:179], a[208:211], v[184:187], v[176:179]\n"
"ds_read_b128 v[188:191], v2 offset:8192\n"
"ds_read_b128 v[192:195], v2 offset:9216\n"
"v_mfma_f32_16x16x32_bf16 v[176:179], a[212:215], v[104:107], v[176:179]\n"
"ds_write_b128 v3, v[168:171] offset:7168\n"
"v_mfma_f32_16x16x32_bf16 v[176:179], a[216:219], v[108:111], v[176:179]\n"
"v_mfma_f32_16x16x32_bf16 v[176:179], a[220:223], v[136:139], v[176:179]\n"
"v_mfma_f32_16x16x32_bf16 v[168:171], a[252:255], v[136:139], 0\n"
"s_mov_b32 s98, .Lmk_b0-.Lmk_g39\n"
"s_getpc_b64 s[84:85]\n"
".Lmk_g39:\n"
"s_add_u32 s84, s84, s98\n"
"s_addc_u32 s85, s85, 0\n"
"s_and_b32 s84, s84, -16\n"
"v_mfma_f32_16x16x32_bf16 v[168:171], a[248:251], v[108:111], v[168:171]\n"
"s_and_b32 s85, s85, 0xffff\n"
"s_sub_u32 s98, .Lmk_end-.Lmk_g39, s98\n"
"s_min_u32 s86, s98, 0x3000\n"
"s_mov_b32 s87, 0x20000\n"
"v_mfma_f32_16x16x32_bf16 v[168:171], a[244:247], v[104:107], v[168:171]\n"
"s_mov_b32 m0, 0x27800\n"
"s_nop 0\n"
"buffer_load_dwordx4 v0, s[84:87], s99 offen lds\n"
"v_mfma_f32_16x16x32_bf16 v[168:171], a[240:243], v[184:187], v[168:171]\n"
"s_waitcnt lgkmcnt(1)\n"
"v_lshlrev_b32_e32 v255, 16, v188\n"
"v_and_b32_e32 v254, 0xffff0000, v188\n"
"v_mfma_f32_16x16x32_bf16 v[168:171], a[236:239], v[100:103], v[168:171]\n"
"v_lshlrev_b32_e32 v253, 16, v189\n"
"v_and_b32_e32 v252, 0xffff0000, v189\n"
"v_mfma_f32_16x16x32_bf16 v[168:171], a[232:235], v[180:183], v[168:171]\n"
"ds_write_b128 v3, v[176:179] offset:8192\n"
"v_mfma_f32_16x16x32_bf16 v[168:171], a[228:231], v[128:131], v[168:171]\n"
"v_lshlrev_b32_e32 v251, 16, v190\n"
"v_and_b32_e32 v250, 0xffff0000, v190\n"
"v_mfma_f32_16x16x32_bf16 v[168:171], a[224:227], v[216:219], v[168:171]\n"
"s_waitcnt vmcnt(15)\n"
"v_lshlrev_b32_e32 v249, 16, v191\n"
"v_and_b32_e32 v248, 0xffff0000, v191\n"
"; @A G2 b.2\n"
"v_mfma_f32_16x16x32_bf16 v[100:103], a[128:131], v[144:147], 0\n"
"v_lshlrev_b32_e32 v247, 16, v192\n"
"v_and_b32_e32 v246, 0xffff0000, v192\n"
"v_mfma_f32_16x16x32_bf16 v[100:103], a[132:135], v[140:143], v[100:103]\n"
"v_lshlrev_b32_e32 v245, 16, v193\n"
"v_and_b32_e32 v244, 0xffff0000, v193\n"
"v_mfma_f32_16x16x32_bf16 v[100:103], a[136:139], v[116:119], v[100:103]\n"
"v_lshlrev_b32_e32 v243, 16, v194\n"
"v_and_b32_e32 v242, 0xffff0000, v194\n"
"v_mfma_f32_16x16x32_bf16 v[100:103], a[140:143], v[112:115], v[100:103]\n"
"s_waitcnt vmcnt(9)\n"
"v_lshlrev_b32_e32 v241, 16, v195\n"
"v_and_b32_e32 v240, 0xffff0000, v195\n"
"v_mfma_f32_16x16x32_bf16 v[100:103], a[144:147], v[92:95], v[100:103]\n"
"v_mfma_f32_16x16x32_bf16 v[100:103], a[148:151], v[96:99], v[100:103]\n"
"ds_write_b128 v3, v[168:171] offset:9216\n"
"v_mfma_f32_16x16x32_bf16 v[100:103], a[152:155], v[148:151], v[100:103]\n"
"v_mfma_f32_16x16x32_bf16 v[100:103], a[156:159], v[88:91], v[100:103]\n"
"v_mfma_f32_16x16x32_bf16 v[104:107], a[188:191], v[88:91], 0\n"
"v_mfma_f32_16x16x32_bf16 v[104:107], a[184:187], v[148:151], v[104:107]\n"
"s_waitcnt lgkmcnt(0)\n"
"s_barrier\n"
"v_mfma_f32_16x16x32_bf16 v[104:107], a[180:183], v[96:99], v[104:107]\n"
"ds_read_b128 v[108:111], v4\n"
"ds_read_b128 v[128:131], v5\n"
"v_mfma_f32_16x16x32_bf16 v[104:107], a[176:179], v[92:95], v[104:107]\n"
"v_mfma_f32_16x16x32_bf16 v[104:107], a[172:175], v[112:115], v[104:107]\n"
"s_add_u32 s98, s56, 0\n"
"v_cmp_u_f32_e32 vcc, v100, v100\n"
"v_mfma_f32_16x16x32_bf16 v[104:107], a[168:171], v[116:119], v[104:107]\n"
"s_cbranch_vccnz .Lmk_p37s\n"
".Lmk_p37r:\n"
"v_mfma_f32_16x16x32_bf16 v[104:107], a[164:167], v[140:143], v[104:107]\n"
"ds_write_b128 v3, v[100:103] offset:12288\n"
"v_mfma_f32_16x16x32_bf16 v[104:107], a[160:163], v[144:147], v[104:107]\n"
"buffer_load_dwordx4 v[100:103], v0, s[4:7], s98 offen sc1\n"
"v_mfma_f32_16x16x32_bf16 v[136:139], a[192:195], v[144:147], 0\n"
"ds_read_b128 v[168:171], v4 offset:1024\n"
"ds_read_b128 v[176:179], v5 offset:1024\n"
"v_mfma_f32_16x16x32_bf16 v[136:139], a[196:199], v[140:143], v[136:139]\n"
"s_waitcnt lgkmcnt(3)\n"
"v_add_f32_e32 v108, v108, v255\n"
"v_add_f32_e32 v109, v109, v254\n"
"v_mfma_f32_16x16x32_bf16 v[136:139], a[200:203], v[116:119], v[136:139]\n"
"buffer_load_dwordx4 v[180:183], v0, s[4:7], s98 offen offset:1024 sc1\n"
"v_mfma_f32_16x16x32_bf16 v[136:139], a[204:207], v[112:115], v[136:139]\n"
"v_add_f32_e32 v110, v110, v253\n"
"v_add_f32_e32 v111, v111, v252\n"
"v_mfma_f32_16x16x32_bf16 v[136:139], a[208:211], v[92:95], v[136:139]\n"
"v_add_f32_e32 v108, v108, v128\n"
"v_add_f32_e32 v109, v109, v129\n"
"v_mfma_f32_16x16x32_bf16 v[136:139], a[212:215], v[96:99], v[136:139]\n"
"ds_write_b128 v3, v[104:107] offset:13312\n"
"v_mfma_f32_16x16x32_bf16 v[136:139], a[216:219], v[148:151], v[136:139]\n"
"buffer_load_dwordx4 v[104:107], v0, s[4:7], s98 offen offset:2048 sc1\n"
"v_mfma_f32_16x16x32_bf16 v[136:139], a[220:223], v[88:91], v[136:139]\n"
"ds_read_b128 v[184:187], v4 offset:2048\n"
"ds_read_b128 v[188:191], v5 offset:2048\n"
"v_mfma_f32_16x16x32_bf16 v[192:195], a[252:255], v[88:91], 0\n"
"s_waitcnt lgkmcnt(3)\n"
"v_add_f32_e32 v110, v110, v130\n"
"v_add_f32_e32 v111, v111, v131\n"
"v_mfma_f32_16x16x32_bf16 v[192:195], a[248:251], v[148:151], v[192:195]\n"
"buffer_load_dwordx4 v[88:91], v0, s[4:7], s98 offen offset:3072 sc1\n"
"v_mfma_f32_16x16x32_bf16 v[192:195], a[244:247], v[96:99], v[192:195]\n"
"v_add_f32_e32 v168, v168, v251\n"
"v_add_f32_e32 v169, v169, v250\n"
"v_mfma_f32_16x16x32_bf16 v[192:195], a[240:243], v[92:95], v[192:195]\n"
"v_add_f32_e32 v170, v170, v249\n"
"v_add_f32_e32 v171, v171, v248\n"
"v_mfma_f32_16x16x32_bf16 v[192:195], a[236:239], v[112:115], v[192:195]\n"
"buffer_load_dwordx4 v[92:95], v1, s[4:7], s98 offen sc1\n"
"v_mfma_f32_16x16x32_bf16 v[192:195], a[232:235], v[116:119], v[192:195]\n"
"ds_write_b128 v3, v[136:139] offset:14336\n"
"v_mfma_f32_16x16x32_bf16 v[192:195], a[228:231], v[140:143], v[192:195]\n"
"ds_read_b128 v[96:99], v4 offset:3072\n"
"ds_read_b128 v[112:115], v5 offset:3072\n"
"v_mfma_f32_16x16x32_bf16 v[192:195], a[224:227], v[144:147], v[192:195]\n"
"s_waitcnt vmcnt(10)\n"
"buffer_load_dwordx4 v[116:119], v1, s[4:7], s98 offen offset:1024 sc1\n"
"; @A G2 b.3\n"
"v_mfma_f32_16x16x32_bf16 v[128:131], a[128:131], v[160:163], v[108:111]\n"
"v_add_f32_e32 v168, v168, v176\n"
"v_add_f32_e32 v169, v169, v177\n"
"v_mfma_f32_16x16x32_bf16 v[128:131], a[132:135], v[124:127], v[128:131]\n"
"s_waitcnt lgkmcnt(3)\n"
"v_add_f32_e32 v170, v170, v178\n"
"v_add_f32_e32 v171, v171, v179\n"
"v_mfma_f32_16x16x32_bf16 v[128:131], a[136:139], v[120:123], v[128:131]\n"
"buffer_load_dwordx4 v[136:139], v1, s[4:7], s98 offen offset:2048 sc1\n"
"v_mfma_f32_16x16x32_bf16 v[128:131], a[140:143], v[132:135], v[128:131]\n"
"s_waitcnt vmcnt(8)\n"
"v_add_f32_e32 v184, v184, v247\n"
"v_add_f32_e32 v185, v185, v246\n"
"v_mfma_f32_16x16x32_bf16 v[128:131], a[144:147], v[156:159], v[128:131]\n"
"v_add_f32_e32 v186, v186, v245\n"
"v_add_f32_e32 v187, v187, v244\n"
"v_mfma_f32_16x16x32_bf16 v[128:131], a[148:151], v[164:167], v[128:131]\n"
"ds_write_b128 v3, v[192:195] offset:15360\n"
"v_mfma_f32_16x16x32_bf16 v[128:131], a[152:155], v[172:175], v[128:131]\n"
"buffer_load_dwordx4 v[140:143], v1, s[4:7], s98 offen offset:3072 sc1\n"
"v_mfma_f32_16x16x32_bf16 v[128:131], a[156:159], v[152:155], v[128:131]\n"
"s_add_u32 s98, s57, 0\n"
"v_add_f32_e32 v184, v184, v188\n"
"v_add_f32_e32 v185, v185, v189\n"
"v_mfma_f32_16x16x32_bf16 v[144:147], a[188:191], v[152:155], v[168:171]\n"
"s_waitcnt lgkmcnt(1)\n"
"v_add_f32_e32 v186, v186, v190\n"
"v_add_f32_e32 v187, v187, v191\n"
"v_mfma_f32_16x16x32_bf16 v[144:147], a[184:187], v[172:175], v[144:147]\n"
"s_waitcnt lgkmcnt(0)\n"
"s_barrier\n"
"v_mfma_f32_16x16x32_bf16 v[144:147], a[180:183], v[164:167], v[144:147]\n"
"buffer_load_dwordx4 v[148:151], v0, s[4:7], s98 offen sc1\n"
"v_mfma_f32_16x16x32_bf16 v[144:147], a[176:179], v[156:159], v[144:147]\n"
"v_add_f32_e32 v96, v96, v243\n"
"v_add_f32_e32 v97, v97, v242\n"
"v_mfma_f32_16x16x32_bf16 v[144:147], a[172:175], v[132:135], v[144:147]\n"
"v_cmp_u_f32_e32 vcc, v128, v128\n"
"v_add_f32_e32 v98, v98, v241\n"
"v_mfma_f32_16x16x32_bf16 v[144:147], a[168:171], v[120:123], v[144:147]\n"
"s_cbranch_vccnz .Lmk_p38s\n"
".Lmk_p38r:\n"
"v_add_f32_e32 v99, v99, v240\n"
"v_mfma_f32_16x16x32_bf16 v[144:147], a[164:167], v[124:127], v[144:147]\n"
"buffer_load_dwordx4 v[108:111], v0, s[4:7], s98 offen offset:1024 sc1\n"
"v_mfma_f32_16x16x32_bf16 v[144:147], a[160:163], v[160:163], v[144:147]\n"
"v_add_f32_e32 v96, v96, v112\n"
"v_add_f32_e32 v97, v97, v113\n"
"v_mfma_f32_16x16x32_bf16 v[184:187], a[192:195], v[160:163], v[184:187]\n"
"v_add_f32_e32 v98, v98, v114\n"
"v_add_f32_e32 v99, v99, v115\n"
"v_mfma_f32_16x16x32_bf16 v[184:187], a[196:199], v[124:127], v[184:187]\n"
"buffer_load_dwordx4 v[112:115], v0, s[4:7], s98 offen offset:2048 sc1\n"
"v_mfma_f32_16x16x32_bf16 v[184:187], a[200:203], v[120:123], v[184:187]\n"
"ds_read_b128 v[168:171], v6\n"
"ds_read_b128 v[176:179], v6 offset:1024\n"
"v_mfma_f32_16x16x32_bf16 v[184:187], a[204:207], v[132:135], v[184:187]\n"
"ds_read_b128 v[188:191], v6 offset:2048\n"
"ds_read_b128 v[192:195], v6 offset:3072\n"
"v_mfma_f32_16x16x32_bf16 v[184:187], a[208:211], v[156:159], v[184:187]\n"
"buffer_load_dwordx4 v[196:199], v0, s[4:7], s98 offen offset:3072 sc1\n"
"v_mfma_f32_16x16x32_bf16 v[184:187], a[212:215], v[164:167], v[184:187]\n"
"v_mfma_f32_16x16x32_bf16 v[184:187], a[216:219], v[172:175], v[184:187]\n"
"v_mfma_f32_16x16x32_bf16 v[184:187], a[220:223], v[152:155], v[184:187]\n"
"buffer_load_dwordx4 v[200:203], v1, s[4:7], s98 offen sc1\n"
"v_mfma_f32_16x16x32_bf16 v[96:99], a[252:255], v[152:155], v[96:99]\n"
"s_waitcnt lgkmcnt(0)\n"
"v_add_f32_e32 v168, v128, v168\n"
"v_add_f32_e32 v169, v129, v169\n"
"v_mfma_f32_16x16x32_bf16 v[96:99], a[248:251], v[172:175], v[96:99]\n"
"v_add_f32_e32 v170, v130, v170\n"
"v_add_f32_e32 v171, v131, v171\n"
"v_mfma_f32_16x16x32_bf16 v[96:99], a[244:247], v[164:167], v[96:99]\n"
"buffer_load_dwordx4 v[128:131], v1, s[4:7], s98 offen offset:1024 sc1\n"
"v_mfma_f32_16x16x32_bf16 v[96:99], a[240:243], v[156:159], v[96:99]\n"
"v_add_f32_e32 v176, v144, v176\n"
"v_add_f32_e32 v177, v145, v177\n"
"v_mfma_f32_16x16x32_bf16 v[96:99], a[236:239], v[132:135], v[96:99]\n"
"v_add_f32_e32 v178, v146, v178\n"
"v_add_f32_e32 v179, v147, v179\n"
"v_mfma_f32_16x16x32_bf16 v[96:99], a[232:235], v[120:123], v[96:99]\n"
"buffer_load_dwordx4 v[120:123], v1, s[4:7], s98 offen offset:2048 sc1\n"
"v_mfma_f32_16x16x32_bf16 v[96:99], a[228:231], v[124:127], v[96:99]\n"
"v_cvt_pk_bf16_f32 v124, v168, v169\n"
"v_cvt_pk_bf16_f32 v125, v170, v171\n"
"v_mfma_f32_16x16x32_bf16 v[96:99], a[224:227], v[160:163], v[96:99]\n"
"s_add_u32 s97, s61, 0x440000\n"
"v_cvt_pk_bf16_f32 v126, v176, v177\n"
"v_cvt_pk_bf16_f32 v127, v178, v179\n"
"buffer_store_dwordx4 v[124:127], v0, s[4:7], s97 offen sc1\n"
"ds_write_b128 v2, v[124:127] offset:8192\n"
"buffer_load_dwordx4 v[132:135], v1, s[4:7], s98 offen offset:3072 sc1\n"
"; @A fin b\n"
"s_add_u32 s98, s61, 0x440000\n"
"v_add_f32_e32 v188, v184, v188\n"
"v_add_f32_e32 v189, v185, v189\n"
"v_add_f32_e32 v190, v186, v190\n"
"v_add_f32_e32 v191, v187, v191\n"
"v_add_f32_e32 v192, v96, v192\n"
"v_add_f32_e32 v193, v97, v193\n"
"v_add_f32_e32 v194, v98, v194\n"
"v_add_f32_e32 v195, v99, v195\n"
"v_cvt_pk_bf16_f32 v96, v188, v189\n"
"v_cvt_pk_bf16_f32 v97, v190, v191\n"
"v_cvt_pk_bf16_f32 v98, v192, v193\n"
"v_cvt_pk_bf16_f32 v99, v194, v195\n"
"buffer_store_dwordx4 v[96:99], v0, s[4:7], s98 offen offset:1024 sc1\n"
"s_waitcnt vmcnt(16)\n"
"; @A G2 a.0\n"
"v_mfma_f32_16x16x32_bf16 v[144:147], a[128:131], v[100:103], 0\n"
"s_add_u32 s98, s58, 0\n"
"buffer_load_dwordx4 v[152:155], v0, s[4:7], s98 offen sc1\n"
"v_mfma_f32_16x16x32_bf16 v[144:147], a[132:135], v[180:183], v[144:147]\n"
"s_waitcnt vmcnt(15)\n"
"ds_write_b128 v2, v[96:99] offset:9216\n"
"v_mfma_f32_16x16x32_bf16 v[144:147], a[136:139], v[104:107], v[144:147]\n"
"v_mov_b32_e32 v156, 0x3f803f80\n"
"v_mov_b32_e32 v157, 0x3f803f80\n"
"v_mfma_f32_16x16x32_bf16 v[144:147], a[140:143], v[88:91], v[144:147]\n"
"s_waitcnt vmcnt(13)\n"
"v_mov_b32_e32 v158, 0x3f803f80\n"
"v_mov_b32_e32 v159, 0x3f803f80\n"
"v_mfma_f32_16x16x32_bf16 v[144:147], a[144:147], v[92:95], v[144:147]\n"
"buffer_load_dwordx4 v[160:163], v0, s[4:7], s98 offen offset:1024 sc1\n"
"v_mfma_f32_16x16x32_bf16 v[164:167], v[156:159], v[124:127], 0\n"
"v_mfma_f32_16x16x32_bf16 v[164:167], v[156:159], v[96:99], v[164:167]\n"
"v_mfma_f32_16x16x32_bf16 v[156:159], v[124:127], v[124:127], 0\n"
"v_mfma_f32_16x16x32_bf16 v[156:159], v[96:99], v[96:99], v[156:159]\n"
"v_mfma_f32_16x16x32_bf16 v[144:147], a[148:151], v[116:119], v[144:147]\n"
"s_waitcnt vmcnt(12)\n"
"v_mfma_f32_16x16x32_bf16 v[144:147], a[152:155], v[136:139], v[144:147]\n"
"v_mfma_f32_16x16x32_bf16 v[144:147], a[156:159], v[140:143], v[144:147]\n"
"v_mfma_f32_16x16x32_bf16 v[96:99], a[188:191], v[140:143], 0\n"
"buffer_load_dwordx4 v[124:127], v0, s[4:7], s98 offen offset:2048 sc1\n"
"v_mfma_f32_16x16x32_bf16 v[96:99], a[184:187], v[136:139], v[96:99]\n"
"s_mov_b32 s96, 0xaaaaaaaa\n"
"s_mov_b32 s97, 0xaaaaaaaa\n"
"v_mfma_f32_16x16x32_bf16 v[96:99], a[180:183], v[116:119], v[96:99]\n"
"v_mfma_f32_16x16x32_bf16 v[96:99], a[176:179], v[92:95], v[96:99]\n"
"v_mfma_f32_16x16x32_bf16 v[96:99], a[172:175], v[88:91], v[96:99]\n"
"v_cmp_u_f32_e32 vcc, v144, v144\n"
"v_cndmask_b32_e64 v156, v156, v157, s[96:97]\n"
"v_mfma_f32_16x16x32_bf16 v[96:99], a[168:171], v[104:107], v[96:99]\n"
"s_cbranch_vccnz .Lmk_p40s\n"
".Lmk_p40r:\n"
"v_cndmask_b32_e64 v158, v158, v159, s[96:97]\n"
"v_mfma_f32_16x16x32_bf16 v[96:99], a[164:167], v[180:183], v[96:99]\n"
"ds_write_b128 v3, v[144:147]\n"
"v_mfma_f32_16x16x32_bf16 v[96:99], a[160:163], v[100:103], v[96:99]\n"
"buffer_load_dwordx4 v[144:147], v0, s[4:7], s98 offen offset:3072 sc1\n"
"v_mfma_f32_16x16x32_bf16 v[168:171], a[192:195], v[100:103], 0\n"
"s_mov_b32 s96, 0xcccccccc\n"
"s_mov_b32 s97, 0xcccccccc\n"
"v_mfma_f32_16x16x32_bf16 v[168:171], a[196:199], v[180:183], v[168:171]\n"
"v_cndmask_b32_e64 v165, v156, v158, s[96:97]\n"
"v_mfma_f32_16x16x32_bf16 v[168:171], a[200:203], v[104:107], v[168:171]\n"
"s_mov_b32 s96, 0xf0000f\n"
"s_mov_b32 s97, 0xf0000f00\n"
"v_mfma_f32_16x16x32_bf16 v[168:171], a[204:207], v[88:91], v[168:171]\n"
"buffer_load_dwordx4 v[156:159], v1, s[4:7], s98 offen sc1\n"
"v_mfma_f32_16x16x32_bf16 v[168:171], a[208:211], v[92:95], v[168:171]\n"
"v_cndmask_b32_e64 v164, v164, v165, s[96:97]\n"
"s_add_u32 s97, s63, 0x44000\n"
"v_mfma_f32_16x16x32_bf16 v[168:171], a[212:215], v[116:119], v[168:171]\n"
"ds_write_b128 v3, v[96:99] offset:1024\n"
"v_mfma_f32_16x16x32_bf16 v[168:171], a[216:219], v[136:139], v[168:171]\n"
"buffer_store_dword v164, v14, s[4:7], s97 offen sc1\n"
"v_mfma_f32_16x16x32_bf16 v[168:171], a[220:223], v[140:143], v[168:171]\n"
"buffer_load_dwordx4 v[164:167], v1, s[4:7], s98 offen offset:1024 sc1\n"
"v_mfma_f32_16x16x32_bf16 v[96:99], a[252:255], v[140:143], 0\n"
"v_mfma_f32_16x16x32_bf16 v[96:99], a[248:251], v[136:139], v[96:99]\n"
"v_mfma_f32_16x16x32_bf16 v[96:99], a[244:247], v[116:119], v[96:99]\n"
"v_mfma_f32_16x16x32_bf16 v[96:99], a[240:243], v[92:95], v[96:99]\n"
"buffer_load_dwordx4 v[116:119], v1, s[4:7], s98 offen offset:2048 sc1\n"
"v_mfma_f32_16x16x32_bf16 v[96:99], a[236:239], v[88:91], v[96:99]\n"
"v_mfma_f32_16x16x32_bf16 v[96:99], a[232:235], v[104:107], v[96:99]\n"
"ds_write_b128 v3, v[168:171] offset:2048\n"
"v_mfma_f32_16x16x32_bf16 v[96:99], a[228:231], v[180:183], v[96:99]\n"
"v_mfma_f32_16x16x32_bf16 v[96:99], a[224:227], v[100:103], v[96:99]\n"
"s_waitcnt vmcnt(14)\n"
"buffer_load_dwordx4 v[136:139], v1, s[4:7], s98 offen offset:3072 sc1\n"
"; @A G2 a.1\n"
"v_mfma_f32_16x16x32_bf16 v[88:91], a[128:131], v[148:151], 0\n"
"v_mfma_f32_16x16x32_bf16 v[88:91], a[132:135], v[108:111], v[88:91]\n"
"s_add_u32 s98, s59, 0\n"
"v_mfma_f32_16x16x32_bf16 v[88:91], a[136:139], v[112:115], v[88:91]\n"
"v_mfma_f32_16x16x32_bf16 v[88:91], a[140:143], v[196:199], v[88:91]\n"
"s_waitcnt vmcnt(10)\n"
"buffer_load_dwordx4 v[168:171], v0, s[4:7], s98 offen sc1\n"
"v_mfma_f32_16x16x32_bf16 v[88:91], a[144:147], v[200:203], v[88:91]\n"
"v_mfma_f32_16x16x32_bf16 v[88:91], a[148:151], v[128:131], v[88:91]\n"
"ds_write_b128 v3, v[96:99] offset:3072\n"
"v_mfma_f32_16x16x32_bf16 v[88:91], a[152:155], v[120:123], v[88:91]\n"
"v_mfma_f32_16x16x32_bf16 v[88:91], a[156:159], v[132:135], v[88:91]\n"
"buffer_load_dwordx4 v[172:175], v0, s[4:7], s98 offen offset:1024 sc1\n"
"v_mfma_f32_16x16x32_bf16 v[92:95], a[188:191], v[132:135], 0\n"
"s_add_u32 s97, s75, 0x10000\n"
"buffer_load_dwordx2 v[254:255], v13, s[4:7], s97 offen sc1\n"
"v_mfma_f32_16x16x32_bf16 v[92:95], a[184:187], v[120:123], v[92:95]\n"
"v_mfma_f32_16x16x32_bf16 v[92:95], a[180:183], v[128:131], v[92:95]\n"
"v_mfma_f32_16x16x32_bf16 v[92:95], a[176:179], v[200:203], v[92:95]\n"
"buffer_load_dwordx4 v[176:179], v0, s[4:7], s98 offen offset:2048 sc1\n"
"v_mfma_f32_16x16x32_bf16 v[92:95], a[172:175], v[196:199], v[92:95]\n"
"v_cmp_u_f32_e32 vcc, v88, v88\n"
"v_mfma_f32_16x16x32_bf16 v[92:95], a[168:171], v[112:115], v[92:95]\n"
"s_cbranch_vccnz .Lmk_p41s\n"
".Lmk_p41r:\n"
"v_mfma_f32_16x16x32_bf16 v[92:95], a[164:167], v[108:111], v[92:95]\n"
"ds_write_b128 v3, v[88:91] offset:6144\n"
"v_mfma_f32_16x16x32_bf16 v[92:95], a[160:163], v[148:151], v[92:95]\n"
"buffer_load_dwordx4 v[180:183], v0, s[4:7], s98 offen offset:3072 sc1\n"
"v_mfma_f32_16x16x32_bf16 v[88:91], a[192:195], v[148:151], 0\n"
"v_mfma_f32_16x16x32_bf16 v[88:91], a[196:199], v[108:111], v[88:91]\n"
"v_mfma_f32_16x16x32_bf16 v[88:91], a[200:203], v[112:115], v[88:91]\n"
"v_mfma_f32_16x16x32_bf16 v[88:91], a[204:207], v[196:199], v[88:91]\n"
"buffer_load_dwordx4 v[184:187], v1, s[4:7], s98 offen sc1\n"
"v_mfma_f32_16x16x32_bf16 v[88:91], a[208:211], v[200:203], v[88:91]\n"
"ds_read_b128 v[96:99], v2\n"
"ds_read_b128 v[100:103], v2 offset:1024\n"
"v_mfma_f32_16x16x32_bf16 v[88:91], a[212:215], v[128:131], v[88:91]\n"
"ds_write_b128 v3, v[92:95] offset:7168\n"
"v_mfma_f32_16x16x32_bf16 v[88:91], a[216:219], v[120:123], v[88:91]\n"
"v_mfma_f32_16x16x32_bf16 v[88:91], a[220:223], v[132:135], v[88:91]\n"
"buffer_load_dwordx4 v[188:191], v1, s[4:7], s98 offen offset:1024 sc1\n"
"v_mfma_f32_16x16x32_bf16 v[92:95], a[252:255], v[132:135], 0\n"
"v_mfma_f32_16x16x32_bf16 v[92:95], a[248:251], v[120:123], v[92:95]\n"
"s_waitcnt lgkmcnt(1)\n"
"v_lshlrev_b32_e32 v253, 16, v96\n"
"v_and_b32_e32 v252, 0xffff0000, v96\n"
"v_mfma_f32_16x16x32_bf16 v[92:95], a[244:247], v[128:131], v[92:95]\n"
"v_lshlrev_b32_e32 v251, 16, v97\n"
"v_and_b32_e32 v250, 0xffff0000, v97\n"
"v_mfma_f32_16x16x32_bf16 v[92:95], a[240:243], v[200:203], v[92:95]\n"
"buffer_load_dwordx4 v[192:195], v1, s[4:7], s98 offen offset:2048 sc1\n"
"v_mfma_f32_16x16x32_bf16 v[92:95], a[236:239], v[196:199], v[92:95]\n"
"v_lshlrev_b32_e32 v249, 16, v98\n"
"v_and_b32_e32 v248, 0xffff0000, v98\n"
"v_mfma_f32_16x16x32_bf16 v[92:95], a[232:235], v[112:115], v[92:95]\n"
"ds_write_b128 v3, v[88:91] offset:8192\n"
"v_mfma_f32_16x16x32_bf16 v[92:95], a[228:231], v[108:111], v[92:95]\n"
"v_lshlrev_b32_e32 v247, 16, v99\n"
"v_and_b32_e32 v246, 0xffff0000, v99\n"
"v_mfma_f32_16x16x32_bf16 v[92:95], a[224:227], v[148:151], v[92:95]\n"
"s_waitcnt vmcnt(13)\n"
"buffer_load_dwordx4 v[196:199], v1, s[4:7], s98 offen offset:3072 sc1\n"
"; @A G2 a.2\n"
"v_mfma_f32_16x16x32_bf16 v[96:99], a[128:131], v[152:155], 0\n"
"v_lshlrev_b32_e32 v245, 16, v100\n"
"v_and_b32_e32 v244, 0xffff0000, v100\n"
"v_mfma_f32_16x16x32_bf16 v[96:99], a[132:135], v[160:163], v[96:99]\n"
"v_lshlrev_b32_e32 v243, 16, v101\n"
"v_and_b32_e32 v242, 0xffff0000, v101\n"
"v_mfma_f32_16x16x32_bf16 v[96:99], a[136:139], v[124:127], v[96:99]\n"
"v_lshlrev_b32_e32 v241, 16, v102\n"
"v_and_b32_e32 v240, 0xffff0000, v102\n"
"v_mfma_f32_16x16x32_bf16 v[96:99], a[140:143], v[144:147], v[96:99]\n"
"s_waitcnt vmcnt(9)\n"
"v_lshlrev_b32_e32 v239, 16, v103\n"
"v_and_b32_e32 v238, 0xffff0000, v103\n"
"v_mfma_f32_16x16x32_bf16 v[96:99], a[144:147], v[156:159], v[96:99]\n"
"v_mfma_f32_16x16x32_bf16 v[96:99], a[148:151], v[164:167], v[96:99]\n"
"ds_write_b128 v3, v[92:95] offset:9216\n"
"v_mfma_f32_16x16x32_bf16 v[96:99], a[152:155], v[116:119], v[96:99]\n"
"v_mfma_f32_16x16x32_bf16 v[96:99], a[156:159], v[136:139], v[96:99]\n"
"v_mfma_f32_16x16x32_bf16 v[104:107], a[188:191], v[136:139], 0\n"
"s_add_u32 s98, s52, 0x440000\n"
"buffer_load_dwordx4 v[88:91], v0, s[4:7], s98 offen sc1\n"
"v_mfma_f32_16x16x32_bf16 v[104:107], a[184:187], v[116:119], v[104:107]\n"
"s_waitcnt lgkmcnt(0)\n"
"s_barrier\n"
"v_mfma_f32_16x16x32_bf16 v[104:107], a[180:183], v[164:167], v[104:107]\n"
"ds_read_b128 v[140:143], v4\n"
"ds_read_b128 v[112:115], v5\n"
"v_mfma_f32_16x16x32_bf16 v[104:107], a[176:179], v[156:159], v[104:107]\n"
"buffer_load_dwordx4 v[92:95], v0, s[4:7], s98 offen offset:1024 sc1\n"
"v_mfma_f32_16x16x32_bf16 v[104:107], a[172:175], v[144:147], v[104:107]\n"
"v_cmp_u_f32_e32 vcc, v96, v96\n"
"v_mfma_f32_16x16x32_bf16 v[104:107], a[168:171], v[124:127], v[104:107]\n"
"s_cbranch_vccnz .Lmk_p42s\n"
".Lmk_p42r:\n"
"v_mfma_f32_16x16x32_bf16 v[104:107], a[164:167], v[160:163], v[104:107]\n"
"ds_write_b128 v3, v[96:99] offset:12288\n"
"v_mfma_f32_16x16x32_bf16 v[104:107], a[160:163], v[152:155], v[104:107]\n"
"buffer_load_dwordx4 v[96:99], v0, s[4:7], s98 offen offset:2048 sc1\n"
"v_mfma_f32_16x16x32_bf16 v[120:123], a[192:195], v[152:155], 0\n"
"ds_read_b128 v[148:151], v4 offset:1024\n"
"ds_read_b128 v[128:131], v5 offset:1024\n"
"v_mfma_f32_16x16x32_bf16 v[120:123], a[196:199], v[160:163], v[120:123]\n"
"s_waitcnt lgkmcnt(3)\n"
"v_add_f32_e32 v140, v140, v253\n"
"v_add_f32_e32 v141, v141, v252\n"
"v_mfma_f32_16x16x32_bf16 v[120:123], a[200:203], v[124:127], v[120:123]\n"
"buffer_load_dwordx4 v[100:103], v0, s[4:7], s98 offen offset:3072 sc1\n"
"v_mfma_f32_16x16x32_bf16 v[120:123], a[204:207], v[144:147], v[120:123]\n"
"v_add_f32_e32 v142, v142, v251\n"
"v_add_f32_e32 v143, v143, v250\n"
"v_mfma_f32_16x16x32_bf16 v[120:123], a[208:211], v[156:159], v[120:123]\n"
"v_add_f32_e32 v140, v140, v112\n"
"v_add_f32_e32 v141, v141, v113\n"
"v_mfma_f32_16x16x32_bf16 v[120:123], a[212:215], v[164:167], v[120:123]\n"
"ds_write_b128 v3, v[104:107] offset:13312\n"
"v_mfma_f32_16x16x32_bf16 v[120:123], a[216:219], v[116:119], v[120:123]\n"
"buffer_load_dwordx4 v[104:107], v1, s[4:7], s98 offen sc1\n"
"v_mfma_f32_16x16x32_bf16 v[120:123], a[220:223], v[136:139], v[120:123]\n"
"ds_read_b128 v[108:111], v4 offset:2048\n"
"ds_read_b128 v[200:203], v5 offset:2048\n"
"v_mfma_f32_16x16x32_bf16 v[132:135], a[252:255], v[136:139], 0\n"
"s_waitcnt lgkmcnt(3)\n"
"v_add_f32_e32 v142, v142, v114\n"
"v_add_f32_e32 v143, v143, v115\n"
"v_mfma_f32_16x16x32_bf16 v[132:135], a[248:251], v[116:119], v[132:135]\n"
"buffer_load_dwordx4 v[112:115], v1, s[4:7], s98 offen offset:1024 sc1\n"
"v_mfma_f32_16x16x32_bf16 v[132:135], a[244:247], v[164:167], v[132:135]\n"
"v_add_f32_e32 v148, v148, v249\n"
"v_add_f32_e32 v149, v149, v248\n"
"v_mfma_f32_16x16x32_bf16 v[132:135], a[240:243], v[156:159], v[132:135]\n"
"v_add_f32_e32 v150, v150, v247\n"
"v_add_f32_e32 v151, v151, v246\n"
"v_mfma_f32_16x16x32_bf16 v[132:135], a[236:239], v[144:147], v[132:135]\n"
"buffer_load_dwordx4 v[116:119], v1, s[4:7], s98 offen offset:2048 sc1\n"
"v_mfma_f32_16x16x32_bf16 v[132:135], a[232:235], v[124:127], v[132:135]\n"
"ds_write_b128 v3, v[120:123] offset:14336\n"
"v_mfma_f32_16x16x32_bf16 v[132:135], a[228:231], v[160:163], v[132:135]\n"
"ds_read_b128 v[120:123], v4 offset:3072\n"
"ds_read_b128 v[156:159], v5 offset:3072\n"
"v_mfma_f32_16x16x32_bf16 v[132:135], a[224:227], v[152:155], v[132:135]\n"
"s_waitcnt vmcnt(11)\n"
"buffer_load_dwordx4 v[124:127], v1, s[4:7], s98 offen offset:3072 sc1\n"
"; @A G2 a.3\n"
"v_mfma_f32_16x16x32_bf16 v[204:207], a[128:131], v[168:171], v[140:143]\n"
"s_add_u32 s98, s53, 0x440000\n"
"v_add_f32_e32 v148, v148, v128\n"
"v_add_f32_e32 v149, v149, v129\n"
"v_mfma_f32_16x16x32_bf16 v[204:207], a[132:135], v[172:175], v[204:207]\n"
"s_waitcnt lgkmcnt(3)\n"
"v_add_f32_e32 v150, v150, v130\n"
"v_add_f32_e32 v151, v151, v131\n"
"v_mfma_f32_16x16x32_bf16 v[204:207], a[136:139], v[176:179], v[204:207]\n"
"buffer_load_dwordx4 v[128:131], v0, s[4:7], s98 offen sc1\n"
"v_mfma_f32_16x16x32_bf16 v[204:207], a[140:143], v[180:183], v[204:207]\n"
"s_waitcnt vmcnt(9)\n"
"v_add_f32_e32 v108, v108, v245\n"
"v_add_f32_e32 v109, v109, v244\n"
"v_mfma_f32_16x16x32_bf16 v[204:207], a[144:147], v[184:187], v[204:207]\n"
"v_cmp_ne_u64_e32 vcc, v[254:255], v[18:19]\n"
"v_mfma_f32_16x16x32_bf16 v[204:207], a[148:151], v[188:191], v[204:207]\n"
"ds_write_b128 v3, v[132:135] offset:15360\n"
"v_mfma_f32_16x16x32_bf16 v[204:207], a[152:155], v[192:195], v[204:207]\n"
"s_cbranch_vccnz .Lmk_f13s\n"
".Lmk_f13r:\n"
"v_add_f32_e32 v110, v110, v243\n"
"v_mfma_f32_16x16x32_bf16 v[204:207], a[156:159], v[196:199], v[204:207]\n"
"buffer_load_dwordx4 v[132:135], v0, s[4:7], s98 offen offset:1024 sc1\n"
"v_mfma_f32_16x16x32_bf16 v[208:211], a[188:191], v[196:199], v[148:151]\n"
"v_add_f32_e32 v111, v111, v242\n"
"v_add_f32_e32 v108, v108, v200\n"
"v_mfma_f32_16x16x32_bf16 v[208:211], a[184:187], v[192:195], v[208:211]\n"
"s_waitcnt lgkmcnt(0)\n"
"s_barrier\n"
"v_mfma_f32_16x16x32_bf16 v[208:211], a[180:183], v[188:191], v[208:211]\n"
"buffer_load_dwordx4 v[136:139], v0, s[4:7], s98 offen offset:2048 sc1\n"
"v_mfma_f32_16x16x32_bf16 v[208:211], a[176:179], v[184:187], v[208:211]\n"
"v_add_f32_e32 v109, v109, v201\n"
"v_add_f32_e32 v110, v110, v202\n"
"v_mfma_f32_16x16x32_bf16 v[208:211], a[172:175], v[180:183], v[208:211]\n"
"v_cmp_u_f32_e32 vcc, v204, v204\n"
"v_add_f32_e32 v111, v111, v203\n"
"v_mfma_f32_16x16x32_bf16 v[208:211], a[168:171], v[176:179], v[208:211]\n"
"s_cbranch_vccnz .Lmk_p43s\n"
".Lmk_p43r:\n"
"v_add_f32_e32 v120, v120, v241\n"
"v_mfma_f32_16x16x32_bf16 v[208:211], a[164:167], v[172:175], v[208:211]\n"
"buffer_load_dwordx4 v[140:143], v0, s[4:7], s98 offen offset:3072 sc1\n"
"v_mfma_f32_16x16x32_bf16 v[208:211], a[160:163], v[168:171], v[208:211]\n"
"v_add_f32_e32 v121, v121, v240\n"
"v_add_f32_e32 v122, v122, v239\n"
"v_mfma_f32_16x16x32_bf16 v[108:111], a[192:195], v[168:171], v[108:111]\n"
"v_add_f32_e32 v123, v123, v238\n"
"v_add_f32_e32 v120, v120, v156\n"
"v_mfma_f32_16x16x32_bf16 v[108:111], a[196:199], v[172:175], v[108:111]\n"
"buffer_load_dwordx4 v[144:147], v1, s[4:7], s98 offen sc1\n"
"v_mfma_f32_16x16x32_bf16 v[108:111], a[200:203], v[176:179], v[108:111]\n"
"v_add_f32_e32 v121, v121, v157\n"
"v_add_f32_e32 v122, v122, v158\n"
"v_mfma_f32_16x16x32_bf16 v[108:111], a[204:207], v[180:183], v[108:111]\n"
"ds_read_b128 v[200:203], v6\n"
"v_add_f32_e32 v123, v123, v159\n"
"v_mfma_f32_16x16x32_bf16 v[108:111], a[208:211], v[184:187], v[108:111]\n"
"buffer_load_dwordx4 v[148:151], v1, s[4:7], s98 offen offset:1024 sc1\n"
"v_mfma_f32_16x16x32_bf16 v[108:111], a[212:215], v[188:191], v[108:111]\n"
"ds_read_b128 v[212:215], v6 offset:1024\n"
"ds_read_b128 v[152:155], v6 offset:2048\n"
"v_mfma_f32_16x16x32_bf16 v[108:111], a[216:219], v[192:195], v[108:111]\n"
"ds_read_b128 v[156:159], v6 offset:3072\n"
"v_mfma_f32_16x16x32_bf16 v[108:111], a[220:223], v[196:199], v[108:111]\n"
"buffer_load_dwordx4 v[160:163], v1, s[4:7], s98 offen offset:2048 sc1\n"
"v_mfma_f32_16x16x32_bf16 v[120:123], a[252:255], v[196:199], v[120:123]\n"
"v_mfma_f32_16x16x32_bf16 v[120:123], a[248:251], v[192:195], v[120:123]\n"
"v_mfma_f32_16x16x32_bf16 v[120:123], a[244:247], v[188:191], v[120:123]\n"
"buffer_load_dwordx4 v[164:167], v1, s[4:7], s98 offen offset:3072 sc1\n"
"v_mfma_f32_16x16x32_bf16 v[120:123], a[240:243], v[184:187], v[120:123]\n"
"s_waitcnt lgkmcnt(0)\n"
"v_add_f32_e32 v200, v204, v200\n"
"v_add_f32_e32 v201, v205, v201\n"
"v_mfma_f32_16x16x32_bf16 v[120:123], a[236:239], v[180:183], v[120:123]\n"
"v_add_f32_e32 v202, v206, v202\n"
"v_add_f32_e32 v203, v207, v203\n"
"v_mfma_f32_16x16x32_bf16 v[120:123], a[232:235], v[176:179], v[120:123]\n"
"v_add_f32_e32 v212, v208, v212\n"
"v_add_f32_e32 v213, v209, v213\n"
"v_mfma_f32_16x16x32_bf16 v[120:123], a[228:231], v[172:175], v[120:123]\n"
"v_add_f32_e32 v214, v210, v214\n"
"v_add_f32_e32 v215, v211, v215\n"
"v_mfma_f32_16x16x32_bf16 v[120:123], a[224:227], v[168:171], v[120:123]\n"
"s_add_u32 s98, s61, 0x400000\n"
"v_cvt_pk_bf16_f32 v168, v200, v201\n"
"v_cvt_pk_bf16_f32 v169, v202, v203\n"
"v_cvt_pk_bf16_f32 v170, v212, v213\n"
"v_cvt_pk_bf16_f32 v171, v214, v215\n"
"buffer_store_dwordx4 v[168:171], v0, s[4:7], s98 offen sc1\n"
"ds_write_b128 v2, v[168:171]\n"
".Lmk_loop:\n"
".Lmk_b0:\n"
"s_waitcnt vmcnt(13)\n"
"; @B G1 0.0\n"
"v_mfma_f32_16x16x32_bf16 v[172:175], a[0:3], v[88:91], 0\n"
"v_mfma_f32_16x16x32_bf16 v[172:175], a[4:7], v[92:95], v[172:175]\n"
"s_add_u32 s98, s62, 0x44000\n"
"v_mfma_f32_16x16x32_bf16 v[172:175], a[8:11], v[96:99], v[172:175]\n"
"s_add_u32 s97, s54, 0x440000\n"
"buffer_load_dwordx4 v[176:179], v0, s[4:7], s97 offen sc1\n"
"v_mfma_f32_16x16x32_bf16 v[172:175], a[12:15], v[100:103], v[172:175]\n"
"s_waitcnt vmcnt(10)\n"
"v_mfma_f32_16x16x32_bf16 v[172:175], a[16:19], v[104:107], v[172:175]\n"
"buffer_load_dword v255, v10, s[4:7], s98 offen sc1\n"
"v_mfma_f32_16x16x32_bf16 v[172:175], a[20:23], v[112:115], v[172:175]\n"
"v_add_f32_e32 v152, v108, v152\n"
"v_add_f32_e32 v153, v109, v153\n"
"v_mfma_f32_16x16x32_bf16 v[172:175], a[24:27], v[116:119], v[172:175]\n"
"v_add_f32_e32 v154, v110, v154\n"
"v_add_f32_e32 v155, v111, v155\n"
"v_mfma_f32_16x16x32_bf16 v[172:175], a[28:31], v[124:127], v[172:175]\n"
"buffer_load_dwordx4 v[108:111], v0, s[4:7], s97 offen offset:1024 sc1\n"
"v_mfma_f32_16x16x32_bf16 v[180:183], a[60:63], v[124:127], 0\n"
"buffer_load_dword v254, v10, s[4:7], s98 offen offset:256 sc1\n"
"v_mfma_f32_16x16x32_bf16 v[180:183], a[56:59], v[116:119], v[180:183]\n"
"v_add_f32_e32 v156, v120, v156\n"
"v_add_f32_e32 v157, v121, v157\n"
"v_mfma_f32_16x16x32_bf16 v[180:183], a[52:55], v[112:115], v[180:183]\n"
"v_add_f32_e32 v158, v122, v158\n"
"v_add_f32_e32 v159, v123, v159\n"
"v_mfma_f32_16x16x32_bf16 v[180:183], a[48:51], v[104:107], v[180:183]\n"
"buffer_load_dword v253, v10, s[4:7], s98 offen offset:512 sc1\n"
"v_mfma_f32_16x16x32_bf16 v[180:183], a[44:47], v[100:103], v[180:183]\n"
"v_cmp_u_f32_e32 vcc, v172, v172\n"
"v_cvt_pk_bf16_f32 v120, v152, v153\n"
"v_mfma_f32_16x16x32_bf16 v[180:183], a[40:43], v[96:99], v[180:183]\n"
"s_cbranch_vccnz .Lmk_p48s\n"
".Lmk_p48r:\n"
"v_cvt_pk_bf16_f32 v121, v154, v155\n"
"v_mfma_f32_16x16x32_bf16 v[180:183], a[36:39], v[92:95], v[180:183]\n"
"buffer_load_dwordx4 v[152:155], v0, s[4:7], s97 offen offset:2048 sc1\n"
"v_mfma_f32_16x16x32_bf16 v[180:183], a[32:35], v[88:91], v[180:183]\n"
"ds_write_b128 v3, v[172:175]\n"
"v_mfma_f32_16x16x32_bf16 v[172:175], a[64:67], v[88:91], 0\n"
"buffer_load_dword v252, v10, s[4:7], s98 offen offset:768 sc1\n"
"v_mfma_f32_16x16x32_bf16 v[172:175], a[68:71], v[92:95], v[172:175]\n"
"buffer_load_dwordx4 v[184:187], v0, s[4:7], s97 offen offset:3072 sc1\n"
"v_mfma_f32_16x16x32_bf16 v[172:175], a[72:75], v[96:99], v[172:175]\n"
"s_add_u32 s96, s61, 0x400000\n"
"v_cvt_pk_bf16_f32 v122, v156, v157\n"
"v_cvt_pk_bf16_f32 v123, v158, v159\n"
"v_mfma_f32_16x16x32_bf16 v[172:175], a[76:79], v[100:103], v[172:175]\n"
"buffer_load_dword v251, v10, s[4:7], s98 offen offset:1024 sc1\n"
"v_mfma_f32_16x16x32_bf16 v[172:175], a[80:83], v[104:107], v[172:175]\n"
"buffer_store_dwordx4 v[120:123], v0, s[4:7], s96 offen offset:1024 sc1\n"
"v_mfma_f32_16x16x32_bf16 v[172:175], a[84:87], v[112:115], v[172:175]\n"
"ds_write_b128 v3, v[180:183] offset:1024\n"
"v_mfma_f32_16x16x32_bf16 v[172:175], a[88:91], v[116:119], v[172:175]\n"
"buffer_load_dwordx4 v[156:159], v1, s[4:7], s97 offen sc1\n"
"v_mfma_f32_16x16x32_bf16 v[172:175], a[92:95], v[124:127], v[172:175]\n"
"buffer_load_dword v250, v10, s[4:7], s98 offen offset:1280 sc1\n"
"v_mfma_f32_16x16x32_bf16 v[180:183], a[124:127], v[124:127], 0\n"
"ds_write_b128 v2, v[120:123] offset:1024\n"
"v_mfma_f32_16x16x32_bf16 v[180:183], a[120:123], v[116:119], v[180:183]\n"
"v_mov_b32_e32 v188, 0x3f803f80\n"
"v_mov_b32_e32 v189, 0x3f803f80\n"
"v_mfma_f32_16x16x32_bf16 v[180:183], a[116:119], v[112:115], v[180:183]\n"
"buffer_load_dword v249, v10, s[4:7], s98 offen offset:1536 sc1\n"
"v_mfma_f32_16x16x32_bf16 v[180:183], a[112:115], v[104:107], v[180:183]\n"
"buffer_load_dwordx4 v[192:195], v1, s[4:7], s97 offen offset:1024 sc1\n"
"v_mfma_f32_16x16x32_bf16 v[180:183], a[108:111], v[100:103], v[180:183]\n"
"v_mov_b32_e32 v190, 0x3f803f80\n"
"v_mov_b32_e32 v191, 0x3f803f80\n"
"v_mfma_f32_16x16x32_bf16 v[180:183], a[104:107], v[96:99], v[180:183]\n"
"ds_write_b128 v3, v[172:175] offset:2048\n"
"v_mfma_f32_16x16x32_bf16 v[172:175], v[188:191], v[168:171], 0\n"
"v_mfma_f32_16x16x32_bf16 v[172:175], v[188:191], v[120:123], v[172:175]\n"
"v_mfma_f32_16x16x32_bf16 v[188:191], v[168:171], v[168:171], 0\n"
"v_mfma_f32_16x16x32_bf16 v[188:191], v[120:123], v[120:123], v[188:191]\n"
"v_mfma_f32_16x16x32_bf16 v[180:183], a[100:103], v[92:95], v[180:183]\n"
"v_mov_b32_e32 v120, -1\n"
"v_mov_b32_e32 v121, -1\n"
"v_mfma_f32_16x16x32_bf16 v[180:183], a[96:99], v[88:91], v[180:183]\n"
"v_mov_b32_e32 v122, -1\n"
"v_mov_b32_e32 v123, -1\n"
"v_mfma_f32_16x16x32_bf16 v[168:171], v[24:27], v[88:91], 0\n"
"buffer_load_dwordx4 v[196:199], v1, s[4:7], s97 offen offset:2048 sc1\n"
"v_mfma_f32_16x16x32_bf16 v[168:171], v[28:31], v[92:95], v[168:171]\n"
"s_add_u32 s96, s60, 0x448000\n"
"buffer_store_dwordx4 v[120:123], v0, s[4:7], s96 offen sc1\n"
"v_mfma_f32_16x16x32_bf16 v[168:171], v[32:35], v[96:99], v[168:171]\n"
"buffer_load_dword v248, v10, s[4:7], s98 offen offset:1792 sc1\n"
"v_mfma_f32_16x16x32_bf16 v[168:171], v[36:39], v[100:103], v[168:171]\n"
"s_mov_b32 s94, 0xaaaaaaaa\n"
"s_mov_b32 s95, 0xaaaaaaaa\n"
"v_mfma_f32_16x16x32_bf16 v[168:171], v[40:43], v[104:107], v[168:171]\n"
"v_mfma_f32_16x16x32_bf16 v[168:171], v[44:47], v[112:115], v[168:171]\n"
"buffer_load_dwordx4 v[200:203], v1, s[4:7], s97 offen offset:3072 sc1\n"
"v_mfma_f32_16x16x32_bf16 v[168:171], v[48:51], v[116:119], v[168:171]\n"
"ds_write_b128 v3, v[180:183] offset:3072\n"
"v_mfma_f32_16x16x32_bf16 v[168:171], v[52:55], v[124:127], v[168:171]\n"
"v_cndmask_b32_e64 v188, v188, v189, s[94:95]\n"
"v_cndmask_b32_e64 v190, v190, v191, s[94:95]\n"
"v_mfma_f32_16x16x32_bf16 v[180:183], v[84:87], v[124:127], 0\n"
"s_mov_b32 s94, 0xcccccccc\n"
"s_mov_b32 s95, 0xcccccccc\n"
"v_mfma_f32_16x16x32_bf16 v[180:183], v[80:83], v[116:119], v[180:183]\n"
"v_cndmask_b32_e64 v173, v188, v190, s[94:95]\n"
"v_mfma_f32_16x16x32_bf16 v[180:183], v[76:79], v[112:115], v[180:183]\n"
"s_mov_b32 s94, 0xf0000f\n"
"s_mov_b32 s95, 0xf0000f00\n"
"v_mfma_f32_16x16x32_bf16 v[180:183], v[72:75], v[104:107], v[180:183]\n"
"s_add_u32 s98, s63, 0x40000\n"
"v_cndmask_b32_e64 v172, v172, v173, s[94:95]\n"
"v_mfma_f32_16x16x32_bf16 v[180:183], v[68:71], v[100:103], v[180:183]\n"
"s_add_u32 s97, s70, 0x200000\n"
"buffer_load_dwordx4 a[128:131], v0, s[4:7], s97 offen sc1\n"
"v_mfma_f32_16x16x32_bf16 v[180:183], v[64:67], v[96:99], v[180:183]\n"
"ds_write_b128 v3, v[168:171] offset:4096\n"
"v_mfma_f32_16x16x32_bf16 v[180:183], v[60:63], v[92:95], v[180:183]\n"
"buffer_store_dword v172, v14, s[4:7], s98 offen sc1\n"
"v_mfma_f32_16x16x32_bf16 v[180:183], v[56:59], v[88:91], v[180:183]\n"
"s_waitcnt vmcnt(25)\n"
"; @B G1 0.1\n"
"v_mfma_f32_16x16x32_bf16 v[88:91], a[0:3], v[128:131], 0\n"
"v_mfma_f32_16x16x32_bf16 v[88:91], a[4:7], v[132:135], v[88:91]\n"
"v_mfma_f32_16x16x32_bf16 v[88:91], a[8:11], v[136:139], v[88:91]\n"
"s_add_u32 s98, s55, 0x440000\n"
"buffer_load_dwordx4 v[92:95], v0, s[4:7], s98 offen sc1\n"
"v_mfma_f32_16x16x32_bf16 v[88:91], a[12:15], v[140:143], v[88:91]\n"
"s_waitcnt vmcnt(22)\n"
"v_mfma_f32_16x16x32_bf16 v[88:91], a[16:19], v[144:147], v[88:91]\n"
"v_mfma_f32_16x16x32_bf16 v[88:91], a[20:23], v[148:151], v[88:91]\n"
"ds_write_b128 v3, v[180:183] offset:5120\n"
"v_mfma_f32_16x16x32_bf16 v[88:91], a[24:27], v[160:163], v[88:91]\n"
"v_mfma_f32_16x16x32_bf16 v[88:91], a[28:31], v[164:167], v[88:91]\n"
"buffer_load_dwordx4 v[96:99], v0, s[4:7], s98 offen offset:1024 sc1\n"
"v_mfma_f32_16x16x32_bf16 v[100:103], a[60:63], v[164:167], 0\n"
"v_mfma_f32_16x16x32_bf16 v[100:103], a[56:59], v[160:163], v[100:103]\n"
"v_mfma_f32_16x16x32_bf16 v[100:103], a[52:55], v[148:151], v[100:103]\n"
"v_mfma_f32_16x16x32_bf16 v[100:103], a[48:51], v[144:147], v[100:103]\n"
"v_mfma_f32_16x16x32_bf16 v[100:103], a[44:47], v[140:143], v[100:103]\n"
"s_nop 1\n"
"v_cmp_u_f32_e32 vcc, v88, v88\n"
"v_mfma_f32_16x16x32_bf16 v[100:103], a[40:43], v[136:139], v[100:103]\n"
"s_cbranch_vccnz .Lmk_p49s\n"
".Lmk_p49r:\n"
"v_mfma_f32_16x16x32_bf16 v[100:103], a[36:39], v[132:135], v[100:103]\n"
"buffer_load_dwordx4 v[104:107], v0, s[4:7], s98 offen offset:2048 sc1\n"
"v_mfma_f32_16x16x32_bf16 v[100:103], a[32:35], v[128:131], v[100:103]\n"
"ds_write_b128 v3, v[88:91] offset:6144\n"
"v_mfma_f32_16x16x32_bf16 v[88:91], a[64:67], v[128:131], 0\n"
"buffer_load_dwordx4 a[132:135], v0, s[4:7], s97 offen offset:1024 sc1\n"
"v_mfma_f32_16x16x32_bf16 v[88:91], a[68:71], v[132:135], v[88:91]\n"
"buffer_load_dwordx4 v[112:115], v0, s[4:7], s98 offen offset:3072 sc1\n"
"v_mfma_f32_16x16x32_bf16 v[88:91], a[72:75], v[136:139], v[88:91]\n"
"v_mfma_f32_16x16x32_bf16 v[88:91], a[76:79], v[140:143], v[88:91]\n"
"v_mfma_f32_16x16x32_bf16 v[88:91], a[80:83], v[144:147], v[88:91]\n"
"v_mfma_f32_16x16x32_bf16 v[88:91], a[84:87], v[148:151], v[88:91]\n"
"ds_write_b128 v3, v[100:103] offset:7168\n"
"v_mfma_f32_16x16x32_bf16 v[88:91], a[88:91], v[160:163], v[88:91]\n"
"buffer_load_dwordx4 v[100:103], v1, s[4:7], s98 offen sc1\n"
"v_mfma_f32_16x16x32_bf16 v[88:91], a[92:95], v[164:167], v[88:91]\n"
"v_mfma_f32_16x16x32_bf16 v[116:119], a[124:127], v[164:167], 0\n"
"v_mfma_f32_16x16x32_bf16 v[116:119], a[120:123], v[160:163], v[116:119]\n"
"v_mfma_f32_16x16x32_bf16 v[116:119], a[116:119], v[148:151], v[116:119]\n"
"v_mfma_f32_16x16x32_bf16 v[116:119], a[112:115], v[144:147], v[116:119]\n"
"buffer_load_dwordx4 v[124:127], v1, s[4:7], s98 offen offset:1024 sc1\n"
"v_mfma_f32_16x16x32_bf16 v[116:119], a[108:111], v[140:143], v[116:119]\n"
"v_mfma_f32_16x16x32_bf16 v[116:119], a[104:107], v[136:139], v[116:119]\n"
"s_nop 0\n"
"ds_write_b128 v3, v[88:91] offset:8192\n"
"v_mfma_f32_16x16x32_bf16 v[116:119], a[100:103], v[132:135], v[116:119]\n"
"v_mfma_f32_16x16x32_bf16 v[116:119], a[96:99], v[128:131], v[116:119]\n"
"v_mfma_f32_16x16x32_bf16 v[88:91], v[24:27], v[128:131], 0\n"
"buffer_load_dwordx4 v[168:171], v1, s[4:7], s98 offen offset:2048 sc1\n"
"v_mfma_f32_16x16x32_bf16 v[88:91], v[28:31], v[132:135], v[88:91]\n"
"s_add_u32 s96, s60, 0x440000\n"
"v_mfma_f32_16x16x32_bf16 v[88:91], v[32:35], v[136:139], v[88:91]\n"
"v_mfma_f32_16x16x32_bf16 v[88:91], v[36:39], v[140:143], v[88:91]\n"
"buffer_store_dwordx4 v[120:123], v0, s[4:7], s96 offen sc1\n"
"v_mfma_f32_16x16x32_bf16 v[88:91], v[40:43], v[144:147], v[88:91]\n"
"buffer_load_dwordx4 a[136:139], v0, s[4:7], s97 offen offset:2048 sc1\n"
"v_mfma_f32_16x16x32_bf16 v[88:91], v[44:47], v[148:151], v[88:91]\n"
"buffer_load_dwordx4 v[172:175], v1, s[4:7], s98 offen offset:3072 sc1\n"
"v_mfma_f32_16x16x32_bf16 v[88:91], v[48:51], v[160:163], v[88:91]\n"
"ds_write_b128 v3, v[116:119] offset:9216\n"
"v_mfma_f32_16x16x32_bf16 v[88:91], v[52:55], v[164:167], v[88:91]\n"
"s_waitcnt vmcnt(14)\n"
"v_add_f32_e32 v255, v255, v251\n"
"v_add_f32_e32 v254, v254, v250\n"
"v_mfma_f32_16x16x32_bf16 v[116:119], v[84:87], v[164:167], 0\n"
"v_add_f32_e32 v253, v253, v249\n"
"v_add_f32_e32 v252, v252, v248\n"
"v_mfma_f32_16x16x32_bf16 v[116:119], v[80:83], v[160:163], v[116:119]\n"
"v_add_f32_e32 v255, v255, v253\n"
"v_add_f32_e32 v254, v254, v252\n"
"v_mfma_f32_16x16x32_bf16 v[116:119], v[76:79], v[148:151], v[116:119]\n"
"v_add_f32_e32 v255, v255, v254\n"
"v_cmp_u_f32_e32 vcc, v255, v255\n"
"v_mfma_f32_16x16x32_bf16 v[116:119], v[72:75], v[144:147], v[116:119]\n"
"s_cbranch_vccnz .Lmk_m52s\n"
".Lmk_m52r:\n"
"v_mov_b32_e32 v254, v255\n"
"v_mfma_f32_16x16x32_bf16 v[116:119], v[68:71], v[140:143], v[116:119]\n"
"s_nop 0\n"
"v_permlane32_swap_b32_e32 v255, v254\n"
"v_add_f32_e32 v255, v255, v254\n"
"v_mfma_f32_16x16x32_bf16 v[116:119], v[64:67], v[136:139], v[116:119]\n"
"ds_write_b128 v3, v[88:91] offset:10240\n"
"v_mfma_f32_16x16x32_bf16 v[116:119], v[60:63], v[132:135], v[116:119]\n"
"v_mul_f32_e32 v255, 0x3a800000, v255\n"
"v_mov_b32_e32 v254, v255\n"
"v_mfma_f32_16x16x32_bf16 v[116:119], v[56:59], v[128:131], v[116:119]\n"
"s_nop 0\n"
"v_permlane16_swap_b32_e32 v255, v254\n"
"v_cvt_pk_bf16_f32 v253, -v255, 0\n"
"; @B G1 0.2\n"
"v_mfma_f32_16x16x32_bf16 v[88:91], a[0:3], v[176:179], 0\n"
"v_fma_f32 v254, -v255, v255, v254\n"
"v_lshlrev_b32_e32 v253, 16, v253\n"
"v_mfma_f32_16x16x32_bf16 v[88:91], a[4:7], v[108:111], v[88:91]\n"
"v_max_f32_e32 v254, 0, v254\n"
"v_cvt_pk_bf16_f32 v254, v254, 0\n"
"v_mfma_f32_16x16x32_bf16 v[88:91], a[8:11], v[152:155], v[88:91]\n"
"v_lshlrev_b32_e32 v254, 16, v254\n"
"v_add_f32_e32 v254, 0x3727c5ac, v254\n"
"v_mfma_f32_16x16x32_bf16 v[88:91], a[12:15], v[184:187], v[88:91]\n"
"s_waitcnt vmcnt(13)\n"
"v_cvt_pk_bf16_f32 v254, v254, 0\n"
"v_lshlrev_b32_e32 v254, 16, v254\n"
"v_mfma_f32_16x16x32_bf16 v[88:91], a[16:19], v[156:159], v[88:91]\n"
"s_mul_i32 s98, s0, .Lmk_exit-.Lmk_b2\n"
"s_add_u32 s98, s98, .Lmk_b2-.Lmk_g47\n"
"s_getpc_b64 s[84:85]\n"
".Lmk_g47:\n"
"s_add_u32 s84, s84, s98\n"
"s_addc_u32 s85, s85, 0\n"
"v_mfma_f32_16x16x32_bf16 v[88:91], a[20:23], v[192:195], v[88:91]\n"
"ds_write_b128 v3, v[116:119] offset:11264\n"
"v_mfma_f32_16x16x32_bf16 v[88:91], a[24:27], v[196:199], v[88:91]\n"
"s_and_b32 s84, s84, -16\n"
"s_and_b32 s85, s85, 0xffff\n"
"s_sub_u32 s98, .Lmk_end-.Lmk_g47, s98\n"
"s_min_u32 s86, s98, 0x4000\n"
"s_mov_b32 s87, 0x20000\n"
"v_mfma_f32_16x16x32_bf16 v[88:91], a[28:31], v[200:203], v[88:91]\n"
"s_mov_b32 m0, 0x27800\n"
"s_nop 0\n"
"buffer_load_dwordx4 v0, s[84:87], s99 offen lds\n"
"v_mfma_f32_16x16x32_bf16 v[116:119], a[60:63], v[200:203], 0\n"
"buffer_load_dwordx4 a[140:143], v0, s[4:7], s97 offen offset:3072 sc1\n"
"v_mfma_f32_16x16x32_bf16 v[116:119], a[56:59], v[196:199], v[116:119]\n"
"v_sqrt_f32_e32 v254, v254\n"
"v_mfma_f32_16x16x32_bf16 v[116:119], a[52:55], v[192:195], v[116:119]\n"
"s_add_u32 s98, s52, 0x448000\n"
"buffer_load_dwordx4 v[128:131], v0, s[4:7], s98 offen sc1\n"
"v_mfma_f32_16x16x32_bf16 v[116:119], a[48:51], v[156:159], v[116:119]\n"
"s_waitcnt lgkmcnt(0)\n"
"s_barrier\n"
"v_mfma_f32_16x16x32_bf16 v[116:119], a[44:47], v[184:187], v[116:119]\n"
"v_cmp_u_f32_e32 vcc, v88, v88\n"
"v_cvt_pk_bf16_f32 v254, v254, 0\n"
"v_mfma_f32_16x16x32_bf16 v[116:119], a[40:43], v[152:155], v[116:119]\n"
"s_cbranch_vccnz .Lmk_p50s\n"
".Lmk_p50r:\n"
"v_lshlrev_b32_e32 v254, 16, v254\n"
"v_mfma_f32_16x16x32_bf16 v[116:119], a[36:39], v[108:111], v[116:119]\n"
"ds_write_b128 v3, v[88:91] offset:12288\n"
"v_mfma_f32_16x16x32_bf16 v[116:119], a[32:35], v[176:179], v[116:119]\n"
"buffer_load_dwordx4 v[88:91], v0, s[4:7], s98 offen offset:1024 sc1\n"
"v_mfma_f32_16x16x32_bf16 v[132:135], a[64:67], v[176:179], 0\n"
"v_rcp_f32_e32 v255, v254\n"
"v_mfma_f32_16x16x32_bf16 v[132:135], a[68:71], v[108:111], v[132:135]\n"
"ds_read_b128 v[136:139], v4\n"
"v_mul_f32_e32 v254, 0x3e8293ee, v255\n"
"v_mfma_f32_16x16x32_bf16 v[132:135], a[72:75], v[152:155], v[132:135]\n"
"ds_read_b128 v[140:143], v9 offset:640\n"
"ds_read_b128 v[144:147], v5\n"
"v_mfma_f32_16x16x32_bf16 v[132:135], a[76:79], v[184:187], v[132:135]\n"
"ds_read_b128 v[148:151], v4 offset:1024\n"
"ds_read_b128 v[160:163], v9 offset:704\n"
"v_mfma_f32_16x16x32_bf16 v[132:135], a[80:83], v[156:159], v[132:135]\n"
"buffer_load_dwordx4 v[164:167], v0, s[4:7], s98 offen offset:2048 sc1\n"
"v_mfma_f32_16x16x32_bf16 v[132:135], a[84:87], v[192:195], v[132:135]\n"
"ds_write_b128 v3, v[116:119] offset:13312\n"
"v_mfma_f32_16x16x32_bf16 v[132:135], a[88:91], v[196:199], v[132:135]\n"
"ds_read_b128 v[116:119], v5 offset:1024\n"
"v_mfma_f32_16x16x32_bf16 v[132:135], a[92:95], v[200:203], v[132:135]\n"
"s_waitcnt lgkmcnt(4)\n"
"v_fmac_f32_e32 v136, v253, v140\n"
"v_fmac_f32_e32 v137, v253, v141\n"
"v_mfma_f32_16x16x32_bf16 v[180:183], a[124:127], v[200:203], 0\n"
"v_fmac_f32_e32 v138, v253, v142\n"
"v_fmac_f32_e32 v139, v253, v143\n"
"v_mfma_f32_16x16x32_bf16 v[180:183], a[120:123], v[196:199], v[180:183]\n"
"buffer_load_dwordx4 v[140:143], v0, s[4:7], s98 offen offset:3072 sc1\n"
"v_mfma_f32_16x16x32_bf16 v[180:183], a[116:119], v[192:195], v[180:183]\n"
"ds_read_b128 v[188:191], v4 offset:2048\n"
"ds_read_b128 v[204:207], v9 offset:768\n"
"v_mfma_f32_16x16x32_bf16 v[180:183], a[112:115], v[156:159], v[180:183]\n"
"ds_read_b128 v[208:211], v5 offset:2048\n"
"v_add_f32_e32 v136, v136, v144\n"
"v_mfma_f32_16x16x32_bf16 v[180:183], a[108:111], v[184:187], v[180:183]\n"
"buffer_load_dwordx4 a[144:147], v1, s[4:7], s97 offen sc1\n"
"v_mfma_f32_16x16x32_bf16 v[180:183], a[104:107], v[152:155], v[180:183]\n"
"ds_write_b128 v3, v[132:135] offset:14336\n"
"v_mfma_f32_16x16x32_bf16 v[180:183], a[100:103], v[108:111], v[180:183]\n"
"buffer_load_dwordx4 v[132:135], v1, s[4:7], s98 offen sc1\n"
"v_mfma_f32_16x16x32_bf16 v[180:183], a[96:99], v[176:179], v[180:183]\n"
"v_add_f32_e32 v137, v137, v145\n"
"v_add_f32_e32 v138, v138, v146\n"
"v_mfma_f32_16x16x32_bf16 v[212:215], v[24:27], v[176:179], 0\n"
"s_waitcnt lgkmcnt(4)\n"
"v_add_f32_e32 v139, v139, v147\n"
"v_fmac_f32_e32 v148, v253, v160\n"
"v_mfma_f32_16x16x32_bf16 v[212:215], v[28:31], v[108:111], v[212:215]\n"
"ds_read_b128 v[144:147], v4 offset:3072\n"
"ds_read_b128 v[216:219], v9 offset:832\n"
"v_mfma_f32_16x16x32_bf16 v[212:215], v[32:35], v[152:155], v[212:215]\n"
"ds_read_b128 v[220:223], v5 offset:3072\n"
"v_fmac_f32_e32 v149, v253, v161\n"
"v_mfma_f32_16x16x32_bf16 v[212:215], v[36:39], v[184:187], v[212:215]\n"
"buffer_load_dwordx4 v[224:227], v1, s[4:7], s98 offen offset:1024 sc1\n"
"v_mfma_f32_16x16x32_bf16 v[212:215], v[40:43], v[156:159], v[212:215]\n"
"s_add_u32 s96, s60, 0x400000\n"
"v_fmac_f32_e32 v150, v253, v162\n"
"v_fmac_f32_e32 v151, v253, v163\n"
"v_mfma_f32_16x16x32_bf16 v[212:215], v[44:47], v[192:195], v[212:215]\n"
"ds_write_b128 v3, v[180:183] offset:15360\n"
"v_mfma_f32_16x16x32_bf16 v[212:215], v[48:51], v[196:199], v[212:215]\n"
"buffer_store_dwordx4 v[120:123], v0, s[4:7], s96 offen sc1\n"
"v_mfma_f32_16x16x32_bf16 v[212:215], v[52:55], v[200:203], v[212:215]\n"
"v_add_f32_e32 v148, v148, v116\n"
"v_add_f32_e32 v149, v149, v117\n"
"v_mfma_f32_16x16x32_bf16 v[160:163], v[84:87], v[200:203], 0\n"
"buffer_load_dwordx4 v[180:183], v1, s[4:7], s98 offen offset:2048 sc1\n"
"v_mfma_f32_16x16x32_bf16 v[160:163], v[80:83], v[196:199], v[160:163]\n"
"s_add_u32 s96, s68, s80\n"
"s_add_u32 s96, s96, 0x10000\n"
"s_waitcnt vmcnt(22)\n"
"v_mfma_f32_16x16x32_bf16 v[160:163], v[76:79], v[192:195], v[160:163]\n"
"buffer_store_dwordx2 v[18:19], v11, s[4:7], s96 offen sc1\n"
"v_mfma_f32_16x16x32_bf16 v[160:163], v[72:75], v[156:159], v[160:163]\n"
"ds_read_b128 v[156:159], v4 offset:4096\n"
"ds_read_b128 v[192:195], v9 offset:512\n"
"v_mfma_f32_16x16x32_bf16 v[160:163], v[68:71], v[184:187], v[160:163]\n"
"ds_read_b128 v[184:187], v5 offset:4096\n"
"v_add_f32_e32 v150, v150, v118\n"
"v_mfma_f32_16x16x32_bf16 v[160:163], v[64:67], v[152:155], v[160:163]\n"
"buffer_load_dwordx4 v[152:155], v1, s[4:7], s98 offen offset:3072 sc1\n"
"v_mfma_f32_16x16x32_bf16 v[160:163], v[60:63], v[108:111], v[160:163]\n"
"ds_write_b128 v3, v[212:215] offset:16384\n"
"v_mfma_f32_16x16x32_bf16 v[160:163], v[56:59], v[176:179], v[160:163]\n"
"s_waitcnt vmcnt(19)\n"
"ds_read_b128 v[108:111], v4 offset:5120\n"
"ds_read_b128 v[176:179], v9 offset:576\n"
"; @B G1 0.3\n"
"v_mfma_f32_16x16x32_bf16 v[196:199], a[0:3], v[92:95], v[136:139]\n"
"buffer_load_dwordx4 a[148:151], v1, s[4:7], s97 offen offset:1024 sc1\n"
"v_mfma_f32_16x16x32_bf16 v[196:199], a[4:7], v[96:99], v[196:199]\n"
"ds_read_b128 v[200:203], v5 offset:5120\n"
"v_add_f32_e32 v151, v151, v119\n"
"v_mfma_f32_16x16x32_bf16 v[196:199], a[8:11], v[104:107], v[196:199]\n"
"s_add_u32 s98, s53, 0x448000\n"
"buffer_load_dwordx4 v[116:119], v0, s[4:7], s98 offen sc1\n"
"v_mfma_f32_16x16x32_bf16 v[196:199], a[12:15], v[112:115], v[196:199]\n"
"s_waitcnt vmcnt(15)\n"
"s_waitcnt lgkmcnt(12)\n"
"v_fmac_f32_e32 v188, v253, v204\n"
"v_mfma_f32_16x16x32_bf16 v[196:199], a[16:19], v[100:103], v[196:199]\n"
"v_fmac_f32_e32 v189, v253, v205\n"
"v_fmac_f32_e32 v190, v253, v206\n"
"v_mfma_f32_16x16x32_bf16 v[196:199], a[20:23], v[124:127], v[196:199]\n"
"ds_write_b128 v3, v[160:163] offset:17408\n"
"v_mfma_f32_16x16x32_bf16 v[196:199], a[24:27], v[168:171], v[196:199]\n"
"v_fmac_f32_e32 v191, v253, v207\n"
"v_add_f32_e32 v188, v188, v208\n"
"v_mfma_f32_16x16x32_bf16 v[196:199], a[28:31], v[172:175], v[196:199]\n"
"buffer_load_dwordx4 v[160:163], v0, s[4:7], s98 offen offset:1024 sc1\n"
"v_mfma_f32_16x16x32_bf16 v[204:207], a[60:63], v[172:175], v[148:151]\n"
"v_add_f32_e32 v189, v189, v209\n"
"v_add_f32_e32 v190, v190, v210\n"
"v_mfma_f32_16x16x32_bf16 v[204:207], a[56:59], v[168:171], v[204:207]\n"
"s_waitcnt lgkmcnt(9)\n"
"v_add_f32_e32 v191, v191, v211\n"
"v_fmac_f32_e32 v144, v253, v216\n"
"v_mfma_f32_16x16x32_bf16 v[204:207], a[52:55], v[124:127], v[204:207]\n"
"s_waitcnt lgkmcnt(0)\n"
"s_barrier\n"
"v_mfma_f32_16x16x32_bf16 v[204:207], a[48:51], v[100:103], v[204:207]\n"
"ds_read_b128 v[208:211], v6\n"
"ds_read_b128 v[212:215], v6 offset:1024\n"
"v_mfma_f32_16x16x32_bf16 v[204:207], a[44:47], v[112:115], v[204:207]\n"
"v_cmp_u_f32_e32 vcc, v196, v196\n"
"v_fmac_f32_e32 v145, v253, v217\n"
"v_mfma_f32_16x16x32_bf16 v[204:207], a[40:43], v[104:107], v[204:207]\n"
"s_cbranch_vccnz .Lmk_p51s\n"
".Lmk_p51r:\n"
"v_fmac_f32_e32 v146, v253, v218\n"
"v_mfma_f32_16x16x32_bf16 v[204:207], a[36:39], v[96:99], v[204:207]\n"
"buffer_load_dwordx4 v[136:139], v0, s[4:7], s98 offen offset:2048 sc1\n"
"v_mfma_f32_16x16x32_bf16 v[204:207], a[32:35], v[92:95], v[204:207]\n"
"v_fmac_f32_e32 v147, v253, v219\n"
"v_add_f32_e32 v144, v144, v220\n"
"v_mfma_f32_16x16x32_bf16 v[188:191], a[64:67], v[92:95], v[188:191]\n"
"v_add_f32_e32 v145, v145, v221\n"
"v_add_f32_e32 v146, v146, v222\n"
"v_mfma_f32_16x16x32_bf16 v[188:191], a[68:71], v[96:99], v[188:191]\n"
"buffer_load_dwordx4 v[148:151], v0, s[4:7], s98 offen offset:3072 sc1\n"
"v_mfma_f32_16x16x32_bf16 v[188:191], a[72:75], v[104:107], v[188:191]\n"
"v_add_f32_e32 v147, v147, v223\n"
"v_fmac_f32_e32 v156, v253, v192\n"
"v_mfma_f32_16x16x32_bf16 v[188:191], a[76:79], v[112:115], v[188:191]\n"
"v_fmac_f32_e32 v157, v253, v193\n"
"v_fmac_f32_e32 v158, v253, v194\n"
"v_mfma_f32_16x16x32_bf16 v[188:191], a[80:83], v[100:103], v[188:191]\n"
"buffer_load_dwordx4 a[152:155], v1, s[4:7], s97 offen offset:2048 sc1\n"
"v_mfma_f32_16x16x32_bf16 v[188:191], a[84:87], v[124:127], v[188:191]\n"
"s_waitcnt lgkmcnt(0)\n"
"v_fmac_f32_e32 v159, v253, v195\n"
"v_add_f32_e32 v156, v156, v184\n"
"v_mfma_f32_16x16x32_bf16 v[188:191], a[88:91], v[168:171], v[188:191]\n"
"buffer_load_dwordx4 v[192:195], v1, s[4:7], s98 offen sc1\n"
"v_mfma_f32_16x16x32_bf16 v[188:191], a[92:95], v[172:175], v[188:191]\n"
"ds_read_b128 v[216:219], v6 offset:2048\n"
"ds_read_b128 v[220:223], v6 offset:3072\n"
"v_mfma_f32_16x16x32_bf16 v[144:147], a[124:127], v[172:175], v[144:147]\n"
"v_add_f32_e32 v157, v157, v185\n"
"v_add_f32_e32 v158, v158, v186\n"
"v_mfma_f32_16x16x32_bf16 v[144:147], a[120:123], v[168:171], v[144:147]\n"
"v_add_f32_e32 v159, v159, v187\n"
"v_fmac_f32_e32 v108, v253, v176\n"
"v_mfma_f32_16x16x32_bf16 v[144:147], a[116:119], v[124:127], v[144:147]\n"
"v_fmac_f32_e32 v109, v253, v177\n"
"v_fmac_f32_e32 v110, v253, v178\n"
"v_mfma_f32_16x16x32_bf16 v[144:147], a[112:115], v[100:103], v[144:147]\n"
"buffer_load_dwordx4 v[184:187], v1, s[4:7], s98 offen offset:1024 sc1\n"
"v_mfma_f32_16x16x32_bf16 v[144:147], a[108:111], v[112:115], v[144:147]\n"
"v_fmac_f32_e32 v111, v253, v179\n"
"v_add_f32_e32 v108, v108, v200\n"
"v_mfma_f32_16x16x32_bf16 v[144:147], a[104:107], v[104:107], v[144:147]\n"
"v_add_f32_e32 v109, v109, v201\n"
"v_add_f32_e32 v110, v110, v202\n"
"v_mfma_f32_16x16x32_bf16 v[144:147], a[100:103], v[96:99], v[144:147]\n"
"v_add_f32_e32 v111, v111, v203\n"
"v_add_f32_e32 v196, v196, v208\n"
"v_mfma_f32_16x16x32_bf16 v[144:147], a[96:99], v[92:95], v[144:147]\n"
"v_add_f32_e32 v197, v197, v209\n"
"v_add_f32_e32 v198, v198, v210\n"
"v_mfma_f32_16x16x32_bf16 v[156:159], v[24:27], v[92:95], v[156:159]\n"
"buffer_load_dwordx4 v[176:179], v1, s[4:7], s98 offen offset:2048 sc1\n"
"v_mfma_f32_16x16x32_bf16 v[156:159], v[28:31], v[96:99], v[156:159]\n"
"v_add_f32_e32 v199, v199, v211\n"
"v_add_f32_e32 v204, v204, v212\n"
"v_mfma_f32_16x16x32_bf16 v[156:159], v[32:35], v[104:107], v[156:159]\n"
"v_add_f32_e32 v205, v205, v213\n"
"v_add_f32_e32 v206, v206, v214\n"
"v_mfma_f32_16x16x32_bf16 v[156:159], v[36:39], v[112:115], v[156:159]\n"
"v_add_f32_e32 v207, v207, v215\n"
"v_mul_f32_e32 v196, v196, v255\n"
"v_mfma_f32_16x16x32_bf16 v[156:159], v[40:43], v[100:103], v[156:159]\n"
"s_waitcnt lgkmcnt(0)\n"
"v_mul_f32_e32 v197, v197, v255\n"
"v_mul_f32_e32 v198, v198, v255\n"
"v_mfma_f32_16x16x32_bf16 v[156:159], v[44:47], v[124:127], v[156:159]\n"
"buffer_load_dwordx4 v[200:203], v1, s[4:7], s98 offen offset:3072 sc1\n"
"v_mfma_f32_16x16x32_bf16 v[156:159], v[48:51], v[168:171], v[156:159]\n"
"s_add_u32 s98, s60, 0x408000\n"
"ds_read_b128 v[208:211], v6 offset:4096\n"
"ds_read_b128 v[212:215], v6 offset:5120\n"
"v_mfma_f32_16x16x32_bf16 v[156:159], v[52:55], v[172:175], v[156:159]\n"
"v_mul_f32_e32 v199, v199, v255\n"
"v_mul_f32_e32 v204, v204, v255\n"
"v_mfma_f32_16x16x32_bf16 v[108:111], v[84:87], v[172:175], v[108:111]\n"
"buffer_store_dwordx4 v[120:123], v0, s[4:7], s98 offen sc1\n"
"v_mfma_f32_16x16x32_bf16 v[108:111], v[80:83], v[168:171], v[108:111]\n"
"v_mul_f32_e32 v205, v205, v255\n"
"v_mul_f32_e32 v206, v206, v255\n"
"v_mfma_f32_16x16x32_bf16 v[108:111], v[76:79], v[124:127], v[108:111]\n"
"v_mul_f32_e32 v207, v207, v255\n"
"v_cvt_pk_bf16_f32 v120, v196, v197\n"
"v_mfma_f32_16x16x32_bf16 v[108:111], v[72:75], v[100:103], v[108:111]\n"
"v_cvt_pk_bf16_f32 v121, v198, v199\n"
"v_cvt_pk_bf16_f32 v122, v204, v205\n"
"v_mfma_f32_16x16x32_bf16 v[108:111], v[68:71], v[112:115], v[108:111]\n"
"v_cvt_pk_bf16_f32 v123, v206, v207\n"
"v_add_f32_e32 v188, v188, v216\n"
"v_mfma_f32_16x16x32_bf16 v[108:111], v[64:67], v[104:107], v[108:111]\n"
"ds_write_b128 v7, v[120:123] offset:24576\n"
"v_mfma_f32_16x16x32_bf16 v[108:111], v[60:63], v[96:99], v[108:111]\n"
"v_add_f32_e32 v189, v189, v217\n"
"v_add_f32_e32 v190, v190, v218\n"
"v_mfma_f32_16x16x32_bf16 v[108:111], v[56:59], v[92:95], v[108:111]\n"
"v_add_f32_e32 v191, v191, v219\n"
"v_add_f32_e32 v144, v144, v220\n"
"v_add_f32_e32 v145, v145, v221\n"
"v_add_f32_e32 v146, v146, v222\n"
"v_add_f32_e32 v147, v147, v223\n"
"v_mul_f32_e32 v188, v188, v255\n"
"v_mul_f32_e32 v189, v189, v255\n"
"v_mul_f32_e32 v190, v190, v255\n"
"v_mul_f32_e32 v191, v191, v255\n"
"v_mul_f32_e32 v144, v144, v255\n"
"v_mul_f32_e32 v145, v145, v255\n"
"v_mul_f32_e32 v146, v146, v255\n"
"v_mul_f32_e32 v147, v147, v255\n"
"v_cvt_pk_bf16_f32 v92, v188, v189\n"
"v_cvt_pk_bf16_f32 v93, v190, v191\n"
"v_cvt_pk_bf16_f32 v94, v144, v145\n"
"v_cvt_pk_bf16_f32 v95, v146, v147\n"
"ds_write_b128 v7, v[92:95] offset:57344\n"
".Lmk_b1:\n"
"s_waitcnt vmcnt(18)\n"
"; @B G1 1.0\n"
"v_mfma_f32_16x16x32_bf16 v[92:95], a[0:3], v[128:131], 0\n"
"v_mfma_f32_16x16x32_bf16 v[92:95], a[4:7], v[88:91], v[92:95]\n"
"s_add_u32 s98, s62, 0x44800\n"
"v_mfma_f32_16x16x32_bf16 v[92:95], a[8:11], v[164:167], v[92:95]\n"
"s_add_u32 s96, s54, 0x448000\n"
"buffer_load_dwordx4 v[96:99], v0, s[4:7], s96 offen sc1\n"
"v_mfma_f32_16x16x32_bf16 v[92:95], a[12:15], v[140:143], v[92:95]\n"
"s_waitcnt vmcnt(12)\n"
"v_mfma_f32_16x16x32_bf16 v[92:95], a[16:19], v[132:135], v[92:95]\n"
"buffer_load_dword v255, v10, s[4:7], s98 offen sc1\n"
"v_mfma_f32_16x16x32_bf16 v[92:95], a[20:23], v[224:227], v[92:95]\n"
"s_waitcnt lgkmcnt(2)\n"
"v_add_f32_e32 v156, v156, v208\n"
"v_add_f32_e32 v157, v157, v209\n"
"v_mfma_f32_16x16x32_bf16 v[92:95], a[24:27], v[180:183], v[92:95]\n"
"v_add_f32_e32 v158, v158, v210\n"
"v_add_f32_e32 v159, v159, v211\n"
"v_mfma_f32_16x16x32_bf16 v[92:95], a[28:31], v[152:155], v[92:95]\n"
"buffer_load_dwordx4 v[100:103], v0, s[4:7], s96 offen offset:1024 sc1\n"
"v_mfma_f32_16x16x32_bf16 v[104:107], a[60:63], v[152:155], 0\n"
"buffer_load_dwordx4 a[156:159], v1, s[4:7], s97 offen offset:3072 sc1\n"
"v_mfma_f32_16x16x32_bf16 v[104:107], a[56:59], v[180:183], v[104:107]\n"
"buffer_load_dword v253, v10, s[4:7], s98 offen offset:256 sc1\n"
"v_mfma_f32_16x16x32_bf16 v[104:107], a[52:55], v[224:227], v[104:107]\n"
"v_add_f32_e32 v108, v108, v212\n"
"v_add_f32_e32 v109, v109, v213\n"
"v_mfma_f32_16x16x32_bf16 v[104:107], a[48:51], v[132:135], v[104:107]\n"
"v_add_f32_e32 v110, v110, v214\n"
"v_add_f32_e32 v111, v111, v215\n"
"v_mfma_f32_16x16x32_bf16 v[104:107], a[44:47], v[140:143], v[104:107]\n"
"v_cmp_u_f32_e32 vcc, v92, v92\n"
"v_mul_f32_e32 v156, v156, v254\n"
"v_mfma_f32_16x16x32_bf16 v[104:107], a[40:43], v[164:167], v[104:107]\n"
"s_cbranch_vccnz .Lmk_p53s\n"
".Lmk_p53r:\n"
"v_mul_f32_e32 v157, v157, v254\n"
"v_mfma_f32_16x16x32_bf16 v[104:107], a[36:39], v[88:91], v[104:107]\n"
"buffer_load_dwordx4 v[112:115], v0, s[4:7], s96 offen offset:2048 sc1\n"
"v_mfma_f32_16x16x32_bf16 v[104:107], a[32:35], v[128:131], v[104:107]\n"
"ds_write_b128 v3, v[92:95]\n"
"v_mfma_f32_16x16x32_bf16 v[92:95], a[64:67], v[128:131], 0\n"
"buffer_load_dword v252, v10, s[4:7], s98 offen offset:512 sc1\n"
"v_mfma_f32_16x16x32_bf16 v[92:95], a[68:71], v[88:91], v[92:95]\n"
"buffer_load_dwordx4 v[120:123], v0, s[4:7], s96 offen offset:3072 sc1\n"
"v_mfma_f32_16x16x32_bf16 v[92:95], a[72:75], v[164:167], v[92:95]\n"
"v_mul_f32_e32 v158, v158, v254\n"
"v_mul_f32_e32 v159, v159, v254\n"
"v_mfma_f32_16x16x32_bf16 v[92:95], a[76:79], v[140:143], v[92:95]\n"
"buffer_load_dword v251, v10, s[4:7], s98 offen offset:768 sc1\n"
"v_mfma_f32_16x16x32_bf16 v[92:95], a[80:83], v[132:135], v[92:95]\n"
"v_mul_f32_e32 v108, v108, v254\n"
"v_mul_f32_e32 v109, v109, v254\n"
"v_mfma_f32_16x16x32_bf16 v[92:95], a[84:87], v[224:227], v[92:95]\n"
"ds_write_b128 v3, v[104:107] offset:1024\n"
"v_mfma_f32_16x16x32_bf16 v[92:95], a[88:91], v[180:183], v[92:95]\n"
"buffer_load_dwordx4 v[104:107], v1, s[4:7], s96 offen sc1\n"
"v_mfma_f32_16x16x32_bf16 v[92:95], a[92:95], v[152:155], v[92:95]\n"
"buffer_load_dword v250, v10, s[4:7], s98 offen offset:1024 sc1\n"
"v_mfma_f32_16x16x32_bf16 v[124:127], a[124:127], v[152:155], 0\n"
"v_mul_f32_e32 v110, v110, v254\n"
"v_mul_f32_e32 v111, v111, v254\n"
"v_mfma_f32_16x16x32_bf16 v[124:127], a[120:123], v[180:183], v[124:127]\n"
"v_cvt_pk_bf16_f32 v144, v156, v157\n"
"v_cvt_pk_bf16_f32 v145, v158, v159\n"
"v_mfma_f32_16x16x32_bf16 v[124:127], a[116:119], v[224:227], v[124:127]\n"
"s_add_u32 s97, s70, 0x202000\n"
"buffer_load_dword v254, v10, s[4:7], s98 offen offset:1280 sc1\n"
"v_mfma_f32_16x16x32_bf16 v[124:127], a[112:115], v[132:135], v[124:127]\n"
"buffer_load_dwordx4 v[156:159], v1, s[4:7], s96 offen offset:1024 sc1\n"
"v_mfma_f32_16x16x32_bf16 v[124:127], a[108:111], v[140:143], v[124:127]\n"
"buffer_load_dwordx4 a[160:163], v0, s[4:7], s97 offen sc1\n"
"v_mfma_f32_16x16x32_bf16 v[124:127], a[104:107], v[164:167], v[124:127]\n"
"ds_write_b128 v3, v[92:95] offset:2048\n"
"v_mfma_f32_16x16x32_bf16 v[124:127], a[100:103], v[88:91], v[124:127]\n"
"buffer_load_dword v249, v10, s[4:7], s98 offen offset:1536 sc1\n"
"v_mfma_f32_16x16x32_bf16 v[124:127], a[96:99], v[128:131], v[124:127]\n"
"v_cvt_pk_bf16_f32 v146, v108, v109\n"
"v_cvt_pk_bf16_f32 v147, v110, v111\n"
"v_mfma_f32_16x16x32_bf16 v[92:95], v[24:27], v[128:131], 0\n"
"buffer_load_dwordx4 v[108:111], v1, s[4:7], s96 offen offset:2048 sc1\n"
"v_mfma_f32_16x16x32_bf16 v[92:95], v[28:31], v[88:91], v[92:95]\n"
"buffer_load_dword v248, v10, s[4:7], s98 offen offset:1792 sc1\n"
"v_mfma_f32_16x16x32_bf16 v[92:95], v[32:35], v[164:167], v[92:95]\n"
"ds_write_b128 v2, v[144:147] offset:49152\n"
"v_mfma_f32_16x16x32_bf16 v[92:95], v[36:39], v[140:143], v[92:95]\n"
"v_mfma_f32_16x16x32_bf16 v[92:95], v[40:43], v[132:135], v[92:95]\n"
"v_mfma_f32_16x16x32_bf16 v[92:95], v[44:47], v[224:227], v[92:95]\n"
"buffer_load_dwordx4 v[144:147], v1, s[4:7], s96 offen offset:3072 sc1\n"
"v_mfma_f32_16x16x32_bf16 v[92:95], v[48:51], v[180:183], v[92:95]\n"
"ds_write_b128 v3, v[124:127] offset:3072\n"
"v_mfma_f32_16x16x32_bf16 v[92:95], v[52:55], v[152:155], v[92:95]\n"
"v_mfma_f32_16x16x32_bf16 v[124:127], v[84:87], v[152:155], 0\n"
"v_mfma_f32_16x16x32_bf16 v[124:127], v[80:83], v[180:183], v[124:127]\n"
"v_mfma_f32_16x16x32_bf16 v[124:127], v[76:79], v[224:227], v[124:127]\n"
"v_mfma_f32_16x16x32_bf16 v[124:127], v[72:75], v[132:135], v[124:127]\n"
"v_mfma_f32_16x16x32_bf16 v[124:127], v[68:71], v[140:143], v[124:127]\n"
"v_mfma_f32_16x16x32_bf16 v[124:127], v[64:67], v[164:167], v[124:127]\n"
"s_nop 1\n"
"ds_write_b128 v3, v[92:95] offset:4096\n"
"v_mfma_f32_16x16x32_bf16 v[124:127], v[60:63], v[88:91], v[124:127]\n"
"v_mfma_f32_16x16x32_bf16 v[124:127], v[56:59], v[128:131], v[124:127]\n"
"s_waitcnt vmcnt(24)\n"
"; @B G1 1.1\n"
"v_mfma_f32_16x16x32_bf16 v[88:91], a[0:3], v[116:119], 0\n"
"buffer_load_dwordx4 a[164:167], v0, s[4:7], s97 offen offset:1024 sc1\n"
"v_mfma_f32_16x16x32_bf16 v[88:91], a[4:7], v[160:163], v[88:91]\n"
"v_mfma_f32_16x16x32_bf16 v[88:91], a[8:11], v[136:139], v[88:91]\n"
"s_add_u32 s98, s55, 0x448000\n"
"buffer_load_dwordx4 v[92:95], v0, s[4:7], s98 offen sc1\n"
"v_mfma_f32_16x16x32_bf16 v[88:91], a[12:15], v[148:151], v[88:91]\n"
"s_waitcnt vmcnt(21)\n"
"v_mfma_f32_16x16x32_bf16 v[88:91], a[16:19], v[192:195], v[88:91]\n"
"v_mfma_f32_16x16x32_bf16 v[88:91], a[20:23], v[184:187], v[88:91]\n"
"ds_write_b128 v3, v[124:127] offset:5120\n"
"v_mfma_f32_16x16x32_bf16 v[88:91], a[24:27], v[176:179], v[88:91]\n"
"v_mfma_f32_16x16x32_bf16 v[88:91], a[28:31], v[200:203], v[88:91]\n"
"buffer_load_dwordx4 v[124:127], v0, s[4:7], s98 offen offset:1024 sc1\n"
"v_mfma_f32_16x16x32_bf16 v[128:131], a[60:63], v[200:203], 0\n"
"v_mfma_f32_16x16x32_bf16 v[128:131], a[56:59], v[176:179], v[128:131]\n"
"v_mfma_f32_16x16x32_bf16 v[128:131], a[52:55], v[184:187], v[128:131]\n"
"v_mfma_f32_16x16x32_bf16 v[128:131], a[48:51], v[192:195], v[128:131]\n"
"v_mfma_f32_16x16x32_bf16 v[128:131], a[44:47], v[148:151], v[128:131]\n"
"s_add_u32 s96, s67, 0x10000\n"
"s_nop 0\n"
"v_cmp_u_f32_e32 vcc, v88, v88\n"
"v_mfma_f32_16x16x32_bf16 v[128:131], a[40:43], v[136:139], v[128:131]\n"
"s_cbranch_vccnz .Lmk_p54s\n"
".Lmk_p54r:\n"
"v_mfma_f32_16x16x32_bf16 v[128:131], a[36:39], v[160:163], v[128:131]\n"
"buffer_load_dwordx4 v[132:135], v0, s[4:7], s98 offen offset:2048 sc1\n"
"v_mfma_f32_16x16x32_bf16 v[128:131], a[32:35], v[116:119], v[128:131]\n"
"ds_write_b128 v3, v[88:91] offset:6144\n"
"v_mfma_f32_16x16x32_bf16 v[88:91], a[64:67], v[116:119], 0\n"
"buffer_load_dwordx2 v[246:247], v12, s[4:7], s96 offen sc1\n"
"v_mfma_f32_16x16x32_bf16 v[88:91], a[68:71], v[160:163], v[88:91]\n"
"buffer_load_dwordx4 v[140:143], v0, s[4:7], s98 offen offset:3072 sc1\n"
"v_mfma_f32_16x16x32_bf16 v[88:91], a[72:75], v[136:139], v[88:91]\n"
"v_mfma_f32_16x16x32_bf16 v[88:91], a[76:79], v[148:151], v[88:91]\n"
"v_mfma_f32_16x16x32_bf16 v[88:91], a[80:83], v[192:195], v[88:91]\n"
"v_mov_b32_e32 v152, -1\n"
"v_mov_b32_e32 v153, -1\n"
"v_mfma_f32_16x16x32_bf16 v[88:91], a[84:87], v[184:187], v[88:91]\n"
"ds_write_b128 v3, v[128:131] offset:7168\n"
"v_mfma_f32_16x16x32_bf16 v[88:91], a[88:91], v[176:179], v[88:91]\n"
"buffer_load_dwordx4 v[128:131], v1, s[4:7], s98 offen sc1\n"
"v_mfma_f32_16x16x32_bf16 v[88:91], a[92:95], v[200:203], v[88:91]\n"
"v_mov_b32_e32 v154, -1\n"
"v_mov_b32_e32 v155, -1\n"
"v_mfma_f32_16x16x32_bf16 v[164:167], a[124:127], v[200:203], 0\n"
"s_add_u32 s95, s61, 0x840000\n"
"buffer_store_dwordx4 v[152:155], v0, s[8:11], s95 offen sc1\n"
"v_mfma_f32_16x16x32_bf16 v[164:167], a[120:123], v[176:179], v[164:167]\n"
"buffer_load_dwordx4 a[168:171], v0, s[4:7], s97 offen offset:2048 sc1\n"
"v_mfma_f32_16x16x32_bf16 v[164:167], a[116:119], v[184:187], v[164:167]\n"
"v_mfma_f32_16x16x32_bf16 v[164:167], a[112:115], v[192:195], v[164:167]\n"
"buffer_load_dwordx4 v[168:171], v1, s[4:7], s98 offen offset:1024 sc1\n"
"v_mfma_f32_16x16x32_bf16 v[164:167], a[108:111], v[148:151], v[164:167]\n"
"v_mfma_f32_16x16x32_bf16 v[164:167], a[104:107], v[136:139], v[164:167]\n"
"ds_write_b128 v3, v[88:91] offset:8192\n"
"v_mfma_f32_16x16x32_bf16 v[164:167], a[100:103], v[160:163], v[164:167]\n"
"v_mfma_f32_16x16x32_bf16 v[164:167], a[96:99], v[116:119], v[164:167]\n"
"v_mfma_f32_16x16x32_bf16 v[88:91], v[24:27], v[116:119], 0\n"
"buffer_load_dwordx4 v[172:175], v1, s[4:7], s98 offen offset:2048 sc1\n"
"v_mfma_f32_16x16x32_bf16 v[88:91], v[28:31], v[160:163], v[88:91]\n"
"v_mfma_f32_16x16x32_bf16 v[88:91], v[32:35], v[136:139], v[88:91]\n"
"v_mfma_f32_16x16x32_bf16 v[88:91], v[36:39], v[148:151], v[88:91]\n"
"v_mfma_f32_16x16x32_bf16 v[88:91], v[40:43], v[192:195], v[88:91]\n"
"s_waitcnt vmcnt(12)\n"
"v_add_f32_e32 v255, v255, v250\n"
"v_add_f32_e32 v253, v253, v254\n"
"v_mfma_f32_16x16x32_bf16 v[88:91], v[44:47], v[184:187], v[88:91]\n"
"buffer_load_dwordx4 v[180:183], v1, s[4:7], s98 offen offset:3072 sc1\n"
"v_mfma_f32_16x16x32_bf16 v[88:91], v[48:51], v[176:179], v[88:91]\n"
"ds_write_b128 v3, v[164:167] offset:9216\n"
"v_mfma_f32_16x16x32_bf16 v[88:91], v[52:55], v[200:203], v[88:91]\n"
"v_add_f32_e32 v252, v252, v249\n"
"v_add_f32_e32 v251, v251, v248\n"
"v_mfma_f32_16x16x32_bf16 v[164:167], v[84:87], v[200:203], 0\n"
"v_add_f32_e32 v255, v255, v252\n"
"v_add_f32_e32 v253, v253, v251\n"
"v_mfma_f32_16x16x32_bf16 v[164:167], v[80:83], v[176:179], v[164:167]\n"
"v_add_f32_e32 v255, v255, v253\n"
"v_cmp_u_f32_e32 vcc, v255, v255\n"
"v_mfma_f32_16x16x32_bf16 v[164:167], v[76:79], v[184:187], v[164:167]\n"
"s_cbranch_vccnz .Lmk_m57s\n"
".Lmk_m57r:\n"
"v_mov_b32_e32 v253, v255\n"
"v_mfma_f32_16x16x32_bf16 v[164:167], v[72:75], v[192:195], v[164:167]\n"
"s_nop 0\n"
"v_permlane32_swap_b32_e32 v255, v253\n"
"v_add_f32_e32 v255, v255, v253\n"
"v_mfma_f32_16x16x32_bf16 v[164:167], v[68:71], v[148:151], v[164:167]\n"
"v_mul_f32_e32 v255, 0x3a800000, v255\n"
"v_mov_b32_e32 v253, v255\n"
"v_mfma_f32_16x16x32_bf16 v[164:167], v[64:67], v[136:139], v[164:167]\n"
"ds_write_b128 v3, v[88:91] offset:10240\n"
"v_mfma_f32_16x16x32_bf16 v[164:167], v[60:63], v[160:163], v[164:167]\n"
"buffer_load_dwordx4 a[172:175], v0, s[4:7], s97 offen offset:3072 sc1\n"
"v_mfma_f32_16x16x32_bf16 v[164:167], v[56:59], v[116:119], v[164:167]\n"
"v_permlane16_swap_b32_e32 v255, v253\n"
"v_cvt_pk_bf16_f32 v254, -v255, 0\n"
"; @B G1 1.2\n"
"v_mfma_f32_16x16x32_bf16 v[88:91], a[0:3], v[96:99], 0\n"
"v_fma_f32 v253, -v255, v255, v253\n"
"v_lshlrev_b32_e32 v254, 16, v254\n"
"v_mfma_f32_16x16x32_bf16 v[88:91], a[4:7], v[100:103], v[88:91]\n"
"s_waitcnt vmcnt(8)\n"
"v_cmp_ne_u64_e32 vcc, v[246:247], v[18:19]\n"
"v_mfma_f32_16x16x32_bf16 v[88:91], a[8:11], v[112:115], v[88:91]\n"
"s_cbranch_vccnz .Lmk_f44s\n"
".Lmk_f44r:\n"
"v_max_f32_e32 v253, 0, v253\n"
"v_mfma_f32_16x16x32_bf16 v[88:91], a[12:15], v[120:123], v[88:91]\n"
"v_cvt_pk_bf16_f32 v253, v253, 0\n"
"v_lshlrev_b32_e32 v253, 16, v253\n"
"v_mfma_f32_16x16x32_bf16 v[88:91], a[16:19], v[104:107], v[88:91]\n"
"v_add_f32_e32 v253, 0x3727c5ac, v253\n"
"v_cvt_pk_bf16_f32 v253, v253, 0\n"
"v_mfma_f32_16x16x32_bf16 v[88:91], a[20:23], v[156:159], v[88:91]\n"
"ds_write_b128 v3, v[164:167] offset:11264\n"
"v_mfma_f32_16x16x32_bf16 v[88:91], a[24:27], v[108:111], v[88:91]\n"
"v_lshlrev_b32_e32 v253, 16, v253\n"
"v_mfma_f32_16x16x32_bf16 v[88:91], a[28:31], v[144:147], v[88:91]\n"
"v_sqrt_f32_e32 v253, v253\n"
"v_mfma_f32_16x16x32_bf16 v[116:119], a[60:63], v[144:147], 0\n"
"v_cvt_pk_bf16_f32 v253, v253, 0\n"
"v_lshlrev_b32_e32 v253, 16, v253\n"
"v_mfma_f32_16x16x32_bf16 v[116:119], a[56:59], v[108:111], v[116:119]\n"
"v_rcp_f32_e32 v255, v253\n"
"v_mfma_f32_16x16x32_bf16 v[116:119], a[52:55], v[156:159], v[116:119]\n"
"s_add_u32 s98, s52, 0x408000\n"
"buffer_load_dwordx4 v[136:139], v0, s[4:7], s98 offen sc1\n"
"v_mfma_f32_16x16x32_bf16 v[116:119], a[48:51], v[104:107], v[116:119]\n"
"s_waitcnt lgkmcnt(0)\n"
"s_barrier\n"
"v_mfma_f32_16x16x32_bf16 v[116:119], a[44:47], v[120:123], v[116:119]\n"
"v_cmp_u_f32_e32 vcc, v88, v88\n"
"v_mul_f32_e32 v253, 0x3e8293ee, v255\n"
"v_mfma_f32_16x16x32_bf16 v[116:119], a[40:43], v[112:115], v[116:119]\n"
"s_cbranch_vccnz .Lmk_p55s\n"
".Lmk_p55r:\n"
"ds_read_b128 v[148:151], v4\n"
"v_mfma_f32_16x16x32_bf16 v[116:119], a[36:39], v[100:103], v[116:119]\n"
"ds_write_b128 v3, v[88:91] offset:12288\n"
"v_mfma_f32_16x16x32_bf16 v[116:119], a[32:35], v[96:99], v[116:119]\n"
"buffer_load_dwordx4 v[88:91], v0, s[4:7], s98 offen offset:1024 sc1\n"
"v_mfma_f32_16x16x32_bf16 v[160:163], a[64:67], v[96:99], 0\n"
"ds_read_b128 v[164:167], v9 offset:640\n"
"ds_read_b128 v[176:179], v5\n"
"v_mfma_f32_16x16x32_bf16 v[160:163], a[68:71], v[100:103], v[160:163]\n"
"v_mfma_f32_16x16x32_bf16 v[160:163], a[72:75], v[112:115], v[160:163]\n"
"buffer_load_dwordx4 a[176:179], v1, s[4:7], s97 offen sc1\n"
"v_mfma_f32_16x16x32_bf16 v[160:163], a[76:79], v[120:123], v[160:163]\n"
"ds_read_b128 v[184:187], v4 offset:1024\n"
"ds_read_b128 v[188:191], v9 offset:704\n"
"v_mfma_f32_16x16x32_bf16 v[160:163], a[80:83], v[104:107], v[160:163]\n"
"buffer_load_dwordx4 v[192:195], v0, s[4:7], s98 offen offset:2048 sc1\n"
"v_mfma_f32_16x16x32_bf16 v[160:163], a[84:87], v[156:159], v[160:163]\n"
"ds_write_b128 v3, v[116:119] offset:13312\n"
"v_mfma_f32_16x16x32_bf16 v[160:163], a[88:91], v[108:111], v[160:163]\n"
"ds_read_b128 v[116:119], v5 offset:1024\n"
"s_waitcnt lgkmcnt(4)\n"
"v_mfma_f32_16x16x32_bf16 v[160:163], a[92:95], v[144:147], v[160:163]\n"
"v_fmac_f32_e32 v148, v254, v164\n"
"v_fmac_f32_e32 v149, v254, v165\n"
"v_mfma_f32_16x16x32_bf16 v[196:199], a[124:127], v[144:147], 0\n"
"v_fmac_f32_e32 v150, v254, v166\n"
"v_fmac_f32_e32 v151, v254, v167\n"
"v_mfma_f32_16x16x32_bf16 v[196:199], a[120:123], v[108:111], v[196:199]\n"
"buffer_load_dwordx4 v[164:167], v0, s[4:7], s98 offen offset:3072 sc1\n"
"v_mfma_f32_16x16x32_bf16 v[196:199], a[116:119], v[156:159], v[196:199]\n"
"ds_read_b128 v[200:203], v4 offset:2048\n"
"ds_read_b128 v[204:207], v9 offset:768\n"
"v_mfma_f32_16x16x32_bf16 v[196:199], a[112:115], v[104:107], v[196:199]\n"
"ds_read_b128 v[208:211], v5 offset:2048\n"
"v_add_f32_e32 v148, v148, v176\n"
"v_mfma_f32_16x16x32_bf16 v[196:199], a[108:111], v[120:123], v[196:199]\n"
"v_add_f32_e32 v149, v149, v177\n"
"v_add_f32_e32 v150, v150, v178\n"
"v_mfma_f32_16x16x32_bf16 v[196:199], a[104:107], v[112:115], v[196:199]\n"
"ds_write_b128 v3, v[160:163] offset:14336\n"
"v_mfma_f32_16x16x32_bf16 v[196:199], a[100:103], v[100:103], v[196:199]\n"
"buffer_load_dwordx4 v[160:163], v1, s[4:7], s98 offen sc1\n"
"v_mfma_f32_16x16x32_bf16 v[196:199], a[96:99], v[96:99], v[196:199]\n"
"s_add_u32 s96, s61, 0x840000\n"
"v_add_f32_e32 v151, v151, v179\n"
"s_waitcnt lgkmcnt(4)\n"
"v_mfma_f32_16x16x32_bf16 v[176:179], v[24:27], v[96:99], 0\n"
"buffer_store_dwordx4 v[152:155], v0, s[8:11], s96 offen offset:1024 sc1\n"
"v_mfma_f32_16x16x32_bf16 v[176:179], v[28:31], v[100:103], v[176:179]\n"
"ds_read_b128 v[212:215], v4 offset:3072\n"
"ds_read_b128 v[216:219], v9 offset:832\n"
"v_mfma_f32_16x16x32_bf16 v[176:179], v[32:35], v[112:115], v[176:179]\n"
"ds_read_b128 v[220:223], v5 offset:3072\n"
"v_fmac_f32_e32 v184, v254, v188\n"
"v_mfma_f32_16x16x32_bf16 v[176:179], v[36:39], v[120:123], v[176:179]\n"
"buffer_load_dwordx4 v[224:227], v1, s[4:7], s98 offen offset:1024 sc1\n"
"v_mfma_f32_16x16x32_bf16 v[176:179], v[40:43], v[104:107], v[176:179]\n"
"v_fmac_f32_e32 v185, v254, v189\n"
"v_fmac_f32_e32 v186, v254, v190\n"
"v_mfma_f32_16x16x32_bf16 v[176:179], v[44:47], v[156:159], v[176:179]\n"
"ds_write_b128 v3, v[196:199] offset:15360\n"
"v_mfma_f32_16x16x32_bf16 v[176:179], v[48:51], v[108:111], v[176:179]\n"
"buffer_load_dwordx4 a[180:183], v1, s[4:7], s97 offen offset:1024 sc1\n"
"v_mfma_f32_16x16x32_bf16 v[176:179], v[52:55], v[144:147], v[176:179]\n"
"v_fmac_f32_e32 v187, v254, v191\n"
"v_add_f32_e32 v184, v184, v116\n"
"v_mfma_f32_16x16x32_bf16 v[188:191], v[84:87], v[144:147], 0\n"
"buffer_load_dwordx4 v[144:147], v1, s[4:7], s98 offen offset:2048 sc1\n"
"v_mfma_f32_16x16x32_bf16 v[188:191], v[80:83], v[108:111], v[188:191]\n"
"ds_read_b128 v[108:111], v4 offset:4096\n"
"ds_read_b128 v[196:199], v9 offset:512\n"
"v_mfma_f32_16x16x32_bf16 v[188:191], v[76:79], v[156:159], v[188:191]\n"
"ds_read_b128 v[156:159], v5 offset:4096\n"
"v_add_f32_e32 v185, v185, v117\n"
"v_mfma_f32_16x16x32_bf16 v[188:191], v[72:75], v[104:107], v[188:191]\n"
"s_waitcnt lgkmcnt(8)\n"
"v_add_f32_e32 v186, v186, v118\n"
"v_add_f32_e32 v187, v187, v119\n"
"v_mfma_f32_16x16x32_bf16 v[188:191], v[68:71], v[120:123], v[188:191]\n"
"v_fmac_f32_e32 v200, v254, v204\n"
"v_fmac_f32_e32 v201, v254, v205\n"
"v_mfma_f32_16x16x32_bf16 v[188:191], v[64:67], v[112:115], v[188:191]\n"
"buffer_load_dwordx4 v[104:107], v1, s[4:7], s98 offen offset:3072 sc1\n"
"v_mfma_f32_16x16x32_bf16 v[188:191], v[60:63], v[100:103], v[188:191]\n"
"ds_write_b128 v3, v[176:179] offset:16384\n"
"v_mfma_f32_16x16x32_bf16 v[188:191], v[56:59], v[96:99], v[188:191]\n"
"s_waitcnt vmcnt(18)\n"
"ds_read_b128 v[96:99], v4 offset:5120\n"
"ds_read_b128 v[100:103], v9 offset:576\n"
"; @B G1 1.3\n"
"v_mfma_f32_16x16x32_bf16 v[112:115], a[0:3], v[92:95], v[148:151]\n"
"ds_read_b128 v[116:119], v5 offset:5120\n"
"v_fmac_f32_e32 v202, v254, v206\n"
"v_mfma_f32_16x16x32_bf16 v[112:115], a[4:7], v[124:127], v[112:115]\n"
"v_fmac_f32_e32 v203, v254, v207\n"
"v_add_f32_e32 v200, v200, v208\n"
"v_mfma_f32_16x16x32_bf16 v[112:115], a[8:11], v[132:135], v[112:115]\n"
"s_add_u32 s98, s53, 0x408000\n"
"buffer_load_dwordx4 v[120:123], v0, s[4:7], s98 offen sc1\n"
"v_mfma_f32_16x16x32_bf16 v[112:115], a[12:15], v[140:143], v[112:115]\n"
"s_waitcnt vmcnt(13)\n"
"v_add_f32_e32 v201, v201, v209\n"
"v_add_f32_e32 v202, v202, v210\n"
"v_mfma_f32_16x16x32_bf16 v[112:115], a[16:19], v[128:131], v[112:115]\n"
"s_waitcnt lgkmcnt(8)\n"
"v_add_f32_e32 v203, v203, v211\n"
"v_fmac_f32_e32 v212, v254, v216\n"
"v_mfma_f32_16x16x32_bf16 v[112:115], a[20:23], v[168:171], v[112:115]\n"
"ds_write_b128 v3, v[188:191] offset:17408\n"
"v_mfma_f32_16x16x32_bf16 v[112:115], a[24:27], v[172:175], v[112:115]\n"
"v_fmac_f32_e32 v213, v254, v217\n"
"v_fmac_f32_e32 v214, v254, v218\n"
"v_mfma_f32_16x16x32_bf16 v[112:115], a[28:31], v[180:183], v[112:115]\n"
"buffer_load_dwordx4 v[176:179], v0, s[4:7], s98 offen offset:1024 sc1\n"
"v_mfma_f32_16x16x32_bf16 v[188:191], a[60:63], v[180:183], v[184:187]\n"
"v_fmac_f32_e32 v215, v254, v219\n"
"v_add_f32_e32 v212, v212, v220\n"
"v_mfma_f32_16x16x32_bf16 v[188:191], a[56:59], v[172:175], v[188:191]\n"
"v_add_f32_e32 v213, v213, v221\n"
"v_add_f32_e32 v214, v214, v222\n"
"v_mfma_f32_16x16x32_bf16 v[188:191], a[52:55], v[168:171], v[188:191]\n"
"s_waitcnt lgkmcnt(0)\n"
"s_barrier\n"
"v_mfma_f32_16x16x32_bf16 v[188:191], a[48:51], v[128:131], v[188:191]\n"
"buffer_load_dwordx4 a[184:187], v1, s[4:7], s97 offen offset:2048 sc1\n"
"v_mfma_f32_16x16x32_bf16 v[188:191], a[44:47], v[140:143], v[188:191]\n"
"ds_read_b128 v[204:207], v6\n"
"v_cmp_u_f32_e32 vcc, v112, v112\n"
"v_mfma_f32_16x16x32_bf16 v[188:191], a[40:43], v[132:135], v[188:191]\n"
"s_cbranch_vccnz .Lmk_p56s\n"
".Lmk_p56r:\n"
"ds_read_b128 v[148:151], v6 offset:1024\n"
"v_mfma_f32_16x16x32_bf16 v[188:191], a[36:39], v[124:127], v[188:191]\n"
"buffer_load_dwordx4 v[184:187], v0, s[4:7], s98 offen offset:2048 sc1\n"
"v_mfma_f32_16x16x32_bf16 v[188:191], a[32:35], v[92:95], v[188:191]\n"
"v_add_f32_e32 v215, v215, v223\n"
"v_fmac_f32_e32 v108, v254, v196\n"
"v_mfma_f32_16x16x32_bf16 v[200:203], a[64:67], v[92:95], v[200:203]\n"
"v_fmac_f32_e32 v109, v254, v197\n"
"v_fmac_f32_e32 v110, v254, v198\n"
"v_mfma_f32_16x16x32_bf16 v[200:203], a[68:71], v[124:127], v[200:203]\n"
"buffer_load_dwordx4 v[208:211], v0, s[4:7], s98 offen offset:3072 sc1\n"
"v_mfma_f32_16x16x32_bf16 v[200:203], a[72:75], v[132:135], v[200:203]\n"
"v_fmac_f32_e32 v111, v254, v199\n"
"v_add_f32_e32 v108, v108, v156\n"
"v_mfma_f32_16x16x32_bf16 v[200:203], a[76:79], v[140:143], v[200:203]\n"
"v_add_f32_e32 v109, v109, v157\n"
"v_add_f32_e32 v110, v110, v158\n"
"v_mfma_f32_16x16x32_bf16 v[200:203], a[80:83], v[128:131], v[200:203]\n"
"v_add_f32_e32 v111, v111, v159\n"
"v_fmac_f32_e32 v96, v254, v100\n"
"v_mfma_f32_16x16x32_bf16 v[200:203], a[84:87], v[168:171], v[200:203]\n"
"s_waitcnt lgkmcnt(0)\n"
"v_fmac_f32_e32 v97, v254, v101\n"
"v_fmac_f32_e32 v98, v254, v102\n"
"v_mfma_f32_16x16x32_bf16 v[200:203], a[88:91], v[172:175], v[200:203]\n"
"buffer_load_dwordx4 v[156:159], v1, s[4:7], s98 offen sc1\n"
"v_mfma_f32_16x16x32_bf16 v[200:203], a[92:95], v[180:183], v[200:203]\n"
"ds_read_b128 v[196:199], v6 offset:2048\n"
"ds_read_b128 v[216:219], v6 offset:3072\n"
"v_mfma_f32_16x16x32_bf16 v[212:215], a[124:127], v[180:183], v[212:215]\n"
"v_fmac_f32_e32 v99, v254, v103\n"
"v_add_f32_e32 v96, v96, v116\n"
"v_mfma_f32_16x16x32_bf16 v[212:215], a[120:123], v[172:175], v[212:215]\n"
"v_add_f32_e32 v97, v97, v117\n"
"v_add_f32_e32 v98, v98, v118\n"
"v_mfma_f32_16x16x32_bf16 v[212:215], a[116:119], v[168:171], v[212:215]\n"
"v_add_f32_e32 v99, v99, v119\n"
"v_add_f32_e32 v112, v112, v204\n"
"v_mfma_f32_16x16x32_bf16 v[212:215], a[112:115], v[128:131], v[212:215]\n"
"buffer_load_dwordx4 v[100:103], v1, s[4:7], s98 offen offset:1024 sc1\n"
"v_mfma_f32_16x16x32_bf16 v[212:215], a[108:111], v[140:143], v[212:215]\n"
"v_add_f32_e32 v113, v113, v205\n"
"v_add_f32_e32 v114, v114, v206\n"
"v_mfma_f32_16x16x32_bf16 v[212:215], a[104:107], v[132:135], v[212:215]\n"
"v_add_f32_e32 v115, v115, v207\n"
"v_add_f32_e32 v188, v188, v148\n"
"v_mfma_f32_16x16x32_bf16 v[212:215], a[100:103], v[124:127], v[212:215]\n"
"v_add_f32_e32 v189, v189, v149\n"
"v_add_f32_e32 v190, v190, v150\n"
"v_mfma_f32_16x16x32_bf16 v[212:215], a[96:99], v[92:95], v[212:215]\n"
"buffer_load_dwordx4 a[188:191], v1, s[4:7], s97 offen offset:3072 sc1\n"
"v_mfma_f32_16x16x32_bf16 v[108:111], v[24:27], v[92:95], v[108:111]\n"
"buffer_load_dwordx4 v[116:119], v1, s[4:7], s98 offen offset:2048 sc1\n"
"v_mfma_f32_16x16x32_bf16 v[108:111], v[28:31], v[124:127], v[108:111]\n"
"v_add_f32_e32 v191, v191, v151\n"
"v_mul_f32_e32 v112, v112, v255\n"
"v_mfma_f32_16x16x32_bf16 v[108:111], v[32:35], v[132:135], v[108:111]\n"
"v_mul_f32_e32 v113, v113, v255\n"
"v_mul_f32_e32 v114, v114, v255\n"
"v_mfma_f32_16x16x32_bf16 v[108:111], v[36:39], v[140:143], v[108:111]\n"
"v_mul_f32_e32 v115, v115, v255\n"
"v_mul_f32_e32 v188, v188, v255\n"
"v_mfma_f32_16x16x32_bf16 v[108:111], v[40:43], v[128:131], v[108:111]\n"
"s_waitcnt lgkmcnt(0)\n"
"v_mul_f32_e32 v189, v189, v255\n"
"v_mul_f32_e32 v190, v190, v255\n"
"v_mfma_f32_16x16x32_bf16 v[108:111], v[44:47], v[168:171], v[108:111]\n"
"buffer_load_dwordx4 v[148:151], v1, s[4:7], s98 offen offset:3072 sc1\n"
"v_mfma_f32_16x16x32_bf16 v[108:111], v[48:51], v[172:175], v[108:111]\n"
"s_add_u32 s98, s63, 0x84000\n"
"ds_read_b128 v[204:207], v6 offset:4096\n"
"ds_read_b128 v[220:223], v6 offset:5120\n"
"v_mfma_f32_16x16x32_bf16 v[108:111], v[52:55], v[180:183], v[108:111]\n"
"v_mul_f32_e32 v191, v191, v255\n"
"v_cvt_pk_bf16_f32 v228, v112, v113\n"
"v_mfma_f32_16x16x32_bf16 v[96:99], v[84:87], v[180:183], v[96:99]\n"
"buffer_store_dword v152, v14, s[8:11], s98 offen sc1\n"
"v_mfma_f32_16x16x32_bf16 v[96:99], v[80:83], v[172:175], v[96:99]\n"
"v_cvt_pk_bf16_f32 v229, v114, v115\n"
"v_cvt_pk_bf16_f32 v230, v188, v189\n"
"v_mfma_f32_16x16x32_bf16 v[96:99], v[76:79], v[168:171], v[96:99]\n"
"v_cvt_pk_bf16_f32 v231, v190, v191\n"
"v_add_f32_e32 v200, v200, v196\n"
"v_mfma_f32_16x16x32_bf16 v[96:99], v[72:75], v[128:131], v[96:99]\n"
"ds_write_b128 v8, v[228:231] offset:25600\n"
"v_mfma_f32_16x16x32_bf16 v[96:99], v[68:71], v[140:143], v[96:99]\n"
"v_add_f32_e32 v201, v201, v197\n"
"v_add_f32_e32 v202, v202, v198\n"
"v_mfma_f32_16x16x32_bf16 v[96:99], v[64:67], v[132:135], v[96:99]\n"
"v_add_f32_e32 v203, v203, v199\n"
"v_add_f32_e32 v212, v212, v216\n"
"v_mfma_f32_16x16x32_bf16 v[96:99], v[60:63], v[124:127], v[96:99]\n"
"v_add_f32_e32 v213, v213, v217\n"
"v_add_f32_e32 v214, v214, v218\n"
"v_mfma_f32_16x16x32_bf16 v[96:99], v[56:59], v[92:95], v[96:99]\n"
"v_add_f32_e32 v215, v215, v219\n"
"v_mul_f32_e32 v200, v200, v255\n"
"v_mul_f32_e32 v201, v201, v255\n"
"v_mul_f32_e32 v202, v202, v255\n"
"v_mul_f32_e32 v203, v203, v255\n"
"v_mul_f32_e32 v212, v212, v255\n"
"v_mul_f32_e32 v213, v213, v255\n"
"v_mul_f32_e32 v214, v214, v255\n"
"v_mul_f32_e32 v215, v215, v255\n"
"v_cvt_pk_bf16_f32 v92, v200, v201\n"
"v_cvt_pk_bf16_f32 v93, v202, v203\n"
"v_cvt_pk_bf16_f32 v94, v212, v213\n"
"v_cvt_pk_bf16_f32 v95, v214, v215\n"
"ds_write_b128 v7, v[92:95] offset:58368\n"
".Lmk_b2:\n"
"s_waitcnt vmcnt(17)\n"
"; @B G1 2.0\n"
"v_mfma_f32_16x16x32_bf16 v[112:115], a[0:3], v[136:139], 0\n"
"v_mfma_f32_16x16x32_bf16 v[112:115], a[4:7], v[88:91], v[112:115]\n"
"s_add_u32 s98, s62, 0x40800\n"
"v_mfma_f32_16x16x32_bf16 v[112:115], a[8:11], v[192:195], v[112:115]\n"
"s_add_u32 s97, s54, 0x408000\n"
"buffer_load_dwordx4 v[124:127], v0, s[4:7], s97 offen sc1\n"
"v_mfma_f32_16x16x32_bf16 v[112:115], a[12:15], v[164:167], v[112:115]\n"
"s_waitcnt vmcnt(12)\n"
"v_mfma_f32_16x16x32_bf16 v[112:115], a[16:19], v[160:163], v[112:115]\n"
"buffer_load_dword v255, v10, s[4:7], s98 offen sc1\n"
"v_mfma_f32_16x16x32_bf16 v[112:115], a[20:23], v[224:227], v[112:115]\n"
"s_waitcnt lgkmcnt(2)\n"
"v_add_f32_e32 v108, v108, v204\n"
"v_add_f32_e32 v109, v109, v205\n"
"v_mfma_f32_16x16x32_bf16 v[112:115], a[24:27], v[144:147], v[112:115]\n"
"v_add_f32_e32 v110, v110, v206\n"
"v_add_f32_e32 v111, v111, v207\n"
"v_mfma_f32_16x16x32_bf16 v[112:115], a[28:31], v[104:107], v[112:115]\n"
"buffer_load_dwordx4 v[128:131], v0, s[4:7], s97 offen offset:1024 sc1\n"
"v_mfma_f32_16x16x32_bf16 v[132:135], a[60:63], v[104:107], 0\n"
"s_add_u32 s96, s70, 0x204000\n"
"buffer_load_dwordx4 a[192:195], v0, s[4:7], s96 offen sc1\n"
"v_mfma_f32_16x16x32_bf16 v[132:135], a[56:59], v[144:147], v[132:135]\n"
"buffer_load_dword v254, v10, s[4:7], s98 offen offset:256 sc1\n"
"v_mfma_f32_16x16x32_bf16 v[132:135], a[52:55], v[224:227], v[132:135]\n"
"v_add_f32_e32 v96, v96, v220\n"
"v_add_f32_e32 v97, v97, v221\n"
"v_mfma_f32_16x16x32_bf16 v[132:135], a[48:51], v[160:163], v[132:135]\n"
"v_add_f32_e32 v98, v98, v222\n"
"v_add_f32_e32 v99, v99, v223\n"
"v_mfma_f32_16x16x32_bf16 v[132:135], a[44:47], v[164:167], v[132:135]\n"
"buffer_load_dwordx4 v[140:143], v0, s[4:7], s97 offen offset:2048 sc1\n"
"v_mfma_f32_16x16x32_bf16 v[132:135], a[40:43], v[192:195], v[132:135]\n"
"ds_write_b128 v3, v[112:115]\n"
"v_mfma_f32_16x16x32_bf16 v[132:135], a[36:39], v[88:91], v[132:135]\n"
"buffer_load_dword v252, v10, s[4:7], s98 offen offset:512 sc1\n"
"v_mfma_f32_16x16x32_bf16 v[132:135], a[32:35], v[136:139], v[132:135]\n"
"v_mul_f32_e32 v108, v108, v253\n"
"v_mul_f32_e32 v109, v109, v253\n"
"v_mfma_f32_16x16x32_bf16 v[112:115], a[64:67], v[136:139], 0\n"
"v_mul_f32_e32 v110, v110, v253\n"
"v_mul_f32_e32 v111, v111, v253\n"
"v_mfma_f32_16x16x32_bf16 v[112:115], a[68:71], v[88:91], v[112:115]\n"
"buffer_load_dwordx4 v[152:155], v0, s[4:7], s97 offen offset:3072 sc1\n"
"v_mfma_f32_16x16x32_bf16 v[112:115], a[72:75], v[192:195], v[112:115]\n"
"buffer_load_dword v251, v10, s[4:7], s98 offen offset:768 sc1\n"
"v_mfma_f32_16x16x32_bf16 v[112:115], a[76:79], v[164:167], v[112:115]\n"
"v_mul_f32_e32 v96, v96, v253\n"
"v_mul_f32_e32 v97, v97, v253\n"
"v_mfma_f32_16x16x32_bf16 v[112:115], a[80:83], v[160:163], v[112:115]\n"
"v_mul_f32_e32 v98, v98, v253\n"
"v_mul_f32_e32 v99, v99, v253\n"
"v_mfma_f32_16x16x32_bf16 v[112:115], a[84:87], v[224:227], v[112:115]\n"
"ds_write_b128 v3, v[132:135] offset:1024\n"
"v_mfma_f32_16x16x32_bf16 v[112:115], a[88:91], v[144:147], v[112:115]\n"
"buffer_load_dwordx4 v[132:135], v1, s[4:7], s97 offen sc1\n"
"v_mfma_f32_16x16x32_bf16 v[112:115], a[92:95], v[104:107], v[112:115]\n"
"buffer_load_dword v253, v10, s[4:7], s98 offen offset:1024 sc1\n"
"v_mfma_f32_16x16x32_bf16 v[168:171], a[124:127], v[104:107], 0\n"
"v_cvt_pk_bf16_f32 v172, v108, v109\n"
"v_cvt_pk_bf16_f32 v173, v110, v111\n"
"v_mfma_f32_16x16x32_bf16 v[168:171], a[120:123], v[144:147], v[168:171]\n"
"v_cvt_pk_bf16_f32 v174, v96, v97\n"
"v_cvt_pk_bf16_f32 v175, v98, v99\n"
"v_mfma_f32_16x16x32_bf16 v[168:171], a[116:119], v[224:227], v[168:171]\n"
"buffer_load_dword v250, v10, s[4:7], s98 offen offset:1280 sc1\n"
"v_mfma_f32_16x16x32_bf16 v[168:171], a[112:115], v[160:163], v[168:171]\n"
"buffer_load_dwordx4 v[96:99], v1, s[4:7], s97 offen offset:1024 sc1\n"
"v_mfma_f32_16x16x32_bf16 v[168:171], a[108:111], v[164:167], v[168:171]\n"
"buffer_load_dwordx4 a[196:199], v0, s[4:7], s96 offen offset:1024 sc1\n"
"v_mfma_f32_16x16x32_bf16 v[168:171], a[104:107], v[192:195], v[168:171]\n"
"ds_write_b128 v3, v[112:115] offset:2048\n"
"v_mfma_f32_16x16x32_bf16 v[168:171], a[100:103], v[88:91], v[168:171]\n"
"buffer_load_dword v249, v10, s[4:7], s98 offen offset:1536 sc1\n"
"v_mfma_f32_16x16x32_bf16 v[168:171], a[96:99], v[136:139], v[168:171]\n"
"ds_write_b128 v2, v[172:175] offset:50176\n"
"v_mfma_f32_16x16x32_bf16 v[108:111], v[24:27], v[136:139], 0\n"
"buffer_load_dwordx4 v[112:115], v1, s[4:7], s97 offen offset:2048 sc1\n"
"s_add_u32 s95, s64, 0x802000\n"
"v_mfma_f32_16x16x32_bf16 v[108:111], v[28:31], v[88:91], v[108:111]\n"
"buffer_load_dword v248, v10, s[4:7], s98 offen offset:1792 sc1\n"
"v_add_u32_e32 v247, 0x26c00, v0\n"
"v_mfma_f32_16x16x32_bf16 v[108:111], v[32:35], v[192:195], v[108:111]\n"
"buffer_store_dwordx4 v[228:231], v8, s[12:15], s95 offen offset:1024 sc1\n"
"v_mfma_f32_16x16x32_bf16 v[108:111], v[36:39], v[164:167], v[108:111]\n"
"ds_read_b128 v[172:175], v247\n"
"ds_read_b128 v[180:183], v247 offset:1024\n"
"v_xor_b32_e32 v247, 32, v15\n"
"v_mfma_f32_16x16x32_bf16 v[108:111], v[40:43], v[160:163], v[108:111]\n"
"ds_read_b128 v[188:191], v2 offset:50176\n"
"ds_read_b128 v[196:199], v15 offset:24576\n"
"ds_read_b128 v[200:203], v247 offset:24832\n"
"v_mfma_f32_16x16x32_bf16 v[108:111], v[44:47], v[224:227], v[108:111]\n"
"buffer_load_dwordx4 v[204:207], v1, s[4:7], s97 offen offset:3072 sc1\n"
"v_mfma_f32_16x16x32_bf16 v[108:111], v[48:51], v[144:147], v[108:111]\n"
"ds_write_b128 v3, v[168:171] offset:3072\n"
"v_mfma_f32_16x16x32_bf16 v[108:111], v[52:55], v[104:107], v[108:111]\n"
"v_mfma_f32_16x16x32_bf16 v[168:171], v[84:87], v[104:107], 0\n"
"v_mfma_f32_16x16x32_bf16 v[168:171], v[80:83], v[144:147], v[168:171]\n"
"s_waitcnt lgkmcnt(1)\n"
"v_mfma_f32_16x16x32_bf16 v[172:175], v[196:199], v[188:191], v[172:175]\n"
"v_mfma_f32_16x16x32_bf16 v[180:183], v[200:203], v[188:191], v[180:183]\n"
"ds_read_b64_tr_b16 v[104:105], v16 offset:24576\n"
"v_xor_b32_e32 v247, 32, v16\n"
"v_mfma_f32_16x16x32_bf16 v[168:171], v[76:79], v[224:227], v[168:171]\n"
"ds_read_b64_tr_b16 v[106:107], v247 offset:24832\n"
"ds_read_b64_tr_b16 v[144:145], v247 offset:24576\n"
"ds_read_b64_tr_b16 v[146:147], v16 offset:24832\n"
"v_mov_b32_e32 v188, 0x3f803f80\n"
"v_mfma_f32_16x16x32_bf16 v[168:171], v[72:75], v[160:163], v[168:171]\n"
"v_mov_b32_e32 v189, 0x3f803f80\n"
"v_mov_b32_e32 v190, 0x3f803f80\n"
"v_mov_b32_e32 v191, 0x3f803f80\n"
"v_mfma_f32_16x16x32_bf16 v[168:171], v[68:71], v[164:167], v[168:171]\n"
"v_max_f32_e32 v247, v172, v173\n"
"v_max3_f32 v247, v247, v174, v175\n"
"v_max3_f32 v247, v247, v180, v181\n"
"v_mfma_f32_16x16x32_bf16 v[168:171], v[64:67], v[192:195], v[168:171]\n"
"ds_write_b128 v3, v[108:111] offset:4096\n"
"v_mfma_f32_16x16x32_bf16 v[168:171], v[60:63], v[88:91], v[168:171]\n"
"v_max3_f32 v247, v247, v182, v183\n"
"v_mov_b32_e32 v246, v247\n"
"v_mfma_f32_16x16x32_bf16 v[168:171], v[56:59], v[136:139], v[168:171]\n"
"s_waitcnt vmcnt(25)\n"
"v_permlane16_swap_b32_e32 v247, v246\n"
"v_max_f32_e32 v247, v247, v246\n"
"v_mov_b32_e32 v246, v247\n"
"; @B G1 2.1\n"
"v_mfma_f32_16x16x32_bf16 v[88:91], a[0:3], v[120:123], 0\n"
"buffer_load_dwordx4 a[200:203], v0, s[4:7], s96 offen offset:2048 sc1\n"
"v_permlane32_swap_b32_e32 v247, v246\n"
"v_mfma_f32_16x16x32_bf16 v[88:91], a[4:7], v[176:179], v[88:91]\n"
"v_max_f32_e32 v247, v247, v246\n"
"v_sub_f32_e32 v172, v172, v247\n"
"v_sub_f32_e32 v173, v173, v247\n"
"v_mfma_f32_16x16x32_bf16 v[88:91], a[8:11], v[184:187], v[88:91]\n"
"s_add_u32 s98, s55, 0x408000\n"
"buffer_load_dwordx4 v[108:111], v0, s[4:7], s98 offen sc1\n"
"v_mfma_f32_16x16x32_bf16 v[88:91], a[12:15], v[208:211], v[88:91]\n"
"s_waitcnt vmcnt(22)\n"
"v_sub_f32_e32 v174, v174, v247\n"
"v_sub_f32_e32 v175, v175, v247\n"
"v_sub_f32_e32 v180, v180, v247\n"
"v_mfma_f32_16x16x32_bf16 v[88:91], a[16:19], v[156:159], v[88:91]\n"
"v_sub_f32_e32 v181, v181, v247\n"
"v_sub_f32_e32 v182, v182, v247\n"
"v_sub_f32_e32 v183, v183, v247\n"
"v_mfma_f32_16x16x32_bf16 v[88:91], a[20:23], v[100:103], v[88:91]\n"
"ds_write_b128 v3, v[168:171] offset:5120\n"
"v_mfma_f32_16x16x32_bf16 v[88:91], a[24:27], v[116:119], v[88:91]\n"
"v_exp_f32_e32 v172, v172\n"
"v_mfma_f32_16x16x32_bf16 v[88:91], a[28:31], v[148:151], v[88:91]\n"
"buffer_load_dwordx4 v[136:139], v0, s[4:7], s98 offen offset:1024 sc1\n"
"v_mfma_f32_16x16x32_bf16 v[160:163], a[60:63], v[148:151], 0\n"
"s_add_u32 s97, s64, 0x806000\n"
"v_exp_f32_e32 v173, v173\n"
"v_mfma_f32_16x16x32_bf16 v[160:163], a[56:59], v[116:119], v[160:163]\n"
"v_exp_f32_e32 v174, v174\n"
"v_mfma_f32_16x16x32_bf16 v[160:163], a[52:55], v[100:103], v[160:163]\n"
"buffer_store_dwordx4 v[92:95], v7, s[12:15], s97 offen offset:1024 sc1\n"
"v_mfma_f32_16x16x32_bf16 v[160:163], a[48:51], v[156:159], v[160:163]\n"
"v_exp_f32_e32 v175, v175\n"
"v_mfma_f32_16x16x32_bf16 v[160:163], a[44:47], v[208:211], v[160:163]\n"
"buffer_load_dwordx4 v[92:95], v0, s[4:7], s98 offen offset:2048 sc1\n"
"v_mfma_f32_16x16x32_bf16 v[160:163], a[40:43], v[184:187], v[160:163]\n"
"ds_write_b128 v3, v[88:91] offset:6144\n"
"v_mfma_f32_16x16x32_bf16 v[160:163], a[36:39], v[176:179], v[160:163]\n"
"v_exp_f32_e32 v180, v180\n"
"v_mfma_f32_16x16x32_bf16 v[160:163], a[32:35], v[120:123], v[160:163]\n"
"v_exp_f32_e32 v181, v181\n"
"v_mfma_f32_16x16x32_bf16 v[88:91], a[64:67], v[120:123], 0\n"
"v_exp_f32_e32 v182, v182\n"
"v_mfma_f32_16x16x32_bf16 v[88:91], a[68:71], v[176:179], v[88:91]\n"
"buffer_load_dwordx4 v[164:167], v0, s[4:7], s98 offen offset:3072 sc1\n"
"v_mfma_f32_16x16x32_bf16 v[88:91], a[72:75], v[184:187], v[88:91]\n"
"v_exp_f32_e32 v183, v183\n"
"v_cvt_pk_bf16_f32 v168, v172, v173\n"
"v_mfma_f32_16x16x32_bf16 v[88:91], a[76:79], v[208:211], v[88:91]\n"
"v_cvt_pk_bf16_f32 v169, v174, v175\n"
"v_cvt_pk_bf16_f32 v170, v180, v181\n"
"v_cvt_pk_bf16_f32 v171, v182, v183\n"
"v_mfma_f32_16x16x32_bf16 v[88:91], a[80:83], v[156:159], v[88:91]\n"
"s_waitcnt lgkmcnt(3)\n"
"v_mov_b32_e32 v172, -1\n"
"v_mov_b32_e32 v173, -1\n"
"v_mfma_f32_16x16x32_bf16 v[180:183], v[188:191], v[168:171], 0\n"
"v_mfma_f32_16x16x32_bf16 v[188:191], v[104:107], v[168:171], 0\n"
"v_mfma_f32_16x16x32_bf16 v[104:107], v[144:147], v[168:171], 0\n"
"v_mfma_f32_16x16x32_bf16 v[88:91], a[84:87], v[100:103], v[88:91]\n"
"ds_write_b128 v3, v[160:163] offset:7168\n"
"v_mfma_f32_16x16x32_bf16 v[88:91], a[88:91], v[116:119], v[88:91]\n"
"buffer_load_dwordx4 v[144:147], v1, s[4:7], s98 offen sc1\n"
"v_mfma_f32_16x16x32_bf16 v[88:91], a[92:95], v[148:151], v[88:91]\n"
"v_mov_b32_e32 v174, -1\n"
"v_mov_b32_e32 v175, -1\n"
"v_mfma_f32_16x16x32_bf16 v[160:163], a[124:127], v[148:151], 0\n"
"s_add_u32 s97, s60, 0x848000\n"
"buffer_store_dwordx4 v[172:175], v0, s[8:11], s97 offen sc1\n"
"v_mfma_f32_16x16x32_bf16 v[160:163], a[120:123], v[116:119], v[160:163]\n"
"buffer_load_dwordx4 a[204:207], v0, s[4:7], s96 offen offset:3072 sc1\n"
"v_mfma_f32_16x16x32_bf16 v[160:163], a[116:119], v[100:103], v[160:163]\n"
"v_rcp_f32_e32 v180, v180\n"
"v_mfma_f32_16x16x32_bf16 v[160:163], a[112:115], v[156:159], v[160:163]\n"
"buffer_load_dwordx4 v[168:171], v1, s[4:7], s98 offen offset:1024 sc1\n"
"v_mul_f32_e32 v188, v188, v180\n"
"v_mfma_f32_16x16x32_bf16 v[160:163], a[108:111], v[208:211], v[160:163]\n"
"v_mul_f32_e32 v189, v189, v180\n"
"v_mul_f32_e32 v190, v190, v180\n"
"v_mul_f32_e32 v191, v191, v180\n"
"v_mfma_f32_16x16x32_bf16 v[160:163], a[104:107], v[184:187], v[160:163]\n"
"ds_write_b128 v3, v[88:91] offset:8192\n"
"v_mfma_f32_16x16x32_bf16 v[160:163], a[100:103], v[176:179], v[160:163]\n"
"v_mul_f32_e32 v104, v104, v180\n"
"v_mul_f32_e32 v105, v105, v180\n"
"v_mul_f32_e32 v106, v106, v180\n"
"v_mfma_f32_16x16x32_bf16 v[160:163], a[96:99], v[120:123], v[160:163]\n"
"v_mul_f32_e32 v107, v107, v180\n"
"v_cvt_pk_bf16_f32 v88, v188, v189\n"
"v_cvt_pk_bf16_f32 v89, v190, v191\n"
"v_mfma_f32_16x16x32_bf16 v[180:183], v[24:27], v[120:123], 0\n"
"buffer_load_dwordx4 v[188:191], v1, s[4:7], s98 offen offset:2048 sc1\n"
"v_cvt_pk_bf16_f32 v90, v104, v105\n"
"v_mfma_f32_16x16x32_bf16 v[180:183], v[28:31], v[176:179], v[180:183]\n"
"s_add_u32 s97, s60, 0x448000\n"
"v_cvt_pk_bf16_f32 v91, v106, v107\n"
"v_mfma_f32_16x16x32_bf16 v[180:183], v[32:35], v[184:187], v[180:183]\n"
"buffer_store_dwordx4 v[88:91], v0, s[4:7], s97 offen sc1\n"
"v_mfma_f32_16x16x32_bf16 v[180:183], v[36:39], v[208:211], v[180:183]\n"
"v_mfma_f32_16x16x32_bf16 v[180:183], v[40:43], v[156:159], v[180:183]\n"
"s_waitcnt vmcnt(14)\n"
"v_add_f32_e32 v255, v255, v253\n"
"v_add_f32_e32 v254, v254, v250\n"
"v_mfma_f32_16x16x32_bf16 v[180:183], v[44:47], v[100:103], v[180:183]\n"
"buffer_load_dwordx4 v[88:91], v1, s[4:7], s98 offen offset:3072 sc1\n"
"v_mfma_f32_16x16x32_bf16 v[180:183], v[48:51], v[116:119], v[180:183]\n"
"ds_write_b128 v3, v[160:163] offset:9216\n"
"v_mfma_f32_16x16x32_bf16 v[180:183], v[52:55], v[148:151], v[180:183]\n"
"v_add_f32_e32 v252, v252, v249\n"
"v_add_f32_e32 v251, v251, v248\n"
"v_mfma_f32_16x16x32_bf16 v[104:107], v[84:87], v[148:151], 0\n"
"v_add_f32_e32 v255, v255, v252\n"
"v_add_f32_e32 v254, v254, v251\n"
"v_mfma_f32_16x16x32_bf16 v[104:107], v[80:83], v[116:119], v[104:107]\n"
"v_add_f32_e32 v255, v255, v254\n"
"v_mov_b32_e32 v254, v255\n"
"v_mfma_f32_16x16x32_bf16 v[104:107], v[76:79], v[100:103], v[104:107]\n"
"s_nop 0\n"
"v_permlane32_swap_b32_e32 v255, v254\n"
"v_add_f32_e32 v255, v255, v254\n"
"v_mfma_f32_16x16x32_bf16 v[104:107], v[72:75], v[156:159], v[104:107]\n"
"v_mul_f32_e32 v255, 0x3a800000, v255\n"
"v_mov_b32_e32 v254, v255\n"
"v_mfma_f32_16x16x32_bf16 v[104:107], v[68:71], v[208:211], v[104:107]\n"
"s_nop 0\n"
"v_permlane16_swap_b32_e32 v255, v254\n"
"v_cvt_pk_bf16_f32 v253, -v255, 0\n"
"v_mfma_f32_16x16x32_bf16 v[104:107], v[64:67], v[184:187], v[104:107]\n"
"ds_write_b128 v3, v[180:183] offset:10240\n"
"v_mfma_f32_16x16x32_bf16 v[104:107], v[60:63], v[176:179], v[104:107]\n"
"buffer_load_dwordx4 a[208:211], v1, s[4:7], s96 offen sc1\n"
"v_mfma_f32_16x16x32_bf16 v[104:107], v[56:59], v[120:123], v[104:107]\n"
"v_fma_f32 v254, -v255, v255, v254\n"
"v_lshlrev_b32_e32 v253, 16, v253\n"
"; @B G1 2.2\n"
"v_mfma_f32_16x16x32_bf16 v[100:103], a[0:3], v[124:127], 0\n"
"v_max_f32_e32 v254, 0, v254\n"
"v_cvt_pk_bf16_f32 v254, v254, 0\n"
"v_mfma_f32_16x16x32_bf16 v[100:103], a[4:7], v[128:131], v[100:103]\n"
"v_lshlrev_b32_e32 v254, 16, v254\n"
"v_add_f32_e32 v254, 0x3727c5ac, v254\n"
"v_mfma_f32_16x16x32_bf16 v[100:103], a[8:11], v[140:143], v[100:103]\n"
"v_cvt_pk_bf16_f32 v254, v254, 0\n"
"v_lshlrev_b32_e32 v254, 16, v254\n"
"v_mfma_f32_16x16x32_bf16 v[100:103], a[12:15], v[152:155], v[100:103]\n"
"s_waitcnt vmcnt(14)\n"
"v_sqrt_f32_e32 v254, v254\n"
"v_mfma_f32_16x16x32_bf16 v[100:103], a[16:19], v[132:135], v[100:103]\n"
"s_add_u32 s98, s68, 0x10000\n"
"v_cvt_pk_bf16_f32 v254, v254, 0\n"
"v_mfma_f32_16x16x32_bf16 v[100:103], a[20:23], v[96:99], v[100:103]\n"
"ds_write_b128 v3, v[104:107] offset:11264\n"
"v_mfma_f32_16x16x32_bf16 v[100:103], a[24:27], v[112:115], v[100:103]\n"
"buffer_store_dwordx2 v[18:19], v11, s[4:7], s98 offen offset:32 sc1\n"
"v_mfma_f32_16x16x32_bf16 v[100:103], a[28:31], v[204:207], v[100:103]\n"
"v_lshlrev_b32_e32 v254, 16, v254\n"
"v_mfma_f32_16x16x32_bf16 v[104:107], a[60:63], v[204:207], 0\n"
"v_rcp_f32_e32 v255, v254\n"
"v_mfma_f32_16x16x32_bf16 v[104:107], a[56:59], v[112:115], v[104:107]\n"
"v_mul_f32_e32 v254, 0x3e8293ee, v255\n"
"v_mfma_f32_16x16x32_bf16 v[104:107], a[52:55], v[96:99], v[104:107]\n"
"s_add_u32 s98, s52, 0x400000\n"
"buffer_load_dwordx4 v[116:119], v0, s[4:7], s98 offen sc1\n"
"v_mfma_f32_16x16x32_bf16 v[104:107], a[48:51], v[132:135], v[104:107]\n"
"s_waitcnt lgkmcnt(0)\n"
"s_barrier\n"
"v_mfma_f32_16x16x32_bf16 v[104:107], a[44:47], v[152:155], v[104:107]\n"
"ds_read_b128 v[120:123], v4\n"
"ds_read_b128 v[148:151], v9 offset:640\n"
"v_mfma_f32_16x16x32_bf16 v[104:107], a[40:43], v[140:143], v[104:107]\n"
"ds_write_b128 v3, v[100:103] offset:12288\n"
"v_mfma_f32_16x16x32_bf16 v[104:107], a[36:39], v[128:131], v[104:107]\n"
"ds_read_b128 v[100:103], v5\n"
"v_mfma_f32_16x16x32_bf16 v[104:107], a[32:35], v[124:127], v[104:107]\n"
"buffer_load_dwordx4 v[156:159], v0, s[4:7], s98 offen offset:1024 sc1\n"
"v_mfma_f32_16x16x32_bf16 v[160:163], a[64:67], v[124:127], 0\n"
"v_mfma_f32_16x16x32_bf16 v[160:163], a[68:71], v[128:131], v[160:163]\n"
"v_mfma_f32_16x16x32_bf16 v[160:163], a[72:75], v[140:143], v[160:163]\n"
"buffer_load_dwordx4 a[212:215], v1, s[4:7], s96 offen offset:1024 sc1\n"
"v_mfma_f32_16x16x32_bf16 v[160:163], a[76:79], v[152:155], v[160:163]\n"
"ds_read_b128 v[176:179], v4 offset:1024\n"
"ds_read_b128 v[180:183], v9 offset:704\n"
"v_mfma_f32_16x16x32_bf16 v[160:163], a[80:83], v[132:135], v[160:163]\n"
"buffer_load_dwordx4 v[184:187], v0, s[4:7], s98 offen offset:2048 sc1\n"
"v_mfma_f32_16x16x32_bf16 v[160:163], a[84:87], v[96:99], v[160:163]\n"
"ds_write_b128 v3, v[104:107] offset:13312\n"
"v_mfma_f32_16x16x32_bf16 v[160:163], a[88:91], v[112:115], v[160:163]\n"
"ds_read_b128 v[104:107], v5 offset:1024\n"
"s_waitcnt lgkmcnt(4)\n"
"v_mfma_f32_16x16x32_bf16 v[160:163], a[92:95], v[204:207], v[160:163]\n"
"v_fmac_f32_e32 v120, v253, v148\n"
"v_fmac_f32_e32 v121, v253, v149\n"
"v_mfma_f32_16x16x32_bf16 v[192:195], a[124:127], v[204:207], 0\n"
"v_fmac_f32_e32 v122, v253, v150\n"
"v_fmac_f32_e32 v123, v253, v151\n"
"v_mfma_f32_16x16x32_bf16 v[192:195], a[120:123], v[112:115], v[192:195]\n"
"buffer_load_dwordx4 v[148:151], v0, s[4:7], s98 offen offset:3072 sc1\n"
"v_mfma_f32_16x16x32_bf16 v[192:195], a[116:119], v[96:99], v[192:195]\n"
"ds_read_b128 v[196:199], v4 offset:2048\n"
"ds_read_b128 v[200:203], v9 offset:768\n"
"v_mfma_f32_16x16x32_bf16 v[192:195], a[112:115], v[132:135], v[192:195]\n"
"ds_read_b128 v[208:211], v5 offset:2048\n"
"v_add_f32_e32 v120, v120, v100\n"
"v_mfma_f32_16x16x32_bf16 v[192:195], a[108:111], v[152:155], v[192:195]\n"
"v_add_f32_e32 v121, v121, v101\n"
"v_add_f32_e32 v122, v122, v102\n"
"v_mfma_f32_16x16x32_bf16 v[192:195], a[104:107], v[140:143], v[192:195]\n"
"ds_write_b128 v3, v[160:163] offset:14336\n"
"v_mfma_f32_16x16x32_bf16 v[192:195], a[100:103], v[128:131], v[192:195]\n"
"buffer_load_dwordx4 v[160:163], v1, s[4:7], s98 offen sc1\n"
"v_mfma_f32_16x16x32_bf16 v[192:195], a[96:99], v[124:127], v[192:195]\n"
"s_add_u32 s97, s60, 0x840000\n"
"v_add_f32_e32 v123, v123, v103\n"
"s_waitcnt lgkmcnt(4)\n"
"v_mfma_f32_16x16x32_bf16 v[100:103], v[24:27], v[124:127], 0\n"
"buffer_store_dwordx4 v[172:175], v0, s[8:11], s97 offen sc1\n"
"v_mfma_f32_16x16x32_bf16 v[100:103], v[28:31], v[128:131], v[100:103]\n"
"ds_read_b128 v[212:215], v4 offset:3072\n"
"ds_read_b128 v[216:219], v9 offset:832\n"
"v_mfma_f32_16x16x32_bf16 v[100:103], v[32:35], v[140:143], v[100:103]\n"
"s_waitcnt vmcnt(18)\n"
"s_add_u32 s97, s68, 0x11000\n"
"ds_read_b128 v[220:223], v5 offset:3072\n"
"v_mfma_f32_16x16x32_bf16 v[100:103], v[36:39], v[152:155], v[100:103]\n"
"buffer_load_dwordx4 v[224:227], v1, s[4:7], s98 offen offset:1024 sc1\n"
"v_mfma_f32_16x16x32_bf16 v[100:103], v[40:43], v[132:135], v[100:103]\n"
"buffer_store_dwordx2 v[18:19], v11, s[12:15], s97 offen offset:64 sc1\n"
"v_mfma_f32_16x16x32_bf16 v[100:103], v[44:47], v[96:99], v[100:103]\n"
"ds_write_b128 v3, v[192:195] offset:15360\n"
"v_mfma_f32_16x16x32_bf16 v[100:103], v[48:51], v[112:115], v[100:103]\n"
"buffer_load_dwordx4 a[216:219], v1, s[4:7], s96 offen offset:2048 sc1\n"
"v_mfma_f32_16x16x32_bf16 v[100:103], v[52:55], v[204:207], v[100:103]\n"
"v_fmac_f32_e32 v176, v253, v180\n"
"v_fmac_f32_e32 v177, v253, v181\n"
"v_mfma_f32_16x16x32_bf16 v[192:195], v[84:87], v[204:207], 0\n"
"buffer_load_dwordx4 v[204:207], v1, s[4:7], s98 offen offset:2048 sc1\n"
"v_mfma_f32_16x16x32_bf16 v[192:195], v[80:83], v[112:115], v[192:195]\n"
"ds_read_b128 v[112:115], v4 offset:4096\n"
"ds_read_b128 v[228:231], v9 offset:512\n"
"v_mfma_f32_16x16x32_bf16 v[192:195], v[76:79], v[96:99], v[192:195]\n"
"ds_read_b128 v[96:99], v5 offset:4096\n"
"v_fmac_f32_e32 v178, v253, v182\n"
"v_mfma_f32_16x16x32_bf16 v[192:195], v[72:75], v[132:135], v[192:195]\n"
"v_fmac_f32_e32 v179, v253, v183\n"
"v_add_f32_e32 v176, v176, v104\n"
"v_mfma_f32_16x16x32_bf16 v[192:195], v[68:71], v[152:155], v[192:195]\n"
"v_add_f32_e32 v177, v177, v105\n"
"v_add_f32_e32 v178, v178, v106\n"
"v_mfma_f32_16x16x32_bf16 v[192:195], v[64:67], v[140:143], v[192:195]\n"
"buffer_load_dwordx4 v[132:135], v1, s[4:7], s98 offen offset:3072 sc1\n"
"v_mfma_f32_16x16x32_bf16 v[192:195], v[60:63], v[128:131], v[192:195]\n"
"ds_write_b128 v3, v[100:103] offset:16384\n"
"v_mfma_f32_16x16x32_bf16 v[192:195], v[56:59], v[124:127], v[192:195]\n"
"s_waitcnt vmcnt(21)\n"
"ds_read_b128 v[100:103], v4 offset:5120\n"
"ds_read_b128 v[124:127], v9 offset:576\n"
"; @B G1 2.3\n"
"v_mfma_f32_16x16x32_bf16 v[120:123], a[0:3], v[108:111], v[120:123]\n"
"ds_read_b128 v[128:131], v5 offset:5120\n"
"v_add_f32_e32 v179, v179, v107\n"
"v_mfma_f32_16x16x32_bf16 v[120:123], a[4:7], v[136:139], v[120:123]\n"
"s_waitcnt lgkmcnt(12)\n"
"v_fmac_f32_e32 v196, v253, v200\n"
"v_fmac_f32_e32 v197, v253, v201\n"
"v_mfma_f32_16x16x32_bf16 v[120:123], a[8:11], v[92:95], v[120:123]\n"
"s_add_u32 s98, s53, 0x400000\n"
"buffer_load_dwordx4 v[104:107], v0, s[4:7], s98 offen sc1\n"
"v_mfma_f32_16x16x32_bf16 v[120:123], a[12:15], v[164:167], v[120:123]\n"
"s_waitcnt vmcnt(15)\n"
"v_fmac_f32_e32 v198, v253, v202\n"
"v_fmac_f32_e32 v199, v253, v203\n"
"v_mfma_f32_16x16x32_bf16 v[120:123], a[16:19], v[144:147], v[120:123]\n"
"v_add_f32_e32 v196, v196, v208\n"
"v_add_f32_e32 v197, v197, v209\n"
"v_mfma_f32_16x16x32_bf16 v[120:123], a[20:23], v[168:171], v[120:123]\n"
"ds_write_b128 v3, v[192:195] offset:17408\n"
"v_mfma_f32_16x16x32_bf16 v[120:123], a[24:27], v[188:191], v[120:123]\n"
"s_waitcnt lgkmcnt(9)\n"
"v_add_f32_e32 v198, v198, v210\n"
"v_add_f32_e32 v199, v199, v211\n"
"v_mfma_f32_16x16x32_bf16 v[120:123], a[28:31], v[88:91], v[120:123]\n"
"buffer_load_dwordx4 v[140:143], v0, s[4:7], s98 offen offset:1024 sc1\n"
"v_mfma_f32_16x16x32_bf16 v[176:179], a[60:63], v[88:91], v[176:179]\n"
"v_fmac_f32_e32 v212, v253, v216\n"
"v_fmac_f32_e32 v213, v253, v217\n"
"v_mfma_f32_16x16x32_bf16 v[176:179], a[56:59], v[188:191], v[176:179]\n"
"v_fmac_f32_e32 v214, v253, v218\n"
"v_fmac_f32_e32 v215, v253, v219\n"
"v_mfma_f32_16x16x32_bf16 v[176:179], a[52:55], v[168:171], v[176:179]\n"
"s_waitcnt lgkmcnt(0)\n"
"s_barrier\n"
"v_mfma_f32_16x16x32_bf16 v[176:179], a[48:51], v[144:147], v[176:179]\n"
"buffer_load_dwordx4 a[220:223], v1, s[4:7], s96 offen offset:3072 sc1\n"
"v_mfma_f32_16x16x32_bf16 v[176:179], a[44:47], v[164:167], v[176:179]\n"
"buffer_load_dwordx4 v[152:155], v0, s[4:7], s98 offen offset:2048 sc1\n"
"v_mfma_f32_16x16x32_bf16 v[176:179], a[40:43], v[92:95], v[176:179]\n"
"ds_read_b128 v[180:183], v6\n"
"ds_read_b128 v[192:195], v6 offset:1024\n"
"v_mfma_f32_16x16x32_bf16 v[176:179], a[36:39], v[136:139], v[176:179]\n"
"v_add_f32_e32 v212, v212, v220\n"
"v_add_f32_e32 v213, v213, v221\n"
"v_mfma_f32_16x16x32_bf16 v[176:179], a[32:35], v[108:111], v[176:179]\n"
"v_add_f32_e32 v214, v214, v222\n"
"v_add_f32_e32 v215, v215, v223\n"
"v_mfma_f32_16x16x32_bf16 v[196:199], a[64:67], v[108:111], v[196:199]\n"
"v_fmac_f32_e32 v112, v253, v228\n"
"v_fmac_f32_e32 v113, v253, v229\n"
"v_mfma_f32_16x16x32_bf16 v[196:199], a[68:71], v[136:139], v[196:199]\n"
"buffer_load_dwordx4 v[200:203], v0, s[4:7], s98 offen offset:3072 sc1\n"
"v_mfma_f32_16x16x32_bf16 v[196:199], a[72:75], v[92:95], v[196:199]\n"
"v_fmac_f32_e32 v114, v253, v230\n"
"v_fmac_f32_e32 v115, v253, v231\n"
"v_mfma_f32_16x16x32_bf16 v[196:199], a[76:79], v[164:167], v[196:199]\n"
"v_add_f32_e32 v112, v112, v96\n"
"v_add_f32_e32 v113, v113, v97\n"
"v_mfma_f32_16x16x32_bf16 v[196:199], a[80:83], v[144:147], v[196:199]\n"
"v_add_f32_e32 v114, v114, v98\n"
"v_add_f32_e32 v115, v115, v99\n"
"v_mfma_f32_16x16x32_bf16 v[196:199], a[84:87], v[168:171], v[196:199]\n"
"s_waitcnt lgkmcnt(0)\n"
"v_fmac_f32_e32 v100, v253, v124\n"
"v_fmac_f32_e32 v101, v253, v125\n"
"v_mfma_f32_16x16x32_bf16 v[196:199], a[88:91], v[188:191], v[196:199]\n"
"buffer_load_dwordx4 v[96:99], v1, s[4:7], s98 offen sc1\n"
"v_mfma_f32_16x16x32_bf16 v[196:199], a[92:95], v[88:91], v[196:199]\n"
"ds_read_b128 v[208:211], v6 offset:2048\n"
"ds_read_b128 v[216:219], v6 offset:3072\n"
"v_mfma_f32_16x16x32_bf16 v[212:215], a[124:127], v[88:91], v[212:215]\n"
"v_fmac_f32_e32 v102, v253, v126\n"
"v_fmac_f32_e32 v103, v253, v127\n"
"v_mfma_f32_16x16x32_bf16 v[212:215], a[120:123], v[188:191], v[212:215]\n"
"v_add_f32_e32 v100, v100, v128\n"
"v_add_f32_e32 v101, v101, v129\n"
"v_mfma_f32_16x16x32_bf16 v[212:215], a[116:119], v[168:171], v[212:215]\n"
"v_add_f32_e32 v102, v102, v130\n"
"v_add_f32_e32 v103, v103, v131\n"
"v_mfma_f32_16x16x32_bf16 v[212:215], a[112:115], v[144:147], v[212:215]\n"
"buffer_load_dwordx4 v[124:127], v1, s[4:7], s98 offen offset:1024 sc1\n"
"v_mfma_f32_16x16x32_bf16 v[212:215], a[108:111], v[164:167], v[212:215]\n"
"v_add_f32_e32 v120, v120, v180\n"
"v_add_f32_e32 v121, v121, v181\n"
"v_mfma_f32_16x16x32_bf16 v[212:215], a[104:107], v[92:95], v[212:215]\n"
"s_add_u32 s97, s70, 0x206000\n"
"v_add_f32_e32 v122, v122, v182\n"
"v_add_f32_e32 v123, v123, v183\n"
"v_mfma_f32_16x16x32_bf16 v[212:215], a[100:103], v[136:139], v[212:215]\n"
"v_add_f32_e32 v176, v176, v192\n"
"v_add_f32_e32 v177, v177, v193\n"
"v_mfma_f32_16x16x32_bf16 v[212:215], a[96:99], v[108:111], v[212:215]\n"
"buffer_load_dwordx4 a[224:227], v0, s[4:7], s97 offen sc1\n"
"v_mfma_f32_16x16x32_bf16 v[112:115], v[24:27], v[108:111], v[112:115]\n"
"buffer_load_dwordx4 v[128:131], v1, s[4:7], s98 offen offset:2048 sc1\n"
"v_mfma_f32_16x16x32_bf16 v[112:115], v[28:31], v[136:139], v[112:115]\n"
"v_add_f32_e32 v178, v178, v194\n"
"v_add_f32_e32 v179, v179, v195\n"
"v_mfma_f32_16x16x32_bf16 v[112:115], v[32:35], v[92:95], v[112:115]\n"
"v_mul_f32_e32 v120, v120, v255\n"
"v_mul_f32_e32 v121, v121, v255\n"
"v_mfma_f32_16x16x32_bf16 v[112:115], v[36:39], v[164:167], v[112:115]\n"
"v_mul_f32_e32 v122, v122, v255\n"
"v_mul_f32_e32 v123, v123, v255\n"
"v_mfma_f32_16x16x32_bf16 v[112:115], v[40:43], v[144:147], v[112:115]\n"
"s_waitcnt lgkmcnt(0)\n"
"v_mul_f32_e32 v176, v176, v255\n"
"v_mul_f32_e32 v177, v177, v255\n"
"v_mfma_f32_16x16x32_bf16 v[112:115], v[44:47], v[168:171], v[112:115]\n"
"buffer_load_dwordx4 v[180:183], v1, s[4:7], s98 offen offset:3072 sc1\n"
"v_mfma_f32_16x16x32_bf16 v[112:115], v[48:51], v[188:191], v[112:115]\n"
"s_add_u32 s98, s60, 0x800000\n"
"ds_read_b128 v[192:195], v6 offset:4096\n"
"ds_read_b128 v[220:223], v6 offset:5120\n"
"v_mfma_f32_16x16x32_bf16 v[112:115], v[52:55], v[88:91], v[112:115]\n"
"v_mul_f32_e32 v178, v178, v255\n"
"v_mul_f32_e32 v179, v179, v255\n"
"v_mfma_f32_16x16x32_bf16 v[100:103], v[84:87], v[88:91], v[100:103]\n"
"buffer_store_dwordx4 v[172:175], v0, s[8:11], s98 offen sc1\n"
"v_mfma_f32_16x16x32_bf16 v[100:103], v[80:83], v[188:191], v[100:103]\n"
"v_cvt_pk_bf16_f32 v88, v120, v121\n"
"v_cvt_pk_bf16_f32 v89, v122, v123\n"
"v_mfma_f32_16x16x32_bf16 v[100:103], v[76:79], v[168:171], v[100:103]\n"
"v_cvt_pk_bf16_f32 v90, v176, v177\n"
"v_cvt_pk_bf16_f32 v91, v178, v179\n"
"v_mfma_f32_16x16x32_bf16 v[100:103], v[72:75], v[144:147], v[100:103]\n"
"ds_write_b128 v8, v[88:91] offset:17408\n"
"v_mfma_f32_16x16x32_bf16 v[100:103], v[68:71], v[164:167], v[100:103]\n"
"v_add_f32_e32 v196, v196, v208\n"
"v_add_f32_e32 v197, v197, v209\n"
"v_mfma_f32_16x16x32_bf16 v[100:103], v[64:67], v[92:95], v[100:103]\n"
"v_add_f32_e32 v198, v198, v210\n"
"v_add_f32_e32 v199, v199, v211\n"
"v_mfma_f32_16x16x32_bf16 v[100:103], v[60:63], v[136:139], v[100:103]\n"
"v_add_f32_e32 v212, v212, v216\n"
"v_add_f32_e32 v213, v213, v217\n"
"v_mfma_f32_16x16x32_bf16 v[100:103], v[56:59], v[108:111], v[100:103]\n"
"v_add_f32_e32 v214, v214, v218\n"
"v_add_f32_e32 v215, v215, v219\n"
"v_mul_f32_e32 v196, v196, v255\n"
"v_mul_f32_e32 v197, v197, v255\n"
"v_mul_f32_e32 v198, v198, v255\n"
"v_mul_f32_e32 v199, v199, v255\n"
"v_mul_f32_e32 v212, v212, v255\n"
"v_mul_f32_e32 v213, v213, v255\n"
"v_mul_f32_e32 v214, v214, v255\n"
"v_mul_f32_e32 v215, v215, v255\n"
"v_cvt_pk_bf16_f32 v88, v196, v197\n"
"v_cvt_pk_bf16_f32 v89, v198, v199\n"
"v_cvt_pk_bf16_f32 v90, v212, v213\n"
"v_cvt_pk_bf16_f32 v91, v214, v215\n"
"ds_write_b128 v7, v[88:91] offset:50176\n"
".Lmk_b3:\n"
"s_waitcnt vmcnt(18)\n"
"; @B G1 3.0\n"
"v_mfma_f32_16x16x32_bf16 v[88:91], a[0:3], v[116:119], 0\n"
"v_mfma_f32_16x16x32_bf16 v[88:91], a[4:7], v[156:159], v[88:91]\n"
"s_add_u32 s98, s62, 0x40000\n"
"v_mfma_f32_16x16x32_bf16 v[88:91], a[8:11], v[184:187], v[88:91]\n"
"s_add_u32 s96, s54, 0x400000\n"
"buffer_load_dwordx4 v[92:95], v0, s[4:7], s96 offen sc1\n"
"v_mfma_f32_16x16x32_bf16 v[88:91], a[12:15], v[148:151], v[88:91]\n"
"s_waitcnt vmcnt(12)\n"
"v_mfma_f32_16x16x32_bf16 v[88:91], a[16:19], v[160:163], v[88:91]\n"
"buffer_load_dword v255, v10, s[4:7], s98 offen sc1\n"
"v_mfma_f32_16x16x32_bf16 v[88:91], a[20:23], v[224:227], v[88:91]\n"
"s_waitcnt lgkmcnt(2)\n"
"v_add_f32_e32 v112, v112, v192\n"
"v_add_f32_e32 v113, v113, v193\n"
"v_mfma_f32_16x16x32_bf16 v[88:91], a[24:27], v[204:207], v[88:91]\n"
"v_add_f32_e32 v114, v114, v194\n"
"v_add_f32_e32 v115, v115, v195\n"
"v_mfma_f32_16x16x32_bf16 v[88:91], a[28:31], v[132:135], v[88:91]\n"
"buffer_load_dwordx4 v[108:111], v0, s[4:7], s96 offen offset:1024 sc1\n"
"v_mfma_f32_16x16x32_bf16 v[120:123], a[60:63], v[132:135], 0\n"
"s_waitcnt lgkmcnt(0)\n"
"s_barrier\n"
"v_mfma_f32_16x16x32_bf16 v[120:123], a[56:59], v[204:207], v[120:123]\n"
"buffer_load_dwordx4 a[228:231], v0, s[4:7], s97 offen offset:1024 sc1\n"
"v_mfma_f32_16x16x32_bf16 v[120:123], a[52:55], v[224:227], v[120:123]\n"
"buffer_load_dword v253, v10, s[4:7], s98 offen offset:256 sc1\n"
"v_mfma_f32_16x16x32_bf16 v[120:123], a[48:51], v[160:163], v[120:123]\n"
"v_add_f32_e32 v100, v100, v220\n"
"v_add_f32_e32 v101, v101, v221\n"
"v_mfma_f32_16x16x32_bf16 v[120:123], a[44:47], v[148:151], v[120:123]\n"
"buffer_load_dwordx4 v[136:139], v0, s[4:7], s96 offen offset:2048 sc1\n"
"v_mfma_f32_16x16x32_bf16 v[120:123], a[40:43], v[184:187], v[120:123]\n"
"ds_write_b128 v3, v[88:91]\n"
"v_mfma_f32_16x16x32_bf16 v[120:123], a[36:39], v[156:159], v[120:123]\n"
"buffer_load_dword v252, v10, s[4:7], s98 offen offset:512 sc1\n"
"v_mfma_f32_16x16x32_bf16 v[120:123], a[32:35], v[116:119], v[120:123]\n"
"v_add_f32_e32 v102, v102, v222\n"
"v_add_f32_e32 v103, v103, v223\n"
"v_mfma_f32_16x16x32_bf16 v[88:91], a[64:67], v[116:119], 0\n"
"v_mul_f32_e32 v112, v112, v254\n"
"v_mul_f32_e32 v113, v113, v254\n"
"v_mfma_f32_16x16x32_bf16 v[88:91], a[68:71], v[156:159], v[88:91]\n"
"buffer_load_dwordx4 v[144:147], v0, s[4:7], s96 offen offset:3072 sc1\n"
"v_mfma_f32_16x16x32_bf16 v[88:91], a[72:75], v[184:187], v[88:91]\n"
"buffer_load_dword v251, v10, s[4:7], s98 offen offset:768 sc1\n"
"v_mfma_f32_16x16x32_bf16 v[88:91], a[76:79], v[148:151], v[88:91]\n"
"v_mul_f32_e32 v114, v114, v254\n"
"v_mul_f32_e32 v115, v115, v254\n"
"v_mfma_f32_16x16x32_bf16 v[88:91], a[80:83], v[160:163], v[88:91]\n"
"v_mul_f32_e32 v100, v100, v254\n"
"v_mul_f32_e32 v101, v101, v254\n"
"v_add_u32_e32 v250, 0x26c00, v0\n"
"v_mfma_f32_16x16x32_bf16 v[88:91], a[84:87], v[224:227], v[88:91]\n"
"ds_write_b128 v3, v[120:123] offset:1024\n"
"v_mfma_f32_16x16x32_bf16 v[88:91], a[88:91], v[204:207], v[88:91]\n"
"buffer_load_dwordx4 v[120:123], v1, s[4:7], s96 offen sc1\n"
"ds_read_b128 v[164:167], v250\n"
"v_mfma_f32_16x16x32_bf16 v[88:91], a[92:95], v[132:135], v[88:91]\n"
"buffer_load_dwordx4 a[232:235], v0, s[4:7], s97 offen offset:2048 sc1\n"
"ds_read_b128 v[168:171], v250 offset:1024\n"
"v_mfma_f32_16x16x32_bf16 v[172:175], a[124:127], v[132:135], 0\n"
"buffer_load_dword v250, v10, s[4:7], s98 offen offset:1024 sc1\n"
"v_xor_b32_e32 v249, 32, v17\n"
"v_mfma_f32_16x16x32_bf16 v[172:175], a[120:123], v[204:207], v[172:175]\n"
"ds_read_b128 v[176:179], v2 offset:49152\n"
"v_mul_f32_e32 v102, v102, v254\n"
"v_mul_f32_e32 v103, v103, v254\n"
"v_mfma_f32_16x16x32_bf16 v[172:175], a[116:119], v[224:227], v[172:175]\n"
"v_cvt_pk_bf16_f32 v188, v112, v113\n"
"v_cvt_pk_bf16_f32 v189, v114, v115\n"
"v_mfma_f32_16x16x32_bf16 v[172:175], a[112:115], v[160:163], v[172:175]\n"
"ds_read_b128 v[112:115], v17 offset:23552\n"
"buffer_load_dwordx4 v[192:195], v1, s[4:7], s96 offen offset:1024 sc1\n"
"ds_read_b128 v[196:199], v249 offset:23808\n"
"v_mfma_f32_16x16x32_bf16 v[172:175], a[108:111], v[148:151], v[172:175]\n"
"buffer_load_dword v254, v10, s[4:7], s98 offen offset:1280 sc1\n"
"v_mfma_f32_16x16x32_bf16 v[172:175], a[104:107], v[184:187], v[172:175]\n"
"ds_write_b128 v3, v[88:91] offset:2048\n"
"v_mfma_f32_16x16x32_bf16 v[172:175], a[100:103], v[156:159], v[172:175]\n"
"v_cvt_pk_bf16_f32 v190, v100, v101\n"
"v_cvt_pk_bf16_f32 v191, v102, v103\n"
"v_mfma_f32_16x16x32_bf16 v[172:175], a[96:99], v[116:119], v[172:175]\n"
"buffer_load_dword v249, v10, s[4:7], s98 offen offset:1536 sc1\n"
"v_mfma_f32_16x16x32_bf16 v[88:91], v[24:27], v[116:119], 0\n"
"buffer_load_dwordx4 v[100:103], v1, s[4:7], s96 offen offset:2048 sc1\n"
"s_waitcnt lgkmcnt(1)\n"
"v_mfma_f32_16x16x32_bf16 v[164:167], v[112:115], v[176:179], v[164:167]\n"
"v_mfma_f32_16x16x32_bf16 v[168:171], v[196:199], v[176:179], v[168:171]\n"
"v_mfma_f32_16x16x32_bf16 v[88:91], v[28:31], v[156:159], v[88:91]\n"
"ds_write_b128 v2, v[188:191] offset:17408\n"
"v_mfma_f32_16x16x32_bf16 v[88:91], v[32:35], v[184:187], v[88:91]\n"
"buffer_load_dword v248, v10, s[4:7], s98 offen offset:1792 sc1\n"
"v_xor_b32_e32 v247, 32, v16\n"
"v_mfma_f32_16x16x32_bf16 v[88:91], v[36:39], v[148:151], v[88:91]\n"
"ds_read_b64_tr_b16 v[112:113], v16 offset:23552\n"
"ds_read_b64_tr_b16 v[114:115], v247 offset:23808\n"
"ds_read_b64_tr_b16 v[176:177], v247 offset:23552\n"
"v_mfma_f32_16x16x32_bf16 v[88:91], v[40:43], v[160:163], v[88:91]\n"
"buffer_load_dwordx4 a[236:239], v0, s[4:7], s97 offen offset:3072 sc1\n"
"ds_read_b64_tr_b16 v[178:179], v16 offset:23808\n"
"v_mfma_f32_16x16x32_bf16 v[88:91], v[44:47], v[224:227], v[88:91]\n"
"buffer_load_dwordx4 v[188:191], v1, s[4:7], s96 offen offset:3072 sc1\n"
"v_mov_b32_e32 v196, 0x3f803f80\n"
"v_mfma_f32_16x16x32_bf16 v[88:91], v[48:51], v[204:207], v[88:91]\n"
"ds_write_b128 v3, v[172:175] offset:3072\n"
"v_mfma_f32_16x16x32_bf16 v[88:91], v[52:55], v[132:135], v[88:91]\n"
"v_mov_b32_e32 v197, 0x3f803f80\n"
"v_mov_b32_e32 v198, 0x3f803f80\n"
"v_mov_b32_e32 v199, 0x3f803f80\n"
"v_mfma_f32_16x16x32_bf16 v[172:175], v[84:87], v[132:135], 0\n"
"v_max_f32_e32 v247, v164, v165\n"
"v_max3_f32 v247, v247, v166, v167\n"
"v_max3_f32 v247, v247, v168, v169\n"
"v_mfma_f32_16x16x32_bf16 v[172:175], v[80:83], v[204:207], v[172:175]\n"
"v_max3_f32 v247, v247, v170, v171\n"
"v_mov_b32_e32 v246, v247\n"
"v_mfma_f32_16x16x32_bf16 v[172:175], v[76:79], v[224:227], v[172:175]\n"
"s_nop 0\n"
"v_permlane16_swap_b32_e32 v247, v246\n"
"v_max_f32_e32 v247, v247, v246\n"
"v_mov_b32_e32 v246, v247\n"
"v_mfma_f32_16x16x32_bf16 v[172:175], v[72:75], v[160:163], v[172:175]\n"
"s_nop 0\n"
"v_permlane32_swap_b32_e32 v247, v246\n"
"v_max_f32_e32 v247, v247, v246\n"
"v_sub_f32_e32 v164, v164, v247\n"
"v_mfma_f32_16x16x32_bf16 v[172:175], v[68:71], v[148:151], v[172:175]\n"
"v_sub_f32_e32 v165, v165, v247\n"
"v_sub_f32_e32 v166, v166, v247\n"
"v_sub_f32_e32 v167, v167, v247\n"
"v_mfma_f32_16x16x32_bf16 v[172:175], v[64:67], v[184:187], v[172:175]\n"
"ds_write_b128 v3, v[88:91] offset:4096\n"
"v_mfma_f32_16x16x32_bf16 v[172:175], v[60:63], v[156:159], v[172:175]\n"
"v_sub_f32_e32 v168, v168, v247\n"
"v_sub_f32_e32 v169, v169, v247\n"
"v_sub_f32_e32 v170, v170, v247\n"
"v_mfma_f32_16x16x32_bf16 v[172:175], v[56:59], v[116:119], v[172:175]\n"
"s_waitcnt vmcnt(25)\n"
"v_sub_f32_e32 v171, v171, v247\n"
"v_exp_f32_e32 v164, v164\n"
"; @B G1 3.1\n"
"v_mfma_f32_16x16x32_bf16 v[88:91], a[0:3], v[104:107], 0\n"
"v_exp_f32_e32 v165, v165\n"
"v_mfma_f32_16x16x32_bf16 v[88:91], a[4:7], v[140:143], v[88:91]\n"
"buffer_load_dwordx4 a[240:243], v1, s[4:7], s97 offen sc1\n"
"v_mfma_f32_16x16x32_bf16 v[88:91], a[8:11], v[152:155], v[88:91]\n"
"s_add_u32 s98, s55, 0x400000\n"
"buffer_load_dwordx4 v[116:119], v0, s[4:7], s98 offen sc1\n"
"v_mfma_f32_16x16x32_bf16 v[88:91], a[12:15], v[200:203], v[88:91]\n"
"s_waitcnt vmcnt(22)\n"
"v_exp_f32_e32 v166, v166\n"
"v_mfma_f32_16x16x32_bf16 v[88:91], a[16:19], v[96:99], v[88:91]\n"
"v_exp_f32_e32 v167, v167\n"
"v_mfma_f32_16x16x32_bf16 v[88:91], a[20:23], v[124:127], v[88:91]\n"
"ds_write_b128 v3, v[172:175] offset:5120\n"
"v_mfma_f32_16x16x32_bf16 v[88:91], a[24:27], v[128:131], v[88:91]\n"
"v_exp_f32_e32 v168, v168\n"
"v_mfma_f32_16x16x32_bf16 v[88:91], a[28:31], v[180:183], v[88:91]\n"
"buffer_load_dwordx4 v[132:135], v0, s[4:7], s98 offen offset:1024 sc1\n"
"v_mfma_f32_16x16x32_bf16 v[148:151], a[60:63], v[180:183], 0\n"
"v_exp_f32_e32 v169, v169\n"
"v_mfma_f32_16x16x32_bf16 v[148:151], a[56:59], v[128:131], v[148:151]\n"
"v_exp_f32_e32 v170, v170\n"
"v_mfma_f32_16x16x32_bf16 v[148:151], a[52:55], v[124:127], v[148:151]\n"
"v_exp_f32_e32 v171, v171\n"
"v_cvt_pk_bf16_f32 v156, v164, v165\n"
"v_mfma_f32_16x16x32_bf16 v[148:151], a[48:51], v[96:99], v[148:151]\n"
"v_cvt_pk_bf16_f32 v157, v166, v167\n"
"v_cvt_pk_bf16_f32 v158, v168, v169\n"
"v_cvt_pk_bf16_f32 v159, v170, v171\n"
"v_mfma_f32_16x16x32_bf16 v[148:151], a[44:47], v[200:203], v[148:151]\n"
"buffer_load_dwordx4 v[160:163], v0, s[4:7], s98 offen offset:2048 sc1\n"
"s_waitcnt lgkmcnt(3)\n"
"v_mfma_f32_16x16x32_bf16 v[164:167], v[196:199], v[156:159], 0\n"
"v_mfma_f32_16x16x32_bf16 v[168:171], v[112:115], v[156:159], 0\n"
"v_mfma_f32_16x16x32_bf16 v[112:115], v[176:179], v[156:159], 0\n"
"v_mfma_f32_16x16x32_bf16 v[148:151], a[40:43], v[152:155], v[148:151]\n"
"ds_write_b128 v3, v[88:91] offset:6144\n"
"v_mfma_f32_16x16x32_bf16 v[148:151], a[36:39], v[140:143], v[148:151]\n"
"s_add_u32 s96, s73, 0\n"
"buffer_load_dwordx2 v[246:247], off, s[4:7], s96 sc1\n"
"v_mfma_f32_16x16x32_bf16 v[148:151], a[32:35], v[104:107], v[148:151]\n"
"buffer_load_dwordx4 a[244:247], v1, s[4:7], s97 offen offset:1024 sc1\n"
"v_mfma_f32_16x16x32_bf16 v[88:91], a[64:67], v[104:107], 0\n"
"v_mfma_f32_16x16x32_bf16 v[88:91], a[68:71], v[140:143], v[88:91]\n"
"buffer_load_dwordx4 v[156:159], v0, s[4:7], s98 offen offset:3072 sc1\n"
"v_mfma_f32_16x16x32_bf16 v[88:91], a[72:75], v[152:155], v[88:91]\n"
"v_rcp_f32_e32 v164, v164\n"
"v_mfma_f32_16x16x32_bf16 v[88:91], a[76:79], v[200:203], v[88:91]\n"
"v_mul_f32_e32 v168, v168, v164\n"
"v_mul_f32_e32 v169, v169, v164\n"
"v_mul_f32_e32 v170, v170, v164\n"
"v_mfma_f32_16x16x32_bf16 v[88:91], a[80:83], v[96:99], v[88:91]\n"
"v_mov_b32_e32 v172, -1\n"
"v_mov_b32_e32 v173, -1\n"
"v_mul_f32_e32 v171, v171, v164\n"
"v_mfma_f32_16x16x32_bf16 v[88:91], a[84:87], v[124:127], v[88:91]\n"
"ds_write_b128 v3, v[148:151] offset:7168\n"
"v_mfma_f32_16x16x32_bf16 v[88:91], a[88:91], v[128:131], v[88:91]\n"
"buffer_load_dwordx4 v[148:151], v1, s[4:7], s98 offen sc1\n"
"v_mul_f32_e32 v112, v112, v164\n"
"v_mfma_f32_16x16x32_bf16 v[88:91], a[92:95], v[180:183], v[88:91]\n"
"v_mov_b32_e32 v174, -1\n"
"v_mov_b32_e32 v175, -1\n"
"v_mul_f32_e32 v113, v113, v164\n"
"v_mfma_f32_16x16x32_bf16 v[176:179], a[124:127], v[180:183], 0\n"
"s_add_u32 s95, s60, 0x808000\n"
"buffer_store_dwordx4 v[172:175], v0, s[8:11], s95 offen sc1\n"
"v_mfma_f32_16x16x32_bf16 v[176:179], a[120:123], v[128:131], v[176:179]\n"
"v_mul_f32_e32 v114, v114, v164\n"
"v_mul_f32_e32 v115, v115, v164\n"
"v_cvt_pk_bf16_f32 v164, v168, v169\n"
"v_mfma_f32_16x16x32_bf16 v[176:179], a[116:119], v[124:127], v[176:179]\n"
"s_add_u32 s95, s60, 0x440000\n"
"v_cvt_pk_bf16_f32 v165, v170, v171\n"
"v_cvt_pk_bf16_f32 v166, v112, v113\n"
"v_cvt_pk_bf16_f32 v167, v114, v115\n"
"v_mfma_f32_16x16x32_bf16 v[176:179], a[112:115], v[96:99], v[176:179]\n"
"buffer_load_dwordx4 v[112:115], v1, s[4:7], s98 offen offset:1024 sc1\n"
"v_mfma_f32_16x16x32_bf16 v[176:179], a[108:111], v[200:203], v[176:179]\n"
"buffer_load_dwordx4 a[248:251], v1, s[4:7], s97 offen offset:2048 sc1\n"
"v_mfma_f32_16x16x32_bf16 v[176:179], a[104:107], v[152:155], v[176:179]\n"
"ds_write_b128 v3, v[88:91] offset:8192\n"
"v_mfma_f32_16x16x32_bf16 v[176:179], a[100:103], v[140:143], v[176:179]\n"
"buffer_store_dwordx4 v[164:167], v0, s[4:7], s95 offen sc1\n"
"v_mfma_f32_16x16x32_bf16 v[176:179], a[96:99], v[104:107], v[176:179]\n"
";\n"
"v_mfma_f32_16x16x32_bf16 v[88:91], v[24:27], v[104:107], 0\n"
"buffer_load_dwordx4 v[164:167], v1, s[4:7], s98 offen offset:2048 sc1\n"
"v_mfma_f32_16x16x32_bf16 v[88:91], v[28:31], v[140:143], v[88:91]\n"
"v_mfma_f32_16x16x32_bf16 v[88:91], v[32:35], v[152:155], v[88:91]\n"
"v_mfma_f32_16x16x32_bf16 v[88:91], v[36:39], v[200:203], v[88:91]\n"
"v_mfma_f32_16x16x32_bf16 v[88:91], v[40:43], v[96:99], v[88:91]\n"
"s_waitcnt vmcnt(15)\n"
"v_add_f32_e32 v255, v255, v250\n"
"v_add_f32_e32 v253, v253, v254\n"
"v_mfma_f32_16x16x32_bf16 v[88:91], v[44:47], v[124:127], v[88:91]\n"
"buffer_load_dwordx4 v[168:171], v1, s[4:7], s98 offen offset:3072 sc1\n"
"v_mfma_f32_16x16x32_bf16 v[88:91], v[48:51], v[128:131], v[88:91]\n"
"ds_write_b128 v3, v[176:179] offset:9216\n"
"v_mfma_f32_16x16x32_bf16 v[88:91], v[52:55], v[180:183], v[88:91]\n"
"v_add_f32_e32 v252, v252, v249\n"
"v_add_f32_e32 v251, v251, v248\n"
"v_mfma_f32_16x16x32_bf16 v[176:179], v[84:87], v[180:183], 0\n"
"v_add_f32_e32 v255, v255, v252\n"
"v_add_f32_e32 v253, v253, v251\n"
"v_mfma_f32_16x16x32_bf16 v[176:179], v[80:83], v[128:131], v[176:179]\n"
"buffer_load_dwordx4 a[252:255], v1, s[4:7], s97 offen offset:3072 sc1\n"
"v_mfma_f32_16x16x32_bf16 v[176:179], v[76:79], v[124:127], v[176:179]\n"
"v_add_f32_e32 v255, v255, v253\n"
"v_mov_b32_e32 v253, v255\n"
"v_mfma_f32_16x16x32_bf16 v[176:179], v[72:75], v[96:99], v[176:179]\n"
"s_nop 0\n"
"v_permlane32_swap_b32_e32 v255, v253\n"
"v_add_f32_e32 v255, v255, v253\n"
"v_mfma_f32_16x16x32_bf16 v[176:179], v[68:71], v[200:203], v[176:179]\n"
"v_mul_f32_e32 v255, 0x3a800000, v255\n"
"v_mov_b32_e32 v253, v255\n"
"v_mfma_f32_16x16x32_bf16 v[176:179], v[64:67], v[152:155], v[176:179]\n"
"ds_write_b128 v3, v[88:91] offset:10240\n"
"v_mfma_f32_16x16x32_bf16 v[176:179], v[60:63], v[140:143], v[176:179]\n"
"v_permlane16_swap_b32_e32 v255, v253\n"
"v_cvt_pk_bf16_f32 v254, -v255, 0\n"
"v_mfma_f32_16x16x32_bf16 v[176:179], v[56:59], v[104:107], v[176:179]\n"
"v_fma_f32 v253, -v255, v255, v253\n"
"v_lshlrev_b32_e32 v254, 16, v254\n"
"; @B G1 3.2\n"
"v_mfma_f32_16x16x32_bf16 v[88:91], a[0:3], v[92:95], 0\n"
"s_add_u32 s98, s67, 0x10020\n"
"buffer_load_dwordx2 v[250:251], v12, s[4:7], s98 offen sc1\n"
"v_mfma_f32_16x16x32_bf16 v[88:91], a[4:7], v[108:111], v[88:91]\n"
"v_max_f32_e32 v253, 0, v253\n"
"v_cvt_pk_bf16_f32 v253, v253, 0\n"
"v_mfma_f32_16x16x32_bf16 v[88:91], a[8:11], v[136:139], v[88:91]\n"
"v_lshlrev_b32_e32 v253, 16, v253\n"
"v_add_f32_e32 v253, 0x3727c5ac, v253\n"
"v_mfma_f32_16x16x32_bf16 v[88:91], a[12:15], v[144:147], v[88:91]\n"
"s_waitcnt vmcnt(16)\n"
"v_cvt_pk_bf16_f32 v253, v253, 0\n"
"v_lshlrev_b32_e32 v253, 16, v253\n"
"v_mfma_f32_16x16x32_bf16 v[88:91], a[16:19], v[120:123], v[88:91]\n"
"v_sqrt_f32_e32 v253, v253\n"
"v_mfma_f32_16x16x32_bf16 v[88:91], a[20:23], v[192:195], v[88:91]\n"
"ds_write_b128 v3, v[176:179] offset:11264\n"
"v_mfma_f32_16x16x32_bf16 v[88:91], a[24:27], v[100:103], v[88:91]\n"
"v_cvt_pk_bf16_f32 v253, v253, 0\n"
"v_lshlrev_b32_e32 v253, 16, v253\n"
"v_mfma_f32_16x16x32_bf16 v[88:91], a[28:31], v[188:191], v[88:91]\n"
"v_rcp_f32_e32 v255, v253\n"
"v_mfma_f32_16x16x32_bf16 v[96:99], a[60:63], v[188:191], 0\n"
"v_mul_f32_e32 v253, 0x3e8293ee, v255\n"
"v_add_u32_e32 v252, 0x26c00, v0\n"
"v_mfma_f32_16x16x32_bf16 v[96:99], a[56:59], v[100:103], v[96:99]\n"
"ds_read_b128 v[104:107], v252\n"
"ds_read_b128 v[124:127], v252 offset:1024\n"
"v_mfma_f32_16x16x32_bf16 v[96:99], a[52:55], v[192:195], v[96:99]\n"
"s_waitcnt lgkmcnt(0)\n"
"s_barrier\n"
"v_mfma_f32_16x16x32_bf16 v[96:99], a[48:51], v[120:123], v[96:99]\n"
"s_add_u32 s97, s61, 0xc40000\n"
"ds_read_b128 v[128:131], v4\n"
"ds_read_b128 v[140:143], v9 offset:640\n"
"v_mfma_f32_16x16x32_bf16 v[96:99], a[44:47], v[144:147], v[96:99]\n"
"ds_read_b128 v[152:155], v5\n"
"v_add_u32_e32 v252, 0x26c00, v0\n"
"v_mfma_f32_16x16x32_bf16 v[96:99], a[40:43], v[136:139], v[96:99]\n"
"ds_write_b128 v3, v[88:91] offset:12288\n"
"v_mfma_f32_16x16x32_bf16 v[96:99], a[36:39], v[108:111], v[96:99]\n"
"buffer_store_dwordx4 v[172:175], v0, s[8:11], s97 offen sc1\n"
"v_mfma_f32_16x16x32_bf16 v[96:99], a[32:35], v[92:95], v[96:99]\n"
"ds_read_b128 v[88:91], v252\n"
"ds_read_b128 v[176:179], v252 offset:1024\n"
"v_mfma_f32_16x16x32_bf16 v[180:183], a[64:67], v[92:95], 0\n"
"v_mov_b32_e32 v252, 0xff800000\n"
"v_mfma_f32_16x16x32_bf16 v[180:183], a[68:71], v[108:111], v[180:183]\n"
"s_waitcnt lgkmcnt(3)\n"
"v_fmac_f32_e32 v128, v254, v140\n"
"v_fmac_f32_e32 v129, v254, v141\n"
"v_mfma_f32_16x16x32_bf16 v[180:183], a[72:75], v[136:139], v[180:183]\n"
"ds_read_b128 v[184:187], v4 offset:1024\n"
"ds_read_b128 v[196:199], v9 offset:704\n"
"v_mfma_f32_16x16x32_bf16 v[180:183], a[76:79], v[144:147], v[180:183]\n"
"ds_read_b128 v[200:203], v5 offset:1024\n"
"v_fmac_f32_e32 v130, v254, v142\n"
"v_mfma_f32_16x16x32_bf16 v[180:183], a[80:83], v[120:123], v[180:183]\n"
"v_fmac_f32_e32 v131, v254, v143\n"
"v_add_f32_e32 v128, v128, v152\n"
"v_mfma_f32_16x16x32_bf16 v[180:183], a[84:87], v[192:195], v[180:183]\n"
"ds_write_b128 v3, v[96:99] offset:13312\n"
"v_mfma_f32_16x16x32_bf16 v[180:183], a[88:91], v[100:103], v[180:183]\n"
"s_waitcnt lgkmcnt(4)\n"
"v_add_f32_e32 v129, v129, v153\n"
"v_add_f32_e32 v130, v130, v154\n"
"v_mfma_f32_16x16x32_bf16 v[180:183], a[92:95], v[188:191], v[180:183]\n"
"v_add_f32_e32 v131, v131, v155\n"
"v_cndmask_b32_e64 v88, v88, v252, s[82:83]\n"
"v_mfma_f32_16x16x32_bf16 v[96:99], a[124:127], v[188:191], 0\n"
"s_waitcnt vmcnt(12)\n"
"v_cmp_ne_u64_e32 vcc, v[246:247], v[18:19]\n"
"v_mfma_f32_16x16x32_bf16 v[96:99], a[120:123], v[100:103], v[96:99]\n"
"s_cbranch_vccnz .Lmk_f45s\n"
".Lmk_f45r:\n"
"s_mov_b32 m0, 0x7c00\n"
"v_mfma_f32_16x16x32_bf16 v[96:99], a[116:119], v[192:195], v[96:99]\n"
"s_add_u32 s97, s66, 0x3c00\n"
"buffer_load_dwordx4 v0, s[4:7], s97 offen sc1 lds\n"
"v_mfma_f32_16x16x32_bf16 v[96:99], a[112:115], v[120:123], v[96:99]\n"
"s_mov_b32 m0, 0xfc00\n"
"s_add_u32 s97, s66, 0x7c00\n"
"ds_read_b128 v[140:143], v4 offset:2048\n"
"v_mfma_f32_16x16x32_bf16 v[96:99], a[108:111], v[144:147], v[96:99]\n"
"buffer_load_dwordx4 v0, s[4:7], s97 offen sc1 lds\n"
"v_mfma_f32_16x16x32_bf16 v[96:99], a[104:107], v[136:139], v[96:99]\n"
"ds_write_b128 v3, v[180:183] offset:14336\n"
"v_mfma_f32_16x16x32_bf16 v[96:99], a[100:103], v[108:111], v[96:99]\n"
"ds_read_b128 v[152:155], v9 offset:768\n"
"ds_read_b128 v[180:183], v5 offset:2048\n"
"v_mfma_f32_16x16x32_bf16 v[96:99], a[96:99], v[92:95], v[96:99]\n"
"s_waitcnt lgkmcnt(5)\n"
"v_fmac_f32_e32 v184, v254, v196\n"
"v_fmac_f32_e32 v185, v254, v197\n"
"v_mfma_f32_16x16x32_bf16 v[204:207], v[24:27], v[92:95], 0\n"
"ds_read_b128 v[208:211], v4 offset:3072\n"
"ds_read_b128 v[212:215], v9 offset:832\n"
"v_mfma_f32_16x16x32_bf16 v[204:207], v[28:31], v[108:111], v[204:207]\n"
"ds_read_b128 v[216:219], v5 offset:3072\n"
"v_fmac_f32_e32 v186, v254, v198\n"
"v_mfma_f32_16x16x32_bf16 v[204:207], v[32:35], v[136:139], v[204:207]\n"
"v_fmac_f32_e32 v187, v254, v199\n"
"v_add_f32_e32 v184, v184, v200\n"
"v_mfma_f32_16x16x32_bf16 v[204:207], v[36:39], v[144:147], v[204:207]\n"
"v_add_f32_e32 v185, v185, v201\n"
"v_add_f32_e32 v186, v186, v202\n"
"v_mfma_f32_16x16x32_bf16 v[204:207], v[40:43], v[120:123], v[204:207]\n"
"s_waitcnt lgkmcnt(3)\n"
"v_add_f32_e32 v187, v187, v203\n"
"v_fmac_f32_e32 v140, v254, v152\n"
"v_mfma_f32_16x16x32_bf16 v[204:207], v[44:47], v[192:195], v[204:207]\n"
"ds_write_b128 v3, v[96:99] offset:15360\n"
"v_mfma_f32_16x16x32_bf16 v[204:207], v[48:51], v[100:103], v[204:207]\n"
"v_fmac_f32_e32 v141, v254, v153\n"
"v_fmac_f32_e32 v142, v254, v154\n"
"v_mfma_f32_16x16x32_bf16 v[204:207], v[52:55], v[188:191], v[204:207]\n"
"ds_read_b128 v[96:99], v4 offset:4096\n"
"ds_read_b128 v[196:199], v9 offset:512\n"
"v_mfma_f32_16x16x32_bf16 v[200:203], v[84:87], v[188:191], 0\n"
"ds_read_b128 v[188:191], v5 offset:4096\n"
"v_fmac_f32_e32 v143, v254, v155\n"
"v_mfma_f32_16x16x32_bf16 v[200:203], v[80:83], v[100:103], v[200:203]\n"
"v_add_f32_e32 v140, v140, v180\n"
"v_add_f32_e32 v141, v141, v181\n"
"v_mfma_f32_16x16x32_bf16 v[200:203], v[76:79], v[192:195], v[200:203]\n"
"s_waitcnt lgkmcnt(4)\n"
"v_add_f32_e32 v142, v142, v182\n"
"v_add_f32_e32 v143, v143, v183\n"
"v_mfma_f32_16x16x32_bf16 v[200:203], v[72:75], v[120:123], v[200:203]\n"
"v_fmac_f32_e32 v208, v254, v212\n"
"v_fmac_f32_e32 v209, v254, v213\n"
"v_mfma_f32_16x16x32_bf16 v[200:203], v[68:71], v[144:147], v[200:203]\n"
"v_fmac_f32_e32 v210, v254, v214\n"
"v_fmac_f32_e32 v211, v254, v215\n"
"v_mfma_f32_16x16x32_bf16 v[200:203], v[64:67], v[136:139], v[200:203]\n"
"ds_write_b128 v3, v[204:207] offset:16384\n"
"v_mfma_f32_16x16x32_bf16 v[200:203], v[60:63], v[108:111], v[200:203]\n"
"ds_read_b128 v[100:103], v4 offset:5120\n"
"ds_read_b128 v[108:111], v9 offset:576\n"
"v_mfma_f32_16x16x32_bf16 v[200:203], v[56:59], v[92:95], v[200:203]\n"
"s_waitcnt vmcnt(12)\n"
"s_waitcnt vmcnt(3)\n"
"v_cmp_ne_u64_e32 vcc, v[250:251], v[18:19]\n"
"; @B G1 3.3\n"
"v_mfma_f32_16x16x32_bf16 v[128:131], a[0:3], v[116:119], v[128:131]\n"
"s_cbranch_vccnz .Lmk_f46s\n"
".Lmk_f46r:\n"
"ds_read_b128 v[92:95], v5 offset:5120\n"
"v_mfma_f32_16x16x32_bf16 v[128:131], a[4:7], v[132:135], v[128:131]\n"
"s_add_u32 s98, s61, 0xc40000\n"
"s_add_u32 s97, s56, 0x440000\n"
"v_add_f32_e32 v208, v208, v216\n"
"v_mfma_f32_16x16x32_bf16 v[128:131], a[8:11], v[160:163], v[128:131]\n"
"buffer_load_dwordx4 v[120:123], v0, s[4:7], s97 offen sc1\n"
"v_mfma_f32_16x16x32_bf16 v[128:131], a[12:15], v[156:159], v[128:131]\n"
"buffer_store_dwordx4 v[172:175], v0, s[8:11], s98 offen offset:1024 sc1\n"
"v_mfma_f32_16x16x32_bf16 v[128:131], a[16:19], v[148:151], v[128:131]\n"
"s_add_u32 s98, s69, 0\n"
"v_add_f32_e32 v209, v209, v217\n"
"v_add_f32_e32 v210, v210, v218\n"
"v_mfma_f32_16x16x32_bf16 v[128:131], a[20:23], v[112:115], v[128:131]\n"
"ds_write_b128 v3, v[200:203] offset:17408\n"
"v_mfma_f32_16x16x32_bf16 v[128:131], a[24:27], v[164:167], v[128:131]\n"
"buffer_load_dwordx4 v[136:139], v0, s[4:7], s97 offen offset:1024 sc1\n"
"v_mfma_f32_16x16x32_bf16 v[128:131], a[28:31], v[168:171], v[128:131]\n"
"s_waitcnt lgkmcnt(5)\n"
"v_add_f32_e32 v211, v211, v219\n"
"v_fmac_f32_e32 v96, v254, v196\n"
"v_mfma_f32_16x16x32_bf16 v[184:187], a[60:63], v[168:171], v[184:187]\n"
"v_fmac_f32_e32 v97, v254, v197\n"
"v_fmac_f32_e32 v98, v254, v198\n"
"v_mfma_f32_16x16x32_bf16 v[184:187], a[56:59], v[164:167], v[184:187]\n"
"buffer_load_dwordx4 v[144:147], v0, s[4:7], s97 offen offset:2048 sc1\n"
"v_mfma_f32_16x16x32_bf16 v[184:187], a[52:55], v[112:115], v[184:187]\n"
"s_waitcnt lgkmcnt(0)\n"
"s_barrier\n"
"v_mfma_f32_16x16x32_bf16 v[184:187], a[48:51], v[148:151], v[184:187]\n"
"ds_read_b128 v[152:155], v6\n"
"ds_read_b128 v[180:183], v6 offset:1024\n"
"v_mfma_f32_16x16x32_bf16 v[184:187], a[44:47], v[156:159], v[184:187]\n"
"buffer_load_dwordx4 v[192:195], v0, s[4:7], s97 offen offset:3072 sc1\n"
"v_mfma_f32_16x16x32_bf16 v[184:187], a[40:43], v[160:163], v[184:187]\n"
"v_fmac_f32_e32 v99, v254, v199\n"
"v_add_f32_e32 v96, v96, v188\n"
"v_mfma_f32_16x16x32_bf16 v[184:187], a[36:39], v[132:135], v[184:187]\n"
"v_add_f32_e32 v97, v97, v189\n"
"v_add_f32_e32 v98, v98, v190\n"
"v_mfma_f32_16x16x32_bf16 v[184:187], a[32:35], v[116:119], v[184:187]\n"
"buffer_load_dwordx4 v[196:199], v1, s[4:7], s97 offen sc1\n"
"v_mfma_f32_16x16x32_bf16 v[140:143], a[64:67], v[116:119], v[140:143]\n"
"v_add_f32_e32 v99, v99, v191\n"
"v_fmac_f32_e32 v100, v254, v108\n"
"v_mfma_f32_16x16x32_bf16 v[140:143], a[68:71], v[132:135], v[140:143]\n"
"v_fmac_f32_e32 v101, v254, v109\n"
"v_fmac_f32_e32 v102, v254, v110\n"
"v_mfma_f32_16x16x32_bf16 v[140:143], a[72:75], v[160:163], v[140:143]\n"
"buffer_load_dwordx4 v[188:191], v1, s[4:7], s97 offen offset:1024 sc1\n"
"v_mfma_f32_16x16x32_bf16 v[140:143], a[76:79], v[156:159], v[140:143]\n"
"v_fmac_f32_e32 v103, v254, v111\n"
"v_add_f32_e32 v100, v100, v92\n"
"v_mfma_f32_16x16x32_bf16 v[140:143], a[80:83], v[148:151], v[140:143]\n"
"s_waitcnt lgkmcnt(0)\n"
"v_add_f32_e32 v101, v101, v93\n"
"v_add_f32_e32 v102, v102, v94\n"
"v_mfma_f32_16x16x32_bf16 v[140:143], a[84:87], v[112:115], v[140:143]\n"
"buffer_load_dwordx4 v[108:111], v1, s[4:7], s97 offen offset:2048 sc1\n"
"v_mfma_f32_16x16x32_bf16 v[140:143], a[88:91], v[164:167], v[140:143]\n"
"ds_read_b128 v[200:203], v6 offset:2048\n"
"ds_read_b128 v[204:207], v6 offset:3072\n"
"v_mfma_f32_16x16x32_bf16 v[140:143], a[92:95], v[168:171], v[140:143]\n"
"v_add_f32_e32 v103, v103, v95\n"
"v_add_f32_e32 v128, v128, v152\n"
"v_mfma_f32_16x16x32_bf16 v[208:211], a[124:127], v[168:171], v[208:211]\n"
"buffer_load_dwordx4 v[92:95], v1, s[4:7], s97 offen offset:3072 sc1\n"
"v_mfma_f32_16x16x32_bf16 v[208:211], a[120:123], v[164:167], v[208:211]\n"
"s_add_u32 s97, s57, 0x440000\n"
"v_add_f32_e32 v129, v129, v153\n"
"v_add_f32_e32 v130, v130, v154\n"
"v_mfma_f32_16x16x32_bf16 v[208:211], a[116:119], v[112:115], v[208:211]\n"
"v_add_f32_e32 v131, v131, v155\n"
"v_add_f32_e32 v184, v184, v180\n"
"v_mfma_f32_16x16x32_bf16 v[208:211], a[112:115], v[148:151], v[208:211]\n"
"buffer_load_dwordx4 v[152:155], v0, s[4:7], s97 offen sc1\n"
"v_mfma_f32_16x16x32_bf16 v[208:211], a[108:111], v[156:159], v[208:211]\n"
"v_add_f32_e32 v185, v185, v181\n"
"v_add_f32_e32 v186, v186, v182\n"
"v_mfma_f32_16x16x32_bf16 v[208:211], a[104:107], v[160:163], v[208:211]\n"
"v_add_f32_e32 v187, v187, v183\n"
"v_mul_f32_e32 v128, v128, v255\n"
"v_mfma_f32_16x16x32_bf16 v[208:211], a[100:103], v[132:135], v[208:211]\n"
"buffer_load_dwordx4 v[180:183], v0, s[4:7], s97 offen offset:1024 sc1\n"
"v_mfma_f32_16x16x32_bf16 v[208:211], a[96:99], v[116:119], v[208:211]\n"
"v_mul_f32_e32 v129, v129, v255\n"
"v_mul_f32_e32 v130, v130, v255\n"
"v_mfma_f32_16x16x32_bf16 v[96:99], v[24:27], v[116:119], v[96:99]\n"
"v_mul_f32_e32 v131, v131, v255\n"
"v_mul_f32_e32 v184, v184, v255\n"
"v_mfma_f32_16x16x32_bf16 v[96:99], v[28:31], v[132:135], v[96:99]\n"
"buffer_load_dwordx4 v[24:27], v0, s[4:7], s97 offen offset:2048 sc1\n"
"v_mfma_f32_16x16x32_bf16 v[96:99], v[32:35], v[160:163], v[96:99]\n"
"v_mul_f32_e32 v185, v185, v255\n"
"v_mul_f32_e32 v186, v186, v255\n"
"v_mfma_f32_16x16x32_bf16 v[96:99], v[36:39], v[156:159], v[96:99]\n"
"v_mul_f32_e32 v187, v187, v255\n"
"v_cvt_pk_bf16_f32 v28, v128, v129\n"
"v_mfma_f32_16x16x32_bf16 v[96:99], v[40:43], v[148:151], v[96:99]\n"
"buffer_load_dwordx4 v[32:35], v0, s[4:7], s97 offen offset:3072 sc1\n"
"v_mfma_f32_16x16x32_bf16 v[96:99], v[44:47], v[112:115], v[96:99]\n"
"s_waitcnt lgkmcnt(0)\n"
"v_cvt_pk_bf16_f32 v29, v130, v131\n"
"v_cvt_pk_bf16_f32 v30, v184, v185\n"
"v_mfma_f32_16x16x32_bf16 v[96:99], v[48:51], v[164:167], v[96:99]\n"
"s_add_u32 s96, s63, 0xc4000\n"
"ds_read_b128 v[36:39], v6 offset:4096\n"
"ds_read_b128 v[40:43], v6 offset:5120\n"
"v_mfma_f32_16x16x32_bf16 v[96:99], v[52:55], v[168:171], v[96:99]\n"
"buffer_load_dwordx4 v[44:47], v1, s[4:7], s97 offen sc1\n"
"v_mfma_f32_16x16x32_bf16 v[100:103], v[84:87], v[168:171], v[100:103]\n"
"buffer_store_dword v172, v14, s[8:11], s96 offen sc1\n"
"v_mfma_f32_16x16x32_bf16 v[100:103], v[80:83], v[164:167], v[100:103]\n"
"v_cvt_pk_bf16_f32 v31, v186, v187\n"
"v_add_f32_e32 v140, v140, v200\n"
"v_mfma_f32_16x16x32_bf16 v[100:103], v[76:79], v[112:115], v[100:103]\n"
"buffer_load_dwordx4 v[48:51], v1, s[4:7], s97 offen offset:1024 sc1\n"
"ds_write_b128 v7, v[28:31] offset:16384\n"
"v_mfma_f32_16x16x32_bf16 v[100:103], v[72:75], v[148:151], v[100:103]\n"
"v_add_f32_e32 v141, v141, v201\n"
"v_add_f32_e32 v142, v142, v202\n"
"v_mfma_f32_16x16x32_bf16 v[100:103], v[68:71], v[156:159], v[100:103]\n"
"v_add_f32_e32 v143, v143, v203\n"
"v_add_f32_e32 v208, v208, v204\n"
"v_mfma_f32_16x16x32_bf16 v[100:103], v[64:67], v[160:163], v[100:103]\n"
"buffer_load_dwordx4 v[28:31], v1, s[4:7], s97 offen offset:2048 sc1\n"
"v_mfma_f32_16x16x32_bf16 v[100:103], v[60:63], v[132:135], v[100:103]\n"
"v_add_f32_e32 v209, v209, v205\n"
"v_add_f32_e32 v210, v210, v206\n"
"v_mfma_f32_16x16x32_bf16 v[100:103], v[56:59], v[116:119], v[100:103]\n"
"buffer_load_dwordx4 a[0:3], v0, s[8:11], s98 offen sc1\n"
"buffer_load_dwordx4 v[56:59], v1, s[4:7], s97 offen offset:3072 sc1\n"
"v_add_f32_e32 v211, v211, v207\n"
"v_mul_f32_e32 v140, v140, v255\n"
"v_mul_f32_e32 v141, v141, v255\n"
"v_mul_f32_e32 v142, v142, v255\n"
"v_mul_f32_e32 v143, v143, v255\n"
"v_mul_f32_e32 v208, v208, v255\n"
"v_mul_f32_e32 v209, v209, v255\n"
"v_mul_f32_e32 v210, v210, v255\n"
"v_mul_f32_e32 v211, v211, v255\n"
"v_cvt_pk_bf16_f32 v52, v140, v141\n"
"v_cvt_pk_bf16_f32 v53, v142, v143\n"
"v_cvt_pk_bf16_f32 v54, v208, v209\n"
"v_cvt_pk_bf16_f32 v55, v210, v211\n"
"v_cndmask_b32_e64 v89, v89, v252, s[82:83]\n"
"v_cndmask_b32_e64 v90, v90, v252, s[82:83]\n"
"v_cndmask_b32_e64 v91, v91, v252, s[82:83]\n"
"v_cndmask_b32_e64 v176, v176, v252, s[82:83]\n"
"v_cndmask_b32_e64 v177, v177, v252, s[82:83]\n"
"v_cndmask_b32_e64 v178, v178, v252, s[82:83]\n"
"v_cndmask_b32_e64 v179, v179, v252, s[82:83]\n"
"ds_write_b128 v7, v[52:55] offset:49152\n"
"buffer_load_dwordx4 a[4:7], v0, s[8:11], s98 offen offset:1024 sc1\n"
"buffer_load_dwordx4 a[8:11], v0, s[8:11], s98 offen offset:2048 sc1\n"
"buffer_load_dwordx4 a[12:15], v0, s[8:11], s98 offen offset:3072 sc1\n"
"buffer_load_dwordx4 a[16:19], v1, s[8:11], s98 offen sc1\n"
"buffer_load_dwordx4 a[20:23], v1, s[8:11], s98 offen offset:1024 sc1\n"
".Lmk_b4:\n"
"; @B tail\n"
";\n"
"s_waitcnt lgkmcnt(2)\n"
"v_add_f32_e32 v96, v96, v36\n"
"v_add_f32_e32 v97, v97, v37\n"
"v_add_f32_e32 v98, v98, v38\n"
"v_add_f32_e32 v99, v99, v39\n"
"v_add_f32_e32 v100, v100, v40\n"
"v_add_f32_e32 v101, v101, v41\n"
"v_add_f32_e32 v102, v102, v42\n"
"v_add_f32_e32 v103, v103, v43\n"
"v_mul_f32_e32 v96, v96, v253\n"
"v_mul_f32_e32 v97, v97, v253\n"
"v_mul_f32_e32 v98, v98, v253\n"
"v_mul_f32_e32 v99, v99, v253\n"
"v_mul_f32_e32 v100, v100, v253\n"
"v_mul_f32_e32 v101, v101, v253\n"
"v_mul_f32_e32 v102, v102, v253\n"
"v_mul_f32_e32 v103, v103, v253\n"
"v_cvt_pk_bf16_f32 v36, v96, v97\n"
"v_cvt_pk_bf16_f32 v37, v98, v99\n"
"v_cvt_pk_bf16_f32 v38, v100, v101\n"
"v_cvt_pk_bf16_f32 v39, v102, v103\n"
"ds_write_b128 v2, v[36:39] offset:16384\n"
"s_waitcnt vmcnt(24)\n"
";\n"
"; @B AT a\n"
";\n"
"s_cmp_lg_u32 s0, 0\n"
"s_cbranch_scc1 .Lmk_exit\n"
"ds_read_b128 v[36:39], v2 offset:17408\n"
"ds_read_b128 v[40:43], v15 offset:16384\n"
"v_xor_b32_e32 v255, 32, v15\n"
"ds_read_b128 v[52:55], v255 offset:16640\n"
"ds_read_b128 v[60:63], v2 offset:16384\n"
"ds_read_b128 v[64:67], v17 offset:15360\n"
"v_xor_b32_e32 v255, 32, v17\n"
"ds_read_b128 v[68:71], v255 offset:15616\n"
"ds_read_b64_tr_b16 v[72:73], v16 offset:16384\n"
"v_xor_b32_e32 v255, 32, v16\n"
"ds_read_b64_tr_b16 v[74:75], v255 offset:16640\n"
"ds_read_b64_tr_b16 v[76:77], v255 offset:16384\n"
"ds_read_b64_tr_b16 v[78:79], v16 offset:16640\n"
"ds_read_b64_tr_b16 v[84:85], v16 offset:15360\n"
"v_mov_b32_e32 v80, 0x3f803f80\n"
"v_mov_b32_e32 v81, 0x3f803f80\n"
"v_mov_b32_e32 v82, 0x3f803f80\n"
"v_mov_b32_e32 v83, 0x3f803f80\n"
"v_xor_b32_e32 v255, 32, v16\n"
"ds_read_b64_tr_b16 v[86:87], v255 offset:15616\n"
"s_waitcnt lgkmcnt(13)\n"
"ds_read_b64_tr_b16 v[96:97], v255 offset:15360\n"
"ds_read_b64_tr_b16 v[98:99], v16 offset:15616\n"
"v_mov_b32_e32 v100, 0x3f803f80\n"
"v_mov_b32_e32 v101, 0x3f803f80\n"
"v_mov_b32_e32 v102, 0x3f803f80\n"
"v_mov_b32_e32 v103, 0x3f803f80\n"
"s_waitcnt lgkmcnt(11)\n"
"v_mfma_f32_16x16x32_bf16 v[104:107], v[40:43], v[36:39], v[104:107]\n"
"v_mfma_f32_16x16x32_bf16 v[124:127], v[52:55], v[36:39], v[124:127]\n"
"s_waitcnt lgkmcnt(8)\n"
"v_mfma_f32_16x16x32_bf16 v[88:91], v[64:67], v[60:63], v[88:91]\n"
"v_mfma_f32_16x16x32_bf16 v[176:179], v[68:71], v[60:63], v[176:179]\n"
"s_waitcnt lgkmcnt(4)\n"
"s_nop 2\n"
"v_max_f32_e32 v255, v104, v105\n"
"s_nop 1\n"
"v_max_f32_e32 v254, v88, v89\n"
"v_max3_f32 v255, v255, v106, v107\n"
"v_max3_f32 v254, v254, v90, v91\n"
"v_max3_f32 v255, v255, v124, v125\n"
"v_max3_f32 v254, v254, v176, v177\n"
"v_max3_f32 v255, v255, v126, v127\n"
"v_max3_f32 v254, v254, v178, v179\n"
"v_mov_b32_e32 v253, v255\n"
"v_mov_b32_e32 v252, v254\n"
"s_nop 0\n"
"v_permlane16_swap_b32_e32 v255, v253\n"
"v_permlane16_swap_b32_e32 v254, v252\n"
"v_max_f32_e32 v255, v255, v253\n"
"v_max_f32_e32 v254, v254, v252\n"
"v_mov_b32_e32 v253, v255\n"
"v_mov_b32_e32 v252, v254\n"
"s_nop 0\n"
"v_permlane32_swap_b32_e32 v255, v253\n"
"v_permlane32_swap_b32_e32 v254, v252\n"
"v_max_f32_e32 v255, v255, v253\n"
"v_max_f32_e32 v254, v254, v252\n"
"v_sub_f32_e32 v104, v104, v255\n"
"v_sub_f32_e32 v105, v105, v255\n"
"v_sub_f32_e32 v106, v106, v255\n"
"v_sub_f32_e32 v107, v107, v255\n"
"v_sub_f32_e32 v124, v124, v255\n"
"v_sub_f32_e32 v125, v125, v255\n"
"v_sub_f32_e32 v126, v126, v255\n"
"v_sub_f32_e32 v127, v127, v255\n"
"v_exp_f32_e32 v104, v104\n"
"v_exp_f32_e32 v105, v105\n"
"v_exp_f32_e32 v106, v106\n"
"v_exp_f32_e32 v107, v107\n"
"v_exp_f32_e32 v124, v124\n"
"v_exp_f32_e32 v125, v125\n"
"v_exp_f32_e32 v126, v126\n"
"v_exp_f32_e32 v127, v127\n"
"v_cvt_pk_bf16_f32 v36, v104, v105\n"
"v_cvt_pk_bf16_f32 v37, v106, v107\n"
"v_cvt_pk_bf16_f32 v38, v124, v125\n"
"v_cvt_pk_bf16_f32 v39, v126, v127\n"
"v_sub_f32_e32 v88, v88, v254\n"
"v_sub_f32_e32 v89, v89, v254\n"
"v_sub_f32_e32 v90, v90, v254\n"
"v_sub_f32_e32 v91, v91, v254\n"
"v_sub_f32_e32 v176, v176, v254\n"
"v_sub_f32_e32 v177, v177, v254\n"
"v_sub_f32_e32 v178, v178, v254\n"
"v_mfma_f32_16x16x32_bf16 v[40:43], v[80:83], v[36:39], 0\n"
"v_sub_f32_e32 v179, v179, v254\n"
"v_exp_f32_e32 v88, v88\n"
"v_exp_f32_e32 v89, v89\n"
"v_exp_f32_e32 v90, v90\n"
"v_mfma_f32_16x16x32_bf16 v[52:55], v[72:75], v[36:39], 0\n"
"v_exp_f32_e32 v91, v91\n"
"v_exp_f32_e32 v176, v176\n"
"v_exp_f32_e32 v177, v177\n"
"v_exp_f32_e32 v178, v178\n"
"v_mfma_f32_16x16x32_bf16 v[60:63], v[76:79], v[36:39], 0\n"
"s_waitcnt lgkmcnt(0)\n"
"v_exp_f32_e32 v179, v179\n"
"v_cvt_pk_bf16_f32 v36, v88, v89\n"
"v_cvt_pk_bf16_f32 v37, v90, v91\n"
"v_cvt_pk_bf16_f32 v38, v176, v177\n"
"v_cvt_pk_bf16_f32 v39, v178, v179\n"
"v_rcp_f32_e32 v40, v40\n"
"s_nop 0\n"
"v_mul_f32_e32 v52, v52, v40\n"
"v_mul_f32_e32 v53, v53, v40\n"
"v_mul_f32_e32 v54, v54, v40\n"
"v_mfma_f32_16x16x32_bf16 v[64:67], v[100:103], v[36:39], 0\n"
"v_mul_f32_e32 v55, v55, v40\n"
"v_mul_f32_e32 v60, v60, v40\n"
"v_mul_f32_e32 v61, v61, v40\n"
"v_mul_f32_e32 v62, v62, v40\n"
"v_mul_f32_e32 v63, v63, v40\n"
"v_mfma_f32_16x16x32_bf16 v[40:43], v[84:87], v[36:39], 0\n"
"s_add_u32 s97, s60, 0x408000\n"
"v_cvt_pk_bf16_f32 v68, v52, v53\n"
"v_cvt_pk_bf16_f32 v69, v54, v55\n"
"v_cvt_pk_bf16_f32 v70, v60, v61\n"
"v_cvt_pk_bf16_f32 v71, v62, v63\n"
"v_mfma_f32_16x16x32_bf16 v[52:55], v[96:99], v[36:39], 0\n"
"buffer_store_dwordx4 v[68:71], v0, s[4:7], s97 offen sc1\n"
"s_add_u32 s97, s60, 0x400000\n"
"v_rcp_f32_e32 v64, v64\n"
"s_nop 0\n"
"v_mul_f32_e32 v40, v40, v64\n"
"v_mul_f32_e32 v41, v41, v64\n"
"v_mul_f32_e32 v42, v42, v64\n"
"v_mul_f32_e32 v43, v43, v64\n"
"v_mul_f32_e32 v52, v52, v64\n"
"v_mul_f32_e32 v53, v53, v64\n"
"v_mul_f32_e32 v54, v54, v64\n"
"v_mul_f32_e32 v55, v55, v64\n"
"v_cvt_pk_bf16_f32 v36, v40, v41\n"
"v_cvt_pk_bf16_f32 v37, v42, v43\n"
"v_cvt_pk_bf16_f32 v38, v52, v53\n"
"v_cvt_pk_bf16_f32 v39, v54, v55\n"
"buffer_store_dwordx4 v[36:39], v0, s[4:7], s97 offen sc1\n"
";\n"
"s_add_u32 s97, s58, 0x440000\n"
"buffer_load_dwordx4 v[36:39], v0, s[4:7], s97 offen sc1\n"
"buffer_load_dwordx4 v[40:43], v0, s[4:7], s97 offen offset:1024 sc1\n"
"buffer_load_dwordx4 v[52:55], v0, s[4:7], s97 offen offset:2048 sc1\n"
"buffer_load_dwordx4 v[60:63], v0, s[4:7], s97 offen offset:3072 sc1\n"
"buffer_load_dwordx4 v[64:67], v1, s[4:7], s97 offen sc1\n"
"buffer_load_dwordx4 v[68:71], v1, s[4:7], s97 offen offset:1024 sc1\n"
"buffer_load_dwordx4 v[72:75], v1, s[4:7], s97 offen offset:2048 sc1\n"
"buffer_load_dwordx4 v[76:79], v1, s[4:7], s97 offen offset:3072 sc1\n"
"buffer_load_dwordx4 a[24:27], v1, s[8:11], s98 offen offset:2048 sc1\n"
"buffer_load_dwordx4 a[28:31], v1, s[8:11], s98 offen offset:3072 sc1\n"
"s_add_u32 s98, s69, 0x2000\n"
"buffer_load_dwordx4 a[32:35], v0, s[8:11], s98 offen sc1\n"
"buffer_load_dwordx4 a[36:39], v0, s[8:11], s98 offen offset:1024 sc1\n"
"buffer_load_dwordx4 a[40:43], v0, s[8:11], s98 offen offset:2048 sc1\n"
"buffer_load_dwordx4 a[44:47], v0, s[8:11], s98 offen offset:3072 sc1\n"
"buffer_load_dwordx4 a[48:51], v1, s[8:11], s98 offen sc1\n"
"buffer_load_dwordx4 a[52:55], v1, s[8:11], s98 offen offset:1024 sc1\n"
"s_waitcnt vmcnt(39)\n"
"; @B G2 b.0\n"
"v_mfma_f32_16x16x32_bf16 v[80:83], a[128:131], v[120:123], 0\n"
"v_mfma_f32_16x16x32_bf16 v[80:83], a[132:135], v[136:139], v[80:83]\n"
"s_waitcnt vmcnt(37)\n"
"v_mfma_f32_16x16x32_bf16 v[80:83], a[136:139], v[144:147], v[80:83]\n"
"s_add_u32 s97, s59, 0x440000\n"
"buffer_load_dwordx4 v[84:87], v0, s[4:7], s97 offen sc1\n"
"v_mfma_f32_16x16x32_bf16 v[80:83], a[140:143], v[192:195], v[80:83]\n"
"s_waitcnt vmcnt(36)\n"
"v_mfma_f32_16x16x32_bf16 v[80:83], a[144:147], v[196:199], v[80:83]\n"
"v_mfma_f32_16x16x32_bf16 v[80:83], a[148:151], v[188:191], v[80:83]\n"
"s_waitcnt vmcnt(34)\n"
"buffer_load_dwordx4 v[88:91], v0, s[4:7], s97 offen offset:1024 sc1\n"
"v_mfma_f32_16x16x32_bf16 v[80:83], a[152:155], v[108:111], v[80:83]\n"
"v_mfma_f32_16x16x32_bf16 v[80:83], a[156:159], v[92:95], v[80:83]\n"
"v_mfma_f32_16x16x32_bf16 v[96:99], a[188:191], v[92:95], 0\n"
"buffer_load_dwordx4 v[100:103], v0, s[4:7], s97 offen offset:2048 sc1\n"
"v_mfma_f32_16x16x32_bf16 v[96:99], a[184:187], v[108:111], v[96:99]\n"
"v_mfma_f32_16x16x32_bf16 v[96:99], a[180:183], v[188:191], v[96:99]\n"
"v_mfma_f32_16x16x32_bf16 v[96:99], a[176:179], v[196:199], v[96:99]\n"
"buffer_load_dwordx4 v[104:107], v0, s[4:7], s97 offen offset:3072 sc1\n"
"v_mfma_f32_16x16x32_bf16 v[96:99], a[172:175], v[192:195], v[96:99]\n"
"s_nop 0\n"
"v_cmp_u_f32_e32 vcc, v80, v80\n"
"v_mfma_f32_16x16x32_bf16 v[96:99], a[168:171], v[144:147], v[96:99]\n"
"s_cbranch_vccnz .Lmk_p68s\n"
".Lmk_p68r:\n"
"v_mfma_f32_16x16x32_bf16 v[96:99], a[164:167], v[136:139], v[96:99]\n"
"ds_write_b128 v3, v[80:83]\n"
"v_mfma_f32_16x16x32_bf16 v[96:99], a[160:163], v[120:123], v[96:99]\n"
"buffer_load_dwordx4 v[80:83], v1, s[4:7], s97 offen sc1\n"
"v_mfma_f32_16x16x32_bf16 v[112:115], a[192:195], v[120:123], 0\n"
"v_mfma_f32_16x16x32_bf16 v[112:115], a[196:199], v[136:139], v[112:115]\n"
"v_mfma_f32_16x16x32_bf16 v[112:115], a[200:203], v[144:147], v[112:115]\n"
"buffer_load_dwordx4 v[116:119], v1, s[4:7], s97 offen offset:1024 sc1\n"
"v_mfma_f32_16x16x32_bf16 v[112:115], a[204:207], v[192:195], v[112:115]\n"
"v_mfma_f32_16x16x32_bf16 v[112:115], a[208:211], v[196:199], v[112:115]\n"
"v_mfma_f32_16x16x32_bf16 v[112:115], a[212:215], v[188:191], v[112:115]\n"
"ds_write_b128 v3, v[96:99] offset:1024\n"
"v_mfma_f32_16x16x32_bf16 v[112:115], a[216:219], v[108:111], v[112:115]\n"
"buffer_load_dwordx4 v[96:99], v1, s[4:7], s97 offen offset:2048 sc1\n"
"v_mfma_f32_16x16x32_bf16 v[112:115], a[220:223], v[92:95], v[112:115]\n"
"v_mfma_f32_16x16x32_bf16 v[124:127], a[252:255], v[92:95], 0\n"
"v_mfma_f32_16x16x32_bf16 v[124:127], a[248:251], v[108:111], v[124:127]\n"
"buffer_load_dwordx4 v[92:95], v1, s[4:7], s97 offen offset:3072 sc1\n"
"v_mfma_f32_16x16x32_bf16 v[124:127], a[244:247], v[188:191], v[124:127]\n"
"v_mfma_f32_16x16x32_bf16 v[124:127], a[240:243], v[196:199], v[124:127]\n"
"v_mfma_f32_16x16x32_bf16 v[124:127], a[236:239], v[192:195], v[124:127]\n"
"s_mov_b32 s97, .Lmk_a0-.Lmk_g72\n"
"s_getpc_b64 s[84:85]\n"
".Lmk_g72:\n"
"s_add_u32 s84, s84, s97\n"
"s_addc_u32 s85, s85, 0\n"
"s_and_b32 s84, s84, -16\n"
"v_mfma_f32_16x16x32_bf16 v[124:127], a[232:235], v[144:147], v[124:127]\n"
"ds_write_b128 v3, v[112:115] offset:2048\n"
"v_mfma_f32_16x16x32_bf16 v[124:127], a[228:231], v[136:139], v[124:127]\n"
"s_and_b32 s85, s85, 0xffff\n"
"s_sub_u32 s97, .Lmk_end-.Lmk_g72, s97\n"
"s_min_u32 s86, s97, 0x3000\n"
"s_mov_b32 s87, 0x20000\n"
"v_mfma_f32_16x16x32_bf16 v[124:127], a[224:227], v[120:123], v[124:127]\n"
"s_waitcnt vmcnt(37)\n"
"s_mov_b32 m0, 0x27800\n"
"s_nop 0\n"
"buffer_load_dwordx4 v0, s[84:87], s99 offen lds\n"
"; @B G2 b.1\n"
"v_mfma_f32_16x16x32_bf16 v[108:111], a[128:131], v[152:155], 0\n"
"v_mfma_f32_16x16x32_bf16 v[108:111], a[132:135], v[180:183], v[108:111]\n"
"v_mfma_f32_16x16x32_bf16 v[108:111], a[136:139], v[24:27], v[108:111]\n"
"v_mfma_f32_16x16x32_bf16 v[108:111], a[140:143], v[32:35], v[108:111]\n"
"s_waitcnt vmcnt(32)\n"
"v_mfma_f32_16x16x32_bf16 v[108:111], a[144:147], v[44:47], v[108:111]\n"
"v_mfma_f32_16x16x32_bf16 v[108:111], a[148:151], v[48:51], v[108:111]\n"
"ds_write_b128 v3, v[124:127] offset:3072\n"
"v_mfma_f32_16x16x32_bf16 v[108:111], a[152:155], v[28:31], v[108:111]\n"
"v_mfma_f32_16x16x32_bf16 v[108:111], a[156:159], v[56:59], v[108:111]\n"
"v_mfma_f32_16x16x32_bf16 v[112:115], a[188:191], v[56:59], 0\n"
"s_add_u32 s97, s56, 0x400000\n"
"buffer_load_dwordx4 v[120:123], v0, s[4:7], s97 offen sc1\n"
"v_mfma_f32_16x16x32_bf16 v[112:115], a[184:187], v[28:31], v[112:115]\n"
"v_mfma_f32_16x16x32_bf16 v[112:115], a[180:183], v[48:51], v[112:115]\n"
"v_mfma_f32_16x16x32_bf16 v[112:115], a[176:179], v[44:47], v[112:115]\n"
"buffer_load_dwordx4 v[124:127], v0, s[4:7], s97 offen offset:1024 sc1\n"
"v_mfma_f32_16x16x32_bf16 v[112:115], a[172:175], v[32:35], v[112:115]\n"
"v_cmp_u_f32_e32 vcc, v108, v108\n"
"v_mfma_f32_16x16x32_bf16 v[112:115], a[168:171], v[24:27], v[112:115]\n"
"s_cbranch_vccnz .Lmk_p69s\n"
".Lmk_p69r:\n"
"v_mfma_f32_16x16x32_bf16 v[112:115], a[164:167], v[180:183], v[112:115]\n"
"ds_write_b128 v3, v[108:111] offset:6144\n"
"v_mfma_f32_16x16x32_bf16 v[112:115], a[160:163], v[152:155], v[112:115]\n"
"buffer_load_dwordx4 v[108:111], v0, s[4:7], s97 offen offset:2048 sc1\n"
"v_mfma_f32_16x16x32_bf16 v[128:131], a[192:195], v[152:155], 0\n"
"v_mfma_f32_16x16x32_bf16 v[128:131], a[196:199], v[180:183], v[128:131]\n"
"v_mfma_f32_16x16x32_bf16 v[128:131], a[200:203], v[24:27], v[128:131]\n"
"buffer_load_dwordx4 v[132:135], v0, s[4:7], s97 offen offset:3072 sc1\n"
"v_mfma_f32_16x16x32_bf16 v[128:131], a[204:207], v[32:35], v[128:131]\n"
"v_mfma_f32_16x16x32_bf16 v[128:131], a[208:211], v[44:47], v[128:131]\n"
"ds_read_b128 v[136:139], v2 offset:8192\n"
"ds_read_b128 v[140:143], v2 offset:9216\n"
"v_mfma_f32_16x16x32_bf16 v[128:131], a[212:215], v[48:51], v[128:131]\n"
"ds_write_b128 v3, v[112:115] offset:7168\n"
"v_mfma_f32_16x16x32_bf16 v[128:131], a[216:219], v[28:31], v[128:131]\n"
"buffer_load_dwordx4 v[112:115], v1, s[4:7], s97 offen sc1\n"
"v_mfma_f32_16x16x32_bf16 v[128:131], a[220:223], v[56:59], v[128:131]\n"
"v_mfma_f32_16x16x32_bf16 v[144:147], a[252:255], v[56:59], 0\n"
"v_mfma_f32_16x16x32_bf16 v[144:147], a[248:251], v[28:31], v[144:147]\n"
"buffer_load_dwordx4 v[28:31], v1, s[4:7], s97 offen offset:1024 sc1\n"
"s_waitcnt lgkmcnt(1)\n"
"v_mfma_f32_16x16x32_bf16 v[144:147], a[244:247], v[48:51], v[144:147]\n"
"v_lshlrev_b32_e32 v255, 16, v136\n"
"v_and_b32_e32 v254, 0xffff0000, v136\n"
"v_lshlrev_b32_e32 v253, 16, v137\n"
"v_mfma_f32_16x16x32_bf16 v[144:147], a[240:243], v[44:47], v[144:147]\n"
"v_and_b32_e32 v252, 0xffff0000, v137\n"
"v_lshlrev_b32_e32 v251, 16, v138\n"
"v_and_b32_e32 v250, 0xffff0000, v138\n"
"v_mfma_f32_16x16x32_bf16 v[144:147], a[236:239], v[32:35], v[144:147]\n"
"buffer_load_dwordx4 v[32:35], v1, s[4:7], s97 offen offset:2048 sc1\n"
"v_lshlrev_b32_e32 v249, 16, v139\n"
"v_mfma_f32_16x16x32_bf16 v[144:147], a[232:235], v[24:27], v[144:147]\n"
"ds_write_b128 v3, v[128:131] offset:8192\n"
"v_mfma_f32_16x16x32_bf16 v[144:147], a[228:231], v[180:183], v[144:147]\n"
"v_and_b32_e32 v248, 0xffff0000, v139\n"
"v_lshlrev_b32_e32 v247, 16, v140\n"
"v_and_b32_e32 v246, 0xffff0000, v140\n"
"v_mfma_f32_16x16x32_bf16 v[144:147], a[224:227], v[152:155], v[144:147]\n"
"s_waitcnt vmcnt(28)\n"
"buffer_load_dwordx4 v[24:27], v1, s[4:7], s97 offen offset:3072 sc1\n"
"; @B G2 b.2\n"
"v_mfma_f32_16x16x32_bf16 v[44:47], a[128:131], v[36:39], 0\n"
"s_add_u32 s97, s57, 0x400000\n"
"v_lshlrev_b32_e32 v245, 16, v141\n"
"v_and_b32_e32 v244, 0xffff0000, v141\n"
"v_lshlrev_b32_e32 v243, 16, v142\n"
"v_mfma_f32_16x16x32_bf16 v[44:47], a[132:135], v[40:43], v[44:47]\n"
"v_and_b32_e32 v242, 0xffff0000, v142\n"
"v_lshlrev_b32_e32 v241, 16, v143\n"
"v_and_b32_e32 v240, 0xffff0000, v143\n"
"v_mfma_f32_16x16x32_bf16 v[44:47], a[136:139], v[52:55], v[44:47]\n"
"buffer_load_dwordx4 v[48:51], v0, s[4:7], s97 offen sc1\n"
"v_mfma_f32_16x16x32_bf16 v[44:47], a[140:143], v[60:63], v[44:47]\n"
"s_waitcnt vmcnt(26)\n"
"v_mfma_f32_16x16x32_bf16 v[44:47], a[144:147], v[64:67], v[44:47]\n"
"v_mfma_f32_16x16x32_bf16 v[44:47], a[148:151], v[68:71], v[44:47]\n"
"ds_write_b128 v3, v[144:147] offset:9216\n"
"v_mfma_f32_16x16x32_bf16 v[44:47], a[152:155], v[72:75], v[44:47]\n"
"buffer_load_dwordx4 v[56:59], v0, s[4:7], s97 offen offset:1024 sc1\n"
"v_mfma_f32_16x16x32_bf16 v[44:47], a[156:159], v[76:79], v[44:47]\n"
"v_mfma_f32_16x16x32_bf16 v[128:131], a[188:191], v[76:79], 0\n"
"v_mfma_f32_16x16x32_bf16 v[128:131], a[184:187], v[72:75], v[128:131]\n"
"s_waitcnt lgkmcnt(0)\n"
"s_barrier\n"
"v_mfma_f32_16x16x32_bf16 v[128:131], a[180:183], v[68:71], v[128:131]\n"
"buffer_load_dwordx4 v[136:139], v0, s[4:7], s97 offen offset:2048 sc1\n"
"v_mfma_f32_16x16x32_bf16 v[128:131], a[176:179], v[64:67], v[128:131]\n"
"ds_read_b128 v[140:143], v4\n"
"ds_read_b128 v[144:147], v5\n"
"v_mfma_f32_16x16x32_bf16 v[128:131], a[172:175], v[60:63], v[128:131]\n"
"v_cmp_u_f32_e32 vcc, v44, v44\n"
"v_mfma_f32_16x16x32_bf16 v[128:131], a[168:171], v[52:55], v[128:131]\n"
"s_cbranch_vccnz .Lmk_p70s\n"
".Lmk_p70r:\n"
"v_mfma_f32_16x16x32_bf16 v[128:131], a[164:167], v[40:43], v[128:131]\n"
"ds_write_b128 v3, v[44:47] offset:12288\n"
"v_mfma_f32_16x16x32_bf16 v[128:131], a[160:163], v[36:39], v[128:131]\n"
"buffer_load_dwordx4 v[44:47], v0, s[4:7], s97 offen offset:3072 sc1\n"
"v_mfma_f32_16x16x32_bf16 v[148:151], a[192:195], v[36:39], 0\n"
"ds_read_b128 v[152:155], v4 offset:1024\n"
"ds_read_b128 v[156:159], v5 offset:1024\n"
"s_waitcnt lgkmcnt(3)\n"
"v_mfma_f32_16x16x32_bf16 v[148:151], a[196:199], v[40:43], v[148:151]\n"
"v_add_f32_e32 v140, v140, v255\n"
"v_add_f32_e32 v141, v141, v254\n"
"v_add_f32_e32 v142, v142, v253\n"
"v_mfma_f32_16x16x32_bf16 v[148:151], a[200:203], v[52:55], v[148:151]\n"
"buffer_load_dwordx4 v[160:163], v1, s[4:7], s97 offen sc1\n"
"v_add_f32_e32 v143, v143, v252\n"
"v_mfma_f32_16x16x32_bf16 v[148:151], a[204:207], v[60:63], v[148:151]\n"
"v_add_f32_e32 v140, v140, v144\n"
"v_add_f32_e32 v141, v141, v145\n"
"v_add_f32_e32 v142, v142, v146\n"
"v_mfma_f32_16x16x32_bf16 v[148:151], a[208:211], v[64:67], v[148:151]\n"
"v_add_f32_e32 v143, v143, v147\n"
"v_mfma_f32_16x16x32_bf16 v[148:151], a[212:215], v[68:71], v[148:151]\n"
"ds_write_b128 v3, v[128:131] offset:13312\n"
"v_mfma_f32_16x16x32_bf16 v[148:151], a[216:219], v[72:75], v[148:151]\n"
"buffer_load_dwordx4 v[128:131], v1, s[4:7], s97 offen offset:1024 sc1\n"
"s_waitcnt lgkmcnt(1)\n"
"v_mfma_f32_16x16x32_bf16 v[148:151], a[220:223], v[76:79], v[148:151]\n"
"ds_read_b128 v[144:147], v4 offset:2048\n"
"ds_read_b128 v[164:167], v5 offset:2048\n"
"v_add_f32_e32 v152, v152, v251\n"
"v_mfma_f32_16x16x32_bf16 v[168:171], a[252:255], v[76:79], 0\n"
"v_add_f32_e32 v153, v153, v250\n"
"v_add_f32_e32 v154, v154, v249\n"
"v_add_f32_e32 v155, v155, v248\n"
"v_mfma_f32_16x16x32_bf16 v[168:171], a[248:251], v[72:75], v[168:171]\n"
"buffer_load_dwordx4 v[72:75], v1, s[4:7], s97 offen offset:2048 sc1\n"
"v_add_f32_e32 v152, v152, v156\n"
"v_mfma_f32_16x16x32_bf16 v[168:171], a[244:247], v[68:71], v[168:171]\n"
"v_add_f32_e32 v153, v153, v157\n"
"v_add_f32_e32 v154, v154, v158\n"
"v_add_f32_e32 v155, v155, v159\n"
"v_mfma_f32_16x16x32_bf16 v[168:171], a[240:243], v[64:67], v[168:171]\n"
"v_mfma_f32_16x16x32_bf16 v[168:171], a[236:239], v[60:63], v[168:171]\n"
"buffer_load_dwordx4 v[60:63], v1, s[4:7], s97 offen offset:3072 sc1\n"
"s_waitcnt lgkmcnt(0)\n"
"v_mfma_f32_16x16x32_bf16 v[168:171], a[232:235], v[52:55], v[168:171]\n"
"ds_write_b128 v3, v[148:151] offset:14336\n"
"v_mfma_f32_16x16x32_bf16 v[168:171], a[228:231], v[40:43], v[168:171]\n"
"s_add_u32 s97, s58, 0x400000\n"
"ds_read_b128 v[40:43], v4 offset:3072\n"
"ds_read_b128 v[52:55], v5 offset:3072\n"
"v_add_f32_e32 v144, v144, v247\n"
"v_mfma_f32_16x16x32_bf16 v[168:171], a[224:227], v[36:39], v[168:171]\n"
"s_waitcnt vmcnt(21)\n"
"buffer_load_dwordx4 v[36:39], v0, s[4:7], s97 offen sc1\n"
"; @B G2 b.3\n"
"v_mfma_f32_16x16x32_bf16 v[64:67], a[128:131], v[84:87], v[140:143]\n"
"v_add_f32_e32 v145, v145, v246\n"
"v_add_f32_e32 v146, v146, v245\n"
"v_add_f32_e32 v147, v147, v244\n"
"v_mfma_f32_16x16x32_bf16 v[64:67], a[132:135], v[88:91], v[64:67]\n"
"v_add_f32_e32 v144, v144, v164\n"
"v_add_f32_e32 v145, v145, v165\n"
"v_add_f32_e32 v146, v146, v166\n"
"v_mfma_f32_16x16x32_bf16 v[64:67], a[136:139], v[100:103], v[64:67]\n"
"buffer_load_dwordx4 v[68:71], v0, s[4:7], s97 offen offset:1024 sc1\n"
"v_add_f32_e32 v147, v147, v167\n"
"v_mfma_f32_16x16x32_bf16 v[64:67], a[140:143], v[104:107], v[64:67]\n"
"s_waitcnt vmcnt(19)\n"
"s_waitcnt lgkmcnt(0)\n"
"v_add_f32_e32 v40, v40, v243\n"
"v_add_f32_e32 v41, v41, v242\n"
"v_mfma_f32_16x16x32_bf16 v[64:67], a[144:147], v[80:83], v[64:67]\n"
"v_add_f32_e32 v42, v42, v241\n"
"v_add_f32_e32 v43, v43, v240\n"
"v_add_f32_e32 v40, v40, v52\n"
"v_mfma_f32_16x16x32_bf16 v[64:67], a[148:151], v[116:119], v[64:67]\n"
"ds_write_b128 v3, v[168:171] offset:15360\n"
"v_mfma_f32_16x16x32_bf16 v[64:67], a[152:155], v[96:99], v[64:67]\n"
"buffer_load_dwordx4 v[76:79], v0, s[4:7], s97 offen offset:2048 sc1\n"
"v_add_f32_e32 v41, v41, v53\n"
"v_mfma_f32_16x16x32_bf16 v[64:67], a[156:159], v[92:95], v[64:67]\n"
"v_add_f32_e32 v42, v42, v54\n"
"v_add_f32_e32 v43, v43, v55\n"
"v_mfma_f32_16x16x32_bf16 v[52:55], a[188:191], v[92:95], v[152:155]\n"
"v_mfma_f32_16x16x32_bf16 v[52:55], a[184:187], v[96:99], v[52:55]\n"
"s_waitcnt lgkmcnt(0)\n"
"s_barrier\n"
"v_mfma_f32_16x16x32_bf16 v[52:55], a[180:183], v[116:119], v[52:55]\n"
"buffer_load_dwordx4 v[148:151], v0, s[4:7], s97 offen offset:3072 sc1\n"
"v_mfma_f32_16x16x32_bf16 v[52:55], a[176:179], v[80:83], v[52:55]\n"
"ds_read_b128 v[156:159], v6\n"
"ds_read_b128 v[164:167], v6 offset:1024\n"
"v_mfma_f32_16x16x32_bf16 v[52:55], a[172:175], v[104:107], v[52:55]\n"
"ds_read_b128 v[168:171], v6 offset:2048\n"
"v_cmp_u_f32_e32 vcc, v64, v64\n"
"v_mfma_f32_16x16x32_bf16 v[52:55], a[168:171], v[100:103], v[52:55]\n"
"s_cbranch_vccnz .Lmk_p71s\n"
".Lmk_p71r:\n"
"ds_read_b128 v[140:143], v6 offset:3072\n"
"v_mfma_f32_16x16x32_bf16 v[52:55], a[164:167], v[88:91], v[52:55]\n"
"buffer_load_dwordx4 v[152:155], v1, s[4:7], s97 offen sc1\n"
"v_mfma_f32_16x16x32_bf16 v[52:55], a[160:163], v[84:87], v[52:55]\n"
"v_mfma_f32_16x16x32_bf16 v[144:147], a[192:195], v[84:87], v[144:147]\n"
"v_mfma_f32_16x16x32_bf16 v[144:147], a[196:199], v[88:91], v[144:147]\n"
"buffer_load_dwordx4 v[172:175], v1, s[4:7], s97 offen offset:1024 sc1\n"
"v_mfma_f32_16x16x32_bf16 v[144:147], a[200:203], v[100:103], v[144:147]\n"
"v_mfma_f32_16x16x32_bf16 v[144:147], a[204:207], v[104:107], v[144:147]\n"
"v_mfma_f32_16x16x32_bf16 v[144:147], a[208:211], v[80:83], v[144:147]\n"
"buffer_load_dwordx4 v[176:179], v1, s[4:7], s97 offen offset:2048 sc1\n"
"s_waitcnt lgkmcnt(0)\n"
"v_mfma_f32_16x16x32_bf16 v[144:147], a[212:215], v[116:119], v[144:147]\n"
"v_add_f32_e32 v156, v64, v156\n"
"v_add_f32_e32 v157, v65, v157\n"
"v_add_f32_e32 v158, v66, v158\n"
"v_mfma_f32_16x16x32_bf16 v[144:147], a[216:219], v[96:99], v[144:147]\n"
"v_add_f32_e32 v159, v67, v159\n"
"v_add_f32_e32 v164, v52, v164\n"
"v_add_f32_e32 v165, v53, v165\n"
"v_mfma_f32_16x16x32_bf16 v[144:147], a[220:223], v[92:95], v[144:147]\n"
"buffer_load_dwordx4 v[64:67], v1, s[4:7], s97 offen offset:3072 sc1\n"
"v_add_f32_e32 v166, v54, v166\n"
"v_mfma_f32_16x16x32_bf16 v[40:43], a[252:255], v[92:95], v[40:43]\n"
"v_add_f32_e32 v167, v55, v167\n"
"v_cvt_pk_bf16_f32 v52, v156, v157\n"
"v_cvt_pk_bf16_f32 v53, v158, v159\n"
"v_mfma_f32_16x16x32_bf16 v[40:43], a[248:251], v[96:99], v[40:43]\n"
"s_add_u32 s97, s61, 0x840000\n"
"v_cvt_pk_bf16_f32 v54, v164, v165\n"
"v_cvt_pk_bf16_f32 v55, v166, v167\n"
"v_mfma_f32_16x16x32_bf16 v[40:43], a[244:247], v[116:119], v[40:43]\n"
"buffer_store_dwordx4 v[52:55], v0, s[4:7], s97 offen sc1\n"
"v_mfma_f32_16x16x32_bf16 v[40:43], a[240:243], v[80:83], v[40:43]\n"
"ds_write_b128 v2, v[52:55] offset:8192\n"
"v_mfma_f32_16x16x32_bf16 v[40:43], a[236:239], v[104:107], v[40:43]\n"
"v_mfma_f32_16x16x32_bf16 v[40:43], a[232:235], v[100:103], v[40:43]\n"
"v_mfma_f32_16x16x32_bf16 v[40:43], a[228:231], v[88:91], v[40:43]\n"
"v_mfma_f32_16x16x32_bf16 v[40:43], a[224:227], v[84:87], v[40:43]\n"
"; @B fin b\n"
"s_add_u32 s97, s61, 0x840000\n"
"v_add_f32_e32 v168, v144, v168\n"
"v_add_f32_e32 v169, v145, v169\n"
"v_add_f32_e32 v170, v146, v170\n"
"v_add_f32_e32 v171, v147, v171\n"
"s_nop 2\n"
"v_add_f32_e32 v140, v40, v140\n"
"v_add_f32_e32 v141, v41, v141\n"
"v_add_f32_e32 v142, v42, v142\n"
"v_add_f32_e32 v143, v43, v143\n"
"v_cvt_pk_bf16_f32 v40, v168, v169\n"
"v_cvt_pk_bf16_f32 v41, v170, v171\n"
"v_cvt_pk_bf16_f32 v42, v140, v141\n"
"v_cvt_pk_bf16_f32 v43, v142, v143\n"
"buffer_store_dwordx4 v[40:43], v0, s[4:7], s97 offen offset:1024 sc1\n"
"s_add_u32 s97, s59, 0x400000\n"
"buffer_load_dwordx4 v[80:83], v0, s[4:7], s97 offen sc1\n"
"buffer_load_dwordx4 v[84:87], v0, s[4:7], s97 offen offset:1024 sc1\n"
"buffer_load_dwordx4 v[88:91], v0, s[4:7], s97 offen offset:2048 sc1\n"
"buffer_load_dwordx4 v[92:95], v0, s[4:7], s97 offen offset:3072 sc1\n"
"buffer_load_dwordx4 v[96:99], v1, s[4:7], s97 offen sc1\n"
"buffer_load_dwordx4 v[100:103], v1, s[4:7], s97 offen offset:1024 sc1\n"
"buffer_load_dwordx4 v[104:107], v1, s[4:7], s97 offen offset:2048 sc1\n"
"buffer_load_dwordx4 v[116:119], v1, s[4:7], s97 offen offset:3072 sc1\n"
"s_waitcnt vmcnt(32)\n"
"; @B G2 a.0\n"
"v_mfma_f32_16x16x32_bf16 v[140:143], a[128:131], v[120:123], 0\n"
"v_mfma_f32_16x16x32_bf16 v[140:143], a[132:135], v[124:127], v[140:143]\n"
"s_waitcnt vmcnt(30)\n"
"ds_write_b128 v2, v[40:43] offset:9216\n"
"v_mfma_f32_16x16x32_bf16 v[140:143], a[136:139], v[108:111], v[140:143]\n"
"v_mov_b32_e32 v144, 0x3f803f80\n"
"v_mov_b32_e32 v145, 0x3f803f80\n"
"v_mfma_f32_16x16x32_bf16 v[140:143], a[140:143], v[132:135], v[140:143]\n"
"s_waitcnt vmcnt(28)\n"
"v_mov_b32_e32 v146, 0x3f803f80\n"
"v_mov_b32_e32 v147, 0x3f803f80\n"
"v_mfma_f32_16x16x32_bf16 v[140:143], a[144:147], v[112:115], v[140:143]\n"
"s_nop 0\n"
"v_mfma_f32_16x16x32_bf16 v[156:159], v[144:147], v[52:55], 0\n"
"v_mfma_f32_16x16x32_bf16 v[156:159], v[144:147], v[40:43], v[156:159]\n"
"v_mfma_f32_16x16x32_bf16 v[144:147], v[52:55], v[52:55], 0\n"
"v_mfma_f32_16x16x32_bf16 v[144:147], v[40:43], v[40:43], v[144:147]\n"
"buffer_load_dwordx4 a[56:59], v1, s[8:11], s98 offen offset:2048 sc1\n"
"v_mfma_f32_16x16x32_bf16 v[140:143], a[148:151], v[28:31], v[140:143]\n"
"s_waitcnt vmcnt(27)\n"
"v_mfma_f32_16x16x32_bf16 v[140:143], a[152:155], v[32:35], v[140:143]\n"
"v_mfma_f32_16x16x32_bf16 v[140:143], a[156:159], v[24:27], v[140:143]\n"
"buffer_load_dwordx4 a[60:63], v1, s[8:11], s98 offen offset:3072 sc1\n"
"v_mfma_f32_16x16x32_bf16 v[40:43], a[188:191], v[24:27], 0\n"
"s_mov_b32 s96, 0xaaaaaaaa\n"
"s_mov_b32 s97, 0xaaaaaaaa\n"
"s_add_u32 s98, s69, 0x4000\n"
"v_mfma_f32_16x16x32_bf16 v[40:43], a[184:187], v[32:35], v[40:43]\n"
"v_mfma_f32_16x16x32_bf16 v[40:43], a[180:183], v[28:31], v[40:43]\n"
"buffer_load_dwordx4 a[64:67], v0, s[8:11], s98 offen sc1\n"
"v_mfma_f32_16x16x32_bf16 v[40:43], a[176:179], v[112:115], v[40:43]\n"
"v_cndmask_b32_e64 v144, v144, v145, s[96:97]\n"
"v_cndmask_b32_e64 v146, v146, v147, s[96:97]\n"
"v_mfma_f32_16x16x32_bf16 v[40:43], a[172:175], v[132:135], v[40:43]\n"
"v_cmp_u_f32_e32 vcc, v140, v140\n"
"v_mfma_f32_16x16x32_bf16 v[40:43], a[168:171], v[108:111], v[40:43]\n"
"s_cbranch_vccnz .Lmk_p73s\n"
".Lmk_p73r:\n"
"v_mfma_f32_16x16x32_bf16 v[40:43], a[164:167], v[124:127], v[40:43]\n"
"ds_write_b128 v3, v[140:143]\n"
"v_mfma_f32_16x16x32_bf16 v[40:43], a[160:163], v[120:123], v[40:43]\n"
"s_mov_b32 s96, 0xcccccccc\n"
"s_mov_b32 s97, 0xcccccccc\n"
"v_mfma_f32_16x16x32_bf16 v[52:55], a[192:195], v[120:123], 0\n"
"v_cndmask_b32_e64 v157, v144, v146, s[96:97]\n"
"v_mfma_f32_16x16x32_bf16 v[52:55], a[196:199], v[124:127], v[52:55]\n"
"s_mov_b32 s96, 0xf0000f\n"
"s_mov_b32 s97, 0xf0000f00\n"
"v_mfma_f32_16x16x32_bf16 v[52:55], a[200:203], v[108:111], v[52:55]\n"
"v_cndmask_b32_e64 v156, v156, v157, s[96:97]\n"
"s_add_u32 s97, s63, 0x84000\n"
"v_mfma_f32_16x16x32_bf16 v[52:55], a[204:207], v[132:135], v[52:55]\n"
"buffer_store_dword v156, v14, s[4:7], s97 offen sc1\n"
"v_mfma_f32_16x16x32_bf16 v[52:55], a[208:211], v[112:115], v[52:55]\n"
"buffer_load_dwordx4 a[68:71], v0, s[8:11], s98 offen offset:1024 sc1\n"
"v_mfma_f32_16x16x32_bf16 v[52:55], a[212:215], v[28:31], v[52:55]\n"
"ds_write_b128 v3, v[40:43] offset:1024\n"
"v_mfma_f32_16x16x32_bf16 v[52:55], a[216:219], v[32:35], v[52:55]\n"
"v_mfma_f32_16x16x32_bf16 v[52:55], a[220:223], v[24:27], v[52:55]\n"
"buffer_load_dwordx4 a[72:75], v0, s[8:11], s98 offen offset:2048 sc1\n"
"v_mfma_f32_16x16x32_bf16 v[40:43], a[252:255], v[24:27], 0\n"
"v_mfma_f32_16x16x32_bf16 v[40:43], a[248:251], v[32:35], v[40:43]\n"
"v_mfma_f32_16x16x32_bf16 v[40:43], a[244:247], v[28:31], v[40:43]\n"
"buffer_load_dwordx4 a[76:79], v0, s[8:11], s98 offen offset:3072 sc1\n"
"v_mfma_f32_16x16x32_bf16 v[40:43], a[240:243], v[112:115], v[40:43]\n"
"v_mfma_f32_16x16x32_bf16 v[40:43], a[236:239], v[132:135], v[40:43]\n"
"v_mfma_f32_16x16x32_bf16 v[40:43], a[232:235], v[108:111], v[40:43]\n"
"ds_write_b128 v3, v[52:55] offset:2048\n"
"v_mfma_f32_16x16x32_bf16 v[40:43], a[228:231], v[124:127], v[40:43]\n"
"buffer_load_dwordx4 a[80:83], v1, s[8:11], s98 offen sc1\n"
"v_mfma_f32_16x16x32_bf16 v[40:43], a[224:227], v[120:123], v[40:43]\n"
"s_waitcnt vmcnt(30)\n"
"; @B G2 a.1\n"
"v_mfma_f32_16x16x32_bf16 v[24:27], a[128:131], v[48:51], 0\n"
"v_mfma_f32_16x16x32_bf16 v[24:27], a[132:135], v[56:59], v[24:27]\n"
"buffer_load_dwordx4 a[84:87], v1, s[8:11], s98 offen offset:1024 sc1\n"
"v_mfma_f32_16x16x32_bf16 v[24:27], a[136:139], v[136:139], v[24:27]\n"
"v_mfma_f32_16x16x32_bf16 v[24:27], a[140:143], v[44:47], v[24:27]\n"
"s_waitcnt vmcnt(27)\n"
"v_mfma_f32_16x16x32_bf16 v[24:27], a[144:147], v[160:163], v[24:27]\n"
"buffer_load_dwordx4 a[88:91], v1, s[8:11], s98 offen offset:2048 sc1\n"
"v_mfma_f32_16x16x32_bf16 v[24:27], a[148:151], v[128:131], v[24:27]\n"
"ds_write_b128 v3, v[40:43] offset:3072\n"
"v_mfma_f32_16x16x32_bf16 v[24:27], a[152:155], v[72:75], v[24:27]\n"
"v_mfma_f32_16x16x32_bf16 v[24:27], a[156:159], v[60:63], v[24:27]\n"
"buffer_load_dwordx4 a[92:95], v1, s[8:11], s98 offen offset:3072 sc1\n"
"v_mfma_f32_16x16x32_bf16 v[28:31], a[188:191], v[60:63], 0\n"
"s_add_u32 s98, s69, 0x6000\n"
"v_mfma_f32_16x16x32_bf16 v[28:31], a[184:187], v[72:75], v[28:31]\n"
"v_mfma_f32_16x16x32_bf16 v[28:31], a[180:183], v[128:131], v[28:31]\n"
"buffer_load_dwordx4 a[96:99], v0, s[8:11], s98 offen sc1\n"
"v_mfma_f32_16x16x32_bf16 v[28:31], a[176:179], v[160:163], v[28:31]\n"
"v_mfma_f32_16x16x32_bf16 v[28:31], a[172:175], v[44:47], v[28:31]\n"
"v_cmp_u_f32_e32 vcc, v24, v24\n"
"v_mfma_f32_16x16x32_bf16 v[28:31], a[168:171], v[136:139], v[28:31]\n"
"s_cbranch_vccnz .Lmk_p74s\n"
".Lmk_p74r:\n"
"v_mfma_f32_16x16x32_bf16 v[28:31], a[164:167], v[56:59], v[28:31]\n"
"ds_write_b128 v3, v[24:27] offset:6144\n"
"v_mfma_f32_16x16x32_bf16 v[28:31], a[160:163], v[48:51], v[28:31]\n"
"buffer_load_dwordx4 a[100:103], v0, s[8:11], s98 offen offset:1024 sc1\n"
"v_mfma_f32_16x16x32_bf16 v[24:27], a[192:195], v[48:51], 0\n"
"v_mfma_f32_16x16x32_bf16 v[24:27], a[196:199], v[56:59], v[24:27]\n"
"v_mfma_f32_16x16x32_bf16 v[24:27], a[200:203], v[136:139], v[24:27]\n"
"buffer_load_dwordx4 a[104:107], v0, s[8:11], s98 offen offset:2048 sc1\n"
"v_mfma_f32_16x16x32_bf16 v[24:27], a[204:207], v[44:47], v[24:27]\n"
"v_mfma_f32_16x16x32_bf16 v[24:27], a[208:211], v[160:163], v[24:27]\n"
"ds_read_b128 v[32:35], v2\n"
"ds_read_b128 v[40:43], v2 offset:1024\n"
"v_mfma_f32_16x16x32_bf16 v[24:27], a[212:215], v[128:131], v[24:27]\n"
"ds_write_b128 v3, v[28:31] offset:7168\n"
"v_mfma_f32_16x16x32_bf16 v[24:27], a[216:219], v[72:75], v[24:27]\n"
"buffer_load_dwordx4 a[108:111], v0, s[8:11], s98 offen offset:3072 sc1\n"
"v_mfma_f32_16x16x32_bf16 v[24:27], a[220:223], v[60:63], v[24:27]\n"
"v_mfma_f32_16x16x32_bf16 v[28:31], a[252:255], v[60:63], 0\n"
"v_mfma_f32_16x16x32_bf16 v[28:31], a[248:251], v[72:75], v[28:31]\n"
"buffer_load_dwordx4 a[112:115], v1, s[8:11], s98 offen sc1\n"
"s_waitcnt lgkmcnt(1)\n"
"v_mfma_f32_16x16x32_bf16 v[28:31], a[244:247], v[128:131], v[28:31]\n"
"v_lshlrev_b32_e32 v255, 16, v32\n"
"v_and_b32_e32 v254, 0xffff0000, v32\n"
"v_lshlrev_b32_e32 v253, 16, v33\n"
"v_mfma_f32_16x16x32_bf16 v[28:31], a[240:243], v[160:163], v[28:31]\n"
"v_and_b32_e32 v252, 0xffff0000, v33\n"
"v_lshlrev_b32_e32 v251, 16, v34\n"
"v_and_b32_e32 v250, 0xffff0000, v34\n"
"v_mfma_f32_16x16x32_bf16 v[28:31], a[236:239], v[44:47], v[28:31]\n"
"buffer_load_dwordx4 a[116:119], v1, s[8:11], s98 offen offset:1024 sc1\n"
"v_lshlrev_b32_e32 v249, 16, v35\n"
"v_mfma_f32_16x16x32_bf16 v[28:31], a[232:235], v[136:139], v[28:31]\n"
"ds_write_b128 v3, v[24:27] offset:8192\n"
"v_mfma_f32_16x16x32_bf16 v[28:31], a[228:231], v[56:59], v[28:31]\n"
"v_and_b32_e32 v248, 0xffff0000, v35\n"
"v_lshlrev_b32_e32 v247, 16, v40\n"
"v_and_b32_e32 v246, 0xffff0000, v40\n"
"v_mfma_f32_16x16x32_bf16 v[28:31], a[224:227], v[48:51], v[28:31]\n"
"s_waitcnt vmcnt(31)\n"
"buffer_load_dwordx4 a[120:123], v1, s[8:11], s98 offen offset:2048 sc1\n"
"; @B G2 a.2\n"
"v_mfma_f32_16x16x32_bf16 v[24:27], a[128:131], v[36:39], 0\n"
"v_lshlrev_b32_e32 v245, 16, v41\n"
"v_and_b32_e32 v244, 0xffff0000, v41\n"
"v_lshlrev_b32_e32 v243, 16, v42\n"
"v_mfma_f32_16x16x32_bf16 v[24:27], a[132:135], v[68:71], v[24:27]\n"
"v_and_b32_e32 v242, 0xffff0000, v42\n"
"v_lshlrev_b32_e32 v241, 16, v43\n"
"v_and_b32_e32 v240, 0xffff0000, v43\n"
"v_mfma_f32_16x16x32_bf16 v[24:27], a[136:139], v[76:79], v[24:27]\n"
"buffer_load_dwordx4 a[124:127], v1, s[8:11], s98 offen offset:3072 sc1\n"
"v_mfma_f32_16x16x32_bf16 v[24:27], a[140:143], v[148:151], v[24:27]\n"
"s_waitcnt vmcnt(29)\n"
"s_add_u32 s98, s69, 0x8000\n"
"v_mfma_f32_16x16x32_bf16 v[24:27], a[144:147], v[152:155], v[24:27]\n"
"v_mfma_f32_16x16x32_bf16 v[24:27], a[148:151], v[172:175], v[24:27]\n"
"ds_write_b128 v3, v[28:31] offset:9216\n"
"v_mfma_f32_16x16x32_bf16 v[24:27], a[152:155], v[176:179], v[24:27]\n"
"buffer_load_dwordx4 v[28:31], v0, s[8:11], s98 offen sc1\n"
"v_mfma_f32_16x16x32_bf16 v[24:27], a[156:159], v[64:67], v[24:27]\n"
"v_mfma_f32_16x16x32_bf16 v[32:35], a[188:191], v[64:67], 0\n"
"v_mfma_f32_16x16x32_bf16 v[32:35], a[184:187], v[176:179], v[32:35]\n"
"s_waitcnt lgkmcnt(0)\n"
"s_barrier\n"
"v_mfma_f32_16x16x32_bf16 v[32:35], a[180:183], v[172:175], v[32:35]\n"
"buffer_load_dwordx4 v[40:43], v0, s[8:11], s98 offen offset:1024 sc1\n"
"v_mfma_f32_16x16x32_bf16 v[32:35], a[176:179], v[152:155], v[32:35]\n"
"ds_read_b128 v[44:47], v4\n"
"ds_read_b128 v[48:51], v5\n"
"v_mfma_f32_16x16x32_bf16 v[32:35], a[172:175], v[148:151], v[32:35]\n"
"v_cmp_u_f32_e32 vcc, v24, v24\n"
"v_mfma_f32_16x16x32_bf16 v[32:35], a[168:171], v[76:79], v[32:35]\n"
"s_cbranch_vccnz .Lmk_p75s\n"
".Lmk_p75r:\n"
"v_mfma_f32_16x16x32_bf16 v[32:35], a[164:167], v[68:71], v[32:35]\n"
"ds_write_b128 v3, v[24:27] offset:12288\n"
"v_mfma_f32_16x16x32_bf16 v[32:35], a[160:163], v[36:39], v[32:35]\n"
"buffer_load_dwordx4 v[24:27], v0, s[8:11], s98 offen offset:2048 sc1\n"
"v_mfma_f32_16x16x32_bf16 v[52:55], a[192:195], v[36:39], 0\n"
"ds_read_b128 v[56:59], v4 offset:1024\n"
"ds_read_b128 v[60:63], v5 offset:1024\n"
"s_waitcnt lgkmcnt(3)\n"
"v_mfma_f32_16x16x32_bf16 v[52:55], a[196:199], v[68:71], v[52:55]\n"
"v_add_f32_e32 v44, v44, v255\n"
"v_add_f32_e32 v45, v45, v254\n"
"v_add_f32_e32 v46, v46, v253\n"
"v_mfma_f32_16x16x32_bf16 v[52:55], a[200:203], v[76:79], v[52:55]\n"
"buffer_load_dwordx4 v[72:75], v0, s[8:11], s98 offen offset:3072 sc1\n"
"v_add_f32_e32 v47, v47, v252\n"
"v_mfma_f32_16x16x32_bf16 v[52:55], a[204:207], v[148:151], v[52:55]\n"
"v_add_f32_e32 v44, v44, v48\n"
"v_add_f32_e32 v45, v45, v49\n"
"v_add_f32_e32 v46, v46, v50\n"
"v_mfma_f32_16x16x32_bf16 v[52:55], a[208:211], v[152:155], v[52:55]\n"
"v_add_f32_e32 v47, v47, v51\n"
"v_mfma_f32_16x16x32_bf16 v[52:55], a[212:215], v[172:175], v[52:55]\n"
"ds_write_b128 v3, v[32:35] offset:13312\n"
"v_mfma_f32_16x16x32_bf16 v[52:55], a[216:219], v[176:179], v[52:55]\n"
"buffer_load_dwordx4 v[32:35], v1, s[8:11], s98 offen sc1\n"
"s_waitcnt lgkmcnt(1)\n"
"v_mfma_f32_16x16x32_bf16 v[52:55], a[220:223], v[64:67], v[52:55]\n"
"ds_read_b128 v[48:51], v4 offset:2048\n"
"ds_read_b128 v[108:111], v5 offset:2048\n"
"v_add_f32_e32 v56, v56, v251\n"
"v_mfma_f32_16x16x32_bf16 v[112:115], a[252:255], v[64:67], 0\n"
"v_add_f32_e32 v57, v57, v250\n"
"v_add_f32_e32 v58, v58, v249\n"
"v_add_f32_e32 v59, v59, v248\n"
"v_mfma_f32_16x16x32_bf16 v[112:115], a[248:251], v[176:179], v[112:115]\n"
"buffer_load_dwordx4 v[64:67], v1, s[8:11], s98 offen offset:1024 sc1\n"
"v_add_f32_e32 v56, v56, v60\n"
"v_mfma_f32_16x16x32_bf16 v[112:115], a[244:247], v[172:175], v[112:115]\n"
"v_add_f32_e32 v57, v57, v61\n"
"v_add_f32_e32 v58, v58, v62\n"
"v_add_f32_e32 v59, v59, v63\n"
"v_mfma_f32_16x16x32_bf16 v[112:115], a[240:243], v[152:155], v[112:115]\n"
"v_mfma_f32_16x16x32_bf16 v[112:115], a[236:239], v[148:151], v[112:115]\n"
"buffer_load_dwordx4 v[60:63], v1, s[8:11], s98 offen offset:2048 sc1\n"
"s_waitcnt lgkmcnt(0)\n"
"v_mfma_f32_16x16x32_bf16 v[112:115], a[232:235], v[76:79], v[112:115]\n"
"ds_write_b128 v3, v[52:55] offset:14336\n"
"v_mfma_f32_16x16x32_bf16 v[112:115], a[228:231], v[68:71], v[112:115]\n"
"ds_read_b128 v[52:55], v4 offset:3072\n"
"ds_read_b128 v[68:71], v5 offset:3072\n"
"v_add_f32_e32 v48, v48, v247\n"
"v_mfma_f32_16x16x32_bf16 v[112:115], a[224:227], v[36:39], v[112:115]\n"
"s_waitcnt vmcnt(30)\n"
"buffer_load_dwordx4 v[36:39], v1, s[8:11], s98 offen offset:3072 sc1\n"
"; @B G2 a.3\n"
"v_mfma_f32_16x16x32_bf16 v[76:79], a[128:131], v[80:83], v[44:47]\n"
"s_add_u32 s98, s69, 0xa000\n"
"v_add_f32_e32 v49, v49, v246\n"
"v_add_f32_e32 v50, v50, v245\n"
"v_add_f32_e32 v51, v51, v244\n"
"v_mfma_f32_16x16x32_bf16 v[76:79], a[132:135], v[84:87], v[76:79]\n"
"v_add_f32_e32 v48, v48, v108\n"
"v_add_f32_e32 v49, v49, v109\n"
"v_add_f32_e32 v50, v50, v110\n"
"v_mfma_f32_16x16x32_bf16 v[76:79], a[136:139], v[88:91], v[76:79]\n"
"buffer_load_dwordx4 v[120:123], v0, s[8:11], s98 offen sc1\n"
"v_add_f32_e32 v51, v51, v111\n"
"v_mfma_f32_16x16x32_bf16 v[76:79], a[140:143], v[92:95], v[76:79]\n"
"s_waitcnt vmcnt(28)\n"
"s_waitcnt lgkmcnt(0)\n"
"v_add_f32_e32 v52, v52, v243\n"
"v_add_f32_e32 v53, v53, v242\n"
"v_mfma_f32_16x16x32_bf16 v[76:79], a[144:147], v[96:99], v[76:79]\n"
"v_add_f32_e32 v54, v54, v241\n"
"v_add_f32_e32 v55, v55, v240\n"
"v_add_f32_e32 v52, v52, v68\n"
"v_mfma_f32_16x16x32_bf16 v[76:79], a[148:151], v[100:103], v[76:79]\n"
"ds_write_b128 v3, v[112:115] offset:15360\n"
"v_mfma_f32_16x16x32_bf16 v[76:79], a[152:155], v[104:107], v[76:79]\n"
"buffer_load_dwordx4 v[108:111], v0, s[8:11], s98 offen offset:1024 sc1\n"
"v_add_f32_e32 v53, v53, v69\n"
"v_mfma_f32_16x16x32_bf16 v[76:79], a[156:159], v[116:119], v[76:79]\n"
"v_add_f32_e32 v54, v54, v70\n"
"v_add_f32_e32 v55, v55, v71\n"
"v_mfma_f32_16x16x32_bf16 v[68:71], a[188:191], v[116:119], v[56:59]\n"
"v_mfma_f32_16x16x32_bf16 v[68:71], a[184:187], v[104:107], v[68:71]\n"
"s_waitcnt lgkmcnt(0)\n"
"s_barrier\n"
"v_mfma_f32_16x16x32_bf16 v[68:71], a[180:183], v[100:103], v[68:71]\n"
"buffer_load_dwordx4 v[112:115], v0, s[8:11], s98 offen offset:2048 sc1\n"
"v_mfma_f32_16x16x32_bf16 v[68:71], a[176:179], v[96:99], v[68:71]\n"
"ds_read_b128 v[124:127], v6\n"
"ds_read_b128 v[128:131], v6 offset:1024\n"
"v_mfma_f32_16x16x32_bf16 v[68:71], a[172:175], v[92:95], v[68:71]\n"
"ds_read_b128 v[132:135], v6 offset:2048\n"
"v_cmp_u_f32_e32 vcc, v76, v76\n"
"v_mfma_f32_16x16x32_bf16 v[68:71], a[168:171], v[88:91], v[68:71]\n"
"s_cbranch_vccnz .Lmk_p76s\n"
".Lmk_p76r:\n"
"ds_read_b128 v[44:47], v6 offset:3072\n"
"v_mfma_f32_16x16x32_bf16 v[68:71], a[164:167], v[84:87], v[68:71]\n"
"buffer_load_dwordx4 v[56:59], v0, s[8:11], s98 offen offset:3072 sc1\n"
"v_mfma_f32_16x16x32_bf16 v[68:71], a[160:163], v[80:83], v[68:71]\n"
"v_mfma_f32_16x16x32_bf16 v[48:51], a[192:195], v[80:83], v[48:51]\n"
"v_mfma_f32_16x16x32_bf16 v[48:51], a[196:199], v[84:87], v[48:51]\n"
"buffer_load_dwordx4 v[136:139], v1, s[8:11], s98 offen sc1\n"
"v_mfma_f32_16x16x32_bf16 v[48:51], a[200:203], v[88:91], v[48:51]\n"
"v_mfma_f32_16x16x32_bf16 v[48:51], a[204:207], v[92:95], v[48:51]\n"
"v_mfma_f32_16x16x32_bf16 v[48:51], a[208:211], v[96:99], v[48:51]\n"
"buffer_load_dwordx4 v[140:143], v1, s[8:11], s98 offen offset:1024 sc1\n"
"s_waitcnt lgkmcnt(0)\n"
"v_mfma_f32_16x16x32_bf16 v[48:51], a[212:215], v[100:103], v[48:51]\n"
"v_add_f32_e32 v124, v76, v124\n"
"v_add_f32_e32 v125, v77, v125\n"
"v_add_f32_e32 v126, v78, v126\n"
"v_mfma_f32_16x16x32_bf16 v[48:51], a[216:219], v[104:107], v[48:51]\n"
"v_add_f32_e32 v127, v79, v127\n"
"v_add_f32_e32 v128, v68, v128\n"
"v_add_f32_e32 v129, v69, v129\n"
"v_mfma_f32_16x16x32_bf16 v[48:51], a[220:223], v[116:119], v[48:51]\n"
"buffer_load_dwordx4 v[76:79], v1, s[8:11], s98 offen offset:2048 sc1\n"
"v_add_f32_e32 v130, v70, v130\n"
"v_mfma_f32_16x16x32_bf16 v[52:55], a[252:255], v[116:119], v[52:55]\n"
"v_add_f32_e32 v131, v71, v131\n"
"v_cvt_pk_bf16_f32 v68, v124, v125\n"
"v_cvt_pk_bf16_f32 v69, v126, v127\n"
"v_mfma_f32_16x16x32_bf16 v[52:55], a[248:251], v[104:107], v[52:55]\n"
"s_add_u32 s97, s61, 0x800000\n"
"v_cvt_pk_bf16_f32 v70, v128, v129\n"
"v_cvt_pk_bf16_f32 v71, v130, v131\n"
"v_mfma_f32_16x16x32_bf16 v[52:55], a[244:247], v[100:103], v[52:55]\n"
"buffer_load_dwordx4 v[100:103], v1, s[8:11], s98 offen offset:3072 sc1\n"
"v_mfma_f32_16x16x32_bf16 v[52:55], a[240:243], v[96:99], v[52:55]\n"
"s_add_u32 s98, s52, 0x840000\n"
"buffer_load_dwordx4 v[96:99], v0, s[8:11], s98 offen sc1\n"
"v_mfma_f32_16x16x32_bf16 v[52:55], a[236:239], v[92:95], v[52:55]\n"
"buffer_store_dwordx4 v[68:71], v0, s[4:7], s97 offen sc1\n"
"v_mfma_f32_16x16x32_bf16 v[52:55], a[232:235], v[88:91], v[52:55]\n"
"buffer_load_dwordx4 v[88:91], v0, s[8:11], s98 offen offset:1024 sc1\n"
"v_mfma_f32_16x16x32_bf16 v[52:55], a[228:231], v[84:87], v[52:55]\n"
"ds_write_b128 v2, v[68:71]\n"
"v_mfma_f32_16x16x32_bf16 v[52:55], a[224:227], v[80:83], v[52:55]\n"
"buffer_load_dwordx4 v[80:83], v0, s[8:11], s98 offen offset:2048 sc1\n"
"buffer_load_dwordx4 v[84:87], v0, s[8:11], s98 offen offset:3072 sc1\n"
"buffer_load_dwordx4 v[92:95], v1, s[8:11], s98 offen sc1\n"
"buffer_load_dwordx4 v[104:107], v1, s[8:11], s98 offen offset:1024 sc1\n"
"buffer_load_dwordx4 v[116:119], v1, s[8:11], s98 offen offset:2048 sc1\n"
"buffer_load_dwordx4 v[124:127], v1, s[8:11], s98 offen offset:3072 sc1\n"
"s_add_u32 s98, s53, 0x840000\n"
"buffer_load_dwordx4 v[128:131], v0, s[8:11], s98 offen sc1\n"
"buffer_load_dwordx4 v[144:147], v0, s[8:11], s98 offen offset:1024 sc1\n"
"buffer_load_dwordx4 v[148:151], v0, s[8:11], s98 offen offset:2048 sc1\n"
"buffer_load_dwordx4 v[152:155], v0, s[8:11], s98 offen offset:3072 sc1\n"
"buffer_load_dwordx4 v[156:159], v1, s[8:11], s98 offen sc1\n"
"buffer_load_dwordx4 v[160:163], v1, s[8:11], s98 offen offset:1024 sc1\n"
"buffer_load_dwordx4 v[164:167], v1, s[8:11], s98 offen offset:2048 sc1\n"
"buffer_load_dwordx4 v[168:171], v1, s[8:11], s98 offen offset:3072 sc1\n"
"s_lshr_b32 s98, s79, 7\n"
"s_add_u32 s52, s52, 0x800000\n"
"s_add_u32 s53, s53, 0x800000\n"
"s_add_u32 s54, s54, 0x800000\n"
"s_add_u32 s55, s55, 0x800000\n"
"s_add_u32 s56, s56, 0x800000\n"
"s_add_u32 s57, s57, 0x800000\n"
"s_add_u32 s58, s58, 0x800000\n"
"s_add_u32 s59, s59, 0x800000\n"
"s_add_u32 s60, s60, 0x800000\n"
"s_add_u32 s61, s61, 0x800000\n"
"s_add_u32 s62, s62, 0x80000\n"
"s_add_u32 s63, s63, 0x80000\n"
"s_add_u32 s64, s64, 0x1000000\n"
"s_add_u32 s65, s65, s79\n"
"s_add_u32 s66, s66, s79\n"
"s_add_u32 s67, s67, 0x20000\n"
"s_add_u32 s68, s68, 0x20000\n"
"s_add_u32 s71, s71, s98\n"
"s_add_u32 s73, s73, s98\n"
"s_mov_b32 s0, 1\n"
"s_mov_b32 s10, 0\n"
".Lmk_a0:\n"
"s_waitcnt vmcnt(12)\n"
"; @A G1 0.0\n"
"v_mfma_f32_16x16x32_bf16 v[172:175], a[0:3], v[96:99], 0\n"
"v_mfma_f32_16x16x32_bf16 v[172:175], a[4:7], v[88:91], v[172:175]\n"
"s_add_u32 s98, s62, 0x4000\n"
"v_mfma_f32_16x16x32_bf16 v[172:175], a[8:11], v[80:83], v[172:175]\n"
"s_add_u32 s97, s54, 0x40000\n"
"buffer_load_dwordx4 v[176:179], v0, s[4:7], s97 offen sc1\n"
"v_mfma_f32_16x16x32_bf16 v[172:175], a[12:15], v[84:87], v[172:175]\n"
"s_waitcnt vmcnt(9)\n"
"v_mfma_f32_16x16x32_bf16 v[172:175], a[16:19], v[92:95], v[172:175]\n"
"buffer_load_dword v255, v10, s[4:7], s98 offen sc1\n"
"v_mfma_f32_16x16x32_bf16 v[172:175], a[20:23], v[104:107], v[172:175]\n"
"v_add_f32_e32 v132, v48, v132\n"
"v_add_f32_e32 v133, v49, v133\n"
"v_mfma_f32_16x16x32_bf16 v[172:175], a[24:27], v[116:119], v[172:175]\n"
"v_add_f32_e32 v134, v50, v134\n"
"v_add_f32_e32 v135, v51, v135\n"
"v_mfma_f32_16x16x32_bf16 v[172:175], a[28:31], v[124:127], v[172:175]\n"
"buffer_load_dwordx4 v[48:51], v0, s[4:7], s97 offen offset:1024 sc1\n"
"v_mfma_f32_16x16x32_bf16 v[180:183], a[60:63], v[124:127], 0\n"
"buffer_load_dword v254, v10, s[4:7], s98 offen offset:256 sc1\n"
"v_mfma_f32_16x16x32_bf16 v[180:183], a[56:59], v[116:119], v[180:183]\n"
"v_add_f32_e32 v44, v52, v44\n"
"v_add_f32_e32 v45, v53, v45\n"
"v_mfma_f32_16x16x32_bf16 v[180:183], a[52:55], v[104:107], v[180:183]\n"
"v_add_f32_e32 v46, v54, v46\n"
"v_add_f32_e32 v47, v55, v47\n"
"v_mfma_f32_16x16x32_bf16 v[180:183], a[48:51], v[92:95], v[180:183]\n"
"buffer_load_dword v253, v10, s[4:7], s98 offen offset:512 sc1\n"
"v_mfma_f32_16x16x32_bf16 v[180:183], a[44:47], v[84:87], v[180:183]\n"
"v_cmp_u_f32_e32 vcc, v172, v172\n"
"v_cvt_pk_bf16_f32 v52, v132, v133\n"
"v_mfma_f32_16x16x32_bf16 v[180:183], a[40:43], v[80:83], v[180:183]\n"
"s_cbranch_vccnz .Lmk_p81s\n"
".Lmk_p81r:\n"
"v_cvt_pk_bf16_f32 v53, v134, v135\n"
"v_mfma_f32_16x16x32_bf16 v[180:183], a[36:39], v[88:91], v[180:183]\n"
"buffer_load_dwordx4 v[132:135], v0, s[4:7], s97 offen offset:2048 sc1\n"
"v_mfma_f32_16x16x32_bf16 v[180:183], a[32:35], v[96:99], v[180:183]\n"
"ds_write_b128 v3, v[172:175]\n"
"v_mfma_f32_16x16x32_bf16 v[172:175], a[64:67], v[96:99], 0\n"
"buffer_load_dword v252, v10, s[4:7], s98 offen offset:768 sc1\n"
"v_mfma_f32_16x16x32_bf16 v[172:175], a[68:71], v[88:91], v[172:175]\n"
"buffer_load_dwordx4 v[184:187], v0, s[4:7], s97 offen offset:3072 sc1\n"
"v_mfma_f32_16x16x32_bf16 v[172:175], a[72:75], v[80:83], v[172:175]\n"
"v_cvt_pk_bf16_f32 v54, v44, v45\n"
"v_cvt_pk_bf16_f32 v55, v46, v47\n"
"v_mfma_f32_16x16x32_bf16 v[172:175], a[76:79], v[84:87], v[172:175]\n"
"buffer_load_dword v251, v10, s[4:7], s98 offen offset:1024 sc1\n"
"v_mfma_f32_16x16x32_bf16 v[172:175], a[80:83], v[92:95], v[172:175]\n"
"buffer_store_dwordx4 v[52:55], v0, s[4:7], s61 offen offset:1024 sc1\n"
"v_mfma_f32_16x16x32_bf16 v[172:175], a[84:87], v[104:107], v[172:175]\n"
"ds_write_b128 v3, v[180:183] offset:1024\n"
"v_mfma_f32_16x16x32_bf16 v[172:175], a[88:91], v[116:119], v[172:175]\n"
"buffer_load_dwordx4 v[44:47], v1, s[4:7], s97 offen sc1\n"
"v_mfma_f32_16x16x32_bf16 v[172:175], a[92:95], v[124:127], v[172:175]\n"
"buffer_load_dword v250, v10, s[4:7], s98 offen offset:1280 sc1\n"
"v_mfma_f32_16x16x32_bf16 v[180:183], a[124:127], v[124:127], 0\n"
"ds_write_b128 v2, v[52:55] offset:1024\n"
"v_mfma_f32_16x16x32_bf16 v[180:183], a[120:123], v[116:119], v[180:183]\n"
"v_mov_b32_e32 v188, 0x3f803f80\n"
"v_mov_b32_e32 v189, 0x3f803f80\n"
"v_mfma_f32_16x16x32_bf16 v[180:183], a[116:119], v[104:107], v[180:183]\n"
"buffer_load_dword v249, v10, s[4:7], s98 offen offset:1536 sc1\n"
"v_mfma_f32_16x16x32_bf16 v[180:183], a[112:115], v[92:95], v[180:183]\n"
"buffer_load_dwordx4 v[192:195], v1, s[4:7], s97 offen offset:1024 sc1\n"
"v_mfma_f32_16x16x32_bf16 v[180:183], a[108:111], v[84:87], v[180:183]\n"
"v_mov_b32_e32 v190, 0x3f803f80\n"
"v_mov_b32_e32 v191, 0x3f803f80\n"
"v_mfma_f32_16x16x32_bf16 v[180:183], a[104:107], v[80:83], v[180:183]\n"
"ds_write_b128 v3, v[172:175] offset:2048\n"
"v_mfma_f32_16x16x32_bf16 v[172:175], v[188:191], v[68:71], 0\n"
"v_mfma_f32_16x16x32_bf16 v[172:175], v[188:191], v[52:55], v[172:175]\n"
"v_mfma_f32_16x16x32_bf16 v[188:191], v[68:71], v[68:71], 0\n"
"v_mfma_f32_16x16x32_bf16 v[188:191], v[52:55], v[52:55], v[188:191]\n"
"v_mfma_f32_16x16x32_bf16 v[180:183], a[100:103], v[88:91], v[180:183]\n"
"v_mov_b32_e32 v52, -1\n"
"v_mov_b32_e32 v53, -1\n"
"v_mfma_f32_16x16x32_bf16 v[180:183], a[96:99], v[96:99], v[180:183]\n"
"v_mov_b32_e32 v54, -1\n"
"v_mov_b32_e32 v55, -1\n"
"v_mfma_f32_16x16x32_bf16 v[68:71], v[28:31], v[96:99], 0\n"
"buffer_load_dwordx4 v[196:199], v1, s[4:7], s97 offen offset:2048 sc1\n"
"v_mfma_f32_16x16x32_bf16 v[68:71], v[40:43], v[88:91], v[68:71]\n"
"s_add_u32 s96, s64, 0x4000\n"
"buffer_store_dwordx4 v[52:55], v2, s[4:7], s96 offen sc1\n"
"v_mfma_f32_16x16x32_bf16 v[68:71], v[24:27], v[80:83], v[68:71]\n"
"buffer_load_dword v248, v10, s[4:7], s98 offen offset:1792 sc1\n"
"v_mfma_f32_16x16x32_bf16 v[68:71], v[72:75], v[84:87], v[68:71]\n"
"s_mov_b32 s94, 0xaaaaaaaa\n"
"s_mov_b32 s95, 0xaaaaaaaa\n"
"v_mfma_f32_16x16x32_bf16 v[68:71], v[32:35], v[92:95], v[68:71]\n"
"v_mfma_f32_16x16x32_bf16 v[68:71], v[64:67], v[104:107], v[68:71]\n"
"buffer_load_dwordx4 v[200:203], v1, s[4:7], s97 offen offset:3072 sc1\n"
"v_mfma_f32_16x16x32_bf16 v[68:71], v[60:63], v[116:119], v[68:71]\n"
"ds_write_b128 v3, v[180:183] offset:3072\n"
"v_mfma_f32_16x16x32_bf16 v[68:71], v[36:39], v[124:127], v[68:71]\n"
"v_cndmask_b32_e64 v188, v188, v189, s[94:95]\n"
"v_cndmask_b32_e64 v190, v190, v191, s[94:95]\n"
"v_mfma_f32_16x16x32_bf16 v[180:183], v[100:103], v[124:127], 0\n"
"s_mov_b32 s94, 0xcccccccc\n"
"s_mov_b32 s95, 0xcccccccc\n"
"v_mfma_f32_16x16x32_bf16 v[180:183], v[76:79], v[116:119], v[180:183]\n"
"v_cndmask_b32_e64 v173, v188, v190, s[94:95]\n"
"v_mfma_f32_16x16x32_bf16 v[180:183], v[140:143], v[104:107], v[180:183]\n"
"s_mov_b32 s94, 0xf0000f\n"
"s_mov_b32 s95, 0xf0000f00\n"
"v_mfma_f32_16x16x32_bf16 v[180:183], v[136:139], v[92:95], v[180:183]\n"
"v_cndmask_b32_e64 v172, v172, v173, s[94:95]\n"
"v_mfma_f32_16x16x32_bf16 v[180:183], v[56:59], v[84:87], v[180:183]\n"
"buffer_store_dword v172, v14, s[4:7], s63 offen sc1\n"
"v_mfma_f32_16x16x32_bf16 v[180:183], v[112:115], v[80:83], v[180:183]\n"
"ds_write_b128 v3, v[68:71] offset:4096\n"
"v_mfma_f32_16x16x32_bf16 v[180:183], v[108:111], v[88:91], v[180:183]\n"
"v_mfma_f32_16x16x32_bf16 v[180:183], v[120:123], v[96:99], v[180:183]\n"
"s_waitcnt vmcnt(23)\n"
"; @A G1 0.1\n"
"v_mfma_f32_16x16x32_bf16 v[68:71], a[0:3], v[128:131], 0\n"
"v_mfma_f32_16x16x32_bf16 v[68:71], a[4:7], v[144:147], v[68:71]\n"
"v_mfma_f32_16x16x32_bf16 v[68:71], a[8:11], v[148:151], v[68:71]\n"
"s_add_u32 s98, s55, 0x40000\n"
"buffer_load_dwordx4 v[80:83], v0, s[4:7], s98 offen sc1\n"
"v_mfma_f32_16x16x32_bf16 v[68:71], a[12:15], v[152:155], v[68:71]\n"
"s_waitcnt vmcnt(20)\n"
"v_mfma_f32_16x16x32_bf16 v[68:71], a[16:19], v[156:159], v[68:71]\n"
"v_mfma_f32_16x16x32_bf16 v[68:71], a[20:23], v[160:163], v[68:71]\n"
"ds_write_b128 v3, v[180:183] offset:5120\n"
"v_mfma_f32_16x16x32_bf16 v[68:71], a[24:27], v[164:167], v[68:71]\n"
"v_mfma_f32_16x16x32_bf16 v[68:71], a[28:31], v[168:171], v[68:71]\n"
"buffer_load_dwordx4 v[84:87], v0, s[4:7], s98 offen offset:1024 sc1\n"
"v_mfma_f32_16x16x32_bf16 v[88:91], a[60:63], v[168:171], 0\n"
"v_mfma_f32_16x16x32_bf16 v[88:91], a[56:59], v[164:167], v[88:91]\n"
"v_mfma_f32_16x16x32_bf16 v[88:91], a[52:55], v[160:163], v[88:91]\n"
"v_mfma_f32_16x16x32_bf16 v[88:91], a[48:51], v[156:159], v[88:91]\n"
"v_mfma_f32_16x16x32_bf16 v[88:91], a[44:47], v[152:155], v[88:91]\n"
"s_nop 1\n"
"v_cmp_u_f32_e32 vcc, v68, v68\n"
"v_mfma_f32_16x16x32_bf16 v[88:91], a[40:43], v[148:151], v[88:91]\n"
"s_cbranch_vccnz .Lmk_p82s\n"
".Lmk_p82r:\n"
"v_mfma_f32_16x16x32_bf16 v[88:91], a[36:39], v[144:147], v[88:91]\n"
"buffer_load_dwordx4 v[92:95], v0, s[4:7], s98 offen offset:2048 sc1\n"
"v_mfma_f32_16x16x32_bf16 v[88:91], a[32:35], v[128:131], v[88:91]\n"
"ds_write_b128 v3, v[68:71] offset:6144\n"
"v_mfma_f32_16x16x32_bf16 v[68:71], a[64:67], v[128:131], 0\n"
"v_mfma_f32_16x16x32_bf16 v[68:71], a[68:71], v[144:147], v[68:71]\n"
"buffer_load_dwordx4 v[96:99], v0, s[4:7], s98 offen offset:3072 sc1\n"
"v_mfma_f32_16x16x32_bf16 v[68:71], a[72:75], v[148:151], v[68:71]\n"
"v_mfma_f32_16x16x32_bf16 v[68:71], a[76:79], v[152:155], v[68:71]\n"
"v_mfma_f32_16x16x32_bf16 v[68:71], a[80:83], v[156:159], v[68:71]\n"
"v_mfma_f32_16x16x32_bf16 v[68:71], a[84:87], v[160:163], v[68:71]\n"
"ds_write_b128 v3, v[88:91] offset:7168\n"
"v_mfma_f32_16x16x32_bf16 v[68:71], a[88:91], v[164:167], v[68:71]\n"
"buffer_load_dwordx4 v[88:91], v1, s[4:7], s98 offen sc1\n"
"v_mfma_f32_16x16x32_bf16 v[68:71], a[92:95], v[168:171], v[68:71]\n"
"v_mfma_f32_16x16x32_bf16 v[104:107], a[124:127], v[168:171], 0\n"
"v_mfma_f32_16x16x32_bf16 v[104:107], a[120:123], v[164:167], v[104:107]\n"
"v_mfma_f32_16x16x32_bf16 v[104:107], a[116:119], v[160:163], v[104:107]\n"
"v_mfma_f32_16x16x32_bf16 v[104:107], a[112:115], v[156:159], v[104:107]\n"
"buffer_load_dwordx4 v[116:119], v1, s[4:7], s98 offen offset:1024 sc1\n"
"v_mfma_f32_16x16x32_bf16 v[104:107], a[108:111], v[152:155], v[104:107]\n"
"v_mfma_f32_16x16x32_bf16 v[104:107], a[104:107], v[148:151], v[104:107]\n"
"s_nop 0\n"
"ds_write_b128 v3, v[68:71] offset:8192\n"
"v_mfma_f32_16x16x32_bf16 v[104:107], a[100:103], v[144:147], v[104:107]\n"
"v_mfma_f32_16x16x32_bf16 v[104:107], a[96:99], v[128:131], v[104:107]\n"
"v_mfma_f32_16x16x32_bf16 v[68:71], v[28:31], v[128:131], 0\n"
"buffer_load_dwordx4 v[124:127], v1, s[4:7], s98 offen offset:2048 sc1\n"
"v_mfma_f32_16x16x32_bf16 v[68:71], v[40:43], v[144:147], v[68:71]\n"
"s_add_u32 s97, s64, 0x4000\n"
"v_mfma_f32_16x16x32_bf16 v[68:71], v[24:27], v[148:151], v[68:71]\n"
"v_mfma_f32_16x16x32_bf16 v[68:71], v[72:75], v[152:155], v[68:71]\n"
"buffer_store_dwordx4 v[52:55], v2, s[4:7], s97 offen offset:1024 sc1\n"
"v_mfma_f32_16x16x32_bf16 v[68:71], v[32:35], v[156:159], v[68:71]\n"
"s_waitcnt vmcnt(10)\n"
"v_add_f32_e32 v255, v255, v251\n"
"v_add_f32_e32 v254, v254, v250\n"
"v_mfma_f32_16x16x32_bf16 v[68:71], v[64:67], v[160:163], v[68:71]\n"
"buffer_load_dwordx4 v[172:175], v1, s[4:7], s98 offen offset:3072 sc1\n"
"v_mfma_f32_16x16x32_bf16 v[68:71], v[60:63], v[164:167], v[68:71]\n"
"ds_write_b128 v3, v[104:107] offset:9216\n"
"v_mfma_f32_16x16x32_bf16 v[68:71], v[36:39], v[168:171], v[68:71]\n"
"v_add_f32_e32 v253, v253, v249\n"
"v_add_f32_e32 v252, v252, v248\n"
"v_mfma_f32_16x16x32_bf16 v[104:107], v[100:103], v[168:171], 0\n"
"v_add_f32_e32 v255, v255, v253\n"
"v_add_f32_e32 v254, v254, v252\n"
"v_mfma_f32_16x16x32_bf16 v[104:107], v[76:79], v[164:167], v[104:107]\n"
"v_add_f32_e32 v255, v255, v254\n"
"v_cmp_u_f32_e32 vcc, v255, v255\n"
"v_mfma_f32_16x16x32_bf16 v[104:107], v[140:143], v[160:163], v[104:107]\n"
"s_cbranch_vccnz .Lmk_m85s\n"
".Lmk_m85r:\n"
"v_mov_b32_e32 v254, v255\n"
"v_mfma_f32_16x16x32_bf16 v[104:107], v[136:139], v[156:159], v[104:107]\n"
"s_nop 0\n"
"v_permlane32_swap_b32_e32 v255, v254\n"
"v_add_f32_e32 v255, v255, v254\n"
"v_mfma_f32_16x16x32_bf16 v[104:107], v[56:59], v[152:155], v[104:107]\n"
"v_mul_f32_e32 v255, 0x3a800000, v255\n"
"v_mov_b32_e32 v254, v255\n"
"v_mfma_f32_16x16x32_bf16 v[104:107], v[112:115], v[148:151], v[104:107]\n"
"ds_write_b128 v3, v[68:71] offset:10240\n"
"v_mfma_f32_16x16x32_bf16 v[104:107], v[108:111], v[144:147], v[104:107]\n"
"v_permlane16_swap_b32_e32 v255, v254\n"
"v_cvt_pk_bf16_f32 v253, -v255, 0\n"
"v_mfma_f32_16x16x32_bf16 v[104:107], v[120:123], v[128:131], v[104:107]\n"
"v_fma_f32 v254, -v255, v255, v254\n"
"v_lshlrev_b32_e32 v253, 16, v253\n"
"; @A G1 0.2\n"
"v_mfma_f32_16x16x32_bf16 v[68:71], a[0:3], v[176:179], 0\n"
"v_max_f32_e32 v254, 0, v254\n"
"v_cvt_pk_bf16_f32 v254, v254, 0\n"
"v_mfma_f32_16x16x32_bf16 v[68:71], a[4:7], v[48:51], v[68:71]\n"
"v_lshlrev_b32_e32 v254, 16, v254\n"
"v_add_f32_e32 v254, 0x3727c5ac, v254\n"
"v_mfma_f32_16x16x32_bf16 v[68:71], a[8:11], v[132:135], v[68:71]\n"
"v_cvt_pk_bf16_f32 v254, v254, 0\n"
"v_lshlrev_b32_e32 v254, 16, v254\n"
"v_mfma_f32_16x16x32_bf16 v[68:71], a[12:15], v[184:187], v[68:71]\n"
"s_waitcnt vmcnt(10)\n"
"v_sqrt_f32_e32 v254, v254\n"
"v_mfma_f32_16x16x32_bf16 v[68:71], a[16:19], v[44:47], v[68:71]\n"
"s_mov_b32 s98, .Lmk_a2-.Lmk_g80\n"
"s_getpc_b64 s[84:85]\n"
".Lmk_g80:\n"
"s_add_u32 s84, s84, s98\n"
"s_addc_u32 s85, s85, 0\n"
"s_and_b32 s84, s84, -16\n"
"v_mfma_f32_16x16x32_bf16 v[68:71], a[20:23], v[192:195], v[68:71]\n"
"ds_write_b128 v3, v[104:107] offset:11264\n"
"v_mfma_f32_16x16x32_bf16 v[68:71], a[24:27], v[196:199], v[68:71]\n"
"s_and_b32 s85, s85, 0xffff\n"
"s_sub_u32 s98, .Lmk_end-.Lmk_g80, s98\n"
"s_min_u32 s86, s98, 0x3000\n"
"s_mov_b32 s87, 0x20000\n"
"v_mfma_f32_16x16x32_bf16 v[68:71], a[28:31], v[200:203], v[68:71]\n"
"s_mov_b32 m0, 0x27800\n"
"s_nop 0\n"
"buffer_load_dwordx4 v0, s[84:87], s99 offen lds\n"
"v_mfma_f32_16x16x32_bf16 v[104:107], a[60:63], v[200:203], 0\n"
"v_cvt_pk_bf16_f32 v254, v254, 0\n"
"v_lshlrev_b32_e32 v254, 16, v254\n"
"v_mfma_f32_16x16x32_bf16 v[104:107], a[56:59], v[196:199], v[104:107]\n"
"v_rcp_f32_e32 v255, v254\n"
"v_mfma_f32_16x16x32_bf16 v[104:107], a[52:55], v[192:195], v[104:107]\n"
"s_add_u32 s98, s52, 0x48000\n"
"buffer_load_dwordx4 v[128:131], v0, s[4:7], s98 offen sc1\n"
"v_mfma_f32_16x16x32_bf16 v[104:107], a[48:51], v[44:47], v[104:107]\n"
"s_waitcnt lgkmcnt(0)\n"
"s_barrier\n"
"v_mfma_f32_16x16x32_bf16 v[104:107], a[44:47], v[184:187], v[104:107]\n"
"v_cmp_u_f32_e32 vcc, v68, v68\n"
"v_mul_f32_e32 v254, 0x3e8293ee, v255\n"
"v_mfma_f32_16x16x32_bf16 v[104:107], a[40:43], v[132:135], v[104:107]\n"
"s_cbranch_vccnz .Lmk_p83s\n"
".Lmk_p83r:\n"
"ds_read_b128 v[144:147], v4\n"
"v_mfma_f32_16x16x32_bf16 v[104:107], a[36:39], v[48:51], v[104:107]\n"
"ds_write_b128 v3, v[68:71] offset:12288\n"
"v_mfma_f32_16x16x32_bf16 v[104:107], a[32:35], v[176:179], v[104:107]\n"
"buffer_load_dwordx4 v[68:71], v0, s[4:7], s98 offen offset:1024 sc1\n"
"v_mfma_f32_16x16x32_bf16 v[148:151], a[64:67], v[176:179], 0\n"
"ds_read_b128 v[152:155], v9 offset:128\n"
"ds_read_b128 v[156:159], v5\n"
"v_mfma_f32_16x16x32_bf16 v[148:151], a[68:71], v[48:51], v[148:151]\n"
"v_mfma_f32_16x16x32_bf16 v[148:151], a[72:75], v[132:135], v[148:151]\n"
"v_mfma_f32_16x16x32_bf16 v[148:151], a[76:79], v[184:187], v[148:151]\n"
"ds_read_b128 v[160:163], v4 offset:1024\n"
"ds_read_b128 v[164:167], v9 offset:192\n"
"v_mfma_f32_16x16x32_bf16 v[148:151], a[80:83], v[44:47], v[148:151]\n"
"buffer_load_dwordx4 v[168:171], v0, s[4:7], s98 offen offset:2048 sc1\n"
"v_mfma_f32_16x16x32_bf16 v[148:151], a[84:87], v[192:195], v[148:151]\n"
"ds_write_b128 v3, v[104:107] offset:13312\n"
"v_mfma_f32_16x16x32_bf16 v[148:151], a[88:91], v[196:199], v[148:151]\n"
"ds_read_b128 v[104:107], v5 offset:1024\n"
"s_waitcnt lgkmcnt(4)\n"
"v_mfma_f32_16x16x32_bf16 v[148:151], a[92:95], v[200:203], v[148:151]\n"
"v_fmac_f32_e32 v144, v253, v152\n"
"v_fmac_f32_e32 v145, v253, v153\n"
"v_mfma_f32_16x16x32_bf16 v[180:183], a[124:127], v[200:203], 0\n"
"v_fmac_f32_e32 v146, v253, v154\n"
"v_fmac_f32_e32 v147, v253, v155\n"
"v_mfma_f32_16x16x32_bf16 v[180:183], a[120:123], v[196:199], v[180:183]\n"
"buffer_load_dwordx4 v[152:155], v0, s[4:7], s98 offen offset:3072 sc1\n"
"v_mfma_f32_16x16x32_bf16 v[180:183], a[116:119], v[192:195], v[180:183]\n"
"ds_read_b128 v[188:191], v4 offset:2048\n"
"ds_read_b128 v[204:207], v9 offset:256\n"
"v_mfma_f32_16x16x32_bf16 v[180:183], a[112:115], v[44:47], v[180:183]\n"
"ds_read_b128 v[208:211], v5 offset:2048\n"
"v_add_f32_e32 v144, v144, v156\n"
"v_mfma_f32_16x16x32_bf16 v[180:183], a[108:111], v[184:187], v[180:183]\n"
"v_add_f32_e32 v145, v145, v157\n"
"v_add_f32_e32 v146, v146, v158\n"
"v_mfma_f32_16x16x32_bf16 v[180:183], a[104:107], v[132:135], v[180:183]\n"
"ds_write_b128 v3, v[148:151] offset:14336\n"
"v_mfma_f32_16x16x32_bf16 v[180:183], a[100:103], v[48:51], v[180:183]\n"
"buffer_load_dwordx4 v[148:151], v1, s[4:7], s98 offen sc1\n"
"v_mfma_f32_16x16x32_bf16 v[180:183], a[96:99], v[176:179], v[180:183]\n"
"s_waitcnt lgkmcnt(4)\n"
"v_add_f32_e32 v147, v147, v159\n"
"v_fmac_f32_e32 v160, v253, v164\n"
"v_mfma_f32_16x16x32_bf16 v[156:159], v[28:31], v[176:179], 0\n"
"v_fmac_f32_e32 v161, v253, v165\n"
"v_fmac_f32_e32 v162, v253, v166\n"
"v_mfma_f32_16x16x32_bf16 v[156:159], v[40:43], v[48:51], v[156:159]\n"
"ds_read_b128 v[212:215], v4 offset:3072\n"
"ds_read_b128 v[216:219], v9 offset:320\n"
"v_mfma_f32_16x16x32_bf16 v[156:159], v[24:27], v[132:135], v[156:159]\n"
"ds_read_b128 v[220:223], v5 offset:3072\n"
"v_fmac_f32_e32 v163, v253, v167\n"
"v_mfma_f32_16x16x32_bf16 v[156:159], v[72:75], v[184:187], v[156:159]\n"
"buffer_load_dwordx4 v[164:167], v1, s[4:7], s98 offen offset:1024 sc1\n"
"v_mfma_f32_16x16x32_bf16 v[156:159], v[32:35], v[44:47], v[156:159]\n"
"s_add_u32 s97, s64, 0x8000\n"
"v_add_f32_e32 v160, v160, v104\n"
"v_add_f32_e32 v161, v161, v105\n"
"v_mfma_f32_16x16x32_bf16 v[156:159], v[64:67], v[192:195], v[156:159]\n"
"ds_write_b128 v3, v[180:183] offset:15360\n"
"v_mfma_f32_16x16x32_bf16 v[156:159], v[60:63], v[196:199], v[156:159]\n"
"buffer_store_dwordx4 v[52:55], v2, s[4:7], s97 offen sc1\n"
"v_mfma_f32_16x16x32_bf16 v[156:159], v[36:39], v[200:203], v[156:159]\n"
"s_waitcnt lgkmcnt(5)\n"
"v_add_f32_e32 v162, v162, v106\n"
"v_add_f32_e32 v163, v163, v107\n"
"v_mfma_f32_16x16x32_bf16 v[104:107], v[100:103], v[200:203], 0\n"
"buffer_load_dwordx4 v[180:183], v1, s[4:7], s98 offen offset:2048 sc1\n"
"v_mfma_f32_16x16x32_bf16 v[104:107], v[76:79], v[196:199], v[104:107]\n"
"s_add_u32 s97, s68, s80\n"
"s_waitcnt vmcnt(18)\n"
"ds_read_b128 v[196:199], v4 offset:4096\n"
"v_mfma_f32_16x16x32_bf16 v[104:107], v[140:143], v[192:195], v[104:107]\n"
"buffer_store_dwordx2 v[18:19], v11, s[4:7], s97 offen sc1\n"
"v_mfma_f32_16x16x32_bf16 v[104:107], v[136:139], v[44:47], v[104:107]\n"
"ds_read_b128 v[44:47], v9\n"
"ds_read_b128 v[192:195], v5 offset:4096\n"
"v_mfma_f32_16x16x32_bf16 v[104:107], v[56:59], v[184:187], v[104:107]\n"
"v_fmac_f32_e32 v188, v253, v204\n"
"v_fmac_f32_e32 v189, v253, v205\n"
"v_mfma_f32_16x16x32_bf16 v[104:107], v[112:115], v[132:135], v[104:107]\n"
"buffer_load_dwordx4 v[132:135], v1, s[4:7], s98 offen offset:3072 sc1\n"
"v_mfma_f32_16x16x32_bf16 v[104:107], v[108:111], v[48:51], v[104:107]\n"
"ds_write_b128 v3, v[156:159] offset:16384\n"
"v_mfma_f32_16x16x32_bf16 v[104:107], v[120:123], v[176:179], v[104:107]\n"
"s_waitcnt vmcnt(16)\n"
"ds_read_b128 v[48:51], v4 offset:5120\n"
"ds_read_b128 v[156:159], v9 offset:64\n"
"; @A G1 0.3\n"
"v_mfma_f32_16x16x32_bf16 v[176:179], a[0:3], v[80:83], v[144:147]\n"
"ds_read_b128 v[184:187], v5 offset:5120\n"
"v_fmac_f32_e32 v190, v253, v206\n"
"v_mfma_f32_16x16x32_bf16 v[176:179], a[4:7], v[84:87], v[176:179]\n"
"v_fmac_f32_e32 v191, v253, v207\n"
"v_add_f32_e32 v188, v188, v208\n"
"v_mfma_f32_16x16x32_bf16 v[176:179], a[8:11], v[92:95], v[176:179]\n"
"s_add_u32 s98, s53, 0x48000\n"
"buffer_load_dwordx4 v[200:203], v0, s[4:7], s98 offen sc1\n"
"v_mfma_f32_16x16x32_bf16 v[176:179], a[12:15], v[96:99], v[176:179]\n"
"s_waitcnt vmcnt(12)\n"
"v_add_f32_e32 v189, v189, v209\n"
"v_add_f32_e32 v190, v190, v210\n"
"v_mfma_f32_16x16x32_bf16 v[176:179], a[16:19], v[88:91], v[176:179]\n"
"s_waitcnt lgkmcnt(8)\n"
"v_add_f32_e32 v191, v191, v211\n"
"v_fmac_f32_e32 v212, v253, v216\n"
"v_mfma_f32_16x16x32_bf16 v[176:179], a[20:23], v[116:119], v[176:179]\n"
"ds_write_b128 v3, v[104:107] offset:17408\n"
"v_mfma_f32_16x16x32_bf16 v[176:179], a[24:27], v[124:127], v[176:179]\n"
"v_fmac_f32_e32 v213, v253, v217\n"
"v_fmac_f32_e32 v214, v253, v218\n"
"v_mfma_f32_16x16x32_bf16 v[176:179], a[28:31], v[172:175], v[176:179]\n"
"buffer_load_dwordx4 v[104:107], v0, s[4:7], s98 offen offset:1024 sc1\n"
"v_mfma_f32_16x16x32_bf16 v[204:207], a[60:63], v[172:175], v[160:163]\n"
"v_fmac_f32_e32 v215, v253, v219\n"
"v_add_f32_e32 v212, v212, v220\n"
"v_mfma_f32_16x16x32_bf16 v[204:207], a[56:59], v[124:127], v[204:207]\n"
"v_add_f32_e32 v213, v213, v221\n"
"v_add_f32_e32 v214, v214, v222\n"
"v_mfma_f32_16x16x32_bf16 v[204:207], a[52:55], v[116:119], v[204:207]\n"
"s_waitcnt lgkmcnt(0)\n"
"s_barrier\n"
"v_mfma_f32_16x16x32_bf16 v[204:207], a[48:51], v[88:91], v[204:207]\n"
"ds_read_b128 v[208:211], v6\n"
"ds_read_b128 v[216:219], v6 offset:1024\n"
"v_mfma_f32_16x16x32_bf16 v[204:207], a[44:47], v[96:99], v[204:207]\n"
"v_cmp_u_f32_e32 vcc, v176, v176\n"
"v_add_f32_e32 v215, v215, v223\n"
"v_mfma_f32_16x16x32_bf16 v[204:207], a[40:43], v[92:95], v[204:207]\n"
"s_cbranch_vccnz .Lmk_p84s\n"
".Lmk_p84r:\n"
"v_fmac_f32_e32 v196, v253, v44\n"
"v_mfma_f32_16x16x32_bf16 v[204:207], a[36:39], v[84:87], v[204:207]\n"
"buffer_load_dwordx4 v[144:147], v0, s[4:7], s98 offen offset:2048 sc1\n"
"v_mfma_f32_16x16x32_bf16 v[204:207], a[32:35], v[80:83], v[204:207]\n"
"v_fmac_f32_e32 v197, v253, v45\n"
"v_fmac_f32_e32 v198, v253, v46\n"
"v_mfma_f32_16x16x32_bf16 v[188:191], a[64:67], v[80:83], v[188:191]\n"
"v_fmac_f32_e32 v199, v253, v47\n"
"v_add_f32_e32 v196, v196, v192\n"
"v_mfma_f32_16x16x32_bf16 v[188:191], a[68:71], v[84:87], v[188:191]\n"
"buffer_load_dwordx4 v[44:47], v0, s[4:7], s98 offen offset:3072 sc1\n"
"v_mfma_f32_16x16x32_bf16 v[188:191], a[72:75], v[92:95], v[188:191]\n"
"v_add_f32_e32 v197, v197, v193\n"
"v_add_f32_e32 v198, v198, v194\n"
"v_mfma_f32_16x16x32_bf16 v[188:191], a[76:79], v[96:99], v[188:191]\n"
"v_add_f32_e32 v199, v199, v195\n"
"v_fmac_f32_e32 v48, v253, v156\n"
"v_mfma_f32_16x16x32_bf16 v[188:191], a[80:83], v[88:91], v[188:191]\n"
"s_waitcnt lgkmcnt(0)\n"
"v_fmac_f32_e32 v49, v253, v157\n"
"v_fmac_f32_e32 v50, v253, v158\n"
"v_mfma_f32_16x16x32_bf16 v[188:191], a[84:87], v[116:119], v[188:191]\n"
"v_fmac_f32_e32 v51, v253, v159\n"
"v_add_f32_e32 v48, v48, v184\n"
"v_mfma_f32_16x16x32_bf16 v[188:191], a[88:91], v[124:127], v[188:191]\n"
"buffer_load_dwordx4 v[156:159], v1, s[4:7], s98 offen sc1\n"
"v_mfma_f32_16x16x32_bf16 v[188:191], a[92:95], v[172:175], v[188:191]\n"
"ds_read_b128 v[160:163], v6 offset:2048\n"
"ds_read_b128 v[192:195], v6 offset:3072\n"
"v_mfma_f32_16x16x32_bf16 v[212:215], a[124:127], v[172:175], v[212:215]\n"
"v_add_f32_e32 v49, v49, v185\n"
"v_add_f32_e32 v50, v50, v186\n"
"v_mfma_f32_16x16x32_bf16 v[212:215], a[120:123], v[124:127], v[212:215]\n"
"v_add_f32_e32 v51, v51, v187\n"
"v_add_f32_e32 v176, v176, v208\n"
"v_mfma_f32_16x16x32_bf16 v[212:215], a[116:119], v[116:119], v[212:215]\n"
"v_add_f32_e32 v177, v177, v209\n"
"v_add_f32_e32 v178, v178, v210\n"
"v_mfma_f32_16x16x32_bf16 v[212:215], a[112:115], v[88:91], v[212:215]\n"
"buffer_load_dwordx4 v[184:187], v1, s[4:7], s98 offen offset:1024 sc1\n"
"v_mfma_f32_16x16x32_bf16 v[212:215], a[108:111], v[96:99], v[212:215]\n"
"v_add_f32_e32 v179, v179, v211\n"
"v_add_f32_e32 v204, v204, v216\n"
"v_mfma_f32_16x16x32_bf16 v[212:215], a[104:107], v[92:95], v[212:215]\n"
"v_add_f32_e32 v205, v205, v217\n"
"v_add_f32_e32 v206, v206, v218\n"
"v_mfma_f32_16x16x32_bf16 v[212:215], a[100:103], v[84:87], v[212:215]\n"
"v_add_f32_e32 v207, v207, v219\n"
"v_mul_f32_e32 v176, v176, v255\n"
"v_mfma_f32_16x16x32_bf16 v[212:215], a[96:99], v[80:83], v[212:215]\n"
"v_mul_f32_e32 v177, v177, v255\n"
"v_mul_f32_e32 v178, v178, v255\n"
"v_mfma_f32_16x16x32_bf16 v[196:199], v[28:31], v[80:83], v[196:199]\n"
"buffer_load_dwordx4 v[208:211], v1, s[4:7], s98 offen offset:2048 sc1\n"
"v_mfma_f32_16x16x32_bf16 v[196:199], v[40:43], v[84:87], v[196:199]\n"
"v_mul_f32_e32 v179, v179, v255\n"
"v_mul_f32_e32 v204, v204, v255\n"
"v_mfma_f32_16x16x32_bf16 v[196:199], v[24:27], v[92:95], v[196:199]\n"
"v_mul_f32_e32 v205, v205, v255\n"
"v_mul_f32_e32 v206, v206, v255\n"
"v_mfma_f32_16x16x32_bf16 v[196:199], v[72:75], v[96:99], v[196:199]\n"
"v_mul_f32_e32 v207, v207, v255\n"
"v_cvt_pk_bf16_f32 v216, v176, v177\n"
"v_mfma_f32_16x16x32_bf16 v[196:199], v[32:35], v[88:91], v[196:199]\n"
"s_waitcnt lgkmcnt(0)\n"
"v_cvt_pk_bf16_f32 v217, v178, v179\n"
"v_cvt_pk_bf16_f32 v218, v204, v205\n"
"v_mfma_f32_16x16x32_bf16 v[196:199], v[64:67], v[116:119], v[196:199]\n"
"buffer_load_dwordx4 v[176:179], v1, s[4:7], s98 offen offset:3072 sc1\n"
"v_mfma_f32_16x16x32_bf16 v[196:199], v[60:63], v[124:127], v[196:199]\n"
"s_add_u32 s98, s64, 0x8000\n"
"ds_read_b128 v[220:223], v6 offset:4096\n"
"ds_read_b128 v[224:227], v6 offset:5120\n"
"v_mfma_f32_16x16x32_bf16 v[196:199], v[36:39], v[172:175], v[196:199]\n"
"v_cvt_pk_bf16_f32 v219, v206, v207\n"
"v_add_f32_e32 v188, v188, v160\n"
"v_mfma_f32_16x16x32_bf16 v[48:51], v[100:103], v[172:175], v[48:51]\n"
"buffer_store_dwordx4 v[52:55], v2, s[4:7], s98 offen offset:1024 sc1\n"
"v_mfma_f32_16x16x32_bf16 v[48:51], v[76:79], v[124:127], v[48:51]\n"
"ds_write_b128 v7, v[216:219] offset:24576\n"
"v_mfma_f32_16x16x32_bf16 v[48:51], v[140:143], v[116:119], v[48:51]\n"
"v_add_f32_e32 v189, v189, v161\n"
"v_add_f32_e32 v190, v190, v162\n"
"v_mfma_f32_16x16x32_bf16 v[48:51], v[136:139], v[88:91], v[48:51]\n"
"v_add_f32_e32 v191, v191, v163\n"
"v_add_f32_e32 v212, v212, v192\n"
"v_mfma_f32_16x16x32_bf16 v[48:51], v[56:59], v[96:99], v[48:51]\n"
"v_add_f32_e32 v213, v213, v193\n"
"v_add_f32_e32 v214, v214, v194\n"
"v_mfma_f32_16x16x32_bf16 v[48:51], v[112:115], v[92:95], v[48:51]\n"
"v_add_f32_e32 v215, v215, v195\n"
"v_mul_f32_e32 v188, v188, v255\n"
"v_mfma_f32_16x16x32_bf16 v[48:51], v[108:111], v[84:87], v[48:51]\n"
"v_mul_f32_e32 v189, v189, v255\n"
"v_mul_f32_e32 v190, v190, v255\n"
"v_mfma_f32_16x16x32_bf16 v[48:51], v[120:123], v[80:83], v[48:51]\n"
"v_mul_f32_e32 v191, v191, v255\n"
"v_mul_f32_e32 v212, v212, v255\n"
"v_mul_f32_e32 v213, v213, v255\n"
"v_mul_f32_e32 v214, v214, v255\n"
"v_mul_f32_e32 v215, v215, v255\n"
"v_cvt_pk_bf16_f32 v52, v188, v189\n"
"v_cvt_pk_bf16_f32 v53, v190, v191\n"
"v_cvt_pk_bf16_f32 v54, v212, v213\n"
"v_cvt_pk_bf16_f32 v55, v214, v215\n"
"ds_write_b128 v7, v[52:55] offset:57344\n"
".Lmk_a1:\n"
"s_waitcnt vmcnt(15)\n"
"; @A G1 1.0\n"
"v_mfma_f32_16x16x32_bf16 v[80:83], a[0:3], v[128:131], 0\n"
"v_mfma_f32_16x16x32_bf16 v[80:83], a[4:7], v[68:71], v[80:83]\n"
"s_add_u32 s98, s62, 0x4800\n"
"v_mfma_f32_16x16x32_bf16 v[80:83], a[8:11], v[168:171], v[80:83]\n"
"s_add_u32 s97, s54, 0x48000\n"
"buffer_load_dwordx4 v[84:87], v0, s[4:7], s97 offen sc1\n"
"v_mfma_f32_16x16x32_bf16 v[80:83], a[12:15], v[152:155], v[80:83]\n"
"s_waitcnt vmcnt(10)\n"
"v_mfma_f32_16x16x32_bf16 v[80:83], a[16:19], v[148:151], v[80:83]\n"
"buffer_load_dword v255, v10, s[4:7], s98 offen sc1\n"
"v_mfma_f32_16x16x32_bf16 v[80:83], a[20:23], v[164:167], v[80:83]\n"
"s_waitcnt lgkmcnt(2)\n"
"v_add_f32_e32 v196, v196, v220\n"
"v_add_f32_e32 v197, v197, v221\n"
"v_mfma_f32_16x16x32_bf16 v[80:83], a[24:27], v[180:183], v[80:83]\n"
"v_add_f32_e32 v198, v198, v222\n"
"v_add_f32_e32 v199, v199, v223\n"
"v_mfma_f32_16x16x32_bf16 v[80:83], a[28:31], v[132:135], v[80:83]\n"
"buffer_load_dwordx4 v[88:91], v0, s[4:7], s97 offen offset:1024 sc1\n"
"v_mfma_f32_16x16x32_bf16 v[92:95], a[60:63], v[132:135], 0\n"
"s_add_u32 s96, s70, 0\n"
"buffer_load_dwordx4 a[128:131], v0, s[4:7], s96 offen sc1\n"
"v_mfma_f32_16x16x32_bf16 v[92:95], a[56:59], v[180:183], v[92:95]\n"
"buffer_load_dword v253, v10, s[4:7], s98 offen offset:256 sc1\n"
"v_mfma_f32_16x16x32_bf16 v[92:95], a[52:55], v[164:167], v[92:95]\n"
"v_add_f32_e32 v48, v48, v224\n"
"v_add_f32_e32 v49, v49, v225\n"
"v_mfma_f32_16x16x32_bf16 v[92:95], a[48:51], v[148:151], v[92:95]\n"
"v_add_f32_e32 v50, v50, v226\n"
"v_add_f32_e32 v51, v51, v227\n"
"v_mfma_f32_16x16x32_bf16 v[92:95], a[44:47], v[152:155], v[92:95]\n"
"v_cmp_u_f32_e32 vcc, v80, v80\n"
"v_mul_f32_e32 v196, v196, v254\n"
"v_mfma_f32_16x16x32_bf16 v[92:95], a[40:43], v[168:171], v[92:95]\n"
"s_cbranch_vccnz .Lmk_p86s\n"
".Lmk_p86r:\n"
"v_mul_f32_e32 v197, v197, v254\n"
"v_mfma_f32_16x16x32_bf16 v[92:95], a[36:39], v[68:71], v[92:95]\n"
"buffer_load_dwordx4 v[96:99], v0, s[4:7], s97 offen offset:2048 sc1\n"
"v_mfma_f32_16x16x32_bf16 v[92:95], a[32:35], v[128:131], v[92:95]\n"
"ds_write_b128 v3, v[80:83]\n"
"v_mfma_f32_16x16x32_bf16 v[80:83], a[64:67], v[128:131], 0\n"
"buffer_load_dword v252, v10, s[4:7], s98 offen offset:512 sc1\n"
"v_mfma_f32_16x16x32_bf16 v[80:83], a[68:71], v[68:71], v[80:83]\n"
"buffer_load_dwordx4 v[116:119], v0, s[4:7], s97 offen offset:3072 sc1\n"
"v_mfma_f32_16x16x32_bf16 v[80:83], a[72:75], v[168:171], v[80:83]\n"
"v_mul_f32_e32 v198, v198, v254\n"
"v_mul_f32_e32 v199, v199, v254\n"
"v_mfma_f32_16x16x32_bf16 v[80:83], a[76:79], v[152:155], v[80:83]\n"
"buffer_load_dword v251, v10, s[4:7], s98 offen offset:768 sc1\n"
"v_mfma_f32_16x16x32_bf16 v[80:83], a[80:83], v[148:151], v[80:83]\n"
"v_mul_f32_e32 v48, v48, v254\n"
"v_mul_f32_e32 v49, v49, v254\n"
"v_mfma_f32_16x16x32_bf16 v[80:83], a[84:87], v[164:167], v[80:83]\n"
"ds_write_b128 v3, v[92:95] offset:1024\n"
"v_mfma_f32_16x16x32_bf16 v[80:83], a[88:91], v[180:183], v[80:83]\n"
"buffer_load_dwordx4 v[92:95], v1, s[4:7], s97 offen sc1\n"
"v_mfma_f32_16x16x32_bf16 v[80:83], a[92:95], v[132:135], v[80:83]\n"
"buffer_load_dword v250, v10, s[4:7], s98 offen offset:1024 sc1\n"
"v_mfma_f32_16x16x32_bf16 v[124:127], a[124:127], v[132:135], 0\n"
"v_mul_f32_e32 v50, v50, v254\n"
"v_mul_f32_e32 v51, v51, v254\n"
"v_mfma_f32_16x16x32_bf16 v[124:127], a[120:123], v[180:183], v[124:127]\n"
"v_cvt_pk_bf16_f32 v160, v196, v197\n"
"v_cvt_pk_bf16_f32 v161, v198, v199\n"
"v_mfma_f32_16x16x32_bf16 v[124:127], a[116:119], v[164:167], v[124:127]\n"
"buffer_load_dword v254, v10, s[4:7], s98 offen offset:1280 sc1\n"
"v_mfma_f32_16x16x32_bf16 v[124:127], a[112:115], v[148:151], v[124:127]\n"
"buffer_load_dwordx4 v[172:175], v1, s[4:7], s97 offen offset:1024 sc1\n"
"v_mfma_f32_16x16x32_bf16 v[124:127], a[108:111], v[152:155], v[124:127]\n"
"buffer_load_dwordx4 a[132:135], v0, s[4:7], s96 offen offset:1024 sc1\n"
"v_mfma_f32_16x16x32_bf16 v[124:127], a[104:107], v[168:171], v[124:127]\n"
"ds_write_b128 v3, v[80:83] offset:2048\n"
"v_mfma_f32_16x16x32_bf16 v[124:127], a[100:103], v[68:71], v[124:127]\n"
"buffer_load_dword v249, v10, s[4:7], s98 offen offset:1536 sc1\n"
"v_mfma_f32_16x16x32_bf16 v[124:127], a[96:99], v[128:131], v[124:127]\n"
"v_cvt_pk_bf16_f32 v162, v48, v49\n"
"v_cvt_pk_bf16_f32 v163, v50, v51\n"
"v_mfma_f32_16x16x32_bf16 v[48:51], v[28:31], v[128:131], 0\n"
"buffer_load_dwordx4 v[80:83], v1, s[4:7], s97 offen offset:2048 sc1\n"
"v_mfma_f32_16x16x32_bf16 v[48:51], v[40:43], v[68:71], v[48:51]\n"
"buffer_load_dword v248, v10, s[4:7], s98 offen offset:1792 sc1\n"
"v_mfma_f32_16x16x32_bf16 v[48:51], v[24:27], v[168:171], v[48:51]\n"
"ds_write_b128 v2, v[160:163] offset:49152\n"
"v_mfma_f32_16x16x32_bf16 v[48:51], v[72:75], v[152:155], v[48:51]\n"
"v_mfma_f32_16x16x32_bf16 v[48:51], v[32:35], v[148:151], v[48:51]\n"
"v_mfma_f32_16x16x32_bf16 v[48:51], v[64:67], v[164:167], v[48:51]\n"
"buffer_load_dwordx4 v[160:163], v1, s[4:7], s97 offen offset:3072 sc1\n"
"v_mfma_f32_16x16x32_bf16 v[48:51], v[60:63], v[180:183], v[48:51]\n"
"ds_write_b128 v3, v[124:127] offset:3072\n"
"v_mfma_f32_16x16x32_bf16 v[48:51], v[36:39], v[132:135], v[48:51]\n"
"v_mfma_f32_16x16x32_bf16 v[124:127], v[100:103], v[132:135], 0\n"
"v_mfma_f32_16x16x32_bf16 v[124:127], v[76:79], v[180:183], v[124:127]\n"
"v_mfma_f32_16x16x32_bf16 v[124:127], v[140:143], v[164:167], v[124:127]\n"
"v_mfma_f32_16x16x32_bf16 v[124:127], v[136:139], v[148:151], v[124:127]\n"
"v_mfma_f32_16x16x32_bf16 v[124:127], v[56:59], v[152:155], v[124:127]\n"
"v_mfma_f32_16x16x32_bf16 v[124:127], v[112:115], v[168:171], v[124:127]\n"
"s_nop 1\n"
"ds_write_b128 v3, v[48:51] offset:4096\n"
"v_mfma_f32_16x16x32_bf16 v[124:127], v[108:111], v[68:71], v[124:127]\n"
"v_mfma_f32_16x16x32_bf16 v[124:127], v[120:123], v[128:131], v[124:127]\n"
"s_waitcnt vmcnt(23)\n"
"; @A G1 1.1\n"
"v_mfma_f32_16x16x32_bf16 v[48:51], a[0:3], v[200:203], 0\n"
"buffer_load_dwordx4 a[136:139], v0, s[4:7], s96 offen offset:2048 sc1\n"
"v_mfma_f32_16x16x32_bf16 v[48:51], a[4:7], v[104:107], v[48:51]\n"
"v_mfma_f32_16x16x32_bf16 v[48:51], a[8:11], v[144:147], v[48:51]\n"
"s_add_u32 s98, s55, 0x48000\n"
"buffer_load_dwordx4 v[68:71], v0, s[4:7], s98 offen sc1\n"
"v_mfma_f32_16x16x32_bf16 v[48:51], a[12:15], v[44:47], v[48:51]\n"
"s_waitcnt vmcnt(21)\n"
"v_mfma_f32_16x16x32_bf16 v[48:51], a[16:19], v[156:159], v[48:51]\n"
"v_mfma_f32_16x16x32_bf16 v[48:51], a[20:23], v[184:187], v[48:51]\n"
"ds_write_b128 v3, v[124:127] offset:5120\n"
"v_mfma_f32_16x16x32_bf16 v[48:51], a[24:27], v[208:211], v[48:51]\n"
"v_mfma_f32_16x16x32_bf16 v[48:51], a[28:31], v[176:179], v[48:51]\n"
"buffer_load_dwordx4 v[124:127], v0, s[4:7], s98 offen offset:1024 sc1\n"
"v_mfma_f32_16x16x32_bf16 v[128:131], a[60:63], v[176:179], 0\n"
"v_mfma_f32_16x16x32_bf16 v[128:131], a[56:59], v[208:211], v[128:131]\n"
"v_mfma_f32_16x16x32_bf16 v[128:131], a[52:55], v[184:187], v[128:131]\n"
"v_mfma_f32_16x16x32_bf16 v[128:131], a[48:51], v[156:159], v[128:131]\n"
"v_mfma_f32_16x16x32_bf16 v[128:131], a[44:47], v[44:47], v[128:131]\n"
"s_add_u32 s97, s67, 0\n"
"s_nop 0\n"
"v_cmp_u_f32_e32 vcc, v48, v48\n"
"v_mfma_f32_16x16x32_bf16 v[128:131], a[40:43], v[144:147], v[128:131]\n"
"s_cbranch_vccnz .Lmk_p87s\n"
".Lmk_p87r:\n"
"v_mfma_f32_16x16x32_bf16 v[128:131], a[36:39], v[104:107], v[128:131]\n"
"buffer_load_dwordx4 v[132:135], v0, s[4:7], s98 offen offset:2048 sc1\n"
"s_add_u32 s95, s64, 0x2000\n"
"v_mfma_f32_16x16x32_bf16 v[128:131], a[32:35], v[200:203], v[128:131]\n"
"ds_write_b128 v3, v[48:51] offset:6144\n"
"v_mfma_f32_16x16x32_bf16 v[48:51], a[64:67], v[200:203], 0\n"
"buffer_load_dwordx2 v[246:247], v12, s[4:7], s97 offen sc1\n"
"v_mfma_f32_16x16x32_bf16 v[48:51], a[68:71], v[104:107], v[48:51]\n"
"buffer_load_dwordx4 v[148:151], v0, s[4:7], s98 offen offset:3072 sc1\n"
"v_mfma_f32_16x16x32_bf16 v[48:51], a[72:75], v[144:147], v[48:51]\n"
"buffer_store_dwordx4 v[216:219], v7, s[4:7], s95 offen sc1\n"
"v_mfma_f32_16x16x32_bf16 v[48:51], a[76:79], v[44:47], v[48:51]\n"
"v_mfma_f32_16x16x32_bf16 v[48:51], a[80:83], v[156:159], v[48:51]\n"
"buffer_load_dwordx4 a[140:143], v0, s[4:7], s96 offen offset:3072 sc1\n"
"v_mfma_f32_16x16x32_bf16 v[48:51], a[84:87], v[184:187], v[48:51]\n"
"ds_write_b128 v3, v[128:131] offset:7168\n"
"v_mfma_f32_16x16x32_bf16 v[48:51], a[88:91], v[208:211], v[48:51]\n"
"buffer_load_dwordx4 v[128:131], v1, s[4:7], s98 offen sc1\n"
"v_mfma_f32_16x16x32_bf16 v[48:51], a[92:95], v[176:179], v[48:51]\n"
"v_mfma_f32_16x16x32_bf16 v[152:155], a[124:127], v[176:179], 0\n"
"v_mfma_f32_16x16x32_bf16 v[152:155], a[120:123], v[208:211], v[152:155]\n"
"v_mfma_f32_16x16x32_bf16 v[152:155], a[116:119], v[184:187], v[152:155]\n"
"v_mfma_f32_16x16x32_bf16 v[152:155], a[112:115], v[156:159], v[152:155]\n"
"buffer_load_dwordx4 v[164:167], v1, s[4:7], s98 offen offset:1024 sc1\n"
"v_mfma_f32_16x16x32_bf16 v[152:155], a[108:111], v[44:47], v[152:155]\n"
"v_mfma_f32_16x16x32_bf16 v[152:155], a[104:107], v[144:147], v[152:155]\n"
"s_nop 0\n"
"ds_write_b128 v3, v[48:51] offset:8192\n"
"v_mfma_f32_16x16x32_bf16 v[152:155], a[100:103], v[104:107], v[152:155]\n"
"v_mfma_f32_16x16x32_bf16 v[152:155], a[96:99], v[200:203], v[152:155]\n"
"v_mfma_f32_16x16x32_bf16 v[48:51], v[28:31], v[200:203], 0\n"
"buffer_load_dwordx4 v[168:171], v1, s[4:7], s98 offen offset:2048 sc1\n"
"v_mfma_f32_16x16x32_bf16 v[48:51], v[40:43], v[104:107], v[48:51]\n"
"v_mfma_f32_16x16x32_bf16 v[48:51], v[24:27], v[144:147], v[48:51]\n"
"v_mfma_f32_16x16x32_bf16 v[48:51], v[72:75], v[44:47], v[48:51]\n"
"v_mfma_f32_16x16x32_bf16 v[48:51], v[32:35], v[156:159], v[48:51]\n"
"s_waitcnt vmcnt(12)\n"
"v_add_f32_e32 v255, v255, v250\n"
"v_add_f32_e32 v253, v253, v254\n"
"v_mfma_f32_16x16x32_bf16 v[48:51], v[64:67], v[184:187], v[48:51]\n"
"buffer_load_dwordx4 v[180:183], v1, s[4:7], s98 offen offset:3072 sc1\n"
"v_mfma_f32_16x16x32_bf16 v[48:51], v[60:63], v[208:211], v[48:51]\n"
"ds_write_b128 v3, v[152:155] offset:9216\n"
"v_mfma_f32_16x16x32_bf16 v[48:51], v[36:39], v[176:179], v[48:51]\n"
"v_add_f32_e32 v252, v252, v249\n"
"v_add_f32_e32 v251, v251, v248\n"
"v_mfma_f32_16x16x32_bf16 v[152:155], v[100:103], v[176:179], 0\n"
"buffer_load_dwordx4 a[144:147], v1, s[4:7], s96 offen sc1\n"
"s_add_u32 s98, s64, 0x6000\n"
"v_mfma_f32_16x16x32_bf16 v[152:155], v[76:79], v[208:211], v[152:155]\n"
"v_add_f32_e32 v255, v255, v252\n"
"v_add_f32_e32 v253, v253, v251\n"
"v_mfma_f32_16x16x32_bf16 v[152:155], v[140:143], v[184:187], v[152:155]\n"
"buffer_store_dwordx4 v[52:55], v7, s[4:7], s98 offen sc1\n"
"v_mfma_f32_16x16x32_bf16 v[152:155], v[136:139], v[156:159], v[152:155]\n"
"v_add_f32_e32 v255, v255, v253\n"
"v_cmp_u_f32_e32 vcc, v255, v255\n"
"v_mfma_f32_16x16x32_bf16 v[152:155], v[56:59], v[44:47], v[152:155]\n"
"s_cbranch_vccnz .Lmk_m90s\n"
".Lmk_m90r:\n"
"v_mov_b32_e32 v253, v255\n"
"v_mfma_f32_16x16x32_bf16 v[152:155], v[112:115], v[144:147], v[152:155]\n"
"ds_write_b128 v3, v[48:51] offset:10240\n"
"v_mfma_f32_16x16x32_bf16 v[152:155], v[108:111], v[104:107], v[152:155]\n"
"v_permlane32_swap_b32_e32 v255, v253\n"
"v_add_f32_e32 v255, v255, v253\n"
"v_mfma_f32_16x16x32_bf16 v[152:155], v[120:123], v[200:203], v[152:155]\n"
"v_mul_f32_e32 v255, 0x3a800000, v255\n"
"v_mov_b32_e32 v253, v255\n"
"; @A G1 1.2\n"
"v_mfma_f32_16x16x32_bf16 v[44:47], a[0:3], v[84:87], 0\n"
"s_nop 0\n"
"v_permlane16_swap_b32_e32 v255, v253\n"
"v_cvt_pk_bf16_f32 v254, -v255, 0\n"
"v_mfma_f32_16x16x32_bf16 v[44:47], a[4:7], v[88:91], v[44:47]\n"
"s_waitcnt vmcnt(9)\n"
"v_cmp_ne_u64_e32 vcc, v[246:247], v[18:19]\n"
"v_mfma_f32_16x16x32_bf16 v[44:47], a[8:11], v[96:99], v[44:47]\n"
"s_cbranch_vccnz .Lmk_f77s\n"
".Lmk_f77r:\n"
"v_fma_f32 v253, -v255, v255, v253\n"
"v_mfma_f32_16x16x32_bf16 v[44:47], a[12:15], v[116:119], v[44:47]\n"
"v_lshlrev_b32_e32 v254, 16, v254\n"
"v_max_f32_e32 v253, 0, v253\n"
"v_mfma_f32_16x16x32_bf16 v[44:47], a[16:19], v[92:95], v[44:47]\n"
"v_cvt_pk_bf16_f32 v253, v253, 0\n"
"v_lshlrev_b32_e32 v253, 16, v253\n"
"v_mfma_f32_16x16x32_bf16 v[44:47], a[20:23], v[172:175], v[44:47]\n"
"ds_write_b128 v3, v[152:155] offset:11264\n"
"v_mfma_f32_16x16x32_bf16 v[44:47], a[24:27], v[80:83], v[44:47]\n"
"v_add_f32_e32 v253, 0x3727c5ac, v253\n"
"v_cvt_pk_bf16_f32 v253, v253, 0\n"
"v_mfma_f32_16x16x32_bf16 v[44:47], a[28:31], v[160:163], v[44:47]\n"
"v_lshlrev_b32_e32 v253, 16, v253\n"
"v_mfma_f32_16x16x32_bf16 v[48:51], a[60:63], v[160:163], 0\n"
"v_sqrt_f32_e32 v253, v253\n"
"v_mfma_f32_16x16x32_bf16 v[48:51], a[56:59], v[80:83], v[48:51]\n"
"v_cvt_pk_bf16_f32 v253, v253, 0\n"
"v_lshlrev_b32_e32 v253, 16, v253\n"
"v_mfma_f32_16x16x32_bf16 v[48:51], a[52:55], v[172:175], v[48:51]\n"
"s_add_u32 s98, s52, 0x8000\n"
"buffer_load_dwordx4 v[52:55], v0, s[4:7], s98 offen sc1\n"
"v_mfma_f32_16x16x32_bf16 v[48:51], a[48:51], v[92:95], v[48:51]\n"
"s_waitcnt lgkmcnt(0)\n"
"s_barrier\n"
"v_mfma_f32_16x16x32_bf16 v[48:51], a[44:47], v[116:119], v[48:51]\n"
"ds_read_b128 v[104:107], v4\n"
"v_cmp_u_f32_e32 vcc, v44, v44\n"
"v_mfma_f32_16x16x32_bf16 v[48:51], a[40:43], v[96:99], v[48:51]\n"
"s_cbranch_vccnz .Lmk_p88s\n"
".Lmk_p88r:\n"
"ds_read_b128 v[144:147], v9 offset:128\n"
"v_mfma_f32_16x16x32_bf16 v[48:51], a[36:39], v[88:91], v[48:51]\n"
"ds_write_b128 v3, v[44:47] offset:12288\n"
"v_mfma_f32_16x16x32_bf16 v[48:51], a[32:35], v[84:87], v[48:51]\n"
"buffer_load_dwordx4 v[44:47], v0, s[4:7], s98 offen offset:1024 sc1\n"
"v_mfma_f32_16x16x32_bf16 v[152:155], a[64:67], v[84:87], 0\n"
"buffer_load_dwordx4 a[148:151], v1, s[4:7], s96 offen offset:1024 sc1\n"
"v_mfma_f32_16x16x32_bf16 v[152:155], a[68:71], v[88:91], v[152:155]\n"
"v_rcp_f32_e32 v255, v253\n"
"v_mfma_f32_16x16x32_bf16 v[152:155], a[72:75], v[96:99], v[152:155]\n"
"ds_read_b128 v[156:159], v5\n"
"v_mul_f32_e32 v253, 0x3e8293ee, v255\n"
"v_mfma_f32_16x16x32_bf16 v[152:155], a[76:79], v[116:119], v[152:155]\n"
"ds_read_b128 v[176:179], v4 offset:1024\n"
"ds_read_b128 v[184:187], v9 offset:192\n"
"v_mfma_f32_16x16x32_bf16 v[152:155], a[80:83], v[92:95], v[152:155]\n"
"buffer_load_dwordx4 v[188:191], v0, s[4:7], s98 offen offset:2048 sc1\n"
"v_mfma_f32_16x16x32_bf16 v[152:155], a[84:87], v[172:175], v[152:155]\n"
"ds_write_b128 v3, v[48:51] offset:13312\n"
"v_mfma_f32_16x16x32_bf16 v[152:155], a[88:91], v[80:83], v[152:155]\n"
"ds_read_b128 v[48:51], v5 offset:1024\n"
"v_mfma_f32_16x16x32_bf16 v[152:155], a[92:95], v[160:163], v[152:155]\n"
"s_waitcnt lgkmcnt(4)\n"
"v_fmac_f32_e32 v104, v254, v144\n"
"v_fmac_f32_e32 v105, v254, v145\n"
"v_mfma_f32_16x16x32_bf16 v[192:195], a[124:127], v[160:163], 0\n"
"v_fmac_f32_e32 v106, v254, v146\n"
"v_fmac_f32_e32 v107, v254, v147\n"
"v_mfma_f32_16x16x32_bf16 v[192:195], a[120:123], v[80:83], v[192:195]\n"
"buffer_load_dwordx4 v[144:147], v0, s[4:7], s98 offen offset:3072 sc1\n"
"v_mfma_f32_16x16x32_bf16 v[192:195], a[116:119], v[172:175], v[192:195]\n"
"ds_read_b128 v[196:199], v4 offset:2048\n"
"ds_read_b128 v[200:203], v9 offset:256\n"
"v_mfma_f32_16x16x32_bf16 v[192:195], a[112:115], v[92:95], v[192:195]\n"
"ds_read_b128 v[204:207], v5 offset:2048\n"
"v_add_f32_e32 v104, v104, v156\n"
"v_mfma_f32_16x16x32_bf16 v[192:195], a[108:111], v[116:119], v[192:195]\n"
"v_add_f32_e32 v105, v105, v157\n"
"v_add_f32_e32 v106, v106, v158\n"
"v_mfma_f32_16x16x32_bf16 v[192:195], a[104:107], v[96:99], v[192:195]\n"
"ds_write_b128 v3, v[152:155] offset:14336\n"
"v_mfma_f32_16x16x32_bf16 v[192:195], a[100:103], v[88:91], v[192:195]\n"
"buffer_load_dwordx4 v[152:155], v1, s[4:7], s98 offen sc1\n"
"v_mfma_f32_16x16x32_bf16 v[192:195], a[96:99], v[84:87], v[192:195]\n"
"s_waitcnt lgkmcnt(4)\n"
"v_add_f32_e32 v107, v107, v159\n"
"v_fmac_f32_e32 v176, v254, v184\n"
"v_mfma_f32_16x16x32_bf16 v[156:159], v[28:31], v[84:87], 0\n"
"v_fmac_f32_e32 v177, v254, v185\n"
"v_fmac_f32_e32 v178, v254, v186\n"
"v_mfma_f32_16x16x32_bf16 v[156:159], v[40:43], v[88:91], v[156:159]\n"
"ds_read_b128 v[208:211], v4 offset:3072\n"
"ds_read_b128 v[212:215], v9 offset:320\n"
"v_mfma_f32_16x16x32_bf16 v[156:159], v[24:27], v[96:99], v[156:159]\n"
"ds_read_b128 v[216:219], v5 offset:3072\n"
"v_fmac_f32_e32 v179, v254, v187\n"
"v_mfma_f32_16x16x32_bf16 v[156:159], v[72:75], v[116:119], v[156:159]\n"
"buffer_load_dwordx4 v[184:187], v1, s[4:7], s98 offen offset:1024 sc1\n"
"v_mfma_f32_16x16x32_bf16 v[156:159], v[32:35], v[92:95], v[156:159]\n"
"buffer_load_dwordx4 a[152:155], v1, s[4:7], s96 offen offset:2048 sc1\n"
"v_mfma_f32_16x16x32_bf16 v[156:159], v[64:67], v[172:175], v[156:159]\n"
"ds_write_b128 v3, v[192:195] offset:15360\n"
"v_mfma_f32_16x16x32_bf16 v[156:159], v[60:63], v[80:83], v[156:159]\n"
"v_add_f32_e32 v176, v176, v48\n"
"v_add_f32_e32 v177, v177, v49\n"
"v_mfma_f32_16x16x32_bf16 v[156:159], v[36:39], v[160:163], v[156:159]\n"
"s_waitcnt lgkmcnt(5)\n"
"v_add_f32_e32 v178, v178, v50\n"
"v_add_f32_e32 v179, v179, v51\n"
"v_mfma_f32_16x16x32_bf16 v[48:51], v[100:103], v[160:163], 0\n"
"buffer_load_dwordx4 v[160:163], v1, s[4:7], s98 offen offset:2048 sc1\n"
"v_mfma_f32_16x16x32_bf16 v[48:51], v[76:79], v[80:83], v[48:51]\n"
"ds_read_b128 v[80:83], v4 offset:4096\n"
"ds_read_b128 v[192:195], v9\n"
"v_mfma_f32_16x16x32_bf16 v[48:51], v[140:143], v[172:175], v[48:51]\n"
"ds_read_b128 v[172:175], v5 offset:4096\n"
"v_fmac_f32_e32 v196, v254, v200\n"
"v_mfma_f32_16x16x32_bf16 v[48:51], v[136:139], v[92:95], v[48:51]\n"
"v_fmac_f32_e32 v197, v254, v201\n"
"v_fmac_f32_e32 v198, v254, v202\n"
"v_mfma_f32_16x16x32_bf16 v[48:51], v[56:59], v[116:119], v[48:51]\n"
"v_fmac_f32_e32 v199, v254, v203\n"
"v_add_f32_e32 v196, v196, v204\n"
"v_mfma_f32_16x16x32_bf16 v[48:51], v[112:115], v[96:99], v[48:51]\n"
"buffer_load_dwordx4 v[92:95], v1, s[4:7], s98 offen offset:3072 sc1\n"
"v_mfma_f32_16x16x32_bf16 v[48:51], v[108:111], v[88:91], v[48:51]\n"
"ds_write_b128 v3, v[156:159] offset:16384\n"
"v_mfma_f32_16x16x32_bf16 v[48:51], v[120:123], v[84:87], v[48:51]\n"
"s_waitcnt vmcnt(18)\n"
"ds_read_b128 v[84:87], v4 offset:5120\n"
"ds_read_b128 v[88:91], v9 offset:64\n"
"; @A G1 1.3\n"
"v_mfma_f32_16x16x32_bf16 v[96:99], a[0:3], v[68:71], v[104:107]\n"
"ds_read_b128 v[116:119], v5 offset:5120\n"
"v_add_f32_e32 v197, v197, v205\n"
"v_mfma_f32_16x16x32_bf16 v[96:99], a[4:7], v[124:127], v[96:99]\n"
"s_waitcnt lgkmcnt(8)\n"
"v_add_f32_e32 v198, v198, v206\n"
"v_add_f32_e32 v199, v199, v207\n"
"v_mfma_f32_16x16x32_bf16 v[96:99], a[8:11], v[132:135], v[96:99]\n"
"s_add_u32 s98, s53, 0x8000\n"
"buffer_load_dwordx4 v[156:159], v0, s[4:7], s98 offen sc1\n"
"v_mfma_f32_16x16x32_bf16 v[96:99], a[12:15], v[148:151], v[96:99]\n"
"s_waitcnt vmcnt(13)\n"
"v_fmac_f32_e32 v208, v254, v212\n"
"v_fmac_f32_e32 v209, v254, v213\n"
"v_mfma_f32_16x16x32_bf16 v[96:99], a[16:19], v[128:131], v[96:99]\n"
"v_fmac_f32_e32 v210, v254, v214\n"
"v_fmac_f32_e32 v211, v254, v215\n"
"v_mfma_f32_16x16x32_bf16 v[96:99], a[20:23], v[164:167], v[96:99]\n"
"ds_write_b128 v3, v[48:51] offset:17408\n"
"v_mfma_f32_16x16x32_bf16 v[96:99], a[24:27], v[168:171], v[96:99]\n"
"v_add_f32_e32 v208, v208, v216\n"
"v_add_f32_e32 v209, v209, v217\n"
"v_mfma_f32_16x16x32_bf16 v[96:99], a[28:31], v[180:183], v[96:99]\n"
"buffer_load_dwordx4 v[48:51], v0, s[4:7], s98 offen offset:1024 sc1\n"
"v_mfma_f32_16x16x32_bf16 v[200:203], a[60:63], v[180:183], v[176:179]\n"
"buffer_load_dwordx4 a[156:159], v1, s[4:7], s96 offen offset:3072 sc1\n"
"v_mfma_f32_16x16x32_bf16 v[200:203], a[56:59], v[168:171], v[200:203]\n"
"s_waitcnt lgkmcnt(5)\n"
"v_add_f32_e32 v210, v210, v218\n"
"v_add_f32_e32 v211, v211, v219\n"
"v_mfma_f32_16x16x32_bf16 v[200:203], a[52:55], v[164:167], v[200:203]\n"
"s_waitcnt lgkmcnt(0)\n"
"s_barrier\n"
"v_mfma_f32_16x16x32_bf16 v[200:203], a[48:51], v[128:131], v[200:203]\n"
"ds_read_b128 v[204:207], v6\n"
"ds_read_b128 v[212:215], v6 offset:1024\n"
"v_mfma_f32_16x16x32_bf16 v[200:203], a[44:47], v[148:151], v[200:203]\n"
"v_cmp_u_f32_e32 vcc, v96, v96\n"
"v_fmac_f32_e32 v80, v254, v192\n"
"v_mfma_f32_16x16x32_bf16 v[200:203], a[40:43], v[132:135], v[200:203]\n"
"s_cbranch_vccnz .Lmk_p89s\n"
".Lmk_p89r:\n"
"v_fmac_f32_e32 v81, v254, v193\n"
"v_mfma_f32_16x16x32_bf16 v[200:203], a[36:39], v[124:127], v[200:203]\n"
"buffer_load_dwordx4 v[104:107], v0, s[4:7], s98 offen offset:2048 sc1\n"
"v_mfma_f32_16x16x32_bf16 v[200:203], a[32:35], v[68:71], v[200:203]\n"
"v_fmac_f32_e32 v82, v254, v194\n"
"v_fmac_f32_e32 v83, v254, v195\n"
"v_mfma_f32_16x16x32_bf16 v[196:199], a[64:67], v[68:71], v[196:199]\n"
"v_add_f32_e32 v80, v80, v172\n"
"v_add_f32_e32 v81, v81, v173\n"
"v_mfma_f32_16x16x32_bf16 v[196:199], a[68:71], v[124:127], v[196:199]\n"
"buffer_load_dwordx4 v[176:179], v0, s[4:7], s98 offen offset:3072 sc1\n"
"v_mfma_f32_16x16x32_bf16 v[196:199], a[72:75], v[132:135], v[196:199]\n"
"v_add_f32_e32 v82, v82, v174\n"
"v_add_f32_e32 v83, v83, v175\n"
"v_mfma_f32_16x16x32_bf16 v[196:199], a[76:79], v[148:151], v[196:199]\n"
"v_fmac_f32_e32 v84, v254, v88\n"
"v_fmac_f32_e32 v85, v254, v89\n"
"v_mfma_f32_16x16x32_bf16 v[196:199], a[80:83], v[128:131], v[196:199]\n"
"s_waitcnt lgkmcnt(0)\n"
"v_fmac_f32_e32 v86, v254, v90\n"
"v_fmac_f32_e32 v87, v254, v91\n"
"v_mfma_f32_16x16x32_bf16 v[196:199], a[84:87], v[164:167], v[196:199]\n"
"v_add_f32_e32 v84, v84, v116\n"
"v_add_f32_e32 v85, v85, v117\n"
"v_mfma_f32_16x16x32_bf16 v[196:199], a[88:91], v[168:171], v[196:199]\n"
"buffer_load_dwordx4 v[88:91], v1, s[4:7], s98 offen sc1\n"
"v_mfma_f32_16x16x32_bf16 v[196:199], a[92:95], v[180:183], v[196:199]\n"
"ds_read_b128 v[172:175], v6 offset:2048\n"
"ds_read_b128 v[192:195], v6 offset:3072\n"
"v_mfma_f32_16x16x32_bf16 v[208:211], a[124:127], v[180:183], v[208:211]\n"
"v_add_f32_e32 v86, v86, v118\n"
"v_add_f32_e32 v87, v87, v119\n"
"v_mfma_f32_16x16x32_bf16 v[208:211], a[120:123], v[168:171], v[208:211]\n"
"v_add_f32_e32 v96, v96, v204\n"
"v_add_f32_e32 v97, v97, v205\n"
"v_mfma_f32_16x16x32_bf16 v[208:211], a[116:119], v[164:167], v[208:211]\n"
"s_add_u32 s97, s70, 0x2000\n"
"v_add_f32_e32 v98, v98, v206\n"
"v_add_f32_e32 v99, v99, v207\n"
"v_mfma_f32_16x16x32_bf16 v[208:211], a[112:115], v[128:131], v[208:211]\n"
"buffer_load_dwordx4 v[116:119], v1, s[4:7], s98 offen offset:1024 sc1\n"
"v_mfma_f32_16x16x32_bf16 v[208:211], a[108:111], v[148:151], v[208:211]\n"
"buffer_load_dwordx4 a[160:163], v0, s[4:7], s97 offen sc1\n"
"v_mfma_f32_16x16x32_bf16 v[208:211], a[104:107], v[132:135], v[208:211]\n"
"v_add_f32_e32 v200, v200, v212\n"
"v_add_f32_e32 v201, v201, v213\n"
"v_mfma_f32_16x16x32_bf16 v[208:211], a[100:103], v[124:127], v[208:211]\n"
"v_add_f32_e32 v202, v202, v214\n"
"v_add_f32_e32 v203, v203, v215\n"
"v_mfma_f32_16x16x32_bf16 v[208:211], a[96:99], v[68:71], v[208:211]\n"
"v_mul_f32_e32 v96, v96, v255\n"
"v_mul_f32_e32 v97, v97, v255\n"
"v_mfma_f32_16x16x32_bf16 v[80:83], v[28:31], v[68:71], v[80:83]\n"
"buffer_load_dwordx4 v[204:207], v1, s[4:7], s98 offen offset:2048 sc1\n"
"v_mfma_f32_16x16x32_bf16 v[80:83], v[40:43], v[124:127], v[80:83]\n"
"v_mul_f32_e32 v98, v98, v255\n"
"v_mul_f32_e32 v99, v99, v255\n"
"v_mfma_f32_16x16x32_bf16 v[80:83], v[24:27], v[132:135], v[80:83]\n"
"v_mul_f32_e32 v200, v200, v255\n"
"v_mul_f32_e32 v201, v201, v255\n"
"v_mfma_f32_16x16x32_bf16 v[80:83], v[72:75], v[148:151], v[80:83]\n"
"v_mul_f32_e32 v202, v202, v255\n"
"v_mul_f32_e32 v203, v203, v255\n"
"v_mfma_f32_16x16x32_bf16 v[80:83], v[32:35], v[128:131], v[80:83]\n"
"s_waitcnt lgkmcnt(0)\n"
"v_cvt_pk_bf16_f32 v212, v96, v97\n"
"v_cvt_pk_bf16_f32 v213, v98, v99\n"
"v_mfma_f32_16x16x32_bf16 v[80:83], v[64:67], v[164:167], v[80:83]\n"
"buffer_load_dwordx4 v[96:99], v1, s[4:7], s98 offen offset:3072 sc1\n"
"v_mfma_f32_16x16x32_bf16 v[80:83], v[60:63], v[168:171], v[80:83]\n"
"ds_read_b128 v[216:219], v6 offset:4096\n"
"ds_read_b128 v[220:223], v6 offset:5120\n"
"v_mfma_f32_16x16x32_bf16 v[80:83], v[36:39], v[180:183], v[80:83]\n"
"v_cvt_pk_bf16_f32 v214, v200, v201\n"
"v_cvt_pk_bf16_f32 v215, v202, v203\n"
"v_mfma_f32_16x16x32_bf16 v[84:87], v[100:103], v[180:183], v[84:87]\n"
"ds_write_b128 v8, v[212:215] offset:25600\n"
"v_mfma_f32_16x16x32_bf16 v[84:87], v[76:79], v[168:171], v[84:87]\n"
"v_add_f32_e32 v196, v196, v172\n"
"v_add_f32_e32 v197, v197, v173\n"
"v_mfma_f32_16x16x32_bf16 v[84:87], v[140:143], v[164:167], v[84:87]\n"
"v_add_f32_e32 v198, v198, v174\n"
"v_add_f32_e32 v199, v199, v175\n"
"v_mfma_f32_16x16x32_bf16 v[84:87], v[136:139], v[128:131], v[84:87]\n"
"v_add_f32_e32 v208, v208, v192\n"
"v_add_f32_e32 v209, v209, v193\n"
"v_mfma_f32_16x16x32_bf16 v[84:87], v[56:59], v[148:151], v[84:87]\n"
"v_add_f32_e32 v210, v210, v194\n"
"v_add_f32_e32 v211, v211, v195\n"
"v_mfma_f32_16x16x32_bf16 v[84:87], v[112:115], v[132:135], v[84:87]\n"
"v_mul_f32_e32 v196, v196, v255\n"
"v_mul_f32_e32 v197, v197, v255\n"
"v_mfma_f32_16x16x32_bf16 v[84:87], v[108:111], v[124:127], v[84:87]\n"
"v_mul_f32_e32 v198, v198, v255\n"
"v_mul_f32_e32 v199, v199, v255\n"
"v_mfma_f32_16x16x32_bf16 v[84:87], v[120:123], v[68:71], v[84:87]\n"
"v_mul_f32_e32 v208, v208, v255\n"
"v_mul_f32_e32 v209, v209, v255\n"
"v_mul_f32_e32 v210, v210, v255\n"
"v_mul_f32_e32 v211, v211, v255\n"
"v_cvt_pk_bf16_f32 v68, v196, v197\n"
"v_cvt_pk_bf16_f32 v69, v198, v199\n"
"v_cvt_pk_bf16_f32 v70, v208, v209\n"
"v_cvt_pk_bf16_f32 v71, v210, v211\n"
"ds_write_b128 v7, v[68:71] offset:58368\n"
".Lmk_a2:\n"
"s_waitcnt vmcnt(15)\n"
"; @A G1 2.0\n"
"v_mfma_f32_16x16x32_bf16 v[124:127], a[0:3], v[52:55], 0\n"
"v_mfma_f32_16x16x32_bf16 v[124:127], a[4:7], v[44:47], v[124:127]\n"
"s_add_u32 s98, s62, 0x800\n"
"v_mfma_f32_16x16x32_bf16 v[124:127], a[8:11], v[188:191], v[124:127]\n"
"s_add_u32 s96, s54, 0x8000\n"
"buffer_load_dwordx4 v[128:131], v0, s[4:7], s96 offen sc1\n"
"v_mfma_f32_16x16x32_bf16 v[124:127], a[12:15], v[144:147], v[124:127]\n"
"s_waitcnt vmcnt(11)\n"
"v_mfma_f32_16x16x32_bf16 v[124:127], a[16:19], v[152:155], v[124:127]\n"
"buffer_load_dword v255, v10, s[4:7], s98 offen sc1\n"
"v_mfma_f32_16x16x32_bf16 v[124:127], a[20:23], v[184:187], v[124:127]\n"
"s_waitcnt lgkmcnt(2)\n"
"v_add_f32_e32 v80, v80, v216\n"
"v_add_f32_e32 v81, v81, v217\n"
"v_mfma_f32_16x16x32_bf16 v[124:127], a[24:27], v[160:163], v[124:127]\n"
"v_add_f32_e32 v82, v82, v218\n"
"v_add_f32_e32 v83, v83, v219\n"
"v_mfma_f32_16x16x32_bf16 v[124:127], a[28:31], v[92:95], v[124:127]\n"
"buffer_load_dwordx4 v[132:135], v0, s[4:7], s96 offen offset:1024 sc1\n"
"v_mfma_f32_16x16x32_bf16 v[148:151], a[60:63], v[92:95], 0\n"
"buffer_load_dwordx4 a[164:167], v0, s[4:7], s97 offen offset:1024 sc1\n"
"v_mfma_f32_16x16x32_bf16 v[148:151], a[56:59], v[160:163], v[148:151]\n"
"buffer_load_dword v254, v10, s[4:7], s98 offen offset:256 sc1\n"
"v_mfma_f32_16x16x32_bf16 v[148:151], a[52:55], v[184:187], v[148:151]\n"
"v_add_f32_e32 v84, v84, v220\n"
"v_add_f32_e32 v85, v85, v221\n"
"v_mfma_f32_16x16x32_bf16 v[148:151], a[48:51], v[152:155], v[148:151]\n"
"v_add_f32_e32 v86, v86, v222\n"
"v_add_f32_e32 v87, v87, v223\n"
"v_mfma_f32_16x16x32_bf16 v[148:151], a[44:47], v[144:147], v[148:151]\n"
"buffer_load_dwordx4 v[164:167], v0, s[4:7], s96 offen offset:2048 sc1\n"
"v_mfma_f32_16x16x32_bf16 v[148:151], a[40:43], v[188:191], v[148:151]\n"
"ds_write_b128 v3, v[124:127]\n"
"v_mfma_f32_16x16x32_bf16 v[148:151], a[36:39], v[44:47], v[148:151]\n"
"buffer_load_dword v252, v10, s[4:7], s98 offen offset:512 sc1\n"
"v_mfma_f32_16x16x32_bf16 v[148:151], a[32:35], v[52:55], v[148:151]\n"
"v_mul_f32_e32 v80, v80, v253\n"
"v_mul_f32_e32 v81, v81, v253\n"
"v_mfma_f32_16x16x32_bf16 v[124:127], a[64:67], v[52:55], 0\n"
"v_mul_f32_e32 v82, v82, v253\n"
"v_mul_f32_e32 v83, v83, v253\n"
"v_mfma_f32_16x16x32_bf16 v[124:127], a[68:71], v[44:47], v[124:127]\n"
"buffer_load_dwordx4 v[168:171], v0, s[4:7], s96 offen offset:3072 sc1\n"
"v_mfma_f32_16x16x32_bf16 v[124:127], a[72:75], v[188:191], v[124:127]\n"
"buffer_load_dword v251, v10, s[4:7], s98 offen offset:768 sc1\n"
"v_mfma_f32_16x16x32_bf16 v[124:127], a[76:79], v[144:147], v[124:127]\n"
"v_mul_f32_e32 v84, v84, v253\n"
"v_mul_f32_e32 v85, v85, v253\n"
"v_mfma_f32_16x16x32_bf16 v[124:127], a[80:83], v[152:155], v[124:127]\n"
"v_mul_f32_e32 v86, v86, v253\n"
"v_mul_f32_e32 v87, v87, v253\n"
"v_mfma_f32_16x16x32_bf16 v[124:127], a[84:87], v[184:187], v[124:127]\n"
"ds_write_b128 v3, v[148:151] offset:1024\n"
"v_mfma_f32_16x16x32_bf16 v[124:127], a[88:91], v[160:163], v[124:127]\n"
"buffer_load_dwordx4 v[148:151], v1, s[4:7], s96 offen sc1\n"
"v_mfma_f32_16x16x32_bf16 v[124:127], a[92:95], v[92:95], v[124:127]\n"
"buffer_load_dword v253, v10, s[4:7], s98 offen offset:1024 sc1\n"
"v_mfma_f32_16x16x32_bf16 v[172:175], a[124:127], v[92:95], 0\n"
"v_cvt_pk_bf16_f32 v180, v80, v81\n"
"v_cvt_pk_bf16_f32 v181, v82, v83\n"
"v_mfma_f32_16x16x32_bf16 v[172:175], a[120:123], v[160:163], v[172:175]\n"
"v_cvt_pk_bf16_f32 v182, v84, v85\n"
"v_cvt_pk_bf16_f32 v183, v86, v87\n"
"v_mfma_f32_16x16x32_bf16 v[172:175], a[116:119], v[184:187], v[172:175]\n"
"buffer_load_dword v250, v10, s[4:7], s98 offen offset:1280 sc1\n"
"v_mfma_f32_16x16x32_bf16 v[172:175], a[112:115], v[152:155], v[172:175]\n"
"buffer_load_dwordx4 v[80:83], v1, s[4:7], s96 offen offset:1024 sc1\n"
"v_mfma_f32_16x16x32_bf16 v[172:175], a[108:111], v[144:147], v[172:175]\n"
"buffer_load_dwordx4 a[168:171], v0, s[4:7], s97 offen offset:2048 sc1\n"
"v_mfma_f32_16x16x32_bf16 v[172:175], a[104:107], v[188:191], v[172:175]\n"
"ds_write_b128 v3, v[124:127] offset:2048\n"
"v_mfma_f32_16x16x32_bf16 v[172:175], a[100:103], v[44:47], v[172:175]\n"
"buffer_load_dword v249, v10, s[4:7], s98 offen offset:1536 sc1\n"
"v_mfma_f32_16x16x32_bf16 v[172:175], a[96:99], v[52:55], v[172:175]\n"
"ds_write_b128 v2, v[180:183] offset:50176\n"
"v_mfma_f32_16x16x32_bf16 v[84:87], v[28:31], v[52:55], 0\n"
"buffer_load_dwordx4 v[124:127], v1, s[4:7], s96 offen offset:2048 sc1\n"
"s_add_u32 s95, s64, 0x2000\n"
"v_mfma_f32_16x16x32_bf16 v[84:87], v[40:43], v[44:47], v[84:87]\n"
"buffer_load_dword v248, v10, s[4:7], s98 offen offset:1792 sc1\n"
"v_mfma_f32_16x16x32_bf16 v[84:87], v[24:27], v[188:191], v[84:87]\n"
"buffer_store_dwordx4 v[212:215], v8, s[4:7], s95 offen offset:1024 sc1\n"
"v_mfma_f32_16x16x32_bf16 v[84:87], v[72:75], v[144:147], v[84:87]\n"
"v_mfma_f32_16x16x32_bf16 v[84:87], v[32:35], v[152:155], v[84:87]\n"
"v_mfma_f32_16x16x32_bf16 v[84:87], v[64:67], v[184:187], v[84:87]\n"
"buffer_load_dwordx4 v[180:183], v1, s[4:7], s96 offen offset:3072 sc1\n"
"v_mfma_f32_16x16x32_bf16 v[84:87], v[60:63], v[160:163], v[84:87]\n"
"ds_write_b128 v3, v[172:175] offset:3072\n"
"v_mfma_f32_16x16x32_bf16 v[84:87], v[36:39], v[92:95], v[84:87]\n"
"v_mfma_f32_16x16x32_bf16 v[172:175], v[100:103], v[92:95], 0\n"
"v_mfma_f32_16x16x32_bf16 v[172:175], v[76:79], v[160:163], v[172:175]\n"
"v_mfma_f32_16x16x32_bf16 v[172:175], v[140:143], v[184:187], v[172:175]\n"
"v_mfma_f32_16x16x32_bf16 v[172:175], v[136:139], v[152:155], v[172:175]\n"
"v_mfma_f32_16x16x32_bf16 v[172:175], v[56:59], v[144:147], v[172:175]\n"
"v_mfma_f32_16x16x32_bf16 v[172:175], v[112:115], v[188:191], v[172:175]\n"
"s_nop 1\n"
"ds_write_b128 v3, v[84:87] offset:4096\n"
"v_mfma_f32_16x16x32_bf16 v[172:175], v[108:111], v[44:47], v[172:175]\n"
"v_mfma_f32_16x16x32_bf16 v[172:175], v[120:123], v[52:55], v[172:175]\n"
"s_waitcnt vmcnt(24)\n"
"; @A G1 2.1\n"
"v_mfma_f32_16x16x32_bf16 v[44:47], a[0:3], v[156:159], 0\n"
"buffer_load_dwordx4 a[172:175], v0, s[4:7], s97 offen offset:3072 sc1\n"
"v_mfma_f32_16x16x32_bf16 v[44:47], a[4:7], v[48:51], v[44:47]\n"
"v_mfma_f32_16x16x32_bf16 v[44:47], a[8:11], v[104:107], v[44:47]\n"
"s_add_u32 s98, s55, 0x8000\n"
"buffer_load_dwordx4 v[52:55], v0, s[4:7], s98 offen sc1\n"
"v_mfma_f32_16x16x32_bf16 v[44:47], a[12:15], v[176:179], v[44:47]\n"
"s_waitcnt vmcnt(21)\n"
"v_mfma_f32_16x16x32_bf16 v[44:47], a[16:19], v[88:91], v[44:47]\n"
"v_mfma_f32_16x16x32_bf16 v[44:47], a[20:23], v[116:119], v[44:47]\n"
"ds_write_b128 v3, v[172:175] offset:5120\n"
"v_mfma_f32_16x16x32_bf16 v[44:47], a[24:27], v[204:207], v[44:47]\n"
"v_mfma_f32_16x16x32_bf16 v[44:47], a[28:31], v[96:99], v[44:47]\n"
"buffer_load_dwordx4 v[84:87], v0, s[4:7], s98 offen offset:1024 sc1\n"
"v_mfma_f32_16x16x32_bf16 v[92:95], a[60:63], v[96:99], 0\n"
"s_add_u32 s96, s64, 0x6000\n"
"v_mfma_f32_16x16x32_bf16 v[92:95], a[56:59], v[204:207], v[92:95]\n"
"v_mfma_f32_16x16x32_bf16 v[92:95], a[52:55], v[116:119], v[92:95]\n"
"buffer_store_dwordx4 v[68:71], v7, s[4:7], s96 offen offset:1024 sc1\n"
"v_mfma_f32_16x16x32_bf16 v[92:95], a[48:51], v[88:91], v[92:95]\n"
"v_mfma_f32_16x16x32_bf16 v[92:95], a[44:47], v[176:179], v[92:95]\n"
"buffer_load_dwordx4 v[68:71], v0, s[4:7], s98 offen offset:2048 sc1\n"
"v_mfma_f32_16x16x32_bf16 v[92:95], a[40:43], v[104:107], v[92:95]\n"
"ds_write_b128 v3, v[44:47] offset:6144\n"
"v_mfma_f32_16x16x32_bf16 v[92:95], a[36:39], v[48:51], v[92:95]\n"
"v_mfma_f32_16x16x32_bf16 v[92:95], a[32:35], v[156:159], v[92:95]\n"
"v_mfma_f32_16x16x32_bf16 v[44:47], a[64:67], v[156:159], 0\n"
"v_mfma_f32_16x16x32_bf16 v[44:47], a[68:71], v[48:51], v[44:47]\n"
"buffer_load_dwordx4 v[144:147], v0, s[4:7], s98 offen offset:3072 sc1\n"
"v_mfma_f32_16x16x32_bf16 v[44:47], a[72:75], v[104:107], v[44:47]\n"
"v_mfma_f32_16x16x32_bf16 v[44:47], a[76:79], v[176:179], v[44:47]\n"
"v_mfma_f32_16x16x32_bf16 v[44:47], a[80:83], v[88:91], v[44:47]\n"
"buffer_load_dwordx4 a[176:179], v1, s[4:7], s97 offen sc1\n"
"v_mfma_f32_16x16x32_bf16 v[44:47], a[84:87], v[116:119], v[44:47]\n"
"ds_write_b128 v3, v[92:95] offset:7168\n"
"v_mfma_f32_16x16x32_bf16 v[44:47], a[88:91], v[204:207], v[44:47]\n"
"buffer_load_dwordx4 v[92:95], v1, s[4:7], s98 offen sc1\n"
"v_mfma_f32_16x16x32_bf16 v[44:47], a[92:95], v[96:99], v[44:47]\n"
"v_mfma_f32_16x16x32_bf16 v[152:155], a[124:127], v[96:99], 0\n"
"v_mfma_f32_16x16x32_bf16 v[152:155], a[120:123], v[204:207], v[152:155]\n"
"v_mfma_f32_16x16x32_bf16 v[152:155], a[116:119], v[116:119], v[152:155]\n"
"v_mfma_f32_16x16x32_bf16 v[152:155], a[112:115], v[88:91], v[152:155]\n"
"buffer_load_dwordx4 v[160:163], v1, s[4:7], s98 offen offset:1024 sc1\n"
"v_mfma_f32_16x16x32_bf16 v[152:155], a[108:111], v[176:179], v[152:155]\n"
"v_mfma_f32_16x16x32_bf16 v[152:155], a[104:107], v[104:107], v[152:155]\n"
"s_nop 0\n"
"ds_write_b128 v3, v[44:47] offset:8192\n"
"v_mfma_f32_16x16x32_bf16 v[152:155], a[100:103], v[48:51], v[152:155]\n"
"v_mfma_f32_16x16x32_bf16 v[152:155], a[96:99], v[156:159], v[152:155]\n"
"v_mfma_f32_16x16x32_bf16 v[44:47], v[28:31], v[156:159], 0\n"
"buffer_load_dwordx4 v[172:175], v1, s[4:7], s98 offen offset:2048 sc1\n"
"v_mfma_f32_16x16x32_bf16 v[44:47], v[40:43], v[48:51], v[44:47]\n"
"v_mfma_f32_16x16x32_bf16 v[44:47], v[24:27], v[104:107], v[44:47]\n"
"v_mfma_f32_16x16x32_bf16 v[44:47], v[72:75], v[176:179], v[44:47]\n"
"v_mfma_f32_16x16x32_bf16 v[44:47], v[32:35], v[88:91], v[44:47]\n"
"s_waitcnt vmcnt(12)\n"
"v_add_f32_e32 v255, v255, v253\n"
"v_add_f32_e32 v254, v254, v250\n"
"v_mfma_f32_16x16x32_bf16 v[44:47], v[64:67], v[116:119], v[44:47]\n"
"buffer_load_dwordx4 v[184:187], v1, s[4:7], s98 offen offset:3072 sc1\n"
"v_mfma_f32_16x16x32_bf16 v[44:47], v[60:63], v[204:207], v[44:47]\n"
"ds_write_b128 v3, v[152:155] offset:9216\n"
"v_mfma_f32_16x16x32_bf16 v[44:47], v[36:39], v[96:99], v[44:47]\n"
"v_add_f32_e32 v252, v252, v249\n"
"v_add_f32_e32 v251, v251, v248\n"
"v_mfma_f32_16x16x32_bf16 v[152:155], v[100:103], v[96:99], 0\n"
"buffer_load_dwordx4 a[180:183], v1, s[4:7], s97 offen offset:1024 sc1\n"
"v_mfma_f32_16x16x32_bf16 v[152:155], v[76:79], v[204:207], v[152:155]\n"
"v_add_f32_e32 v255, v255, v252\n"
"v_add_f32_e32 v254, v254, v251\n"
"v_mfma_f32_16x16x32_bf16 v[152:155], v[140:143], v[116:119], v[152:155]\n"
"v_add_f32_e32 v255, v255, v254\n"
"v_mov_b32_e32 v254, v255\n"
"v_mfma_f32_16x16x32_bf16 v[152:155], v[136:139], v[88:91], v[152:155]\n"
"s_nop 0\n"
"v_permlane32_swap_b32_e32 v255, v254\n"
"v_add_f32_e32 v255, v255, v254\n"
"v_mfma_f32_16x16x32_bf16 v[152:155], v[56:59], v[176:179], v[152:155]\n"
"v_mul_f32_e32 v255, 0x3a800000, v255\n"
"v_mov_b32_e32 v254, v255\n"
"v_mfma_f32_16x16x32_bf16 v[152:155], v[112:115], v[104:107], v[152:155]\n"
"ds_write_b128 v3, v[44:47] offset:10240\n"
"v_mfma_f32_16x16x32_bf16 v[152:155], v[108:111], v[48:51], v[152:155]\n"
"v_permlane16_swap_b32_e32 v255, v254\n"
"v_cvt_pk_bf16_f32 v253, -v255, 0\n"
"v_mfma_f32_16x16x32_bf16 v[152:155], v[120:123], v[156:159], v[152:155]\n"
"v_fma_f32 v254, -v255, v255, v254\n"
"v_lshlrev_b32_e32 v253, 16, v253\n"
"; @A G1 2.2\n"
"v_mfma_f32_16x16x32_bf16 v[44:47], a[0:3], v[128:131], 0\n"
"v_max_f32_e32 v254, 0, v254\n"
"v_cvt_pk_bf16_f32 v254, v254, 0\n"
"v_mfma_f32_16x16x32_bf16 v[44:47], a[4:7], v[132:135], v[44:47]\n"
"v_lshlrev_b32_e32 v254, 16, v254\n"
"v_add_f32_e32 v254, 0x3727c5ac, v254\n"
"v_mfma_f32_16x16x32_bf16 v[44:47], a[8:11], v[164:167], v[44:47]\n"
"v_cvt_pk_bf16_f32 v254, v254, 0\n"
"v_lshlrev_b32_e32 v254, 16, v254\n"
"v_mfma_f32_16x16x32_bf16 v[44:47], a[12:15], v[168:171], v[44:47]\n"
"s_waitcnt vmcnt(12)\n"
"v_sqrt_f32_e32 v254, v254\n"
"v_mfma_f32_16x16x32_bf16 v[44:47], a[16:19], v[148:151], v[44:47]\n"
"s_mov_b32 s98, .Lmk_a4-.Lmk_g91\n"
"s_getpc_b64 s[84:85]\n"
".Lmk_g91:\n"
"s_add_u32 s84, s84, s98\n"
"s_addc_u32 s85, s85, 0\n"
"s_and_b32 s84, s84, -16\n"
"v_mfma_f32_16x16x32_bf16 v[44:47], a[20:23], v[80:83], v[44:47]\n"
"ds_write_b128 v3, v[152:155] offset:11264\n"
"v_mfma_f32_16x16x32_bf16 v[44:47], a[24:27], v[124:127], v[44:47]\n"
"s_and_b32 s85, s85, 0xffff\n"
"s_sub_u32 s98, .Lmk_end-.Lmk_g91, s98\n"
"s_min_u32 s86, s98, 0x4000\n"
"s_mov_b32 s87, 0x20000\n"
"v_mfma_f32_16x16x32_bf16 v[44:47], a[28:31], v[180:183], v[44:47]\n"
"s_mov_b32 m0, 0x27800\n"
"s_nop 0\n"
"buffer_load_dwordx4 v0, s[84:87], s99 offen lds\n"
"v_mfma_f32_16x16x32_bf16 v[48:51], a[60:63], v[180:183], 0\n"
"v_cvt_pk_bf16_f32 v254, v254, 0\n"
"v_lshlrev_b32_e32 v254, 16, v254\n"
"v_mfma_f32_16x16x32_bf16 v[48:51], a[56:59], v[124:127], v[48:51]\n"
"v_rcp_f32_e32 v255, v254\n"
"v_mfma_f32_16x16x32_bf16 v[48:51], a[52:55], v[80:83], v[48:51]\n"
"s_add_u32 s98, s52, 0\n"
"buffer_load_dwordx4 v[88:91], v0, s[4:7], s98 offen sc1\n"
"v_mfma_f32_16x16x32_bf16 v[48:51], a[48:51], v[148:151], v[48:51]\n"
"s_waitcnt lgkmcnt(0)\n"
"s_barrier\n"
"v_mfma_f32_16x16x32_bf16 v[48:51], a[44:47], v[168:171], v[48:51]\n"
"buffer_load_dwordx4 a[184:187], v1, s[4:7], s97 offen offset:2048 sc1\n"
"v_mfma_f32_16x16x32_bf16 v[48:51], a[40:43], v[164:167], v[48:51]\n"
"ds_write_b128 v3, v[44:47] offset:12288\n"
"v_mfma_f32_16x16x32_bf16 v[48:51], a[36:39], v[132:135], v[48:51]\n"
"ds_read_b128 v[44:47], v4\n"
"v_mul_f32_e32 v254, 0x3e8293ee, v255\n"
"v_mfma_f32_16x16x32_bf16 v[48:51], a[32:35], v[128:131], v[48:51]\n"
"buffer_load_dwordx4 v[96:99], v0, s[4:7], s98 offen offset:1024 sc1\n"
"v_mfma_f32_16x16x32_bf16 v[104:107], a[64:67], v[128:131], 0\n"
"ds_read_b128 v[116:119], v9 offset:128\n"
"ds_read_b128 v[152:155], v5\n"
"v_mfma_f32_16x16x32_bf16 v[104:107], a[68:71], v[132:135], v[104:107]\n"
"v_mfma_f32_16x16x32_bf16 v[104:107], a[72:75], v[164:167], v[104:107]\n"
"v_mfma_f32_16x16x32_bf16 v[104:107], a[76:79], v[168:171], v[104:107]\n"
"ds_read_b128 v[156:159], v4 offset:1024\n"
"ds_read_b128 v[176:179], v9 offset:192\n"
"v_mfma_f32_16x16x32_bf16 v[104:107], a[80:83], v[148:151], v[104:107]\n"
"buffer_load_dwordx4 v[188:191], v0, s[4:7], s98 offen offset:2048 sc1\n"
"v_mfma_f32_16x16x32_bf16 v[104:107], a[84:87], v[80:83], v[104:107]\n"
"ds_write_b128 v3, v[48:51] offset:13312\n"
"v_mfma_f32_16x16x32_bf16 v[104:107], a[88:91], v[124:127], v[104:107]\n"
"ds_read_b128 v[48:51], v5 offset:1024\n"
"s_waitcnt lgkmcnt(4)\n"
"v_mfma_f32_16x16x32_bf16 v[104:107], a[92:95], v[180:183], v[104:107]\n"
"v_fmac_f32_e32 v44, v253, v116\n"
"v_fmac_f32_e32 v45, v253, v117\n"
"v_mfma_f32_16x16x32_bf16 v[192:195], a[124:127], v[180:183], 0\n"
"v_fmac_f32_e32 v46, v253, v118\n"
"v_fmac_f32_e32 v47, v253, v119\n"
"v_mfma_f32_16x16x32_bf16 v[192:195], a[120:123], v[124:127], v[192:195]\n"
"buffer_load_dwordx4 v[116:119], v0, s[4:7], s98 offen offset:3072 sc1\n"
"v_mfma_f32_16x16x32_bf16 v[192:195], a[116:119], v[80:83], v[192:195]\n"
"ds_read_b128 v[196:199], v4 offset:2048\n"
"ds_read_b128 v[200:203], v9 offset:256\n"
"v_mfma_f32_16x16x32_bf16 v[192:195], a[112:115], v[148:151], v[192:195]\n"
"ds_read_b128 v[204:207], v5 offset:2048\n"
"v_add_f32_e32 v44, v44, v152\n"
"v_mfma_f32_16x16x32_bf16 v[192:195], a[108:111], v[168:171], v[192:195]\n"
"v_add_f32_e32 v45, v45, v153\n"
"v_add_f32_e32 v46, v46, v154\n"
"v_mfma_f32_16x16x32_bf16 v[192:195], a[104:107], v[164:167], v[192:195]\n"
"ds_write_b128 v3, v[104:107] offset:14336\n"
"v_mfma_f32_16x16x32_bf16 v[192:195], a[100:103], v[132:135], v[192:195]\n"
"buffer_load_dwordx4 v[104:107], v1, s[4:7], s98 offen sc1\n"
"v_mfma_f32_16x16x32_bf16 v[192:195], a[96:99], v[128:131], v[192:195]\n"
"s_waitcnt lgkmcnt(4)\n"
"v_add_f32_e32 v47, v47, v155\n"
"v_fmac_f32_e32 v156, v253, v176\n"
"v_mfma_f32_16x16x32_bf16 v[152:155], v[28:31], v[128:131], 0\n"
"buffer_load_dwordx4 a[188:191], v1, s[4:7], s97 offen offset:3072 sc1\n"
"v_mfma_f32_16x16x32_bf16 v[152:155], v[40:43], v[132:135], v[152:155]\n"
"ds_read_b128 v[208:211], v4 offset:3072\n"
"ds_read_b128 v[212:215], v9 offset:320\n"
"v_mfma_f32_16x16x32_bf16 v[152:155], v[24:27], v[164:167], v[152:155]\n"
"s_waitcnt vmcnt(16)\n"
"s_add_u32 s97, s68, 0x1000\n"
"ds_read_b128 v[216:219], v5 offset:3072\n"
"v_mfma_f32_16x16x32_bf16 v[152:155], v[72:75], v[168:171], v[152:155]\n"
"buffer_load_dwordx4 v[220:223], v1, s[4:7], s98 offen offset:1024 sc1\n"
"v_mfma_f32_16x16x32_bf16 v[152:155], v[32:35], v[148:151], v[152:155]\n"
"buffer_store_dwordx2 v[18:19], v11, s[4:7], s97 offen offset:64 sc1\n"
"v_mfma_f32_16x16x32_bf16 v[152:155], v[64:67], v[80:83], v[152:155]\n"
"ds_write_b128 v3, v[192:195] offset:15360\n"
"v_mfma_f32_16x16x32_bf16 v[152:155], v[60:63], v[124:127], v[152:155]\n"
"v_fmac_f32_e32 v157, v253, v177\n"
"v_fmac_f32_e32 v158, v253, v178\n"
"v_mfma_f32_16x16x32_bf16 v[152:155], v[36:39], v[180:183], v[152:155]\n"
"v_fmac_f32_e32 v159, v253, v179\n"
"v_add_f32_e32 v156, v156, v48\n"
"v_mfma_f32_16x16x32_bf16 v[176:179], v[100:103], v[180:183], 0\n"
"buffer_load_dwordx4 v[180:183], v1, s[4:7], s98 offen offset:2048 sc1\n"
"v_mfma_f32_16x16x32_bf16 v[176:179], v[76:79], v[124:127], v[176:179]\n"
"ds_read_b128 v[124:127], v4 offset:4096\n"
"ds_read_b128 v[192:195], v9\n"
"v_mfma_f32_16x16x32_bf16 v[176:179], v[140:143], v[80:83], v[176:179]\n"
"ds_read_b128 v[80:83], v5 offset:4096\n"
"v_add_f32_e32 v157, v157, v49\n"
"v_mfma_f32_16x16x32_bf16 v[176:179], v[136:139], v[148:151], v[176:179]\n"
"s_waitcnt lgkmcnt(8)\n"
"v_add_f32_e32 v158, v158, v50\n"
"v_add_f32_e32 v159, v159, v51\n"
"v_mfma_f32_16x16x32_bf16 v[176:179], v[56:59], v[168:171], v[176:179]\n"
"v_fmac_f32_e32 v196, v253, v200\n"
"v_fmac_f32_e32 v197, v253, v201\n"
"v_mfma_f32_16x16x32_bf16 v[176:179], v[112:115], v[164:167], v[176:179]\n"
"buffer_load_dwordx4 v[48:51], v1, s[4:7], s98 offen offset:3072 sc1\n"
"v_mfma_f32_16x16x32_bf16 v[176:179], v[108:111], v[132:135], v[176:179]\n"
"ds_write_b128 v3, v[152:155] offset:16384\n"
"v_mfma_f32_16x16x32_bf16 v[176:179], v[120:123], v[128:131], v[176:179]\n"
"s_waitcnt vmcnt(18)\n"
"ds_read_b128 v[128:131], v4 offset:5120\n"
"ds_read_b128 v[132:135], v9 offset:64\n"
"; @A G1 2.3\n"
"v_mfma_f32_16x16x32_bf16 v[44:47], a[0:3], v[52:55], v[44:47]\n"
"ds_read_b128 v[148:151], v5 offset:5120\n"
"v_fmac_f32_e32 v198, v253, v202\n"
"v_mfma_f32_16x16x32_bf16 v[44:47], a[4:7], v[84:87], v[44:47]\n"
"v_fmac_f32_e32 v199, v253, v203\n"
"v_add_f32_e32 v196, v196, v204\n"
"v_mfma_f32_16x16x32_bf16 v[44:47], a[8:11], v[68:71], v[44:47]\n"
"s_add_u32 s98, s53, 0\n"
"buffer_load_dwordx4 v[152:155], v0, s[4:7], s98 offen sc1\n"
"v_mfma_f32_16x16x32_bf16 v[44:47], a[12:15], v[144:147], v[44:47]\n"
"s_waitcnt vmcnt(14)\n"
"s_add_u32 s97, s70, 0x4000\n"
"v_add_f32_e32 v197, v197, v205\n"
"v_mfma_f32_16x16x32_bf16 v[44:47], a[16:19], v[92:95], v[44:47]\n"
"buffer_load_dwordx4 a[192:195], v0, s[4:7], s97 offen sc1\n"
"v_mfma_f32_16x16x32_bf16 v[44:47], a[20:23], v[160:163], v[44:47]\n"
"ds_write_b128 v3, v[176:179] offset:17408\n"
"v_mfma_f32_16x16x32_bf16 v[44:47], a[24:27], v[172:175], v[44:47]\n"
"s_waitcnt lgkmcnt(9)\n"
"v_add_f32_e32 v198, v198, v206\n"
"v_add_f32_e32 v199, v199, v207\n"
"v_mfma_f32_16x16x32_bf16 v[44:47], a[28:31], v[184:187], v[44:47]\n"
"buffer_load_dwordx4 v[164:167], v0, s[4:7], s98 offen offset:1024 sc1\n"
"v_mfma_f32_16x16x32_bf16 v[156:159], a[60:63], v[184:187], v[156:159]\n"
"v_fmac_f32_e32 v208, v253, v212\n"
"v_fmac_f32_e32 v209, v253, v213\n"
"v_mfma_f32_16x16x32_bf16 v[156:159], a[56:59], v[172:175], v[156:159]\n"
"v_fmac_f32_e32 v210, v253, v214\n"
"v_fmac_f32_e32 v211, v253, v215\n"
"v_mfma_f32_16x16x32_bf16 v[156:159], a[52:55], v[160:163], v[156:159]\n"
"s_waitcnt lgkmcnt(0)\n"
"s_barrier\n"
"v_mfma_f32_16x16x32_bf16 v[156:159], a[48:51], v[92:95], v[156:159]\n"
"ds_read_b128 v[168:171], v6\n"
"ds_read_b128 v[176:179], v6 offset:1024\n"
"v_mfma_f32_16x16x32_bf16 v[156:159], a[44:47], v[144:147], v[156:159]\n"
"buffer_load_dwordx4 v[200:203], v0, s[4:7], s98 offen offset:2048 sc1\n"
"v_mfma_f32_16x16x32_bf16 v[156:159], a[40:43], v[68:71], v[156:159]\n"
"v_add_f32_e32 v208, v208, v216\n"
"v_add_f32_e32 v209, v209, v217\n"
"v_mfma_f32_16x16x32_bf16 v[156:159], a[36:39], v[84:87], v[156:159]\n"
"v_add_f32_e32 v210, v210, v218\n"
"v_add_f32_e32 v211, v211, v219\n"
"v_mfma_f32_16x16x32_bf16 v[156:159], a[32:35], v[52:55], v[156:159]\n"
"v_fmac_f32_e32 v124, v253, v192\n"
"v_fmac_f32_e32 v125, v253, v193\n"
"v_mfma_f32_16x16x32_bf16 v[196:199], a[64:67], v[52:55], v[196:199]\n"
"v_fmac_f32_e32 v126, v253, v194\n"
"v_fmac_f32_e32 v127, v253, v195\n"
"v_mfma_f32_16x16x32_bf16 v[196:199], a[68:71], v[84:87], v[196:199]\n"
"buffer_load_dwordx4 v[192:195], v0, s[4:7], s98 offen offset:3072 sc1\n"
"v_mfma_f32_16x16x32_bf16 v[196:199], a[72:75], v[68:71], v[196:199]\n"
"v_add_f32_e32 v124, v124, v80\n"
"v_add_f32_e32 v125, v125, v81\n"
"v_mfma_f32_16x16x32_bf16 v[196:199], a[76:79], v[144:147], v[196:199]\n"
"v_add_f32_e32 v126, v126, v82\n"
"v_add_f32_e32 v127, v127, v83\n"
"v_mfma_f32_16x16x32_bf16 v[196:199], a[80:83], v[92:95], v[196:199]\n"
"s_waitcnt lgkmcnt(0)\n"
"v_fmac_f32_e32 v128, v253, v132\n"
"v_fmac_f32_e32 v129, v253, v133\n"
"v_mfma_f32_16x16x32_bf16 v[196:199], a[84:87], v[160:163], v[196:199]\n"
"v_fmac_f32_e32 v130, v253, v134\n"
"v_fmac_f32_e32 v131, v253, v135\n"
"v_mfma_f32_16x16x32_bf16 v[196:199], a[88:91], v[172:175], v[196:199]\n"
"buffer_load_dwordx4 v[80:83], v1, s[4:7], s98 offen sc1\n"
"v_mfma_f32_16x16x32_bf16 v[196:199], a[92:95], v[184:187], v[196:199]\n"
"ds_read_b128 v[132:135], v6 offset:2048\n"
"ds_read_b128 v[204:207], v6 offset:3072\n"
"v_mfma_f32_16x16x32_bf16 v[208:211], a[124:127], v[184:187], v[208:211]\n"
"buffer_load_dwordx4 a[196:199], v0, s[4:7], s97 offen offset:1024 sc1\n"
"v_mfma_f32_16x16x32_bf16 v[208:211], a[120:123], v[172:175], v[208:211]\n"
"v_add_f32_e32 v128, v128, v148\n"
"v_add_f32_e32 v129, v129, v149\n"
"v_mfma_f32_16x16x32_bf16 v[208:211], a[116:119], v[160:163], v[208:211]\n"
"v_add_f32_e32 v130, v130, v150\n"
"v_add_f32_e32 v131, v131, v151\n"
"v_mfma_f32_16x16x32_bf16 v[208:211], a[112:115], v[92:95], v[208:211]\n"
"buffer_load_dwordx4 v[148:151], v1, s[4:7], s98 offen offset:1024 sc1\n"
"v_mfma_f32_16x16x32_bf16 v[208:211], a[108:111], v[144:147], v[208:211]\n"
"v_add_f32_e32 v44, v44, v168\n"
"v_add_f32_e32 v45, v45, v169\n"
"v_mfma_f32_16x16x32_bf16 v[208:211], a[104:107], v[68:71], v[208:211]\n"
"v_add_f32_e32 v46, v46, v170\n"
"v_add_f32_e32 v47, v47, v171\n"
"v_mfma_f32_16x16x32_bf16 v[208:211], a[100:103], v[84:87], v[208:211]\n"
"v_add_f32_e32 v156, v156, v176\n"
"v_add_f32_e32 v157, v157, v177\n"
"v_mfma_f32_16x16x32_bf16 v[208:211], a[96:99], v[52:55], v[208:211]\n"
"v_add_f32_e32 v158, v158, v178\n"
"v_add_f32_e32 v159, v159, v179\n"
"v_mfma_f32_16x16x32_bf16 v[124:127], v[28:31], v[52:55], v[124:127]\n"
"buffer_load_dwordx4 v[168:171], v1, s[4:7], s98 offen offset:2048 sc1\n"
"v_mfma_f32_16x16x32_bf16 v[124:127], v[40:43], v[84:87], v[124:127]\n"
"v_mul_f32_e32 v44, v44, v255\n"
"v_mul_f32_e32 v45, v45, v255\n"
"v_mfma_f32_16x16x32_bf16 v[124:127], v[24:27], v[68:71], v[124:127]\n"
"v_mul_f32_e32 v46, v46, v255\n"
"v_mul_f32_e32 v47, v47, v255\n"
"v_mfma_f32_16x16x32_bf16 v[124:127], v[72:75], v[144:147], v[124:127]\n"
"v_mul_f32_e32 v156, v156, v255\n"
"v_mul_f32_e32 v157, v157, v255\n"
"v_mfma_f32_16x16x32_bf16 v[124:127], v[32:35], v[92:95], v[124:127]\n"
"s_waitcnt lgkmcnt(0)\n"
"v_mul_f32_e32 v158, v158, v255\n"
"v_mul_f32_e32 v159, v159, v255\n"
"v_mfma_f32_16x16x32_bf16 v[124:127], v[64:67], v[160:163], v[124:127]\n"
"buffer_load_dwordx4 v[176:179], v1, s[4:7], s98 offen offset:3072 sc1\n"
"v_mfma_f32_16x16x32_bf16 v[124:127], v[60:63], v[172:175], v[124:127]\n"
"ds_read_b128 v[212:215], v6 offset:4096\n"
"ds_read_b128 v[216:219], v6 offset:5120\n"
"v_mfma_f32_16x16x32_bf16 v[124:127], v[36:39], v[184:187], v[124:127]\n"
"v_cvt_pk_bf16_f32 v224, v44, v45\n"
"v_cvt_pk_bf16_f32 v225, v46, v47\n"
"v_mfma_f32_16x16x32_bf16 v[128:131], v[100:103], v[184:187], v[128:131]\n"
"v_cvt_pk_bf16_f32 v226, v156, v157\n"
"v_cvt_pk_bf16_f32 v227, v158, v159\n"
"v_mfma_f32_16x16x32_bf16 v[128:131], v[76:79], v[172:175], v[128:131]\n"
"ds_write_b128 v8, v[224:227] offset:17408\n"
"v_mfma_f32_16x16x32_bf16 v[128:131], v[140:143], v[160:163], v[128:131]\n"
"v_add_f32_e32 v196, v196, v132\n"
"v_add_f32_e32 v197, v197, v133\n"
"v_mfma_f32_16x16x32_bf16 v[128:131], v[136:139], v[92:95], v[128:131]\n"
"v_add_f32_e32 v198, v198, v134\n"
"v_add_f32_e32 v199, v199, v135\n"
"v_mfma_f32_16x16x32_bf16 v[128:131], v[56:59], v[144:147], v[128:131]\n"
"v_add_f32_e32 v208, v208, v204\n"
"v_add_f32_e32 v209, v209, v205\n"
"v_mfma_f32_16x16x32_bf16 v[128:131], v[112:115], v[68:71], v[128:131]\n"
"v_add_f32_e32 v210, v210, v206\n"
"v_add_f32_e32 v211, v211, v207\n"
"v_mfma_f32_16x16x32_bf16 v[128:131], v[108:111], v[84:87], v[128:131]\n"
"v_mul_f32_e32 v196, v196, v255\n"
"v_mul_f32_e32 v197, v197, v255\n"
"v_mfma_f32_16x16x32_bf16 v[128:131], v[120:123], v[52:55], v[128:131]\n"
"v_mul_f32_e32 v198, v198, v255\n"
"v_mul_f32_e32 v199, v199, v255\n"
"v_mul_f32_e32 v208, v208, v255\n"
"v_mul_f32_e32 v209, v209, v255\n"
"v_mul_f32_e32 v210, v210, v255\n"
"v_mul_f32_e32 v211, v211, v255\n"
"v_cvt_pk_bf16_f32 v44, v196, v197\n"
"v_cvt_pk_bf16_f32 v45, v198, v199\n"
"v_cvt_pk_bf16_f32 v46, v208, v209\n"
"v_cvt_pk_bf16_f32 v47, v210, v211\n"
"ds_write_b128 v7, v[44:47] offset:50176\n"
".Lmk_a3:\n"
"s_waitcnt vmcnt(16)\n"
"; @A G1 3.0\n"
"v_mfma_f32_16x16x32_bf16 v[52:55], a[0:3], v[88:91], 0\n"
"v_mfma_f32_16x16x32_bf16 v[52:55], a[4:7], v[96:99], v[52:55]\n"
"s_add_u32 s98, s62, 0\n"
"v_mfma_f32_16x16x32_bf16 v[52:55], a[8:11], v[188:191], v[52:55]\n"
"s_add_u32 s96, s54, 0\n"
"buffer_load_dwordx4 v[68:71], v0, s[4:7], s96 offen sc1\n"
"v_mfma_f32_16x16x32_bf16 v[52:55], a[12:15], v[116:119], v[52:55]\n"
"s_waitcnt vmcnt(11)\n"
"v_mfma_f32_16x16x32_bf16 v[52:55], a[16:19], v[104:107], v[52:55]\n"
"buffer_load_dword v255, v10, s[4:7], s98 offen sc1\n"
"v_mfma_f32_16x16x32_bf16 v[52:55], a[20:23], v[220:223], v[52:55]\n"
"s_waitcnt lgkmcnt(2)\n"
"v_add_f32_e32 v124, v124, v212\n"
"v_add_f32_e32 v125, v125, v213\n"
"v_mfma_f32_16x16x32_bf16 v[52:55], a[24:27], v[180:183], v[52:55]\n"
"v_add_f32_e32 v126, v126, v214\n"
"v_add_f32_e32 v127, v127, v215\n"
"v_mfma_f32_16x16x32_bf16 v[52:55], a[28:31], v[48:51], v[52:55]\n"
"buffer_load_dwordx4 v[84:87], v0, s[4:7], s96 offen offset:1024 sc1\n"
"v_mfma_f32_16x16x32_bf16 v[92:95], a[60:63], v[48:51], 0\n"
"buffer_load_dwordx4 a[200:203], v0, s[4:7], s97 offen offset:2048 sc1\n"
"v_mfma_f32_16x16x32_bf16 v[92:95], a[56:59], v[180:183], v[92:95]\n"
"buffer_load_dword v253, v10, s[4:7], s98 offen offset:256 sc1\n"
"v_mfma_f32_16x16x32_bf16 v[92:95], a[52:55], v[220:223], v[92:95]\n"
"v_add_f32_e32 v128, v128, v216\n"
"v_add_f32_e32 v129, v129, v217\n"
"v_mfma_f32_16x16x32_bf16 v[92:95], a[48:51], v[104:107], v[92:95]\n"
"v_add_f32_e32 v130, v130, v218\n"
"v_add_f32_e32 v131, v131, v219\n"
"v_mfma_f32_16x16x32_bf16 v[92:95], a[44:47], v[116:119], v[92:95]\n"
"buffer_load_dwordx4 v[132:135], v0, s[4:7], s96 offen offset:2048 sc1\n"
"v_mfma_f32_16x16x32_bf16 v[92:95], a[40:43], v[188:191], v[92:95]\n"
"ds_write_b128 v3, v[52:55]\n"
"v_mfma_f32_16x16x32_bf16 v[92:95], a[36:39], v[96:99], v[92:95]\n"
"buffer_load_dword v252, v10, s[4:7], s98 offen offset:512 sc1\n"
"v_mfma_f32_16x16x32_bf16 v[92:95], a[32:35], v[88:91], v[92:95]\n"
"v_mul_f32_e32 v124, v124, v254\n"
"v_mul_f32_e32 v125, v125, v254\n"
"v_mfma_f32_16x16x32_bf16 v[52:55], a[64:67], v[88:91], 0\n"
"v_mul_f32_e32 v126, v126, v254\n"
"v_mul_f32_e32 v127, v127, v254\n"
"v_mfma_f32_16x16x32_bf16 v[52:55], a[68:71], v[96:99], v[52:55]\n"
"buffer_load_dwordx4 v[144:147], v0, s[4:7], s96 offen offset:3072 sc1\n"
"v_mfma_f32_16x16x32_bf16 v[52:55], a[72:75], v[188:191], v[52:55]\n"
"buffer_load_dword v251, v10, s[4:7], s98 offen offset:768 sc1\n"
"v_mfma_f32_16x16x32_bf16 v[52:55], a[76:79], v[116:119], v[52:55]\n"
"v_mul_f32_e32 v128, v128, v254\n"
"v_mul_f32_e32 v129, v129, v254\n"
"v_mfma_f32_16x16x32_bf16 v[52:55], a[80:83], v[104:107], v[52:55]\n"
"v_mul_f32_e32 v130, v130, v254\n"
"v_mul_f32_e32 v131, v131, v254\n"
"v_mfma_f32_16x16x32_bf16 v[52:55], a[84:87], v[220:223], v[52:55]\n"
"ds_write_b128 v3, v[92:95] offset:1024\n"
"v_mfma_f32_16x16x32_bf16 v[52:55], a[88:91], v[180:183], v[52:55]\n"
"buffer_load_dwordx4 v[92:95], v1, s[4:7], s96 offen sc1\n"
"v_mfma_f32_16x16x32_bf16 v[52:55], a[92:95], v[48:51], v[52:55]\n"
"buffer_load_dword v254, v10, s[4:7], s98 offen offset:1024 sc1\n"
"v_mfma_f32_16x16x32_bf16 v[156:159], a[124:127], v[48:51], 0\n"
"v_cvt_pk_bf16_f32 v160, v124, v125\n"
"v_cvt_pk_bf16_f32 v161, v126, v127\n"
"v_mfma_f32_16x16x32_bf16 v[156:159], a[120:123], v[180:183], v[156:159]\n"
"v_cvt_pk_bf16_f32 v162, v128, v129\n"
"v_cvt_pk_bf16_f32 v163, v130, v131\n"
"v_mfma_f32_16x16x32_bf16 v[156:159], a[116:119], v[220:223], v[156:159]\n"
"buffer_load_dwordx4 a[204:207], v0, s[4:7], s97 offen offset:3072 sc1\n"
"v_mfma_f32_16x16x32_bf16 v[156:159], a[112:115], v[104:107], v[156:159]\n"
"buffer_load_dwordx4 v[124:127], v1, s[4:7], s96 offen offset:1024 sc1\n"
"v_mfma_f32_16x16x32_bf16 v[156:159], a[108:111], v[116:119], v[156:159]\n"
"buffer_load_dword v250, v10, s[4:7], s98 offen offset:1280 sc1\n"
"v_mfma_f32_16x16x32_bf16 v[156:159], a[104:107], v[188:191], v[156:159]\n"
"ds_write_b128 v3, v[52:55] offset:2048\n"
"v_mfma_f32_16x16x32_bf16 v[156:159], a[100:103], v[96:99], v[156:159]\n"
"ds_write_b128 v2, v[160:163] offset:17408\n"
"v_mfma_f32_16x16x32_bf16 v[156:159], a[96:99], v[88:91], v[156:159]\n"
"buffer_load_dword v249, v10, s[4:7], s98 offen offset:1536 sc1\n"
"v_mfma_f32_16x16x32_bf16 v[52:55], v[28:31], v[88:91], 0\n"
"buffer_load_dwordx4 v[128:131], v1, s[4:7], s96 offen offset:2048 sc1\n"
"v_mfma_f32_16x16x32_bf16 v[52:55], v[40:43], v[96:99], v[52:55]\n"
"buffer_store_dwordx4 v[224:227], v8, s[4:7], s64 offen offset:1024 sc1\n"
"v_mfma_f32_16x16x32_bf16 v[52:55], v[24:27], v[188:191], v[52:55]\n"
"buffer_load_dword v248, v10, s[4:7], s98 offen offset:1792 sc1\n"
"v_mfma_f32_16x16x32_bf16 v[52:55], v[72:75], v[116:119], v[52:55]\n"
"v_mfma_f32_16x16x32_bf16 v[52:55], v[32:35], v[104:107], v[52:55]\n"
"v_mfma_f32_16x16x32_bf16 v[52:55], v[64:67], v[220:223], v[52:55]\n"
"buffer_load_dwordx4 v[160:163], v1, s[4:7], s96 offen offset:3072 sc1\n"
"v_mfma_f32_16x16x32_bf16 v[52:55], v[60:63], v[180:183], v[52:55]\n"
"ds_write_b128 v3, v[156:159] offset:3072\n"
"v_mfma_f32_16x16x32_bf16 v[52:55], v[36:39], v[48:51], v[52:55]\n"
"v_mfma_f32_16x16x32_bf16 v[156:159], v[100:103], v[48:51], 0\n"
"v_mfma_f32_16x16x32_bf16 v[156:159], v[76:79], v[180:183], v[156:159]\n"
"v_mfma_f32_16x16x32_bf16 v[156:159], v[140:143], v[220:223], v[156:159]\n"
"v_mfma_f32_16x16x32_bf16 v[156:159], v[136:139], v[104:107], v[156:159]\n"
"v_mfma_f32_16x16x32_bf16 v[156:159], v[56:59], v[116:119], v[156:159]\n"
"buffer_load_dwordx4 a[208:211], v1, s[4:7], s97 offen sc1\n"
"v_mfma_f32_16x16x32_bf16 v[156:159], v[112:115], v[188:191], v[156:159]\n"
"s_nop 0\n"
"ds_write_b128 v3, v[52:55] offset:4096\n"
"v_mfma_f32_16x16x32_bf16 v[156:159], v[108:111], v[96:99], v[156:159]\n"
"v_mfma_f32_16x16x32_bf16 v[156:159], v[120:123], v[88:91], v[156:159]\n"
"s_waitcnt vmcnt(25)\n"
"; @A G1 3.1\n"
"v_mfma_f32_16x16x32_bf16 v[48:51], a[0:3], v[152:155], 0\n"
"v_mfma_f32_16x16x32_bf16 v[48:51], a[4:7], v[164:167], v[48:51]\n"
"v_mfma_f32_16x16x32_bf16 v[48:51], a[8:11], v[200:203], v[48:51]\n"
"s_add_u32 s98, s55, 0\n"
"buffer_load_dwordx4 v[52:55], v0, s[4:7], s98 offen sc1\n"
"v_mfma_f32_16x16x32_bf16 v[48:51], a[12:15], v[192:195], v[48:51]\n"
"s_waitcnt vmcnt(21)\n"
"v_mfma_f32_16x16x32_bf16 v[48:51], a[16:19], v[80:83], v[48:51]\n"
"v_mfma_f32_16x16x32_bf16 v[48:51], a[20:23], v[148:151], v[48:51]\n"
"ds_write_b128 v3, v[156:159] offset:5120\n"
"v_mfma_f32_16x16x32_bf16 v[48:51], a[24:27], v[168:171], v[48:51]\n"
"v_mfma_f32_16x16x32_bf16 v[48:51], a[28:31], v[176:179], v[48:51]\n"
"buffer_load_dwordx4 v[88:91], v0, s[4:7], s98 offen offset:1024 sc1\n"
"s_add_u32 s96, s64, 0x4000\n"
"v_mfma_f32_16x16x32_bf16 v[96:99], a[60:63], v[176:179], 0\n"
"v_mfma_f32_16x16x32_bf16 v[96:99], a[56:59], v[168:171], v[96:99]\n"
"buffer_store_dwordx4 v[44:47], v7, s[4:7], s96 offen offset:1024 sc1\n"
"v_mfma_f32_16x16x32_bf16 v[96:99], a[52:55], v[148:151], v[96:99]\n"
"v_mfma_f32_16x16x32_bf16 v[96:99], a[48:51], v[80:83], v[96:99]\n"
"v_mfma_f32_16x16x32_bf16 v[96:99], a[44:47], v[192:195], v[96:99]\n"
"buffer_load_dwordx4 v[44:47], v0, s[4:7], s98 offen offset:2048 sc1\n"
"v_mfma_f32_16x16x32_bf16 v[96:99], a[40:43], v[200:203], v[96:99]\n"
"ds_write_b128 v3, v[48:51] offset:6144\n"
"v_mfma_f32_16x16x32_bf16 v[96:99], a[36:39], v[164:167], v[96:99]\n"
"buffer_load_dwordx4 a[212:215], v1, s[4:7], s97 offen offset:1024 sc1\n"
"v_mfma_f32_16x16x32_bf16 v[96:99], a[32:35], v[152:155], v[96:99]\n"
"v_mfma_f32_16x16x32_bf16 v[48:51], a[64:67], v[152:155], 0\n"
"v_mfma_f32_16x16x32_bf16 v[48:51], a[68:71], v[164:167], v[48:51]\n"
"buffer_load_dwordx4 v[104:107], v0, s[4:7], s98 offen offset:3072 sc1\n"
"v_mfma_f32_16x16x32_bf16 v[48:51], a[72:75], v[200:203], v[48:51]\n"
"v_mfma_f32_16x16x32_bf16 v[48:51], a[76:79], v[192:195], v[48:51]\n"
"v_mfma_f32_16x16x32_bf16 v[48:51], a[80:83], v[80:83], v[48:51]\n"
"v_mfma_f32_16x16x32_bf16 v[48:51], a[84:87], v[148:151], v[48:51]\n"
"s_nop 0\n"
"ds_write_b128 v3, v[96:99] offset:7168\n"
"v_mfma_f32_16x16x32_bf16 v[48:51], a[88:91], v[168:171], v[48:51]\n"
"buffer_load_dwordx4 v[96:99], v1, s[4:7], s98 offen sc1\n"
"v_mfma_f32_16x16x32_bf16 v[48:51], a[92:95], v[176:179], v[48:51]\n"
"v_mfma_f32_16x16x32_bf16 v[116:119], a[124:127], v[176:179], 0\n"
"v_mfma_f32_16x16x32_bf16 v[116:119], a[120:123], v[168:171], v[116:119]\n"
"v_mfma_f32_16x16x32_bf16 v[116:119], a[116:119], v[148:151], v[116:119]\n"
"v_mfma_f32_16x16x32_bf16 v[116:119], a[112:115], v[80:83], v[116:119]\n"
"buffer_load_dwordx4 v[156:159], v1, s[4:7], s98 offen offset:1024 sc1\n"
"v_mfma_f32_16x16x32_bf16 v[116:119], a[108:111], v[192:195], v[116:119]\n"
"v_mfma_f32_16x16x32_bf16 v[116:119], a[104:107], v[200:203], v[116:119]\n"
"s_nop 0\n"
"ds_write_b128 v3, v[48:51] offset:8192\n"
"v_mfma_f32_16x16x32_bf16 v[116:119], a[100:103], v[164:167], v[116:119]\n"
"v_mfma_f32_16x16x32_bf16 v[116:119], a[96:99], v[152:155], v[116:119]\n"
"v_mfma_f32_16x16x32_bf16 v[48:51], v[28:31], v[152:155], 0\n"
"buffer_load_dwordx4 v[172:175], v1, s[4:7], s98 offen offset:2048 sc1\n"
"v_mfma_f32_16x16x32_bf16 v[48:51], v[40:43], v[164:167], v[48:51]\n"
"buffer_load_dwordx4 a[216:219], v1, s[4:7], s97 offen offset:2048 sc1\n"
"v_mfma_f32_16x16x32_bf16 v[48:51], v[24:27], v[200:203], v[48:51]\n"
"v_mfma_f32_16x16x32_bf16 v[48:51], v[72:75], v[192:195], v[48:51]\n"
"v_mfma_f32_16x16x32_bf16 v[48:51], v[32:35], v[80:83], v[48:51]\n"
"s_waitcnt vmcnt(12)\n"
"v_add_f32_e32 v255, v255, v254\n"
"v_add_f32_e32 v253, v253, v250\n"
"v_mfma_f32_16x16x32_bf16 v[48:51], v[64:67], v[148:151], v[48:51]\n"
"buffer_load_dwordx4 v[180:183], v1, s[4:7], s98 offen offset:3072 sc1\n"
"v_mfma_f32_16x16x32_bf16 v[48:51], v[60:63], v[168:171], v[48:51]\n"
"ds_write_b128 v3, v[116:119] offset:9216\n"
"v_mfma_f32_16x16x32_bf16 v[48:51], v[36:39], v[176:179], v[48:51]\n"
"v_add_f32_e32 v252, v252, v249\n"
"v_add_f32_e32 v251, v251, v248\n"
"v_mfma_f32_16x16x32_bf16 v[116:119], v[100:103], v[176:179], 0\n"
"v_add_f32_e32 v255, v255, v252\n"
"v_add_f32_e32 v253, v253, v251\n"
"v_mfma_f32_16x16x32_bf16 v[116:119], v[76:79], v[168:171], v[116:119]\n"
"v_add_f32_e32 v255, v255, v253\n"
"v_mov_b32_e32 v253, v255\n"
"v_mfma_f32_16x16x32_bf16 v[116:119], v[140:143], v[148:151], v[116:119]\n"
"s_add_u32 s98, s71, 0\n"
"buffer_load_dwordx2 v[250:251], off, s[4:7], s98 sc1\n"
"v_mfma_f32_16x16x32_bf16 v[116:119], v[136:139], v[80:83], v[116:119]\n"
"v_permlane32_swap_b32_e32 v255, v253\n"
"v_add_f32_e32 v255, v255, v253\n"
"v_mfma_f32_16x16x32_bf16 v[116:119], v[56:59], v[192:195], v[116:119]\n"
"v_mul_f32_e32 v255, 0x3a800000, v255\n"
"v_mov_b32_e32 v253, v255\n"
"v_mfma_f32_16x16x32_bf16 v[116:119], v[112:115], v[200:203], v[116:119]\n"
"ds_write_b128 v3, v[48:51] offset:10240\n"
"v_mfma_f32_16x16x32_bf16 v[116:119], v[108:111], v[164:167], v[116:119]\n"
"buffer_store_dwordx2 v[18:19], v11, s[4:7], s68 offen offset:32 sc1\n"
"v_mfma_f32_16x16x32_bf16 v[116:119], v[120:123], v[152:155], v[116:119]\n"
"v_permlane16_swap_b32_e32 v255, v253\n"
"v_cvt_pk_bf16_f32 v254, -v255, 0\n"
"; @A G1 3.2\n"
"v_mfma_f32_16x16x32_bf16 v[48:51], a[0:3], v[68:71], 0\n"
"v_fma_f32 v253, -v255, v255, v253\n"
"v_lshlrev_b32_e32 v254, 16, v254\n"
"v_mfma_f32_16x16x32_bf16 v[48:51], a[4:7], v[84:87], v[48:51]\n"
"v_max_f32_e32 v253, 0, v253\n"
"v_cvt_pk_bf16_f32 v253, v253, 0\n"
"v_mfma_f32_16x16x32_bf16 v[48:51], a[8:11], v[132:135], v[48:51]\n"
"buffer_load_dwordx4 a[220:223], v1, s[4:7], s97 offen offset:3072 sc1\n"
"v_mfma_f32_16x16x32_bf16 v[48:51], a[12:15], v[144:147], v[48:51]\n"
"s_waitcnt vmcnt(15)\n"
"v_lshlrev_b32_e32 v253, 16, v253\n"
"v_add_f32_e32 v253, 0x3727c5ac, v253\n"
"v_mfma_f32_16x16x32_bf16 v[48:51], a[16:19], v[92:95], v[48:51]\n"
"s_add_u32 s97, s70, 0x6000\n"
"v_cvt_pk_bf16_f32 v253, v253, 0\n"
"v_lshlrev_b32_e32 v253, 16, v253\n"
"v_mfma_f32_16x16x32_bf16 v[48:51], a[20:23], v[124:127], v[48:51]\n"
"ds_write_b128 v3, v[116:119] offset:11264\n"
"v_mfma_f32_16x16x32_bf16 v[48:51], a[24:27], v[128:131], v[48:51]\n"
"buffer_load_dwordx4 a[224:227], v0, s[4:7], s97 offen sc1\n"
"v_mfma_f32_16x16x32_bf16 v[48:51], a[28:31], v[160:163], v[48:51]\n"
"v_sqrt_f32_e32 v253, v253\n"
"v_mfma_f32_16x16x32_bf16 v[80:83], a[60:63], v[160:163], 0\n"
"v_cvt_pk_bf16_f32 v253, v253, 0\n"
"v_lshlrev_b32_e32 v253, 16, v253\n"
"v_mfma_f32_16x16x32_bf16 v[80:83], a[56:59], v[128:131], v[80:83]\n"
"v_rcp_f32_e32 v255, v253\n"
"v_mfma_f32_16x16x32_bf16 v[80:83], a[52:55], v[124:127], v[80:83]\n"
"s_waitcnt lgkmcnt(0)\n"
"s_barrier\n"
"v_mfma_f32_16x16x32_bf16 v[80:83], a[48:51], v[92:95], v[80:83]\n"
"buffer_load_dwordx4 a[228:231], v0, s[4:7], s97 offen offset:1024 sc1\n"
"v_mfma_f32_16x16x32_bf16 v[80:83], a[44:47], v[144:147], v[80:83]\n"
"ds_read_b128 v[116:119], v4\n"
"v_mul_f32_e32 v253, 0x3e8293ee, v255\n"
"v_mfma_f32_16x16x32_bf16 v[80:83], a[40:43], v[132:135], v[80:83]\n"
"ds_write_b128 v3, v[48:51] offset:12288\n"
"v_mfma_f32_16x16x32_bf16 v[80:83], a[36:39], v[84:87], v[80:83]\n"
"ds_read_b128 v[48:51], v9 offset:128\n"
"ds_read_b128 v[148:151], v5\n"
"v_mfma_f32_16x16x32_bf16 v[80:83], a[32:35], v[68:71], v[80:83]\n"
"buffer_load_dwordx4 a[232:235], v0, s[4:7], s97 offen offset:2048 sc1\n"
"v_mfma_f32_16x16x32_bf16 v[152:155], a[64:67], v[68:71], 0\n"
"v_mfma_f32_16x16x32_bf16 v[152:155], a[68:71], v[84:87], v[152:155]\n"
"v_mfma_f32_16x16x32_bf16 v[152:155], a[72:75], v[132:135], v[152:155]\n"
"ds_read_b128 v[164:167], v4 offset:1024\n"
"ds_read_b128 v[168:171], v9 offset:192\n"
"v_mfma_f32_16x16x32_bf16 v[152:155], a[76:79], v[144:147], v[152:155]\n"
"buffer_load_dwordx4 a[236:239], v0, s[4:7], s97 offen offset:3072 sc1\n"
"v_mfma_f32_16x16x32_bf16 v[152:155], a[80:83], v[92:95], v[152:155]\n"
"ds_read_b128 v[176:179], v5 offset:1024\n"
"s_waitcnt lgkmcnt(3)\n"
"v_mfma_f32_16x16x32_bf16 v[152:155], a[84:87], v[124:127], v[152:155]\n"
"ds_write_b128 v3, v[80:83] offset:13312\n"
"v_mfma_f32_16x16x32_bf16 v[152:155], a[88:91], v[128:131], v[152:155]\n"
"v_fmac_f32_e32 v116, v254, v48\n"
"v_fmac_f32_e32 v117, v254, v49\n"
"v_mfma_f32_16x16x32_bf16 v[152:155], a[92:95], v[160:163], v[152:155]\n"
"buffer_load_dwordx4 a[240:243], v1, s[4:7], s97 offen sc1\n"
"v_mfma_f32_16x16x32_bf16 v[80:83], a[124:127], v[160:163], 0\n"
"v_fmac_f32_e32 v118, v254, v50\n"
"v_fmac_f32_e32 v119, v254, v51\n"
"v_mfma_f32_16x16x32_bf16 v[80:83], a[120:123], v[128:131], v[80:83]\n"
"ds_read_b128 v[48:51], v4 offset:2048\n"
"ds_read_b128 v[184:187], v9 offset:256\n"
"v_mfma_f32_16x16x32_bf16 v[80:83], a[116:119], v[124:127], v[80:83]\n"
"ds_read_b128 v[188:191], v5 offset:2048\n"
"v_add_f32_e32 v116, v116, v148\n"
"v_mfma_f32_16x16x32_bf16 v[80:83], a[112:115], v[92:95], v[80:83]\n"
"buffer_load_dwordx4 a[244:247], v1, s[4:7], s97 offen offset:1024 sc1\n"
"v_mfma_f32_16x16x32_bf16 v[80:83], a[108:111], v[144:147], v[80:83]\n"
"v_add_f32_e32 v117, v117, v149\n"
"v_add_f32_e32 v118, v118, v150\n"
"v_mfma_f32_16x16x32_bf16 v[80:83], a[104:107], v[132:135], v[80:83]\n"
"ds_write_b128 v3, v[152:155] offset:14336\n"
"v_mfma_f32_16x16x32_bf16 v[80:83], a[100:103], v[84:87], v[80:83]\n"
"s_waitcnt lgkmcnt(5)\n"
"v_add_f32_e32 v119, v119, v151\n"
"v_fmac_f32_e32 v164, v254, v168\n"
"v_mfma_f32_16x16x32_bf16 v[80:83], a[96:99], v[68:71], v[80:83]\n"
"buffer_load_dwordx4 a[248:251], v1, s[4:7], s97 offen offset:2048 sc1\n"
"v_mfma_f32_16x16x32_bf16 v[148:151], v[28:31], v[68:71], 0\n"
"ds_read_b128 v[152:155], v4 offset:3072\n"
"ds_read_b128 v[192:195], v9 offset:320\n"
"v_mfma_f32_16x16x32_bf16 v[148:151], v[40:43], v[84:87], v[148:151]\n"
"ds_read_b128 v[196:199], v5 offset:3072\n"
"v_fmac_f32_e32 v165, v254, v169\n"
"v_mfma_f32_16x16x32_bf16 v[148:151], v[24:27], v[132:135], v[148:151]\n"
"v_fmac_f32_e32 v166, v254, v170\n"
"v_fmac_f32_e32 v167, v254, v171\n"
"v_mfma_f32_16x16x32_bf16 v[148:151], v[72:75], v[144:147], v[148:151]\n"
"buffer_load_dwordx4 a[252:255], v1, s[4:7], s97 offen offset:3072 sc1\n"
"v_mfma_f32_16x16x32_bf16 v[148:151], v[32:35], v[92:95], v[148:151]\n"
"v_add_f32_e32 v164, v164, v176\n"
"v_add_f32_e32 v165, v165, v177\n"
"v_mfma_f32_16x16x32_bf16 v[148:151], v[64:67], v[124:127], v[148:151]\n"
"ds_write_b128 v3, v[80:83] offset:15360\n"
"v_mfma_f32_16x16x32_bf16 v[148:151], v[60:63], v[128:131], v[148:151]\n"
"s_waitcnt lgkmcnt(5)\n"
"v_add_f32_e32 v166, v166, v178\n"
"v_add_f32_e32 v167, v167, v179\n"
"v_mfma_f32_16x16x32_bf16 v[148:151], v[36:39], v[160:163], v[148:151]\n"
"ds_read_b128 v[80:83], v4 offset:4096\n"
"ds_read_b128 v[168:171], v9\n"
"v_mfma_f32_16x16x32_bf16 v[176:179], v[100:103], v[160:163], 0\n"
"ds_read_b128 v[160:163], v5 offset:4096\n"
"v_fmac_f32_e32 v48, v254, v184\n"
"v_mfma_f32_16x16x32_bf16 v[176:179], v[76:79], v[128:131], v[176:179]\n"
"v_fmac_f32_e32 v49, v254, v185\n"
"v_fmac_f32_e32 v50, v254, v186\n"
"v_mfma_f32_16x16x32_bf16 v[176:179], v[140:143], v[124:127], v[176:179]\n"
"v_fmac_f32_e32 v51, v254, v187\n"
"v_add_f32_e32 v48, v48, v188\n"
"v_mfma_f32_16x16x32_bf16 v[176:179], v[136:139], v[92:95], v[176:179]\n"
"v_add_f32_e32 v49, v49, v189\n"
"v_add_f32_e32 v50, v50, v190\n"
"v_mfma_f32_16x16x32_bf16 v[176:179], v[56:59], v[144:147], v[176:179]\n"
"s_waitcnt vmcnt(10)\n"
"v_cmp_ne_u64_e32 vcc, v[250:251], v[18:19]\n"
"v_mfma_f32_16x16x32_bf16 v[176:179], v[112:115], v[132:135], v[176:179]\n"
"ds_write_b128 v3, v[148:151] offset:16384\n"
"v_mfma_f32_16x16x32_bf16 v[176:179], v[108:111], v[84:87], v[176:179]\n"
"s_cbranch_vccnz .Lmk_f78s\n"
".Lmk_f78r:\n"
"s_add_u32 m0, s77, 0x6000\n"
"v_mfma_f32_16x16x32_bf16 v[176:179], v[120:123], v[68:71], v[176:179]\n"
"s_add_u32 s98, s65, 0x2000\n"
"ds_read_b128 v[68:71], v4 offset:5120\n"
"; @A G1 3.3\n"
"v_mfma_f32_16x16x32_bf16 v[116:119], a[0:3], v[52:55], v[116:119]\n"
"buffer_load_dwordx4 v2, s[4:7], s98 offen sc1 lds\n"
"v_mfma_f32_16x16x32_bf16 v[116:119], a[4:7], v[88:91], v[116:119]\n"
"buffer_load_dwordx4 v2, s[4:7], s98 offen offset:1024 sc1 lds\n"
"v_mfma_f32_16x16x32_bf16 v[116:119], a[8:11], v[44:47], v[116:119]\n"
"ds_read_b128 v[84:87], v9 offset:64\n"
"ds_read_b128 v[92:95], v5 offset:5120\n"
"v_mfma_f32_16x16x32_bf16 v[116:119], a[12:15], v[104:107], v[116:119]\n"
"s_waitcnt lgkmcnt(8)\n"
"v_add_f32_e32 v51, v51, v191\n"
"v_mfma_f32_16x16x32_bf16 v[116:119], a[16:19], v[96:99], v[116:119]\n"
"v_fmac_f32_e32 v152, v254, v192\n"
"v_fmac_f32_e32 v153, v254, v193\n"
"v_mfma_f32_16x16x32_bf16 v[116:119], a[20:23], v[156:159], v[116:119]\n"
"ds_write_b128 v3, v[176:179] offset:17408\n"
"v_mfma_f32_16x16x32_bf16 v[116:119], a[24:27], v[172:175], v[116:119]\n"
"v_fmac_f32_e32 v154, v254, v194\n"
"v_fmac_f32_e32 v155, v254, v195\n"
"v_mfma_f32_16x16x32_bf16 v[116:119], a[28:31], v[180:183], v[116:119]\n"
"s_add_u32 m0, s77, 0xe000\n"
"s_add_u32 s98, s65, 0x6000\n"
"v_add_f32_e32 v152, v152, v196\n"
"v_mfma_f32_16x16x32_bf16 v[164:167], a[60:63], v[180:183], v[164:167]\n"
"buffer_load_dwordx4 v2, s[4:7], s98 offen sc1 lds\n"
"v_mfma_f32_16x16x32_bf16 v[164:167], a[56:59], v[172:175], v[164:167]\n"
"buffer_load_dwordx4 v2, s[4:7], s98 offen offset:1024 sc1 lds\n"
"v_mfma_f32_16x16x32_bf16 v[164:167], a[52:55], v[156:159], v[164:167]\n"
"s_waitcnt lgkmcnt(0)\n"
"s_barrier\n"
"v_mfma_f32_16x16x32_bf16 v[164:167], a[48:51], v[96:99], v[164:167]\n"
"ds_read_b128 v[124:127], v6\n"
"ds_read_b128 v[128:131], v6 offset:1024\n"
"v_mfma_f32_16x16x32_bf16 v[164:167], a[44:47], v[104:107], v[164:167]\n"
"s_add_u32 s98, s69, 0x600000\n"
"buffer_load_dwordx4 a[0:3], v0, s[4:7], s98 offen sc1\n"
"v_mfma_f32_16x16x32_bf16 v[164:167], a[40:43], v[44:47], v[164:167]\n"
"v_add_f32_e32 v153, v153, v197\n"
"v_add_f32_e32 v154, v154, v198\n"
"v_mfma_f32_16x16x32_bf16 v[164:167], a[36:39], v[88:91], v[164:167]\n"
"v_add_f32_e32 v155, v155, v199\n"
"v_fmac_f32_e32 v80, v254, v168\n"
"v_mfma_f32_16x16x32_bf16 v[164:167], a[32:35], v[52:55], v[164:167]\n"
"buffer_load_dwordx4 a[4:7], v0, s[4:7], s98 offen offset:1024 sc1\n"
"v_mfma_f32_16x16x32_bf16 v[48:51], a[64:67], v[52:55], v[48:51]\n"
"v_fmac_f32_e32 v81, v254, v169\n"
"v_fmac_f32_e32 v82, v254, v170\n"
"v_mfma_f32_16x16x32_bf16 v[48:51], a[68:71], v[88:91], v[48:51]\n"
"v_fmac_f32_e32 v83, v254, v171\n"
"v_add_f32_e32 v80, v80, v160\n"
"v_mfma_f32_16x16x32_bf16 v[48:51], a[72:75], v[44:47], v[48:51]\n"
"buffer_load_dwordx4 a[8:11], v0, s[4:7], s98 offen offset:2048 sc1\n"
"v_mfma_f32_16x16x32_bf16 v[48:51], a[76:79], v[104:107], v[48:51]\n"
"v_add_f32_e32 v81, v81, v161\n"
"v_add_f32_e32 v82, v82, v162\n"
"v_mfma_f32_16x16x32_bf16 v[48:51], a[80:83], v[96:99], v[48:51]\n"
"v_add_f32_e32 v83, v83, v163\n"
"v_fmac_f32_e32 v68, v254, v84\n"
"v_mfma_f32_16x16x32_bf16 v[48:51], a[84:87], v[156:159], v[48:51]\n"
"buffer_load_dwordx4 a[12:15], v0, s[4:7], s98 offen offset:3072 sc1\n"
"v_mfma_f32_16x16x32_bf16 v[48:51], a[88:91], v[172:175], v[48:51]\n"
"ds_read_b128 v[132:135], v6 offset:2048\n"
"ds_read_b128 v[144:147], v6 offset:3072\n"
"v_mfma_f32_16x16x32_bf16 v[48:51], a[92:95], v[180:183], v[48:51]\n"
"s_waitcnt lgkmcnt(2)\n"
"v_fmac_f32_e32 v69, v254, v85\n"
"v_fmac_f32_e32 v70, v254, v86\n"
"v_mfma_f32_16x16x32_bf16 v[152:155], a[124:127], v[180:183], v[152:155]\n"
"buffer_load_dwordx4 a[16:19], v1, s[4:7], s98 offen sc1\n"
"v_mfma_f32_16x16x32_bf16 v[152:155], a[120:123], v[172:175], v[152:155]\n"
"v_fmac_f32_e32 v71, v254, v87\n"
"v_add_f32_e32 v68, v68, v92\n"
"v_mfma_f32_16x16x32_bf16 v[152:155], a[116:119], v[156:159], v[152:155]\n"
"v_add_f32_e32 v69, v69, v93\n"
"v_add_f32_e32 v70, v70, v94\n"
"v_mfma_f32_16x16x32_bf16 v[152:155], a[112:115], v[96:99], v[152:155]\n"
"buffer_load_dwordx4 a[20:23], v1, s[4:7], s98 offen offset:1024 sc1\n"
"v_mfma_f32_16x16x32_bf16 v[152:155], a[108:111], v[104:107], v[152:155]\n"
"v_add_f32_e32 v71, v71, v95\n"
"v_add_f32_e32 v116, v116, v124\n"
"v_mfma_f32_16x16x32_bf16 v[152:155], a[104:107], v[44:47], v[152:155]\n"
"v_add_f32_e32 v117, v117, v125\n"
"v_add_f32_e32 v118, v118, v126\n"
"v_mfma_f32_16x16x32_bf16 v[152:155], a[100:103], v[88:91], v[152:155]\n"
"buffer_load_dwordx4 a[24:27], v1, s[4:7], s98 offen offset:2048 sc1\n"
"v_mfma_f32_16x16x32_bf16 v[152:155], a[96:99], v[52:55], v[152:155]\n"
"v_add_f32_e32 v119, v119, v127\n"
"v_add_f32_e32 v164, v164, v128\n"
"v_mfma_f32_16x16x32_bf16 v[80:83], v[28:31], v[52:55], v[80:83]\n"
"v_add_f32_e32 v165, v165, v129\n"
"v_add_f32_e32 v166, v166, v130\n"
"v_mfma_f32_16x16x32_bf16 v[80:83], v[40:43], v[88:91], v[80:83]\n"
"buffer_load_dwordx4 a[28:31], v1, s[4:7], s98 offen offset:3072 sc1\n"
"v_mfma_f32_16x16x32_bf16 v[80:83], v[24:27], v[44:47], v[80:83]\n"
"v_add_f32_e32 v167, v167, v131\n"
"v_mul_f32_e32 v116, v116, v255\n"
"v_mfma_f32_16x16x32_bf16 v[80:83], v[72:75], v[104:107], v[80:83]\n"
"v_mul_f32_e32 v117, v117, v255\n"
"v_mul_f32_e32 v118, v118, v255\n"
"v_mfma_f32_16x16x32_bf16 v[80:83], v[32:35], v[96:99], v[80:83]\n"
"s_waitcnt lgkmcnt(0)\n"
"v_mul_f32_e32 v119, v119, v255\n"
"v_mul_f32_e32 v164, v164, v255\n"
"v_mfma_f32_16x16x32_bf16 v[80:83], v[64:67], v[156:159], v[80:83]\n"
"v_mul_f32_e32 v165, v165, v255\n"
"v_mul_f32_e32 v166, v166, v255\n"
"v_mfma_f32_16x16x32_bf16 v[80:83], v[60:63], v[172:175], v[80:83]\n"
"ds_read_b128 v[24:27], v6 offset:4096\n"
"ds_read_b128 v[28:31], v6 offset:5120\n"
"v_mfma_f32_16x16x32_bf16 v[80:83], v[36:39], v[180:183], v[80:83]\n"
"v_mul_f32_e32 v167, v167, v255\n"
"v_cvt_pk_bf16_f32 v32, v116, v117\n"
"v_mfma_f32_16x16x32_bf16 v[68:71], v[100:103], v[180:183], v[68:71]\n"
"v_cvt_pk_bf16_f32 v33, v118, v119\n"
"v_cvt_pk_bf16_f32 v34, v164, v165\n"
"v_mfma_f32_16x16x32_bf16 v[68:71], v[76:79], v[172:175], v[68:71]\n"
"v_cvt_pk_bf16_f32 v35, v166, v167\n"
"v_add_f32_e32 v48, v48, v132\n"
"v_mfma_f32_16x16x32_bf16 v[68:71], v[140:143], v[156:159], v[68:71]\n"
"ds_write_b128 v7, v[32:35] offset:16384\n"
"v_mfma_f32_16x16x32_bf16 v[68:71], v[136:139], v[96:99], v[68:71]\n"
"buffer_store_dwordx4 v[32:35], v7, s[4:7], s64 offen sc1\n"
"v_mfma_f32_16x16x32_bf16 v[68:71], v[56:59], v[104:107], v[68:71]\n"
"v_add_f32_e32 v49, v49, v133\n"
"v_add_f32_e32 v50, v50, v134\n"
"v_mfma_f32_16x16x32_bf16 v[68:71], v[112:115], v[44:47], v[68:71]\n"
"v_add_f32_e32 v51, v51, v135\n"
"v_add_f32_e32 v152, v152, v144\n"
"v_mfma_f32_16x16x32_bf16 v[68:71], v[108:111], v[88:91], v[68:71]\n"
"v_add_f32_e32 v153, v153, v145\n"
"v_add_f32_e32 v154, v154, v146\n"
"v_mfma_f32_16x16x32_bf16 v[68:71], v[120:123], v[52:55], v[68:71]\n"
"v_add_f32_e32 v155, v155, v147\n"
"v_mul_f32_e32 v48, v48, v255\n"
"v_mul_f32_e32 v49, v49, v255\n"
"v_mul_f32_e32 v50, v50, v255\n"
"v_mul_f32_e32 v51, v51, v255\n"
"v_mul_f32_e32 v152, v152, v255\n"
"v_mul_f32_e32 v153, v153, v255\n"
"v_mul_f32_e32 v154, v154, v255\n"
"v_mul_f32_e32 v155, v155, v255\n"
"v_cvt_pk_bf16_f32 v32, v48, v49\n"
"v_cvt_pk_bf16_f32 v33, v50, v51\n"
"v_cvt_pk_bf16_f32 v34, v152, v153\n"
"v_cvt_pk_bf16_f32 v35, v154, v155\n"
"v_xor_b32_e32 v255, 32, v16\n"
"v_xor_b32_e32 v254, 32, v15\n"
"v_mov_b32_e32 v88, 0x3f803f80\n"
"v_mov_b32_e32 v89, 0x3f803f80\n"
"v_mov_b32_e32 v90, 0x3f803f80\n"
"v_mov_b32_e32 v91, 0x3f803f80\n"
"v_mov_b32_e32 v252, 0x53800000\n"
"ds_write_b128 v7, v[32:35] offset:49152\n"
".Lmk_a4:\n"
"; @A tail\n"
"s_add_u32 s98, s64, 0x4000\n"
"buffer_store_dwordx4 v[32:35], v7, s[4:7], s98 offen sc1\n"
"s_waitcnt lgkmcnt(2)\n"
"v_add_f32_e32 v80, v80, v24\n"
"v_add_f32_e32 v81, v81, v25\n"
"v_add_f32_e32 v82, v82, v26\n"
"v_add_f32_e32 v83, v83, v27\n"
"v_add_f32_e32 v68, v68, v28\n"
"v_add_f32_e32 v69, v69, v29\n"
"v_add_f32_e32 v70, v70, v30\n"
"v_add_f32_e32 v71, v71, v31\n"
"v_mul_f32_e32 v80, v80, v253\n"
"v_mul_f32_e32 v81, v81, v253\n"
"v_mul_f32_e32 v82, v82, v253\n"
"v_mul_f32_e32 v83, v83, v253\n"
"v_mul_f32_e32 v68, v68, v253\n"
"v_mul_f32_e32 v69, v69, v253\n"
"v_mul_f32_e32 v70, v70, v253\n"
"v_mul_f32_e32 v71, v71, v253\n"
"v_cvt_pk_bf16_f32 v24, v80, v81\n"
"v_cvt_pk_bf16_f32 v25, v82, v83\n"
"v_cvt_pk_bf16_f32 v26, v68, v69\n"
"v_cvt_pk_bf16_f32 v27, v70, v71\n"
"ds_write_b128 v2, v[24:27] offset:16384\n"
"s_waitcnt lgkmcnt(0)\n"
"s_barrier\n"
"ds_read_b128 v[28:31], v2 offset:49152\n"
"ds_read_b128 v[32:35], v2 offset:50176\n"
"ds_read_b128 v[92:95], v2 offset:16384\n"
"ds_read_b128 v[96:99], v2 offset:17408\n"
"; @A AT b\n"
"s_mov_b32 s2, 0\n"
"v_mov_b32_e32 v253, 0xff800000\n"
"v_mov_b32_e32 v251, 0xff800000\n"
"v_mov_b32_e32 v250, 0x7f800000\n"
"v_mov_b32_e32 v249, 0x7f800000\n"
".Lmk_a96:\n"
"ds_read_b128 v[24:27], v15 offset:24576\n"
"ds_read_b128 v[36:39], v254 offset:24832\n"
"ds_read_b128 v[40:43], v15 offset:22528\n"
"ds_read_b128 v[44:47], v254 offset:22784\n"
"s_cmp_gt_i32 s78, 11\n"
"s_cselect_b32 s98, 0x80000000, 0\n"
"s_or_b32 s98, s98, 0x7f800000\n"
"s_waitcnt lgkmcnt(2)\n"
"v_mfma_f32_16x16x32_bf16 v[48:51], v[24:27], v[28:31], 0\n"
"v_mfma_f32_16x16x32_bf16 v[52:55], v[24:27], v[32:35], 0\n"
"v_mfma_f32_16x16x32_bf16 v[56:59], v[36:39], v[32:35], 0\n"
"v_mfma_f32_16x16x32_bf16 v[24:27], v[36:39], v[28:31], 0\n"
"s_waitcnt lgkmcnt(0)\n"
"s_nop 3\n"
"v_max_f32_e32 v248, v48, v49\n"
"v_max_f32_e32 v247, v52, v53\n"
"v_max3_f32 v248, v248, v50, v51\n"
"v_max3_f32 v247, v247, v54, v55\n"
"v_max3_f32 v248, v248, v24, v25\n"
"v_max3_f32 v247, v247, v56, v57\n"
"v_max3_f32 v248, v248, v26, v27\n"
"v_max3_f32 v247, v247, v58, v59\n"
"v_mov_b32_e32 v246, v248\n"
"v_mov_b32_e32 v245, v247\n"
"s_nop 0\n"
"v_permlane16_swap_b32_e32 v248, v246\n"
"v_permlane16_swap_b32_e32 v247, v245\n"
"v_max_f32_e32 v248, v248, v246\n"
"v_max_f32_e32 v247, v247, v245\n"
"v_mov_b32_e32 v246, v248\n"
"v_mov_b32_e32 v245, v247\n"
"s_nop 0\n"
"v_permlane32_swap_b32_e32 v248, v246\n"
"v_permlane32_swap_b32_e32 v247, v245\n"
"v_max_f32_e32 v248, v248, v246\n"
"v_max_f32_e32 v247, v247, v245\n"
"v_med3_f32 v248, v248, v253, v250\n"
"v_med3_f32 v247, v247, v251, v249\n"
"v_min_f32_e64 v36, s98, -v248\n"
"v_min_f32_e64 v37, s98, -v248\n"
"v_min_f32_e64 v38, s98, -v248\n"
"v_min_f32_e64 v39, s98, -v248\n"
"v_min_f32_e64 v60, s98, -v247\n"
"v_min_f32_e64 v61, s98, -v247\n"
"v_min_f32_e64 v62, s98, -v247\n"
"v_min_f32_e64 v63, s98, -v247\n"
"v_mfma_f32_16x16x32_bf16 v[64:67], v[40:43], v[28:31], v[36:39]\n"
"ds_read_b128 v[68:71], v15 offset:20480\n"
"s_add_u32 s98, s69, 0x602000\n"
"buffer_load_dwordx4 a[32:35], v0, s[4:7], s98 offen sc1\n"
"v_sub_f32_e32 v48, v48, v248\n"
"v_sub_f32_e32 v49, v49, v248\n"
"v_sub_f32_e32 v50, v50, v248\n"
"v_mfma_f32_16x16x32_bf16 v[72:75], v[40:43], v[32:35], v[60:63]\n"
"buffer_load_dwordx4 a[36:39], v0, s[4:7], s98 offen offset:1024 sc1\n"
"v_sub_f32_e32 v51, v51, v248\n"
"v_sub_f32_e32 v24, v24, v248\n"
"v_sub_f32_e32 v25, v25, v248\n"
"v_sub_f32_e32 v26, v26, v248\n"
"v_sub_f32_e32 v27, v27, v248\n"
"v_mfma_f32_16x16x32_bf16 v[40:43], v[44:47], v[32:35], v[60:63]\n"
"ds_read_b128 v[76:79], v254 offset:20736\n"
"v_sub_f32_e32 v52, v52, v247\n"
"v_sub_f32_e32 v53, v53, v247\n"
"v_sub_f32_e32 v54, v54, v247\n"
"v_sub_f32_e32 v55, v55, v247\n"
"v_mfma_f32_16x16x32_bf16 v[80:83], v[44:47], v[28:31], v[36:39]\n"
"buffer_load_dwordx4 a[40:43], v0, s[4:7], s98 offen offset:2048 sc1\n"
"v_sub_f32_e32 v56, v56, v247\n"
"v_sub_f32_e32 v57, v57, v247\n"
"v_sub_f32_e32 v58, v58, v247\n"
"v_sub_f32_e32 v59, v59, v247\n"
"s_waitcnt lgkmcnt(0)\n"
"v_mfma_f32_16x16x32_bf16 v[44:47], v[68:71], v[28:31], v[36:39]\n"
"ds_read_b128 v[84:87], v15 offset:18432\n"
"buffer_load_dwordx4 a[44:47], v0, s[4:7], s98 offen offset:3072 sc1\n"
"ds_read_b128 v[100:103], v254 offset:18688\n"
"v_exp_f32_e32 v64, v64\n"
"v_exp_f32_e32 v65, v65\n"
"v_exp_f32_e32 v66, v66\n"
"v_exp_f32_e32 v67, v67\n"
"v_exp_f32_e32 v80, v80\n"
"v_mfma_f32_16x16x32_bf16 v[104:107], v[68:71], v[32:35], v[60:63]\n"
"ds_read_b64_tr_b16 v[68:69], v16 offset:22528\n"
"buffer_load_dwordx4 a[48:51], v1, s[4:7], s98 offen sc1\n"
"v_exp_f32_e32 v81, v81\n"
"v_exp_f32_e32 v82, v82\n"
"v_exp_f32_e32 v83, v83\n"
"v_cvt_pk_bf16_f32 v108, v64, v65\n"
"v_cvt_pk_bf16_f32 v109, v66, v67\n"
"v_cvt_pk_bf16_f32 v110, v80, v81\n"
"v_mfma_f32_16x16x32_bf16 v[64:67], v[76:79], v[32:35], v[60:63]\n"
"ds_read_b64_tr_b16 v[70:71], v255 offset:22784\n"
"ds_read_b64_tr_b16 v[80:81], v255 offset:22528\n"
"buffer_load_dwordx4 a[52:55], v1, s[4:7], s98 offen offset:1024 sc1\n"
"v_cvt_pk_bf16_f32 v111, v82, v83\n"
"v_exp_f32_e32 v72, v72\n"
"v_exp_f32_e32 v73, v73\n"
"v_exp_f32_e32 v74, v74\n"
"v_exp_f32_e32 v75, v75\n"
"v_mfma_f32_16x16x32_bf16 v[112:115], v[76:79], v[28:31], v[36:39]\n"
"ds_read_b64_tr_b16 v[82:83], v16 offset:22784\n"
"v_exp_f32_e32 v40, v40\n"
"v_exp_f32_e32 v41, v41\n"
"v_exp_f32_e32 v42, v42\n"
"v_exp_f32_e32 v43, v43\n"
"v_cvt_pk_bf16_f32 v76, v72, v73\n"
"v_cvt_pk_bf16_f32 v77, v74, v75\n"
"v_cvt_pk_bf16_f32 v78, v40, v41\n"
"v_cvt_pk_bf16_f32 v79, v42, v43\n"
"s_waitcnt lgkmcnt(4)\n"
"v_mfma_f32_16x16x32_bf16 v[40:43], v[84:87], v[28:31], v[36:39]\n"
"ds_read_b128 v[72:75], v15 offset:16384\n"
"v_exp_f32_e32 v44, v44\n"
"v_exp_f32_e32 v45, v45\n"
"v_exp_f32_e32 v46, v46\n"
"v_mfma_f32_16x16x32_bf16 v[116:119], v[84:87], v[32:35], v[60:63]\n"
"buffer_load_dwordx4 a[56:59], v1, s[4:7], s98 offen offset:2048 sc1\n"
"v_exp_f32_e32 v47, v47\n"
"v_mfma_f32_16x16x32_bf16 v[84:87], v[100:103], v[32:35], v[60:63]\n"
"ds_read_b128 v[120:123], v254 offset:16640\n"
"v_exp_f32_e32 v112, v112\n"
"v_exp_f32_e32 v113, v113\n"
"v_mfma_f32_16x16x32_bf16 v[124:127], v[100:103], v[28:31], v[36:39]\n"
"ds_read_b64_tr_b16 v[100:101], v16 offset:20480\n"
"v_exp_f32_e32 v114, v114\n"
"v_exp_f32_e32 v115, v115\n"
"v_cvt_pk_bf16_f32 v128, v44, v45\n"
"s_waitcnt lgkmcnt(3)\n"
"v_mfma_f32_16x16x32_bf16 v[132:135], v[88:91], v[108:111], 0\n"
"buffer_load_dwordx4 a[60:63], v1, s[4:7], s98 offen offset:3072 sc1\n"
"v_cvt_pk_bf16_f32 v129, v46, v47\n"
"v_cvt_pk_bf16_f32 v130, v112, v113\n"
"v_cvt_pk_bf16_f32 v131, v114, v115\n"
"v_mfma_f32_16x16x32_bf16 v[44:47], v[68:71], v[108:111], 0\n"
"ds_read_b64_tr_b16 v[102:103], v255 offset:20736\n"
"v_exp_f32_e32 v104, v104\n"
"v_exp_f32_e32 v105, v105\n"
"v_mfma_f32_16x16x32_bf16 v[112:115], v[80:83], v[108:111], 0\n"
"ds_read_b64_tr_b16 v[108:109], v255 offset:20480\n"
"s_add_u32 s98, s69, 0x604000\n"
"buffer_load_dwordx4 a[64:67], v0, s[4:7], s98 offen sc1\n"
"v_exp_f32_e32 v106, v106\n"
"v_exp_f32_e32 v107, v107\n"
"v_mfma_f32_16x16x32_bf16 v[136:139], v[80:83], v[76:79], 0\n"
"v_exp_f32_e32 v64, v64\n"
"v_exp_f32_e32 v65, v65\n"
"v_mfma_f32_16x16x32_bf16 v[80:83], v[68:71], v[76:79], 0\n"
"ds_read_b64_tr_b16 v[110:111], v16 offset:20736\n"
"v_exp_f32_e32 v66, v66\n"
"v_exp_f32_e32 v67, v67\n"
"v_mfma_f32_16x16x32_bf16 v[68:71], v[88:91], v[76:79], 0\n"
"s_waitcnt vmcnt(19)\n"
"v_cvt_pk_bf16_f32 v76, v104, v105\n"
"v_cvt_pk_bf16_f32 v77, v106, v107\n"
"v_cvt_pk_bf16_f32 v78, v64, v65\n"
"v_cvt_pk_bf16_f32 v79, v66, v67\n"
"s_cmp_lg_u32 s2, 0\n"
"s_cbranch_scc1 .Lmk_s98\n"
"s_barrier\n"
".Lmk_s98:\n"
"s_waitcnt lgkmcnt(4)\n"
"v_mfma_f32_16x16x32_bf16 v[64:67], v[72:75], v[28:31], v[36:39]\n"
"ds_read_b128 v[104:107], v15 offset:14336\n"
"v_exp_f32_e32 v40, v40\n"
"v_exp_f32_e32 v41, v41\n"
"v_exp_f32_e32 v42, v42\n"
"v_mfma_f32_16x16x32_bf16 v[140:143], v[72:75], v[32:35], v[60:63]\n"
"buffer_load_dwordx4 a[68:71], v0, s[4:7], s98 offen offset:1024 sc1\n"
"v_exp_f32_e32 v43, v43\n"
"v_mfma_f32_16x16x32_bf16 v[72:75], v[120:123], v[32:35], v[60:63]\n"
"ds_read_b128 v[144:147], v254 offset:14592\n"
"v_exp_f32_e32 v124, v124\n"
"v_exp_f32_e32 v125, v125\n"
"v_mfma_f32_16x16x32_bf16 v[148:151], v[120:123], v[28:31], v[36:39]\n"
"ds_read_b64_tr_b16 v[120:121], v16 offset:18432\n"
"v_exp_f32_e32 v126, v126\n"
"v_exp_f32_e32 v127, v127\n"
"s_waitcnt lgkmcnt(3)\n"
"v_mfma_f32_16x16x32_bf16 v[132:135], v[88:91], v[128:131], v[132:135]\n"
"buffer_load_dwordx4 a[72:75], v0, s[4:7], s98 offen offset:2048 sc1\n"
"v_cvt_pk_bf16_f32 v152, v40, v41\n"
"v_cvt_pk_bf16_f32 v153, v42, v43\n"
"v_cvt_pk_bf16_f32 v154, v124, v125\n"
"v_mfma_f32_16x16x32_bf16 v[44:47], v[100:103], v[128:131], v[44:47]\n"
"ds_read_b64_tr_b16 v[122:123], v255 offset:18688\n"
"v_cvt_pk_bf16_f32 v155, v126, v127\n"
"v_exp_f32_e32 v116, v116\n"
"v_exp_f32_e32 v117, v117\n"
"v_mfma_f32_16x16x32_bf16 v[112:115], v[108:111], v[128:131], v[112:115]\n"
"ds_read_b64_tr_b16 v[40:41], v255 offset:18432\n"
"v_exp_f32_e32 v118, v118\n"
"v_exp_f32_e32 v119, v119\n"
"v_mfma_f32_16x16x32_bf16 v[136:139], v[108:111], v[76:79], v[136:139]\n"
"buffer_load_dwordx4 a[76:79], v0, s[4:7], s98 offen offset:3072 sc1\n"
"v_exp_f32_e32 v84, v84\n"
"v_exp_f32_e32 v85, v85\n"
"v_mfma_f32_16x16x32_bf16 v[80:83], v[100:103], v[76:79], v[80:83]\n"
"ds_read_b64_tr_b16 v[42:43], v16 offset:18688\n"
"v_exp_f32_e32 v86, v86\n"
"v_exp_f32_e32 v87, v87\n"
"v_mfma_f32_16x16x32_bf16 v[68:71], v[88:91], v[76:79], v[68:71]\n"
"s_waitcnt lgkmcnt(4)\n"
"v_cvt_pk_bf16_f32 v76, v116, v117\n"
"v_cvt_pk_bf16_f32 v77, v118, v119\n"
"v_cvt_pk_bf16_f32 v78, v84, v85\n"
"v_cvt_pk_bf16_f32 v79, v86, v87\n"
"s_cmp_eq_i32 s78, 8\n"
"s_cbranch_scc1 .Lmk_x99s\n"
".Lmk_x99r:\n"
"v_mfma_f32_16x16x32_bf16 v[84:87], v[104:107], v[28:31], v[36:39]\n"
"ds_read_b128 v[100:103], v15 offset:12288\n"
"v_exp_f32_e32 v64, v64\n"
"v_exp_f32_e32 v65, v65\n"
"v_exp_f32_e32 v66, v66\n"
"v_mfma_f32_16x16x32_bf16 v[108:111], v[104:107], v[32:35], v[60:63]\n"
"buffer_load_dwordx4 a[80:83], v1, s[4:7], s98 offen sc1\n"
"v_exp_f32_e32 v67, v67\n"
"v_mfma_f32_16x16x32_bf16 v[104:107], v[144:147], v[32:35], v[60:63]\n"
"ds_read_b128 v[116:119], v254 offset:12544\n"
"v_exp_f32_e32 v148, v148\n"
"v_exp_f32_e32 v149, v149\n"
"v_mfma_f32_16x16x32_bf16 v[124:127], v[144:147], v[28:31], v[36:39]\n"
"ds_read_b64_tr_b16 v[128:129], v16 offset:16384\n"
"v_exp_f32_e32 v150, v150\n"
"v_exp_f32_e32 v151, v151\n"
"s_waitcnt lgkmcnt(3)\n"
"v_mfma_f32_16x16x32_bf16 v[132:135], v[88:91], v[152:155], v[132:135]\n"
"buffer_load_dwordx4 a[84:87], v1, s[4:7], s98 offen offset:1024 sc1\n"
"v_cvt_pk_bf16_f32 v144, v64, v65\n"
"v_cvt_pk_bf16_f32 v145, v66, v67\n"
"v_cvt_pk_bf16_f32 v146, v148, v149\n"
"v_mfma_f32_16x16x32_bf16 v[44:47], v[120:123], v[152:155], v[44:47]\n"
"ds_read_b64_tr_b16 v[130:131], v255 offset:16640\n"
"v_cvt_pk_bf16_f32 v147, v150, v151\n"
"v_exp_f32_e32 v140, v140\n"
"v_exp_f32_e32 v141, v141\n"
"v_mfma_f32_16x16x32_bf16 v[112:115], v[40:43], v[152:155], v[112:115]\n"
"ds_read_b64_tr_b16 v[64:65], v255 offset:16384\n"
"v_exp_f32_e32 v142, v142\n"
"v_exp_f32_e32 v143, v143\n"
"v_mfma_f32_16x16x32_bf16 v[136:139], v[40:43], v[76:79], v[136:139]\n"
"buffer_load_dwordx4 a[88:91], v1, s[4:7], s98 offen offset:2048 sc1\n"
"v_exp_f32_e32 v72, v72\n"
"v_exp_f32_e32 v73, v73\n"
"v_mfma_f32_16x16x32_bf16 v[80:83], v[120:123], v[76:79], v[80:83]\n"
"ds_read_b64_tr_b16 v[66:67], v16 offset:16640\n"
"v_exp_f32_e32 v74, v74\n"
"v_exp_f32_e32 v75, v75\n"
"v_mfma_f32_16x16x32_bf16 v[68:71], v[88:91], v[76:79], v[68:71]\n"
"s_waitcnt lgkmcnt(4)\n"
"v_cvt_pk_bf16_f32 v40, v140, v141\n"
"v_cvt_pk_bf16_f32 v41, v142, v143\n"
"v_cvt_pk_bf16_f32 v42, v72, v73\n"
"v_cvt_pk_bf16_f32 v43, v74, v75\n"
"s_cmp_eq_i32 s78, 7\n"
"s_cbranch_scc1 .Lmk_x100s\n"
".Lmk_x100r:\n"
"v_mfma_f32_16x16x32_bf16 v[72:75], v[100:103], v[28:31], v[36:39]\n"
"ds_read_b128 v[76:79], v15 offset:10240\n"
"v_exp_f32_e32 v84, v84\n"
"v_exp_f32_e32 v85, v85\n"
"v_exp_f32_e32 v86, v86\n"
"v_mfma_f32_16x16x32_bf16 v[120:123], v[100:103], v[32:35], v[60:63]\n"
"buffer_load_dwordx4 a[92:95], v1, s[4:7], s98 offen offset:3072 sc1\n"
"v_exp_f32_e32 v87, v87\n"
"v_mfma_f32_16x16x32_bf16 v[100:103], v[116:119], v[32:35], v[60:63]\n"
"ds_read_b128 v[140:143], v254 offset:10496\n"
"v_exp_f32_e32 v124, v124\n"
"v_exp_f32_e32 v125, v125\n"
"v_mfma_f32_16x16x32_bf16 v[148:151], v[116:119], v[28:31], v[36:39]\n"
"ds_read_b64_tr_b16 v[116:117], v16 offset:14336\n"
"v_exp_f32_e32 v126, v126\n"
"v_exp_f32_e32 v127, v127\n"
"v_cvt_pk_bf16_f32 v152, v84, v85\n"
"s_waitcnt lgkmcnt(3)\n"
"v_mfma_f32_16x16x32_bf16 v[132:135], v[88:91], v[144:147], v[132:135]\n"
"s_add_u32 s98, s69, 0x606000\n"
"buffer_load_dwordx4 a[96:99], v0, s[4:7], s98 offen sc1\n"
"v_cvt_pk_bf16_f32 v153, v86, v87\n"
"v_cvt_pk_bf16_f32 v154, v124, v125\n"
"v_mfma_f32_16x16x32_bf16 v[44:47], v[128:131], v[144:147], v[44:47]\n"
"ds_read_b64_tr_b16 v[118:119], v255 offset:14592\n"
"v_cvt_pk_bf16_f32 v155, v126, v127\n"
"v_exp_f32_e32 v108, v108\n"
"v_exp_f32_e32 v109, v109\n"
"v_mfma_f32_16x16x32_bf16 v[112:115], v[64:67], v[144:147], v[112:115]\n"
"ds_read_b64_tr_b16 v[84:85], v255 offset:14336\n"
"v_exp_f32_e32 v110, v110\n"
"v_exp_f32_e32 v111, v111\n"
"v_mfma_f32_16x16x32_bf16 v[136:139], v[64:67], v[40:43], v[136:139]\n"
"buffer_load_dwordx4 a[100:103], v0, s[4:7], s98 offen offset:1024 sc1\n"
"v_exp_f32_e32 v104, v104\n"
"v_exp_f32_e32 v105, v105\n"
"v_mfma_f32_16x16x32_bf16 v[80:83], v[128:131], v[40:43], v[80:83]\n"
"ds_read_b64_tr_b16 v[86:87], v16 offset:14592\n"
"v_exp_f32_e32 v106, v106\n"
"v_exp_f32_e32 v107, v107\n"
"v_mfma_f32_16x16x32_bf16 v[68:71], v[88:91], v[40:43], v[68:71]\n"
"s_waitcnt lgkmcnt(4)\n"
"v_cvt_pk_bf16_f32 v40, v108, v109\n"
"v_cvt_pk_bf16_f32 v41, v110, v111\n"
"v_cvt_pk_bf16_f32 v42, v104, v105\n"
"v_cvt_pk_bf16_f32 v43, v106, v107\n"
"s_cmp_eq_i32 s78, 6\n"
"s_cbranch_scc1 .Lmk_x101s\n"
".Lmk_x101r:\n"
"v_mfma_f32_16x16x32_bf16 v[64:67], v[76:79], v[28:31], v[36:39]\n"
"ds_read_b128 v[104:107], v15 offset:8192\n"
"v_exp_f32_e32 v72, v72\n"
"v_exp_f32_e32 v73, v73\n"
"v_exp_f32_e32 v74, v74\n"
"v_mfma_f32_16x16x32_bf16 v[108:111], v[76:79], v[32:35], v[60:63]\n"
"buffer_load_dwordx4 a[104:107], v0, s[4:7], s98 offen offset:2048 sc1\n"
"v_exp_f32_e32 v75, v75\n"
"v_mfma_f32_16x16x32_bf16 v[76:79], v[140:143], v[32:35], v[60:63]\n"
"ds_read_b128 v[124:127], v254 offset:8448\n"
"v_exp_f32_e32 v148, v148\n"
"v_exp_f32_e32 v149, v149\n"
"v_mfma_f32_16x16x32_bf16 v[128:131], v[140:143], v[28:31], v[36:39]\n"
"ds_read_b64_tr_b16 v[140:141], v16 offset:12288\n"
"v_exp_f32_e32 v150, v150\n"
"v_exp_f32_e32 v151, v151\n"
"s_waitcnt lgkmcnt(3)\n"
"v_mfma_f32_16x16x32_bf16 v[132:135], v[88:91], v[152:155], v[132:135]\n"
"buffer_load_dwordx4 a[108:111], v0, s[4:7], s98 offen offset:3072 sc1\n"
"v_cvt_pk_bf16_f32 v144, v72, v73\n"
"v_cvt_pk_bf16_f32 v145, v74, v75\n"
"v_cvt_pk_bf16_f32 v146, v148, v149\n"
"v_mfma_f32_16x16x32_bf16 v[44:47], v[116:119], v[152:155], v[44:47]\n"
"ds_read_b64_tr_b16 v[142:143], v255 offset:12544\n"
"v_cvt_pk_bf16_f32 v147, v150, v151\n"
"v_exp_f32_e32 v120, v120\n"
"v_exp_f32_e32 v121, v121\n"
"v_mfma_f32_16x16x32_bf16 v[112:115], v[84:87], v[152:155], v[112:115]\n"
"ds_read_b64_tr_b16 v[72:73], v255 offset:12288\n"
"v_exp_f32_e32 v122, v122\n"
"v_exp_f32_e32 v123, v123\n"
"v_mfma_f32_16x16x32_bf16 v[136:139], v[84:87], v[40:43], v[136:139]\n"
"buffer_load_dwordx4 a[112:115], v1, s[4:7], s98 offen sc1\n"
"v_exp_f32_e32 v100, v100\n"
"v_exp_f32_e32 v101, v101\n"
"v_mfma_f32_16x16x32_bf16 v[80:83], v[116:119], v[40:43], v[80:83]\n"
"ds_read_b64_tr_b16 v[74:75], v16 offset:12544\n"
"v_exp_f32_e32 v102, v102\n"
"v_exp_f32_e32 v103, v103\n"
"v_mfma_f32_16x16x32_bf16 v[68:71], v[88:91], v[40:43], v[68:71]\n"
"s_waitcnt lgkmcnt(4)\n"
"v_cvt_pk_bf16_f32 v40, v120, v121\n"
"v_cvt_pk_bf16_f32 v41, v122, v123\n"
"v_cvt_pk_bf16_f32 v42, v100, v101\n"
"v_cvt_pk_bf16_f32 v43, v102, v103\n"
"s_cmp_eq_i32 s78, 5\n"
"s_cbranch_scc1 .Lmk_x102s\n"
".Lmk_x102r:\n"
"v_mfma_f32_16x16x32_bf16 v[84:87], v[104:107], v[28:31], v[36:39]\n"
"ds_read_b64_tr_b16 v[100:101], v16 offset:10240\n"
"ds_read_b64_tr_b16 v[102:103], v255 offset:10496\n"
"v_exp_f32_e32 v64, v64\n"
"v_exp_f32_e32 v65, v65\n"
"v_mfma_f32_16x16x32_bf16 v[116:119], v[104:107], v[32:35], v[60:63]\n"
"buffer_load_dwordx4 a[116:119], v1, s[4:7], s98 offen offset:1024 sc1\n"
"v_exp_f32_e32 v66, v66\n"
"v_exp_f32_e32 v67, v67\n"
"v_mfma_f32_16x16x32_bf16 v[104:107], v[124:127], v[32:35], v[60:63]\n"
"ds_read_b64_tr_b16 v[60:61], v255 offset:10240\n"
"ds_read_b64_tr_b16 v[62:63], v16 offset:10496\n"
"v_exp_f32_e32 v128, v128\n"
"v_exp_f32_e32 v129, v129\n"
"v_mfma_f32_16x16x32_bf16 v[120:123], v[124:127], v[28:31], v[36:39]\n"
"ds_read_b64_tr_b16 v[36:37], v16 offset:24576\n"
"v_exp_f32_e32 v130, v130\n"
"v_exp_f32_e32 v131, v131\n"
"s_waitcnt lgkmcnt(5)\n"
"v_mfma_f32_16x16x32_bf16 v[132:135], v[88:91], v[144:147], v[132:135]\n"
"buffer_load_dwordx4 a[120:123], v1, s[4:7], s98 offen offset:2048 sc1\n"
"ds_read_b64_tr_b16 v[38:39], v255 offset:24832\n"
"v_cvt_pk_bf16_f32 v124, v64, v65\n"
"v_cvt_pk_bf16_f32 v125, v66, v67\n"
"v_cvt_pk_bf16_f32 v126, v128, v129\n"
"v_mfma_f32_16x16x32_bf16 v[44:47], v[140:143], v[144:147], v[44:47]\n"
"ds_read_b64_tr_b16 v[64:65], v255 offset:24576\n"
"v_cvt_pk_bf16_f32 v127, v130, v131\n"
"v_exp_f32_e32 v108, v108\n"
"v_exp_f32_e32 v109, v109\n"
"v_mfma_f32_16x16x32_bf16 v[112:115], v[72:75], v[144:147], v[112:115]\n"
"ds_read_b64_tr_b16 v[66:67], v16 offset:24832\n"
"v_exp_f32_e32 v110, v110\n"
"v_exp_f32_e32 v111, v111\n"
"v_mfma_f32_16x16x32_bf16 v[136:139], v[72:75], v[40:43], v[136:139]\n"
"buffer_load_dwordx4 a[124:127], v1, s[4:7], s98 offen offset:3072 sc1\n"
"v_exp_f32_e32 v76, v76\n"
"v_add_u32_e32 v248, 0x26400, v0\n"
"v_exp_f32_e32 v77, v77\n"
"v_mfma_f32_16x16x32_bf16 v[80:83], v[140:143], v[40:43], v[80:83]\n"
"ds_read_b128 v[72:75], v248\n"
"v_exp_f32_e32 v78, v78\n"
"v_exp_f32_e32 v79, v79\n"
"v_mfma_f32_16x16x32_bf16 v[68:71], v[88:91], v[40:43], v[68:71]\n"
"ds_read_b128 v[128:131], v248 offset:1024\n"
"v_cvt_pk_bf16_f32 v40, v108, v109\n"
"v_cvt_pk_bf16_f32 v41, v110, v111\n"
"v_cvt_pk_bf16_f32 v42, v76, v77\n"
"v_cvt_pk_bf16_f32 v43, v78, v79\n"
"s_waitcnt lgkmcnt(6)\n"
"v_mfma_f32_16x16x32_bf16 v[132:135], v[88:91], v[124:127], v[132:135]\n"
"ds_read_b64_tr_b16 v[76:77], v16 offset:8192\n"
"v_cndmask_b32_e64 v84, v48, v84, s[20:21]\n"
"v_cndmask_b32_e64 v85, v49, v85, s[22:23]\n"
"v_cndmask_b32_e64 v86, v50, v86, s[24:25]\n"
"s_add_u32 m0, s77, 0x4000\n"
"s_add_u32 s98, s65, 0\n"
"buffer_load_dwordx4 v2, s[4:7], s98 offen sc1 lds\n"
"buffer_load_dwordx4 v2, s[4:7], s98 offen offset:1024 sc1 lds\n"
"v_cndmask_b32_e64 v87, v51, v87, s[26:27]\n"
"v_cndmask_b32_e64 v120, v24, v120, s[28:29]\n"
"v_mfma_f32_16x16x32_bf16 v[44:47], v[100:103], v[124:127], v[44:47]\n"
"ds_read_b64_tr_b16 v[78:79], v255 offset:8448\n"
"v_cndmask_b32_e64 v121, v25, v121, s[30:31]\n"
"v_cndmask_b32_e64 v122, v26, v122, s[32:33]\n"
"v_cndmask_b32_e64 v123, v27, v123, s[34:35]\n"
"v_exp_f32_e32 v84, v84\n"
"v_exp_f32_e32 v85, v85\n"
"v_exp_f32_e32 v86, v86\n"
"v_exp_f32_e32 v87, v87\n"
"v_mfma_f32_16x16x32_bf16 v[112:115], v[60:63], v[124:127], v[112:115]\n"
"v_exp_f32_e32 v120, v120\n"
"v_exp_f32_e32 v121, v121\n"
"v_exp_f32_e32 v122, v122\n"
"v_exp_f32_e32 v123, v123\n"
"v_cvt_pk_bf16_f32 v48, v84, v85\n"
"v_cvt_pk_bf16_f32 v49, v86, v87\n"
"v_cvt_pk_bf16_f32 v50, v120, v121\n"
"v_mfma_f32_16x16x32_bf16 v[136:139], v[60:63], v[40:43], v[136:139]\n"
"ds_read_b64_tr_b16 v[60:61], v255 offset:8192\n"
"s_add_u32 s98, s69, 0x608000\n"
"buffer_load_dwordx4 v[24:27], v0, s[4:7], s98 offen sc1\n"
"v_cvt_pk_bf16_f32 v51, v122, v123\n"
"v_cndmask_b32_e64 v116, v52, v116, s[36:37]\n"
"v_cndmask_b32_e64 v117, v53, v117, s[38:39]\n"
"v_cndmask_b32_e64 v118, v54, v118, s[40:41]\n"
"v_cndmask_b32_e64 v119, v55, v119, s[42:43]\n"
"v_cndmask_b32_e64 v104, v56, v104, s[44:45]\n"
"v_cndmask_b32_e64 v105, v57, v105, s[46:47]\n"
"v_mfma_f32_16x16x32_bf16 v[80:83], v[100:103], v[40:43], v[80:83]\n"
"ds_read_b64_tr_b16 v[62:63], v16 offset:8448\n"
"v_cndmask_b32_e64 v106, v58, v106, s[48:49]\n"
"v_cndmask_b32_e64 v107, v59, v107, s[50:51]\n"
"v_exp_f32_e32 v116, v116\n"
"v_exp_f32_e32 v117, v117\n"
"v_exp_f32_e32 v118, v118\n"
"v_exp_f32_e32 v119, v119\n"
"v_exp_f32_e32 v104, v104\n"
"v_mfma_f32_16x16x32_bf16 v[68:71], v[88:91], v[40:43], v[68:71]\n"
"s_waitcnt lgkmcnt(0)\n"
"v_exp_f32_e32 v105, v105\n"
"v_exp_f32_e32 v106, v106\n"
"v_exp_f32_e32 v107, v107\n"
"v_cvt_pk_bf16_f32 v40, v116, v117\n"
"v_cvt_pk_bf16_f32 v41, v118, v119\n"
"v_cvt_pk_bf16_f32 v42, v104, v105\n"
"v_cvt_pk_bf16_f32 v43, v106, v107\n"
"v_mfma_f32_16x16x32_bf16 v[132:135], v[88:91], v[48:51], v[132:135]\n"
"v_and_b32_e32 v72, v48, v72\n"
"v_and_b32_e32 v73, v49, v73\n"
"v_and_b32_e32 v74, v50, v74\n"
"v_and_b32_e32 v75, v51, v75\n"
"v_mfma_f32_16x16x32_bf16 v[68:71], v[88:91], v[40:43], v[68:71]\n"
"v_xor_b32_e32 v52, v48, v72\n"
"v_xor_b32_e32 v53, v49, v73\n"
"v_xor_b32_e32 v54, v50, v74\n"
"v_xor_b32_e32 v55, v51, v75\n"
"v_mfma_f32_16x16x32_bf16 v[44:47], v[76:79], v[72:75], v[44:47]\n"
"v_and_b32_e32 v128, v40, v128\n"
"v_and_b32_e32 v129, v41, v129\n"
"v_mfma_f32_16x16x32_bf16 v[112:115], v[60:63], v[72:75], v[112:115]\n"
"v_and_b32_e32 v130, v42, v130\n"
"v_and_b32_e32 v131, v43, v131\n"
"v_mfma_f32_16x16x32_bf16 v[44:47], v[36:39], v[52:55], v[44:47]\n"
"v_xor_b32_e32 v48, v40, v128\n"
"v_xor_b32_e32 v49, v41, v129\n"
"v_mfma_f32_16x16x32_bf16 v[112:115], v[64:67], v[52:55], v[112:115]\n"
"v_xor_b32_e32 v50, v42, v130\n"
"v_xor_b32_e32 v51, v43, v131\n"
"v_mfma_f32_16x16x32_bf16 v[80:83], v[76:79], v[128:131], v[80:83]\n"
"s_add_u32 m0, s77, 0xc000\n"
"s_add_u32 s97, s65, 0x4000\n"
"buffer_load_dwordx4 v2, s[4:7], s97 offen sc1 lds\n"
"buffer_load_dwordx4 v2, s[4:7], s97 offen offset:1024 sc1 lds\n"
"v_mfma_f32_16x16x32_bf16 v[136:139], v[60:63], v[128:131], v[136:139]\n"
"v_mfma_f32_16x16x32_bf16 v[80:83], v[36:39], v[48:51], v[80:83]\n"
"v_mfma_f32_16x16x32_bf16 v[136:139], v[64:67], v[48:51], v[136:139]\n"
"v_max_f32_e32 v248, v132, v68\n"
"v_rcp_f32_e32 v132, v132\n"
"v_rcp_f32_e32 v68, v68\n"
"s_nop 0\n"
"v_max3_f32 v248, v248, v132, v68\n"
"v_cmp_nlt_f32_e32 vcc, v248, v252\n"
"s_cbranch_vccnz .Lmk_b97\n"
".Lmk_k103:\n"
"s_add_u32 s97, s60, 0x40000\n"
"v_mul_f32_e32 v44, v44, v132\n"
"v_mul_f32_e32 v45, v45, v132\n"
"v_mul_f32_e32 v46, v46, v132\n"
"v_mul_f32_e32 v47, v47, v132\n"
"v_mul_f32_e32 v112, v112, v132\n"
"v_mul_f32_e32 v113, v113, v132\n"
"v_mul_f32_e32 v114, v114, v132\n"
"v_mul_f32_e32 v115, v115, v132\n"
"v_cvt_pk_bf16_f32 v28, v44, v45\n"
"v_cvt_pk_bf16_f32 v29, v46, v47\n"
"v_cvt_pk_bf16_f32 v30, v112, v113\n"
"v_cvt_pk_bf16_f32 v31, v114, v115\n"
"buffer_store_dwordx4 v[28:31], v0, s[4:7], s97 offen sc1\n"
"s_add_u32 s97, s60, 0x48000\n"
"v_mul_f32_e32 v80, v80, v68\n"
"v_mul_f32_e32 v81, v81, v68\n"
"v_mul_f32_e32 v82, v82, v68\n"
"v_mul_f32_e32 v83, v83, v68\n"
"v_mul_f32_e32 v136, v136, v68\n"
"v_mul_f32_e32 v137, v137, v68\n"
"v_mul_f32_e32 v138, v138, v68\n"
"v_mul_f32_e32 v139, v139, v68\n"
"v_cvt_pk_bf16_f32 v28, v80, v81\n"
"v_cvt_pk_bf16_f32 v29, v82, v83\n"
"v_cvt_pk_bf16_f32 v30, v136, v137\n"
"v_cvt_pk_bf16_f32 v31, v138, v139\n"
"buffer_store_dwordx4 v[28:31], v0, s[4:7], s97 offen sc1\n"
";\n"
"; @A AT a\n"
"s_mov_b32 s2, 0\n"
"v_mov_b32_e32 v253, 0xff800000\n"
"v_mov_b32_e32 v251, 0xff800000\n"
"v_mov_b32_e32 v250, 0x7f800000\n"
"v_mov_b32_e32 v249, 0x7f800000\n"
".Lmk_a104:\n"
"ds_read_b128 v[28:31], v15 offset:16384\n"
"ds_read_b128 v[32:35], v254 offset:16640\n"
"ds_read_b128 v[36:39], v15 offset:14336\n"
"ds_read_b128 v[40:43], v254 offset:14592\n"
"s_cmp_gt_i32 s78, 7\n"
"s_cselect_b32 s97, 0x80000000, 0\n"
"s_or_b32 s97, s97, 0x7f800000\n"
"s_add_u32 s96, s67, 32\n"
"buffer_load_dwordx2 v[246:247], v12, s[4:7], s96 offen sc1\n"
"s_waitcnt lgkmcnt(2)\n"
"v_mfma_f32_16x16x32_bf16 v[100:103], v[28:31], v[92:95], 0\n"
"v_mfma_f32_16x16x32_bf16 v[104:107], v[28:31], v[96:99], 0\n"
"v_mfma_f32_16x16x32_bf16 v[108:111], v[32:35], v[96:99], 0\n"
"v_mfma_f32_16x16x32_bf16 v[112:115], v[32:35], v[92:95], 0\n"
"s_waitcnt lgkmcnt(0)\n"
"s_nop 3\n"
"v_max_f32_e32 v248, v100, v101\n"
"v_max_f32_e32 v245, v104, v105\n"
"v_max3_f32 v248, v248, v102, v103\n"
"v_max3_f32 v245, v245, v106, v107\n"
"v_max3_f32 v248, v248, v112, v113\n"
"v_max3_f32 v245, v245, v108, v109\n"
"v_max3_f32 v248, v248, v114, v115\n"
"v_max3_f32 v245, v245, v110, v111\n"
"v_mov_b32_e32 v244, v248\n"
"v_mov_b32_e32 v243, v245\n"
"s_nop 0\n"
"v_permlane16_swap_b32_e32 v248, v244\n"
"v_permlane16_swap_b32_e32 v245, v243\n"
"v_max_f32_e32 v248, v248, v244\n"
"v_max_f32_e32 v245, v245, v243\n"
"v_mov_b32_e32 v244, v248\n"
"v_mov_b32_e32 v243, v245\n"
"s_nop 0\n"
"v_permlane32_swap_b32_e32 v248, v244\n"
"v_permlane32_swap_b32_e32 v245, v243\n"
"v_max_f32_e32 v248, v248, v244\n"
"v_max_f32_e32 v245, v245, v243\n"
"v_med3_f32 v248, v248, v253, v250\n"
"v_med3_f32 v245, v245, v251, v249\n"
"v_min_f32_e64 v116, s97, -v248\n"
"v_min_f32_e64 v117, s97, -v248\n"
"v_min_f32_e64 v118, s97, -v248\n"
"v_min_f32_e64 v119, s97, -v248\n"
"v_min_f32_e64 v120, s97, -v245\n"
"v_min_f32_e64 v121, s97, -v245\n"
"v_min_f32_e64 v122, s97, -v245\n"
"v_min_f32_e64 v123, s97, -v245\n"
"v_mfma_f32_16x16x32_bf16 v[44:47], v[36:39], v[92:95], v[116:119]\n"
"ds_read_b128 v[32:35], v15 offset:12288\n"
"v_sub_f32_e32 v100, v100, v248\n"
"v_sub_f32_e32 v101, v101, v248\n"
"v_sub_f32_e32 v102, v102, v248\n"
"v_sub_f32_e32 v103, v103, v248\n"
"v_mfma_f32_16x16x32_bf16 v[48:51], v[36:39], v[96:99], v[120:123]\n"
"v_sub_f32_e32 v112, v112, v248\n"
"v_sub_f32_e32 v113, v113, v248\n"
"v_sub_f32_e32 v114, v114, v248\n"
"v_sub_f32_e32 v115, v115, v248\n"
"v_mfma_f32_16x16x32_bf16 v[52:55], v[40:43], v[96:99], v[120:123]\n"
"ds_read_b128 v[56:59], v254 offset:12544\n"
"v_sub_f32_e32 v104, v104, v245\n"
"v_sub_f32_e32 v105, v105, v245\n"
"v_sub_f32_e32 v106, v106, v245\n"
"v_sub_f32_e32 v107, v107, v245\n"
"v_mfma_f32_16x16x32_bf16 v[36:39], v[40:43], v[92:95], v[116:119]\n"
"s_waitcnt lgkmcnt(0)\n"
"v_sub_f32_e32 v108, v108, v245\n"
"v_sub_f32_e32 v109, v109, v245\n"
"v_sub_f32_e32 v110, v110, v245\n"
"v_sub_f32_e32 v111, v111, v245\n"
"s_cmp_eq_i32 s78, 7\n"
"s_cbranch_scc1 .Lmk_x106s\n"
".Lmk_x106r:\n"
"v_mfma_f32_16x16x32_bf16 v[60:63], v[32:35], v[92:95], v[116:119]\n"
"ds_read_b128 v[40:43], v15 offset:10240\n"
"buffer_load_dwordx4 v[28:31], v0, s[4:7], s98 offen offset:1024 sc1\n"
"ds_read_b128 v[64:67], v254 offset:10496\n"
"v_exp_f32_e32 v44, v44\n"
"v_exp_f32_e32 v45, v45\n"
"v_exp_f32_e32 v46, v46\n"
"v_exp_f32_e32 v47, v47\n"
"v_exp_f32_e32 v36, v36\n"
"v_mfma_f32_16x16x32_bf16 v[68:71], v[32:35], v[96:99], v[120:123]\n"
"ds_read_b64_tr_b16 v[72:73], v16 offset:14336\n"
"buffer_load_dwordx4 v[32:35], v0, s[4:7], s98 offen offset:2048 sc1\n"
"v_exp_f32_e32 v37, v37\n"
"v_exp_f32_e32 v38, v38\n"
"v_exp_f32_e32 v39, v39\n"
"v_cvt_pk_bf16_f32 v76, v44, v45\n"
"v_cvt_pk_bf16_f32 v77, v46, v47\n"
"v_cvt_pk_bf16_f32 v78, v36, v37\n"
"v_mfma_f32_16x16x32_bf16 v[80:83], v[56:59], v[96:99], v[120:123]\n"
"ds_read_b64_tr_b16 v[74:75], v255 offset:14592\n"
"ds_read_b64_tr_b16 v[84:85], v255 offset:14336\n"
"v_cvt_pk_bf16_f32 v79, v38, v39\n"
"v_exp_f32_e32 v48, v48\n"
"v_exp_f32_e32 v49, v49\n"
"v_exp_f32_e32 v50, v50\n"
"v_exp_f32_e32 v51, v51\n"
"v_mfma_f32_16x16x32_bf16 v[124:127], v[56:59], v[92:95], v[116:119]\n"
"buffer_load_dwordx4 v[36:39], v0, s[4:7], s98 offen offset:3072 sc1\n"
"ds_read_b64_tr_b16 v[86:87], v16 offset:14592\n"
"v_exp_f32_e32 v52, v52\n"
"v_exp_f32_e32 v53, v53\n"
"v_exp_f32_e32 v54, v54\n"
"v_exp_f32_e32 v55, v55\n"
"v_cvt_pk_bf16_f32 v56, v48, v49\n"
"v_cvt_pk_bf16_f32 v57, v50, v51\n"
"v_cvt_pk_bf16_f32 v58, v52, v53\n"
"v_cvt_pk_bf16_f32 v59, v54, v55\n"
"s_waitcnt lgkmcnt(4)\n"
"s_cmp_eq_i32 s78, 6\n"
"s_cbranch_scc1 .Lmk_x107s\n"
".Lmk_x107r:\n"
"v_mfma_f32_16x16x32_bf16 v[128:131], v[40:43], v[92:95], v[116:119]\n"
"ds_read_b128 v[132:135], v15 offset:8192\n"
"ds_read_b128 v[136:139], v254 offset:8448\n"
"v_exp_f32_e32 v60, v60\n"
"v_exp_f32_e32 v61, v61\n"
"v_mfma_f32_16x16x32_bf16 v[140:143], v[40:43], v[96:99], v[120:123]\n"
"buffer_load_dwordx4 v[40:43], v1, s[4:7], s98 offen sc1\n"
"v_exp_f32_e32 v62, v62\n"
"v_exp_f32_e32 v63, v63\n"
"v_mfma_f32_16x16x32_bf16 v[144:147], v[64:67], v[96:99], v[120:123]\n"
"ds_read_b64_tr_b16 v[148:149], v16 offset:12288\n"
"ds_read_b64_tr_b16 v[150:151], v255 offset:12544\n"
"v_exp_f32_e32 v124, v124\n"
"v_exp_f32_e32 v125, v125\n"
"v_mfma_f32_16x16x32_bf16 v[152:155], v[64:67], v[92:95], v[116:119]\n"
"ds_read_b64_tr_b16 v[64:65], v255 offset:12288\n"
"v_exp_f32_e32 v126, v126\n"
"v_exp_f32_e32 v127, v127\n"
"s_waitcnt lgkmcnt(5)\n"
"v_mfma_f32_16x16x32_bf16 v[156:159], v[88:91], v[76:79], 0\n"
"buffer_load_dwordx4 v[44:47], v1, s[4:7], s98 offen offset:1024 sc1\n"
"v_cvt_pk_bf16_f32 v160, v60, v61\n"
"v_cvt_pk_bf16_f32 v161, v62, v63\n"
"v_cvt_pk_bf16_f32 v162, v124, v125\n"
"v_cvt_pk_bf16_f32 v163, v126, v127\n"
"v_mfma_f32_16x16x32_bf16 v[124:127], v[72:75], v[76:79], 0\n"
"ds_read_b64_tr_b16 v[66:67], v16 offset:12544\n"
"s_waitcnt vmcnt(8)\n"
"ds_read_b128 v[164:167], v2 offset:16384\n"
"v_exp_f32_e32 v68, v68\n"
"v_exp_f32_e32 v69, v69\n"
"v_mfma_f32_16x16x32_bf16 v[168:171], v[84:87], v[76:79], 0\n"
"v_exp_f32_e32 v70, v70\n"
"v_exp_f32_e32 v71, v71\n"
"v_mfma_f32_16x16x32_bf16 v[172:175], v[84:87], v[56:59], 0\n"
"buffer_load_dwordx4 v[48:51], v1, s[4:7], s98 offen offset:2048 sc1\n"
"ds_read_b128 v[76:79], v2 offset:17408\n"
"v_exp_f32_e32 v80, v80\n"
"v_exp_f32_e32 v81, v81\n"
"v_mfma_f32_16x16x32_bf16 v[176:179], v[72:75], v[56:59], 0\n"
"ds_read_b128 v[72:75], v2 offset:49152\n"
"v_exp_f32_e32 v82, v82\n"
"v_exp_f32_e32 v83, v83\n"
"v_mfma_f32_16x16x32_bf16 v[180:183], v[88:91], v[56:59], 0\n"
"ds_read_b128 v[84:87], v2 offset:50176\n"
"v_cvt_pk_bf16_f32 v184, v68, v69\n"
"v_cvt_pk_bf16_f32 v185, v70, v71\n"
"v_cvt_pk_bf16_f32 v186, v80, v81\n"
"v_cvt_pk_bf16_f32 v187, v82, v83\n"
"s_waitcnt lgkmcnt(8)\n"
"s_cmp_eq_i32 s78, 5\n"
"s_cbranch_scc1 .Lmk_x108s\n"
".Lmk_x108r:\n"
"v_mfma_f32_16x16x32_bf16 v[80:83], v[132:135], v[92:95], v[116:119]\n"
"ds_read_b64_tr_b16 v[68:69], v16 offset:10240\n"
"ds_read_b64_tr_b16 v[70:71], v255 offset:10496\n"
"buffer_load_dwordx4 v[52:55], v1, s[4:7], s98 offen offset:3072 sc1\n"
"v_exp_f32_e32 v128, v128\n"
"v_exp_f32_e32 v129, v129\n"
"v_mfma_f32_16x16x32_bf16 v[188:191], v[132:135], v[96:99], v[120:123]\n"
"ds_read_b64_tr_b16 v[132:133], v255 offset:10240\n"
"v_exp_f32_e32 v130, v130\n"
"v_exp_f32_e32 v131, v131\n"
"v_mfma_f32_16x16x32_bf16 v[192:195], v[136:139], v[96:99], v[120:123]\n"
"ds_read_b64_tr_b16 v[134:135], v16 offset:10496\n"
"v_exp_f32_e32 v152, v152\n"
"v_exp_f32_e32 v153, v153\n"
"v_exp_f32_e32 v154, v154\n"
"v_mfma_f32_16x16x32_bf16 v[196:199], v[136:139], v[92:95], v[116:119]\n"
"s_waitcnt lgkmcnt(4)\n"
"s_add_u32 s97, s69, 0x60a000\n"
"buffer_load_dwordx4 v[56:59], v0, s[4:7], s97 offen sc1\n"
"v_max_u32_e32 v248, v164, v165\n"
"v_exp_f32_e32 v155, v155\n"
"v_mfma_f32_16x16x32_bf16 v[156:159], v[88:91], v[160:163], v[156:159]\n"
"v_max3_u32 v248, v248, v166, v167\n"
"v_cvt_pk_bf16_f32 v136, v128, v129\n"
"v_cvt_pk_bf16_f32 v137, v130, v131\n"
"v_cvt_pk_bf16_f32 v138, v152, v153\n"
"v_mfma_f32_16x16x32_bf16 v[124:127], v[148:151], v[160:163], v[124:127]\n"
"v_max3_u32 v248, v248, v76, v77\n"
"v_cvt_pk_bf16_f32 v139, v154, v155\n"
"v_exp_f32_e32 v140, v140\n"
"v_max3_u32 v248, v248, v78, v79\n"
"v_exp_f32_e32 v141, v141\n"
"v_mfma_f32_16x16x32_bf16 v[168:171], v[64:67], v[160:163], v[168:171]\n"
"v_exp_f32_e32 v142, v142\n"
"v_max3_u32 v248, v248, v72, v73\n"
"v_exp_f32_e32 v143, v143\n"
"v_mfma_f32_16x16x32_bf16 v[172:175], v[64:67], v[184:187], v[172:175]\n"
"buffer_load_dwordx4 v[60:63], v0, s[4:7], s97 offen offset:1024 sc1\n"
"v_max3_u32 v248, v248, v74, v75\n"
"v_exp_f32_e32 v144, v144\n"
"v_exp_f32_e32 v145, v145\n"
"v_mfma_f32_16x16x32_bf16 v[176:179], v[148:151], v[184:187], v[176:179]\n"
"v_max3_u32 v248, v248, v84, v85\n"
"v_exp_f32_e32 v146, v146\n"
"v_max3_u32 v248, v248, v86, v87\n"
"v_exp_f32_e32 v147, v147\n"
"v_mfma_f32_16x16x32_bf16 v[180:183], v[88:91], v[184:187], v[180:183]\n"
"v_cvt_pk_bf16_f32 v128, v140, v141\n"
"v_cmp_eq_u32_e32 vcc, -1, v248\n"
"v_cvt_pk_bf16_f32 v129, v142, v143\n"
"v_cvt_pk_bf16_f32 v130, v144, v145\n"
"v_cvt_pk_bf16_f32 v131, v146, v147\n"
"s_cbranch_vccnz .Lmk_h95s\n"
".Lmk_h95r:\n"
"s_cmp_lg_u32 s2, 0\n"
"s_cbranch_scc1 .Lmk_s109\n"
"s_barrier\n"
".Lmk_s109:\n"
"ds_read_b128 v[72:75], v15 offset:6144\n"
"ds_read_b128 v[76:79], v254 offset:6400\n"
"s_waitcnt lgkmcnt(2)\n"
"v_mfma_f32_16x16x32_bf16 v[156:159], v[88:91], v[136:139], v[156:159]\n"
"ds_read_b128 v[84:87], v15 offset:4096\n"
"v_exp_f32_e32 v80, v80\n"
"v_exp_f32_e32 v81, v81\n"
"v_exp_f32_e32 v82, v82\n"
"v_mfma_f32_16x16x32_bf16 v[124:127], v[68:71], v[136:139], v[124:127]\n"
"buffer_load_dwordx4 v[64:67], v0, s[4:7], s97 offen offset:2048 sc1\n"
"ds_read_b128 v[140:143], v254 offset:4352\n"
"v_exp_f32_e32 v83, v83\n"
"v_mfma_f32_16x16x32_bf16 v[168:171], v[132:135], v[136:139], v[168:171]\n"
"ds_read_b64_tr_b16 v[136:137], v16 offset:8192\n"
"v_exp_f32_e32 v196, v196\n"
"v_exp_f32_e32 v197, v197\n"
"v_mfma_f32_16x16x32_bf16 v[172:175], v[132:135], v[128:131], v[172:175]\n"
"ds_read_b64_tr_b16 v[138:139], v255 offset:8448\n"
"v_exp_f32_e32 v198, v198\n"
"v_exp_f32_e32 v199, v199\n"
"v_mfma_f32_16x16x32_bf16 v[176:179], v[68:71], v[128:131], v[176:179]\n"
"buffer_load_dwordx4 v[68:71], v0, s[4:7], s97 offen offset:3072 sc1\n"
"v_cvt_pk_bf16_f32 v132, v80, v81\n"
"v_cvt_pk_bf16_f32 v133, v82, v83\n"
"v_cvt_pk_bf16_f32 v134, v196, v197\n"
"v_cvt_pk_bf16_f32 v135, v198, v199\n"
"v_mfma_f32_16x16x32_bf16 v[180:183], v[88:91], v[128:131], v[180:183]\n"
"ds_read_b64_tr_b16 v[128:129], v255 offset:8192\n"
"v_exp_f32_e32 v188, v188\n"
"v_exp_f32_e32 v189, v189\n"
"s_waitcnt lgkmcnt(5)\n"
"v_mfma_f32_16x16x32_bf16 v[144:147], v[72:75], v[92:95], v[116:119]\n"
"ds_read_b64_tr_b16 v[130:131], v16 offset:8448\n"
"v_exp_f32_e32 v190, v190\n"
"v_exp_f32_e32 v191, v191\n"
"v_exp_f32_e32 v192, v192\n"
"v_mfma_f32_16x16x32_bf16 v[148:151], v[72:75], v[96:99], v[120:123]\n"
"buffer_load_dwordx4 v[72:75], v1, s[4:7], s97 offen sc1\n"
"s_waitcnt vmcnt(12)\n"
"v_cmp_ne_u64_e32 vcc, v[246:247], v[18:19]\n"
"v_mfma_f32_16x16x32_bf16 v[152:155], v[76:79], v[96:99], v[120:123]\n"
"v_exp_f32_e32 v193, v193\n"
"v_exp_f32_e32 v194, v194\n"
"v_exp_f32_e32 v195, v195\n"
"v_mfma_f32_16x16x32_bf16 v[160:163], v[76:79], v[92:95], v[116:119]\n"
"s_cbranch_vccnz .Lmk_f79s\n"
".Lmk_f79r:\n"
"v_cvt_pk_bf16_f32 v164, v188, v189\n"
"v_cvt_pk_bf16_f32 v165, v190, v191\n"
"v_cvt_pk_bf16_f32 v166, v192, v193\n"
"v_cvt_pk_bf16_f32 v167, v194, v195\n"
"s_waitcnt lgkmcnt(4)\n"
"v_mfma_f32_16x16x32_bf16 v[184:187], v[84:87], v[92:95], v[116:119]\n"
"ds_read_b128 v[188:191], v15 offset:2048\n"
"v_exp_f32_e32 v144, v144\n"
"v_exp_f32_e32 v145, v145\n"
"v_exp_f32_e32 v146, v146\n"
"v_mfma_f32_16x16x32_bf16 v[192:195], v[84:87], v[96:99], v[120:123]\n"
"buffer_load_dwordx4 v[76:79], v1, s[4:7], s97 offen offset:1024 sc1\n"
"v_exp_f32_e32 v147, v147\n"
"v_mfma_f32_16x16x32_bf16 v[196:199], v[140:143], v[96:99], v[120:123]\n"
"ds_read_b128 v[200:203], v254 offset:2304\n"
"v_exp_f32_e32 v160, v160\n"
"v_exp_f32_e32 v161, v161\n"
"v_mfma_f32_16x16x32_bf16 v[204:207], v[140:143], v[92:95], v[116:119]\n"
"ds_read_b64_tr_b16 v[140:141], v16 offset:6144\n"
"v_exp_f32_e32 v162, v162\n"
"v_exp_f32_e32 v163, v163\n"
"s_waitcnt lgkmcnt(3)\n"
"v_mfma_f32_16x16x32_bf16 v[156:159], v[88:91], v[132:135], v[156:159]\n"
"buffer_load_dwordx4 v[80:83], v1, s[4:7], s97 offen offset:2048 sc1\n"
"v_cvt_pk_bf16_f32 v208, v144, v145\n"
"v_cvt_pk_bf16_f32 v209, v146, v147\n"
"v_cvt_pk_bf16_f32 v210, v160, v161\n"
"v_mfma_f32_16x16x32_bf16 v[124:127], v[136:139], v[132:135], v[124:127]\n"
"ds_read_b64_tr_b16 v[142:143], v255 offset:6400\n"
"v_cvt_pk_bf16_f32 v211, v162, v163\n"
"v_exp_f32_e32 v148, v148\n"
"v_exp_f32_e32 v149, v149\n"
"v_mfma_f32_16x16x32_bf16 v[168:171], v[128:131], v[132:135], v[168:171]\n"
"ds_read_b64_tr_b16 v[132:133], v255 offset:6144\n"
"v_exp_f32_e32 v150, v150\n"
"v_exp_f32_e32 v151, v151\n"
"v_mfma_f32_16x16x32_bf16 v[172:175], v[128:131], v[164:167], v[172:175]\n"
"buffer_load_dwordx4 v[84:87], v1, s[4:7], s97 offen offset:3072 sc1\n"
"v_exp_f32_e32 v152, v152\n"
"v_exp_f32_e32 v153, v153\n"
"v_mfma_f32_16x16x32_bf16 v[176:179], v[136:139], v[164:167], v[176:179]\n"
"ds_read_b64_tr_b16 v[134:135], v16 offset:6400\n"
"v_exp_f32_e32 v154, v154\n"
"v_exp_f32_e32 v155, v155\n"
"v_mfma_f32_16x16x32_bf16 v[180:183], v[88:91], v[164:167], v[180:183]\n"
"s_waitcnt lgkmcnt(4)\n"
"v_cvt_pk_bf16_f32 v128, v148, v149\n"
"v_cvt_pk_bf16_f32 v129, v150, v151\n"
"v_cvt_pk_bf16_f32 v130, v152, v153\n"
"v_cvt_pk_bf16_f32 v131, v154, v155\n"
"v_mfma_f32_16x16x32_bf16 v[136:139], v[188:191], v[92:95], v[116:119]\n"
"ds_read_b128 v[144:147], v15\n"
"v_exp_f32_e32 v184, v184\n"
"v_exp_f32_e32 v185, v185\n"
"v_exp_f32_e32 v186, v186\n"
"v_mfma_f32_16x16x32_bf16 v[148:151], v[188:191], v[96:99], v[120:123]\n"
"s_add_u32 s97, s56, 0x40000\n"
"buffer_load_dwordx4 v[152:155], v0, s[4:7], s97 offen sc1\n"
"v_exp_f32_e32 v187, v187\n"
"v_mfma_f32_16x16x32_bf16 v[160:163], v[200:203], v[96:99], v[120:123]\n"
"ds_read_b128 v[164:167], v254 offset:256\n"
"v_exp_f32_e32 v204, v204\n"
"v_exp_f32_e32 v205, v205\n"
"v_exp_f32_e32 v206, v206\n"
"v_mfma_f32_16x16x32_bf16 v[188:191], v[200:203], v[92:95], v[116:119]\n"
"buffer_load_dwordx4 v[200:203], v0, s[4:7], s97 offen offset:1024 sc1\n"
"ds_read_b64_tr_b16 v[212:213], v16 offset:4096\n"
"v_exp_f32_e32 v207, v207\n"
"s_waitcnt lgkmcnt(3)\n"
"v_mfma_f32_16x16x32_bf16 v[156:159], v[88:91], v[208:211], v[156:159]\n"
"v_cvt_pk_bf16_f32 v216, v184, v185\n"
"v_cvt_pk_bf16_f32 v217, v186, v187\n"
"v_cvt_pk_bf16_f32 v218, v204, v205\n"
"v_cvt_pk_bf16_f32 v219, v206, v207\n"
"v_mfma_f32_16x16x32_bf16 v[124:127], v[140:143], v[208:211], v[124:127]\n"
"buffer_load_dwordx4 v[184:187], v0, s[4:7], s97 offen offset:2048 sc1\n"
"ds_read_b64_tr_b16 v[214:215], v255 offset:4352\n"
"v_exp_f32_e32 v192, v192\n"
"v_exp_f32_e32 v193, v193\n"
"v_mfma_f32_16x16x32_bf16 v[168:171], v[132:135], v[208:211], v[168:171]\n"
"buffer_load_dwordx4 v[204:207], v0, s[4:7], s97 offen offset:3072 sc1\n"
"v_exp_f32_e32 v194, v194\n"
"v_exp_f32_e32 v195, v195\n"
"v_mfma_f32_16x16x32_bf16 v[172:175], v[132:135], v[128:131], v[172:175]\n"
"ds_read_b64_tr_b16 v[132:133], v255 offset:4096\n"
"v_exp_f32_e32 v196, v196\n"
"v_exp_f32_e32 v197, v197\n"
"v_mfma_f32_16x16x32_bf16 v[176:179], v[140:143], v[128:131], v[176:179]\n"
"buffer_load_dwordx4 v[140:143], v1, s[4:7], s97 offen sc1\n"
"ds_read_b64_tr_b16 v[134:135], v16 offset:4352\n"
"v_exp_f32_e32 v198, v198\n"
"v_exp_f32_e32 v199, v199\n"
"v_mfma_f32_16x16x32_bf16 v[180:183], v[88:91], v[128:131], v[180:183]\n"
"s_waitcnt lgkmcnt(4)\n"
"v_cvt_pk_bf16_f32 v128, v192, v193\n"
"v_cvt_pk_bf16_f32 v129, v194, v195\n"
"v_cvt_pk_bf16_f32 v130, v196, v197\n"
"v_cvt_pk_bf16_f32 v131, v198, v199\n"
"v_mfma_f32_16x16x32_bf16 v[192:195], v[144:147], v[92:95], v[116:119]\n"
"ds_read_b64_tr_b16 v[196:197], v16 offset:2048\n"
"ds_read_b64_tr_b16 v[198:199], v255 offset:2304\n"
"buffer_load_dwordx4 v[208:211], v1, s[4:7], s97 offen offset:1024 sc1\n"
"v_exp_f32_e32 v136, v136\n"
"v_exp_f32_e32 v137, v137\n"
"v_mfma_f32_16x16x32_bf16 v[220:223], v[144:147], v[96:99], v[120:123]\n"
"ds_read_b64_tr_b16 v[144:145], v255 offset:2048\n"
"v_exp_f32_e32 v138, v138\n"
"v_exp_f32_e32 v139, v139\n"
"v_mfma_f32_16x16x32_bf16 v[224:227], v[164:167], v[96:99], v[120:123]\n"
"buffer_load_dwordx4 v[120:123], v1, s[4:7], s97 offen offset:2048 sc1\n"
"v_exp_f32_e32 v188, v188\n"
"v_exp_f32_e32 v189, v189\n"
"v_mfma_f32_16x16x32_bf16 v[228:231], v[164:167], v[92:95], v[116:119]\n"
"ds_read_b64_tr_b16 v[146:147], v16 offset:2304\n"
"ds_read_b64_tr_b16 v[116:117], v16 offset:16384\n"
"v_exp_f32_e32 v190, v190\n"
"v_exp_f32_e32 v191, v191\n"
"s_waitcnt lgkmcnt(5)\n"
"v_mfma_f32_16x16x32_bf16 v[156:159], v[88:91], v[216:219], v[156:159]\n"
"v_cvt_pk_bf16_f32 v164, v136, v137\n"
"v_cvt_pk_bf16_f32 v165, v138, v139\n"
"v_cvt_pk_bf16_f32 v166, v188, v189\n"
"v_mfma_f32_16x16x32_bf16 v[124:127], v[212:215], v[216:219], v[124:127]\n"
"buffer_load_dwordx4 v[136:139], v1, s[4:7], s97 offen offset:3072 sc1\n"
"ds_read_b64_tr_b16 v[118:119], v255 offset:16640\n"
"ds_read_b64_tr_b16 v[188:189], v255 offset:16384\n"
"v_cvt_pk_bf16_f32 v167, v190, v191\n"
"v_exp_f32_e32 v148, v148\n"
"v_exp_f32_e32 v149, v149\n"
"v_exp_f32_e32 v150, v150\n"
"v_mfma_f32_16x16x32_bf16 v[168:171], v[132:135], v[216:219], v[168:171]\n"
"s_add_u32 s97, s57, 0x40000\n"
"buffer_load_dwordx4 v[216:219], v0, s[4:7], s97 offen sc1\n"
"ds_read_b64_tr_b16 v[190:191], v16 offset:16640\n"
"v_exp_f32_e32 v151, v151\n"
"v_mfma_f32_16x16x32_bf16 v[172:175], v[132:135], v[128:131], v[172:175]\n"
"v_exp_f32_e32 v160, v160\n"
"v_add_u32_e32 v248, 0x26400, v0\n"
"v_exp_f32_e32 v161, v161\n"
"v_mfma_f32_16x16x32_bf16 v[176:179], v[212:215], v[128:131], v[176:179]\n"
"buffer_load_dwordx4 v[132:135], v0, s[4:7], s97 offen offset:1024 sc1\n"
"ds_read_b128 v[212:215], v248\n"
"v_exp_f32_e32 v162, v162\n"
"v_exp_f32_e32 v163, v163\n"
"v_mfma_f32_16x16x32_bf16 v[180:183], v[88:91], v[128:131], v[180:183]\n"
"ds_read_b128 v[232:235], v248 offset:1024\n"
"v_cvt_pk_bf16_f32 v128, v148, v149\n"
"v_cvt_pk_bf16_f32 v129, v150, v151\n"
"v_cvt_pk_bf16_f32 v130, v160, v161\n"
"v_cvt_pk_bf16_f32 v131, v162, v163\n"
"s_waitcnt lgkmcnt(6)\n"
"v_mfma_f32_16x16x32_bf16 v[156:159], v[88:91], v[164:167], v[156:159]\n"
"ds_read_b64_tr_b16 v[148:149], v16\n"
"buffer_load_dwordx4 v[160:163], v0, s[4:7], s97 offen offset:2048 sc1\n"
"v_cndmask_b32_e64 v192, v100, v192, s[20:21]\n"
"v_cndmask_b32_e64 v193, v101, v193, s[22:23]\n"
"v_cndmask_b32_e64 v194, v102, v194, s[24:25]\n"
"v_cndmask_b32_e64 v195, v103, v195, s[26:27]\n"
"v_cndmask_b32_e64 v228, v112, v228, s[28:29]\n"
"v_cndmask_b32_e64 v229, v113, v229, s[30:31]\n"
"v_cndmask_b32_e64 v230, v114, v230, s[32:33]\n"
"v_cndmask_b32_e64 v231, v115, v231, s[34:35]\n"
"v_exp_f32_e32 v192, v192\n"
"v_mfma_f32_16x16x32_bf16 v[124:127], v[196:199], v[164:167], v[124:127]\n"
"ds_read_b64_tr_b16 v[150:151], v255 offset:256\n"
"buffer_load_dwordx4 v[100:103], v0, s[4:7], s97 offen offset:3072 sc1\n"
"v_exp_f32_e32 v193, v193\n"
"v_exp_f32_e32 v194, v194\n"
"v_exp_f32_e32 v195, v195\n"
"v_exp_f32_e32 v228, v228\n"
"v_mfma_f32_16x16x32_bf16 v[168:171], v[144:147], v[164:167], v[168:171]\n"
"buffer_load_dwordx4 v[164:167], v1, s[4:7], s97 offen sc1\n"
"v_exp_f32_e32 v229, v229\n"
"v_exp_f32_e32 v230, v230\n"
"v_exp_f32_e32 v231, v231\n"
"v_cvt_pk_bf16_f32 v112, v192, v193\n"
"v_cvt_pk_bf16_f32 v113, v194, v195\n"
"v_cvt_pk_bf16_f32 v114, v228, v229\n"
"v_mfma_f32_16x16x32_bf16 v[172:175], v[144:147], v[128:131], v[172:175]\n"
"ds_read_b64_tr_b16 v[144:145], v255\n"
"v_cvt_pk_bf16_f32 v115, v230, v231\n"
"v_cndmask_b32_e64 v220, v104, v220, s[36:37]\n"
"v_cndmask_b32_e64 v221, v105, v221, s[38:39]\n"
"v_cndmask_b32_e64 v222, v106, v222, s[40:41]\n"
"v_cndmask_b32_e64 v223, v107, v223, s[42:43]\n"
"v_cndmask_b32_e64 v224, v108, v224, s[44:45]\n"
"v_cndmask_b32_e64 v225, v109, v225, s[46:47]\n"
"v_cndmask_b32_e64 v226, v110, v226, s[48:49]\n"
"v_cndmask_b32_e64 v227, v111, v227, s[50:51]\n"
"v_mfma_f32_16x16x32_bf16 v[176:179], v[196:199], v[128:131], v[176:179]\n"
"buffer_load_dwordx4 v[104:107], v1, s[4:7], s97 offen offset:1024 sc1\n"
"ds_read_b64_tr_b16 v[146:147], v16 offset:256\n"
"buffer_load_dwordx4 v[108:111], v1, s[4:7], s97 offen offset:2048 sc1\n"
"v_exp_f32_e32 v220, v220\n"
"v_exp_f32_e32 v221, v221\n"
"v_exp_f32_e32 v222, v222\n"
"v_exp_f32_e32 v223, v223\n"
"v_exp_f32_e32 v224, v224\n"
"v_mfma_f32_16x16x32_bf16 v[180:183], v[88:91], v[128:131], v[180:183]\n"
"s_waitcnt lgkmcnt(0)\n"
"v_exp_f32_e32 v225, v225\n"
"v_exp_f32_e32 v226, v226\n"
"v_exp_f32_e32 v227, v227\n"
"v_cvt_pk_bf16_f32 v128, v220, v221\n"
"v_cvt_pk_bf16_f32 v129, v222, v223\n"
"v_cvt_pk_bf16_f32 v130, v224, v225\n"
"v_cvt_pk_bf16_f32 v131, v226, v227\n"
"v_mfma_f32_16x16x32_bf16 v[156:159], v[88:91], v[112:115], v[156:159]\n"
"v_and_b32_e32 v212, v112, v212\n"
"v_and_b32_e32 v213, v113, v213\n"
"v_and_b32_e32 v214, v114, v214\n"
"v_and_b32_e32 v215, v115, v215\n"
"v_mfma_f32_16x16x32_bf16 v[180:183], v[88:91], v[128:131], v[180:183]\n"
"v_xor_b32_e32 v192, v112, v212\n"
"v_xor_b32_e32 v193, v113, v213\n"
"v_xor_b32_e32 v194, v114, v214\n"
"v_xor_b32_e32 v195, v115, v215\n"
"v_mfma_f32_16x16x32_bf16 v[124:127], v[148:151], v[212:215], v[124:127]\n"
"v_and_b32_e32 v232, v128, v232\n"
"v_and_b32_e32 v233, v129, v233\n"
"v_mfma_f32_16x16x32_bf16 v[168:171], v[144:147], v[212:215], v[168:171]\n"
"v_and_b32_e32 v234, v130, v234\n"
"v_and_b32_e32 v235, v131, v235\n"
"v_mfma_f32_16x16x32_bf16 v[124:127], v[116:119], v[192:195], v[124:127]\n"
"v_xor_b32_e32 v112, v128, v232\n"
"v_xor_b32_e32 v113, v129, v233\n"
"v_mfma_f32_16x16x32_bf16 v[168:171], v[188:191], v[192:195], v[168:171]\n"
"v_xor_b32_e32 v114, v130, v234\n"
"v_xor_b32_e32 v115, v131, v235\n"
"v_mfma_f32_16x16x32_bf16 v[176:179], v[148:151], v[232:235], v[176:179]\n"
"buffer_load_dwordx4 v[128:131], v1, s[4:7], s97 offen offset:3072 sc1\n"
"s_add_u32 s97, s58, 0x40000\n"
"buffer_load_dwordx4 v[148:151], v0, s[4:7], s97 offen sc1\n"
"v_mfma_f32_16x16x32_bf16 v[172:175], v[144:147], v[232:235], v[172:175]\n"
"buffer_load_dwordx4 v[144:147], v0, s[4:7], s97 offen offset:1024 sc1\n"
"v_mfma_f32_16x16x32_bf16 v[176:179], v[116:119], v[112:115], v[176:179]\n"
"buffer_load_dwordx4 v[116:119], v0, s[4:7], s97 offen offset:2048 sc1\n"
"v_mfma_f32_16x16x32_bf16 v[172:175], v[188:191], v[112:115], v[172:175]\n"
"buffer_load_dwordx4 v[112:115], v0, s[4:7], s97 offen offset:3072 sc1\n"
"v_max_f32_e32 v248, v156, v180\n"
"v_rcp_f32_e32 v156, v156\n"
"v_rcp_f32_e32 v180, v180\n"
"s_nop 0\n"
"v_max3_f32 v248, v248, v156, v180\n"
"v_cmp_nlt_f32_e32 vcc, v248, v252\n"
"s_cbranch_vccnz .Lmk_b105\n"
".Lmk_k110:\n"
"v_mul_f32_e32 v124, v124, v156\n"
"v_mul_f32_e32 v125, v125, v156\n"
"v_mul_f32_e32 v126, v126, v156\n"
"v_mul_f32_e32 v127, v127, v156\n"
"v_mul_f32_e32 v168, v168, v156\n"
"v_mul_f32_e32 v169, v169, v156\n"
"v_mul_f32_e32 v170, v170, v156\n"
"v_mul_f32_e32 v171, v171, v156\n"
"v_cvt_pk_bf16_f32 v88, v124, v125\n"
"v_cvt_pk_bf16_f32 v89, v126, v127\n"
"v_cvt_pk_bf16_f32 v90, v168, v169\n"
"v_cvt_pk_bf16_f32 v91, v170, v171\n"
"buffer_store_dwordx4 v[88:91], v0, s[4:7], s60 offen sc1\n"
"s_add_u32 s98, s60, 0x8000\n"
"v_mul_f32_e32 v176, v176, v180\n"
"v_mul_f32_e32 v177, v177, v180\n"
"v_mul_f32_e32 v178, v178, v180\n"
"v_mul_f32_e32 v179, v179, v180\n"
"v_mul_f32_e32 v172, v172, v180\n"
"v_mul_f32_e32 v173, v173, v180\n"
"v_mul_f32_e32 v174, v174, v180\n"
"v_mul_f32_e32 v175, v175, v180\n"
"v_cvt_pk_bf16_f32 v88, v176, v177\n"
"v_cvt_pk_bf16_f32 v89, v178, v179\n"
"v_cvt_pk_bf16_f32 v90, v172, v173\n"
"v_cvt_pk_bf16_f32 v91, v174, v175\n"
"buffer_store_dwordx4 v[88:91], v0, s[4:7], s98 offen sc1\n"
";\n"
"s_waitcnt vmcnt(20)\n"
"; @A G2 b.0\n"
"s_nop 0\n"
"v_mfma_f32_16x16x32_bf16 v[88:91], a[128:131], v[152:155], 0\n"
"v_mfma_f32_16x16x32_bf16 v[88:91], a[132:135], v[200:203], v[88:91]\n"
"s_waitcnt vmcnt(18)\n"
"v_mfma_f32_16x16x32_bf16 v[88:91], a[136:139], v[184:187], v[88:91]\n"
"buffer_load_dwordx4 v[92:95], v1, s[4:7], s97 offen sc1\n"
"v_mfma_f32_16x16x32_bf16 v[88:91], a[140:143], v[204:207], v[88:91]\n"
"s_waitcnt vmcnt(17)\n"
"v_mfma_f32_16x16x32_bf16 v[88:91], a[144:147], v[140:143], v[88:91]\n"
"v_mfma_f32_16x16x32_bf16 v[88:91], a[148:151], v[208:211], v[88:91]\n"
"s_waitcnt vmcnt(15)\n"
"v_mfma_f32_16x16x32_bf16 v[88:91], a[152:155], v[120:123], v[88:91]\n"
"buffer_load_dwordx4 v[96:99], v1, s[4:7], s97 offen offset:1024 sc1\n"
"v_mfma_f32_16x16x32_bf16 v[88:91], a[156:159], v[136:139], v[88:91]\n"
"v_mfma_f32_16x16x32_bf16 v[124:127], a[188:191], v[136:139], 0\n"
"v_mfma_f32_16x16x32_bf16 v[124:127], a[184:187], v[120:123], v[124:127]\n"
"v_mfma_f32_16x16x32_bf16 v[124:127], a[180:183], v[208:211], v[124:127]\n"
"buffer_load_dwordx4 v[156:159], v1, s[4:7], s97 offen offset:2048 sc1\n"
"v_mfma_f32_16x16x32_bf16 v[124:127], a[176:179], v[140:143], v[124:127]\n"
"v_mfma_f32_16x16x32_bf16 v[124:127], a[172:175], v[204:207], v[124:127]\n"
"s_nop 1\n"
"v_cmp_u_f32_e32 vcc, v88, v88\n"
"v_mfma_f32_16x16x32_bf16 v[124:127], a[168:171], v[184:187], v[124:127]\n"
"s_cbranch_vccnz .Lmk_p111s\n"
".Lmk_p111r:\n"
"v_mfma_f32_16x16x32_bf16 v[124:127], a[164:167], v[200:203], v[124:127]\n"
"ds_write_b128 v3, v[88:91]\n"
"v_mfma_f32_16x16x32_bf16 v[124:127], a[160:163], v[152:155], v[124:127]\n"
"buffer_load_dwordx4 v[88:91], v1, s[4:7], s97 offen offset:3072 sc1\n"
"v_mfma_f32_16x16x32_bf16 v[168:171], a[192:195], v[152:155], 0\n"
"v_mfma_f32_16x16x32_bf16 v[168:171], a[196:199], v[200:203], v[168:171]\n"
"s_add_u32 s98, s59, 0x40000\n"
"v_mfma_f32_16x16x32_bf16 v[168:171], a[200:203], v[184:187], v[168:171]\n"
"v_mfma_f32_16x16x32_bf16 v[168:171], a[204:207], v[204:207], v[168:171]\n"
"buffer_load_dwordx4 v[172:175], v0, s[4:7], s98 offen sc1\n"
"v_mfma_f32_16x16x32_bf16 v[168:171], a[208:211], v[140:143], v[168:171]\n"
"v_mfma_f32_16x16x32_bf16 v[168:171], a[212:215], v[208:211], v[168:171]\n"
"ds_write_b128 v3, v[124:127] offset:1024\n"
"v_mfma_f32_16x16x32_bf16 v[168:171], a[216:219], v[120:123], v[168:171]\n"
"v_mfma_f32_16x16x32_bf16 v[168:171], a[220:223], v[136:139], v[168:171]\n"
"buffer_load_dwordx4 v[124:127], v0, s[4:7], s98 offen offset:1024 sc1\n"
"v_mfma_f32_16x16x32_bf16 v[176:179], a[252:255], v[136:139], 0\n"
"v_mfma_f32_16x16x32_bf16 v[176:179], a[248:251], v[120:123], v[176:179]\n"
"v_mfma_f32_16x16x32_bf16 v[176:179], a[244:247], v[208:211], v[176:179]\n"
"v_mfma_f32_16x16x32_bf16 v[176:179], a[240:243], v[140:143], v[176:179]\n"
"buffer_load_dwordx4 v[120:123], v0, s[4:7], s98 offen offset:2048 sc1\n"
"v_mfma_f32_16x16x32_bf16 v[176:179], a[236:239], v[204:207], v[176:179]\n"
"v_mfma_f32_16x16x32_bf16 v[176:179], a[232:235], v[184:187], v[176:179]\n"
"ds_write_b128 v3, v[168:171] offset:2048\n"
"v_mfma_f32_16x16x32_bf16 v[176:179], a[228:231], v[200:203], v[176:179]\n"
"v_mfma_f32_16x16x32_bf16 v[176:179], a[224:227], v[152:155], v[176:179]\n"
"s_waitcnt vmcnt(17)\n"
"buffer_load_dwordx4 v[136:139], v0, s[4:7], s98 offen offset:3072 sc1\n"
"; @A G2 b.1\n"
"v_mfma_f32_16x16x32_bf16 v[140:143], a[128:131], v[216:219], 0\n"
"v_mfma_f32_16x16x32_bf16 v[140:143], a[132:135], v[132:135], v[140:143]\n"
"v_mfma_f32_16x16x32_bf16 v[140:143], a[136:139], v[160:163], v[140:143]\n"
"v_mfma_f32_16x16x32_bf16 v[140:143], a[140:143], v[100:103], v[140:143]\n"
"s_waitcnt vmcnt(14)\n"
"buffer_load_dwordx4 v[152:155], v1, s[4:7], s98 offen sc1\n"
"v_mfma_f32_16x16x32_bf16 v[140:143], a[144:147], v[164:167], v[140:143]\n"
"v_mfma_f32_16x16x32_bf16 v[140:143], a[148:151], v[104:107], v[140:143]\n"
"ds_write_b128 v3, v[176:179] offset:3072\n"
"v_mfma_f32_16x16x32_bf16 v[140:143], a[152:155], v[108:111], v[140:143]\n"
"v_mfma_f32_16x16x32_bf16 v[140:143], a[156:159], v[128:131], v[140:143]\n"
"buffer_load_dwordx4 v[168:171], v1, s[4:7], s98 offen offset:1024 sc1\n"
"v_mfma_f32_16x16x32_bf16 v[176:179], a[188:191], v[128:131], 0\n"
"v_mfma_f32_16x16x32_bf16 v[176:179], a[184:187], v[108:111], v[176:179]\n"
"v_mfma_f32_16x16x32_bf16 v[176:179], a[180:183], v[104:107], v[176:179]\n"
"v_mfma_f32_16x16x32_bf16 v[176:179], a[176:179], v[164:167], v[176:179]\n"
"buffer_load_dwordx4 v[180:183], v1, s[4:7], s98 offen offset:2048 sc1\n"
"v_mfma_f32_16x16x32_bf16 v[176:179], a[172:175], v[100:103], v[176:179]\n"
"s_nop 0\n"
"v_cmp_u_f32_e32 vcc, v140, v140\n"
"v_mfma_f32_16x16x32_bf16 v[176:179], a[168:171], v[160:163], v[176:179]\n"
"s_cbranch_vccnz .Lmk_p112s\n"
".Lmk_p112r:\n"
"v_mfma_f32_16x16x32_bf16 v[176:179], a[164:167], v[132:135], v[176:179]\n"
"ds_write_b128 v3, v[140:143] offset:6144\n"
"v_mfma_f32_16x16x32_bf16 v[176:179], a[160:163], v[216:219], v[176:179]\n"
"buffer_load_dwordx4 v[140:143], v1, s[4:7], s98 offen offset:3072 sc1\n"
"v_mfma_f32_16x16x32_bf16 v[184:187], a[192:195], v[216:219], 0\n"
"v_mfma_f32_16x16x32_bf16 v[184:187], a[196:199], v[132:135], v[184:187]\n"
"v_mfma_f32_16x16x32_bf16 v[184:187], a[200:203], v[160:163], v[184:187]\n"
"v_mfma_f32_16x16x32_bf16 v[184:187], a[204:207], v[100:103], v[184:187]\n"
"v_mfma_f32_16x16x32_bf16 v[184:187], a[208:211], v[164:167], v[184:187]\n"
"ds_read_b128 v[188:191], v2 offset:8192\n"
"ds_read_b128 v[192:195], v2 offset:9216\n"
"v_mfma_f32_16x16x32_bf16 v[184:187], a[212:215], v[104:107], v[184:187]\n"
"ds_write_b128 v3, v[176:179] offset:7168\n"
"v_mfma_f32_16x16x32_bf16 v[184:187], a[216:219], v[108:111], v[184:187]\n"
"v_mfma_f32_16x16x32_bf16 v[184:187], a[220:223], v[128:131], v[184:187]\n"
"v_mfma_f32_16x16x32_bf16 v[176:179], a[252:255], v[128:131], 0\n"
"v_mfma_f32_16x16x32_bf16 v[176:179], a[248:251], v[108:111], v[176:179]\n"
"s_waitcnt lgkmcnt(1)\n"
"v_lshlrev_b32_e32 v255, 16, v188\n"
"v_and_b32_e32 v254, 0xffff0000, v188\n"
"v_mfma_f32_16x16x32_bf16 v[176:179], a[244:247], v[104:107], v[176:179]\n"
"v_lshlrev_b32_e32 v253, 16, v189\n"
"v_and_b32_e32 v252, 0xffff0000, v189\n"
"v_mfma_f32_16x16x32_bf16 v[176:179], a[240:243], v[164:167], v[176:179]\n"
"v_lshlrev_b32_e32 v251, 16, v190\n"
"v_and_b32_e32 v250, 0xffff0000, v190\n"
"v_mfma_f32_16x16x32_bf16 v[176:179], a[236:239], v[100:103], v[176:179]\n"
"v_lshlrev_b32_e32 v249, 16, v191\n"
"v_and_b32_e32 v248, 0xffff0000, v191\n"
"v_mfma_f32_16x16x32_bf16 v[176:179], a[232:235], v[160:163], v[176:179]\n"
"ds_write_b128 v3, v[184:187] offset:8192\n"
"v_mfma_f32_16x16x32_bf16 v[176:179], a[228:231], v[132:135], v[176:179]\n"
"v_lshlrev_b32_e32 v247, 16, v192\n"
"v_and_b32_e32 v246, 0xffff0000, v192\n"
"v_mfma_f32_16x16x32_bf16 v[176:179], a[224:227], v[216:219], v[176:179]\n"
"s_waitcnt vmcnt(14)\n"
"v_lshlrev_b32_e32 v245, 16, v193\n"
"v_and_b32_e32 v244, 0xffff0000, v193\n"
"; @A G2 b.2\n"
"v_mfma_f32_16x16x32_bf16 v[100:103], a[128:131], v[148:151], 0\n"
"v_lshlrev_b32_e32 v243, 16, v194\n"
"v_and_b32_e32 v242, 0xffff0000, v194\n"
"v_mfma_f32_16x16x32_bf16 v[100:103], a[132:135], v[144:147], v[100:103]\n"
"v_lshlrev_b32_e32 v241, 16, v195\n"
"v_and_b32_e32 v240, 0xffff0000, v195\n"
"v_mfma_f32_16x16x32_bf16 v[100:103], a[136:139], v[116:119], v[100:103]\n"
"v_mfma_f32_16x16x32_bf16 v[100:103], a[140:143], v[112:115], v[100:103]\n"
"s_waitcnt vmcnt(8)\n"
"v_mfma_f32_16x16x32_bf16 v[100:103], a[144:147], v[92:95], v[100:103]\n"
"v_mfma_f32_16x16x32_bf16 v[100:103], a[148:151], v[96:99], v[100:103]\n"
"ds_write_b128 v3, v[176:179] offset:9216\n"
"v_mfma_f32_16x16x32_bf16 v[100:103], a[152:155], v[156:159], v[100:103]\n"
"v_mfma_f32_16x16x32_bf16 v[100:103], a[156:159], v[88:91], v[100:103]\n"
"v_mfma_f32_16x16x32_bf16 v[104:107], a[188:191], v[88:91], 0\n"
"v_mfma_f32_16x16x32_bf16 v[104:107], a[184:187], v[156:159], v[104:107]\n"
"s_waitcnt lgkmcnt(0)\n"
"s_barrier\n"
"v_mfma_f32_16x16x32_bf16 v[104:107], a[180:183], v[96:99], v[104:107]\n"
"ds_read_b128 v[108:111], v4\n"
"ds_read_b128 v[128:131], v5\n"
"v_mfma_f32_16x16x32_bf16 v[104:107], a[176:179], v[92:95], v[104:107]\n"
"v_mfma_f32_16x16x32_bf16 v[104:107], a[172:175], v[112:115], v[104:107]\n"
"s_add_u32 s98, s56, 0\n"
"v_cmp_u_f32_e32 vcc, v100, v100\n"
"v_mfma_f32_16x16x32_bf16 v[104:107], a[168:171], v[116:119], v[104:107]\n"
"s_cbranch_vccnz .Lmk_p113s\n"
".Lmk_p113r:\n"
"v_mfma_f32_16x16x32_bf16 v[104:107], a[164:167], v[144:147], v[104:107]\n"
"ds_write_b128 v3, v[100:103] offset:12288\n"
"v_mfma_f32_16x16x32_bf16 v[104:107], a[160:163], v[148:151], v[104:107]\n"
"buffer_load_dwordx4 v[100:103], v0, s[4:7], s98 offen sc1\n"
"v_mfma_f32_16x16x32_bf16 v[132:135], a[192:195], v[148:151], 0\n"
"ds_read_b128 v[160:163], v4 offset:1024\n"
"ds_read_b128 v[164:167], v5 offset:1024\n"
"v_mfma_f32_16x16x32_bf16 v[132:135], a[196:199], v[144:147], v[132:135]\n"
"s_waitcnt lgkmcnt(3)\n"
"v_add_f32_e32 v108, v108, v255\n"
"v_add_f32_e32 v109, v109, v254\n"
"v_mfma_f32_16x16x32_bf16 v[132:135], a[200:203], v[116:119], v[132:135]\n"
"buffer_load_dwordx4 v[176:179], v0, s[4:7], s98 offen offset:1024 sc1\n"
"v_mfma_f32_16x16x32_bf16 v[132:135], a[204:207], v[112:115], v[132:135]\n"
"v_add_f32_e32 v110, v110, v253\n"
"v_add_f32_e32 v111, v111, v252\n"
"v_mfma_f32_16x16x32_bf16 v[132:135], a[208:211], v[92:95], v[132:135]\n"
"v_add_f32_e32 v108, v108, v128\n"
"v_add_f32_e32 v109, v109, v129\n"
"v_mfma_f32_16x16x32_bf16 v[132:135], a[212:215], v[96:99], v[132:135]\n"
"ds_write_b128 v3, v[104:107] offset:13312\n"
"v_mfma_f32_16x16x32_bf16 v[132:135], a[216:219], v[156:159], v[132:135]\n"
"buffer_load_dwordx4 v[104:107], v0, s[4:7], s98 offen offset:2048 sc1\n"
"v_mfma_f32_16x16x32_bf16 v[132:135], a[220:223], v[88:91], v[132:135]\n"
"ds_read_b128 v[184:187], v4 offset:2048\n"
"ds_read_b128 v[188:191], v5 offset:2048\n"
"v_mfma_f32_16x16x32_bf16 v[192:195], a[252:255], v[88:91], 0\n"
"s_waitcnt lgkmcnt(3)\n"
"v_add_f32_e32 v110, v110, v130\n"
"v_add_f32_e32 v111, v111, v131\n"
"v_mfma_f32_16x16x32_bf16 v[192:195], a[248:251], v[156:159], v[192:195]\n"
"buffer_load_dwordx4 v[88:91], v0, s[4:7], s98 offen offset:3072 sc1\n"
"v_mfma_f32_16x16x32_bf16 v[192:195], a[244:247], v[96:99], v[192:195]\n"
"v_add_f32_e32 v160, v160, v251\n"
"v_add_f32_e32 v161, v161, v250\n"
"v_mfma_f32_16x16x32_bf16 v[192:195], a[240:243], v[92:95], v[192:195]\n"
"v_add_f32_e32 v162, v162, v249\n"
"v_add_f32_e32 v163, v163, v248\n"
"v_mfma_f32_16x16x32_bf16 v[192:195], a[236:239], v[112:115], v[192:195]\n"
"buffer_load_dwordx4 v[92:95], v1, s[4:7], s98 offen sc1\n"
"v_mfma_f32_16x16x32_bf16 v[192:195], a[232:235], v[116:119], v[192:195]\n"
"ds_write_b128 v3, v[132:135] offset:14336\n"
"v_mfma_f32_16x16x32_bf16 v[192:195], a[228:231], v[144:147], v[192:195]\n"
"ds_read_b128 v[96:99], v4 offset:3072\n"
"ds_read_b128 v[112:115], v5 offset:3072\n"
"v_mfma_f32_16x16x32_bf16 v[192:195], a[224:227], v[148:151], v[192:195]\n"
"s_waitcnt vmcnt(9)\n"
"buffer_load_dwordx4 v[116:119], v1, s[4:7], s98 offen offset:1024 sc1\n"
"; @A G2 b.3\n"
"v_mfma_f32_16x16x32_bf16 v[128:131], a[128:131], v[172:175], v[108:111]\n"
"v_add_f32_e32 v160, v160, v164\n"
"v_add_f32_e32 v161, v161, v165\n"
"v_mfma_f32_16x16x32_bf16 v[128:131], a[132:135], v[124:127], v[128:131]\n"
"s_waitcnt lgkmcnt(3)\n"
"v_add_f32_e32 v162, v162, v166\n"
"v_add_f32_e32 v163, v163, v167\n"
"v_mfma_f32_16x16x32_bf16 v[128:131], a[136:139], v[120:123], v[128:131]\n"
"buffer_load_dwordx4 v[132:135], v1, s[4:7], s98 offen offset:2048 sc1\n"
"v_mfma_f32_16x16x32_bf16 v[128:131], a[140:143], v[136:139], v[128:131]\n"
"s_waitcnt vmcnt(7)\n"
"v_add_f32_e32 v184, v184, v247\n"
"v_add_f32_e32 v185, v185, v246\n"
"v_mfma_f32_16x16x32_bf16 v[128:131], a[144:147], v[152:155], v[128:131]\n"
"v_add_f32_e32 v186, v186, v245\n"
"v_add_f32_e32 v187, v187, v244\n"
"v_mfma_f32_16x16x32_bf16 v[128:131], a[148:151], v[168:171], v[128:131]\n"
"ds_write_b128 v3, v[192:195] offset:15360\n"
"v_mfma_f32_16x16x32_bf16 v[128:131], a[152:155], v[180:183], v[128:131]\n"
"buffer_load_dwordx4 v[144:147], v1, s[4:7], s98 offen offset:3072 sc1\n"
"v_mfma_f32_16x16x32_bf16 v[128:131], a[156:159], v[140:143], v[128:131]\n"
"s_add_u32 s98, s57, 0\n"
"v_add_f32_e32 v184, v184, v188\n"
"v_add_f32_e32 v185, v185, v189\n"
"v_mfma_f32_16x16x32_bf16 v[148:151], a[188:191], v[140:143], v[160:163]\n"
"s_waitcnt lgkmcnt(1)\n"
"v_add_f32_e32 v186, v186, v190\n"
"v_add_f32_e32 v187, v187, v191\n"
"v_mfma_f32_16x16x32_bf16 v[148:151], a[184:187], v[180:183], v[148:151]\n"
"s_waitcnt lgkmcnt(0)\n"
"s_barrier\n"
"v_mfma_f32_16x16x32_bf16 v[148:151], a[180:183], v[168:171], v[148:151]\n"
"buffer_load_dwordx4 v[156:159], v0, s[4:7], s98 offen sc1\n"
"v_mfma_f32_16x16x32_bf16 v[148:151], a[176:179], v[152:155], v[148:151]\n"
"v_add_f32_e32 v96, v96, v243\n"
"v_add_f32_e32 v97, v97, v242\n"
"v_mfma_f32_16x16x32_bf16 v[148:151], a[172:175], v[136:139], v[148:151]\n"
"v_cmp_u_f32_e32 vcc, v128, v128\n"
"v_add_f32_e32 v98, v98, v241\n"
"v_mfma_f32_16x16x32_bf16 v[148:151], a[168:171], v[120:123], v[148:151]\n"
"s_cbranch_vccnz .Lmk_p114s\n"
".Lmk_p114r:\n"
"v_add_f32_e32 v99, v99, v240\n"
"v_mfma_f32_16x16x32_bf16 v[148:151], a[164:167], v[124:127], v[148:151]\n"
"buffer_load_dwordx4 v[108:111], v0, s[4:7], s98 offen offset:1024 sc1\n"
"v_mfma_f32_16x16x32_bf16 v[148:151], a[160:163], v[172:175], v[148:151]\n"
"v_add_f32_e32 v96, v96, v112\n"
"v_add_f32_e32 v97, v97, v113\n"
"v_mfma_f32_16x16x32_bf16 v[184:187], a[192:195], v[172:175], v[184:187]\n"
"v_add_f32_e32 v98, v98, v114\n"
"v_add_f32_e32 v99, v99, v115\n"
"v_mfma_f32_16x16x32_bf16 v[184:187], a[196:199], v[124:127], v[184:187]\n"
"buffer_load_dwordx4 v[112:115], v0, s[4:7], s98 offen offset:2048 sc1\n"
"v_mfma_f32_16x16x32_bf16 v[184:187], a[200:203], v[120:123], v[184:187]\n"
"ds_read_b128 v[160:163], v6\n"
"ds_read_b128 v[164:167], v6 offset:1024\n"
"v_mfma_f32_16x16x32_bf16 v[184:187], a[204:207], v[136:139], v[184:187]\n"
"ds_read_b128 v[188:191], v6 offset:2048\n"
"ds_read_b128 v[192:195], v6 offset:3072\n"
"v_mfma_f32_16x16x32_bf16 v[184:187], a[208:211], v[152:155], v[184:187]\n"
"buffer_load_dwordx4 v[196:199], v0, s[4:7], s98 offen offset:3072 sc1\n"
"v_mfma_f32_16x16x32_bf16 v[184:187], a[212:215], v[168:171], v[184:187]\n"
"v_mfma_f32_16x16x32_bf16 v[184:187], a[216:219], v[180:183], v[184:187]\n"
"v_mfma_f32_16x16x32_bf16 v[184:187], a[220:223], v[140:143], v[184:187]\n"
"buffer_load_dwordx4 v[200:203], v1, s[4:7], s98 offen sc1\n"
"v_mfma_f32_16x16x32_bf16 v[96:99], a[252:255], v[140:143], v[96:99]\n"
"s_waitcnt lgkmcnt(0)\n"
"v_add_f32_e32 v160, v128, v160\n"
"v_add_f32_e32 v161, v129, v161\n"
"v_mfma_f32_16x16x32_bf16 v[96:99], a[248:251], v[180:183], v[96:99]\n"
"v_add_f32_e32 v162, v130, v162\n"
"v_add_f32_e32 v163, v131, v163\n"
"v_mfma_f32_16x16x32_bf16 v[96:99], a[244:247], v[168:171], v[96:99]\n"
"buffer_load_dwordx4 v[128:131], v1, s[4:7], s98 offen offset:1024 sc1\n"
"v_mfma_f32_16x16x32_bf16 v[96:99], a[240:243], v[152:155], v[96:99]\n"
"v_add_f32_e32 v164, v148, v164\n"
"v_add_f32_e32 v165, v149, v165\n"
"v_mfma_f32_16x16x32_bf16 v[96:99], a[236:239], v[136:139], v[96:99]\n"
"v_add_f32_e32 v166, v150, v166\n"
"v_add_f32_e32 v167, v151, v167\n"
"v_mfma_f32_16x16x32_bf16 v[96:99], a[232:235], v[120:123], v[96:99]\n"
"buffer_load_dwordx4 v[120:123], v1, s[4:7], s98 offen offset:2048 sc1\n"
"v_mfma_f32_16x16x32_bf16 v[96:99], a[228:231], v[124:127], v[96:99]\n"
"v_cvt_pk_bf16_f32 v124, v160, v161\n"
"v_cvt_pk_bf16_f32 v125, v162, v163\n"
"v_mfma_f32_16x16x32_bf16 v[96:99], a[224:227], v[172:175], v[96:99]\n"
"s_add_u32 s97, s61, 0x440000\n"
"v_cvt_pk_bf16_f32 v126, v164, v165\n"
"v_cvt_pk_bf16_f32 v127, v166, v167\n"
"buffer_store_dwordx4 v[124:127], v0, s[4:7], s97 offen sc1\n"
"ds_write_b128 v2, v[124:127] offset:8192\n"
"buffer_load_dwordx4 v[136:139], v1, s[4:7], s98 offen offset:3072 sc1\n"
"; @A fin b\n"
"s_add_u32 s98, s61, 0x440000\n"
"v_add_f32_e32 v188, v184, v188\n"
"v_add_f32_e32 v189, v185, v189\n"
"v_add_f32_e32 v190, v186, v190\n"
"v_add_f32_e32 v191, v187, v191\n"
"v_add_f32_e32 v192, v96, v192\n"
"v_add_f32_e32 v193, v97, v193\n"
"v_add_f32_e32 v194, v98, v194\n"
"v_add_f32_e32 v195, v99, v195\n"
"v_cvt_pk_bf16_f32 v96, v188, v189\n"
"v_cvt_pk_bf16_f32 v97, v190, v191\n"
"v_cvt_pk_bf16_f32 v98, v192, v193\n"
"v_cvt_pk_bf16_f32 v99, v194, v195\n"
"buffer_store_dwordx4 v[96:99], v0, s[4:7], s98 offen offset:1024 sc1\n"
"s_waitcnt vmcnt(16)\n"
"; @A G2 a.0\n"
"v_mfma_f32_16x16x32_bf16 v[140:143], a[128:131], v[100:103], 0\n"
"s_add_u32 s98, s58, 0\n"
"buffer_load_dwordx4 v[148:151], v0, s[4:7], s98 offen sc1\n"
"v_mfma_f32_16x16x32_bf16 v[140:143], a[132:135], v[176:179], v[140:143]\n"
"s_waitcnt vmcnt(15)\n"
"ds_write_b128 v2, v[96:99] offset:9216\n"
"v_mfma_f32_16x16x32_bf16 v[140:143], a[136:139], v[104:107], v[140:143]\n"
"v_mov_b32_e32 v152, 0x3f803f80\n"
"v_mov_b32_e32 v153, 0x3f803f80\n"
"v_mfma_f32_16x16x32_bf16 v[140:143], a[140:143], v[88:91], v[140:143]\n"
"s_waitcnt vmcnt(13)\n"
"v_mov_b32_e32 v154, 0x3f803f80\n"
"v_mov_b32_e32 v155, 0x3f803f80\n"
"v_mfma_f32_16x16x32_bf16 v[140:143], a[144:147], v[92:95], v[140:143]\n"
"buffer_load_dwordx4 v[160:163], v0, s[4:7], s98 offen offset:1024 sc1\n"
"v_mfma_f32_16x16x32_bf16 v[164:167], v[152:155], v[124:127], 0\n"
"v_mfma_f32_16x16x32_bf16 v[164:167], v[152:155], v[96:99], v[164:167]\n"
"v_mfma_f32_16x16x32_bf16 v[152:155], v[124:127], v[124:127], 0\n"
"v_mfma_f32_16x16x32_bf16 v[152:155], v[96:99], v[96:99], v[152:155]\n"
"v_mfma_f32_16x16x32_bf16 v[140:143], a[148:151], v[116:119], v[140:143]\n"
"s_waitcnt vmcnt(12)\n"
"v_mfma_f32_16x16x32_bf16 v[140:143], a[152:155], v[132:135], v[140:143]\n"
"v_mfma_f32_16x16x32_bf16 v[140:143], a[156:159], v[144:147], v[140:143]\n"
"v_mfma_f32_16x16x32_bf16 v[96:99], a[188:191], v[144:147], 0\n"
"buffer_load_dwordx4 v[124:127], v0, s[4:7], s98 offen offset:2048 sc1\n"
"v_mfma_f32_16x16x32_bf16 v[96:99], a[184:187], v[132:135], v[96:99]\n"
"s_mov_b32 s96, 0xaaaaaaaa\n"
"s_mov_b32 s97, 0xaaaaaaaa\n"
"v_mfma_f32_16x16x32_bf16 v[96:99], a[180:183], v[116:119], v[96:99]\n"
"v_mfma_f32_16x16x32_bf16 v[96:99], a[176:179], v[92:95], v[96:99]\n"
"v_mfma_f32_16x16x32_bf16 v[96:99], a[172:175], v[88:91], v[96:99]\n"
"v_cmp_u_f32_e32 vcc, v140, v140\n"
"v_cndmask_b32_e64 v152, v152, v153, s[96:97]\n"
"v_mfma_f32_16x16x32_bf16 v[96:99], a[168:171], v[104:107], v[96:99]\n"
"s_cbranch_vccnz .Lmk_p115s\n"
".Lmk_p115r:\n"
"v_cndmask_b32_e64 v154, v154, v155, s[96:97]\n"
"v_mfma_f32_16x16x32_bf16 v[96:99], a[164:167], v[176:179], v[96:99]\n"
"ds_write_b128 v3, v[140:143]\n"
"v_mfma_f32_16x16x32_bf16 v[96:99], a[160:163], v[100:103], v[96:99]\n"
"buffer_load_dwordx4 v[140:143], v0, s[4:7], s98 offen offset:3072 sc1\n"
"v_mfma_f32_16x16x32_bf16 v[168:171], a[192:195], v[100:103], 0\n"
"s_mov_b32 s96, 0xcccccccc\n"
"s_mov_b32 s97, 0xcccccccc\n"
"v_mfma_f32_16x16x32_bf16 v[168:171], a[196:199], v[176:179], v[168:171]\n"
"v_cndmask_b32_e64 v165, v152, v154, s[96:97]\n"
"v_mfma_f32_16x16x32_bf16 v[168:171], a[200:203], v[104:107], v[168:171]\n"
"s_mov_b32 s96, 0xf0000f\n"
"s_mov_b32 s97, 0xf0000f00\n"
"v_mfma_f32_16x16x32_bf16 v[168:171], a[204:207], v[88:91], v[168:171]\n"
"buffer_load_dwordx4 v[152:155], v1, s[4:7], s98 offen sc1\n"
"v_mfma_f32_16x16x32_bf16 v[168:171], a[208:211], v[92:95], v[168:171]\n"
"v_cndmask_b32_e64 v164, v164, v165, s[96:97]\n"
"s_add_u32 s97, s63, 0x44000\n"
"v_mfma_f32_16x16x32_bf16 v[168:171], a[212:215], v[116:119], v[168:171]\n"
"ds_write_b128 v3, v[96:99] offset:1024\n"
"v_mfma_f32_16x16x32_bf16 v[168:171], a[216:219], v[132:135], v[168:171]\n"
"buffer_store_dword v164, v14, s[4:7], s97 offen sc1\n"
"v_mfma_f32_16x16x32_bf16 v[168:171], a[220:223], v[144:147], v[168:171]\n"
"buffer_load_dwordx4 v[164:167], v1, s[4:7], s98 offen offset:1024 sc1\n"
"v_mfma_f32_16x16x32_bf16 v[96:99], a[252:255], v[144:147], 0\n"
"v_mfma_f32_16x16x32_bf16 v[96:99], a[248:251], v[132:135], v[96:99]\n"
"v_mfma_f32_16x16x32_bf16 v[96:99], a[244:247], v[116:119], v[96:99]\n"
"v_mfma_f32_16x16x32_bf16 v[96:99], a[240:243], v[92:95], v[96:99]\n"
"buffer_load_dwordx4 v[116:119], v1, s[4:7], s98 offen offset:2048 sc1\n"
"v_mfma_f32_16x16x32_bf16 v[96:99], a[236:239], v[88:91], v[96:99]\n"
"v_mfma_f32_16x16x32_bf16 v[96:99], a[232:235], v[104:107], v[96:99]\n"
"ds_write_b128 v3, v[168:171] offset:2048\n"
"v_mfma_f32_16x16x32_bf16 v[96:99], a[228:231], v[176:179], v[96:99]\n"
"v_mfma_f32_16x16x32_bf16 v[96:99], a[224:227], v[100:103], v[96:99]\n"
"s_waitcnt vmcnt(14)\n"
"buffer_load_dwordx4 v[132:135], v1, s[4:7], s98 offen offset:3072 sc1\n"
"; @A G2 a.1\n"
"v_mfma_f32_16x16x32_bf16 v[88:91], a[128:131], v[156:159], 0\n"
"v_mfma_f32_16x16x32_bf16 v[88:91], a[132:135], v[108:111], v[88:91]\n"
"s_add_u32 s98, s59, 0\n"
"v_mfma_f32_16x16x32_bf16 v[88:91], a[136:139], v[112:115], v[88:91]\n"
"v_mfma_f32_16x16x32_bf16 v[88:91], a[140:143], v[196:199], v[88:91]\n"
"s_waitcnt vmcnt(10)\n"
"buffer_load_dwordx4 v[172:175], v0, s[4:7], s98 offen sc1\n"
"v_mfma_f32_16x16x32_bf16 v[88:91], a[144:147], v[200:203], v[88:91]\n"
"v_mfma_f32_16x16x32_bf16 v[88:91], a[148:151], v[128:131], v[88:91]\n"
"ds_write_b128 v3, v[96:99] offset:3072\n"
"v_mfma_f32_16x16x32_bf16 v[88:91], a[152:155], v[120:123], v[88:91]\n"
"v_mfma_f32_16x16x32_bf16 v[88:91], a[156:159], v[136:139], v[88:91]\n"
"buffer_load_dwordx4 v[176:179], v0, s[4:7], s98 offen offset:1024 sc1\n"
"v_mfma_f32_16x16x32_bf16 v[92:95], a[188:191], v[136:139], 0\n"
"v_mfma_f32_16x16x32_bf16 v[92:95], a[184:187], v[120:123], v[92:95]\n"
"v_mfma_f32_16x16x32_bf16 v[92:95], a[180:183], v[128:131], v[92:95]\n"
"v_mfma_f32_16x16x32_bf16 v[92:95], a[176:179], v[200:203], v[92:95]\n"
"buffer_load_dwordx4 v[168:171], v0, s[4:7], s98 offen offset:2048 sc1\n"
"v_mfma_f32_16x16x32_bf16 v[92:95], a[172:175], v[196:199], v[92:95]\n"
"s_nop 0\n"
"v_cmp_u_f32_e32 vcc, v88, v88\n"
"v_mfma_f32_16x16x32_bf16 v[92:95], a[168:171], v[112:115], v[92:95]\n"
"s_cbranch_vccnz .Lmk_p116s\n"
".Lmk_p116r:\n"
"v_mfma_f32_16x16x32_bf16 v[92:95], a[164:167], v[108:111], v[92:95]\n"
"ds_write_b128 v3, v[88:91] offset:6144\n"
"v_mfma_f32_16x16x32_bf16 v[92:95], a[160:163], v[156:159], v[92:95]\n"
"buffer_load_dwordx4 v[180:183], v0, s[4:7], s98 offen offset:3072 sc1\n"
"v_mfma_f32_16x16x32_bf16 v[88:91], a[192:195], v[156:159], 0\n"
"v_mfma_f32_16x16x32_bf16 v[88:91], a[196:199], v[108:111], v[88:91]\n"
"v_mfma_f32_16x16x32_bf16 v[88:91], a[200:203], v[112:115], v[88:91]\n"
"v_mfma_f32_16x16x32_bf16 v[88:91], a[204:207], v[196:199], v[88:91]\n"
"buffer_load_dwordx4 v[184:187], v1, s[4:7], s98 offen sc1\n"
"v_mfma_f32_16x16x32_bf16 v[88:91], a[208:211], v[200:203], v[88:91]\n"
"ds_read_b128 v[96:99], v2\n"
"ds_read_b128 v[100:103], v2 offset:1024\n"
"v_mfma_f32_16x16x32_bf16 v[88:91], a[212:215], v[128:131], v[88:91]\n"
"ds_write_b128 v3, v[92:95] offset:7168\n"
"v_mfma_f32_16x16x32_bf16 v[88:91], a[216:219], v[120:123], v[88:91]\n"
"v_mfma_f32_16x16x32_bf16 v[88:91], a[220:223], v[136:139], v[88:91]\n"
"buffer_load_dwordx4 v[188:191], v1, s[4:7], s98 offen offset:1024 sc1\n"
"v_mfma_f32_16x16x32_bf16 v[92:95], a[252:255], v[136:139], 0\n"
"v_mfma_f32_16x16x32_bf16 v[92:95], a[248:251], v[120:123], v[92:95]\n"
"s_waitcnt lgkmcnt(1)\n"
"v_lshlrev_b32_e32 v255, 16, v96\n"
"v_and_b32_e32 v254, 0xffff0000, v96\n"
"v_mfma_f32_16x16x32_bf16 v[92:95], a[244:247], v[128:131], v[92:95]\n"
"v_lshlrev_b32_e32 v253, 16, v97\n"
"v_and_b32_e32 v252, 0xffff0000, v97\n"
"v_mfma_f32_16x16x32_bf16 v[92:95], a[240:243], v[200:203], v[92:95]\n"
"buffer_load_dwordx4 v[192:195], v1, s[4:7], s98 offen offset:2048 sc1\n"
"v_mfma_f32_16x16x32_bf16 v[92:95], a[236:239], v[196:199], v[92:95]\n"
"v_lshlrev_b32_e32 v251, 16, v98\n"
"v_and_b32_e32 v250, 0xffff0000, v98\n"
"v_mfma_f32_16x16x32_bf16 v[92:95], a[232:235], v[112:115], v[92:95]\n"
"ds_write_b128 v3, v[88:91] offset:8192\n"
"v_mfma_f32_16x16x32_bf16 v[92:95], a[228:231], v[108:111], v[92:95]\n"
"v_lshlrev_b32_e32 v249, 16, v99\n"
"v_and_b32_e32 v248, 0xffff0000, v99\n"
"v_mfma_f32_16x16x32_bf16 v[92:95], a[224:227], v[156:159], v[92:95]\n"
"s_waitcnt vmcnt(12)\n"
"buffer_load_dwordx4 v[196:199], v1, s[4:7], s98 offen offset:3072 sc1\n"
"; @A G2 a.2\n"
"v_mfma_f32_16x16x32_bf16 v[96:99], a[128:131], v[148:151], 0\n"
"v_lshlrev_b32_e32 v247, 16, v100\n"
"v_and_b32_e32 v246, 0xffff0000, v100\n"
"v_mfma_f32_16x16x32_bf16 v[96:99], a[132:135], v[160:163], v[96:99]\n"
"v_lshlrev_b32_e32 v245, 16, v101\n"
"v_and_b32_e32 v244, 0xffff0000, v101\n"
"v_mfma_f32_16x16x32_bf16 v[96:99], a[136:139], v[124:127], v[96:99]\n"
"v_lshlrev_b32_e32 v243, 16, v102\n"
"v_and_b32_e32 v242, 0xffff0000, v102\n"
"v_mfma_f32_16x16x32_bf16 v[96:99], a[140:143], v[140:143], v[96:99]\n"
"s_waitcnt vmcnt(8)\n"
"v_lshlrev_b32_e32 v241, 16, v103\n"
"v_and_b32_e32 v240, 0xffff0000, v103\n"
"v_mfma_f32_16x16x32_bf16 v[96:99], a[144:147], v[152:155], v[96:99]\n"
"v_mfma_f32_16x16x32_bf16 v[96:99], a[148:151], v[164:167], v[96:99]\n"
"ds_write_b128 v3, v[92:95] offset:9216\n"
"v_mfma_f32_16x16x32_bf16 v[96:99], a[152:155], v[116:119], v[96:99]\n"
"v_mfma_f32_16x16x32_bf16 v[96:99], a[156:159], v[132:135], v[96:99]\n"
"v_mfma_f32_16x16x32_bf16 v[104:107], a[188:191], v[132:135], 0\n"
"s_add_u32 s98, s52, 0x440000\n"
"buffer_load_dwordx4 v[88:91], v0, s[4:7], s98 offen sc1\n"
"v_mfma_f32_16x16x32_bf16 v[104:107], a[184:187], v[116:119], v[104:107]\n"
"s_waitcnt lgkmcnt(0)\n"
"s_barrier\n"
"v_mfma_f32_16x16x32_bf16 v[104:107], a[180:183], v[164:167], v[104:107]\n"
"ds_read_b128 v[144:147], v4\n"
"ds_read_b128 v[112:115], v5\n"
"v_mfma_f32_16x16x32_bf16 v[104:107], a[176:179], v[152:155], v[104:107]\n"
"buffer_load_dwordx4 v[92:95], v0, s[4:7], s98 offen offset:1024 sc1\n"
"v_mfma_f32_16x16x32_bf16 v[104:107], a[172:175], v[140:143], v[104:107]\n"
"v_cmp_u_f32_e32 vcc, v96, v96\n"
"v_mfma_f32_16x16x32_bf16 v[104:107], a[168:171], v[124:127], v[104:107]\n"
"s_cbranch_vccnz .Lmk_p117s\n"
".Lmk_p117r:\n"
"v_mfma_f32_16x16x32_bf16 v[104:107], a[164:167], v[160:163], v[104:107]\n"
"ds_write_b128 v3, v[96:99] offset:12288\n"
"v_mfma_f32_16x16x32_bf16 v[104:107], a[160:163], v[148:151], v[104:107]\n"
"buffer_load_dwordx4 v[96:99], v0, s[4:7], s98 offen offset:2048 sc1\n"
"v_mfma_f32_16x16x32_bf16 v[120:123], a[192:195], v[148:151], 0\n"
"ds_read_b128 v[156:159], v4 offset:1024\n"
"ds_read_b128 v[128:131], v5 offset:1024\n"
"v_mfma_f32_16x16x32_bf16 v[120:123], a[196:199], v[160:163], v[120:123]\n"
"s_waitcnt lgkmcnt(3)\n"
"v_add_f32_e32 v144, v144, v255\n"
"v_add_f32_e32 v145, v145, v254\n"
"v_mfma_f32_16x16x32_bf16 v[120:123], a[200:203], v[124:127], v[120:123]\n"
"buffer_load_dwordx4 v[100:103], v0, s[4:7], s98 offen offset:3072 sc1\n"
"v_mfma_f32_16x16x32_bf16 v[120:123], a[204:207], v[140:143], v[120:123]\n"
"v_add_f32_e32 v146, v146, v253\n"
"v_add_f32_e32 v147, v147, v252\n"
"v_mfma_f32_16x16x32_bf16 v[120:123], a[208:211], v[152:155], v[120:123]\n"
"v_add_f32_e32 v144, v144, v112\n"
"v_add_f32_e32 v145, v145, v113\n"
"v_mfma_f32_16x16x32_bf16 v[120:123], a[212:215], v[164:167], v[120:123]\n"
"ds_write_b128 v3, v[104:107] offset:13312\n"
"v_mfma_f32_16x16x32_bf16 v[120:123], a[216:219], v[116:119], v[120:123]\n"
"buffer_load_dwordx4 v[104:107], v1, s[4:7], s98 offen sc1\n"
"v_mfma_f32_16x16x32_bf16 v[120:123], a[220:223], v[132:135], v[120:123]\n"
"ds_read_b128 v[108:111], v4 offset:2048\n"
"ds_read_b128 v[136:139], v5 offset:2048\n"
"v_mfma_f32_16x16x32_bf16 v[200:203], a[252:255], v[132:135], 0\n"
"s_waitcnt lgkmcnt(3)\n"
"v_add_f32_e32 v146, v146, v114\n"
"v_add_f32_e32 v147, v147, v115\n"
"v_mfma_f32_16x16x32_bf16 v[200:203], a[248:251], v[116:119], v[200:203]\n"
"buffer_load_dwordx4 v[112:115], v1, s[4:7], s98 offen offset:1024 sc1\n"
"v_mfma_f32_16x16x32_bf16 v[200:203], a[244:247], v[164:167], v[200:203]\n"
"v_add_f32_e32 v156, v156, v251\n"
"v_add_f32_e32 v157, v157, v250\n"
"v_mfma_f32_16x16x32_bf16 v[200:203], a[240:243], v[152:155], v[200:203]\n"
"v_add_f32_e32 v158, v158, v249\n"
"v_add_f32_e32 v159, v159, v248\n"
"v_mfma_f32_16x16x32_bf16 v[200:203], a[236:239], v[140:143], v[200:203]\n"
"buffer_load_dwordx4 v[116:119], v1, s[4:7], s98 offen offset:2048 sc1\n"
"v_mfma_f32_16x16x32_bf16 v[200:203], a[232:235], v[124:127], v[200:203]\n"
"ds_write_b128 v3, v[120:123] offset:14336\n"
"v_mfma_f32_16x16x32_bf16 v[200:203], a[228:231], v[160:163], v[200:203]\n"
"ds_read_b128 v[120:123], v4 offset:3072\n"
"ds_read_b128 v[152:155], v5 offset:3072\n"
"v_mfma_f32_16x16x32_bf16 v[200:203], a[224:227], v[148:151], v[200:203]\n"
"s_waitcnt vmcnt(11)\n"
"buffer_load_dwordx4 v[124:127], v1, s[4:7], s98 offen offset:3072 sc1\n"
"; @A G2 a.3\n"
"v_mfma_f32_16x16x32_bf16 v[164:167], a[128:131], v[172:175], v[144:147]\n"
"s_add_u32 s98, s53, 0x440000\n"
"v_add_f32_e32 v156, v156, v128\n"
"v_add_f32_e32 v157, v157, v129\n"
"v_mfma_f32_16x16x32_bf16 v[164:167], a[132:135], v[176:179], v[164:167]\n"
"s_waitcnt lgkmcnt(3)\n"
"v_add_f32_e32 v158, v158, v130\n"
"v_add_f32_e32 v159, v159, v131\n"
"v_mfma_f32_16x16x32_bf16 v[164:167], a[136:139], v[168:171], v[164:167]\n"
"buffer_load_dwordx4 v[128:131], v0, s[4:7], s98 offen sc1\n"
"v_mfma_f32_16x16x32_bf16 v[164:167], a[140:143], v[180:183], v[164:167]\n"
"s_waitcnt vmcnt(9)\n"
"v_add_f32_e32 v108, v108, v247\n"
"v_add_f32_e32 v109, v109, v246\n"
"v_mfma_f32_16x16x32_bf16 v[164:167], a[144:147], v[184:187], v[164:167]\n"
"v_add_f32_e32 v110, v110, v245\n"
"v_add_f32_e32 v111, v111, v244\n"
"v_mfma_f32_16x16x32_bf16 v[164:167], a[148:151], v[188:191], v[164:167]\n"
"ds_write_b128 v3, v[200:203] offset:15360\n"
"v_mfma_f32_16x16x32_bf16 v[164:167], a[152:155], v[192:195], v[164:167]\n"
"buffer_load_dwordx4 v[132:135], v0, s[4:7], s98 offen offset:1024 sc1\n"
"v_mfma_f32_16x16x32_bf16 v[164:167], a[156:159], v[196:199], v[164:167]\n"
"v_add_f32_e32 v108, v108, v136\n"
"v_add_f32_e32 v109, v109, v137\n"
"v_mfma_f32_16x16x32_bf16 v[200:203], a[188:191], v[196:199], v[156:159]\n"
"s_waitcnt lgkmcnt(1)\n"
"v_add_f32_e32 v110, v110, v138\n"
"v_add_f32_e32 v111, v111, v139\n"
"v_mfma_f32_16x16x32_bf16 v[200:203], a[184:187], v[192:195], v[200:203]\n"
"s_waitcnt lgkmcnt(0)\n"
"s_barrier\n"
"v_mfma_f32_16x16x32_bf16 v[200:203], a[180:183], v[188:191], v[200:203]\n"
"buffer_load_dwordx4 v[136:139], v0, s[4:7], s98 offen offset:2048 sc1\n"
"v_mfma_f32_16x16x32_bf16 v[200:203], a[176:179], v[184:187], v[200:203]\n"
"v_add_f32_e32 v120, v120, v243\n"
"v_add_f32_e32 v121, v121, v242\n"
"v_mfma_f32_16x16x32_bf16 v[200:203], a[172:175], v[180:183], v[200:203]\n"
"v_cmp_u_f32_e32 vcc, v164, v164\n"
"v_add_f32_e32 v122, v122, v241\n"
"v_mfma_f32_16x16x32_bf16 v[200:203], a[168:171], v[168:171], v[200:203]\n"
"s_cbranch_vccnz .Lmk_p118s\n"
".Lmk_p118r:\n"
"v_add_f32_e32 v123, v123, v240\n"
"v_mfma_f32_16x16x32_bf16 v[200:203], a[164:167], v[176:179], v[200:203]\n"
"buffer_load_dwordx4 v[140:143], v0, s[4:7], s98 offen offset:3072 sc1\n"
"v_mfma_f32_16x16x32_bf16 v[200:203], a[160:163], v[172:175], v[200:203]\n"
"v_add_f32_e32 v120, v120, v152\n"
"v_add_f32_e32 v121, v121, v153\n"
"v_mfma_f32_16x16x32_bf16 v[108:111], a[192:195], v[172:175], v[108:111]\n"
"v_add_f32_e32 v122, v122, v154\n"
"v_add_f32_e32 v123, v123, v155\n"
"v_mfma_f32_16x16x32_bf16 v[108:111], a[196:199], v[176:179], v[108:111]\n"
"buffer_load_dwordx4 v[144:147], v1, s[4:7], s98 offen sc1\n"
"v_mfma_f32_16x16x32_bf16 v[108:111], a[200:203], v[168:171], v[108:111]\n"
"ds_read_b128 v[204:207], v6\n"
"ds_read_b128 v[208:211], v6 offset:1024\n"
"v_mfma_f32_16x16x32_bf16 v[108:111], a[204:207], v[180:183], v[108:111]\n"
"ds_read_b128 v[152:155], v6 offset:2048\n"
"ds_read_b128 v[156:159], v6 offset:3072\n"
"v_mfma_f32_16x16x32_bf16 v[108:111], a[208:211], v[184:187], v[108:111]\n"
"buffer_load_dwordx4 v[148:151], v1, s[4:7], s98 offen offset:1024 sc1\n"
"v_mfma_f32_16x16x32_bf16 v[108:111], a[212:215], v[188:191], v[108:111]\n"
"v_mfma_f32_16x16x32_bf16 v[108:111], a[216:219], v[192:195], v[108:111]\n"
"v_mfma_f32_16x16x32_bf16 v[108:111], a[220:223], v[196:199], v[108:111]\n"
"buffer_load_dwordx4 v[160:163], v1, s[4:7], s98 offen offset:2048 sc1\n"
"v_mfma_f32_16x16x32_bf16 v[120:123], a[252:255], v[196:199], v[120:123]\n"
"s_waitcnt lgkmcnt(0)\n"
"v_add_f32_e32 v204, v164, v204\n"
"v_add_f32_e32 v205, v165, v205\n"
"v_mfma_f32_16x16x32_bf16 v[120:123], a[248:251], v[192:195], v[120:123]\n"
"v_add_f32_e32 v206, v166, v206\n"
"v_add_f32_e32 v207, v167, v207\n"
"v_mfma_f32_16x16x32_bf16 v[120:123], a[244:247], v[188:191], v[120:123]\n"
"buffer_load_dwordx4 v[164:167], v1, s[4:7], s98 offen offset:3072 sc1\n"
"v_mfma_f32_16x16x32_bf16 v[120:123], a[240:243], v[184:187], v[120:123]\n"
"v_add_f32_e32 v208, v200, v208\n"
"v_add_f32_e32 v209, v201, v209\n"
"v_mfma_f32_16x16x32_bf16 v[120:123], a[236:239], v[180:183], v[120:123]\n"
"v_add_f32_e32 v210, v202, v210\n"
"v_add_f32_e32 v211, v203, v211\n"
"v_mfma_f32_16x16x32_bf16 v[120:123], a[232:235], v[168:171], v[120:123]\n"
"v_cvt_pk_bf16_f32 v168, v204, v205\n"
"v_cvt_pk_bf16_f32 v169, v206, v207\n"
"v_mfma_f32_16x16x32_bf16 v[120:123], a[228:231], v[176:179], v[120:123]\n"
"s_add_u32 s98, s61, 0x400000\n"
"v_cvt_pk_bf16_f32 v170, v208, v209\n"
"v_cvt_pk_bf16_f32 v171, v210, v211\n"
"v_mfma_f32_16x16x32_bf16 v[120:123], a[224:227], v[172:175], v[120:123]\n"
"buffer_store_dwordx4 v[168:171], v0, s[4:7], s98 offen sc1\n"
"ds_write_b128 v2, v[168:171]\n"
"s_branch .Lmk_loop\n"
".Lmk_f1s:\n"
";\n"
"s_mov_b32 s1, 0x10000\n"
".Lmk_f1l:\n"
"buffer_load_dwordx2 v[250:251], v12, s[4:7], s98 offen sc1\n"
"s_waitcnt vmcnt(0)\n"
"v_cmp_ne_u64_e32 vcc, v[250:251], v[18:19]\n"
"s_cbranch_vccz .Lmk_f1r\n"
"s_sub_u32 s1, s1, 1\n"
"s_cbranch_scc0 .Lmk_f1l\n"
"s_branch .Lmk_f1r\n"
".Lmk_f2s:\n"
";\n"
"s_mov_b32 s1, 0x10000\n"
".Lmk_f2l:\n"
"buffer_load_dwordx2 v[246:247], v12, s[4:7], s97 offen sc1\n"
"s_waitcnt vmcnt(0)\n"
"v_cmp_ne_u64_e32 vcc, v[246:247], v[18:19]\n"
"s_cbranch_vccz .Lmk_f2r\n"
"s_sub_u32 s1, s1, 1\n"
"s_cbranch_scc0 .Lmk_f2l\n"
"s_branch .Lmk_f2r\n"
".Lmk_f5s:\n"
";\n"
"s_mov_b32 s1, 0x10000\n"
".Lmk_f5l:\n"
"buffer_load_dwordx2 v[250:251], v41, s[4:7], s97 offen sc1\n"
"s_waitcnt vmcnt(0)\n"
"v_cmp_ne_u64_e32 vcc, v[250:251], v[18:19]\n"
"s_cbranch_vccz .Lmk_f5r\n"
"s_sub_u32 s1, s1, 1\n"
"s_cbranch_scc0 .Lmk_f5l\n"
"s_branch .Lmk_f5r\n"
".Lmk_w7s:\n"
"buffer_store_dwordx4 a[4:7], v0, s[4:7], s97 offen sc1\n"
"s_branch .Lmk_w7r\n"
".Lmk_w8s:\n"
"buffer_store_dwordx4 a[36:39], v0, s[4:7], s97 offen sc1\n"
"s_branch .Lmk_w8r\n"
".Lmk_w9s:\n"
"buffer_store_dwordx4 a[68:71], v0, s[4:7], s96 offen sc1\n"
"s_branch .Lmk_w9r\n"
".Lmk_w10s:\n"
"buffer_store_dwordx4 a[100:103], v0, s[4:7], s96 offen sc1\n"
"s_branch .Lmk_w10r\n"
".Lmk_w11s:\n"
"buffer_store_dwordx4 v[52:55], v0, s[4:7], s96 offen sc1\n"
"s_branch .Lmk_w11r\n"
".Lmk_w12s:\n"
"buffer_store_dwordx4 v[56:59], v0, s[4:7], s96 offen sc1\n"
"s_branch .Lmk_w12r\n"
".Lmk_f6s:\n"
";\n"
"s_mov_b32 s1, 0x10000\n"
".Lmk_f6l:\n"
"buffer_load_dwordx2 v[246:247], v13, s[4:7], s97 offen sc1\n"
"s_waitcnt vmcnt(0)\n"
"v_cmp_ne_u64_e32 vcc, v[246:247], v[18:19]\n"
"s_cbranch_vccz .Lmk_f6r\n"
"s_sub_u32 s1, s1, 1\n"
"s_cbranch_scc0 .Lmk_f6l\n"
"s_branch .Lmk_f6r\n"
".Lmk_f3s:\n"
";\n"
"s_mov_b32 s1, 0x10000\n"
".Lmk_f3l:\n"
"buffer_load_dwordx2 v[250:251], off, s[4:7], s96 sc1\n"
"s_waitcnt vmcnt(0)\n"
"v_cmp_ne_u64_e32 vcc, v[250:251], v[18:19]\n"
"s_cbranch_vccz .Lmk_f3r\n"
"s_sub_u32 s1, s1, 1\n"
"s_cbranch_scc0 .Lmk_f3l\n"
"s_branch .Lmk_f3r\n"
".Lmk_x23s:\n"
"s_nop 3\n"
"v_mov_b32_e32 v24, 0xff800000\n"
"v_mov_b32_e32 v25, 0xff800000\n"
"v_mov_b32_e32 v26, 0xff800000\n"
"v_mov_b32_e32 v27, 0xff800000\n"
"v_mov_b32_e32 v36, 0xff800000\n"
"v_mov_b32_e32 v37, 0xff800000\n"
"v_mov_b32_e32 v38, 0xff800000\n"
"v_mov_b32_e32 v39, 0xff800000\n"
"s_nop 1\n"
"s_branch .Lmk_x23r\n"
".Lmk_x24s:\n"
"s_nop 3\n"
"v_mov_b32_e32 v24, 0xff800000\n"
"v_mov_b32_e32 v25, 0xff800000\n"
"v_mov_b32_e32 v26, 0xff800000\n"
"v_mov_b32_e32 v27, 0xff800000\n"
"v_mov_b32_e32 v36, 0xff800000\n"
"v_mov_b32_e32 v37, 0xff800000\n"
"v_mov_b32_e32 v38, 0xff800000\n"
"v_mov_b32_e32 v39, 0xff800000\n"
"s_nop 1\n"
"s_branch .Lmk_x24r\n"
".Lmk_x25s:\n"
"s_nop 3\n"
"v_mov_b32_e32 v24, 0xff800000\n"
"v_mov_b32_e32 v25, 0xff800000\n"
"v_mov_b32_e32 v26, 0xff800000\n"
"v_mov_b32_e32 v27, 0xff800000\n"
"v_mov_b32_e32 v36, 0xff800000\n"
"v_mov_b32_e32 v37, 0xff800000\n"
"v_mov_b32_e32 v38, 0xff800000\n"
"v_mov_b32_e32 v39, 0xff800000\n"
"s_nop 1\n"
"s_branch .Lmk_x25r\n"
".Lmk_x26s:\n"
"s_nop 3\n"
"v_mov_b32_e32 v24, 0xff800000\n"
"v_mov_b32_e32 v25, 0xff800000\n"
"v_mov_b32_e32 v26, 0xff800000\n"
"v_mov_b32_e32 v27, 0xff800000\n"
"v_mov_b32_e32 v36, 0xff800000\n"
"v_mov_b32_e32 v37, 0xff800000\n"
"v_mov_b32_e32 v38, 0xff800000\n"
"v_mov_b32_e32 v39, 0xff800000\n"
"s_nop 1\n"
"s_branch .Lmk_x26r\n"
".Lmk_f19s:\n"
";\n"
"s_mov_b32 s1, 0x10000\n"
".Lmk_f19l:\n"
"buffer_load_dwordx2 v[238:239], off, s[4:7], s97 sc1\n"
"s_waitcnt vmcnt(0)\n"
"v_cmp_ne_u64_e32 vcc, v[238:239], v[18:19]\n"
"s_cbranch_vccz .Lmk_f19r\n"
"s_sub_u32 s1, s1, 1\n"
"s_cbranch_scc0 .Lmk_f19l\n"
"s_branch .Lmk_f19r\n"
".Lmk_b21:\n"
"s_cmp_lg_u32 s2, 0\n"
"s_cbranch_scc1 .Lmk_k27\n"
"s_mov_b32 s2, 1\n"
"s_waitcnt vmcnt(0) lgkmcnt(0)\n"
"s_nop 7\n"
"s_mov_b32 s97, 11\n"
"v_add_u32_e32 v248, 0x3800, v15\n"
"v_add_u32_e32 v247, 0x3800, v254\n"
"ds_read_b128 v[36:39], v15 offset:24576\n"
"ds_read_b128 v[40:43], v254 offset:24832\n"
"s_waitcnt lgkmcnt(0)\n"
"v_mfma_f32_16x16x32_bf16 v[48:51], v[36:39], v[28:31], 0\n"
"v_mfma_f32_16x16x32_bf16 v[52:55], v[36:39], v[32:35], 0\n"
"v_mfma_f32_16x16x32_bf16 v[36:39], v[40:43], v[32:35], 0\n"
"v_mfma_f32_16x16x32_bf16 v[56:59], v[40:43], v[28:31], 0\n"
".Lmk_b21l:\n"
"ds_read_b128 v[40:43], v248 offset:8192\n"
"ds_read_b128 v[60:63], v247 offset:8448\n"
"s_cmp_gt_i32 s78, s97\n"
"s_cselect_b32 s96, 0xff800000, 0\n"
"v_mov_b32_e32 v64, s96\n"
"v_mov_b32_e32 v65, s96\n"
"v_mov_b32_e32 v66, s96\n"
"v_mov_b32_e32 v67, s96\n"
"v_subrev_u32_e32 v248, 0x800, v248\n"
"v_subrev_u32_e32 v247, 0x800, v247\n"
"s_waitcnt lgkmcnt(0)\n"
"v_mfma_f32_16x16x32_bf16 v[72:75], v[40:43], v[28:31], v[64:67]\n"
"v_mfma_f32_16x16x32_bf16 v[76:79], v[40:43], v[32:35], v[64:67]\n"
"v_mfma_f32_16x16x32_bf16 v[40:43], v[60:63], v[32:35], v[64:67]\n"
"v_mfma_f32_16x16x32_bf16 v[84:87], v[60:63], v[28:31], v[64:67]\n"
"s_nop 7\n"
"s_cmp_lg_u32 s97, 4\n"
"s_cbranch_scc1 .Lmk_b21m\n"
"v_cndmask_b32_e64 v72, v48, v72, s[20:21]\n"
"v_cndmask_b32_e64 v73, v49, v73, s[22:23]\n"
"v_cndmask_b32_e64 v74, v50, v74, s[24:25]\n"
"v_cndmask_b32_e64 v75, v51, v75, s[26:27]\n"
"v_cndmask_b32_e64 v84, v56, v84, s[28:29]\n"
"v_cndmask_b32_e64 v85, v57, v85, s[30:31]\n"
"v_cndmask_b32_e64 v86, v58, v86, s[32:33]\n"
"v_cndmask_b32_e64 v87, v59, v87, s[34:35]\n"
"v_cndmask_b32_e64 v76, v52, v76, s[36:37]\n"
"v_cndmask_b32_e64 v77, v53, v77, s[38:39]\n"
"v_cndmask_b32_e64 v78, v54, v78, s[40:41]\n"
"v_cndmask_b32_e64 v79, v55, v79, s[42:43]\n"
"v_cndmask_b32_e64 v40, v36, v40, s[44:45]\n"
"v_cndmask_b32_e64 v41, v37, v41, s[46:47]\n"
"v_cndmask_b32_e64 v42, v38, v42, s[48:49]\n"
"v_cndmask_b32_e64 v43, v39, v43, s[50:51]\n"
".Lmk_b21m:\n"
"v_max3_f32 v253, v253, v72, v73\n"
"v_max3_f32 v251, v251, v76, v77\n"
"v_max3_f32 v253, v253, v74, v75\n"
"v_max3_f32 v251, v251, v78, v79\n"
"v_max3_f32 v253, v253, v84, v85\n"
"v_max3_f32 v251, v251, v40, v41\n"
"v_max3_f32 v253, v253, v86, v87\n"
"v_max3_f32 v251, v251, v42, v43\n"
"s_sub_u32 s97, s97, 1\n"
"s_cmp_ge_i32 s97, 4\n"
"s_cbranch_scc1 .Lmk_b21l\n"
"v_mov_b32_e32 v248, v253\n"
"v_mov_b32_e32 v247, v251\n"
"s_nop 0\n"
"v_permlane16_swap_b32_e32 v253, v248\n"
"v_permlane16_swap_b32_e32 v251, v247\n"
"v_max_f32_e32 v253, v253, v248\n"
"v_max_f32_e32 v251, v251, v247\n"
"v_mov_b32_e32 v248, v253\n"
"v_mov_b32_e32 v247, v251\n"
"s_nop 0\n"
"v_permlane32_swap_b32_e32 v253, v248\n"
"v_permlane32_swap_b32_e32 v251, v247\n"
"v_max_f32_e32 v253, v253, v248\n"
"v_max_f32_e32 v251, v251, v247\n"
"v_mov_b32_e32 v250, v253\n"
"v_mov_b32_e32 v249, v251\n"
"s_branch .Lmk_a20\n"
".Lmk_x30s:\n"
"s_nop 3\n"
"v_mov_b32_e32 v116, 0xff800000\n"
"v_mov_b32_e32 v117, 0xff800000\n"
"v_mov_b32_e32 v118, 0xff800000\n"
"v_mov_b32_e32 v119, 0xff800000\n"
"v_mov_b32_e32 v120, 0xff800000\n"
"v_mov_b32_e32 v121, 0xff800000\n"
"v_mov_b32_e32 v122, 0xff800000\n"
"v_mov_b32_e32 v123, 0xff800000\n"
"s_nop 1\n"
"s_branch .Lmk_x30r\n"
".Lmk_x31s:\n"
"s_nop 3\n"
"v_mov_b32_e32 v116, 0xff800000\n"
"v_mov_b32_e32 v117, 0xff800000\n"
"v_mov_b32_e32 v118, 0xff800000\n"
"v_mov_b32_e32 v119, 0xff800000\n"
"v_mov_b32_e32 v120, 0xff800000\n"
"v_mov_b32_e32 v121, 0xff800000\n"
"v_mov_b32_e32 v122, 0xff800000\n"
"v_mov_b32_e32 v123, 0xff800000\n"
"s_nop 1\n"
"s_branch .Lmk_x31r\n"
".Lmk_x32s:\n"
"s_nop 3\n"
"v_mov_b32_e32 v116, 0xff800000\n"
"v_mov_b32_e32 v117, 0xff800000\n"
"v_mov_b32_e32 v118, 0xff800000\n"
"v_mov_b32_e32 v119, 0xff800000\n"
"v_mov_b32_e32 v120, 0xff800000\n"
"v_mov_b32_e32 v121, 0xff800000\n"
"v_mov_b32_e32 v122, 0xff800000\n"
"v_mov_b32_e32 v123, 0xff800000\n"
"s_nop 1\n"
"s_branch .Lmk_x32r\n"
".Lmk_f4s:\n"
";\n"
"s_mov_b32 s1, 0x10000\n"
".Lmk_f4l:\n"
"buffer_load_dwordx2 v[246:247], v12, s[4:7], s96 offen sc1\n"
"s_waitcnt vmcnt(0)\n"
"v_cmp_ne_u64_e32 vcc, v[246:247], v[18:19]\n"
"s_cbranch_vccz .Lmk_f4r\n"
"s_sub_u32 s1, s1, 1\n"
"s_cbranch_scc0 .Lmk_f4l\n"
"s_branch .Lmk_f4r\n"
".Lmk_b29:\n"
"s_cmp_lg_u32 s2, 0\n"
"s_cbranch_scc1 .Lmk_k34\n"
"s_mov_b32 s2, 1\n"
"s_waitcnt vmcnt(0) lgkmcnt(0)\n"
"s_nop 7\n"
"s_mov_b32 s96, 7\n"
"v_add_u32_e32 v248, 0x3800, v15\n"
"v_add_u32_e32 v247, 0x3800, v254\n"
"ds_read_b128 v[172:175], v15 offset:16384\n"
"ds_read_b128 v[192:195], v254 offset:16640\n"
"s_waitcnt lgkmcnt(0)\n"
"v_mfma_f32_16x16x32_bf16 v[196:199], v[172:175], v[92:95], 0\n"
"v_mfma_f32_16x16x32_bf16 v[212:215], v[172:175], v[96:99], 0\n"
"v_mfma_f32_16x16x32_bf16 v[172:175], v[192:195], v[96:99], 0\n"
"v_mfma_f32_16x16x32_bf16 v[220:223], v[192:195], v[92:95], 0\n"
".Lmk_b29l:\n"
"ds_read_b128 v[192:195], v248\n"
"ds_read_b128 v[224:227], v247 offset:256\n"
"s_cmp_gt_i32 s78, s96\n"
"s_cselect_b32 s95, 0xff800000, 0\n"
"v_mov_b32_e32 v228, s95\n"
"v_mov_b32_e32 v229, s95\n"
"v_mov_b32_e32 v230, s95\n"
"v_mov_b32_e32 v231, s95\n"
"v_subrev_u32_e32 v248, 0x800, v248\n"
"v_subrev_u32_e32 v247, 0x800, v247\n"
"s_waitcnt lgkmcnt(0)\n"
"v_mfma_f32_16x16x32_bf16 v[232:235], v[192:195], v[92:95], v[228:231]\n"
"v_mfma_f32_16x16x32_bf16 v[236:239], v[192:195], v[96:99], v[228:231]\n"
"v_mfma_f32_16x16x32_bf16 v[192:195], v[224:227], v[96:99], v[228:231]\n"
"v_mfma_f32_16x16x32_bf16 v[240:243], v[224:227], v[92:95], v[228:231]\n"
"s_nop 7\n"
"s_cmp_lg_u32 s96, 0\n"
"s_cbranch_scc1 .Lmk_b29m\n"
"v_cndmask_b32_e64 v232, v196, v232, s[20:21]\n"
"v_cndmask_b32_e64 v233, v197, v233, s[22:23]\n"
"v_cndmask_b32_e64 v234, v198, v234, s[24:25]\n"
"v_cndmask_b32_e64 v235, v199, v235, s[26:27]\n"
"v_cndmask_b32_e64 v240, v220, v240, s[28:29]\n"
"v_cndmask_b32_e64 v241, v221, v241, s[30:31]\n"
"v_cndmask_b32_e64 v242, v222, v242, s[32:33]\n"
"v_cndmask_b32_e64 v243, v223, v243, s[34:35]\n"
"v_cndmask_b32_e64 v236, v212, v236, s[36:37]\n"
"v_cndmask_b32_e64 v237, v213, v237, s[38:39]\n"
"v_cndmask_b32_e64 v238, v214, v238, s[40:41]\n"
"v_cndmask_b32_e64 v239, v215, v239, s[42:43]\n"
"v_cndmask_b32_e64 v192, v172, v192, s[44:45]\n"
"v_cndmask_b32_e64 v193, v173, v193, s[46:47]\n"
"v_cndmask_b32_e64 v194, v174, v194, s[48:49]\n"
"v_cndmask_b32_e64 v195, v175, v195, s[50:51]\n"
".Lmk_b29m:\n"
"v_max3_f32 v253, v253, v232, v233\n"
"v_max3_f32 v251, v251, v236, v237\n"
"v_max3_f32 v253, v253, v234, v235\n"
"v_max3_f32 v251, v251, v238, v239\n"
"v_max3_f32 v253, v253, v240, v241\n"
"v_max3_f32 v251, v251, v192, v193\n"
"v_max3_f32 v253, v253, v242, v243\n"
"v_max3_f32 v251, v251, v194, v195\n"
"s_sub_u32 s96, s96, 1\n"
"s_cmp_ge_i32 s96, 0\n"
"s_cbranch_scc1 .Lmk_b29l\n"
"v_mov_b32_e32 v248, v253\n"
"v_mov_b32_e32 v247, v251\n"
"s_nop 0\n"
"v_permlane16_swap_b32_e32 v253, v248\n"
"v_permlane16_swap_b32_e32 v251, v247\n"
"v_max_f32_e32 v253, v253, v248\n"
"v_max_f32_e32 v251, v251, v247\n"
"v_mov_b32_e32 v248, v253\n"
"v_mov_b32_e32 v247, v251\n"
"s_nop 0\n"
"v_permlane32_swap_b32_e32 v253, v248\n"
"v_permlane32_swap_b32_e32 v251, v247\n"
"v_max_f32_e32 v253, v253, v248\n"
"v_max_f32_e32 v251, v251, v247\n"
"v_mov_b32_e32 v250, v253\n"
"v_mov_b32_e32 v249, v251\n"
"s_branch .Lmk_a28\n"
".Lmk_p35s:\n"
";\n"
"s_waitcnt vmcnt(16)\n"
"s_mov_b32 s1, 0x10000\n"
".Lmk_p35l:\n"
"s_add_u32 s98, s56, 0x40000\n"
"buffer_load_dwordx4 v[176:179], v0, s[4:7], s98 offen sc1\n"
"buffer_load_dwordx4 v[200:203], v0, s[4:7], s98 offen offset:1024 sc1\n"
"buffer_load_dwordx4 v[188:191], v0, s[4:7], s98 offen offset:2048 sc1\n"
"buffer_load_dwordx4 v[204:207], v0, s[4:7], s98 offen offset:3072 sc1\n"
"buffer_load_dwordx4 v[152:155], v1, s[4:7], s98 offen sc1\n"
"buffer_load_dwordx4 v[208:211], v1, s[4:7], s98 offen offset:1024 sc1\n"
"buffer_load_dwordx4 v[120:123], v1, s[4:7], s98 offen offset:2048 sc1\n"
"buffer_load_dwordx4 v[132:135], v1, s[4:7], s98 offen offset:3072 sc1\n"
"s_add_u32 s98, s57, 0x40000\n"
"buffer_load_dwordx4 v[216:219], v0, s[4:7], s98 offen sc1\n"
"buffer_load_dwordx4 v[128:131], v0, s[4:7], s98 offen offset:1024 sc1\n"
"buffer_load_dwordx4 v[180:183], v0, s[4:7], s98 offen offset:2048 sc1\n"
"buffer_load_dwordx4 v[100:103], v0, s[4:7], s98 offen offset:3072 sc1\n"
"buffer_load_dwordx4 v[184:187], v1, s[4:7], s98 offen sc1\n"
"buffer_load_dwordx4 v[104:107], v1, s[4:7], s98 offen offset:1024 sc1\n"
"buffer_load_dwordx4 v[108:111], v1, s[4:7], s98 offen offset:2048 sc1\n"
"buffer_load_dwordx4 v[136:139], v1, s[4:7], s98 offen offset:3072 sc1\n"
"s_add_u32 s98, s58, 0x40000\n"
"buffer_load_dwordx4 v[144:147], v0, s[4:7], s98 offen sc1\n"
"buffer_load_dwordx4 v[140:143], v0, s[4:7], s98 offen offset:1024 sc1\n"
"buffer_load_dwordx4 v[116:119], v0, s[4:7], s98 offen offset:2048 sc1\n"
"buffer_load_dwordx4 v[112:115], v0, s[4:7], s98 offen offset:3072 sc1\n"
"buffer_load_dwordx4 v[92:95], v1, s[4:7], s98 offen sc1\n"
"buffer_load_dwordx4 v[96:99], v1, s[4:7], s98 offen offset:1024 sc1\n"
"buffer_load_dwordx4 v[148:151], v1, s[4:7], s98 offen offset:2048 sc1\n"
"s_waitcnt vmcnt(0)\n"
"v_max_u32_e32 v255, v176, v177\n"
"v_max3_u32 v255, v255, v178, v179\n"
"v_max3_u32 v255, v255, v200, v201\n"
"v_max3_u32 v255, v255, v202, v203\n"
"v_max3_u32 v255, v255, v188, v189\n"
"v_max3_u32 v255, v255, v190, v191\n"
"v_max3_u32 v255, v255, v204, v205\n"
"v_max3_u32 v255, v255, v206, v207\n"
"v_max3_u32 v255, v255, v152, v153\n"
"v_max3_u32 v255, v255, v154, v155\n"
"v_max3_u32 v255, v255, v208, v209\n"
"v_max3_u32 v255, v255, v210, v211\n"
"v_max3_u32 v255, v255, v120, v121\n"
"v_max3_u32 v255, v255, v122, v123\n"
"v_max3_u32 v255, v255, v132, v133\n"
"v_max3_u32 v255, v255, v134, v135\n"
"v_cmp_eq_u32_e32 vcc, -1, v255\n"
"s_cbranch_vccz .Lmk_p35k\n"
"s_sub_u32 s1, s1, 1\n"
"s_cbranch_scc0 .Lmk_p35l\n"
".Lmk_p35k:\n"
"v_mfma_f32_16x16x32_bf16 v[88:91], a[128:131], v[176:179], 0\n"
"v_mfma_f32_16x16x32_bf16 v[88:91], a[132:135], v[200:203], v[88:91]\n"
"v_mfma_f32_16x16x32_bf16 v[88:91], a[136:139], v[188:191], v[88:91]\n"
"v_mfma_f32_16x16x32_bf16 v[88:91], a[140:143], v[204:207], v[88:91]\n"
"v_mfma_f32_16x16x32_bf16 v[88:91], a[144:147], v[152:155], v[88:91]\n"
"v_mfma_f32_16x16x32_bf16 v[88:91], a[148:151], v[208:211], v[88:91]\n"
"v_mfma_f32_16x16x32_bf16 v[88:91], a[152:155], v[120:123], v[88:91]\n"
"v_mfma_f32_16x16x32_bf16 v[88:91], a[156:159], v[132:135], v[88:91]\n"
"v_mfma_f32_16x16x32_bf16 v[124:127], a[188:191], v[132:135], 0\n"
"v_mfma_f32_16x16x32_bf16 v[124:127], a[184:187], v[120:123], v[124:127]\n"
"v_mfma_f32_16x16x32_bf16 v[124:127], a[180:183], v[208:211], v[124:127]\n"
"v_mfma_f32_16x16x32_bf16 v[124:127], a[176:179], v[152:155], v[124:127]\n"
"v_mfma_f32_16x16x32_bf16 v[124:127], a[172:175], v[204:207], v[124:127]\n"
"v_mfma_f32_16x16x32_bf16 v[124:127], a[168:171], v[188:191], v[124:127]\n"
"s_nop 7\n"
"s_branch .Lmk_p35r\n"
".Lmk_p36s:\n"
";\n"
"s_waitcnt vmcnt(16)\n"
"s_mov_b32 s1, 0x10000\n"
".Lmk_p36l:\n"
"s_add_u32 s97, s57, 0x40000\n"
"buffer_load_dwordx4 v[216:219], v0, s[4:7], s97 offen sc1\n"
"buffer_load_dwordx4 v[128:131], v0, s[4:7], s97 offen offset:1024 sc1\n"
"buffer_load_dwordx4 v[180:183], v0, s[4:7], s97 offen offset:2048 sc1\n"
"buffer_load_dwordx4 v[100:103], v0, s[4:7], s97 offen offset:3072 sc1\n"
"buffer_load_dwordx4 v[184:187], v1, s[4:7], s97 offen sc1\n"
"buffer_load_dwordx4 v[104:107], v1, s[4:7], s97 offen offset:1024 sc1\n"
"buffer_load_dwordx4 v[108:111], v1, s[4:7], s97 offen offset:2048 sc1\n"
"buffer_load_dwordx4 v[136:139], v1, s[4:7], s97 offen offset:3072 sc1\n"
"s_add_u32 s97, s58, 0x40000\n"
"buffer_load_dwordx4 v[144:147], v0, s[4:7], s97 offen sc1\n"
"buffer_load_dwordx4 v[140:143], v0, s[4:7], s97 offen offset:1024 sc1\n"
"buffer_load_dwordx4 v[116:119], v0, s[4:7], s97 offen offset:2048 sc1\n"
"buffer_load_dwordx4 v[112:115], v0, s[4:7], s97 offen offset:3072 sc1\n"
"buffer_load_dwordx4 v[92:95], v1, s[4:7], s97 offen sc1\n"
"buffer_load_dwordx4 v[96:99], v1, s[4:7], s97 offen offset:1024 sc1\n"
"buffer_load_dwordx4 v[148:151], v1, s[4:7], s97 offen offset:2048 sc1\n"
"buffer_load_dwordx4 v[88:91], v1, s[4:7], s97 offen offset:3072 sc1\n"
"s_add_u32 s97, s59, 0x40000\n"
"buffer_load_dwordx4 v[160:163], v0, s[4:7], s97 offen sc1\n"
"buffer_load_dwordx4 v[124:127], v0, s[4:7], s97 offen offset:1024 sc1\n"
"buffer_load_dwordx4 v[120:123], v0, s[4:7], s97 offen offset:2048 sc1\n"
"buffer_load_dwordx4 v[132:135], v0, s[4:7], s97 offen offset:3072 sc1\n"
"buffer_load_dwordx4 v[156:159], v1, s[4:7], s97 offen sc1\n"
"buffer_load_dwordx4 v[164:167], v1, s[4:7], s97 offen offset:1024 sc1\n"
"buffer_load_dwordx4 v[172:175], v1, s[4:7], s97 offen offset:2048 sc1\n"
"s_waitcnt vmcnt(0)\n"
"v_max_u32_e32 v255, v216, v217\n"
"v_max3_u32 v255, v255, v218, v219\n"
"v_max3_u32 v255, v255, v128, v129\n"
"v_max3_u32 v255, v255, v130, v131\n"
"v_max3_u32 v255, v255, v180, v181\n"
"v_max3_u32 v255, v255, v182, v183\n"
"v_max3_u32 v255, v255, v100, v101\n"
"v_max3_u32 v255, v255, v102, v103\n"
"v_max3_u32 v255, v255, v184, v185\n"
"v_max3_u32 v255, v255, v186, v187\n"
"v_max3_u32 v255, v255, v104, v105\n"
"v_max3_u32 v255, v255, v106, v107\n"
"v_max3_u32 v255, v255, v108, v109\n"
"v_max3_u32 v255, v255, v110, v111\n"
"v_max3_u32 v255, v255, v136, v137\n"
"v_max3_u32 v255, v255, v138, v139\n"
"v_cmp_eq_u32_e32 vcc, -1, v255\n"
"s_cbranch_vccz .Lmk_p36k\n"
"s_sub_u32 s1, s1, 1\n"
"s_cbranch_scc0 .Lmk_p36l\n"
".Lmk_p36k:\n"
"v_mfma_f32_16x16x32_bf16 v[152:155], a[128:131], v[216:219], 0\n"
"v_mfma_f32_16x16x32_bf16 v[152:155], a[132:135], v[128:131], v[152:155]\n"
"v_mfma_f32_16x16x32_bf16 v[152:155], a[136:139], v[180:183], v[152:155]\n"
"v_mfma_f32_16x16x32_bf16 v[152:155], a[140:143], v[100:103], v[152:155]\n"
"v_mfma_f32_16x16x32_bf16 v[152:155], a[144:147], v[184:187], v[152:155]\n"
"v_mfma_f32_16x16x32_bf16 v[152:155], a[148:151], v[104:107], v[152:155]\n"
"v_mfma_f32_16x16x32_bf16 v[152:155], a[152:155], v[108:111], v[152:155]\n"
"v_mfma_f32_16x16x32_bf16 v[152:155], a[156:159], v[136:139], v[152:155]\n"
"v_mfma_f32_16x16x32_bf16 v[168:171], a[188:191], v[136:139], 0\n"
"v_mfma_f32_16x16x32_bf16 v[168:171], a[184:187], v[108:111], v[168:171]\n"
"v_mfma_f32_16x16x32_bf16 v[168:171], a[180:183], v[104:107], v[168:171]\n"
"v_mfma_f32_16x16x32_bf16 v[168:171], a[176:179], v[184:187], v[168:171]\n"
"v_mfma_f32_16x16x32_bf16 v[168:171], a[172:175], v[100:103], v[168:171]\n"
"v_mfma_f32_16x16x32_bf16 v[168:171], a[168:171], v[180:183], v[168:171]\n"
"s_nop 7\n"
"s_branch .Lmk_p36r\n"
".Lmk_p37s:\n"
";\n"
"s_waitcnt vmcnt(16)\n"
"s_mov_b32 s1, 0x10000\n"
".Lmk_p37l:\n"
"s_add_u32 s97, s58, 0x40000\n"
"buffer_load_dwordx4 v[144:147], v0, s[4:7], s97 offen sc1\n"
"buffer_load_dwordx4 v[140:143], v0, s[4:7], s97 offen offset:1024 sc1\n"
"buffer_load_dwordx4 v[116:119], v0, s[4:7], s97 offen offset:2048 sc1\n"
"buffer_load_dwordx4 v[112:115], v0, s[4:7], s97 offen offset:3072 sc1\n"
"buffer_load_dwordx4 v[92:95], v1, s[4:7], s97 offen sc1\n"
"buffer_load_dwordx4 v[96:99], v1, s[4:7], s97 offen offset:1024 sc1\n"
"buffer_load_dwordx4 v[148:151], v1, s[4:7], s97 offen offset:2048 sc1\n"
"buffer_load_dwordx4 v[88:91], v1, s[4:7], s97 offen offset:3072 sc1\n"
"s_add_u32 s97, s59, 0x40000\n"
"buffer_load_dwordx4 v[160:163], v0, s[4:7], s97 offen sc1\n"
"buffer_load_dwordx4 v[124:127], v0, s[4:7], s97 offen offset:1024 sc1\n"
"buffer_load_dwordx4 v[120:123], v0, s[4:7], s97 offen offset:2048 sc1\n"
"buffer_load_dwordx4 v[132:135], v0, s[4:7], s97 offen offset:3072 sc1\n"
"buffer_load_dwordx4 v[156:159], v1, s[4:7], s97 offen sc1\n"
"buffer_load_dwordx4 v[164:167], v1, s[4:7], s97 offen offset:1024 sc1\n"
"buffer_load_dwordx4 v[172:175], v1, s[4:7], s97 offen offset:2048 sc1\n"
"buffer_load_dwordx4 v[152:155], v1, s[4:7], s97 offen offset:3072 sc1\n"
"s_waitcnt vmcnt(0)\n"
"v_max_u32_e32 v239, v144, v145\n"
"v_max3_u32 v239, v239, v146, v147\n"
"v_max3_u32 v239, v239, v140, v141\n"
"v_max3_u32 v239, v239, v142, v143\n"
"v_max3_u32 v239, v239, v116, v117\n"
"v_max3_u32 v239, v239, v118, v119\n"
"v_max3_u32 v239, v239, v112, v113\n"
"v_max3_u32 v239, v239, v114, v115\n"
"v_max3_u32 v239, v239, v92, v93\n"
"v_max3_u32 v239, v239, v94, v95\n"
"v_max3_u32 v239, v239, v96, v97\n"
"v_max3_u32 v239, v239, v98, v99\n"
"v_max3_u32 v239, v239, v148, v149\n"
"v_max3_u32 v239, v239, v150, v151\n"
"v_max3_u32 v239, v239, v88, v89\n"
"v_max3_u32 v239, v239, v90, v91\n"
"v_cmp_eq_u32_e32 vcc, -1, v239\n"
"s_cbranch_vccz .Lmk_p37k\n"
"s_sub_u32 s1, s1, 1\n"
"s_cbranch_scc0 .Lmk_p37l\n"
".Lmk_p37k:\n"
"v_mfma_f32_16x16x32_bf16 v[100:103], a[128:131], v[144:147], 0\n"
"v_mfma_f32_16x16x32_bf16 v[100:103], a[132:135], v[140:143], v[100:103]\n"
"v_mfma_f32_16x16x32_bf16 v[100:103], a[136:139], v[116:119], v[100:103]\n"
"v_mfma_f32_16x16x32_bf16 v[100:103], a[140:143], v[112:115], v[100:103]\n"
"v_mfma_f32_16x16x32_bf16 v[100:103], a[144:147], v[92:95], v[100:103]\n"
"v_mfma_f32_16x16x32_bf16 v[100:103], a[148:151], v[96:99], v[100:103]\n"
"v_mfma_f32_16x16x32_bf16 v[100:103], a[152:155], v[148:151], v[100:103]\n"
"v_mfma_f32_16x16x32_bf16 v[100:103], a[156:159], v[88:91], v[100:103]\n"
"v_mfma_f32_16x16x32_bf16 v[104:107], a[188:191], v[88:91], 0\n"
"v_mfma_f32_16x16x32_bf16 v[104:107], a[184:187], v[148:151], v[104:107]\n"
"v_mfma_f32_16x16x32_bf16 v[104:107], a[180:183], v[96:99], v[104:107]\n"
"v_mfma_f32_16x16x32_bf16 v[104:107], a[176:179], v[92:95], v[104:107]\n"
"v_mfma_f32_16x16x32_bf16 v[104:107], a[172:175], v[112:115], v[104:107]\n"
"v_mfma_f32_16x16x32_bf16 v[104:107], a[168:171], v[116:119], v[104:107]\n"
"s_nop 7\n"
"s_branch .Lmk_p37r\n"
".Lmk_p38s:\n"
";\n"
"s_waitcnt vmcnt(16)\n"
"s_mov_b32 s1, 0x10000\n"
".Lmk_p38l:\n"
"s_add_u32 s97, s59, 0x40000\n"
"buffer_load_dwordx4 v[160:163], v0, s[4:7], s97 offen sc1\n"
"buffer_load_dwordx4 v[124:127], v0, s[4:7], s97 offen offset:1024 sc1\n"
"buffer_load_dwordx4 v[120:123], v0, s[4:7], s97 offen offset:2048 sc1\n"
"buffer_load_dwordx4 v[132:135], v0, s[4:7], s97 offen offset:3072 sc1\n"
"buffer_load_dwordx4 v[156:159], v1, s[4:7], s97 offen sc1\n"
"buffer_load_dwordx4 v[164:167], v1, s[4:7], s97 offen offset:1024 sc1\n"
"buffer_load_dwordx4 v[172:175], v1, s[4:7], s97 offen offset:2048 sc1\n"
"buffer_load_dwordx4 v[152:155], v1, s[4:7], s97 offen offset:3072 sc1\n"
"s_add_u32 s97, s56, 0\n"
"buffer_load_dwordx4 v[100:103], v0, s[4:7], s97 offen sc1\n"
"buffer_load_dwordx4 v[180:183], v0, s[4:7], s97 offen offset:1024 sc1\n"
"buffer_load_dwordx4 v[104:107], v0, s[4:7], s97 offen offset:2048 sc1\n"
"buffer_load_dwordx4 v[88:91], v0, s[4:7], s97 offen offset:3072 sc1\n"
"buffer_load_dwordx4 v[92:95], v1, s[4:7], s97 offen sc1\n"
"buffer_load_dwordx4 v[116:119], v1, s[4:7], s97 offen offset:1024 sc1\n"
"buffer_load_dwordx4 v[136:139], v1, s[4:7], s97 offen offset:2048 sc1\n"
"buffer_load_dwordx4 v[140:143], v1, s[4:7], s97 offen offset:3072 sc1\n"
"s_add_u32 s97, s57, 0\n"
"buffer_load_dwordx4 v[148:151], v0, s[4:7], s97 offen sc1\n"
"s_waitcnt vmcnt(0)\n"
"v_max_u32_e32 v255, v160, v161\n"
"v_max3_u32 v255, v255, v162, v163\n"
"v_max3_u32 v255, v255, v124, v125\n"
"v_max3_u32 v255, v255, v126, v127\n"
"v_max3_u32 v255, v255, v120, v121\n"
"v_max3_u32 v255, v255, v122, v123\n"
"v_max3_u32 v255, v255, v132, v133\n"
"v_max3_u32 v255, v255, v134, v135\n"
"v_max3_u32 v255, v255, v156, v157\n"
"v_max3_u32 v255, v255, v158, v159\n"
"v_max3_u32 v255, v255, v164, v165\n"
"v_max3_u32 v255, v255, v166, v167\n"
"v_max3_u32 v255, v255, v172, v173\n"
"v_max3_u32 v255, v255, v174, v175\n"
"v_max3_u32 v255, v255, v152, v153\n"
"v_max3_u32 v255, v255, v154, v155\n"
"v_cmp_eq_u32_e32 vcc, -1, v255\n"
"s_cbranch_vccz .Lmk_p38k\n"
"s_sub_u32 s1, s1, 1\n"
"s_cbranch_scc0 .Lmk_p38l\n"
".Lmk_p38k:\n"
"v_mfma_f32_16x16x32_bf16 v[128:131], a[128:131], v[160:163], v[108:111]\n"
"v_mfma_f32_16x16x32_bf16 v[128:131], a[132:135], v[124:127], v[128:131]\n"
"v_mfma_f32_16x16x32_bf16 v[128:131], a[136:139], v[120:123], v[128:131]\n"
"v_mfma_f32_16x16x32_bf16 v[128:131], a[140:143], v[132:135], v[128:131]\n"
"v_mfma_f32_16x16x32_bf16 v[128:131], a[144:147], v[156:159], v[128:131]\n"
"v_mfma_f32_16x16x32_bf16 v[128:131], a[148:151], v[164:167], v[128:131]\n"
"v_mfma_f32_16x16x32_bf16 v[128:131], a[152:155], v[172:175], v[128:131]\n"
"v_mfma_f32_16x16x32_bf16 v[128:131], a[156:159], v[152:155], v[128:131]\n"
"v_mfma_f32_16x16x32_bf16 v[144:147], a[188:191], v[152:155], v[168:171]\n"
"v_mfma_f32_16x16x32_bf16 v[144:147], a[184:187], v[172:175], v[144:147]\n"
"v_mfma_f32_16x16x32_bf16 v[144:147], a[180:183], v[164:167], v[144:147]\n"
"v_mfma_f32_16x16x32_bf16 v[144:147], a[176:179], v[156:159], v[144:147]\n"
"v_mfma_f32_16x16x32_bf16 v[144:147], a[172:175], v[132:135], v[144:147]\n"
"v_mfma_f32_16x16x32_bf16 v[144:147], a[168:171], v[120:123], v[144:147]\n"
"s_nop 7\n"
"s_branch .Lmk_p38r\n"
".Lmk_p40s:\n"
";\n"
"s_waitcnt vmcnt(16)\n"
"s_mov_b32 s1, 0x10000\n"
".Lmk_p40l:\n"
"s_add_u32 s95, s56, 0\n"
"buffer_load_dwordx4 v[100:103], v0, s[4:7], s95 offen sc1\n"
"buffer_load_dwordx4 v[180:183], v0, s[4:7], s95 offen offset:1024 sc1\n"
"buffer_load_dwordx4 v[104:107], v0, s[4:7], s95 offen offset:2048 sc1\n"
"buffer_load_dwordx4 v[88:91], v0, s[4:7], s95 offen offset:3072 sc1\n"
"buffer_load_dwordx4 v[92:95], v1, s[4:7], s95 offen sc1\n"
"buffer_load_dwordx4 v[116:119], v1, s[4:7], s95 offen offset:1024 sc1\n"
"buffer_load_dwordx4 v[136:139], v1, s[4:7], s95 offen offset:2048 sc1\n"
"buffer_load_dwordx4 v[140:143], v1, s[4:7], s95 offen offset:3072 sc1\n"
"s_add_u32 s95, s57, 0\n"
"buffer_load_dwordx4 v[148:151], v0, s[4:7], s95 offen sc1\n"
"buffer_load_dwordx4 v[108:111], v0, s[4:7], s95 offen offset:1024 sc1\n"
"buffer_load_dwordx4 v[112:115], v0, s[4:7], s95 offen offset:2048 sc1\n"
"buffer_load_dwordx4 v[196:199], v0, s[4:7], s95 offen offset:3072 sc1\n"
"buffer_load_dwordx4 v[200:203], v1, s[4:7], s95 offen sc1\n"
"buffer_load_dwordx4 v[128:131], v1, s[4:7], s95 offen offset:1024 sc1\n"
"buffer_load_dwordx4 v[120:123], v1, s[4:7], s95 offen offset:2048 sc1\n"
"buffer_load_dwordx4 v[132:135], v1, s[4:7], s95 offen offset:3072 sc1\n"
"s_add_u32 s95, s58, 0\n"
"buffer_load_dwordx4 v[152:155], v0, s[4:7], s95 offen sc1\n"
"buffer_load_dwordx4 v[160:163], v0, s[4:7], s95 offen offset:1024 sc1\n"
"buffer_load_dwordx4 v[124:127], v0, s[4:7], s95 offen offset:2048 sc1\n"
"s_waitcnt vmcnt(0)\n"
"v_max_u32_e32 v255, v100, v101\n"
"v_max3_u32 v255, v255, v102, v103\n"
"v_max3_u32 v255, v255, v180, v181\n"
"v_max3_u32 v255, v255, v182, v183\n"
"v_max3_u32 v255, v255, v104, v105\n"
"v_max3_u32 v255, v255, v106, v107\n"
"v_max3_u32 v255, v255, v88, v89\n"
"v_max3_u32 v255, v255, v90, v91\n"
"v_max3_u32 v255, v255, v92, v93\n"
"v_max3_u32 v255, v255, v94, v95\n"
"v_max3_u32 v255, v255, v116, v117\n"
"v_max3_u32 v255, v255, v118, v119\n"
"v_max3_u32 v255, v255, v136, v137\n"
"v_max3_u32 v255, v255, v138, v139\n"
"v_max3_u32 v255, v255, v140, v141\n"
"v_max3_u32 v255, v255, v142, v143\n"
"v_cmp_eq_u32_e32 vcc, -1, v255\n"
"s_cbranch_vccz .Lmk_p40k\n"
"s_sub_u32 s1, s1, 1\n"
"s_cbranch_scc0 .Lmk_p40l\n"
".Lmk_p40k:\n"
"v_mfma_f32_16x16x32_bf16 v[144:147], a[128:131], v[100:103], 0\n"
"v_mfma_f32_16x16x32_bf16 v[144:147], a[132:135], v[180:183], v[144:147]\n"
"v_mfma_f32_16x16x32_bf16 v[144:147], a[136:139], v[104:107], v[144:147]\n"
"v_mfma_f32_16x16x32_bf16 v[144:147], a[140:143], v[88:91], v[144:147]\n"
"v_mfma_f32_16x16x32_bf16 v[144:147], a[144:147], v[92:95], v[144:147]\n"
"v_mfma_f32_16x16x32_bf16 v[144:147], a[148:151], v[116:119], v[144:147]\n"
"v_mfma_f32_16x16x32_bf16 v[144:147], a[152:155], v[136:139], v[144:147]\n"
"v_mfma_f32_16x16x32_bf16 v[144:147], a[156:159], v[140:143], v[144:147]\n"
"v_mfma_f32_16x16x32_bf16 v[96:99], a[188:191], v[140:143], 0\n"
"v_mfma_f32_16x16x32_bf16 v[96:99], a[184:187], v[136:139], v[96:99]\n"
"v_mfma_f32_16x16x32_bf16 v[96:99], a[180:183], v[116:119], v[96:99]\n"
"v_mfma_f32_16x16x32_bf16 v[96:99], a[176:179], v[92:95], v[96:99]\n"
"v_mfma_f32_16x16x32_bf16 v[96:99], a[172:175], v[88:91], v[96:99]\n"
"v_mfma_f32_16x16x32_bf16 v[96:99], a[168:171], v[104:107], v[96:99]\n"
"s_nop 7\n"
"s_branch .Lmk_p40r\n"
".Lmk_p41s:\n"
";\n"
"s_waitcnt vmcnt(16)\n"
"s_mov_b32 s1, 0x10000\n"
".Lmk_p41l:\n"
"s_add_u32 s96, s57, 0\n"
"buffer_load_dwordx4 v[148:151], v0, s[4:7], s96 offen sc1\n"
"buffer_load_dwordx4 v[108:111], v0, s[4:7], s96 offen offset:1024 sc1\n"
"buffer_load_dwordx4 v[112:115], v0, s[4:7], s96 offen offset:2048 sc1\n"
"buffer_load_dwordx4 v[196:199], v0, s[4:7], s96 offen offset:3072 sc1\n"
"buffer_load_dwordx4 v[200:203], v1, s[4:7], s96 offen sc1\n"
"buffer_load_dwordx4 v[128:131], v1, s[4:7], s96 offen offset:1024 sc1\n"
"buffer_load_dwordx4 v[120:123], v1, s[4:7], s96 offen offset:2048 sc1\n"
"buffer_load_dwordx4 v[132:135], v1, s[4:7], s96 offen offset:3072 sc1\n"
"s_add_u32 s96, s58, 0\n"
"buffer_load_dwordx4 v[152:155], v0, s[4:7], s96 offen sc1\n"
"buffer_load_dwordx4 v[160:163], v0, s[4:7], s96 offen offset:1024 sc1\n"
"buffer_load_dwordx4 v[124:127], v0, s[4:7], s96 offen offset:2048 sc1\n"
"buffer_load_dwordx4 v[144:147], v0, s[4:7], s96 offen offset:3072 sc1\n"
"buffer_load_dwordx4 v[156:159], v1, s[4:7], s96 offen sc1\n"
"buffer_load_dwordx4 v[164:167], v1, s[4:7], s96 offen offset:1024 sc1\n"
"buffer_load_dwordx4 v[116:119], v1, s[4:7], s96 offen offset:2048 sc1\n"
"buffer_load_dwordx4 v[136:139], v1, s[4:7], s96 offen offset:3072 sc1\n"
"s_add_u32 s96, s59, 0\n"
"buffer_load_dwordx4 v[168:171], v0, s[4:7], s96 offen sc1\n"
"buffer_load_dwordx4 v[172:175], v0, s[4:7], s96 offen offset:1024 sc1\n"
"buffer_load_dwordx4 v[176:179], v0, s[4:7], s96 offen offset:2048 sc1\n"
"s_waitcnt vmcnt(0)\n"
"v_max_u32_e32 v253, v148, v149\n"
"v_max3_u32 v253, v253, v150, v151\n"
"v_max3_u32 v253, v253, v108, v109\n"
"v_max3_u32 v253, v253, v110, v111\n"
"v_max3_u32 v253, v253, v112, v113\n"
"v_max3_u32 v253, v253, v114, v115\n"
"v_max3_u32 v253, v253, v196, v197\n"
"v_max3_u32 v253, v253, v198, v199\n"
"v_max3_u32 v253, v253, v200, v201\n"
"v_max3_u32 v253, v253, v202, v203\n"
"v_max3_u32 v253, v253, v128, v129\n"
"v_max3_u32 v253, v253, v130, v131\n"
"v_max3_u32 v253, v253, v120, v121\n"
"v_max3_u32 v253, v253, v122, v123\n"
"v_max3_u32 v253, v253, v132, v133\n"
"v_max3_u32 v253, v253, v134, v135\n"
"v_cmp_eq_u32_e32 vcc, -1, v253\n"
"s_cbranch_vccz .Lmk_p41k\n"
"s_sub_u32 s1, s1, 1\n"
"s_cbranch_scc0 .Lmk_p41l\n"
".Lmk_p41k:\n"
"v_mfma_f32_16x16x32_bf16 v[88:91], a[128:131], v[148:151], 0\n"
"v_mfma_f32_16x16x32_bf16 v[88:91], a[132:135], v[108:111], v[88:91]\n"
"v_mfma_f32_16x16x32_bf16 v[88:91], a[136:139], v[112:115], v[88:91]\n"
"v_mfma_f32_16x16x32_bf16 v[88:91], a[140:143], v[196:199], v[88:91]\n"
"v_mfma_f32_16x16x32_bf16 v[88:91], a[144:147], v[200:203], v[88:91]\n"
"v_mfma_f32_16x16x32_bf16 v[88:91], a[148:151], v[128:131], v[88:91]\n"
"v_mfma_f32_16x16x32_bf16 v[88:91], a[152:155], v[120:123], v[88:91]\n"
"v_mfma_f32_16x16x32_bf16 v[88:91], a[156:159], v[132:135], v[88:91]\n"
"v_mfma_f32_16x16x32_bf16 v[92:95], a[188:191], v[132:135], 0\n"
"v_mfma_f32_16x16x32_bf16 v[92:95], a[184:187], v[120:123], v[92:95]\n"
"v_mfma_f32_16x16x32_bf16 v[92:95], a[180:183], v[128:131], v[92:95]\n"
"v_mfma_f32_16x16x32_bf16 v[92:95], a[176:179], v[200:203], v[92:95]\n"
"v_mfma_f32_16x16x32_bf16 v[92:95], a[172:175], v[196:199], v[92:95]\n"
"v_mfma_f32_16x16x32_bf16 v[92:95], a[168:171], v[112:115], v[92:95]\n"
"s_nop 7\n"
"s_branch .Lmk_p41r\n"
".Lmk_p42s:\n"
";\n"
"s_waitcnt vmcnt(16)\n"
"s_mov_b32 s1, 0x10000\n"
".Lmk_p42l:\n"
"s_add_u32 s96, s58, 0\n"
"buffer_load_dwordx4 v[152:155], v0, s[4:7], s96 offen sc1\n"
"buffer_load_dwordx4 v[160:163], v0, s[4:7], s96 offen offset:1024 sc1\n"
"buffer_load_dwordx4 v[124:127], v0, s[4:7], s96 offen offset:2048 sc1\n"
"buffer_load_dwordx4 v[144:147], v0, s[4:7], s96 offen offset:3072 sc1\n"
"buffer_load_dwordx4 v[156:159], v1, s[4:7], s96 offen sc1\n"
"buffer_load_dwordx4 v[164:167], v1, s[4:7], s96 offen offset:1024 sc1\n"
"buffer_load_dwordx4 v[116:119], v1, s[4:7], s96 offen offset:2048 sc1\n"
"buffer_load_dwordx4 v[136:139], v1, s[4:7], s96 offen offset:3072 sc1\n"
"s_add_u32 s96, s59, 0\n"
"buffer_load_dwordx4 v[168:171], v0, s[4:7], s96 offen sc1\n"
"buffer_load_dwordx4 v[172:175], v0, s[4:7], s96 offen offset:1024 sc1\n"
"buffer_load_dwordx4 v[176:179], v0, s[4:7], s96 offen offset:2048 sc1\n"
"buffer_load_dwordx4 v[180:183], v0, s[4:7], s96 offen offset:3072 sc1\n"
"buffer_load_dwordx4 v[184:187], v1, s[4:7], s96 offen sc1\n"
"buffer_load_dwordx4 v[188:191], v1, s[4:7], s96 offen offset:1024 sc1\n"
"buffer_load_dwordx4 v[192:195], v1, s[4:7], s96 offen offset:2048 sc1\n"
"buffer_load_dwordx4 v[196:199], v1, s[4:7], s96 offen offset:3072 sc1\n"
"s_add_u32 s96, s52, 0x440000\n"
"buffer_load_dwordx4 v[88:91], v0, s[4:7], s96 offen sc1\n"
"buffer_load_dwordx4 v[92:95], v0, s[4:7], s96 offen offset:1024 sc1\n"
"s_waitcnt vmcnt(0)\n"
"v_max_u32_e32 v237, v152, v153\n"
"v_max3_u32 v237, v237, v154, v155\n"
"v_max3_u32 v237, v237, v160, v161\n"
"v_max3_u32 v237, v237, v162, v163\n"
"v_max3_u32 v237, v237, v124, v125\n"
"v_max3_u32 v237, v237, v126, v127\n"
"v_max3_u32 v237, v237, v144, v145\n"
"v_max3_u32 v237, v237, v146, v147\n"
"v_max3_u32 v237, v237, v156, v157\n"
"v_max3_u32 v237, v237, v158, v159\n"
"v_max3_u32 v237, v237, v164, v165\n"
"v_max3_u32 v237, v237, v166, v167\n"
"v_max3_u32 v237, v237, v116, v117\n"
"v_max3_u32 v237, v237, v118, v119\n"
"v_max3_u32 v237, v237, v136, v137\n"
"v_max3_u32 v237, v237, v138, v139\n"
"v_cmp_eq_u32_e32 vcc, -1, v237\n"
"s_cbranch_vccz .Lmk_p42k\n"
"s_sub_u32 s1, s1, 1\n"
"s_cbranch_scc0 .Lmk_p42l\n"
".Lmk_p42k:\n"
"v_mfma_f32_16x16x32_bf16 v[96:99], a[128:131], v[152:155], 0\n"
"v_mfma_f32_16x16x32_bf16 v[96:99], a[132:135], v[160:163], v[96:99]\n"
"v_mfma_f32_16x16x32_bf16 v[96:99], a[136:139], v[124:127], v[96:99]\n"
"v_mfma_f32_16x16x32_bf16 v[96:99], a[140:143], v[144:147], v[96:99]\n"
"v_mfma_f32_16x16x32_bf16 v[96:99], a[144:147], v[156:159], v[96:99]\n"
"v_mfma_f32_16x16x32_bf16 v[96:99], a[148:151], v[164:167], v[96:99]\n"
"v_mfma_f32_16x16x32_bf16 v[96:99], a[152:155], v[116:119], v[96:99]\n"
"v_mfma_f32_16x16x32_bf16 v[96:99], a[156:159], v[136:139], v[96:99]\n"
"v_mfma_f32_16x16x32_bf16 v[104:107], a[188:191], v[136:139], 0\n"
"v_mfma_f32_16x16x32_bf16 v[104:107], a[184:187], v[116:119], v[104:107]\n"
"v_mfma_f32_16x16x32_bf16 v[104:107], a[180:183], v[164:167], v[104:107]\n"
"v_mfma_f32_16x16x32_bf16 v[104:107], a[176:179], v[156:159], v[104:107]\n"
"v_mfma_f32_16x16x32_bf16 v[104:107], a[172:175], v[144:147], v[104:107]\n"
"v_mfma_f32_16x16x32_bf16 v[104:107], a[168:171], v[124:127], v[104:107]\n"
"s_nop 7\n"
"s_branch .Lmk_p42r\n"
".Lmk_f13s:\n"
";\n"
"s_mov_b32 s1, 0x10000\n"
".Lmk_f13l:\n"
"buffer_load_dwordx2 v[254:255], v13, s[4:7], s97 offen sc1\n"
"s_waitcnt vmcnt(0)\n"
"v_cmp_ne_u64_e32 vcc, v[254:255], v[18:19]\n"
"s_cbranch_vccz .Lmk_f13r\n"
"s_sub_u32 s1, s1, 1\n"
"s_cbranch_scc0 .Lmk_f13l\n"
"s_branch .Lmk_f13r\n"
".Lmk_p43s:\n"
";\n"
"s_waitcnt vmcnt(16)\n"
"s_mov_b32 s1, 0x10000\n"
".Lmk_p43l:\n"
"s_add_u32 s97, s59, 0\n"
"buffer_load_dwordx4 v[168:171], v0, s[4:7], s97 offen sc1\n"
"buffer_load_dwordx4 v[172:175], v0, s[4:7], s97 offen offset:1024 sc1\n"
"buffer_load_dwordx4 v[176:179], v0, s[4:7], s97 offen offset:2048 sc1\n"
"buffer_load_dwordx4 v[180:183], v0, s[4:7], s97 offen offset:3072 sc1\n"
"buffer_load_dwordx4 v[184:187], v1, s[4:7], s97 offen sc1\n"
"buffer_load_dwordx4 v[188:191], v1, s[4:7], s97 offen offset:1024 sc1\n"
"buffer_load_dwordx4 v[192:195], v1, s[4:7], s97 offen offset:2048 sc1\n"
"buffer_load_dwordx4 v[196:199], v1, s[4:7], s97 offen offset:3072 sc1\n"
"s_add_u32 s97, s52, 0x440000\n"
"buffer_load_dwordx4 v[88:91], v0, s[4:7], s97 offen sc1\n"
"buffer_load_dwordx4 v[92:95], v0, s[4:7], s97 offen offset:1024 sc1\n"
"buffer_load_dwordx4 v[96:99], v0, s[4:7], s97 offen offset:2048 sc1\n"
"buffer_load_dwordx4 v[100:103], v0, s[4:7], s97 offen offset:3072 sc1\n"
"buffer_load_dwordx4 v[104:107], v1, s[4:7], s97 offen sc1\n"
"buffer_load_dwordx4 v[112:115], v1, s[4:7], s97 offen offset:1024 sc1\n"
"buffer_load_dwordx4 v[116:119], v1, s[4:7], s97 offen offset:2048 sc1\n"
"buffer_load_dwordx4 v[124:127], v1, s[4:7], s97 offen offset:3072 sc1\n"
"s_add_u32 s97, s53, 0x440000\n"
"buffer_load_dwordx4 v[128:131], v0, s[4:7], s97 offen sc1\n"
"buffer_load_dwordx4 v[132:135], v0, s[4:7], s97 offen offset:1024 sc1\n"
"buffer_load_dwordx4 v[136:139], v0, s[4:7], s97 offen offset:2048 sc1\n"
"s_waitcnt vmcnt(0)\n"
"v_max_u32_e32 v255, v168, v169\n"
"v_max3_u32 v255, v255, v170, v171\n"
"v_max3_u32 v255, v255, v172, v173\n"
"v_max3_u32 v255, v255, v174, v175\n"
"v_max3_u32 v255, v255, v176, v177\n"
"v_max3_u32 v255, v255, v178, v179\n"
"v_max3_u32 v255, v255, v180, v181\n"
"v_max3_u32 v255, v255, v182, v183\n"
"v_max3_u32 v255, v255, v184, v185\n"
"v_max3_u32 v255, v255, v186, v187\n"
"v_max3_u32 v255, v255, v188, v189\n"
"v_max3_u32 v255, v255, v190, v191\n"
"v_max3_u32 v255, v255, v192, v193\n"
"v_max3_u32 v255, v255, v194, v195\n"
"v_max3_u32 v255, v255, v196, v197\n"
"v_max3_u32 v255, v255, v198, v199\n"
"v_cmp_eq_u32_e32 vcc, -1, v255\n"
"s_cbranch_vccz .Lmk_p43k\n"
"s_sub_u32 s1, s1, 1\n"
"s_cbranch_scc0 .Lmk_p43l\n"
".Lmk_p43k:\n"
"v_mfma_f32_16x16x32_bf16 v[204:207], a[128:131], v[168:171], v[140:143]\n"
"v_mfma_f32_16x16x32_bf16 v[204:207], a[132:135], v[172:175], v[204:207]\n"
"v_mfma_f32_16x16x32_bf16 v[204:207], a[136:139], v[176:179], v[204:207]\n"
"v_mfma_f32_16x16x32_bf16 v[204:207], a[140:143], v[180:183], v[204:207]\n"
"v_mfma_f32_16x16x32_bf16 v[204:207], a[144:147], v[184:187], v[204:207]\n"
"v_mfma_f32_16x16x32_bf16 v[204:207], a[148:151], v[188:191], v[204:207]\n"
"v_mfma_f32_16x16x32_bf16 v[204:207], a[152:155], v[192:195], v[204:207]\n"
"v_mfma_f32_16x16x32_bf16 v[204:207], a[156:159], v[196:199], v[204:207]\n"
"v_mfma_f32_16x16x32_bf16 v[208:211], a[188:191], v[196:199], v[148:151]\n"
"v_mfma_f32_16x16x32_bf16 v[208:211], a[184:187], v[192:195], v[208:211]\n"
"v_mfma_f32_16x16x32_bf16 v[208:211], a[180:183], v[188:191], v[208:211]\n"
"v_mfma_f32_16x16x32_bf16 v[208:211], a[176:179], v[184:187], v[208:211]\n"
"v_mfma_f32_16x16x32_bf16 v[208:211], a[172:175], v[180:183], v[208:211]\n"
"v_mfma_f32_16x16x32_bf16 v[208:211], a[168:171], v[176:179], v[208:211]\n"
"s_nop 7\n"
"s_branch .Lmk_p43r\n"
".Lmk_p48s:\n"
";\n"
"s_waitcnt vmcnt(16)\n"
"s_mov_b32 s1, 0x10000\n"
".Lmk_p48l:\n"
"s_add_u32 s96, s52, 0x440000\n"
"buffer_load_dwordx4 v[88:91], v0, s[4:7], s96 offen sc1\n"
"buffer_load_dwordx4 v[92:95], v0, s[4:7], s96 offen offset:1024 sc1\n"
"buffer_load_dwordx4 v[96:99], v0, s[4:7], s96 offen offset:2048 sc1\n"
"buffer_load_dwordx4 v[100:103], v0, s[4:7], s96 offen offset:3072 sc1\n"
"buffer_load_dwordx4 v[104:107], v1, s[4:7], s96 offen sc1\n"
"buffer_load_dwordx4 v[112:115], v1, s[4:7], s96 offen offset:1024 sc1\n"
"buffer_load_dwordx4 v[116:119], v1, s[4:7], s96 offen offset:2048 sc1\n"
"buffer_load_dwordx4 v[124:127], v1, s[4:7], s96 offen offset:3072 sc1\n"
"s_add_u32 s96, s53, 0x440000\n"
"buffer_load_dwordx4 v[128:131], v0, s[4:7], s96 offen sc1\n"
"buffer_load_dwordx4 v[132:135], v0, s[4:7], s96 offen offset:1024 sc1\n"
"buffer_load_dwordx4 v[136:139], v0, s[4:7], s96 offen offset:2048 sc1\n"
"buffer_load_dwordx4 v[140:143], v0, s[4:7], s96 offen offset:3072 sc1\n"
"buffer_load_dwordx4 v[144:147], v1, s[4:7], s96 offen sc1\n"
"buffer_load_dwordx4 v[148:151], v1, s[4:7], s96 offen offset:1024 sc1\n"
"buffer_load_dwordx4 v[160:163], v1, s[4:7], s96 offen offset:2048 sc1\n"
"buffer_load_dwordx4 v[164:167], v1, s[4:7], s96 offen offset:3072 sc1\n"
"s_add_u32 s96, s54, 0x440000\n"
"buffer_load_dwordx4 v[176:179], v0, s[4:7], s96 offen sc1\n"
"buffer_load_dwordx4 v[108:111], v0, s[4:7], s96 offen offset:1024 sc1\n"
"s_waitcnt vmcnt(0)\n"
"v_max_u32_e32 v252, v88, v89\n"
"v_max3_u32 v252, v252, v90, v91\n"
"v_max3_u32 v252, v252, v92, v93\n"
"v_max3_u32 v252, v252, v94, v95\n"
"v_max3_u32 v252, v252, v96, v97\n"
"v_max3_u32 v252, v252, v98, v99\n"
"v_max3_u32 v252, v252, v100, v101\n"
"v_max3_u32 v252, v252, v102, v103\n"
"v_max3_u32 v252, v252, v104, v105\n"
"v_max3_u32 v252, v252, v106, v107\n"
"v_max3_u32 v252, v252, v112, v113\n"
"v_max3_u32 v252, v252, v114, v115\n"
"v_max3_u32 v252, v252, v116, v117\n"
"v_max3_u32 v252, v252, v118, v119\n"
"v_max3_u32 v252, v252, v124, v125\n"
"v_max3_u32 v252, v252, v126, v127\n"
"v_cmp_eq_u32_e32 vcc, -1, v252\n"
"s_cbranch_vccz .Lmk_p48k\n"
"s_sub_u32 s1, s1, 1\n"
"s_cbranch_scc0 .Lmk_p48l\n"
".Lmk_p48k:\n"
"v_mfma_f32_16x16x32_bf16 v[172:175], a[0:3], v[88:91], 0\n"
"v_mfma_f32_16x16x32_bf16 v[172:175], a[4:7], v[92:95], v[172:175]\n"
"v_mfma_f32_16x16x32_bf16 v[172:175], a[8:11], v[96:99], v[172:175]\n"
"v_mfma_f32_16x16x32_bf16 v[172:175], a[12:15], v[100:103], v[172:175]\n"
"v_mfma_f32_16x16x32_bf16 v[172:175], a[16:19], v[104:107], v[172:175]\n"
"v_mfma_f32_16x16x32_bf16 v[172:175], a[20:23], v[112:115], v[172:175]\n"
"v_mfma_f32_16x16x32_bf16 v[172:175], a[24:27], v[116:119], v[172:175]\n"
"v_mfma_f32_16x16x32_bf16 v[172:175], a[28:31], v[124:127], v[172:175]\n"
"v_mfma_f32_16x16x32_bf16 v[180:183], a[60:63], v[124:127], 0\n"
"v_mfma_f32_16x16x32_bf16 v[180:183], a[56:59], v[116:119], v[180:183]\n"
"v_mfma_f32_16x16x32_bf16 v[180:183], a[52:55], v[112:115], v[180:183]\n"
"v_mfma_f32_16x16x32_bf16 v[180:183], a[48:51], v[104:107], v[180:183]\n"
"v_mfma_f32_16x16x32_bf16 v[180:183], a[44:47], v[100:103], v[180:183]\n"
"v_mfma_f32_16x16x32_bf16 v[180:183], a[40:43], v[96:99], v[180:183]\n"
"s_nop 7\n"
"s_branch .Lmk_p48r\n"
".Lmk_p49s:\n"
";\n"
"s_waitcnt vmcnt(16)\n"
"s_mov_b32 s1, 0x10000\n"
".Lmk_p49l:\n"
"s_add_u32 s96, s53, 0x440000\n"
"buffer_load_dwordx4 v[128:131], v0, s[4:7], s96 offen sc1\n"
"buffer_load_dwordx4 v[132:135], v0, s[4:7], s96 offen offset:1024 sc1\n"
"buffer_load_dwordx4 v[136:139], v0, s[4:7], s96 offen offset:2048 sc1\n"
"buffer_load_dwordx4 v[140:143], v0, s[4:7], s96 offen offset:3072 sc1\n"
"buffer_load_dwordx4 v[144:147], v1, s[4:7], s96 offen sc1\n"
"buffer_load_dwordx4 v[148:151], v1, s[4:7], s96 offen offset:1024 sc1\n"
"buffer_load_dwordx4 v[160:163], v1, s[4:7], s96 offen offset:2048 sc1\n"
"buffer_load_dwordx4 v[164:167], v1, s[4:7], s96 offen offset:3072 sc1\n"
"s_add_u32 s96, s54, 0x440000\n"
"buffer_load_dwordx4 v[176:179], v0, s[4:7], s96 offen sc1\n"
"buffer_load_dwordx4 v[108:111], v0, s[4:7], s96 offen offset:1024 sc1\n"
"buffer_load_dwordx4 v[152:155], v0, s[4:7], s96 offen offset:2048 sc1\n"
"buffer_load_dwordx4 v[184:187], v0, s[4:7], s96 offen offset:3072 sc1\n"
"buffer_load_dwordx4 v[156:159], v1, s[4:7], s96 offen sc1\n"
"buffer_load_dwordx4 v[192:195], v1, s[4:7], s96 offen offset:1024 sc1\n"
"buffer_load_dwordx4 v[196:199], v1, s[4:7], s96 offen offset:2048 sc1\n"
"buffer_load_dwordx4 v[200:203], v1, s[4:7], s96 offen offset:3072 sc1\n"
"s_add_u32 s96, s55, 0x440000\n"
"buffer_load_dwordx4 v[92:95], v0, s[4:7], s96 offen sc1\n"
"buffer_load_dwordx4 v[96:99], v0, s[4:7], s96 offen offset:1024 sc1\n"
"s_waitcnt vmcnt(0)\n"
"v_max_u32_e32 v247, v128, v129\n"
"v_max3_u32 v247, v247, v130, v131\n"
"v_max3_u32 v247, v247, v132, v133\n"
"v_max3_u32 v247, v247, v134, v135\n"
"v_max3_u32 v247, v247, v136, v137\n"
"v_max3_u32 v247, v247, v138, v139\n"
"v_max3_u32 v247, v247, v140, v141\n"
"v_max3_u32 v247, v247, v142, v143\n"
"v_max3_u32 v247, v247, v144, v145\n"
"v_max3_u32 v247, v247, v146, v147\n"
"v_max3_u32 v247, v247, v148, v149\n"
"v_max3_u32 v247, v247, v150, v151\n"
"v_max3_u32 v247, v247, v160, v161\n"
"v_max3_u32 v247, v247, v162, v163\n"
"v_max3_u32 v247, v247, v164, v165\n"
"v_max3_u32 v247, v247, v166, v167\n"
"v_cmp_eq_u32_e32 vcc, -1, v247\n"
"s_cbranch_vccz .Lmk_p49k\n"
"s_sub_u32 s1, s1, 1\n"
"s_cbranch_scc0 .Lmk_p49l\n"
".Lmk_p49k:\n"
"v_mfma_f32_16x16x32_bf16 v[88:91], a[0:3], v[128:131], 0\n"
"v_mfma_f32_16x16x32_bf16 v[88:91], a[4:7], v[132:135], v[88:91]\n"
"v_mfma_f32_16x16x32_bf16 v[88:91], a[8:11], v[136:139], v[88:91]\n"
"v_mfma_f32_16x16x32_bf16 v[88:91], a[12:15], v[140:143], v[88:91]\n"
"v_mfma_f32_16x16x32_bf16 v[88:91], a[16:19], v[144:147], v[88:91]\n"
"v_mfma_f32_16x16x32_bf16 v[88:91], a[20:23], v[148:151], v[88:91]\n"
"v_mfma_f32_16x16x32_bf16 v[88:91], a[24:27], v[160:163], v[88:91]\n"
"v_mfma_f32_16x16x32_bf16 v[88:91], a[28:31], v[164:167], v[88:91]\n"
"v_mfma_f32_16x16x32_bf16 v[100:103], a[60:63], v[164:167], 0\n"
"v_mfma_f32_16x16x32_bf16 v[100:103], a[56:59], v[160:163], v[100:103]\n"
"v_mfma_f32_16x16x32_bf16 v[100:103], a[52:55], v[148:151], v[100:103]\n"
"v_mfma_f32_16x16x32_bf16 v[100:103], a[48:51], v[144:147], v[100:103]\n"
"v_mfma_f32_16x16x32_bf16 v[100:103], a[44:47], v[140:143], v[100:103]\n"
"v_mfma_f32_16x16x32_bf16 v[100:103], a[40:43], v[136:139], v[100:103]\n"
"s_nop 7\n"
"s_branch .Lmk_p49r\n"
".Lmk_m52s:\n"
";\n"
"s_waitcnt vmcnt(16)\n"
"s_mov_b32 s1, 0x10000\n"
".Lmk_m52l:\n"
"s_add_u32 s98, s62, 0x44000\n"
"buffer_load_dword v255, v10, s[4:7], s98 offen sc1\n"
"buffer_load_dword v254, v10, s[4:7], s98 offen offset:256 sc1\n"
"buffer_load_dword v253, v10, s[4:7], s98 offen offset:512 sc1\n"
"buffer_load_dword v252, v10, s[4:7], s98 offen offset:768 sc1\n"
"buffer_load_dword v251, v10, s[4:7], s98 offen offset:1024 sc1\n"
"buffer_load_dword v250, v10, s[4:7], s98 offen offset:1280 sc1\n"
"buffer_load_dword v249, v10, s[4:7], s98 offen offset:1536 sc1\n"
"buffer_load_dword v248, v10, s[4:7], s98 offen offset:1792 sc1\n"
"s_waitcnt vmcnt(0)\n"
"v_max_u32_e32 v247, v255, v254\n"
"v_max3_u32 v247, v247, v253, v252\n"
"v_max3_u32 v247, v247, v251, v250\n"
"v_max3_u32 v247, v247, v249, v248\n"
"v_cmp_eq_u32_e32 vcc, -1, v247\n"
"s_cbranch_vccz .Lmk_m52k\n"
"s_sub_u32 s1, s1, 1\n"
"s_cbranch_scc0 .Lmk_m52l\n"
".Lmk_m52k:\n"
"v_add_f32_e32 v255, v255, v251\n"
"v_add_f32_e32 v254, v254, v250\n"
"v_add_f32_e32 v253, v253, v249\n"
"v_add_f32_e32 v252, v252, v248\n"
"v_add_f32_e32 v255, v255, v253\n"
"v_add_f32_e32 v254, v254, v252\n"
"v_add_f32_e32 v255, v255, v254\n"
"s_nop 1\n"
"s_branch .Lmk_m52r\n"
".Lmk_p50s:\n"
";\n"
"s_waitcnt vmcnt(16)\n"
"s_mov_b32 s1, 0x10000\n"
".Lmk_p50l:\n"
"s_add_u32 s96, s54, 0x440000\n"
"buffer_load_dwordx4 v[176:179], v0, s[4:7], s96 offen sc1\n"
"buffer_load_dwordx4 v[108:111], v0, s[4:7], s96 offen offset:1024 sc1\n"
"buffer_load_dwordx4 v[152:155], v0, s[4:7], s96 offen offset:2048 sc1\n"
"buffer_load_dwordx4 v[184:187], v0, s[4:7], s96 offen offset:3072 sc1\n"
"buffer_load_dwordx4 v[156:159], v1, s[4:7], s96 offen sc1\n"
"buffer_load_dwordx4 v[192:195], v1, s[4:7], s96 offen offset:1024 sc1\n"
"buffer_load_dwordx4 v[196:199], v1, s[4:7], s96 offen offset:2048 sc1\n"
"buffer_load_dwordx4 v[200:203], v1, s[4:7], s96 offen offset:3072 sc1\n"
"s_add_u32 s96, s55, 0x440000\n"
"buffer_load_dwordx4 v[92:95], v0, s[4:7], s96 offen sc1\n"
"buffer_load_dwordx4 v[96:99], v0, s[4:7], s96 offen offset:1024 sc1\n"
"buffer_load_dwordx4 v[104:107], v0, s[4:7], s96 offen offset:2048 sc1\n"
"buffer_load_dwordx4 v[112:115], v0, s[4:7], s96 offen offset:3072 sc1\n"
"buffer_load_dwordx4 v[100:103], v1, s[4:7], s96 offen sc1\n"
"buffer_load_dwordx4 v[124:127], v1, s[4:7], s96 offen offset:1024 sc1\n"
"buffer_load_dwordx4 v[168:171], v1, s[4:7], s96 offen offset:2048 sc1\n"
"buffer_load_dwordx4 v[172:175], v1, s[4:7], s96 offen offset:3072 sc1\n"
"s_add_u32 s96, s52, 0x448000\n"
"buffer_load_dwordx4 v[128:131], v0, s[4:7], s96 offen sc1\n"
"s_waitcnt vmcnt(0)\n"
"v_max_u32_e32 v255, v176, v177\n"
"v_max3_u32 v255, v255, v178, v179\n"
"v_max3_u32 v255, v255, v108, v109\n"
"v_max3_u32 v255, v255, v110, v111\n"
"v_max3_u32 v255, v255, v152, v153\n"
"v_max3_u32 v255, v255, v154, v155\n"
"v_max3_u32 v255, v255, v184, v185\n"
"v_max3_u32 v255, v255, v186, v187\n"
"v_max3_u32 v255, v255, v156, v157\n"
"v_max3_u32 v255, v255, v158, v159\n"
"v_max3_u32 v255, v255, v192, v193\n"
"v_max3_u32 v255, v255, v194, v195\n"
"v_max3_u32 v255, v255, v196, v197\n"
"v_max3_u32 v255, v255, v198, v199\n"
"v_max3_u32 v255, v255, v200, v201\n"
"v_max3_u32 v255, v255, v202, v203\n"
"v_cmp_eq_u32_e32 vcc, -1, v255\n"
"s_cbranch_vccz .Lmk_p50k\n"
"s_sub_u32 s1, s1, 1\n"
"s_cbranch_scc0 .Lmk_p50l\n"
".Lmk_p50k:\n"
"v_mfma_f32_16x16x32_bf16 v[88:91], a[0:3], v[176:179], 0\n"
"v_mfma_f32_16x16x32_bf16 v[88:91], a[4:7], v[108:111], v[88:91]\n"
"v_mfma_f32_16x16x32_bf16 v[88:91], a[8:11], v[152:155], v[88:91]\n"
"v_mfma_f32_16x16x32_bf16 v[88:91], a[12:15], v[184:187], v[88:91]\n"
"v_mfma_f32_16x16x32_bf16 v[88:91], a[16:19], v[156:159], v[88:91]\n"
"v_mfma_f32_16x16x32_bf16 v[88:91], a[20:23], v[192:195], v[88:91]\n"
"v_mfma_f32_16x16x32_bf16 v[88:91], a[24:27], v[196:199], v[88:91]\n"
"v_mfma_f32_16x16x32_bf16 v[88:91], a[28:31], v[200:203], v[88:91]\n"
"v_mfma_f32_16x16x32_bf16 v[116:119], a[60:63], v[200:203], 0\n"
"v_mfma_f32_16x16x32_bf16 v[116:119], a[56:59], v[196:199], v[116:119]\n"
"v_mfma_f32_16x16x32_bf16 v[116:119], a[52:55], v[192:195], v[116:119]\n"
"v_mfma_f32_16x16x32_bf16 v[116:119], a[48:51], v[156:159], v[116:119]\n"
"v_mfma_f32_16x16x32_bf16 v[116:119], a[44:47], v[184:187], v[116:119]\n"
"v_mfma_f32_16x16x32_bf16 v[116:119], a[40:43], v[152:155], v[116:119]\n"
"s_nop 7\n"
"s_branch .Lmk_p50r\n"
".Lmk_p51s:\n"
";\n"
"s_waitcnt vmcnt(16)\n"
"s_mov_b32 s1, 0x10000\n"
".Lmk_p51l:\n"
"s_add_u32 s96, s55, 0x440000\n"
"buffer_load_dwordx4 v[92:95], v0, s[4:7], s96 offen sc1\n"
"buffer_load_dwordx4 v[96:99], v0, s[4:7], s96 offen offset:1024 sc1\n"
"buffer_load_dwordx4 v[104:107], v0, s[4:7], s96 offen offset:2048 sc1\n"
"buffer_load_dwordx4 v[112:115], v0, s[4:7], s96 offen offset:3072 sc1\n"
"buffer_load_dwordx4 v[100:103], v1, s[4:7], s96 offen sc1\n"
"buffer_load_dwordx4 v[124:127], v1, s[4:7], s96 offen offset:1024 sc1\n"
"buffer_load_dwordx4 v[168:171], v1, s[4:7], s96 offen offset:2048 sc1\n"
"buffer_load_dwordx4 v[172:175], v1, s[4:7], s96 offen offset:3072 sc1\n"
"s_add_u32 s96, s52, 0x448000\n"
"buffer_load_dwordx4 v[128:131], v0, s[4:7], s96 offen sc1\n"
"buffer_load_dwordx4 v[88:91], v0, s[4:7], s96 offen offset:1024 sc1\n"
"buffer_load_dwordx4 v[164:167], v0, s[4:7], s96 offen offset:2048 sc1\n"
"buffer_load_dwordx4 v[140:143], v0, s[4:7], s96 offen offset:3072 sc1\n"
"buffer_load_dwordx4 v[132:135], v1, s[4:7], s96 offen sc1\n"
"buffer_load_dwordx4 v[224:227], v1, s[4:7], s96 offen offset:1024 sc1\n"
"buffer_load_dwordx4 v[180:183], v1, s[4:7], s96 offen offset:2048 sc1\n"
"buffer_load_dwordx4 v[152:155], v1, s[4:7], s96 offen offset:3072 sc1\n"
"s_add_u32 s96, s53, 0x448000\n"
"buffer_load_dwordx4 v[116:119], v0, s[4:7], s96 offen sc1\n"
"buffer_load_dwordx4 v[160:163], v0, s[4:7], s96 offen offset:1024 sc1\n"
"s_waitcnt vmcnt(0)\n"
"v_max_u32_e32 v252, v92, v93\n"
"v_max3_u32 v252, v252, v94, v95\n"
"v_max3_u32 v252, v252, v96, v97\n"
"v_max3_u32 v252, v252, v98, v99\n"
"v_max3_u32 v252, v252, v104, v105\n"
"v_max3_u32 v252, v252, v106, v107\n"
"v_max3_u32 v252, v252, v112, v113\n"
"v_max3_u32 v252, v252, v114, v115\n"
"v_max3_u32 v252, v252, v100, v101\n"
"v_max3_u32 v252, v252, v102, v103\n"
"v_max3_u32 v252, v252, v124, v125\n"
"v_max3_u32 v252, v252, v126, v127\n"
"v_max3_u32 v252, v252, v168, v169\n"
"v_max3_u32 v252, v252, v170, v171\n"
"v_max3_u32 v252, v252, v172, v173\n"
"v_max3_u32 v252, v252, v174, v175\n"
"v_cmp_eq_u32_e32 vcc, -1, v252\n"
"s_cbranch_vccz .Lmk_p51k\n"
"s_sub_u32 s1, s1, 1\n"
"s_cbranch_scc0 .Lmk_p51l\n"
".Lmk_p51k:\n"
"v_mfma_f32_16x16x32_bf16 v[196:199], a[0:3], v[92:95], v[136:139]\n"
"v_mfma_f32_16x16x32_bf16 v[196:199], a[4:7], v[96:99], v[196:199]\n"
"v_mfma_f32_16x16x32_bf16 v[196:199], a[8:11], v[104:107], v[196:199]\n"
"v_mfma_f32_16x16x32_bf16 v[196:199], a[12:15], v[112:115], v[196:199]\n"
"v_mfma_f32_16x16x32_bf16 v[196:199], a[16:19], v[100:103], v[196:199]\n"
"v_mfma_f32_16x16x32_bf16 v[196:199], a[20:23], v[124:127], v[196:199]\n"
"v_mfma_f32_16x16x32_bf16 v[196:199], a[24:27], v[168:171], v[196:199]\n"
"v_mfma_f32_16x16x32_bf16 v[196:199], a[28:31], v[172:175], v[196:199]\n"
"v_mfma_f32_16x16x32_bf16 v[204:207], a[60:63], v[172:175], v[148:151]\n"
"v_mfma_f32_16x16x32_bf16 v[204:207], a[56:59], v[168:171], v[204:207]\n"
"v_mfma_f32_16x16x32_bf16 v[204:207], a[52:55], v[124:127], v[204:207]\n"
"v_mfma_f32_16x16x32_bf16 v[204:207], a[48:51], v[100:103], v[204:207]\n"
"v_mfma_f32_16x16x32_bf16 v[204:207], a[44:47], v[112:115], v[204:207]\n"
"v_mfma_f32_16x16x32_bf16 v[204:207], a[40:43], v[104:107], v[204:207]\n"
"s_nop 7\n"
"s_branch .Lmk_p51r\n"
".Lmk_p53s:\n"
";\n"
"s_waitcnt vmcnt(16)\n"
"s_mov_b32 s1, 0x10000\n"
".Lmk_p53l:\n"
"s_add_u32 s97, s52, 0x448000\n"
"buffer_load_dwordx4 v[128:131], v0, s[4:7], s97 offen sc1\n"
"buffer_load_dwordx4 v[88:91], v0, s[4:7], s97 offen offset:1024 sc1\n"
"buffer_load_dwordx4 v[164:167], v0, s[4:7], s97 offen offset:2048 sc1\n"
"buffer_load_dwordx4 v[140:143], v0, s[4:7], s97 offen offset:3072 sc1\n"
"buffer_load_dwordx4 v[132:135], v1, s[4:7], s97 offen sc1\n"
"buffer_load_dwordx4 v[224:227], v1, s[4:7], s97 offen offset:1024 sc1\n"
"buffer_load_dwordx4 v[180:183], v1, s[4:7], s97 offen offset:2048 sc1\n"
"buffer_load_dwordx4 v[152:155], v1, s[4:7], s97 offen offset:3072 sc1\n"
"s_add_u32 s97, s53, 0x448000\n"
"buffer_load_dwordx4 v[116:119], v0, s[4:7], s97 offen sc1\n"
"buffer_load_dwordx4 v[160:163], v0, s[4:7], s97 offen offset:1024 sc1\n"
"buffer_load_dwordx4 v[136:139], v0, s[4:7], s97 offen offset:2048 sc1\n"
"buffer_load_dwordx4 v[148:151], v0, s[4:7], s97 offen offset:3072 sc1\n"
"buffer_load_dwordx4 v[192:195], v1, s[4:7], s97 offen sc1\n"
"buffer_load_dwordx4 v[184:187], v1, s[4:7], s97 offen offset:1024 sc1\n"
"buffer_load_dwordx4 v[176:179], v1, s[4:7], s97 offen offset:2048 sc1\n"
"buffer_load_dwordx4 v[200:203], v1, s[4:7], s97 offen offset:3072 sc1\n"
"s_add_u32 s97, s54, 0x448000\n"
"buffer_load_dwordx4 v[96:99], v0, s[4:7], s97 offen sc1\n"
"buffer_load_dwordx4 v[100:103], v0, s[4:7], s97 offen offset:1024 sc1\n"
"s_waitcnt vmcnt(0)\n"
"v_max_u32_e32 v252, v128, v129\n"
"v_max3_u32 v252, v252, v130, v131\n"
"v_max3_u32 v252, v252, v88, v89\n"
"v_max3_u32 v252, v252, v90, v91\n"
"v_max3_u32 v252, v252, v164, v165\n"
"v_max3_u32 v252, v252, v166, v167\n"
"v_max3_u32 v252, v252, v140, v141\n"
"v_max3_u32 v252, v252, v142, v143\n"
"v_max3_u32 v252, v252, v132, v133\n"
"v_max3_u32 v252, v252, v134, v135\n"
"v_max3_u32 v252, v252, v224, v225\n"
"v_max3_u32 v252, v252, v226, v227\n"
"v_max3_u32 v252, v252, v180, v181\n"
"v_max3_u32 v252, v252, v182, v183\n"
"v_max3_u32 v252, v252, v152, v153\n"
"v_max3_u32 v252, v252, v154, v155\n"
"v_cmp_eq_u32_e32 vcc, -1, v252\n"
"s_cbranch_vccz .Lmk_p53k\n"
"s_sub_u32 s1, s1, 1\n"
"s_cbranch_scc0 .Lmk_p53l\n"
".Lmk_p53k:\n"
"v_mfma_f32_16x16x32_bf16 v[92:95], a[0:3], v[128:131], 0\n"
"v_mfma_f32_16x16x32_bf16 v[92:95], a[4:7], v[88:91], v[92:95]\n"
"v_mfma_f32_16x16x32_bf16 v[92:95], a[8:11], v[164:167], v[92:95]\n"
"v_mfma_f32_16x16x32_bf16 v[92:95], a[12:15], v[140:143], v[92:95]\n"
"v_mfma_f32_16x16x32_bf16 v[92:95], a[16:19], v[132:135], v[92:95]\n"
"v_mfma_f32_16x16x32_bf16 v[92:95], a[20:23], v[224:227], v[92:95]\n"
"v_mfma_f32_16x16x32_bf16 v[92:95], a[24:27], v[180:183], v[92:95]\n"
"v_mfma_f32_16x16x32_bf16 v[92:95], a[28:31], v[152:155], v[92:95]\n"
"v_mfma_f32_16x16x32_bf16 v[104:107], a[60:63], v[152:155], 0\n"
"v_mfma_f32_16x16x32_bf16 v[104:107], a[56:59], v[180:183], v[104:107]\n"
"v_mfma_f32_16x16x32_bf16 v[104:107], a[52:55], v[224:227], v[104:107]\n"
"v_mfma_f32_16x16x32_bf16 v[104:107], a[48:51], v[132:135], v[104:107]\n"
"v_mfma_f32_16x16x32_bf16 v[104:107], a[44:47], v[140:143], v[104:107]\n"
"v_mfma_f32_16x16x32_bf16 v[104:107], a[40:43], v[164:167], v[104:107]\n"
"s_nop 7\n"
"s_branch .Lmk_p53r\n"
".Lmk_p54s:\n"
";\n"
"s_waitcnt vmcnt(16)\n"
"s_mov_b32 s1, 0x10000\n"
".Lmk_p54l:\n"
"s_add_u32 s95, s53, 0x448000\n"
"buffer_load_dwordx4 v[116:119], v0, s[4:7], s95 offen sc1\n"
"buffer_load_dwordx4 v[160:163], v0, s[4:7], s95 offen offset:1024 sc1\n"
"buffer_load_dwordx4 v[136:139], v0, s[4:7], s95 offen offset:2048 sc1\n"
"buffer_load_dwordx4 v[148:151], v0, s[4:7], s95 offen offset:3072 sc1\n"
"buffer_load_dwordx4 v[192:195], v1, s[4:7], s95 offen sc1\n"
"buffer_load_dwordx4 v[184:187], v1, s[4:7], s95 offen offset:1024 sc1\n"
"buffer_load_dwordx4 v[176:179], v1, s[4:7], s95 offen offset:2048 sc1\n"
"buffer_load_dwordx4 v[200:203], v1, s[4:7], s95 offen offset:3072 sc1\n"
"s_add_u32 s95, s54, 0x448000\n"
"buffer_load_dwordx4 v[96:99], v0, s[4:7], s95 offen sc1\n"
"buffer_load_dwordx4 v[100:103], v0, s[4:7], s95 offen offset:1024 sc1\n"
"buffer_load_dwordx4 v[112:115], v0, s[4:7], s95 offen offset:2048 sc1\n"
"buffer_load_dwordx4 v[120:123], v0, s[4:7], s95 offen offset:3072 sc1\n"
"buffer_load_dwordx4 v[104:107], v1, s[4:7], s95 offen sc1\n"
"buffer_load_dwordx4 v[156:159], v1, s[4:7], s95 offen offset:1024 sc1\n"
"buffer_load_dwordx4 v[108:111], v1, s[4:7], s95 offen offset:2048 sc1\n"
"buffer_load_dwordx4 v[144:147], v1, s[4:7], s95 offen offset:3072 sc1\n"
"s_add_u32 s95, s55, 0x448000\n"
"buffer_load_dwordx4 v[92:95], v0, s[4:7], s95 offen sc1\n"
"buffer_load_dwordx4 v[124:127], v0, s[4:7], s95 offen offset:1024 sc1\n"
"s_waitcnt vmcnt(0)\n"
"v_max_u32_e32 v247, v116, v117\n"
"v_max3_u32 v247, v247, v118, v119\n"
"v_max3_u32 v247, v247, v160, v161\n"
"v_max3_u32 v247, v247, v162, v163\n"
"v_max3_u32 v247, v247, v136, v137\n"
"v_max3_u32 v247, v247, v138, v139\n"
"v_max3_u32 v247, v247, v148, v149\n"
"v_max3_u32 v247, v247, v150, v151\n"
"v_max3_u32 v247, v247, v192, v193\n"
"v_max3_u32 v247, v247, v194, v195\n"
"v_max3_u32 v247, v247, v184, v185\n"
"v_max3_u32 v247, v247, v186, v187\n"
"v_max3_u32 v247, v247, v176, v177\n"
"v_max3_u32 v247, v247, v178, v179\n"
"v_max3_u32 v247, v247, v200, v201\n"
"v_max3_u32 v247, v247, v202, v203\n"
"v_cmp_eq_u32_e32 vcc, -1, v247\n"
"s_cbranch_vccz .Lmk_p54k\n"
"s_sub_u32 s1, s1, 1\n"
"s_cbranch_scc0 .Lmk_p54l\n"
".Lmk_p54k:\n"
"v_mfma_f32_16x16x32_bf16 v[88:91], a[0:3], v[116:119], 0\n"
"v_mfma_f32_16x16x32_bf16 v[88:91], a[4:7], v[160:163], v[88:91]\n"
"v_mfma_f32_16x16x32_bf16 v[88:91], a[8:11], v[136:139], v[88:91]\n"
"v_mfma_f32_16x16x32_bf16 v[88:91], a[12:15], v[148:151], v[88:91]\n"
"v_mfma_f32_16x16x32_bf16 v[88:91], a[16:19], v[192:195], v[88:91]\n"
"v_mfma_f32_16x16x32_bf16 v[88:91], a[20:23], v[184:187], v[88:91]\n"
"v_mfma_f32_16x16x32_bf16 v[88:91], a[24:27], v[176:179], v[88:91]\n"
"v_mfma_f32_16x16x32_bf16 v[88:91], a[28:31], v[200:203], v[88:91]\n"
"v_mfma_f32_16x16x32_bf16 v[128:131], a[60:63], v[200:203], 0\n"
"v_mfma_f32_16x16x32_bf16 v[128:131], a[56:59], v[176:179], v[128:131]\n"
"v_mfma_f32_16x16x32_bf16 v[128:131], a[52:55], v[184:187], v[128:131]\n"
"v_mfma_f32_16x16x32_bf16 v[128:131], a[48:51], v[192:195], v[128:131]\n"
"v_mfma_f32_16x16x32_bf16 v[128:131], a[44:47], v[148:151], v[128:131]\n"
"v_mfma_f32_16x16x32_bf16 v[128:131], a[40:43], v[136:139], v[128:131]\n"
"s_nop 7\n"
"s_branch .Lmk_p54r\n"
".Lmk_m57s:\n"
";\n"
"s_waitcnt vmcnt(16)\n"
"s_mov_b32 s1, 0x10000\n"
".Lmk_m57l:\n"
"s_add_u32 s98, s62, 0x44800\n"
"buffer_load_dword v255, v10, s[4:7], s98 offen sc1\n"
"buffer_load_dword v253, v10, s[4:7], s98 offen offset:256 sc1\n"
"buffer_load_dword v252, v10, s[4:7], s98 offen offset:512 sc1\n"
"buffer_load_dword v251, v10, s[4:7], s98 offen offset:768 sc1\n"
"buffer_load_dword v250, v10, s[4:7], s98 offen offset:1024 sc1\n"
"buffer_load_dword v254, v10, s[4:7], s98 offen offset:1280 sc1\n"
"buffer_load_dword v249, v10, s[4:7], s98 offen offset:1536 sc1\n"
"buffer_load_dword v248, v10, s[4:7], s98 offen offset:1792 sc1\n"
"s_waitcnt vmcnt(0)\n"
"v_max_u32_e32 v245, v255, v253\n"
"v_max3_u32 v245, v245, v252, v251\n"
"v_max3_u32 v245, v245, v250, v254\n"
"v_max3_u32 v245, v245, v249, v248\n"
"v_cmp_eq_u32_e32 vcc, -1, v245\n"
"s_cbranch_vccz .Lmk_m57k\n"
"s_sub_u32 s1, s1, 1\n"
"s_cbranch_scc0 .Lmk_m57l\n"
".Lmk_m57k:\n"
"v_add_f32_e32 v255, v255, v250\n"
"v_add_f32_e32 v253, v253, v254\n"
"v_add_f32_e32 v252, v252, v249\n"
"v_add_f32_e32 v251, v251, v248\n"
"v_add_f32_e32 v255, v255, v252\n"
"v_add_f32_e32 v253, v253, v251\n"
"v_add_f32_e32 v255, v255, v253\n"
"s_nop 1\n"
"s_branch .Lmk_m57r\n"
".Lmk_f44s:\n"
";\n"
"s_mov_b32 s1, 0x10000\n"
".Lmk_f44l:\n"
"buffer_load_dwordx2 v[246:247], v12, s[4:7], s96 offen sc1\n"
"s_waitcnt vmcnt(0)\n"
"v_cmp_ne_u64_e32 vcc, v[246:247], v[18:19]\n"
"s_cbranch_vccz .Lmk_f44r\n"
"s_sub_u32 s1, s1, 1\n"
"s_cbranch_scc0 .Lmk_f44l\n"
"s_branch .Lmk_f44r\n"
".Lmk_p55s:\n"
";\n"
"s_waitcnt vmcnt(16)\n"
"s_mov_b32 s1, 0x10000\n"
".Lmk_p55l:\n"
"s_add_u32 s96, s54, 0x448000\n"
"buffer_load_dwordx4 v[96:99], v0, s[4:7], s96 offen sc1\n"
"buffer_load_dwordx4 v[100:103], v0, s[4:7], s96 offen offset:1024 sc1\n"
"buffer_load_dwordx4 v[112:115], v0, s[4:7], s96 offen offset:2048 sc1\n"
"buffer_load_dwordx4 v[120:123], v0, s[4:7], s96 offen offset:3072 sc1\n"
"buffer_load_dwordx4 v[104:107], v1, s[4:7], s96 offen sc1\n"
"buffer_load_dwordx4 v[156:159], v1, s[4:7], s96 offen offset:1024 sc1\n"
"buffer_load_dwordx4 v[108:111], v1, s[4:7], s96 offen offset:2048 sc1\n"
"buffer_load_dwordx4 v[144:147], v1, s[4:7], s96 offen offset:3072 sc1\n"
"s_add_u32 s96, s55, 0x448000\n"
"buffer_load_dwordx4 v[92:95], v0, s[4:7], s96 offen sc1\n"
"buffer_load_dwordx4 v[124:127], v0, s[4:7], s96 offen offset:1024 sc1\n"
"buffer_load_dwordx4 v[132:135], v0, s[4:7], s96 offen offset:2048 sc1\n"
"buffer_load_dwordx4 v[140:143], v0, s[4:7], s96 offen offset:3072 sc1\n"
"buffer_load_dwordx4 v[128:131], v1, s[4:7], s96 offen sc1\n"
"buffer_load_dwordx4 v[168:171], v1, s[4:7], s96 offen offset:1024 sc1\n"
"buffer_load_dwordx4 v[172:175], v1, s[4:7], s96 offen offset:2048 sc1\n"
"buffer_load_dwordx4 v[180:183], v1, s[4:7], s96 offen offset:3072 sc1\n"
"s_waitcnt vmcnt(0)\n"
"v_max_u32_e32 v252, v96, v97\n"
"v_max3_u32 v252, v252, v98, v99\n"
"v_max3_u32 v252, v252, v100, v101\n"
"v_max3_u32 v252, v252, v102, v103\n"
"v_max3_u32 v252, v252, v112, v113\n"
"v_max3_u32 v252, v252, v114, v115\n"
"v_max3_u32 v252, v252, v120, v121\n"
"v_max3_u32 v252, v252, v122, v123\n"
"v_max3_u32 v252, v252, v104, v105\n"
"v_max3_u32 v252, v252, v106, v107\n"
"v_max3_u32 v252, v252, v156, v157\n"
"v_max3_u32 v252, v252, v158, v159\n"
"v_max3_u32 v252, v252, v108, v109\n"
"v_max3_u32 v252, v252, v110, v111\n"
"v_max3_u32 v252, v252, v144, v145\n"
"v_max3_u32 v252, v252, v146, v147\n"
"v_cmp_eq_u32_e32 vcc, -1, v252\n"
"s_cbranch_vccz .Lmk_p55k\n"
"s_sub_u32 s1, s1, 1\n"
"s_cbranch_scc0 .Lmk_p55l\n"
".Lmk_p55k:\n"
"v_mfma_f32_16x16x32_bf16 v[88:91], a[0:3], v[96:99], 0\n"
"v_mfma_f32_16x16x32_bf16 v[88:91], a[4:7], v[100:103], v[88:91]\n"
"v_mfma_f32_16x16x32_bf16 v[88:91], a[8:11], v[112:115], v[88:91]\n"
"v_mfma_f32_16x16x32_bf16 v[88:91], a[12:15], v[120:123], v[88:91]\n"
"v_mfma_f32_16x16x32_bf16 v[88:91], a[16:19], v[104:107], v[88:91]\n"
"v_mfma_f32_16x16x32_bf16 v[88:91], a[20:23], v[156:159], v[88:91]\n"
"v_mfma_f32_16x16x32_bf16 v[88:91], a[24:27], v[108:111], v[88:91]\n"
"v_mfma_f32_16x16x32_bf16 v[88:91], a[28:31], v[144:147], v[88:91]\n"
"v_mfma_f32_16x16x32_bf16 v[116:119], a[60:63], v[144:147], 0\n"
"v_mfma_f32_16x16x32_bf16 v[116:119], a[56:59], v[108:111], v[116:119]\n"
"v_mfma_f32_16x16x32_bf16 v[116:119], a[52:55], v[156:159], v[116:119]\n"
"v_mfma_f32_16x16x32_bf16 v[116:119], a[48:51], v[104:107], v[116:119]\n"
"v_mfma_f32_16x16x32_bf16 v[116:119], a[44:47], v[120:123], v[116:119]\n"
"v_mfma_f32_16x16x32_bf16 v[116:119], a[40:43], v[112:115], v[116:119]\n"
"s_nop 7\n"
"s_branch .Lmk_p55r\n"
".Lmk_p56s:\n"
";\n"
"s_waitcnt vmcnt(16)\n"
"s_mov_b32 s1, 0x10000\n"
".Lmk_p56l:\n"
"s_add_u32 s96, s55, 0x448000\n"
"buffer_load_dwordx4 v[92:95], v0, s[4:7], s96 offen sc1\n"
"buffer_load_dwordx4 v[124:127], v0, s[4:7], s96 offen offset:1024 sc1\n"
"buffer_load_dwordx4 v[132:135], v0, s[4:7], s96 offen offset:2048 sc1\n"
"buffer_load_dwordx4 v[140:143], v0, s[4:7], s96 offen offset:3072 sc1\n"
"buffer_load_dwordx4 v[128:131], v1, s[4:7], s96 offen sc1\n"
"buffer_load_dwordx4 v[168:171], v1, s[4:7], s96 offen offset:1024 sc1\n"
"buffer_load_dwordx4 v[172:175], v1, s[4:7], s96 offen offset:2048 sc1\n"
"buffer_load_dwordx4 v[180:183], v1, s[4:7], s96 offen offset:3072 sc1\n"
"s_waitcnt vmcnt(0)\n"
"v_max_u32_e32 v252, v92, v93\n"
"v_max3_u32 v252, v252, v94, v95\n"
"v_max3_u32 v252, v252, v124, v125\n"
"v_max3_u32 v252, v252, v126, v127\n"
"v_max3_u32 v252, v252, v132, v133\n"
"v_max3_u32 v252, v252, v134, v135\n"
"v_max3_u32 v252, v252, v140, v141\n"
"v_max3_u32 v252, v252, v142, v143\n"
"v_max3_u32 v252, v252, v128, v129\n"
"v_max3_u32 v252, v252, v130, v131\n"
"v_max3_u32 v252, v252, v168, v169\n"
"v_max3_u32 v252, v252, v170, v171\n"
"v_max3_u32 v252, v252, v172, v173\n"
"v_max3_u32 v252, v252, v174, v175\n"
"v_max3_u32 v252, v252, v180, v181\n"
"v_max3_u32 v252, v252, v182, v183\n"
"v_cmp_eq_u32_e32 vcc, -1, v252\n"
"s_cbranch_vccz .Lmk_p56k\n"
"s_sub_u32 s1, s1, 1\n"
"s_cbranch_scc0 .Lmk_p56l\n"
".Lmk_p56k:\n"
"v_mfma_f32_16x16x32_bf16 v[112:115], a[0:3], v[92:95], v[148:151]\n"
"v_mfma_f32_16x16x32_bf16 v[112:115], a[4:7], v[124:127], v[112:115]\n"
"v_mfma_f32_16x16x32_bf16 v[112:115], a[8:11], v[132:135], v[112:115]\n"
"v_mfma_f32_16x16x32_bf16 v[112:115], a[12:15], v[140:143], v[112:115]\n"
"v_mfma_f32_16x16x32_bf16 v[112:115], a[16:19], v[128:131], v[112:115]\n"
"v_mfma_f32_16x16x32_bf16 v[112:115], a[20:23], v[168:171], v[112:115]\n"
"v_mfma_f32_16x16x32_bf16 v[112:115], a[24:27], v[172:175], v[112:115]\n"
"v_mfma_f32_16x16x32_bf16 v[112:115], a[28:31], v[180:183], v[112:115]\n"
"v_mfma_f32_16x16x32_bf16 v[188:191], a[60:63], v[180:183], v[184:187]\n"
"v_mfma_f32_16x16x32_bf16 v[188:191], a[56:59], v[172:175], v[188:191]\n"
"v_mfma_f32_16x16x32_bf16 v[188:191], a[52:55], v[168:171], v[188:191]\n"
"v_mfma_f32_16x16x32_bf16 v[188:191], a[48:51], v[128:131], v[188:191]\n"
"v_mfma_f32_16x16x32_bf16 v[188:191], a[44:47], v[140:143], v[188:191]\n"
"v_mfma_f32_16x16x32_bf16 v[188:191], a[40:43], v[132:135], v[188:191]\n"
"s_nop 7\n"
"s_branch .Lmk_p56r\n"
".Lmk_f45s:\n"
";\n"
"s_mov_b32 s1, 0x10000\n"
".Lmk_f45l:\n"
"buffer_load_dwordx2 v[246:247], off, s[4:7], s96 sc1\n"
"s_waitcnt vmcnt(0)\n"
"v_cmp_ne_u64_e32 vcc, v[246:247], v[18:19]\n"
"s_cbranch_vccz .Lmk_f45r\n"
"s_sub_u32 s1, s1, 1\n"
"s_cbranch_scc0 .Lmk_f45l\n"
"s_branch .Lmk_f45r\n"
".Lmk_f46s:\n"
";\n"
"s_mov_b32 s1, 0x10000\n"
".Lmk_f46l:\n"
"buffer_load_dwordx2 v[250:251], v12, s[4:7], s98 offen sc1\n"
"s_waitcnt vmcnt(0)\n"
"v_cmp_ne_u64_e32 vcc, v[250:251], v[18:19]\n"
"s_cbranch_vccz .Lmk_f46r\n"
"s_sub_u32 s1, s1, 1\n"
"s_cbranch_scc0 .Lmk_f46l\n"
"s_branch .Lmk_f46r\n"
".Lmk_p60s:\n"
";\n"
"s_waitcnt vmcnt(16)\n"
"s_mov_b32 s1, 0x10000\n"
".Lmk_p60l:\n"
"s_add_u32 s96, s56, 0x440000\n"
"buffer_load_dwordx4 v[120:123], v0, s[4:7], s96 offen sc1\n"
"buffer_load_dwordx4 v[136:139], v0, s[4:7], s96 offen offset:1024 sc1\n"
"buffer_load_dwordx4 v[144:147], v0, s[4:7], s96 offen offset:2048 sc1\n"
"buffer_load_dwordx4 v[192:195], v0, s[4:7], s96 offen offset:3072 sc1\n"
"buffer_load_dwordx4 v[196:199], v1, s[4:7], s96 offen sc1\n"
"buffer_load_dwordx4 v[188:191], v1, s[4:7], s96 offen offset:1024 sc1\n"
"buffer_load_dwordx4 v[108:111], v1, s[4:7], s96 offen offset:2048 sc1\n"
"buffer_load_dwordx4 v[92:95], v1, s[4:7], s96 offen offset:3072 sc1\n"
"s_add_u32 s96, s57, 0x440000\n"
"buffer_load_dwordx4 v[152:155], v0, s[4:7], s96 offen sc1\n"
"buffer_load_dwordx4 v[180:183], v0, s[4:7], s96 offen offset:1024 sc1\n"
"buffer_load_dwordx4 v[24:27], v0, s[4:7], s96 offen offset:2048 sc1\n"
"buffer_load_dwordx4 v[32:35], v0, s[4:7], s96 offen offset:3072 sc1\n"
"buffer_load_dwordx4 v[44:47], v1, s[4:7], s96 offen sc1\n"
"buffer_load_dwordx4 v[48:51], v1, s[4:7], s96 offen offset:1024 sc1\n"
"buffer_load_dwordx4 v[28:31], v1, s[4:7], s96 offen offset:2048 sc1\n"
"buffer_load_dwordx4 v[56:59], v1, s[4:7], s96 offen offset:3072 sc1\n"
"s_add_u32 s96, s58, 0x440000\n"
"buffer_load_dwordx4 v[40:43], v0, s[4:7], s96 offen sc1\n"
"buffer_load_dwordx4 v[52:55], v0, s[4:7], s96 offen offset:1024 sc1\n"
"buffer_load_dwordx4 v[64:67], v0, s[4:7], s96 offen offset:2048 sc1\n"
"s_waitcnt vmcnt(0)\n"
"v_max_u32_e32 v255, v120, v121\n"
"v_max3_u32 v255, v255, v122, v123\n"
"v_max3_u32 v255, v255, v136, v137\n"
"v_max3_u32 v255, v255, v138, v139\n"
"v_max3_u32 v255, v255, v144, v145\n"
"v_max3_u32 v255, v255, v146, v147\n"
"v_max3_u32 v255, v255, v192, v193\n"
"v_max3_u32 v255, v255, v194, v195\n"
"v_max3_u32 v255, v255, v196, v197\n"
"v_max3_u32 v255, v255, v198, v199\n"
"v_max3_u32 v255, v255, v188, v189\n"
"v_max3_u32 v255, v255, v190, v191\n"
"v_max3_u32 v255, v255, v108, v109\n"
"v_max3_u32 v255, v255, v110, v111\n"
"v_max3_u32 v255, v255, v92, v93\n"
"v_max3_u32 v255, v255, v94, v95\n"
"v_cmp_eq_u32_e32 vcc, -1, v255\n"
"s_cbranch_vccz .Lmk_p60k\n"
"s_sub_u32 s1, s1, 1\n"
"s_cbranch_scc0 .Lmk_p60l\n"
".Lmk_p60k:\n"
"v_mfma_f32_16x16x32_bf16 v[36:39], a[128:131], v[120:123], 0\n"
"v_mfma_f32_16x16x32_bf16 v[36:39], a[132:135], v[136:139], v[36:39]\n"
"v_mfma_f32_16x16x32_bf16 v[36:39], a[136:139], v[144:147], v[36:39]\n"
"v_mfma_f32_16x16x32_bf16 v[36:39], a[140:143], v[192:195], v[36:39]\n"
"v_mfma_f32_16x16x32_bf16 v[36:39], a[144:147], v[196:199], v[36:39]\n"
"v_mfma_f32_16x16x32_bf16 v[36:39], a[148:151], v[188:191], v[36:39]\n"
"v_mfma_f32_16x16x32_bf16 v[36:39], a[152:155], v[108:111], v[36:39]\n"
"v_mfma_f32_16x16x32_bf16 v[36:39], a[156:159], v[92:95], v[36:39]\n"
"v_mfma_f32_16x16x32_bf16 v[60:63], a[188:191], v[92:95], 0\n"
"v_mfma_f32_16x16x32_bf16 v[60:63], a[184:187], v[108:111], v[60:63]\n"
"v_mfma_f32_16x16x32_bf16 v[60:63], a[180:183], v[188:191], v[60:63]\n"
"v_mfma_f32_16x16x32_bf16 v[60:63], a[176:179], v[196:199], v[60:63]\n"
"v_mfma_f32_16x16x32_bf16 v[60:63], a[172:175], v[192:195], v[60:63]\n"
"v_mfma_f32_16x16x32_bf16 v[60:63], a[168:171], v[144:147], v[60:63]\n"
"s_nop 7\n"
"s_branch .Lmk_p60r\n"
".Lmk_p61s:\n"
";\n"
"s_waitcnt vmcnt(16)\n"
"s_mov_b32 s1, 0x10000\n"
".Lmk_p61l:\n"
"s_add_u32 s96, s57, 0x440000\n"
"buffer_load_dwordx4 v[152:155], v0, s[4:7], s96 offen sc1\n"
"buffer_load_dwordx4 v[180:183], v0, s[4:7], s96 offen offset:1024 sc1\n"
"buffer_load_dwordx4 v[24:27], v0, s[4:7], s96 offen offset:2048 sc1\n"
"buffer_load_dwordx4 v[32:35], v0, s[4:7], s96 offen offset:3072 sc1\n"
"buffer_load_dwordx4 v[44:47], v1, s[4:7], s96 offen sc1\n"
"buffer_load_dwordx4 v[48:51], v1, s[4:7], s96 offen offset:1024 sc1\n"
"buffer_load_dwordx4 v[28:31], v1, s[4:7], s96 offen offset:2048 sc1\n"
"buffer_load_dwordx4 v[56:59], v1, s[4:7], s96 offen offset:3072 sc1\n"
"s_add_u32 s96, s58, 0x440000\n"
"buffer_load_dwordx4 v[40:43], v0, s[4:7], s96 offen sc1\n"
"buffer_load_dwordx4 v[52:55], v0, s[4:7], s96 offen offset:1024 sc1\n"
"buffer_load_dwordx4 v[64:67], v0, s[4:7], s96 offen offset:2048 sc1\n"
"buffer_load_dwordx4 v[36:39], v0, s[4:7], s96 offen offset:3072 sc1\n"
"buffer_load_dwordx4 v[72:75], v1, s[4:7], s96 offen sc1\n"
"buffer_load_dwordx4 v[60:63], v1, s[4:7], s96 offen offset:1024 sc1\n"
"buffer_load_dwordx4 v[80:83], v1, s[4:7], s96 offen offset:2048 sc1\n"
"buffer_load_dwordx4 v[68:71], v1, s[4:7], s96 offen offset:3072 sc1\n"
"s_add_u32 s96, s59, 0x440000\n"
"buffer_load_dwordx4 v[96:99], v0, s[4:7], s96 offen sc1\n"
"buffer_load_dwordx4 v[76:79], v0, s[4:7], s96 offen offset:1024 sc1\n"
"buffer_load_dwordx4 v[112:115], v0, s[4:7], s96 offen offset:2048 sc1\n"
"s_waitcnt vmcnt(0)\n"
"v_max_u32_e32 v254, v152, v153\n"
"v_max3_u32 v254, v254, v154, v155\n"
"v_max3_u32 v254, v254, v180, v181\n"
"v_max3_u32 v254, v254, v182, v183\n"
"v_max3_u32 v254, v254, v24, v25\n"
"v_max3_u32 v254, v254, v26, v27\n"
"v_max3_u32 v254, v254, v32, v33\n"
"v_max3_u32 v254, v254, v34, v35\n"
"v_max3_u32 v254, v254, v44, v45\n"
"v_max3_u32 v254, v254, v46, v47\n"
"v_max3_u32 v254, v254, v48, v49\n"
"v_max3_u32 v254, v254, v50, v51\n"
"v_max3_u32 v254, v254, v28, v29\n"
"v_max3_u32 v254, v254, v30, v31\n"
"v_max3_u32 v254, v254, v56, v57\n"
"v_max3_u32 v254, v254, v58, v59\n"
"v_cmp_eq_u32_e32 vcc, -1, v254\n"
"s_cbranch_vccz .Lmk_p61k\n"
"s_sub_u32 s1, s1, 1\n"
"s_cbranch_scc0 .Lmk_p61l\n"
".Lmk_p61k:\n"
"v_mfma_f32_16x16x32_bf16 v[84:87], a[128:131], v[152:155], 0\n"
"v_mfma_f32_16x16x32_bf16 v[84:87], a[132:135], v[180:183], v[84:87]\n"
"v_mfma_f32_16x16x32_bf16 v[84:87], a[136:139], v[24:27], v[84:87]\n"
"v_mfma_f32_16x16x32_bf16 v[84:87], a[140:143], v[32:35], v[84:87]\n"
"v_mfma_f32_16x16x32_bf16 v[84:87], a[144:147], v[44:47], v[84:87]\n"
"v_mfma_f32_16x16x32_bf16 v[84:87], a[148:151], v[48:51], v[84:87]\n"
"v_mfma_f32_16x16x32_bf16 v[84:87], a[152:155], v[28:31], v[84:87]\n"
"v_mfma_f32_16x16x32_bf16 v[84:87], a[156:159], v[56:59], v[84:87]\n"
"v_mfma_f32_16x16x32_bf16 v[100:103], a[188:191], v[56:59], 0\n"
"v_mfma_f32_16x16x32_bf16 v[100:103], a[184:187], v[28:31], v[100:103]\n"
"v_mfma_f32_16x16x32_bf16 v[100:103], a[180:183], v[48:51], v[100:103]\n"
"v_mfma_f32_16x16x32_bf16 v[100:103], a[176:179], v[44:47], v[100:103]\n"
"v_mfma_f32_16x16x32_bf16 v[100:103], a[172:175], v[32:35], v[100:103]\n"
"v_mfma_f32_16x16x32_bf16 v[100:103], a[168:171], v[24:27], v[100:103]\n"
"s_nop 7\n"
"s_branch .Lmk_p61r\n"
".Lmk_p62s:\n"
";\n"
"s_waitcnt vmcnt(16)\n"
"s_mov_b32 s1, 0x10000\n"
".Lmk_p62l:\n"
"s_add_u32 s96, s58, 0x440000\n"
"buffer_load_dwordx4 v[40:43], v0, s[4:7], s96 offen sc1\n"
"buffer_load_dwordx4 v[52:55], v0, s[4:7], s96 offen offset:1024 sc1\n"
"buffer_load_dwordx4 v[64:67], v0, s[4:7], s96 offen offset:2048 sc1\n"
"buffer_load_dwordx4 v[36:39], v0, s[4:7], s96 offen offset:3072 sc1\n"
"buffer_load_dwordx4 v[72:75], v1, s[4:7], s96 offen sc1\n"
"buffer_load_dwordx4 v[60:63], v1, s[4:7], s96 offen offset:1024 sc1\n"
"buffer_load_dwordx4 v[80:83], v1, s[4:7], s96 offen offset:2048 sc1\n"
"buffer_load_dwordx4 v[68:71], v1, s[4:7], s96 offen offset:3072 sc1\n"
"s_add_u32 s96, s59, 0x440000\n"
"buffer_load_dwordx4 v[96:99], v0, s[4:7], s96 offen sc1\n"
"buffer_load_dwordx4 v[76:79], v0, s[4:7], s96 offen offset:1024 sc1\n"
"buffer_load_dwordx4 v[112:115], v0, s[4:7], s96 offen offset:2048 sc1\n"
"buffer_load_dwordx4 v[84:87], v0, s[4:7], s96 offen offset:3072 sc1\n"
"buffer_load_dwordx4 v[128:131], v1, s[4:7], s96 offen sc1\n"
"buffer_load_dwordx4 v[100:103], v1, s[4:7], s96 offen offset:1024 sc1\n"
"buffer_load_dwordx4 v[156:159], v1, s[4:7], s96 offen offset:2048 sc1\n"
"buffer_load_dwordx4 v[116:119], v1, s[4:7], s96 offen offset:3072 sc1\n"
"s_waitcnt vmcnt(0)\n"
"v_max_u32_e32 v237, v40, v41\n"
"v_max3_u32 v237, v237, v42, v43\n"
"v_max3_u32 v237, v237, v52, v53\n"
"v_max3_u32 v237, v237, v54, v55\n"
"v_max3_u32 v237, v237, v64, v65\n"
"v_max3_u32 v237, v237, v66, v67\n"
"v_max3_u32 v237, v237, v36, v37\n"
"v_max3_u32 v237, v237, v38, v39\n"
"v_max3_u32 v237, v237, v72, v73\n"
"v_max3_u32 v237, v237, v74, v75\n"
"v_max3_u32 v237, v237, v60, v61\n"
"v_max3_u32 v237, v237, v62, v63\n"
"v_max3_u32 v237, v237, v80, v81\n"
"v_max3_u32 v237, v237, v82, v83\n"
"v_max3_u32 v237, v237, v68, v69\n"
"v_max3_u32 v237, v237, v70, v71\n"
"v_cmp_eq_u32_e32 vcc, -1, v237\n"
"s_cbranch_vccz .Lmk_p62k\n"
"s_sub_u32 s1, s1, 1\n"
"s_cbranch_scc0 .Lmk_p62l\n"
".Lmk_p62k:\n"
"v_mfma_f32_16x16x32_bf16 v[132:135], a[128:131], v[40:43], 0\n"
"v_mfma_f32_16x16x32_bf16 v[132:135], a[132:135], v[52:55], v[132:135]\n"
"v_mfma_f32_16x16x32_bf16 v[132:135], a[136:139], v[64:67], v[132:135]\n"
"v_mfma_f32_16x16x32_bf16 v[132:135], a[140:143], v[36:39], v[132:135]\n"
"v_mfma_f32_16x16x32_bf16 v[132:135], a[144:147], v[72:75], v[132:135]\n"
"v_mfma_f32_16x16x32_bf16 v[132:135], a[148:151], v[60:63], v[132:135]\n"
"v_mfma_f32_16x16x32_bf16 v[132:135], a[152:155], v[80:83], v[132:135]\n"
"v_mfma_f32_16x16x32_bf16 v[132:135], a[156:159], v[68:71], v[132:135]\n"
"v_mfma_f32_16x16x32_bf16 v[140:143], a[188:191], v[68:71], 0\n"
"v_mfma_f32_16x16x32_bf16 v[140:143], a[184:187], v[80:83], v[140:143]\n"
"v_mfma_f32_16x16x32_bf16 v[140:143], a[180:183], v[60:63], v[140:143]\n"
"v_mfma_f32_16x16x32_bf16 v[140:143], a[176:179], v[72:75], v[140:143]\n"
"v_mfma_f32_16x16x32_bf16 v[140:143], a[172:175], v[36:39], v[140:143]\n"
"v_mfma_f32_16x16x32_bf16 v[140:143], a[168:171], v[64:67], v[140:143]\n"
"s_nop 7\n"
"s_branch .Lmk_p62r\n"
".Lmk_p63s:\n"
";\n"
"s_waitcnt vmcnt(16)\n"
"s_mov_b32 s1, 0x10000\n"
".Lmk_p63l:\n"
"s_add_u32 s96, s59, 0x440000\n"
"buffer_load_dwordx4 v[96:99], v0, s[4:7], s96 offen sc1\n"
"buffer_load_dwordx4 v[76:79], v0, s[4:7], s96 offen offset:1024 sc1\n"
"buffer_load_dwordx4 v[112:115], v0, s[4:7], s96 offen offset:2048 sc1\n"
"buffer_load_dwordx4 v[84:87], v0, s[4:7], s96 offen offset:3072 sc1\n"
"buffer_load_dwordx4 v[128:131], v1, s[4:7], s96 offen sc1\n"
"buffer_load_dwordx4 v[100:103], v1, s[4:7], s96 offen offset:1024 sc1\n"
"buffer_load_dwordx4 v[156:159], v1, s[4:7], s96 offen offset:2048 sc1\n"
"buffer_load_dwordx4 v[116:119], v1, s[4:7], s96 offen offset:3072 sc1\n"
"s_add_u32 s96, s56, 0x400000\n"
"buffer_load_dwordx4 v[132:135], v0, s[4:7], s96 offen sc1\n"
"buffer_load_dwordx4 v[184:187], v0, s[4:7], s96 offen offset:1024 sc1\n"
"buffer_load_dwordx4 v[140:143], v0, s[4:7], s96 offen offset:2048 sc1\n"
"buffer_load_dwordx4 v[68:71], v0, s[4:7], s96 offen offset:3072 sc1\n"
"buffer_load_dwordx4 v[36:39], v1, s[4:7], s96 offen sc1\n"
"buffer_load_dwordx4 v[40:43], v1, s[4:7], s96 offen offset:1024 sc1\n"
"buffer_load_dwordx4 v[72:75], v1, s[4:7], s96 offen offset:2048 sc1\n"
"buffer_load_dwordx4 v[80:83], v1, s[4:7], s96 offen offset:3072 sc1\n"
"s_add_u32 s96, s57, 0x400000\n"
"buffer_load_dwordx4 v[164:167], v0, s[4:7], s96 offen sc1\n"
"s_waitcnt vmcnt(0)\n"
"v_max_u32_e32 v254, v96, v97\n"
"v_max3_u32 v254, v254, v98, v99\n"
"v_max3_u32 v254, v254, v76, v77\n"
"v_max3_u32 v254, v254, v78, v79\n"
"v_max3_u32 v254, v254, v112, v113\n"
"v_max3_u32 v254, v254, v114, v115\n"
"v_max3_u32 v254, v254, v84, v85\n"
"v_max3_u32 v254, v254, v86, v87\n"
"v_max3_u32 v254, v254, v128, v129\n"
"v_max3_u32 v254, v254, v130, v131\n"
"v_max3_u32 v254, v254, v100, v101\n"
"v_max3_u32 v254, v254, v102, v103\n"
"v_max3_u32 v254, v254, v156, v157\n"
"v_max3_u32 v254, v254, v158, v159\n"
"v_max3_u32 v254, v254, v116, v117\n"
"v_max3_u32 v254, v254, v118, v119\n"
"v_cmp_eq_u32_e32 vcc, -1, v254\n"
"s_cbranch_vccz .Lmk_p63k\n"
"s_sub_u32 s1, s1, 1\n"
"s_cbranch_scc0 .Lmk_p63l\n"
".Lmk_p63k:\n"
"v_mfma_f32_16x16x32_bf16 v[64:67], a[128:131], v[96:99], v[148:151]\n"
"v_mfma_f32_16x16x32_bf16 v[64:67], a[132:135], v[76:79], v[64:67]\n"
"v_mfma_f32_16x16x32_bf16 v[64:67], a[136:139], v[112:115], v[64:67]\n"
"v_mfma_f32_16x16x32_bf16 v[64:67], a[140:143], v[84:87], v[64:67]\n"
"v_mfma_f32_16x16x32_bf16 v[64:67], a[144:147], v[128:131], v[64:67]\n"
"v_mfma_f32_16x16x32_bf16 v[64:67], a[148:151], v[100:103], v[64:67]\n"
"v_mfma_f32_16x16x32_bf16 v[64:67], a[152:155], v[156:159], v[64:67]\n"
"v_mfma_f32_16x16x32_bf16 v[64:67], a[156:159], v[116:119], v[64:67]\n"
"v_mfma_f32_16x16x32_bf16 v[60:63], a[188:191], v[116:119], v[168:171]\n"
"v_mfma_f32_16x16x32_bf16 v[60:63], a[184:187], v[156:159], v[60:63]\n"
"v_mfma_f32_16x16x32_bf16 v[60:63], a[180:183], v[100:103], v[60:63]\n"
"v_mfma_f32_16x16x32_bf16 v[60:63], a[176:179], v[128:131], v[60:63]\n"
"v_mfma_f32_16x16x32_bf16 v[60:63], a[172:175], v[84:87], v[60:63]\n"
"v_mfma_f32_16x16x32_bf16 v[60:63], a[168:171], v[112:115], v[60:63]\n"
"s_nop 7\n"
"s_branch .Lmk_p63r\n"
".Lmk_p64s:\n"
";\n"
"s_waitcnt vmcnt(16)\n"
"s_mov_b32 s1, 0x10000\n"
".Lmk_p64l:\n"
"s_add_u32 s96, s56, 0x400000\n"
"buffer_load_dwordx4 v[132:135], v0, s[4:7], s96 offen sc1\n"
"buffer_load_dwordx4 v[184:187], v0, s[4:7], s96 offen offset:1024 sc1\n"
"buffer_load_dwordx4 v[140:143], v0, s[4:7], s96 offen offset:2048 sc1\n"
"buffer_load_dwordx4 v[68:71], v0, s[4:7], s96 offen offset:3072 sc1\n"
"buffer_load_dwordx4 v[36:39], v1, s[4:7], s96 offen sc1\n"
"buffer_load_dwordx4 v[40:43], v1, s[4:7], s96 offen offset:1024 sc1\n"
"buffer_load_dwordx4 v[72:75], v1, s[4:7], s96 offen offset:2048 sc1\n"
"buffer_load_dwordx4 v[80:83], v1, s[4:7], s96 offen offset:3072 sc1\n"
"s_add_u32 s96, s57, 0x400000\n"
"buffer_load_dwordx4 v[164:167], v0, s[4:7], s96 offen sc1\n"
"buffer_load_dwordx4 v[168:171], v0, s[4:7], s96 offen offset:1024 sc1\n"
"buffer_load_dwordx4 v[208:211], v0, s[4:7], s96 offen offset:2048 sc1\n"
"buffer_load_dwordx4 v[212:215], v0, s[4:7], s96 offen offset:3072 sc1\n"
"buffer_load_dwordx4 v[64:67], v1, s[4:7], s96 offen sc1\n"
"buffer_load_dwordx4 v[100:103], v1, s[4:7], s96 offen offset:1024 sc1\n"
"buffer_load_dwordx4 v[60:63], v1, s[4:7], s96 offen offset:2048 sc1\n"
"buffer_load_dwordx4 v[76:79], v1, s[4:7], s96 offen offset:3072 sc1\n"
"s_add_u32 s96, s58, 0x400000\n"
"buffer_load_dwordx4 v[96:99], v0, s[4:7], s96 offen sc1\n"
"buffer_load_dwordx4 v[112:115], v0, s[4:7], s96 offen offset:1024 sc1\n"
"buffer_load_dwordx4 v[52:55], v0, s[4:7], s96 offen offset:2048 sc1\n"
"buffer_load_dwordx4 v[148:151], v0, s[4:7], s96 offen offset:3072 sc1\n"
"s_waitcnt vmcnt(0)\n"
"v_max_u32_e32 v255, v132, v133\n"
"v_max3_u32 v255, v255, v134, v135\n"
"v_max3_u32 v255, v255, v184, v185\n"
"v_max3_u32 v255, v255, v186, v187\n"
"v_max3_u32 v255, v255, v140, v141\n"
"v_max3_u32 v255, v255, v142, v143\n"
"v_max3_u32 v255, v255, v68, v69\n"
"v_max3_u32 v255, v255, v70, v71\n"
"v_max3_u32 v255, v255, v36, v37\n"
"v_max3_u32 v255, v255, v38, v39\n"
"v_max3_u32 v255, v255, v40, v41\n"
"v_max3_u32 v255, v255, v42, v43\n"
"v_max3_u32 v255, v255, v72, v73\n"
"v_max3_u32 v255, v255, v74, v75\n"
"v_max3_u32 v255, v255, v80, v81\n"
"v_max3_u32 v255, v255, v82, v83\n"
"v_cmp_eq_u32_e32 vcc, -1, v255\n"
"s_cbranch_vccz .Lmk_p64k\n"
"s_sub_u32 s1, s1, 1\n"
"s_cbranch_scc0 .Lmk_p64l\n"
".Lmk_p64k:\n"
"v_mfma_f32_16x16x32_bf16 v[84:87], a[128:131], v[132:135], 0\n"
"v_mfma_f32_16x16x32_bf16 v[84:87], a[132:135], v[184:187], v[84:87]\n"
"v_mfma_f32_16x16x32_bf16 v[84:87], a[136:139], v[140:143], v[84:87]\n"
"v_mfma_f32_16x16x32_bf16 v[84:87], a[140:143], v[68:71], v[84:87]\n"
"v_mfma_f32_16x16x32_bf16 v[84:87], a[144:147], v[36:39], v[84:87]\n"
"v_mfma_f32_16x16x32_bf16 v[84:87], a[148:151], v[40:43], v[84:87]\n"
"v_mfma_f32_16x16x32_bf16 v[84:87], a[152:155], v[72:75], v[84:87]\n"
"v_mfma_f32_16x16x32_bf16 v[84:87], a[156:159], v[80:83], v[84:87]\n"
"v_mfma_f32_16x16x32_bf16 v[128:131], a[188:191], v[80:83], 0\n"
"v_mfma_f32_16x16x32_bf16 v[128:131], a[184:187], v[72:75], v[128:131]\n"
"v_mfma_f32_16x16x32_bf16 v[128:131], a[180:183], v[40:43], v[128:131]\n"
"v_mfma_f32_16x16x32_bf16 v[128:131], a[176:179], v[36:39], v[128:131]\n"
"v_mfma_f32_16x16x32_bf16 v[128:131], a[172:175], v[68:71], v[128:131]\n"
"v_mfma_f32_16x16x32_bf16 v[128:131], a[168:171], v[140:143], v[128:131]\n"
"s_nop 7\n"
"s_branch .Lmk_p64r\n"
".Lmk_p65s:\n"
";\n"
"s_waitcnt vmcnt(16)\n"
"s_mov_b32 s1, 0x10000\n"
".Lmk_p65l:\n"
"s_add_u32 s96, s57, 0x400000\n"
"buffer_load_dwordx4 v[164:167], v0, s[4:7], s96 offen sc1\n"
"buffer_load_dwordx4 v[168:171], v0, s[4:7], s96 offen offset:1024 sc1\n"
"buffer_load_dwordx4 v[208:211], v0, s[4:7], s96 offen offset:2048 sc1\n"
"buffer_load_dwordx4 v[212:215], v0, s[4:7], s96 offen offset:3072 sc1\n"
"buffer_load_dwordx4 v[64:67], v1, s[4:7], s96 offen sc1\n"
"buffer_load_dwordx4 v[100:103], v1, s[4:7], s96 offen offset:1024 sc1\n"
"buffer_load_dwordx4 v[60:63], v1, s[4:7], s96 offen offset:2048 sc1\n"
"buffer_load_dwordx4 v[76:79], v1, s[4:7], s96 offen offset:3072 sc1\n"
"s_add_u32 s96, s58, 0x400000\n"
"buffer_load_dwordx4 v[96:99], v0, s[4:7], s96 offen sc1\n"
"buffer_load_dwordx4 v[112:115], v0, s[4:7], s96 offen offset:1024 sc1\n"
"buffer_load_dwordx4 v[52:55], v0, s[4:7], s96 offen offset:2048 sc1\n"
"buffer_load_dwordx4 v[148:151], v0, s[4:7], s96 offen offset:3072 sc1\n"
"buffer_load_dwordx4 v[84:87], v1, s[4:7], s96 offen sc1\n"
"buffer_load_dwordx4 v[172:175], v1, s[4:7], s96 offen offset:1024 sc1\n"
"buffer_load_dwordx4 v[128:131], v1, s[4:7], s96 offen offset:2048 sc1\n"
"buffer_load_dwordx4 v[72:75], v1, s[4:7], s96 offen offset:3072 sc1\n"
"s_add_u32 s96, s59, 0x400000\n"
"buffer_load_dwordx4 v[36:39], v0, s[4:7], s96 offen sc1\n"
"buffer_load_dwordx4 v[40:43], v0, s[4:7], s96 offen offset:1024 sc1\n"
"buffer_load_dwordx4 v[80:83], v0, s[4:7], s96 offen offset:2048 sc1\n"
"buffer_load_dwordx4 v[132:135], v0, s[4:7], s96 offen offset:3072 sc1\n"
"buffer_load_dwordx4 v[156:159], v1, s[4:7], s96 offen sc1\n"
"s_waitcnt vmcnt(0)\n"
"v_max_u32_e32 v254, v164, v165\n"
"v_max3_u32 v254, v254, v166, v167\n"
"v_max3_u32 v254, v254, v168, v169\n"
"v_max3_u32 v254, v254, v170, v171\n"
"v_max3_u32 v254, v254, v208, v209\n"
"v_max3_u32 v254, v254, v210, v211\n"
"v_max3_u32 v254, v254, v212, v213\n"
"v_max3_u32 v254, v254, v214, v215\n"
"v_max3_u32 v254, v254, v64, v65\n"
"v_max3_u32 v254, v254, v66, v67\n"
"v_max3_u32 v254, v254, v100, v101\n"
"v_max3_u32 v254, v254, v102, v103\n"
"v_max3_u32 v254, v254, v60, v61\n"
"v_max3_u32 v254, v254, v62, v63\n"
"v_max3_u32 v254, v254, v76, v77\n"
"v_max3_u32 v254, v254, v78, v79\n"
"v_cmp_eq_u32_e32 vcc, -1, v254\n"
"s_cbranch_vccz .Lmk_p65k\n"
"s_sub_u32 s1, s1, 1\n"
"s_cbranch_scc0 .Lmk_p65l\n"
".Lmk_p65k:\n"
"v_mfma_f32_16x16x32_bf16 v[68:71], a[128:131], v[164:167], 0\n"
"v_mfma_f32_16x16x32_bf16 v[68:71], a[132:135], v[168:171], v[68:71]\n"
"v_mfma_f32_16x16x32_bf16 v[68:71], a[136:139], v[208:211], v[68:71]\n"
"v_mfma_f32_16x16x32_bf16 v[68:71], a[140:143], v[212:215], v[68:71]\n"
"v_mfma_f32_16x16x32_bf16 v[68:71], a[144:147], v[64:67], v[68:71]\n"
"v_mfma_f32_16x16x32_bf16 v[68:71], a[148:151], v[100:103], v[68:71]\n"
"v_mfma_f32_16x16x32_bf16 v[68:71], a[152:155], v[60:63], v[68:71]\n"
"v_mfma_f32_16x16x32_bf16 v[68:71], a[156:159], v[76:79], v[68:71]\n"
"v_mfma_f32_16x16x32_bf16 v[140:143], a[188:191], v[76:79], 0\n"
"v_mfma_f32_16x16x32_bf16 v[140:143], a[184:187], v[60:63], v[140:143]\n"
"v_mfma_f32_16x16x32_bf16 v[140:143], a[180:183], v[100:103], v[140:143]\n"
"v_mfma_f32_16x16x32_bf16 v[140:143], a[176:179], v[64:67], v[140:143]\n"
"v_mfma_f32_16x16x32_bf16 v[140:143], a[172:175], v[212:215], v[140:143]\n"
"v_mfma_f32_16x16x32_bf16 v[140:143], a[168:171], v[208:211], v[140:143]\n"
"s_nop 7\n"
"s_branch .Lmk_p65r\n"
".Lmk_p66s:\n"
";\n"
"s_waitcnt vmcnt(16)\n"
"s_mov_b32 s1, 0x10000\n"
".Lmk_p66l:\n"
"s_add_u32 s97, s58, 0x400000\n"
"buffer_load_dwordx4 v[96:99], v0, s[4:7], s97 offen sc1\n"
"buffer_load_dwordx4 v[112:115], v0, s[4:7], s97 offen offset:1024 sc1\n"
"buffer_load_dwordx4 v[52:55], v0, s[4:7], s97 offen offset:2048 sc1\n"
"buffer_load_dwordx4 v[148:151], v0, s[4:7], s97 offen offset:3072 sc1\n"
"buffer_load_dwordx4 v[84:87], v1, s[4:7], s97 offen sc1\n"
"buffer_load_dwordx4 v[172:175], v1, s[4:7], s97 offen offset:1024 sc1\n"
"buffer_load_dwordx4 v[128:131], v1, s[4:7], s97 offen offset:2048 sc1\n"
"buffer_load_dwordx4 v[72:75], v1, s[4:7], s97 offen offset:3072 sc1\n"
"s_add_u32 s97, s59, 0x400000\n"
"buffer_load_dwordx4 v[36:39], v0, s[4:7], s97 offen sc1\n"
"buffer_load_dwordx4 v[40:43], v0, s[4:7], s97 offen offset:1024 sc1\n"
"buffer_load_dwordx4 v[80:83], v0, s[4:7], s97 offen offset:2048 sc1\n"
"buffer_load_dwordx4 v[132:135], v0, s[4:7], s97 offen offset:3072 sc1\n"
"buffer_load_dwordx4 v[156:159], v1, s[4:7], s97 offen sc1\n"
"buffer_load_dwordx4 v[68:71], v1, s[4:7], s97 offen offset:1024 sc1\n"
"buffer_load_dwordx4 v[200:203], v1, s[4:7], s97 offen offset:2048 sc1\n"
"buffer_load_dwordx4 v[140:143], v1, s[4:7], s97 offen offset:3072 sc1\n"
"s_waitcnt vmcnt(0)\n"
"v_max_u32_e32 v253, v96, v97\n"
"v_max3_u32 v253, v253, v98, v99\n"
"v_max3_u32 v253, v253, v112, v113\n"
"v_max3_u32 v253, v253, v114, v115\n"
"v_max3_u32 v253, v253, v52, v53\n"
"v_max3_u32 v253, v253, v54, v55\n"
"v_max3_u32 v253, v253, v148, v149\n"
"v_max3_u32 v253, v253, v150, v151\n"
"v_max3_u32 v253, v253, v84, v85\n"
"v_max3_u32 v253, v253, v86, v87\n"
"v_max3_u32 v253, v253, v172, v173\n"
"v_max3_u32 v253, v253, v174, v175\n"
"v_max3_u32 v253, v253, v128, v129\n"
"v_max3_u32 v253, v253, v130, v131\n"
"v_max3_u32 v253, v253, v72, v73\n"
"v_max3_u32 v253, v253, v74, v75\n"
"v_cmp_eq_u32_e32 vcc, -1, v253\n"
"s_cbranch_vccz .Lmk_p66k\n"
"s_sub_u32 s1, s1, 1\n"
"s_cbranch_scc0 .Lmk_p66l\n"
".Lmk_p66k:\n"
"v_mfma_f32_16x16x32_bf16 v[60:63], a[128:131], v[96:99], 0\n"
"v_mfma_f32_16x16x32_bf16 v[60:63], a[132:135], v[112:115], v[60:63]\n"
"v_mfma_f32_16x16x32_bf16 v[60:63], a[136:139], v[52:55], v[60:63]\n"
"v_mfma_f32_16x16x32_bf16 v[60:63], a[140:143], v[148:151], v[60:63]\n"
"v_mfma_f32_16x16x32_bf16 v[60:63], a[144:147], v[84:87], v[60:63]\n"
"v_mfma_f32_16x16x32_bf16 v[60:63], a[148:151], v[172:175], v[60:63]\n"
"v_mfma_f32_16x16x32_bf16 v[60:63], a[152:155], v[128:131], v[60:63]\n"
"v_mfma_f32_16x16x32_bf16 v[60:63], a[156:159], v[72:75], v[60:63]\n"
"v_mfma_f32_16x16x32_bf16 v[64:67], a[188:191], v[72:75], 0\n"
"v_mfma_f32_16x16x32_bf16 v[64:67], a[184:187], v[128:131], v[64:67]\n"
"v_mfma_f32_16x16x32_bf16 v[64:67], a[180:183], v[172:175], v[64:67]\n"
"v_mfma_f32_16x16x32_bf16 v[64:67], a[176:179], v[84:87], v[64:67]\n"
"v_mfma_f32_16x16x32_bf16 v[64:67], a[172:175], v[148:151], v[64:67]\n"
"v_mfma_f32_16x16x32_bf16 v[64:67], a[168:171], v[52:55], v[64:67]\n"
"s_nop 7\n"
"s_branch .Lmk_p66r\n"
".Lmk_p67s:\n"
";\n"
"s_waitcnt vmcnt(16)\n"
"s_mov_b32 s1, 0x10000\n"
".Lmk_p67l:\n"
"s_add_u32 s97, s59, 0x400000\n"
"buffer_load_dwordx4 v[36:39], v0, s[4:7], s97 offen sc1\n"
"buffer_load_dwordx4 v[40:43], v0, s[4:7], s97 offen offset:1024 sc1\n"
"buffer_load_dwordx4 v[80:83], v0, s[4:7], s97 offen offset:2048 sc1\n"
"buffer_load_dwordx4 v[132:135], v0, s[4:7], s97 offen offset:3072 sc1\n"
"buffer_load_dwordx4 v[156:159], v1, s[4:7], s97 offen sc1\n"
"buffer_load_dwordx4 v[68:71], v1, s[4:7], s97 offen offset:1024 sc1\n"
"buffer_load_dwordx4 v[200:203], v1, s[4:7], s97 offen offset:2048 sc1\n"
"buffer_load_dwordx4 v[140:143], v1, s[4:7], s97 offen offset:3072 sc1\n"
"s_waitcnt vmcnt(0)\n"
"v_max_u32_e32 v254, v36, v37\n"
"v_max3_u32 v254, v254, v38, v39\n"
"v_max3_u32 v254, v254, v40, v41\n"
"v_max3_u32 v254, v254, v42, v43\n"
"v_max3_u32 v254, v254, v80, v81\n"
"v_max3_u32 v254, v254, v82, v83\n"
"v_max3_u32 v254, v254, v132, v133\n"
"v_max3_u32 v254, v254, v134, v135\n"
"v_max3_u32 v254, v254, v156, v157\n"
"v_max3_u32 v254, v254, v158, v159\n"
"v_max3_u32 v254, v254, v68, v69\n"
"v_max3_u32 v254, v254, v70, v71\n"
"v_max3_u32 v254, v254, v200, v201\n"
"v_max3_u32 v254, v254, v202, v203\n"
"v_max3_u32 v254, v254, v140, v141\n"
"v_max3_u32 v254, v254, v142, v143\n"
"v_cmp_eq_u32_e32 vcc, -1, v254\n"
"s_cbranch_vccz .Lmk_p67k\n"
"s_sub_u32 s1, s1, 1\n"
"s_cbranch_scc0 .Lmk_p67l\n"
".Lmk_p67k:\n"
"v_mfma_f32_16x16x32_bf16 v[52:55], a[128:131], v[36:39], v[76:79]\n"
"v_mfma_f32_16x16x32_bf16 v[52:55], a[132:135], v[40:43], v[52:55]\n"
"v_mfma_f32_16x16x32_bf16 v[52:55], a[136:139], v[80:83], v[52:55]\n"
"v_mfma_f32_16x16x32_bf16 v[52:55], a[140:143], v[132:135], v[52:55]\n"
"v_mfma_f32_16x16x32_bf16 v[52:55], a[144:147], v[156:159], v[52:55]\n"
"v_mfma_f32_16x16x32_bf16 v[52:55], a[148:151], v[68:71], v[52:55]\n"
"v_mfma_f32_16x16x32_bf16 v[52:55], a[152:155], v[200:203], v[52:55]\n"
"v_mfma_f32_16x16x32_bf16 v[52:55], a[156:159], v[140:143], v[52:55]\n"
"v_mfma_f32_16x16x32_bf16 v[60:63], a[188:191], v[140:143], v[116:119]\n"
"v_mfma_f32_16x16x32_bf16 v[60:63], a[184:187], v[200:203], v[60:63]\n"
"v_mfma_f32_16x16x32_bf16 v[60:63], a[180:183], v[68:71], v[60:63]\n"
"v_mfma_f32_16x16x32_bf16 v[60:63], a[176:179], v[156:159], v[60:63]\n"
"v_mfma_f32_16x16x32_bf16 v[60:63], a[172:175], v[132:135], v[60:63]\n"
"v_mfma_f32_16x16x32_bf16 v[60:63], a[168:171], v[80:83], v[60:63]\n"
"s_nop 7\n"
"s_branch .Lmk_p67r\n"
".Lmk_p68s:\n"
";\n"
"s_waitcnt vmcnt(16)\n"
"s_mov_b32 s1, 0x10000\n"
".Lmk_p68l:\n"
"s_add_u32 s96, s56, 0x440000\n"
"buffer_load_dwordx4 v[120:123], v0, s[4:7], s96 offen sc1\n"
"buffer_load_dwordx4 v[136:139], v0, s[4:7], s96 offen offset:1024 sc1\n"
"buffer_load_dwordx4 v[144:147], v0, s[4:7], s96 offen offset:2048 sc1\n"
"buffer_load_dwordx4 v[192:195], v0, s[4:7], s96 offen offset:3072 sc1\n"
"buffer_load_dwordx4 v[196:199], v1, s[4:7], s96 offen sc1\n"
"buffer_load_dwordx4 v[188:191], v1, s[4:7], s96 offen offset:1024 sc1\n"
"buffer_load_dwordx4 v[108:111], v1, s[4:7], s96 offen offset:2048 sc1\n"
"buffer_load_dwordx4 v[92:95], v1, s[4:7], s96 offen offset:3072 sc1\n"
"s_add_u32 s96, s57, 0x440000\n"
"buffer_load_dwordx4 v[152:155], v0, s[4:7], s96 offen sc1\n"
"buffer_load_dwordx4 v[180:183], v0, s[4:7], s96 offen offset:1024 sc1\n"
"buffer_load_dwordx4 v[24:27], v0, s[4:7], s96 offen offset:2048 sc1\n"
"buffer_load_dwordx4 v[32:35], v0, s[4:7], s96 offen offset:3072 sc1\n"
"buffer_load_dwordx4 v[44:47], v1, s[4:7], s96 offen sc1\n"
"buffer_load_dwordx4 v[48:51], v1, s[4:7], s96 offen offset:1024 sc1\n"
"buffer_load_dwordx4 v[28:31], v1, s[4:7], s96 offen offset:2048 sc1\n"
"buffer_load_dwordx4 v[56:59], v1, s[4:7], s96 offen offset:3072 sc1\n"
"s_add_u32 s96, s58, 0x440000\n"
"buffer_load_dwordx4 v[36:39], v0, s[4:7], s96 offen sc1\n"
"buffer_load_dwordx4 v[40:43], v0, s[4:7], s96 offen offset:1024 sc1\n"
"buffer_load_dwordx4 v[52:55], v0, s[4:7], s96 offen offset:2048 sc1\n"
"buffer_load_dwordx4 v[60:63], v0, s[4:7], s96 offen offset:3072 sc1\n"
"buffer_load_dwordx4 v[64:67], v1, s[4:7], s96 offen sc1\n"
"buffer_load_dwordx4 v[68:71], v1, s[4:7], s96 offen offset:1024 sc1\n"
"buffer_load_dwordx4 v[72:75], v1, s[4:7], s96 offen offset:2048 sc1\n"
"buffer_load_dwordx4 v[76:79], v1, s[4:7], s96 offen offset:3072 sc1\n"
"s_add_u32 s96, s59, 0x440000\n"
"buffer_load_dwordx4 v[84:87], v0, s[4:7], s96 offen sc1\n"
"buffer_load_dwordx4 v[88:91], v0, s[4:7], s96 offen offset:1024 sc1\n"
"buffer_load_dwordx4 v[100:103], v0, s[4:7], s96 offen offset:2048 sc1\n"
"buffer_load_dwordx4 v[104:107], v0, s[4:7], s96 offen offset:3072 sc1\n"
"s_waitcnt vmcnt(0)\n"
"v_max_u32_e32 v255, v120, v121\n"
"v_max3_u32 v255, v255, v122, v123\n"
"v_max3_u32 v255, v255, v136, v137\n"
"v_max3_u32 v255, v255, v138, v139\n"
"v_max3_u32 v255, v255, v144, v145\n"
"v_max3_u32 v255, v255, v146, v147\n"
"v_max3_u32 v255, v255, v192, v193\n"
"v_max3_u32 v255, v255, v194, v195\n"
"v_max3_u32 v255, v255, v196, v197\n"
"v_max3_u32 v255, v255, v198, v199\n"
"v_max3_u32 v255, v255, v188, v189\n"
"v_max3_u32 v255, v255, v190, v191\n"
"v_max3_u32 v255, v255, v108, v109\n"
"v_max3_u32 v255, v255, v110, v111\n"
"v_max3_u32 v255, v255, v92, v93\n"
"v_max3_u32 v255, v255, v94, v95\n"
"v_cmp_eq_u32_e32 vcc, -1, v255\n"
"s_cbranch_vccz .Lmk_p68k\n"
"s_sub_u32 s1, s1, 1\n"
"s_cbranch_scc0 .Lmk_p68l\n"
".Lmk_p68k:\n"
"v_mfma_f32_16x16x32_bf16 v[80:83], a[128:131], v[120:123], 0\n"
"v_mfma_f32_16x16x32_bf16 v[80:83], a[132:135], v[136:139], v[80:83]\n"
"v_mfma_f32_16x16x32_bf16 v[80:83], a[136:139], v[144:147], v[80:83]\n"
"v_mfma_f32_16x16x32_bf16 v[80:83], a[140:143], v[192:195], v[80:83]\n"
"v_mfma_f32_16x16x32_bf16 v[80:83], a[144:147], v[196:199], v[80:83]\n"
"v_mfma_f32_16x16x32_bf16 v[80:83], a[148:151], v[188:191], v[80:83]\n"
"v_mfma_f32_16x16x32_bf16 v[80:83], a[152:155], v[108:111], v[80:83]\n"
"v_mfma_f32_16x16x32_bf16 v[80:83], a[156:159], v[92:95], v[80:83]\n"
"v_mfma_f32_16x16x32_bf16 v[96:99], a[188:191], v[92:95], 0\n"
"v_mfma_f32_16x16x32_bf16 v[96:99], a[184:187], v[108:111], v[96:99]\n"
"v_mfma_f32_16x16x32_bf16 v[96:99], a[180:183], v[188:191], v[96:99]\n"
"v_mfma_f32_16x16x32_bf16 v[96:99], a[176:179], v[196:199], v[96:99]\n"
"v_mfma_f32_16x16x32_bf16 v[96:99], a[172:175], v[192:195], v[96:99]\n"
"v_mfma_f32_16x16x32_bf16 v[96:99], a[168:171], v[144:147], v[96:99]\n"
"s_nop 7\n"
"s_branch .Lmk_p68r\n"
".Lmk_p69s:\n"
";\n"
"s_waitcnt vmcnt(16)\n"
"s_mov_b32 s1, 0x10000\n"
".Lmk_p69l:\n"
"s_add_u32 s96, s57, 0x440000\n"
"buffer_load_dwordx4 v[152:155], v0, s[4:7], s96 offen sc1\n"
"buffer_load_dwordx4 v[180:183], v0, s[4:7], s96 offen offset:1024 sc1\n"
"buffer_load_dwordx4 v[24:27], v0, s[4:7], s96 offen offset:2048 sc1\n"
"buffer_load_dwordx4 v[32:35], v0, s[4:7], s96 offen offset:3072 sc1\n"
"buffer_load_dwordx4 v[44:47], v1, s[4:7], s96 offen sc1\n"
"buffer_load_dwordx4 v[48:51], v1, s[4:7], s96 offen offset:1024 sc1\n"
"buffer_load_dwordx4 v[28:31], v1, s[4:7], s96 offen offset:2048 sc1\n"
"buffer_load_dwordx4 v[56:59], v1, s[4:7], s96 offen offset:3072 sc1\n"
"s_add_u32 s96, s58, 0x440000\n"
"buffer_load_dwordx4 v[36:39], v0, s[4:7], s96 offen sc1\n"
"buffer_load_dwordx4 v[40:43], v0, s[4:7], s96 offen offset:1024 sc1\n"
"buffer_load_dwordx4 v[52:55], v0, s[4:7], s96 offen offset:2048 sc1\n"
"buffer_load_dwordx4 v[60:63], v0, s[4:7], s96 offen offset:3072 sc1\n"
"buffer_load_dwordx4 v[64:67], v1, s[4:7], s96 offen sc1\n"
"buffer_load_dwordx4 v[68:71], v1, s[4:7], s96 offen offset:1024 sc1\n"
"buffer_load_dwordx4 v[72:75], v1, s[4:7], s96 offen offset:2048 sc1\n"
"buffer_load_dwordx4 v[76:79], v1, s[4:7], s96 offen offset:3072 sc1\n"
"s_add_u32 s96, s59, 0x440000\n"
"buffer_load_dwordx4 v[84:87], v0, s[4:7], s96 offen sc1\n"
"buffer_load_dwordx4 v[88:91], v0, s[4:7], s96 offen offset:1024 sc1\n"
"buffer_load_dwordx4 v[100:103], v0, s[4:7], s96 offen offset:2048 sc1\n"
"buffer_load_dwordx4 v[104:107], v0, s[4:7], s96 offen offset:3072 sc1\n"
"buffer_load_dwordx4 v[80:83], v1, s[4:7], s96 offen sc1\n"
"buffer_load_dwordx4 v[116:119], v1, s[4:7], s96 offen offset:1024 sc1\n"
"buffer_load_dwordx4 v[96:99], v1, s[4:7], s96 offen offset:2048 sc1\n"
"buffer_load_dwordx4 v[92:95], v1, s[4:7], s96 offen offset:3072 sc1\n"
"s_add_u32 s96, s56, 0x400000\n"
"buffer_load_dwordx4 v[120:123], v0, s[4:7], s96 offen sc1\n"
"buffer_load_dwordx4 v[124:127], v0, s[4:7], s96 offen offset:1024 sc1\n"
"s_waitcnt vmcnt(0)\n"
"v_max_u32_e32 v255, v152, v153\n"
"v_max3_u32 v255, v255, v154, v155\n"
"v_max3_u32 v255, v255, v180, v181\n"
"v_max3_u32 v255, v255, v182, v183\n"
"v_max3_u32 v255, v255, v24, v25\n"
"v_max3_u32 v255, v255, v26, v27\n"
"v_max3_u32 v255, v255, v32, v33\n"
"v_max3_u32 v255, v255, v34, v35\n"
"v_max3_u32 v255, v255, v44, v45\n"
"v_max3_u32 v255, v255, v46, v47\n"
"v_max3_u32 v255, v255, v48, v49\n"
"v_max3_u32 v255, v255, v50, v51\n"
"v_max3_u32 v255, v255, v28, v29\n"
"v_max3_u32 v255, v255, v30, v31\n"
"v_max3_u32 v255, v255, v56, v57\n"
"v_max3_u32 v255, v255, v58, v59\n"
"v_cmp_eq_u32_e32 vcc, -1, v255\n"
"s_cbranch_vccz .Lmk_p69k\n"
"s_sub_u32 s1, s1, 1\n"
"s_cbranch_scc0 .Lmk_p69l\n"
".Lmk_p69k:\n"
"v_mfma_f32_16x16x32_bf16 v[108:111], a[128:131], v[152:155], 0\n"
"v_mfma_f32_16x16x32_bf16 v[108:111], a[132:135], v[180:183], v[108:111]\n"
"v_mfma_f32_16x16x32_bf16 v[108:111], a[136:139], v[24:27], v[108:111]\n"
"v_mfma_f32_16x16x32_bf16 v[108:111], a[140:143], v[32:35], v[108:111]\n"
"v_mfma_f32_16x16x32_bf16 v[108:111], a[144:147], v[44:47], v[108:111]\n"
"v_mfma_f32_16x16x32_bf16 v[108:111], a[148:151], v[48:51], v[108:111]\n"
"v_mfma_f32_16x16x32_bf16 v[108:111], a[152:155], v[28:31], v[108:111]\n"
"v_mfma_f32_16x16x32_bf16 v[108:111], a[156:159], v[56:59], v[108:111]\n"
"v_mfma_f32_16x16x32_bf16 v[112:115], a[188:191], v[56:59], 0\n"
"v_mfma_f32_16x16x32_bf16 v[112:115], a[184:187], v[28:31], v[112:115]\n"
"v_mfma_f32_16x16x32_bf16 v[112:115], a[180:183], v[48:51], v[112:115]\n"
"v_mfma_f32_16x16x32_bf16 v[112:115], a[176:179], v[44:47], v[112:115]\n"
"v_mfma_f32_16x16x32_bf16 v[112:115], a[172:175], v[32:35], v[112:115]\n"
"v_mfma_f32_16x16x32_bf16 v[112:115], a[168:171], v[24:27], v[112:115]\n"
"s_nop 7\n"
"s_branch .Lmk_p69r\n"
".Lmk_p70s:\n"
";\n"
"s_waitcnt vmcnt(16)\n"
"s_mov_b32 s1, 0x10000\n"
".Lmk_p70l:\n"
"s_add_u32 s96, s58, 0x440000\n"
"buffer_load_dwordx4 v[36:39], v0, s[4:7], s96 offen sc1\n"
"buffer_load_dwordx4 v[40:43], v0, s[4:7], s96 offen offset:1024 sc1\n"
"buffer_load_dwordx4 v[52:55], v0, s[4:7], s96 offen offset:2048 sc1\n"
"buffer_load_dwordx4 v[60:63], v0, s[4:7], s96 offen offset:3072 sc1\n"
"buffer_load_dwordx4 v[64:67], v1, s[4:7], s96 offen sc1\n"
"buffer_load_dwordx4 v[68:71], v1, s[4:7], s96 offen offset:1024 sc1\n"
"buffer_load_dwordx4 v[72:75], v1, s[4:7], s96 offen offset:2048 sc1\n"
"buffer_load_dwordx4 v[76:79], v1, s[4:7], s96 offen offset:3072 sc1\n"
"s_add_u32 s96, s59, 0x440000\n"
"buffer_load_dwordx4 v[84:87], v0, s[4:7], s96 offen sc1\n"
"buffer_load_dwordx4 v[88:91], v0, s[4:7], s96 offen offset:1024 sc1\n"
"buffer_load_dwordx4 v[100:103], v0, s[4:7], s96 offen offset:2048 sc1\n"
"buffer_load_dwordx4 v[104:107], v0, s[4:7], s96 offen offset:3072 sc1\n"
"buffer_load_dwordx4 v[80:83], v1, s[4:7], s96 offen sc1\n"
"buffer_load_dwordx4 v[116:119], v1, s[4:7], s96 offen offset:1024 sc1\n"
"buffer_load_dwordx4 v[96:99], v1, s[4:7], s96 offen offset:2048 sc1\n"
"buffer_load_dwordx4 v[92:95], v1, s[4:7], s96 offen offset:3072 sc1\n"
"s_add_u32 s96, s56, 0x400000\n"
"buffer_load_dwordx4 v[120:123], v0, s[4:7], s96 offen sc1\n"
"buffer_load_dwordx4 v[124:127], v0, s[4:7], s96 offen offset:1024 sc1\n"
"buffer_load_dwordx4 v[108:111], v0, s[4:7], s96 offen offset:2048 sc1\n"
"buffer_load_dwordx4 v[132:135], v0, s[4:7], s96 offen offset:3072 sc1\n"
"buffer_load_dwordx4 v[112:115], v1, s[4:7], s96 offen sc1\n"
"buffer_load_dwordx4 v[28:31], v1, s[4:7], s96 offen offset:1024 sc1\n"
"buffer_load_dwordx4 v[32:35], v1, s[4:7], s96 offen offset:2048 sc1\n"
"buffer_load_dwordx4 v[24:27], v1, s[4:7], s96 offen offset:3072 sc1\n"
"s_add_u32 s96, s57, 0x400000\n"
"buffer_load_dwordx4 v[48:51], v0, s[4:7], s96 offen sc1\n"
"buffer_load_dwordx4 v[56:59], v0, s[4:7], s96 offen offset:1024 sc1\n"
"buffer_load_dwordx4 v[136:139], v0, s[4:7], s96 offen offset:2048 sc1\n"
"s_waitcnt vmcnt(0)\n"
"v_max_u32_e32 v239, v36, v37\n"
"v_max3_u32 v239, v239, v38, v39\n"
"v_max3_u32 v239, v239, v40, v41\n"
"v_max3_u32 v239, v239, v42, v43\n"
"v_max3_u32 v239, v239, v52, v53\n"
"v_max3_u32 v239, v239, v54, v55\n"
"v_max3_u32 v239, v239, v60, v61\n"
"v_max3_u32 v239, v239, v62, v63\n"
"v_max3_u32 v239, v239, v64, v65\n"
"v_max3_u32 v239, v239, v66, v67\n"
"v_max3_u32 v239, v239, v68, v69\n"
"v_max3_u32 v239, v239, v70, v71\n"
"v_max3_u32 v239, v239, v72, v73\n"
"v_max3_u32 v239, v239, v74, v75\n"
"v_max3_u32 v239, v239, v76, v77\n"
"v_max3_u32 v239, v239, v78, v79\n"
"v_cmp_eq_u32_e32 vcc, -1, v239\n"
"s_cbranch_vccz .Lmk_p70k\n"
"s_sub_u32 s1, s1, 1\n"
"s_cbranch_scc0 .Lmk_p70l\n"
".Lmk_p70k:\n"
"v_mfma_f32_16x16x32_bf16 v[44:47], a[128:131], v[36:39], 0\n"
"v_mfma_f32_16x16x32_bf16 v[44:47], a[132:135], v[40:43], v[44:47]\n"
"v_mfma_f32_16x16x32_bf16 v[44:47], a[136:139], v[52:55], v[44:47]\n"
"v_mfma_f32_16x16x32_bf16 v[44:47], a[140:143], v[60:63], v[44:47]\n"
"v_mfma_f32_16x16x32_bf16 v[44:47], a[144:147], v[64:67], v[44:47]\n"
"v_mfma_f32_16x16x32_bf16 v[44:47], a[148:151], v[68:71], v[44:47]\n"
"v_mfma_f32_16x16x32_bf16 v[44:47], a[152:155], v[72:75], v[44:47]\n"
"v_mfma_f32_16x16x32_bf16 v[44:47], a[156:159], v[76:79], v[44:47]\n"
"v_mfma_f32_16x16x32_bf16 v[128:131], a[188:191], v[76:79], 0\n"
"v_mfma_f32_16x16x32_bf16 v[128:131], a[184:187], v[72:75], v[128:131]\n"
"v_mfma_f32_16x16x32_bf16 v[128:131], a[180:183], v[68:71], v[128:131]\n"
"v_mfma_f32_16x16x32_bf16 v[128:131], a[176:179], v[64:67], v[128:131]\n"
"v_mfma_f32_16x16x32_bf16 v[128:131], a[172:175], v[60:63], v[128:131]\n"
"v_mfma_f32_16x16x32_bf16 v[128:131], a[168:171], v[52:55], v[128:131]\n"
"s_nop 7\n"
"s_branch .Lmk_p70r\n"
".Lmk_p71s:\n"
";\n"
"s_waitcnt vmcnt(16)\n"
"s_mov_b32 s1, 0x10000\n"
".Lmk_p71l:\n"
"s_add_u32 s96, s59, 0x440000\n"
"buffer_load_dwordx4 v[84:87], v0, s[4:7], s96 offen sc1\n"
"buffer_load_dwordx4 v[88:91], v0, s[4:7], s96 offen offset:1024 sc1\n"
"buffer_load_dwordx4 v[100:103], v0, s[4:7], s96 offen offset:2048 sc1\n"
"buffer_load_dwordx4 v[104:107], v0, s[4:7], s96 offen offset:3072 sc1\n"
"buffer_load_dwordx4 v[80:83], v1, s[4:7], s96 offen sc1\n"
"buffer_load_dwordx4 v[116:119], v1, s[4:7], s96 offen offset:1024 sc1\n"
"buffer_load_dwordx4 v[96:99], v1, s[4:7], s96 offen offset:2048 sc1\n"
"buffer_load_dwordx4 v[92:95], v1, s[4:7], s96 offen offset:3072 sc1\n"
"s_add_u32 s96, s56, 0x400000\n"
"buffer_load_dwordx4 v[120:123], v0, s[4:7], s96 offen sc1\n"
"buffer_load_dwordx4 v[124:127], v0, s[4:7], s96 offen offset:1024 sc1\n"
"buffer_load_dwordx4 v[108:111], v0, s[4:7], s96 offen offset:2048 sc1\n"
"buffer_load_dwordx4 v[132:135], v0, s[4:7], s96 offen offset:3072 sc1\n"
"buffer_load_dwordx4 v[112:115], v1, s[4:7], s96 offen sc1\n"
"buffer_load_dwordx4 v[28:31], v1, s[4:7], s96 offen offset:1024 sc1\n"
"buffer_load_dwordx4 v[32:35], v1, s[4:7], s96 offen offset:2048 sc1\n"
"buffer_load_dwordx4 v[24:27], v1, s[4:7], s96 offen offset:3072 sc1\n"
"s_add_u32 s96, s57, 0x400000\n"
"buffer_load_dwordx4 v[48:51], v0, s[4:7], s96 offen sc1\n"
"buffer_load_dwordx4 v[56:59], v0, s[4:7], s96 offen offset:1024 sc1\n"
"buffer_load_dwordx4 v[136:139], v0, s[4:7], s96 offen offset:2048 sc1\n"
"buffer_load_dwordx4 v[44:47], v0, s[4:7], s96 offen offset:3072 sc1\n"
"buffer_load_dwordx4 v[160:163], v1, s[4:7], s96 offen sc1\n"
"buffer_load_dwordx4 v[128:131], v1, s[4:7], s96 offen offset:1024 sc1\n"
"buffer_load_dwordx4 v[72:75], v1, s[4:7], s96 offen offset:2048 sc1\n"
"buffer_load_dwordx4 v[60:63], v1, s[4:7], s96 offen offset:3072 sc1\n"
"s_add_u32 s96, s58, 0x400000\n"
"buffer_load_dwordx4 v[36:39], v0, s[4:7], s96 offen sc1\n"
"buffer_load_dwordx4 v[68:71], v0, s[4:7], s96 offen offset:1024 sc1\n"
"buffer_load_dwordx4 v[76:79], v0, s[4:7], s96 offen offset:2048 sc1\n"
"buffer_load_dwordx4 v[148:151], v0, s[4:7], s96 offen offset:3072 sc1\n"
"s_waitcnt vmcnt(0)\n"
"v_max_u32_e32 v255, v84, v85\n"
"v_max3_u32 v255, v255, v86, v87\n"
"v_max3_u32 v255, v255, v88, v89\n"
"v_max3_u32 v255, v255, v90, v91\n"
"v_max3_u32 v255, v255, v100, v101\n"
"v_max3_u32 v255, v255, v102, v103\n"
"v_max3_u32 v255, v255, v104, v105\n"
"v_max3_u32 v255, v255, v106, v107\n"
"v_max3_u32 v255, v255, v80, v81\n"
"v_max3_u32 v255, v255, v82, v83\n"
"v_max3_u32 v255, v255, v116, v117\n"
"v_max3_u32 v255, v255, v118, v119\n"
"v_max3_u32 v255, v255, v96, v97\n"
"v_max3_u32 v255, v255, v98, v99\n"
"v_max3_u32 v255, v255, v92, v93\n"
"v_max3_u32 v255, v255, v94, v95\n"
"v_cmp_eq_u32_e32 vcc, -1, v255\n"
"s_cbranch_vccz .Lmk_p71k\n"
"s_sub_u32 s1, s1, 1\n"
"s_cbranch_scc0 .Lmk_p71l\n"
".Lmk_p71k:\n"
"v_mfma_f32_16x16x32_bf16 v[64:67], a[128:131], v[84:87], v[140:143]\n"
"v_mfma_f32_16x16x32_bf16 v[64:67], a[132:135], v[88:91], v[64:67]\n"
"v_mfma_f32_16x16x32_bf16 v[64:67], a[136:139], v[100:103], v[64:67]\n"
"v_mfma_f32_16x16x32_bf16 v[64:67], a[140:143], v[104:107], v[64:67]\n"
"v_mfma_f32_16x16x32_bf16 v[64:67], a[144:147], v[80:83], v[64:67]\n"
"v_mfma_f32_16x16x32_bf16 v[64:67], a[148:151], v[116:119], v[64:67]\n"
"v_mfma_f32_16x16x32_bf16 v[64:67], a[152:155], v[96:99], v[64:67]\n"
"v_mfma_f32_16x16x32_bf16 v[64:67], a[156:159], v[92:95], v[64:67]\n"
"v_mfma_f32_16x16x32_bf16 v[52:55], a[188:191], v[92:95], v[152:155]\n"
"v_mfma_f32_16x16x32_bf16 v[52:55], a[184:187], v[96:99], v[52:55]\n"
"v_mfma_f32_16x16x32_bf16 v[52:55], a[180:183], v[116:119], v[52:55]\n"
"v_mfma_f32_16x16x32_bf16 v[52:55], a[176:179], v[80:83], v[52:55]\n"
"v_mfma_f32_16x16x32_bf16 v[52:55], a[172:175], v[104:107], v[52:55]\n"
"v_mfma_f32_16x16x32_bf16 v[52:55], a[168:171], v[100:103], v[52:55]\n"
"s_nop 7\n"
"s_branch .Lmk_p71r\n"
".Lmk_p73s:\n"
";\n"
"s_waitcnt vmcnt(16)\n"
"s_mov_b32 s1, 0x10000\n"
".Lmk_p73l:\n"
"s_add_u32 s95, s56, 0x400000\n"
"buffer_load_dwordx4 v[120:123], v0, s[4:7], s95 offen sc1\n"
"buffer_load_dwordx4 v[124:127], v0, s[4:7], s95 offen offset:1024 sc1\n"
"buffer_load_dwordx4 v[108:111], v0, s[4:7], s95 offen offset:2048 sc1\n"
"buffer_load_dwordx4 v[132:135], v0, s[4:7], s95 offen offset:3072 sc1\n"
"buffer_load_dwordx4 v[112:115], v1, s[4:7], s95 offen sc1\n"
"buffer_load_dwordx4 v[28:31], v1, s[4:7], s95 offen offset:1024 sc1\n"
"buffer_load_dwordx4 v[32:35], v1, s[4:7], s95 offen offset:2048 sc1\n"
"buffer_load_dwordx4 v[24:27], v1, s[4:7], s95 offen offset:3072 sc1\n"
"s_add_u32 s95, s57, 0x400000\n"
"buffer_load_dwordx4 v[48:51], v0, s[4:7], s95 offen sc1\n"
"buffer_load_dwordx4 v[56:59], v0, s[4:7], s95 offen offset:1024 sc1\n"
"buffer_load_dwordx4 v[136:139], v0, s[4:7], s95 offen offset:2048 sc1\n"
"buffer_load_dwordx4 v[44:47], v0, s[4:7], s95 offen offset:3072 sc1\n"
"buffer_load_dwordx4 v[160:163], v1, s[4:7], s95 offen sc1\n"
"buffer_load_dwordx4 v[128:131], v1, s[4:7], s95 offen offset:1024 sc1\n"
"buffer_load_dwordx4 v[72:75], v1, s[4:7], s95 offen offset:2048 sc1\n"
"buffer_load_dwordx4 v[60:63], v1, s[4:7], s95 offen offset:3072 sc1\n"
"s_add_u32 s95, s58, 0x400000\n"
"buffer_load_dwordx4 v[36:39], v0, s[4:7], s95 offen sc1\n"
"buffer_load_dwordx4 v[68:71], v0, s[4:7], s95 offen offset:1024 sc1\n"
"buffer_load_dwordx4 v[76:79], v0, s[4:7], s95 offen offset:2048 sc1\n"
"buffer_load_dwordx4 v[148:151], v0, s[4:7], s95 offen offset:3072 sc1\n"
"buffer_load_dwordx4 v[152:155], v1, s[4:7], s95 offen sc1\n"
"buffer_load_dwordx4 v[172:175], v1, s[4:7], s95 offen offset:1024 sc1\n"
"buffer_load_dwordx4 v[176:179], v1, s[4:7], s95 offen offset:2048 sc1\n"
"buffer_load_dwordx4 v[64:67], v1, s[4:7], s95 offen offset:3072 sc1\n"
"s_add_u32 s95, s59, 0x400000\n"
"buffer_load_dwordx4 v[80:83], v0, s[4:7], s95 offen sc1\n"
"buffer_load_dwordx4 v[84:87], v0, s[4:7], s95 offen offset:1024 sc1\n"
"buffer_load_dwordx4 v[88:91], v0, s[4:7], s95 offen offset:2048 sc1\n"
"buffer_load_dwordx4 v[92:95], v0, s[4:7], s95 offen offset:3072 sc1\n"
"buffer_load_dwordx4 v[96:99], v1, s[4:7], s95 offen sc1\n"
"buffer_load_dwordx4 v[100:103], v1, s[4:7], s95 offen offset:1024 sc1\n"
"buffer_load_dwordx4 v[104:107], v1, s[4:7], s95 offen offset:2048 sc1\n"
"buffer_load_dwordx4 v[116:119], v1, s[4:7], s95 offen offset:3072 sc1\n"
"s_waitcnt vmcnt(0)\n"
"v_max_u32_e32 v255, v120, v121\n"
"v_max3_u32 v255, v255, v122, v123\n"
"v_max3_u32 v255, v255, v124, v125\n"
"v_max3_u32 v255, v255, v126, v127\n"
"v_max3_u32 v255, v255, v108, v109\n"
"v_max3_u32 v255, v255, v110, v111\n"
"v_max3_u32 v255, v255, v132, v133\n"
"v_max3_u32 v255, v255, v134, v135\n"
"v_max3_u32 v255, v255, v112, v113\n"
"v_max3_u32 v255, v255, v114, v115\n"
"v_max3_u32 v255, v255, v28, v29\n"
"v_max3_u32 v255, v255, v30, v31\n"
"v_max3_u32 v255, v255, v32, v33\n"
"v_max3_u32 v255, v255, v34, v35\n"
"v_max3_u32 v255, v255, v24, v25\n"
"v_max3_u32 v255, v255, v26, v27\n"
"v_cmp_eq_u32_e32 vcc, -1, v255\n"
"s_cbranch_vccz .Lmk_p73k\n"
"s_sub_u32 s1, s1, 1\n"
"s_cbranch_scc0 .Lmk_p73l\n"
".Lmk_p73k:\n"
"v_mfma_f32_16x16x32_bf16 v[140:143], a[128:131], v[120:123], 0\n"
"v_mfma_f32_16x16x32_bf16 v[140:143], a[132:135], v[124:127], v[140:143]\n"
"v_mfma_f32_16x16x32_bf16 v[140:143], a[136:139], v[108:111], v[140:143]\n"
"v_mfma_f32_16x16x32_bf16 v[140:143], a[140:143], v[132:135], v[140:143]\n"
"v_mfma_f32_16x16x32_bf16 v[140:143], a[144:147], v[112:115], v[140:143]\n"
"v_mfma_f32_16x16x32_bf16 v[140:143], a[148:151], v[28:31], v[140:143]\n"
"v_mfma_f32_16x16x32_bf16 v[140:143], a[152:155], v[32:35], v[140:143]\n"
"v_mfma_f32_16x16x32_bf16 v[140:143], a[156:159], v[24:27], v[140:143]\n"
"v_mfma_f32_16x16x32_bf16 v[40:43], a[188:191], v[24:27], 0\n"
"v_mfma_f32_16x16x32_bf16 v[40:43], a[184:187], v[32:35], v[40:43]\n"
"v_mfma_f32_16x16x32_bf16 v[40:43], a[180:183], v[28:31], v[40:43]\n"
"v_mfma_f32_16x16x32_bf16 v[40:43], a[176:179], v[112:115], v[40:43]\n"
"v_mfma_f32_16x16x32_bf16 v[40:43], a[172:175], v[132:135], v[40:43]\n"
"v_mfma_f32_16x16x32_bf16 v[40:43], a[168:171], v[108:111], v[40:43]\n"
"s_nop 7\n"
"s_branch .Lmk_p73r\n"
".Lmk_p74s:\n"
";\n"
"s_waitcnt vmcnt(16)\n"
"s_mov_b32 s1, 0x10000\n"
".Lmk_p74l:\n"
"s_add_u32 s97, s57, 0x400000\n"
"buffer_load_dwordx4 v[48:51], v0, s[4:7], s97 offen sc1\n"
"buffer_load_dwordx4 v[56:59], v0, s[4:7], s97 offen offset:1024 sc1\n"
"buffer_load_dwordx4 v[136:139], v0, s[4:7], s97 offen offset:2048 sc1\n"
"buffer_load_dwordx4 v[44:47], v0, s[4:7], s97 offen offset:3072 sc1\n"
"buffer_load_dwordx4 v[160:163], v1, s[4:7], s97 offen sc1\n"
"buffer_load_dwordx4 v[128:131], v1, s[4:7], s97 offen offset:1024 sc1\n"
"buffer_load_dwordx4 v[72:75], v1, s[4:7], s97 offen offset:2048 sc1\n"
"buffer_load_dwordx4 v[60:63], v1, s[4:7], s97 offen offset:3072 sc1\n"
"s_add_u32 s97, s58, 0x400000\n"
"buffer_load_dwordx4 v[36:39], v0, s[4:7], s97 offen sc1\n"
"buffer_load_dwordx4 v[68:71], v0, s[4:7], s97 offen offset:1024 sc1\n"
"buffer_load_dwordx4 v[76:79], v0, s[4:7], s97 offen offset:2048 sc1\n"
"buffer_load_dwordx4 v[148:151], v0, s[4:7], s97 offen offset:3072 sc1\n"
"buffer_load_dwordx4 v[152:155], v1, s[4:7], s97 offen sc1\n"
"buffer_load_dwordx4 v[172:175], v1, s[4:7], s97 offen offset:1024 sc1\n"
"buffer_load_dwordx4 v[176:179], v1, s[4:7], s97 offen offset:2048 sc1\n"
"buffer_load_dwordx4 v[64:67], v1, s[4:7], s97 offen offset:3072 sc1\n"
"s_add_u32 s97, s59, 0x400000\n"
"buffer_load_dwordx4 v[80:83], v0, s[4:7], s97 offen sc1\n"
"buffer_load_dwordx4 v[84:87], v0, s[4:7], s97 offen offset:1024 sc1\n"
"buffer_load_dwordx4 v[88:91], v0, s[4:7], s97 offen offset:2048 sc1\n"
"buffer_load_dwordx4 v[92:95], v0, s[4:7], s97 offen offset:3072 sc1\n"
"buffer_load_dwordx4 v[96:99], v1, s[4:7], s97 offen sc1\n"
"buffer_load_dwordx4 v[100:103], v1, s[4:7], s97 offen offset:1024 sc1\n"
"buffer_load_dwordx4 v[104:107], v1, s[4:7], s97 offen offset:2048 sc1\n"
"buffer_load_dwordx4 v[116:119], v1, s[4:7], s97 offen offset:3072 sc1\n"
"s_waitcnt vmcnt(0)\n"
"v_max_u32_e32 v255, v48, v49\n"
"v_max3_u32 v255, v255, v50, v51\n"
"v_max3_u32 v255, v255, v56, v57\n"
"v_max3_u32 v255, v255, v58, v59\n"
"v_max3_u32 v255, v255, v136, v137\n"
"v_max3_u32 v255, v255, v138, v139\n"
"v_max3_u32 v255, v255, v44, v45\n"
"v_max3_u32 v255, v255, v46, v47\n"
"v_max3_u32 v255, v255, v160, v161\n"
"v_max3_u32 v255, v255, v162, v163\n"
"v_max3_u32 v255, v255, v128, v129\n"
"v_max3_u32 v255, v255, v130, v131\n"
"v_max3_u32 v255, v255, v72, v73\n"
"v_max3_u32 v255, v255, v74, v75\n"
"v_max3_u32 v255, v255, v60, v61\n"
"v_max3_u32 v255, v255, v62, v63\n"
"v_cmp_eq_u32_e32 vcc, -1, v255\n"
"s_cbranch_vccz .Lmk_p74k\n"
"s_sub_u32 s1, s1, 1\n"
"s_cbranch_scc0 .Lmk_p74l\n"
".Lmk_p74k:\n"
"v_mfma_f32_16x16x32_bf16 v[24:27], a[128:131], v[48:51], 0\n"
"v_mfma_f32_16x16x32_bf16 v[24:27], a[132:135], v[56:59], v[24:27]\n"
"v_mfma_f32_16x16x32_bf16 v[24:27], a[136:139], v[136:139], v[24:27]\n"
"v_mfma_f32_16x16x32_bf16 v[24:27], a[140:143], v[44:47], v[24:27]\n"
"v_mfma_f32_16x16x32_bf16 v[24:27], a[144:147], v[160:163], v[24:27]\n"
"v_mfma_f32_16x16x32_bf16 v[24:27], a[148:151], v[128:131], v[24:27]\n"
"v_mfma_f32_16x16x32_bf16 v[24:27], a[152:155], v[72:75], v[24:27]\n"
"v_mfma_f32_16x16x32_bf16 v[24:27], a[156:159], v[60:63], v[24:27]\n"
"v_mfma_f32_16x16x32_bf16 v[28:31], a[188:191], v[60:63], 0\n"
"v_mfma_f32_16x16x32_bf16 v[28:31], a[184:187], v[72:75], v[28:31]\n"
"v_mfma_f32_16x16x32_bf16 v[28:31], a[180:183], v[128:131], v[28:31]\n"
"v_mfma_f32_16x16x32_bf16 v[28:31], a[176:179], v[160:163], v[28:31]\n"
"v_mfma_f32_16x16x32_bf16 v[28:31], a[172:175], v[44:47], v[28:31]\n"
"v_mfma_f32_16x16x32_bf16 v[28:31], a[168:171], v[136:139], v[28:31]\n"
"s_nop 7\n"
"s_branch .Lmk_p74r\n"
".Lmk_p75s:\n"
";\n"
"s_waitcnt vmcnt(16)\n"
"s_mov_b32 s1, 0x10000\n"
".Lmk_p75l:\n"
"s_add_u32 s97, s58, 0x400000\n"
"buffer_load_dwordx4 v[36:39], v0, s[4:7], s97 offen sc1\n"
"buffer_load_dwordx4 v[68:71], v0, s[4:7], s97 offen offset:1024 sc1\n"
"buffer_load_dwordx4 v[76:79], v0, s[4:7], s97 offen offset:2048 sc1\n"
"buffer_load_dwordx4 v[148:151], v0, s[4:7], s97 offen offset:3072 sc1\n"
"buffer_load_dwordx4 v[152:155], v1, s[4:7], s97 offen sc1\n"
"buffer_load_dwordx4 v[172:175], v1, s[4:7], s97 offen offset:1024 sc1\n"
"buffer_load_dwordx4 v[176:179], v1, s[4:7], s97 offen offset:2048 sc1\n"
"buffer_load_dwordx4 v[64:67], v1, s[4:7], s97 offen offset:3072 sc1\n"
"s_add_u32 s97, s59, 0x400000\n"
"buffer_load_dwordx4 v[80:83], v0, s[4:7], s97 offen sc1\n"
"buffer_load_dwordx4 v[84:87], v0, s[4:7], s97 offen offset:1024 sc1\n"
"buffer_load_dwordx4 v[88:91], v0, s[4:7], s97 offen offset:2048 sc1\n"
"buffer_load_dwordx4 v[92:95], v0, s[4:7], s97 offen offset:3072 sc1\n"
"buffer_load_dwordx4 v[96:99], v1, s[4:7], s97 offen sc1\n"
"buffer_load_dwordx4 v[100:103], v1, s[4:7], s97 offen offset:1024 sc1\n"
"buffer_load_dwordx4 v[104:107], v1, s[4:7], s97 offen offset:2048 sc1\n"
"buffer_load_dwordx4 v[116:119], v1, s[4:7], s97 offen offset:3072 sc1\n"
"s_waitcnt vmcnt(0)\n"
"v_max_u32_e32 v239, v36, v37\n"
"v_max3_u32 v239, v239, v38, v39\n"
"v_max3_u32 v239, v239, v68, v69\n"
"v_max3_u32 v239, v239, v70, v71\n"
"v_max3_u32 v239, v239, v76, v77\n"
"v_max3_u32 v239, v239, v78, v79\n"
"v_max3_u32 v239, v239, v148, v149\n"
"v_max3_u32 v239, v239, v150, v151\n"
"v_max3_u32 v239, v239, v152, v153\n"
"v_max3_u32 v239, v239, v154, v155\n"
"v_max3_u32 v239, v239, v172, v173\n"
"v_max3_u32 v239, v239, v174, v175\n"
"v_max3_u32 v239, v239, v176, v177\n"
"v_max3_u32 v239, v239, v178, v179\n"
"v_max3_u32 v239, v239, v64, v65\n"
"v_max3_u32 v239, v239, v66, v67\n"
"v_cmp_eq_u32_e32 vcc, -1, v239\n"
"s_cbranch_vccz .Lmk_p75k\n"
"s_sub_u32 s1, s1, 1\n"
"s_cbranch_scc0 .Lmk_p75l\n"
".Lmk_p75k:\n"
"v_mfma_f32_16x16x32_bf16 v[24:27], a[128:131], v[36:39], 0\n"
"v_mfma_f32_16x16x32_bf16 v[24:27], a[132:135], v[68:71], v[24:27]\n"
"v_mfma_f32_16x16x32_bf16 v[24:27], a[136:139], v[76:79], v[24:27]\n"
"v_mfma_f32_16x16x32_bf16 v[24:27], a[140:143], v[148:151], v[24:27]\n"
"v_mfma_f32_16x16x32_bf16 v[24:27], a[144:147], v[152:155], v[24:27]\n"
"v_mfma_f32_16x16x32_bf16 v[24:27], a[148:151], v[172:175], v[24:27]\n"
"v_mfma_f32_16x16x32_bf16 v[24:27], a[152:155], v[176:179], v[24:27]\n"
"v_mfma_f32_16x16x32_bf16 v[24:27], a[156:159], v[64:67], v[24:27]\n"
"v_mfma_f32_16x16x32_bf16 v[32:35], a[188:191], v[64:67], 0\n"
"v_mfma_f32_16x16x32_bf16 v[32:35], a[184:187], v[176:179], v[32:35]\n"
"v_mfma_f32_16x16x32_bf16 v[32:35], a[180:183], v[172:175], v[32:35]\n"
"v_mfma_f32_16x16x32_bf16 v[32:35], a[176:179], v[152:155], v[32:35]\n"
"v_mfma_f32_16x16x32_bf16 v[32:35], a[172:175], v[148:151], v[32:35]\n"
"v_mfma_f32_16x16x32_bf16 v[32:35], a[168:171], v[76:79], v[32:35]\n"
"s_nop 7\n"
"s_branch .Lmk_p75r\n"
".Lmk_p76s:\n"
";\n"
"s_waitcnt vmcnt(16)\n"
"s_mov_b32 s1, 0x10000\n"
".Lmk_p76l:\n"
"s_add_u32 s97, s59, 0x400000\n"
"buffer_load_dwordx4 v[80:83], v0, s[4:7], s97 offen sc1\n"
"buffer_load_dwordx4 v[84:87], v0, s[4:7], s97 offen offset:1024 sc1\n"
"buffer_load_dwordx4 v[88:91], v0, s[4:7], s97 offen offset:2048 sc1\n"
"buffer_load_dwordx4 v[92:95], v0, s[4:7], s97 offen offset:3072 sc1\n"
"buffer_load_dwordx4 v[96:99], v1, s[4:7], s97 offen sc1\n"
"buffer_load_dwordx4 v[100:103], v1, s[4:7], s97 offen offset:1024 sc1\n"
"buffer_load_dwordx4 v[104:107], v1, s[4:7], s97 offen offset:2048 sc1\n"
"buffer_load_dwordx4 v[116:119], v1, s[4:7], s97 offen offset:3072 sc1\n"
"s_waitcnt vmcnt(0)\n"
"v_max_u32_e32 v255, v80, v81\n"
"v_max3_u32 v255, v255, v82, v83\n"
"v_max3_u32 v255, v255, v84, v85\n"
"v_max3_u32 v255, v255, v86, v87\n"
"v_max3_u32 v255, v255, v88, v89\n"
"v_max3_u32 v255, v255, v90, v91\n"
"v_max3_u32 v255, v255, v92, v93\n"
"v_max3_u32 v255, v255, v94, v95\n"
"v_max3_u32 v255, v255, v96, v97\n"
"v_max3_u32 v255, v255, v98, v99\n"
"v_max3_u32 v255, v255, v100, v101\n"
"v_max3_u32 v255, v255, v102, v103\n"
"v_max3_u32 v255, v255, v104, v105\n"
"v_max3_u32 v255, v255, v106, v107\n"
"v_max3_u32 v255, v255, v116, v117\n"
"v_max3_u32 v255, v255, v118, v119\n"
"v_cmp_eq_u32_e32 vcc, -1, v255\n"
"s_cbranch_vccz .Lmk_p76k\n"
"s_sub_u32 s1, s1, 1\n"
"s_cbranch_scc0 .Lmk_p76l\n"
".Lmk_p76k:\n"
"v_mfma_f32_16x16x32_bf16 v[76:79], a[128:131], v[80:83], v[44:47]\n"
"v_mfma_f32_16x16x32_bf16 v[76:79], a[132:135], v[84:87], v[76:79]\n"
"v_mfma_f32_16x16x32_bf16 v[76:79], a[136:139], v[88:91], v[76:79]\n"
"v_mfma_f32_16x16x32_bf16 v[76:79], a[140:143], v[92:95], v[76:79]\n"
"v_mfma_f32_16x16x32_bf16 v[76:79], a[144:147], v[96:99], v[76:79]\n"
"v_mfma_f32_16x16x32_bf16 v[76:79], a[148:151], v[100:103], v[76:79]\n"
"v_mfma_f32_16x16x32_bf16 v[76:79], a[152:155], v[104:107], v[76:79]\n"
"v_mfma_f32_16x16x32_bf16 v[76:79], a[156:159], v[116:119], v[76:79]\n"
"v_mfma_f32_16x16x32_bf16 v[68:71], a[188:191], v[116:119], v[56:59]\n"
"v_mfma_f32_16x16x32_bf16 v[68:71], a[184:187], v[104:107], v[68:71]\n"
"v_mfma_f32_16x16x32_bf16 v[68:71], a[180:183], v[100:103], v[68:71]\n"
"v_mfma_f32_16x16x32_bf16 v[68:71], a[176:179], v[96:99], v[68:71]\n"
"v_mfma_f32_16x16x32_bf16 v[68:71], a[172:175], v[92:95], v[68:71]\n"
"v_mfma_f32_16x16x32_bf16 v[68:71], a[168:171], v[88:91], v[68:71]\n"
"s_nop 7\n"
"s_branch .Lmk_p76r\n"
".Lmk_p81s:\n"
";\n"
"s_waitcnt vmcnt(16)\n"
"s_mov_b32 s1, 0x10000\n"
".Lmk_p81l:\n"
"s_add_u32 s96, s52, 0x40000\n"
"buffer_load_dwordx4 v[96:99], v0, s[4:7], s96 offen sc1\n"
"buffer_load_dwordx4 v[88:91], v0, s[4:7], s96 offen offset:1024 sc1\n"
"buffer_load_dwordx4 v[80:83], v0, s[4:7], s96 offen offset:2048 sc1\n"
"buffer_load_dwordx4 v[84:87], v0, s[4:7], s96 offen offset:3072 sc1\n"
"buffer_load_dwordx4 v[92:95], v1, s[4:7], s96 offen sc1\n"
"buffer_load_dwordx4 v[104:107], v1, s[4:7], s96 offen offset:1024 sc1\n"
"buffer_load_dwordx4 v[116:119], v1, s[4:7], s96 offen offset:2048 sc1\n"
"buffer_load_dwordx4 v[124:127], v1, s[4:7], s96 offen offset:3072 sc1\n"
"s_add_u32 s96, s53, 0x40000\n"
"buffer_load_dwordx4 v[128:131], v0, s[4:7], s96 offen sc1\n"
"buffer_load_dwordx4 v[144:147], v0, s[4:7], s96 offen offset:1024 sc1\n"
"buffer_load_dwordx4 v[148:151], v0, s[4:7], s96 offen offset:2048 sc1\n"
"buffer_load_dwordx4 v[152:155], v0, s[4:7], s96 offen offset:3072 sc1\n"
"buffer_load_dwordx4 v[156:159], v1, s[4:7], s96 offen sc1\n"
"buffer_load_dwordx4 v[160:163], v1, s[4:7], s96 offen offset:1024 sc1\n"
"buffer_load_dwordx4 v[164:167], v1, s[4:7], s96 offen offset:2048 sc1\n"
"buffer_load_dwordx4 v[168:171], v1, s[4:7], s96 offen offset:3072 sc1\n"
"s_add_u32 s96, s54, 0x40000\n"
"buffer_load_dwordx4 v[176:179], v0, s[4:7], s96 offen sc1\n"
"buffer_load_dwordx4 v[48:51], v0, s[4:7], s96 offen offset:1024 sc1\n"
"s_waitcnt vmcnt(0)\n"
"v_max_u32_e32 v252, v96, v97\n"
"v_max3_u32 v252, v252, v98, v99\n"
"v_max3_u32 v252, v252, v88, v89\n"
"v_max3_u32 v252, v252, v90, v91\n"
"v_max3_u32 v252, v252, v80, v81\n"
"v_max3_u32 v252, v252, v82, v83\n"
"v_max3_u32 v252, v252, v84, v85\n"
"v_max3_u32 v252, v252, v86, v87\n"
"v_max3_u32 v252, v252, v92, v93\n"
"v_max3_u32 v252, v252, v94, v95\n"
"v_max3_u32 v252, v252, v104, v105\n"
"v_max3_u32 v252, v252, v106, v107\n"
"v_max3_u32 v252, v252, v116, v117\n"
"v_max3_u32 v252, v252, v118, v119\n"
"v_max3_u32 v252, v252, v124, v125\n"
"v_max3_u32 v252, v252, v126, v127\n"
"v_cmp_eq_u32_e32 vcc, -1, v252\n"
"s_cbranch_vccz .Lmk_p81k\n"
"s_sub_u32 s1, s1, 1\n"
"s_cbranch_scc0 .Lmk_p81l\n"
".Lmk_p81k:\n"
"v_mfma_f32_16x16x32_bf16 v[172:175], a[0:3], v[96:99], 0\n"
"v_mfma_f32_16x16x32_bf16 v[172:175], a[4:7], v[88:91], v[172:175]\n"
"v_mfma_f32_16x16x32_bf16 v[172:175], a[8:11], v[80:83], v[172:175]\n"
"v_mfma_f32_16x16x32_bf16 v[172:175], a[12:15], v[84:87], v[172:175]\n"
"v_mfma_f32_16x16x32_bf16 v[172:175], a[16:19], v[92:95], v[172:175]\n"
"v_mfma_f32_16x16x32_bf16 v[172:175], a[20:23], v[104:107], v[172:175]\n"
"v_mfma_f32_16x16x32_bf16 v[172:175], a[24:27], v[116:119], v[172:175]\n"
"v_mfma_f32_16x16x32_bf16 v[172:175], a[28:31], v[124:127], v[172:175]\n"
"v_mfma_f32_16x16x32_bf16 v[180:183], a[60:63], v[124:127], 0\n"
"v_mfma_f32_16x16x32_bf16 v[180:183], a[56:59], v[116:119], v[180:183]\n"
"v_mfma_f32_16x16x32_bf16 v[180:183], a[52:55], v[104:107], v[180:183]\n"
"v_mfma_f32_16x16x32_bf16 v[180:183], a[48:51], v[92:95], v[180:183]\n"
"v_mfma_f32_16x16x32_bf16 v[180:183], a[44:47], v[84:87], v[180:183]\n"
"v_mfma_f32_16x16x32_bf16 v[180:183], a[40:43], v[80:83], v[180:183]\n"
"s_nop 7\n"
"s_branch .Lmk_p81r\n"
".Lmk_p82s:\n"
";\n"
"s_waitcnt vmcnt(16)\n"
"s_mov_b32 s1, 0x10000\n"
".Lmk_p82l:\n"
"s_add_u32 s97, s53, 0x40000\n"
"buffer_load_dwordx4 v[128:131], v0, s[4:7], s97 offen sc1\n"
"buffer_load_dwordx4 v[144:147], v0, s[4:7], s97 offen offset:1024 sc1\n"
"buffer_load_dwordx4 v[148:151], v0, s[4:7], s97 offen offset:2048 sc1\n"
"buffer_load_dwordx4 v[152:155], v0, s[4:7], s97 offen offset:3072 sc1\n"
"buffer_load_dwordx4 v[156:159], v1, s[4:7], s97 offen sc1\n"
"buffer_load_dwordx4 v[160:163], v1, s[4:7], s97 offen offset:1024 sc1\n"
"buffer_load_dwordx4 v[164:167], v1, s[4:7], s97 offen offset:2048 sc1\n"
"buffer_load_dwordx4 v[168:171], v1, s[4:7], s97 offen offset:3072 sc1\n"
"s_add_u32 s97, s54, 0x40000\n"
"buffer_load_dwordx4 v[176:179], v0, s[4:7], s97 offen sc1\n"
"buffer_load_dwordx4 v[48:51], v0, s[4:7], s97 offen offset:1024 sc1\n"
"buffer_load_dwordx4 v[132:135], v0, s[4:7], s97 offen offset:2048 sc1\n"
"buffer_load_dwordx4 v[184:187], v0, s[4:7], s97 offen offset:3072 sc1\n"
"buffer_load_dwordx4 v[44:47], v1, s[4:7], s97 offen sc1\n"
"buffer_load_dwordx4 v[192:195], v1, s[4:7], s97 offen offset:1024 sc1\n"
"buffer_load_dwordx4 v[196:199], v1, s[4:7], s97 offen offset:2048 sc1\n"
"buffer_load_dwordx4 v[200:203], v1, s[4:7], s97 offen offset:3072 sc1\n"
"s_add_u32 s97, s55, 0x40000\n"
"buffer_load_dwordx4 v[80:83], v0, s[4:7], s97 offen sc1\n"
"buffer_load_dwordx4 v[84:87], v0, s[4:7], s97 offen offset:1024 sc1\n"
"s_waitcnt vmcnt(0)\n"
"v_max_u32_e32 v247, v128, v129\n"
"v_max3_u32 v247, v247, v130, v131\n"
"v_max3_u32 v247, v247, v144, v145\n"
"v_max3_u32 v247, v247, v146, v147\n"
"v_max3_u32 v247, v247, v148, v149\n"
"v_max3_u32 v247, v247, v150, v151\n"
"v_max3_u32 v247, v247, v152, v153\n"
"v_max3_u32 v247, v247, v154, v155\n"
"v_max3_u32 v247, v247, v156, v157\n"
"v_max3_u32 v247, v247, v158, v159\n"
"v_max3_u32 v247, v247, v160, v161\n"
"v_max3_u32 v247, v247, v162, v163\n"
"v_max3_u32 v247, v247, v164, v165\n"
"v_max3_u32 v247, v247, v166, v167\n"
"v_max3_u32 v247, v247, v168, v169\n"
"v_max3_u32 v247, v247, v170, v171\n"
"v_cmp_eq_u32_e32 vcc, -1, v247\n"
"s_cbranch_vccz .Lmk_p82k\n"
"s_sub_u32 s1, s1, 1\n"
"s_cbranch_scc0 .Lmk_p82l\n"
".Lmk_p82k:\n"
"v_mfma_f32_16x16x32_bf16 v[68:71], a[0:3], v[128:131], 0\n"
"v_mfma_f32_16x16x32_bf16 v[68:71], a[4:7], v[144:147], v[68:71]\n"
"v_mfma_f32_16x16x32_bf16 v[68:71], a[8:11], v[148:151], v[68:71]\n"
"v_mfma_f32_16x16x32_bf16 v[68:71], a[12:15], v[152:155], v[68:71]\n"
"v_mfma_f32_16x16x32_bf16 v[68:71], a[16:19], v[156:159], v[68:71]\n"
"v_mfma_f32_16x16x32_bf16 v[68:71], a[20:23], v[160:163], v[68:71]\n"
"v_mfma_f32_16x16x32_bf16 v[68:71], a[24:27], v[164:167], v[68:71]\n"
"v_mfma_f32_16x16x32_bf16 v[68:71], a[28:31], v[168:171], v[68:71]\n"
"v_mfma_f32_16x16x32_bf16 v[88:91], a[60:63], v[168:171], 0\n"
"v_mfma_f32_16x16x32_bf16 v[88:91], a[56:59], v[164:167], v[88:91]\n"
"v_mfma_f32_16x16x32_bf16 v[88:91], a[52:55], v[160:163], v[88:91]\n"
"v_mfma_f32_16x16x32_bf16 v[88:91], a[48:51], v[156:159], v[88:91]\n"
"v_mfma_f32_16x16x32_bf16 v[88:91], a[44:47], v[152:155], v[88:91]\n"
"v_mfma_f32_16x16x32_bf16 v[88:91], a[40:43], v[148:151], v[88:91]\n"
"s_nop 7\n"
"s_branch .Lmk_p82r\n"
".Lmk_m85s:\n"
";\n"
"s_waitcnt vmcnt(16)\n"
"s_mov_b32 s1, 0x10000\n"
".Lmk_m85l:\n"
"s_add_u32 s98, s62, 0x4000\n"
"buffer_load_dword v255, v10, s[4:7], s98 offen sc1\n"
"buffer_load_dword v254, v10, s[4:7], s98 offen offset:256 sc1\n"
"buffer_load_dword v253, v10, s[4:7], s98 offen offset:512 sc1\n"
"buffer_load_dword v252, v10, s[4:7], s98 offen offset:768 sc1\n"
"buffer_load_dword v251, v10, s[4:7], s98 offen offset:1024 sc1\n"
"buffer_load_dword v250, v10, s[4:7], s98 offen offset:1280 sc1\n"
"buffer_load_dword v249, v10, s[4:7], s98 offen offset:1536 sc1\n"
"buffer_load_dword v248, v10, s[4:7], s98 offen offset:1792 sc1\n"
"s_waitcnt vmcnt(0)\n"
"v_max_u32_e32 v247, v255, v254\n"
"v_max3_u32 v247, v247, v253, v252\n"
"v_max3_u32 v247, v247, v251, v250\n"
"v_max3_u32 v247, v247, v249, v248\n"
"v_cmp_eq_u32_e32 vcc, -1, v247\n"
"s_cbranch_vccz .Lmk_m85k\n"
"s_sub_u32 s1, s1, 1\n"
"s_cbranch_scc0 .Lmk_m85l\n"
".Lmk_m85k:\n"
"v_add_f32_e32 v255, v255, v251\n"
"v_add_f32_e32 v254, v254, v250\n"
"v_add_f32_e32 v253, v253, v249\n"
"v_add_f32_e32 v252, v252, v248\n"
"v_add_f32_e32 v255, v255, v253\n"
"v_add_f32_e32 v254, v254, v252\n"
"v_add_f32_e32 v255, v255, v254\n"
"s_nop 1\n"
"s_branch .Lmk_m85r\n"
".Lmk_p83s:\n"
";\n"
"s_waitcnt vmcnt(16)\n"
"s_mov_b32 s1, 0x10000\n"
".Lmk_p83l:\n"
"s_add_u32 s97, s54, 0x40000\n"
"buffer_load_dwordx4 v[176:179], v0, s[4:7], s97 offen sc1\n"
"buffer_load_dwordx4 v[48:51], v0, s[4:7], s97 offen offset:1024 sc1\n"
"buffer_load_dwordx4 v[132:135], v0, s[4:7], s97 offen offset:2048 sc1\n"
"buffer_load_dwordx4 v[184:187], v0, s[4:7], s97 offen offset:3072 sc1\n"
"buffer_load_dwordx4 v[44:47], v1, s[4:7], s97 offen sc1\n"
"buffer_load_dwordx4 v[192:195], v1, s[4:7], s97 offen offset:1024 sc1\n"
"buffer_load_dwordx4 v[196:199], v1, s[4:7], s97 offen offset:2048 sc1\n"
"buffer_load_dwordx4 v[200:203], v1, s[4:7], s97 offen offset:3072 sc1\n"
"s_add_u32 s97, s55, 0x40000\n"
"buffer_load_dwordx4 v[80:83], v0, s[4:7], s97 offen sc1\n"
"buffer_load_dwordx4 v[84:87], v0, s[4:7], s97 offen offset:1024 sc1\n"
"buffer_load_dwordx4 v[92:95], v0, s[4:7], s97 offen offset:2048 sc1\n"
"buffer_load_dwordx4 v[96:99], v0, s[4:7], s97 offen offset:3072 sc1\n"
"buffer_load_dwordx4 v[88:91], v1, s[4:7], s97 offen sc1\n"
"buffer_load_dwordx4 v[116:119], v1, s[4:7], s97 offen offset:1024 sc1\n"
"buffer_load_dwordx4 v[124:127], v1, s[4:7], s97 offen offset:2048 sc1\n"
"buffer_load_dwordx4 v[172:175], v1, s[4:7], s97 offen offset:3072 sc1\n"
"s_add_u32 s97, s52, 0x48000\n"
"buffer_load_dwordx4 v[128:131], v0, s[4:7], s97 offen sc1\n"
"s_waitcnt vmcnt(0)\n"
"v_max_u32_e32 v252, v176, v177\n"
"v_max3_u32 v252, v252, v178, v179\n"
"v_max3_u32 v252, v252, v48, v49\n"
"v_max3_u32 v252, v252, v50, v51\n"
"v_max3_u32 v252, v252, v132, v133\n"
"v_max3_u32 v252, v252, v134, v135\n"
"v_max3_u32 v252, v252, v184, v185\n"
"v_max3_u32 v252, v252, v186, v187\n"
"v_max3_u32 v252, v252, v44, v45\n"
"v_max3_u32 v252, v252, v46, v47\n"
"v_max3_u32 v252, v252, v192, v193\n"
"v_max3_u32 v252, v252, v194, v195\n"
"v_max3_u32 v252, v252, v196, v197\n"
"v_max3_u32 v252, v252, v198, v199\n"
"v_max3_u32 v252, v252, v200, v201\n"
"v_max3_u32 v252, v252, v202, v203\n"
"v_cmp_eq_u32_e32 vcc, -1, v252\n"
"s_cbranch_vccz .Lmk_p83k\n"
"s_sub_u32 s1, s1, 1\n"
"s_cbranch_scc0 .Lmk_p83l\n"
".Lmk_p83k:\n"
"v_mfma_f32_16x16x32_bf16 v[68:71], a[0:3], v[176:179], 0\n"
"v_mfma_f32_16x16x32_bf16 v[68:71], a[4:7], v[48:51], v[68:71]\n"
"v_mfma_f32_16x16x32_bf16 v[68:71], a[8:11], v[132:135], v[68:71]\n"
"v_mfma_f32_16x16x32_bf16 v[68:71], a[12:15], v[184:187], v[68:71]\n"
"v_mfma_f32_16x16x32_bf16 v[68:71], a[16:19], v[44:47], v[68:71]\n"
"v_mfma_f32_16x16x32_bf16 v[68:71], a[20:23], v[192:195], v[68:71]\n"
"v_mfma_f32_16x16x32_bf16 v[68:71], a[24:27], v[196:199], v[68:71]\n"
"v_mfma_f32_16x16x32_bf16 v[68:71], a[28:31], v[200:203], v[68:71]\n"
"v_mfma_f32_16x16x32_bf16 v[104:107], a[60:63], v[200:203], 0\n"
"v_mfma_f32_16x16x32_bf16 v[104:107], a[56:59], v[196:199], v[104:107]\n"
"v_mfma_f32_16x16x32_bf16 v[104:107], a[52:55], v[192:195], v[104:107]\n"
"v_mfma_f32_16x16x32_bf16 v[104:107], a[48:51], v[44:47], v[104:107]\n"
"v_mfma_f32_16x16x32_bf16 v[104:107], a[44:47], v[184:187], v[104:107]\n"
"v_mfma_f32_16x16x32_bf16 v[104:107], a[40:43], v[132:135], v[104:107]\n"
"s_nop 7\n"
"s_branch .Lmk_p83r\n"
".Lmk_p84s:\n"
";\n"
"s_waitcnt vmcnt(16)\n"
"s_mov_b32 s1, 0x10000\n"
".Lmk_p84l:\n"
"s_add_u32 s97, s55, 0x40000\n"
"buffer_load_dwordx4 v[80:83], v0, s[4:7], s97 offen sc1\n"
"buffer_load_dwordx4 v[84:87], v0, s[4:7], s97 offen offset:1024 sc1\n"
"buffer_load_dwordx4 v[92:95], v0, s[4:7], s97 offen offset:2048 sc1\n"
"buffer_load_dwordx4 v[96:99], v0, s[4:7], s97 offen offset:3072 sc1\n"
"buffer_load_dwordx4 v[88:91], v1, s[4:7], s97 offen sc1\n"
"buffer_load_dwordx4 v[116:119], v1, s[4:7], s97 offen offset:1024 sc1\n"
"buffer_load_dwordx4 v[124:127], v1, s[4:7], s97 offen offset:2048 sc1\n"
"buffer_load_dwordx4 v[172:175], v1, s[4:7], s97 offen offset:3072 sc1\n"
"s_add_u32 s97, s52, 0x48000\n"
"buffer_load_dwordx4 v[128:131], v0, s[4:7], s97 offen sc1\n"
"buffer_load_dwordx4 v[68:71], v0, s[4:7], s97 offen offset:1024 sc1\n"
"buffer_load_dwordx4 v[168:171], v0, s[4:7], s97 offen offset:2048 sc1\n"
"buffer_load_dwordx4 v[152:155], v0, s[4:7], s97 offen offset:3072 sc1\n"
"buffer_load_dwordx4 v[148:151], v1, s[4:7], s97 offen sc1\n"
"buffer_load_dwordx4 v[164:167], v1, s[4:7], s97 offen offset:1024 sc1\n"
"buffer_load_dwordx4 v[180:183], v1, s[4:7], s97 offen offset:2048 sc1\n"
"buffer_load_dwordx4 v[132:135], v1, s[4:7], s97 offen offset:3072 sc1\n"
"s_add_u32 s97, s53, 0x48000\n"
"buffer_load_dwordx4 v[200:203], v0, s[4:7], s97 offen sc1\n"
"buffer_load_dwordx4 v[104:107], v0, s[4:7], s97 offen offset:1024 sc1\n"
"s_waitcnt vmcnt(0)\n"
"v_max_u32_e32 v252, v80, v81\n"
"v_max3_u32 v252, v252, v82, v83\n"
"v_max3_u32 v252, v252, v84, v85\n"
"v_max3_u32 v252, v252, v86, v87\n"
"v_max3_u32 v252, v252, v92, v93\n"
"v_max3_u32 v252, v252, v94, v95\n"
"v_max3_u32 v252, v252, v96, v97\n"
"v_max3_u32 v252, v252, v98, v99\n"
"v_max3_u32 v252, v252, v88, v89\n"
"v_max3_u32 v252, v252, v90, v91\n"
"v_max3_u32 v252, v252, v116, v117\n"
"v_max3_u32 v252, v252, v118, v119\n"
"v_max3_u32 v252, v252, v124, v125\n"
"v_max3_u32 v252, v252, v126, v127\n"
"v_max3_u32 v252, v252, v172, v173\n"
"v_max3_u32 v252, v252, v174, v175\n"
"v_cmp_eq_u32_e32 vcc, -1, v252\n"
"s_cbranch_vccz .Lmk_p84k\n"
"s_sub_u32 s1, s1, 1\n"
"s_cbranch_scc0 .Lmk_p84l\n"
".Lmk_p84k:\n"
"v_mfma_f32_16x16x32_bf16 v[176:179], a[0:3], v[80:83], v[144:147]\n"
"v_mfma_f32_16x16x32_bf16 v[176:179], a[4:7], v[84:87], v[176:179]\n"
"v_mfma_f32_16x16x32_bf16 v[176:179], a[8:11], v[92:95], v[176:179]\n"
"v_mfma_f32_16x16x32_bf16 v[176:179], a[12:15], v[96:99], v[176:179]\n"
"v_mfma_f32_16x16x32_bf16 v[176:179], a[16:19], v[88:91], v[176:179]\n"
"v_mfma_f32_16x16x32_bf16 v[176:179], a[20:23], v[116:119], v[176:179]\n"
"v_mfma_f32_16x16x32_bf16 v[176:179], a[24:27], v[124:127], v[176:179]\n"
"v_mfma_f32_16x16x32_bf16 v[176:179], a[28:31], v[172:175], v[176:179]\n"
"v_mfma_f32_16x16x32_bf16 v[204:207], a[60:63], v[172:175], v[160:163]\n"
"v_mfma_f32_16x16x32_bf16 v[204:207], a[56:59], v[124:127], v[204:207]\n"
"v_mfma_f32_16x16x32_bf16 v[204:207], a[52:55], v[116:119], v[204:207]\n"
"v_mfma_f32_16x16x32_bf16 v[204:207], a[48:51], v[88:91], v[204:207]\n"
"v_mfma_f32_16x16x32_bf16 v[204:207], a[44:47], v[96:99], v[204:207]\n"
"v_mfma_f32_16x16x32_bf16 v[204:207], a[40:43], v[92:95], v[204:207]\n"
"s_nop 7\n"
"s_branch .Lmk_p84r\n"
".Lmk_p86s:\n"
";\n"
"s_waitcnt vmcnt(16)\n"
"s_mov_b32 s1, 0x10000\n"
".Lmk_p86l:\n"
"s_add_u32 s95, s52, 0x48000\n"
"buffer_load_dwordx4 v[128:131], v0, s[4:7], s95 offen sc1\n"
"buffer_load_dwordx4 v[68:71], v0, s[4:7], s95 offen offset:1024 sc1\n"
"buffer_load_dwordx4 v[168:171], v0, s[4:7], s95 offen offset:2048 sc1\n"
"buffer_load_dwordx4 v[152:155], v0, s[4:7], s95 offen offset:3072 sc1\n"
"buffer_load_dwordx4 v[148:151], v1, s[4:7], s95 offen sc1\n"
"buffer_load_dwordx4 v[164:167], v1, s[4:7], s95 offen offset:1024 sc1\n"
"buffer_load_dwordx4 v[180:183], v1, s[4:7], s95 offen offset:2048 sc1\n"
"buffer_load_dwordx4 v[132:135], v1, s[4:7], s95 offen offset:3072 sc1\n"
"s_add_u32 s95, s53, 0x48000\n"
"buffer_load_dwordx4 v[200:203], v0, s[4:7], s95 offen sc1\n"
"buffer_load_dwordx4 v[104:107], v0, s[4:7], s95 offen offset:1024 sc1\n"
"buffer_load_dwordx4 v[144:147], v0, s[4:7], s95 offen offset:2048 sc1\n"
"buffer_load_dwordx4 v[44:47], v0, s[4:7], s95 offen offset:3072 sc1\n"
"buffer_load_dwordx4 v[156:159], v1, s[4:7], s95 offen sc1\n"
"buffer_load_dwordx4 v[184:187], v1, s[4:7], s95 offen offset:1024 sc1\n"
"buffer_load_dwordx4 v[208:211], v1, s[4:7], s95 offen offset:2048 sc1\n"
"buffer_load_dwordx4 v[176:179], v1, s[4:7], s95 offen offset:3072 sc1\n"
"s_add_u32 s95, s54, 0x48000\n"
"buffer_load_dwordx4 v[84:87], v0, s[4:7], s95 offen sc1\n"
"buffer_load_dwordx4 v[88:91], v0, s[4:7], s95 offen offset:1024 sc1\n"
"s_waitcnt vmcnt(0)\n"
"v_max_u32_e32 v252, v128, v129\n"
"v_max3_u32 v252, v252, v130, v131\n"
"v_max3_u32 v252, v252, v68, v69\n"
"v_max3_u32 v252, v252, v70, v71\n"
"v_max3_u32 v252, v252, v168, v169\n"
"v_max3_u32 v252, v252, v170, v171\n"
"v_max3_u32 v252, v252, v152, v153\n"
"v_max3_u32 v252, v252, v154, v155\n"
"v_max3_u32 v252, v252, v148, v149\n"
"v_max3_u32 v252, v252, v150, v151\n"
"v_max3_u32 v252, v252, v164, v165\n"
"v_max3_u32 v252, v252, v166, v167\n"
"v_max3_u32 v252, v252, v180, v181\n"
"v_max3_u32 v252, v252, v182, v183\n"
"v_max3_u32 v252, v252, v132, v133\n"
"v_max3_u32 v252, v252, v134, v135\n"
"v_cmp_eq_u32_e32 vcc, -1, v252\n"
"s_cbranch_vccz .Lmk_p86k\n"
"s_sub_u32 s1, s1, 1\n"
"s_cbranch_scc0 .Lmk_p86l\n"
".Lmk_p86k:\n"
"v_mfma_f32_16x16x32_bf16 v[80:83], a[0:3], v[128:131], 0\n"
"v_mfma_f32_16x16x32_bf16 v[80:83], a[4:7], v[68:71], v[80:83]\n"
"v_mfma_f32_16x16x32_bf16 v[80:83], a[8:11], v[168:171], v[80:83]\n"
"v_mfma_f32_16x16x32_bf16 v[80:83], a[12:15], v[152:155], v[80:83]\n"
"v_mfma_f32_16x16x32_bf16 v[80:83], a[16:19], v[148:151], v[80:83]\n"
"v_mfma_f32_16x16x32_bf16 v[80:83], a[20:23], v[164:167], v[80:83]\n"
"v_mfma_f32_16x16x32_bf16 v[80:83], a[24:27], v[180:183], v[80:83]\n"
"v_mfma_f32_16x16x32_bf16 v[80:83], a[28:31], v[132:135], v[80:83]\n"
"v_mfma_f32_16x16x32_bf16 v[92:95], a[60:63], v[132:135], 0\n"
"v_mfma_f32_16x16x32_bf16 v[92:95], a[56:59], v[180:183], v[92:95]\n"
"v_mfma_f32_16x16x32_bf16 v[92:95], a[52:55], v[164:167], v[92:95]\n"
"v_mfma_f32_16x16x32_bf16 v[92:95], a[48:51], v[148:151], v[92:95]\n"
"v_mfma_f32_16x16x32_bf16 v[92:95], a[44:47], v[152:155], v[92:95]\n"
"v_mfma_f32_16x16x32_bf16 v[92:95], a[40:43], v[168:171], v[92:95]\n"
"s_nop 7\n"
"s_branch .Lmk_p86r\n"
".Lmk_p87s:\n"
";\n"
"s_waitcnt vmcnt(16)\n"
"s_mov_b32 s1, 0x10000\n"
".Lmk_p87l:\n"
"s_add_u32 s95, s53, 0x48000\n"
"buffer_load_dwordx4 v[200:203], v0, s[4:7], s95 offen sc1\n"
"buffer_load_dwordx4 v[104:107], v0, s[4:7], s95 offen offset:1024 sc1\n"
"buffer_load_dwordx4 v[144:147], v0, s[4:7], s95 offen offset:2048 sc1\n"
"buffer_load_dwordx4 v[44:47], v0, s[4:7], s95 offen offset:3072 sc1\n"
"buffer_load_dwordx4 v[156:159], v1, s[4:7], s95 offen sc1\n"
"buffer_load_dwordx4 v[184:187], v1, s[4:7], s95 offen offset:1024 sc1\n"
"buffer_load_dwordx4 v[208:211], v1, s[4:7], s95 offen offset:2048 sc1\n"
"buffer_load_dwordx4 v[176:179], v1, s[4:7], s95 offen offset:3072 sc1\n"
"s_add_u32 s95, s54, 0x48000\n"
"buffer_load_dwordx4 v[84:87], v0, s[4:7], s95 offen sc1\n"
"buffer_load_dwordx4 v[88:91], v0, s[4:7], s95 offen offset:1024 sc1\n"
"buffer_load_dwordx4 v[96:99], v0, s[4:7], s95 offen offset:2048 sc1\n"
"buffer_load_dwordx4 v[116:119], v0, s[4:7], s95 offen offset:3072 sc1\n"
"buffer_load_dwordx4 v[92:95], v1, s[4:7], s95 offen sc1\n"
"buffer_load_dwordx4 v[172:175], v1, s[4:7], s95 offen offset:1024 sc1\n"
"buffer_load_dwordx4 v[80:83], v1, s[4:7], s95 offen offset:2048 sc1\n"
"buffer_load_dwordx4 v[160:163], v1, s[4:7], s95 offen offset:3072 sc1\n"
"s_add_u32 s95, s55, 0x48000\n"
"buffer_load_dwordx4 v[68:71], v0, s[4:7], s95 offen sc1\n"
"buffer_load_dwordx4 v[124:127], v0, s[4:7], s95 offen offset:1024 sc1\n"
"s_waitcnt vmcnt(0)\n"
"v_max_u32_e32 v247, v200, v201\n"
"v_max3_u32 v247, v247, v202, v203\n"
"v_max3_u32 v247, v247, v104, v105\n"
"v_max3_u32 v247, v247, v106, v107\n"
"v_max3_u32 v247, v247, v144, v145\n"
"v_max3_u32 v247, v247, v146, v147\n"
"v_max3_u32 v247, v247, v44, v45\n"
"v_max3_u32 v247, v247, v46, v47\n"
"v_max3_u32 v247, v247, v156, v157\n"
"v_max3_u32 v247, v247, v158, v159\n"
"v_max3_u32 v247, v247, v184, v185\n"
"v_max3_u32 v247, v247, v186, v187\n"
"v_max3_u32 v247, v247, v208, v209\n"
"v_max3_u32 v247, v247, v210, v211\n"
"v_max3_u32 v247, v247, v176, v177\n"
"v_max3_u32 v247, v247, v178, v179\n"
"v_cmp_eq_u32_e32 vcc, -1, v247\n"
"s_cbranch_vccz .Lmk_p87k\n"
"s_sub_u32 s1, s1, 1\n"
"s_cbranch_scc0 .Lmk_p87l\n"
".Lmk_p87k:\n"
"v_mfma_f32_16x16x32_bf16 v[48:51], a[0:3], v[200:203], 0\n"
"v_mfma_f32_16x16x32_bf16 v[48:51], a[4:7], v[104:107], v[48:51]\n"
"v_mfma_f32_16x16x32_bf16 v[48:51], a[8:11], v[144:147], v[48:51]\n"
"v_mfma_f32_16x16x32_bf16 v[48:51], a[12:15], v[44:47], v[48:51]\n"
"v_mfma_f32_16x16x32_bf16 v[48:51], a[16:19], v[156:159], v[48:51]\n"
"v_mfma_f32_16x16x32_bf16 v[48:51], a[20:23], v[184:187], v[48:51]\n"
"v_mfma_f32_16x16x32_bf16 v[48:51], a[24:27], v[208:211], v[48:51]\n"
"v_mfma_f32_16x16x32_bf16 v[48:51], a[28:31], v[176:179], v[48:51]\n"
"v_mfma_f32_16x16x32_bf16 v[128:131], a[60:63], v[176:179], 0\n"
"v_mfma_f32_16x16x32_bf16 v[128:131], a[56:59], v[208:211], v[128:131]\n"
"v_mfma_f32_16x16x32_bf16 v[128:131], a[52:55], v[184:187], v[128:131]\n"
"v_mfma_f32_16x16x32_bf16 v[128:131], a[48:51], v[156:159], v[128:131]\n"
"v_mfma_f32_16x16x32_bf16 v[128:131], a[44:47], v[44:47], v[128:131]\n"
"v_mfma_f32_16x16x32_bf16 v[128:131], a[40:43], v[144:147], v[128:131]\n"
"s_nop 7\n"
"s_branch .Lmk_p87r\n"
".Lmk_m90s:\n"
";\n"
"s_waitcnt vmcnt(16)\n"
"s_mov_b32 s1, 0x10000\n"
".Lmk_m90l:\n"
"s_add_u32 s98, s62, 0x4800\n"
"buffer_load_dword v255, v10, s[4:7], s98 offen sc1\n"
"buffer_load_dword v253, v10, s[4:7], s98 offen offset:256 sc1\n"
"buffer_load_dword v252, v10, s[4:7], s98 offen offset:512 sc1\n"
"buffer_load_dword v251, v10, s[4:7], s98 offen offset:768 sc1\n"
"buffer_load_dword v250, v10, s[4:7], s98 offen offset:1024 sc1\n"
"buffer_load_dword v254, v10, s[4:7], s98 offen offset:1280 sc1\n"
"buffer_load_dword v249, v10, s[4:7], s98 offen offset:1536 sc1\n"
"buffer_load_dword v248, v10, s[4:7], s98 offen offset:1792 sc1\n"
"s_waitcnt vmcnt(0)\n"
"v_max_u32_e32 v245, v255, v253\n"
"v_max3_u32 v245, v245, v252, v251\n"
"v_max3_u32 v245, v245, v250, v254\n"
"v_max3_u32 v245, v245, v249, v248\n"
"v_cmp_eq_u32_e32 vcc, -1, v245\n"
"s_cbranch_vccz .Lmk_m90k\n"
"s_sub_u32 s1, s1, 1\n"
"s_cbranch_scc0 .Lmk_m90l\n"
".Lmk_m90k:\n"
"v_add_f32_e32 v255, v255, v250\n"
"v_add_f32_e32 v253, v253, v254\n"
"v_add_f32_e32 v252, v252, v249\n"
"v_add_f32_e32 v251, v251, v248\n"
"v_add_f32_e32 v255, v255, v252\n"
"v_add_f32_e32 v253, v253, v251\n"
"v_add_f32_e32 v255, v255, v253\n"
"s_nop 1\n"
"s_branch .Lmk_m90r\n"
".Lmk_f77s:\n"
";\n"
"s_mov_b32 s1, 0x10000\n"
".Lmk_f77l:\n"
"buffer_load_dwordx2 v[246:247], v12, s[4:7], s97 offen sc1\n"
"s_waitcnt vmcnt(0)\n"
"v_cmp_ne_u64_e32 vcc, v[246:247], v[18:19]\n"
"s_cbranch_vccz .Lmk_f77r\n"
"s_sub_u32 s1, s1, 1\n"
"s_cbranch_scc0 .Lmk_f77l\n"
"s_branch .Lmk_f77r\n"
".Lmk_p88s:\n"
";\n"
"s_waitcnt vmcnt(16)\n"
"s_mov_b32 s1, 0x10000\n"
".Lmk_p88l:\n"
"s_add_u32 s97, s54, 0x48000\n"
"buffer_load_dwordx4 v[84:87], v0, s[4:7], s97 offen sc1\n"
"buffer_load_dwordx4 v[88:91], v0, s[4:7], s97 offen offset:1024 sc1\n"
"buffer_load_dwordx4 v[96:99], v0, s[4:7], s97 offen offset:2048 sc1\n"
"buffer_load_dwordx4 v[116:119], v0, s[4:7], s97 offen offset:3072 sc1\n"
"buffer_load_dwordx4 v[92:95], v1, s[4:7], s97 offen sc1\n"
"buffer_load_dwordx4 v[172:175], v1, s[4:7], s97 offen offset:1024 sc1\n"
"buffer_load_dwordx4 v[80:83], v1, s[4:7], s97 offen offset:2048 sc1\n"
"buffer_load_dwordx4 v[160:163], v1, s[4:7], s97 offen offset:3072 sc1\n"
"s_add_u32 s97, s55, 0x48000\n"
"buffer_load_dwordx4 v[68:71], v0, s[4:7], s97 offen sc1\n"
"buffer_load_dwordx4 v[124:127], v0, s[4:7], s97 offen offset:1024 sc1\n"
"buffer_load_dwordx4 v[132:135], v0, s[4:7], s97 offen offset:2048 sc1\n"
"buffer_load_dwordx4 v[148:151], v0, s[4:7], s97 offen offset:3072 sc1\n"
"buffer_load_dwordx4 v[128:131], v1, s[4:7], s97 offen sc1\n"
"buffer_load_dwordx4 v[164:167], v1, s[4:7], s97 offen offset:1024 sc1\n"
"buffer_load_dwordx4 v[168:171], v1, s[4:7], s97 offen offset:2048 sc1\n"
"buffer_load_dwordx4 v[180:183], v1, s[4:7], s97 offen offset:3072 sc1\n"
"s_waitcnt vmcnt(0)\n"
"v_max_u32_e32 v255, v84, v85\n"
"v_max3_u32 v255, v255, v86, v87\n"
"v_max3_u32 v255, v255, v88, v89\n"
"v_max3_u32 v255, v255, v90, v91\n"
"v_max3_u32 v255, v255, v96, v97\n"
"v_max3_u32 v255, v255, v98, v99\n"
"v_max3_u32 v255, v255, v116, v117\n"
"v_max3_u32 v255, v255, v118, v119\n"
"v_max3_u32 v255, v255, v92, v93\n"
"v_max3_u32 v255, v255, v94, v95\n"
"v_max3_u32 v255, v255, v172, v173\n"
"v_max3_u32 v255, v255, v174, v175\n"
"v_max3_u32 v255, v255, v80, v81\n"
"v_max3_u32 v255, v255, v82, v83\n"
"v_max3_u32 v255, v255, v160, v161\n"
"v_max3_u32 v255, v255, v162, v163\n"
"v_cmp_eq_u32_e32 vcc, -1, v255\n"
"s_cbranch_vccz .Lmk_p88k\n"
"s_sub_u32 s1, s1, 1\n"
"s_cbranch_scc0 .Lmk_p88l\n"
".Lmk_p88k:\n"
"v_mfma_f32_16x16x32_bf16 v[44:47], a[0:3], v[84:87], 0\n"
"v_mfma_f32_16x16x32_bf16 v[44:47], a[4:7], v[88:91], v[44:47]\n"
"v_mfma_f32_16x16x32_bf16 v[44:47], a[8:11], v[96:99], v[44:47]\n"
"v_mfma_f32_16x16x32_bf16 v[44:47], a[12:15], v[116:119], v[44:47]\n"
"v_mfma_f32_16x16x32_bf16 v[44:47], a[16:19], v[92:95], v[44:47]\n"
"v_mfma_f32_16x16x32_bf16 v[44:47], a[20:23], v[172:175], v[44:47]\n"
"v_mfma_f32_16x16x32_bf16 v[44:47], a[24:27], v[80:83], v[44:47]\n"
"v_mfma_f32_16x16x32_bf16 v[44:47], a[28:31], v[160:163], v[44:47]\n"
"v_mfma_f32_16x16x32_bf16 v[48:51], a[60:63], v[160:163], 0\n"
"v_mfma_f32_16x16x32_bf16 v[48:51], a[56:59], v[80:83], v[48:51]\n"
"v_mfma_f32_16x16x32_bf16 v[48:51], a[52:55], v[172:175], v[48:51]\n"
"v_mfma_f32_16x16x32_bf16 v[48:51], a[48:51], v[92:95], v[48:51]\n"
"v_mfma_f32_16x16x32_bf16 v[48:51], a[44:47], v[116:119], v[48:51]\n"
"v_mfma_f32_16x16x32_bf16 v[48:51], a[40:43], v[96:99], v[48:51]\n"
"s_nop 7\n"
"s_branch .Lmk_p88r\n"
".Lmk_p89s:\n"
";\n"
"s_waitcnt vmcnt(16)\n"
"s_mov_b32 s1, 0x10000\n"
".Lmk_p89l:\n"
"s_add_u32 s97, s55, 0x48000\n"
"buffer_load_dwordx4 v[68:71], v0, s[4:7], s97 offen sc1\n"
"buffer_load_dwordx4 v[124:127], v0, s[4:7], s97 offen offset:1024 sc1\n"
"buffer_load_dwordx4 v[132:135], v0, s[4:7], s97 offen offset:2048 sc1\n"
"buffer_load_dwordx4 v[148:151], v0, s[4:7], s97 offen offset:3072 sc1\n"
"buffer_load_dwordx4 v[128:131], v1, s[4:7], s97 offen sc1\n"
"buffer_load_dwordx4 v[164:167], v1, s[4:7], s97 offen offset:1024 sc1\n"
"buffer_load_dwordx4 v[168:171], v1, s[4:7], s97 offen offset:2048 sc1\n"
"buffer_load_dwordx4 v[180:183], v1, s[4:7], s97 offen offset:3072 sc1\n"
"s_waitcnt vmcnt(0)\n"
"v_max_u32_e32 v252, v68, v69\n"
"v_max3_u32 v252, v252, v70, v71\n"
"v_max3_u32 v252, v252, v124, v125\n"
"v_max3_u32 v252, v252, v126, v127\n"
"v_max3_u32 v252, v252, v132, v133\n"
"v_max3_u32 v252, v252, v134, v135\n"
"v_max3_u32 v252, v252, v148, v149\n"
"v_max3_u32 v252, v252, v150, v151\n"
"v_max3_u32 v252, v252, v128, v129\n"
"v_max3_u32 v252, v252, v130, v131\n"
"v_max3_u32 v252, v252, v164, v165\n"
"v_max3_u32 v252, v252, v166, v167\n"
"v_max3_u32 v252, v252, v168, v169\n"
"v_max3_u32 v252, v252, v170, v171\n"
"v_max3_u32 v252, v252, v180, v181\n"
"v_max3_u32 v252, v252, v182, v183\n"
"v_cmp_eq_u32_e32 vcc, -1, v252\n"
"s_cbranch_vccz .Lmk_p89k\n"
"s_sub_u32 s1, s1, 1\n"
"s_cbranch_scc0 .Lmk_p89l\n"
".Lmk_p89k:\n"
"v_mfma_f32_16x16x32_bf16 v[96:99], a[0:3], v[68:71], v[104:107]\n"
"v_mfma_f32_16x16x32_bf16 v[96:99], a[4:7], v[124:127], v[96:99]\n"
"v_mfma_f32_16x16x32_bf16 v[96:99], a[8:11], v[132:135], v[96:99]\n"
"v_mfma_f32_16x16x32_bf16 v[96:99], a[12:15], v[148:151], v[96:99]\n"
"v_mfma_f32_16x16x32_bf16 v[96:99], a[16:19], v[128:131], v[96:99]\n"
"v_mfma_f32_16x16x32_bf16 v[96:99], a[20:23], v[164:167], v[96:99]\n"
"v_mfma_f32_16x16x32_bf16 v[96:99], a[24:27], v[168:171], v[96:99]\n"
"v_mfma_f32_16x16x32_bf16 v[96:99], a[28:31], v[180:183], v[96:99]\n"
"v_mfma_f32_16x16x32_bf16 v[200:203], a[60:63], v[180:183], v[176:179]\n"
"v_mfma_f32_16x16x32_bf16 v[200:203], a[56:59], v[168:171], v[200:203]\n"
"v_mfma_f32_16x16x32_bf16 v[200:203], a[52:55], v[164:167], v[200:203]\n"
"v_mfma_f32_16x16x32_bf16 v[200:203], a[48:51], v[128:131], v[200:203]\n"
"v_mfma_f32_16x16x32_bf16 v[200:203], a[44:47], v[148:151], v[200:203]\n"
"v_mfma_f32_16x16x32_bf16 v[200:203], a[40:43], v[132:135], v[200:203]\n"
"s_nop 7\n"
"s_branch .Lmk_p89r\n"
".Lmk_f78s:\n"
";\n"
"s_mov_b32 s1, 0x10000\n"
".Lmk_f78l:\n"
"buffer_load_dwordx2 v[250:251], off, s[4:7], s98 sc1\n"
"s_waitcnt vmcnt(0)\n"
"v_cmp_ne_u64_e32 vcc, v[250:251], v[18:19]\n"
"s_cbranch_vccz .Lmk_f78r\n"
"s_sub_u32 s1, s1, 1\n"
"s_cbranch_scc0 .Lmk_f78l\n"
"s_branch .Lmk_f78r\n"
".Lmk_x99s:\n"
"s_nop 3\n"
"v_mov_b32_e32 v36, 0xff800000\n"
"v_mov_b32_e32 v37, 0xff800000\n"
"v_mov_b32_e32 v38, 0xff800000\n"
"v_mov_b32_e32 v39, 0xff800000\n"
"v_mov_b32_e32 v60, 0xff800000\n"
"v_mov_b32_e32 v61, 0xff800000\n"
"v_mov_b32_e32 v62, 0xff800000\n"
"v_mov_b32_e32 v63, 0xff800000\n"
"s_nop 1\n"
"s_branch .Lmk_x99r\n"
".Lmk_x100s:\n"
"s_nop 3\n"
"v_mov_b32_e32 v36, 0xff800000\n"
"v_mov_b32_e32 v37, 0xff800000\n"
"v_mov_b32_e32 v38, 0xff800000\n"
"v_mov_b32_e32 v39, 0xff800000\n"
"v_mov_b32_e32 v60, 0xff800000\n"
"v_mov_b32_e32 v61, 0xff800000\n"
"v_mov_b32_e32 v62, 0xff800000\n"
"v_mov_b32_e32 v63, 0xff800000\n"
"s_nop 1\n"
"s_branch .Lmk_x100r\n"
".Lmk_x101s:\n"
"s_nop 3\n"
"v_mov_b32_e32 v36, 0xff800000\n"
"v_mov_b32_e32 v37, 0xff800000\n"
"v_mov_b32_e32 v38, 0xff800000\n"
"v_mov_b32_e32 v39, 0xff800000\n"
"v_mov_b32_e32 v60, 0xff800000\n"
"v_mov_b32_e32 v61, 0xff800000\n"
"v_mov_b32_e32 v62, 0xff800000\n"
"v_mov_b32_e32 v63, 0xff800000\n"
"s_nop 1\n"
"s_branch .Lmk_x101r\n"
".Lmk_x102s:\n"
"s_nop 3\n"
"v_mov_b32_e32 v36, 0xff800000\n"
"v_mov_b32_e32 v37, 0xff800000\n"
"v_mov_b32_e32 v38, 0xff800000\n"
"v_mov_b32_e32 v39, 0xff800000\n"
"v_mov_b32_e32 v60, 0xff800000\n"
"v_mov_b32_e32 v61, 0xff800000\n"
"v_mov_b32_e32 v62, 0xff800000\n"
"v_mov_b32_e32 v63, 0xff800000\n"
"s_nop 1\n"
"s_branch .Lmk_x102r\n"
".Lmk_b97:\n"
"s_cmp_lg_u32 s2, 0\n"
"s_cbranch_scc1 .Lmk_k103\n"
"s_mov_b32 s2, 1\n"
"s_waitcnt vmcnt(0) lgkmcnt(0)\n"
"s_nop 7\n"
"s_mov_b32 s97, 11\n"
"v_add_u32_e32 v248, 0x3800, v15\n"
"v_add_u32_e32 v247, 0x3800, v254\n"
"ds_read_b128 v[36:39], v15 offset:24576\n"
"ds_read_b128 v[40:43], v254 offset:24832\n"
"s_waitcnt lgkmcnt(0)\n"
"v_mfma_f32_16x16x32_bf16 v[48:51], v[36:39], v[28:31], 0\n"
"v_mfma_f32_16x16x32_bf16 v[52:55], v[36:39], v[32:35], 0\n"
"v_mfma_f32_16x16x32_bf16 v[36:39], v[40:43], v[32:35], 0\n"
"v_mfma_f32_16x16x32_bf16 v[56:59], v[40:43], v[28:31], 0\n"
".Lmk_b97l:\n"
"ds_read_b128 v[40:43], v248 offset:8192\n"
"ds_read_b128 v[60:63], v247 offset:8448\n"
"s_cmp_gt_i32 s78, s97\n"
"s_cselect_b32 s96, 0xff800000, 0\n"
"v_mov_b32_e32 v64, s96\n"
"v_mov_b32_e32 v65, s96\n"
"v_mov_b32_e32 v66, s96\n"
"v_mov_b32_e32 v67, s96\n"
"v_subrev_u32_e32 v248, 0x800, v248\n"
"v_subrev_u32_e32 v247, 0x800, v247\n"
"s_waitcnt lgkmcnt(0)\n"
"v_mfma_f32_16x16x32_bf16 v[72:75], v[40:43], v[28:31], v[64:67]\n"
"v_mfma_f32_16x16x32_bf16 v[76:79], v[40:43], v[32:35], v[64:67]\n"
"v_mfma_f32_16x16x32_bf16 v[40:43], v[60:63], v[32:35], v[64:67]\n"
"v_mfma_f32_16x16x32_bf16 v[84:87], v[60:63], v[28:31], v[64:67]\n"
"s_nop 7\n"
"s_cmp_lg_u32 s97, 4\n"
"s_cbranch_scc1 .Lmk_b97m\n"
"v_cndmask_b32_e64 v72, v48, v72, s[20:21]\n"
"v_cndmask_b32_e64 v73, v49, v73, s[22:23]\n"
"v_cndmask_b32_e64 v74, v50, v74, s[24:25]\n"
"v_cndmask_b32_e64 v75, v51, v75, s[26:27]\n"
"v_cndmask_b32_e64 v84, v56, v84, s[28:29]\n"
"v_cndmask_b32_e64 v85, v57, v85, s[30:31]\n"
"v_cndmask_b32_e64 v86, v58, v86, s[32:33]\n"
"v_cndmask_b32_e64 v87, v59, v87, s[34:35]\n"
"v_cndmask_b32_e64 v76, v52, v76, s[36:37]\n"
"v_cndmask_b32_e64 v77, v53, v77, s[38:39]\n"
"v_cndmask_b32_e64 v78, v54, v78, s[40:41]\n"
"v_cndmask_b32_e64 v79, v55, v79, s[42:43]\n"
"v_cndmask_b32_e64 v40, v36, v40, s[44:45]\n"
"v_cndmask_b32_e64 v41, v37, v41, s[46:47]\n"
"v_cndmask_b32_e64 v42, v38, v42, s[48:49]\n"
"v_cndmask_b32_e64 v43, v39, v43, s[50:51]\n"
".Lmk_b97m:\n"
"v_max3_f32 v253, v253, v72, v73\n"
"v_max3_f32 v251, v251, v76, v77\n"
"v_max3_f32 v253, v253, v74, v75\n"
"v_max3_f32 v251, v251, v78, v79\n"
"v_max3_f32 v253, v253, v84, v85\n"
"v_max3_f32 v251, v251, v40, v41\n"
"v_max3_f32 v253, v253, v86, v87\n"
"v_max3_f32 v251, v251, v42, v43\n"
"s_sub_u32 s97, s97, 1\n"
"s_cmp_ge_i32 s97, 4\n"
"s_cbranch_scc1 .Lmk_b97l\n"
"v_mov_b32_e32 v248, v253\n"
"v_mov_b32_e32 v247, v251\n"
"s_nop 0\n"
"v_permlane16_swap_b32_e32 v253, v248\n"
"v_permlane16_swap_b32_e32 v251, v247\n"
"v_max_f32_e32 v253, v253, v248\n"
"v_max_f32_e32 v251, v251, v247\n"
"v_mov_b32_e32 v248, v253\n"
"v_mov_b32_e32 v247, v251\n"
"s_nop 0\n"
"v_permlane32_swap_b32_e32 v253, v248\n"
"v_permlane32_swap_b32_e32 v251, v247\n"
"v_max_f32_e32 v253, v253, v248\n"
"v_max_f32_e32 v251, v251, v247\n"
"v_mov_b32_e32 v250, v253\n"
"v_mov_b32_e32 v249, v251\n"
"s_branch .Lmk_a96\n"
".Lmk_x106s:\n"
"s_nop 3\n"
"v_mov_b32_e32 v116, 0xff800000\n"
"v_mov_b32_e32 v117, 0xff800000\n"
"v_mov_b32_e32 v118, 0xff800000\n"
"v_mov_b32_e32 v119, 0xff800000\n"
"v_mov_b32_e32 v120, 0xff800000\n"
"v_mov_b32_e32 v121, 0xff800000\n"
"v_mov_b32_e32 v122, 0xff800000\n"
"v_mov_b32_e32 v123, 0xff800000\n"
"s_nop 1\n"
"s_branch .Lmk_x106r\n"
".Lmk_x107s:\n"
"s_nop 3\n"
"v_mov_b32_e32 v116, 0xff800000\n"
"v_mov_b32_e32 v117, 0xff800000\n"
"v_mov_b32_e32 v118, 0xff800000\n"
"v_mov_b32_e32 v119, 0xff800000\n"
"v_mov_b32_e32 v120, 0xff800000\n"
"v_mov_b32_e32 v121, 0xff800000\n"
"v_mov_b32_e32 v122, 0xff800000\n"
"v_mov_b32_e32 v123, 0xff800000\n"
"s_nop 1\n"
"s_branch .Lmk_x107r\n"
".Lmk_x108s:\n"
"s_nop 3\n"
"v_mov_b32_e32 v116, 0xff800000\n"
"v_mov_b32_e32 v117, 0xff800000\n"
"v_mov_b32_e32 v118, 0xff800000\n"
"v_mov_b32_e32 v119, 0xff800000\n"
"v_mov_b32_e32 v120, 0xff800000\n"
"v_mov_b32_e32 v121, 0xff800000\n"
"v_mov_b32_e32 v122, 0xff800000\n"
"v_mov_b32_e32 v123, 0xff800000\n"
"s_nop 1\n"
"s_branch .Lmk_x108r\n"
".Lmk_h95s:\n"
";\n"
"s_waitcnt vmcnt(16)\n"
"s_mov_b32 s1, 0x10000\n"
".Lmk_h95l:\n"
"s_add_u32 m0, s77, 0x4000\n"
"s_add_u32 s95, s65, 0\n"
"buffer_load_dwordx4 v2, s[4:7], s95 offen sc1 lds\n"
"buffer_load_dwordx4 v2, s[4:7], s95 offen offset:1024 sc1 lds\n"
"s_add_u32 m0, s77, 0xc000\n"
"s_add_u32 s95, s65, 0x4000\n"
"buffer_load_dwordx4 v2, s[4:7], s95 offen sc1 lds\n"
"buffer_load_dwordx4 v2, s[4:7], s95 offen offset:1024 sc1 lds\n"
"s_waitcnt vmcnt(0)\n"
"ds_read_b128 v[164:167], v2 offset:16384\n"
"ds_read_b128 v[76:79], v2 offset:17408\n"
"ds_read_b128 v[72:75], v2 offset:49152\n"
"ds_read_b128 v[84:87], v2 offset:50176\n"
"s_waitcnt lgkmcnt(0)\n"
"s_waitcnt vmcnt(0)\n"
"v_max_u32_e32 v248, v164, v165\n"
"v_max3_u32 v248, v248, v166, v167\n"
"v_max3_u32 v248, v248, v76, v77\n"
"v_max3_u32 v248, v248, v78, v79\n"
"v_max3_u32 v248, v248, v72, v73\n"
"v_max3_u32 v248, v248, v74, v75\n"
"v_max3_u32 v248, v248, v84, v85\n"
"v_max3_u32 v248, v248, v86, v87\n"
"v_cmp_eq_u32_e32 vcc, -1, v248\n"
"s_cbranch_vccz .Lmk_h95k\n"
"s_sub_u32 s1, s1, 1\n"
"s_cbranch_scc0 .Lmk_h95l\n"
".Lmk_h95k:\n"
"s_branch .Lmk_h95r\n"
".Lmk_f79s:\n"
";\n"
"s_mov_b32 s1, 0x10000\n"
".Lmk_f79l:\n"
"buffer_load_dwordx2 v[246:247], v12, s[4:7], s96 offen sc1\n"
"s_waitcnt vmcnt(0)\n"
"v_cmp_ne_u64_e32 vcc, v[246:247], v[18:19]\n"
"s_cbranch_vccz .Lmk_f79r\n"
"s_sub_u32 s1, s1, 1\n"
"s_cbranch_scc0 .Lmk_f79l\n"
"s_branch .Lmk_f79r\n"
".Lmk_b105:\n"
"s_cmp_lg_u32 s2, 0\n"
"s_cbranch_scc1 .Lmk_k110\n"
"s_mov_b32 s2, 1\n"
"s_waitcnt vmcnt(0) lgkmcnt(0)\n"
"s_nop 7\n"
"s_mov_b32 s96, 7\n"
"v_add_u32_e32 v248, 0x3800, v15\n"
"v_add_u32_e32 v247, 0x3800, v254\n"
"ds_read_b128 v[188:191], v15 offset:16384\n"
"ds_read_b128 v[192:195], v254 offset:16640\n"
"s_waitcnt lgkmcnt(0)\n"
"v_mfma_f32_16x16x32_bf16 v[196:199], v[188:191], v[92:95], 0\n"
"v_mfma_f32_16x16x32_bf16 v[212:215], v[188:191], v[96:99], 0\n"
"v_mfma_f32_16x16x32_bf16 v[188:191], v[192:195], v[96:99], 0\n"
"v_mfma_f32_16x16x32_bf16 v[220:223], v[192:195], v[92:95], 0\n"
".Lmk_b105l:\n"
"ds_read_b128 v[192:195], v248\n"
"ds_read_b128 v[224:227], v247 offset:256\n"
"s_cmp_gt_i32 s78, s96\n"
"s_cselect_b32 s95, 0xff800000, 0\n"
"v_mov_b32_e32 v228, s95\n"
"v_mov_b32_e32 v229, s95\n"
"v_mov_b32_e32 v230, s95\n"
"v_mov_b32_e32 v231, s95\n"
"v_subrev_u32_e32 v248, 0x800, v248\n"
"v_subrev_u32_e32 v247, 0x800, v247\n"
"s_waitcnt lgkmcnt(0)\n"
"v_mfma_f32_16x16x32_bf16 v[232:235], v[192:195], v[92:95], v[228:231]\n"
"v_mfma_f32_16x16x32_bf16 v[236:239], v[192:195], v[96:99], v[228:231]\n"
"v_mfma_f32_16x16x32_bf16 v[192:195], v[224:227], v[96:99], v[228:231]\n"
"v_mfma_f32_16x16x32_bf16 v[240:243], v[224:227], v[92:95], v[228:231]\n"
"s_nop 7\n"
"s_cmp_lg_u32 s96, 0\n"
"s_cbranch_scc1 .Lmk_b105m\n"
"v_cndmask_b32_e64 v232, v196, v232, s[20:21]\n"
"v_cndmask_b32_e64 v233, v197, v233, s[22:23]\n"
"v_cndmask_b32_e64 v234, v198, v234, s[24:25]\n"
"v_cndmask_b32_e64 v235, v199, v235, s[26:27]\n"
"v_cndmask_b32_e64 v240, v220, v240, s[28:29]\n"
"v_cndmask_b32_e64 v241, v221, v241, s[30:31]\n"
"v_cndmask_b32_e64 v242, v222, v242, s[32:33]\n"
"v_cndmask_b32_e64 v243, v223, v243, s[34:35]\n"
"v_cndmask_b32_e64 v236, v212, v236, s[36:37]\n"
"v_cndmask_b32_e64 v237, v213, v237, s[38:39]\n"
"v_cndmask_b32_e64 v238, v214, v238, s[40:41]\n"
"v_cndmask_b32_e64 v239, v215, v239, s[42:43]\n"
"v_cndmask_b32_e64 v192, v188, v192, s[44:45]\n"
"v_cndmask_b32_e64 v193, v189, v193, s[46:47]\n"
"v_cndmask_b32_e64 v194, v190, v194, s[48:49]\n"
"v_cndmask_b32_e64 v195, v191, v195, s[50:51]\n"
".Lmk_b105m:\n"
"v_max3_f32 v253, v253, v232, v233\n"
"v_max3_f32 v251, v251, v236, v237\n"
"v_max3_f32 v253, v253, v234, v235\n"
"v_max3_f32 v251, v251, v238, v239\n"
"v_max3_f32 v253, v253, v240, v241\n"
"v_max3_f32 v251, v251, v192, v193\n"
"v_max3_f32 v253, v253, v242, v243\n"
"v_max3_f32 v251, v251, v194, v195\n"
"s_sub_u32 s96, s96, 1\n"
"s_cmp_ge_i32 s96, 0\n"
"s_cbranch_scc1 .Lmk_b105l\n"
"v_mov_b32_e32 v248, v253\n"
"v_mov_b32_e32 v247, v251\n"
"s_nop 0\n"
"v_permlane16_swap_b32_e32 v253, v248\n"
"v_permlane16_swap_b32_e32 v251, v247\n"
"v_max_f32_e32 v253, v253, v248\n"
"v_max_f32_e32 v251, v251, v247\n"
"v_mov_b32_e32 v248, v253\n"
"v_mov_b32_e32 v247, v251\n"
"s_nop 0\n"
"v_permlane32_swap_b32_e32 v253, v248\n"
"v_permlane32_swap_b32_e32 v251, v247\n"
"v_max_f32_e32 v253, v253, v248\n"
"v_max_f32_e32 v251, v251, v247\n"
"v_mov_b32_e32 v250, v253\n"
"v_mov_b32_e32 v249, v251\n"
"s_branch .Lmk_a104\n"
".Lmk_p111s:\n"
";\n"
"s_waitcnt vmcnt(16)\n"
"s_mov_b32 s1, 0x10000\n"
".Lmk_p111l:\n"
"s_add_u32 s98, s56, 0x40000\n"
"buffer_load_dwordx4 v[152:155], v0, s[4:7], s98 offen sc1\n"
"buffer_load_dwordx4 v[200:203], v0, s[4:7], s98 offen offset:1024 sc1\n"
"buffer_load_dwordx4 v[184:187], v0, s[4:7], s98 offen offset:2048 sc1\n"
"buffer_load_dwordx4 v[204:207], v0, s[4:7], s98 offen offset:3072 sc1\n"
"buffer_load_dwordx4 v[140:143], v1, s[4:7], s98 offen sc1\n"
"buffer_load_dwordx4 v[208:211], v1, s[4:7], s98 offen offset:1024 sc1\n"
"buffer_load_dwordx4 v[120:123], v1, s[4:7], s98 offen offset:2048 sc1\n"
"buffer_load_dwordx4 v[136:139], v1, s[4:7], s98 offen offset:3072 sc1\n"
"s_add_u32 s98, s57, 0x40000\n"
"buffer_load_dwordx4 v[216:219], v0, s[4:7], s98 offen sc1\n"
"buffer_load_dwordx4 v[132:135], v0, s[4:7], s98 offen offset:1024 sc1\n"
"buffer_load_dwordx4 v[160:163], v0, s[4:7], s98 offen offset:2048 sc1\n"
"buffer_load_dwordx4 v[100:103], v0, s[4:7], s98 offen offset:3072 sc1\n"
"buffer_load_dwordx4 v[164:167], v1, s[4:7], s98 offen sc1\n"
"buffer_load_dwordx4 v[104:107], v1, s[4:7], s98 offen offset:1024 sc1\n"
"buffer_load_dwordx4 v[108:111], v1, s[4:7], s98 offen offset:2048 sc1\n"
"buffer_load_dwordx4 v[128:131], v1, s[4:7], s98 offen offset:3072 sc1\n"
"s_add_u32 s98, s58, 0x40000\n"
"buffer_load_dwordx4 v[148:151], v0, s[4:7], s98 offen sc1\n"
"buffer_load_dwordx4 v[144:147], v0, s[4:7], s98 offen offset:1024 sc1\n"
"buffer_load_dwordx4 v[116:119], v0, s[4:7], s98 offen offset:2048 sc1\n"
"buffer_load_dwordx4 v[112:115], v0, s[4:7], s98 offen offset:3072 sc1\n"
"buffer_load_dwordx4 v[92:95], v1, s[4:7], s98 offen sc1\n"
"buffer_load_dwordx4 v[96:99], v1, s[4:7], s98 offen offset:1024 sc1\n"
"buffer_load_dwordx4 v[156:159], v1, s[4:7], s98 offen offset:2048 sc1\n"
"s_waitcnt vmcnt(0)\n"
"v_max_u32_e32 v255, v152, v153\n"
"v_max3_u32 v255, v255, v154, v155\n"
"v_max3_u32 v255, v255, v200, v201\n"
"v_max3_u32 v255, v255, v202, v203\n"
"v_max3_u32 v255, v255, v184, v185\n"
"v_max3_u32 v255, v255, v186, v187\n"
"v_max3_u32 v255, v255, v204, v205\n"
"v_max3_u32 v255, v255, v206, v207\n"
"v_max3_u32 v255, v255, v140, v141\n"
"v_max3_u32 v255, v255, v142, v143\n"
"v_max3_u32 v255, v255, v208, v209\n"
"v_max3_u32 v255, v255, v210, v211\n"
"v_max3_u32 v255, v255, v120, v121\n"
"v_max3_u32 v255, v255, v122, v123\n"
"v_max3_u32 v255, v255, v136, v137\n"
"v_max3_u32 v255, v255, v138, v139\n"
"v_cmp_eq_u32_e32 vcc, -1, v255\n"
"s_cbranch_vccz .Lmk_p111k\n"
"s_sub_u32 s1, s1, 1\n"
"s_cbranch_scc0 .Lmk_p111l\n"
".Lmk_p111k:\n"
"v_mfma_f32_16x16x32_bf16 v[88:91], a[128:131], v[152:155], 0\n"
"v_mfma_f32_16x16x32_bf16 v[88:91], a[132:135], v[200:203], v[88:91]\n"
"v_mfma_f32_16x16x32_bf16 v[88:91], a[136:139], v[184:187], v[88:91]\n"
"v_mfma_f32_16x16x32_bf16 v[88:91], a[140:143], v[204:207], v[88:91]\n"
"v_mfma_f32_16x16x32_bf16 v[88:91], a[144:147], v[140:143], v[88:91]\n"
"v_mfma_f32_16x16x32_bf16 v[88:91], a[148:151], v[208:211], v[88:91]\n"
"v_mfma_f32_16x16x32_bf16 v[88:91], a[152:155], v[120:123], v[88:91]\n"
"v_mfma_f32_16x16x32_bf16 v[88:91], a[156:159], v[136:139], v[88:91]\n"
"v_mfma_f32_16x16x32_bf16 v[124:127], a[188:191], v[136:139], 0\n"
"v_mfma_f32_16x16x32_bf16 v[124:127], a[184:187], v[120:123], v[124:127]\n"
"v_mfma_f32_16x16x32_bf16 v[124:127], a[180:183], v[208:211], v[124:127]\n"
"v_mfma_f32_16x16x32_bf16 v[124:127], a[176:179], v[140:143], v[124:127]\n"
"v_mfma_f32_16x16x32_bf16 v[124:127], a[172:175], v[204:207], v[124:127]\n"
"v_mfma_f32_16x16x32_bf16 v[124:127], a[168:171], v[184:187], v[124:127]\n"
"s_nop 7\n"
"s_branch .Lmk_p111r\n"
".Lmk_p112s:\n"
";\n"
"s_waitcnt vmcnt(16)\n"
"s_mov_b32 s1, 0x10000\n"
".Lmk_p112l:\n"
"s_add_u32 s97, s57, 0x40000\n"
"buffer_load_dwordx4 v[216:219], v0, s[4:7], s97 offen sc1\n"
"buffer_load_dwordx4 v[132:135], v0, s[4:7], s97 offen offset:1024 sc1\n"
"buffer_load_dwordx4 v[160:163], v0, s[4:7], s97 offen offset:2048 sc1\n"
"buffer_load_dwordx4 v[100:103], v0, s[4:7], s97 offen offset:3072 sc1\n"
"buffer_load_dwordx4 v[164:167], v1, s[4:7], s97 offen sc1\n"
"buffer_load_dwordx4 v[104:107], v1, s[4:7], s97 offen offset:1024 sc1\n"
"buffer_load_dwordx4 v[108:111], v1, s[4:7], s97 offen offset:2048 sc1\n"
"buffer_load_dwordx4 v[128:131], v1, s[4:7], s97 offen offset:3072 sc1\n"
"s_add_u32 s97, s58, 0x40000\n"
"buffer_load_dwordx4 v[148:151], v0, s[4:7], s97 offen sc1\n"
"buffer_load_dwordx4 v[144:147], v0, s[4:7], s97 offen offset:1024 sc1\n"
"buffer_load_dwordx4 v[116:119], v0, s[4:7], s97 offen offset:2048 sc1\n"
"buffer_load_dwordx4 v[112:115], v0, s[4:7], s97 offen offset:3072 sc1\n"
"buffer_load_dwordx4 v[92:95], v1, s[4:7], s97 offen sc1\n"
"buffer_load_dwordx4 v[96:99], v1, s[4:7], s97 offen offset:1024 sc1\n"
"buffer_load_dwordx4 v[156:159], v1, s[4:7], s97 offen offset:2048 sc1\n"
"buffer_load_dwordx4 v[88:91], v1, s[4:7], s97 offen offset:3072 sc1\n"
"s_add_u32 s97, s59, 0x40000\n"
"buffer_load_dwordx4 v[172:175], v0, s[4:7], s97 offen sc1\n"
"buffer_load_dwordx4 v[124:127], v0, s[4:7], s97 offen offset:1024 sc1\n"
"buffer_load_dwordx4 v[120:123], v0, s[4:7], s97 offen offset:2048 sc1\n"
"buffer_load_dwordx4 v[136:139], v0, s[4:7], s97 offen offset:3072 sc1\n"
"buffer_load_dwordx4 v[152:155], v1, s[4:7], s97 offen sc1\n"
"buffer_load_dwordx4 v[168:171], v1, s[4:7], s97 offen offset:1024 sc1\n"
"buffer_load_dwordx4 v[180:183], v1, s[4:7], s97 offen offset:2048 sc1\n"
"s_waitcnt vmcnt(0)\n"
"v_max_u32_e32 v255, v216, v217\n"
"v_max3_u32 v255, v255, v218, v219\n"
"v_max3_u32 v255, v255, v132, v133\n"
"v_max3_u32 v255, v255, v134, v135\n"
"v_max3_u32 v255, v255, v160, v161\n"
"v_max3_u32 v255, v255, v162, v163\n"
"v_max3_u32 v255, v255, v100, v101\n"
"v_max3_u32 v255, v255, v102, v103\n"
"v_max3_u32 v255, v255, v164, v165\n"
"v_max3_u32 v255, v255, v166, v167\n"
"v_max3_u32 v255, v255, v104, v105\n"
"v_max3_u32 v255, v255, v106, v107\n"
"v_max3_u32 v255, v255, v108, v109\n"
"v_max3_u32 v255, v255, v110, v111\n"
"v_max3_u32 v255, v255, v128, v129\n"
"v_max3_u32 v255, v255, v130, v131\n"
"v_cmp_eq_u32_e32 vcc, -1, v255\n"
"s_cbranch_vccz .Lmk_p112k\n"
"s_sub_u32 s1, s1, 1\n"
"s_cbranch_scc0 .Lmk_p112l\n"
".Lmk_p112k:\n"
"v_mfma_f32_16x16x32_bf16 v[140:143], a[128:131], v[216:219], 0\n"
"v_mfma_f32_16x16x32_bf16 v[140:143], a[132:135], v[132:135], v[140:143]\n"
"v_mfma_f32_16x16x32_bf16 v[140:143], a[136:139], v[160:163], v[140:143]\n"
"v_mfma_f32_16x16x32_bf16 v[140:143], a[140:143], v[100:103], v[140:143]\n"
"v_mfma_f32_16x16x32_bf16 v[140:143], a[144:147], v[164:167], v[140:143]\n"
"v_mfma_f32_16x16x32_bf16 v[140:143], a[148:151], v[104:107], v[140:143]\n"
"v_mfma_f32_16x16x32_bf16 v[140:143], a[152:155], v[108:111], v[140:143]\n"
"v_mfma_f32_16x16x32_bf16 v[140:143], a[156:159], v[128:131], v[140:143]\n"
"v_mfma_f32_16x16x32_bf16 v[176:179], a[188:191], v[128:131], 0\n"
"v_mfma_f32_16x16x32_bf16 v[176:179], a[184:187], v[108:111], v[176:179]\n"
"v_mfma_f32_16x16x32_bf16 v[176:179], a[180:183], v[104:107], v[176:179]\n"
"v_mfma_f32_16x16x32_bf16 v[176:179], a[176:179], v[164:167], v[176:179]\n"
"v_mfma_f32_16x16x32_bf16 v[176:179], a[172:175], v[100:103], v[176:179]\n"
"v_mfma_f32_16x16x32_bf16 v[176:179], a[168:171], v[160:163], v[176:179]\n"
"s_nop 7\n"
"s_branch .Lmk_p112r\n"
".Lmk_p113s:\n"
";\n"
"s_waitcnt vmcnt(16)\n"
"s_mov_b32 s1, 0x10000\n"
".Lmk_p113l:\n"
"s_add_u32 s97, s58, 0x40000\n"
"buffer_load_dwordx4 v[148:151], v0, s[4:7], s97 offen sc1\n"
"buffer_load_dwordx4 v[144:147], v0, s[4:7], s97 offen offset:1024 sc1\n"
"buffer_load_dwordx4 v[116:119], v0, s[4:7], s97 offen offset:2048 sc1\n"
"buffer_load_dwordx4 v[112:115], v0, s[4:7], s97 offen offset:3072 sc1\n"
"buffer_load_dwordx4 v[92:95], v1, s[4:7], s97 offen sc1\n"
"buffer_load_dwordx4 v[96:99], v1, s[4:7], s97 offen offset:1024 sc1\n"
"buffer_load_dwordx4 v[156:159], v1, s[4:7], s97 offen offset:2048 sc1\n"
"buffer_load_dwordx4 v[88:91], v1, s[4:7], s97 offen offset:3072 sc1\n"
"s_add_u32 s97, s59, 0x40000\n"
"buffer_load_dwordx4 v[172:175], v0, s[4:7], s97 offen sc1\n"
"buffer_load_dwordx4 v[124:127], v0, s[4:7], s97 offen offset:1024 sc1\n"
"buffer_load_dwordx4 v[120:123], v0, s[4:7], s97 offen offset:2048 sc1\n"
"buffer_load_dwordx4 v[136:139], v0, s[4:7], s97 offen offset:3072 sc1\n"
"buffer_load_dwordx4 v[152:155], v1, s[4:7], s97 offen sc1\n"
"buffer_load_dwordx4 v[168:171], v1, s[4:7], s97 offen offset:1024 sc1\n"
"buffer_load_dwordx4 v[180:183], v1, s[4:7], s97 offen offset:2048 sc1\n"
"buffer_load_dwordx4 v[140:143], v1, s[4:7], s97 offen offset:3072 sc1\n"
"s_waitcnt vmcnt(0)\n"
"v_max_u32_e32 v239, v148, v149\n"
"v_max3_u32 v239, v239, v150, v151\n"
"v_max3_u32 v239, v239, v144, v145\n"
"v_max3_u32 v239, v239, v146, v147\n"
"v_max3_u32 v239, v239, v116, v117\n"
"v_max3_u32 v239, v239, v118, v119\n"
"v_max3_u32 v239, v239, v112, v113\n"
"v_max3_u32 v239, v239, v114, v115\n"
"v_max3_u32 v239, v239, v92, v93\n"
"v_max3_u32 v239, v239, v94, v95\n"
"v_max3_u32 v239, v239, v96, v97\n"
"v_max3_u32 v239, v239, v98, v99\n"
"v_max3_u32 v239, v239, v156, v157\n"
"v_max3_u32 v239, v239, v158, v159\n"
"v_max3_u32 v239, v239, v88, v89\n"
"v_max3_u32 v239, v239, v90, v91\n"
"v_cmp_eq_u32_e32 vcc, -1, v239\n"
"s_cbranch_vccz .Lmk_p113k\n"
"s_sub_u32 s1, s1, 1\n"
"s_cbranch_scc0 .Lmk_p113l\n"
".Lmk_p113k:\n"
"v_mfma_f32_16x16x32_bf16 v[100:103], a[128:131], v[148:151], 0\n"
"v_mfma_f32_16x16x32_bf16 v[100:103], a[132:135], v[144:147], v[100:103]\n"
"v_mfma_f32_16x16x32_bf16 v[100:103], a[136:139], v[116:119], v[100:103]\n"
"v_mfma_f32_16x16x32_bf16 v[100:103], a[140:143], v[112:115], v[100:103]\n"
"v_mfma_f32_16x16x32_bf16 v[100:103], a[144:147], v[92:95], v[100:103]\n"
"v_mfma_f32_16x16x32_bf16 v[100:103], a[148:151], v[96:99], v[100:103]\n"
"v_mfma_f32_16x16x32_bf16 v[100:103], a[152:155], v[156:159], v[100:103]\n"
"v_mfma_f32_16x16x32_bf16 v[100:103], a[156:159], v[88:91], v[100:103]\n"
"v_mfma_f32_16x16x32_bf16 v[104:107], a[188:191], v[88:91], 0\n"
"v_mfma_f32_16x16x32_bf16 v[104:107], a[184:187], v[156:159], v[104:107]\n"
"v_mfma_f32_16x16x32_bf16 v[104:107], a[180:183], v[96:99], v[104:107]\n"
"v_mfma_f32_16x16x32_bf16 v[104:107], a[176:179], v[92:95], v[104:107]\n"
"v_mfma_f32_16x16x32_bf16 v[104:107], a[172:175], v[112:115], v[104:107]\n"
"v_mfma_f32_16x16x32_bf16 v[104:107], a[168:171], v[116:119], v[104:107]\n"
"s_nop 7\n"
"s_branch .Lmk_p113r\n"
".Lmk_p114s:\n"
";\n"
"s_waitcnt vmcnt(16)\n"
"s_mov_b32 s1, 0x10000\n"
".Lmk_p114l:\n"
"s_add_u32 s97, s59, 0x40000\n"
"buffer_load_dwordx4 v[172:175], v0, s[4:7], s97 offen sc1\n"
"buffer_load_dwordx4 v[124:127], v0, s[4:7], s97 offen offset:1024 sc1\n"
"buffer_load_dwordx4 v[120:123], v0, s[4:7], s97 offen offset:2048 sc1\n"
"buffer_load_dwordx4 v[136:139], v0, s[4:7], s97 offen offset:3072 sc1\n"
"buffer_load_dwordx4 v[152:155], v1, s[4:7], s97 offen sc1\n"
"buffer_load_dwordx4 v[168:171], v1, s[4:7], s97 offen offset:1024 sc1\n"
"buffer_load_dwordx4 v[180:183], v1, s[4:7], s97 offen offset:2048 sc1\n"
"buffer_load_dwordx4 v[140:143], v1, s[4:7], s97 offen offset:3072 sc1\n"
"s_add_u32 s97, s56, 0\n"
"buffer_load_dwordx4 v[100:103], v0, s[4:7], s97 offen sc1\n"
"buffer_load_dwordx4 v[176:179], v0, s[4:7], s97 offen offset:1024 sc1\n"
"buffer_load_dwordx4 v[104:107], v0, s[4:7], s97 offen offset:2048 sc1\n"
"buffer_load_dwordx4 v[88:91], v0, s[4:7], s97 offen offset:3072 sc1\n"
"buffer_load_dwordx4 v[92:95], v1, s[4:7], s97 offen sc1\n"
"buffer_load_dwordx4 v[116:119], v1, s[4:7], s97 offen offset:1024 sc1\n"
"buffer_load_dwordx4 v[132:135], v1, s[4:7], s97 offen offset:2048 sc1\n"
"buffer_load_dwordx4 v[144:147], v1, s[4:7], s97 offen offset:3072 sc1\n"
"s_add_u32 s97, s57, 0\n"
"buffer_load_dwordx4 v[156:159], v0, s[4:7], s97 offen sc1\n"
"s_waitcnt vmcnt(0)\n"
"v_max_u32_e32 v255, v172, v173\n"
"v_max3_u32 v255, v255, v174, v175\n"
"v_max3_u32 v255, v255, v124, v125\n"
"v_max3_u32 v255, v255, v126, v127\n"
"v_max3_u32 v255, v255, v120, v121\n"
"v_max3_u32 v255, v255, v122, v123\n"
"v_max3_u32 v255, v255, v136, v137\n"
"v_max3_u32 v255, v255, v138, v139\n"
"v_max3_u32 v255, v255, v152, v153\n"
"v_max3_u32 v255, v255, v154, v155\n"
"v_max3_u32 v255, v255, v168, v169\n"
"v_max3_u32 v255, v255, v170, v171\n"
"v_max3_u32 v255, v255, v180, v181\n"
"v_max3_u32 v255, v255, v182, v183\n"
"v_max3_u32 v255, v255, v140, v141\n"
"v_max3_u32 v255, v255, v142, v143\n"
"v_cmp_eq_u32_e32 vcc, -1, v255\n"
"s_cbranch_vccz .Lmk_p114k\n"
"s_sub_u32 s1, s1, 1\n"
"s_cbranch_scc0 .Lmk_p114l\n"
".Lmk_p114k:\n"
"v_mfma_f32_16x16x32_bf16 v[128:131], a[128:131], v[172:175], v[108:111]\n"
"v_mfma_f32_16x16x32_bf16 v[128:131], a[132:135], v[124:127], v[128:131]\n"
"v_mfma_f32_16x16x32_bf16 v[128:131], a[136:139], v[120:123], v[128:131]\n"
"v_mfma_f32_16x16x32_bf16 v[128:131], a[140:143], v[136:139], v[128:131]\n"
"v_mfma_f32_16x16x32_bf16 v[128:131], a[144:147], v[152:155], v[128:131]\n"
"v_mfma_f32_16x16x32_bf16 v[128:131], a[148:151], v[168:171], v[128:131]\n"
"v_mfma_f32_16x16x32_bf16 v[128:131], a[152:155], v[180:183], v[128:131]\n"
"v_mfma_f32_16x16x32_bf16 v[128:131], a[156:159], v[140:143], v[128:131]\n"
"v_mfma_f32_16x16x32_bf16 v[148:151], a[188:191], v[140:143], v[160:163]\n"
"v_mfma_f32_16x16x32_bf16 v[148:151], a[184:187], v[180:183], v[148:151]\n"
"v_mfma_f32_16x16x32_bf16 v[148:151], a[180:183], v[168:171], v[148:151]\n"
"v_mfma_f32_16x16x32_bf16 v[148:151], a[176:179], v[152:155], v[148:151]\n"
"v_mfma_f32_16x16x32_bf16 v[148:151], a[172:175], v[136:139], v[148:151]\n"
"v_mfma_f32_16x16x32_bf16 v[148:151], a[168:171], v[120:123], v[148:151]\n"
"s_nop 7\n"
"s_branch .Lmk_p114r\n"
".Lmk_p115s:\n"
";\n"
"s_waitcnt vmcnt(16)\n"
"s_mov_b32 s1, 0x10000\n"
".Lmk_p115l:\n"
"s_add_u32 s95, s56, 0\n"
"buffer_load_dwordx4 v[100:103], v0, s[4:7], s95 offen sc1\n"
"buffer_load_dwordx4 v[176:179], v0, s[4:7], s95 offen offset:1024 sc1\n"
"buffer_load_dwordx4 v[104:107], v0, s[4:7], s95 offen offset:2048 sc1\n"
"buffer_load_dwordx4 v[88:91], v0, s[4:7], s95 offen offset:3072 sc1\n"
"buffer_load_dwordx4 v[92:95], v1, s[4:7], s95 offen sc1\n"
"buffer_load_dwordx4 v[116:119], v1, s[4:7], s95 offen offset:1024 sc1\n"
"buffer_load_dwordx4 v[132:135], v1, s[4:7], s95 offen offset:2048 sc1\n"
"buffer_load_dwordx4 v[144:147], v1, s[4:7], s95 offen offset:3072 sc1\n"
"s_add_u32 s95, s57, 0\n"
"buffer_load_dwordx4 v[156:159], v0, s[4:7], s95 offen sc1\n"
"buffer_load_dwordx4 v[108:111], v0, s[4:7], s95 offen offset:1024 sc1\n"
"buffer_load_dwordx4 v[112:115], v0, s[4:7], s95 offen offset:2048 sc1\n"
"buffer_load_dwordx4 v[196:199], v0, s[4:7], s95 offen offset:3072 sc1\n"
"buffer_load_dwordx4 v[200:203], v1, s[4:7], s95 offen sc1\n"
"buffer_load_dwordx4 v[128:131], v1, s[4:7], s95 offen offset:1024 sc1\n"
"buffer_load_dwordx4 v[120:123], v1, s[4:7], s95 offen offset:2048 sc1\n"
"buffer_load_dwordx4 v[136:139], v1, s[4:7], s95 offen offset:3072 sc1\n"
"s_add_u32 s95, s58, 0\n"
"buffer_load_dwordx4 v[148:151], v0, s[4:7], s95 offen sc1\n"
"buffer_load_dwordx4 v[160:163], v0, s[4:7], s95 offen offset:1024 sc1\n"
"buffer_load_dwordx4 v[124:127], v0, s[4:7], s95 offen offset:2048 sc1\n"
"s_waitcnt vmcnt(0)\n"
"v_max_u32_e32 v255, v100, v101\n"
"v_max3_u32 v255, v255, v102, v103\n"
"v_max3_u32 v255, v255, v176, v177\n"
"v_max3_u32 v255, v255, v178, v179\n"
"v_max3_u32 v255, v255, v104, v105\n"
"v_max3_u32 v255, v255, v106, v107\n"
"v_max3_u32 v255, v255, v88, v89\n"
"v_max3_u32 v255, v255, v90, v91\n"
"v_max3_u32 v255, v255, v92, v93\n"
"v_max3_u32 v255, v255, v94, v95\n"
"v_max3_u32 v255, v255, v116, v117\n"
"v_max3_u32 v255, v255, v118, v119\n"
"v_max3_u32 v255, v255, v132, v133\n"
"v_max3_u32 v255, v255, v134, v135\n"
"v_max3_u32 v255, v255, v144, v145\n"
"v_max3_u32 v255, v255, v146, v147\n"
"v_cmp_eq_u32_e32 vcc, -1, v255\n"
"s_cbranch_vccz .Lmk_p115k\n"
"s_sub_u32 s1, s1, 1\n"
"s_cbranch_scc0 .Lmk_p115l\n"
".Lmk_p115k:\n"
"v_mfma_f32_16x16x32_bf16 v[140:143], a[128:131], v[100:103], 0\n"
"v_mfma_f32_16x16x32_bf16 v[140:143], a[132:135], v[176:179], v[140:143]\n"
"v_mfma_f32_16x16x32_bf16 v[140:143], a[136:139], v[104:107], v[140:143]\n"
"v_mfma_f32_16x16x32_bf16 v[140:143], a[140:143], v[88:91], v[140:143]\n"
"v_mfma_f32_16x16x32_bf16 v[140:143], a[144:147], v[92:95], v[140:143]\n"
"v_mfma_f32_16x16x32_bf16 v[140:143], a[148:151], v[116:119], v[140:143]\n"
"v_mfma_f32_16x16x32_bf16 v[140:143], a[152:155], v[132:135], v[140:143]\n"
"v_mfma_f32_16x16x32_bf16 v[140:143], a[156:159], v[144:147], v[140:143]\n"
"v_mfma_f32_16x16x32_bf16 v[96:99], a[188:191], v[144:147], 0\n"
"v_mfma_f32_16x16x32_bf16 v[96:99], a[184:187], v[132:135], v[96:99]\n"
"v_mfma_f32_16x16x32_bf16 v[96:99], a[180:183], v[116:119], v[96:99]\n"
"v_mfma_f32_16x16x32_bf16 v[96:99], a[176:179], v[92:95], v[96:99]\n"
"v_mfma_f32_16x16x32_bf16 v[96:99], a[172:175], v[88:91], v[96:99]\n"
"v_mfma_f32_16x16x32_bf16 v[96:99], a[168:171], v[104:107], v[96:99]\n"
"s_nop 7\n"
"s_branch .Lmk_p115r\n"
".Lmk_p116s:\n"
";\n"
"s_waitcnt vmcnt(16)\n"
"s_mov_b32 s1, 0x10000\n"
".Lmk_p116l:\n"
"s_add_u32 s97, s57, 0\n"
"buffer_load_dwordx4 v[156:159], v0, s[4:7], s97 offen sc1\n"
"buffer_load_dwordx4 v[108:111], v0, s[4:7], s97 offen offset:1024 sc1\n"
"buffer_load_dwordx4 v[112:115], v0, s[4:7], s97 offen offset:2048 sc1\n"
"buffer_load_dwordx4 v[196:199], v0, s[4:7], s97 offen offset:3072 sc1\n"
"buffer_load_dwordx4 v[200:203], v1, s[4:7], s97 offen sc1\n"
"buffer_load_dwordx4 v[128:131], v1, s[4:7], s97 offen offset:1024 sc1\n"
"buffer_load_dwordx4 v[120:123], v1, s[4:7], s97 offen offset:2048 sc1\n"
"buffer_load_dwordx4 v[136:139], v1, s[4:7], s97 offen offset:3072 sc1\n"
"s_add_u32 s97, s58, 0\n"
"buffer_load_dwordx4 v[148:151], v0, s[4:7], s97 offen sc1\n"
"buffer_load_dwordx4 v[160:163], v0, s[4:7], s97 offen offset:1024 sc1\n"
"buffer_load_dwordx4 v[124:127], v0, s[4:7], s97 offen offset:2048 sc1\n"
"buffer_load_dwordx4 v[140:143], v0, s[4:7], s97 offen offset:3072 sc1\n"
"buffer_load_dwordx4 v[152:155], v1, s[4:7], s97 offen sc1\n"
"buffer_load_dwordx4 v[164:167], v1, s[4:7], s97 offen offset:1024 sc1\n"
"buffer_load_dwordx4 v[116:119], v1, s[4:7], s97 offen offset:2048 sc1\n"
"buffer_load_dwordx4 v[132:135], v1, s[4:7], s97 offen offset:3072 sc1\n"
"s_add_u32 s97, s59, 0\n"
"buffer_load_dwordx4 v[172:175], v0, s[4:7], s97 offen sc1\n"
"buffer_load_dwordx4 v[176:179], v0, s[4:7], s97 offen offset:1024 sc1\n"
"buffer_load_dwordx4 v[168:171], v0, s[4:7], s97 offen offset:2048 sc1\n"
"s_waitcnt vmcnt(0)\n"
"v_max_u32_e32 v255, v156, v157\n"
"v_max3_u32 v255, v255, v158, v159\n"
"v_max3_u32 v255, v255, v108, v109\n"
"v_max3_u32 v255, v255, v110, v111\n"
"v_max3_u32 v255, v255, v112, v113\n"
"v_max3_u32 v255, v255, v114, v115\n"
"v_max3_u32 v255, v255, v196, v197\n"
"v_max3_u32 v255, v255, v198, v199\n"
"v_max3_u32 v255, v255, v200, v201\n"
"v_max3_u32 v255, v255, v202, v203\n"
"v_max3_u32 v255, v255, v128, v129\n"
"v_max3_u32 v255, v255, v130, v131\n"
"v_max3_u32 v255, v255, v120, v121\n"
"v_max3_u32 v255, v255, v122, v123\n"
"v_max3_u32 v255, v255, v136, v137\n"
"v_max3_u32 v255, v255, v138, v139\n"
"v_cmp_eq_u32_e32 vcc, -1, v255\n"
"s_cbranch_vccz .Lmk_p116k\n"
"s_sub_u32 s1, s1, 1\n"
"s_cbranch_scc0 .Lmk_p116l\n"
".Lmk_p116k:\n"
"v_mfma_f32_16x16x32_bf16 v[88:91], a[128:131], v[156:159], 0\n"
"v_mfma_f32_16x16x32_bf16 v[88:91], a[132:135], v[108:111], v[88:91]\n"
"v_mfma_f32_16x16x32_bf16 v[88:91], a[136:139], v[112:115], v[88:91]\n"
"v_mfma_f32_16x16x32_bf16 v[88:91], a[140:143], v[196:199], v[88:91]\n"
"v_mfma_f32_16x16x32_bf16 v[88:91], a[144:147], v[200:203], v[88:91]\n"
"v_mfma_f32_16x16x32_bf16 v[88:91], a[148:151], v[128:131], v[88:91]\n"
"v_mfma_f32_16x16x32_bf16 v[88:91], a[152:155], v[120:123], v[88:91]\n"
"v_mfma_f32_16x16x32_bf16 v[88:91], a[156:159], v[136:139], v[88:91]\n"
"v_mfma_f32_16x16x32_bf16 v[92:95], a[188:191], v[136:139], 0\n"
"v_mfma_f32_16x16x32_bf16 v[92:95], a[184:187], v[120:123], v[92:95]\n"
"v_mfma_f32_16x16x32_bf16 v[92:95], a[180:183], v[128:131], v[92:95]\n"
"v_mfma_f32_16x16x32_bf16 v[92:95], a[176:179], v[200:203], v[92:95]\n"
"v_mfma_f32_16x16x32_bf16 v[92:95], a[172:175], v[196:199], v[92:95]\n"
"v_mfma_f32_16x16x32_bf16 v[92:95], a[168:171], v[112:115], v[92:95]\n"
"s_nop 7\n"
"s_branch .Lmk_p116r\n"
".Lmk_p117s:\n"
";\n"
"s_waitcnt vmcnt(16)\n"
"s_mov_b32 s1, 0x10000\n"
".Lmk_p117l:\n"
"s_add_u32 s97, s58, 0\n"
"buffer_load_dwordx4 v[148:151], v0, s[4:7], s97 offen sc1\n"
"buffer_load_dwordx4 v[160:163], v0, s[4:7], s97 offen offset:1024 sc1\n"
"buffer_load_dwordx4 v[124:127], v0, s[4:7], s97 offen offset:2048 sc1\n"
"buffer_load_dwordx4 v[140:143], v0, s[4:7], s97 offen offset:3072 sc1\n"
"buffer_load_dwordx4 v[152:155], v1, s[4:7], s97 offen sc1\n"
"buffer_load_dwordx4 v[164:167], v1, s[4:7], s97 offen offset:1024 sc1\n"
"buffer_load_dwordx4 v[116:119], v1, s[4:7], s97 offen offset:2048 sc1\n"
"buffer_load_dwordx4 v[132:135], v1, s[4:7], s97 offen offset:3072 sc1\n"
"s_add_u32 s97, s59, 0\n"
"buffer_load_dwordx4 v[172:175], v0, s[4:7], s97 offen sc1\n"
"buffer_load_dwordx4 v[176:179], v0, s[4:7], s97 offen offset:1024 sc1\n"
"buffer_load_dwordx4 v[168:171], v0, s[4:7], s97 offen offset:2048 sc1\n"
"buffer_load_dwordx4 v[180:183], v0, s[4:7], s97 offen offset:3072 sc1\n"
"buffer_load_dwordx4 v[184:187], v1, s[4:7], s97 offen sc1\n"
"buffer_load_dwordx4 v[188:191], v1, s[4:7], s97 offen offset:1024 sc1\n"
"buffer_load_dwordx4 v[192:195], v1, s[4:7], s97 offen offset:2048 sc1\n"
"buffer_load_dwordx4 v[196:199], v1, s[4:7], s97 offen offset:3072 sc1\n"
"s_add_u32 s97, s52, 0x440000\n"
"buffer_load_dwordx4 v[88:91], v0, s[4:7], s97 offen sc1\n"
"buffer_load_dwordx4 v[92:95], v0, s[4:7], s97 offen offset:1024 sc1\n"
"s_waitcnt vmcnt(0)\n"
"v_max_u32_e32 v239, v148, v149\n"
"v_max3_u32 v239, v239, v150, v151\n"
"v_max3_u32 v239, v239, v160, v161\n"
"v_max3_u32 v239, v239, v162, v163\n"
"v_max3_u32 v239, v239, v124, v125\n"
"v_max3_u32 v239, v239, v126, v127\n"
"v_max3_u32 v239, v239, v140, v141\n"
"v_max3_u32 v239, v239, v142, v143\n"
"v_max3_u32 v239, v239, v152, v153\n"
"v_max3_u32 v239, v239, v154, v155\n"
"v_max3_u32 v239, v239, v164, v165\n"
"v_max3_u32 v239, v239, v166, v167\n"
"v_max3_u32 v239, v239, v116, v117\n"
"v_max3_u32 v239, v239, v118, v119\n"
"v_max3_u32 v239, v239, v132, v133\n"
"v_max3_u32 v239, v239, v134, v135\n"
"v_cmp_eq_u32_e32 vcc, -1, v239\n"
"s_cbranch_vccz .Lmk_p117k\n"
"s_sub_u32 s1, s1, 1\n"
"s_cbranch_scc0 .Lmk_p117l\n"
".Lmk_p117k:\n"
"v_mfma_f32_16x16x32_bf16 v[96:99], a[128:131], v[148:151], 0\n"
"v_mfma_f32_16x16x32_bf16 v[96:99], a[132:135], v[160:163], v[96:99]\n"
"v_mfma_f32_16x16x32_bf16 v[96:99], a[136:139], v[124:127], v[96:99]\n"
"v_mfma_f32_16x16x32_bf16 v[96:99], a[140:143], v[140:143], v[96:99]\n"
"v_mfma_f32_16x16x32_bf16 v[96:99], a[144:147], v[152:155], v[96:99]\n"
"v_mfma_f32_16x16x32_bf16 v[96:99], a[148:151], v[164:167], v[96:99]\n"
"v_mfma_f32_16x16x32_bf16 v[96:99], a[152:155], v[116:119], v[96:99]\n"
"v_mfma_f32_16x16x32_bf16 v[96:99], a[156:159], v[132:135], v[96:99]\n"
"v_mfma_f32_16x16x32_bf16 v[104:107], a[188:191], v[132:135], 0\n"
"v_mfma_f32_16x16x32_bf16 v[104:107], a[184:187], v[116:119], v[104:107]\n"
"v_mfma_f32_16x16x32_bf16 v[104:107], a[180:183], v[164:167], v[104:107]\n"
"v_mfma_f32_16x16x32_bf16 v[104:107], a[176:179], v[152:155], v[104:107]\n"
"v_mfma_f32_16x16x32_bf16 v[104:107], a[172:175], v[140:143], v[104:107]\n"
"v_mfma_f32_16x16x32_bf16 v[104:107], a[168:171], v[124:127], v[104:107]\n"
"s_nop 7\n"
"s_branch .Lmk_p117r\n"
".Lmk_p118s:\n"
";\n"
"s_waitcnt vmcnt(16)\n"
"s_mov_b32 s1, 0x10000\n"
".Lmk_p118l:\n"
"s_add_u32 s97, s59, 0\n"
"buffer_load_dwordx4 v[172:175], v0, s[4:7], s97 offen sc1\n"
"buffer_load_dwordx4 v[176:179], v0, s[4:7], s97 offen offset:1024 sc1\n"
"buffer_load_dwordx4 v[168:171], v0, s[4:7], s97 offen offset:2048 sc1\n"
"buffer_load_dwordx4 v[180:183], v0, s[4:7], s97 offen offset:3072 sc1\n"
"buffer_load_dwordx4 v[184:187], v1, s[4:7], s97 offen sc1\n"
"buffer_load_dwordx4 v[188:191], v1, s[4:7], s97 offen offset:1024 sc1\n"
"buffer_load_dwordx4 v[192:195], v1, s[4:7], s97 offen offset:2048 sc1\n"
"buffer_load_dwordx4 v[196:199], v1, s[4:7], s97 offen offset:3072 sc1\n"
"s_add_u32 s97, s52, 0x440000\n"
"buffer_load_dwordx4 v[88:91], v0, s[4:7], s97 offen sc1\n"
"buffer_load_dwordx4 v[92:95], v0, s[4:7], s97 offen offset:1024 sc1\n"
"buffer_load_dwordx4 v[96:99], v0, s[4:7], s97 offen offset:2048 sc1\n"
"buffer_load_dwordx4 v[100:103], v0, s[4:7], s97 offen offset:3072 sc1\n"
"buffer_load_dwordx4 v[104:107], v1, s[4:7], s97 offen sc1\n"
"buffer_load_dwordx4 v[112:115], v1, s[4:7], s97 offen offset:1024 sc1\n"
"buffer_load_dwordx4 v[116:119], v1, s[4:7], s97 offen offset:2048 sc1\n"
"buffer_load_dwordx4 v[124:127], v1, s[4:7], s97 offen offset:3072 sc1\n"
"s_add_u32 s97, s53, 0x440000\n"
"buffer_load_dwordx4 v[128:131], v0, s[4:7], s97 offen sc1\n"
"buffer_load_dwordx4 v[132:135], v0, s[4:7], s97 offen offset:1024 sc1\n"
"buffer_load_dwordx4 v[136:139], v0, s[4:7], s97 offen offset:2048 sc1\n"
"s_waitcnt vmcnt(0)\n"
"v_max_u32_e32 v255, v172, v173\n"
"v_max3_u32 v255, v255, v174, v175\n"
"v_max3_u32 v255, v255, v176, v177\n"
"v_max3_u32 v255, v255, v178, v179\n"
"v_max3_u32 v255, v255, v168, v169\n"
"v_max3_u32 v255, v255, v170, v171\n"
"v_max3_u32 v255, v255, v180, v181\n"
"v_max3_u32 v255, v255, v182, v183\n"
"v_max3_u32 v255, v255, v184, v185\n"
"v_max3_u32 v255, v255, v186, v187\n"
"v_max3_u32 v255, v255, v188, v189\n"
"v_max3_u32 v255, v255, v190, v191\n"
"v_max3_u32 v255, v255, v192, v193\n"
"v_max3_u32 v255, v255, v194, v195\n"
"v_max3_u32 v255, v255, v196, v197\n"
"v_max3_u32 v255, v255, v198, v199\n"
"v_cmp_eq_u32_e32 vcc, -1, v255\n"
"s_cbranch_vccz .Lmk_p118k\n"
"s_sub_u32 s1, s1, 1\n"
"s_cbranch_scc0 .Lmk_p118l\n"
".Lmk_p118k:\n"
"v_mfma_f32_16x16x32_bf16 v[164:167], a[128:131], v[172:175], v[144:147]\n"
"v_mfma_f32_16x16x32_bf16 v[164:167], a[132:135], v[176:179], v[164:167]\n"
"v_mfma_f32_16x16x32_bf16 v[164:167], a[136:139], v[168:171], v[164:167]\n"
"v_mfma_f32_16x16x32_bf16 v[164:167], a[140:143], v[180:183], v[164:167]\n"
"v_mfma_f32_16x16x32_bf16 v[164:167], a[144:147], v[184:187], v[164:167]\n"
"v_mfma_f32_16x16x32_bf16 v[164:167], a[148:151], v[188:191], v[164:167]\n"
"v_mfma_f32_16x16x32_bf16 v[164:167], a[152:155], v[192:195], v[164:167]\n"
"v_mfma_f32_16x16x32_bf16 v[164:167], a[156:159], v[196:199], v[164:167]\n"
"v_mfma_f32_16x16x32_bf16 v[200:203], a[188:191], v[196:199], v[156:159]\n"
"v_mfma_f32_16x16x32_bf16 v[200:203], a[184:187], v[192:195], v[200:203]\n"
"v_mfma_f32_16x16x32_bf16 v[200:203], a[180:183], v[188:191], v[200:203]\n"
"v_mfma_f32_16x16x32_bf16 v[200:203], a[176:179], v[184:187], v[200:203]\n"
"v_mfma_f32_16x16x32_bf16 v[200:203], a[172:175], v[180:183], v[200:203]\n"
"v_mfma_f32_16x16x32_bf16 v[200:203], a[168:171], v[168:171], v[200:203]\n"
"s_nop 7\n"
"s_branch .Lmk_p118r\n"
".Lmk_exit:\n"
"v_xor_b32_e32 v255, 32, v15\n"
"ds_read_b128 v[36:39], v2 offset:17408\n"
"ds_read_b128 v[40:43], v15 offset:16384\n"
"ds_read_b128 v[52:55], v255 offset:16640\n"
"v_xor_b32_e32 v255, 32, v17\n"
"ds_read_b128 v[60:63], v2 offset:16384\n"
"ds_read_b128 v[64:67], v17 offset:15360\n"
"ds_read_b128 v[68:71], v255 offset:15616\n"
"v_xor_b32_e32 v255, 32, v16\n"
"ds_read_b64_tr_b16 v[72:73], v16 offset:16384\n"
"ds_read_b64_tr_b16 v[74:75], v255 offset:16640\n"
"ds_read_b64_tr_b16 v[76:77], v255 offset:16384\n"
"ds_read_b64_tr_b16 v[78:79], v16 offset:16640\n"
"v_mov_b32_e32 v80, 0x3f803f80\n"
"v_mov_b32_e32 v81, 0x3f803f80\n"
"v_mov_b32_e32 v82, 0x3f803f80\n"
"v_mov_b32_e32 v83, 0x3f803f80\n"
"v_xor_b32_e32 v255, 32, v16\n"
"ds_read_b64_tr_b16 v[84:85], v16 offset:15360\n"
"ds_read_b64_tr_b16 v[86:87], v255 offset:15616\n"
"s_waitcnt lgkmcnt(13)\n"
"ds_read_b64_tr_b16 v[96:97], v255 offset:15360\n"
"ds_read_b64_tr_b16 v[98:99], v16 offset:15616\n"
"v_mov_b32_e32 v100, 0x3f803f80\n"
"v_mov_b32_e32 v101, 0x3f803f80\n"
"v_mov_b32_e32 v102, 0x3f803f80\n"
"v_mov_b32_e32 v103, 0x3f803f80\n"
"s_waitcnt lgkmcnt(11)\n"
"v_mfma_f32_16x16x32_bf16 v[104:107], v[40:43], v[36:39], v[104:107]\n"
"v_mfma_f32_16x16x32_bf16 v[124:127], v[52:55], v[36:39], v[124:127]\n"
"s_waitcnt lgkmcnt(8)\n"
"v_mfma_f32_16x16x32_bf16 v[88:91], v[64:67], v[60:63], v[88:91]\n"
"v_mfma_f32_16x16x32_bf16 v[176:179], v[68:71], v[60:63], v[176:179]\n"
"s_nop 3\n"
"v_max_f32_e32 v255, v104, v105\n"
"s_nop 1\n"
"v_max_f32_e32 v254, v88, v89\n"
"v_max3_f32 v255, v255, v106, v107\n"
"v_max3_f32 v254, v254, v90, v91\n"
"v_max3_f32 v255, v255, v124, v125\n"
"v_max3_f32 v254, v254, v176, v177\n"
"v_max3_f32 v255, v255, v126, v127\n"
"v_max3_f32 v254, v254, v178, v179\n"
"v_mov_b32_e32 v253, v255\n"
"v_mov_b32_e32 v252, v254\n"
"s_nop 0\n"
"v_permlane16_swap_b32_e32 v255, v253\n"
"v_permlane16_swap_b32_e32 v254, v252\n"
"v_max_f32_e32 v255, v255, v253\n"
"v_max_f32_e32 v254, v254, v252\n"
"v_mov_b32_e32 v253, v255\n"
"v_mov_b32_e32 v252, v254\n"
"s_nop 0\n"
"v_permlane32_swap_b32_e32 v255, v253\n"
"v_permlane32_swap_b32_e32 v254, v252\n"
"v_max_f32_e32 v255, v255, v253\n"
"v_max_f32_e32 v254, v254, v252\n"
"v_sub_f32_e32 v104, v104, v255\n"
"v_sub_f32_e32 v105, v105, v255\n"
"v_sub_f32_e32 v106, v106, v255\n"
"v_sub_f32_e32 v107, v107, v255\n"
"v_sub_f32_e32 v124, v124, v255\n"
"v_sub_f32_e32 v125, v125, v255\n"
"v_sub_f32_e32 v126, v126, v255\n"
"v_sub_f32_e32 v127, v127, v255\n"
"v_exp_f32_e32 v104, v104\n"
"v_exp_f32_e32 v105, v105\n"
"v_exp_f32_e32 v106, v106\n"
"v_exp_f32_e32 v107, v107\n"
"v_exp_f32_e32 v124, v124\n"
"v_exp_f32_e32 v125, v125\n"
"v_exp_f32_e32 v126, v126\n"
"v_exp_f32_e32 v127, v127\n"
"v_cvt_pk_bf16_f32 v36, v104, v105\n"
"v_cvt_pk_bf16_f32 v37, v106, v107\n"
"v_cvt_pk_bf16_f32 v38, v124, v125\n"
"v_cvt_pk_bf16_f32 v39, v126, v127\n"
"s_waitcnt lgkmcnt(4)\n"
"v_sub_f32_e32 v88, v88, v254\n"
"v_sub_f32_e32 v89, v89, v254\n"
"v_sub_f32_e32 v90, v90, v254\n"
"v_sub_f32_e32 v91, v91, v254\n"
"v_sub_f32_e32 v176, v176, v254\n"
"v_sub_f32_e32 v177, v177, v254\n"
"v_sub_f32_e32 v178, v178, v254\n"
"v_mfma_f32_16x16x32_bf16 v[40:43], v[80:83], v[36:39], 0\n"
"v_sub_f32_e32 v179, v179, v254\n"
"v_exp_f32_e32 v88, v88\n"
"v_exp_f32_e32 v89, v89\n"
"v_exp_f32_e32 v90, v90\n"
"v_mfma_f32_16x16x32_bf16 v[52:55], v[72:75], v[36:39], 0\n"
"v_exp_f32_e32 v91, v91\n"
"v_exp_f32_e32 v176, v176\n"
"v_exp_f32_e32 v177, v177\n"
"v_exp_f32_e32 v178, v178\n"
"v_mfma_f32_16x16x32_bf16 v[60:63], v[76:79], v[36:39], 0\n"
"v_exp_f32_e32 v179, v179\n"
"v_cvt_pk_bf16_f32 v36, v88, v89\n"
"v_cvt_pk_bf16_f32 v37, v90, v91\n"
"v_cvt_pk_bf16_f32 v38, v176, v177\n"
"v_cvt_pk_bf16_f32 v39, v178, v179\n"
"s_waitcnt lgkmcnt(0)\n"
"v_rcp_f32_e32 v40, v40\n"
"s_nop 0\n"
"v_mul_f32_e32 v52, v52, v40\n"
"v_mul_f32_e32 v53, v53, v40\n"
"v_mfma_f32_16x16x32_bf16 v[64:67], v[100:103], v[36:39], 0\n"
"v_mul_f32_e32 v54, v54, v40\n"
"v_mul_f32_e32 v55, v55, v40\n"
"v_mul_f32_e32 v60, v60, v40\n"
"v_mfma_f32_16x16x32_bf16 v[68:71], v[84:87], v[36:39], 0\n"
"v_mul_f32_e32 v61, v61, v40\n"
"v_mul_f32_e32 v62, v62, v40\n"
"v_mul_f32_e32 v63, v63, v40\n"
"v_cvt_pk_bf16_f32 v40, v52, v53\n"
"v_mfma_f32_16x16x32_bf16 v[72:75], v[96:99], v[36:39], 0\n"
"v_cvt_pk_bf16_f32 v41, v54, v55\n"
"v_cvt_pk_bf16_f32 v42, v60, v61\n"
"v_cvt_pk_bf16_f32 v43, v62, v63\n"
"s_add_u32 s97, s60, 0x408000\n"
"buffer_store_dwordx4 v[40:43], v0, s[4:7], s97 offen sc1\n"
"v_rcp_f32_e32 v64, v64\n"
"s_nop 0\n"
"v_mul_f32_e32 v68, v68, v64\n"
"v_mul_f32_e32 v69, v69, v64\n"
"v_mul_f32_e32 v70, v70, v64\n"
"v_mul_f32_e32 v71, v71, v64\n"
"v_mul_f32_e32 v72, v72, v64\n"
"v_mul_f32_e32 v73, v73, v64\n"
"v_mul_f32_e32 v74, v74, v64\n"
"v_mul_f32_e32 v75, v75, v64\n"
"v_cvt_pk_bf16_f32 v36, v68, v69\n"
"v_cvt_pk_bf16_f32 v37, v70, v71\n"
"v_cvt_pk_bf16_f32 v38, v72, v73\n"
"v_cvt_pk_bf16_f32 v39, v74, v75\n"
"s_add_u32 s97, s60, 0x400000\n"
"buffer_store_dwordx4 v[36:39], v0, s[4:7], s97 offen sc1\n"
";\n"
"s_waitcnt vmcnt(23)\n"
"; @B G2 b.0\n"
"s_nop 0\n"
"v_mfma_f32_16x16x32_bf16 v[36:39], a[128:131], v[120:123], 0\n"
"s_add_u32 s97, s58, 0x440000\n"
"buffer_load_dwordx4 v[40:43], v0, s[4:7], s97 offen sc1\n"
"v_mfma_f32_16x16x32_bf16 v[36:39], a[132:135], v[136:139], v[36:39]\n"
"s_waitcnt vmcnt(22)\n"
"v_mfma_f32_16x16x32_bf16 v[36:39], a[136:139], v[144:147], v[36:39]\n"
"v_mfma_f32_16x16x32_bf16 v[36:39], a[140:143], v[192:195], v[36:39]\n"
"s_waitcnt vmcnt(20)\n"
"v_mfma_f32_16x16x32_bf16 v[36:39], a[144:147], v[196:199], v[36:39]\n"
"buffer_load_dwordx4 v[52:55], v0, s[4:7], s97 offen offset:1024 sc1\n"
"v_mfma_f32_16x16x32_bf16 v[36:39], a[148:151], v[188:191], v[36:39]\n"
"s_waitcnt vmcnt(19)\n"
"v_mfma_f32_16x16x32_bf16 v[36:39], a[152:155], v[108:111], v[36:39]\n"
"v_mfma_f32_16x16x32_bf16 v[36:39], a[156:159], v[92:95], v[36:39]\n"
"v_mfma_f32_16x16x32_bf16 v[60:63], a[188:191], v[92:95], 0\n"
"buffer_load_dwordx4 v[64:67], v0, s[4:7], s97 offen offset:2048 sc1\n"
"v_mfma_f32_16x16x32_bf16 v[60:63], a[184:187], v[108:111], v[60:63]\n"
"v_mfma_f32_16x16x32_bf16 v[60:63], a[180:183], v[188:191], v[60:63]\n"
"v_mfma_f32_16x16x32_bf16 v[60:63], a[176:179], v[196:199], v[60:63]\n"
"v_mfma_f32_16x16x32_bf16 v[60:63], a[172:175], v[192:195], v[60:63]\n"
"s_nop 1\n"
"v_cmp_u_f32_e32 vcc, v36, v36\n"
"v_mfma_f32_16x16x32_bf16 v[60:63], a[168:171], v[144:147], v[60:63]\n"
"s_cbranch_vccnz .Lmk_p60s\n"
".Lmk_p60r:\n"
"v_mfma_f32_16x16x32_bf16 v[60:63], a[164:167], v[136:139], v[60:63]\n"
"ds_write_b128 v3, v[36:39]\n"
"v_mfma_f32_16x16x32_bf16 v[60:63], a[160:163], v[120:123], v[60:63]\n"
"buffer_load_dwordx4 v[36:39], v0, s[4:7], s97 offen offset:3072 sc1\n"
"v_mfma_f32_16x16x32_bf16 v[68:71], a[192:195], v[120:123], 0\n"
"v_bfe_u32 v255, v0, 4, 4\n"
"v_bfe_u32 v254, v0, 9, 1\n"
"v_mfma_f32_16x16x32_bf16 v[68:71], a[196:199], v[136:139], v[68:71]\n"
"v_and_b32_e32 v253, 7, v255\n"
"v_xor_b32_e32 v254, v254, v253\n"
"v_mfma_f32_16x16x32_bf16 v[68:71], a[200:203], v[144:147], v[68:71]\n"
"v_lshlrev_b32_e32 v255, 7, v255\n"
"v_lshl_add_u32 v255, v254, 4, v255\n"
"v_mfma_f32_16x16x32_bf16 v[68:71], a[204:207], v[192:195], v[68:71]\n"
"buffer_load_dwordx4 v[72:75], v1, s[4:7], s97 offen sc1\n"
"v_mfma_f32_16x16x32_bf16 v[68:71], a[208:211], v[196:199], v[68:71]\n"
"v_bfe_u32 v254, v0, 8, 1\n"
"v_lshl_add_u32 v255, v254, 3, v255\n"
"v_mfma_f32_16x16x32_bf16 v[68:71], a[212:215], v[188:191], v[68:71]\n"
"ds_write_b128 v3, v[60:63] offset:1024\n"
"v_mfma_f32_16x16x32_bf16 v[68:71], a[216:219], v[108:111], v[68:71]\n"
"v_add_u32_e32 v255, s77, v255\n"
"v_bfe_u32 v253, v0, 7, 3\n"
"v_mfma_f32_16x16x32_bf16 v[68:71], a[220:223], v[92:95], v[68:71]\n"
"buffer_load_dwordx4 v[60:63], v1, s[4:7], s97 offen offset:1024 sc1\n"
"v_mfma_f32_16x16x32_bf16 v[76:79], a[252:255], v[92:95], 0\n"
"v_bfe_u32 v254, v0, 4, 3\n"
"v_xor_b32_e32 v254, v254, v253\n"
"v_mfma_f32_16x16x32_bf16 v[76:79], a[248:251], v[108:111], v[76:79]\n"
"v_lshlrev_b32_e32 v253, 11, v253\n"
"v_lshl_add_u32 v253, v254, 4, v253\n"
"v_mfma_f32_16x16x32_bf16 v[76:79], a[244:247], v[188:191], v[76:79]\n"
"v_mfma_f32_16x16x32_bf16 v[76:79], a[240:243], v[196:199], v[76:79]\n"
"buffer_load_dwordx4 v[80:83], v1, s[4:7], s97 offen offset:2048 sc1\n"
"v_mfma_f32_16x16x32_bf16 v[76:79], a[236:239], v[192:195], v[76:79]\n"
"v_mfma_f32_16x16x32_bf16 v[76:79], a[232:235], v[144:147], v[76:79]\n"
"ds_write_b128 v3, v[68:71] offset:2048\n"
"v_mfma_f32_16x16x32_bf16 v[76:79], a[228:231], v[136:139], v[76:79]\n"
"v_mfma_f32_16x16x32_bf16 v[76:79], a[224:227], v[120:123], v[76:79]\n"
"s_waitcnt vmcnt(20)\n"
"buffer_load_dwordx4 v[68:71], v1, s[4:7], s97 offen offset:3072 sc1\n"
"; @B G2 b.1\n"
"v_mfma_f32_16x16x32_bf16 v[84:87], a[128:131], v[152:155], 0\n"
"v_mfma_f32_16x16x32_bf16 v[84:87], a[132:135], v[180:183], v[84:87]\n"
"s_add_u32 s97, s59, 0x440000\n"
"v_mfma_f32_16x16x32_bf16 v[84:87], a[136:139], v[24:27], v[84:87]\n"
"v_mfma_f32_16x16x32_bf16 v[84:87], a[140:143], v[32:35], v[84:87]\n"
"s_waitcnt vmcnt(15)\n"
"buffer_load_dwordx4 v[96:99], v0, s[4:7], s97 offen sc1\n"
"v_mfma_f32_16x16x32_bf16 v[84:87], a[144:147], v[44:47], v[84:87]\n"
"v_mfma_f32_16x16x32_bf16 v[84:87], a[148:151], v[48:51], v[84:87]\n"
"ds_write_b128 v3, v[76:79] offset:3072\n"
"v_mfma_f32_16x16x32_bf16 v[84:87], a[152:155], v[28:31], v[84:87]\n"
"v_mfma_f32_16x16x32_bf16 v[84:87], a[156:159], v[56:59], v[84:87]\n"
"buffer_load_dwordx4 v[76:79], v0, s[4:7], s97 offen offset:1024 sc1\n"
"v_mfma_f32_16x16x32_bf16 v[100:103], a[188:191], v[56:59], 0\n"
"v_mfma_f32_16x16x32_bf16 v[100:103], a[184:187], v[28:31], v[100:103]\n"
"v_mfma_f32_16x16x32_bf16 v[100:103], a[180:183], v[48:51], v[100:103]\n"
"v_mfma_f32_16x16x32_bf16 v[100:103], a[176:179], v[44:47], v[100:103]\n"
"buffer_load_dwordx4 v[112:115], v0, s[4:7], s97 offen offset:2048 sc1\n"
"v_mfma_f32_16x16x32_bf16 v[100:103], a[172:175], v[32:35], v[100:103]\n"
"s_nop 0\n"
"v_cmp_u_f32_e32 vcc, v84, v84\n"
"v_mfma_f32_16x16x32_bf16 v[100:103], a[168:171], v[24:27], v[100:103]\n"
"s_cbranch_vccnz .Lmk_p61s\n"
".Lmk_p61r:\n"
"v_mfma_f32_16x16x32_bf16 v[100:103], a[164:167], v[180:183], v[100:103]\n"
"ds_write_b128 v3, v[84:87] offset:6144\n"
"v_mfma_f32_16x16x32_bf16 v[100:103], a[160:163], v[152:155], v[100:103]\n"
"buffer_load_dwordx4 v[84:87], v0, s[4:7], s97 offen offset:3072 sc1\n"
"v_mfma_f32_16x16x32_bf16 v[116:119], a[192:195], v[152:155], 0\n"
"v_mfma_f32_16x16x32_bf16 v[116:119], a[196:199], v[180:183], v[116:119]\n"
"v_mfma_f32_16x16x32_bf16 v[116:119], a[200:203], v[24:27], v[116:119]\n"
"v_mfma_f32_16x16x32_bf16 v[116:119], a[204:207], v[32:35], v[116:119]\n"
"buffer_load_dwordx4 v[128:131], v1, s[4:7], s97 offen sc1\n"
"v_mfma_f32_16x16x32_bf16 v[116:119], a[208:211], v[44:47], v[116:119]\n"
"ds_read_b128 v[132:135], v2 offset:8192\n"
"ds_read_b128 v[140:143], v2 offset:9216\n"
"v_mfma_f32_16x16x32_bf16 v[116:119], a[212:215], v[48:51], v[116:119]\n"
"ds_write_b128 v3, v[100:103] offset:7168\n"
"v_mfma_f32_16x16x32_bf16 v[116:119], a[216:219], v[28:31], v[116:119]\n"
"v_mfma_f32_16x16x32_bf16 v[116:119], a[220:223], v[56:59], v[116:119]\n"
"buffer_load_dwordx4 v[100:103], v1, s[4:7], s97 offen offset:1024 sc1\n"
"v_mfma_f32_16x16x32_bf16 v[148:151], a[252:255], v[56:59], 0\n"
"v_mfma_f32_16x16x32_bf16 v[148:151], a[248:251], v[28:31], v[148:151]\n"
"s_waitcnt lgkmcnt(1)\n"
"v_lshlrev_b32_e32 v254, 16, v132\n"
"v_and_b32_e32 v252, 0xffff0000, v132\n"
"v_lshlrev_b32_e32 v251, 16, v133\n"
"v_mfma_f32_16x16x32_bf16 v[148:151], a[244:247], v[48:51], v[148:151]\n"
"v_and_b32_e32 v250, 0xffff0000, v133\n"
"v_lshlrev_b32_e32 v249, 16, v134\n"
"v_and_b32_e32 v248, 0xffff0000, v134\n"
"v_mfma_f32_16x16x32_bf16 v[148:151], a[240:243], v[44:47], v[148:151]\n"
"buffer_load_dwordx4 v[156:159], v1, s[4:7], s97 offen offset:2048 sc1\n"
"v_lshlrev_b32_e32 v247, 16, v135\n"
"v_mfma_f32_16x16x32_bf16 v[148:151], a[236:239], v[32:35], v[148:151]\n"
"v_and_b32_e32 v246, 0xffff0000, v135\n"
"v_lshlrev_b32_e32 v245, 16, v140\n"
"v_and_b32_e32 v244, 0xffff0000, v140\n"
"v_mfma_f32_16x16x32_bf16 v[148:151], a[232:235], v[24:27], v[148:151]\n"
"ds_write_b128 v3, v[116:119] offset:8192\n"
"v_mfma_f32_16x16x32_bf16 v[148:151], a[228:231], v[180:183], v[148:151]\n"
"v_lshlrev_b32_e32 v243, 16, v141\n"
"v_and_b32_e32 v242, 0xffff0000, v141\n"
"v_lshlrev_b32_e32 v241, 16, v142\n"
"v_mfma_f32_16x16x32_bf16 v[148:151], a[224:227], v[152:155], v[148:151]\n"
"s_waitcnt vmcnt(11)\n"
"buffer_load_dwordx4 v[116:119], v1, s[4:7], s97 offen offset:3072 sc1\n"
"; @B G2 b.2\n"
"v_mfma_f32_16x16x32_bf16 v[132:135], a[128:131], v[40:43], 0\n"
"v_and_b32_e32 v240, 0xffff0000, v142\n"
"v_lshlrev_b32_e32 v239, 16, v143\n"
"v_and_b32_e32 v238, 0xffff0000, v143\n"
"v_mfma_f32_16x16x32_bf16 v[132:135], a[132:135], v[52:55], v[132:135]\n"
"v_mfma_f32_16x16x32_bf16 v[132:135], a[136:139], v[64:67], v[132:135]\n"
"v_mfma_f32_16x16x32_bf16 v[132:135], a[140:143], v[36:39], v[132:135]\n"
"s_waitcnt vmcnt(8)\n"
"v_mfma_f32_16x16x32_bf16 v[132:135], a[144:147], v[72:75], v[132:135]\n"
"v_mfma_f32_16x16x32_bf16 v[132:135], a[148:151], v[60:63], v[132:135]\n"
"ds_write_b128 v3, v[148:151] offset:9216\n"
"v_mfma_f32_16x16x32_bf16 v[132:135], a[152:155], v[80:83], v[132:135]\n"
"v_mfma_f32_16x16x32_bf16 v[132:135], a[156:159], v[68:71], v[132:135]\n"
"v_mfma_f32_16x16x32_bf16 v[140:143], a[188:191], v[68:71], 0\n"
"v_mfma_f32_16x16x32_bf16 v[140:143], a[184:187], v[80:83], v[140:143]\n"
"s_waitcnt lgkmcnt(0)\n"
"s_barrier\n"
"v_mfma_f32_16x16x32_bf16 v[140:143], a[180:183], v[60:63], v[140:143]\n"
"ds_read_b128 v[148:151], v4\n"
"ds_read_b128 v[160:163], v5\n"
"v_mfma_f32_16x16x32_bf16 v[140:143], a[176:179], v[72:75], v[140:143]\n"
"v_mfma_f32_16x16x32_bf16 v[140:143], a[172:175], v[36:39], v[140:143]\n"
"s_add_u32 s97, s56, 0x400000\n"
"v_cmp_u_f32_e32 vcc, v132, v132\n"
"v_mfma_f32_16x16x32_bf16 v[140:143], a[168:171], v[64:67], v[140:143]\n"
"s_cbranch_vccnz .Lmk_p62s\n"
".Lmk_p62r:\n"
"v_mfma_f32_16x16x32_bf16 v[140:143], a[164:167], v[52:55], v[140:143]\n"
"ds_write_b128 v3, v[132:135] offset:12288\n"
"v_mfma_f32_16x16x32_bf16 v[140:143], a[160:163], v[40:43], v[140:143]\n"
"buffer_load_dwordx4 v[132:135], v0, s[4:7], s97 offen sc1\n"
"s_waitcnt lgkmcnt(1)\n"
"v_mfma_f32_16x16x32_bf16 v[164:167], a[192:195], v[40:43], 0\n"
"ds_read_b128 v[168:171], v4 offset:1024\n"
"ds_read_b128 v[172:175], v5 offset:1024\n"
"v_add_f32_e32 v148, v148, v254\n"
"v_mfma_f32_16x16x32_bf16 v[164:167], a[196:199], v[52:55], v[164:167]\n"
"v_add_f32_e32 v149, v149, v252\n"
"v_add_f32_e32 v150, v150, v251\n"
"v_add_f32_e32 v151, v151, v250\n"
"v_mfma_f32_16x16x32_bf16 v[164:167], a[200:203], v[64:67], v[164:167]\n"
"buffer_load_dwordx4 v[184:187], v0, s[4:7], s97 offen offset:1024 sc1\n"
"v_add_f32_e32 v148, v148, v160\n"
"v_mfma_f32_16x16x32_bf16 v[164:167], a[204:207], v[36:39], v[164:167]\n"
"v_add_f32_e32 v149, v149, v161\n"
"v_add_f32_e32 v150, v150, v162\n"
"v_add_f32_e32 v151, v151, v163\n"
"v_mfma_f32_16x16x32_bf16 v[164:167], a[208:211], v[72:75], v[164:167]\n"
"v_mfma_f32_16x16x32_bf16 v[164:167], a[212:215], v[60:63], v[164:167]\n"
"ds_write_b128 v3, v[140:143] offset:13312\n"
"v_mfma_f32_16x16x32_bf16 v[164:167], a[216:219], v[80:83], v[164:167]\n"
"buffer_load_dwordx4 v[140:143], v0, s[4:7], s97 offen offset:2048 sc1\n"
"s_waitcnt lgkmcnt(1)\n"
"v_mfma_f32_16x16x32_bf16 v[164:167], a[220:223], v[68:71], v[164:167]\n"
"ds_read_b128 v[160:163], v4 offset:2048\n"
"ds_read_b128 v[200:203], v5 offset:2048\n"
"v_add_f32_e32 v168, v168, v249\n"
"v_mfma_f32_16x16x32_bf16 v[204:207], a[252:255], v[68:71], 0\n"
"v_add_f32_e32 v169, v169, v248\n"
"v_add_f32_e32 v170, v170, v247\n"
"v_add_f32_e32 v171, v171, v246\n"
"v_mfma_f32_16x16x32_bf16 v[204:207], a[248:251], v[80:83], v[204:207]\n"
"buffer_load_dwordx4 v[68:71], v0, s[4:7], s97 offen offset:3072 sc1\n"
"v_add_f32_e32 v168, v168, v172\n"
"v_mfma_f32_16x16x32_bf16 v[204:207], a[244:247], v[60:63], v[204:207]\n"
"v_add_f32_e32 v169, v169, v173\n"
"v_add_f32_e32 v170, v170, v174\n"
"v_add_f32_e32 v171, v171, v175\n"
"v_mfma_f32_16x16x32_bf16 v[204:207], a[240:243], v[72:75], v[204:207]\n"
"v_mfma_f32_16x16x32_bf16 v[204:207], a[236:239], v[36:39], v[204:207]\n"
"buffer_load_dwordx4 v[36:39], v1, s[4:7], s97 offen sc1\n"
"s_waitcnt lgkmcnt(0)\n"
"v_mfma_f32_16x16x32_bf16 v[204:207], a[232:235], v[64:67], v[204:207]\n"
"ds_write_b128 v3, v[164:167] offset:14336\n"
"v_mfma_f32_16x16x32_bf16 v[204:207], a[228:231], v[52:55], v[204:207]\n"
"ds_read_b128 v[52:55], v4 offset:3072\n"
"ds_read_b128 v[60:63], v5 offset:3072\n"
"v_add_f32_e32 v160, v160, v245\n"
"v_mfma_f32_16x16x32_bf16 v[204:207], a[224:227], v[40:43], v[204:207]\n"
"s_waitcnt vmcnt(9)\n"
"buffer_load_dwordx4 v[40:43], v1, s[4:7], s97 offen offset:1024 sc1\n"
"; @B G2 b.3\n"
"v_mfma_f32_16x16x32_bf16 v[64:67], a[128:131], v[96:99], v[148:151]\n"
"v_add_f32_e32 v161, v161, v244\n"
"v_add_f32_e32 v162, v162, v243\n"
"v_add_f32_e32 v163, v163, v242\n"
"v_mfma_f32_16x16x32_bf16 v[64:67], a[132:135], v[76:79], v[64:67]\n"
"v_add_f32_e32 v160, v160, v200\n"
"v_add_f32_e32 v161, v161, v201\n"
"v_add_f32_e32 v162, v162, v202\n"
"v_mfma_f32_16x16x32_bf16 v[64:67], a[136:139], v[112:115], v[64:67]\n"
"buffer_load_dwordx4 v[72:75], v1, s[4:7], s97 offen offset:2048 sc1\n"
"v_add_f32_e32 v163, v163, v203\n"
"v_mfma_f32_16x16x32_bf16 v[64:67], a[140:143], v[84:87], v[64:67]\n"
"s_waitcnt vmcnt(7)\n"
"s_waitcnt lgkmcnt(0)\n"
"v_add_f32_e32 v52, v52, v241\n"
"v_add_f32_e32 v53, v53, v240\n"
"v_mfma_f32_16x16x32_bf16 v[64:67], a[144:147], v[128:131], v[64:67]\n"
"v_add_f32_e32 v54, v54, v239\n"
"v_add_f32_e32 v55, v55, v238\n"
"v_add_f32_e32 v52, v52, v60\n"
"v_mfma_f32_16x16x32_bf16 v[64:67], a[148:151], v[100:103], v[64:67]\n"
"ds_write_b128 v3, v[204:207] offset:15360\n"
"v_mfma_f32_16x16x32_bf16 v[64:67], a[152:155], v[156:159], v[64:67]\n"
"buffer_load_dwordx4 v[80:83], v1, s[4:7], s97 offen offset:3072 sc1\n"
"v_add_f32_e32 v53, v53, v61\n"
"v_mfma_f32_16x16x32_bf16 v[64:67], a[156:159], v[116:119], v[64:67]\n"
"s_add_u32 s97, s57, 0x400000\n"
"v_add_f32_e32 v54, v54, v62\n"
"v_add_f32_e32 v55, v55, v63\n"
"v_mfma_f32_16x16x32_bf16 v[60:63], a[188:191], v[116:119], v[168:171]\n"
"v_mfma_f32_16x16x32_bf16 v[60:63], a[184:187], v[156:159], v[60:63]\n"
"s_waitcnt lgkmcnt(0)\n"
"s_barrier\n"
"v_mfma_f32_16x16x32_bf16 v[60:63], a[180:183], v[100:103], v[60:63]\n"
"buffer_load_dwordx4 v[164:167], v0, s[4:7], s97 offen sc1\n"
"v_mfma_f32_16x16x32_bf16 v[60:63], a[176:179], v[128:131], v[60:63]\n"
"ds_read_b128 v[172:175], v6\n"
"ds_read_b128 v[200:203], v6 offset:1024\n"
"v_mfma_f32_16x16x32_bf16 v[60:63], a[172:175], v[84:87], v[60:63]\n"
"ds_read_b128 v[204:207], v6 offset:2048\n"
"v_cmp_u_f32_e32 vcc, v64, v64\n"
"v_mfma_f32_16x16x32_bf16 v[60:63], a[168:171], v[112:115], v[60:63]\n"
"s_cbranch_vccnz .Lmk_p63s\n"
".Lmk_p63r:\n"
"ds_read_b128 v[148:151], v6 offset:3072\n"
"v_mfma_f32_16x16x32_bf16 v[60:63], a[164:167], v[76:79], v[60:63]\n"
"buffer_load_dwordx4 v[168:171], v0, s[4:7], s97 offen offset:1024 sc1\n"
"v_mfma_f32_16x16x32_bf16 v[60:63], a[160:163], v[96:99], v[60:63]\n"
"v_mfma_f32_16x16x32_bf16 v[160:163], a[192:195], v[96:99], v[160:163]\n"
"v_mfma_f32_16x16x32_bf16 v[160:163], a[196:199], v[76:79], v[160:163]\n"
"buffer_load_dwordx4 v[208:211], v0, s[4:7], s97 offen offset:2048 sc1\n"
"v_mfma_f32_16x16x32_bf16 v[160:163], a[200:203], v[112:115], v[160:163]\n"
"v_mfma_f32_16x16x32_bf16 v[160:163], a[204:207], v[84:87], v[160:163]\n"
"v_mfma_f32_16x16x32_bf16 v[160:163], a[208:211], v[128:131], v[160:163]\n"
"buffer_load_dwordx4 v[212:215], v0, s[4:7], s97 offen offset:3072 sc1\n"
"s_waitcnt lgkmcnt(0)\n"
"v_mfma_f32_16x16x32_bf16 v[160:163], a[212:215], v[100:103], v[160:163]\n"
"v_add_f32_e32 v172, v64, v172\n"
"v_add_f32_e32 v173, v65, v173\n"
"v_add_f32_e32 v174, v66, v174\n"
"v_mfma_f32_16x16x32_bf16 v[160:163], a[216:219], v[156:159], v[160:163]\n"
"v_add_f32_e32 v175, v67, v175\n"
"v_add_f32_e32 v200, v60, v200\n"
"v_add_f32_e32 v201, v61, v201\n"
"v_mfma_f32_16x16x32_bf16 v[160:163], a[220:223], v[116:119], v[160:163]\n"
"buffer_load_dwordx4 v[64:67], v1, s[4:7], s97 offen sc1\n"
"v_add_f32_e32 v202, v62, v202\n"
"v_mfma_f32_16x16x32_bf16 v[52:55], a[252:255], v[116:119], v[52:55]\n"
"v_add_f32_e32 v203, v63, v203\n"
"v_cvt_pk_bf16_f32 v60, v172, v173\n"
"v_cvt_pk_bf16_f32 v61, v174, v175\n"
"v_mfma_f32_16x16x32_bf16 v[52:55], a[248:251], v[156:159], v[52:55]\n"
"v_cvt_pk_bf16_f32 v62, v200, v201\n"
"v_cvt_pk_bf16_f32 v63, v202, v203\n"
"v_xor_b32_e32 v254, 32, v255\n"
"v_mfma_f32_16x16x32_bf16 v[52:55], a[244:247], v[100:103], v[52:55]\n"
"buffer_load_dwordx4 v[100:103], v1, s[4:7], s97 offen offset:1024 sc1\n"
"v_mfma_f32_16x16x32_bf16 v[52:55], a[240:243], v[128:131], v[52:55]\n"
"ds_write_b64 v255, v[60:61] offset:8192\n"
"v_mfma_f32_16x16x32_bf16 v[52:55], a[236:239], v[84:87], v[52:55]\n"
"ds_write_b64 v254, v[62:63] offset:8192\n"
"v_mfma_f32_16x16x32_bf16 v[52:55], a[232:235], v[112:115], v[52:55]\n"
"buffer_load_dwordx4 v[60:63], v1, s[4:7], s97 offen offset:2048 sc1\n"
"v_mfma_f32_16x16x32_bf16 v[52:55], a[228:231], v[76:79], v[52:55]\n"
"v_mfma_f32_16x16x32_bf16 v[52:55], a[224:227], v[96:99], v[52:55]\n"
"buffer_load_dwordx4 v[76:79], v1, s[4:7], s97 offen offset:3072 sc1\n"
"s_waitcnt vmcnt(14)\n"
"; @B G2 a.0\n"
"v_mfma_f32_16x16x32_bf16 v[84:87], a[128:131], v[132:135], 0\n"
"s_add_u32 s97, s58, 0x400000\n"
"buffer_load_dwordx4 v[96:99], v0, s[4:7], s97 offen sc1\n"
"v_mfma_f32_16x16x32_bf16 v[84:87], a[132:135], v[184:187], v[84:87]\n"
"s_waitcnt vmcnt(13)\n"
"v_add_f32_e32 v204, v160, v204\n"
"v_add_f32_e32 v205, v161, v205\n"
"v_mfma_f32_16x16x32_bf16 v[84:87], a[136:139], v[140:143], v[84:87]\n"
"v_add_f32_e32 v206, v162, v206\n"
"v_add_f32_e32 v207, v163, v207\n"
"v_mfma_f32_16x16x32_bf16 v[84:87], a[140:143], v[68:71], v[84:87]\n"
"s_waitcnt vmcnt(11)\n"
"buffer_load_dwordx4 v[112:115], v0, s[4:7], s97 offen offset:1024 sc1\n"
"v_mfma_f32_16x16x32_bf16 v[84:87], a[144:147], v[36:39], v[84:87]\n"
"v_add_f32_e32 v148, v52, v148\n"
"v_add_f32_e32 v149, v53, v149\n"
"v_mfma_f32_16x16x32_bf16 v[84:87], a[148:151], v[40:43], v[84:87]\n"
"s_waitcnt vmcnt(10)\n"
"v_add_f32_e32 v150, v54, v150\n"
"v_add_f32_e32 v151, v55, v151\n"
"v_mfma_f32_16x16x32_bf16 v[84:87], a[152:155], v[72:75], v[84:87]\n"
"buffer_load_dwordx4 v[52:55], v0, s[4:7], s97 offen offset:2048 sc1\n"
"v_mfma_f32_16x16x32_bf16 v[84:87], a[156:159], v[80:83], v[84:87]\n"
"v_cvt_pk_bf16_f32 v116, v204, v205\n"
"v_cvt_pk_bf16_f32 v117, v206, v207\n"
"v_mfma_f32_16x16x32_bf16 v[128:131], a[188:191], v[80:83], 0\n"
"v_cvt_pk_bf16_f32 v118, v148, v149\n"
"v_cvt_pk_bf16_f32 v119, v150, v151\n"
"v_mfma_f32_16x16x32_bf16 v[128:131], a[184:187], v[72:75], v[128:131]\n"
"buffer_load_dwordx4 v[148:151], v0, s[4:7], s97 offen offset:3072 sc1\n"
"v_mfma_f32_16x16x32_bf16 v[128:131], a[180:183], v[40:43], v[128:131]\n"
"v_xor_b32_e32 v254, 64, v255\n"
"v_xor_b32_e32 v252, 0x60, v255\n"
"v_mfma_f32_16x16x32_bf16 v[128:131], a[176:179], v[36:39], v[128:131]\n"
"ds_write_b64 v254, v[116:117] offset:8192\n"
"v_mfma_f32_16x16x32_bf16 v[128:131], a[172:175], v[68:71], v[128:131]\n"
"v_cmp_u_f32_e32 vcc, v84, v84\n"
"v_mfma_f32_16x16x32_bf16 v[128:131], a[168:171], v[140:143], v[128:131]\n"
"s_cbranch_vccnz .Lmk_p64s\n"
".Lmk_p64r:\n"
"v_mfma_f32_16x16x32_bf16 v[128:131], a[164:167], v[184:187], v[128:131]\n"
"ds_write_b128 v3, v[84:87]\n"
"v_mfma_f32_16x16x32_bf16 v[128:131], a[160:163], v[132:135], v[128:131]\n"
"buffer_load_dwordx4 v[84:87], v1, s[4:7], s97 offen sc1\n"
"v_mfma_f32_16x16x32_bf16 v[156:159], a[192:195], v[132:135], 0\n"
"ds_write_b64 v252, v[118:119] offset:8192\n"
"v_mfma_f32_16x16x32_bf16 v[156:159], a[196:199], v[184:187], v[156:159]\n"
"ds_read_b128 v[116:119], v2 offset:8192\n"
"ds_read_b128 v[160:163], v2 offset:9216\n"
"v_mfma_f32_16x16x32_bf16 v[156:159], a[200:203], v[140:143], v[156:159]\n"
"buffer_load_dwordx4 v[172:175], v1, s[4:7], s97 offen offset:1024 sc1\n"
"v_mfma_f32_16x16x32_bf16 v[156:159], a[204:207], v[68:71], v[156:159]\n"
"v_bfe_u32 v255, v0, 4, 4\n"
"v_bfe_u32 v254, v0, 9, 1\n"
"v_mfma_f32_16x16x32_bf16 v[156:159], a[208:211], v[36:39], v[156:159]\n"
"v_and_b32_e32 v252, 7, v255\n"
"v_xor_b32_e32 v254, v254, v252\n"
"v_mfma_f32_16x16x32_bf16 v[156:159], a[212:215], v[40:43], v[156:159]\n"
"ds_write_b128 v3, v[128:131] offset:1024\n"
"v_mfma_f32_16x16x32_bf16 v[156:159], a[216:219], v[72:75], v[156:159]\n"
"buffer_load_dwordx4 v[128:131], v1, s[4:7], s97 offen offset:2048 sc1\n"
"v_mfma_f32_16x16x32_bf16 v[156:159], a[220:223], v[80:83], v[156:159]\n"
"s_waitcnt lgkmcnt(1)\n"
"v_lshlrev_b32_e32 v255, 7, v255\n"
"v_lshl_add_u32 v255, v254, 4, v255\n"
"v_mfma_f32_16x16x32_bf16 v[200:203], a[252:255], v[80:83], 0\n"
"v_bfe_u32 v254, v0, 8, 1\n"
"v_lshl_add_u32 v255, v254, 3, v255\n"
"v_mfma_f32_16x16x32_bf16 v[200:203], a[248:251], v[72:75], v[200:203]\n"
"buffer_load_dwordx4 v[72:75], v1, s[4:7], s97 offen offset:3072 sc1\n"
"v_mfma_f32_16x16x32_bf16 v[200:203], a[244:247], v[40:43], v[200:203]\n"
"s_add_u32 s97, s59, 0x400000\n"
"v_add_u32_e32 v255, s77, v255\n"
"v_bfe_u32 v252, v0, 7, 3\n"
"v_mfma_f32_16x16x32_bf16 v[200:203], a[240:243], v[36:39], v[200:203]\n"
"v_bfe_u32 v254, v0, 4, 3\n"
"v_xor_b32_e32 v254, v254, v252\n"
"v_mfma_f32_16x16x32_bf16 v[200:203], a[236:239], v[68:71], v[200:203]\n"
"buffer_load_dwordx4 v[36:39], v0, s[4:7], s97 offen sc1\n"
"v_mfma_f32_16x16x32_bf16 v[200:203], a[232:235], v[140:143], v[200:203]\n"
"ds_write_b128 v3, v[156:159] offset:2048\n"
"v_mfma_f32_16x16x32_bf16 v[200:203], a[228:231], v[184:187], v[200:203]\n"
"v_lshlrev_b32_e32 v252, 11, v252\n"
"v_lshl_add_u32 v252, v254, 4, v252\n"
"v_mfma_f32_16x16x32_bf16 v[200:203], a[224:227], v[132:135], v[200:203]\n"
"s_waitcnt vmcnt(13)\n"
"buffer_load_dwordx4 v[40:43], v0, s[4:7], s97 offen offset:1024 sc1\n"
"; @B G2 a.1\n"
"v_mfma_f32_16x16x32_bf16 v[68:71], a[128:131], v[164:167], 0\n"
"v_mfma_f32_16x16x32_bf16 v[68:71], a[132:135], v[168:171], v[68:71]\n"
"v_mfma_f32_16x16x32_bf16 v[68:71], a[136:139], v[208:211], v[68:71]\n"
"buffer_load_dwordx4 v[80:83], v0, s[4:7], s97 offen offset:2048 sc1\n"
"v_mfma_f32_16x16x32_bf16 v[68:71], a[140:143], v[212:215], v[68:71]\n"
"s_waitcnt vmcnt(11)\n"
"v_mfma_f32_16x16x32_bf16 v[68:71], a[144:147], v[64:67], v[68:71]\n"
"v_mfma_f32_16x16x32_bf16 v[68:71], a[148:151], v[100:103], v[68:71]\n"
"ds_write_b128 v3, v[200:203] offset:3072\n"
"v_mfma_f32_16x16x32_bf16 v[68:71], a[152:155], v[60:63], v[68:71]\n"
"buffer_load_dwordx4 v[132:135], v0, s[4:7], s97 offen offset:3072 sc1\n"
"v_mfma_f32_16x16x32_bf16 v[68:71], a[156:159], v[76:79], v[68:71]\n"
"v_mfma_f32_16x16x32_bf16 v[140:143], a[188:191], v[76:79], 0\n"
"v_mfma_f32_16x16x32_bf16 v[140:143], a[184:187], v[60:63], v[140:143]\n"
"buffer_load_dwordx4 v[156:159], v1, s[4:7], s97 offen sc1\n"
"v_mfma_f32_16x16x32_bf16 v[140:143], a[180:183], v[100:103], v[140:143]\n"
"v_mfma_f32_16x16x32_bf16 v[140:143], a[176:179], v[64:67], v[140:143]\n"
"v_mfma_f32_16x16x32_bf16 v[140:143], a[172:175], v[212:215], v[140:143]\n"
"s_nop 1\n"
"v_cmp_u_f32_e32 vcc, v68, v68\n"
"v_mfma_f32_16x16x32_bf16 v[140:143], a[168:171], v[208:211], v[140:143]\n"
"s_cbranch_vccnz .Lmk_p65s\n"
".Lmk_p65r:\n"
"v_mfma_f32_16x16x32_bf16 v[140:143], a[164:167], v[168:171], v[140:143]\n"
"ds_write_b128 v3, v[68:71] offset:6144\n"
"v_mfma_f32_16x16x32_bf16 v[140:143], a[160:163], v[164:167], v[140:143]\n"
"buffer_load_dwordx4 v[68:71], v1, s[4:7], s97 offen offset:1024 sc1\n"
"v_mfma_f32_16x16x32_bf16 v[184:187], a[192:195], v[164:167], 0\n"
"v_mfma_f32_16x16x32_bf16 v[184:187], a[196:199], v[168:171], v[184:187]\n"
"v_mfma_f32_16x16x32_bf16 v[184:187], a[200:203], v[208:211], v[184:187]\n"
"buffer_load_dwordx4 v[200:203], v1, s[4:7], s97 offen offset:2048 sc1\n"
"v_mfma_f32_16x16x32_bf16 v[184:187], a[204:207], v[212:215], v[184:187]\n"
"v_mfma_f32_16x16x32_bf16 v[184:187], a[208:211], v[64:67], v[184:187]\n"
"ds_read_b128 v[204:207], v2\n"
"ds_read_b128 v[216:219], v2 offset:1024\n"
"v_mfma_f32_16x16x32_bf16 v[184:187], a[212:215], v[100:103], v[184:187]\n"
"ds_write_b128 v3, v[140:143] offset:7168\n"
"v_mfma_f32_16x16x32_bf16 v[184:187], a[216:219], v[60:63], v[184:187]\n"
"buffer_load_dwordx4 v[140:143], v1, s[4:7], s97 offen offset:3072 sc1\n"
"v_mfma_f32_16x16x32_bf16 v[184:187], a[220:223], v[76:79], v[184:187]\n"
"v_mfma_f32_16x16x32_bf16 v[220:223], a[252:255], v[76:79], 0\n"
"v_mfma_f32_16x16x32_bf16 v[220:223], a[248:251], v[60:63], v[220:223]\n"
"s_waitcnt lgkmcnt(1)\n"
"v_lshlrev_b32_e32 v254, 16, v204\n"
"v_and_b32_e32 v251, 0xffff0000, v204\n"
"v_lshlrev_b32_e32 v250, 16, v205\n"
"v_mfma_f32_16x16x32_bf16 v[220:223], a[244:247], v[100:103], v[220:223]\n"
"v_and_b32_e32 v249, 0xffff0000, v205\n"
"v_lshlrev_b32_e32 v248, 16, v206\n"
"v_and_b32_e32 v247, 0xffff0000, v206\n"
"v_mfma_f32_16x16x32_bf16 v[220:223], a[240:243], v[64:67], v[220:223]\n"
"v_lshlrev_b32_e32 v246, 16, v207\n"
"v_and_b32_e32 v245, 0xffff0000, v207\n"
"v_lshlrev_b32_e32 v244, 16, v216\n"
"v_mfma_f32_16x16x32_bf16 v[220:223], a[236:239], v[212:215], v[220:223]\n"
"s_add_u32 s97, s74, 0x40000\n"
"buffer_store_dwordx4 v[116:119], v253, s[16:19], s97 offen sc1\n"
"v_mfma_f32_16x16x32_bf16 v[220:223], a[232:235], v[208:211], v[220:223]\n"
"ds_write_b128 v3, v[184:187] offset:8192\n"
"v_mfma_f32_16x16x32_bf16 v[220:223], a[228:231], v[168:171], v[220:223]\n"
"v_and_b32_e32 v243, 0xffff0000, v216\n"
"v_lshlrev_b32_e32 v242, 16, v217\n"
"v_and_b32_e32 v241, 0xffff0000, v217\n"
"v_mfma_f32_16x16x32_bf16 v[220:223], a[224:227], v[164:167], v[220:223]\n"
"s_waitcnt vmcnt(13)\n"
"v_lshlrev_b32_e32 v240, 16, v218\n"
"v_and_b32_e32 v239, 0xffff0000, v218\n"
"v_lshlrev_b32_e32 v238, 16, v219\n"
"; @B G2 a.2\n"
"v_mfma_f32_16x16x32_bf16 v[60:63], a[128:131], v[96:99], 0\n"
"v_and_b32_e32 v237, 0xffff0000, v219\n"
"v_mfma_f32_16x16x32_bf16 v[60:63], a[132:135], v[112:115], v[60:63]\n"
"v_mfma_f32_16x16x32_bf16 v[60:63], a[136:139], v[52:55], v[60:63]\n"
"v_mfma_f32_16x16x32_bf16 v[60:63], a[140:143], v[148:151], v[60:63]\n"
"s_waitcnt vmcnt(9)\n"
"v_mfma_f32_16x16x32_bf16 v[60:63], a[144:147], v[84:87], v[60:63]\n"
"v_mfma_f32_16x16x32_bf16 v[60:63], a[148:151], v[172:175], v[60:63]\n"
"ds_write_b128 v3, v[220:223] offset:9216\n"
"v_mfma_f32_16x16x32_bf16 v[60:63], a[152:155], v[128:131], v[60:63]\n"
"s_add_u32 s97, s74, 0x44000\n"
"v_mfma_f32_16x16x32_bf16 v[60:63], a[156:159], v[72:75], v[60:63]\n"
"v_mfma_f32_16x16x32_bf16 v[64:67], a[188:191], v[72:75], 0\n"
"buffer_store_dwordx4 v[160:163], v253, s[16:19], s97 offen sc1\n"
"v_mfma_f32_16x16x32_bf16 v[64:67], a[184:187], v[128:131], v[64:67]\n"
"s_waitcnt lgkmcnt(0)\n"
"s_barrier\n"
"v_mfma_f32_16x16x32_bf16 v[64:67], a[180:183], v[172:175], v[64:67]\n"
"ds_read_b128 v[76:79], v4\n"
"ds_read_b128 v[100:103], v5\n"
"v_mfma_f32_16x16x32_bf16 v[64:67], a[176:179], v[84:87], v[64:67]\n"
"v_mfma_f32_16x16x32_bf16 v[64:67], a[172:175], v[148:151], v[64:67]\n"
"v_cmp_u_f32_e32 vcc, v60, v60\n"
"v_mfma_f32_16x16x32_bf16 v[64:67], a[168:171], v[52:55], v[64:67]\n"
"s_cbranch_vccnz .Lmk_p66s\n"
".Lmk_p66r:\n"
"v_mfma_f32_16x16x32_bf16 v[64:67], a[164:167], v[112:115], v[64:67]\n"
"ds_write_b128 v3, v[60:63] offset:12288\n"
"v_mfma_f32_16x16x32_bf16 v[64:67], a[160:163], v[96:99], v[64:67]\n"
"s_waitcnt lgkmcnt(1)\n"
"v_add_f32_e32 v76, v76, v254\n"
"v_add_f32_e32 v77, v77, v251\n"
"v_add_f32_e32 v78, v78, v250\n"
"v_mfma_f32_16x16x32_bf16 v[60:63], a[192:195], v[96:99], 0\n"
"ds_read_b128 v[116:119], v4 offset:1024\n"
"ds_read_b128 v[160:163], v5 offset:1024\n"
"v_add_f32_e32 v79, v79, v249\n"
"v_mfma_f32_16x16x32_bf16 v[60:63], a[196:199], v[112:115], v[60:63]\n"
"v_add_f32_e32 v76, v76, v100\n"
"v_add_f32_e32 v77, v77, v101\n"
"v_add_f32_e32 v78, v78, v102\n"
"v_mfma_f32_16x16x32_bf16 v[60:63], a[200:203], v[52:55], v[60:63]\n"
"v_add_f32_e32 v79, v79, v103\n"
"v_mfma_f32_16x16x32_bf16 v[60:63], a[204:207], v[148:151], v[60:63]\n"
"v_mfma_f32_16x16x32_bf16 v[60:63], a[208:211], v[84:87], v[60:63]\n"
"v_mfma_f32_16x16x32_bf16 v[60:63], a[212:215], v[172:175], v[60:63]\n"
"ds_write_b128 v3, v[64:67] offset:13312\n"
"v_mfma_f32_16x16x32_bf16 v[60:63], a[216:219], v[128:131], v[60:63]\n"
"ds_read_b128 v[64:67], v4 offset:2048\n"
"ds_read_b128 v[100:103], v5 offset:2048\n"
"s_waitcnt lgkmcnt(3)\n"
"v_mfma_f32_16x16x32_bf16 v[60:63], a[220:223], v[72:75], v[60:63]\n"
"v_add_f32_e32 v116, v116, v248\n"
"v_add_f32_e32 v117, v117, v247\n"
"v_add_f32_e32 v118, v118, v246\n"
"v_mfma_f32_16x16x32_bf16 v[164:167], a[252:255], v[72:75], 0\n"
"v_add_f32_e32 v119, v119, v245\n"
"v_add_f32_e32 v116, v116, v160\n"
"v_add_f32_e32 v117, v117, v161\n"
"v_mfma_f32_16x16x32_bf16 v[164:167], a[248:251], v[128:131], v[164:167]\n"
"v_add_f32_e32 v118, v118, v162\n"
"v_add_f32_e32 v119, v119, v163\n"
"v_mfma_f32_16x16x32_bf16 v[164:167], a[244:247], v[172:175], v[164:167]\n"
"v_mfma_f32_16x16x32_bf16 v[164:167], a[240:243], v[84:87], v[164:167]\n"
"s_waitcnt lgkmcnt(0)\n"
"v_add_f32_e32 v64, v64, v244\n"
"v_add_f32_e32 v65, v65, v243\n"
"v_add_f32_e32 v66, v66, v242\n"
"v_mfma_f32_16x16x32_bf16 v[164:167], a[236:239], v[148:151], v[164:167]\n"
"ds_read_b128 v[72:75], v4 offset:3072\n"
"ds_read_b128 v[84:87], v5 offset:3072\n"
"v_add_f32_e32 v67, v67, v241\n"
"v_mfma_f32_16x16x32_bf16 v[164:167], a[232:235], v[52:55], v[164:167]\n"
"ds_write_b128 v3, v[60:63] offset:14336\n"
"v_mfma_f32_16x16x32_bf16 v[164:167], a[228:231], v[112:115], v[164:167]\n"
"v_add_f32_e32 v64, v64, v100\n"
"v_add_f32_e32 v65, v65, v101\n"
"v_add_f32_e32 v66, v66, v102\n"
"v_mfma_f32_16x16x32_bf16 v[164:167], a[224:227], v[96:99], v[164:167]\n"
"s_waitcnt vmcnt(6)\n"
"v_add_f32_e32 v67, v67, v103\n"
"; @B G2 a.3\n"
"v_mfma_f32_16x16x32_bf16 v[52:55], a[128:131], v[36:39], v[76:79]\n"
"v_mfma_f32_16x16x32_bf16 v[52:55], a[132:135], v[40:43], v[52:55]\n"
"s_waitcnt lgkmcnt(1)\n"
"v_add_f32_e32 v72, v72, v240\n"
"v_add_f32_e32 v73, v73, v239\n"
"v_add_f32_e32 v74, v74, v238\n"
"v_mfma_f32_16x16x32_bf16 v[52:55], a[136:139], v[80:83], v[52:55]\n"
"v_add_f32_e32 v75, v75, v237\n"
"v_add_f32_e32 v72, v72, v84\n"
"v_add_f32_e32 v73, v73, v85\n"
"v_mfma_f32_16x16x32_bf16 v[52:55], a[140:143], v[132:135], v[52:55]\n"
"s_waitcnt vmcnt(2)\n"
"v_add_f32_e32 v74, v74, v86\n"
"v_add_f32_e32 v75, v75, v87\n"
"v_mfma_f32_16x16x32_bf16 v[52:55], a[144:147], v[156:159], v[52:55]\n"
"v_mfma_f32_16x16x32_bf16 v[52:55], a[148:151], v[68:71], v[52:55]\n"
"ds_write_b128 v3, v[164:167] offset:15360\n"
"v_mfma_f32_16x16x32_bf16 v[52:55], a[152:155], v[200:203], v[52:55]\n"
"v_mfma_f32_16x16x32_bf16 v[52:55], a[156:159], v[140:143], v[52:55]\n"
"v_mfma_f32_16x16x32_bf16 v[60:63], a[188:191], v[140:143], v[116:119]\n"
"v_mfma_f32_16x16x32_bf16 v[60:63], a[184:187], v[200:203], v[60:63]\n"
"s_waitcnt lgkmcnt(0)\n"
"s_barrier\n"
"v_mfma_f32_16x16x32_bf16 v[60:63], a[180:183], v[68:71], v[60:63]\n"
"ds_read_b128 v[84:87], v6\n"
"ds_read_b128 v[96:99], v6 offset:1024\n"
"v_mfma_f32_16x16x32_bf16 v[60:63], a[176:179], v[156:159], v[60:63]\n"
"ds_read_b128 v[100:103], v6 offset:2048\n"
"ds_read_b128 v[112:115], v6 offset:3072\n"
"v_mfma_f32_16x16x32_bf16 v[60:63], a[172:175], v[132:135], v[60:63]\n"
"v_cmp_u_f32_e32 vcc, v52, v52\n"
"v_mfma_f32_16x16x32_bf16 v[60:63], a[168:171], v[80:83], v[60:63]\n"
"s_cbranch_vccnz .Lmk_p67s\n"
".Lmk_p67r:\n"
"v_mfma_f32_16x16x32_bf16 v[60:63], a[164:167], v[40:43], v[60:63]\n"
"v_mfma_f32_16x16x32_bf16 v[60:63], a[160:163], v[36:39], v[60:63]\n"
"v_mfma_f32_16x16x32_bf16 v[64:67], a[192:195], v[36:39], v[64:67]\n"
"v_mfma_f32_16x16x32_bf16 v[64:67], a[196:199], v[40:43], v[64:67]\n"
"v_mfma_f32_16x16x32_bf16 v[64:67], a[200:203], v[80:83], v[64:67]\n"
"v_mfma_f32_16x16x32_bf16 v[64:67], a[204:207], v[132:135], v[64:67]\n"
"v_mfma_f32_16x16x32_bf16 v[64:67], a[208:211], v[156:159], v[64:67]\n"
"s_waitcnt lgkmcnt(0)\n"
"v_add_f32_e32 v84, v52, v84\n"
"v_add_f32_e32 v85, v53, v85\n"
"v_add_f32_e32 v86, v54, v86\n"
"v_mfma_f32_16x16x32_bf16 v[64:67], a[212:215], v[68:71], v[64:67]\n"
"v_add_f32_e32 v87, v55, v87\n"
"v_add_f32_e32 v96, v60, v96\n"
"v_add_f32_e32 v97, v61, v97\n"
"v_mfma_f32_16x16x32_bf16 v[64:67], a[216:219], v[200:203], v[64:67]\n"
"v_add_f32_e32 v98, v62, v98\n"
"v_add_f32_e32 v99, v63, v99\n"
"v_cvt_pk_bf16_f32 v52, v84, v85\n"
"v_mfma_f32_16x16x32_bf16 v[64:67], a[220:223], v[140:143], v[64:67]\n"
"v_cvt_pk_bf16_f32 v53, v86, v87\n"
"v_cvt_pk_bf16_f32 v54, v96, v97\n"
"v_cvt_pk_bf16_f32 v55, v98, v99\n"
"v_mfma_f32_16x16x32_bf16 v[72:75], a[252:255], v[140:143], v[72:75]\n"
"v_xor_b32_e32 v254, 32, v255\n"
"v_mfma_f32_16x16x32_bf16 v[72:75], a[248:251], v[200:203], v[72:75]\n"
"ds_write_b64 v255, v[52:53] offset:0\n"
"v_mfma_f32_16x16x32_bf16 v[72:75], a[244:247], v[68:71], v[72:75]\n"
"ds_write_b64 v254, v[54:55] offset:0\n"
"v_mfma_f32_16x16x32_bf16 v[72:75], a[240:243], v[156:159], v[72:75]\n"
"v_mfma_f32_16x16x32_bf16 v[72:75], a[236:239], v[132:135], v[72:75]\n"
"v_mfma_f32_16x16x32_bf16 v[72:75], a[232:235], v[80:83], v[72:75]\n"
"v_mfma_f32_16x16x32_bf16 v[72:75], a[228:231], v[40:43], v[72:75]\n"
"v_mfma_f32_16x16x32_bf16 v[72:75], a[224:227], v[36:39], v[72:75]\n"
"v_add_f32_e32 v100, v64, v100\n"
"v_add_f32_e32 v101, v65, v101\n"
"v_add_f32_e32 v102, v66, v102\n"
"v_add_f32_e32 v103, v67, v103\n"
"s_nop 3\n"
"v_add_f32_e32 v112, v72, v112\n"
"v_add_f32_e32 v113, v73, v113\n"
"v_add_f32_e32 v114, v74, v114\n"
"v_add_f32_e32 v115, v75, v115\n"
"v_cvt_pk_bf16_f32 v36, v100, v101\n"
"v_cvt_pk_bf16_f32 v37, v102, v103\n"
"v_cvt_pk_bf16_f32 v38, v112, v113\n"
"v_cvt_pk_bf16_f32 v39, v114, v115\n"
"v_xor_b32_e32 v254, 64, v255\n"
"v_xor_b32_e32 v253, 0x60, v255\n"
"ds_write_b64 v254, v[36:37] offset:0\n"
"ds_write_b64 v253, v[38:39] offset:0\n"
"ds_read_b128 v[36:39], v2\n"
"ds_read_b128 v[40:43], v2 offset:1024\n"
"s_waitcnt lgkmcnt(0)\n"
"buffer_store_dwordx4 v[36:39], v252, s[16:19], s74 offen sc1\n"
"s_add_u32 s97, s74, 0x4000\n"
"buffer_store_dwordx4 v[40:43], v252, s[16:19], s97 offen sc1\n"
"s_cmp_eq_u32 s81, 0\n"
"s_cbranch_scc1 .Lmk_fin\n"
"buffer_store_dwordx2 v[18:19], v11, s[4:7], s81 offen sc1\n"
".Lmk_fin:\n"
"s_endpgm\n"
".Lmk_end:\n"
    :: "{s[4:7]}"(wsr), "{s[8:11]}"(wsr), "{s[12:15]}"(wsk), "{s[16:19]}"(outr), "{s[52:67]}"(sa), "{s[68:83]}"(sb),
       "{s[20:27]}"(pa), "{s[28:31]}"(wa), "{s[32:35]}"(wb), "{s[36:39]}"(oa), "{s[40:43]}"(ob), "{s[44:51]}"(pc), "{s99}"(uni((bix * 4 + w) * 1024)),
       "{v[0:15]}"(va), "{v[16:23]}"(vb), "{v[32:47]}"(pz), "{v48}"(sta), "{v50}"(pf)
    : "memory", "v255", "a255", "s98", "vcc");
  __builtin_unreachable();
}

}

extern "C" void kernel_launch(void* const* d_in, const int* in_sizes, int n_in, void* d_out, int out_size,
                              void* d_ws, size_t ws_size, hipStream_t stream) {
  (void)hipFuncSetAttribute((const void*)mega_kernel, hipFuncAttributeMaxDynamicSharedMemorySize, LDS_BYTES);
  mega_kernel<<<256, 256, LDS_BYTES, stream>>>((const u16*)d_in[0], (const u16*)d_in[1], (const u16*)d_in[2],
                                               (const u16*)d_in[4], (const u16*)d_in[5], (u16*)d_out, (char*)d_ws);
}
```

```cpp
#include <hip/hip_runtime.h>
#include <stdint.h>

namespace {

typedef unsigned short u16;
typedef unsigned long long u64;
typedef short s16x4 __attribute__((ext_vector_type(4)));
typedef float f32x4 __attribute__((ext_vector_type(4)));
typedef unsigned u32x4 __attribute__((ext_vector_type(4)));
typedef unsigned u32x2 __attribute__((ext_vector_type(2)));
typedef unsigned u32x8 __attribute__((ext_vector_type(8)));
typedef unsigned u32x16 __attribute__((ext_vector_type(16)));
typedef __amdgpu_buffer_rsrc_t rsrc_t;
#define LAS __attribute__((address_space(3)))
#define DI __device__ __forceinline__
#define LDSP(a) ((LAS char*)(size_t)(a))

constexpr unsigned MB = 1u << 20;
constexpr unsigned WS_XF = 0;
constexpr unsigned WS_OF = 16 * MB;
constexpr unsigned WS_KV = 32 * MB;
constexpr unsigned WS_WF = 64 * MB;
constexpr unsigned WS_WO = 76 * MB;
constexpr unsigned WS_ST = 80 * MB;
constexpr unsigned WS_CS = 81 * MB;
constexpr unsigned WS_FL = 82 * MB;
constexpr unsigned WS_EP = WS_FL + 2048 * 128;
constexpr unsigned WS_DBG = 96 * MB;
constexpr unsigned OOB = 0x80000000u;
constexpr int L_RES = 0, L_K = 16384, L_V = 49152, L_INB = 81920, L_CS = 155648, L_PM = L_CS + 1024, L_MB = L_PM + 2048, L_SCR = L_MB + 2048;
constexpr int L_WS = 57344;
constexpr int LDS_BYTES = 163840;

template <int N> DI void wait_vm() { asm volatile("s_waitcnt vmcnt(%0)" ::"n"(N) : "memory"); }
template <int N> DI void nops() { asm volatile("s_nop %0" ::"n"(N - 1)); }
template <int OFF> DI void ld8(u32x2& d, rsrc_t r, unsigned v, unsigned s) {
  asm volatile("buffer_load_dwordx2 %0, %1, %2, %3 offen offset:%4 sc1" : "=v"(d) : "v"(v), "s"(r), "s"(s), "i"(OFF) : "memory");
}
template <int OFF> DI void st16(const u32x4& d, rsrc_t r, unsigned v, unsigned s) {
  asm volatile("buffer_store_dwordx4 %0, %1, %2, %3 offen offset:%4 sc1\n\ts_nop 1" :: "v"(d), "v"(v), "s"(r), "s"(s), "i"(OFF) : "memory");
}
template <int OFF> DI void st8(const u32x2& d, rsrc_t r, unsigned v, unsigned s) {
  asm volatile("buffer_store_dwordx2 %0, %1, %2, %3 offen offset:%4 sc1" :: "v"(d), "v"(v), "s"(r), "s"(s), "i"(OFF) : "memory");
}
DI void st4(unsigned d, rsrc_t r, unsigned v, unsigned s) {
  asm volatile("buffer_store_dword %0, %1, %2, %3 offen sc1" :: "v"(d), "v"(v), "s"(r), "s"(s) : "memory");
}
DI void dma(rsrc_t r, unsigned v, unsigned s, unsigned lm) {
  asm volatile("s_mov_b32 m0, %3\n\ts_nop 0\n\tbuffer_load_dwordx4 %0, %1, %2 offen lds" :: "v"(v), "s"(r), "s"(s), "s"(lm) : "memory");
}
DI void touch(rsrc_t r, unsigned v) {
  asm volatile("s_mov_b32 m0, %2\n\ts_nop 0\n\tbuffer_load_dword %0, %1, 0 offen lds" :: "v"(v), "s"(r), "s"((unsigned)L_SCR) : "memory");
}
DI void mma_v(f32x4& c, const u32x4& a, const u32x4& b) { asm volatile("v_mfma_f32_16x16x32_bf16 %0, %1, %2, %0" : "+v"(c) : "v"(a), "v"(b)); }
DI void mm0_v(f32x4& c, const u32x4& a, const u32x4& b) { asm volatile("v_mfma_f32_16x16x32_bf16 %0, %1, %2, 0" : "=v"(c) : "v"(a), "v"(b)); }
DI unsigned uni(unsigned v) { v = __builtin_amdgcn_readfirstlane(v); asm volatile("" : "+s"(v)); return v; }
template <class T> DI void pin(T& v) { asm volatile("" : "+v"(v)); }
DI u32x2 lds_tr(unsigned a) { return __builtin_bit_cast(u32x2, __builtin_amdgcn_ds_read_tr16_b64_v4i16((LAS s16x4*)LDSP(a))); }
DI u32x4 cat(const u32x2& a, const u32x2& b) { return __builtin_shufflevector(a, b, 0, 1, 2, 3); }
DI u32x2 lds_r2(unsigned a) { return *(LAS u32x2*)LDSP(a); }
DI void lds_w4(unsigned a, const u32x4& v) { *(LAS u32x4*)LDSP(a) = v; }
DI unsigned rot(unsigned g, unsigned sl) { return (g + (0x641u >> 4 * sl)) & 15; }
DI int swzK(int u) { return ((((u >> 1) ^ u) & 1) << 1) | (u >> 1); }

#define STAMP(i)

__global__ __launch_bounds__(256, 1) void mega_kernel(const u16* __restrict__ x0, const u16* __restrict__ WqA,
                                                      const u16* __restrict__ WoA, const u16* __restrict__ WqB,
                                                      const u16* __restrict__ WoB, u16* __restrict__ out, char* ws) {
  const unsigned bid = blockIdx.x, w = __builtin_amdgcn_readfirstlane(threadIdx.x >> 6);
  unsigned lane = threadIdx.x & 63;
  unsigned stw = 0;
  STAMP(0);
  u32x2 tag;
  {
    u64 t0; unsigned t1, t2, t3, v1, v2;
    asm volatile(
      "s_getpc_b64 %[t0]\n.Lmk_here:\n"
      "s_lshr_b32 %[t1], %[bid], 3\n s_lshl2_add_u32 %[t1], %[t1], %[w]\n s_lshl_b32 %[t1], %[t1], 10\n"
      "v_lshlrev_b32 %[v1], 4, %[lane]\n"
      "s_mov_b64 s[88:89], %[t0]\n s_and_b32 s88, s88, -16\n s_and_b32 s89, s89, 0xffff\n s_mov_b32 s90, (.Lmk_end-.Lmk_here)&0xfffffff0\n s_mov_b32 s91, 0x20000\n"
      "s_mov_b32 m0, %[scr]\n"
      "v_lshrrev_b32 %[v2], 3, %[lane]\n"
      "buffer_load_dwordx4 %[v1], s[88:91], %[t1] offen lds\n"
      "v_and_b32 %[v1], 0x70, %[v1]\n v_lshl_or_b32 %[v1], %[v2], 11, %[v1]\n"
      "s_and_b32 %[t1], %[bid], 4\n s_lshr_b32 %[t2], %[bid], 6\n s_add_u32 %[t1], %[t1], %[t2]\n s_lshl3_add_u32 %[t1], %[t1], %[w]\n"
      "s_and_b32 %[t3], %[bid], 1\n s_lshl1_add_u32 %[t1], %[t1], %[t3]\n s_lshl_b32 %[t1], %[t1], 8\n"
      "s_bfe_u32 %[t3], %[bid], 0x30003\n s_add_u32 %[t1], %[t1], %[t3]\n s_and_b32 %[t3], %[bid], 2\n s_lshl2_add_u32 %[t1], %[t3], %[t1]\n s_lshl_b32 %[t1], %[t1], 7\n"
      "s_lshl_b32 %[t3], %[w], 11\n"
      "v_lshrrev_b32 %[v2], 2, %[lane]\n v_mul_u32_u24 %[v2], 0x1800, %[v2]\n"
      : [t0] "=&s"(t0), [t1] "=&s"(t1), [t2] "=&s"(t2), [t3] "=&s"(t3), [v1] "=&v"(v1), [v2] "=&v"(v2)
      : [bid] "s"(bid), [w] "s"(w), [lane] "v"(lane), [scr] "i"(L_SCR + 1024) : "memory", "s88", "s89", "s90", "s91", "scc");
    v2 += ((lane & 3) ^ ((lane >> 3) & 2)) * 16;
    const unsigned par_ = bid >> 3 & 1;
    const unsigned src = w * 0x180000 + (bid & 3) * 512 + (bid >> 3 & 7) * 64 + (2 * (bid >> 6) + par_) * 196608, dst = par_ ? L_INB + w * 18432 : L_WS + w * 6144;
    asm volatile(
      "s_mov_b64 s[88:89], %[x]\n s_and_b32 s89, s89, 0xffff\n s_mov_b32 s90, 0x400000\n s_mov_b32 s91, 0x20000\n"
      "s_add_u32 m0, %[t3], 0x2000\n s_add_u32 %[t2], %[t1], 0x40000\n buffer_load_dwordx4 %[v1], s[88:91], %[t2] offen lds\n"
      "s_add_u32 m0, %[t3], 0x2400\n s_add_u32 %[t2], %[t1], 0x44000\n buffer_load_dwordx4 %[v1], s[88:91], %[t2] offen lds\n"
      "s_mov_b32 m0, %[t3]\n s_add_u32 %[t2], %[t1], 0x4000\n buffer_load_dwordx4 %[v1], s[88:91], %[t1] offen lds\n"
      "s_add_u32 m0, %[t3], 0x400\n s_mov_b32 %[t1], %[ep]\n buffer_load_dwordx4 %[v1], s[88:91], %[t2] offen lds\n"
      "s_mov_b64 s[88:89], %[ws]\n s_and_b32 s89, s89, 0xffff\n s_mov_b32 s90, 0x8000000\n"
      "buffer_load_dwordx2 %[tag], off, s[88:91], %[t1] sc1\n"
      : [t1] "+s"(t1), [t2] "+s"(t2), [tag] "=&v"(tag), [lane] "+v"(lane)
      : [t3] "s"(t3), [v1] "v"(v1), [v2] "v"(v2), [x] "s"(x0), [ws] "s"(ws), [wq] "s"(WqA), [src] "s"(src), [dst] "s"(dst), [ep] "i"(WS_EP),
        "s"(WoA), "s"(WqB), "s"(WoB), "s"(out) : "memory", "s88", "s89", "s90", "s91", "scc");
  }
  const unsigned xcd = bid & 7, bix = bid >> 3;
  const unsigned c = (xcd >> 2) * 4 + (bix >> 3), h = (xcd & 3) * 8 + (bix & 7), par = h & 1, g = h >> 1;
  const unsigned p2 = h >> 3 & 1, g2 = (h >> 4) * 8 + (h & 7);
  const unsigned fr = lane & 15, fq = lane >> 4;
  const unsigned dl = 0, wr = (w + dl) & 3;
  const rsrc_t wsr = __builtin_amdgcn_make_buffer_rsrc(ws, 0, 128 * MB, 0x00020000);
  const unsigned l16 = lane * 16, lw = w * 2048 + l16;
  const unsigned fl0 = lane == 0 ? 0u : OOB;
  const unsigned myfl = WS_FL + (c * 64 + h) * 128 + w * 8;
  const unsigned osrc0 = (lane >> 3) * 2048 + (lane & 7) * 16;
  const rsrc_t wa = __builtin_amdgcn_make_buffer_rsrc((void*)WqA, 0, 6 * MB, 0x00020000);
  const unsigned rho = 0, wbase = w * 256 * 6144 + h * 64;
  const unsigned wv = (lane >> 2) * 6144 + ((lane & 3) ^ ((lane >> 3) & 2)) * 16;
  auto piece = [&](int q, int j) __attribute__((always_inline)) {
    const unsigned k = j ^ par, src = uni(wbase + (((rho + 2 * q) & 7) + k) * 196608), dst = uni(q + k ? L_INB + w * 18432 + (2 * q + k - 1) * 6144 : L_WS + w * 6144);
#pragma unroll
    for (int i = 0; i < 6; ++i) dma(wa, wv, src + (i & 1) * 98304 + (i >> 1) * 2048, dst + i * 1024);
  };
  const unsigned kb = 2 * c + par;
  const rsrc_t oa = __builtin_amdgcn_make_buffer_rsrc((void*)WoA, 0, 2 * MB, 0x00020000);
  const rsrc_t wb = __builtin_amdgcn_make_buffer_rsrc((void*)WqB, 0, 6 * MB, 0x00020000);
  const rsrc_t ob = __builtin_amdgcn_make_buffer_rsrc((void*)WoB, 0, 2 * MB, 0x00020000);
  const unsigned wsrc = (w * 16 + (lane >> 3)) * 6144 + (lane & 7) * 16, osrc = w * 16 * 2048 + osrc0;
  auto get_o = [&](int t) __attribute__((always_inline)) {
#pragma unroll
    for (int i = 0; i < 2; ++i) dma(t ? ob : oa, osrc, (kb * 64 + i * 8) * 2048 + rot(g, 1 + t) * 128, L_V + 8192 + t * 8192 + w * 2048 + i * 1024);
  };
  auto get_q = [&](int t) __attribute__((always_inline)) {
#pragma unroll
    for (int i = 0; i < 6; ++i)
      dma(t ? wb : wa, wsrc, (kb * 64 + (i & 1) * 8) * 6144 + (i >> 1) * 2048 + rot(g, i >> 1) * 128, (t ? L_K : L_INB) + (i >> 1) * 8192 + w * 2048 + (i & 1) * 1024);
  };
  const unsigned fr8 = fq == fr >> 2 ? 64 + fr * 4 : fq == ((fr >> 2) + 1 & 3) ? fr * 4 : OOB;
  u32x4 ones = {0x3F803F80u, 0x3F803F80u, 0x3F803F80u, 0x3F803F80u};
  unsigned sta = 0;
  auto publish = [&](int s, bool now) __attribute__((always_inline)) {
    const unsigned a = L_RES + s * 8192 + w * 2048 + fr * 128 + fq * 8, T = c * 16 + 8 * s + 2 * wr + p2;
    u32x4 y0 = cat(lds_r2(a), lds_r2(a + 32)), y1 = cat(lds_r2(a + 64), lds_r2(a + 96));
    f32x4 sm, gm;
    pin(y0); pin(y1); pin(ones);
    nops<2>();
    mm0_v(sm, ones, y0); mm0_v(gm, y0, y0); mma_v(sm, ones, y1); mma_v(gm, y1, y1);
    if (now) { st16<0>(y0, wsr, l16, WS_XF + T * 32768 + g2 * 2048); st16<1024>(y1, wsr, l16, WS_XF + T * 32768 + g2 * 2048); }
    lds_w4(lw + L_RES + s * 8192, y0); lds_w4(lw + L_RES + s * 8192 + 1024, y1);
    nops<8>();
    pin(sm); pin(gm);
    const unsigned st = __float_as_uint(fq != fr >> 2 ? sm[0] : fr & 2 ? (fr & 1 ? gm[3] : gm[2]) : (fr & 1 ? gm[1] : gm[0]));
    if (now) st4(st, wsr, fr8, WS_ST + T * 2048 + g2 * 128); else sta = st;
  };
  auto next_tag = [&]() __attribute__((always_inline)) {
    pin(tag);
    const u64 t = (((u64)tag[1] << 32) | tag[0]) + 1; tag = (u32x2){(unsigned)t, (unsigned)(t >> 32)};
  };
  const unsigned cb = c * 16 * 32768, skv = WS_KV + (c * 32 + h) * 32768;
  const unsigned soo = WS_OF + cb + wr * 65536 + h * 1024, sxo = WS_XF + cb + (2 * wr + p2) * 32768 + g2 * 2048, sso = WS_ST + (c * 16 + 2 * wr + p2) * 2048 + g2 * 128;
  auto clear = [&](int from, int to) __attribute__((always_inline)) {
    u32x4 z = {~0u, ~0u, ~0u, ~0u};
    pin(z);
#pragma unroll
    for (int k = from; k < to; ++k) {
      if (k < 8) st16<0>(z, wsr, l16, soo + (k == 4 ? 9 : k == 5 ? 8 : k - 6) * 32768);
      else if (k < 10) st16<0>(z, wsr, l16, sxo + 4 * MB + 8 * 32768 + (k - 8) * 1024);
      else st4(z[0], wsr, fr8, sso + 136 * 2048);
    }
  };
  ;
  get_q(0);
  STAMP(29);

  unsigned ps[16];
  u32x8 pv = {};
#pragma unroll
  for (int j = 0; j < 3; ++j) {
    const unsigned ct = (w & 1) * 3 + j;
    pv[1 + j] = L_K + (ct >> 1) * 8192 + (4 * fq + (fr >> 2)) * 128 + (w >> 1) * 64 + (ct & 1) * 32 + (lane & 3) * 8;
    ps[j] = WS_WF + 6 * MB + ((2 * rot(g, ct >> 1) + (w >> 1)) * 192 + (kb >> 2) * 48 + (ct + 4) % 6 * 8 + (kb & 3) * 2) * 1024;
    ps[10 + j] = kb * 64 * 6144 + j * 2048 + rot(g, j) * 128;
  }
  { const unsigned ct = (w & 1) * 3 + (fr >> 1);
    pv[4] = ((2 * rot(g, ct >> 1) + (w >> 1)) * 16384 + ct * 128 + (fr & 1) * 64 + fq * 16) | (fr < 6 ? 0u : OOB); }
  ps[3] = WS_CS + 524288 + kb * 1024;
  ps[6] = (c >> 2) | par << 1 | (bix * 4 + w) << 10;                                            ps[7] = L_WS + w * 6144;
  ps[8] = wbase;                                             ps[9] = rho;
  ps[13] = kb * 64 * 2048 + rot(g, 1) * 128;                 ps[14] = kb * 64 * 2048 + rot(g, 2) * 128;
  ps[15] = WS_WF + (h * 4 + w) * 49152 + (rho + (c >> 2)) * 1024;
  pv[5] = wsrc;                                              pv[6] = osrc;
  u32x16 pw = {wv, L_WS + w * 6144 + (4 * fq + (fr >> 2)) * 64 + (fq & 1) * 32 + (lane & 3) * 8,
                    l16 + rho * 1024, l16 + ((rho + 2) & 7) * 1024, l16 + ((rho + 4) & 7) * 1024, l16 + ((rho + 6) & 7) * 1024,
                    L_WS + w * 6144 + (fr == 0 ? fq * 16 : 1024 + l16), L_WS + (w * 24 + (lane < 24 ? lane : 0)) * 4,
                    (w * 128 + (lane >> 3 & 1) * 64 + 2 * ((g2 - rot(0, 1 + (fq & 1))) & 15) + (lane >> 2 & 1)) * 128 + (lane & 3) * 8};
  if (w < 2) {
    u32x4 pm;
#pragma unroll
    for (int k = 0; k < 4; ++k)
      pm[k] = (8 * fq + 2 * k > 16 * w + fr ? 0xFFFFu : 0u) | (8 * fq + 2 * k + 1 > 16 * w + fr ? 0xFFFF0000u : 0u);
    lds_w4(L_PM + w * 1024 + l16, pm);
  } else {
    const unsigned want = (unsigned)((0x5555ull << 16) >> (fr + 25 - 8 * fq));
    u32x4 mb;
#pragma unroll
    for (int k = 0; k < 4; ++k) mb[k] = (want >> (15 - 4 * w - k)) & 1 ? 0u : 0xFF800000u;
    lds_w4(L_MB + (w - 2) * 1024 + l16, mb);
  }
  pv[0] = L_V + 8192 + (16 * (fq & 1) + 4 * (fq >> 1) + (fr >> 2)) * 128 + w * 32 + (lane & 3) * 8;
  pv[7] = lane == 0 ? 32 * 128 : OOB;
  ps[4] = WS_WO + (rot(g, 1) * 128 + (kb >> 2) * 32 + w * 8 + (kb & 3) * 2) * 1024;
  ps[5] = WS_WO + 2 * MB + (rot(g, 2) * 128 + (kb >> 2) * 32 + w * 8 + (kb & 3) * 2) * 1024;
  const unsigned krow = (8 * (fr >> 2) + (fr & 3)) * 64 + wr * 2048 + L_K;
  const unsigned kv0 = 16384 + wr * 2048 + fr * 64 + ((fq ^ ((fr >> 3) << 1) ^ ((fr >> 1) & 3)) * 16);
  u32x16 va = {l16, l16 + 4096, lw, L_INB + w * 18432 + l16, L_INB + ((w + 3) & 3) * 18432 + l16, L_INB + ((w + 2) & 3) * 18432 + 6144 + l16,
                     L_INB + ((w + 1) & 3) * 18432 + 12288 + l16, kv0, kv0 ^ 48, L_CS + fq * 16, lane * 4, fl0,
                     ((lane & 31) >> 2) * 128 + (lane & 3) * 8,
                     fq < 3 ? (w * 128 + (lane >> 3 & 1) * 64 + 2 * ((2 * g - rot(g, fq % 3)) & 15) + (lane >> 2 & 1)) * 128 + (lane & 3) * 8 : ((lane & 7) * 64 + h) * 128 + w * 8,
                     fr8, krow + (fq ^ swzK(fr >> 2) ^ ((fr >> 1) & 1)) * 16};
  u32x8 vb = {L_V + wr * 2048 + (8 * fq + (fr >> 2)) * 64 + 8 * (lane & 1) + (((lane >> 1) & 1) ^ ((fq & 1) << 1) ^ (fr >> 3)) * 16,
                    krow + (fq ^ swzK(((fr >> 2) + 2) & 3) ^ ((fr >> 1) & 1)) * 16, 0, 0, 0,
                    lane < 24 ? L_CS + w * 96 + lane * 4 : L_SCR, w == 0 ? bid * 1024 + lane * 4 : OOB, 0};
  u32x16 pz = {pw[2], pw[3], pw[4], pw[5], pv[1], pv[2], pv[3], pv[4], pv[0], pw[8], pw[0], pw[1], pw[6], pw[7], pv[5], pv[6]};
  unsigned pf = pv[7];
  pin(va); pin(vb); pin(pz); pin(pf);
  auto convert = [&]() __attribute__((always_inline)) {
    f32x4 cs[3];
    u32x4 f[2][3];
    asm volatile("s_waitcnt lgkmcnt(0)\n\ts_barrier" ::: "memory");
#pragma unroll
    for (int kk = 0; kk < 2; ++kk)
#pragma unroll
      for (int j = 0; j < 3; ++j) {
        const unsigned a = pz[4 + j] + (L_INB - L_K) + kk * 4096;
        f[kk][j] = cat(lds_tr(a), lds_tr(a + 2048));
      }
    pin(ones);
#pragma unroll
    for (int kk = 0; kk < 2; ++kk)
#pragma unroll
      for (int j = 0; j < 3; ++j) {
        if (kk) mma_v(cs[j], f[kk][j], ones); else mm0_v(cs[j], f[kk][j], ones);
        st16<0>(f[kk][j], wsr, l16, uni(ps[j] - 6 * MB + kk * 1024));
      }
    nops<8>();
    pin(cs[0]); pin(cs[1]); pin(cs[2]);
    const f32x4 v = fr < 2 ? cs[0] : fr < 4 ? cs[1] : cs[2];
    st16<0>(__builtin_bit_cast(u32x4, v), wsr, pz[7], uni(ps[3] - 524288));
  };
  wait_vm<0>();
  next_tag();
  STAMP(18);
  convert();
  publish(1, true);
  publish(0, false);
  STAMP(1);
  touch(wsr, lane < 4 ? WS_FL + (lane >> 1) * 524288 + (w * 512 + c * 64 + (lane & 1) * 32 + h) * 128 : OOB);
  wait_vm<4>();
  st8<96>(tag, wsr, fl0, myfl);
  wait_vm<2>();
  st8<0>(tag, wsr, lane == 0 ? 32 * 128 : OOB, myfl + (2 * g2 + p2 - h) * 128);
  get_o(0);
  get_o(1);
  get_q(1);
  STAMP(15);

  u32x16 sa, sb;
#pragma unroll
  for (int i = 0; i < 4; ++i) {
    sa[i] = WS_XF + cb + w * 8192 + ((wr + 1 + i) & 3) * 65536;
    sa[4 + i] = sa[i] + WS_OF + p2 * 32768;
  }
  sa[8] = soo;                                              sa[9] = sxo;
  sa[10] = WS_ST + (c * 16 + 2 * wr) * 2048;                sa[11] = sso;
  sa[12] = skv - 16384;                                     sa[13] = c ? skv - 32 * 32768 : WS_XF + (h & 15) * 32768;
  sa[14] = c ? skv - 32 * 32768 + 8 * MB : sa[13];          sa[15] = WS_FL + (c * 64 + 8 * w) * 128;
  sb[0] = myfl;                                             sb[1] = WS_WF + (h * 4 + w) * 49152;
  sb[2] = WS_WO + (g2 * 4 + w) * 32768;                     sb[3] = c ? myfl - w * 8 + ((w - dl) & 3) * 8 - 32 * 128 + 64 : myfl + 4096;
  sb[4] = w * 96;                                           sb[5] = c ? myfl + 65536 - w * 8 + ((3 - dl) & 3) * 8 - 32 * 128 + 64 : myfl + 4096;
  sb[6] = ((c * 16 + 2 * wr + p2) * 16 * 1024 + g2 * 64) * 2;  sb[7] = WS_FL + 96;
  sb[8] = WS_CS + h * 16384;                                sb[9] = w * 2048;
  sb[10] = c ? -8 : 8 - wr;                                  sb[11] = c ? 16 * MB : 0;
  sb[12] = (2 * g2 + p2 - h) * 128;                                sb[13] = bid == 0 && w == 0 ? WS_EP : 0;
  sb[14] = c == 0 && wr == 0 ? ~0u : 0u;                     sb[15] = 0;
#pragma unroll
  for (int i = 0; i < 16; ++i) { sa[i] = uni(sa[i]); sb[i] = uni(sb[i]); }
  const rsrc_t wsk = __builtin_amdgcn_make_buffer_rsrc(ws, 0, wr == 3 ? 128 * MB : 0u, 0x00020000);
  const rsrc_t outr = __builtin_amdgcn_make_buffer_rsrc(out, 0, 4 * MB, 0x00020000);
  u32x8 pa, pc;
#pragma unroll
  for (int i = 0; i < 8; ++i) { pa[i] = uni(ps[i]); pc[i] = uni(ps[8 + i]); }
  STAMP(17);

  vb[2] = tag[0]; vb[3] = tag[1];
  STAMP(19);
  vb[7] = stw;
  asm volatile(
"; @entry\n"
"s_mov_b32 s0, 0\n"
"s_mov_b32 s3, 0\n"
"s_waitcnt lgkmcnt(0)\n"
"ds_read_b128 v[24:27], v2\n"
"ds_read_b128 v[28:31], v2 offset:1024\n"
"s_waitcnt lgkmcnt(0)\n"
"buffer_store_dwordx4 v[24:27], v0, s[4:7], s61 offen sc1\n"
"buffer_store_dwordx4 v[28:31], v0, s[4:7], s61 offen offset:1024 sc1\n"
"buffer_store_dword v48, v14, s[4:7], s63 offen sc1\n"
"s_waitcnt vmcnt(9)\n"
"s_waitcnt lgkmcnt(0)\n"
"s_barrier\n"
"ds_read_b64_tr_b16 v[24:25], v40\n"
"ds_read_b64_tr_b16 v[26:27], v40 offset:1024\n"
"ds_read_b64_tr_b16 v[28:29], v40 offset:4096\n"
"ds_read_b64_tr_b16 v[30:31], v40 offset:5120\n"
"ds_read_b64_tr_b16 v[44:45], v40 offset:8192\n"
"ds_read_b64_tr_b16 v[46:47], v40 offset:9216\n"
"ds_read_b64_tr_b16 v[48:49], v40 offset:12288\n"
"ds_read_b64_tr_b16 v[50:51], v40 offset:13312\n"
"s_waitcnt lgkmcnt(2)\n"
"buffer_store_dwordx4 v[24:27], v0, s[4:7], s24 offen sc1\n"
"s_waitcnt lgkmcnt(0)\n"
"buffer_store_dwordx4 v[28:31], v0, s[4:7], s24 offen offset:1024 sc1\n"
"buffer_store_dwordx4 v[44:47], v0, s[4:7], s25 offen sc1\n"
"buffer_store_dwordx4 v[48:51], v0, s[4:7], s25 offen offset:1024 sc1\n"
"s_add_u32 s98, s75, 0\n"
"buffer_load_dwordx2 v[254:255], v13, s[4:7], s98 offen sc1\n"
"s_add_u32 s97, s67, 0x1000\n"
"buffer_load_dwordx2 v[252:253], v12, s[4:7], s97 offen sc1\n"
"s_getpc_b64 s[84:85]\n"
".Lmk_h2:\n"
"s_and_b32 s84, s84, -16\n"
"s_and_b32 s85, s85, 0xffff\n"
"s_mov_b32 s86, (.Lmk_end-.Lmk_h2)&0xFFFFFFF0\n"
"s_mov_b32 s87, 0x20000\n"
"s_and_b32 s96, s26, 0xfffffc00\n"
"s_mov_b32 m0, 0x27800\n"
"s_add_u32 s96, s96, 0x1e000\n"
"buffer_load_dwordx4 v0, s[84:87], s96 offen lds\n"
"s_waitcnt vmcnt(2)\n"
"v_cmp_ne_u64_e32 vcc, v[254:255], v[18:19]\n"
"s_cbranch_vccnz .Lmk_f2s\n"
".Lmk_f2r:\n"
"s_waitcnt vmcnt(1)\n"
"v_cmp_ne_u64_e32 vcc, v[252:253], v[18:19]\n"
"s_cbranch_vccnz .Lmk_f1s\n"
".Lmk_f1r:\n"
"s_add_u32 s98, s52, 0x40000\n"
"buffer_load_dwordx4 v[24:27], v32, s[4:7], s98 offen sc1\n"
"buffer_load_dwordx4 v[28:31], v32, s[4:7], s98 offen offset:1024 sc1\n"
"buffer_load_dwordx4 v[44:47], v33, s[4:7], s98 offen sc1\n"
"buffer_load_dwordx4 v[48:51], v33, s[4:7], s98 offen offset:1024 sc1\n"
"buffer_load_dwordx4 v[52:55], v34, s[4:7], s98 offen sc1\n"
"buffer_load_dwordx4 v[56:59], v34, s[4:7], s98 offen offset:1024 sc1\n"
"buffer_load_dwordx4 v[60:63], v35, s[4:7], s98 offen sc1\n"
"buffer_load_dwordx4 v[64:67], v35, s[4:7], s98 offen offset:1024 sc1\n"
"s_add_u32 s98, s69, 0\n"
"buffer_load_dwordx4 a[0:3], v0, s[4:7], s98 offen sc1\n"
"buffer_load_dwordx4 a[4:7], v0, s[4:7], s98 offen offset:1024 sc1\n"
"buffer_load_dwordx4 a[8:11], v0, s[4:7], s98 offen offset:2048 sc1\n"
"buffer_load_dwordx4 a[12:15], v0, s[4:7], s98 offen offset:3072 sc1\n"
"buffer_load_dwordx4 a[16:19], v1, s[4:7], s98 offen sc1\n"
"buffer_load_dwordx4 a[20:23], v1, s[4:7], s98 offen offset:1024 sc1\n"
"buffer_load_dwordx4 a[24:27], v1, s[4:7], s98 offen offset:2048 sc1\n"
"buffer_load_dwordx4 a[28:31], v1, s[4:7], s98 offen offset:3072 sc1\n"
"s_waitcnt lgkmcnt(0)\n"
"s_barrier\n"
"s_add_u32 s98, s76, 0\n"
"v_bfe_u32 v255, v0, 2, 7\n"
"v_add_u32_e32 v255, s72, v255\n"
"v_and_b32_e32 v254, 0xffffffc0, v255\n"
"v_add_u32_e32 v255, v255, v254\n"
"v_and_b32_e32 v254, 0x200, v0\n"
"v_lshl_add_u32 v255, v254, 4, v255\n"
"buffer_load_dword v254, v255, s[4:7], s98 offen sc1\n"
"buffer_load_dword v253, v255, s[4:7], s98 offen offset:1024 sc1\n"
"buffer_load_dword v252, v255, s[4:7], s98 offen offset:2048 sc1\n"
"buffer_load_dword v251, v255, s[4:7], s98 offen offset:3072 sc1\n"
"s_add_u32 s98, s76, 0x1000\n"
"buffer_load_dword v250, v255, s[4:7], s98 offen sc1\n"
"buffer_load_dword v249, v255, s[4:7], s98 offen offset:1024 sc1\n"
"buffer_load_dword v248, v255, s[4:7], s98 offen offset:2048 sc1\n"
"buffer_load_dword v247, v255, s[4:7], s98 offen offset:3072 sc1\n"
"s_add_u32 s98, s69, 0x2000\n"
"buffer_load_dwordx4 a[32:35], v0, s[4:7], s98 offen sc1\n"
"buffer_load_dwordx4 a[36:39], v0, s[4:7], s98 offen offset:1024 sc1\n"
"buffer_load_dwordx4 a[40:43], v0, s[4:7], s98 offen offset:2048 sc1\n"
"buffer_load_dwordx4 a[44:47], v0, s[4:7], s98 offen offset:3072 sc1\n"
"buffer_load_dwordx4 a[48:51], v1, s[4:7], s98 offen sc1\n"
"buffer_load_dwordx4 a[52:55], v1, s[4:7], s98 offen offset:1024 sc1\n"
"buffer_load_dwordx4 a[56:59], v1, s[4:7], s98 offen offset:2048 sc1\n"
"buffer_load_dwordx4 a[60:63], v1, s[4:7], s98 offen offset:3072 sc1\n"
"s_add_u32 s98, s53, 0x40000\n"
"buffer_load_dwordx4 v[68:71], v32, s[4:7], s98 offen sc1\n"
"buffer_load_dwordx4 v[72:75], v32, s[4:7], s98 offen offset:1024 sc1\n"
"buffer_load_dwordx4 v[76:79], v33, s[4:7], s98 offen sc1\n"
"buffer_load_dwordx4 v[80:83], v33, s[4:7], s98 offen offset:1024 sc1\n"
"buffer_load_dwordx4 v[84:87], v34, s[4:7], s98 offen sc1\n"
"buffer_load_dwordx4 v[88:91], v34, s[4:7], s98 offen offset:1024 sc1\n"
"buffer_load_dwordx4 v[92:95], v35, s[4:7], s98 offen sc1\n"
"buffer_load_dwordx4 v[96:99], v35, s[4:7], s98 offen offset:1024 sc1\n"
"s_add_u32 s98, s69, 0x4000\n"
"buffer_load_dwordx4 a[64:67], v0, s[4:7], s98 offen sc1\n"
"buffer_load_dwordx4 a[68:71], v0, s[4:7], s98 offen offset:1024 sc1\n"
"buffer_load_dwordx4 a[72:75], v0, s[4:7], s98 offen offset:2048 sc1\n"
"buffer_load_dwordx4 a[76:79], v0, s[4:7], s98 offen offset:3072 sc1\n"
"buffer_load_dwordx4 a[80:83], v1, s[4:7], s98 offen sc1\n"
"buffer_load_dwordx4 a[84:87], v1, s[4:7], s98 offen offset:1024 sc1\n"
"buffer_load_dwordx4 a[88:91], v1, s[4:7], s98 offen offset:2048 sc1\n"
"buffer_load_dwordx4 a[92:95], v1, s[4:7], s98 offen offset:3072 sc1\n"
"s_add_u32 s98, s69, 0x6000\n"
"buffer_load_dwordx4 a[96:99], v0, s[4:7], s98 offen sc1\n"
"buffer_load_dwordx4 a[100:103], v0, s[4:7], s98 offen offset:1024 sc1\n"
"buffer_load_dwordx4 a[104:107], v0, s[4:7], s98 offen offset:2048 sc1\n"
"buffer_load_dwordx4 a[108:111], v0, s[4:7], s98 offen offset:3072 sc1\n"
"buffer_load_dwordx4 a[112:115], v1, s[4:7], s98 offen sc1\n"
"buffer_load_dwordx4 a[116:119], v1, s[4:7], s98 offen offset:1024 sc1\n"
"buffer_load_dwordx4 a[120:123], v1, s[4:7], s98 offen offset:2048 sc1\n"
"buffer_load_dwordx4 a[124:127], v1, s[4:7], s98 offen offset:3072 sc1\n"
"s_waitcnt vmcnt(32)\n"
"v_add_f32_e32 v254, v254, v253\n"
"v_add_f32_e32 v254, v254, v252\n"
"v_add_f32_e32 v254, v254, v251\n"
"v_add_f32_e32 v254, v254, v250\n"
"v_add_f32_e32 v254, v254, v249\n"
"v_add_f32_e32 v254, v254, v248\n"
"v_add_f32_e32 v254, v254, v247\n"
"v_mov_b32_e32 v253, v254\n"
"s_nop 1\n"
"v_permlane32_swap_b32_e32 v254, v253\n"
"v_add_f32_e32 v254, v254, v253\n"
"ds_write_b32 v21, v254 offset:0\n"
".Lmk_z0:\n"
"; @A G1 0.0\n"
"v_mfma_f32_16x16x32_bf16 v[100:103], a[0:3], v[24:27], 0\n"
"s_add_u32 s98, s69, 0x8000\n"
"buffer_load_dwordx4 v[104:107], v0, s[4:7], s98 offen sc1\n"
"v_mfma_f32_16x16x32_bf16 v[100:103], a[4:7], v[28:31], v[100:103]\n"
"s_add_u32 s97, s62, 0x4000\n"
"v_mfma_f32_16x16x32_bf16 v[100:103], a[8:11], v[44:47], v[100:103]\n"
"buffer_load_dwordx4 v[108:111], v0, s[4:7], s98 offen offset:1024 sc1\n"
"v_mfma_f32_16x16x32_bf16 v[100:103], a[12:15], v[48:51], v[100:103]\n"
"v_mfma_f32_16x16x32_bf16 v[100:103], a[16:19], v[52:55], v[100:103]\n"
"buffer_load_dwordx4 v[112:115], v0, s[4:7], s98 offen offset:2048 sc1\n"
"v_mfma_f32_16x16x32_bf16 v[100:103], a[20:23], v[56:59], v[100:103]\n"
"buffer_load_dword v255, v10, s[4:7], s97 offen sc1\n"
"ds_read_b64_tr_b16 v[116:117], v36\n"
"v_mfma_f32_16x16x32_bf16 v[100:103], a[24:27], v[60:63], v[100:103]\n"
"buffer_load_dwordx4 v[120:123], v0, s[4:7], s98 offen offset:3072 sc1\n"
"ds_read_b64_tr_b16 v[118:119], v36 offset:2048\n"
"v_mfma_f32_16x16x32_bf16 v[100:103], a[28:31], v[64:67], v[100:103]\n"
"s_waitcnt vmcnt(29)\n"
"s_mov_b32 s94, 0x30003\n"
"s_mov_b32 s95, 0x30003\n"
"v_mov_b32_e32 v127, 0x3f803f80\n"
"v_mfma_f32_16x16x32_bf16 v[128:131], a[60:63], v[64:67], 0\n"
"buffer_load_dwordx4 v[132:135], v1, s[4:7], s98 offen sc1\n"
"v_cndmask_b32_e64 v124, 0, v127, s[94:95]\n"
"v_mfma_f32_16x16x32_bf16 v[128:131], a[56:59], v[60:63], v[128:131]\n"
"buffer_load_dword v254, v10, s[4:7], s97 offen offset:256 sc1\n"
"v_cndmask_b32_e64 v125, 0, v127, s[94:95]\n"
"v_mfma_f32_16x16x32_bf16 v[128:131], a[52:55], v[56:59], v[128:131]\n"
"buffer_load_dwordx4 v[136:139], v1, s[4:7], s98 offen offset:1024 sc1\n"
"v_cndmask_b32_e64 v126, 0, v127, s[94:95]\n"
"v_mfma_f32_16x16x32_bf16 v[128:131], a[48:51], v[52:55], v[128:131]\n"
"s_add_u32 s96, s68, 0x10000\n"
"v_cndmask_b32_e64 v127, 0, v127, s[94:95]\n"
"v_mfma_f32_16x16x32_bf16 v[128:131], a[44:47], v[48:51], v[128:131]\n"
"buffer_load_dwordx4 v[140:143], v1, s[4:7], s98 offen offset:2048 sc1\n"
"s_waitcnt lgkmcnt(0)\n"
"v_mfma_f32_16x16x32_bf16 v[144:147], v[116:119], v[124:127], 0\n"
"buffer_store_dwordx4 v[116:119], v0, s[4:7], s20 offen sc1\n"
"v_mfma_f32_16x16x32_bf16 v[128:131], a[40:43], v[44:47], v[128:131]\n"
"ds_write_b128 v3, v[100:103]\n"
"v_mfma_f32_16x16x32_bf16 v[128:131], a[36:39], v[28:31], v[128:131]\n"
"buffer_load_dwordx4 v[100:103], v1, s[4:7], s98 offen offset:3072 sc1\n"
"v_mfma_f32_16x16x32_bf16 v[128:131], a[32:35], v[24:27], v[128:131]\n"
"s_waitcnt vmcnt(23)\n"
"s_add_u32 s98, s69, 0xa000\n"
"v_mfma_f32_16x16x32_bf16 v[116:119], a[64:67], v[24:27], 0\n"
"buffer_load_dwordx4 v[124:127], v0, s[4:7], s98 offen sc1\n"
"v_mfma_f32_16x16x32_bf16 v[116:119], a[68:71], v[28:31], v[116:119]\n"
"buffer_store_dwordx2 v[18:19], v11, s[4:7], s96 offen offset:96 sc1\n"
"v_mfma_f32_16x16x32_bf16 v[116:119], a[72:75], v[44:47], v[116:119]\n"
"buffer_load_dwordx4 v[148:151], v0, s[4:7], s98 offen offset:1024 sc1\n"
"v_mfma_f32_16x16x32_bf16 v[116:119], a[76:79], v[48:51], v[116:119]\n"
"s_waitcnt vmcnt(22)\n"
"buffer_load_dword v253, v10, s[4:7], s97 offen offset:512 sc1\n"
"v_mfma_f32_16x16x32_bf16 v[116:119], a[80:83], v[52:55], v[116:119]\n"
"buffer_load_dwordx4 v[152:155], v0, s[4:7], s98 offen offset:2048 sc1\n"
"v_mfma_f32_16x16x32_bf16 v[116:119], a[84:87], v[56:59], v[116:119]\n"
"ds_write_b128 v3, v[128:131] offset:1024\n"
"v_mfma_f32_16x16x32_bf16 v[116:119], a[88:91], v[60:63], v[116:119]\n"
"buffer_load_dwordx4 v[128:131], v0, s[4:7], s98 offen offset:3072 sc1\n"
"v_mfma_f32_16x16x32_bf16 v[116:119], a[92:95], v[64:67], v[116:119]\n"
"s_waitcnt vmcnt(17)\n"
"buffer_load_dword v252, v10, s[4:7], s97 offen offset:768 sc1\n"
"v_mfma_f32_16x16x32_bf16 v[156:159], a[124:127], v[64:67], 0\n"
"buffer_load_dwordx4 v[160:163], v1, s[4:7], s98 offen sc1\n"
"v_mfma_f32_16x16x32_bf16 v[156:159], a[120:123], v[60:63], v[156:159]\n"
"v_mfma_f32_16x16x32_bf16 v[156:159], a[116:119], v[56:59], v[156:159]\n"
"buffer_load_dwordx4 v[164:167], v1, s[4:7], s98 offen offset:1024 sc1\n"
"v_mfma_f32_16x16x32_bf16 v[156:159], a[112:115], v[52:55], v[156:159]\n"
"buffer_load_dword v251, v10, s[4:7], s97 offen offset:1024 sc1\n"
"v_mfma_f32_16x16x32_bf16 v[156:159], a[108:111], v[48:51], v[156:159]\n"
"buffer_load_dwordx4 v[168:171], v1, s[4:7], s98 offen offset:2048 sc1\n"
"v_mfma_f32_16x16x32_bf16 v[156:159], a[104:107], v[44:47], v[156:159]\n"
"ds_write_b128 v3, v[116:119] offset:2048\n"
"v_mfma_f32_16x16x32_bf16 v[156:159], a[100:103], v[28:31], v[156:159]\n"
"buffer_load_dwordx4 v[116:119], v1, s[4:7], s98 offen offset:3072 sc1\n"
"v_mfma_f32_16x16x32_bf16 v[156:159], a[96:99], v[24:27], v[156:159]\n"
"s_waitcnt vmcnt(18)\n"
"buffer_load_dword v250, v10, s[4:7], s97 offen offset:1280 sc1\n"
"v_mfma_f32_16x16x32_bf16 v[172:175], v[104:107], v[24:27], 0\n"
"s_add_u32 s98, s54, 0x40000\n"
"buffer_load_dwordx4 v[176:179], v32, s[4:7], s98 offen sc1\n"
"v_mfma_f32_16x16x32_bf16 v[172:175], v[108:111], v[28:31], v[172:175]\n"
"v_mfma_f32_16x16x32_bf16 v[172:175], v[112:115], v[44:47], v[172:175]\n"
"buffer_load_dword v249, v10, s[4:7], s97 offen offset:1536 sc1\n"
"v_mfma_f32_16x16x32_bf16 v[172:175], v[120:123], v[48:51], v[172:175]\n"
"s_waitcnt vmcnt(15)\n"
"v_mfma_f32_16x16x32_bf16 v[172:175], v[132:135], v[52:55], v[172:175]\n"
"buffer_load_dwordx4 v[180:183], v32, s[4:7], s98 offen offset:1024 sc1\n"
"v_mfma_f32_16x16x32_bf16 v[172:175], v[136:139], v[56:59], v[172:175]\n"
"ds_write_b128 v3, v[156:159] offset:3072\n"
"v_mfma_f32_16x16x32_bf16 v[172:175], v[140:143], v[60:63], v[172:175]\n"
"buffer_load_dword v248, v10, s[4:7], s97 offen offset:1792 sc1\n"
"v_mfma_f32_16x16x32_bf16 v[172:175], v[100:103], v[64:67], v[172:175]\n"
"s_waitcnt vmcnt(5)\n"
"v_mfma_f32_16x16x32_bf16 v[156:159], v[116:119], v[64:67], 0\n"
"buffer_load_dwordx4 v[64:67], v33, s[4:7], s98 offen sc1\n"
"ds_read_b64_tr_b16 v[184:185], v36 offset:4096\n"
"v_mfma_f32_16x16x32_bf16 v[156:159], v[168:171], v[60:63], v[156:159]\n"
"ds_read_b64_tr_b16 v[186:187], v36 offset:6144\n"
"s_mov_b32 s96, 0x30003\n"
"s_mov_b32 s97, 0x30003\n"
"v_mfma_f32_16x16x32_bf16 v[156:159], v[164:167], v[56:59], v[156:159]\n"
"v_mov_b32_e32 v59, 0x3f803f80\n"
"v_cndmask_b32_e64 v56, 0, v59, s[96:97]\n"
"v_cndmask_b32_e64 v57, 0, v59, s[96:97]\n"
"v_mfma_f32_16x16x32_bf16 v[156:159], v[160:163], v[52:55], v[156:159]\n"
"v_cndmask_b32_e64 v58, 0, v59, s[96:97]\n"
"v_cndmask_b32_e64 v59, 0, v59, s[96:97]\n"
"v_mfma_f32_16x16x32_bf16 v[156:159], v[128:131], v[48:51], v[156:159]\n"
"buffer_load_dwordx4 v[48:51], v33, s[4:7], s98 offen offset:1024 sc1\n"
"s_waitcnt lgkmcnt(0)\n"
"v_mfma_f32_16x16x32_bf16 v[144:147], v[184:187], v[56:59], v[144:147]\n"
"buffer_store_dwordx4 v[184:187], v0, s[4:7], s20 offen offset:1024 sc1\n"
"v_mfma_f32_16x16x32_bf16 v[156:159], v[152:155], v[44:47], v[156:159]\n"
"ds_write_b128 v3, v[172:175] offset:4096\n"
"v_mfma_f32_16x16x32_bf16 v[156:159], v[148:151], v[28:31], v[156:159]\n"
"v_mfma_f32_16x16x32_bf16 v[156:159], v[124:127], v[24:27], v[156:159]\n"
"; @A G1 0.1\n"
"v_mfma_f32_16x16x32_bf16 v[24:27], a[0:3], v[68:71], 0\n"
"buffer_load_dwordx4 v[28:31], v34, s[4:7], s98 offen sc1\n"
"v_mfma_f32_16x16x32_bf16 v[24:27], a[4:7], v[72:75], v[24:27]\n"
"v_mfma_f32_16x16x32_bf16 v[24:27], a[8:11], v[76:79], v[24:27]\n"
"s_add_u32 s97, s55, 0x40000\n"
"buffer_load_dwordx4 v[44:47], v32, s[4:7], s97 offen sc1\n"
"v_mfma_f32_16x16x32_bf16 v[24:27], a[12:15], v[80:83], v[24:27]\n"
"v_mfma_f32_16x16x32_bf16 v[24:27], a[16:19], v[84:87], v[24:27]\n"
"buffer_load_dwordx4 v[52:55], v34, s[4:7], s98 offen offset:1024 sc1\n"
"v_mfma_f32_16x16x32_bf16 v[24:27], a[20:23], v[88:91], v[24:27]\n"
"ds_write_b128 v3, v[156:159] offset:5120\n"
"v_mfma_f32_16x16x32_bf16 v[24:27], a[24:27], v[92:95], v[24:27]\n"
"v_mfma_f32_16x16x32_bf16 v[24:27], a[28:31], v[96:99], v[24:27]\n"
"buffer_load_dwordx4 v[56:59], v32, s[4:7], s97 offen offset:1024 sc1\n"
"v_mfma_f32_16x16x32_bf16 v[60:63], a[60:63], v[96:99], 0\n"
"buffer_load_dwordx4 v[156:159], v35, s[4:7], s98 offen sc1\n"
"v_mfma_f32_16x16x32_bf16 v[60:63], a[56:59], v[92:95], v[60:63]\n"
"v_mfma_f32_16x16x32_bf16 v[60:63], a[52:55], v[88:91], v[60:63]\n"
"v_mfma_f32_16x16x32_bf16 v[60:63], a[48:51], v[84:87], v[60:63]\n"
"v_mfma_f32_16x16x32_bf16 v[60:63], a[44:47], v[80:83], v[60:63]\n"
"buffer_load_dwordx4 v[172:175], v35, s[4:7], s98 offen offset:1024 sc1\n"
"v_mfma_f32_16x16x32_bf16 v[60:63], a[40:43], v[76:79], v[60:63]\n"
"buffer_load_dwordx4 v[184:187], v33, s[4:7], s97 offen sc1\n"
"v_mfma_f32_16x16x32_bf16 v[60:63], a[36:39], v[72:75], v[60:63]\n"
"ds_write_b128 v3, v[24:27] offset:6144\n"
"v_mfma_f32_16x16x32_bf16 v[60:63], a[32:35], v[68:71], v[60:63]\n"
"v_mfma_f32_16x16x32_bf16 v[24:27], a[64:67], v[68:71], 0\n"
"v_mfma_f32_16x16x32_bf16 v[24:27], a[68:71], v[72:75], v[24:27]\n"
"buffer_load_dwordx4 v[188:191], v33, s[4:7], s97 offen offset:1024 sc1\n"
"v_mfma_f32_16x16x32_bf16 v[24:27], a[72:75], v[76:79], v[24:27]\n"
"v_mfma_f32_16x16x32_bf16 v[24:27], a[76:79], v[80:83], v[24:27]\n"
"v_mfma_f32_16x16x32_bf16 v[24:27], a[80:83], v[84:87], v[24:27]\n"
"v_mfma_f32_16x16x32_bf16 v[24:27], a[84:87], v[88:91], v[24:27]\n"
"s_nop 0\n"
"ds_write_b128 v3, v[60:63] offset:7168\n"
"v_mfma_f32_16x16x32_bf16 v[24:27], a[88:91], v[92:95], v[24:27]\n"
"buffer_load_dwordx4 v[60:63], v34, s[4:7], s97 offen sc1\n"
"v_mfma_f32_16x16x32_bf16 v[24:27], a[92:95], v[96:99], v[24:27]\n"
"v_mfma_f32_16x16x32_bf16 v[192:195], a[124:127], v[96:99], 0\n"
"v_mfma_f32_16x16x32_bf16 v[192:195], a[120:123], v[92:95], v[192:195]\n"
"v_mfma_f32_16x16x32_bf16 v[192:195], a[116:119], v[88:91], v[192:195]\n"
"v_mfma_f32_16x16x32_bf16 v[192:195], a[112:115], v[84:87], v[192:195]\n"
"buffer_load_dwordx4 v[196:199], v34, s[4:7], s97 offen offset:1024 sc1\n"
"v_mfma_f32_16x16x32_bf16 v[192:195], a[108:111], v[80:83], v[192:195]\n"
"v_mfma_f32_16x16x32_bf16 v[192:195], a[104:107], v[76:79], v[192:195]\n"
"s_nop 0\n"
"ds_write_b128 v3, v[24:27] offset:8192\n"
"v_mfma_f32_16x16x32_bf16 v[192:195], a[100:103], v[72:75], v[192:195]\n"
"v_mfma_f32_16x16x32_bf16 v[192:195], a[96:99], v[68:71], v[192:195]\n"
"v_mfma_f32_16x16x32_bf16 v[24:27], v[104:107], v[68:71], 0\n"
"buffer_load_dwordx4 v[200:203], v35, s[4:7], s97 offen sc1\n"
"v_mfma_f32_16x16x32_bf16 v[24:27], v[108:111], v[72:75], v[24:27]\n"
"v_mfma_f32_16x16x32_bf16 v[24:27], v[112:115], v[76:79], v[24:27]\n"
"v_mfma_f32_16x16x32_bf16 v[24:27], v[120:123], v[80:83], v[24:27]\n"
"v_mfma_f32_16x16x32_bf16 v[24:27], v[132:135], v[84:87], v[24:27]\n"
"s_waitcnt vmcnt(14)\n"
"ds_read_b64_tr_b16 v[204:205], v37\n"
"v_add_f32_e32 v255, v255, v251\n"
"v_add_f32_e32 v254, v254, v250\n"
"v_mfma_f32_16x16x32_bf16 v[24:27], v[136:139], v[88:91], v[24:27]\n"
"buffer_load_dwordx4 v[208:211], v35, s[4:7], s97 offen offset:1024 sc1\n"
"ds_read_b64_tr_b16 v[206:207], v37 offset:2048\n"
"v_mfma_f32_16x16x32_bf16 v[24:27], v[140:143], v[92:95], v[24:27]\n"
"ds_write_b128 v3, v[192:195] offset:9216\n"
"v_mfma_f32_16x16x32_bf16 v[24:27], v[100:103], v[96:99], v[24:27]\n"
"s_mov_b32 s96, 0xc000c\n"
"v_add_f32_e32 v253, v253, v249\n"
"v_add_f32_e32 v252, v252, v248\n"
"s_mov_b32 s97, 0xc000c\n"
"v_mfma_f32_16x16x32_bf16 v[192:195], v[116:119], v[96:99], 0\n"
"v_add_f32_e32 v255, v255, v253\n"
"v_add_f32_e32 v254, v254, v252\n"
"v_mov_b32_e32 v99, 0x3f803f80\n"
"v_mfma_f32_16x16x32_bf16 v[192:195], v[168:171], v[92:95], v[192:195]\n"
"v_add_f32_e32 v255, v255, v254\n"
"v_mov_b32_e32 v254, v255\n"
"v_cndmask_b32_e64 v96, 0, v99, s[96:97]\n"
"v_mfma_f32_16x16x32_bf16 v[192:195], v[164:167], v[88:91], v[192:195]\n"
"v_permlane32_swap_b32_e32 v255, v254\n"
"v_add_f32_e32 v255, v255, v254\n"
"v_cndmask_b32_e64 v97, 0, v99, s[96:97]\n"
"v_mfma_f32_16x16x32_bf16 v[192:195], v[160:163], v[84:87], v[192:195]\n"
"v_mul_f32_e32 v255, 0x3a800000, v255\n"
"v_mov_b32_e32 v254, v255\n"
"v_cndmask_b32_e64 v98, 0, v99, s[96:97]\n"
"v_mfma_f32_16x16x32_bf16 v[192:195], v[128:131], v[80:83], v[192:195]\n"
"v_permlane16_swap_b32_e32 v255, v254\n"
"v_cvt_pk_bf16_f32 v253, -v255, 0\n"
"v_cndmask_b32_e64 v99, 0, v99, s[96:97]\n"
"v_mfma_f32_16x16x32_bf16 v[192:195], v[152:155], v[76:79], v[192:195]\n"
"ds_write_b128 v3, v[24:27] offset:10240\n"
"v_mfma_f32_16x16x32_bf16 v[192:195], v[148:151], v[72:75], v[192:195]\n"
"s_waitcnt lgkmcnt(2)\n"
"v_fma_f32 v254, -v255, v255, v254\n"
"v_lshlrev_b32_e32 v253, 16, v253\n"
"v_mfma_f32_16x16x32_bf16 v[144:147], v[204:207], v[96:99], v[144:147]\n"
"buffer_store_dwordx4 v[204:207], v0, s[4:7], s21 offen sc1\n"
"v_mfma_f32_16x16x32_bf16 v[192:195], v[124:127], v[68:71], v[192:195]\n"
"s_waitcnt vmcnt(14)\n"
"v_max_f32_e32 v254, 0, v254\n"
"v_cvt_pk_bf16_f32 v254, v254, 0\n"
"; @A G1 0.2\n"
"v_mfma_f32_16x16x32_bf16 v[24:27], a[0:3], v[176:179], 0\n"
"v_lshlrev_b32_e32 v254, 16, v254\n"
"v_add_f32_e32 v254, 0x3727c5ac, v254\n"
"v_mfma_f32_16x16x32_bf16 v[24:27], a[4:7], v[180:183], v[24:27]\n"
"v_cvt_pk_bf16_f32 v254, v254, 0\n"
"v_lshlrev_b32_e32 v254, 16, v254\n"
"v_mfma_f32_16x16x32_bf16 v[24:27], a[8:11], v[64:67], v[24:27]\n"
"v_sqrt_f32_e32 v254, v254\n"
"v_mfma_f32_16x16x32_bf16 v[24:27], a[12:15], v[48:51], v[24:27]\n"
"s_waitcnt vmcnt(7)\n"
"v_cvt_pk_bf16_f32 v254, v254, 0\n"
"v_lshlrev_b32_e32 v254, 16, v254\n"
"v_mfma_f32_16x16x32_bf16 v[24:27], a[16:19], v[28:31], v[24:27]\n"
"v_rcp_f32_e32 v255, v254\n"
"v_mfma_f32_16x16x32_bf16 v[24:27], a[20:23], v[52:55], v[24:27]\n"
"ds_write_b128 v3, v[192:195] offset:11264\n"
"v_mfma_f32_16x16x32_bf16 v[24:27], a[24:27], v[156:159], v[24:27]\n"
"v_mul_f32_e32 v254, 0x3e8293ee, v255\n"
"v_mfma_f32_16x16x32_bf16 v[24:27], a[28:31], v[172:175], v[24:27]\n"
"v_mfma_f32_16x16x32_bf16 v[68:71], a[60:63], v[172:175], 0\n"
"v_mfma_f32_16x16x32_bf16 v[68:71], a[56:59], v[156:159], v[68:71]\n"
"v_mfma_f32_16x16x32_bf16 v[68:71], a[52:55], v[52:55], v[68:71]\n"
"s_add_u32 s98, s52, 0x48000\n"
"buffer_load_dwordx4 v[72:75], v32, s[4:7], s98 offen sc1\n"
"v_mfma_f32_16x16x32_bf16 v[68:71], a[48:51], v[28:31], v[68:71]\n"
"s_waitcnt lgkmcnt(0)\n"
"s_barrier\n"
"v_mfma_f32_16x16x32_bf16 v[68:71], a[44:47], v[48:51], v[68:71]\n"
"ds_read_b128 v[76:79], v4\n"
"ds_read_b128 v[80:83], v9 offset:128\n"
"v_mfma_f32_16x16x32_bf16 v[68:71], a[40:43], v[64:67], v[68:71]\n"
"ds_write_b128 v3, v[24:27] offset:12288\n"
"v_mfma_f32_16x16x32_bf16 v[68:71], a[36:39], v[180:183], v[68:71]\n"
"ds_read_b128 v[24:27], v5\n"
"v_mfma_f32_16x16x32_bf16 v[68:71], a[32:35], v[176:179], v[68:71]\n"
"buffer_load_dwordx4 v[84:87], v32, s[4:7], s98 offen offset:1024 sc1\n"
"v_mfma_f32_16x16x32_bf16 v[88:91], a[64:67], v[176:179], 0\n"
"v_mfma_f32_16x16x32_bf16 v[88:91], a[68:71], v[180:183], v[88:91]\n"
"v_mfma_f32_16x16x32_bf16 v[88:91], a[72:75], v[64:67], v[88:91]\n"
"v_mfma_f32_16x16x32_bf16 v[88:91], a[76:79], v[48:51], v[88:91]\n"
"ds_read_b128 v[92:95], v4 offset:1024\n"
"ds_read_b128 v[96:99], v9 offset:192\n"
"v_mfma_f32_16x16x32_bf16 v[88:91], a[80:83], v[28:31], v[88:91]\n"
"buffer_load_dwordx4 v[192:195], v33, s[4:7], s98 offen sc1\n"
"v_mfma_f32_16x16x32_bf16 v[88:91], a[84:87], v[52:55], v[88:91]\n"
"ds_write_b128 v3, v[68:71] offset:13312\n"
"v_mfma_f32_16x16x32_bf16 v[88:91], a[88:91], v[156:159], v[88:91]\n"
"ds_read_b128 v[68:71], v5 offset:1024\n"
"s_waitcnt lgkmcnt(4)\n"
"v_mfma_f32_16x16x32_bf16 v[88:91], a[92:95], v[172:175], v[88:91]\n"
"v_fmac_f32_e32 v76, v253, v80\n"
"v_fmac_f32_e32 v77, v253, v81\n"
"v_mfma_f32_16x16x32_bf16 v[204:207], a[124:127], v[172:175], 0\n"
"v_fmac_f32_e32 v78, v253, v82\n"
"v_fmac_f32_e32 v79, v253, v83\n"
"v_mfma_f32_16x16x32_bf16 v[204:207], a[120:123], v[156:159], v[204:207]\n"
"buffer_load_dwordx4 v[80:83], v33, s[4:7], s98 offen offset:1024 sc1\n"
"v_mfma_f32_16x16x32_bf16 v[204:207], a[116:119], v[52:55], v[204:207]\n"
"ds_read_b128 v[212:215], v4 offset:2048\n"
"ds_read_b128 v[216:219], v9 offset:256\n"
"v_mfma_f32_16x16x32_bf16 v[204:207], a[112:115], v[28:31], v[204:207]\n"
"ds_read_b128 v[220:223], v5 offset:2048\n"
"v_add_f32_e32 v76, v76, v24\n"
"v_mfma_f32_16x16x32_bf16 v[204:207], a[108:111], v[48:51], v[204:207]\n"
"v_add_f32_e32 v77, v77, v25\n"
"v_add_f32_e32 v78, v78, v26\n"
"v_mfma_f32_16x16x32_bf16 v[204:207], a[104:107], v[64:67], v[204:207]\n"
"ds_write_b128 v3, v[88:91] offset:14336\n"
"v_mfma_f32_16x16x32_bf16 v[204:207], a[100:103], v[180:183], v[204:207]\n"
"buffer_load_dwordx4 v[88:91], v34, s[4:7], s98 offen sc1\n"
"v_mfma_f32_16x16x32_bf16 v[204:207], a[96:99], v[176:179], v[204:207]\n"
"s_waitcnt lgkmcnt(4)\n"
"v_add_f32_e32 v79, v79, v27\n"
"v_fmac_f32_e32 v92, v253, v96\n"
"v_mfma_f32_16x16x32_bf16 v[24:27], v[104:107], v[176:179], 0\n"
"ds_read_b64_tr_b16 v[224:225], v37 offset:4096\n"
"v_fmac_f32_e32 v93, v253, v97\n"
"v_fmac_f32_e32 v94, v253, v98\n"
"v_mfma_f32_16x16x32_bf16 v[24:27], v[108:111], v[180:183], v[24:27]\n"
"ds_read_b128 v[228:231], v4 offset:3072\n"
"ds_read_b128 v[232:235], v9 offset:320\n"
"ds_read_b64_tr_b16 v[226:227], v37 offset:6144\n"
"v_mfma_f32_16x16x32_bf16 v[24:27], v[112:115], v[64:67], v[24:27]\n"
"ds_read_b128 v[236:239], v5 offset:3072\n"
"v_fmac_f32_e32 v95, v253, v99\n"
"s_mov_b32 s96, 0xc000c\n"
"v_mfma_f32_16x16x32_bf16 v[24:27], v[120:123], v[48:51], v[24:27]\n"
"buffer_load_dwordx4 v[96:99], v34, s[4:7], s98 offen offset:1024 sc1\n"
"s_mov_b32 s97, 0xc000c\n"
"v_mfma_f32_16x16x32_bf16 v[24:27], v[132:135], v[28:31], v[24:27]\n"
"v_add_f32_e32 v92, v92, v68\n"
"v_add_f32_e32 v93, v93, v69\n"
"v_mov_b32_e32 v243, 0x3f803f80\n"
"v_mfma_f32_16x16x32_bf16 v[24:27], v[136:139], v[52:55], v[24:27]\n"
"ds_write_b128 v3, v[204:207] offset:15360\n"
"v_mfma_f32_16x16x32_bf16 v[24:27], v[140:143], v[156:159], v[24:27]\n"
"s_waitcnt lgkmcnt(7)\n"
"v_add_f32_e32 v94, v94, v70\n"
"v_add_f32_e32 v95, v95, v71\n"
"v_cndmask_b32_e64 v240, 0, v243, s[96:97]\n"
"v_mfma_f32_16x16x32_bf16 v[24:27], v[100:103], v[172:175], v[24:27]\n"
"v_fmac_f32_e32 v212, v253, v216\n"
"v_fmac_f32_e32 v213, v253, v217\n"
"v_cndmask_b32_e64 v241, 0, v243, s[96:97]\n"
"v_mfma_f32_16x16x32_bf16 v[68:71], v[116:119], v[172:175], 0\n"
"buffer_load_dwordx4 v[172:175], v35, s[4:7], s98 offen sc1\n"
"v_cndmask_b32_e64 v242, 0, v243, s[96:97]\n"
"v_mfma_f32_16x16x32_bf16 v[68:71], v[168:171], v[156:159], v[68:71]\n"
"s_add_u32 s95, s68, s80\n"
"ds_read_b128 v[156:159], v4 offset:4096\n"
"v_cndmask_b32_e64 v243, 0, v243, s[96:97]\n"
"v_mfma_f32_16x16x32_bf16 v[68:71], v[164:167], v[52:55], v[68:71]\n"
"buffer_store_dwordx2 v[18:19], v11, s[4:7], s95 offen sc1\n"
"v_mfma_f32_16x16x32_bf16 v[68:71], v[160:163], v[28:31], v[68:71]\n"
"ds_read_b128 v[28:31], v9\n"
"ds_read_b128 v[52:55], v5 offset:4096\n"
"s_waitcnt lgkmcnt(5)\n"
"v_mfma_f32_16x16x32_bf16 v[144:147], v[224:227], v[240:243], v[144:147]\n"
"buffer_store_dwordx4 v[224:227], v0, s[4:7], s21 offen offset:1024 sc1\n"
"v_mfma_f32_16x16x32_bf16 v[68:71], v[128:131], v[48:51], v[68:71]\n"
"s_add_u32 s97, s75, 0x10000\n"
"buffer_load_dwordx2 v[250:251], v41, s[4:7], s97 offen sc1\n"
"v_mfma_f32_16x16x32_bf16 v[68:71], v[152:155], v[64:67], v[68:71]\n"
"buffer_load_dwordx4 v[48:51], v35, s[4:7], s98 offen offset:1024 sc1\n"
"v_mfma_f32_16x16x32_bf16 v[68:71], v[148:151], v[180:183], v[68:71]\n"
"ds_write_b128 v3, v[24:27] offset:16384\n"
"v_mfma_f32_16x16x32_bf16 v[68:71], v[124:127], v[176:179], v[68:71]\n"
"s_waitcnt vmcnt(16)\n"
"ds_read_b128 v[24:27], v4 offset:5120\n"
"ds_read_b128 v[64:67], v9 offset:64\n"
"; @A G1 0.3\n"
"v_mfma_f32_16x16x32_bf16 v[76:79], a[0:3], v[44:47], v[76:79]\n"
"ds_read_b128 v[176:179], v5 offset:5120\n"
"v_fmac_f32_e32 v214, v253, v218\n"
"v_mfma_f32_16x16x32_bf16 v[76:79], a[4:7], v[56:59], v[76:79]\n"
"v_fmac_f32_e32 v215, v253, v219\n"
"v_add_f32_e32 v212, v212, v220\n"
"v_mfma_f32_16x16x32_bf16 v[76:79], a[8:11], v[184:187], v[76:79]\n"
"s_add_u32 s98, s53, 0x48000\n"
"buffer_load_dwordx4 v[180:183], v32, s[4:7], s98 offen sc1\n"
"v_mfma_f32_16x16x32_bf16 v[76:79], a[12:15], v[188:191], v[76:79]\n"
"s_waitcnt vmcnt(13)\n"
"v_add_f32_e32 v213, v213, v221\n"
"v_add_f32_e32 v214, v214, v222\n"
"v_mfma_f32_16x16x32_bf16 v[76:79], a[16:19], v[60:63], v[76:79]\n"
"s_waitcnt lgkmcnt(8)\n"
"v_add_f32_e32 v215, v215, v223\n"
"v_fmac_f32_e32 v228, v253, v232\n"
"v_mfma_f32_16x16x32_bf16 v[76:79], a[20:23], v[196:199], v[76:79]\n"
"ds_write_b128 v3, v[68:71] offset:17408\n"
"v_mfma_f32_16x16x32_bf16 v[76:79], a[24:27], v[200:203], v[76:79]\n"
"v_fmac_f32_e32 v229, v253, v233\n"
"v_fmac_f32_e32 v230, v253, v234\n"
"v_mfma_f32_16x16x32_bf16 v[76:79], a[28:31], v[208:211], v[76:79]\n"
"buffer_load_dwordx4 v[68:71], v32, s[4:7], s98 offen offset:1024 sc1\n"
"v_mfma_f32_16x16x32_bf16 v[92:95], a[60:63], v[208:211], v[92:95]\n"
"v_fmac_f32_e32 v231, v253, v235\n"
"v_add_f32_e32 v228, v228, v236\n"
"v_mfma_f32_16x16x32_bf16 v[92:95], a[56:59], v[200:203], v[92:95]\n"
"v_add_f32_e32 v229, v229, v237\n"
"v_add_f32_e32 v230, v230, v238\n"
"v_mfma_f32_16x16x32_bf16 v[92:95], a[52:55], v[196:199], v[92:95]\n"
"s_waitcnt lgkmcnt(0)\n"
"s_barrier\n"
"v_mfma_f32_16x16x32_bf16 v[92:95], a[48:51], v[60:63], v[92:95]\n"
"ds_read_b128 v[204:207], v6\n"
"ds_read_b128 v[216:219], v6 offset:1024\n"
"v_mfma_f32_16x16x32_bf16 v[92:95], a[44:47], v[188:191], v[92:95]\n"
"buffer_load_dwordx4 v[220:223], v33, s[4:7], s98 offen sc1\n"
"v_mfma_f32_16x16x32_bf16 v[92:95], a[40:43], v[184:187], v[92:95]\n"
"v_add_f32_e32 v231, v231, v239\n"
"v_fmac_f32_e32 v156, v253, v28\n"
"v_mfma_f32_16x16x32_bf16 v[92:95], a[36:39], v[56:59], v[92:95]\n"
"v_fmac_f32_e32 v157, v253, v29\n"
"v_fmac_f32_e32 v158, v253, v30\n"
"v_mfma_f32_16x16x32_bf16 v[92:95], a[32:35], v[44:47], v[92:95]\n"
"v_fmac_f32_e32 v159, v253, v31\n"
"v_add_f32_e32 v156, v156, v52\n"
"v_mfma_f32_16x16x32_bf16 v[212:215], a[64:67], v[44:47], v[212:215]\n"
"v_add_f32_e32 v157, v157, v53\n"
"v_add_f32_e32 v158, v158, v54\n"
"v_mfma_f32_16x16x32_bf16 v[212:215], a[68:71], v[56:59], v[212:215]\n"
"buffer_load_dwordx4 v[28:31], v33, s[4:7], s98 offen offset:1024 sc1\n"
"v_mfma_f32_16x16x32_bf16 v[212:215], a[72:75], v[184:187], v[212:215]\n"
"v_add_f32_e32 v159, v159, v55\n"
"v_fmac_f32_e32 v24, v253, v64\n"
"v_mfma_f32_16x16x32_bf16 v[212:215], a[76:79], v[188:191], v[212:215]\n"
"v_fmac_f32_e32 v25, v253, v65\n"
"v_fmac_f32_e32 v26, v253, v66\n"
"v_mfma_f32_16x16x32_bf16 v[212:215], a[80:83], v[60:63], v[212:215]\n"
"s_waitcnt lgkmcnt(0)\n"
"v_fmac_f32_e32 v27, v253, v67\n"
"v_add_f32_e32 v24, v24, v176\n"
"v_mfma_f32_16x16x32_bf16 v[212:215], a[84:87], v[196:199], v[212:215]\n"
"v_add_f32_e32 v25, v25, v177\n"
"v_add_f32_e32 v26, v26, v178\n"
"v_mfma_f32_16x16x32_bf16 v[212:215], a[88:91], v[200:203], v[212:215]\n"
"buffer_load_dwordx4 v[52:55], v34, s[4:7], s98 offen sc1\n"
"v_mfma_f32_16x16x32_bf16 v[212:215], a[92:95], v[208:211], v[212:215]\n"
"ds_read_b128 v[64:67], v6 offset:2048\n"
"ds_read_b128 v[224:227], v6 offset:3072\n"
"v_mfma_f32_16x16x32_bf16 v[228:231], a[124:127], v[208:211], v[228:231]\n"
"v_add_f32_e32 v27, v27, v179\n"
"v_add_f32_e32 v76, v76, v204\n"
"v_mfma_f32_16x16x32_bf16 v[228:231], a[120:123], v[200:203], v[228:231]\n"
"v_add_f32_e32 v77, v77, v205\n"
"v_add_f32_e32 v78, v78, v206\n"
"v_mfma_f32_16x16x32_bf16 v[228:231], a[116:119], v[196:199], v[228:231]\n"
"v_add_f32_e32 v79, v79, v207\n"
"v_add_f32_e32 v92, v92, v216\n"
"v_mfma_f32_16x16x32_bf16 v[228:231], a[112:115], v[60:63], v[228:231]\n"
"buffer_load_dwordx4 v[176:179], v34, s[4:7], s98 offen offset:1024 sc1\n"
"v_mfma_f32_16x16x32_bf16 v[228:231], a[108:111], v[188:191], v[228:231]\n"
"ds_read_b64_tr_b16 v[204:205], v38\n"
"v_add_f32_e32 v93, v93, v217\n"
"v_add_f32_e32 v94, v94, v218\n"
"v_mfma_f32_16x16x32_bf16 v[228:231], a[104:107], v[184:187], v[228:231]\n"
"ds_read_b64_tr_b16 v[206:207], v38 offset:2048\n"
"v_add_f32_e32 v95, v95, v219\n"
"v_mul_f32_e32 v76, v76, v255\n"
"v_mfma_f32_16x16x32_bf16 v[228:231], a[100:103], v[56:59], v[228:231]\n"
"s_mov_b32 s94, 0x300030\n"
"v_mul_f32_e32 v77, v77, v255\n"
"v_mul_f32_e32 v78, v78, v255\n"
"s_mov_b32 s95, 0x300030\n"
"v_mfma_f32_16x16x32_bf16 v[228:231], a[96:99], v[44:47], v[228:231]\n"
"v_mul_f32_e32 v79, v79, v255\n"
"v_mul_f32_e32 v92, v92, v255\n"
"v_mov_b32_e32 v219, 0x3f803f80\n"
"v_mfma_f32_16x16x32_bf16 v[156:159], v[104:107], v[44:47], v[156:159]\n"
"buffer_load_dwordx4 v[232:235], v35, s[4:7], s98 offen sc1\n"
"v_cndmask_b32_e64 v216, 0, v219, s[94:95]\n"
"v_mfma_f32_16x16x32_bf16 v[156:159], v[108:111], v[56:59], v[156:159]\n"
"v_mul_f32_e32 v93, v93, v255\n"
"v_mul_f32_e32 v94, v94, v255\n"
"v_cndmask_b32_e64 v217, 0, v219, s[94:95]\n"
"v_mfma_f32_16x16x32_bf16 v[156:159], v[112:115], v[184:187], v[156:159]\n"
"v_mul_f32_e32 v95, v95, v255\n"
"v_cvt_pk_bf16_f32 v236, v76, v77\n"
"v_cndmask_b32_e64 v218, 0, v219, s[94:95]\n"
"v_mfma_f32_16x16x32_bf16 v[156:159], v[120:123], v[188:191], v[156:159]\n"
"v_cvt_pk_bf16_f32 v237, v78, v79\n"
"v_cvt_pk_bf16_f32 v238, v92, v93\n"
"v_cndmask_b32_e64 v219, 0, v219, s[94:95]\n"
"v_mfma_f32_16x16x32_bf16 v[156:159], v[132:135], v[60:63], v[156:159]\n"
"s_waitcnt lgkmcnt(2)\n"
"v_cvt_pk_bf16_f32 v239, v94, v95\n"
"v_add_f32_e32 v212, v212, v64\n"
"s_waitcnt lgkmcnt(0)\n"
"v_mfma_f32_16x16x32_bf16 v[144:147], v[204:207], v[216:219], v[144:147]\n"
"buffer_store_dwordx4 v[204:207], v0, s[4:7], s22 offen sc1\n"
"v_mfma_f32_16x16x32_bf16 v[156:159], v[136:139], v[196:199], v[156:159]\n"
"buffer_load_dwordx4 v[76:79], v35, s[4:7], s98 offen offset:1024 sc1\n"
"ds_write_b128 v7, v[236:239] offset:24576\n"
"v_mfma_f32_16x16x32_bf16 v[156:159], v[140:143], v[200:203], v[156:159]\n"
"ds_read_b128 v[92:95], v6 offset:4096\n"
"ds_read_b128 v[204:207], v6 offset:5120\n"
"v_mfma_f32_16x16x32_bf16 v[156:159], v[100:103], v[208:211], v[156:159]\n"
"v_add_f32_e32 v213, v213, v65\n"
"v_add_f32_e32 v214, v214, v66\n"
"v_mfma_f32_16x16x32_bf16 v[24:27], v[116:119], v[208:211], v[24:27]\n"
"s_waitcnt vmcnt(10)\n"
"v_cmp_ne_u64_e32 vcc, v[250:251], v[18:19]\n"
"v_mfma_f32_16x16x32_bf16 v[24:27], v[168:171], v[200:203], v[24:27]\n"
"s_cbranch_vccnz .Lmk_f6s\n"
".Lmk_f6r:\n"
"v_add_f32_e32 v215, v215, v67\n"
"v_mfma_f32_16x16x32_bf16 v[24:27], v[164:167], v[196:199], v[24:27]\n"
"v_add_f32_e32 v228, v228, v224\n"
"v_add_f32_e32 v229, v229, v225\n"
"v_mfma_f32_16x16x32_bf16 v[24:27], v[160:163], v[60:63], v[24:27]\n"
"v_add_f32_e32 v230, v230, v226\n"
"v_add_f32_e32 v231, v231, v227\n"
"v_mfma_f32_16x16x32_bf16 v[24:27], v[128:131], v[188:191], v[24:27]\n"
"v_mul_f32_e32 v212, v212, v255\n"
"v_mul_f32_e32 v213, v213, v255\n"
"v_mfma_f32_16x16x32_bf16 v[24:27], v[152:155], v[184:187], v[24:27]\n"
"v_mul_f32_e32 v214, v214, v255\n"
"v_mul_f32_e32 v215, v215, v255\n"
"v_mfma_f32_16x16x32_bf16 v[24:27], v[148:151], v[56:59], v[24:27]\n"
"v_mul_f32_e32 v228, v228, v255\n"
"v_mul_f32_e32 v229, v229, v255\n"
"v_mfma_f32_16x16x32_bf16 v[24:27], v[124:127], v[44:47], v[24:27]\n"
"v_mul_f32_e32 v230, v230, v255\n"
"v_mul_f32_e32 v231, v231, v255\n"
"v_cvt_pk_bf16_f32 v40, v212, v213\n"
"v_cvt_pk_bf16_f32 v41, v214, v215\n"
"v_cvt_pk_bf16_f32 v42, v228, v229\n"
"v_cvt_pk_bf16_f32 v43, v230, v231\n"
"ds_write_b128 v7, v[40:43] offset:57344\n"
".Lmk_z1:\n"
"; @A G1 1.0\n"
"v_mfma_f32_16x16x32_bf16 v[44:47], a[0:3], v[72:75], 0\n"
"v_mfma_f32_16x16x32_bf16 v[44:47], a[4:7], v[84:87], v[44:47]\n"
"s_add_u32 s98, s62, 0x4800\n"
"v_mfma_f32_16x16x32_bf16 v[44:47], a[8:11], v[192:195], v[44:47]\n"
"s_add_u32 s97, s54, 0x48000\n"
"buffer_load_dwordx4 v[56:59], v32, s[4:7], s97 offen sc1\n"
"v_mfma_f32_16x16x32_bf16 v[44:47], a[12:15], v[80:83], v[44:47]\n"
"s_waitcnt vmcnt(10)\n"
"v_mfma_f32_16x16x32_bf16 v[44:47], a[16:19], v[88:91], v[44:47]\n"
"buffer_load_dword v255, v10, s[4:7], s98 offen sc1\n"
"v_mfma_f32_16x16x32_bf16 v[44:47], a[20:23], v[96:99], v[44:47]\n"
"s_waitcnt lgkmcnt(1)\n"
"v_add_f32_e32 v156, v156, v92\n"
"v_add_f32_e32 v157, v157, v93\n"
"v_mfma_f32_16x16x32_bf16 v[44:47], a[24:27], v[172:175], v[44:47]\n"
"v_add_f32_e32 v158, v158, v94\n"
"v_add_f32_e32 v159, v159, v95\n"
"v_mfma_f32_16x16x32_bf16 v[44:47], a[28:31], v[48:51], v[44:47]\n"
"buffer_load_dwordx4 v[60:63], v32, s[4:7], s97 offen offset:1024 sc1\n"
"v_mfma_f32_16x16x32_bf16 v[64:67], a[60:63], v[48:51], 0\n"
"s_add_u32 s96, s70, 0\n"
"buffer_load_dwordx4 a[128:131], v0, s[4:7], s96 offen sc1\n"
"v_mfma_f32_16x16x32_bf16 v[64:67], a[56:59], v[172:175], v[64:67]\n"
"buffer_load_dword v253, v10, s[4:7], s98 offen offset:256 sc1\n"
"v_mfma_f32_16x16x32_bf16 v[64:67], a[52:55], v[96:99], v[64:67]\n"
"v_add_f32_e32 v24, v24, v204\n"
"v_add_f32_e32 v25, v25, v205\n"
"v_mfma_f32_16x16x32_bf16 v[64:67], a[48:51], v[88:91], v[64:67]\n"
"v_add_f32_e32 v26, v26, v206\n"
"v_add_f32_e32 v27, v27, v207\n"
"v_mfma_f32_16x16x32_bf16 v[64:67], a[44:47], v[80:83], v[64:67]\n"
"buffer_load_dwordx4 v[92:95], v33, s[4:7], s97 offen sc1\n"
"v_mfma_f32_16x16x32_bf16 v[64:67], a[40:43], v[192:195], v[64:67]\n"
"ds_write_b128 v3, v[44:47]\n"
"v_mfma_f32_16x16x32_bf16 v[64:67], a[36:39], v[84:87], v[64:67]\n"
"buffer_load_dword v252, v10, s[4:7], s98 offen offset:512 sc1\n"
"v_mfma_f32_16x16x32_bf16 v[64:67], a[32:35], v[72:75], v[64:67]\n"
"v_mul_f32_e32 v156, v156, v254\n"
"v_mul_f32_e32 v157, v157, v254\n"
"v_mfma_f32_16x16x32_bf16 v[44:47], a[64:67], v[72:75], 0\n"
"v_mul_f32_e32 v158, v158, v254\n"
"v_mul_f32_e32 v159, v159, v254\n"
"v_mfma_f32_16x16x32_bf16 v[44:47], a[68:71], v[84:87], v[44:47]\n"
"buffer_load_dwordx4 v[184:187], v33, s[4:7], s97 offen offset:1024 sc1\n"
"v_mfma_f32_16x16x32_bf16 v[44:47], a[72:75], v[192:195], v[44:47]\n"
"buffer_load_dword v251, v10, s[4:7], s98 offen offset:768 sc1\n"
"v_mfma_f32_16x16x32_bf16 v[44:47], a[76:79], v[80:83], v[44:47]\n"
"v_mul_f32_e32 v24, v24, v254\n"
"v_mul_f32_e32 v25, v25, v254\n"
"v_mfma_f32_16x16x32_bf16 v[44:47], a[80:83], v[88:91], v[44:47]\n"
"v_mul_f32_e32 v26, v26, v254\n"
"v_mul_f32_e32 v27, v27, v254\n"
"v_mfma_f32_16x16x32_bf16 v[44:47], a[84:87], v[96:99], v[44:47]\n"
"ds_write_b128 v3, v[64:67] offset:1024\n"
"v_mfma_f32_16x16x32_bf16 v[44:47], a[88:91], v[172:175], v[44:47]\n"
"buffer_load_dwordx4 v[64:67], v34, s[4:7], s97 offen sc1\n"
"v_mfma_f32_16x16x32_bf16 v[44:47], a[92:95], v[48:51], v[44:47]\n"
"buffer_load_dword v254, v10, s[4:7], s98 offen offset:1024 sc1\n"
"v_mfma_f32_16x16x32_bf16 v[188:191], a[124:127], v[48:51], 0\n"
"v_cvt_pk_bf16_f32 v196, v156, v157\n"
"v_cvt_pk_bf16_f32 v197, v158, v159\n"
"v_mfma_f32_16x16x32_bf16 v[188:191], a[120:123], v[172:175], v[188:191]\n"
"v_cvt_pk_bf16_f32 v198, v24, v25\n"
"v_cvt_pk_bf16_f32 v199, v26, v27\n"
"v_mfma_f32_16x16x32_bf16 v[188:191], a[116:119], v[96:99], v[188:191]\n"
"buffer_load_dword v250, v10, s[4:7], s98 offen offset:1280 sc1\n"
"v_mfma_f32_16x16x32_bf16 v[188:191], a[112:115], v[88:91], v[188:191]\n"
"buffer_load_dwordx4 v[24:27], v34, s[4:7], s97 offen offset:1024 sc1\n"
"v_mfma_f32_16x16x32_bf16 v[188:191], a[108:111], v[80:83], v[188:191]\n"
"buffer_load_dwordx4 a[132:135], v0, s[4:7], s96 offen offset:1024 sc1\n"
"v_mfma_f32_16x16x32_bf16 v[188:191], a[104:107], v[192:195], v[188:191]\n"
"ds_write_b128 v3, v[44:47] offset:2048\n"
"v_mfma_f32_16x16x32_bf16 v[188:191], a[100:103], v[84:87], v[188:191]\n"
"buffer_load_dword v249, v10, s[4:7], s98 offen offset:1536 sc1\n"
"v_mfma_f32_16x16x32_bf16 v[188:191], a[96:99], v[72:75], v[188:191]\n"
"ds_write_b128 v2, v[196:199] offset:49152\n"
"v_mfma_f32_16x16x32_bf16 v[44:47], v[104:107], v[72:75], 0\n"
"buffer_load_dwordx4 v[156:159], v35, s[4:7], s97 offen sc1\n"
"v_mfma_f32_16x16x32_bf16 v[44:47], v[108:111], v[84:87], v[44:47]\n"
"buffer_load_dword v248, v10, s[4:7], s98 offen offset:1792 sc1\n"
"v_mfma_f32_16x16x32_bf16 v[44:47], v[112:115], v[192:195], v[44:47]\n"
"v_mfma_f32_16x16x32_bf16 v[44:47], v[120:123], v[80:83], v[44:47]\n"
"v_mfma_f32_16x16x32_bf16 v[44:47], v[132:135], v[88:91], v[44:47]\n"
"v_mfma_f32_16x16x32_bf16 v[44:47], v[136:139], v[96:99], v[44:47]\n"
"buffer_load_dwordx4 v[196:199], v35, s[4:7], s97 offen offset:1024 sc1\n"
"v_mfma_f32_16x16x32_bf16 v[44:47], v[140:143], v[172:175], v[44:47]\n"
"ds_write_b128 v3, v[188:191] offset:3072\n"
"v_mfma_f32_16x16x32_bf16 v[44:47], v[100:103], v[48:51], v[44:47]\n"
"v_mfma_f32_16x16x32_bf16 v[188:191], v[116:119], v[48:51], 0\n"
"v_mfma_f32_16x16x32_bf16 v[188:191], v[168:171], v[172:175], v[188:191]\n"
"v_mfma_f32_16x16x32_bf16 v[188:191], v[164:167], v[96:99], v[188:191]\n"
"v_mfma_f32_16x16x32_bf16 v[188:191], v[160:163], v[88:91], v[188:191]\n"
"v_mfma_f32_16x16x32_bf16 v[188:191], v[128:131], v[80:83], v[188:191]\n"
"v_mfma_f32_16x16x32_bf16 v[188:191], v[152:155], v[192:195], v[188:191]\n"
"s_nop 1\n"
"ds_write_b128 v3, v[44:47] offset:4096\n"
"v_mfma_f32_16x16x32_bf16 v[188:191], v[148:151], v[84:87], v[188:191]\n"
"v_mfma_f32_16x16x32_bf16 v[188:191], v[124:127], v[72:75], v[188:191]\n"
"s_waitcnt vmcnt(23)\n"
"; @A G1 1.1\n"
"v_mfma_f32_16x16x32_bf16 v[44:47], a[0:3], v[180:183], 0\n"
"buffer_load_dwordx4 a[136:139], v0, s[4:7], s96 offen offset:2048 sc1\n"
"v_mfma_f32_16x16x32_bf16 v[44:47], a[4:7], v[68:71], v[44:47]\n"
"v_mov_b32_e32 v48, -1\n"
"v_mov_b32_e32 v49, -1\n"
"v_mfma_f32_16x16x32_bf16 v[44:47], a[8:11], v[220:223], v[44:47]\n"
"s_add_u32 s98, s55, 0x48000\n"
"buffer_load_dwordx4 v[72:75], v32, s[4:7], s98 offen sc1\n"
"v_mfma_f32_16x16x32_bf16 v[44:47], a[12:15], v[28:31], v[44:47]\n"
"s_waitcnt vmcnt(20)\n"
"v_mov_b32_e32 v50, -1\n"
"v_mov_b32_e32 v51, -1\n"
"v_mfma_f32_16x16x32_bf16 v[44:47], a[16:19], v[52:55], v[44:47]\n"
"ds_read_b64_tr_b16 v[80:81], v38 offset:4096\n"
"ds_read_b64_tr_b16 v[82:83], v38 offset:6144\n"
"s_mov_b32 s94, 0x300030\n"
"v_mfma_f32_16x16x32_bf16 v[44:47], a[20:23], v[176:179], v[44:47]\n"
"ds_write_b128 v3, v[188:191] offset:5120\n"
"v_mfma_f32_16x16x32_bf16 v[44:47], a[24:27], v[232:235], v[44:47]\n"
"s_mov_b32 s95, 0x300030\n"
"v_mov_b32_e32 v87, 0x3f803f80\n"
"v_cndmask_b32_e64 v84, 0, v87, s[94:95]\n"
"v_cndmask_b32_e64 v85, 0, v87, s[94:95]\n"
"v_mfma_f32_16x16x32_bf16 v[44:47], a[28:31], v[76:79], v[44:47]\n"
"buffer_load_dwordx4 v[88:91], v32, s[4:7], s98 offen offset:1024 sc1\n"
"v_cndmask_b32_e64 v86, 0, v87, s[94:95]\n"
"v_mfma_f32_16x16x32_bf16 v[96:99], a[60:63], v[76:79], 0\n"
"v_cndmask_b32_e64 v87, 0, v87, s[94:95]\n"
"v_mfma_f32_16x16x32_bf16 v[96:99], a[56:59], v[232:235], v[96:99]\n"
"s_waitcnt lgkmcnt(1)\n"
"v_mfma_f32_16x16x32_bf16 v[144:147], v[80:83], v[84:87], v[144:147]\n"
"buffer_store_dwordx4 v[80:83], v0, s[4:7], s22 offen offset:1024 sc1\n"
"v_mfma_f32_16x16x32_bf16 v[96:99], a[52:55], v[176:179], v[96:99]\n"
"v_mfma_f32_16x16x32_bf16 v[96:99], a[48:51], v[52:55], v[96:99]\n"
"v_mfma_f32_16x16x32_bf16 v[96:99], a[44:47], v[28:31], v[96:99]\n"
"buffer_load_dwordx4 v[80:83], v33, s[4:7], s98 offen sc1\n"
"v_mfma_f32_16x16x32_bf16 v[96:99], a[40:43], v[220:223], v[96:99]\n"
"ds_write_b128 v3, v[44:47] offset:6144\n"
"v_mfma_f32_16x16x32_bf16 v[96:99], a[36:39], v[68:71], v[96:99]\n"
"s_add_u32 s97, s67, 0\n"
"buffer_load_dwordx2 v[246:247], v12, s[4:7], s97 offen sc1\n"
"s_add_u32 s95, s64, 0x2000\n"
"v_mfma_f32_16x16x32_bf16 v[96:99], a[32:35], v[180:183], v[96:99]\n"
"buffer_store_dwordx4 v[236:239], v7, s[4:7], s95 offen sc1\n"
"v_mfma_f32_16x16x32_bf16 v[44:47], a[64:67], v[180:183], 0\n"
"v_mfma_f32_16x16x32_bf16 v[44:47], a[68:71], v[68:71], v[44:47]\n"
"buffer_load_dwordx4 v[84:87], v33, s[4:7], s98 offen offset:1024 sc1\n"
"v_mfma_f32_16x16x32_bf16 v[44:47], a[72:75], v[220:223], v[44:47]\n"
"v_mfma_f32_16x16x32_bf16 v[44:47], a[76:79], v[28:31], v[44:47]\n"
"v_mfma_f32_16x16x32_bf16 v[44:47], a[80:83], v[52:55], v[44:47]\n"
"buffer_load_dwordx4 a[140:143], v0, s[4:7], s96 offen offset:3072 sc1\n"
"v_mfma_f32_16x16x32_bf16 v[44:47], a[84:87], v[176:179], v[44:47]\n"
"ds_write_b128 v3, v[96:99] offset:7168\n"
"v_mfma_f32_16x16x32_bf16 v[44:47], a[88:91], v[232:235], v[44:47]\n"
"buffer_load_dwordx4 v[96:99], v34, s[4:7], s98 offen sc1\n"
"v_mfma_f32_16x16x32_bf16 v[44:47], a[92:95], v[76:79], v[44:47]\n"
"v_mfma_f32_16x16x32_bf16 v[172:175], a[124:127], v[76:79], 0\n"
"v_mfma_f32_16x16x32_bf16 v[172:175], a[120:123], v[232:235], v[172:175]\n"
"v_mfma_f32_16x16x32_bf16 v[172:175], a[116:119], v[176:179], v[172:175]\n"
"v_mfma_f32_16x16x32_bf16 v[172:175], a[112:115], v[52:55], v[172:175]\n"
"buffer_load_dwordx4 v[188:191], v34, s[4:7], s98 offen offset:1024 sc1\n"
"v_mfma_f32_16x16x32_bf16 v[172:175], a[108:111], v[28:31], v[172:175]\n"
"v_mfma_f32_16x16x32_bf16 v[172:175], a[104:107], v[220:223], v[172:175]\n"
"s_nop 0\n"
"ds_write_b128 v3, v[44:47] offset:8192\n"
"v_mfma_f32_16x16x32_bf16 v[172:175], a[100:103], v[68:71], v[172:175]\n"
"v_mfma_f32_16x16x32_bf16 v[172:175], a[96:99], v[180:183], v[172:175]\n"
"v_mfma_f32_16x16x32_bf16 v[44:47], v[104:107], v[180:183], 0\n"
"buffer_load_dwordx4 v[192:195], v35, s[4:7], s98 offen sc1\n"
"v_mfma_f32_16x16x32_bf16 v[44:47], v[108:111], v[68:71], v[44:47]\n"
"buffer_store_dwordx4 v[144:147], v39, s[4:7], s23 offen sc1\n"
"v_mfma_f32_16x16x32_bf16 v[44:47], v[112:115], v[220:223], v[44:47]\n"
"v_mfma_f32_16x16x32_bf16 v[44:47], v[120:123], v[28:31], v[44:47]\n"
"v_mfma_f32_16x16x32_bf16 v[44:47], v[132:135], v[52:55], v[44:47]\n"
"s_waitcnt vmcnt(14)\n"
"v_add_f32_e32 v255, v255, v254\n"
"v_add_f32_e32 v253, v253, v250\n"
"v_mfma_f32_16x16x32_bf16 v[44:47], v[136:139], v[176:179], v[44:47]\n"
"buffer_load_dwordx4 v[36:39], v35, s[4:7], s98 offen offset:1024 sc1\n"
"s_add_u32 s98, s64, 0x6000\n"
"v_mfma_f32_16x16x32_bf16 v[44:47], v[140:143], v[232:235], v[44:47]\n"
"ds_write_b128 v3, v[172:175] offset:9216\n"
"v_mfma_f32_16x16x32_bf16 v[44:47], v[100:103], v[76:79], v[44:47]\n"
"buffer_store_dwordx4 v[40:43], v7, s[4:7], s98 offen sc1\n"
"s_nop 1\n"
"v_mfma_f32_16x16x32_bf16 v[40:43], v[116:119], v[76:79], 0\n"
"buffer_load_dwordx4 a[144:147], v1, s[4:7], s96 offen sc1\n"
"v_mfma_f32_16x16x32_bf16 v[40:43], v[168:171], v[232:235], v[40:43]\n"
"v_add_f32_e32 v252, v252, v249\n"
"v_add_f32_e32 v251, v251, v248\n"
"v_mfma_f32_16x16x32_bf16 v[40:43], v[164:167], v[176:179], v[40:43]\n"
"v_add_f32_e32 v255, v255, v252\n"
"v_add_f32_e32 v253, v253, v251\n"
"v_mfma_f32_16x16x32_bf16 v[40:43], v[160:163], v[52:55], v[40:43]\n"
"v_add_f32_e32 v255, v255, v253\n"
"v_mov_b32_e32 v253, v255\n"
"v_mfma_f32_16x16x32_bf16 v[40:43], v[128:131], v[28:31], v[40:43]\n"
"s_nop 0\n"
"v_permlane32_swap_b32_e32 v255, v253\n"
"v_add_f32_e32 v255, v255, v253\n"
"v_mfma_f32_16x16x32_bf16 v[40:43], v[152:155], v[220:223], v[40:43]\n"
"ds_write_b128 v3, v[44:47] offset:10240\n"
"v_mfma_f32_16x16x32_bf16 v[40:43], v[148:151], v[68:71], v[40:43]\n"
"v_mul_f32_e32 v255, 0x3a800000, v255\n"
"v_mov_b32_e32 v253, v255\n"
"v_mfma_f32_16x16x32_bf16 v[40:43], v[124:127], v[180:183], v[40:43]\n"
"s_nop 0\n"
"v_permlane16_swap_b32_e32 v255, v253\n"
"v_cvt_pk_bf16_f32 v254, -v255, 0\n"
"; @A G1 1.2\n"
"v_mfma_f32_16x16x32_bf16 v[28:31], a[0:3], v[56:59], 0\n"
"v_fma_f32 v253, -v255, v255, v253\n"
"v_lshlrev_b32_e32 v254, 16, v254\n"
"v_mfma_f32_16x16x32_bf16 v[28:31], a[4:7], v[60:63], v[28:31]\n"
"s_waitcnt vmcnt(10)\n"
"v_cmp_ne_u64_e32 vcc, v[246:247], v[18:19]\n"
"v_mfma_f32_16x16x32_bf16 v[28:31], a[8:11], v[92:95], v[28:31]\n"
"s_cbranch_vccnz .Lmk_f3s\n"
".Lmk_f3r:\n"
"v_max_f32_e32 v253, 0, v253\n"
"v_mfma_f32_16x16x32_bf16 v[28:31], a[12:15], v[184:187], v[28:31]\n"
"v_cvt_pk_bf16_f32 v253, v253, 0\n"
"v_lshlrev_b32_e32 v253, 16, v253\n"
"v_mfma_f32_16x16x32_bf16 v[28:31], a[16:19], v[64:67], v[28:31]\n"
"v_add_f32_e32 v253, 0x3727c5ac, v253\n"
"v_cvt_pk_bf16_f32 v253, v253, 0\n"
"v_mfma_f32_16x16x32_bf16 v[28:31], a[20:23], v[24:27], v[28:31]\n"
"ds_write_b128 v3, v[40:43] offset:11264\n"
"v_mfma_f32_16x16x32_bf16 v[28:31], a[24:27], v[156:159], v[28:31]\n"
"v_lshlrev_b32_e32 v253, 16, v253\n"
"v_mfma_f32_16x16x32_bf16 v[28:31], a[28:31], v[196:199], v[28:31]\n"
"v_sqrt_f32_e32 v253, v253\n"
"v_mfma_f32_16x16x32_bf16 v[40:43], a[60:63], v[196:199], 0\n"
"v_cvt_pk_bf16_f32 v253, v253, 0\n"
"v_lshlrev_b32_e32 v253, 16, v253\n"
"v_mfma_f32_16x16x32_bf16 v[40:43], a[56:59], v[156:159], v[40:43]\n"
"v_rcp_f32_e32 v255, v253\n"
"v_mfma_f32_16x16x32_bf16 v[40:43], a[52:55], v[24:27], v[40:43]\n"
"s_add_u32 s98, s52, 0x8000\n"
"buffer_load_dwordx4 v[44:47], v32, s[4:7], s98 offen sc1\n"
"v_mfma_f32_16x16x32_bf16 v[40:43], a[48:51], v[64:67], v[40:43]\n"
"s_waitcnt lgkmcnt(0)\n"
"s_barrier\n"
"v_mfma_f32_16x16x32_bf16 v[40:43], a[44:47], v[184:187], v[40:43]\n"
"buffer_load_dwordx4 a[148:151], v1, s[4:7], s96 offen offset:1024 sc1\n"
"v_mfma_f32_16x16x32_bf16 v[40:43], a[40:43], v[92:95], v[40:43]\n"
"ds_write_b128 v3, v[28:31] offset:12288\n"
"v_mfma_f32_16x16x32_bf16 v[40:43], a[36:39], v[60:63], v[40:43]\n"
"ds_read_b128 v[28:31], v4\n"
"v_mul_f32_e32 v253, 0x3e8293ee, v255\n"
"v_mfma_f32_16x16x32_bf16 v[40:43], a[32:35], v[56:59], v[40:43]\n"
"buffer_load_dwordx4 v[52:55], v32, s[4:7], s98 offen offset:1024 sc1\n"
"v_mfma_f32_16x16x32_bf16 v[68:71], a[64:67], v[56:59], 0\n"
"ds_read_b128 v[76:79], v9 offset:128\n"
"ds_read_b128 v[144:147], v5\n"
"v_mfma_f32_16x16x32_bf16 v[68:71], a[68:71], v[60:63], v[68:71]\n"
"v_mfma_f32_16x16x32_bf16 v[68:71], a[72:75], v[92:95], v[68:71]\n"
"s_add_u32 s97, s60, 0x48000\n"
"buffer_store_dwordx4 v[48:51], v0, s[4:7], s97 offen sc1\n"
"v_mfma_f32_16x16x32_bf16 v[68:71], a[76:79], v[184:187], v[68:71]\n"
"ds_read_b128 v[172:175], v4 offset:1024\n"
"ds_read_b128 v[176:179], v9 offset:192\n"
"v_mfma_f32_16x16x32_bf16 v[68:71], a[80:83], v[64:67], v[68:71]\n"
"buffer_load_dwordx4 v[180:183], v33, s[4:7], s98 offen sc1\n"
"v_mfma_f32_16x16x32_bf16 v[68:71], a[84:87], v[24:27], v[68:71]\n"
"ds_write_b128 v3, v[40:43] offset:13312\n"
"v_mfma_f32_16x16x32_bf16 v[68:71], a[88:91], v[156:159], v[68:71]\n"
"ds_read_b128 v[40:43], v5 offset:1024\n"
"s_waitcnt lgkmcnt(4)\n"
"v_mfma_f32_16x16x32_bf16 v[68:71], a[92:95], v[196:199], v[68:71]\n"
"v_fmac_f32_e32 v28, v254, v76\n"
"v_fmac_f32_e32 v29, v254, v77\n"
"v_mfma_f32_16x16x32_bf16 v[200:203], a[124:127], v[196:199], 0\n"
"v_fmac_f32_e32 v30, v254, v78\n"
"v_fmac_f32_e32 v31, v254, v79\n"
"v_mfma_f32_16x16x32_bf16 v[200:203], a[120:123], v[156:159], v[200:203]\n"
"buffer_load_dwordx4 v[76:79], v33, s[4:7], s98 offen offset:1024 sc1\n"
"v_mfma_f32_16x16x32_bf16 v[200:203], a[116:119], v[24:27], v[200:203]\n"
"ds_read_b128 v[204:207], v4 offset:2048\n"
"ds_read_b128 v[208:211], v9 offset:256\n"
"v_mfma_f32_16x16x32_bf16 v[200:203], a[112:115], v[64:67], v[200:203]\n"
"ds_read_b128 v[212:215], v5 offset:2048\n"
"v_add_f32_e32 v28, v28, v144\n"
"v_mfma_f32_16x16x32_bf16 v[200:203], a[108:111], v[184:187], v[200:203]\n"
"v_add_f32_e32 v29, v29, v145\n"
"v_add_f32_e32 v30, v30, v146\n"
"v_mfma_f32_16x16x32_bf16 v[200:203], a[104:107], v[92:95], v[200:203]\n"
"ds_write_b128 v3, v[68:71] offset:14336\n"
"v_mfma_f32_16x16x32_bf16 v[200:203], a[100:103], v[60:63], v[200:203]\n"
"buffer_load_dwordx4 v[68:71], v34, s[4:7], s98 offen sc1\n"
"v_mfma_f32_16x16x32_bf16 v[200:203], a[96:99], v[56:59], v[200:203]\n"
"s_waitcnt lgkmcnt(4)\n"
"v_add_f32_e32 v31, v31, v147\n"
"v_fmac_f32_e32 v172, v254, v176\n"
"v_mfma_f32_16x16x32_bf16 v[144:147], v[104:107], v[56:59], 0\n"
"buffer_load_dwordx4 a[152:155], v1, s[4:7], s96 offen offset:2048 sc1\n"
"v_mfma_f32_16x16x32_bf16 v[144:147], v[108:111], v[60:63], v[144:147]\n"
"ds_read_b128 v[216:219], v4 offset:3072\n"
"ds_read_b128 v[220:223], v9 offset:320\n"
"v_mfma_f32_16x16x32_bf16 v[144:147], v[112:115], v[92:95], v[144:147]\n"
"ds_read_b128 v[224:227], v5 offset:3072\n"
"v_fmac_f32_e32 v173, v254, v177\n"
"v_mfma_f32_16x16x32_bf16 v[144:147], v[120:123], v[184:187], v[144:147]\n"
"buffer_load_dwordx4 v[228:231], v34, s[4:7], s98 offen offset:1024 sc1\n"
"v_mfma_f32_16x16x32_bf16 v[144:147], v[132:135], v[64:67], v[144:147]\n"
"v_fmac_f32_e32 v174, v254, v178\n"
"v_fmac_f32_e32 v175, v254, v179\n"
"v_mfma_f32_16x16x32_bf16 v[144:147], v[136:139], v[24:27], v[144:147]\n"
"ds_write_b128 v3, v[200:203] offset:15360\n"
"v_mfma_f32_16x16x32_bf16 v[144:147], v[140:143], v[156:159], v[144:147]\n"
"v_add_f32_e32 v172, v172, v40\n"
"v_add_f32_e32 v173, v173, v41\n"
"v_mfma_f32_16x16x32_bf16 v[144:147], v[100:103], v[196:199], v[144:147]\n"
"s_waitcnt lgkmcnt(5)\n"
"v_add_f32_e32 v174, v174, v42\n"
"v_add_f32_e32 v175, v175, v43\n"
"v_mfma_f32_16x16x32_bf16 v[40:43], v[116:119], v[196:199], 0\n"
"buffer_load_dwordx4 v[176:179], v35, s[4:7], s98 offen sc1\n"
"v_mfma_f32_16x16x32_bf16 v[40:43], v[168:171], v[156:159], v[40:43]\n"
"ds_read_b128 v[156:159], v4 offset:4096\n"
"ds_read_b128 v[196:199], v9\n"
"v_mfma_f32_16x16x32_bf16 v[40:43], v[164:167], v[24:27], v[40:43]\n"
"ds_read_b128 v[24:27], v5 offset:4096\n"
"v_fmac_f32_e32 v204, v254, v208\n"
"v_mfma_f32_16x16x32_bf16 v[40:43], v[160:163], v[64:67], v[40:43]\n"
"v_fmac_f32_e32 v205, v254, v209\n"
"v_fmac_f32_e32 v206, v254, v210\n"
"v_mfma_f32_16x16x32_bf16 v[40:43], v[128:131], v[184:187], v[40:43]\n"
"v_fmac_f32_e32 v207, v254, v211\n"
"v_add_f32_e32 v204, v204, v212\n"
"v_mfma_f32_16x16x32_bf16 v[40:43], v[152:155], v[92:95], v[40:43]\n"
"buffer_load_dwordx4 v[64:67], v35, s[4:7], s98 offen offset:1024 sc1\n"
"v_mfma_f32_16x16x32_bf16 v[40:43], v[148:151], v[60:63], v[40:43]\n"
"ds_write_b128 v3, v[144:147] offset:16384\n"
"v_mfma_f32_16x16x32_bf16 v[40:43], v[124:127], v[56:59], v[40:43]\n"
"s_waitcnt vmcnt(19)\n"
"ds_read_b128 v[56:59], v4 offset:5120\n"
"ds_read_b128 v[60:63], v9 offset:64\n"
"; @A G1 1.3\n"
"v_mfma_f32_16x16x32_bf16 v[28:31], a[0:3], v[72:75], v[28:31]\n"
"ds_read_b128 v[92:95], v5 offset:5120\n"
"v_add_f32_e32 v205, v205, v213\n"
"v_mfma_f32_16x16x32_bf16 v[28:31], a[4:7], v[88:91], v[28:31]\n"
"s_waitcnt lgkmcnt(8)\n"
"v_add_f32_e32 v206, v206, v214\n"
"v_add_f32_e32 v207, v207, v215\n"
"v_mfma_f32_16x16x32_bf16 v[28:31], a[8:11], v[80:83], v[28:31]\n"
"s_add_u32 s98, s53, 0x8000\n"
"buffer_load_dwordx4 v[144:147], v32, s[4:7], s98 offen sc1\n"
"v_mfma_f32_16x16x32_bf16 v[28:31], a[12:15], v[84:87], v[28:31]\n"
"s_waitcnt vmcnt(14)\n"
"s_add_u32 s97, s60, 0x40000\n"
"buffer_store_dwordx4 v[48:51], v0, s[4:7], s97 offen sc1\n"
"v_fmac_f32_e32 v216, v254, v220\n"
"v_fmac_f32_e32 v217, v254, v221\n"
"v_mfma_f32_16x16x32_bf16 v[28:31], a[16:19], v[96:99], v[28:31]\n"
"buffer_load_dwordx4 a[156:159], v1, s[4:7], s96 offen offset:3072 sc1\n"
"v_mfma_f32_16x16x32_bf16 v[28:31], a[20:23], v[188:191], v[28:31]\n"
"ds_write_b128 v3, v[40:43] offset:17408\n"
"v_mfma_f32_16x16x32_bf16 v[28:31], a[24:27], v[192:195], v[28:31]\n"
"v_fmac_f32_e32 v218, v254, v222\n"
"v_fmac_f32_e32 v219, v254, v223\n"
"v_mfma_f32_16x16x32_bf16 v[28:31], a[28:31], v[36:39], v[28:31]\n"
"buffer_load_dwordx4 v[40:43], v32, s[4:7], s98 offen offset:1024 sc1\n"
"v_mfma_f32_16x16x32_bf16 v[172:175], a[60:63], v[36:39], v[172:175]\n"
"v_add_f32_e32 v216, v216, v224\n"
"v_add_f32_e32 v217, v217, v225\n"
"v_mfma_f32_16x16x32_bf16 v[172:175], a[56:59], v[192:195], v[172:175]\n"
"s_waitcnt lgkmcnt(5)\n"
"v_add_f32_e32 v218, v218, v226\n"
"v_add_f32_e32 v219, v219, v227\n"
"v_mfma_f32_16x16x32_bf16 v[172:175], a[52:55], v[188:191], v[172:175]\n"
"s_waitcnt lgkmcnt(0)\n"
"s_barrier\n"
"v_mfma_f32_16x16x32_bf16 v[172:175], a[48:51], v[96:99], v[172:175]\n"
"ds_read_b128 v[184:187], v6\n"
"ds_read_b128 v[200:203], v6 offset:1024\n"
"v_mfma_f32_16x16x32_bf16 v[172:175], a[44:47], v[84:87], v[172:175]\n"
"buffer_load_dwordx4 v[208:211], v33, s[4:7], s98 offen sc1\n"
"v_mfma_f32_16x16x32_bf16 v[172:175], a[40:43], v[80:83], v[172:175]\n"
"v_fmac_f32_e32 v156, v254, v196\n"
"v_fmac_f32_e32 v157, v254, v197\n"
"v_mfma_f32_16x16x32_bf16 v[172:175], a[36:39], v[88:91], v[172:175]\n"
"v_fmac_f32_e32 v158, v254, v198\n"
"v_fmac_f32_e32 v159, v254, v199\n"
"v_mfma_f32_16x16x32_bf16 v[172:175], a[32:35], v[72:75], v[172:175]\n"
"v_add_f32_e32 v156, v156, v24\n"
"v_add_f32_e32 v157, v157, v25\n"
"v_mfma_f32_16x16x32_bf16 v[204:207], a[64:67], v[72:75], v[204:207]\n"
"v_add_f32_e32 v158, v158, v26\n"
"v_add_f32_e32 v159, v159, v27\n"
"v_mfma_f32_16x16x32_bf16 v[204:207], a[68:71], v[88:91], v[204:207]\n"
"buffer_load_dwordx4 v[24:27], v33, s[4:7], s98 offen offset:1024 sc1\n"
"v_mfma_f32_16x16x32_bf16 v[204:207], a[72:75], v[80:83], v[204:207]\n"
"v_fmac_f32_e32 v56, v254, v60\n"
"v_fmac_f32_e32 v57, v254, v61\n"
"v_mfma_f32_16x16x32_bf16 v[204:207], a[76:79], v[84:87], v[204:207]\n"
"v_fmac_f32_e32 v58, v254, v62\n"
"v_fmac_f32_e32 v59, v254, v63\n"
"v_mfma_f32_16x16x32_bf16 v[204:207], a[80:83], v[96:99], v[204:207]\n"
"s_waitcnt lgkmcnt(0)\n"
"v_add_f32_e32 v56, v56, v92\n"
"v_add_f32_e32 v57, v57, v93\n"
"v_mfma_f32_16x16x32_bf16 v[204:207], a[84:87], v[188:191], v[204:207]\n"
"v_add_f32_e32 v58, v58, v94\n"
"v_add_f32_e32 v59, v59, v95\n"
"v_mfma_f32_16x16x32_bf16 v[204:207], a[88:91], v[192:195], v[204:207]\n"
"buffer_load_dwordx4 v[60:63], v34, s[4:7], s98 offen sc1\n"
"v_mfma_f32_16x16x32_bf16 v[204:207], a[92:95], v[36:39], v[204:207]\n"
"s_add_u32 s97, s70, 0x2000\n"
"ds_read_b128 v[92:95], v6 offset:2048\n"
"ds_read_b128 v[196:199], v6 offset:3072\n"
"v_mfma_f32_16x16x32_bf16 v[216:219], a[124:127], v[36:39], v[216:219]\n"
"buffer_load_dwordx4 a[160:163], v0, s[4:7], s97 offen sc1\n"
"v_mfma_f32_16x16x32_bf16 v[216:219], a[120:123], v[192:195], v[216:219]\n"
"v_add_f32_e32 v28, v28, v184\n"
"v_add_f32_e32 v29, v29, v185\n"
"v_mfma_f32_16x16x32_bf16 v[216:219], a[116:119], v[188:191], v[216:219]\n"
"v_add_f32_e32 v30, v30, v186\n"
"v_add_f32_e32 v31, v31, v187\n"
"v_mfma_f32_16x16x32_bf16 v[216:219], a[112:115], v[96:99], v[216:219]\n"
"buffer_load_dwordx4 v[184:187], v34, s[4:7], s98 offen offset:1024 sc1\n"
"v_mfma_f32_16x16x32_bf16 v[216:219], a[108:111], v[84:87], v[216:219]\n"
"v_add_f32_e32 v172, v172, v200\n"
"v_add_f32_e32 v173, v173, v201\n"
"v_mfma_f32_16x16x32_bf16 v[216:219], a[104:107], v[80:83], v[216:219]\n"
"v_add_f32_e32 v174, v174, v202\n"
"v_add_f32_e32 v175, v175, v203\n"
"v_mfma_f32_16x16x32_bf16 v[216:219], a[100:103], v[88:91], v[216:219]\n"
"v_mul_f32_e32 v28, v28, v255\n"
"v_mul_f32_e32 v29, v29, v255\n"
"v_mfma_f32_16x16x32_bf16 v[216:219], a[96:99], v[72:75], v[216:219]\n"
"v_mul_f32_e32 v30, v30, v255\n"
"v_mul_f32_e32 v31, v31, v255\n"
"v_mfma_f32_16x16x32_bf16 v[156:159], v[104:107], v[72:75], v[156:159]\n"
"buffer_load_dwordx4 v[200:203], v35, s[4:7], s98 offen sc1\n"
"v_mfma_f32_16x16x32_bf16 v[156:159], v[108:111], v[88:91], v[156:159]\n"
"v_mul_f32_e32 v172, v172, v255\n"
"v_mul_f32_e32 v173, v173, v255\n"
"v_mfma_f32_16x16x32_bf16 v[156:159], v[112:115], v[80:83], v[156:159]\n"
"v_mul_f32_e32 v174, v174, v255\n"
"v_mul_f32_e32 v175, v175, v255\n"
"v_mfma_f32_16x16x32_bf16 v[156:159], v[120:123], v[84:87], v[156:159]\n"
"v_cvt_pk_bf16_f32 v212, v28, v29\n"
"v_cvt_pk_bf16_f32 v213, v30, v31\n"
"v_mfma_f32_16x16x32_bf16 v[156:159], v[132:135], v[96:99], v[156:159]\n"
"s_waitcnt lgkmcnt(0)\n"
"buffer_store_dwordx4 v[48:51], v0, s[4:7], s60 offen sc1\n"
"v_cvt_pk_bf16_f32 v214, v172, v173\n"
"v_cvt_pk_bf16_f32 v215, v174, v175\n"
"v_mfma_f32_16x16x32_bf16 v[156:159], v[136:139], v[188:191], v[156:159]\n"
"buffer_load_dwordx4 v[28:31], v35, s[4:7], s98 offen offset:1024 sc1\n"
"ds_write_b128 v8, v[212:215] offset:25600\n"
"v_mfma_f32_16x16x32_bf16 v[156:159], v[140:143], v[192:195], v[156:159]\n"
"ds_read_b128 v[48:51], v6 offset:4096\n"
"ds_read_b128 v[172:175], v6 offset:5120\n"
"v_mfma_f32_16x16x32_bf16 v[156:159], v[100:103], v[36:39], v[156:159]\n"
"v_add_f32_e32 v204, v204, v92\n"
"v_add_f32_e32 v205, v205, v93\n"
"v_mfma_f32_16x16x32_bf16 v[56:59], v[116:119], v[36:39], v[56:59]\n"
"v_add_f32_e32 v206, v206, v94\n"
"v_add_f32_e32 v207, v207, v95\n"
"v_mfma_f32_16x16x32_bf16 v[56:59], v[168:171], v[192:195], v[56:59]\n"
"v_add_f32_e32 v216, v216, v196\n"
"v_add_f32_e32 v217, v217, v197\n"
"v_mfma_f32_16x16x32_bf16 v[56:59], v[164:167], v[188:191], v[56:59]\n"
"v_add_f32_e32 v218, v218, v198\n"
"v_add_f32_e32 v219, v219, v199\n"
"v_mfma_f32_16x16x32_bf16 v[56:59], v[160:163], v[96:99], v[56:59]\n"
"v_mul_f32_e32 v204, v204, v255\n"
"v_mul_f32_e32 v205, v205, v255\n"
"v_mfma_f32_16x16x32_bf16 v[56:59], v[128:131], v[84:87], v[56:59]\n"
"v_mul_f32_e32 v206, v206, v255\n"
"v_mul_f32_e32 v207, v207, v255\n"
"v_mfma_f32_16x16x32_bf16 v[56:59], v[152:155], v[80:83], v[56:59]\n"
"v_mul_f32_e32 v216, v216, v255\n"
"v_mul_f32_e32 v217, v217, v255\n"
"v_mfma_f32_16x16x32_bf16 v[56:59], v[148:151], v[88:91], v[56:59]\n"
"v_mul_f32_e32 v218, v218, v255\n"
"v_mul_f32_e32 v219, v219, v255\n"
"v_mfma_f32_16x16x32_bf16 v[56:59], v[124:127], v[72:75], v[56:59]\n"
"v_cvt_pk_bf16_f32 v36, v204, v205\n"
"v_cvt_pk_bf16_f32 v37, v206, v207\n"
"v_cvt_pk_bf16_f32 v38, v216, v217\n"
"v_cvt_pk_bf16_f32 v39, v218, v219\n"
"ds_write_b128 v7, v[36:39] offset:58368\n"
".Lmk_z2:\n"
"s_waitcnt vmcnt(17)\n"
"; @A G1 2.0\n"
"v_mfma_f32_16x16x32_bf16 v[72:75], a[0:3], v[44:47], 0\n"
"v_mfma_f32_16x16x32_bf16 v[72:75], a[4:7], v[52:55], v[72:75]\n"
"s_add_u32 s98, s62, 0x800\n"
"v_mfma_f32_16x16x32_bf16 v[72:75], a[8:11], v[180:183], v[72:75]\n"
"s_add_u32 s96, s54, 0x8000\n"
"buffer_load_dwordx4 v[80:83], v32, s[4:7], s96 offen sc1\n"
"v_mfma_f32_16x16x32_bf16 v[72:75], a[12:15], v[76:79], v[72:75]\n"
"s_waitcnt vmcnt(13)\n"
"v_mfma_f32_16x16x32_bf16 v[72:75], a[16:19], v[68:71], v[72:75]\n"
"buffer_load_dword v255, v10, s[4:7], s98 offen sc1\n"
"v_mfma_f32_16x16x32_bf16 v[72:75], a[20:23], v[228:231], v[72:75]\n"
"s_waitcnt lgkmcnt(1)\n"
"v_add_f32_e32 v156, v156, v48\n"
"v_add_f32_e32 v157, v157, v49\n"
"v_mfma_f32_16x16x32_bf16 v[72:75], a[24:27], v[176:179], v[72:75]\n"
"v_add_f32_e32 v158, v158, v50\n"
"v_add_f32_e32 v159, v159, v51\n"
"v_mfma_f32_16x16x32_bf16 v[72:75], a[28:31], v[64:67], v[72:75]\n"
"buffer_load_dwordx4 v[48:51], v32, s[4:7], s96 offen offset:1024 sc1\n"
"v_mfma_f32_16x16x32_bf16 v[84:87], a[60:63], v[64:67], 0\n"
"buffer_load_dwordx4 a[164:167], v0, s[4:7], s97 offen offset:1024 sc1\n"
"v_mfma_f32_16x16x32_bf16 v[84:87], a[56:59], v[176:179], v[84:87]\n"
"buffer_load_dword v254, v10, s[4:7], s98 offen offset:256 sc1\n"
"v_mfma_f32_16x16x32_bf16 v[84:87], a[52:55], v[228:231], v[84:87]\n"
"v_add_f32_e32 v56, v56, v172\n"
"v_add_f32_e32 v57, v57, v173\n"
"v_mfma_f32_16x16x32_bf16 v[84:87], a[48:51], v[68:71], v[84:87]\n"
"v_add_f32_e32 v58, v58, v174\n"
"v_add_f32_e32 v59, v59, v175\n"
"v_mfma_f32_16x16x32_bf16 v[84:87], a[44:47], v[76:79], v[84:87]\n"
"buffer_load_dwordx4 v[88:91], v33, s[4:7], s96 offen sc1\n"
"v_mfma_f32_16x16x32_bf16 v[84:87], a[40:43], v[180:183], v[84:87]\n"
"ds_write_b128 v3, v[72:75]\n"
"v_mfma_f32_16x16x32_bf16 v[84:87], a[36:39], v[52:55], v[84:87]\n"
"buffer_load_dword v252, v10, s[4:7], s98 offen offset:512 sc1\n"
"v_mfma_f32_16x16x32_bf16 v[84:87], a[32:35], v[44:47], v[84:87]\n"
"v_mul_f32_e32 v156, v156, v253\n"
"v_mul_f32_e32 v157, v157, v253\n"
"v_mfma_f32_16x16x32_bf16 v[72:75], a[64:67], v[44:47], 0\n"
"v_mul_f32_e32 v158, v158, v253\n"
"v_mul_f32_e32 v159, v159, v253\n"
"v_mfma_f32_16x16x32_bf16 v[72:75], a[68:71], v[52:55], v[72:75]\n"
"buffer_load_dwordx4 v[92:95], v33, s[4:7], s96 offen offset:1024 sc1\n"
"v_mfma_f32_16x16x32_bf16 v[72:75], a[72:75], v[180:183], v[72:75]\n"
"buffer_load_dword v251, v10, s[4:7], s98 offen offset:768 sc1\n"
"v_mfma_f32_16x16x32_bf16 v[72:75], a[76:79], v[76:79], v[72:75]\n"
"v_mul_f32_e32 v56, v56, v253\n"
"v_mul_f32_e32 v57, v57, v253\n"
"v_mfma_f32_16x16x32_bf16 v[72:75], a[80:83], v[68:71], v[72:75]\n"
"v_mul_f32_e32 v58, v58, v253\n"
"v_mul_f32_e32 v59, v59, v253\n"
"v_mfma_f32_16x16x32_bf16 v[72:75], a[84:87], v[228:231], v[72:75]\n"
"ds_write_b128 v3, v[84:87] offset:1024\n"
"v_mfma_f32_16x16x32_bf16 v[72:75], a[88:91], v[176:179], v[72:75]\n"
"buffer_load_dwordx4 v[84:87], v34, s[4:7], s96 offen sc1\n"
"v_mfma_f32_16x16x32_bf16 v[72:75], a[92:95], v[64:67], v[72:75]\n"
"buffer_load_dword v253, v10, s[4:7], s98 offen offset:1024 sc1\n"
"v_mfma_f32_16x16x32_bf16 v[96:99], a[124:127], v[64:67], 0\n"
"v_cvt_pk_bf16_f32 v172, v156, v157\n"
"v_cvt_pk_bf16_f32 v173, v158, v159\n"
"v_mfma_f32_16x16x32_bf16 v[96:99], a[120:123], v[176:179], v[96:99]\n"
"v_cvt_pk_bf16_f32 v174, v56, v57\n"
"v_cvt_pk_bf16_f32 v175, v58, v59\n"
"v_mfma_f32_16x16x32_bf16 v[96:99], a[116:119], v[228:231], v[96:99]\n"
"buffer_load_dword v250, v10, s[4:7], s98 offen offset:1280 sc1\n"
"v_mfma_f32_16x16x32_bf16 v[96:99], a[112:115], v[68:71], v[96:99]\n"
"buffer_load_dwordx4 v[56:59], v34, s[4:7], s96 offen offset:1024 sc1\n"
"v_mfma_f32_16x16x32_bf16 v[96:99], a[108:111], v[76:79], v[96:99]\n"
"buffer_load_dwordx4 a[168:171], v0, s[4:7], s97 offen offset:2048 sc1\n"
"v_mfma_f32_16x16x32_bf16 v[96:99], a[104:107], v[180:183], v[96:99]\n"
"ds_write_b128 v3, v[72:75] offset:2048\n"
"v_mfma_f32_16x16x32_bf16 v[96:99], a[100:103], v[52:55], v[96:99]\n"
"buffer_load_dword v249, v10, s[4:7], s98 offen offset:1536 sc1\n"
"v_mfma_f32_16x16x32_bf16 v[96:99], a[96:99], v[44:47], v[96:99]\n"
"ds_write_b128 v2, v[172:175] offset:50176\n"
"v_mfma_f32_16x16x32_bf16 v[72:75], v[104:107], v[44:47], 0\n"
"buffer_load_dwordx4 v[156:159], v35, s[4:7], s96 offen sc1\n"
"s_add_u32 s95, s64, 0x2000\n"
"v_mfma_f32_16x16x32_bf16 v[72:75], v[108:111], v[52:55], v[72:75]\n"
"buffer_load_dword v248, v10, s[4:7], s98 offen offset:1792 sc1\n"
"v_mfma_f32_16x16x32_bf16 v[72:75], v[112:115], v[180:183], v[72:75]\n"
"buffer_store_dwordx4 v[212:215], v8, s[4:7], s95 offen offset:1024 sc1\n"
"v_mfma_f32_16x16x32_bf16 v[72:75], v[120:123], v[76:79], v[72:75]\n"
"v_mfma_f32_16x16x32_bf16 v[72:75], v[132:135], v[68:71], v[72:75]\n"
"v_mfma_f32_16x16x32_bf16 v[72:75], v[136:139], v[228:231], v[72:75]\n"
"buffer_load_dwordx4 v[172:175], v35, s[4:7], s96 offen offset:1024 sc1\n"
"v_mfma_f32_16x16x32_bf16 v[72:75], v[140:143], v[176:179], v[72:75]\n"
"ds_write_b128 v3, v[96:99] offset:3072\n"
"v_mfma_f32_16x16x32_bf16 v[72:75], v[100:103], v[64:67], v[72:75]\n"
"v_mfma_f32_16x16x32_bf16 v[96:99], v[116:119], v[64:67], 0\n"
"v_mfma_f32_16x16x32_bf16 v[96:99], v[168:171], v[176:179], v[96:99]\n"
"v_mfma_f32_16x16x32_bf16 v[96:99], v[164:167], v[228:231], v[96:99]\n"
"v_mfma_f32_16x16x32_bf16 v[96:99], v[160:163], v[68:71], v[96:99]\n"
"v_mfma_f32_16x16x32_bf16 v[96:99], v[128:131], v[76:79], v[96:99]\n"
"v_mfma_f32_16x16x32_bf16 v[96:99], v[152:155], v[180:183], v[96:99]\n"
"s_nop 1\n"
"ds_write_b128 v3, v[72:75] offset:4096\n"
"v_mfma_f32_16x16x32_bf16 v[96:99], v[148:151], v[52:55], v[96:99]\n"
"v_mfma_f32_16x16x32_bf16 v[96:99], v[124:127], v[44:47], v[96:99]\n"
"s_waitcnt vmcnt(25)\n"
"; @A G1 2.1\n"
"v_mfma_f32_16x16x32_bf16 v[44:47], a[0:3], v[144:147], 0\n"
"buffer_load_dwordx4 a[172:175], v0, s[4:7], s97 offen offset:3072 sc1\n"
"v_mfma_f32_16x16x32_bf16 v[44:47], a[4:7], v[40:43], v[44:47]\n"
"v_mov_b32_e32 v52, -1\n"
"v_mov_b32_e32 v53, -1\n"
"v_mfma_f32_16x16x32_bf16 v[44:47], a[8:11], v[208:211], v[44:47]\n"
"s_add_u32 s98, s55, 0x8000\n"
"buffer_load_dwordx4 v[64:67], v32, s[4:7], s98 offen sc1\n"
"v_mfma_f32_16x16x32_bf16 v[44:47], a[12:15], v[24:27], v[44:47]\n"
"s_waitcnt vmcnt(21)\n"
"v_mov_b32_e32 v54, -1\n"
"v_mov_b32_e32 v55, -1\n"
"v_mfma_f32_16x16x32_bf16 v[44:47], a[16:19], v[60:63], v[44:47]\n"
"s_add_u32 s96, s60, 0x8000\n"
"buffer_store_dwordx4 v[52:55], v0, s[4:7], s96 offen sc1\n"
"v_mfma_f32_16x16x32_bf16 v[44:47], a[20:23], v[184:187], v[44:47]\n"
"ds_write_b128 v3, v[96:99] offset:5120\n"
"v_mfma_f32_16x16x32_bf16 v[44:47], a[24:27], v[200:203], v[44:47]\n"
"v_mfma_f32_16x16x32_bf16 v[44:47], a[28:31], v[28:31], v[44:47]\n"
"buffer_load_dwordx4 v[68:71], v32, s[4:7], s98 offen offset:1024 sc1\n"
"v_mfma_f32_16x16x32_bf16 v[72:75], a[60:63], v[28:31], 0\n"
"s_add_u32 s96, s64, 0x6000\n"
"v_mfma_f32_16x16x32_bf16 v[72:75], a[56:59], v[200:203], v[72:75]\n"
"v_mfma_f32_16x16x32_bf16 v[72:75], a[52:55], v[184:187], v[72:75]\n"
"buffer_store_dwordx4 v[36:39], v7, s[4:7], s96 offen offset:1024 sc1\n"
"v_mfma_f32_16x16x32_bf16 v[72:75], a[48:51], v[60:63], v[72:75]\n"
"v_mfma_f32_16x16x32_bf16 v[72:75], a[44:47], v[24:27], v[72:75]\n"
"buffer_load_dwordx4 v[36:39], v33, s[4:7], s98 offen sc1\n"
"v_mfma_f32_16x16x32_bf16 v[72:75], a[40:43], v[208:211], v[72:75]\n"
"ds_write_b128 v3, v[44:47] offset:6144\n"
"v_mfma_f32_16x16x32_bf16 v[72:75], a[36:39], v[40:43], v[72:75]\n"
"v_mfma_f32_16x16x32_bf16 v[72:75], a[32:35], v[144:147], v[72:75]\n"
"v_mfma_f32_16x16x32_bf16 v[44:47], a[64:67], v[144:147], 0\n"
"v_mfma_f32_16x16x32_bf16 v[44:47], a[68:71], v[40:43], v[44:47]\n"
"buffer_load_dwordx4 v[76:79], v33, s[4:7], s98 offen offset:1024 sc1\n"
"v_mfma_f32_16x16x32_bf16 v[44:47], a[72:75], v[208:211], v[44:47]\n"
"v_mfma_f32_16x16x32_bf16 v[44:47], a[76:79], v[24:27], v[44:47]\n"
"v_mfma_f32_16x16x32_bf16 v[44:47], a[80:83], v[60:63], v[44:47]\n"
"buffer_load_dwordx4 a[176:179], v1, s[4:7], s97 offen sc1\n"
"v_mfma_f32_16x16x32_bf16 v[44:47], a[84:87], v[184:187], v[44:47]\n"
"ds_write_b128 v3, v[72:75] offset:7168\n"
"v_mfma_f32_16x16x32_bf16 v[44:47], a[88:91], v[200:203], v[44:47]\n"
"buffer_load_dwordx4 v[72:75], v34, s[4:7], s98 offen sc1\n"
"v_mfma_f32_16x16x32_bf16 v[44:47], a[92:95], v[28:31], v[44:47]\n"
"v_mfma_f32_16x16x32_bf16 v[96:99], a[124:127], v[28:31], 0\n"
"s_add_u32 s96, s61, 0x440000\n"
"buffer_store_dwordx4 v[52:55], v0, s[4:7], s96 offen sc1\n"
"v_mfma_f32_16x16x32_bf16 v[96:99], a[120:123], v[200:203], v[96:99]\n"
"v_mfma_f32_16x16x32_bf16 v[96:99], a[116:119], v[184:187], v[96:99]\n"
"v_mfma_f32_16x16x32_bf16 v[96:99], a[112:115], v[60:63], v[96:99]\n"
"buffer_load_dwordx4 v[176:179], v34, s[4:7], s98 offen offset:1024 sc1\n"
"v_mfma_f32_16x16x32_bf16 v[96:99], a[108:111], v[24:27], v[96:99]\n"
"v_mfma_f32_16x16x32_bf16 v[96:99], a[104:107], v[208:211], v[96:99]\n"
"ds_write_b128 v3, v[44:47] offset:8192\n"
"v_mfma_f32_16x16x32_bf16 v[96:99], a[100:103], v[40:43], v[96:99]\n"
"v_mfma_f32_16x16x32_bf16 v[96:99], a[96:99], v[144:147], v[96:99]\n"
"v_mfma_f32_16x16x32_bf16 v[44:47], v[104:107], v[144:147], 0\n"
"buffer_load_dwordx4 v[180:183], v35, s[4:7], s98 offen sc1\n"
"v_mfma_f32_16x16x32_bf16 v[44:47], v[108:111], v[40:43], v[44:47]\n"
"v_mfma_f32_16x16x32_bf16 v[44:47], v[112:115], v[208:211], v[44:47]\n"
"v_mfma_f32_16x16x32_bf16 v[44:47], v[120:123], v[24:27], v[44:47]\n"
"v_mfma_f32_16x16x32_bf16 v[44:47], v[132:135], v[60:63], v[44:47]\n"
"s_waitcnt vmcnt(14)\n"
"v_add_f32_e32 v255, v255, v253\n"
"v_add_f32_e32 v254, v254, v250\n"
"v_mfma_f32_16x16x32_bf16 v[44:47], v[136:139], v[184:187], v[44:47]\n"
"buffer_load_dwordx4 v[188:191], v35, s[4:7], s98 offen offset:1024 sc1\n"
"v_mfma_f32_16x16x32_bf16 v[44:47], v[140:143], v[200:203], v[44:47]\n"
"ds_write_b128 v3, v[96:99] offset:9216\n"
"v_mfma_f32_16x16x32_bf16 v[44:47], v[100:103], v[28:31], v[44:47]\n"
"v_add_f32_e32 v252, v252, v249\n"
"v_add_f32_e32 v251, v251, v248\n"
"v_mfma_f32_16x16x32_bf16 v[96:99], v[116:119], v[28:31], 0\n"
"buffer_load_dwordx4 a[180:183], v1, s[4:7], s97 offen offset:1024 sc1\n"
"v_mfma_f32_16x16x32_bf16 v[96:99], v[168:171], v[200:203], v[96:99]\n"
"v_add_f32_e32 v255, v255, v252\n"
"v_add_f32_e32 v254, v254, v251\n"
"v_mfma_f32_16x16x32_bf16 v[96:99], v[164:167], v[184:187], v[96:99]\n"
"v_add_f32_e32 v255, v255, v254\n"
"v_mov_b32_e32 v254, v255\n"
"v_mfma_f32_16x16x32_bf16 v[96:99], v[160:163], v[60:63], v[96:99]\n"
"s_nop 0\n"
"v_permlane32_swap_b32_e32 v255, v254\n"
"v_add_f32_e32 v255, v255, v254\n"
"v_mfma_f32_16x16x32_bf16 v[96:99], v[128:131], v[24:27], v[96:99]\n"
"v_mul_f32_e32 v255, 0x3a800000, v255\n"
"v_mov_b32_e32 v254, v255\n"
"v_mfma_f32_16x16x32_bf16 v[96:99], v[152:155], v[208:211], v[96:99]\n"
"ds_write_b128 v3, v[44:47] offset:10240\n"
"v_mfma_f32_16x16x32_bf16 v[96:99], v[148:151], v[40:43], v[96:99]\n"
"v_permlane16_swap_b32_e32 v255, v254\n"
"v_cvt_pk_bf16_f32 v253, -v255, 0\n"
"v_mfma_f32_16x16x32_bf16 v[96:99], v[124:127], v[144:147], v[96:99]\n"
"v_fma_f32 v254, -v255, v255, v254\n"
"v_lshlrev_b32_e32 v253, 16, v253\n"
"; @A G1 2.2\n"
"v_mfma_f32_16x16x32_bf16 v[24:27], a[0:3], v[80:83], 0\n"
"s_add_u32 s98, s61, 0x440000\n"
"buffer_store_dwordx4 v[52:55], v0, s[4:7], s98 offen offset:1024 sc1\n"
"v_max_f32_e32 v254, 0, v254\n"
"v_cvt_pk_bf16_f32 v254, v254, 0\n"
"v_mfma_f32_16x16x32_bf16 v[24:27], a[4:7], v[48:51], v[24:27]\n"
"v_lshlrev_b32_e32 v254, 16, v254\n"
"v_add_f32_e32 v254, 0x3727c5ac, v254\n"
"v_mfma_f32_16x16x32_bf16 v[24:27], a[8:11], v[88:91], v[24:27]\n"
"v_cvt_pk_bf16_f32 v254, v254, 0\n"
"v_lshlrev_b32_e32 v254, 16, v254\n"
"v_mfma_f32_16x16x32_bf16 v[24:27], a[12:15], v[92:95], v[24:27]\n"
"s_waitcnt vmcnt(15)\n"
"v_sqrt_f32_e32 v254, v254\n"
"v_mfma_f32_16x16x32_bf16 v[24:27], a[16:19], v[84:87], v[24:27]\n"
"s_mov_b32 s98, .Lmk_z4-.Lmk_g11\n"
"s_getpc_b64 s[84:85]\n"
".Lmk_g11:\n"
"s_add_u32 s84, s84, s98\n"
"s_addc_u32 s85, s85, 0\n"
"s_and_b32 s84, s84, -16\n"
"v_mfma_f32_16x16x32_bf16 v[24:27], a[20:23], v[56:59], v[24:27]\n"
"ds_write_b128 v3, v[96:99] offset:11264\n"
"v_mfma_f32_16x16x32_bf16 v[24:27], a[24:27], v[156:159], v[24:27]\n"
"s_and_b32 s85, s85, 0xffff\n"
"s_sub_u32 s98, .Lmk_end-.Lmk_g11, s98\n"
"s_min_u32 s86, s98, 0x4000\n"
"s_mov_b32 s87, 0x20000\n"
"v_mfma_f32_16x16x32_bf16 v[24:27], a[28:31], v[172:175], v[24:27]\n"
"s_mov_b32 m0, 0x27800\n"
"s_nop 0\n"
"buffer_load_dwordx4 v0, s[84:87], s99 offen lds\n"
"v_mfma_f32_16x16x32_bf16 v[28:31], a[60:63], v[172:175], 0\n"
"v_cvt_pk_bf16_f32 v254, v254, 0\n"
"v_lshlrev_b32_e32 v254, 16, v254\n"
"v_mfma_f32_16x16x32_bf16 v[28:31], a[56:59], v[156:159], v[28:31]\n"
"v_rcp_f32_e32 v255, v254\n"
"v_mfma_f32_16x16x32_bf16 v[28:31], a[52:55], v[56:59], v[28:31]\n"
"s_add_u32 s98, s52, 0\n"
"buffer_load_dwordx4 v[40:43], v32, s[4:7], s98 offen sc1\n"
"v_mfma_f32_16x16x32_bf16 v[28:31], a[48:51], v[84:87], v[28:31]\n"
"s_waitcnt lgkmcnt(0)\n"
"s_barrier\n"
"v_mfma_f32_16x16x32_bf16 v[28:31], a[44:47], v[92:95], v[28:31]\n"
"buffer_load_dwordx4 a[184:187], v1, s[4:7], s97 offen offset:2048 sc1\n"
"v_mfma_f32_16x16x32_bf16 v[28:31], a[40:43], v[88:91], v[28:31]\n"
"ds_write_b128 v3, v[24:27] offset:12288\n"
"v_mfma_f32_16x16x32_bf16 v[28:31], a[36:39], v[48:51], v[28:31]\n"
"ds_read_b128 v[24:27], v4\n"
"v_mul_f32_e32 v254, 0x3e8293ee, v255\n"
"v_mfma_f32_16x16x32_bf16 v[28:31], a[32:35], v[80:83], v[28:31]\n"
"buffer_load_dwordx4 v[44:47], v32, s[4:7], s98 offen offset:1024 sc1\n"
"v_mfma_f32_16x16x32_bf16 v[60:63], a[64:67], v[80:83], 0\n"
"ds_read_b128 v[96:99], v9 offset:128\n"
"ds_read_b128 v[144:147], v5\n"
"v_mfma_f32_16x16x32_bf16 v[60:63], a[68:71], v[48:51], v[60:63]\n"
"v_mfma_f32_16x16x32_bf16 v[60:63], a[72:75], v[88:91], v[60:63]\n"
"v_mfma_f32_16x16x32_bf16 v[60:63], a[76:79], v[92:95], v[60:63]\n"
"ds_read_b128 v[184:187], v4 offset:1024\n"
"ds_read_b128 v[192:195], v9 offset:192\n"
"v_mfma_f32_16x16x32_bf16 v[60:63], a[80:83], v[84:87], v[60:63]\n"
"buffer_load_dwordx4 v[196:199], v33, s[4:7], s98 offen sc1\n"
"v_mfma_f32_16x16x32_bf16 v[60:63], a[84:87], v[56:59], v[60:63]\n"
"ds_write_b128 v3, v[28:31] offset:13312\n"
"v_mfma_f32_16x16x32_bf16 v[60:63], a[88:91], v[156:159], v[60:63]\n"
"ds_read_b128 v[28:31], v5 offset:1024\n"
"s_waitcnt lgkmcnt(4)\n"
"v_mfma_f32_16x16x32_bf16 v[60:63], a[92:95], v[172:175], v[60:63]\n"
"v_fmac_f32_e32 v24, v253, v96\n"
"v_fmac_f32_e32 v25, v253, v97\n"
"v_mfma_f32_16x16x32_bf16 v[200:203], a[124:127], v[172:175], 0\n"
"s_add_u32 s96, s63, 0x44000\n"
"buffer_store_dword v52, v14, s[4:7], s96 offen sc1\n"
"v_fmac_f32_e32 v26, v253, v98\n"
"v_fmac_f32_e32 v27, v253, v99\n"
"v_mfma_f32_16x16x32_bf16 v[200:203], a[120:123], v[156:159], v[200:203]\n"
"buffer_load_dwordx4 v[52:55], v33, s[4:7], s98 offen offset:1024 sc1\n"
"v_mfma_f32_16x16x32_bf16 v[200:203], a[116:119], v[56:59], v[200:203]\n"
"ds_read_b128 v[96:99], v4 offset:2048\n"
"ds_read_b128 v[204:207], v9 offset:256\n"
"v_mfma_f32_16x16x32_bf16 v[200:203], a[112:115], v[84:87], v[200:203]\n"
"ds_read_b128 v[208:211], v5 offset:2048\n"
"v_add_f32_e32 v24, v24, v144\n"
"v_mfma_f32_16x16x32_bf16 v[200:203], a[108:111], v[92:95], v[200:203]\n"
"v_add_f32_e32 v25, v25, v145\n"
"v_add_f32_e32 v26, v26, v146\n"
"v_mfma_f32_16x16x32_bf16 v[200:203], a[104:107], v[88:91], v[200:203]\n"
"ds_write_b128 v3, v[60:63] offset:14336\n"
"v_mfma_f32_16x16x32_bf16 v[200:203], a[100:103], v[48:51], v[200:203]\n"
"buffer_load_dwordx4 v[60:63], v34, s[4:7], s98 offen sc1\n"
"v_mfma_f32_16x16x32_bf16 v[200:203], a[96:99], v[80:83], v[200:203]\n"
"s_waitcnt lgkmcnt(4)\n"
"v_add_f32_e32 v27, v27, v147\n"
"v_fmac_f32_e32 v184, v253, v192\n"
"v_mfma_f32_16x16x32_bf16 v[144:147], v[104:107], v[80:83], 0\n"
"buffer_load_dwordx4 a[188:191], v1, s[4:7], s97 offen offset:3072 sc1\n"
"v_mfma_f32_16x16x32_bf16 v[144:147], v[108:111], v[48:51], v[144:147]\n"
"ds_read_b128 v[212:215], v4 offset:3072\n"
"ds_read_b128 v[216:219], v9 offset:320\n"
"v_mfma_f32_16x16x32_bf16 v[144:147], v[112:115], v[88:91], v[144:147]\n"
"s_waitcnt vmcnt(19)\n"
"s_add_u32 s97, s68, 0x1000\n"
"ds_read_b128 v[220:223], v5 offset:3072\n"
"v_mfma_f32_16x16x32_bf16 v[144:147], v[120:123], v[92:95], v[144:147]\n"
"buffer_load_dwordx4 v[224:227], v34, s[4:7], s98 offen offset:1024 sc1\n"
"v_mfma_f32_16x16x32_bf16 v[144:147], v[132:135], v[84:87], v[144:147]\n"
"buffer_store_dwordx2 v[18:19], v11, s[4:7], s97 offen offset:64 sc1\n"
"v_mfma_f32_16x16x32_bf16 v[144:147], v[136:139], v[56:59], v[144:147]\n"
"ds_write_b128 v3, v[200:203] offset:15360\n"
"v_mfma_f32_16x16x32_bf16 v[144:147], v[140:143], v[156:159], v[144:147]\n"
"v_fmac_f32_e32 v185, v253, v193\n"
"v_fmac_f32_e32 v186, v253, v194\n"
"v_mfma_f32_16x16x32_bf16 v[144:147], v[100:103], v[172:175], v[144:147]\n"
"v_fmac_f32_e32 v187, v253, v195\n"
"v_add_f32_e32 v184, v184, v28\n"
"v_mfma_f32_16x16x32_bf16 v[192:195], v[116:119], v[172:175], 0\n"
"buffer_load_dwordx4 v[172:175], v35, s[4:7], s98 offen sc1\n"
"v_mfma_f32_16x16x32_bf16 v[192:195], v[168:171], v[156:159], v[192:195]\n"
"ds_read_b128 v[156:159], v4 offset:4096\n"
"ds_read_b128 v[200:203], v9\n"
"v_mfma_f32_16x16x32_bf16 v[192:195], v[164:167], v[56:59], v[192:195]\n"
"ds_read_b128 v[56:59], v5 offset:4096\n"
"v_add_f32_e32 v185, v185, v29\n"
"v_mfma_f32_16x16x32_bf16 v[192:195], v[160:163], v[84:87], v[192:195]\n"
"s_waitcnt lgkmcnt(8)\n"
"v_add_f32_e32 v186, v186, v30\n"
"v_add_f32_e32 v187, v187, v31\n"
"v_mfma_f32_16x16x32_bf16 v[192:195], v[128:131], v[92:95], v[192:195]\n"
"v_fmac_f32_e32 v96, v253, v204\n"
"v_fmac_f32_e32 v97, v253, v205\n"
"v_mfma_f32_16x16x32_bf16 v[192:195], v[152:155], v[88:91], v[192:195]\n"
"buffer_load_dwordx4 v[28:31], v35, s[4:7], s98 offen offset:1024 sc1\n"
"v_mfma_f32_16x16x32_bf16 v[192:195], v[148:151], v[48:51], v[192:195]\n"
"ds_write_b128 v3, v[144:147] offset:16384\n"
"v_mfma_f32_16x16x32_bf16 v[192:195], v[124:127], v[80:83], v[192:195]\n"
"s_waitcnt vmcnt(21)\n"
"ds_read_b128 v[48:51], v4 offset:5120\n"
"ds_read_b128 v[80:83], v9 offset:64\n"
"; @A G1 2.3\n"
"v_mfma_f32_16x16x32_bf16 v[24:27], a[0:3], v[64:67], v[24:27]\n"
"ds_read_b128 v[84:87], v5 offset:5120\n"
"v_fmac_f32_e32 v98, v253, v206\n"
"v_mfma_f32_16x16x32_bf16 v[24:27], a[4:7], v[68:71], v[24:27]\n"
"v_fmac_f32_e32 v99, v253, v207\n"
"v_add_f32_e32 v96, v96, v208\n"
"v_mfma_f32_16x16x32_bf16 v[24:27], a[8:11], v[36:39], v[24:27]\n"
"s_add_u32 s98, s53, 0\n"
"buffer_load_dwordx4 v[88:91], v32, s[4:7], s98 offen sc1\n"
"v_mfma_f32_16x16x32_bf16 v[24:27], a[12:15], v[76:79], v[24:27]\n"
"s_waitcnt vmcnt(16)\n"
"s_add_u32 s97, s70, 0x4000\n"
"v_add_f32_e32 v97, v97, v209\n"
"v_mfma_f32_16x16x32_bf16 v[24:27], a[16:19], v[72:75], v[24:27]\n"
"buffer_load_dwordx4 a[192:195], v0, s[4:7], s97 offen sc1\n"
"v_mfma_f32_16x16x32_bf16 v[24:27], a[20:23], v[176:179], v[24:27]\n"
"ds_write_b128 v3, v[192:195] offset:17408\n"
"v_mfma_f32_16x16x32_bf16 v[24:27], a[24:27], v[180:183], v[24:27]\n"
"s_waitcnt lgkmcnt(9)\n"
"v_add_f32_e32 v98, v98, v210\n"
"v_add_f32_e32 v99, v99, v211\n"
"v_mfma_f32_16x16x32_bf16 v[24:27], a[28:31], v[188:191], v[24:27]\n"
"buffer_load_dwordx4 v[92:95], v32, s[4:7], s98 offen offset:1024 sc1\n"
"v_mfma_f32_16x16x32_bf16 v[184:187], a[60:63], v[188:191], v[184:187]\n"
"v_fmac_f32_e32 v212, v253, v216\n"
"v_fmac_f32_e32 v213, v253, v217\n"
"v_mfma_f32_16x16x32_bf16 v[184:187], a[56:59], v[180:183], v[184:187]\n"
"v_fmac_f32_e32 v214, v253, v218\n"
"v_fmac_f32_e32 v215, v253, v219\n"
"v_mfma_f32_16x16x32_bf16 v[184:187], a[52:55], v[176:179], v[184:187]\n"
"s_waitcnt lgkmcnt(0)\n"
"s_barrier\n"
"v_mfma_f32_16x16x32_bf16 v[184:187], a[48:51], v[72:75], v[184:187]\n"
"ds_read_b128 v[144:147], v6\n"
"ds_read_b128 v[192:195], v6 offset:1024\n"
"v_mfma_f32_16x16x32_bf16 v[184:187], a[44:47], v[76:79], v[184:187]\n"
"buffer_load_dwordx4 v[204:207], v33, s[4:7], s98 offen sc1\n"
"v_mfma_f32_16x16x32_bf16 v[184:187], a[40:43], v[36:39], v[184:187]\n"
"v_add_f32_e32 v212, v212, v220\n"
"v_add_f32_e32 v213, v213, v221\n"
"v_mfma_f32_16x16x32_bf16 v[184:187], a[36:39], v[68:71], v[184:187]\n"
"v_add_f32_e32 v214, v214, v222\n"
"v_add_f32_e32 v215, v215, v223\n"
"v_mfma_f32_16x16x32_bf16 v[184:187], a[32:35], v[64:67], v[184:187]\n"
"v_fmac_f32_e32 v156, v253, v200\n"
"v_fmac_f32_e32 v157, v253, v201\n"
"v_mfma_f32_16x16x32_bf16 v[96:99], a[64:67], v[64:67], v[96:99]\n"
"v_fmac_f32_e32 v158, v253, v202\n"
"v_fmac_f32_e32 v159, v253, v203\n"
"v_mfma_f32_16x16x32_bf16 v[96:99], a[68:71], v[68:71], v[96:99]\n"
"buffer_load_dwordx4 v[200:203], v33, s[4:7], s98 offen offset:1024 sc1\n"
"v_mfma_f32_16x16x32_bf16 v[96:99], a[72:75], v[36:39], v[96:99]\n"
"v_add_f32_e32 v156, v156, v56\n"
"v_add_f32_e32 v157, v157, v57\n"
"v_mfma_f32_16x16x32_bf16 v[96:99], a[76:79], v[76:79], v[96:99]\n"
"v_add_f32_e32 v158, v158, v58\n"
"v_add_f32_e32 v159, v159, v59\n"
"v_mfma_f32_16x16x32_bf16 v[96:99], a[80:83], v[72:75], v[96:99]\n"
"s_waitcnt lgkmcnt(0)\n"
"v_fmac_f32_e32 v48, v253, v80\n"
"v_fmac_f32_e32 v49, v253, v81\n"
"v_mfma_f32_16x16x32_bf16 v[96:99], a[84:87], v[176:179], v[96:99]\n"
"v_fmac_f32_e32 v50, v253, v82\n"
"v_fmac_f32_e32 v51, v253, v83\n"
"v_mfma_f32_16x16x32_bf16 v[96:99], a[88:91], v[180:183], v[96:99]\n"
"buffer_load_dwordx4 v[56:59], v34, s[4:7], s98 offen sc1\n"
"v_mfma_f32_16x16x32_bf16 v[96:99], a[92:95], v[188:191], v[96:99]\n"
"ds_read_b128 v[80:83], v6 offset:2048\n"
"ds_read_b128 v[208:211], v6 offset:3072\n"
"v_mfma_f32_16x16x32_bf16 v[212:215], a[124:127], v[188:191], v[212:215]\n"
"buffer_load_dwordx4 a[196:199], v0, s[4:7], s97 offen offset:1024 sc1\n"
"v_mfma_f32_16x16x32_bf16 v[212:215], a[120:123], v[180:183], v[212:215]\n"
"v_add_f32_e32 v48, v48, v84\n"
"v_add_f32_e32 v49, v49, v85\n"
"v_mfma_f32_16x16x32_bf16 v[212:215], a[116:119], v[176:179], v[212:215]\n"
"s_add_u32 s96, s68, 0x1000\n"
"buffer_store_dwordx2 v[18:19], v11, s[4:7], s96 offen offset:96 sc1\n"
"v_add_f32_e32 v50, v50, v86\n"
"v_add_f32_e32 v51, v51, v87\n"
"v_mfma_f32_16x16x32_bf16 v[212:215], a[112:115], v[72:75], v[212:215]\n"
"buffer_load_dwordx4 v[84:87], v34, s[4:7], s98 offen offset:1024 sc1\n"
"v_mfma_f32_16x16x32_bf16 v[212:215], a[108:111], v[76:79], v[212:215]\n"
"v_add_f32_e32 v24, v24, v144\n"
"v_add_f32_e32 v25, v25, v145\n"
"v_mfma_f32_16x16x32_bf16 v[212:215], a[104:107], v[36:39], v[212:215]\n"
"v_add_f32_e32 v26, v26, v146\n"
"v_add_f32_e32 v27, v27, v147\n"
"v_mfma_f32_16x16x32_bf16 v[212:215], a[100:103], v[68:71], v[212:215]\n"
"v_add_f32_e32 v184, v184, v192\n"
"v_add_f32_e32 v185, v185, v193\n"
"v_mfma_f32_16x16x32_bf16 v[212:215], a[96:99], v[64:67], v[212:215]\n"
"v_add_f32_e32 v186, v186, v194\n"
"v_add_f32_e32 v187, v187, v195\n"
"v_mfma_f32_16x16x32_bf16 v[156:159], v[104:107], v[64:67], v[156:159]\n"
"buffer_load_dwordx4 v[144:147], v35, s[4:7], s98 offen sc1\n"
"v_mfma_f32_16x16x32_bf16 v[156:159], v[108:111], v[68:71], v[156:159]\n"
"v_mul_f32_e32 v24, v24, v255\n"
"v_mul_f32_e32 v25, v25, v255\n"
"v_mfma_f32_16x16x32_bf16 v[156:159], v[112:115], v[36:39], v[156:159]\n"
"v_mul_f32_e32 v26, v26, v255\n"
"v_mul_f32_e32 v27, v27, v255\n"
"v_mfma_f32_16x16x32_bf16 v[156:159], v[120:123], v[76:79], v[156:159]\n"
"v_mul_f32_e32 v184, v184, v255\n"
"v_mul_f32_e32 v185, v185, v255\n"
"v_mfma_f32_16x16x32_bf16 v[156:159], v[132:135], v[72:75], v[156:159]\n"
"s_waitcnt lgkmcnt(0)\n"
"v_mul_f32_e32 v186, v186, v255\n"
"v_mul_f32_e32 v187, v187, v255\n"
"v_mfma_f32_16x16x32_bf16 v[156:159], v[136:139], v[176:179], v[156:159]\n"
"buffer_load_dwordx4 v[192:195], v35, s[4:7], s98 offen offset:1024 sc1\n"
"v_mfma_f32_16x16x32_bf16 v[156:159], v[140:143], v[180:183], v[156:159]\n"
"ds_read_b128 v[216:219], v6 offset:4096\n"
"ds_read_b128 v[220:223], v6 offset:5120\n"
"v_mfma_f32_16x16x32_bf16 v[156:159], v[100:103], v[188:191], v[156:159]\n"
"v_cvt_pk_bf16_f32 v228, v24, v25\n"
"v_cvt_pk_bf16_f32 v229, v26, v27\n"
"v_mfma_f32_16x16x32_bf16 v[48:51], v[116:119], v[188:191], v[48:51]\n"
"v_cvt_pk_bf16_f32 v230, v184, v185\n"
"v_cvt_pk_bf16_f32 v231, v186, v187\n"
"v_mfma_f32_16x16x32_bf16 v[48:51], v[168:171], v[180:183], v[48:51]\n"
"ds_write_b128 v8, v[228:231] offset:17408\n"
"v_mfma_f32_16x16x32_bf16 v[48:51], v[164:167], v[176:179], v[48:51]\n"
"v_add_f32_e32 v96, v96, v80\n"
"v_add_f32_e32 v97, v97, v81\n"
"v_mfma_f32_16x16x32_bf16 v[48:51], v[160:163], v[72:75], v[48:51]\n"
"v_add_f32_e32 v98, v98, v82\n"
"v_add_f32_e32 v99, v99, v83\n"
"v_mfma_f32_16x16x32_bf16 v[48:51], v[128:131], v[76:79], v[48:51]\n"
"v_add_f32_e32 v212, v212, v208\n"
"v_add_f32_e32 v213, v213, v209\n"
"v_mfma_f32_16x16x32_bf16 v[48:51], v[152:155], v[36:39], v[48:51]\n"
"v_add_f32_e32 v214, v214, v210\n"
"v_add_f32_e32 v215, v215, v211\n"
"v_mfma_f32_16x16x32_bf16 v[48:51], v[148:151], v[68:71], v[48:51]\n"
"v_mul_f32_e32 v96, v96, v255\n"
"v_mul_f32_e32 v97, v97, v255\n"
"v_mfma_f32_16x16x32_bf16 v[48:51], v[124:127], v[64:67], v[48:51]\n"
"v_mul_f32_e32 v98, v98, v255\n"
"v_mul_f32_e32 v99, v99, v255\n"
"v_mul_f32_e32 v212, v212, v255\n"
"v_mul_f32_e32 v213, v213, v255\n"
"v_mul_f32_e32 v214, v214, v255\n"
"v_mul_f32_e32 v215, v215, v255\n"
"v_cvt_pk_bf16_f32 v24, v96, v97\n"
"v_cvt_pk_bf16_f32 v25, v98, v99\n"
"v_cvt_pk_bf16_f32 v26, v212, v213\n"
"v_cvt_pk_bf16_f32 v27, v214, v215\n"
"ds_write_b128 v7, v[24:27] offset:50176\n"
".Lmk_z3:\n"
"s_waitcnt vmcnt(17)\n"
"; @A G1 3.0\n"
"v_mfma_f32_16x16x32_bf16 v[36:39], a[0:3], v[40:43], 0\n"
"v_mfma_f32_16x16x32_bf16 v[36:39], a[4:7], v[44:47], v[36:39]\n"
"s_add_u32 s98, s62, 0\n"
"v_mfma_f32_16x16x32_bf16 v[36:39], a[8:11], v[196:199], v[36:39]\n"
"s_add_u32 s96, s54, 0\n"
"buffer_load_dwordx4 v[64:67], v32, s[4:7], s96 offen sc1\n"
"v_mfma_f32_16x16x32_bf16 v[36:39], a[12:15], v[52:55], v[36:39]\n"
"s_waitcnt vmcnt(12)\n"
"v_mfma_f32_16x16x32_bf16 v[36:39], a[16:19], v[60:63], v[36:39]\n"
"buffer_load_dword v255, v10, s[4:7], s98 offen sc1\n"
"v_mfma_f32_16x16x32_bf16 v[36:39], a[20:23], v[224:227], v[36:39]\n"
"s_waitcnt lgkmcnt(2)\n"
"v_add_f32_e32 v156, v156, v216\n"
"v_add_f32_e32 v157, v157, v217\n"
"v_mfma_f32_16x16x32_bf16 v[36:39], a[24:27], v[172:175], v[36:39]\n"
"v_add_f32_e32 v158, v158, v218\n"
"v_add_f32_e32 v159, v159, v219\n"
"v_mfma_f32_16x16x32_bf16 v[36:39], a[28:31], v[28:31], v[36:39]\n"
"buffer_load_dwordx4 v[68:71], v32, s[4:7], s96 offen offset:1024 sc1\n"
"v_mfma_f32_16x16x32_bf16 v[72:75], a[60:63], v[28:31], 0\n"
"buffer_load_dwordx4 a[200:203], v0, s[4:7], s97 offen offset:2048 sc1\n"
"v_mfma_f32_16x16x32_bf16 v[72:75], a[56:59], v[172:175], v[72:75]\n"
"buffer_load_dword v253, v10, s[4:7], s98 offen offset:256 sc1\n"
"v_mfma_f32_16x16x32_bf16 v[72:75], a[52:55], v[224:227], v[72:75]\n"
"v_add_f32_e32 v48, v48, v220\n"
"v_add_f32_e32 v49, v49, v221\n"
"v_mfma_f32_16x16x32_bf16 v[72:75], a[48:51], v[60:63], v[72:75]\n"
"v_add_f32_e32 v50, v50, v222\n"
"v_add_f32_e32 v51, v51, v223\n"
"v_mfma_f32_16x16x32_bf16 v[72:75], a[44:47], v[52:55], v[72:75]\n"
"buffer_load_dwordx4 v[76:79], v33, s[4:7], s96 offen sc1\n"
"v_mfma_f32_16x16x32_bf16 v[72:75], a[40:43], v[196:199], v[72:75]\n"
"ds_write_b128 v3, v[36:39]\n"
"v_mfma_f32_16x16x32_bf16 v[72:75], a[36:39], v[44:47], v[72:75]\n"
"buffer_load_dword v252, v10, s[4:7], s98 offen offset:512 sc1\n"
"v_mfma_f32_16x16x32_bf16 v[72:75], a[32:35], v[40:43], v[72:75]\n"
"v_mul_f32_e32 v156, v156, v254\n"
"v_mul_f32_e32 v157, v157, v254\n"
"v_mfma_f32_16x16x32_bf16 v[36:39], a[64:67], v[40:43], 0\n"
"v_mul_f32_e32 v158, v158, v254\n"
"v_mul_f32_e32 v159, v159, v254\n"
"v_mfma_f32_16x16x32_bf16 v[36:39], a[68:71], v[44:47], v[36:39]\n"
"buffer_load_dwordx4 v[80:83], v33, s[4:7], s96 offen offset:1024 sc1\n"
"v_mfma_f32_16x16x32_bf16 v[36:39], a[72:75], v[196:199], v[36:39]\n"
"buffer_load_dword v251, v10, s[4:7], s98 offen offset:768 sc1\n"
"v_mfma_f32_16x16x32_bf16 v[36:39], a[76:79], v[52:55], v[36:39]\n"
"v_mul_f32_e32 v48, v48, v254\n"
"v_mul_f32_e32 v49, v49, v254\n"
"v_mfma_f32_16x16x32_bf16 v[36:39], a[80:83], v[60:63], v[36:39]\n"
"v_mul_f32_e32 v50, v50, v254\n"
"v_mul_f32_e32 v51, v51, v254\n"
"v_mfma_f32_16x16x32_bf16 v[36:39], a[84:87], v[224:227], v[36:39]\n"
"ds_write_b128 v3, v[72:75] offset:1024\n"
"v_mfma_f32_16x16x32_bf16 v[36:39], a[88:91], v[172:175], v[36:39]\n"
"buffer_load_dwordx4 v[72:75], v34, s[4:7], s96 offen sc1\n"
"v_mfma_f32_16x16x32_bf16 v[36:39], a[92:95], v[28:31], v[36:39]\n"
"buffer_load_dword v254, v10, s[4:7], s98 offen offset:1024 sc1\n"
"v_mfma_f32_16x16x32_bf16 v[96:99], a[124:127], v[28:31], 0\n"
"v_cvt_pk_bf16_f32 v176, v156, v157\n"
"v_cvt_pk_bf16_f32 v177, v158, v159\n"
"v_mfma_f32_16x16x32_bf16 v[96:99], a[120:123], v[172:175], v[96:99]\n"
"v_cvt_pk_bf16_f32 v178, v48, v49\n"
"v_cvt_pk_bf16_f32 v179, v50, v51\n"
"v_mfma_f32_16x16x32_bf16 v[96:99], a[116:119], v[224:227], v[96:99]\n"
"buffer_load_dwordx4 a[204:207], v0, s[4:7], s97 offen offset:3072 sc1\n"
"v_mfma_f32_16x16x32_bf16 v[96:99], a[112:115], v[60:63], v[96:99]\n"
"buffer_load_dwordx4 v[48:51], v34, s[4:7], s96 offen offset:1024 sc1\n"
"v_mfma_f32_16x16x32_bf16 v[96:99], a[108:111], v[52:55], v[96:99]\n"
"buffer_load_dword v250, v10, s[4:7], s98 offen offset:1280 sc1\n"
"v_mfma_f32_16x16x32_bf16 v[96:99], a[104:107], v[196:199], v[96:99]\n"
"ds_write_b128 v3, v[36:39] offset:2048\n"
"v_mfma_f32_16x16x32_bf16 v[96:99], a[100:103], v[44:47], v[96:99]\n"
"ds_write_b128 v2, v[176:179] offset:17408\n"
"v_mfma_f32_16x16x32_bf16 v[96:99], a[96:99], v[40:43], v[96:99]\n"
"buffer_load_dword v249, v10, s[4:7], s98 offen offset:1536 sc1\n"
"v_mfma_f32_16x16x32_bf16 v[36:39], v[104:107], v[40:43], 0\n"
"buffer_load_dwordx4 v[156:159], v35, s[4:7], s96 offen sc1\n"
"v_mfma_f32_16x16x32_bf16 v[36:39], v[108:111], v[44:47], v[36:39]\n"
"buffer_store_dwordx4 v[228:231], v8, s[4:7], s64 offen offset:1024 sc1\n"
"v_mfma_f32_16x16x32_bf16 v[36:39], v[112:115], v[196:199], v[36:39]\n"
"buffer_load_dword v248, v10, s[4:7], s98 offen offset:1792 sc1\n"
"v_mfma_f32_16x16x32_bf16 v[36:39], v[120:123], v[52:55], v[36:39]\n"
"v_mfma_f32_16x16x32_bf16 v[36:39], v[132:135], v[60:63], v[36:39]\n"
"v_mfma_f32_16x16x32_bf16 v[36:39], v[136:139], v[224:227], v[36:39]\n"
"buffer_load_dwordx4 v[176:179], v35, s[4:7], s96 offen offset:1024 sc1\n"
"v_mfma_f32_16x16x32_bf16 v[36:39], v[140:143], v[172:175], v[36:39]\n"
"ds_write_b128 v3, v[96:99] offset:3072\n"
"v_mfma_f32_16x16x32_bf16 v[36:39], v[100:103], v[28:31], v[36:39]\n"
"v_mfma_f32_16x16x32_bf16 v[96:99], v[116:119], v[28:31], 0\n"
"v_mfma_f32_16x16x32_bf16 v[96:99], v[168:171], v[172:175], v[96:99]\n"
"v_mfma_f32_16x16x32_bf16 v[96:99], v[164:167], v[224:227], v[96:99]\n"
"v_mfma_f32_16x16x32_bf16 v[96:99], v[160:163], v[60:63], v[96:99]\n"
"v_mfma_f32_16x16x32_bf16 v[96:99], v[128:131], v[52:55], v[96:99]\n"
"buffer_load_dwordx4 a[208:211], v1, s[4:7], s97 offen sc1\n"
"v_mfma_f32_16x16x32_bf16 v[96:99], v[152:155], v[196:199], v[96:99]\n"
"s_nop 0\n"
"ds_write_b128 v3, v[36:39] offset:4096\n"
"v_mfma_f32_16x16x32_bf16 v[96:99], v[148:151], v[44:47], v[96:99]\n"
"v_mfma_f32_16x16x32_bf16 v[96:99], v[124:127], v[40:43], v[96:99]\n"
"s_waitcnt vmcnt(26)\n"
"; @A G1 3.1\n"
"v_mfma_f32_16x16x32_bf16 v[28:31], a[0:3], v[88:91], 0\n"
"v_mfma_f32_16x16x32_bf16 v[28:31], a[4:7], v[92:95], v[28:31]\n"
"v_mfma_f32_16x16x32_bf16 v[28:31], a[8:11], v[204:207], v[28:31]\n"
"s_add_u32 s98, s55, 0\n"
"buffer_load_dwordx4 v[36:39], v32, s[4:7], s98 offen sc1\n"
"v_mfma_f32_16x16x32_bf16 v[28:31], a[12:15], v[200:203], v[28:31]\n"
"s_waitcnt vmcnt(21)\n"
"v_mfma_f32_16x16x32_bf16 v[28:31], a[16:19], v[56:59], v[28:31]\n"
"v_mfma_f32_16x16x32_bf16 v[28:31], a[20:23], v[84:87], v[28:31]\n"
"ds_write_b128 v3, v[96:99] offset:5120\n"
"v_mfma_f32_16x16x32_bf16 v[28:31], a[24:27], v[144:147], v[28:31]\n"
"v_mfma_f32_16x16x32_bf16 v[28:31], a[28:31], v[192:195], v[28:31]\n"
"buffer_load_dwordx4 v[40:43], v32, s[4:7], s98 offen offset:1024 sc1\n"
"s_add_u32 s96, s64, 0x4000\n"
"v_mfma_f32_16x16x32_bf16 v[44:47], a[60:63], v[192:195], 0\n"
"v_mfma_f32_16x16x32_bf16 v[44:47], a[56:59], v[144:147], v[44:47]\n"
"buffer_store_dwordx4 v[24:27], v7, s[4:7], s96 offen offset:1024 sc1\n"
"v_mfma_f32_16x16x32_bf16 v[44:47], a[52:55], v[84:87], v[44:47]\n"
"v_mfma_f32_16x16x32_bf16 v[44:47], a[48:51], v[56:59], v[44:47]\n"
"v_mfma_f32_16x16x32_bf16 v[44:47], a[44:47], v[200:203], v[44:47]\n"
"buffer_load_dwordx4 v[24:27], v33, s[4:7], s98 offen sc1\n"
"v_mfma_f32_16x16x32_bf16 v[44:47], a[40:43], v[204:207], v[44:47]\n"
"ds_write_b128 v3, v[28:31] offset:6144\n"
"v_mfma_f32_16x16x32_bf16 v[44:47], a[36:39], v[92:95], v[44:47]\n"
"buffer_load_dwordx4 a[212:215], v1, s[4:7], s97 offen offset:1024 sc1\n"
"v_mfma_f32_16x16x32_bf16 v[44:47], a[32:35], v[88:91], v[44:47]\n"
"s_mov_b32 s20, 0xff0000\n"
"s_mov_b32 s21, 0xffffffff\n"
"s_mov_b32 s22, 0x1ff0001\n"
"v_mfma_f32_16x16x32_bf16 v[28:31], a[64:67], v[88:91], 0\n"
"s_mov_b32 s23, 0xffffffff\n"
"s_mov_b32 s24, 0x3ff0003\n"
"s_mov_b32 s25, 0xffffffff\n"
"v_mfma_f32_16x16x32_bf16 v[28:31], a[68:71], v[92:95], v[28:31]\n"
"buffer_load_dwordx4 v[52:55], v33, s[4:7], s98 offen offset:1024 sc1\n"
"v_mfma_f32_16x16x32_bf16 v[28:31], a[72:75], v[204:207], v[28:31]\n"
"s_mov_b32 s26, 0x7ff0007\n"
"s_mov_b32 s27, 0xffffffff\n"
"s_mov_b32 s28, 0xfff000f\n"
"v_mfma_f32_16x16x32_bf16 v[28:31], a[76:79], v[200:203], v[28:31]\n"
"s_mov_b32 s29, 0xffffffff\n"
"s_mov_b32 s30, 0x1fff001f\n"
"s_mov_b32 s31, 0xffffffff\n"
"v_mfma_f32_16x16x32_bf16 v[28:31], a[80:83], v[56:59], v[28:31]\n"
"s_mov_b32 s32, 0x3fff003f\n"
"s_mov_b32 s33, 0xffffffff\n"
"s_mov_b32 s34, 0x7fff007f\n"
"v_mfma_f32_16x16x32_bf16 v[28:31], a[84:87], v[84:87], v[28:31]\n"
"ds_write_b128 v3, v[44:47] offset:7168\n"
"v_mfma_f32_16x16x32_bf16 v[28:31], a[88:91], v[144:147], v[28:31]\n"
"buffer_load_dwordx4 v[44:47], v34, s[4:7], s98 offen sc1\n"
"v_mfma_f32_16x16x32_bf16 v[28:31], a[92:95], v[192:195], v[28:31]\n"
"s_mov_b32 s35, 0xffffffff\n"
"s_mov_b32 s36, 0\n"
"s_mov_b32 s37, 0xff0000\n"
"v_mfma_f32_16x16x32_bf16 v[60:63], a[124:127], v[192:195], 0\n"
"s_mov_b32 s38, 0\n"
"s_mov_b32 s39, 0x1ff0001\n"
"s_mov_b32 s40, 0\n"
"v_mfma_f32_16x16x32_bf16 v[60:63], a[120:123], v[144:147], v[60:63]\n"
"s_mov_b32 s41, 0x3ff0003\n"
"s_mov_b32 s42, 0\n"
"s_mov_b32 s43, 0x7ff0007\n"
"v_mfma_f32_16x16x32_bf16 v[60:63], a[116:119], v[84:87], v[60:63]\n"
"s_mov_b32 s44, 0\n"
"s_mov_b32 s45, 0xfff000f\n"
"s_mov_b32 s46, 0\n"
"v_mfma_f32_16x16x32_bf16 v[60:63], a[112:115], v[56:59], v[60:63]\n"
"buffer_load_dwordx4 v[96:99], v34, s[4:7], s98 offen offset:1024 sc1\n"
"v_mfma_f32_16x16x32_bf16 v[60:63], a[108:111], v[200:203], v[60:63]\n"
"s_mov_b32 s47, 0x1fff001f\n"
"s_mov_b32 s48, 0\n"
"s_mov_b32 s49, 0x3fff003f\n"
"v_mfma_f32_16x16x32_bf16 v[60:63], a[104:107], v[204:207], v[60:63]\n"
"ds_write_b128 v3, v[28:31] offset:8192\n"
"v_mfma_f32_16x16x32_bf16 v[60:63], a[100:103], v[92:95], v[60:63]\n"
"s_mov_b32 s50, 0\n"
"s_mov_b32 s51, 0x7fff007f\n"
"v_mfma_f32_16x16x32_bf16 v[60:63], a[96:99], v[88:91], v[60:63]\n"
"v_mfma_f32_16x16x32_bf16 v[28:31], v[104:107], v[88:91], 0\n"
"buffer_load_dwordx4 v[172:175], v35, s[4:7], s98 offen sc1\n"
"v_mfma_f32_16x16x32_bf16 v[28:31], v[108:111], v[92:95], v[28:31]\n"
"buffer_load_dwordx4 a[216:219], v1, s[4:7], s97 offen offset:2048 sc1\n"
"v_mfma_f32_16x16x32_bf16 v[28:31], v[112:115], v[204:207], v[28:31]\n"
"v_mfma_f32_16x16x32_bf16 v[28:31], v[120:123], v[200:203], v[28:31]\n"
"v_mfma_f32_16x16x32_bf16 v[28:31], v[132:135], v[56:59], v[28:31]\n"
"s_waitcnt vmcnt(12)\n"
"v_add_f32_e32 v255, v255, v254\n"
"v_add_f32_e32 v253, v253, v250\n"
"v_mfma_f32_16x16x32_bf16 v[28:31], v[136:139], v[84:87], v[28:31]\n"
"buffer_load_dwordx4 v[180:183], v35, s[4:7], s98 offen offset:1024 sc1\n"
"v_mfma_f32_16x16x32_bf16 v[28:31], v[140:143], v[144:147], v[28:31]\n"
"ds_write_b128 v3, v[60:63] offset:9216\n"
"v_mfma_f32_16x16x32_bf16 v[28:31], v[100:103], v[192:195], v[28:31]\n"
"s_add_u32 s98, s75, 0x1000\n"
"buffer_load_dwordx2 v[246:247], v13, s[4:7], s98 offen sc1\n"
"v_mfma_f32_16x16x32_bf16 v[32:35], v[116:119], v[192:195], 0\n"
"v_add_f32_e32 v252, v252, v249\n"
"v_add_f32_e32 v251, v251, v248\n"
"v_mfma_f32_16x16x32_bf16 v[32:35], v[168:171], v[144:147], v[32:35]\n"
"v_add_f32_e32 v255, v255, v252\n"
"v_add_f32_e32 v253, v253, v251\n"
"v_mfma_f32_16x16x32_bf16 v[32:35], v[164:167], v[84:87], v[32:35]\n"
"s_add_u32 s96, s71, 0\n"
"buffer_load_dwordx2 v[250:251], off, s[4:7], s96 sc1\n"
"v_mfma_f32_16x16x32_bf16 v[32:35], v[160:163], v[56:59], v[32:35]\n"
"v_add_f32_e32 v255, v255, v253\n"
"v_mov_b32_e32 v253, v255\n"
"v_mfma_f32_16x16x32_bf16 v[32:35], v[128:131], v[200:203], v[32:35]\n"
"s_nop 0\n"
"v_permlane32_swap_b32_e32 v255, v253\n"
"v_add_f32_e32 v255, v255, v253\n"
"v_mfma_f32_16x16x32_bf16 v[32:35], v[152:155], v[204:207], v[32:35]\n"
"ds_write_b128 v3, v[28:31] offset:10240\n"
"v_mfma_f32_16x16x32_bf16 v[32:35], v[148:151], v[92:95], v[32:35]\n"
"buffer_store_dwordx2 v[18:19], v11, s[4:7], s68 offen offset:32 sc1\n"
"v_mul_f32_e32 v255, 0x3a800000, v255\n"
"v_mov_b32_e32 v253, v255\n"
"v_mfma_f32_16x16x32_bf16 v[32:35], v[124:127], v[88:91], v[32:35]\n"
"s_nop 0\n"
"v_permlane16_swap_b32_e32 v255, v253\n"
"v_cvt_pk_bf16_f32 v254, -v255, 0\n"
"; @A G1 3.2\n"
"v_mfma_f32_16x16x32_bf16 v[28:31], a[0:3], v[64:67], 0\n"
"v_fma_f32 v253, -v255, v255, v253\n"
"v_lshlrev_b32_e32 v254, 16, v254\n"
"v_mfma_f32_16x16x32_bf16 v[28:31], a[4:7], v[68:71], v[28:31]\n"
"v_max_f32_e32 v253, 0, v253\n"
"v_cvt_pk_bf16_f32 v253, v253, 0\n"
"v_mfma_f32_16x16x32_bf16 v[28:31], a[8:11], v[76:79], v[28:31]\n"
"buffer_load_dwordx4 a[220:223], v1, s[4:7], s97 offen offset:3072 sc1\n"
"v_mfma_f32_16x16x32_bf16 v[28:31], a[12:15], v[80:83], v[28:31]\n"
"s_waitcnt vmcnt(16)\n"
"v_lshlrev_b32_e32 v253, 16, v253\n"
"v_add_f32_e32 v253, 0x3727c5ac, v253\n"
"v_mfma_f32_16x16x32_bf16 v[28:31], a[16:19], v[72:75], v[28:31]\n"
"s_add_u32 s97, s70, 0x6000\n"
"v_cvt_pk_bf16_f32 v253, v253, 0\n"
"v_lshlrev_b32_e32 v253, 16, v253\n"
"v_mfma_f32_16x16x32_bf16 v[28:31], a[20:23], v[48:51], v[28:31]\n"
"ds_write_b128 v3, v[32:35] offset:11264\n"
"v_mfma_f32_16x16x32_bf16 v[28:31], a[24:27], v[156:159], v[28:31]\n"
"buffer_load_dwordx4 a[224:227], v0, s[4:7], s97 offen sc1\n"
"v_mfma_f32_16x16x32_bf16 v[28:31], a[28:31], v[176:179], v[28:31]\n"
"v_sqrt_f32_e32 v253, v253\n"
"v_mfma_f32_16x16x32_bf16 v[32:35], a[60:63], v[176:179], 0\n"
"v_cvt_pk_bf16_f32 v253, v253, 0\n"
"v_lshlrev_b32_e32 v253, 16, v253\n"
"v_mfma_f32_16x16x32_bf16 v[32:35], a[56:59], v[156:159], v[32:35]\n"
"v_rcp_f32_e32 v255, v253\n"
"v_mfma_f32_16x16x32_bf16 v[32:35], a[52:55], v[48:51], v[32:35]\n"
"s_waitcnt lgkmcnt(0)\n"
"s_barrier\n"
"v_mfma_f32_16x16x32_bf16 v[32:35], a[48:51], v[72:75], v[32:35]\n"
"buffer_load_dwordx4 a[228:231], v0, s[4:7], s97 offen offset:1024 sc1\n"
"v_mfma_f32_16x16x32_bf16 v[32:35], a[44:47], v[80:83], v[32:35]\n"
"ds_read_b128 v[56:59], v4\n"
"v_mul_f32_e32 v253, 0x3e8293ee, v255\n"
"v_mfma_f32_16x16x32_bf16 v[32:35], a[40:43], v[76:79], v[32:35]\n"
"ds_write_b128 v3, v[28:31] offset:12288\n"
"v_mfma_f32_16x16x32_bf16 v[32:35], a[36:39], v[68:71], v[32:35]\n"
"ds_read_b128 v[28:31], v9 offset:128\n"
"ds_read_b128 v[60:63], v5\n"
"v_mfma_f32_16x16x32_bf16 v[32:35], a[32:35], v[64:67], v[32:35]\n"
"buffer_load_dwordx4 a[232:235], v0, s[4:7], s97 offen offset:2048 sc1\n"
"v_mfma_f32_16x16x32_bf16 v[84:87], a[64:67], v[64:67], 0\n"
"v_mfma_f32_16x16x32_bf16 v[84:87], a[68:71], v[68:71], v[84:87]\n"
"v_mfma_f32_16x16x32_bf16 v[84:87], a[72:75], v[76:79], v[84:87]\n"
"ds_read_b128 v[88:91], v4 offset:1024\n"
"ds_read_b128 v[92:95], v9 offset:192\n"
"v_mfma_f32_16x16x32_bf16 v[84:87], a[76:79], v[80:83], v[84:87]\n"
"buffer_load_dwordx4 a[236:239], v0, s[4:7], s97 offen offset:3072 sc1\n"
"v_mfma_f32_16x16x32_bf16 v[84:87], a[80:83], v[72:75], v[84:87]\n"
"ds_read_b128 v[144:147], v5 offset:1024\n"
"s_waitcnt lgkmcnt(3)\n"
"v_mfma_f32_16x16x32_bf16 v[84:87], a[84:87], v[48:51], v[84:87]\n"
"ds_write_b128 v3, v[32:35] offset:13312\n"
"v_mfma_f32_16x16x32_bf16 v[84:87], a[88:91], v[156:159], v[84:87]\n"
"v_fmac_f32_e32 v56, v254, v28\n"
"v_fmac_f32_e32 v57, v254, v29\n"
"v_mfma_f32_16x16x32_bf16 v[84:87], a[92:95], v[176:179], v[84:87]\n"
"buffer_load_dwordx4 a[240:243], v1, s[4:7], s97 offen sc1\n"
"v_mfma_f32_16x16x32_bf16 v[32:35], a[124:127], v[176:179], 0\n"
"v_fmac_f32_e32 v58, v254, v30\n"
"v_fmac_f32_e32 v59, v254, v31\n"
"v_mfma_f32_16x16x32_bf16 v[32:35], a[120:123], v[156:159], v[32:35]\n"
"ds_read_b128 v[28:31], v4 offset:2048\n"
"ds_read_b128 v[184:187], v9 offset:256\n"
"v_mfma_f32_16x16x32_bf16 v[32:35], a[116:119], v[48:51], v[32:35]\n"
"ds_read_b128 v[188:191], v5 offset:2048\n"
"v_add_f32_e32 v56, v56, v60\n"
"v_mfma_f32_16x16x32_bf16 v[32:35], a[112:115], v[72:75], v[32:35]\n"
"buffer_load_dwordx4 a[244:247], v1, s[4:7], s97 offen offset:1024 sc1\n"
"v_mfma_f32_16x16x32_bf16 v[32:35], a[108:111], v[80:83], v[32:35]\n"
"v_add_f32_e32 v57, v57, v61\n"
"v_add_f32_e32 v58, v58, v62\n"
"v_mfma_f32_16x16x32_bf16 v[32:35], a[104:107], v[76:79], v[32:35]\n"
"ds_write_b128 v3, v[84:87] offset:14336\n"
"v_mfma_f32_16x16x32_bf16 v[32:35], a[100:103], v[68:71], v[32:35]\n"
"s_waitcnt lgkmcnt(5)\n"
"v_add_f32_e32 v59, v59, v63\n"
"v_fmac_f32_e32 v88, v254, v92\n"
"v_mfma_f32_16x16x32_bf16 v[32:35], a[96:99], v[64:67], v[32:35]\n"
"buffer_load_dwordx4 a[248:251], v1, s[4:7], s97 offen offset:2048 sc1\n"
"v_mfma_f32_16x16x32_bf16 v[60:63], v[104:107], v[64:67], 0\n"
"ds_read_b128 v[84:87], v4 offset:3072\n"
"ds_read_b128 v[192:195], v9 offset:320\n"
"v_mfma_f32_16x16x32_bf16 v[60:63], v[108:111], v[68:71], v[60:63]\n"
"ds_read_b128 v[196:199], v5 offset:3072\n"
"v_fmac_f32_e32 v89, v254, v93\n"
"v_mfma_f32_16x16x32_bf16 v[60:63], v[112:115], v[76:79], v[60:63]\n"
"v_fmac_f32_e32 v90, v254, v94\n"
"v_fmac_f32_e32 v91, v254, v95\n"
"v_mfma_f32_16x16x32_bf16 v[60:63], v[120:123], v[80:83], v[60:63]\n"
"buffer_load_dwordx4 a[252:255], v1, s[4:7], s97 offen offset:3072 sc1\n"
"v_mfma_f32_16x16x32_bf16 v[60:63], v[132:135], v[72:75], v[60:63]\n"
"s_waitcnt vmcnt(11)\n"
"v_cmp_ne_u64_e32 vcc, v[246:247], v[18:19]\n"
"v_mfma_f32_16x16x32_bf16 v[60:63], v[136:139], v[48:51], v[60:63]\n"
"ds_write_b128 v3, v[32:35] offset:15360\n"
"v_mfma_f32_16x16x32_bf16 v[60:63], v[140:143], v[156:159], v[60:63]\n"
"s_cbranch_vccnz .Lmk_f7s\n"
".Lmk_f7r:\n"
"v_add_f32_e32 v88, v88, v144\n"
"v_mfma_f32_16x16x32_bf16 v[60:63], v[100:103], v[176:179], v[60:63]\n"
"ds_read_b128 v[32:35], v4 offset:4096\n"
"ds_read_b128 v[92:95], v9\n"
"v_mfma_f32_16x16x32_bf16 v[200:203], v[116:119], v[176:179], 0\n"
"ds_read_b128 v[176:179], v5 offset:4096\n"
"v_add_f32_e32 v89, v89, v145\n"
"v_mfma_f32_16x16x32_bf16 v[200:203], v[168:171], v[156:159], v[200:203]\n"
"s_waitcnt lgkmcnt(8)\n"
"v_add_f32_e32 v90, v90, v146\n"
"v_add_f32_e32 v91, v91, v147\n"
"v_mfma_f32_16x16x32_bf16 v[200:203], v[164:167], v[48:51], v[200:203]\n"
"v_fmac_f32_e32 v28, v254, v184\n"
"v_fmac_f32_e32 v29, v254, v185\n"
"v_mfma_f32_16x16x32_bf16 v[200:203], v[160:163], v[72:75], v[200:203]\n"
"v_fmac_f32_e32 v30, v254, v186\n"
"v_fmac_f32_e32 v31, v254, v187\n"
"v_mfma_f32_16x16x32_bf16 v[200:203], v[128:131], v[80:83], v[200:203]\n"
"s_waitcnt vmcnt(10)\n"
"v_cmp_ne_u64_e32 vcc, v[250:251], v[18:19]\n"
"v_mfma_f32_16x16x32_bf16 v[200:203], v[152:155], v[76:79], v[200:203]\n"
"ds_write_b128 v3, v[60:63] offset:16384\n"
"v_mfma_f32_16x16x32_bf16 v[200:203], v[148:151], v[68:71], v[200:203]\n"
"s_cbranch_vccnz .Lmk_f4s\n"
".Lmk_f4r:\n"
"s_add_u32 m0, s77, 0x6000\n"
"v_mfma_f32_16x16x32_bf16 v[200:203], v[124:127], v[64:67], v[200:203]\n"
"s_add_u32 s98, s65, 0x2000\n"
"ds_read_b128 v[48:51], v4 offset:5120\n"
"; @A G1 3.3\n"
"v_mfma_f32_16x16x32_bf16 v[56:59], a[0:3], v[36:39], v[56:59]\n"
"buffer_load_dwordx4 v2, s[4:7], s98 offen sc1 lds\n"
"v_mfma_f32_16x16x32_bf16 v[56:59], a[4:7], v[40:43], v[56:59]\n"
"buffer_load_dwordx4 v2, s[4:7], s98 offen offset:1024 sc1 lds\n"
"v_mfma_f32_16x16x32_bf16 v[56:59], a[8:11], v[24:27], v[56:59]\n"
"ds_read_b128 v[60:63], v9 offset:64\n"
"ds_read_b128 v[64:67], v5 offset:5120\n"
"v_mfma_f32_16x16x32_bf16 v[56:59], a[12:15], v[52:55], v[56:59]\n"
"v_add_f32_e32 v28, v28, v188\n"
"v_add_f32_e32 v29, v29, v189\n"
"v_mfma_f32_16x16x32_bf16 v[56:59], a[16:19], v[44:47], v[56:59]\n"
"s_waitcnt lgkmcnt(8)\n"
"v_add_f32_e32 v30, v30, v190\n"
"v_add_f32_e32 v31, v31, v191\n"
"v_mfma_f32_16x16x32_bf16 v[56:59], a[20:23], v[96:99], v[56:59]\n"
"ds_write_b128 v3, v[200:203] offset:17408\n"
"v_mfma_f32_16x16x32_bf16 v[56:59], a[24:27], v[172:175], v[56:59]\n"
"v_fmac_f32_e32 v84, v254, v192\n"
"v_fmac_f32_e32 v85, v254, v193\n"
"v_mfma_f32_16x16x32_bf16 v[56:59], a[28:31], v[180:183], v[56:59]\n"
"s_add_u32 m0, s77, 0xe000\n"
"s_add_u32 s98, s65, 0x6000\n"
"v_fmac_f32_e32 v86, v254, v194\n"
"v_mfma_f32_16x16x32_bf16 v[88:91], a[60:63], v[180:183], v[88:91]\n"
"buffer_load_dwordx4 v2, s[4:7], s98 offen sc1 lds\n"
"v_mfma_f32_16x16x32_bf16 v[88:91], a[56:59], v[172:175], v[88:91]\n"
"buffer_load_dwordx4 v2, s[4:7], s98 offen offset:1024 sc1 lds\n"
"v_mfma_f32_16x16x32_bf16 v[88:91], a[52:55], v[96:99], v[88:91]\n"
"s_waitcnt lgkmcnt(0)\n"
"s_barrier\n"
"v_mfma_f32_16x16x32_bf16 v[88:91], a[48:51], v[44:47], v[88:91]\n"
"ds_read_b128 v[68:71], v6\n"
"ds_read_b128 v[72:75], v6 offset:1024\n"
"v_mfma_f32_16x16x32_bf16 v[88:91], a[44:47], v[52:55], v[88:91]\n"
"s_add_u32 s98, s69, 0x600000\n"
"buffer_load_dwordx4 a[0:3], v0, s[4:7], s98 offen sc1\n"
"v_mfma_f32_16x16x32_bf16 v[88:91], a[40:43], v[24:27], v[88:91]\n"
"v_fmac_f32_e32 v87, v254, v195\n"
"v_add_f32_e32 v84, v84, v196\n"
"v_mfma_f32_16x16x32_bf16 v[88:91], a[36:39], v[40:43], v[88:91]\n"
"v_add_f32_e32 v85, v85, v197\n"
"v_add_f32_e32 v86, v86, v198\n"
"v_mfma_f32_16x16x32_bf16 v[88:91], a[32:35], v[36:39], v[88:91]\n"
"buffer_load_dwordx4 a[4:7], v0, s[4:7], s98 offen offset:1024 sc1\n"
"v_mfma_f32_16x16x32_bf16 v[28:31], a[64:67], v[36:39], v[28:31]\n"
"v_add_f32_e32 v87, v87, v199\n"
"v_fmac_f32_e32 v32, v254, v92\n"
"v_mfma_f32_16x16x32_bf16 v[28:31], a[68:71], v[40:43], v[28:31]\n"
"v_fmac_f32_e32 v33, v254, v93\n"
"v_fmac_f32_e32 v34, v254, v94\n"
"v_mfma_f32_16x16x32_bf16 v[28:31], a[72:75], v[24:27], v[28:31]\n"
"buffer_load_dwordx4 a[8:11], v0, s[4:7], s98 offen offset:2048 sc1\n"
"v_mfma_f32_16x16x32_bf16 v[28:31], a[76:79], v[52:55], v[28:31]\n"
"v_fmac_f32_e32 v35, v254, v95\n"
"v_add_f32_e32 v32, v32, v176\n"
"v_mfma_f32_16x16x32_bf16 v[28:31], a[80:83], v[44:47], v[28:31]\n"
"s_waitcnt lgkmcnt(0)\n"
"v_add_f32_e32 v33, v33, v177\n"
"v_add_f32_e32 v34, v34, v178\n"
"v_mfma_f32_16x16x32_bf16 v[28:31], a[84:87], v[96:99], v[28:31]\n"
"buffer_load_dwordx4 a[12:15], v0, s[4:7], s98 offen offset:3072 sc1\n"
"v_mfma_f32_16x16x32_bf16 v[28:31], a[88:91], v[172:175], v[28:31]\n"
"ds_read_b128 v[76:79], v6 offset:2048\n"
"ds_read_b128 v[80:83], v6 offset:3072\n"
"v_mfma_f32_16x16x32_bf16 v[28:31], a[92:95], v[180:183], v[28:31]\n"
"v_add_f32_e32 v35, v35, v179\n"
"v_fmac_f32_e32 v48, v254, v60\n"
"v_mfma_f32_16x16x32_bf16 v[84:87], a[124:127], v[180:183], v[84:87]\n"
"buffer_load_dwordx4 a[16:19], v1, s[4:7], s98 offen sc1\n"
"v_mfma_f32_16x16x32_bf16 v[84:87], a[120:123], v[172:175], v[84:87]\n"
"v_fmac_f32_e32 v49, v254, v61\n"
"v_fmac_f32_e32 v50, v254, v62\n"
"v_mfma_f32_16x16x32_bf16 v[84:87], a[116:119], v[96:99], v[84:87]\n"
"v_fmac_f32_e32 v51, v254, v63\n"
"v_add_f32_e32 v48, v48, v64\n"
"v_mfma_f32_16x16x32_bf16 v[84:87], a[112:115], v[44:47], v[84:87]\n"
"buffer_load_dwordx4 a[20:23], v1, s[4:7], s98 offen offset:1024 sc1\n"
"v_mfma_f32_16x16x32_bf16 v[84:87], a[108:111], v[52:55], v[84:87]\n"
"v_add_f32_e32 v49, v49, v65\n"
"v_add_f32_e32 v50, v50, v66\n"
"v_mfma_f32_16x16x32_bf16 v[84:87], a[104:107], v[24:27], v[84:87]\n"
"v_add_f32_e32 v51, v51, v67\n"
"v_add_f32_e32 v56, v56, v68\n"
"v_mfma_f32_16x16x32_bf16 v[84:87], a[100:103], v[40:43], v[84:87]\n"
"buffer_load_dwordx4 a[24:27], v1, s[4:7], s98 offen offset:2048 sc1\n"
"v_mfma_f32_16x16x32_bf16 v[84:87], a[96:99], v[36:39], v[84:87]\n"
"v_add_f32_e32 v57, v57, v69\n"
"v_add_f32_e32 v58, v58, v70\n"
"v_mfma_f32_16x16x32_bf16 v[32:35], v[104:107], v[36:39], v[32:35]\n"
"v_add_f32_e32 v59, v59, v71\n"
"v_add_f32_e32 v88, v88, v72\n"
"v_mfma_f32_16x16x32_bf16 v[32:35], v[108:111], v[40:43], v[32:35]\n"
"buffer_load_dwordx4 a[28:31], v1, s[4:7], s98 offen offset:3072 sc1\n"
"v_mfma_f32_16x16x32_bf16 v[32:35], v[112:115], v[24:27], v[32:35]\n"
"v_add_f32_e32 v89, v89, v73\n"
"v_add_f32_e32 v90, v90, v74\n"
"v_mfma_f32_16x16x32_bf16 v[32:35], v[120:123], v[52:55], v[32:35]\n"
"v_add_f32_e32 v91, v91, v75\n"
"v_mul_f32_e32 v56, v56, v255\n"
"v_mfma_f32_16x16x32_bf16 v[32:35], v[132:135], v[44:47], v[32:35]\n"
"s_waitcnt lgkmcnt(0)\n"
"v_mul_f32_e32 v57, v57, v255\n"
"v_mul_f32_e32 v58, v58, v255\n"
"v_mfma_f32_16x16x32_bf16 v[32:35], v[136:139], v[96:99], v[32:35]\n"
"v_mul_f32_e32 v59, v59, v255\n"
"v_mul_f32_e32 v88, v88, v255\n"
"v_mfma_f32_16x16x32_bf16 v[32:35], v[140:143], v[172:175], v[32:35]\n"
"ds_read_b128 v[60:63], v6 offset:4096\n"
"ds_read_b128 v[64:67], v6 offset:5120\n"
"v_mfma_f32_16x16x32_bf16 v[32:35], v[100:103], v[180:183], v[32:35]\n"
"v_mul_f32_e32 v89, v89, v255\n"
"v_mul_f32_e32 v90, v90, v255\n"
"v_mfma_f32_16x16x32_bf16 v[48:51], v[116:119], v[180:183], v[48:51]\n"
"v_mul_f32_e32 v91, v91, v255\n"
"v_cvt_pk_bf16_f32 v68, v56, v57\n"
"v_mfma_f32_16x16x32_bf16 v[48:51], v[168:171], v[172:175], v[48:51]\n"
"v_cvt_pk_bf16_f32 v69, v58, v59\n"
"v_cvt_pk_bf16_f32 v70, v88, v89\n"
"v_mfma_f32_16x16x32_bf16 v[48:51], v[164:167], v[96:99], v[48:51]\n"
"v_cvt_pk_bf16_f32 v71, v90, v91\n"
"v_add_f32_e32 v28, v28, v76\n"
"v_mfma_f32_16x16x32_bf16 v[48:51], v[160:163], v[44:47], v[48:51]\n"
"ds_write_b128 v7, v[68:71] offset:16384\n"
"v_mfma_f32_16x16x32_bf16 v[48:51], v[128:131], v[52:55], v[48:51]\n"
"buffer_store_dwordx4 v[68:71], v7, s[4:7], s64 offen sc1\n"
"v_mfma_f32_16x16x32_bf16 v[48:51], v[152:155], v[24:27], v[48:51]\n"
"v_add_f32_e32 v29, v29, v77\n"
"v_add_f32_e32 v30, v30, v78\n"
"v_mfma_f32_16x16x32_bf16 v[48:51], v[148:151], v[40:43], v[48:51]\n"
"v_add_f32_e32 v31, v31, v79\n"
"v_add_f32_e32 v84, v84, v80\n"
"v_mfma_f32_16x16x32_bf16 v[48:51], v[124:127], v[36:39], v[48:51]\n"
"v_add_f32_e32 v85, v85, v81\n"
"v_add_f32_e32 v86, v86, v82\n"
"v_add_f32_e32 v87, v87, v83\n"
"v_mul_f32_e32 v28, v28, v255\n"
"v_mul_f32_e32 v29, v29, v255\n"
"v_mul_f32_e32 v30, v30, v255\n"
"v_mul_f32_e32 v31, v31, v255\n"
"v_mul_f32_e32 v84, v84, v255\n"
"v_mul_f32_e32 v85, v85, v255\n"
"v_mul_f32_e32 v86, v86, v255\n"
"v_mul_f32_e32 v87, v87, v255\n"
"v_cvt_pk_bf16_f32 v24, v28, v29\n"
"v_cvt_pk_bf16_f32 v25, v30, v31\n"
"v_cvt_pk_bf16_f32 v26, v84, v85\n"
"v_cvt_pk_bf16_f32 v27, v86, v87\n"
"v_xor_b32_e32 v255, 32, v16\n"
"v_xor_b32_e32 v254, 32, v15\n"
"v_mov_b32_e32 v88, 0x3f803f80\n"
"v_mov_b32_e32 v89, 0x3f803f80\n"
"v_mov_b32_e32 v90, 0x3f803f80\n"
"v_mov_b32_e32 v91, 0x3f803f80\n"
"v_mov_b32_e32 v252, 0x53800000\n"
"ds_write_b128 v7, v[24:27] offset:49152\n"
".Lmk_z4:\n"
"; @A tail\n"
"s_add_u32 s98, s64, 0x4000\n"
"buffer_store_dwordx4 v[24:27], v7, s[4:7], s98 offen sc1\n"
"s_waitcnt lgkmcnt(2)\n"
"v_add_f32_e32 v32, v32, v60\n"
"v_add_f32_e32 v33, v33, v61\n"
"v_add_f32_e32 v34, v34, v62\n"
"v_add_f32_e32 v35, v35, v63\n"
"v_add_f32_e32 v48, v48, v64\n"
"v_add_f32_e32 v49, v49, v65\n"
"v_add_f32_e32 v50, v50, v66\n"
"v_add_f32_e32 v51, v51, v67\n"
"v_mul_f32_e32 v32, v32, v253\n"
"v_mul_f32_e32 v33, v33, v253\n"
"v_mul_f32_e32 v34, v34, v253\n"
"v_mul_f32_e32 v35, v35, v253\n"
"v_mul_f32_e32 v48, v48, v253\n"
"v_mul_f32_e32 v49, v49, v253\n"
"v_mul_f32_e32 v50, v50, v253\n"
"v_mul_f32_e32 v51, v51, v253\n"
"v_cvt_pk_bf16_f32 v24, v32, v33\n"
"v_cvt_pk_bf16_f32 v25, v34, v35\n"
"v_cvt_pk_bf16_f32 v26, v48, v49\n"
"v_cvt_pk_bf16_f32 v27, v50, v51\n"
"ds_write_b128 v2, v[24:27] offset:16384\n"
"s_waitcnt lgkmcnt(0)\n"
"s_barrier\n"
"ds_read_b128 v[28:31], v2 offset:49152\n"
"ds_read_b128 v[32:35], v2 offset:50176\n"
"ds_read_b128 v[92:95], v2 offset:16384\n"
"ds_read_b128 v[96:99], v2 offset:17408\n"
"; @A AT b\n"
"s_mov_b32 s2, 0\n"
"v_mov_b32_e32 v253, 0xff800000\n"
"v_mov_b32_e32 v251, 0xff800000\n"
"v_mov_b32_e32 v250, 0x7f800000\n"
"v_mov_b32_e32 v249, 0x7f800000\n"
".Lmk_a15:\n"
"ds_read_b128 v[24:27], v15 offset:24576\n"
"ds_read_b128 v[36:39], v254 offset:24832\n"
"ds_read_b128 v[40:43], v15 offset:22528\n"
"ds_read_b128 v[44:47], v254 offset:22784\n"
"s_cmp_gt_i32 s78, 11\n"
"s_cselect_b32 s98, 0x80000000, 0\n"
"s_or_b32 s98, s98, 0x7f800000\n"
"s_waitcnt lgkmcnt(2)\n"
"v_mfma_f32_16x16x32_bf16 v[48:51], v[24:27], v[28:31], 0\n"
"v_mfma_f32_16x16x32_bf16 v[52:55], v[24:27], v[32:35], 0\n"
"v_mfma_f32_16x16x32_bf16 v[56:59], v[36:39], v[32:35], 0\n"
"v_mfma_f32_16x16x32_bf16 v[60:63], v[36:39], v[28:31], 0\n"
"s_waitcnt lgkmcnt(0)\n"
"s_nop 3\n"
"v_max_f32_e32 v248, v48, v49\n"
"v_max_f32_e32 v247, v52, v53\n"
"v_max3_f32 v248, v248, v50, v51\n"
"v_max3_f32 v247, v247, v54, v55\n"
"v_max3_f32 v248, v248, v60, v61\n"
"v_max3_f32 v247, v247, v56, v57\n"
"v_max3_f32 v248, v248, v62, v63\n"
"v_max3_f32 v247, v247, v58, v59\n"
"v_mov_b32_e32 v246, v248\n"
"v_mov_b32_e32 v245, v247\n"
"s_nop 0\n"
"v_permlane16_swap_b32_e32 v248, v246\n"
"v_permlane16_swap_b32_e32 v247, v245\n"
"v_max_f32_e32 v248, v248, v246\n"
"v_max_f32_e32 v247, v247, v245\n"
"v_mov_b32_e32 v246, v248\n"
"v_mov_b32_e32 v245, v247\n"
"s_nop 0\n"
"v_permlane32_swap_b32_e32 v248, v246\n"
"v_permlane32_swap_b32_e32 v247, v245\n"
"v_max_f32_e32 v248, v248, v246\n"
"v_max_f32_e32 v247, v247, v245\n"
"v_med3_f32 v248, v248, v253, v250\n"
"v_med3_f32 v247, v247, v251, v249\n"
"v_min_f32_e64 v24, s98, -v248\n"
"v_min_f32_e64 v25, s98, -v248\n"
"v_min_f32_e64 v26, s98, -v248\n"
"v_min_f32_e64 v27, s98, -v248\n"
"v_min_f32_e64 v36, s98, -v247\n"
"v_min_f32_e64 v37, s98, -v247\n"
"v_min_f32_e64 v38, s98, -v247\n"
"v_min_f32_e64 v39, s98, -v247\n"
"v_mfma_f32_16x16x32_bf16 v[64:67], v[40:43], v[28:31], v[24:27]\n"
"ds_read_b128 v[68:71], v15 offset:20480\n"
"s_add_u32 s98, s69, 0x602000\n"
"buffer_load_dwordx4 a[32:35], v0, s[4:7], s98 offen sc1\n"
"v_sub_f32_e32 v48, v48, v248\n"
"v_sub_f32_e32 v49, v49, v248\n"
"v_sub_f32_e32 v50, v50, v248\n"
"v_mfma_f32_16x16x32_bf16 v[72:75], v[40:43], v[32:35], v[36:39]\n"
"buffer_load_dwordx4 a[36:39], v0, s[4:7], s98 offen offset:1024 sc1\n"
"v_sub_f32_e32 v51, v51, v248\n"
"v_sub_f32_e32 v60, v60, v248\n"
"v_sub_f32_e32 v61, v61, v248\n"
"v_sub_f32_e32 v62, v62, v248\n"
"v_sub_f32_e32 v63, v63, v248\n"
"v_mfma_f32_16x16x32_bf16 v[40:43], v[44:47], v[32:35], v[36:39]\n"
"ds_read_b128 v[76:79], v254 offset:20736\n"
"v_sub_f32_e32 v52, v52, v247\n"
"v_sub_f32_e32 v53, v53, v247\n"
"v_sub_f32_e32 v54, v54, v247\n"
"v_sub_f32_e32 v55, v55, v247\n"
"v_mfma_f32_16x16x32_bf16 v[80:83], v[44:47], v[28:31], v[24:27]\n"
"buffer_load_dwordx4 a[40:43], v0, s[4:7], s98 offen offset:2048 sc1\n"
"v_sub_f32_e32 v56, v56, v247\n"
"v_sub_f32_e32 v57, v57, v247\n"
"v_sub_f32_e32 v58, v58, v247\n"
"v_sub_f32_e32 v59, v59, v247\n"
"s_waitcnt lgkmcnt(0)\n"
"v_mfma_f32_16x16x32_bf16 v[44:47], v[68:71], v[28:31], v[24:27]\n"
"ds_read_b128 v[84:87], v15 offset:18432\n"
"buffer_load_dwordx4 a[44:47], v0, s[4:7], s98 offen offset:3072 sc1\n"
"ds_read_b128 v[100:103], v254 offset:18688\n"
"v_exp_f32_e32 v64, v64\n"
"v_exp_f32_e32 v65, v65\n"
"v_exp_f32_e32 v66, v66\n"
"v_exp_f32_e32 v67, v67\n"
"v_exp_f32_e32 v80, v80\n"
"v_exp_f32_e32 v81, v81\n"
"v_mfma_f32_16x16x32_bf16 v[104:107], v[68:71], v[32:35], v[36:39]\n"
"buffer_load_dwordx4 a[48:51], v1, s[4:7], s98 offen sc1\n"
"ds_read_b64_tr_b16 v[68:69], v16 offset:22528\n"
"buffer_load_dwordx4 a[52:55], v1, s[4:7], s98 offen offset:1024 sc1\n"
"ds_read_b64_tr_b16 v[70:71], v255 offset:22784\n"
"v_exp_f32_e32 v82, v82\n"
"v_exp_f32_e32 v83, v83\n"
"v_cvt_pk_bf16_f32 v108, v64, v65\n"
"v_cvt_pk_bf16_f32 v109, v66, v67\n"
"v_cvt_pk_bf16_f32 v110, v80, v81\n"
"v_cvt_pk_bf16_f32 v111, v82, v83\n"
"v_exp_f32_e32 v72, v72\n"
"v_mfma_f32_16x16x32_bf16 v[64:67], v[76:79], v[32:35], v[36:39]\n"
"s_add_u32 s97, s76, 0x80000\n"
"v_exp_f32_e32 v73, v73\n"
"v_exp_f32_e32 v74, v74\n"
"v_exp_f32_e32 v75, v75\n"
"v_bfe_u32 v248, v0, 2, 7\n"
"v_add_u32_e32 v248, s72, v248\n"
"v_and_b32_e32 v247, 0xffffffc0, v248\n"
"v_add_u32_e32 v248, v248, v247\n"
"v_and_b32_e32 v247, 0x200, v0\n"
"v_lshl_add_u32 v248, v247, 4, v248\n"
"v_mfma_f32_16x16x32_bf16 v[112:115], v[76:79], v[28:31], v[24:27]\n"
"buffer_load_dword v247, v248, s[4:7], s97 offen sc1\n"
"ds_read_b64_tr_b16 v[80:81], v255 offset:22528\n"
"buffer_load_dword v246, v248, s[4:7], s97 offen offset:1024 sc1\n"
"ds_read_b64_tr_b16 v[82:83], v16 offset:22784\n"
"v_exp_f32_e32 v40, v40\n"
"v_exp_f32_e32 v41, v41\n"
"v_exp_f32_e32 v42, v42\n"
"v_exp_f32_e32 v43, v43\n"
"v_cvt_pk_bf16_f32 v76, v72, v73\n"
"v_cvt_pk_bf16_f32 v77, v74, v75\n"
"v_cvt_pk_bf16_f32 v78, v40, v41\n"
"v_cvt_pk_bf16_f32 v79, v42, v43\n"
"s_waitcnt lgkmcnt(4)\n"
"v_mfma_f32_16x16x32_bf16 v[40:43], v[84:87], v[28:31], v[24:27]\n"
"ds_read_b128 v[72:75], v15 offset:16384\n"
"v_exp_f32_e32 v44, v44\n"
"v_exp_f32_e32 v45, v45\n"
"v_exp_f32_e32 v46, v46\n"
"v_mfma_f32_16x16x32_bf16 v[116:119], v[84:87], v[32:35], v[36:39]\n"
"buffer_load_dwordx4 a[56:59], v1, s[4:7], s98 offen offset:2048 sc1\n"
"ds_read_b128 v[84:87], v254 offset:16640\n"
"v_exp_f32_e32 v47, v47\n"
"v_exp_f32_e32 v112, v112\n"
"v_mfma_f32_16x16x32_bf16 v[120:123], v[100:103], v[32:35], v[36:39]\n"
"ds_read_b64_tr_b16 v[124:125], v16 offset:20480\n"
"v_exp_f32_e32 v113, v113\n"
"v_exp_f32_e32 v114, v114\n"
"v_mfma_f32_16x16x32_bf16 v[128:131], v[100:103], v[28:31], v[24:27]\n"
"buffer_load_dwordx4 a[60:63], v1, s[4:7], s98 offen offset:3072 sc1\n"
"v_exp_f32_e32 v115, v115\n"
"v_cvt_pk_bf16_f32 v100, v44, v45\n"
"v_cvt_pk_bf16_f32 v101, v46, v47\n"
"s_waitcnt lgkmcnt(3)\n"
"v_mfma_f32_16x16x32_bf16 v[44:47], v[88:91], v[108:111], 0\n"
"ds_read_b64_tr_b16 v[126:127], v255 offset:20736\n"
"s_add_u32 s98, s69, 0x604000\n"
"buffer_load_dwordx4 a[64:67], v0, s[4:7], s98 offen sc1\n"
"v_cvt_pk_bf16_f32 v102, v112, v113\n"
"v_cvt_pk_bf16_f32 v103, v114, v115\n"
"v_exp_f32_e32 v104, v104\n"
"v_mfma_f32_16x16x32_bf16 v[112:115], v[68:71], v[108:111], 0\n"
"ds_read_b64_tr_b16 v[132:133], v255 offset:20480\n"
"v_exp_f32_e32 v105, v105\n"
"v_exp_f32_e32 v106, v106\n"
"v_mfma_f32_16x16x32_bf16 v[136:139], v[80:83], v[108:111], 0\n"
"buffer_load_dword v245, v248, s[4:7], s97 offen offset:2048 sc1\n"
"v_exp_f32_e32 v107, v107\n"
"v_exp_f32_e32 v64, v64\n"
"v_mfma_f32_16x16x32_bf16 v[108:111], v[80:83], v[76:79], 0\n"
"ds_read_b64_tr_b16 v[134:135], v16 offset:20736\n"
"v_exp_f32_e32 v65, v65\n"
"v_exp_f32_e32 v66, v66\n"
"v_mfma_f32_16x16x32_bf16 v[80:83], v[68:71], v[76:79], 0\n"
"buffer_load_dword v244, v248, s[4:7], s97 offen offset:3072 sc1\n"
"s_waitcnt vmcnt(13)\n"
"buffer_store_dwordx2 v[18:19], v11, s[4:7], s68 offen offset:64 sc1\n"
"v_exp_f32_e32 v67, v67\n"
"v_mfma_f32_16x16x32_bf16 v[68:71], v[88:91], v[76:79], 0\n"
"s_cmp_lg_u32 s2, 0\n"
"v_cvt_pk_bf16_f32 v76, v104, v105\n"
"v_cvt_pk_bf16_f32 v77, v106, v107\n"
"v_cvt_pk_bf16_f32 v78, v64, v65\n"
"v_cvt_pk_bf16_f32 v79, v66, v67\n"
"s_cbranch_scc1 .Lmk_s17\n"
"s_barrier\n"
".Lmk_s17:\n"
"s_waitcnt lgkmcnt(4)\n"
"v_mfma_f32_16x16x32_bf16 v[64:67], v[72:75], v[28:31], v[24:27]\n"
"ds_read_b128 v[104:107], v15 offset:14336\n"
"v_exp_f32_e32 v40, v40\n"
"v_exp_f32_e32 v41, v41\n"
"v_exp_f32_e32 v42, v42\n"
"v_mfma_f32_16x16x32_bf16 v[140:143], v[72:75], v[32:35], v[36:39]\n"
"buffer_load_dwordx4 a[68:71], v0, s[4:7], s98 offen offset:1024 sc1\n"
"ds_read_b128 v[72:75], v254 offset:14592\n"
"v_exp_f32_e32 v43, v43\n"
"v_mfma_f32_16x16x32_bf16 v[144:147], v[84:87], v[32:35], v[36:39]\n"
"v_exp_f32_e32 v128, v128\n"
"v_exp_f32_e32 v129, v129\n"
"v_exp_f32_e32 v130, v130\n"
"v_mfma_f32_16x16x32_bf16 v[148:151], v[84:87], v[28:31], v[24:27]\n"
"buffer_load_dwordx4 a[72:75], v0, s[4:7], s98 offen offset:2048 sc1\n"
"ds_read_b64_tr_b16 v[84:85], v16 offset:18432\n"
"v_exp_f32_e32 v131, v131\n"
"s_waitcnt lgkmcnt(3)\n"
"v_mfma_f32_16x16x32_bf16 v[44:47], v[88:91], v[100:103], v[44:47]\n"
"buffer_load_dwordx4 a[76:79], v0, s[4:7], s98 offen offset:3072 sc1\n"
"v_cvt_pk_bf16_f32 v152, v40, v41\n"
"v_cvt_pk_bf16_f32 v153, v42, v43\n"
"v_cvt_pk_bf16_f32 v154, v128, v129\n"
"v_cvt_pk_bf16_f32 v155, v130, v131\n"
"v_mfma_f32_16x16x32_bf16 v[112:115], v[124:127], v[100:103], v[112:115]\n"
"ds_read_b64_tr_b16 v[86:87], v255 offset:18688\n"
"v_exp_f32_e32 v116, v116\n"
"v_exp_f32_e32 v117, v117\n"
"v_exp_f32_e32 v118, v118\n"
"v_mfma_f32_16x16x32_bf16 v[136:139], v[132:135], v[100:103], v[136:139]\n"
"s_add_u32 s97, s76, 0x81000\n"
"buffer_load_dword v243, v248, s[4:7], s97 offen sc1\n"
"v_exp_f32_e32 v119, v119\n"
"v_mfma_f32_16x16x32_bf16 v[108:111], v[132:135], v[76:79], v[108:111]\n"
"ds_read_b64_tr_b16 v[40:41], v255 offset:18432\n"
"v_exp_f32_e32 v120, v120\n"
"v_exp_f32_e32 v121, v121\n"
"v_mfma_f32_16x16x32_bf16 v[80:83], v[124:127], v[76:79], v[80:83]\n"
"buffer_load_dword v242, v248, s[4:7], s97 offen offset:1024 sc1\n"
"ds_read_b64_tr_b16 v[42:43], v16 offset:18688\n"
"v_exp_f32_e32 v122, v122\n"
"v_exp_f32_e32 v123, v123\n"
"v_mfma_f32_16x16x32_bf16 v[68:71], v[88:91], v[76:79], v[68:71]\n"
"s_waitcnt lgkmcnt(4)\n"
"v_cvt_pk_bf16_f32 v76, v116, v117\n"
"v_cvt_pk_bf16_f32 v77, v118, v119\n"
"v_cvt_pk_bf16_f32 v78, v120, v121\n"
"v_cvt_pk_bf16_f32 v79, v122, v123\n"
"s_cmp_eq_i32 s78, 8\n"
"s_cbranch_scc1 .Lmk_x18s\n"
".Lmk_x18r:\n"
"v_mfma_f32_16x16x32_bf16 v[100:103], v[104:107], v[28:31], v[24:27]\n"
"ds_read_b128 v[116:119], v15 offset:12288\n"
"v_exp_f32_e32 v64, v64\n"
"v_exp_f32_e32 v65, v65\n"
"v_exp_f32_e32 v66, v66\n"
"v_mfma_f32_16x16x32_bf16 v[120:123], v[104:107], v[32:35], v[36:39]\n"
"buffer_load_dwordx4 a[80:83], v1, s[4:7], s98 offen sc1\n"
"ds_read_b128 v[104:107], v254 offset:12544\n"
"v_exp_f32_e32 v67, v67\n"
"v_mfma_f32_16x16x32_bf16 v[124:127], v[72:75], v[32:35], v[36:39]\n"
"v_exp_f32_e32 v148, v148\n"
"v_exp_f32_e32 v149, v149\n"
"v_exp_f32_e32 v150, v150\n"
"v_mfma_f32_16x16x32_bf16 v[128:131], v[72:75], v[28:31], v[24:27]\n"
"buffer_load_dwordx4 a[84:87], v1, s[4:7], s98 offen offset:1024 sc1\n"
"ds_read_b64_tr_b16 v[72:73], v16 offset:16384\n"
"v_exp_f32_e32 v151, v151\n"
"s_waitcnt lgkmcnt(3)\n"
"v_mfma_f32_16x16x32_bf16 v[44:47], v[88:91], v[152:155], v[44:47]\n"
"buffer_load_dwordx4 a[88:91], v1, s[4:7], s98 offen offset:2048 sc1\n"
"v_cvt_pk_bf16_f32 v132, v64, v65\n"
"v_cvt_pk_bf16_f32 v133, v66, v67\n"
"v_cvt_pk_bf16_f32 v134, v148, v149\n"
"v_cvt_pk_bf16_f32 v135, v150, v151\n"
"v_mfma_f32_16x16x32_bf16 v[112:115], v[84:87], v[152:155], v[112:115]\n"
"ds_read_b64_tr_b16 v[74:75], v255 offset:16640\n"
"v_exp_f32_e32 v140, v140\n"
"v_exp_f32_e32 v141, v141\n"
"v_mfma_f32_16x16x32_bf16 v[136:139], v[40:43], v[152:155], v[136:139]\n"
"buffer_load_dword v241, v248, s[4:7], s97 offen offset:2048 sc1\n"
"v_exp_f32_e32 v142, v142\n"
"v_exp_f32_e32 v143, v143\n"
"v_mfma_f32_16x16x32_bf16 v[108:111], v[40:43], v[76:79], v[108:111]\n"
"ds_read_b64_tr_b16 v[40:41], v255 offset:16384\n"
"v_exp_f32_e32 v144, v144\n"
"v_exp_f32_e32 v145, v145\n"
"v_mfma_f32_16x16x32_bf16 v[80:83], v[84:87], v[76:79], v[80:83]\n"
"buffer_load_dword v240, v248, s[4:7], s97 offen offset:3072 sc1\n"
"ds_read_b64_tr_b16 v[42:43], v16 offset:16640\n"
"v_exp_f32_e32 v146, v146\n"
"v_exp_f32_e32 v147, v147\n"
"v_mfma_f32_16x16x32_bf16 v[68:71], v[88:91], v[76:79], v[68:71]\n"
"s_waitcnt lgkmcnt(4)\n"
"v_cvt_pk_bf16_f32 v64, v140, v141\n"
"v_cvt_pk_bf16_f32 v65, v142, v143\n"
"v_cvt_pk_bf16_f32 v66, v144, v145\n"
"v_cvt_pk_bf16_f32 v67, v146, v147\n"
"s_cmp_eq_i32 s78, 7\n"
"s_cbranch_scc1 .Lmk_x19s\n"
".Lmk_x19r:\n"
"v_mfma_f32_16x16x32_bf16 v[76:79], v[116:119], v[28:31], v[24:27]\n"
"ds_read_b128 v[84:87], v15 offset:10240\n"
"v_exp_f32_e32 v100, v100\n"
"v_exp_f32_e32 v101, v101\n"
"v_exp_f32_e32 v102, v102\n"
"v_mfma_f32_16x16x32_bf16 v[140:143], v[116:119], v[32:35], v[36:39]\n"
"buffer_load_dwordx4 a[92:95], v1, s[4:7], s98 offen offset:3072 sc1\n"
"v_exp_f32_e32 v103, v103\n"
"v_mfma_f32_16x16x32_bf16 v[116:119], v[104:107], v[32:35], v[36:39]\n"
"ds_read_b128 v[144:147], v254 offset:10496\n"
"v_exp_f32_e32 v128, v128\n"
"v_exp_f32_e32 v129, v129\n"
"v_mfma_f32_16x16x32_bf16 v[148:151], v[104:107], v[28:31], v[24:27]\n"
"ds_read_b64_tr_b16 v[104:105], v16 offset:14336\n"
"v_exp_f32_e32 v130, v130\n"
"v_exp_f32_e32 v131, v131\n"
"v_cvt_pk_bf16_f32 v152, v100, v101\n"
"s_waitcnt lgkmcnt(3)\n"
"v_mfma_f32_16x16x32_bf16 v[44:47], v[88:91], v[132:135], v[44:47]\n"
"s_add_u32 s98, s69, 0x606000\n"
"buffer_load_dwordx4 a[96:99], v0, s[4:7], s98 offen sc1\n"
"v_cvt_pk_bf16_f32 v153, v102, v103\n"
"v_cvt_pk_bf16_f32 v154, v128, v129\n"
"v_mfma_f32_16x16x32_bf16 v[112:115], v[72:75], v[132:135], v[112:115]\n"
"ds_read_b64_tr_b16 v[106:107], v255 offset:14592\n"
"v_cvt_pk_bf16_f32 v155, v130, v131\n"
"v_exp_f32_e32 v120, v120\n"
"v_exp_f32_e32 v121, v121\n"
"v_mfma_f32_16x16x32_bf16 v[136:139], v[40:43], v[132:135], v[136:139]\n"
"ds_read_b64_tr_b16 v[100:101], v255 offset:14336\n"
"v_exp_f32_e32 v122, v122\n"
"v_exp_f32_e32 v123, v123\n"
"v_mfma_f32_16x16x32_bf16 v[108:111], v[40:43], v[64:67], v[108:111]\n"
"buffer_load_dwordx4 a[100:103], v0, s[4:7], s98 offen offset:1024 sc1\n"
"v_exp_f32_e32 v124, v124\n"
"v_exp_f32_e32 v125, v125\n"
"v_mfma_f32_16x16x32_bf16 v[80:83], v[72:75], v[64:67], v[80:83]\n"
"ds_read_b64_tr_b16 v[102:103], v16 offset:14592\n"
"v_exp_f32_e32 v126, v126\n"
"v_exp_f32_e32 v127, v127\n"
"v_mfma_f32_16x16x32_bf16 v[68:71], v[88:91], v[64:67], v[68:71]\n"
"s_waitcnt lgkmcnt(4)\n"
"v_cvt_pk_bf16_f32 v40, v120, v121\n"
"v_cvt_pk_bf16_f32 v41, v122, v123\n"
"v_cvt_pk_bf16_f32 v42, v124, v125\n"
"v_cvt_pk_bf16_f32 v43, v126, v127\n"
"s_cmp_eq_i32 s78, 6\n"
"s_cbranch_scc1 .Lmk_x20s\n"
".Lmk_x20r:\n"
"v_mfma_f32_16x16x32_bf16 v[64:67], v[84:87], v[28:31], v[24:27]\n"
"ds_read_b128 v[72:75], v15 offset:8192\n"
"v_exp_f32_e32 v76, v76\n"
"v_exp_f32_e32 v77, v77\n"
"v_exp_f32_e32 v78, v78\n"
"v_mfma_f32_16x16x32_bf16 v[120:123], v[84:87], v[32:35], v[36:39]\n"
"buffer_load_dwordx4 a[104:107], v0, s[4:7], s98 offen offset:2048 sc1\n"
"v_exp_f32_e32 v79, v79\n"
"v_mfma_f32_16x16x32_bf16 v[84:87], v[144:147], v[32:35], v[36:39]\n"
"ds_read_b128 v[124:127], v254 offset:8448\n"
"v_exp_f32_e32 v148, v148\n"
"v_exp_f32_e32 v149, v149\n"
"v_mfma_f32_16x16x32_bf16 v[128:131], v[144:147], v[28:31], v[24:27]\n"
"ds_read_b64_tr_b16 v[132:133], v16 offset:12288\n"
"v_exp_f32_e32 v150, v150\n"
"v_exp_f32_e32 v151, v151\n"
"s_waitcnt lgkmcnt(3)\n"
"v_mfma_f32_16x16x32_bf16 v[44:47], v[88:91], v[152:155], v[44:47]\n"
"buffer_load_dwordx4 a[108:111], v0, s[4:7], s98 offen offset:3072 sc1\n"
"v_cvt_pk_bf16_f32 v144, v76, v77\n"
"v_cvt_pk_bf16_f32 v145, v78, v79\n"
"v_cvt_pk_bf16_f32 v146, v148, v149\n"
"v_mfma_f32_16x16x32_bf16 v[112:115], v[104:107], v[152:155], v[112:115]\n"
"ds_read_b64_tr_b16 v[134:135], v255 offset:12544\n"
"v_cvt_pk_bf16_f32 v147, v150, v151\n"
"v_exp_f32_e32 v140, v140\n"
"v_exp_f32_e32 v141, v141\n"
"v_mfma_f32_16x16x32_bf16 v[136:139], v[100:103], v[152:155], v[136:139]\n"
"ds_read_b64_tr_b16 v[76:77], v255 offset:12288\n"
"v_exp_f32_e32 v142, v142\n"
"v_exp_f32_e32 v143, v143\n"
"v_mfma_f32_16x16x32_bf16 v[108:111], v[100:103], v[40:43], v[108:111]\n"
"buffer_load_dwordx4 a[112:115], v1, s[4:7], s98 offen sc1\n"
"v_exp_f32_e32 v116, v116\n"
"v_exp_f32_e32 v117, v117\n"
"v_mfma_f32_16x16x32_bf16 v[80:83], v[104:107], v[40:43], v[80:83]\n"
"ds_read_b64_tr_b16 v[78:79], v16 offset:12544\n"
"v_exp_f32_e32 v118, v118\n"
"v_exp_f32_e32 v119, v119\n"
"v_mfma_f32_16x16x32_bf16 v[68:71], v[88:91], v[40:43], v[68:71]\n"
"s_waitcnt lgkmcnt(4)\n"
"v_cvt_pk_bf16_f32 v40, v140, v141\n"
"v_cvt_pk_bf16_f32 v41, v142, v143\n"
"v_cvt_pk_bf16_f32 v42, v116, v117\n"
"v_cvt_pk_bf16_f32 v43, v118, v119\n"
"s_cmp_eq_i32 s78, 5\n"
"s_cbranch_scc1 .Lmk_x21s\n"
".Lmk_x21r:\n"
"v_mfma_f32_16x16x32_bf16 v[100:103], v[72:75], v[28:31], v[24:27]\n"
"ds_read_b64_tr_b16 v[104:105], v16 offset:10240\n"
"ds_read_b64_tr_b16 v[106:107], v255 offset:10496\n"
"v_exp_f32_e32 v64, v64\n"
"v_exp_f32_e32 v65, v65\n"
"v_mfma_f32_16x16x32_bf16 v[116:119], v[72:75], v[32:35], v[36:39]\n"
"s_add_u32 s97, s71, 0xfffff000\n"
"buffer_load_dwordx2 v[238:239], off, s[4:7], s97 sc1\n"
"v_exp_f32_e32 v66, v66\n"
"v_exp_f32_e32 v67, v67\n"
"v_mfma_f32_16x16x32_bf16 v[72:75], v[124:127], v[32:35], v[36:39]\n"
"ds_read_b64_tr_b16 v[36:37], v255 offset:10240\n"
"v_exp_f32_e32 v128, v128\n"
"v_exp_f32_e32 v129, v129\n"
"v_mfma_f32_16x16x32_bf16 v[140:143], v[124:127], v[28:31], v[24:27]\n"
"ds_read_b64_tr_b16 v[38:39], v16 offset:10496\n"
"buffer_load_dwordx4 a[116:119], v1, s[4:7], s98 offen offset:1024 sc1\n"
"v_exp_f32_e32 v130, v130\n"
"v_exp_f32_e32 v131, v131\n"
"s_waitcnt lgkmcnt(4)\n"
"v_mfma_f32_16x16x32_bf16 v[44:47], v[88:91], v[144:147], v[44:47]\n"
"ds_read_b64_tr_b16 v[124:125], v16 offset:24576\n"
"ds_read_b64_tr_b16 v[126:127], v255 offset:24832\n"
"v_cvt_pk_bf16_f32 v148, v64, v65\n"
"v_cvt_pk_bf16_f32 v149, v66, v67\n"
"v_cvt_pk_bf16_f32 v150, v128, v129\n"
"v_mfma_f32_16x16x32_bf16 v[112:115], v[132:135], v[144:147], v[112:115]\n"
"ds_read_b64_tr_b16 v[64:65], v255 offset:24576\n"
"buffer_load_dwordx4 a[120:123], v1, s[4:7], s98 offen offset:2048 sc1\n"
"v_cvt_pk_bf16_f32 v151, v130, v131\n"
"v_exp_f32_e32 v120, v120\n"
"v_mfma_f32_16x16x32_bf16 v[136:139], v[76:79], v[144:147], v[136:139]\n"
"ds_read_b64_tr_b16 v[66:67], v16 offset:24832\n"
"v_exp_f32_e32 v121, v121\n"
"v_exp_f32_e32 v122, v122\n"
"v_exp_f32_e32 v123, v123\n"
"v_mfma_f32_16x16x32_bf16 v[108:111], v[76:79], v[40:43], v[108:111]\n"
"v_exp_f32_e32 v84, v84\n"
"v_add_u32_e32 v248, 0x26400, v0\n"
"v_exp_f32_e32 v85, v85\n"
"v_mfma_f32_16x16x32_bf16 v[80:83], v[132:135], v[40:43], v[80:83]\n"
"buffer_load_dwordx4 a[124:127], v1, s[4:7], s98 offen offset:3072 sc1\n"
"ds_read_b128 v[76:79], v248\n"
"v_exp_f32_e32 v86, v86\n"
"v_exp_f32_e32 v87, v87\n"
"v_mfma_f32_16x16x32_bf16 v[68:71], v[88:91], v[40:43], v[68:71]\n"
"ds_read_b128 v[128:131], v248 offset:1024\n"
"v_cvt_pk_bf16_f32 v40, v120, v121\n"
"v_cvt_pk_bf16_f32 v41, v122, v123\n"
"v_cvt_pk_bf16_f32 v42, v84, v85\n"
"v_cvt_pk_bf16_f32 v43, v86, v87\n"
"s_waitcnt lgkmcnt(6)\n"
"v_mfma_f32_16x16x32_bf16 v[44:47], v[88:91], v[148:151], v[44:47]\n"
"ds_read_b64_tr_b16 v[84:85], v16 offset:8192\n"
"s_add_u32 s98, s69, 0x608000\n"
"buffer_load_dwordx4 v[24:27], v0, s[4:7], s98 offen sc1\n"
"ds_read_b64_tr_b16 v[86:87], v255 offset:8448\n"
"v_cndmask_b32_e64 v100, v48, v100, s[20:21]\n"
"v_cndmask_b32_e64 v101, v49, v101, s[22:23]\n"
"v_cndmask_b32_e64 v102, v50, v102, s[24:25]\n"
"v_cndmask_b32_e64 v103, v51, v103, s[26:27]\n"
"v_cndmask_b32_e64 v140, v60, v140, s[28:29]\n"
"v_cndmask_b32_e64 v141, v61, v141, s[30:31]\n"
"v_cndmask_b32_e64 v142, v62, v142, s[32:33]\n"
"v_mfma_f32_16x16x32_bf16 v[112:115], v[104:107], v[148:151], v[112:115]\n"
"ds_read_b64_tr_b16 v[48:49], v255 offset:8192\n"
"ds_read_b64_tr_b16 v[50:51], v16 offset:8448\n"
"v_cndmask_b32_e64 v143, v63, v143, s[34:35]\n"
"v_exp_f32_e32 v100, v100\n"
"v_exp_f32_e32 v101, v101\n"
"v_exp_f32_e32 v102, v102\n"
"s_waitcnt vmcnt(11)\n"
"v_exp_f32_e32 v103, v103\n"
"v_exp_f32_e32 v140, v140\n"
"v_mfma_f32_16x16x32_bf16 v[136:139], v[36:39], v[148:151], v[136:139]\n"
"v_add_f32_e32 v247, v247, v246\n"
"v_exp_f32_e32 v141, v141\n"
"v_exp_f32_e32 v142, v142\n"
"v_add_f32_e32 v247, v247, v245\n"
"v_exp_f32_e32 v143, v143\n"
"v_cvt_pk_bf16_f32 v60, v100, v101\n"
"v_add_f32_e32 v247, v247, v244\n"
"v_cvt_pk_bf16_f32 v61, v102, v103\n"
"v_cvt_pk_bf16_f32 v62, v140, v141\n"
"v_mfma_f32_16x16x32_bf16 v[108:111], v[36:39], v[40:43], v[108:111]\n"
"v_cvt_pk_bf16_f32 v63, v142, v143\n"
"v_add_f32_e32 v247, v247, v243\n"
"v_cndmask_b32_e64 v116, v52, v116, s[36:37]\n"
"v_cndmask_b32_e64 v117, v53, v117, s[38:39]\n"
"v_cndmask_b32_e64 v118, v54, v118, s[40:41]\n"
"v_cndmask_b32_e64 v119, v55, v119, s[42:43]\n"
"v_add_f32_e32 v247, v247, v242\n"
"v_cndmask_b32_e64 v72, v56, v72, s[44:45]\n"
"v_cndmask_b32_e64 v73, v57, v73, s[46:47]\n"
"v_cndmask_b32_e64 v74, v58, v74, s[48:49]\n"
"v_add_f32_e32 v247, v247, v241\n"
"v_cndmask_b32_e64 v75, v59, v75, s[50:51]\n"
"v_exp_f32_e32 v116, v116\n"
"v_mfma_f32_16x16x32_bf16 v[80:83], v[104:107], v[40:43], v[80:83]\n"
"v_exp_f32_e32 v117, v117\n"
"v_add_f32_e32 v247, v247, v240\n"
"v_exp_f32_e32 v118, v118\n"
"v_mov_b32_e32 v246, v247\n"
"v_exp_f32_e32 v119, v119\n"
"v_exp_f32_e32 v72, v72\n"
"v_permlane32_swap_b32_e32 v247, v246\n"
"v_exp_f32_e32 v73, v73\n"
"v_mfma_f32_16x16x32_bf16 v[68:71], v[88:91], v[40:43], v[68:71]\n"
"v_exp_f32_e32 v74, v74\n"
"v_add_f32_e32 v247, v247, v246\n"
"v_exp_f32_e32 v75, v75\n"
"v_cvt_pk_bf16_f32 v36, v116, v117\n"
"ds_write_b32 v21, v247 offset:512\n"
"v_cvt_pk_bf16_f32 v37, v118, v119\n"
"v_cvt_pk_bf16_f32 v38, v72, v73\n"
"v_cvt_pk_bf16_f32 v39, v74, v75\n"
"s_waitcnt lgkmcnt(1)\n"
"v_mfma_f32_16x16x32_bf16 v[44:47], v[88:91], v[60:63], v[44:47]\n"
"v_and_b32_e32 v76, v60, v76\n"
"v_and_b32_e32 v77, v61, v77\n"
"v_and_b32_e32 v78, v62, v78\n"
"v_and_b32_e32 v79, v63, v79\n"
"v_mfma_f32_16x16x32_bf16 v[68:71], v[88:91], v[36:39], v[68:71]\n"
"v_xor_b32_e32 v40, v60, v76\n"
"v_xor_b32_e32 v41, v61, v77\n"
"v_xor_b32_e32 v42, v62, v78\n"
"v_xor_b32_e32 v43, v63, v79\n"
"v_mfma_f32_16x16x32_bf16 v[112:115], v[84:87], v[76:79], v[112:115]\n"
"v_and_b32_e32 v128, v36, v128\n"
"v_and_b32_e32 v129, v37, v129\n"
"v_mfma_f32_16x16x32_bf16 v[136:139], v[48:51], v[76:79], v[136:139]\n"
"v_and_b32_e32 v130, v38, v130\n"
"v_and_b32_e32 v131, v39, v131\n"
"v_mfma_f32_16x16x32_bf16 v[112:115], v[124:127], v[40:43], v[112:115]\n"
"v_xor_b32_e32 v52, v36, v128\n"
"v_xor_b32_e32 v53, v37, v129\n"
"v_mfma_f32_16x16x32_bf16 v[136:139], v[64:67], v[40:43], v[136:139]\n"
"v_xor_b32_e32 v54, v38, v130\n"
"v_xor_b32_e32 v55, v39, v131\n"
"v_mfma_f32_16x16x32_bf16 v[80:83], v[84:87], v[128:131], v[80:83]\n"
"s_waitcnt vmcnt(4)\n"
"v_cmp_ne_u64_e32 vcc, v[238:239], v[18:19]\n"
"s_nop 0\n"
"s_cbranch_vccnz .Lmk_f14s\n"
".Lmk_f14r:\n"
"v_mfma_f32_16x16x32_bf16 v[108:111], v[48:51], v[128:131], v[108:111]\n"
"s_add_u32 m0, s77, 0x4000\n"
"s_add_u32 s97, s65, 0\n"
"buffer_load_dwordx4 v2, s[4:7], s97 offen sc1 lds\n"
"buffer_load_dwordx4 v2, s[4:7], s97 offen offset:1024 sc1 lds\n"
"v_mfma_f32_16x16x32_bf16 v[80:83], v[124:127], v[52:55], v[80:83]\n"
"s_add_u32 m0, s77, 0xc000\n"
"s_add_u32 s97, s65, 0x4000\n"
"buffer_load_dwordx4 v2, s[4:7], s97 offen sc1 lds\n"
"buffer_load_dwordx4 v2, s[4:7], s97 offen offset:1024 sc1 lds\n"
"v_mfma_f32_16x16x32_bf16 v[108:111], v[64:67], v[52:55], v[108:111]\n"
"v_max_f32_e32 v248, v44, v68\n"
"v_rcp_f32_e32 v44, v44\n"
"v_rcp_f32_e32 v68, v68\n"
"s_nop 0\n"
"v_max3_f32 v248, v248, v44, v68\n"
"v_cmp_nlt_f32_e32 vcc, v248, v252\n"
"s_cbranch_vccnz .Lmk_b16\n"
".Lmk_k22:\n"
"s_add_u32 s97, s60, 0x40000\n"
"v_mul_f32_e32 v112, v112, v44\n"
"v_mul_f32_e32 v113, v113, v44\n"
"v_mul_f32_e32 v114, v114, v44\n"
"v_mul_f32_e32 v115, v115, v44\n"
"v_mul_f32_e32 v136, v136, v44\n"
"v_mul_f32_e32 v137, v137, v44\n"
"v_mul_f32_e32 v138, v138, v44\n"
"v_mul_f32_e32 v139, v139, v44\n"
"v_cvt_pk_bf16_f32 v28, v112, v113\n"
"v_cvt_pk_bf16_f32 v29, v114, v115\n"
"v_cvt_pk_bf16_f32 v30, v136, v137\n"
"v_cvt_pk_bf16_f32 v31, v138, v139\n"
"buffer_store_dwordx4 v[28:31], v0, s[4:7], s97 offen sc1\n"
"s_add_u32 s97, s60, 0x48000\n"
"v_mul_f32_e32 v80, v80, v68\n"
"v_mul_f32_e32 v81, v81, v68\n"
"v_mul_f32_e32 v82, v82, v68\n"
"v_mul_f32_e32 v83, v83, v68\n"
"v_mul_f32_e32 v108, v108, v68\n"
"v_mul_f32_e32 v109, v109, v68\n"
"v_mul_f32_e32 v110, v110, v68\n"
"v_mul_f32_e32 v111, v111, v68\n"
"v_cvt_pk_bf16_f32 v28, v80, v81\n"
"v_cvt_pk_bf16_f32 v29, v82, v83\n"
"v_cvt_pk_bf16_f32 v30, v108, v109\n"
"v_cvt_pk_bf16_f32 v31, v110, v111\n"
"buffer_store_dwordx4 v[28:31], v0, s[4:7], s97 offen sc1\n"
";\n"
"; @A AT a\n"
"s_mov_b32 s2, 0\n"
"v_mov_b32_e32 v253, 0xff800000\n"
"v_mov_b32_e32 v251, 0xff800000\n"
"v_mov_b32_e32 v250, 0x7f800000\n"
"v_mov_b32_e32 v249, 0x7f800000\n"
".Lmk_a23:\n"
"ds_read_b128 v[28:31], v15 offset:16384\n"
"ds_read_b128 v[32:35], v254 offset:16640\n"
"ds_read_b128 v[36:39], v15 offset:14336\n"
"ds_read_b128 v[40:43], v254 offset:14592\n"
"s_cmp_gt_i32 s78, 7\n"
"s_cselect_b32 s97, 0x80000000, 0\n"
"s_or_b32 s97, s97, 0x7f800000\n"
"s_add_u32 s96, s67, 32\n"
"buffer_load_dwordx2 v[246:247], v12, s[4:7], s96 offen sc1\n"
"s_waitcnt lgkmcnt(2)\n"
"v_mfma_f32_16x16x32_bf16 v[100:103], v[28:31], v[92:95], 0\n"
"v_mfma_f32_16x16x32_bf16 v[104:107], v[28:31], v[96:99], 0\n"
"v_mfma_f32_16x16x32_bf16 v[108:111], v[32:35], v[96:99], 0\n"
"v_mfma_f32_16x16x32_bf16 v[112:115], v[32:35], v[92:95], 0\n"
"s_waitcnt lgkmcnt(0)\n"
"s_nop 3\n"
"v_max_f32_e32 v248, v100, v101\n"
"v_max_f32_e32 v245, v104, v105\n"
"v_max3_f32 v248, v248, v102, v103\n"
"v_max3_f32 v245, v245, v106, v107\n"
"v_max3_f32 v248, v248, v112, v113\n"
"v_max3_f32 v245, v245, v108, v109\n"
"v_max3_f32 v248, v248, v114, v115\n"
"v_max3_f32 v245, v245, v110, v111\n"
"v_mov_b32_e32 v244, v248\n"
"v_mov_b32_e32 v243, v245\n"
"s_nop 0\n"
"v_permlane16_swap_b32_e32 v248, v244\n"
"v_permlane16_swap_b32_e32 v245, v243\n"
"v_max_f32_e32 v248, v248, v244\n"
"v_max_f32_e32 v245, v245, v243\n"
"v_mov_b32_e32 v244, v248\n"
"v_mov_b32_e32 v243, v245\n"
"s_nop 0\n"
"v_permlane32_swap_b32_e32 v248, v244\n"
"v_permlane32_swap_b32_e32 v245, v243\n"
"v_max_f32_e32 v248, v248, v244\n"
"v_max_f32_e32 v245, v245, v243\n"
"v_med3_f32 v248, v248, v253, v250\n"
"v_med3_f32 v245, v245, v251, v249\n"
"v_min_f32_e64 v116, s97, -v248\n"
"v_min_f32_e64 v117, s97, -v248\n"
"v_min_f32_e64 v118, s97, -v248\n"
"v_min_f32_e64 v119, s97, -v248\n"
"v_min_f32_e64 v120, s97, -v245\n"
"v_min_f32_e64 v121, s97, -v245\n"
"v_min_f32_e64 v122, s97, -v245\n"
"v_min_f32_e64 v123, s97, -v245\n"
"v_mfma_f32_16x16x32_bf16 v[44:47], v[36:39], v[92:95], v[116:119]\n"
"ds_read_b128 v[32:35], v15 offset:12288\n"
"v_sub_f32_e32 v100, v100, v248\n"
"v_sub_f32_e32 v101, v101, v248\n"
"v_sub_f32_e32 v102, v102, v248\n"
"v_sub_f32_e32 v103, v103, v248\n"
"v_mfma_f32_16x16x32_bf16 v[48:51], v[36:39], v[96:99], v[120:123]\n"
"v_sub_f32_e32 v112, v112, v248\n"
"v_sub_f32_e32 v113, v113, v248\n"
"v_sub_f32_e32 v114, v114, v248\n"
"v_sub_f32_e32 v115, v115, v248\n"
"v_mfma_f32_16x16x32_bf16 v[52:55], v[40:43], v[96:99], v[120:123]\n"
"ds_read_b128 v[56:59], v254 offset:12544\n"
"v_sub_f32_e32 v104, v104, v245\n"
"v_sub_f32_e32 v105, v105, v245\n"
"v_sub_f32_e32 v106, v106, v245\n"
"v_sub_f32_e32 v107, v107, v245\n"
"v_mfma_f32_16x16x32_bf16 v[36:39], v[40:43], v[92:95], v[116:119]\n"
"s_waitcnt lgkmcnt(0)\n"
"v_sub_f32_e32 v108, v108, v245\n"
"v_sub_f32_e32 v109, v109, v245\n"
"v_sub_f32_e32 v110, v110, v245\n"
"v_sub_f32_e32 v111, v111, v245\n"
"s_cmp_eq_i32 s78, 7\n"
"s_cbranch_scc1 .Lmk_x25s\n"
".Lmk_x25r:\n"
"v_mfma_f32_16x16x32_bf16 v[60:63], v[32:35], v[92:95], v[116:119]\n"
"ds_read_b128 v[40:43], v15 offset:10240\n"
"buffer_load_dwordx4 v[28:31], v0, s[4:7], s98 offen offset:1024 sc1\n"
"ds_read_b128 v[64:67], v254 offset:10496\n"
"v_exp_f32_e32 v44, v44\n"
"v_exp_f32_e32 v45, v45\n"
"v_exp_f32_e32 v46, v46\n"
"v_exp_f32_e32 v47, v47\n"
"v_exp_f32_e32 v36, v36\n"
"v_mfma_f32_16x16x32_bf16 v[68:71], v[32:35], v[96:99], v[120:123]\n"
"ds_read_b64_tr_b16 v[72:73], v16 offset:14336\n"
"buffer_load_dwordx4 v[32:35], v0, s[4:7], s98 offen offset:2048 sc1\n"
"v_exp_f32_e32 v37, v37\n"
"v_exp_f32_e32 v38, v38\n"
"v_exp_f32_e32 v39, v39\n"
"v_cvt_pk_bf16_f32 v76, v44, v45\n"
"v_cvt_pk_bf16_f32 v77, v46, v47\n"
"v_cvt_pk_bf16_f32 v78, v36, v37\n"
"v_mfma_f32_16x16x32_bf16 v[80:83], v[56:59], v[96:99], v[120:123]\n"
"ds_read_b64_tr_b16 v[74:75], v255 offset:14592\n"
"ds_read_b64_tr_b16 v[84:85], v255 offset:14336\n"
"v_cvt_pk_bf16_f32 v79, v38, v39\n"
"v_exp_f32_e32 v48, v48\n"
"v_exp_f32_e32 v49, v49\n"
"v_exp_f32_e32 v50, v50\n"
"v_exp_f32_e32 v51, v51\n"
"v_mfma_f32_16x16x32_bf16 v[124:127], v[56:59], v[92:95], v[116:119]\n"
"buffer_load_dwordx4 v[36:39], v0, s[4:7], s98 offen offset:3072 sc1\n"
"ds_read_b64_tr_b16 v[86:87], v16 offset:14592\n"
"v_exp_f32_e32 v52, v52\n"
"v_exp_f32_e32 v53, v53\n"
"v_exp_f32_e32 v54, v54\n"
"v_exp_f32_e32 v55, v55\n"
"v_cvt_pk_bf16_f32 v56, v48, v49\n"
"v_cvt_pk_bf16_f32 v57, v50, v51\n"
"v_cvt_pk_bf16_f32 v58, v52, v53\n"
"v_cvt_pk_bf16_f32 v59, v54, v55\n"
"s_waitcnt lgkmcnt(4)\n"
"s_cmp_eq_i32 s78, 6\n"
"s_cbranch_scc1 .Lmk_x26s\n"
".Lmk_x26r:\n"
"v_mfma_f32_16x16x32_bf16 v[128:131], v[40:43], v[92:95], v[116:119]\n"
"ds_read_b128 v[52:55], v15 offset:8192\n"
"v_exp_f32_e32 v60, v60\n"
"v_exp_f32_e32 v61, v61\n"
"v_exp_f32_e32 v62, v62\n"
"v_mfma_f32_16x16x32_bf16 v[132:135], v[40:43], v[96:99], v[120:123]\n"
"buffer_load_dwordx4 v[40:43], v1, s[4:7], s98 offen sc1\n"
"v_exp_f32_e32 v63, v63\n"
"v_mfma_f32_16x16x32_bf16 v[136:139], v[64:67], v[96:99], v[120:123]\n"
"ds_read_b128 v[140:143], v254 offset:8448\n"
"v_exp_f32_e32 v124, v124\n"
"v_exp_f32_e32 v125, v125\n"
"v_mfma_f32_16x16x32_bf16 v[144:147], v[64:67], v[92:95], v[116:119]\n"
"ds_read_b64_tr_b16 v[64:65], v16 offset:12288\n"
"v_exp_f32_e32 v126, v126\n"
"v_exp_f32_e32 v127, v127\n"
"s_waitcnt lgkmcnt(3)\n"
"v_mfma_f32_16x16x32_bf16 v[148:151], v[88:91], v[76:79], 0\n"
"buffer_load_dwordx4 v[44:47], v1, s[4:7], s98 offen offset:1024 sc1\n"
"v_cvt_pk_bf16_f32 v152, v60, v61\n"
"v_cvt_pk_bf16_f32 v153, v62, v63\n"
"v_cvt_pk_bf16_f32 v154, v124, v125\n"
"v_mfma_f32_16x16x32_bf16 v[156:159], v[72:75], v[76:79], 0\n"
"ds_read_b64_tr_b16 v[66:67], v255 offset:12544\n"
"v_cvt_pk_bf16_f32 v155, v126, v127\n"
"v_exp_f32_e32 v68, v68\n"
"v_exp_f32_e32 v69, v69\n"
"v_mfma_f32_16x16x32_bf16 v[124:127], v[84:87], v[76:79], 0\n"
"ds_read_b64_tr_b16 v[60:61], v255 offset:12288\n"
"v_exp_f32_e32 v70, v70\n"
"v_exp_f32_e32 v71, v71\n"
"v_mfma_f32_16x16x32_bf16 v[160:163], v[84:87], v[56:59], 0\n"
"buffer_load_dwordx4 v[48:51], v1, s[4:7], s98 offen offset:2048 sc1\n"
"v_exp_f32_e32 v80, v80\n"
"v_exp_f32_e32 v81, v81\n"
"v_mfma_f32_16x16x32_bf16 v[164:167], v[72:75], v[56:59], 0\n"
"ds_read_b64_tr_b16 v[62:63], v16 offset:12544\n"
"v_exp_f32_e32 v82, v82\n"
"v_exp_f32_e32 v83, v83\n"
"v_mfma_f32_16x16x32_bf16 v[168:171], v[88:91], v[56:59], 0\n"
"s_waitcnt lgkmcnt(4)\n"
"v_cvt_pk_bf16_f32 v72, v68, v69\n"
"v_cvt_pk_bf16_f32 v73, v70, v71\n"
"v_cvt_pk_bf16_f32 v74, v80, v81\n"
"v_cvt_pk_bf16_f32 v75, v82, v83\n"
"s_cmp_eq_i32 s78, 5\n"
"s_cbranch_scc1 .Lmk_x27s\n"
".Lmk_x27r:\n"
"v_mfma_f32_16x16x32_bf16 v[76:79], v[52:55], v[92:95], v[116:119]\n"
"ds_read_b64_tr_b16 v[68:69], v16 offset:10240\n"
"v_exp_f32_e32 v128, v128\n"
"v_exp_f32_e32 v129, v129\n"
"v_exp_f32_e32 v130, v130\n"
"v_mfma_f32_16x16x32_bf16 v[80:83], v[52:55], v[96:99], v[120:123]\n"
"buffer_load_dwordx4 v[52:55], v1, s[4:7], s98 offen offset:3072 sc1\n"
"v_exp_f32_e32 v131, v131\n"
"v_mfma_f32_16x16x32_bf16 v[84:87], v[140:143], v[96:99], v[120:123]\n"
"ds_read_b64_tr_b16 v[70:71], v255 offset:10496\n"
"v_exp_f32_e32 v144, v144\n"
"v_exp_f32_e32 v145, v145\n"
"v_mfma_f32_16x16x32_bf16 v[172:175], v[140:143], v[92:95], v[116:119]\n"
"s_waitcnt lgkmcnt(2)\n"
"v_exp_f32_e32 v146, v146\n"
"v_exp_f32_e32 v147, v147\n"
"v_cvt_pk_bf16_f32 v140, v128, v129\n"
"v_mfma_f32_16x16x32_bf16 v[148:151], v[88:91], v[152:155], v[148:151]\n"
"s_add_u32 s97, s69, 0x60a000\n"
"buffer_load_dwordx4 v[56:59], v0, s[4:7], s97 offen sc1\n"
"v_cvt_pk_bf16_f32 v141, v130, v131\n"
"v_cvt_pk_bf16_f32 v142, v144, v145\n"
"v_mfma_f32_16x16x32_bf16 v[156:159], v[64:67], v[152:155], v[156:159]\n"
"ds_read_b64_tr_b16 v[128:129], v255 offset:10240\n"
"v_cvt_pk_bf16_f32 v143, v146, v147\n"
"v_exp_f32_e32 v132, v132\n"
"v_exp_f32_e32 v133, v133\n"
"v_mfma_f32_16x16x32_bf16 v[124:127], v[60:63], v[152:155], v[124:127]\n"
"v_exp_f32_e32 v134, v134\n"
"v_exp_f32_e32 v135, v135\n"
"v_mfma_f32_16x16x32_bf16 v[160:163], v[60:63], v[72:75], v[160:163]\n"
"buffer_load_dwordx4 v[60:63], v0, s[4:7], s97 offen offset:1024 sc1\n"
"ds_read_b64_tr_b16 v[130:131], v16 offset:10496\n"
"v_exp_f32_e32 v136, v136\n"
"v_mfma_f32_16x16x32_bf16 v[164:167], v[64:67], v[72:75], v[164:167]\n"
"v_exp_f32_e32 v137, v137\n"
"v_exp_f32_e32 v138, v138\n"
"v_exp_f32_e32 v139, v139\n"
"v_mfma_f32_16x16x32_bf16 v[168:171], v[88:91], v[72:75], v[168:171]\n"
"s_waitcnt vmcnt(12)\n"
"v_cvt_pk_bf16_f32 v72, v132, v133\n"
"v_cvt_pk_bf16_f32 v73, v134, v135\n"
"v_cvt_pk_bf16_f32 v74, v136, v137\n"
"v_cvt_pk_bf16_f32 v75, v138, v139\n"
"s_cmp_lg_u32 s2, 0\n"
"s_cbranch_scc1 .Lmk_s28\n"
"s_barrier\n"
".Lmk_s28:\n"
"ds_read_b128 v[132:135], v15 offset:6144\n"
"ds_read_b128 v[136:139], v254 offset:6400\n"
"s_waitcnt lgkmcnt(2)\n"
"v_mfma_f32_16x16x32_bf16 v[148:151], v[88:91], v[140:143], v[148:151]\n"
"ds_read_b128 v[144:147], v15 offset:4096\n"
"v_exp_f32_e32 v76, v76\n"
"v_exp_f32_e32 v77, v77\n"
"v_exp_f32_e32 v78, v78\n"
"v_mfma_f32_16x16x32_bf16 v[156:159], v[68:71], v[140:143], v[156:159]\n"
"buffer_load_dwordx4 v[64:67], v0, s[4:7], s97 offen offset:2048 sc1\n"
"ds_read_b128 v[152:155], v254 offset:4352\n"
"v_exp_f32_e32 v79, v79\n"
"v_mfma_f32_16x16x32_bf16 v[124:127], v[128:131], v[140:143], v[124:127]\n"
"ds_read_b64_tr_b16 v[140:141], v16 offset:8192\n"
"v_exp_f32_e32 v172, v172\n"
"v_exp_f32_e32 v173, v173\n"
"v_mfma_f32_16x16x32_bf16 v[160:163], v[128:131], v[72:75], v[160:163]\n"
"ds_read_b64_tr_b16 v[142:143], v255 offset:8448\n"
"v_exp_f32_e32 v174, v174\n"
"v_exp_f32_e32 v175, v175\n"
"v_mfma_f32_16x16x32_bf16 v[164:167], v[68:71], v[72:75], v[164:167]\n"
"buffer_load_dwordx4 v[68:71], v0, s[4:7], s97 offen offset:3072 sc1\n"
"v_cvt_pk_bf16_f32 v128, v76, v77\n"
"v_cvt_pk_bf16_f32 v129, v78, v79\n"
"v_cvt_pk_bf16_f32 v130, v172, v173\n"
"v_cvt_pk_bf16_f32 v131, v174, v175\n"
"v_mfma_f32_16x16x32_bf16 v[168:171], v[88:91], v[72:75], v[168:171]\n"
"ds_read_b64_tr_b16 v[172:173], v255 offset:8192\n"
"v_exp_f32_e32 v80, v80\n"
"v_exp_f32_e32 v81, v81\n"
"s_waitcnt lgkmcnt(5)\n"
"v_mfma_f32_16x16x32_bf16 v[176:179], v[132:135], v[92:95], v[116:119]\n"
"ds_read_b64_tr_b16 v[174:175], v16 offset:8448\n"
"v_exp_f32_e32 v82, v82\n"
"v_exp_f32_e32 v83, v83\n"
"v_exp_f32_e32 v84, v84\n"
"v_mfma_f32_16x16x32_bf16 v[180:183], v[132:135], v[96:99], v[120:123]\n"
"buffer_load_dwordx4 v[72:75], v1, s[4:7], s97 offen sc1\n"
"s_waitcnt vmcnt(12)\n"
"v_cmp_ne_u64_e32 vcc, v[246:247], v[18:19]\n"
"v_mfma_f32_16x16x32_bf16 v[132:135], v[136:139], v[96:99], v[120:123]\n"
"v_exp_f32_e32 v85, v85\n"
"v_exp_f32_e32 v86, v86\n"
"v_exp_f32_e32 v87, v87\n"
"v_mfma_f32_16x16x32_bf16 v[184:187], v[136:139], v[92:95], v[116:119]\n"
"s_cbranch_vccnz .Lmk_f5s\n"
".Lmk_f5r:\n"
"v_cvt_pk_bf16_f32 v136, v80, v81\n"
"v_cvt_pk_bf16_f32 v137, v82, v83\n"
"v_cvt_pk_bf16_f32 v138, v84, v85\n"
"v_cvt_pk_bf16_f32 v139, v86, v87\n"
"s_waitcnt lgkmcnt(4)\n"
"v_mfma_f32_16x16x32_bf16 v[188:191], v[144:147], v[92:95], v[116:119]\n"
"ds_read_b128 v[192:195], v15 offset:2048\n"
"v_exp_f32_e32 v176, v176\n"
"v_exp_f32_e32 v177, v177\n"
"v_exp_f32_e32 v178, v178\n"
"v_mfma_f32_16x16x32_bf16 v[196:199], v[144:147], v[96:99], v[120:123]\n"
"buffer_load_dwordx4 v[76:79], v1, s[4:7], s97 offen offset:1024 sc1\n"
"v_exp_f32_e32 v179, v179\n"
"v_mfma_f32_16x16x32_bf16 v[144:147], v[152:155], v[96:99], v[120:123]\n"
"ds_read_b128 v[200:203], v254 offset:2304\n"
"v_exp_f32_e32 v184, v184\n"
"v_exp_f32_e32 v185, v185\n"
"v_mfma_f32_16x16x32_bf16 v[204:207], v[152:155], v[92:95], v[116:119]\n"
"ds_read_b64_tr_b16 v[152:153], v16 offset:6144\n"
"v_exp_f32_e32 v186, v186\n"
"v_exp_f32_e32 v187, v187\n"
"s_waitcnt lgkmcnt(3)\n"
"v_mfma_f32_16x16x32_bf16 v[148:151], v[88:91], v[128:131], v[148:151]\n"
"buffer_load_dwordx4 v[80:83], v1, s[4:7], s97 offen offset:2048 sc1\n"
"v_cvt_pk_bf16_f32 v208, v176, v177\n"
"v_cvt_pk_bf16_f32 v209, v178, v179\n"
"v_cvt_pk_bf16_f32 v210, v184, v185\n"
"v_mfma_f32_16x16x32_bf16 v[156:159], v[140:143], v[128:131], v[156:159]\n"
"ds_read_b64_tr_b16 v[154:155], v255 offset:6400\n"
"v_cvt_pk_bf16_f32 v211, v186, v187\n"
"v_exp_f32_e32 v180, v180\n"
"v_exp_f32_e32 v181, v181\n"
"v_mfma_f32_16x16x32_bf16 v[124:127], v[172:175], v[128:131], v[124:127]\n"
"ds_read_b64_tr_b16 v[128:129], v255 offset:6144\n"
"v_exp_f32_e32 v182, v182\n"
"v_exp_f32_e32 v183, v183\n"
"v_mfma_f32_16x16x32_bf16 v[160:163], v[172:175], v[136:139], v[160:163]\n"
"buffer_load_dwordx4 v[84:87], v1, s[4:7], s97 offen offset:3072 sc1\n"
"v_exp_f32_e32 v132, v132\n"
"v_exp_f32_e32 v133, v133\n"
"v_mfma_f32_16x16x32_bf16 v[164:167], v[140:143], v[136:139], v[164:167]\n"
"ds_read_b64_tr_b16 v[130:131], v16 offset:6400\n"
"v_exp_f32_e32 v134, v134\n"
"v_exp_f32_e32 v135, v135\n"
"v_mfma_f32_16x16x32_bf16 v[168:171], v[88:91], v[136:139], v[168:171]\n"
"s_waitcnt lgkmcnt(4)\n"
"v_cvt_pk_bf16_f32 v136, v180, v181\n"
"v_cvt_pk_bf16_f32 v137, v182, v183\n"
"v_cvt_pk_bf16_f32 v138, v132, v133\n"
"v_cvt_pk_bf16_f32 v139, v134, v135\n"
"v_mfma_f32_16x16x32_bf16 v[132:135], v[192:195], v[92:95], v[116:119]\n"
"ds_read_b128 v[140:143], v15\n"
"v_exp_f32_e32 v188, v188\n"
"v_exp_f32_e32 v189, v189\n"
"v_exp_f32_e32 v190, v190\n"
"v_mfma_f32_16x16x32_bf16 v[172:175], v[192:195], v[96:99], v[120:123]\n"
"s_add_u32 s97, s56, 0x40000\n"
"buffer_load_dwordx4 v[176:179], v0, s[4:7], s97 offen sc1\n"
"v_exp_f32_e32 v191, v191\n"
"v_mfma_f32_16x16x32_bf16 v[180:183], v[200:203], v[96:99], v[120:123]\n"
"ds_read_b128 v[184:187], v254 offset:256\n"
"v_exp_f32_e32 v204, v204\n"
"v_exp_f32_e32 v205, v205\n"
"v_exp_f32_e32 v206, v206\n"
"v_mfma_f32_16x16x32_bf16 v[192:195], v[200:203], v[92:95], v[116:119]\n"
"buffer_load_dwordx4 v[200:203], v0, s[4:7], s97 offen offset:1024 sc1\n"
"ds_read_b64_tr_b16 v[212:213], v16 offset:4096\n"
"v_exp_f32_e32 v207, v207\n"
"s_waitcnt lgkmcnt(3)\n"
"v_mfma_f32_16x16x32_bf16 v[148:151], v[88:91], v[208:211], v[148:151]\n"
"v_cvt_pk_bf16_f32 v216, v188, v189\n"
"v_cvt_pk_bf16_f32 v217, v190, v191\n"
"v_cvt_pk_bf16_f32 v218, v204, v205\n"
"v_cvt_pk_bf16_f32 v219, v206, v207\n"
"v_mfma_f32_16x16x32_bf16 v[156:159], v[152:155], v[208:211], v[156:159]\n"
"buffer_load_dwordx4 v[188:191], v0, s[4:7], s97 offen offset:2048 sc1\n"
"ds_read_b64_tr_b16 v[214:215], v255 offset:4352\n"
"v_exp_f32_e32 v196, v196\n"
"v_exp_f32_e32 v197, v197\n"
"v_mfma_f32_16x16x32_bf16 v[124:127], v[128:131], v[208:211], v[124:127]\n"
"buffer_load_dwordx4 v[204:207], v0, s[4:7], s97 offen offset:3072 sc1\n"
"v_exp_f32_e32 v198, v198\n"
"v_exp_f32_e32 v199, v199\n"
"v_mfma_f32_16x16x32_bf16 v[160:163], v[128:131], v[136:139], v[160:163]\n"
"ds_read_b64_tr_b16 v[128:129], v255 offset:4096\n"
"v_exp_f32_e32 v144, v144\n"
"v_exp_f32_e32 v145, v145\n"
"v_mfma_f32_16x16x32_bf16 v[164:167], v[152:155], v[136:139], v[164:167]\n"
"buffer_load_dwordx4 v[152:155], v1, s[4:7], s97 offen sc1\n"
"ds_read_b64_tr_b16 v[130:131], v16 offset:4352\n"
"v_exp_f32_e32 v146, v146\n"
"v_exp_f32_e32 v147, v147\n"
"v_mfma_f32_16x16x32_bf16 v[168:171], v[88:91], v[136:139], v[168:171]\n"
"s_waitcnt lgkmcnt(4)\n"
"v_cvt_pk_bf16_f32 v136, v196, v197\n"
"v_cvt_pk_bf16_f32 v137, v198, v199\n"
"v_cvt_pk_bf16_f32 v138, v144, v145\n"
"v_cvt_pk_bf16_f32 v139, v146, v147\n"
"v_mfma_f32_16x16x32_bf16 v[144:147], v[140:143], v[92:95], v[116:119]\n"
"ds_read_b64_tr_b16 v[196:197], v16 offset:2048\n"
"ds_read_b64_tr_b16 v[198:199], v255 offset:2304\n"
"buffer_load_dwordx4 v[208:211], v1, s[4:7], s97 offen offset:1024 sc1\n"
"v_exp_f32_e32 v132, v132\n"
"v_exp_f32_e32 v133, v133\n"
"v_mfma_f32_16x16x32_bf16 v[220:223], v[140:143], v[96:99], v[120:123]\n"
"ds_read_b64_tr_b16 v[140:141], v255 offset:2048\n"
"v_exp_f32_e32 v134, v134\n"
"v_exp_f32_e32 v135, v135\n"
"v_mfma_f32_16x16x32_bf16 v[224:227], v[184:187], v[96:99], v[120:123]\n"
"buffer_load_dwordx4 v[120:123], v1, s[4:7], s97 offen offset:2048 sc1\n"
"v_exp_f32_e32 v192, v192\n"
"v_exp_f32_e32 v193, v193\n"
"v_mfma_f32_16x16x32_bf16 v[228:231], v[184:187], v[92:95], v[116:119]\n"
"ds_read_b64_tr_b16 v[142:143], v16 offset:2304\n"
"ds_read_b64_tr_b16 v[116:117], v16 offset:16384\n"
"v_exp_f32_e32 v194, v194\n"
"v_exp_f32_e32 v195, v195\n"
"s_waitcnt lgkmcnt(5)\n"
"v_mfma_f32_16x16x32_bf16 v[148:151], v[88:91], v[216:219], v[148:151]\n"
"v_cvt_pk_bf16_f32 v184, v132, v133\n"
"v_cvt_pk_bf16_f32 v185, v134, v135\n"
"v_cvt_pk_bf16_f32 v186, v192, v193\n"
"v_mfma_f32_16x16x32_bf16 v[156:159], v[212:215], v[216:219], v[156:159]\n"
"buffer_load_dwordx4 v[132:135], v1, s[4:7], s97 offen offset:3072 sc1\n"
"ds_read_b64_tr_b16 v[118:119], v255 offset:16640\n"
"ds_read_b64_tr_b16 v[192:193], v255 offset:16384\n"
"v_cvt_pk_bf16_f32 v187, v194, v195\n"
"v_exp_f32_e32 v172, v172\n"
"v_exp_f32_e32 v173, v173\n"
"v_exp_f32_e32 v174, v174\n"
"v_mfma_f32_16x16x32_bf16 v[124:127], v[128:131], v[216:219], v[124:127]\n"
"s_add_u32 s97, s57, 0x40000\n"
"buffer_load_dwordx4 v[216:219], v0, s[4:7], s97 offen sc1\n"
"ds_read_b64_tr_b16 v[194:195], v16 offset:16640\n"
"v_exp_f32_e32 v175, v175\n"
"v_mfma_f32_16x16x32_bf16 v[160:163], v[128:131], v[136:139], v[160:163]\n"
"v_exp_f32_e32 v180, v180\n"
"v_add_u32_e32 v248, 0x26400, v0\n"
"v_exp_f32_e32 v181, v181\n"
"v_mfma_f32_16x16x32_bf16 v[164:167], v[212:215], v[136:139], v[164:167]\n"
"buffer_load_dwordx4 v[128:131], v0, s[4:7], s97 offen offset:1024 sc1\n"
"ds_read_b128 v[212:215], v248\n"
"v_exp_f32_e32 v182, v182\n"
"v_exp_f32_e32 v183, v183\n"
"v_mfma_f32_16x16x32_bf16 v[168:171], v[88:91], v[136:139], v[168:171]\n"
"ds_read_b128 v[232:235], v248 offset:1024\n"
"v_cvt_pk_bf16_f32 v136, v172, v173\n"
"v_cvt_pk_bf16_f32 v137, v174, v175\n"
"v_cvt_pk_bf16_f32 v138, v180, v181\n"
"v_cvt_pk_bf16_f32 v139, v182, v183\n"
"s_waitcnt lgkmcnt(6)\n"
"v_mfma_f32_16x16x32_bf16 v[148:151], v[88:91], v[184:187], v[148:151]\n"
"ds_read_b64_tr_b16 v[172:173], v16\n"
"buffer_load_dwordx4 v[180:183], v0, s[4:7], s97 offen offset:2048 sc1\n"
"v_cndmask_b32_e64 v144, v100, v144, s[20:21]\n"
"v_cndmask_b32_e64 v145, v101, v145, s[22:23]\n"
"v_cndmask_b32_e64 v146, v102, v146, s[24:25]\n"
"v_cndmask_b32_e64 v147, v103, v147, s[26:27]\n"
"v_cndmask_b32_e64 v228, v112, v228, s[28:29]\n"
"v_cndmask_b32_e64 v229, v113, v229, s[30:31]\n"
"v_cndmask_b32_e64 v230, v114, v230, s[32:33]\n"
"v_cndmask_b32_e64 v231, v115, v231, s[34:35]\n"
"v_exp_f32_e32 v144, v144\n"
"v_mfma_f32_16x16x32_bf16 v[156:159], v[196:199], v[184:187], v[156:159]\n"
"ds_read_b64_tr_b16 v[174:175], v255 offset:256\n"
"buffer_load_dwordx4 v[100:103], v0, s[4:7], s97 offen offset:3072 sc1\n"
"v_exp_f32_e32 v145, v145\n"
"v_exp_f32_e32 v146, v146\n"
"v_exp_f32_e32 v147, v147\n"
"v_exp_f32_e32 v228, v228\n"
"v_mfma_f32_16x16x32_bf16 v[124:127], v[140:143], v[184:187], v[124:127]\n"
"buffer_load_dwordx4 v[184:187], v1, s[4:7], s97 offen sc1\n"
"v_exp_f32_e32 v229, v229\n"
"v_exp_f32_e32 v230, v230\n"
"v_exp_f32_e32 v231, v231\n"
"v_cvt_pk_bf16_f32 v112, v144, v145\n"
"v_cvt_pk_bf16_f32 v113, v146, v147\n"
"v_cvt_pk_bf16_f32 v114, v228, v229\n"
"v_mfma_f32_16x16x32_bf16 v[160:163], v[140:143], v[136:139], v[160:163]\n"
"ds_read_b64_tr_b16 v[140:141], v255\n"
"v_cvt_pk_bf16_f32 v115, v230, v231\n"
"v_cndmask_b32_e64 v220, v104, v220, s[36:37]\n"
"v_cndmask_b32_e64 v221, v105, v221, s[38:39]\n"
"v_cndmask_b32_e64 v222, v106, v222, s[40:41]\n"
"v_cndmask_b32_e64 v223, v107, v223, s[42:43]\n"
"v_cndmask_b32_e64 v224, v108, v224, s[44:45]\n"
"v_cndmask_b32_e64 v225, v109, v225, s[46:47]\n"
"v_cndmask_b32_e64 v226, v110, v226, s[48:49]\n"
"v_cndmask_b32_e64 v227, v111, v227, s[50:51]\n"
"v_mfma_f32_16x16x32_bf16 v[164:167], v[196:199], v[136:139], v[164:167]\n"
"buffer_load_dwordx4 v[104:107], v1, s[4:7], s97 offen offset:1024 sc1\n"
"ds_read_b64_tr_b16 v[142:143], v16 offset:256\n"
"buffer_load_dwordx4 v[108:111], v1, s[4:7], s97 offen offset:2048 sc1\n"
"v_exp_f32_e32 v220, v220\n"
"v_exp_f32_e32 v221, v221\n"
"v_exp_f32_e32 v222, v222\n"
"v_exp_f32_e32 v223, v223\n"
"v_exp_f32_e32 v224, v224\n"
"v_mfma_f32_16x16x32_bf16 v[168:171], v[88:91], v[136:139], v[168:171]\n"
"s_waitcnt lgkmcnt(0)\n"
"v_exp_f32_e32 v225, v225\n"
"v_exp_f32_e32 v226, v226\n"
"v_exp_f32_e32 v227, v227\n"
"v_cvt_pk_bf16_f32 v136, v220, v221\n"
"v_cvt_pk_bf16_f32 v137, v222, v223\n"
"v_cvt_pk_bf16_f32 v138, v224, v225\n"
"v_cvt_pk_bf16_f32 v139, v226, v227\n"
"v_mfma_f32_16x16x32_bf16 v[148:151], v[88:91], v[112:115], v[148:151]\n"
"v_and_b32_e32 v212, v112, v212\n"
"v_and_b32_e32 v213, v113, v213\n"
"v_and_b32_e32 v214, v114, v214\n"
"v_and_b32_e32 v215, v115, v215\n"
"v_mfma_f32_16x16x32_bf16 v[168:171], v[88:91], v[136:139], v[168:171]\n"
"v_xor_b32_e32 v144, v112, v212\n"
"v_xor_b32_e32 v145, v113, v213\n"
"v_xor_b32_e32 v146, v114, v214\n"
"v_xor_b32_e32 v147, v115, v215\n"
"v_mfma_f32_16x16x32_bf16 v[156:159], v[172:175], v[212:215], v[156:159]\n"
"v_and_b32_e32 v232, v136, v232\n"
"v_and_b32_e32 v233, v137, v233\n"
"v_mfma_f32_16x16x32_bf16 v[124:127], v[140:143], v[212:215], v[124:127]\n"
"v_and_b32_e32 v234, v138, v234\n"
"v_and_b32_e32 v235, v139, v235\n"
"v_mfma_f32_16x16x32_bf16 v[156:159], v[116:119], v[144:147], v[156:159]\n"
"v_xor_b32_e32 v112, v136, v232\n"
"v_xor_b32_e32 v113, v137, v233\n"
"v_mfma_f32_16x16x32_bf16 v[124:127], v[192:195], v[144:147], v[124:127]\n"
"v_xor_b32_e32 v114, v138, v234\n"
"v_xor_b32_e32 v115, v139, v235\n"
"v_mfma_f32_16x16x32_bf16 v[164:167], v[172:175], v[232:235], v[164:167]\n"
"buffer_load_dwordx4 v[136:139], v1, s[4:7], s97 offen offset:3072 sc1\n"
"s_add_u32 s97, s58, 0x40000\n"
"buffer_load_dwordx4 v[144:147], v0, s[4:7], s97 offen sc1\n"
"v_mfma_f32_16x16x32_bf16 v[160:163], v[140:143], v[232:235], v[160:163]\n"
"buffer_load_dwordx4 v[140:143], v0, s[4:7], s97 offen offset:1024 sc1\n"
"v_mfma_f32_16x16x32_bf16 v[164:167], v[116:119], v[112:115], v[164:167]\n"
"buffer_load_dwordx4 v[116:119], v0, s[4:7], s97 offen offset:2048 sc1\n"
"v_mfma_f32_16x16x32_bf16 v[160:163], v[192:195], v[112:115], v[160:163]\n"
"buffer_load_dwordx4 v[112:115], v0, s[4:7], s97 offen offset:3072 sc1\n"
"v_max_f32_e32 v248, v148, v168\n"
"v_rcp_f32_e32 v148, v148\n"
"v_rcp_f32_e32 v168, v168\n"
"s_nop 0\n"
"v_max3_f32 v248, v248, v148, v168\n"
"v_cmp_nlt_f32_e32 vcc, v248, v252\n"
"s_cbranch_vccnz .Lmk_b24\n"
".Lmk_k29:\n"
"v_mul_f32_e32 v156, v156, v148\n"
"v_mul_f32_e32 v157, v157, v148\n"
"v_mul_f32_e32 v158, v158, v148\n"
"v_mul_f32_e32 v159, v159, v148\n"
"v_mul_f32_e32 v124, v124, v148\n"
"v_mul_f32_e32 v125, v125, v148\n"
"v_mul_f32_e32 v126, v126, v148\n"
"v_mul_f32_e32 v127, v127, v148\n"
"v_cvt_pk_bf16_f32 v88, v156, v157\n"
"v_cvt_pk_bf16_f32 v89, v158, v159\n"
"v_cvt_pk_bf16_f32 v90, v124, v125\n"
"v_cvt_pk_bf16_f32 v91, v126, v127\n"
"buffer_store_dwordx4 v[88:91], v0, s[4:7], s60 offen sc1\n"
"s_add_u32 s98, s60, 0x8000\n"
"v_mul_f32_e32 v164, v164, v168\n"
"v_mul_f32_e32 v165, v165, v168\n"
"v_mul_f32_e32 v166, v166, v168\n"
"v_mul_f32_e32 v167, v167, v168\n"
"v_mul_f32_e32 v160, v160, v168\n"
"v_mul_f32_e32 v161, v161, v168\n"
"v_mul_f32_e32 v162, v162, v168\n"
"v_mul_f32_e32 v163, v163, v168\n"
"v_cvt_pk_bf16_f32 v88, v164, v165\n"
"v_cvt_pk_bf16_f32 v89, v166, v167\n"
"v_cvt_pk_bf16_f32 v90, v160, v161\n"
"v_cvt_pk_bf16_f32 v91, v162, v163\n"
"buffer_store_dwordx4 v[88:91], v0, s[4:7], s98 offen sc1\n"
";\n"
"s_waitcnt vmcnt(20)\n"
"; @A G2 b.0\n"
"s_nop 0\n"
"v_mfma_f32_16x16x32_bf16 v[88:91], a[128:131], v[176:179], 0\n"
"v_mfma_f32_16x16x32_bf16 v[88:91], a[132:135], v[200:203], v[88:91]\n"
"s_waitcnt vmcnt(18)\n"
"v_mfma_f32_16x16x32_bf16 v[88:91], a[136:139], v[188:191], v[88:91]\n"
"buffer_load_dwordx4 v[92:95], v1, s[4:7], s97 offen sc1\n"
"v_mfma_f32_16x16x32_bf16 v[88:91], a[140:143], v[204:207], v[88:91]\n"
"s_waitcnt vmcnt(17)\n"
"v_mfma_f32_16x16x32_bf16 v[88:91], a[144:147], v[152:155], v[88:91]\n"
"v_mfma_f32_16x16x32_bf16 v[88:91], a[148:151], v[208:211], v[88:91]\n"
"s_waitcnt vmcnt(15)\n"
"v_mfma_f32_16x16x32_bf16 v[88:91], a[152:155], v[120:123], v[88:91]\n"
"buffer_load_dwordx4 v[96:99], v1, s[4:7], s97 offen offset:1024 sc1\n"
"v_mfma_f32_16x16x32_bf16 v[88:91], a[156:159], v[132:135], v[88:91]\n"
"v_mfma_f32_16x16x32_bf16 v[124:127], a[188:191], v[132:135], 0\n"
"v_mfma_f32_16x16x32_bf16 v[124:127], a[184:187], v[120:123], v[124:127]\n"
"v_mfma_f32_16x16x32_bf16 v[124:127], a[180:183], v[208:211], v[124:127]\n"
"buffer_load_dwordx4 v[148:151], v1, s[4:7], s97 offen offset:2048 sc1\n"
"v_mfma_f32_16x16x32_bf16 v[124:127], a[176:179], v[152:155], v[124:127]\n"
"v_mfma_f32_16x16x32_bf16 v[124:127], a[172:175], v[204:207], v[124:127]\n"
"s_nop 1\n"
"v_cmp_u_f32_e32 vcc, v88, v88\n"
"v_mfma_f32_16x16x32_bf16 v[124:127], a[168:171], v[188:191], v[124:127]\n"
"s_cbranch_vccnz .Lmk_p30s\n"
".Lmk_p30r:\n"
"v_mfma_f32_16x16x32_bf16 v[124:127], a[164:167], v[200:203], v[124:127]\n"
"ds_write_b128 v3, v[88:91]\n"
"v_mfma_f32_16x16x32_bf16 v[124:127], a[160:163], v[176:179], v[124:127]\n"
"buffer_load_dwordx4 v[88:91], v1, s[4:7], s97 offen offset:3072 sc1\n"
"v_mfma_f32_16x16x32_bf16 v[156:159], a[192:195], v[176:179], 0\n"
"v_mfma_f32_16x16x32_bf16 v[156:159], a[196:199], v[200:203], v[156:159]\n"
"s_add_u32 s98, s59, 0x40000\n"
"v_mfma_f32_16x16x32_bf16 v[156:159], a[200:203], v[188:191], v[156:159]\n"
"v_mfma_f32_16x16x32_bf16 v[156:159], a[204:207], v[204:207], v[156:159]\n"
"buffer_load_dwordx4 v[160:163], v0, s[4:7], s98 offen sc1\n"
"v_mfma_f32_16x16x32_bf16 v[156:159], a[208:211], v[152:155], v[156:159]\n"
"v_mfma_f32_16x16x32_bf16 v[156:159], a[212:215], v[208:211], v[156:159]\n"
"ds_write_b128 v3, v[124:127] offset:1024\n"
"v_mfma_f32_16x16x32_bf16 v[156:159], a[216:219], v[120:123], v[156:159]\n"
"v_mfma_f32_16x16x32_bf16 v[156:159], a[220:223], v[132:135], v[156:159]\n"
"buffer_load_dwordx4 v[124:127], v0, s[4:7], s98 offen offset:1024 sc1\n"
"v_mfma_f32_16x16x32_bf16 v[164:167], a[252:255], v[132:135], 0\n"
"v_mfma_f32_16x16x32_bf16 v[164:167], a[248:251], v[120:123], v[164:167]\n"
"v_mfma_f32_16x16x32_bf16 v[164:167], a[244:247], v[208:211], v[164:167]\n"
"v_mfma_f32_16x16x32_bf16 v[164:167], a[240:243], v[152:155], v[164:167]\n"
"buffer_load_dwordx4 v[120:123], v0, s[4:7], s98 offen offset:2048 sc1\n"
"v_mfma_f32_16x16x32_bf16 v[164:167], a[236:239], v[204:207], v[164:167]\n"
"v_mfma_f32_16x16x32_bf16 v[164:167], a[232:235], v[188:191], v[164:167]\n"
"ds_write_b128 v3, v[156:159] offset:2048\n"
"v_mfma_f32_16x16x32_bf16 v[164:167], a[228:231], v[200:203], v[164:167]\n"
"v_mfma_f32_16x16x32_bf16 v[164:167], a[224:227], v[176:179], v[164:167]\n"
"s_waitcnt vmcnt(17)\n"
"buffer_load_dwordx4 v[132:135], v0, s[4:7], s98 offen offset:3072 sc1\n"
"; @A G2 b.1\n"
"v_mfma_f32_16x16x32_bf16 v[152:155], a[128:131], v[216:219], 0\n"
"v_mfma_f32_16x16x32_bf16 v[152:155], a[132:135], v[128:131], v[152:155]\n"
"v_mfma_f32_16x16x32_bf16 v[152:155], a[136:139], v[180:183], v[152:155]\n"
"v_mfma_f32_16x16x32_bf16 v[152:155], a[140:143], v[100:103], v[152:155]\n"
"s_waitcnt vmcnt(14)\n"
"buffer_load_dwordx4 v[156:159], v1, s[4:7], s98 offen sc1\n"
"v_mfma_f32_16x16x32_bf16 v[152:155], a[144:147], v[184:187], v[152:155]\n"
"v_mfma_f32_16x16x32_bf16 v[152:155], a[148:151], v[104:107], v[152:155]\n"
"ds_write_b128 v3, v[164:167] offset:3072\n"
"v_mfma_f32_16x16x32_bf16 v[152:155], a[152:155], v[108:111], v[152:155]\n"
"v_mfma_f32_16x16x32_bf16 v[152:155], a[156:159], v[136:139], v[152:155]\n"
"buffer_load_dwordx4 v[164:167], v1, s[4:7], s98 offen offset:1024 sc1\n"
"v_mfma_f32_16x16x32_bf16 v[168:171], a[188:191], v[136:139], 0\n"
"v_mfma_f32_16x16x32_bf16 v[168:171], a[184:187], v[108:111], v[168:171]\n"
"v_mfma_f32_16x16x32_bf16 v[168:171], a[180:183], v[104:107], v[168:171]\n"
"v_mfma_f32_16x16x32_bf16 v[168:171], a[176:179], v[184:187], v[168:171]\n"
"buffer_load_dwordx4 v[172:175], v1, s[4:7], s98 offen offset:2048 sc1\n"
"v_mfma_f32_16x16x32_bf16 v[168:171], a[172:175], v[100:103], v[168:171]\n"
"s_nop 0\n"
"v_cmp_u_f32_e32 vcc, v152, v152\n"
"v_mfma_f32_16x16x32_bf16 v[168:171], a[168:171], v[180:183], v[168:171]\n"
"s_cbranch_vccnz .Lmk_p31s\n"
".Lmk_p31r:\n"
"v_mfma_f32_16x16x32_bf16 v[168:171], a[164:167], v[128:131], v[168:171]\n"
"ds_write_b128 v3, v[152:155] offset:6144\n"
"v_mfma_f32_16x16x32_bf16 v[168:171], a[160:163], v[216:219], v[168:171]\n"
"buffer_load_dwordx4 v[152:155], v1, s[4:7], s98 offen offset:3072 sc1\n"
"v_mfma_f32_16x16x32_bf16 v[176:179], a[192:195], v[216:219], 0\n"
"v_mfma_f32_16x16x32_bf16 v[176:179], a[196:199], v[128:131], v[176:179]\n"
"v_mfma_f32_16x16x32_bf16 v[176:179], a[200:203], v[180:183], v[176:179]\n"
"v_mfma_f32_16x16x32_bf16 v[176:179], a[204:207], v[100:103], v[176:179]\n"
"v_mfma_f32_16x16x32_bf16 v[176:179], a[208:211], v[184:187], v[176:179]\n"
"ds_read_b128 v[188:191], v2 offset:8192\n"
"ds_read_b128 v[192:195], v2 offset:9216\n"
"v_mfma_f32_16x16x32_bf16 v[176:179], a[212:215], v[104:107], v[176:179]\n"
"ds_write_b128 v3, v[168:171] offset:7168\n"
"v_mfma_f32_16x16x32_bf16 v[176:179], a[216:219], v[108:111], v[176:179]\n"
"v_mfma_f32_16x16x32_bf16 v[176:179], a[220:223], v[136:139], v[176:179]\n"
"v_mfma_f32_16x16x32_bf16 v[168:171], a[252:255], v[136:139], 0\n"
"s_mov_b32 s98, .Lmk_b0-.Lmk_g34\n"
"s_getpc_b64 s[84:85]\n"
".Lmk_g34:\n"
"s_add_u32 s84, s84, s98\n"
"s_addc_u32 s85, s85, 0\n"
"s_and_b32 s84, s84, -16\n"
"v_mfma_f32_16x16x32_bf16 v[168:171], a[248:251], v[108:111], v[168:171]\n"
"s_and_b32 s85, s85, 0xffff\n"
"s_sub_u32 s98, .Lmk_end-.Lmk_g34, s98\n"
"s_min_u32 s86, s98, 0x3000\n"
"s_mov_b32 s87, 0x20000\n"
"v_mfma_f32_16x16x32_bf16 v[168:171], a[244:247], v[104:107], v[168:171]\n"
"s_mov_b32 m0, 0x27800\n"
"s_nop 0\n"
"buffer_load_dwordx4 v0, s[84:87], s99 offen lds\n"
"v_mfma_f32_16x16x32_bf16 v[168:171], a[240:243], v[184:187], v[168:171]\n"
"s_waitcnt lgkmcnt(1)\n"
"v_lshlrev_b32_e32 v255, 16, v188\n"
"v_and_b32_e32 v254, 0xffff0000, v188\n"
"v_mfma_f32_16x16x32_bf16 v[168:171], a[236:239], v[100:103], v[168:171]\n"
"v_lshlrev_b32_e32 v253, 16, v189\n"
"v_and_b32_e32 v252, 0xffff0000, v189\n"
"v_mfma_f32_16x16x32_bf16 v[168:171], a[232:235], v[180:183], v[168:171]\n"
"ds_write_b128 v3, v[176:179] offset:8192\n"
"v_mfma_f32_16x16x32_bf16 v[168:171], a[228:231], v[128:131], v[168:171]\n"
"v_lshlrev_b32_e32 v251, 16, v190\n"
"v_and_b32_e32 v250, 0xffff0000, v190\n"
"v_mfma_f32_16x16x32_bf16 v[168:171], a[224:227], v[216:219], v[168:171]\n"
"s_waitcnt vmcnt(15)\n"
"v_lshlrev_b32_e32 v249, 16, v191\n"
"v_and_b32_e32 v248, 0xffff0000, v191\n"
"; @A G2 b.2\n"
"v_mfma_f32_16x16x32_bf16 v[100:103], a[128:131], v[144:147], 0\n"
"v_lshlrev_b32_e32 v247, 16, v192\n"
"v_and_b32_e32 v246, 0xffff0000, v192\n"
"v_mfma_f32_16x16x32_bf16 v[100:103], a[132:135], v[140:143], v[100:103]\n"
"v_lshlrev_b32_e32 v245, 16, v193\n"
"v_and_b32_e32 v244, 0xffff0000, v193\n"
"v_mfma_f32_16x16x32_bf16 v[100:103], a[136:139], v[116:119], v[100:103]\n"
"v_lshlrev_b32_e32 v243, 16, v194\n"
"v_and_b32_e32 v242, 0xffff0000, v194\n"
"v_mfma_f32_16x16x32_bf16 v[100:103], a[140:143], v[112:115], v[100:103]\n"
"s_waitcnt vmcnt(9)\n"
"v_lshlrev_b32_e32 v241, 16, v195\n"
"v_and_b32_e32 v240, 0xffff0000, v195\n"
"v_mfma_f32_16x16x32_bf16 v[100:103], a[144:147], v[92:95], v[100:103]\n"
"v_mfma_f32_16x16x32_bf16 v[100:103], a[148:151], v[96:99], v[100:103]\n"
"ds_write_b128 v3, v[168:171] offset:9216\n"
"v_mfma_f32_16x16x32_bf16 v[100:103], a[152:155], v[148:151], v[100:103]\n"
"v_mfma_f32_16x16x32_bf16 v[100:103], a[156:159], v[88:91], v[100:103]\n"
"v_mfma_f32_16x16x32_bf16 v[104:107], a[188:191], v[88:91], 0\n"
"v_mfma_f32_16x16x32_bf16 v[104:107], a[184:187], v[148:151], v[104:107]\n"
"s_waitcnt lgkmcnt(0)\n"
"s_barrier\n"
"v_mfma_f32_16x16x32_bf16 v[104:107], a[180:183], v[96:99], v[104:107]\n"
"ds_read_b128 v[108:111], v4\n"
"ds_read_b128 v[128:131], v5\n"
"v_mfma_f32_16x16x32_bf16 v[104:107], a[176:179], v[92:95], v[104:107]\n"
"v_mfma_f32_16x16x32_bf16 v[104:107], a[172:175], v[112:115], v[104:107]\n"
"s_add_u32 s98, s56, 0\n"
"v_cmp_u_f32_e32 vcc, v100, v100\n"
"v_mfma_f32_16x16x32_bf16 v[104:107], a[168:171], v[116:119], v[104:107]\n"
"s_cbranch_vccnz .Lmk_p32s\n"
".Lmk_p32r:\n"
"v_mfma_f32_16x16x32_bf16 v[104:107], a[164:167], v[140:143], v[104:107]\n"
"ds_write_b128 v3, v[100:103] offset:12288\n"
"v_mfma_f32_16x16x32_bf16 v[104:107], a[160:163], v[144:147], v[104:107]\n"
"buffer_load_dwordx4 v[100:103], v0, s[4:7], s98 offen sc1\n"
"v_mfma_f32_16x16x32_bf16 v[136:139], a[192:195], v[144:147], 0\n"
"ds_read_b128 v[168:171], v4 offset:1024\n"
"ds_read_b128 v[176:179], v5 offset:1024\n"
"v_mfma_f32_16x16x32_bf16 v[136:139], a[196:199], v[140:143], v[136:139]\n"
"s_waitcnt lgkmcnt(3)\n"
"v_add_f32_e32 v108, v108, v255\n"
"v_add_f32_e32 v109, v109, v254\n"
"v_mfma_f32_16x16x32_bf16 v[136:139], a[200:203], v[116:119], v[136:139]\n"
"buffer_load_dwordx4 v[180:183], v0, s[4:7], s98 offen offset:1024 sc1\n"
"v_mfma_f32_16x16x32_bf16 v[136:139], a[204:207], v[112:115], v[136:139]\n"
"v_add_f32_e32 v110, v110, v253\n"
"v_add_f32_e32 v111, v111, v252\n"
"v_mfma_f32_16x16x32_bf16 v[136:139], a[208:211], v[92:95], v[136:139]\n"
"v_add_f32_e32 v108, v108, v128\n"
"v_add_f32_e32 v109, v109, v129\n"
"v_mfma_f32_16x16x32_bf16 v[136:139], a[212:215], v[96:99], v[136:139]\n"
"ds_write_b128 v3, v[104:107] offset:13312\n"
"v_mfma_f32_16x16x32_bf16 v[136:139], a[216:219], v[148:151], v[136:139]\n"
"buffer_load_dwordx4 v[104:107], v0, s[4:7], s98 offen offset:2048 sc1\n"
"v_mfma_f32_16x16x32_bf16 v[136:139], a[220:223], v[88:91], v[136:139]\n"
"ds_read_b128 v[184:187], v4 offset:2048\n"
"ds_read_b128 v[188:191], v5 offset:2048\n"
"v_mfma_f32_16x16x32_bf16 v[192:195], a[252:255], v[88:91], 0\n"
"s_waitcnt lgkmcnt(3)\n"
"v_add_f32_e32 v110, v110, v130\n"
"v_add_f32_e32 v111, v111, v131\n"
"v_mfma_f32_16x16x32_bf16 v[192:195], a[248:251], v[148:151], v[192:195]\n"
"buffer_load_dwordx4 v[88:91], v0, s[4:7], s98 offen offset:3072 sc1\n"
"v_mfma_f32_16x16x32_bf16 v[192:195], a[244:247], v[96:99], v[192:195]\n"
"v_add_f32_e32 v168, v168, v251\n"
"v_add_f32_e32 v169, v169, v250\n"
"v_mfma_f32_16x16x32_bf16 v[192:195], a[240:243], v[92:95], v[192:195]\n"
"v_add_f32_e32 v170, v170, v249\n"
"v_add_f32_e32 v171, v171, v248\n"
"v_mfma_f32_16x16x32_bf16 v[192:195], a[236:239], v[112:115], v[192:195]\n"
"buffer_load_dwordx4 v[92:95], v1, s[4:7], s98 offen sc1\n"
"v_mfma_f32_16x16x32_bf16 v[192:195], a[232:235], v[116:119], v[192:195]\n"
"ds_write_b128 v3, v[136:139] offset:14336\n"
"v_mfma_f32_16x16x32_bf16 v[192:195], a[228:231], v[140:143], v[192:195]\n"
"ds_read_b128 v[96:99], v4 offset:3072\n"
"ds_read_b128 v[112:115], v5 offset:3072\n"
"v_mfma_f32_16x16x32_bf16 v[192:195], a[224:227], v[144:147], v[192:195]\n"
"s_waitcnt vmcnt(10)\n"
"buffer_load_dwordx4 v[116:119], v1, s[4:7], s98 offen offset:1024 sc1\n"
"; @A G2 b.3\n"
"v_mfma_f32_16x16x32_bf16 v[128:131], a[128:131], v[160:163], v[108:111]\n"
"v_add_f32_e32 v168, v168, v176\n"
"v_add_f32_e32 v169, v169, v177\n"
"v_mfma_f32_16x16x32_bf16 v[128:131], a[132:135], v[124:127], v[128:131]\n"
"s_waitcnt lgkmcnt(3)\n"
"v_add_f32_e32 v170, v170, v178\n"
"v_add_f32_e32 v171, v171, v179\n"
"v_mfma_f32_16x16x32_bf16 v[128:131], a[136:139], v[120:123], v[128:131]\n"
"buffer_load_dwordx4 v[136:139], v1, s[4:7], s98 offen offset:2048 sc1\n"
"v_mfma_f32_16x16x32_bf16 v[128:131], a[140:143], v[132:135], v[128:131]\n"
"s_waitcnt vmcnt(8)\n"
"v_add_f32_e32 v184, v184, v247\n"
"v_add_f32_e32 v185, v185, v246\n"
"v_mfma_f32_16x16x32_bf16 v[128:131], a[144:147], v[156:159], v[128:131]\n"
"v_add_f32_e32 v186, v186, v245\n"
"v_add_f32_e32 v187, v187, v244\n"
"v_mfma_f32_16x16x32_bf16 v[128:131], a[148:151], v[164:167], v[128:131]\n"
"ds_write_b128 v3, v[192:195] offset:15360\n"
"v_mfma_f32_16x16x32_bf16 v[128:131], a[152:155], v[172:175], v[128:131]\n"
"buffer_load_dwordx4 v[140:143], v1, s[4:7], s98 offen offset:3072 sc1\n"
"v_mfma_f32_16x16x32_bf16 v[128:131], a[156:159], v[152:155], v[128:131]\n"
"s_add_u32 s98, s57, 0\n"
"v_add_f32_e32 v184, v184, v188\n"
"v_add_f32_e32 v185, v185, v189\n"
"v_mfma_f32_16x16x32_bf16 v[144:147], a[188:191], v[152:155], v[168:171]\n"
"s_waitcnt lgkmcnt(1)\n"
"v_add_f32_e32 v186, v186, v190\n"
"v_add_f32_e32 v187, v187, v191\n"
"v_mfma_f32_16x16x32_bf16 v[144:147], a[184:187], v[172:175], v[144:147]\n"
"s_waitcnt lgkmcnt(0)\n"
"s_barrier\n"
"v_mfma_f32_16x16x32_bf16 v[144:147], a[180:183], v[164:167], v[144:147]\n"
"buffer_load_dwordx4 v[148:151], v0, s[4:7], s98 offen sc1\n"
"v_mfma_f32_16x16x32_bf16 v[144:147], a[176:179], v[156:159], v[144:147]\n"
"v_add_f32_e32 v96, v96, v243\n"
"v_add_f32_e32 v97, v97, v242\n"
"v_mfma_f32_16x16x32_bf16 v[144:147], a[172:175], v[132:135], v[144:147]\n"
"v_cmp_u_f32_e32 vcc, v128, v128\n"
"v_add_f32_e32 v98, v98, v241\n"
"v_mfma_f32_16x16x32_bf16 v[144:147], a[168:171], v[120:123], v[144:147]\n"
"s_cbranch_vccnz .Lmk_p33s\n"
".Lmk_p33r:\n"
"v_add_f32_e32 v99, v99, v240\n"
"v_mfma_f32_16x16x32_bf16 v[144:147], a[164:167], v[124:127], v[144:147]\n"
"buffer_load_dwordx4 v[108:111], v0, s[4:7], s98 offen offset:1024 sc1\n"
"v_mfma_f32_16x16x32_bf16 v[144:147], a[160:163], v[160:163], v[144:147]\n"
"v_add_f32_e32 v96, v96, v112\n"
"v_add_f32_e32 v97, v97, v113\n"
"v_mfma_f32_16x16x32_bf16 v[184:187], a[192:195], v[160:163], v[184:187]\n"
"v_add_f32_e32 v98, v98, v114\n"
"v_add_f32_e32 v99, v99, v115\n"
"v_mfma_f32_16x16x32_bf16 v[184:187], a[196:199], v[124:127], v[184:187]\n"
"buffer_load_dwordx4 v[112:115], v0, s[4:7], s98 offen offset:2048 sc1\n"
"v_mfma_f32_16x16x32_bf16 v[184:187], a[200:203], v[120:123], v[184:187]\n"
"ds_read_b128 v[168:171], v6\n"
"ds_read_b128 v[176:179], v6 offset:1024\n"
"v_mfma_f32_16x16x32_bf16 v[184:187], a[204:207], v[132:135], v[184:187]\n"
"ds_read_b128 v[188:191], v6 offset:2048\n"
"ds_read_b128 v[192:195], v6 offset:3072\n"
"v_mfma_f32_16x16x32_bf16 v[184:187], a[208:211], v[156:159], v[184:187]\n"
"buffer_load_dwordx4 v[196:199], v0, s[4:7], s98 offen offset:3072 sc1\n"
"v_mfma_f32_16x16x32_bf16 v[184:187], a[212:215], v[164:167], v[184:187]\n"
"v_mfma_f32_16x16x32_bf16 v[184:187], a[216:219], v[172:175], v[184:187]\n"
"v_mfma_f32_16x16x32_bf16 v[184:187], a[220:223], v[152:155], v[184:187]\n"
"buffer_load_dwordx4 v[200:203], v1, s[4:7], s98 offen sc1\n"
"v_mfma_f32_16x16x32_bf16 v[96:99], a[252:255], v[152:155], v[96:99]\n"
"s_waitcnt lgkmcnt(0)\n"
"v_add_f32_e32 v168, v128, v168\n"
"v_add_f32_e32 v169, v129, v169\n"
"v_mfma_f32_16x16x32_bf16 v[96:99], a[248:251], v[172:175], v[96:99]\n"
"v_add_f32_e32 v170, v130, v170\n"
"v_add_f32_e32 v171, v131, v171\n"
"v_mfma_f32_16x16x32_bf16 v[96:99], a[244:247], v[164:167], v[96:99]\n"
"buffer_load_dwordx4 v[128:131], v1, s[4:7], s98 offen offset:1024 sc1\n"
"v_mfma_f32_16x16x32_bf16 v[96:99], a[240:243], v[156:159], v[96:99]\n"
"v_add_f32_e32 v176, v144, v176\n"
"v_add_f32_e32 v177, v145, v177\n"
"v_mfma_f32_16x16x32_bf16 v[96:99], a[236:239], v[132:135], v[96:99]\n"
"v_add_f32_e32 v178, v146, v178\n"
"v_add_f32_e32 v179, v147, v179\n"
"v_mfma_f32_16x16x32_bf16 v[96:99], a[232:235], v[120:123], v[96:99]\n"
"buffer_load_dwordx4 v[120:123], v1, s[4:7], s98 offen offset:2048 sc1\n"
"v_mfma_f32_16x16x32_bf16 v[96:99], a[228:231], v[124:127], v[96:99]\n"
"v_cvt_pk_bf16_f32 v124, v168, v169\n"
"v_cvt_pk_bf16_f32 v125, v170, v171\n"
"v_mfma_f32_16x16x32_bf16 v[96:99], a[224:227], v[160:163], v[96:99]\n"
"s_add_u32 s97, s61, 0x440000\n"
"v_cvt_pk_bf16_f32 v126, v176, v177\n"
"v_cvt_pk_bf16_f32 v127, v178, v179\n"
"buffer_store_dwordx4 v[124:127], v0, s[4:7], s97 offen sc1\n"
"ds_write_b128 v2, v[124:127] offset:8192\n"
"buffer_load_dwordx4 v[132:135], v1, s[4:7], s98 offen offset:3072 sc1\n"
"; @A fin b\n"
"s_add_u32 s98, s61, 0x440000\n"
"v_add_f32_e32 v188, v184, v188\n"
"v_add_f32_e32 v189, v185, v189\n"
"v_add_f32_e32 v190, v186, v190\n"
"v_add_f32_e32 v191, v187, v191\n"
"v_add_f32_e32 v192, v96, v192\n"
"v_add_f32_e32 v193, v97, v193\n"
"v_add_f32_e32 v194, v98, v194\n"
"v_add_f32_e32 v195, v99, v195\n"
"v_cvt_pk_bf16_f32 v96, v188, v189\n"
"v_cvt_pk_bf16_f32 v97, v190, v191\n"
"v_cvt_pk_bf16_f32 v98, v192, v193\n"
"v_cvt_pk_bf16_f32 v99, v194, v195\n"
"buffer_store_dwordx4 v[96:99], v0, s[4:7], s98 offen offset:1024 sc1\n"
"s_waitcnt vmcnt(16)\n"
"; @A G2 a.0\n"
"v_mfma_f32_16x16x32_bf16 v[144:147], a[128:131], v[100:103], 0\n"
"s_add_u32 s98, s58, 0\n"
"buffer_load_dwordx4 v[152:155], v0, s[4:7], s98 offen sc1\n"
"v_mfma_f32_16x16x32_bf16 v[144:147], a[132:135], v[180:183], v[144:147]\n"
"s_waitcnt vmcnt(15)\n"
"ds_write_b128 v2, v[96:99] offset:9216\n"
"v_mfma_f32_16x16x32_bf16 v[144:147], a[136:139], v[104:107], v[144:147]\n"
"v_mov_b32_e32 v156, 0x3f803f80\n"
"v_mov_b32_e32 v157, 0x3f803f80\n"
"v_mfma_f32_16x16x32_bf16 v[144:147], a[140:143], v[88:91], v[144:147]\n"
"s_waitcnt vmcnt(13)\n"
"v_mov_b32_e32 v158, 0x3f803f80\n"
"v_mov_b32_e32 v159, 0x3f803f80\n"
"v_mfma_f32_16x16x32_bf16 v[144:147], a[144:147], v[92:95], v[144:147]\n"
"buffer_load_dwordx4 v[160:163], v0, s[4:7], s98 offen offset:1024 sc1\n"
"v_mfma_f32_16x16x32_bf16 v[164:167], v[156:159], v[124:127], 0\n"
"v_mfma_f32_16x16x32_bf16 v[164:167], v[156:159], v[96:99], v[164:167]\n"
"v_mfma_f32_16x16x32_bf16 v[156:159], v[124:127], v[124:127], 0\n"
"v_mfma_f32_16x16x32_bf16 v[156:159], v[96:99], v[96:99], v[156:159]\n"
"v_mfma_f32_16x16x32_bf16 v[144:147], a[148:151], v[116:119], v[144:147]\n"
"s_waitcnt vmcnt(12)\n"
"v_mfma_f32_16x16x32_bf16 v[144:147], a[152:155], v[136:139], v[144:147]\n"
"v_mfma_f32_16x16x32_bf16 v[144:147], a[156:159], v[140:143], v[144:147]\n"
"v_mfma_f32_16x16x32_bf16 v[96:99], a[188:191], v[140:143], 0\n"
"buffer_load_dwordx4 v[124:127], v0, s[4:7], s98 offen offset:2048 sc1\n"
"v_mfma_f32_16x16x32_bf16 v[96:99], a[184:187], v[136:139], v[96:99]\n"
"s_mov_b32 s96, 0xaaaaaaaa\n"
"s_mov_b32 s97, 0xaaaaaaaa\n"
"v_mfma_f32_16x16x32_bf16 v[96:99], a[180:183], v[116:119], v[96:99]\n"
"v_mfma_f32_16x16x32_bf16 v[96:99], a[176:179], v[92:95], v[96:99]\n"
"v_mfma_f32_16x16x32_bf16 v[96:99], a[172:175], v[88:91], v[96:99]\n"
"v_cmp_u_f32_e32 vcc, v144, v144\n"
"v_cndmask_b32_e64 v156, v156, v157, s[96:97]\n"
"v_mfma_f32_16x16x32_bf16 v[96:99], a[168:171], v[104:107], v[96:99]\n"
"s_cbranch_vccnz .Lmk_p35s\n"
".Lmk_p35r:\n"
"v_cndmask_b32_e64 v158, v158, v159, s[96:97]\n"
"v_mfma_f32_16x16x32_bf16 v[96:99], a[164:167], v[180:183], v[96:99]\n"
"ds_write_b128 v3, v[144:147]\n"
"v_mfma_f32_16x16x32_bf16 v[96:99], a[160:163], v[100:103], v[96:99]\n"
"buffer_load_dwordx4 v[144:147], v0, s[4:7], s98 offen offset:3072 sc1\n"
"v_mfma_f32_16x16x32_bf16 v[168:171], a[192:195], v[100:103], 0\n"
"s_mov_b32 s96, 0xcccccccc\n"
"s_mov_b32 s97, 0xcccccccc\n"
"v_mfma_f32_16x16x32_bf16 v[168:171], a[196:199], v[180:183], v[168:171]\n"
"v_cndmask_b32_e64 v165, v156, v158, s[96:97]\n"
"v_mfma_f32_16x16x32_bf16 v[168:171], a[200:203], v[104:107], v[168:171]\n"
"s_mov_b32 s96, 0xf0000f\n"
"s_mov_b32 s97, 0xf0000f00\n"
"v_mfma_f32_16x16x32_bf16 v[168:171], a[204:207], v[88:91], v[168:171]\n"
"buffer_load_dwordx4 v[156:159], v1, s[4:7], s98 offen sc1\n"
"v_mfma_f32_16x16x32_bf16 v[168:171], a[208:211], v[92:95], v[168:171]\n"
"v_cndmask_b32_e64 v164, v164, v165, s[96:97]\n"
"s_add_u32 s97, s63, 0x44000\n"
"v_mfma_f32_16x16x32_bf16 v[168:171], a[212:215], v[116:119], v[168:171]\n"
"ds_write_b128 v3, v[96:99] offset:1024\n"
"v_mfma_f32_16x16x32_bf16 v[168:171], a[216:219], v[136:139], v[168:171]\n"
"buffer_store_dword v164, v14, s[4:7], s97 offen sc1\n"
"v_mfma_f32_16x16x32_bf16 v[168:171], a[220:223], v[140:143], v[168:171]\n"
"buffer_load_dwordx4 v[164:167], v1, s[4:7], s98 offen offset:1024 sc1\n"
"v_mfma_f32_16x16x32_bf16 v[96:99], a[252:255], v[140:143], 0\n"
"v_mfma_f32_16x16x32_bf16 v[96:99], a[248:251], v[136:139], v[96:99]\n"
"v_mfma_f32_16x16x32_bf16 v[96:99], a[244:247], v[116:119], v[96:99]\n"
"v_mfma_f32_16x16x32_bf16 v[96:99], a[240:243], v[92:95], v[96:99]\n"
"buffer_load_dwordx4 v[116:119], v1, s[4:7], s98 offen offset:2048 sc1\n"
"v_mfma_f32_16x16x32_bf16 v[96:99], a[236:239], v[88:91], v[96:99]\n"
"v_mfma_f32_16x16x32_bf16 v[96:99], a[232:235], v[104:107], v[96:99]\n"
"ds_write_b128 v3, v[168:171] offset:2048\n"
"v_mfma_f32_16x16x32_bf16 v[96:99], a[228:231], v[180:183], v[96:99]\n"
"v_mfma_f32_16x16x32_bf16 v[96:99], a[224:227], v[100:103], v[96:99]\n"
"s_waitcnt vmcnt(14)\n"
"buffer_load_dwordx4 v[136:139], v1, s[4:7], s98 offen offset:3072 sc1\n"
"; @A G2 a.1\n"
"v_mfma_f32_16x16x32_bf16 v[88:91], a[128:131], v[148:151], 0\n"
"v_mfma_f32_16x16x32_bf16 v[88:91], a[132:135], v[108:111], v[88:91]\n"
"s_add_u32 s98, s59, 0\n"
"v_mfma_f32_16x16x32_bf16 v[88:91], a[136:139], v[112:115], v[88:91]\n"
"v_mfma_f32_16x16x32_bf16 v[88:91], a[140:143], v[196:199], v[88:91]\n"
"s_waitcnt vmcnt(10)\n"
"buffer_load_dwordx4 v[172:175], v0, s[4:7], s98 offen sc1\n"
"v_mfma_f32_16x16x32_bf16 v[88:91], a[144:147], v[200:203], v[88:91]\n"
"v_mfma_f32_16x16x32_bf16 v[88:91], a[148:151], v[128:131], v[88:91]\n"
"ds_write_b128 v3, v[96:99] offset:3072\n"
"v_mfma_f32_16x16x32_bf16 v[88:91], a[152:155], v[120:123], v[88:91]\n"
"v_mfma_f32_16x16x32_bf16 v[88:91], a[156:159], v[132:135], v[88:91]\n"
"buffer_load_dwordx4 v[176:179], v0, s[4:7], s98 offen offset:1024 sc1\n"
"v_mfma_f32_16x16x32_bf16 v[92:95], a[188:191], v[132:135], 0\n"
"v_mfma_f32_16x16x32_bf16 v[92:95], a[184:187], v[120:123], v[92:95]\n"
"v_mfma_f32_16x16x32_bf16 v[92:95], a[180:183], v[128:131], v[92:95]\n"
"v_mfma_f32_16x16x32_bf16 v[92:95], a[176:179], v[200:203], v[92:95]\n"
"buffer_load_dwordx4 v[168:171], v0, s[4:7], s98 offen offset:2048 sc1\n"
"v_mfma_f32_16x16x32_bf16 v[92:95], a[172:175], v[196:199], v[92:95]\n"
"s_nop 0\n"
"v_cmp_u_f32_e32 vcc, v88, v88\n"
"v_mfma_f32_16x16x32_bf16 v[92:95], a[168:171], v[112:115], v[92:95]\n"
"s_cbranch_vccnz .Lmk_p36s\n"
".Lmk_p36r:\n"
"v_mfma_f32_16x16x32_bf16 v[92:95], a[164:167], v[108:111], v[92:95]\n"
"ds_write_b128 v3, v[88:91] offset:6144\n"
"v_mfma_f32_16x16x32_bf16 v[92:95], a[160:163], v[148:151], v[92:95]\n"
"buffer_load_dwordx4 v[180:183], v0, s[4:7], s98 offen offset:3072 sc1\n"
"v_mfma_f32_16x16x32_bf16 v[88:91], a[192:195], v[148:151], 0\n"
"v_mfma_f32_16x16x32_bf16 v[88:91], a[196:199], v[108:111], v[88:91]\n"
"v_mfma_f32_16x16x32_bf16 v[88:91], a[200:203], v[112:115], v[88:91]\n"
"v_mfma_f32_16x16x32_bf16 v[88:91], a[204:207], v[196:199], v[88:91]\n"
"buffer_load_dwordx4 v[184:187], v1, s[4:7], s98 offen sc1\n"
"v_mfma_f32_16x16x32_bf16 v[88:91], a[208:211], v[200:203], v[88:91]\n"
"ds_read_b128 v[96:99], v2\n"
"ds_read_b128 v[100:103], v2 offset:1024\n"
"v_mfma_f32_16x16x32_bf16 v[88:91], a[212:215], v[128:131], v[88:91]\n"
"ds_write_b128 v3, v[92:95] offset:7168\n"
"v_mfma_f32_16x16x32_bf16 v[88:91], a[216:219], v[120:123], v[88:91]\n"
"v_mfma_f32_16x16x32_bf16 v[88:91], a[220:223], v[132:135], v[88:91]\n"
"buffer_load_dwordx4 v[188:191], v1, s[4:7], s98 offen offset:1024 sc1\n"
"v_mfma_f32_16x16x32_bf16 v[92:95], a[252:255], v[132:135], 0\n"
"v_mfma_f32_16x16x32_bf16 v[92:95], a[248:251], v[120:123], v[92:95]\n"
"s_waitcnt lgkmcnt(1)\n"
"v_lshlrev_b32_e32 v255, 16, v96\n"
"v_and_b32_e32 v254, 0xffff0000, v96\n"
"v_mfma_f32_16x16x32_bf16 v[92:95], a[244:247], v[128:131], v[92:95]\n"
"v_lshlrev_b32_e32 v253, 16, v97\n"
"v_and_b32_e32 v252, 0xffff0000, v97\n"
"v_mfma_f32_16x16x32_bf16 v[92:95], a[240:243], v[200:203], v[92:95]\n"
"buffer_load_dwordx4 v[192:195], v1, s[4:7], s98 offen offset:2048 sc1\n"
"v_mfma_f32_16x16x32_bf16 v[92:95], a[236:239], v[196:199], v[92:95]\n"
"v_lshlrev_b32_e32 v251, 16, v98\n"
"v_and_b32_e32 v250, 0xffff0000, v98\n"
"v_mfma_f32_16x16x32_bf16 v[92:95], a[232:235], v[112:115], v[92:95]\n"
"ds_write_b128 v3, v[88:91] offset:8192\n"
"v_mfma_f32_16x16x32_bf16 v[92:95], a[228:231], v[108:111], v[92:95]\n"
"v_lshlrev_b32_e32 v249, 16, v99\n"
"v_and_b32_e32 v248, 0xffff0000, v99\n"
"v_mfma_f32_16x16x32_bf16 v[92:95], a[224:227], v[148:151], v[92:95]\n"
"s_waitcnt vmcnt(12)\n"
"buffer_load_dwordx4 v[196:199], v1, s[4:7], s98 offen offset:3072 sc1\n"
"; @A G2 a.2\n"
"v_mfma_f32_16x16x32_bf16 v[96:99], a[128:131], v[152:155], 0\n"
"v_lshlrev_b32_e32 v247, 16, v100\n"
"v_and_b32_e32 v246, 0xffff0000, v100\n"
"v_mfma_f32_16x16x32_bf16 v[96:99], a[132:135], v[160:163], v[96:99]\n"
"v_lshlrev_b32_e32 v245, 16, v101\n"
"v_and_b32_e32 v244, 0xffff0000, v101\n"
"v_mfma_f32_16x16x32_bf16 v[96:99], a[136:139], v[124:127], v[96:99]\n"
"v_lshlrev_b32_e32 v243, 16, v102\n"
"v_and_b32_e32 v242, 0xffff0000, v102\n"
"v_mfma_f32_16x16x32_bf16 v[96:99], a[140:143], v[144:147], v[96:99]\n"
"s_waitcnt vmcnt(8)\n"
"v_lshlrev_b32_e32 v241, 16, v103\n"
"v_and_b32_e32 v240, 0xffff0000, v103\n"
"v_mfma_f32_16x16x32_bf16 v[96:99], a[144:147], v[156:159], v[96:99]\n"
"v_mfma_f32_16x16x32_bf16 v[96:99], a[148:151], v[164:167], v[96:99]\n"
"ds_write_b128 v3, v[92:95] offset:9216\n"
"v_mfma_f32_16x16x32_bf16 v[96:99], a[152:155], v[116:119], v[96:99]\n"
"v_mfma_f32_16x16x32_bf16 v[96:99], a[156:159], v[136:139], v[96:99]\n"
"v_mfma_f32_16x16x32_bf16 v[104:107], a[188:191], v[136:139], 0\n"
"s_add_u32 s98, s52, 0x440000\n"
"buffer_load_dwordx4 v[88:91], v0, s[4:7], s98 offen sc1\n"
"v_mfma_f32_16x16x32_bf16 v[104:107], a[184:187], v[116:119], v[104:107]\n"
"s_waitcnt lgkmcnt(0)\n"
"s_barrier\n"
"v_mfma_f32_16x16x32_bf16 v[104:107], a[180:183], v[164:167], v[104:107]\n"
"ds_read_b128 v[140:143], v4\n"
"ds_read_b128 v[112:115], v5\n"
"v_mfma_f32_16x16x32_bf16 v[104:107], a[176:179], v[156:159], v[104:107]\n"
"buffer_load_dwordx4 v[92:95], v0, s[4:7], s98 offen offset:1024 sc1\n"
"v_mfma_f32_16x16x32_bf16 v[104:107], a[172:175], v[144:147], v[104:107]\n"
"v_cmp_u_f32_e32 vcc, v96, v96\n"
"v_mfma_f32_16x16x32_bf16 v[104:107], a[168:171], v[124:127], v[104:107]\n"
"s_cbranch_vccnz .Lmk_p37s\n"
".Lmk_p37r:\n"
"v_mfma_f32_16x16x32_bf16 v[104:107], a[164:167], v[160:163], v[104:107]\n"
"ds_write_b128 v3, v[96:99] offset:12288\n"
"v_mfma_f32_16x16x32_bf16 v[104:107], a[160:163], v[152:155], v[104:107]\n"
"buffer_load_dwordx4 v[96:99], v0, s[4:7], s98 offen offset:2048 sc1\n"
"v_mfma_f32_16x16x32_bf16 v[120:123], a[192:195], v[152:155], 0\n"
"ds_read_b128 v[148:151], v4 offset:1024\n"
"ds_read_b128 v[128:131], v5 offset:1024\n"
"v_mfma_f32_16x16x32_bf16 v[120:123], a[196:199], v[160:163], v[120:123]\n"
"s_waitcnt lgkmcnt(3)\n"
"v_add_f32_e32 v140, v140, v255\n"
"v_add_f32_e32 v141, v141, v254\n"
"v_mfma_f32_16x16x32_bf16 v[120:123], a[200:203], v[124:127], v[120:123]\n"
"buffer_load_dwordx4 v[100:103], v0, s[4:7], s98 offen offset:3072 sc1\n"
"v_mfma_f32_16x16x32_bf16 v[120:123], a[204:207], v[144:147], v[120:123]\n"
"v_add_f32_e32 v142, v142, v253\n"
"v_add_f32_e32 v143, v143, v252\n"
"v_mfma_f32_16x16x32_bf16 v[120:123], a[208:211], v[156:159], v[120:123]\n"
"v_add_f32_e32 v140, v140, v112\n"
"v_add_f32_e32 v141, v141, v113\n"
"v_mfma_f32_16x16x32_bf16 v[120:123], a[212:215], v[164:167], v[120:123]\n"
"ds_write_b128 v3, v[104:107] offset:13312\n"
"v_mfma_f32_16x16x32_bf16 v[120:123], a[216:219], v[116:119], v[120:123]\n"
"buffer_load_dwordx4 v[104:107], v1, s[4:7], s98 offen sc1\n"
"v_mfma_f32_16x16x32_bf16 v[120:123], a[220:223], v[136:139], v[120:123]\n"
"ds_read_b128 v[108:111], v4 offset:2048\n"
"ds_read_b128 v[200:203], v5 offset:2048\n"
"v_mfma_f32_16x16x32_bf16 v[132:135], a[252:255], v[136:139], 0\n"
"s_waitcnt lgkmcnt(3)\n"
"v_add_f32_e32 v142, v142, v114\n"
"v_add_f32_e32 v143, v143, v115\n"
"v_mfma_f32_16x16x32_bf16 v[132:135], a[248:251], v[116:119], v[132:135]\n"
"buffer_load_dwordx4 v[112:115], v1, s[4:7], s98 offen offset:1024 sc1\n"
"v_mfma_f32_16x16x32_bf16 v[132:135], a[244:247], v[164:167], v[132:135]\n"
"v_add_f32_e32 v148, v148, v251\n"
"v_add_f32_e32 v149, v149, v250\n"
"v_mfma_f32_16x16x32_bf16 v[132:135], a[240:243], v[156:159], v[132:135]\n"
"v_add_f32_e32 v150, v150, v249\n"
"v_add_f32_e32 v151, v151, v248\n"
"v_mfma_f32_16x16x32_bf16 v[132:135], a[236:239], v[144:147], v[132:135]\n"
"buffer_load_dwordx4 v[116:119], v1, s[4:7], s98 offen offset:2048 sc1\n"
"v_mfma_f32_16x16x32_bf16 v[132:135], a[232:235], v[124:127], v[132:135]\n"
"ds_write_b128 v3, v[120:123] offset:14336\n"
"v_mfma_f32_16x16x32_bf16 v[132:135], a[228:231], v[160:163], v[132:135]\n"
"ds_read_b128 v[120:123], v4 offset:3072\n"
"ds_read_b128 v[144:147], v5 offset:3072\n"
"v_mfma_f32_16x16x32_bf16 v[132:135], a[224:227], v[152:155], v[132:135]\n"
"s_waitcnt vmcnt(11)\n"
"buffer_load_dwordx4 v[124:127], v1, s[4:7], s98 offen offset:3072 sc1\n"
"; @A G2 a.3\n"
"v_mfma_f32_16x16x32_bf16 v[164:167], a[128:131], v[172:175], v[140:143]\n"
"s_add_u32 s98, s53, 0x440000\n"
"v_add_f32_e32 v148, v148, v128\n"
"v_add_f32_e32 v149, v149, v129\n"
"v_mfma_f32_16x16x32_bf16 v[164:167], a[132:135], v[176:179], v[164:167]\n"
"s_waitcnt lgkmcnt(3)\n"
"v_add_f32_e32 v150, v150, v130\n"
"v_add_f32_e32 v151, v151, v131\n"
"v_mfma_f32_16x16x32_bf16 v[164:167], a[136:139], v[168:171], v[164:167]\n"
"buffer_load_dwordx4 v[128:131], v0, s[4:7], s98 offen sc1\n"
"v_mfma_f32_16x16x32_bf16 v[164:167], a[140:143], v[180:183], v[164:167]\n"
"s_waitcnt vmcnt(9)\n"
"v_add_f32_e32 v108, v108, v247\n"
"v_add_f32_e32 v109, v109, v246\n"
"v_mfma_f32_16x16x32_bf16 v[164:167], a[144:147], v[184:187], v[164:167]\n"
"v_add_f32_e32 v110, v110, v245\n"
"v_add_f32_e32 v111, v111, v244\n"
"v_mfma_f32_16x16x32_bf16 v[164:167], a[148:151], v[188:191], v[164:167]\n"
"ds_write_b128 v3, v[132:135] offset:15360\n"
"v_mfma_f32_16x16x32_bf16 v[164:167], a[152:155], v[192:195], v[164:167]\n"
"buffer_load_dwordx4 v[132:135], v0, s[4:7], s98 offen offset:1024 sc1\n"
"v_mfma_f32_16x16x32_bf16 v[164:167], a[156:159], v[196:199], v[164:167]\n"
"v_add_f32_e32 v108, v108, v200\n"
"v_add_f32_e32 v109, v109, v201\n"
"v_mfma_f32_16x16x32_bf16 v[204:207], a[188:191], v[196:199], v[148:151]\n"
"s_waitcnt lgkmcnt(1)\n"
"v_add_f32_e32 v110, v110, v202\n"
"v_add_f32_e32 v111, v111, v203\n"
"v_mfma_f32_16x16x32_bf16 v[204:207], a[184:187], v[192:195], v[204:207]\n"
"s_waitcnt lgkmcnt(0)\n"
"s_barrier\n"
"v_mfma_f32_16x16x32_bf16 v[204:207], a[180:183], v[188:191], v[204:207]\n"
"buffer_load_dwordx4 v[136:139], v0, s[4:7], s98 offen offset:2048 sc1\n"
"v_mfma_f32_16x16x32_bf16 v[204:207], a[176:179], v[184:187], v[204:207]\n"
"v_add_f32_e32 v120, v120, v243\n"
"v_add_f32_e32 v121, v121, v242\n"
"v_mfma_f32_16x16x32_bf16 v[204:207], a[172:175], v[180:183], v[204:207]\n"
"v_cmp_u_f32_e32 vcc, v164, v164\n"
"v_add_f32_e32 v122, v122, v241\n"
"v_mfma_f32_16x16x32_bf16 v[204:207], a[168:171], v[168:171], v[204:207]\n"
"s_cbranch_vccnz .Lmk_p38s\n"
".Lmk_p38r:\n"
"v_add_f32_e32 v123, v123, v240\n"
"v_mfma_f32_16x16x32_bf16 v[204:207], a[164:167], v[176:179], v[204:207]\n"
"buffer_load_dwordx4 v[140:143], v0, s[4:7], s98 offen offset:3072 sc1\n"
"v_mfma_f32_16x16x32_bf16 v[204:207], a[160:163], v[172:175], v[204:207]\n"
"v_add_f32_e32 v120, v120, v144\n"
"v_add_f32_e32 v121, v121, v145\n"
"v_mfma_f32_16x16x32_bf16 v[108:111], a[192:195], v[172:175], v[108:111]\n"
"v_add_f32_e32 v122, v122, v146\n"
"v_add_f32_e32 v123, v123, v147\n"
"v_mfma_f32_16x16x32_bf16 v[108:111], a[196:199], v[176:179], v[108:111]\n"
"buffer_load_dwordx4 v[144:147], v1, s[4:7], s98 offen sc1\n"
"v_mfma_f32_16x16x32_bf16 v[108:111], a[200:203], v[168:171], v[108:111]\n"
"ds_read_b128 v[200:203], v6\n"
"ds_read_b128 v[208:211], v6 offset:1024\n"
"v_mfma_f32_16x16x32_bf16 v[108:111], a[204:207], v[180:183], v[108:111]\n"
"ds_read_b128 v[148:151], v6 offset:2048\n"
"ds_read_b128 v[152:155], v6 offset:3072\n"
"v_mfma_f32_16x16x32_bf16 v[108:111], a[208:211], v[184:187], v[108:111]\n"
"buffer_load_dwordx4 v[156:159], v1, s[4:7], s98 offen offset:1024 sc1\n"
"v_mfma_f32_16x16x32_bf16 v[108:111], a[212:215], v[188:191], v[108:111]\n"
"v_mfma_f32_16x16x32_bf16 v[108:111], a[216:219], v[192:195], v[108:111]\n"
"v_mfma_f32_16x16x32_bf16 v[108:111], a[220:223], v[196:199], v[108:111]\n"
"buffer_load_dwordx4 v[160:163], v1, s[4:7], s98 offen offset:2048 sc1\n"
"v_mfma_f32_16x16x32_bf16 v[120:123], a[252:255], v[196:199], v[120:123]\n"
"s_waitcnt lgkmcnt(0)\n"
"v_add_f32_e32 v200, v164, v200\n"
"v_add_f32_e32 v201, v165, v201\n"
"v_mfma_f32_16x16x32_bf16 v[120:123], a[248:251], v[192:195], v[120:123]\n"
"v_add_f32_e32 v202, v166, v202\n"
"v_add_f32_e32 v203, v167, v203\n"
"v_mfma_f32_16x16x32_bf16 v[120:123], a[244:247], v[188:191], v[120:123]\n"
"buffer_load_dwordx4 v[164:167], v1, s[4:7], s98 offen offset:3072 sc1\n"
"v_mfma_f32_16x16x32_bf16 v[120:123], a[240:243], v[184:187], v[120:123]\n"
"v_add_f32_e32 v208, v204, v208\n"
"v_add_f32_e32 v209, v205, v209\n"
"v_mfma_f32_16x16x32_bf16 v[120:123], a[236:239], v[180:183], v[120:123]\n"
"v_add_f32_e32 v210, v206, v210\n"
"v_add_f32_e32 v211, v207, v211\n"
"v_mfma_f32_16x16x32_bf16 v[120:123], a[232:235], v[168:171], v[120:123]\n"
"v_cvt_pk_bf16_f32 v168, v200, v201\n"
"v_cvt_pk_bf16_f32 v169, v202, v203\n"
"v_mfma_f32_16x16x32_bf16 v[120:123], a[228:231], v[176:179], v[120:123]\n"
"s_add_u32 s98, s61, 0x400000\n"
"v_cvt_pk_bf16_f32 v170, v208, v209\n"
"v_cvt_pk_bf16_f32 v171, v210, v211\n"
"v_mfma_f32_16x16x32_bf16 v[120:123], a[224:227], v[172:175], v[120:123]\n"
"buffer_store_dwordx4 v[168:171], v0, s[4:7], s98 offen sc1\n"
"ds_write_b128 v2, v[168:171]\n"
".Lmk_loop:\n"
".Lmk_b0:\n"
"s_waitcnt vmcnt(13)\n"
"; @B G1 0.0\n"
"v_mfma_f32_16x16x32_bf16 v[172:175], a[0:3], v[88:91], 0\n"
"v_mfma_f32_16x16x32_bf16 v[172:175], a[4:7], v[92:95], v[172:175]\n"
"s_add_u32 s98, s62, 0x44000\n"
"v_mfma_f32_16x16x32_bf16 v[172:175], a[8:11], v[96:99], v[172:175]\n"
"s_add_u32 s97, s54, 0x440000\n"
"buffer_load_dwordx4 v[176:179], v0, s[4:7], s97 offen sc1\n"
"v_mfma_f32_16x16x32_bf16 v[172:175], a[12:15], v[100:103], v[172:175]\n"
"s_waitcnt vmcnt(10)\n"
"v_mfma_f32_16x16x32_bf16 v[172:175], a[16:19], v[104:107], v[172:175]\n"
"buffer_load_dword v255, v10, s[4:7], s98 offen sc1\n"
"v_mfma_f32_16x16x32_bf16 v[172:175], a[20:23], v[112:115], v[172:175]\n"
"v_add_f32_e32 v148, v108, v148\n"
"v_add_f32_e32 v149, v109, v149\n"
"v_mfma_f32_16x16x32_bf16 v[172:175], a[24:27], v[116:119], v[172:175]\n"
"v_add_f32_e32 v150, v110, v150\n"
"v_add_f32_e32 v151, v111, v151\n"
"v_mfma_f32_16x16x32_bf16 v[172:175], a[28:31], v[124:127], v[172:175]\n"
"buffer_load_dwordx4 v[108:111], v0, s[4:7], s97 offen offset:1024 sc1\n"
"v_mfma_f32_16x16x32_bf16 v[180:183], a[60:63], v[124:127], 0\n"
"buffer_load_dword v254, v10, s[4:7], s98 offen offset:256 sc1\n"
"v_mfma_f32_16x16x32_bf16 v[180:183], a[56:59], v[116:119], v[180:183]\n"
"v_add_f32_e32 v152, v120, v152\n"
"v_add_f32_e32 v153, v121, v153\n"
"v_mfma_f32_16x16x32_bf16 v[180:183], a[52:55], v[112:115], v[180:183]\n"
"v_add_f32_e32 v154, v122, v154\n"
"v_add_f32_e32 v155, v123, v155\n"
"v_mfma_f32_16x16x32_bf16 v[180:183], a[48:51], v[104:107], v[180:183]\n"
"buffer_load_dword v253, v10, s[4:7], s98 offen offset:512 sc1\n"
"v_mfma_f32_16x16x32_bf16 v[180:183], a[44:47], v[100:103], v[180:183]\n"
"v_cmp_u_f32_e32 vcc, v172, v172\n"
"v_cvt_pk_bf16_f32 v120, v148, v149\n"
"v_mfma_f32_16x16x32_bf16 v[180:183], a[40:43], v[96:99], v[180:183]\n"
"s_cbranch_vccnz .Lmk_p43s\n"
".Lmk_p43r:\n"
"v_cvt_pk_bf16_f32 v121, v150, v151\n"
"v_mfma_f32_16x16x32_bf16 v[180:183], a[36:39], v[92:95], v[180:183]\n"
"buffer_load_dwordx4 v[148:151], v0, s[4:7], s97 offen offset:2048 sc1\n"
"v_mfma_f32_16x16x32_bf16 v[180:183], a[32:35], v[88:91], v[180:183]\n"
"ds_write_b128 v3, v[172:175]\n"
"v_mfma_f32_16x16x32_bf16 v[172:175], a[64:67], v[88:91], 0\n"
"buffer_load_dword v252, v10, s[4:7], s98 offen offset:768 sc1\n"
"v_mfma_f32_16x16x32_bf16 v[172:175], a[68:71], v[92:95], v[172:175]\n"
"buffer_load_dwordx4 v[184:187], v0, s[4:7], s97 offen offset:3072 sc1\n"
"v_mfma_f32_16x16x32_bf16 v[172:175], a[72:75], v[96:99], v[172:175]\n"
"s_add_u32 s96, s61, 0x400000\n"
"v_cvt_pk_bf16_f32 v122, v152, v153\n"
"v_cvt_pk_bf16_f32 v123, v154, v155\n"
"v_mfma_f32_16x16x32_bf16 v[172:175], a[76:79], v[100:103], v[172:175]\n"
"buffer_load_dword v251, v10, s[4:7], s98 offen offset:1024 sc1\n"
"v_mfma_f32_16x16x32_bf16 v[172:175], a[80:83], v[104:107], v[172:175]\n"
"buffer_store_dwordx4 v[120:123], v0, s[4:7], s96 offen offset:1024 sc1\n"
"v_mfma_f32_16x16x32_bf16 v[172:175], a[84:87], v[112:115], v[172:175]\n"
"ds_write_b128 v3, v[180:183] offset:1024\n"
"v_mfma_f32_16x16x32_bf16 v[172:175], a[88:91], v[116:119], v[172:175]\n"
"buffer_load_dwordx4 v[152:155], v1, s[4:7], s97 offen sc1\n"
"v_mfma_f32_16x16x32_bf16 v[172:175], a[92:95], v[124:127], v[172:175]\n"
"buffer_load_dword v250, v10, s[4:7], s98 offen offset:1280 sc1\n"
"v_mfma_f32_16x16x32_bf16 v[180:183], a[124:127], v[124:127], 0\n"
"ds_write_b128 v2, v[120:123] offset:1024\n"
"v_mfma_f32_16x16x32_bf16 v[180:183], a[120:123], v[116:119], v[180:183]\n"
"v_mov_b32_e32 v188, 0x3f803f80\n"
"v_mov_b32_e32 v189, 0x3f803f80\n"
"v_mfma_f32_16x16x32_bf16 v[180:183], a[116:119], v[112:115], v[180:183]\n"
"buffer_load_dword v249, v10, s[4:7], s98 offen offset:1536 sc1\n"
"v_mfma_f32_16x16x32_bf16 v[180:183], a[112:115], v[104:107], v[180:183]\n"
"buffer_load_dwordx4 v[192:195], v1, s[4:7], s97 offen offset:1024 sc1\n"
"v_mfma_f32_16x16x32_bf16 v[180:183], a[108:111], v[100:103], v[180:183]\n"
"v_mov_b32_e32 v190, 0x3f803f80\n"
"v_mov_b32_e32 v191, 0x3f803f80\n"
"v_mfma_f32_16x16x32_bf16 v[180:183], a[104:107], v[96:99], v[180:183]\n"
"ds_write_b128 v3, v[172:175] offset:2048\n"
"v_mfma_f32_16x16x32_bf16 v[172:175], v[188:191], v[168:171], 0\n"
"v_mfma_f32_16x16x32_bf16 v[172:175], v[188:191], v[120:123], v[172:175]\n"
"v_mfma_f32_16x16x32_bf16 v[188:191], v[168:171], v[168:171], 0\n"
"v_mfma_f32_16x16x32_bf16 v[188:191], v[120:123], v[120:123], v[188:191]\n"
"v_mfma_f32_16x16x32_bf16 v[180:183], a[100:103], v[92:95], v[180:183]\n"
"v_mov_b32_e32 v120, -1\n"
"v_mov_b32_e32 v121, -1\n"
"v_mfma_f32_16x16x32_bf16 v[180:183], a[96:99], v[88:91], v[180:183]\n"
"v_mov_b32_e32 v122, -1\n"
"v_mov_b32_e32 v123, -1\n"
"v_mfma_f32_16x16x32_bf16 v[168:171], v[24:27], v[88:91], 0\n"
"buffer_load_dwordx4 v[196:199], v1, s[4:7], s97 offen offset:2048 sc1\n"
"v_mfma_f32_16x16x32_bf16 v[168:171], v[28:31], v[92:95], v[168:171]\n"
"s_add_u32 s96, s60, 0x448000\n"
"buffer_store_dwordx4 v[120:123], v0, s[4:7], s96 offen sc1\n"
"v_mfma_f32_16x16x32_bf16 v[168:171], v[32:35], v[96:99], v[168:171]\n"
"buffer_load_dword v248, v10, s[4:7], s98 offen offset:1792 sc1\n"
"v_mfma_f32_16x16x32_bf16 v[168:171], v[36:39], v[100:103], v[168:171]\n"
"s_mov_b32 s94, 0xaaaaaaaa\n"
"s_mov_b32 s95, 0xaaaaaaaa\n"
"v_mfma_f32_16x16x32_bf16 v[168:171], v[40:43], v[104:107], v[168:171]\n"
"v_mfma_f32_16x16x32_bf16 v[168:171], v[44:47], v[112:115], v[168:171]\n"
"buffer_load_dwordx4 v[200:203], v1, s[4:7], s97 offen offset:3072 sc1\n"
"v_mfma_f32_16x16x32_bf16 v[168:171], v[48:51], v[116:119], v[168:171]\n"
"ds_write_b128 v3, v[180:183] offset:3072\n"
"v_mfma_f32_16x16x32_bf16 v[168:171], v[52:55], v[124:127], v[168:171]\n"
"v_cndmask_b32_e64 v188, v188, v189, s[94:95]\n"
"v_cndmask_b32_e64 v190, v190, v191, s[94:95]\n"
"v_mfma_f32_16x16x32_bf16 v[180:183], v[84:87], v[124:127], 0\n"
"s_mov_b32 s94, 0xcccccccc\n"
"s_mov_b32 s95, 0xcccccccc\n"
"v_mfma_f32_16x16x32_bf16 v[180:183], v[80:83], v[116:119], v[180:183]\n"
"v_cndmask_b32_e64 v173, v188, v190, s[94:95]\n"
"v_mfma_f32_16x16x32_bf16 v[180:183], v[76:79], v[112:115], v[180:183]\n"
"s_mov_b32 s94, 0xf0000f\n"
"s_mov_b32 s95, 0xf0000f00\n"
"v_mfma_f32_16x16x32_bf16 v[180:183], v[72:75], v[104:107], v[180:183]\n"
"s_add_u32 s98, s63, 0x40000\n"
"v_cndmask_b32_e64 v172, v172, v173, s[94:95]\n"
"v_mfma_f32_16x16x32_bf16 v[180:183], v[68:71], v[100:103], v[180:183]\n"
"s_add_u32 s97, s70, 0x200000\n"
"buffer_load_dwordx4 a[128:131], v0, s[4:7], s97 offen sc1\n"
"v_mfma_f32_16x16x32_bf16 v[180:183], v[64:67], v[96:99], v[180:183]\n"
"ds_write_b128 v3, v[168:171] offset:4096\n"
"v_mfma_f32_16x16x32_bf16 v[180:183], v[60:63], v[92:95], v[180:183]\n"
"buffer_store_dword v172, v14, s[4:7], s98 offen sc1\n"
"v_mfma_f32_16x16x32_bf16 v[180:183], v[56:59], v[88:91], v[180:183]\n"
"s_waitcnt vmcnt(25)\n"
"; @B G1 0.1\n"
"v_mfma_f32_16x16x32_bf16 v[88:91], a[0:3], v[128:131], 0\n"
"v_mfma_f32_16x16x32_bf16 v[88:91], a[4:7], v[132:135], v[88:91]\n"
"v_mfma_f32_16x16x32_bf16 v[88:91], a[8:11], v[136:139], v[88:91]\n"
"s_add_u32 s98, s55, 0x440000\n"
"buffer_load_dwordx4 v[92:95], v0, s[4:7], s98 offen sc1\n"
"v_mfma_f32_16x16x32_bf16 v[88:91], a[12:15], v[140:143], v[88:91]\n"
"s_waitcnt vmcnt(22)\n"
"v_mfma_f32_16x16x32_bf16 v[88:91], a[16:19], v[144:147], v[88:91]\n"
"v_mfma_f32_16x16x32_bf16 v[88:91], a[20:23], v[156:159], v[88:91]\n"
"ds_write_b128 v3, v[180:183] offset:5120\n"
"v_mfma_f32_16x16x32_bf16 v[88:91], a[24:27], v[160:163], v[88:91]\n"
"v_mfma_f32_16x16x32_bf16 v[88:91], a[28:31], v[164:167], v[88:91]\n"
"buffer_load_dwordx4 v[96:99], v0, s[4:7], s98 offen offset:1024 sc1\n"
"v_mfma_f32_16x16x32_bf16 v[100:103], a[60:63], v[164:167], 0\n"
"v_mfma_f32_16x16x32_bf16 v[100:103], a[56:59], v[160:163], v[100:103]\n"
"v_mfma_f32_16x16x32_bf16 v[100:103], a[52:55], v[156:159], v[100:103]\n"
"v_mfma_f32_16x16x32_bf16 v[100:103], a[48:51], v[144:147], v[100:103]\n"
"v_mfma_f32_16x16x32_bf16 v[100:103], a[44:47], v[140:143], v[100:103]\n"
"s_nop 1\n"
"v_cmp_u_f32_e32 vcc, v88, v88\n"
"v_mfma_f32_16x16x32_bf16 v[100:103], a[40:43], v[136:139], v[100:103]\n"
"s_cbranch_vccnz .Lmk_p44s\n"
".Lmk_p44r:\n"
"v_mfma_f32_16x16x32_bf16 v[100:103], a[36:39], v[132:135], v[100:103]\n"
"buffer_load_dwordx4 v[104:107], v0, s[4:7], s98 offen offset:2048 sc1\n"
"v_mfma_f32_16x16x32_bf16 v[100:103], a[32:35], v[128:131], v[100:103]\n"
"ds_write_b128 v3, v[88:91] offset:6144\n"
"v_mfma_f32_16x16x32_bf16 v[88:91], a[64:67], v[128:131], 0\n"
"buffer_load_dwordx4 a[132:135], v0, s[4:7], s97 offen offset:1024 sc1\n"
"v_mfma_f32_16x16x32_bf16 v[88:91], a[68:71], v[132:135], v[88:91]\n"
"buffer_load_dwordx4 v[112:115], v0, s[4:7], s98 offen offset:3072 sc1\n"
"v_mfma_f32_16x16x32_bf16 v[88:91], a[72:75], v[136:139], v[88:91]\n"
"v_mfma_f32_16x16x32_bf16 v[88:91], a[76:79], v[140:143], v[88:91]\n"
"v_mfma_f32_16x16x32_bf16 v[88:91], a[80:83], v[144:147], v[88:91]\n"
"v_mfma_f32_16x16x32_bf16 v[88:91], a[84:87], v[156:159], v[88:91]\n"
"ds_write_b128 v3, v[100:103] offset:7168\n"
"v_mfma_f32_16x16x32_bf16 v[88:91], a[88:91], v[160:163], v[88:91]\n"
"buffer_load_dwordx4 v[100:103], v1, s[4:7], s98 offen sc1\n"
"v_mfma_f32_16x16x32_bf16 v[88:91], a[92:95], v[164:167], v[88:91]\n"
"v_mfma_f32_16x16x32_bf16 v[116:119], a[124:127], v[164:167], 0\n"
"v_mfma_f32_16x16x32_bf16 v[116:119], a[120:123], v[160:163], v[116:119]\n"
"v_mfma_f32_16x16x32_bf16 v[116:119], a[116:119], v[156:159], v[116:119]\n"
"v_mfma_f32_16x16x32_bf16 v[116:119], a[112:115], v[144:147], v[116:119]\n"
"buffer_load_dwordx4 v[124:127], v1, s[4:7], s98 offen offset:1024 sc1\n"
"v_mfma_f32_16x16x32_bf16 v[116:119], a[108:111], v[140:143], v[116:119]\n"
"v_mfma_f32_16x16x32_bf16 v[116:119], a[104:107], v[136:139], v[116:119]\n"
"s_nop 0\n"
"ds_write_b128 v3, v[88:91] offset:8192\n"
"v_mfma_f32_16x16x32_bf16 v[116:119], a[100:103], v[132:135], v[116:119]\n"
"v_mfma_f32_16x16x32_bf16 v[116:119], a[96:99], v[128:131], v[116:119]\n"
"v_mfma_f32_16x16x32_bf16 v[88:91], v[24:27], v[128:131], 0\n"
"buffer_load_dwordx4 v[168:171], v1, s[4:7], s98 offen offset:2048 sc1\n"
"v_mfma_f32_16x16x32_bf16 v[88:91], v[28:31], v[132:135], v[88:91]\n"
"s_add_u32 s96, s60, 0x440000\n"
"v_mfma_f32_16x16x32_bf16 v[88:91], v[32:35], v[136:139], v[88:91]\n"
"v_mfma_f32_16x16x32_bf16 v[88:91], v[36:39], v[140:143], v[88:91]\n"
"buffer_store_dwordx4 v[120:123], v0, s[4:7], s96 offen sc1\n"
"v_mfma_f32_16x16x32_bf16 v[88:91], v[40:43], v[144:147], v[88:91]\n"
"buffer_load_dwordx4 a[136:139], v0, s[4:7], s97 offen offset:2048 sc1\n"
"v_mfma_f32_16x16x32_bf16 v[88:91], v[44:47], v[156:159], v[88:91]\n"
"buffer_load_dwordx4 v[172:175], v1, s[4:7], s98 offen offset:3072 sc1\n"
"v_mfma_f32_16x16x32_bf16 v[88:91], v[48:51], v[160:163], v[88:91]\n"
"ds_write_b128 v3, v[116:119] offset:9216\n"
"v_mfma_f32_16x16x32_bf16 v[88:91], v[52:55], v[164:167], v[88:91]\n"
"s_waitcnt vmcnt(14)\n"
"v_add_f32_e32 v255, v255, v251\n"
"v_add_f32_e32 v254, v254, v250\n"
"v_mfma_f32_16x16x32_bf16 v[116:119], v[84:87], v[164:167], 0\n"
"v_add_f32_e32 v253, v253, v249\n"
"v_add_f32_e32 v252, v252, v248\n"
"v_mfma_f32_16x16x32_bf16 v[116:119], v[80:83], v[160:163], v[116:119]\n"
"v_add_f32_e32 v255, v255, v253\n"
"v_add_f32_e32 v254, v254, v252\n"
"v_mfma_f32_16x16x32_bf16 v[116:119], v[76:79], v[156:159], v[116:119]\n"
"v_add_f32_e32 v255, v255, v254\n"
"v_cmp_u_f32_e32 vcc, v255, v255\n"
"v_mfma_f32_16x16x32_bf16 v[116:119], v[72:75], v[144:147], v[116:119]\n"
"s_cbranch_vccnz .Lmk_m47s\n"
".Lmk_m47r:\n"
"v_mov_b32_e32 v254, v255\n"
"v_mfma_f32_16x16x32_bf16 v[116:119], v[68:71], v[140:143], v[116:119]\n"
"s_nop 0\n"
"v_permlane32_swap_b32_e32 v255, v254\n"
"v_add_f32_e32 v255, v255, v254\n"
"v_mfma_f32_16x16x32_bf16 v[116:119], v[64:67], v[136:139], v[116:119]\n"
"ds_write_b128 v3, v[88:91] offset:10240\n"
"v_mfma_f32_16x16x32_bf16 v[116:119], v[60:63], v[132:135], v[116:119]\n"
"v_mul_f32_e32 v255, 0x3a800000, v255\n"
"v_mov_b32_e32 v254, v255\n"
"v_mfma_f32_16x16x32_bf16 v[116:119], v[56:59], v[128:131], v[116:119]\n"
"s_nop 0\n"
"v_permlane16_swap_b32_e32 v255, v254\n"
"v_cvt_pk_bf16_f32 v253, -v255, 0\n"
"; @B G1 0.2\n"
"v_mfma_f32_16x16x32_bf16 v[88:91], a[0:3], v[176:179], 0\n"
"v_fma_f32 v254, -v255, v255, v254\n"
"v_lshlrev_b32_e32 v253, 16, v253\n"
"v_mfma_f32_16x16x32_bf16 v[88:91], a[4:7], v[108:111], v[88:91]\n"
"v_max_f32_e32 v254, 0, v254\n"
"v_cvt_pk_bf16_f32 v254, v254, 0\n"
"v_mfma_f32_16x16x32_bf16 v[88:91], a[8:11], v[148:151], v[88:91]\n"
"v_lshlrev_b32_e32 v254, 16, v254\n"
"v_add_f32_e32 v254, 0x3727c5ac, v254\n"
"v_mfma_f32_16x16x32_bf16 v[88:91], a[12:15], v[184:187], v[88:91]\n"
"s_waitcnt vmcnt(13)\n"
"v_cvt_pk_bf16_f32 v254, v254, 0\n"
"v_lshlrev_b32_e32 v254, 16, v254\n"
"v_mfma_f32_16x16x32_bf16 v[88:91], a[16:19], v[152:155], v[88:91]\n"
"s_mul_i32 s98, s0, .Lmk_exit-.Lmk_b2\n"
"s_add_u32 s98, s98, .Lmk_b2-.Lmk_g42\n"
"s_getpc_b64 s[84:85]\n"
".Lmk_g42:\n"
"s_add_u32 s84, s84, s98\n"
"s_addc_u32 s85, s85, 0\n"
"v_mfma_f32_16x16x32_bf16 v[88:91], a[20:23], v[192:195], v[88:91]\n"
"ds_write_b128 v3, v[116:119] offset:11264\n"
"v_mfma_f32_16x16x32_bf16 v[88:91], a[24:27], v[196:199], v[88:91]\n"
"s_and_b32 s84, s84, -16\n"
"s_and_b32 s85, s85, 0xffff\n"
"s_sub_u32 s98, .Lmk_end-.Lmk_g42, s98\n"
"s_min_u32 s86, s98, 0x4000\n"
"s_mov_b32 s87, 0x20000\n"
"v_mfma_f32_16x16x32_bf16 v[88:91], a[28:31], v[200:203], v[88:91]\n"
"s_mov_b32 m0, 0x27800\n"
"s_nop 0\n"
"buffer_load_dwordx4 v0, s[84:87], s99 offen lds\n"
"v_mfma_f32_16x16x32_bf16 v[116:119], a[60:63], v[200:203], 0\n"
"buffer_load_dwordx4 a[140:143], v0, s[4:7], s97 offen offset:3072 sc1\n"
"v_mfma_f32_16x16x32_bf16 v[116:119], a[56:59], v[196:199], v[116:119]\n"
"v_sqrt_f32_e32 v254, v254\n"
"v_mfma_f32_16x16x32_bf16 v[116:119], a[52:55], v[192:195], v[116:119]\n"
"s_add_u32 s98, s52, 0x448000\n"
"buffer_load_dwordx4 v[128:131], v0, s[4:7], s98 offen sc1\n"
"v_mfma_f32_16x16x32_bf16 v[116:119], a[48:51], v[152:155], v[116:119]\n"
"s_waitcnt lgkmcnt(0)\n"
"s_barrier\n"
"v_mfma_f32_16x16x32_bf16 v[116:119], a[44:47], v[184:187], v[116:119]\n"
"v_cmp_u_f32_e32 vcc, v88, v88\n"
"v_cvt_pk_bf16_f32 v254, v254, 0\n"
"v_mfma_f32_16x16x32_bf16 v[116:119], a[40:43], v[148:151], v[116:119]\n"
"s_cbranch_vccnz .Lmk_p45s\n"
".Lmk_p45r:\n"
"v_lshlrev_b32_e32 v254, 16, v254\n"
"v_mfma_f32_16x16x32_bf16 v[116:119], a[36:39], v[108:111], v[116:119]\n"
"ds_write_b128 v3, v[88:91] offset:12288\n"
"v_mfma_f32_16x16x32_bf16 v[116:119], a[32:35], v[176:179], v[116:119]\n"
"buffer_load_dwordx4 v[88:91], v0, s[4:7], s98 offen offset:1024 sc1\n"
"v_mfma_f32_16x16x32_bf16 v[132:135], a[64:67], v[176:179], 0\n"
"v_rcp_f32_e32 v255, v254\n"
"v_mfma_f32_16x16x32_bf16 v[132:135], a[68:71], v[108:111], v[132:135]\n"
"ds_read_b128 v[136:139], v4\n"
"v_mul_f32_e32 v254, 0x3e8293ee, v255\n"
"v_mfma_f32_16x16x32_bf16 v[132:135], a[72:75], v[148:151], v[132:135]\n"
"ds_read_b128 v[140:143], v9 offset:640\n"
"ds_read_b128 v[144:147], v5\n"
"v_mfma_f32_16x16x32_bf16 v[132:135], a[76:79], v[184:187], v[132:135]\n"
"ds_read_b128 v[156:159], v4 offset:1024\n"
"ds_read_b128 v[160:163], v9 offset:704\n"
"v_mfma_f32_16x16x32_bf16 v[132:135], a[80:83], v[152:155], v[132:135]\n"
"buffer_load_dwordx4 v[164:167], v0, s[4:7], s98 offen offset:2048 sc1\n"
"v_mfma_f32_16x16x32_bf16 v[132:135], a[84:87], v[192:195], v[132:135]\n"
"ds_write_b128 v3, v[116:119] offset:13312\n"
"v_mfma_f32_16x16x32_bf16 v[132:135], a[88:91], v[196:199], v[132:135]\n"
"ds_read_b128 v[116:119], v5 offset:1024\n"
"v_mfma_f32_16x16x32_bf16 v[132:135], a[92:95], v[200:203], v[132:135]\n"
"s_waitcnt lgkmcnt(4)\n"
"v_fmac_f32_e32 v136, v253, v140\n"
"v_fmac_f32_e32 v137, v253, v141\n"
"v_mfma_f32_16x16x32_bf16 v[180:183], a[124:127], v[200:203], 0\n"
"v_fmac_f32_e32 v138, v253, v142\n"
"v_fmac_f32_e32 v139, v253, v143\n"
"v_mfma_f32_16x16x32_bf16 v[180:183], a[120:123], v[196:199], v[180:183]\n"
"buffer_load_dwordx4 v[140:143], v0, s[4:7], s98 offen offset:3072 sc1\n"
"v_mfma_f32_16x16x32_bf16 v[180:183], a[116:119], v[192:195], v[180:183]\n"
"ds_read_b128 v[188:191], v4 offset:2048\n"
"ds_read_b128 v[204:207], v9 offset:768\n"
"v_mfma_f32_16x16x32_bf16 v[180:183], a[112:115], v[152:155], v[180:183]\n"
"ds_read_b128 v[208:211], v5 offset:2048\n"
"v_add_f32_e32 v136, v136, v144\n"
"v_mfma_f32_16x16x32_bf16 v[180:183], a[108:111], v[184:187], v[180:183]\n"
"buffer_load_dwordx4 a[144:147], v1, s[4:7], s97 offen sc1\n"
"v_mfma_f32_16x16x32_bf16 v[180:183], a[104:107], v[148:151], v[180:183]\n"
"ds_write_b128 v3, v[132:135] offset:14336\n"
"v_mfma_f32_16x16x32_bf16 v[180:183], a[100:103], v[108:111], v[180:183]\n"
"buffer_load_dwordx4 v[132:135], v1, s[4:7], s98 offen sc1\n"
"v_mfma_f32_16x16x32_bf16 v[180:183], a[96:99], v[176:179], v[180:183]\n"
"v_add_f32_e32 v137, v137, v145\n"
"v_add_f32_e32 v138, v138, v146\n"
"v_mfma_f32_16x16x32_bf16 v[212:215], v[24:27], v[176:179], 0\n"
"s_waitcnt lgkmcnt(4)\n"
"v_add_f32_e32 v139, v139, v147\n"
"v_fmac_f32_e32 v156, v253, v160\n"
"v_mfma_f32_16x16x32_bf16 v[212:215], v[28:31], v[108:111], v[212:215]\n"
"ds_read_b128 v[144:147], v4 offset:3072\n"
"ds_read_b128 v[216:219], v9 offset:832\n"
"v_mfma_f32_16x16x32_bf16 v[212:215], v[32:35], v[148:151], v[212:215]\n"
"ds_read_b128 v[220:223], v5 offset:3072\n"
"v_fmac_f32_e32 v157, v253, v161\n"
"v_mfma_f32_16x16x32_bf16 v[212:215], v[36:39], v[184:187], v[212:215]\n"
"buffer_load_dwordx4 v[224:227], v1, s[4:7], s98 offen offset:1024 sc1\n"
"v_mfma_f32_16x16x32_bf16 v[212:215], v[40:43], v[152:155], v[212:215]\n"
"s_add_u32 s96, s60, 0x400000\n"
"v_fmac_f32_e32 v158, v253, v162\n"
"v_fmac_f32_e32 v159, v253, v163\n"
"v_mfma_f32_16x16x32_bf16 v[212:215], v[44:47], v[192:195], v[212:215]\n"
"ds_write_b128 v3, v[180:183] offset:15360\n"
"v_mfma_f32_16x16x32_bf16 v[212:215], v[48:51], v[196:199], v[212:215]\n"
"buffer_store_dwordx4 v[120:123], v0, s[4:7], s96 offen sc1\n"
"v_mfma_f32_16x16x32_bf16 v[212:215], v[52:55], v[200:203], v[212:215]\n"
"v_add_f32_e32 v156, v156, v116\n"
"v_add_f32_e32 v157, v157, v117\n"
"v_mfma_f32_16x16x32_bf16 v[160:163], v[84:87], v[200:203], 0\n"
"buffer_load_dwordx4 v[180:183], v1, s[4:7], s98 offen offset:2048 sc1\n"
"v_mfma_f32_16x16x32_bf16 v[160:163], v[80:83], v[196:199], v[160:163]\n"
"s_add_u32 s96, s68, s80\n"
"s_add_u32 s96, s96, 0x10000\n"
"s_waitcnt vmcnt(22)\n"
"v_mfma_f32_16x16x32_bf16 v[160:163], v[76:79], v[192:195], v[160:163]\n"
"buffer_store_dwordx2 v[18:19], v11, s[4:7], s96 offen sc1\n"
"v_mfma_f32_16x16x32_bf16 v[160:163], v[72:75], v[152:155], v[160:163]\n"
"ds_read_b128 v[152:155], v4 offset:4096\n"
"ds_read_b128 v[192:195], v9 offset:512\n"
"v_mfma_f32_16x16x32_bf16 v[160:163], v[68:71], v[184:187], v[160:163]\n"
"ds_read_b128 v[184:187], v5 offset:4096\n"
"v_add_f32_e32 v158, v158, v118\n"
"v_mfma_f32_16x16x32_bf16 v[160:163], v[64:67], v[148:151], v[160:163]\n"
"buffer_load_dwordx4 v[148:151], v1, s[4:7], s98 offen offset:3072 sc1\n"
"v_mfma_f32_16x16x32_bf16 v[160:163], v[60:63], v[108:111], v[160:163]\n"
"ds_write_b128 v3, v[212:215] offset:16384\n"
"v_mfma_f32_16x16x32_bf16 v[160:163], v[56:59], v[176:179], v[160:163]\n"
"s_waitcnt vmcnt(19)\n"
"ds_read_b128 v[108:111], v4 offset:5120\n"
"ds_read_b128 v[176:179], v9 offset:576\n"
"; @B G1 0.3\n"
"v_mfma_f32_16x16x32_bf16 v[196:199], a[0:3], v[92:95], v[136:139]\n"
"buffer_load_dwordx4 a[148:151], v1, s[4:7], s97 offen offset:1024 sc1\n"
"v_mfma_f32_16x16x32_bf16 v[196:199], a[4:7], v[96:99], v[196:199]\n"
"ds_read_b128 v[200:203], v5 offset:5120\n"
"v_add_f32_e32 v159, v159, v119\n"
"v_mfma_f32_16x16x32_bf16 v[196:199], a[8:11], v[104:107], v[196:199]\n"
"s_add_u32 s98, s53, 0x448000\n"
"buffer_load_dwordx4 v[116:119], v0, s[4:7], s98 offen sc1\n"
"v_mfma_f32_16x16x32_bf16 v[196:199], a[12:15], v[112:115], v[196:199]\n"
"s_waitcnt vmcnt(15)\n"
"s_waitcnt lgkmcnt(12)\n"
"v_fmac_f32_e32 v188, v253, v204\n"
"v_mfma_f32_16x16x32_bf16 v[196:199], a[16:19], v[100:103], v[196:199]\n"
"v_fmac_f32_e32 v189, v253, v205\n"
"v_fmac_f32_e32 v190, v253, v206\n"
"v_mfma_f32_16x16x32_bf16 v[196:199], a[20:23], v[124:127], v[196:199]\n"
"ds_write_b128 v3, v[160:163] offset:17408\n"
"v_mfma_f32_16x16x32_bf16 v[196:199], a[24:27], v[168:171], v[196:199]\n"
"v_fmac_f32_e32 v191, v253, v207\n"
"v_add_f32_e32 v188, v188, v208\n"
"v_mfma_f32_16x16x32_bf16 v[196:199], a[28:31], v[172:175], v[196:199]\n"
"buffer_load_dwordx4 v[160:163], v0, s[4:7], s98 offen offset:1024 sc1\n"
"v_mfma_f32_16x16x32_bf16 v[204:207], a[60:63], v[172:175], v[156:159]\n"
"v_add_f32_e32 v189, v189, v209\n"
"v_add_f32_e32 v190, v190, v210\n"
"v_mfma_f32_16x16x32_bf16 v[204:207], a[56:59], v[168:171], v[204:207]\n"
"s_waitcnt lgkmcnt(9)\n"
"v_add_f32_e32 v191, v191, v211\n"
"v_fmac_f32_e32 v144, v253, v216\n"
"v_mfma_f32_16x16x32_bf16 v[204:207], a[52:55], v[124:127], v[204:207]\n"
"s_waitcnt lgkmcnt(0)\n"
"s_barrier\n"
"v_mfma_f32_16x16x32_bf16 v[204:207], a[48:51], v[100:103], v[204:207]\n"
"ds_read_b128 v[208:211], v6\n"
"ds_read_b128 v[212:215], v6 offset:1024\n"
"v_mfma_f32_16x16x32_bf16 v[204:207], a[44:47], v[112:115], v[204:207]\n"
"v_cmp_u_f32_e32 vcc, v196, v196\n"
"v_fmac_f32_e32 v145, v253, v217\n"
"v_mfma_f32_16x16x32_bf16 v[204:207], a[40:43], v[104:107], v[204:207]\n"
"s_cbranch_vccnz .Lmk_p46s\n"
".Lmk_p46r:\n"
"v_fmac_f32_e32 v146, v253, v218\n"
"v_mfma_f32_16x16x32_bf16 v[204:207], a[36:39], v[96:99], v[204:207]\n"
"buffer_load_dwordx4 v[136:139], v0, s[4:7], s98 offen offset:2048 sc1\n"
"v_mfma_f32_16x16x32_bf16 v[204:207], a[32:35], v[92:95], v[204:207]\n"
"v_fmac_f32_e32 v147, v253, v219\n"
"v_add_f32_e32 v144, v144, v220\n"
"v_mfma_f32_16x16x32_bf16 v[188:191], a[64:67], v[92:95], v[188:191]\n"
"v_add_f32_e32 v145, v145, v221\n"
"v_add_f32_e32 v146, v146, v222\n"
"v_mfma_f32_16x16x32_bf16 v[188:191], a[68:71], v[96:99], v[188:191]\n"
"buffer_load_dwordx4 v[156:159], v0, s[4:7], s98 offen offset:3072 sc1\n"
"v_mfma_f32_16x16x32_bf16 v[188:191], a[72:75], v[104:107], v[188:191]\n"
"v_add_f32_e32 v147, v147, v223\n"
"v_fmac_f32_e32 v152, v253, v192\n"
"v_mfma_f32_16x16x32_bf16 v[188:191], a[76:79], v[112:115], v[188:191]\n"
"v_fmac_f32_e32 v153, v253, v193\n"
"v_fmac_f32_e32 v154, v253, v194\n"
"v_mfma_f32_16x16x32_bf16 v[188:191], a[80:83], v[100:103], v[188:191]\n"
"buffer_load_dwordx4 a[152:155], v1, s[4:7], s97 offen offset:2048 sc1\n"
"v_mfma_f32_16x16x32_bf16 v[188:191], a[84:87], v[124:127], v[188:191]\n"
"s_waitcnt lgkmcnt(0)\n"
"v_fmac_f32_e32 v155, v253, v195\n"
"v_add_f32_e32 v152, v152, v184\n"
"v_mfma_f32_16x16x32_bf16 v[188:191], a[88:91], v[168:171], v[188:191]\n"
"buffer_load_dwordx4 v[192:195], v1, s[4:7], s98 offen sc1\n"
"v_mfma_f32_16x16x32_bf16 v[188:191], a[92:95], v[172:175], v[188:191]\n"
"ds_read_b128 v[216:219], v6 offset:2048\n"
"ds_read_b128 v[220:223], v6 offset:3072\n"
"v_mfma_f32_16x16x32_bf16 v[144:147], a[124:127], v[172:175], v[144:147]\n"
"v_add_f32_e32 v153, v153, v185\n"
"v_add_f32_e32 v154, v154, v186\n"
"v_mfma_f32_16x16x32_bf16 v[144:147], a[120:123], v[168:171], v[144:147]\n"
"v_add_f32_e32 v155, v155, v187\n"
"v_fmac_f32_e32 v108, v253, v176\n"
"v_mfma_f32_16x16x32_bf16 v[144:147], a[116:119], v[124:127], v[144:147]\n"
"v_fmac_f32_e32 v109, v253, v177\n"
"v_fmac_f32_e32 v110, v253, v178\n"
"v_mfma_f32_16x16x32_bf16 v[144:147], a[112:115], v[100:103], v[144:147]\n"
"buffer_load_dwordx4 v[184:187], v1, s[4:7], s98 offen offset:1024 sc1\n"
"v_mfma_f32_16x16x32_bf16 v[144:147], a[108:111], v[112:115], v[144:147]\n"
"v_fmac_f32_e32 v111, v253, v179\n"
"v_add_f32_e32 v108, v108, v200\n"
"v_mfma_f32_16x16x32_bf16 v[144:147], a[104:107], v[104:107], v[144:147]\n"
"v_add_f32_e32 v109, v109, v201\n"
"v_add_f32_e32 v110, v110, v202\n"
"v_mfma_f32_16x16x32_bf16 v[144:147], a[100:103], v[96:99], v[144:147]\n"
"v_add_f32_e32 v111, v111, v203\n"
"v_add_f32_e32 v196, v196, v208\n"
"v_mfma_f32_16x16x32_bf16 v[144:147], a[96:99], v[92:95], v[144:147]\n"
"v_add_f32_e32 v197, v197, v209\n"
"v_add_f32_e32 v198, v198, v210\n"
"v_mfma_f32_16x16x32_bf16 v[152:155], v[24:27], v[92:95], v[152:155]\n"
"buffer_load_dwordx4 v[176:179], v1, s[4:7], s98 offen offset:2048 sc1\n"
"v_mfma_f32_16x16x32_bf16 v[152:155], v[28:31], v[96:99], v[152:155]\n"
"v_add_f32_e32 v199, v199, v211\n"
"v_add_f32_e32 v204, v204, v212\n"
"v_mfma_f32_16x16x32_bf16 v[152:155], v[32:35], v[104:107], v[152:155]\n"
"v_add_f32_e32 v205, v205, v213\n"
"v_add_f32_e32 v206, v206, v214\n"
"v_mfma_f32_16x16x32_bf16 v[152:155], v[36:39], v[112:115], v[152:155]\n"
"v_add_f32_e32 v207, v207, v215\n"
"v_mul_f32_e32 v196, v196, v255\n"
"v_mfma_f32_16x16x32_bf16 v[152:155], v[40:43], v[100:103], v[152:155]\n"
"s_waitcnt lgkmcnt(0)\n"
"v_mul_f32_e32 v197, v197, v255\n"
"v_mul_f32_e32 v198, v198, v255\n"
"v_mfma_f32_16x16x32_bf16 v[152:155], v[44:47], v[124:127], v[152:155]\n"
"buffer_load_dwordx4 v[200:203], v1, s[4:7], s98 offen offset:3072 sc1\n"
"v_mfma_f32_16x16x32_bf16 v[152:155], v[48:51], v[168:171], v[152:155]\n"
"s_add_u32 s98, s60, 0x408000\n"
"ds_read_b128 v[208:211], v6 offset:4096\n"
"ds_read_b128 v[212:215], v6 offset:5120\n"
"v_mfma_f32_16x16x32_bf16 v[152:155], v[52:55], v[172:175], v[152:155]\n"
"v_mul_f32_e32 v199, v199, v255\n"
"v_mul_f32_e32 v204, v204, v255\n"
"v_mfma_f32_16x16x32_bf16 v[108:111], v[84:87], v[172:175], v[108:111]\n"
"buffer_store_dwordx4 v[120:123], v0, s[4:7], s98 offen sc1\n"
"v_mfma_f32_16x16x32_bf16 v[108:111], v[80:83], v[168:171], v[108:111]\n"
"v_mul_f32_e32 v205, v205, v255\n"
"v_mul_f32_e32 v206, v206, v255\n"
"v_mfma_f32_16x16x32_bf16 v[108:111], v[76:79], v[124:127], v[108:111]\n"
"v_mul_f32_e32 v207, v207, v255\n"
"v_cvt_pk_bf16_f32 v120, v196, v197\n"
"v_mfma_f32_16x16x32_bf16 v[108:111], v[72:75], v[100:103], v[108:111]\n"
"v_cvt_pk_bf16_f32 v121, v198, v199\n"
"v_cvt_pk_bf16_f32 v122, v204, v205\n"
"v_mfma_f32_16x16x32_bf16 v[108:111], v[68:71], v[112:115], v[108:111]\n"
"v_cvt_pk_bf16_f32 v123, v206, v207\n"
"v_add_f32_e32 v188, v188, v216\n"
"v_mfma_f32_16x16x32_bf16 v[108:111], v[64:67], v[104:107], v[108:111]\n"
"ds_write_b128 v7, v[120:123] offset:24576\n"
"v_mfma_f32_16x16x32_bf16 v[108:111], v[60:63], v[96:99], v[108:111]\n"
"v_add_f32_e32 v189, v189, v217\n"
"v_add_f32_e32 v190, v190, v218\n"
"v_mfma_f32_16x16x32_bf16 v[108:111], v[56:59], v[92:95], v[108:111]\n"
"v_add_f32_e32 v191, v191, v219\n"
"v_add_f32_e32 v144, v144, v220\n"
"v_add_f32_e32 v145, v145, v221\n"
"v_add_f32_e32 v146, v146, v222\n"
"v_add_f32_e32 v147, v147, v223\n"
"v_mul_f32_e32 v188, v188, v255\n"
"v_mul_f32_e32 v189, v189, v255\n"
"v_mul_f32_e32 v190, v190, v255\n"
"v_mul_f32_e32 v191, v191, v255\n"
"v_mul_f32_e32 v144, v144, v255\n"
"v_mul_f32_e32 v145, v145, v255\n"
"v_mul_f32_e32 v146, v146, v255\n"
"v_mul_f32_e32 v147, v147, v255\n"
"v_cvt_pk_bf16_f32 v92, v188, v189\n"
"v_cvt_pk_bf16_f32 v93, v190, v191\n"
"v_cvt_pk_bf16_f32 v94, v144, v145\n"
"v_cvt_pk_bf16_f32 v95, v146, v147\n"
"ds_write_b128 v7, v[92:95] offset:57344\n"
".Lmk_b1:\n"
"s_waitcnt vmcnt(18)\n"
"; @B G1 1.0\n"
"v_mfma_f32_16x16x32_bf16 v[92:95], a[0:3], v[128:131], 0\n"
"v_mfma_f32_16x16x32_bf16 v[92:95], a[4:7], v[88:91], v[92:95]\n"
"s_add_u32 s98, s62, 0x44800\n"
"v_mfma_f32_16x16x32_bf16 v[92:95], a[8:11], v[164:167], v[92:95]\n"
"s_add_u32 s96, s54, 0x448000\n"
"buffer_load_dwordx4 v[96:99], v0, s[4:7], s96 offen sc1\n"
"v_mfma_f32_16x16x32_bf16 v[92:95], a[12:15], v[140:143], v[92:95]\n"
"s_waitcnt vmcnt(12)\n"
"v_mfma_f32_16x16x32_bf16 v[92:95], a[16:19], v[132:135], v[92:95]\n"
"buffer_load_dword v255, v10, s[4:7], s98 offen sc1\n"
"v_mfma_f32_16x16x32_bf16 v[92:95], a[20:23], v[224:227], v[92:95]\n"
"s_waitcnt lgkmcnt(2)\n"
"v_add_f32_e32 v152, v152, v208\n"
"v_add_f32_e32 v153, v153, v209\n"
"v_mfma_f32_16x16x32_bf16 v[92:95], a[24:27], v[180:183], v[92:95]\n"
"v_add_f32_e32 v154, v154, v210\n"
"v_add_f32_e32 v155, v155, v211\n"
"v_mfma_f32_16x16x32_bf16 v[92:95], a[28:31], v[148:151], v[92:95]\n"
"buffer_load_dwordx4 v[100:103], v0, s[4:7], s96 offen offset:1024 sc1\n"
"v_mfma_f32_16x16x32_bf16 v[104:107], a[60:63], v[148:151], 0\n"
"buffer_load_dwordx4 a[156:159], v1, s[4:7], s97 offen offset:3072 sc1\n"
"v_mfma_f32_16x16x32_bf16 v[104:107], a[56:59], v[180:183], v[104:107]\n"
"buffer_load_dword v253, v10, s[4:7], s98 offen offset:256 sc1\n"
"v_mfma_f32_16x16x32_bf16 v[104:107], a[52:55], v[224:227], v[104:107]\n"
"v_add_f32_e32 v108, v108, v212\n"
"v_add_f32_e32 v109, v109, v213\n"
"v_mfma_f32_16x16x32_bf16 v[104:107], a[48:51], v[132:135], v[104:107]\n"
"v_add_f32_e32 v110, v110, v214\n"
"v_add_f32_e32 v111, v111, v215\n"
"v_mfma_f32_16x16x32_bf16 v[104:107], a[44:47], v[140:143], v[104:107]\n"
"v_cmp_u_f32_e32 vcc, v92, v92\n"
"v_mul_f32_e32 v152, v152, v254\n"
"v_mfma_f32_16x16x32_bf16 v[104:107], a[40:43], v[164:167], v[104:107]\n"
"s_cbranch_vccnz .Lmk_p48s\n"
".Lmk_p48r:\n"
"v_mul_f32_e32 v153, v153, v254\n"
"v_mfma_f32_16x16x32_bf16 v[104:107], a[36:39], v[88:91], v[104:107]\n"
"buffer_load_dwordx4 v[112:115], v0, s[4:7], s96 offen offset:2048 sc1\n"
"v_mfma_f32_16x16x32_bf16 v[104:107], a[32:35], v[128:131], v[104:107]\n"
"ds_write_b128 v3, v[92:95]\n"
"v_mfma_f32_16x16x32_bf16 v[92:95], a[64:67], v[128:131], 0\n"
"buffer_load_dword v252, v10, s[4:7], s98 offen offset:512 sc1\n"
"v_mfma_f32_16x16x32_bf16 v[92:95], a[68:71], v[88:91], v[92:95]\n"
"buffer_load_dwordx4 v[120:123], v0, s[4:7], s96 offen offset:3072 sc1\n"
"v_mfma_f32_16x16x32_bf16 v[92:95], a[72:75], v[164:167], v[92:95]\n"
"v_mul_f32_e32 v154, v154, v254\n"
"v_mul_f32_e32 v155, v155, v254\n"
"v_mfma_f32_16x16x32_bf16 v[92:95], a[76:79], v[140:143], v[92:95]\n"
"buffer_load_dword v251, v10, s[4:7], s98 offen offset:768 sc1\n"
"v_mfma_f32_16x16x32_bf16 v[92:95], a[80:83], v[132:135], v[92:95]\n"
"v_mul_f32_e32 v108, v108, v254\n"
"v_mul_f32_e32 v109, v109, v254\n"
"v_mfma_f32_16x16x32_bf16 v[92:95], a[84:87], v[224:227], v[92:95]\n"
"ds_write_b128 v3, v[104:107] offset:1024\n"
"v_mfma_f32_16x16x32_bf16 v[92:95], a[88:91], v[180:183], v[92:95]\n"
"buffer_load_dwordx4 v[104:107], v1, s[4:7], s96 offen sc1\n"
"v_mfma_f32_16x16x32_bf16 v[92:95], a[92:95], v[148:151], v[92:95]\n"
"buffer_load_dword v250, v10, s[4:7], s98 offen offset:1024 sc1\n"
"v_mfma_f32_16x16x32_bf16 v[124:127], a[124:127], v[148:151], 0\n"
"v_mul_f32_e32 v110, v110, v254\n"
"v_mul_f32_e32 v111, v111, v254\n"
"v_mfma_f32_16x16x32_bf16 v[124:127], a[120:123], v[180:183], v[124:127]\n"
"v_cvt_pk_bf16_f32 v144, v152, v153\n"
"v_cvt_pk_bf16_f32 v145, v154, v155\n"
"v_mfma_f32_16x16x32_bf16 v[124:127], a[116:119], v[224:227], v[124:127]\n"
"s_add_u32 s97, s70, 0x202000\n"
"buffer_load_dword v254, v10, s[4:7], s98 offen offset:1280 sc1\n"
"v_mfma_f32_16x16x32_bf16 v[124:127], a[112:115], v[132:135], v[124:127]\n"
"buffer_load_dwordx4 v[152:155], v1, s[4:7], s96 offen offset:1024 sc1\n"
"v_mfma_f32_16x16x32_bf16 v[124:127], a[108:111], v[140:143], v[124:127]\n"
"buffer_load_dwordx4 a[160:163], v0, s[4:7], s97 offen sc1\n"
"v_mfma_f32_16x16x32_bf16 v[124:127], a[104:107], v[164:167], v[124:127]\n"
"ds_write_b128 v3, v[92:95] offset:2048\n"
"v_mfma_f32_16x16x32_bf16 v[124:127], a[100:103], v[88:91], v[124:127]\n"
"buffer_load_dword v249, v10, s[4:7], s98 offen offset:1536 sc1\n"
"v_mfma_f32_16x16x32_bf16 v[124:127], a[96:99], v[128:131], v[124:127]\n"
"v_cvt_pk_bf16_f32 v146, v108, v109\n"
"v_cvt_pk_bf16_f32 v147, v110, v111\n"
"v_mfma_f32_16x16x32_bf16 v[92:95], v[24:27], v[128:131], 0\n"
"buffer_load_dwordx4 v[108:111], v1, s[4:7], s96 offen offset:2048 sc1\n"
"v_mfma_f32_16x16x32_bf16 v[92:95], v[28:31], v[88:91], v[92:95]\n"
"buffer_load_dword v248, v10, s[4:7], s98 offen offset:1792 sc1\n"
"v_mfma_f32_16x16x32_bf16 v[92:95], v[32:35], v[164:167], v[92:95]\n"
"ds_write_b128 v2, v[144:147] offset:49152\n"
"v_mfma_f32_16x16x32_bf16 v[92:95], v[36:39], v[140:143], v[92:95]\n"
"v_mfma_f32_16x16x32_bf16 v[92:95], v[40:43], v[132:135], v[92:95]\n"
"v_mfma_f32_16x16x32_bf16 v[92:95], v[44:47], v[224:227], v[92:95]\n"
"buffer_load_dwordx4 v[144:147], v1, s[4:7], s96 offen offset:3072 sc1\n"
"v_mfma_f32_16x16x32_bf16 v[92:95], v[48:51], v[180:183], v[92:95]\n"
"ds_write_b128 v3, v[124:127] offset:3072\n"
"v_mfma_f32_16x16x32_bf16 v[92:95], v[52:55], v[148:151], v[92:95]\n"
"v_mfma_f32_16x16x32_bf16 v[124:127], v[84:87], v[148:151], 0\n"
"v_mfma_f32_16x16x32_bf16 v[124:127], v[80:83], v[180:183], v[124:127]\n"
"v_mfma_f32_16x16x32_bf16 v[124:127], v[76:79], v[224:227], v[124:127]\n"
"v_mfma_f32_16x16x32_bf16 v[124:127], v[72:75], v[132:135], v[124:127]\n"
"v_mfma_f32_16x16x32_bf16 v[124:127], v[68:71], v[140:143], v[124:127]\n"
"v_mfma_f32_16x16x32_bf16 v[124:127], v[64:67], v[164:167], v[124:127]\n"
"s_nop 1\n"
"ds_write_b128 v3, v[92:95] offset:4096\n"
"v_mfma_f32_16x16x32_bf16 v[124:127], v[60:63], v[88:91], v[124:127]\n"
"v_mfma_f32_16x16x32_bf16 v[124:127], v[56:59], v[128:131], v[124:127]\n"
"s_waitcnt vmcnt(24)\n"
"; @B G1 1.1\n"
"v_mfma_f32_16x16x32_bf16 v[88:91], a[0:3], v[116:119], 0\n"
"buffer_load_dwordx4 a[164:167], v0, s[4:7], s97 offen offset:1024 sc1\n"
"v_mfma_f32_16x16x32_bf16 v[88:91], a[4:7], v[160:163], v[88:91]\n"
"v_mfma_f32_16x16x32_bf16 v[88:91], a[8:11], v[136:139], v[88:91]\n"
"s_add_u32 s98, s55, 0x448000\n"
"buffer_load_dwordx4 v[92:95], v0, s[4:7], s98 offen sc1\n"
"v_mfma_f32_16x16x32_bf16 v[88:91], a[12:15], v[156:159], v[88:91]\n"
"s_waitcnt vmcnt(21)\n"
"v_mfma_f32_16x16x32_bf16 v[88:91], a[16:19], v[192:195], v[88:91]\n"
"v_mfma_f32_16x16x32_bf16 v[88:91], a[20:23], v[184:187], v[88:91]\n"
"ds_write_b128 v3, v[124:127] offset:5120\n"
"v_mfma_f32_16x16x32_bf16 v[88:91], a[24:27], v[176:179], v[88:91]\n"
"v_mfma_f32_16x16x32_bf16 v[88:91], a[28:31], v[200:203], v[88:91]\n"
"buffer_load_dwordx4 v[124:127], v0, s[4:7], s98 offen offset:1024 sc1\n"
"v_mfma_f32_16x16x32_bf16 v[128:131], a[60:63], v[200:203], 0\n"
"v_mfma_f32_16x16x32_bf16 v[128:131], a[56:59], v[176:179], v[128:131]\n"
"v_mfma_f32_16x16x32_bf16 v[128:131], a[52:55], v[184:187], v[128:131]\n"
"v_mfma_f32_16x16x32_bf16 v[128:131], a[48:51], v[192:195], v[128:131]\n"
"v_mfma_f32_16x16x32_bf16 v[128:131], a[44:47], v[156:159], v[128:131]\n"
"s_add_u32 s96, s67, 0x10000\n"
"s_nop 0\n"
"v_cmp_u_f32_e32 vcc, v88, v88\n"
"v_mfma_f32_16x16x32_bf16 v[128:131], a[40:43], v[136:139], v[128:131]\n"
"s_cbranch_vccnz .Lmk_p49s\n"
".Lmk_p49r:\n"
"v_mfma_f32_16x16x32_bf16 v[128:131], a[36:39], v[160:163], v[128:131]\n"
"buffer_load_dwordx4 v[132:135], v0, s[4:7], s98 offen offset:2048 sc1\n"
"v_mfma_f32_16x16x32_bf16 v[128:131], a[32:35], v[116:119], v[128:131]\n"
"ds_write_b128 v3, v[88:91] offset:6144\n"
"v_mfma_f32_16x16x32_bf16 v[88:91], a[64:67], v[116:119], 0\n"
"buffer_load_dwordx2 v[246:247], v12, s[4:7], s96 offen sc1\n"
"v_mfma_f32_16x16x32_bf16 v[88:91], a[68:71], v[160:163], v[88:91]\n"
"buffer_load_dwordx4 v[140:143], v0, s[4:7], s98 offen offset:3072 sc1\n"
"v_mfma_f32_16x16x32_bf16 v[88:91], a[72:75], v[136:139], v[88:91]\n"
"v_mfma_f32_16x16x32_bf16 v[88:91], a[76:79], v[156:159], v[88:91]\n"
"v_mfma_f32_16x16x32_bf16 v[88:91], a[80:83], v[192:195], v[88:91]\n"
"v_mov_b32_e32 v148, -1\n"
"v_mov_b32_e32 v149, -1\n"
"v_mfma_f32_16x16x32_bf16 v[88:91], a[84:87], v[184:187], v[88:91]\n"
"ds_write_b128 v3, v[128:131] offset:7168\n"
"v_mfma_f32_16x16x32_bf16 v[88:91], a[88:91], v[176:179], v[88:91]\n"
"buffer_load_dwordx4 v[128:131], v1, s[4:7], s98 offen sc1\n"
"v_mfma_f32_16x16x32_bf16 v[88:91], a[92:95], v[200:203], v[88:91]\n"
"v_mov_b32_e32 v150, -1\n"
"v_mov_b32_e32 v151, -1\n"
"v_mfma_f32_16x16x32_bf16 v[164:167], a[124:127], v[200:203], 0\n"
"s_add_u32 s95, s61, 0x840000\n"
"buffer_store_dwordx4 v[148:151], v0, s[8:11], s95 offen sc1\n"
"v_mfma_f32_16x16x32_bf16 v[164:167], a[120:123], v[176:179], v[164:167]\n"
"buffer_load_dwordx4 a[168:171], v0, s[4:7], s97 offen offset:2048 sc1\n"
"v_mfma_f32_16x16x32_bf16 v[164:167], a[116:119], v[184:187], v[164:167]\n"
"v_mfma_f32_16x16x32_bf16 v[164:167], a[112:115], v[192:195], v[164:167]\n"
"buffer_load_dwordx4 v[168:171], v1, s[4:7], s98 offen offset:1024 sc1\n"
"v_mfma_f32_16x16x32_bf16 v[164:167], a[108:111], v[156:159], v[164:167]\n"
"v_mfma_f32_16x16x32_bf16 v[164:167], a[104:107], v[136:139], v[164:167]\n"
"ds_write_b128 v3, v[88:91] offset:8192\n"
"v_mfma_f32_16x16x32_bf16 v[164:167], a[100:103], v[160:163], v[164:167]\n"
"v_mfma_f32_16x16x32_bf16 v[164:167], a[96:99], v[116:119], v[164:167]\n"
"v_mfma_f32_16x16x32_bf16 v[88:91], v[24:27], v[116:119], 0\n"
"buffer_load_dwordx4 v[172:175], v1, s[4:7], s98 offen offset:2048 sc1\n"
"v_mfma_f32_16x16x32_bf16 v[88:91], v[28:31], v[160:163], v[88:91]\n"
"v_mfma_f32_16x16x32_bf16 v[88:91], v[32:35], v[136:139], v[88:91]\n"
"v_mfma_f32_16x16x32_bf16 v[88:91], v[36:39], v[156:159], v[88:91]\n"
"v_mfma_f32_16x16x32_bf16 v[88:91], v[40:43], v[192:195], v[88:91]\n"
"s_waitcnt vmcnt(12)\n"
"v_add_f32_e32 v255, v255, v250\n"
"v_add_f32_e32 v253, v253, v254\n"
"v_mfma_f32_16x16x32_bf16 v[88:91], v[44:47], v[184:187], v[88:91]\n"
"buffer_load_dwordx4 v[180:183], v1, s[4:7], s98 offen offset:3072 sc1\n"
"v_mfma_f32_16x16x32_bf16 v[88:91], v[48:51], v[176:179], v[88:91]\n"
"ds_write_b128 v3, v[164:167] offset:9216\n"
"v_mfma_f32_16x16x32_bf16 v[88:91], v[52:55], v[200:203], v[88:91]\n"
"v_add_f32_e32 v252, v252, v249\n"
"v_add_f32_e32 v251, v251, v248\n"
"v_mfma_f32_16x16x32_bf16 v[164:167], v[84:87], v[200:203], 0\n"
"v_add_f32_e32 v255, v255, v252\n"
"v_add_f32_e32 v253, v253, v251\n"
"v_mfma_f32_16x16x32_bf16 v[164:167], v[80:83], v[176:179], v[164:167]\n"
"v_add_f32_e32 v255, v255, v253\n"
"v_cmp_u_f32_e32 vcc, v255, v255\n"
"v_mfma_f32_16x16x32_bf16 v[164:167], v[76:79], v[184:187], v[164:167]\n"
"s_cbranch_vccnz .Lmk_m52s\n"
".Lmk_m52r:\n"
"v_mov_b32_e32 v253, v255\n"
"v_mfma_f32_16x16x32_bf16 v[164:167], v[72:75], v[192:195], v[164:167]\n"
"s_nop 0\n"
"v_permlane32_swap_b32_e32 v255, v253\n"
"v_add_f32_e32 v255, v255, v253\n"
"v_mfma_f32_16x16x32_bf16 v[164:167], v[68:71], v[156:159], v[164:167]\n"
"v_mul_f32_e32 v255, 0x3a800000, v255\n"
"v_mov_b32_e32 v253, v255\n"
"v_mfma_f32_16x16x32_bf16 v[164:167], v[64:67], v[136:139], v[164:167]\n"
"ds_write_b128 v3, v[88:91] offset:10240\n"
"v_mfma_f32_16x16x32_bf16 v[164:167], v[60:63], v[160:163], v[164:167]\n"
"buffer_load_dwordx4 a[172:175], v0, s[4:7], s97 offen offset:3072 sc1\n"
"v_mfma_f32_16x16x32_bf16 v[164:167], v[56:59], v[116:119], v[164:167]\n"
"v_permlane16_swap_b32_e32 v255, v253\n"
"v_cvt_pk_bf16_f32 v254, -v255, 0\n"
"; @B G1 1.2\n"
"v_mfma_f32_16x16x32_bf16 v[88:91], a[0:3], v[96:99], 0\n"
"v_fma_f32 v253, -v255, v255, v253\n"
"v_lshlrev_b32_e32 v254, 16, v254\n"
"v_mfma_f32_16x16x32_bf16 v[88:91], a[4:7], v[100:103], v[88:91]\n"
"s_waitcnt vmcnt(8)\n"
"v_cmp_ne_u64_e32 vcc, v[246:247], v[18:19]\n"
"v_mfma_f32_16x16x32_bf16 v[88:91], a[8:11], v[112:115], v[88:91]\n"
"s_cbranch_vccnz .Lmk_f39s\n"
".Lmk_f39r:\n"
"v_max_f32_e32 v253, 0, v253\n"
"v_mfma_f32_16x16x32_bf16 v[88:91], a[12:15], v[120:123], v[88:91]\n"
"v_cvt_pk_bf16_f32 v253, v253, 0\n"
"v_lshlrev_b32_e32 v253, 16, v253\n"
"v_mfma_f32_16x16x32_bf16 v[88:91], a[16:19], v[104:107], v[88:91]\n"
"v_add_f32_e32 v253, 0x3727c5ac, v253\n"
"v_cvt_pk_bf16_f32 v253, v253, 0\n"
"v_mfma_f32_16x16x32_bf16 v[88:91], a[20:23], v[152:155], v[88:91]\n"
"ds_write_b128 v3, v[164:167] offset:11264\n"
"v_mfma_f32_16x16x32_bf16 v[88:91], a[24:27], v[108:111], v[88:91]\n"
"v_lshlrev_b32_e32 v253, 16, v253\n"
"v_mfma_f32_16x16x32_bf16 v[88:91], a[28:31], v[144:147], v[88:91]\n"
"v_sqrt_f32_e32 v253, v253\n"
"v_mfma_f32_16x16x32_bf16 v[116:119], a[60:63], v[144:147], 0\n"
"v_cvt_pk_bf16_f32 v253, v253, 0\n"
"v_lshlrev_b32_e32 v253, 16, v253\n"
"v_mfma_f32_16x16x32_bf16 v[116:119], a[56:59], v[108:111], v[116:119]\n"
"v_rcp_f32_e32 v255, v253\n"
"v_mfma_f32_16x16x32_bf16 v[116:119], a[52:55], v[152:155], v[116:119]\n"
"s_add_u32 s98, s52, 0x408000\n"
"buffer_load_dwordx4 v[136:139], v0, s[4:7], s98 offen sc1\n"
"v_mfma_f32_16x16x32_bf16 v[116:119], a[48:51], v[104:107], v[116:119]\n"
"s_waitcnt lgkmcnt(0)\n"
"s_barrier\n"
"v_mfma_f32_16x16x32_bf16 v[116:119], a[44:47], v[120:123], v[116:119]\n"
"v_cmp_u_f32_e32 vcc, v88, v88\n"
"v_mul_f32_e32 v253, 0x3e8293ee, v255\n"
"v_mfma_f32_16x16x32_bf16 v[116:119], a[40:43], v[112:115], v[116:119]\n"
"s_cbranch_vccnz .Lmk_p50s\n"
".Lmk_p50r:\n"
"ds_read_b128 v[156:159], v4\n"
"v_mfma_f32_16x16x32_bf16 v[116:119], a[36:39], v[100:103], v[116:119]\n"
"ds_write_b128 v3, v[88:91] offset:12288\n"
"v_mfma_f32_16x16x32_bf16 v[116:119], a[32:35], v[96:99], v[116:119]\n"
"buffer_load_dwordx4 v[88:91], v0, s[4:7], s98 offen offset:1024 sc1\n"
"v_mfma_f32_16x16x32_bf16 v[160:163], a[64:67], v[96:99], 0\n"
"ds_read_b128 v[164:167], v9 offset:640\n"
"ds_read_b128 v[176:179], v5\n"
"v_mfma_f32_16x16x32_bf16 v[160:163], a[68:71], v[100:103], v[160:163]\n"
"v_mfma_f32_16x16x32_bf16 v[160:163], a[72:75], v[112:115], v[160:163]\n"
"buffer_load_dwordx4 a[176:179], v1, s[4:7], s97 offen sc1\n"
"v_mfma_f32_16x16x32_bf16 v[160:163], a[76:79], v[120:123], v[160:163]\n"
"ds_read_b128 v[184:187], v4 offset:1024\n"
"ds_read_b128 v[188:191], v9 offset:704\n"
"v_mfma_f32_16x16x32_bf16 v[160:163], a[80:83], v[104:107], v[160:163]\n"
"buffer_load_dwordx4 v[192:195], v0, s[4:7], s98 offen offset:2048 sc1\n"
"v_mfma_f32_16x16x32_bf16 v[160:163], a[84:87], v[152:155], v[160:163]\n"
"ds_write_b128 v3, v[116:119] offset:13312\n"
"v_mfma_f32_16x16x32_bf16 v[160:163], a[88:91], v[108:111], v[160:163]\n"
"ds_read_b128 v[116:119], v5 offset:1024\n"
"s_waitcnt lgkmcnt(4)\n"
"v_mfma_f32_16x16x32_bf16 v[160:163], a[92:95], v[144:147], v[160:163]\n"
"v_fmac_f32_e32 v156, v254, v164\n"
"v_fmac_f32_e32 v157, v254, v165\n"
"v_mfma_f32_16x16x32_bf16 v[196:199], a[124:127], v[144:147], 0\n"
"v_fmac_f32_e32 v158, v254, v166\n"
"v_fmac_f32_e32 v159, v254, v167\n"
"v_mfma_f32_16x16x32_bf16 v[196:199], a[120:123], v[108:111], v[196:199]\n"
"buffer_load_dwordx4 v[164:167], v0, s[4:7], s98 offen offset:3072 sc1\n"
"v_mfma_f32_16x16x32_bf16 v[196:199], a[116:119], v[152:155], v[196:199]\n"
"ds_read_b128 v[200:203], v4 offset:2048\n"
"ds_read_b128 v[204:207], v9 offset:768\n"
"v_mfma_f32_16x16x32_bf16 v[196:199], a[112:115], v[104:107], v[196:199]\n"
"ds_read_b128 v[208:211], v5 offset:2048\n"
"v_add_f32_e32 v156, v156, v176\n"
"v_mfma_f32_16x16x32_bf16 v[196:199], a[108:111], v[120:123], v[196:199]\n"
"v_add_f32_e32 v157, v157, v177\n"
"v_add_f32_e32 v158, v158, v178\n"
"v_mfma_f32_16x16x32_bf16 v[196:199], a[104:107], v[112:115], v[196:199]\n"
"ds_write_b128 v3, v[160:163] offset:14336\n"
"v_mfma_f32_16x16x32_bf16 v[196:199], a[100:103], v[100:103], v[196:199]\n"
"buffer_load_dwordx4 v[160:163], v1, s[4:7], s98 offen sc1\n"
"v_mfma_f32_16x16x32_bf16 v[196:199], a[96:99], v[96:99], v[196:199]\n"
"s_add_u32 s96, s61, 0x840000\n"
"v_add_f32_e32 v159, v159, v179\n"
"s_waitcnt lgkmcnt(4)\n"
"v_mfma_f32_16x16x32_bf16 v[176:179], v[24:27], v[96:99], 0\n"
"buffer_store_dwordx4 v[148:151], v0, s[8:11], s96 offen offset:1024 sc1\n"
"v_mfma_f32_16x16x32_bf16 v[176:179], v[28:31], v[100:103], v[176:179]\n"
"ds_read_b128 v[212:215], v4 offset:3072\n"
"ds_read_b128 v[216:219], v9 offset:832\n"
"v_mfma_f32_16x16x32_bf16 v[176:179], v[32:35], v[112:115], v[176:179]\n"
"ds_read_b128 v[220:223], v5 offset:3072\n"
"v_fmac_f32_e32 v184, v254, v188\n"
"v_mfma_f32_16x16x32_bf16 v[176:179], v[36:39], v[120:123], v[176:179]\n"
"buffer_load_dwordx4 v[224:227], v1, s[4:7], s98 offen offset:1024 sc1\n"
"v_mfma_f32_16x16x32_bf16 v[176:179], v[40:43], v[104:107], v[176:179]\n"
"v_fmac_f32_e32 v185, v254, v189\n"
"v_fmac_f32_e32 v186, v254, v190\n"
"v_mfma_f32_16x16x32_bf16 v[176:179], v[44:47], v[152:155], v[176:179]\n"
"ds_write_b128 v3, v[196:199] offset:15360\n"
"v_mfma_f32_16x16x32_bf16 v[176:179], v[48:51], v[108:111], v[176:179]\n"
"buffer_load_dwordx4 a[180:183], v1, s[4:7], s97 offen offset:1024 sc1\n"
"v_mfma_f32_16x16x32_bf16 v[176:179], v[52:55], v[144:147], v[176:179]\n"
"v_fmac_f32_e32 v187, v254, v191\n"
"v_add_f32_e32 v184, v184, v116\n"
"v_mfma_f32_16x16x32_bf16 v[188:191], v[84:87], v[144:147], 0\n"
"buffer_load_dwordx4 v[144:147], v1, s[4:7], s98 offen offset:2048 sc1\n"
"v_mfma_f32_16x16x32_bf16 v[188:191], v[80:83], v[108:111], v[188:191]\n"
"ds_read_b128 v[108:111], v4 offset:4096\n"
"ds_read_b128 v[196:199], v9 offset:512\n"
"v_mfma_f32_16x16x32_bf16 v[188:191], v[76:79], v[152:155], v[188:191]\n"
"ds_read_b128 v[152:155], v5 offset:4096\n"
"v_add_f32_e32 v185, v185, v117\n"
"v_mfma_f32_16x16x32_bf16 v[188:191], v[72:75], v[104:107], v[188:191]\n"
"s_waitcnt lgkmcnt(8)\n"
"v_add_f32_e32 v186, v186, v118\n"
"v_add_f32_e32 v187, v187, v119\n"
"v_mfma_f32_16x16x32_bf16 v[188:191], v[68:71], v[120:123], v[188:191]\n"
"v_fmac_f32_e32 v200, v254, v204\n"
"v_fmac_f32_e32 v201, v254, v205\n"
"v_mfma_f32_16x16x32_bf16 v[188:191], v[64:67], v[112:115], v[188:191]\n"
"buffer_load_dwordx4 v[104:107], v1, s[4:7], s98 offen offset:3072 sc1\n"
"v_mfma_f32_16x16x32_bf16 v[188:191], v[60:63], v[100:103], v[188:191]\n"
"ds_write_b128 v3, v[176:179] offset:16384\n"
"v_mfma_f32_16x16x32_bf16 v[188:191], v[56:59], v[96:99], v[188:191]\n"
"s_waitcnt vmcnt(18)\n"
"ds_read_b128 v[96:99], v4 offset:5120\n"
"ds_read_b128 v[100:103], v9 offset:576\n"
"; @B G1 1.3\n"
"v_mfma_f32_16x16x32_bf16 v[112:115], a[0:3], v[92:95], v[156:159]\n"
"ds_read_b128 v[116:119], v5 offset:5120\n"
"v_fmac_f32_e32 v202, v254, v206\n"
"v_mfma_f32_16x16x32_bf16 v[112:115], a[4:7], v[124:127], v[112:115]\n"
"v_fmac_f32_e32 v203, v254, v207\n"
"v_add_f32_e32 v200, v200, v208\n"
"v_mfma_f32_16x16x32_bf16 v[112:115], a[8:11], v[132:135], v[112:115]\n"
"s_add_u32 s98, s53, 0x408000\n"
"buffer_load_dwordx4 v[120:123], v0, s[4:7], s98 offen sc1\n"
"v_mfma_f32_16x16x32_bf16 v[112:115], a[12:15], v[140:143], v[112:115]\n"
"s_waitcnt vmcnt(13)\n"
"v_add_f32_e32 v201, v201, v209\n"
"v_add_f32_e32 v202, v202, v210\n"
"v_mfma_f32_16x16x32_bf16 v[112:115], a[16:19], v[128:131], v[112:115]\n"
"s_waitcnt lgkmcnt(8)\n"
"v_add_f32_e32 v203, v203, v211\n"
"v_fmac_f32_e32 v212, v254, v216\n"
"v_mfma_f32_16x16x32_bf16 v[112:115], a[20:23], v[168:171], v[112:115]\n"
"ds_write_b128 v3, v[188:191] offset:17408\n"
"v_mfma_f32_16x16x32_bf16 v[112:115], a[24:27], v[172:175], v[112:115]\n"
"v_fmac_f32_e32 v213, v254, v217\n"
"v_fmac_f32_e32 v214, v254, v218\n"
"v_mfma_f32_16x16x32_bf16 v[112:115], a[28:31], v[180:183], v[112:115]\n"
"buffer_load_dwordx4 v[176:179], v0, s[4:7], s98 offen offset:1024 sc1\n"
"v_mfma_f32_16x16x32_bf16 v[188:191], a[60:63], v[180:183], v[184:187]\n"
"v_fmac_f32_e32 v215, v254, v219\n"
"v_add_f32_e32 v212, v212, v220\n"
"v_mfma_f32_16x16x32_bf16 v[188:191], a[56:59], v[172:175], v[188:191]\n"
"v_add_f32_e32 v213, v213, v221\n"
"v_add_f32_e32 v214, v214, v222\n"
"v_mfma_f32_16x16x32_bf16 v[188:191], a[52:55], v[168:171], v[188:191]\n"
"s_waitcnt lgkmcnt(0)\n"
"s_barrier\n"
"v_mfma_f32_16x16x32_bf16 v[188:191], a[48:51], v[128:131], v[188:191]\n"
"buffer_load_dwordx4 a[184:187], v1, s[4:7], s97 offen offset:2048 sc1\n"
"v_mfma_f32_16x16x32_bf16 v[188:191], a[44:47], v[140:143], v[188:191]\n"
"ds_read_b128 v[204:207], v6\n"
"v_cmp_u_f32_e32 vcc, v112, v112\n"
"v_mfma_f32_16x16x32_bf16 v[188:191], a[40:43], v[132:135], v[188:191]\n"
"s_cbranch_vccnz .Lmk_p51s\n"
".Lmk_p51r:\n"
"ds_read_b128 v[156:159], v6 offset:1024\n"
"v_mfma_f32_16x16x32_bf16 v[188:191], a[36:39], v[124:127], v[188:191]\n"
"buffer_load_dwordx4 v[184:187], v0, s[4:7], s98 offen offset:2048 sc1\n"
"v_mfma_f32_16x16x32_bf16 v[188:191], a[32:35], v[92:95], v[188:191]\n"
"v_add_f32_e32 v215, v215, v223\n"
"v_fmac_f32_e32 v108, v254, v196\n"
"v_mfma_f32_16x16x32_bf16 v[200:203], a[64:67], v[92:95], v[200:203]\n"
"v_fmac_f32_e32 v109, v254, v197\n"
"v_fmac_f32_e32 v110, v254, v198\n"
"v_mfma_f32_16x16x32_bf16 v[200:203], a[68:71], v[124:127], v[200:203]\n"
"buffer_load_dwordx4 v[208:211], v0, s[4:7], s98 offen offset:3072 sc1\n"
"v_mfma_f32_16x16x32_bf16 v[200:203], a[72:75], v[132:135], v[200:203]\n"
"v_fmac_f32_e32 v111, v254, v199\n"
"v_add_f32_e32 v108, v108, v152\n"
"v_mfma_f32_16x16x32_bf16 v[200:203], a[76:79], v[140:143], v[200:203]\n"
"v_add_f32_e32 v109, v109, v153\n"
"v_add_f32_e32 v110, v110, v154\n"
"v_mfma_f32_16x16x32_bf16 v[200:203], a[80:83], v[128:131], v[200:203]\n"
"v_add_f32_e32 v111, v111, v155\n"
"v_fmac_f32_e32 v96, v254, v100\n"
"v_mfma_f32_16x16x32_bf16 v[200:203], a[84:87], v[168:171], v[200:203]\n"
"s_waitcnt lgkmcnt(0)\n"
"v_fmac_f32_e32 v97, v254, v101\n"
"v_fmac_f32_e32 v98, v254, v102\n"
"v_mfma_f32_16x16x32_bf16 v[200:203], a[88:91], v[172:175], v[200:203]\n"
"buffer_load_dwordx4 v[152:155], v1, s[4:7], s98 offen sc1\n"
"v_mfma_f32_16x16x32_bf16 v[200:203], a[92:95], v[180:183], v[200:203]\n"
"ds_read_b128 v[196:199], v6 offset:2048\n"
"ds_read_b128 v[216:219], v6 offset:3072\n"
"v_mfma_f32_16x16x32_bf16 v[212:215], a[124:127], v[180:183], v[212:215]\n"
"v_fmac_f32_e32 v99, v254, v103\n"
"v_add_f32_e32 v96, v96, v116\n"
"v_mfma_f32_16x16x32_bf16 v[212:215], a[120:123], v[172:175], v[212:215]\n"
"v_add_f32_e32 v97, v97, v117\n"
"v_add_f32_e32 v98, v98, v118\n"
"v_mfma_f32_16x16x32_bf16 v[212:215], a[116:119], v[168:171], v[212:215]\n"
"v_add_f32_e32 v99, v99, v119\n"
"v_add_f32_e32 v112, v112, v204\n"
"v_mfma_f32_16x16x32_bf16 v[212:215], a[112:115], v[128:131], v[212:215]\n"
"buffer_load_dwordx4 v[100:103], v1, s[4:7], s98 offen offset:1024 sc1\n"
"v_mfma_f32_16x16x32_bf16 v[212:215], a[108:111], v[140:143], v[212:215]\n"
"v_add_f32_e32 v113, v113, v205\n"
"v_add_f32_e32 v114, v114, v206\n"
"v_mfma_f32_16x16x32_bf16 v[212:215], a[104:107], v[132:135], v[212:215]\n"
"v_add_f32_e32 v115, v115, v207\n"
"v_add_f32_e32 v188, v188, v156\n"
"v_mfma_f32_16x16x32_bf16 v[212:215], a[100:103], v[124:127], v[212:215]\n"
"v_add_f32_e32 v189, v189, v157\n"
"v_add_f32_e32 v190, v190, v158\n"
"v_mfma_f32_16x16x32_bf16 v[212:215], a[96:99], v[92:95], v[212:215]\n"
"buffer_load_dwordx4 a[188:191], v1, s[4:7], s97 offen offset:3072 sc1\n"
"v_mfma_f32_16x16x32_bf16 v[108:111], v[24:27], v[92:95], v[108:111]\n"
"buffer_load_dwordx4 v[116:119], v1, s[4:7], s98 offen offset:2048 sc1\n"
"v_mfma_f32_16x16x32_bf16 v[108:111], v[28:31], v[124:127], v[108:111]\n"
"v_add_f32_e32 v191, v191, v159\n"
"v_mul_f32_e32 v112, v112, v255\n"
"v_mfma_f32_16x16x32_bf16 v[108:111], v[32:35], v[132:135], v[108:111]\n"
"v_mul_f32_e32 v113, v113, v255\n"
"v_mul_f32_e32 v114, v114, v255\n"
"v_mfma_f32_16x16x32_bf16 v[108:111], v[36:39], v[140:143], v[108:111]\n"
"v_mul_f32_e32 v115, v115, v255\n"
"v_mul_f32_e32 v188, v188, v255\n"
"v_mfma_f32_16x16x32_bf16 v[108:111], v[40:43], v[128:131], v[108:111]\n"
"s_waitcnt lgkmcnt(0)\n"
"v_mul_f32_e32 v189, v189, v255\n"
"v_mul_f32_e32 v190, v190, v255\n"
"v_mfma_f32_16x16x32_bf16 v[108:111], v[44:47], v[168:171], v[108:111]\n"
"buffer_load_dwordx4 v[156:159], v1, s[4:7], s98 offen offset:3072 sc1\n"
"v_mfma_f32_16x16x32_bf16 v[108:111], v[48:51], v[172:175], v[108:111]\n"
"s_add_u32 s98, s63, 0x84000\n"
"ds_read_b128 v[204:207], v6 offset:4096\n"
"ds_read_b128 v[220:223], v6 offset:5120\n"
"v_mfma_f32_16x16x32_bf16 v[108:111], v[52:55], v[180:183], v[108:111]\n"
"v_mul_f32_e32 v191, v191, v255\n"
"v_cvt_pk_bf16_f32 v228, v112, v113\n"
"v_mfma_f32_16x16x32_bf16 v[96:99], v[84:87], v[180:183], v[96:99]\n"
"buffer_store_dword v148, v14, s[8:11], s98 offen sc1\n"
"v_mfma_f32_16x16x32_bf16 v[96:99], v[80:83], v[172:175], v[96:99]\n"
"v_cvt_pk_bf16_f32 v229, v114, v115\n"
"v_cvt_pk_bf16_f32 v230, v188, v189\n"
"v_mfma_f32_16x16x32_bf16 v[96:99], v[76:79], v[168:171], v[96:99]\n"
"v_cvt_pk_bf16_f32 v231, v190, v191\n"
"v_add_f32_e32 v200, v200, v196\n"
"v_mfma_f32_16x16x32_bf16 v[96:99], v[72:75], v[128:131], v[96:99]\n"
"ds_write_b128 v8, v[228:231] offset:25600\n"
"v_mfma_f32_16x16x32_bf16 v[96:99], v[68:71], v[140:143], v[96:99]\n"
"v_add_f32_e32 v201, v201, v197\n"
"v_add_f32_e32 v202, v202, v198\n"
"v_mfma_f32_16x16x32_bf16 v[96:99], v[64:67], v[132:135], v[96:99]\n"
"v_add_f32_e32 v203, v203, v199\n"
"v_add_f32_e32 v212, v212, v216\n"
"v_mfma_f32_16x16x32_bf16 v[96:99], v[60:63], v[124:127], v[96:99]\n"
"v_add_f32_e32 v213, v213, v217\n"
"v_add_f32_e32 v214, v214, v218\n"
"v_mfma_f32_16x16x32_bf16 v[96:99], v[56:59], v[92:95], v[96:99]\n"
"v_add_f32_e32 v215, v215, v219\n"
"v_mul_f32_e32 v200, v200, v255\n"
"v_mul_f32_e32 v201, v201, v255\n"
"v_mul_f32_e32 v202, v202, v255\n"
"v_mul_f32_e32 v203, v203, v255\n"
"v_mul_f32_e32 v212, v212, v255\n"
"v_mul_f32_e32 v213, v213, v255\n"
"v_mul_f32_e32 v214, v214, v255\n"
"v_mul_f32_e32 v215, v215, v255\n"
"v_cvt_pk_bf16_f32 v92, v200, v201\n"
"v_cvt_pk_bf16_f32 v93, v202, v203\n"
"v_cvt_pk_bf16_f32 v94, v212, v213\n"
"v_cvt_pk_bf16_f32 v95, v214, v215\n"
"ds_write_b128 v7, v[92:95] offset:58368\n"
".Lmk_b2:\n"
"s_waitcnt vmcnt(17)\n"
"; @B G1 2.0\n"
"v_mfma_f32_16x16x32_bf16 v[112:115], a[0:3], v[136:139], 0\n"
"v_mfma_f32_16x16x32_bf16 v[112:115], a[4:7], v[88:91], v[112:115]\n"
"s_add_u32 s98, s62, 0x40800\n"
"v_mfma_f32_16x16x32_bf16 v[112:115], a[8:11], v[192:195], v[112:115]\n"
"s_add_u32 s97, s54, 0x408000\n"
"buffer_load_dwordx4 v[124:127], v0, s[4:7], s97 offen sc1\n"
"v_mfma_f32_16x16x32_bf16 v[112:115], a[12:15], v[164:167], v[112:115]\n"
"s_waitcnt vmcnt(12)\n"
"v_mfma_f32_16x16x32_bf16 v[112:115], a[16:19], v[160:163], v[112:115]\n"
"buffer_load_dword v255, v10, s[4:7], s98 offen sc1\n"
"v_mfma_f32_16x16x32_bf16 v[112:115], a[20:23], v[224:227], v[112:115]\n"
"s_waitcnt lgkmcnt(2)\n"
"v_add_f32_e32 v108, v108, v204\n"
"v_add_f32_e32 v109, v109, v205\n"
"v_mfma_f32_16x16x32_bf16 v[112:115], a[24:27], v[144:147], v[112:115]\n"
"v_add_f32_e32 v110, v110, v206\n"
"v_add_f32_e32 v111, v111, v207\n"
"v_mfma_f32_16x16x32_bf16 v[112:115], a[28:31], v[104:107], v[112:115]\n"
"buffer_load_dwordx4 v[128:131], v0, s[4:7], s97 offen offset:1024 sc1\n"
"v_mfma_f32_16x16x32_bf16 v[132:135], a[60:63], v[104:107], 0\n"
"s_add_u32 s96, s70, 0x204000\n"
"buffer_load_dwordx4 a[192:195], v0, s[4:7], s96 offen sc1\n"
"v_mfma_f32_16x16x32_bf16 v[132:135], a[56:59], v[144:147], v[132:135]\n"
"buffer_load_dword v254, v10, s[4:7], s98 offen offset:256 sc1\n"
"v_mfma_f32_16x16x32_bf16 v[132:135], a[52:55], v[224:227], v[132:135]\n"
"v_add_f32_e32 v96, v96, v220\n"
"v_add_f32_e32 v97, v97, v221\n"
"v_mfma_f32_16x16x32_bf16 v[132:135], a[48:51], v[160:163], v[132:135]\n"
"v_add_f32_e32 v98, v98, v222\n"
"v_add_f32_e32 v99, v99, v223\n"
"v_mfma_f32_16x16x32_bf16 v[132:135], a[44:47], v[164:167], v[132:135]\n"
"buffer_load_dwordx4 v[140:143], v0, s[4:7], s97 offen offset:2048 sc1\n"
"v_mfma_f32_16x16x32_bf16 v[132:135], a[40:43], v[192:195], v[132:135]\n"
"ds_write_b128 v3, v[112:115]\n"
"v_mfma_f32_16x16x32_bf16 v[132:135], a[36:39], v[88:91], v[132:135]\n"
"buffer_load_dword v252, v10, s[4:7], s98 offen offset:512 sc1\n"
"v_mfma_f32_16x16x32_bf16 v[132:135], a[32:35], v[136:139], v[132:135]\n"
"v_mul_f32_e32 v108, v108, v253\n"
"v_mul_f32_e32 v109, v109, v253\n"
"v_mfma_f32_16x16x32_bf16 v[112:115], a[64:67], v[136:139], 0\n"
"v_mul_f32_e32 v110, v110, v253\n"
"v_mul_f32_e32 v111, v111, v253\n"
"v_mfma_f32_16x16x32_bf16 v[112:115], a[68:71], v[88:91], v[112:115]\n"
"buffer_load_dwordx4 v[148:151], v0, s[4:7], s97 offen offset:3072 sc1\n"
"v_mfma_f32_16x16x32_bf16 v[112:115], a[72:75], v[192:195], v[112:115]\n"
"buffer_load_dword v251, v10, s[4:7], s98 offen offset:768 sc1\n"
"v_mfma_f32_16x16x32_bf16 v[112:115], a[76:79], v[164:167], v[112:115]\n"
"v_mul_f32_e32 v96, v96, v253\n"
"v_mul_f32_e32 v97, v97, v253\n"
"v_mfma_f32_16x16x32_bf16 v[112:115], a[80:83], v[160:163], v[112:115]\n"
"v_mul_f32_e32 v98, v98, v253\n"
"v_mul_f32_e32 v99, v99, v253\n"
"v_mfma_f32_16x16x32_bf16 v[112:115], a[84:87], v[224:227], v[112:115]\n"
"ds_write_b128 v3, v[132:135] offset:1024\n"
"v_mfma_f32_16x16x32_bf16 v[112:115], a[88:91], v[144:147], v[112:115]\n"
"buffer_load_dwordx4 v[132:135], v1, s[4:7], s97 offen sc1\n"
"v_mfma_f32_16x16x32_bf16 v[112:115], a[92:95], v[104:107], v[112:115]\n"
"buffer_load_dword v253, v10, s[4:7], s98 offen offset:1024 sc1\n"
"v_mfma_f32_16x16x32_bf16 v[168:171], a[124:127], v[104:107], 0\n"
"v_cvt_pk_bf16_f32 v172, v108, v109\n"
"v_cvt_pk_bf16_f32 v173, v110, v111\n"
"v_mfma_f32_16x16x32_bf16 v[168:171], a[120:123], v[144:147], v[168:171]\n"
"v_cvt_pk_bf16_f32 v174, v96, v97\n"
"v_cvt_pk_bf16_f32 v175, v98, v99\n"
"v_mfma_f32_16x16x32_bf16 v[168:171], a[116:119], v[224:227], v[168:171]\n"
"buffer_load_dword v250, v10, s[4:7], s98 offen offset:1280 sc1\n"
"v_mfma_f32_16x16x32_bf16 v[168:171], a[112:115], v[160:163], v[168:171]\n"
"buffer_load_dwordx4 v[96:99], v1, s[4:7], s97 offen offset:1024 sc1\n"
"v_mfma_f32_16x16x32_bf16 v[168:171], a[108:111], v[164:167], v[168:171]\n"
"buffer_load_dwordx4 a[196:199], v0, s[4:7], s96 offen offset:1024 sc1\n"
"v_mfma_f32_16x16x32_bf16 v[168:171], a[104:107], v[192:195], v[168:171]\n"
"ds_write_b128 v3, v[112:115] offset:2048\n"
"v_mfma_f32_16x16x32_bf16 v[168:171], a[100:103], v[88:91], v[168:171]\n"
"buffer_load_dword v249, v10, s[4:7], s98 offen offset:1536 sc1\n"
"v_mfma_f32_16x16x32_bf16 v[168:171], a[96:99], v[136:139], v[168:171]\n"
"ds_write_b128 v2, v[172:175] offset:50176\n"
"v_mfma_f32_16x16x32_bf16 v[108:111], v[24:27], v[136:139], 0\n"
"buffer_load_dwordx4 v[112:115], v1, s[4:7], s97 offen offset:2048 sc1\n"
"s_add_u32 s95, s64, 0x802000\n"
"v_mfma_f32_16x16x32_bf16 v[108:111], v[28:31], v[88:91], v[108:111]\n"
"buffer_load_dword v248, v10, s[4:7], s98 offen offset:1792 sc1\n"
"v_add_u32_e32 v247, 0x26c00, v0\n"
"v_mfma_f32_16x16x32_bf16 v[108:111], v[32:35], v[192:195], v[108:111]\n"
"buffer_store_dwordx4 v[228:231], v8, s[12:15], s95 offen offset:1024 sc1\n"
"v_mfma_f32_16x16x32_bf16 v[108:111], v[36:39], v[164:167], v[108:111]\n"
"ds_read_b128 v[172:175], v247\n"
"ds_read_b128 v[180:183], v247 offset:1024\n"
"v_xor_b32_e32 v247, 32, v15\n"
"v_mfma_f32_16x16x32_bf16 v[108:111], v[40:43], v[160:163], v[108:111]\n"
"ds_read_b128 v[188:191], v2 offset:50176\n"
"ds_read_b128 v[196:199], v15 offset:24576\n"
"ds_read_b128 v[200:203], v247 offset:24832\n"
"v_mfma_f32_16x16x32_bf16 v[108:111], v[44:47], v[224:227], v[108:111]\n"
"buffer_load_dwordx4 v[204:207], v1, s[4:7], s97 offen offset:3072 sc1\n"
"v_mfma_f32_16x16x32_bf16 v[108:111], v[48:51], v[144:147], v[108:111]\n"
"ds_write_b128 v3, v[168:171] offset:3072\n"
"v_mfma_f32_16x16x32_bf16 v[108:111], v[52:55], v[104:107], v[108:111]\n"
"v_mfma_f32_16x16x32_bf16 v[168:171], v[84:87], v[104:107], 0\n"
"v_mfma_f32_16x16x32_bf16 v[168:171], v[80:83], v[144:147], v[168:171]\n"
"s_waitcnt lgkmcnt(1)\n"
"v_mfma_f32_16x16x32_bf16 v[172:175], v[196:199], v[188:191], v[172:175]\n"
"v_mfma_f32_16x16x32_bf16 v[180:183], v[200:203], v[188:191], v[180:183]\n"
"ds_read_b64_tr_b16 v[104:105], v16 offset:24576\n"
"v_xor_b32_e32 v247, 32, v16\n"
"v_mfma_f32_16x16x32_bf16 v[168:171], v[76:79], v[224:227], v[168:171]\n"
"ds_read_b64_tr_b16 v[106:107], v247 offset:24832\n"
"ds_read_b64_tr_b16 v[144:145], v247 offset:24576\n"
"ds_read_b64_tr_b16 v[146:147], v16 offset:24832\n"
"v_mov_b32_e32 v188, 0x3f803f80\n"
"v_mfma_f32_16x16x32_bf16 v[168:171], v[72:75], v[160:163], v[168:171]\n"
"v_mov_b32_e32 v189, 0x3f803f80\n"
"v_mov_b32_e32 v190, 0x3f803f80\n"
"v_mov_b32_e32 v191, 0x3f803f80\n"
"v_mfma_f32_16x16x32_bf16 v[168:171], v[68:71], v[164:167], v[168:171]\n"
"v_max_f32_e32 v247, v172, v173\n"
"v_max3_f32 v247, v247, v174, v175\n"
"v_max3_f32 v247, v247, v180, v181\n"
"v_mfma_f32_16x16x32_bf16 v[168:171], v[64:67], v[192:195], v[168:171]\n"
"ds_write_b128 v3, v[108:111] offset:4096\n"
"v_mfma_f32_16x16x32_bf16 v[168:171], v[60:63], v[88:91], v[168:171]\n"
"v_max3_f32 v247, v247, v182, v183\n"
"v_mov_b32_e32 v246, v247\n"
"v_mfma_f32_16x16x32_bf16 v[168:171], v[56:59], v[136:139], v[168:171]\n"
"s_waitcnt vmcnt(25)\n"
"v_permlane16_swap_b32_e32 v247, v246\n"
"v_max_f32_e32 v247, v247, v246\n"
"v_mov_b32_e32 v246, v247\n"
"; @B G1 2.1\n"
"v_mfma_f32_16x16x32_bf16 v[88:91], a[0:3], v[120:123], 0\n"
"buffer_load_dwordx4 a[200:203], v0, s[4:7], s96 offen offset:2048 sc1\n"
"v_permlane32_swap_b32_e32 v247, v246\n"
"v_mfma_f32_16x16x32_bf16 v[88:91], a[4:7], v[176:179], v[88:91]\n"
"v_max_f32_e32 v247, v247, v246\n"
"v_sub_f32_e32 v172, v172, v247\n"
"v_sub_f32_e32 v173, v173, v247\n"
"v_mfma_f32_16x16x32_bf16 v[88:91], a[8:11], v[184:187], v[88:91]\n"
"s_add_u32 s98, s55, 0x408000\n"
"buffer_load_dwordx4 v[108:111], v0, s[4:7], s98 offen sc1\n"
"v_mfma_f32_16x16x32_bf16 v[88:91], a[12:15], v[208:211], v[88:91]\n"
"s_waitcnt vmcnt(22)\n"
"v_sub_f32_e32 v174, v174, v247\n"
"v_sub_f32_e32 v175, v175, v247\n"
"v_sub_f32_e32 v180, v180, v247\n"
"v_mfma_f32_16x16x32_bf16 v[88:91], a[16:19], v[152:155], v[88:91]\n"
"v_sub_f32_e32 v181, v181, v247\n"
"v_sub_f32_e32 v182, v182, v247\n"
"v_sub_f32_e32 v183, v183, v247\n"
"v_mfma_f32_16x16x32_bf16 v[88:91], a[20:23], v[100:103], v[88:91]\n"
"ds_write_b128 v3, v[168:171] offset:5120\n"
"v_mfma_f32_16x16x32_bf16 v[88:91], a[24:27], v[116:119], v[88:91]\n"
"v_exp_f32_e32 v172, v172\n"
"v_mfma_f32_16x16x32_bf16 v[88:91], a[28:31], v[156:159], v[88:91]\n"
"buffer_load_dwordx4 v[136:139], v0, s[4:7], s98 offen offset:1024 sc1\n"
"v_mfma_f32_16x16x32_bf16 v[160:163], a[60:63], v[156:159], 0\n"
"s_add_u32 s97, s64, 0x806000\n"
"v_exp_f32_e32 v173, v173\n"
"v_mfma_f32_16x16x32_bf16 v[160:163], a[56:59], v[116:119], v[160:163]\n"
"v_exp_f32_e32 v174, v174\n"
"v_mfma_f32_16x16x32_bf16 v[160:163], a[52:55], v[100:103], v[160:163]\n"
"buffer_store_dwordx4 v[92:95], v7, s[12:15], s97 offen offset:1024 sc1\n"
"v_mfma_f32_16x16x32_bf16 v[160:163], a[48:51], v[152:155], v[160:163]\n"
"v_exp_f32_e32 v175, v175\n"
"v_mfma_f32_16x16x32_bf16 v[160:163], a[44:47], v[208:211], v[160:163]\n"
"buffer_load_dwordx4 v[92:95], v0, s[4:7], s98 offen offset:2048 sc1\n"
"v_mfma_f32_16x16x32_bf16 v[160:163], a[40:43], v[184:187], v[160:163]\n"
"ds_write_b128 v3, v[88:91] offset:6144\n"
"v_mfma_f32_16x16x32_bf16 v[160:163], a[36:39], v[176:179], v[160:163]\n"
"v_exp_f32_e32 v180, v180\n"
"v_mfma_f32_16x16x32_bf16 v[160:163], a[32:35], v[120:123], v[160:163]\n"
"v_exp_f32_e32 v181, v181\n"
"v_mfma_f32_16x16x32_bf16 v[88:91], a[64:67], v[120:123], 0\n"
"v_exp_f32_e32 v182, v182\n"
"v_mfma_f32_16x16x32_bf16 v[88:91], a[68:71], v[176:179], v[88:91]\n"
"buffer_load_dwordx4 v[164:167], v0, s[4:7], s98 offen offset:3072 sc1\n"
"v_mfma_f32_16x16x32_bf16 v[88:91], a[72:75], v[184:187], v[88:91]\n"
"v_exp_f32_e32 v183, v183\n"
"v_cvt_pk_bf16_f32 v168, v172, v173\n"
"v_mfma_f32_16x16x32_bf16 v[88:91], a[76:79], v[208:211], v[88:91]\n"
"v_cvt_pk_bf16_f32 v169, v174, v175\n"
"v_cvt_pk_bf16_f32 v170, v180, v181\n"
"v_cvt_pk_bf16_f32 v171, v182, v183\n"
"v_mfma_f32_16x16x32_bf16 v[88:91], a[80:83], v[152:155], v[88:91]\n"
"s_waitcnt lgkmcnt(3)\n"
"v_mov_b32_e32 v172, -1\n"
"v_mov_b32_e32 v173, -1\n"
"v_mfma_f32_16x16x32_bf16 v[180:183], v[188:191], v[168:171], 0\n"
"v_mfma_f32_16x16x32_bf16 v[188:191], v[104:107], v[168:171], 0\n"
"v_mfma_f32_16x16x32_bf16 v[104:107], v[144:147], v[168:171], 0\n"
"v_mfma_f32_16x16x32_bf16 v[88:91], a[84:87], v[100:103], v[88:91]\n"
"ds_write_b128 v3, v[160:163] offset:7168\n"
"v_mfma_f32_16x16x32_bf16 v[88:91], a[88:91], v[116:119], v[88:91]\n"
"buffer_load_dwordx4 v[144:147], v1, s[4:7], s98 offen sc1\n"
"v_mfma_f32_16x16x32_bf16 v[88:91], a[92:95], v[156:159], v[88:91]\n"
"v_mov_b32_e32 v174, -1\n"
"v_mov_b32_e32 v175, -1\n"
"v_mfma_f32_16x16x32_bf16 v[160:163], a[124:127], v[156:159], 0\n"
"s_add_u32 s97, s60, 0x848000\n"
"buffer_store_dwordx4 v[172:175], v0, s[8:11], s97 offen sc1\n"
"v_mfma_f32_16x16x32_bf16 v[160:163], a[120:123], v[116:119], v[160:163]\n"
"buffer_load_dwordx4 a[204:207], v0, s[4:7], s96 offen offset:3072 sc1\n"
"v_mfma_f32_16x16x32_bf16 v[160:163], a[116:119], v[100:103], v[160:163]\n"
"v_rcp_f32_e32 v180, v180\n"
"v_mfma_f32_16x16x32_bf16 v[160:163], a[112:115], v[152:155], v[160:163]\n"
"buffer_load_dwordx4 v[168:171], v1, s[4:7], s98 offen offset:1024 sc1\n"
"v_mul_f32_e32 v188, v188, v180\n"
"v_mfma_f32_16x16x32_bf16 v[160:163], a[108:111], v[208:211], v[160:163]\n"
"v_mul_f32_e32 v189, v189, v180\n"
"v_mul_f32_e32 v190, v190, v180\n"
"v_mul_f32_e32 v191, v191, v180\n"
"v_mfma_f32_16x16x32_bf16 v[160:163], a[104:107], v[184:187], v[160:163]\n"
"ds_write_b128 v3, v[88:91] offset:8192\n"
"v_mfma_f32_16x16x32_bf16 v[160:163], a[100:103], v[176:179], v[160:163]\n"
"v_mul_f32_e32 v104, v104, v180\n"
"v_mul_f32_e32 v105, v105, v180\n"
"v_mul_f32_e32 v106, v106, v180\n"
"v_mfma_f32_16x16x32_bf16 v[160:163], a[96:99], v[120:123], v[160:163]\n"
"v_mul_f32_e32 v107, v107, v180\n"
"v_cvt_pk_bf16_f32 v88, v188, v189\n"
"v_cvt_pk_bf16_f32 v89, v190, v191\n"
"v_mfma_f32_16x16x32_bf16 v[180:183], v[24:27], v[120:123], 0\n"
"buffer_load_dwordx4 v[188:191], v1, s[4:7], s98 offen offset:2048 sc1\n"
"v_cvt_pk_bf16_f32 v90, v104, v105\n"
"v_mfma_f32_16x16x32_bf16 v[180:183], v[28:31], v[176:179], v[180:183]\n"
"s_add_u32 s97, s60, 0x448000\n"
"v_cvt_pk_bf16_f32 v91, v106, v107\n"
"v_mfma_f32_16x16x32_bf16 v[180:183], v[32:35], v[184:187], v[180:183]\n"
"buffer_store_dwordx4 v[88:91], v0, s[4:7], s97 offen sc1\n"
"v_mfma_f32_16x16x32_bf16 v[180:183], v[36:39], v[208:211], v[180:183]\n"
"v_mfma_f32_16x16x32_bf16 v[180:183], v[40:43], v[152:155], v[180:183]\n"
"s_waitcnt vmcnt(14)\n"
"v_add_f32_e32 v255, v255, v253\n"
"v_add_f32_e32 v254, v254, v250\n"
"v_mfma_f32_16x16x32_bf16 v[180:183], v[44:47], v[100:103], v[180:183]\n"
"buffer_load_dwordx4 v[88:91], v1, s[4:7], s98 offen offset:3072 sc1\n"
"v_mfma_f32_16x16x32_bf16 v[180:183], v[48:51], v[116:119], v[180:183]\n"
"ds_write_b128 v3, v[160:163] offset:9216\n"
"v_mfma_f32_16x16x32_bf16 v[180:183], v[52:55], v[156:159], v[180:183]\n"
"v_add_f32_e32 v252, v252, v249\n"
"v_add_f32_e32 v251, v251, v248\n"
"v_mfma_f32_16x16x32_bf16 v[104:107], v[84:87], v[156:159], 0\n"
"v_add_f32_e32 v255, v255, v252\n"
"v_add_f32_e32 v254, v254, v251\n"
"v_mfma_f32_16x16x32_bf16 v[104:107], v[80:83], v[116:119], v[104:107]\n"
"v_add_f32_e32 v255, v255, v254\n"
"v_mov_b32_e32 v254, v255\n"
"v_mfma_f32_16x16x32_bf16 v[104:107], v[76:79], v[100:103], v[104:107]\n"
"s_nop 0\n"
"v_permlane32_swap_b32_e32 v255, v254\n"
"v_add_f32_e32 v255, v255, v254\n"
"v_mfma_f32_16x16x32_bf16 v[104:107], v[72:75], v[152:155], v[104:107]\n"
"v_mul_f32_e32 v255, 0x3a800000, v255\n"
"v_mov_b32_e32 v254, v255\n"
"v_mfma_f32_16x16x32_bf16 v[104:107], v[68:71], v[208:211], v[104:107]\n"
"s_nop 0\n"
"v_permlane16_swap_b32_e32 v255, v254\n"
"v_cvt_pk_bf16_f32 v253, -v255, 0\n"
"v_mfma_f32_16x16x32_bf16 v[104:107], v[64:67], v[184:187], v[104:107]\n"
"ds_write_b128 v3, v[180:183] offset:10240\n"
"v_mfma_f32_16x16x32_bf16 v[104:107], v[60:63], v[176:179], v[104:107]\n"
"buffer_load_dwordx4 a[208:211], v1, s[4:7], s96 offen sc1\n"
"v_mfma_f32_16x16x32_bf16 v[104:107], v[56:59], v[120:123], v[104:107]\n"
"v_fma_f32 v254, -v255, v255, v254\n"
"v_lshlrev_b32_e32 v253, 16, v253\n"
"; @B G1 2.2\n"
"v_mfma_f32_16x16x32_bf16 v[100:103], a[0:3], v[124:127], 0\n"
"v_max_f32_e32 v254, 0, v254\n"
"v_cvt_pk_bf16_f32 v254, v254, 0\n"
"v_mfma_f32_16x16x32_bf16 v[100:103], a[4:7], v[128:131], v[100:103]\n"
"v_lshlrev_b32_e32 v254, 16, v254\n"
"v_add_f32_e32 v254, 0x3727c5ac, v254\n"
"v_mfma_f32_16x16x32_bf16 v[100:103], a[8:11], v[140:143], v[100:103]\n"
"v_cvt_pk_bf16_f32 v254, v254, 0\n"
"v_lshlrev_b32_e32 v254, 16, v254\n"
"v_mfma_f32_16x16x32_bf16 v[100:103], a[12:15], v[148:151], v[100:103]\n"
"s_waitcnt vmcnt(14)\n"
"v_sqrt_f32_e32 v254, v254\n"
"v_mfma_f32_16x16x32_bf16 v[100:103], a[16:19], v[132:135], v[100:103]\n"
"s_add_u32 s98, s68, 0x10000\n"
"v_cvt_pk_bf16_f32 v254, v254, 0\n"
"v_mfma_f32_16x16x32_bf16 v[100:103], a[20:23], v[96:99], v[100:103]\n"
"ds_write_b128 v3, v[104:107] offset:11264\n"
"v_mfma_f32_16x16x32_bf16 v[100:103], a[24:27], v[112:115], v[100:103]\n"
"buffer_store_dwordx2 v[18:19], v11, s[4:7], s98 offen offset:32 sc1\n"
"v_mfma_f32_16x16x32_bf16 v[100:103], a[28:31], v[204:207], v[100:103]\n"
"v_lshlrev_b32_e32 v254, 16, v254\n"
"v_mfma_f32_16x16x32_bf16 v[104:107], a[60:63], v[204:207], 0\n"
"v_rcp_f32_e32 v255, v254\n"
"v_mfma_f32_16x16x32_bf16 v[104:107], a[56:59], v[112:115], v[104:107]\n"
"v_mul_f32_e32 v254, 0x3e8293ee, v255\n"
"v_mfma_f32_16x16x32_bf16 v[104:107], a[52:55], v[96:99], v[104:107]\n"
"s_add_u32 s98, s52, 0x400000\n"
"buffer_load_dwordx4 v[116:119], v0, s[4:7], s98 offen sc1\n"
"v_mfma_f32_16x16x32_bf16 v[104:107], a[48:51], v[132:135], v[104:107]\n"
"s_waitcnt lgkmcnt(0)\n"
"s_barrier\n"
"v_mfma_f32_16x16x32_bf16 v[104:107], a[44:47], v[148:151], v[104:107]\n"
"ds_read_b128 v[120:123], v4\n"
"ds_read_b128 v[152:155], v9 offset:640\n"
"v_mfma_f32_16x16x32_bf16 v[104:107], a[40:43], v[140:143], v[104:107]\n"
"ds_write_b128 v3, v[100:103] offset:12288\n"
"v_mfma_f32_16x16x32_bf16 v[104:107], a[36:39], v[128:131], v[104:107]\n"
"ds_read_b128 v[100:103], v5\n"
"v_mfma_f32_16x16x32_bf16 v[104:107], a[32:35], v[124:127], v[104:107]\n"
"buffer_load_dwordx4 v[156:159], v0, s[4:7], s98 offen offset:1024 sc1\n"
"v_mfma_f32_16x16x32_bf16 v[160:163], a[64:67], v[124:127], 0\n"
"v_mfma_f32_16x16x32_bf16 v[160:163], a[68:71], v[128:131], v[160:163]\n"
"v_mfma_f32_16x16x32_bf16 v[160:163], a[72:75], v[140:143], v[160:163]\n"
"buffer_load_dwordx4 a[212:215], v1, s[4:7], s96 offen offset:1024 sc1\n"
"v_mfma_f32_16x16x32_bf16 v[160:163], a[76:79], v[148:151], v[160:163]\n"
"ds_read_b128 v[176:179], v4 offset:1024\n"
"ds_read_b128 v[180:183], v9 offset:704\n"
"v_mfma_f32_16x16x32_bf16 v[160:163], a[80:83], v[132:135], v[160:163]\n"
"buffer_load_dwordx4 v[184:187], v0, s[4:7], s98 offen offset:2048 sc1\n"
"v_mfma_f32_16x16x32_bf16 v[160:163], a[84:87], v[96:99], v[160:163]\n"
"ds_write_b128 v3, v[104:107] offset:13312\n"
"v_mfma_f32_16x16x32_bf16 v[160:163], a[88:91], v[112:115], v[160:163]\n"
"ds_read_b128 v[104:107], v5 offset:1024\n"
"s_waitcnt lgkmcnt(4)\n"
"v_mfma_f32_16x16x32_bf16 v[160:163], a[92:95], v[204:207], v[160:163]\n"
"v_fmac_f32_e32 v120, v253, v152\n"
"v_fmac_f32_e32 v121, v253, v153\n"
"v_mfma_f32_16x16x32_bf16 v[192:195], a[124:127], v[204:207], 0\n"
"v_fmac_f32_e32 v122, v253, v154\n"
"v_fmac_f32_e32 v123, v253, v155\n"
"v_mfma_f32_16x16x32_bf16 v[192:195], a[120:123], v[112:115], v[192:195]\n"
"buffer_load_dwordx4 v[152:155], v0, s[4:7], s98 offen offset:3072 sc1\n"
"v_mfma_f32_16x16x32_bf16 v[192:195], a[116:119], v[96:99], v[192:195]\n"
"ds_read_b128 v[196:199], v4 offset:2048\n"
"ds_read_b128 v[200:203], v9 offset:768\n"
"v_mfma_f32_16x16x32_bf16 v[192:195], a[112:115], v[132:135], v[192:195]\n"
"ds_read_b128 v[208:211], v5 offset:2048\n"
"v_add_f32_e32 v120, v120, v100\n"
"v_mfma_f32_16x16x32_bf16 v[192:195], a[108:111], v[148:151], v[192:195]\n"
"v_add_f32_e32 v121, v121, v101\n"
"v_add_f32_e32 v122, v122, v102\n"
"v_mfma_f32_16x16x32_bf16 v[192:195], a[104:107], v[140:143], v[192:195]\n"
"ds_write_b128 v3, v[160:163] offset:14336\n"
"v_mfma_f32_16x16x32_bf16 v[192:195], a[100:103], v[128:131], v[192:195]\n"
"buffer_load_dwordx4 v[160:163], v1, s[4:7], s98 offen sc1\n"
"v_mfma_f32_16x16x32_bf16 v[192:195], a[96:99], v[124:127], v[192:195]\n"
"s_add_u32 s97, s60, 0x840000\n"
"v_add_f32_e32 v123, v123, v103\n"
"s_waitcnt lgkmcnt(4)\n"
"v_mfma_f32_16x16x32_bf16 v[100:103], v[24:27], v[124:127], 0\n"
"buffer_store_dwordx4 v[172:175], v0, s[8:11], s97 offen sc1\n"
"v_mfma_f32_16x16x32_bf16 v[100:103], v[28:31], v[128:131], v[100:103]\n"
"ds_read_b128 v[212:215], v4 offset:3072\n"
"ds_read_b128 v[216:219], v9 offset:832\n"
"v_mfma_f32_16x16x32_bf16 v[100:103], v[32:35], v[140:143], v[100:103]\n"
"s_waitcnt vmcnt(18)\n"
"s_add_u32 s97, s68, 0x11000\n"
"ds_read_b128 v[220:223], v5 offset:3072\n"
"v_mfma_f32_16x16x32_bf16 v[100:103], v[36:39], v[148:151], v[100:103]\n"
"buffer_load_dwordx4 v[224:227], v1, s[4:7], s98 offen offset:1024 sc1\n"
"v_mfma_f32_16x16x32_bf16 v[100:103], v[40:43], v[132:135], v[100:103]\n"
"buffer_store_dwordx2 v[18:19], v11, s[12:15], s97 offen offset:64 sc1\n"
"v_mfma_f32_16x16x32_bf16 v[100:103], v[44:47], v[96:99], v[100:103]\n"
"ds_write_b128 v3, v[192:195] offset:15360\n"
"v_mfma_f32_16x16x32_bf16 v[100:103], v[48:51], v[112:115], v[100:103]\n"
"buffer_load_dwordx4 a[216:219], v1, s[4:7], s96 offen offset:2048 sc1\n"
"v_mfma_f32_16x16x32_bf16 v[100:103], v[52:55], v[204:207], v[100:103]\n"
"v_fmac_f32_e32 v176, v253, v180\n"
"v_fmac_f32_e32 v177, v253, v181\n"
"v_mfma_f32_16x16x32_bf16 v[192:195], v[84:87], v[204:207], 0\n"
"buffer_load_dwordx4 v[204:207], v1, s[4:7], s98 offen offset:2048 sc1\n"
"v_mfma_f32_16x16x32_bf16 v[192:195], v[80:83], v[112:115], v[192:195]\n"
"ds_read_b128 v[112:115], v4 offset:4096\n"
"ds_read_b128 v[228:231], v9 offset:512\n"
"v_mfma_f32_16x16x32_bf16 v[192:195], v[76:79], v[96:99], v[192:195]\n"
"ds_read_b128 v[96:99], v5 offset:4096\n"
"v_fmac_f32_e32 v178, v253, v182\n"
"v_mfma_f32_16x16x32_bf16 v[192:195], v[72:75], v[132:135], v[192:195]\n"
"v_fmac_f32_e32 v179, v253, v183\n"
"v_add_f32_e32 v176, v176, v104\n"
"v_mfma_f32_16x16x32_bf16 v[192:195], v[68:71], v[148:151], v[192:195]\n"
"v_add_f32_e32 v177, v177, v105\n"
"v_add_f32_e32 v178, v178, v106\n"
"v_mfma_f32_16x16x32_bf16 v[192:195], v[64:67], v[140:143], v[192:195]\n"
"buffer_load_dwordx4 v[132:135], v1, s[4:7], s98 offen offset:3072 sc1\n"
"v_mfma_f32_16x16x32_bf16 v[192:195], v[60:63], v[128:131], v[192:195]\n"
"ds_write_b128 v3, v[100:103] offset:16384\n"
"v_mfma_f32_16x16x32_bf16 v[192:195], v[56:59], v[124:127], v[192:195]\n"
"s_waitcnt vmcnt(21)\n"
"ds_read_b128 v[100:103], v4 offset:5120\n"
"ds_read_b128 v[124:127], v9 offset:576\n"
"; @B G1 2.3\n"
"v_mfma_f32_16x16x32_bf16 v[120:123], a[0:3], v[108:111], v[120:123]\n"
"ds_read_b128 v[128:131], v5 offset:5120\n"
"v_add_f32_e32 v179, v179, v107\n"
"v_mfma_f32_16x16x32_bf16 v[120:123], a[4:7], v[136:139], v[120:123]\n"
"s_waitcnt lgkmcnt(12)\n"
"v_fmac_f32_e32 v196, v253, v200\n"
"v_fmac_f32_e32 v197, v253, v201\n"
"v_mfma_f32_16x16x32_bf16 v[120:123], a[8:11], v[92:95], v[120:123]\n"
"s_add_u32 s98, s53, 0x400000\n"
"buffer_load_dwordx4 v[104:107], v0, s[4:7], s98 offen sc1\n"
"v_mfma_f32_16x16x32_bf16 v[120:123], a[12:15], v[164:167], v[120:123]\n"
"s_waitcnt vmcnt(15)\n"
"v_fmac_f32_e32 v198, v253, v202\n"
"v_fmac_f32_e32 v199, v253, v203\n"
"v_mfma_f32_16x16x32_bf16 v[120:123], a[16:19], v[144:147], v[120:123]\n"
"v_add_f32_e32 v196, v196, v208\n"
"v_add_f32_e32 v197, v197, v209\n"
"v_mfma_f32_16x16x32_bf16 v[120:123], a[20:23], v[168:171], v[120:123]\n"
"ds_write_b128 v3, v[192:195] offset:17408\n"
"v_mfma_f32_16x16x32_bf16 v[120:123], a[24:27], v[188:191], v[120:123]\n"
"s_waitcnt lgkmcnt(9)\n"
"v_add_f32_e32 v198, v198, v210\n"
"v_add_f32_e32 v199, v199, v211\n"
"v_mfma_f32_16x16x32_bf16 v[120:123], a[28:31], v[88:91], v[120:123]\n"
"buffer_load_dwordx4 v[140:143], v0, s[4:7], s98 offen offset:1024 sc1\n"
"v_mfma_f32_16x16x32_bf16 v[176:179], a[60:63], v[88:91], v[176:179]\n"
"v_fmac_f32_e32 v212, v253, v216\n"
"v_fmac_f32_e32 v213, v253, v217\n"
"v_mfma_f32_16x16x32_bf16 v[176:179], a[56:59], v[188:191], v[176:179]\n"
"v_fmac_f32_e32 v214, v253, v218\n"
"v_fmac_f32_e32 v215, v253, v219\n"
"v_mfma_f32_16x16x32_bf16 v[176:179], a[52:55], v[168:171], v[176:179]\n"
"s_waitcnt lgkmcnt(0)\n"
"s_barrier\n"
"v_mfma_f32_16x16x32_bf16 v[176:179], a[48:51], v[144:147], v[176:179]\n"
"buffer_load_dwordx4 a[220:223], v1, s[4:7], s96 offen offset:3072 sc1\n"
"v_mfma_f32_16x16x32_bf16 v[176:179], a[44:47], v[164:167], v[176:179]\n"
"buffer_load_dwordx4 v[148:151], v0, s[4:7], s98 offen offset:2048 sc1\n"
"v_mfma_f32_16x16x32_bf16 v[176:179], a[40:43], v[92:95], v[176:179]\n"
"ds_read_b128 v[180:183], v6\n"
"ds_read_b128 v[192:195], v6 offset:1024\n"
"v_mfma_f32_16x16x32_bf16 v[176:179], a[36:39], v[136:139], v[176:179]\n"
"v_add_f32_e32 v212, v212, v220\n"
"v_add_f32_e32 v213, v213, v221\n"
"v_mfma_f32_16x16x32_bf16 v[176:179], a[32:35], v[108:111], v[176:179]\n"
"v_add_f32_e32 v214, v214, v222\n"
"v_add_f32_e32 v215, v215, v223\n"
"v_mfma_f32_16x16x32_bf16 v[196:199], a[64:67], v[108:111], v[196:199]\n"
"v_fmac_f32_e32 v112, v253, v228\n"
"v_fmac_f32_e32 v113, v253, v229\n"
"v_mfma_f32_16x16x32_bf16 v[196:199], a[68:71], v[136:139], v[196:199]\n"
"buffer_load_dwordx4 v[200:203], v0, s[4:7], s98 offen offset:3072 sc1\n"
"v_mfma_f32_16x16x32_bf16 v[196:199], a[72:75], v[92:95], v[196:199]\n"
"v_fmac_f32_e32 v114, v253, v230\n"
"v_fmac_f32_e32 v115, v253, v231\n"
"v_mfma_f32_16x16x32_bf16 v[196:199], a[76:79], v[164:167], v[196:199]\n"
"v_add_f32_e32 v112, v112, v96\n"
"v_add_f32_e32 v113, v113, v97\n"
"v_mfma_f32_16x16x32_bf16 v[196:199], a[80:83], v[144:147], v[196:199]\n"
"v_add_f32_e32 v114, v114, v98\n"
"v_add_f32_e32 v115, v115, v99\n"
"v_mfma_f32_16x16x32_bf16 v[196:199], a[84:87], v[168:171], v[196:199]\n"
"s_waitcnt lgkmcnt(0)\n"
"v_fmac_f32_e32 v100, v253, v124\n"
"v_fmac_f32_e32 v101, v253, v125\n"
"v_mfma_f32_16x16x32_bf16 v[196:199], a[88:91], v[188:191], v[196:199]\n"
"buffer_load_dwordx4 v[96:99], v1, s[4:7], s98 offen sc1\n"
"v_mfma_f32_16x16x32_bf16 v[196:199], a[92:95], v[88:91], v[196:199]\n"
"ds_read_b128 v[208:211], v6 offset:2048\n"
"ds_read_b128 v[216:219], v6 offset:3072\n"
"v_mfma_f32_16x16x32_bf16 v[212:215], a[124:127], v[88:91], v[212:215]\n"
"v_fmac_f32_e32 v102, v253, v126\n"
"v_fmac_f32_e32 v103, v253, v127\n"
"v_mfma_f32_16x16x32_bf16 v[212:215], a[120:123], v[188:191], v[212:215]\n"
"v_add_f32_e32 v100, v100, v128\n"
"v_add_f32_e32 v101, v101, v129\n"
"v_mfma_f32_16x16x32_bf16 v[212:215], a[116:119], v[168:171], v[212:215]\n"
"v_add_f32_e32 v102, v102, v130\n"
"v_add_f32_e32 v103, v103, v131\n"
"v_mfma_f32_16x16x32_bf16 v[212:215], a[112:115], v[144:147], v[212:215]\n"
"buffer_load_dwordx4 v[124:127], v1, s[4:7], s98 offen offset:1024 sc1\n"
"v_mfma_f32_16x16x32_bf16 v[212:215], a[108:111], v[164:167], v[212:215]\n"
"v_add_f32_e32 v120, v120, v180\n"
"v_add_f32_e32 v121, v121, v181\n"
"v_mfma_f32_16x16x32_bf16 v[212:215], a[104:107], v[92:95], v[212:215]\n"
"s_add_u32 s97, s70, 0x206000\n"
"v_add_f32_e32 v122, v122, v182\n"
"v_add_f32_e32 v123, v123, v183\n"
"v_mfma_f32_16x16x32_bf16 v[212:215], a[100:103], v[136:139], v[212:215]\n"
"v_add_f32_e32 v176, v176, v192\n"
"v_add_f32_e32 v177, v177, v193\n"
"v_mfma_f32_16x16x32_bf16 v[212:215], a[96:99], v[108:111], v[212:215]\n"
"buffer_load_dwordx4 a[224:227], v0, s[4:7], s97 offen sc1\n"
"v_mfma_f32_16x16x32_bf16 v[112:115], v[24:27], v[108:111], v[112:115]\n"
"buffer_load_dwordx4 v[128:131], v1, s[4:7], s98 offen offset:2048 sc1\n"
"v_mfma_f32_16x16x32_bf16 v[112:115], v[28:31], v[136:139], v[112:115]\n"
"v_add_f32_e32 v178, v178, v194\n"
"v_add_f32_e32 v179, v179, v195\n"
"v_mfma_f32_16x16x32_bf16 v[112:115], v[32:35], v[92:95], v[112:115]\n"
"v_mul_f32_e32 v120, v120, v255\n"
"v_mul_f32_e32 v121, v121, v255\n"
"v_mfma_f32_16x16x32_bf16 v[112:115], v[36:39], v[164:167], v[112:115]\n"
"v_mul_f32_e32 v122, v122, v255\n"
"v_mul_f32_e32 v123, v123, v255\n"
"v_mfma_f32_16x16x32_bf16 v[112:115], v[40:43], v[144:147], v[112:115]\n"
"s_waitcnt lgkmcnt(0)\n"
"v_mul_f32_e32 v176, v176, v255\n"
"v_mul_f32_e32 v177, v177, v255\n"
"v_mfma_f32_16x16x32_bf16 v[112:115], v[44:47], v[168:171], v[112:115]\n"
"buffer_load_dwordx4 v[180:183], v1, s[4:7], s98 offen offset:3072 sc1\n"
"v_mfma_f32_16x16x32_bf16 v[112:115], v[48:51], v[188:191], v[112:115]\n"
"s_add_u32 s98, s60, 0x800000\n"
"ds_read_b128 v[192:195], v6 offset:4096\n"
"ds_read_b128 v[220:223], v6 offset:5120\n"
"v_mfma_f32_16x16x32_bf16 v[112:115], v[52:55], v[88:91], v[112:115]\n"
"v_mul_f32_e32 v178, v178, v255\n"
"v_mul_f32_e32 v179, v179, v255\n"
"v_mfma_f32_16x16x32_bf16 v[100:103], v[84:87], v[88:91], v[100:103]\n"
"buffer_store_dwordx4 v[172:175], v0, s[8:11], s98 offen sc1\n"
"v_mfma_f32_16x16x32_bf16 v[100:103], v[80:83], v[188:191], v[100:103]\n"
"v_cvt_pk_bf16_f32 v88, v120, v121\n"
"v_cvt_pk_bf16_f32 v89, v122, v123\n"
"v_mfma_f32_16x16x32_bf16 v[100:103], v[76:79], v[168:171], v[100:103]\n"
"v_cvt_pk_bf16_f32 v90, v176, v177\n"
"v_cvt_pk_bf16_f32 v91, v178, v179\n"
"v_mfma_f32_16x16x32_bf16 v[100:103], v[72:75], v[144:147], v[100:103]\n"
"ds_write_b128 v8, v[88:91] offset:17408\n"
"v_mfma_f32_16x16x32_bf16 v[100:103], v[68:71], v[164:167], v[100:103]\n"
"v_add_f32_e32 v196, v196, v208\n"
"v_add_f32_e32 v197, v197, v209\n"
"v_mfma_f32_16x16x32_bf16 v[100:103], v[64:67], v[92:95], v[100:103]\n"
"v_add_f32_e32 v198, v198, v210\n"
"v_add_f32_e32 v199, v199, v211\n"
"v_mfma_f32_16x16x32_bf16 v[100:103], v[60:63], v[136:139], v[100:103]\n"
"v_add_f32_e32 v212, v212, v216\n"
"v_add_f32_e32 v213, v213, v217\n"
"v_mfma_f32_16x16x32_bf16 v[100:103], v[56:59], v[108:111], v[100:103]\n"
"v_add_f32_e32 v214, v214, v218\n"
"v_add_f32_e32 v215, v215, v219\n"
"v_mul_f32_e32 v196, v196, v255\n"
"v_mul_f32_e32 v197, v197, v255\n"
"v_mul_f32_e32 v198, v198, v255\n"
"v_mul_f32_e32 v199, v199, v255\n"
"v_mul_f32_e32 v212, v212, v255\n"
"v_mul_f32_e32 v213, v213, v255\n"
"v_mul_f32_e32 v214, v214, v255\n"
"v_mul_f32_e32 v215, v215, v255\n"
"v_cvt_pk_bf16_f32 v88, v196, v197\n"
"v_cvt_pk_bf16_f32 v89, v198, v199\n"
"v_cvt_pk_bf16_f32 v90, v212, v213\n"
"v_cvt_pk_bf16_f32 v91, v214, v215\n"
"ds_write_b128 v7, v[88:91] offset:50176\n"
".Lmk_b3:\n"
"s_waitcnt vmcnt(18)\n"
"; @B G1 3.0\n"
"v_mfma_f32_16x16x32_bf16 v[88:91], a[0:3], v[116:119], 0\n"
"v_mfma_f32_16x16x32_bf16 v[88:91], a[4:7], v[156:159], v[88:91]\n"
"s_add_u32 s98, s62, 0x40000\n"
"v_mfma_f32_16x16x32_bf16 v[88:91], a[8:11], v[184:187], v[88:91]\n"
"s_add_u32 s96, s54, 0x400000\n"
"buffer_load_dwordx4 v[92:95], v0, s[4:7], s96 offen sc1\n"
"v_mfma_f32_16x16x32_bf16 v[88:91], a[12:15], v[152:155], v[88:91]\n"
"s_waitcnt vmcnt(12)\n"
"v_mfma_f32_16x16x32_bf16 v[88:91], a[16:19], v[160:163], v[88:91]\n"
"buffer_load_dword v255, v10, s[4:7], s98 offen sc1\n"
"v_mfma_f32_16x16x32_bf16 v[88:91], a[20:23], v[224:227], v[88:91]\n"
"s_waitcnt lgkmcnt(2)\n"
"v_add_f32_e32 v112, v112, v192\n"
"v_add_f32_e32 v113, v113, v193\n"
"v_mfma_f32_16x16x32_bf16 v[88:91], a[24:27], v[204:207], v[88:91]\n"
"v_add_f32_e32 v114, v114, v194\n"
"v_add_f32_e32 v115, v115, v195\n"
"v_mfma_f32_16x16x32_bf16 v[88:91], a[28:31], v[132:135], v[88:91]\n"
"buffer_load_dwordx4 v[108:111], v0, s[4:7], s96 offen offset:1024 sc1\n"
"v_mfma_f32_16x16x32_bf16 v[120:123], a[60:63], v[132:135], 0\n"
"s_waitcnt lgkmcnt(0)\n"
"s_barrier\n"
"v_mfma_f32_16x16x32_bf16 v[120:123], a[56:59], v[204:207], v[120:123]\n"
"buffer_load_dwordx4 a[228:231], v0, s[4:7], s97 offen offset:1024 sc1\n"
"v_mfma_f32_16x16x32_bf16 v[120:123], a[52:55], v[224:227], v[120:123]\n"
"buffer_load_dword v253, v10, s[4:7], s98 offen offset:256 sc1\n"
"v_mfma_f32_16x16x32_bf16 v[120:123], a[48:51], v[160:163], v[120:123]\n"
"v_add_f32_e32 v100, v100, v220\n"
"v_add_f32_e32 v101, v101, v221\n"
"v_mfma_f32_16x16x32_bf16 v[120:123], a[44:47], v[152:155], v[120:123]\n"
"buffer_load_dwordx4 v[136:139], v0, s[4:7], s96 offen offset:2048 sc1\n"
"v_mfma_f32_16x16x32_bf16 v[120:123], a[40:43], v[184:187], v[120:123]\n"
"ds_write_b128 v3, v[88:91]\n"
"v_mfma_f32_16x16x32_bf16 v[120:123], a[36:39], v[156:159], v[120:123]\n"
"buffer_load_dword v252, v10, s[4:7], s98 offen offset:512 sc1\n"
"v_mfma_f32_16x16x32_bf16 v[120:123], a[32:35], v[116:119], v[120:123]\n"
"v_add_f32_e32 v102, v102, v222\n"
"v_add_f32_e32 v103, v103, v223\n"
"v_mfma_f32_16x16x32_bf16 v[88:91], a[64:67], v[116:119], 0\n"
"v_mul_f32_e32 v112, v112, v254\n"
"v_mul_f32_e32 v113, v113, v254\n"
"v_mfma_f32_16x16x32_bf16 v[88:91], a[68:71], v[156:159], v[88:91]\n"
"buffer_load_dwordx4 v[144:147], v0, s[4:7], s96 offen offset:3072 sc1\n"
"v_mfma_f32_16x16x32_bf16 v[88:91], a[72:75], v[184:187], v[88:91]\n"
"buffer_load_dword v251, v10, s[4:7], s98 offen offset:768 sc1\n"
"v_mfma_f32_16x16x32_bf16 v[88:91], a[76:79], v[152:155], v[88:91]\n"
"v_mul_f32_e32 v114, v114, v254\n"
"v_mul_f32_e32 v115, v115, v254\n"
"v_mfma_f32_16x16x32_bf16 v[88:91], a[80:83], v[160:163], v[88:91]\n"
"v_mul_f32_e32 v100, v100, v254\n"
"v_mul_f32_e32 v101, v101, v254\n"
"v_add_u32_e32 v250, 0x26c00, v0\n"
"v_mfma_f32_16x16x32_bf16 v[88:91], a[84:87], v[224:227], v[88:91]\n"
"ds_write_b128 v3, v[120:123] offset:1024\n"
"v_mfma_f32_16x16x32_bf16 v[88:91], a[88:91], v[204:207], v[88:91]\n"
"buffer_load_dwordx4 v[120:123], v1, s[4:7], s96 offen sc1\n"
"ds_read_b128 v[164:167], v250\n"
"v_mfma_f32_16x16x32_bf16 v[88:91], a[92:95], v[132:135], v[88:91]\n"
"buffer_load_dwordx4 a[232:235], v0, s[4:7], s97 offen offset:2048 sc1\n"
"ds_read_b128 v[168:171], v250 offset:1024\n"
"v_mfma_f32_16x16x32_bf16 v[172:175], a[124:127], v[132:135], 0\n"
"buffer_load_dword v250, v10, s[4:7], s98 offen offset:1024 sc1\n"
"v_xor_b32_e32 v249, 32, v17\n"
"v_mfma_f32_16x16x32_bf16 v[172:175], a[120:123], v[204:207], v[172:175]\n"
"ds_read_b128 v[176:179], v2 offset:49152\n"
"v_mul_f32_e32 v102, v102, v254\n"
"v_mul_f32_e32 v103, v103, v254\n"
"v_mfma_f32_16x16x32_bf16 v[172:175], a[116:119], v[224:227], v[172:175]\n"
"v_cvt_pk_bf16_f32 v188, v112, v113\n"
"v_cvt_pk_bf16_f32 v189, v114, v115\n"
"v_mfma_f32_16x16x32_bf16 v[172:175], a[112:115], v[160:163], v[172:175]\n"
"ds_read_b128 v[112:115], v17 offset:23552\n"
"buffer_load_dwordx4 v[192:195], v1, s[4:7], s96 offen offset:1024 sc1\n"
"ds_read_b128 v[196:199], v249 offset:23808\n"
"v_mfma_f32_16x16x32_bf16 v[172:175], a[108:111], v[152:155], v[172:175]\n"
"buffer_load_dword v254, v10, s[4:7], s98 offen offset:1280 sc1\n"
"v_mfma_f32_16x16x32_bf16 v[172:175], a[104:107], v[184:187], v[172:175]\n"
"ds_write_b128 v3, v[88:91] offset:2048\n"
"v_mfma_f32_16x16x32_bf16 v[172:175], a[100:103], v[156:159], v[172:175]\n"
"v_cvt_pk_bf16_f32 v190, v100, v101\n"
"v_cvt_pk_bf16_f32 v191, v102, v103\n"
"v_mfma_f32_16x16x32_bf16 v[172:175], a[96:99], v[116:119], v[172:175]\n"
"buffer_load_dword v249, v10, s[4:7], s98 offen offset:1536 sc1\n"
"v_mfma_f32_16x16x32_bf16 v[88:91], v[24:27], v[116:119], 0\n"
"buffer_load_dwordx4 v[100:103], v1, s[4:7], s96 offen offset:2048 sc1\n"
"s_waitcnt lgkmcnt(1)\n"
"v_mfma_f32_16x16x32_bf16 v[164:167], v[112:115], v[176:179], v[164:167]\n"
"v_mfma_f32_16x16x32_bf16 v[168:171], v[196:199], v[176:179], v[168:171]\n"
"v_mfma_f32_16x16x32_bf16 v[88:91], v[28:31], v[156:159], v[88:91]\n"
"ds_write_b128 v2, v[188:191] offset:17408\n"
"v_mfma_f32_16x16x32_bf16 v[88:91], v[32:35], v[184:187], v[88:91]\n"
"buffer_load_dword v248, v10, s[4:7], s98 offen offset:1792 sc1\n"
"v_xor_b32_e32 v247, 32, v16\n"
"v_mfma_f32_16x16x32_bf16 v[88:91], v[36:39], v[152:155], v[88:91]\n"
"ds_read_b64_tr_b16 v[112:113], v16 offset:23552\n"
"ds_read_b64_tr_b16 v[114:115], v247 offset:23808\n"
"ds_read_b64_tr_b16 v[176:177], v247 offset:23552\n"
"v_mfma_f32_16x16x32_bf16 v[88:91], v[40:43], v[160:163], v[88:91]\n"
"buffer_load_dwordx4 a[236:239], v0, s[4:7], s97 offen offset:3072 sc1\n"
"ds_read_b64_tr_b16 v[178:179], v16 offset:23808\n"
"v_mfma_f32_16x16x32_bf16 v[88:91], v[44:47], v[224:227], v[88:91]\n"
"buffer_load_dwordx4 v[188:191], v1, s[4:7], s96 offen offset:3072 sc1\n"
"v_mov_b32_e32 v196, 0x3f803f80\n"
"v_mfma_f32_16x16x32_bf16 v[88:91], v[48:51], v[204:207], v[88:91]\n"
"ds_write_b128 v3, v[172:175] offset:3072\n"
"v_mfma_f32_16x16x32_bf16 v[88:91], v[52:55], v[132:135], v[88:91]\n"
"v_mov_b32_e32 v197, 0x3f803f80\n"
"v_mov_b32_e32 v198, 0x3f803f80\n"
"v_mov_b32_e32 v199, 0x3f803f80\n"
"v_mfma_f32_16x16x32_bf16 v[172:175], v[84:87], v[132:135], 0\n"
"v_max_f32_e32 v247, v164, v165\n"
"v_max3_f32 v247, v247, v166, v167\n"
"v_max3_f32 v247, v247, v168, v169\n"
"v_mfma_f32_16x16x32_bf16 v[172:175], v[80:83], v[204:207], v[172:175]\n"
"v_max3_f32 v247, v247, v170, v171\n"
"v_mov_b32_e32 v246, v247\n"
"v_mfma_f32_16x16x32_bf16 v[172:175], v[76:79], v[224:227], v[172:175]\n"
"s_nop 0\n"
"v_permlane16_swap_b32_e32 v247, v246\n"
"v_max_f32_e32 v247, v247, v246\n"
"v_mov_b32_e32 v246, v247\n"
"v_mfma_f32_16x16x32_bf16 v[172:175], v[72:75], v[160:163], v[172:175]\n"
"s_nop 0\n"
"v_permlane32_swap_b32_e32 v247, v246\n"
"v_max_f32_e32 v247, v247, v246\n"
"v_sub_f32_e32 v164, v164, v247\n"
"v_mfma_f32_16x16x32_bf16 v[172:175], v[68:71], v[152:155], v[172:175]\n"
"v_sub_f32_e32 v165, v165, v247\n"
"v_sub_f32_e32 v166, v166, v247\n"
"v_sub_f32_e32 v167, v167, v247\n"
"v_mfma_f32_16x16x32_bf16 v[172:175], v[64:67], v[184:187], v[172:175]\n"
"ds_write_b128 v3, v[88:91] offset:4096\n"
"v_mfma_f32_16x16x32_bf16 v[172:175], v[60:63], v[156:159], v[172:175]\n"
"v_sub_f32_e32 v168, v168, v247\n"
"v_sub_f32_e32 v169, v169, v247\n"
"v_sub_f32_e32 v170, v170, v247\n"
"v_mfma_f32_16x16x32_bf16 v[172:175], v[56:59], v[116:119], v[172:175]\n"
"s_waitcnt vmcnt(25)\n"
"v_sub_f32_e32 v171, v171, v247\n"
"v_exp_f32_e32 v164, v164\n"
"; @B G1 3.1\n"
"v_mfma_f32_16x16x32_bf16 v[88:91], a[0:3], v[104:107], 0\n"
"v_exp_f32_e32 v165, v165\n"
"v_mfma_f32_16x16x32_bf16 v[88:91], a[4:7], v[140:143], v[88:91]\n"
"buffer_load_dwordx4 a[240:243], v1, s[4:7], s97 offen sc1\n"
"v_mfma_f32_16x16x32_bf16 v[88:91], a[8:11], v[148:151], v[88:91]\n"
"s_add_u32 s98, s55, 0x400000\n"
"buffer_load_dwordx4 v[116:119], v0, s[4:7], s98 offen sc1\n"
"v_mfma_f32_16x16x32_bf16 v[88:91], a[12:15], v[200:203], v[88:91]\n"
"s_waitcnt vmcnt(22)\n"
"v_exp_f32_e32 v166, v166\n"
"v_mfma_f32_16x16x32_bf16 v[88:91], a[16:19], v[96:99], v[88:91]\n"
"v_exp_f32_e32 v167, v167\n"
"v_mfma_f32_16x16x32_bf16 v[88:91], a[20:23], v[124:127], v[88:91]\n"
"ds_write_b128 v3, v[172:175] offset:5120\n"
"v_mfma_f32_16x16x32_bf16 v[88:91], a[24:27], v[128:131], v[88:91]\n"
"v_exp_f32_e32 v168, v168\n"
"v_mfma_f32_16x16x32_bf16 v[88:91], a[28:31], v[180:183], v[88:91]\n"
"buffer_load_dwordx4 v[132:135], v0, s[4:7], s98 offen offset:1024 sc1\n"
"v_mfma_f32_16x16x32_bf16 v[152:155], a[60:63], v[180:183], 0\n"
"v_exp_f32_e32 v169, v169\n"
"v_mfma_f32_16x16x32_bf16 v[152:155], a[56:59], v[128:131], v[152:155]\n"
"v_exp_f32_e32 v170, v170\n"
"v_mfma_f32_16x16x32_bf16 v[152:155], a[52:55], v[124:127], v[152:155]\n"
"v_exp_f32_e32 v171, v171\n"
"v_cvt_pk_bf16_f32 v156, v164, v165\n"
"v_mfma_f32_16x16x32_bf16 v[152:155], a[48:51], v[96:99], v[152:155]\n"
"v_cvt_pk_bf16_f32 v157, v166, v167\n"
"v_cvt_pk_bf16_f32 v158, v168, v169\n"
"v_cvt_pk_bf16_f32 v159, v170, v171\n"
"v_mfma_f32_16x16x32_bf16 v[152:155], a[44:47], v[200:203], v[152:155]\n"
"buffer_load_dwordx4 v[160:163], v0, s[4:7], s98 offen offset:2048 sc1\n"
"s_waitcnt lgkmcnt(3)\n"
"v_mfma_f32_16x16x32_bf16 v[164:167], v[196:199], v[156:159], 0\n"
"v_mfma_f32_16x16x32_bf16 v[168:171], v[112:115], v[156:159], 0\n"
"v_mfma_f32_16x16x32_bf16 v[112:115], v[176:179], v[156:159], 0\n"
"v_mfma_f32_16x16x32_bf16 v[152:155], a[40:43], v[148:151], v[152:155]\n"
"ds_write_b128 v3, v[88:91] offset:6144\n"
"v_mfma_f32_16x16x32_bf16 v[152:155], a[36:39], v[140:143], v[152:155]\n"
"s_add_u32 s96, s73, 0\n"
"buffer_load_dwordx2 v[246:247], off, s[4:7], s96 sc1\n"
"v_mfma_f32_16x16x32_bf16 v[152:155], a[32:35], v[104:107], v[152:155]\n"
"buffer_load_dwordx4 a[244:247], v1, s[4:7], s97 offen offset:1024 sc1\n"
"v_mfma_f32_16x16x32_bf16 v[88:91], a[64:67], v[104:107], 0\n"
"v_mfma_f32_16x16x32_bf16 v[88:91], a[68:71], v[140:143], v[88:91]\n"
"buffer_load_dwordx4 v[156:159], v0, s[4:7], s98 offen offset:3072 sc1\n"
"v_mfma_f32_16x16x32_bf16 v[88:91], a[72:75], v[148:151], v[88:91]\n"
"v_rcp_f32_e32 v164, v164\n"
"v_mfma_f32_16x16x32_bf16 v[88:91], a[76:79], v[200:203], v[88:91]\n"
"v_mul_f32_e32 v168, v168, v164\n"
"v_mul_f32_e32 v169, v169, v164\n"
"v_mul_f32_e32 v170, v170, v164\n"
"v_mfma_f32_16x16x32_bf16 v[88:91], a[80:83], v[96:99], v[88:91]\n"
"v_mov_b32_e32 v172, -1\n"
"v_mov_b32_e32 v173, -1\n"
"v_mul_f32_e32 v171, v171, v164\n"
"v_mfma_f32_16x16x32_bf16 v[88:91], a[84:87], v[124:127], v[88:91]\n"
"ds_write_b128 v3, v[152:155] offset:7168\n"
"v_mfma_f32_16x16x32_bf16 v[88:91], a[88:91], v[128:131], v[88:91]\n"
"buffer_load_dwordx4 v[152:155], v1, s[4:7], s98 offen sc1\n"
"v_mul_f32_e32 v112, v112, v164\n"
"v_mfma_f32_16x16x32_bf16 v[88:91], a[92:95], v[180:183], v[88:91]\n"
"v_mov_b32_e32 v174, -1\n"
"v_mov_b32_e32 v175, -1\n"
"v_mul_f32_e32 v113, v113, v164\n"
"v_mfma_f32_16x16x32_bf16 v[176:179], a[124:127], v[180:183], 0\n"
"s_add_u32 s95, s60, 0x808000\n"
"buffer_store_dwordx4 v[172:175], v0, s[8:11], s95 offen sc1\n"
"v_mfma_f32_16x16x32_bf16 v[176:179], a[120:123], v[128:131], v[176:179]\n"
"v_mul_f32_e32 v114, v114, v164\n"
"v_mul_f32_e32 v115, v115, v164\n"
"v_cvt_pk_bf16_f32 v164, v168, v169\n"
"v_mfma_f32_16x16x32_bf16 v[176:179], a[116:119], v[124:127], v[176:179]\n"
"s_add_u32 s95, s60, 0x440000\n"
"v_cvt_pk_bf16_f32 v165, v170, v171\n"
"v_cvt_pk_bf16_f32 v166, v112, v113\n"
"v_cvt_pk_bf16_f32 v167, v114, v115\n"
"v_mfma_f32_16x16x32_bf16 v[176:179], a[112:115], v[96:99], v[176:179]\n"
"buffer_load_dwordx4 v[112:115], v1, s[4:7], s98 offen offset:1024 sc1\n"
"v_mfma_f32_16x16x32_bf16 v[176:179], a[108:111], v[200:203], v[176:179]\n"
"buffer_load_dwordx4 a[248:251], v1, s[4:7], s97 offen offset:2048 sc1\n"
"v_mfma_f32_16x16x32_bf16 v[176:179], a[104:107], v[148:151], v[176:179]\n"
"ds_write_b128 v3, v[88:91] offset:8192\n"
"v_mfma_f32_16x16x32_bf16 v[176:179], a[100:103], v[140:143], v[176:179]\n"
"buffer_store_dwordx4 v[164:167], v0, s[4:7], s95 offen sc1\n"
"v_mfma_f32_16x16x32_bf16 v[176:179], a[96:99], v[104:107], v[176:179]\n"
";\n"
"v_mfma_f32_16x16x32_bf16 v[88:91], v[24:27], v[104:107], 0\n"
"buffer_load_dwordx4 v[164:167], v1, s[4:7], s98 offen offset:2048 sc1\n"
"v_mfma_f32_16x16x32_bf16 v[88:91], v[28:31], v[140:143], v[88:91]\n"
"v_mfma_f32_16x16x32_bf16 v[88:91], v[32:35], v[148:151], v[88:91]\n"
"v_mfma_f32_16x16x32_bf16 v[88:91], v[36:39], v[200:203], v[88:91]\n"
"v_mfma_f32_16x16x32_bf16 v[88:91], v[40:43], v[96:99], v[88:91]\n"
"s_waitcnt vmcnt(15)\n"
"v_add_f32_e32 v255, v255, v250\n"
"v_add_f32_e32 v253, v253, v254\n"
"v_mfma_f32_16x16x32_bf16 v[88:91], v[44:47], v[124:127], v[88:91]\n"
"buffer_load_dwordx4 v[168:171], v1, s[4:7], s98 offen offset:3072 sc1\n"
"v_mfma_f32_16x16x32_bf16 v[88:91], v[48:51], v[128:131], v[88:91]\n"
"ds_write_b128 v3, v[176:179] offset:9216\n"
"v_mfma_f32_16x16x32_bf16 v[88:91], v[52:55], v[180:183], v[88:91]\n"
"v_add_f32_e32 v252, v252, v249\n"
"v_add_f32_e32 v251, v251, v248\n"
"v_mfma_f32_16x16x32_bf16 v[176:179], v[84:87], v[180:183], 0\n"
"v_add_f32_e32 v255, v255, v252\n"
"v_add_f32_e32 v253, v253, v251\n"
"v_mfma_f32_16x16x32_bf16 v[176:179], v[80:83], v[128:131], v[176:179]\n"
"buffer_load_dwordx4 a[252:255], v1, s[4:7], s97 offen offset:3072 sc1\n"
"v_mfma_f32_16x16x32_bf16 v[176:179], v[76:79], v[124:127], v[176:179]\n"
"v_add_f32_e32 v255, v255, v253\n"
"v_mov_b32_e32 v253, v255\n"
"v_mfma_f32_16x16x32_bf16 v[176:179], v[72:75], v[96:99], v[176:179]\n"
"s_nop 0\n"
"v_permlane32_swap_b32_e32 v255, v253\n"
"v_add_f32_e32 v255, v255, v253\n"
"v_mfma_f32_16x16x32_bf16 v[176:179], v[68:71], v[200:203], v[176:179]\n"
"v_mul_f32_e32 v255, 0x3a800000, v255\n"
"v_mov_b32_e32 v253, v255\n"
"v_mfma_f32_16x16x32_bf16 v[176:179], v[64:67], v[148:151], v[176:179]\n"
"ds_write_b128 v3, v[88:91] offset:10240\n"
"v_mfma_f32_16x16x32_bf16 v[176:179], v[60:63], v[140:143], v[176:179]\n"
"v_permlane16_swap_b32_e32 v255, v253\n"
"v_cvt_pk_bf16_f32 v254, -v255, 0\n"
"v_mfma_f32_16x16x32_bf16 v[176:179], v[56:59], v[104:107], v[176:179]\n"
"v_fma_f32 v253, -v255, v255, v253\n"
"v_lshlrev_b32_e32 v254, 16, v254\n"
"; @B G1 3.2\n"
"v_mfma_f32_16x16x32_bf16 v[88:91], a[0:3], v[92:95], 0\n"
"s_add_u32 s98, s67, 0x10020\n"
"buffer_load_dwordx2 v[250:251], v12, s[4:7], s98 offen sc1\n"
"v_mfma_f32_16x16x32_bf16 v[88:91], a[4:7], v[108:111], v[88:91]\n"
"v_max_f32_e32 v253, 0, v253\n"
"v_cvt_pk_bf16_f32 v253, v253, 0\n"
"v_mfma_f32_16x16x32_bf16 v[88:91], a[8:11], v[136:139], v[88:91]\n"
"v_lshlrev_b32_e32 v253, 16, v253\n"
"v_add_f32_e32 v253, 0x3727c5ac, v253\n"
"v_mfma_f32_16x16x32_bf16 v[88:91], a[12:15], v[144:147], v[88:91]\n"
"s_waitcnt vmcnt(16)\n"
"v_cvt_pk_bf16_f32 v253, v253, 0\n"
"v_lshlrev_b32_e32 v253, 16, v253\n"
"v_mfma_f32_16x16x32_bf16 v[88:91], a[16:19], v[120:123], v[88:91]\n"
"v_sqrt_f32_e32 v253, v253\n"
"v_mfma_f32_16x16x32_bf16 v[88:91], a[20:23], v[192:195], v[88:91]\n"
"ds_write_b128 v3, v[176:179] offset:11264\n"
"v_mfma_f32_16x16x32_bf16 v[88:91], a[24:27], v[100:103], v[88:91]\n"
"v_cvt_pk_bf16_f32 v253, v253, 0\n"
"v_lshlrev_b32_e32 v253, 16, v253\n"
"v_mfma_f32_16x16x32_bf16 v[88:91], a[28:31], v[188:191], v[88:91]\n"
"v_rcp_f32_e32 v255, v253\n"
"v_mfma_f32_16x16x32_bf16 v[96:99], a[60:63], v[188:191], 0\n"
"v_mul_f32_e32 v253, 0x3e8293ee, v255\n"
"v_add_u32_e32 v252, 0x26c00, v0\n"
"v_mfma_f32_16x16x32_bf16 v[96:99], a[56:59], v[100:103], v[96:99]\n"
"ds_read_b128 v[104:107], v252\n"
"ds_read_b128 v[124:127], v252 offset:1024\n"
"v_mfma_f32_16x16x32_bf16 v[96:99], a[52:55], v[192:195], v[96:99]\n"
"s_waitcnt lgkmcnt(0)\n"
"s_barrier\n"
"v_mfma_f32_16x16x32_bf16 v[96:99], a[48:51], v[120:123], v[96:99]\n"
"s_add_u32 s97, s61, 0xc40000\n"
"ds_read_b128 v[128:131], v4\n"
"ds_read_b128 v[140:143], v9 offset:640\n"
"v_mfma_f32_16x16x32_bf16 v[96:99], a[44:47], v[144:147], v[96:99]\n"
"ds_read_b128 v[148:151], v5\n"
"v_add_u32_e32 v252, 0x26c00, v0\n"
"v_mfma_f32_16x16x32_bf16 v[96:99], a[40:43], v[136:139], v[96:99]\n"
"ds_write_b128 v3, v[88:91] offset:12288\n"
"v_mfma_f32_16x16x32_bf16 v[96:99], a[36:39], v[108:111], v[96:99]\n"
"buffer_store_dwordx4 v[172:175], v0, s[8:11], s97 offen sc1\n"
"v_mfma_f32_16x16x32_bf16 v[96:99], a[32:35], v[92:95], v[96:99]\n"
"ds_read_b128 v[88:91], v252\n"
"ds_read_b128 v[176:179], v252 offset:1024\n"
"v_mfma_f32_16x16x32_bf16 v[180:183], a[64:67], v[92:95], 0\n"
"v_mov_b32_e32 v252, 0xff800000\n"
"v_mfma_f32_16x16x32_bf16 v[180:183], a[68:71], v[108:111], v[180:183]\n"
"s_waitcnt lgkmcnt(3)\n"
"v_fmac_f32_e32 v128, v254, v140\n"
"v_fmac_f32_e32 v129, v254, v141\n"
"v_mfma_f32_16x16x32_bf16 v[180:183], a[72:75], v[136:139], v[180:183]\n"
"ds_read_b128 v[184:187], v4 offset:1024\n"
"ds_read_b128 v[196:199], v9 offset:704\n"
"v_mfma_f32_16x16x32_bf16 v[180:183], a[76:79], v[144:147], v[180:183]\n"
"ds_read_b128 v[200:203], v5 offset:1024\n"
"v_fmac_f32_e32 v130, v254, v142\n"
"v_mfma_f32_16x16x32_bf16 v[180:183], a[80:83], v[120:123], v[180:183]\n"
"v_fmac_f32_e32 v131, v254, v143\n"
"v_add_f32_e32 v128, v128, v148\n"
"v_mfma_f32_16x16x32_bf16 v[180:183], a[84:87], v[192:195], v[180:183]\n"
"ds_write_b128 v3, v[96:99] offset:13312\n"
"v_mfma_f32_16x16x32_bf16 v[180:183], a[88:91], v[100:103], v[180:183]\n"
"s_waitcnt lgkmcnt(4)\n"
"v_add_f32_e32 v129, v129, v149\n"
"v_add_f32_e32 v130, v130, v150\n"
"v_mfma_f32_16x16x32_bf16 v[180:183], a[92:95], v[188:191], v[180:183]\n"
"v_add_f32_e32 v131, v131, v151\n"
"v_cndmask_b32_e64 v88, v88, v252, s[82:83]\n"
"v_mfma_f32_16x16x32_bf16 v[96:99], a[124:127], v[188:191], 0\n"
"s_waitcnt vmcnt(12)\n"
"v_cmp_ne_u64_e32 vcc, v[246:247], v[18:19]\n"
"v_mfma_f32_16x16x32_bf16 v[96:99], a[120:123], v[100:103], v[96:99]\n"
"s_cbranch_vccnz .Lmk_f40s\n"
".Lmk_f40r:\n"
"s_mov_b32 m0, 0x7c00\n"
"v_mfma_f32_16x16x32_bf16 v[96:99], a[116:119], v[192:195], v[96:99]\n"
"s_add_u32 s97, s66, 0x3c00\n"
"buffer_load_dwordx4 v0, s[4:7], s97 offen sc1 lds\n"
"v_mfma_f32_16x16x32_bf16 v[96:99], a[112:115], v[120:123], v[96:99]\n"
"s_mov_b32 m0, 0xfc00\n"
"s_add_u32 s97, s66, 0x7c00\n"
"ds_read_b128 v[140:143], v4 offset:2048\n"
"v_mfma_f32_16x16x32_bf16 v[96:99], a[108:111], v[144:147], v[96:99]\n"
"buffer_load_dwordx4 v0, s[4:7], s97 offen sc1 lds\n"
"v_mfma_f32_16x16x32_bf16 v[96:99], a[104:107], v[136:139], v[96:99]\n"
"ds_write_b128 v3, v[180:183] offset:14336\n"
"v_mfma_f32_16x16x32_bf16 v[96:99], a[100:103], v[108:111], v[96:99]\n"
"ds_read_b128 v[148:151], v9 offset:768\n"
"ds_read_b128 v[180:183], v5 offset:2048\n"
"v_mfma_f32_16x16x32_bf16 v[96:99], a[96:99], v[92:95], v[96:99]\n"
"s_waitcnt lgkmcnt(5)\n"
"v_fmac_f32_e32 v184, v254, v196\n"
"v_fmac_f32_e32 v185, v254, v197\n"
"v_mfma_f32_16x16x32_bf16 v[204:207], v[24:27], v[92:95], 0\n"
"ds_read_b128 v[208:211], v4 offset:3072\n"
"ds_read_b128 v[212:215], v9 offset:832\n"
"v_mfma_f32_16x16x32_bf16 v[204:207], v[28:31], v[108:111], v[204:207]\n"
"ds_read_b128 v[216:219], v5 offset:3072\n"
"v_fmac_f32_e32 v186, v254, v198\n"
"v_mfma_f32_16x16x32_bf16 v[204:207], v[32:35], v[136:139], v[204:207]\n"
"v_fmac_f32_e32 v187, v254, v199\n"
"v_add_f32_e32 v184, v184, v200\n"
"v_mfma_f32_16x16x32_bf16 v[204:207], v[36:39], v[144:147], v[204:207]\n"
"v_add_f32_e32 v185, v185, v201\n"
"v_add_f32_e32 v186, v186, v202\n"
"v_mfma_f32_16x16x32_bf16 v[204:207], v[40:43], v[120:123], v[204:207]\n"
"s_waitcnt lgkmcnt(3)\n"
"v_add_f32_e32 v187, v187, v203\n"
"v_fmac_f32_e32 v140, v254, v148\n"
"v_mfma_f32_16x16x32_bf16 v[204:207], v[44:47], v[192:195], v[204:207]\n"
"ds_write_b128 v3, v[96:99] offset:15360\n"
"v_mfma_f32_16x16x32_bf16 v[204:207], v[48:51], v[100:103], v[204:207]\n"
"v_fmac_f32_e32 v141, v254, v149\n"
"v_fmac_f32_e32 v142, v254, v150\n"
"v_mfma_f32_16x16x32_bf16 v[204:207], v[52:55], v[188:191], v[204:207]\n"
"ds_read_b128 v[96:99], v4 offset:4096\n"
"ds_read_b128 v[196:199], v9 offset:512\n"
"v_mfma_f32_16x16x32_bf16 v[200:203], v[84:87], v[188:191], 0\n"
"ds_read_b128 v[188:191], v5 offset:4096\n"
"v_fmac_f32_e32 v143, v254, v151\n"
"v_mfma_f32_16x16x32_bf16 v[200:203], v[80:83], v[100:103], v[200:203]\n"
"v_add_f32_e32 v140, v140, v180\n"
"v_add_f32_e32 v141, v141, v181\n"
"v_mfma_f32_16x16x32_bf16 v[200:203], v[76:79], v[192:195], v[200:203]\n"
"s_waitcnt lgkmcnt(4)\n"
"v_add_f32_e32 v142, v142, v182\n"
"v_add_f32_e32 v143, v143, v183\n"
"v_mfma_f32_16x16x32_bf16 v[200:203], v[72:75], v[120:123], v[200:203]\n"
"v_fmac_f32_e32 v208, v254, v212\n"
"v_fmac_f32_e32 v209, v254, v213\n"
"v_mfma_f32_16x16x32_bf16 v[200:203], v[68:71], v[144:147], v[200:203]\n"
"v_fmac_f32_e32 v210, v254, v214\n"
"v_fmac_f32_e32 v211, v254, v215\n"
"v_mfma_f32_16x16x32_bf16 v[200:203], v[64:67], v[136:139], v[200:203]\n"
"ds_write_b128 v3, v[204:207] offset:16384\n"
"v_mfma_f32_16x16x32_bf16 v[200:203], v[60:63], v[108:111], v[200:203]\n"
"ds_read_b128 v[100:103], v4 offset:5120\n"
"ds_read_b128 v[108:111], v9 offset:576\n"
"v_mfma_f32_16x16x32_bf16 v[200:203], v[56:59], v[92:95], v[200:203]\n"
"s_waitcnt vmcnt(12)\n"
"s_waitcnt vmcnt(3)\n"
"v_cmp_ne_u64_e32 vcc, v[250:251], v[18:19]\n"
"; @B G1 3.3\n"
"v_mfma_f32_16x16x32_bf16 v[128:131], a[0:3], v[116:119], v[128:131]\n"
"s_cbranch_vccnz .Lmk_f41s\n"
".Lmk_f41r:\n"
"ds_read_b128 v[92:95], v5 offset:5120\n"
"v_mfma_f32_16x16x32_bf16 v[128:131], a[4:7], v[132:135], v[128:131]\n"
"s_add_u32 s98, s61, 0xc40000\n"
"s_add_u32 s97, s56, 0x440000\n"
"v_add_f32_e32 v208, v208, v216\n"
"v_mfma_f32_16x16x32_bf16 v[128:131], a[8:11], v[160:163], v[128:131]\n"
"buffer_load_dwordx4 v[120:123], v0, s[4:7], s97 offen sc1\n"
"v_mfma_f32_16x16x32_bf16 v[128:131], a[12:15], v[156:159], v[128:131]\n"
"buffer_store_dwordx4 v[172:175], v0, s[8:11], s98 offen offset:1024 sc1\n"
"v_mfma_f32_16x16x32_bf16 v[128:131], a[16:19], v[152:155], v[128:131]\n"
"s_add_u32 s98, s69, 0\n"
"v_add_f32_e32 v209, v209, v217\n"
"v_add_f32_e32 v210, v210, v218\n"
"v_mfma_f32_16x16x32_bf16 v[128:131], a[20:23], v[112:115], v[128:131]\n"
"ds_write_b128 v3, v[200:203] offset:17408\n"
"v_mfma_f32_16x16x32_bf16 v[128:131], a[24:27], v[164:167], v[128:131]\n"
"buffer_load_dwordx4 v[136:139], v0, s[4:7], s97 offen offset:1024 sc1\n"
"v_mfma_f32_16x16x32_bf16 v[128:131], a[28:31], v[168:171], v[128:131]\n"
"s_waitcnt lgkmcnt(5)\n"
"v_add_f32_e32 v211, v211, v219\n"
"v_fmac_f32_e32 v96, v254, v196\n"
"v_mfma_f32_16x16x32_bf16 v[184:187], a[60:63], v[168:171], v[184:187]\n"
"v_fmac_f32_e32 v97, v254, v197\n"
"v_fmac_f32_e32 v98, v254, v198\n"
"v_mfma_f32_16x16x32_bf16 v[184:187], a[56:59], v[164:167], v[184:187]\n"
"buffer_load_dwordx4 v[144:147], v0, s[4:7], s97 offen offset:2048 sc1\n"
"v_mfma_f32_16x16x32_bf16 v[184:187], a[52:55], v[112:115], v[184:187]\n"
"s_waitcnt lgkmcnt(0)\n"
"s_barrier\n"
"v_mfma_f32_16x16x32_bf16 v[184:187], a[48:51], v[152:155], v[184:187]\n"
"ds_read_b128 v[148:151], v6\n"
"ds_read_b128 v[180:183], v6 offset:1024\n"
"v_mfma_f32_16x16x32_bf16 v[184:187], a[44:47], v[156:159], v[184:187]\n"
"buffer_load_dwordx4 v[192:195], v0, s[4:7], s97 offen offset:3072 sc1\n"
"v_mfma_f32_16x16x32_bf16 v[184:187], a[40:43], v[160:163], v[184:187]\n"
"v_fmac_f32_e32 v99, v254, v199\n"
"v_add_f32_e32 v96, v96, v188\n"
"v_mfma_f32_16x16x32_bf16 v[184:187], a[36:39], v[132:135], v[184:187]\n"
"v_add_f32_e32 v97, v97, v189\n"
"v_add_f32_e32 v98, v98, v190\n"
"v_mfma_f32_16x16x32_bf16 v[184:187], a[32:35], v[116:119], v[184:187]\n"
"buffer_load_dwordx4 v[196:199], v1, s[4:7], s97 offen sc1\n"
"v_mfma_f32_16x16x32_bf16 v[140:143], a[64:67], v[116:119], v[140:143]\n"
"v_add_f32_e32 v99, v99, v191\n"
"v_fmac_f32_e32 v100, v254, v108\n"
"v_mfma_f32_16x16x32_bf16 v[140:143], a[68:71], v[132:135], v[140:143]\n"
"v_fmac_f32_e32 v101, v254, v109\n"
"v_fmac_f32_e32 v102, v254, v110\n"
"v_mfma_f32_16x16x32_bf16 v[140:143], a[72:75], v[160:163], v[140:143]\n"
"buffer_load_dwordx4 v[188:191], v1, s[4:7], s97 offen offset:1024 sc1\n"
"v_mfma_f32_16x16x32_bf16 v[140:143], a[76:79], v[156:159], v[140:143]\n"
"v_fmac_f32_e32 v103, v254, v111\n"
"v_add_f32_e32 v100, v100, v92\n"
"v_mfma_f32_16x16x32_bf16 v[140:143], a[80:83], v[152:155], v[140:143]\n"
"s_waitcnt lgkmcnt(0)\n"
"v_add_f32_e32 v101, v101, v93\n"
"v_add_f32_e32 v102, v102, v94\n"
"v_mfma_f32_16x16x32_bf16 v[140:143], a[84:87], v[112:115], v[140:143]\n"
"buffer_load_dwordx4 v[108:111], v1, s[4:7], s97 offen offset:2048 sc1\n"
"v_mfma_f32_16x16x32_bf16 v[140:143], a[88:91], v[164:167], v[140:143]\n"
"ds_read_b128 v[200:203], v6 offset:2048\n"
"ds_read_b128 v[204:207], v6 offset:3072\n"
"v_mfma_f32_16x16x32_bf16 v[140:143], a[92:95], v[168:171], v[140:143]\n"
"v_add_f32_e32 v103, v103, v95\n"
"v_add_f32_e32 v128, v128, v148\n"
"v_mfma_f32_16x16x32_bf16 v[208:211], a[124:127], v[168:171], v[208:211]\n"
"buffer_load_dwordx4 v[92:95], v1, s[4:7], s97 offen offset:3072 sc1\n"
"v_mfma_f32_16x16x32_bf16 v[208:211], a[120:123], v[164:167], v[208:211]\n"
"s_add_u32 s97, s57, 0x440000\n"
"v_add_f32_e32 v129, v129, v149\n"
"v_add_f32_e32 v130, v130, v150\n"
"v_mfma_f32_16x16x32_bf16 v[208:211], a[116:119], v[112:115], v[208:211]\n"
"v_add_f32_e32 v131, v131, v151\n"
"v_add_f32_e32 v184, v184, v180\n"
"v_mfma_f32_16x16x32_bf16 v[208:211], a[112:115], v[152:155], v[208:211]\n"
"buffer_load_dwordx4 v[148:151], v0, s[4:7], s97 offen sc1\n"
"v_mfma_f32_16x16x32_bf16 v[208:211], a[108:111], v[156:159], v[208:211]\n"
"v_add_f32_e32 v185, v185, v181\n"
"v_add_f32_e32 v186, v186, v182\n"
"v_mfma_f32_16x16x32_bf16 v[208:211], a[104:107], v[160:163], v[208:211]\n"
"v_add_f32_e32 v187, v187, v183\n"
"v_mul_f32_e32 v128, v128, v255\n"
"v_mfma_f32_16x16x32_bf16 v[208:211], a[100:103], v[132:135], v[208:211]\n"
"buffer_load_dwordx4 v[180:183], v0, s[4:7], s97 offen offset:1024 sc1\n"
"v_mfma_f32_16x16x32_bf16 v[208:211], a[96:99], v[116:119], v[208:211]\n"
"v_mul_f32_e32 v129, v129, v255\n"
"v_mul_f32_e32 v130, v130, v255\n"
"v_mfma_f32_16x16x32_bf16 v[96:99], v[24:27], v[116:119], v[96:99]\n"
"v_mul_f32_e32 v131, v131, v255\n"
"v_mul_f32_e32 v184, v184, v255\n"
"v_mfma_f32_16x16x32_bf16 v[96:99], v[28:31], v[132:135], v[96:99]\n"
"buffer_load_dwordx4 v[24:27], v0, s[4:7], s97 offen offset:2048 sc1\n"
"v_mfma_f32_16x16x32_bf16 v[96:99], v[32:35], v[160:163], v[96:99]\n"
"v_mul_f32_e32 v185, v185, v255\n"
"v_mul_f32_e32 v186, v186, v255\n"
"v_mfma_f32_16x16x32_bf16 v[96:99], v[36:39], v[156:159], v[96:99]\n"
"v_mul_f32_e32 v187, v187, v255\n"
"v_cvt_pk_bf16_f32 v28, v128, v129\n"
"v_mfma_f32_16x16x32_bf16 v[96:99], v[40:43], v[152:155], v[96:99]\n"
"buffer_load_dwordx4 v[32:35], v0, s[4:7], s97 offen offset:3072 sc1\n"
"v_mfma_f32_16x16x32_bf16 v[96:99], v[44:47], v[112:115], v[96:99]\n"
"s_waitcnt lgkmcnt(0)\n"
"v_cvt_pk_bf16_f32 v29, v130, v131\n"
"v_cvt_pk_bf16_f32 v30, v184, v185\n"
"v_mfma_f32_16x16x32_bf16 v[96:99], v[48:51], v[164:167], v[96:99]\n"
"s_add_u32 s96, s63, 0xc4000\n"
"ds_read_b128 v[36:39], v6 offset:4096\n"
"ds_read_b128 v[40:43], v6 offset:5120\n"
"v_mfma_f32_16x16x32_bf16 v[96:99], v[52:55], v[168:171], v[96:99]\n"
"buffer_load_dwordx4 v[44:47], v1, s[4:7], s97 offen sc1\n"
"v_mfma_f32_16x16x32_bf16 v[100:103], v[84:87], v[168:171], v[100:103]\n"
"buffer_store_dword v172, v14, s[8:11], s96 offen sc1\n"
"v_mfma_f32_16x16x32_bf16 v[100:103], v[80:83], v[164:167], v[100:103]\n"
"v_cvt_pk_bf16_f32 v31, v186, v187\n"
"v_add_f32_e32 v140, v140, v200\n"
"v_mfma_f32_16x16x32_bf16 v[100:103], v[76:79], v[112:115], v[100:103]\n"
"buffer_load_dwordx4 v[48:51], v1, s[4:7], s97 offen offset:1024 sc1\n"
"ds_write_b128 v7, v[28:31] offset:16384\n"
"v_mfma_f32_16x16x32_bf16 v[100:103], v[72:75], v[152:155], v[100:103]\n"
"v_add_f32_e32 v141, v141, v201\n"
"v_add_f32_e32 v142, v142, v202\n"
"v_mfma_f32_16x16x32_bf16 v[100:103], v[68:71], v[156:159], v[100:103]\n"
"v_add_f32_e32 v143, v143, v203\n"
"v_add_f32_e32 v208, v208, v204\n"
"v_mfma_f32_16x16x32_bf16 v[100:103], v[64:67], v[160:163], v[100:103]\n"
"buffer_load_dwordx4 v[28:31], v1, s[4:7], s97 offen offset:2048 sc1\n"
"v_mfma_f32_16x16x32_bf16 v[100:103], v[60:63], v[132:135], v[100:103]\n"
"v_add_f32_e32 v209, v209, v205\n"
"v_add_f32_e32 v210, v210, v206\n"
"v_mfma_f32_16x16x32_bf16 v[100:103], v[56:59], v[116:119], v[100:103]\n"
"buffer_load_dwordx4 a[0:3], v0, s[8:11], s98 offen sc1\n"
"buffer_load_dwordx4 v[56:59], v1, s[4:7], s97 offen offset:3072 sc1\n"
"v_add_f32_e32 v211, v211, v207\n"
"v_mul_f32_e32 v140, v140, v255\n"
"v_mul_f32_e32 v141, v141, v255\n"
"v_mul_f32_e32 v142, v142, v255\n"
"v_mul_f32_e32 v143, v143, v255\n"
"v_mul_f32_e32 v208, v208, v255\n"
"v_mul_f32_e32 v209, v209, v255\n"
"v_mul_f32_e32 v210, v210, v255\n"
"v_mul_f32_e32 v211, v211, v255\n"
"v_cvt_pk_bf16_f32 v52, v140, v141\n"
"v_cvt_pk_bf16_f32 v53, v142, v143\n"
"v_cvt_pk_bf16_f32 v54, v208, v209\n"
"v_cvt_pk_bf16_f32 v55, v210, v211\n"
"v_cndmask_b32_e64 v89, v89, v252, s[82:83]\n"
"v_cndmask_b32_e64 v90, v90, v252, s[82:83]\n"
"v_cndmask_b32_e64 v91, v91, v252, s[82:83]\n"
"v_cndmask_b32_e64 v176, v176, v252, s[82:83]\n"
"v_cndmask_b32_e64 v177, v177, v252, s[82:83]\n"
"v_cndmask_b32_e64 v178, v178, v252, s[82:83]\n"
"v_cndmask_b32_e64 v179, v179, v252, s[82:83]\n"
"ds_write_b128 v7, v[52:55] offset:49152\n"
"buffer_load_dwordx4 a[4:7], v0, s[8:11], s98 offen offset:1024 sc1\n"
"buffer_load_dwordx4 a[8:11], v0, s[8:11], s98 offen offset:2048 sc1\n"
"buffer_load_dwordx4 a[12:15], v0, s[8:11], s98 offen offset:3072 sc1\n"
"buffer_load_dwordx4 a[16:19], v1, s[8:11], s98 offen sc1\n"
"buffer_load_dwordx4 a[20:23], v1, s[8:11], s98 offen offset:1024 sc1\n"
".Lmk_b4:\n"
"; @B tail\n"
";\n"
"s_waitcnt lgkmcnt(2)\n"
"v_add_f32_e32 v96, v96, v36\n"
"v_add_f32_e32 v97, v97, v37\n"
"v_add_f32_e32 v98, v98, v38\n"
"v_add_f32_e32 v99, v99, v39\n"
"v_add_f32_e32 v100, v100, v40\n"
"v_add_f32_e32 v101, v101, v41\n"
"v_add_f32_e32 v102, v102, v42\n"
"v_add_f32_e32 v103, v103, v43\n"
"v_mul_f32_e32 v96, v96, v253\n"
"v_mul_f32_e32 v97, v97, v253\n"
"v_mul_f32_e32 v98, v98, v253\n"
"v_mul_f32_e32 v99, v99, v253\n"
"v_mul_f32_e32 v100, v100, v253\n"
"v_mul_f32_e32 v101, v101, v253\n"
"v_mul_f32_e32 v102, v102, v253\n"
"v_mul_f32_e32 v103, v103, v253\n"
"v_cvt_pk_bf16_f32 v36, v96, v97\n"
"v_cvt_pk_bf16_f32 v37, v98, v99\n"
"v_cvt_pk_bf16_f32 v38, v100, v101\n"
"v_cvt_pk_bf16_f32 v39, v102, v103\n"
"ds_write_b128 v2, v[36:39] offset:16384\n"
"s_waitcnt vmcnt(24)\n"
";\n"
"; @B AT a\n"
";\n"
"s_cmp_lg_u32 s0, 0\n"
"s_cbranch_scc1 .Lmk_exit\n"
"ds_read_b128 v[36:39], v2 offset:17408\n"
"ds_read_b128 v[40:43], v15 offset:16384\n"
"v_xor_b32_e32 v255, 32, v15\n"
"ds_read_b128 v[52:55], v255 offset:16640\n"
"ds_read_b128 v[60:63], v2 offset:16384\n"
"ds_read_b128 v[64:67], v17 offset:15360\n"
"v_xor_b32_e32 v255, 32, v17\n"
"ds_read_b128 v[68:71], v255 offset:15616\n"
"ds_read_b64_tr_b16 v[72:73], v16 offset:16384\n"
"v_xor_b32_e32 v255, 32, v16\n"
"ds_read_b64_tr_b16 v[74:75], v255 offset:16640\n"
"ds_read_b64_tr_b16 v[76:77], v255 offset:16384\n"
"ds_read_b64_tr_b16 v[78:79], v16 offset:16640\n"
"ds_read_b64_tr_b16 v[84:85], v16 offset:15360\n"
"v_mov_b32_e32 v80, 0x3f803f80\n"
"v_mov_b32_e32 v81, 0x3f803f80\n"
"v_mov_b32_e32 v82, 0x3f803f80\n"
"v_mov_b32_e32 v83, 0x3f803f80\n"
"v_xor_b32_e32 v255, 32, v16\n"
"ds_read_b64_tr_b16 v[86:87], v255 offset:15616\n"
"s_waitcnt lgkmcnt(13)\n"
"ds_read_b64_tr_b16 v[96:97], v255 offset:15360\n"
"ds_read_b64_tr_b16 v[98:99], v16 offset:15616\n"
"v_mov_b32_e32 v100, 0x3f803f80\n"
"v_mov_b32_e32 v101, 0x3f803f80\n"
"v_mov_b32_e32 v102, 0x3f803f80\n"
"v_mov_b32_e32 v103, 0x3f803f80\n"
"s_waitcnt lgkmcnt(11)\n"
"v_mfma_f32_16x16x32_bf16 v[104:107], v[40:43], v[36:39], v[104:107]\n"
"v_mfma_f32_16x16x32_bf16 v[124:127], v[52:55], v[36:39], v[124:127]\n"
"s_waitcnt lgkmcnt(8)\n"
"v_mfma_f32_16x16x32_bf16 v[88:91], v[64:67], v[60:63], v[88:91]\n"
"v_mfma_f32_16x16x32_bf16 v[176:179], v[68:71], v[60:63], v[176:179]\n"
"s_waitcnt lgkmcnt(4)\n"
"s_nop 2\n"
"v_max_f32_e32 v255, v104, v105\n"
"s_nop 1\n"
"v_max_f32_e32 v254, v88, v89\n"
"v_max3_f32 v255, v255, v106, v107\n"
"v_max3_f32 v254, v254, v90, v91\n"
"v_max3_f32 v255, v255, v124, v125\n"
"v_max3_f32 v254, v254, v176, v177\n"
"v_max3_f32 v255, v255, v126, v127\n"
"v_max3_f32 v254, v254, v178, v179\n"
"v_mov_b32_e32 v253, v255\n"
"v_mov_b32_e32 v252, v254\n"
"s_nop 0\n"
"v_permlane16_swap_b32_e32 v255, v253\n"
"v_permlane16_swap_b32_e32 v254, v252\n"
"v_max_f32_e32 v255, v255, v253\n"
"v_max_f32_e32 v254, v254, v252\n"
"v_mov_b32_e32 v253, v255\n"
"v_mov_b32_e32 v252, v254\n"
"s_nop 0\n"
"v_permlane32_swap_b32_e32 v255, v253\n"
"v_permlane32_swap_b32_e32 v254, v252\n"
"v_max_f32_e32 v255, v255, v253\n"
"v_max_f32_e32 v254, v254, v252\n"
"v_sub_f32_e32 v104, v104, v255\n"
"v_sub_f32_e32 v105, v105, v255\n"
"v_sub_f32_e32 v106, v106, v255\n"
"v_sub_f32_e32 v107, v107, v255\n"
"v_sub_f32_e32 v124, v124, v255\n"
"v_sub_f32_e32 v125, v125, v255\n"
"v_sub_f32_e32 v126, v126, v255\n"
"v_sub_f32_e32 v127, v127, v255\n"
"v_exp_f32_e32 v104, v104\n"
"v_exp_f32_e32 v105, v105\n"
"v_exp_f32_e32 v106, v106\n"
"v_exp_f32_e32 v107, v107\n"
"v_exp_f32_e32 v124, v124\n"
"v_exp_f32_e32 v125, v125\n"
"v_exp_f32_e32 v126, v126\n"
"v_exp_f32_e32 v127, v127\n"
"v_cvt_pk_bf16_f32 v36, v104, v105\n"
"v_cvt_pk_bf16_f32 v37, v106, v107\n"
"v_cvt_pk_bf16_f32 v38, v124, v125\n"
"v_cvt_pk_bf16_f32 v39, v126, v127\n"
"v_sub_f32_e32 v88, v88, v254\n"
"v_sub_f32_e32 v89, v89, v254\n"
"v_sub_f32_e32 v90, v90, v254\n"
"v_sub_f32_e32 v91, v91, v254\n"
"v_sub_f32_e32 v176, v176, v254\n"
"v_sub_f32_e32 v177, v177, v254\n"
"v_sub_f32_e32 v178, v178, v254\n"
"v_mfma_f32_16x16x32_bf16 v[40:43], v[80:83], v[36:39], 0\n"
"v_sub_f32_e32 v179, v179, v254\n"
"v_exp_f32_e32 v88, v88\n"
"v_exp_f32_e32 v89, v89\n"
"v_exp_f32_e32 v90, v90\n"
"v_mfma_f32_16x16x32_bf16 v[52:55], v[72:75], v[36:39], 0\n"
"v_exp_f32_e32 v91, v91\n"
"v_exp_f32_e32 v176, v176\n"
"v_exp_f32_e32 v177, v177\n"
"v_exp_f32_e32 v178, v178\n"
"v_mfma_f32_16x16x32_bf16 v[60:63], v[76:79], v[36:39], 0\n"
"s_waitcnt lgkmcnt(0)\n"
"v_exp_f32_e32 v179, v179\n"
"v_cvt_pk_bf16_f32 v36, v88, v89\n"
"v_cvt_pk_bf16_f32 v37, v90, v91\n"
"v_cvt_pk_bf16_f32 v38, v176, v177\n"
"v_cvt_pk_bf16_f32 v39, v178, v179\n"
"v_rcp_f32_e32 v40, v40\n"
"s_nop 0\n"
"v_mul_f32_e32 v52, v52, v40\n"
"v_mul_f32_e32 v53, v53, v40\n"
"v_mul_f32_e32 v54, v54, v40\n"
"v_mfma_f32_16x16x32_bf16 v[64:67], v[100:103], v[36:39], 0\n"
"v_mul_f32_e32 v55, v55, v40\n"
"v_mul_f32_e32 v60, v60, v40\n"
"v_mul_f32_e32 v61, v61, v40\n"
"v_mul_f32_e32 v62, v62, v40\n"
"v_mul_f32_e32 v63, v63, v40\n"
"v_mfma_f32_16x16x32_bf16 v[40:43], v[84:87], v[36:39], 0\n"
"s_add_u32 s97, s60, 0x408000\n"
"v_cvt_pk_bf16_f32 v68, v52, v53\n"
"v_cvt_pk_bf16_f32 v69, v54, v55\n"
"v_cvt_pk_bf16_f32 v70, v60, v61\n"
"v_cvt_pk_bf16_f32 v71, v62, v63\n"
"v_mfma_f32_16x16x32_bf16 v[52:55], v[96:99], v[36:39], 0\n"
"buffer_store_dwordx4 v[68:71], v0, s[4:7], s97 offen sc1\n"
"s_add_u32 s97, s60, 0x400000\n"
"v_rcp_f32_e32 v64, v64\n"
"s_nop 0\n"
"v_mul_f32_e32 v40, v40, v64\n"
"v_mul_f32_e32 v41, v41, v64\n"
"v_mul_f32_e32 v42, v42, v64\n"
"v_mul_f32_e32 v43, v43, v64\n"
"v_mul_f32_e32 v52, v52, v64\n"
"v_mul_f32_e32 v53, v53, v64\n"
"v_mul_f32_e32 v54, v54, v64\n"
"v_mul_f32_e32 v55, v55, v64\n"
"v_cvt_pk_bf16_f32 v36, v40, v41\n"
"v_cvt_pk_bf16_f32 v37, v42, v43\n"
"v_cvt_pk_bf16_f32 v38, v52, v53\n"
"v_cvt_pk_bf16_f32 v39, v54, v55\n"
"buffer_store_dwordx4 v[36:39], v0, s[4:7], s97 offen sc1\n"
";\n"
"s_add_u32 s97, s58, 0x440000\n"
"buffer_load_dwordx4 v[36:39], v0, s[4:7], s97 offen sc1\n"
"buffer_load_dwordx4 v[40:43], v0, s[4:7], s97 offen offset:1024 sc1\n"
"buffer_load_dwordx4 v[52:55], v0, s[4:7], s97 offen offset:2048 sc1\n"
"buffer_load_dwordx4 v[60:63], v0, s[4:7], s97 offen offset:3072 sc1\n"
"buffer_load_dwordx4 v[64:67], v1, s[4:7], s97 offen sc1\n"
"buffer_load_dwordx4 v[68:71], v1, s[4:7], s97 offen offset:1024 sc1\n"
"buffer_load_dwordx4 v[72:75], v1, s[4:7], s97 offen offset:2048 sc1\n"
"buffer_load_dwordx4 v[76:79], v1, s[4:7], s97 offen offset:3072 sc1\n"
"buffer_load_dwordx4 a[24:27], v1, s[8:11], s98 offen offset:2048 sc1\n"
"buffer_load_dwordx4 a[28:31], v1, s[8:11], s98 offen offset:3072 sc1\n"
"s_add_u32 s98, s69, 0x2000\n"
"buffer_load_dwordx4 a[32:35], v0, s[8:11], s98 offen sc1\n"
"buffer_load_dwordx4 a[36:39], v0, s[8:11], s98 offen offset:1024 sc1\n"
"buffer_load_dwordx4 a[40:43], v0, s[8:11], s98 offen offset:2048 sc1\n"
"buffer_load_dwordx4 a[44:47], v0, s[8:11], s98 offen offset:3072 sc1\n"
"buffer_load_dwordx4 a[48:51], v1, s[8:11], s98 offen sc1\n"
"buffer_load_dwordx4 a[52:55], v1, s[8:11], s98 offen offset:1024 sc1\n"
"s_waitcnt vmcnt(39)\n"
"; @B G2 b.0\n"
"v_mfma_f32_16x16x32_bf16 v[80:83], a[128:131], v[120:123], 0\n"
"v_mfma_f32_16x16x32_bf16 v[80:83], a[132:135], v[136:139], v[80:83]\n"
"s_waitcnt vmcnt(37)\n"
"v_mfma_f32_16x16x32_bf16 v[80:83], a[136:139], v[144:147], v[80:83]\n"
"s_add_u32 s97, s59, 0x440000\n"
"buffer_load_dwordx4 v[84:87], v0, s[4:7], s97 offen sc1\n"
"v_mfma_f32_16x16x32_bf16 v[80:83], a[140:143], v[192:195], v[80:83]\n"
"s_waitcnt vmcnt(36)\n"
"v_mfma_f32_16x16x32_bf16 v[80:83], a[144:147], v[196:199], v[80:83]\n"
"v_mfma_f32_16x16x32_bf16 v[80:83], a[148:151], v[188:191], v[80:83]\n"
"s_waitcnt vmcnt(34)\n"
"buffer_load_dwordx4 v[88:91], v0, s[4:7], s97 offen offset:1024 sc1\n"
"v_mfma_f32_16x16x32_bf16 v[80:83], a[152:155], v[108:111], v[80:83]\n"
"v_mfma_f32_16x16x32_bf16 v[80:83], a[156:159], v[92:95], v[80:83]\n"
"v_mfma_f32_16x16x32_bf16 v[96:99], a[188:191], v[92:95], 0\n"
"buffer_load_dwordx4 v[100:103], v0, s[4:7], s97 offen offset:2048 sc1\n"
"v_mfma_f32_16x16x32_bf16 v[96:99], a[184:187], v[108:111], v[96:99]\n"
"v_mfma_f32_16x16x32_bf16 v[96:99], a[180:183], v[188:191], v[96:99]\n"
"v_mfma_f32_16x16x32_bf16 v[96:99], a[176:179], v[196:199], v[96:99]\n"
"buffer_load_dwordx4 v[104:107], v0, s[4:7], s97 offen offset:3072 sc1\n"
"v_mfma_f32_16x16x32_bf16 v[96:99], a[172:175], v[192:195], v[96:99]\n"
"s_nop 0\n"
"v_cmp_u_f32_e32 vcc, v80, v80\n"
"v_mfma_f32_16x16x32_bf16 v[96:99], a[168:171], v[144:147], v[96:99]\n"
"s_cbranch_vccnz .Lmk_p63s\n"
".Lmk_p63r:\n"
"v_mfma_f32_16x16x32_bf16 v[96:99], a[164:167], v[136:139], v[96:99]\n"
"ds_write_b128 v3, v[80:83]\n"
"v_mfma_f32_16x16x32_bf16 v[96:99], a[160:163], v[120:123], v[96:99]\n"
"buffer_load_dwordx4 v[80:83], v1, s[4:7], s97 offen sc1\n"
"v_mfma_f32_16x16x32_bf16 v[112:115], a[192:195], v[120:123], 0\n"
"v_mfma_f32_16x16x32_bf16 v[112:115], a[196:199], v[136:139], v[112:115]\n"
"v_mfma_f32_16x16x32_bf16 v[112:115], a[200:203], v[144:147], v[112:115]\n"
"buffer_load_dwordx4 v[116:119], v1, s[4:7], s97 offen offset:1024 sc1\n"
"v_mfma_f32_16x16x32_bf16 v[112:115], a[204:207], v[192:195], v[112:115]\n"
"v_mfma_f32_16x16x32_bf16 v[112:115], a[208:211], v[196:199], v[112:115]\n"
"v_mfma_f32_16x16x32_bf16 v[112:115], a[212:215], v[188:191], v[112:115]\n"
"ds_write_b128 v3, v[96:99] offset:1024\n"
"v_mfma_f32_16x16x32_bf16 v[112:115], a[216:219], v[108:111], v[112:115]\n"
"buffer_load_dwordx4 v[96:99], v1, s[4:7], s97 offen offset:2048 sc1\n"
"v_mfma_f32_16x16x32_bf16 v[112:115], a[220:223], v[92:95], v[112:115]\n"
"v_mfma_f32_16x16x32_bf16 v[124:127], a[252:255], v[92:95], 0\n"
"v_mfma_f32_16x16x32_bf16 v[124:127], a[248:251], v[108:111], v[124:127]\n"
"buffer_load_dwordx4 v[92:95], v1, s[4:7], s97 offen offset:3072 sc1\n"
"v_mfma_f32_16x16x32_bf16 v[124:127], a[244:247], v[188:191], v[124:127]\n"
"v_mfma_f32_16x16x32_bf16 v[124:127], a[240:243], v[196:199], v[124:127]\n"
"v_mfma_f32_16x16x32_bf16 v[124:127], a[236:239], v[192:195], v[124:127]\n"
"s_mov_b32 s97, .Lmk_a0-.Lmk_g67\n"
"s_getpc_b64 s[84:85]\n"
".Lmk_g67:\n"
"s_add_u32 s84, s84, s97\n"
"s_addc_u32 s85, s85, 0\n"
"s_and_b32 s84, s84, -16\n"
"v_mfma_f32_16x16x32_bf16 v[124:127], a[232:235], v[144:147], v[124:127]\n"
"ds_write_b128 v3, v[112:115] offset:2048\n"
"v_mfma_f32_16x16x32_bf16 v[124:127], a[228:231], v[136:139], v[124:127]\n"
"s_and_b32 s85, s85, 0xffff\n"
"s_sub_u32 s97, .Lmk_end-.Lmk_g67, s97\n"
"s_min_u32 s86, s97, 0x3000\n"
"s_mov_b32 s87, 0x20000\n"
"v_mfma_f32_16x16x32_bf16 v[124:127], a[224:227], v[120:123], v[124:127]\n"
"s_waitcnt vmcnt(37)\n"
"s_mov_b32 m0, 0x27800\n"
"s_nop 0\n"
"buffer_load_dwordx4 v0, s[84:87], s99 offen lds\n"
"; @B G2 b.1\n"
"v_mfma_f32_16x16x32_bf16 v[108:111], a[128:131], v[148:151], 0\n"
"v_mfma_f32_16x16x32_bf16 v[108:111], a[132:135], v[180:183], v[108:111]\n"
"v_mfma_f32_16x16x32_bf16 v[108:111], a[136:139], v[24:27], v[108:111]\n"
"v_mfma_f32_16x16x32_bf16 v[108:111], a[140:143], v[32:35], v[108:111]\n"
"s_waitcnt vmcnt(32)\n"
"v_mfma_f32_16x16x32_bf16 v[108:111], a[144:147], v[44:47], v[108:111]\n"
"v_mfma_f32_16x16x32_bf16 v[108:111], a[148:151], v[48:51], v[108:111]\n"
"ds_write_b128 v3, v[124:127] offset:3072\n"
"v_mfma_f32_16x16x32_bf16 v[108:111], a[152:155], v[28:31], v[108:111]\n"
"v_mfma_f32_16x16x32_bf16 v[108:111], a[156:159], v[56:59], v[108:111]\n"
"v_mfma_f32_16x16x32_bf16 v[112:115], a[188:191], v[56:59], 0\n"
"s_add_u32 s97, s56, 0x400000\n"
"buffer_load_dwordx4 v[120:123], v0, s[4:7], s97 offen sc1\n"
"v_mfma_f32_16x16x32_bf16 v[112:115], a[184:187], v[28:31], v[112:115]\n"
"v_mfma_f32_16x16x32_bf16 v[112:115], a[180:183], v[48:51], v[112:115]\n"
"v_mfma_f32_16x16x32_bf16 v[112:115], a[176:179], v[44:47], v[112:115]\n"
"buffer_load_dwordx4 v[124:127], v0, s[4:7], s97 offen offset:1024 sc1\n"
"v_mfma_f32_16x16x32_bf16 v[112:115], a[172:175], v[32:35], v[112:115]\n"
"v_cmp_u_f32_e32 vcc, v108, v108\n"
"v_mfma_f32_16x16x32_bf16 v[112:115], a[168:171], v[24:27], v[112:115]\n"
"s_cbranch_vccnz .Lmk_p64s\n"
".Lmk_p64r:\n"
"v_mfma_f32_16x16x32_bf16 v[112:115], a[164:167], v[180:183], v[112:115]\n"
"ds_write_b128 v3, v[108:111] offset:6144\n"
"v_mfma_f32_16x16x32_bf16 v[112:115], a[160:163], v[148:151], v[112:115]\n"
"buffer_load_dwordx4 v[108:111], v0, s[4:7], s97 offen offset:2048 sc1\n"
"v_mfma_f32_16x16x32_bf16 v[128:131], a[192:195], v[148:151], 0\n"
"v_mfma_f32_16x16x32_bf16 v[128:131], a[196:199], v[180:183], v[128:131]\n"
"v_mfma_f32_16x16x32_bf16 v[128:131], a[200:203], v[24:27], v[128:131]\n"
"buffer_load_dwordx4 v[132:135], v0, s[4:7], s97 offen offset:3072 sc1\n"
"v_mfma_f32_16x16x32_bf16 v[128:131], a[204:207], v[32:35], v[128:131]\n"
"v_mfma_f32_16x16x32_bf16 v[128:131], a[208:211], v[44:47], v[128:131]\n"
"ds_read_b128 v[136:139], v2 offset:8192\n"
"ds_read_b128 v[140:143], v2 offset:9216\n"
"v_mfma_f32_16x16x32_bf16 v[128:131], a[212:215], v[48:51], v[128:131]\n"
"ds_write_b128 v3, v[112:115] offset:7168\n"
"v_mfma_f32_16x16x32_bf16 v[128:131], a[216:219], v[28:31], v[128:131]\n"
"buffer_load_dwordx4 v[112:115], v1, s[4:7], s97 offen sc1\n"
"v_mfma_f32_16x16x32_bf16 v[128:131], a[220:223], v[56:59], v[128:131]\n"
"v_mfma_f32_16x16x32_bf16 v[144:147], a[252:255], v[56:59], 0\n"
"v_mfma_f32_16x16x32_bf16 v[144:147], a[248:251], v[28:31], v[144:147]\n"
"buffer_load_dwordx4 v[28:31], v1, s[4:7], s97 offen offset:1024 sc1\n"
"s_waitcnt lgkmcnt(1)\n"
"v_mfma_f32_16x16x32_bf16 v[144:147], a[244:247], v[48:51], v[144:147]\n"
"v_lshlrev_b32_e32 v255, 16, v136\n"
"v_and_b32_e32 v254, 0xffff0000, v136\n"
"v_lshlrev_b32_e32 v253, 16, v137\n"
"v_mfma_f32_16x16x32_bf16 v[144:147], a[240:243], v[44:47], v[144:147]\n"
"v_and_b32_e32 v252, 0xffff0000, v137\n"
"v_lshlrev_b32_e32 v251, 16, v138\n"
"v_and_b32_e32 v250, 0xffff0000, v138\n"
"v_mfma_f32_16x16x32_bf16 v[144:147], a[236:239], v[32:35], v[144:147]\n"
"buffer_load_dwordx4 v[32:35], v1, s[4:7], s97 offen offset:2048 sc1\n"
"v_lshlrev_b32_e32 v249, 16, v139\n"
"v_mfma_f32_16x16x32_bf16 v[144:147], a[232:235], v[24:27], v[144:147]\n"
"ds_write_b128 v3, v[128:131] offset:8192\n"
"v_mfma_f32_16x16x32_bf16 v[144:147], a[228:231], v[180:183], v[144:147]\n"
"v_and_b32_e32 v248, 0xffff0000, v139\n"
"v_lshlrev_b32_e32 v247, 16, v140\n"
"v_and_b32_e32 v246, 0xffff0000, v140\n"
"v_mfma_f32_16x16x32_bf16 v[144:147], a[224:227], v[148:151], v[144:147]\n"
"s_waitcnt vmcnt(28)\n"
"buffer_load_dwordx4 v[24:27], v1, s[4:7], s97 offen offset:3072 sc1\n"
"; @B G2 b.2\n"
"v_mfma_f32_16x16x32_bf16 v[44:47], a[128:131], v[36:39], 0\n"
"s_add_u32 s97, s57, 0x400000\n"
"v_lshlrev_b32_e32 v245, 16, v141\n"
"v_and_b32_e32 v244, 0xffff0000, v141\n"
"v_lshlrev_b32_e32 v243, 16, v142\n"
"v_mfma_f32_16x16x32_bf16 v[44:47], a[132:135], v[40:43], v[44:47]\n"
"v_and_b32_e32 v242, 0xffff0000, v142\n"
"v_lshlrev_b32_e32 v241, 16, v143\n"
"v_and_b32_e32 v240, 0xffff0000, v143\n"
"v_mfma_f32_16x16x32_bf16 v[44:47], a[136:139], v[52:55], v[44:47]\n"
"buffer_load_dwordx4 v[48:51], v0, s[4:7], s97 offen sc1\n"
"v_mfma_f32_16x16x32_bf16 v[44:47], a[140:143], v[60:63], v[44:47]\n"
"s_waitcnt vmcnt(26)\n"
"v_mfma_f32_16x16x32_bf16 v[44:47], a[144:147], v[64:67], v[44:47]\n"
"v_mfma_f32_16x16x32_bf16 v[44:47], a[148:151], v[68:71], v[44:47]\n"
"ds_write_b128 v3, v[144:147] offset:9216\n"
"v_mfma_f32_16x16x32_bf16 v[44:47], a[152:155], v[72:75], v[44:47]\n"
"buffer_load_dwordx4 v[56:59], v0, s[4:7], s97 offen offset:1024 sc1\n"
"v_mfma_f32_16x16x32_bf16 v[44:47], a[156:159], v[76:79], v[44:47]\n"
"v_mfma_f32_16x16x32_bf16 v[128:131], a[188:191], v[76:79], 0\n"
"v_mfma_f32_16x16x32_bf16 v[128:131], a[184:187], v[72:75], v[128:131]\n"
"s_waitcnt lgkmcnt(0)\n"
"s_barrier\n"
"v_mfma_f32_16x16x32_bf16 v[128:131], a[180:183], v[68:71], v[128:131]\n"
"buffer_load_dwordx4 v[136:139], v0, s[4:7], s97 offen offset:2048 sc1\n"
"v_mfma_f32_16x16x32_bf16 v[128:131], a[176:179], v[64:67], v[128:131]\n"
"ds_read_b128 v[140:143], v4\n"
"ds_read_b128 v[144:147], v5\n"
"v_mfma_f32_16x16x32_bf16 v[128:131], a[172:175], v[60:63], v[128:131]\n"
"v_cmp_u_f32_e32 vcc, v44, v44\n"
"v_mfma_f32_16x16x32_bf16 v[128:131], a[168:171], v[52:55], v[128:131]\n"
"s_cbranch_vccnz .Lmk_p65s\n"
".Lmk_p65r:\n"
"v_mfma_f32_16x16x32_bf16 v[128:131], a[164:167], v[40:43], v[128:131]\n"
"ds_write_b128 v3, v[44:47] offset:12288\n"
"v_mfma_f32_16x16x32_bf16 v[128:131], a[160:163], v[36:39], v[128:131]\n"
"buffer_load_dwordx4 v[44:47], v0, s[4:7], s97 offen offset:3072 sc1\n"
"v_mfma_f32_16x16x32_bf16 v[148:151], a[192:195], v[36:39], 0\n"
"ds_read_b128 v[152:155], v4 offset:1024\n"
"ds_read_b128 v[156:159], v5 offset:1024\n"
"s_waitcnt lgkmcnt(3)\n"
"v_mfma_f32_16x16x32_bf16 v[148:151], a[196:199], v[40:43], v[148:151]\n"
"v_add_f32_e32 v140, v140, v255\n"
"v_add_f32_e32 v141, v141, v254\n"
"v_add_f32_e32 v142, v142, v253\n"
"v_mfma_f32_16x16x32_bf16 v[148:151], a[200:203], v[52:55], v[148:151]\n"
"buffer_load_dwordx4 v[160:163], v1, s[4:7], s97 offen sc1\n"
"v_add_f32_e32 v143, v143, v252\n"
"v_mfma_f32_16x16x32_bf16 v[148:151], a[204:207], v[60:63], v[148:151]\n"
"v_add_f32_e32 v140, v140, v144\n"
"v_add_f32_e32 v141, v141, v145\n"
"v_add_f32_e32 v142, v142, v146\n"
"v_mfma_f32_16x16x32_bf16 v[148:151], a[208:211], v[64:67], v[148:151]\n"
"v_add_f32_e32 v143, v143, v147\n"
"v_mfma_f32_16x16x32_bf16 v[148:151], a[212:215], v[68:71], v[148:151]\n"
"ds_write_b128 v3, v[128:131] offset:13312\n"
"v_mfma_f32_16x16x32_bf16 v[148:151], a[216:219], v[72:75], v[148:151]\n"
"buffer_load_dwordx4 v[128:131], v1, s[4:7], s97 offen offset:1024 sc1\n"
"s_waitcnt lgkmcnt(1)\n"
"v_mfma_f32_16x16x32_bf16 v[148:151], a[220:223], v[76:79], v[148:151]\n"
"ds_read_b128 v[144:147], v4 offset:2048\n"
"ds_read_b128 v[164:167], v5 offset:2048\n"
"v_add_f32_e32 v152, v152, v251\n"
"v_mfma_f32_16x16x32_bf16 v[168:171], a[252:255], v[76:79], 0\n"
"v_add_f32_e32 v153, v153, v250\n"
"v_add_f32_e32 v154, v154, v249\n"
"v_add_f32_e32 v155, v155, v248\n"
"v_mfma_f32_16x16x32_bf16 v[168:171], a[248:251], v[72:75], v[168:171]\n"
"buffer_load_dwordx4 v[72:75], v1, s[4:7], s97 offen offset:2048 sc1\n"
"v_add_f32_e32 v152, v152, v156\n"
"v_mfma_f32_16x16x32_bf16 v[168:171], a[244:247], v[68:71], v[168:171]\n"
"v_add_f32_e32 v153, v153, v157\n"
"v_add_f32_e32 v154, v154, v158\n"
"v_add_f32_e32 v155, v155, v159\n"
"v_mfma_f32_16x16x32_bf16 v[168:171], a[240:243], v[64:67], v[168:171]\n"
"v_mfma_f32_16x16x32_bf16 v[168:171], a[236:239], v[60:63], v[168:171]\n"
"buffer_load_dwordx4 v[60:63], v1, s[4:7], s97 offen offset:3072 sc1\n"
"s_waitcnt lgkmcnt(0)\n"
"v_mfma_f32_16x16x32_bf16 v[168:171], a[232:235], v[52:55], v[168:171]\n"
"ds_write_b128 v3, v[148:151] offset:14336\n"
"v_mfma_f32_16x16x32_bf16 v[168:171], a[228:231], v[40:43], v[168:171]\n"
"s_add_u32 s97, s58, 0x400000\n"
"ds_read_b128 v[40:43], v4 offset:3072\n"
"ds_read_b128 v[52:55], v5 offset:3072\n"
"v_add_f32_e32 v144, v144, v247\n"
"v_mfma_f32_16x16x32_bf16 v[168:171], a[224:227], v[36:39], v[168:171]\n"
"s_waitcnt vmcnt(21)\n"
"buffer_load_dwordx4 v[36:39], v0, s[4:7], s97 offen sc1\n"
"; @B G2 b.3\n"
"v_mfma_f32_16x16x32_bf16 v[64:67], a[128:131], v[84:87], v[140:143]\n"
"v_add_f32_e32 v145, v145, v246\n"
"v_add_f32_e32 v146, v146, v245\n"
"v_add_f32_e32 v147, v147, v244\n"
"v_mfma_f32_16x16x32_bf16 v[64:67], a[132:135], v[88:91], v[64:67]\n"
"v_add_f32_e32 v144, v144, v164\n"
"v_add_f32_e32 v145, v145, v165\n"
"v_add_f32_e32 v146, v146, v166\n"
"v_mfma_f32_16x16x32_bf16 v[64:67], a[136:139], v[100:103], v[64:67]\n"
"buffer_load_dwordx4 v[68:71], v0, s[4:7], s97 offen offset:1024 sc1\n"
"v_add_f32_e32 v147, v147, v167\n"
"v_mfma_f32_16x16x32_bf16 v[64:67], a[140:143], v[104:107], v[64:67]\n"
"s_waitcnt vmcnt(19)\n"
"s_waitcnt lgkmcnt(0)\n"
"v_add_f32_e32 v40, v40, v243\n"
"v_add_f32_e32 v41, v41, v242\n"
"v_mfma_f32_16x16x32_bf16 v[64:67], a[144:147], v[80:83], v[64:67]\n"
"v_add_f32_e32 v42, v42, v241\n"
"v_add_f32_e32 v43, v43, v240\n"
"v_add_f32_e32 v40, v40, v52\n"
"v_mfma_f32_16x16x32_bf16 v[64:67], a[148:151], v[116:119], v[64:67]\n"
"ds_write_b128 v3, v[168:171] offset:15360\n"
"v_mfma_f32_16x16x32_bf16 v[64:67], a[152:155], v[96:99], v[64:67]\n"
"buffer_load_dwordx4 v[76:79], v0, s[4:7], s97 offen offset:2048 sc1\n"
"v_add_f32_e32 v41, v41, v53\n"
"v_mfma_f32_16x16x32_bf16 v[64:67], a[156:159], v[92:95], v[64:67]\n"
"v_add_f32_e32 v42, v42, v54\n"
"v_add_f32_e32 v43, v43, v55\n"
"v_mfma_f32_16x16x32_bf16 v[52:55], a[188:191], v[92:95], v[152:155]\n"
"v_mfma_f32_16x16x32_bf16 v[52:55], a[184:187], v[96:99], v[52:55]\n"
"s_waitcnt lgkmcnt(0)\n"
"s_barrier\n"
"v_mfma_f32_16x16x32_bf16 v[52:55], a[180:183], v[116:119], v[52:55]\n"
"buffer_load_dwordx4 v[148:151], v0, s[4:7], s97 offen offset:3072 sc1\n"
"v_mfma_f32_16x16x32_bf16 v[52:55], a[176:179], v[80:83], v[52:55]\n"
"ds_read_b128 v[156:159], v6\n"
"ds_read_b128 v[164:167], v6 offset:1024\n"
"v_mfma_f32_16x16x32_bf16 v[52:55], a[172:175], v[104:107], v[52:55]\n"
"ds_read_b128 v[168:171], v6 offset:2048\n"
"v_cmp_u_f32_e32 vcc, v64, v64\n"
"v_mfma_f32_16x16x32_bf16 v[52:55], a[168:171], v[100:103], v[52:55]\n"
"s_cbranch_vccnz .Lmk_p66s\n"
".Lmk_p66r:\n"
"ds_read_b128 v[140:143], v6 offset:3072\n"
"v_mfma_f32_16x16x32_bf16 v[52:55], a[164:167], v[88:91], v[52:55]\n"
"buffer_load_dwordx4 v[152:155], v1, s[4:7], s97 offen sc1\n"
"v_mfma_f32_16x16x32_bf16 v[52:55], a[160:163], v[84:87], v[52:55]\n"
"v_mfma_f32_16x16x32_bf16 v[144:147], a[192:195], v[84:87], v[144:147]\n"
"v_mfma_f32_16x16x32_bf16 v[144:147], a[196:199], v[88:91], v[144:147]\n"
"buffer_load_dwordx4 v[172:175], v1, s[4:7], s97 offen offset:1024 sc1\n"
"v_mfma_f32_16x16x32_bf16 v[144:147], a[200:203], v[100:103], v[144:147]\n"
"v_mfma_f32_16x16x32_bf16 v[144:147], a[204:207], v[104:107], v[144:147]\n"
"v_mfma_f32_16x16x32_bf16 v[144:147], a[208:211], v[80:83], v[144:147]\n"
"buffer_load_dwordx4 v[176:179], v1, s[4:7], s97 offen offset:2048 sc1\n"
"s_waitcnt lgkmcnt(0)\n"
"v_mfma_f32_16x16x32_bf16 v[144:147], a[212:215], v[116:119], v[144:147]\n"
"v_add_f32_e32 v156, v64, v156\n"
"v_add_f32_e32 v157, v65, v157\n"
"v_add_f32_e32 v158, v66, v158\n"
"v_mfma_f32_16x16x32_bf16 v[144:147], a[216:219], v[96:99], v[144:147]\n"
"v_add_f32_e32 v159, v67, v159\n"
"v_add_f32_e32 v164, v52, v164\n"
"v_add_f32_e32 v165, v53, v165\n"
"v_mfma_f32_16x16x32_bf16 v[144:147], a[220:223], v[92:95], v[144:147]\n"
"buffer_load_dwordx4 v[64:67], v1, s[4:7], s97 offen offset:3072 sc1\n"
"v_add_f32_e32 v166, v54, v166\n"
"v_mfma_f32_16x16x32_bf16 v[40:43], a[252:255], v[92:95], v[40:43]\n"
"v_add_f32_e32 v167, v55, v167\n"
"v_cvt_pk_bf16_f32 v52, v156, v157\n"
"v_cvt_pk_bf16_f32 v53, v158, v159\n"
"v_mfma_f32_16x16x32_bf16 v[40:43], a[248:251], v[96:99], v[40:43]\n"
"s_add_u32 s97, s61, 0x840000\n"
"v_cvt_pk_bf16_f32 v54, v164, v165\n"
"v_cvt_pk_bf16_f32 v55, v166, v167\n"
"v_mfma_f32_16x16x32_bf16 v[40:43], a[244:247], v[116:119], v[40:43]\n"
"buffer_store_dwordx4 v[52:55], v0, s[4:7], s97 offen sc1\n"
"v_mfma_f32_16x16x32_bf16 v[40:43], a[240:243], v[80:83], v[40:43]\n"
"ds_write_b128 v2, v[52:55] offset:8192\n"
"v_mfma_f32_16x16x32_bf16 v[40:43], a[236:239], v[104:107], v[40:43]\n"
"v_mfma_f32_16x16x32_bf16 v[40:43], a[232:235], v[100:103], v[40:43]\n"
"v_mfma_f32_16x16x32_bf16 v[40:43], a[228:231], v[88:91], v[40:43]\n"
"v_mfma_f32_16x16x32_bf16 v[40:43], a[224:227], v[84:87], v[40:43]\n"
"; @B fin b\n"
"s_add_u32 s97, s61, 0x840000\n"
"v_add_f32_e32 v168, v144, v168\n"
"v_add_f32_e32 v169, v145, v169\n"
"v_add_f32_e32 v170, v146, v170\n"
"v_add_f32_e32 v171, v147, v171\n"
"s_nop 2\n"
"v_add_f32_e32 v140, v40, v140\n"
"v_add_f32_e32 v141, v41, v141\n"
"v_add_f32_e32 v142, v42, v142\n"
"v_add_f32_e32 v143, v43, v143\n"
"v_cvt_pk_bf16_f32 v40, v168, v169\n"
"v_cvt_pk_bf16_f32 v41, v170, v171\n"
"v_cvt_pk_bf16_f32 v42, v140, v141\n"
"v_cvt_pk_bf16_f32 v43, v142, v143\n"
"buffer_store_dwordx4 v[40:43], v0, s[4:7], s97 offen offset:1024 sc1\n"
"s_add_u32 s97, s59, 0x400000\n"
"buffer_load_dwordx4 v[80:83], v0, s[4:7], s97 offen sc1\n"
"buffer_load_dwordx4 v[84:87], v0, s[4:7], s97 offen offset:1024 sc1\n"
"buffer_load_dwordx4 v[88:91], v0, s[4:7], s97 offen offset:2048 sc1\n"
"buffer_load_dwordx4 v[92:95], v0, s[4:7], s97 offen offset:3072 sc1\n"
"buffer_load_dwordx4 v[96:99], v1, s[4:7], s97 offen sc1\n"
"buffer_load_dwordx4 v[100:103], v1, s[4:7], s97 offen offset:1024 sc1\n"
"buffer_load_dwordx4 v[104:107], v1, s[4:7], s97 offen offset:2048 sc1\n"
"buffer_load_dwordx4 v[116:119], v1, s[4:7], s97 offen offset:3072 sc1\n"
"s_waitcnt vmcnt(32)\n"
"; @B G2 a.0\n"
"v_mfma_f32_16x16x32_bf16 v[140:143], a[128:131], v[120:123], 0\n"
"v_mfma_f32_16x16x32_bf16 v[140:143], a[132:135], v[124:127], v[140:143]\n"
"s_waitcnt vmcnt(30)\n"
"ds_write_b128 v2, v[40:43] offset:9216\n"
"v_mfma_f32_16x16x32_bf16 v[140:143], a[136:139], v[108:111], v[140:143]\n"
"v_mov_b32_e32 v144, 0x3f803f80\n"
"v_mov_b32_e32 v145, 0x3f803f80\n"
"v_mfma_f32_16x16x32_bf16 v[140:143], a[140:143], v[132:135], v[140:143]\n"
"s_waitcnt vmcnt(28)\n"
"v_mov_b32_e32 v146, 0x3f803f80\n"
"v_mov_b32_e32 v147, 0x3f803f80\n"
"v_mfma_f32_16x16x32_bf16 v[140:143], a[144:147], v[112:115], v[140:143]\n"
"s_nop 0\n"
"v_mfma_f32_16x16x32_bf16 v[156:159], v[144:147], v[52:55], 0\n"
"v_mfma_f32_16x16x32_bf16 v[156:159], v[144:147], v[40:43], v[156:159]\n"
"v_mfma_f32_16x16x32_bf16 v[144:147], v[52:55], v[52:55], 0\n"
"v_mfma_f32_16x16x32_bf16 v[144:147], v[40:43], v[40:43], v[144:147]\n"
"buffer_load_dwordx4 a[56:59], v1, s[8:11], s98 offen offset:2048 sc1\n"
"v_mfma_f32_16x16x32_bf16 v[140:143], a[148:151], v[28:31], v[140:143]\n"
"s_waitcnt vmcnt(27)\n"
"v_mfma_f32_16x16x32_bf16 v[140:143], a[152:155], v[32:35], v[140:143]\n"
"v_mfma_f32_16x16x32_bf16 v[140:143], a[156:159], v[24:27], v[140:143]\n"
"buffer_load_dwordx4 a[60:63], v1, s[8:11], s98 offen offset:3072 sc1\n"
"v_mfma_f32_16x16x32_bf16 v[40:43], a[188:191], v[24:27], 0\n"
"s_mov_b32 s96, 0xaaaaaaaa\n"
"s_mov_b32 s97, 0xaaaaaaaa\n"
"s_add_u32 s98, s69, 0x4000\n"
"v_mfma_f32_16x16x32_bf16 v[40:43], a[184:187], v[32:35], v[40:43]\n"
"v_mfma_f32_16x16x32_bf16 v[40:43], a[180:183], v[28:31], v[40:43]\n"
"buffer_load_dwordx4 a[64:67], v0, s[8:11], s98 offen sc1\n"
"v_mfma_f32_16x16x32_bf16 v[40:43], a[176:179], v[112:115], v[40:43]\n"
"v_cndmask_b32_e64 v144, v144, v145, s[96:97]\n"
"v_cndmask_b32_e64 v146, v146, v147, s[96:97]\n"
"v_mfma_f32_16x16x32_bf16 v[40:43], a[172:175], v[132:135], v[40:43]\n"
"v_cmp_u_f32_e32 vcc, v140, v140\n"
"v_mfma_f32_16x16x32_bf16 v[40:43], a[168:171], v[108:111], v[40:43]\n"
"s_cbranch_vccnz .Lmk_p68s\n"
".Lmk_p68r:\n"
"v_mfma_f32_16x16x32_bf16 v[40:43], a[164:167], v[124:127], v[40:43]\n"
"ds_write_b128 v3, v[140:143]\n"
"v_mfma_f32_16x16x32_bf16 v[40:43], a[160:163], v[120:123], v[40:43]\n"
"s_mov_b32 s96, 0xcccccccc\n"
"s_mov_b32 s97, 0xcccccccc\n"
"v_mfma_f32_16x16x32_bf16 v[52:55], a[192:195], v[120:123], 0\n"
"v_cndmask_b32_e64 v157, v144, v146, s[96:97]\n"
"v_mfma_f32_16x16x32_bf16 v[52:55], a[196:199], v[124:127], v[52:55]\n"
"s_mov_b32 s96, 0xf0000f\n"
"s_mov_b32 s97, 0xf0000f00\n"
"v_mfma_f32_16x16x32_bf16 v[52:55], a[200:203], v[108:111], v[52:55]\n"
"v_cndmask_b32_e64 v156, v156, v157, s[96:97]\n"
"s_add_u32 s97, s63, 0x84000\n"
"v_mfma_f32_16x16x32_bf16 v[52:55], a[204:207], v[132:135], v[52:55]\n"
"buffer_store_dword v156, v14, s[4:7], s97 offen sc1\n"
"v_mfma_f32_16x16x32_bf16 v[52:55], a[208:211], v[112:115], v[52:55]\n"
"buffer_load_dwordx4 a[68:71], v0, s[8:11], s98 offen offset:1024 sc1\n"
"v_mfma_f32_16x16x32_bf16 v[52:55], a[212:215], v[28:31], v[52:55]\n"
"ds_write_b128 v3, v[40:43] offset:1024\n"
"v_mfma_f32_16x16x32_bf16 v[52:55], a[216:219], v[32:35], v[52:55]\n"
"v_mfma_f32_16x16x32_bf16 v[52:55], a[220:223], v[24:27], v[52:55]\n"
"buffer_load_dwordx4 a[72:75], v0, s[8:11], s98 offen offset:2048 sc1\n"
"v_mfma_f32_16x16x32_bf16 v[40:43], a[252:255], v[24:27], 0\n"
"v_mfma_f32_16x16x32_bf16 v[40:43], a[248:251], v[32:35], v[40:43]\n"
"v_mfma_f32_16x16x32_bf16 v[40:43], a[244:247], v[28:31], v[40:43]\n"
"buffer_load_dwordx4 a[76:79], v0, s[8:11], s98 offen offset:3072 sc1\n"
"v_mfma_f32_16x16x32_bf16 v[40:43], a[240:243], v[112:115], v[40:43]\n"
"v_mfma_f32_16x16x32_bf16 v[40:43], a[236:239], v[132:135], v[40:43]\n"
"v_mfma_f32_16x16x32_bf16 v[40:43], a[232:235], v[108:111], v[40:43]\n"
"ds_write_b128 v3, v[52:55] offset:2048\n"
"v_mfma_f32_16x16x32_bf16 v[40:43], a[228:231], v[124:127], v[40:43]\n"
"buffer_load_dwordx4 a[80:83], v1, s[8:11], s98 offen sc1\n"
"v_mfma_f32_16x16x32_bf16 v[40:43], a[224:227], v[120:123], v[40:43]\n"
"s_waitcnt vmcnt(30)\n"
"; @B G2 a.1\n"
"v_mfma_f32_16x16x32_bf16 v[24:27], a[128:131], v[48:51], 0\n"
"v_mfma_f32_16x16x32_bf16 v[24:27], a[132:135], v[56:59], v[24:27]\n"
"buffer_load_dwordx4 a[84:87], v1, s[8:11], s98 offen offset:1024 sc1\n"
"v_mfma_f32_16x16x32_bf16 v[24:27], a[136:139], v[136:139], v[24:27]\n"
"v_mfma_f32_16x16x32_bf16 v[24:27], a[140:143], v[44:47], v[24:27]\n"
"s_waitcnt vmcnt(27)\n"
"v_mfma_f32_16x16x32_bf16 v[24:27], a[144:147], v[160:163], v[24:27]\n"
"buffer_load_dwordx4 a[88:91], v1, s[8:11], s98 offen offset:2048 sc1\n"
"v_mfma_f32_16x16x32_bf16 v[24:27], a[148:151], v[128:131], v[24:27]\n"
"ds_write_b128 v3, v[40:43] offset:3072\n"
"v_mfma_f32_16x16x32_bf16 v[24:27], a[152:155], v[72:75], v[24:27]\n"
"v_mfma_f32_16x16x32_bf16 v[24:27], a[156:159], v[60:63], v[24:27]\n"
"buffer_load_dwordx4 a[92:95], v1, s[8:11], s98 offen offset:3072 sc1\n"
"v_mfma_f32_16x16x32_bf16 v[28:31], a[188:191], v[60:63], 0\n"
"s_add_u32 s98, s69, 0x6000\n"
"v_mfma_f32_16x16x32_bf16 v[28:31], a[184:187], v[72:75], v[28:31]\n"
"v_mfma_f32_16x16x32_bf16 v[28:31], a[180:183], v[128:131], v[28:31]\n"
"buffer_load_dwordx4 a[96:99], v0, s[8:11], s98 offen sc1\n"
"v_mfma_f32_16x16x32_bf16 v[28:31], a[176:179], v[160:163], v[28:31]\n"
"v_mfma_f32_16x16x32_bf16 v[28:31], a[172:175], v[44:47], v[28:31]\n"
"v_cmp_u_f32_e32 vcc, v24, v24\n"
"v_mfma_f32_16x16x32_bf16 v[28:31], a[168:171], v[136:139], v[28:31]\n"
"s_cbranch_vccnz .Lmk_p69s\n"
".Lmk_p69r:\n"
"v_mfma_f32_16x16x32_bf16 v[28:31], a[164:167], v[56:59], v[28:31]\n"
"ds_write_b128 v3, v[24:27] offset:6144\n"
"v_mfma_f32_16x16x32_bf16 v[28:31], a[160:163], v[48:51], v[28:31]\n"
"buffer_load_dwordx4 a[100:103], v0, s[8:11], s98 offen offset:1024 sc1\n"
"v_mfma_f32_16x16x32_bf16 v[24:27], a[192:195], v[48:51], 0\n"
"v_mfma_f32_16x16x32_bf16 v[24:27], a[196:199], v[56:59], v[24:27]\n"
"v_mfma_f32_16x16x32_bf16 v[24:27], a[200:203], v[136:139], v[24:27]\n"
"buffer_load_dwordx4 a[104:107], v0, s[8:11], s98 offen offset:2048 sc1\n"
"v_mfma_f32_16x16x32_bf16 v[24:27], a[204:207], v[44:47], v[24:27]\n"
"v_mfma_f32_16x16x32_bf16 v[24:27], a[208:211], v[160:163], v[24:27]\n"
"ds_read_b128 v[32:35], v2\n"
"ds_read_b128 v[40:43], v2 offset:1024\n"
"v_mfma_f32_16x16x32_bf16 v[24:27], a[212:215], v[128:131], v[24:27]\n"
"ds_write_b128 v3, v[28:31] offset:7168\n"
"v_mfma_f32_16x16x32_bf16 v[24:27], a[216:219], v[72:75], v[24:27]\n"
"buffer_load_dwordx4 a[108:111], v0, s[8:11], s98 offen offset:3072 sc1\n"
"v_mfma_f32_16x16x32_bf16 v[24:27], a[220:223], v[60:63], v[24:27]\n"
"v_mfma_f32_16x16x32_bf16 v[28:31], a[252:255], v[60:63], 0\n"
"v_mfma_f32_16x16x32_bf16 v[28:31], a[248:251], v[72:75], v[28:31]\n"
"buffer_load_dwordx4 a[112:115], v1, s[8:11], s98 offen sc1\n"
"s_waitcnt lgkmcnt(1)\n"
"v_mfma_f32_16x16x32_bf16 v[28:31], a[244:247], v[128:131], v[28:31]\n"
"v_lshlrev_b32_e32 v255, 16, v32\n"
"v_and_b32_e32 v254, 0xffff0000, v32\n"
"v_lshlrev_b32_e32 v253, 16, v33\n"
"v_mfma_f32_16x16x32_bf16 v[28:31], a[240:243], v[160:163], v[28:31]\n"
"v_and_b32_e32 v252, 0xffff0000, v33\n"
"v_lshlrev_b32_e32 v251, 16, v34\n"
"v_and_b32_e32 v250, 0xffff0000, v34\n"
"v_mfma_f32_16x16x32_bf16 v[28:31], a[236:239], v[44:47], v[28:31]\n"
"buffer_load_dwordx4 a[116:119], v1, s[8:11], s98 offen offset:1024 sc1\n"
"v_lshlrev_b32_e32 v249, 16, v35\n"
"v_mfma_f32_16x16x32_bf16 v[28:31], a[232:235], v[136:139], v[28:31]\n"
"ds_write_b128 v3, v[24:27] offset:8192\n"
"v_mfma_f32_16x16x32_bf16 v[28:31], a[228:231], v[56:59], v[28:31]\n"
"v_and_b32_e32 v248, 0xffff0000, v35\n"
"v_lshlrev_b32_e32 v247, 16, v40\n"
"v_and_b32_e32 v246, 0xffff0000, v40\n"
"v_mfma_f32_16x16x32_bf16 v[28:31], a[224:227], v[48:51], v[28:31]\n"
"s_waitcnt vmcnt(31)\n"
"buffer_load_dwordx4 a[120:123], v1, s[8:11], s98 offen offset:2048 sc1\n"
"; @B G2 a.2\n"
"v_mfma_f32_16x16x32_bf16 v[24:27], a[128:131], v[36:39], 0\n"
"v_lshlrev_b32_e32 v245, 16, v41\n"
"v_and_b32_e32 v244, 0xffff0000, v41\n"
"v_lshlrev_b32_e32 v243, 16, v42\n"
"v_mfma_f32_16x16x32_bf16 v[24:27], a[132:135], v[68:71], v[24:27]\n"
"v_and_b32_e32 v242, 0xffff0000, v42\n"
"v_lshlrev_b32_e32 v241, 16, v43\n"
"v_and_b32_e32 v240, 0xffff0000, v43\n"
"v_mfma_f32_16x16x32_bf16 v[24:27], a[136:139], v[76:79], v[24:27]\n"
"buffer_load_dwordx4 a[124:127], v1, s[8:11], s98 offen offset:3072 sc1\n"
"v_mfma_f32_16x16x32_bf16 v[24:27], a[140:143], v[148:151], v[24:27]\n"
"s_waitcnt vmcnt(29)\n"
"s_add_u32 s98, s69, 0x8000\n"
"v_mfma_f32_16x16x32_bf16 v[24:27], a[144:147], v[152:155], v[24:27]\n"
"v_mfma_f32_16x16x32_bf16 v[24:27], a[148:151], v[172:175], v[24:27]\n"
"ds_write_b128 v3, v[28:31] offset:9216\n"
"v_mfma_f32_16x16x32_bf16 v[24:27], a[152:155], v[176:179], v[24:27]\n"
"buffer_load_dwordx4 v[28:31], v0, s[8:11], s98 offen sc1\n"
"v_mfma_f32_16x16x32_bf16 v[24:27], a[156:159], v[64:67], v[24:27]\n"
"v_mfma_f32_16x16x32_bf16 v[32:35], a[188:191], v[64:67], 0\n"
"v_mfma_f32_16x16x32_bf16 v[32:35], a[184:187], v[176:179], v[32:35]\n"
"s_waitcnt lgkmcnt(0)\n"
"s_barrier\n"
"v_mfma_f32_16x16x32_bf16 v[32:35], a[180:183], v[172:175], v[32:35]\n"
"buffer_load_dwordx4 v[40:43], v0, s[8:11], s98 offen offset:1024 sc1\n"
"v_mfma_f32_16x16x32_bf16 v[32:35], a[176:179], v[152:155], v[32:35]\n"
"ds_read_b128 v[44:47], v4\n"
"ds_read_b128 v[48:51], v5\n"
"v_mfma_f32_16x16x32_bf16 v[32:35], a[172:175], v[148:151], v[32:35]\n"
"v_cmp_u_f32_e32 vcc, v24, v24\n"
"v_mfma_f32_16x16x32_bf16 v[32:35], a[168:171], v[76:79], v[32:35]\n"
"s_cbranch_vccnz .Lmk_p70s\n"
".Lmk_p70r:\n"
"v_mfma_f32_16x16x32_bf16 v[32:35], a[164:167], v[68:71], v[32:35]\n"
"ds_write_b128 v3, v[24:27] offset:12288\n"
"v_mfma_f32_16x16x32_bf16 v[32:35], a[160:163], v[36:39], v[32:35]\n"
"buffer_load_dwordx4 v[24:27], v0, s[8:11], s98 offen offset:2048 sc1\n"
"v_mfma_f32_16x16x32_bf16 v[52:55], a[192:195], v[36:39], 0\n"
"ds_read_b128 v[56:59], v4 offset:1024\n"
"ds_read_b128 v[60:63], v5 offset:1024\n"
"s_waitcnt lgkmcnt(3)\n"
"v_mfma_f32_16x16x32_bf16 v[52:55], a[196:199], v[68:71], v[52:55]\n"
"v_add_f32_e32 v44, v44, v255\n"
"v_add_f32_e32 v45, v45, v254\n"
"v_add_f32_e32 v46, v46, v253\n"
"v_mfma_f32_16x16x32_bf16 v[52:55], a[200:203], v[76:79], v[52:55]\n"
"buffer_load_dwordx4 v[72:75], v0, s[8:11], s98 offen offset:3072 sc1\n"
"v_add_f32_e32 v47, v47, v252\n"
"v_mfma_f32_16x16x32_bf16 v[52:55], a[204:207], v[148:151], v[52:55]\n"
"v_add_f32_e32 v44, v44, v48\n"
"v_add_f32_e32 v45, v45, v49\n"
"v_add_f32_e32 v46, v46, v50\n"
"v_mfma_f32_16x16x32_bf16 v[52:55], a[208:211], v[152:155], v[52:55]\n"
"v_add_f32_e32 v47, v47, v51\n"
"v_mfma_f32_16x16x32_bf16 v[52:55], a[212:215], v[172:175], v[52:55]\n"
"ds_write_b128 v3, v[32:35] offset:13312\n"
"v_mfma_f32_16x16x32_bf16 v[52:55], a[216:219], v[176:179], v[52:55]\n"
"buffer_load_dwordx4 v[32:35], v1, s[8:11], s98 offen sc1\n"
"s_waitcnt lgkmcnt(1)\n"
"v_mfma_f32_16x16x32_bf16 v[52:55], a[220:223], v[64:67], v[52:55]\n"
"ds_read_b128 v[48:51], v4 offset:2048\n"
"ds_read_b128 v[108:111], v5 offset:2048\n"
"v_add_f32_e32 v56, v56, v251\n"
"v_mfma_f32_16x16x32_bf16 v[112:115], a[252:255], v[64:67], 0\n"
"v_add_f32_e32 v57, v57, v250\n"
"v_add_f32_e32 v58, v58, v249\n"
"v_add_f32_e32 v59, v59, v248\n"
"v_mfma_f32_16x16x32_bf16 v[112:115], a[248:251], v[176:179], v[112:115]\n"
"buffer_load_dwordx4 v[64:67], v1, s[8:11], s98 offen offset:1024 sc1\n"
"v_add_f32_e32 v56, v56, v60\n"
"v_mfma_f32_16x16x32_bf16 v[112:115], a[244:247], v[172:175], v[112:115]\n"
"v_add_f32_e32 v57, v57, v61\n"
"v_add_f32_e32 v58, v58, v62\n"
"v_add_f32_e32 v59, v59, v63\n"
"v_mfma_f32_16x16x32_bf16 v[112:115], a[240:243], v[152:155], v[112:115]\n"
"v_mfma_f32_16x16x32_bf16 v[112:115], a[236:239], v[148:151], v[112:115]\n"
"buffer_load_dwordx4 v[60:63], v1, s[8:11], s98 offen offset:2048 sc1\n"
"s_waitcnt lgkmcnt(0)\n"
"v_mfma_f32_16x16x32_bf16 v[112:115], a[232:235], v[76:79], v[112:115]\n"
"ds_write_b128 v3, v[52:55] offset:14336\n"
"v_mfma_f32_16x16x32_bf16 v[112:115], a[228:231], v[68:71], v[112:115]\n"
"ds_read_b128 v[52:55], v4 offset:3072\n"
"ds_read_b128 v[68:71], v5 offset:3072\n"
"v_add_f32_e32 v48, v48, v247\n"
"v_mfma_f32_16x16x32_bf16 v[112:115], a[224:227], v[36:39], v[112:115]\n"
"s_waitcnt vmcnt(30)\n"
"buffer_load_dwordx4 v[36:39], v1, s[8:11], s98 offen offset:3072 sc1\n"
"; @B G2 a.3\n"
"v_mfma_f32_16x16x32_bf16 v[76:79], a[128:131], v[80:83], v[44:47]\n"
"s_add_u32 s98, s69, 0xa000\n"
"v_add_f32_e32 v49, v49, v246\n"
"v_add_f32_e32 v50, v50, v245\n"
"v_add_f32_e32 v51, v51, v244\n"
"v_mfma_f32_16x16x32_bf16 v[76:79], a[132:135], v[84:87], v[76:79]\n"
"v_add_f32_e32 v48, v48, v108\n"
"v_add_f32_e32 v49, v49, v109\n"
"v_add_f32_e32 v50, v50, v110\n"
"v_mfma_f32_16x16x32_bf16 v[76:79], a[136:139], v[88:91], v[76:79]\n"
"buffer_load_dwordx4 v[120:123], v0, s[8:11], s98 offen sc1\n"
"v_add_f32_e32 v51, v51, v111\n"
"v_mfma_f32_16x16x32_bf16 v[76:79], a[140:143], v[92:95], v[76:79]\n"
"s_waitcnt vmcnt(28)\n"
"s_waitcnt lgkmcnt(0)\n"
"v_add_f32_e32 v52, v52, v243\n"
"v_add_f32_e32 v53, v53, v242\n"
"v_mfma_f32_16x16x32_bf16 v[76:79], a[144:147], v[96:99], v[76:79]\n"
"v_add_f32_e32 v54, v54, v241\n"
"v_add_f32_e32 v55, v55, v240\n"
"v_add_f32_e32 v52, v52, v68\n"
"v_mfma_f32_16x16x32_bf16 v[76:79], a[148:151], v[100:103], v[76:79]\n"
"ds_write_b128 v3, v[112:115] offset:15360\n"
"v_mfma_f32_16x16x32_bf16 v[76:79], a[152:155], v[104:107], v[76:79]\n"
"buffer_load_dwordx4 v[108:111], v0, s[8:11], s98 offen offset:1024 sc1\n"
"v_add_f32_e32 v53, v53, v69\n"
"v_mfma_f32_16x16x32_bf16 v[76:79], a[156:159], v[116:119], v[76:79]\n"
"v_add_f32_e32 v54, v54, v70\n"
"v_add_f32_e32 v55, v55, v71\n"
"v_mfma_f32_16x16x32_bf16 v[68:71], a[188:191], v[116:119], v[56:59]\n"
"v_mfma_f32_16x16x32_bf16 v[68:71], a[184:187], v[104:107], v[68:71]\n"
"s_waitcnt lgkmcnt(0)\n"
"s_barrier\n"
"v_mfma_f32_16x16x32_bf16 v[68:71], a[180:183], v[100:103], v[68:71]\n"
"buffer_load_dwordx4 v[112:115], v0, s[8:11], s98 offen offset:2048 sc1\n"
"v_mfma_f32_16x16x32_bf16 v[68:71], a[176:179], v[96:99], v[68:71]\n"
"ds_read_b128 v[124:127], v6\n"
"ds_read_b128 v[128:131], v6 offset:1024\n"
"v_mfma_f32_16x16x32_bf16 v[68:71], a[172:175], v[92:95], v[68:71]\n"
"ds_read_b128 v[132:135], v6 offset:2048\n"
"v_cmp_u_f32_e32 vcc, v76, v76\n"
"v_mfma_f32_16x16x32_bf16 v[68:71], a[168:171], v[88:91], v[68:71]\n"
"s_cbranch_vccnz .Lmk_p71s\n"
".Lmk_p71r:\n"
"ds_read_b128 v[44:47], v6 offset:3072\n"
"v_mfma_f32_16x16x32_bf16 v[68:71], a[164:167], v[84:87], v[68:71]\n"
"buffer_load_dwordx4 v[56:59], v0, s[8:11], s98 offen offset:3072 sc1\n"
"v_mfma_f32_16x16x32_bf16 v[68:71], a[160:163], v[80:83], v[68:71]\n"
"v_mfma_f32_16x16x32_bf16 v[48:51], a[192:195], v[80:83], v[48:51]\n"
"v_mfma_f32_16x16x32_bf16 v[48:51], a[196:199], v[84:87], v[48:51]\n"
"buffer_load_dwordx4 v[136:139], v1, s[8:11], s98 offen sc1\n"
"v_mfma_f32_16x16x32_bf16 v[48:51], a[200:203], v[88:91], v[48:51]\n"
"v_mfma_f32_16x16x32_bf16 v[48:51], a[204:207], v[92:95], v[48:51]\n"
"v_mfma_f32_16x16x32_bf16 v[48:51], a[208:211], v[96:99], v[48:51]\n"
"buffer_load_dwordx4 v[140:143], v1, s[8:11], s98 offen offset:1024 sc1\n"
"s_waitcnt lgkmcnt(0)\n"
"v_mfma_f32_16x16x32_bf16 v[48:51], a[212:215], v[100:103], v[48:51]\n"
"v_add_f32_e32 v124, v76, v124\n"
"v_add_f32_e32 v125, v77, v125\n"
"v_add_f32_e32 v126, v78, v126\n"
"v_mfma_f32_16x16x32_bf16 v[48:51], a[216:219], v[104:107], v[48:51]\n"
"v_add_f32_e32 v127, v79, v127\n"
"v_add_f32_e32 v128, v68, v128\n"
"v_add_f32_e32 v129, v69, v129\n"
"v_mfma_f32_16x16x32_bf16 v[48:51], a[220:223], v[116:119], v[48:51]\n"
"buffer_load_dwordx4 v[76:79], v1, s[8:11], s98 offen offset:2048 sc1\n"
"v_add_f32_e32 v130, v70, v130\n"
"v_mfma_f32_16x16x32_bf16 v[52:55], a[252:255], v[116:119], v[52:55]\n"
"v_add_f32_e32 v131, v71, v131\n"
"v_cvt_pk_bf16_f32 v68, v124, v125\n"
"v_cvt_pk_bf16_f32 v69, v126, v127\n"
"v_mfma_f32_16x16x32_bf16 v[52:55], a[248:251], v[104:107], v[52:55]\n"
"s_add_u32 s97, s61, 0x800000\n"
"v_cvt_pk_bf16_f32 v70, v128, v129\n"
"v_cvt_pk_bf16_f32 v71, v130, v131\n"
"v_mfma_f32_16x16x32_bf16 v[52:55], a[244:247], v[100:103], v[52:55]\n"
"buffer_load_dwordx4 v[100:103], v1, s[8:11], s98 offen offset:3072 sc1\n"
"v_mfma_f32_16x16x32_bf16 v[52:55], a[240:243], v[96:99], v[52:55]\n"
"s_add_u32 s98, s52, 0x840000\n"
"buffer_load_dwordx4 v[96:99], v0, s[8:11], s98 offen sc1\n"
"v_mfma_f32_16x16x32_bf16 v[52:55], a[236:239], v[92:95], v[52:55]\n"
"buffer_store_dwordx4 v[68:71], v0, s[4:7], s97 offen sc1\n"
"v_mfma_f32_16x16x32_bf16 v[52:55], a[232:235], v[88:91], v[52:55]\n"
"buffer_load_dwordx4 v[88:91], v0, s[8:11], s98 offen offset:1024 sc1\n"
"v_mfma_f32_16x16x32_bf16 v[52:55], a[228:231], v[84:87], v[52:55]\n"
"ds_write_b128 v2, v[68:71]\n"
"v_mfma_f32_16x16x32_bf16 v[52:55], a[224:227], v[80:83], v[52:55]\n"
"buffer_load_dwordx4 v[80:83], v0, s[8:11], s98 offen offset:2048 sc1\n"
"buffer_load_dwordx4 v[84:87], v0, s[8:11], s98 offen offset:3072 sc1\n"
"buffer_load_dwordx4 v[92:95], v1, s[8:11], s98 offen sc1\n"
"buffer_load_dwordx4 v[104:107], v1, s[8:11], s98 offen offset:1024 sc1\n"
"buffer_load_dwordx4 v[116:119], v1, s[8:11], s98 offen offset:2048 sc1\n"
"buffer_load_dwordx4 v[124:127], v1, s[8:11], s98 offen offset:3072 sc1\n"
"s_add_u32 s98, s53, 0x840000\n"
"buffer_load_dwordx4 v[128:131], v0, s[8:11], s98 offen sc1\n"
"buffer_load_dwordx4 v[144:147], v0, s[8:11], s98 offen offset:1024 sc1\n"
"buffer_load_dwordx4 v[148:151], v0, s[8:11], s98 offen offset:2048 sc1\n"
"buffer_load_dwordx4 v[152:155], v0, s[8:11], s98 offen offset:3072 sc1\n"
"buffer_load_dwordx4 v[156:159], v1, s[8:11], s98 offen sc1\n"
"buffer_load_dwordx4 v[160:163], v1, s[8:11], s98 offen offset:1024 sc1\n"
"buffer_load_dwordx4 v[164:167], v1, s[8:11], s98 offen offset:2048 sc1\n"
"buffer_load_dwordx4 v[168:171], v1, s[8:11], s98 offen offset:3072 sc1\n"
"s_lshr_b32 s98, s79, 7\n"
"s_add_u32 s52, s52, 0x800000\n"
"s_add_u32 s53, s53, 0x800000\n"
"s_add_u32 s54, s54, 0x800000\n"
"s_add_u32 s55, s55, 0x800000\n"
"s_add_u32 s56, s56, 0x800000\n"
"s_add_u32 s57, s57, 0x800000\n"
"s_add_u32 s58, s58, 0x800000\n"
"s_add_u32 s59, s59, 0x800000\n"
"s_add_u32 s60, s60, 0x800000\n"
"s_add_u32 s61, s61, 0x800000\n"
"s_add_u32 s62, s62, 0x80000\n"
"s_add_u32 s63, s63, 0x80000\n"
"s_add_u32 s64, s64, 0x1000000\n"
"s_add_u32 s65, s65, s79\n"
"s_add_u32 s66, s66, s79\n"
"s_add_u32 s67, s67, 0x20000\n"
"s_add_u32 s68, s68, 0x20000\n"
"s_add_u32 s71, s71, s98\n"
"s_add_u32 s73, s73, s98\n"
"s_mov_b32 s0, 1\n"
"s_mov_b32 s10, 0\n"
".Lmk_a0:\n"
"s_waitcnt vmcnt(12)\n"
"; @A G1 0.0\n"
"v_mfma_f32_16x16x32_bf16 v[172:175], a[0:3], v[96:99], 0\n"
"v_mfma_f32_16x16x32_bf16 v[172:175], a[4:7], v[88:91], v[172:175]\n"
"s_add_u32 s98, s62, 0x4000\n"
"v_mfma_f32_16x16x32_bf16 v[172:175], a[8:11], v[80:83], v[172:175]\n"
"s_add_u32 s97, s54, 0x40000\n"
"buffer_load_dwordx4 v[176:179], v0, s[4:7], s97 offen sc1\n"
"v_mfma_f32_16x16x32_bf16 v[172:175], a[12:15], v[84:87], v[172:175]\n"
"s_waitcnt vmcnt(9)\n"
"v_mfma_f32_16x16x32_bf16 v[172:175], a[16:19], v[92:95], v[172:175]\n"
"buffer_load_dword v255, v10, s[4:7], s98 offen sc1\n"
"v_mfma_f32_16x16x32_bf16 v[172:175], a[20:23], v[104:107], v[172:175]\n"
"v_add_f32_e32 v132, v48, v132\n"
"v_add_f32_e32 v133, v49, v133\n"
"v_mfma_f32_16x16x32_bf16 v[172:175], a[24:27], v[116:119], v[172:175]\n"
"v_add_f32_e32 v134, v50, v134\n"
"v_add_f32_e32 v135, v51, v135\n"
"v_mfma_f32_16x16x32_bf16 v[172:175], a[28:31], v[124:127], v[172:175]\n"
"buffer_load_dwordx4 v[48:51], v0, s[4:7], s97 offen offset:1024 sc1\n"
"v_mfma_f32_16x16x32_bf16 v[180:183], a[60:63], v[124:127], 0\n"
"buffer_load_dword v254, v10, s[4:7], s98 offen offset:256 sc1\n"
"v_mfma_f32_16x16x32_bf16 v[180:183], a[56:59], v[116:119], v[180:183]\n"
"v_add_f32_e32 v44, v52, v44\n"
"v_add_f32_e32 v45, v53, v45\n"
"v_mfma_f32_16x16x32_bf16 v[180:183], a[52:55], v[104:107], v[180:183]\n"
"v_add_f32_e32 v46, v54, v46\n"
"v_add_f32_e32 v47, v55, v47\n"
"v_mfma_f32_16x16x32_bf16 v[180:183], a[48:51], v[92:95], v[180:183]\n"
"buffer_load_dword v253, v10, s[4:7], s98 offen offset:512 sc1\n"
"v_mfma_f32_16x16x32_bf16 v[180:183], a[44:47], v[84:87], v[180:183]\n"
"v_cmp_u_f32_e32 vcc, v172, v172\n"
"v_cvt_pk_bf16_f32 v52, v132, v133\n"
"v_mfma_f32_16x16x32_bf16 v[180:183], a[40:43], v[80:83], v[180:183]\n"
"s_cbranch_vccnz .Lmk_p76s\n"
".Lmk_p76r:\n"
"v_cvt_pk_bf16_f32 v53, v134, v135\n"
"v_mfma_f32_16x16x32_bf16 v[180:183], a[36:39], v[88:91], v[180:183]\n"
"buffer_load_dwordx4 v[132:135], v0, s[4:7], s97 offen offset:2048 sc1\n"
"v_mfma_f32_16x16x32_bf16 v[180:183], a[32:35], v[96:99], v[180:183]\n"
"ds_write_b128 v3, v[172:175]\n"
"v_mfma_f32_16x16x32_bf16 v[172:175], a[64:67], v[96:99], 0\n"
"buffer_load_dword v252, v10, s[4:7], s98 offen offset:768 sc1\n"
"v_mfma_f32_16x16x32_bf16 v[172:175], a[68:71], v[88:91], v[172:175]\n"
"buffer_load_dwordx4 v[184:187], v0, s[4:7], s97 offen offset:3072 sc1\n"
"v_mfma_f32_16x16x32_bf16 v[172:175], a[72:75], v[80:83], v[172:175]\n"
"v_cvt_pk_bf16_f32 v54, v44, v45\n"
"v_cvt_pk_bf16_f32 v55, v46, v47\n"
"v_mfma_f32_16x16x32_bf16 v[172:175], a[76:79], v[84:87], v[172:175]\n"
"buffer_load_dword v251, v10, s[4:7], s98 offen offset:1024 sc1\n"
"v_mfma_f32_16x16x32_bf16 v[172:175], a[80:83], v[92:95], v[172:175]\n"
"buffer_store_dwordx4 v[52:55], v0, s[4:7], s61 offen offset:1024 sc1\n"
"v_mfma_f32_16x16x32_bf16 v[172:175], a[84:87], v[104:107], v[172:175]\n"
"ds_write_b128 v3, v[180:183] offset:1024\n"
"v_mfma_f32_16x16x32_bf16 v[172:175], a[88:91], v[116:119], v[172:175]\n"
"buffer_load_dwordx4 v[44:47], v1, s[4:7], s97 offen sc1\n"
"v_mfma_f32_16x16x32_bf16 v[172:175], a[92:95], v[124:127], v[172:175]\n"
"buffer_load_dword v250, v10, s[4:7], s98 offen offset:1280 sc1\n"
"v_mfma_f32_16x16x32_bf16 v[180:183], a[124:127], v[124:127], 0\n"
"ds_write_b128 v2, v[52:55] offset:1024\n"
"v_mfma_f32_16x16x32_bf16 v[180:183], a[120:123], v[116:119], v[180:183]\n"
"v_mov_b32_e32 v188, 0x3f803f80\n"
"v_mov_b32_e32 v189, 0x3f803f80\n"
"v_mfma_f32_16x16x32_bf16 v[180:183], a[116:119], v[104:107], v[180:183]\n"
"buffer_load_dword v249, v10, s[4:7], s98 offen offset:1536 sc1\n"
"v_mfma_f32_16x16x32_bf16 v[180:183], a[112:115], v[92:95], v[180:183]\n"
"buffer_load_dwordx4 v[192:195], v1, s[4:7], s97 offen offset:1024 sc1\n"
"v_mfma_f32_16x16x32_bf16 v[180:183], a[108:111], v[84:87], v[180:183]\n"
"v_mov_b32_e32 v190, 0x3f803f80\n"
"v_mov_b32_e32 v191, 0x3f803f80\n"
"v_mfma_f32_16x16x32_bf16 v[180:183], a[104:107], v[80:83], v[180:183]\n"
"ds_write_b128 v3, v[172:175] offset:2048\n"
"v_mfma_f32_16x16x32_bf16 v[172:175], v[188:191], v[68:71], 0\n"
"v_mfma_f32_16x16x32_bf16 v[172:175], v[188:191], v[52:55], v[172:175]\n"
"v_mfma_f32_16x16x32_bf16 v[188:191], v[68:71], v[68:71], 0\n"
"v_mfma_f32_16x16x32_bf16 v[188:191], v[52:55], v[52:55], v[188:191]\n"
"v_mfma_f32_16x16x32_bf16 v[180:183], a[100:103], v[88:91], v[180:183]\n"
"v_mov_b32_e32 v52, -1\n"
"v_mov_b32_e32 v53, -1\n"
"v_mfma_f32_16x16x32_bf16 v[180:183], a[96:99], v[96:99], v[180:183]\n"
"v_mov_b32_e32 v54, -1\n"
"v_mov_b32_e32 v55, -1\n"
"v_mfma_f32_16x16x32_bf16 v[68:71], v[28:31], v[96:99], 0\n"
"buffer_load_dwordx4 v[196:199], v1, s[4:7], s97 offen offset:2048 sc1\n"
"v_mfma_f32_16x16x32_bf16 v[68:71], v[40:43], v[88:91], v[68:71]\n"
"s_add_u32 s96, s64, 0x4000\n"
"buffer_store_dwordx4 v[52:55], v2, s[4:7], s96 offen sc1\n"
"v_mfma_f32_16x16x32_bf16 v[68:71], v[24:27], v[80:83], v[68:71]\n"
"buffer_load_dword v248, v10, s[4:7], s98 offen offset:1792 sc1\n"
"v_mfma_f32_16x16x32_bf16 v[68:71], v[72:75], v[84:87], v[68:71]\n"
"s_mov_b32 s94, 0xaaaaaaaa\n"
"s_mov_b32 s95, 0xaaaaaaaa\n"
"v_mfma_f32_16x16x32_bf16 v[68:71], v[32:35], v[92:95], v[68:71]\n"
"v_mfma_f32_16x16x32_bf16 v[68:71], v[64:67], v[104:107], v[68:71]\n"
"buffer_load_dwordx4 v[200:203], v1, s[4:7], s97 offen offset:3072 sc1\n"
"v_mfma_f32_16x16x32_bf16 v[68:71], v[60:63], v[116:119], v[68:71]\n"
"ds_write_b128 v3, v[180:183] offset:3072\n"
"v_mfma_f32_16x16x32_bf16 v[68:71], v[36:39], v[124:127], v[68:71]\n"
"v_cndmask_b32_e64 v188, v188, v189, s[94:95]\n"
"v_cndmask_b32_e64 v190, v190, v191, s[94:95]\n"
"v_mfma_f32_16x16x32_bf16 v[180:183], v[100:103], v[124:127], 0\n"
"s_mov_b32 s94, 0xcccccccc\n"
"s_mov_b32 s95, 0xcccccccc\n"
"v_mfma_f32_16x16x32_bf16 v[180:183], v[76:79], v[116:119], v[180:183]\n"
"v_cndmask_b32_e64 v173, v188, v190, s[94:95]\n"
"v_mfma_f32_16x16x32_bf16 v[180:183], v[140:143], v[104:107], v[180:183]\n"
"s_mov_b32 s94, 0xf0000f\n"
"s_mov_b32 s95, 0xf0000f00\n"
"v_mfma_f32_16x16x32_bf16 v[180:183], v[136:139], v[92:95], v[180:183]\n"
"v_cndmask_b32_e64 v172, v172, v173, s[94:95]\n"
"v_mfma_f32_16x16x32_bf16 v[180:183], v[56:59], v[84:87], v[180:183]\n"
"buffer_store_dword v172, v14, s[4:7], s63 offen sc1\n"
"v_mfma_f32_16x16x32_bf16 v[180:183], v[112:115], v[80:83], v[180:183]\n"
"ds_write_b128 v3, v[68:71] offset:4096\n"
"v_mfma_f32_16x16x32_bf16 v[180:183], v[108:111], v[88:91], v[180:183]\n"
"v_mfma_f32_16x16x32_bf16 v[180:183], v[120:123], v[96:99], v[180:183]\n"
"s_waitcnt vmcnt(23)\n"
"; @A G1 0.1\n"
"v_mfma_f32_16x16x32_bf16 v[68:71], a[0:3], v[128:131], 0\n"
"v_mfma_f32_16x16x32_bf16 v[68:71], a[4:7], v[144:147], v[68:71]\n"
"v_mfma_f32_16x16x32_bf16 v[68:71], a[8:11], v[148:151], v[68:71]\n"
"s_add_u32 s98, s55, 0x40000\n"
"buffer_load_dwordx4 v[80:83], v0, s[4:7], s98 offen sc1\n"
"v_mfma_f32_16x16x32_bf16 v[68:71], a[12:15], v[152:155], v[68:71]\n"
"s_waitcnt vmcnt(20)\n"
"v_mfma_f32_16x16x32_bf16 v[68:71], a[16:19], v[156:159], v[68:71]\n"
"v_mfma_f32_16x16x32_bf16 v[68:71], a[20:23], v[160:163], v[68:71]\n"
"ds_write_b128 v3, v[180:183] offset:5120\n"
"v_mfma_f32_16x16x32_bf16 v[68:71], a[24:27], v[164:167], v[68:71]\n"
"v_mfma_f32_16x16x32_bf16 v[68:71], a[28:31], v[168:171], v[68:71]\n"
"buffer_load_dwordx4 v[84:87], v0, s[4:7], s98 offen offset:1024 sc1\n"
"v_mfma_f32_16x16x32_bf16 v[88:91], a[60:63], v[168:171], 0\n"
"v_mfma_f32_16x16x32_bf16 v[88:91], a[56:59], v[164:167], v[88:91]\n"
"v_mfma_f32_16x16x32_bf16 v[88:91], a[52:55], v[160:163], v[88:91]\n"
"v_mfma_f32_16x16x32_bf16 v[88:91], a[48:51], v[156:159], v[88:91]\n"
"v_mfma_f32_16x16x32_bf16 v[88:91], a[44:47], v[152:155], v[88:91]\n"
"s_nop 1\n"
"v_cmp_u_f32_e32 vcc, v68, v68\n"
"v_mfma_f32_16x16x32_bf16 v[88:91], a[40:43], v[148:151], v[88:91]\n"
"s_cbranch_vccnz .Lmk_p77s\n"
".Lmk_p77r:\n"
"v_mfma_f32_16x16x32_bf16 v[88:91], a[36:39], v[144:147], v[88:91]\n"
"buffer_load_dwordx4 v[92:95], v0, s[4:7], s98 offen offset:2048 sc1\n"
"v_mfma_f32_16x16x32_bf16 v[88:91], a[32:35], v[128:131], v[88:91]\n"
"ds_write_b128 v3, v[68:71] offset:6144\n"
"v_mfma_f32_16x16x32_bf16 v[68:71], a[64:67], v[128:131], 0\n"
"v_mfma_f32_16x16x32_bf16 v[68:71], a[68:71], v[144:147], v[68:71]\n"
"buffer_load_dwordx4 v[96:99], v0, s[4:7], s98 offen offset:3072 sc1\n"
"v_mfma_f32_16x16x32_bf16 v[68:71], a[72:75], v[148:151], v[68:71]\n"
"v_mfma_f32_16x16x32_bf16 v[68:71], a[76:79], v[152:155], v[68:71]\n"
"v_mfma_f32_16x16x32_bf16 v[68:71], a[80:83], v[156:159], v[68:71]\n"
"v_mfma_f32_16x16x32_bf16 v[68:71], a[84:87], v[160:163], v[68:71]\n"
"ds_write_b128 v3, v[88:91] offset:7168\n"
"v_mfma_f32_16x16x32_bf16 v[68:71], a[88:91], v[164:167], v[68:71]\n"
"buffer_load_dwordx4 v[88:91], v1, s[4:7], s98 offen sc1\n"
"v_mfma_f32_16x16x32_bf16 v[68:71], a[92:95], v[168:171], v[68:71]\n"
"v_mfma_f32_16x16x32_bf16 v[104:107], a[124:127], v[168:171], 0\n"
"v_mfma_f32_16x16x32_bf16 v[104:107], a[120:123], v[164:167], v[104:107]\n"
"v_mfma_f32_16x16x32_bf16 v[104:107], a[116:119], v[160:163], v[104:107]\n"
"v_mfma_f32_16x16x32_bf16 v[104:107], a[112:115], v[156:159], v[104:107]\n"
"buffer_load_dwordx4 v[116:119], v1, s[4:7], s98 offen offset:1024 sc1\n"
"v_mfma_f32_16x16x32_bf16 v[104:107], a[108:111], v[152:155], v[104:107]\n"
"v_mfma_f32_16x16x32_bf16 v[104:107], a[104:107], v[148:151], v[104:107]\n"
"s_nop 0\n"
"ds_write_b128 v3, v[68:71] offset:8192\n"
"v_mfma_f32_16x16x32_bf16 v[104:107], a[100:103], v[144:147], v[104:107]\n"
"v_mfma_f32_16x16x32_bf16 v[104:107], a[96:99], v[128:131], v[104:107]\n"
"v_mfma_f32_16x16x32_bf16 v[68:71], v[28:31], v[128:131], 0\n"
"buffer_load_dwordx4 v[124:127], v1, s[4:7], s98 offen offset:2048 sc1\n"
"v_mfma_f32_16x16x32_bf16 v[68:71], v[40:43], v[144:147], v[68:71]\n"
"s_add_u32 s97, s64, 0x4000\n"
"v_mfma_f32_16x16x32_bf16 v[68:71], v[24:27], v[148:151], v[68:71]\n"
"v_mfma_f32_16x16x32_bf16 v[68:71], v[72:75], v[152:155], v[68:71]\n"
"buffer_store_dwordx4 v[52:55], v2, s[4:7], s97 offen offset:1024 sc1\n"
"v_mfma_f32_16x16x32_bf16 v[68:71], v[32:35], v[156:159], v[68:71]\n"
"s_waitcnt vmcnt(10)\n"
"v_add_f32_e32 v255, v255, v251\n"
"v_add_f32_e32 v254, v254, v250\n"
"v_mfma_f32_16x16x32_bf16 v[68:71], v[64:67], v[160:163], v[68:71]\n"
"buffer_load_dwordx4 v[172:175], v1, s[4:7], s98 offen offset:3072 sc1\n"
"v_mfma_f32_16x16x32_bf16 v[68:71], v[60:63], v[164:167], v[68:71]\n"
"ds_write_b128 v3, v[104:107] offset:9216\n"
"v_mfma_f32_16x16x32_bf16 v[68:71], v[36:39], v[168:171], v[68:71]\n"
"v_add_f32_e32 v253, v253, v249\n"
"v_add_f32_e32 v252, v252, v248\n"
"v_mfma_f32_16x16x32_bf16 v[104:107], v[100:103], v[168:171], 0\n"
"v_add_f32_e32 v255, v255, v253\n"
"v_add_f32_e32 v254, v254, v252\n"
"v_mfma_f32_16x16x32_bf16 v[104:107], v[76:79], v[164:167], v[104:107]\n"
"v_add_f32_e32 v255, v255, v254\n"
"v_cmp_u_f32_e32 vcc, v255, v255\n"
"v_mfma_f32_16x16x32_bf16 v[104:107], v[140:143], v[160:163], v[104:107]\n"
"s_cbranch_vccnz .Lmk_m80s\n"
".Lmk_m80r:\n"
"v_mov_b32_e32 v254, v255\n"
"v_mfma_f32_16x16x32_bf16 v[104:107], v[136:139], v[156:159], v[104:107]\n"
"s_nop 0\n"
"v_permlane32_swap_b32_e32 v255, v254\n"
"v_add_f32_e32 v255, v255, v254\n"
"v_mfma_f32_16x16x32_bf16 v[104:107], v[56:59], v[152:155], v[104:107]\n"
"v_mul_f32_e32 v255, 0x3a800000, v255\n"
"v_mov_b32_e32 v254, v255\n"
"v_mfma_f32_16x16x32_bf16 v[104:107], v[112:115], v[148:151], v[104:107]\n"
"ds_write_b128 v3, v[68:71] offset:10240\n"
"v_mfma_f32_16x16x32_bf16 v[104:107], v[108:111], v[144:147], v[104:107]\n"
"v_permlane16_swap_b32_e32 v255, v254\n"
"v_cvt_pk_bf16_f32 v253, -v255, 0\n"
"v_mfma_f32_16x16x32_bf16 v[104:107], v[120:123], v[128:131], v[104:107]\n"
"v_fma_f32 v254, -v255, v255, v254\n"
"v_lshlrev_b32_e32 v253, 16, v253\n"
"; @A G1 0.2\n"
"v_mfma_f32_16x16x32_bf16 v[68:71], a[0:3], v[176:179], 0\n"
"v_max_f32_e32 v254, 0, v254\n"
"v_cvt_pk_bf16_f32 v254, v254, 0\n"
"v_mfma_f32_16x16x32_bf16 v[68:71], a[4:7], v[48:51], v[68:71]\n"
"v_lshlrev_b32_e32 v254, 16, v254\n"
"v_add_f32_e32 v254, 0x3727c5ac, v254\n"
"v_mfma_f32_16x16x32_bf16 v[68:71], a[8:11], v[132:135], v[68:71]\n"
"v_cvt_pk_bf16_f32 v254, v254, 0\n"
"v_lshlrev_b32_e32 v254, 16, v254\n"
"v_mfma_f32_16x16x32_bf16 v[68:71], a[12:15], v[184:187], v[68:71]\n"
"s_waitcnt vmcnt(10)\n"
"v_sqrt_f32_e32 v254, v254\n"
"v_mfma_f32_16x16x32_bf16 v[68:71], a[16:19], v[44:47], v[68:71]\n"
"s_mov_b32 s98, .Lmk_a2-.Lmk_g75\n"
"s_getpc_b64 s[84:85]\n"
".Lmk_g75:\n"
"s_add_u32 s84, s84, s98\n"
"s_addc_u32 s85, s85, 0\n"
"s_and_b32 s84, s84, -16\n"
"v_mfma_f32_16x16x32_bf16 v[68:71], a[20:23], v[192:195], v[68:71]\n"
"ds_write_b128 v3, v[104:107] offset:11264\n"
"v_mfma_f32_16x16x32_bf16 v[68:71], a[24:27], v[196:199], v[68:71]\n"
"s_and_b32 s85, s85, 0xffff\n"
"s_sub_u32 s98, .Lmk_end-.Lmk_g75, s98\n"
"s_min_u32 s86, s98, 0x3000\n"
"s_mov_b32 s87, 0x20000\n"
"v_mfma_f32_16x16x32_bf16 v[68:71], a[28:31], v[200:203], v[68:71]\n"
"s_mov_b32 m0, 0x27800\n"
"s_nop 0\n"
"buffer_load_dwordx4 v0, s[84:87], s99 offen lds\n"
"v_mfma_f32_16x16x32_bf16 v[104:107], a[60:63], v[200:203], 0\n"
"v_cvt_pk_bf16_f32 v254, v254, 0\n"
"v_lshlrev_b32_e32 v254, 16, v254\n"
"v_mfma_f32_16x16x32_bf16 v[104:107], a[56:59], v[196:199], v[104:107]\n"
"v_rcp_f32_e32 v255, v254\n"
"v_mfma_f32_16x16x32_bf16 v[104:107], a[52:55], v[192:195], v[104:107]\n"
"s_add_u32 s98, s52, 0x48000\n"
"buffer_load_dwordx4 v[128:131], v0, s[4:7], s98 offen sc1\n"
"v_mfma_f32_16x16x32_bf16 v[104:107], a[48:51], v[44:47], v[104:107]\n"
"s_waitcnt lgkmcnt(0)\n"
"s_barrier\n"
"v_mfma_f32_16x16x32_bf16 v[104:107], a[44:47], v[184:187], v[104:107]\n"
"v_cmp_u_f32_e32 vcc, v68, v68\n"
"v_mul_f32_e32 v254, 0x3e8293ee, v255\n"
"v_mfma_f32_16x16x32_bf16 v[104:107], a[40:43], v[132:135], v[104:107]\n"
"s_cbranch_vccnz .Lmk_p78s\n"
".Lmk_p78r:\n"
"ds_read_b128 v[144:147], v4\n"
"v_mfma_f32_16x16x32_bf16 v[104:107], a[36:39], v[48:51], v[104:107]\n"
"ds_write_b128 v3, v[68:71] offset:12288\n"
"v_mfma_f32_16x16x32_bf16 v[104:107], a[32:35], v[176:179], v[104:107]\n"
"buffer_load_dwordx4 v[68:71], v0, s[4:7], s98 offen offset:1024 sc1\n"
"v_mfma_f32_16x16x32_bf16 v[148:151], a[64:67], v[176:179], 0\n"
"ds_read_b128 v[152:155], v9 offset:128\n"
"ds_read_b128 v[156:159], v5\n"
"v_mfma_f32_16x16x32_bf16 v[148:151], a[68:71], v[48:51], v[148:151]\n"
"v_mfma_f32_16x16x32_bf16 v[148:151], a[72:75], v[132:135], v[148:151]\n"
"v_mfma_f32_16x16x32_bf16 v[148:151], a[76:79], v[184:187], v[148:151]\n"
"ds_read_b128 v[160:163], v4 offset:1024\n"
"ds_read_b128 v[164:167], v9 offset:192\n"
"v_mfma_f32_16x16x32_bf16 v[148:151], a[80:83], v[44:47], v[148:151]\n"
"buffer_load_dwordx4 v[168:171], v0, s[4:7], s98 offen offset:2048 sc1\n"
"v_mfma_f32_16x16x32_bf16 v[148:151], a[84:87], v[192:195], v[148:151]\n"
"ds_write_b128 v3, v[104:107] offset:13312\n"
"v_mfma_f32_16x16x32_bf16 v[148:151], a[88:91], v[196:199], v[148:151]\n"
"ds_read_b128 v[104:107], v5 offset:1024\n"
"s_waitcnt lgkmcnt(4)\n"
"v_mfma_f32_16x16x32_bf16 v[148:151], a[92:95], v[200:203], v[148:151]\n"
"v_fmac_f32_e32 v144, v253, v152\n"
"v_fmac_f32_e32 v145, v253, v153\n"
"v_mfma_f32_16x16x32_bf16 v[180:183], a[124:127], v[200:203], 0\n"
"v_fmac_f32_e32 v146, v253, v154\n"
"v_fmac_f32_e32 v147, v253, v155\n"
"v_mfma_f32_16x16x32_bf16 v[180:183], a[120:123], v[196:199], v[180:183]\n"
"buffer_load_dwordx4 v[152:155], v0, s[4:7], s98 offen offset:3072 sc1\n"
"v_mfma_f32_16x16x32_bf16 v[180:183], a[116:119], v[192:195], v[180:183]\n"
"ds_read_b128 v[188:191], v4 offset:2048\n"
"ds_read_b128 v[204:207], v9 offset:256\n"
"v_mfma_f32_16x16x32_bf16 v[180:183], a[112:115], v[44:47], v[180:183]\n"
"ds_read_b128 v[208:211], v5 offset:2048\n"
"v_add_f32_e32 v144, v144, v156\n"
"v_mfma_f32_16x16x32_bf16 v[180:183], a[108:111], v[184:187], v[180:183]\n"
"v_add_f32_e32 v145, v145, v157\n"
"v_add_f32_e32 v146, v146, v158\n"
"v_mfma_f32_16x16x32_bf16 v[180:183], a[104:107], v[132:135], v[180:183]\n"
"ds_write_b128 v3, v[148:151] offset:14336\n"
"v_mfma_f32_16x16x32_bf16 v[180:183], a[100:103], v[48:51], v[180:183]\n"
"buffer_load_dwordx4 v[148:151], v1, s[4:7], s98 offen sc1\n"
"v_mfma_f32_16x16x32_bf16 v[180:183], a[96:99], v[176:179], v[180:183]\n"
"s_waitcnt lgkmcnt(4)\n"
"v_add_f32_e32 v147, v147, v159\n"
"v_fmac_f32_e32 v160, v253, v164\n"
"v_mfma_f32_16x16x32_bf16 v[156:159], v[28:31], v[176:179], 0\n"
"v_fmac_f32_e32 v161, v253, v165\n"
"v_fmac_f32_e32 v162, v253, v166\n"
"v_mfma_f32_16x16x32_bf16 v[156:159], v[40:43], v[48:51], v[156:159]\n"
"ds_read_b128 v[212:215], v4 offset:3072\n"
"ds_read_b128 v[216:219], v9 offset:320\n"
"v_mfma_f32_16x16x32_bf16 v[156:159], v[24:27], v[132:135], v[156:159]\n"
"ds_read_b128 v[220:223], v5 offset:3072\n"
"v_fmac_f32_e32 v163, v253, v167\n"
"v_mfma_f32_16x16x32_bf16 v[156:159], v[72:75], v[184:187], v[156:159]\n"
"buffer_load_dwordx4 v[164:167], v1, s[4:7], s98 offen offset:1024 sc1\n"
"v_mfma_f32_16x16x32_bf16 v[156:159], v[32:35], v[44:47], v[156:159]\n"
"s_add_u32 s97, s64, 0x8000\n"
"v_add_f32_e32 v160, v160, v104\n"
"v_add_f32_e32 v161, v161, v105\n"
"v_mfma_f32_16x16x32_bf16 v[156:159], v[64:67], v[192:195], v[156:159]\n"
"ds_write_b128 v3, v[180:183] offset:15360\n"
"v_mfma_f32_16x16x32_bf16 v[156:159], v[60:63], v[196:199], v[156:159]\n"
"buffer_store_dwordx4 v[52:55], v2, s[4:7], s97 offen sc1\n"
"v_mfma_f32_16x16x32_bf16 v[156:159], v[36:39], v[200:203], v[156:159]\n"
"s_waitcnt lgkmcnt(5)\n"
"v_add_f32_e32 v162, v162, v106\n"
"v_add_f32_e32 v163, v163, v107\n"
"v_mfma_f32_16x16x32_bf16 v[104:107], v[100:103], v[200:203], 0\n"
"buffer_load_dwordx4 v[180:183], v1, s[4:7], s98 offen offset:2048 sc1\n"
"v_mfma_f32_16x16x32_bf16 v[104:107], v[76:79], v[196:199], v[104:107]\n"
"s_add_u32 s97, s68, s80\n"
"s_waitcnt vmcnt(18)\n"
"ds_read_b128 v[196:199], v4 offset:4096\n"
"v_mfma_f32_16x16x32_bf16 v[104:107], v[140:143], v[192:195], v[104:107]\n"
"buffer_store_dwordx2 v[18:19], v11, s[4:7], s97 offen sc1\n"
"v_mfma_f32_16x16x32_bf16 v[104:107], v[136:139], v[44:47], v[104:107]\n"
"ds_read_b128 v[44:47], v9\n"
"ds_read_b128 v[192:195], v5 offset:4096\n"
"v_mfma_f32_16x16x32_bf16 v[104:107], v[56:59], v[184:187], v[104:107]\n"
"v_fmac_f32_e32 v188, v253, v204\n"
"v_fmac_f32_e32 v189, v253, v205\n"
"v_mfma_f32_16x16x32_bf16 v[104:107], v[112:115], v[132:135], v[104:107]\n"
"buffer_load_dwordx4 v[132:135], v1, s[4:7], s98 offen offset:3072 sc1\n"
"v_mfma_f32_16x16x32_bf16 v[104:107], v[108:111], v[48:51], v[104:107]\n"
"ds_write_b128 v3, v[156:159] offset:16384\n"
"v_mfma_f32_16x16x32_bf16 v[104:107], v[120:123], v[176:179], v[104:107]\n"
"s_waitcnt vmcnt(16)\n"
"ds_read_b128 v[48:51], v4 offset:5120\n"
"ds_read_b128 v[156:159], v9 offset:64\n"
"; @A G1 0.3\n"
"v_mfma_f32_16x16x32_bf16 v[176:179], a[0:3], v[80:83], v[144:147]\n"
"ds_read_b128 v[184:187], v5 offset:5120\n"
"v_fmac_f32_e32 v190, v253, v206\n"
"v_mfma_f32_16x16x32_bf16 v[176:179], a[4:7], v[84:87], v[176:179]\n"
"v_fmac_f32_e32 v191, v253, v207\n"
"v_add_f32_e32 v188, v188, v208\n"
"v_mfma_f32_16x16x32_bf16 v[176:179], a[8:11], v[92:95], v[176:179]\n"
"s_add_u32 s98, s53, 0x48000\n"
"buffer_load_dwordx4 v[200:203], v0, s[4:7], s98 offen sc1\n"
"v_mfma_f32_16x16x32_bf16 v[176:179], a[12:15], v[96:99], v[176:179]\n"
"s_waitcnt vmcnt(12)\n"
"v_add_f32_e32 v189, v189, v209\n"
"v_add_f32_e32 v190, v190, v210\n"
"v_mfma_f32_16x16x32_bf16 v[176:179], a[16:19], v[88:91], v[176:179]\n"
"s_waitcnt lgkmcnt(8)\n"
"v_add_f32_e32 v191, v191, v211\n"
"v_fmac_f32_e32 v212, v253, v216\n"
"v_mfma_f32_16x16x32_bf16 v[176:179], a[20:23], v[116:119], v[176:179]\n"
"ds_write_b128 v3, v[104:107] offset:17408\n"
"v_mfma_f32_16x16x32_bf16 v[176:179], a[24:27], v[124:127], v[176:179]\n"
"v_fmac_f32_e32 v213, v253, v217\n"
"v_fmac_f32_e32 v214, v253, v218\n"
"v_mfma_f32_16x16x32_bf16 v[176:179], a[28:31], v[172:175], v[176:179]\n"
"buffer_load_dwordx4 v[104:107], v0, s[4:7], s98 offen offset:1024 sc1\n"
"v_mfma_f32_16x16x32_bf16 v[204:207], a[60:63], v[172:175], v[160:163]\n"
"v_fmac_f32_e32 v215, v253, v219\n"
"v_add_f32_e32 v212, v212, v220\n"
"v_mfma_f32_16x16x32_bf16 v[204:207], a[56:59], v[124:127], v[204:207]\n"
"v_add_f32_e32 v213, v213, v221\n"
"v_add_f32_e32 v214, v214, v222\n"
"v_mfma_f32_16x16x32_bf16 v[204:207], a[52:55], v[116:119], v[204:207]\n"
"s_waitcnt lgkmcnt(0)\n"
"s_barrier\n"
"v_mfma_f32_16x16x32_bf16 v[204:207], a[48:51], v[88:91], v[204:207]\n"
"ds_read_b128 v[208:211], v6\n"
"ds_read_b128 v[216:219], v6 offset:1024\n"
"v_mfma_f32_16x16x32_bf16 v[204:207], a[44:47], v[96:99], v[204:207]\n"
"v_cmp_u_f32_e32 vcc, v176, v176\n"
"v_add_f32_e32 v215, v215, v223\n"
"v_mfma_f32_16x16x32_bf16 v[204:207], a[40:43], v[92:95], v[204:207]\n"
"s_cbranch_vccnz .Lmk_p79s\n"
".Lmk_p79r:\n"
"v_fmac_f32_e32 v196, v253, v44\n"
"v_mfma_f32_16x16x32_bf16 v[204:207], a[36:39], v[84:87], v[204:207]\n"
"buffer_load_dwordx4 v[144:147], v0, s[4:7], s98 offen offset:2048 sc1\n"
"v_mfma_f32_16x16x32_bf16 v[204:207], a[32:35], v[80:83], v[204:207]\n"
"v_fmac_f32_e32 v197, v253, v45\n"
"v_fmac_f32_e32 v198, v253, v46\n"
"v_mfma_f32_16x16x32_bf16 v[188:191], a[64:67], v[80:83], v[188:191]\n"
"v_fmac_f32_e32 v199, v253, v47\n"
"v_add_f32_e32 v196, v196, v192\n"
"v_mfma_f32_16x16x32_bf16 v[188:191], a[68:71], v[84:87], v[188:191]\n"
"buffer_load_dwordx4 v[44:47], v0, s[4:7], s98 offen offset:3072 sc1\n"
"v_mfma_f32_16x16x32_bf16 v[188:191], a[72:75], v[92:95], v[188:191]\n"
"v_add_f32_e32 v197, v197, v193\n"
"v_add_f32_e32 v198, v198, v194\n"
"v_mfma_f32_16x16x32_bf16 v[188:191], a[76:79], v[96:99], v[188:191]\n"
"v_add_f32_e32 v199, v199, v195\n"
"v_fmac_f32_e32 v48, v253, v156\n"
"v_mfma_f32_16x16x32_bf16 v[188:191], a[80:83], v[88:91], v[188:191]\n"
"s_waitcnt lgkmcnt(0)\n"
"v_fmac_f32_e32 v49, v253, v157\n"
"v_fmac_f32_e32 v50, v253, v158\n"
"v_mfma_f32_16x16x32_bf16 v[188:191], a[84:87], v[116:119], v[188:191]\n"
"v_fmac_f32_e32 v51, v253, v159\n"
"v_add_f32_e32 v48, v48, v184\n"
"v_mfma_f32_16x16x32_bf16 v[188:191], a[88:91], v[124:127], v[188:191]\n"
"buffer_load_dwordx4 v[156:159], v1, s[4:7], s98 offen sc1\n"
"v_mfma_f32_16x16x32_bf16 v[188:191], a[92:95], v[172:175], v[188:191]\n"
"ds_read_b128 v[160:163], v6 offset:2048\n"
"ds_read_b128 v[192:195], v6 offset:3072\n"
"v_mfma_f32_16x16x32_bf16 v[212:215], a[124:127], v[172:175], v[212:215]\n"
"v_add_f32_e32 v49, v49, v185\n"
"v_add_f32_e32 v50, v50, v186\n"
"v_mfma_f32_16x16x32_bf16 v[212:215], a[120:123], v[124:127], v[212:215]\n"
"v_add_f32_e32 v51, v51, v187\n"
"v_add_f32_e32 v176, v176, v208\n"
"v_mfma_f32_16x16x32_bf16 v[212:215], a[116:119], v[116:119], v[212:215]\n"
"v_add_f32_e32 v177, v177, v209\n"
"v_add_f32_e32 v178, v178, v210\n"
"v_mfma_f32_16x16x32_bf16 v[212:215], a[112:115], v[88:91], v[212:215]\n"
"buffer_load_dwordx4 v[184:187], v1, s[4:7], s98 offen offset:1024 sc1\n"
"v_mfma_f32_16x16x32_bf16 v[212:215], a[108:111], v[96:99], v[212:215]\n"
"v_add_f32_e32 v179, v179, v211\n"
"v_add_f32_e32 v204, v204, v216\n"
"v_mfma_f32_16x16x32_bf16 v[212:215], a[104:107], v[92:95], v[212:215]\n"
"v_add_f32_e32 v205, v205, v217\n"
"v_add_f32_e32 v206, v206, v218\n"
"v_mfma_f32_16x16x32_bf16 v[212:215], a[100:103], v[84:87], v[212:215]\n"
"v_add_f32_e32 v207, v207, v219\n"
"v_mul_f32_e32 v176, v176, v255\n"
"v_mfma_f32_16x16x32_bf16 v[212:215], a[96:99], v[80:83], v[212:215]\n"
"v_mul_f32_e32 v177, v177, v255\n"
"v_mul_f32_e32 v178, v178, v255\n"
"v_mfma_f32_16x16x32_bf16 v[196:199], v[28:31], v[80:83], v[196:199]\n"
"buffer_load_dwordx4 v[208:211], v1, s[4:7], s98 offen offset:2048 sc1\n"
"v_mfma_f32_16x16x32_bf16 v[196:199], v[40:43], v[84:87], v[196:199]\n"
"v_mul_f32_e32 v179, v179, v255\n"
"v_mul_f32_e32 v204, v204, v255\n"
"v_mfma_f32_16x16x32_bf16 v[196:199], v[24:27], v[92:95], v[196:199]\n"
"v_mul_f32_e32 v205, v205, v255\n"
"v_mul_f32_e32 v206, v206, v255\n"
"v_mfma_f32_16x16x32_bf16 v[196:199], v[72:75], v[96:99], v[196:199]\n"
"v_mul_f32_e32 v207, v207, v255\n"
"v_cvt_pk_bf16_f32 v216, v176, v177\n"
"v_mfma_f32_16x16x32_bf16 v[196:199], v[32:35], v[88:91], v[196:199]\n"
"s_waitcnt lgkmcnt(0)\n"
"v_cvt_pk_bf16_f32 v217, v178, v179\n"
"v_cvt_pk_bf16_f32 v218, v204, v205\n"
"v_mfma_f32_16x16x32_bf16 v[196:199], v[64:67], v[116:119], v[196:199]\n"
"buffer_load_dwordx4 v[176:179], v1, s[4:7], s98 offen offset:3072 sc1\n"
"v_mfma_f32_16x16x32_bf16 v[196:199], v[60:63], v[124:127], v[196:199]\n"
"s_add_u32 s98, s64, 0x8000\n"
"ds_read_b128 v[220:223], v6 offset:4096\n"
"ds_read_b128 v[224:227], v6 offset:5120\n"
"v_mfma_f32_16x16x32_bf16 v[196:199], v[36:39], v[172:175], v[196:199]\n"
"v_cvt_pk_bf16_f32 v219, v206, v207\n"
"v_add_f32_e32 v188, v188, v160\n"
"v_mfma_f32_16x16x32_bf16 v[48:51], v[100:103], v[172:175], v[48:51]\n"
"buffer_store_dwordx4 v[52:55], v2, s[4:7], s98 offen offset:1024 sc1\n"
"v_mfma_f32_16x16x32_bf16 v[48:51], v[76:79], v[124:127], v[48:51]\n"
"ds_write_b128 v7, v[216:219] offset:24576\n"
"v_mfma_f32_16x16x32_bf16 v[48:51], v[140:143], v[116:119], v[48:51]\n"
"v_add_f32_e32 v189, v189, v161\n"
"v_add_f32_e32 v190, v190, v162\n"
"v_mfma_f32_16x16x32_bf16 v[48:51], v[136:139], v[88:91], v[48:51]\n"
"v_add_f32_e32 v191, v191, v163\n"
"v_add_f32_e32 v212, v212, v192\n"
"v_mfma_f32_16x16x32_bf16 v[48:51], v[56:59], v[96:99], v[48:51]\n"
"v_add_f32_e32 v213, v213, v193\n"
"v_add_f32_e32 v214, v214, v194\n"
"v_mfma_f32_16x16x32_bf16 v[48:51], v[112:115], v[92:95], v[48:51]\n"
"v_add_f32_e32 v215, v215, v195\n"
"v_mul_f32_e32 v188, v188, v255\n"
"v_mfma_f32_16x16x32_bf16 v[48:51], v[108:111], v[84:87], v[48:51]\n"
"v_mul_f32_e32 v189, v189, v255\n"
"v_mul_f32_e32 v190, v190, v255\n"
"v_mfma_f32_16x16x32_bf16 v[48:51], v[120:123], v[80:83], v[48:51]\n"
"v_mul_f32_e32 v191, v191, v255\n"
"v_mul_f32_e32 v212, v212, v255\n"
"v_mul_f32_e32 v213, v213, v255\n"
"v_mul_f32_e32 v214, v214, v255\n"
"v_mul_f32_e32 v215, v215, v255\n"
"v_cvt_pk_bf16_f32 v52, v188, v189\n"
"v_cvt_pk_bf16_f32 v53, v190, v191\n"
"v_cvt_pk_bf16_f32 v54, v212, v213\n"
"v_cvt_pk_bf16_f32 v55, v214, v215\n"
"ds_write_b128 v7, v[52:55] offset:57344\n"
".Lmk_a1:\n"
"s_waitcnt vmcnt(15)\n"
"; @A G1 1.0\n"
"v_mfma_f32_16x16x32_bf16 v[80:83], a[0:3], v[128:131], 0\n"
"v_mfma_f32_16x16x32_bf16 v[80:83], a[4:7], v[68:71], v[80:83]\n"
"s_add_u32 s98, s62, 0x4800\n"
"v_mfma_f32_16x16x32_bf16 v[80:83], a[8:11], v[168:171], v[80:83]\n"
"s_add_u32 s97, s54, 0x48000\n"
"buffer_load_dwordx4 v[84:87], v0, s[4:7], s97 offen sc1\n"
"v_mfma_f32_16x16x32_bf16 v[80:83], a[12:15], v[152:155], v[80:83]\n"
"s_waitcnt vmcnt(10)\n"
"v_mfma_f32_16x16x32_bf16 v[80:83], a[16:19], v[148:151], v[80:83]\n"
"buffer_load_dword v255, v10, s[4:7], s98 offen sc1\n"
"v_mfma_f32_16x16x32_bf16 v[80:83], a[20:23], v[164:167], v[80:83]\n"
"s_waitcnt lgkmcnt(2)\n"
"v_add_f32_e32 v196, v196, v220\n"
"v_add_f32_e32 v197, v197, v221\n"
"v_mfma_f32_16x16x32_bf16 v[80:83], a[24:27], v[180:183], v[80:83]\n"
"v_add_f32_e32 v198, v198, v222\n"
"v_add_f32_e32 v199, v199, v223\n"
"v_mfma_f32_16x16x32_bf16 v[80:83], a[28:31], v[132:135], v[80:83]\n"
"buffer_load_dwordx4 v[88:91], v0, s[4:7], s97 offen offset:1024 sc1\n"
"v_mfma_f32_16x16x32_bf16 v[92:95], a[60:63], v[132:135], 0\n"
"s_add_u32 s96, s70, 0\n"
"buffer_load_dwordx4 a[128:131], v0, s[4:7], s96 offen sc1\n"
"v_mfma_f32_16x16x32_bf16 v[92:95], a[56:59], v[180:183], v[92:95]\n"
"buffer_load_dword v253, v10, s[4:7], s98 offen offset:256 sc1\n"
"v_mfma_f32_16x16x32_bf16 v[92:95], a[52:55], v[164:167], v[92:95]\n"
"v_add_f32_e32 v48, v48, v224\n"
"v_add_f32_e32 v49, v49, v225\n"
"v_mfma_f32_16x16x32_bf16 v[92:95], a[48:51], v[148:151], v[92:95]\n"
"v_add_f32_e32 v50, v50, v226\n"
"v_add_f32_e32 v51, v51, v227\n"
"v_mfma_f32_16x16x32_bf16 v[92:95], a[44:47], v[152:155], v[92:95]\n"
"v_cmp_u_f32_e32 vcc, v80, v80\n"
"v_mul_f32_e32 v196, v196, v254\n"
"v_mfma_f32_16x16x32_bf16 v[92:95], a[40:43], v[168:171], v[92:95]\n"
"s_cbranch_vccnz .Lmk_p81s\n"
".Lmk_p81r:\n"
"v_mul_f32_e32 v197, v197, v254\n"
"v_mfma_f32_16x16x32_bf16 v[92:95], a[36:39], v[68:71], v[92:95]\n"
"buffer_load_dwordx4 v[96:99], v0, s[4:7], s97 offen offset:2048 sc1\n"
"v_mfma_f32_16x16x32_bf16 v[92:95], a[32:35], v[128:131], v[92:95]\n"
"ds_write_b128 v3, v[80:83]\n"
"v_mfma_f32_16x16x32_bf16 v[80:83], a[64:67], v[128:131], 0\n"
"buffer_load_dword v252, v10, s[4:7], s98 offen offset:512 sc1\n"
"v_mfma_f32_16x16x32_bf16 v[80:83], a[68:71], v[68:71], v[80:83]\n"
"buffer_load_dwordx4 v[116:119], v0, s[4:7], s97 offen offset:3072 sc1\n"
"v_mfma_f32_16x16x32_bf16 v[80:83], a[72:75], v[168:171], v[80:83]\n"
"v_mul_f32_e32 v198, v198, v254\n"
"v_mul_f32_e32 v199, v199, v254\n"
"v_mfma_f32_16x16x32_bf16 v[80:83], a[76:79], v[152:155], v[80:83]\n"
"buffer_load_dword v251, v10, s[4:7], s98 offen offset:768 sc1\n"
"v_mfma_f32_16x16x32_bf16 v[80:83], a[80:83], v[148:151], v[80:83]\n"
"v_mul_f32_e32 v48, v48, v254\n"
"v_mul_f32_e32 v49, v49, v254\n"
"v_mfma_f32_16x16x32_bf16 v[80:83], a[84:87], v[164:167], v[80:83]\n"
"ds_write_b128 v3, v[92:95] offset:1024\n"
"v_mfma_f32_16x16x32_bf16 v[80:83], a[88:91], v[180:183], v[80:83]\n"
"buffer_load_dwordx4 v[92:95], v1, s[4:7], s97 offen sc1\n"
"v_mfma_f32_16x16x32_bf16 v[80:83], a[92:95], v[132:135], v[80:83]\n"
"buffer_load_dword v250, v10, s[4:7], s98 offen offset:1024 sc1\n"
"v_mfma_f32_16x16x32_bf16 v[124:127], a[124:127], v[132:135], 0\n"
"v_mul_f32_e32 v50, v50, v254\n"
"v_mul_f32_e32 v51, v51, v254\n"
"v_mfma_f32_16x16x32_bf16 v[124:127], a[120:123], v[180:183], v[124:127]\n"
"v_cvt_pk_bf16_f32 v160, v196, v197\n"
"v_cvt_pk_bf16_f32 v161, v198, v199\n"
"v_mfma_f32_16x16x32_bf16 v[124:127], a[116:119], v[164:167], v[124:127]\n"
"buffer_load_dword v254, v10, s[4:7], s98 offen offset:1280 sc1\n"
"v_mfma_f32_16x16x32_bf16 v[124:127], a[112:115], v[148:151], v[124:127]\n"
"buffer_load_dwordx4 v[172:175], v1, s[4:7], s97 offen offset:1024 sc1\n"
"v_mfma_f32_16x16x32_bf16 v[124:127], a[108:111], v[152:155], v[124:127]\n"
"buffer_load_dwordx4 a[132:135], v0, s[4:7], s96 offen offset:1024 sc1\n"
"v_mfma_f32_16x16x32_bf16 v[124:127], a[104:107], v[168:171], v[124:127]\n"
"ds_write_b128 v3, v[80:83] offset:2048\n"
"v_mfma_f32_16x16x32_bf16 v[124:127], a[100:103], v[68:71], v[124:127]\n"
"buffer_load_dword v249, v10, s[4:7], s98 offen offset:1536 sc1\n"
"v_mfma_f32_16x16x32_bf16 v[124:127], a[96:99], v[128:131], v[124:127]\n"
"v_cvt_pk_bf16_f32 v162, v48, v49\n"
"v_cvt_pk_bf16_f32 v163, v50, v51\n"
"v_mfma_f32_16x16x32_bf16 v[48:51], v[28:31], v[128:131], 0\n"
"buffer_load_dwordx4 v[80:83], v1, s[4:7], s97 offen offset:2048 sc1\n"
"v_mfma_f32_16x16x32_bf16 v[48:51], v[40:43], v[68:71], v[48:51]\n"
"buffer_load_dword v248, v10, s[4:7], s98 offen offset:1792 sc1\n"
"v_mfma_f32_16x16x32_bf16 v[48:51], v[24:27], v[168:171], v[48:51]\n"
"ds_write_b128 v2, v[160:163] offset:49152\n"
"v_mfma_f32_16x16x32_bf16 v[48:51], v[72:75], v[152:155], v[48:51]\n"
"v_mfma_f32_16x16x32_bf16 v[48:51], v[32:35], v[148:151], v[48:51]\n"
"v_mfma_f32_16x16x32_bf16 v[48:51], v[64:67], v[164:167], v[48:51]\n"
"buffer_load_dwordx4 v[160:163], v1, s[4:7], s97 offen offset:3072 sc1\n"
"v_mfma_f32_16x16x32_bf16 v[48:51], v[60:63], v[180:183], v[48:51]\n"
"ds_write_b128 v3, v[124:127] offset:3072\n"
"v_mfma_f32_16x16x32_bf16 v[48:51], v[36:39], v[132:135], v[48:51]\n"
"v_mfma_f32_16x16x32_bf16 v[124:127], v[100:103], v[132:135], 0\n"
"v_mfma_f32_16x16x32_bf16 v[124:127], v[76:79], v[180:183], v[124:127]\n"
"v_mfma_f32_16x16x32_bf16 v[124:127], v[140:143], v[164:167], v[124:127]\n"
"v_mfma_f32_16x16x32_bf16 v[124:127], v[136:139], v[148:151], v[124:127]\n"
"v_mfma_f32_16x16x32_bf16 v[124:127], v[56:59], v[152:155], v[124:127]\n"
"v_mfma_f32_16x16x32_bf16 v[124:127], v[112:115], v[168:171], v[124:127]\n"
"s_nop 1\n"
"ds_write_b128 v3, v[48:51] offset:4096\n"
"v_mfma_f32_16x16x32_bf16 v[124:127], v[108:111], v[68:71], v[124:127]\n"
"v_mfma_f32_16x16x32_bf16 v[124:127], v[120:123], v[128:131], v[124:127]\n"
"s_waitcnt vmcnt(23)\n"
"; @A G1 1.1\n"
"v_mfma_f32_16x16x32_bf16 v[48:51], a[0:3], v[200:203], 0\n"
"buffer_load_dwordx4 a[136:139], v0, s[4:7], s96 offen offset:2048 sc1\n"
"v_mfma_f32_16x16x32_bf16 v[48:51], a[4:7], v[104:107], v[48:51]\n"
"v_mfma_f32_16x16x32_bf16 v[48:51], a[8:11], v[144:147], v[48:51]\n"
"s_add_u32 s98, s55, 0x48000\n"
"buffer_load_dwordx4 v[68:71], v0, s[4:7], s98 offen sc1\n"
"v_mfma_f32_16x16x32_bf16 v[48:51], a[12:15], v[44:47], v[48:51]\n"
"s_waitcnt vmcnt(21)\n"
"v_mfma_f32_16x16x32_bf16 v[48:51], a[16:19], v[156:159], v[48:51]\n"
"v_mfma_f32_16x16x32_bf16 v[48:51], a[20:23], v[184:187], v[48:51]\n"
"ds_write_b128 v3, v[124:127] offset:5120\n"
"v_mfma_f32_16x16x32_bf16 v[48:51], a[24:27], v[208:211], v[48:51]\n"
"v_mfma_f32_16x16x32_bf16 v[48:51], a[28:31], v[176:179], v[48:51]\n"
"buffer_load_dwordx4 v[124:127], v0, s[4:7], s98 offen offset:1024 sc1\n"
"v_mfma_f32_16x16x32_bf16 v[128:131], a[60:63], v[176:179], 0\n"
"v_mfma_f32_16x16x32_bf16 v[128:131], a[56:59], v[208:211], v[128:131]\n"
"v_mfma_f32_16x16x32_bf16 v[128:131], a[52:55], v[184:187], v[128:131]\n"
"v_mfma_f32_16x16x32_bf16 v[128:131], a[48:51], v[156:159], v[128:131]\n"
"v_mfma_f32_16x16x32_bf16 v[128:131], a[44:47], v[44:47], v[128:131]\n"
"s_add_u32 s97, s67, 0\n"
"s_nop 0\n"
"v_cmp_u_f32_e32 vcc, v48, v48\n"
"v_mfma_f32_16x16x32_bf16 v[128:131], a[40:43], v[144:147], v[128:131]\n"
"s_cbranch_vccnz .Lmk_p82s\n"
".Lmk_p82r:\n"
"v_mfma_f32_16x16x32_bf16 v[128:131], a[36:39], v[104:107], v[128:131]\n"
"buffer_load_dwordx4 v[132:135], v0, s[4:7], s98 offen offset:2048 sc1\n"
"s_add_u32 s95, s64, 0x2000\n"
"v_mfma_f32_16x16x32_bf16 v[128:131], a[32:35], v[200:203], v[128:131]\n"
"ds_write_b128 v3, v[48:51] offset:6144\n"
"v_mfma_f32_16x16x32_bf16 v[48:51], a[64:67], v[200:203], 0\n"
"buffer_load_dwordx2 v[246:247], v12, s[4:7], s97 offen sc1\n"
"v_mfma_f32_16x16x32_bf16 v[48:51], a[68:71], v[104:107], v[48:51]\n"
"buffer_load_dwordx4 v[148:151], v0, s[4:7], s98 offen offset:3072 sc1\n"
"v_mfma_f32_16x16x32_bf16 v[48:51], a[72:75], v[144:147], v[48:51]\n"
"buffer_store_dwordx4 v[216:219], v7, s[4:7], s95 offen sc1\n"
"v_mfma_f32_16x16x32_bf16 v[48:51], a[76:79], v[44:47], v[48:51]\n"
"v_mfma_f32_16x16x32_bf16 v[48:51], a[80:83], v[156:159], v[48:51]\n"
"buffer_load_dwordx4 a[140:143], v0, s[4:7], s96 offen offset:3072 sc1\n"
"v_mfma_f32_16x16x32_bf16 v[48:51], a[84:87], v[184:187], v[48:51]\n"
"ds_write_b128 v3, v[128:131] offset:7168\n"
"v_mfma_f32_16x16x32_bf16 v[48:51], a[88:91], v[208:211], v[48:51]\n"
"buffer_load_dwordx4 v[128:131], v1, s[4:7], s98 offen sc1\n"
"v_mfma_f32_16x16x32_bf16 v[48:51], a[92:95], v[176:179], v[48:51]\n"
"v_mfma_f32_16x16x32_bf16 v[152:155], a[124:127], v[176:179], 0\n"
"v_mfma_f32_16x16x32_bf16 v[152:155], a[120:123], v[208:211], v[152:155]\n"
"v_mfma_f32_16x16x32_bf16 v[152:155], a[116:119], v[184:187], v[152:155]\n"
"v_mfma_f32_16x16x32_bf16 v[152:155], a[112:115], v[156:159], v[152:155]\n"
"buffer_load_dwordx4 v[164:167], v1, s[4:7], s98 offen offset:1024 sc1\n"
"v_mfma_f32_16x16x32_bf16 v[152:155], a[108:111], v[44:47], v[152:155]\n"
"v_mfma_f32_16x16x32_bf16 v[152:155], a[104:107], v[144:147], v[152:155]\n"
"s_nop 0\n"
"ds_write_b128 v3, v[48:51] offset:8192\n"
"v_mfma_f32_16x16x32_bf16 v[152:155], a[100:103], v[104:107], v[152:155]\n"
"v_mfma_f32_16x16x32_bf16 v[152:155], a[96:99], v[200:203], v[152:155]\n"
"v_mfma_f32_16x16x32_bf16 v[48:51], v[28:31], v[200:203], 0\n"
"buffer_load_dwordx4 v[168:171], v1, s[4:7], s98 offen offset:2048 sc1\n"
"v_mfma_f32_16x16x32_bf16 v[48:51], v[40:43], v[104:107], v[48:51]\n"
"v_mfma_f32_16x16x32_bf16 v[48:51], v[24:27], v[144:147], v[48:51]\n"
"v_mfma_f32_16x16x32_bf16 v[48:51], v[72:75], v[44:47], v[48:51]\n"
"v_mfma_f32_16x16x32_bf16 v[48:51], v[32:35], v[156:159], v[48:51]\n"
"s_waitcnt vmcnt(12)\n"
"v_add_f32_e32 v255, v255, v250\n"
"v_add_f32_e32 v253, v253, v254\n"
"v_mfma_f32_16x16x32_bf16 v[48:51], v[64:67], v[184:187], v[48:51]\n"
"buffer_load_dwordx4 v[180:183], v1, s[4:7], s98 offen offset:3072 sc1\n"
"v_mfma_f32_16x16x32_bf16 v[48:51], v[60:63], v[208:211], v[48:51]\n"
"ds_write_b128 v3, v[152:155] offset:9216\n"
"v_mfma_f32_16x16x32_bf16 v[48:51], v[36:39], v[176:179], v[48:51]\n"
"v_add_f32_e32 v252, v252, v249\n"
"v_add_f32_e32 v251, v251, v248\n"
"v_mfma_f32_16x16x32_bf16 v[152:155], v[100:103], v[176:179], 0\n"
"buffer_load_dwordx4 a[144:147], v1, s[4:7], s96 offen sc1\n"
"s_add_u32 s98, s64, 0x6000\n"
"v_mfma_f32_16x16x32_bf16 v[152:155], v[76:79], v[208:211], v[152:155]\n"
"v_add_f32_e32 v255, v255, v252\n"
"v_add_f32_e32 v253, v253, v251\n"
"v_mfma_f32_16x16x32_bf16 v[152:155], v[140:143], v[184:187], v[152:155]\n"
"buffer_store_dwordx4 v[52:55], v7, s[4:7], s98 offen sc1\n"
"v_mfma_f32_16x16x32_bf16 v[152:155], v[136:139], v[156:159], v[152:155]\n"
"v_add_f32_e32 v255, v255, v253\n"
"v_cmp_u_f32_e32 vcc, v255, v255\n"
"v_mfma_f32_16x16x32_bf16 v[152:155], v[56:59], v[44:47], v[152:155]\n"
"s_cbranch_vccnz .Lmk_m85s\n"
".Lmk_m85r:\n"
"v_mov_b32_e32 v253, v255\n"
"v_mfma_f32_16x16x32_bf16 v[152:155], v[112:115], v[144:147], v[152:155]\n"
"ds_write_b128 v3, v[48:51] offset:10240\n"
"v_mfma_f32_16x16x32_bf16 v[152:155], v[108:111], v[104:107], v[152:155]\n"
"v_permlane32_swap_b32_e32 v255, v253\n"
"v_add_f32_e32 v255, v255, v253\n"
"v_mfma_f32_16x16x32_bf16 v[152:155], v[120:123], v[200:203], v[152:155]\n"
"v_mul_f32_e32 v255, 0x3a800000, v255\n"
"v_mov_b32_e32 v253, v255\n"
"; @A G1 1.2\n"
"v_mfma_f32_16x16x32_bf16 v[44:47], a[0:3], v[84:87], 0\n"
"s_nop 0\n"
"v_permlane16_swap_b32_e32 v255, v253\n"
"v_cvt_pk_bf16_f32 v254, -v255, 0\n"
"v_mfma_f32_16x16x32_bf16 v[44:47], a[4:7], v[88:91], v[44:47]\n"
"s_waitcnt vmcnt(9)\n"
"v_cmp_ne_u64_e32 vcc, v[246:247], v[18:19]\n"
"v_mfma_f32_16x16x32_bf16 v[44:47], a[8:11], v[96:99], v[44:47]\n"
"s_cbranch_vccnz .Lmk_f72s\n"
".Lmk_f72r:\n"
"v_fma_f32 v253, -v255, v255, v253\n"
"v_mfma_f32_16x16x32_bf16 v[44:47], a[12:15], v[116:119], v[44:47]\n"
"v_lshlrev_b32_e32 v254, 16, v254\n"
"v_max_f32_e32 v253, 0, v253\n"
"v_mfma_f32_16x16x32_bf16 v[44:47], a[16:19], v[92:95], v[44:47]\n"
"v_cvt_pk_bf16_f32 v253, v253, 0\n"
"v_lshlrev_b32_e32 v253, 16, v253\n"
"v_mfma_f32_16x16x32_bf16 v[44:47], a[20:23], v[172:175], v[44:47]\n"
"ds_write_b128 v3, v[152:155] offset:11264\n"
"v_mfma_f32_16x16x32_bf16 v[44:47], a[24:27], v[80:83], v[44:47]\n"
"v_add_f32_e32 v253, 0x3727c5ac, v253\n"
"v_cvt_pk_bf16_f32 v253, v253, 0\n"
"v_mfma_f32_16x16x32_bf16 v[44:47], a[28:31], v[160:163], v[44:47]\n"
"v_lshlrev_b32_e32 v253, 16, v253\n"
"v_mfma_f32_16x16x32_bf16 v[48:51], a[60:63], v[160:163], 0\n"
"v_sqrt_f32_e32 v253, v253\n"
"v_mfma_f32_16x16x32_bf16 v[48:51], a[56:59], v[80:83], v[48:51]\n"
"v_cvt_pk_bf16_f32 v253, v253, 0\n"
"v_lshlrev_b32_e32 v253, 16, v253\n"
"v_mfma_f32_16x16x32_bf16 v[48:51], a[52:55], v[172:175], v[48:51]\n"
"s_add_u32 s98, s52, 0x8000\n"
"buffer_load_dwordx4 v[52:55], v0, s[4:7], s98 offen sc1\n"
"v_mfma_f32_16x16x32_bf16 v[48:51], a[48:51], v[92:95], v[48:51]\n"
"s_waitcnt lgkmcnt(0)\n"
"s_barrier\n"
"v_mfma_f32_16x16x32_bf16 v[48:51], a[44:47], v[116:119], v[48:51]\n"
"ds_read_b128 v[104:107], v4\n"
"v_cmp_u_f32_e32 vcc, v44, v44\n"
"v_mfma_f32_16x16x32_bf16 v[48:51], a[40:43], v[96:99], v[48:51]\n"
"s_cbranch_vccnz .Lmk_p83s\n"
".Lmk_p83r:\n"
"ds_read_b128 v[144:147], v9 offset:128\n"
"v_mfma_f32_16x16x32_bf16 v[48:51], a[36:39], v[88:91], v[48:51]\n"
"ds_write_b128 v3, v[44:47] offset:12288\n"
"v_mfma_f32_16x16x32_bf16 v[48:51], a[32:35], v[84:87], v[48:51]\n"
"buffer_load_dwordx4 v[44:47], v0, s[4:7], s98 offen offset:1024 sc1\n"
"v_mfma_f32_16x16x32_bf16 v[152:155], a[64:67], v[84:87], 0\n"
"buffer_load_dwordx4 a[148:151], v1, s[4:7], s96 offen offset:1024 sc1\n"
"v_mfma_f32_16x16x32_bf16 v[152:155], a[68:71], v[88:91], v[152:155]\n"
"v_rcp_f32_e32 v255, v253\n"
"v_mfma_f32_16x16x32_bf16 v[152:155], a[72:75], v[96:99], v[152:155]\n"
"ds_read_b128 v[156:159], v5\n"
"v_mul_f32_e32 v253, 0x3e8293ee, v255\n"
"v_mfma_f32_16x16x32_bf16 v[152:155], a[76:79], v[116:119], v[152:155]\n"
"ds_read_b128 v[176:179], v4 offset:1024\n"
"ds_read_b128 v[184:187], v9 offset:192\n"
"v_mfma_f32_16x16x32_bf16 v[152:155], a[80:83], v[92:95], v[152:155]\n"
"buffer_load_dwordx4 v[188:191], v0, s[4:7], s98 offen offset:2048 sc1\n"
"v_mfma_f32_16x16x32_bf16 v[152:155], a[84:87], v[172:175], v[152:155]\n"
"ds_write_b128 v3, v[48:51] offset:13312\n"
"v_mfma_f32_16x16x32_bf16 v[152:155], a[88:91], v[80:83], v[152:155]\n"
"ds_read_b128 v[48:51], v5 offset:1024\n"
"v_mfma_f32_16x16x32_bf16 v[152:155], a[92:95], v[160:163], v[152:155]\n"
"s_waitcnt lgkmcnt(4)\n"
"v_fmac_f32_e32 v104, v254, v144\n"
"v_fmac_f32_e32 v105, v254, v145\n"
"v_mfma_f32_16x16x32_bf16 v[192:195], a[124:127], v[160:163], 0\n"
"v_fmac_f32_e32 v106, v254, v146\n"
"v_fmac_f32_e32 v107, v254, v147\n"
"v_mfma_f32_16x16x32_bf16 v[192:195], a[120:123], v[80:83], v[192:195]\n"
"buffer_load_dwordx4 v[144:147], v0, s[4:7], s98 offen offset:3072 sc1\n"
"v_mfma_f32_16x16x32_bf16 v[192:195], a[116:119], v[172:175], v[192:195]\n"
"ds_read_b128 v[196:199], v4 offset:2048\n"
"ds_read_b128 v[200:203], v9 offset:256\n"
"v_mfma_f32_16x16x32_bf16 v[192:195], a[112:115], v[92:95], v[192:195]\n"
"ds_read_b128 v[204:207], v5 offset:2048\n"
"v_add_f32_e32 v104, v104, v156\n"
"v_mfma_f32_16x16x32_bf16 v[192:195], a[108:111], v[116:119], v[192:195]\n"
"v_add_f32_e32 v105, v105, v157\n"
"v_add_f32_e32 v106, v106, v158\n"
"v_mfma_f32_16x16x32_bf16 v[192:195], a[104:107], v[96:99], v[192:195]\n"
"ds_write_b128 v3, v[152:155] offset:14336\n"
"v_mfma_f32_16x16x32_bf16 v[192:195], a[100:103], v[88:91], v[192:195]\n"
"buffer_load_dwordx4 v[152:155], v1, s[4:7], s98 offen sc1\n"
"v_mfma_f32_16x16x32_bf16 v[192:195], a[96:99], v[84:87], v[192:195]\n"
"s_waitcnt lgkmcnt(4)\n"
"v_add_f32_e32 v107, v107, v159\n"
"v_fmac_f32_e32 v176, v254, v184\n"
"v_mfma_f32_16x16x32_bf16 v[156:159], v[28:31], v[84:87], 0\n"
"v_fmac_f32_e32 v177, v254, v185\n"
"v_fmac_f32_e32 v178, v254, v186\n"
"v_mfma_f32_16x16x32_bf16 v[156:159], v[40:43], v[88:91], v[156:159]\n"
"ds_read_b128 v[208:211], v4 offset:3072\n"
"ds_read_b128 v[212:215], v9 offset:320\n"
"v_mfma_f32_16x16x32_bf16 v[156:159], v[24:27], v[96:99], v[156:159]\n"
"ds_read_b128 v[216:219], v5 offset:3072\n"
"v_fmac_f32_e32 v179, v254, v187\n"
"v_mfma_f32_16x16x32_bf16 v[156:159], v[72:75], v[116:119], v[156:159]\n"
"buffer_load_dwordx4 v[184:187], v1, s[4:7], s98 offen offset:1024 sc1\n"
"v_mfma_f32_16x16x32_bf16 v[156:159], v[32:35], v[92:95], v[156:159]\n"
"buffer_load_dwordx4 a[152:155], v1, s[4:7], s96 offen offset:2048 sc1\n"
"v_mfma_f32_16x16x32_bf16 v[156:159], v[64:67], v[172:175], v[156:159]\n"
"ds_write_b128 v3, v[192:195] offset:15360\n"
"v_mfma_f32_16x16x32_bf16 v[156:159], v[60:63], v[80:83], v[156:159]\n"
"v_add_f32_e32 v176, v176, v48\n"
"v_add_f32_e32 v177, v177, v49\n"
"v_mfma_f32_16x16x32_bf16 v[156:159], v[36:39], v[160:163], v[156:159]\n"
"s_waitcnt lgkmcnt(5)\n"
"v_add_f32_e32 v178, v178, v50\n"
"v_add_f32_e32 v179, v179, v51\n"
"v_mfma_f32_16x16x32_bf16 v[48:51], v[100:103], v[160:163], 0\n"
"buffer_load_dwordx4 v[160:163], v1, s[4:7], s98 offen offset:2048 sc1\n"
"v_mfma_f32_16x16x32_bf16 v[48:51], v[76:79], v[80:83], v[48:51]\n"
"ds_read_b128 v[80:83], v4 offset:4096\n"
"ds_read_b128 v[192:195], v9\n"
"v_mfma_f32_16x16x32_bf16 v[48:51], v[140:143], v[172:175], v[48:51]\n"
"ds_read_b128 v[172:175], v5 offset:4096\n"
"v_fmac_f32_e32 v196, v254, v200\n"
"v_mfma_f32_16x16x32_bf16 v[48:51], v[136:139], v[92:95], v[48:51]\n"
"v_fmac_f32_e32 v197, v254, v201\n"
"v_fmac_f32_e32 v198, v254, v202\n"
"v_mfma_f32_16x16x32_bf16 v[48:51], v[56:59], v[116:119], v[48:51]\n"
"v_fmac_f32_e32 v199, v254, v203\n"
"v_add_f32_e32 v196, v196, v204\n"
"v_mfma_f32_16x16x32_bf16 v[48:51], v[112:115], v[96:99], v[48:51]\n"
"buffer_load_dwordx4 v[92:95], v1, s[4:7], s98 offen offset:3072 sc1\n"
"v_mfma_f32_16x16x32_bf16 v[48:51], v[108:111], v[88:91], v[48:51]\n"
"ds_write_b128 v3, v[156:159] offset:16384\n"
"v_mfma_f32_16x16x32_bf16 v[48:51], v[120:123], v[84:87], v[48:51]\n"
"s_waitcnt vmcnt(18)\n"
"ds_read_b128 v[84:87], v4 offset:5120\n"
"ds_read_b128 v[88:91], v9 offset:64\n"
"; @A G1 1.3\n"
"v_mfma_f32_16x16x32_bf16 v[96:99], a[0:3], v[68:71], v[104:107]\n"
"ds_read_b128 v[116:119], v5 offset:5120\n"
"v_add_f32_e32 v197, v197, v205\n"
"v_mfma_f32_16x16x32_bf16 v[96:99], a[4:7], v[124:127], v[96:99]\n"
"s_waitcnt lgkmcnt(8)\n"
"v_add_f32_e32 v198, v198, v206\n"
"v_add_f32_e32 v199, v199, v207\n"
"v_mfma_f32_16x16x32_bf16 v[96:99], a[8:11], v[132:135], v[96:99]\n"
"s_add_u32 s98, s53, 0x8000\n"
"buffer_load_dwordx4 v[156:159], v0, s[4:7], s98 offen sc1\n"
"v_mfma_f32_16x16x32_bf16 v[96:99], a[12:15], v[148:151], v[96:99]\n"
"s_waitcnt vmcnt(13)\n"
"v_fmac_f32_e32 v208, v254, v212\n"
"v_fmac_f32_e32 v209, v254, v213\n"
"v_mfma_f32_16x16x32_bf16 v[96:99], a[16:19], v[128:131], v[96:99]\n"
"v_fmac_f32_e32 v210, v254, v214\n"
"v_fmac_f32_e32 v211, v254, v215\n"
"v_mfma_f32_16x16x32_bf16 v[96:99], a[20:23], v[164:167], v[96:99]\n"
"ds_write_b128 v3, v[48:51] offset:17408\n"
"v_mfma_f32_16x16x32_bf16 v[96:99], a[24:27], v[168:171], v[96:99]\n"
"v_add_f32_e32 v208, v208, v216\n"
"v_add_f32_e32 v209, v209, v217\n"
"v_mfma_f32_16x16x32_bf16 v[96:99], a[28:31], v[180:183], v[96:99]\n"
"buffer_load_dwordx4 v[48:51], v0, s[4:7], s98 offen offset:1024 sc1\n"
"v_mfma_f32_16x16x32_bf16 v[200:203], a[60:63], v[180:183], v[176:179]\n"
"buffer_load_dwordx4 a[156:159], v1, s[4:7], s96 offen offset:3072 sc1\n"
"v_mfma_f32_16x16x32_bf16 v[200:203], a[56:59], v[168:171], v[200:203]\n"
"s_waitcnt lgkmcnt(5)\n"
"v_add_f32_e32 v210, v210, v218\n"
"v_add_f32_e32 v211, v211, v219\n"
"v_mfma_f32_16x16x32_bf16 v[200:203], a[52:55], v[164:167], v[200:203]\n"
"s_waitcnt lgkmcnt(0)\n"
"s_barrier\n"
"v_mfma_f32_16x16x32_bf16 v[200:203], a[48:51], v[128:131], v[200:203]\n"
"ds_read_b128 v[204:207], v6\n"
"ds_read_b128 v[212:215], v6 offset:1024\n"
"v_mfma_f32_16x16x32_bf16 v[200:203], a[44:47], v[148:151], v[200:203]\n"
"v_cmp_u_f32_e32 vcc, v96, v96\n"
"v_fmac_f32_e32 v80, v254, v192\n"
"v_mfma_f32_16x16x32_bf16 v[200:203], a[40:43], v[132:135], v[200:203]\n"
"s_cbranch_vccnz .Lmk_p84s\n"
".Lmk_p84r:\n"
"v_fmac_f32_e32 v81, v254, v193\n"
"v_mfma_f32_16x16x32_bf16 v[200:203], a[36:39], v[124:127], v[200:203]\n"
"buffer_load_dwordx4 v[104:107], v0, s[4:7], s98 offen offset:2048 sc1\n"
"v_mfma_f32_16x16x32_bf16 v[200:203], a[32:35], v[68:71], v[200:203]\n"
"v_fmac_f32_e32 v82, v254, v194\n"
"v_fmac_f32_e32 v83, v254, v195\n"
"v_mfma_f32_16x16x32_bf16 v[196:199], a[64:67], v[68:71], v[196:199]\n"
"v_add_f32_e32 v80, v80, v172\n"
"v_add_f32_e32 v81, v81, v173\n"
"v_mfma_f32_16x16x32_bf16 v[196:199], a[68:71], v[124:127], v[196:199]\n"
"buffer_load_dwordx4 v[176:179], v0, s[4:7], s98 offen offset:3072 sc1\n"
"v_mfma_f32_16x16x32_bf16 v[196:199], a[72:75], v[132:135], v[196:199]\n"
"v_add_f32_e32 v82, v82, v174\n"
"v_add_f32_e32 v83, v83, v175\n"
"v_mfma_f32_16x16x32_bf16 v[196:199], a[76:79], v[148:151], v[196:199]\n"
"v_fmac_f32_e32 v84, v254, v88\n"
"v_fmac_f32_e32 v85, v254, v89\n"
"v_mfma_f32_16x16x32_bf16 v[196:199], a[80:83], v[128:131], v[196:199]\n"
"s_waitcnt lgkmcnt(0)\n"
"v_fmac_f32_e32 v86, v254, v90\n"
"v_fmac_f32_e32 v87, v254, v91\n"
"v_mfma_f32_16x16x32_bf16 v[196:199], a[84:87], v[164:167], v[196:199]\n"
"v_add_f32_e32 v84, v84, v116\n"
"v_add_f32_e32 v85, v85, v117\n"
"v_mfma_f32_16x16x32_bf16 v[196:199], a[88:91], v[168:171], v[196:199]\n"
"buffer_load_dwordx4 v[88:91], v1, s[4:7], s98 offen sc1\n"
"v_mfma_f32_16x16x32_bf16 v[196:199], a[92:95], v[180:183], v[196:199]\n"
"ds_read_b128 v[172:175], v6 offset:2048\n"
"ds_read_b128 v[192:195], v6 offset:3072\n"
"v_mfma_f32_16x16x32_bf16 v[208:211], a[124:127], v[180:183], v[208:211]\n"
"v_add_f32_e32 v86, v86, v118\n"
"v_add_f32_e32 v87, v87, v119\n"
"v_mfma_f32_16x16x32_bf16 v[208:211], a[120:123], v[168:171], v[208:211]\n"
"v_add_f32_e32 v96, v96, v204\n"
"v_add_f32_e32 v97, v97, v205\n"
"v_mfma_f32_16x16x32_bf16 v[208:211], a[116:119], v[164:167], v[208:211]\n"
"s_add_u32 s97, s70, 0x2000\n"
"v_add_f32_e32 v98, v98, v206\n"
"v_add_f32_e32 v99, v99, v207\n"
"v_mfma_f32_16x16x32_bf16 v[208:211], a[112:115], v[128:131], v[208:211]\n"
"buffer_load_dwordx4 v[116:119], v1, s[4:7], s98 offen offset:1024 sc1\n"
"v_mfma_f32_16x16x32_bf16 v[208:211], a[108:111], v[148:151], v[208:211]\n"
"buffer_load_dwordx4 a[160:163], v0, s[4:7], s97 offen sc1\n"
"v_mfma_f32_16x16x32_bf16 v[208:211], a[104:107], v[132:135], v[208:211]\n"
"v_add_f32_e32 v200, v200, v212\n"
"v_add_f32_e32 v201, v201, v213\n"
"v_mfma_f32_16x16x32_bf16 v[208:211], a[100:103], v[124:127], v[208:211]\n"
"v_add_f32_e32 v202, v202, v214\n"
"v_add_f32_e32 v203, v203, v215\n"
"v_mfma_f32_16x16x32_bf16 v[208:211], a[96:99], v[68:71], v[208:211]\n"
"v_mul_f32_e32 v96, v96, v255\n"
"v_mul_f32_e32 v97, v97, v255\n"
"v_mfma_f32_16x16x32_bf16 v[80:83], v[28:31], v[68:71], v[80:83]\n"
"buffer_load_dwordx4 v[204:207], v1, s[4:7], s98 offen offset:2048 sc1\n"
"v_mfma_f32_16x16x32_bf16 v[80:83], v[40:43], v[124:127], v[80:83]\n"
"v_mul_f32_e32 v98, v98, v255\n"
"v_mul_f32_e32 v99, v99, v255\n"
"v_mfma_f32_16x16x32_bf16 v[80:83], v[24:27], v[132:135], v[80:83]\n"
"v_mul_f32_e32 v200, v200, v255\n"
"v_mul_f32_e32 v201, v201, v255\n"
"v_mfma_f32_16x16x32_bf16 v[80:83], v[72:75], v[148:151], v[80:83]\n"
"v_mul_f32_e32 v202, v202, v255\n"
"v_mul_f32_e32 v203, v203, v255\n"
"v_mfma_f32_16x16x32_bf16 v[80:83], v[32:35], v[128:131], v[80:83]\n"
"s_waitcnt lgkmcnt(0)\n"
"v_cvt_pk_bf16_f32 v212, v96, v97\n"
"v_cvt_pk_bf16_f32 v213, v98, v99\n"
"v_mfma_f32_16x16x32_bf16 v[80:83], v[64:67], v[164:167], v[80:83]\n"
"buffer_load_dwordx4 v[96:99], v1, s[4:7], s98 offen offset:3072 sc1\n"
"v_mfma_f32_16x16x32_bf16 v[80:83], v[60:63], v[168:171], v[80:83]\n"
"ds_read_b128 v[216:219], v6 offset:4096\n"
"ds_read_b128 v[220:223], v6 offset:5120\n"
"v_mfma_f32_16x16x32_bf16 v[80:83], v[36:39], v[180:183], v[80:83]\n"
"v_cvt_pk_bf16_f32 v214, v200, v201\n"
"v_cvt_pk_bf16_f32 v215, v202, v203\n"
"v_mfma_f32_16x16x32_bf16 v[84:87], v[100:103], v[180:183], v[84:87]\n"
"ds_write_b128 v8, v[212:215] offset:25600\n"
"v_mfma_f32_16x16x32_bf16 v[84:87], v[76:79], v[168:171], v[84:87]\n"
"v_add_f32_e32 v196, v196, v172\n"
"v_add_f32_e32 v197, v197, v173\n"
"v_mfma_f32_16x16x32_bf16 v[84:87], v[140:143], v[164:167], v[84:87]\n"
"v_add_f32_e32 v198, v198, v174\n"
"v_add_f32_e32 v199, v199, v175\n"
"v_mfma_f32_16x16x32_bf16 v[84:87], v[136:139], v[128:131], v[84:87]\n"
"v_add_f32_e32 v208, v208, v192\n"
"v_add_f32_e32 v209, v209, v193\n"
"v_mfma_f32_16x16x32_bf16 v[84:87], v[56:59], v[148:151], v[84:87]\n"
"v_add_f32_e32 v210, v210, v194\n"
"v_add_f32_e32 v211, v211, v195\n"
"v_mfma_f32_16x16x32_bf16 v[84:87], v[112:115], v[132:135], v[84:87]\n"
"v_mul_f32_e32 v196, v196, v255\n"
"v_mul_f32_e32 v197, v197, v255\n"
"v_mfma_f32_16x16x32_bf16 v[84:87], v[108:111], v[124:127], v[84:87]\n"
"v_mul_f32_e32 v198, v198, v255\n"
"v_mul_f32_e32 v199, v199, v255\n"
"v_mfma_f32_16x16x32_bf16 v[84:87], v[120:123], v[68:71], v[84:87]\n"
"v_mul_f32_e32 v208, v208, v255\n"
"v_mul_f32_e32 v209, v209, v255\n"
"v_mul_f32_e32 v210, v210, v255\n"
"v_mul_f32_e32 v211, v211, v255\n"
"v_cvt_pk_bf16_f32 v68, v196, v197\n"
"v_cvt_pk_bf16_f32 v69, v198, v199\n"
"v_cvt_pk_bf16_f32 v70, v208, v209\n"
"v_cvt_pk_bf16_f32 v71, v210, v211\n"
"ds_write_b128 v7, v[68:71] offset:58368\n"
".Lmk_a2:\n"
"s_waitcnt vmcnt(15)\n"
"; @A G1 2.0\n"
"v_mfma_f32_16x16x32_bf16 v[124:127], a[0:3], v[52:55], 0\n"
"v_mfma_f32_16x16x32_bf16 v[124:127], a[4:7], v[44:47], v[124:127]\n"
"s_add_u32 s98, s62, 0x800\n"
"v_mfma_f32_16x16x32_bf16 v[124:127], a[8:11], v[188:191], v[124:127]\n"
"s_add_u32 s96, s54, 0x8000\n"
"buffer_load_dwordx4 v[128:131], v0, s[4:7], s96 offen sc1\n"
"v_mfma_f32_16x16x32_bf16 v[124:127], a[12:15], v[144:147], v[124:127]\n"
"s_waitcnt vmcnt(11)\n"
"v_mfma_f32_16x16x32_bf16 v[124:127], a[16:19], v[152:155], v[124:127]\n"
"buffer_load_dword v255, v10, s[4:7], s98 offen sc1\n"
"v_mfma_f32_16x16x32_bf16 v[124:127], a[20:23], v[184:187], v[124:127]\n"
"s_waitcnt lgkmcnt(2)\n"
"v_add_f32_e32 v80, v80, v216\n"
"v_add_f32_e32 v81, v81, v217\n"
"v_mfma_f32_16x16x32_bf16 v[124:127], a[24:27], v[160:163], v[124:127]\n"
"v_add_f32_e32 v82, v82, v218\n"
"v_add_f32_e32 v83, v83, v219\n"
"v_mfma_f32_16x16x32_bf16 v[124:127], a[28:31], v[92:95], v[124:127]\n"
"buffer_load_dwordx4 v[132:135], v0, s[4:7], s96 offen offset:1024 sc1\n"
"v_mfma_f32_16x16x32_bf16 v[148:151], a[60:63], v[92:95], 0\n"
"buffer_load_dwordx4 a[164:167], v0, s[4:7], s97 offen offset:1024 sc1\n"
"v_mfma_f32_16x16x32_bf16 v[148:151], a[56:59], v[160:163], v[148:151]\n"
"buffer_load_dword v254, v10, s[4:7], s98 offen offset:256 sc1\n"
"v_mfma_f32_16x16x32_bf16 v[148:151], a[52:55], v[184:187], v[148:151]\n"
"v_add_f32_e32 v84, v84, v220\n"
"v_add_f32_e32 v85, v85, v221\n"
"v_mfma_f32_16x16x32_bf16 v[148:151], a[48:51], v[152:155], v[148:151]\n"
"v_add_f32_e32 v86, v86, v222\n"
"v_add_f32_e32 v87, v87, v223\n"
"v_mfma_f32_16x16x32_bf16 v[148:151], a[44:47], v[144:147], v[148:151]\n"
"buffer_load_dwordx4 v[164:167], v0, s[4:7], s96 offen offset:2048 sc1\n"
"v_mfma_f32_16x16x32_bf16 v[148:151], a[40:43], v[188:191], v[148:151]\n"
"ds_write_b128 v3, v[124:127]\n"
"v_mfma_f32_16x16x32_bf16 v[148:151], a[36:39], v[44:47], v[148:151]\n"
"buffer_load_dword v252, v10, s[4:7], s98 offen offset:512 sc1\n"
"v_mfma_f32_16x16x32_bf16 v[148:151], a[32:35], v[52:55], v[148:151]\n"
"v_mul_f32_e32 v80, v80, v253\n"
"v_mul_f32_e32 v81, v81, v253\n"
"v_mfma_f32_16x16x32_bf16 v[124:127], a[64:67], v[52:55], 0\n"
"v_mul_f32_e32 v82, v82, v253\n"
"v_mul_f32_e32 v83, v83, v253\n"
"v_mfma_f32_16x16x32_bf16 v[124:127], a[68:71], v[44:47], v[124:127]\n"
"buffer_load_dwordx4 v[168:171], v0, s[4:7], s96 offen offset:3072 sc1\n"
"v_mfma_f32_16x16x32_bf16 v[124:127], a[72:75], v[188:191], v[124:127]\n"
"buffer_load_dword v251, v10, s[4:7], s98 offen offset:768 sc1\n"
"v_mfma_f32_16x16x32_bf16 v[124:127], a[76:79], v[144:147], v[124:127]\n"
"v_mul_f32_e32 v84, v84, v253\n"
"v_mul_f32_e32 v85, v85, v253\n"
"v_mfma_f32_16x16x32_bf16 v[124:127], a[80:83], v[152:155], v[124:127]\n"
"v_mul_f32_e32 v86, v86, v253\n"
"v_mul_f32_e32 v87, v87, v253\n"
"v_mfma_f32_16x16x32_bf16 v[124:127], a[84:87], v[184:187], v[124:127]\n"
"ds_write_b128 v3, v[148:151] offset:1024\n"
"v_mfma_f32_16x16x32_bf16 v[124:127], a[88:91], v[160:163], v[124:127]\n"
"buffer_load_dwordx4 v[148:151], v1, s[4:7], s96 offen sc1\n"
"v_mfma_f32_16x16x32_bf16 v[124:127], a[92:95], v[92:95], v[124:127]\n"
"buffer_load_dword v253, v10, s[4:7], s98 offen offset:1024 sc1\n"
"v_mfma_f32_16x16x32_bf16 v[172:175], a[124:127], v[92:95], 0\n"
"v_cvt_pk_bf16_f32 v180, v80, v81\n"
"v_cvt_pk_bf16_f32 v181, v82, v83\n"
"v_mfma_f32_16x16x32_bf16 v[172:175], a[120:123], v[160:163], v[172:175]\n"
"v_cvt_pk_bf16_f32 v182, v84, v85\n"
"v_cvt_pk_bf16_f32 v183, v86, v87\n"
"v_mfma_f32_16x16x32_bf16 v[172:175], a[116:119], v[184:187], v[172:175]\n"
"buffer_load_dword v250, v10, s[4:7], s98 offen offset:1280 sc1\n"
"v_mfma_f32_16x16x32_bf16 v[172:175], a[112:115], v[152:155], v[172:175]\n"
"buffer_load_dwordx4 v[80:83], v1, s[4:7], s96 offen offset:1024 sc1\n"
"v_mfma_f32_16x16x32_bf16 v[172:175], a[108:111], v[144:147], v[172:175]\n"
"buffer_load_dwordx4 a[168:171], v0, s[4:7], s97 offen offset:2048 sc1\n"
"v_mfma_f32_16x16x32_bf16 v[172:175], a[104:107], v[188:191], v[172:175]\n"
"ds_write_b128 v3, v[124:127] offset:2048\n"
"v_mfma_f32_16x16x32_bf16 v[172:175], a[100:103], v[44:47], v[172:175]\n"
"buffer_load_dword v249, v10, s[4:7], s98 offen offset:1536 sc1\n"
"v_mfma_f32_16x16x32_bf16 v[172:175], a[96:99], v[52:55], v[172:175]\n"
"ds_write_b128 v2, v[180:183] offset:50176\n"
"v_mfma_f32_16x16x32_bf16 v[84:87], v[28:31], v[52:55], 0\n"
"buffer_load_dwordx4 v[124:127], v1, s[4:7], s96 offen offset:2048 sc1\n"
"s_add_u32 s95, s64, 0x2000\n"
"v_mfma_f32_16x16x32_bf16 v[84:87], v[40:43], v[44:47], v[84:87]\n"
"buffer_load_dword v248, v10, s[4:7], s98 offen offset:1792 sc1\n"
"v_mfma_f32_16x16x32_bf16 v[84:87], v[24:27], v[188:191], v[84:87]\n"
"buffer_store_dwordx4 v[212:215], v8, s[4:7], s95 offen offset:1024 sc1\n"
"v_mfma_f32_16x16x32_bf16 v[84:87], v[72:75], v[144:147], v[84:87]\n"
"v_mfma_f32_16x16x32_bf16 v[84:87], v[32:35], v[152:155], v[84:87]\n"
"v_mfma_f32_16x16x32_bf16 v[84:87], v[64:67], v[184:187], v[84:87]\n"
"buffer_load_dwordx4 v[180:183], v1, s[4:7], s96 offen offset:3072 sc1\n"
"v_mfma_f32_16x16x32_bf16 v[84:87], v[60:63], v[160:163], v[84:87]\n"
"ds_write_b128 v3, v[172:175] offset:3072\n"
"v_mfma_f32_16x16x32_bf16 v[84:87], v[36:39], v[92:95], v[84:87]\n"
"v_mfma_f32_16x16x32_bf16 v[172:175], v[100:103], v[92:95], 0\n"
"v_mfma_f32_16x16x32_bf16 v[172:175], v[76:79], v[160:163], v[172:175]\n"
"v_mfma_f32_16x16x32_bf16 v[172:175], v[140:143], v[184:187], v[172:175]\n"
"v_mfma_f32_16x16x32_bf16 v[172:175], v[136:139], v[152:155], v[172:175]\n"
"v_mfma_f32_16x16x32_bf16 v[172:175], v[56:59], v[144:147], v[172:175]\n"
"v_mfma_f32_16x16x32_bf16 v[172:175], v[112:115], v[188:191], v[172:175]\n"
"s_nop 1\n"
"ds_write_b128 v3, v[84:87] offset:4096\n"
"v_mfma_f32_16x16x32_bf16 v[172:175], v[108:111], v[44:47], v[172:175]\n"
"v_mfma_f32_16x16x32_bf16 v[172:175], v[120:123], v[52:55], v[172:175]\n"
"s_waitcnt vmcnt(24)\n"
"; @A G1 2.1\n"
"v_mfma_f32_16x16x32_bf16 v[44:47], a[0:3], v[156:159], 0\n"
"buffer_load_dwordx4 a[172:175], v0, s[4:7], s97 offen offset:3072 sc1\n"
"v_mfma_f32_16x16x32_bf16 v[44:47], a[4:7], v[48:51], v[44:47]\n"
"v_mfma_f32_16x16x32_bf16 v[44:47], a[8:11], v[104:107], v[44:47]\n"
"s_add_u32 s98, s55, 0x8000\n"
"buffer_load_dwordx4 v[52:55], v0, s[4:7], s98 offen sc1\n"
"v_mfma_f32_16x16x32_bf16 v[44:47], a[12:15], v[176:179], v[44:47]\n"
"s_waitcnt vmcnt(21)\n"
"v_mfma_f32_16x16x32_bf16 v[44:47], a[16:19], v[88:91], v[44:47]\n"
"v_mfma_f32_16x16x32_bf16 v[44:47], a[20:23], v[116:119], v[44:47]\n"
"ds_write_b128 v3, v[172:175] offset:5120\n"
"v_mfma_f32_16x16x32_bf16 v[44:47], a[24:27], v[204:207], v[44:47]\n"
"v_mfma_f32_16x16x32_bf16 v[44:47], a[28:31], v[96:99], v[44:47]\n"
"buffer_load_dwordx4 v[84:87], v0, s[4:7], s98 offen offset:1024 sc1\n"
"v_mfma_f32_16x16x32_bf16 v[92:95], a[60:63], v[96:99], 0\n"
"s_add_u32 s96, s64, 0x6000\n"
"v_mfma_f32_16x16x32_bf16 v[92:95], a[56:59], v[204:207], v[92:95]\n"
"v_mfma_f32_16x16x32_bf16 v[92:95], a[52:55], v[116:119], v[92:95]\n"
"buffer_store_dwordx4 v[68:71], v7, s[4:7], s96 offen offset:1024 sc1\n"
"v_mfma_f32_16x16x32_bf16 v[92:95], a[48:51], v[88:91], v[92:95]\n"
"v_mfma_f32_16x16x32_bf16 v[92:95], a[44:47], v[176:179], v[92:95]\n"
"buffer_load_dwordx4 v[68:71], v0, s[4:7], s98 offen offset:2048 sc1\n"
"v_mfma_f32_16x16x32_bf16 v[92:95], a[40:43], v[104:107], v[92:95]\n"
"ds_write_b128 v3, v[44:47] offset:6144\n"
"v_mfma_f32_16x16x32_bf16 v[92:95], a[36:39], v[48:51], v[92:95]\n"
"v_mfma_f32_16x16x32_bf16 v[92:95], a[32:35], v[156:159], v[92:95]\n"
"v_mfma_f32_16x16x32_bf16 v[44:47], a[64:67], v[156:159], 0\n"
"v_mfma_f32_16x16x32_bf16 v[44:47], a[68:71], v[48:51], v[44:47]\n"
"buffer_load_dwordx4 v[144:147], v0, s[4:7], s98 offen offset:3072 sc1\n"
"v_mfma_f32_16x16x32_bf16 v[44:47], a[72:75], v[104:107], v[44:47]\n"
"v_mfma_f32_16x16x32_bf16 v[44:47], a[76:79], v[176:179], v[44:47]\n"
"v_mfma_f32_16x16x32_bf16 v[44:47], a[80:83], v[88:91], v[44:47]\n"
"buffer_load_dwordx4 a[176:179], v1, s[4:7], s97 offen sc1\n"
"v_mfma_f32_16x16x32_bf16 v[44:47], a[84:87], v[116:119], v[44:47]\n"
"ds_write_b128 v3, v[92:95] offset:7168\n"
"v_mfma_f32_16x16x32_bf16 v[44:47], a[88:91], v[204:207], v[44:47]\n"
"buffer_load_dwordx4 v[92:95], v1, s[4:7], s98 offen sc1\n"
"v_mfma_f32_16x16x32_bf16 v[44:47], a[92:95], v[96:99], v[44:47]\n"
"v_mfma_f32_16x16x32_bf16 v[152:155], a[124:127], v[96:99], 0\n"
"v_mfma_f32_16x16x32_bf16 v[152:155], a[120:123], v[204:207], v[152:155]\n"
"v_mfma_f32_16x16x32_bf16 v[152:155], a[116:119], v[116:119], v[152:155]\n"
"v_mfma_f32_16x16x32_bf16 v[152:155], a[112:115], v[88:91], v[152:155]\n"
"buffer_load_dwordx4 v[160:163], v1, s[4:7], s98 offen offset:1024 sc1\n"
"v_mfma_f32_16x16x32_bf16 v[152:155], a[108:111], v[176:179], v[152:155]\n"
"v_mfma_f32_16x16x32_bf16 v[152:155], a[104:107], v[104:107], v[152:155]\n"
"s_nop 0\n"
"ds_write_b128 v3, v[44:47] offset:8192\n"
"v_mfma_f32_16x16x32_bf16 v[152:155], a[100:103], v[48:51], v[152:155]\n"
"v_mfma_f32_16x16x32_bf16 v[152:155], a[96:99], v[156:159], v[152:155]\n"
"v_mfma_f32_16x16x32_bf16 v[44:47], v[28:31], v[156:159], 0\n"
"buffer_load_dwordx4 v[172:175], v1, s[4:7], s98 offen offset:2048 sc1\n"
"v_mfma_f32_16x16x32_bf16 v[44:47], v[40:43], v[48:51], v[44:47]\n"
"v_mfma_f32_16x16x32_bf16 v[44:47], v[24:27], v[104:107], v[44:47]\n"
"v_mfma_f32_16x16x32_bf16 v[44:47], v[72:75], v[176:179], v[44:47]\n"
"v_mfma_f32_16x16x32_bf16 v[44:47], v[32:35], v[88:91], v[44:47]\n"
"s_waitcnt vmcnt(12)\n"
"v_add_f32_e32 v255, v255, v253\n"
"v_add_f32_e32 v254, v254, v250\n"
"v_mfma_f32_16x16x32_bf16 v[44:47], v[64:67], v[116:119], v[44:47]\n"
"buffer_load_dwordx4 v[184:187], v1, s[4:7], s98 offen offset:3072 sc1\n"
"v_mfma_f32_16x16x32_bf16 v[44:47], v[60:63], v[204:207], v[44:47]\n"
"ds_write_b128 v3, v[152:155] offset:9216\n"
"v_mfma_f32_16x16x32_bf16 v[44:47], v[36:39], v[96:99], v[44:47]\n"
"v_add_f32_e32 v252, v252, v249\n"
"v_add_f32_e32 v251, v251, v248\n"
"v_mfma_f32_16x16x32_bf16 v[152:155], v[100:103], v[96:99], 0\n"
"buffer_load_dwordx4 a[180:183], v1, s[4:7], s97 offen offset:1024 sc1\n"
"v_mfma_f32_16x16x32_bf16 v[152:155], v[76:79], v[204:207], v[152:155]\n"
"v_add_f32_e32 v255, v255, v252\n"
"v_add_f32_e32 v254, v254, v251\n"
"v_mfma_f32_16x16x32_bf16 v[152:155], v[140:143], v[116:119], v[152:155]\n"
"v_add_f32_e32 v255, v255, v254\n"
"v_mov_b32_e32 v254, v255\n"
"v_mfma_f32_16x16x32_bf16 v[152:155], v[136:139], v[88:91], v[152:155]\n"
"s_nop 0\n"
"v_permlane32_swap_b32_e32 v255, v254\n"
"v_add_f32_e32 v255, v255, v254\n"
"v_mfma_f32_16x16x32_bf16 v[152:155], v[56:59], v[176:179], v[152:155]\n"
"v_mul_f32_e32 v255, 0x3a800000, v255\n"
"v_mov_b32_e32 v254, v255\n"
"v_mfma_f32_16x16x32_bf16 v[152:155], v[112:115], v[104:107], v[152:155]\n"
"ds_write_b128 v3, v[44:47] offset:10240\n"
"v_mfma_f32_16x16x32_bf16 v[152:155], v[108:111], v[48:51], v[152:155]\n"
"v_permlane16_swap_b32_e32 v255, v254\n"
"v_cvt_pk_bf16_f32 v253, -v255, 0\n"
"v_mfma_f32_16x16x32_bf16 v[152:155], v[120:123], v[156:159], v[152:155]\n"
"v_fma_f32 v254, -v255, v255, v254\n"
"v_lshlrev_b32_e32 v253, 16, v253\n"
"; @A G1 2.2\n"
"v_mfma_f32_16x16x32_bf16 v[44:47], a[0:3], v[128:131], 0\n"
"v_max_f32_e32 v254, 0, v254\n"
"v_cvt_pk_bf16_f32 v254, v254, 0\n"
"v_mfma_f32_16x16x32_bf16 v[44:47], a[4:7], v[132:135], v[44:47]\n"
"v_lshlrev_b32_e32 v254, 16, v254\n"
"v_add_f32_e32 v254, 0x3727c5ac, v254\n"
"v_mfma_f32_16x16x32_bf16 v[44:47], a[8:11], v[164:167], v[44:47]\n"
"v_cvt_pk_bf16_f32 v254, v254, 0\n"
"v_lshlrev_b32_e32 v254, 16, v254\n"
"v_mfma_f32_16x16x32_bf16 v[44:47], a[12:15], v[168:171], v[44:47]\n"
"s_waitcnt vmcnt(12)\n"
"v_sqrt_f32_e32 v254, v254\n"
"v_mfma_f32_16x16x32_bf16 v[44:47], a[16:19], v[148:151], v[44:47]\n"
"s_mov_b32 s98, .Lmk_a4-.Lmk_g86\n"
"s_getpc_b64 s[84:85]\n"
".Lmk_g86:\n"
"s_add_u32 s84, s84, s98\n"
"s_addc_u32 s85, s85, 0\n"
"s_and_b32 s84, s84, -16\n"
"v_mfma_f32_16x16x32_bf16 v[44:47], a[20:23], v[80:83], v[44:47]\n"
"ds_write_b128 v3, v[152:155] offset:11264\n"
"v_mfma_f32_16x16x32_bf16 v[44:47], a[24:27], v[124:127], v[44:47]\n"
"s_and_b32 s85, s85, 0xffff\n"
"s_sub_u32 s98, .Lmk_end-.Lmk_g86, s98\n"
"s_min_u32 s86, s98, 0x4000\n"
"s_mov_b32 s87, 0x20000\n"
"v_mfma_f32_16x16x32_bf16 v[44:47], a[28:31], v[180:183], v[44:47]\n"
"s_mov_b32 m0, 0x27800\n"
"s_nop 0\n"
"buffer_load_dwordx4 v0, s[84:87], s99 offen lds\n"
"v_mfma_f32_16x16x32_bf16 v[48:51], a[60:63], v[180:183], 0\n"
"v_cvt_pk_bf16_f32 v254, v254, 0\n"
"v_lshlrev_b32_e32 v254, 16, v254\n"
"v_mfma_f32_16x16x32_bf16 v[48:51], a[56:59], v[124:127], v[48:51]\n"
"v_rcp_f32_e32 v255, v254\n"
"v_mfma_f32_16x16x32_bf16 v[48:51], a[52:55], v[80:83], v[48:51]\n"
"s_add_u32 s98, s52, 0\n"
"buffer_load_dwordx4 v[88:91], v0, s[4:7], s98 offen sc1\n"
"v_mfma_f32_16x16x32_bf16 v[48:51], a[48:51], v[148:151], v[48:51]\n"
"s_waitcnt lgkmcnt(0)\n"
"s_barrier\n"
"v_mfma_f32_16x16x32_bf16 v[48:51], a[44:47], v[168:171], v[48:51]\n"
"buffer_load_dwordx4 a[184:187], v1, s[4:7], s97 offen offset:2048 sc1\n"
"v_mfma_f32_16x16x32_bf16 v[48:51], a[40:43], v[164:167], v[48:51]\n"
"ds_write_b128 v3, v[44:47] offset:12288\n"
"v_mfma_f32_16x16x32_bf16 v[48:51], a[36:39], v[132:135], v[48:51]\n"
"ds_read_b128 v[44:47], v4\n"
"v_mul_f32_e32 v254, 0x3e8293ee, v255\n"
"v_mfma_f32_16x16x32_bf16 v[48:51], a[32:35], v[128:131], v[48:51]\n"
"buffer_load_dwordx4 v[96:99], v0, s[4:7], s98 offen offset:1024 sc1\n"
"v_mfma_f32_16x16x32_bf16 v[104:107], a[64:67], v[128:131], 0\n"
"ds_read_b128 v[116:119], v9 offset:128\n"
"ds_read_b128 v[152:155], v5\n"
"v_mfma_f32_16x16x32_bf16 v[104:107], a[68:71], v[132:135], v[104:107]\n"
"v_mfma_f32_16x16x32_bf16 v[104:107], a[72:75], v[164:167], v[104:107]\n"
"v_mfma_f32_16x16x32_bf16 v[104:107], a[76:79], v[168:171], v[104:107]\n"
"ds_read_b128 v[156:159], v4 offset:1024\n"
"ds_read_b128 v[176:179], v9 offset:192\n"
"v_mfma_f32_16x16x32_bf16 v[104:107], a[80:83], v[148:151], v[104:107]\n"
"buffer_load_dwordx4 v[188:191], v0, s[4:7], s98 offen offset:2048 sc1\n"
"v_mfma_f32_16x16x32_bf16 v[104:107], a[84:87], v[80:83], v[104:107]\n"
"ds_write_b128 v3, v[48:51] offset:13312\n"
"v_mfma_f32_16x16x32_bf16 v[104:107], a[88:91], v[124:127], v[104:107]\n"
"ds_read_b128 v[48:51], v5 offset:1024\n"
"s_waitcnt lgkmcnt(4)\n"
"v_mfma_f32_16x16x32_bf16 v[104:107], a[92:95], v[180:183], v[104:107]\n"
"v_fmac_f32_e32 v44, v253, v116\n"
"v_fmac_f32_e32 v45, v253, v117\n"
"v_mfma_f32_16x16x32_bf16 v[192:195], a[124:127], v[180:183], 0\n"
"v_fmac_f32_e32 v46, v253, v118\n"
"v_fmac_f32_e32 v47, v253, v119\n"
"v_mfma_f32_16x16x32_bf16 v[192:195], a[120:123], v[124:127], v[192:195]\n"
"buffer_load_dwordx4 v[116:119], v0, s[4:7], s98 offen offset:3072 sc1\n"
"v_mfma_f32_16x16x32_bf16 v[192:195], a[116:119], v[80:83], v[192:195]\n"
"ds_read_b128 v[196:199], v4 offset:2048\n"
"ds_read_b128 v[200:203], v9 offset:256\n"
"v_mfma_f32_16x16x32_bf16 v[192:195], a[112:115], v[148:151], v[192:195]\n"
"ds_read_b128 v[204:207], v5 offset:2048\n"
"v_add_f32_e32 v44, v44, v152\n"
"v_mfma_f32_16x16x32_bf16 v[192:195], a[108:111], v[168:171], v[192:195]\n"
"v_add_f32_e32 v45, v45, v153\n"
"v_add_f32_e32 v46, v46, v154\n"
"v_mfma_f32_16x16x32_bf16 v[192:195], a[104:107], v[164:167], v[192:195]\n"
"ds_write_b128 v3, v[104:107] offset:14336\n"
"v_mfma_f32_16x16x32_bf16 v[192:195], a[100:103], v[132:135], v[192:195]\n"
"buffer_load_dwordx4 v[104:107], v1, s[4:7], s98 offen sc1\n"
"v_mfma_f32_16x16x32_bf16 v[192:195], a[96:99], v[128:131], v[192:195]\n"
"s_waitcnt lgkmcnt(4)\n"
"v_add_f32_e32 v47, v47, v155\n"
"v_fmac_f32_e32 v156, v253, v176\n"
"v_mfma_f32_16x16x32_bf16 v[152:155], v[28:31], v[128:131], 0\n"
"buffer_load_dwordx4 a[188:191], v1, s[4:7], s97 offen offset:3072 sc1\n"
"v_mfma_f32_16x16x32_bf16 v[152:155], v[40:43], v[132:135], v[152:155]\n"
"ds_read_b128 v[208:211], v4 offset:3072\n"
"ds_read_b128 v[212:215], v9 offset:320\n"
"v_mfma_f32_16x16x32_bf16 v[152:155], v[24:27], v[164:167], v[152:155]\n"
"s_waitcnt vmcnt(16)\n"
"s_add_u32 s97, s68, 0x1000\n"
"ds_read_b128 v[216:219], v5 offset:3072\n"
"v_mfma_f32_16x16x32_bf16 v[152:155], v[72:75], v[168:171], v[152:155]\n"
"buffer_load_dwordx4 v[220:223], v1, s[4:7], s98 offen offset:1024 sc1\n"
"v_mfma_f32_16x16x32_bf16 v[152:155], v[32:35], v[148:151], v[152:155]\n"
"buffer_store_dwordx2 v[18:19], v11, s[4:7], s97 offen offset:64 sc1\n"
"v_mfma_f32_16x16x32_bf16 v[152:155], v[64:67], v[80:83], v[152:155]\n"
"ds_write_b128 v3, v[192:195] offset:15360\n"
"v_mfma_f32_16x16x32_bf16 v[152:155], v[60:63], v[124:127], v[152:155]\n"
"v_fmac_f32_e32 v157, v253, v177\n"
"v_fmac_f32_e32 v158, v253, v178\n"
"v_mfma_f32_16x16x32_bf16 v[152:155], v[36:39], v[180:183], v[152:155]\n"
"v_fmac_f32_e32 v159, v253, v179\n"
"v_add_f32_e32 v156, v156, v48\n"
"v_mfma_f32_16x16x32_bf16 v[176:179], v[100:103], v[180:183], 0\n"
"buffer_load_dwordx4 v[180:183], v1, s[4:7], s98 offen offset:2048 sc1\n"
"v_mfma_f32_16x16x32_bf16 v[176:179], v[76:79], v[124:127], v[176:179]\n"
"ds_read_b128 v[124:127], v4 offset:4096\n"
"ds_read_b128 v[192:195], v9\n"
"v_mfma_f32_16x16x32_bf16 v[176:179], v[140:143], v[80:83], v[176:179]\n"
"ds_read_b128 v[80:83], v5 offset:4096\n"
"v_add_f32_e32 v157, v157, v49\n"
"v_mfma_f32_16x16x32_bf16 v[176:179], v[136:139], v[148:151], v[176:179]\n"
"s_waitcnt lgkmcnt(8)\n"
"v_add_f32_e32 v158, v158, v50\n"
"v_add_f32_e32 v159, v159, v51\n"
"v_mfma_f32_16x16x32_bf16 v[176:179], v[56:59], v[168:171], v[176:179]\n"
"v_fmac_f32_e32 v196, v253, v200\n"
"v_fmac_f32_e32 v197, v253, v201\n"
"v_mfma_f32_16x16x32_bf16 v[176:179], v[112:115], v[164:167], v[176:179]\n"
"buffer_load_dwordx4 v[48:51], v1, s[4:7], s98 offen offset:3072 sc1\n"
"v_mfma_f32_16x16x32_bf16 v[176:179], v[108:111], v[132:135], v[176:179]\n"
"ds_write_b128 v3, v[152:155] offset:16384\n"
"v_mfma_f32_16x16x32_bf16 v[176:179], v[120:123], v[128:131], v[176:179]\n"
"s_waitcnt vmcnt(18)\n"
"ds_read_b128 v[128:131], v4 offset:5120\n"
"ds_read_b128 v[132:135], v9 offset:64\n"
"; @A G1 2.3\n"
"v_mfma_f32_16x16x32_bf16 v[44:47], a[0:3], v[52:55], v[44:47]\n"
"ds_read_b128 v[148:151], v5 offset:5120\n"
"v_fmac_f32_e32 v198, v253, v202\n"
"v_mfma_f32_16x16x32_bf16 v[44:47], a[4:7], v[84:87], v[44:47]\n"
"v_fmac_f32_e32 v199, v253, v203\n"
"v_add_f32_e32 v196, v196, v204\n"
"v_mfma_f32_16x16x32_bf16 v[44:47], a[8:11], v[68:71], v[44:47]\n"
"s_add_u32 s98, s53, 0\n"
"buffer_load_dwordx4 v[152:155], v0, s[4:7], s98 offen sc1\n"
"v_mfma_f32_16x16x32_bf16 v[44:47], a[12:15], v[144:147], v[44:47]\n"
"s_waitcnt vmcnt(14)\n"
"s_add_u32 s97, s70, 0x4000\n"
"v_add_f32_e32 v197, v197, v205\n"
"v_mfma_f32_16x16x32_bf16 v[44:47], a[16:19], v[92:95], v[44:47]\n"
"buffer_load_dwordx4 a[192:195], v0, s[4:7], s97 offen sc1\n"
"v_mfma_f32_16x16x32_bf16 v[44:47], a[20:23], v[160:163], v[44:47]\n"
"ds_write_b128 v3, v[176:179] offset:17408\n"
"v_mfma_f32_16x16x32_bf16 v[44:47], a[24:27], v[172:175], v[44:47]\n"
"s_waitcnt lgkmcnt(9)\n"
"v_add_f32_e32 v198, v198, v206\n"
"v_add_f32_e32 v199, v199, v207\n"
"v_mfma_f32_16x16x32_bf16 v[44:47], a[28:31], v[184:187], v[44:47]\n"
"buffer_load_dwordx4 v[164:167], v0, s[4:7], s98 offen offset:1024 sc1\n"
"v_mfma_f32_16x16x32_bf16 v[156:159], a[60:63], v[184:187], v[156:159]\n"
"v_fmac_f32_e32 v208, v253, v212\n"
"v_fmac_f32_e32 v209, v253, v213\n"
"v_mfma_f32_16x16x32_bf16 v[156:159], a[56:59], v[172:175], v[156:159]\n"
"v_fmac_f32_e32 v210, v253, v214\n"
"v_fmac_f32_e32 v211, v253, v215\n"
"v_mfma_f32_16x16x32_bf16 v[156:159], a[52:55], v[160:163], v[156:159]\n"
"s_waitcnt lgkmcnt(0)\n"
"s_barrier\n"
"v_mfma_f32_16x16x32_bf16 v[156:159], a[48:51], v[92:95], v[156:159]\n"
"ds_read_b128 v[168:171], v6\n"
"ds_read_b128 v[176:179], v6 offset:1024\n"
"v_mfma_f32_16x16x32_bf16 v[156:159], a[44:47], v[144:147], v[156:159]\n"
"buffer_load_dwordx4 v[200:203], v0, s[4:7], s98 offen offset:2048 sc1\n"
"v_mfma_f32_16x16x32_bf16 v[156:159], a[40:43], v[68:71], v[156:159]\n"
"v_add_f32_e32 v208, v208, v216\n"
"v_add_f32_e32 v209, v209, v217\n"
"v_mfma_f32_16x16x32_bf16 v[156:159], a[36:39], v[84:87], v[156:159]\n"
"v_add_f32_e32 v210, v210, v218\n"
"v_add_f32_e32 v211, v211, v219\n"
"v_mfma_f32_16x16x32_bf16 v[156:159], a[32:35], v[52:55], v[156:159]\n"
"v_fmac_f32_e32 v124, v253, v192\n"
"v_fmac_f32_e32 v125, v253, v193\n"
"v_mfma_f32_16x16x32_bf16 v[196:199], a[64:67], v[52:55], v[196:199]\n"
"v_fmac_f32_e32 v126, v253, v194\n"
"v_fmac_f32_e32 v127, v253, v195\n"
"v_mfma_f32_16x16x32_bf16 v[196:199], a[68:71], v[84:87], v[196:199]\n"
"buffer_load_dwordx4 v[192:195], v0, s[4:7], s98 offen offset:3072 sc1\n"
"v_mfma_f32_16x16x32_bf16 v[196:199], a[72:75], v[68:71], v[196:199]\n"
"v_add_f32_e32 v124, v124, v80\n"
"v_add_f32_e32 v125, v125, v81\n"
"v_mfma_f32_16x16x32_bf16 v[196:199], a[76:79], v[144:147], v[196:199]\n"
"v_add_f32_e32 v126, v126, v82\n"
"v_add_f32_e32 v127, v127, v83\n"
"v_mfma_f32_16x16x32_bf16 v[196:199], a[80:83], v[92:95], v[196:199]\n"
"s_waitcnt lgkmcnt(0)\n"
"v_fmac_f32_e32 v128, v253, v132\n"
"v_fmac_f32_e32 v129, v253, v133\n"
"v_mfma_f32_16x16x32_bf16 v[196:199], a[84:87], v[160:163], v[196:199]\n"
"v_fmac_f32_e32 v130, v253, v134\n"
"v_fmac_f32_e32 v131, v253, v135\n"
"v_mfma_f32_16x16x32_bf16 v[196:199], a[88:91], v[172:175], v[196:199]\n"
"buffer_load_dwordx4 v[80:83], v1, s[4:7], s98 offen sc1\n"
"v_mfma_f32_16x16x32_bf16 v[196:199], a[92:95], v[184:187], v[196:199]\n"
"ds_read_b128 v[132:135], v6 offset:2048\n"
"ds_read_b128 v[204:207], v6 offset:3072\n"
"v_mfma_f32_16x16x32_bf16 v[208:211], a[124:127], v[184:187], v[208:211]\n"
"buffer_load_dwordx4 a[196:199], v0, s[4:7], s97 offen offset:1024 sc1\n"
"v_mfma_f32_16x16x32_bf16 v[208:211], a[120:123], v[172:175], v[208:211]\n"
"v_add_f32_e32 v128, v128, v148\n"
"v_add_f32_e32 v129, v129, v149\n"
"v_mfma_f32_16x16x32_bf16 v[208:211], a[116:119], v[160:163], v[208:211]\n"
"v_add_f32_e32 v130, v130, v150\n"
"v_add_f32_e32 v131, v131, v151\n"
"v_mfma_f32_16x16x32_bf16 v[208:211], a[112:115], v[92:95], v[208:211]\n"
"buffer_load_dwordx4 v[148:151], v1, s[4:7], s98 offen offset:1024 sc1\n"
"v_mfma_f32_16x16x32_bf16 v[208:211], a[108:111], v[144:147], v[208:211]\n"
"v_add_f32_e32 v44, v44, v168\n"
"v_add_f32_e32 v45, v45, v169\n"
"v_mfma_f32_16x16x32_bf16 v[208:211], a[104:107], v[68:71], v[208:211]\n"
"v_add_f32_e32 v46, v46, v170\n"
"v_add_f32_e32 v47, v47, v171\n"
"v_mfma_f32_16x16x32_bf16 v[208:211], a[100:103], v[84:87], v[208:211]\n"
"v_add_f32_e32 v156, v156, v176\n"
"v_add_f32_e32 v157, v157, v177\n"
"v_mfma_f32_16x16x32_bf16 v[208:211], a[96:99], v[52:55], v[208:211]\n"
"v_add_f32_e32 v158, v158, v178\n"
"v_add_f32_e32 v159, v159, v179\n"
"v_mfma_f32_16x16x32_bf16 v[124:127], v[28:31], v[52:55], v[124:127]\n"
"buffer_load_dwordx4 v[168:171], v1, s[4:7], s98 offen offset:2048 sc1\n"
"v_mfma_f32_16x16x32_bf16 v[124:127], v[40:43], v[84:87], v[124:127]\n"
"v_mul_f32_e32 v44, v44, v255\n"
"v_mul_f32_e32 v45, v45, v255\n"
"v_mfma_f32_16x16x32_bf16 v[124:127], v[24:27], v[68:71], v[124:127]\n"
"v_mul_f32_e32 v46, v46, v255\n"
"v_mul_f32_e32 v47, v47, v255\n"
"v_mfma_f32_16x16x32_bf16 v[124:127], v[72:75], v[144:147], v[124:127]\n"
"v_mul_f32_e32 v156, v156, v255\n"
"v_mul_f32_e32 v157, v157, v255\n"
"v_mfma_f32_16x16x32_bf16 v[124:127], v[32:35], v[92:95], v[124:127]\n"
"s_waitcnt lgkmcnt(0)\n"
"v_mul_f32_e32 v158, v158, v255\n"
"v_mul_f32_e32 v159, v159, v255\n"
"v_mfma_f32_16x16x32_bf16 v[124:127], v[64:67], v[160:163], v[124:127]\n"
"buffer_load_dwordx4 v[176:179], v1, s[4:7], s98 offen offset:3072 sc1\n"
"v_mfma_f32_16x16x32_bf16 v[124:127], v[60:63], v[172:175], v[124:127]\n"
"ds_read_b128 v[212:215], v6 offset:4096\n"
"ds_read_b128 v[216:219], v6 offset:5120\n"
"v_mfma_f32_16x16x32_bf16 v[124:127], v[36:39], v[184:187], v[124:127]\n"
"v_cvt_pk_bf16_f32 v224, v44, v45\n"
"v_cvt_pk_bf16_f32 v225, v46, v47\n"
"v_mfma_f32_16x16x32_bf16 v[128:131], v[100:103], v[184:187], v[128:131]\n"
"v_cvt_pk_bf16_f32 v226, v156, v157\n"
"v_cvt_pk_bf16_f32 v227, v158, v159\n"
"v_mfma_f32_16x16x32_bf16 v[128:131], v[76:79], v[172:175], v[128:131]\n"
"ds_write_b128 v8, v[224:227] offset:17408\n"
"v_mfma_f32_16x16x32_bf16 v[128:131], v[140:143], v[160:163], v[128:131]\n"
"v_add_f32_e32 v196, v196, v132\n"
"v_add_f32_e32 v197, v197, v133\n"
"v_mfma_f32_16x16x32_bf16 v[128:131], v[136:139], v[92:95], v[128:131]\n"
"v_add_f32_e32 v198, v198, v134\n"
"v_add_f32_e32 v199, v199, v135\n"
"v_mfma_f32_16x16x32_bf16 v[128:131], v[56:59], v[144:147], v[128:131]\n"
"v_add_f32_e32 v208, v208, v204\n"
"v_add_f32_e32 v209, v209, v205\n"
"v_mfma_f32_16x16x32_bf16 v[128:131], v[112:115], v[68:71], v[128:131]\n"
"v_add_f32_e32 v210, v210, v206\n"
"v_add_f32_e32 v211, v211, v207\n"
"v_mfma_f32_16x16x32_bf16 v[128:131], v[108:111], v[84:87], v[128:131]\n"
"v_mul_f32_e32 v196, v196, v255\n"
"v_mul_f32_e32 v197, v197, v255\n"
"v_mfma_f32_16x16x32_bf16 v[128:131], v[120:123], v[52:55], v[128:131]\n"
"v_mul_f32_e32 v198, v198, v255\n"
"v_mul_f32_e32 v199, v199, v255\n"
"v_mul_f32_e32 v208, v208, v255\n"
"v_mul_f32_e32 v209, v209, v255\n"
"v_mul_f32_e32 v210, v210, v255\n"
"v_mul_f32_e32 v211, v211, v255\n"
"v_cvt_pk_bf16_f32 v44, v196, v197\n"
"v_cvt_pk_bf16_f32 v45, v198, v199\n"
"v_cvt_pk_bf16_f32 v46, v208, v209\n"
"v_cvt_pk_bf16_f32 v47, v210, v211\n"
"ds_write_b128 v7, v[44:47] offset:50176\n"
".Lmk_a3:\n"
"s_waitcnt vmcnt(16)\n"
"; @A G1 3.0\n"
"v_mfma_f32_16x16x32_bf16 v[52:55], a[0:3], v[88:91], 0\n"
"v_mfma_f32_16x16x32_bf16 v[52:55], a[4:7], v[96:99], v[52:55]\n"
"s_add_u32 s98, s62, 0\n"
"v_mfma_f32_16x16x32_bf16 v[52:55], a[8:11], v[188:191], v[52:55]\n"
"s_add_u32 s96, s54, 0\n"
"buffer_load_dwordx4 v[68:71], v0, s[4:7], s96 offen sc1\n"
"v_mfma_f32_16x16x32_bf16 v[52:55], a[12:15], v[116:119], v[52:55]\n"
"s_waitcnt vmcnt(11)\n"
"v_mfma_f32_16x16x32_bf16 v[52:55], a[16:19], v[104:107], v[52:55]\n"
"buffer_load_dword v255, v10, s[4:7], s98 offen sc1\n"
"v_mfma_f32_16x16x32_bf16 v[52:55], a[20:23], v[220:223], v[52:55]\n"
"s_waitcnt lgkmcnt(2)\n"
"v_add_f32_e32 v124, v124, v212\n"
"v_add_f32_e32 v125, v125, v213\n"
"v_mfma_f32_16x16x32_bf16 v[52:55], a[24:27], v[180:183], v[52:55]\n"
"v_add_f32_e32 v126, v126, v214\n"
"v_add_f32_e32 v127, v127, v215\n"
"v_mfma_f32_16x16x32_bf16 v[52:55], a[28:31], v[48:51], v[52:55]\n"
"buffer_load_dwordx4 v[84:87], v0, s[4:7], s96 offen offset:1024 sc1\n"
"v_mfma_f32_16x16x32_bf16 v[92:95], a[60:63], v[48:51], 0\n"
"buffer_load_dwordx4 a[200:203], v0, s[4:7], s97 offen offset:2048 sc1\n"
"v_mfma_f32_16x16x32_bf16 v[92:95], a[56:59], v[180:183], v[92:95]\n"
"buffer_load_dword v253, v10, s[4:7], s98 offen offset:256 sc1\n"
"v_mfma_f32_16x16x32_bf16 v[92:95], a[52:55], v[220:223], v[92:95]\n"
"v_add_f32_e32 v128, v128, v216\n"
"v_add_f32_e32 v129, v129, v217\n"
"v_mfma_f32_16x16x32_bf16 v[92:95], a[48:51], v[104:107], v[92:95]\n"
"v_add_f32_e32 v130, v130, v218\n"
"v_add_f32_e32 v131, v131, v219\n"
"v_mfma_f32_16x16x32_bf16 v[92:95], a[44:47], v[116:119], v[92:95]\n"
"buffer_load_dwordx4 v[132:135], v0, s[4:7], s96 offen offset:2048 sc1\n"
"v_mfma_f32_16x16x32_bf16 v[92:95], a[40:43], v[188:191], v[92:95]\n"
"ds_write_b128 v3, v[52:55]\n"
"v_mfma_f32_16x16x32_bf16 v[92:95], a[36:39], v[96:99], v[92:95]\n"
"buffer_load_dword v252, v10, s[4:7], s98 offen offset:512 sc1\n"
"v_mfma_f32_16x16x32_bf16 v[92:95], a[32:35], v[88:91], v[92:95]\n"
"v_mul_f32_e32 v124, v124, v254\n"
"v_mul_f32_e32 v125, v125, v254\n"
"v_mfma_f32_16x16x32_bf16 v[52:55], a[64:67], v[88:91], 0\n"
"v_mul_f32_e32 v126, v126, v254\n"
"v_mul_f32_e32 v127, v127, v254\n"
"v_mfma_f32_16x16x32_bf16 v[52:55], a[68:71], v[96:99], v[52:55]\n"
"buffer_load_dwordx4 v[144:147], v0, s[4:7], s96 offen offset:3072 sc1\n"
"v_mfma_f32_16x16x32_bf16 v[52:55], a[72:75], v[188:191], v[52:55]\n"
"buffer_load_dword v251, v10, s[4:7], s98 offen offset:768 sc1\n"
"v_mfma_f32_16x16x32_bf16 v[52:55], a[76:79], v[116:119], v[52:55]\n"
"v_mul_f32_e32 v128, v128, v254\n"
"v_mul_f32_e32 v129, v129, v254\n"
"v_mfma_f32_16x16x32_bf16 v[52:55], a[80:83], v[104:107], v[52:55]\n"
"v_mul_f32_e32 v130, v130, v254\n"
"v_mul_f32_e32 v131, v131, v254\n"
"v_mfma_f32_16x16x32_bf16 v[52:55], a[84:87], v[220:223], v[52:55]\n"
"ds_write_b128 v3, v[92:95] offset:1024\n"
"v_mfma_f32_16x16x32_bf16 v[52:55], a[88:91], v[180:183], v[52:55]\n"
"buffer_load_dwordx4 v[92:95], v1, s[4:7], s96 offen sc1\n"
"v_mfma_f32_16x16x32_bf16 v[52:55], a[92:95], v[48:51], v[52:55]\n"
"buffer_load_dword v254, v10, s[4:7], s98 offen offset:1024 sc1\n"
"v_mfma_f32_16x16x32_bf16 v[156:159], a[124:127], v[48:51], 0\n"
"v_cvt_pk_bf16_f32 v160, v124, v125\n"
"v_cvt_pk_bf16_f32 v161, v126, v127\n"
"v_mfma_f32_16x16x32_bf16 v[156:159], a[120:123], v[180:183], v[156:159]\n"
"v_cvt_pk_bf16_f32 v162, v128, v129\n"
"v_cvt_pk_bf16_f32 v163, v130, v131\n"
"v_mfma_f32_16x16x32_bf16 v[156:159], a[116:119], v[220:223], v[156:159]\n"
"buffer_load_dwordx4 a[204:207], v0, s[4:7], s97 offen offset:3072 sc1\n"
"v_mfma_f32_16x16x32_bf16 v[156:159], a[112:115], v[104:107], v[156:159]\n"
"buffer_load_dwordx4 v[124:127], v1, s[4:7], s96 offen offset:1024 sc1\n"
"v_mfma_f32_16x16x32_bf16 v[156:159], a[108:111], v[116:119], v[156:159]\n"
"buffer_load_dword v250, v10, s[4:7], s98 offen offset:1280 sc1\n"
"v_mfma_f32_16x16x32_bf16 v[156:159], a[104:107], v[188:191], v[156:159]\n"
"ds_write_b128 v3, v[52:55] offset:2048\n"
"v_mfma_f32_16x16x32_bf16 v[156:159], a[100:103], v[96:99], v[156:159]\n"
"ds_write_b128 v2, v[160:163] offset:17408\n"
"v_mfma_f32_16x16x32_bf16 v[156:159], a[96:99], v[88:91], v[156:159]\n"
"buffer_load_dword v249, v10, s[4:7], s98 offen offset:1536 sc1\n"
"v_mfma_f32_16x16x32_bf16 v[52:55], v[28:31], v[88:91], 0\n"
"buffer_load_dwordx4 v[128:131], v1, s[4:7], s96 offen offset:2048 sc1\n"
"v_mfma_f32_16x16x32_bf16 v[52:55], v[40:43], v[96:99], v[52:55]\n"
"buffer_store_dwordx4 v[224:227], v8, s[4:7], s64 offen offset:1024 sc1\n"
"v_mfma_f32_16x16x32_bf16 v[52:55], v[24:27], v[188:191], v[52:55]\n"
"buffer_load_dword v248, v10, s[4:7], s98 offen offset:1792 sc1\n"
"v_mfma_f32_16x16x32_bf16 v[52:55], v[72:75], v[116:119], v[52:55]\n"
"v_mfma_f32_16x16x32_bf16 v[52:55], v[32:35], v[104:107], v[52:55]\n"
"v_mfma_f32_16x16x32_bf16 v[52:55], v[64:67], v[220:223], v[52:55]\n"
"buffer_load_dwordx4 v[160:163], v1, s[4:7], s96 offen offset:3072 sc1\n"
"v_mfma_f32_16x16x32_bf16 v[52:55], v[60:63], v[180:183], v[52:55]\n"
"ds_write_b128 v3, v[156:159] offset:3072\n"
"v_mfma_f32_16x16x32_bf16 v[52:55], v[36:39], v[48:51], v[52:55]\n"
"v_mfma_f32_16x16x32_bf16 v[156:159], v[100:103], v[48:51], 0\n"
"v_mfma_f32_16x16x32_bf16 v[156:159], v[76:79], v[180:183], v[156:159]\n"
"v_mfma_f32_16x16x32_bf16 v[156:159], v[140:143], v[220:223], v[156:159]\n"
"v_mfma_f32_16x16x32_bf16 v[156:159], v[136:139], v[104:107], v[156:159]\n"
"v_mfma_f32_16x16x32_bf16 v[156:159], v[56:59], v[116:119], v[156:159]\n"
"buffer_load_dwordx4 a[208:211], v1, s[4:7], s97 offen sc1\n"
"v_mfma_f32_16x16x32_bf16 v[156:159], v[112:115], v[188:191], v[156:159]\n"
"s_nop 0\n"
"ds_write_b128 v3, v[52:55] offset:4096\n"
"v_mfma_f32_16x16x32_bf16 v[156:159], v[108:111], v[96:99], v[156:159]\n"
"v_mfma_f32_16x16x32_bf16 v[156:159], v[120:123], v[88:91], v[156:159]\n"
"s_waitcnt vmcnt(25)\n"
"; @A G1 3.1\n"
"v_mfma_f32_16x16x32_bf16 v[48:51], a[0:3], v[152:155], 0\n"
"v_mfma_f32_16x16x32_bf16 v[48:51], a[4:7], v[164:167], v[48:51]\n"
"v_mfma_f32_16x16x32_bf16 v[48:51], a[8:11], v[200:203], v[48:51]\n"
"s_add_u32 s98, s55, 0\n"
"buffer_load_dwordx4 v[52:55], v0, s[4:7], s98 offen sc1\n"
"v_mfma_f32_16x16x32_bf16 v[48:51], a[12:15], v[192:195], v[48:51]\n"
"s_waitcnt vmcnt(21)\n"
"v_mfma_f32_16x16x32_bf16 v[48:51], a[16:19], v[80:83], v[48:51]\n"
"v_mfma_f32_16x16x32_bf16 v[48:51], a[20:23], v[148:151], v[48:51]\n"
"ds_write_b128 v3, v[156:159] offset:5120\n"
"v_mfma_f32_16x16x32_bf16 v[48:51], a[24:27], v[168:171], v[48:51]\n"
"v_mfma_f32_16x16x32_bf16 v[48:51], a[28:31], v[176:179], v[48:51]\n"
"buffer_load_dwordx4 v[88:91], v0, s[4:7], s98 offen offset:1024 sc1\n"
"s_add_u32 s96, s64, 0x4000\n"
"v_mfma_f32_16x16x32_bf16 v[96:99], a[60:63], v[176:179], 0\n"
"v_mfma_f32_16x16x32_bf16 v[96:99], a[56:59], v[168:171], v[96:99]\n"
"buffer_store_dwordx4 v[44:47], v7, s[4:7], s96 offen offset:1024 sc1\n"
"v_mfma_f32_16x16x32_bf16 v[96:99], a[52:55], v[148:151], v[96:99]\n"
"v_mfma_f32_16x16x32_bf16 v[96:99], a[48:51], v[80:83], v[96:99]\n"
"v_mfma_f32_16x16x32_bf16 v[96:99], a[44:47], v[192:195], v[96:99]\n"
"buffer_load_dwordx4 v[44:47], v0, s[4:7], s98 offen offset:2048 sc1\n"
"v_mfma_f32_16x16x32_bf16 v[96:99], a[40:43], v[200:203], v[96:99]\n"
"ds_write_b128 v3, v[48:51] offset:6144\n"
"v_mfma_f32_16x16x32_bf16 v[96:99], a[36:39], v[164:167], v[96:99]\n"
"buffer_load_dwordx4 a[212:215], v1, s[4:7], s97 offen offset:1024 sc1\n"
"v_mfma_f32_16x16x32_bf16 v[96:99], a[32:35], v[152:155], v[96:99]\n"
"v_mfma_f32_16x16x32_bf16 v[48:51], a[64:67], v[152:155], 0\n"
"v_mfma_f32_16x16x32_bf16 v[48:51], a[68:71], v[164:167], v[48:51]\n"
"buffer_load_dwordx4 v[104:107], v0, s[4:7], s98 offen offset:3072 sc1\n"
"v_mfma_f32_16x16x32_bf16 v[48:51], a[72:75], v[200:203], v[48:51]\n"
"v_mfma_f32_16x16x32_bf16 v[48:51], a[76:79], v[192:195], v[48:51]\n"
"v_mfma_f32_16x16x32_bf16 v[48:51], a[80:83], v[80:83], v[48:51]\n"
"v_mfma_f32_16x16x32_bf16 v[48:51], a[84:87], v[148:151], v[48:51]\n"
"s_nop 0\n"
"ds_write_b128 v3, v[96:99] offset:7168\n"
"v_mfma_f32_16x16x32_bf16 v[48:51], a[88:91], v[168:171], v[48:51]\n"
"buffer_load_dwordx4 v[96:99], v1, s[4:7], s98 offen sc1\n"
"v_mfma_f32_16x16x32_bf16 v[48:51], a[92:95], v[176:179], v[48:51]\n"
"v_mfma_f32_16x16x32_bf16 v[116:119], a[124:127], v[176:179], 0\n"
"v_mfma_f32_16x16x32_bf16 v[116:119], a[120:123], v[168:171], v[116:119]\n"
"v_mfma_f32_16x16x32_bf16 v[116:119], a[116:119], v[148:151], v[116:119]\n"
"v_mfma_f32_16x16x32_bf16 v[116:119], a[112:115], v[80:83], v[116:119]\n"
"buffer_load_dwordx4 v[156:159], v1, s[4:7], s98 offen offset:1024 sc1\n"
"v_mfma_f32_16x16x32_bf16 v[116:119], a[108:111], v[192:195], v[116:119]\n"
"v_mfma_f32_16x16x32_bf16 v[116:119], a[104:107], v[200:203], v[116:119]\n"
"s_nop 0\n"
"ds_write_b128 v3, v[48:51] offset:8192\n"
"v_mfma_f32_16x16x32_bf16 v[116:119], a[100:103], v[164:167], v[116:119]\n"
"v_mfma_f32_16x16x32_bf16 v[116:119], a[96:99], v[152:155], v[116:119]\n"
"v_mfma_f32_16x16x32_bf16 v[48:51], v[28:31], v[152:155], 0\n"
"buffer_load_dwordx4 v[172:175], v1, s[4:7], s98 offen offset:2048 sc1\n"
"v_mfma_f32_16x16x32_bf16 v[48:51], v[40:43], v[164:167], v[48:51]\n"
"buffer_load_dwordx4 a[216:219], v1, s[4:7], s97 offen offset:2048 sc1\n"
"v_mfma_f32_16x16x32_bf16 v[48:51], v[24:27], v[200:203], v[48:51]\n"
"v_mfma_f32_16x16x32_bf16 v[48:51], v[72:75], v[192:195], v[48:51]\n"
"v_mfma_f32_16x16x32_bf16 v[48:51], v[32:35], v[80:83], v[48:51]\n"
"s_waitcnt vmcnt(12)\n"
"v_add_f32_e32 v255, v255, v254\n"
"v_add_f32_e32 v253, v253, v250\n"
"v_mfma_f32_16x16x32_bf16 v[48:51], v[64:67], v[148:151], v[48:51]\n"
"buffer_load_dwordx4 v[180:183], v1, s[4:7], s98 offen offset:3072 sc1\n"
"v_mfma_f32_16x16x32_bf16 v[48:51], v[60:63], v[168:171], v[48:51]\n"
"ds_write_b128 v3, v[116:119] offset:9216\n"
"v_mfma_f32_16x16x32_bf16 v[48:51], v[36:39], v[176:179], v[48:51]\n"
"v_add_f32_e32 v252, v252, v249\n"
"v_add_f32_e32 v251, v251, v248\n"
"v_mfma_f32_16x16x32_bf16 v[116:119], v[100:103], v[176:179], 0\n"
"v_add_f32_e32 v255, v255, v252\n"
"v_add_f32_e32 v253, v253, v251\n"
"v_mfma_f32_16x16x32_bf16 v[116:119], v[76:79], v[168:171], v[116:119]\n"
"v_add_f32_e32 v255, v255, v253\n"
"v_mov_b32_e32 v253, v255\n"
"v_mfma_f32_16x16x32_bf16 v[116:119], v[140:143], v[148:151], v[116:119]\n"
"s_add_u32 s98, s71, 0\n"
"buffer_load_dwordx2 v[250:251], off, s[4:7], s98 sc1\n"
"v_mfma_f32_16x16x32_bf16 v[116:119], v[136:139], v[80:83], v[116:119]\n"
"v_permlane32_swap_b32_e32 v255, v253\n"
"v_add_f32_e32 v255, v255, v253\n"
"v_mfma_f32_16x16x32_bf16 v[116:119], v[56:59], v[192:195], v[116:119]\n"
"v_mul_f32_e32 v255, 0x3a800000, v255\n"
"v_mov_b32_e32 v253, v255\n"
"v_mfma_f32_16x16x32_bf16 v[116:119], v[112:115], v[200:203], v[116:119]\n"
"ds_write_b128 v3, v[48:51] offset:10240\n"
"v_mfma_f32_16x16x32_bf16 v[116:119], v[108:111], v[164:167], v[116:119]\n"
"buffer_store_dwordx2 v[18:19], v11, s[4:7], s68 offen offset:32 sc1\n"
"v_mfma_f32_16x16x32_bf16 v[116:119], v[120:123], v[152:155], v[116:119]\n"
"v_permlane16_swap_b32_e32 v255, v253\n"
"v_cvt_pk_bf16_f32 v254, -v255, 0\n"
"; @A G1 3.2\n"
"v_mfma_f32_16x16x32_bf16 v[48:51], a[0:3], v[68:71], 0\n"
"v_fma_f32 v253, -v255, v255, v253\n"
"v_lshlrev_b32_e32 v254, 16, v254\n"
"v_mfma_f32_16x16x32_bf16 v[48:51], a[4:7], v[84:87], v[48:51]\n"
"v_max_f32_e32 v253, 0, v253\n"
"v_cvt_pk_bf16_f32 v253, v253, 0\n"
"v_mfma_f32_16x16x32_bf16 v[48:51], a[8:11], v[132:135], v[48:51]\n"
"buffer_load_dwordx4 a[220:223], v1, s[4:7], s97 offen offset:3072 sc1\n"
"v_mfma_f32_16x16x32_bf16 v[48:51], a[12:15], v[144:147], v[48:51]\n"
"s_waitcnt vmcnt(15)\n"
"v_lshlrev_b32_e32 v253, 16, v253\n"
"v_add_f32_e32 v253, 0x3727c5ac, v253\n"
"v_mfma_f32_16x16x32_bf16 v[48:51], a[16:19], v[92:95], v[48:51]\n"
"s_add_u32 s97, s70, 0x6000\n"
"v_cvt_pk_bf16_f32 v253, v253, 0\n"
"v_lshlrev_b32_e32 v253, 16, v253\n"
"v_mfma_f32_16x16x32_bf16 v[48:51], a[20:23], v[124:127], v[48:51]\n"
"ds_write_b128 v3, v[116:119] offset:11264\n"
"v_mfma_f32_16x16x32_bf16 v[48:51], a[24:27], v[128:131], v[48:51]\n"
"buffer_load_dwordx4 a[224:227], v0, s[4:7], s97 offen sc1\n"
"v_mfma_f32_16x16x32_bf16 v[48:51], a[28:31], v[160:163], v[48:51]\n"
"v_sqrt_f32_e32 v253, v253\n"
"v_mfma_f32_16x16x32_bf16 v[80:83], a[60:63], v[160:163], 0\n"
"v_cvt_pk_bf16_f32 v253, v253, 0\n"
"v_lshlrev_b32_e32 v253, 16, v253\n"
"v_mfma_f32_16x16x32_bf16 v[80:83], a[56:59], v[128:131], v[80:83]\n"
"v_rcp_f32_e32 v255, v253\n"
"v_mfma_f32_16x16x32_bf16 v[80:83], a[52:55], v[124:127], v[80:83]\n"
"s_waitcnt lgkmcnt(0)\n"
"s_barrier\n"
"v_mfma_f32_16x16x32_bf16 v[80:83], a[48:51], v[92:95], v[80:83]\n"
"buffer_load_dwordx4 a[228:231], v0, s[4:7], s97 offen offset:1024 sc1\n"
"v_mfma_f32_16x16x32_bf16 v[80:83], a[44:47], v[144:147], v[80:83]\n"
"ds_read_b128 v[116:119], v4\n"
"v_mul_f32_e32 v253, 0x3e8293ee, v255\n"
"v_mfma_f32_16x16x32_bf16 v[80:83], a[40:43], v[132:135], v[80:83]\n"
"ds_write_b128 v3, v[48:51] offset:12288\n"
"v_mfma_f32_16x16x32_bf16 v[80:83], a[36:39], v[84:87], v[80:83]\n"
"ds_read_b128 v[48:51], v9 offset:128\n"
"ds_read_b128 v[148:151], v5\n"
"v_mfma_f32_16x16x32_bf16 v[80:83], a[32:35], v[68:71], v[80:83]\n"
"buffer_load_dwordx4 a[232:235], v0, s[4:7], s97 offen offset:2048 sc1\n"
"v_mfma_f32_16x16x32_bf16 v[152:155], a[64:67], v[68:71], 0\n"
"v_mfma_f32_16x16x32_bf16 v[152:155], a[68:71], v[84:87], v[152:155]\n"
"v_mfma_f32_16x16x32_bf16 v[152:155], a[72:75], v[132:135], v[152:155]\n"
"ds_read_b128 v[164:167], v4 offset:1024\n"
"ds_read_b128 v[168:171], v9 offset:192\n"
"v_mfma_f32_16x16x32_bf16 v[152:155], a[76:79], v[144:147], v[152:155]\n"
"buffer_load_dwordx4 a[236:239], v0, s[4:7], s97 offen offset:3072 sc1\n"
"v_mfma_f32_16x16x32_bf16 v[152:155], a[80:83], v[92:95], v[152:155]\n"
"ds_read_b128 v[176:179], v5 offset:1024\n"
"s_waitcnt lgkmcnt(3)\n"
"v_mfma_f32_16x16x32_bf16 v[152:155], a[84:87], v[124:127], v[152:155]\n"
"ds_write_b128 v3, v[80:83] offset:13312\n"
"v_mfma_f32_16x16x32_bf16 v[152:155], a[88:91], v[128:131], v[152:155]\n"
"v_fmac_f32_e32 v116, v254, v48\n"
"v_fmac_f32_e32 v117, v254, v49\n"
"v_mfma_f32_16x16x32_bf16 v[152:155], a[92:95], v[160:163], v[152:155]\n"
"buffer_load_dwordx4 a[240:243], v1, s[4:7], s97 offen sc1\n"
"v_mfma_f32_16x16x32_bf16 v[80:83], a[124:127], v[160:163], 0\n"
"v_fmac_f32_e32 v118, v254, v50\n"
"v_fmac_f32_e32 v119, v254, v51\n"
"v_mfma_f32_16x16x32_bf16 v[80:83], a[120:123], v[128:131], v[80:83]\n"
"ds_read_b128 v[48:51], v4 offset:2048\n"
"ds_read_b128 v[184:187], v9 offset:256\n"
"v_mfma_f32_16x16x32_bf16 v[80:83], a[116:119], v[124:127], v[80:83]\n"
"ds_read_b128 v[188:191], v5 offset:2048\n"
"v_add_f32_e32 v116, v116, v148\n"
"v_mfma_f32_16x16x32_bf16 v[80:83], a[112:115], v[92:95], v[80:83]\n"
"buffer_load_dwordx4 a[244:247], v1, s[4:7], s97 offen offset:1024 sc1\n"
"v_mfma_f32_16x16x32_bf16 v[80:83], a[108:111], v[144:147], v[80:83]\n"
"v_add_f32_e32 v117, v117, v149\n"
"v_add_f32_e32 v118, v118, v150\n"
"v_mfma_f32_16x16x32_bf16 v[80:83], a[104:107], v[132:135], v[80:83]\n"
"ds_write_b128 v3, v[152:155] offset:14336\n"
"v_mfma_f32_16x16x32_bf16 v[80:83], a[100:103], v[84:87], v[80:83]\n"
"s_waitcnt lgkmcnt(5)\n"
"v_add_f32_e32 v119, v119, v151\n"
"v_fmac_f32_e32 v164, v254, v168\n"
"v_mfma_f32_16x16x32_bf16 v[80:83], a[96:99], v[68:71], v[80:83]\n"
"buffer_load_dwordx4 a[248:251], v1, s[4:7], s97 offen offset:2048 sc1\n"
"v_mfma_f32_16x16x32_bf16 v[148:151], v[28:31], v[68:71], 0\n"
"ds_read_b128 v[152:155], v4 offset:3072\n"
"ds_read_b128 v[192:195], v9 offset:320\n"
"v_mfma_f32_16x16x32_bf16 v[148:151], v[40:43], v[84:87], v[148:151]\n"
"ds_read_b128 v[196:199], v5 offset:3072\n"
"v_fmac_f32_e32 v165, v254, v169\n"
"v_mfma_f32_16x16x32_bf16 v[148:151], v[24:27], v[132:135], v[148:151]\n"
"v_fmac_f32_e32 v166, v254, v170\n"
"v_fmac_f32_e32 v167, v254, v171\n"
"v_mfma_f32_16x16x32_bf16 v[148:151], v[72:75], v[144:147], v[148:151]\n"
"buffer_load_dwordx4 a[252:255], v1, s[4:7], s97 offen offset:3072 sc1\n"
"v_mfma_f32_16x16x32_bf16 v[148:151], v[32:35], v[92:95], v[148:151]\n"
"v_add_f32_e32 v164, v164, v176\n"
"v_add_f32_e32 v165, v165, v177\n"
"v_mfma_f32_16x16x32_bf16 v[148:151], v[64:67], v[124:127], v[148:151]\n"
"ds_write_b128 v3, v[80:83] offset:15360\n"
"v_mfma_f32_16x16x32_bf16 v[148:151], v[60:63], v[128:131], v[148:151]\n"
"s_waitcnt lgkmcnt(5)\n"
"v_add_f32_e32 v166, v166, v178\n"
"v_add_f32_e32 v167, v167, v179\n"
"v_mfma_f32_16x16x32_bf16 v[148:151], v[36:39], v[160:163], v[148:151]\n"
"ds_read_b128 v[80:83], v4 offset:4096\n"
"ds_read_b128 v[168:171], v9\n"
"v_mfma_f32_16x16x32_bf16 v[176:179], v[100:103], v[160:163], 0\n"
"ds_read_b128 v[160:163], v5 offset:4096\n"
"v_fmac_f32_e32 v48, v254, v184\n"
"v_mfma_f32_16x16x32_bf16 v[176:179], v[76:79], v[128:131], v[176:179]\n"
"v_fmac_f32_e32 v49, v254, v185\n"
"v_fmac_f32_e32 v50, v254, v186\n"
"v_mfma_f32_16x16x32_bf16 v[176:179], v[140:143], v[124:127], v[176:179]\n"
"v_fmac_f32_e32 v51, v254, v187\n"
"v_add_f32_e32 v48, v48, v188\n"
"v_mfma_f32_16x16x32_bf16 v[176:179], v[136:139], v[92:95], v[176:179]\n"
"v_add_f32_e32 v49, v49, v189\n"
"v_add_f32_e32 v50, v50, v190\n"
"v_mfma_f32_16x16x32_bf16 v[176:179], v[56:59], v[144:147], v[176:179]\n"
"s_waitcnt vmcnt(10)\n"
"v_cmp_ne_u64_e32 vcc, v[250:251], v[18:19]\n"
"v_mfma_f32_16x16x32_bf16 v[176:179], v[112:115], v[132:135], v[176:179]\n"
"ds_write_b128 v3, v[148:151] offset:16384\n"
"v_mfma_f32_16x16x32_bf16 v[176:179], v[108:111], v[84:87], v[176:179]\n"
"s_cbranch_vccnz .Lmk_f73s\n"
".Lmk_f73r:\n"
"s_add_u32 m0, s77, 0x6000\n"
"v_mfma_f32_16x16x32_bf16 v[176:179], v[120:123], v[68:71], v[176:179]\n"
"s_add_u32 s98, s65, 0x2000\n"
"ds_read_b128 v[68:71], v4 offset:5120\n"
"; @A G1 3.3\n"
"v_mfma_f32_16x16x32_bf16 v[116:119], a[0:3], v[52:55], v[116:119]\n"
"buffer_load_dwordx4 v2, s[4:7], s98 offen sc1 lds\n"
"v_mfma_f32_16x16x32_bf16 v[116:119], a[4:7], v[88:91], v[116:119]\n"
"buffer_load_dwordx4 v2, s[4:7], s98 offen offset:1024 sc1 lds\n"
"v_mfma_f32_16x16x32_bf16 v[116:119], a[8:11], v[44:47], v[116:119]\n"
"ds_read_b128 v[84:87], v9 offset:64\n"
"ds_read_b128 v[92:95], v5 offset:5120\n"
"v_mfma_f32_16x16x32_bf16 v[116:119], a[12:15], v[104:107], v[116:119]\n"
"s_waitcnt lgkmcnt(8)\n"
"v_add_f32_e32 v51, v51, v191\n"
"v_mfma_f32_16x16x32_bf16 v[116:119], a[16:19], v[96:99], v[116:119]\n"
"v_fmac_f32_e32 v152, v254, v192\n"
"v_fmac_f32_e32 v153, v254, v193\n"
"v_mfma_f32_16x16x32_bf16 v[116:119], a[20:23], v[156:159], v[116:119]\n"
"ds_write_b128 v3, v[176:179] offset:17408\n"
"v_mfma_f32_16x16x32_bf16 v[116:119], a[24:27], v[172:175], v[116:119]\n"
"v_fmac_f32_e32 v154, v254, v194\n"
"v_fmac_f32_e32 v155, v254, v195\n"
"v_mfma_f32_16x16x32_bf16 v[116:119], a[28:31], v[180:183], v[116:119]\n"
"s_add_u32 m0, s77, 0xe000\n"
"s_add_u32 s98, s65, 0x6000\n"
"v_add_f32_e32 v152, v152, v196\n"
"v_mfma_f32_16x16x32_bf16 v[164:167], a[60:63], v[180:183], v[164:167]\n"
"buffer_load_dwordx4 v2, s[4:7], s98 offen sc1 lds\n"
"v_mfma_f32_16x16x32_bf16 v[164:167], a[56:59], v[172:175], v[164:167]\n"
"buffer_load_dwordx4 v2, s[4:7], s98 offen offset:1024 sc1 lds\n"
"v_mfma_f32_16x16x32_bf16 v[164:167], a[52:55], v[156:159], v[164:167]\n"
"s_waitcnt lgkmcnt(0)\n"
"s_barrier\n"
"v_mfma_f32_16x16x32_bf16 v[164:167], a[48:51], v[96:99], v[164:167]\n"
"ds_read_b128 v[124:127], v6\n"
"ds_read_b128 v[128:131], v6 offset:1024\n"
"v_mfma_f32_16x16x32_bf16 v[164:167], a[44:47], v[104:107], v[164:167]\n"
"s_add_u32 s98, s69, 0x600000\n"
"buffer_load_dwordx4 a[0:3], v0, s[4:7], s98 offen sc1\n"
"v_mfma_f32_16x16x32_bf16 v[164:167], a[40:43], v[44:47], v[164:167]\n"
"v_add_f32_e32 v153, v153, v197\n"
"v_add_f32_e32 v154, v154, v198\n"
"v_mfma_f32_16x16x32_bf16 v[164:167], a[36:39], v[88:91], v[164:167]\n"
"v_add_f32_e32 v155, v155, v199\n"
"v_fmac_f32_e32 v80, v254, v168\n"
"v_mfma_f32_16x16x32_bf16 v[164:167], a[32:35], v[52:55], v[164:167]\n"
"buffer_load_dwordx4 a[4:7], v0, s[4:7], s98 offen offset:1024 sc1\n"
"v_mfma_f32_16x16x32_bf16 v[48:51], a[64:67], v[52:55], v[48:51]\n"
"v_fmac_f32_e32 v81, v254, v169\n"
"v_fmac_f32_e32 v82, v254, v170\n"
"v_mfma_f32_16x16x32_bf16 v[48:51], a[68:71], v[88:91], v[48:51]\n"
"v_fmac_f32_e32 v83, v254, v171\n"
"v_add_f32_e32 v80, v80, v160\n"
"v_mfma_f32_16x16x32_bf16 v[48:51], a[72:75], v[44:47], v[48:51]\n"
"buffer_load_dwordx4 a[8:11], v0, s[4:7], s98 offen offset:2048 sc1\n"
"v_mfma_f32_16x16x32_bf16 v[48:51], a[76:79], v[104:107], v[48:51]\n"
"v_add_f32_e32 v81, v81, v161\n"
"v_add_f32_e32 v82, v82, v162\n"
"v_mfma_f32_16x16x32_bf16 v[48:51], a[80:83], v[96:99], v[48:51]\n"
"v_add_f32_e32 v83, v83, v163\n"
"v_fmac_f32_e32 v68, v254, v84\n"
"v_mfma_f32_16x16x32_bf16 v[48:51], a[84:87], v[156:159], v[48:51]\n"
"buffer_load_dwordx4 a[12:15], v0, s[4:7], s98 offen offset:3072 sc1\n"
"v_mfma_f32_16x16x32_bf16 v[48:51], a[88:91], v[172:175], v[48:51]\n"
"ds_read_b128 v[132:135], v6 offset:2048\n"
"ds_read_b128 v[144:147], v6 offset:3072\n"
"v_mfma_f32_16x16x32_bf16 v[48:51], a[92:95], v[180:183], v[48:51]\n"
"s_waitcnt lgkmcnt(2)\n"
"v_fmac_f32_e32 v69, v254, v85\n"
"v_fmac_f32_e32 v70, v254, v86\n"
"v_mfma_f32_16x16x32_bf16 v[152:155], a[124:127], v[180:183], v[152:155]\n"
"buffer_load_dwordx4 a[16:19], v1, s[4:7], s98 offen sc1\n"
"v_mfma_f32_16x16x32_bf16 v[152:155], a[120:123], v[172:175], v[152:155]\n"
"v_fmac_f32_e32 v71, v254, v87\n"
"v_add_f32_e32 v68, v68, v92\n"
"v_mfma_f32_16x16x32_bf16 v[152:155], a[116:119], v[156:159], v[152:155]\n"
"v_add_f32_e32 v69, v69, v93\n"
"v_add_f32_e32 v70, v70, v94\n"
"v_mfma_f32_16x16x32_bf16 v[152:155], a[112:115], v[96:99], v[152:155]\n"
"buffer_load_dwordx4 a[20:23], v1, s[4:7], s98 offen offset:1024 sc1\n"
"v_mfma_f32_16x16x32_bf16 v[152:155], a[108:111], v[104:107], v[152:155]\n"
"v_add_f32_e32 v71, v71, v95\n"
"v_add_f32_e32 v116, v116, v124\n"
"v_mfma_f32_16x16x32_bf16 v[152:155], a[104:107], v[44:47], v[152:155]\n"
"v_add_f32_e32 v117, v117, v125\n"
"v_add_f32_e32 v118, v118, v126\n"
"v_mfma_f32_16x16x32_bf16 v[152:155], a[100:103], v[88:91], v[152:155]\n"
"buffer_load_dwordx4 a[24:27], v1, s[4:7], s98 offen offset:2048 sc1\n"
"v_mfma_f32_16x16x32_bf16 v[152:155], a[96:99], v[52:55], v[152:155]\n"
"v_add_f32_e32 v119, v119, v127\n"
"v_add_f32_e32 v164, v164, v128\n"
"v_mfma_f32_16x16x32_bf16 v[80:83], v[28:31], v[52:55], v[80:83]\n"
"v_add_f32_e32 v165, v165, v129\n"
"v_add_f32_e32 v166, v166, v130\n"
"v_mfma_f32_16x16x32_bf16 v[80:83], v[40:43], v[88:91], v[80:83]\n"
"buffer_load_dwordx4 a[28:31], v1, s[4:7], s98 offen offset:3072 sc1\n"
"v_mfma_f32_16x16x32_bf16 v[80:83], v[24:27], v[44:47], v[80:83]\n"
"v_add_f32_e32 v167, v167, v131\n"
"v_mul_f32_e32 v116, v116, v255\n"
"v_mfma_f32_16x16x32_bf16 v[80:83], v[72:75], v[104:107], v[80:83]\n"
"v_mul_f32_e32 v117, v117, v255\n"
"v_mul_f32_e32 v118, v118, v255\n"
"v_mfma_f32_16x16x32_bf16 v[80:83], v[32:35], v[96:99], v[80:83]\n"
"s_waitcnt lgkmcnt(0)\n"
"v_mul_f32_e32 v119, v119, v255\n"
"v_mul_f32_e32 v164, v164, v255\n"
"v_mfma_f32_16x16x32_bf16 v[80:83], v[64:67], v[156:159], v[80:83]\n"
"v_mul_f32_e32 v165, v165, v255\n"
"v_mul_f32_e32 v166, v166, v255\n"
"v_mfma_f32_16x16x32_bf16 v[80:83], v[60:63], v[172:175], v[80:83]\n"
"ds_read_b128 v[24:27], v6 offset:4096\n"
"ds_read_b128 v[28:31], v6 offset:5120\n"
"v_mfma_f32_16x16x32_bf16 v[80:83], v[36:39], v[180:183], v[80:83]\n"
"v_mul_f32_e32 v167, v167, v255\n"
"v_cvt_pk_bf16_f32 v32, v116, v117\n"
"v_mfma_f32_16x16x32_bf16 v[68:71], v[100:103], v[180:183], v[68:71]\n"
"v_cvt_pk_bf16_f32 v33, v118, v119\n"
"v_cvt_pk_bf16_f32 v34, v164, v165\n"
"v_mfma_f32_16x16x32_bf16 v[68:71], v[76:79], v[172:175], v[68:71]\n"
"v_cvt_pk_bf16_f32 v35, v166, v167\n"
"v_add_f32_e32 v48, v48, v132\n"
"v_mfma_f32_16x16x32_bf16 v[68:71], v[140:143], v[156:159], v[68:71]\n"
"ds_write_b128 v7, v[32:35] offset:16384\n"
"v_mfma_f32_16x16x32_bf16 v[68:71], v[136:139], v[96:99], v[68:71]\n"
"buffer_store_dwordx4 v[32:35], v7, s[4:7], s64 offen sc1\n"
"v_mfma_f32_16x16x32_bf16 v[68:71], v[56:59], v[104:107], v[68:71]\n"
"v_add_f32_e32 v49, v49, v133\n"
"v_add_f32_e32 v50, v50, v134\n"
"v_mfma_f32_16x16x32_bf16 v[68:71], v[112:115], v[44:47], v[68:71]\n"
"v_add_f32_e32 v51, v51, v135\n"
"v_add_f32_e32 v152, v152, v144\n"
"v_mfma_f32_16x16x32_bf16 v[68:71], v[108:111], v[88:91], v[68:71]\n"
"v_add_f32_e32 v153, v153, v145\n"
"v_add_f32_e32 v154, v154, v146\n"
"v_mfma_f32_16x16x32_bf16 v[68:71], v[120:123], v[52:55], v[68:71]\n"
"v_add_f32_e32 v155, v155, v147\n"
"v_mul_f32_e32 v48, v48, v255\n"
"v_mul_f32_e32 v49, v49, v255\n"
"v_mul_f32_e32 v50, v50, v255\n"
"v_mul_f32_e32 v51, v51, v255\n"
"v_mul_f32_e32 v152, v152, v255\n"
"v_mul_f32_e32 v153, v153, v255\n"
"v_mul_f32_e32 v154, v154, v255\n"
"v_mul_f32_e32 v155, v155, v255\n"
"v_cvt_pk_bf16_f32 v32, v48, v49\n"
"v_cvt_pk_bf16_f32 v33, v50, v51\n"
"v_cvt_pk_bf16_f32 v34, v152, v153\n"
"v_cvt_pk_bf16_f32 v35, v154, v155\n"
"v_xor_b32_e32 v255, 32, v16\n"
"v_xor_b32_e32 v254, 32, v15\n"
"v_mov_b32_e32 v88, 0x3f803f80\n"
"v_mov_b32_e32 v89, 0x3f803f80\n"
"v_mov_b32_e32 v90, 0x3f803f80\n"
"v_mov_b32_e32 v91, 0x3f803f80\n"
"v_mov_b32_e32 v252, 0x53800000\n"
"ds_write_b128 v7, v[32:35] offset:49152\n"
".Lmk_a4:\n"
"; @A tail\n"
"s_add_u32 s98, s64, 0x4000\n"
"buffer_store_dwordx4 v[32:35], v7, s[4:7], s98 offen sc1\n"
"s_waitcnt lgkmcnt(2)\n"
"v_add_f32_e32 v80, v80, v24\n"
"v_add_f32_e32 v81, v81, v25\n"
"v_add_f32_e32 v82, v82, v26\n"
"v_add_f32_e32 v83, v83, v27\n"
"v_add_f32_e32 v68, v68, v28\n"
"v_add_f32_e32 v69, v69, v29\n"
"v_add_f32_e32 v70, v70, v30\n"
"v_add_f32_e32 v71, v71, v31\n"
"v_mul_f32_e32 v80, v80, v253\n"
"v_mul_f32_e32 v81, v81, v253\n"
"v_mul_f32_e32 v82, v82, v253\n"
"v_mul_f32_e32 v83, v83, v253\n"
"v_mul_f32_e32 v68, v68, v253\n"
"v_mul_f32_e32 v69, v69, v253\n"
"v_mul_f32_e32 v70, v70, v253\n"
"v_mul_f32_e32 v71, v71, v253\n"
"v_cvt_pk_bf16_f32 v24, v80, v81\n"
"v_cvt_pk_bf16_f32 v25, v82, v83\n"
"v_cvt_pk_bf16_f32 v26, v68, v69\n"
"v_cvt_pk_bf16_f32 v27, v70, v71\n"
"ds_write_b128 v2, v[24:27] offset:16384\n"
"s_waitcnt lgkmcnt(0)\n"
"s_barrier\n"
"ds_read_b128 v[28:31], v2 offset:49152\n"
"ds_read_b128 v[32:35], v2 offset:50176\n"
"ds_read_b128 v[92:95], v2 offset:16384\n"
"ds_read_b128 v[96:99], v2 offset:17408\n"
"; @A AT b\n"
"s_mov_b32 s2, 0\n"
"v_mov_b32_e32 v253, 0xff800000\n"
"v_mov_b32_e32 v251, 0xff800000\n"
"v_mov_b32_e32 v250, 0x7f800000\n"
"v_mov_b32_e32 v249, 0x7f800000\n"
".Lmk_a91:\n"
"ds_read_b128 v[24:27], v15 offset:24576\n"
"ds_read_b128 v[36:39], v254 offset:24832\n"
"ds_read_b128 v[40:43], v15 offset:22528\n"
"ds_read_b128 v[44:47], v254 offset:22784\n"
"s_cmp_gt_i32 s78, 11\n"
"s_cselect_b32 s98, 0x80000000, 0\n"
"s_or_b32 s98, s98, 0x7f800000\n"
"s_waitcnt lgkmcnt(2)\n"
"v_mfma_f32_16x16x32_bf16 v[48:51], v[24:27], v[28:31], 0\n"
"v_mfma_f32_16x16x32_bf16 v[52:55], v[24:27], v[32:35], 0\n"
"v_mfma_f32_16x16x32_bf16 v[56:59], v[36:39], v[32:35], 0\n"
"v_mfma_f32_16x16x32_bf16 v[24:27], v[36:39], v[28:31], 0\n"
"s_waitcnt lgkmcnt(0)\n"
"s_nop 3\n"
"v_max_f32_e32 v248, v48, v49\n"
"v_max_f32_e32 v247, v52, v53\n"
"v_max3_f32 v248, v248, v50, v51\n"
"v_max3_f32 v247, v247, v54, v55\n"
"v_max3_f32 v248, v248, v24, v25\n"
"v_max3_f32 v247, v247, v56, v57\n"
"v_max3_f32 v248, v248, v26, v27\n"
"v_max3_f32 v247, v247, v58, v59\n"
"v_mov_b32_e32 v246, v248\n"
"v_mov_b32_e32 v245, v247\n"
"s_nop 0\n"
"v_permlane16_swap_b32_e32 v248, v246\n"
"v_permlane16_swap_b32_e32 v247, v245\n"
"v_max_f32_e32 v248, v248, v246\n"
"v_max_f32_e32 v247, v247, v245\n"
"v_mov_b32_e32 v246, v248\n"
"v_mov_b32_e32 v245, v247\n"
"s_nop 0\n"
"v_permlane32_swap_b32_e32 v248, v246\n"
"v_permlane32_swap_b32_e32 v247, v245\n"
"v_max_f32_e32 v248, v248, v246\n"
"v_max_f32_e32 v247, v247, v245\n"
"v_med3_f32 v248, v248, v253, v250\n"
"v_med3_f32 v247, v247, v251, v249\n"
"v_min_f32_e64 v36, s98, -v248\n"
"v_min_f32_e64 v37, s98, -v248\n"
"v_min_f32_e64 v38, s98, -v248\n"
"v_min_f32_e64 v39, s98, -v248\n"
"v_min_f32_e64 v60, s98, -v247\n"
"v_min_f32_e64 v61, s98, -v247\n"
"v_min_f32_e64 v62, s98, -v247\n"
"v_min_f32_e64 v63, s98, -v247\n"
"v_mfma_f32_16x16x32_bf16 v[64:67], v[40:43], v[28:31], v[36:39]\n"
"ds_read_b128 v[68:71], v15 offset:20480\n"
"s_add_u32 s98, s69, 0x602000\n"
"buffer_load_dwordx4 a[32:35], v0, s[4:7], s98 offen sc1\n"
"v_sub_f32_e32 v48, v48, v248\n"
"v_sub_f32_e32 v49, v49, v248\n"
"v_sub_f32_e32 v50, v50, v248\n"
"v_mfma_f32_16x16x32_bf16 v[72:75], v[40:43], v[32:35], v[60:63]\n"
"buffer_load_dwordx4 a[36:39], v0, s[4:7], s98 offen offset:1024 sc1\n"
"v_sub_f32_e32 v51, v51, v248\n"
"v_sub_f32_e32 v24, v24, v248\n"
"v_sub_f32_e32 v25, v25, v248\n"
"v_sub_f32_e32 v26, v26, v248\n"
"v_sub_f32_e32 v27, v27, v248\n"
"v_mfma_f32_16x16x32_bf16 v[40:43], v[44:47], v[32:35], v[60:63]\n"
"ds_read_b128 v[76:79], v254 offset:20736\n"
"v_sub_f32_e32 v52, v52, v247\n"
"v_sub_f32_e32 v53, v53, v247\n"
"v_sub_f32_e32 v54, v54, v247\n"
"v_sub_f32_e32 v55, v55, v247\n"
"v_mfma_f32_16x16x32_bf16 v[80:83], v[44:47], v[28:31], v[36:39]\n"
"buffer_load_dwordx4 a[40:43], v0, s[4:7], s98 offen offset:2048 sc1\n"
"v_sub_f32_e32 v56, v56, v247\n"
"v_sub_f32_e32 v57, v57, v247\n"
"v_sub_f32_e32 v58, v58, v247\n"
"v_sub_f32_e32 v59, v59, v247\n"
"s_waitcnt lgkmcnt(0)\n"
"v_mfma_f32_16x16x32_bf16 v[44:47], v[68:71], v[28:31], v[36:39]\n"
"ds_read_b128 v[84:87], v15 offset:18432\n"
"buffer_load_dwordx4 a[44:47], v0, s[4:7], s98 offen offset:3072 sc1\n"
"ds_read_b128 v[100:103], v254 offset:18688\n"
"v_exp_f32_e32 v64, v64\n"
"v_exp_f32_e32 v65, v65\n"
"v_exp_f32_e32 v66, v66\n"
"v_exp_f32_e32 v67, v67\n"
"v_exp_f32_e32 v80, v80\n"
"v_mfma_f32_16x16x32_bf16 v[104:107], v[68:71], v[32:35], v[60:63]\n"
"ds_read_b64_tr_b16 v[68:69], v16 offset:22528\n"
"buffer_load_dwordx4 a[48:51], v1, s[4:7], s98 offen sc1\n"
"v_exp_f32_e32 v81, v81\n"
"v_exp_f32_e32 v82, v82\n"
"v_exp_f32_e32 v83, v83\n"
"v_cvt_pk_bf16_f32 v108, v64, v65\n"
"v_cvt_pk_bf16_f32 v109, v66, v67\n"
"v_cvt_pk_bf16_f32 v110, v80, v81\n"
"v_mfma_f32_16x16x32_bf16 v[64:67], v[76:79], v[32:35], v[60:63]\n"
"ds_read_b64_tr_b16 v[70:71], v255 offset:22784\n"
"ds_read_b64_tr_b16 v[80:81], v255 offset:22528\n"
"buffer_load_dwordx4 a[52:55], v1, s[4:7], s98 offen offset:1024 sc1\n"
"v_cvt_pk_bf16_f32 v111, v82, v83\n"
"v_exp_f32_e32 v72, v72\n"
"v_exp_f32_e32 v73, v73\n"
"v_exp_f32_e32 v74, v74\n"
"v_exp_f32_e32 v75, v75\n"
"v_mfma_f32_16x16x32_bf16 v[112:115], v[76:79], v[28:31], v[36:39]\n"
"ds_read_b64_tr_b16 v[82:83], v16 offset:22784\n"
"v_exp_f32_e32 v40, v40\n"
"v_exp_f32_e32 v41, v41\n"
"v_exp_f32_e32 v42, v42\n"
"v_exp_f32_e32 v43, v43\n"
"v_cvt_pk_bf16_f32 v76, v72, v73\n"
"v_cvt_pk_bf16_f32 v77, v74, v75\n"
"v_cvt_pk_bf16_f32 v78, v40, v41\n"
"v_cvt_pk_bf16_f32 v79, v42, v43\n"
"s_waitcnt lgkmcnt(4)\n"
"v_mfma_f32_16x16x32_bf16 v[40:43], v[84:87], v[28:31], v[36:39]\n"
"ds_read_b128 v[72:75], v15 offset:16384\n"
"v_exp_f32_e32 v44, v44\n"
"v_exp_f32_e32 v45, v45\n"
"v_exp_f32_e32 v46, v46\n"
"v_mfma_f32_16x16x32_bf16 v[116:119], v[84:87], v[32:35], v[60:63]\n"
"buffer_load_dwordx4 a[56:59], v1, s[4:7], s98 offen offset:2048 sc1\n"
"v_exp_f32_e32 v47, v47\n"
"v_mfma_f32_16x16x32_bf16 v[84:87], v[100:103], v[32:35], v[60:63]\n"
"ds_read_b128 v[120:123], v254 offset:16640\n"
"v_exp_f32_e32 v112, v112\n"
"v_exp_f32_e32 v113, v113\n"
"v_mfma_f32_16x16x32_bf16 v[124:127], v[100:103], v[28:31], v[36:39]\n"
"ds_read_b64_tr_b16 v[100:101], v16 offset:20480\n"
"v_exp_f32_e32 v114, v114\n"
"v_exp_f32_e32 v115, v115\n"
"v_cvt_pk_bf16_f32 v128, v44, v45\n"
"s_waitcnt lgkmcnt(3)\n"
"v_mfma_f32_16x16x32_bf16 v[132:135], v[88:91], v[108:111], 0\n"
"buffer_load_dwordx4 a[60:63], v1, s[4:7], s98 offen offset:3072 sc1\n"
"v_cvt_pk_bf16_f32 v129, v46, v47\n"
"v_cvt_pk_bf16_f32 v130, v112, v113\n"
"v_cvt_pk_bf16_f32 v131, v114, v115\n"
"v_mfma_f32_16x16x32_bf16 v[44:47], v[68:71], v[108:111], 0\n"
"ds_read_b64_tr_b16 v[102:103], v255 offset:20736\n"
"v_exp_f32_e32 v104, v104\n"
"v_exp_f32_e32 v105, v105\n"
"v_mfma_f32_16x16x32_bf16 v[112:115], v[80:83], v[108:111], 0\n"
"ds_read_b64_tr_b16 v[108:109], v255 offset:20480\n"
"s_add_u32 s98, s69, 0x604000\n"
"buffer_load_dwordx4 a[64:67], v0, s[4:7], s98 offen sc1\n"
"v_exp_f32_e32 v106, v106\n"
"v_exp_f32_e32 v107, v107\n"
"v_mfma_f32_16x16x32_bf16 v[136:139], v[80:83], v[76:79], 0\n"
"v_exp_f32_e32 v64, v64\n"
"v_exp_f32_e32 v65, v65\n"
"v_mfma_f32_16x16x32_bf16 v[80:83], v[68:71], v[76:79], 0\n"
"ds_read_b64_tr_b16 v[110:111], v16 offset:20736\n"
"v_exp_f32_e32 v66, v66\n"
"v_exp_f32_e32 v67, v67\n"
"v_mfma_f32_16x16x32_bf16 v[68:71], v[88:91], v[76:79], 0\n"
"s_waitcnt vmcnt(19)\n"
"v_cvt_pk_bf16_f32 v76, v104, v105\n"
"v_cvt_pk_bf16_f32 v77, v106, v107\n"
"v_cvt_pk_bf16_f32 v78, v64, v65\n"
"v_cvt_pk_bf16_f32 v79, v66, v67\n"
"s_cmp_lg_u32 s2, 0\n"
"s_cbranch_scc1 .Lmk_s93\n"
"s_barrier\n"
".Lmk_s93:\n"
"s_waitcnt lgkmcnt(4)\n"
"v_mfma_f32_16x16x32_bf16 v[64:67], v[72:75], v[28:31], v[36:39]\n"
"ds_read_b128 v[104:107], v15 offset:14336\n"
"v_exp_f32_e32 v40, v40\n"
"v_exp_f32_e32 v41, v41\n"
"v_exp_f32_e32 v42, v42\n"
"v_mfma_f32_16x16x32_bf16 v[140:143], v[72:75], v[32:35], v[60:63]\n"
"buffer_load_dwordx4 a[68:71], v0, s[4:7], s98 offen offset:1024 sc1\n"
"v_exp_f32_e32 v43, v43\n"
"v_mfma_f32_16x16x32_bf16 v[72:75], v[120:123], v[32:35], v[60:63]\n"
"ds_read_b128 v[144:147], v254 offset:14592\n"
"v_exp_f32_e32 v124, v124\n"
"v_exp_f32_e32 v125, v125\n"
"v_mfma_f32_16x16x32_bf16 v[148:151], v[120:123], v[28:31], v[36:39]\n"
"ds_read_b64_tr_b16 v[120:121], v16 offset:18432\n"
"v_exp_f32_e32 v126, v126\n"
"v_exp_f32_e32 v127, v127\n"
"s_waitcnt lgkmcnt(3)\n"
"v_mfma_f32_16x16x32_bf16 v[132:135], v[88:91], v[128:131], v[132:135]\n"
"buffer_load_dwordx4 a[72:75], v0, s[4:7], s98 offen offset:2048 sc1\n"
"v_cvt_pk_bf16_f32 v152, v40, v41\n"
"v_cvt_pk_bf16_f32 v153, v42, v43\n"
"v_cvt_pk_bf16_f32 v154, v124, v125\n"
"v_mfma_f32_16x16x32_bf16 v[44:47], v[100:103], v[128:131], v[44:47]\n"
"ds_read_b64_tr_b16 v[122:123], v255 offset:18688\n"
"v_cvt_pk_bf16_f32 v155, v126, v127\n"
"v_exp_f32_e32 v116, v116\n"
"v_exp_f32_e32 v117, v117\n"
"v_mfma_f32_16x16x32_bf16 v[112:115], v[108:111], v[128:131], v[112:115]\n"
"ds_read_b64_tr_b16 v[40:41], v255 offset:18432\n"
"v_exp_f32_e32 v118, v118\n"
"v_exp_f32_e32 v119, v119\n"
"v_mfma_f32_16x16x32_bf16 v[136:139], v[108:111], v[76:79], v[136:139]\n"
"buffer_load_dwordx4 a[76:79], v0, s[4:7], s98 offen offset:3072 sc1\n"
"v_exp_f32_e32 v84, v84\n"
"v_exp_f32_e32 v85, v85\n"
"v_mfma_f32_16x16x32_bf16 v[80:83], v[100:103], v[76:79], v[80:83]\n"
"ds_read_b64_tr_b16 v[42:43], v16 offset:18688\n"
"v_exp_f32_e32 v86, v86\n"
"v_exp_f32_e32 v87, v87\n"
"v_mfma_f32_16x16x32_bf16 v[68:71], v[88:91], v[76:79], v[68:71]\n"
"s_waitcnt lgkmcnt(4)\n"
"v_cvt_pk_bf16_f32 v76, v116, v117\n"
"v_cvt_pk_bf16_f32 v77, v118, v119\n"
"v_cvt_pk_bf16_f32 v78, v84, v85\n"
"v_cvt_pk_bf16_f32 v79, v86, v87\n"
"s_cmp_eq_i32 s78, 8\n"
"s_cbranch_scc1 .Lmk_x94s\n"
".Lmk_x94r:\n"
"v_mfma_f32_16x16x32_bf16 v[84:87], v[104:107], v[28:31], v[36:39]\n"
"ds_read_b128 v[100:103], v15 offset:12288\n"
"v_exp_f32_e32 v64, v64\n"
"v_exp_f32_e32 v65, v65\n"
"v_exp_f32_e32 v66, v66\n"
"v_mfma_f32_16x16x32_bf16 v[108:111], v[104:107], v[32:35], v[60:63]\n"
"buffer_load_dwordx4 a[80:83], v1, s[4:7], s98 offen sc1\n"
"v_exp_f32_e32 v67, v67\n"
"v_mfma_f32_16x16x32_bf16 v[104:107], v[144:147], v[32:35], v[60:63]\n"
"ds_read_b128 v[116:119], v254 offset:12544\n"
"v_exp_f32_e32 v148, v148\n"
"v_exp_f32_e32 v149, v149\n"
"v_mfma_f32_16x16x32_bf16 v[124:127], v[144:147], v[28:31], v[36:39]\n"
"ds_read_b64_tr_b16 v[128:129], v16 offset:16384\n"
"v_exp_f32_e32 v150, v150\n"
"v_exp_f32_e32 v151, v151\n"
"s_waitcnt lgkmcnt(3)\n"
"v_mfma_f32_16x16x32_bf16 v[132:135], v[88:91], v[152:155], v[132:135]\n"
"buffer_load_dwordx4 a[84:87], v1, s[4:7], s98 offen offset:1024 sc1\n"
"v_cvt_pk_bf16_f32 v144, v64, v65\n"
"v_cvt_pk_bf16_f32 v145, v66, v67\n"
"v_cvt_pk_bf16_f32 v146, v148, v149\n"
"v_mfma_f32_16x16x32_bf16 v[44:47], v[120:123], v[152:155], v[44:47]\n"
"ds_read_b64_tr_b16 v[130:131], v255 offset:16640\n"
"v_cvt_pk_bf16_f32 v147, v150, v151\n"
"v_exp_f32_e32 v140, v140\n"
"v_exp_f32_e32 v141, v141\n"
"v_mfma_f32_16x16x32_bf16 v[112:115], v[40:43], v[152:155], v[112:115]\n"
"ds_read_b64_tr_b16 v[64:65], v255 offset:16384\n"
"v_exp_f32_e32 v142, v142\n"
"v_exp_f32_e32 v143, v143\n"
"v_mfma_f32_16x16x32_bf16 v[136:139], v[40:43], v[76:79], v[136:139]\n"
"buffer_load_dwordx4 a[88:91], v1, s[4:7], s98 offen offset:2048 sc1\n"
"v_exp_f32_e32 v72, v72\n"
"v_exp_f32_e32 v73, v73\n"
"v_mfma_f32_16x16x32_bf16 v[80:83], v[120:123], v[76:79], v[80:83]\n"
"ds_read_b64_tr_b16 v[66:67], v16 offset:16640\n"
"v_exp_f32_e32 v74, v74\n"
"v_exp_f32_e32 v75, v75\n"
"v_mfma_f32_16x16x32_bf16 v[68:71], v[88:91], v[76:79], v[68:71]\n"
"s_waitcnt lgkmcnt(4)\n"
"v_cvt_pk_bf16_f32 v40, v140, v141\n"
"v_cvt_pk_bf16_f32 v41, v142, v143\n"
"v_cvt_pk_bf16_f32 v42, v72, v73\n"
"v_cvt_pk_bf16_f32 v43, v74, v75\n"
"s_cmp_eq_i32 s78, 7\n"
"s_cbranch_scc1 .Lmk_x95s\n"
".Lmk_x95r:\n"
"v_mfma_f32_16x16x32_bf16 v[72:75], v[100:103], v[28:31], v[36:39]\n"
"ds_read_b128 v[76:79], v15 offset:10240\n"
"v_exp_f32_e32 v84, v84\n"
"v_exp_f32_e32 v85, v85\n"
"v_exp_f32_e32 v86, v86\n"
"v_mfma_f32_16x16x32_bf16 v[120:123], v[100:103], v[32:35], v[60:63]\n"
"buffer_load_dwordx4 a[92:95], v1, s[4:7], s98 offen offset:3072 sc1\n"
"v_exp_f32_e32 v87, v87\n"
"v_mfma_f32_16x16x32_bf16 v[100:103], v[116:119], v[32:35], v[60:63]\n"
"ds_read_b128 v[140:143], v254 offset:10496\n"
"v_exp_f32_e32 v124, v124\n"
"v_exp_f32_e32 v125, v125\n"
"v_mfma_f32_16x16x32_bf16 v[148:151], v[116:119], v[28:31], v[36:39]\n"
"ds_read_b64_tr_b16 v[116:117], v16 offset:14336\n"
"v_exp_f32_e32 v126, v126\n"
"v_exp_f32_e32 v127, v127\n"
"v_cvt_pk_bf16_f32 v152, v84, v85\n"
"s_waitcnt lgkmcnt(3)\n"
"v_mfma_f32_16x16x32_bf16 v[132:135], v[88:91], v[144:147], v[132:135]\n"
"s_add_u32 s98, s69, 0x606000\n"
"buffer_load_dwordx4 a[96:99], v0, s[4:7], s98 offen sc1\n"
"v_cvt_pk_bf16_f32 v153, v86, v87\n"
"v_cvt_pk_bf16_f32 v154, v124, v125\n"
"v_mfma_f32_16x16x32_bf16 v[44:47], v[128:131], v[144:147], v[44:47]\n"
"ds_read_b64_tr_b16 v[118:119], v255 offset:14592\n"
"v_cvt_pk_bf16_f32 v155, v126, v127\n"
"v_exp_f32_e32 v108, v108\n"
"v_exp_f32_e32 v109, v109\n"
"v_mfma_f32_16x16x32_bf16 v[112:115], v[64:67], v[144:147], v[112:115]\n"
"ds_read_b64_tr_b16 v[84:85], v255 offset:14336\n"
"v_exp_f32_e32 v110, v110\n"
"v_exp_f32_e32 v111, v111\n"
"v_mfma_f32_16x16x32_bf16 v[136:139], v[64:67], v[40:43], v[136:139]\n"
"buffer_load_dwordx4 a[100:103], v0, s[4:7], s98 offen offset:1024 sc1\n"
"v_exp_f32_e32 v104, v104\n"
"v_exp_f32_e32 v105, v105\n"
"v_mfma_f32_16x16x32_bf16 v[80:83], v[128:131], v[40:43], v[80:83]\n"
"ds_read_b64_tr_b16 v[86:87], v16 offset:14592\n"
"v_exp_f32_e32 v106, v106\n"
"v_exp_f32_e32 v107, v107\n"
"v_mfma_f32_16x16x32_bf16 v[68:71], v[88:91], v[40:43], v[68:71]\n"
"s_waitcnt lgkmcnt(4)\n"
"v_cvt_pk_bf16_f32 v40, v108, v109\n"
"v_cvt_pk_bf16_f32 v41, v110, v111\n"
"v_cvt_pk_bf16_f32 v42, v104, v105\n"
"v_cvt_pk_bf16_f32 v43, v106, v107\n"
"s_cmp_eq_i32 s78, 6\n"
"s_cbranch_scc1 .Lmk_x96s\n"
".Lmk_x96r:\n"
"v_mfma_f32_16x16x32_bf16 v[64:67], v[76:79], v[28:31], v[36:39]\n"
"ds_read_b128 v[104:107], v15 offset:8192\n"
"v_exp_f32_e32 v72, v72\n"
"v_exp_f32_e32 v73, v73\n"
"v_exp_f32_e32 v74, v74\n"
"v_mfma_f32_16x16x32_bf16 v[108:111], v[76:79], v[32:35], v[60:63]\n"
"buffer_load_dwordx4 a[104:107], v0, s[4:7], s98 offen offset:2048 sc1\n"
"v_exp_f32_e32 v75, v75\n"
"v_mfma_f32_16x16x32_bf16 v[76:79], v[140:143], v[32:35], v[60:63]\n"
"ds_read_b128 v[124:127], v254 offset:8448\n"
"v_exp_f32_e32 v148, v148\n"
"v_exp_f32_e32 v149, v149\n"
"v_mfma_f32_16x16x32_bf16 v[128:131], v[140:143], v[28:31], v[36:39]\n"
"ds_read_b64_tr_b16 v[140:141], v16 offset:12288\n"
"v_exp_f32_e32 v150, v150\n"
"v_exp_f32_e32 v151, v151\n"
"s_waitcnt lgkmcnt(3)\n"
"v_mfma_f32_16x16x32_bf16 v[132:135], v[88:91], v[152:155], v[132:135]\n"
"buffer_load_dwordx4 a[108:111], v0, s[4:7], s98 offen offset:3072 sc1\n"
"v_cvt_pk_bf16_f32 v144, v72, v73\n"
"v_cvt_pk_bf16_f32 v145, v74, v75\n"
"v_cvt_pk_bf16_f32 v146, v148, v149\n"
"v_mfma_f32_16x16x32_bf16 v[44:47], v[116:119], v[152:155], v[44:47]\n"
"ds_read_b64_tr_b16 v[142:143], v255 offset:12544\n"
"v_cvt_pk_bf16_f32 v147, v150, v151\n"
"v_exp_f32_e32 v120, v120\n"
"v_exp_f32_e32 v121, v121\n"
"v_mfma_f32_16x16x32_bf16 v[112:115], v[84:87], v[152:155], v[112:115]\n"
"ds_read_b64_tr_b16 v[72:73], v255 offset:12288\n"
"v_exp_f32_e32 v122, v122\n"
"v_exp_f32_e32 v123, v123\n"
"v_mfma_f32_16x16x32_bf16 v[136:139], v[84:87], v[40:43], v[136:139]\n"
"buffer_load_dwordx4 a[112:115], v1, s[4:7], s98 offen sc1\n"
"v_exp_f32_e32 v100, v100\n"
"v_exp_f32_e32 v101, v101\n"
"v_mfma_f32_16x16x32_bf16 v[80:83], v[116:119], v[40:43], v[80:83]\n"
"ds_read_b64_tr_b16 v[74:75], v16 offset:12544\n"
"v_exp_f32_e32 v102, v102\n"
"v_exp_f32_e32 v103, v103\n"
"v_mfma_f32_16x16x32_bf16 v[68:71], v[88:91], v[40:43], v[68:71]\n"
"s_waitcnt lgkmcnt(4)\n"
"v_cvt_pk_bf16_f32 v40, v120, v121\n"
"v_cvt_pk_bf16_f32 v41, v122, v123\n"
"v_cvt_pk_bf16_f32 v42, v100, v101\n"
"v_cvt_pk_bf16_f32 v43, v102, v103\n"
"s_cmp_eq_i32 s78, 5\n"
"s_cbranch_scc1 .Lmk_x97s\n"
".Lmk_x97r:\n"
"v_mfma_f32_16x16x32_bf16 v[84:87], v[104:107], v[28:31], v[36:39]\n"
"ds_read_b64_tr_b16 v[100:101], v16 offset:10240\n"
"ds_read_b64_tr_b16 v[102:103], v255 offset:10496\n"
"v_exp_f32_e32 v64, v64\n"
"v_exp_f32_e32 v65, v65\n"
"v_mfma_f32_16x16x32_bf16 v[116:119], v[104:107], v[32:35], v[60:63]\n"
"buffer_load_dwordx4 a[116:119], v1, s[4:7], s98 offen offset:1024 sc1\n"
"v_exp_f32_e32 v66, v66\n"
"v_exp_f32_e32 v67, v67\n"
"v_mfma_f32_16x16x32_bf16 v[104:107], v[124:127], v[32:35], v[60:63]\n"
"ds_read_b64_tr_b16 v[60:61], v255 offset:10240\n"
"ds_read_b64_tr_b16 v[62:63], v16 offset:10496\n"
"v_exp_f32_e32 v128, v128\n"
"v_exp_f32_e32 v129, v129\n"
"v_mfma_f32_16x16x32_bf16 v[120:123], v[124:127], v[28:31], v[36:39]\n"
"ds_read_b64_tr_b16 v[36:37], v16 offset:24576\n"
"v_exp_f32_e32 v130, v130\n"
"v_exp_f32_e32 v131, v131\n"
"s_waitcnt lgkmcnt(5)\n"
"v_mfma_f32_16x16x32_bf16 v[132:135], v[88:91], v[144:147], v[132:135]\n"
"buffer_load_dwordx4 a[120:123], v1, s[4:7], s98 offen offset:2048 sc1\n"
"ds_read_b64_tr_b16 v[38:39], v255 offset:24832\n"
"v_cvt_pk_bf16_f32 v124, v64, v65\n"
"v_cvt_pk_bf16_f32 v125, v66, v67\n"
"v_cvt_pk_bf16_f32 v126, v128, v129\n"
"v_mfma_f32_16x16x32_bf16 v[44:47], v[140:143], v[144:147], v[44:47]\n"
"ds_read_b64_tr_b16 v[64:65], v255 offset:24576\n"
"v_cvt_pk_bf16_f32 v127, v130, v131\n"
"v_exp_f32_e32 v108, v108\n"
"v_exp_f32_e32 v109, v109\n"
"v_mfma_f32_16x16x32_bf16 v[112:115], v[72:75], v[144:147], v[112:115]\n"
"ds_read_b64_tr_b16 v[66:67], v16 offset:24832\n"
"v_exp_f32_e32 v110, v110\n"
"v_exp_f32_e32 v111, v111\n"
"v_mfma_f32_16x16x32_bf16 v[136:139], v[72:75], v[40:43], v[136:139]\n"
"buffer_load_dwordx4 a[124:127], v1, s[4:7], s98 offen offset:3072 sc1\n"
"v_exp_f32_e32 v76, v76\n"
"v_add_u32_e32 v248, 0x26400, v0\n"
"v_exp_f32_e32 v77, v77\n"
"v_mfma_f32_16x16x32_bf16 v[80:83], v[140:143], v[40:43], v[80:83]\n"
"ds_read_b128 v[72:75], v248\n"
"v_exp_f32_e32 v78, v78\n"
"v_exp_f32_e32 v79, v79\n"
"v_mfma_f32_16x16x32_bf16 v[68:71], v[88:91], v[40:43], v[68:71]\n"
"ds_read_b128 v[128:131], v248 offset:1024\n"
"v_cvt_pk_bf16_f32 v40, v108, v109\n"
"v_cvt_pk_bf16_f32 v41, v110, v111\n"
"v_cvt_pk_bf16_f32 v42, v76, v77\n"
"v_cvt_pk_bf16_f32 v43, v78, v79\n"
"s_waitcnt lgkmcnt(6)\n"
"v_mfma_f32_16x16x32_bf16 v[132:135], v[88:91], v[124:127], v[132:135]\n"
"ds_read_b64_tr_b16 v[76:77], v16 offset:8192\n"
"v_cndmask_b32_e64 v84, v48, v84, s[20:21]\n"
"v_cndmask_b32_e64 v85, v49, v85, s[22:23]\n"
"v_cndmask_b32_e64 v86, v50, v86, s[24:25]\n"
"s_add_u32 m0, s77, 0x4000\n"
"s_add_u32 s98, s65, 0\n"
"buffer_load_dwordx4 v2, s[4:7], s98 offen sc1 lds\n"
"buffer_load_dwordx4 v2, s[4:7], s98 offen offset:1024 sc1 lds\n"
"v_cndmask_b32_e64 v87, v51, v87, s[26:27]\n"
"v_cndmask_b32_e64 v120, v24, v120, s[28:29]\n"
"v_mfma_f32_16x16x32_bf16 v[44:47], v[100:103], v[124:127], v[44:47]\n"
"ds_read_b64_tr_b16 v[78:79], v255 offset:8448\n"
"v_cndmask_b32_e64 v121, v25, v121, s[30:31]\n"
"v_cndmask_b32_e64 v122, v26, v122, s[32:33]\n"
"v_cndmask_b32_e64 v123, v27, v123, s[34:35]\n"
"v_exp_f32_e32 v84, v84\n"
"v_exp_f32_e32 v85, v85\n"
"v_exp_f32_e32 v86, v86\n"
"v_exp_f32_e32 v87, v87\n"
"v_mfma_f32_16x16x32_bf16 v[112:115], v[60:63], v[124:127], v[112:115]\n"
"v_exp_f32_e32 v120, v120\n"
"v_exp_f32_e32 v121, v121\n"
"v_exp_f32_e32 v122, v122\n"
"v_exp_f32_e32 v123, v123\n"
"v_cvt_pk_bf16_f32 v48, v84, v85\n"
"v_cvt_pk_bf16_f32 v49, v86, v87\n"
"v_cvt_pk_bf16_f32 v50, v120, v121\n"
"v_mfma_f32_16x16x32_bf16 v[136:139], v[60:63], v[40:43], v[136:139]\n"
"ds_read_b64_tr_b16 v[60:61], v255 offset:8192\n"
"s_add_u32 s98, s69, 0x608000\n"
"buffer_load_dwordx4 v[24:27], v0, s[4:7], s98 offen sc1\n"
"v_cvt_pk_bf16_f32 v51, v122, v123\n"
"v_cndmask_b32_e64 v116, v52, v116, s[36:37]\n"
"v_cndmask_b32_e64 v117, v53, v117, s[38:39]\n"
"v_cndmask_b32_e64 v118, v54, v118, s[40:41]\n"
"v_cndmask_b32_e64 v119, v55, v119, s[42:43]\n"
"v_cndmask_b32_e64 v104, v56, v104, s[44:45]\n"
"v_cndmask_b32_e64 v105, v57, v105, s[46:47]\n"
"v_mfma_f32_16x16x32_bf16 v[80:83], v[100:103], v[40:43], v[80:83]\n"
"ds_read_b64_tr_b16 v[62:63], v16 offset:8448\n"
"v_cndmask_b32_e64 v106, v58, v106, s[48:49]\n"
"v_cndmask_b32_e64 v107, v59, v107, s[50:51]\n"
"v_exp_f32_e32 v116, v116\n"
"v_exp_f32_e32 v117, v117\n"
"v_exp_f32_e32 v118, v118\n"
"v_exp_f32_e32 v119, v119\n"
"v_exp_f32_e32 v104, v104\n"
"v_mfma_f32_16x16x32_bf16 v[68:71], v[88:91], v[40:43], v[68:71]\n"
"s_waitcnt lgkmcnt(0)\n"
"v_exp_f32_e32 v105, v105\n"
"v_exp_f32_e32 v106, v106\n"
"v_exp_f32_e32 v107, v107\n"
"v_cvt_pk_bf16_f32 v40, v116, v117\n"
"v_cvt_pk_bf16_f32 v41, v118, v119\n"
"v_cvt_pk_bf16_f32 v42, v104, v105\n"
"v_cvt_pk_bf16_f32 v43, v106, v107\n"
"v_mfma_f32_16x16x32_bf16 v[132:135], v[88:91], v[48:51], v[132:135]\n"
"v_and_b32_e32 v72, v48, v72\n"
"v_and_b32_e32 v73, v49, v73\n"
"v_and_b32_e32 v74, v50, v74\n"
"v_and_b32_e32 v75, v51, v75\n"
"v_mfma_f32_16x16x32_bf16 v[68:71], v[88:91], v[40:43], v[68:71]\n"
"v_xor_b32_e32 v52, v48, v72\n"
"v_xor_b32_e32 v53, v49, v73\n"
"v_xor_b32_e32 v54, v50, v74\n"
"v_xor_b32_e32 v55, v51, v75\n"
"v_mfma_f32_16x16x32_bf16 v[44:47], v[76:79], v[72:75], v[44:47]\n"
"v_and_b32_e32 v128, v40, v128\n"
"v_and_b32_e32 v129, v41, v129\n"
"v_mfma_f32_16x16x32_bf16 v[112:115], v[60:63], v[72:75], v[112:115]\n"
"v_and_b32_e32 v130, v42, v130\n"
"v_and_b32_e32 v131, v43, v131\n"
"v_mfma_f32_16x16x32_bf16 v[44:47], v[36:39], v[52:55], v[44:47]\n"
"v_xor_b32_e32 v48, v40, v128\n"
"v_xor_b32_e32 v49, v41, v129\n"
"v_mfma_f32_16x16x32_bf16 v[112:115], v[64:67], v[52:55], v[112:115]\n"
"v_xor_b32_e32 v50, v42, v130\n"
"v_xor_b32_e32 v51, v43, v131\n"
"v_mfma_f32_16x16x32_bf16 v[80:83], v[76:79], v[128:131], v[80:83]\n"
"s_add_u32 m0, s77, 0xc000\n"
"s_add_u32 s97, s65, 0x4000\n"
"buffer_load_dwordx4 v2, s[4:7], s97 offen sc1 lds\n"
"buffer_load_dwordx4 v2, s[4:7], s97 offen offset:1024 sc1 lds\n"
"v_mfma_f32_16x16x32_bf16 v[136:139], v[60:63], v[128:131], v[136:139]\n"
"v_mfma_f32_16x16x32_bf16 v[80:83], v[36:39], v[48:51], v[80:83]\n"
"v_mfma_f32_16x16x32_bf16 v[136:139], v[64:67], v[48:51], v[136:139]\n"
"v_max_f32_e32 v248, v132, v68\n"
"v_rcp_f32_e32 v132, v132\n"
"v_rcp_f32_e32 v68, v68\n"
"s_nop 0\n"
"v_max3_f32 v248, v248, v132, v68\n"
"v_cmp_nlt_f32_e32 vcc, v248, v252\n"
"s_cbranch_vccnz .Lmk_b92\n"
".Lmk_k98:\n"
"s_add_u32 s97, s60, 0x40000\n"
"v_mul_f32_e32 v44, v44, v132\n"
"v_mul_f32_e32 v45, v45, v132\n"
"v_mul_f32_e32 v46, v46, v132\n"
"v_mul_f32_e32 v47, v47, v132\n"
"v_mul_f32_e32 v112, v112, v132\n"
"v_mul_f32_e32 v113, v113, v132\n"
"v_mul_f32_e32 v114, v114, v132\n"
"v_mul_f32_e32 v115, v115, v132\n"
"v_cvt_pk_bf16_f32 v28, v44, v45\n"
"v_cvt_pk_bf16_f32 v29, v46, v47\n"
"v_cvt_pk_bf16_f32 v30, v112, v113\n"
"v_cvt_pk_bf16_f32 v31, v114, v115\n"
"buffer_store_dwordx4 v[28:31], v0, s[4:7], s97 offen sc1\n"
"s_add_u32 s97, s60, 0x48000\n"
"v_mul_f32_e32 v80, v80, v68\n"
"v_mul_f32_e32 v81, v81, v68\n"
"v_mul_f32_e32 v82, v82, v68\n"
"v_mul_f32_e32 v83, v83, v68\n"
"v_mul_f32_e32 v136, v136, v68\n"
"v_mul_f32_e32 v137, v137, v68\n"
"v_mul_f32_e32 v138, v138, v68\n"
"v_mul_f32_e32 v139, v139, v68\n"
"v_cvt_pk_bf16_f32 v28, v80, v81\n"
"v_cvt_pk_bf16_f32 v29, v82, v83\n"
"v_cvt_pk_bf16_f32 v30, v136, v137\n"
"v_cvt_pk_bf16_f32 v31, v138, v139\n"
"buffer_store_dwordx4 v[28:31], v0, s[4:7], s97 offen sc1\n"
";\n"
"; @A AT a\n"
"s_mov_b32 s2, 0\n"
"v_mov_b32_e32 v253, 0xff800000\n"
"v_mov_b32_e32 v251, 0xff800000\n"
"v_mov_b32_e32 v250, 0x7f800000\n"
"v_mov_b32_e32 v249, 0x7f800000\n"
".Lmk_a99:\n"
"ds_read_b128 v[28:31], v15 offset:16384\n"
"ds_read_b128 v[32:35], v254 offset:16640\n"
"ds_read_b128 v[36:39], v15 offset:14336\n"
"ds_read_b128 v[40:43], v254 offset:14592\n"
"s_cmp_gt_i32 s78, 7\n"
"s_cselect_b32 s97, 0x80000000, 0\n"
"s_or_b32 s97, s97, 0x7f800000\n"
"s_add_u32 s96, s67, 32\n"
"buffer_load_dwordx2 v[246:247], v12, s[4:7], s96 offen sc1\n"
"s_waitcnt lgkmcnt(2)\n"
"v_mfma_f32_16x16x32_bf16 v[100:103], v[28:31], v[92:95], 0\n"
"v_mfma_f32_16x16x32_bf16 v[104:107], v[28:31], v[96:99], 0\n"
"v_mfma_f32_16x16x32_bf16 v[108:111], v[32:35], v[96:99], 0\n"
"v_mfma_f32_16x16x32_bf16 v[112:115], v[32:35], v[92:95], 0\n"
"s_waitcnt lgkmcnt(0)\n"
"s_nop 3\n"
"v_max_f32_e32 v248, v100, v101\n"
"v_max_f32_e32 v245, v104, v105\n"
"v_max3_f32 v248, v248, v102, v103\n"
"v_max3_f32 v245, v245, v106, v107\n"
"v_max3_f32 v248, v248, v112, v113\n"
"v_max3_f32 v245, v245, v108, v109\n"
"v_max3_f32 v248, v248, v114, v115\n"
"v_max3_f32 v245, v245, v110, v111\n"
"v_mov_b32_e32 v244, v248\n"
"v_mov_b32_e32 v243, v245\n"
"s_nop 0\n"
"v_permlane16_swap_b32_e32 v248, v244\n"
"v_permlane16_swap_b32_e32 v245, v243\n"
"v_max_f32_e32 v248, v248, v244\n"
"v_max_f32_e32 v245, v245, v243\n"
"v_mov_b32_e32 v244, v248\n"
"v_mov_b32_e32 v243, v245\n"
"s_nop 0\n"
"v_permlane32_swap_b32_e32 v248, v244\n"
"v_permlane32_swap_b32_e32 v245, v243\n"
"v_max_f32_e32 v248, v248, v244\n"
"v_max_f32_e32 v245, v245, v243\n"
"v_med3_f32 v248, v248, v253, v250\n"
"v_med3_f32 v245, v245, v251, v249\n"
"v_min_f32_e64 v116, s97, -v248\n"
"v_min_f32_e64 v117, s97, -v248\n"
"v_min_f32_e64 v118, s97, -v248\n"
"v_min_f32_e64 v119, s97, -v248\n"
"v_min_f32_e64 v120, s97, -v245\n"
"v_min_f32_e64 v121, s97, -v245\n"
"v_min_f32_e64 v122, s97, -v245\n"
"v_min_f32_e64 v123, s97, -v245\n"
"v_mfma_f32_16x16x32_bf16 v[44:47], v[36:39], v[92:95], v[116:119]\n"
"ds_read_b128 v[32:35], v15 offset:12288\n"
"v_sub_f32_e32 v100, v100, v248\n"
"v_sub_f32_e32 v101, v101, v248\n"
"v_sub_f32_e32 v102, v102, v248\n"
"v_sub_f32_e32 v103, v103, v248\n"
"v_mfma_f32_16x16x32_bf16 v[48:51], v[36:39], v[96:99], v[120:123]\n"
"v_sub_f32_e32 v112, v112, v248\n"
"v_sub_f32_e32 v113, v113, v248\n"
"v_sub_f32_e32 v114, v114, v248\n"
"v_sub_f32_e32 v115, v115, v248\n"
"v_mfma_f32_16x16x32_bf16 v[52:55], v[40:43], v[96:99], v[120:123]\n"
"ds_read_b128 v[56:59], v254 offset:12544\n"
"v_sub_f32_e32 v104, v104, v245\n"
"v_sub_f32_e32 v105, v105, v245\n"
"v_sub_f32_e32 v106, v106, v245\n"
"v_sub_f32_e32 v107, v107, v245\n"
"v_mfma_f32_16x16x32_bf16 v[36:39], v[40:43], v[92:95], v[116:119]\n"
"s_waitcnt lgkmcnt(0)\n"
"v_sub_f32_e32 v108, v108, v245\n"
"v_sub_f32_e32 v109, v109, v245\n"
"v_sub_f32_e32 v110, v110, v245\n"
"v_sub_f32_e32 v111, v111, v245\n"
"s_cmp_eq_i32 s78, 7\n"
"s_cbranch_scc1 .Lmk_x101s\n"
".Lmk_x101r:\n"
"v_mfma_f32_16x16x32_bf16 v[60:63], v[32:35], v[92:95], v[116:119]\n"
"ds_read_b128 v[40:43], v15 offset:10240\n"
"buffer_load_dwordx4 v[28:31], v0, s[4:7], s98 offen offset:1024 sc1\n"
"ds_read_b128 v[64:67], v254 offset:10496\n"
"v_exp_f32_e32 v44, v44\n"
"v_exp_f32_e32 v45, v45\n"
"v_exp_f32_e32 v46, v46\n"
"v_exp_f32_e32 v47, v47\n"
"v_exp_f32_e32 v36, v36\n"
"v_mfma_f32_16x16x32_bf16 v[68:71], v[32:35], v[96:99], v[120:123]\n"
"ds_read_b64_tr_b16 v[72:73], v16 offset:14336\n"
"buffer_load_dwordx4 v[32:35], v0, s[4:7], s98 offen offset:2048 sc1\n"
"v_exp_f32_e32 v37, v37\n"
"v_exp_f32_e32 v38, v38\n"
"v_exp_f32_e32 v39, v39\n"
"v_cvt_pk_bf16_f32 v76, v44, v45\n"
"v_cvt_pk_bf16_f32 v77, v46, v47\n"
"v_cvt_pk_bf16_f32 v78, v36, v37\n"
"v_mfma_f32_16x16x32_bf16 v[80:83], v[56:59], v[96:99], v[120:123]\n"
"ds_read_b64_tr_b16 v[74:75], v255 offset:14592\n"
"ds_read_b64_tr_b16 v[84:85], v255 offset:14336\n"
"v_cvt_pk_bf16_f32 v79, v38, v39\n"
"v_exp_f32_e32 v48, v48\n"
"v_exp_f32_e32 v49, v49\n"
"v_exp_f32_e32 v50, v50\n"
"v_exp_f32_e32 v51, v51\n"
"v_mfma_f32_16x16x32_bf16 v[124:127], v[56:59], v[92:95], v[116:119]\n"
"buffer_load_dwordx4 v[36:39], v0, s[4:7], s98 offen offset:3072 sc1\n"
"ds_read_b64_tr_b16 v[86:87], v16 offset:14592\n"
"v_exp_f32_e32 v52, v52\n"
"v_exp_f32_e32 v53, v53\n"
"v_exp_f32_e32 v54, v54\n"
"v_exp_f32_e32 v55, v55\n"
"v_cvt_pk_bf16_f32 v56, v48, v49\n"
"v_cvt_pk_bf16_f32 v57, v50, v51\n"
"v_cvt_pk_bf16_f32 v58, v52, v53\n"
"v_cvt_pk_bf16_f32 v59, v54, v55\n"
"s_waitcnt lgkmcnt(4)\n"
"s_cmp_eq_i32 s78, 6\n"
"s_cbranch_scc1 .Lmk_x102s\n"
".Lmk_x102r:\n"
"v_mfma_f32_16x16x32_bf16 v[128:131], v[40:43], v[92:95], v[116:119]\n"
"ds_read_b128 v[132:135], v15 offset:8192\n"
"ds_read_b128 v[136:139], v254 offset:8448\n"
"v_exp_f32_e32 v60, v60\n"
"v_exp_f32_e32 v61, v61\n"
"v_mfma_f32_16x16x32_bf16 v[140:143], v[40:43], v[96:99], v[120:123]\n"
"buffer_load_dwordx4 v[40:43], v1, s[4:7], s98 offen sc1\n"
"v_exp_f32_e32 v62, v62\n"
"v_exp_f32_e32 v63, v63\n"
"v_mfma_f32_16x16x32_bf16 v[144:147], v[64:67], v[96:99], v[120:123]\n"
"ds_read_b64_tr_b16 v[148:149], v16 offset:12288\n"
"ds_read_b64_tr_b16 v[150:151], v255 offset:12544\n"
"v_exp_f32_e32 v124, v124\n"
"v_exp_f32_e32 v125, v125\n"
"v_mfma_f32_16x16x32_bf16 v[152:155], v[64:67], v[92:95], v[116:119]\n"
"ds_read_b64_tr_b16 v[64:65], v255 offset:12288\n"
"v_exp_f32_e32 v126, v126\n"
"v_exp_f32_e32 v127, v127\n"
"s_waitcnt lgkmcnt(5)\n"
"v_mfma_f32_16x16x32_bf16 v[156:159], v[88:91], v[76:79], 0\n"
"buffer_load_dwordx4 v[44:47], v1, s[4:7], s98 offen offset:1024 sc1\n"
"v_cvt_pk_bf16_f32 v160, v60, v61\n"
"v_cvt_pk_bf16_f32 v161, v62, v63\n"
"v_cvt_pk_bf16_f32 v162, v124, v125\n"
"v_cvt_pk_bf16_f32 v163, v126, v127\n"
"v_mfma_f32_16x16x32_bf16 v[124:127], v[72:75], v[76:79], 0\n"
"ds_read_b64_tr_b16 v[66:67], v16 offset:12544\n"
"s_waitcnt vmcnt(8)\n"
"ds_read_b128 v[164:167], v2 offset:16384\n"
"v_exp_f32_e32 v68, v68\n"
"v_exp_f32_e32 v69, v69\n"
"v_mfma_f32_16x16x32_bf16 v[168:171], v[84:87], v[76:79], 0\n"
"v_exp_f32_e32 v70, v70\n"
"v_exp_f32_e32 v71, v71\n"
"v_mfma_f32_16x16x32_bf16 v[172:175], v[84:87], v[56:59], 0\n"
"buffer_load_dwordx4 v[48:51], v1, s[4:7], s98 offen offset:2048 sc1\n"
"ds_read_b128 v[76:79], v2 offset:17408\n"
"v_exp_f32_e32 v80, v80\n"
"v_exp_f32_e32 v81, v81\n"
"v_mfma_f32_16x16x32_bf16 v[176:179], v[72:75], v[56:59], 0\n"
"ds_read_b128 v[72:75], v2 offset:49152\n"
"v_exp_f32_e32 v82, v82\n"
"v_exp_f32_e32 v83, v83\n"
"v_mfma_f32_16x16x32_bf16 v[180:183], v[88:91], v[56:59], 0\n"
"ds_read_b128 v[84:87], v2 offset:50176\n"
"v_cvt_pk_bf16_f32 v184, v68, v69\n"
"v_cvt_pk_bf16_f32 v185, v70, v71\n"
"v_cvt_pk_bf16_f32 v186, v80, v81\n"
"v_cvt_pk_bf16_f32 v187, v82, v83\n"
"s_waitcnt lgkmcnt(8)\n"
"s_cmp_eq_i32 s78, 5\n"
"s_cbranch_scc1 .Lmk_x103s\n"
".Lmk_x103r:\n"
"v_mfma_f32_16x16x32_bf16 v[80:83], v[132:135], v[92:95], v[116:119]\n"
"ds_read_b64_tr_b16 v[68:69], v16 offset:10240\n"
"ds_read_b64_tr_b16 v[70:71], v255 offset:10496\n"
"buffer_load_dwordx4 v[52:55], v1, s[4:7], s98 offen offset:3072 sc1\n"
"v_exp_f32_e32 v128, v128\n"
"v_exp_f32_e32 v129, v129\n"
"v_mfma_f32_16x16x32_bf16 v[188:191], v[132:135], v[96:99], v[120:123]\n"
"ds_read_b64_tr_b16 v[132:133], v255 offset:10240\n"
"v_exp_f32_e32 v130, v130\n"
"v_exp_f32_e32 v131, v131\n"
"v_mfma_f32_16x16x32_bf16 v[192:195], v[136:139], v[96:99], v[120:123]\n"
"ds_read_b64_tr_b16 v[134:135], v16 offset:10496\n"
"v_exp_f32_e32 v152, v152\n"
"v_exp_f32_e32 v153, v153\n"
"v_exp_f32_e32 v154, v154\n"
"v_mfma_f32_16x16x32_bf16 v[196:199], v[136:139], v[92:95], v[116:119]\n"
"s_waitcnt lgkmcnt(4)\n"
"s_add_u32 s97, s69, 0x60a000\n"
"buffer_load_dwordx4 v[56:59], v0, s[4:7], s97 offen sc1\n"
"v_max_u32_e32 v248, v164, v165\n"
"v_exp_f32_e32 v155, v155\n"
"v_mfma_f32_16x16x32_bf16 v[156:159], v[88:91], v[160:163], v[156:159]\n"
"v_max3_u32 v248, v248, v166, v167\n"
"v_cvt_pk_bf16_f32 v136, v128, v129\n"
"v_cvt_pk_bf16_f32 v137, v130, v131\n"
"v_cvt_pk_bf16_f32 v138, v152, v153\n"
"v_mfma_f32_16x16x32_bf16 v[124:127], v[148:151], v[160:163], v[124:127]\n"
"v_max3_u32 v248, v248, v76, v77\n"
"v_cvt_pk_bf16_f32 v139, v154, v155\n"
"v_exp_f32_e32 v140, v140\n"
"v_max3_u32 v248, v248, v78, v79\n"
"v_exp_f32_e32 v141, v141\n"
"v_mfma_f32_16x16x32_bf16 v[168:171], v[64:67], v[160:163], v[168:171]\n"
"v_exp_f32_e32 v142, v142\n"
"v_max3_u32 v248, v248, v72, v73\n"
"v_exp_f32_e32 v143, v143\n"
"v_mfma_f32_16x16x32_bf16 v[172:175], v[64:67], v[184:187], v[172:175]\n"
"buffer_load_dwordx4 v[60:63], v0, s[4:7], s97 offen offset:1024 sc1\n"
"v_max3_u32 v248, v248, v74, v75\n"
"v_exp_f32_e32 v144, v144\n"
"v_exp_f32_e32 v145, v145\n"
"v_mfma_f32_16x16x32_bf16 v[176:179], v[148:151], v[184:187], v[176:179]\n"
"v_max3_u32 v248, v248, v84, v85\n"
"v_exp_f32_e32 v146, v146\n"
"v_max3_u32 v248, v248, v86, v87\n"
"v_exp_f32_e32 v147, v147\n"
"v_mfma_f32_16x16x32_bf16 v[180:183], v[88:91], v[184:187], v[180:183]\n"
"v_cvt_pk_bf16_f32 v128, v140, v141\n"
"v_cmp_eq_u32_e32 vcc, -1, v248\n"
"v_cvt_pk_bf16_f32 v129, v142, v143\n"
"v_cvt_pk_bf16_f32 v130, v144, v145\n"
"v_cvt_pk_bf16_f32 v131, v146, v147\n"
"s_cbranch_vccnz .Lmk_h90s\n"
".Lmk_h90r:\n"
"s_cmp_lg_u32 s2, 0\n"
"s_cbranch_scc1 .Lmk_s104\n"
"s_barrier\n"
".Lmk_s104:\n"
"ds_read_b128 v[72:75], v15 offset:6144\n"
"ds_read_b128 v[76:79], v254 offset:6400\n"
"s_waitcnt lgkmcnt(2)\n"
"v_mfma_f32_16x16x32_bf16 v[156:159], v[88:91], v[136:139], v[156:159]\n"
"ds_read_b128 v[84:87], v15 offset:4096\n"
"v_exp_f32_e32 v80, v80\n"
"v_exp_f32_e32 v81, v81\n"
"v_exp_f32_e32 v82, v82\n"
"v_mfma_f32_16x16x32_bf16 v[124:127], v[68:71], v[136:139], v[124:127]\n"
"buffer_load_dwordx4 v[64:67], v0, s[4:7], s97 offen offset:2048 sc1\n"
"ds_read_b128 v[140:143], v254 offset:4352\n"
"v_exp_f32_e32 v83, v83\n"
"v_mfma_f32_16x16x32_bf16 v[168:171], v[132:135], v[136:139], v[168:171]\n"
"ds_read_b64_tr_b16 v[136:137], v16 offset:8192\n"
"v_exp_f32_e32 v196, v196\n"
"v_exp_f32_e32 v197, v197\n"
"v_mfma_f32_16x16x32_bf16 v[172:175], v[132:135], v[128:131], v[172:175]\n"
"ds_read_b64_tr_b16 v[138:139], v255 offset:8448\n"
"v_exp_f32_e32 v198, v198\n"
"v_exp_f32_e32 v199, v199\n"
"v_mfma_f32_16x16x32_bf16 v[176:179], v[68:71], v[128:131], v[176:179]\n"
"buffer_load_dwordx4 v[68:71], v0, s[4:7], s97 offen offset:3072 sc1\n"
"v_cvt_pk_bf16_f32 v132, v80, v81\n"
"v_cvt_pk_bf16_f32 v133, v82, v83\n"
"v_cvt_pk_bf16_f32 v134, v196, v197\n"
"v_cvt_pk_bf16_f32 v135, v198, v199\n"
"v_mfma_f32_16x16x32_bf16 v[180:183], v[88:91], v[128:131], v[180:183]\n"
"ds_read_b64_tr_b16 v[128:129], v255 offset:8192\n"
"v_exp_f32_e32 v188, v188\n"
"v_exp_f32_e32 v189, v189\n"
"s_waitcnt lgkmcnt(5)\n"
"v_mfma_f32_16x16x32_bf16 v[144:147], v[72:75], v[92:95], v[116:119]\n"
"ds_read_b64_tr_b16 v[130:131], v16 offset:8448\n"
"v_exp_f32_e32 v190, v190\n"
"v_exp_f32_e32 v191, v191\n"
"v_exp_f32_e32 v192, v192\n"
"v_mfma_f32_16x16x32_bf16 v[148:151], v[72:75], v[96:99], v[120:123]\n"
"buffer_load_dwordx4 v[72:75], v1, s[4:7], s97 offen sc1\n"
"s_waitcnt vmcnt(12)\n"
"v_cmp_ne_u64_e32 vcc, v[246:247], v[18:19]\n"
"v_mfma_f32_16x16x32_bf16 v[152:155], v[76:79], v[96:99], v[120:123]\n"
"v_exp_f32_e32 v193, v193\n"
"v_exp_f32_e32 v194, v194\n"
"v_exp_f32_e32 v195, v195\n"
"v_mfma_f32_16x16x32_bf16 v[160:163], v[76:79], v[92:95], v[116:119]\n"
"s_cbranch_vccnz .Lmk_f74s\n"
".Lmk_f74r:\n"
"v_cvt_pk_bf16_f32 v164, v188, v189\n"
"v_cvt_pk_bf16_f32 v165, v190, v191\n"
"v_cvt_pk_bf16_f32 v166, v192, v193\n"
"v_cvt_pk_bf16_f32 v167, v194, v195\n"
"s_waitcnt lgkmcnt(4)\n"
"v_mfma_f32_16x16x32_bf16 v[184:187], v[84:87], v[92:95], v[116:119]\n"
"ds_read_b128 v[188:191], v15 offset:2048\n"
"v_exp_f32_e32 v144, v144\n"
"v_exp_f32_e32 v145, v145\n"
"v_exp_f32_e32 v146, v146\n"
"v_mfma_f32_16x16x32_bf16 v[192:195], v[84:87], v[96:99], v[120:123]\n"
"buffer_load_dwordx4 v[76:79], v1, s[4:7], s97 offen offset:1024 sc1\n"
"v_exp_f32_e32 v147, v147\n"
"v_mfma_f32_16x16x32_bf16 v[196:199], v[140:143], v[96:99], v[120:123]\n"
"ds_read_b128 v[200:203], v254 offset:2304\n"
"v_exp_f32_e32 v160, v160\n"
"v_exp_f32_e32 v161, v161\n"
"v_mfma_f32_16x16x32_bf16 v[204:207], v[140:143], v[92:95], v[116:119]\n"
"ds_read_b64_tr_b16 v[140:141], v16 offset:6144\n"
"v_exp_f32_e32 v162, v162\n"
"v_exp_f32_e32 v163, v163\n"
"s_waitcnt lgkmcnt(3)\n"
"v_mfma_f32_16x16x32_bf16 v[156:159], v[88:91], v[132:135], v[156:159]\n"
"buffer_load_dwordx4 v[80:83], v1, s[4:7], s97 offen offset:2048 sc1\n"
"v_cvt_pk_bf16_f32 v208, v144, v145\n"
"v_cvt_pk_bf16_f32 v209, v146, v147\n"
"v_cvt_pk_bf16_f32 v210, v160, v161\n"
"v_mfma_f32_16x16x32_bf16 v[124:127], v[136:139], v[132:135], v[124:127]\n"
"ds_read_b64_tr_b16 v[142:143], v255 offset:6400\n"
"v_cvt_pk_bf16_f32 v211, v162, v163\n"
"v_exp_f32_e32 v148, v148\n"
"v_exp_f32_e32 v149, v149\n"
"v_mfma_f32_16x16x32_bf16 v[168:171], v[128:131], v[132:135], v[168:171]\n"
"ds_read_b64_tr_b16 v[132:133], v255 offset:6144\n"
"v_exp_f32_e32 v150, v150\n"
"v_exp_f32_e32 v151, v151\n"
"v_mfma_f32_16x16x32_bf16 v[172:175], v[128:131], v[164:167], v[172:175]\n"
"buffer_load_dwordx4 v[84:87], v1, s[4:7], s97 offen offset:3072 sc1\n"
"v_exp_f32_e32 v152, v152\n"
"v_exp_f32_e32 v153, v153\n"
"v_mfma_f32_16x16x32_bf16 v[176:179], v[136:139], v[164:167], v[176:179]\n"
"ds_read_b64_tr_b16 v[134:135], v16 offset:6400\n"
"v_exp_f32_e32 v154, v154\n"
"v_exp_f32_e32 v155, v155\n"
"v_mfma_f32_16x16x32_bf16 v[180:183], v[88:91], v[164:167], v[180:183]\n"
"s_waitcnt lgkmcnt(4)\n"
"v_cvt_pk_bf16_f32 v128, v148, v149\n"
"v_cvt_pk_bf16_f32 v129, v150, v151\n"
"v_cvt_pk_bf16_f32 v130, v152, v153\n"
"v_cvt_pk_bf16_f32 v131, v154, v155\n"
"v_mfma_f32_16x16x32_bf16 v[136:139], v[188:191], v[92:95], v[116:119]\n"
"ds_read_b128 v[144:147], v15\n"
"v_exp_f32_e32 v184, v184\n"
"v_exp_f32_e32 v185, v185\n"
"v_exp_f32_e32 v186, v186\n"
"v_mfma_f32_16x16x32_bf16 v[148:151], v[188:191], v[96:99], v[120:123]\n"
"s_add_u32 s97, s56, 0x40000\n"
"buffer_load_dwordx4 v[152:155], v0, s[4:7], s97 offen sc1\n"
"v_exp_f32_e32 v187, v187\n"
"v_mfma_f32_16x16x32_bf16 v[160:163], v[200:203], v[96:99], v[120:123]\n"
"ds_read_b128 v[164:167], v254 offset:256\n"
"v_exp_f32_e32 v204, v204\n"
"v_exp_f32_e32 v205, v205\n"
"v_exp_f32_e32 v206, v206\n"
"v_mfma_f32_16x16x32_bf16 v[188:191], v[200:203], v[92:95], v[116:119]\n"
"buffer_load_dwordx4 v[200:203], v0, s[4:7], s97 offen offset:1024 sc1\n"
"ds_read_b64_tr_b16 v[212:213], v16 offset:4096\n"
"v_exp_f32_e32 v207, v207\n"
"s_waitcnt lgkmcnt(3)\n"
"v_mfma_f32_16x16x32_bf16 v[156:159], v[88:91], v[208:211], v[156:159]\n"
"v_cvt_pk_bf16_f32 v216, v184, v185\n"
"v_cvt_pk_bf16_f32 v217, v186, v187\n"
"v_cvt_pk_bf16_f32 v218, v204, v205\n"
"v_cvt_pk_bf16_f32 v219, v206, v207\n"
"v_mfma_f32_16x16x32_bf16 v[124:127], v[140:143], v[208:211], v[124:127]\n"
"buffer_load_dwordx4 v[184:187], v0, s[4:7], s97 offen offset:2048 sc1\n"
"ds_read_b64_tr_b16 v[214:215], v255 offset:4352\n"
"v_exp_f32_e32 v192, v192\n"
"v_exp_f32_e32 v193, v193\n"
"v_mfma_f32_16x16x32_bf16 v[168:171], v[132:135], v[208:211], v[168:171]\n"
"buffer_load_dwordx4 v[204:207], v0, s[4:7], s97 offen offset:3072 sc1\n"
"v_exp_f32_e32 v194, v194\n"
"v_exp_f32_e32 v195, v195\n"
"v_mfma_f32_16x16x32_bf16 v[172:175], v[132:135], v[128:131], v[172:175]\n"
"ds_read_b64_tr_b16 v[132:133], v255 offset:4096\n"
"v_exp_f32_e32 v196, v196\n"
"v_exp_f32_e32 v197, v197\n"
"v_mfma_f32_16x16x32_bf16 v[176:179], v[140:143], v[128:131], v[176:179]\n"
"buffer_load_dwordx4 v[140:143], v1, s[4:7], s97 offen sc1\n"
"ds_read_b64_tr_b16 v[134:135], v16 offset:4352\n"
"v_exp_f32_e32 v198, v198\n"
"v_exp_f32_e32 v199, v199\n"
"v_mfma_f32_16x16x32_bf16 v[180:183], v[88:91], v[128:131], v[180:183]\n"
"s_waitcnt lgkmcnt(4)\n"
"v_cvt_pk_bf16_f32 v128, v192, v193\n"
"v_cvt_pk_bf16_f32 v129, v194, v195\n"
"v_cvt_pk_bf16_f32 v130, v196, v197\n"
"v_cvt_pk_bf16_f32 v131, v198, v199\n"
"v_mfma_f32_16x16x32_bf16 v[192:195], v[144:147], v[92:95], v[116:119]\n"
"ds_read_b64_tr_b16 v[196:197], v16 offset:2048\n"
"ds_read_b64_tr_b16 v[198:199], v255 offset:2304\n"
"buffer_load_dwordx4 v[208:211], v1, s[4:7], s97 offen offset:1024 sc1\n"
"v_exp_f32_e32 v136, v136\n"
"v_exp_f32_e32 v137, v137\n"
"v_mfma_f32_16x16x32_bf16 v[220:223], v[144:147], v[96:99], v[120:123]\n"
"ds_read_b64_tr_b16 v[144:145], v255 offset:2048\n"
"v_exp_f32_e32 v138, v138\n"
"v_exp_f32_e32 v139, v139\n"
"v_mfma_f32_16x16x32_bf16 v[224:227], v[164:167], v[96:99], v[120:123]\n"
"buffer_load_dwordx4 v[120:123], v1, s[4:7], s97 offen offset:2048 sc1\n"
"v_exp_f32_e32 v188, v188\n"
"v_exp_f32_e32 v189, v189\n"
"v_mfma_f32_16x16x32_bf16 v[228:231], v[164:167], v[92:95], v[116:119]\n"
"ds_read_b64_tr_b16 v[146:147], v16 offset:2304\n"
"ds_read_b64_tr_b16 v[116:117], v16 offset:16384\n"
"v_exp_f32_e32 v190, v190\n"
"v_exp_f32_e32 v191, v191\n"
"s_waitcnt lgkmcnt(5)\n"
"v_mfma_f32_16x16x32_bf16 v[156:159], v[88:91], v[216:219], v[156:159]\n"
"v_cvt_pk_bf16_f32 v164, v136, v137\n"
"v_cvt_pk_bf16_f32 v165, v138, v139\n"
"v_cvt_pk_bf16_f32 v166, v188, v189\n"
"v_mfma_f32_16x16x32_bf16 v[124:127], v[212:215], v[216:219], v[124:127]\n"
"buffer_load_dwordx4 v[136:139], v1, s[4:7], s97 offen offset:3072 sc1\n"
"ds_read_b64_tr_b16 v[118:119], v255 offset:16640\n"
"ds_read_b64_tr_b16 v[188:189], v255 offset:16384\n"
"v_cvt_pk_bf16_f32 v167, v190, v191\n"
"v_exp_f32_e32 v148, v148\n"
"v_exp_f32_e32 v149, v149\n"
"v_exp_f32_e32 v150, v150\n"
"v_mfma_f32_16x16x32_bf16 v[168:171], v[132:135], v[216:219], v[168:171]\n"
"s_add_u32 s97, s57, 0x40000\n"
"buffer_load_dwordx4 v[216:219], v0, s[4:7], s97 offen sc1\n"
"ds_read_b64_tr_b16 v[190:191], v16 offset:16640\n"
"v_exp_f32_e32 v151, v151\n"
"v_mfma_f32_16x16x32_bf16 v[172:175], v[132:135], v[128:131], v[172:175]\n"
"v_exp_f32_e32 v160, v160\n"
"v_add_u32_e32 v248, 0x26400, v0\n"
"v_exp_f32_e32 v161, v161\n"
"v_mfma_f32_16x16x32_bf16 v[176:179], v[212:215], v[128:131], v[176:179]\n"
"buffer_load_dwordx4 v[132:135], v0, s[4:7], s97 offen offset:1024 sc1\n"
"ds_read_b128 v[212:215], v248\n"
"v_exp_f32_e32 v162, v162\n"
"v_exp_f32_e32 v163, v163\n"
"v_mfma_f32_16x16x32_bf16 v[180:183], v[88:91], v[128:131], v[180:183]\n"
"ds_read_b128 v[232:235], v248 offset:1024\n"
"v_cvt_pk_bf16_f32 v128, v148, v149\n"
"v_cvt_pk_bf16_f32 v129, v150, v151\n"
"v_cvt_pk_bf16_f32 v130, v160, v161\n"
"v_cvt_pk_bf16_f32 v131, v162, v163\n"
"s_waitcnt lgkmcnt(6)\n"
"v_mfma_f32_16x16x32_bf16 v[156:159], v[88:91], v[164:167], v[156:159]\n"
"ds_read_b64_tr_b16 v[148:149], v16\n"
"buffer_load_dwordx4 v[160:163], v0, s[4:7], s97 offen offset:2048 sc1\n"
"v_cndmask_b32_e64 v192, v100, v192, s[20:21]\n"
"v_cndmask_b32_e64 v193, v101, v193, s[22:23]\n"
"v_cndmask_b32_e64 v194, v102, v194, s[24:25]\n"
"v_cndmask_b32_e64 v195, v103, v195, s[26:27]\n"
"v_cndmask_b32_e64 v228, v112, v228, s[28:29]\n"
"v_cndmask_b32_e64 v229, v113, v229, s[30:31]\n"
"v_cndmask_b32_e64 v230, v114, v230, s[32:33]\n"
"v_cndmask_b32_e64 v231, v115, v231, s[34:35]\n"
"v_exp_f32_e32 v192, v192\n"
"v_mfma_f32_16x16x32_bf16 v[124:127], v[196:199], v[164:167], v[124:127]\n"
"ds_read_b64_tr_b16 v[150:151], v255 offset:256\n"
"buffer_load_dwordx4 v[100:103], v0, s[4:7], s97 offen offset:3072 sc1\n"
"v_exp_f32_e32 v193, v193\n"
"v_exp_f32_e32 v194, v194\n"
"v_exp_f32_e32 v195, v195\n"
"v_exp_f32_e32 v228, v228\n"
"v_mfma_f32_16x16x32_bf16 v[168:171], v[144:147], v[164:167], v[168:171]\n"
"buffer_load_dwordx4 v[164:167], v1, s[4:7], s97 offen sc1\n"
"v_exp_f32_e32 v229, v229\n"
"v_exp_f32_e32 v230, v230\n"
"v_exp_f32_e32 v231, v231\n"
"v_cvt_pk_bf16_f32 v112, v192, v193\n"
"v_cvt_pk_bf16_f32 v113, v194, v195\n"
"v_cvt_pk_bf16_f32 v114, v228, v229\n"
"v_mfma_f32_16x16x32_bf16 v[172:175], v[144:147], v[128:131], v[172:175]\n"
"ds_read_b64_tr_b16 v[144:145], v255\n"
"v_cvt_pk_bf16_f32 v115, v230, v231\n"
"v_cndmask_b32_e64 v220, v104, v220, s[36:37]\n"
"v_cndmask_b32_e64 v221, v105, v221, s[38:39]\n"
"v_cndmask_b32_e64 v222, v106, v222, s[40:41]\n"
"v_cndmask_b32_e64 v223, v107, v223, s[42:43]\n"
"v_cndmask_b32_e64 v224, v108, v224, s[44:45]\n"
"v_cndmask_b32_e64 v225, v109, v225, s[46:47]\n"
"v_cndmask_b32_e64 v226, v110, v226, s[48:49]\n"
"v_cndmask_b32_e64 v227, v111, v227, s[50:51]\n"
"v_mfma_f32_16x16x32_bf16 v[176:179], v[196:199], v[128:131], v[176:179]\n"
"buffer_load_dwordx4 v[104:107], v1, s[4:7], s97 offen offset:1024 sc1\n"
"ds_read_b64_tr_b16 v[146:147], v16 offset:256\n"
"buffer_load_dwordx4 v[108:111], v1, s[4:7], s97 offen offset:2048 sc1\n"
"v_exp_f32_e32 v220, v220\n"
"v_exp_f32_e32 v221, v221\n"
"v_exp_f32_e32 v222, v222\n"
"v_exp_f32_e32 v223, v223\n"
"v_exp_f32_e32 v224, v224\n"
"v_mfma_f32_16x16x32_bf16 v[180:183], v[88:91], v[128:131], v[180:183]\n"
"s_waitcnt lgkmcnt(0)\n"
"v_exp_f32_e32 v225, v225\n"
"v_exp_f32_e32 v226, v226\n"
"v_exp_f32_e32 v227, v227\n"
"v_cvt_pk_bf16_f32 v128, v220, v221\n"
"v_cvt_pk_bf16_f32 v129, v222, v223\n"
"v_cvt_pk_bf16_f32 v130, v224, v225\n"
"v_cvt_pk_bf16_f32 v131, v226, v227\n"
"v_mfma_f32_16x16x32_bf16 v[156:159], v[88:91], v[112:115], v[156:159]\n"
"v_and_b32_e32 v212, v112, v212\n"
"v_and_b32_e32 v213, v113, v213\n"
"v_and_b32_e32 v214, v114, v214\n"
"v_and_b32_e32 v215, v115, v215\n"
"v_mfma_f32_16x16x32_bf16 v[180:183], v[88:91], v[128:131], v[180:183]\n"
"v_xor_b32_e32 v192, v112, v212\n"
"v_xor_b32_e32 v193, v113, v213\n"
"v_xor_b32_e32 v194, v114, v214\n"
"v_xor_b32_e32 v195, v115, v215\n"
"v_mfma_f32_16x16x32_bf16 v[124:127], v[148:151], v[212:215], v[124:127]\n"
"v_and_b32_e32 v232, v128, v232\n"
"v_and_b32_e32 v233, v129, v233\n"
"v_mfma_f32_16x16x32_bf16 v[168:171], v[144:147], v[212:215], v[168:171]\n"
"v_and_b32_e32 v234, v130, v234\n"
"v_and_b32_e32 v235, v131, v235\n"
"v_mfma_f32_16x16x32_bf16 v[124:127], v[116:119], v[192:195], v[124:127]\n"
"v_xor_b32_e32 v112, v128, v232\n"
"v_xor_b32_e32 v113, v129, v233\n"
"v_mfma_f32_16x16x32_bf16 v[168:171], v[188:191], v[192:195], v[168:171]\n"
"v_xor_b32_e32 v114, v130, v234\n"
"v_xor_b32_e32 v115, v131, v235\n"
"v_mfma_f32_16x16x32_bf16 v[176:179], v[148:151], v[232:235], v[176:179]\n"
"buffer_load_dwordx4 v[128:131], v1, s[4:7], s97 offen offset:3072 sc1\n"
"s_add_u32 s97, s58, 0x40000\n"
"buffer_load_dwordx4 v[148:151], v0, s[4:7], s97 offen sc1\n"
"v_mfma_f32_16x16x32_bf16 v[172:175], v[144:147], v[232:235], v[172:175]\n"
"buffer_load_dwordx4 v[144:147], v0, s[4:7], s97 offen offset:1024 sc1\n"
"v_mfma_f32_16x16x32_bf16 v[176:179], v[116:119], v[112:115], v[176:179]\n"
"buffer_load_dwordx4 v[116:119], v0, s[4:7], s97 offen offset:2048 sc1\n"
"v_mfma_f32_16x16x32_bf16 v[172:175], v[188:191], v[112:115], v[172:175]\n"
"buffer_load_dwordx4 v[112:115], v0, s[4:7], s97 offen offset:3072 sc1\n"
"v_max_f32_e32 v248, v156, v180\n"
"v_rcp_f32_e32 v156, v156\n"
"v_rcp_f32_e32 v180, v180\n"
"s_nop 0\n"
"v_max3_f32 v248, v248, v156, v180\n"
"v_cmp_nlt_f32_e32 vcc, v248, v252\n"
"s_cbranch_vccnz .Lmk_b100\n"
".Lmk_k105:\n"
"v_mul_f32_e32 v124, v124, v156\n"
"v_mul_f32_e32 v125, v125, v156\n"
"v_mul_f32_e32 v126, v126, v156\n"
"v_mul_f32_e32 v127, v127, v156\n"
"v_mul_f32_e32 v168, v168, v156\n"
"v_mul_f32_e32 v169, v169, v156\n"
"v_mul_f32_e32 v170, v170, v156\n"
"v_mul_f32_e32 v171, v171, v156\n"
"v_cvt_pk_bf16_f32 v88, v124, v125\n"
"v_cvt_pk_bf16_f32 v89, v126, v127\n"
"v_cvt_pk_bf16_f32 v90, v168, v169\n"
"v_cvt_pk_bf16_f32 v91, v170, v171\n"
"buffer_store_dwordx4 v[88:91], v0, s[4:7], s60 offen sc1\n"
"s_add_u32 s98, s60, 0x8000\n"
"v_mul_f32_e32 v176, v176, v180\n"
"v_mul_f32_e32 v177, v177, v180\n"
"v_mul_f32_e32 v178, v178, v180\n"
"v_mul_f32_e32 v179, v179, v180\n"
"v_mul_f32_e32 v172, v172, v180\n"
"v_mul_f32_e32 v173, v173, v180\n"
"v_mul_f32_e32 v174, v174, v180\n"
"v_mul_f32_e32 v175, v175, v180\n"
"v_cvt_pk_bf16_f32 v88, v176, v177\n"
"v_cvt_pk_bf16_f32 v89, v178, v179\n"
"v_cvt_pk_bf16_f32 v90, v172, v173\n"
"v_cvt_pk_bf16_f32 v91, v174, v175\n"
"buffer_store_dwordx4 v[88:91], v0, s[4:7], s98 offen sc1\n"
";\n"
"s_waitcnt vmcnt(20)\n"
"; @A G2 b.0\n"
"s_nop 0\n"
"v_mfma_f32_16x16x32_bf16 v[88:91], a[128:131], v[152:155], 0\n"
"v_mfma_f32_16x16x32_bf16 v[88:91], a[132:135], v[200:203], v[88:91]\n"
"s_waitcnt vmcnt(18)\n"
"v_mfma_f32_16x16x32_bf16 v[88:91], a[136:139], v[184:187], v[88:91]\n"
"buffer_load_dwordx4 v[92:95], v1, s[4:7], s97 offen sc1\n"
"v_mfma_f32_16x16x32_bf16 v[88:91], a[140:143], v[204:207], v[88:91]\n"
"s_waitcnt vmcnt(17)\n"
"v_mfma_f32_16x16x32_bf16 v[88:91], a[144:147], v[140:143], v[88:91]\n"
"v_mfma_f32_16x16x32_bf16 v[88:91], a[148:151], v[208:211], v[88:91]\n"
"s_waitcnt vmcnt(15)\n"
"v_mfma_f32_16x16x32_bf16 v[88:91], a[152:155], v[120:123], v[88:91]\n"
"buffer_load_dwordx4 v[96:99], v1, s[4:7], s97 offen offset:1024 sc1\n"
"v_mfma_f32_16x16x32_bf16 v[88:91], a[156:159], v[136:139], v[88:91]\n"
"v_mfma_f32_16x16x32_bf16 v[124:127], a[188:191], v[136:139], 0\n"
"v_mfma_f32_16x16x32_bf16 v[124:127], a[184:187], v[120:123], v[124:127]\n"
"v_mfma_f32_16x16x32_bf16 v[124:127], a[180:183], v[208:211], v[124:127]\n"
"buffer_load_dwordx4 v[156:159], v1, s[4:7], s97 offen offset:2048 sc1\n"
"v_mfma_f32_16x16x32_bf16 v[124:127], a[176:179], v[140:143], v[124:127]\n"
"v_mfma_f32_16x16x32_bf16 v[124:127], a[172:175], v[204:207], v[124:127]\n"
"s_nop 1\n"
"v_cmp_u_f32_e32 vcc, v88, v88\n"
"v_mfma_f32_16x16x32_bf16 v[124:127], a[168:171], v[184:187], v[124:127]\n"
"s_cbranch_vccnz .Lmk_p106s\n"
".Lmk_p106r:\n"
"v_mfma_f32_16x16x32_bf16 v[124:127], a[164:167], v[200:203], v[124:127]\n"
"ds_write_b128 v3, v[88:91]\n"
"v_mfma_f32_16x16x32_bf16 v[124:127], a[160:163], v[152:155], v[124:127]\n"
"buffer_load_dwordx4 v[88:91], v1, s[4:7], s97 offen offset:3072 sc1\n"
"v_mfma_f32_16x16x32_bf16 v[168:171], a[192:195], v[152:155], 0\n"
"v_mfma_f32_16x16x32_bf16 v[168:171], a[196:199], v[200:203], v[168:171]\n"
"s_add_u32 s98, s59, 0x40000\n"
"v_mfma_f32_16x16x32_bf16 v[168:171], a[200:203], v[184:187], v[168:171]\n"
"v_mfma_f32_16x16x32_bf16 v[168:171], a[204:207], v[204:207], v[168:171]\n"
"buffer_load_dwordx4 v[172:175], v0, s[4:7], s98 offen sc1\n"
"v_mfma_f32_16x16x32_bf16 v[168:171], a[208:211], v[140:143], v[168:171]\n"
"v_mfma_f32_16x16x32_bf16 v[168:171], a[212:215], v[208:211], v[168:171]\n"
"ds_write_b128 v3, v[124:127] offset:1024\n"
"v_mfma_f32_16x16x32_bf16 v[168:171], a[216:219], v[120:123], v[168:171]\n"
"v_mfma_f32_16x16x32_bf16 v[168:171], a[220:223], v[136:139], v[168:171]\n"
"buffer_load_dwordx4 v[124:127], v0, s[4:7], s98 offen offset:1024 sc1\n"
"v_mfma_f32_16x16x32_bf16 v[176:179], a[252:255], v[136:139], 0\n"
"v_mfma_f32_16x16x32_bf16 v[176:179], a[248:251], v[120:123], v[176:179]\n"
"v_mfma_f32_16x16x32_bf16 v[176:179], a[244:247], v[208:211], v[176:179]\n"
"v_mfma_f32_16x16x32_bf16 v[176:179], a[240:243], v[140:143], v[176:179]\n"
"buffer_load_dwordx4 v[120:123], v0, s[4:7], s98 offen offset:2048 sc1\n"
"v_mfma_f32_16x16x32_bf16 v[176:179], a[236:239], v[204:207], v[176:179]\n"
"v_mfma_f32_16x16x32_bf16 v[176:179], a[232:235], v[184:187], v[176:179]\n"
"ds_write_b128 v3, v[168:171] offset:2048\n"
"v_mfma_f32_16x16x32_bf16 v[176:179], a[228:231], v[200:203], v[176:179]\n"
"v_mfma_f32_16x16x32_bf16 v[176:179], a[224:227], v[152:155], v[176:179]\n"
"s_waitcnt vmcnt(17)\n"
"buffer_load_dwordx4 v[136:139], v0, s[4:7], s98 offen offset:3072 sc1\n"
"; @A G2 b.1\n"
"v_mfma_f32_16x16x32_bf16 v[140:143], a[128:131], v[216:219], 0\n"
"v_mfma_f32_16x16x32_bf16 v[140:143], a[132:135], v[132:135], v[140:143]\n"
"v_mfma_f32_16x16x32_bf16 v[140:143], a[136:139], v[160:163], v[140:143]\n"
"v_mfma_f32_16x16x32_bf16 v[140:143], a[140:143], v[100:103], v[140:143]\n"
"s_waitcnt vmcnt(14)\n"
"buffer_load_dwordx4 v[152:155], v1, s[4:7], s98 offen sc1\n"
"v_mfma_f32_16x16x32_bf16 v[140:143], a[144:147], v[164:167], v[140:143]\n"
"v_mfma_f32_16x16x32_bf16 v[140:143], a[148:151], v[104:107], v[140:143]\n"
"ds_write_b128 v3, v[176:179] offset:3072\n"
"v_mfma_f32_16x16x32_bf16 v[140:143], a[152:155], v[108:111], v[140:143]\n"
"v_mfma_f32_16x16x32_bf16 v[140:143], a[156:159], v[128:131], v[140:143]\n"
"buffer_load_dwordx4 v[168:171], v1, s[4:7], s98 offen offset:1024 sc1\n"
"v_mfma_f32_16x16x32_bf16 v[176:179], a[188:191], v[128:131], 0\n"
"v_mfma_f32_16x16x32_bf16 v[176:179], a[184:187], v[108:111], v[176:179]\n"
"v_mfma_f32_16x16x32_bf16 v[176:179], a[180:183], v[104:107], v[176:179]\n"
"v_mfma_f32_16x16x32_bf16 v[176:179], a[176:179], v[164:167], v[176:179]\n"
"buffer_load_dwordx4 v[180:183], v1, s[4:7], s98 offen offset:2048 sc1\n"
"v_mfma_f32_16x16x32_bf16 v[176:179], a[172:175], v[100:103], v[176:179]\n"
"s_nop 0\n"
"v_cmp_u_f32_e32 vcc, v140, v140\n"
"v_mfma_f32_16x16x32_bf16 v[176:179], a[168:171], v[160:163], v[176:179]\n"
"s_cbranch_vccnz .Lmk_p107s\n"
".Lmk_p107r:\n"
"v_mfma_f32_16x16x32_bf16 v[176:179], a[164:167], v[132:135], v[176:179]\n"
"ds_write_b128 v3, v[140:143] offset:6144\n"
"v_mfma_f32_16x16x32_bf16 v[176:179], a[160:163], v[216:219], v[176:179]\n"
"buffer_load_dwordx4 v[140:143], v1, s[4:7], s98 offen offset:3072 sc1\n"
"v_mfma_f32_16x16x32_bf16 v[184:187], a[192:195], v[216:219], 0\n"
"v_mfma_f32_16x16x32_bf16 v[184:187], a[196:199], v[132:135], v[184:187]\n"
"v_mfma_f32_16x16x32_bf16 v[184:187], a[200:203], v[160:163], v[184:187]\n"
"v_mfma_f32_16x16x32_bf16 v[184:187], a[204:207], v[100:103], v[184:187]\n"
"v_mfma_f32_16x16x32_bf16 v[184:187], a[208:211], v[164:167], v[184:187]\n"
"ds_read_b128 v[188:191], v2 offset:8192\n"
"ds_read_b128 v[192:195], v2 offset:9216\n"
"v_mfma_f32_16x16x32_bf16 v[184:187], a[212:215], v[104:107], v[184:187]\n"
"ds_write_b128 v3, v[176:179] offset:7168\n"
"v_mfma_f32_16x16x32_bf16 v[184:187], a[216:219], v[108:111], v[184:187]\n"
"v_mfma_f32_16x16x32_bf16 v[184:187], a[220:223], v[128:131], v[184:187]\n"
"v_mfma_f32_16x16x32_bf16 v[176:179], a[252:255], v[128:131], 0\n"
"v_mfma_f32_16x16x32_bf16 v[176:179], a[248:251], v[108:111], v[176:179]\n"
"s_waitcnt lgkmcnt(1)\n"
"v_lshlrev_b32_e32 v255, 16, v188\n"
"v_and_b32_e32 v254, 0xffff0000, v188\n"
"v_mfma_f32_16x16x32_bf16 v[176:179], a[244:247], v[104:107], v[176:179]\n"
"v_lshlrev_b32_e32 v253, 16, v189\n"
"v_and_b32_e32 v252, 0xffff0000, v189\n"
"v_mfma_f32_16x16x32_bf16 v[176:179], a[240:243], v[164:167], v[176:179]\n"
"v_lshlrev_b32_e32 v251, 16, v190\n"
"v_and_b32_e32 v250, 0xffff0000, v190\n"
"v_mfma_f32_16x16x32_bf16 v[176:179], a[236:239], v[100:103], v[176:179]\n"
"v_lshlrev_b32_e32 v249, 16, v191\n"
"v_and_b32_e32 v248, 0xffff0000, v191\n"
"v_mfma_f32_16x16x32_bf16 v[176:179], a[232:235], v[160:163], v[176:179]\n"
"ds_write_b128 v3, v[184:187] offset:8192\n"
"v_mfma_f32_16x16x32_bf16 v[176:179], a[228:231], v[132:135], v[176:179]\n"
"v_lshlrev_b32_e32 v247, 16, v192\n"
"v_and_b32_e32 v246, 0xffff0000, v192\n"
"v_mfma_f32_16x16x32_bf16 v[176:179], a[224:227], v[216:219], v[176:179]\n"
"s_waitcnt vmcnt(14)\n"
"v_lshlrev_b32_e32 v245, 16, v193\n"
"v_and_b32_e32 v244, 0xffff0000, v193\n"
"; @A G2 b.2\n"
"v_mfma_f32_16x16x32_bf16 v[100:103], a[128:131], v[148:151], 0\n"
"v_lshlrev_b32_e32 v243, 16, v194\n"
"v_and_b32_e32 v242, 0xffff0000, v194\n"
"v_mfma_f32_16x16x32_bf16 v[100:103], a[132:135], v[144:147], v[100:103]\n"
"v_lshlrev_b32_e32 v241, 16, v195\n"
"v_and_b32_e32 v240, 0xffff0000, v195\n"
"v_mfma_f32_16x16x32_bf16 v[100:103], a[136:139], v[116:119], v[100:103]\n"
"v_mfma_f32_16x16x32_bf16 v[100:103], a[140:143], v[112:115], v[100:103]\n"
"s_waitcnt vmcnt(8)\n"
"v_mfma_f32_16x16x32_bf16 v[100:103], a[144:147], v[92:95], v[100:103]\n"
"v_mfma_f32_16x16x32_bf16 v[100:103], a[148:151], v[96:99], v[100:103]\n"
"ds_write_b128 v3, v[176:179] offset:9216\n"
"v_mfma_f32_16x16x32_bf16 v[100:103], a[152:155], v[156:159], v[100:103]\n"
"v_mfma_f32_16x16x32_bf16 v[100:103], a[156:159], v[88:91], v[100:103]\n"
"v_mfma_f32_16x16x32_bf16 v[104:107], a[188:191], v[88:91], 0\n"
"v_mfma_f32_16x16x32_bf16 v[104:107], a[184:187], v[156:159], v[104:107]\n"
"s_waitcnt lgkmcnt(0)\n"
"s_barrier\n"
"v_mfma_f32_16x16x32_bf16 v[104:107], a[180:183], v[96:99], v[104:107]\n"
"ds_read_b128 v[108:111], v4\n"
"ds_read_b128 v[128:131], v5\n"
"v_mfma_f32_16x16x32_bf16 v[104:107], a[176:179], v[92:95], v[104:107]\n"
"v_mfma_f32_16x16x32_bf16 v[104:107], a[172:175], v[112:115], v[104:107]\n"
"s_add_u32 s98, s56, 0\n"
"v_cmp_u_f32_e32 vcc, v100, v100\n"
"v_mfma_f32_16x16x32_bf16 v[104:107], a[168:171], v[116:119], v[104:107]\n"
"s_cbranch_vccnz .Lmk_p108s\n"
".Lmk_p108r:\n"
"v_mfma_f32_16x16x32_bf16 v[104:107], a[164:167], v[144:147], v[104:107]\n"
"ds_write_b128 v3, v[100:103] offset:12288\n"
"v_mfma_f32_16x16x32_bf16 v[104:107], a[160:163], v[148:151], v[104:107]\n"
"buffer_load_dwordx4 v[100:103], v0, s[4:7], s98 offen sc1\n"
"v_mfma_f32_16x16x32_bf16 v[132:135], a[192:195], v[148:151], 0\n"
"ds_read_b128 v[160:163], v4 offset:1024\n"
"ds_read_b128 v[164:167], v5 offset:1024\n"
"v_mfma_f32_16x16x32_bf16 v[132:135], a[196:199], v[144:147], v[132:135]\n"
"s_waitcnt lgkmcnt(3)\n"
"v_add_f32_e32 v108, v108, v255\n"
"v_add_f32_e32 v109, v109, v254\n"
"v_mfma_f32_16x16x32_bf16 v[132:135], a[200:203], v[116:119], v[132:135]\n"
"buffer_load_dwordx4 v[176:179], v0, s[4:7], s98 offen offset:1024 sc1\n"
"v_mfma_f32_16x16x32_bf16 v[132:135], a[204:207], v[112:115], v[132:135]\n"
"v_add_f32_e32 v110, v110, v253\n"
"v_add_f32_e32 v111, v111, v252\n"
"v_mfma_f32_16x16x32_bf16 v[132:135], a[208:211], v[92:95], v[132:135]\n"
"v_add_f32_e32 v108, v108, v128\n"
"v_add_f32_e32 v109, v109, v129\n"
"v_mfma_f32_16x16x32_bf16 v[132:135], a[212:215], v[96:99], v[132:135]\n"
"ds_write_b128 v3, v[104:107] offset:13312\n"
"v_mfma_f32_16x16x32_bf16 v[132:135], a[216:219], v[156:159], v[132:135]\n"
"buffer_load_dwordx4 v[104:107], v0, s[4:7], s98 offen offset:2048 sc1\n"
"v_mfma_f32_16x16x32_bf16 v[132:135], a[220:223], v[88:91], v[132:135]\n"
"ds_read_b128 v[184:187], v4 offset:2048\n"
"ds_read_b128 v[188:191], v5 offset:2048\n"
"v_mfma_f32_16x16x32_bf16 v[192:195], a[252:255], v[88:91], 0\n"
"s_waitcnt lgkmcnt(3)\n"
"v_add_f32_e32 v110, v110, v130\n"
"v_add_f32_e32 v111, v111, v131\n"
"v_mfma_f32_16x16x32_bf16 v[192:195], a[248:251], v[156:159], v[192:195]\n"
"buffer_load_dwordx4 v[88:91], v0, s[4:7], s98 offen offset:3072 sc1\n"
"v_mfma_f32_16x16x32_bf16 v[192:195], a[244:247], v[96:99], v[192:195]\n"
"v_add_f32_e32 v160, v160, v251\n"
"v_add_f32_e32 v161, v161, v250\n"
"v_mfma_f32_16x16x32_bf16 v[192:195], a[240:243], v[92:95], v[192:195]\n"
"v_add_f32_e32 v162, v162, v249\n"
"v_add_f32_e32 v163, v163, v248\n"
"v_mfma_f32_16x16x32_bf16 v[192:195], a[236:239], v[112:115], v[192:195]\n"
"buffer_load_dwordx4 v[92:95], v1, s[4:7], s98 offen sc1\n"
"v_mfma_f32_16x16x32_bf16 v[192:195], a[232:235], v[116:119], v[192:195]\n"
"ds_write_b128 v3, v[132:135] offset:14336\n"
"v_mfma_f32_16x16x32_bf16 v[192:195], a[228:231], v[144:147], v[192:195]\n"
"ds_read_b128 v[96:99], v4 offset:3072\n"
"ds_read_b128 v[112:115], v5 offset:3072\n"
"v_mfma_f32_16x16x32_bf16 v[192:195], a[224:227], v[148:151], v[192:195]\n"
"s_waitcnt vmcnt(9)\n"
"buffer_load_dwordx4 v[116:119], v1, s[4:7], s98 offen offset:1024 sc1\n"
"; @A G2 b.3\n"
"v_mfma_f32_16x16x32_bf16 v[128:131], a[128:131], v[172:175], v[108:111]\n"
"v_add_f32_e32 v160, v160, v164\n"
"v_add_f32_e32 v161, v161, v165\n"
"v_mfma_f32_16x16x32_bf16 v[128:131], a[132:135], v[124:127], v[128:131]\n"
"s_waitcnt lgkmcnt(3)\n"
"v_add_f32_e32 v162, v162, v166\n"
"v_add_f32_e32 v163, v163, v167\n"
"v_mfma_f32_16x16x32_bf16 v[128:131], a[136:139], v[120:123], v[128:131]\n"
"buffer_load_dwordx4 v[132:135], v1, s[4:7], s98 offen offset:2048 sc1\n"
"v_mfma_f32_16x16x32_bf16 v[128:131], a[140:143], v[136:139], v[128:131]\n"
"s_waitcnt vmcnt(7)\n"
"v_add_f32_e32 v184, v184, v247\n"
"v_add_f32_e32 v185, v185, v246\n"
"v_mfma_f32_16x16x32_bf16 v[128:131], a[144:147], v[152:155], v[128:131]\n"
"v_add_f32_e32 v186, v186, v245\n"
"v_add_f32_e32 v187, v187, v244\n"
"v_mfma_f32_16x16x32_bf16 v[128:131], a[148:151], v[168:171], v[128:131]\n"
"ds_write_b128 v3, v[192:195] offset:15360\n"
"v_mfma_f32_16x16x32_bf16 v[128:131], a[152:155], v[180:183], v[128:131]\n"
"buffer_load_dwordx4 v[144:147], v1, s[4:7], s98 offen offset:3072 sc1\n"
"v_mfma_f32_16x16x32_bf16 v[128:131], a[156:159], v[140:143], v[128:131]\n"
"s_add_u32 s98, s57, 0\n"
"v_add_f32_e32 v184, v184, v188\n"
"v_add_f32_e32 v185, v185, v189\n"
"v_mfma_f32_16x16x32_bf16 v[148:151], a[188:191], v[140:143], v[160:163]\n"
"s_waitcnt lgkmcnt(1)\n"
"v_add_f32_e32 v186, v186, v190\n"
"v_add_f32_e32 v187, v187, v191\n"
"v_mfma_f32_16x16x32_bf16 v[148:151], a[184:187], v[180:183], v[148:151]\n"
"s_waitcnt lgkmcnt(0)\n"
"s_barrier\n"
"v_mfma_f32_16x16x32_bf16 v[148:151], a[180:183], v[168:171], v[148:151]\n"
"buffer_load_dwordx4 v[156:159], v0, s[4:7], s98 offen sc1\n"
"v_mfma_f32_16x16x32_bf16 v[148:151], a[176:179], v[152:155], v[148:151]\n"
"v_add_f32_e32 v96, v96, v243\n"
"v_add_f32_e32 v97, v97, v242\n"
"v_mfma_f32_16x16x32_bf16 v[148:151], a[172:175], v[136:139], v[148:151]\n"
"v_cmp_u_f32_e32 vcc, v128, v128\n"
"v_add_f32_e32 v98, v98, v241\n"
"v_mfma_f32_16x16x32_bf16 v[148:151], a[168:171], v[120:123], v[148:151]\n"
"s_cbranch_vccnz .Lmk_p109s\n"
".Lmk_p109r:\n"
"v_add_f32_e32 v99, v99, v240\n"
"v_mfma_f32_16x16x32_bf16 v[148:151], a[164:167], v[124:127], v[148:151]\n"
"buffer_load_dwordx4 v[108:111], v0, s[4:7], s98 offen offset:1024 sc1\n"
"v_mfma_f32_16x16x32_bf16 v[148:151], a[160:163], v[172:175], v[148:151]\n"
"v_add_f32_e32 v96, v96, v112\n"
"v_add_f32_e32 v97, v97, v113\n"
"v_mfma_f32_16x16x32_bf16 v[184:187], a[192:195], v[172:175], v[184:187]\n"
"v_add_f32_e32 v98, v98, v114\n"
"v_add_f32_e32 v99, v99, v115\n"
"v_mfma_f32_16x16x32_bf16 v[184:187], a[196:199], v[124:127], v[184:187]\n"
"buffer_load_dwordx4 v[112:115], v0, s[4:7], s98 offen offset:2048 sc1\n"
"v_mfma_f32_16x16x32_bf16 v[184:187], a[200:203], v[120:123], v[184:187]\n"
"ds_read_b128 v[160:163], v6\n"
"ds_read_b128 v[164:167], v6 offset:1024\n"
"v_mfma_f32_16x16x32_bf16 v[184:187], a[204:207], v[136:139], v[184:187]\n"
"ds_read_b128 v[188:191], v6 offset:2048\n"
"ds_read_b128 v[192:195], v6 offset:3072\n"
"v_mfma_f32_16x16x32_bf16 v[184:187], a[208:211], v[152:155], v[184:187]\n"
"buffer_load_dwordx4 v[196:199], v0, s[4:7], s98 offen offset:3072 sc1\n"
"v_mfma_f32_16x16x32_bf16 v[184:187], a[212:215], v[168:171], v[184:187]\n"
"v_mfma_f32_16x16x32_bf16 v[184:187], a[216:219], v[180:183], v[184:187]\n"
"v_mfma_f32_16x16x32_bf16 v[184:187], a[220:223], v[140:143], v[184:187]\n"
"buffer_load_dwordx4 v[200:203], v1, s[4:7], s98 offen sc1\n"
"v_mfma_f32_16x16x32_bf16 v[96:99], a[252:255], v[140:143], v[96:99]\n"
"s_waitcnt lgkmcnt(0)\n"
"v_add_f32_e32 v160, v128, v160\n"
"v_add_f32_e32 v161, v129, v161\n"
"v_mfma_f32_16x16x32_bf16 v[96:99], a[248:251], v[180:183], v[96:99]\n"
"v_add_f32_e32 v162, v130, v162\n"
"v_add_f32_e32 v163, v131, v163\n"
"v_mfma_f32_16x16x32_bf16 v[96:99], a[244:247], v[168:171], v[96:99]\n"
"buffer_load_dwordx4 v[128:131], v1, s[4:7], s98 offen offset:1024 sc1\n"
"v_mfma_f32_16x16x32_bf16 v[96:99], a[240:243], v[152:155], v[96:99]\n"
"v_add_f32_e32 v164, v148, v164\n"
"v_add_f32_e32 v165, v149, v165\n"
"v_mfma_f32_16x16x32_bf16 v[96:99], a[236:239], v[136:139], v[96:99]\n"
"v_add_f32_e32 v166, v150, v166\n"
"v_add_f32_e32 v167, v151, v167\n"
"v_mfma_f32_16x16x32_bf16 v[96:99], a[232:235], v[120:123], v[96:99]\n"
"buffer_load_dwordx4 v[120:123], v1, s[4:7], s98 offen offset:2048 sc1\n"
"v_mfma_f32_16x16x32_bf16 v[96:99], a[228:231], v[124:127], v[96:99]\n"
"v_cvt_pk_bf16_f32 v124, v160, v161\n"
"v_cvt_pk_bf16_f32 v125, v162, v163\n"
"v_mfma_f32_16x16x32_bf16 v[96:99], a[224:227], v[172:175], v[96:99]\n"
"s_add_u32 s97, s61, 0x440000\n"
"v_cvt_pk_bf16_f32 v126, v164, v165\n"
"v_cvt_pk_bf16_f32 v127, v166, v167\n"
"buffer_store_dwordx4 v[124:127], v0, s[4:7], s97 offen sc1\n"
"ds_write_b128 v2, v[124:127] offset:8192\n"
"buffer_load_dwordx4 v[136:139], v1, s[4:7], s98 offen offset:3072 sc1\n"
"; @A fin b\n"
"s_add_u32 s98, s61, 0x440000\n"
"v_add_f32_e32 v188, v184, v188\n"
"v_add_f32_e32 v189, v185, v189\n"
"v_add_f32_e32 v190, v186, v190\n"
"v_add_f32_e32 v191, v187, v191\n"
"v_add_f32_e32 v192, v96, v192\n"
"v_add_f32_e32 v193, v97, v193\n"
"v_add_f32_e32 v194, v98, v194\n"
"v_add_f32_e32 v195, v99, v195\n"
"v_cvt_pk_bf16_f32 v96, v188, v189\n"
"v_cvt_pk_bf16_f32 v97, v190, v191\n"
"v_cvt_pk_bf16_f32 v98, v192, v193\n"
"v_cvt_pk_bf16_f32 v99, v194, v195\n"
"buffer_store_dwordx4 v[96:99], v0, s[4:7], s98 offen offset:1024 sc1\n"
"s_waitcnt vmcnt(16)\n"
"; @A G2 a.0\n"
"v_mfma_f32_16x16x32_bf16 v[140:143], a[128:131], v[100:103], 0\n"
"s_add_u32 s98, s58, 0\n"
"buffer_load_dwordx4 v[148:151], v0, s[4:7], s98 offen sc1\n"
"v_mfma_f32_16x16x32_bf16 v[140:143], a[132:135], v[176:179], v[140:143]\n"
"s_waitcnt vmcnt(15)\n"
"ds_write_b128 v2, v[96:99] offset:9216\n"
"v_mfma_f32_16x16x32_bf16 v[140:143], a[136:139], v[104:107], v[140:143]\n"
"v_mov_b32_e32 v152, 0x3f803f80\n"
"v_mov_b32_e32 v153, 0x3f803f80\n"
"v_mfma_f32_16x16x32_bf16 v[140:143], a[140:143], v[88:91], v[140:143]\n"
"s_waitcnt vmcnt(13)\n"
"v_mov_b32_e32 v154, 0x3f803f80\n"
"v_mov_b32_e32 v155, 0x3f803f80\n"
"v_mfma_f32_16x16x32_bf16 v[140:143], a[144:147], v[92:95], v[140:143]\n"
"buffer_load_dwordx4 v[160:163], v0, s[4:7], s98 offen offset:1024 sc1\n"
"v_mfma_f32_16x16x32_bf16 v[164:167], v[152:155], v[124:127], 0\n"
"v_mfma_f32_16x16x32_bf16 v[164:167], v[152:155], v[96:99], v[164:167]\n"
"v_mfma_f32_16x16x32_bf16 v[152:155], v[124:127], v[124:127], 0\n"
"v_mfma_f32_16x16x32_bf16 v[152:155], v[96:99], v[96:99], v[152:155]\n"
"v_mfma_f32_16x16x32_bf16 v[140:143], a[148:151], v[116:119], v[140:143]\n"
"s_waitcnt vmcnt(12)\n"
"v_mfma_f32_16x16x32_bf16 v[140:143], a[152:155], v[132:135], v[140:143]\n"
"v_mfma_f32_16x16x32_bf16 v[140:143], a[156:159], v[144:147], v[140:143]\n"
"v_mfma_f32_16x16x32_bf16 v[96:99], a[188:191], v[144:147], 0\n"
"buffer_load_dwordx4 v[124:127], v0, s[4:7], s98 offen offset:2048 sc1\n"
"v_mfma_f32_16x16x32_bf16 v[96:99], a[184:187], v[132:135], v[96:99]\n"
"s_mov_b32 s96, 0xaaaaaaaa\n"
"s_mov_b32 s97, 0xaaaaaaaa\n"
"v_mfma_f32_16x16x32_bf16 v[96:99], a[180:183], v[116:119], v[96:99]\n"
"v_mfma_f32_16x16x32_bf16 v[96:99], a[176:179], v[92:95], v[96:99]\n"
"v_mfma_f32_16x16x32_bf16 v[96:99], a[172:175], v[88:91], v[96:99]\n"
"v_cmp_u_f32_e32 vcc, v140, v140\n"
"v_cndmask_b32_e64 v152, v152, v153, s[96:97]\n"
"v_mfma_f32_16x16x32_bf16 v[96:99], a[168:171], v[104:107], v[96:99]\n"
"s_cbranch_vccnz .Lmk_p110s\n"
".Lmk_p110r:\n"
"v_cndmask_b32_e64 v154, v154, v155, s[96:97]\n"
"v_mfma_f32_16x16x32_bf16 v[96:99], a[164:167], v[176:179], v[96:99]\n"
"ds_write_b128 v3, v[140:143]\n"
"v_mfma_f32_16x16x32_bf16 v[96:99], a[160:163], v[100:103], v[96:99]\n"
"buffer_load_dwordx4 v[140:143], v0, s[4:7], s98 offen offset:3072 sc1\n"
"v_mfma_f32_16x16x32_bf16 v[168:171], a[192:195], v[100:103], 0\n"
"s_mov_b32 s96, 0xcccccccc\n"
"s_mov_b32 s97, 0xcccccccc\n"
"v_mfma_f32_16x16x32_bf16 v[168:171], a[196:199], v[176:179], v[168:171]\n"
"v_cndmask_b32_e64 v165, v152, v154, s[96:97]\n"
"v_mfma_f32_16x16x32_bf16 v[168:171], a[200:203], v[104:107], v[168:171]\n"
"s_mov_b32 s96, 0xf0000f\n"
"s_mov_b32 s97, 0xf0000f00\n"
"v_mfma_f32_16x16x32_bf16 v[168:171], a[204:207], v[88:91], v[168:171]\n"
"buffer_load_dwordx4 v[152:155], v1, s[4:7], s98 offen sc1\n"
"v_mfma_f32_16x16x32_bf16 v[168:171], a[208:211], v[92:95], v[168:171]\n"
"v_cndmask_b32_e64 v164, v164, v165, s[96:97]\n"
"s_add_u32 s97, s63, 0x44000\n"
"v_mfma_f32_16x16x32_bf16 v[168:171], a[212:215], v[116:119], v[168:171]\n"
"ds_write_b128 v3, v[96:99] offset:1024\n"
"v_mfma_f32_16x16x32_bf16 v[168:171], a[216:219], v[132:135], v[168:171]\n"
"buffer_store_dword v164, v14, s[4:7], s97 offen sc1\n"
"v_mfma_f32_16x16x32_bf16 v[168:171], a[220:223], v[144:147], v[168:171]\n"
"buffer_load_dwordx4 v[164:167], v1, s[4:7], s98 offen offset:1024 sc1\n"
"v_mfma_f32_16x16x32_bf16 v[96:99], a[252:255], v[144:147], 0\n"
"v_mfma_f32_16x16x32_bf16 v[96:99], a[248:251], v[132:135], v[96:99]\n"
"v_mfma_f32_16x16x32_bf16 v[96:99], a[244:247], v[116:119], v[96:99]\n"
"v_mfma_f32_16x16x32_bf16 v[96:99], a[240:243], v[92:95], v[96:99]\n"
"buffer_load_dwordx4 v[116:119], v1, s[4:7], s98 offen offset:2048 sc1\n"
"v_mfma_f32_16x16x32_bf16 v[96:99], a[236:239], v[88:91], v[96:99]\n"
"v_mfma_f32_16x16x32_bf16 v[96:99], a[232:235], v[104:107], v[96:99]\n"
"ds_write_b128 v3, v[168:171] offset:2048\n"
"v_mfma_f32_16x16x32_bf16 v[96:99], a[228:231], v[176:179], v[96:99]\n"
"v_mfma_f32_16x16x32_bf16 v[96:99], a[224:227], v[100:103], v[96:99]\n"
"s_waitcnt vmcnt(14)\n"
"buffer_load_dwordx4 v[132:135], v1, s[4:7], s98 offen offset:3072 sc1\n"
"; @A G2 a.1\n"
"v_mfma_f32_16x16x32_bf16 v[88:91], a[128:131], v[156:159], 0\n"
"v_mfma_f32_16x16x32_bf16 v[88:91], a[132:135], v[108:111], v[88:91]\n"
"s_add_u32 s98, s59, 0\n"
"v_mfma_f32_16x16x32_bf16 v[88:91], a[136:139], v[112:115], v[88:91]\n"
"v_mfma_f32_16x16x32_bf16 v[88:91], a[140:143], v[196:199], v[88:91]\n"
"s_waitcnt vmcnt(10)\n"
"buffer_load_dwordx4 v[172:175], v0, s[4:7], s98 offen sc1\n"
"v_mfma_f32_16x16x32_bf16 v[88:91], a[144:147], v[200:203], v[88:91]\n"
"v_mfma_f32_16x16x32_bf16 v[88:91], a[148:151], v[128:131], v[88:91]\n"
"ds_write_b128 v3, v[96:99] offset:3072\n"
"v_mfma_f32_16x16x32_bf16 v[88:91], a[152:155], v[120:123], v[88:91]\n"
"v_mfma_f32_16x16x32_bf16 v[88:91], a[156:159], v[136:139], v[88:91]\n"
"buffer_load_dwordx4 v[176:179], v0, s[4:7], s98 offen offset:1024 sc1\n"
"v_mfma_f32_16x16x32_bf16 v[92:95], a[188:191], v[136:139], 0\n"
"v_mfma_f32_16x16x32_bf16 v[92:95], a[184:187], v[120:123], v[92:95]\n"
"v_mfma_f32_16x16x32_bf16 v[92:95], a[180:183], v[128:131], v[92:95]\n"
"v_mfma_f32_16x16x32_bf16 v[92:95], a[176:179], v[200:203], v[92:95]\n"
"buffer_load_dwordx4 v[168:171], v0, s[4:7], s98 offen offset:2048 sc1\n"
"v_mfma_f32_16x16x32_bf16 v[92:95], a[172:175], v[196:199], v[92:95]\n"
"s_nop 0\n"
"v_cmp_u_f32_e32 vcc, v88, v88\n"
"v_mfma_f32_16x16x32_bf16 v[92:95], a[168:171], v[112:115], v[92:95]\n"
"s_cbranch_vccnz .Lmk_p111s\n"
".Lmk_p111r:\n"
"v_mfma_f32_16x16x32_bf16 v[92:95], a[164:167], v[108:111], v[92:95]\n"
"ds_write_b128 v3, v[88:91] offset:6144\n"
"v_mfma_f32_16x16x32_bf16 v[92:95], a[160:163], v[156:159], v[92:95]\n"
"buffer_load_dwordx4 v[180:183], v0, s[4:7], s98 offen offset:3072 sc1\n"
"v_mfma_f32_16x16x32_bf16 v[88:91], a[192:195], v[156:159], 0\n"
"v_mfma_f32_16x16x32_bf16 v[88:91], a[196:199], v[108:111], v[88:91]\n"
"v_mfma_f32_16x16x32_bf16 v[88:91], a[200:203], v[112:115], v[88:91]\n"
"v_mfma_f32_16x16x32_bf16 v[88:91], a[204:207], v[196:199], v[88:91]\n"
"buffer_load_dwordx4 v[184:187], v1, s[4:7], s98 offen sc1\n"
"v_mfma_f32_16x16x32_bf16 v[88:91], a[208:211], v[200:203], v[88:91]\n"
"ds_read_b128 v[96:99], v2\n"
"ds_read_b128 v[100:103], v2 offset:1024\n"
"v_mfma_f32_16x16x32_bf16 v[88:91], a[212:215], v[128:131], v[88:91]\n"
"ds_write_b128 v3, v[92:95] offset:7168\n"
"v_mfma_f32_16x16x32_bf16 v[88:91], a[216:219], v[120:123], v[88:91]\n"
"v_mfma_f32_16x16x32_bf16 v[88:91], a[220:223], v[136:139], v[88:91]\n"
"buffer_load_dwordx4 v[188:191], v1, s[4:7], s98 offen offset:1024 sc1\n"
"v_mfma_f32_16x16x32_bf16 v[92:95], a[252:255], v[136:139], 0\n"
"v_mfma_f32_16x16x32_bf16 v[92:95], a[248:251], v[120:123], v[92:95]\n"
"s_waitcnt lgkmcnt(1)\n"
"v_lshlrev_b32_e32 v255, 16, v96\n"
"v_and_b32_e32 v254, 0xffff0000, v96\n"
"v_mfma_f32_16x16x32_bf16 v[92:95], a[244:247], v[128:131], v[92:95]\n"
"v_lshlrev_b32_e32 v253, 16, v97\n"
"v_and_b32_e32 v252, 0xffff0000, v97\n"
"v_mfma_f32_16x16x32_bf16 v[92:95], a[240:243], v[200:203], v[92:95]\n"
"buffer_load_dwordx4 v[192:195], v1, s[4:7], s98 offen offset:2048 sc1\n"
"v_mfma_f32_16x16x32_bf16 v[92:95], a[236:239], v[196:199], v[92:95]\n"
"v_lshlrev_b32_e32 v251, 16, v98\n"
"v_and_b32_e32 v250, 0xffff0000, v98\n"
"v_mfma_f32_16x16x32_bf16 v[92:95], a[232:235], v[112:115], v[92:95]\n"
"ds_write_b128 v3, v[88:91] offset:8192\n"
"v_mfma_f32_16x16x32_bf16 v[92:95], a[228:231], v[108:111], v[92:95]\n"
"v_lshlrev_b32_e32 v249, 16, v99\n"
"v_and_b32_e32 v248, 0xffff0000, v99\n"
"v_mfma_f32_16x16x32_bf16 v[92:95], a[224:227], v[156:159], v[92:95]\n"
"s_waitcnt vmcnt(12)\n"
"buffer_load_dwordx4 v[196:199], v1, s[4:7], s98 offen offset:3072 sc1\n"
"; @A G2 a.2\n"
"v_mfma_f32_16x16x32_bf16 v[96:99], a[128:131], v[148:151], 0\n"
"v_lshlrev_b32_e32 v247, 16, v100\n"
"v_and_b32_e32 v246, 0xffff0000, v100\n"
"v_mfma_f32_16x16x32_bf16 v[96:99], a[132:135], v[160:163], v[96:99]\n"
"v_lshlrev_b32_e32 v245, 16, v101\n"
"v_and_b32_e32 v244, 0xffff0000, v101\n"
"v_mfma_f32_16x16x32_bf16 v[96:99], a[136:139], v[124:127], v[96:99]\n"
"v_lshlrev_b32_e32 v243, 16, v102\n"
"v_and_b32_e32 v242, 0xffff0000, v102\n"
"v_mfma_f32_16x16x32_bf16 v[96:99], a[140:143], v[140:143], v[96:99]\n"
"s_waitcnt vmcnt(8)\n"
"v_lshlrev_b32_e32 v241, 16, v103\n"
"v_and_b32_e32 v240, 0xffff0000, v103\n"
"v_mfma_f32_16x16x32_bf16 v[96:99], a[144:147], v[152:155], v[96:99]\n"
"v_mfma_f32_16x16x32_bf16 v[96:99], a[148:151], v[164:167], v[96:99]\n"
"ds_write_b128 v3, v[92:95] offset:9216\n"
"v_mfma_f32_16x16x32_bf16 v[96:99], a[152:155], v[116:119], v[96:99]\n"
"v_mfma_f32_16x16x32_bf16 v[96:99], a[156:159], v[132:135], v[96:99]\n"
"v_mfma_f32_16x16x32_bf16 v[104:107], a[188:191], v[132:135], 0\n"
"s_add_u32 s98, s52, 0x440000\n"
"buffer_load_dwordx4 v[88:91], v0, s[4:7], s98 offen sc1\n"
"v_mfma_f32_16x16x32_bf16 v[104:107], a[184:187], v[116:119], v[104:107]\n"
"s_waitcnt lgkmcnt(0)\n"
"s_barrier\n"
"v_mfma_f32_16x16x32_bf16 v[104:107], a[180:183], v[164:167], v[104:107]\n"
"ds_read_b128 v[144:147], v4\n"
"ds_read_b128 v[112:115], v5\n"
"v_mfma_f32_16x16x32_bf16 v[104:107], a[176:179], v[152:155], v[104:107]\n"
"buffer_load_dwordx4 v[92:95], v0, s[4:7], s98 offen offset:1024 sc1\n"
"v_mfma_f32_16x16x32_bf16 v[104:107], a[172:175], v[140:143], v[104:107]\n"
"v_cmp_u_f32_e32 vcc, v96, v96\n"
"v_mfma_f32_16x16x32_bf16 v[104:107], a[168:171], v[124:127], v[104:107]\n"
"s_cbranch_vccnz .Lmk_p112s\n"
".Lmk_p112r:\n"
"v_mfma_f32_16x16x32_bf16 v[104:107], a[164:167], v[160:163], v[104:107]\n"
"ds_write_b128 v3, v[96:99] offset:12288\n"
"v_mfma_f32_16x16x32_bf16 v[104:107], a[160:163], v[148:151], v[104:107]\n"
"buffer_load_dwordx4 v[96:99], v0, s[4:7], s98 offen offset:2048 sc1\n"
"v_mfma_f32_16x16x32_bf16 v[120:123], a[192:195], v[148:151], 0\n"
"ds_read_b128 v[156:159], v4 offset:1024\n"
"ds_read_b128 v[128:131], v5 offset:1024\n"
"v_mfma_f32_16x16x32_bf16 v[120:123], a[196:199], v[160:163], v[120:123]\n"
"s_waitcnt lgkmcnt(3)\n"
"v_add_f32_e32 v144, v144, v255\n"
"v_add_f32_e32 v145, v145, v254\n"
"v_mfma_f32_16x16x32_bf16 v[120:123], a[200:203], v[124:127], v[120:123]\n"
"buffer_load_dwordx4 v[100:103], v0, s[4:7], s98 offen offset:3072 sc1\n"
"v_mfma_f32_16x16x32_bf16 v[120:123], a[204:207], v[140:143], v[120:123]\n"
"v_add_f32_e32 v146, v146, v253\n"
"v_add_f32_e32 v147, v147, v252\n"
"v_mfma_f32_16x16x32_bf16 v[120:123], a[208:211], v[152:155], v[120:123]\n"
"v_add_f32_e32 v144, v144, v112\n"
"v_add_f32_e32 v145, v145, v113\n"
"v_mfma_f32_16x16x32_bf16 v[120:123], a[212:215], v[164:167], v[120:123]\n"
"ds_write_b128 v3, v[104:107] offset:13312\n"
"v_mfma_f32_16x16x32_bf16 v[120:123], a[216:219], v[116:119], v[120:123]\n"
"buffer_load_dwordx4 v[104:107], v1, s[4:7], s98 offen sc1\n"
"v_mfma_f32_16x16x32_bf16 v[120:123], a[220:223], v[132:135], v[120:123]\n"
"ds_read_b128 v[108:111], v4 offset:2048\n"
"ds_read_b128 v[136:139], v5 offset:2048\n"
"v_mfma_f32_16x16x32_bf16 v[200:203], a[252:255], v[132:135], 0\n"
"s_waitcnt lgkmcnt(3)\n"
"v_add_f32_e32 v146, v146, v114\n"
"v_add_f32_e32 v147, v147, v115\n"
"v_mfma_f32_16x16x32_bf16 v[200:203], a[248:251], v[116:119], v[200:203]\n"
"buffer_load_dwordx4 v[112:115], v1, s[4:7], s98 offen offset:1024 sc1\n"
"v_mfma_f32_16x16x32_bf16 v[200:203], a[244:247], v[164:167], v[200:203]\n"
"v_add_f32_e32 v156, v156, v251\n"
"v_add_f32_e32 v157, v157, v250\n"
"v_mfma_f32_16x16x32_bf16 v[200:203], a[240:243], v[152:155], v[200:203]\n"
"v_add_f32_e32 v158, v158, v249\n"
"v_add_f32_e32 v159, v159, v248\n"
"v_mfma_f32_16x16x32_bf16 v[200:203], a[236:239], v[140:143], v[200:203]\n"
"buffer_load_dwordx4 v[116:119], v1, s[4:7], s98 offen offset:2048 sc1\n"
"v_mfma_f32_16x16x32_bf16 v[200:203], a[232:235], v[124:127], v[200:203]\n"
"ds_write_b128 v3, v[120:123] offset:14336\n"
"v_mfma_f32_16x16x32_bf16 v[200:203], a[228:231], v[160:163], v[200:203]\n"
"ds_read_b128 v[120:123], v4 offset:3072\n"
"ds_read_b128 v[152:155], v5 offset:3072\n"
"v_mfma_f32_16x16x32_bf16 v[200:203], a[224:227], v[148:151], v[200:203]\n"
"s_waitcnt vmcnt(11)\n"
"buffer_load_dwordx4 v[124:127], v1, s[4:7], s98 offen offset:3072 sc1\n"
"; @A G2 a.3\n"
"v_mfma_f32_16x16x32_bf16 v[164:167], a[128:131], v[172:175], v[144:147]\n"
"s_add_u32 s98, s53, 0x440000\n"
"v_add_f32_e32 v156, v156, v128\n"
"v_add_f32_e32 v157, v157, v129\n"
"v_mfma_f32_16x16x32_bf16 v[164:167], a[132:135], v[176:179], v[164:167]\n"
"s_waitcnt lgkmcnt(3)\n"
"v_add_f32_e32 v158, v158, v130\n"
"v_add_f32_e32 v159, v159, v131\n"
"v_mfma_f32_16x16x32_bf16 v[164:167], a[136:139], v[168:171], v[164:167]\n"
"buffer_load_dwordx4 v[128:131], v0, s[4:7], s98 offen sc1\n"
"v_mfma_f32_16x16x32_bf16 v[164:167], a[140:143], v[180:183], v[164:167]\n"
"s_waitcnt vmcnt(9)\n"
"v_add_f32_e32 v108, v108, v247\n"
"v_add_f32_e32 v109, v109, v246\n"
"v_mfma_f32_16x16x32_bf16 v[164:167], a[144:147], v[184:187], v[164:167]\n"
"v_add_f32_e32 v110, v110, v245\n"
"v_add_f32_e32 v111, v111, v244\n"
"v_mfma_f32_16x16x32_bf16 v[164:167], a[148:151], v[188:191], v[164:167]\n"
"ds_write_b128 v3, v[200:203] offset:15360\n"
"v_mfma_f32_16x16x32_bf16 v[164:167], a[152:155], v[192:195], v[164:167]\n"
"buffer_load_dwordx4 v[132:135], v0, s[4:7], s98 offen offset:1024 sc1\n"
"v_mfma_f32_16x16x32_bf16 v[164:167], a[156:159], v[196:199], v[164:167]\n"
"v_add_f32_e32 v108, v108, v136\n"
"v_add_f32_e32 v109, v109, v137\n"
"v_mfma_f32_16x16x32_bf16 v[200:203], a[188:191], v[196:199], v[156:159]\n"
"s_waitcnt lgkmcnt(1)\n"
"v_add_f32_e32 v110, v110, v138\n"
"v_add_f32_e32 v111, v111, v139\n"
"v_mfma_f32_16x16x32_bf16 v[200:203], a[184:187], v[192:195], v[200:203]\n"
"s_waitcnt lgkmcnt(0)\n"
"s_barrier\n"
"v_mfma_f32_16x16x32_bf16 v[200:203], a[180:183], v[188:191], v[200:203]\n"
"buffer_load_dwordx4 v[136:139], v0, s[4:7], s98 offen offset:2048 sc1\n"
"v_mfma_f32_16x16x32_bf16 v[200:203], a[176:179], v[184:187], v[200:203]\n"
"v_add_f32_e32 v120, v120, v243\n"
"v_add_f32_e32 v121, v121, v242\n"
"v_mfma_f32_16x16x32_bf16 v[200:203], a[172:175], v[180:183], v[200:203]\n"
"v_cmp_u_f32_e32 vcc, v164, v164\n"
"v_add_f32_e32 v122, v122, v241\n"
"v_mfma_f32_16x16x32_bf16 v[200:203], a[168:171], v[168:171], v[200:203]\n"
"s_cbranch_vccnz .Lmk_p113s\n"
".Lmk_p113r:\n"
"v_add_f32_e32 v123, v123, v240\n"
"v_mfma_f32_16x16x32_bf16 v[200:203], a[164:167], v[176:179], v[200:203]\n"
"buffer_load_dwordx4 v[140:143], v0, s[4:7], s98 offen offset:3072 sc1\n"
"v_mfma_f32_16x16x32_bf16 v[200:203], a[160:163], v[172:175], v[200:203]\n"
"v_add_f32_e32 v120, v120, v152\n"
"v_add_f32_e32 v121, v121, v153\n"
"v_mfma_f32_16x16x32_bf16 v[108:111], a[192:195], v[172:175], v[108:111]\n"
"v_add_f32_e32 v122, v122, v154\n"
"v_add_f32_e32 v123, v123, v155\n"
"v_mfma_f32_16x16x32_bf16 v[108:111], a[196:199], v[176:179], v[108:111]\n"
"buffer_load_dwordx4 v[144:147], v1, s[4:7], s98 offen sc1\n"
"v_mfma_f32_16x16x32_bf16 v[108:111], a[200:203], v[168:171], v[108:111]\n"
"ds_read_b128 v[204:207], v6\n"
"ds_read_b128 v[208:211], v6 offset:1024\n"
"v_mfma_f32_16x16x32_bf16 v[108:111], a[204:207], v[180:183], v[108:111]\n"
"ds_read_b128 v[148:151], v6 offset:2048\n"
"ds_read_b128 v[152:155], v6 offset:3072\n"
"v_mfma_f32_16x16x32_bf16 v[108:111], a[208:211], v[184:187], v[108:111]\n"
"buffer_load_dwordx4 v[156:159], v1, s[4:7], s98 offen offset:1024 sc1\n"
"v_mfma_f32_16x16x32_bf16 v[108:111], a[212:215], v[188:191], v[108:111]\n"
"v_mfma_f32_16x16x32_bf16 v[108:111], a[216:219], v[192:195], v[108:111]\n"
"v_mfma_f32_16x16x32_bf16 v[108:111], a[220:223], v[196:199], v[108:111]\n"
"buffer_load_dwordx4 v[160:163], v1, s[4:7], s98 offen offset:2048 sc1\n"
"v_mfma_f32_16x16x32_bf16 v[120:123], a[252:255], v[196:199], v[120:123]\n"
"s_waitcnt lgkmcnt(0)\n"
"v_add_f32_e32 v204, v164, v204\n"
"v_add_f32_e32 v205, v165, v205\n"
"v_mfma_f32_16x16x32_bf16 v[120:123], a[248:251], v[192:195], v[120:123]\n"
"v_add_f32_e32 v206, v166, v206\n"
"v_add_f32_e32 v207, v167, v207\n"
"v_mfma_f32_16x16x32_bf16 v[120:123], a[244:247], v[188:191], v[120:123]\n"
"buffer_load_dwordx4 v[164:167], v1, s[4:7], s98 offen offset:3072 sc1\n"
"v_mfma_f32_16x16x32_bf16 v[120:123], a[240:243], v[184:187], v[120:123]\n"
"v_add_f32_e32 v208, v200, v208\n"
"v_add_f32_e32 v209, v201, v209\n"
"v_mfma_f32_16x16x32_bf16 v[120:123], a[236:239], v[180:183], v[120:123]\n"
"v_add_f32_e32 v210, v202, v210\n"
"v_add_f32_e32 v211, v203, v211\n"
"v_mfma_f32_16x16x32_bf16 v[120:123], a[232:235], v[168:171], v[120:123]\n"
"v_cvt_pk_bf16_f32 v168, v204, v205\n"
"v_cvt_pk_bf16_f32 v169, v206, v207\n"
"v_mfma_f32_16x16x32_bf16 v[120:123], a[228:231], v[176:179], v[120:123]\n"
"s_add_u32 s98, s61, 0x400000\n"
"v_cvt_pk_bf16_f32 v170, v208, v209\n"
"v_cvt_pk_bf16_f32 v171, v210, v211\n"
"v_mfma_f32_16x16x32_bf16 v[120:123], a[224:227], v[172:175], v[120:123]\n"
"buffer_store_dwordx4 v[168:171], v0, s[4:7], s98 offen sc1\n"
"ds_write_b128 v2, v[168:171]\n"
"s_branch .Lmk_loop\n"
".Lmk_f2s:\n"
";\n"
"s_mov_b32 s1, 0x10000\n"
".Lmk_f2l:\n"
"buffer_load_dwordx2 v[254:255], v13, s[4:7], s98 offen sc1\n"
"s_waitcnt vmcnt(0)\n"
"v_cmp_ne_u64_e32 vcc, v[254:255], v[18:19]\n"
"s_cbranch_vccz .Lmk_f2r\n"
"s_sub_u32 s1, s1, 1\n"
"s_cbranch_scc0 .Lmk_f2l\n"
"s_branch .Lmk_f2r\n"
".Lmk_f1s:\n"
";\n"
"s_mov_b32 s1, 0x10000\n"
".Lmk_f1l:\n"
"buffer_load_dwordx2 v[252:253], v12, s[4:7], s97 offen sc1\n"
"s_waitcnt vmcnt(0)\n"
"v_cmp_ne_u64_e32 vcc, v[252:253], v[18:19]\n"
"s_cbranch_vccz .Lmk_f1r\n"
"s_sub_u32 s1, s1, 1\n"
"s_cbranch_scc0 .Lmk_f1l\n"
"s_branch .Lmk_f1r\n"
".Lmk_f6s:\n"
";\n"
"s_mov_b32 s1, 0x10000\n"
".Lmk_f6l:\n"
"buffer_load_dwordx2 v[250:251], v41, s[4:7], s97 offen sc1\n"
"s_waitcnt vmcnt(0)\n"
"v_cmp_ne_u64_e32 vcc, v[250:251], v[18:19]\n"
"s_cbranch_vccz .Lmk_f6r\n"
"s_sub_u32 s1, s1, 1\n"
"s_cbranch_scc0 .Lmk_f6l\n"
"s_branch .Lmk_f6r\n"
".Lmk_f3s:\n"
";\n"
"s_mov_b32 s1, 0x10000\n"
".Lmk_f3l:\n"
"buffer_load_dwordx2 v[246:247], v12, s[4:7], s97 offen sc1\n"
"s_waitcnt vmcnt(0)\n"
"v_cmp_ne_u64_e32 vcc, v[246:247], v[18:19]\n"
"s_cbranch_vccz .Lmk_f3r\n"
"s_sub_u32 s1, s1, 1\n"
"s_cbranch_scc0 .Lmk_f3l\n"
"s_branch .Lmk_f3r\n"
".Lmk_f7s:\n"
";\n"
"s_mov_b32 s1, 0x10000\n"
".Lmk_f7l:\n"
"buffer_load_dwordx2 v[246:247], v13, s[4:7], s98 offen sc1\n"
"s_waitcnt vmcnt(0)\n"
"v_cmp_ne_u64_e32 vcc, v[246:247], v[18:19]\n"
"s_cbranch_vccz .Lmk_f7r\n"
"s_sub_u32 s1, s1, 1\n"
"s_cbranch_scc0 .Lmk_f7l\n"
"s_branch .Lmk_f7r\n"
".Lmk_f4s:\n"
";\n"
"s_mov_b32 s1, 0x10000\n"
".Lmk_f4l:\n"
"buffer_load_dwordx2 v[250:251], off, s[4:7], s96 sc1\n"
"s_waitcnt vmcnt(0)\n"
"v_cmp_ne_u64_e32 vcc, v[250:251], v[18:19]\n"
"s_cbranch_vccz .Lmk_f4r\n"
"s_sub_u32 s1, s1, 1\n"
"s_cbranch_scc0 .Lmk_f4l\n"
"s_branch .Lmk_f4r\n"
".Lmk_x18s:\n"
"s_nop 3\n"
"v_mov_b32_e32 v24, 0xff800000\n"
"v_mov_b32_e32 v25, 0xff800000\n"
"v_mov_b32_e32 v26, 0xff800000\n"
"v_mov_b32_e32 v27, 0xff800000\n"
"v_mov_b32_e32 v36, 0xff800000\n"
"v_mov_b32_e32 v37, 0xff800000\n"
"v_mov_b32_e32 v38, 0xff800000\n"
"v_mov_b32_e32 v39, 0xff800000\n"
"s_nop 1\n"
"s_branch .Lmk_x18r\n"
".Lmk_x19s:\n"
"s_nop 3\n"
"v_mov_b32_e32 v24, 0xff800000\n"
"v_mov_b32_e32 v25, 0xff800000\n"
"v_mov_b32_e32 v26, 0xff800000\n"
"v_mov_b32_e32 v27, 0xff800000\n"
"v_mov_b32_e32 v36, 0xff800000\n"
"v_mov_b32_e32 v37, 0xff800000\n"
"v_mov_b32_e32 v38, 0xff800000\n"
"v_mov_b32_e32 v39, 0xff800000\n"
"s_nop 1\n"
"s_branch .Lmk_x19r\n"
".Lmk_x20s:\n"
"s_nop 3\n"
"v_mov_b32_e32 v24, 0xff800000\n"
"v_mov_b32_e32 v25, 0xff800000\n"
"v_mov_b32_e32 v26, 0xff800000\n"
"v_mov_b32_e32 v27, 0xff800000\n"
"v_mov_b32_e32 v36, 0xff800000\n"
"v_mov_b32_e32 v37, 0xff800000\n"
"v_mov_b32_e32 v38, 0xff800000\n"
"v_mov_b32_e32 v39, 0xff800000\n"
"s_nop 1\n"
"s_branch .Lmk_x20r\n"
".Lmk_x21s:\n"
"s_nop 3\n"
"v_mov_b32_e32 v24, 0xff800000\n"
"v_mov_b32_e32 v25, 0xff800000\n"
"v_mov_b32_e32 v26, 0xff800000\n"
"v_mov_b32_e32 v27, 0xff800000\n"
"v_mov_b32_e32 v36, 0xff800000\n"
"v_mov_b32_e32 v37, 0xff800000\n"
"v_mov_b32_e32 v38, 0xff800000\n"
"v_mov_b32_e32 v39, 0xff800000\n"
"s_nop 1\n"
"s_branch .Lmk_x21r\n"
".Lmk_f14s:\n"
";\n"
"s_mov_b32 s1, 0x10000\n"
".Lmk_f14l:\n"
"buffer_load_dwordx2 v[238:239], off, s[4:7], s97 sc1\n"
"s_waitcnt vmcnt(0)\n"
"v_cmp_ne_u64_e32 vcc, v[238:239], v[18:19]\n"
"s_cbranch_vccz .Lmk_f14r\n"
"s_sub_u32 s1, s1, 1\n"
"s_cbranch_scc0 .Lmk_f14l\n"
"s_branch .Lmk_f14r\n"
".Lmk_b16:\n"
"s_cmp_lg_u32 s2, 0\n"
"s_cbranch_scc1 .Lmk_k22\n"
"s_mov_b32 s2, 1\n"
"s_waitcnt vmcnt(0) lgkmcnt(0)\n"
"s_nop 7\n"
"s_mov_b32 s97, 11\n"
"v_add_u32_e32 v248, 0x3800, v15\n"
"v_add_u32_e32 v247, 0x3800, v254\n"
"ds_read_b128 v[36:39], v15 offset:24576\n"
"ds_read_b128 v[40:43], v254 offset:24832\n"
"s_waitcnt lgkmcnt(0)\n"
"v_mfma_f32_16x16x32_bf16 v[48:51], v[36:39], v[28:31], 0\n"
"v_mfma_f32_16x16x32_bf16 v[52:55], v[36:39], v[32:35], 0\n"
"v_mfma_f32_16x16x32_bf16 v[36:39], v[40:43], v[32:35], 0\n"
"v_mfma_f32_16x16x32_bf16 v[56:59], v[40:43], v[28:31], 0\n"
".Lmk_b16l:\n"
"ds_read_b128 v[40:43], v248 offset:8192\n"
"ds_read_b128 v[60:63], v247 offset:8448\n"
"s_cmp_gt_i32 s78, s97\n"
"s_cselect_b32 s96, 0xff800000, 0\n"
"v_mov_b32_e32 v64, s96\n"
"v_mov_b32_e32 v65, s96\n"
"v_mov_b32_e32 v66, s96\n"
"v_mov_b32_e32 v67, s96\n"
"v_subrev_u32_e32 v248, 0x800, v248\n"
"v_subrev_u32_e32 v247, 0x800, v247\n"
"s_waitcnt lgkmcnt(0)\n"
"v_mfma_f32_16x16x32_bf16 v[72:75], v[40:43], v[28:31], v[64:67]\n"
"v_mfma_f32_16x16x32_bf16 v[76:79], v[40:43], v[32:35], v[64:67]\n"
"v_mfma_f32_16x16x32_bf16 v[40:43], v[60:63], v[32:35], v[64:67]\n"
"v_mfma_f32_16x16x32_bf16 v[84:87], v[60:63], v[28:31], v[64:67]\n"
"s_nop 7\n"
"s_cmp_lg_u32 s97, 4\n"
"s_cbranch_scc1 .Lmk_b16m\n"
"v_cndmask_b32_e64 v72, v48, v72, s[20:21]\n"
"v_cndmask_b32_e64 v73, v49, v73, s[22:23]\n"
"v_cndmask_b32_e64 v74, v50, v74, s[24:25]\n"
"v_cndmask_b32_e64 v75, v51, v75, s[26:27]\n"
"v_cndmask_b32_e64 v84, v56, v84, s[28:29]\n"
"v_cndmask_b32_e64 v85, v57, v85, s[30:31]\n"
"v_cndmask_b32_e64 v86, v58, v86, s[32:33]\n"
"v_cndmask_b32_e64 v87, v59, v87, s[34:35]\n"
"v_cndmask_b32_e64 v76, v52, v76, s[36:37]\n"
"v_cndmask_b32_e64 v77, v53, v77, s[38:39]\n"
"v_cndmask_b32_e64 v78, v54, v78, s[40:41]\n"
"v_cndmask_b32_e64 v79, v55, v79, s[42:43]\n"
"v_cndmask_b32_e64 v40, v36, v40, s[44:45]\n"
"v_cndmask_b32_e64 v41, v37, v41, s[46:47]\n"
"v_cndmask_b32_e64 v42, v38, v42, s[48:49]\n"
"v_cndmask_b32_e64 v43, v39, v43, s[50:51]\n"
".Lmk_b16m:\n"
"v_max3_f32 v253, v253, v72, v73\n"
"v_max3_f32 v251, v251, v76, v77\n"
"v_max3_f32 v253, v253, v74, v75\n"
"v_max3_f32 v251, v251, v78, v79\n"
"v_max3_f32 v253, v253, v84, v85\n"
"v_max3_f32 v251, v251, v40, v41\n"
"v_max3_f32 v253, v253, v86, v87\n"
"v_max3_f32 v251, v251, v42, v43\n"
"s_sub_u32 s97, s97, 1\n"
"s_cmp_ge_i32 s97, 4\n"
"s_cbranch_scc1 .Lmk_b16l\n"
"v_mov_b32_e32 v248, v253\n"
"v_mov_b32_e32 v247, v251\n"
"s_nop 0\n"
"v_permlane16_swap_b32_e32 v253, v248\n"
"v_permlane16_swap_b32_e32 v251, v247\n"
"v_max_f32_e32 v253, v253, v248\n"
"v_max_f32_e32 v251, v251, v247\n"
"v_mov_b32_e32 v248, v253\n"
"v_mov_b32_e32 v247, v251\n"
"s_nop 0\n"
"v_permlane32_swap_b32_e32 v253, v248\n"
"v_permlane32_swap_b32_e32 v251, v247\n"
"v_max_f32_e32 v253, v253, v248\n"
"v_max_f32_e32 v251, v251, v247\n"
"v_mov_b32_e32 v250, v253\n"
"v_mov_b32_e32 v249, v251\n"
"s_branch .Lmk_a15\n"
".Lmk_x25s:\n"
"s_nop 3\n"
"v_mov_b32_e32 v116, 0xff800000\n"
"v_mov_b32_e32 v117, 0xff800000\n"
"v_mov_b32_e32 v118, 0xff800000\n"
"v_mov_b32_e32 v119, 0xff800000\n"
"v_mov_b32_e32 v120, 0xff800000\n"
"v_mov_b32_e32 v121, 0xff800000\n"
"v_mov_b32_e32 v122, 0xff800000\n"
"v_mov_b32_e32 v123, 0xff800000\n"
"s_nop 1\n"
"s_branch .Lmk_x25r\n"
".Lmk_x26s:\n"
"s_nop 3\n"
"v_mov_b32_e32 v116, 0xff800000\n"
"v_mov_b32_e32 v117, 0xff800000\n"
"v_mov_b32_e32 v118, 0xff800000\n"
"v_mov_b32_e32 v119, 0xff800000\n"
"v_mov_b32_e32 v120, 0xff800000\n"
"v_mov_b32_e32 v121, 0xff800000\n"
"v_mov_b32_e32 v122, 0xff800000\n"
"v_mov_b32_e32 v123, 0xff800000\n"
"s_nop 1\n"
"s_branch .Lmk_x26r\n"
".Lmk_x27s:\n"
"s_nop 3\n"
"v_mov_b32_e32 v116, 0xff800000\n"
"v_mov_b32_e32 v117, 0xff800000\n"
"v_mov_b32_e32 v118, 0xff800000\n"
"v_mov_b32_e32 v119, 0xff800000\n"
"v_mov_b32_e32 v120, 0xff800000\n"
"v_mov_b32_e32 v121, 0xff800000\n"
"v_mov_b32_e32 v122, 0xff800000\n"
"v_mov_b32_e32 v123, 0xff800000\n"
"s_nop 1\n"
"s_branch .Lmk_x27r\n"
".Lmk_f5s:\n"
";\n"
"s_mov_b32 s1, 0x10000\n"
".Lmk_f5l:\n"
"buffer_load_dwordx2 v[246:247], v12, s[4:7], s96 offen sc1\n"
"s_waitcnt vmcnt(0)\n"
"v_cmp_ne_u64_e32 vcc, v[246:247], v[18:19]\n"
"s_cbranch_vccz .Lmk_f5r\n"
"s_sub_u32 s1, s1, 1\n"
"s_cbranch_scc0 .Lmk_f5l\n"
"s_branch .Lmk_f5r\n"
".Lmk_b24:\n"
"s_cmp_lg_u32 s2, 0\n"
"s_cbranch_scc1 .Lmk_k29\n"
"s_mov_b32 s2, 1\n"
"s_waitcnt vmcnt(0) lgkmcnt(0)\n"
"s_nop 7\n"
"s_mov_b32 s96, 7\n"
"v_add_u32_e32 v248, 0x3800, v15\n"
"v_add_u32_e32 v247, 0x3800, v254\n"
"ds_read_b128 v[172:175], v15 offset:16384\n"
"ds_read_b128 v[192:195], v254 offset:16640\n"
"s_waitcnt lgkmcnt(0)\n"
"v_mfma_f32_16x16x32_bf16 v[196:199], v[172:175], v[92:95], 0\n"
"v_mfma_f32_16x16x32_bf16 v[212:215], v[172:175], v[96:99], 0\n"
"v_mfma_f32_16x16x32_bf16 v[172:175], v[192:195], v[96:99], 0\n"
"v_mfma_f32_16x16x32_bf16 v[220:223], v[192:195], v[92:95], 0\n"
".Lmk_b24l:\n"
"ds_read_b128 v[192:195], v248\n"
"ds_read_b128 v[224:227], v247 offset:256\n"
"s_cmp_gt_i32 s78, s96\n"
"s_cselect_b32 s95, 0xff800000, 0\n"
"v_mov_b32_e32 v228, s95\n"
"v_mov_b32_e32 v229, s95\n"
"v_mov_b32_e32 v230, s95\n"
"v_mov_b32_e32 v231, s95\n"
"v_subrev_u32_e32 v248, 0x800, v248\n"
"v_subrev_u32_e32 v247, 0x800, v247\n"
"s_waitcnt lgkmcnt(0)\n"
"v_mfma_f32_16x16x32_bf16 v[232:235], v[192:195], v[92:95], v[228:231]\n"
"v_mfma_f32_16x16x32_bf16 v[236:239], v[192:195], v[96:99], v[228:231]\n"
"v_mfma_f32_16x16x32_bf16 v[192:195], v[224:227], v[96:99], v[228:231]\n"
"v_mfma_f32_16x16x32_bf16 v[240:243], v[224:227], v[92:95], v[228:231]\n"
"s_nop 7\n"
"s_cmp_lg_u32 s96, 0\n"
"s_cbranch_scc1 .Lmk_b24m\n"
"v_cndmask_b32_e64 v232, v196, v232, s[20:21]\n"
"v_cndmask_b32_e64 v233, v197, v233, s[22:23]\n"
"v_cndmask_b32_e64 v234, v198, v234, s[24:25]\n"
"v_cndmask_b32_e64 v235, v199, v235, s[26:27]\n"
"v_cndmask_b32_e64 v240, v220, v240, s[28:29]\n"
"v_cndmask_b32_e64 v241, v221, v241, s[30:31]\n"
"v_cndmask_b32_e64 v242, v222, v242, s[32:33]\n"
"v_cndmask_b32_e64 v243, v223, v243, s[34:35]\n"
"v_cndmask_b32_e64 v236, v212, v236, s[36:37]\n"
"v_cndmask_b32_e64 v237, v213, v237, s[38:39]\n"
"v_cndmask_b32_e64 v238, v214, v238, s[40:41]\n"
"v_cndmask_b32_e64 v239, v215, v239, s[42:43]\n"
"v_cndmask_b32_e64 v192, v172, v192, s[44:45]\n"
"v_cndmask_b32_e64 v193, v173, v193, s[46:47]\n"
"v_cndmask_b32_e64 v194, v174, v194, s[48:49]\n"
"v_cndmask_b32_e64 v195, v175, v195, s[50:51]\n"
".Lmk_b24m:\n"
"v_max3_f32 v253, v253, v232, v233\n"
"v_max3_f32 v251, v251, v236, v237\n"
"v_max3_f32 v253, v253, v234, v235\n"
"v_max3_f32 v251, v251, v238, v239\n"
"v_max3_f32 v253, v253, v240, v241\n"
"v_max3_f32 v251, v251, v192, v193\n"
"v_max3_f32 v253, v253, v242, v243\n"
"v_max3_f32 v251, v251, v194, v195\n"
"s_sub_u32 s96, s96, 1\n"
"s_cmp_ge_i32 s96, 0\n"
"s_cbranch_scc1 .Lmk_b24l\n"
"v_mov_b32_e32 v248, v253\n"
"v_mov_b32_e32 v247, v251\n"
"s_nop 0\n"
"v_permlane16_swap_b32_e32 v253, v248\n"
"v_permlane16_swap_b32_e32 v251, v247\n"
"v_max_f32_e32 v253, v253, v248\n"
"v_max_f32_e32 v251, v251, v247\n"
"v_mov_b32_e32 v248, v253\n"
"v_mov_b32_e32 v247, v251\n"
"s_nop 0\n"
"v_permlane32_swap_b32_e32 v253, v248\n"
"v_permlane32_swap_b32_e32 v251, v247\n"
"v_max_f32_e32 v253, v253, v248\n"
"v_max_f32_e32 v251, v251, v247\n"
"v_mov_b32_e32 v250, v253\n"
"v_mov_b32_e32 v249, v251\n"
"s_branch .Lmk_a23\n"
".Lmk_p30s:\n"
";\n"
"s_waitcnt vmcnt(16)\n"
"s_mov_b32 s1, 0x10000\n"
".Lmk_p30l:\n"
"s_add_u32 s98, s56, 0x40000\n"
"buffer_load_dwordx4 v[176:179], v0, s[4:7], s98 offen sc1\n"
"buffer_load_dwordx4 v[200:203], v0, s[4:7], s98 offen offset:1024 sc1\n"
"buffer_load_dwordx4 v[188:191], v0, s[4:7], s98 offen offset:2048 sc1\n"
"buffer_load_dwordx4 v[204:207], v0, s[4:7], s98 offen offset:3072 sc1\n"
"buffer_load_dwordx4 v[152:155], v1, s[4:7], s98 offen sc1\n"
"buffer_load_dwordx4 v[208:211], v1, s[4:7], s98 offen offset:1024 sc1\n"
"buffer_load_dwordx4 v[120:123], v1, s[4:7], s98 offen offset:2048 sc1\n"
"buffer_load_dwordx4 v[132:135], v1, s[4:7], s98 offen offset:3072 sc1\n"
"s_add_u32 s98, s57, 0x40000\n"
"buffer_load_dwordx4 v[216:219], v0, s[4:7], s98 offen sc1\n"
"buffer_load_dwordx4 v[128:131], v0, s[4:7], s98 offen offset:1024 sc1\n"
"buffer_load_dwordx4 v[180:183], v0, s[4:7], s98 offen offset:2048 sc1\n"
"buffer_load_dwordx4 v[100:103], v0, s[4:7], s98 offen offset:3072 sc1\n"
"buffer_load_dwordx4 v[184:187], v1, s[4:7], s98 offen sc1\n"
"buffer_load_dwordx4 v[104:107], v1, s[4:7], s98 offen offset:1024 sc1\n"
"buffer_load_dwordx4 v[108:111], v1, s[4:7], s98 offen offset:2048 sc1\n"
"buffer_load_dwordx4 v[136:139], v1, s[4:7], s98 offen offset:3072 sc1\n"
"s_add_u32 s98, s58, 0x40000\n"
"buffer_load_dwordx4 v[144:147], v0, s[4:7], s98 offen sc1\n"
"buffer_load_dwordx4 v[140:143], v0, s[4:7], s98 offen offset:1024 sc1\n"
"buffer_load_dwordx4 v[116:119], v0, s[4:7], s98 offen offset:2048 sc1\n"
"buffer_load_dwordx4 v[112:115], v0, s[4:7], s98 offen offset:3072 sc1\n"
"buffer_load_dwordx4 v[92:95], v1, s[4:7], s98 offen sc1\n"
"buffer_load_dwordx4 v[96:99], v1, s[4:7], s98 offen offset:1024 sc1\n"
"buffer_load_dwordx4 v[148:151], v1, s[4:7], s98 offen offset:2048 sc1\n"
"s_waitcnt vmcnt(0)\n"
"v_max_u32_e32 v255, v176, v177\n"
"v_max3_u32 v255, v255, v178, v179\n"
"v_max3_u32 v255, v255, v200, v201\n"
"v_max3_u32 v255, v255, v202, v203\n"
"v_max3_u32 v255, v255, v188, v189\n"
"v_max3_u32 v255, v255, v190, v191\n"
"v_max3_u32 v255, v255, v204, v205\n"
"v_max3_u32 v255, v255, v206, v207\n"
"v_max3_u32 v255, v255, v152, v153\n"
"v_max3_u32 v255, v255, v154, v155\n"
"v_max3_u32 v255, v255, v208, v209\n"
"v_max3_u32 v255, v255, v210, v211\n"
"v_max3_u32 v255, v255, v120, v121\n"
"v_max3_u32 v255, v255, v122, v123\n"
"v_max3_u32 v255, v255, v132, v133\n"
"v_max3_u32 v255, v255, v134, v135\n"
"v_cmp_eq_u32_e32 vcc, -1, v255\n"
"s_cbranch_vccz .Lmk_p30k\n"
"s_sub_u32 s1, s1, 1\n"
"s_cbranch_scc0 .Lmk_p30l\n"
".Lmk_p30k:\n"
"v_mfma_f32_16x16x32_bf16 v[88:91], a[128:131], v[176:179], 0\n"
"v_mfma_f32_16x16x32_bf16 v[88:91], a[132:135], v[200:203], v[88:91]\n"
"v_mfma_f32_16x16x32_bf16 v[88:91], a[136:139], v[188:191], v[88:91]\n"
"v_mfma_f32_16x16x32_bf16 v[88:91], a[140:143], v[204:207], v[88:91]\n"
"v_mfma_f32_16x16x32_bf16 v[88:91], a[144:147], v[152:155], v[88:91]\n"
"v_mfma_f32_16x16x32_bf16 v[88:91], a[148:151], v[208:211], v[88:91]\n"
"v_mfma_f32_16x16x32_bf16 v[88:91], a[152:155], v[120:123], v[88:91]\n"
"v_mfma_f32_16x16x32_bf16 v[88:91], a[156:159], v[132:135], v[88:91]\n"
"v_mfma_f32_16x16x32_bf16 v[124:127], a[188:191], v[132:135], 0\n"
"v_mfma_f32_16x16x32_bf16 v[124:127], a[184:187], v[120:123], v[124:127]\n"
"v_mfma_f32_16x16x32_bf16 v[124:127], a[180:183], v[208:211], v[124:127]\n"
"v_mfma_f32_16x16x32_bf16 v[124:127], a[176:179], v[152:155], v[124:127]\n"
"v_mfma_f32_16x16x32_bf16 v[124:127], a[172:175], v[204:207], v[124:127]\n"
"v_mfma_f32_16x16x32_bf16 v[124:127], a[168:171], v[188:191], v[124:127]\n"
"s_nop 7\n"
"s_branch .Lmk_p30r\n"
".Lmk_p31s:\n"
";\n"
"s_waitcnt vmcnt(16)\n"
"s_mov_b32 s1, 0x10000\n"
".Lmk_p31l:\n"
"s_add_u32 s97, s57, 0x40000\n"
"buffer_load_dwordx4 v[216:219], v0, s[4:7], s97 offen sc1\n"
"buffer_load_dwordx4 v[128:131], v0, s[4:7], s97 offen offset:1024 sc1\n"
"buffer_load_dwordx4 v[180:183], v0, s[4:7], s97 offen offset:2048 sc1\n"
"buffer_load_dwordx4 v[100:103], v0, s[4:7], s97 offen offset:3072 sc1\n"
"buffer_load_dwordx4 v[184:187], v1, s[4:7], s97 offen sc1\n"
"buffer_load_dwordx4 v[104:107], v1, s[4:7], s97 offen offset:1024 sc1\n"
"buffer_load_dwordx4 v[108:111], v1, s[4:7], s97 offen offset:2048 sc1\n"
"buffer_load_dwordx4 v[136:139], v1, s[4:7], s97 offen offset:3072 sc1\n"
"s_add_u32 s97, s58, 0x40000\n"
"buffer_load_dwordx4 v[144:147], v0, s[4:7], s97 offen sc1\n"
"buffer_load_dwordx4 v[140:143], v0, s[4:7], s97 offen offset:1024 sc1\n"
"buffer_load_dwordx4 v[116:119], v0, s[4:7], s97 offen offset:2048 sc1\n"
"buffer_load_dwordx4 v[112:115], v0, s[4:7], s97 offen offset:3072 sc1\n"
"buffer_load_dwordx4 v[92:95], v1, s[4:7], s97 offen sc1\n"
"buffer_load_dwordx4 v[96:99], v1, s[4:7], s97 offen offset:1024 sc1\n"
"buffer_load_dwordx4 v[148:151], v1, s[4:7], s97 offen offset:2048 sc1\n"
"buffer_load_dwordx4 v[88:91], v1, s[4:7], s97 offen offset:3072 sc1\n"
"s_add_u32 s97, s59, 0x40000\n"
"buffer_load_dwordx4 v[160:163], v0, s[4:7], s97 offen sc1\n"
"buffer_load_dwordx4 v[124:127], v0, s[4:7], s97 offen offset:1024 sc1\n"
"buffer_load_dwordx4 v[120:123], v0, s[4:7], s97 offen offset:2048 sc1\n"
"buffer_load_dwordx4 v[132:135], v0, s[4:7], s97 offen offset:3072 sc1\n"
"buffer_load_dwordx4 v[156:159], v1, s[4:7], s97 offen sc1\n"
"buffer_load_dwordx4 v[164:167], v1, s[4:7], s97 offen offset:1024 sc1\n"
"buffer_load_dwordx4 v[172:175], v1, s[4:7], s97 offen offset:2048 sc1\n"
"s_waitcnt vmcnt(0)\n"
"v_max_u32_e32 v255, v216, v217\n"
"v_max3_u32 v255, v255, v218, v219\n"
"v_max3_u32 v255, v255, v128, v129\n"
"v_max3_u32 v255, v255, v130, v131\n"
"v_max3_u32 v255, v255, v180, v181\n"
"v_max3_u32 v255, v255, v182, v183\n"
"v_max3_u32 v255, v255, v100, v101\n"
"v_max3_u32 v255, v255, v102, v103\n"
"v_max3_u32 v255, v255, v184, v185\n"
"v_max3_u32 v255, v255, v186, v187\n"
"v_max3_u32 v255, v255, v104, v105\n"
"v_max3_u32 v255, v255, v106, v107\n"
"v_max3_u32 v255, v255, v108, v109\n"
"v_max3_u32 v255, v255, v110, v111\n"
"v_max3_u32 v255, v255, v136, v137\n"
"v_max3_u32 v255, v255, v138, v139\n"
"v_cmp_eq_u32_e32 vcc, -1, v255\n"
"s_cbranch_vccz .Lmk_p31k\n"
"s_sub_u32 s1, s1, 1\n"
"s_cbranch_scc0 .Lmk_p31l\n"
".Lmk_p31k:\n"
"v_mfma_f32_16x16x32_bf16 v[152:155], a[128:131], v[216:219], 0\n"
"v_mfma_f32_16x16x32_bf16 v[152:155], a[132:135], v[128:131], v[152:155]\n"
"v_mfma_f32_16x16x32_bf16 v[152:155], a[136:139], v[180:183], v[152:155]\n"
"v_mfma_f32_16x16x32_bf16 v[152:155], a[140:143], v[100:103], v[152:155]\n"
"v_mfma_f32_16x16x32_bf16 v[152:155], a[144:147], v[184:187], v[152:155]\n"
"v_mfma_f32_16x16x32_bf16 v[152:155], a[148:151], v[104:107], v[152:155]\n"
"v_mfma_f32_16x16x32_bf16 v[152:155], a[152:155], v[108:111], v[152:155]\n"
"v_mfma_f32_16x16x32_bf16 v[152:155], a[156:159], v[136:139], v[152:155]\n"
"v_mfma_f32_16x16x32_bf16 v[168:171], a[188:191], v[136:139], 0\n"
"v_mfma_f32_16x16x32_bf16 v[168:171], a[184:187], v[108:111], v[168:171]\n"
"v_mfma_f32_16x16x32_bf16 v[168:171], a[180:183], v[104:107], v[168:171]\n"
"v_mfma_f32_16x16x32_bf16 v[168:171], a[176:179], v[184:187], v[168:171]\n"
"v_mfma_f32_16x16x32_bf16 v[168:171], a[172:175], v[100:103], v[168:171]\n"
"v_mfma_f32_16x16x32_bf16 v[168:171], a[168:171], v[180:183], v[168:171]\n"
"s_nop 7\n"
"s_branch .Lmk_p31r\n"
".Lmk_p32s:\n"
";\n"
"s_waitcnt vmcnt(16)\n"
"s_mov_b32 s1, 0x10000\n"
".Lmk_p32l:\n"
"s_add_u32 s97, s58, 0x40000\n"
"buffer_load_dwordx4 v[144:147], v0, s[4:7], s97 offen sc1\n"
"buffer_load_dwordx4 v[140:143], v0, s[4:7], s97 offen offset:1024 sc1\n"
"buffer_load_dwordx4 v[116:119], v0, s[4:7], s97 offen offset:2048 sc1\n"
"buffer_load_dwordx4 v[112:115], v0, s[4:7], s97 offen offset:3072 sc1\n"
"buffer_load_dwordx4 v[92:95], v1, s[4:7], s97 offen sc1\n"
"buffer_load_dwordx4 v[96:99], v1, s[4:7], s97 offen offset:1024 sc1\n"
"buffer_load_dwordx4 v[148:151], v1, s[4:7], s97 offen offset:2048 sc1\n"
"buffer_load_dwordx4 v[88:91], v1, s[4:7], s97 offen offset:3072 sc1\n"
"s_add_u32 s97, s59, 0x40000\n"
"buffer_load_dwordx4 v[160:163], v0, s[4:7], s97 offen sc1\n"
"buffer_load_dwordx4 v[124:127], v0, s[4:7], s97 offen offset:1024 sc1\n"
"buffer_load_dwordx4 v[120:123], v0, s[4:7], s97 offen offset:2048 sc1\n"
"buffer_load_dwordx4 v[132:135], v0, s[4:7], s97 offen offset:3072 sc1\n"
"buffer_load_dwordx4 v[156:159], v1, s[4:7], s97 offen sc1\n"
"buffer_load_dwordx4 v[164:167], v1, s[4:7], s97 offen offset:1024 sc1\n"
"buffer_load_dwordx4 v[172:175], v1, s[4:7], s97 offen offset:2048 sc1\n"
"buffer_load_dwordx4 v[152:155], v1, s[4:7], s97 offen offset:3072 sc1\n"
"s_waitcnt vmcnt(0)\n"
"v_max_u32_e32 v239, v144, v145\n"
"v_max3_u32 v239, v239, v146, v147\n"
"v_max3_u32 v239, v239, v140, v141\n"
"v_max3_u32 v239, v239, v142, v143\n"
"v_max3_u32 v239, v239, v116, v117\n"
"v_max3_u32 v239, v239, v118, v119\n"
"v_max3_u32 v239, v239, v112, v113\n"
"v_max3_u32 v239, v239, v114, v115\n"
"v_max3_u32 v239, v239, v92, v93\n"
"v_max3_u32 v239, v239, v94, v95\n"
"v_max3_u32 v239, v239, v96, v97\n"
"v_max3_u32 v239, v239, v98, v99\n"
"v_max3_u32 v239, v239, v148, v149\n"
"v_max3_u32 v239, v239, v150, v151\n"
"v_max3_u32 v239, v239, v88, v89\n"
"v_max3_u32 v239, v239, v90, v91\n"
"v_cmp_eq_u32_e32 vcc, -1, v239\n"
"s_cbranch_vccz .Lmk_p32k\n"
"s_sub_u32 s1, s1, 1\n"
"s_cbranch_scc0 .Lmk_p32l\n"
".Lmk_p32k:\n"
"v_mfma_f32_16x16x32_bf16 v[100:103], a[128:131], v[144:147], 0\n"
"v_mfma_f32_16x16x32_bf16 v[100:103], a[132:135], v[140:143], v[100:103]\n"
"v_mfma_f32_16x16x32_bf16 v[100:103], a[136:139], v[116:119], v[100:103]\n"
"v_mfma_f32_16x16x32_bf16 v[100:103], a[140:143], v[112:115], v[100:103]\n"
"v_mfma_f32_16x16x32_bf16 v[100:103], a[144:147], v[92:95], v[100:103]\n"
"v_mfma_f32_16x16x32_bf16 v[100:103], a[148:151], v[96:99], v[100:103]\n"
"v_mfma_f32_16x16x32_bf16 v[100:103], a[152:155], v[148:151], v[100:103]\n"
"v_mfma_f32_16x16x32_bf16 v[100:103], a[156:159], v[88:91], v[100:103]\n"
"v_mfma_f32_16x16x32_bf16 v[104:107], a[188:191], v[88:91], 0\n"
"v_mfma_f32_16x16x32_bf16 v[104:107], a[184:187], v[148:151], v[104:107]\n"
"v_mfma_f32_16x16x32_bf16 v[104:107], a[180:183], v[96:99], v[104:107]\n"
"v_mfma_f32_16x16x32_bf16 v[104:107], a[176:179], v[92:95], v[104:107]\n"
"v_mfma_f32_16x16x32_bf16 v[104:107], a[172:175], v[112:115], v[104:107]\n"
"v_mfma_f32_16x16x32_bf16 v[104:107], a[168:171], v[116:119], v[104:107]\n"
"s_nop 7\n"
"s_branch .Lmk_p32r\n"
".Lmk_p33s:\n"
";\n"
"s_waitcnt vmcnt(16)\n"
"s_mov_b32 s1, 0x10000\n"
".Lmk_p33l:\n"
"s_add_u32 s97, s59, 0x40000\n"
"buffer_load_dwordx4 v[160:163], v0, s[4:7], s97 offen sc1\n"
"buffer_load_dwordx4 v[124:127], v0, s[4:7], s97 offen offset:1024 sc1\n"
"buffer_load_dwordx4 v[120:123], v0, s[4:7], s97 offen offset:2048 sc1\n"
"buffer_load_dwordx4 v[132:135], v0, s[4:7], s97 offen offset:3072 sc1\n"
"buffer_load_dwordx4 v[156:159], v1, s[4:7], s97 offen sc1\n"
"buffer_load_dwordx4 v[164:167], v1, s[4:7], s97 offen offset:1024 sc1\n"
"buffer_load_dwordx4 v[172:175], v1, s[4:7], s97 offen offset:2048 sc1\n"
"buffer_load_dwordx4 v[152:155], v1, s[4:7], s97 offen offset:3072 sc1\n"
"s_add_u32 s97, s56, 0\n"
"buffer_load_dwordx4 v[100:103], v0, s[4:7], s97 offen sc1\n"
"buffer_load_dwordx4 v[180:183], v0, s[4:7], s97 offen offset:1024 sc1\n"
"buffer_load_dwordx4 v[104:107], v0, s[4:7], s97 offen offset:2048 sc1\n"
"buffer_load_dwordx4 v[88:91], v0, s[4:7], s97 offen offset:3072 sc1\n"
"buffer_load_dwordx4 v[92:95], v1, s[4:7], s97 offen sc1\n"
"buffer_load_dwordx4 v[116:119], v1, s[4:7], s97 offen offset:1024 sc1\n"
"buffer_load_dwordx4 v[136:139], v1, s[4:7], s97 offen offset:2048 sc1\n"
"buffer_load_dwordx4 v[140:143], v1, s[4:7], s97 offen offset:3072 sc1\n"
"s_add_u32 s97, s57, 0\n"
"buffer_load_dwordx4 v[148:151], v0, s[4:7], s97 offen sc1\n"
"s_waitcnt vmcnt(0)\n"
"v_max_u32_e32 v255, v160, v161\n"
"v_max3_u32 v255, v255, v162, v163\n"
"v_max3_u32 v255, v255, v124, v125\n"
"v_max3_u32 v255, v255, v126, v127\n"
"v_max3_u32 v255, v255, v120, v121\n"
"v_max3_u32 v255, v255, v122, v123\n"
"v_max3_u32 v255, v255, v132, v133\n"
"v_max3_u32 v255, v255, v134, v135\n"
"v_max3_u32 v255, v255, v156, v157\n"
"v_max3_u32 v255, v255, v158, v159\n"
"v_max3_u32 v255, v255, v164, v165\n"
"v_max3_u32 v255, v255, v166, v167\n"
"v_max3_u32 v255, v255, v172, v173\n"
"v_max3_u32 v255, v255, v174, v175\n"
"v_max3_u32 v255, v255, v152, v153\n"
"v_max3_u32 v255, v255, v154, v155\n"
"v_cmp_eq_u32_e32 vcc, -1, v255\n"
"s_cbranch_vccz .Lmk_p33k\n"
"s_sub_u32 s1, s1, 1\n"
"s_cbranch_scc0 .Lmk_p33l\n"
".Lmk_p33k:\n"
"v_mfma_f32_16x16x32_bf16 v[128:131], a[128:131], v[160:163], v[108:111]\n"
"v_mfma_f32_16x16x32_bf16 v[128:131], a[132:135], v[124:127], v[128:131]\n"
"v_mfma_f32_16x16x32_bf16 v[128:131], a[136:139], v[120:123], v[128:131]\n"
"v_mfma_f32_16x16x32_bf16 v[128:131], a[140:143], v[132:135], v[128:131]\n"
"v_mfma_f32_16x16x32_bf16 v[128:131], a[144:147], v[156:159], v[128:131]\n"
"v_mfma_f32_16x16x32_bf16 v[128:131], a[148:151], v[164:167], v[128:131]\n"
"v_mfma_f32_16x16x32_bf16 v[128:131], a[152:155], v[172:175], v[128:131]\n"
"v_mfma_f32_16x16x32_bf16 v[128:131], a[156:159], v[152:155], v[128:131]\n"
"v_mfma_f32_16x16x32_bf16 v[144:147], a[188:191], v[152:155], v[168:171]\n"
"v_mfma_f32_16x16x32_bf16 v[144:147], a[184:187], v[172:175], v[144:147]\n"
"v_mfma_f32_16x16x32_bf16 v[144:147], a[180:183], v[164:167], v[144:147]\n"
"v_mfma_f32_16x16x32_bf16 v[144:147], a[176:179], v[156:159], v[144:147]\n"
"v_mfma_f32_16x16x32_bf16 v[144:147], a[172:175], v[132:135], v[144:147]\n"
"v_mfma_f32_16x16x32_bf16 v[144:147], a[168:171], v[120:123], v[144:147]\n"
"s_nop 7\n"
"s_branch .Lmk_p33r\n"
".Lmk_p35s:\n"
";\n"
"s_waitcnt vmcnt(16)\n"
"s_mov_b32 s1, 0x10000\n"
".Lmk_p35l:\n"
"s_add_u32 s95, s56, 0\n"
"buffer_load_dwordx4 v[100:103], v0, s[4:7], s95 offen sc1\n"
"buffer_load_dwordx4 v[180:183], v0, s[4:7], s95 offen offset:1024 sc1\n"
"buffer_load_dwordx4 v[104:107], v0, s[4:7], s95 offen offset:2048 sc1\n"
"buffer_load_dwordx4 v[88:91], v0, s[4:7], s95 offen offset:3072 sc1\n"
"buffer_load_dwordx4 v[92:95], v1, s[4:7], s95 offen sc1\n"
"buffer_load_dwordx4 v[116:119], v1, s[4:7], s95 offen offset:1024 sc1\n"
"buffer_load_dwordx4 v[136:139], v1, s[4:7], s95 offen offset:2048 sc1\n"
"buffer_load_dwordx4 v[140:143], v1, s[4:7], s95 offen offset:3072 sc1\n"
"s_add_u32 s95, s57, 0\n"
"buffer_load_dwordx4 v[148:151], v0, s[4:7], s95 offen sc1\n"
"buffer_load_dwordx4 v[108:111], v0, s[4:7], s95 offen offset:1024 sc1\n"
"buffer_load_dwordx4 v[112:115], v0, s[4:7], s95 offen offset:2048 sc1\n"
"buffer_load_dwordx4 v[196:199], v0, s[4:7], s95 offen offset:3072 sc1\n"
"buffer_load_dwordx4 v[200:203], v1, s[4:7], s95 offen sc1\n"
"buffer_load_dwordx4 v[128:131], v1, s[4:7], s95 offen offset:1024 sc1\n"
"buffer_load_dwordx4 v[120:123], v1, s[4:7], s95 offen offset:2048 sc1\n"
"buffer_load_dwordx4 v[132:135], v1, s[4:7], s95 offen offset:3072 sc1\n"
"s_add_u32 s95, s58, 0\n"
"buffer_load_dwordx4 v[152:155], v0, s[4:7], s95 offen sc1\n"
"buffer_load_dwordx4 v[160:163], v0, s[4:7], s95 offen offset:1024 sc1\n"
"buffer_load_dwordx4 v[124:127], v0, s[4:7], s95 offen offset:2048 sc1\n"
"s_waitcnt vmcnt(0)\n"
"v_max_u32_e32 v255, v100, v101\n"
"v_max3_u32 v255, v255, v102, v103\n"
"v_max3_u32 v255, v255, v180, v181\n"
"v_max3_u32 v255, v255, v182, v183\n"
"v_max3_u32 v255, v255, v104, v105\n"
"v_max3_u32 v255, v255, v106, v107\n"
"v_max3_u32 v255, v255, v88, v89\n"
"v_max3_u32 v255, v255, v90, v91\n"
"v_max3_u32 v255, v255, v92, v93\n"
"v_max3_u32 v255, v255, v94, v95\n"
"v_max3_u32 v255, v255, v116, v117\n"
"v_max3_u32 v255, v255, v118, v119\n"
"v_max3_u32 v255, v255, v136, v137\n"
"v_max3_u32 v255, v255, v138, v139\n"
"v_max3_u32 v255, v255, v140, v141\n"
"v_max3_u32 v255, v255, v142, v143\n"
"v_cmp_eq_u32_e32 vcc, -1, v255\n"
"s_cbranch_vccz .Lmk_p35k\n"
"s_sub_u32 s1, s1, 1\n"
"s_cbranch_scc0 .Lmk_p35l\n"
".Lmk_p35k:\n"
"v_mfma_f32_16x16x32_bf16 v[144:147], a[128:131], v[100:103], 0\n"
"v_mfma_f32_16x16x32_bf16 v[144:147], a[132:135], v[180:183], v[144:147]\n"
"v_mfma_f32_16x16x32_bf16 v[144:147], a[136:139], v[104:107], v[144:147]\n"
"v_mfma_f32_16x16x32_bf16 v[144:147], a[140:143], v[88:91], v[144:147]\n"
"v_mfma_f32_16x16x32_bf16 v[144:147], a[144:147], v[92:95], v[144:147]\n"
"v_mfma_f32_16x16x32_bf16 v[144:147], a[148:151], v[116:119], v[144:147]\n"
"v_mfma_f32_16x16x32_bf16 v[144:147], a[152:155], v[136:139], v[144:147]\n"
"v_mfma_f32_16x16x32_bf16 v[144:147], a[156:159], v[140:143], v[144:147]\n"
"v_mfma_f32_16x16x32_bf16 v[96:99], a[188:191], v[140:143], 0\n"
"v_mfma_f32_16x16x32_bf16 v[96:99], a[184:187], v[136:139], v[96:99]\n"
"v_mfma_f32_16x16x32_bf16 v[96:99], a[180:183], v[116:119], v[96:99]\n"
"v_mfma_f32_16x16x32_bf16 v[96:99], a[176:179], v[92:95], v[96:99]\n"
"v_mfma_f32_16x16x32_bf16 v[96:99], a[172:175], v[88:91], v[96:99]\n"
"v_mfma_f32_16x16x32_bf16 v[96:99], a[168:171], v[104:107], v[96:99]\n"
"s_nop 7\n"
"s_branch .Lmk_p35r\n"
".Lmk_p36s:\n"
";\n"
"s_waitcnt vmcnt(16)\n"
"s_mov_b32 s1, 0x10000\n"
".Lmk_p36l:\n"
"s_add_u32 s97, s57, 0\n"
"buffer_load_dwordx4 v[148:151], v0, s[4:7], s97 offen sc1\n"
"buffer_load_dwordx4 v[108:111], v0, s[4:7], s97 offen offset:1024 sc1\n"
"buffer_load_dwordx4 v[112:115], v0, s[4:7], s97 offen offset:2048 sc1\n"
"buffer_load_dwordx4 v[196:199], v0, s[4:7], s97 offen offset:3072 sc1\n"
"buffer_load_dwordx4 v[200:203], v1, s[4:7], s97 offen sc1\n"
"buffer_load_dwordx4 v[128:131], v1, s[4:7], s97 offen offset:1024 sc1\n"
"buffer_load_dwordx4 v[120:123], v1, s[4:7], s97 offen offset:2048 sc1\n"
"buffer_load_dwordx4 v[132:135], v1, s[4:7], s97 offen offset:3072 sc1\n"
"s_add_u32 s97, s58, 0\n"
"buffer_load_dwordx4 v[152:155], v0, s[4:7], s97 offen sc1\n"
"buffer_load_dwordx4 v[160:163], v0, s[4:7], s97 offen offset:1024 sc1\n"
"buffer_load_dwordx4 v[124:127], v0, s[4:7], s97 offen offset:2048 sc1\n"
"buffer_load_dwordx4 v[144:147], v0, s[4:7], s97 offen offset:3072 sc1\n"
"buffer_load_dwordx4 v[156:159], v1, s[4:7], s97 offen sc1\n"
"buffer_load_dwordx4 v[164:167], v1, s[4:7], s97 offen offset:1024 sc1\n"
"buffer_load_dwordx4 v[116:119], v1, s[4:7], s97 offen offset:2048 sc1\n"
"buffer_load_dwordx4 v[136:139], v1, s[4:7], s97 offen offset:3072 sc1\n"
"s_add_u32 s97, s59, 0\n"
"buffer_load_dwordx4 v[172:175], v0, s[4:7], s97 offen sc1\n"
"buffer_load_dwordx4 v[176:179], v0, s[4:7], s97 offen offset:1024 sc1\n"
"buffer_load_dwordx4 v[168:171], v0, s[4:7], s97 offen offset:2048 sc1\n"
"s_waitcnt vmcnt(0)\n"
"v_max_u32_e32 v255, v148, v149\n"
"v_max3_u32 v255, v255, v150, v151\n"
"v_max3_u32 v255, v255, v108, v109\n"
"v_max3_u32 v255, v255, v110, v111\n"
"v_max3_u32 v255, v255, v112, v113\n"
"v_max3_u32 v255, v255, v114, v115\n"
"v_max3_u32 v255, v255, v196, v197\n"
"v_max3_u32 v255, v255, v198, v199\n"
"v_max3_u32 v255, v255, v200, v201\n"
"v_max3_u32 v255, v255, v202, v203\n"
"v_max3_u32 v255, v255, v128, v129\n"
"v_max3_u32 v255, v255, v130, v131\n"
"v_max3_u32 v255, v255, v120, v121\n"
"v_max3_u32 v255, v255, v122, v123\n"
"v_max3_u32 v255, v255, v132, v133\n"
"v_max3_u32 v255, v255, v134, v135\n"
"v_cmp_eq_u32_e32 vcc, -1, v255\n"
"s_cbranch_vccz .Lmk_p36k\n"
"s_sub_u32 s1, s1, 1\n"
"s_cbranch_scc0 .Lmk_p36l\n"
".Lmk_p36k:\n"
"v_mfma_f32_16x16x32_bf16 v[88:91], a[128:131], v[148:151], 0\n"
"v_mfma_f32_16x16x32_bf16 v[88:91], a[132:135], v[108:111], v[88:91]\n"
"v_mfma_f32_16x16x32_bf16 v[88:91], a[136:139], v[112:115], v[88:91]\n"
"v_mfma_f32_16x16x32_bf16 v[88:91], a[140:143], v[196:199], v[88:91]\n"
"v_mfma_f32_16x16x32_bf16 v[88:91], a[144:147], v[200:203], v[88:91]\n"
"v_mfma_f32_16x16x32_bf16 v[88:91], a[148:151], v[128:131], v[88:91]\n"
"v_mfma_f32_16x16x32_bf16 v[88:91], a[152:155], v[120:123], v[88:91]\n"
"v_mfma_f32_16x16x32_bf16 v[88:91], a[156:159], v[132:135], v[88:91]\n"
"v_mfma_f32_16x16x32_bf16 v[92:95], a[188:191], v[132:135], 0\n"
"v_mfma_f32_16x16x32_bf16 v[92:95], a[184:187], v[120:123], v[92:95]\n"
"v_mfma_f32_16x16x32_bf16 v[92:95], a[180:183], v[128:131], v[92:95]\n"
"v_mfma_f32_16x16x32_bf16 v[92:95], a[176:179], v[200:203], v[92:95]\n"
"v_mfma_f32_16x16x32_bf16 v[92:95], a[172:175], v[196:199], v[92:95]\n"
"v_mfma_f32_16x16x32_bf16 v[92:95], a[168:171], v[112:115], v[92:95]\n"
"s_nop 7\n"
"s_branch .Lmk_p36r\n"
".Lmk_p37s:\n"
";\n"
"s_waitcnt vmcnt(16)\n"
"s_mov_b32 s1, 0x10000\n"
".Lmk_p37l:\n"
"s_add_u32 s97, s58, 0\n"
"buffer_load_dwordx4 v[152:155], v0, s[4:7], s97 offen sc1\n"
"buffer_load_dwordx4 v[160:163], v0, s[4:7], s97 offen offset:1024 sc1\n"
"buffer_load_dwordx4 v[124:127], v0, s[4:7], s97 offen offset:2048 sc1\n"
"buffer_load_dwordx4 v[144:147], v0, s[4:7], s97 offen offset:3072 sc1\n"
"buffer_load_dwordx4 v[156:159], v1, s[4:7], s97 offen sc1\n"
"buffer_load_dwordx4 v[164:167], v1, s[4:7], s97 offen offset:1024 sc1\n"
"buffer_load_dwordx4 v[116:119], v1, s[4:7], s97 offen offset:2048 sc1\n"
"buffer_load_dwordx4 v[136:139], v1, s[4:7], s97 offen offset:3072 sc1\n"
"s_add_u32 s97, s59, 0\n"
"buffer_load_dwordx4 v[172:175], v0, s[4:7], s97 offen sc1\n"
"buffer_load_dwordx4 v[176:179], v0, s[4:7], s97 offen offset:1024 sc1\n"
"buffer_load_dwordx4 v[168:171], v0, s[4:7], s97 offen offset:2048 sc1\n"
"buffer_load_dwordx4 v[180:183], v0, s[4:7], s97 offen offset:3072 sc1\n"
"buffer_load_dwordx4 v[184:187], v1, s[4:7], s97 offen sc1\n"
"buffer_load_dwordx4 v[188:191], v1, s[4:7], s97 offen offset:1024 sc1\n"
"buffer_load_dwordx4 v[192:195], v1, s[4:7], s97 offen offset:2048 sc1\n"
"buffer_load_dwordx4 v[196:199], v1, s[4:7], s97 offen offset:3072 sc1\n"
"s_add_u32 s97, s52, 0x440000\n"
"buffer_load_dwordx4 v[88:91], v0, s[4:7], s97 offen sc1\n"
"buffer_load_dwordx4 v[92:95], v0, s[4:7], s97 offen offset:1024 sc1\n"
"s_waitcnt vmcnt(0)\n"
"v_max_u32_e32 v239, v152, v153\n"
"v_max3_u32 v239, v239, v154, v155\n"
"v_max3_u32 v239, v239, v160, v161\n"
"v_max3_u32 v239, v239, v162, v163\n"
"v_max3_u32 v239, v239, v124, v125\n"
"v_max3_u32 v239, v239, v126, v127\n"
"v_max3_u32 v239, v239, v144, v145\n"
"v_max3_u32 v239, v239, v146, v147\n"
"v_max3_u32 v239, v239, v156, v157\n"
"v_max3_u32 v239, v239, v158, v159\n"
"v_max3_u32 v239, v239, v164, v165\n"
"v_max3_u32 v239, v239, v166, v167\n"
"v_max3_u32 v239, v239, v116, v117\n"
"v_max3_u32 v239, v239, v118, v119\n"
"v_max3_u32 v239, v239, v136, v137\n"
"v_max3_u32 v239, v239, v138, v139\n"
"v_cmp_eq_u32_e32 vcc, -1, v239\n"
"s_cbranch_vccz .Lmk_p37k\n"
"s_sub_u32 s1, s1, 1\n"
"s_cbranch_scc0 .Lmk_p37l\n"
".Lmk_p37k:\n"
"v_mfma_f32_16x16x32_bf16 v[96:99], a[128:131], v[152:155], 0\n"
"v_mfma_f32_16x16x32_bf16 v[96:99], a[132:135], v[160:163], v[96:99]\n"
"v_mfma_f32_16x16x32_bf16 v[96:99], a[136:139], v[124:127], v[96:99]\n"
"v_mfma_f32_16x16x32_bf16 v[96:99], a[140:143], v[144:147], v[96:99]\n"
"v_mfma_f32_16x16x32_bf16 v[96:99], a[144:147], v[156:159], v[96:99]\n"
"v_mfma_f32_16x16x32_bf16 v[96:99], a[148:151], v[164:167], v[96:99]\n"
"v_mfma_f32_16x16x32_bf16 v[96:99], a[152:155], v[116:119], v[96:99]\n"
"v_mfma_f32_16x16x32_bf16 v[96:99], a[156:159], v[136:139], v[96:99]\n"
"v_mfma_f32_16x16x32_bf16 v[104:107], a[188:191], v[136:139], 0\n"
"v_mfma_f32_16x16x32_bf16 v[104:107], a[184:187], v[116:119], v[104:107]\n"
"v_mfma_f32_16x16x32_bf16 v[104:107], a[180:183], v[164:167], v[104:107]\n"
"v_mfma_f32_16x16x32_bf16 v[104:107], a[176:179], v[156:159], v[104:107]\n"
"v_mfma_f32_16x16x32_bf16 v[104:107], a[172:175], v[144:147], v[104:107]\n"
"v_mfma_f32_16x16x32_bf16 v[104:107], a[168:171], v[124:127], v[104:107]\n"
"s_nop 7\n"
"s_branch .Lmk_p37r\n"
".Lmk_p38s:\n"
";\n"
"s_waitcnt vmcnt(16)\n"
"s_mov_b32 s1, 0x10000\n"
".Lmk_p38l:\n"
"s_add_u32 s97, s59, 0\n"
"buffer_load_dwordx4 v[172:175], v0, s[4:7], s97 offen sc1\n"
"buffer_load_dwordx4 v[176:179], v0, s[4:7], s97 offen offset:1024 sc1\n"
"buffer_load_dwordx4 v[168:171], v0, s[4:7], s97 offen offset:2048 sc1\n"
"buffer_load_dwordx4 v[180:183], v0, s[4:7], s97 offen offset:3072 sc1\n"
"buffer_load_dwordx4 v[184:187], v1, s[4:7], s97 offen sc1\n"
"buffer_load_dwordx4 v[188:191], v1, s[4:7], s97 offen offset:1024 sc1\n"
"buffer_load_dwordx4 v[192:195], v1, s[4:7], s97 offen offset:2048 sc1\n"
"buffer_load_dwordx4 v[196:199], v1, s[4:7], s97 offen offset:3072 sc1\n"
"s_add_u32 s97, s52, 0x440000\n"
"buffer_load_dwordx4 v[88:91], v0, s[4:7], s97 offen sc1\n"
"buffer_load_dwordx4 v[92:95], v0, s[4:7], s97 offen offset:1024 sc1\n"
"buffer_load_dwordx4 v[96:99], v0, s[4:7], s97 offen offset:2048 sc1\n"
"buffer_load_dwordx4 v[100:103], v0, s[4:7], s97 offen offset:3072 sc1\n"
"buffer_load_dwordx4 v[104:107], v1, s[4:7], s97 offen sc1\n"
"buffer_load_dwordx4 v[112:115], v1, s[4:7], s97 offen offset:1024 sc1\n"
"buffer_load_dwordx4 v[116:119], v1, s[4:7], s97 offen offset:2048 sc1\n"
"buffer_load_dwordx4 v[124:127], v1, s[4:7], s97 offen offset:3072 sc1\n"
"s_add_u32 s97, s53, 0x440000\n"
"buffer_load_dwordx4 v[128:131], v0, s[4:7], s97 offen sc1\n"
"buffer_load_dwordx4 v[132:135], v0, s[4:7], s97 offen offset:1024 sc1\n"
"buffer_load_dwordx4 v[136:139], v0, s[4:7], s97 offen offset:2048 sc1\n"
"s_waitcnt vmcnt(0)\n"
"v_max_u32_e32 v255, v172, v173\n"
"v_max3_u32 v255, v255, v174, v175\n"
"v_max3_u32 v255, v255, v176, v177\n"
"v_max3_u32 v255, v255, v178, v179\n"
"v_max3_u32 v255, v255, v168, v169\n"
"v_max3_u32 v255, v255, v170, v171\n"
"v_max3_u32 v255, v255, v180, v181\n"
"v_max3_u32 v255, v255, v182, v183\n"
"v_max3_u32 v255, v255, v184, v185\n"
"v_max3_u32 v255, v255, v186, v187\n"
"v_max3_u32 v255, v255, v188, v189\n"
"v_max3_u32 v255, v255, v190, v191\n"
"v_max3_u32 v255, v255, v192, v193\n"
"v_max3_u32 v255, v255, v194, v195\n"
"v_max3_u32 v255, v255, v196, v197\n"
"v_max3_u32 v255, v255, v198, v199\n"
"v_cmp_eq_u32_e32 vcc, -1, v255\n"
"s_cbranch_vccz .Lmk_p38k\n"
"s_sub_u32 s1, s1, 1\n"
"s_cbranch_scc0 .Lmk_p38l\n"
".Lmk_p38k:\n"
"v_mfma_f32_16x16x32_bf16 v[164:167], a[128:131], v[172:175], v[140:143]\n"
"v_mfma_f32_16x16x32_bf16 v[164:167], a[132:135], v[176:179], v[164:167]\n"
"v_mfma_f32_16x16x32_bf16 v[164:167], a[136:139], v[168:171], v[164:167]\n"
"v_mfma_f32_16x16x32_bf16 v[164:167], a[140:143], v[180:183], v[164:167]\n"
"v_mfma_f32_16x16x32_bf16 v[164:167], a[144:147], v[184:187], v[164:167]\n"
"v_mfma_f32_16x16x32_bf16 v[164:167], a[148:151], v[188:191], v[164:167]\n"
"v_mfma_f32_16x16x32_bf16 v[164:167], a[152:155], v[192:195], v[164:167]\n"
"v_mfma_f32_16x16x32_bf16 v[164:167], a[156:159], v[196:199], v[164:167]\n"
"v_mfma_f32_16x16x32_bf16 v[204:207], a[188:191], v[196:199], v[148:151]\n"
"v_mfma_f32_16x16x32_bf16 v[204:207], a[184:187], v[192:195], v[204:207]\n"
"v_mfma_f32_16x16x32_bf16 v[204:207], a[180:183], v[188:191], v[204:207]\n"
"v_mfma_f32_16x16x32_bf16 v[204:207], a[176:179], v[184:187], v[204:207]\n"
"v_mfma_f32_16x16x32_bf16 v[204:207], a[172:175], v[180:183], v[204:207]\n"
"v_mfma_f32_16x16x32_bf16 v[204:207], a[168:171], v[168:171], v[204:207]\n"
"s_nop 7\n"
"s_branch .Lmk_p38r\n"
".Lmk_p43s:\n"
";\n"
"s_waitcnt vmcnt(16)\n"
"s_mov_b32 s1, 0x10000\n"
".Lmk_p43l:\n"
"s_add_u32 s96, s52, 0x440000\n"
"buffer_load_dwordx4 v[88:91], v0, s[4:7], s96 offen sc1\n"
"buffer_load_dwordx4 v[92:95], v0, s[4:7], s96 offen offset:1024 sc1\n"
"buffer_load_dwordx4 v[96:99], v0, s[4:7], s96 offen offset:2048 sc1\n"
"buffer_load_dwordx4 v[100:103], v0, s[4:7], s96 offen offset:3072 sc1\n"
"buffer_load_dwordx4 v[104:107], v1, s[4:7], s96 offen sc1\n"
"buffer_load_dwordx4 v[112:115], v1, s[4:7], s96 offen offset:1024 sc1\n"
"buffer_load_dwordx4 v[116:119], v1, s[4:7], s96 offen offset:2048 sc1\n"
"buffer_load_dwordx4 v[124:127], v1, s[4:7], s96 offen offset:3072 sc1\n"
"s_add_u32 s96, s53, 0x440000\n"
"buffer_load_dwordx4 v[128:131], v0, s[4:7], s96 offen sc1\n"
"buffer_load_dwordx4 v[132:135], v0, s[4:7], s96 offen offset:1024 sc1\n"
"buffer_load_dwordx4 v[136:139], v0, s[4:7], s96 offen offset:2048 sc1\n"
"buffer_load_dwordx4 v[140:143], v0, s[4:7], s96 offen offset:3072 sc1\n"
"buffer_load_dwordx4 v[144:147], v1, s[4:7], s96 offen sc1\n"
"buffer_load_dwordx4 v[156:159], v1, s[4:7], s96 offen offset:1024 sc1\n"
"buffer_load_dwordx4 v[160:163], v1, s[4:7], s96 offen offset:2048 sc1\n"
"buffer_load_dwordx4 v[164:167], v1, s[4:7], s96 offen offset:3072 sc1\n"
"s_add_u32 s96, s54, 0x440000\n"
"buffer_load_dwordx4 v[176:179], v0, s[4:7], s96 offen sc1\n"
"buffer_load_dwordx4 v[108:111], v0, s[4:7], s96 offen offset:1024 sc1\n"
"s_waitcnt vmcnt(0)\n"
"v_max_u32_e32 v252, v88, v89\n"
"v_max3_u32 v252, v252, v90, v91\n"
"v_max3_u32 v252, v252, v92, v93\n"
"v_max3_u32 v252, v252, v94, v95\n"
"v_max3_u32 v252, v252, v96, v97\n"
"v_max3_u32 v252, v252, v98, v99\n"
"v_max3_u32 v252, v252, v100, v101\n"
"v_max3_u32 v252, v252, v102, v103\n"
"v_max3_u32 v252, v252, v104, v105\n"
"v_max3_u32 v252, v252, v106, v107\n"
"v_max3_u32 v252, v252, v112, v113\n"
"v_max3_u32 v252, v252, v114, v115\n"
"v_max3_u32 v252, v252, v116, v117\n"
"v_max3_u32 v252, v252, v118, v119\n"
"v_max3_u32 v252, v252, v124, v125\n"
"v_max3_u32 v252, v252, v126, v127\n"
"v_cmp_eq_u32_e32 vcc, -1, v252\n"
"s_cbranch_vccz .Lmk_p43k\n"
"s_sub_u32 s1, s1, 1\n"
"s_cbranch_scc0 .Lmk_p43l\n"
".Lmk_p43k:\n"
"v_mfma_f32_16x16x32_bf16 v[172:175], a[0:3], v[88:91], 0\n"
"v_mfma_f32_16x16x32_bf16 v[172:175], a[4:7], v[92:95], v[172:175]\n"
"v_mfma_f32_16x16x32_bf16 v[172:175], a[8:11], v[96:99], v[172:175]\n"
"v_mfma_f32_16x16x32_bf16 v[172:175], a[12:15], v[100:103], v[172:175]\n"
"v_mfma_f32_16x16x32_bf16 v[172:175], a[16:19], v[104:107], v[172:175]\n"
"v_mfma_f32_16x16x32_bf16 v[172:175], a[20:23], v[112:115], v[172:175]\n"
"v_mfma_f32_16x16x32_bf16 v[172:175], a[24:27], v[116:119], v[172:175]\n"
"v_mfma_f32_16x16x32_bf16 v[172:175], a[28:31], v[124:127], v[172:175]\n"
"v_mfma_f32_16x16x32_bf16 v[180:183], a[60:63], v[124:127], 0\n"
"v_mfma_f32_16x16x32_bf16 v[180:183], a[56:59], v[116:119], v[180:183]\n"
"v_mfma_f32_16x16x32_bf16 v[180:183], a[52:55], v[112:115], v[180:183]\n"
"v_mfma_f32_16x16x32_bf16 v[180:183], a[48:51], v[104:107], v[180:183]\n"
"v_mfma_f32_16x16x32_bf16 v[180:183], a[44:47], v[100:103], v[180:183]\n"
"v_mfma_f32_16x16x32_bf16 v[180:183], a[40:43], v[96:99], v[180:183]\n"
"s_nop 7\n"
"s_branch .Lmk_p43r\n"
".Lmk_p44s:\n"
";\n"
"s_waitcnt vmcnt(16)\n"
"s_mov_b32 s1, 0x10000\n"
".Lmk_p44l:\n"
"s_add_u32 s96, s53, 0x440000\n"
"buffer_load_dwordx4 v[128:131], v0, s[4:7], s96 offen sc1\n"
"buffer_load_dwordx4 v[132:135], v0, s[4:7], s96 offen offset:1024 sc1\n"
"buffer_load_dwordx4 v[136:139], v0, s[4:7], s96 offen offset:2048 sc1\n"
"buffer_load_dwordx4 v[140:143], v0, s[4:7], s96 offen offset:3072 sc1\n"
"buffer_load_dwordx4 v[144:147], v1, s[4:7], s96 offen sc1\n"
"buffer_load_dwordx4 v[156:159], v1, s[4:7], s96 offen offset:1024 sc1\n"
"buffer_load_dwordx4 v[160:163], v1, s[4:7], s96 offen offset:2048 sc1\n"
"buffer_load_dwordx4 v[164:167], v1, s[4:7], s96 offen offset:3072 sc1\n"
"s_add_u32 s96, s54, 0x440000\n"
"buffer_load_dwordx4 v[176:179], v0, s[4:7], s96 offen sc1\n"
"buffer_load_dwordx4 v[108:111], v0, s[4:7], s96 offen offset:1024 sc1\n"
"buffer_load_dwordx4 v[148:151], v0, s[4:7], s96 offen offset:2048 sc1\n"
"buffer_load_dwordx4 v[184:187], v0, s[4:7], s96 offen offset:3072 sc1\n"
"buffer_load_dwordx4 v[152:155], v1, s[4:7], s96 offen sc1\n"
"buffer_load_dwordx4 v[192:195], v1, s[4:7], s96 offen offset:1024 sc1\n"
"buffer_load_dwordx4 v[196:199], v1, s[4:7], s96 offen offset:2048 sc1\n"
"buffer_load_dwordx4 v[200:203], v1, s[4:7], s96 offen offset:3072 sc1\n"
"s_add_u32 s96, s55, 0x440000\n"
"buffer_load_dwordx4 v[92:95], v0, s[4:7], s96 offen sc1\n"
"buffer_load_dwordx4 v[96:99], v0, s[4:7], s96 offen offset:1024 sc1\n"
"s_waitcnt vmcnt(0)\n"
"v_max_u32_e32 v247, v128, v129\n"
"v_max3_u32 v247, v247, v130, v131\n"
"v_max3_u32 v247, v247, v132, v133\n"
"v_max3_u32 v247, v247, v134, v135\n"
"v_max3_u32 v247, v247, v136, v137\n"
"v_max3_u32 v247, v247, v138, v139\n"
"v_max3_u32 v247, v247, v140, v141\n"
"v_max3_u32 v247, v247, v142, v143\n"
"v_max3_u32 v247, v247, v144, v145\n"
"v_max3_u32 v247, v247, v146, v147\n"
"v_max3_u32 v247, v247, v156, v157\n"
"v_max3_u32 v247, v247, v158, v159\n"
"v_max3_u32 v247, v247, v160, v161\n"
"v_max3_u32 v247, v247, v162, v163\n"
"v_max3_u32 v247, v247, v164, v165\n"
"v_max3_u32 v247, v247, v166, v167\n"
"v_cmp_eq_u32_e32 vcc, -1, v247\n"
"s_cbranch_vccz .Lmk_p44k\n"
"s_sub_u32 s1, s1, 1\n"
"s_cbranch_scc0 .Lmk_p44l\n"
".Lmk_p44k:\n"
"v_mfma_f32_16x16x32_bf16 v[88:91], a[0:3], v[128:131], 0\n"
"v_mfma_f32_16x16x32_bf16 v[88:91], a[4:7], v[132:135], v[88:91]\n"
"v_mfma_f32_16x16x32_bf16 v[88:91], a[8:11], v[136:139], v[88:91]\n"
"v_mfma_f32_16x16x32_bf16 v[88:91], a[12:15], v[140:143], v[88:91]\n"
"v_mfma_f32_16x16x32_bf16 v[88:91], a[16:19], v[144:147], v[88:91]\n"
"v_mfma_f32_16x16x32_bf16 v[88:91], a[20:23], v[156:159], v[88:91]\n"
"v_mfma_f32_16x16x32_bf16 v[88:91], a[24:27], v[160:163], v[88:91]\n"
"v_mfma_f32_16x16x32_bf16 v[88:91], a[28:31], v[164:167], v[88:91]\n"
"v_mfma_f32_16x16x32_bf16 v[100:103], a[60:63], v[164:167], 0\n"
"v_mfma_f32_16x16x32_bf16 v[100:103], a[56:59], v[160:163], v[100:103]\n"
"v_mfma_f32_16x16x32_bf16 v[100:103], a[52:55], v[156:159], v[100:103]\n"
"v_mfma_f32_16x16x32_bf16 v[100:103], a[48:51], v[144:147], v[100:103]\n"
"v_mfma_f32_16x16x32_bf16 v[100:103], a[44:47], v[140:143], v[100:103]\n"
"v_mfma_f32_16x16x32_bf16 v[100:103], a[40:43], v[136:139], v[100:103]\n"
"s_nop 7\n"
"s_branch .Lmk_p44r\n"
".Lmk_m47s:\n"
";\n"
"s_waitcnt vmcnt(16)\n"
"s_mov_b32 s1, 0x10000\n"
".Lmk_m47l:\n"
"s_add_u32 s98, s62, 0x44000\n"
"buffer_load_dword v255, v10, s[4:7], s98 offen sc1\n"
"buffer_load_dword v254, v10, s[4:7], s98 offen offset:256 sc1\n"
"buffer_load_dword v253, v10, s[4:7], s98 offen offset:512 sc1\n"
"buffer_load_dword v252, v10, s[4:7], s98 offen offset:768 sc1\n"
"buffer_load_dword v251, v10, s[4:7], s98 offen offset:1024 sc1\n"
"buffer_load_dword v250, v10, s[4:7], s98 offen offset:1280 sc1\n"
"buffer_load_dword v249, v10, s[4:7], s98 offen offset:1536 sc1\n"
"buffer_load_dword v248, v10, s[4:7], s98 offen offset:1792 sc1\n"
"s_waitcnt vmcnt(0)\n"
"v_max_u32_e32 v247, v255, v254\n"
"v_max3_u32 v247, v247, v253, v252\n"
"v_max3_u32 v247, v247, v251, v250\n"
"v_max3_u32 v247, v247, v249, v248\n"
"v_cmp_eq_u32_e32 vcc, -1, v247\n"
"s_cbranch_vccz .Lmk_m47k\n"
"s_sub_u32 s1, s1, 1\n"
"s_cbranch_scc0 .Lmk_m47l\n"
".Lmk_m47k:\n"
"v_add_f32_e32 v255, v255, v251\n"
"v_add_f32_e32 v254, v254, v250\n"
"v_add_f32_e32 v253, v253, v249\n"
"v_add_f32_e32 v252, v252, v248\n"
"v_add_f32_e32 v255, v255, v253\n"
"v_add_f32_e32 v254, v254, v252\n"
"v_add_f32_e32 v255, v255, v254\n"
"s_nop 1\n"
"s_branch .Lmk_m47r\n"
".Lmk_p45s:\n"
";\n"
"s_waitcnt vmcnt(16)\n"
"s_mov_b32 s1, 0x10000\n"
".Lmk_p45l:\n"
"s_add_u32 s96, s54, 0x440000\n"
"buffer_load_dwordx4 v[176:179], v0, s[4:7], s96 offen sc1\n"
"buffer_load_dwordx4 v[108:111], v0, s[4:7], s96 offen offset:1024 sc1\n"
"buffer_load_dwordx4 v[148:151], v0, s[4:7], s96 offen offset:2048 sc1\n"
"buffer_load_dwordx4 v[184:187], v0, s[4:7], s96 offen offset:3072 sc1\n"
"buffer_load_dwordx4 v[152:155], v1, s[4:7], s96 offen sc1\n"
"buffer_load_dwordx4 v[192:195], v1, s[4:7], s96 offen offset:1024 sc1\n"
"buffer_load_dwordx4 v[196:199], v1, s[4:7], s96 offen offset:2048 sc1\n"
"buffer_load_dwordx4 v[200:203], v1, s[4:7], s96 offen offset:3072 sc1\n"
"s_add_u32 s96, s55, 0x440000\n"
"buffer_load_dwordx4 v[92:95], v0, s[4:7], s96 offen sc1\n"
"buffer_load_dwordx4 v[96:99], v0, s[4:7], s96 offen offset:1024 sc1\n"
"buffer_load_dwordx4 v[104:107], v0, s[4:7], s96 offen offset:2048 sc1\n"
"buffer_load_dwordx4 v[112:115], v0, s[4:7], s96 offen offset:3072 sc1\n"
"buffer_load_dwordx4 v[100:103], v1, s[4:7], s96 offen sc1\n"
"buffer_load_dwordx4 v[124:127], v1, s[4:7], s96 offen offset:1024 sc1\n"
"buffer_load_dwordx4 v[168:171], v1, s[4:7], s96 offen offset:2048 sc1\n"
"buffer_load_dwordx4 v[172:175], v1, s[4:7], s96 offen offset:3072 sc1\n"
"s_add_u32 s96, s52, 0x448000\n"
"buffer_load_dwordx4 v[128:131], v0, s[4:7], s96 offen sc1\n"
"s_waitcnt vmcnt(0)\n"
"v_max_u32_e32 v255, v176, v177\n"
"v_max3_u32 v255, v255, v178, v179\n"
"v_max3_u32 v255, v255, v108, v109\n"
"v_max3_u32 v255, v255, v110, v111\n"
"v_max3_u32 v255, v255, v148, v149\n"
"v_max3_u32 v255, v255, v150, v151\n"
"v_max3_u32 v255, v255, v184, v185\n"
"v_max3_u32 v255, v255, v186, v187\n"
"v_max3_u32 v255, v255, v152, v153\n"
"v_max3_u32 v255, v255, v154, v155\n"
"v_max3_u32 v255, v255, v192, v193\n"
"v_max3_u32 v255, v255, v194, v195\n"
"v_max3_u32 v255, v255, v196, v197\n"
"v_max3_u32 v255, v255, v198, v199\n"
"v_max3_u32 v255, v255, v200, v201\n"
"v_max3_u32 v255, v255, v202, v203\n"
"v_cmp_eq_u32_e32 vcc, -1, v255\n"
"s_cbranch_vccz .Lmk_p45k\n"
"s_sub_u32 s1, s1, 1\n"
"s_cbranch_scc0 .Lmk_p45l\n"
".Lmk_p45k:\n"
"v_mfma_f32_16x16x32_bf16 v[88:91], a[0:3], v[176:179], 0\n"
"v_mfma_f32_16x16x32_bf16 v[88:91], a[4:7], v[108:111], v[88:91]\n"
"v_mfma_f32_16x16x32_bf16 v[88:91], a[8:11], v[148:151], v[88:91]\n"
"v_mfma_f32_16x16x32_bf16 v[88:91], a[12:15], v[184:187], v[88:91]\n"
"v_mfma_f32_16x16x32_bf16 v[88:91], a[16:19], v[152:155], v[88:91]\n"
"v_mfma_f32_16x16x32_bf16 v[88:91], a[20:23], v[192:195], v[88:91]\n"
"v_mfma_f32_16x16x32_bf16 v[88:91], a[24:27], v[196:199], v[88:91]\n"
"v_mfma_f32_16x16x32_bf16 v[88:91], a[28:31], v[200:203], v[88:91]\n"
"v_mfma_f32_16x16x32_bf16 v[116:119], a[60:63], v[200:203], 0\n"
"v_mfma_f32_16x16x32_bf16 v[116:119], a[56:59], v[196:199], v[116:119]\n"
"v_mfma_f32_16x16x32_bf16 v[116:119], a[52:55], v[192:195], v[116:119]\n"
"v_mfma_f32_16x16x32_bf16 v[116:119], a[48:51], v[152:155], v[116:119]\n"
"v_mfma_f32_16x16x32_bf16 v[116:119], a[44:47], v[184:187], v[116:119]\n"
"v_mfma_f32_16x16x32_bf16 v[116:119], a[40:43], v[148:151], v[116:119]\n"
"s_nop 7\n"
"s_branch .Lmk_p45r\n"
".Lmk_p46s:\n"
";\n"
"s_waitcnt vmcnt(16)\n"
"s_mov_b32 s1, 0x10000\n"
".Lmk_p46l:\n"
"s_add_u32 s96, s55, 0x440000\n"
"buffer_load_dwordx4 v[92:95], v0, s[4:7], s96 offen sc1\n"
"buffer_load_dwordx4 v[96:99], v0, s[4:7], s96 offen offset:1024 sc1\n"
"buffer_load_dwordx4 v[104:107], v0, s[4:7], s96 offen offset:2048 sc1\n"
"buffer_load_dwordx4 v[112:115], v0, s[4:7], s96 offen offset:3072 sc1\n"
"buffer_load_dwordx4 v[100:103], v1, s[4:7], s96 offen sc1\n"
"buffer_load_dwordx4 v[124:127], v1, s[4:7], s96 offen offset:1024 sc1\n"
"buffer_load_dwordx4 v[168:171], v1, s[4:7], s96 offen offset:2048 sc1\n"
"buffer_load_dwordx4 v[172:175], v1, s[4:7], s96 offen offset:3072 sc1\n"
"s_add_u32 s96, s52, 0x448000\n"
"buffer_load_dwordx4 v[128:131], v0, s[4:7], s96 offen sc1\n"
"buffer_load_dwordx4 v[88:91], v0, s[4:7], s96 offen offset:1024 sc1\n"
"buffer_load_dwordx4 v[164:167], v0, s[4:7], s96 offen offset:2048 sc1\n"
"buffer_load_dwordx4 v[140:143], v0, s[4:7], s96 offen offset:3072 sc1\n"
"buffer_load_dwordx4 v[132:135], v1, s[4:7], s96 offen sc1\n"
"buffer_load_dwordx4 v[224:227], v1, s[4:7], s96 offen offset:1024 sc1\n"
"buffer_load_dwordx4 v[180:183], v1, s[4:7], s96 offen offset:2048 sc1\n"
"buffer_load_dwordx4 v[148:151], v1, s[4:7], s96 offen offset:3072 sc1\n"
"s_add_u32 s96, s53, 0x448000\n"
"buffer_load_dwordx4 v[116:119], v0, s[4:7], s96 offen sc1\n"
"buffer_load_dwordx4 v[160:163], v0, s[4:7], s96 offen offset:1024 sc1\n"
"s_waitcnt vmcnt(0)\n"
"v_max_u32_e32 v252, v92, v93\n"
"v_max3_u32 v252, v252, v94, v95\n"
"v_max3_u32 v252, v252, v96, v97\n"
"v_max3_u32 v252, v252, v98, v99\n"
"v_max3_u32 v252, v252, v104, v105\n"
"v_max3_u32 v252, v252, v106, v107\n"
"v_max3_u32 v252, v252, v112, v113\n"
"v_max3_u32 v252, v252, v114, v115\n"
"v_max3_u32 v252, v252, v100, v101\n"
"v_max3_u32 v252, v252, v102, v103\n"
"v_max3_u32 v252, v252, v124, v125\n"
"v_max3_u32 v252, v252, v126, v127\n"
"v_max3_u32 v252, v252, v168, v169\n"
"v_max3_u32 v252, v252, v170, v171\n"
"v_max3_u32 v252, v252, v172, v173\n"
"v_max3_u32 v252, v252, v174, v175\n"
"v_cmp_eq_u32_e32 vcc, -1, v252\n"
"s_cbranch_vccz .Lmk_p46k\n"
"s_sub_u32 s1, s1, 1\n"
"s_cbranch_scc0 .Lmk_p46l\n"
".Lmk_p46k:\n"
"v_mfma_f32_16x16x32_bf16 v[196:199], a[0:3], v[92:95], v[136:139]\n"
"v_mfma_f32_16x16x32_bf16 v[196:199], a[4:7], v[96:99], v[196:199]\n"
"v_mfma_f32_16x16x32_bf16 v[196:199], a[8:11], v[104:107], v[196:199]\n"
"v_mfma_f32_16x16x32_bf16 v[196:199], a[12:15], v[112:115], v[196:199]\n"
"v_mfma_f32_16x16x32_bf16 v[196:199], a[16:19], v[100:103], v[196:199]\n"
"v_mfma_f32_16x16x32_bf16 v[196:199], a[20:23], v[124:127], v[196:199]\n"
"v_mfma_f32_16x16x32_bf16 v[196:199], a[24:27], v[168:171], v[196:199]\n"
"v_mfma_f32_16x16x32_bf16 v[196:199], a[28:31], v[172:175], v[196:199]\n"
"v_mfma_f32_16x16x32_bf16 v[204:207], a[60:63], v[172:175], v[156:159]\n"
"v_mfma_f32_16x16x32_bf16 v[204:207], a[56:59], v[168:171], v[204:207]\n"
"v_mfma_f32_16x16x32_bf16 v[204:207], a[52:55], v[124:127], v[204:207]\n"
"v_mfma_f32_16x16x32_bf16 v[204:207], a[48:51], v[100:103], v[204:207]\n"
"v_mfma_f32_16x16x32_bf16 v[204:207], a[44:47], v[112:115], v[204:207]\n"
"v_mfma_f32_16x16x32_bf16 v[204:207], a[40:43], v[104:107], v[204:207]\n"
"s_nop 7\n"
"s_branch .Lmk_p46r\n"
".Lmk_p48s:\n"
";\n"
"s_waitcnt vmcnt(16)\n"
"s_mov_b32 s1, 0x10000\n"
".Lmk_p48l:\n"
"s_add_u32 s97, s52, 0x448000\n"
"buffer_load_dwordx4 v[128:131], v0, s[4:7], s97 offen sc1\n"
"buffer_load_dwordx4 v[88:91], v0, s[4:7], s97 offen offset:1024 sc1\n"
"buffer_load_dwordx4 v[164:167], v0, s[4:7], s97 offen offset:2048 sc1\n"
"buffer_load_dwordx4 v[140:143], v0, s[4:7], s97 offen offset:3072 sc1\n"
"buffer_load_dwordx4 v[132:135], v1, s[4:7], s97 offen sc1\n"
"buffer_load_dwordx4 v[224:227], v1, s[4:7], s97 offen offset:1024 sc1\n"
"buffer_load_dwordx4 v[180:183], v1, s[4:7], s97 offen offset:2048 sc1\n"
"buffer_load_dwordx4 v[148:151], v1, s[4:7], s97 offen offset:3072 sc1\n"
"s_add_u32 s97, s53, 0x448000\n"
"buffer_load_dwordx4 v[116:119], v0, s[4:7], s97 offen sc1\n"
"buffer_load_dwordx4 v[160:163], v0, s[4:7], s97 offen offset:1024 sc1\n"
"buffer_load_dwordx4 v[136:139], v0, s[4:7], s97 offen offset:2048 sc1\n"
"buffer_load_dwordx4 v[156:159], v0, s[4:7], s97 offen offset:3072 sc1\n"
"buffer_load_dwordx4 v[192:195], v1, s[4:7], s97 offen sc1\n"
"buffer_load_dwordx4 v[184:187], v1, s[4:7], s97 offen offset:1024 sc1\n"
"buffer_load_dwordx4 v[176:179], v1, s[4:7], s97 offen offset:2048 sc1\n"
"buffer_load_dwordx4 v[200:203], v1, s[4:7], s97 offen offset:3072 sc1\n"
"s_add_u32 s97, s54, 0x448000\n"
"buffer_load_dwordx4 v[96:99], v0, s[4:7], s97 offen sc1\n"
"buffer_load_dwordx4 v[100:103], v0, s[4:7], s97 offen offset:1024 sc1\n"
"s_waitcnt vmcnt(0)\n"
"v_max_u32_e32 v252, v128, v129\n"
"v_max3_u32 v252, v252, v130, v131\n"
"v_max3_u32 v252, v252, v88, v89\n"
"v_max3_u32 v252, v252, v90, v91\n"
"v_max3_u32 v252, v252, v164, v165\n"
"v_max3_u32 v252, v252, v166, v167\n"
"v_max3_u32 v252, v252, v140, v141\n"
"v_max3_u32 v252, v252, v142, v143\n"
"v_max3_u32 v252, v252, v132, v133\n"
"v_max3_u32 v252, v252, v134, v135\n"
"v_max3_u32 v252, v252, v224, v225\n"
"v_max3_u32 v252, v252, v226, v227\n"
"v_max3_u32 v252, v252, v180, v181\n"
"v_max3_u32 v252, v252, v182, v183\n"
"v_max3_u32 v252, v252, v148, v149\n"
"v_max3_u32 v252, v252, v150, v151\n"
"v_cmp_eq_u32_e32 vcc, -1, v252\n"
"s_cbranch_vccz .Lmk_p48k\n"
"s_sub_u32 s1, s1, 1\n"
"s_cbranch_scc0 .Lmk_p48l\n"
".Lmk_p48k:\n"
"v_mfma_f32_16x16x32_bf16 v[92:95], a[0:3], v[128:131], 0\n"
"v_mfma_f32_16x16x32_bf16 v[92:95], a[4:7], v[88:91], v[92:95]\n"
"v_mfma_f32_16x16x32_bf16 v[92:95], a[8:11], v[164:167], v[92:95]\n"
"v_mfma_f32_16x16x32_bf16 v[92:95], a[12:15], v[140:143], v[92:95]\n"
"v_mfma_f32_16x16x32_bf16 v[92:95], a[16:19], v[132:135], v[92:95]\n"
"v_mfma_f32_16x16x32_bf16 v[92:95], a[20:23], v[224:227], v[92:95]\n"
"v_mfma_f32_16x16x32_bf16 v[92:95], a[24:27], v[180:183], v[92:95]\n"
"v_mfma_f32_16x16x32_bf16 v[92:95], a[28:31], v[148:151], v[92:95]\n"
"v_mfma_f32_16x16x32_bf16 v[104:107], a[60:63], v[148:151], 0\n"
"v_mfma_f32_16x16x32_bf16 v[104:107], a[56:59], v[180:183], v[104:107]\n"
"v_mfma_f32_16x16x32_bf16 v[104:107], a[52:55], v[224:227], v[104:107]\n"
"v_mfma_f32_16x16x32_bf16 v[104:107], a[48:51], v[132:135], v[104:107]\n"
"v_mfma_f32_16x16x32_bf16 v[104:107], a[44:47], v[140:143], v[104:107]\n"
"v_mfma_f32_16x16x32_bf16 v[104:107], a[40:43], v[164:167], v[104:107]\n"
"s_nop 7\n"
"s_branch .Lmk_p48r\n"
".Lmk_p49s:\n"
";\n"
"s_waitcnt vmcnt(16)\n"
"s_mov_b32 s1, 0x10000\n"
".Lmk_p49l:\n"
"s_add_u32 s95, s53, 0x448000\n"
"buffer_load_dwordx4 v[116:119], v0, s[4:7], s95 offen sc1\n"
"buffer_load_dwordx4 v[160:163], v0, s[4:7], s95 offen offset:1024 sc1\n"
"buffer_load_dwordx4 v[136:139], v0, s[4:7], s95 offen offset:2048 sc1\n"
"buffer_load_dwordx4 v[156:159], v0, s[4:7], s95 offen offset:3072 sc1\n"
"buffer_load_dwordx4 v[192:195], v1, s[4:7], s95 offen sc1\n"
"buffer_load_dwordx4 v[184:187], v1, s[4:7], s95 offen offset:1024 sc1\n"
"buffer_load_dwordx4 v[176:179], v1, s[4:7], s95 offen offset:2048 sc1\n"
"buffer_load_dwordx4 v[200:203], v1, s[4:7], s95 offen offset:3072 sc1\n"
"s_add_u32 s95, s54, 0x448000\n"
"buffer_load_dwordx4 v[96:99], v0, s[4:7], s95 offen sc1\n"
"buffer_load_dwordx4 v[100:103], v0, s[4:7], s95 offen offset:1024 sc1\n"
"buffer_load_dwordx4 v[112:115], v0, s[4:7], s95 offen offset:2048 sc1\n"
"buffer_load_dwordx4 v[120:123], v0, s[4:7], s95 offen offset:3072 sc1\n"
"buffer_load_dwordx4 v[104:107], v1, s[4:7], s95 offen sc1\n"
"buffer_load_dwordx4 v[152:155], v1, s[4:7], s95 offen offset:1024 sc1\n"
"buffer_load_dwordx4 v[108:111], v1, s[4:7], s95 offen offset:2048 sc1\n"
"buffer_load_dwordx4 v[144:147], v1, s[4:7], s95 offen offset:3072 sc1\n"
"s_add_u32 s95, s55, 0x448000\n"
"buffer_load_dwordx4 v[92:95], v0, s[4:7], s95 offen sc1\n"
"buffer_load_dwordx4 v[124:127], v0, s[4:7], s95 offen offset:1024 sc1\n"
"s_waitcnt vmcnt(0)\n"
"v_max_u32_e32 v247, v116, v117\n"
"v_max3_u32 v247, v247, v118, v119\n"
"v_max3_u32 v247, v247, v160, v161\n"
"v_max3_u32 v247, v247, v162, v163\n"
"v_max3_u32 v247, v247, v136, v137\n"
"v_max3_u32 v247, v247, v138, v139\n"
"v_max3_u32 v247, v247, v156, v157\n"
"v_max3_u32 v247, v247, v158, v159\n"
"v_max3_u32 v247, v247, v192, v193\n"
"v_max3_u32 v247, v247, v194, v195\n"
"v_max3_u32 v247, v247, v184, v185\n"
"v_max3_u32 v247, v247, v186, v187\n"
"v_max3_u32 v247, v247, v176, v177\n"
"v_max3_u32 v247, v247, v178, v179\n"
"v_max3_u32 v247, v247, v200, v201\n"
"v_max3_u32 v247, v247, v202, v203\n"
"v_cmp_eq_u32_e32 vcc, -1, v247\n"
"s_cbranch_vccz .Lmk_p49k\n"
"s_sub_u32 s1, s1, 1\n"
"s_cbranch_scc0 .Lmk_p49l\n"
".Lmk_p49k:\n"
"v_mfma_f32_16x16x32_bf16 v[88:91], a[0:3], v[116:119], 0\n"
"v_mfma_f32_16x16x32_bf16 v[88:91], a[4:7], v[160:163], v[88:91]\n"
"v_mfma_f32_16x16x32_bf16 v[88:91], a[8:11], v[136:139], v[88:91]\n"
"v_mfma_f32_16x16x32_bf16 v[88:91], a[12:15], v[156:159], v[88:91]\n"
"v_mfma_f32_16x16x32_bf16 v[88:91], a[16:19], v[192:195], v[88:91]\n"
"v_mfma_f32_16x16x32_bf16 v[88:91], a[20:23], v[184:187], v[88:91]\n"
"v_mfma_f32_16x16x32_bf16 v[88:91], a[24:27], v[176:179], v[88:91]\n"
"v_mfma_f32_16x16x32_bf16 v[88:91], a[28:31], v[200:203], v[88:91]\n"
"v_mfma_f32_16x16x32_bf16 v[128:131], a[60:63], v[200:203], 0\n"
"v_mfma_f32_16x16x32_bf16 v[128:131], a[56:59], v[176:179], v[128:131]\n"
"v_mfma_f32_16x16x32_bf16 v[128:131], a[52:55], v[184:187], v[128:131]\n"
"v_mfma_f32_16x16x32_bf16 v[128:131], a[48:51], v[192:195], v[128:131]\n"
"v_mfma_f32_16x16x32_bf16 v[128:131], a[44:47], v[156:159], v[128:131]\n"
"v_mfma_f32_16x16x32_bf16 v[128:131], a[40:43], v[136:139], v[128:131]\n"
"s_nop 7\n"
"s_branch .Lmk_p49r\n"
".Lmk_m52s:\n"
";\n"
"s_waitcnt vmcnt(16)\n"
"s_mov_b32 s1, 0x10000\n"
".Lmk_m52l:\n"
"s_add_u32 s98, s62, 0x44800\n"
"buffer_load_dword v255, v10, s[4:7], s98 offen sc1\n"
"buffer_load_dword v253, v10, s[4:7], s98 offen offset:256 sc1\n"
"buffer_load_dword v252, v10, s[4:7], s98 offen offset:512 sc1\n"
"buffer_load_dword v251, v10, s[4:7], s98 offen offset:768 sc1\n"
"buffer_load_dword v250, v10, s[4:7], s98 offen offset:1024 sc1\n"
"buffer_load_dword v254, v10, s[4:7], s98 offen offset:1280 sc1\n"
"buffer_load_dword v249, v10, s[4:7], s98 offen offset:1536 sc1\n"
"buffer_load_dword v248, v10, s[4:7], s98 offen offset:1792 sc1\n"
"s_waitcnt vmcnt(0)\n"
"v_max_u32_e32 v245, v255, v253\n"
"v_max3_u32 v245, v245, v252, v251\n"
"v_max3_u32 v245, v245, v250, v254\n"
"v_max3_u32 v245, v245, v249, v248\n"
"v_cmp_eq_u32_e32 vcc, -1, v245\n"
"s_cbranch_vccz .Lmk_m52k\n"
"s_sub_u32 s1, s1, 1\n"
"s_cbranch_scc0 .Lmk_m52l\n"
".Lmk_m52k:\n"
"v_add_f32_e32 v255, v255, v250\n"
"v_add_f32_e32 v253, v253, v254\n"
"v_add_f32_e32 v252, v252, v249\n"
"v_add_f32_e32 v251, v251, v248\n"
"v_add_f32_e32 v255, v255, v252\n"
"v_add_f32_e32 v253, v253, v251\n"
"v_add_f32_e32 v255, v255, v253\n"
"s_nop 1\n"
"s_branch .Lmk_m52r\n"
".Lmk_f39s:\n"
";\n"
"s_mov_b32 s1, 0x10000\n"
".Lmk_f39l:\n"
"buffer_load_dwordx2 v[246:247], v12, s[4:7], s96 offen sc1\n"
"s_waitcnt vmcnt(0)\n"
"v_cmp_ne_u64_e32 vcc, v[246:247], v[18:19]\n"
"s_cbranch_vccz .Lmk_f39r\n"
"s_sub_u32 s1, s1, 1\n"
"s_cbranch_scc0 .Lmk_f39l\n"
"s_branch .Lmk_f39r\n"
".Lmk_p50s:\n"
";\n"
"s_waitcnt vmcnt(16)\n"
"s_mov_b32 s1, 0x10000\n"
".Lmk_p50l:\n"
"s_add_u32 s96, s54, 0x448000\n"
"buffer_load_dwordx4 v[96:99], v0, s[4:7], s96 offen sc1\n"
"buffer_load_dwordx4 v[100:103], v0, s[4:7], s96 offen offset:1024 sc1\n"
"buffer_load_dwordx4 v[112:115], v0, s[4:7], s96 offen offset:2048 sc1\n"
"buffer_load_dwordx4 v[120:123], v0, s[4:7], s96 offen offset:3072 sc1\n"
"buffer_load_dwordx4 v[104:107], v1, s[4:7], s96 offen sc1\n"
"buffer_load_dwordx4 v[152:155], v1, s[4:7], s96 offen offset:1024 sc1\n"
"buffer_load_dwordx4 v[108:111], v1, s[4:7], s96 offen offset:2048 sc1\n"
"buffer_load_dwordx4 v[144:147], v1, s[4:7], s96 offen offset:3072 sc1\n"
"s_add_u32 s96, s55, 0x448000\n"
"buffer_load_dwordx4 v[92:95], v0, s[4:7], s96 offen sc1\n"
"buffer_load_dwordx4 v[124:127], v0, s[4:7], s96 offen offset:1024 sc1\n"
"buffer_load_dwordx4 v[132:135], v0, s[4:7], s96 offen offset:2048 sc1\n"
"buffer_load_dwordx4 v[140:143], v0, s[4:7], s96 offen offset:3072 sc1\n"
"buffer_load_dwordx4 v[128:131], v1, s[4:7], s96 offen sc1\n"
"buffer_load_dwordx4 v[168:171], v1, s[4:7], s96 offen offset:1024 sc1\n"
"buffer_load_dwordx4 v[172:175], v1, s[4:7], s96 offen offset:2048 sc1\n"
"buffer_load_dwordx4 v[180:183], v1, s[4:7], s96 offen offset:3072 sc1\n"
"s_waitcnt vmcnt(0)\n"
"v_max_u32_e32 v252, v96, v97\n"
"v_max3_u32 v252, v252, v98, v99\n"
"v_max3_u32 v252, v252, v100, v101\n"
"v_max3_u32 v252, v252, v102, v103\n"
"v_max3_u32 v252, v252, v112, v113\n"
"v_max3_u32 v252, v252, v114, v115\n"
"v_max3_u32 v252, v252, v120, v121\n"
"v_max3_u32 v252, v252, v122, v123\n"
"v_max3_u32 v252, v252, v104, v105\n"
"v_max3_u32 v252, v252, v106, v107\n"
"v_max3_u32 v252, v252, v152, v153\n"
"v_max3_u32 v252, v252, v154, v155\n"
"v_max3_u32 v252, v252, v108, v109\n"
"v_max3_u32 v252, v252, v110, v111\n"
"v_max3_u32 v252, v252, v144, v145\n"
"v_max3_u32 v252, v252, v146, v147\n"
"v_cmp_eq_u32_e32 vcc, -1, v252\n"
"s_cbranch_vccz .Lmk_p50k\n"
"s_sub_u32 s1, s1, 1\n"
"s_cbranch_scc0 .Lmk_p50l\n"
".Lmk_p50k:\n"
"v_mfma_f32_16x16x32_bf16 v[88:91], a[0:3], v[96:99], 0\n"
"v_mfma_f32_16x16x32_bf16 v[88:91], a[4:7], v[100:103], v[88:91]\n"
"v_mfma_f32_16x16x32_bf16 v[88:91], a[8:11], v[112:115], v[88:91]\n"
"v_mfma_f32_16x16x32_bf16 v[88:91], a[12:15], v[120:123], v[88:91]\n"
"v_mfma_f32_16x16x32_bf16 v[88:91], a[16:19], v[104:107], v[88:91]\n"
"v_mfma_f32_16x16x32_bf16 v[88:91], a[20:23], v[152:155], v[88:91]\n"
"v_mfma_f32_16x16x32_bf16 v[88:91], a[24:27], v[108:111], v[88:91]\n"
"v_mfma_f32_16x16x32_bf16 v[88:91], a[28:31], v[144:147], v[88:91]\n"
"v_mfma_f32_16x16x32_bf16 v[116:119], a[60:63], v[144:147], 0\n"
"v_mfma_f32_16x16x32_bf16 v[116:119], a[56:59], v[108:111], v[116:119]\n"
"v_mfma_f32_16x16x32_bf16 v[116:119], a[52:55], v[152:155], v[116:119]\n"
"v_mfma_f32_16x16x32_bf16 v[116:119], a[48:51], v[104:107], v[116:119]\n"
"v_mfma_f32_16x16x32_bf16 v[116:119], a[44:47], v[120:123], v[116:119]\n"
"v_mfma_f32_16x16x32_bf16 v[116:119], a[40:43], v[112:115], v[116:119]\n"
"s_nop 7\n"
"s_branch .Lmk_p50r\n"
".Lmk_p51s:\n"
";\n"
"s_waitcnt vmcnt(16)\n"
"s_mov_b32 s1, 0x10000\n"
".Lmk_p51l:\n"
"s_add_u32 s96, s55, 0x448000\n"
"buffer_load_dwordx4 v[92:95], v0, s[4:7], s96 offen sc1\n"
"buffer_load_dwordx4 v[124:127], v0, s[4:7], s96 offen offset:1024 sc1\n"
"buffer_load_dwordx4 v[132:135], v0, s[4:7], s96 offen offset:2048 sc1\n"
"buffer_load_dwordx4 v[140:143], v0, s[4:7], s96 offen offset:3072 sc1\n"
"buffer_load_dwordx4 v[128:131], v1, s[4:7], s96 offen sc1\n"
"buffer_load_dwordx4 v[168:171], v1, s[4:7], s96 offen offset:1024 sc1\n"
"buffer_load_dwordx4 v[172:175], v1, s[4:7], s96 offen offset:2048 sc1\n"
"buffer_load_dwordx4 v[180:183], v1, s[4:7], s96 offen offset:3072 sc1\n"
"s_waitcnt vmcnt(0)\n"
"v_max_u32_e32 v252, v92, v93\n"
"v_max3_u32 v252, v252, v94, v95\n"
"v_max3_u32 v252, v252, v124, v125\n"
"v_max3_u32 v252, v252, v126, v127\n"
"v_max3_u32 v252, v252, v132, v133\n"
"v_max3_u32 v252, v252, v134, v135\n"
"v_max3_u32 v252, v252, v140, v141\n"
"v_max3_u32 v252, v252, v142, v143\n"
"v_max3_u32 v252, v252, v128, v129\n"
"v_max3_u32 v252, v252, v130, v131\n"
"v_max3_u32 v252, v252, v168, v169\n"
"v_max3_u32 v252, v252, v170, v171\n"
"v_max3_u32 v252, v252, v172, v173\n"
"v_max3_u32 v252, v252, v174, v175\n"
"v_max3_u32 v252, v252, v180, v181\n"
"v_max3_u32 v252, v252, v182, v183\n"
"v_cmp_eq_u32_e32 vcc, -1, v252\n"
"s_cbranch_vccz .Lmk_p51k\n"
"s_sub_u32 s1, s1, 1\n"
"s_cbranch_scc0 .Lmk_p51l\n"
".Lmk_p51k:\n"
"v_mfma_f32_16x16x32_bf16 v[112:115], a[0:3], v[92:95], v[156:159]\n"
"v_mfma_f32_16x16x32_bf16 v[112:115], a[4:7], v[124:127], v[112:115]\n"
"v_mfma_f32_16x16x32_bf16 v[112:115], a[8:11], v[132:135], v[112:115]\n"
"v_mfma_f32_16x16x32_bf16 v[112:115], a[12:15], v[140:143], v[112:115]\n"
"v_mfma_f32_16x16x32_bf16 v[112:115], a[16:19], v[128:131], v[112:115]\n"
"v_mfma_f32_16x16x32_bf16 v[112:115], a[20:23], v[168:171], v[112:115]\n"
"v_mfma_f32_16x16x32_bf16 v[112:115], a[24:27], v[172:175], v[112:115]\n"
"v_mfma_f32_16x16x32_bf16 v[112:115], a[28:31], v[180:183], v[112:115]\n"
"v_mfma_f32_16x16x32_bf16 v[188:191], a[60:63], v[180:183], v[184:187]\n"
"v_mfma_f32_16x16x32_bf16 v[188:191], a[56:59], v[172:175], v[188:191]\n"
"v_mfma_f32_16x16x32_bf16 v[188:191], a[52:55], v[168:171], v[188:191]\n"
"v_mfma_f32_16x16x32_bf16 v[188:191], a[48:51], v[128:131], v[188:191]\n"
"v_mfma_f32_16x16x32_bf16 v[188:191], a[44:47], v[140:143], v[188:191]\n"
"v_mfma_f32_16x16x32_bf16 v[188:191], a[40:43], v[132:135], v[188:191]\n"
"s_nop 7\n"
"s_branch .Lmk_p51r\n"
".Lmk_f40s:\n"
";\n"
"s_mov_b32 s1, 0x10000\n"
".Lmk_f40l:\n"
"buffer_load_dwordx2 v[246:247], off, s[4:7], s96 sc1\n"
"s_waitcnt vmcnt(0)\n"
"v_cmp_ne_u64_e32 vcc, v[246:247], v[18:19]\n"
"s_cbranch_vccz .Lmk_f40r\n"
"s_sub_u32 s1, s1, 1\n"
"s_cbranch_scc0 .Lmk_f40l\n"
"s_branch .Lmk_f40r\n"
".Lmk_f41s:\n"
";\n"
"s_mov_b32 s1, 0x10000\n"
".Lmk_f41l:\n"
"buffer_load_dwordx2 v[250:251], v12, s[4:7], s98 offen sc1\n"
"s_waitcnt vmcnt(0)\n"
"v_cmp_ne_u64_e32 vcc, v[250:251], v[18:19]\n"
"s_cbranch_vccz .Lmk_f41r\n"
"s_sub_u32 s1, s1, 1\n"
"s_cbranch_scc0 .Lmk_f41l\n"
"s_branch .Lmk_f41r\n"
".Lmk_p55s:\n"
";\n"
"s_waitcnt vmcnt(16)\n"
"s_mov_b32 s1, 0x10000\n"
".Lmk_p55l:\n"
"s_add_u32 s96, s56, 0x440000\n"
"buffer_load_dwordx4 v[120:123], v0, s[4:7], s96 offen sc1\n"
"buffer_load_dwordx4 v[136:139], v0, s[4:7], s96 offen offset:1024 sc1\n"
"buffer_load_dwordx4 v[144:147], v0, s[4:7], s96 offen offset:2048 sc1\n"
"buffer_load_dwordx4 v[192:195], v0, s[4:7], s96 offen offset:3072 sc1\n"
"buffer_load_dwordx4 v[196:199], v1, s[4:7], s96 offen sc1\n"
"buffer_load_dwordx4 v[188:191], v1, s[4:7], s96 offen offset:1024 sc1\n"
"buffer_load_dwordx4 v[108:111], v1, s[4:7], s96 offen offset:2048 sc1\n"
"buffer_load_dwordx4 v[92:95], v1, s[4:7], s96 offen offset:3072 sc1\n"
"s_add_u32 s96, s57, 0x440000\n"
"buffer_load_dwordx4 v[148:151], v0, s[4:7], s96 offen sc1\n"
"buffer_load_dwordx4 v[180:183], v0, s[4:7], s96 offen offset:1024 sc1\n"
"buffer_load_dwordx4 v[24:27], v0, s[4:7], s96 offen offset:2048 sc1\n"
"buffer_load_dwordx4 v[32:35], v0, s[4:7], s96 offen offset:3072 sc1\n"
"buffer_load_dwordx4 v[44:47], v1, s[4:7], s96 offen sc1\n"
"buffer_load_dwordx4 v[48:51], v1, s[4:7], s96 offen offset:1024 sc1\n"
"buffer_load_dwordx4 v[28:31], v1, s[4:7], s96 offen offset:2048 sc1\n"
"buffer_load_dwordx4 v[56:59], v1, s[4:7], s96 offen offset:3072 sc1\n"
"s_add_u32 s96, s58, 0x440000\n"
"buffer_load_dwordx4 v[40:43], v0, s[4:7], s96 offen sc1\n"
"buffer_load_dwordx4 v[52:55], v0, s[4:7], s96 offen offset:1024 sc1\n"
"buffer_load_dwordx4 v[64:67], v0, s[4:7], s96 offen offset:2048 sc1\n"
"s_waitcnt vmcnt(0)\n"
"v_max_u32_e32 v255, v120, v121\n"
"v_max3_u32 v255, v255, v122, v123\n"
"v_max3_u32 v255, v255, v136, v137\n"
"v_max3_u32 v255, v255, v138, v139\n"
"v_max3_u32 v255, v255, v144, v145\n"
"v_max3_u32 v255, v255, v146, v147\n"
"v_max3_u32 v255, v255, v192, v193\n"
"v_max3_u32 v255, v255, v194, v195\n"
"v_max3_u32 v255, v255, v196, v197\n"
"v_max3_u32 v255, v255, v198, v199\n"
"v_max3_u32 v255, v255, v188, v189\n"
"v_max3_u32 v255, v255, v190, v191\n"
"v_max3_u32 v255, v255, v108, v109\n"
"v_max3_u32 v255, v255, v110, v111\n"
"v_max3_u32 v255, v255, v92, v93\n"
"v_max3_u32 v255, v255, v94, v95\n"
"v_cmp_eq_u32_e32 vcc, -1, v255\n"
"s_cbranch_vccz .Lmk_p55k\n"
"s_sub_u32 s1, s1, 1\n"
"s_cbranch_scc0 .Lmk_p55l\n"
".Lmk_p55k:\n"
"v_mfma_f32_16x16x32_bf16 v[36:39], a[128:131], v[120:123], 0\n"
"v_mfma_f32_16x16x32_bf16 v[36:39], a[132:135], v[136:139], v[36:39]\n"
"v_mfma_f32_16x16x32_bf16 v[36:39], a[136:139], v[144:147], v[36:39]\n"
"v_mfma_f32_16x16x32_bf16 v[36:39], a[140:143], v[192:195], v[36:39]\n"
"v_mfma_f32_16x16x32_bf16 v[36:39], a[144:147], v[196:199], v[36:39]\n"
"v_mfma_f32_16x16x32_bf16 v[36:39], a[148:151], v[188:191], v[36:39]\n"
"v_mfma_f32_16x16x32_bf16 v[36:39], a[152:155], v[108:111], v[36:39]\n"
"v_mfma_f32_16x16x32_bf16 v[36:39], a[156:159], v[92:95], v[36:39]\n"
"v_mfma_f32_16x16x32_bf16 v[60:63], a[188:191], v[92:95], 0\n"
"v_mfma_f32_16x16x32_bf16 v[60:63], a[184:187], v[108:111], v[60:63]\n"
"v_mfma_f32_16x16x32_bf16 v[60:63], a[180:183], v[188:191], v[60:63]\n"
"v_mfma_f32_16x16x32_bf16 v[60:63], a[176:179], v[196:199], v[60:63]\n"
"v_mfma_f32_16x16x32_bf16 v[60:63], a[172:175], v[192:195], v[60:63]\n"
"v_mfma_f32_16x16x32_bf16 v[60:63], a[168:171], v[144:147], v[60:63]\n"
"s_nop 7\n"
"s_branch .Lmk_p55r\n"
".Lmk_p56s:\n"
";\n"
"s_waitcnt vmcnt(16)\n"
"s_mov_b32 s1, 0x10000\n"
".Lmk_p56l:\n"
"s_add_u32 s96, s57, 0x440000\n"
"buffer_load_dwordx4 v[148:151], v0, s[4:7], s96 offen sc1\n"
"buffer_load_dwordx4 v[180:183], v0, s[4:7], s96 offen offset:1024 sc1\n"
"buffer_load_dwordx4 v[24:27], v0, s[4:7], s96 offen offset:2048 sc1\n"
"buffer_load_dwordx4 v[32:35], v0, s[4:7], s96 offen offset:3072 sc1\n"
"buffer_load_dwordx4 v[44:47], v1, s[4:7], s96 offen sc1\n"
"buffer_load_dwordx4 v[48:51], v1, s[4:7], s96 offen offset:1024 sc1\n"
"buffer_load_dwordx4 v[28:31], v1, s[4:7], s96 offen offset:2048 sc1\n"
"buffer_load_dwordx4 v[56:59], v1, s[4:7], s96 offen offset:3072 sc1\n"
"s_add_u32 s96, s58, 0x440000\n"
"buffer_load_dwordx4 v[40:43], v0, s[4:7], s96 offen sc1\n"
"buffer_load_dwordx4 v[52:55], v0, s[4:7], s96 offen offset:1024 sc1\n"
"buffer_load_dwordx4 v[64:67], v0, s[4:7], s96 offen offset:2048 sc1\n"
"buffer_load_dwordx4 v[36:39], v0, s[4:7], s96 offen offset:3072 sc1\n"
"buffer_load_dwordx4 v[72:75], v1, s[4:7], s96 offen sc1\n"
"buffer_load_dwordx4 v[60:63], v1, s[4:7], s96 offen offset:1024 sc1\n"
"buffer_load_dwordx4 v[80:83], v1, s[4:7], s96 offen offset:2048 sc1\n"
"buffer_load_dwordx4 v[68:71], v1, s[4:7], s96 offen offset:3072 sc1\n"
"s_add_u32 s96, s59, 0x440000\n"
"buffer_load_dwordx4 v[96:99], v0, s[4:7], s96 offen sc1\n"
"buffer_load_dwordx4 v[76:79], v0, s[4:7], s96 offen offset:1024 sc1\n"
"buffer_load_dwordx4 v[112:115], v0, s[4:7], s96 offen offset:2048 sc1\n"
"s_waitcnt vmcnt(0)\n"
"v_max_u32_e32 v254, v148, v149\n"
"v_max3_u32 v254, v254, v150, v151\n"
"v_max3_u32 v254, v254, v180, v181\n"
"v_max3_u32 v254, v254, v182, v183\n"
"v_max3_u32 v254, v254, v24, v25\n"
"v_max3_u32 v254, v254, v26, v27\n"
"v_max3_u32 v254, v254, v32, v33\n"
"v_max3_u32 v254, v254, v34, v35\n"
"v_max3_u32 v254, v254, v44, v45\n"
"v_max3_u32 v254, v254, v46, v47\n"
"v_max3_u32 v254, v254, v48, v49\n"
"v_max3_u32 v254, v254, v50, v51\n"
"v_max3_u32 v254, v254, v28, v29\n"
"v_max3_u32 v254, v254, v30, v31\n"
"v_max3_u32 v254, v254, v56, v57\n"
"v_max3_u32 v254, v254, v58, v59\n"
"v_cmp_eq_u32_e32 vcc, -1, v254\n"
"s_cbranch_vccz .Lmk_p56k\n"
"s_sub_u32 s1, s1, 1\n"
"s_cbranch_scc0 .Lmk_p56l\n"
".Lmk_p56k:\n"
"v_mfma_f32_16x16x32_bf16 v[84:87], a[128:131], v[148:151], 0\n"
"v_mfma_f32_16x16x32_bf16 v[84:87], a[132:135], v[180:183], v[84:87]\n"
"v_mfma_f32_16x16x32_bf16 v[84:87], a[136:139], v[24:27], v[84:87]\n"
"v_mfma_f32_16x16x32_bf16 v[84:87], a[140:143], v[32:35], v[84:87]\n"
"v_mfma_f32_16x16x32_bf16 v[84:87], a[144:147], v[44:47], v[84:87]\n"
"v_mfma_f32_16x16x32_bf16 v[84:87], a[148:151], v[48:51], v[84:87]\n"
"v_mfma_f32_16x16x32_bf16 v[84:87], a[152:155], v[28:31], v[84:87]\n"
"v_mfma_f32_16x16x32_bf16 v[84:87], a[156:159], v[56:59], v[84:87]\n"
"v_mfma_f32_16x16x32_bf16 v[100:103], a[188:191], v[56:59], 0\n"
"v_mfma_f32_16x16x32_bf16 v[100:103], a[184:187], v[28:31], v[100:103]\n"
"v_mfma_f32_16x16x32_bf16 v[100:103], a[180:183], v[48:51], v[100:103]\n"
"v_mfma_f32_16x16x32_bf16 v[100:103], a[176:179], v[44:47], v[100:103]\n"
"v_mfma_f32_16x16x32_bf16 v[100:103], a[172:175], v[32:35], v[100:103]\n"
"v_mfma_f32_16x16x32_bf16 v[100:103], a[168:171], v[24:27], v[100:103]\n"
"s_nop 7\n"
"s_branch .Lmk_p56r\n"
".Lmk_p57s:\n"
";\n"
"s_waitcnt vmcnt(16)\n"
"s_mov_b32 s1, 0x10000\n"
".Lmk_p57l:\n"
"s_add_u32 s96, s58, 0x440000\n"
"buffer_load_dwordx4 v[40:43], v0, s[4:7], s96 offen sc1\n"
"buffer_load_dwordx4 v[52:55], v0, s[4:7], s96 offen offset:1024 sc1\n"
"buffer_load_dwordx4 v[64:67], v0, s[4:7], s96 offen offset:2048 sc1\n"
"buffer_load_dwordx4 v[36:39], v0, s[4:7], s96 offen offset:3072 sc1\n"
"buffer_load_dwordx4 v[72:75], v1, s[4:7], s96 offen sc1\n"
"buffer_load_dwordx4 v[60:63], v1, s[4:7], s96 offen offset:1024 sc1\n"
"buffer_load_dwordx4 v[80:83], v1, s[4:7], s96 offen offset:2048 sc1\n"
"buffer_load_dwordx4 v[68:71], v1, s[4:7], s96 offen offset:3072 sc1\n"
"s_add_u32 s96, s59, 0x440000\n"
"buffer_load_dwordx4 v[96:99], v0, s[4:7], s96 offen sc1\n"
"buffer_load_dwordx4 v[76:79], v0, s[4:7], s96 offen offset:1024 sc1\n"
"buffer_load_dwordx4 v[112:115], v0, s[4:7], s96 offen offset:2048 sc1\n"
"buffer_load_dwordx4 v[84:87], v0, s[4:7], s96 offen offset:3072 sc1\n"
"buffer_load_dwordx4 v[128:131], v1, s[4:7], s96 offen sc1\n"
"buffer_load_dwordx4 v[100:103], v1, s[4:7], s96 offen offset:1024 sc1\n"
"buffer_load_dwordx4 v[156:159], v1, s[4:7], s96 offen offset:2048 sc1\n"
"buffer_load_dwordx4 v[116:119], v1, s[4:7], s96 offen offset:3072 sc1\n"
"s_waitcnt vmcnt(0)\n"
"v_max_u32_e32 v237, v40, v41\n"
"v_max3_u32 v237, v237, v42, v43\n"
"v_max3_u32 v237, v237, v52, v53\n"
"v_max3_u32 v237, v237, v54, v55\n"
"v_max3_u32 v237, v237, v64, v65\n"
"v_max3_u32 v237, v237, v66, v67\n"
"v_max3_u32 v237, v237, v36, v37\n"
"v_max3_u32 v237, v237, v38, v39\n"
"v_max3_u32 v237, v237, v72, v73\n"
"v_max3_u32 v237, v237, v74, v75\n"
"v_max3_u32 v237, v237, v60, v61\n"
"v_max3_u32 v237, v237, v62, v63\n"
"v_max3_u32 v237, v237, v80, v81\n"
"v_max3_u32 v237, v237, v82, v83\n"
"v_max3_u32 v237, v237, v68, v69\n"
"v_max3_u32 v237, v237, v70, v71\n"
"v_cmp_eq_u32_e32 vcc, -1, v237\n"
"s_cbranch_vccz .Lmk_p57k\n"
"s_sub_u32 s1, s1, 1\n"
"s_cbranch_scc0 .Lmk_p57l\n"
".Lmk_p57k:\n"
"v_mfma_f32_16x16x32_bf16 v[132:135], a[128:131], v[40:43], 0\n"
"v_mfma_f32_16x16x32_bf16 v[132:135], a[132:135], v[52:55], v[132:135]\n"
"v_mfma_f32_16x16x32_bf16 v[132:135], a[136:139], v[64:67], v[132:135]\n"
"v_mfma_f32_16x16x32_bf16 v[132:135], a[140:143], v[36:39], v[132:135]\n"
"v_mfma_f32_16x16x32_bf16 v[132:135], a[144:147], v[72:75], v[132:135]\n"
"v_mfma_f32_16x16x32_bf16 v[132:135], a[148:151], v[60:63], v[132:135]\n"
"v_mfma_f32_16x16x32_bf16 v[132:135], a[152:155], v[80:83], v[132:135]\n"
"v_mfma_f32_16x16x32_bf16 v[132:135], a[156:159], v[68:71], v[132:135]\n"
"v_mfma_f32_16x16x32_bf16 v[140:143], a[188:191], v[68:71], 0\n"
"v_mfma_f32_16x16x32_bf16 v[140:143], a[184:187], v[80:83], v[140:143]\n"
"v_mfma_f32_16x16x32_bf16 v[140:143], a[180:183], v[60:63], v[140:143]\n"
"v_mfma_f32_16x16x32_bf16 v[140:143], a[176:179], v[72:75], v[140:143]\n"
"v_mfma_f32_16x16x32_bf16 v[140:143], a[172:175], v[36:39], v[140:143]\n"
"v_mfma_f32_16x16x32_bf16 v[140:143], a[168:171], v[64:67], v[140:143]\n"
"s_nop 7\n"
"s_branch .Lmk_p57r\n"
".Lmk_p58s:\n"
";\n"
"s_waitcnt vmcnt(16)\n"
"s_mov_b32 s1, 0x10000\n"
".Lmk_p58l:\n"
"s_add_u32 s96, s59, 0x440000\n"
"buffer_load_dwordx4 v[96:99], v0, s[4:7], s96 offen sc1\n"
"buffer_load_dwordx4 v[76:79], v0, s[4:7], s96 offen offset:1024 sc1\n"
"buffer_load_dwordx4 v[112:115], v0, s[4:7], s96 offen offset:2048 sc1\n"
"buffer_load_dwordx4 v[84:87], v0, s[4:7], s96 offen offset:3072 sc1\n"
"buffer_load_dwordx4 v[128:131], v1, s[4:7], s96 offen sc1\n"
"buffer_load_dwordx4 v[100:103], v1, s[4:7], s96 offen offset:1024 sc1\n"
"buffer_load_dwordx4 v[156:159], v1, s[4:7], s96 offen offset:2048 sc1\n"
"buffer_load_dwordx4 v[116:119], v1, s[4:7], s96 offen offset:3072 sc1\n"
"s_add_u32 s96, s56, 0x400000\n"
"buffer_load_dwordx4 v[132:135], v0, s[4:7], s96 offen sc1\n"
"buffer_load_dwordx4 v[184:187], v0, s[4:7], s96 offen offset:1024 sc1\n"
"buffer_load_dwordx4 v[140:143], v0, s[4:7], s96 offen offset:2048 sc1\n"
"buffer_load_dwordx4 v[68:71], v0, s[4:7], s96 offen offset:3072 sc1\n"
"buffer_load_dwordx4 v[36:39], v1, s[4:7], s96 offen sc1\n"
"buffer_load_dwordx4 v[40:43], v1, s[4:7], s96 offen offset:1024 sc1\n"
"buffer_load_dwordx4 v[72:75], v1, s[4:7], s96 offen offset:2048 sc1\n"
"buffer_load_dwordx4 v[80:83], v1, s[4:7], s96 offen offset:3072 sc1\n"
"s_add_u32 s96, s57, 0x400000\n"
"buffer_load_dwordx4 v[164:167], v0, s[4:7], s96 offen sc1\n"
"s_waitcnt vmcnt(0)\n"
"v_max_u32_e32 v254, v96, v97\n"
"v_max3_u32 v254, v254, v98, v99\n"
"v_max3_u32 v254, v254, v76, v77\n"
"v_max3_u32 v254, v254, v78, v79\n"
"v_max3_u32 v254, v254, v112, v113\n"
"v_max3_u32 v254, v254, v114, v115\n"
"v_max3_u32 v254, v254, v84, v85\n"
"v_max3_u32 v254, v254, v86, v87\n"
"v_max3_u32 v254, v254, v128, v129\n"
"v_max3_u32 v254, v254, v130, v131\n"
"v_max3_u32 v254, v254, v100, v101\n"
"v_max3_u32 v254, v254, v102, v103\n"
"v_max3_u32 v254, v254, v156, v157\n"
"v_max3_u32 v254, v254, v158, v159\n"
"v_max3_u32 v254, v254, v116, v117\n"
"v_max3_u32 v254, v254, v118, v119\n"
"v_cmp_eq_u32_e32 vcc, -1, v254\n"
"s_cbranch_vccz .Lmk_p58k\n"
"s_sub_u32 s1, s1, 1\n"
"s_cbranch_scc0 .Lmk_p58l\n"
".Lmk_p58k:\n"
"v_mfma_f32_16x16x32_bf16 v[64:67], a[128:131], v[96:99], v[152:155]\n"
"v_mfma_f32_16x16x32_bf16 v[64:67], a[132:135], v[76:79], v[64:67]\n"
"v_mfma_f32_16x16x32_bf16 v[64:67], a[136:139], v[112:115], v[64:67]\n"
"v_mfma_f32_16x16x32_bf16 v[64:67], a[140:143], v[84:87], v[64:67]\n"
"v_mfma_f32_16x16x32_bf16 v[64:67], a[144:147], v[128:131], v[64:67]\n"
"v_mfma_f32_16x16x32_bf16 v[64:67], a[148:151], v[100:103], v[64:67]\n"
"v_mfma_f32_16x16x32_bf16 v[64:67], a[152:155], v[156:159], v[64:67]\n"
"v_mfma_f32_16x16x32_bf16 v[64:67], a[156:159], v[116:119], v[64:67]\n"
"v_mfma_f32_16x16x32_bf16 v[60:63], a[188:191], v[116:119], v[168:171]\n"
"v_mfma_f32_16x16x32_bf16 v[60:63], a[184:187], v[156:159], v[60:63]\n"
"v_mfma_f32_16x16x32_bf16 v[60:63], a[180:183], v[100:103], v[60:63]\n"
"v_mfma_f32_16x16x32_bf16 v[60:63], a[176:179], v[128:131], v[60:63]\n"
"v_mfma_f32_16x16x32_bf16 v[60:63], a[172:175], v[84:87], v[60:63]\n"
"v_mfma_f32_16x16x32_bf16 v[60:63], a[168:171], v[112:115], v[60:63]\n"
"s_nop 7\n"
"s_branch .Lmk_p58r\n"
".Lmk_p59s:\n"
";\n"
"s_waitcnt vmcnt(16)\n"
"s_mov_b32 s1, 0x10000\n"
".Lmk_p59l:\n"
"s_add_u32 s96, s56, 0x400000\n"
"buffer_load_dwordx4 v[132:135], v0, s[4:7], s96 offen sc1\n"
"buffer_load_dwordx4 v[184:187], v0, s[4:7], s96 offen offset:1024 sc1\n"
"buffer_load_dwordx4 v[140:143], v0, s[4:7], s96 offen offset:2048 sc1\n"
"buffer_load_dwordx4 v[68:71], v0, s[4:7], s96 offen offset:3072 sc1\n"
"buffer_load_dwordx4 v[36:39], v1, s[4:7], s96 offen sc1\n"
"buffer_load_dwordx4 v[40:43], v1, s[4:7], s96 offen offset:1024 sc1\n"
"buffer_load_dwordx4 v[72:75], v1, s[4:7], s96 offen offset:2048 sc1\n"
"buffer_load_dwordx4 v[80:83], v1, s[4:7], s96 offen offset:3072 sc1\n"
"s_add_u32 s96, s57, 0x400000\n"
"buffer_load_dwordx4 v[164:167], v0, s[4:7], s96 offen sc1\n"
"buffer_load_dwordx4 v[168:171], v0, s[4:7], s96 offen offset:1024 sc1\n"
"buffer_load_dwordx4 v[208:211], v0, s[4:7], s96 offen offset:2048 sc1\n"
"buffer_load_dwordx4 v[212:215], v0, s[4:7], s96 offen offset:3072 sc1\n"
"buffer_load_dwordx4 v[64:67], v1, s[4:7], s96 offen sc1\n"
"buffer_load_dwordx4 v[100:103], v1, s[4:7], s96 offen offset:1024 sc1\n"
"buffer_load_dwordx4 v[60:63], v1, s[4:7], s96 offen offset:2048 sc1\n"
"buffer_load_dwordx4 v[76:79], v1, s[4:7], s96 offen offset:3072 sc1\n"
"s_add_u32 s96, s58, 0x400000\n"
"buffer_load_dwordx4 v[96:99], v0, s[4:7], s96 offen sc1\n"
"buffer_load_dwordx4 v[112:115], v0, s[4:7], s96 offen offset:1024 sc1\n"
"buffer_load_dwordx4 v[52:55], v0, s[4:7], s96 offen offset:2048 sc1\n"
"buffer_load_dwordx4 v[152:155], v0, s[4:7], s96 offen offset:3072 sc1\n"
"s_waitcnt vmcnt(0)\n"
"v_max_u32_e32 v255, v132, v133\n"
"v_max3_u32 v255, v255, v134, v135\n"
"v_max3_u32 v255, v255, v184, v185\n"
"v_max3_u32 v255, v255, v186, v187\n"
"v_max3_u32 v255, v255, v140, v141\n"
"v_max3_u32 v255, v255, v142, v143\n"
"v_max3_u32 v255, v255, v68, v69\n"
"v_max3_u32 v255, v255, v70, v71\n"
"v_max3_u32 v255, v255, v36, v37\n"
"v_max3_u32 v255, v255, v38, v39\n"
"v_max3_u32 v255, v255, v40, v41\n"
"v_max3_u32 v255, v255, v42, v43\n"
"v_max3_u32 v255, v255, v72, v73\n"
"v_max3_u32 v255, v255, v74, v75\n"
"v_max3_u32 v255, v255, v80, v81\n"
"v_max3_u32 v255, v255, v82, v83\n"
"v_cmp_eq_u32_e32 vcc, -1, v255\n"
"s_cbranch_vccz .Lmk_p59k\n"
"s_sub_u32 s1, s1, 1\n"
"s_cbranch_scc0 .Lmk_p59l\n"
".Lmk_p59k:\n"
"v_mfma_f32_16x16x32_bf16 v[84:87], a[128:131], v[132:135], 0\n"
"v_mfma_f32_16x16x32_bf16 v[84:87], a[132:135], v[184:187], v[84:87]\n"
"v_mfma_f32_16x16x32_bf16 v[84:87], a[136:139], v[140:143], v[84:87]\n"
"v_mfma_f32_16x16x32_bf16 v[84:87], a[140:143], v[68:71], v[84:87]\n"
"v_mfma_f32_16x16x32_bf16 v[84:87], a[144:147], v[36:39], v[84:87]\n"
"v_mfma_f32_16x16x32_bf16 v[84:87], a[148:151], v[40:43], v[84:87]\n"
"v_mfma_f32_16x16x32_bf16 v[84:87], a[152:155], v[72:75], v[84:87]\n"
"v_mfma_f32_16x16x32_bf16 v[84:87], a[156:159], v[80:83], v[84:87]\n"
"v_mfma_f32_16x16x32_bf16 v[128:131], a[188:191], v[80:83], 0\n"
"v_mfma_f32_16x16x32_bf16 v[128:131], a[184:187], v[72:75], v[128:131]\n"
"v_mfma_f32_16x16x32_bf16 v[128:131], a[180:183], v[40:43], v[128:131]\n"
"v_mfma_f32_16x16x32_bf16 v[128:131], a[176:179], v[36:39], v[128:131]\n"
"v_mfma_f32_16x16x32_bf16 v[128:131], a[172:175], v[68:71], v[128:131]\n"
"v_mfma_f32_16x16x32_bf16 v[128:131], a[168:171], v[140:143], v[128:131]\n"
"s_nop 7\n"
"s_branch .Lmk_p59r\n"
".Lmk_p60s:\n"
";\n"
"s_waitcnt vmcnt(16)\n"
"s_mov_b32 s1, 0x10000\n"
".Lmk_p60l:\n"
"s_add_u32 s96, s57, 0x400000\n"
"buffer_load_dwordx4 v[164:167], v0, s[4:7], s96 offen sc1\n"
"buffer_load_dwordx4 v[168:171], v0, s[4:7], s96 offen offset:1024 sc1\n"
"buffer_load_dwordx4 v[208:211], v0, s[4:7], s96 offen offset:2048 sc1\n"
"buffer_load_dwordx4 v[212:215], v0, s[4:7], s96 offen offset:3072 sc1\n"
"buffer_load_dwordx4 v[64:67], v1, s[4:7], s96 offen sc1\n"
"buffer_load_dwordx4 v[100:103], v1, s[4:7], s96 offen offset:1024 sc1\n"
"buffer_load_dwordx4 v[60:63], v1, s[4:7], s96 offen offset:2048 sc1\n"
"buffer_load_dwordx4 v[76:79], v1, s[4:7], s96 offen offset:3072 sc1\n"
"s_add_u32 s96, s58, 0x400000\n"
"buffer_load_dwordx4 v[96:99], v0, s[4:7], s96 offen sc1\n"
"buffer_load_dwordx4 v[112:115], v0, s[4:7], s96 offen offset:1024 sc1\n"
"buffer_load_dwordx4 v[52:55], v0, s[4:7], s96 offen offset:2048 sc1\n"
"buffer_load_dwordx4 v[152:155], v0, s[4:7], s96 offen offset:3072 sc1\n"
"buffer_load_dwordx4 v[84:87], v1, s[4:7], s96 offen sc1\n"
"buffer_load_dwordx4 v[172:175], v1, s[4:7], s96 offen offset:1024 sc1\n"
"buffer_load_dwordx4 v[128:131], v1, s[4:7], s96 offen offset:2048 sc1\n"
"buffer_load_dwordx4 v[72:75], v1, s[4:7], s96 offen offset:3072 sc1\n"
"s_add_u32 s96, s59, 0x400000\n"
"buffer_load_dwordx4 v[36:39], v0, s[4:7], s96 offen sc1\n"
"buffer_load_dwordx4 v[40:43], v0, s[4:7], s96 offen offset:1024 sc1\n"
"buffer_load_dwordx4 v[80:83], v0, s[4:7], s96 offen offset:2048 sc1\n"
"buffer_load_dwordx4 v[132:135], v0, s[4:7], s96 offen offset:3072 sc1\n"
"buffer_load_dwordx4 v[156:159], v1, s[4:7], s96 offen sc1\n"
"s_waitcnt vmcnt(0)\n"
"v_max_u32_e32 v254, v164, v165\n"
"v_max3_u32 v254, v254, v166, v167\n"
"v_max3_u32 v254, v254, v168, v169\n"
"v_max3_u32 v254, v254, v170, v171\n"
"v_max3_u32 v254, v254, v208, v209\n"
"v_max3_u32 v254, v254, v210, v211\n"
"v_max3_u32 v254, v254, v212, v213\n"
"v_max3_u32 v254, v254, v214, v215\n"
"v_max3_u32 v254, v254, v64, v65\n"
"v_max3_u32 v254, v254, v66, v67\n"
"v_max3_u32 v254, v254, v100, v101\n"
"v_max3_u32 v254, v254, v102, v103\n"
"v_max3_u32 v254, v254, v60, v61\n"
"v_max3_u32 v254, v254, v62, v63\n"
"v_max3_u32 v254, v254, v76, v77\n"
"v_max3_u32 v254, v254, v78, v79\n"
"v_cmp_eq_u32_e32 vcc, -1, v254\n"
"s_cbranch_vccz .Lmk_p60k\n"
"s_sub_u32 s1, s1, 1\n"
"s_cbranch_scc0 .Lmk_p60l\n"
".Lmk_p60k:\n"
"v_mfma_f32_16x16x32_bf16 v[68:71], a[128:131], v[164:167], 0\n"
"v_mfma_f32_16x16x32_bf16 v[68:71], a[132:135], v[168:171], v[68:71]\n"
"v_mfma_f32_16x16x32_bf16 v[68:71], a[136:139], v[208:211], v[68:71]\n"
"v_mfma_f32_16x16x32_bf16 v[68:71], a[140:143], v[212:215], v[68:71]\n"
"v_mfma_f32_16x16x32_bf16 v[68:71], a[144:147], v[64:67], v[68:71]\n"
"v_mfma_f32_16x16x32_bf16 v[68:71], a[148:151], v[100:103], v[68:71]\n"
"v_mfma_f32_16x16x32_bf16 v[68:71], a[152:155], v[60:63], v[68:71]\n"
"v_mfma_f32_16x16x32_bf16 v[68:71], a[156:159], v[76:79], v[68:71]\n"
"v_mfma_f32_16x16x32_bf16 v[140:143], a[188:191], v[76:79], 0\n"
"v_mfma_f32_16x16x32_bf16 v[140:143], a[184:187], v[60:63], v[140:143]\n"
"v_mfma_f32_16x16x32_bf16 v[140:143], a[180:183], v[100:103], v[140:143]\n"
"v_mfma_f32_16x16x32_bf16 v[140:143], a[176:179], v[64:67], v[140:143]\n"
"v_mfma_f32_16x16x32_bf16 v[140:143], a[172:175], v[212:215], v[140:143]\n"
"v_mfma_f32_16x16x32_bf16 v[140:143], a[168:171], v[208:211], v[140:143]\n"
"s_nop 7\n"
"s_branch .Lmk_p60r\n"
".Lmk_p61s:\n"
";\n"
"s_waitcnt vmcnt(16)\n"
"s_mov_b32 s1, 0x10000\n"
".Lmk_p61l:\n"
"s_add_u32 s97, s58, 0x400000\n"
"buffer_load_dwordx4 v[96:99], v0, s[4:7], s97 offen sc1\n"
"buffer_load_dwordx4 v[112:115], v0, s[4:7], s97 offen offset:1024 sc1\n"
"buffer_load_dwordx4 v[52:55], v0, s[4:7], s97 offen offset:2048 sc1\n"
"buffer_load_dwordx4 v[152:155], v0, s[4:7], s97 offen offset:3072 sc1\n"
"buffer_load_dwordx4 v[84:87], v1, s[4:7], s97 offen sc1\n"
"buffer_load_dwordx4 v[172:175], v1, s[4:7], s97 offen offset:1024 sc1\n"
"buffer_load_dwordx4 v[128:131], v1, s[4:7], s97 offen offset:2048 sc1\n"
"buffer_load_dwordx4 v[72:75], v1, s[4:7], s97 offen offset:3072 sc1\n"
"s_add_u32 s97, s59, 0x400000\n"
"buffer_load_dwordx4 v[36:39], v0, s[4:7], s97 offen sc1\n"
"buffer_load_dwordx4 v[40:43], v0, s[4:7], s97 offen offset:1024 sc1\n"
"buffer_load_dwordx4 v[80:83], v0, s[4:7], s97 offen offset:2048 sc1\n"
"buffer_load_dwordx4 v[132:135], v0, s[4:7], s97 offen offset:3072 sc1\n"
"buffer_load_dwordx4 v[156:159], v1, s[4:7], s97 offen sc1\n"
"buffer_load_dwordx4 v[68:71], v1, s[4:7], s97 offen offset:1024 sc1\n"
"buffer_load_dwordx4 v[200:203], v1, s[4:7], s97 offen offset:2048 sc1\n"
"buffer_load_dwordx4 v[140:143], v1, s[4:7], s97 offen offset:3072 sc1\n"
"s_waitcnt vmcnt(0)\n"
"v_max_u32_e32 v253, v96, v97\n"
"v_max3_u32 v253, v253, v98, v99\n"
"v_max3_u32 v253, v253, v112, v113\n"
"v_max3_u32 v253, v253, v114, v115\n"
"v_max3_u32 v253, v253, v52, v53\n"
"v_max3_u32 v253, v253, v54, v55\n"
"v_max3_u32 v253, v253, v152, v153\n"
"v_max3_u32 v253, v253, v154, v155\n"
"v_max3_u32 v253, v253, v84, v85\n"
"v_max3_u32 v253, v253, v86, v87\n"
"v_max3_u32 v253, v253, v172, v173\n"
"v_max3_u32 v253, v253, v174, v175\n"
"v_max3_u32 v253, v253, v128, v129\n"
"v_max3_u32 v253, v253, v130, v131\n"
"v_max3_u32 v253, v253, v72, v73\n"
"v_max3_u32 v253, v253, v74, v75\n"
"v_cmp_eq_u32_e32 vcc, -1, v253\n"
"s_cbranch_vccz .Lmk_p61k\n"
"s_sub_u32 s1, s1, 1\n"
"s_cbranch_scc0 .Lmk_p61l\n"
".Lmk_p61k:\n"
"v_mfma_f32_16x16x32_bf16 v[60:63], a[128:131], v[96:99], 0\n"
"v_mfma_f32_16x16x32_bf16 v[60:63], a[132:135], v[112:115], v[60:63]\n"
"v_mfma_f32_16x16x32_bf16 v[60:63], a[136:139], v[52:55], v[60:63]\n"
"v_mfma_f32_16x16x32_bf16 v[60:63], a[140:143], v[152:155], v[60:63]\n"
"v_mfma_f32_16x16x32_bf16 v[60:63], a[144:147], v[84:87], v[60:63]\n"
"v_mfma_f32_16x16x32_bf16 v[60:63], a[148:151], v[172:175], v[60:63]\n"
"v_mfma_f32_16x16x32_bf16 v[60:63], a[152:155], v[128:131], v[60:63]\n"
"v_mfma_f32_16x16x32_bf16 v[60:63], a[156:159], v[72:75], v[60:63]\n"
"v_mfma_f32_16x16x32_bf16 v[64:67], a[188:191], v[72:75], 0\n"
"v_mfma_f32_16x16x32_bf16 v[64:67], a[184:187], v[128:131], v[64:67]\n"
"v_mfma_f32_16x16x32_bf16 v[64:67], a[180:183], v[172:175], v[64:67]\n"
"v_mfma_f32_16x16x32_bf16 v[64:67], a[176:179], v[84:87], v[64:67]\n"
"v_mfma_f32_16x16x32_bf16 v[64:67], a[172:175], v[152:155], v[64:67]\n"
"v_mfma_f32_16x16x32_bf16 v[64:67], a[168:171], v[52:55], v[64:67]\n"
"s_nop 7\n"
"s_branch .Lmk_p61r\n"
".Lmk_p62s:\n"
";\n"
"s_waitcnt vmcnt(16)\n"
"s_mov_b32 s1, 0x10000\n"
".Lmk_p62l:\n"
"s_add_u32 s97, s59, 0x400000\n"
"buffer_load_dwordx4 v[36:39], v0, s[4:7], s97 offen sc1\n"
"buffer_load_dwordx4 v[40:43], v0, s[4:7], s97 offen offset:1024 sc1\n"
"buffer_load_dwordx4 v[80:83], v0, s[4:7], s97 offen offset:2048 sc1\n"
"buffer_load_dwordx4 v[132:135], v0, s[4:7], s97 offen offset:3072 sc1\n"
"buffer_load_dwordx4 v[156:159], v1, s[4:7], s97 offen sc1\n"
"buffer_load_dwordx4 v[68:71], v1, s[4:7], s97 offen offset:1024 sc1\n"
"buffer_load_dwordx4 v[200:203], v1, s[4:7], s97 offen offset:2048 sc1\n"
"buffer_load_dwordx4 v[140:143], v1, s[4:7], s97 offen offset:3072 sc1\n"
"s_waitcnt vmcnt(0)\n"
"v_max_u32_e32 v254, v36, v37\n"
"v_max3_u32 v254, v254, v38, v39\n"
"v_max3_u32 v254, v254, v40, v41\n"
"v_max3_u32 v254, v254, v42, v43\n"
"v_max3_u32 v254, v254, v80, v81\n"
"v_max3_u32 v254, v254, v82, v83\n"
"v_max3_u32 v254, v254, v132, v133\n"
"v_max3_u32 v254, v254, v134, v135\n"
"v_max3_u32 v254, v254, v156, v157\n"
"v_max3_u32 v254, v254, v158, v159\n"
"v_max3_u32 v254, v254, v68, v69\n"
"v_max3_u32 v254, v254, v70, v71\n"
"v_max3_u32 v254, v254, v200, v201\n"
"v_max3_u32 v254, v254, v202, v203\n"
"v_max3_u32 v254, v254, v140, v141\n"
"v_max3_u32 v254, v254, v142, v143\n"
"v_cmp_eq_u32_e32 vcc, -1, v254\n"
"s_cbranch_vccz .Lmk_p62k\n"
"s_sub_u32 s1, s1, 1\n"
"s_cbranch_scc0 .Lmk_p62l\n"
".Lmk_p62k:\n"
"v_mfma_f32_16x16x32_bf16 v[52:55], a[128:131], v[36:39], v[76:79]\n"
"v_mfma_f32_16x16x32_bf16 v[52:55], a[132:135], v[40:43], v[52:55]\n"
"v_mfma_f32_16x16x32_bf16 v[52:55], a[136:139], v[80:83], v[52:55]\n"
"v_mfma_f32_16x16x32_bf16 v[52:55], a[140:143], v[132:135], v[52:55]\n"
"v_mfma_f32_16x16x32_bf16 v[52:55], a[144:147], v[156:159], v[52:55]\n"
"v_mfma_f32_16x16x32_bf16 v[52:55], a[148:151], v[68:71], v[52:55]\n"
"v_mfma_f32_16x16x32_bf16 v[52:55], a[152:155], v[200:203], v[52:55]\n"
"v_mfma_f32_16x16x32_bf16 v[52:55], a[156:159], v[140:143], v[52:55]\n"
"v_mfma_f32_16x16x32_bf16 v[60:63], a[188:191], v[140:143], v[116:119]\n"
"v_mfma_f32_16x16x32_bf16 v[60:63], a[184:187], v[200:203], v[60:63]\n"
"v_mfma_f32_16x16x32_bf16 v[60:63], a[180:183], v[68:71], v[60:63]\n"
"v_mfma_f32_16x16x32_bf16 v[60:63], a[176:179], v[156:159], v[60:63]\n"
"v_mfma_f32_16x16x32_bf16 v[60:63], a[172:175], v[132:135], v[60:63]\n"
"v_mfma_f32_16x16x32_bf16 v[60:63], a[168:171], v[80:83], v[60:63]\n"
"s_nop 7\n"
"s_branch .Lmk_p62r\n"
".Lmk_p63s:\n"
";\n"
"s_waitcnt vmcnt(16)\n"
"s_mov_b32 s1, 0x10000\n"
".Lmk_p63l:\n"
"s_add_u32 s96, s56, 0x440000\n"
"buffer_load_dwordx4 v[120:123], v0, s[4:7], s96 offen sc1\n"
"buffer_load_dwordx4 v[136:139], v0, s[4:7], s96 offen offset:1024 sc1\n"
"buffer_load_dwordx4 v[144:147], v0, s[4:7], s96 offen offset:2048 sc1\n"
"buffer_load_dwordx4 v[192:195], v0, s[4:7], s96 offen offset:3072 sc1\n"
"buffer_load_dwordx4 v[196:199], v1, s[4:7], s96 offen sc1\n"
"buffer_load_dwordx4 v[188:191], v1, s[4:7], s96 offen offset:1024 sc1\n"
"buffer_load_dwordx4 v[108:111], v1, s[4:7], s96 offen offset:2048 sc1\n"
"buffer_load_dwordx4 v[92:95], v1, s[4:7], s96 offen offset:3072 sc1\n"
"s_add_u32 s96, s57, 0x440000\n"
"buffer_load_dwordx4 v[148:151], v0, s[4:7], s96 offen sc1\n"
"buffer_load_dwordx4 v[180:183], v0, s[4:7], s96 offen offset:1024 sc1\n"
"buffer_load_dwordx4 v[24:27], v0, s[4:7], s96 offen offset:2048 sc1\n"
"buffer_load_dwordx4 v[32:35], v0, s[4:7], s96 offen offset:3072 sc1\n"
"buffer_load_dwordx4 v[44:47], v1, s[4:7], s96 offen sc1\n"
"buffer_load_dwordx4 v[48:51], v1, s[4:7], s96 offen offset:1024 sc1\n"
"buffer_load_dwordx4 v[28:31], v1, s[4:7], s96 offen offset:2048 sc1\n"
"buffer_load_dwordx4 v[56:59], v1, s[4:7], s96 offen offset:3072 sc1\n"
"s_add_u32 s96, s58, 0x440000\n"
"buffer_load_dwordx4 v[36:39], v0, s[4:7], s96 offen sc1\n"
"buffer_load_dwordx4 v[40:43], v0, s[4:7], s96 offen offset:1024 sc1\n"
"buffer_load_dwordx4 v[52:55], v0, s[4:7], s96 offen offset:2048 sc1\n"
"buffer_load_dwordx4 v[60:63], v0, s[4:7], s96 offen offset:3072 sc1\n"
"buffer_load_dwordx4 v[64:67], v1, s[4:7], s96 offen sc1\n"
"buffer_load_dwordx4 v[68:71], v1, s[4:7], s96 offen offset:1024 sc1\n"
"buffer_load_dwordx4 v[72:75], v1, s[4:7], s96 offen offset:2048 sc1\n"
"buffer_load_dwordx4 v[76:79], v1, s[4:7], s96 offen offset:3072 sc1\n"
"s_add_u32 s96, s59, 0x440000\n"
"buffer_load_dwordx4 v[84:87], v0, s[4:7], s96 offen sc1\n"
"buffer_load_dwordx4 v[88:91], v0, s[4:7], s96 offen offset:1024 sc1\n"
"buffer_load_dwordx4 v[100:103], v0, s[4:7], s96 offen offset:2048 sc1\n"
"buffer_load_dwordx4 v[104:107], v0, s[4:7], s96 offen offset:3072 sc1\n"
"s_waitcnt vmcnt(0)\n"
"v_max_u32_e32 v255, v120, v121\n"
"v_max3_u32 v255, v255, v122, v123\n"
"v_max3_u32 v255, v255, v136, v137\n"
"v_max3_u32 v255, v255, v138, v139\n"
"v_max3_u32 v255, v255, v144, v145\n"
"v_max3_u32 v255, v255, v146, v147\n"
"v_max3_u32 v255, v255, v192, v193\n"
"v_max3_u32 v255, v255, v194, v195\n"
"v_max3_u32 v255, v255, v196, v197\n"
"v_max3_u32 v255, v255, v198, v199\n"
"v_max3_u32 v255, v255, v188, v189\n"
"v_max3_u32 v255, v255, v190, v191\n"
"v_max3_u32 v255, v255, v108, v109\n"
"v_max3_u32 v255, v255, v110, v111\n"
"v_max3_u32 v255, v255, v92, v93\n"
"v_max3_u32 v255, v255, v94, v95\n"
"v_cmp_eq_u32_e32 vcc, -1, v255\n"
"s_cbranch_vccz .Lmk_p63k\n"
"s_sub_u32 s1, s1, 1\n"
"s_cbranch_scc0 .Lmk_p63l\n"
".Lmk_p63k:\n"
"v_mfma_f32_16x16x32_bf16 v[80:83], a[128:131], v[120:123], 0\n"
"v_mfma_f32_16x16x32_bf16 v[80:83], a[132:135], v[136:139], v[80:83]\n"
"v_mfma_f32_16x16x32_bf16 v[80:83], a[136:139], v[144:147], v[80:83]\n"
"v_mfma_f32_16x16x32_bf16 v[80:83], a[140:143], v[192:195], v[80:83]\n"
"v_mfma_f32_16x16x32_bf16 v[80:83], a[144:147], v[196:199], v[80:83]\n"
"v_mfma_f32_16x16x32_bf16 v[80:83], a[148:151], v[188:191], v[80:83]\n"
"v_mfma_f32_16x16x32_bf16 v[80:83], a[152:155], v[108:111], v[80:83]\n"
"v_mfma_f32_16x16x32_bf16 v[80:83], a[156:159], v[92:95], v[80:83]\n"
"v_mfma_f32_16x16x32_bf16 v[96:99], a[188:191], v[92:95], 0\n"
"v_mfma_f32_16x16x32_bf16 v[96:99], a[184:187], v[108:111], v[96:99]\n"
"v_mfma_f32_16x16x32_bf16 v[96:99], a[180:183], v[188:191], v[96:99]\n"
"v_mfma_f32_16x16x32_bf16 v[96:99], a[176:179], v[196:199], v[96:99]\n"
"v_mfma_f32_16x16x32_bf16 v[96:99], a[172:175], v[192:195], v[96:99]\n"
"v_mfma_f32_16x16x32_bf16 v[96:99], a[168:171], v[144:147], v[96:99]\n"
"s_nop 7\n"
"s_branch .Lmk_p63r\n"
".Lmk_p64s:\n"
";\n"
"s_waitcnt vmcnt(16)\n"
"s_mov_b32 s1, 0x10000\n"
".Lmk_p64l:\n"
"s_add_u32 s96, s57, 0x440000\n"
"buffer_load_dwordx4 v[148:151], v0, s[4:7], s96 offen sc1\n"
"buffer_load_dwordx4 v[180:183], v0, s[4:7], s96 offen offset:1024 sc1\n"
"buffer_load_dwordx4 v[24:27], v0, s[4:7], s96 offen offset:2048 sc1\n"
"buffer_load_dwordx4 v[32:35], v0, s[4:7], s96 offen offset:3072 sc1\n"
"buffer_load_dwordx4 v[44:47], v1, s[4:7], s96 offen sc1\n"
"buffer_load_dwordx4 v[48:51], v1, s[4:7], s96 offen offset:1024 sc1\n"
"buffer_load_dwordx4 v[28:31], v1, s[4:7], s96 offen offset:2048 sc1\n"
"buffer_load_dwordx4 v[56:59], v1, s[4:7], s96 offen offset:3072 sc1\n"
"s_add_u32 s96, s58, 0x440000\n"
"buffer_load_dwordx4 v[36:39], v0, s[4:7], s96 offen sc1\n"
"buffer_load_dwordx4 v[40:43], v0, s[4:7], s96 offen offset:1024 sc1\n"
"buffer_load_dwordx4 v[52:55], v0, s[4:7], s96 offen offset:2048 sc1\n"
"buffer_load_dwordx4 v[60:63], v0, s[4:7], s96 offen offset:3072 sc1\n"
"buffer_load_dwordx4 v[64:67], v1, s[4:7], s96 offen sc1\n"
"buffer_load_dwordx4 v[68:71], v1, s[4:7], s96 offen offset:1024 sc1\n"
"buffer_load_dwordx4 v[72:75], v1, s[4:7], s96 offen offset:2048 sc1\n"
"buffer_load_dwordx4 v[76:79], v1, s[4:7], s96 offen offset:3072 sc1\n"
"s_add_u32 s96, s59, 0x440000\n"
"buffer_load_dwordx4 v[84:87], v0, s[4:7], s96 offen sc1\n"
"buffer_load_dwordx4 v[88:91], v0, s[4:7], s96 offen offset:1024 sc1\n"
"buffer_load_dwordx4 v[100:103], v0, s[4:7], s96 offen offset:2048 sc1\n"
"buffer_load_dwordx4 v[104:107], v0, s[4:7], s96 offen offset:3072 sc1\n"
"buffer_load_dwordx4 v[80:83], v1, s[4:7], s96 offen sc1\n"
"buffer_load_dwordx4 v[116:119], v1, s[4:7], s96 offen offset:1024 sc1\n"
"buffer_load_dwordx4 v[96:99], v1, s[4:7], s96 offen offset:2048 sc1\n"
"buffer_load_dwordx4 v[92:95], v1, s[4:7], s96 offen offset:3072 sc1\n"
"s_add_u32 s96, s56, 0x400000\n"
"buffer_load_dwordx4 v[120:123], v0, s[4:7], s96 offen sc1\n"
"buffer_load_dwordx4 v[124:127], v0, s[4:7], s96 offen offset:1024 sc1\n"
"s_waitcnt vmcnt(0)\n"
"v_max_u32_e32 v255, v148, v149\n"
"v_max3_u32 v255, v255, v150, v151\n"
"v_max3_u32 v255, v255, v180, v181\n"
"v_max3_u32 v255, v255, v182, v183\n"
"v_max3_u32 v255, v255, v24, v25\n"
"v_max3_u32 v255, v255, v26, v27\n"
"v_max3_u32 v255, v255, v32, v33\n"
"v_max3_u32 v255, v255, v34, v35\n"
"v_max3_u32 v255, v255, v44, v45\n"
"v_max3_u32 v255, v255, v46, v47\n"
"v_max3_u32 v255, v255, v48, v49\n"
"v_max3_u32 v255, v255, v50, v51\n"
"v_max3_u32 v255, v255, v28, v29\n"
"v_max3_u32 v255, v255, v30, v31\n"
"v_max3_u32 v255, v255, v56, v57\n"
"v_max3_u32 v255, v255, v58, v59\n"
"v_cmp_eq_u32_e32 vcc, -1, v255\n"
"s_cbranch_vccz .Lmk_p64k\n"
"s_sub_u32 s1, s1, 1\n"
"s_cbranch_scc0 .Lmk_p64l\n"
".Lmk_p64k:\n"
"v_mfma_f32_16x16x32_bf16 v[108:111], a[128:131], v[148:151], 0\n"
"v_mfma_f32_16x16x32_bf16 v[108:111], a[132:135], v[180:183], v[108:111]\n"
"v_mfma_f32_16x16x32_bf16 v[108:111], a[136:139], v[24:27], v[108:111]\n"
"v_mfma_f32_16x16x32_bf16 v[108:111], a[140:143], v[32:35], v[108:111]\n"
"v_mfma_f32_16x16x32_bf16 v[108:111], a[144:147], v[44:47], v[108:111]\n"
"v_mfma_f32_16x16x32_bf16 v[108:111], a[148:151], v[48:51], v[108:111]\n"
"v_mfma_f32_16x16x32_bf16 v[108:111], a[152:155], v[28:31], v[108:111]\n"
"v_mfma_f32_16x16x32_bf16 v[108:111], a[156:159], v[56:59], v[108:111]\n"
"v_mfma_f32_16x16x32_bf16 v[112:115], a[188:191], v[56:59], 0\n"
"v_mfma_f32_16x16x32_bf16 v[112:115], a[184:187], v[28:31], v[112:115]\n"
"v_mfma_f32_16x16x32_bf16 v[112:115], a[180:183], v[48:51], v[112:115]\n"
"v_mfma_f32_16x16x32_bf16 v[112:115], a[176:179], v[44:47], v[112:115]\n"
"v_mfma_f32_16x16x32_bf16 v[112:115], a[172:175], v[32:35], v[112:115]\n"
"v_mfma_f32_16x16x32_bf16 v[112:115], a[168:171], v[24:27], v[112:115]\n"
"s_nop 7\n"
"s_branch .Lmk_p64r\n"
".Lmk_p65s:\n"
";\n"
"s_waitcnt vmcnt(16)\n"
"s_mov_b32 s1, 0x10000\n"
".Lmk_p65l:\n"
"s_add_u32 s96, s58, 0x440000\n"
"buffer_load_dwordx4 v[36:39], v0, s[4:7], s96 offen sc1\n"
"buffer_load_dwordx4 v[40:43], v0, s[4:7], s96 offen offset:1024 sc1\n"
"buffer_load_dwordx4 v[52:55], v0, s[4:7], s96 offen offset:2048 sc1\n"
"buffer_load_dwordx4 v[60:63], v0, s[4:7], s96 offen offset:3072 sc1\n"
"buffer_load_dwordx4 v[64:67], v1, s[4:7], s96 offen sc1\n"
"buffer_load_dwordx4 v[68:71], v1, s[4:7], s96 offen offset:1024 sc1\n"
"buffer_load_dwordx4 v[72:75], v1, s[4:7], s96 offen offset:2048 sc1\n"
"buffer_load_dwordx4 v[76:79], v1, s[4:7], s96 offen offset:3072 sc1\n"
"s_add_u32 s96, s59, 0x440000\n"
"buffer_load_dwordx4 v[84:87], v0, s[4:7], s96 offen sc1\n"
"buffer_load_dwordx4 v[88:91], v0, s[4:7], s96 offen offset:1024 sc1\n"
"buffer_load_dwordx4 v[100:103], v0, s[4:7], s96 offen offset:2048 sc1\n"
"buffer_load_dwordx4 v[104:107], v0, s[4:7], s96 offen offset:3072 sc1\n"
"buffer_load_dwordx4 v[80:83], v1, s[4:7], s96 offen sc1\n"
"buffer_load_dwordx4 v[116:119], v1, s[4:7], s96 offen offset:1024 sc1\n"
"buffer_load_dwordx4 v[96:99], v1, s[4:7], s96 offen offset:2048 sc1\n"
"buffer_load_dwordx4 v[92:95], v1, s[4:7], s96 offen offset:3072 sc1\n"
"s_add_u32 s96, s56, 0x400000\n"
"buffer_load_dwordx4 v[120:123], v0, s[4:7], s96 offen sc1\n"
"buffer_load_dwordx4 v[124:127], v0, s[4:7], s96 offen offset:1024 sc1\n"
"buffer_load_dwordx4 v[108:111], v0, s[4:7], s96 offen offset:2048 sc1\n"
"buffer_load_dwordx4 v[132:135], v0, s[4:7], s96 offen offset:3072 sc1\n"
"buffer_load_dwordx4 v[112:115], v1, s[4:7], s96 offen sc1\n"
"buffer_load_dwordx4 v[28:31], v1, s[4:7], s96 offen offset:1024 sc1\n"
"buffer_load_dwordx4 v[32:35], v1, s[4:7], s96 offen offset:2048 sc1\n"
"buffer_load_dwordx4 v[24:27], v1, s[4:7], s96 offen offset:3072 sc1\n"
"s_add_u32 s96, s57, 0x400000\n"
"buffer_load_dwordx4 v[48:51], v0, s[4:7], s96 offen sc1\n"
"buffer_load_dwordx4 v[56:59], v0, s[4:7], s96 offen offset:1024 sc1\n"
"buffer_load_dwordx4 v[136:139], v0, s[4:7], s96 offen offset:2048 sc1\n"
"s_waitcnt vmcnt(0)\n"
"v_max_u32_e32 v239, v36, v37\n"
"v_max3_u32 v239, v239, v38, v39\n"
"v_max3_u32 v239, v239, v40, v41\n"
"v_max3_u32 v239, v239, v42, v43\n"
"v_max3_u32 v239, v239, v52, v53\n"
"v_max3_u32 v239, v239, v54, v55\n"
"v_max3_u32 v239, v239, v60, v61\n"
"v_max3_u32 v239, v239, v62, v63\n"
"v_max3_u32 v239, v239, v64, v65\n"
"v_max3_u32 v239, v239, v66, v67\n"
"v_max3_u32 v239, v239, v68, v69\n"
"v_max3_u32 v239, v239, v70, v71\n"
"v_max3_u32 v239, v239, v72, v73\n"
"v_max3_u32 v239, v239, v74, v75\n"
"v_max3_u32 v239, v239, v76, v77\n"
"v_max3_u32 v239, v239, v78, v79\n"
"v_cmp_eq_u32_e32 vcc, -1, v239\n"
"s_cbranch_vccz .Lmk_p65k\n"
"s_sub_u32 s1, s1, 1\n"
"s_cbranch_scc0 .Lmk_p65l\n"
".Lmk_p65k:\n"
"v_mfma_f32_16x16x32_bf16 v[44:47], a[128:131], v[36:39], 0\n"
"v_mfma_f32_16x16x32_bf16 v[44:47], a[132:135], v[40:43], v[44:47]\n"
"v_mfma_f32_16x16x32_bf16 v[44:47], a[136:139], v[52:55], v[44:47]\n"
"v_mfma_f32_16x16x32_bf16 v[44:47], a[140:143], v[60:63], v[44:47]\n"
"v_mfma_f32_16x16x32_bf16 v[44:47], a[144:147], v[64:67], v[44:47]\n"
"v_mfma_f32_16x16x32_bf16 v[44:47], a[148:151], v[68:71], v[44:47]\n"
"v_mfma_f32_16x16x32_bf16 v[44:47], a[152:155], v[72:75], v[44:47]\n"
"v_mfma_f32_16x16x32_bf16 v[44:47], a[156:159], v[76:79], v[44:47]\n"
"v_mfma_f32_16x16x32_bf16 v[128:131], a[188:191], v[76:79], 0\n"
"v_mfma_f32_16x16x32_bf16 v[128:131], a[184:187], v[72:75], v[128:131]\n"
"v_mfma_f32_16x16x32_bf16 v[128:131], a[180:183], v[68:71], v[128:131]\n"
"v_mfma_f32_16x16x32_bf16 v[128:131], a[176:179], v[64:67], v[128:131]\n"
"v_mfma_f32_16x16x32_bf16 v[128:131], a[172:175], v[60:63], v[128:131]\n"
"v_mfma_f32_16x16x32_bf16 v[128:131], a[168:171], v[52:55], v[128:131]\n"
"s_nop 7\n"
"s_branch .Lmk_p65r\n"
".Lmk_p66s:\n"
";\n"
"s_waitcnt vmcnt(16)\n"
"s_mov_b32 s1, 0x10000\n"
".Lmk_p66l:\n"
"s_add_u32 s96, s59, 0x440000\n"
"buffer_load_dwordx4 v[84:87], v0, s[4:7], s96 offen sc1\n"
"buffer_load_dwordx4 v[88:91], v0, s[4:7], s96 offen offset:1024 sc1\n"
"buffer_load_dwordx4 v[100:103], v0, s[4:7], s96 offen offset:2048 sc1\n"
"buffer_load_dwordx4 v[104:107], v0, s[4:7], s96 offen offset:3072 sc1\n"
"buffer_load_dwordx4 v[80:83], v1, s[4:7], s96 offen sc1\n"
"buffer_load_dwordx4 v[116:119], v1, s[4:7], s96 offen offset:1024 sc1\n"
"buffer_load_dwordx4 v[96:99], v1, s[4:7], s96 offen offset:2048 sc1\n"
"buffer_load_dwordx4 v[92:95], v1, s[4:7], s96 offen offset:3072 sc1\n"
"s_add_u32 s96, s56, 0x400000\n"
"buffer_load_dwordx4 v[120:123], v0, s[4:7], s96 offen sc1\n"
"buffer_load_dwordx4 v[124:127], v0, s[4:7], s96 offen offset:1024 sc1\n"
"buffer_load_dwordx4 v[108:111], v0, s[4:7], s96 offen offset:2048 sc1\n"
"buffer_load_dwordx4 v[132:135], v0, s[4:7], s96 offen offset:3072 sc1\n"
"buffer_load_dwordx4 v[112:115], v1, s[4:7], s96 offen sc1\n"
"buffer_load_dwordx4 v[28:31], v1, s[4:7], s96 offen offset:1024 sc1\n"
"buffer_load_dwordx4 v[32:35], v1, s[4:7], s96 offen offset:2048 sc1\n"
"buffer_load_dwordx4 v[24:27], v1, s[4:7], s96 offen offset:3072 sc1\n"
"s_add_u32 s96, s57, 0x400000\n"
"buffer_load_dwordx4 v[48:51], v0, s[4:7], s96 offen sc1\n"
"buffer_load_dwordx4 v[56:59], v0, s[4:7], s96 offen offset:1024 sc1\n"
"buffer_load_dwordx4 v[136:139], v0, s[4:7], s96 offen offset:2048 sc1\n"
"buffer_load_dwordx4 v[44:47], v0, s[4:7], s96 offen offset:3072 sc1\n"
"buffer_load_dwordx4 v[160:163], v1, s[4:7], s96 offen sc1\n"
"buffer_load_dwordx4 v[128:131], v1, s[4:7], s96 offen offset:1024 sc1\n"
"buffer_load_dwordx4 v[72:75], v1, s[4:7], s96 offen offset:2048 sc1\n"
"buffer_load_dwordx4 v[60:63], v1, s[4:7], s96 offen offset:3072 sc1\n"
"s_add_u32 s96, s58, 0x400000\n"
"buffer_load_dwordx4 v[36:39], v0, s[4:7], s96 offen sc1\n"
"buffer_load_dwordx4 v[68:71], v0, s[4:7], s96 offen offset:1024 sc1\n"
"buffer_load_dwordx4 v[76:79], v0, s[4:7], s96 offen offset:2048 sc1\n"
"buffer_load_dwordx4 v[148:151], v0, s[4:7], s96 offen offset:3072 sc1\n"
"s_waitcnt vmcnt(0)\n"
"v_max_u32_e32 v255, v84, v85\n"
"v_max3_u32 v255, v255, v86, v87\n"
"v_max3_u32 v255, v255, v88, v89\n"
"v_max3_u32 v255, v255, v90, v91\n"
"v_max3_u32 v255, v255, v100, v101\n"
"v_max3_u32 v255, v255, v102, v103\n"
"v_max3_u32 v255, v255, v104, v105\n"
"v_max3_u32 v255, v255, v106, v107\n"
"v_max3_u32 v255, v255, v80, v81\n"
"v_max3_u32 v255, v255, v82, v83\n"
"v_max3_u32 v255, v255, v116, v117\n"
"v_max3_u32 v255, v255, v118, v119\n"
"v_max3_u32 v255, v255, v96, v97\n"
"v_max3_u32 v255, v255, v98, v99\n"
"v_max3_u32 v255, v255, v92, v93\n"
"v_max3_u32 v255, v255, v94, v95\n"
"v_cmp_eq_u32_e32 vcc, -1, v255\n"
"s_cbranch_vccz .Lmk_p66k\n"
"s_sub_u32 s1, s1, 1\n"
"s_cbranch_scc0 .Lmk_p66l\n"
".Lmk_p66k:\n"
"v_mfma_f32_16x16x32_bf16 v[64:67], a[128:131], v[84:87], v[140:143]\n"
"v_mfma_f32_16x16x32_bf16 v[64:67], a[132:135], v[88:91], v[64:67]\n"
"v_mfma_f32_16x16x32_bf16 v[64:67], a[136:139], v[100:103], v[64:67]\n"
"v_mfma_f32_16x16x32_bf16 v[64:67], a[140:143], v[104:107], v[64:67]\n"
"v_mfma_f32_16x16x32_bf16 v[64:67], a[144:147], v[80:83], v[64:67]\n"
"v_mfma_f32_16x16x32_bf16 v[64:67], a[148:151], v[116:119], v[64:67]\n"
"v_mfma_f32_16x16x32_bf16 v[64:67], a[152:155], v[96:99], v[64:67]\n"
"v_mfma_f32_16x16x32_bf16 v[64:67], a[156:159], v[92:95], v[64:67]\n"
"v_mfma_f32_16x16x32_bf16 v[52:55], a[188:191], v[92:95], v[152:155]\n"
"v_mfma_f32_16x16x32_bf16 v[52:55], a[184:187], v[96:99], v[52:55]\n"
"v_mfma_f32_16x16x32_bf16 v[52:55], a[180:183], v[116:119], v[52:55]\n"
"v_mfma_f32_16x16x32_bf16 v[52:55], a[176:179], v[80:83], v[52:55]\n"
"v_mfma_f32_16x16x32_bf16 v[52:55], a[172:175], v[104:107], v[52:55]\n"
"v_mfma_f32_16x16x32_bf16 v[52:55], a[168:171], v[100:103], v[52:55]\n"
"s_nop 7\n"
"s_branch .Lmk_p66r\n"
".Lmk_p68s:\n"
";\n"
"s_waitcnt vmcnt(16)\n"
"s_mov_b32 s1, 0x10000\n"
".Lmk_p68l:\n"
"s_add_u32 s95, s56, 0x400000\n"
"buffer_load_dwordx4 v[120:123], v0, s[4:7], s95 offen sc1\n"
"buffer_load_dwordx4 v[124:127], v0, s[4:7], s95 offen offset:1024 sc1\n"
"buffer_load_dwordx4 v[108:111], v0, s[4:7], s95 offen offset:2048 sc1\n"
"buffer_load_dwordx4 v[132:135], v0, s[4:7], s95 offen offset:3072 sc1\n"
"buffer_load_dwordx4 v[112:115], v1, s[4:7], s95 offen sc1\n"
"buffer_load_dwordx4 v[28:31], v1, s[4:7], s95 offen offset:1024 sc1\n"
"buffer_load_dwordx4 v[32:35], v1, s[4:7], s95 offen offset:2048 sc1\n"
"buffer_load_dwordx4 v[24:27], v1, s[4:7], s95 offen offset:3072 sc1\n"
"s_add_u32 s95, s57, 0x400000\n"
"buffer_load_dwordx4 v[48:51], v0, s[4:7], s95 offen sc1\n"
"buffer_load_dwordx4 v[56:59], v0, s[4:7], s95 offen offset:1024 sc1\n"
"buffer_load_dwordx4 v[136:139], v0, s[4:7], s95 offen offset:2048 sc1\n"
"buffer_load_dwordx4 v[44:47], v0, s[4:7], s95 offen offset:3072 sc1\n"
"buffer_load_dwordx4 v[160:163], v1, s[4:7], s95 offen sc1\n"
"buffer_load_dwordx4 v[128:131], v1, s[4:7], s95 offen offset:1024 sc1\n"
"buffer_load_dwordx4 v[72:75], v1, s[4:7], s95 offen offset:2048 sc1\n"
"buffer_load_dwordx4 v[60:63], v1, s[4:7], s95 offen offset:3072 sc1\n"
"s_add_u32 s95, s58, 0x400000\n"
"buffer_load_dwordx4 v[36:39], v0, s[4:7], s95 offen sc1\n"
"buffer_load_dwordx4 v[68:71], v0, s[4:7], s95 offen offset:1024 sc1\n"
"buffer_load_dwordx4 v[76:79], v0, s[4:7], s95 offen offset:2048 sc1\n"
"buffer_load_dwordx4 v[148:151], v0, s[4:7], s95 offen offset:3072 sc1\n"
"buffer_load_dwordx4 v[152:155], v1, s[4:7], s95 offen sc1\n"
"buffer_load_dwordx4 v[172:175], v1, s[4:7], s95 offen offset:1024 sc1\n"
"buffer_load_dwordx4 v[176:179], v1, s[4:7], s95 offen offset:2048 sc1\n"
"buffer_load_dwordx4 v[64:67], v1, s[4:7], s95 offen offset:3072 sc1\n"
"s_add_u32 s95, s59, 0x400000\n"
"buffer_load_dwordx4 v[80:83], v0, s[4:7], s95 offen sc1\n"
"buffer_load_dwordx4 v[84:87], v0, s[4:7], s95 offen offset:1024 sc1\n"
"buffer_load_dwordx4 v[88:91], v0, s[4:7], s95 offen offset:2048 sc1\n"
"buffer_load_dwordx4 v[92:95], v0, s[4:7], s95 offen offset:3072 sc1\n"
"buffer_load_dwordx4 v[96:99], v1, s[4:7], s95 offen sc1\n"
"buffer_load_dwordx4 v[100:103], v1, s[4:7], s95 offen offset:1024 sc1\n"
"buffer_load_dwordx4 v[104:107], v1, s[4:7], s95 offen offset:2048 sc1\n"
"buffer_load_dwordx4 v[116:119], v1, s[4:7], s95 offen offset:3072 sc1\n"
"s_waitcnt vmcnt(0)\n"
"v_max_u32_e32 v255, v120, v121\n"
"v_max3_u32 v255, v255, v122, v123\n"
"v_max3_u32 v255, v255, v124, v125\n"
"v_max3_u32 v255, v255, v126, v127\n"
"v_max3_u32 v255, v255, v108, v109\n"
"v_max3_u32 v255, v255, v110, v111\n"
"v_max3_u32 v255, v255, v132, v133\n"
"v_max3_u32 v255, v255, v134, v135\n"
"v_max3_u32 v255, v255, v112, v113\n"
"v_max3_u32 v255, v255, v114, v115\n"
"v_max3_u32 v255, v255, v28, v29\n"
"v_max3_u32 v255, v255, v30, v31\n"
"v_max3_u32 v255, v255, v32, v33\n"
"v_max3_u32 v255, v255, v34, v35\n"
"v_max3_u32 v255, v255, v24, v25\n"
"v_max3_u32 v255, v255, v26, v27\n"
"v_cmp_eq_u32_e32 vcc, -1, v255\n"
"s_cbranch_vccz .Lmk_p68k\n"
"s_sub_u32 s1, s1, 1\n"
"s_cbranch_scc0 .Lmk_p68l\n"
".Lmk_p68k:\n"
"v_mfma_f32_16x16x32_bf16 v[140:143], a[128:131], v[120:123], 0\n"
"v_mfma_f32_16x16x32_bf16 v[140:143], a[132:135], v[124:127], v[140:143]\n"
"v_mfma_f32_16x16x32_bf16 v[140:143], a[136:139], v[108:111], v[140:143]\n"
"v_mfma_f32_16x16x32_bf16 v[140:143], a[140:143], v[132:135], v[140:143]\n"
"v_mfma_f32_16x16x32_bf16 v[140:143], a[144:147], v[112:115], v[140:143]\n"
"v_mfma_f32_16x16x32_bf16 v[140:143], a[148:151], v[28:31], v[140:143]\n"
"v_mfma_f32_16x16x32_bf16 v[140:143], a[152:155], v[32:35], v[140:143]\n"
"v_mfma_f32_16x16x32_bf16 v[140:143], a[156:159], v[24:27], v[140:143]\n"
"v_mfma_f32_16x16x32_bf16 v[40:43], a[188:191], v[24:27], 0\n"
"v_mfma_f32_16x16x32_bf16 v[40:43], a[184:187], v[32:35], v[40:43]\n"
"v_mfma_f32_16x16x32_bf16 v[40:43], a[180:183], v[28:31], v[40:43]\n"
"v_mfma_f32_16x16x32_bf16 v[40:43], a[176:179], v[112:115], v[40:43]\n"
"v_mfma_f32_16x16x32_bf16 v[40:43], a[172:175], v[132:135], v[40:43]\n"
"v_mfma_f32_16x16x32_bf16 v[40:43], a[168:171], v[108:111], v[40:43]\n"
"s_nop 7\n"
"s_branch .Lmk_p68r\n"
".Lmk_p69s:\n"
";\n"
"s_waitcnt vmcnt(16)\n"
"s_mov_b32 s1, 0x10000\n"
".Lmk_p69l:\n"
"s_add_u32 s97, s57, 0x400000\n"
"buffer_load_dwordx4 v[48:51], v0, s[4:7], s97 offen sc1\n"
"buffer_load_dwordx4 v[56:59], v0, s[4:7], s97 offen offset:1024 sc1\n"
"buffer_load_dwordx4 v[136:139], v0, s[4:7], s97 offen offset:2048 sc1\n"
"buffer_load_dwordx4 v[44:47], v0, s[4:7], s97 offen offset:3072 sc1\n"
"buffer_load_dwordx4 v[160:163], v1, s[4:7], s97 offen sc1\n"
"buffer_load_dwordx4 v[128:131], v1, s[4:7], s97 offen offset:1024 sc1\n"
"buffer_load_dwordx4 v[72:75], v1, s[4:7], s97 offen offset:2048 sc1\n"
"buffer_load_dwordx4 v[60:63], v1, s[4:7], s97 offen offset:3072 sc1\n"
"s_add_u32 s97, s58, 0x400000\n"
"buffer_load_dwordx4 v[36:39], v0, s[4:7], s97 offen sc1\n"
"buffer_load_dwordx4 v[68:71], v0, s[4:7], s97 offen offset:1024 sc1\n"
"buffer_load_dwordx4 v[76:79], v0, s[4:7], s97 offen offset:2048 sc1\n"
"buffer_load_dwordx4 v[148:151], v0, s[4:7], s97 offen offset:3072 sc1\n"
"buffer_load_dwordx4 v[152:155], v1, s[4:7], s97 offen sc1\n"
"buffer_load_dwordx4 v[172:175], v1, s[4:7], s97 offen offset:1024 sc1\n"
"buffer_load_dwordx4 v[176:179], v1, s[4:7], s97 offen offset:2048 sc1\n"
"buffer_load_dwordx4 v[64:67], v1, s[4:7], s97 offen offset:3072 sc1\n"
"s_add_u32 s97, s59, 0x400000\n"
"buffer_load_dwordx4 v[80:83], v0, s[4:7], s97 offen sc1\n"
"buffer_load_dwordx4 v[84:87], v0, s[4:7], s97 offen offset:1024 sc1\n"
"buffer_load_dwordx4 v[88:91], v0, s[4:7], s97 offen offset:2048 sc1\n"
"buffer_load_dwordx4 v[92:95], v0, s[4:7], s97 offen offset:3072 sc1\n"
"buffer_load_dwordx4 v[96:99], v1, s[4:7], s97 offen sc1\n"
"buffer_load_dwordx4 v[100:103], v1, s[4:7], s97 offen offset:1024 sc1\n"
"buffer_load_dwordx4 v[104:107], v1, s[4:7], s97 offen offset:2048 sc1\n"
"buffer_load_dwordx4 v[116:119], v1, s[4:7], s97 offen offset:3072 sc1\n"
"s_waitcnt vmcnt(0)\n"
"v_max_u32_e32 v255, v48, v49\n"
"v_max3_u32 v255, v255, v50, v51\n"
"v_max3_u32 v255, v255, v56, v57\n"
"v_max3_u32 v255, v255, v58, v59\n"
"v_max3_u32 v255, v255, v136, v137\n"
"v_max3_u32 v255, v255, v138, v139\n"
"v_max3_u32 v255, v255, v44, v45\n"
"v_max3_u32 v255, v255, v46, v47\n"
"v_max3_u32 v255, v255, v160, v161\n"
"v_max3_u32 v255, v255, v162, v163\n"
"v_max3_u32 v255, v255, v128, v129\n"
"v_max3_u32 v255, v255, v130, v131\n"
"v_max3_u32 v255, v255, v72, v73\n"
"v_max3_u32 v255, v255, v74, v75\n"
"v_max3_u32 v255, v255, v60, v61\n"
"v_max3_u32 v255, v255, v62, v63\n"
"v_cmp_eq_u32_e32 vcc, -1, v255\n"
"s_cbranch_vccz .Lmk_p69k\n"
"s_sub_u32 s1, s1, 1\n"
"s_cbranch_scc0 .Lmk_p69l\n"
".Lmk_p69k:\n"
"v_mfma_f32_16x16x32_bf16 v[24:27], a[128:131], v[48:51], 0\n"
"v_mfma_f32_16x16x32_bf16 v[24:27], a[132:135], v[56:59], v[24:27]\n"
"v_mfma_f32_16x16x32_bf16 v[24:27], a[136:139], v[136:139], v[24:27]\n"
"v_mfma_f32_16x16x32_bf16 v[24:27], a[140:143], v[44:47], v[24:27]\n"
"v_mfma_f32_16x16x32_bf16 v[24:27], a[144:147], v[160:163], v[24:27]\n"
"v_mfma_f32_16x16x32_bf16 v[24:27], a[148:151], v[128:131], v[24:27]\n"
"v_mfma_f32_16x16x32_bf16 v[24:27], a[152:155], v[72:75], v[24:27]\n"
"v_mfma_f32_16x16x32_bf16 v[24:27], a[156:159], v[60:63], v[24:27]\n"
"v_mfma_f32_16x16x32_bf16 v[28:31], a[188:191], v[60:63], 0\n"
"v_mfma_f32_16x16x32_bf16 v[28:31], a[184:187], v[72:75], v[28:31]\n"
"v_mfma_f32_16x16x32_bf16 v[28:31], a[180:183], v[128:131], v[28:31]\n"
"v_mfma_f32_16x16x32_bf16 v[28:31], a[176:179], v[160:163], v[28:31]\n"
"v_mfma_f32_16x16x32_bf16 v[28:31], a[172:175], v[44:47], v[28:31]\n"
"v_mfma_f32_16x16x32_bf16 v[28:31], a[168:171], v[136:139], v[28:31]\n"
"s_nop 7\n"
"s_branch .Lmk_p69r\n"
".Lmk_p70s:\n"
";\n"
"s_waitcnt vmcnt(16)\n"
"s_mov_b32 s1, 0x10000\n"
".Lmk_p70l:\n"
"s_add_u32 s97, s58, 0x400000\n"
"buffer_load_dwordx4 v[36:39], v0, s[4:7], s97 offen sc1\n"
"buffer_load_dwordx4 v[68:71], v0, s[4:7], s97 offen offset:1024 sc1\n"
"buffer_load_dwordx4 v[76:79], v0, s[4:7], s97 offen offset:2048 sc1\n"
"buffer_load_dwordx4 v[148:151], v0, s[4:7], s97 offen offset:3072 sc1\n"
"buffer_load_dwordx4 v[152:155], v1, s[4:7], s97 offen sc1\n"
"buffer_load_dwordx4 v[172:175], v1, s[4:7], s97 offen offset:1024 sc1\n"
"buffer_load_dwordx4 v[176:179], v1, s[4:7], s97 offen offset:2048 sc1\n"
"buffer_load_dwordx4 v[64:67], v1, s[4:7], s97 offen offset:3072 sc1\n"
"s_add_u32 s97, s59, 0x400000\n"
"buffer_load_dwordx4 v[80:83], v0, s[4:7], s97 offen sc1\n"
"buffer_load_dwordx4 v[84:87], v0, s[4:7], s97 offen offset:1024 sc1\n"
"buffer_load_dwordx4 v[88:91], v0, s[4:7], s97 offen offset:2048 sc1\n"
"buffer_load_dwordx4 v[92:95], v0, s[4:7], s97 offen offset:3072 sc1\n"
"buffer_load_dwordx4 v[96:99], v1, s[4:7], s97 offen sc1\n"
"buffer_load_dwordx4 v[100:103], v1, s[4:7], s97 offen offset:1024 sc1\n"
"buffer_load_dwordx4 v[104:107], v1, s[4:7], s97 offen offset:2048 sc1\n"
"buffer_load_dwordx4 v[116:119], v1, s[4:7], s97 offen offset:3072 sc1\n"
"s_waitcnt vmcnt(0)\n"
"v_max_u32_e32 v239, v36, v37\n"
"v_max3_u32 v239, v239, v38, v39\n"
"v_max3_u32 v239, v239, v68, v69\n"
"v_max3_u32 v239, v239, v70, v71\n"
"v_max3_u32 v239, v239, v76, v77\n"
"v_max3_u32 v239, v239, v78, v79\n"
"v_max3_u32 v239, v239, v148, v149\n"
"v_max3_u32 v239, v239, v150, v151\n"
"v_max3_u32 v239, v239, v152, v153\n"
"v_max3_u32 v239, v239, v154, v155\n"
"v_max3_u32 v239, v239, v172, v173\n"
"v_max3_u32 v239, v239, v174, v175\n"
"v_max3_u32 v239, v239, v176, v177\n"
"v_max3_u32 v239, v239, v178, v179\n"
"v_max3_u32 v239, v239, v64, v65\n"
"v_max3_u32 v239, v239, v66, v67\n"
"v_cmp_eq_u32_e32 vcc, -1, v239\n"
"s_cbranch_vccz .Lmk_p70k\n"
"s_sub_u32 s1, s1, 1\n"
"s_cbranch_scc0 .Lmk_p70l\n"
".Lmk_p70k:\n"
"v_mfma_f32_16x16x32_bf16 v[24:27], a[128:131], v[36:39], 0\n"
"v_mfma_f32_16x16x32_bf16 v[24:27], a[132:135], v[68:71], v[24:27]\n"
"v_mfma_f32_16x16x32_bf16 v[24:27], a[136:139], v[76:79], v[24:27]\n"
"v_mfma_f32_16x16x32_bf16 v[24:27], a[140:143], v[148:151], v[24:27]\n"
"v_mfma_f32_16x16x32_bf16 v[24:27], a[144:147], v[152:155], v[24:27]\n"
"v_mfma_f32_16x16x32_bf16 v[24:27], a[148:151], v[172:175], v[24:27]\n"
"v_mfma_f32_16x16x32_bf16 v[24:27], a[152:155], v[176:179], v[24:27]\n"
"v_mfma_f32_16x16x32_bf16 v[24:27], a[156:159], v[64:67], v[24:27]\n"
"v_mfma_f32_16x16x32_bf16 v[32:35], a[188:191], v[64:67], 0\n"
"v_mfma_f32_16x16x32_bf16 v[32:35], a[184:187], v[176:179], v[32:35]\n"
"v_mfma_f32_16x16x32_bf16 v[32:35], a[180:183], v[172:175], v[32:35]\n"
"v_mfma_f32_16x16x32_bf16 v[32:35], a[176:179], v[152:155], v[32:35]\n"
"v_mfma_f32_16x16x32_bf16 v[32:35], a[172:175], v[148:151], v[32:35]\n"
"v_mfma_f32_16x16x32_bf16 v[32:35], a[168:171], v[76:79], v[32:35]\n"
"s_nop 7\n"
"s_branch .Lmk_p70r\n"
".Lmk_p71s:\n"
";\n"
"s_waitcnt vmcnt(16)\n"
"s_mov_b32 s1, 0x10000\n"
".Lmk_p71l:\n"
"s_add_u32 s97, s59, 0x400000\n"
"buffer_load_dwordx4 v[80:83], v0, s[4:7], s97 offen sc1\n"
"buffer_load_dwordx4 v[84:87], v0, s[4:7], s97 offen offset:1024 sc1\n"
"buffer_load_dwordx4 v[88:91], v0, s[4:7], s97 offen offset:2048 sc1\n"
"buffer_load_dwordx4 v[92:95], v0, s[4:7], s97 offen offset:3072 sc1\n"
"buffer_load_dwordx4 v[96:99], v1, s[4:7], s97 offen sc1\n"
"buffer_load_dwordx4 v[100:103], v1, s[4:7], s97 offen offset:1024 sc1\n"
"buffer_load_dwordx4 v[104:107], v1, s[4:7], s97 offen offset:2048 sc1\n"
"buffer_load_dwordx4 v[116:119], v1, s[4:7], s97 offen offset:3072 sc1\n"
"s_waitcnt vmcnt(0)\n"
"v_max_u32_e32 v255, v80, v81\n"
"v_max3_u32 v255, v255, v82, v83\n"
"v_max3_u32 v255, v255, v84, v85\n"
"v_max3_u32 v255, v255, v86, v87\n"
"v_max3_u32 v255, v255, v88, v89\n"
"v_max3_u32 v255, v255, v90, v91\n"
"v_max3_u32 v255, v255, v92, v93\n"
"v_max3_u32 v255, v255, v94, v95\n"
"v_max3_u32 v255, v255, v96, v97\n"
"v_max3_u32 v255, v255, v98, v99\n"
"v_max3_u32 v255, v255, v100, v101\n"
"v_max3_u32 v255, v255, v102, v103\n"
"v_max3_u32 v255, v255, v104, v105\n"
"v_max3_u32 v255, v255, v106, v107\n"
"v_max3_u32 v255, v255, v116, v117\n"
"v_max3_u32 v255, v255, v118, v119\n"
"v_cmp_eq_u32_e32 vcc, -1, v255\n"
"s_cbranch_vccz .Lmk_p71k\n"
"s_sub_u32 s1, s1, 1\n"
"s_cbranch_scc0 .Lmk_p71l\n"
".Lmk_p71k:\n"
"v_mfma_f32_16x16x32_bf16 v[76:79], a[128:131], v[80:83], v[44:47]\n"
"v_mfma_f32_16x16x32_bf16 v[76:79], a[132:135], v[84:87], v[76:79]\n"
"v_mfma_f32_16x16x32_bf16 v[76:79], a[136:139], v[88:91], v[76:79]\n"
"v_mfma_f32_16x16x32_bf16 v[76:79], a[140:143], v[92:95], v[76:79]\n"
"v_mfma_f32_16x16x32_bf16 v[76:79], a[144:147], v[96:99], v[76:79]\n"
"v_mfma_f32_16x16x32_bf16 v[76:79], a[148:151], v[100:103], v[76:79]\n"
"v_mfma_f32_16x16x32_bf16 v[76:79], a[152:155], v[104:107], v[76:79]\n"
"v_mfma_f32_16x16x32_bf16 v[76:79], a[156:159], v[116:119], v[76:79]\n"
"v_mfma_f32_16x16x32_bf16 v[68:71], a[188:191], v[116:119], v[56:59]\n"
"v_mfma_f32_16x16x32_bf16 v[68:71], a[184:187], v[104:107], v[68:71]\n"
"v_mfma_f32_16x16x32_bf16 v[68:71], a[180:183], v[100:103], v[68:71]\n"
"v_mfma_f32_16x16x32_bf16 v[68:71], a[176:179], v[96:99], v[68:71]\n"
"v_mfma_f32_16x16x32_bf16 v[68:71], a[172:175], v[92:95], v[68:71]\n"
"v_mfma_f32_16x16x32_bf16 v[68:71], a[168:171], v[88:91], v[68:71]\n"
"s_nop 7\n"
"s_branch .Lmk_p71r\n"
".Lmk_p76s:\n"
";\n"
"s_waitcnt vmcnt(16)\n"
"s_mov_b32 s1, 0x10000\n"
".Lmk_p76l:\n"
"s_add_u32 s96, s52, 0x40000\n"
"buffer_load_dwordx4 v[96:99], v0, s[4:7], s96 offen sc1\n"
"buffer_load_dwordx4 v[88:91], v0, s[4:7], s96 offen offset:1024 sc1\n"
"buffer_load_dwordx4 v[80:83], v0, s[4:7], s96 offen offset:2048 sc1\n"
"buffer_load_dwordx4 v[84:87], v0, s[4:7], s96 offen offset:3072 sc1\n"
"buffer_load_dwordx4 v[92:95], v1, s[4:7], s96 offen sc1\n"
"buffer_load_dwordx4 v[104:107], v1, s[4:7], s96 offen offset:1024 sc1\n"
"buffer_load_dwordx4 v[116:119], v1, s[4:7], s96 offen offset:2048 sc1\n"
"buffer_load_dwordx4 v[124:127], v1, s[4:7], s96 offen offset:3072 sc1\n"
"s_add_u32 s96, s53, 0x40000\n"
"buffer_load_dwordx4 v[128:131], v0, s[4:7], s96 offen sc1\n"
"buffer_load_dwordx4 v[144:147], v0, s[4:7], s96 offen offset:1024 sc1\n"
"buffer_load_dwordx4 v[148:151], v0, s[4:7], s96 offen offset:2048 sc1\n"
"buffer_load_dwordx4 v[152:155], v0, s[4:7], s96 offen offset:3072 sc1\n"
"buffer_load_dwordx4 v[156:159], v1, s[4:7], s96 offen sc1\n"
"buffer_load_dwordx4 v[160:163], v1, s[4:7], s96 offen offset:1024 sc1\n"
"buffer_load_dwordx4 v[164:167], v1, s[4:7], s96 offen offset:2048 sc1\n"
"buffer_load_dwordx4 v[168:171], v1, s[4:7], s96 offen offset:3072 sc1\n"
"s_add_u32 s96, s54, 0x40000\n"
"buffer_load_dwordx4 v[176:179], v0, s[4:7], s96 offen sc1\n"
"buffer_load_dwordx4 v[48:51], v0, s[4:7], s96 offen offset:1024 sc1\n"
"s_waitcnt vmcnt(0)\n"
"v_max_u32_e32 v252, v96, v97\n"
"v_max3_u32 v252, v252, v98, v99\n"
"v_max3_u32 v252, v252, v88, v89\n"
"v_max3_u32 v252, v252, v90, v91\n"
"v_max3_u32 v252, v252, v80, v81\n"
"v_max3_u32 v252, v252, v82, v83\n"
"v_max3_u32 v252, v252, v84, v85\n"
"v_max3_u32 v252, v252, v86, v87\n"
"v_max3_u32 v252, v252, v92, v93\n"
"v_max3_u32 v252, v252, v94, v95\n"
"v_max3_u32 v252, v252, v104, v105\n"
"v_max3_u32 v252, v252, v106, v107\n"
"v_max3_u32 v252, v252, v116, v117\n"
"v_max3_u32 v252, v252, v118, v119\n"
"v_max3_u32 v252, v252, v124, v125\n"
"v_max3_u32 v252, v252, v126, v127\n"
"v_cmp_eq_u32_e32 vcc, -1, v252\n"
"s_cbranch_vccz .Lmk_p76k\n"
"s_sub_u32 s1, s1, 1\n"
"s_cbranch_scc0 .Lmk_p76l\n"
".Lmk_p76k:\n"
"v_mfma_f32_16x16x32_bf16 v[172:175], a[0:3], v[96:99], 0\n"
"v_mfma_f32_16x16x32_bf16 v[172:175], a[4:7], v[88:91], v[172:175]\n"
"v_mfma_f32_16x16x32_bf16 v[172:175], a[8:11], v[80:83], v[172:175]\n"
"v_mfma_f32_16x16x32_bf16 v[172:175], a[12:15], v[84:87], v[172:175]\n"
"v_mfma_f32_16x16x32_bf16 v[172:175], a[16:19], v[92:95], v[172:175]\n"
"v_mfma_f32_16x16x32_bf16 v[172:175], a[20:23], v[104:107], v[172:175]\n"
"v_mfma_f32_16x16x32_bf16 v[172:175], a[24:27], v[116:119], v[172:175]\n"
"v_mfma_f32_16x16x32_bf16 v[172:175], a[28:31], v[124:127], v[172:175]\n"
"v_mfma_f32_16x16x32_bf16 v[180:183], a[60:63], v[124:127], 0\n"
"v_mfma_f32_16x16x32_bf16 v[180:183], a[56:59], v[116:119], v[180:183]\n"
"v_mfma_f32_16x16x32_bf16 v[180:183], a[52:55], v[104:107], v[180:183]\n"
"v_mfma_f32_16x16x32_bf16 v[180:183], a[48:51], v[92:95], v[180:183]\n"
"v_mfma_f32_16x16x32_bf16 v[180:183], a[44:47], v[84:87], v[180:183]\n"
"v_mfma_f32_16x16x32_bf16 v[180:183], a[40:43], v[80:83], v[180:183]\n"
"s_nop 7\n"
"s_branch .Lmk_p76r\n"
".Lmk_p77s:\n"
";\n"
"s_waitcnt vmcnt(16)\n"
"s_mov_b32 s1, 0x10000\n"
".Lmk_p77l:\n"
"s_add_u32 s97, s53, 0x40000\n"
"buffer_load_dwordx4 v[128:131], v0, s[4:7], s97 offen sc1\n"
"buffer_load_dwordx4 v[144:147], v0, s[4:7], s97 offen offset:1024 sc1\n"
"buffer_load_dwordx4 v[148:151], v0, s[4:7], s97 offen offset:2048 sc1\n"
"buffer_load_dwordx4 v[152:155], v0, s[4:7], s97 offen offset:3072 sc1\n"
"buffer_load_dwordx4 v[156:159], v1, s[4:7], s97 offen sc1\n"
"buffer_load_dwordx4 v[160:163], v1, s[4:7], s97 offen offset:1024 sc1\n"
"buffer_load_dwordx4 v[164:167], v1, s[4:7], s97 offen offset:2048 sc1\n"
"buffer_load_dwordx4 v[168:171], v1, s[4:7], s97 offen offset:3072 sc1\n"
"s_add_u32 s97, s54, 0x40000\n"
"buffer_load_dwordx4 v[176:179], v0, s[4:7], s97 offen sc1\n"
"buffer_load_dwordx4 v[48:51], v0, s[4:7], s97 offen offset:1024 sc1\n"
"buffer_load_dwordx4 v[132:135], v0, s[4:7], s97 offen offset:2048 sc1\n"
"buffer_load_dwordx4 v[184:187], v0, s[4:7], s97 offen offset:3072 sc1\n"
"buffer_load_dwordx4 v[44:47], v1, s[4:7], s97 offen sc1\n"
"buffer_load_dwordx4 v[192:195], v1, s[4:7], s97 offen offset:1024 sc1\n"
"buffer_load_dwordx4 v[196:199], v1, s[4:7], s97 offen offset:2048 sc1\n"
"buffer_load_dwordx4 v[200:203], v1, s[4:7], s97 offen offset:3072 sc1\n"
"s_add_u32 s97, s55, 0x40000\n"
"buffer_load_dwordx4 v[80:83], v0, s[4:7], s97 offen sc1\n"
"buffer_load_dwordx4 v[84:87], v0, s[4:7], s97 offen offset:1024 sc1\n"
"s_waitcnt vmcnt(0)\n"
"v_max_u32_e32 v247, v128, v129\n"
"v_max3_u32 v247, v247, v130, v131\n"
"v_max3_u32 v247, v247, v144, v145\n"
"v_max3_u32 v247, v247, v146, v147\n"
"v_max3_u32 v247, v247, v148, v149\n"
"v_max3_u32 v247, v247, v150, v151\n"
"v_max3_u32 v247, v247, v152, v153\n"
"v_max3_u32 v247, v247, v154, v155\n"
"v_max3_u32 v247, v247, v156, v157\n"
"v_max3_u32 v247, v247, v158, v159\n"
"v_max3_u32 v247, v247, v160, v161\n"
"v_max3_u32 v247, v247, v162, v163\n"
"v_max3_u32 v247, v247, v164, v165\n"
"v_max3_u32 v247, v247, v166, v167\n"
"v_max3_u32 v247, v247, v168, v169\n"
"v_max3_u32 v247, v247, v170, v171\n"
"v_cmp_eq_u32_e32 vcc, -1, v247\n"
"s_cbranch_vccz .Lmk_p77k\n"
"s_sub_u32 s1, s1, 1\n"
"s_cbranch_scc0 .Lmk_p77l\n"
".Lmk_p77k:\n"
"v_mfma_f32_16x16x32_bf16 v[68:71], a[0:3], v[128:131], 0\n"
"v_mfma_f32_16x16x32_bf16 v[68:71], a[4:7], v[144:147], v[68:71]\n"
"v_mfma_f32_16x16x32_bf16 v[68:71], a[8:11], v[148:151], v[68:71]\n"
"v_mfma_f32_16x16x32_bf16 v[68:71], a[12:15], v[152:155], v[68:71]\n"
"v_mfma_f32_16x16x32_bf16 v[68:71], a[16:19], v[156:159], v[68:71]\n"
"v_mfma_f32_16x16x32_bf16 v[68:71], a[20:23], v[160:163], v[68:71]\n"
"v_mfma_f32_16x16x32_bf16 v[68:71], a[24:27], v[164:167], v[68:71]\n"
"v_mfma_f32_16x16x32_bf16 v[68:71], a[28:31], v[168:171], v[68:71]\n"
"v_mfma_f32_16x16x32_bf16 v[88:91], a[60:63], v[168:171], 0\n"
"v_mfma_f32_16x16x32_bf16 v[88:91], a[56:59], v[164:167], v[88:91]\n"
"v_mfma_f32_16x16x32_bf16 v[88:91], a[52:55], v[160:163], v[88:91]\n"
"v_mfma_f32_16x16x32_bf16 v[88:91], a[48:51], v[156:159], v[88:91]\n"
"v_mfma_f32_16x16x32_bf16 v[88:91], a[44:47], v[152:155], v[88:91]\n"
"v_mfma_f32_16x16x32_bf16 v[88:91], a[40:43], v[148:151], v[88:91]\n"
"s_nop 7\n"
"s_branch .Lmk_p77r\n"
".Lmk_m80s:\n"
";\n"
"s_waitcnt vmcnt(16)\n"
"s_mov_b32 s1, 0x10000\n"
".Lmk_m80l:\n"
"s_add_u32 s98, s62, 0x4000\n"
"buffer_load_dword v255, v10, s[4:7], s98 offen sc1\n"
"buffer_load_dword v254, v10, s[4:7], s98 offen offset:256 sc1\n"
"buffer_load_dword v253, v10, s[4:7], s98 offen offset:512 sc1\n"
"buffer_load_dword v252, v10, s[4:7], s98 offen offset:768 sc1\n"
"buffer_load_dword v251, v10, s[4:7], s98 offen offset:1024 sc1\n"
"buffer_load_dword v250, v10, s[4:7], s98 offen offset:1280 sc1\n"
"buffer_load_dword v249, v10, s[4:7], s98 offen offset:1536 sc1\n"
"buffer_load_dword v248, v10, s[4:7], s98 offen offset:1792 sc1\n"
"s_waitcnt vmcnt(0)\n"
"v_max_u32_e32 v247, v255, v254\n"
"v_max3_u32 v247, v247, v253, v252\n"
"v_max3_u32 v247, v247, v251, v250\n"
"v_max3_u32 v247, v247, v249, v248\n"
"v_cmp_eq_u32_e32 vcc, -1, v247\n"
"s_cbranch_vccz .Lmk_m80k\n"
"s_sub_u32 s1, s1, 1\n"
"s_cbranch_scc0 .Lmk_m80l\n"
".Lmk_m80k:\n"
"v_add_f32_e32 v255, v255, v251\n"
"v_add_f32_e32 v254, v254, v250\n"
"v_add_f32_e32 v253, v253, v249\n"
"v_add_f32_e32 v252, v252, v248\n"
"v_add_f32_e32 v255, v255, v253\n"
"v_add_f32_e32 v254, v254, v252\n"
"v_add_f32_e32 v255, v255, v254\n"
"s_nop 1\n"
"s_branch .Lmk_m80r\n"
".Lmk_p78s:\n"
";\n"
"s_waitcnt vmcnt(16)\n"
"s_mov_b32 s1, 0x10000\n"
".Lmk_p78l:\n"
"s_add_u32 s97, s54, 0x40000\n"
"buffer_load_dwordx4 v[176:179], v0, s[4:7], s97 offen sc1\n"
"buffer_load_dwordx4 v[48:51], v0, s[4:7], s97 offen offset:1024 sc1\n"
"buffer_load_dwordx4 v[132:135], v0, s[4:7], s97 offen offset:2048 sc1\n"
"buffer_load_dwordx4 v[184:187], v0, s[4:7], s97 offen offset:3072 sc1\n"
"buffer_load_dwordx4 v[44:47], v1, s[4:7], s97 offen sc1\n"
"buffer_load_dwordx4 v[192:195], v1, s[4:7], s97 offen offset:1024 sc1\n"
"buffer_load_dwordx4 v[196:199], v1, s[4:7], s97 offen offset:2048 sc1\n"
"buffer_load_dwordx4 v[200:203], v1, s[4:7], s97 offen offset:3072 sc1\n"
"s_add_u32 s97, s55, 0x40000\n"
"buffer_load_dwordx4 v[80:83], v0, s[4:7], s97 offen sc1\n"
"buffer_load_dwordx4 v[84:87], v0, s[4:7], s97 offen offset:1024 sc1\n"
"buffer_load_dwordx4 v[92:95], v0, s[4:7], s97 offen offset:2048 sc1\n"
"buffer_load_dwordx4 v[96:99], v0, s[4:7], s97 offen offset:3072 sc1\n"
"buffer_load_dwordx4 v[88:91], v1, s[4:7], s97 offen sc1\n"
"buffer_load_dwordx4 v[116:119], v1, s[4:7], s97 offen offset:1024 sc1\n"
"buffer_load_dwordx4 v[124:127], v1, s[4:7], s97 offen offset:2048 sc1\n"
"buffer_load_dwordx4 v[172:175], v1, s[4:7], s97 offen offset:3072 sc1\n"
"s_add_u32 s97, s52, 0x48000\n"
"buffer_load_dwordx4 v[128:131], v0, s[4:7], s97 offen sc1\n"
"s_waitcnt vmcnt(0)\n"
"v_max_u32_e32 v252, v176, v177\n"
"v_max3_u32 v252, v252, v178, v179\n"
"v_max3_u32 v252, v252, v48, v49\n"
"v_max3_u32 v252, v252, v50, v51\n"
"v_max3_u32 v252, v252, v132, v133\n"
"v_max3_u32 v252, v252, v134, v135\n"
"v_max3_u32 v252, v252, v184, v185\n"
"v_max3_u32 v252, v252, v186, v187\n"
"v_max3_u32 v252, v252, v44, v45\n"
"v_max3_u32 v252, v252, v46, v47\n"
"v_max3_u32 v252, v252, v192, v193\n"
"v_max3_u32 v252, v252, v194, v195\n"
"v_max3_u32 v252, v252, v196, v197\n"
"v_max3_u32 v252, v252, v198, v199\n"
"v_max3_u32 v252, v252, v200, v201\n"
"v_max3_u32 v252, v252, v202, v203\n"
"v_cmp_eq_u32_e32 vcc, -1, v252\n"
"s_cbranch_vccz .Lmk_p78k\n"
"s_sub_u32 s1, s1, 1\n"
"s_cbranch_scc0 .Lmk_p78l\n"
".Lmk_p78k:\n"
"v_mfma_f32_16x16x32_bf16 v[68:71], a[0:3], v[176:179], 0\n"
"v_mfma_f32_16x16x32_bf16 v[68:71], a[4:7], v[48:51], v[68:71]\n"
"v_mfma_f32_16x16x32_bf16 v[68:71], a[8:11], v[132:135], v[68:71]\n"
"v_mfma_f32_16x16x32_bf16 v[68:71], a[12:15], v[184:187], v[68:71]\n"
"v_mfma_f32_16x16x32_bf16 v[68:71], a[16:19], v[44:47], v[68:71]\n"
"v_mfma_f32_16x16x32_bf16 v[68:71], a[20:23], v[192:195], v[68:71]\n"
"v_mfma_f32_16x16x32_bf16 v[68:71], a[24:27], v[196:199], v[68:71]\n"
"v_mfma_f32_16x16x32_bf16 v[68:71], a[28:31], v[200:203], v[68:71]\n"
"v_mfma_f32_16x16x32_bf16 v[104:107], a[60:63], v[200:203], 0\n"
"v_mfma_f32_16x16x32_bf16 v[104:107], a[56:59], v[196:199], v[104:107]\n"
"v_mfma_f32_16x16x32_bf16 v[104:107], a[52:55], v[192:195], v[104:107]\n"
"v_mfma_f32_16x16x32_bf16 v[104:107], a[48:51], v[44:47], v[104:107]\n"
"v_mfma_f32_16x16x32_bf16 v[104:107], a[44:47], v[184:187], v[104:107]\n"
"v_mfma_f32_16x16x32_bf16 v[104:107], a[40:43], v[132:135], v[104:107]\n"
"s_nop 7\n"
"s_branch .Lmk_p78r\n"
".Lmk_p79s:\n"
";\n"
"s_waitcnt vmcnt(16)\n"
"s_mov_b32 s1, 0x10000\n"
".Lmk_p79l:\n"
"s_add_u32 s97, s55, 0x40000\n"
"buffer_load_dwordx4 v[80:83], v0, s[4:7], s97 offen sc1\n"
"buffer_load_dwordx4 v[84:87], v0, s[4:7], s97 offen offset:1024 sc1\n"
"buffer_load_dwordx4 v[92:95], v0, s[4:7], s97 offen offset:2048 sc1\n"
"buffer_load_dwordx4 v[96:99], v0, s[4:7], s97 offen offset:3072 sc1\n"
"buffer_load_dwordx4 v[88:91], v1, s[4:7], s97 offen sc1\n"
"buffer_load_dwordx4 v[116:119], v1, s[4:7], s97 offen offset:1024 sc1\n"
"buffer_load_dwordx4 v[124:127], v1, s[4:7], s97 offen offset:2048 sc1\n"
"buffer_load_dwordx4 v[172:175], v1, s[4:7], s97 offen offset:3072 sc1\n"
"s_add_u32 s97, s52, 0x48000\n"
"buffer_load_dwordx4 v[128:131], v0, s[4:7], s97 offen sc1\n"
"buffer_load_dwordx4 v[68:71], v0, s[4:7], s97 offen offset:1024 sc1\n"
"buffer_load_dwordx4 v[168:171], v0, s[4:7], s97 offen offset:2048 sc1\n"
"buffer_load_dwordx4 v[152:155], v0, s[4:7], s97 offen offset:3072 sc1\n"
"buffer_load_dwordx4 v[148:151], v1, s[4:7], s97 offen sc1\n"
"buffer_load_dwordx4 v[164:167], v1, s[4:7], s97 offen offset:1024 sc1\n"
"buffer_load_dwordx4 v[180:183], v1, s[4:7], s97 offen offset:2048 sc1\n"
"buffer_load_dwordx4 v[132:135], v1, s[4:7], s97 offen offset:3072 sc1\n"
"s_add_u32 s97, s53, 0x48000\n"
"buffer_load_dwordx4 v[200:203], v0, s[4:7], s97 offen sc1\n"
"buffer_load_dwordx4 v[104:107], v0, s[4:7], s97 offen offset:1024 sc1\n"
"s_waitcnt vmcnt(0)\n"
"v_max_u32_e32 v252, v80, v81\n"
"v_max3_u32 v252, v252, v82, v83\n"
"v_max3_u32 v252, v252, v84, v85\n"
"v_max3_u32 v252, v252, v86, v87\n"
"v_max3_u32 v252, v252, v92, v93\n"
"v_max3_u32 v252, v252, v94, v95\n"
"v_max3_u32 v252, v252, v96, v97\n"
"v_max3_u32 v252, v252, v98, v99\n"
"v_max3_u32 v252, v252, v88, v89\n"
"v_max3_u32 v252, v252, v90, v91\n"
"v_max3_u32 v252, v252, v116, v117\n"
"v_max3_u32 v252, v252, v118, v119\n"
"v_max3_u32 v252, v252, v124, v125\n"
"v_max3_u32 v252, v252, v126, v127\n"
"v_max3_u32 v252, v252, v172, v173\n"
"v_max3_u32 v252, v252, v174, v175\n"
"v_cmp_eq_u32_e32 vcc, -1, v252\n"
"s_cbranch_vccz .Lmk_p79k\n"
"s_sub_u32 s1, s1, 1\n"
"s_cbranch_scc0 .Lmk_p79l\n"
".Lmk_p79k:\n"
"v_mfma_f32_16x16x32_bf16 v[176:179], a[0:3], v[80:83], v[144:147]\n"
"v_mfma_f32_16x16x32_bf16 v[176:179], a[4:7], v[84:87], v[176:179]\n"
"v_mfma_f32_16x16x32_bf16 v[176:179], a[8:11], v[92:95], v[176:179]\n"
"v_mfma_f32_16x16x32_bf16 v[176:179], a[12:15], v[96:99], v[176:179]\n"
"v_mfma_f32_16x16x32_bf16 v[176:179], a[16:19], v[88:91], v[176:179]\n"
"v_mfma_f32_16x16x32_bf16 v[176:179], a[20:23], v[116:119], v[176:179]\n"
"v_mfma_f32_16x16x32_bf16 v[176:179], a[24:27], v[124:127], v[176:179]\n"
"v_mfma_f32_16x16x32_bf16 v[176:179], a[28:31], v[172:175], v[176:179]\n"
"v_mfma_f32_16x16x32_bf16 v[204:207], a[60:63], v[172:175], v[160:163]\n"
"v_mfma_f32_16x16x32_bf16 v[204:207], a[56:59], v[124:127], v[204:207]\n"
"v_mfma_f32_16x16x32_bf16 v[204:207], a[52:55], v[116:119], v[204:207]\n"
"v_mfma_f32_16x16x32_bf16 v[204:207], a[48:51], v[88:91], v[204:207]\n"
"v_mfma_f32_16x16x32_bf16 v[204:207], a[44:47], v[96:99], v[204:207]\n"
"v_mfma_f32_16x16x32_bf16 v[204:207], a[40:43], v[92:95], v[204:207]\n"
"s_nop 7\n"
"s_branch .Lmk_p79r\n"
".Lmk_p81s:\n"
";\n"
"s_waitcnt vmcnt(16)\n"
"s_mov_b32 s1, 0x10000\n"
".Lmk_p81l:\n"
"s_add_u32 s95, s52, 0x48000\n"
"buffer_load_dwordx4 v[128:131], v0, s[4:7], s95 offen sc1\n"
"buffer_load_dwordx4 v[68:71], v0, s[4:7], s95 offen offset:1024 sc1\n"
"buffer_load_dwordx4 v[168:171], v0, s[4:7], s95 offen offset:2048 sc1\n"
"buffer_load_dwordx4 v[152:155], v0, s[4:7], s95 offen offset:3072 sc1\n"
"buffer_load_dwordx4 v[148:151], v1, s[4:7], s95 offen sc1\n"
"buffer_load_dwordx4 v[164:167], v1, s[4:7], s95 offen offset:1024 sc1\n"
"buffer_load_dwordx4 v[180:183], v1, s[4:7], s95 offen offset:2048 sc1\n"
"buffer_load_dwordx4 v[132:135], v1, s[4:7], s95 offen offset:3072 sc1\n"
"s_add_u32 s95, s53, 0x48000\n"
"buffer_load_dwordx4 v[200:203], v0, s[4:7], s95 offen sc1\n"
"buffer_load_dwordx4 v[104:107], v0, s[4:7], s95 offen offset:1024 sc1\n"
"buffer_load_dwordx4 v[144:147], v0, s[4:7], s95 offen offset:2048 sc1\n"
"buffer_load_dwordx4 v[44:47], v0, s[4:7], s95 offen offset:3072 sc1\n"
"buffer_load_dwordx4 v[156:159], v1, s[4:7], s95 offen sc1\n"
"buffer_load_dwordx4 v[184:187], v1, s[4:7], s95 offen offset:1024 sc1\n"
"buffer_load_dwordx4 v[208:211], v1, s[4:7], s95 offen offset:2048 sc1\n"
"buffer_load_dwordx4 v[176:179], v1, s[4:7], s95 offen offset:3072 sc1\n"
"s_add_u32 s95, s54, 0x48000\n"
"buffer_load_dwordx4 v[84:87], v0, s[4:7], s95 offen sc1\n"
"buffer_load_dwordx4 v[88:91], v0, s[4:7], s95 offen offset:1024 sc1\n"
"s_waitcnt vmcnt(0)\n"
"v_max_u32_e32 v252, v128, v129\n"
"v_max3_u32 v252, v252, v130, v131\n"
"v_max3_u32 v252, v252, v68, v69\n"
"v_max3_u32 v252, v252, v70, v71\n"
"v_max3_u32 v252, v252, v168, v169\n"
"v_max3_u32 v252, v252, v170, v171\n"
"v_max3_u32 v252, v252, v152, v153\n"
"v_max3_u32 v252, v252, v154, v155\n"
"v_max3_u32 v252, v252, v148, v149\n"
"v_max3_u32 v252, v252, v150, v151\n"
"v_max3_u32 v252, v252, v164, v165\n"
"v_max3_u32 v252, v252, v166, v167\n"
"v_max3_u32 v252, v252, v180, v181\n"
"v_max3_u32 v252, v252, v182, v183\n"
"v_max3_u32 v252, v252, v132, v133\n"
"v_max3_u32 v252, v252, v134, v135\n"
"v_cmp_eq_u32_e32 vcc, -1, v252\n"
"s_cbranch_vccz .Lmk_p81k\n"
"s_sub_u32 s1, s1, 1\n"
"s_cbranch_scc0 .Lmk_p81l\n"
".Lmk_p81k:\n"
"v_mfma_f32_16x16x32_bf16 v[80:83], a[0:3], v[128:131], 0\n"
"v_mfma_f32_16x16x32_bf16 v[80:83], a[4:7], v[68:71], v[80:83]\n"
"v_mfma_f32_16x16x32_bf16 v[80:83], a[8:11], v[168:171], v[80:83]\n"
"v_mfma_f32_16x16x32_bf16 v[80:83], a[12:15], v[152:155], v[80:83]\n"
"v_mfma_f32_16x16x32_bf16 v[80:83], a[16:19], v[148:151], v[80:83]\n"
"v_mfma_f32_16x16x32_bf16 v[80:83], a[20:23], v[164:167], v[80:83]\n"
"v_mfma_f32_16x16x32_bf16 v[80:83], a[24:27], v[180:183], v[80:83]\n"
"v_mfma_f32_16x16x32_bf16 v[80:83], a[28:31], v[132:135], v[80:83]\n"
"v_mfma_f32_16x16x32_bf16 v[92:95], a[60:63], v[132:135], 0\n"
"v_mfma_f32_16x16x32_bf16 v[92:95], a[56:59], v[180:183], v[92:95]\n"
"v_mfma_f32_16x16x32_bf16 v[92:95], a[52:55], v[164:167], v[92:95]\n"
"v_mfma_f32_16x16x32_bf16 v[92:95], a[48:51], v[148:151], v[92:95]\n"
"v_mfma_f32_16x16x32_bf16 v[92:95], a[44:47], v[152:155], v[92:95]\n"
"v_mfma_f32_16x16x32_bf16 v[92:95], a[40:43], v[168:171], v[92:95]\n"
"s_nop 7\n"
"s_branch .Lmk_p81r\n"
".Lmk_p82s:\n"
";\n"
"s_waitcnt vmcnt(16)\n"
"s_mov_b32 s1, 0x10000\n"
".Lmk_p82l:\n"
"s_add_u32 s95, s53, 0x48000\n"
"buffer_load_dwordx4 v[200:203], v0, s[4:7], s95 offen sc1\n"
"buffer_load_dwordx4 v[104:107], v0, s[4:7], s95 offen offset:1024 sc1\n"
"buffer_load_dwordx4 v[144:147], v0, s[4:7], s95 offen offset:2048 sc1\n"
"buffer_load_dwordx4 v[44:47], v0, s[4:7], s95 offen offset:3072 sc1\n"
"buffer_load_dwordx4 v[156:159], v1, s[4:7], s95 offen sc1\n"
"buffer_load_dwordx4 v[184:187], v1, s[4:7], s95 offen offset:1024 sc1\n"
"buffer_load_dwordx4 v[208:211], v1, s[4:7], s95 offen offset:2048 sc1\n"
"buffer_load_dwordx4 v[176:179], v1, s[4:7], s95 offen offset:3072 sc1\n"
"s_add_u32 s95, s54, 0x48000\n"
"buffer_load_dwordx4 v[84:87], v0, s[4:7], s95 offen sc1\n"
"buffer_load_dwordx4 v[88:91], v0, s[4:7], s95 offen offset:1024 sc1\n"
"buffer_load_dwordx4 v[96:99], v0, s[4:7], s95 offen offset:2048 sc1\n"
"buffer_load_dwordx4 v[116:119], v0, s[4:7], s95 offen offset:3072 sc1\n"
"buffer_load_dwordx4 v[92:95], v1, s[4:7], s95 offen sc1\n"
"buffer_load_dwordx4 v[172:175], v1, s[4:7], s95 offen offset:1024 sc1\n"
"buffer_load_dwordx4 v[80:83], v1, s[4:7], s95 offen offset:2048 sc1\n"
"buffer_load_dwordx4 v[160:163], v1, s[4:7], s95 offen offset:3072 sc1\n"
"s_add_u32 s95, s55, 0x48000\n"
"buffer_load_dwordx4 v[68:71], v0, s[4:7], s95 offen sc1\n"
"buffer_load_dwordx4 v[124:127], v0, s[4:7], s95 offen offset:1024 sc1\n"
"s_waitcnt vmcnt(0)\n"
"v_max_u32_e32 v247, v200, v201\n"
"v_max3_u32 v247, v247, v202, v203\n"
"v_max3_u32 v247, v247, v104, v105\n"
"v_max3_u32 v247, v247, v106, v107\n"
"v_max3_u32 v247, v247, v144, v145\n"
"v_max3_u32 v247, v247, v146, v147\n"
"v_max3_u32 v247, v247, v44, v45\n"
"v_max3_u32 v247, v247, v46, v47\n"
"v_max3_u32 v247, v247, v156, v157\n"
"v_max3_u32 v247, v247, v158, v159\n"
"v_max3_u32 v247, v247, v184, v185\n"
"v_max3_u32 v247, v247, v186, v187\n"
"v_max3_u32 v247, v247, v208, v209\n"
"v_max3_u32 v247, v247, v210, v211\n"
"v_max3_u32 v247, v247, v176, v177\n"
"v_max3_u32 v247, v247, v178, v179\n"
"v_cmp_eq_u32_e32 vcc, -1, v247\n"
"s_cbranch_vccz .Lmk_p82k\n"
"s_sub_u32 s1, s1, 1\n"
"s_cbranch_scc0 .Lmk_p82l\n"
".Lmk_p82k:\n"
"v_mfma_f32_16x16x32_bf16 v[48:51], a[0:3], v[200:203], 0\n"
"v_mfma_f32_16x16x32_bf16 v[48:51], a[4:7], v[104:107], v[48:51]\n"
"v_mfma_f32_16x16x32_bf16 v[48:51], a[8:11], v[144:147], v[48:51]\n"
"v_mfma_f32_16x16x32_bf16 v[48:51], a[12:15], v[44:47], v[48:51]\n"
"v_mfma_f32_16x16x32_bf16 v[48:51], a[16:19], v[156:159], v[48:51]\n"
"v_mfma_f32_16x16x32_bf16 v[48:51], a[20:23], v[184:187], v[48:51]\n"
"v_mfma_f32_16x16x32_bf16 v[48:51], a[24:27], v[208:211], v[48:51]\n"
"v_mfma_f32_16x16x32_bf16 v[48:51], a[28:31], v[176:179], v[48:51]\n"
"v_mfma_f32_16x16x32_bf16 v[128:131], a[60:63], v[176:179], 0\n"
"v_mfma_f32_16x16x32_bf16 v[128:131], a[56:59], v[208:211], v[128:131]\n"
"v_mfma_f32_16x16x32_bf16 v[128:131], a[52:55], v[184:187], v[128:131]\n"
"v_mfma_f32_16x16x32_bf16 v[128:131], a[48:51], v[156:159], v[128:131]\n"
"v_mfma_f32_16x16x32_bf16 v[128:131], a[44:47], v[44:47], v[128:131]\n"
"v_mfma_f32_16x16x32_bf16 v[128:131], a[40:43], v[144:147], v[128:131]\n"
"s_nop 7\n"
"s_branch .Lmk_p82r\n"
".Lmk_m85s:\n"
";\n"
"s_waitcnt vmcnt(16)\n"
"s_mov_b32 s1, 0x10000\n"
".Lmk_m85l:\n"
"s_add_u32 s98, s62, 0x4800\n"
"buffer_load_dword v255, v10, s[4:7], s98 offen sc1\n"
"buffer_load_dword v253, v10, s[4:7], s98 offen offset:256 sc1\n"
"buffer_load_dword v252, v10, s[4:7], s98 offen offset:512 sc1\n"
"buffer_load_dword v251, v10, s[4:7], s98 offen offset:768 sc1\n"
"buffer_load_dword v250, v10, s[4:7], s98 offen offset:1024 sc1\n"
"buffer_load_dword v254, v10, s[4:7], s98 offen offset:1280 sc1\n"
"buffer_load_dword v249, v10, s[4:7], s98 offen offset:1536 sc1\n"
"buffer_load_dword v248, v10, s[4:7], s98 offen offset:1792 sc1\n"
"s_waitcnt vmcnt(0)\n"
"v_max_u32_e32 v245, v255, v253\n"
"v_max3_u32 v245, v245, v252, v251\n"
"v_max3_u32 v245, v245, v250, v254\n"
"v_max3_u32 v245, v245, v249, v248\n"
"v_cmp_eq_u32_e32 vcc, -1, v245\n"
"s_cbranch_vccz .Lmk_m85k\n"
"s_sub_u32 s1, s1, 1\n"
"s_cbranch_scc0 .Lmk_m85l\n"
".Lmk_m85k:\n"
"v_add_f32_e32 v255, v255, v250\n"
"v_add_f32_e32 v253, v253, v254\n"
"v_add_f32_e32 v252, v252, v249\n"
"v_add_f32_e32 v251, v251, v248\n"
"v_add_f32_e32 v255, v255, v252\n"
"v_add_f32_e32 v253, v253, v251\n"
"v_add_f32_e32 v255, v255, v253\n"
"s_nop 1\n"
"s_branch .Lmk_m85r\n"
".Lmk_f72s:\n"
";\n"
"s_mov_b32 s1, 0x10000\n"
".Lmk_f72l:\n"
"buffer_load_dwordx2 v[246:247], v12, s[4:7], s97 offen sc1\n"
"s_waitcnt vmcnt(0)\n"
"v_cmp_ne_u64_e32 vcc, v[246:247], v[18:19]\n"
"s_cbranch_vccz .Lmk_f72r\n"
"s_sub_u32 s1, s1, 1\n"
"s_cbranch_scc0 .Lmk_f72l\n"
"s_branch .Lmk_f72r\n"
".Lmk_p83s:\n"
";\n"
"s_waitcnt vmcnt(16)\n"
"s_mov_b32 s1, 0x10000\n"
".Lmk_p83l:\n"
"s_add_u32 s97, s54, 0x48000\n"
"buffer_load_dwordx4 v[84:87], v0, s[4:7], s97 offen sc1\n"
"buffer_load_dwordx4 v[88:91], v0, s[4:7], s97 offen offset:1024 sc1\n"
"buffer_load_dwordx4 v[96:99], v0, s[4:7], s97 offen offset:2048 sc1\n"
"buffer_load_dwordx4 v[116:119], v0, s[4:7], s97 offen offset:3072 sc1\n"
"buffer_load_dwordx4 v[92:95], v1, s[4:7], s97 offen sc1\n"
"buffer_load_dwordx4 v[172:175], v1, s[4:7], s97 offen offset:1024 sc1\n"
"buffer_load_dwordx4 v[80:83], v1, s[4:7], s97 offen offset:2048 sc1\n"
"buffer_load_dwordx4 v[160:163], v1, s[4:7], s97 offen offset:3072 sc1\n"
"s_add_u32 s97, s55, 0x48000\n"
"buffer_load_dwordx4 v[68:71], v0, s[4:7], s97 offen sc1\n"
"buffer_load_dwordx4 v[124:127], v0, s[4:7], s97 offen offset:1024 sc1\n"
"buffer_load_dwordx4 v[132:135], v0, s[4:7], s97 offen offset:2048 sc1\n"
"buffer_load_dwordx4 v[148:151], v0, s[4:7], s97 offen offset:3072 sc1\n"
"buffer_load_dwordx4 v[128:131], v1, s[4:7], s97 offen sc1\n"
"buffer_load_dwordx4 v[164:167], v1, s[4:7], s97 offen offset:1024 sc1\n"
"buffer_load_dwordx4 v[168:171], v1, s[4:7], s97 offen offset:2048 sc1\n"
"buffer_load_dwordx4 v[180:183], v1, s[4:7], s97 offen offset:3072 sc1\n"
"s_waitcnt vmcnt(0)\n"
"v_max_u32_e32 v255, v84, v85\n"
"v_max3_u32 v255, v255, v86, v87\n"
"v_max3_u32 v255, v255, v88, v89\n"
"v_max3_u32 v255, v255, v90, v91\n"
"v_max3_u32 v255, v255, v96, v97\n"
"v_max3_u32 v255, v255, v98, v99\n"
"v_max3_u32 v255, v255, v116, v117\n"
"v_max3_u32 v255, v255, v118, v119\n"
"v_max3_u32 v255, v255, v92, v93\n"
"v_max3_u32 v255, v255, v94, v95\n"
"v_max3_u32 v255, v255, v172, v173\n"
"v_max3_u32 v255, v255, v174, v175\n"
"v_max3_u32 v255, v255, v80, v81\n"
"v_max3_u32 v255, v255, v82, v83\n"
"v_max3_u32 v255, v255, v160, v161\n"
"v_max3_u32 v255, v255, v162, v163\n"
"v_cmp_eq_u32_e32 vcc, -1, v255\n"
"s_cbranch_vccz .Lmk_p83k\n"
"s_sub_u32 s1, s1, 1\n"
"s_cbranch_scc0 .Lmk_p83l\n"
".Lmk_p83k:\n"
"v_mfma_f32_16x16x32_bf16 v[44:47], a[0:3], v[84:87], 0\n"
"v_mfma_f32_16x16x32_bf16 v[44:47], a[4:7], v[88:91], v[44:47]\n"
"v_mfma_f32_16x16x32_bf16 v[44:47], a[8:11], v[96:99], v[44:47]\n"
"v_mfma_f32_16x16x32_bf16 v[44:47], a[12:15], v[116:119], v[44:47]\n"
"v_mfma_f32_16x16x32_bf16 v[44:47], a[16:19], v[92:95], v[44:47]\n"
"v_mfma_f32_16x16x32_bf16 v[44:47], a[20:23], v[172:175], v[44:47]\n"
"v_mfma_f32_16x16x32_bf16 v[44:47], a[24:27], v[80:83], v[44:47]\n"
"v_mfma_f32_16x16x32_bf16 v[44:47], a[28:31], v[160:163], v[44:47]\n"
"v_mfma_f32_16x16x32_bf16 v[48:51], a[60:63], v[160:163], 0\n"
"v_mfma_f32_16x16x32_bf16 v[48:51], a[56:59], v[80:83], v[48:51]\n"
"v_mfma_f32_16x16x32_bf16 v[48:51], a[52:55], v[172:175], v[48:51]\n"
"v_mfma_f32_16x16x32_bf16 v[48:51], a[48:51], v[92:95], v[48:51]\n"
"v_mfma_f32_16x16x32_bf16 v[48:51], a[44:47], v[116:119], v[48:51]\n"
"v_mfma_f32_16x16x32_bf16 v[48:51], a[40:43], v[96:99], v[48:51]\n"
"s_nop 7\n"
"s_branch .Lmk_p83r\n"
".Lmk_p84s:\n"
";\n"
"s_waitcnt vmcnt(16)\n"
"s_mov_b32 s1, 0x10000\n"
".Lmk_p84l:\n"
"s_add_u32 s97, s55, 0x48000\n"
"buffer_load_dwordx4 v[68:71], v0, s[4:7], s97 offen sc1\n"
"buffer_load_dwordx4 v[124:127], v0, s[4:7], s97 offen offset:1024 sc1\n"
"buffer_load_dwordx4 v[132:135], v0, s[4:7], s97 offen offset:2048 sc1\n"
"buffer_load_dwordx4 v[148:151], v0, s[4:7], s97 offen offset:3072 sc1\n"
"buffer_load_dwordx4 v[128:131], v1, s[4:7], s97 offen sc1\n"
"buffer_load_dwordx4 v[164:167], v1, s[4:7], s97 offen offset:1024 sc1\n"
"buffer_load_dwordx4 v[168:171], v1, s[4:7], s97 offen offset:2048 sc1\n"
"buffer_load_dwordx4 v[180:183], v1, s[4:7], s97 offen offset:3072 sc1\n"
"s_waitcnt vmcnt(0)\n"
"v_max_u32_e32 v252, v68, v69\n"
"v_max3_u32 v252, v252, v70, v71\n"
"v_max3_u32 v252, v252, v124, v125\n"
"v_max3_u32 v252, v252, v126, v127\n"
"v_max3_u32 v252, v252, v132, v133\n"
"v_max3_u32 v252, v252, v134, v135\n"
"v_max3_u32 v252, v252, v148, v149\n"
"v_max3_u32 v252, v252, v150, v151\n"
"v_max3_u32 v252, v252, v128, v129\n"
"v_max3_u32 v252, v252, v130, v131\n"
"v_max3_u32 v252, v252, v164, v165\n"
"v_max3_u32 v252, v252, v166, v167\n"
"v_max3_u32 v252, v252, v168, v169\n"
"v_max3_u32 v252, v252, v170, v171\n"
"v_max3_u32 v252, v252, v180, v181\n"
"v_max3_u32 v252, v252, v182, v183\n"
"v_cmp_eq_u32_e32 vcc, -1, v252\n"
"s_cbranch_vccz .Lmk_p84k\n"
"s_sub_u32 s1, s1, 1\n"
"s_cbranch_scc0 .Lmk_p84l\n"
".Lmk_p84k:\n"
"v_mfma_f32_16x16x32_bf16 v[96:99], a[0:3], v[68:71], v[104:107]\n"
"v_mfma_f32_16x16x32_bf16 v[96:99], a[4:7], v[124:127], v[96:99]\n"
"v_mfma_f32_16x16x32_bf16 v[96:99], a[8:11], v[132:135], v[96:99]\n"
"v_mfma_f32_16x16x32_bf16 v[96:99], a[12:15], v[148:151], v[96:99]\n"
"v_mfma_f32_16x16x32_bf16 v[96:99], a[16:19], v[128:131], v[96:99]\n"
"v_mfma_f32_16x16x32_bf16 v[96:99], a[20:23], v[164:167], v[96:99]\n"
"v_mfma_f32_16x16x32_bf16 v[96:99], a[24:27], v[168:171], v[96:99]\n"
"v_mfma_f32_16x16x32_bf16 v[96:99], a[28:31], v[180:183], v[96:99]\n"
"v_mfma_f32_16x16x32_bf16 v[200:203], a[60:63], v[180:183], v[176:179]\n"
"v_mfma_f32_16x16x32_bf16 v[200:203], a[56:59], v[168:171], v[200:203]\n"
"v_mfma_f32_16x16x32_bf16 v[200:203], a[52:55], v[164:167], v[200:203]\n"
"v_mfma_f32_16x16x32_bf16 v[200:203], a[48:51], v[128:131], v[200:203]\n"
"v_mfma_f32_16x16x32_bf16 v[200:203], a[44:47], v[148:151], v[200:203]\n"
"v_mfma_f32_16x16x32_bf16 v[200:203], a[40:43], v[132:135], v[200:203]\n"
"s_nop 7\n"
"s_branch .Lmk_p84r\n"
".Lmk_f73s:\n"
";\n"
"s_mov_b32 s1, 0x10000\n"
".Lmk_f73l:\n"
"buffer_load_dwordx2 v[250:251], off, s[4:7], s98 sc1\n"
"s_waitcnt vmcnt(0)\n"
"v_cmp_ne_u64_e32 vcc, v[250:251], v[18:19]\n"
"s_cbranch_vccz .Lmk_f73r\n"
"s_sub_u32 s1, s1, 1\n"
"s_cbranch_scc0 .Lmk_f73l\n"
"s_branch .Lmk_f73r\n"
".Lmk_x94s:\n"
"s_nop 3\n"
"v_mov_b32_e32 v36, 0xff800000\n"
"v_mov_b32_e32 v37, 0xff800000\n"
"v_mov_b32_e32 v38, 0xff800000\n"
"v_mov_b32_e32 v39, 0xff800000\n"
"v_mov_b32_e32 v60, 0xff800000\n"
"v_mov_b32_e32 v61, 0xff800000\n"
"v_mov_b32_e32 v62, 0xff800000\n"
"v_mov_b32_e32 v63, 0xff800000\n"
"s_nop 1\n"
"s_branch .Lmk_x94r\n"
".Lmk_x95s:\n"
"s_nop 3\n"
"v_mov_b32_e32 v36, 0xff800000\n"
"v_mov_b32_e32 v37, 0xff800000\n"
"v_mov_b32_e32 v38, 0xff800000\n"
"v_mov_b32_e32 v39, 0xff800000\n"
"v_mov_b32_e32 v60, 0xff800000\n"
"v_mov_b32_e32 v61, 0xff800000\n"
"v_mov_b32_e32 v62, 0xff800000\n"
"v_mov_b32_e32 v63, 0xff800000\n"
"s_nop 1\n"
"s_branch .Lmk_x95r\n"
".Lmk_x96s:\n"
"s_nop 3\n"
"v_mov_b32_e32 v36, 0xff800000\n"
"v_mov_b32_e32 v37, 0xff800000\n"
"v_mov_b32_e32 v38, 0xff800000\n"
"v_mov_b32_e32 v39, 0xff800000\n"
"v_mov_b32_e32 v60, 0xff800000\n"
"v_mov_b32_e32 v61, 0xff800000\n"
"v_mov_b32_e32 v62, 0xff800000\n"
"v_mov_b32_e32 v63, 0xff800000\n"
"s_nop 1\n"
"s_branch .Lmk_x96r\n"
".Lmk_x97s:\n"
"s_nop 3\n"
"v_mov_b32_e32 v36, 0xff800000\n"
"v_mov_b32_e32 v37, 0xff800000\n"
"v_mov_b32_e32 v38, 0xff800000\n"
"v_mov_b32_e32 v39, 0xff800000\n"
"v_mov_b32_e32 v60, 0xff800000\n"
"v_mov_b32_e32 v61, 0xff800000\n"
"v_mov_b32_e32 v62, 0xff800000\n"
"v_mov_b32_e32 v63, 0xff800000\n"
"s_nop 1\n"
"s_branch .Lmk_x97r\n"
".Lmk_b92:\n"
"s_cmp_lg_u32 s2, 0\n"
"s_cbranch_scc1 .Lmk_k98\n"
"s_mov_b32 s2, 1\n"
"s_waitcnt vmcnt(0) lgkmcnt(0)\n"
"s_nop 7\n"
"s_mov_b32 s97, 11\n"
"v_add_u32_e32 v248, 0x3800, v15\n"
"v_add_u32_e32 v247, 0x3800, v254\n"
"ds_read_b128 v[36:39], v15 offset:24576\n"
"ds_read_b128 v[40:43], v254 offset:24832\n"
"s_waitcnt lgkmcnt(0)\n"
"v_mfma_f32_16x16x32_bf16 v[48:51], v[36:39], v[28:31], 0\n"
"v_mfma_f32_16x16x32_bf16 v[52:55], v[36:39], v[32:35], 0\n"
"v_mfma_f32_16x16x32_bf16 v[36:39], v[40:43], v[32:35], 0\n"
"v_mfma_f32_16x16x32_bf16 v[56:59], v[40:43], v[28:31], 0\n"
".Lmk_b92l:\n"
"ds_read_b128 v[40:43], v248 offset:8192\n"
"ds_read_b128 v[60:63], v247 offset:8448\n"
"s_cmp_gt_i32 s78, s97\n"
"s_cselect_b32 s96, 0xff800000, 0\n"
"v_mov_b32_e32 v64, s96\n"
"v_mov_b32_e32 v65, s96\n"
"v_mov_b32_e32 v66, s96\n"
"v_mov_b32_e32 v67, s96\n"
"v_subrev_u32_e32 v248, 0x800, v248\n"
"v_subrev_u32_e32 v247, 0x800, v247\n"
"s_waitcnt lgkmcnt(0)\n"
"v_mfma_f32_16x16x32_bf16 v[72:75], v[40:43], v[28:31], v[64:67]\n"
"v_mfma_f32_16x16x32_bf16 v[76:79], v[40:43], v[32:35], v[64:67]\n"
"v_mfma_f32_16x16x32_bf16 v[40:43], v[60:63], v[32:35], v[64:67]\n"
"v_mfma_f32_16x16x32_bf16 v[84:87], v[60:63], v[28:31], v[64:67]\n"
"s_nop 7\n"
"s_cmp_lg_u32 s97, 4\n"
"s_cbranch_scc1 .Lmk_b92m\n"
"v_cndmask_b32_e64 v72, v48, v72, s[20:21]\n"
"v_cndmask_b32_e64 v73, v49, v73, s[22:23]\n"
"v_cndmask_b32_e64 v74, v50, v74, s[24:25]\n"
"v_cndmask_b32_e64 v75, v51, v75, s[26:27]\n"
"v_cndmask_b32_e64 v84, v56, v84, s[28:29]\n"
"v_cndmask_b32_e64 v85, v57, v85, s[30:31]\n"
"v_cndmask_b32_e64 v86, v58, v86, s[32:33]\n"
"v_cndmask_b32_e64 v87, v59, v87, s[34:35]\n"
"v_cndmask_b32_e64 v76, v52, v76, s[36:37]\n"
"v_cndmask_b32_e64 v77, v53, v77, s[38:39]\n"
"v_cndmask_b32_e64 v78, v54, v78, s[40:41]\n"
"v_cndmask_b32_e64 v79, v55, v79, s[42:43]\n"
"v_cndmask_b32_e64 v40, v36, v40, s[44:45]\n"
"v_cndmask_b32_e64 v41, v37, v41, s[46:47]\n"
"v_cndmask_b32_e64 v42, v38, v42, s[48:49]\n"
"v_cndmask_b32_e64 v43, v39, v43, s[50:51]\n"
".Lmk_b92m:\n"
"v_max3_f32 v253, v253, v72, v73\n"
"v_max3_f32 v251, v251, v76, v77\n"
"v_max3_f32 v253, v253, v74, v75\n"
"v_max3_f32 v251, v251, v78, v79\n"
"v_max3_f32 v253, v253, v84, v85\n"
"v_max3_f32 v251, v251, v40, v41\n"
"v_max3_f32 v253, v253, v86, v87\n"
"v_max3_f32 v251, v251, v42, v43\n"
"s_sub_u32 s97, s97, 1\n"
"s_cmp_ge_i32 s97, 4\n"
"s_cbranch_scc1 .Lmk_b92l\n"
"v_mov_b32_e32 v248, v253\n"
"v_mov_b32_e32 v247, v251\n"
"s_nop 0\n"
"v_permlane16_swap_b32_e32 v253, v248\n"
"v_permlane16_swap_b32_e32 v251, v247\n"
"v_max_f32_e32 v253, v253, v248\n"
"v_max_f32_e32 v251, v251, v247\n"
"v_mov_b32_e32 v248, v253\n"
"v_mov_b32_e32 v247, v251\n"
"s_nop 0\n"
"v_permlane32_swap_b32_e32 v253, v248\n"
"v_permlane32_swap_b32_e32 v251, v247\n"
"v_max_f32_e32 v253, v253, v248\n"
"v_max_f32_e32 v251, v251, v247\n"
"v_mov_b32_e32 v250, v253\n"
"v_mov_b32_e32 v249, v251\n"
"s_branch .Lmk_a91\n"
".Lmk_x101s:\n"
"s_nop 3\n"
"v_mov_b32_e32 v116, 0xff800000\n"
"v_mov_b32_e32 v117, 0xff800000\n"
"v_mov_b32_e32 v118, 0xff800000\n"
"v_mov_b32_e32 v119, 0xff800000\n"
"v_mov_b32_e32 v120, 0xff800000\n"
"v_mov_b32_e32 v121, 0xff800000\n"
"v_mov_b32_e32 v122, 0xff800000\n"
"v_mov_b32_e32 v123, 0xff800000\n"
"s_nop 1\n"
"s_branch .Lmk_x101r\n"
".Lmk_x102s:\n"
"s_nop 3\n"
"v_mov_b32_e32 v116, 0xff800000\n"
"v_mov_b32_e32 v117, 0xff800000\n"
"v_mov_b32_e32 v118, 0xff800000\n"
"v_mov_b32_e32 v119, 0xff800000\n"
"v_mov_b32_e32 v120, 0xff800000\n"
"v_mov_b32_e32 v121, 0xff800000\n"
"v_mov_b32_e32 v122, 0xff800000\n"
"v_mov_b32_e32 v123, 0xff800000\n"
"s_nop 1\n"
"s_branch .Lmk_x102r\n"
".Lmk_x103s:\n"
"s_nop 3\n"
"v_mov_b32_e32 v116, 0xff800000\n"
"v_mov_b32_e32 v117, 0xff800000\n"
"v_mov_b32_e32 v118, 0xff800000\n"
"v_mov_b32_e32 v119, 0xff800000\n"
"v_mov_b32_e32 v120, 0xff800000\n"
"v_mov_b32_e32 v121, 0xff800000\n"
"v_mov_b32_e32 v122, 0xff800000\n"
"v_mov_b32_e32 v123, 0xff800000\n"
"s_nop 1\n"
"s_branch .Lmk_x103r\n"
".Lmk_h90s:\n"
";\n"
"s_waitcnt vmcnt(16)\n"
"s_mov_b32 s1, 0x10000\n"
".Lmk_h90l:\n"
"s_add_u32 m0, s77, 0x4000\n"
"s_add_u32 s95, s65, 0\n"
"buffer_load_dwordx4 v2, s[4:7], s95 offen sc1 lds\n"
"buffer_load_dwordx4 v2, s[4:7], s95 offen offset:1024 sc1 lds\n"
"s_add_u32 m0, s77, 0xc000\n"
"s_add_u32 s95, s65, 0x4000\n"
"buffer_load_dwordx4 v2, s[4:7], s95 offen sc1 lds\n"
"buffer_load_dwordx4 v2, s[4:7], s95 offen offset:1024 sc1 lds\n"
"s_waitcnt vmcnt(0)\n"
"ds_read_b128 v[164:167], v2 offset:16384\n"
"ds_read_b128 v[76:79], v2 offset:17408\n"
"ds_read_b128 v[72:75], v2 offset:49152\n"
"ds_read_b128 v[84:87], v2 offset:50176\n"
"s_waitcnt lgkmcnt(0)\n"
"s_waitcnt vmcnt(0)\n"
"v_max_u32_e32 v248, v164, v165\n"
"v_max3_u32 v248, v248, v166, v167\n"
"v_max3_u32 v248, v248, v76, v77\n"
"v_max3_u32 v248, v248, v78, v79\n"
"v_max3_u32 v248, v248, v72, v73\n"
"v_max3_u32 v248, v248, v74, v75\n"
"v_max3_u32 v248, v248, v84, v85\n"
"v_max3_u32 v248, v248, v86, v87\n"
"v_cmp_eq_u32_e32 vcc, -1, v248\n"
"s_cbranch_vccz .Lmk_h90k\n"
"s_sub_u32 s1, s1, 1\n"
"s_cbranch_scc0 .Lmk_h90l\n"
".Lmk_h90k:\n"
"s_branch .Lmk_h90r\n"
".Lmk_f74s:\n"
";\n"
"s_mov_b32 s1, 0x10000\n"
".Lmk_f74l:\n"
"buffer_load_dwordx2 v[246:247], v12, s[4:7], s96 offen sc1\n"
"s_waitcnt vmcnt(0)\n"
"v_cmp_ne_u64_e32 vcc, v[246:247], v[18:19]\n"
"s_cbranch_vccz .Lmk_f74r\n"
"s_sub_u32 s1, s1, 1\n"
"s_cbranch_scc0 .Lmk_f74l\n"
"s_branch .Lmk_f74r\n"
".Lmk_b100:\n"
"s_cmp_lg_u32 s2, 0\n"
"s_cbranch_scc1 .Lmk_k105\n"
"s_mov_b32 s2, 1\n"
"s_waitcnt vmcnt(0) lgkmcnt(0)\n"
"s_nop 7\n"
"s_mov_b32 s96, 7\n"
"v_add_u32_e32 v248, 0x3800, v15\n"
"v_add_u32_e32 v247, 0x3800, v254\n"
"ds_read_b128 v[188:191], v15 offset:16384\n"
"ds_read_b128 v[192:195], v254 offset:16640\n"
"s_waitcnt lgkmcnt(0)\n"
"v_mfma_f32_16x16x32_bf16 v[196:199], v[188:191], v[92:95], 0\n"
"v_mfma_f32_16x16x32_bf16 v[212:215], v[188:191], v[96:99], 0\n"
"v_mfma_f32_16x16x32_bf16 v[188:191], v[192:195], v[96:99], 0\n"
"v_mfma_f32_16x16x32_bf16 v[220:223], v[192:195], v[92:95], 0\n"
".Lmk_b100l:\n"
"ds_read_b128 v[192:195], v248\n"
"ds_read_b128 v[224:227], v247 offset:256\n"
"s_cmp_gt_i32 s78, s96\n"
"s_cselect_b32 s95, 0xff800000, 0\n"
"v_mov_b32_e32 v228, s95\n"
"v_mov_b32_e32 v229, s95\n"
"v_mov_b32_e32 v230, s95\n"
"v_mov_b32_e32 v231, s95\n"
"v_subrev_u32_e32 v248, 0x800, v248\n"
"v_subrev_u32_e32 v247, 0x800, v247\n"
"s_waitcnt lgkmcnt(0)\n"
"v_mfma_f32_16x16x32_bf16 v[232:235], v[192:195], v[92:95], v[228:231]\n"
"v_mfma_f32_16x16x32_bf16 v[236:239], v[192:195], v[96:99], v[228:231]\n"
"v_mfma_f32_16x16x32_bf16 v[192:195], v[224:227], v[96:99], v[228:231]\n"
"v_mfma_f32_16x16x32_bf16 v[240:243], v[224:227], v[92:95], v[228:231]\n"
"s_nop 7\n"
"s_cmp_lg_u32 s96, 0\n"
"s_cbranch_scc1 .Lmk_b100m\n"
"v_cndmask_b32_e64 v232, v196, v232, s[20:21]\n"
"v_cndmask_b32_e64 v233, v197, v233, s[22:23]\n"
"v_cndmask_b32_e64 v234, v198, v234, s[24:25]\n"
"v_cndmask_b32_e64 v235, v199, v235, s[26:27]\n"
"v_cndmask_b32_e64 v240, v220, v240, s[28:29]\n"
"v_cndmask_b32_e64 v241, v221, v241, s[30:31]\n"
"v_cndmask_b32_e64 v242, v222, v242, s[32:33]\n"
"v_cndmask_b32_e64 v243, v223, v243, s[34:35]\n"
"v_cndmask_b32_e64 v236, v212, v236, s[36:37]\n"
"v_cndmask_b32_e64 v237, v213, v237, s[38:39]\n"
"v_cndmask_b32_e64 v238, v214, v238, s[40:41]\n"
"v_cndmask_b32_e64 v239, v215, v239, s[42:43]\n"
"v_cndmask_b32_e64 v192, v188, v192, s[44:45]\n"
"v_cndmask_b32_e64 v193, v189, v193, s[46:47]\n"
"v_cndmask_b32_e64 v194, v190, v194, s[48:49]\n"
"v_cndmask_b32_e64 v195, v191, v195, s[50:51]\n"
".Lmk_b100m:\n"
"v_max3_f32 v253, v253, v232, v233\n"
"v_max3_f32 v251, v251, v236, v237\n"
"v_max3_f32 v253, v253, v234, v235\n"
"v_max3_f32 v251, v251, v238, v239\n"
"v_max3_f32 v253, v253, v240, v241\n"
"v_max3_f32 v251, v251, v192, v193\n"
"v_max3_f32 v253, v253, v242, v243\n"
"v_max3_f32 v251, v251, v194, v195\n"
"s_sub_u32 s96, s96, 1\n"
"s_cmp_ge_i32 s96, 0\n"
"s_cbranch_scc1 .Lmk_b100l\n"
"v_mov_b32_e32 v248, v253\n"
"v_mov_b32_e32 v247, v251\n"
"s_nop 0\n"
"v_permlane16_swap_b32_e32 v253, v248\n"
"v_permlane16_swap_b32_e32 v251, v247\n"
"v_max_f32_e32 v253, v253, v248\n"
"v_max_f32_e32 v251, v251, v247\n"
"v_mov_b32_e32 v248, v253\n"
"v_mov_b32_e32 v247, v251\n"
"s_nop 0\n"
"v_permlane32_swap_b32_e32 v253, v248\n"
"v_permlane32_swap_b32_e32 v251, v247\n"
"v_max_f32_e32 v253, v253, v248\n"
"v_max_f32_e32 v251, v251, v247\n"
"v_mov_b32_e32 v250, v253\n"
"v_mov_b32_e32 v249, v251\n"
"s_branch .Lmk_a99\n"
".Lmk_p106s:\n"
";\n"
"s_waitcnt vmcnt(16)\n"
"s_mov_b32 s1, 0x10000\n"
".Lmk_p106l:\n"
"s_add_u32 s98, s56, 0x40000\n"
"buffer_load_dwordx4 v[152:155], v0, s[4:7], s98 offen sc1\n"
"buffer_load_dwordx4 v[200:203], v0, s[4:7], s98 offen offset:1024 sc1\n"
"buffer_load_dwordx4 v[184:187], v0, s[4:7], s98 offen offset:2048 sc1\n"
"buffer_load_dwordx4 v[204:207], v0, s[4:7], s98 offen offset:3072 sc1\n"
"buffer_load_dwordx4 v[140:143], v1, s[4:7], s98 offen sc1\n"
"buffer_load_dwordx4 v[208:211], v1, s[4:7], s98 offen offset:1024 sc1\n"
"buffer_load_dwordx4 v[120:123], v1, s[4:7], s98 offen offset:2048 sc1\n"
"buffer_load_dwordx4 v[136:139], v1, s[4:7], s98 offen offset:3072 sc1\n"
"s_add_u32 s98, s57, 0x40000\n"
"buffer_load_dwordx4 v[216:219], v0, s[4:7], s98 offen sc1\n"
"buffer_load_dwordx4 v[132:135], v0, s[4:7], s98 offen offset:1024 sc1\n"
"buffer_load_dwordx4 v[160:163], v0, s[4:7], s98 offen offset:2048 sc1\n"
"buffer_load_dwordx4 v[100:103], v0, s[4:7], s98 offen offset:3072 sc1\n"
"buffer_load_dwordx4 v[164:167], v1, s[4:7], s98 offen sc1\n"
"buffer_load_dwordx4 v[104:107], v1, s[4:7], s98 offen offset:1024 sc1\n"
"buffer_load_dwordx4 v[108:111], v1, s[4:7], s98 offen offset:2048 sc1\n"
"buffer_load_dwordx4 v[128:131], v1, s[4:7], s98 offen offset:3072 sc1\n"
"s_add_u32 s98, s58, 0x40000\n"
"buffer_load_dwordx4 v[148:151], v0, s[4:7], s98 offen sc1\n"
"buffer_load_dwordx4 v[144:147], v0, s[4:7], s98 offen offset:1024 sc1\n"
"buffer_load_dwordx4 v[116:119], v0, s[4:7], s98 offen offset:2048 sc1\n"
"buffer_load_dwordx4 v[112:115], v0, s[4:7], s98 offen offset:3072 sc1\n"
"buffer_load_dwordx4 v[92:95], v1, s[4:7], s98 offen sc1\n"
"buffer_load_dwordx4 v[96:99], v1, s[4:7], s98 offen offset:1024 sc1\n"
"buffer_load_dwordx4 v[156:159], v1, s[4:7], s98 offen offset:2048 sc1\n"
"s_waitcnt vmcnt(0)\n"
"v_max_u32_e32 v255, v152, v153\n"
"v_max3_u32 v255, v255, v154, v155\n"
"v_max3_u32 v255, v255, v200, v201\n"
"v_max3_u32 v255, v255, v202, v203\n"
"v_max3_u32 v255, v255, v184, v185\n"
"v_max3_u32 v255, v255, v186, v187\n"
"v_max3_u32 v255, v255, v204, v205\n"
"v_max3_u32 v255, v255, v206, v207\n"
"v_max3_u32 v255, v255, v140, v141\n"
"v_max3_u32 v255, v255, v142, v143\n"
"v_max3_u32 v255, v255, v208, v209\n"
"v_max3_u32 v255, v255, v210, v211\n"
"v_max3_u32 v255, v255, v120, v121\n"
"v_max3_u32 v255, v255, v122, v123\n"
"v_max3_u32 v255, v255, v136, v137\n"
"v_max3_u32 v255, v255, v138, v139\n"
"v_cmp_eq_u32_e32 vcc, -1, v255\n"
"s_cbranch_vccz .Lmk_p106k\n"
"s_sub_u32 s1, s1, 1\n"
"s_cbranch_scc0 .Lmk_p106l\n"
".Lmk_p106k:\n"
"v_mfma_f32_16x16x32_bf16 v[88:91], a[128:131], v[152:155], 0\n"
"v_mfma_f32_16x16x32_bf16 v[88:91], a[132:135], v[200:203], v[88:91]\n"
"v_mfma_f32_16x16x32_bf16 v[88:91], a[136:139], v[184:187], v[88:91]\n"
"v_mfma_f32_16x16x32_bf16 v[88:91], a[140:143], v[204:207], v[88:91]\n"
"v_mfma_f32_16x16x32_bf16 v[88:91], a[144:147], v[140:143], v[88:91]\n"
"v_mfma_f32_16x16x32_bf16 v[88:91], a[148:151], v[208:211], v[88:91]\n"
"v_mfma_f32_16x16x32_bf16 v[88:91], a[152:155], v[120:123], v[88:91]\n"
"v_mfma_f32_16x16x32_bf16 v[88:91], a[156:159], v[136:139], v[88:91]\n"
"v_mfma_f32_16x16x32_bf16 v[124:127], a[188:191], v[136:139], 0\n"
"v_mfma_f32_16x16x32_bf16 v[124:127], a[184:187], v[120:123], v[124:127]\n"
"v_mfma_f32_16x16x32_bf16 v[124:127], a[180:183], v[208:211], v[124:127]\n"
"v_mfma_f32_16x16x32_bf16 v[124:127], a[176:179], v[140:143], v[124:127]\n"
"v_mfma_f32_16x16x32_bf16 v[124:127], a[172:175], v[204:207], v[124:127]\n"
"v_mfma_f32_16x16x32_bf16 v[124:127], a[168:171], v[184:187], v[124:127]\n"
"s_nop 7\n"
"s_branch .Lmk_p106r\n"
".Lmk_p107s:\n"
";\n"
"s_waitcnt vmcnt(16)\n"
"s_mov_b32 s1, 0x10000\n"
".Lmk_p107l:\n"
"s_add_u32 s97, s57, 0x40000\n"
"buffer_load_dwordx4 v[216:219], v0, s[4:7], s97 offen sc1\n"
"buffer_load_dwordx4 v[132:135], v0, s[4:7], s97 offen offset:1024 sc1\n"
"buffer_load_dwordx4 v[160:163], v0, s[4:7], s97 offen offset:2048 sc1\n"
"buffer_load_dwordx4 v[100:103], v0, s[4:7], s97 offen offset:3072 sc1\n"
"buffer_load_dwordx4 v[164:167], v1, s[4:7], s97 offen sc1\n"
"buffer_load_dwordx4 v[104:107], v1, s[4:7], s97 offen offset:1024 sc1\n"
"buffer_load_dwordx4 v[108:111], v1, s[4:7], s97 offen offset:2048 sc1\n"
"buffer_load_dwordx4 v[128:131], v1, s[4:7], s97 offen offset:3072 sc1\n"
"s_add_u32 s97, s58, 0x40000\n"
"buffer_load_dwordx4 v[148:151], v0, s[4:7], s97 offen sc1\n"
"buffer_load_dwordx4 v[144:147], v0, s[4:7], s97 offen offset:1024 sc1\n"
"buffer_load_dwordx4 v[116:119], v0, s[4:7], s97 offen offset:2048 sc1\n"
"buffer_load_dwordx4 v[112:115], v0, s[4:7], s97 offen offset:3072 sc1\n"
"buffer_load_dwordx4 v[92:95], v1, s[4:7], s97 offen sc1\n"
"buffer_load_dwordx4 v[96:99], v1, s[4:7], s97 offen offset:1024 sc1\n"
"buffer_load_dwordx4 v[156:159], v1, s[4:7], s97 offen offset:2048 sc1\n"
"buffer_load_dwordx4 v[88:91], v1, s[4:7], s97 offen offset:3072 sc1\n"
"s_add_u32 s97, s59, 0x40000\n"
"buffer_load_dwordx4 v[172:175], v0, s[4:7], s97 offen sc1\n"
"buffer_load_dwordx4 v[124:127], v0, s[4:7], s97 offen offset:1024 sc1\n"
"buffer_load_dwordx4 v[120:123], v0, s[4:7], s97 offen offset:2048 sc1\n"
"buffer_load_dwordx4 v[136:139], v0, s[4:7], s97 offen offset:3072 sc1\n"
"buffer_load_dwordx4 v[152:155], v1, s[4:7], s97 offen sc1\n"
"buffer_load_dwordx4 v[168:171], v1, s[4:7], s97 offen offset:1024 sc1\n"
"buffer_load_dwordx4 v[180:183], v1, s[4:7], s97 offen offset:2048 sc1\n"
"s_waitcnt vmcnt(0)\n"
"v_max_u32_e32 v255, v216, v217\n"
"v_max3_u32 v255, v255, v218, v219\n"
"v_max3_u32 v255, v255, v132, v133\n"
"v_max3_u32 v255, v255, v134, v135\n"
"v_max3_u32 v255, v255, v160, v161\n"
"v_max3_u32 v255, v255, v162, v163\n"
"v_max3_u32 v255, v255, v100, v101\n"
"v_max3_u32 v255, v255, v102, v103\n"
"v_max3_u32 v255, v255, v164, v165\n"
"v_max3_u32 v255, v255, v166, v167\n"
"v_max3_u32 v255, v255, v104, v105\n"
"v_max3_u32 v255, v255, v106, v107\n"
"v_max3_u32 v255, v255, v108, v109\n"
"v_max3_u32 v255, v255, v110, v111\n"
"v_max3_u32 v255, v255, v128, v129\n"
"v_max3_u32 v255, v255, v130, v131\n"
"v_cmp_eq_u32_e32 vcc, -1, v255\n"
"s_cbranch_vccz .Lmk_p107k\n"
"s_sub_u32 s1, s1, 1\n"
"s_cbranch_scc0 .Lmk_p107l\n"
".Lmk_p107k:\n"
"v_mfma_f32_16x16x32_bf16 v[140:143], a[128:131], v[216:219], 0\n"
"v_mfma_f32_16x16x32_bf16 v[140:143], a[132:135], v[132:135], v[140:143]\n"
"v_mfma_f32_16x16x32_bf16 v[140:143], a[136:139], v[160:163], v[140:143]\n"
"v_mfma_f32_16x16x32_bf16 v[140:143], a[140:143], v[100:103], v[140:143]\n"
"v_mfma_f32_16x16x32_bf16 v[140:143], a[144:147], v[164:167], v[140:143]\n"
"v_mfma_f32_16x16x32_bf16 v[140:143], a[148:151], v[104:107], v[140:143]\n"
"v_mfma_f32_16x16x32_bf16 v[140:143], a[152:155], v[108:111], v[140:143]\n"
"v_mfma_f32_16x16x32_bf16 v[140:143], a[156:159], v[128:131], v[140:143]\n"
"v_mfma_f32_16x16x32_bf16 v[176:179], a[188:191], v[128:131], 0\n"
"v_mfma_f32_16x16x32_bf16 v[176:179], a[184:187], v[108:111], v[176:179]\n"
"v_mfma_f32_16x16x32_bf16 v[176:179], a[180:183], v[104:107], v[176:179]\n"
"v_mfma_f32_16x16x32_bf16 v[176:179], a[176:179], v[164:167], v[176:179]\n"
"v_mfma_f32_16x16x32_bf16 v[176:179], a[172:175], v[100:103], v[176:179]\n"
"v_mfma_f32_16x16x32_bf16 v[176:179], a[168:171], v[160:163], v[176:179]\n"
"s_nop 7\n"
"s_branch .Lmk_p107r\n"
".Lmk_p108s:\n"
";\n"
"s_waitcnt vmcnt(16)\n"
"s_mov_b32 s1, 0x10000\n"
".Lmk_p108l:\n"
"s_add_u32 s97, s58, 0x40000\n"
"buffer_load_dwordx4 v[148:151], v0, s[4:7], s97 offen sc1\n"
"buffer_load_dwordx4 v[144:147], v0, s[4:7], s97 offen offset:1024 sc1\n"
"buffer_load_dwordx4 v[116:119], v0, s[4:7], s97 offen offset:2048 sc1\n"
"buffer_load_dwordx4 v[112:115], v0, s[4:7], s97 offen offset:3072 sc1\n"
"buffer_load_dwordx4 v[92:95], v1, s[4:7], s97 offen sc1\n"
"buffer_load_dwordx4 v[96:99], v1, s[4:7], s97 offen offset:1024 sc1\n"
"buffer_load_dwordx4 v[156:159], v1, s[4:7], s97 offen offset:2048 sc1\n"
"buffer_load_dwordx4 v[88:91], v1, s[4:7], s97 offen offset:3072 sc1\n"
"s_add_u32 s97, s59, 0x40000\n"
"buffer_load_dwordx4 v[172:175], v0, s[4:7], s97 offen sc1\n"
"buffer_load_dwordx4 v[124:127], v0, s[4:7], s97 offen offset:1024 sc1\n"
"buffer_load_dwordx4 v[120:123], v0, s[4:7], s97 offen offset:2048 sc1\n"
"buffer_load_dwordx4 v[136:139], v0, s[4:7], s97 offen offset:3072 sc1\n"
"buffer_load_dwordx4 v[152:155], v1, s[4:7], s97 offen sc1\n"
"buffer_load_dwordx4 v[168:171], v1, s[4:7], s97 offen offset:1024 sc1\n"
"buffer_load_dwordx4 v[180:183], v1, s[4:7], s97 offen offset:2048 sc1\n"
"buffer_load_dwordx4 v[140:143], v1, s[4:7], s97 offen offset:3072 sc1\n"
"s_waitcnt vmcnt(0)\n"
"v_max_u32_e32 v239, v148, v149\n"
"v_max3_u32 v239, v239, v150, v151\n"
"v_max3_u32 v239, v239, v144, v145\n"
"v_max3_u32 v239, v239, v146, v147\n"
"v_max3_u32 v239, v239, v116, v117\n"
"v_max3_u32 v239, v239, v118, v119\n"
"v_max3_u32 v239, v239, v112, v113\n"
"v_max3_u32 v239, v239, v114, v115\n"
"v_max3_u32 v239, v239, v92, v93\n"
"v_max3_u32 v239, v239, v94, v95\n"
"v_max3_u32 v239, v239, v96, v97\n"
"v_max3_u32 v239, v239, v98, v99\n"
"v_max3_u32 v239, v239, v156, v157\n"
"v_max3_u32 v239, v239, v158, v159\n"
"v_max3_u32 v239, v239, v88, v89\n"
"v_max3_u32 v239, v239, v90, v91\n"
"v_cmp_eq_u32_e32 vcc, -1, v239\n"
"s_cbranch_vccz .Lmk_p108k\n"
"s_sub_u32 s1, s1, 1\n"
"s_cbranch_scc0 .Lmk_p108l\n"
".Lmk_p108k:\n"
"v_mfma_f32_16x16x32_bf16 v[100:103], a[128:131], v[148:151], 0\n"
"v_mfma_f32_16x16x32_bf16 v[100:103], a[132:135], v[144:147], v[100:103]\n"
"v_mfma_f32_16x16x32_bf16 v[100:103], a[136:139], v[116:119], v[100:103]\n"
"v_mfma_f32_16x16x32_bf16 v[100:103], a[140:143], v[112:115], v[100:103]\n"
"v_mfma_f32_16x16x32_bf16 v[100:103], a[144:147], v[92:95], v[100:103]\n"
"v_mfma_f32_16x16x32_bf16 v[100:103], a[148:151], v[96:99], v[100:103]\n"
"v_mfma_f32_16x16x32_bf16 v[100:103], a[152:155], v[156:159], v[100:103]\n"
"v_mfma_f32_16x16x32_bf16 v[100:103], a[156:159], v[88:91], v[100:103]\n"
"v_mfma_f32_16x16x32_bf16 v[104:107], a[188:191], v[88:91], 0\n"
"v_mfma_f32_16x16x32_bf16 v[104:107], a[184:187], v[156:159], v[104:107]\n"
"v_mfma_f32_16x16x32_bf16 v[104:107], a[180:183], v[96:99], v[104:107]\n"
"v_mfma_f32_16x16x32_bf16 v[104:107], a[176:179], v[92:95], v[104:107]\n"
"v_mfma_f32_16x16x32_bf16 v[104:107], a[172:175], v[112:115], v[104:107]\n"
"v_mfma_f32_16x16x32_bf16 v[104:107], a[168:171], v[116:119], v[104:107]\n"
"s_nop 7\n"
"s_branch .Lmk_p108r\n"
".Lmk_p109s:\n"
";\n"
"s_waitcnt vmcnt(16)\n"
"s_mov_b32 s1, 0x10000\n"
".Lmk_p109l:\n"
"s_add_u32 s97, s59, 0x40000\n"
"buffer_load_dwordx4 v[172:175], v0, s[4:7], s97 offen sc1\n"
"buffer_load_dwordx4 v[124:127], v0, s[4:7], s97 offen offset:1024 sc1\n"
"buffer_load_dwordx4 v[120:123], v0, s[4:7], s97 offen offset:2048 sc1\n"
"buffer_load_dwordx4 v[136:139], v0, s[4:7], s97 offen offset:3072 sc1\n"
"buffer_load_dwordx4 v[152:155], v1, s[4:7], s97 offen sc1\n"
"buffer_load_dwordx4 v[168:171], v1, s[4:7], s97 offen offset:1024 sc1\n"
"buffer_load_dwordx4 v[180:183], v1, s[4:7], s97 offen offset:2048 sc1\n"
"buffer_load_dwordx4 v[140:143], v1, s[4:7], s97 offen offset:3072 sc1\n"
"s_add_u32 s97, s56, 0\n"
"buffer_load_dwordx4 v[100:103], v0, s[4:7], s97 offen sc1\n"
"buffer_load_dwordx4 v[176:179], v0, s[4:7], s97 offen offset:1024 sc1\n"
"buffer_load_dwordx4 v[104:107], v0, s[4:7], s97 offen offset:2048 sc1\n"
"buffer_load_dwordx4 v[88:91], v0, s[4:7], s97 offen offset:3072 sc1\n"
"buffer_load_dwordx4 v[92:95], v1, s[4:7], s97 offen sc1\n"
"buffer_load_dwordx4 v[116:119], v1, s[4:7], s97 offen offset:1024 sc1\n"
"buffer_load_dwordx4 v[132:135], v1, s[4:7], s97 offen offset:2048 sc1\n"
"buffer_load_dwordx4 v[144:147], v1, s[4:7], s97 offen offset:3072 sc1\n"
"s_add_u32 s97, s57, 0\n"
"buffer_load_dwordx4 v[156:159], v0, s[4:7], s97 offen sc1\n"
"s_waitcnt vmcnt(0)\n"
"v_max_u32_e32 v255, v172, v173\n"
"v_max3_u32 v255, v255, v174, v175\n"
"v_max3_u32 v255, v255, v124, v125\n"
"v_max3_u32 v255, v255, v126, v127\n"
"v_max3_u32 v255, v255, v120, v121\n"
"v_max3_u32 v255, v255, v122, v123\n"
"v_max3_u32 v255, v255, v136, v137\n"
"v_max3_u32 v255, v255, v138, v139\n"
"v_max3_u32 v255, v255, v152, v153\n"
"v_max3_u32 v255, v255, v154, v155\n"
"v_max3_u32 v255, v255, v168, v169\n"
"v_max3_u32 v255, v255, v170, v171\n"
"v_max3_u32 v255, v255, v180, v181\n"
"v_max3_u32 v255, v255, v182, v183\n"
"v_max3_u32 v255, v255, v140, v141\n"
"v_max3_u32 v255, v255, v142, v143\n"
"v_cmp_eq_u32_e32 vcc, -1, v255\n"
"s_cbranch_vccz .Lmk_p109k\n"
"s_sub_u32 s1, s1, 1\n"
"s_cbranch_scc0 .Lmk_p109l\n"
".Lmk_p109k:\n"
"v_mfma_f32_16x16x32_bf16 v[128:131], a[128:131], v[172:175], v[108:111]\n"
"v_mfma_f32_16x16x32_bf16 v[128:131], a[132:135], v[124:127], v[128:131]\n"
"v_mfma_f32_16x16x32_bf16 v[128:131], a[136:139], v[120:123], v[128:131]\n"
"v_mfma_f32_16x16x32_bf16 v[128:131], a[140:143], v[136:139], v[128:131]\n"
"v_mfma_f32_16x16x32_bf16 v[128:131], a[144:147], v[152:155], v[128:131]\n"
"v_mfma_f32_16x16x32_bf16 v[128:131], a[148:151], v[168:171], v[128:131]\n"
"v_mfma_f32_16x16x32_bf16 v[128:131], a[152:155], v[180:183], v[128:131]\n"
"v_mfma_f32_16x16x32_bf16 v[128:131], a[156:159], v[140:143], v[128:131]\n"
"v_mfma_f32_16x16x32_bf16 v[148:151], a[188:191], v[140:143], v[160:163]\n"
"v_mfma_f32_16x16x32_bf16 v[148:151], a[184:187], v[180:183], v[148:151]\n"
"v_mfma_f32_16x16x32_bf16 v[148:151], a[180:183], v[168:171], v[148:151]\n"
"v_mfma_f32_16x16x32_bf16 v[148:151], a[176:179], v[152:155], v[148:151]\n"
"v_mfma_f32_16x16x32_bf16 v[148:151], a[172:175], v[136:139], v[148:151]\n"
"v_mfma_f32_16x16x32_bf16 v[148:151], a[168:171], v[120:123], v[148:151]\n"
"s_nop 7\n"
"s_branch .Lmk_p109r\n"
".Lmk_p110s:\n"
";\n"
"s_waitcnt vmcnt(16)\n"
"s_mov_b32 s1, 0x10000\n"
".Lmk_p110l:\n"
"s_add_u32 s95, s56, 0\n"
"buffer_load_dwordx4 v[100:103], v0, s[4:7], s95 offen sc1\n"
"buffer_load_dwordx4 v[176:179], v0, s[4:7], s95 offen offset:1024 sc1\n"
"buffer_load_dwordx4 v[104:107], v0, s[4:7], s95 offen offset:2048 sc1\n"
"buffer_load_dwordx4 v[88:91], v0, s[4:7], s95 offen offset:3072 sc1\n"
"buffer_load_dwordx4 v[92:95], v1, s[4:7], s95 offen sc1\n"
"buffer_load_dwordx4 v[116:119], v1, s[4:7], s95 offen offset:1024 sc1\n"
"buffer_load_dwordx4 v[132:135], v1, s[4:7], s95 offen offset:2048 sc1\n"
"buffer_load_dwordx4 v[144:147], v1, s[4:7], s95 offen offset:3072 sc1\n"
"s_add_u32 s95, s57, 0\n"
"buffer_load_dwordx4 v[156:159], v0, s[4:7], s95 offen sc1\n"
"buffer_load_dwordx4 v[108:111], v0, s[4:7], s95 offen offset:1024 sc1\n"
"buffer_load_dwordx4 v[112:115], v0, s[4:7], s95 offen offset:2048 sc1\n"
"buffer_load_dwordx4 v[196:199], v0, s[4:7], s95 offen offset:3072 sc1\n"
"buffer_load_dwordx4 v[200:203], v1, s[4:7], s95 offen sc1\n"
"buffer_load_dwordx4 v[128:131], v1, s[4:7], s95 offen offset:1024 sc1\n"
"buffer_load_dwordx4 v[120:123], v1, s[4:7], s95 offen offset:2048 sc1\n"
"buffer_load_dwordx4 v[136:139], v1, s[4:7], s95 offen offset:3072 sc1\n"
"s_add_u32 s95, s58, 0\n"
"buffer_load_dwordx4 v[148:151], v0, s[4:7], s95 offen sc1\n"
"buffer_load_dwordx4 v[160:163], v0, s[4:7], s95 offen offset:1024 sc1\n"
"buffer_load_dwordx4 v[124:127], v0, s[4:7], s95 offen offset:2048 sc1\n"
"s_waitcnt vmcnt(0)\n"
"v_max_u32_e32 v255, v100, v101\n"
"v_max3_u32 v255, v255, v102, v103\n"
"v_max3_u32 v255, v255, v176, v177\n"
"v_max3_u32 v255, v255, v178, v179\n"
"v_max3_u32 v255, v255, v104, v105\n"
"v_max3_u32 v255, v255, v106, v107\n"
"v_max3_u32 v255, v255, v88, v89\n"
"v_max3_u32 v255, v255, v90, v91\n"
"v_max3_u32 v255, v255, v92, v93\n"
"v_max3_u32 v255, v255, v94, v95\n"
"v_max3_u32 v255, v255, v116, v117\n"
"v_max3_u32 v255, v255, v118, v119\n"
"v_max3_u32 v255, v255, v132, v133\n"
"v_max3_u32 v255, v255, v134, v135\n"
"v_max3_u32 v255, v255, v144, v145\n"
"v_max3_u32 v255, v255, v146, v147\n"
"v_cmp_eq_u32_e32 vcc, -1, v255\n"
"s_cbranch_vccz .Lmk_p110k\n"
"s_sub_u32 s1, s1, 1\n"
"s_cbranch_scc0 .Lmk_p110l\n"
".Lmk_p110k:\n"
"v_mfma_f32_16x16x32_bf16 v[140:143], a[128:131], v[100:103], 0\n"
"v_mfma_f32_16x16x32_bf16 v[140:143], a[132:135], v[176:179], v[140:143]\n"
"v_mfma_f32_16x16x32_bf16 v[140:143], a[136:139], v[104:107], v[140:143]\n"
"v_mfma_f32_16x16x32_bf16 v[140:143], a[140:143], v[88:91], v[140:143]\n"
"v_mfma_f32_16x16x32_bf16 v[140:143], a[144:147], v[92:95], v[140:143]\n"
"v_mfma_f32_16x16x32_bf16 v[140:143], a[148:151], v[116:119], v[140:143]\n"
"v_mfma_f32_16x16x32_bf16 v[140:143], a[152:155], v[132:135], v[140:143]\n"
"v_mfma_f32_16x16x32_bf16 v[140:143], a[156:159], v[144:147], v[140:143]\n"
"v_mfma_f32_16x16x32_bf16 v[96:99], a[188:191], v[144:147], 0\n"
"v_mfma_f32_16x16x32_bf16 v[96:99], a[184:187], v[132:135], v[96:99]\n"
"v_mfma_f32_16x16x32_bf16 v[96:99], a[180:183], v[116:119], v[96:99]\n"
"v_mfma_f32_16x16x32_bf16 v[96:99], a[176:179], v[92:95], v[96:99]\n"
"v_mfma_f32_16x16x32_bf16 v[96:99], a[172:175], v[88:91], v[96:99]\n"
"v_mfma_f32_16x16x32_bf16 v[96:99], a[168:171], v[104:107], v[96:99]\n"
"s_nop 7\n"
"s_branch .Lmk_p110r\n"
".Lmk_p111s:\n"
";\n"
"s_waitcnt vmcnt(16)\n"
"s_mov_b32 s1, 0x10000\n"
".Lmk_p111l:\n"
"s_add_u32 s97, s57, 0\n"
"buffer_load_dwordx4 v[156:159], v0, s[4:7], s97 offen sc1\n"
"buffer_load_dwordx4 v[108:111], v0, s[4:7], s97 offen offset:1024 sc1\n"
"buffer_load_dwordx4 v[112:115], v0, s[4:7], s97 offen offset:2048 sc1\n"
"buffer_load_dwordx4 v[196:199], v0, s[4:7], s97 offen offset:3072 sc1\n"
"buffer_load_dwordx4 v[200:203], v1, s[4:7], s97 offen sc1\n"
"buffer_load_dwordx4 v[128:131], v1, s[4:7], s97 offen offset:1024 sc1\n"
"buffer_load_dwordx4 v[120:123], v1, s[4:7], s97 offen offset:2048 sc1\n"
"buffer_load_dwordx4 v[136:139], v1, s[4:7], s97 offen offset:3072 sc1\n"
"s_add_u32 s97, s58, 0\n"
"buffer_load_dwordx4 v[148:151], v0, s[4:7], s97 offen sc1\n"
"buffer_load_dwordx4 v[160:163], v0, s[4:7], s97 offen offset:1024 sc1\n"
"buffer_load_dwordx4 v[124:127], v0, s[4:7], s97 offen offset:2048 sc1\n"
"buffer_load_dwordx4 v[140:143], v0, s[4:7], s97 offen offset:3072 sc1\n"
"buffer_load_dwordx4 v[152:155], v1, s[4:7], s97 offen sc1\n"
"buffer_load_dwordx4 v[164:167], v1, s[4:7], s97 offen offset:1024 sc1\n"
"buffer_load_dwordx4 v[116:119], v1, s[4:7], s97 offen offset:2048 sc1\n"
"buffer_load_dwordx4 v[132:135], v1, s[4:7], s97 offen offset:3072 sc1\n"
"s_add_u32 s97, s59, 0\n"
"buffer_load_dwordx4 v[172:175], v0, s[4:7], s97 offen sc1\n"
"buffer_load_dwordx4 v[176:179], v0, s[4:7], s97 offen offset:1024 sc1\n"
"buffer_load_dwordx4 v[168:171], v0, s[4:7], s97 offen offset:2048 sc1\n"
"s_waitcnt vmcnt(0)\n"
"v_max_u32_e32 v255, v156, v157\n"
"v_max3_u32 v255, v255, v158, v159\n"
"v_max3_u32 v255, v255, v108, v109\n"
"v_max3_u32 v255, v255, v110, v111\n"
"v_max3_u32 v255, v255, v112, v113\n"
"v_max3_u32 v255, v255, v114, v115\n"
"v_max3_u32 v255, v255, v196, v197\n"
"v_max3_u32 v255, v255, v198, v199\n"
"v_max3_u32 v255, v255, v200, v201\n"
"v_max3_u32 v255, v255, v202, v203\n"
"v_max3_u32 v255, v255, v128, v129\n"
"v_max3_u32 v255, v255, v130, v131\n"
"v_max3_u32 v255, v255, v120, v121\n"
"v_max3_u32 v255, v255, v122, v123\n"
"v_max3_u32 v255, v255, v136, v137\n"
"v_max3_u32 v255, v255, v138, v139\n"
"v_cmp_eq_u32_e32 vcc, -1, v255\n"
"s_cbranch_vccz .Lmk_p111k\n"
"s_sub_u32 s1, s1, 1\n"
"s_cbranch_scc0 .Lmk_p111l\n"
".Lmk_p111k:\n"
"v_mfma_f32_16x16x32_bf16 v[88:91], a[128:131], v[156:159], 0\n"
"v_mfma_f32_16x16x32_bf16 v[88:91], a[132:135], v[108:111], v[88:91]\n"
"v_mfma_f32_16x16x32_bf16 v[88:91], a[136:139], v[112:115], v[88:91]\n"
"v_mfma_f32_16x16x32_bf16 v[88:91], a[140:143], v[196:199], v[88:91]\n"
"v_mfma_f32_16x16x32_bf16 v[88:91], a[144:147], v[200:203], v[88:91]\n"
"v_mfma_f32_16x16x32_bf16 v[88:91], a[148:151], v[128:131], v[88:91]\n"
"v_mfma_f32_16x16x32_bf16 v[88:91], a[152:155], v[120:123], v[88:91]\n"
"v_mfma_f32_16x16x32_bf16 v[88:91], a[156:159], v[136:139], v[88:91]\n"
"v_mfma_f32_16x16x32_bf16 v[92:95], a[188:191], v[136:139], 0\n"
"v_mfma_f32_16x16x32_bf16 v[92:95], a[184:187], v[120:123], v[92:95]\n"
"v_mfma_f32_16x16x32_bf16 v[92:95], a[180:183], v[128:131], v[92:95]\n"
"v_mfma_f32_16x16x32_bf16 v[92:95], a[176:179], v[200:203], v[92:95]\n"
"v_mfma_f32_16x16x32_bf16 v[92:95], a[172:175], v[196:199], v[92:95]\n"
"v_mfma_f32_16x16x32_bf16 v[92:95], a[168:171], v[112:115], v[92:95]\n"
"s_nop 7\n"
"s_branch .Lmk_p111r\n"
".Lmk_p112s:\n"
";\n"
"s_waitcnt vmcnt(16)\n"
"s_mov_b32 s1, 0x10000\n"
".Lmk_p112l:\n"
"s_add_u32 s97, s58, 0\n"
"buffer_load_dwordx4 v[148:151], v0, s[4:7], s97 offen sc1\n"
"buffer_load_dwordx4 v[160:163], v0, s[4:7], s97 offen offset:1024 sc1\n"
"buffer_load_dwordx4 v[124:127], v0, s[4:7], s97 offen offset:2048 sc1\n"
"buffer_load_dwordx4 v[140:143], v0, s[4:7], s97 offen offset:3072 sc1\n"
"buffer_load_dwordx4 v[152:155], v1, s[4:7], s97 offen sc1\n"
"buffer_load_dwordx4 v[164:167], v1, s[4:7], s97 offen offset:1024 sc1\n"
"buffer_load_dwordx4 v[116:119], v1, s[4:7], s97 offen offset:2048 sc1\n"
"buffer_load_dwordx4 v[132:135], v1, s[4:7], s97 offen offset:3072 sc1\n"
"s_add_u32 s97, s59, 0\n"
"buffer_load_dwordx4 v[172:175], v0, s[4:7], s97 offen sc1\n"
"buffer_load_dwordx4 v[176:179], v0, s[4:7], s97 offen offset:1024 sc1\n"
"buffer_load_dwordx4 v[168:171], v0, s[4:7], s97 offen offset:2048 sc1\n"
"buffer_load_dwordx4 v[180:183], v0, s[4:7], s97 offen offset:3072 sc1\n"
"buffer_load_dwordx4 v[184:187], v1, s[4:7], s97 offen sc1\n"
"buffer_load_dwordx4 v[188:191], v1, s[4:7], s97 offen offset:1024 sc1\n"
"buffer_load_dwordx4 v[192:195], v1, s[4:7], s97 offen offset:2048 sc1\n"
"buffer_load_dwordx4 v[196:199], v1, s[4:7], s97 offen offset:3072 sc1\n"
"s_add_u32 s97, s52, 0x440000\n"
"buffer_load_dwordx4 v[88:91], v0, s[4:7], s97 offen sc1\n"
"buffer_load_dwordx4 v[92:95], v0, s[4:7], s97 offen offset:1024 sc1\n"
"s_waitcnt vmcnt(0)\n"
"v_max_u32_e32 v239, v148, v149\n"
"v_max3_u32 v239, v239, v150, v151\n"
"v_max3_u32 v239, v239, v160, v161\n"
"v_max3_u32 v239, v239, v162, v163\n"
"v_max3_u32 v239, v239, v124, v125\n"
"v_max3_u32 v239, v239, v126, v127\n"
"v_max3_u32 v239, v239, v140, v141\n"
"v_max3_u32 v239, v239, v142, v143\n"
"v_max3_u32 v239, v239, v152, v153\n"
"v_max3_u32 v239, v239, v154, v155\n"
"v_max3_u32 v239, v239, v164, v165\n"
"v_max3_u32 v239, v239, v166, v167\n"
"v_max3_u32 v239, v239, v116, v117\n"
"v_max3_u32 v239, v239, v118, v119\n"
"v_max3_u32 v239, v239, v132, v133\n"
"v_max3_u32 v239, v239, v134, v135\n"
"v_cmp_eq_u32_e32 vcc, -1, v239\n"
"s_cbranch_vccz .Lmk_p112k\n"
"s_sub_u32 s1, s1, 1\n"
"s_cbranch_scc0 .Lmk_p112l\n"
".Lmk_p112k:\n"
"v_mfma_f32_16x16x32_bf16 v[96:99], a[128:131], v[148:151], 0\n"
"v_mfma_f32_16x16x32_bf16 v[96:99], a[132:135], v[160:163], v[96:99]\n"
"v_mfma_f32_16x16x32_bf16 v[96:99], a[136:139], v[124:127], v[96:99]\n"
"v_mfma_f32_16x16x32_bf16 v[96:99], a[140:143], v[140:143], v[96:99]\n"
"v_mfma_f32_16x16x32_bf16 v[96:99], a[144:147], v[152:155], v[96:99]\n"
"v_mfma_f32_16x16x32_bf16 v[96:99], a[148:151], v[164:167], v[96:99]\n"
"v_mfma_f32_16x16x32_bf16 v[96:99], a[152:155], v[116:119], v[96:99]\n"
"v_mfma_f32_16x16x32_bf16 v[96:99], a[156:159], v[132:135], v[96:99]\n"
"v_mfma_f32_16x16x32_bf16 v[104:107], a[188:191], v[132:135], 0\n"
"v_mfma_f32_16x16x32_bf16 v[104:107], a[184:187], v[116:119], v[104:107]\n"
"v_mfma_f32_16x16x32_bf16 v[104:107], a[180:183], v[164:167], v[104:107]\n"
"v_mfma_f32_16x16x32_bf16 v[104:107], a[176:179], v[152:155], v[104:107]\n"
"v_mfma_f32_16x16x32_bf16 v[104:107], a[172:175], v[140:143], v[104:107]\n"
"v_mfma_f32_16x16x32_bf16 v[104:107], a[168:171], v[124:127], v[104:107]\n"
"s_nop 7\n"
"s_branch .Lmk_p112r\n"
".Lmk_p113s:\n"
";\n"
"s_waitcnt vmcnt(16)\n"
"s_mov_b32 s1, 0x10000\n"
".Lmk_p113l:\n"
"s_add_u32 s97, s59, 0\n"
"buffer_load_dwordx4 v[172:175], v0, s[4:7], s97 offen sc1\n"
"buffer_load_dwordx4 v[176:179], v0, s[4:7], s97 offen offset:1024 sc1\n"
"buffer_load_dwordx4 v[168:171], v0, s[4:7], s97 offen offset:2048 sc1\n"
"buffer_load_dwordx4 v[180:183], v0, s[4:7], s97 offen offset:3072 sc1\n"
"buffer_load_dwordx4 v[184:187], v1, s[4:7], s97 offen sc1\n"
"buffer_load_dwordx4 v[188:191], v1, s[4:7], s97 offen offset:1024 sc1\n"
"buffer_load_dwordx4 v[192:195], v1, s[4:7], s97 offen offset:2048 sc1\n"
"buffer_load_dwordx4 v[196:199], v1, s[4:7], s97 offen offset:3072 sc1\n"
"s_add_u32 s97, s52, 0x440000\n"
"buffer_load_dwordx4 v[88:91], v0, s[4:7], s97 offen sc1\n"
"buffer_load_dwordx4 v[92:95], v0, s[4:7], s97 offen offset:1024 sc1\n"
"buffer_load_dwordx4 v[96:99], v0, s[4:7], s97 offen offset:2048 sc1\n"
"buffer_load_dwordx4 v[100:103], v0, s[4:7], s97 offen offset:3072 sc1\n"
"buffer_load_dwordx4 v[104:107], v1, s[4:7], s97 offen sc1\n"
"buffer_load_dwordx4 v[112:115], v1, s[4:7], s97 offen offset:1024 sc1\n"
"buffer_load_dwordx4 v[116:119], v1, s[4:7], s97 offen offset:2048 sc1\n"
"buffer_load_dwordx4 v[124:127], v1, s[4:7], s97 offen offset:3072 sc1\n"
"s_add_u32 s97, s53, 0x440000\n"
"buffer_load_dwordx4 v[128:131], v0, s[4:7], s97 offen sc1\n"
"buffer_load_dwordx4 v[132:135], v0, s[4:7], s97 offen offset:1024 sc1\n"
"buffer_load_dwordx4 v[136:139], v0, s[4:7], s97 offen offset:2048 sc1\n"
"s_waitcnt vmcnt(0)\n"
"v_max_u32_e32 v255, v172, v173\n"
"v_max3_u32 v255, v255, v174, v175\n"
"v_max3_u32 v255, v255, v176, v177\n"
"v_max3_u32 v255, v255, v178, v179\n"
"v_max3_u32 v255, v255, v168, v169\n"
"v_max3_u32 v255, v255, v170, v171\n"
"v_max3_u32 v255, v255, v180, v181\n"
"v_max3_u32 v255, v255, v182, v183\n"
"v_max3_u32 v255, v255, v184, v185\n"
"v_max3_u32 v255, v255, v186, v187\n"
"v_max3_u32 v255, v255, v188, v189\n"
"v_max3_u32 v255, v255, v190, v191\n"
"v_max3_u32 v255, v255, v192, v193\n"
"v_max3_u32 v255, v255, v194, v195\n"
"v_max3_u32 v255, v255, v196, v197\n"
"v_max3_u32 v255, v255, v198, v199\n"
"v_cmp_eq_u32_e32 vcc, -1, v255\n"
"s_cbranch_vccz .Lmk_p113k\n"
"s_sub_u32 s1, s1, 1\n"
"s_cbranch_scc0 .Lmk_p113l\n"
".Lmk_p113k:\n"
"v_mfma_f32_16x16x32_bf16 v[164:167], a[128:131], v[172:175], v[144:147]\n"
"v_mfma_f32_16x16x32_bf16 v[164:167], a[132:135], v[176:179], v[164:167]\n"
"v_mfma_f32_16x16x32_bf16 v[164:167], a[136:139], v[168:171], v[164:167]\n"
"v_mfma_f32_16x16x32_bf16 v[164:167], a[140:143], v[180:183], v[164:167]\n"
"v_mfma_f32_16x16x32_bf16 v[164:167], a[144:147], v[184:187], v[164:167]\n"
"v_mfma_f32_16x16x32_bf16 v[164:167], a[148:151], v[188:191], v[164:167]\n"
"v_mfma_f32_16x16x32_bf16 v[164:167], a[152:155], v[192:195], v[164:167]\n"
"v_mfma_f32_16x16x32_bf16 v[164:167], a[156:159], v[196:199], v[164:167]\n"
"v_mfma_f32_16x16x32_bf16 v[200:203], a[188:191], v[196:199], v[156:159]\n"
"v_mfma_f32_16x16x32_bf16 v[200:203], a[184:187], v[192:195], v[200:203]\n"
"v_mfma_f32_16x16x32_bf16 v[200:203], a[180:183], v[188:191], v[200:203]\n"
"v_mfma_f32_16x16x32_bf16 v[200:203], a[176:179], v[184:187], v[200:203]\n"
"v_mfma_f32_16x16x32_bf16 v[200:203], a[172:175], v[180:183], v[200:203]\n"
"v_mfma_f32_16x16x32_bf16 v[200:203], a[168:171], v[168:171], v[200:203]\n"
"s_nop 7\n"
"s_branch .Lmk_p113r\n"
".Lmk_exit:\n"
"v_xor_b32_e32 v255, 32, v15\n"
"ds_read_b128 v[36:39], v2 offset:17408\n"
"ds_read_b128 v[40:43], v15 offset:16384\n"
"ds_read_b128 v[52:55], v255 offset:16640\n"
"v_xor_b32_e32 v255, 32, v17\n"
"ds_read_b128 v[60:63], v2 offset:16384\n"
"ds_read_b128 v[64:67], v17 offset:15360\n"
"ds_read_b128 v[68:71], v255 offset:15616\n"
"v_xor_b32_e32 v255, 32, v16\n"
"ds_read_b64_tr_b16 v[72:73], v16 offset:16384\n"
"ds_read_b64_tr_b16 v[74:75], v255 offset:16640\n"
"ds_read_b64_tr_b16 v[76:77], v255 offset:16384\n"
"ds_read_b64_tr_b16 v[78:79], v16 offset:16640\n"
"v_mov_b32_e32 v80, 0x3f803f80\n"
"v_mov_b32_e32 v81, 0x3f803f80\n"
"v_mov_b32_e32 v82, 0x3f803f80\n"
"v_mov_b32_e32 v83, 0x3f803f80\n"
"v_xor_b32_e32 v255, 32, v16\n"
"ds_read_b64_tr_b16 v[84:85], v16 offset:15360\n"
"ds_read_b64_tr_b16 v[86:87], v255 offset:15616\n"
"s_waitcnt lgkmcnt(13)\n"
"ds_read_b64_tr_b16 v[96:97], v255 offset:15360\n"
"ds_read_b64_tr_b16 v[98:99], v16 offset:15616\n"
"v_mov_b32_e32 v100, 0x3f803f80\n"
"v_mov_b32_e32 v101, 0x3f803f80\n"
"v_mov_b32_e32 v102, 0x3f803f80\n"
"v_mov_b32_e32 v103, 0x3f803f80\n"
"s_waitcnt lgkmcnt(11)\n"
"v_mfma_f32_16x16x32_bf16 v[104:107], v[40:43], v[36:39], v[104:107]\n"
"v_mfma_f32_16x16x32_bf16 v[124:127], v[52:55], v[36:39], v[124:127]\n"
"s_waitcnt lgkmcnt(8)\n"
"v_mfma_f32_16x16x32_bf16 v[88:91], v[64:67], v[60:63], v[88:91]\n"
"v_mfma_f32_16x16x32_bf16 v[176:179], v[68:71], v[60:63], v[176:179]\n"
"s_nop 3\n"
"v_max_f32_e32 v255, v104, v105\n"
"s_nop 1\n"
"v_max_f32_e32 v254, v88, v89\n"
"v_max3_f32 v255, v255, v106, v107\n"
"v_max3_f32 v254, v254, v90, v91\n"
"v_max3_f32 v255, v255, v124, v125\n"
"v_max3_f32 v254, v254, v176, v177\n"
"v_max3_f32 v255, v255, v126, v127\n"
"v_max3_f32 v254, v254, v178, v179\n"
"v_mov_b32_e32 v253, v255\n"
"v_mov_b32_e32 v252, v254\n"
"s_nop 0\n"
"v_permlane16_swap_b32_e32 v255, v253\n"
"v_permlane16_swap_b32_e32 v254, v252\n"
"v_max_f32_e32 v255, v255, v253\n"
"v_max_f32_e32 v254, v254, v252\n"
"v_mov_b32_e32 v253, v255\n"
"v_mov_b32_e32 v252, v254\n"
"s_nop 0\n"
"v_permlane32_swap_b32_e32 v255, v253\n"
"v_permlane32_swap_b32_e32 v254, v252\n"
"v_max_f32_e32 v255, v255, v253\n"
"v_max_f32_e32 v254, v254, v252\n"
"v_sub_f32_e32 v104, v104, v255\n"
"v_sub_f32_e32 v105, v105, v255\n"
"v_sub_f32_e32 v106, v106, v255\n"
"v_sub_f32_e32 v107, v107, v255\n"
"v_sub_f32_e32 v124, v124, v255\n"
"v_sub_f32_e32 v125, v125, v255\n"
"v_sub_f32_e32 v126, v126, v255\n"
"v_sub_f32_e32 v127, v127, v255\n"
"v_exp_f32_e32 v104, v104\n"
"v_exp_f32_e32 v105, v105\n"
"v_exp_f32_e32 v106, v106\n"
"v_exp_f32_e32 v107, v107\n"
"v_exp_f32_e32 v124, v124\n"
"v_exp_f32_e32 v125, v125\n"
"v_exp_f32_e32 v126, v126\n"
"v_exp_f32_e32 v127, v127\n"
"v_cvt_pk_bf16_f32 v36, v104, v105\n"
"v_cvt_pk_bf16_f32 v37, v106, v107\n"
"v_cvt_pk_bf16_f32 v38, v124, v125\n"
"v_cvt_pk_bf16_f32 v39, v126, v127\n"
"s_waitcnt lgkmcnt(4)\n"
"v_sub_f32_e32 v88, v88, v254\n"
"v_sub_f32_e32 v89, v89, v254\n"
"v_sub_f32_e32 v90, v90, v254\n"
"v_sub_f32_e32 v91, v91, v254\n"
"v_sub_f32_e32 v176, v176, v254\n"
"v_sub_f32_e32 v177, v177, v254\n"
"v_sub_f32_e32 v178, v178, v254\n"
"v_mfma_f32_16x16x32_bf16 v[40:43], v[80:83], v[36:39], 0\n"
"v_sub_f32_e32 v179, v179, v254\n"
"v_exp_f32_e32 v88, v88\n"
"v_exp_f32_e32 v89, v89\n"
"v_exp_f32_e32 v90, v90\n"
"v_mfma_f32_16x16x32_bf16 v[52:55], v[72:75], v[36:39], 0\n"
"v_exp_f32_e32 v91, v91\n"
"v_exp_f32_e32 v176, v176\n"
"v_exp_f32_e32 v177, v177\n"
"v_exp_f32_e32 v178, v178\n"
"v_mfma_f32_16x16x32_bf16 v[60:63], v[76:79], v[36:39], 0\n"
"v_exp_f32_e32 v179, v179\n"
"v_cvt_pk_bf16_f32 v36, v88, v89\n"
"v_cvt_pk_bf16_f32 v37, v90, v91\n"
"v_cvt_pk_bf16_f32 v38, v176, v177\n"
"v_cvt_pk_bf16_f32 v39, v178, v179\n"
"s_waitcnt lgkmcnt(0)\n"
"v_rcp_f32_e32 v40, v40\n"
"s_nop 0\n"
"v_mul_f32_e32 v52, v52, v40\n"
"v_mul_f32_e32 v53, v53, v40\n"
"v_mfma_f32_16x16x32_bf16 v[64:67], v[100:103], v[36:39], 0\n"
"v_mul_f32_e32 v54, v54, v40\n"
"v_mul_f32_e32 v55, v55, v40\n"
"v_mul_f32_e32 v60, v60, v40\n"
"v_mfma_f32_16x16x32_bf16 v[68:71], v[84:87], v[36:39], 0\n"
"v_mul_f32_e32 v61, v61, v40\n"
"v_mul_f32_e32 v62, v62, v40\n"
"v_mul_f32_e32 v63, v63, v40\n"
"v_cvt_pk_bf16_f32 v40, v52, v53\n"
"v_mfma_f32_16x16x32_bf16 v[72:75], v[96:99], v[36:39], 0\n"
"v_cvt_pk_bf16_f32 v41, v54, v55\n"
"v_cvt_pk_bf16_f32 v42, v60, v61\n"
"v_cvt_pk_bf16_f32 v43, v62, v63\n"
"s_add_u32 s97, s60, 0x408000\n"
"buffer_store_dwordx4 v[40:43], v0, s[4:7], s97 offen sc1\n"
"v_rcp_f32_e32 v64, v64\n"
"s_nop 0\n"
"v_mul_f32_e32 v68, v68, v64\n"
"v_mul_f32_e32 v69, v69, v64\n"
"v_mul_f32_e32 v70, v70, v64\n"
"v_mul_f32_e32 v71, v71, v64\n"
"v_mul_f32_e32 v72, v72, v64\n"
"v_mul_f32_e32 v73, v73, v64\n"
"v_mul_f32_e32 v74, v74, v64\n"
"v_mul_f32_e32 v75, v75, v64\n"
"v_cvt_pk_bf16_f32 v36, v68, v69\n"
"v_cvt_pk_bf16_f32 v37, v70, v71\n"
"v_cvt_pk_bf16_f32 v38, v72, v73\n"
"v_cvt_pk_bf16_f32 v39, v74, v75\n"
"s_add_u32 s97, s60, 0x400000\n"
"buffer_store_dwordx4 v[36:39], v0, s[4:7], s97 offen sc1\n"
";\n"
"s_waitcnt vmcnt(23)\n"
"; @B G2 b.0\n"
"s_nop 0\n"
"v_mfma_f32_16x16x32_bf16 v[36:39], a[128:131], v[120:123], 0\n"
"s_add_u32 s97, s58, 0x440000\n"
"buffer_load_dwordx4 v[40:43], v0, s[4:7], s97 offen sc1\n"
"v_mfma_f32_16x16x32_bf16 v[36:39], a[132:135], v[136:139], v[36:39]\n"
"s_waitcnt vmcnt(22)\n"
"v_mfma_f32_16x16x32_bf16 v[36:39], a[136:139], v[144:147], v[36:39]\n"
"v_mfma_f32_16x16x32_bf16 v[36:39], a[140:143], v[192:195], v[36:39]\n"
"s_waitcnt vmcnt(20)\n"
"v_mfma_f32_16x16x32_bf16 v[36:39], a[144:147], v[196:199], v[36:39]\n"
"buffer_load_dwordx4 v[52:55], v0, s[4:7], s97 offen offset:1024 sc1\n"
"v_mfma_f32_16x16x32_bf16 v[36:39], a[148:151], v[188:191], v[36:39]\n"
"s_waitcnt vmcnt(19)\n"
"v_mfma_f32_16x16x32_bf16 v[36:39], a[152:155], v[108:111], v[36:39]\n"
"v_mfma_f32_16x16x32_bf16 v[36:39], a[156:159], v[92:95], v[36:39]\n"
"v_mfma_f32_16x16x32_bf16 v[60:63], a[188:191], v[92:95], 0\n"
"buffer_load_dwordx4 v[64:67], v0, s[4:7], s97 offen offset:2048 sc1\n"
"v_mfma_f32_16x16x32_bf16 v[60:63], a[184:187], v[108:111], v[60:63]\n"
"v_mfma_f32_16x16x32_bf16 v[60:63], a[180:183], v[188:191], v[60:63]\n"
"v_mfma_f32_16x16x32_bf16 v[60:63], a[176:179], v[196:199], v[60:63]\n"
"v_mfma_f32_16x16x32_bf16 v[60:63], a[172:175], v[192:195], v[60:63]\n"
"s_nop 1\n"
"v_cmp_u_f32_e32 vcc, v36, v36\n"
"v_mfma_f32_16x16x32_bf16 v[60:63], a[168:171], v[144:147], v[60:63]\n"
"s_cbranch_vccnz .Lmk_p55s\n"
".Lmk_p55r:\n"
"v_mfma_f32_16x16x32_bf16 v[60:63], a[164:167], v[136:139], v[60:63]\n"
"ds_write_b128 v3, v[36:39]\n"
"v_mfma_f32_16x16x32_bf16 v[60:63], a[160:163], v[120:123], v[60:63]\n"
"buffer_load_dwordx4 v[36:39], v0, s[4:7], s97 offen offset:3072 sc1\n"
"v_mfma_f32_16x16x32_bf16 v[68:71], a[192:195], v[120:123], 0\n"
"v_bfe_u32 v255, v0, 4, 4\n"
"v_bfe_u32 v254, v0, 9, 1\n"
"v_mfma_f32_16x16x32_bf16 v[68:71], a[196:199], v[136:139], v[68:71]\n"
"v_and_b32_e32 v253, 7, v255\n"
"v_xor_b32_e32 v254, v254, v253\n"
"v_mfma_f32_16x16x32_bf16 v[68:71], a[200:203], v[144:147], v[68:71]\n"
"v_lshlrev_b32_e32 v255, 7, v255\n"
"v_lshl_add_u32 v255, v254, 4, v255\n"
"v_mfma_f32_16x16x32_bf16 v[68:71], a[204:207], v[192:195], v[68:71]\n"
"buffer_load_dwordx4 v[72:75], v1, s[4:7], s97 offen sc1\n"
"v_mfma_f32_16x16x32_bf16 v[68:71], a[208:211], v[196:199], v[68:71]\n"
"v_bfe_u32 v254, v0, 8, 1\n"
"v_lshl_add_u32 v255, v254, 3, v255\n"
"v_mfma_f32_16x16x32_bf16 v[68:71], a[212:215], v[188:191], v[68:71]\n"
"ds_write_b128 v3, v[60:63] offset:1024\n"
"v_mfma_f32_16x16x32_bf16 v[68:71], a[216:219], v[108:111], v[68:71]\n"
"v_add_u32_e32 v255, s77, v255\n"
"v_bfe_u32 v253, v0, 7, 3\n"
"v_mfma_f32_16x16x32_bf16 v[68:71], a[220:223], v[92:95], v[68:71]\n"
"buffer_load_dwordx4 v[60:63], v1, s[4:7], s97 offen offset:1024 sc1\n"
"v_mfma_f32_16x16x32_bf16 v[76:79], a[252:255], v[92:95], 0\n"
"v_bfe_u32 v254, v0, 4, 3\n"
"v_xor_b32_e32 v254, v254, v253\n"
"v_mfma_f32_16x16x32_bf16 v[76:79], a[248:251], v[108:111], v[76:79]\n"
"v_lshlrev_b32_e32 v253, 11, v253\n"
"v_lshl_add_u32 v253, v254, 4, v253\n"
"v_mfma_f32_16x16x32_bf16 v[76:79], a[244:247], v[188:191], v[76:79]\n"
"v_mfma_f32_16x16x32_bf16 v[76:79], a[240:243], v[196:199], v[76:79]\n"
"buffer_load_dwordx4 v[80:83], v1, s[4:7], s97 offen offset:2048 sc1\n"
"v_mfma_f32_16x16x32_bf16 v[76:79], a[236:239], v[192:195], v[76:79]\n"
"v_mfma_f32_16x16x32_bf16 v[76:79], a[232:235], v[144:147], v[76:79]\n"
"ds_write_b128 v3, v[68:71] offset:2048\n"
"v_mfma_f32_16x16x32_bf16 v[76:79], a[228:231], v[136:139], v[76:79]\n"
"v_mfma_f32_16x16x32_bf16 v[76:79], a[224:227], v[120:123], v[76:79]\n"
"s_waitcnt vmcnt(20)\n"
"buffer_load_dwordx4 v[68:71], v1, s[4:7], s97 offen offset:3072 sc1\n"
"; @B G2 b.1\n"
"v_mfma_f32_16x16x32_bf16 v[84:87], a[128:131], v[148:151], 0\n"
"v_mfma_f32_16x16x32_bf16 v[84:87], a[132:135], v[180:183], v[84:87]\n"
"s_add_u32 s97, s59, 0x440000\n"
"v_mfma_f32_16x16x32_bf16 v[84:87], a[136:139], v[24:27], v[84:87]\n"
"v_mfma_f32_16x16x32_bf16 v[84:87], a[140:143], v[32:35], v[84:87]\n"
"s_waitcnt vmcnt(15)\n"
"buffer_load_dwordx4 v[96:99], v0, s[4:7], s97 offen sc1\n"
"v_mfma_f32_16x16x32_bf16 v[84:87], a[144:147], v[44:47], v[84:87]\n"
"v_mfma_f32_16x16x32_bf16 v[84:87], a[148:151], v[48:51], v[84:87]\n"
"ds_write_b128 v3, v[76:79] offset:3072\n"
"v_mfma_f32_16x16x32_bf16 v[84:87], a[152:155], v[28:31], v[84:87]\n"
"v_mfma_f32_16x16x32_bf16 v[84:87], a[156:159], v[56:59], v[84:87]\n"
"buffer_load_dwordx4 v[76:79], v0, s[4:7], s97 offen offset:1024 sc1\n"
"v_mfma_f32_16x16x32_bf16 v[100:103], a[188:191], v[56:59], 0\n"
"v_mfma_f32_16x16x32_bf16 v[100:103], a[184:187], v[28:31], v[100:103]\n"
"v_mfma_f32_16x16x32_bf16 v[100:103], a[180:183], v[48:51], v[100:103]\n"
"v_mfma_f32_16x16x32_bf16 v[100:103], a[176:179], v[44:47], v[100:103]\n"
"buffer_load_dwordx4 v[112:115], v0, s[4:7], s97 offen offset:2048 sc1\n"
"v_mfma_f32_16x16x32_bf16 v[100:103], a[172:175], v[32:35], v[100:103]\n"
"s_nop 0\n"
"v_cmp_u_f32_e32 vcc, v84, v84\n"
"v_mfma_f32_16x16x32_bf16 v[100:103], a[168:171], v[24:27], v[100:103]\n"
"s_cbranch_vccnz .Lmk_p56s\n"
".Lmk_p56r:\n"
"v_mfma_f32_16x16x32_bf16 v[100:103], a[164:167], v[180:183], v[100:103]\n"
"ds_write_b128 v3, v[84:87] offset:6144\n"
"v_mfma_f32_16x16x32_bf16 v[100:103], a[160:163], v[148:151], v[100:103]\n"
"buffer_load_dwordx4 v[84:87], v0, s[4:7], s97 offen offset:3072 sc1\n"
"v_mfma_f32_16x16x32_bf16 v[116:119], a[192:195], v[148:151], 0\n"
"v_mfma_f32_16x16x32_bf16 v[116:119], a[196:199], v[180:183], v[116:119]\n"
"v_mfma_f32_16x16x32_bf16 v[116:119], a[200:203], v[24:27], v[116:119]\n"
"v_mfma_f32_16x16x32_bf16 v[116:119], a[204:207], v[32:35], v[116:119]\n"
"buffer_load_dwordx4 v[128:131], v1, s[4:7], s97 offen sc1\n"
"v_mfma_f32_16x16x32_bf16 v[116:119], a[208:211], v[44:47], v[116:119]\n"
"ds_read_b128 v[132:135], v2 offset:8192\n"
"ds_read_b128 v[140:143], v2 offset:9216\n"
"v_mfma_f32_16x16x32_bf16 v[116:119], a[212:215], v[48:51], v[116:119]\n"
"ds_write_b128 v3, v[100:103] offset:7168\n"
"v_mfma_f32_16x16x32_bf16 v[116:119], a[216:219], v[28:31], v[116:119]\n"
"v_mfma_f32_16x16x32_bf16 v[116:119], a[220:223], v[56:59], v[116:119]\n"
"buffer_load_dwordx4 v[100:103], v1, s[4:7], s97 offen offset:1024 sc1\n"
"v_mfma_f32_16x16x32_bf16 v[152:155], a[252:255], v[56:59], 0\n"
"v_mfma_f32_16x16x32_bf16 v[152:155], a[248:251], v[28:31], v[152:155]\n"
"s_waitcnt lgkmcnt(1)\n"
"v_lshlrev_b32_e32 v254, 16, v132\n"
"v_and_b32_e32 v252, 0xffff0000, v132\n"
"v_lshlrev_b32_e32 v251, 16, v133\n"
"v_mfma_f32_16x16x32_bf16 v[152:155], a[244:247], v[48:51], v[152:155]\n"
"v_and_b32_e32 v250, 0xffff0000, v133\n"
"v_lshlrev_b32_e32 v249, 16, v134\n"
"v_and_b32_e32 v248, 0xffff0000, v134\n"
"v_mfma_f32_16x16x32_bf16 v[152:155], a[240:243], v[44:47], v[152:155]\n"
"buffer_load_dwordx4 v[156:159], v1, s[4:7], s97 offen offset:2048 sc1\n"
"v_lshlrev_b32_e32 v247, 16, v135\n"
"v_mfma_f32_16x16x32_bf16 v[152:155], a[236:239], v[32:35], v[152:155]\n"
"v_and_b32_e32 v246, 0xffff0000, v135\n"
"v_lshlrev_b32_e32 v245, 16, v140\n"
"v_and_b32_e32 v244, 0xffff0000, v140\n"
"v_mfma_f32_16x16x32_bf16 v[152:155], a[232:235], v[24:27], v[152:155]\n"
"ds_write_b128 v3, v[116:119] offset:8192\n"
"v_mfma_f32_16x16x32_bf16 v[152:155], a[228:231], v[180:183], v[152:155]\n"
"v_lshlrev_b32_e32 v243, 16, v141\n"
"v_and_b32_e32 v242, 0xffff0000, v141\n"
"v_lshlrev_b32_e32 v241, 16, v142\n"
"v_mfma_f32_16x16x32_bf16 v[152:155], a[224:227], v[148:151], v[152:155]\n"
"s_waitcnt vmcnt(11)\n"
"buffer_load_dwordx4 v[116:119], v1, s[4:7], s97 offen offset:3072 sc1\n"
"; @B G2 b.2\n"
"v_mfma_f32_16x16x32_bf16 v[132:135], a[128:131], v[40:43], 0\n"
"v_and_b32_e32 v240, 0xffff0000, v142\n"
"v_lshlrev_b32_e32 v239, 16, v143\n"
"v_and_b32_e32 v238, 0xffff0000, v143\n"
"v_mfma_f32_16x16x32_bf16 v[132:135], a[132:135], v[52:55], v[132:135]\n"
"v_mfma_f32_16x16x32_bf16 v[132:135], a[136:139], v[64:67], v[132:135]\n"
"v_mfma_f32_16x16x32_bf16 v[132:135], a[140:143], v[36:39], v[132:135]\n"
"s_waitcnt vmcnt(8)\n"
"v_mfma_f32_16x16x32_bf16 v[132:135], a[144:147], v[72:75], v[132:135]\n"
"v_mfma_f32_16x16x32_bf16 v[132:135], a[148:151], v[60:63], v[132:135]\n"
"ds_write_b128 v3, v[152:155] offset:9216\n"
"v_mfma_f32_16x16x32_bf16 v[132:135], a[152:155], v[80:83], v[132:135]\n"
"v_mfma_f32_16x16x32_bf16 v[132:135], a[156:159], v[68:71], v[132:135]\n"
"v_mfma_f32_16x16x32_bf16 v[140:143], a[188:191], v[68:71], 0\n"
"v_mfma_f32_16x16x32_bf16 v[140:143], a[184:187], v[80:83], v[140:143]\n"
"s_waitcnt lgkmcnt(0)\n"
"s_barrier\n"
"v_mfma_f32_16x16x32_bf16 v[140:143], a[180:183], v[60:63], v[140:143]\n"
"ds_read_b128 v[152:155], v4\n"
"ds_read_b128 v[160:163], v5\n"
"v_mfma_f32_16x16x32_bf16 v[140:143], a[176:179], v[72:75], v[140:143]\n"
"v_mfma_f32_16x16x32_bf16 v[140:143], a[172:175], v[36:39], v[140:143]\n"
"s_add_u32 s97, s56, 0x400000\n"
"v_cmp_u_f32_e32 vcc, v132, v132\n"
"v_mfma_f32_16x16x32_bf16 v[140:143], a[168:171], v[64:67], v[140:143]\n"
"s_cbranch_vccnz .Lmk_p57s\n"
".Lmk_p57r:\n"
"v_mfma_f32_16x16x32_bf16 v[140:143], a[164:167], v[52:55], v[140:143]\n"
"ds_write_b128 v3, v[132:135] offset:12288\n"
"v_mfma_f32_16x16x32_bf16 v[140:143], a[160:163], v[40:43], v[140:143]\n"
"buffer_load_dwordx4 v[132:135], v0, s[4:7], s97 offen sc1\n"
"s_waitcnt lgkmcnt(1)\n"
"v_mfma_f32_16x16x32_bf16 v[164:167], a[192:195], v[40:43], 0\n"
"ds_read_b128 v[168:171], v4 offset:1024\n"
"ds_read_b128 v[172:175], v5 offset:1024\n"
"v_add_f32_e32 v152, v152, v254\n"
"v_mfma_f32_16x16x32_bf16 v[164:167], a[196:199], v[52:55], v[164:167]\n"
"v_add_f32_e32 v153, v153, v252\n"
"v_add_f32_e32 v154, v154, v251\n"
"v_add_f32_e32 v155, v155, v250\n"
"v_mfma_f32_16x16x32_bf16 v[164:167], a[200:203], v[64:67], v[164:167]\n"
"buffer_load_dwordx4 v[184:187], v0, s[4:7], s97 offen offset:1024 sc1\n"
"v_add_f32_e32 v152, v152, v160\n"
"v_mfma_f32_16x16x32_bf16 v[164:167], a[204:207], v[36:39], v[164:167]\n"
"v_add_f32_e32 v153, v153, v161\n"
"v_add_f32_e32 v154, v154, v162\n"
"v_add_f32_e32 v155, v155, v163\n"
"v_mfma_f32_16x16x32_bf16 v[164:167], a[208:211], v[72:75], v[164:167]\n"
"v_mfma_f32_16x16x32_bf16 v[164:167], a[212:215], v[60:63], v[164:167]\n"
"ds_write_b128 v3, v[140:143] offset:13312\n"
"v_mfma_f32_16x16x32_bf16 v[164:167], a[216:219], v[80:83], v[164:167]\n"
"buffer_load_dwordx4 v[140:143], v0, s[4:7], s97 offen offset:2048 sc1\n"
"s_waitcnt lgkmcnt(1)\n"
"v_mfma_f32_16x16x32_bf16 v[164:167], a[220:223], v[68:71], v[164:167]\n"
"ds_read_b128 v[160:163], v4 offset:2048\n"
"ds_read_b128 v[200:203], v5 offset:2048\n"
"v_add_f32_e32 v168, v168, v249\n"
"v_mfma_f32_16x16x32_bf16 v[204:207], a[252:255], v[68:71], 0\n"
"v_add_f32_e32 v169, v169, v248\n"
"v_add_f32_e32 v170, v170, v247\n"
"v_add_f32_e32 v171, v171, v246\n"
"v_mfma_f32_16x16x32_bf16 v[204:207], a[248:251], v[80:83], v[204:207]\n"
"buffer_load_dwordx4 v[68:71], v0, s[4:7], s97 offen offset:3072 sc1\n"
"v_add_f32_e32 v168, v168, v172\n"
"v_mfma_f32_16x16x32_bf16 v[204:207], a[244:247], v[60:63], v[204:207]\n"
"v_add_f32_e32 v169, v169, v173\n"
"v_add_f32_e32 v170, v170, v174\n"
"v_add_f32_e32 v171, v171, v175\n"
"v_mfma_f32_16x16x32_bf16 v[204:207], a[240:243], v[72:75], v[204:207]\n"
"v_mfma_f32_16x16x32_bf16 v[204:207], a[236:239], v[36:39], v[204:207]\n"
"buffer_load_dwordx4 v[36:39], v1, s[4:7], s97 offen sc1\n"
"s_waitcnt lgkmcnt(0)\n"
"v_mfma_f32_16x16x32_bf16 v[204:207], a[232:235], v[64:67], v[204:207]\n"
"ds_write_b128 v3, v[164:167] offset:14336\n"
"v_mfma_f32_16x16x32_bf16 v[204:207], a[228:231], v[52:55], v[204:207]\n"
"ds_read_b128 v[52:55], v4 offset:3072\n"
"ds_read_b128 v[60:63], v5 offset:3072\n"
"v_add_f32_e32 v160, v160, v245\n"
"v_mfma_f32_16x16x32_bf16 v[204:207], a[224:227], v[40:43], v[204:207]\n"
"s_waitcnt vmcnt(9)\n"
"buffer_load_dwordx4 v[40:43], v1, s[4:7], s97 offen offset:1024 sc1\n"
"; @B G2 b.3\n"
"v_mfma_f32_16x16x32_bf16 v[64:67], a[128:131], v[96:99], v[152:155]\n"
"v_add_f32_e32 v161, v161, v244\n"
"v_add_f32_e32 v162, v162, v243\n"
"v_add_f32_e32 v163, v163, v242\n"
"v_mfma_f32_16x16x32_bf16 v[64:67], a[132:135], v[76:79], v[64:67]\n"
"v_add_f32_e32 v160, v160, v200\n"
"v_add_f32_e32 v161, v161, v201\n"
"v_add_f32_e32 v162, v162, v202\n"
"v_mfma_f32_16x16x32_bf16 v[64:67], a[136:139], v[112:115], v[64:67]\n"
"buffer_load_dwordx4 v[72:75], v1, s[4:7], s97 offen offset:2048 sc1\n"
"v_add_f32_e32 v163, v163, v203\n"
"v_mfma_f32_16x16x32_bf16 v[64:67], a[140:143], v[84:87], v[64:67]\n"
"s_waitcnt vmcnt(7)\n"
"s_waitcnt lgkmcnt(0)\n"
"v_add_f32_e32 v52, v52, v241\n"
"v_add_f32_e32 v53, v53, v240\n"
"v_mfma_f32_16x16x32_bf16 v[64:67], a[144:147], v[128:131], v[64:67]\n"
"v_add_f32_e32 v54, v54, v239\n"
"v_add_f32_e32 v55, v55, v238\n"
"v_add_f32_e32 v52, v52, v60\n"
"v_mfma_f32_16x16x32_bf16 v[64:67], a[148:151], v[100:103], v[64:67]\n"
"ds_write_b128 v3, v[204:207] offset:15360\n"
"v_mfma_f32_16x16x32_bf16 v[64:67], a[152:155], v[156:159], v[64:67]\n"
"buffer_load_dwordx4 v[80:83], v1, s[4:7], s97 offen offset:3072 sc1\n"
"v_add_f32_e32 v53, v53, v61\n"
"v_mfma_f32_16x16x32_bf16 v[64:67], a[156:159], v[116:119], v[64:67]\n"
"s_add_u32 s97, s57, 0x400000\n"
"v_add_f32_e32 v54, v54, v62\n"
"v_add_f32_e32 v55, v55, v63\n"
"v_mfma_f32_16x16x32_bf16 v[60:63], a[188:191], v[116:119], v[168:171]\n"
"v_mfma_f32_16x16x32_bf16 v[60:63], a[184:187], v[156:159], v[60:63]\n"
"s_waitcnt lgkmcnt(0)\n"
"s_barrier\n"
"v_mfma_f32_16x16x32_bf16 v[60:63], a[180:183], v[100:103], v[60:63]\n"
"buffer_load_dwordx4 v[164:167], v0, s[4:7], s97 offen sc1\n"
"v_mfma_f32_16x16x32_bf16 v[60:63], a[176:179], v[128:131], v[60:63]\n"
"ds_read_b128 v[172:175], v6\n"
"ds_read_b128 v[200:203], v6 offset:1024\n"
"v_mfma_f32_16x16x32_bf16 v[60:63], a[172:175], v[84:87], v[60:63]\n"
"ds_read_b128 v[204:207], v6 offset:2048\n"
"v_cmp_u_f32_e32 vcc, v64, v64\n"
"v_mfma_f32_16x16x32_bf16 v[60:63], a[168:171], v[112:115], v[60:63]\n"
"s_cbranch_vccnz .Lmk_p58s\n"
".Lmk_p58r:\n"
"ds_read_b128 v[152:155], v6 offset:3072\n"
"v_mfma_f32_16x16x32_bf16 v[60:63], a[164:167], v[76:79], v[60:63]\n"
"buffer_load_dwordx4 v[168:171], v0, s[4:7], s97 offen offset:1024 sc1\n"
"v_mfma_f32_16x16x32_bf16 v[60:63], a[160:163], v[96:99], v[60:63]\n"
"v_mfma_f32_16x16x32_bf16 v[160:163], a[192:195], v[96:99], v[160:163]\n"
"v_mfma_f32_16x16x32_bf16 v[160:163], a[196:199], v[76:79], v[160:163]\n"
"buffer_load_dwordx4 v[208:211], v0, s[4:7], s97 offen offset:2048 sc1\n"
"v_mfma_f32_16x16x32_bf16 v[160:163], a[200:203], v[112:115], v[160:163]\n"
"v_mfma_f32_16x16x32_bf16 v[160:163], a[204:207], v[84:87], v[160:163]\n"
"v_mfma_f32_16x16x32_bf16 v[160:163], a[208:211], v[128:131], v[160:163]\n"
"buffer_load_dwordx4 v[212:215], v0, s[4:7], s97 offen offset:3072 sc1\n"
"s_waitcnt lgkmcnt(0)\n"
"v_mfma_f32_16x16x32_bf16 v[160:163], a[212:215], v[100:103], v[160:163]\n"
"v_add_f32_e32 v172, v64, v172\n"
"v_add_f32_e32 v173, v65, v173\n"
"v_add_f32_e32 v174, v66, v174\n"
"v_mfma_f32_16x16x32_bf16 v[160:163], a[216:219], v[156:159], v[160:163]\n"
"v_add_f32_e32 v175, v67, v175\n"
"v_add_f32_e32 v200, v60, v200\n"
"v_add_f32_e32 v201, v61, v201\n"
"v_mfma_f32_16x16x32_bf16 v[160:163], a[220:223], v[116:119], v[160:163]\n"
"buffer_load_dwordx4 v[64:67], v1, s[4:7], s97 offen sc1\n"
"v_add_f32_e32 v202, v62, v202\n"
"v_mfma_f32_16x16x32_bf16 v[52:55], a[252:255], v[116:119], v[52:55]\n"
"v_add_f32_e32 v203, v63, v203\n"
"v_cvt_pk_bf16_f32 v60, v172, v173\n"
"v_cvt_pk_bf16_f32 v61, v174, v175\n"
"v_mfma_f32_16x16x32_bf16 v[52:55], a[248:251], v[156:159], v[52:55]\n"
"v_cvt_pk_bf16_f32 v62, v200, v201\n"
"v_cvt_pk_bf16_f32 v63, v202, v203\n"
"v_xor_b32_e32 v254, 32, v255\n"
"v_mfma_f32_16x16x32_bf16 v[52:55], a[244:247], v[100:103], v[52:55]\n"
"buffer_load_dwordx4 v[100:103], v1, s[4:7], s97 offen offset:1024 sc1\n"
"v_mfma_f32_16x16x32_bf16 v[52:55], a[240:243], v[128:131], v[52:55]\n"
"ds_write_b64 v255, v[60:61] offset:8192\n"
"v_mfma_f32_16x16x32_bf16 v[52:55], a[236:239], v[84:87], v[52:55]\n"
"ds_write_b64 v254, v[62:63] offset:8192\n"
"v_mfma_f32_16x16x32_bf16 v[52:55], a[232:235], v[112:115], v[52:55]\n"
"buffer_load_dwordx4 v[60:63], v1, s[4:7], s97 offen offset:2048 sc1\n"
"v_mfma_f32_16x16x32_bf16 v[52:55], a[228:231], v[76:79], v[52:55]\n"
"v_mfma_f32_16x16x32_bf16 v[52:55], a[224:227], v[96:99], v[52:55]\n"
"buffer_load_dwordx4 v[76:79], v1, s[4:7], s97 offen offset:3072 sc1\n"
"s_waitcnt vmcnt(14)\n"
"; @B G2 a.0\n"
"v_mfma_f32_16x16x32_bf16 v[84:87], a[128:131], v[132:135], 0\n"
"s_add_u32 s97, s58, 0x400000\n"
"buffer_load_dwordx4 v[96:99], v0, s[4:7], s97 offen sc1\n"
"v_mfma_f32_16x16x32_bf16 v[84:87], a[132:135], v[184:187], v[84:87]\n"
"s_waitcnt vmcnt(13)\n"
"v_add_f32_e32 v204, v160, v204\n"
"v_add_f32_e32 v205, v161, v205\n"
"v_mfma_f32_16x16x32_bf16 v[84:87], a[136:139], v[140:143], v[84:87]\n"
"v_add_f32_e32 v206, v162, v206\n"
"v_add_f32_e32 v207, v163, v207\n"
"v_mfma_f32_16x16x32_bf16 v[84:87], a[140:143], v[68:71], v[84:87]\n"
"s_waitcnt vmcnt(11)\n"
"buffer_load_dwordx4 v[112:115], v0, s[4:7], s97 offen offset:1024 sc1\n"
"v_mfma_f32_16x16x32_bf16 v[84:87], a[144:147], v[36:39], v[84:87]\n"
"v_add_f32_e32 v152, v52, v152\n"
"v_add_f32_e32 v153, v53, v153\n"
"v_mfma_f32_16x16x32_bf16 v[84:87], a[148:151], v[40:43], v[84:87]\n"
"s_waitcnt vmcnt(10)\n"
"v_add_f32_e32 v154, v54, v154\n"
"v_add_f32_e32 v155, v55, v155\n"
"v_mfma_f32_16x16x32_bf16 v[84:87], a[152:155], v[72:75], v[84:87]\n"
"buffer_load_dwordx4 v[52:55], v0, s[4:7], s97 offen offset:2048 sc1\n"
"v_mfma_f32_16x16x32_bf16 v[84:87], a[156:159], v[80:83], v[84:87]\n"
"v_cvt_pk_bf16_f32 v116, v204, v205\n"
"v_cvt_pk_bf16_f32 v117, v206, v207\n"
"v_mfma_f32_16x16x32_bf16 v[128:131], a[188:191], v[80:83], 0\n"
"v_cvt_pk_bf16_f32 v118, v152, v153\n"
"v_cvt_pk_bf16_f32 v119, v154, v155\n"
"v_mfma_f32_16x16x32_bf16 v[128:131], a[184:187], v[72:75], v[128:131]\n"
"buffer_load_dwordx4 v[152:155], v0, s[4:7], s97 offen offset:3072 sc1\n"
"v_mfma_f32_16x16x32_bf16 v[128:131], a[180:183], v[40:43], v[128:131]\n"
"v_xor_b32_e32 v254, 64, v255\n"
"v_xor_b32_e32 v252, 0x60, v255\n"
"v_mfma_f32_16x16x32_bf16 v[128:131], a[176:179], v[36:39], v[128:131]\n"
"ds_write_b64 v254, v[116:117] offset:8192\n"
"v_mfma_f32_16x16x32_bf16 v[128:131], a[172:175], v[68:71], v[128:131]\n"
"v_cmp_u_f32_e32 vcc, v84, v84\n"
"v_mfma_f32_16x16x32_bf16 v[128:131], a[168:171], v[140:143], v[128:131]\n"
"s_cbranch_vccnz .Lmk_p59s\n"
".Lmk_p59r:\n"
"v_mfma_f32_16x16x32_bf16 v[128:131], a[164:167], v[184:187], v[128:131]\n"
"ds_write_b128 v3, v[84:87]\n"
"v_mfma_f32_16x16x32_bf16 v[128:131], a[160:163], v[132:135], v[128:131]\n"
"buffer_load_dwordx4 v[84:87], v1, s[4:7], s97 offen sc1\n"
"v_mfma_f32_16x16x32_bf16 v[156:159], a[192:195], v[132:135], 0\n"
"ds_write_b64 v252, v[118:119] offset:8192\n"
"v_mfma_f32_16x16x32_bf16 v[156:159], a[196:199], v[184:187], v[156:159]\n"
"ds_read_b128 v[116:119], v2 offset:8192\n"
"ds_read_b128 v[160:163], v2 offset:9216\n"
"v_mfma_f32_16x16x32_bf16 v[156:159], a[200:203], v[140:143], v[156:159]\n"
"buffer_load_dwordx4 v[172:175], v1, s[4:7], s97 offen offset:1024 sc1\n"
"v_mfma_f32_16x16x32_bf16 v[156:159], a[204:207], v[68:71], v[156:159]\n"
"v_bfe_u32 v255, v0, 4, 4\n"
"v_bfe_u32 v254, v0, 9, 1\n"
"v_mfma_f32_16x16x32_bf16 v[156:159], a[208:211], v[36:39], v[156:159]\n"
"v_and_b32_e32 v252, 7, v255\n"
"v_xor_b32_e32 v254, v254, v252\n"
"v_mfma_f32_16x16x32_bf16 v[156:159], a[212:215], v[40:43], v[156:159]\n"
"ds_write_b128 v3, v[128:131] offset:1024\n"
"v_mfma_f32_16x16x32_bf16 v[156:159], a[216:219], v[72:75], v[156:159]\n"
"buffer_load_dwordx4 v[128:131], v1, s[4:7], s97 offen offset:2048 sc1\n"
"v_mfma_f32_16x16x32_bf16 v[156:159], a[220:223], v[80:83], v[156:159]\n"
"s_waitcnt lgkmcnt(1)\n"
"v_lshlrev_b32_e32 v255, 7, v255\n"
"v_lshl_add_u32 v255, v254, 4, v255\n"
"v_mfma_f32_16x16x32_bf16 v[200:203], a[252:255], v[80:83], 0\n"
"v_bfe_u32 v254, v0, 8, 1\n"
"v_lshl_add_u32 v255, v254, 3, v255\n"
"v_mfma_f32_16x16x32_bf16 v[200:203], a[248:251], v[72:75], v[200:203]\n"
"buffer_load_dwordx4 v[72:75], v1, s[4:7], s97 offen offset:3072 sc1\n"
"v_mfma_f32_16x16x32_bf16 v[200:203], a[244:247], v[40:43], v[200:203]\n"
"s_add_u32 s97, s59, 0x400000\n"
"v_add_u32_e32 v255, s77, v255\n"
"v_bfe_u32 v252, v0, 7, 3\n"
"v_mfma_f32_16x16x32_bf16 v[200:203], a[240:243], v[36:39], v[200:203]\n"
"v_bfe_u32 v254, v0, 4, 3\n"
"v_xor_b32_e32 v254, v254, v252\n"
"v_mfma_f32_16x16x32_bf16 v[200:203], a[236:239], v[68:71], v[200:203]\n"
"buffer_load_dwordx4 v[36:39], v0, s[4:7], s97 offen sc1\n"
"v_mfma_f32_16x16x32_bf16 v[200:203], a[232:235], v[140:143], v[200:203]\n"
"ds_write_b128 v3, v[156:159] offset:2048\n"
"v_mfma_f32_16x16x32_bf16 v[200:203], a[228:231], v[184:187], v[200:203]\n"
"v_lshlrev_b32_e32 v252, 11, v252\n"
"v_lshl_add_u32 v252, v254, 4, v252\n"
"v_mfma_f32_16x16x32_bf16 v[200:203], a[224:227], v[132:135], v[200:203]\n"
"s_waitcnt vmcnt(13)\n"
"buffer_load_dwordx4 v[40:43], v0, s[4:7], s97 offen offset:1024 sc1\n"
"; @B G2 a.1\n"
"v_mfma_f32_16x16x32_bf16 v[68:71], a[128:131], v[164:167], 0\n"
"v_mfma_f32_16x16x32_bf16 v[68:71], a[132:135], v[168:171], v[68:71]\n"
"v_mfma_f32_16x16x32_bf16 v[68:71], a[136:139], v[208:211], v[68:71]\n"
"buffer_load_dwordx4 v[80:83], v0, s[4:7], s97 offen offset:2048 sc1\n"
"v_mfma_f32_16x16x32_bf16 v[68:71], a[140:143], v[212:215], v[68:71]\n"
"s_waitcnt vmcnt(11)\n"
"v_mfma_f32_16x16x32_bf16 v[68:71], a[144:147], v[64:67], v[68:71]\n"
"v_mfma_f32_16x16x32_bf16 v[68:71], a[148:151], v[100:103], v[68:71]\n"
"ds_write_b128 v3, v[200:203] offset:3072\n"
"v_mfma_f32_16x16x32_bf16 v[68:71], a[152:155], v[60:63], v[68:71]\n"
"buffer_load_dwordx4 v[132:135], v0, s[4:7], s97 offen offset:3072 sc1\n"
"v_mfma_f32_16x16x32_bf16 v[68:71], a[156:159], v[76:79], v[68:71]\n"
"v_mfma_f32_16x16x32_bf16 v[140:143], a[188:191], v[76:79], 0\n"
"v_mfma_f32_16x16x32_bf16 v[140:143], a[184:187], v[60:63], v[140:143]\n"
"buffer_load_dwordx4 v[156:159], v1, s[4:7], s97 offen sc1\n"
"v_mfma_f32_16x16x32_bf16 v[140:143], a[180:183], v[100:103], v[140:143]\n"
"v_mfma_f32_16x16x32_bf16 v[140:143], a[176:179], v[64:67], v[140:143]\n"
"v_mfma_f32_16x16x32_bf16 v[140:143], a[172:175], v[212:215], v[140:143]\n"
"s_nop 1\n"
"v_cmp_u_f32_e32 vcc, v68, v68\n"
"v_mfma_f32_16x16x32_bf16 v[140:143], a[168:171], v[208:211], v[140:143]\n"
"s_cbranch_vccnz .Lmk_p60s\n"
".Lmk_p60r:\n"
"v_mfma_f32_16x16x32_bf16 v[140:143], a[164:167], v[168:171], v[140:143]\n"
"ds_write_b128 v3, v[68:71] offset:6144\n"
"v_mfma_f32_16x16x32_bf16 v[140:143], a[160:163], v[164:167], v[140:143]\n"
"buffer_load_dwordx4 v[68:71], v1, s[4:7], s97 offen offset:1024 sc1\n"
"v_mfma_f32_16x16x32_bf16 v[184:187], a[192:195], v[164:167], 0\n"
"v_mfma_f32_16x16x32_bf16 v[184:187], a[196:199], v[168:171], v[184:187]\n"
"v_mfma_f32_16x16x32_bf16 v[184:187], a[200:203], v[208:211], v[184:187]\n"
"buffer_load_dwordx4 v[200:203], v1, s[4:7], s97 offen offset:2048 sc1\n"
"v_mfma_f32_16x16x32_bf16 v[184:187], a[204:207], v[212:215], v[184:187]\n"
"v_mfma_f32_16x16x32_bf16 v[184:187], a[208:211], v[64:67], v[184:187]\n"
"ds_read_b128 v[204:207], v2\n"
"ds_read_b128 v[216:219], v2 offset:1024\n"
"v_mfma_f32_16x16x32_bf16 v[184:187], a[212:215], v[100:103], v[184:187]\n"
"ds_write_b128 v3, v[140:143] offset:7168\n"
"v_mfma_f32_16x16x32_bf16 v[184:187], a[216:219], v[60:63], v[184:187]\n"
"buffer_load_dwordx4 v[140:143], v1, s[4:7], s97 offen offset:3072 sc1\n"
"v_mfma_f32_16x16x32_bf16 v[184:187], a[220:223], v[76:79], v[184:187]\n"
"v_mfma_f32_16x16x32_bf16 v[220:223], a[252:255], v[76:79], 0\n"
"v_mfma_f32_16x16x32_bf16 v[220:223], a[248:251], v[60:63], v[220:223]\n"
"s_waitcnt lgkmcnt(1)\n"
"v_lshlrev_b32_e32 v254, 16, v204\n"
"v_and_b32_e32 v251, 0xffff0000, v204\n"
"v_lshlrev_b32_e32 v250, 16, v205\n"
"v_mfma_f32_16x16x32_bf16 v[220:223], a[244:247], v[100:103], v[220:223]\n"
"v_and_b32_e32 v249, 0xffff0000, v205\n"
"v_lshlrev_b32_e32 v248, 16, v206\n"
"v_and_b32_e32 v247, 0xffff0000, v206\n"
"v_mfma_f32_16x16x32_bf16 v[220:223], a[240:243], v[64:67], v[220:223]\n"
"v_lshlrev_b32_e32 v246, 16, v207\n"
"v_and_b32_e32 v245, 0xffff0000, v207\n"
"v_lshlrev_b32_e32 v244, 16, v216\n"
"v_mfma_f32_16x16x32_bf16 v[220:223], a[236:239], v[212:215], v[220:223]\n"
"s_add_u32 s97, s74, 0x40000\n"
"buffer_store_dwordx4 v[116:119], v253, s[16:19], s97 offen sc1\n"
"v_mfma_f32_16x16x32_bf16 v[220:223], a[232:235], v[208:211], v[220:223]\n"
"ds_write_b128 v3, v[184:187] offset:8192\n"
"v_mfma_f32_16x16x32_bf16 v[220:223], a[228:231], v[168:171], v[220:223]\n"
"v_and_b32_e32 v243, 0xffff0000, v216\n"
"v_lshlrev_b32_e32 v242, 16, v217\n"
"v_and_b32_e32 v241, 0xffff0000, v217\n"
"v_mfma_f32_16x16x32_bf16 v[220:223], a[224:227], v[164:167], v[220:223]\n"
"s_waitcnt vmcnt(13)\n"
"v_lshlrev_b32_e32 v240, 16, v218\n"
"v_and_b32_e32 v239, 0xffff0000, v218\n"
"v_lshlrev_b32_e32 v238, 16, v219\n"
"; @B G2 a.2\n"
"v_mfma_f32_16x16x32_bf16 v[60:63], a[128:131], v[96:99], 0\n"
"v_and_b32_e32 v237, 0xffff0000, v219\n"
"v_mfma_f32_16x16x32_bf16 v[60:63], a[132:135], v[112:115], v[60:63]\n"
"v_mfma_f32_16x16x32_bf16 v[60:63], a[136:139], v[52:55], v[60:63]\n"
"v_mfma_f32_16x16x32_bf16 v[60:63], a[140:143], v[152:155], v[60:63]\n"
"s_waitcnt vmcnt(9)\n"
"v_mfma_f32_16x16x32_bf16 v[60:63], a[144:147], v[84:87], v[60:63]\n"
"v_mfma_f32_16x16x32_bf16 v[60:63], a[148:151], v[172:175], v[60:63]\n"
"ds_write_b128 v3, v[220:223] offset:9216\n"
"v_mfma_f32_16x16x32_bf16 v[60:63], a[152:155], v[128:131], v[60:63]\n"
"s_add_u32 s97, s74, 0x44000\n"
"v_mfma_f32_16x16x32_bf16 v[60:63], a[156:159], v[72:75], v[60:63]\n"
"v_mfma_f32_16x16x32_bf16 v[64:67], a[188:191], v[72:75], 0\n"
"buffer_store_dwordx4 v[160:163], v253, s[16:19], s97 offen sc1\n"
"v_mfma_f32_16x16x32_bf16 v[64:67], a[184:187], v[128:131], v[64:67]\n"
"s_waitcnt lgkmcnt(0)\n"
"s_barrier\n"
"v_mfma_f32_16x16x32_bf16 v[64:67], a[180:183], v[172:175], v[64:67]\n"
"ds_read_b128 v[76:79], v4\n"
"ds_read_b128 v[100:103], v5\n"
"v_mfma_f32_16x16x32_bf16 v[64:67], a[176:179], v[84:87], v[64:67]\n"
"v_mfma_f32_16x16x32_bf16 v[64:67], a[172:175], v[152:155], v[64:67]\n"
"v_cmp_u_f32_e32 vcc, v60, v60\n"
"v_mfma_f32_16x16x32_bf16 v[64:67], a[168:171], v[52:55], v[64:67]\n"
"s_cbranch_vccnz .Lmk_p61s\n"
".Lmk_p61r:\n"
"v_mfma_f32_16x16x32_bf16 v[64:67], a[164:167], v[112:115], v[64:67]\n"
"ds_write_b128 v3, v[60:63] offset:12288\n"
"v_mfma_f32_16x16x32_bf16 v[64:67], a[160:163], v[96:99], v[64:67]\n"
"s_waitcnt lgkmcnt(1)\n"
"v_add_f32_e32 v76, v76, v254\n"
"v_add_f32_e32 v77, v77, v251\n"
"v_add_f32_e32 v78, v78, v250\n"
"v_mfma_f32_16x16x32_bf16 v[60:63], a[192:195], v[96:99], 0\n"
"ds_read_b128 v[116:119], v4 offset:1024\n"
"ds_read_b128 v[160:163], v5 offset:1024\n"
"v_add_f32_e32 v79, v79, v249\n"
"v_mfma_f32_16x16x32_bf16 v[60:63], a[196:199], v[112:115], v[60:63]\n"
"v_add_f32_e32 v76, v76, v100\n"
"v_add_f32_e32 v77, v77, v101\n"
"v_add_f32_e32 v78, v78, v102\n"
"v_mfma_f32_16x16x32_bf16 v[60:63], a[200:203], v[52:55], v[60:63]\n"
"v_add_f32_e32 v79, v79, v103\n"
"v_mfma_f32_16x16x32_bf16 v[60:63], a[204:207], v[152:155], v[60:63]\n"
"v_mfma_f32_16x16x32_bf16 v[60:63], a[208:211], v[84:87], v[60:63]\n"
"v_mfma_f32_16x16x32_bf16 v[60:63], a[212:215], v[172:175], v[60:63]\n"
"ds_write_b128 v3, v[64:67] offset:13312\n"
"v_mfma_f32_16x16x32_bf16 v[60:63], a[216:219], v[128:131], v[60:63]\n"
"ds_read_b128 v[64:67], v4 offset:2048\n"
"ds_read_b128 v[100:103], v5 offset:2048\n"
"s_waitcnt lgkmcnt(3)\n"
"v_mfma_f32_16x16x32_bf16 v[60:63], a[220:223], v[72:75], v[60:63]\n"
"v_add_f32_e32 v116, v116, v248\n"
"v_add_f32_e32 v117, v117, v247\n"
"v_add_f32_e32 v118, v118, v246\n"
"v_mfma_f32_16x16x32_bf16 v[164:167], a[252:255], v[72:75], 0\n"
"v_add_f32_e32 v119, v119, v245\n"
"v_add_f32_e32 v116, v116, v160\n"
"v_add_f32_e32 v117, v117, v161\n"
"v_mfma_f32_16x16x32_bf16 v[164:167], a[248:251], v[128:131], v[164:167]\n"
"v_add_f32_e32 v118, v118, v162\n"
"v_add_f32_e32 v119, v119, v163\n"
"v_mfma_f32_16x16x32_bf16 v[164:167], a[244:247], v[172:175], v[164:167]\n"
"v_mfma_f32_16x16x32_bf16 v[164:167], a[240:243], v[84:87], v[164:167]\n"
"s_waitcnt lgkmcnt(0)\n"
"v_add_f32_e32 v64, v64, v244\n"
"v_add_f32_e32 v65, v65, v243\n"
"v_add_f32_e32 v66, v66, v242\n"
"v_mfma_f32_16x16x32_bf16 v[164:167], a[236:239], v[152:155], v[164:167]\n"
"ds_read_b128 v[72:75], v4 offset:3072\n"
"ds_read_b128 v[84:87], v5 offset:3072\n"
"v_add_f32_e32 v67, v67, v241\n"
"v_mfma_f32_16x16x32_bf16 v[164:167], a[232:235], v[52:55], v[164:167]\n"
"ds_write_b128 v3, v[60:63] offset:14336\n"
"v_mfma_f32_16x16x32_bf16 v[164:167], a[228:231], v[112:115], v[164:167]\n"
"v_add_f32_e32 v64, v64, v100\n"
"v_add_f32_e32 v65, v65, v101\n"
"v_add_f32_e32 v66, v66, v102\n"
"v_mfma_f32_16x16x32_bf16 v[164:167], a[224:227], v[96:99], v[164:167]\n"
"s_waitcnt vmcnt(6)\n"
"v_add_f32_e32 v67, v67, v103\n"
"; @B G2 a.3\n"
"v_mfma_f32_16x16x32_bf16 v[52:55], a[128:131], v[36:39], v[76:79]\n"
"v_mfma_f32_16x16x32_bf16 v[52:55], a[132:135], v[40:43], v[52:55]\n"
"s_waitcnt lgkmcnt(1)\n"
"v_add_f32_e32 v72, v72, v240\n"
"v_add_f32_e32 v73, v73, v239\n"
"v_add_f32_e32 v74, v74, v238\n"
"v_mfma_f32_16x16x32_bf16 v[52:55], a[136:139], v[80:83], v[52:55]\n"
"v_add_f32_e32 v75, v75, v237\n"
"v_add_f32_e32 v72, v72, v84\n"
"v_add_f32_e32 v73, v73, v85\n"
"v_mfma_f32_16x16x32_bf16 v[52:55], a[140:143], v[132:135], v[52:55]\n"
"s_waitcnt vmcnt(2)\n"
"v_add_f32_e32 v74, v74, v86\n"
"v_add_f32_e32 v75, v75, v87\n"
"v_mfma_f32_16x16x32_bf16 v[52:55], a[144:147], v[156:159], v[52:55]\n"
"v_mfma_f32_16x16x32_bf16 v[52:55], a[148:151], v[68:71], v[52:55]\n"
"ds_write_b128 v3, v[164:167] offset:15360\n"
"v_mfma_f32_16x16x32_bf16 v[52:55], a[152:155], v[200:203], v[52:55]\n"
"v_mfma_f32_16x16x32_bf16 v[52:55], a[156:159], v[140:143], v[52:55]\n"
"v_mfma_f32_16x16x32_bf16 v[60:63], a[188:191], v[140:143], v[116:119]\n"
"v_mfma_f32_16x16x32_bf16 v[60:63], a[184:187], v[200:203], v[60:63]\n"
"s_waitcnt lgkmcnt(0)\n"
"s_barrier\n"
"v_mfma_f32_16x16x32_bf16 v[60:63], a[180:183], v[68:71], v[60:63]\n"
"ds_read_b128 v[84:87], v6\n"
"ds_read_b128 v[96:99], v6 offset:1024\n"
"v_mfma_f32_16x16x32_bf16 v[60:63], a[176:179], v[156:159], v[60:63]\n"
"ds_read_b128 v[100:103], v6 offset:2048\n"
"ds_read_b128 v[112:115], v6 offset:3072\n"
"v_mfma_f32_16x16x32_bf16 v[60:63], a[172:175], v[132:135], v[60:63]\n"
"v_cmp_u_f32_e32 vcc, v52, v52\n"
"v_mfma_f32_16x16x32_bf16 v[60:63], a[168:171], v[80:83], v[60:63]\n"
"s_cbranch_vccnz .Lmk_p62s\n"
".Lmk_p62r:\n"
"v_mfma_f32_16x16x32_bf16 v[60:63], a[164:167], v[40:43], v[60:63]\n"
"v_mfma_f32_16x16x32_bf16 v[60:63], a[160:163], v[36:39], v[60:63]\n"
"v_mfma_f32_16x16x32_bf16 v[64:67], a[192:195], v[36:39], v[64:67]\n"
"v_mfma_f32_16x16x32_bf16 v[64:67], a[196:199], v[40:43], v[64:67]\n"
"v_mfma_f32_16x16x32_bf16 v[64:67], a[200:203], v[80:83], v[64:67]\n"
"v_mfma_f32_16x16x32_bf16 v[64:67], a[204:207], v[132:135], v[64:67]\n"
"v_mfma_f32_16x16x32_bf16 v[64:67], a[208:211], v[156:159], v[64:67]\n"
"s_waitcnt lgkmcnt(0)\n"
"v_add_f32_e32 v84, v52, v84\n"
"v_add_f32_e32 v85, v53, v85\n"
"v_add_f32_e32 v86, v54, v86\n"
"v_mfma_f32_16x16x32_bf16 v[64:67], a[212:215], v[68:71], v[64:67]\n"
"v_add_f32_e32 v87, v55, v87\n"
"v_add_f32_e32 v96, v60, v96\n"
"v_add_f32_e32 v97, v61, v97\n"
"v_mfma_f32_16x16x32_bf16 v[64:67], a[216:219], v[200:203], v[64:67]\n"
"v_add_f32_e32 v98, v62, v98\n"
"v_add_f32_e32 v99, v63, v99\n"
"v_cvt_pk_bf16_f32 v52, v84, v85\n"
"v_mfma_f32_16x16x32_bf16 v[64:67], a[220:223], v[140:143], v[64:67]\n"
"v_cvt_pk_bf16_f32 v53, v86, v87\n"
"v_cvt_pk_bf16_f32 v54, v96, v97\n"
"v_cvt_pk_bf16_f32 v55, v98, v99\n"
"v_mfma_f32_16x16x32_bf16 v[72:75], a[252:255], v[140:143], v[72:75]\n"
"v_xor_b32_e32 v254, 32, v255\n"
"v_mfma_f32_16x16x32_bf16 v[72:75], a[248:251], v[200:203], v[72:75]\n"
"ds_write_b64 v255, v[52:53] offset:0\n"
"v_mfma_f32_16x16x32_bf16 v[72:75], a[244:247], v[68:71], v[72:75]\n"
"ds_write_b64 v254, v[54:55] offset:0\n"
"v_mfma_f32_16x16x32_bf16 v[72:75], a[240:243], v[156:159], v[72:75]\n"
"v_mfma_f32_16x16x32_bf16 v[72:75], a[236:239], v[132:135], v[72:75]\n"
"v_mfma_f32_16x16x32_bf16 v[72:75], a[232:235], v[80:83], v[72:75]\n"
"v_mfma_f32_16x16x32_bf16 v[72:75], a[228:231], v[40:43], v[72:75]\n"
"v_mfma_f32_16x16x32_bf16 v[72:75], a[224:227], v[36:39], v[72:75]\n"
"v_add_f32_e32 v100, v64, v100\n"
"v_add_f32_e32 v101, v65, v101\n"
"v_add_f32_e32 v102, v66, v102\n"
"v_add_f32_e32 v103, v67, v103\n"
"s_nop 3\n"
"v_add_f32_e32 v112, v72, v112\n"
"v_add_f32_e32 v113, v73, v113\n"
"v_add_f32_e32 v114, v74, v114\n"
"v_add_f32_e32 v115, v75, v115\n"
"v_cvt_pk_bf16_f32 v36, v100, v101\n"
"v_cvt_pk_bf16_f32 v37, v102, v103\n"
"v_cvt_pk_bf16_f32 v38, v112, v113\n"
"v_cvt_pk_bf16_f32 v39, v114, v115\n"
"v_xor_b32_e32 v254, 64, v255\n"
"v_xor_b32_e32 v253, 0x60, v255\n"
"ds_write_b64 v254, v[36:37] offset:0\n"
"ds_write_b64 v253, v[38:39] offset:0\n"
"ds_read_b128 v[36:39], v2\n"
"ds_read_b128 v[40:43], v2 offset:1024\n"
"s_waitcnt lgkmcnt(0)\n"
"buffer_store_dwordx4 v[36:39], v252, s[16:19], s74 offen sc1\n"
"s_add_u32 s97, s74, 0x4000\n"
"buffer_store_dwordx4 v[40:43], v252, s[16:19], s97 offen sc1\n"
"s_cmp_eq_u32 s81, 0\n"
"s_cbranch_scc1 .Lmk_fin\n"
"buffer_store_dwordx2 v[18:19], v11, s[4:7], s81 offen sc1\n"
".Lmk_fin:\n"
"s_endpgm\n"
".Lmk_end:\n"
    :: "{s[4:7]}"(wsr), "{s[8:11]}"(wsr), "{s[12:15]}"(wsk), "{s[16:19]}"(outr), "{s[52:67]}"(sa), "{s[68:83]}"(sb),
       "{s[20:27]}"(pa), "{s[28:31]}"(wa), "{s[32:35]}"(wb), "{s[36:39]}"(oa), "{s[40:43]}"(ob), "{s[44:51]}"(pc), "{s99}"(uni((bix * 4 + w) * 1024)),
       "{v[0:15]}"(va), "{v[16:23]}"(vb), "{v[32:47]}"(pz), "{v48}"(sta), "{v50}"(pf)
    : "memory", "v255", "a255", "s98", "vcc");
  __builtin_unreachable();
}

}

extern "C" void kernel_launch(void* const* d_in, const int* in_sizes, int n_in, void* d_out, int out_size,
                              void* d_ws, size_t ws_size, hipStream_t stream) {
  (void)hipFuncSetAttribute((const void*)mega_kernel, hipFuncAttributeMaxDynamicSharedMemorySize, LDS_BYTES);
  mega_kernel<<<256, 256, LDS_BYTES, stream>>>((const u16*)d_in[0], (const u16*)d_in[1], (const u16*)d_in[2],
                                               (const u16*)d_in[4], (const u16*)d_in[5], (u16*)d_out, (char*)d_ws);
}
```

```cpp
#include <hip/hip_runtime.h>
#include <stdint.h>

namespace {

typedef unsigned short u16;
typedef unsigned long long u64;
typedef short s16x4 __attribute__((ext_vector_type(4)));
typedef float f32x4 __attribute__((ext_vector_type(4)));
typedef unsigned u32x4 __attribute__((ext_vector_type(4)));
typedef unsigned u32x2 __attribute__((ext_vector_type(2)));
typedef unsigned u32x8 __attribute__((ext_vector_type(8)));
typedef unsigned u32x16 __attribute__((ext_vector_type(16)));
typedef __amdgpu_buffer_rsrc_t rsrc_t;
#define LAS __attribute__((address_space(3)))
#define DI __device__ __forceinline__
#define LDSP(a) ((LAS char*)(size_t)(a))

constexpr unsigned MB = 1u << 20;
constexpr unsigned WS_XF = 0;
constexpr unsigned WS_OF = 16 * MB;
constexpr unsigned WS_KV = 32 * MB;
constexpr unsigned WS_WF = 64 * MB;
constexpr unsigned WS_WO = 76 * MB;
constexpr unsigned WS_ST = 80 * MB;
constexpr unsigned WS_CS = 81 * MB;
constexpr unsigned WS_FL = 82 * MB;
constexpr unsigned WS_EP = WS_FL + 2048 * 128;
constexpr unsigned WS_DBG = 96 * MB;
constexpr unsigned OOB = 0x80000000u;
constexpr int L_RES = 0, L_K = 16384, L_V = 49152, L_INB = 81920, L_CS = 155648, L_PM = L_CS + 1024, L_MB = L_PM + 2048, L_SCR = L_MB + 2048;
constexpr int L_WS = 57344;
constexpr int LDS_BYTES = 163840;

template <int N> DI void wait_vm() { asm volatile("s_waitcnt vmcnt(%0)" ::"n"(N) : "memory"); }
template <int N> DI void nops() { asm volatile("s_nop %0" ::"n"(N - 1)); }
template <int OFF> DI void ld8(u32x2& d, rsrc_t r, unsigned v, unsigned s) {
  asm volatile("buffer_load_dwordx2 %0, %1, %2, %3 offen offset:%4 sc1" : "=v"(d) : "v"(v), "s"(r), "s"(s), "i"(OFF) : "memory");
}
template <int OFF> DI void st16(const u32x4& d, rsrc_t r, unsigned v, unsigned s) {
  asm volatile("buffer_store_dwordx4 %0, %1, %2, %3 offen offset:%4 sc1\n\ts_nop 1" :: "v"(d), "v"(v), "s"(r), "s"(s), "i"(OFF) : "memory");
}
template <int OFF> DI void st8(const u32x2& d, rsrc_t r, unsigned v, unsigned s) {
  asm volatile("buffer_store_dwordx2 %0, %1, %2, %3 offen offset:%4 sc1" :: "v"(d), "v"(v), "s"(r), "s"(s), "i"(OFF) : "memory");
}
DI void st4(unsigned d, rsrc_t r, unsigned v, unsigned s) {
  asm volatile("buffer_store_dword %0, %1, %2, %3 offen sc1" :: "v"(d), "v"(v), "s"(r), "s"(s) : "memory");
}
DI void dma(rsrc_t r, unsigned v, unsigned s, unsigned lm) {
  asm volatile("s_mov_b32 m0, %3\n\ts_nop 0\n\tbuffer_load_dwordx4 %0, %1, %2 offen lds" :: "v"(v), "s"(r), "s"(s), "s"(lm) : "memory");
}
DI void touch(rsrc_t r, unsigned v) {
  asm volatile("s_mov_b32 m0, %2\n\ts_nop 0\n\tbuffer_load_dword %0, %1, 0 offen lds" :: "v"(v), "s"(r), "s"((unsigned)L_SCR) : "memory");
}
DI void mma_v(f32x4& c, const u32x4& a, const u32x4& b) { asm volatile("v_mfma_f32_16x16x32_bf16 %0, %1, %2, %0" : "+v"(c) : "v"(a), "v"(b)); }
DI void mm0_v(f32x4& c, const u32x4& a, const u32x4& b) { asm volatile("v_mfma_f32_16x16x32_bf16 %0, %1, %2, 0" : "=v"(c) : "v"(a), "v"(b)); }
DI unsigned uni(unsigned v) { v = __builtin_amdgcn_readfirstlane(v); asm volatile("" : "+s"(v)); return v; }
template <class T> DI void pin(T& v) { asm volatile("" : "+v"(v)); }
DI u32x2 lds_tr(unsigned a) { return __builtin_bit_cast(u32x2, __builtin_amdgcn_ds_read_tr16_b64_v4i16((LAS s16x4*)LDSP(a))); }
DI u32x4 cat(const u32x2& a, const u32x2& b) { return __builtin_shufflevector(a, b, 0, 1, 2, 3); }
DI u32x2 lds_r2(unsigned a) { return *(LAS u32x2*)LDSP(a); }
DI void lds_w4(unsigned a, const u32x4& v) { *(LAS u32x4*)LDSP(a) = v; }
DI unsigned rot(unsigned g, unsigned sl) { return (g + (0x641u >> 4 * sl)) & 15; }
DI int swzK(int u) { return ((((u >> 1) ^ u) & 1) << 1) | (u >> 1); }

#define STAMP(i)

__global__ __launch_bounds__(256, 1) void mega_kernel(const u16* __restrict__ x0, const u16* __restrict__ WqA,
                                                      const u16* __restrict__ WoA, const u16* __restrict__ WqB,
                                                      const u16* __restrict__ WoB, u16* __restrict__ out, char* ws) {
  const unsigned bid = blockIdx.x, w = __builtin_amdgcn_readfirstlane(threadIdx.x >> 6);
  unsigned lane = threadIdx.x & 63;
  unsigned stw = 0;
  STAMP(0);
  u32x2 tag;
  {
    u64 t0; unsigned t1, t2, t3, v1, v2;
    asm volatile(
      "s_getpc_b64 %[t0]\n.Lmk_here:\n"
      "s_lshr_b32 %[t1], %[bid], 3\n s_lshl2_add_u32 %[t1], %[t1], %[w]\n s_lshl_b32 %[t1], %[t1], 10\n"
      "v_lshlrev_b32 %[v1], 4, %[lane]\n"
      "s_mov_b64 s[88:89], %[t0]\n s_and_b32 s88, s88, -16\n s_and_b32 s89, s89, 0xffff\n s_mov_b32 s90, (.Lmk_end-.Lmk_here)&0xfffffff0\n s_mov_b32 s91, 0x20000\n"
      "s_mov_b32 m0, %[scr]\n"
      "v_lshrrev_b32 %[v2], 3, %[lane]\n"
      "buffer_load_dwordx4 %[v1], s[88:91], %[t1] offen lds\n"
      "v_and_b32 %[v1], 0x70, %[v1]\n v_lshl_or_b32 %[v1], %[v2], 11, %[v1]\n"
      "s_and_b32 %[t1], %[bid], 4\n s_lshr_b32 %[t2], %[bid], 6\n s_add_u32 %[t1], %[t1], %[t2]\n s_lshl3_add_u32 %[t1], %[t1], %[w]\n"
      "s_and_b32 %[t3], %[bid], 1\n s_lshl1_add_u32 %[t1], %[t1], %[t3]\n s_lshl_b32 %[t1], %[t1], 8\n"
      "s_bfe_u32 %[t3], %[bid], 0x30003\n s_add_u32 %[t1], %[t1], %[t3]\n s_and_b32 %[t3], %[bid], 2\n s_lshl2_add_u32 %[t1], %[t3], %[t1]\n s_lshl_b32 %[t1], %[t1], 7\n"
      "s_lshl_b32 %[t3], %[w], 11\n"
      : [t0] "=&s"(t0), [t1] "=&s"(t1), [t2] "=&s"(t2), [t3] "=&s"(t3), [v1] "=&v"(v1), [v2] "=&v"(v2)
      : [bid] "s"(bid), [w] "s"(w), [lane] "v"(lane), [scr] "i"(L_SCR + 1024) : "memory", "s88", "s89", "s90", "s91", "scc");
    v2 = v2 * 6144 + (lane & 7) * 16;
    const unsigned g_ = (bid & 3) * 4 + (bid >> 4 & 3);
    const unsigned src = (((bid & 4) + (bid >> 6)) * 128 + (bid >> 3 & 1) * 64 + w * 16) * 6144, dst = L_INB + w * 2048;
    const unsigned r0 = src + rot(g_, 0) * 128, r1 = src + 2048 + rot(g_, 1) * 128, r2 = src + 4096 + rot(g_, 2) * 128;
    asm volatile(
      "s_mov_b64 s[88:89], %[wq]\n s_and_b32 s89, s89, 0xffff\n s_mov_b32 s90, 0x600000\n s_mov_b32 s91, 0x20000\n"
      "s_mov_b32 m0, %[dst]\n s_add_u32 %[t2], %[r0], 0xc000\n buffer_load_dwordx4 %[v2], s[88:91], %[r0] offen lds\n"
      "s_add_u32 m0, %[dst], 0x400\n s_nop 0\n buffer_load_dwordx4 %[v2], s[88:91], %[t2] offen lds\n"
      "s_add_u32 m0, %[dst], 0x2000\n s_add_u32 %[t2], %[r1], 0xc000\n buffer_load_dwordx4 %[v2], s[88:91], %[r1] offen lds\n"
      "s_add_u32 m0, %[dst], 0x2400\n s_nop 0\n buffer_load_dwordx4 %[v2], s[88:91], %[t2] offen lds\n"
      "s_add_u32 m0, %[dst], 0x4000\n s_add_u32 %[t2], %[r2], 0xc000\n buffer_load_dwordx4 %[v2], s[88:91], %[r2] offen lds\n"
      "s_add_u32 m0, %[dst], 0x4400\n s_nop 0\n buffer_load_dwordx4 %[v2], s[88:91], %[t2] offen lds\n"
      "s_mov_b64 s[88:89], %[x]\n s_and_b32 s89, s89, 0xffff\n s_mov_b32 s90, 0x400000\n s_mov_b32 s91, 0x20000\n"
      "s_add_u32 m0, %[t3], 0x2000\n s_add_u32 %[t2], %[t1], 0x40000\n buffer_load_dwordx4 %[v1], s[88:91], %[t2] offen lds\n"
      "s_add_u32 m0, %[t3], 0x2400\n s_add_u32 %[t2], %[t1], 0x44000\n buffer_load_dwordx4 %[v1], s[88:91], %[t2] offen lds\n"
      "s_mov_b32 m0, %[t3]\n s_add_u32 %[t2], %[t1], 0x4000\n buffer_load_dwordx4 %[v1], s[88:91], %[t1] offen lds\n"
      "s_add_u32 m0, %[t3], 0x400\n s_mov_b32 %[t1], %[ep]\n buffer_load_dwordx4 %[v1], s[88:91], %[t2] offen lds\n"
      "s_mov_b64 s[88:89], %[ws]\n s_and_b32 s89, s89, 0xffff\n s_mov_b32 s90, 0x8000000\n"
      "buffer_load_dwordx2 %[tag], off, s[88:91], %[t1] sc1\n"
      : [t1] "+s"(t1), [t2] "+s"(t2), [tag] "=&v"(tag), [lane] "+v"(lane)
      : [t3] "s"(t3), [v1] "v"(v1), [v2] "v"(v2), [x] "s"(x0), [ws] "s"(ws), [wq] "s"(WqA), [dst] "s"(dst), [ep] "i"(WS_EP),
        [r0] "s"(r0), [r1] "s"(r1), [r2] "s"(r2),
        "s"(WoA), "s"(WqB), "s"(WoB), "s"(out) : "memory", "s88", "s89", "s90", "s91", "scc");
  }
  const unsigned xcd = bid & 7, bix = bid >> 3;
  const unsigned c = (xcd >> 2) * 4 + (bix >> 3), h = (xcd & 3) * 8 + (bix & 7), par = h & 1, g = h >> 1;
  const unsigned p2 = h >> 3 & 1, g2 = (h >> 4) * 8 + (h & 7);
  const unsigned fr = lane & 15, fq = lane >> 4;
  const unsigned dl = 0, wr = (w + dl) & 3;
  const rsrc_t wsr = __builtin_amdgcn_make_buffer_rsrc(ws, 0, 128 * MB, 0x00020000);
  const unsigned l16 = lane * 16, lw = w * 2048 + l16;
  const unsigned fl0 = lane == 0 ? 0u : OOB;
  const unsigned myfl = WS_FL + (c * 64 + h) * 128 + w * 8;
  const unsigned osrc0 = (lane >> 3) * 2048 + (lane & 7) * 16;
  const rsrc_t wa = __builtin_amdgcn_make_buffer_rsrc((void*)WqA, 0, 6 * MB, 0x00020000);
  const unsigned rho = 0, wbase = w * 256 * 6144 + h * 64;
  const unsigned wv = (lane >> 2) * 6144 + ((lane & 3) ^ ((lane >> 3) & 2)) * 16;
  auto piece = [&](int q, int j) __attribute__((always_inline)) {
    const unsigned k = j ^ par, src = uni(wbase + (((rho + 2 * q) & 7) + k) * 196608), dst = uni(q + k ? L_INB + w * 18432 + (2 * q + k - 1) * 6144 : L_WS + w * 6144);
#pragma unroll
    for (int i = 0; i < 6; ++i) dma(wa, wv, src + (i & 1) * 98304 + (i >> 1) * 2048, dst + i * 1024);
  };
  const unsigned kb = 2 * c + par;
  const rsrc_t oa = __builtin_amdgcn_make_buffer_rsrc((void*)WoA, 0, 2 * MB, 0x00020000);
  const rsrc_t wb = __builtin_amdgcn_make_buffer_rsrc((void*)WqB, 0, 6 * MB, 0x00020000);
  const rsrc_t ob = __builtin_amdgcn_make_buffer_rsrc((void*)WoB, 0, 2 * MB, 0x00020000);
  const unsigned wsrc = (w * 16 + (lane >> 3)) * 6144 + (lane & 7) * 16, osrc = w * 16 * 2048 + osrc0;
  auto get_o = [&](int t) __attribute__((always_inline)) {
#pragma unroll
    for (int i = 0; i < 2; ++i) dma(t ? ob : oa, osrc, (kb * 64 + i * 8) * 2048 + rot(g, 1 + t) * 128, L_V + 8192 + t * 8192 + w * 2048 + i * 1024);
  };
  auto get_q = [&](int t) __attribute__((always_inline)) {
#pragma unroll
    for (int i = 0; i < 6; ++i)
      dma(t ? wb : wa, wsrc, (kb * 64 + (i & 1) * 8) * 6144 + (i >> 1) * 2048 + rot(g, i >> 1) * 128, (t ? L_K : L_INB) + (i >> 1) * 8192 + w * 2048 + (i & 1) * 1024);
  };
  const unsigned fr8 = fq == fr >> 2 ? 64 + fr * 4 : fq == ((fr >> 2) + 1 & 3) ? fr * 4 : OOB;
  u32x4 ones = {0x3F803F80u, 0x3F803F80u, 0x3F803F80u, 0x3F803F80u};
  unsigned sta = 0;
  auto publish = [&](int s, bool now) __attribute__((always_inline)) {
    const unsigned a = L_RES + s * 8192 + w * 2048 + fr * 128 + fq * 8, T = c * 16 + 8 * s + 2 * wr + p2;
    u32x4 y0 = cat(lds_r2(a), lds_r2(a + 32)), y1 = cat(lds_r2(a + 64), lds_r2(a + 96));
    f32x4 sm, gm;
    pin(y0); pin(y1); pin(ones);
    nops<2>();
    mm0_v(sm, ones, y0); mm0_v(gm, y0, y0); mma_v(sm, ones, y1); mma_v(gm, y1, y1);
    if (now) { st16<0>(y0, wsr, l16, WS_XF + T * 32768 + g2 * 2048); st16<1024>(y1, wsr, l16, WS_XF + T * 32768 + g2 * 2048); }
    lds_w4(lw + L_RES + s * 8192, y0); lds_w4(lw + L_RES + s * 8192 + 1024, y1);
    nops<8>();
    pin(sm); pin(gm);
    const unsigned st = __float_as_uint(fq != fr >> 2 ? sm[0] : fr & 2 ? (fr & 1 ? gm[3] : gm[2]) : (fr & 1 ? gm[1] : gm[0]));
    if (now) st4(st, wsr, fr8, WS_ST + T * 2048 + g2 * 128); else sta = st;
  };
  auto next_tag = [&]() __attribute__((always_inline)) {
    pin(tag);
    const u64 t = (((u64)tag[1] << 32) | tag[0]) + 1; tag = (u32x2){(unsigned)t, (unsigned)(t >> 32)};
  };
  const unsigned cb = c * 16 * 32768, skv = WS_KV + (c * 32 + h) * 32768;
  const unsigned soo = WS_OF + cb + wr * 65536 + h * 1024, sxo = WS_XF + cb + (2 * wr + p2) * 32768 + g2 * 2048, sso = WS_ST + (c * 16 + 2 * wr + p2) * 2048 + g2 * 128;
  auto clear = [&](int from, int to) __attribute__((always_inline)) {
    u32x4 z = {~0u, ~0u, ~0u, ~0u};
    pin(z);
#pragma unroll
    for (int k = from; k < to; ++k) {
      if (k < 8) st16<0>(z, wsr, l16, soo + (k == 4 ? 9 : k == 5 ? 8 : k - 6) * 32768);
      else if (k < 10) st16<0>(z, wsr, l16, sxo + 4 * MB + 8 * 32768 + (k - 8) * 1024);
      else st4(z[0], wsr, fr8, sso + 136 * 2048);
    }
  };
  ;
  ;
  touch(wsr, lane < 4 ? WS_FL + (lane >> 1) * 524288 + (w * 512 + c * 64 + (lane & 1) * 32 + h) * 128 : OOB);
  STAMP(29);

  unsigned ps[16];
  u32x8 pv = {};
#pragma unroll
  for (int j = 0; j < 3; ++j) {
    const unsigned ct = (w & 1) * 3 + j;
    pv[1 + j] = L_K + (ct >> 1) * 8192 + (4 * fq + (fr >> 2)) * 128 + (w >> 1) * 64 + (ct & 1) * 32 + (lane & 3) * 8;
    ps[j] = WS_WF + 6 * MB + ((2 * rot(g, ct >> 1) + (w >> 1)) * 192 + (kb >> 2) * 48 + (ct + 4) % 6 * 8 + (kb & 3) * 2) * 1024;
    ps[10 + j] = kb * 64 * 6144 + j * 2048 + rot(g, j) * 128;
  }
  { const unsigned ct = (w & 1) * 3 + (fr >> 1);
    pv[4] = ((2 * rot(g, ct >> 1) + (w >> 1)) * 16384 + ct * 128 + (fr & 1) * 64 + fq * 16) | (fr < 6 ? 0u : OOB); }
  ps[3] = WS_CS + 524288 + kb * 1024;
  ps[6] = (c >> 2) | par << 1 | (bix * 4 + w) << 10;                                            ps[7] = L_WS + w * 6144;
  ps[8] = wbase;                                             ps[9] = rho;
  ps[13] = kb * 64 * 2048 + rot(g, 1) * 128;                 ps[14] = kb * 64 * 2048 + rot(g, 2) * 128;
  ps[15] = WS_WF + (h * 4 + w) * 49152 + (rho + (c >> 2)) * 1024;
  pv[5] = wsrc;                                              pv[6] = osrc;
  u32x16 pw = {wv, L_WS + w * 6144 + (4 * fq + (fr >> 2)) * 64 + (fq & 1) * 32 + (lane & 3) * 8,
                    l16 + rho * 1024, l16 + ((rho + 2) & 7) * 1024, l16 + ((rho + 4) & 7) * 1024, l16 + ((rho + 6) & 7) * 1024,
                    L_WS + w * 6144 + (fr == 0 ? fq * 16 : 1024 + l16), L_WS + (w * 24 + (lane < 24 ? lane : 0)) * 4,
                    (w * 128 + (lane >> 3 & 1) * 64 + 2 * ((g2 - rot(0, 1 + (fq & 1))) & 15) + (lane >> 2 & 1)) * 128 + (lane & 3) * 8};
  if (w < 2) {
    u32x4 pm;
#pragma unroll
    for (int k = 0; k < 4; ++k)
      pm[k] = (8 * fq + 2 * k > 16 * w + fr ? 0xFFFFu : 0u) | (8 * fq + 2 * k + 1 > 16 * w + fr ? 0xFFFF0000u : 0u);
    lds_w4(L_PM + w * 1024 + l16, pm);
  } else {
    const unsigned want = (unsigned)((0x5555ull << 16) >> (fr + 25 - 8 * fq));
    u32x4 mb;
#pragma unroll
    for (int k = 0; k < 4; ++k) mb[k] = (want >> (15 - 4 * w - k)) & 1 ? 0u : 0xFF800000u;
    lds_w4(L_MB + (w - 2) * 1024 + l16, mb);
  }
  pv[0] = L_V + 8192 + (16 * (fq & 1) + 4 * (fq >> 1) + (fr >> 2)) * 128 + w * 32 + (lane & 3) * 8;
  pv[7] = lane == 0 ? 32 * 128 : OOB;
  ps[4] = WS_WO + (rot(g, 1) * 128 + (kb >> 2) * 32 + w * 8 + (kb & 3) * 2) * 1024;
  ps[5] = WS_WO + 2 * MB + (rot(g, 2) * 128 + (kb >> 2) * 32 + w * 8 + (kb & 3) * 2) * 1024;
  const unsigned krow = (8 * (fr >> 2) + (fr & 3)) * 64 + wr * 2048 + L_K;
  const unsigned kv0 = 16384 + wr * 2048 + fr * 64 + ((fq ^ ((fr >> 3) << 1) ^ ((fr >> 1) & 3)) * 16);
  u32x16 va = {l16, l16 + 4096, lw, L_INB + w * 18432 + l16, L_INB + ((w + 3) & 3) * 18432 + l16, L_INB + ((w + 2) & 3) * 18432 + 6144 + l16,
                     L_INB + ((w + 1) & 3) * 18432 + 12288 + l16, kv0, kv0 ^ 48, L_CS + fq * 16, lane * 4, fl0,
                     ((lane & 31) >> 2) * 128 + (lane & 3) * 8,
                     fq < 3 ? (w * 128 + (lane >> 3 & 1) * 64 + 2 * ((2 * g - rot(g, fq % 3)) & 15) + (lane >> 2 & 1)) * 128 + (lane & 3) * 8 : ((lane & 7) * 64 + h) * 128 + w * 8,
                     fr8, krow + (fq ^ swzK(fr >> 2) ^ ((fr >> 1) & 1)) * 16};
  u32x8 vb = {L_V + wr * 2048 + (8 * fq + (fr >> 2)) * 64 + 8 * (lane & 1) + (((lane >> 1) & 1) ^ ((fq & 1) << 1) ^ (fr >> 3)) * 16,
                    krow + (fq ^ swzK(((fr >> 2) + 2) & 3) ^ ((fr >> 1) & 1)) * 16, 0, 0, 0,
                    lane < 24 ? L_CS + w * 96 + lane * 4 : L_SCR, w == 0 ? bid * 1024 + lane * 4 : OOB, 0};
  u32x16 pz = {pw[2], pw[3], pw[4], pw[5], pv[1], pv[2], pv[3], pv[4], pv[0], pw[8], pw[0], pw[1], pw[6], pw[7], pv[5], pv[6]};
  unsigned pf = pv[7];
  pin(va); pin(vb); pin(pz); pin(pf);
  auto convert = [&]() __attribute__((always_inline)) {
    f32x4 cs[3];
    u32x4 f[2][3];
    asm volatile("s_waitcnt lgkmcnt(0)\n\ts_barrier" ::: "memory");
#pragma unroll
    for (int kk = 0; kk < 2; ++kk)
#pragma unroll
      for (int j = 0; j < 3; ++j) {
        const unsigned a = pz[4 + j] + (L_INB - L_K) + kk * 4096;
        f[kk][j] = cat(lds_tr(a), lds_tr(a + 2048));
      }
    pin(ones);
#pragma unroll
    for (int kk = 0; kk < 2; ++kk)
#pragma unroll
      for (int j = 0; j < 3; ++j) {
        if (kk) mma_v(cs[j], f[kk][j], ones); else mm0_v(cs[j], f[kk][j], ones);
        st16<0>(f[kk][j], wsr, l16, uni(ps[j] - 6 * MB + kk * 1024));
      }
    nops<8>();
    pin(cs[0]); pin(cs[1]); pin(cs[2]);
    const f32x4 v = fr < 2 ? cs[0] : fr < 4 ? cs[1] : cs[2];
    st16<0>(__builtin_bit_cast(u32x4, v), wsr, pz[7], uni(ps[3] - 524288));
  };
  wait_vm<6>();
  STAMP(18);
  convert();
  wait_vm<8>();
  next_tag();
  publish(1, true);
  STAMP(1);
  wait_vm<3>();
  st8<96>(tag, wsr, fl0, myfl);
  get_o(0);
  get_o(1);
  get_q(1);
  publish(0, false);
  wait_vm<11>();
  st8<0>(tag, wsr, lane == 0 ? 32 * 128 : OOB, myfl + (2 * g2 + p2 - h) * 128);
  STAMP(15);

  u32x16 sa, sb;
#pragma unroll
  for (int i = 0; i < 4; ++i) {
    sa[i] = WS_XF + cb + w * 8192 + ((wr + 1 + i) & 3) * 65536;
    sa[4 + i] = sa[i] + WS_OF + p2 * 32768;
  }
  sa[8] = soo;                                              sa[9] = sxo;
  sa[10] = WS_ST + (c * 16 + 2 * wr) * 2048;                sa[11] = sso;
  sa[12] = skv - 16384;                                     sa[13] = c ? skv - 32 * 32768 : WS_XF + (h & 15) * 32768;
  sa[14] = c ? skv - 32 * 32768 + 8 * MB : sa[13];          sa[15] = WS_FL + (c * 64 + 8 * w) * 128;
  sb[0] = myfl;                                             sb[1] = WS_WF + (h * 4 + w) * 49152;
  sb[2] = WS_WO + (g2 * 4 + w) * 32768;                     sb[3] = c ? myfl - w * 8 + ((w - dl) & 3) * 8 - 32 * 128 + 64 : myfl + 4096;
  sb[4] = w * 96;                                           sb[5] = c ? myfl + 65536 - w * 8 + ((3 - dl) & 3) * 8 - 32 * 128 + 64 : myfl + 4096;
  sb[6] = ((c * 16 + 2 * wr + p2) * 16 * 1024 + g2 * 64) * 2;  sb[7] = WS_FL + 96;
  sb[8] = WS_CS + h * 16384;                                sb[9] = w * 2048;
  sb[10] = c ? -8 : 8 - wr;                                  sb[11] = c ? 16 * MB : 0;
  sb[12] = (2 * g2 + p2 - h) * 128;                                sb[13] = bid == 0 && w == 0 ? WS_EP : 0;
  sb[14] = c == 0 && wr == 0 ? ~0u : 0u;                     sb[15] = 0;
#pragma unroll
  for (int i = 0; i < 16; ++i) { sa[i] = uni(sa[i]); sb[i] = uni(sb[i]); }
  const rsrc_t wsk = __builtin_amdgcn_make_buffer_rsrc(ws, 0, wr == 3 ? 128 * MB : 0u, 0x00020000);
  const rsrc_t outr = __builtin_amdgcn_make_buffer_rsrc(out, 0, 4 * MB, 0x00020000);
  u32x8 pa, pc;
#pragma unroll
  for (int i = 0; i < 8; ++i) { pa[i] = uni(ps[i]); pc[i] = uni(ps[8 + i]); }
  STAMP(17);

  vb[2] = tag[0]; vb[3] = tag[1];
  STAMP(19);
  vb[7] = stw;
  asm volatile(
"; @entry\n"
"s_mov_b32 s0, 0\n"
"s_mov_b32 s3, 0\n"
"s_waitcnt lgkmcnt(0)\n"
"ds_read_b128 v[24:27], v2\n"
"ds_read_b128 v[28:31], v2 offset:1024\n"
"s_waitcnt lgkmcnt(0)\n"
"buffer_store_dwordx4 v[24:27], v0, s[4:7], s61 offen sc1\n"
"buffer_store_dwordx4 v[28:31], v0, s[4:7], s61 offen offset:1024 sc1\n"
"buffer_store_dword v48, v14, s[4:7], s63 offen sc1\n"
"s_waitcnt vmcnt(10)\n"
"s_waitcnt lgkmcnt(0)\n"
"s_barrier\n"
"ds_read_b64_tr_b16 v[24:25], v40\n"
"ds_read_b64_tr_b16 v[26:27], v40 offset:1024\n"
"ds_read_b64_tr_b16 v[28:29], v40 offset:4096\n"
"ds_read_b64_tr_b16 v[30:31], v40 offset:5120\n"
"ds_read_b64_tr_b16 v[32:33], v40 offset:8192\n"
"ds_read_b64_tr_b16 v[34:35], v40 offset:9216\n"
"ds_read_b64_tr_b16 v[44:45], v40 offset:12288\n"
"ds_read_b64_tr_b16 v[46:47], v40 offset:13312\n"
"s_waitcnt lgkmcnt(2)\n"
"buffer_store_dwordx4 v[24:27], v0, s[4:7], s24 offen sc1\n"
"s_waitcnt lgkmcnt(0)\n"
"buffer_store_dwordx4 v[28:31], v0, s[4:7], s24 offen offset:1024 sc1\n"
"buffer_store_dwordx4 v[32:35], v0, s[4:7], s25 offen sc1\n"
"buffer_store_dwordx4 v[44:47], v0, s[4:7], s25 offen offset:1024 sc1\n"
"s_add_u32 s98, s75, 0\n"
"buffer_load_dwordx2 v[254:255], v13, s[4:7], s98 offen sc1\n"
"s_getpc_b64 s[84:85]\n"
".Lmk_h2:\n"
"s_and_b32 s84, s84, -16\n"
"s_and_b32 s85, s85, 0xffff\n"
"s_mov_b32 s86, (.Lmk_end-.Lmk_h2)&0xFFFFFFF0\n"
"s_mov_b32 s87, 0x20000\n"
"s_and_b32 s97, s26, 0xfffffc00\n"
"s_mov_b32 m0, 0x27800\n"
"s_add_u32 s97, s97, 0x1e000\n"
"buffer_load_dwordx4 v0, s[84:87], s97 offen lds\n"
"s_waitcnt vmcnt(1)\n"
"v_cmp_ne_u64_e32 vcc, v[254:255], v[18:19]\n"
"s_cbranch_vccnz .Lmk_f2s\n"
".Lmk_f2r:\n"
"s_add_u32 s98, s69, 0\n"
"buffer_load_dwordx4 a[0:3], v0, s[4:7], s98 offen sc1\n"
"buffer_load_dwordx4 a[4:7], v0, s[4:7], s98 offen offset:1024 sc1\n"
"buffer_load_dwordx4 a[8:11], v0, s[4:7], s98 offen offset:2048 sc1\n"
"buffer_load_dwordx4 a[12:15], v0, s[4:7], s98 offen offset:3072 sc1\n"
"buffer_load_dwordx4 a[16:19], v1, s[4:7], s98 offen sc1\n"
"buffer_load_dwordx4 a[20:23], v1, s[4:7], s98 offen offset:1024 sc1\n"
"buffer_load_dwordx4 a[24:27], v1, s[4:7], s98 offen offset:2048 sc1\n"
"buffer_load_dwordx4 a[28:31], v1, s[4:7], s98 offen offset:3072 sc1\n"
"s_add_u32 s98, s67, 0x1000\n"
"buffer_load_dwordx2 v[254:255], v12, s[4:7], s98 offen sc1\n"
"s_add_u32 s97, s69, 0x2000\n"
"buffer_load_dwordx4 a[32:35], v0, s[4:7], s97 offen sc1\n"
"buffer_load_dwordx4 a[36:39], v0, s[4:7], s97 offen offset:1024 sc1\n"
"buffer_load_dwordx4 a[40:43], v0, s[4:7], s97 offen offset:2048 sc1\n"
"buffer_load_dwordx4 a[44:47], v0, s[4:7], s97 offen offset:3072 sc1\n"
"buffer_load_dwordx4 a[48:51], v1, s[4:7], s97 offen sc1\n"
"buffer_load_dwordx4 a[52:55], v1, s[4:7], s97 offen offset:1024 sc1\n"
"buffer_load_dwordx4 a[56:59], v1, s[4:7], s97 offen offset:2048 sc1\n"
"buffer_load_dwordx4 a[60:63], v1, s[4:7], s97 offen offset:3072 sc1\n"
"s_waitcnt vmcnt(8)\n"
"v_cmp_ne_u64_e32 vcc, v[254:255], v[18:19]\n"
"s_cbranch_vccnz .Lmk_f1s\n"
".Lmk_f1r:\n"
"s_add_u32 s98, s52, 0x40000\n"
"buffer_load_dwordx4 v[24:27], v0, s[4:7], s98 offen sc1\n"
"buffer_load_dwordx4 v[28:31], v0, s[4:7], s98 offen offset:1024 sc1\n"
"buffer_load_dwordx4 v[32:35], v0, s[4:7], s98 offen offset:2048 sc1\n"
"buffer_load_dwordx4 v[44:47], v0, s[4:7], s98 offen offset:3072 sc1\n"
"buffer_load_dwordx4 v[48:51], v1, s[4:7], s98 offen sc1\n"
"buffer_load_dwordx4 v[52:55], v1, s[4:7], s98 offen offset:1024 sc1\n"
"buffer_load_dwordx4 v[56:59], v1, s[4:7], s98 offen offset:2048 sc1\n"
"buffer_load_dwordx4 v[60:63], v1, s[4:7], s98 offen offset:3072 sc1\n"
"s_waitcnt lgkmcnt(0)\n"
"s_barrier\n"
"s_add_u32 s98, s76, 0\n"
"v_bfe_u32 v255, v0, 2, 7\n"
"v_add_u32_e32 v255, s72, v255\n"
"v_and_b32_e32 v254, 0xffffffc0, v255\n"
"v_add_u32_e32 v255, v255, v254\n"
"v_and_b32_e32 v254, 0x200, v0\n"
"v_lshl_add_u32 v255, v254, 4, v255\n"
"buffer_load_dword v254, v255, s[4:7], s98 offen sc1\n"
"buffer_load_dword v253, v255, s[4:7], s98 offen offset:1024 sc1\n"
"buffer_load_dword v252, v255, s[4:7], s98 offen offset:2048 sc1\n"
"buffer_load_dword v251, v255, s[4:7], s98 offen offset:3072 sc1\n"
"s_add_u32 s98, s76, 0x1000\n"
"buffer_load_dword v250, v255, s[4:7], s98 offen sc1\n"
"buffer_load_dword v249, v255, s[4:7], s98 offen offset:1024 sc1\n"
"buffer_load_dword v248, v255, s[4:7], s98 offen offset:2048 sc1\n"
"buffer_load_dword v247, v255, s[4:7], s98 offen offset:3072 sc1\n"
"s_add_u32 s98, s69, 0x4000\n"
"buffer_load_dwordx4 a[64:67], v0, s[4:7], s98 offen sc1\n"
"buffer_load_dwordx4 a[68:71], v0, s[4:7], s98 offen offset:1024 sc1\n"
"buffer_load_dwordx4 a[72:75], v0, s[4:7], s98 offen offset:2048 sc1\n"
"buffer_load_dwordx4 a[76:79], v0, s[4:7], s98 offen offset:3072 sc1\n"
"buffer_load_dwordx4 a[80:83], v1, s[4:7], s98 offen sc1\n"
"buffer_load_dwordx4 a[84:87], v1, s[4:7], s98 offen offset:1024 sc1\n"
"buffer_load_dwordx4 a[88:91], v1, s[4:7], s98 offen offset:2048 sc1\n"
"buffer_load_dwordx4 a[92:95], v1, s[4:7], s98 offen offset:3072 sc1\n"
"s_add_u32 s98, s53, 0x40000\n"
"buffer_load_dwordx4 v[64:67], v0, s[4:7], s98 offen sc1\n"
"buffer_load_dwordx4 v[68:71], v0, s[4:7], s98 offen offset:1024 sc1\n"
"buffer_load_dwordx4 v[72:75], v0, s[4:7], s98 offen offset:2048 sc1\n"
"buffer_load_dwordx4 v[76:79], v0, s[4:7], s98 offen offset:3072 sc1\n"
"buffer_load_dwordx4 v[80:83], v1, s[4:7], s98 offen sc1\n"
"buffer_load_dwordx4 v[84:87], v1, s[4:7], s98 offen offset:1024 sc1\n"
"buffer_load_dwordx4 v[88:91], v1, s[4:7], s98 offen offset:2048 sc1\n"
"buffer_load_dwordx4 v[92:95], v1, s[4:7], s98 offen offset:3072 sc1\n"
"s_add_u32 s98, s69, 0x6000\n"
"buffer_load_dwordx4 a[96:99], v0, s[4:7], s98 offen sc1\n"
"buffer_load_dwordx4 a[100:103], v0, s[4:7], s98 offen offset:1024 sc1\n"
"buffer_load_dwordx4 a[104:107], v0, s[4:7], s98 offen offset:2048 sc1\n"
"buffer_load_dwordx4 a[108:111], v0, s[4:7], s98 offen offset:3072 sc1\n"
"buffer_load_dwordx4 a[112:115], v1, s[4:7], s98 offen sc1\n"
"buffer_load_dwordx4 a[116:119], v1, s[4:7], s98 offen offset:1024 sc1\n"
"buffer_load_dwordx4 a[120:123], v1, s[4:7], s98 offen offset:2048 sc1\n"
"buffer_load_dwordx4 a[124:127], v1, s[4:7], s98 offen offset:3072 sc1\n"
"s_waitcnt vmcnt(24)\n"
"v_add_f32_e32 v254, v254, v253\n"
"v_add_f32_e32 v254, v254, v252\n"
"v_add_f32_e32 v254, v254, v251\n"
"v_add_f32_e32 v254, v254, v250\n"
"v_add_f32_e32 v254, v254, v249\n"
"v_add_f32_e32 v254, v254, v248\n"
"v_add_f32_e32 v254, v254, v247\n"
"v_mov_b32_e32 v253, v254\n"
"s_nop 1\n"
"v_permlane32_swap_b32_e32 v254, v253\n"
"v_add_f32_e32 v254, v254, v253\n"
"ds_write_b32 v21, v254 offset:0\n"
".Lmk_z0:\n"
"; @A G1 0.0\n"
"v_mfma_f32_16x16x32_bf16 v[96:99], a[0:3], v[24:27], 0\n"
"s_add_u32 s98, s69, 0x8000\n"
"buffer_load_dwordx4 v[100:103], v0, s[4:7], s98 offen sc1\n"
"v_mfma_f32_16x16x32_bf16 v[96:99], a[4:7], v[28:31], v[96:99]\n"
"s_add_u32 s97, s62, 0x4000\n"
"v_mfma_f32_16x16x32_bf16 v[96:99], a[8:11], v[32:35], v[96:99]\n"
"buffer_load_dwordx4 v[104:107], v0, s[4:7], s98 offen offset:1024 sc1\n"
"v_mfma_f32_16x16x32_bf16 v[96:99], a[12:15], v[44:47], v[96:99]\n"
"v_mfma_f32_16x16x32_bf16 v[96:99], a[16:19], v[48:51], v[96:99]\n"
"buffer_load_dwordx4 v[108:111], v0, s[4:7], s98 offen offset:2048 sc1\n"
"v_mfma_f32_16x16x32_bf16 v[96:99], a[20:23], v[52:55], v[96:99]\n"
"buffer_load_dword v255, v10, s[4:7], s97 offen sc1\n"
"ds_read_b64_tr_b16 v[112:113], v36\n"
"v_mfma_f32_16x16x32_bf16 v[96:99], a[24:27], v[56:59], v[96:99]\n"
"buffer_load_dwordx4 v[116:119], v0, s[4:7], s98 offen offset:3072 sc1\n"
"ds_read_b64_tr_b16 v[114:115], v36 offset:2048\n"
"v_mfma_f32_16x16x32_bf16 v[96:99], a[28:31], v[60:63], v[96:99]\n"
"s_mov_b32 s94, 0x30003\n"
"s_mov_b32 s95, 0x30003\n"
"v_mov_b32_e32 v123, 0x3f803f80\n"
"v_mfma_f32_16x16x32_bf16 v[124:127], a[60:63], v[60:63], 0\n"
"buffer_load_dwordx4 v[128:131], v1, s[4:7], s98 offen sc1\n"
"v_cndmask_b32_e64 v120, 0, v123, s[94:95]\n"
"v_mfma_f32_16x16x32_bf16 v[124:127], a[56:59], v[56:59], v[124:127]\n"
"buffer_load_dword v254, v10, s[4:7], s97 offen offset:256 sc1\n"
"v_cndmask_b32_e64 v121, 0, v123, s[94:95]\n"
"v_mfma_f32_16x16x32_bf16 v[124:127], a[52:55], v[52:55], v[124:127]\n"
"buffer_load_dwordx4 v[132:135], v1, s[4:7], s98 offen offset:1024 sc1\n"
"v_cndmask_b32_e64 v122, 0, v123, s[94:95]\n"
"v_mfma_f32_16x16x32_bf16 v[124:127], a[48:51], v[48:51], v[124:127]\n"
"s_add_u32 s96, s68, 0x10000\n"
"v_cndmask_b32_e64 v123, 0, v123, s[94:95]\n"
"v_mfma_f32_16x16x32_bf16 v[124:127], a[44:47], v[44:47], v[124:127]\n"
"buffer_load_dwordx4 v[136:139], v1, s[4:7], s98 offen offset:2048 sc1\n"
"s_waitcnt lgkmcnt(0)\n"
"v_mfma_f32_16x16x32_bf16 v[140:143], v[112:115], v[120:123], 0\n"
"buffer_store_dwordx4 v[112:115], v0, s[4:7], s20 offen sc1\n"
"v_mfma_f32_16x16x32_bf16 v[124:127], a[40:43], v[32:35], v[124:127]\n"
"ds_write_b128 v3, v[96:99]\n"
"v_mfma_f32_16x16x32_bf16 v[124:127], a[36:39], v[28:31], v[124:127]\n"
"buffer_load_dwordx4 v[96:99], v1, s[4:7], s98 offen offset:3072 sc1\n"
"v_mfma_f32_16x16x32_bf16 v[124:127], a[32:35], v[24:27], v[124:127]\n"
"s_waitcnt vmcnt(31)\n"
"s_add_u32 s98, s69, 0xa000\n"
"v_mfma_f32_16x16x32_bf16 v[112:115], a[64:67], v[24:27], 0\n"
"buffer_load_dwordx4 v[120:123], v0, s[4:7], s98 offen sc1\n"
"v_mfma_f32_16x16x32_bf16 v[112:115], a[68:71], v[28:31], v[112:115]\n"
"buffer_store_dwordx2 v[18:19], v11, s[4:7], s96 offen offset:96 sc1\n"
"v_mfma_f32_16x16x32_bf16 v[112:115], a[72:75], v[32:35], v[112:115]\n"
"buffer_load_dwordx4 v[144:147], v0, s[4:7], s98 offen offset:1024 sc1\n"
"v_mfma_f32_16x16x32_bf16 v[112:115], a[76:79], v[44:47], v[112:115]\n"
"s_waitcnt vmcnt(30)\n"
"buffer_load_dword v253, v10, s[4:7], s97 offen offset:512 sc1\n"
"v_mfma_f32_16x16x32_bf16 v[112:115], a[80:83], v[48:51], v[112:115]\n"
"buffer_load_dwordx4 v[148:151], v0, s[4:7], s98 offen offset:2048 sc1\n"
"v_mfma_f32_16x16x32_bf16 v[112:115], a[84:87], v[52:55], v[112:115]\n"
"ds_write_b128 v3, v[124:127] offset:1024\n"
"v_mfma_f32_16x16x32_bf16 v[112:115], a[88:91], v[56:59], v[112:115]\n"
"buffer_load_dwordx4 v[124:127], v0, s[4:7], s98 offen offset:3072 sc1\n"
"v_mfma_f32_16x16x32_bf16 v[112:115], a[92:95], v[60:63], v[112:115]\n"
"s_waitcnt vmcnt(17)\n"
"buffer_load_dword v252, v10, s[4:7], s97 offen offset:768 sc1\n"
"v_mfma_f32_16x16x32_bf16 v[152:155], a[124:127], v[60:63], 0\n"
"buffer_load_dwordx4 v[156:159], v1, s[4:7], s98 offen sc1\n"
"v_mfma_f32_16x16x32_bf16 v[152:155], a[120:123], v[56:59], v[152:155]\n"
"v_mfma_f32_16x16x32_bf16 v[152:155], a[116:119], v[52:55], v[152:155]\n"
"buffer_load_dwordx4 v[160:163], v1, s[4:7], s98 offen offset:1024 sc1\n"
"v_mfma_f32_16x16x32_bf16 v[152:155], a[112:115], v[48:51], v[152:155]\n"
"buffer_load_dword v251, v10, s[4:7], s97 offen offset:1024 sc1\n"
"v_mfma_f32_16x16x32_bf16 v[152:155], a[108:111], v[44:47], v[152:155]\n"
"buffer_load_dwordx4 v[164:167], v1, s[4:7], s98 offen offset:2048 sc1\n"
"v_mfma_f32_16x16x32_bf16 v[152:155], a[104:107], v[32:35], v[152:155]\n"
"ds_write_b128 v3, v[112:115] offset:2048\n"
"v_mfma_f32_16x16x32_bf16 v[152:155], a[100:103], v[28:31], v[152:155]\n"
"buffer_load_dwordx4 v[112:115], v1, s[4:7], s98 offen offset:3072 sc1\n"
"v_mfma_f32_16x16x32_bf16 v[152:155], a[96:99], v[24:27], v[152:155]\n"
"s_waitcnt vmcnt(18)\n"
"buffer_load_dword v250, v10, s[4:7], s97 offen offset:1280 sc1\n"
"v_mfma_f32_16x16x32_bf16 v[168:171], v[100:103], v[24:27], 0\n"
"s_add_u32 s98, s54, 0x40000\n"
"buffer_load_dwordx4 v[172:175], v0, s[4:7], s98 offen sc1\n"
"v_mfma_f32_16x16x32_bf16 v[168:171], v[104:107], v[28:31], v[168:171]\n"
"v_mfma_f32_16x16x32_bf16 v[168:171], v[108:111], v[32:35], v[168:171]\n"
"buffer_load_dword v249, v10, s[4:7], s97 offen offset:1536 sc1\n"
"v_mfma_f32_16x16x32_bf16 v[168:171], v[116:119], v[44:47], v[168:171]\n"
"s_waitcnt vmcnt(15)\n"
"v_mfma_f32_16x16x32_bf16 v[168:171], v[128:131], v[48:51], v[168:171]\n"
"buffer_load_dwordx4 v[176:179], v0, s[4:7], s98 offen offset:1024 sc1\n"
"v_mfma_f32_16x16x32_bf16 v[168:171], v[132:135], v[52:55], v[168:171]\n"
"ds_write_b128 v3, v[152:155] offset:3072\n"
"v_mfma_f32_16x16x32_bf16 v[168:171], v[136:139], v[56:59], v[168:171]\n"
"buffer_load_dword v248, v10, s[4:7], s97 offen offset:1792 sc1\n"
"v_mfma_f32_16x16x32_bf16 v[168:171], v[96:99], v[60:63], v[168:171]\n"
"s_waitcnt vmcnt(5)\n"
"v_mfma_f32_16x16x32_bf16 v[152:155], v[112:115], v[60:63], 0\n"
"buffer_load_dwordx4 v[60:63], v0, s[4:7], s98 offen offset:2048 sc1\n"
"ds_read_b64_tr_b16 v[180:181], v36 offset:4096\n"
"v_mfma_f32_16x16x32_bf16 v[152:155], v[164:167], v[56:59], v[152:155]\n"
"ds_read_b64_tr_b16 v[182:183], v36 offset:6144\n"
"s_mov_b32 s96, 0x30003\n"
"s_mov_b32 s97, 0x30003\n"
"v_mfma_f32_16x16x32_bf16 v[152:155], v[160:163], v[52:55], v[152:155]\n"
"v_mov_b32_e32 v55, 0x3f803f80\n"
"v_cndmask_b32_e64 v52, 0, v55, s[96:97]\n"
"v_cndmask_b32_e64 v53, 0, v55, s[96:97]\n"
"v_mfma_f32_16x16x32_bf16 v[152:155], v[156:159], v[48:51], v[152:155]\n"
"v_cndmask_b32_e64 v54, 0, v55, s[96:97]\n"
"v_cndmask_b32_e64 v55, 0, v55, s[96:97]\n"
"v_mfma_f32_16x16x32_bf16 v[152:155], v[124:127], v[44:47], v[152:155]\n"
"buffer_load_dwordx4 v[44:47], v0, s[4:7], s98 offen offset:3072 sc1\n"
"s_waitcnt lgkmcnt(0)\n"
"v_mfma_f32_16x16x32_bf16 v[140:143], v[180:183], v[52:55], v[140:143]\n"
"buffer_store_dwordx4 v[180:183], v0, s[4:7], s20 offen offset:1024 sc1\n"
"v_mfma_f32_16x16x32_bf16 v[152:155], v[148:151], v[32:35], v[152:155]\n"
"ds_write_b128 v3, v[168:171] offset:4096\n"
"v_mfma_f32_16x16x32_bf16 v[152:155], v[144:147], v[28:31], v[152:155]\n"
"v_mfma_f32_16x16x32_bf16 v[152:155], v[120:123], v[24:27], v[152:155]\n"
"; @A G1 0.1\n"
"v_mfma_f32_16x16x32_bf16 v[24:27], a[0:3], v[64:67], 0\n"
"buffer_load_dwordx4 v[28:31], v1, s[4:7], s98 offen sc1\n"
"v_mfma_f32_16x16x32_bf16 v[24:27], a[4:7], v[68:71], v[24:27]\n"
"v_mfma_f32_16x16x32_bf16 v[24:27], a[8:11], v[72:75], v[24:27]\n"
"s_add_u32 s97, s55, 0x40000\n"
"buffer_load_dwordx4 v[32:35], v0, s[4:7], s97 offen sc1\n"
"v_mfma_f32_16x16x32_bf16 v[24:27], a[12:15], v[76:79], v[24:27]\n"
"v_mfma_f32_16x16x32_bf16 v[24:27], a[16:19], v[80:83], v[24:27]\n"
"buffer_load_dwordx4 v[48:51], v1, s[4:7], s98 offen offset:1024 sc1\n"
"v_mfma_f32_16x16x32_bf16 v[24:27], a[20:23], v[84:87], v[24:27]\n"
"ds_write_b128 v3, v[152:155] offset:5120\n"
"v_mfma_f32_16x16x32_bf16 v[24:27], a[24:27], v[88:91], v[24:27]\n"
"v_mfma_f32_16x16x32_bf16 v[24:27], a[28:31], v[92:95], v[24:27]\n"
"buffer_load_dwordx4 v[52:55], v0, s[4:7], s97 offen offset:1024 sc1\n"
"v_mfma_f32_16x16x32_bf16 v[56:59], a[60:63], v[92:95], 0\n"
"buffer_load_dwordx4 v[152:155], v1, s[4:7], s98 offen offset:2048 sc1\n"
"v_mfma_f32_16x16x32_bf16 v[56:59], a[56:59], v[88:91], v[56:59]\n"
"v_mfma_f32_16x16x32_bf16 v[56:59], a[52:55], v[84:87], v[56:59]\n"
"v_mfma_f32_16x16x32_bf16 v[56:59], a[48:51], v[80:83], v[56:59]\n"
"v_mfma_f32_16x16x32_bf16 v[56:59], a[44:47], v[76:79], v[56:59]\n"
"buffer_load_dwordx4 v[168:171], v1, s[4:7], s98 offen offset:3072 sc1\n"
"v_mfma_f32_16x16x32_bf16 v[56:59], a[40:43], v[72:75], v[56:59]\n"
"buffer_load_dwordx4 v[180:183], v0, s[4:7], s97 offen offset:2048 sc1\n"
"v_mfma_f32_16x16x32_bf16 v[56:59], a[36:39], v[68:71], v[56:59]\n"
"ds_write_b128 v3, v[24:27] offset:6144\n"
"v_mfma_f32_16x16x32_bf16 v[56:59], a[32:35], v[64:67], v[56:59]\n"
"v_mfma_f32_16x16x32_bf16 v[24:27], a[64:67], v[64:67], 0\n"
"v_mfma_f32_16x16x32_bf16 v[24:27], a[68:71], v[68:71], v[24:27]\n"
"buffer_load_dwordx4 v[184:187], v0, s[4:7], s97 offen offset:3072 sc1\n"
"v_mfma_f32_16x16x32_bf16 v[24:27], a[72:75], v[72:75], v[24:27]\n"
"v_mfma_f32_16x16x32_bf16 v[24:27], a[76:79], v[76:79], v[24:27]\n"
"v_mfma_f32_16x16x32_bf16 v[24:27], a[80:83], v[80:83], v[24:27]\n"
"v_mfma_f32_16x16x32_bf16 v[24:27], a[84:87], v[84:87], v[24:27]\n"
"s_nop 0\n"
"ds_write_b128 v3, v[56:59] offset:7168\n"
"v_mfma_f32_16x16x32_bf16 v[24:27], a[88:91], v[88:91], v[24:27]\n"
"buffer_load_dwordx4 v[56:59], v1, s[4:7], s97 offen sc1\n"
"v_mfma_f32_16x16x32_bf16 v[24:27], a[92:95], v[92:95], v[24:27]\n"
"v_mfma_f32_16x16x32_bf16 v[188:191], a[124:127], v[92:95], 0\n"
"v_mfma_f32_16x16x32_bf16 v[188:191], a[120:123], v[88:91], v[188:191]\n"
"v_mfma_f32_16x16x32_bf16 v[188:191], a[116:119], v[84:87], v[188:191]\n"
"v_mfma_f32_16x16x32_bf16 v[188:191], a[112:115], v[80:83], v[188:191]\n"
"buffer_load_dwordx4 v[192:195], v1, s[4:7], s97 offen offset:1024 sc1\n"
"v_mfma_f32_16x16x32_bf16 v[188:191], a[108:111], v[76:79], v[188:191]\n"
"v_mfma_f32_16x16x32_bf16 v[188:191], a[104:107], v[72:75], v[188:191]\n"
"s_nop 0\n"
"ds_write_b128 v3, v[24:27] offset:8192\n"
"v_mfma_f32_16x16x32_bf16 v[188:191], a[100:103], v[68:71], v[188:191]\n"
"v_mfma_f32_16x16x32_bf16 v[188:191], a[96:99], v[64:67], v[188:191]\n"
"v_mfma_f32_16x16x32_bf16 v[24:27], v[100:103], v[64:67], 0\n"
"buffer_load_dwordx4 v[196:199], v1, s[4:7], s97 offen offset:2048 sc1\n"
"v_mfma_f32_16x16x32_bf16 v[24:27], v[104:107], v[68:71], v[24:27]\n"
"v_mfma_f32_16x16x32_bf16 v[24:27], v[108:111], v[72:75], v[24:27]\n"
"v_mfma_f32_16x16x32_bf16 v[24:27], v[116:119], v[76:79], v[24:27]\n"
"v_mfma_f32_16x16x32_bf16 v[24:27], v[128:131], v[80:83], v[24:27]\n"
"s_waitcnt vmcnt(14)\n"
"ds_read_b64_tr_b16 v[200:201], v37\n"
"v_add_f32_e32 v255, v255, v251\n"
"v_add_f32_e32 v254, v254, v250\n"
"v_mfma_f32_16x16x32_bf16 v[24:27], v[132:135], v[84:87], v[24:27]\n"
"buffer_load_dwordx4 v[204:207], v1, s[4:7], s97 offen offset:3072 sc1\n"
"ds_read_b64_tr_b16 v[202:203], v37 offset:2048\n"
"v_mfma_f32_16x16x32_bf16 v[24:27], v[136:139], v[88:91], v[24:27]\n"
"ds_write_b128 v3, v[188:191] offset:9216\n"
"v_mfma_f32_16x16x32_bf16 v[24:27], v[96:99], v[92:95], v[24:27]\n"
"s_mov_b32 s96, 0xc000c\n"
"v_add_f32_e32 v253, v253, v249\n"
"v_add_f32_e32 v252, v252, v248\n"
"s_mov_b32 s97, 0xc000c\n"
"v_mfma_f32_16x16x32_bf16 v[188:191], v[112:115], v[92:95], 0\n"
"v_add_f32_e32 v255, v255, v253\n"
"v_add_f32_e32 v254, v254, v252\n"
"v_mov_b32_e32 v95, 0x3f803f80\n"
"v_mfma_f32_16x16x32_bf16 v[188:191], v[164:167], v[88:91], v[188:191]\n"
"v_add_f32_e32 v255, v255, v254\n"
"v_mov_b32_e32 v254, v255\n"
"v_cndmask_b32_e64 v92, 0, v95, s[96:97]\n"
"v_mfma_f32_16x16x32_bf16 v[188:191], v[160:163], v[84:87], v[188:191]\n"
"v_permlane32_swap_b32_e32 v255, v254\n"
"v_add_f32_e32 v255, v255, v254\n"
"v_cndmask_b32_e64 v93, 0, v95, s[96:97]\n"
"v_mfma_f32_16x16x32_bf16 v[188:191], v[156:159], v[80:83], v[188:191]\n"
"v_mul_f32_e32 v255, 0x3a800000, v255\n"
"v_mov_b32_e32 v254, v255\n"
"v_cndmask_b32_e64 v94, 0, v95, s[96:97]\n"
"v_mfma_f32_16x16x32_bf16 v[188:191], v[124:127], v[76:79], v[188:191]\n"
"v_permlane16_swap_b32_e32 v255, v254\n"
"v_cvt_pk_bf16_f32 v253, -v255, 0\n"
"v_cndmask_b32_e64 v95, 0, v95, s[96:97]\n"
"v_mfma_f32_16x16x32_bf16 v[188:191], v[148:151], v[72:75], v[188:191]\n"
"ds_write_b128 v3, v[24:27] offset:10240\n"
"v_mfma_f32_16x16x32_bf16 v[188:191], v[144:147], v[68:71], v[188:191]\n"
"s_waitcnt lgkmcnt(2)\n"
"v_fma_f32 v254, -v255, v255, v254\n"
"v_lshlrev_b32_e32 v253, 16, v253\n"
"v_mfma_f32_16x16x32_bf16 v[140:143], v[200:203], v[92:95], v[140:143]\n"
"buffer_store_dwordx4 v[200:203], v0, s[4:7], s21 offen sc1\n"
"v_mfma_f32_16x16x32_bf16 v[188:191], v[120:123], v[64:67], v[188:191]\n"
"s_waitcnt vmcnt(14)\n"
"v_max_f32_e32 v254, 0, v254\n"
"v_cvt_pk_bf16_f32 v254, v254, 0\n"
"; @A G1 0.2\n"
"v_mfma_f32_16x16x32_bf16 v[24:27], a[0:3], v[172:175], 0\n"
"v_lshlrev_b32_e32 v254, 16, v254\n"
"v_add_f32_e32 v254, 0x3727c5ac, v254\n"
"v_mfma_f32_16x16x32_bf16 v[24:27], a[4:7], v[176:179], v[24:27]\n"
"v_cvt_pk_bf16_f32 v254, v254, 0\n"
"v_lshlrev_b32_e32 v254, 16, v254\n"
"v_mfma_f32_16x16x32_bf16 v[24:27], a[8:11], v[60:63], v[24:27]\n"
"v_sqrt_f32_e32 v254, v254\n"
"v_mfma_f32_16x16x32_bf16 v[24:27], a[12:15], v[44:47], v[24:27]\n"
"s_waitcnt vmcnt(7)\n"
"v_cvt_pk_bf16_f32 v254, v254, 0\n"
"v_lshlrev_b32_e32 v254, 16, v254\n"
"v_mfma_f32_16x16x32_bf16 v[24:27], a[16:19], v[28:31], v[24:27]\n"
"v_rcp_f32_e32 v255, v254\n"
"v_mfma_f32_16x16x32_bf16 v[24:27], a[20:23], v[48:51], v[24:27]\n"
"ds_write_b128 v3, v[188:191] offset:11264\n"
"v_mfma_f32_16x16x32_bf16 v[24:27], a[24:27], v[152:155], v[24:27]\n"
"v_mul_f32_e32 v254, 0x3e8293ee, v255\n"
"v_mfma_f32_16x16x32_bf16 v[24:27], a[28:31], v[168:171], v[24:27]\n"
"v_mfma_f32_16x16x32_bf16 v[64:67], a[60:63], v[168:171], 0\n"
"v_mfma_f32_16x16x32_bf16 v[64:67], a[56:59], v[152:155], v[64:67]\n"
"v_mfma_f32_16x16x32_bf16 v[64:67], a[52:55], v[48:51], v[64:67]\n"
"s_add_u32 s98, s52, 0x48000\n"
"buffer_load_dwordx4 v[68:71], v0, s[4:7], s98 offen sc1\n"
"v_mfma_f32_16x16x32_bf16 v[64:67], a[48:51], v[28:31], v[64:67]\n"
"s_waitcnt lgkmcnt(0)\n"
"s_barrier\n"
"v_mfma_f32_16x16x32_bf16 v[64:67], a[44:47], v[44:47], v[64:67]\n"
"ds_read_b128 v[72:75], v4\n"
"ds_read_b128 v[76:79], v9 offset:128\n"
"v_mfma_f32_16x16x32_bf16 v[64:67], a[40:43], v[60:63], v[64:67]\n"
"ds_write_b128 v3, v[24:27] offset:12288\n"
"v_mfma_f32_16x16x32_bf16 v[64:67], a[36:39], v[176:179], v[64:67]\n"
"ds_read_b128 v[24:27], v5\n"
"v_mfma_f32_16x16x32_bf16 v[64:67], a[32:35], v[172:175], v[64:67]\n"
"buffer_load_dwordx4 v[80:83], v0, s[4:7], s98 offen offset:1024 sc1\n"
"v_mfma_f32_16x16x32_bf16 v[84:87], a[64:67], v[172:175], 0\n"
"v_mfma_f32_16x16x32_bf16 v[84:87], a[68:71], v[176:179], v[84:87]\n"
"v_mfma_f32_16x16x32_bf16 v[84:87], a[72:75], v[60:63], v[84:87]\n"
"v_mfma_f32_16x16x32_bf16 v[84:87], a[76:79], v[44:47], v[84:87]\n"
"ds_read_b128 v[88:91], v4 offset:1024\n"
"ds_read_b128 v[92:95], v9 offset:192\n"
"v_mfma_f32_16x16x32_bf16 v[84:87], a[80:83], v[28:31], v[84:87]\n"
"buffer_load_dwordx4 v[188:191], v0, s[4:7], s98 offen offset:2048 sc1\n"
"v_mfma_f32_16x16x32_bf16 v[84:87], a[84:87], v[48:51], v[84:87]\n"
"ds_write_b128 v3, v[64:67] offset:13312\n"
"v_mfma_f32_16x16x32_bf16 v[84:87], a[88:91], v[152:155], v[84:87]\n"
"ds_read_b128 v[64:67], v5 offset:1024\n"
"s_waitcnt lgkmcnt(4)\n"
"v_mfma_f32_16x16x32_bf16 v[84:87], a[92:95], v[168:171], v[84:87]\n"
"v_fmac_f32_e32 v72, v253, v76\n"
"v_fmac_f32_e32 v73, v253, v77\n"
"v_mfma_f32_16x16x32_bf16 v[200:203], a[124:127], v[168:171], 0\n"
"v_fmac_f32_e32 v74, v253, v78\n"
"v_fmac_f32_e32 v75, v253, v79\n"
"v_mfma_f32_16x16x32_bf16 v[200:203], a[120:123], v[152:155], v[200:203]\n"
"buffer_load_dwordx4 v[76:79], v0, s[4:7], s98 offen offset:3072 sc1\n"
"v_mfma_f32_16x16x32_bf16 v[200:203], a[116:119], v[48:51], v[200:203]\n"
"ds_read_b128 v[208:211], v4 offset:2048\n"
"ds_read_b128 v[212:215], v9 offset:256\n"
"v_mfma_f32_16x16x32_bf16 v[200:203], a[112:115], v[28:31], v[200:203]\n"
"ds_read_b128 v[216:219], v5 offset:2048\n"
"v_add_f32_e32 v72, v72, v24\n"
"v_mfma_f32_16x16x32_bf16 v[200:203], a[108:111], v[44:47], v[200:203]\n"
"v_add_f32_e32 v73, v73, v25\n"
"v_add_f32_e32 v74, v74, v26\n"
"v_mfma_f32_16x16x32_bf16 v[200:203], a[104:107], v[60:63], v[200:203]\n"
"ds_write_b128 v3, v[84:87] offset:14336\n"
"v_mfma_f32_16x16x32_bf16 v[200:203], a[100:103], v[176:179], v[200:203]\n"
"buffer_load_dwordx4 v[84:87], v1, s[4:7], s98 offen sc1\n"
"v_mfma_f32_16x16x32_bf16 v[200:203], a[96:99], v[172:175], v[200:203]\n"
"s_waitcnt lgkmcnt(4)\n"
"v_add_f32_e32 v75, v75, v27\n"
"v_fmac_f32_e32 v88, v253, v92\n"
"v_mfma_f32_16x16x32_bf16 v[24:27], v[100:103], v[172:175], 0\n"
"ds_read_b64_tr_b16 v[220:221], v37 offset:4096\n"
"v_fmac_f32_e32 v89, v253, v93\n"
"v_fmac_f32_e32 v90, v253, v94\n"
"v_mfma_f32_16x16x32_bf16 v[24:27], v[104:107], v[176:179], v[24:27]\n"
"ds_read_b128 v[224:227], v4 offset:3072\n"
"ds_read_b128 v[228:231], v9 offset:320\n"
"ds_read_b64_tr_b16 v[222:223], v37 offset:6144\n"
"v_mfma_f32_16x16x32_bf16 v[24:27], v[108:111], v[60:63], v[24:27]\n"
"ds_read_b128 v[232:235], v5 offset:3072\n"
"v_fmac_f32_e32 v91, v253, v95\n"
"s_mov_b32 s96, 0xc000c\n"
"v_mfma_f32_16x16x32_bf16 v[24:27], v[116:119], v[44:47], v[24:27]\n"
"buffer_load_dwordx4 v[92:95], v1, s[4:7], s98 offen offset:1024 sc1\n"
"s_mov_b32 s97, 0xc000c\n"
"v_mfma_f32_16x16x32_bf16 v[24:27], v[128:131], v[28:31], v[24:27]\n"
"v_add_f32_e32 v88, v88, v64\n"
"v_add_f32_e32 v89, v89, v65\n"
"v_mov_b32_e32 v239, 0x3f803f80\n"
"v_mfma_f32_16x16x32_bf16 v[24:27], v[132:135], v[48:51], v[24:27]\n"
"ds_write_b128 v3, v[200:203] offset:15360\n"
"v_mfma_f32_16x16x32_bf16 v[24:27], v[136:139], v[152:155], v[24:27]\n"
"s_waitcnt lgkmcnt(7)\n"
"v_add_f32_e32 v90, v90, v66\n"
"v_add_f32_e32 v91, v91, v67\n"
"v_cndmask_b32_e64 v236, 0, v239, s[96:97]\n"
"v_mfma_f32_16x16x32_bf16 v[24:27], v[96:99], v[168:171], v[24:27]\n"
"v_fmac_f32_e32 v208, v253, v212\n"
"v_fmac_f32_e32 v209, v253, v213\n"
"v_cndmask_b32_e64 v237, 0, v239, s[96:97]\n"
"v_mfma_f32_16x16x32_bf16 v[64:67], v[112:115], v[168:171], 0\n"
"buffer_load_dwordx4 v[168:171], v1, s[4:7], s98 offen offset:2048 sc1\n"
"v_cndmask_b32_e64 v238, 0, v239, s[96:97]\n"
"v_mfma_f32_16x16x32_bf16 v[64:67], v[164:167], v[152:155], v[64:67]\n"
"s_add_u32 s95, s68, s80\n"
"ds_read_b128 v[152:155], v4 offset:4096\n"
"v_cndmask_b32_e64 v239, 0, v239, s[96:97]\n"
"v_mfma_f32_16x16x32_bf16 v[64:67], v[160:163], v[48:51], v[64:67]\n"
"buffer_store_dwordx2 v[18:19], v11, s[4:7], s95 offen sc1\n"
"v_mfma_f32_16x16x32_bf16 v[64:67], v[156:159], v[28:31], v[64:67]\n"
"ds_read_b128 v[28:31], v9\n"
"ds_read_b128 v[48:51], v5 offset:4096\n"
"s_waitcnt lgkmcnt(5)\n"
"v_mfma_f32_16x16x32_bf16 v[140:143], v[220:223], v[236:239], v[140:143]\n"
"buffer_store_dwordx4 v[220:223], v0, s[4:7], s21 offen offset:1024 sc1\n"
"v_mfma_f32_16x16x32_bf16 v[64:67], v[124:127], v[44:47], v[64:67]\n"
"s_add_u32 s97, s75, 0x10000\n"
"buffer_load_dwordx2 v[250:251], v41, s[4:7], s97 offen sc1\n"
"v_mfma_f32_16x16x32_bf16 v[64:67], v[148:151], v[60:63], v[64:67]\n"
"buffer_load_dwordx4 v[44:47], v1, s[4:7], s98 offen offset:3072 sc1\n"
"v_mfma_f32_16x16x32_bf16 v[64:67], v[144:147], v[176:179], v[64:67]\n"
"ds_write_b128 v3, v[24:27] offset:16384\n"
"v_mfma_f32_16x16x32_bf16 v[64:67], v[120:123], v[172:175], v[64:67]\n"
"s_waitcnt vmcnt(16)\n"
"ds_read_b128 v[24:27], v4 offset:5120\n"
"ds_read_b128 v[60:63], v9 offset:64\n"
"; @A G1 0.3\n"
"v_mfma_f32_16x16x32_bf16 v[72:75], a[0:3], v[32:35], v[72:75]\n"
"ds_read_b128 v[172:175], v5 offset:5120\n"
"v_fmac_f32_e32 v210, v253, v214\n"
"v_mfma_f32_16x16x32_bf16 v[72:75], a[4:7], v[52:55], v[72:75]\n"
"v_fmac_f32_e32 v211, v253, v215\n"
"v_add_f32_e32 v208, v208, v216\n"
"v_mfma_f32_16x16x32_bf16 v[72:75], a[8:11], v[180:183], v[72:75]\n"
"s_add_u32 s98, s53, 0x48000\n"
"buffer_load_dwordx4 v[176:179], v0, s[4:7], s98 offen sc1\n"
"v_mfma_f32_16x16x32_bf16 v[72:75], a[12:15], v[184:187], v[72:75]\n"
"s_waitcnt vmcnt(13)\n"
"v_add_f32_e32 v209, v209, v217\n"
"v_add_f32_e32 v210, v210, v218\n"
"v_mfma_f32_16x16x32_bf16 v[72:75], a[16:19], v[56:59], v[72:75]\n"
"s_waitcnt lgkmcnt(8)\n"
"v_add_f32_e32 v211, v211, v219\n"
"v_fmac_f32_e32 v224, v253, v228\n"
"v_mfma_f32_16x16x32_bf16 v[72:75], a[20:23], v[192:195], v[72:75]\n"
"ds_write_b128 v3, v[64:67] offset:17408\n"
"v_mfma_f32_16x16x32_bf16 v[72:75], a[24:27], v[196:199], v[72:75]\n"
"v_fmac_f32_e32 v225, v253, v229\n"
"v_fmac_f32_e32 v226, v253, v230\n"
"v_mfma_f32_16x16x32_bf16 v[72:75], a[28:31], v[204:207], v[72:75]\n"
"buffer_load_dwordx4 v[64:67], v0, s[4:7], s98 offen offset:1024 sc1\n"
"v_mfma_f32_16x16x32_bf16 v[88:91], a[60:63], v[204:207], v[88:91]\n"
"v_fmac_f32_e32 v227, v253, v231\n"
"v_add_f32_e32 v224, v224, v232\n"
"v_mfma_f32_16x16x32_bf16 v[88:91], a[56:59], v[196:199], v[88:91]\n"
"v_add_f32_e32 v225, v225, v233\n"
"v_add_f32_e32 v226, v226, v234\n"
"v_mfma_f32_16x16x32_bf16 v[88:91], a[52:55], v[192:195], v[88:91]\n"
"s_waitcnt lgkmcnt(0)\n"
"s_barrier\n"
"v_mfma_f32_16x16x32_bf16 v[88:91], a[48:51], v[56:59], v[88:91]\n"
"ds_read_b128 v[200:203], v6\n"
"ds_read_b128 v[212:215], v6 offset:1024\n"
"v_mfma_f32_16x16x32_bf16 v[88:91], a[44:47], v[184:187], v[88:91]\n"
"buffer_load_dwordx4 v[216:219], v0, s[4:7], s98 offen offset:2048 sc1\n"
"v_mfma_f32_16x16x32_bf16 v[88:91], a[40:43], v[180:183], v[88:91]\n"
"v_add_f32_e32 v227, v227, v235\n"
"v_fmac_f32_e32 v152, v253, v28\n"
"v_mfma_f32_16x16x32_bf16 v[88:91], a[36:39], v[52:55], v[88:91]\n"
"v_fmac_f32_e32 v153, v253, v29\n"
"v_fmac_f32_e32 v154, v253, v30\n"
"v_mfma_f32_16x16x32_bf16 v[88:91], a[32:35], v[32:35], v[88:91]\n"
"v_fmac_f32_e32 v155, v253, v31\n"
"v_add_f32_e32 v152, v152, v48\n"
"v_mfma_f32_16x16x32_bf16 v[208:211], a[64:67], v[32:35], v[208:211]\n"
"v_add_f32_e32 v153, v153, v49\n"
"v_add_f32_e32 v154, v154, v50\n"
"v_mfma_f32_16x16x32_bf16 v[208:211], a[68:71], v[52:55], v[208:211]\n"
"buffer_load_dwordx4 v[28:31], v0, s[4:7], s98 offen offset:3072 sc1\n"
"v_mfma_f32_16x16x32_bf16 v[208:211], a[72:75], v[180:183], v[208:211]\n"
"v_add_f32_e32 v155, v155, v51\n"
"v_fmac_f32_e32 v24, v253, v60\n"
"v_mfma_f32_16x16x32_bf16 v[208:211], a[76:79], v[184:187], v[208:211]\n"
"v_fmac_f32_e32 v25, v253, v61\n"
"v_fmac_f32_e32 v26, v253, v62\n"
"v_mfma_f32_16x16x32_bf16 v[208:211], a[80:83], v[56:59], v[208:211]\n"
"s_waitcnt lgkmcnt(0)\n"
"v_fmac_f32_e32 v27, v253, v63\n"
"v_add_f32_e32 v24, v24, v172\n"
"v_mfma_f32_16x16x32_bf16 v[208:211], a[84:87], v[192:195], v[208:211]\n"
"v_add_f32_e32 v25, v25, v173\n"
"v_add_f32_e32 v26, v26, v174\n"
"v_mfma_f32_16x16x32_bf16 v[208:211], a[88:91], v[196:199], v[208:211]\n"
"buffer_load_dwordx4 v[48:51], v1, s[4:7], s98 offen sc1\n"
"v_mfma_f32_16x16x32_bf16 v[208:211], a[92:95], v[204:207], v[208:211]\n"
"ds_read_b128 v[60:63], v6 offset:2048\n"
"ds_read_b128 v[220:223], v6 offset:3072\n"
"v_mfma_f32_16x16x32_bf16 v[224:227], a[124:127], v[204:207], v[224:227]\n"
"v_add_f32_e32 v27, v27, v175\n"
"v_add_f32_e32 v72, v72, v200\n"
"v_mfma_f32_16x16x32_bf16 v[224:227], a[120:123], v[196:199], v[224:227]\n"
"v_add_f32_e32 v73, v73, v201\n"
"v_add_f32_e32 v74, v74, v202\n"
"v_mfma_f32_16x16x32_bf16 v[224:227], a[116:119], v[192:195], v[224:227]\n"
"v_add_f32_e32 v75, v75, v203\n"
"v_add_f32_e32 v88, v88, v212\n"
"v_mfma_f32_16x16x32_bf16 v[224:227], a[112:115], v[56:59], v[224:227]\n"
"buffer_load_dwordx4 v[172:175], v1, s[4:7], s98 offen offset:1024 sc1\n"
"v_mfma_f32_16x16x32_bf16 v[224:227], a[108:111], v[184:187], v[224:227]\n"
"ds_read_b64_tr_b16 v[200:201], v38\n"
"v_add_f32_e32 v89, v89, v213\n"
"v_add_f32_e32 v90, v90, v214\n"
"v_mfma_f32_16x16x32_bf16 v[224:227], a[104:107], v[180:183], v[224:227]\n"
"ds_read_b64_tr_b16 v[202:203], v38 offset:2048\n"
"v_add_f32_e32 v91, v91, v215\n"
"v_mul_f32_e32 v72, v72, v255\n"
"v_mfma_f32_16x16x32_bf16 v[224:227], a[100:103], v[52:55], v[224:227]\n"
"s_mov_b32 s94, 0x300030\n"
"v_mul_f32_e32 v73, v73, v255\n"
"v_mul_f32_e32 v74, v74, v255\n"
"s_mov_b32 s95, 0x300030\n"
"v_mfma_f32_16x16x32_bf16 v[224:227], a[96:99], v[32:35], v[224:227]\n"
"v_mul_f32_e32 v75, v75, v255\n"
"v_mul_f32_e32 v88, v88, v255\n"
"v_mov_b32_e32 v215, 0x3f803f80\n"
"v_mfma_f32_16x16x32_bf16 v[152:155], v[100:103], v[32:35], v[152:155]\n"
"buffer_load_dwordx4 v[228:231], v1, s[4:7], s98 offen offset:2048 sc1\n"
"v_cndmask_b32_e64 v212, 0, v215, s[94:95]\n"
"v_mfma_f32_16x16x32_bf16 v[152:155], v[104:107], v[52:55], v[152:155]\n"
"v_mul_f32_e32 v89, v89, v255\n"
"v_mul_f32_e32 v90, v90, v255\n"
"v_cndmask_b32_e64 v213, 0, v215, s[94:95]\n"
"v_mfma_f32_16x16x32_bf16 v[152:155], v[108:111], v[180:183], v[152:155]\n"
"v_mul_f32_e32 v91, v91, v255\n"
"v_cvt_pk_bf16_f32 v232, v72, v73\n"
"v_cndmask_b32_e64 v214, 0, v215, s[94:95]\n"
"v_mfma_f32_16x16x32_bf16 v[152:155], v[116:119], v[184:187], v[152:155]\n"
"v_cvt_pk_bf16_f32 v233, v74, v75\n"
"v_cvt_pk_bf16_f32 v234, v88, v89\n"
"v_cndmask_b32_e64 v215, 0, v215, s[94:95]\n"
"v_mfma_f32_16x16x32_bf16 v[152:155], v[128:131], v[56:59], v[152:155]\n"
"s_waitcnt lgkmcnt(2)\n"
"v_cvt_pk_bf16_f32 v235, v90, v91\n"
"v_add_f32_e32 v208, v208, v60\n"
"s_waitcnt lgkmcnt(0)\n"
"v_mfma_f32_16x16x32_bf16 v[140:143], v[200:203], v[212:215], v[140:143]\n"
"buffer_store_dwordx4 v[200:203], v0, s[4:7], s22 offen sc1\n"
"v_mfma_f32_16x16x32_bf16 v[152:155], v[132:135], v[192:195], v[152:155]\n"
"buffer_load_dwordx4 v[72:75], v1, s[4:7], s98 offen offset:3072 sc1\n"
"ds_write_b128 v7, v[232:235] offset:24576\n"
"v_mfma_f32_16x16x32_bf16 v[152:155], v[136:139], v[196:199], v[152:155]\n"
"ds_read_b128 v[88:91], v6 offset:4096\n"
"ds_read_b128 v[200:203], v6 offset:5120\n"
"v_mfma_f32_16x16x32_bf16 v[152:155], v[96:99], v[204:207], v[152:155]\n"
"v_add_f32_e32 v209, v209, v61\n"
"v_add_f32_e32 v210, v210, v62\n"
"v_mfma_f32_16x16x32_bf16 v[24:27], v[112:115], v[204:207], v[24:27]\n"
"s_waitcnt vmcnt(10)\n"
"v_cmp_ne_u64_e32 vcc, v[250:251], v[18:19]\n"
"v_mfma_f32_16x16x32_bf16 v[24:27], v[164:167], v[196:199], v[24:27]\n"
"s_cbranch_vccnz .Lmk_f6s\n"
".Lmk_f6r:\n"
"v_add_f32_e32 v211, v211, v63\n"
"v_mfma_f32_16x16x32_bf16 v[24:27], v[160:163], v[192:195], v[24:27]\n"
"v_add_f32_e32 v224, v224, v220\n"
"v_add_f32_e32 v225, v225, v221\n"
"v_mfma_f32_16x16x32_bf16 v[24:27], v[156:159], v[56:59], v[24:27]\n"
"v_add_f32_e32 v226, v226, v222\n"
"v_add_f32_e32 v227, v227, v223\n"
"v_mfma_f32_16x16x32_bf16 v[24:27], v[124:127], v[184:187], v[24:27]\n"
"v_mul_f32_e32 v208, v208, v255\n"
"v_mul_f32_e32 v209, v209, v255\n"
"v_mfma_f32_16x16x32_bf16 v[24:27], v[148:151], v[180:183], v[24:27]\n"
"v_mul_f32_e32 v210, v210, v255\n"
"v_mul_f32_e32 v211, v211, v255\n"
"v_mfma_f32_16x16x32_bf16 v[24:27], v[144:147], v[52:55], v[24:27]\n"
"v_mul_f32_e32 v224, v224, v255\n"
"v_mul_f32_e32 v225, v225, v255\n"
"v_mfma_f32_16x16x32_bf16 v[24:27], v[120:123], v[32:35], v[24:27]\n"
"v_mul_f32_e32 v226, v226, v255\n"
"v_mul_f32_e32 v227, v227, v255\n"
"v_cvt_pk_bf16_f32 v32, v208, v209\n"
"v_cvt_pk_bf16_f32 v33, v210, v211\n"
"v_cvt_pk_bf16_f32 v34, v224, v225\n"
"v_cvt_pk_bf16_f32 v35, v226, v227\n"
"ds_write_b128 v7, v[32:35] offset:57344\n"
".Lmk_z1:\n"
"; @A G1 1.0\n"
"v_mfma_f32_16x16x32_bf16 v[40:43], a[0:3], v[68:71], 0\n"
"v_mfma_f32_16x16x32_bf16 v[40:43], a[4:7], v[80:83], v[40:43]\n"
"s_add_u32 s98, s62, 0x4800\n"
"v_mfma_f32_16x16x32_bf16 v[40:43], a[8:11], v[188:191], v[40:43]\n"
"s_add_u32 s97, s54, 0x48000\n"
"buffer_load_dwordx4 v[52:55], v0, s[4:7], s97 offen sc1\n"
"v_mfma_f32_16x16x32_bf16 v[40:43], a[12:15], v[76:79], v[40:43]\n"
"s_waitcnt vmcnt(10)\n"
"v_mfma_f32_16x16x32_bf16 v[40:43], a[16:19], v[84:87], v[40:43]\n"
"buffer_load_dword v255, v10, s[4:7], s98 offen sc1\n"
"v_mfma_f32_16x16x32_bf16 v[40:43], a[20:23], v[92:95], v[40:43]\n"
"s_waitcnt lgkmcnt(1)\n"
"v_add_f32_e32 v152, v152, v88\n"
"v_add_f32_e32 v153, v153, v89\n"
"v_mfma_f32_16x16x32_bf16 v[40:43], a[24:27], v[168:171], v[40:43]\n"
"v_add_f32_e32 v154, v154, v90\n"
"v_add_f32_e32 v155, v155, v91\n"
"v_mfma_f32_16x16x32_bf16 v[40:43], a[28:31], v[44:47], v[40:43]\n"
"buffer_load_dwordx4 v[56:59], v0, s[4:7], s97 offen offset:1024 sc1\n"
"v_mfma_f32_16x16x32_bf16 v[60:63], a[60:63], v[44:47], 0\n"
"s_add_u32 s96, s70, 0\n"
"buffer_load_dwordx4 a[128:131], v0, s[4:7], s96 offen sc1\n"
"v_mfma_f32_16x16x32_bf16 v[60:63], a[56:59], v[168:171], v[60:63]\n"
"buffer_load_dword v253, v10, s[4:7], s98 offen offset:256 sc1\n"
"v_mfma_f32_16x16x32_bf16 v[60:63], a[52:55], v[92:95], v[60:63]\n"
"v_add_f32_e32 v24, v24, v200\n"
"v_add_f32_e32 v25, v25, v201\n"
"v_mfma_f32_16x16x32_bf16 v[60:63], a[48:51], v[84:87], v[60:63]\n"
"v_add_f32_e32 v26, v26, v202\n"
"v_add_f32_e32 v27, v27, v203\n"
"v_mfma_f32_16x16x32_bf16 v[60:63], a[44:47], v[76:79], v[60:63]\n"
"buffer_load_dwordx4 v[88:91], v0, s[4:7], s97 offen offset:2048 sc1\n"
"v_mfma_f32_16x16x32_bf16 v[60:63], a[40:43], v[188:191], v[60:63]\n"
"ds_write_b128 v3, v[40:43]\n"
"v_mfma_f32_16x16x32_bf16 v[60:63], a[36:39], v[80:83], v[60:63]\n"
"buffer_load_dword v252, v10, s[4:7], s98 offen offset:512 sc1\n"
"v_mfma_f32_16x16x32_bf16 v[60:63], a[32:35], v[68:71], v[60:63]\n"
"v_mul_f32_e32 v152, v152, v254\n"
"v_mul_f32_e32 v153, v153, v254\n"
"v_mfma_f32_16x16x32_bf16 v[40:43], a[64:67], v[68:71], 0\n"
"v_mul_f32_e32 v154, v154, v254\n"
"v_mul_f32_e32 v155, v155, v254\n"
"v_mfma_f32_16x16x32_bf16 v[40:43], a[68:71], v[80:83], v[40:43]\n"
"buffer_load_dwordx4 v[180:183], v0, s[4:7], s97 offen offset:3072 sc1\n"
"v_mfma_f32_16x16x32_bf16 v[40:43], a[72:75], v[188:191], v[40:43]\n"
"buffer_load_dword v251, v10, s[4:7], s98 offen offset:768 sc1\n"
"v_mfma_f32_16x16x32_bf16 v[40:43], a[76:79], v[76:79], v[40:43]\n"
"v_mul_f32_e32 v24, v24, v254\n"
"v_mul_f32_e32 v25, v25, v254\n"
"v_mfma_f32_16x16x32_bf16 v[40:43], a[80:83], v[84:87], v[40:43]\n"
"v_mul_f32_e32 v26, v26, v254\n"
"v_mul_f32_e32 v27, v27, v254\n"
"v_mfma_f32_16x16x32_bf16 v[40:43], a[84:87], v[92:95], v[40:43]\n"
"ds_write_b128 v3, v[60:63] offset:1024\n"
"v_mfma_f32_16x16x32_bf16 v[40:43], a[88:91], v[168:171], v[40:43]\n"
"buffer_load_dwordx4 v[60:63], v1, s[4:7], s97 offen sc1\n"
"v_mfma_f32_16x16x32_bf16 v[40:43], a[92:95], v[44:47], v[40:43]\n"
"buffer_load_dword v254, v10, s[4:7], s98 offen offset:1024 sc1\n"
"v_mfma_f32_16x16x32_bf16 v[184:187], a[124:127], v[44:47], 0\n"
"v_cvt_pk_bf16_f32 v192, v152, v153\n"
"v_cvt_pk_bf16_f32 v193, v154, v155\n"
"v_mfma_f32_16x16x32_bf16 v[184:187], a[120:123], v[168:171], v[184:187]\n"
"v_cvt_pk_bf16_f32 v194, v24, v25\n"
"v_cvt_pk_bf16_f32 v195, v26, v27\n"
"v_mfma_f32_16x16x32_bf16 v[184:187], a[116:119], v[92:95], v[184:187]\n"
"buffer_load_dword v250, v10, s[4:7], s98 offen offset:1280 sc1\n"
"v_mfma_f32_16x16x32_bf16 v[184:187], a[112:115], v[84:87], v[184:187]\n"
"buffer_load_dwordx4 v[24:27], v1, s[4:7], s97 offen offset:1024 sc1\n"
"v_mfma_f32_16x16x32_bf16 v[184:187], a[108:111], v[76:79], v[184:187]\n"
"buffer_load_dwordx4 a[132:135], v0, s[4:7], s96 offen offset:1024 sc1\n"
"v_mfma_f32_16x16x32_bf16 v[184:187], a[104:107], v[188:191], v[184:187]\n"
"ds_write_b128 v3, v[40:43] offset:2048\n"
"v_mfma_f32_16x16x32_bf16 v[184:187], a[100:103], v[80:83], v[184:187]\n"
"buffer_load_dword v249, v10, s[4:7], s98 offen offset:1536 sc1\n"
"v_mfma_f32_16x16x32_bf16 v[184:187], a[96:99], v[68:71], v[184:187]\n"
"ds_write_b128 v2, v[192:195] offset:49152\n"
"v_mfma_f32_16x16x32_bf16 v[40:43], v[100:103], v[68:71], 0\n"
"buffer_load_dwordx4 v[152:155], v1, s[4:7], s97 offen offset:2048 sc1\n"
"v_mfma_f32_16x16x32_bf16 v[40:43], v[104:107], v[80:83], v[40:43]\n"
"buffer_load_dword v248, v10, s[4:7], s98 offen offset:1792 sc1\n"
"v_mfma_f32_16x16x32_bf16 v[40:43], v[108:111], v[188:191], v[40:43]\n"
"v_mfma_f32_16x16x32_bf16 v[40:43], v[116:119], v[76:79], v[40:43]\n"
"v_mfma_f32_16x16x32_bf16 v[40:43], v[128:131], v[84:87], v[40:43]\n"
"v_mfma_f32_16x16x32_bf16 v[40:43], v[132:135], v[92:95], v[40:43]\n"
"buffer_load_dwordx4 v[192:195], v1, s[4:7], s97 offen offset:3072 sc1\n"
"v_mfma_f32_16x16x32_bf16 v[40:43], v[136:139], v[168:171], v[40:43]\n"
"ds_write_b128 v3, v[184:187] offset:3072\n"
"v_mfma_f32_16x16x32_bf16 v[40:43], v[96:99], v[44:47], v[40:43]\n"
"v_mfma_f32_16x16x32_bf16 v[184:187], v[112:115], v[44:47], 0\n"
"v_mfma_f32_16x16x32_bf16 v[184:187], v[164:167], v[168:171], v[184:187]\n"
"v_mfma_f32_16x16x32_bf16 v[184:187], v[160:163], v[92:95], v[184:187]\n"
"v_mfma_f32_16x16x32_bf16 v[184:187], v[156:159], v[84:87], v[184:187]\n"
"v_mfma_f32_16x16x32_bf16 v[184:187], v[124:127], v[76:79], v[184:187]\n"
"v_mfma_f32_16x16x32_bf16 v[184:187], v[148:151], v[188:191], v[184:187]\n"
"s_nop 1\n"
"ds_write_b128 v3, v[40:43] offset:4096\n"
"v_mfma_f32_16x16x32_bf16 v[184:187], v[144:147], v[80:83], v[184:187]\n"
"v_mfma_f32_16x16x32_bf16 v[184:187], v[120:123], v[68:71], v[184:187]\n"
"s_waitcnt vmcnt(23)\n"
"; @A G1 1.1\n"
"v_mfma_f32_16x16x32_bf16 v[40:43], a[0:3], v[176:179], 0\n"
"buffer_load_dwordx4 a[136:139], v0, s[4:7], s96 offen offset:2048 sc1\n"
"v_mfma_f32_16x16x32_bf16 v[40:43], a[4:7], v[64:67], v[40:43]\n"
"v_mov_b32_e32 v44, -1\n"
"v_mov_b32_e32 v45, -1\n"
"v_mfma_f32_16x16x32_bf16 v[40:43], a[8:11], v[216:219], v[40:43]\n"
"s_add_u32 s98, s55, 0x48000\n"
"buffer_load_dwordx4 v[68:71], v0, s[4:7], s98 offen sc1\n"
"v_mfma_f32_16x16x32_bf16 v[40:43], a[12:15], v[28:31], v[40:43]\n"
"s_waitcnt vmcnt(20)\n"
"v_mov_b32_e32 v46, -1\n"
"v_mov_b32_e32 v47, -1\n"
"v_mfma_f32_16x16x32_bf16 v[40:43], a[16:19], v[48:51], v[40:43]\n"
"ds_read_b64_tr_b16 v[76:77], v38 offset:4096\n"
"ds_read_b64_tr_b16 v[78:79], v38 offset:6144\n"
"s_mov_b32 s94, 0x300030\n"
"v_mfma_f32_16x16x32_bf16 v[40:43], a[20:23], v[172:175], v[40:43]\n"
"ds_write_b128 v3, v[184:187] offset:5120\n"
"v_mfma_f32_16x16x32_bf16 v[40:43], a[24:27], v[228:231], v[40:43]\n"
"s_mov_b32 s95, 0x300030\n"
"v_mov_b32_e32 v83, 0x3f803f80\n"
"v_cndmask_b32_e64 v80, 0, v83, s[94:95]\n"
"v_cndmask_b32_e64 v81, 0, v83, s[94:95]\n"
"v_mfma_f32_16x16x32_bf16 v[40:43], a[28:31], v[72:75], v[40:43]\n"
"buffer_load_dwordx4 v[84:87], v0, s[4:7], s98 offen offset:1024 sc1\n"
"v_cndmask_b32_e64 v82, 0, v83, s[94:95]\n"
"v_mfma_f32_16x16x32_bf16 v[92:95], a[60:63], v[72:75], 0\n"
"v_cndmask_b32_e64 v83, 0, v83, s[94:95]\n"
"v_mfma_f32_16x16x32_bf16 v[92:95], a[56:59], v[228:231], v[92:95]\n"
"s_waitcnt lgkmcnt(1)\n"
"v_mfma_f32_16x16x32_bf16 v[140:143], v[76:79], v[80:83], v[140:143]\n"
"buffer_store_dwordx4 v[76:79], v0, s[4:7], s22 offen offset:1024 sc1\n"
"v_mfma_f32_16x16x32_bf16 v[92:95], a[52:55], v[172:175], v[92:95]\n"
"v_mfma_f32_16x16x32_bf16 v[92:95], a[48:51], v[48:51], v[92:95]\n"
"v_mfma_f32_16x16x32_bf16 v[92:95], a[44:47], v[28:31], v[92:95]\n"
"buffer_load_dwordx4 v[76:79], v0, s[4:7], s98 offen offset:2048 sc1\n"
"v_mfma_f32_16x16x32_bf16 v[92:95], a[40:43], v[216:219], v[92:95]\n"
"ds_write_b128 v3, v[40:43] offset:6144\n"
"v_mfma_f32_16x16x32_bf16 v[92:95], a[36:39], v[64:67], v[92:95]\n"
"s_add_u32 s97, s67, 0\n"
"buffer_load_dwordx2 v[246:247], v12, s[4:7], s97 offen sc1\n"
"s_add_u32 s95, s64, 0x2000\n"
"v_mfma_f32_16x16x32_bf16 v[92:95], a[32:35], v[176:179], v[92:95]\n"
"buffer_store_dwordx4 v[232:235], v7, s[4:7], s95 offen sc1\n"
"v_mfma_f32_16x16x32_bf16 v[40:43], a[64:67], v[176:179], 0\n"
"v_mfma_f32_16x16x32_bf16 v[40:43], a[68:71], v[64:67], v[40:43]\n"
"buffer_load_dwordx4 v[80:83], v0, s[4:7], s98 offen offset:3072 sc1\n"
"v_mfma_f32_16x16x32_bf16 v[40:43], a[72:75], v[216:219], v[40:43]\n"
"v_mfma_f32_16x16x32_bf16 v[40:43], a[76:79], v[28:31], v[40:43]\n"
"v_mfma_f32_16x16x32_bf16 v[40:43], a[80:83], v[48:51], v[40:43]\n"
"buffer_load_dwordx4 a[140:143], v0, s[4:7], s96 offen offset:3072 sc1\n"
"v_mfma_f32_16x16x32_bf16 v[40:43], a[84:87], v[172:175], v[40:43]\n"
"ds_write_b128 v3, v[92:95] offset:7168\n"
"v_mfma_f32_16x16x32_bf16 v[40:43], a[88:91], v[228:231], v[40:43]\n"
"buffer_load_dwordx4 v[92:95], v1, s[4:7], s98 offen sc1\n"
"v_mfma_f32_16x16x32_bf16 v[40:43], a[92:95], v[72:75], v[40:43]\n"
"v_mfma_f32_16x16x32_bf16 v[168:171], a[124:127], v[72:75], 0\n"
"v_mfma_f32_16x16x32_bf16 v[168:171], a[120:123], v[228:231], v[168:171]\n"
"v_mfma_f32_16x16x32_bf16 v[168:171], a[116:119], v[172:175], v[168:171]\n"
"v_mfma_f32_16x16x32_bf16 v[168:171], a[112:115], v[48:51], v[168:171]\n"
"buffer_load_dwordx4 v[184:187], v1, s[4:7], s98 offen offset:1024 sc1\n"
"v_mfma_f32_16x16x32_bf16 v[168:171], a[108:111], v[28:31], v[168:171]\n"
"v_mfma_f32_16x16x32_bf16 v[168:171], a[104:107], v[216:219], v[168:171]\n"
"s_nop 0\n"
"ds_write_b128 v3, v[40:43] offset:8192\n"
"v_mfma_f32_16x16x32_bf16 v[168:171], a[100:103], v[64:67], v[168:171]\n"
"v_mfma_f32_16x16x32_bf16 v[168:171], a[96:99], v[176:179], v[168:171]\n"
"v_mfma_f32_16x16x32_bf16 v[40:43], v[100:103], v[176:179], 0\n"
"buffer_load_dwordx4 v[188:191], v1, s[4:7], s98 offen offset:2048 sc1\n"
"v_mfma_f32_16x16x32_bf16 v[40:43], v[104:107], v[64:67], v[40:43]\n"
"buffer_store_dwordx4 v[140:143], v39, s[4:7], s23 offen sc1\n"
"v_mfma_f32_16x16x32_bf16 v[40:43], v[108:111], v[216:219], v[40:43]\n"
"v_mfma_f32_16x16x32_bf16 v[40:43], v[116:119], v[28:31], v[40:43]\n"
"v_mfma_f32_16x16x32_bf16 v[40:43], v[128:131], v[48:51], v[40:43]\n"
"s_waitcnt vmcnt(14)\n"
"v_add_f32_e32 v255, v255, v254\n"
"v_add_f32_e32 v253, v253, v250\n"
"v_mfma_f32_16x16x32_bf16 v[40:43], v[132:135], v[172:175], v[40:43]\n"
"buffer_load_dwordx4 v[36:39], v1, s[4:7], s98 offen offset:3072 sc1\n"
"s_add_u32 s98, s64, 0x6000\n"
"v_mfma_f32_16x16x32_bf16 v[40:43], v[136:139], v[228:231], v[40:43]\n"
"ds_write_b128 v3, v[168:171] offset:9216\n"
"v_mfma_f32_16x16x32_bf16 v[40:43], v[96:99], v[72:75], v[40:43]\n"
"buffer_store_dwordx4 v[32:35], v7, s[4:7], s98 offen sc1\n"
"s_nop 1\n"
"v_mfma_f32_16x16x32_bf16 v[32:35], v[112:115], v[72:75], 0\n"
"buffer_load_dwordx4 a[144:147], v1, s[4:7], s96 offen sc1\n"
"v_mfma_f32_16x16x32_bf16 v[32:35], v[164:167], v[228:231], v[32:35]\n"
"v_add_f32_e32 v252, v252, v249\n"
"v_add_f32_e32 v251, v251, v248\n"
"v_mfma_f32_16x16x32_bf16 v[32:35], v[160:163], v[172:175], v[32:35]\n"
"v_add_f32_e32 v255, v255, v252\n"
"v_add_f32_e32 v253, v253, v251\n"
"v_mfma_f32_16x16x32_bf16 v[32:35], v[156:159], v[48:51], v[32:35]\n"
"v_add_f32_e32 v255, v255, v253\n"
"v_mov_b32_e32 v253, v255\n"
"v_mfma_f32_16x16x32_bf16 v[32:35], v[124:127], v[28:31], v[32:35]\n"
"s_nop 0\n"
"v_permlane32_swap_b32_e32 v255, v253\n"
"v_add_f32_e32 v255, v255, v253\n"
"v_mfma_f32_16x16x32_bf16 v[32:35], v[148:151], v[216:219], v[32:35]\n"
"ds_write_b128 v3, v[40:43] offset:10240\n"
"v_mfma_f32_16x16x32_bf16 v[32:35], v[144:147], v[64:67], v[32:35]\n"
"v_mul_f32_e32 v255, 0x3a800000, v255\n"
"v_mov_b32_e32 v253, v255\n"
"v_mfma_f32_16x16x32_bf16 v[32:35], v[120:123], v[176:179], v[32:35]\n"
"s_nop 0\n"
"v_permlane16_swap_b32_e32 v255, v253\n"
"v_cvt_pk_bf16_f32 v254, -v255, 0\n"
"; @A G1 1.2\n"
"v_mfma_f32_16x16x32_bf16 v[28:31], a[0:3], v[52:55], 0\n"
"v_fma_f32 v253, -v255, v255, v253\n"
"v_lshlrev_b32_e32 v254, 16, v254\n"
"v_mfma_f32_16x16x32_bf16 v[28:31], a[4:7], v[56:59], v[28:31]\n"
"s_waitcnt vmcnt(10)\n"
"v_cmp_ne_u64_e32 vcc, v[246:247], v[18:19]\n"
"v_mfma_f32_16x16x32_bf16 v[28:31], a[8:11], v[88:91], v[28:31]\n"
"s_cbranch_vccnz .Lmk_f3s\n"
".Lmk_f3r:\n"
"v_max_f32_e32 v253, 0, v253\n"
"v_mfma_f32_16x16x32_bf16 v[28:31], a[12:15], v[180:183], v[28:31]\n"
"v_cvt_pk_bf16_f32 v253, v253, 0\n"
"v_lshlrev_b32_e32 v253, 16, v253\n"
"v_mfma_f32_16x16x32_bf16 v[28:31], a[16:19], v[60:63], v[28:31]\n"
"v_add_f32_e32 v253, 0x3727c5ac, v253\n"
"v_cvt_pk_bf16_f32 v253, v253, 0\n"
"v_mfma_f32_16x16x32_bf16 v[28:31], a[20:23], v[24:27], v[28:31]\n"
"ds_write_b128 v3, v[32:35] offset:11264\n"
"v_mfma_f32_16x16x32_bf16 v[28:31], a[24:27], v[152:155], v[28:31]\n"
"v_lshlrev_b32_e32 v253, 16, v253\n"
"v_mfma_f32_16x16x32_bf16 v[28:31], a[28:31], v[192:195], v[28:31]\n"
"v_sqrt_f32_e32 v253, v253\n"
"v_mfma_f32_16x16x32_bf16 v[32:35], a[60:63], v[192:195], 0\n"
"v_cvt_pk_bf16_f32 v253, v253, 0\n"
"v_lshlrev_b32_e32 v253, 16, v253\n"
"v_mfma_f32_16x16x32_bf16 v[32:35], a[56:59], v[152:155], v[32:35]\n"
"v_rcp_f32_e32 v255, v253\n"
"v_mfma_f32_16x16x32_bf16 v[32:35], a[52:55], v[24:27], v[32:35]\n"
"s_add_u32 s98, s52, 0x8000\n"
"buffer_load_dwordx4 v[40:43], v0, s[4:7], s98 offen sc1\n"
"v_mfma_f32_16x16x32_bf16 v[32:35], a[48:51], v[60:63], v[32:35]\n"
"s_waitcnt lgkmcnt(0)\n"
"s_barrier\n"
"v_mfma_f32_16x16x32_bf16 v[32:35], a[44:47], v[180:183], v[32:35]\n"
"buffer_load_dwordx4 a[148:151], v1, s[4:7], s96 offen offset:1024 sc1\n"
"v_mfma_f32_16x16x32_bf16 v[32:35], a[40:43], v[88:91], v[32:35]\n"
"ds_write_b128 v3, v[28:31] offset:12288\n"
"v_mfma_f32_16x16x32_bf16 v[32:35], a[36:39], v[56:59], v[32:35]\n"
"ds_read_b128 v[28:31], v4\n"
"v_mul_f32_e32 v253, 0x3e8293ee, v255\n"
"v_mfma_f32_16x16x32_bf16 v[32:35], a[32:35], v[52:55], v[32:35]\n"
"buffer_load_dwordx4 v[48:51], v0, s[4:7], s98 offen offset:1024 sc1\n"
"v_mfma_f32_16x16x32_bf16 v[64:67], a[64:67], v[52:55], 0\n"
"ds_read_b128 v[72:75], v9 offset:128\n"
"ds_read_b128 v[140:143], v5\n"
"v_mfma_f32_16x16x32_bf16 v[64:67], a[68:71], v[56:59], v[64:67]\n"
"v_mfma_f32_16x16x32_bf16 v[64:67], a[72:75], v[88:91], v[64:67]\n"
"s_add_u32 s97, s60, 0x48000\n"
"buffer_store_dwordx4 v[44:47], v0, s[4:7], s97 offen sc1\n"
"v_mfma_f32_16x16x32_bf16 v[64:67], a[76:79], v[180:183], v[64:67]\n"
"ds_read_b128 v[168:171], v4 offset:1024\n"
"ds_read_b128 v[172:175], v9 offset:192\n"
"v_mfma_f32_16x16x32_bf16 v[64:67], a[80:83], v[60:63], v[64:67]\n"
"buffer_load_dwordx4 v[176:179], v0, s[4:7], s98 offen offset:2048 sc1\n"
"v_mfma_f32_16x16x32_bf16 v[64:67], a[84:87], v[24:27], v[64:67]\n"
"ds_write_b128 v3, v[32:35] offset:13312\n"
"v_mfma_f32_16x16x32_bf16 v[64:67], a[88:91], v[152:155], v[64:67]\n"
"ds_read_b128 v[32:35], v5 offset:1024\n"
"s_waitcnt lgkmcnt(4)\n"
"v_mfma_f32_16x16x32_bf16 v[64:67], a[92:95], v[192:195], v[64:67]\n"
"v_fmac_f32_e32 v28, v254, v72\n"
"v_fmac_f32_e32 v29, v254, v73\n"
"v_mfma_f32_16x16x32_bf16 v[196:199], a[124:127], v[192:195], 0\n"
"v_fmac_f32_e32 v30, v254, v74\n"
"v_fmac_f32_e32 v31, v254, v75\n"
"v_mfma_f32_16x16x32_bf16 v[196:199], a[120:123], v[152:155], v[196:199]\n"
"buffer_load_dwordx4 v[72:75], v0, s[4:7], s98 offen offset:3072 sc1\n"
"v_mfma_f32_16x16x32_bf16 v[196:199], a[116:119], v[24:27], v[196:199]\n"
"ds_read_b128 v[200:203], v4 offset:2048\n"
"ds_read_b128 v[204:207], v9 offset:256\n"
"v_mfma_f32_16x16x32_bf16 v[196:199], a[112:115], v[60:63], v[196:199]\n"
"ds_read_b128 v[208:211], v5 offset:2048\n"
"v_add_f32_e32 v28, v28, v140\n"
"v_mfma_f32_16x16x32_bf16 v[196:199], a[108:111], v[180:183], v[196:199]\n"
"v_add_f32_e32 v29, v29, v141\n"
"v_add_f32_e32 v30, v30, v142\n"
"v_mfma_f32_16x16x32_bf16 v[196:199], a[104:107], v[88:91], v[196:199]\n"
"ds_write_b128 v3, v[64:67] offset:14336\n"
"v_mfma_f32_16x16x32_bf16 v[196:199], a[100:103], v[56:59], v[196:199]\n"
"buffer_load_dwordx4 v[64:67], v1, s[4:7], s98 offen sc1\n"
"v_mfma_f32_16x16x32_bf16 v[196:199], a[96:99], v[52:55], v[196:199]\n"
"s_waitcnt lgkmcnt(4)\n"
"v_add_f32_e32 v31, v31, v143\n"
"v_fmac_f32_e32 v168, v254, v172\n"
"v_mfma_f32_16x16x32_bf16 v[140:143], v[100:103], v[52:55], 0\n"
"buffer_load_dwordx4 a[152:155], v1, s[4:7], s96 offen offset:2048 sc1\n"
"v_mfma_f32_16x16x32_bf16 v[140:143], v[104:107], v[56:59], v[140:143]\n"
"ds_read_b128 v[212:215], v4 offset:3072\n"
"ds_read_b128 v[216:219], v9 offset:320\n"
"v_mfma_f32_16x16x32_bf16 v[140:143], v[108:111], v[88:91], v[140:143]\n"
"ds_read_b128 v[220:223], v5 offset:3072\n"
"v_fmac_f32_e32 v169, v254, v173\n"
"v_mfma_f32_16x16x32_bf16 v[140:143], v[116:119], v[180:183], v[140:143]\n"
"buffer_load_dwordx4 v[224:227], v1, s[4:7], s98 offen offset:1024 sc1\n"
"v_mfma_f32_16x16x32_bf16 v[140:143], v[128:131], v[60:63], v[140:143]\n"
"v_fmac_f32_e32 v170, v254, v174\n"
"v_fmac_f32_e32 v171, v254, v175\n"
"v_mfma_f32_16x16x32_bf16 v[140:143], v[132:135], v[24:27], v[140:143]\n"
"ds_write_b128 v3, v[196:199] offset:15360\n"
"v_mfma_f32_16x16x32_bf16 v[140:143], v[136:139], v[152:155], v[140:143]\n"
"v_add_f32_e32 v168, v168, v32\n"
"v_add_f32_e32 v169, v169, v33\n"
"v_mfma_f32_16x16x32_bf16 v[140:143], v[96:99], v[192:195], v[140:143]\n"
"s_waitcnt lgkmcnt(5)\n"
"v_add_f32_e32 v170, v170, v34\n"
"v_add_f32_e32 v171, v171, v35\n"
"v_mfma_f32_16x16x32_bf16 v[32:35], v[112:115], v[192:195], 0\n"
"buffer_load_dwordx4 v[172:175], v1, s[4:7], s98 offen offset:2048 sc1\n"
"v_mfma_f32_16x16x32_bf16 v[32:35], v[164:167], v[152:155], v[32:35]\n"
"ds_read_b128 v[152:155], v4 offset:4096\n"
"ds_read_b128 v[192:195], v9\n"
"v_mfma_f32_16x16x32_bf16 v[32:35], v[160:163], v[24:27], v[32:35]\n"
"ds_read_b128 v[24:27], v5 offset:4096\n"
"v_fmac_f32_e32 v200, v254, v204\n"
"v_mfma_f32_16x16x32_bf16 v[32:35], v[156:159], v[60:63], v[32:35]\n"
"v_fmac_f32_e32 v201, v254, v205\n"
"v_fmac_f32_e32 v202, v254, v206\n"
"v_mfma_f32_16x16x32_bf16 v[32:35], v[124:127], v[180:183], v[32:35]\n"
"v_fmac_f32_e32 v203, v254, v207\n"
"v_add_f32_e32 v200, v200, v208\n"
"v_mfma_f32_16x16x32_bf16 v[32:35], v[148:151], v[88:91], v[32:35]\n"
"buffer_load_dwordx4 v[60:63], v1, s[4:7], s98 offen offset:3072 sc1\n"
"v_mfma_f32_16x16x32_bf16 v[32:35], v[144:147], v[56:59], v[32:35]\n"
"ds_write_b128 v3, v[140:143] offset:16384\n"
"v_mfma_f32_16x16x32_bf16 v[32:35], v[120:123], v[52:55], v[32:35]\n"
"s_waitcnt vmcnt(19)\n"
"ds_read_b128 v[52:55], v4 offset:5120\n"
"ds_read_b128 v[56:59], v9 offset:64\n"
"; @A G1 1.3\n"
"v_mfma_f32_16x16x32_bf16 v[28:31], a[0:3], v[68:71], v[28:31]\n"
"ds_read_b128 v[88:91], v5 offset:5120\n"
"v_add_f32_e32 v201, v201, v209\n"
"v_mfma_f32_16x16x32_bf16 v[28:31], a[4:7], v[84:87], v[28:31]\n"
"s_waitcnt lgkmcnt(8)\n"
"v_add_f32_e32 v202, v202, v210\n"
"v_add_f32_e32 v203, v203, v211\n"
"v_mfma_f32_16x16x32_bf16 v[28:31], a[8:11], v[76:79], v[28:31]\n"
"s_add_u32 s98, s53, 0x8000\n"
"buffer_load_dwordx4 v[140:143], v0, s[4:7], s98 offen sc1\n"
"v_mfma_f32_16x16x32_bf16 v[28:31], a[12:15], v[80:83], v[28:31]\n"
"s_waitcnt vmcnt(14)\n"
"s_add_u32 s97, s60, 0x40000\n"
"buffer_store_dwordx4 v[44:47], v0, s[4:7], s97 offen sc1\n"
"v_fmac_f32_e32 v212, v254, v216\n"
"v_fmac_f32_e32 v213, v254, v217\n"
"v_mfma_f32_16x16x32_bf16 v[28:31], a[16:19], v[92:95], v[28:31]\n"
"buffer_load_dwordx4 a[156:159], v1, s[4:7], s96 offen offset:3072 sc1\n"
"v_mfma_f32_16x16x32_bf16 v[28:31], a[20:23], v[184:187], v[28:31]\n"
"ds_write_b128 v3, v[32:35] offset:17408\n"
"v_mfma_f32_16x16x32_bf16 v[28:31], a[24:27], v[188:191], v[28:31]\n"
"v_fmac_f32_e32 v214, v254, v218\n"
"v_fmac_f32_e32 v215, v254, v219\n"
"v_mfma_f32_16x16x32_bf16 v[28:31], a[28:31], v[36:39], v[28:31]\n"
"buffer_load_dwordx4 v[32:35], v0, s[4:7], s98 offen offset:1024 sc1\n"
"v_mfma_f32_16x16x32_bf16 v[168:171], a[60:63], v[36:39], v[168:171]\n"
"v_add_f32_e32 v212, v212, v220\n"
"v_add_f32_e32 v213, v213, v221\n"
"v_mfma_f32_16x16x32_bf16 v[168:171], a[56:59], v[188:191], v[168:171]\n"
"s_waitcnt lgkmcnt(5)\n"
"v_add_f32_e32 v214, v214, v222\n"
"v_add_f32_e32 v215, v215, v223\n"
"v_mfma_f32_16x16x32_bf16 v[168:171], a[52:55], v[184:187], v[168:171]\n"
"s_waitcnt lgkmcnt(0)\n"
"s_barrier\n"
"v_mfma_f32_16x16x32_bf16 v[168:171], a[48:51], v[92:95], v[168:171]\n"
"ds_read_b128 v[180:183], v6\n"
"ds_read_b128 v[196:199], v6 offset:1024\n"
"v_mfma_f32_16x16x32_bf16 v[168:171], a[44:47], v[80:83], v[168:171]\n"
"buffer_load_dwordx4 v[204:207], v0, s[4:7], s98 offen offset:2048 sc1\n"
"v_mfma_f32_16x16x32_bf16 v[168:171], a[40:43], v[76:79], v[168:171]\n"
"v_fmac_f32_e32 v152, v254, v192\n"
"v_fmac_f32_e32 v153, v254, v193\n"
"v_mfma_f32_16x16x32_bf16 v[168:171], a[36:39], v[84:87], v[168:171]\n"
"v_fmac_f32_e32 v154, v254, v194\n"
"v_fmac_f32_e32 v155, v254, v195\n"
"v_mfma_f32_16x16x32_bf16 v[168:171], a[32:35], v[68:71], v[168:171]\n"
"v_add_f32_e32 v152, v152, v24\n"
"v_add_f32_e32 v153, v153, v25\n"
"v_mfma_f32_16x16x32_bf16 v[200:203], a[64:67], v[68:71], v[200:203]\n"
"v_add_f32_e32 v154, v154, v26\n"
"v_add_f32_e32 v155, v155, v27\n"
"v_mfma_f32_16x16x32_bf16 v[200:203], a[68:71], v[84:87], v[200:203]\n"
"buffer_load_dwordx4 v[24:27], v0, s[4:7], s98 offen offset:3072 sc1\n"
"v_mfma_f32_16x16x32_bf16 v[200:203], a[72:75], v[76:79], v[200:203]\n"
"v_fmac_f32_e32 v52, v254, v56\n"
"v_fmac_f32_e32 v53, v254, v57\n"
"v_mfma_f32_16x16x32_bf16 v[200:203], a[76:79], v[80:83], v[200:203]\n"
"v_fmac_f32_e32 v54, v254, v58\n"
"v_fmac_f32_e32 v55, v254, v59\n"
"v_mfma_f32_16x16x32_bf16 v[200:203], a[80:83], v[92:95], v[200:203]\n"
"s_waitcnt lgkmcnt(0)\n"
"v_add_f32_e32 v52, v52, v88\n"
"v_add_f32_e32 v53, v53, v89\n"
"v_mfma_f32_16x16x32_bf16 v[200:203], a[84:87], v[184:187], v[200:203]\n"
"v_add_f32_e32 v54, v54, v90\n"
"v_add_f32_e32 v55, v55, v91\n"
"v_mfma_f32_16x16x32_bf16 v[200:203], a[88:91], v[188:191], v[200:203]\n"
"buffer_load_dwordx4 v[56:59], v1, s[4:7], s98 offen sc1\n"
"v_mfma_f32_16x16x32_bf16 v[200:203], a[92:95], v[36:39], v[200:203]\n"
"s_add_u32 s97, s70, 0x2000\n"
"ds_read_b128 v[88:91], v6 offset:2048\n"
"ds_read_b128 v[192:195], v6 offset:3072\n"
"v_mfma_f32_16x16x32_bf16 v[212:215], a[124:127], v[36:39], v[212:215]\n"
"buffer_load_dwordx4 a[160:163], v0, s[4:7], s97 offen sc1\n"
"v_mfma_f32_16x16x32_bf16 v[212:215], a[120:123], v[188:191], v[212:215]\n"
"v_add_f32_e32 v28, v28, v180\n"
"v_add_f32_e32 v29, v29, v181\n"
"v_mfma_f32_16x16x32_bf16 v[212:215], a[116:119], v[184:187], v[212:215]\n"
"v_add_f32_e32 v30, v30, v182\n"
"v_add_f32_e32 v31, v31, v183\n"
"v_mfma_f32_16x16x32_bf16 v[212:215], a[112:115], v[92:95], v[212:215]\n"
"buffer_load_dwordx4 v[180:183], v1, s[4:7], s98 offen offset:1024 sc1\n"
"v_mfma_f32_16x16x32_bf16 v[212:215], a[108:111], v[80:83], v[212:215]\n"
"v_add_f32_e32 v168, v168, v196\n"
"v_add_f32_e32 v169, v169, v197\n"
"v_mfma_f32_16x16x32_bf16 v[212:215], a[104:107], v[76:79], v[212:215]\n"
"v_add_f32_e32 v170, v170, v198\n"
"v_add_f32_e32 v171, v171, v199\n"
"v_mfma_f32_16x16x32_bf16 v[212:215], a[100:103], v[84:87], v[212:215]\n"
"v_mul_f32_e32 v28, v28, v255\n"
"v_mul_f32_e32 v29, v29, v255\n"
"v_mfma_f32_16x16x32_bf16 v[212:215], a[96:99], v[68:71], v[212:215]\n"
"v_mul_f32_e32 v30, v30, v255\n"
"v_mul_f32_e32 v31, v31, v255\n"
"v_mfma_f32_16x16x32_bf16 v[152:155], v[100:103], v[68:71], v[152:155]\n"
"buffer_load_dwordx4 v[196:199], v1, s[4:7], s98 offen offset:2048 sc1\n"
"v_mfma_f32_16x16x32_bf16 v[152:155], v[104:107], v[84:87], v[152:155]\n"
"v_mul_f32_e32 v168, v168, v255\n"
"v_mul_f32_e32 v169, v169, v255\n"
"v_mfma_f32_16x16x32_bf16 v[152:155], v[108:111], v[76:79], v[152:155]\n"
"v_mul_f32_e32 v170, v170, v255\n"
"v_mul_f32_e32 v171, v171, v255\n"
"v_mfma_f32_16x16x32_bf16 v[152:155], v[116:119], v[80:83], v[152:155]\n"
"v_cvt_pk_bf16_f32 v208, v28, v29\n"
"v_cvt_pk_bf16_f32 v209, v30, v31\n"
"v_mfma_f32_16x16x32_bf16 v[152:155], v[128:131], v[92:95], v[152:155]\n"
"s_waitcnt lgkmcnt(0)\n"
"buffer_store_dwordx4 v[44:47], v0, s[4:7], s60 offen sc1\n"
"v_cvt_pk_bf16_f32 v210, v168, v169\n"
"v_cvt_pk_bf16_f32 v211, v170, v171\n"
"v_mfma_f32_16x16x32_bf16 v[152:155], v[132:135], v[184:187], v[152:155]\n"
"buffer_load_dwordx4 v[28:31], v1, s[4:7], s98 offen offset:3072 sc1\n"
"ds_write_b128 v8, v[208:211] offset:25600\n"
"v_mfma_f32_16x16x32_bf16 v[152:155], v[136:139], v[188:191], v[152:155]\n"
"ds_read_b128 v[44:47], v6 offset:4096\n"
"ds_read_b128 v[168:171], v6 offset:5120\n"
"v_mfma_f32_16x16x32_bf16 v[152:155], v[96:99], v[36:39], v[152:155]\n"
"v_add_f32_e32 v200, v200, v88\n"
"v_add_f32_e32 v201, v201, v89\n"
"v_mfma_f32_16x16x32_bf16 v[52:55], v[112:115], v[36:39], v[52:55]\n"
"v_add_f32_e32 v202, v202, v90\n"
"v_add_f32_e32 v203, v203, v91\n"
"v_mfma_f32_16x16x32_bf16 v[52:55], v[164:167], v[188:191], v[52:55]\n"
"v_add_f32_e32 v212, v212, v192\n"
"v_add_f32_e32 v213, v213, v193\n"
"v_mfma_f32_16x16x32_bf16 v[52:55], v[160:163], v[184:187], v[52:55]\n"
"v_add_f32_e32 v214, v214, v194\n"
"v_add_f32_e32 v215, v215, v195\n"
"v_mfma_f32_16x16x32_bf16 v[52:55], v[156:159], v[92:95], v[52:55]\n"
"v_mul_f32_e32 v200, v200, v255\n"
"v_mul_f32_e32 v201, v201, v255\n"
"v_mfma_f32_16x16x32_bf16 v[52:55], v[124:127], v[80:83], v[52:55]\n"
"v_mul_f32_e32 v202, v202, v255\n"
"v_mul_f32_e32 v203, v203, v255\n"
"v_mfma_f32_16x16x32_bf16 v[52:55], v[148:151], v[76:79], v[52:55]\n"
"v_mul_f32_e32 v212, v212, v255\n"
"v_mul_f32_e32 v213, v213, v255\n"
"v_mfma_f32_16x16x32_bf16 v[52:55], v[144:147], v[84:87], v[52:55]\n"
"v_mul_f32_e32 v214, v214, v255\n"
"v_mul_f32_e32 v215, v215, v255\n"
"v_mfma_f32_16x16x32_bf16 v[52:55], v[120:123], v[68:71], v[52:55]\n"
"v_cvt_pk_bf16_f32 v36, v200, v201\n"
"v_cvt_pk_bf16_f32 v37, v202, v203\n"
"v_cvt_pk_bf16_f32 v38, v212, v213\n"
"v_cvt_pk_bf16_f32 v39, v214, v215\n"
"ds_write_b128 v7, v[36:39] offset:58368\n"
".Lmk_z2:\n"
"s_waitcnt vmcnt(17)\n"
"; @A G1 2.0\n"
"v_mfma_f32_16x16x32_bf16 v[68:71], a[0:3], v[40:43], 0\n"
"v_mfma_f32_16x16x32_bf16 v[68:71], a[4:7], v[48:51], v[68:71]\n"
"s_add_u32 s98, s62, 0x800\n"
"v_mfma_f32_16x16x32_bf16 v[68:71], a[8:11], v[176:179], v[68:71]\n"
"s_add_u32 s96, s54, 0x8000\n"
"buffer_load_dwordx4 v[76:79], v0, s[4:7], s96 offen sc1\n"
"v_mfma_f32_16x16x32_bf16 v[68:71], a[12:15], v[72:75], v[68:71]\n"
"s_waitcnt vmcnt(13)\n"
"v_mfma_f32_16x16x32_bf16 v[68:71], a[16:19], v[64:67], v[68:71]\n"
"buffer_load_dword v255, v10, s[4:7], s98 offen sc1\n"
"v_mfma_f32_16x16x32_bf16 v[68:71], a[20:23], v[224:227], v[68:71]\n"
"s_waitcnt lgkmcnt(1)\n"
"v_add_f32_e32 v152, v152, v44\n"
"v_add_f32_e32 v153, v153, v45\n"
"v_mfma_f32_16x16x32_bf16 v[68:71], a[24:27], v[172:175], v[68:71]\n"
"v_add_f32_e32 v154, v154, v46\n"
"v_add_f32_e32 v155, v155, v47\n"
"v_mfma_f32_16x16x32_bf16 v[68:71], a[28:31], v[60:63], v[68:71]\n"
"buffer_load_dwordx4 v[44:47], v0, s[4:7], s96 offen offset:1024 sc1\n"
"v_mfma_f32_16x16x32_bf16 v[80:83], a[60:63], v[60:63], 0\n"
"buffer_load_dwordx4 a[164:167], v0, s[4:7], s97 offen offset:1024 sc1\n"
"v_mfma_f32_16x16x32_bf16 v[80:83], a[56:59], v[172:175], v[80:83]\n"
"buffer_load_dword v254, v10, s[4:7], s98 offen offset:256 sc1\n"
"v_mfma_f32_16x16x32_bf16 v[80:83], a[52:55], v[224:227], v[80:83]\n"
"v_add_f32_e32 v52, v52, v168\n"
"v_add_f32_e32 v53, v53, v169\n"
"v_mfma_f32_16x16x32_bf16 v[80:83], a[48:51], v[64:67], v[80:83]\n"
"v_add_f32_e32 v54, v54, v170\n"
"v_add_f32_e32 v55, v55, v171\n"
"v_mfma_f32_16x16x32_bf16 v[80:83], a[44:47], v[72:75], v[80:83]\n"
"buffer_load_dwordx4 v[84:87], v0, s[4:7], s96 offen offset:2048 sc1\n"
"v_mfma_f32_16x16x32_bf16 v[80:83], a[40:43], v[176:179], v[80:83]\n"
"ds_write_b128 v3, v[68:71]\n"
"v_mfma_f32_16x16x32_bf16 v[80:83], a[36:39], v[48:51], v[80:83]\n"
"buffer_load_dword v252, v10, s[4:7], s98 offen offset:512 sc1\n"
"v_mfma_f32_16x16x32_bf16 v[80:83], a[32:35], v[40:43], v[80:83]\n"
"v_mul_f32_e32 v152, v152, v253\n"
"v_mul_f32_e32 v153, v153, v253\n"
"v_mfma_f32_16x16x32_bf16 v[68:71], a[64:67], v[40:43], 0\n"
"v_mul_f32_e32 v154, v154, v253\n"
"v_mul_f32_e32 v155, v155, v253\n"
"v_mfma_f32_16x16x32_bf16 v[68:71], a[68:71], v[48:51], v[68:71]\n"
"buffer_load_dwordx4 v[88:91], v0, s[4:7], s96 offen offset:3072 sc1\n"
"v_mfma_f32_16x16x32_bf16 v[68:71], a[72:75], v[176:179], v[68:71]\n"
"buffer_load_dword v251, v10, s[4:7], s98 offen offset:768 sc1\n"
"v_mfma_f32_16x16x32_bf16 v[68:71], a[76:79], v[72:75], v[68:71]\n"
"v_mul_f32_e32 v52, v52, v253\n"
"v_mul_f32_e32 v53, v53, v253\n"
"v_mfma_f32_16x16x32_bf16 v[68:71], a[80:83], v[64:67], v[68:71]\n"
"v_mul_f32_e32 v54, v54, v253\n"
"v_mul_f32_e32 v55, v55, v253\n"
"v_mfma_f32_16x16x32_bf16 v[68:71], a[84:87], v[224:227], v[68:71]\n"
"ds_write_b128 v3, v[80:83] offset:1024\n"
"v_mfma_f32_16x16x32_bf16 v[68:71], a[88:91], v[172:175], v[68:71]\n"
"buffer_load_dwordx4 v[80:83], v1, s[4:7], s96 offen sc1\n"
"v_mfma_f32_16x16x32_bf16 v[68:71], a[92:95], v[60:63], v[68:71]\n"
"buffer_load_dword v253, v10, s[4:7], s98 offen offset:1024 sc1\n"
"v_mfma_f32_16x16x32_bf16 v[92:95], a[124:127], v[60:63], 0\n"
"v_cvt_pk_bf16_f32 v168, v152, v153\n"
"v_cvt_pk_bf16_f32 v169, v154, v155\n"
"v_mfma_f32_16x16x32_bf16 v[92:95], a[120:123], v[172:175], v[92:95]\n"
"v_cvt_pk_bf16_f32 v170, v52, v53\n"
"v_cvt_pk_bf16_f32 v171, v54, v55\n"
"v_mfma_f32_16x16x32_bf16 v[92:95], a[116:119], v[224:227], v[92:95]\n"
"buffer_load_dword v250, v10, s[4:7], s98 offen offset:1280 sc1\n"
"v_mfma_f32_16x16x32_bf16 v[92:95], a[112:115], v[64:67], v[92:95]\n"
"buffer_load_dwordx4 v[52:55], v1, s[4:7], s96 offen offset:1024 sc1\n"
"v_mfma_f32_16x16x32_bf16 v[92:95], a[108:111], v[72:75], v[92:95]\n"
"buffer_load_dwordx4 a[168:171], v0, s[4:7], s97 offen offset:2048 sc1\n"
"v_mfma_f32_16x16x32_bf16 v[92:95], a[104:107], v[176:179], v[92:95]\n"
"ds_write_b128 v3, v[68:71] offset:2048\n"
"v_mfma_f32_16x16x32_bf16 v[92:95], a[100:103], v[48:51], v[92:95]\n"
"buffer_load_dword v249, v10, s[4:7], s98 offen offset:1536 sc1\n"
"v_mfma_f32_16x16x32_bf16 v[92:95], a[96:99], v[40:43], v[92:95]\n"
"ds_write_b128 v2, v[168:171] offset:50176\n"
"v_mfma_f32_16x16x32_bf16 v[68:71], v[100:103], v[40:43], 0\n"
"buffer_load_dwordx4 v[152:155], v1, s[4:7], s96 offen offset:2048 sc1\n"
"s_add_u32 s95, s64, 0x2000\n"
"v_mfma_f32_16x16x32_bf16 v[68:71], v[104:107], v[48:51], v[68:71]\n"
"buffer_load_dword v248, v10, s[4:7], s98 offen offset:1792 sc1\n"
"v_mfma_f32_16x16x32_bf16 v[68:71], v[108:111], v[176:179], v[68:71]\n"
"buffer_store_dwordx4 v[208:211], v8, s[4:7], s95 offen offset:1024 sc1\n"
"v_mfma_f32_16x16x32_bf16 v[68:71], v[116:119], v[72:75], v[68:71]\n"
"v_mfma_f32_16x16x32_bf16 v[68:71], v[128:131], v[64:67], v[68:71]\n"
"v_mfma_f32_16x16x32_bf16 v[68:71], v[132:135], v[224:227], v[68:71]\n"
"buffer_load_dwordx4 v[168:171], v1, s[4:7], s96 offen offset:3072 sc1\n"
"v_mfma_f32_16x16x32_bf16 v[68:71], v[136:139], v[172:175], v[68:71]\n"
"ds_write_b128 v3, v[92:95] offset:3072\n"
"v_mfma_f32_16x16x32_bf16 v[68:71], v[96:99], v[60:63], v[68:71]\n"
"v_mfma_f32_16x16x32_bf16 v[92:95], v[112:115], v[60:63], 0\n"
"v_mfma_f32_16x16x32_bf16 v[92:95], v[164:167], v[172:175], v[92:95]\n"
"v_mfma_f32_16x16x32_bf16 v[92:95], v[160:163], v[224:227], v[92:95]\n"
"v_mfma_f32_16x16x32_bf16 v[92:95], v[156:159], v[64:67], v[92:95]\n"
"v_mfma_f32_16x16x32_bf16 v[92:95], v[124:127], v[72:75], v[92:95]\n"
"v_mfma_f32_16x16x32_bf16 v[92:95], v[148:151], v[176:179], v[92:95]\n"
"s_nop 1\n"
"ds_write_b128 v3, v[68:71] offset:4096\n"
"v_mfma_f32_16x16x32_bf16 v[92:95], v[144:147], v[48:51], v[92:95]\n"
"v_mfma_f32_16x16x32_bf16 v[92:95], v[120:123], v[40:43], v[92:95]\n"
"s_waitcnt vmcnt(25)\n"
"; @A G1 2.1\n"
"v_mfma_f32_16x16x32_bf16 v[40:43], a[0:3], v[140:143], 0\n"
"buffer_load_dwordx4 a[172:175], v0, s[4:7], s97 offen offset:3072 sc1\n"
"v_mfma_f32_16x16x32_bf16 v[40:43], a[4:7], v[32:35], v[40:43]\n"
"v_mov_b32_e32 v48, -1\n"
"v_mov_b32_e32 v49, -1\n"
"v_mfma_f32_16x16x32_bf16 v[40:43], a[8:11], v[204:207], v[40:43]\n"
"s_add_u32 s98, s55, 0x8000\n"
"buffer_load_dwordx4 v[60:63], v0, s[4:7], s98 offen sc1\n"
"v_mfma_f32_16x16x32_bf16 v[40:43], a[12:15], v[24:27], v[40:43]\n"
"s_waitcnt vmcnt(21)\n"
"v_mov_b32_e32 v50, -1\n"
"v_mov_b32_e32 v51, -1\n"
"v_mfma_f32_16x16x32_bf16 v[40:43], a[16:19], v[56:59], v[40:43]\n"
"s_add_u32 s96, s60, 0x8000\n"
"buffer_store_dwordx4 v[48:51], v0, s[4:7], s96 offen sc1\n"
"v_mfma_f32_16x16x32_bf16 v[40:43], a[20:23], v[180:183], v[40:43]\n"
"ds_write_b128 v3, v[92:95] offset:5120\n"
"v_mfma_f32_16x16x32_bf16 v[40:43], a[24:27], v[196:199], v[40:43]\n"
"v_mfma_f32_16x16x32_bf16 v[40:43], a[28:31], v[28:31], v[40:43]\n"
"buffer_load_dwordx4 v[64:67], v0, s[4:7], s98 offen offset:1024 sc1\n"
"v_mfma_f32_16x16x32_bf16 v[68:71], a[60:63], v[28:31], 0\n"
"s_add_u32 s96, s64, 0x6000\n"
"v_mfma_f32_16x16x32_bf16 v[68:71], a[56:59], v[196:199], v[68:71]\n"
"v_mfma_f32_16x16x32_bf16 v[68:71], a[52:55], v[180:183], v[68:71]\n"
"buffer_store_dwordx4 v[36:39], v7, s[4:7], s96 offen offset:1024 sc1\n"
"v_mfma_f32_16x16x32_bf16 v[68:71], a[48:51], v[56:59], v[68:71]\n"
"v_mfma_f32_16x16x32_bf16 v[68:71], a[44:47], v[24:27], v[68:71]\n"
"buffer_load_dwordx4 v[36:39], v0, s[4:7], s98 offen offset:2048 sc1\n"
"v_mfma_f32_16x16x32_bf16 v[68:71], a[40:43], v[204:207], v[68:71]\n"
"ds_write_b128 v3, v[40:43] offset:6144\n"
"v_mfma_f32_16x16x32_bf16 v[68:71], a[36:39], v[32:35], v[68:71]\n"
"v_mfma_f32_16x16x32_bf16 v[68:71], a[32:35], v[140:143], v[68:71]\n"
"v_mfma_f32_16x16x32_bf16 v[40:43], a[64:67], v[140:143], 0\n"
"v_mfma_f32_16x16x32_bf16 v[40:43], a[68:71], v[32:35], v[40:43]\n"
"buffer_load_dwordx4 v[72:75], v0, s[4:7], s98 offen offset:3072 sc1\n"
"v_mfma_f32_16x16x32_bf16 v[40:43], a[72:75], v[204:207], v[40:43]\n"
"v_mfma_f32_16x16x32_bf16 v[40:43], a[76:79], v[24:27], v[40:43]\n"
"v_mfma_f32_16x16x32_bf16 v[40:43], a[80:83], v[56:59], v[40:43]\n"
"buffer_load_dwordx4 a[176:179], v1, s[4:7], s97 offen sc1\n"
"v_mfma_f32_16x16x32_bf16 v[40:43], a[84:87], v[180:183], v[40:43]\n"
"ds_write_b128 v3, v[68:71] offset:7168\n"
"v_mfma_f32_16x16x32_bf16 v[40:43], a[88:91], v[196:199], v[40:43]\n"
"buffer_load_dwordx4 v[68:71], v1, s[4:7], s98 offen sc1\n"
"v_mfma_f32_16x16x32_bf16 v[40:43], a[92:95], v[28:31], v[40:43]\n"
"v_mfma_f32_16x16x32_bf16 v[92:95], a[124:127], v[28:31], 0\n"
"s_add_u32 s96, s61, 0x440000\n"
"buffer_store_dwordx4 v[48:51], v0, s[4:7], s96 offen sc1\n"
"v_mfma_f32_16x16x32_bf16 v[92:95], a[120:123], v[196:199], v[92:95]\n"
"v_mfma_f32_16x16x32_bf16 v[92:95], a[116:119], v[180:183], v[92:95]\n"
"v_mfma_f32_16x16x32_bf16 v[92:95], a[112:115], v[56:59], v[92:95]\n"
"buffer_load_dwordx4 v[172:175], v1, s[4:7], s98 offen offset:1024 sc1\n"
"v_mfma_f32_16x16x32_bf16 v[92:95], a[108:111], v[24:27], v[92:95]\n"
"v_mfma_f32_16x16x32_bf16 v[92:95], a[104:107], v[204:207], v[92:95]\n"
"ds_write_b128 v3, v[40:43] offset:8192\n"
"v_mfma_f32_16x16x32_bf16 v[92:95], a[100:103], v[32:35], v[92:95]\n"
"v_mfma_f32_16x16x32_bf16 v[92:95], a[96:99], v[140:143], v[92:95]\n"
"v_mfma_f32_16x16x32_bf16 v[40:43], v[100:103], v[140:143], 0\n"
"buffer_load_dwordx4 v[176:179], v1, s[4:7], s98 offen offset:2048 sc1\n"
"v_mfma_f32_16x16x32_bf16 v[40:43], v[104:107], v[32:35], v[40:43]\n"
"v_mfma_f32_16x16x32_bf16 v[40:43], v[108:111], v[204:207], v[40:43]\n"
"v_mfma_f32_16x16x32_bf16 v[40:43], v[116:119], v[24:27], v[40:43]\n"
"v_mfma_f32_16x16x32_bf16 v[40:43], v[128:131], v[56:59], v[40:43]\n"
"s_waitcnt vmcnt(14)\n"
"v_add_f32_e32 v255, v255, v253\n"
"v_add_f32_e32 v254, v254, v250\n"
"v_mfma_f32_16x16x32_bf16 v[40:43], v[132:135], v[180:183], v[40:43]\n"
"buffer_load_dwordx4 v[184:187], v1, s[4:7], s98 offen offset:3072 sc1\n"
"v_mfma_f32_16x16x32_bf16 v[40:43], v[136:139], v[196:199], v[40:43]\n"
"ds_write_b128 v3, v[92:95] offset:9216\n"
"v_mfma_f32_16x16x32_bf16 v[40:43], v[96:99], v[28:31], v[40:43]\n"
"v_add_f32_e32 v252, v252, v249\n"
"v_add_f32_e32 v251, v251, v248\n"
"v_mfma_f32_16x16x32_bf16 v[92:95], v[112:115], v[28:31], 0\n"
"buffer_load_dwordx4 a[180:183], v1, s[4:7], s97 offen offset:1024 sc1\n"
"v_mfma_f32_16x16x32_bf16 v[92:95], v[164:167], v[196:199], v[92:95]\n"
"v_add_f32_e32 v255, v255, v252\n"
"v_add_f32_e32 v254, v254, v251\n"
"v_mfma_f32_16x16x32_bf16 v[92:95], v[160:163], v[180:183], v[92:95]\n"
"v_add_f32_e32 v255, v255, v254\n"
"v_mov_b32_e32 v254, v255\n"
"v_mfma_f32_16x16x32_bf16 v[92:95], v[156:159], v[56:59], v[92:95]\n"
"s_nop 0\n"
"v_permlane32_swap_b32_e32 v255, v254\n"
"v_add_f32_e32 v255, v255, v254\n"
"v_mfma_f32_16x16x32_bf16 v[92:95], v[124:127], v[24:27], v[92:95]\n"
"v_mul_f32_e32 v255, 0x3a800000, v255\n"
"v_mov_b32_e32 v254, v255\n"
"v_mfma_f32_16x16x32_bf16 v[92:95], v[148:151], v[204:207], v[92:95]\n"
"ds_write_b128 v3, v[40:43] offset:10240\n"
"v_mfma_f32_16x16x32_bf16 v[92:95], v[144:147], v[32:35], v[92:95]\n"
"v_permlane16_swap_b32_e32 v255, v254\n"
"v_cvt_pk_bf16_f32 v253, -v255, 0\n"
"v_mfma_f32_16x16x32_bf16 v[92:95], v[120:123], v[140:143], v[92:95]\n"
"v_fma_f32 v254, -v255, v255, v254\n"
"v_lshlrev_b32_e32 v253, 16, v253\n"
"; @A G1 2.2\n"
"v_mfma_f32_16x16x32_bf16 v[24:27], a[0:3], v[76:79], 0\n"
"s_add_u32 s98, s61, 0x440000\n"
"buffer_store_dwordx4 v[48:51], v0, s[4:7], s98 offen offset:1024 sc1\n"
"v_max_f32_e32 v254, 0, v254\n"
"v_cvt_pk_bf16_f32 v254, v254, 0\n"
"v_mfma_f32_16x16x32_bf16 v[24:27], a[4:7], v[44:47], v[24:27]\n"
"v_lshlrev_b32_e32 v254, 16, v254\n"
"v_add_f32_e32 v254, 0x3727c5ac, v254\n"
"v_mfma_f32_16x16x32_bf16 v[24:27], a[8:11], v[84:87], v[24:27]\n"
"v_cvt_pk_bf16_f32 v254, v254, 0\n"
"v_lshlrev_b32_e32 v254, 16, v254\n"
"v_mfma_f32_16x16x32_bf16 v[24:27], a[12:15], v[88:91], v[24:27]\n"
"s_waitcnt vmcnt(15)\n"
"v_sqrt_f32_e32 v254, v254\n"
"v_mfma_f32_16x16x32_bf16 v[24:27], a[16:19], v[80:83], v[24:27]\n"
"s_mov_b32 s98, .Lmk_z4-.Lmk_g11\n"
"s_getpc_b64 s[84:85]\n"
".Lmk_g11:\n"
"s_add_u32 s84, s84, s98\n"
"s_addc_u32 s85, s85, 0\n"
"s_and_b32 s84, s84, -16\n"
"v_mfma_f32_16x16x32_bf16 v[24:27], a[20:23], v[52:55], v[24:27]\n"
"ds_write_b128 v3, v[92:95] offset:11264\n"
"v_mfma_f32_16x16x32_bf16 v[24:27], a[24:27], v[152:155], v[24:27]\n"
"s_and_b32 s85, s85, 0xffff\n"
"s_sub_u32 s98, .Lmk_end-.Lmk_g11, s98\n"
"s_min_u32 s86, s98, 0x4000\n"
"s_mov_b32 s87, 0x20000\n"
"v_mfma_f32_16x16x32_bf16 v[24:27], a[28:31], v[168:171], v[24:27]\n"
"s_mov_b32 m0, 0x27800\n"
"s_nop 0\n"
"buffer_load_dwordx4 v0, s[84:87], s99 offen lds\n"
"v_mfma_f32_16x16x32_bf16 v[28:31], a[60:63], v[168:171], 0\n"
"v_cvt_pk_bf16_f32 v254, v254, 0\n"
"v_lshlrev_b32_e32 v254, 16, v254\n"
"v_mfma_f32_16x16x32_bf16 v[28:31], a[56:59], v[152:155], v[28:31]\n"
"v_rcp_f32_e32 v255, v254\n"
"v_mfma_f32_16x16x32_bf16 v[28:31], a[52:55], v[52:55], v[28:31]\n"
"s_add_u32 s98, s52, 0\n"
"buffer_load_dwordx4 v[32:35], v0, s[4:7], s98 offen sc1\n"
"v_mfma_f32_16x16x32_bf16 v[28:31], a[48:51], v[80:83], v[28:31]\n"
"s_waitcnt lgkmcnt(0)\n"
"s_barrier\n"
"v_mfma_f32_16x16x32_bf16 v[28:31], a[44:47], v[88:91], v[28:31]\n"
"buffer_load_dwordx4 a[184:187], v1, s[4:7], s97 offen offset:2048 sc1\n"
"v_mfma_f32_16x16x32_bf16 v[28:31], a[40:43], v[84:87], v[28:31]\n"
"ds_write_b128 v3, v[24:27] offset:12288\n"
"v_mfma_f32_16x16x32_bf16 v[28:31], a[36:39], v[44:47], v[28:31]\n"
"ds_read_b128 v[24:27], v4\n"
"v_mul_f32_e32 v254, 0x3e8293ee, v255\n"
"v_mfma_f32_16x16x32_bf16 v[28:31], a[32:35], v[76:79], v[28:31]\n"
"buffer_load_dwordx4 v[40:43], v0, s[4:7], s98 offen offset:1024 sc1\n"
"v_mfma_f32_16x16x32_bf16 v[56:59], a[64:67], v[76:79], 0\n"
"ds_read_b128 v[92:95], v9 offset:128\n"
"ds_read_b128 v[140:143], v5\n"
"v_mfma_f32_16x16x32_bf16 v[56:59], a[68:71], v[44:47], v[56:59]\n"
"v_mfma_f32_16x16x32_bf16 v[56:59], a[72:75], v[84:87], v[56:59]\n"
"v_mfma_f32_16x16x32_bf16 v[56:59], a[76:79], v[88:91], v[56:59]\n"
"ds_read_b128 v[180:183], v4 offset:1024\n"
"ds_read_b128 v[188:191], v9 offset:192\n"
"v_mfma_f32_16x16x32_bf16 v[56:59], a[80:83], v[80:83], v[56:59]\n"
"buffer_load_dwordx4 v[192:195], v0, s[4:7], s98 offen offset:2048 sc1\n"
"v_mfma_f32_16x16x32_bf16 v[56:59], a[84:87], v[52:55], v[56:59]\n"
"ds_write_b128 v3, v[28:31] offset:13312\n"
"v_mfma_f32_16x16x32_bf16 v[56:59], a[88:91], v[152:155], v[56:59]\n"
"ds_read_b128 v[28:31], v5 offset:1024\n"
"s_waitcnt lgkmcnt(4)\n"
"v_mfma_f32_16x16x32_bf16 v[56:59], a[92:95], v[168:171], v[56:59]\n"
"v_fmac_f32_e32 v24, v253, v92\n"
"v_fmac_f32_e32 v25, v253, v93\n"
"v_mfma_f32_16x16x32_bf16 v[196:199], a[124:127], v[168:171], 0\n"
"s_add_u32 s96, s63, 0x44000\n"
"buffer_store_dword v48, v14, s[4:7], s96 offen sc1\n"
"v_fmac_f32_e32 v26, v253, v94\n"
"v_fmac_f32_e32 v27, v253, v95\n"
"v_mfma_f32_16x16x32_bf16 v[196:199], a[120:123], v[152:155], v[196:199]\n"
"buffer_load_dwordx4 v[48:51], v0, s[4:7], s98 offen offset:3072 sc1\n"
"v_mfma_f32_16x16x32_bf16 v[196:199], a[116:119], v[52:55], v[196:199]\n"
"ds_read_b128 v[92:95], v4 offset:2048\n"
"ds_read_b128 v[200:203], v9 offset:256\n"
"v_mfma_f32_16x16x32_bf16 v[196:199], a[112:115], v[80:83], v[196:199]\n"
"ds_read_b128 v[204:207], v5 offset:2048\n"
"v_add_f32_e32 v24, v24, v140\n"
"v_mfma_f32_16x16x32_bf16 v[196:199], a[108:111], v[88:91], v[196:199]\n"
"v_add_f32_e32 v25, v25, v141\n"
"v_add_f32_e32 v26, v26, v142\n"
"v_mfma_f32_16x16x32_bf16 v[196:199], a[104:107], v[84:87], v[196:199]\n"
"ds_write_b128 v3, v[56:59] offset:14336\n"
"v_mfma_f32_16x16x32_bf16 v[196:199], a[100:103], v[44:47], v[196:199]\n"
"buffer_load_dwordx4 v[56:59], v1, s[4:7], s98 offen sc1\n"
"v_mfma_f32_16x16x32_bf16 v[196:199], a[96:99], v[76:79], v[196:199]\n"
"s_waitcnt lgkmcnt(4)\n"
"v_add_f32_e32 v27, v27, v143\n"
"v_fmac_f32_e32 v180, v253, v188\n"
"v_mfma_f32_16x16x32_bf16 v[140:143], v[100:103], v[76:79], 0\n"
"buffer_load_dwordx4 a[188:191], v1, s[4:7], s97 offen offset:3072 sc1\n"
"v_mfma_f32_16x16x32_bf16 v[140:143], v[104:107], v[44:47], v[140:143]\n"
"ds_read_b128 v[208:211], v4 offset:3072\n"
"ds_read_b128 v[212:215], v9 offset:320\n"
"v_mfma_f32_16x16x32_bf16 v[140:143], v[108:111], v[84:87], v[140:143]\n"
"s_waitcnt vmcnt(19)\n"
"s_add_u32 s97, s68, 0x1000\n"
"ds_read_b128 v[216:219], v5 offset:3072\n"
"v_mfma_f32_16x16x32_bf16 v[140:143], v[116:119], v[88:91], v[140:143]\n"
"buffer_load_dwordx4 v[220:223], v1, s[4:7], s98 offen offset:1024 sc1\n"
"v_mfma_f32_16x16x32_bf16 v[140:143], v[128:131], v[80:83], v[140:143]\n"
"buffer_store_dwordx2 v[18:19], v11, s[4:7], s97 offen offset:64 sc1\n"
"v_mfma_f32_16x16x32_bf16 v[140:143], v[132:135], v[52:55], v[140:143]\n"
"ds_write_b128 v3, v[196:199] offset:15360\n"
"v_mfma_f32_16x16x32_bf16 v[140:143], v[136:139], v[152:155], v[140:143]\n"
"v_fmac_f32_e32 v181, v253, v189\n"
"v_fmac_f32_e32 v182, v253, v190\n"
"v_mfma_f32_16x16x32_bf16 v[140:143], v[96:99], v[168:171], v[140:143]\n"
"v_fmac_f32_e32 v183, v253, v191\n"
"v_add_f32_e32 v180, v180, v28\n"
"v_mfma_f32_16x16x32_bf16 v[188:191], v[112:115], v[168:171], 0\n"
"buffer_load_dwordx4 v[168:171], v1, s[4:7], s98 offen offset:2048 sc1\n"
"v_mfma_f32_16x16x32_bf16 v[188:191], v[164:167], v[152:155], v[188:191]\n"
"ds_read_b128 v[152:155], v4 offset:4096\n"
"ds_read_b128 v[196:199], v9\n"
"v_mfma_f32_16x16x32_bf16 v[188:191], v[160:163], v[52:55], v[188:191]\n"
"ds_read_b128 v[52:55], v5 offset:4096\n"
"v_add_f32_e32 v181, v181, v29\n"
"v_mfma_f32_16x16x32_bf16 v[188:191], v[156:159], v[80:83], v[188:191]\n"
"s_waitcnt lgkmcnt(8)\n"
"v_add_f32_e32 v182, v182, v30\n"
"v_add_f32_e32 v183, v183, v31\n"
"v_mfma_f32_16x16x32_bf16 v[188:191], v[124:127], v[88:91], v[188:191]\n"
"v_fmac_f32_e32 v92, v253, v200\n"
"v_fmac_f32_e32 v93, v253, v201\n"
"v_mfma_f32_16x16x32_bf16 v[188:191], v[148:151], v[84:87], v[188:191]\n"
"buffer_load_dwordx4 v[28:31], v1, s[4:7], s98 offen offset:3072 sc1\n"
"v_mfma_f32_16x16x32_bf16 v[188:191], v[144:147], v[44:47], v[188:191]\n"
"ds_write_b128 v3, v[140:143] offset:16384\n"
"v_mfma_f32_16x16x32_bf16 v[188:191], v[120:123], v[76:79], v[188:191]\n"
"s_waitcnt vmcnt(21)\n"
"ds_read_b128 v[44:47], v4 offset:5120\n"
"ds_read_b128 v[76:79], v9 offset:64\n"
"; @A G1 2.3\n"
"v_mfma_f32_16x16x32_bf16 v[24:27], a[0:3], v[60:63], v[24:27]\n"
"ds_read_b128 v[80:83], v5 offset:5120\n"
"v_fmac_f32_e32 v94, v253, v202\n"
"v_mfma_f32_16x16x32_bf16 v[24:27], a[4:7], v[64:67], v[24:27]\n"
"v_fmac_f32_e32 v95, v253, v203\n"
"v_add_f32_e32 v92, v92, v204\n"
"v_mfma_f32_16x16x32_bf16 v[24:27], a[8:11], v[36:39], v[24:27]\n"
"s_add_u32 s98, s53, 0\n"
"buffer_load_dwordx4 v[84:87], v0, s[4:7], s98 offen sc1\n"
"v_mfma_f32_16x16x32_bf16 v[24:27], a[12:15], v[72:75], v[24:27]\n"
"s_waitcnt vmcnt(16)\n"
"s_add_u32 s97, s70, 0x4000\n"
"v_add_f32_e32 v93, v93, v205\n"
"v_mfma_f32_16x16x32_bf16 v[24:27], a[16:19], v[68:71], v[24:27]\n"
"buffer_load_dwordx4 a[192:195], v0, s[4:7], s97 offen sc1\n"
"v_mfma_f32_16x16x32_bf16 v[24:27], a[20:23], v[172:175], v[24:27]\n"
"ds_write_b128 v3, v[188:191] offset:17408\n"
"v_mfma_f32_16x16x32_bf16 v[24:27], a[24:27], v[176:179], v[24:27]\n"
"s_waitcnt lgkmcnt(9)\n"
"v_add_f32_e32 v94, v94, v206\n"
"v_add_f32_e32 v95, v95, v207\n"
"v_mfma_f32_16x16x32_bf16 v[24:27], a[28:31], v[184:187], v[24:27]\n"
"buffer_load_dwordx4 v[88:91], v0, s[4:7], s98 offen offset:1024 sc1\n"
"v_mfma_f32_16x16x32_bf16 v[180:183], a[60:63], v[184:187], v[180:183]\n"
"v_fmac_f32_e32 v208, v253, v212\n"
"v_fmac_f32_e32 v209, v253, v213\n"
"v_mfma_f32_16x16x32_bf16 v[180:183], a[56:59], v[176:179], v[180:183]\n"
"v_fmac_f32_e32 v210, v253, v214\n"
"v_fmac_f32_e32 v211, v253, v215\n"
"v_mfma_f32_16x16x32_bf16 v[180:183], a[52:55], v[172:175], v[180:183]\n"
"s_waitcnt lgkmcnt(0)\n"
"s_barrier\n"
"v_mfma_f32_16x16x32_bf16 v[180:183], a[48:51], v[68:71], v[180:183]\n"
"ds_read_b128 v[140:143], v6\n"
"ds_read_b128 v[188:191], v6 offset:1024\n"
"v_mfma_f32_16x16x32_bf16 v[180:183], a[44:47], v[72:75], v[180:183]\n"
"buffer_load_dwordx4 v[200:203], v0, s[4:7], s98 offen offset:2048 sc1\n"
"v_mfma_f32_16x16x32_bf16 v[180:183], a[40:43], v[36:39], v[180:183]\n"
"v_add_f32_e32 v208, v208, v216\n"
"v_add_f32_e32 v209, v209, v217\n"
"v_mfma_f32_16x16x32_bf16 v[180:183], a[36:39], v[64:67], v[180:183]\n"
"v_add_f32_e32 v210, v210, v218\n"
"v_add_f32_e32 v211, v211, v219\n"
"v_mfma_f32_16x16x32_bf16 v[180:183], a[32:35], v[60:63], v[180:183]\n"
"v_fmac_f32_e32 v152, v253, v196\n"
"v_fmac_f32_e32 v153, v253, v197\n"
"v_mfma_f32_16x16x32_bf16 v[92:95], a[64:67], v[60:63], v[92:95]\n"
"v_fmac_f32_e32 v154, v253, v198\n"
"v_fmac_f32_e32 v155, v253, v199\n"
"v_mfma_f32_16x16x32_bf16 v[92:95], a[68:71], v[64:67], v[92:95]\n"
"buffer_load_dwordx4 v[196:199], v0, s[4:7], s98 offen offset:3072 sc1\n"
"v_mfma_f32_16x16x32_bf16 v[92:95], a[72:75], v[36:39], v[92:95]\n"
"v_add_f32_e32 v152, v152, v52\n"
"v_add_f32_e32 v153, v153, v53\n"
"v_mfma_f32_16x16x32_bf16 v[92:95], a[76:79], v[72:75], v[92:95]\n"
"v_add_f32_e32 v154, v154, v54\n"
"v_add_f32_e32 v155, v155, v55\n"
"v_mfma_f32_16x16x32_bf16 v[92:95], a[80:83], v[68:71], v[92:95]\n"
"s_waitcnt lgkmcnt(0)\n"
"v_fmac_f32_e32 v44, v253, v76\n"
"v_fmac_f32_e32 v45, v253, v77\n"
"v_mfma_f32_16x16x32_bf16 v[92:95], a[84:87], v[172:175], v[92:95]\n"
"v_fmac_f32_e32 v46, v253, v78\n"
"v_fmac_f32_e32 v47, v253, v79\n"
"v_mfma_f32_16x16x32_bf16 v[92:95], a[88:91], v[176:179], v[92:95]\n"
"buffer_load_dwordx4 v[52:55], v1, s[4:7], s98 offen sc1\n"
"v_mfma_f32_16x16x32_bf16 v[92:95], a[92:95], v[184:187], v[92:95]\n"
"ds_read_b128 v[76:79], v6 offset:2048\n"
"ds_read_b128 v[204:207], v6 offset:3072\n"
"v_mfma_f32_16x16x32_bf16 v[208:211], a[124:127], v[184:187], v[208:211]\n"
"buffer_load_dwordx4 a[196:199], v0, s[4:7], s97 offen offset:1024 sc1\n"
"v_mfma_f32_16x16x32_bf16 v[208:211], a[120:123], v[176:179], v[208:211]\n"
"v_add_f32_e32 v44, v44, v80\n"
"v_add_f32_e32 v45, v45, v81\n"
"v_mfma_f32_16x16x32_bf16 v[208:211], a[116:119], v[172:175], v[208:211]\n"
"s_add_u32 s96, s68, 0x1000\n"
"buffer_store_dwordx2 v[18:19], v11, s[4:7], s96 offen offset:96 sc1\n"
"v_add_f32_e32 v46, v46, v82\n"
"v_add_f32_e32 v47, v47, v83\n"
"v_mfma_f32_16x16x32_bf16 v[208:211], a[112:115], v[68:71], v[208:211]\n"
"buffer_load_dwordx4 v[80:83], v1, s[4:7], s98 offen offset:1024 sc1\n"
"v_mfma_f32_16x16x32_bf16 v[208:211], a[108:111], v[72:75], v[208:211]\n"
"v_add_f32_e32 v24, v24, v140\n"
"v_add_f32_e32 v25, v25, v141\n"
"v_mfma_f32_16x16x32_bf16 v[208:211], a[104:107], v[36:39], v[208:211]\n"
"v_add_f32_e32 v26, v26, v142\n"
"v_add_f32_e32 v27, v27, v143\n"
"v_mfma_f32_16x16x32_bf16 v[208:211], a[100:103], v[64:67], v[208:211]\n"
"v_add_f32_e32 v180, v180, v188\n"
"v_add_f32_e32 v181, v181, v189\n"
"v_mfma_f32_16x16x32_bf16 v[208:211], a[96:99], v[60:63], v[208:211]\n"
"v_add_f32_e32 v182, v182, v190\n"
"v_add_f32_e32 v183, v183, v191\n"
"v_mfma_f32_16x16x32_bf16 v[152:155], v[100:103], v[60:63], v[152:155]\n"
"buffer_load_dwordx4 v[140:143], v1, s[4:7], s98 offen offset:2048 sc1\n"
"v_mfma_f32_16x16x32_bf16 v[152:155], v[104:107], v[64:67], v[152:155]\n"
"v_mul_f32_e32 v24, v24, v255\n"
"v_mul_f32_e32 v25, v25, v255\n"
"v_mfma_f32_16x16x32_bf16 v[152:155], v[108:111], v[36:39], v[152:155]\n"
"v_mul_f32_e32 v26, v26, v255\n"
"v_mul_f32_e32 v27, v27, v255\n"
"v_mfma_f32_16x16x32_bf16 v[152:155], v[116:119], v[72:75], v[152:155]\n"
"v_mul_f32_e32 v180, v180, v255\n"
"v_mul_f32_e32 v181, v181, v255\n"
"v_mfma_f32_16x16x32_bf16 v[152:155], v[128:131], v[68:71], v[152:155]\n"
"s_waitcnt lgkmcnt(0)\n"
"v_mul_f32_e32 v182, v182, v255\n"
"v_mul_f32_e32 v183, v183, v255\n"
"v_mfma_f32_16x16x32_bf16 v[152:155], v[132:135], v[172:175], v[152:155]\n"
"buffer_load_dwordx4 v[188:191], v1, s[4:7], s98 offen offset:3072 sc1\n"
"v_mfma_f32_16x16x32_bf16 v[152:155], v[136:139], v[176:179], v[152:155]\n"
"ds_read_b128 v[212:215], v6 offset:4096\n"
"ds_read_b128 v[216:219], v6 offset:5120\n"
"v_mfma_f32_16x16x32_bf16 v[152:155], v[96:99], v[184:187], v[152:155]\n"
"v_cvt_pk_bf16_f32 v224, v24, v25\n"
"v_cvt_pk_bf16_f32 v225, v26, v27\n"
"v_mfma_f32_16x16x32_bf16 v[44:47], v[112:115], v[184:187], v[44:47]\n"
"v_cvt_pk_bf16_f32 v226, v180, v181\n"
"v_cvt_pk_bf16_f32 v227, v182, v183\n"
"v_mfma_f32_16x16x32_bf16 v[44:47], v[164:167], v[176:179], v[44:47]\n"
"ds_write_b128 v8, v[224:227] offset:17408\n"
"v_mfma_f32_16x16x32_bf16 v[44:47], v[160:163], v[172:175], v[44:47]\n"
"v_add_f32_e32 v92, v92, v76\n"
"v_add_f32_e32 v93, v93, v77\n"
"v_mfma_f32_16x16x32_bf16 v[44:47], v[156:159], v[68:71], v[44:47]\n"
"v_add_f32_e32 v94, v94, v78\n"
"v_add_f32_e32 v95, v95, v79\n"
"v_mfma_f32_16x16x32_bf16 v[44:47], v[124:127], v[72:75], v[44:47]\n"
"v_add_f32_e32 v208, v208, v204\n"
"v_add_f32_e32 v209, v209, v205\n"
"v_mfma_f32_16x16x32_bf16 v[44:47], v[148:151], v[36:39], v[44:47]\n"
"v_add_f32_e32 v210, v210, v206\n"
"v_add_f32_e32 v211, v211, v207\n"
"v_mfma_f32_16x16x32_bf16 v[44:47], v[144:147], v[64:67], v[44:47]\n"
"v_mul_f32_e32 v92, v92, v255\n"
"v_mul_f32_e32 v93, v93, v255\n"
"v_mfma_f32_16x16x32_bf16 v[44:47], v[120:123], v[60:63], v[44:47]\n"
"v_mul_f32_e32 v94, v94, v255\n"
"v_mul_f32_e32 v95, v95, v255\n"
"v_mul_f32_e32 v208, v208, v255\n"
"v_mul_f32_e32 v209, v209, v255\n"
"v_mul_f32_e32 v210, v210, v255\n"
"v_mul_f32_e32 v211, v211, v255\n"
"v_cvt_pk_bf16_f32 v24, v92, v93\n"
"v_cvt_pk_bf16_f32 v25, v94, v95\n"
"v_cvt_pk_bf16_f32 v26, v208, v209\n"
"v_cvt_pk_bf16_f32 v27, v210, v211\n"
"ds_write_b128 v7, v[24:27] offset:50176\n"
".Lmk_z3:\n"
"s_waitcnt vmcnt(17)\n"
"; @A G1 3.0\n"
"v_mfma_f32_16x16x32_bf16 v[36:39], a[0:3], v[32:35], 0\n"
"v_mfma_f32_16x16x32_bf16 v[36:39], a[4:7], v[40:43], v[36:39]\n"
"s_add_u32 s98, s62, 0\n"
"v_mfma_f32_16x16x32_bf16 v[36:39], a[8:11], v[192:195], v[36:39]\n"
"s_add_u32 s96, s54, 0\n"
"buffer_load_dwordx4 v[60:63], v0, s[4:7], s96 offen sc1\n"
"v_mfma_f32_16x16x32_bf16 v[36:39], a[12:15], v[48:51], v[36:39]\n"
"s_waitcnt vmcnt(12)\n"
"v_mfma_f32_16x16x32_bf16 v[36:39], a[16:19], v[56:59], v[36:39]\n"
"buffer_load_dword v255, v10, s[4:7], s98 offen sc1\n"
"v_mfma_f32_16x16x32_bf16 v[36:39], a[20:23], v[220:223], v[36:39]\n"
"s_waitcnt lgkmcnt(2)\n"
"v_add_f32_e32 v152, v152, v212\n"
"v_add_f32_e32 v153, v153, v213\n"
"v_mfma_f32_16x16x32_bf16 v[36:39], a[24:27], v[168:171], v[36:39]\n"
"v_add_f32_e32 v154, v154, v214\n"
"v_add_f32_e32 v155, v155, v215\n"
"v_mfma_f32_16x16x32_bf16 v[36:39], a[28:31], v[28:31], v[36:39]\n"
"buffer_load_dwordx4 v[64:67], v0, s[4:7], s96 offen offset:1024 sc1\n"
"v_mfma_f32_16x16x32_bf16 v[68:71], a[60:63], v[28:31], 0\n"
"buffer_load_dwordx4 a[200:203], v0, s[4:7], s97 offen offset:2048 sc1\n"
"v_mfma_f32_16x16x32_bf16 v[68:71], a[56:59], v[168:171], v[68:71]\n"
"buffer_load_dword v253, v10, s[4:7], s98 offen offset:256 sc1\n"
"v_mfma_f32_16x16x32_bf16 v[68:71], a[52:55], v[220:223], v[68:71]\n"
"v_add_f32_e32 v44, v44, v216\n"
"v_add_f32_e32 v45, v45, v217\n"
"v_mfma_f32_16x16x32_bf16 v[68:71], a[48:51], v[56:59], v[68:71]\n"
"v_add_f32_e32 v46, v46, v218\n"
"v_add_f32_e32 v47, v47, v219\n"
"v_mfma_f32_16x16x32_bf16 v[68:71], a[44:47], v[48:51], v[68:71]\n"
"buffer_load_dwordx4 v[72:75], v0, s[4:7], s96 offen offset:2048 sc1\n"
"v_mfma_f32_16x16x32_bf16 v[68:71], a[40:43], v[192:195], v[68:71]\n"
"ds_write_b128 v3, v[36:39]\n"
"v_mfma_f32_16x16x32_bf16 v[68:71], a[36:39], v[40:43], v[68:71]\n"
"buffer_load_dword v252, v10, s[4:7], s98 offen offset:512 sc1\n"
"v_mfma_f32_16x16x32_bf16 v[68:71], a[32:35], v[32:35], v[68:71]\n"
"v_mul_f32_e32 v152, v152, v254\n"
"v_mul_f32_e32 v153, v153, v254\n"
"v_mfma_f32_16x16x32_bf16 v[36:39], a[64:67], v[32:35], 0\n"
"v_mul_f32_e32 v154, v154, v254\n"
"v_mul_f32_e32 v155, v155, v254\n"
"v_mfma_f32_16x16x32_bf16 v[36:39], a[68:71], v[40:43], v[36:39]\n"
"buffer_load_dwordx4 v[76:79], v0, s[4:7], s96 offen offset:3072 sc1\n"
"v_mfma_f32_16x16x32_bf16 v[36:39], a[72:75], v[192:195], v[36:39]\n"
"buffer_load_dword v251, v10, s[4:7], s98 offen offset:768 sc1\n"
"v_mfma_f32_16x16x32_bf16 v[36:39], a[76:79], v[48:51], v[36:39]\n"
"v_mul_f32_e32 v44, v44, v254\n"
"v_mul_f32_e32 v45, v45, v254\n"
"v_mfma_f32_16x16x32_bf16 v[36:39], a[80:83], v[56:59], v[36:39]\n"
"v_mul_f32_e32 v46, v46, v254\n"
"v_mul_f32_e32 v47, v47, v254\n"
"v_mfma_f32_16x16x32_bf16 v[36:39], a[84:87], v[220:223], v[36:39]\n"
"ds_write_b128 v3, v[68:71] offset:1024\n"
"v_mfma_f32_16x16x32_bf16 v[36:39], a[88:91], v[168:171], v[36:39]\n"
"buffer_load_dwordx4 v[68:71], v1, s[4:7], s96 offen sc1\n"
"v_mfma_f32_16x16x32_bf16 v[36:39], a[92:95], v[28:31], v[36:39]\n"
"buffer_load_dword v254, v10, s[4:7], s98 offen offset:1024 sc1\n"
"v_mfma_f32_16x16x32_bf16 v[92:95], a[124:127], v[28:31], 0\n"
"v_cvt_pk_bf16_f32 v172, v152, v153\n"
"v_cvt_pk_bf16_f32 v173, v154, v155\n"
"v_mfma_f32_16x16x32_bf16 v[92:95], a[120:123], v[168:171], v[92:95]\n"
"v_cvt_pk_bf16_f32 v174, v44, v45\n"
"v_cvt_pk_bf16_f32 v175, v46, v47\n"
"v_mfma_f32_16x16x32_bf16 v[92:95], a[116:119], v[220:223], v[92:95]\n"
"buffer_load_dwordx4 a[204:207], v0, s[4:7], s97 offen offset:3072 sc1\n"
"v_mfma_f32_16x16x32_bf16 v[92:95], a[112:115], v[56:59], v[92:95]\n"
"buffer_load_dwordx4 v[44:47], v1, s[4:7], s96 offen offset:1024 sc1\n"
"v_mfma_f32_16x16x32_bf16 v[92:95], a[108:111], v[48:51], v[92:95]\n"
"buffer_load_dword v250, v10, s[4:7], s98 offen offset:1280 sc1\n"
"v_mfma_f32_16x16x32_bf16 v[92:95], a[104:107], v[192:195], v[92:95]\n"
"ds_write_b128 v3, v[36:39] offset:2048\n"
"v_mfma_f32_16x16x32_bf16 v[92:95], a[100:103], v[40:43], v[92:95]\n"
"ds_write_b128 v2, v[172:175] offset:17408\n"
"v_mfma_f32_16x16x32_bf16 v[92:95], a[96:99], v[32:35], v[92:95]\n"
"buffer_load_dword v249, v10, s[4:7], s98 offen offset:1536 sc1\n"
"v_mfma_f32_16x16x32_bf16 v[36:39], v[100:103], v[32:35], 0\n"
"buffer_load_dwordx4 v[152:155], v1, s[4:7], s96 offen offset:2048 sc1\n"
"v_mfma_f32_16x16x32_bf16 v[36:39], v[104:107], v[40:43], v[36:39]\n"
"buffer_store_dwordx4 v[224:227], v8, s[4:7], s64 offen offset:1024 sc1\n"
"v_mfma_f32_16x16x32_bf16 v[36:39], v[108:111], v[192:195], v[36:39]\n"
"buffer_load_dword v248, v10, s[4:7], s98 offen offset:1792 sc1\n"
"v_mfma_f32_16x16x32_bf16 v[36:39], v[116:119], v[48:51], v[36:39]\n"
"v_mfma_f32_16x16x32_bf16 v[36:39], v[128:131], v[56:59], v[36:39]\n"
"v_mfma_f32_16x16x32_bf16 v[36:39], v[132:135], v[220:223], v[36:39]\n"
"buffer_load_dwordx4 v[172:175], v1, s[4:7], s96 offen offset:3072 sc1\n"
"v_mfma_f32_16x16x32_bf16 v[36:39], v[136:139], v[168:171], v[36:39]\n"
"ds_write_b128 v3, v[92:95] offset:3072\n"
"v_mfma_f32_16x16x32_bf16 v[36:39], v[96:99], v[28:31], v[36:39]\n"
"v_mfma_f32_16x16x32_bf16 v[92:95], v[112:115], v[28:31], 0\n"
"v_mfma_f32_16x16x32_bf16 v[92:95], v[164:167], v[168:171], v[92:95]\n"
"v_mfma_f32_16x16x32_bf16 v[92:95], v[160:163], v[220:223], v[92:95]\n"
"v_mfma_f32_16x16x32_bf16 v[92:95], v[156:159], v[56:59], v[92:95]\n"
"v_mfma_f32_16x16x32_bf16 v[92:95], v[124:127], v[48:51], v[92:95]\n"
"buffer_load_dwordx4 a[208:211], v1, s[4:7], s97 offen sc1\n"
"v_mfma_f32_16x16x32_bf16 v[92:95], v[148:151], v[192:195], v[92:95]\n"
"s_nop 0\n"
"ds_write_b128 v3, v[36:39] offset:4096\n"
"v_mfma_f32_16x16x32_bf16 v[92:95], v[144:147], v[40:43], v[92:95]\n"
"v_mfma_f32_16x16x32_bf16 v[92:95], v[120:123], v[32:35], v[92:95]\n"
"s_waitcnt vmcnt(26)\n"
"; @A G1 3.1\n"
"v_mfma_f32_16x16x32_bf16 v[28:31], a[0:3], v[84:87], 0\n"
"v_mfma_f32_16x16x32_bf16 v[28:31], a[4:7], v[88:91], v[28:31]\n"
"v_mfma_f32_16x16x32_bf16 v[28:31], a[8:11], v[200:203], v[28:31]\n"
"s_add_u32 s98, s55, 0\n"
"buffer_load_dwordx4 v[32:35], v0, s[4:7], s98 offen sc1\n"
"v_mfma_f32_16x16x32_bf16 v[28:31], a[12:15], v[196:199], v[28:31]\n"
"s_waitcnt vmcnt(21)\n"
"v_mfma_f32_16x16x32_bf16 v[28:31], a[16:19], v[52:55], v[28:31]\n"
"v_mfma_f32_16x16x32_bf16 v[28:31], a[20:23], v[80:83], v[28:31]\n"
"ds_write_b128 v3, v[92:95] offset:5120\n"
"v_mfma_f32_16x16x32_bf16 v[28:31], a[24:27], v[140:143], v[28:31]\n"
"v_mfma_f32_16x16x32_bf16 v[28:31], a[28:31], v[188:191], v[28:31]\n"
"buffer_load_dwordx4 v[36:39], v0, s[4:7], s98 offen offset:1024 sc1\n"
"s_add_u32 s96, s64, 0x4000\n"
"v_mfma_f32_16x16x32_bf16 v[40:43], a[60:63], v[188:191], 0\n"
"v_mfma_f32_16x16x32_bf16 v[40:43], a[56:59], v[140:143], v[40:43]\n"
"buffer_store_dwordx4 v[24:27], v7, s[4:7], s96 offen offset:1024 sc1\n"
"v_mfma_f32_16x16x32_bf16 v[40:43], a[52:55], v[80:83], v[40:43]\n"
"v_mfma_f32_16x16x32_bf16 v[40:43], a[48:51], v[52:55], v[40:43]\n"
"v_mfma_f32_16x16x32_bf16 v[40:43], a[44:47], v[196:199], v[40:43]\n"
"buffer_load_dwordx4 v[24:27], v0, s[4:7], s98 offen offset:2048 sc1\n"
"v_mfma_f32_16x16x32_bf16 v[40:43], a[40:43], v[200:203], v[40:43]\n"
"ds_write_b128 v3, v[28:31] offset:6144\n"
"v_mfma_f32_16x16x32_bf16 v[40:43], a[36:39], v[88:91], v[40:43]\n"
"buffer_load_dwordx4 a[212:215], v1, s[4:7], s97 offen offset:1024 sc1\n"
"v_mfma_f32_16x16x32_bf16 v[40:43], a[32:35], v[84:87], v[40:43]\n"
"s_mov_b32 s20, 0xff0000\n"
"s_mov_b32 s21, 0xffffffff\n"
"s_mov_b32 s22, 0x1ff0001\n"
"v_mfma_f32_16x16x32_bf16 v[28:31], a[64:67], v[84:87], 0\n"
"s_mov_b32 s23, 0xffffffff\n"
"s_mov_b32 s24, 0x3ff0003\n"
"s_mov_b32 s25, 0xffffffff\n"
"v_mfma_f32_16x16x32_bf16 v[28:31], a[68:71], v[88:91], v[28:31]\n"
"buffer_load_dwordx4 v[48:51], v0, s[4:7], s98 offen offset:3072 sc1\n"
"v_mfma_f32_16x16x32_bf16 v[28:31], a[72:75], v[200:203], v[28:31]\n"
"s_mov_b32 s26, 0x7ff0007\n"
"s_mov_b32 s27, 0xffffffff\n"
"s_mov_b32 s28, 0xfff000f\n"
"v_mfma_f32_16x16x32_bf16 v[28:31], a[76:79], v[196:199], v[28:31]\n"
"s_mov_b32 s29, 0xffffffff\n"
"s_mov_b32 s30, 0x1fff001f\n"
"s_mov_b32 s31, 0xffffffff\n"
"v_mfma_f32_16x16x32_bf16 v[28:31], a[80:83], v[52:55], v[28:31]\n"
"s_mov_b32 s32, 0x3fff003f\n"
"s_mov_b32 s33, 0xffffffff\n"
"s_mov_b32 s34, 0x7fff007f\n"
"v_mfma_f32_16x16x32_bf16 v[28:31], a[84:87], v[80:83], v[28:31]\n"
"ds_write_b128 v3, v[40:43] offset:7168\n"
"v_mfma_f32_16x16x32_bf16 v[28:31], a[88:91], v[140:143], v[28:31]\n"
"buffer_load_dwordx4 v[40:43], v1, s[4:7], s98 offen sc1\n"
"v_mfma_f32_16x16x32_bf16 v[28:31], a[92:95], v[188:191], v[28:31]\n"
"s_mov_b32 s35, 0xffffffff\n"
"s_mov_b32 s36, 0\n"
"s_mov_b32 s37, 0xff0000\n"
"v_mfma_f32_16x16x32_bf16 v[56:59], a[124:127], v[188:191], 0\n"
"s_mov_b32 s38, 0\n"
"s_mov_b32 s39, 0x1ff0001\n"
"s_mov_b32 s40, 0\n"
"v_mfma_f32_16x16x32_bf16 v[56:59], a[120:123], v[140:143], v[56:59]\n"
"s_mov_b32 s41, 0x3ff0003\n"
"s_mov_b32 s42, 0\n"
"s_mov_b32 s43, 0x7ff0007\n"
"v_mfma_f32_16x16x32_bf16 v[56:59], a[116:119], v[80:83], v[56:59]\n"
"s_mov_b32 s44, 0\n"
"s_mov_b32 s45, 0xfff000f\n"
"s_mov_b32 s46, 0\n"
"v_mfma_f32_16x16x32_bf16 v[56:59], a[112:115], v[52:55], v[56:59]\n"
"buffer_load_dwordx4 v[92:95], v1, s[4:7], s98 offen offset:1024 sc1\n"
"v_mfma_f32_16x16x32_bf16 v[56:59], a[108:111], v[196:199], v[56:59]\n"
"s_mov_b32 s47, 0x1fff001f\n"
"s_mov_b32 s48, 0\n"
"s_mov_b32 s49, 0x3fff003f\n"
"v_mfma_f32_16x16x32_bf16 v[56:59], a[104:107], v[200:203], v[56:59]\n"
"ds_write_b128 v3, v[28:31] offset:8192\n"
"v_mfma_f32_16x16x32_bf16 v[56:59], a[100:103], v[88:91], v[56:59]\n"
"s_mov_b32 s50, 0\n"
"s_mov_b32 s51, 0x7fff007f\n"
"v_mfma_f32_16x16x32_bf16 v[56:59], a[96:99], v[84:87], v[56:59]\n"
"v_mfma_f32_16x16x32_bf16 v[28:31], v[100:103], v[84:87], 0\n"
"buffer_load_dwordx4 v[168:171], v1, s[4:7], s98 offen offset:2048 sc1\n"
"v_mfma_f32_16x16x32_bf16 v[28:31], v[104:107], v[88:91], v[28:31]\n"
"buffer_load_dwordx4 a[216:219], v1, s[4:7], s97 offen offset:2048 sc1\n"
"v_mfma_f32_16x16x32_bf16 v[28:31], v[108:111], v[200:203], v[28:31]\n"
"v_mfma_f32_16x16x32_bf16 v[28:31], v[116:119], v[196:199], v[28:31]\n"
"v_mfma_f32_16x16x32_bf16 v[28:31], v[128:131], v[52:55], v[28:31]\n"
"s_waitcnt vmcnt(12)\n"
"v_add_f32_e32 v255, v255, v254\n"
"v_add_f32_e32 v253, v253, v250\n"
"v_mfma_f32_16x16x32_bf16 v[28:31], v[132:135], v[80:83], v[28:31]\n"
"buffer_load_dwordx4 v[176:179], v1, s[4:7], s98 offen offset:3072 sc1\n"
"v_mfma_f32_16x16x32_bf16 v[28:31], v[136:139], v[140:143], v[28:31]\n"
"ds_write_b128 v3, v[56:59] offset:9216\n"
"v_mfma_f32_16x16x32_bf16 v[28:31], v[96:99], v[188:191], v[28:31]\n"
"s_add_u32 s98, s75, 0x1000\n"
"buffer_load_dwordx2 v[246:247], v13, s[4:7], s98 offen sc1\n"
"v_mfma_f32_16x16x32_bf16 v[56:59], v[112:115], v[188:191], 0\n"
"v_add_f32_e32 v252, v252, v249\n"
"v_add_f32_e32 v251, v251, v248\n"
"v_mfma_f32_16x16x32_bf16 v[56:59], v[164:167], v[140:143], v[56:59]\n"
"v_add_f32_e32 v255, v255, v252\n"
"v_add_f32_e32 v253, v253, v251\n"
"v_mfma_f32_16x16x32_bf16 v[56:59], v[160:163], v[80:83], v[56:59]\n"
"s_add_u32 s96, s71, 0\n"
"buffer_load_dwordx2 v[250:251], off, s[4:7], s96 sc1\n"
"v_mfma_f32_16x16x32_bf16 v[56:59], v[156:159], v[52:55], v[56:59]\n"
"v_add_f32_e32 v255, v255, v253\n"
"v_mov_b32_e32 v253, v255\n"
"v_mfma_f32_16x16x32_bf16 v[56:59], v[124:127], v[196:199], v[56:59]\n"
"s_nop 0\n"
"v_permlane32_swap_b32_e32 v255, v253\n"
"v_add_f32_e32 v255, v255, v253\n"
"v_mfma_f32_16x16x32_bf16 v[56:59], v[148:151], v[200:203], v[56:59]\n"
"ds_write_b128 v3, v[28:31] offset:10240\n"
"v_mfma_f32_16x16x32_bf16 v[56:59], v[144:147], v[88:91], v[56:59]\n"
"buffer_store_dwordx2 v[18:19], v11, s[4:7], s68 offen offset:32 sc1\n"
"v_mul_f32_e32 v255, 0x3a800000, v255\n"
"v_mov_b32_e32 v253, v255\n"
"v_mfma_f32_16x16x32_bf16 v[56:59], v[120:123], v[84:87], v[56:59]\n"
"s_nop 0\n"
"v_permlane16_swap_b32_e32 v255, v253\n"
"v_cvt_pk_bf16_f32 v254, -v255, 0\n"
"; @A G1 3.2\n"
"v_mfma_f32_16x16x32_bf16 v[28:31], a[0:3], v[60:63], 0\n"
"v_fma_f32 v253, -v255, v255, v253\n"
"v_lshlrev_b32_e32 v254, 16, v254\n"
"v_mfma_f32_16x16x32_bf16 v[28:31], a[4:7], v[64:67], v[28:31]\n"
"v_max_f32_e32 v253, 0, v253\n"
"v_cvt_pk_bf16_f32 v253, v253, 0\n"
"v_mfma_f32_16x16x32_bf16 v[28:31], a[8:11], v[72:75], v[28:31]\n"
"buffer_load_dwordx4 a[220:223], v1, s[4:7], s97 offen offset:3072 sc1\n"
"v_mfma_f32_16x16x32_bf16 v[28:31], a[12:15], v[76:79], v[28:31]\n"
"s_waitcnt vmcnt(16)\n"
"v_lshlrev_b32_e32 v253, 16, v253\n"
"v_add_f32_e32 v253, 0x3727c5ac, v253\n"
"v_mfma_f32_16x16x32_bf16 v[28:31], a[16:19], v[68:71], v[28:31]\n"
"s_add_u32 s97, s70, 0x6000\n"
"v_cvt_pk_bf16_f32 v253, v253, 0\n"
"v_lshlrev_b32_e32 v253, 16, v253\n"
"v_mfma_f32_16x16x32_bf16 v[28:31], a[20:23], v[44:47], v[28:31]\n"
"ds_write_b128 v3, v[56:59] offset:11264\n"
"v_mfma_f32_16x16x32_bf16 v[28:31], a[24:27], v[152:155], v[28:31]\n"
"buffer_load_dwordx4 a[224:227], v0, s[4:7], s97 offen sc1\n"
"v_mfma_f32_16x16x32_bf16 v[28:31], a[28:31], v[172:175], v[28:31]\n"
"v_sqrt_f32_e32 v253, v253\n"
"v_mfma_f32_16x16x32_bf16 v[52:55], a[60:63], v[172:175], 0\n"
"v_cvt_pk_bf16_f32 v253, v253, 0\n"
"v_lshlrev_b32_e32 v253, 16, v253\n"
"v_mfma_f32_16x16x32_bf16 v[52:55], a[56:59], v[152:155], v[52:55]\n"
"v_rcp_f32_e32 v255, v253\n"
"v_mfma_f32_16x16x32_bf16 v[52:55], a[52:55], v[44:47], v[52:55]\n"
"s_waitcnt lgkmcnt(0)\n"
"s_barrier\n"
"v_mfma_f32_16x16x32_bf16 v[52:55], a[48:51], v[68:71], v[52:55]\n"
"buffer_load_dwordx4 a[228:231], v0, s[4:7], s97 offen offset:1024 sc1\n"
"v_mfma_f32_16x16x32_bf16 v[52:55], a[44:47], v[76:79], v[52:55]\n"
"ds_read_b128 v[56:59], v4\n"
"v_mul_f32_e32 v253, 0x3e8293ee, v255\n"
"v_mfma_f32_16x16x32_bf16 v[52:55], a[40:43], v[72:75], v[52:55]\n"
"ds_write_b128 v3, v[28:31] offset:12288\n"
"v_mfma_f32_16x16x32_bf16 v[52:55], a[36:39], v[64:67], v[52:55]\n"
"ds_read_b128 v[28:31], v9 offset:128\n"
"ds_read_b128 v[80:83], v5\n"
"v_mfma_f32_16x16x32_bf16 v[52:55], a[32:35], v[60:63], v[52:55]\n"
"buffer_load_dwordx4 a[232:235], v0, s[4:7], s97 offen offset:2048 sc1\n"
"v_mfma_f32_16x16x32_bf16 v[84:87], a[64:67], v[60:63], 0\n"
"v_mfma_f32_16x16x32_bf16 v[84:87], a[68:71], v[64:67], v[84:87]\n"
"v_mfma_f32_16x16x32_bf16 v[84:87], a[72:75], v[72:75], v[84:87]\n"
"ds_read_b128 v[88:91], v4 offset:1024\n"
"ds_read_b128 v[140:143], v9 offset:192\n"
"v_mfma_f32_16x16x32_bf16 v[84:87], a[76:79], v[76:79], v[84:87]\n"
"buffer_load_dwordx4 a[236:239], v0, s[4:7], s97 offen offset:3072 sc1\n"
"v_mfma_f32_16x16x32_bf16 v[84:87], a[80:83], v[68:71], v[84:87]\n"
"ds_read_b128 v[180:183], v5 offset:1024\n"
"s_waitcnt lgkmcnt(3)\n"
"v_mfma_f32_16x16x32_bf16 v[84:87], a[84:87], v[44:47], v[84:87]\n"
"ds_write_b128 v3, v[52:55] offset:13312\n"
"v_mfma_f32_16x16x32_bf16 v[84:87], a[88:91], v[152:155], v[84:87]\n"
"v_fmac_f32_e32 v56, v254, v28\n"
"v_fmac_f32_e32 v57, v254, v29\n"
"v_mfma_f32_16x16x32_bf16 v[84:87], a[92:95], v[172:175], v[84:87]\n"
"buffer_load_dwordx4 a[240:243], v1, s[4:7], s97 offen sc1\n"
"v_mfma_f32_16x16x32_bf16 v[52:55], a[124:127], v[172:175], 0\n"
"v_fmac_f32_e32 v58, v254, v30\n"
"v_fmac_f32_e32 v59, v254, v31\n"
"v_mfma_f32_16x16x32_bf16 v[52:55], a[120:123], v[152:155], v[52:55]\n"
"ds_read_b128 v[28:31], v4 offset:2048\n"
"ds_read_b128 v[184:187], v9 offset:256\n"
"v_mfma_f32_16x16x32_bf16 v[52:55], a[116:119], v[44:47], v[52:55]\n"
"ds_read_b128 v[188:191], v5 offset:2048\n"
"v_add_f32_e32 v56, v56, v80\n"
"v_mfma_f32_16x16x32_bf16 v[52:55], a[112:115], v[68:71], v[52:55]\n"
"buffer_load_dwordx4 a[244:247], v1, s[4:7], s97 offen offset:1024 sc1\n"
"v_mfma_f32_16x16x32_bf16 v[52:55], a[108:111], v[76:79], v[52:55]\n"
"v_add_f32_e32 v57, v57, v81\n"
"v_add_f32_e32 v58, v58, v82\n"
"v_mfma_f32_16x16x32_bf16 v[52:55], a[104:107], v[72:75], v[52:55]\n"
"ds_write_b128 v3, v[84:87] offset:14336\n"
"v_mfma_f32_16x16x32_bf16 v[52:55], a[100:103], v[64:67], v[52:55]\n"
"s_waitcnt lgkmcnt(5)\n"
"v_add_f32_e32 v59, v59, v83\n"
"v_fmac_f32_e32 v88, v254, v140\n"
"v_mfma_f32_16x16x32_bf16 v[52:55], a[96:99], v[60:63], v[52:55]\n"
"buffer_load_dwordx4 a[248:251], v1, s[4:7], s97 offen offset:2048 sc1\n"
"v_mfma_f32_16x16x32_bf16 v[80:83], v[100:103], v[60:63], 0\n"
"ds_read_b128 v[84:87], v4 offset:3072\n"
"ds_read_b128 v[192:195], v9 offset:320\n"
"v_mfma_f32_16x16x32_bf16 v[80:83], v[104:107], v[64:67], v[80:83]\n"
"ds_read_b128 v[196:199], v5 offset:3072\n"
"v_fmac_f32_e32 v89, v254, v141\n"
"v_mfma_f32_16x16x32_bf16 v[80:83], v[108:111], v[72:75], v[80:83]\n"
"v_fmac_f32_e32 v90, v254, v142\n"
"v_fmac_f32_e32 v91, v254, v143\n"
"v_mfma_f32_16x16x32_bf16 v[80:83], v[116:119], v[76:79], v[80:83]\n"
"buffer_load_dwordx4 a[252:255], v1, s[4:7], s97 offen offset:3072 sc1\n"
"v_mfma_f32_16x16x32_bf16 v[80:83], v[128:131], v[68:71], v[80:83]\n"
"s_waitcnt vmcnt(11)\n"
"v_cmp_ne_u64_e32 vcc, v[246:247], v[18:19]\n"
"v_mfma_f32_16x16x32_bf16 v[80:83], v[132:135], v[44:47], v[80:83]\n"
"ds_write_b128 v3, v[52:55] offset:15360\n"
"v_mfma_f32_16x16x32_bf16 v[80:83], v[136:139], v[152:155], v[80:83]\n"
"s_cbranch_vccnz .Lmk_f7s\n"
".Lmk_f7r:\n"
"v_add_f32_e32 v88, v88, v180\n"
"v_mfma_f32_16x16x32_bf16 v[80:83], v[96:99], v[172:175], v[80:83]\n"
"ds_read_b128 v[52:55], v4 offset:4096\n"
"ds_read_b128 v[140:143], v9\n"
"v_mfma_f32_16x16x32_bf16 v[200:203], v[112:115], v[172:175], 0\n"
"ds_read_b128 v[172:175], v5 offset:4096\n"
"v_add_f32_e32 v89, v89, v181\n"
"v_mfma_f32_16x16x32_bf16 v[200:203], v[164:167], v[152:155], v[200:203]\n"
"s_waitcnt lgkmcnt(8)\n"
"v_add_f32_e32 v90, v90, v182\n"
"v_add_f32_e32 v91, v91, v183\n"
"v_mfma_f32_16x16x32_bf16 v[200:203], v[160:163], v[44:47], v[200:203]\n"
"v_fmac_f32_e32 v28, v254, v184\n"
"v_fmac_f32_e32 v29, v254, v185\n"
"v_mfma_f32_16x16x32_bf16 v[200:203], v[156:159], v[68:71], v[200:203]\n"
"v_fmac_f32_e32 v30, v254, v186\n"
"v_fmac_f32_e32 v31, v254, v187\n"
"v_mfma_f32_16x16x32_bf16 v[200:203], v[124:127], v[76:79], v[200:203]\n"
"s_waitcnt vmcnt(10)\n"
"v_cmp_ne_u64_e32 vcc, v[250:251], v[18:19]\n"
"v_mfma_f32_16x16x32_bf16 v[200:203], v[148:151], v[72:75], v[200:203]\n"
"ds_write_b128 v3, v[80:83] offset:16384\n"
"v_mfma_f32_16x16x32_bf16 v[200:203], v[144:147], v[64:67], v[200:203]\n"
"s_cbranch_vccnz .Lmk_f4s\n"
".Lmk_f4r:\n"
"s_add_u32 m0, s77, 0x6000\n"
"v_mfma_f32_16x16x32_bf16 v[200:203], v[120:123], v[60:63], v[200:203]\n"
"s_add_u32 s98, s65, 0x2000\n"
"ds_read_b128 v[44:47], v4 offset:5120\n"
"; @A G1 3.3\n"
"v_mfma_f32_16x16x32_bf16 v[56:59], a[0:3], v[32:35], v[56:59]\n"
"buffer_load_dwordx4 v2, s[4:7], s98 offen sc1 lds\n"
"v_mfma_f32_16x16x32_bf16 v[56:59], a[4:7], v[36:39], v[56:59]\n"
"buffer_load_dwordx4 v2, s[4:7], s98 offen offset:1024 sc1 lds\n"
"v_mfma_f32_16x16x32_bf16 v[56:59], a[8:11], v[24:27], v[56:59]\n"
"ds_read_b128 v[60:63], v9 offset:64\n"
"ds_read_b128 v[64:67], v5 offset:5120\n"
"v_mfma_f32_16x16x32_bf16 v[56:59], a[12:15], v[48:51], v[56:59]\n"
"v_add_f32_e32 v28, v28, v188\n"
"v_add_f32_e32 v29, v29, v189\n"
"v_mfma_f32_16x16x32_bf16 v[56:59], a[16:19], v[40:43], v[56:59]\n"
"s_waitcnt lgkmcnt(8)\n"
"v_add_f32_e32 v30, v30, v190\n"
"v_add_f32_e32 v31, v31, v191\n"
"v_mfma_f32_16x16x32_bf16 v[56:59], a[20:23], v[92:95], v[56:59]\n"
"ds_write_b128 v3, v[200:203] offset:17408\n"
"v_mfma_f32_16x16x32_bf16 v[56:59], a[24:27], v[168:171], v[56:59]\n"
"v_fmac_f32_e32 v84, v254, v192\n"
"v_fmac_f32_e32 v85, v254, v193\n"
"v_mfma_f32_16x16x32_bf16 v[56:59], a[28:31], v[176:179], v[56:59]\n"
"s_add_u32 m0, s77, 0xe000\n"
"s_add_u32 s98, s65, 0x6000\n"
"v_fmac_f32_e32 v86, v254, v194\n"
"v_mfma_f32_16x16x32_bf16 v[88:91], a[60:63], v[176:179], v[88:91]\n"
"buffer_load_dwordx4 v2, s[4:7], s98 offen sc1 lds\n"
"v_mfma_f32_16x16x32_bf16 v[88:91], a[56:59], v[168:171], v[88:91]\n"
"buffer_load_dwordx4 v2, s[4:7], s98 offen offset:1024 sc1 lds\n"
"v_mfma_f32_16x16x32_bf16 v[88:91], a[52:55], v[92:95], v[88:91]\n"
"s_waitcnt lgkmcnt(0)\n"
"s_barrier\n"
"v_mfma_f32_16x16x32_bf16 v[88:91], a[48:51], v[40:43], v[88:91]\n"
"ds_read_b128 v[68:71], v6\n"
"ds_read_b128 v[72:75], v6 offset:1024\n"
"v_mfma_f32_16x16x32_bf16 v[88:91], a[44:47], v[48:51], v[88:91]\n"
"s_add_u32 s98, s69, 0x600000\n"
"buffer_load_dwordx4 a[0:3], v0, s[4:7], s98 offen sc1\n"
"v_mfma_f32_16x16x32_bf16 v[88:91], a[40:43], v[24:27], v[88:91]\n"
"v_fmac_f32_e32 v87, v254, v195\n"
"v_add_f32_e32 v84, v84, v196\n"
"v_mfma_f32_16x16x32_bf16 v[88:91], a[36:39], v[36:39], v[88:91]\n"
"v_add_f32_e32 v85, v85, v197\n"
"v_add_f32_e32 v86, v86, v198\n"
"v_mfma_f32_16x16x32_bf16 v[88:91], a[32:35], v[32:35], v[88:91]\n"
"buffer_load_dwordx4 a[4:7], v0, s[4:7], s98 offen offset:1024 sc1\n"
"v_mfma_f32_16x16x32_bf16 v[28:31], a[64:67], v[32:35], v[28:31]\n"
"v_add_f32_e32 v87, v87, v199\n"
"v_fmac_f32_e32 v52, v254, v140\n"
"v_mfma_f32_16x16x32_bf16 v[28:31], a[68:71], v[36:39], v[28:31]\n"
"v_fmac_f32_e32 v53, v254, v141\n"
"v_fmac_f32_e32 v54, v254, v142\n"
"v_mfma_f32_16x16x32_bf16 v[28:31], a[72:75], v[24:27], v[28:31]\n"
"buffer_load_dwordx4 a[8:11], v0, s[4:7], s98 offen offset:2048 sc1\n"
"v_mfma_f32_16x16x32_bf16 v[28:31], a[76:79], v[48:51], v[28:31]\n"
"v_fmac_f32_e32 v55, v254, v143\n"
"v_add_f32_e32 v52, v52, v172\n"
"v_mfma_f32_16x16x32_bf16 v[28:31], a[80:83], v[40:43], v[28:31]\n"
"s_waitcnt lgkmcnt(0)\n"
"v_add_f32_e32 v53, v53, v173\n"
"v_add_f32_e32 v54, v54, v174\n"
"v_mfma_f32_16x16x32_bf16 v[28:31], a[84:87], v[92:95], v[28:31]\n"
"buffer_load_dwordx4 a[12:15], v0, s[4:7], s98 offen offset:3072 sc1\n"
"v_mfma_f32_16x16x32_bf16 v[28:31], a[88:91], v[168:171], v[28:31]\n"
"ds_read_b128 v[76:79], v6 offset:2048\n"
"ds_read_b128 v[80:83], v6 offset:3072\n"
"v_mfma_f32_16x16x32_bf16 v[28:31], a[92:95], v[176:179], v[28:31]\n"
"v_add_f32_e32 v55, v55, v175\n"
"v_fmac_f32_e32 v44, v254, v60\n"
"v_mfma_f32_16x16x32_bf16 v[84:87], a[124:127], v[176:179], v[84:87]\n"
"buffer_load_dwordx4 a[16:19], v1, s[4:7], s98 offen sc1\n"
"v_mfma_f32_16x16x32_bf16 v[84:87], a[120:123], v[168:171], v[84:87]\n"
"v_fmac_f32_e32 v45, v254, v61\n"
"v_fmac_f32_e32 v46, v254, v62\n"
"v_mfma_f32_16x16x32_bf16 v[84:87], a[116:119], v[92:95], v[84:87]\n"
"v_fmac_f32_e32 v47, v254, v63\n"
"v_add_f32_e32 v44, v44, v64\n"
"v_mfma_f32_16x16x32_bf16 v[84:87], a[112:115], v[40:43], v[84:87]\n"
"buffer_load_dwordx4 a[20:23], v1, s[4:7], s98 offen offset:1024 sc1\n"
"v_mfma_f32_16x16x32_bf16 v[84:87], a[108:111], v[48:51], v[84:87]\n"
"v_add_f32_e32 v45, v45, v65\n"
"v_add_f32_e32 v46, v46, v66\n"
"v_mfma_f32_16x16x32_bf16 v[84:87], a[104:107], v[24:27], v[84:87]\n"
"v_add_f32_e32 v47, v47, v67\n"
"v_add_f32_e32 v56, v56, v68\n"
"v_mfma_f32_16x16x32_bf16 v[84:87], a[100:103], v[36:39], v[84:87]\n"
"buffer_load_dwordx4 a[24:27], v1, s[4:7], s98 offen offset:2048 sc1\n"
"v_mfma_f32_16x16x32_bf16 v[84:87], a[96:99], v[32:35], v[84:87]\n"
"v_add_f32_e32 v57, v57, v69\n"
"v_add_f32_e32 v58, v58, v70\n"
"v_mfma_f32_16x16x32_bf16 v[52:55], v[100:103], v[32:35], v[52:55]\n"
"v_add_f32_e32 v59, v59, v71\n"
"v_add_f32_e32 v88, v88, v72\n"
"v_mfma_f32_16x16x32_bf16 v[52:55], v[104:107], v[36:39], v[52:55]\n"
"buffer_load_dwordx4 a[28:31], v1, s[4:7], s98 offen offset:3072 sc1\n"
"v_mfma_f32_16x16x32_bf16 v[52:55], v[108:111], v[24:27], v[52:55]\n"
"v_add_f32_e32 v89, v89, v73\n"
"v_add_f32_e32 v90, v90, v74\n"
"v_mfma_f32_16x16x32_bf16 v[52:55], v[116:119], v[48:51], v[52:55]\n"
"v_add_f32_e32 v91, v91, v75\n"
"v_mul_f32_e32 v56, v56, v255\n"
"v_mfma_f32_16x16x32_bf16 v[52:55], v[128:131], v[40:43], v[52:55]\n"
"s_waitcnt lgkmcnt(0)\n"
"v_mul_f32_e32 v57, v57, v255\n"
"v_mul_f32_e32 v58, v58, v255\n"
"v_mfma_f32_16x16x32_bf16 v[52:55], v[132:135], v[92:95], v[52:55]\n"
"v_mul_f32_e32 v59, v59, v255\n"
"v_mul_f32_e32 v88, v88, v255\n"
"v_mfma_f32_16x16x32_bf16 v[52:55], v[136:139], v[168:171], v[52:55]\n"
"ds_read_b128 v[60:63], v6 offset:4096\n"
"ds_read_b128 v[64:67], v6 offset:5120\n"
"v_mfma_f32_16x16x32_bf16 v[52:55], v[96:99], v[176:179], v[52:55]\n"
"v_mul_f32_e32 v89, v89, v255\n"
"v_mul_f32_e32 v90, v90, v255\n"
"v_mfma_f32_16x16x32_bf16 v[44:47], v[112:115], v[176:179], v[44:47]\n"
"v_mul_f32_e32 v91, v91, v255\n"
"v_cvt_pk_bf16_f32 v68, v56, v57\n"
"v_mfma_f32_16x16x32_bf16 v[44:47], v[164:167], v[168:171], v[44:47]\n"
"v_cvt_pk_bf16_f32 v69, v58, v59\n"
"v_cvt_pk_bf16_f32 v70, v88, v89\n"
"v_mfma_f32_16x16x32_bf16 v[44:47], v[160:163], v[92:95], v[44:47]\n"
"v_cvt_pk_bf16_f32 v71, v90, v91\n"
"v_add_f32_e32 v28, v28, v76\n"
"v_mfma_f32_16x16x32_bf16 v[44:47], v[156:159], v[40:43], v[44:47]\n"
"ds_write_b128 v7, v[68:71] offset:16384\n"
"v_mfma_f32_16x16x32_bf16 v[44:47], v[124:127], v[48:51], v[44:47]\n"
"buffer_store_dwordx4 v[68:71], v7, s[4:7], s64 offen sc1\n"
"v_mfma_f32_16x16x32_bf16 v[44:47], v[148:151], v[24:27], v[44:47]\n"
"v_add_f32_e32 v29, v29, v77\n"
"v_add_f32_e32 v30, v30, v78\n"
"v_mfma_f32_16x16x32_bf16 v[44:47], v[144:147], v[36:39], v[44:47]\n"
"v_add_f32_e32 v31, v31, v79\n"
"v_add_f32_e32 v84, v84, v80\n"
"v_mfma_f32_16x16x32_bf16 v[44:47], v[120:123], v[32:35], v[44:47]\n"
"v_add_f32_e32 v85, v85, v81\n"
"v_add_f32_e32 v86, v86, v82\n"
"v_add_f32_e32 v87, v87, v83\n"
"v_mul_f32_e32 v28, v28, v255\n"
"v_mul_f32_e32 v29, v29, v255\n"
"v_mul_f32_e32 v30, v30, v255\n"
"v_mul_f32_e32 v31, v31, v255\n"
"v_mul_f32_e32 v84, v84, v255\n"
"v_mul_f32_e32 v85, v85, v255\n"
"v_mul_f32_e32 v86, v86, v255\n"
"v_mul_f32_e32 v87, v87, v255\n"
"v_cvt_pk_bf16_f32 v24, v28, v29\n"
"v_cvt_pk_bf16_f32 v25, v30, v31\n"
"v_cvt_pk_bf16_f32 v26, v84, v85\n"
"v_cvt_pk_bf16_f32 v27, v86, v87\n"
"v_xor_b32_e32 v255, 32, v16\n"
"v_xor_b32_e32 v254, 32, v15\n"
"v_mov_b32_e32 v88, 0x3f803f80\n"
"v_mov_b32_e32 v89, 0x3f803f80\n"
"v_mov_b32_e32 v90, 0x3f803f80\n"
"v_mov_b32_e32 v91, 0x3f803f80\n"
"v_mov_b32_e32 v252, 0x53800000\n"
"ds_write_b128 v7, v[24:27] offset:49152\n"
".Lmk_z4:\n"
"; @A tail\n"
"s_add_u32 s98, s64, 0x4000\n"
"buffer_store_dwordx4 v[24:27], v7, s[4:7], s98 offen sc1\n"
"s_waitcnt lgkmcnt(2)\n"
"v_add_f32_e32 v52, v52, v60\n"
"v_add_f32_e32 v53, v53, v61\n"
"v_add_f32_e32 v54, v54, v62\n"
"v_add_f32_e32 v55, v55, v63\n"
"v_add_f32_e32 v44, v44, v64\n"
"v_add_f32_e32 v45, v45, v65\n"
"v_add_f32_e32 v46, v46, v66\n"
"v_add_f32_e32 v47, v47, v67\n"
"v_mul_f32_e32 v52, v52, v253\n"
"v_mul_f32_e32 v53, v53, v253\n"
"v_mul_f32_e32 v54, v54, v253\n"
"v_mul_f32_e32 v55, v55, v253\n"
"v_mul_f32_e32 v44, v44, v253\n"
"v_mul_f32_e32 v45, v45, v253\n"
"v_mul_f32_e32 v46, v46, v253\n"
"v_mul_f32_e32 v47, v47, v253\n"
"v_cvt_pk_bf16_f32 v24, v52, v53\n"
"v_cvt_pk_bf16_f32 v25, v54, v55\n"
"v_cvt_pk_bf16_f32 v26, v44, v45\n"
"v_cvt_pk_bf16_f32 v27, v46, v47\n"
"ds_write_b128 v2, v[24:27] offset:16384\n"
"s_waitcnt lgkmcnt(0)\n"
"s_barrier\n"
"ds_read_b128 v[28:31], v2 offset:49152\n"
"ds_read_b128 v[32:35], v2 offset:50176\n"
"ds_read_b128 v[92:95], v2 offset:16384\n"
"ds_read_b128 v[96:99], v2 offset:17408\n"
"; @A AT b\n"
"s_mov_b32 s2, 0\n"
"v_mov_b32_e32 v253, 0xff800000\n"
"v_mov_b32_e32 v251, 0xff800000\n"
"v_mov_b32_e32 v250, 0x7f800000\n"
"v_mov_b32_e32 v249, 0x7f800000\n"
".Lmk_a15:\n"
"ds_read_b128 v[24:27], v15 offset:24576\n"
"ds_read_b128 v[36:39], v254 offset:24832\n"
"ds_read_b128 v[40:43], v15 offset:22528\n"
"ds_read_b128 v[44:47], v254 offset:22784\n"
"s_cmp_gt_i32 s78, 11\n"
"s_cselect_b32 s98, 0x80000000, 0\n"
"s_or_b32 s98, s98, 0x7f800000\n"
"s_waitcnt lgkmcnt(2)\n"
"v_mfma_f32_16x16x32_bf16 v[48:51], v[24:27], v[28:31], 0\n"
"v_mfma_f32_16x16x32_bf16 v[52:55], v[24:27], v[32:35], 0\n"
"v_mfma_f32_16x16x32_bf16 v[56:59], v[36:39], v[32:35], 0\n"
"v_mfma_f32_16x16x32_bf16 v[60:63], v[36:39], v[28:31], 0\n"
"s_waitcnt lgkmcnt(0)\n"
"s_nop 3\n"
"v_max_f32_e32 v248, v48, v49\n"
"v_max_f32_e32 v247, v52, v53\n"
"v_max3_f32 v248, v248, v50, v51\n"
"v_max3_f32 v247, v247, v54, v55\n"
"v_max3_f32 v248, v248, v60, v61\n"
"v_max3_f32 v247, v247, v56, v57\n"
"v_max3_f32 v248, v248, v62, v63\n"
"v_max3_f32 v247, v247, v58, v59\n"
"v_mov_b32_e32 v246, v248\n"
"v_mov_b32_e32 v245, v247\n"
"s_nop 0\n"
"v_permlane16_swap_b32_e32 v248, v246\n"
"v_permlane16_swap_b32_e32 v247, v245\n"
"v_max_f32_e32 v248, v248, v246\n"
"v_max_f32_e32 v247, v247, v245\n"
"v_mov_b32_e32 v246, v248\n"
"v_mov_b32_e32 v245, v247\n"
"s_nop 0\n"
"v_permlane32_swap_b32_e32 v248, v246\n"
"v_permlane32_swap_b32_e32 v247, v245\n"
"v_max_f32_e32 v248, v248, v246\n"
"v_max_f32_e32 v247, v247, v245\n"
"v_med3_f32 v248, v248, v253, v250\n"
"v_med3_f32 v247, v247, v251, v249\n"
"v_min_f32_e64 v24, s98, -v248\n"
"v_min_f32_e64 v25, s98, -v248\n"
"v_min_f32_e64 v26, s98, -v248\n"
"v_min_f32_e64 v27, s98, -v248\n"
"v_min_f32_e64 v36, s98, -v247\n"
"v_min_f32_e64 v37, s98, -v247\n"
"v_min_f32_e64 v38, s98, -v247\n"
"v_min_f32_e64 v39, s98, -v247\n"
"v_mfma_f32_16x16x32_bf16 v[64:67], v[40:43], v[28:31], v[24:27]\n"
"ds_read_b128 v[68:71], v15 offset:20480\n"
"s_add_u32 s98, s69, 0x602000\n"
"buffer_load_dwordx4 a[32:35], v0, s[4:7], s98 offen sc1\n"
"v_sub_f32_e32 v48, v48, v248\n"
"v_sub_f32_e32 v49, v49, v248\n"
"v_sub_f32_e32 v50, v50, v248\n"
"v_mfma_f32_16x16x32_bf16 v[72:75], v[40:43], v[32:35], v[36:39]\n"
"buffer_load_dwordx4 a[36:39], v0, s[4:7], s98 offen offset:1024 sc1\n"
"v_sub_f32_e32 v51, v51, v248\n"
"v_sub_f32_e32 v60, v60, v248\n"
"v_sub_f32_e32 v61, v61, v248\n"
"v_sub_f32_e32 v62, v62, v248\n"
"v_sub_f32_e32 v63, v63, v248\n"
"v_mfma_f32_16x16x32_bf16 v[40:43], v[44:47], v[32:35], v[36:39]\n"
"ds_read_b128 v[76:79], v254 offset:20736\n"
"v_sub_f32_e32 v52, v52, v247\n"
"v_sub_f32_e32 v53, v53, v247\n"
"v_sub_f32_e32 v54, v54, v247\n"
"v_sub_f32_e32 v55, v55, v247\n"
"v_mfma_f32_16x16x32_bf16 v[80:83], v[44:47], v[28:31], v[24:27]\n"
"buffer_load_dwordx4 a[40:43], v0, s[4:7], s98 offen offset:2048 sc1\n"
"v_sub_f32_e32 v56, v56, v247\n"
"v_sub_f32_e32 v57, v57, v247\n"
"v_sub_f32_e32 v58, v58, v247\n"
"v_sub_f32_e32 v59, v59, v247\n"
"s_waitcnt lgkmcnt(0)\n"
"v_mfma_f32_16x16x32_bf16 v[44:47], v[68:71], v[28:31], v[24:27]\n"
"ds_read_b128 v[84:87], v15 offset:18432\n"
"buffer_load_dwordx4 a[44:47], v0, s[4:7], s98 offen offset:3072 sc1\n"
"ds_read_b128 v[100:103], v254 offset:18688\n"
"v_exp_f32_e32 v64, v64\n"
"v_exp_f32_e32 v65, v65\n"
"v_exp_f32_e32 v66, v66\n"
"v_exp_f32_e32 v67, v67\n"
"v_exp_f32_e32 v80, v80\n"
"v_exp_f32_e32 v81, v81\n"
"v_mfma_f32_16x16x32_bf16 v[104:107], v[68:71], v[32:35], v[36:39]\n"
"buffer_load_dwordx4 a[48:51], v1, s[4:7], s98 offen sc1\n"
"ds_read_b64_tr_b16 v[68:69], v16 offset:22528\n"
"buffer_load_dwordx4 a[52:55], v1, s[4:7], s98 offen offset:1024 sc1\n"
"ds_read_b64_tr_b16 v[70:71], v255 offset:22784\n"
"v_exp_f32_e32 v82, v82\n"
"v_exp_f32_e32 v83, v83\n"
"v_cvt_pk_bf16_f32 v108, v64, v65\n"
"v_cvt_pk_bf16_f32 v109, v66, v67\n"
"v_cvt_pk_bf16_f32 v110, v80, v81\n"
"v_cvt_pk_bf16_f32 v111, v82, v83\n"
"v_exp_f32_e32 v72, v72\n"
"v_mfma_f32_16x16x32_bf16 v[64:67], v[76:79], v[32:35], v[36:39]\n"
"s_add_u32 s97, s76, 0x80000\n"
"v_exp_f32_e32 v73, v73\n"
"v_exp_f32_e32 v74, v74\n"
"v_exp_f32_e32 v75, v75\n"
"v_bfe_u32 v248, v0, 2, 7\n"
"v_add_u32_e32 v248, s72, v248\n"
"v_and_b32_e32 v247, 0xffffffc0, v248\n"
"v_add_u32_e32 v248, v248, v247\n"
"v_and_b32_e32 v247, 0x200, v0\n"
"v_lshl_add_u32 v248, v247, 4, v248\n"
"v_mfma_f32_16x16x32_bf16 v[112:115], v[76:79], v[28:31], v[24:27]\n"
"buffer_load_dword v247, v248, s[4:7], s97 offen sc1\n"
"ds_read_b64_tr_b16 v[80:81], v255 offset:22528\n"
"buffer_load_dword v246, v248, s[4:7], s97 offen offset:1024 sc1\n"
"ds_read_b64_tr_b16 v[82:83], v16 offset:22784\n"
"v_exp_f32_e32 v40, v40\n"
"v_exp_f32_e32 v41, v41\n"
"v_exp_f32_e32 v42, v42\n"
"v_exp_f32_e32 v43, v43\n"
"v_cvt_pk_bf16_f32 v76, v72, v73\n"
"v_cvt_pk_bf16_f32 v77, v74, v75\n"
"v_cvt_pk_bf16_f32 v78, v40, v41\n"
"v_cvt_pk_bf16_f32 v79, v42, v43\n"
"s_waitcnt lgkmcnt(4)\n"
"v_mfma_f32_16x16x32_bf16 v[40:43], v[84:87], v[28:31], v[24:27]\n"
"ds_read_b128 v[72:75], v15 offset:16384\n"
"v_exp_f32_e32 v44, v44\n"
"v_exp_f32_e32 v45, v45\n"
"v_exp_f32_e32 v46, v46\n"
"v_mfma_f32_16x16x32_bf16 v[116:119], v[84:87], v[32:35], v[36:39]\n"
"buffer_load_dwordx4 a[56:59], v1, s[4:7], s98 offen offset:2048 sc1\n"
"ds_read_b128 v[84:87], v254 offset:16640\n"
"v_exp_f32_e32 v47, v47\n"
"v_exp_f32_e32 v112, v112\n"
"v_mfma_f32_16x16x32_bf16 v[120:123], v[100:103], v[32:35], v[36:39]\n"
"ds_read_b64_tr_b16 v[124:125], v16 offset:20480\n"
"v_exp_f32_e32 v113, v113\n"
"v_exp_f32_e32 v114, v114\n"
"v_mfma_f32_16x16x32_bf16 v[128:131], v[100:103], v[28:31], v[24:27]\n"
"buffer_load_dwordx4 a[60:63], v1, s[4:7], s98 offen offset:3072 sc1\n"
"v_exp_f32_e32 v115, v115\n"
"v_cvt_pk_bf16_f32 v100, v44, v45\n"
"v_cvt_pk_bf16_f32 v101, v46, v47\n"
"s_waitcnt lgkmcnt(3)\n"
"v_mfma_f32_16x16x32_bf16 v[44:47], v[88:91], v[108:111], 0\n"
"ds_read_b64_tr_b16 v[126:127], v255 offset:20736\n"
"s_add_u32 s98, s69, 0x604000\n"
"buffer_load_dwordx4 a[64:67], v0, s[4:7], s98 offen sc1\n"
"v_cvt_pk_bf16_f32 v102, v112, v113\n"
"v_cvt_pk_bf16_f32 v103, v114, v115\n"
"v_exp_f32_e32 v104, v104\n"
"v_mfma_f32_16x16x32_bf16 v[112:115], v[68:71], v[108:111], 0\n"
"ds_read_b64_tr_b16 v[132:133], v255 offset:20480\n"
"v_exp_f32_e32 v105, v105\n"
"v_exp_f32_e32 v106, v106\n"
"v_mfma_f32_16x16x32_bf16 v[136:139], v[80:83], v[108:111], 0\n"
"buffer_load_dword v245, v248, s[4:7], s97 offen offset:2048 sc1\n"
"v_exp_f32_e32 v107, v107\n"
"v_exp_f32_e32 v64, v64\n"
"v_mfma_f32_16x16x32_bf16 v[108:111], v[80:83], v[76:79], 0\n"
"ds_read_b64_tr_b16 v[134:135], v16 offset:20736\n"
"v_exp_f32_e32 v65, v65\n"
"v_exp_f32_e32 v66, v66\n"
"v_mfma_f32_16x16x32_bf16 v[80:83], v[68:71], v[76:79], 0\n"
"buffer_load_dword v244, v248, s[4:7], s97 offen offset:3072 sc1\n"
"s_waitcnt vmcnt(13)\n"
"buffer_store_dwordx2 v[18:19], v11, s[4:7], s68 offen offset:64 sc1\n"
"v_exp_f32_e32 v67, v67\n"
"v_mfma_f32_16x16x32_bf16 v[68:71], v[88:91], v[76:79], 0\n"
"s_cmp_lg_u32 s2, 0\n"
"v_cvt_pk_bf16_f32 v76, v104, v105\n"
"v_cvt_pk_bf16_f32 v77, v106, v107\n"
"v_cvt_pk_bf16_f32 v78, v64, v65\n"
"v_cvt_pk_bf16_f32 v79, v66, v67\n"
"s_cbranch_scc1 .Lmk_s17\n"
"s_barrier\n"
".Lmk_s17:\n"
"s_waitcnt lgkmcnt(4)\n"
"v_mfma_f32_16x16x32_bf16 v[64:67], v[72:75], v[28:31], v[24:27]\n"
"ds_read_b128 v[104:107], v15 offset:14336\n"
"v_exp_f32_e32 v40, v40\n"
"v_exp_f32_e32 v41, v41\n"
"v_exp_f32_e32 v42, v42\n"
"v_mfma_f32_16x16x32_bf16 v[140:143], v[72:75], v[32:35], v[36:39]\n"
"buffer_load_dwordx4 a[68:71], v0, s[4:7], s98 offen offset:1024 sc1\n"
"ds_read_b128 v[72:75], v254 offset:14592\n"
"v_exp_f32_e32 v43, v43\n"
"v_mfma_f32_16x16x32_bf16 v[144:147], v[84:87], v[32:35], v[36:39]\n"
"v_exp_f32_e32 v128, v128\n"
"v_exp_f32_e32 v129, v129\n"
"v_exp_f32_e32 v130, v130\n"
"v_mfma_f32_16x16x32_bf16 v[148:151], v[84:87], v[28:31], v[24:27]\n"
"buffer_load_dwordx4 a[72:75], v0, s[4:7], s98 offen offset:2048 sc1\n"
"ds_read_b64_tr_b16 v[84:85], v16 offset:18432\n"
"v_exp_f32_e32 v131, v131\n"
"s_waitcnt lgkmcnt(3)\n"
"v_mfma_f32_16x16x32_bf16 v[44:47], v[88:91], v[100:103], v[44:47]\n"
"buffer_load_dwordx4 a[76:79], v0, s[4:7], s98 offen offset:3072 sc1\n"
"v_cvt_pk_bf16_f32 v152, v40, v41\n"
"v_cvt_pk_bf16_f32 v153, v42, v43\n"
"v_cvt_pk_bf16_f32 v154, v128, v129\n"
"v_cvt_pk_bf16_f32 v155, v130, v131\n"
"v_mfma_f32_16x16x32_bf16 v[112:115], v[124:127], v[100:103], v[112:115]\n"
"ds_read_b64_tr_b16 v[86:87], v255 offset:18688\n"
"v_exp_f32_e32 v116, v116\n"
"v_exp_f32_e32 v117, v117\n"
"v_exp_f32_e32 v118, v118\n"
"v_mfma_f32_16x16x32_bf16 v[136:139], v[132:135], v[100:103], v[136:139]\n"
"s_add_u32 s97, s76, 0x81000\n"
"buffer_load_dword v243, v248, s[4:7], s97 offen sc1\n"
"v_exp_f32_e32 v119, v119\n"
"v_mfma_f32_16x16x32_bf16 v[108:111], v[132:135], v[76:79], v[108:111]\n"
"ds_read_b64_tr_b16 v[40:41], v255 offset:18432\n"
"v_exp_f32_e32 v120, v120\n"
"v_exp_f32_e32 v121, v121\n"
"v_mfma_f32_16x16x32_bf16 v[80:83], v[124:127], v[76:79], v[80:83]\n"
"buffer_load_dword v242, v248, s[4:7], s97 offen offset:1024 sc1\n"
"ds_read_b64_tr_b16 v[42:43], v16 offset:18688\n"
"v_exp_f32_e32 v122, v122\n"
"v_exp_f32_e32 v123, v123\n"
"v_mfma_f32_16x16x32_bf16 v[68:71], v[88:91], v[76:79], v[68:71]\n"
"s_waitcnt lgkmcnt(4)\n"
"v_cvt_pk_bf16_f32 v76, v116, v117\n"
"v_cvt_pk_bf16_f32 v77, v118, v119\n"
"v_cvt_pk_bf16_f32 v78, v120, v121\n"
"v_cvt_pk_bf16_f32 v79, v122, v123\n"
"s_cmp_eq_i32 s78, 8\n"
"s_cbranch_scc1 .Lmk_x18s\n"
".Lmk_x18r:\n"
"v_mfma_f32_16x16x32_bf16 v[100:103], v[104:107], v[28:31], v[24:27]\n"
"ds_read_b128 v[116:119], v15 offset:12288\n"
"v_exp_f32_e32 v64, v64\n"
"v_exp_f32_e32 v65, v65\n"
"v_exp_f32_e32 v66, v66\n"
"v_mfma_f32_16x16x32_bf16 v[120:123], v[104:107], v[32:35], v[36:39]\n"
"buffer_load_dwordx4 a[80:83], v1, s[4:7], s98 offen sc1\n"
"ds_read_b128 v[104:107], v254 offset:12544\n"
"v_exp_f32_e32 v67, v67\n"
"v_mfma_f32_16x16x32_bf16 v[124:127], v[72:75], v[32:35], v[36:39]\n"
"v_exp_f32_e32 v148, v148\n"
"v_exp_f32_e32 v149, v149\n"
"v_exp_f32_e32 v150, v150\n"
"v_mfma_f32_16x16x32_bf16 v[128:131], v[72:75], v[28:31], v[24:27]\n"
"buffer_load_dwordx4 a[84:87], v1, s[4:7], s98 offen offset:1024 sc1\n"
"ds_read_b64_tr_b16 v[72:73], v16 offset:16384\n"
"v_exp_f32_e32 v151, v151\n"
"s_waitcnt lgkmcnt(3)\n"
"v_mfma_f32_16x16x32_bf16 v[44:47], v[88:91], v[152:155], v[44:47]\n"
"buffer_load_dwordx4 a[88:91], v1, s[4:7], s98 offen offset:2048 sc1\n"
"v_cvt_pk_bf16_f32 v132, v64, v65\n"
"v_cvt_pk_bf16_f32 v133, v66, v67\n"
"v_cvt_pk_bf16_f32 v134, v148, v149\n"
"v_cvt_pk_bf16_f32 v135, v150, v151\n"
"v_mfma_f32_16x16x32_bf16 v[112:115], v[84:87], v[152:155], v[112:115]\n"
"ds_read_b64_tr_b16 v[74:75], v255 offset:16640\n"
"v_exp_f32_e32 v140, v140\n"
"v_exp_f32_e32 v141, v141\n"
"v_mfma_f32_16x16x32_bf16 v[136:139], v[40:43], v[152:155], v[136:139]\n"
"buffer_load_dword v241, v248, s[4:7], s97 offen offset:2048 sc1\n"
"v_exp_f32_e32 v142, v142\n"
"v_exp_f32_e32 v143, v143\n"
"v_mfma_f32_16x16x32_bf16 v[108:111], v[40:43], v[76:79], v[108:111]\n"
"ds_read_b64_tr_b16 v[40:41], v255 offset:16384\n"
"v_exp_f32_e32 v144, v144\n"
"v_exp_f32_e32 v145, v145\n"
"v_mfma_f32_16x16x32_bf16 v[80:83], v[84:87], v[76:79], v[80:83]\n"
"buffer_load_dword v240, v248, s[4:7], s97 offen offset:3072 sc1\n"
"ds_read_b64_tr_b16 v[42:43], v16 offset:16640\n"
"v_exp_f32_e32 v146, v146\n"
"v_exp_f32_e32 v147, v147\n"
"v_mfma_f32_16x16x32_bf16 v[68:71], v[88:91], v[76:79], v[68:71]\n"
"s_waitcnt lgkmcnt(4)\n"
"v_cvt_pk_bf16_f32 v64, v140, v141\n"
"v_cvt_pk_bf16_f32 v65, v142, v143\n"
"v_cvt_pk_bf16_f32 v66, v144, v145\n"
"v_cvt_pk_bf16_f32 v67, v146, v147\n"
"s_cmp_eq_i32 s78, 7\n"
"s_cbranch_scc1 .Lmk_x19s\n"
".Lmk_x19r:\n"
"v_mfma_f32_16x16x32_bf16 v[76:79], v[116:119], v[28:31], v[24:27]\n"
"ds_read_b128 v[84:87], v15 offset:10240\n"
"v_exp_f32_e32 v100, v100\n"
"v_exp_f32_e32 v101, v101\n"
"v_exp_f32_e32 v102, v102\n"
"v_mfma_f32_16x16x32_bf16 v[140:143], v[116:119], v[32:35], v[36:39]\n"
"buffer_load_dwordx4 a[92:95], v1, s[4:7], s98 offen offset:3072 sc1\n"
"v_exp_f32_e32 v103, v103\n"
"v_mfma_f32_16x16x32_bf16 v[116:119], v[104:107], v[32:35], v[36:39]\n"
"ds_read_b128 v[144:147], v254 offset:10496\n"
"v_exp_f32_e32 v128, v128\n"
"v_exp_f32_e32 v129, v129\n"
"v_mfma_f32_16x16x32_bf16 v[148:151], v[104:107], v[28:31], v[24:27]\n"
"ds_read_b64_tr_b16 v[104:105], v16 offset:14336\n"
"v_exp_f32_e32 v130, v130\n"
"v_exp_f32_e32 v131, v131\n"
"v_cvt_pk_bf16_f32 v152, v100, v101\n"
"s_waitcnt lgkmcnt(3)\n"
"v_mfma_f32_16x16x32_bf16 v[44:47], v[88:91], v[132:135], v[44:47]\n"
"s_add_u32 s98, s69, 0x606000\n"
"buffer_load_dwordx4 a[96:99], v0, s[4:7], s98 offen sc1\n"
"v_cvt_pk_bf16_f32 v153, v102, v103\n"
"v_cvt_pk_bf16_f32 v154, v128, v129\n"
"v_mfma_f32_16x16x32_bf16 v[112:115], v[72:75], v[132:135], v[112:115]\n"
"ds_read_b64_tr_b16 v[106:107], v255 offset:14592\n"
"v_cvt_pk_bf16_f32 v155, v130, v131\n"
"v_exp_f32_e32 v120, v120\n"
"v_exp_f32_e32 v121, v121\n"
"v_mfma_f32_16x16x32_bf16 v[136:139], v[40:43], v[132:135], v[136:139]\n"
"ds_read_b64_tr_b16 v[100:101], v255 offset:14336\n"
"v_exp_f32_e32 v122, v122\n"
"v_exp_f32_e32 v123, v123\n"
"v_mfma_f32_16x16x32_bf16 v[108:111], v[40:43], v[64:67], v[108:111]\n"
"buffer_load_dwordx4 a[100:103], v0, s[4:7], s98 offen offset:1024 sc1\n"
"v_exp_f32_e32 v124, v124\n"
"v_exp_f32_e32 v125, v125\n"
"v_mfma_f32_16x16x32_bf16 v[80:83], v[72:75], v[64:67], v[80:83]\n"
"ds_read_b64_tr_b16 v[102:103], v16 offset:14592\n"
"v_exp_f32_e32 v126, v126\n"
"v_exp_f32_e32 v127, v127\n"
"v_mfma_f32_16x16x32_bf16 v[68:71], v[88:91], v[64:67], v[68:71]\n"
"s_waitcnt lgkmcnt(4)\n"
"v_cvt_pk_bf16_f32 v40, v120, v121\n"
"v_cvt_pk_bf16_f32 v41, v122, v123\n"
"v_cvt_pk_bf16_f32 v42, v124, v125\n"
"v_cvt_pk_bf16_f32 v43, v126, v127\n"
"s_cmp_eq_i32 s78, 6\n"
"s_cbranch_scc1 .Lmk_x20s\n"
".Lmk_x20r:\n"
"v_mfma_f32_16x16x32_bf16 v[64:67], v[84:87], v[28:31], v[24:27]\n"
"ds_read_b128 v[72:75], v15 offset:8192\n"
"v_exp_f32_e32 v76, v76\n"
"v_exp_f32_e32 v77, v77\n"
"v_exp_f32_e32 v78, v78\n"
"v_mfma_f32_16x16x32_bf16 v[120:123], v[84:87], v[32:35], v[36:39]\n"
"buffer_load_dwordx4 a[104:107], v0, s[4:7], s98 offen offset:2048 sc1\n"
"v_exp_f32_e32 v79, v79\n"
"v_mfma_f32_16x16x32_bf16 v[84:87], v[144:147], v[32:35], v[36:39]\n"
"ds_read_b128 v[124:127], v254 offset:8448\n"
"v_exp_f32_e32 v148, v148\n"
"v_exp_f32_e32 v149, v149\n"
"v_mfma_f32_16x16x32_bf16 v[128:131], v[144:147], v[28:31], v[24:27]\n"
"ds_read_b64_tr_b16 v[132:133], v16 offset:12288\n"
"v_exp_f32_e32 v150, v150\n"
"v_exp_f32_e32 v151, v151\n"
"s_waitcnt lgkmcnt(3)\n"
"v_mfma_f32_16x16x32_bf16 v[44:47], v[88:91], v[152:155], v[44:47]\n"
"buffer_load_dwordx4 a[108:111], v0, s[4:7], s98 offen offset:3072 sc1\n"
"v_cvt_pk_bf16_f32 v144, v76, v77\n"
"v_cvt_pk_bf16_f32 v145, v78, v79\n"
"v_cvt_pk_bf16_f32 v146, v148, v149\n"
"v_mfma_f32_16x16x32_bf16 v[112:115], v[104:107], v[152:155], v[112:115]\n"
"ds_read_b64_tr_b16 v[134:135], v255 offset:12544\n"
"v_cvt_pk_bf16_f32 v147, v150, v151\n"
"v_exp_f32_e32 v140, v140\n"
"v_exp_f32_e32 v141, v141\n"
"v_mfma_f32_16x16x32_bf16 v[136:139], v[100:103], v[152:155], v[136:139]\n"
"ds_read_b64_tr_b16 v[76:77], v255 offset:12288\n"
"v_exp_f32_e32 v142, v142\n"
"v_exp_f32_e32 v143, v143\n"
"v_mfma_f32_16x16x32_bf16 v[108:111], v[100:103], v[40:43], v[108:111]\n"
"buffer_load_dwordx4 a[112:115], v1, s[4:7], s98 offen sc1\n"
"v_exp_f32_e32 v116, v116\n"
"v_exp_f32_e32 v117, v117\n"
"v_mfma_f32_16x16x32_bf16 v[80:83], v[104:107], v[40:43], v[80:83]\n"
"ds_read_b64_tr_b16 v[78:79], v16 offset:12544\n"
"v_exp_f32_e32 v118, v118\n"
"v_exp_f32_e32 v119, v119\n"
"v_mfma_f32_16x16x32_bf16 v[68:71], v[88:91], v[40:43], v[68:71]\n"
"s_waitcnt lgkmcnt(4)\n"
"v_cvt_pk_bf16_f32 v40, v140, v141\n"
"v_cvt_pk_bf16_f32 v41, v142, v143\n"
"v_cvt_pk_bf16_f32 v42, v116, v117\n"
"v_cvt_pk_bf16_f32 v43, v118, v119\n"
"s_cmp_eq_i32 s78, 5\n"
"s_cbranch_scc1 .Lmk_x21s\n"
".Lmk_x21r:\n"
"v_mfma_f32_16x16x32_bf16 v[100:103], v[72:75], v[28:31], v[24:27]\n"
"ds_read_b64_tr_b16 v[104:105], v16 offset:10240\n"
"ds_read_b64_tr_b16 v[106:107], v255 offset:10496\n"
"v_exp_f32_e32 v64, v64\n"
"v_exp_f32_e32 v65, v65\n"
"v_mfma_f32_16x16x32_bf16 v[116:119], v[72:75], v[32:35], v[36:39]\n"
"s_add_u32 s97, s71, 0xfffff000\n"
"buffer_load_dwordx2 v[238:239], off, s[4:7], s97 sc1\n"
"v_exp_f32_e32 v66, v66\n"
"v_exp_f32_e32 v67, v67\n"
"v_mfma_f32_16x16x32_bf16 v[72:75], v[124:127], v[32:35], v[36:39]\n"
"ds_read_b64_tr_b16 v[36:37], v255 offset:10240\n"
"v_exp_f32_e32 v128, v128\n"
"v_exp_f32_e32 v129, v129\n"
"v_mfma_f32_16x16x32_bf16 v[140:143], v[124:127], v[28:31], v[24:27]\n"
"ds_read_b64_tr_b16 v[38:39], v16 offset:10496\n"
"buffer_load_dwordx4 a[116:119], v1, s[4:7], s98 offen offset:1024 sc1\n"
"v_exp_f32_e32 v130, v130\n"
"v_exp_f32_e32 v131, v131\n"
"s_waitcnt lgkmcnt(4)\n"
"v_mfma_f32_16x16x32_bf16 v[44:47], v[88:91], v[144:147], v[44:47]\n"
"ds_read_b64_tr_b16 v[124:125], v16 offset:24576\n"
"ds_read_b64_tr_b16 v[126:127], v255 offset:24832\n"
"v_cvt_pk_bf16_f32 v148, v64, v65\n"
"v_cvt_pk_bf16_f32 v149, v66, v67\n"
"v_cvt_pk_bf16_f32 v150, v128, v129\n"
"v_mfma_f32_16x16x32_bf16 v[112:115], v[132:135], v[144:147], v[112:115]\n"
"ds_read_b64_tr_b16 v[64:65], v255 offset:24576\n"
"buffer_load_dwordx4 a[120:123], v1, s[4:7], s98 offen offset:2048 sc1\n"
"v_cvt_pk_bf16_f32 v151, v130, v131\n"
"v_exp_f32_e32 v120, v120\n"
"v_mfma_f32_16x16x32_bf16 v[136:139], v[76:79], v[144:147], v[136:139]\n"
"ds_read_b64_tr_b16 v[66:67], v16 offset:24832\n"
"v_exp_f32_e32 v121, v121\n"
"v_exp_f32_e32 v122, v122\n"
"v_exp_f32_e32 v123, v123\n"
"v_mfma_f32_16x16x32_bf16 v[108:111], v[76:79], v[40:43], v[108:111]\n"
"v_exp_f32_e32 v84, v84\n"
"v_add_u32_e32 v248, 0x26400, v0\n"
"v_exp_f32_e32 v85, v85\n"
"v_mfma_f32_16x16x32_bf16 v[80:83], v[132:135], v[40:43], v[80:83]\n"
"buffer_load_dwordx4 a[124:127], v1, s[4:7], s98 offen offset:3072 sc1\n"
"ds_read_b128 v[76:79], v248\n"
"v_exp_f32_e32 v86, v86\n"
"v_exp_f32_e32 v87, v87\n"
"v_mfma_f32_16x16x32_bf16 v[68:71], v[88:91], v[40:43], v[68:71]\n"
"ds_read_b128 v[128:131], v248 offset:1024\n"
"v_cvt_pk_bf16_f32 v40, v120, v121\n"
"v_cvt_pk_bf16_f32 v41, v122, v123\n"
"v_cvt_pk_bf16_f32 v42, v84, v85\n"
"v_cvt_pk_bf16_f32 v43, v86, v87\n"
"s_waitcnt lgkmcnt(6)\n"
"v_mfma_f32_16x16x32_bf16 v[44:47], v[88:91], v[148:151], v[44:47]\n"
"ds_read_b64_tr_b16 v[84:85], v16 offset:8192\n"
"s_add_u32 s98, s69, 0x608000\n"
"buffer_load_dwordx4 v[24:27], v0, s[4:7], s98 offen sc1\n"
"ds_read_b64_tr_b16 v[86:87], v255 offset:8448\n"
"v_cndmask_b32_e64 v100, v48, v100, s[20:21]\n"
"v_cndmask_b32_e64 v101, v49, v101, s[22:23]\n"
"v_cndmask_b32_e64 v102, v50, v102, s[24:25]\n"
"v_cndmask_b32_e64 v103, v51, v103, s[26:27]\n"
"v_cndmask_b32_e64 v140, v60, v140, s[28:29]\n"
"v_cndmask_b32_e64 v141, v61, v141, s[30:31]\n"
"v_cndmask_b32_e64 v142, v62, v142, s[32:33]\n"
"v_mfma_f32_16x16x32_bf16 v[112:115], v[104:107], v[148:151], v[112:115]\n"
"ds_read_b64_tr_b16 v[48:49], v255 offset:8192\n"
"ds_read_b64_tr_b16 v[50:51], v16 offset:8448\n"
"v_cndmask_b32_e64 v143, v63, v143, s[34:35]\n"
"v_exp_f32_e32 v100, v100\n"
"v_exp_f32_e32 v101, v101\n"
"v_exp_f32_e32 v102, v102\n"
"s_waitcnt vmcnt(11)\n"
"v_exp_f32_e32 v103, v103\n"
"v_exp_f32_e32 v140, v140\n"
"v_mfma_f32_16x16x32_bf16 v[136:139], v[36:39], v[148:151], v[136:139]\n"
"v_add_f32_e32 v247, v247, v246\n"
"v_exp_f32_e32 v141, v141\n"
"v_exp_f32_e32 v142, v142\n"
"v_add_f32_e32 v247, v247, v245\n"
"v_exp_f32_e32 v143, v143\n"
"v_cvt_pk_bf16_f32 v60, v100, v101\n"
"v_add_f32_e32 v247, v247, v244\n"
"v_cvt_pk_bf16_f32 v61, v102, v103\n"
"v_cvt_pk_bf16_f32 v62, v140, v141\n"
"v_mfma_f32_16x16x32_bf16 v[108:111], v[36:39], v[40:43], v[108:111]\n"
"v_cvt_pk_bf16_f32 v63, v142, v143\n"
"v_add_f32_e32 v247, v247, v243\n"
"v_cndmask_b32_e64 v116, v52, v116, s[36:37]\n"
"v_cndmask_b32_e64 v117, v53, v117, s[38:39]\n"
"v_cndmask_b32_e64 v118, v54, v118, s[40:41]\n"
"v_cndmask_b32_e64 v119, v55, v119, s[42:43]\n"
"v_add_f32_e32 v247, v247, v242\n"
"v_cndmask_b32_e64 v72, v56, v72, s[44:45]\n"
"v_cndmask_b32_e64 v73, v57, v73, s[46:47]\n"
"v_cndmask_b32_e64 v74, v58, v74, s[48:49]\n"
"v_add_f32_e32 v247, v247, v241\n"
"v_cndmask_b32_e64 v75, v59, v75, s[50:51]\n"
"v_exp_f32_e32 v116, v116\n"
"v_mfma_f32_16x16x32_bf16 v[80:83], v[104:107], v[40:43], v[80:83]\n"
"v_exp_f32_e32 v117, v117\n"
"v_add_f32_e32 v247, v247, v240\n"
"v_exp_f32_e32 v118, v118\n"
"v_mov_b32_e32 v246, v247\n"
"v_exp_f32_e32 v119, v119\n"
"v_exp_f32_e32 v72, v72\n"
"v_permlane32_swap_b32_e32 v247, v246\n"
"v_exp_f32_e32 v73, v73\n"
"v_mfma_f32_16x16x32_bf16 v[68:71], v[88:91], v[40:43], v[68:71]\n"
"v_exp_f32_e32 v74, v74\n"
"v_add_f32_e32 v247, v247, v246\n"
"v_exp_f32_e32 v75, v75\n"
"v_cvt_pk_bf16_f32 v36, v116, v117\n"
"ds_write_b32 v21, v247 offset:512\n"
"v_cvt_pk_bf16_f32 v37, v118, v119\n"
"v_cvt_pk_bf16_f32 v38, v72, v73\n"
"v_cvt_pk_bf16_f32 v39, v74, v75\n"
"s_waitcnt lgkmcnt(1)\n"
"v_mfma_f32_16x16x32_bf16 v[44:47], v[88:91], v[60:63], v[44:47]\n"
"v_and_b32_e32 v76, v60, v76\n"
"v_and_b32_e32 v77, v61, v77\n"
"v_and_b32_e32 v78, v62, v78\n"
"v_and_b32_e32 v79, v63, v79\n"
"v_mfma_f32_16x16x32_bf16 v[68:71], v[88:91], v[36:39], v[68:71]\n"
"v_xor_b32_e32 v40, v60, v76\n"
"v_xor_b32_e32 v41, v61, v77\n"
"v_xor_b32_e32 v42, v62, v78\n"
"v_xor_b32_e32 v43, v63, v79\n"
"v_mfma_f32_16x16x32_bf16 v[112:115], v[84:87], v[76:79], v[112:115]\n"
"v_and_b32_e32 v128, v36, v128\n"
"v_and_b32_e32 v129, v37, v129\n"
"v_mfma_f32_16x16x32_bf16 v[136:139], v[48:51], v[76:79], v[136:139]\n"
"v_and_b32_e32 v130, v38, v130\n"
"v_and_b32_e32 v131, v39, v131\n"
"v_mfma_f32_16x16x32_bf16 v[112:115], v[124:127], v[40:43], v[112:115]\n"
"v_xor_b32_e32 v52, v36, v128\n"
"v_xor_b32_e32 v53, v37, v129\n"
"v_mfma_f32_16x16x32_bf16 v[136:139], v[64:67], v[40:43], v[136:139]\n"
"v_xor_b32_e32 v54, v38, v130\n"
"v_xor_b32_e32 v55, v39, v131\n"
"v_mfma_f32_16x16x32_bf16 v[80:83], v[84:87], v[128:131], v[80:83]\n"
"s_waitcnt vmcnt(4)\n"
"v_cmp_ne_u64_e32 vcc, v[238:239], v[18:19]\n"
"s_nop 0\n"
"s_cbranch_vccnz .Lmk_f14s\n"
".Lmk_f14r:\n"
"v_mfma_f32_16x16x32_bf16 v[108:111], v[48:51], v[128:131], v[108:111]\n"
"s_add_u32 m0, s77, 0x4000\n"
"s_add_u32 s97, s65, 0\n"
"buffer_load_dwordx4 v2, s[4:7], s97 offen sc1 lds\n"
"buffer_load_dwordx4 v2, s[4:7], s97 offen offset:1024 sc1 lds\n"
"v_mfma_f32_16x16x32_bf16 v[80:83], v[124:127], v[52:55], v[80:83]\n"
"s_add_u32 m0, s77, 0xc000\n"
"s_add_u32 s97, s65, 0x4000\n"
"buffer_load_dwordx4 v2, s[4:7], s97 offen sc1 lds\n"
"buffer_load_dwordx4 v2, s[4:7], s97 offen offset:1024 sc1 lds\n"
"v_mfma_f32_16x16x32_bf16 v[108:111], v[64:67], v[52:55], v[108:111]\n"
"v_max_f32_e32 v248, v44, v68\n"
"v_rcp_f32_e32 v44, v44\n"
"v_rcp_f32_e32 v68, v68\n"
"s_nop 0\n"
"v_max3_f32 v248, v248, v44, v68\n"
"v_cmp_nlt_f32_e32 vcc, v248, v252\n"
"s_cbranch_vccnz .Lmk_b16\n"
".Lmk_k22:\n"
"s_add_u32 s97, s60, 0x40000\n"
"v_mul_f32_e32 v112, v112, v44\n"
"v_mul_f32_e32 v113, v113, v44\n"
"v_mul_f32_e32 v114, v114, v44\n"
"v_mul_f32_e32 v115, v115, v44\n"
"v_mul_f32_e32 v136, v136, v44\n"
"v_mul_f32_e32 v137, v137, v44\n"
"v_mul_f32_e32 v138, v138, v44\n"
"v_mul_f32_e32 v139, v139, v44\n"
"v_cvt_pk_bf16_f32 v28, v112, v113\n"
"v_cvt_pk_bf16_f32 v29, v114, v115\n"
"v_cvt_pk_bf16_f32 v30, v136, v137\n"
"v_cvt_pk_bf16_f32 v31, v138, v139\n"
"buffer_store_dwordx4 v[28:31], v0, s[4:7], s97 offen sc1\n"
"s_add_u32 s97, s60, 0x48000\n"
"v_mul_f32_e32 v80, v80, v68\n"
"v_mul_f32_e32 v81, v81, v68\n"
"v_mul_f32_e32 v82, v82, v68\n"
"v_mul_f32_e32 v83, v83, v68\n"
"v_mul_f32_e32 v108, v108, v68\n"
"v_mul_f32_e32 v109, v109, v68\n"
"v_mul_f32_e32 v110, v110, v68\n"
"v_mul_f32_e32 v111, v111, v68\n"
"v_cvt_pk_bf16_f32 v28, v80, v81\n"
"v_cvt_pk_bf16_f32 v29, v82, v83\n"
"v_cvt_pk_bf16_f32 v30, v108, v109\n"
"v_cvt_pk_bf16_f32 v31, v110, v111\n"
"buffer_store_dwordx4 v[28:31], v0, s[4:7], s97 offen sc1\n"
";\n"
"; @A AT a\n"
"s_mov_b32 s2, 0\n"
"v_mov_b32_e32 v253, 0xff800000\n"
"v_mov_b32_e32 v251, 0xff800000\n"
"v_mov_b32_e32 v250, 0x7f800000\n"
"v_mov_b32_e32 v249, 0x7f800000\n"
".Lmk_a23:\n"
"ds_read_b128 v[28:31], v15 offset:16384\n"
"ds_read_b128 v[32:35], v254 offset:16640\n"
"ds_read_b128 v[36:39], v15 offset:14336\n"
"ds_read_b128 v[40:43], v254 offset:14592\n"
"s_cmp_gt_i32 s78, 7\n"
"s_cselect_b32 s97, 0x80000000, 0\n"
"s_or_b32 s97, s97, 0x7f800000\n"
"s_add_u32 s96, s67, 32\n"
"buffer_load_dwordx2 v[246:247], v12, s[4:7], s96 offen sc1\n"
"s_waitcnt lgkmcnt(2)\n"
"v_mfma_f32_16x16x32_bf16 v[100:103], v[28:31], v[92:95], 0\n"
"v_mfma_f32_16x16x32_bf16 v[104:107], v[28:31], v[96:99], 0\n"
"v_mfma_f32_16x16x32_bf16 v[108:111], v[32:35], v[96:99], 0\n"
"v_mfma_f32_16x16x32_bf16 v[112:115], v[32:35], v[92:95], 0\n"
"s_waitcnt lgkmcnt(0)\n"
"s_nop 3\n"
"v_max_f32_e32 v248, v100, v101\n"
"v_max_f32_e32 v245, v104, v105\n"
"v_max3_f32 v248, v248, v102, v103\n"
"v_max3_f32 v245, v245, v106, v107\n"
"v_max3_f32 v248, v248, v112, v113\n"
"v_max3_f32 v245, v245, v108, v109\n"
"v_max3_f32 v248, v248, v114, v115\n"
"v_max3_f32 v245, v245, v110, v111\n"
"v_mov_b32_e32 v244, v248\n"
"v_mov_b32_e32 v243, v245\n"
"s_nop 0\n"
"v_permlane16_swap_b32_e32 v248, v244\n"
"v_permlane16_swap_b32_e32 v245, v243\n"
"v_max_f32_e32 v248, v248, v244\n"
"v_max_f32_e32 v245, v245, v243\n"
"v_mov_b32_e32 v244, v248\n"
"v_mov_b32_e32 v243, v245\n"
"s_nop 0\n"
"v_permlane32_swap_b32_e32 v248, v244\n"
"v_permlane32_swap_b32_e32 v245, v243\n"
"v_max_f32_e32 v248, v248, v244\n"
"v_max_f32_e32 v245, v245, v243\n"
"v_med3_f32 v248, v248, v253, v250\n"
"v_med3_f32 v245, v245, v251, v249\n"
"v_min_f32_e64 v116, s97, -v248\n"
"v_min_f32_e64 v117, s97, -v248\n"
"v_min_f32_e64 v118, s97, -v248\n"
"v_min_f32_e64 v119, s97, -v248\n"
"v_min_f32_e64 v120, s97, -v245\n"
"v_min_f32_e64 v121, s97, -v245\n"
"v_min_f32_e64 v122, s97, -v245\n"
"v_min_f32_e64 v123, s97, -v245\n"
"v_mfma_f32_16x16x32_bf16 v[44:47], v[36:39], v[92:95], v[116:119]\n"
"ds_read_b128 v[32:35], v15 offset:12288\n"
"v_sub_f32_e32 v100, v100, v248\n"
"v_sub_f32_e32 v101, v101, v248\n"
"v_sub_f32_e32 v102, v102, v248\n"
"v_sub_f32_e32 v103, v103, v248\n"
"v_mfma_f32_16x16x32_bf16 v[48:51], v[36:39], v[96:99], v[120:123]\n"
"v_sub_f32_e32 v112, v112, v248\n"
"v_sub_f32_e32 v113, v113, v248\n"
"v_sub_f32_e32 v114, v114, v248\n"
"v_sub_f32_e32 v115, v115, v248\n"
"v_mfma_f32_16x16x32_bf16 v[52:55], v[40:43], v[96:99], v[120:123]\n"
"ds_read_b128 v[56:59], v254 offset:12544\n"
"v_sub_f32_e32 v104, v104, v245\n"
"v_sub_f32_e32 v105, v105, v245\n"
"v_sub_f32_e32 v106, v106, v245\n"
"v_sub_f32_e32 v107, v107, v245\n"
"v_mfma_f32_16x16x32_bf16 v[36:39], v[40:43], v[92:95], v[116:119]\n"
"s_waitcnt lgkmcnt(0)\n"
"v_sub_f32_e32 v108, v108, v245\n"
"v_sub_f32_e32 v109, v109, v245\n"
"v_sub_f32_e32 v110, v110, v245\n"
"v_sub_f32_e32 v111, v111, v245\n"
"s_cmp_eq_i32 s78, 7\n"
"s_cbranch_scc1 .Lmk_x25s\n"
".Lmk_x25r:\n"
"v_mfma_f32_16x16x32_bf16 v[60:63], v[32:35], v[92:95], v[116:119]\n"
"ds_read_b128 v[40:43], v15 offset:10240\n"
"buffer_load_dwordx4 v[28:31], v0, s[4:7], s98 offen offset:1024 sc1\n"
"ds_read_b128 v[64:67], v254 offset:10496\n"
"v_exp_f32_e32 v44, v44\n"
"v_exp_f32_e32 v45, v45\n"
"v_exp_f32_e32 v46, v46\n"
"v_exp_f32_e32 v47, v47\n"
"v_exp_f32_e32 v36, v36\n"
"v_mfma_f32_16x16x32_bf16 v[68:71], v[32:35], v[96:99], v[120:123]\n"
"ds_read_b64_tr_b16 v[72:73], v16 offset:14336\n"
"buffer_load_dwordx4 v[32:35], v0, s[4:7], s98 offen offset:2048 sc1\n"
"v_exp_f32_e32 v37, v37\n"
"v_exp_f32_e32 v38, v38\n"
"v_exp_f32_e32 v39, v39\n"
"v_cvt_pk_bf16_f32 v76, v44, v45\n"
"v_cvt_pk_bf16_f32 v77, v46, v47\n"
"v_cvt_pk_bf16_f32 v78, v36, v37\n"
"v_mfma_f32_16x16x32_bf16 v[80:83], v[56:59], v[96:99], v[120:123]\n"
"ds_read_b64_tr_b16 v[74:75], v255 offset:14592\n"
"ds_read_b64_tr_b16 v[84:85], v255 offset:14336\n"
"v_cvt_pk_bf16_f32 v79, v38, v39\n"
"v_exp_f32_e32 v48, v48\n"
"v_exp_f32_e32 v49, v49\n"
"v_exp_f32_e32 v50, v50\n"
"v_exp_f32_e32 v51, v51\n"
"v_mfma_f32_16x16x32_bf16 v[124:127], v[56:59], v[92:95], v[116:119]\n"
"buffer_load_dwordx4 v[36:39], v0, s[4:7], s98 offen offset:3072 sc1\n"
"ds_read_b64_tr_b16 v[86:87], v16 offset:14592\n"
"v_exp_f32_e32 v52, v52\n"
"v_exp_f32_e32 v53, v53\n"
"v_exp_f32_e32 v54, v54\n"
"v_exp_f32_e32 v55, v55\n"
"v_cvt_pk_bf16_f32 v56, v48, v49\n"
"v_cvt_pk_bf16_f32 v57, v50, v51\n"
"v_cvt_pk_bf16_f32 v58, v52, v53\n"
"v_cvt_pk_bf16_f32 v59, v54, v55\n"
"s_waitcnt lgkmcnt(4)\n"
"s_cmp_eq_i32 s78, 6\n"
"s_cbranch_scc1 .Lmk_x26s\n"
".Lmk_x26r:\n"
"v_mfma_f32_16x16x32_bf16 v[128:131], v[40:43], v[92:95], v[116:119]\n"
"ds_read_b128 v[52:55], v15 offset:8192\n"
"v_exp_f32_e32 v60, v60\n"
"v_exp_f32_e32 v61, v61\n"
"v_exp_f32_e32 v62, v62\n"
"v_mfma_f32_16x16x32_bf16 v[132:135], v[40:43], v[96:99], v[120:123]\n"
"buffer_load_dwordx4 v[40:43], v1, s[4:7], s98 offen sc1\n"
"v_exp_f32_e32 v63, v63\n"
"v_mfma_f32_16x16x32_bf16 v[136:139], v[64:67], v[96:99], v[120:123]\n"
"ds_read_b128 v[140:143], v254 offset:8448\n"
"v_exp_f32_e32 v124, v124\n"
"v_exp_f32_e32 v125, v125\n"
"v_mfma_f32_16x16x32_bf16 v[144:147], v[64:67], v[92:95], v[116:119]\n"
"ds_read_b64_tr_b16 v[64:65], v16 offset:12288\n"
"v_exp_f32_e32 v126, v126\n"
"v_exp_f32_e32 v127, v127\n"
"s_waitcnt lgkmcnt(3)\n"
"v_mfma_f32_16x16x32_bf16 v[148:151], v[88:91], v[76:79], 0\n"
"buffer_load_dwordx4 v[44:47], v1, s[4:7], s98 offen offset:1024 sc1\n"
"v_cvt_pk_bf16_f32 v152, v60, v61\n"
"v_cvt_pk_bf16_f32 v153, v62, v63\n"
"v_cvt_pk_bf16_f32 v154, v124, v125\n"
"v_mfma_f32_16x16x32_bf16 v[156:159], v[72:75], v[76:79], 0\n"
"ds_read_b64_tr_b16 v[66:67], v255 offset:12544\n"
"v_cvt_pk_bf16_f32 v155, v126, v127\n"
"v_exp_f32_e32 v68, v68\n"
"v_exp_f32_e32 v69, v69\n"
"v_mfma_f32_16x16x32_bf16 v[124:127], v[84:87], v[76:79], 0\n"
"ds_read_b64_tr_b16 v[60:61], v255 offset:12288\n"
"v_exp_f32_e32 v70, v70\n"
"v_exp_f32_e32 v71, v71\n"
"v_mfma_f32_16x16x32_bf16 v[160:163], v[84:87], v[56:59], 0\n"
"buffer_load_dwordx4 v[48:51], v1, s[4:7], s98 offen offset:2048 sc1\n"
"v_exp_f32_e32 v80, v80\n"
"v_exp_f32_e32 v81, v81\n"
"v_mfma_f32_16x16x32_bf16 v[164:167], v[72:75], v[56:59], 0\n"
"ds_read_b64_tr_b16 v[62:63], v16 offset:12544\n"
"v_exp_f32_e32 v82, v82\n"
"v_exp_f32_e32 v83, v83\n"
"v_mfma_f32_16x16x32_bf16 v[168:171], v[88:91], v[56:59], 0\n"
"s_waitcnt lgkmcnt(4)\n"
"v_cvt_pk_bf16_f32 v72, v68, v69\n"
"v_cvt_pk_bf16_f32 v73, v70, v71\n"
"v_cvt_pk_bf16_f32 v74, v80, v81\n"
"v_cvt_pk_bf16_f32 v75, v82, v83\n"
"s_cmp_eq_i32 s78, 5\n"
"s_cbranch_scc1 .Lmk_x27s\n"
".Lmk_x27r:\n"
"v_mfma_f32_16x16x32_bf16 v[76:79], v[52:55], v[92:95], v[116:119]\n"
"ds_read_b64_tr_b16 v[68:69], v16 offset:10240\n"
"v_exp_f32_e32 v128, v128\n"
"v_exp_f32_e32 v129, v129\n"
"v_exp_f32_e32 v130, v130\n"
"v_mfma_f32_16x16x32_bf16 v[80:83], v[52:55], v[96:99], v[120:123]\n"
"buffer_load_dwordx4 v[52:55], v1, s[4:7], s98 offen offset:3072 sc1\n"
"v_exp_f32_e32 v131, v131\n"
"v_mfma_f32_16x16x32_bf16 v[84:87], v[140:143], v[96:99], v[120:123]\n"
"ds_read_b64_tr_b16 v[70:71], v255 offset:10496\n"
"v_exp_f32_e32 v144, v144\n"
"v_exp_f32_e32 v145, v145\n"
"v_mfma_f32_16x16x32_bf16 v[172:175], v[140:143], v[92:95], v[116:119]\n"
"s_waitcnt lgkmcnt(2)\n"
"v_exp_f32_e32 v146, v146\n"
"v_exp_f32_e32 v147, v147\n"
"v_cvt_pk_bf16_f32 v140, v128, v129\n"
"v_mfma_f32_16x16x32_bf16 v[148:151], v[88:91], v[152:155], v[148:151]\n"
"s_add_u32 s97, s69, 0x60a000\n"
"buffer_load_dwordx4 v[56:59], v0, s[4:7], s97 offen sc1\n"
"v_cvt_pk_bf16_f32 v141, v130, v131\n"
"v_cvt_pk_bf16_f32 v142, v144, v145\n"
"v_mfma_f32_16x16x32_bf16 v[156:159], v[64:67], v[152:155], v[156:159]\n"
"ds_read_b64_tr_b16 v[128:129], v255 offset:10240\n"
"v_cvt_pk_bf16_f32 v143, v146, v147\n"
"v_exp_f32_e32 v132, v132\n"
"v_exp_f32_e32 v133, v133\n"
"v_mfma_f32_16x16x32_bf16 v[124:127], v[60:63], v[152:155], v[124:127]\n"
"v_exp_f32_e32 v134, v134\n"
"v_exp_f32_e32 v135, v135\n"
"v_mfma_f32_16x16x32_bf16 v[160:163], v[60:63], v[72:75], v[160:163]\n"
"buffer_load_dwordx4 v[60:63], v0, s[4:7], s97 offen offset:1024 sc1\n"
"ds_read_b64_tr_b16 v[130:131], v16 offset:10496\n"
"v_exp_f32_e32 v136, v136\n"
"v_mfma_f32_16x16x32_bf16 v[164:167], v[64:67], v[72:75], v[164:167]\n"
"v_exp_f32_e32 v137, v137\n"
"v_exp_f32_e32 v138, v138\n"
"v_exp_f32_e32 v139, v139\n"
"v_mfma_f32_16x16x32_bf16 v[168:171], v[88:91], v[72:75], v[168:171]\n"
"s_waitcnt vmcnt(12)\n"
"v_cvt_pk_bf16_f32 v72, v132, v133\n"
"v_cvt_pk_bf16_f32 v73, v134, v135\n"
"v_cvt_pk_bf16_f32 v74, v136, v137\n"
"v_cvt_pk_bf16_f32 v75, v138, v139\n"
"s_cmp_lg_u32 s2, 0\n"
"s_cbranch_scc1 .Lmk_s28\n"
"s_barrier\n"
".Lmk_s28:\n"
"ds_read_b128 v[132:135], v15 offset:6144\n"
"ds_read_b128 v[136:139], v254 offset:6400\n"
"s_waitcnt lgkmcnt(2)\n"
"v_mfma_f32_16x16x32_bf16 v[148:151], v[88:91], v[140:143], v[148:151]\n"
"ds_read_b128 v[144:147], v15 offset:4096\n"
"v_exp_f32_e32 v76, v76\n"
"v_exp_f32_e32 v77, v77\n"
"v_exp_f32_e32 v78, v78\n"
"v_mfma_f32_16x16x32_bf16 v[156:159], v[68:71], v[140:143], v[156:159]\n"
"buffer_load_dwordx4 v[64:67], v0, s[4:7], s97 offen offset:2048 sc1\n"
"ds_read_b128 v[152:155], v254 offset:4352\n"
"v_exp_f32_e32 v79, v79\n"
"v_mfma_f32_16x16x32_bf16 v[124:127], v[128:131], v[140:143], v[124:127]\n"
"ds_read_b64_tr_b16 v[140:141], v16 offset:8192\n"
"v_exp_f32_e32 v172, v172\n"
"v_exp_f32_e32 v173, v173\n"
"v_mfma_f32_16x16x32_bf16 v[160:163], v[128:131], v[72:75], v[160:163]\n"
"ds_read_b64_tr_b16 v[142:143], v255 offset:8448\n"
"v_exp_f32_e32 v174, v174\n"
"v_exp_f32_e32 v175, v175\n"
"v_mfma_f32_16x16x32_bf16 v[164:167], v[68:71], v[72:75], v[164:167]\n"
"buffer_load_dwordx4 v[68:71], v0, s[4:7], s97 offen offset:3072 sc1\n"
"v_cvt_pk_bf16_f32 v128, v76, v77\n"
"v_cvt_pk_bf16_f32 v129, v78, v79\n"
"v_cvt_pk_bf16_f32 v130, v172, v173\n"
"v_cvt_pk_bf16_f32 v131, v174, v175\n"
"v_mfma_f32_16x16x32_bf16 v[168:171], v[88:91], v[72:75], v[168:171]\n"
"ds_read_b64_tr_b16 v[172:173], v255 offset:8192\n"
"v_exp_f32_e32 v80, v80\n"
"v_exp_f32_e32 v81, v81\n"
"s_waitcnt lgkmcnt(5)\n"
"v_mfma_f32_16x16x32_bf16 v[176:179], v[132:135], v[92:95], v[116:119]\n"
"ds_read_b64_tr_b16 v[174:175], v16 offset:8448\n"
"v_exp_f32_e32 v82, v82\n"
"v_exp_f32_e32 v83, v83\n"
"v_exp_f32_e32 v84, v84\n"
"v_mfma_f32_16x16x32_bf16 v[180:183], v[132:135], v[96:99], v[120:123]\n"
"buffer_load_dwordx4 v[72:75], v1, s[4:7], s97 offen sc1\n"
"s_waitcnt vmcnt(12)\n"
"v_cmp_ne_u64_e32 vcc, v[246:247], v[18:19]\n"
"v_mfma_f32_16x16x32_bf16 v[132:135], v[136:139], v[96:99], v[120:123]\n"
"v_exp_f32_e32 v85, v85\n"
"v_exp_f32_e32 v86, v86\n"
"v_exp_f32_e32 v87, v87\n"
"v_mfma_f32_16x16x32_bf16 v[184:187], v[136:139], v[92:95], v[116:119]\n"
"s_cbranch_vccnz .Lmk_f5s\n"
".Lmk_f5r:\n"
"v_cvt_pk_bf16_f32 v136, v80, v81\n"
"v_cvt_pk_bf16_f32 v137, v82, v83\n"
"v_cvt_pk_bf16_f32 v138, v84, v85\n"
"v_cvt_pk_bf16_f32 v139, v86, v87\n"
"s_waitcnt lgkmcnt(4)\n"
"v_mfma_f32_16x16x32_bf16 v[188:191], v[144:147], v[92:95], v[116:119]\n"
"ds_read_b128 v[192:195], v15 offset:2048\n"
"v_exp_f32_e32 v176, v176\n"
"v_exp_f32_e32 v177, v177\n"
"v_exp_f32_e32 v178, v178\n"
"v_mfma_f32_16x16x32_bf16 v[196:199], v[144:147], v[96:99], v[120:123]\n"
"buffer_load_dwordx4 v[76:79], v1, s[4:7], s97 offen offset:1024 sc1\n"
"v_exp_f32_e32 v179, v179\n"
"v_mfma_f32_16x16x32_bf16 v[144:147], v[152:155], v[96:99], v[120:123]\n"
"ds_read_b128 v[200:203], v254 offset:2304\n"
"v_exp_f32_e32 v184, v184\n"
"v_exp_f32_e32 v185, v185\n"
"v_mfma_f32_16x16x32_bf16 v[204:207], v[152:155], v[92:95], v[116:119]\n"
"ds_read_b64_tr_b16 v[152:153], v16 offset:6144\n"
"v_exp_f32_e32 v186, v186\n"
"v_exp_f32_e32 v187, v187\n"
"s_waitcnt lgkmcnt(3)\n"
"v_mfma_f32_16x16x32_bf16 v[148:151], v[88:91], v[128:131], v[148:151]\n"
"buffer_load_dwordx4 v[80:83], v1, s[4:7], s97 offen offset:2048 sc1\n"
"v_cvt_pk_bf16_f32 v208, v176, v177\n"
"v_cvt_pk_bf16_f32 v209, v178, v179\n"
"v_cvt_pk_bf16_f32 v210, v184, v185\n"
"v_mfma_f32_16x16x32_bf16 v[156:159], v[140:143], v[128:131], v[156:159]\n"
"ds_read_b64_tr_b16 v[154:155], v255 offset:6400\n"
"v_cvt_pk_bf16_f32 v211, v186, v187\n"
"v_exp_f32_e32 v180, v180\n"
"v_exp_f32_e32 v181, v181\n"
"v_mfma_f32_16x16x32_bf16 v[124:127], v[172:175], v[128:131], v[124:127]\n"
"ds_read_b64_tr_b16 v[128:129], v255 offset:6144\n"
"v_exp_f32_e32 v182, v182\n"
"v_exp_f32_e32 v183, v183\n"
"v_mfma_f32_16x16x32_bf16 v[160:163], v[172:175], v[136:139], v[160:163]\n"
"buffer_load_dwordx4 v[84:87], v1, s[4:7], s97 offen offset:3072 sc1\n"
"v_exp_f32_e32 v132, v132\n"
"v_exp_f32_e32 v133, v133\n"
"v_mfma_f32_16x16x32_bf16 v[164:167], v[140:143], v[136:139], v[164:167]\n"
"ds_read_b64_tr_b16 v[130:131], v16 offset:6400\n"
"v_exp_f32_e32 v134, v134\n"
"v_exp_f32_e32 v135, v135\n"
"v_mfma_f32_16x16x32_bf16 v[168:171], v[88:91], v[136:139], v[168:171]\n"
"s_waitcnt lgkmcnt(4)\n"
"v_cvt_pk_bf16_f32 v136, v180, v181\n"
"v_cvt_pk_bf16_f32 v137, v182, v183\n"
"v_cvt_pk_bf16_f32 v138, v132, v133\n"
"v_cvt_pk_bf16_f32 v139, v134, v135\n"
"v_mfma_f32_16x16x32_bf16 v[132:135], v[192:195], v[92:95], v[116:119]\n"
"ds_read_b128 v[140:143], v15\n"
"v_exp_f32_e32 v188, v188\n"
"v_exp_f32_e32 v189, v189\n"
"v_exp_f32_e32 v190, v190\n"
"v_mfma_f32_16x16x32_bf16 v[172:175], v[192:195], v[96:99], v[120:123]\n"
"s_add_u32 s97, s56, 0x40000\n"
"buffer_load_dwordx4 v[176:179], v0, s[4:7], s97 offen sc1\n"
"v_exp_f32_e32 v191, v191\n"
"v_mfma_f32_16x16x32_bf16 v[180:183], v[200:203], v[96:99], v[120:123]\n"
"ds_read_b128 v[184:187], v254 offset:256\n"
"v_exp_f32_e32 v204, v204\n"
"v_exp_f32_e32 v205, v205\n"
"v_exp_f32_e32 v206, v206\n"
"v_mfma_f32_16x16x32_bf16 v[192:195], v[200:203], v[92:95], v[116:119]\n"
"buffer_load_dwordx4 v[200:203], v0, s[4:7], s97 offen offset:1024 sc1\n"
"ds_read_b64_tr_b16 v[212:213], v16 offset:4096\n"
"v_exp_f32_e32 v207, v207\n"
"s_waitcnt lgkmcnt(3)\n"
"v_mfma_f32_16x16x32_bf16 v[148:151], v[88:91], v[208:211], v[148:151]\n"
"v_cvt_pk_bf16_f32 v216, v188, v189\n"
"v_cvt_pk_bf16_f32 v217, v190, v191\n"
"v_cvt_pk_bf16_f32 v218, v204, v205\n"
"v_cvt_pk_bf16_f32 v219, v206, v207\n"
"v_mfma_f32_16x16x32_bf16 v[156:159], v[152:155], v[208:211], v[156:159]\n"
"buffer_load_dwordx4 v[188:191], v0, s[4:7], s97 offen offset:2048 sc1\n"
"ds_read_b64_tr_b16 v[214:215], v255 offset:4352\n"
"v_exp_f32_e32 v196, v196\n"
"v_exp_f32_e32 v197, v197\n"
"v_mfma_f32_16x16x32_bf16 v[124:127], v[128:131], v[208:211], v[124:127]\n"
"buffer_load_dwordx4 v[204:207], v0, s[4:7], s97 offen offset:3072 sc1\n"
"v_exp_f32_e32 v198, v198\n"
"v_exp_f32_e32 v199, v199\n"
"v_mfma_f32_16x16x32_bf16 v[160:163], v[128:131], v[136:139], v[160:163]\n"
"ds_read_b64_tr_b16 v[128:129], v255 offset:4096\n"
"v_exp_f32_e32 v144, v144\n"
"v_exp_f32_e32 v145, v145\n"
"v_mfma_f32_16x16x32_bf16 v[164:167], v[152:155], v[136:139], v[164:167]\n"
"buffer_load_dwordx4 v[152:155], v1, s[4:7], s97 offen sc1\n"
"ds_read_b64_tr_b16 v[130:131], v16 offset:4352\n"
"v_exp_f32_e32 v146, v146\n"
"v_exp_f32_e32 v147, v147\n"
"v_mfma_f32_16x16x32_bf16 v[168:171], v[88:91], v[136:139], v[168:171]\n"
"s_waitcnt lgkmcnt(4)\n"
"v_cvt_pk_bf16_f32 v136, v196, v197\n"
"v_cvt_pk_bf16_f32 v137, v198, v199\n"
"v_cvt_pk_bf16_f32 v138, v144, v145\n"
"v_cvt_pk_bf16_f32 v139, v146, v147\n"
"v_mfma_f32_16x16x32_bf16 v[144:147], v[140:143], v[92:95], v[116:119]\n"
"ds_read_b64_tr_b16 v[196:197], v16 offset:2048\n"
"ds_read_b64_tr_b16 v[198:199], v255 offset:2304\n"
"buffer_load_dwordx4 v[208:211], v1, s[4:7], s97 offen offset:1024 sc1\n"
"v_exp_f32_e32 v132, v132\n"
"v_exp_f32_e32 v133, v133\n"
"v_mfma_f32_16x16x32_bf16 v[220:223], v[140:143], v[96:99], v[120:123]\n"
"ds_read_b64_tr_b16 v[140:141], v255 offset:2048\n"
"v_exp_f32_e32 v134, v134\n"
"v_exp_f32_e32 v135, v135\n"
"v_mfma_f32_16x16x32_bf16 v[224:227], v[184:187], v[96:99], v[120:123]\n"
"buffer_load_dwordx4 v[120:123], v1, s[4:7], s97 offen offset:2048 sc1\n"
"v_exp_f32_e32 v192, v192\n"
"v_exp_f32_e32 v193, v193\n"
"v_mfma_f32_16x16x32_bf16 v[228:231], v[184:187], v[92:95], v[116:119]\n"
"ds_read_b64_tr_b16 v[142:143], v16 offset:2304\n"
"ds_read_b64_tr_b16 v[116:117], v16 offset:16384\n"
"v_exp_f32_e32 v194, v194\n"
"v_exp_f32_e32 v195, v195\n"
"s_waitcnt lgkmcnt(5)\n"
"v_mfma_f32_16x16x32_bf16 v[148:151], v[88:91], v[216:219], v[148:151]\n"
"v_cvt_pk_bf16_f32 v184, v132, v133\n"
"v_cvt_pk_bf16_f32 v185, v134, v135\n"
"v_cvt_pk_bf16_f32 v186, v192, v193\n"
"v_mfma_f32_16x16x32_bf16 v[156:159], v[212:215], v[216:219], v[156:159]\n"
"buffer_load_dwordx4 v[132:135], v1, s[4:7], s97 offen offset:3072 sc1\n"
"ds_read_b64_tr_b16 v[118:119], v255 offset:16640\n"
"ds_read_b64_tr_b16 v[192:193], v255 offset:16384\n"
"v_cvt_pk_bf16_f32 v187, v194, v195\n"
"v_exp_f32_e32 v172, v172\n"
"v_exp_f32_e32 v173, v173\n"
"v_exp_f32_e32 v174, v174\n"
"v_mfma_f32_16x16x32_bf16 v[124:127], v[128:131], v[216:219], v[124:127]\n"
"s_add_u32 s97, s57, 0x40000\n"
"buffer_load_dwordx4 v[216:219], v0, s[4:7], s97 offen sc1\n"
"ds_read_b64_tr_b16 v[194:195], v16 offset:16640\n"
"v_exp_f32_e32 v175, v175\n"
"v_mfma_f32_16x16x32_bf16 v[160:163], v[128:131], v[136:139], v[160:163]\n"
"v_exp_f32_e32 v180, v180\n"
"v_add_u32_e32 v248, 0x26400, v0\n"
"v_exp_f32_e32 v181, v181\n"
"v_mfma_f32_16x16x32_bf16 v[164:167], v[212:215], v[136:139], v[164:167]\n"
"buffer_load_dwordx4 v[128:131], v0, s[4:7], s97 offen offset:1024 sc1\n"
"ds_read_b128 v[212:215], v248\n"
"v_exp_f32_e32 v182, v182\n"
"v_exp_f32_e32 v183, v183\n"
"v_mfma_f32_16x16x32_bf16 v[168:171], v[88:91], v[136:139], v[168:171]\n"
"ds_read_b128 v[232:235], v248 offset:1024\n"
"v_cvt_pk_bf16_f32 v136, v172, v173\n"
"v_cvt_pk_bf16_f32 v137, v174, v175\n"
"v_cvt_pk_bf16_f32 v138, v180, v181\n"
"v_cvt_pk_bf16_f32 v139, v182, v183\n"
"s_waitcnt lgkmcnt(6)\n"
"v_mfma_f32_16x16x32_bf16 v[148:151], v[88:91], v[184:187], v[148:151]\n"
"ds_read_b64_tr_b16 v[172:173], v16\n"
"buffer_load_dwordx4 v[180:183], v0, s[4:7], s97 offen offset:2048 sc1\n"
"v_cndmask_b32_e64 v144, v100, v144, s[20:21]\n"
"v_cndmask_b32_e64 v145, v101, v145, s[22:23]\n"
"v_cndmask_b32_e64 v146, v102, v146, s[24:25]\n"
"v_cndmask_b32_e64 v147, v103, v147, s[26:27]\n"
"v_cndmask_b32_e64 v228, v112, v228, s[28:29]\n"
"v_cndmask_b32_e64 v229, v113, v229, s[30:31]\n"
"v_cndmask_b32_e64 v230, v114, v230, s[32:33]\n"
"v_cndmask_b32_e64 v231, v115, v231, s[34:35]\n"
"v_exp_f32_e32 v144, v144\n"
"v_mfma_f32_16x16x32_bf16 v[156:159], v[196:199], v[184:187], v[156:159]\n"
"ds_read_b64_tr_b16 v[174:175], v255 offset:256\n"
"buffer_load_dwordx4 v[100:103], v0, s[4:7], s97 offen offset:3072 sc1\n"
"v_exp_f32_e32 v145, v145\n"
"v_exp_f32_e32 v146, v146\n"
"v_exp_f32_e32 v147, v147\n"
"v_exp_f32_e32 v228, v228\n"
"v_mfma_f32_16x16x32_bf16 v[124:127], v[140:143], v[184:187], v[124:127]\n"
"buffer_load_dwordx4 v[184:187], v1, s[4:7], s97 offen sc1\n"
"v_exp_f32_e32 v229, v229\n"
"v_exp_f32_e32 v230, v230\n"
"v_exp_f32_e32 v231, v231\n"
"v_cvt_pk_bf16_f32 v112, v144, v145\n"
"v_cvt_pk_bf16_f32 v113, v146, v147\n"
"v_cvt_pk_bf16_f32 v114, v228, v229\n"
"v_mfma_f32_16x16x32_bf16 v[160:163], v[140:143], v[136:139], v[160:163]\n"
"ds_read_b64_tr_b16 v[140:141], v255\n"
"v_cvt_pk_bf16_f32 v115, v230, v231\n"
"v_cndmask_b32_e64 v220, v104, v220, s[36:37]\n"
"v_cndmask_b32_e64 v221, v105, v221, s[38:39]\n"
"v_cndmask_b32_e64 v222, v106, v222, s[40:41]\n"
"v_cndmask_b32_e64 v223, v107, v223, s[42:43]\n"
"v_cndmask_b32_e64 v224, v108, v224, s[44:45]\n"
"v_cndmask_b32_e64 v225, v109, v225, s[46:47]\n"
"v_cndmask_b32_e64 v226, v110, v226, s[48:49]\n"
"v_cndmask_b32_e64 v227, v111, v227, s[50:51]\n"
"v_mfma_f32_16x16x32_bf16 v[164:167], v[196:199], v[136:139], v[164:167]\n"
"buffer_load_dwordx4 v[104:107], v1, s[4:7], s97 offen offset:1024 sc1\n"
"ds_read_b64_tr_b16 v[142:143], v16 offset:256\n"
"buffer_load_dwordx4 v[108:111], v1, s[4:7], s97 offen offset:2048 sc1\n"
"v_exp_f32_e32 v220, v220\n"
"v_exp_f32_e32 v221, v221\n"
"v_exp_f32_e32 v222, v222\n"
"v_exp_f32_e32 v223, v223\n"
"v_exp_f32_e32 v224, v224\n"
"v_mfma_f32_16x16x32_bf16 v[168:171], v[88:91], v[136:139], v[168:171]\n"
"s_waitcnt lgkmcnt(0)\n"
"v_exp_f32_e32 v225, v225\n"
"v_exp_f32_e32 v226, v226\n"
"v_exp_f32_e32 v227, v227\n"
"v_cvt_pk_bf16_f32 v136, v220, v221\n"
"v_cvt_pk_bf16_f32 v137, v222, v223\n"
"v_cvt_pk_bf16_f32 v138, v224, v225\n"
"v_cvt_pk_bf16_f32 v139, v226, v227\n"
"v_mfma_f32_16x16x32_bf16 v[148:151], v[88:91], v[112:115], v[148:151]\n"
"v_and_b32_e32 v212, v112, v212\n"
"v_and_b32_e32 v213, v113, v213\n"
"v_and_b32_e32 v214, v114, v214\n"
"v_and_b32_e32 v215, v115, v215\n"
"v_mfma_f32_16x16x32_bf16 v[168:171], v[88:91], v[136:139], v[168:171]\n"
"v_xor_b32_e32 v144, v112, v212\n"
"v_xor_b32_e32 v145, v113, v213\n"
"v_xor_b32_e32 v146, v114, v214\n"
"v_xor_b32_e32 v147, v115, v215\n"
"v_mfma_f32_16x16x32_bf16 v[156:159], v[172:175], v[212:215], v[156:159]\n"
"v_and_b32_e32 v232, v136, v232\n"
"v_and_b32_e32 v233, v137, v233\n"
"v_mfma_f32_16x16x32_bf16 v[124:127], v[140:143], v[212:215], v[124:127]\n"
"v_and_b32_e32 v234, v138, v234\n"
"v_and_b32_e32 v235, v139, v235\n"
"v_mfma_f32_16x16x32_bf16 v[156:159], v[116:119], v[144:147], v[156:159]\n"
"v_xor_b32_e32 v112, v136, v232\n"
"v_xor_b32_e32 v113, v137, v233\n"
"v_mfma_f32_16x16x32_bf16 v[124:127], v[192:195], v[144:147], v[124:127]\n"
"v_xor_b32_e32 v114, v138, v234\n"
"v_xor_b32_e32 v115, v139, v235\n"
"v_mfma_f32_16x16x32_bf16 v[164:167], v[172:175], v[232:235], v[164:167]\n"
"buffer_load_dwordx4 v[136:139], v1, s[4:7], s97 offen offset:3072 sc1\n"
"s_add_u32 s97, s58, 0x40000\n"
"buffer_load_dwordx4 v[144:147], v0, s[4:7], s97 offen sc1\n"
"v_mfma_f32_16x16x32_bf16 v[160:163], v[140:143], v[232:235], v[160:163]\n"
"buffer_load_dwordx4 v[140:143], v0, s[4:7], s97 offen offset:1024 sc1\n"
"v_mfma_f32_16x16x32_bf16 v[164:167], v[116:119], v[112:115], v[164:167]\n"
"buffer_load_dwordx4 v[116:119], v0, s[4:7], s97 offen offset:2048 sc1\n"
"v_mfma_f32_16x16x32_bf16 v[160:163], v[192:195], v[112:115], v[160:163]\n"
"buffer_load_dwordx4 v[112:115], v0, s[4:7], s97 offen offset:3072 sc1\n"
"v_max_f32_e32 v248, v148, v168\n"
"v_rcp_f32_e32 v148, v148\n"
"v_rcp_f32_e32 v168, v168\n"
"s_nop 0\n"
"v_max3_f32 v248, v248, v148, v168\n"
"v_cmp_nlt_f32_e32 vcc, v248, v252\n"
"s_cbranch_vccnz .Lmk_b24\n"
".Lmk_k29:\n"
"v_mul_f32_e32 v156, v156, v148\n"
"v_mul_f32_e32 v157, v157, v148\n"
"v_mul_f32_e32 v158, v158, v148\n"
"v_mul_f32_e32 v159, v159, v148\n"
"v_mul_f32_e32 v124, v124, v148\n"
"v_mul_f32_e32 v125, v125, v148\n"
"v_mul_f32_e32 v126, v126, v148\n"
"v_mul_f32_e32 v127, v127, v148\n"
"v_cvt_pk_bf16_f32 v88, v156, v157\n"
"v_cvt_pk_bf16_f32 v89, v158, v159\n"
"v_cvt_pk_bf16_f32 v90, v124, v125\n"
"v_cvt_pk_bf16_f32 v91, v126, v127\n"
"buffer_store_dwordx4 v[88:91], v0, s[4:7], s60 offen sc1\n"
"s_add_u32 s98, s60, 0x8000\n"
"v_mul_f32_e32 v164, v164, v168\n"
"v_mul_f32_e32 v165, v165, v168\n"
"v_mul_f32_e32 v166, v166, v168\n"
"v_mul_f32_e32 v167, v167, v168\n"
"v_mul_f32_e32 v160, v160, v168\n"
"v_mul_f32_e32 v161, v161, v168\n"
"v_mul_f32_e32 v162, v162, v168\n"
"v_mul_f32_e32 v163, v163, v168\n"
"v_cvt_pk_bf16_f32 v88, v164, v165\n"
"v_cvt_pk_bf16_f32 v89, v166, v167\n"
"v_cvt_pk_bf16_f32 v90, v160, v161\n"
"v_cvt_pk_bf16_f32 v91, v162, v163\n"
"buffer_store_dwordx4 v[88:91], v0, s[4:7], s98 offen sc1\n"
";\n"
"s_waitcnt vmcnt(20)\n"
"; @A G2 b.0\n"
"s_nop 0\n"
"v_mfma_f32_16x16x32_bf16 v[88:91], a[128:131], v[176:179], 0\n"
"v_mfma_f32_16x16x32_bf16 v[88:91], a[132:135], v[200:203], v[88:91]\n"
"s_waitcnt vmcnt(18)\n"
"v_mfma_f32_16x16x32_bf16 v[88:91], a[136:139], v[188:191], v[88:91]\n"
"buffer_load_dwordx4 v[92:95], v1, s[4:7], s97 offen sc1\n"
"v_mfma_f32_16x16x32_bf16 v[88:91], a[140:143], v[204:207], v[88:91]\n"
"s_waitcnt vmcnt(17)\n"
"v_mfma_f32_16x16x32_bf16 v[88:91], a[144:147], v[152:155], v[88:91]\n"
"v_mfma_f32_16x16x32_bf16 v[88:91], a[148:151], v[208:211], v[88:91]\n"
"s_waitcnt vmcnt(15)\n"
"v_mfma_f32_16x16x32_bf16 v[88:91], a[152:155], v[120:123], v[88:91]\n"
"buffer_load_dwordx4 v[96:99], v1, s[4:7], s97 offen offset:1024 sc1\n"
"v_mfma_f32_16x16x32_bf16 v[88:91], a[156:159], v[132:135], v[88:91]\n"
"v_mfma_f32_16x16x32_bf16 v[124:127], a[188:191], v[132:135], 0\n"
"v_mfma_f32_16x16x32_bf16 v[124:127], a[184:187], v[120:123], v[124:127]\n"
"v_mfma_f32_16x16x32_bf16 v[124:127], a[180:183], v[208:211], v[124:127]\n"
"buffer_load_dwordx4 v[148:151], v1, s[4:7], s97 offen offset:2048 sc1\n"
"v_mfma_f32_16x16x32_bf16 v[124:127], a[176:179], v[152:155], v[124:127]\n"
"v_mfma_f32_16x16x32_bf16 v[124:127], a[172:175], v[204:207], v[124:127]\n"
"s_nop 1\n"
"v_cmp_u_f32_e32 vcc, v88, v88\n"
"v_mfma_f32_16x16x32_bf16 v[124:127], a[168:171], v[188:191], v[124:127]\n"
"s_cbranch_vccnz .Lmk_p30s\n"
".Lmk_p30r:\n"
"v_mfma_f32_16x16x32_bf16 v[124:127], a[164:167], v[200:203], v[124:127]\n"
"ds_write_b128 v3, v[88:91]\n"
"v_mfma_f32_16x16x32_bf16 v[124:127], a[160:163], v[176:179], v[124:127]\n"
"buffer_load_dwordx4 v[88:91], v1, s[4:7], s97 offen offset:3072 sc1\n"
"v_mfma_f32_16x16x32_bf16 v[156:159], a[192:195], v[176:179], 0\n"
"v_mfma_f32_16x16x32_bf16 v[156:159], a[196:199], v[200:203], v[156:159]\n"
"s_add_u32 s98, s59, 0x40000\n"
"v_mfma_f32_16x16x32_bf16 v[156:159], a[200:203], v[188:191], v[156:159]\n"
"v_mfma_f32_16x16x32_bf16 v[156:159], a[204:207], v[204:207], v[156:159]\n"
"buffer_load_dwordx4 v[160:163], v0, s[4:7], s98 offen sc1\n"
"v_mfma_f32_16x16x32_bf16 v[156:159], a[208:211], v[152:155], v[156:159]\n"
"v_mfma_f32_16x16x32_bf16 v[156:159], a[212:215], v[208:211], v[156:159]\n"
"ds_write_b128 v3, v[124:127] offset:1024\n"
"v_mfma_f32_16x16x32_bf16 v[156:159], a[216:219], v[120:123], v[156:159]\n"
"v_mfma_f32_16x16x32_bf16 v[156:159], a[220:223], v[132:135], v[156:159]\n"
"buffer_load_dwordx4 v[124:127], v0, s[4:7], s98 offen offset:1024 sc1\n"
"v_mfma_f32_16x16x32_bf16 v[164:167], a[252:255], v[132:135], 0\n"
"v_mfma_f32_16x16x32_bf16 v[164:167], a[248:251], v[120:123], v[164:167]\n"
"v_mfma_f32_16x16x32_bf16 v[164:167], a[244:247], v[208:211], v[164:167]\n"
"v_mfma_f32_16x16x32_bf16 v[164:167], a[240:243], v[152:155], v[164:167]\n"
"buffer_load_dwordx4 v[120:123], v0, s[4:7], s98 offen offset:2048 sc1\n"
"v_mfma_f32_16x16x32_bf16 v[164:167], a[236:239], v[204:207], v[164:167]\n"
"v_mfma_f32_16x16x32_bf16 v[164:167], a[232:235], v[188:191], v[164:167]\n"
"ds_write_b128 v3, v[156:159] offset:2048\n"
"v_mfma_f32_16x16x32_bf16 v[164:167], a[228:231], v[200:203], v[164:167]\n"
"v_mfma_f32_16x16x32_bf16 v[164:167], a[224:227], v[176:179], v[164:167]\n"
"s_waitcnt vmcnt(17)\n"
"buffer_load_dwordx4 v[132:135], v0, s[4:7], s98 offen offset:3072 sc1\n"
"; @A G2 b.1\n"
"v_mfma_f32_16x16x32_bf16 v[152:155], a[128:131], v[216:219], 0\n"
"v_mfma_f32_16x16x32_bf16 v[152:155], a[132:135], v[128:131], v[152:155]\n"
"v_mfma_f32_16x16x32_bf16 v[152:155], a[136:139], v[180:183], v[152:155]\n"
"v_mfma_f32_16x16x32_bf16 v[152:155], a[140:143], v[100:103], v[152:155]\n"
"s_waitcnt vmcnt(14)\n"
"buffer_load_dwordx4 v[156:159], v1, s[4:7], s98 offen sc1\n"
"v_mfma_f32_16x16x32_bf16 v[152:155], a[144:147], v[184:187], v[152:155]\n"
"v_mfma_f32_16x16x32_bf16 v[152:155], a[148:151], v[104:107], v[152:155]\n"
"ds_write_b128 v3, v[164:167] offset:3072\n"
"v_mfma_f32_16x16x32_bf16 v[152:155], a[152:155], v[108:111], v[152:155]\n"
"v_mfma_f32_16x16x32_bf16 v[152:155], a[156:159], v[136:139], v[152:155]\n"
"buffer_load_dwordx4 v[164:167], v1, s[4:7], s98 offen offset:1024 sc1\n"
"v_mfma_f32_16x16x32_bf16 v[168:171], a[188:191], v[136:139], 0\n"
"v_mfma_f32_16x16x32_bf16 v[168:171], a[184:187], v[108:111], v[168:171]\n"
"v_mfma_f32_16x16x32_bf16 v[168:171], a[180:183], v[104:107], v[168:171]\n"
"v_mfma_f32_16x16x32_bf16 v[168:171], a[176:179], v[184:187], v[168:171]\n"
"buffer_load_dwordx4 v[172:175], v1, s[4:7], s98 offen offset:2048 sc1\n"
"v_mfma_f32_16x16x32_bf16 v[168:171], a[172:175], v[100:103], v[168:171]\n"
"s_nop 0\n"
"v_cmp_u_f32_e32 vcc, v152, v152\n"
"v_mfma_f32_16x16x32_bf16 v[168:171], a[168:171], v[180:183], v[168:171]\n"
"s_cbranch_vccnz .Lmk_p31s\n"
".Lmk_p31r:\n"
"v_mfma_f32_16x16x32_bf16 v[168:171], a[164:167], v[128:131], v[168:171]\n"
"ds_write_b128 v3, v[152:155] offset:6144\n"
"v_mfma_f32_16x16x32_bf16 v[168:171], a[160:163], v[216:219], v[168:171]\n"
"buffer_load_dwordx4 v[152:155], v1, s[4:7], s98 offen offset:3072 sc1\n"
"v_mfma_f32_16x16x32_bf16 v[176:179], a[192:195], v[216:219], 0\n"
"v_mfma_f32_16x16x32_bf16 v[176:179], a[196:199], v[128:131], v[176:179]\n"
"v_mfma_f32_16x16x32_bf16 v[176:179], a[200:203], v[180:183], v[176:179]\n"
"v_mfma_f32_16x16x32_bf16 v[176:179], a[204:207], v[100:103], v[176:179]\n"
"v_mfma_f32_16x16x32_bf16 v[176:179], a[208:211], v[184:187], v[176:179]\n"
"ds_read_b128 v[188:191], v2 offset:8192\n"
"ds_read_b128 v[192:195], v2 offset:9216\n"
"v_mfma_f32_16x16x32_bf16 v[176:179], a[212:215], v[104:107], v[176:179]\n"
"ds_write_b128 v3, v[168:171] offset:7168\n"
"v_mfma_f32_16x16x32_bf16 v[176:179], a[216:219], v[108:111], v[176:179]\n"
"v_mfma_f32_16x16x32_bf16 v[176:179], a[220:223], v[136:139], v[176:179]\n"
"v_mfma_f32_16x16x32_bf16 v[168:171], a[252:255], v[136:139], 0\n"
"s_mov_b32 s98, .Lmk_b0-.Lmk_g34\n"
"s_getpc_b64 s[84:85]\n"
".Lmk_g34:\n"
"s_add_u32 s84, s84, s98\n"
"s_addc_u32 s85, s85, 0\n"
"s_and_b32 s84, s84, -16\n"
"v_mfma_f32_16x16x32_bf16 v[168:171], a[248:251], v[108:111], v[168:171]\n"
"s_and_b32 s85, s85, 0xffff\n"
"s_sub_u32 s98, .Lmk_end-.Lmk_g34, s98\n"
"s_min_u32 s86, s98, 0x3000\n"
"s_mov_b32 s87, 0x20000\n"
"v_mfma_f32_16x16x32_bf16 v[168:171], a[244:247], v[104:107], v[168:171]\n"
"s_mov_b32 m0, 0x27800\n"
"s_nop 0\n"
"buffer_load_dwordx4 v0, s[84:87], s99 offen lds\n"
"v_mfma_f32_16x16x32_bf16 v[168:171], a[240:243], v[184:187], v[168:171]\n"
"s_waitcnt lgkmcnt(1)\n"
"v_lshlrev_b32_e32 v255, 16, v188\n"
"v_and_b32_e32 v254, 0xffff0000, v188\n"
"v_mfma_f32_16x16x32_bf16 v[168:171], a[236:239], v[100:103], v[168:171]\n"
"v_lshlrev_b32_e32 v253, 16, v189\n"
"v_and_b32_e32 v252, 0xffff0000, v189\n"
"v_mfma_f32_16x16x32_bf16 v[168:171], a[232:235], v[180:183], v[168:171]\n"
"ds_write_b128 v3, v[176:179] offset:8192\n"
"v_mfma_f32_16x16x32_bf16 v[168:171], a[228:231], v[128:131], v[168:171]\n"
"v_lshlrev_b32_e32 v251, 16, v190\n"
"v_and_b32_e32 v250, 0xffff0000, v190\n"
"v_mfma_f32_16x16x32_bf16 v[168:171], a[224:227], v[216:219], v[168:171]\n"
"s_waitcnt vmcnt(15)\n"
"v_lshlrev_b32_e32 v249, 16, v191\n"
"v_and_b32_e32 v248, 0xffff0000, v191\n"
"; @A G2 b.2\n"
"v_mfma_f32_16x16x32_bf16 v[100:103], a[128:131], v[144:147], 0\n"
"v_lshlrev_b32_e32 v247, 16, v192\n"
"v_and_b32_e32 v246, 0xffff0000, v192\n"
"v_mfma_f32_16x16x32_bf16 v[100:103], a[132:135], v[140:143], v[100:103]\n"
"v_lshlrev_b32_e32 v245, 16, v193\n"
"v_and_b32_e32 v244, 0xffff0000, v193\n"
"v_mfma_f32_16x16x32_bf16 v[100:103], a[136:139], v[116:119], v[100:103]\n"
"v_lshlrev_b32_e32 v243, 16, v194\n"
"v_and_b32_e32 v242, 0xffff0000, v194\n"
"v_mfma_f32_16x16x32_bf16 v[100:103], a[140:143], v[112:115], v[100:103]\n"
"s_waitcnt vmcnt(9)\n"
"v_lshlrev_b32_e32 v241, 16, v195\n"
"v_and_b32_e32 v240, 0xffff0000, v195\n"
"v_mfma_f32_16x16x32_bf16 v[100:103], a[144:147], v[92:95], v[100:103]\n"
"v_mfma_f32_16x16x32_bf16 v[100:103], a[148:151], v[96:99], v[100:103]\n"
"ds_write_b128 v3, v[168:171] offset:9216\n"
"v_mfma_f32_16x16x32_bf16 v[100:103], a[152:155], v[148:151], v[100:103]\n"
"v_mfma_f32_16x16x32_bf16 v[100:103], a[156:159], v[88:91], v[100:103]\n"
"v_mfma_f32_16x16x32_bf16 v[104:107], a[188:191], v[88:91], 0\n"
"v_mfma_f32_16x16x32_bf16 v[104:107], a[184:187], v[148:151], v[104:107]\n"
"s_waitcnt lgkmcnt(0)\n"
"s_barrier\n"
"v_mfma_f32_16x16x32_bf16 v[104:107], a[180:183], v[96:99], v[104:107]\n"
"ds_read_b128 v[108:111], v4\n"
"ds_read_b128 v[128:131], v5\n"
"v_mfma_f32_16x16x32_bf16 v[104:107], a[176:179], v[92:95], v[104:107]\n"
"v_mfma_f32_16x16x32_bf16 v[104:107], a[172:175], v[112:115], v[104:107]\n"
"s_add_u32 s98, s56, 0\n"
"v_cmp_u_f32_e32 vcc, v100, v100\n"
"v_mfma_f32_16x16x32_bf16 v[104:107], a[168:171], v[116:119], v[104:107]\n"
"s_cbranch_vccnz .Lmk_p32s\n"
".Lmk_p32r:\n"
"v_mfma_f32_16x16x32_bf16 v[104:107], a[164:167], v[140:143], v[104:107]\n"
"ds_write_b128 v3, v[100:103] offset:12288\n"
"v_mfma_f32_16x16x32_bf16 v[104:107], a[160:163], v[144:147], v[104:107]\n"
"buffer_load_dwordx4 v[100:103], v0, s[4:7], s98 offen sc1\n"
"v_mfma_f32_16x16x32_bf16 v[136:139], a[192:195], v[144:147], 0\n"
"ds_read_b128 v[168:171], v4 offset:1024\n"
"ds_read_b128 v[176:179], v5 offset:1024\n"
"v_mfma_f32_16x16x32_bf16 v[136:139], a[196:199], v[140:143], v[136:139]\n"
"s_waitcnt lgkmcnt(3)\n"
"v_add_f32_e32 v108, v108, v255\n"
"v_add_f32_e32 v109, v109, v254\n"
"v_mfma_f32_16x16x32_bf16 v[136:139], a[200:203], v[116:119], v[136:139]\n"
"buffer_load_dwordx4 v[180:183], v0, s[4:7], s98 offen offset:1024 sc1\n"
"v_mfma_f32_16x16x32_bf16 v[136:139], a[204:207], v[112:115], v[136:139]\n"
"v_add_f32_e32 v110, v110, v253\n"
"v_add_f32_e32 v111, v111, v252\n"
"v_mfma_f32_16x16x32_bf16 v[136:139], a[208:211], v[92:95], v[136:139]\n"
"v_add_f32_e32 v108, v108, v128\n"
"v_add_f32_e32 v109, v109, v129\n"
"v_mfma_f32_16x16x32_bf16 v[136:139], a[212:215], v[96:99], v[136:139]\n"
"ds_write_b128 v3, v[104:107] offset:13312\n"
"v_mfma_f32_16x16x32_bf16 v[136:139], a[216:219], v[148:151], v[136:139]\n"
"buffer_load_dwordx4 v[104:107], v0, s[4:7], s98 offen offset:2048 sc1\n"
"v_mfma_f32_16x16x32_bf16 v[136:139], a[220:223], v[88:91], v[136:139]\n"
"ds_read_b128 v[184:187], v4 offset:2048\n"
"ds_read_b128 v[188:191], v5 offset:2048\n"
"v_mfma_f32_16x16x32_bf16 v[192:195], a[252:255], v[88:91], 0\n"
"s_waitcnt lgkmcnt(3)\n"
"v_add_f32_e32 v110, v110, v130\n"
"v_add_f32_e32 v111, v111, v131\n"
"v_mfma_f32_16x16x32_bf16 v[192:195], a[248:251], v[148:151], v[192:195]\n"
"buffer_load_dwordx4 v[88:91], v0, s[4:7], s98 offen offset:3072 sc1\n"
"v_mfma_f32_16x16x32_bf16 v[192:195], a[244:247], v[96:99], v[192:195]\n"
"v_add_f32_e32 v168, v168, v251\n"
"v_add_f32_e32 v169, v169, v250\n"
"v_mfma_f32_16x16x32_bf16 v[192:195], a[240:243], v[92:95], v[192:195]\n"
"v_add_f32_e32 v170, v170, v249\n"
"v_add_f32_e32 v171, v171, v248\n"
"v_mfma_f32_16x16x32_bf16 v[192:195], a[236:239], v[112:115], v[192:195]\n"
"buffer_load_dwordx4 v[92:95], v1, s[4:7], s98 offen sc1\n"
"v_mfma_f32_16x16x32_bf16 v[192:195], a[232:235], v[116:119], v[192:195]\n"
"ds_write_b128 v3, v[136:139] offset:14336\n"
"v_mfma_f32_16x16x32_bf16 v[192:195], a[228:231], v[140:143], v[192:195]\n"
"ds_read_b128 v[96:99], v4 offset:3072\n"
"ds_read_b128 v[112:115], v5 offset:3072\n"
"v_mfma_f32_16x16x32_bf16 v[192:195], a[224:227], v[144:147], v[192:195]\n"
"s_waitcnt vmcnt(10)\n"
"buffer_load_dwordx4 v[116:119], v1, s[4:7], s98 offen offset:1024 sc1\n"
"; @A G2 b.3\n"
"v_mfma_f32_16x16x32_bf16 v[128:131], a[128:131], v[160:163], v[108:111]\n"
"v_add_f32_e32 v168, v168, v176\n"
"v_add_f32_e32 v169, v169, v177\n"
"v_mfma_f32_16x16x32_bf16 v[128:131], a[132:135], v[124:127], v[128:131]\n"
"s_waitcnt lgkmcnt(3)\n"
"v_add_f32_e32 v170, v170, v178\n"
"v_add_f32_e32 v171, v171, v179\n"
"v_mfma_f32_16x16x32_bf16 v[128:131], a[136:139], v[120:123], v[128:131]\n"
"buffer_load_dwordx4 v[136:139], v1, s[4:7], s98 offen offset:2048 sc1\n"
"v_mfma_f32_16x16x32_bf16 v[128:131], a[140:143], v[132:135], v[128:131]\n"
"s_waitcnt vmcnt(8)\n"
"v_add_f32_e32 v184, v184, v247\n"
"v_add_f32_e32 v185, v185, v246\n"
"v_mfma_f32_16x16x32_bf16 v[128:131], a[144:147], v[156:159], v[128:131]\n"
"v_add_f32_e32 v186, v186, v245\n"
"v_add_f32_e32 v187, v187, v244\n"
"v_mfma_f32_16x16x32_bf16 v[128:131], a[148:151], v[164:167], v[128:131]\n"
"ds_write_b128 v3, v[192:195] offset:15360\n"
"v_mfma_f32_16x16x32_bf16 v[128:131], a[152:155], v[172:175], v[128:131]\n"
"buffer_load_dwordx4 v[140:143], v1, s[4:7], s98 offen offset:3072 sc1\n"
"v_mfma_f32_16x16x32_bf16 v[128:131], a[156:159], v[152:155], v[128:131]\n"
"s_add_u32 s98, s57, 0\n"
"v_add_f32_e32 v184, v184, v188\n"
"v_add_f32_e32 v185, v185, v189\n"
"v_mfma_f32_16x16x32_bf16 v[144:147], a[188:191], v[152:155], v[168:171]\n"
"s_waitcnt lgkmcnt(1)\n"
"v_add_f32_e32 v186, v186, v190\n"
"v_add_f32_e32 v187, v187, v191\n"
"v_mfma_f32_16x16x32_bf16 v[144:147], a[184:187], v[172:175], v[144:147]\n"
"s_waitcnt lgkmcnt(0)\n"
"s_barrier\n"
"v_mfma_f32_16x16x32_bf16 v[144:147], a[180:183], v[164:167], v[144:147]\n"
"buffer_load_dwordx4 v[148:151], v0, s[4:7], s98 offen sc1\n"
"v_mfma_f32_16x16x32_bf16 v[144:147], a[176:179], v[156:159], v[144:147]\n"
"v_add_f32_e32 v96, v96, v243\n"
"v_add_f32_e32 v97, v97, v242\n"
"v_mfma_f32_16x16x32_bf16 v[144:147], a[172:175], v[132:135], v[144:147]\n"
"v_cmp_u_f32_e32 vcc, v128, v128\n"
"v_add_f32_e32 v98, v98, v241\n"
"v_mfma_f32_16x16x32_bf16 v[144:147], a[168:171], v[120:123], v[144:147]\n"
"s_cbranch_vccnz .Lmk_p33s\n"
".Lmk_p33r:\n"
"v_add_f32_e32 v99, v99, v240\n"
"v_mfma_f32_16x16x32_bf16 v[144:147], a[164:167], v[124:127], v[144:147]\n"
"buffer_load_dwordx4 v[108:111], v0, s[4:7], s98 offen offset:1024 sc1\n"
"v_mfma_f32_16x16x32_bf16 v[144:147], a[160:163], v[160:163], v[144:147]\n"
"v_add_f32_e32 v96, v96, v112\n"
"v_add_f32_e32 v97, v97, v113\n"
"v_mfma_f32_16x16x32_bf16 v[184:187], a[192:195], v[160:163], v[184:187]\n"
"v_add_f32_e32 v98, v98, v114\n"
"v_add_f32_e32 v99, v99, v115\n"
"v_mfma_f32_16x16x32_bf16 v[184:187], a[196:199], v[124:127], v[184:187]\n"
"buffer_load_dwordx4 v[112:115], v0, s[4:7], s98 offen offset:2048 sc1\n"
"v_mfma_f32_16x16x32_bf16 v[184:187], a[200:203], v[120:123], v[184:187]\n"
"ds_read_b128 v[168:171], v6\n"
"ds_read_b128 v[176:179], v6 offset:1024\n"
"v_mfma_f32_16x16x32_bf16 v[184:187], a[204:207], v[132:135], v[184:187]\n"
"ds_read_b128 v[188:191], v6 offset:2048\n"
"ds_read_b128 v[192:195], v6 offset:3072\n"
"v_mfma_f32_16x16x32_bf16 v[184:187], a[208:211], v[156:159], v[184:187]\n"
"buffer_load_dwordx4 v[196:199], v0, s[4:7], s98 offen offset:3072 sc1\n"
"v_mfma_f32_16x16x32_bf16 v[184:187], a[212:215], v[164:167], v[184:187]\n"
"v_mfma_f32_16x16x32_bf16 v[184:187], a[216:219], v[172:175], v[184:187]\n"
"v_mfma_f32_16x16x32_bf16 v[184:187], a[220:223], v[152:155], v[184:187]\n"
"buffer_load_dwordx4 v[200:203], v1, s[4:7], s98 offen sc1\n"
"v_mfma_f32_16x16x32_bf16 v[96:99], a[252:255], v[152:155], v[96:99]\n"
"s_waitcnt lgkmcnt(0)\n"
"v_add_f32_e32 v168, v128, v168\n"
"v_add_f32_e32 v169, v129, v169\n"
"v_mfma_f32_16x16x32_bf16 v[96:99], a[248:251], v[172:175], v[96:99]\n"
"v_add_f32_e32 v170, v130, v170\n"
"v_add_f32_e32 v171, v131, v171\n"
"v_mfma_f32_16x16x32_bf16 v[96:99], a[244:247], v[164:167], v[96:99]\n"
"buffer_load_dwordx4 v[128:131], v1, s[4:7], s98 offen offset:1024 sc1\n"
"v_mfma_f32_16x16x32_bf16 v[96:99], a[240:243], v[156:159], v[96:99]\n"
"v_add_f32_e32 v176, v144, v176\n"
"v_add_f32_e32 v177, v145, v177\n"
"v_mfma_f32_16x16x32_bf16 v[96:99], a[236:239], v[132:135], v[96:99]\n"
"v_add_f32_e32 v178, v146, v178\n"
"v_add_f32_e32 v179, v147, v179\n"
"v_mfma_f32_16x16x32_bf16 v[96:99], a[232:235], v[120:123], v[96:99]\n"
"buffer_load_dwordx4 v[120:123], v1, s[4:7], s98 offen offset:2048 sc1\n"
"v_mfma_f32_16x16x32_bf16 v[96:99], a[228:231], v[124:127], v[96:99]\n"
"v_cvt_pk_bf16_f32 v124, v168, v169\n"
"v_cvt_pk_bf16_f32 v125, v170, v171\n"
"v_mfma_f32_16x16x32_bf16 v[96:99], a[224:227], v[160:163], v[96:99]\n"
"s_add_u32 s97, s61, 0x440000\n"
"v_cvt_pk_bf16_f32 v126, v176, v177\n"
"v_cvt_pk_bf16_f32 v127, v178, v179\n"
"buffer_store_dwordx4 v[124:127], v0, s[4:7], s97 offen sc1\n"
"ds_write_b128 v2, v[124:127] offset:8192\n"
"buffer_load_dwordx4 v[132:135], v1, s[4:7], s98 offen offset:3072 sc1\n"
"; @A fin b\n"
"s_add_u32 s98, s61, 0x440000\n"
"v_add_f32_e32 v188, v184, v188\n"
"v_add_f32_e32 v189, v185, v189\n"
"v_add_f32_e32 v190, v186, v190\n"
"v_add_f32_e32 v191, v187, v191\n"
"v_add_f32_e32 v192, v96, v192\n"
"v_add_f32_e32 v193, v97, v193\n"
"v_add_f32_e32 v194, v98, v194\n"
"v_add_f32_e32 v195, v99, v195\n"
"v_cvt_pk_bf16_f32 v96, v188, v189\n"
"v_cvt_pk_bf16_f32 v97, v190, v191\n"
"v_cvt_pk_bf16_f32 v98, v192, v193\n"
"v_cvt_pk_bf16_f32 v99, v194, v195\n"
"buffer_store_dwordx4 v[96:99], v0, s[4:7], s98 offen offset:1024 sc1\n"
"s_waitcnt vmcnt(16)\n"
"; @A G2 a.0\n"
"v_mfma_f32_16x16x32_bf16 v[144:147], a[128:131], v[100:103], 0\n"
"s_add_u32 s98, s58, 0\n"
"buffer_load_dwordx4 v[152:155], v0, s[4:7], s98 offen sc1\n"
"v_mfma_f32_16x16x32_bf16 v[144:147], a[132:135], v[180:183], v[144:147]\n"
"s_waitcnt vmcnt(15)\n"
"ds_write_b128 v2, v[96:99] offset:9216\n"
"v_mfma_f32_16x16x32_bf16 v[144:147], a[136:139], v[104:107], v[144:147]\n"
"v_mov_b32_e32 v156, 0x3f803f80\n"
"v_mov_b32_e32 v157, 0x3f803f80\n"
"v_mfma_f32_16x16x32_bf16 v[144:147], a[140:143], v[88:91], v[144:147]\n"
"s_waitcnt vmcnt(13)\n"
"v_mov_b32_e32 v158, 0x3f803f80\n"
"v_mov_b32_e32 v159, 0x3f803f80\n"
"v_mfma_f32_16x16x32_bf16 v[144:147], a[144:147], v[92:95], v[144:147]\n"
"buffer_load_dwordx4 v[160:163], v0, s[4:7], s98 offen offset:1024 sc1\n"
"v_mfma_f32_16x16x32_bf16 v[164:167], v[156:159], v[124:127], 0\n"
"v_mfma_f32_16x16x32_bf16 v[164:167], v[156:159], v[96:99], v[164:167]\n"
"v_mfma_f32_16x16x32_bf16 v[156:159], v[124:127], v[124:127], 0\n"
"v_mfma_f32_16x16x32_bf16 v[156:159], v[96:99], v[96:99], v[156:159]\n"
"v_mfma_f32_16x16x32_bf16 v[144:147], a[148:151], v[116:119], v[144:147]\n"
"s_waitcnt vmcnt(12)\n"
"v_mfma_f32_16x16x32_bf16 v[144:147], a[152:155], v[136:139], v[144:147]\n"
"v_mfma_f32_16x16x32_bf16 v[144:147], a[156:159], v[140:143], v[144:147]\n"
"v_mfma_f32_16x16x32_bf16 v[96:99], a[188:191], v[140:143], 0\n"
"buffer_load_dwordx4 v[124:127], v0, s[4:7], s98 offen offset:2048 sc1\n"
"v_mfma_f32_16x16x32_bf16 v[96:99], a[184:187], v[136:139], v[96:99]\n"
"s_mov_b32 s96, 0xaaaaaaaa\n"
"s_mov_b32 s97, 0xaaaaaaaa\n"
"v_mfma_f32_16x16x32_bf16 v[96:99], a[180:183], v[116:119], v[96:99]\n"
"v_mfma_f32_16x16x32_bf16 v[96:99], a[176:179], v[92:95], v[96:99]\n"
"v_mfma_f32_16x16x32_bf16 v[96:99], a[172:175], v[88:91], v[96:99]\n"
"v_cmp_u_f32_e32 vcc, v144, v144\n"
"v_cndmask_b32_e64 v156, v156, v157, s[96:97]\n"
"v_mfma_f32_16x16x32_bf16 v[96:99], a[168:171], v[104:107], v[96:99]\n"
"s_cbranch_vccnz .Lmk_p35s\n"
".Lmk_p35r:\n"
"v_cndmask_b32_e64 v158, v158, v159, s[96:97]\n"
"v_mfma_f32_16x16x32_bf16 v[96:99], a[164:167], v[180:183], v[96:99]\n"
"ds_write_b128 v3, v[144:147]\n"
"v_mfma_f32_16x16x32_bf16 v[96:99], a[160:163], v[100:103], v[96:99]\n"
"buffer_load_dwordx4 v[144:147], v0, s[4:7], s98 offen offset:3072 sc1\n"
"v_mfma_f32_16x16x32_bf16 v[168:171], a[192:195], v[100:103], 0\n"
"s_mov_b32 s96, 0xcccccccc\n"
"s_mov_b32 s97, 0xcccccccc\n"
"v_mfma_f32_16x16x32_bf16 v[168:171], a[196:199], v[180:183], v[168:171]\n"
"v_cndmask_b32_e64 v165, v156, v158, s[96:97]\n"
"v_mfma_f32_16x16x32_bf16 v[168:171], a[200:203], v[104:107], v[168:171]\n"
"s_mov_b32 s96, 0xf0000f\n"
"s_mov_b32 s97, 0xf0000f00\n"
"v_mfma_f32_16x16x32_bf16 v[168:171], a[204:207], v[88:91], v[168:171]\n"
"buffer_load_dwordx4 v[156:159], v1, s[4:7], s98 offen sc1\n"
"v_mfma_f32_16x16x32_bf16 v[168:171], a[208:211], v[92:95], v[168:171]\n"
"v_cndmask_b32_e64 v164, v164, v165, s[96:97]\n"
"s_add_u32 s97, s63, 0x44000\n"
"v_mfma_f32_16x16x32_bf16 v[168:171], a[212:215], v[116:119], v[168:171]\n"
"ds_write_b128 v3, v[96:99] offset:1024\n"
"v_mfma_f32_16x16x32_bf16 v[168:171], a[216:219], v[136:139], v[168:171]\n"
"buffer_store_dword v164, v14, s[4:7], s97 offen sc1\n"
"v_mfma_f32_16x16x32_bf16 v[168:171], a[220:223], v[140:143], v[168:171]\n"
"buffer_load_dwordx4 v[164:167], v1, s[4:7], s98 offen offset:1024 sc1\n"
"v_mfma_f32_16x16x32_bf16 v[96:99], a[252:255], v[140:143], 0\n"
"v_mfma_f32_16x16x32_bf16 v[96:99], a[248:251], v[136:139], v[96:99]\n"
"v_mfma_f32_16x16x32_bf16 v[96:99], a[244:247], v[116:119], v[96:99]\n"
"v_mfma_f32_16x16x32_bf16 v[96:99], a[240:243], v[92:95], v[96:99]\n"
"buffer_load_dwordx4 v[116:119], v1, s[4:7], s98 offen offset:2048 sc1\n"
"v_mfma_f32_16x16x32_bf16 v[96:99], a[236:239], v[88:91], v[96:99]\n"
"v_mfma_f32_16x16x32_bf16 v[96:99], a[232:235], v[104:107], v[96:99]\n"
"ds_write_b128 v3, v[168:171] offset:2048\n"
"v_mfma_f32_16x16x32_bf16 v[96:99], a[228:231], v[180:183], v[96:99]\n"
"v_mfma_f32_16x16x32_bf16 v[96:99], a[224:227], v[100:103], v[96:99]\n"
"s_waitcnt vmcnt(14)\n"
"buffer_load_dwordx4 v[136:139], v1, s[4:7], s98 offen offset:3072 sc1\n"
"; @A G2 a.1\n"
"v_mfma_f32_16x16x32_bf16 v[88:91], a[128:131], v[148:151], 0\n"
"v_mfma_f32_16x16x32_bf16 v[88:91], a[132:135], v[108:111], v[88:91]\n"
"s_add_u32 s98, s59, 0\n"
"v_mfma_f32_16x16x32_bf16 v[88:91], a[136:139], v[112:115], v[88:91]\n"
"v_mfma_f32_16x16x32_bf16 v[88:91], a[140:143], v[196:199], v[88:91]\n"
"s_waitcnt vmcnt(10)\n"
"buffer_load_dwordx4 v[172:175], v0, s[4:7], s98 offen sc1\n"
"v_mfma_f32_16x16x32_bf16 v[88:91], a[144:147], v[200:203], v[88:91]\n"
"v_mfma_f32_16x16x32_bf16 v[88:91], a[148:151], v[128:131], v[88:91]\n"
"ds_write_b128 v3, v[96:99] offset:3072\n"
"v_mfma_f32_16x16x32_bf16 v[88:91], a[152:155], v[120:123], v[88:91]\n"
"v_mfma_f32_16x16x32_bf16 v[88:91], a[156:159], v[132:135], v[88:91]\n"
"buffer_load_dwordx4 v[176:179], v0, s[4:7], s98 offen offset:1024 sc1\n"
"v_mfma_f32_16x16x32_bf16 v[92:95], a[188:191], v[132:135], 0\n"
"v_mfma_f32_16x16x32_bf16 v[92:95], a[184:187], v[120:123], v[92:95]\n"
"v_mfma_f32_16x16x32_bf16 v[92:95], a[180:183], v[128:131], v[92:95]\n"
"v_mfma_f32_16x16x32_bf16 v[92:95], a[176:179], v[200:203], v[92:95]\n"
"buffer_load_dwordx4 v[168:171], v0, s[4:7], s98 offen offset:2048 sc1\n"
"v_mfma_f32_16x16x32_bf16 v[92:95], a[172:175], v[196:199], v[92:95]\n"
"s_nop 0\n"
"v_cmp_u_f32_e32 vcc, v88, v88\n"
"v_mfma_f32_16x16x32_bf16 v[92:95], a[168:171], v[112:115], v[92:95]\n"
"s_cbranch_vccnz .Lmk_p36s\n"
".Lmk_p36r:\n"
"v_mfma_f32_16x16x32_bf16 v[92:95], a[164:167], v[108:111], v[92:95]\n"
"ds_write_b128 v3, v[88:91] offset:6144\n"
"v_mfma_f32_16x16x32_bf16 v[92:95], a[160:163], v[148:151], v[92:95]\n"
"buffer_load_dwordx4 v[180:183], v0, s[4:7], s98 offen offset:3072 sc1\n"
"v_mfma_f32_16x16x32_bf16 v[88:91], a[192:195], v[148:151], 0\n"
"v_mfma_f32_16x16x32_bf16 v[88:91], a[196:199], v[108:111], v[88:91]\n"
"v_mfma_f32_16x16x32_bf16 v[88:91], a[200:203], v[112:115], v[88:91]\n"
"v_mfma_f32_16x16x32_bf16 v[88:91], a[204:207], v[196:199], v[88:91]\n"
"buffer_load_dwordx4 v[184:187], v1, s[4:7], s98 offen sc1\n"
"v_mfma_f32_16x16x32_bf16 v[88:91], a[208:211], v[200:203], v[88:91]\n"
"ds_read_b128 v[96:99], v2\n"
"ds_read_b128 v[100:103], v2 offset:1024\n"
"v_mfma_f32_16x16x32_bf16 v[88:91], a[212:215], v[128:131], v[88:91]\n"
"ds_write_b128 v3, v[92:95] offset:7168\n"
"v_mfma_f32_16x16x32_bf16 v[88:91], a[216:219], v[120:123], v[88:91]\n"
"v_mfma_f32_16x16x32_bf16 v[88:91], a[220:223], v[132:135], v[88:91]\n"
"buffer_load_dwordx4 v[188:191], v1, s[4:7], s98 offen offset:1024 sc1\n"
"v_mfma_f32_16x16x32_bf16 v[92:95], a[252:255], v[132:135], 0\n"
"v_mfma_f32_16x16x32_bf16 v[92:95], a[248:251], v[120:123], v[92:95]\n"
"s_waitcnt lgkmcnt(1)\n"
"v_lshlrev_b32_e32 v255, 16, v96\n"
"v_and_b32_e32 v254, 0xffff0000, v96\n"
"v_mfma_f32_16x16x32_bf16 v[92:95], a[244:247], v[128:131], v[92:95]\n"
"v_lshlrev_b32_e32 v253, 16, v97\n"
"v_and_b32_e32 v252, 0xffff0000, v97\n"
"v_mfma_f32_16x16x32_bf16 v[92:95], a[240:243], v[200:203], v[92:95]\n"
"buffer_load_dwordx4 v[192:195], v1, s[4:7], s98 offen offset:2048 sc1\n"
"v_mfma_f32_16x16x32_bf16 v[92:95], a[236:239], v[196:199], v[92:95]\n"
"v_lshlrev_b32_e32 v251, 16, v98\n"
"v_and_b32_e32 v250, 0xffff0000, v98\n"
"v_mfma_f32_16x16x32_bf16 v[92:95], a[232:235], v[112:115], v[92:95]\n"
"ds_write_b128 v3, v[88:91] offset:8192\n"
"v_mfma_f32_16x16x32_bf16 v[92:95], a[228:231], v[108:111], v[92:95]\n"
"v_lshlrev_b32_e32 v249, 16, v99\n"
"v_and_b32_e32 v248, 0xffff0000, v99\n"
"v_mfma_f32_16x16x32_bf16 v[92:95], a[224:227], v[148:151], v[92:95]\n"
"s_waitcnt vmcnt(12)\n"
"buffer_load_dwordx4 v[196:199], v1, s[4:7], s98 offen offset:3072 sc1\n"
"; @A G2 a.2\n"
"v_mfma_f32_16x16x32_bf16 v[96:99], a[128:131], v[152:155], 0\n"
"v_lshlrev_b32_e32 v247, 16, v100\n"
"v_and_b32_e32 v246, 0xffff0000, v100\n"
"v_mfma_f32_16x16x32_bf16 v[96:99], a[132:135], v[160:163], v[96:99]\n"
"v_lshlrev_b32_e32 v245, 16, v101\n"
"v_and_b32_e32 v244, 0xffff0000, v101\n"
"v_mfma_f32_16x16x32_bf16 v[96:99], a[136:139], v[124:127], v[96:99]\n"
"v_lshlrev_b32_e32 v243, 16, v102\n"
"v_and_b32_e32 v242, 0xffff0000, v102\n"
"v_mfma_f32_16x16x32_bf16 v[96:99], a[140:143], v[144:147], v[96:99]\n"
"s_waitcnt vmcnt(8)\n"
"v_lshlrev_b32_e32 v241, 16, v103\n"
"v_and_b32_e32 v240, 0xffff0000, v103\n"
"v_mfma_f32_16x16x32_bf16 v[96:99], a[144:147], v[156:159], v[96:99]\n"
"v_mfma_f32_16x16x32_bf16 v[96:99], a[148:151], v[164:167], v[96:99]\n"
"ds_write_b128 v3, v[92:95] offset:9216\n"
"v_mfma_f32_16x16x32_bf16 v[96:99], a[152:155], v[116:119], v[96:99]\n"
"v_mfma_f32_16x16x32_bf16 v[96:99], a[156:159], v[136:139], v[96:99]\n"
"v_mfma_f32_16x16x32_bf16 v[104:107], a[188:191], v[136:139], 0\n"
"s_add_u32 s98, s52, 0x440000\n"
"buffer_load_dwordx4 v[88:91], v0, s[4:7], s98 offen sc1\n"
"v_mfma_f32_16x16x32_bf16 v[104:107], a[184:187], v[116:119], v[104:107]\n"
"s_waitcnt lgkmcnt(0)\n"
"s_barrier\n"
"v_mfma_f32_16x16x32_bf16 v[104:107], a[180:183], v[164:167], v[104:107]\n"
"ds_read_b128 v[140:143], v4\n"
"ds_read_b128 v[112:115], v5\n"
"v_mfma_f32_16x16x32_bf16 v[104:107], a[176:179], v[156:159], v[104:107]\n"
"buffer_load_dwordx4 v[92:95], v0, s[4:7], s98 offen offset:1024 sc1\n"
"v_mfma_f32_16x16x32_bf16 v[104:107], a[172:175], v[144:147], v[104:107]\n"
"v_cmp_u_f32_e32 vcc, v96, v96\n"
"v_mfma_f32_16x16x32_bf16 v[104:107], a[168:171], v[124:127], v[104:107]\n"
"s_cbranch_vccnz .Lmk_p37s\n"
".Lmk_p37r:\n"
"v_mfma_f32_16x16x32_bf16 v[104:107], a[164:167], v[160:163], v[104:107]\n"
"ds_write_b128 v3, v[96:99] offset:12288\n"
"v_mfma_f32_16x16x32_bf16 v[104:107], a[160:163], v[152:155], v[104:107]\n"
"buffer_load_dwordx4 v[96:99], v0, s[4:7], s98 offen offset:2048 sc1\n"
"v_mfma_f32_16x16x32_bf16 v[120:123], a[192:195], v[152:155], 0\n"
"ds_read_b128 v[148:151], v4 offset:1024\n"
"ds_read_b128 v[128:131], v5 offset:1024\n"
"v_mfma_f32_16x16x32_bf16 v[120:123], a[196:199], v[160:163], v[120:123]\n"
"s_waitcnt lgkmcnt(3)\n"
"v_add_f32_e32 v140, v140, v255\n"
"v_add_f32_e32 v141, v141, v254\n"
"v_mfma_f32_16x16x32_bf16 v[120:123], a[200:203], v[124:127], v[120:123]\n"
"buffer_load_dwordx4 v[100:103], v0, s[4:7], s98 offen offset:3072 sc1\n"
"v_mfma_f32_16x16x32_bf16 v[120:123], a[204:207], v[144:147], v[120:123]\n"
"v_add_f32_e32 v142, v142, v253\n"
"v_add_f32_e32 v143, v143, v252\n"
"v_mfma_f32_16x16x32_bf16 v[120:123], a[208:211], v[156:159], v[120:123]\n"
"v_add_f32_e32 v140, v140, v112\n"
"v_add_f32_e32 v141, v141, v113\n"
"v_mfma_f32_16x16x32_bf16 v[120:123], a[212:215], v[164:167], v[120:123]\n"
"ds_write_b128 v3, v[104:107] offset:13312\n"
"v_mfma_f32_16x16x32_bf16 v[120:123], a[216:219], v[116:119], v[120:123]\n"
"buffer_load_dwordx4 v[104:107], v1, s[4:7], s98 offen sc1\n"
"v_mfma_f32_16x16x32_bf16 v[120:123], a[220:223], v[136:139], v[120:123]\n"
"ds_read_b128 v[108:111], v4 offset:2048\n"
"ds_read_b128 v[200:203], v5 offset:2048\n"
"v_mfma_f32_16x16x32_bf16 v[132:135], a[252:255], v[136:139], 0\n"
"s_waitcnt lgkmcnt(3)\n"
"v_add_f32_e32 v142, v142, v114\n"
"v_add_f32_e32 v143, v143, v115\n"
"v_mfma_f32_16x16x32_bf16 v[132:135], a[248:251], v[116:119], v[132:135]\n"
"buffer_load_dwordx4 v[112:115], v1, s[4:7], s98 offen offset:1024 sc1\n"
"v_mfma_f32_16x16x32_bf16 v[132:135], a[244:247], v[164:167], v[132:135]\n"
"v_add_f32_e32 v148, v148, v251\n"
"v_add_f32_e32 v149, v149, v250\n"
"v_mfma_f32_16x16x32_bf16 v[132:135], a[240:243], v[156:159], v[132:135]\n"
"v_add_f32_e32 v150, v150, v249\n"
"v_add_f32_e32 v151, v151, v248\n"
"v_mfma_f32_16x16x32_bf16 v[132:135], a[236:239], v[144:147], v[132:135]\n"
"buffer_load_dwordx4 v[116:119], v1, s[4:7], s98 offen offset:2048 sc1\n"
"v_mfma_f32_16x16x32_bf16 v[132:135], a[232:235], v[124:127], v[132:135]\n"
"ds_write_b128 v3, v[120:123] offset:14336\n"
"v_mfma_f32_16x16x32_bf16 v[132:135], a[228:231], v[160:163], v[132:135]\n"
"ds_read_b128 v[120:123], v4 offset:3072\n"
"ds_read_b128 v[144:147], v5 offset:3072\n"
"v_mfma_f32_16x16x32_bf16 v[132:135], a[224:227], v[152:155], v[132:135]\n"
"s_waitcnt vmcnt(11)\n"
"buffer_load_dwordx4 v[124:127], v1, s[4:7], s98 offen offset:3072 sc1\n"
"; @A G2 a.3\n"
"v_mfma_f32_16x16x32_bf16 v[164:167], a[128:131], v[172:175], v[140:143]\n"
"s_add_u32 s98, s53, 0x440000\n"
"v_add_f32_e32 v148, v148, v128\n"
"v_add_f32_e32 v149, v149, v129\n"
"v_mfma_f32_16x16x32_bf16 v[164:167], a[132:135], v[176:179], v[164:167]\n"
"s_waitcnt lgkmcnt(3)\n"
"v_add_f32_e32 v150, v150, v130\n"
"v_add_f32_e32 v151, v151, v131\n"
"v_mfma_f32_16x16x32_bf16 v[164:167], a[136:139], v[168:171], v[164:167]\n"
"buffer_load_dwordx4 v[128:131], v0, s[4:7], s98 offen sc1\n"
"v_mfma_f32_16x16x32_bf16 v[164:167], a[140:143], v[180:183], v[164:167]\n"
"s_waitcnt vmcnt(9)\n"
"v_add_f32_e32 v108, v108, v247\n"
"v_add_f32_e32 v109, v109, v246\n"
"v_mfma_f32_16x16x32_bf16 v[164:167], a[144:147], v[184:187], v[164:167]\n"
"v_add_f32_e32 v110, v110, v245\n"
"v_add_f32_e32 v111, v111, v244\n"
"v_mfma_f32_16x16x32_bf16 v[164:167], a[148:151], v[188:191], v[164:167]\n"
"ds_write_b128 v3, v[132:135] offset:15360\n"
"v_mfma_f32_16x16x32_bf16 v[164:167], a[152:155], v[192:195], v[164:167]\n"
"buffer_load_dwordx4 v[132:135], v0, s[4:7], s98 offen offset:1024 sc1\n"
"v_mfma_f32_16x16x32_bf16 v[164:167], a[156:159], v[196:199], v[164:167]\n"
"v_add_f32_e32 v108, v108, v200\n"
"v_add_f32_e32 v109, v109, v201\n"
"v_mfma_f32_16x16x32_bf16 v[204:207], a[188:191], v[196:199], v[148:151]\n"
"s_waitcnt lgkmcnt(1)\n"
"v_add_f32_e32 v110, v110, v202\n"
"v_add_f32_e32 v111, v111, v203\n"
"v_mfma_f32_16x16x32_bf16 v[204:207], a[184:187], v[192:195], v[204:207]\n"
"s_waitcnt lgkmcnt(0)\n"
"s_barrier\n"
"v_mfma_f32_16x16x32_bf16 v[204:207], a[180:183], v[188:191], v[204:207]\n"
"buffer_load_dwordx4 v[136:139], v0, s[4:7], s98 offen offset:2048 sc1\n"
"v_mfma_f32_16x16x32_bf16 v[204:207], a[176:179], v[184:187], v[204:207]\n"
"v_add_f32_e32 v120, v120, v243\n"
"v_add_f32_e32 v121, v121, v242\n"
"v_mfma_f32_16x16x32_bf16 v[204:207], a[172:175], v[180:183], v[204:207]\n"
"v_cmp_u_f32_e32 vcc, v164, v164\n"
"v_add_f32_e32 v122, v122, v241\n"
"v_mfma_f32_16x16x32_bf16 v[204:207], a[168:171], v[168:171], v[204:207]\n"
"s_cbranch_vccnz .Lmk_p38s\n"
".Lmk_p38r:\n"
"v_add_f32_e32 v123, v123, v240\n"
"v_mfma_f32_16x16x32_bf16 v[204:207], a[164:167], v[176:179], v[204:207]\n"
"buffer_load_dwordx4 v[140:143], v0, s[4:7], s98 offen offset:3072 sc1\n"
"v_mfma_f32_16x16x32_bf16 v[204:207], a[160:163], v[172:175], v[204:207]\n"
"v_add_f32_e32 v120, v120, v144\n"
"v_add_f32_e32 v121, v121, v145\n"
"v_mfma_f32_16x16x32_bf16 v[108:111], a[192:195], v[172:175], v[108:111]\n"
"v_add_f32_e32 v122, v122, v146\n"
"v_add_f32_e32 v123, v123, v147\n"
"v_mfma_f32_16x16x32_bf16 v[108:111], a[196:199], v[176:179], v[108:111]\n"
"buffer_load_dwordx4 v[144:147], v1, s[4:7], s98 offen sc1\n"
"v_mfma_f32_16x16x32_bf16 v[108:111], a[200:203], v[168:171], v[108:111]\n"
"ds_read_b128 v[200:203], v6\n"
"ds_read_b128 v[208:211], v6 offset:1024\n"
"v_mfma_f32_16x16x32_bf16 v[108:111], a[204:207], v[180:183], v[108:111]\n"
"ds_read_b128 v[148:151], v6 offset:2048\n"
"ds_read_b128 v[152:155], v6 offset:3072\n"
"v_mfma_f32_16x16x32_bf16 v[108:111], a[208:211], v[184:187], v[108:111]\n"
"buffer_load_dwordx4 v[156:159], v1, s[4:7], s98 offen offset:1024 sc1\n"
"v_mfma_f32_16x16x32_bf16 v[108:111], a[212:215], v[188:191], v[108:111]\n"
"v_mfma_f32_16x16x32_bf16 v[108:111], a[216:219], v[192:195], v[108:111]\n"
"v_mfma_f32_16x16x32_bf16 v[108:111], a[220:223], v[196:199], v[108:111]\n"
"buffer_load_dwordx4 v[160:163], v1, s[4:7], s98 offen offset:2048 sc1\n"
"v_mfma_f32_16x16x32_bf16 v[120:123], a[252:255], v[196:199], v[120:123]\n"
"s_waitcnt lgkmcnt(0)\n"
"v_add_f32_e32 v200, v164, v200\n"
"v_add_f32_e32 v201, v165, v201\n"
"v_mfma_f32_16x16x32_bf16 v[120:123], a[248:251], v[192:195], v[120:123]\n"
"v_add_f32_e32 v202, v166, v202\n"
"v_add_f32_e32 v203, v167, v203\n"
"v_mfma_f32_16x16x32_bf16 v[120:123], a[244:247], v[188:191], v[120:123]\n"
"buffer_load_dwordx4 v[164:167], v1, s[4:7], s98 offen offset:3072 sc1\n"
"v_mfma_f32_16x16x32_bf16 v[120:123], a[240:243], v[184:187], v[120:123]\n"
"v_add_f32_e32 v208, v204, v208\n"
"v_add_f32_e32 v209, v205, v209\n"
"v_mfma_f32_16x16x32_bf16 v[120:123], a[236:239], v[180:183], v[120:123]\n"
"v_add_f32_e32 v210, v206, v210\n"
"v_add_f32_e32 v211, v207, v211\n"
"v_mfma_f32_16x16x32_bf16 v[120:123], a[232:235], v[168:171], v[120:123]\n"
"v_cvt_pk_bf16_f32 v168, v200, v201\n"
"v_cvt_pk_bf16_f32 v169, v202, v203\n"
"v_mfma_f32_16x16x32_bf16 v[120:123], a[228:231], v[176:179], v[120:123]\n"
"s_add_u32 s98, s61, 0x400000\n"
"v_cvt_pk_bf16_f32 v170, v208, v209\n"
"v_cvt_pk_bf16_f32 v171, v210, v211\n"
"v_mfma_f32_16x16x32_bf16 v[120:123], a[224:227], v[172:175], v[120:123]\n"
"buffer_store_dwordx4 v[168:171], v0, s[4:7], s98 offen sc1\n"
"ds_write_b128 v2, v[168:171]\n"
".Lmk_loop:\n"
".Lmk_b0:\n"
"s_waitcnt vmcnt(13)\n"
"; @B G1 0.0\n"
"v_mfma_f32_16x16x32_bf16 v[172:175], a[0:3], v[88:91], 0\n"
"v_mfma_f32_16x16x32_bf16 v[172:175], a[4:7], v[92:95], v[172:175]\n"
"s_add_u32 s98, s62, 0x44000\n"
"v_mfma_f32_16x16x32_bf16 v[172:175], a[8:11], v[96:99], v[172:175]\n"
"s_add_u32 s97, s54, 0x440000\n"
"buffer_load_dwordx4 v[176:179], v0, s[4:7], s97 offen sc1\n"
"v_mfma_f32_16x16x32_bf16 v[172:175], a[12:15], v[100:103], v[172:175]\n"
"s_waitcnt vmcnt(10)\n"
"v_mfma_f32_16x16x32_bf16 v[172:175], a[16:19], v[104:107], v[172:175]\n"
"buffer_load_dword v255, v10, s[4:7], s98 offen sc1\n"
"v_mfma_f32_16x16x32_bf16 v[172:175], a[20:23], v[112:115], v[172:175]\n"
"v_add_f32_e32 v148, v108, v148\n"
"v_add_f32_e32 v149, v109, v149\n"
"v_mfma_f32_16x16x32_bf16 v[172:175], a[24:27], v[116:119], v[172:175]\n"
"v_add_f32_e32 v150, v110, v150\n"
"v_add_f32_e32 v151, v111, v151\n"
"v_mfma_f32_16x16x32_bf16 v[172:175], a[28:31], v[124:127], v[172:175]\n"
"buffer_load_dwordx4 v[108:111], v0, s[4:7], s97 offen offset:1024 sc1\n"
"v_mfma_f32_16x16x32_bf16 v[180:183], a[60:63], v[124:127], 0\n"
"buffer_load_dword v254, v10, s[4:7], s98 offen offset:256 sc1\n"
"v_mfma_f32_16x16x32_bf16 v[180:183], a[56:59], v[116:119], v[180:183]\n"
"v_add_f32_e32 v152, v120, v152\n"
"v_add_f32_e32 v153, v121, v153\n"
"v_mfma_f32_16x16x32_bf16 v[180:183], a[52:55], v[112:115], v[180:183]\n"
"v_add_f32_e32 v154, v122, v154\n"
"v_add_f32_e32 v155, v123, v155\n"
"v_mfma_f32_16x16x32_bf16 v[180:183], a[48:51], v[104:107], v[180:183]\n"
"buffer_load_dword v253, v10, s[4:7], s98 offen offset:512 sc1\n"
"v_mfma_f32_16x16x32_bf16 v[180:183], a[44:47], v[100:103], v[180:183]\n"
"v_cmp_u_f32_e32 vcc, v172, v172\n"
"v_cvt_pk_bf16_f32 v120, v148, v149\n"
"v_mfma_f32_16x16x32_bf16 v[180:183], a[40:43], v[96:99], v[180:183]\n"
"s_cbranch_vccnz .Lmk_p43s\n"
".Lmk_p43r:\n"
"v_cvt_pk_bf16_f32 v121, v150, v151\n"
"v_mfma_f32_16x16x32_bf16 v[180:183], a[36:39], v[92:95], v[180:183]\n"
"buffer_load_dwordx4 v[148:151], v0, s[4:7], s97 offen offset:2048 sc1\n"
"v_mfma_f32_16x16x32_bf16 v[180:183], a[32:35], v[88:91], v[180:183]\n"
"ds_write_b128 v3, v[172:175]\n"
"v_mfma_f32_16x16x32_bf16 v[172:175], a[64:67], v[88:91], 0\n"
"buffer_load_dword v252, v10, s[4:7], s98 offen offset:768 sc1\n"
"v_mfma_f32_16x16x32_bf16 v[172:175], a[68:71], v[92:95], v[172:175]\n"
"buffer_load_dwordx4 v[184:187], v0, s[4:7], s97 offen offset:3072 sc1\n"
"v_mfma_f32_16x16x32_bf16 v[172:175], a[72:75], v[96:99], v[172:175]\n"
"s_add_u32 s96, s61, 0x400000\n"
"v_cvt_pk_bf16_f32 v122, v152, v153\n"
"v_cvt_pk_bf16_f32 v123, v154, v155\n"
"v_mfma_f32_16x16x32_bf16 v[172:175], a[76:79], v[100:103], v[172:175]\n"
"buffer_load_dword v251, v10, s[4:7], s98 offen offset:1024 sc1\n"
"v_mfma_f32_16x16x32_bf16 v[172:175], a[80:83], v[104:107], v[172:175]\n"
"buffer_store_dwordx4 v[120:123], v0, s[4:7], s96 offen offset:1024 sc1\n"
"v_mfma_f32_16x16x32_bf16 v[172:175], a[84:87], v[112:115], v[172:175]\n"
"ds_write_b128 v3, v[180:183] offset:1024\n"
"v_mfma_f32_16x16x32_bf16 v[172:175], a[88:91], v[116:119], v[172:175]\n"
"buffer_load_dwordx4 v[152:155], v1, s[4:7], s97 offen sc1\n"
"v_mfma_f32_16x16x32_bf16 v[172:175], a[92:95], v[124:127], v[172:175]\n"
"buffer_load_dword v250, v10, s[4:7], s98 offen offset:1280 sc1\n"
"v_mfma_f32_16x16x32_bf16 v[180:183], a[124:127], v[124:127], 0\n"
"ds_write_b128 v2, v[120:123] offset:1024\n"
"v_mfma_f32_16x16x32_bf16 v[180:183], a[120:123], v[116:119], v[180:183]\n"
"v_mov_b32_e32 v188, 0x3f803f80\n"
"v_mov_b32_e32 v189, 0x3f803f80\n"
"v_mfma_f32_16x16x32_bf16 v[180:183], a[116:119], v[112:115], v[180:183]\n"
"buffer_load_dword v249, v10, s[4:7], s98 offen offset:1536 sc1\n"
"v_mfma_f32_16x16x32_bf16 v[180:183], a[112:115], v[104:107], v[180:183]\n"
"buffer_load_dwordx4 v[192:195], v1, s[4:7], s97 offen offset:1024 sc1\n"
"v_mfma_f32_16x16x32_bf16 v[180:183], a[108:111], v[100:103], v[180:183]\n"
"v_mov_b32_e32 v190, 0x3f803f80\n"
"v_mov_b32_e32 v191, 0x3f803f80\n"
"v_mfma_f32_16x16x32_bf16 v[180:183], a[104:107], v[96:99], v[180:183]\n"
"ds_write_b128 v3, v[172:175] offset:2048\n"
"v_mfma_f32_16x16x32_bf16 v[172:175], v[188:191], v[168:171], 0\n"
"v_mfma_f32_16x16x32_bf16 v[172:175], v[188:191], v[120:123], v[172:175]\n"
"v_mfma_f32_16x16x32_bf16 v[188:191], v[168:171], v[168:171], 0\n"
"v_mfma_f32_16x16x32_bf16 v[188:191], v[120:123], v[120:123], v[188:191]\n"
"v_mfma_f32_16x16x32_bf16 v[180:183], a[100:103], v[92:95], v[180:183]\n"
"v_mov_b32_e32 v120, -1\n"
"v_mov_b32_e32 v121, -1\n"
"v_mfma_f32_16x16x32_bf16 v[180:183], a[96:99], v[88:91], v[180:183]\n"
"v_mov_b32_e32 v122, -1\n"
"v_mov_b32_e32 v123, -1\n"
"v_mfma_f32_16x16x32_bf16 v[168:171], v[24:27], v[88:91], 0\n"
"buffer_load_dwordx4 v[196:199], v1, s[4:7], s97 offen offset:2048 sc1\n"
"v_mfma_f32_16x16x32_bf16 v[168:171], v[28:31], v[92:95], v[168:171]\n"
"s_add_u32 s96, s60, 0x448000\n"
"buffer_store_dwordx4 v[120:123], v0, s[4:7], s96 offen sc1\n"
"v_mfma_f32_16x16x32_bf16 v[168:171], v[32:35], v[96:99], v[168:171]\n"
"buffer_load_dword v248, v10, s[4:7], s98 offen offset:1792 sc1\n"
"v_mfma_f32_16x16x32_bf16 v[168:171], v[36:39], v[100:103], v[168:171]\n"
"s_mov_b32 s94, 0xaaaaaaaa\n"
"s_mov_b32 s95, 0xaaaaaaaa\n"
"v_mfma_f32_16x16x32_bf16 v[168:171], v[40:43], v[104:107], v[168:171]\n"
"v_mfma_f32_16x16x32_bf16 v[168:171], v[44:47], v[112:115], v[168:171]\n"
"buffer_load_dwordx4 v[200:203], v1, s[4:7], s97 offen offset:3072 sc1\n"
"v_mfma_f32_16x16x32_bf16 v[168:171], v[48:51], v[116:119], v[168:171]\n"
"ds_write_b128 v3, v[180:183] offset:3072\n"
"v_mfma_f32_16x16x32_bf16 v[168:171], v[52:55], v[124:127], v[168:171]\n"
"v_cndmask_b32_e64 v188, v188, v189, s[94:95]\n"
"v_cndmask_b32_e64 v190, v190, v191, s[94:95]\n"
"v_mfma_f32_16x16x32_bf16 v[180:183], v[84:87], v[124:127], 0\n"
"s_mov_b32 s94, 0xcccccccc\n"
"s_mov_b32 s95, 0xcccccccc\n"
"v_mfma_f32_16x16x32_bf16 v[180:183], v[80:83], v[116:119], v[180:183]\n"
"v_cndmask_b32_e64 v173, v188, v190, s[94:95]\n"
"v_mfma_f32_16x16x32_bf16 v[180:183], v[76:79], v[112:115], v[180:183]\n"
"s_mov_b32 s94, 0xf0000f\n"
"s_mov_b32 s95, 0xf0000f00\n"
"v_mfma_f32_16x16x32_bf16 v[180:183], v[72:75], v[104:107], v[180:183]\n"
"s_add_u32 s98, s63, 0x40000\n"
"v_cndmask_b32_e64 v172, v172, v173, s[94:95]\n"
"v_mfma_f32_16x16x32_bf16 v[180:183], v[68:71], v[100:103], v[180:183]\n"
"s_add_u32 s97, s70, 0x200000\n"
"buffer_load_dwordx4 a[128:131], v0, s[4:7], s97 offen sc1\n"
"v_mfma_f32_16x16x32_bf16 v[180:183], v[64:67], v[96:99], v[180:183]\n"
"ds_write_b128 v3, v[168:171] offset:4096\n"
"v_mfma_f32_16x16x32_bf16 v[180:183], v[60:63], v[92:95], v[180:183]\n"
"buffer_store_dword v172, v14, s[4:7], s98 offen sc1\n"
"v_mfma_f32_16x16x32_bf16 v[180:183], v[56:59], v[88:91], v[180:183]\n"
"s_waitcnt vmcnt(25)\n"
"; @B G1 0.1\n"
"v_mfma_f32_16x16x32_bf16 v[88:91], a[0:3], v[128:131], 0\n"
"v_mfma_f32_16x16x32_bf16 v[88:91], a[4:7], v[132:135], v[88:91]\n"
"v_mfma_f32_16x16x32_bf16 v[88:91], a[8:11], v[136:139], v[88:91]\n"
"s_add_u32 s98, s55, 0x440000\n"
"buffer_load_dwordx4 v[92:95], v0, s[4:7], s98 offen sc1\n"
"v_mfma_f32_16x16x32_bf16 v[88:91], a[12:15], v[140:143], v[88:91]\n"
"s_waitcnt vmcnt(22)\n"
"v_mfma_f32_16x16x32_bf16 v[88:91], a[16:19], v[144:147], v[88:91]\n"
"v_mfma_f32_16x16x32_bf16 v[88:91], a[20:23], v[156:159], v[88:91]\n"
"ds_write_b128 v3, v[180:183] offset:5120\n"
"v_mfma_f32_16x16x32_bf16 v[88:91], a[24:27], v[160:163], v[88:91]\n"
"v_mfma_f32_16x16x32_bf16 v[88:91], a[28:31], v[164:167], v[88:91]\n"
"buffer_load_dwordx4 v[96:99], v0, s[4:7], s98 offen offset:1024 sc1\n"
"v_mfma_f32_16x16x32_bf16 v[100:103], a[60:63], v[164:167], 0\n"
"v_mfma_f32_16x16x32_bf16 v[100:103], a[56:59], v[160:163], v[100:103]\n"
"v_mfma_f32_16x16x32_bf16 v[100:103], a[52:55], v[156:159], v[100:103]\n"
"v_mfma_f32_16x16x32_bf16 v[100:103], a[48:51], v[144:147], v[100:103]\n"
"v_mfma_f32_16x16x32_bf16 v[100:103], a[44:47], v[140:143], v[100:103]\n"
"s_nop 1\n"
"v_cmp_u_f32_e32 vcc, v88, v88\n"
"v_mfma_f32_16x16x32_bf16 v[100:103], a[40:43], v[136:139], v[100:103]\n"
"s_cbranch_vccnz .Lmk_p44s\n"
".Lmk_p44r:\n"
"v_mfma_f32_16x16x32_bf16 v[100:103], a[36:39], v[132:135], v[100:103]\n"
"buffer_load_dwordx4 v[104:107], v0, s[4:7], s98 offen offset:2048 sc1\n"
"v_mfma_f32_16x16x32_bf16 v[100:103], a[32:35], v[128:131], v[100:103]\n"
"ds_write_b128 v3, v[88:91] offset:6144\n"
"v_mfma_f32_16x16x32_bf16 v[88:91], a[64:67], v[128:131], 0\n"
"buffer_load_dwordx4 a[132:135], v0, s[4:7], s97 offen offset:1024 sc1\n"
"v_mfma_f32_16x16x32_bf16 v[88:91], a[68:71], v[132:135], v[88:91]\n"
"buffer_load_dwordx4 v[112:115], v0, s[4:7], s98 offen offset:3072 sc1\n"
"v_mfma_f32_16x16x32_bf16 v[88:91], a[72:75], v[136:139], v[88:91]\n"
"v_mfma_f32_16x16x32_bf16 v[88:91], a[76:79], v[140:143], v[88:91]\n"
"v_mfma_f32_16x16x32_bf16 v[88:91], a[80:83], v[144:147], v[88:91]\n"
"v_mfma_f32_16x16x32_bf16 v[88:91], a[84:87], v[156:159], v[88:91]\n"
"ds_write_b128 v3, v[100:103] offset:7168\n"
"v_mfma_f32_16x16x32_bf16 v[88:91], a[88:91], v[160:163], v[88:91]\n"
"buffer_load_dwordx4 v[100:103], v1, s[4:7], s98 offen sc1\n"
"v_mfma_f32_16x16x32_bf16 v[88:91], a[92:95], v[164:167], v[88:91]\n"
"v_mfma_f32_16x16x32_bf16 v[116:119], a[124:127], v[164:167], 0\n"
"v_mfma_f32_16x16x32_bf16 v[116:119], a[120:123], v[160:163], v[116:119]\n"
"v_mfma_f32_16x16x32_bf16 v[116:119], a[116:119], v[156:159], v[116:119]\n"
"v_mfma_f32_16x16x32_bf16 v[116:119], a[112:115], v[144:147], v[116:119]\n"
"buffer_load_dwordx4 v[124:127], v1, s[4:7], s98 offen offset:1024 sc1\n"
"v_mfma_f32_16x16x32_bf16 v[116:119], a[108:111], v[140:143], v[116:119]\n"
"v_mfma_f32_16x16x32_bf16 v[116:119], a[104:107], v[136:139], v[116:119]\n"
"s_nop 0\n"
"ds_write_b128 v3, v[88:91] offset:8192\n"
"v_mfma_f32_16x16x32_bf16 v[116:119], a[100:103], v[132:135], v[116:119]\n"
"v_mfma_f32_16x16x32_bf16 v[116:119], a[96:99], v[128:131], v[116:119]\n"
"v_mfma_f32_16x16x32_bf16 v[88:91], v[24:27], v[128:131], 0\n"
"buffer_load_dwordx4 v[168:171], v1, s[4:7], s98 offen offset:2048 sc1\n"
"v_mfma_f32_16x16x32_bf16 v[88:91], v[28:31], v[132:135], v[88:91]\n"
"s_add_u32 s96, s60, 0x440000\n"
"v_mfma_f32_16x16x32_bf16 v[88:91], v[32:35], v[136:139], v[88:91]\n"
"v_mfma_f32_16x16x32_bf16 v[88:91], v[36:39], v[140:143], v[88:91]\n"
"buffer_store_dwordx4 v[120:123], v0, s[4:7], s96 offen sc1\n"
"v_mfma_f32_16x16x32_bf16 v[88:91], v[40:43], v[144:147], v[88:91]\n"
"buffer_load_dwordx4 a[136:139], v0, s[4:7], s97 offen offset:2048 sc1\n"
"v_mfma_f32_16x16x32_bf16 v[88:91], v[44:47], v[156:159], v[88:91]\n"
"buffer_load_dwordx4 v[172:175], v1, s[4:7], s98 offen offset:3072 sc1\n"
"v_mfma_f32_16x16x32_bf16 v[88:91], v[48:51], v[160:163], v[88:91]\n"
"ds_write_b128 v3, v[116:119] offset:9216\n"
"v_mfma_f32_16x16x32_bf16 v[88:91], v[52:55], v[164:167], v[88:91]\n"
"s_waitcnt vmcnt(14)\n"
"v_add_f32_e32 v255, v255, v251\n"
"v_add_f32_e32 v254, v254, v250\n"
"v_mfma_f32_16x16x32_bf16 v[116:119], v[84:87], v[164:167], 0\n"
"v_add_f32_e32 v253, v253, v249\n"
"v_add_f32_e32 v252, v252, v248\n"
"v_mfma_f32_16x16x32_bf16 v[116:119], v[80:83], v[160:163], v[116:119]\n"
"v_add_f32_e32 v255, v255, v253\n"
"v_add_f32_e32 v254, v254, v252\n"
"v_mfma_f32_16x16x32_bf16 v[116:119], v[76:79], v[156:159], v[116:119]\n"
"v_add_f32_e32 v255, v255, v254\n"
"v_cmp_u_f32_e32 vcc, v255, v255\n"
"v_mfma_f32_16x16x32_bf16 v[116:119], v[72:75], v[144:147], v[116:119]\n"
"s_cbranch_vccnz .Lmk_m47s\n"
".Lmk_m47r:\n"
"v_mov_b32_e32 v254, v255\n"
"v_mfma_f32_16x16x32_bf16 v[116:119], v[68:71], v[140:143], v[116:119]\n"
"s_nop 0\n"
"v_permlane32_swap_b32_e32 v255, v254\n"
"v_add_f32_e32 v255, v255, v254\n"
"v_mfma_f32_16x16x32_bf16 v[116:119], v[64:67], v[136:139], v[116:119]\n"
"ds_write_b128 v3, v[88:91] offset:10240\n"
"v_mfma_f32_16x16x32_bf16 v[116:119], v[60:63], v[132:135], v[116:119]\n"
"v_mul_f32_e32 v255, 0x3a800000, v255\n"
"v_mov_b32_e32 v254, v255\n"
"v_mfma_f32_16x16x32_bf16 v[116:119], v[56:59], v[128:131], v[116:119]\n"
"s_nop 0\n"
"v_permlane16_swap_b32_e32 v255, v254\n"
"v_cvt_pk_bf16_f32 v253, -v255, 0\n"
"; @B G1 0.2\n"
"v_mfma_f32_16x16x32_bf16 v[88:91], a[0:3], v[176:179], 0\n"
"v_fma_f32 v254, -v255, v255, v254\n"
"v_lshlrev_b32_e32 v253, 16, v253\n"
"v_mfma_f32_16x16x32_bf16 v[88:91], a[4:7], v[108:111], v[88:91]\n"
"v_max_f32_e32 v254, 0, v254\n"
"v_cvt_pk_bf16_f32 v254, v254, 0\n"
"v_mfma_f32_16x16x32_bf16 v[88:91], a[8:11], v[148:151], v[88:91]\n"
"v_lshlrev_b32_e32 v254, 16, v254\n"
"v_add_f32_e32 v254, 0x3727c5ac, v254\n"
"v_mfma_f32_16x16x32_bf16 v[88:91], a[12:15], v[184:187], v[88:91]\n"
"s_waitcnt vmcnt(13)\n"
"v_cvt_pk_bf16_f32 v254, v254, 0\n"
"v_lshlrev_b32_e32 v254, 16, v254\n"
"v_mfma_f32_16x16x32_bf16 v[88:91], a[16:19], v[152:155], v[88:91]\n"
"s_mul_i32 s98, s0, .Lmk_exit-.Lmk_b2\n"
"s_add_u32 s98, s98, .Lmk_b2-.Lmk_g42\n"
"s_getpc_b64 s[84:85]\n"
".Lmk_g42:\n"
"s_add_u32 s84, s84, s98\n"
"s_addc_u32 s85, s85, 0\n"
"v_mfma_f32_16x16x32_bf16 v[88:91], a[20:23], v[192:195], v[88:91]\n"
"ds_write_b128 v3, v[116:119] offset:11264\n"
"v_mfma_f32_16x16x32_bf16 v[88:91], a[24:27], v[196:199], v[88:91]\n"
"s_and_b32 s84, s84, -16\n"
"s_and_b32 s85, s85, 0xffff\n"
"s_sub_u32 s98, .Lmk_end-.Lmk_g42, s98\n"
"s_min_u32 s86, s98, 0x4000\n"
"s_mov_b32 s87, 0x20000\n"
"v_mfma_f32_16x16x32_bf16 v[88:91], a[28:31], v[200:203], v[88:91]\n"
"s_mov_b32 m0, 0x27800\n"
"s_nop 0\n"
"buffer_load_dwordx4 v0, s[84:87], s99 offen lds\n"
"v_mfma_f32_16x16x32_bf16 v[116:119], a[60:63], v[200:203], 0\n"
"buffer_load_dwordx4 a[140:143], v0, s[4:7], s97 offen offset:3072 sc1\n"
"v_mfma_f32_16x16x32_bf16 v[116:119], a[56:59], v[196:199], v[116:119]\n"
"v_sqrt_f32_e32 v254, v254\n"
"v_mfma_f32_16x16x32_bf16 v[116:119], a[52:55], v[192:195], v[116:119]\n"
"s_add_u32 s98, s52, 0x448000\n"
"buffer_load_dwordx4 v[128:131], v0, s[4:7], s98 offen sc1\n"
"v_mfma_f32_16x16x32_bf16 v[116:119], a[48:51], v[152:155], v[116:119]\n"
"s_waitcnt lgkmcnt(0)\n"
"s_barrier\n"
"v_mfma_f32_16x16x32_bf16 v[116:119], a[44:47], v[184:187], v[116:119]\n"
"v_cmp_u_f32_e32 vcc, v88, v88\n"
"v_cvt_pk_bf16_f32 v254, v254, 0\n"
"v_mfma_f32_16x16x32_bf16 v[116:119], a[40:43], v[148:151], v[116:119]\n"
"s_cbranch_vccnz .Lmk_p45s\n"
".Lmk_p45r:\n"
"v_lshlrev_b32_e32 v254, 16, v254\n"
"v_mfma_f32_16x16x32_bf16 v[116:119], a[36:39], v[108:111], v[116:119]\n"
"ds_write_b128 v3, v[88:91] offset:12288\n"
"v_mfma_f32_16x16x32_bf16 v[116:119], a[32:35], v[176:179], v[116:119]\n"
"buffer_load_dwordx4 v[88:91], v0, s[4:7], s98 offen offset:1024 sc1\n"
"v_mfma_f32_16x16x32_bf16 v[132:135], a[64:67], v[176:179], 0\n"
"v_rcp_f32_e32 v255, v254\n"
"v_mfma_f32_16x16x32_bf16 v[132:135], a[68:71], v[108:111], v[132:135]\n"
"ds_read_b128 v[136:139], v4\n"
"v_mul_f32_e32 v254, 0x3e8293ee, v255\n"
"v_mfma_f32_16x16x32_bf16 v[132:135], a[72:75], v[148:151], v[132:135]\n"
"ds_read_b128 v[140:143], v9 offset:640\n"
"ds_read_b128 v[144:147], v5\n"
"v_mfma_f32_16x16x32_bf16 v[132:135], a[76:79], v[184:187], v[132:135]\n"
"ds_read_b128 v[156:159], v4 offset:1024\n"
"ds_read_b128 v[160:163], v9 offset:704\n"
"v_mfma_f32_16x16x32_bf16 v[132:135], a[80:83], v[152:155], v[132:135]\n"
"buffer_load_dwordx4 v[164:167], v0, s[4:7], s98 offen offset:2048 sc1\n"
"v_mfma_f32_16x16x32_bf16 v[132:135], a[84:87], v[192:195], v[132:135]\n"
"ds_write_b128 v3, v[116:119] offset:13312\n"
"v_mfma_f32_16x16x32_bf16 v[132:135], a[88:91], v[196:199], v[132:135]\n"
"ds_read_b128 v[116:119], v5 offset:1024\n"
"v_mfma_f32_16x16x32_bf16 v[132:135], a[92:95], v[200:203], v[132:135]\n"
"s_waitcnt lgkmcnt(4)\n"
"v_fmac_f32_e32 v136, v253, v140\n"
"v_fmac_f32_e32 v137, v253, v141\n"
"v_mfma_f32_16x16x32_bf16 v[180:183], a[124:127], v[200:203], 0\n"
"v_fmac_f32_e32 v138, v253, v142\n"
"v_fmac_f32_e32 v139, v253, v143\n"
"v_mfma_f32_16x16x32_bf16 v[180:183], a[120:123], v[196:199], v[180:183]\n"
"buffer_load_dwordx4 v[140:143], v0, s[4:7], s98 offen offset:3072 sc1\n"
"v_mfma_f32_16x16x32_bf16 v[180:183], a[116:119], v[192:195], v[180:183]\n"
"ds_read_b128 v[188:191], v4 offset:2048\n"
"ds_read_b128 v[204:207], v9 offset:768\n"
"v_mfma_f32_16x16x32_bf16 v[180:183], a[112:115], v[152:155], v[180:183]\n"
"ds_read_b128 v[208:211], v5 offset:2048\n"
"v_add_f32_e32 v136, v136, v144\n"
"v_mfma_f32_16x16x32_bf16 v[180:183], a[108:111], v[184:187], v[180:183]\n"
"buffer_load_dwordx4 a[144:147], v1, s[4:7], s97 offen sc1\n"
"v_mfma_f32_16x16x32_bf16 v[180:183], a[104:107], v[148:151], v[180:183]\n"
"ds_write_b128 v3, v[132:135] offset:14336\n"
"v_mfma_f32_16x16x32_bf16 v[180:183], a[100:103], v[108:111], v[180:183]\n"
"buffer_load_dwordx4 v[132:135], v1, s[4:7], s98 offen sc1\n"
"v_mfma_f32_16x16x32_bf16 v[180:183], a[96:99], v[176:179], v[180:183]\n"
"v_add_f32_e32 v137, v137, v145\n"
"v_add_f32_e32 v138, v138, v146\n"
"v_mfma_f32_16x16x32_bf16 v[212:215], v[24:27], v[176:179], 0\n"
"s_waitcnt lgkmcnt(4)\n"
"v_add_f32_e32 v139, v139, v147\n"
"v_fmac_f32_e32 v156, v253, v160\n"
"v_mfma_f32_16x16x32_bf16 v[212:215], v[28:31], v[108:111], v[212:215]\n"
"ds_read_b128 v[144:147], v4 offset:3072\n"
"ds_read_b128 v[216:219], v9 offset:832\n"
"v_mfma_f32_16x16x32_bf16 v[212:215], v[32:35], v[148:151], v[212:215]\n"
"ds_read_b128 v[220:223], v5 offset:3072\n"
"v_fmac_f32_e32 v157, v253, v161\n"
"v_mfma_f32_16x16x32_bf16 v[212:215], v[36:39], v[184:187], v[212:215]\n"
"buffer_load_dwordx4 v[224:227], v1, s[4:7], s98 offen offset:1024 sc1\n"
"v_mfma_f32_16x16x32_bf16 v[212:215], v[40:43], v[152:155], v[212:215]\n"
"s_add_u32 s96, s60, 0x400000\n"
"v_fmac_f32_e32 v158, v253, v162\n"
"v_fmac_f32_e32 v159, v253, v163\n"
"v_mfma_f32_16x16x32_bf16 v[212:215], v[44:47], v[192:195], v[212:215]\n"
"ds_write_b128 v3, v[180:183] offset:15360\n"
"v_mfma_f32_16x16x32_bf16 v[212:215], v[48:51], v[196:199], v[212:215]\n"
"buffer_store_dwordx4 v[120:123], v0, s[4:7], s96 offen sc1\n"
"v_mfma_f32_16x16x32_bf16 v[212:215], v[52:55], v[200:203], v[212:215]\n"
"v_add_f32_e32 v156, v156, v116\n"
"v_add_f32_e32 v157, v157, v117\n"
"v_mfma_f32_16x16x32_bf16 v[160:163], v[84:87], v[200:203], 0\n"
"buffer_load_dwordx4 v[180:183], v1, s[4:7], s98 offen offset:2048 sc1\n"
"v_mfma_f32_16x16x32_bf16 v[160:163], v[80:83], v[196:199], v[160:163]\n"
"s_add_u32 s96, s68, s80\n"
"s_add_u32 s96, s96, 0x10000\n"
"s_waitcnt vmcnt(22)\n"
"v_mfma_f32_16x16x32_bf16 v[160:163], v[76:79], v[192:195], v[160:163]\n"
"buffer_store_dwordx2 v[18:19], v11, s[4:7], s96 offen sc1\n"
"v_mfma_f32_16x16x32_bf16 v[160:163], v[72:75], v[152:155], v[160:163]\n"
"ds_read_b128 v[152:155], v4 offset:4096\n"
"ds_read_b128 v[192:195], v9 offset:512\n"
"v_mfma_f32_16x16x32_bf16 v[160:163], v[68:71], v[184:187], v[160:163]\n"
"ds_read_b128 v[184:187], v5 offset:4096\n"
"v_add_f32_e32 v158, v158, v118\n"
"v_mfma_f32_16x16x32_bf16 v[160:163], v[64:67], v[148:151], v[160:163]\n"
"buffer_load_dwordx4 v[148:151], v1, s[4:7], s98 offen offset:3072 sc1\n"
"v_mfma_f32_16x16x32_bf16 v[160:163], v[60:63], v[108:111], v[160:163]\n"
"ds_write_b128 v3, v[212:215] offset:16384\n"
"v_mfma_f32_16x16x32_bf16 v[160:163], v[56:59], v[176:179], v[160:163]\n"
"s_waitcnt vmcnt(19)\n"
"ds_read_b128 v[108:111], v4 offset:5120\n"
"ds_read_b128 v[176:179], v9 offset:576\n"
"; @B G1 0.3\n"
"v_mfma_f32_16x16x32_bf16 v[196:199], a[0:3], v[92:95], v[136:139]\n"
"buffer_load_dwordx4 a[148:151], v1, s[4:7], s97 offen offset:1024 sc1\n"
"v_mfma_f32_16x16x32_bf16 v[196:199], a[4:7], v[96:99], v[196:199]\n"
"ds_read_b128 v[200:203], v5 offset:5120\n"
"v_add_f32_e32 v159, v159, v119\n"
"v_mfma_f32_16x16x32_bf16 v[196:199], a[8:11], v[104:107], v[196:199]\n"
"s_add_u32 s98, s53, 0x448000\n"
"buffer_load_dwordx4 v[116:119], v0, s[4:7], s98 offen sc1\n"
"v_mfma_f32_16x16x32_bf16 v[196:199], a[12:15], v[112:115], v[196:199]\n"
"s_waitcnt vmcnt(15)\n"
"s_waitcnt lgkmcnt(12)\n"
"v_fmac_f32_e32 v188, v253, v204\n"
"v_mfma_f32_16x16x32_bf16 v[196:199], a[16:19], v[100:103], v[196:199]\n"
"v_fmac_f32_e32 v189, v253, v205\n"
"v_fmac_f32_e32 v190, v253, v206\n"
"v_mfma_f32_16x16x32_bf16 v[196:199], a[20:23], v[124:127], v[196:199]\n"
"ds_write_b128 v3, v[160:163] offset:17408\n"
"v_mfma_f32_16x16x32_bf16 v[196:199], a[24:27], v[168:171], v[196:199]\n"
"v_fmac_f32_e32 v191, v253, v207\n"
"v_add_f32_e32 v188, v188, v208\n"
"v_mfma_f32_16x16x32_bf16 v[196:199], a[28:31], v[172:175], v[196:199]\n"
"buffer_load_dwordx4 v[160:163], v0, s[4:7], s98 offen offset:1024 sc1\n"
"v_mfma_f32_16x16x32_bf16 v[204:207], a[60:63], v[172:175], v[156:159]\n"
"v_add_f32_e32 v189, v189, v209\n"
"v_add_f32_e32 v190, v190, v210\n"
"v_mfma_f32_16x16x32_bf16 v[204:207], a[56:59], v[168:171], v[204:207]\n"
"s_waitcnt lgkmcnt(9)\n"
"v_add_f32_e32 v191, v191, v211\n"
"v_fmac_f32_e32 v144, v253, v216\n"
"v_mfma_f32_16x16x32_bf16 v[204:207], a[52:55], v[124:127], v[204:207]\n"
"s_waitcnt lgkmcnt(0)\n"
"s_barrier\n"
"v_mfma_f32_16x16x32_bf16 v[204:207], a[48:51], v[100:103], v[204:207]\n"
"ds_read_b128 v[208:211], v6\n"
"ds_read_b128 v[212:215], v6 offset:1024\n"
"v_mfma_f32_16x16x32_bf16 v[204:207], a[44:47], v[112:115], v[204:207]\n"
"v_cmp_u_f32_e32 vcc, v196, v196\n"
"v_fmac_f32_e32 v145, v253, v217\n"
"v_mfma_f32_16x16x32_bf16 v[204:207], a[40:43], v[104:107], v[204:207]\n"
"s_cbranch_vccnz .Lmk_p46s\n"
".Lmk_p46r:\n"
"v_fmac_f32_e32 v146, v253, v218\n"
"v_mfma_f32_16x16x32_bf16 v[204:207], a[36:39], v[96:99], v[204:207]\n"
"buffer_load_dwordx4 v[136:139], v0, s[4:7], s98 offen offset:2048 sc1\n"
"v_mfma_f32_16x16x32_bf16 v[204:207], a[32:35], v[92:95], v[204:207]\n"
"v_fmac_f32_e32 v147, v253, v219\n"
"v_add_f32_e32 v144, v144, v220\n"
"v_mfma_f32_16x16x32_bf16 v[188:191], a[64:67], v[92:95], v[188:191]\n"
"v_add_f32_e32 v145, v145, v221\n"
"v_add_f32_e32 v146, v146, v222\n"
"v_mfma_f32_16x16x32_bf16 v[188:191], a[68:71], v[96:99], v[188:191]\n"
"buffer_load_dwordx4 v[156:159], v0, s[4:7], s98 offen offset:3072 sc1\n"
"v_mfma_f32_16x16x32_bf16 v[188:191], a[72:75], v[104:107], v[188:191]\n"
"v_add_f32_e32 v147, v147, v223\n"
"v_fmac_f32_e32 v152, v253, v192\n"
"v_mfma_f32_16x16x32_bf16 v[188:191], a[76:79], v[112:115], v[188:191]\n"
"v_fmac_f32_e32 v153, v253, v193\n"
"v_fmac_f32_e32 v154, v253, v194\n"
"v_mfma_f32_16x16x32_bf16 v[188:191], a[80:83], v[100:103], v[188:191]\n"
"buffer_load_dwordx4 a[152:155], v1, s[4:7], s97 offen offset:2048 sc1\n"
"v_mfma_f32_16x16x32_bf16 v[188:191], a[84:87], v[124:127], v[188:191]\n"
"s_waitcnt lgkmcnt(0)\n"
"v_fmac_f32_e32 v155, v253, v195\n"
"v_add_f32_e32 v152, v152, v184\n"
"v_mfma_f32_16x16x32_bf16 v[188:191], a[88:91], v[168:171], v[188:191]\n"
"buffer_load_dwordx4 v[192:195], v1, s[4:7], s98 offen sc1\n"
"v_mfma_f32_16x16x32_bf16 v[188:191], a[92:95], v[172:175], v[188:191]\n"
"ds_read_b128 v[216:219], v6 offset:2048\n"
"ds_read_b128 v[220:223], v6 offset:3072\n"
"v_mfma_f32_16x16x32_bf16 v[144:147], a[124:127], v[172:175], v[144:147]\n"
"v_add_f32_e32 v153, v153, v185\n"
"v_add_f32_e32 v154, v154, v186\n"
"v_mfma_f32_16x16x32_bf16 v[144:147], a[120:123], v[168:171], v[144:147]\n"
"v_add_f32_e32 v155, v155, v187\n"
"v_fmac_f32_e32 v108, v253, v176\n"
"v_mfma_f32_16x16x32_bf16 v[144:147], a[116:119], v[124:127], v[144:147]\n"
"v_fmac_f32_e32 v109, v253, v177\n"
"v_fmac_f32_e32 v110, v253, v178\n"
"v_mfma_f32_16x16x32_bf16 v[144:147], a[112:115], v[100:103], v[144:147]\n"
"buffer_load_dwordx4 v[184:187], v1, s[4:7], s98 offen offset:1024 sc1\n"
"v_mfma_f32_16x16x32_bf16 v[144:147], a[108:111], v[112:115], v[144:147]\n"
"v_fmac_f32_e32 v111, v253, v179\n"
"v_add_f32_e32 v108, v108, v200\n"
"v_mfma_f32_16x16x32_bf16 v[144:147], a[104:107], v[104:107], v[144:147]\n"
"v_add_f32_e32 v109, v109, v201\n"
"v_add_f32_e32 v110, v110, v202\n"
"v_mfma_f32_16x16x32_bf16 v[144:147], a[100:103], v[96:99], v[144:147]\n"
"v_add_f32_e32 v111, v111, v203\n"
"v_add_f32_e32 v196, v196, v208\n"
"v_mfma_f32_16x16x32_bf16 v[144:147], a[96:99], v[92:95], v[144:147]\n"
"v_add_f32_e32 v197, v197, v209\n"
"v_add_f32_e32 v198, v198, v210\n"
"v_mfma_f32_16x16x32_bf16 v[152:155], v[24:27], v[92:95], v[152:155]\n"
"buffer_load_dwordx4 v[176:179], v1, s[4:7], s98 offen offset:2048 sc1\n"
"v_mfma_f32_16x16x32_bf16 v[152:155], v[28:31], v[96:99], v[152:155]\n"
"v_add_f32_e32 v199, v199, v211\n"
"v_add_f32_e32 v204, v204, v212\n"
"v_mfma_f32_16x16x32_bf16 v[152:155], v[32:35], v[104:107], v[152:155]\n"
"v_add_f32_e32 v205, v205, v213\n"
"v_add_f32_e32 v206, v206, v214\n"
"v_mfma_f32_16x16x32_bf16 v[152:155], v[36:39], v[112:115], v[152:155]\n"
"v_add_f32_e32 v207, v207, v215\n"
"v_mul_f32_e32 v196, v196, v255\n"
"v_mfma_f32_16x16x32_bf16 v[152:155], v[40:43], v[100:103], v[152:155]\n"
"s_waitcnt lgkmcnt(0)\n"
"v_mul_f32_e32 v197, v197, v255\n"
"v_mul_f32_e32 v198, v198, v255\n"
"v_mfma_f32_16x16x32_bf16 v[152:155], v[44:47], v[124:127], v[152:155]\n"
"buffer_load_dwordx4 v[200:203], v1, s[4:7], s98 offen offset:3072 sc1\n"
"v_mfma_f32_16x16x32_bf16 v[152:155], v[48:51], v[168:171], v[152:155]\n"
"s_add_u32 s98, s60, 0x408000\n"
"ds_read_b128 v[208:211], v6 offset:4096\n"
"ds_read_b128 v[212:215], v6 offset:5120\n"
"v_mfma_f32_16x16x32_bf16 v[152:155], v[52:55], v[172:175], v[152:155]\n"
"v_mul_f32_e32 v199, v199, v255\n"
"v_mul_f32_e32 v204, v204, v255\n"
"v_mfma_f32_16x16x32_bf16 v[108:111], v[84:87], v[172:175], v[108:111]\n"
"buffer_store_dwordx4 v[120:123], v0, s[4:7], s98 offen sc1\n"
"v_mfma_f32_16x16x32_bf16 v[108:111], v[80:83], v[168:171], v[108:111]\n"
"v_mul_f32_e32 v205, v205, v255\n"
"v_mul_f32_e32 v206, v206, v255\n"
"v_mfma_f32_16x16x32_bf16 v[108:111], v[76:79], v[124:127], v[108:111]\n"
"v_mul_f32_e32 v207, v207, v255\n"
"v_cvt_pk_bf16_f32 v120, v196, v197\n"
"v_mfma_f32_16x16x32_bf16 v[108:111], v[72:75], v[100:103], v[108:111]\n"
"v_cvt_pk_bf16_f32 v121, v198, v199\n"
"v_cvt_pk_bf16_f32 v122, v204, v205\n"
"v_mfma_f32_16x16x32_bf16 v[108:111], v[68:71], v[112:115], v[108:111]\n"
"v_cvt_pk_bf16_f32 v123, v206, v207\n"
"v_add_f32_e32 v188, v188, v216\n"
"v_mfma_f32_16x16x32_bf16 v[108:111], v[64:67], v[104:107], v[108:111]\n"
"ds_write_b128 v7, v[120:123] offset:24576\n"
"v_mfma_f32_16x16x32_bf16 v[108:111], v[60:63], v[96:99], v[108:111]\n"
"v_add_f32_e32 v189, v189, v217\n"
"v_add_f32_e32 v190, v190, v218\n"
"v_mfma_f32_16x16x32_bf16 v[108:111], v[56:59], v[92:95], v[108:111]\n"
"v_add_f32_e32 v191, v191, v219\n"
"v_add_f32_e32 v144, v144, v220\n"
"v_add_f32_e32 v145, v145, v221\n"
"v_add_f32_e32 v146, v146, v222\n"
"v_add_f32_e32 v147, v147, v223\n"
"v_mul_f32_e32 v188, v188, v255\n"
"v_mul_f32_e32 v189, v189, v255\n"
"v_mul_f32_e32 v190, v190, v255\n"
"v_mul_f32_e32 v191, v191, v255\n"
"v_mul_f32_e32 v144, v144, v255\n"
"v_mul_f32_e32 v145, v145, v255\n"
"v_mul_f32_e32 v146, v146, v255\n"
"v_mul_f32_e32 v147, v147, v255\n"
"v_cvt_pk_bf16_f32 v92, v188, v189\n"
"v_cvt_pk_bf16_f32 v93, v190, v191\n"
"v_cvt_pk_bf16_f32 v94, v144, v145\n"
"v_cvt_pk_bf16_f32 v95, v146, v147\n"
"ds_write_b128 v7, v[92:95] offset:57344\n"
".Lmk_b1:\n"
"s_waitcnt vmcnt(18)\n"
"; @B G1 1.0\n"
"v_mfma_f32_16x16x32_bf16 v[92:95], a[0:3], v[128:131], 0\n"
"v_mfma_f32_16x16x32_bf16 v[92:95], a[4:7], v[88:91], v[92:95]\n"
"s_add_u32 s98, s62, 0x44800\n"
"v_mfma_f32_16x16x32_bf16 v[92:95], a[8:11], v[164:167], v[92:95]\n"
"s_add_u32 s96, s54, 0x448000\n"
"buffer_load_dwordx4 v[96:99], v0, s[4:7], s96 offen sc1\n"
"v_mfma_f32_16x16x32_bf16 v[92:95], a[12:15], v[140:143], v[92:95]\n"
"s_waitcnt vmcnt(12)\n"
"v_mfma_f32_16x16x32_bf16 v[92:95], a[16:19], v[132:135], v[92:95]\n"
"buffer_load_dword v255, v10, s[4:7], s98 offen sc1\n"
"v_mfma_f32_16x16x32_bf16 v[92:95], a[20:23], v[224:227], v[92:95]\n"
"s_waitcnt lgkmcnt(2)\n"
"v_add_f32_e32 v152, v152, v208\n"
"v_add_f32_e32 v153, v153, v209\n"
"v_mfma_f32_16x16x32_bf16 v[92:95], a[24:27], v[180:183], v[92:95]\n"
"v_add_f32_e32 v154, v154, v210\n"
"v_add_f32_e32 v155, v155, v211\n"
"v_mfma_f32_16x16x32_bf16 v[92:95], a[28:31], v[148:151], v[92:95]\n"
"buffer_load_dwordx4 v[100:103], v0, s[4:7], s96 offen offset:1024 sc1\n"
"v_mfma_f32_16x16x32_bf16 v[104:107], a[60:63], v[148:151], 0\n"
"buffer_load_dwordx4 a[156:159], v1, s[4:7], s97 offen offset:3072 sc1\n"
"v_mfma_f32_16x16x32_bf16 v[104:107], a[56:59], v[180:183], v[104:107]\n"
"buffer_load_dword v253, v10, s[4:7], s98 offen offset:256 sc1\n"
"v_mfma_f32_16x16x32_bf16 v[104:107], a[52:55], v[224:227], v[104:107]\n"
"v_add_f32_e32 v108, v108, v212\n"
"v_add_f32_e32 v109, v109, v213\n"
"v_mfma_f32_16x16x32_bf16 v[104:107], a[48:51], v[132:135], v[104:107]\n"
"v_add_f32_e32 v110, v110, v214\n"
"v_add_f32_e32 v111, v111, v215\n"
"v_mfma_f32_16x16x32_bf16 v[104:107], a[44:47], v[140:143], v[104:107]\n"
"v_cmp_u_f32_e32 vcc, v92, v92\n"
"v_mul_f32_e32 v152, v152, v254\n"
"v_mfma_f32_16x16x32_bf16 v[104:107], a[40:43], v[164:167], v[104:107]\n"
"s_cbranch_vccnz .Lmk_p48s\n"
".Lmk_p48r:\n"
"v_mul_f32_e32 v153, v153, v254\n"
"v_mfma_f32_16x16x32_bf16 v[104:107], a[36:39], v[88:91], v[104:107]\n"
"buffer_load_dwordx4 v[112:115], v0, s[4:7], s96 offen offset:2048 sc1\n"
"v_mfma_f32_16x16x32_bf16 v[104:107], a[32:35], v[128:131], v[104:107]\n"
"ds_write_b128 v3, v[92:95]\n"
"v_mfma_f32_16x16x32_bf16 v[92:95], a[64:67], v[128:131], 0\n"
"buffer_load_dword v252, v10, s[4:7], s98 offen offset:512 sc1\n"
"v_mfma_f32_16x16x32_bf16 v[92:95], a[68:71], v[88:91], v[92:95]\n"
"buffer_load_dwordx4 v[120:123], v0, s[4:7], s96 offen offset:3072 sc1\n"
"v_mfma_f32_16x16x32_bf16 v[92:95], a[72:75], v[164:167], v[92:95]\n"
"v_mul_f32_e32 v154, v154, v254\n"
"v_mul_f32_e32 v155, v155, v254\n"
"v_mfma_f32_16x16x32_bf16 v[92:95], a[76:79], v[140:143], v[92:95]\n"
"buffer_load_dword v251, v10, s[4:7], s98 offen offset:768 sc1\n"
"v_mfma_f32_16x16x32_bf16 v[92:95], a[80:83], v[132:135], v[92:95]\n"
"v_mul_f32_e32 v108, v108, v254\n"
"v_mul_f32_e32 v109, v109, v254\n"
"v_mfma_f32_16x16x32_bf16 v[92:95], a[84:87], v[224:227], v[92:95]\n"
"ds_write_b128 v3, v[104:107] offset:1024\n"
"v_mfma_f32_16x16x32_bf16 v[92:95], a[88:91], v[180:183], v[92:95]\n"
"buffer_load_dwordx4 v[104:107], v1, s[4:7], s96 offen sc1\n"
"v_mfma_f32_16x16x32_bf16 v[92:95], a[92:95], v[148:151], v[92:95]\n"
"buffer_load_dword v250, v10, s[4:7], s98 offen offset:1024 sc1\n"
"v_mfma_f32_16x16x32_bf16 v[124:127], a[124:127], v[148:151], 0\n"
"v_mul_f32_e32 v110, v110, v254\n"
"v_mul_f32_e32 v111, v111, v254\n"
"v_mfma_f32_16x16x32_bf16 v[124:127], a[120:123], v[180:183], v[124:127]\n"
"v_cvt_pk_bf16_f32 v144, v152, v153\n"
"v_cvt_pk_bf16_f32 v145, v154, v155\n"
"v_mfma_f32_16x16x32_bf16 v[124:127], a[116:119], v[224:227], v[124:127]\n"
"s_add_u32 s97, s70, 0x202000\n"
"buffer_load_dword v254, v10, s[4:7], s98 offen offset:1280 sc1\n"
"v_mfma_f32_16x16x32_bf16 v[124:127], a[112:115], v[132:135], v[124:127]\n"
"buffer_load_dwordx4 v[152:155], v1, s[4:7], s96 offen offset:1024 sc1\n"
"v_mfma_f32_16x16x32_bf16 v[124:127], a[108:111], v[140:143], v[124:127]\n"
"buffer_load_dwordx4 a[160:163], v0, s[4:7], s97 offen sc1\n"
"v_mfma_f32_16x16x32_bf16 v[124:127], a[104:107], v[164:167], v[124:127]\n"
"ds_write_b128 v3, v[92:95] offset:2048\n"
"v_mfma_f32_16x16x32_bf16 v[124:127], a[100:103], v[88:91], v[124:127]\n"
"buffer_load_dword v249, v10, s[4:7], s98 offen offset:1536 sc1\n"
"v_mfma_f32_16x16x32_bf16 v[124:127], a[96:99], v[128:131], v[124:127]\n"
"v_cvt_pk_bf16_f32 v146, v108, v109\n"
"v_cvt_pk_bf16_f32 v147, v110, v111\n"
"v_mfma_f32_16x16x32_bf16 v[92:95], v[24:27], v[128:131], 0\n"
"buffer_load_dwordx4 v[108:111], v1, s[4:7], s96 offen offset:2048 sc1\n"
"v_mfma_f32_16x16x32_bf16 v[92:95], v[28:31], v[88:91], v[92:95]\n"
"buffer_load_dword v248, v10, s[4:7], s98 offen offset:1792 sc1\n"
"v_mfma_f32_16x16x32_bf16 v[92:95], v[32:35], v[164:167], v[92:95]\n"
"ds_write_b128 v2, v[144:147] offset:49152\n"
"v_mfma_f32_16x16x32_bf16 v[92:95], v[36:39], v[140:143], v[92:95]\n"
"v_mfma_f32_16x16x32_bf16 v[92:95], v[40:43], v[132:135], v[92:95]\n"
"v_mfma_f32_16x16x32_bf16 v[92:95], v[44:47], v[224:227], v[92:95]\n"
"buffer_load_dwordx4 v[144:147], v1, s[4:7], s96 offen offset:3072 sc1\n"
"v_mfma_f32_16x16x32_bf16 v[92:95], v[48:51], v[180:183], v[92:95]\n"
"ds_write_b128 v3, v[124:127] offset:3072\n"
"v_mfma_f32_16x16x32_bf16 v[92:95], v[52:55], v[148:151], v[92:95]\n"
"v_mfma_f32_16x16x32_bf16 v[124:127], v[84:87], v[148:151], 0\n"
"v_mfma_f32_16x16x32_bf16 v[124:127], v[80:83], v[180:183], v[124:127]\n"
"v_mfma_f32_16x16x32_bf16 v[124:127], v[76:79], v[224:227], v[124:127]\n"
"v_mfma_f32_16x16x32_bf16 v[124:127], v[72:75], v[132:135], v[124:127]\n"
"v_mfma_f32_16x16x32_bf16 v[124:127], v[68:71], v[140:143], v[124:127]\n"
"v_mfma_f32_16x16x32_bf16 v[124:127], v[64:67], v[164:167], v[124:127]\n"
"s_nop 1\n"
"ds_write_b128 v3, v[92:95] offset:4096\n"
"v_mfma_f32_16x16x32_bf16 v[124:127], v[60:63], v[88:91], v[124:127]\n"
"v_mfma_f32_16x16x32_bf16 v[124:127], v[56:59], v[128:131], v[124:127]\n"
"s_waitcnt vmcnt(24)\n"
"; @B G1 1.1\n"
"v_mfma_f32_16x16x32_bf16 v[88:91], a[0:3], v[116:119], 0\n"
"buffer_load_dwordx4 a[164:167], v0, s[4:7], s97 offen offset:1024 sc1\n"
"v_mfma_f32_16x16x32_bf16 v[88:91], a[4:7], v[160:163], v[88:91]\n"
"v_mfma_f32_16x16x32_bf16 v[88:91], a[8:11], v[136:139], v[88:91]\n"
"s_add_u32 s98, s55, 0x448000\n"
"buffer_load_dwordx4 v[92:95], v0, s[4:7], s98 offen sc1\n"
"v_mfma_f32_16x16x32_bf16 v[88:91], a[12:15], v[156:159], v[88:91]\n"
"s_waitcnt vmcnt(21)\n"
"v_mfma_f32_16x16x32_bf16 v[88:91], a[16:19], v[192:195], v[88:91]\n"
"v_mfma_f32_16x16x32_bf16 v[88:91], a[20:23], v[184:187], v[88:91]\n"
"ds_write_b128 v3, v[124:127] offset:5120\n"
"v_mfma_f32_16x16x32_bf16 v[88:91], a[24:27], v[176:179], v[88:91]\n"
"v_mfma_f32_16x16x32_bf16 v[88:91], a[28:31], v[200:203], v[88:91]\n"
"buffer_load_dwordx4 v[124:127], v0, s[4:7], s98 offen offset:1024 sc1\n"
"v_mfma_f32_16x16x32_bf16 v[128:131], a[60:63], v[200:203], 0\n"
"v_mfma_f32_16x16x32_bf16 v[128:131], a[56:59], v[176:179], v[128:131]\n"
"v_mfma_f32_16x16x32_bf16 v[128:131], a[52:55], v[184:187], v[128:131]\n"
"v_mfma_f32_16x16x32_bf16 v[128:131], a[48:51], v[192:195], v[128:131]\n"
"v_mfma_f32_16x16x32_bf16 v[128:131], a[44:47], v[156:159], v[128:131]\n"
"s_add_u32 s96, s67, 0x10000\n"
"s_nop 0\n"
"v_cmp_u_f32_e32 vcc, v88, v88\n"
"v_mfma_f32_16x16x32_bf16 v[128:131], a[40:43], v[136:139], v[128:131]\n"
"s_cbranch_vccnz .Lmk_p49s\n"
".Lmk_p49r:\n"
"v_mfma_f32_16x16x32_bf16 v[128:131], a[36:39], v[160:163], v[128:131]\n"
"buffer_load_dwordx4 v[132:135], v0, s[4:7], s98 offen offset:2048 sc1\n"
"v_mfma_f32_16x16x32_bf16 v[128:131], a[32:35], v[116:119], v[128:131]\n"
"ds_write_b128 v3, v[88:91] offset:6144\n"
"v_mfma_f32_16x16x32_bf16 v[88:91], a[64:67], v[116:119], 0\n"
"buffer_load_dwordx2 v[246:247], v12, s[4:7], s96 offen sc1\n"
"v_mfma_f32_16x16x32_bf16 v[88:91], a[68:71], v[160:163], v[88:91]\n"
"buffer_load_dwordx4 v[140:143], v0, s[4:7], s98 offen offset:3072 sc1\n"
"v_mfma_f32_16x16x32_bf16 v[88:91], a[72:75], v[136:139], v[88:91]\n"
"v_mfma_f32_16x16x32_bf16 v[88:91], a[76:79], v[156:159], v[88:91]\n"
"v_mfma_f32_16x16x32_bf16 v[88:91], a[80:83], v[192:195], v[88:91]\n"
"v_mov_b32_e32 v148, -1\n"
"v_mov_b32_e32 v149, -1\n"
"v_mfma_f32_16x16x32_bf16 v[88:91], a[84:87], v[184:187], v[88:91]\n"
"ds_write_b128 v3, v[128:131] offset:7168\n"
"v_mfma_f32_16x16x32_bf16 v[88:91], a[88:91], v[176:179], v[88:91]\n"
"buffer_load_dwordx4 v[128:131], v1, s[4:7], s98 offen sc1\n"
"v_mfma_f32_16x16x32_bf16 v[88:91], a[92:95], v[200:203], v[88:91]\n"
"v_mov_b32_e32 v150, -1\n"
"v_mov_b32_e32 v151, -1\n"
"v_mfma_f32_16x16x32_bf16 v[164:167], a[124:127], v[200:203], 0\n"
"s_add_u32 s95, s61, 0x840000\n"
"buffer_store_dwordx4 v[148:151], v0, s[8:11], s95 offen sc1\n"
"v_mfma_f32_16x16x32_bf16 v[164:167], a[120:123], v[176:179], v[164:167]\n"
"buffer_load_dwordx4 a[168:171], v0, s[4:7], s97 offen offset:2048 sc1\n"
"v_mfma_f32_16x16x32_bf16 v[164:167], a[116:119], v[184:187], v[164:167]\n"
"v_mfma_f32_16x16x32_bf16 v[164:167], a[112:115], v[192:195], v[164:167]\n"
"buffer_load_dwordx4 v[168:171], v1, s[4:7], s98 offen offset:1024 sc1\n"
"v_mfma_f32_16x16x32_bf16 v[164:167], a[108:111], v[156:159], v[164:167]\n"
"v_mfma_f32_16x16x32_bf16 v[164:167], a[104:107], v[136:139], v[164:167]\n"
"ds_write_b128 v3, v[88:91] offset:8192\n"
"v_mfma_f32_16x16x32_bf16 v[164:167], a[100:103], v[160:163], v[164:167]\n"
"v_mfma_f32_16x16x32_bf16 v[164:167], a[96:99], v[116:119], v[164:167]\n"
"v_mfma_f32_16x16x32_bf16 v[88:91], v[24:27], v[116:119], 0\n"
"buffer_load_dwordx4 v[172:175], v1, s[4:7], s98 offen offset:2048 sc1\n"
"v_mfma_f32_16x16x32_bf16 v[88:91], v[28:31], v[160:163], v[88:91]\n"
"v_mfma_f32_16x16x32_bf16 v[88:91], v[32:35], v[136:139], v[88:91]\n"
"v_mfma_f32_16x16x32_bf16 v[88:91], v[36:39], v[156:159], v[88:91]\n"
"v_mfma_f32_16x16x32_bf16 v[88:91], v[40:43], v[192:195], v[88:91]\n"
"s_waitcnt vmcnt(12)\n"
"v_add_f32_e32 v255, v255, v250\n"
"v_add_f32_e32 v253, v253, v254\n"
"v_mfma_f32_16x16x32_bf16 v[88:91], v[44:47], v[184:187], v[88:91]\n"
"buffer_load_dwordx4 v[180:183], v1, s[4:7], s98 offen offset:3072 sc1\n"
"v_mfma_f32_16x16x32_bf16 v[88:91], v[48:51], v[176:179], v[88:91]\n"
"ds_write_b128 v3, v[164:167] offset:9216\n"
"v_mfma_f32_16x16x32_bf16 v[88:91], v[52:55], v[200:203], v[88:91]\n"
"v_add_f32_e32 v252, v252, v249\n"
"v_add_f32_e32 v251, v251, v248\n"
"v_mfma_f32_16x16x32_bf16 v[164:167], v[84:87], v[200:203], 0\n"
"v_add_f32_e32 v255, v255, v252\n"
"v_add_f32_e32 v253, v253, v251\n"
"v_mfma_f32_16x16x32_bf16 v[164:167], v[80:83], v[176:179], v[164:167]\n"
"v_add_f32_e32 v255, v255, v253\n"
"v_cmp_u_f32_e32 vcc, v255, v255\n"
"v_mfma_f32_16x16x32_bf16 v[164:167], v[76:79], v[184:187], v[164:167]\n"
"s_cbranch_vccnz .Lmk_m52s\n"
".Lmk_m52r:\n"
"v_mov_b32_e32 v253, v255\n"
"v_mfma_f32_16x16x32_bf16 v[164:167], v[72:75], v[192:195], v[164:167]\n"
"s_nop 0\n"
"v_permlane32_swap_b32_e32 v255, v253\n"
"v_add_f32_e32 v255, v255, v253\n"
"v_mfma_f32_16x16x32_bf16 v[164:167], v[68:71], v[156:159], v[164:167]\n"
"v_mul_f32_e32 v255, 0x3a800000, v255\n"
"v_mov_b32_e32 v253, v255\n"
"v_mfma_f32_16x16x32_bf16 v[164:167], v[64:67], v[136:139], v[164:167]\n"
"ds_write_b128 v3, v[88:91] offset:10240\n"
"v_mfma_f32_16x16x32_bf16 v[164:167], v[60:63], v[160:163], v[164:167]\n"
"buffer_load_dwordx4 a[172:175], v0, s[4:7], s97 offen offset:3072 sc1\n"
"v_mfma_f32_16x16x32_bf16 v[164:167], v[56:59], v[116:119], v[164:167]\n"
"v_permlane16_swap_b32_e32 v255, v253\n"
"v_cvt_pk_bf16_f32 v254, -v255, 0\n"
"; @B G1 1.2\n"
"v_mfma_f32_16x16x32_bf16 v[88:91], a[0:3], v[96:99], 0\n"
"v_fma_f32 v253, -v255, v255, v253\n"
"v_lshlrev_b32_e32 v254, 16, v254\n"
"v_mfma_f32_16x16x32_bf16 v[88:91], a[4:7], v[100:103], v[88:91]\n"
"s_waitcnt vmcnt(8)\n"
"v_cmp_ne_u64_e32 vcc, v[246:247], v[18:19]\n"
"v_mfma_f32_16x16x32_bf16 v[88:91], a[8:11], v[112:115], v[88:91]\n"
"s_cbranch_vccnz .Lmk_f39s\n"
".Lmk_f39r:\n"
"v_max_f32_e32 v253, 0, v253\n"
"v_mfma_f32_16x16x32_bf16 v[88:91], a[12:15], v[120:123], v[88:91]\n"
"v_cvt_pk_bf16_f32 v253, v253, 0\n"
"v_lshlrev_b32_e32 v253, 16, v253\n"
"v_mfma_f32_16x16x32_bf16 v[88:91], a[16:19], v[104:107], v[88:91]\n"
"v_add_f32_e32 v253, 0x3727c5ac, v253\n"
"v_cvt_pk_bf16_f32 v253, v253, 0\n"
"v_mfma_f32_16x16x32_bf16 v[88:91], a[20:23], v[152:155], v[88:91]\n"
"ds_write_b128 v3, v[164:167] offset:11264\n"
"v_mfma_f32_16x16x32_bf16 v[88:91], a[24:27], v[108:111], v[88:91]\n"
"v_lshlrev_b32_e32 v253, 16, v253\n"
"v_mfma_f32_16x16x32_bf16 v[88:91], a[28:31], v[144:147], v[88:91]\n"
"v_sqrt_f32_e32 v253, v253\n"
"v_mfma_f32_16x16x32_bf16 v[116:119], a[60:63], v[144:147], 0\n"
"v_cvt_pk_bf16_f32 v253, v253, 0\n"
"v_lshlrev_b32_e32 v253, 16, v253\n"
"v_mfma_f32_16x16x32_bf16 v[116:119], a[56:59], v[108:111], v[116:119]\n"
"v_rcp_f32_e32 v255, v253\n"
"v_mfma_f32_16x16x32_bf16 v[116:119], a[52:55], v[152:155], v[116:119]\n"
"s_add_u32 s98, s52, 0x408000\n"
"buffer_load_dwordx4 v[136:139], v0, s[4:7], s98 offen sc1\n"
"v_mfma_f32_16x16x32_bf16 v[116:119], a[48:51], v[104:107], v[116:119]\n"
"s_waitcnt lgkmcnt(0)\n"
"s_barrier\n"
"v_mfma_f32_16x16x32_bf16 v[116:119], a[44:47], v[120:123], v[116:119]\n"
"v_cmp_u_f32_e32 vcc, v88, v88\n"
"v_mul_f32_e32 v253, 0x3e8293ee, v255\n"
"v_mfma_f32_16x16x32_bf16 v[116:119], a[40:43], v[112:115], v[116:119]\n"
"s_cbranch_vccnz .Lmk_p50s\n"
".Lmk_p50r:\n"
"ds_read_b128 v[156:159], v4\n"
"v_mfma_f32_16x16x32_bf16 v[116:119], a[36:39], v[100:103], v[116:119]\n"
"ds_write_b128 v3, v[88:91] offset:12288\n"
"v_mfma_f32_16x16x32_bf16 v[116:119], a[32:35], v[96:99], v[116:119]\n"
"buffer_load_dwordx4 v[88:91], v0, s[4:7], s98 offen offset:1024 sc1\n"
"v_mfma_f32_16x16x32_bf16 v[160:163], a[64:67], v[96:99], 0\n"
"ds_read_b128 v[164:167], v9 offset:640\n"
"ds_read_b128 v[176:179], v5\n"
"v_mfma_f32_16x16x32_bf16 v[160:163], a[68:71], v[100:103], v[160:163]\n"
"v_mfma_f32_16x16x32_bf16 v[160:163], a[72:75], v[112:115], v[160:163]\n"
"buffer_load_dwordx4 a[176:179], v1, s[4:7], s97 offen sc1\n"
"v_mfma_f32_16x16x32_bf16 v[160:163], a[76:79], v[120:123], v[160:163]\n"
"ds_read_b128 v[184:187], v4 offset:1024\n"
"ds_read_b128 v[188:191], v9 offset:704\n"
"v_mfma_f32_16x16x32_bf16 v[160:163], a[80:83], v[104:107], v[160:163]\n"
"buffer_load_dwordx4 v[192:195], v0, s[4:7], s98 offen offset:2048 sc1\n"
"v_mfma_f32_16x16x32_bf16 v[160:163], a[84:87], v[152:155], v[160:163]\n"
"ds_write_b128 v3, v[116:119] offset:13312\n"
"v_mfma_f32_16x16x32_bf16 v[160:163], a[88:91], v[108:111], v[160:163]\n"
"ds_read_b128 v[116:119], v5 offset:1024\n"
"s_waitcnt lgkmcnt(4)\n"
"v_mfma_f32_16x16x32_bf16 v[160:163], a[92:95], v[144:147], v[160:163]\n"
"v_fmac_f32_e32 v156, v254, v164\n"
"v_fmac_f32_e32 v157, v254, v165\n"
"v_mfma_f32_16x16x32_bf16 v[196:199], a[124:127], v[144:147], 0\n"
"v_fmac_f32_e32 v158, v254, v166\n"
"v_fmac_f32_e32 v159, v254, v167\n"
"v_mfma_f32_16x16x32_bf16 v[196:199], a[120:123], v[108:111], v[196:199]\n"
"buffer_load_dwordx4 v[164:167], v0, s[4:7], s98 offen offset:3072 sc1\n"
"v_mfma_f32_16x16x32_bf16 v[196:199], a[116:119], v[152:155], v[196:199]\n"
"ds_read_b128 v[200:203], v4 offset:2048\n"
"ds_read_b128 v[204:207], v9 offset:768\n"
"v_mfma_f32_16x16x32_bf16 v[196:199], a[112:115], v[104:107], v[196:199]\n"
"ds_read_b128 v[208:211], v5 offset:2048\n"
"v_add_f32_e32 v156, v156, v176\n"
"v_mfma_f32_16x16x32_bf16 v[196:199], a[108:111], v[120:123], v[196:199]\n"
"v_add_f32_e32 v157, v157, v177\n"
"v_add_f32_e32 v158, v158, v178\n"
"v_mfma_f32_16x16x32_bf16 v[196:199], a[104:107], v[112:115], v[196:199]\n"
"ds_write_b128 v3, v[160:163] offset:14336\n"
"v_mfma_f32_16x16x32_bf16 v[196:199], a[100:103], v[100:103], v[196:199]\n"
"buffer_load_dwordx4 v[160:163], v1, s[4:7], s98 offen sc1\n"
"v_mfma_f32_16x16x32_bf16 v[196:199], a[96:99], v[96:99], v[196:199]\n"
"s_add_u32 s96, s61, 0x840000\n"
"v_add_f32_e32 v159, v159, v179\n"
"s_waitcnt lgkmcnt(4)\n"
"v_mfma_f32_16x16x32_bf16 v[176:179], v[24:27], v[96:99], 0\n"
"buffer_store_dwordx4 v[148:151], v0, s[8:11], s96 offen offset:1024 sc1\n"
"v_mfma_f32_16x16x32_bf16 v[176:179], v[28:31], v[100:103], v[176:179]\n"
"ds_read_b128 v[212:215], v4 offset:3072\n"
"ds_read_b128 v[216:219], v9 offset:832\n"
"v_mfma_f32_16x16x32_bf16 v[176:179], v[32:35], v[112:115], v[176:179]\n"
"ds_read_b128 v[220:223], v5 offset:3072\n"
"v_fmac_f32_e32 v184, v254, v188\n"
"v_mfma_f32_16x16x32_bf16 v[176:179], v[36:39], v[120:123], v[176:179]\n"
"buffer_load_dwordx4 v[224:227], v1, s[4:7], s98 offen offset:1024 sc1\n"
"v_mfma_f32_16x16x32_bf16 v[176:179], v[40:43], v[104:107], v[176:179]\n"
"v_fmac_f32_e32 v185, v254, v189\n"
"v_fmac_f32_e32 v186, v254, v190\n"
"v_mfma_f32_16x16x32_bf16 v[176:179], v[44:47], v[152:155], v[176:179]\n"
"ds_write_b128 v3, v[196:199] offset:15360\n"
"v_mfma_f32_16x16x32_bf16 v[176:179], v[48:51], v[108:111], v[176:179]\n"
"buffer_load_dwordx4 a[180:183], v1, s[4:7], s97 offen offset:1024 sc1\n"
"v_mfma_f32_16x16x32_bf16 v[176:179], v[52:55], v[144:147], v[176:179]\n"
"v_fmac_f32_e32 v187, v254, v191\n"
"v_add_f32_e32 v184, v184, v116\n"
"v_mfma_f32_16x16x32_bf16 v[188:191], v[84:87], v[144:147], 0\n"
"buffer_load_dwordx4 v[144:147], v1, s[4:7], s98 offen offset:2048 sc1\n"
"v_mfma_f32_16x16x32_bf16 v[188:191], v[80:83], v[108:111], v[188:191]\n"
"ds_read_b128 v[108:111], v4 offset:4096\n"
"ds_read_b128 v[196:199], v9 offset:512\n"
"v_mfma_f32_16x16x32_bf16 v[188:191], v[76:79], v[152:155], v[188:191]\n"
"ds_read_b128 v[152:155], v5 offset:4096\n"
"v_add_f32_e32 v185, v185, v117\n"
"v_mfma_f32_16x16x32_bf16 v[188:191], v[72:75], v[104:107], v[188:191]\n"
"s_waitcnt lgkmcnt(8)\n"
"v_add_f32_e32 v186, v186, v118\n"
"v_add_f32_e32 v187, v187, v119\n"
"v_mfma_f32_16x16x32_bf16 v[188:191], v[68:71], v[120:123], v[188:191]\n"
"v_fmac_f32_e32 v200, v254, v204\n"
"v_fmac_f32_e32 v201, v254, v205\n"
"v_mfma_f32_16x16x32_bf16 v[188:191], v[64:67], v[112:115], v[188:191]\n"
"buffer_load_dwordx4 v[104:107], v1, s[4:7], s98 offen offset:3072 sc1\n"
"v_mfma_f32_16x16x32_bf16 v[188:191], v[60:63], v[100:103], v[188:191]\n"
"ds_write_b128 v3, v[176:179] offset:16384\n"
"v_mfma_f32_16x16x32_bf16 v[188:191], v[56:59], v[96:99], v[188:191]\n"
"s_waitcnt vmcnt(18)\n"
"ds_read_b128 v[96:99], v4 offset:5120\n"
"ds_read_b128 v[100:103], v9 offset:576\n"
"; @B G1 1.3\n"
"v_mfma_f32_16x16x32_bf16 v[112:115], a[0:3], v[92:95], v[156:159]\n"
"ds_read_b128 v[116:119], v5 offset:5120\n"
"v_fmac_f32_e32 v202, v254, v206\n"
"v_mfma_f32_16x16x32_bf16 v[112:115], a[4:7], v[124:127], v[112:115]\n"
"v_fmac_f32_e32 v203, v254, v207\n"
"v_add_f32_e32 v200, v200, v208\n"
"v_mfma_f32_16x16x32_bf16 v[112:115], a[8:11], v[132:135], v[112:115]\n"
"s_add_u32 s98, s53, 0x408000\n"
"buffer_load_dwordx4 v[120:123], v0, s[4:7], s98 offen sc1\n"
"v_mfma_f32_16x16x32_bf16 v[112:115], a[12:15], v[140:143], v[112:115]\n"
"s_waitcnt vmcnt(13)\n"
"v_add_f32_e32 v201, v201, v209\n"
"v_add_f32_e32 v202, v202, v210\n"
"v_mfma_f32_16x16x32_bf16 v[112:115], a[16:19], v[128:131], v[112:115]\n"
"s_waitcnt lgkmcnt(8)\n"
"v_add_f32_e32 v203, v203, v211\n"
"v_fmac_f32_e32 v212, v254, v216\n"
"v_mfma_f32_16x16x32_bf16 v[112:115], a[20:23], v[168:171], v[112:115]\n"
"ds_write_b128 v3, v[188:191] offset:17408\n"
"v_mfma_f32_16x16x32_bf16 v[112:115], a[24:27], v[172:175], v[112:115]\n"
"v_fmac_f32_e32 v213, v254, v217\n"
"v_fmac_f32_e32 v214, v254, v218\n"
"v_mfma_f32_16x16x32_bf16 v[112:115], a[28:31], v[180:183], v[112:115]\n"
"buffer_load_dwordx4 v[176:179], v0, s[4:7], s98 offen offset:1024 sc1\n"
"v_mfma_f32_16x16x32_bf16 v[188:191], a[60:63], v[180:183], v[184:187]\n"
"v_fmac_f32_e32 v215, v254, v219\n"
"v_add_f32_e32 v212, v212, v220\n"
"v_mfma_f32_16x16x32_bf16 v[188:191], a[56:59], v[172:175], v[188:191]\n"
"v_add_f32_e32 v213, v213, v221\n"
"v_add_f32_e32 v214, v214, v222\n"
"v_mfma_f32_16x16x32_bf16 v[188:191], a[52:55], v[168:171], v[188:191]\n"
"s_waitcnt lgkmcnt(0)\n"
"s_barrier\n"
"v_mfma_f32_16x16x32_bf16 v[188:191], a[48:51], v[128:131], v[188:191]\n"
"buffer_load_dwordx4 a[184:187], v1, s[4:7], s97 offen offset:2048 sc1\n"
"v_mfma_f32_16x16x32_bf16 v[188:191], a[44:47], v[140:143], v[188:191]\n"
"ds_read_b128 v[204:207], v6\n"
"v_cmp_u_f32_e32 vcc, v112, v112\n"
"v_mfma_f32_16x16x32_bf16 v[188:191], a[40:43], v[132:135], v[188:191]\n"
"s_cbranch_vccnz .Lmk_p51s\n"
".Lmk_p51r:\n"
"ds_read_b128 v[156:159], v6 offset:1024\n"
"v_mfma_f32_16x16x32_bf16 v[188:191], a[36:39], v[124:127], v[188:191]\n"
"buffer_load_dwordx4 v[184:187], v0, s[4:7], s98 offen offset:2048 sc1\n"
"v_mfma_f32_16x16x32_bf16 v[188:191], a[32:35], v[92:95], v[188:191]\n"
"v_add_f32_e32 v215, v215, v223\n"
"v_fmac_f32_e32 v108, v254, v196\n"
"v_mfma_f32_16x16x32_bf16 v[200:203], a[64:67], v[92:95], v[200:203]\n"
"v_fmac_f32_e32 v109, v254, v197\n"
"v_fmac_f32_e32 v110, v254, v198\n"
"v_mfma_f32_16x16x32_bf16 v[200:203], a[68:71], v[124:127], v[200:203]\n"
"buffer_load_dwordx4 v[208:211], v0, s[4:7], s98 offen offset:3072 sc1\n"
"v_mfma_f32_16x16x32_bf16 v[200:203], a[72:75], v[132:135], v[200:203]\n"
"v_fmac_f32_e32 v111, v254, v199\n"
"v_add_f32_e32 v108, v108, v152\n"
"v_mfma_f32_16x16x32_bf16 v[200:203], a[76:79], v[140:143], v[200:203]\n"
"v_add_f32_e32 v109, v109, v153\n"
"v_add_f32_e32 v110, v110, v154\n"
"v_mfma_f32_16x16x32_bf16 v[200:203], a[80:83], v[128:131], v[200:203]\n"
"v_add_f32_e32 v111, v111, v155\n"
"v_fmac_f32_e32 v96, v254, v100\n"
"v_mfma_f32_16x16x32_bf16 v[200:203], a[84:87], v[168:171], v[200:203]\n"
"s_waitcnt lgkmcnt(0)\n"
"v_fmac_f32_e32 v97, v254, v101\n"
"v_fmac_f32_e32 v98, v254, v102\n"
"v_mfma_f32_16x16x32_bf16 v[200:203], a[88:91], v[172:175], v[200:203]\n"
"buffer_load_dwordx4 v[152:155], v1, s[4:7], s98 offen sc1\n"
"v_mfma_f32_16x16x32_bf16 v[200:203], a[92:95], v[180:183], v[200:203]\n"
"ds_read_b128 v[196:199], v6 offset:2048\n"
"ds_read_b128 v[216:219], v6 offset:3072\n"
"v_mfma_f32_16x16x32_bf16 v[212:215], a[124:127], v[180:183], v[212:215]\n"
"v_fmac_f32_e32 v99, v254, v103\n"
"v_add_f32_e32 v96, v96, v116\n"
"v_mfma_f32_16x16x32_bf16 v[212:215], a[120:123], v[172:175], v[212:215]\n"
"v_add_f32_e32 v97, v97, v117\n"
"v_add_f32_e32 v98, v98, v118\n"
"v_mfma_f32_16x16x32_bf16 v[212:215], a[116:119], v[168:171], v[212:215]\n"
"v_add_f32_e32 v99, v99, v119\n"
"v_add_f32_e32 v112, v112, v204\n"
"v_mfma_f32_16x16x32_bf16 v[212:215], a[112:115], v[128:131], v[212:215]\n"
"buffer_load_dwordx4 v[100:103], v1, s[4:7], s98 offen offset:1024 sc1\n"
"v_mfma_f32_16x16x32_bf16 v[212:215], a[108:111], v[140:143], v[212:215]\n"
"v_add_f32_e32 v113, v113, v205\n"
"v_add_f32_e32 v114, v114, v206\n"
"v_mfma_f32_16x16x32_bf16 v[212:215], a[104:107], v[132:135], v[212:215]\n"
"v_add_f32_e32 v115, v115, v207\n"
"v_add_f32_e32 v188, v188, v156\n"
"v_mfma_f32_16x16x32_bf16 v[212:215], a[100:103], v[124:127], v[212:215]\n"
"v_add_f32_e32 v189, v189, v157\n"
"v_add_f32_e32 v190, v190, v158\n"
"v_mfma_f32_16x16x32_bf16 v[212:215], a[96:99], v[92:95], v[212:215]\n"
"buffer_load_dwordx4 a[188:191], v1, s[4:7], s97 offen offset:3072 sc1\n"
"v_mfma_f32_16x16x32_bf16 v[108:111], v[24:27], v[92:95], v[108:111]\n"
"buffer_load_dwordx4 v[116:119], v1, s[4:7], s98 offen offset:2048 sc1\n"
"v_mfma_f32_16x16x32_bf16 v[108:111], v[28:31], v[124:127], v[108:111]\n"
"v_add_f32_e32 v191, v191, v159\n"
"v_mul_f32_e32 v112, v112, v255\n"
"v_mfma_f32_16x16x32_bf16 v[108:111], v[32:35], v[132:135], v[108:111]\n"
"v_mul_f32_e32 v113, v113, v255\n"
"v_mul_f32_e32 v114, v114, v255\n"
"v_mfma_f32_16x16x32_bf16 v[108:111], v[36:39], v[140:143], v[108:111]\n"
"v_mul_f32_e32 v115, v115, v255\n"
"v_mul_f32_e32 v188, v188, v255\n"
"v_mfma_f32_16x16x32_bf16 v[108:111], v[40:43], v[128:131], v[108:111]\n"
"s_waitcnt lgkmcnt(0)\n"
"v_mul_f32_e32 v189, v189, v255\n"
"v_mul_f32_e32 v190, v190, v255\n"
"v_mfma_f32_16x16x32_bf16 v[108:111], v[44:47], v[168:171], v[108:111]\n"
"buffer_load_dwordx4 v[156:159], v1, s[4:7], s98 offen offset:3072 sc1\n"
"v_mfma_f32_16x16x32_bf16 v[108:111], v[48:51], v[172:175], v[108:111]\n"
"s_add_u32 s98, s63, 0x84000\n"
"ds_read_b128 v[204:207], v6 offset:4096\n"
"ds_read_b128 v[220:223], v6 offset:5120\n"
"v_mfma_f32_16x16x32_bf16 v[108:111], v[52:55], v[180:183], v[108:111]\n"
"v_mul_f32_e32 v191, v191, v255\n"
"v_cvt_pk_bf16_f32 v228, v112, v113\n"
"v_mfma_f32_16x16x32_bf16 v[96:99], v[84:87], v[180:183], v[96:99]\n"
"buffer_store_dword v148, v14, s[8:11], s98 offen sc1\n"
"v_mfma_f32_16x16x32_bf16 v[96:99], v[80:83], v[172:175], v[96:99]\n"
"v_cvt_pk_bf16_f32 v229, v114, v115\n"
"v_cvt_pk_bf16_f32 v230, v188, v189\n"
"v_mfma_f32_16x16x32_bf16 v[96:99], v[76:79], v[168:171], v[96:99]\n"
"v_cvt_pk_bf16_f32 v231, v190, v191\n"
"v_add_f32_e32 v200, v200, v196\n"
"v_mfma_f32_16x16x32_bf16 v[96:99], v[72:75], v[128:131], v[96:99]\n"
"ds_write_b128 v8, v[228:231] offset:25600\n"
"v_mfma_f32_16x16x32_bf16 v[96:99], v[68:71], v[140:143], v[96:99]\n"
"v_add_f32_e32 v201, v201, v197\n"
"v_add_f32_e32 v202, v202, v198\n"
"v_mfma_f32_16x16x32_bf16 v[96:99], v[64:67], v[132:135], v[96:99]\n"
"v_add_f32_e32 v203, v203, v199\n"
"v_add_f32_e32 v212, v212, v216\n"
"v_mfma_f32_16x16x32_bf16 v[96:99], v[60:63], v[124:127], v[96:99]\n"
"v_add_f32_e32 v213, v213, v217\n"
"v_add_f32_e32 v214, v214, v218\n"
"v_mfma_f32_16x16x32_bf16 v[96:99], v[56:59], v[92:95], v[96:99]\n"
"v_add_f32_e32 v215, v215, v219\n"
"v_mul_f32_e32 v200, v200, v255\n"
"v_mul_f32_e32 v201, v201, v255\n"
"v_mul_f32_e32 v202, v202, v255\n"
"v_mul_f32_e32 v203, v203, v255\n"
"v_mul_f32_e32 v212, v212, v255\n"
"v_mul_f32_e32 v213, v213, v255\n"
"v_mul_f32_e32 v214, v214, v255\n"
"v_mul_f32_e32 v215, v215, v255\n"
"v_cvt_pk_bf16_f32 v92, v200, v201\n"
"v_cvt_pk_bf16_f32 v93, v202, v203\n"
"v_cvt_pk_bf16_f32 v94, v212, v213\n"
"v_cvt_pk_bf16_f32 v95, v214, v215\n"
"ds_write_b128 v7, v[92:95] offset:58368\n"
".Lmk_b2:\n"
"s_waitcnt vmcnt(17)\n"
"; @B G1 2.0\n"
"v_mfma_f32_16x16x32_bf16 v[112:115], a[0:3], v[136:139], 0\n"
"v_mfma_f32_16x16x32_bf16 v[112:115], a[4:7], v[88:91], v[112:115]\n"
"s_add_u32 s98, s62, 0x40800\n"
"v_mfma_f32_16x16x32_bf16 v[112:115], a[8:11], v[192:195], v[112:115]\n"
"s_add_u32 s97, s54, 0x408000\n"
"buffer_load_dwordx4 v[124:127], v0, s[4:7], s97 offen sc1\n"
"v_mfma_f32_16x16x32_bf16 v[112:115], a[12:15], v[164:167], v[112:115]\n"
"s_waitcnt vmcnt(12)\n"
"v_mfma_f32_16x16x32_bf16 v[112:115], a[16:19], v[160:163], v[112:115]\n"
"buffer_load_dword v255, v10, s[4:7], s98 offen sc1\n"
"v_mfma_f32_16x16x32_bf16 v[112:115], a[20:23], v[224:227], v[112:115]\n"
"s_waitcnt lgkmcnt(2)\n"
"v_add_f32_e32 v108, v108, v204\n"
"v_add_f32_e32 v109, v109, v205\n"
"v_mfma_f32_16x16x32_bf16 v[112:115], a[24:27], v[144:147], v[112:115]\n"
"v_add_f32_e32 v110, v110, v206\n"
"v_add_f32_e32 v111, v111, v207\n"
"v_mfma_f32_16x16x32_bf16 v[112:115], a[28:31], v[104:107], v[112:115]\n"
"buffer_load_dwordx4 v[128:131], v0, s[4:7], s97 offen offset:1024 sc1\n"
"v_mfma_f32_16x16x32_bf16 v[132:135], a[60:63], v[104:107], 0\n"
"s_add_u32 s96, s70, 0x204000\n"
"buffer_load_dwordx4 a[192:195], v0, s[4:7], s96 offen sc1\n"
"v_mfma_f32_16x16x32_bf16 v[132:135], a[56:59], v[144:147], v[132:135]\n"
"buffer_load_dword v254, v10, s[4:7], s98 offen offset:256 sc1\n"
"v_mfma_f32_16x16x32_bf16 v[132:135], a[52:55], v[224:227], v[132:135]\n"
"v_add_f32_e32 v96, v96, v220\n"
"v_add_f32_e32 v97, v97, v221\n"
"v_mfma_f32_16x16x32_bf16 v[132:135], a[48:51], v[160:163], v[132:135]\n"
"v_add_f32_e32 v98, v98, v222\n"
"v_add_f32_e32 v99, v99, v223\n"
"v_mfma_f32_16x16x32_bf16 v[132:135], a[44:47], v[164:167], v[132:135]\n"
"buffer_load_dwordx4 v[140:143], v0, s[4:7], s97 offen offset:2048 sc1\n"
"v_mfma_f32_16x16x32_bf16 v[132:135], a[40:43], v[192:195], v[132:135]\n"
"ds_write_b128 v3, v[112:115]\n"
"v_mfma_f32_16x16x32_bf16 v[132:135], a[36:39], v[88:91], v[132:135]\n"
"buffer_load_dword v252, v10, s[4:7], s98 offen offset:512 sc1\n"
"v_mfma_f32_16x16x32_bf16 v[132:135], a[32:35], v[136:139], v[132:135]\n"
"v_mul_f32_e32 v108, v108, v253\n"
"v_mul_f32_e32 v109, v109, v253\n"
"v_mfma_f32_16x16x32_bf16 v[112:115], a[64:67], v[136:139], 0\n"
"v_mul_f32_e32 v110, v110, v253\n"
"v_mul_f32_e32 v111, v111, v253\n"
"v_mfma_f32_16x16x32_bf16 v[112:115], a[68:71], v[88:91], v[112:115]\n"
"buffer_load_dwordx4 v[148:151], v0, s[4:7], s97 offen offset:3072 sc1\n"
"v_mfma_f32_16x16x32_bf16 v[112:115], a[72:75], v[192:195], v[112:115]\n"
"buffer_load_dword v251, v10, s[4:7], s98 offen offset:768 sc1\n"
"v_mfma_f32_16x16x32_bf16 v[112:115], a[76:79], v[164:167], v[112:115]\n"
"v_mul_f32_e32 v96, v96, v253\n"
"v_mul_f32_e32 v97, v97, v253\n"
"v_mfma_f32_16x16x32_bf16 v[112:115], a[80:83], v[160:163], v[112:115]\n"
"v_mul_f32_e32 v98, v98, v253\n"
"v_mul_f32_e32 v99, v99, v253\n"
"v_mfma_f32_16x16x32_bf16 v[112:115], a[84:87], v[224:227], v[112:115]\n"
"ds_write_b128 v3, v[132:135] offset:1024\n"
"v_mfma_f32_16x16x32_bf16 v[112:115], a[88:91], v[144:147], v[112:115]\n"
"buffer_load_dwordx4 v[132:135], v1, s[4:7], s97 offen sc1\n"
"v_mfma_f32_16x16x32_bf16 v[112:115], a[92:95], v[104:107], v[112:115]\n"
"buffer_load_dword v253, v10, s[4:7], s98 offen offset:1024 sc1\n"
"v_mfma_f32_16x16x32_bf16 v[168:171], a[124:127], v[104:107], 0\n"
"v_cvt_pk_bf16_f32 v172, v108, v109\n"
"v_cvt_pk_bf16_f32 v173, v110, v111\n"
"v_mfma_f32_16x16x32_bf16 v[168:171], a[120:123], v[144:147], v[168:171]\n"
"v_cvt_pk_bf16_f32 v174, v96, v97\n"
"v_cvt_pk_bf16_f32 v175, v98, v99\n"
"v_mfma_f32_16x16x32_bf16 v[168:171], a[116:119], v[224:227], v[168:171]\n"
"buffer_load_dword v250, v10, s[4:7], s98 offen offset:1280 sc1\n"
"v_mfma_f32_16x16x32_bf16 v[168:171], a[112:115], v[160:163], v[168:171]\n"
"buffer_load_dwordx4 v[96:99], v1, s[4:7], s97 offen offset:1024 sc1\n"
"v_mfma_f32_16x16x32_bf16 v[168:171], a[108:111], v[164:167], v[168:171]\n"
"buffer_load_dwordx4 a[196:199], v0, s[4:7], s96 offen offset:1024 sc1\n"
"v_mfma_f32_16x16x32_bf16 v[168:171], a[104:107], v[192:195], v[168:171]\n"
"ds_write_b128 v3, v[112:115] offset:2048\n"
"v_mfma_f32_16x16x32_bf16 v[168:171], a[100:103], v[88:91], v[168:171]\n"
"buffer_load_dword v249, v10, s[4:7], s98 offen offset:1536 sc1\n"
"v_mfma_f32_16x16x32_bf16 v[168:171], a[96:99], v[136:139], v[168:171]\n"
"ds_write_b128 v2, v[172:175] offset:50176\n"
"v_mfma_f32_16x16x32_bf16 v[108:111], v[24:27], v[136:139], 0\n"
"buffer_load_dwordx4 v[112:115], v1, s[4:7], s97 offen offset:2048 sc1\n"
"s_add_u32 s95, s64, 0x802000\n"
"v_mfma_f32_16x16x32_bf16 v[108:111], v[28:31], v[88:91], v[108:111]\n"
"buffer_load_dword v248, v10, s[4:7], s98 offen offset:1792 sc1\n"
"v_add_u32_e32 v247, 0x26c00, v0\n"
"v_mfma_f32_16x16x32_bf16 v[108:111], v[32:35], v[192:195], v[108:111]\n"
"buffer_store_dwordx4 v[228:231], v8, s[12:15], s95 offen offset:1024 sc1\n"
"v_mfma_f32_16x16x32_bf16 v[108:111], v[36:39], v[164:167], v[108:111]\n"
"ds_read_b128 v[172:175], v247\n"
"ds_read_b128 v[180:183], v247 offset:1024\n"
"v_xor_b32_e32 v247, 32, v15\n"
"v_mfma_f32_16x16x32_bf16 v[108:111], v[40:43], v[160:163], v[108:111]\n"
"ds_read_b128 v[188:191], v2 offset:50176\n"
"ds_read_b128 v[196:199], v15 offset:24576\n"
"ds_read_b128 v[200:203], v247 offset:24832\n"
"v_mfma_f32_16x16x32_bf16 v[108:111], v[44:47], v[224:227], v[108:111]\n"
"buffer_load_dwordx4 v[204:207], v1, s[4:7], s97 offen offset:3072 sc1\n"
"v_mfma_f32_16x16x32_bf16 v[108:111], v[48:51], v[144:147], v[108:111]\n"
"ds_write_b128 v3, v[168:171] offset:3072\n"
"v_mfma_f32_16x16x32_bf16 v[108:111], v[52:55], v[104:107], v[108:111]\n"
"v_mfma_f32_16x16x32_bf16 v[168:171], v[84:87], v[104:107], 0\n"
"v_mfma_f32_16x16x32_bf16 v[168:171], v[80:83], v[144:147], v[168:171]\n"
"s_waitcnt lgkmcnt(1)\n"
"v_mfma_f32_16x16x32_bf16 v[172:175], v[196:199], v[188:191], v[172:175]\n"
"v_mfma_f32_16x16x32_bf16 v[180:183], v[200:203], v[188:191], v[180:183]\n"
"ds_read_b64_tr_b16 v[104:105], v16 offset:24576\n"
"v_xor_b32_e32 v247, 32, v16\n"
"v_mfma_f32_16x16x32_bf16 v[168:171], v[76:79], v[224:227], v[168:171]\n"
"ds_read_b64_tr_b16 v[106:107], v247 offset:24832\n"
"ds_read_b64_tr_b16 v[144:145], v247 offset:24576\n"
"ds_read_b64_tr_b16 v[146:147], v16 offset:24832\n"
"v_mov_b32_e32 v188, 0x3f803f80\n"
"v_mfma_f32_16x16x32_bf16 v[168:171], v[72:75], v[160:163], v[168:171]\n"
"v_mov_b32_e32 v189, 0x3f803f80\n"
"v_mov_b32_e32 v190, 0x3f803f80\n"
"v_mov_b32_e32 v191, 0x3f803f80\n"
"v_mfma_f32_16x16x32_bf16 v[168:171], v[68:71], v[164:167], v[168:171]\n"
"v_max_f32_e32 v247, v172, v173\n"
"v_max3_f32 v247, v247, v174, v175\n"
"v_max3_f32 v247, v247, v180, v181\n"
"v_mfma_f32_16x16x32_bf16 v[168:171], v[64:67], v[192:195], v[168:171]\n"
"ds_write_b128 v3, v[108:111] offset:4096\n"
"v_mfma_f32_16x16x32_bf16 v[168:171], v[60:63], v[88:91], v[168:171]\n"
"v_max3_f32 v247, v247, v182, v183\n"
"v_mov_b32_e32 v246, v247\n"
"v_mfma_f32_16x16x32_bf16 v[168:171], v[56:59], v[136:139], v[168:171]\n"
"s_waitcnt vmcnt(25)\n"
"v_permlane16_swap_b32_e32 v247, v246\n"
"v_max_f32_e32 v247, v247, v246\n"
"v_mov_b32_e32 v246, v247\n"
"; @B G1 2.1\n"
"v_mfma_f32_16x16x32_bf16 v[88:91], a[0:3], v[120:123], 0\n"
"buffer_load_dwordx4 a[200:203], v0, s[4:7], s96 offen offset:2048 sc1\n"
"v_permlane32_swap_b32_e32 v247, v246\n"
"v_mfma_f32_16x16x32_bf16 v[88:91], a[4:7], v[176:179], v[88:91]\n"
"v_max_f32_e32 v247, v247, v246\n"
"v_sub_f32_e32 v172, v172, v247\n"
"v_sub_f32_e32 v173, v173, v247\n"
"v_mfma_f32_16x16x32_bf16 v[88:91], a[8:11], v[184:187], v[88:91]\n"
"s_add_u32 s98, s55, 0x408000\n"
"buffer_load_dwordx4 v[108:111], v0, s[4:7], s98 offen sc1\n"
"v_mfma_f32_16x16x32_bf16 v[88:91], a[12:15], v[208:211], v[88:91]\n"
"s_waitcnt vmcnt(22)\n"
"v_sub_f32_e32 v174, v174, v247\n"
"v_sub_f32_e32 v175, v175, v247\n"
"v_sub_f32_e32 v180, v180, v247\n"
"v_mfma_f32_16x16x32_bf16 v[88:91], a[16:19], v[152:155], v[88:91]\n"
"v_sub_f32_e32 v181, v181, v247\n"
"v_sub_f32_e32 v182, v182, v247\n"
"v_sub_f32_e32 v183, v183, v247\n"
"v_mfma_f32_16x16x32_bf16 v[88:91], a[20:23], v[100:103], v[88:91]\n"
"ds_write_b128 v3, v[168:171] offset:5120\n"
"v_mfma_f32_16x16x32_bf16 v[88:91], a[24:27], v[116:119], v[88:91]\n"
"v_exp_f32_e32 v172, v172\n"
"v_mfma_f32_16x16x32_bf16 v[88:91], a[28:31], v[156:159], v[88:91]\n"
"buffer_load_dwordx4 v[136:139], v0, s[4:7], s98 offen offset:1024 sc1\n"
"v_mfma_f32_16x16x32_bf16 v[160:163], a[60:63], v[156:159], 0\n"
"s_add_u32 s97, s64, 0x806000\n"
"v_exp_f32_e32 v173, v173\n"
"v_mfma_f32_16x16x32_bf16 v[160:163], a[56:59], v[116:119], v[160:163]\n"
"v_exp_f32_e32 v174, v174\n"
"v_mfma_f32_16x16x32_bf16 v[160:163], a[52:55], v[100:103], v[160:163]\n"
"buffer_store_dwordx4 v[92:95], v7, s[12:15], s97 offen offset:1024 sc1\n"
"v_mfma_f32_16x16x32_bf16 v[160:163], a[48:51], v[152:155], v[160:163]\n"
"v_exp_f32_e32 v175, v175\n"
"v_mfma_f32_16x16x32_bf16 v[160:163], a[44:47], v[208:211], v[160:163]\n"
"buffer_load_dwordx4 v[92:95], v0, s[4:7], s98 offen offset:2048 sc1\n"
"v_mfma_f32_16x16x32_bf16 v[160:163], a[40:43], v[184:187], v[160:163]\n"
"ds_write_b128 v3, v[88:91] offset:6144\n"
"v_mfma_f32_16x16x32_bf16 v[160:163], a[36:39], v[176:179], v[160:163]\n"
"v_exp_f32_e32 v180, v180\n"
"v_mfma_f32_16x16x32_bf16 v[160:163], a[32:35], v[120:123], v[160:163]\n"
"v_exp_f32_e32 v181, v181\n"
"v_mfma_f32_16x16x32_bf16 v[88:91], a[64:67], v[120:123], 0\n"
"v_exp_f32_e32 v182, v182\n"
"v_mfma_f32_16x16x32_bf16 v[88:91], a[68:71], v[176:179], v[88:91]\n"
"buffer_load_dwordx4 v[164:167], v0, s[4:7], s98 offen offset:3072 sc1\n"
"v_mfma_f32_16x16x32_bf16 v[88:91], a[72:75], v[184:187], v[88:91]\n"
"v_exp_f32_e32 v183, v183\n"
"v_cvt_pk_bf16_f32 v168, v172, v173\n"
"v_mfma_f32_16x16x32_bf16 v[88:91], a[76:79], v[208:211], v[88:91]\n"
"v_cvt_pk_bf16_f32 v169, v174, v175\n"
"v_cvt_pk_bf16_f32 v170, v180, v181\n"
"v_cvt_pk_bf16_f32 v171, v182, v183\n"
"v_mfma_f32_16x16x32_bf16 v[88:91], a[80:83], v[152:155], v[88:91]\n"
"s_waitcnt lgkmcnt(3)\n"
"v_mov_b32_e32 v172, -1\n"
"v_mov_b32_e32 v173, -1\n"
"v_mfma_f32_16x16x32_bf16 v[180:183], v[188:191], v[168:171], 0\n"
"v_mfma_f32_16x16x32_bf16 v[188:191], v[104:107], v[168:171], 0\n"
"v_mfma_f32_16x16x32_bf16 v[104:107], v[144:147], v[168:171], 0\n"
"v_mfma_f32_16x16x32_bf16 v[88:91], a[84:87], v[100:103], v[88:91]\n"
"ds_write_b128 v3, v[160:163] offset:7168\n"
"v_mfma_f32_16x16x32_bf16 v[88:91], a[88:91], v[116:119], v[88:91]\n"
"buffer_load_dwordx4 v[144:147], v1, s[4:7], s98 offen sc1\n"
"v_mfma_f32_16x16x32_bf16 v[88:91], a[92:95], v[156:159], v[88:91]\n"
"v_mov_b32_e32 v174, -1\n"
"v_mov_b32_e32 v175, -1\n"
"v_mfma_f32_16x16x32_bf16 v[160:163], a[124:127], v[156:159], 0\n"
"s_add_u32 s97, s60, 0x848000\n"
"buffer_store_dwordx4 v[172:175], v0, s[8:11], s97 offen sc1\n"
"v_mfma_f32_16x16x32_bf16 v[160:163], a[120:123], v[116:119], v[160:163]\n"
"buffer_load_dwordx4 a[204:207], v0, s[4:7], s96 offen offset:3072 sc1\n"
"v_mfma_f32_16x16x32_bf16 v[160:163], a[116:119], v[100:103], v[160:163]\n"
"v_rcp_f32_e32 v180, v180\n"
"v_mfma_f32_16x16x32_bf16 v[160:163], a[112:115], v[152:155], v[160:163]\n"
"buffer_load_dwordx4 v[168:171], v1, s[4:7], s98 offen offset:1024 sc1\n"
"v_mul_f32_e32 v188, v188, v180\n"
"v_mfma_f32_16x16x32_bf16 v[160:163], a[108:111], v[208:211], v[160:163]\n"
"v_mul_f32_e32 v189, v189, v180\n"
"v_mul_f32_e32 v190, v190, v180\n"
"v_mul_f32_e32 v191, v191, v180\n"
"v_mfma_f32_16x16x32_bf16 v[160:163], a[104:107], v[184:187], v[160:163]\n"
"ds_write_b128 v3, v[88:91] offset:8192\n"
"v_mfma_f32_16x16x32_bf16 v[160:163], a[100:103], v[176:179], v[160:163]\n"
"v_mul_f32_e32 v104, v104, v180\n"
"v_mul_f32_e32 v105, v105, v180\n"
"v_mul_f32_e32 v106, v106, v180\n"
"v_mfma_f32_16x16x32_bf16 v[160:163], a[96:99], v[120:123], v[160:163]\n"
"v_mul_f32_e32 v107, v107, v180\n"
"v_cvt_pk_bf16_f32 v88, v188, v189\n"
"v_cvt_pk_bf16_f32 v89, v190, v191\n"
"v_mfma_f32_16x16x32_bf16 v[180:183], v[24:27], v[120:123], 0\n"
"buffer_load_dwordx4 v[188:191], v1, s[4:7], s98 offen offset:2048 sc1\n"
"v_cvt_pk_bf16_f32 v90, v104, v105\n"
"v_mfma_f32_16x16x32_bf16 v[180:183], v[28:31], v[176:179], v[180:183]\n"
"s_add_u32 s97, s60, 0x448000\n"
"v_cvt_pk_bf16_f32 v91, v106, v107\n"
"v_mfma_f32_16x16x32_bf16 v[180:183], v[32:35], v[184:187], v[180:183]\n"
"buffer_store_dwordx4 v[88:91], v0, s[4:7], s97 offen sc1\n"
"v_mfma_f32_16x16x32_bf16 v[180:183], v[36:39], v[208:211], v[180:183]\n"
"v_mfma_f32_16x16x32_bf16 v[180:183], v[40:43], v[152:155], v[180:183]\n"
"s_waitcnt vmcnt(14)\n"
"v_add_f32_e32 v255, v255, v253\n"
"v_add_f32_e32 v254, v254, v250\n"
"v_mfma_f32_16x16x32_bf16 v[180:183], v[44:47], v[100:103], v[180:183]\n"
"buffer_load_dwordx4 v[88:91], v1, s[4:7], s98 offen offset:3072 sc1\n"
"v_mfma_f32_16x16x32_bf16 v[180:183], v[48:51], v[116:119], v[180:183]\n"
"ds_write_b128 v3, v[160:163] offset:9216\n"
"v_mfma_f32_16x16x32_bf16 v[180:183], v[52:55], v[156:159], v[180:183]\n"
"v_add_f32_e32 v252, v252, v249\n"
"v_add_f32_e32 v251, v251, v248\n"
"v_mfma_f32_16x16x32_bf16 v[104:107], v[84:87], v[156:159], 0\n"
"v_add_f32_e32 v255, v255, v252\n"
"v_add_f32_e32 v254, v254, v251\n"
"v_mfma_f32_16x16x32_bf16 v[104:107], v[80:83], v[116:119], v[104:107]\n"
"v_add_f32_e32 v255, v255, v254\n"
"v_mov_b32_e32 v254, v255\n"
"v_mfma_f32_16x16x32_bf16 v[104:107], v[76:79], v[100:103], v[104:107]\n"
"s_nop 0\n"
"v_permlane32_swap_b32_e32 v255, v254\n"
"v_add_f32_e32 v255, v255, v254\n"
"v_mfma_f32_16x16x32_bf16 v[104:107], v[72:75], v[152:155], v[104:107]\n"
"v_mul_f32_e32 v255, 0x3a800000, v255\n"
"v_mov_b32_e32 v254, v255\n"
"v_mfma_f32_16x16x32_bf16 v[104:107], v[68:71], v[208:211], v[104:107]\n"
"s_nop 0\n"
"v_permlane16_swap_b32_e32 v255, v254\n"
"v_cvt_pk_bf16_f32 v253, -v255, 0\n"
"v_mfma_f32_16x16x32_bf16 v[104:107], v[64:67], v[184:187], v[104:107]\n"
"ds_write_b128 v3, v[180:183] offset:10240\n"
"v_mfma_f32_16x16x32_bf16 v[104:107], v[60:63], v[176:179], v[104:107]\n"
"buffer_load_dwordx4 a[208:211], v1, s[4:7], s96 offen sc1\n"
"v_mfma_f32_16x16x32_bf16 v[104:107], v[56:59], v[120:123], v[104:107]\n"
"v_fma_f32 v254, -v255, v255, v254\n"
"v_lshlrev_b32_e32 v253, 16, v253\n"
"; @B G1 2.2\n"
"v_mfma_f32_16x16x32_bf16 v[100:103], a[0:3], v[124:127], 0\n"
"v_max_f32_e32 v254, 0, v254\n"
"v_cvt_pk_bf16_f32 v254, v254, 0\n"
"v_mfma_f32_16x16x32_bf16 v[100:103], a[4:7], v[128:131], v[100:103]\n"
"v_lshlrev_b32_e32 v254, 16, v254\n"
"v_add_f32_e32 v254, 0x3727c5ac, v254\n"
"v_mfma_f32_16x16x32_bf16 v[100:103], a[8:11], v[140:143], v[100:103]\n"
"v_cvt_pk_bf16_f32 v254, v254, 0\n"
"v_lshlrev_b32_e32 v254, 16, v254\n"
"v_mfma_f32_16x16x32_bf16 v[100:103], a[12:15], v[148:151], v[100:103]\n"
"s_waitcnt vmcnt(14)\n"
"v_sqrt_f32_e32 v254, v254\n"
"v_mfma_f32_16x16x32_bf16 v[100:103], a[16:19], v[132:135], v[100:103]\n"
"s_add_u32 s98, s68, 0x10000\n"
"v_cvt_pk_bf16_f32 v254, v254, 0\n"
"v_mfma_f32_16x16x32_bf16 v[100:103], a[20:23], v[96:99], v[100:103]\n"
"ds_write_b128 v3, v[104:107] offset:11264\n"
"v_mfma_f32_16x16x32_bf16 v[100:103], a[24:27], v[112:115], v[100:103]\n"
"buffer_store_dwordx2 v[18:19], v11, s[4:7], s98 offen offset:32 sc1\n"
"v_mfma_f32_16x16x32_bf16 v[100:103], a[28:31], v[204:207], v[100:103]\n"
"v_lshlrev_b32_e32 v254, 16, v254\n"
"v_mfma_f32_16x16x32_bf16 v[104:107], a[60:63], v[204:207], 0\n"
"v_rcp_f32_e32 v255, v254\n"
"v_mfma_f32_16x16x32_bf16 v[104:107], a[56:59], v[112:115], v[104:107]\n"
"v_mul_f32_e32 v254, 0x3e8293ee, v255\n"
"v_mfma_f32_16x16x32_bf16 v[104:107], a[52:55], v[96:99], v[104:107]\n"
"s_add_u32 s98, s52, 0x400000\n"
"buffer_load_dwordx4 v[116:119], v0, s[4:7], s98 offen sc1\n"
"v_mfma_f32_16x16x32_bf16 v[104:107], a[48:51], v[132:135], v[104:107]\n"
"s_waitcnt lgkmcnt(0)\n"
"s_barrier\n"
"v_mfma_f32_16x16x32_bf16 v[104:107], a[44:47], v[148:151], v[104:107]\n"
"ds_read_b128 v[120:123], v4\n"
"ds_read_b128 v[152:155], v9 offset:640\n"
"v_mfma_f32_16x16x32_bf16 v[104:107], a[40:43], v[140:143], v[104:107]\n"
"ds_write_b128 v3, v[100:103] offset:12288\n"
"v_mfma_f32_16x16x32_bf16 v[104:107], a[36:39], v[128:131], v[104:107]\n"
"ds_read_b128 v[100:103], v5\n"
"v_mfma_f32_16x16x32_bf16 v[104:107], a[32:35], v[124:127], v[104:107]\n"
"buffer_load_dwordx4 v[156:159], v0, s[4:7], s98 offen offset:1024 sc1\n"
"v_mfma_f32_16x16x32_bf16 v[160:163], a[64:67], v[124:127], 0\n"
"v_mfma_f32_16x16x32_bf16 v[160:163], a[68:71], v[128:131], v[160:163]\n"
"v_mfma_f32_16x16x32_bf16 v[160:163], a[72:75], v[140:143], v[160:163]\n"
"buffer_load_dwordx4 a[212:215], v1, s[4:7], s96 offen offset:1024 sc1\n"
"v_mfma_f32_16x16x32_bf16 v[160:163], a[76:79], v[148:151], v[160:163]\n"
"ds_read_b128 v[176:179], v4 offset:1024\n"
"ds_read_b128 v[180:183], v9 offset:704\n"
"v_mfma_f32_16x16x32_bf16 v[160:163], a[80:83], v[132:135], v[160:163]\n"
"buffer_load_dwordx4 v[184:187], v0, s[4:7], s98 offen offset:2048 sc1\n"
"v_mfma_f32_16x16x32_bf16 v[160:163], a[84:87], v[96:99], v[160:163]\n"
"ds_write_b128 v3, v[104:107] offset:13312\n"
"v_mfma_f32_16x16x32_bf16 v[160:163], a[88:91], v[112:115], v[160:163]\n"
"ds_read_b128 v[104:107], v5 offset:1024\n"
"s_waitcnt lgkmcnt(4)\n"
"v_mfma_f32_16x16x32_bf16 v[160:163], a[92:95], v[204:207], v[160:163]\n"
"v_fmac_f32_e32 v120, v253, v152\n"
"v_fmac_f32_e32 v121, v253, v153\n"
"v_mfma_f32_16x16x32_bf16 v[192:195], a[124:127], v[204:207], 0\n"
"v_fmac_f32_e32 v122, v253, v154\n"
"v_fmac_f32_e32 v123, v253, v155\n"
"v_mfma_f32_16x16x32_bf16 v[192:195], a[120:123], v[112:115], v[192:195]\n"
"buffer_load_dwordx4 v[152:155], v0, s[4:7], s98 offen offset:3072 sc1\n"
"v_mfma_f32_16x16x32_bf16 v[192:195], a[116:119], v[96:99], v[192:195]\n"
"ds_read_b128 v[196:199], v4 offset:2048\n"
"ds_read_b128 v[200:203], v9 offset:768\n"
"v_mfma_f32_16x16x32_bf16 v[192:195], a[112:115], v[132:135], v[192:195]\n"
"ds_read_b128 v[208:211], v5 offset:2048\n"
"v_add_f32_e32 v120, v120, v100\n"
"v_mfma_f32_16x16x32_bf16 v[192:195], a[108:111], v[148:151], v[192:195]\n"
"v_add_f32_e32 v121, v121, v101\n"
"v_add_f32_e32 v122, v122, v102\n"
"v_mfma_f32_16x16x32_bf16 v[192:195], a[104:107], v[140:143], v[192:195]\n"
"ds_write_b128 v3, v[160:163] offset:14336\n"
"v_mfma_f32_16x16x32_bf16 v[192:195], a[100:103], v[128:131], v[192:195]\n"
"buffer_load_dwordx4 v[160:163], v1, s[4:7], s98 offen sc1\n"
"v_mfma_f32_16x16x32_bf16 v[192:195], a[96:99], v[124:127], v[192:195]\n"
"s_add_u32 s97, s60, 0x840000\n"
"v_add_f32_e32 v123, v123, v103\n"
"s_waitcnt lgkmcnt(4)\n"
"v_mfma_f32_16x16x32_bf16 v[100:103], v[24:27], v[124:127], 0\n"
"buffer_store_dwordx4 v[172:175], v0, s[8:11], s97 offen sc1\n"
"v_mfma_f32_16x16x32_bf16 v[100:103], v[28:31], v[128:131], v[100:103]\n"
"ds_read_b128 v[212:215], v4 offset:3072\n"
"ds_read_b128 v[216:219], v9 offset:832\n"
"v_mfma_f32_16x16x32_bf16 v[100:103], v[32:35], v[140:143], v[100:103]\n"
"s_waitcnt vmcnt(18)\n"
"s_add_u32 s97, s68, 0x11000\n"
"ds_read_b128 v[220:223], v5 offset:3072\n"
"v_mfma_f32_16x16x32_bf16 v[100:103], v[36:39], v[148:151], v[100:103]\n"
"buffer_load_dwordx4 v[224:227], v1, s[4:7], s98 offen offset:1024 sc1\n"
"v_mfma_f32_16x16x32_bf16 v[100:103], v[40:43], v[132:135], v[100:103]\n"
"buffer_store_dwordx2 v[18:19], v11, s[12:15], s97 offen offset:64 sc1\n"
"v_mfma_f32_16x16x32_bf16 v[100:103], v[44:47], v[96:99], v[100:103]\n"
"ds_write_b128 v3, v[192:195] offset:15360\n"
"v_mfma_f32_16x16x32_bf16 v[100:103], v[48:51], v[112:115], v[100:103]\n"
"buffer_load_dwordx4 a[216:219], v1, s[4:7], s96 offen offset:2048 sc1\n"
"v_mfma_f32_16x16x32_bf16 v[100:103], v[52:55], v[204:207], v[100:103]\n"
"v_fmac_f32_e32 v176, v253, v180\n"
"v_fmac_f32_e32 v177, v253, v181\n"
"v_mfma_f32_16x16x32_bf16 v[192:195], v[84:87], v[204:207], 0\n"
"buffer_load_dwordx4 v[204:207], v1, s[4:7], s98 offen offset:2048 sc1\n"
"v_mfma_f32_16x16x32_bf16 v[192:195], v[80:83], v[112:115], v[192:195]\n"
"ds_read_b128 v[112:115], v4 offset:4096\n"
"ds_read_b128 v[228:231], v9 offset:512\n"
"v_mfma_f32_16x16x32_bf16 v[192:195], v[76:79], v[96:99], v[192:195]\n"
"ds_read_b128 v[96:99], v5 offset:4096\n"
"v_fmac_f32_e32 v178, v253, v182\n"
"v_mfma_f32_16x16x32_bf16 v[192:195], v[72:75], v[132:135], v[192:195]\n"
"v_fmac_f32_e32 v179, v253, v183\n"
"v_add_f32_e32 v176, v176, v104\n"
"v_mfma_f32_16x16x32_bf16 v[192:195], v[68:71], v[148:151], v[192:195]\n"
"v_add_f32_e32 v177, v177, v105\n"
"v_add_f32_e32 v178, v178, v106\n"
"v_mfma_f32_16x16x32_bf16 v[192:195], v[64:67], v[140:143], v[192:195]\n"
"buffer_load_dwordx4 v[132:135], v1, s[4:7], s98 offen offset:3072 sc1\n"
"v_mfma_f32_16x16x32_bf16 v[192:195], v[60:63], v[128:131], v[192:195]\n"
"ds_write_b128 v3, v[100:103] offset:16384\n"
"v_mfma_f32_16x16x32_bf16 v[192:195], v[56:59], v[124:127], v[192:195]\n"
"s_waitcnt vmcnt(21)\n"
"ds_read_b128 v[100:103], v4 offset:5120\n"
"ds_read_b128 v[124:127], v9 offset:576\n"
"; @B G1 2.3\n"
"v_mfma_f32_16x16x32_bf16 v[120:123], a[0:3], v[108:111], v[120:123]\n"
"ds_read_b128 v[128:131], v5 offset:5120\n"
"v_add_f32_e32 v179, v179, v107\n"
"v_mfma_f32_16x16x32_bf16 v[120:123], a[4:7], v[136:139], v[120:123]\n"
"s_waitcnt lgkmcnt(12)\n"
"v_fmac_f32_e32 v196, v253, v200\n"
"v_fmac_f32_e32 v197, v253, v201\n"
"v_mfma_f32_16x16x32_bf16 v[120:123], a[8:11], v[92:95], v[120:123]\n"
"s_add_u32 s98, s53, 0x400000\n"
"buffer_load_dwordx4 v[104:107], v0, s[4:7], s98 offen sc1\n"
"v_mfma_f32_16x16x32_bf16 v[120:123], a[12:15], v[164:167], v[120:123]\n"
"s_waitcnt vmcnt(15)\n"
"v_fmac_f32_e32 v198, v253, v202\n"
"v_fmac_f32_e32 v199, v253, v203\n"
"v_mfma_f32_16x16x32_bf16 v[120:123], a[16:19], v[144:147], v[120:123]\n"
"v_add_f32_e32 v196, v196, v208\n"
"v_add_f32_e32 v197, v197, v209\n"
"v_mfma_f32_16x16x32_bf16 v[120:123], a[20:23], v[168:171], v[120:123]\n"
"ds_write_b128 v3, v[192:195] offset:17408\n"
"v_mfma_f32_16x16x32_bf16 v[120:123], a[24:27], v[188:191], v[120:123]\n"
"s_waitcnt lgkmcnt(9)\n"
"v_add_f32_e32 v198, v198, v210\n"
"v_add_f32_e32 v199, v199, v211\n"
"v_mfma_f32_16x16x32_bf16 v[120:123], a[28:31], v[88:91], v[120:123]\n"
"buffer_load_dwordx4 v[140:143], v0, s[4:7], s98 offen offset:1024 sc1\n"
"v_mfma_f32_16x16x32_bf16 v[176:179], a[60:63], v[88:91], v[176:179]\n"
"v_fmac_f32_e32 v212, v253, v216\n"
"v_fmac_f32_e32 v213, v253, v217\n"
"v_mfma_f32_16x16x32_bf16 v[176:179], a[56:59], v[188:191], v[176:179]\n"
"v_fmac_f32_e32 v214, v253, v218\n"
"v_fmac_f32_e32 v215, v253, v219\n"
"v_mfma_f32_16x16x32_bf16 v[176:179], a[52:55], v[168:171], v[176:179]\n"
"s_waitcnt lgkmcnt(0)\n"
"s_barrier\n"
"v_mfma_f32_16x16x32_bf16 v[176:179], a[48:51], v[144:147], v[176:179]\n"
"buffer_load_dwordx4 a[220:223], v1, s[4:7], s96 offen offset:3072 sc1\n"
"v_mfma_f32_16x16x32_bf16 v[176:179], a[44:47], v[164:167], v[176:179]\n"
"buffer_load_dwordx4 v[148:151], v0, s[4:7], s98 offen offset:2048 sc1\n"
"v_mfma_f32_16x16x32_bf16 v[176:179], a[40:43], v[92:95], v[176:179]\n"
"ds_read_b128 v[180:183], v6\n"
"ds_read_b128 v[192:195], v6 offset:1024\n"
"v_mfma_f32_16x16x32_bf16 v[176:179], a[36:39], v[136:139], v[176:179]\n"
"v_add_f32_e32 v212, v212, v220\n"
"v_add_f32_e32 v213, v213, v221\n"
"v_mfma_f32_16x16x32_bf16 v[176:179], a[32:35], v[108:111], v[176:179]\n"
"v_add_f32_e32 v214, v214, v222\n"
"v_add_f32_e32 v215, v215, v223\n"
"v_mfma_f32_16x16x32_bf16 v[196:199], a[64:67], v[108:111], v[196:199]\n"
"v_fmac_f32_e32 v112, v253, v228\n"
"v_fmac_f32_e32 v113, v253, v229\n"
"v_mfma_f32_16x16x32_bf16 v[196:199], a[68:71], v[136:139], v[196:199]\n"
"buffer_load_dwordx4 v[200:203], v0, s[4:7], s98 offen offset:3072 sc1\n"
"v_mfma_f32_16x16x32_bf16 v[196:199], a[72:75], v[92:95], v[196:199]\n"
"v_fmac_f32_e32 v114, v253, v230\n"
"v_fmac_f32_e32 v115, v253, v231\n"
"v_mfma_f32_16x16x32_bf16 v[196:199], a[76:79], v[164:167], v[196:199]\n"
"v_add_f32_e32 v112, v112, v96\n"
"v_add_f32_e32 v113, v113, v97\n"
"v_mfma_f32_16x16x32_bf16 v[196:199], a[80:83], v[144:147], v[196:199]\n"
"v_add_f32_e32 v114, v114, v98\n"
"v_add_f32_e32 v115, v115, v99\n"
"v_mfma_f32_16x16x32_bf16 v[196:199], a[84:87], v[168:171], v[196:199]\n"
"s_waitcnt lgkmcnt(0)\n"
"v_fmac_f32_e32 v100, v253, v124\n"
"v_fmac_f32_e32 v101, v253, v125\n"
"v_mfma_f32_16x16x32_bf16 v[196:199], a[88:91], v[188:191], v[196:199]\n"
"buffer_load_dwordx4 v[96:99], v1, s[4:7], s98 offen sc1\n"
"v_mfma_f32_16x16x32_bf16 v[196:199], a[92:95], v[88:91], v[196:199]\n"
"ds_read_b128 v[208:211], v6 offset:2048\n"
"ds_read_b128 v[216:219], v6 offset:3072\n"
"v_mfma_f32_16x16x32_bf16 v[212:215], a[124:127], v[88:91], v[212:215]\n"
"v_fmac_f32_e32 v102, v253, v126\n"
"v_fmac_f32_e32 v103, v253, v127\n"
"v_mfma_f32_16x16x32_bf16 v[212:215], a[120:123], v[188:191], v[212:215]\n"
"v_add_f32_e32 v100, v100, v128\n"
"v_add_f32_e32 v101, v101, v129\n"
"v_mfma_f32_16x16x32_bf16 v[212:215], a[116:119], v[168:171], v[212:215]\n"
"v_add_f32_e32 v102, v102, v130\n"
"v_add_f32_e32 v103, v103, v131\n"
"v_mfma_f32_16x16x32_bf16 v[212:215], a[112:115], v[144:147], v[212:215]\n"
"buffer_load_dwordx4 v[124:127], v1, s[4:7], s98 offen offset:1024 sc1\n"
"v_mfma_f32_16x16x32_bf16 v[212:215], a[108:111], v[164:167], v[212:215]\n"
"v_add_f32_e32 v120, v120, v180\n"
"v_add_f32_e32 v121, v121, v181\n"
"v_mfma_f32_16x16x32_bf16 v[212:215], a[104:107], v[92:95], v[212:215]\n"
"s_add_u32 s97, s70, 0x206000\n"
"v_add_f32_e32 v122, v122, v182\n"
"v_add_f32_e32 v123, v123, v183\n"
"v_mfma_f32_16x16x32_bf16 v[212:215], a[100:103], v[136:139], v[212:215]\n"
"v_add_f32_e32 v176, v176, v192\n"
"v_add_f32_e32 v177, v177, v193\n"
"v_mfma_f32_16x16x32_bf16 v[212:215], a[96:99], v[108:111], v[212:215]\n"
"buffer_load_dwordx4 a[224:227], v0, s[4:7], s97 offen sc1\n"
"v_mfma_f32_16x16x32_bf16 v[112:115], v[24:27], v[108:111], v[112:115]\n"
"buffer_load_dwordx4 v[128:131], v1, s[4:7], s98 offen offset:2048 sc1\n"
"v_mfma_f32_16x16x32_bf16 v[112:115], v[28:31], v[136:139], v[112:115]\n"
"v_add_f32_e32 v178, v178, v194\n"
"v_add_f32_e32 v179, v179, v195\n"
"v_mfma_f32_16x16x32_bf16 v[112:115], v[32:35], v[92:95], v[112:115]\n"
"v_mul_f32_e32 v120, v120, v255\n"
"v_mul_f32_e32 v121, v121, v255\n"
"v_mfma_f32_16x16x32_bf16 v[112:115], v[36:39], v[164:167], v[112:115]\n"
"v_mul_f32_e32 v122, v122, v255\n"
"v_mul_f32_e32 v123, v123, v255\n"
"v_mfma_f32_16x16x32_bf16 v[112:115], v[40:43], v[144:147], v[112:115]\n"
"s_waitcnt lgkmcnt(0)\n"
"v_mul_f32_e32 v176, v176, v255\n"
"v_mul_f32_e32 v177, v177, v255\n"
"v_mfma_f32_16x16x32_bf16 v[112:115], v[44:47], v[168:171], v[112:115]\n"
"buffer_load_dwordx4 v[180:183], v1, s[4:7], s98 offen offset:3072 sc1\n"
"v_mfma_f32_16x16x32_bf16 v[112:115], v[48:51], v[188:191], v[112:115]\n"
"s_add_u32 s98, s60, 0x800000\n"
"ds_read_b128 v[192:195], v6 offset:4096\n"
"ds_read_b128 v[220:223], v6 offset:5120\n"
"v_mfma_f32_16x16x32_bf16 v[112:115], v[52:55], v[88:91], v[112:115]\n"
"v_mul_f32_e32 v178, v178, v255\n"
"v_mul_f32_e32 v179, v179, v255\n"
"v_mfma_f32_16x16x32_bf16 v[100:103], v[84:87], v[88:91], v[100:103]\n"
"buffer_store_dwordx4 v[172:175], v0, s[8:11], s98 offen sc1\n"
"v_mfma_f32_16x16x32_bf16 v[100:103], v[80:83], v[188:191], v[100:103]\n"
"v_cvt_pk_bf16_f32 v88, v120, v121\n"
"v_cvt_pk_bf16_f32 v89, v122, v123\n"
"v_mfma_f32_16x16x32_bf16 v[100:103], v[76:79], v[168:171], v[100:103]\n"
"v_cvt_pk_bf16_f32 v90, v176, v177\n"
"v_cvt_pk_bf16_f32 v91, v178, v179\n"
"v_mfma_f32_16x16x32_bf16 v[100:103], v[72:75], v[144:147], v[100:103]\n"
"ds_write_b128 v8, v[88:91] offset:17408\n"
"v_mfma_f32_16x16x32_bf16 v[100:103], v[68:71], v[164:167], v[100:103]\n"
"v_add_f32_e32 v196, v196, v208\n"
"v_add_f32_e32 v197, v197, v209\n"
"v_mfma_f32_16x16x32_bf16 v[100:103], v[64:67], v[92:95], v[100:103]\n"
"v_add_f32_e32 v198, v198, v210\n"
"v_add_f32_e32 v199, v199, v211\n"
"v_mfma_f32_16x16x32_bf16 v[100:103], v[60:63], v[136:139], v[100:103]\n"
"v_add_f32_e32 v212, v212, v216\n"
"v_add_f32_e32 v213, v213, v217\n"
"v_mfma_f32_16x16x32_bf16 v[100:103], v[56:59], v[108:111], v[100:103]\n"
"v_add_f32_e32 v214, v214, v218\n"
"v_add_f32_e32 v215, v215, v219\n"
"v_mul_f32_e32 v196, v196, v255\n"
"v_mul_f32_e32 v197, v197, v255\n"
"v_mul_f32_e32 v198, v198, v255\n"
"v_mul_f32_e32 v199, v199, v255\n"
"v_mul_f32_e32 v212, v212, v255\n"
"v_mul_f32_e32 v213, v213, v255\n"
"v_mul_f32_e32 v214, v214, v255\n"
"v_mul_f32_e32 v215, v215, v255\n"
"v_cvt_pk_bf16_f32 v88, v196, v197\n"
"v_cvt_pk_bf16_f32 v89, v198, v199\n"
"v_cvt_pk_bf16_f32 v90, v212, v213\n"
"v_cvt_pk_bf16_f32 v91, v214, v215\n"
"ds_write_b128 v7, v[88:91] offset:50176\n"
".Lmk_b3:\n"
"s_waitcnt vmcnt(18)\n"
"; @B G1 3.0\n"
"v_mfma_f32_16x16x32_bf16 v[88:91], a[0:3], v[116:119], 0\n"
"v_mfma_f32_16x16x32_bf16 v[88:91], a[4:7], v[156:159], v[88:91]\n"
"s_add_u32 s98, s62, 0x40000\n"
"v_mfma_f32_16x16x32_bf16 v[88:91], a[8:11], v[184:187], v[88:91]\n"
"s_add_u32 s96, s54, 0x400000\n"
"buffer_load_dwordx4 v[92:95], v0, s[4:7], s96 offen sc1\n"
"v_mfma_f32_16x16x32_bf16 v[88:91], a[12:15], v[152:155], v[88:91]\n"
"s_waitcnt vmcnt(12)\n"
"v_mfma_f32_16x16x32_bf16 v[88:91], a[16:19], v[160:163], v[88:91]\n"
"buffer_load_dword v255, v10, s[4:7], s98 offen sc1\n"
"v_mfma_f32_16x16x32_bf16 v[88:91], a[20:23], v[224:227], v[88:91]\n"
"s_waitcnt lgkmcnt(2)\n"
"v_add_f32_e32 v112, v112, v192\n"
"v_add_f32_e32 v113, v113, v193\n"
"v_mfma_f32_16x16x32_bf16 v[88:91], a[24:27], v[204:207], v[88:91]\n"
"v_add_f32_e32 v114, v114, v194\n"
"v_add_f32_e32 v115, v115, v195\n"
"v_mfma_f32_16x16x32_bf16 v[88:91], a[28:31], v[132:135], v[88:91]\n"
"buffer_load_dwordx4 v[108:111], v0, s[4:7], s96 offen offset:1024 sc1\n"
"v_mfma_f32_16x16x32_bf16 v[120:123], a[60:63], v[132:135], 0\n"
"s_waitcnt lgkmcnt(0)\n"
"s_barrier\n"
"v_mfma_f32_16x16x32_bf16 v[120:123], a[56:59], v[204:207], v[120:123]\n"
"buffer_load_dwordx4 a[228:231], v0, s[4:7], s97 offen offset:1024 sc1\n"
"v_mfma_f32_16x16x32_bf16 v[120:123], a[52:55], v[224:227], v[120:123]\n"
"buffer_load_dword v253, v10, s[4:7], s98 offen offset:256 sc1\n"
"v_mfma_f32_16x16x32_bf16 v[120:123], a[48:51], v[160:163], v[120:123]\n"
"v_add_f32_e32 v100, v100, v220\n"
"v_add_f32_e32 v101, v101, v221\n"
"v_mfma_f32_16x16x32_bf16 v[120:123], a[44:47], v[152:155], v[120:123]\n"
"buffer_load_dwordx4 v[136:139], v0, s[4:7], s96 offen offset:2048 sc1\n"
"v_mfma_f32_16x16x32_bf16 v[120:123], a[40:43], v[184:187], v[120:123]\n"
"ds_write_b128 v3, v[88:91]\n"
"v_mfma_f32_16x16x32_bf16 v[120:123], a[36:39], v[156:159], v[120:123]\n"
"buffer_load_dword v252, v10, s[4:7], s98 offen offset:512 sc1\n"
"v_mfma_f32_16x16x32_bf16 v[120:123], a[32:35], v[116:119], v[120:123]\n"
"v_add_f32_e32 v102, v102, v222\n"
"v_add_f32_e32 v103, v103, v223\n"
"v_mfma_f32_16x16x32_bf16 v[88:91], a[64:67], v[116:119], 0\n"
"v_mul_f32_e32 v112, v112, v254\n"
"v_mul_f32_e32 v113, v113, v254\n"
"v_mfma_f32_16x16x32_bf16 v[88:91], a[68:71], v[156:159], v[88:91]\n"
"buffer_load_dwordx4 v[144:147], v0, s[4:7], s96 offen offset:3072 sc1\n"
"v_mfma_f32_16x16x32_bf16 v[88:91], a[72:75], v[184:187], v[88:91]\n"
"buffer_load_dword v251, v10, s[4:7], s98 offen offset:768 sc1\n"
"v_mfma_f32_16x16x32_bf16 v[88:91], a[76:79], v[152:155], v[88:91]\n"
"v_mul_f32_e32 v114, v114, v254\n"
"v_mul_f32_e32 v115, v115, v254\n"
"v_mfma_f32_16x16x32_bf16 v[88:91], a[80:83], v[160:163], v[88:91]\n"
"v_mul_f32_e32 v100, v100, v254\n"
"v_mul_f32_e32 v101, v101, v254\n"
"v_add_u32_e32 v250, 0x26c00, v0\n"
"v_mfma_f32_16x16x32_bf16 v[88:91], a[84:87], v[224:227], v[88:91]\n"
"ds_write_b128 v3, v[120:123] offset:1024\n"
"v_mfma_f32_16x16x32_bf16 v[88:91], a[88:91], v[204:207], v[88:91]\n"
"buffer_load_dwordx4 v[120:123], v1, s[4:7], s96 offen sc1\n"
"ds_read_b128 v[164:167], v250\n"
"v_mfma_f32_16x16x32_bf16 v[88:91], a[92:95], v[132:135], v[88:91]\n"
"buffer_load_dwordx4 a[232:235], v0, s[4:7], s97 offen offset:2048 sc1\n"
"ds_read_b128 v[168:171], v250 offset:1024\n"
"v_mfma_f32_16x16x32_bf16 v[172:175], a[124:127], v[132:135], 0\n"
"buffer_load_dword v250, v10, s[4:7], s98 offen offset:1024 sc1\n"
"v_xor_b32_e32 v249, 32, v17\n"
"v_mfma_f32_16x16x32_bf16 v[172:175], a[120:123], v[204:207], v[172:175]\n"
"ds_read_b128 v[176:179], v2 offset:49152\n"
"v_mul_f32_e32 v102, v102, v254\n"
"v_mul_f32_e32 v103, v103, v254\n"
"v_mfma_f32_16x16x32_bf16 v[172:175], a[116:119], v[224:227], v[172:175]\n"
"v_cvt_pk_bf16_f32 v188, v112, v113\n"
"v_cvt_pk_bf16_f32 v189, v114, v115\n"
"v_mfma_f32_16x16x32_bf16 v[172:175], a[112:115], v[160:163], v[172:175]\n"
"ds_read_b128 v[112:115], v17 offset:23552\n"
"buffer_load_dwordx4 v[192:195], v1, s[4:7], s96 offen offset:1024 sc1\n"
"ds_read_b128 v[196:199], v249 offset:23808\n"
"v_mfma_f32_16x16x32_bf16 v[172:175], a[108:111], v[152:155], v[172:175]\n"
"buffer_load_dword v254, v10, s[4:7], s98 offen offset:1280 sc1\n"
"v_mfma_f32_16x16x32_bf16 v[172:175], a[104:107], v[184:187], v[172:175]\n"
"ds_write_b128 v3, v[88:91] offset:2048\n"
"v_mfma_f32_16x16x32_bf16 v[172:175], a[100:103], v[156:159], v[172:175]\n"
"v_cvt_pk_bf16_f32 v190, v100, v101\n"
"v_cvt_pk_bf16_f32 v191, v102, v103\n"
"v_mfma_f32_16x16x32_bf16 v[172:175], a[96:99], v[116:119], v[172:175]\n"
"buffer_load_dword v249, v10, s[4:7], s98 offen offset:1536 sc1\n"
"v_mfma_f32_16x16x32_bf16 v[88:91], v[24:27], v[116:119], 0\n"
"buffer_load_dwordx4 v[100:103], v1, s[4:7], s96 offen offset:2048 sc1\n"
"s_waitcnt lgkmcnt(1)\n"
"v_mfma_f32_16x16x32_bf16 v[164:167], v[112:115], v[176:179], v[164:167]\n"
"v_mfma_f32_16x16x32_bf16 v[168:171], v[196:199], v[176:179], v[168:171]\n"
"v_mfma_f32_16x16x32_bf16 v[88:91], v[28:31], v[156:159], v[88:91]\n"
"ds_write_b128 v2, v[188:191] offset:17408\n"
"v_mfma_f32_16x16x32_bf16 v[88:91], v[32:35], v[184:187], v[88:91]\n"
"buffer_load_dword v248, v10, s[4:7], s98 offen offset:1792 sc1\n"
"v_xor_b32_e32 v247, 32, v16\n"
"v_mfma_f32_16x16x32_bf16 v[88:91], v[36:39], v[152:155], v[88:91]\n"
"ds_read_b64_tr_b16 v[112:113], v16 offset:23552\n"
"ds_read_b64_tr_b16 v[114:115], v247 offset:23808\n"
"ds_read_b64_tr_b16 v[176:177], v247 offset:23552\n"
"v_mfma_f32_16x16x32_bf16 v[88:91], v[40:43], v[160:163], v[88:91]\n"
"buffer_load_dwordx4 a[236:239], v0, s[4:7], s97 offen offset:3072 sc1\n"
"ds_read_b64_tr_b16 v[178:179], v16 offset:23808\n"
"v_mfma_f32_16x16x32_bf16 v[88:91], v[44:47], v[224:227], v[88:91]\n"
"buffer_load_dwordx4 v[188:191], v1, s[4:7], s96 offen offset:3072 sc1\n"
"v_mov_b32_e32 v196, 0x3f803f80\n"
"v_mfma_f32_16x16x32_bf16 v[88:91], v[48:51], v[204:207], v[88:91]\n"
"ds_write_b128 v3, v[172:175] offset:3072\n"
"v_mfma_f32_16x16x32_bf16 v[88:91], v[52:55], v[132:135], v[88:91]\n"
"v_mov_b32_e32 v197, 0x3f803f80\n"
"v_mov_b32_e32 v198, 0x3f803f80\n"
"v_mov_b32_e32 v199, 0x3f803f80\n"
"v_mfma_f32_16x16x32_bf16 v[172:175], v[84:87], v[132:135], 0\n"
"v_max_f32_e32 v247, v164, v165\n"
"v_max3_f32 v247, v247, v166, v167\n"
"v_max3_f32 v247, v247, v168, v169\n"
"v_mfma_f32_16x16x32_bf16 v[172:175], v[80:83], v[204:207], v[172:175]\n"
"v_max3_f32 v247, v247, v170, v171\n"
"v_mov_b32_e32 v246, v247\n"
"v_mfma_f32_16x16x32_bf16 v[172:175], v[76:79], v[224:227], v[172:175]\n"
"s_nop 0\n"
"v_permlane16_swap_b32_e32 v247, v246\n"
"v_max_f32_e32 v247, v247, v246\n"
"v_mov_b32_e32 v246, v247\n"
"v_mfma_f32_16x16x32_bf16 v[172:175], v[72:75], v[160:163], v[172:175]\n"
"s_nop 0\n"
"v_permlane32_swap_b32_e32 v247, v246\n"
"v_max_f32_e32 v247, v247, v246\n"
"v_sub_f32_e32 v164, v164, v247\n"
"v_mfma_f32_16x16x32_bf16 v[172:175], v[68:71], v[152:155], v[172:175]\n"
"v_sub_f32_e32 v165, v165, v247\n"
"v_sub_f32_e32 v166, v166, v247\n"
"v_sub_f32_e32 v167, v167, v247\n"
"v_mfma_f32_16x16x32_bf16 v[172:175], v[64:67], v[184:187], v[172:175]\n"
"ds_write_b128 v3, v[88:91] offset:4096\n"
"v_mfma_f32_16x16x32_bf16 v[172:175], v[60:63], v[156:159], v[172:175]\n"
"v_sub_f32_e32 v168, v168, v247\n"
"v_sub_f32_e32 v169, v169, v247\n"
"v_sub_f32_e32 v170, v170, v247\n"
"v_mfma_f32_16x16x32_bf16 v[172:175], v[56:59], v[116:119], v[172:175]\n"
"s_waitcnt vmcnt(25)\n"
"v_sub_f32_e32 v171, v171, v247\n"
"v_exp_f32_e32 v164, v164\n"
"; @B G1 3.1\n"
"v_mfma_f32_16x16x32_bf16 v[88:91], a[0:3], v[104:107], 0\n"
"v_exp_f32_e32 v165, v165\n"
"v_mfma_f32_16x16x32_bf16 v[88:91], a[4:7], v[140:143], v[88:91]\n"
"buffer_load_dwordx4 a[240:243], v1, s[4:7], s97 offen sc1\n"
"v_mfma_f32_16x16x32_bf16 v[88:91], a[8:11], v[148:151], v[88:91]\n"
"s_add_u32 s98, s55, 0x400000\n"
"buffer_load_dwordx4 v[116:119], v0, s[4:7], s98 offen sc1\n"
"v_mfma_f32_16x16x32_bf16 v[88:91], a[12:15], v[200:203], v[88:91]\n"
"s_waitcnt vmcnt(22)\n"
"v_exp_f32_e32 v166, v166\n"
"v_mfma_f32_16x16x32_bf16 v[88:91], a[16:19], v[96:99], v[88:91]\n"
"v_exp_f32_e32 v167, v167\n"
"v_mfma_f32_16x16x32_bf16 v[88:91], a[20:23], v[124:127], v[88:91]\n"
"ds_write_b128 v3, v[172:175] offset:5120\n"
"v_mfma_f32_16x16x32_bf16 v[88:91], a[24:27], v[128:131], v[88:91]\n"
"v_exp_f32_e32 v168, v168\n"
"v_mfma_f32_16x16x32_bf16 v[88:91], a[28:31], v[180:183], v[88:91]\n"
"buffer_load_dwordx4 v[132:135], v0, s[4:7], s98 offen offset:1024 sc1\n"
"v_mfma_f32_16x16x32_bf16 v[152:155], a[60:63], v[180:183], 0\n"
"v_exp_f32_e32 v169, v169\n"
"v_mfma_f32_16x16x32_bf16 v[152:155], a[56:59], v[128:131], v[152:155]\n"
"v_exp_f32_e32 v170, v170\n"
"v_mfma_f32_16x16x32_bf16 v[152:155], a[52:55], v[124:127], v[152:155]\n"
"v_exp_f32_e32 v171, v171\n"
"v_cvt_pk_bf16_f32 v156, v164, v165\n"
"v_mfma_f32_16x16x32_bf16 v[152:155], a[48:51], v[96:99], v[152:155]\n"
"v_cvt_pk_bf16_f32 v157, v166, v167\n"
"v_cvt_pk_bf16_f32 v158, v168, v169\n"
"v_cvt_pk_bf16_f32 v159, v170, v171\n"
"v_mfma_f32_16x16x32_bf16 v[152:155], a[44:47], v[200:203], v[152:155]\n"
"buffer_load_dwordx4 v[160:163], v0, s[4:7], s98 offen offset:2048 sc1\n"
"s_waitcnt lgkmcnt(3)\n"
"v_mfma_f32_16x16x32_bf16 v[164:167], v[196:199], v[156:159], 0\n"
"v_mfma_f32_16x16x32_bf16 v[168:171], v[112:115], v[156:159], 0\n"
"v_mfma_f32_16x16x32_bf16 v[112:115], v[176:179], v[156:159], 0\n"
"v_mfma_f32_16x16x32_bf16 v[152:155], a[40:43], v[148:151], v[152:155]\n"
"ds_write_b128 v3, v[88:91] offset:6144\n"
"v_mfma_f32_16x16x32_bf16 v[152:155], a[36:39], v[140:143], v[152:155]\n"
"s_add_u32 s96, s73, 0\n"
"buffer_load_dwordx2 v[246:247], off, s[4:7], s96 sc1\n"
"v_mfma_f32_16x16x32_bf16 v[152:155], a[32:35], v[104:107], v[152:155]\n"
"buffer_load_dwordx4 a[244:247], v1, s[4:7], s97 offen offset:1024 sc1\n"
"v_mfma_f32_16x16x32_bf16 v[88:91], a[64:67], v[104:107], 0\n"
"v_mfma_f32_16x16x32_bf16 v[88:91], a[68:71], v[140:143], v[88:91]\n"
"buffer_load_dwordx4 v[156:159], v0, s[4:7], s98 offen offset:3072 sc1\n"
"v_mfma_f32_16x16x32_bf16 v[88:91], a[72:75], v[148:151], v[88:91]\n"
"v_rcp_f32_e32 v164, v164\n"
"v_mfma_f32_16x16x32_bf16 v[88:91], a[76:79], v[200:203], v[88:91]\n"
"v_mul_f32_e32 v168, v168, v164\n"
"v_mul_f32_e32 v169, v169, v164\n"
"v_mul_f32_e32 v170, v170, v164\n"
"v_mfma_f32_16x16x32_bf16 v[88:91], a[80:83], v[96:99], v[88:91]\n"
"v_mov_b32_e32 v172, -1\n"
"v_mov_b32_e32 v173, -1\n"
"v_mul_f32_e32 v171, v171, v164\n"
"v_mfma_f32_16x16x32_bf16 v[88:91], a[84:87], v[124:127], v[88:91]\n"
"ds_write_b128 v3, v[152:155] offset:7168\n"
"v_mfma_f32_16x16x32_bf16 v[88:91], a[88:91], v[128:131], v[88:91]\n"
"buffer_load_dwordx4 v[152:155], v1, s[4:7], s98 offen sc1\n"
"v_mul_f32_e32 v112, v112, v164\n"
"v_mfma_f32_16x16x32_bf16 v[88:91], a[92:95], v[180:183], v[88:91]\n"
"v_mov_b32_e32 v174, -1\n"
"v_mov_b32_e32 v175, -1\n"
"v_mul_f32_e32 v113, v113, v164\n"
"v_mfma_f32_16x16x32_bf16 v[176:179], a[124:127], v[180:183], 0\n"
"s_add_u32 s95, s60, 0x808000\n"
"buffer_store_dwordx4 v[172:175], v0, s[8:11], s95 offen sc1\n"
"v_mfma_f32_16x16x32_bf16 v[176:179], a[120:123], v[128:131], v[176:179]\n"
"v_mul_f32_e32 v114, v114, v164\n"
"v_mul_f32_e32 v115, v115, v164\n"
"v_cvt_pk_bf16_f32 v164, v168, v169\n"
"v_mfma_f32_16x16x32_bf16 v[176:179], a[116:119], v[124:127], v[176:179]\n"
"s_add_u32 s95, s60, 0x440000\n"
"v_cvt_pk_bf16_f32 v165, v170, v171\n"
"v_cvt_pk_bf16_f32 v166, v112, v113\n"
"v_cvt_pk_bf16_f32 v167, v114, v115\n"
"v_mfma_f32_16x16x32_bf16 v[176:179], a[112:115], v[96:99], v[176:179]\n"
"buffer_load_dwordx4 v[112:115], v1, s[4:7], s98 offen offset:1024 sc1\n"
"v_mfma_f32_16x16x32_bf16 v[176:179], a[108:111], v[200:203], v[176:179]\n"
"buffer_load_dwordx4 a[248:251], v1, s[4:7], s97 offen offset:2048 sc1\n"
"v_mfma_f32_16x16x32_bf16 v[176:179], a[104:107], v[148:151], v[176:179]\n"
"ds_write_b128 v3, v[88:91] offset:8192\n"
"v_mfma_f32_16x16x32_bf16 v[176:179], a[100:103], v[140:143], v[176:179]\n"
"buffer_store_dwordx4 v[164:167], v0, s[4:7], s95 offen sc1\n"
"v_mfma_f32_16x16x32_bf16 v[176:179], a[96:99], v[104:107], v[176:179]\n"
";\n"
"v_mfma_f32_16x16x32_bf16 v[88:91], v[24:27], v[104:107], 0\n"
"buffer_load_dwordx4 v[164:167], v1, s[4:7], s98 offen offset:2048 sc1\n"
"v_mfma_f32_16x16x32_bf16 v[88:91], v[28:31], v[140:143], v[88:91]\n"
"v_mfma_f32_16x16x32_bf16 v[88:91], v[32:35], v[148:151], v[88:91]\n"
"v_mfma_f32_16x16x32_bf16 v[88:91], v[36:39], v[200:203], v[88:91]\n"
"v_mfma_f32_16x16x32_bf16 v[88:91], v[40:43], v[96:99], v[88:91]\n"
"s_waitcnt vmcnt(15)\n"
"v_add_f32_e32 v255, v255, v250\n"
"v_add_f32_e32 v253, v253, v254\n"
"v_mfma_f32_16x16x32_bf16 v[88:91], v[44:47], v[124:127], v[88:91]\n"
"buffer_load_dwordx4 v[168:171], v1, s[4:7], s98 offen offset:3072 sc1\n"
"v_mfma_f32_16x16x32_bf16 v[88:91], v[48:51], v[128:131], v[88:91]\n"
"ds_write_b128 v3, v[176:179] offset:9216\n"
"v_mfma_f32_16x16x32_bf16 v[88:91], v[52:55], v[180:183], v[88:91]\n"
"v_add_f32_e32 v252, v252, v249\n"
"v_add_f32_e32 v251, v251, v248\n"
"v_mfma_f32_16x16x32_bf16 v[176:179], v[84:87], v[180:183], 0\n"
"v_add_f32_e32 v255, v255, v252\n"
"v_add_f32_e32 v253, v253, v251\n"
"v_mfma_f32_16x16x32_bf16 v[176:179], v[80:83], v[128:131], v[176:179]\n"
"buffer_load_dwordx4 a[252:255], v1, s[4:7], s97 offen offset:3072 sc1\n"
"v_mfma_f32_16x16x32_bf16 v[176:179], v[76:79], v[124:127], v[176:179]\n"
"v_add_f32_e32 v255, v255, v253\n"
"v_mov_b32_e32 v253, v255\n"
"v_mfma_f32_16x16x32_bf16 v[176:179], v[72:75], v[96:99], v[176:179]\n"
"s_nop 0\n"
"v_permlane32_swap_b32_e32 v255, v253\n"
"v_add_f32_e32 v255, v255, v253\n"
"v_mfma_f32_16x16x32_bf16 v[176:179], v[68:71], v[200:203], v[176:179]\n"
"v_mul_f32_e32 v255, 0x3a800000, v255\n"
"v_mov_b32_e32 v253, v255\n"
"v_mfma_f32_16x16x32_bf16 v[176:179], v[64:67], v[148:151], v[176:179]\n"
"ds_write_b128 v3, v[88:91] offset:10240\n"
"v_mfma_f32_16x16x32_bf16 v[176:179], v[60:63], v[140:143], v[176:179]\n"
"v_permlane16_swap_b32_e32 v255, v253\n"
"v_cvt_pk_bf16_f32 v254, -v255, 0\n"
"v_mfma_f32_16x16x32_bf16 v[176:179], v[56:59], v[104:107], v[176:179]\n"
"v_fma_f32 v253, -v255, v255, v253\n"
"v_lshlrev_b32_e32 v254, 16, v254\n"
"; @B G1 3.2\n"
"v_mfma_f32_16x16x32_bf16 v[88:91], a[0:3], v[92:95], 0\n"
"s_add_u32 s98, s67, 0x10020\n"
"buffer_load_dwordx2 v[250:251], v12, s[4:7], s98 offen sc1\n"
"v_mfma_f32_16x16x32_bf16 v[88:91], a[4:7], v[108:111], v[88:91]\n"
"v_max_f32_e32 v253, 0, v253\n"
"v_cvt_pk_bf16_f32 v253, v253, 0\n"
"v_mfma_f32_16x16x32_bf16 v[88:91], a[8:11], v[136:139], v[88:91]\n"
"v_lshlrev_b32_e32 v253, 16, v253\n"
"v_add_f32_e32 v253, 0x3727c5ac, v253\n"
"v_mfma_f32_16x16x32_bf16 v[88:91], a[12:15], v[144:147], v[88:91]\n"
"s_waitcnt vmcnt(16)\n"
"v_cvt_pk_bf16_f32 v253, v253, 0\n"
"v_lshlrev_b32_e32 v253, 16, v253\n"
"v_mfma_f32_16x16x32_bf16 v[88:91], a[16:19], v[120:123], v[88:91]\n"
"v_sqrt_f32_e32 v253, v253\n"
"v_mfma_f32_16x16x32_bf16 v[88:91], a[20:23], v[192:195], v[88:91]\n"
"ds_write_b128 v3, v[176:179] offset:11264\n"
"v_mfma_f32_16x16x32_bf16 v[88:91], a[24:27], v[100:103], v[88:91]\n"
"v_cvt_pk_bf16_f32 v253, v253, 0\n"
"v_lshlrev_b32_e32 v253, 16, v253\n"
"v_mfma_f32_16x16x32_bf16 v[88:91], a[28:31], v[188:191], v[88:91]\n"
"v_rcp_f32_e32 v255, v253\n"
"v_mfma_f32_16x16x32_bf16 v[96:99], a[60:63], v[188:191], 0\n"
"v_mul_f32_e32 v253, 0x3e8293ee, v255\n"
"v_add_u32_e32 v252, 0x26c00, v0\n"
"v_mfma_f32_16x16x32_bf16 v[96:99], a[56:59], v[100:103], v[96:99]\n"
"ds_read_b128 v[104:107], v252\n"
"ds_read_b128 v[124:127], v252 offset:1024\n"
"v_mfma_f32_16x16x32_bf16 v[96:99], a[52:55], v[192:195], v[96:99]\n"
"s_waitcnt lgkmcnt(0)\n"
"s_barrier\n"
"v_mfma_f32_16x16x32_bf16 v[96:99], a[48:51], v[120:123], v[96:99]\n"
"s_add_u32 s97, s61, 0xc40000\n"
"ds_read_b128 v[128:131], v4\n"
"ds_read_b128 v[140:143], v9 offset:640\n"
"v_mfma_f32_16x16x32_bf16 v[96:99], a[44:47], v[144:147], v[96:99]\n"
"ds_read_b128 v[148:151], v5\n"
"v_add_u32_e32 v252, 0x26c00, v0\n"
"v_mfma_f32_16x16x32_bf16 v[96:99], a[40:43], v[136:139], v[96:99]\n"
"ds_write_b128 v3, v[88:91] offset:12288\n"
"v_mfma_f32_16x16x32_bf16 v[96:99], a[36:39], v[108:111], v[96:99]\n"
"buffer_store_dwordx4 v[172:175], v0, s[8:11], s97 offen sc1\n"
"v_mfma_f32_16x16x32_bf16 v[96:99], a[32:35], v[92:95], v[96:99]\n"
"ds_read_b128 v[88:91], v252\n"
"ds_read_b128 v[176:179], v252 offset:1024\n"
"v_mfma_f32_16x16x32_bf16 v[180:183], a[64:67], v[92:95], 0\n"
"v_mov_b32_e32 v252, 0xff800000\n"
"v_mfma_f32_16x16x32_bf16 v[180:183], a[68:71], v[108:111], v[180:183]\n"
"s_waitcnt lgkmcnt(3)\n"
"v_fmac_f32_e32 v128, v254, v140\n"
"v_fmac_f32_e32 v129, v254, v141\n"
"v_mfma_f32_16x16x32_bf16 v[180:183], a[72:75], v[136:139], v[180:183]\n"
"ds_read_b128 v[184:187], v4 offset:1024\n"
"ds_read_b128 v[196:199], v9 offset:704\n"
"v_mfma_f32_16x16x32_bf16 v[180:183], a[76:79], v[144:147], v[180:183]\n"
"ds_read_b128 v[200:203], v5 offset:1024\n"
"v_fmac_f32_e32 v130, v254, v142\n"
"v_mfma_f32_16x16x32_bf16 v[180:183], a[80:83], v[120:123], v[180:183]\n"
"v_fmac_f32_e32 v131, v254, v143\n"
"v_add_f32_e32 v128, v128, v148\n"
"v_mfma_f32_16x16x32_bf16 v[180:183], a[84:87], v[192:195], v[180:183]\n"
"ds_write_b128 v3, v[96:99] offset:13312\n"
"v_mfma_f32_16x16x32_bf16 v[180:183], a[88:91], v[100:103], v[180:183]\n"
"s_waitcnt lgkmcnt(4)\n"
"v_add_f32_e32 v129, v129, v149\n"
"v_add_f32_e32 v130, v130, v150\n"
"v_mfma_f32_16x16x32_bf16 v[180:183], a[92:95], v[188:191], v[180:183]\n"
"v_add_f32_e32 v131, v131, v151\n"
"v_cndmask_b32_e64 v88, v88, v252, s[82:83]\n"
"v_mfma_f32_16x16x32_bf16 v[96:99], a[124:127], v[188:191], 0\n"
"s_waitcnt vmcnt(12)\n"
"v_cmp_ne_u64_e32 vcc, v[246:247], v[18:19]\n"
"v_mfma_f32_16x16x32_bf16 v[96:99], a[120:123], v[100:103], v[96:99]\n"
"s_cbranch_vccnz .Lmk_f40s\n"
".Lmk_f40r:\n"
"s_mov_b32 m0, 0x7c00\n"
"v_mfma_f32_16x16x32_bf16 v[96:99], a[116:119], v[192:195], v[96:99]\n"
"s_add_u32 s97, s66, 0x3c00\n"
"buffer_load_dwordx4 v0, s[4:7], s97 offen sc1 lds\n"
"v_mfma_f32_16x16x32_bf16 v[96:99], a[112:115], v[120:123], v[96:99]\n"
"s_mov_b32 m0, 0xfc00\n"
"s_add_u32 s97, s66, 0x7c00\n"
"ds_read_b128 v[140:143], v4 offset:2048\n"
"v_mfma_f32_16x16x32_bf16 v[96:99], a[108:111], v[144:147], v[96:99]\n"
"buffer_load_dwordx4 v0, s[4:7], s97 offen sc1 lds\n"
"v_mfma_f32_16x16x32_bf16 v[96:99], a[104:107], v[136:139], v[96:99]\n"
"ds_write_b128 v3, v[180:183] offset:14336\n"
"v_mfma_f32_16x16x32_bf16 v[96:99], a[100:103], v[108:111], v[96:99]\n"
"ds_read_b128 v[148:151], v9 offset:768\n"
"ds_read_b128 v[180:183], v5 offset:2048\n"
"v_mfma_f32_16x16x32_bf16 v[96:99], a[96:99], v[92:95], v[96:99]\n"
"s_waitcnt lgkmcnt(5)\n"
"v_fmac_f32_e32 v184, v254, v196\n"
"v_fmac_f32_e32 v185, v254, v197\n"
"v_mfma_f32_16x16x32_bf16 v[204:207], v[24:27], v[92:95], 0\n"
"ds_read_b128 v[208:211], v4 offset:3072\n"
"ds_read_b128 v[212:215], v9 offset:832\n"
"v_mfma_f32_16x16x32_bf16 v[204:207], v[28:31], v[108:111], v[204:207]\n"
"ds_read_b128 v[216:219], v5 offset:3072\n"
"v_fmac_f32_e32 v186, v254, v198\n"
"v_mfma_f32_16x16x32_bf16 v[204:207], v[32:35], v[136:139], v[204:207]\n"
"v_fmac_f32_e32 v187, v254, v199\n"
"v_add_f32_e32 v184, v184, v200\n"
"v_mfma_f32_16x16x32_bf16 v[204:207], v[36:39], v[144:147], v[204:207]\n"
"v_add_f32_e32 v185, v185, v201\n"
"v_add_f32_e32 v186, v186, v202\n"
"v_mfma_f32_16x16x32_bf16 v[204:207], v[40:43], v[120:123], v[204:207]\n"
"s_waitcnt lgkmcnt(3)\n"
"v_add_f32_e32 v187, v187, v203\n"
"v_fmac_f32_e32 v140, v254, v148\n"
"v_mfma_f32_16x16x32_bf16 v[204:207], v[44:47], v[192:195], v[204:207]\n"
"ds_write_b128 v3, v[96:99] offset:15360\n"
"v_mfma_f32_16x16x32_bf16 v[204:207], v[48:51], v[100:103], v[204:207]\n"
"v_fmac_f32_e32 v141, v254, v149\n"
"v_fmac_f32_e32 v142, v254, v150\n"
"v_mfma_f32_16x16x32_bf16 v[204:207], v[52:55], v[188:191], v[204:207]\n"
"ds_read_b128 v[96:99], v4 offset:4096\n"
"ds_read_b128 v[196:199], v9 offset:512\n"
"v_mfma_f32_16x16x32_bf16 v[200:203], v[84:87], v[188:191], 0\n"
"ds_read_b128 v[188:191], v5 offset:4096\n"
"v_fmac_f32_e32 v143, v254, v151\n"
"v_mfma_f32_16x16x32_bf16 v[200:203], v[80:83], v[100:103], v[200:203]\n"
"v_add_f32_e32 v140, v140, v180\n"
"v_add_f32_e32 v141, v141, v181\n"
"v_mfma_f32_16x16x32_bf16 v[200:203], v[76:79], v[192:195], v[200:203]\n"
"s_waitcnt lgkmcnt(4)\n"
"v_add_f32_e32 v142, v142, v182\n"
"v_add_f32_e32 v143, v143, v183\n"
"v_mfma_f32_16x16x32_bf16 v[200:203], v[72:75], v[120:123], v[200:203]\n"
"v_fmac_f32_e32 v208, v254, v212\n"
"v_fmac_f32_e32 v209, v254, v213\n"
"v_mfma_f32_16x16x32_bf16 v[200:203], v[68:71], v[144:147], v[200:203]\n"
"v_fmac_f32_e32 v210, v254, v214\n"
"v_fmac_f32_e32 v211, v254, v215\n"
"v_mfma_f32_16x16x32_bf16 v[200:203], v[64:67], v[136:139], v[200:203]\n"
"ds_write_b128 v3, v[204:207] offset:16384\n"
"v_mfma_f32_16x16x32_bf16 v[200:203], v[60:63], v[108:111], v[200:203]\n"
"ds_read_b128 v[100:103], v4 offset:5120\n"
"ds_read_b128 v[108:111], v9 offset:576\n"
"v_mfma_f32_16x16x32_bf16 v[200:203], v[56:59], v[92:95], v[200:203]\n"
"s_waitcnt vmcnt(12)\n"
"s_waitcnt vmcnt(3)\n"
"v_cmp_ne_u64_e32 vcc, v[250:251], v[18:19]\n"
"; @B G1 3.3\n"
"v_mfma_f32_16x16x32_bf16 v[128:131], a[0:3], v[116:119], v[128:131]\n"
"s_cbranch_vccnz .Lmk_f41s\n"
".Lmk_f41r:\n"
"ds_read_b128 v[92:95], v5 offset:5120\n"
"v_mfma_f32_16x16x32_bf16 v[128:131], a[4:7], v[132:135], v[128:131]\n"
"s_add_u32 s98, s61, 0xc40000\n"
"s_add_u32 s97, s56, 0x440000\n"
"v_add_f32_e32 v208, v208, v216\n"
"v_mfma_f32_16x16x32_bf16 v[128:131], a[8:11], v[160:163], v[128:131]\n"
"buffer_load_dwordx4 v[120:123], v0, s[4:7], s97 offen sc1\n"
"v_mfma_f32_16x16x32_bf16 v[128:131], a[12:15], v[156:159], v[128:131]\n"
"buffer_store_dwordx4 v[172:175], v0, s[8:11], s98 offen offset:1024 sc1\n"
"v_mfma_f32_16x16x32_bf16 v[128:131], a[16:19], v[152:155], v[128:131]\n"
"s_add_u32 s98, s69, 0\n"
"v_add_f32_e32 v209, v209, v217\n"
"v_add_f32_e32 v210, v210, v218\n"
"v_mfma_f32_16x16x32_bf16 v[128:131], a[20:23], v[112:115], v[128:131]\n"
"ds_write_b128 v3, v[200:203] offset:17408\n"
"v_mfma_f32_16x16x32_bf16 v[128:131], a[24:27], v[164:167], v[128:131]\n"
"buffer_load_dwordx4 v[136:139], v0, s[4:7], s97 offen offset:1024 sc1\n"
"v_mfma_f32_16x16x32_bf16 v[128:131], a[28:31], v[168:171], v[128:131]\n"
"s_waitcnt lgkmcnt(5)\n"
"v_add_f32_e32 v211, v211, v219\n"
"v_fmac_f32_e32 v96, v254, v196\n"
"v_mfma_f32_16x16x32_bf16 v[184:187], a[60:63], v[168:171], v[184:187]\n"
"v_fmac_f32_e32 v97, v254, v197\n"
"v_fmac_f32_e32 v98, v254, v198\n"
"v_mfma_f32_16x16x32_bf16 v[184:187], a[56:59], v[164:167], v[184:187]\n"
"buffer_load_dwordx4 v[144:147], v0, s[4:7], s97 offen offset:2048 sc1\n"
"v_mfma_f32_16x16x32_bf16 v[184:187], a[52:55], v[112:115], v[184:187]\n"
"s_waitcnt lgkmcnt(0)\n"
"s_barrier\n"
"v_mfma_f32_16x16x32_bf16 v[184:187], a[48:51], v[152:155], v[184:187]\n"
"ds_read_b128 v[148:151], v6\n"
"ds_read_b128 v[180:183], v6 offset:1024\n"
"v_mfma_f32_16x16x32_bf16 v[184:187], a[44:47], v[156:159], v[184:187]\n"
"buffer_load_dwordx4 v[192:195], v0, s[4:7], s97 offen offset:3072 sc1\n"
"v_mfma_f32_16x16x32_bf16 v[184:187], a[40:43], v[160:163], v[184:187]\n"
"v_fmac_f32_e32 v99, v254, v199\n"
"v_add_f32_e32 v96, v96, v188\n"
"v_mfma_f32_16x16x32_bf16 v[184:187], a[36:39], v[132:135], v[184:187]\n"
"v_add_f32_e32 v97, v97, v189\n"
"v_add_f32_e32 v98, v98, v190\n"
"v_mfma_f32_16x16x32_bf16 v[184:187], a[32:35], v[116:119], v[184:187]\n"
"buffer_load_dwordx4 v[196:199], v1, s[4:7], s97 offen sc1\n"
"v_mfma_f32_16x16x32_bf16 v[140:143], a[64:67], v[116:119], v[140:143]\n"
"v_add_f32_e32 v99, v99, v191\n"
"v_fmac_f32_e32 v100, v254, v108\n"
"v_mfma_f32_16x16x32_bf16 v[140:143], a[68:71], v[132:135], v[140:143]\n"
"v_fmac_f32_e32 v101, v254, v109\n"
"v_fmac_f32_e32 v102, v254, v110\n"
"v_mfma_f32_16x16x32_bf16 v[140:143], a[72:75], v[160:163], v[140:143]\n"
"buffer_load_dwordx4 v[188:191], v1, s[4:7], s97 offen offset:1024 sc1\n"
"v_mfma_f32_16x16x32_bf16 v[140:143], a[76:79], v[156:159], v[140:143]\n"
"v_fmac_f32_e32 v103, v254, v111\n"
"v_add_f32_e32 v100, v100, v92\n"
"v_mfma_f32_16x16x32_bf16 v[140:143], a[80:83], v[152:155], v[140:143]\n"
"s_waitcnt lgkmcnt(0)\n"
"v_add_f32_e32 v101, v101, v93\n"
"v_add_f32_e32 v102, v102, v94\n"
"v_mfma_f32_16x16x32_bf16 v[140:143], a[84:87], v[112:115], v[140:143]\n"
"buffer_load_dwordx4 v[108:111], v1, s[4:7], s97 offen offset:2048 sc1\n"
"v_mfma_f32_16x16x32_bf16 v[140:143], a[88:91], v[164:167], v[140:143]\n"
"ds_read_b128 v[200:203], v6 offset:2048\n"
"ds_read_b128 v[204:207], v6 offset:3072\n"
"v_mfma_f32_16x16x32_bf16 v[140:143], a[92:95], v[168:171], v[140:143]\n"
"v_add_f32_e32 v103, v103, v95\n"
"v_add_f32_e32 v128, v128, v148\n"
"v_mfma_f32_16x16x32_bf16 v[208:211], a[124:127], v[168:171], v[208:211]\n"
"buffer_load_dwordx4 v[92:95], v1, s[4:7], s97 offen offset:3072 sc1\n"
"v_mfma_f32_16x16x32_bf16 v[208:211], a[120:123], v[164:167], v[208:211]\n"
"s_add_u32 s97, s57, 0x440000\n"
"v_add_f32_e32 v129, v129, v149\n"
"v_add_f32_e32 v130, v130, v150\n"
"v_mfma_f32_16x16x32_bf16 v[208:211], a[116:119], v[112:115], v[208:211]\n"
"v_add_f32_e32 v131, v131, v151\n"
"v_add_f32_e32 v184, v184, v180\n"
"v_mfma_f32_16x16x32_bf16 v[208:211], a[112:115], v[152:155], v[208:211]\n"
"buffer_load_dwordx4 v[148:151], v0, s[4:7], s97 offen sc1\n"
"v_mfma_f32_16x16x32_bf16 v[208:211], a[108:111], v[156:159], v[208:211]\n"
"v_add_f32_e32 v185, v185, v181\n"
"v_add_f32_e32 v186, v186, v182\n"
"v_mfma_f32_16x16x32_bf16 v[208:211], a[104:107], v[160:163], v[208:211]\n"
"v_add_f32_e32 v187, v187, v183\n"
"v_mul_f32_e32 v128, v128, v255\n"
"v_mfma_f32_16x16x32_bf16 v[208:211], a[100:103], v[132:135], v[208:211]\n"
"buffer_load_dwordx4 v[180:183], v0, s[4:7], s97 offen offset:1024 sc1\n"
"v_mfma_f32_16x16x32_bf16 v[208:211], a[96:99], v[116:119], v[208:211]\n"
"v_mul_f32_e32 v129, v129, v255\n"
"v_mul_f32_e32 v130, v130, v255\n"
"v_mfma_f32_16x16x32_bf16 v[96:99], v[24:27], v[116:119], v[96:99]\n"
"v_mul_f32_e32 v131, v131, v255\n"
"v_mul_f32_e32 v184, v184, v255\n"
"v_mfma_f32_16x16x32_bf16 v[96:99], v[28:31], v[132:135], v[96:99]\n"
"buffer_load_dwordx4 v[24:27], v0, s[4:7], s97 offen offset:2048 sc1\n"
"v_mfma_f32_16x16x32_bf16 v[96:99], v[32:35], v[160:163], v[96:99]\n"
"v_mul_f32_e32 v185, v185, v255\n"
"v_mul_f32_e32 v186, v186, v255\n"
"v_mfma_f32_16x16x32_bf16 v[96:99], v[36:39], v[156:159], v[96:99]\n"
"v_mul_f32_e32 v187, v187, v255\n"
"v_cvt_pk_bf16_f32 v28, v128, v129\n"
"v_mfma_f32_16x16x32_bf16 v[96:99], v[40:43], v[152:155], v[96:99]\n"
"buffer_load_dwordx4 v[32:35], v0, s[4:7], s97 offen offset:3072 sc1\n"
"v_mfma_f32_16x16x32_bf16 v[96:99], v[44:47], v[112:115], v[96:99]\n"
"s_waitcnt lgkmcnt(0)\n"
"v_cvt_pk_bf16_f32 v29, v130, v131\n"
"v_cvt_pk_bf16_f32 v30, v184, v185\n"
"v_mfma_f32_16x16x32_bf16 v[96:99], v[48:51], v[164:167], v[96:99]\n"
"s_add_u32 s96, s63, 0xc4000\n"
"ds_read_b128 v[36:39], v6 offset:4096\n"
"ds_read_b128 v[40:43], v6 offset:5120\n"
"v_mfma_f32_16x16x32_bf16 v[96:99], v[52:55], v[168:171], v[96:99]\n"
"buffer_load_dwordx4 v[44:47], v1, s[4:7], s97 offen sc1\n"
"v_mfma_f32_16x16x32_bf16 v[100:103], v[84:87], v[168:171], v[100:103]\n"
"buffer_store_dword v172, v14, s[8:11], s96 offen sc1\n"
"v_mfma_f32_16x16x32_bf16 v[100:103], v[80:83], v[164:167], v[100:103]\n"
"v_cvt_pk_bf16_f32 v31, v186, v187\n"
"v_add_f32_e32 v140, v140, v200\n"
"v_mfma_f32_16x16x32_bf16 v[100:103], v[76:79], v[112:115], v[100:103]\n"
"buffer_load_dwordx4 v[48:51], v1, s[4:7], s97 offen offset:1024 sc1\n"
"ds_write_b128 v7, v[28:31] offset:16384\n"
"v_mfma_f32_16x16x32_bf16 v[100:103], v[72:75], v[152:155], v[100:103]\n"
"v_add_f32_e32 v141, v141, v201\n"
"v_add_f32_e32 v142, v142, v202\n"
"v_mfma_f32_16x16x32_bf16 v[100:103], v[68:71], v[156:159], v[100:103]\n"
"v_add_f32_e32 v143, v143, v203\n"
"v_add_f32_e32 v208, v208, v204\n"
"v_mfma_f32_16x16x32_bf16 v[100:103], v[64:67], v[160:163], v[100:103]\n"
"buffer_load_dwordx4 v[28:31], v1, s[4:7], s97 offen offset:2048 sc1\n"
"v_mfma_f32_16x16x32_bf16 v[100:103], v[60:63], v[132:135], v[100:103]\n"
"v_add_f32_e32 v209, v209, v205\n"
"v_add_f32_e32 v210, v210, v206\n"
"v_mfma_f32_16x16x32_bf16 v[100:103], v[56:59], v[116:119], v[100:103]\n"
"buffer_load_dwordx4 a[0:3], v0, s[8:11], s98 offen sc1\n"
"buffer_load_dwordx4 v[56:59], v1, s[4:7], s97 offen offset:3072 sc1\n"
"v_add_f32_e32 v211, v211, v207\n"
"v_mul_f32_e32 v140, v140, v255\n"
"v_mul_f32_e32 v141, v141, v255\n"
"v_mul_f32_e32 v142, v142, v255\n"
"v_mul_f32_e32 v143, v143, v255\n"
"v_mul_f32_e32 v208, v208, v255\n"
"v_mul_f32_e32 v209, v209, v255\n"
"v_mul_f32_e32 v210, v210, v255\n"
"v_mul_f32_e32 v211, v211, v255\n"
"v_cvt_pk_bf16_f32 v52, v140, v141\n"
"v_cvt_pk_bf16_f32 v53, v142, v143\n"
"v_cvt_pk_bf16_f32 v54, v208, v209\n"
"v_cvt_pk_bf16_f32 v55, v210, v211\n"
"v_cndmask_b32_e64 v89, v89, v252, s[82:83]\n"
"v_cndmask_b32_e64 v90, v90, v252, s[82:83]\n"
"v_cndmask_b32_e64 v91, v91, v252, s[82:83]\n"
"v_cndmask_b32_e64 v176, v176, v252, s[82:83]\n"
"v_cndmask_b32_e64 v177, v177, v252, s[82:83]\n"
"v_cndmask_b32_e64 v178, v178, v252, s[82:83]\n"
"v_cndmask_b32_e64 v179, v179, v252, s[82:83]\n"
"ds_write_b128 v7, v[52:55] offset:49152\n"
"buffer_load_dwordx4 a[4:7], v0, s[8:11], s98 offen offset:1024 sc1\n"
"buffer_load_dwordx4 a[8:11], v0, s[8:11], s98 offen offset:2048 sc1\n"
"buffer_load_dwordx4 a[12:15], v0, s[8:11], s98 offen offset:3072 sc1\n"
"buffer_load_dwordx4 a[16:19], v1, s[8:11], s98 offen sc1\n"
"buffer_load_dwordx4 a[20:23], v1, s[8:11], s98 offen offset:1024 sc1\n"
".Lmk_b4:\n"
"; @B tail\n"
";\n"
"s_waitcnt lgkmcnt(2)\n"
"v_add_f32_e32 v96, v96, v36\n"
"v_add_f32_e32 v97, v97, v37\n"
"v_add_f32_e32 v98, v98, v38\n"
"v_add_f32_e32 v99, v99, v39\n"
"v_add_f32_e32 v100, v100, v40\n"
"v_add_f32_e32 v101, v101, v41\n"
"v_add_f32_e32 v102, v102, v42\n"
"v_add_f32_e32 v103, v103, v43\n"
"v_mul_f32_e32 v96, v96, v253\n"
"v_mul_f32_e32 v97, v97, v253\n"
"v_mul_f32_e32 v98, v98, v253\n"
"v_mul_f32_e32 v99, v99, v253\n"
"v_mul_f32_e32 v100, v100, v253\n"
"v_mul_f32_e32 v101, v101, v253\n"
"v_mul_f32_e32 v102, v102, v253\n"
"v_mul_f32_e32 v103, v103, v253\n"
"v_cvt_pk_bf16_f32 v36, v96, v97\n"
"v_cvt_pk_bf16_f32 v37, v98, v99\n"
"v_cvt_pk_bf16_f32 v38, v100, v101\n"
"v_cvt_pk_bf16_f32 v39, v102, v103\n"
"ds_write_b128 v2, v[36:39] offset:16384\n"
"s_waitcnt vmcnt(24)\n"
";\n"
"; @B AT a\n"
";\n"
"s_cmp_lg_u32 s0, 0\n"
"s_cbranch_scc1 .Lmk_exit\n"
"ds_read_b128 v[36:39], v2 offset:17408\n"
"ds_read_b128 v[40:43], v15 offset:16384\n"
"v_xor_b32_e32 v255, 32, v15\n"
"ds_read_b128 v[52:55], v255 offset:16640\n"
"ds_read_b128 v[60:63], v2 offset:16384\n"
"ds_read_b128 v[64:67], v17 offset:15360\n"
"v_xor_b32_e32 v255, 32, v17\n"
"ds_read_b128 v[68:71], v255 offset:15616\n"
"ds_read_b64_tr_b16 v[72:73], v16 offset:16384\n"
"v_xor_b32_e32 v255, 32, v16\n"
"ds_read_b64_tr_b16 v[74:75], v255 offset:16640\n"
"ds_read_b64_tr_b16 v[76:77], v255 offset:16384\n"
"ds_read_b64_tr_b16 v[78:79], v16 offset:16640\n"
"ds_read_b64_tr_b16 v[84:85], v16 offset:15360\n"
"v_mov_b32_e32 v80, 0x3f803f80\n"
"v_mov_b32_e32 v81, 0x3f803f80\n"
"v_mov_b32_e32 v82, 0x3f803f80\n"
"v_mov_b32_e32 v83, 0x3f803f80\n"
"v_xor_b32_e32 v255, 32, v16\n"
"ds_read_b64_tr_b16 v[86:87], v255 offset:15616\n"
"s_waitcnt lgkmcnt(13)\n"
"ds_read_b64_tr_b16 v[96:97], v255 offset:15360\n"
"ds_read_b64_tr_b16 v[98:99], v16 offset:15616\n"
"v_mov_b32_e32 v100, 0x3f803f80\n"
"v_mov_b32_e32 v101, 0x3f803f80\n"
"v_mov_b32_e32 v102, 0x3f803f80\n"
"v_mov_b32_e32 v103, 0x3f803f80\n"
"s_waitcnt lgkmcnt(11)\n"
"v_mfma_f32_16x16x32_bf16 v[104:107], v[40:43], v[36:39], v[104:107]\n"
"v_mfma_f32_16x16x32_bf16 v[124:127], v[52:55], v[36:39], v[124:127]\n"
"s_waitcnt lgkmcnt(8)\n"
"v_mfma_f32_16x16x32_bf16 v[88:91], v[64:67], v[60:63], v[88:91]\n"
"v_mfma_f32_16x16x32_bf16 v[176:179], v[68:71], v[60:63], v[176:179]\n"
"s_waitcnt lgkmcnt(4)\n"
"s_nop 2\n"
"v_max_f32_e32 v255, v104, v105\n"
"s_nop 1\n"
"v_max_f32_e32 v254, v88, v89\n"
"v_max3_f32 v255, v255, v106, v107\n"
"v_max3_f32 v254, v254, v90, v91\n"
"v_max3_f32 v255, v255, v124, v125\n"
"v_max3_f32 v254, v254, v176, v177\n"
"v_max3_f32 v255, v255, v126, v127\n"
"v_max3_f32 v254, v254, v178, v179\n"
"v_mov_b32_e32 v253, v255\n"
"v_mov_b32_e32 v252, v254\n"
"s_nop 0\n"
"v_permlane16_swap_b32_e32 v255, v253\n"
"v_permlane16_swap_b32_e32 v254, v252\n"
"v_max_f32_e32 v255, v255, v253\n"
"v_max_f32_e32 v254, v254, v252\n"
"v_mov_b32_e32 v253, v255\n"
"v_mov_b32_e32 v252, v254\n"
"s_nop 0\n"
"v_permlane32_swap_b32_e32 v255, v253\n"
"v_permlane32_swap_b32_e32 v254, v252\n"
"v_max_f32_e32 v255, v255, v253\n"
"v_max_f32_e32 v254, v254, v252\n"
"v_sub_f32_e32 v104, v104, v255\n"
"v_sub_f32_e32 v105, v105, v255\n"
"v_sub_f32_e32 v106, v106, v255\n"
"v_sub_f32_e32 v107, v107, v255\n"
"v_sub_f32_e32 v124, v124, v255\n"
"v_sub_f32_e32 v125, v125, v255\n"
"v_sub_f32_e32 v126, v126, v255\n"
"v_sub_f32_e32 v127, v127, v255\n"
"v_exp_f32_e32 v104, v104\n"
"v_exp_f32_e32 v105, v105\n"
"v_exp_f32_e32 v106, v106\n"
"v_exp_f32_e32 v107, v107\n"
"v_exp_f32_e32 v124, v124\n"
"v_exp_f32_e32 v125, v125\n"
"v_exp_f32_e32 v126, v126\n"
"v_exp_f32_e32 v127, v127\n"
"v_cvt_pk_bf16_f32 v36, v104, v105\n"
"v_cvt_pk_bf16_f32 v37, v106, v107\n"
"v_cvt_pk_bf16_f32 v38, v124, v125\n"
"v_cvt_pk_bf16_f32 v39, v126, v127\n"
"v_sub_f32_e32 v88, v88, v254\n"
"v_sub_f32_e32 v89, v89, v254\n"
"v_sub_f32_e32 v90, v90, v254\n"
"v_sub_f32_e32 v91, v91, v254\n"
"v_sub_f32_e32 v176, v176, v254\n"
"v_sub_f32_e32 v177, v177, v254\n"
"v_sub_f32_e32 v178, v178, v254\n"
"v_mfma_f32_16x16x32_bf16 v[40:43], v[80:83], v[36:39], 0\n"
"v_sub_f32_e32 v179, v179, v254\n"
"v_exp_f32_e32 v88, v88\n"
"v_exp_f32_e32 v89, v89\n"
"v_exp_f32_e32 v90, v90\n"
"v_mfma_f32_16x16x32_bf16 v[52:55], v[72:75], v[36:39], 0\n"
"v_exp_f32_e32 v91, v91\n"
"v_exp_f32_e32 v176, v176\n"
"v_exp_f32_e32 v177, v177\n"
"v_exp_f32_e32 v178, v178\n"
"v_mfma_f32_16x16x32_bf16 v[60:63], v[76:79], v[36:39], 0\n"
"s_waitcnt lgkmcnt(0)\n"
"v_exp_f32_e32 v179, v179\n"
"v_cvt_pk_bf16_f32 v36, v88, v89\n"
"v_cvt_pk_bf16_f32 v37, v90, v91\n"
"v_cvt_pk_bf16_f32 v38, v176, v177\n"
"v_cvt_pk_bf16_f32 v39, v178, v179\n"
"v_rcp_f32_e32 v40, v40\n"
"s_nop 0\n"
"v_mul_f32_e32 v52, v52, v40\n"
"v_mul_f32_e32 v53, v53, v40\n"
"v_mul_f32_e32 v54, v54, v40\n"
"v_mfma_f32_16x16x32_bf16 v[64:67], v[100:103], v[36:39], 0\n"
"v_mul_f32_e32 v55, v55, v40\n"
"v_mul_f32_e32 v60, v60, v40\n"
"v_mul_f32_e32 v61, v61, v40\n"
"v_mul_f32_e32 v62, v62, v40\n"
"v_mul_f32_e32 v63, v63, v40\n"
"v_mfma_f32_16x16x32_bf16 v[40:43], v[84:87], v[36:39], 0\n"
"s_add_u32 s97, s60, 0x408000\n"
"v_cvt_pk_bf16_f32 v68, v52, v53\n"
"v_cvt_pk_bf16_f32 v69, v54, v55\n"
"v_cvt_pk_bf16_f32 v70, v60, v61\n"
"v_cvt_pk_bf16_f32 v71, v62, v63\n"
"v_mfma_f32_16x16x32_bf16 v[52:55], v[96:99], v[36:39], 0\n"
"buffer_store_dwordx4 v[68:71], v0, s[4:7], s97 offen sc1\n"
"s_add_u32 s97, s60, 0x400000\n"
"v_rcp_f32_e32 v64, v64\n"
"s_nop 0\n"
"v_mul_f32_e32 v40, v40, v64\n"
"v_mul_f32_e32 v41, v41, v64\n"
"v_mul_f32_e32 v42, v42, v64\n"
"v_mul_f32_e32 v43, v43, v64\n"
"v_mul_f32_e32 v52, v52, v64\n"
"v_mul_f32_e32 v53, v53, v64\n"
"v_mul_f32_e32 v54, v54, v64\n"
"v_mul_f32_e32 v55, v55, v64\n"
"v_cvt_pk_bf16_f32 v36, v40, v41\n"
"v_cvt_pk_bf16_f32 v37, v42, v43\n"
"v_cvt_pk_bf16_f32 v38, v52, v53\n"
"v_cvt_pk_bf16_f32 v39, v54, v55\n"
"buffer_store_dwordx4 v[36:39], v0, s[4:7], s97 offen sc1\n"
";\n"
"s_add_u32 s97, s58, 0x440000\n"
"buffer_load_dwordx4 v[36:39], v0, s[4:7], s97 offen sc1\n"
"buffer_load_dwordx4 v[40:43], v0, s[4:7], s97 offen offset:1024 sc1\n"
"buffer_load_dwordx4 v[52:55], v0, s[4:7], s97 offen offset:2048 sc1\n"
"buffer_load_dwordx4 v[60:63], v0, s[4:7], s97 offen offset:3072 sc1\n"
"buffer_load_dwordx4 v[64:67], v1, s[4:7], s97 offen sc1\n"
"buffer_load_dwordx4 v[68:71], v1, s[4:7], s97 offen offset:1024 sc1\n"
"buffer_load_dwordx4 v[72:75], v1, s[4:7], s97 offen offset:2048 sc1\n"
"buffer_load_dwordx4 v[76:79], v1, s[4:7], s97 offen offset:3072 sc1\n"
"buffer_load_dwordx4 a[24:27], v1, s[8:11], s98 offen offset:2048 sc1\n"
"buffer_load_dwordx4 a[28:31], v1, s[8:11], s98 offen offset:3072 sc1\n"
"s_add_u32 s98, s69, 0x2000\n"
"buffer_load_dwordx4 a[32:35], v0, s[8:11], s98 offen sc1\n"
"buffer_load_dwordx4 a[36:39], v0, s[8:11], s98 offen offset:1024 sc1\n"
"buffer_load_dwordx4 a[40:43], v0, s[8:11], s98 offen offset:2048 sc1\n"
"buffer_load_dwordx4 a[44:47], v0, s[8:11], s98 offen offset:3072 sc1\n"
"buffer_load_dwordx4 a[48:51], v1, s[8:11], s98 offen sc1\n"
"buffer_load_dwordx4 a[52:55], v1, s[8:11], s98 offen offset:1024 sc1\n"
"s_waitcnt vmcnt(39)\n"
"; @B G2 b.0\n"
"v_mfma_f32_16x16x32_bf16 v[80:83], a[128:131], v[120:123], 0\n"
"v_mfma_f32_16x16x32_bf16 v[80:83], a[132:135], v[136:139], v[80:83]\n"
"s_waitcnt vmcnt(37)\n"
"v_mfma_f32_16x16x32_bf16 v[80:83], a[136:139], v[144:147], v[80:83]\n"
"s_add_u32 s97, s59, 0x440000\n"
"buffer_load_dwordx4 v[84:87], v0, s[4:7], s97 offen sc1\n"
"v_mfma_f32_16x16x32_bf16 v[80:83], a[140:143], v[192:195], v[80:83]\n"
"s_waitcnt vmcnt(36)\n"
"v_mfma_f32_16x16x32_bf16 v[80:83], a[144:147], v[196:199], v[80:83]\n"
"v_mfma_f32_16x16x32_bf16 v[80:83], a[148:151], v[188:191], v[80:83]\n"
"s_waitcnt vmcnt(34)\n"
"buffer_load_dwordx4 v[88:91], v0, s[4:7], s97 offen offset:1024 sc1\n"
"v_mfma_f32_16x16x32_bf16 v[80:83], a[152:155], v[108:111], v[80:83]\n"
"v_mfma_f32_16x16x32_bf16 v[80:83], a[156:159], v[92:95], v[80:83]\n"
"v_mfma_f32_16x16x32_bf16 v[96:99], a[188:191], v[92:95], 0\n"
"buffer_load_dwordx4 v[100:103], v0, s[4:7], s97 offen offset:2048 sc1\n"
"v_mfma_f32_16x16x32_bf16 v[96:99], a[184:187], v[108:111], v[96:99]\n"
"v_mfma_f32_16x16x32_bf16 v[96:99], a[180:183], v[188:191], v[96:99]\n"
"v_mfma_f32_16x16x32_bf16 v[96:99], a[176:179], v[196:199], v[96:99]\n"
"buffer_load_dwordx4 v[104:107], v0, s[4:7], s97 offen offset:3072 sc1\n"
"v_mfma_f32_16x16x32_bf16 v[96:99], a[172:175], v[192:195], v[96:99]\n"
"s_nop 0\n"
"v_cmp_u_f32_e32 vcc, v80, v80\n"
"v_mfma_f32_16x16x32_bf16 v[96:99], a[168:171], v[144:147], v[96:99]\n"
"s_cbranch_vccnz .Lmk_p63s\n"
".Lmk_p63r:\n"
"v_mfma_f32_16x16x32_bf16 v[96:99], a[164:167], v[136:139], v[96:99]\n"
"ds_write_b128 v3, v[80:83]\n"
"v_mfma_f32_16x16x32_bf16 v[96:99], a[160:163], v[120:123], v[96:99]\n"
"buffer_load_dwordx4 v[80:83], v1, s[4:7], s97 offen sc1\n"
"v_mfma_f32_16x16x32_bf16 v[112:115], a[192:195], v[120:123], 0\n"
"v_mfma_f32_16x16x32_bf16 v[112:115], a[196:199], v[136:139], v[112:115]\n"
"v_mfma_f32_16x16x32_bf16 v[112:115], a[200:203], v[144:147], v[112:115]\n"
"buffer_load_dwordx4 v[116:119], v1, s[4:7], s97 offen offset:1024 sc1\n"
"v_mfma_f32_16x16x32_bf16 v[112:115], a[204:207], v[192:195], v[112:115]\n"
"v_mfma_f32_16x16x32_bf16 v[112:115], a[208:211], v[196:199], v[112:115]\n"
"v_mfma_f32_16x16x32_bf16 v[112:115], a[212:215], v[188:191], v[112:115]\n"
"ds_write_b128 v3, v[96:99] offset:1024\n"
"v_mfma_f32_16x16x32_bf16 v[112:115], a[216:219], v[108:111], v[112:115]\n"
"buffer_load_dwordx4 v[96:99], v1, s[4:7], s97 offen offset:2048 sc1\n"
"v_mfma_f32_16x16x32_bf16 v[112:115], a[220:223], v[92:95], v[112:115]\n"
"v_mfma_f32_16x16x32_bf16 v[124:127], a[252:255], v[92:95], 0\n"
"v_mfma_f32_16x16x32_bf16 v[124:127], a[248:251], v[108:111], v[124:127]\n"
"buffer_load_dwordx4 v[92:95], v1, s[4:7], s97 offen offset:3072 sc1\n"
"v_mfma_f32_16x16x32_bf16 v[124:127], a[244:247], v[188:191], v[124:127]\n"
"v_mfma_f32_16x16x32_bf16 v[124:127], a[240:243], v[196:199], v[124:127]\n"
"v_mfma_f32_16x16x32_bf16 v[124:127], a[236:239], v[192:195], v[124:127]\n"
"s_mov_b32 s97, .Lmk_a0-.Lmk_g67\n"
"s_getpc_b64 s[84:85]\n"
".Lmk_g67:\n"
"s_add_u32 s84, s84, s97\n"
"s_addc_u32 s85, s85, 0\n"
"s_and_b32 s84, s84, -16\n"
"v_mfma_f32_16x16x32_bf16 v[124:127], a[232:235], v[144:147], v[124:127]\n"
"ds_write_b128 v3, v[112:115] offset:2048\n"
"v_mfma_f32_16x16x32_bf16 v[124:127], a[228:231], v[136:139], v[124:127]\n"
"s_and_b32 s85, s85, 0xffff\n"
"s_sub_u32 s97, .Lmk_end-.Lmk_g67, s97\n"
"s_min_u32 s86, s97, 0x3000\n"
"s_mov_b32 s87, 0x20000\n"
"v_mfma_f32_16x16x32_bf16 v[124:127], a[224:227], v[120:123], v[124:127]\n"
"s_waitcnt vmcnt(37)\n"
"s_mov_b32 m0, 0x27800\n"
"s_nop 0\n"
"buffer_load_dwordx4 v0, s[84:87], s99 offen lds\n"
"; @B G2 b.1\n"
"v_mfma_f32_16x16x32_bf16 v[108:111], a[128:131], v[148:151], 0\n"
"v_mfma_f32_16x16x32_bf16 v[108:111], a[132:135], v[180:183], v[108:111]\n"
"v_mfma_f32_16x16x32_bf16 v[108:111], a[136:139], v[24:27], v[108:111]\n"
"v_mfma_f32_16x16x32_bf16 v[108:111], a[140:143], v[32:35], v[108:111]\n"
"s_waitcnt vmcnt(32)\n"
"v_mfma_f32_16x16x32_bf16 v[108:111], a[144:147], v[44:47], v[108:111]\n"
"v_mfma_f32_16x16x32_bf16 v[108:111], a[148:151], v[48:51], v[108:111]\n"
"ds_write_b128 v3, v[124:127] offset:3072\n"
"v_mfma_f32_16x16x32_bf16 v[108:111], a[152:155], v[28:31], v[108:111]\n"
"v_mfma_f32_16x16x32_bf16 v[108:111], a[156:159], v[56:59], v[108:111]\n"
"v_mfma_f32_16x16x32_bf16 v[112:115], a[188:191], v[56:59], 0\n"
"s_add_u32 s97, s56, 0x400000\n"
"buffer_load_dwordx4 v[120:123], v0, s[4:7], s97 offen sc1\n"
"v_mfma_f32_16x16x32_bf16 v[112:115], a[184:187], v[28:31], v[112:115]\n"
"v_mfma_f32_16x16x32_bf16 v[112:115], a[180:183], v[48:51], v[112:115]\n"
"v_mfma_f32_16x16x32_bf16 v[112:115], a[176:179], v[44:47], v[112:115]\n"
"buffer_load_dwordx4 v[124:127], v0, s[4:7], s97 offen offset:1024 sc1\n"
"v_mfma_f32_16x16x32_bf16 v[112:115], a[172:175], v[32:35], v[112:115]\n"
"v_cmp_u_f32_e32 vcc, v108, v108\n"
"v_mfma_f32_16x16x32_bf16 v[112:115], a[168:171], v[24:27], v[112:115]\n"
"s_cbranch_vccnz .Lmk_p64s\n"
".Lmk_p64r:\n"
"v_mfma_f32_16x16x32_bf16 v[112:115], a[164:167], v[180:183], v[112:115]\n"
"ds_write_b128 v3, v[108:111] offset:6144\n"
"v_mfma_f32_16x16x32_bf16 v[112:115], a[160:163], v[148:151], v[112:115]\n"
"buffer_load_dwordx4 v[108:111], v0, s[4:7], s97 offen offset:2048 sc1\n"
"v_mfma_f32_16x16x32_bf16 v[128:131], a[192:195], v[148:151], 0\n"
"v_mfma_f32_16x16x32_bf16 v[128:131], a[196:199], v[180:183], v[128:131]\n"
"v_mfma_f32_16x16x32_bf16 v[128:131], a[200:203], v[24:27], v[128:131]\n"
"buffer_load_dwordx4 v[132:135], v0, s[4:7], s97 offen offset:3072 sc1\n"
"v_mfma_f32_16x16x32_bf16 v[128:131], a[204:207], v[32:35], v[128:131]\n"
"v_mfma_f32_16x16x32_bf16 v[128:131], a[208:211], v[44:47], v[128:131]\n"
"ds_read_b128 v[136:139], v2 offset:8192\n"
"ds_read_b128 v[140:143], v2 offset:9216\n"
"v_mfma_f32_16x16x32_bf16 v[128:131], a[212:215], v[48:51], v[128:131]\n"
"ds_write_b128 v3, v[112:115] offset:7168\n"
"v_mfma_f32_16x16x32_bf16 v[128:131], a[216:219], v[28:31], v[128:131]\n"
"buffer_load_dwordx4 v[112:115], v1, s[4:7], s97 offen sc1\n"
"v_mfma_f32_16x16x32_bf16 v[128:131], a[220:223], v[56:59], v[128:131]\n"
"v_mfma_f32_16x16x32_bf16 v[144:147], a[252:255], v[56:59], 0\n"
"v_mfma_f32_16x16x32_bf16 v[144:147], a[248:251], v[28:31], v[144:147]\n"
"buffer_load_dwordx4 v[28:31], v1, s[4:7], s97 offen offset:1024 sc1\n"
"s_waitcnt lgkmcnt(1)\n"
"v_mfma_f32_16x16x32_bf16 v[144:147], a[244:247], v[48:51], v[144:147]\n"
"v_lshlrev_b32_e32 v255, 16, v136\n"
"v_and_b32_e32 v254, 0xffff0000, v136\n"
"v_lshlrev_b32_e32 v253, 16, v137\n"
"v_mfma_f32_16x16x32_bf16 v[144:147], a[240:243], v[44:47], v[144:147]\n"
"v_and_b32_e32 v252, 0xffff0000, v137\n"
"v_lshlrev_b32_e32 v251, 16, v138\n"
"v_and_b32_e32 v250, 0xffff0000, v138\n"
"v_mfma_f32_16x16x32_bf16 v[144:147], a[236:239], v[32:35], v[144:147]\n"
"buffer_load_dwordx4 v[32:35], v1, s[4:7], s97 offen offset:2048 sc1\n"
"v_lshlrev_b32_e32 v249, 16, v139\n"
"v_mfma_f32_16x16x32_bf16 v[144:147], a[232:235], v[24:27], v[144:147]\n"
"ds_write_b128 v3, v[128:131] offset:8192\n"
"v_mfma_f32_16x16x32_bf16 v[144:147], a[228:231], v[180:183], v[144:147]\n"
"v_and_b32_e32 v248, 0xffff0000, v139\n"
"v_lshlrev_b32_e32 v247, 16, v140\n"
"v_and_b32_e32 v246, 0xffff0000, v140\n"
"v_mfma_f32_16x16x32_bf16 v[144:147], a[224:227], v[148:151], v[144:147]\n"
"s_waitcnt vmcnt(28)\n"
"buffer_load_dwordx4 v[24:27], v1, s[4:7], s97 offen offset:3072 sc1\n"
"; @B G2 b.2\n"
"v_mfma_f32_16x16x32_bf16 v[44:47], a[128:131], v[36:39], 0\n"
"s_add_u32 s97, s57, 0x400000\n"
"v_lshlrev_b32_e32 v245, 16, v141\n"
"v_and_b32_e32 v244, 0xffff0000, v141\n"
"v_lshlrev_b32_e32 v243, 16, v142\n"
"v_mfma_f32_16x16x32_bf16 v[44:47], a[132:135], v[40:43], v[44:47]\n"
"v_and_b32_e32 v242, 0xffff0000, v142\n"
"v_lshlrev_b32_e32 v241, 16, v143\n"
"v_and_b32_e32 v240, 0xffff0000, v143\n"
"v_mfma_f32_16x16x32_bf16 v[44:47], a[136:139], v[52:55], v[44:47]\n"
"buffer_load_dwordx4 v[48:51], v0, s[4:7], s97 offen sc1\n"
"v_mfma_f32_16x16x32_bf16 v[44:47], a[140:143], v[60:63], v[44:47]\n"
"s_waitcnt vmcnt(26)\n"
"v_mfma_f32_16x16x32_bf16 v[44:47], a[144:147], v[64:67], v[44:47]\n"
"v_mfma_f32_16x16x32_bf16 v[44:47], a[148:151], v[68:71], v[44:47]\n"
"ds_write_b128 v3, v[144:147] offset:9216\n"
"v_mfma_f32_16x16x32_bf16 v[44:47], a[152:155], v[72:75], v[44:47]\n"
"buffer_load_dwordx4 v[56:59], v0, s[4:7], s97 offen offset:1024 sc1\n"
"v_mfma_f32_16x16x32_bf16 v[44:47], a[156:159], v[76:79], v[44:47]\n"
"v_mfma_f32_16x16x32_bf16 v[128:131], a[188:191], v[76:79], 0\n"
"v_mfma_f32_16x16x32_bf16 v[128:131], a[184:187], v[72:75], v[128:131]\n"
"s_waitcnt lgkmcnt(0)\n"
"s_barrier\n"
"v_mfma_f32_16x16x32_bf16 v[128:131], a[180:183], v[68:71], v[128:131]\n"
"buffer_load_dwordx4 v[136:139], v0, s[4:7], s97 offen offset:2048 sc1\n"
"v_mfma_f32_16x16x32_bf16 v[128:131], a[176:179], v[64:67], v[128:131]\n"
"ds_read_b128 v[140:143], v4\n"
"ds_read_b128 v[144:147], v5\n"
"v_mfma_f32_16x16x32_bf16 v[128:131], a[172:175], v[60:63], v[128:131]\n"
"v_cmp_u_f32_e32 vcc, v44, v44\n"
"v_mfma_f32_16x16x32_bf16 v[128:131], a[168:171], v[52:55], v[128:131]\n"
"s_cbranch_vccnz .Lmk_p65s\n"
".Lmk_p65r:\n"
"v_mfma_f32_16x16x32_bf16 v[128:131], a[164:167], v[40:43], v[128:131]\n"
"ds_write_b128 v3, v[44:47] offset:12288\n"
"v_mfma_f32_16x16x32_bf16 v[128:131], a[160:163], v[36:39], v[128:131]\n"
"buffer_load_dwordx4 v[44:47], v0, s[4:7], s97 offen offset:3072 sc1\n"
"v_mfma_f32_16x16x32_bf16 v[148:151], a[192:195], v[36:39], 0\n"
"ds_read_b128 v[152:155], v4 offset:1024\n"
"ds_read_b128 v[156:159], v5 offset:1024\n"
"s_waitcnt lgkmcnt(3)\n"
"v_mfma_f32_16x16x32_bf16 v[148:151], a[196:199], v[40:43], v[148:151]\n"
"v_add_f32_e32 v140, v140, v255\n"
"v_add_f32_e32 v141, v141, v254\n"
"v_add_f32_e32 v142, v142, v253\n"
"v_mfma_f32_16x16x32_bf16 v[148:151], a[200:203], v[52:55], v[148:151]\n"
"buffer_load_dwordx4 v[160:163], v1, s[4:7], s97 offen sc1\n"
"v_add_f32_e32 v143, v143, v252\n"
"v_mfma_f32_16x16x32_bf16 v[148:151], a[204:207], v[60:63], v[148:151]\n"
"v_add_f32_e32 v140, v140, v144\n"
"v_add_f32_e32 v141, v141, v145\n"
"v_add_f32_e32 v142, v142, v146\n"
"v_mfma_f32_16x16x32_bf16 v[148:151], a[208:211], v[64:67], v[148:151]\n"
"v_add_f32_e32 v143, v143, v147\n"
"v_mfma_f32_16x16x32_bf16 v[148:151], a[212:215], v[68:71], v[148:151]\n"
"ds_write_b128 v3, v[128:131] offset:13312\n"
"v_mfma_f32_16x16x32_bf16 v[148:151], a[216:219], v[72:75], v[148:151]\n"
"buffer_load_dwordx4 v[128:131], v1, s[4:7], s97 offen offset:1024 sc1\n"
"s_waitcnt lgkmcnt(1)\n"
"v_mfma_f32_16x16x32_bf16 v[148:151], a[220:223], v[76:79], v[148:151]\n"
"ds_read_b128 v[144:147], v4 offset:2048\n"
"ds_read_b128 v[164:167], v5 offset:2048\n"
"v_add_f32_e32 v152, v152, v251\n"
"v_mfma_f32_16x16x32_bf16 v[168:171], a[252:255], v[76:79], 0\n"
"v_add_f32_e32 v153, v153, v250\n"
"v_add_f32_e32 v154, v154, v249\n"
"v_add_f32_e32 v155, v155, v248\n"
"v_mfma_f32_16x16x32_bf16 v[168:171], a[248:251], v[72:75], v[168:171]\n"
"buffer_load_dwordx4 v[72:75], v1, s[4:7], s97 offen offset:2048 sc1\n"
"v_add_f32_e32 v152, v152, v156\n"
"v_mfma_f32_16x16x32_bf16 v[168:171], a[244:247], v[68:71], v[168:171]\n"
"v_add_f32_e32 v153, v153, v157\n"
"v_add_f32_e32 v154, v154, v158\n"
"v_add_f32_e32 v155, v155, v159\n"
"v_mfma_f32_16x16x32_bf16 v[168:171], a[240:243], v[64:67], v[168:171]\n"
"v_mfma_f32_16x16x32_bf16 v[168:171], a[236:239], v[60:63], v[168:171]\n"
"buffer_load_dwordx4 v[60:63], v1, s[4:7], s97 offen offset:3072 sc1\n"
"s_waitcnt lgkmcnt(0)\n"
"v_mfma_f32_16x16x32_bf16 v[168:171], a[232:235], v[52:55], v[168:171]\n"
"ds_write_b128 v3, v[148:151] offset:14336\n"
"v_mfma_f32_16x16x32_bf16 v[168:171], a[228:231], v[40:43], v[168:171]\n"
"s_add_u32 s97, s58, 0x400000\n"
"ds_read_b128 v[40:43], v4 offset:3072\n"
"ds_read_b128 v[52:55], v5 offset:3072\n"
"v_add_f32_e32 v144, v144, v247\n"
"v_mfma_f32_16x16x32_bf16 v[168:171], a[224:227], v[36:39], v[168:171]\n"
"s_waitcnt vmcnt(21)\n"
"buffer_load_dwordx4 v[36:39], v0, s[4:7], s97 offen sc1\n"
"; @B G2 b.3\n"
"v_mfma_f32_16x16x32_bf16 v[64:67], a[128:131], v[84:87], v[140:143]\n"
"v_add_f32_e32 v145, v145, v246\n"
"v_add_f32_e32 v146, v146, v245\n"
"v_add_f32_e32 v147, v147, v244\n"
"v_mfma_f32_16x16x32_bf16 v[64:67], a[132:135], v[88:91], v[64:67]\n"
"v_add_f32_e32 v144, v144, v164\n"
"v_add_f32_e32 v145, v145, v165\n"
"v_add_f32_e32 v146, v146, v166\n"
"v_mfma_f32_16x16x32_bf16 v[64:67], a[136:139], v[100:103], v[64:67]\n"
"buffer_load_dwordx4 v[68:71], v0, s[4:7], s97 offen offset:1024 sc1\n"
"v_add_f32_e32 v147, v147, v167\n"
"v_mfma_f32_16x16x32_bf16 v[64:67], a[140:143], v[104:107], v[64:67]\n"
"s_waitcnt vmcnt(19)\n"
"s_waitcnt lgkmcnt(0)\n"
"v_add_f32_e32 v40, v40, v243\n"
"v_add_f32_e32 v41, v41, v242\n"
"v_mfma_f32_16x16x32_bf16 v[64:67], a[144:147], v[80:83], v[64:67]\n"
"v_add_f32_e32 v42, v42, v241\n"
"v_add_f32_e32 v43, v43, v240\n"
"v_add_f32_e32 v40, v40, v52\n"
"v_mfma_f32_16x16x32_bf16 v[64:67], a[148:151], v[116:119], v[64:67]\n"
"ds_write_b128 v3, v[168:171] offset:15360\n"
"v_mfma_f32_16x16x32_bf16 v[64:67], a[152:155], v[96:99], v[64:67]\n"
"buffer_load_dwordx4 v[76:79], v0, s[4:7], s97 offen offset:2048 sc1\n"
"v_add_f32_e32 v41, v41, v53\n"
"v_mfma_f32_16x16x32_bf16 v[64:67], a[156:159], v[92:95], v[64:67]\n"
"v_add_f32_e32 v42, v42, v54\n"
"v_add_f32_e32 v43, v43, v55\n"
"v_mfma_f32_16x16x32_bf16 v[52:55], a[188:191], v[92:95], v[152:155]\n"
"v_mfma_f32_16x16x32_bf16 v[52:55], a[184:187], v[96:99], v[52:55]\n"
"s_waitcnt lgkmcnt(0)\n"
"s_barrier\n"
"v_mfma_f32_16x16x32_bf16 v[52:55], a[180:183], v[116:119], v[52:55]\n"
"buffer_load_dwordx4 v[148:151], v0, s[4:7], s97 offen offset:3072 sc1\n"
"v_mfma_f32_16x16x32_bf16 v[52:55], a[176:179], v[80:83], v[52:55]\n"
"ds_read_b128 v[156:159], v6\n"
"ds_read_b128 v[164:167], v6 offset:1024\n"
"v_mfma_f32_16x16x32_bf16 v[52:55], a[172:175], v[104:107], v[52:55]\n"
"ds_read_b128 v[168:171], v6 offset:2048\n"
"v_cmp_u_f32_e32 vcc, v64, v64\n"
"v_mfma_f32_16x16x32_bf16 v[52:55], a[168:171], v[100:103], v[52:55]\n"
"s_cbranch_vccnz .Lmk_p66s\n"
".Lmk_p66r:\n"
"ds_read_b128 v[140:143], v6 offset:3072\n"
"v_mfma_f32_16x16x32_bf16 v[52:55], a[164:167], v[88:91], v[52:55]\n"
"buffer_load_dwordx4 v[152:155], v1, s[4:7], s97 offen sc1\n"
"v_mfma_f32_16x16x32_bf16 v[52:55], a[160:163], v[84:87], v[52:55]\n"
"v_mfma_f32_16x16x32_bf16 v[144:147], a[192:195], v[84:87], v[144:147]\n"
"v_mfma_f32_16x16x32_bf16 v[144:147], a[196:199], v[88:91], v[144:147]\n"
"buffer_load_dwordx4 v[172:175], v1, s[4:7], s97 offen offset:1024 sc1\n"
"v_mfma_f32_16x16x32_bf16 v[144:147], a[200:203], v[100:103], v[144:147]\n"
"v_mfma_f32_16x16x32_bf16 v[144:147], a[204:207], v[104:107], v[144:147]\n"
"v_mfma_f32_16x16x32_bf16 v[144:147], a[208:211], v[80:83], v[144:147]\n"
"buffer_load_dwordx4 v[176:179], v1, s[4:7], s97 offen offset:2048 sc1\n"
"s_waitcnt lgkmcnt(0)\n"
"v_mfma_f32_16x16x32_bf16 v[144:147], a[212:215], v[116:119], v[144:147]\n"
"v_add_f32_e32 v156, v64, v156\n"
"v_add_f32_e32 v157, v65, v157\n"
"v_add_f32_e32 v158, v66, v158\n"
"v_mfma_f32_16x16x32_bf16 v[144:147], a[216:219], v[96:99], v[144:147]\n"
"v_add_f32_e32 v159, v67, v159\n"
"v_add_f32_e32 v164, v52, v164\n"
"v_add_f32_e32 v165, v53, v165\n"
"v_mfma_f32_16x16x32_bf16 v[144:147], a[220:223], v[92:95], v[144:147]\n"
"buffer_load_dwordx4 v[64:67], v1, s[4:7], s97 offen offset:3072 sc1\n"
"v_add_f32_e32 v166, v54, v166\n"
"v_mfma_f32_16x16x32_bf16 v[40:43], a[252:255], v[92:95], v[40:43]\n"
"v_add_f32_e32 v167, v55, v167\n"
"v_cvt_pk_bf16_f32 v52, v156, v157\n"
"v_cvt_pk_bf16_f32 v53, v158, v159\n"
"v_mfma_f32_16x16x32_bf16 v[40:43], a[248:251], v[96:99], v[40:43]\n"
"s_add_u32 s97, s61, 0x840000\n"
"v_cvt_pk_bf16_f32 v54, v164, v165\n"
"v_cvt_pk_bf16_f32 v55, v166, v167\n"
"v_mfma_f32_16x16x32_bf16 v[40:43], a[244:247], v[116:119], v[40:43]\n"
"buffer_store_dwordx4 v[52:55], v0, s[4:7], s97 offen sc1\n"
"v_mfma_f32_16x16x32_bf16 v[40:43], a[240:243], v[80:83], v[40:43]\n"
"ds_write_b128 v2, v[52:55] offset:8192\n"
"v_mfma_f32_16x16x32_bf16 v[40:43], a[236:239], v[104:107], v[40:43]\n"
"v_mfma_f32_16x16x32_bf16 v[40:43], a[232:235], v[100:103], v[40:43]\n"
"v_mfma_f32_16x16x32_bf16 v[40:43], a[228:231], v[88:91], v[40:43]\n"
"v_mfma_f32_16x16x32_bf16 v[40:43], a[224:227], v[84:87], v[40:43]\n"
"; @B fin b\n"
"s_add_u32 s97, s61, 0x840000\n"
"v_add_f32_e32 v168, v144, v168\n"
"v_add_f32_e32 v169, v145, v169\n"
"v_add_f32_e32 v170, v146, v170\n"
"v_add_f32_e32 v171, v147, v171\n"
"s_nop 2\n"
"v_add_f32_e32 v140, v40, v140\n"
"v_add_f32_e32 v141, v41, v141\n"
"v_add_f32_e32 v142, v42, v142\n"
"v_add_f32_e32 v143, v43, v143\n"
"v_cvt_pk_bf16_f32 v40, v168, v169\n"
"v_cvt_pk_bf16_f32 v41, v170, v171\n"
"v_cvt_pk_bf16_f32 v42, v140, v141\n"
"v_cvt_pk_bf16_f32 v43, v142, v143\n"
"buffer_store_dwordx4 v[40:43], v0, s[4:7], s97 offen offset:1024 sc1\n"
"s_add_u32 s97, s59, 0x400000\n"
"buffer_load_dwordx4 v[80:83], v0, s[4:7], s97 offen sc1\n"
"buffer_load_dwordx4 v[84:87], v0, s[4:7], s97 offen offset:1024 sc1\n"
"buffer_load_dwordx4 v[88:91], v0, s[4:7], s97 offen offset:2048 sc1\n"
"buffer_load_dwordx4 v[92:95], v0, s[4:7], s97 offen offset:3072 sc1\n"
"buffer_load_dwordx4 v[96:99], v1, s[4:7], s97 offen sc1\n"
"buffer_load_dwordx4 v[100:103], v1, s[4:7], s97 offen offset:1024 sc1\n"
"buffer_load_dwordx4 v[104:107], v1, s[4:7], s97 offen offset:2048 sc1\n"
"buffer_load_dwordx4 v[116:119], v1, s[4:7], s97 offen offset:3072 sc1\n"
"s_waitcnt vmcnt(32)\n"
"; @B G2 a.0\n"
"v_mfma_f32_16x16x32_bf16 v[140:143], a[128:131], v[120:123], 0\n"
"v_mfma_f32_16x16x32_bf16 v[140:143], a[132:135], v[124:127], v[140:143]\n"
"s_waitcnt vmcnt(30)\n"
"ds_write_b128 v2, v[40:43] offset:9216\n"
"v_mfma_f32_16x16x32_bf16 v[140:143], a[136:139], v[108:111], v[140:143]\n"
"v_mov_b32_e32 v144, 0x3f803f80\n"
"v_mov_b32_e32 v145, 0x3f803f80\n"
"v_mfma_f32_16x16x32_bf16 v[140:143], a[140:143], v[132:135], v[140:143]\n"
"s_waitcnt vmcnt(28)\n"
"v_mov_b32_e32 v146, 0x3f803f80\n"
"v_mov_b32_e32 v147, 0x3f803f80\n"
"v_mfma_f32_16x16x32_bf16 v[140:143], a[144:147], v[112:115], v[140:143]\n"
"s_nop 0\n"
"v_mfma_f32_16x16x32_bf16 v[156:159], v[144:147], v[52:55], 0\n"
"v_mfma_f32_16x16x32_bf16 v[156:159], v[144:147], v[40:43], v[156:159]\n"
"v_mfma_f32_16x16x32_bf16 v[144:147], v[52:55], v[52:55], 0\n"
"v_mfma_f32_16x16x32_bf16 v[144:147], v[40:43], v[40:43], v[144:147]\n"
"buffer_load_dwordx4 a[56:59], v1, s[8:11], s98 offen offset:2048 sc1\n"
"v_mfma_f32_16x16x32_bf16 v[140:143], a[148:151], v[28:31], v[140:143]\n"
"s_waitcnt vmcnt(27)\n"
"v_mfma_f32_16x16x32_bf16 v[140:143], a[152:155], v[32:35], v[140:143]\n"
"v_mfma_f32_16x16x32_bf16 v[140:143], a[156:159], v[24:27], v[140:143]\n"
"buffer_load_dwordx4 a[60:63], v1, s[8:11], s98 offen offset:3072 sc1\n"
"v_mfma_f32_16x16x32_bf16 v[40:43], a[188:191], v[24:27], 0\n"
"s_mov_b32 s96, 0xaaaaaaaa\n"
"s_mov_b32 s97, 0xaaaaaaaa\n"
"s_add_u32 s98, s69, 0x4000\n"
"v_mfma_f32_16x16x32_bf16 v[40:43], a[184:187], v[32:35], v[40:43]\n"
"v_mfma_f32_16x16x32_bf16 v[40:43], a[180:183], v[28:31], v[40:43]\n"
"buffer_load_dwordx4 a[64:67], v0, s[8:11], s98 offen sc1\n"
"v_mfma_f32_16x16x32_bf16 v[40:43], a[176:179], v[112:115], v[40:43]\n"
"v_cndmask_b32_e64 v144, v144, v145, s[96:97]\n"
"v_cndmask_b32_e64 v146, v146, v147, s[96:97]\n"
"v_mfma_f32_16x16x32_bf16 v[40:43], a[172:175], v[132:135], v[40:43]\n"
"v_cmp_u_f32_e32 vcc, v140, v140\n"
"v_mfma_f32_16x16x32_bf16 v[40:43], a[168:171], v[108:111], v[40:43]\n"
"s_cbranch_vccnz .Lmk_p68s\n"
".Lmk_p68r:\n"
"v_mfma_f32_16x16x32_bf16 v[40:43], a[164:167], v[124:127], v[40:43]\n"
"ds_write_b128 v3, v[140:143]\n"
"v_mfma_f32_16x16x32_bf16 v[40:43], a[160:163], v[120:123], v[40:43]\n"
"s_mov_b32 s96, 0xcccccccc\n"
"s_mov_b32 s97, 0xcccccccc\n"
"v_mfma_f32_16x16x32_bf16 v[52:55], a[192:195], v[120:123], 0\n"
"v_cndmask_b32_e64 v157, v144, v146, s[96:97]\n"
"v_mfma_f32_16x16x32_bf16 v[52:55], a[196:199], v[124:127], v[52:55]\n"
"s_mov_b32 s96, 0xf0000f\n"
"s_mov_b32 s97, 0xf0000f00\n"
"v_mfma_f32_16x16x32_bf16 v[52:55], a[200:203], v[108:111], v[52:55]\n"
"v_cndmask_b32_e64 v156, v156, v157, s[96:97]\n"
"s_add_u32 s97, s63, 0x84000\n"
"v_mfma_f32_16x16x32_bf16 v[52:55], a[204:207], v[132:135], v[52:55]\n"
"buffer_store_dword v156, v14, s[4:7], s97 offen sc1\n"
"v_mfma_f32_16x16x32_bf16 v[52:55], a[208:211], v[112:115], v[52:55]\n"
"buffer_load_dwordx4 a[68:71], v0, s[8:11], s98 offen offset:1024 sc1\n"
"v_mfma_f32_16x16x32_bf16 v[52:55], a[212:215], v[28:31], v[52:55]\n"
"ds_write_b128 v3, v[40:43] offset:1024\n"
"v_mfma_f32_16x16x32_bf16 v[52:55], a[216:219], v[32:35], v[52:55]\n"
"v_mfma_f32_16x16x32_bf16 v[52:55], a[220:223], v[24:27], v[52:55]\n"
"buffer_load_dwordx4 a[72:75], v0, s[8:11], s98 offen offset:2048 sc1\n"
"v_mfma_f32_16x16x32_bf16 v[40:43], a[252:255], v[24:27], 0\n"
"v_mfma_f32_16x16x32_bf16 v[40:43], a[248:251], v[32:35], v[40:43]\n"
"v_mfma_f32_16x16x32_bf16 v[40:43], a[244:247], v[28:31], v[40:43]\n"
"buffer_load_dwordx4 a[76:79], v0, s[8:11], s98 offen offset:3072 sc1\n"
"v_mfma_f32_16x16x32_bf16 v[40:43], a[240:243], v[112:115], v[40:43]\n"
"v_mfma_f32_16x16x32_bf16 v[40:43], a[236:239], v[132:135], v[40:43]\n"
"v_mfma_f32_16x16x32_bf16 v[40:43], a[232:235], v[108:111], v[40:43]\n"
"ds_write_b128 v3, v[52:55] offset:2048\n"
"v_mfma_f32_16x16x32_bf16 v[40:43], a[228:231], v[124:127], v[40:43]\n"
"buffer_load_dwordx4 a[80:83], v1, s[8:11], s98 offen sc1\n"
"v_mfma_f32_16x16x32_bf16 v[40:43], a[224:227], v[120:123], v[40:43]\n"
"s_waitcnt vmcnt(30)\n"
"; @B G2 a.1\n"
"v_mfma_f32_16x16x32_bf16 v[24:27], a[128:131], v[48:51], 0\n"
"v_mfma_f32_16x16x32_bf16 v[24:27], a[132:135], v[56:59], v[24:27]\n"
"buffer_load_dwordx4 a[84:87], v1, s[8:11], s98 offen offset:1024 sc1\n"
"v_mfma_f32_16x16x32_bf16 v[24:27], a[136:139], v[136:139], v[24:27]\n"
"v_mfma_f32_16x16x32_bf16 v[24:27], a[140:143], v[44:47], v[24:27]\n"
"s_waitcnt vmcnt(27)\n"
"v_mfma_f32_16x16x32_bf16 v[24:27], a[144:147], v[160:163], v[24:27]\n"
"buffer_load_dwordx4 a[88:91], v1, s[8:11], s98 offen offset:2048 sc1\n"
"v_mfma_f32_16x16x32_bf16 v[24:27], a[148:151], v[128:131], v[24:27]\n"
"ds_write_b128 v3, v[40:43] offset:3072\n"
"v_mfma_f32_16x16x32_bf16 v[24:27], a[152:155], v[72:75], v[24:27]\n"
"v_mfma_f32_16x16x32_bf16 v[24:27], a[156:159], v[60:63], v[24:27]\n"
"buffer_load_dwordx4 a[92:95], v1, s[8:11], s98 offen offset:3072 sc1\n"
"v_mfma_f32_16x16x32_bf16 v[28:31], a[188:191], v[60:63], 0\n"
"s_add_u32 s98, s69, 0x6000\n"
"v_mfma_f32_16x16x32_bf16 v[28:31], a[184:187], v[72:75], v[28:31]\n"
"v_mfma_f32_16x16x32_bf16 v[28:31], a[180:183], v[128:131], v[28:31]\n"
"buffer_load_dwordx4 a[96:99], v0, s[8:11], s98 offen sc1\n"
"v_mfma_f32_16x16x32_bf16 v[28:31], a[176:179], v[160:163], v[28:31]\n"
"v_mfma_f32_16x16x32_bf16 v[28:31], a[172:175], v[44:47], v[28:31]\n"
"v_cmp_u_f32_e32 vcc, v24, v24\n"
"v_mfma_f32_16x16x32_bf16 v[28:31], a[168:171], v[136:139], v[28:31]\n"
"s_cbranch_vccnz .Lmk_p69s\n"
".Lmk_p69r:\n"
"v_mfma_f32_16x16x32_bf16 v[28:31], a[164:167], v[56:59], v[28:31]\n"
"ds_write_b128 v3, v[24:27] offset:6144\n"
"v_mfma_f32_16x16x32_bf16 v[28:31], a[160:163], v[48:51], v[28:31]\n"
"buffer_load_dwordx4 a[100:103], v0, s[8:11], s98 offen offset:1024 sc1\n"
"v_mfma_f32_16x16x32_bf16 v[24:27], a[192:195], v[48:51], 0\n"
"v_mfma_f32_16x16x32_bf16 v[24:27], a[196:199], v[56:59], v[24:27]\n"
"v_mfma_f32_16x16x32_bf16 v[24:27], a[200:203], v[136:139], v[24:27]\n"
"buffer_load_dwordx4 a[104:107], v0, s[8:11], s98 offen offset:2048 sc1\n"
"v_mfma_f32_16x16x32_bf16 v[24:27], a[204:207], v[44:47], v[24:27]\n"
"v_mfma_f32_16x16x32_bf16 v[24:27], a[208:211], v[160:163], v[24:27]\n"
"ds_read_b128 v[32:35], v2\n"
"ds_read_b128 v[40:43], v2 offset:1024\n"
"v_mfma_f32_16x16x32_bf16 v[24:27], a[212:215], v[128:131], v[24:27]\n"
"ds_write_b128 v3, v[28:31] offset:7168\n"
"v_mfma_f32_16x16x32_bf16 v[24:27], a[216:219], v[72:75], v[24:27]\n"
"buffer_load_dwordx4 a[108:111], v0, s[8:11], s98 offen offset:3072 sc1\n"
"v_mfma_f32_16x16x32_bf16 v[24:27], a[220:223], v[60:63], v[24:27]\n"
"v_mfma_f32_16x16x32_bf16 v[28:31], a[252:255], v[60:63], 0\n"
"v_mfma_f32_16x16x32_bf16 v[28:31], a[248:251], v[72:75], v[28:31]\n"
"buffer_load_dwordx4 a[112:115], v1, s[8:11], s98 offen sc1\n"
"s_waitcnt lgkmcnt(1)\n"
"v_mfma_f32_16x16x32_bf16 v[28:31], a[244:247], v[128:131], v[28:31]\n"
"v_lshlrev_b32_e32 v255, 16, v32\n"
"v_and_b32_e32 v254, 0xffff0000, v32\n"
"v_lshlrev_b32_e32 v253, 16, v33\n"
"v_mfma_f32_16x16x32_bf16 v[28:31], a[240:243], v[160:163], v[28:31]\n"
"v_and_b32_e32 v252, 0xffff0000, v33\n"
"v_lshlrev_b32_e32 v251, 16, v34\n"
"v_and_b32_e32 v250, 0xffff0000, v34\n"
"v_mfma_f32_16x16x32_bf16 v[28:31], a[236:239], v[44:47], v[28:31]\n"
"buffer_load_dwordx4 a[116:119], v1, s[8:11], s98 offen offset:1024 sc1\n"
"v_lshlrev_b32_e32 v249, 16, v35\n"
"v_mfma_f32_16x16x32_bf16 v[28:31], a[232:235], v[136:139], v[28:31]\n"
"ds_write_b128 v3, v[24:27] offset:8192\n"
"v_mfma_f32_16x16x32_bf16 v[28:31], a[228:231], v[56:59], v[28:31]\n"
"v_and_b32_e32 v248, 0xffff0000, v35\n"
"v_lshlrev_b32_e32 v247, 16, v40\n"
"v_and_b32_e32 v246, 0xffff0000, v40\n"
"v_mfma_f32_16x16x32_bf16 v[28:31], a[224:227], v[48:51], v[28:31]\n"
"s_waitcnt vmcnt(31)\n"
"buffer_load_dwordx4 a[120:123], v1, s[8:11], s98 offen offset:2048 sc1\n"
"; @B G2 a.2\n"
"v_mfma_f32_16x16x32_bf16 v[24:27], a[128:131], v[36:39], 0\n"
"v_lshlrev_b32_e32 v245, 16, v41\n"
"v_and_b32_e32 v244, 0xffff0000, v41\n"
"v_lshlrev_b32_e32 v243, 16, v42\n"
"v_mfma_f32_16x16x32_bf16 v[24:27], a[132:135], v[68:71], v[24:27]\n"
"v_and_b32_e32 v242, 0xffff0000, v42\n"
"v_lshlrev_b32_e32 v241, 16, v43\n"
"v_and_b32_e32 v240, 0xffff0000, v43\n"
"v_mfma_f32_16x16x32_bf16 v[24:27], a[136:139], v[76:79], v[24:27]\n"
"buffer_load_dwordx4 a[124:127], v1, s[8:11], s98 offen offset:3072 sc1\n"
"v_mfma_f32_16x16x32_bf16 v[24:27], a[140:143], v[148:151], v[24:27]\n"
"s_waitcnt vmcnt(29)\n"
"s_add_u32 s98, s69, 0x8000\n"
"v_mfma_f32_16x16x32_bf16 v[24:27], a[144:147], v[152:155], v[24:27]\n"
"v_mfma_f32_16x16x32_bf16 v[24:27], a[148:151], v[172:175], v[24:27]\n"
"ds_write_b128 v3, v[28:31] offset:9216\n"
"v_mfma_f32_16x16x32_bf16 v[24:27], a[152:155], v[176:179], v[24:27]\n"
"buffer_load_dwordx4 v[28:31], v0, s[8:11], s98 offen sc1\n"
"v_mfma_f32_16x16x32_bf16 v[24:27], a[156:159], v[64:67], v[24:27]\n"
"v_mfma_f32_16x16x32_bf16 v[32:35], a[188:191], v[64:67], 0\n"
"v_mfma_f32_16x16x32_bf16 v[32:35], a[184:187], v[176:179], v[32:35]\n"
"s_waitcnt lgkmcnt(0)\n"
"s_barrier\n"
"v_mfma_f32_16x16x32_bf16 v[32:35], a[180:183], v[172:175], v[32:35]\n"
"buffer_load_dwordx4 v[40:43], v0, s[8:11], s98 offen offset:1024 sc1\n"
"v_mfma_f32_16x16x32_bf16 v[32:35], a[176:179], v[152:155], v[32:35]\n"
"ds_read_b128 v[44:47], v4\n"
"ds_read_b128 v[48:51], v5\n"
"v_mfma_f32_16x16x32_bf16 v[32:35], a[172:175], v[148:151], v[32:35]\n"
"v_cmp_u_f32_e32 vcc, v24, v24\n"
"v_mfma_f32_16x16x32_bf16 v[32:35], a[168:171], v[76:79], v[32:35]\n"
"s_cbranch_vccnz .Lmk_p70s\n"
".Lmk_p70r:\n"
"v_mfma_f32_16x16x32_bf16 v[32:35], a[164:167], v[68:71], v[32:35]\n"
"ds_write_b128 v3, v[24:27] offset:12288\n"
"v_mfma_f32_16x16x32_bf16 v[32:35], a[160:163], v[36:39], v[32:35]\n"
"buffer_load_dwordx4 v[24:27], v0, s[8:11], s98 offen offset:2048 sc1\n"
"v_mfma_f32_16x16x32_bf16 v[52:55], a[192:195], v[36:39], 0\n"
"ds_read_b128 v[56:59], v4 offset:1024\n"
"ds_read_b128 v[60:63], v5 offset:1024\n"
"s_waitcnt lgkmcnt(3)\n"
"v_mfma_f32_16x16x32_bf16 v[52:55], a[196:199], v[68:71], v[52:55]\n"
"v_add_f32_e32 v44, v44, v255\n"
"v_add_f32_e32 v45, v45, v254\n"
"v_add_f32_e32 v46, v46, v253\n"
"v_mfma_f32_16x16x32_bf16 v[52:55], a[200:203], v[76:79], v[52:55]\n"
"buffer_load_dwordx4 v[72:75], v0, s[8:11], s98 offen offset:3072 sc1\n"
"v_add_f32_e32 v47, v47, v252\n"
"v_mfma_f32_16x16x32_bf16 v[52:55], a[204:207], v[148:151], v[52:55]\n"
"v_add_f32_e32 v44, v44, v48\n"
"v_add_f32_e32 v45, v45, v49\n"
"v_add_f32_e32 v46, v46, v50\n"
"v_mfma_f32_16x16x32_bf16 v[52:55], a[208:211], v[152:155], v[52:55]\n"
"v_add_f32_e32 v47, v47, v51\n"
"v_mfma_f32_16x16x32_bf16 v[52:55], a[212:215], v[172:175], v[52:55]\n"
"ds_write_b128 v3, v[32:35] offset:13312\n"
"v_mfma_f32_16x16x32_bf16 v[52:55], a[216:219], v[176:179], v[52:55]\n"
"buffer_load_dwordx4 v[32:35], v1, s[8:11], s98 offen sc1\n"
"s_waitcnt lgkmcnt(1)\n"
"v_mfma_f32_16x16x32_bf16 v[52:55], a[220:223], v[64:67], v[52:55]\n"
"ds_read_b128 v[48:51], v4 offset:2048\n"
"ds_read_b128 v[108:111], v5 offset:2048\n"
"v_add_f32_e32 v56, v56, v251\n"
"v_mfma_f32_16x16x32_bf16 v[112:115], a[252:255], v[64:67], 0\n"
"v_add_f32_e32 v57, v57, v250\n"
"v_add_f32_e32 v58, v58, v249\n"
"v_add_f32_e32 v59, v59, v248\n"
"v_mfma_f32_16x16x32_bf16 v[112:115], a[248:251], v[176:179], v[112:115]\n"
"buffer_load_dwordx4 v[64:67], v1, s[8:11], s98 offen offset:1024 sc1\n"
"v_add_f32_e32 v56, v56, v60\n"
"v_mfma_f32_16x16x32_bf16 v[112:115], a[244:247], v[172:175], v[112:115]\n"
"v_add_f32_e32 v57, v57, v61\n"
"v_add_f32_e32 v58, v58, v62\n"
"v_add_f32_e32 v59, v59, v63\n"
"v_mfma_f32_16x16x32_bf16 v[112:115], a[240:243], v[152:155], v[112:115]\n"
"v_mfma_f32_16x16x32_bf16 v[112:115], a[236:239], v[148:151], v[112:115]\n"
"buffer_load_dwordx4 v[60:63], v1, s[8:11], s98 offen offset:2048 sc1\n"
"s_waitcnt lgkmcnt(0)\n"
"v_mfma_f32_16x16x32_bf16 v[112:115], a[232:235], v[76:79], v[112:115]\n"
"ds_write_b128 v3, v[52:55] offset:14336\n"
"v_mfma_f32_16x16x32_bf16 v[112:115], a[228:231], v[68:71], v[112:115]\n"
"ds_read_b128 v[52:55], v4 offset:3072\n"
"ds_read_b128 v[68:71], v5 offset:3072\n"
"v_add_f32_e32 v48, v48, v247\n"
"v_mfma_f32_16x16x32_bf16 v[112:115], a[224:227], v[36:39], v[112:115]\n"
"s_waitcnt vmcnt(30)\n"
"buffer_load_dwordx4 v[36:39], v1, s[8:11], s98 offen offset:3072 sc1\n"
"; @B G2 a.3\n"
"v_mfma_f32_16x16x32_bf16 v[76:79], a[128:131], v[80:83], v[44:47]\n"
"s_add_u32 s98, s69, 0xa000\n"
"v_add_f32_e32 v49, v49, v246\n"
"v_add_f32_e32 v50, v50, v245\n"
"v_add_f32_e32 v51, v51, v244\n"
"v_mfma_f32_16x16x32_bf16 v[76:79], a[132:135], v[84:87], v[76:79]\n"
"v_add_f32_e32 v48, v48, v108\n"
"v_add_f32_e32 v49, v49, v109\n"
"v_add_f32_e32 v50, v50, v110\n"
"v_mfma_f32_16x16x32_bf16 v[76:79], a[136:139], v[88:91], v[76:79]\n"
"buffer_load_dwordx4 v[120:123], v0, s[8:11], s98 offen sc1\n"
"v_add_f32_e32 v51, v51, v111\n"
"v_mfma_f32_16x16x32_bf16 v[76:79], a[140:143], v[92:95], v[76:79]\n"
"s_waitcnt vmcnt(28)\n"
"s_waitcnt lgkmcnt(0)\n"
"v_add_f32_e32 v52, v52, v243\n"
"v_add_f32_e32 v53, v53, v242\n"
"v_mfma_f32_16x16x32_bf16 v[76:79], a[144:147], v[96:99], v[76:79]\n"
"v_add_f32_e32 v54, v54, v241\n"
"v_add_f32_e32 v55, v55, v240\n"
"v_add_f32_e32 v52, v52, v68\n"
"v_mfma_f32_16x16x32_bf16 v[76:79], a[148:151], v[100:103], v[76:79]\n"
"ds_write_b128 v3, v[112:115] offset:15360\n"
"v_mfma_f32_16x16x32_bf16 v[76:79], a[152:155], v[104:107], v[76:79]\n"
"buffer_load_dwordx4 v[108:111], v0, s[8:11], s98 offen offset:1024 sc1\n"
"v_add_f32_e32 v53, v53, v69\n"
"v_mfma_f32_16x16x32_bf16 v[76:79], a[156:159], v[116:119], v[76:79]\n"
"v_add_f32_e32 v54, v54, v70\n"
"v_add_f32_e32 v55, v55, v71\n"
"v_mfma_f32_16x16x32_bf16 v[68:71], a[188:191], v[116:119], v[56:59]\n"
"v_mfma_f32_16x16x32_bf16 v[68:71], a[184:187], v[104:107], v[68:71]\n"
"s_waitcnt lgkmcnt(0)\n"
"s_barrier\n"
"v_mfma_f32_16x16x32_bf16 v[68:71], a[180:183], v[100:103], v[68:71]\n"
"buffer_load_dwordx4 v[112:115], v0, s[8:11], s98 offen offset:2048 sc1\n"
"v_mfma_f32_16x16x32_bf16 v[68:71], a[176:179], v[96:99], v[68:71]\n"
"ds_read_b128 v[124:127], v6\n"
"ds_read_b128 v[128:131], v6 offset:1024\n"
"v_mfma_f32_16x16x32_bf16 v[68:71], a[172:175], v[92:95], v[68:71]\n"
"ds_read_b128 v[132:135], v6 offset:2048\n"
"v_cmp_u_f32_e32 vcc, v76, v76\n"
"v_mfma_f32_16x16x32_bf16 v[68:71], a[168:171], v[88:91], v[68:71]\n"
"s_cbranch_vccnz .Lmk_p71s\n"
".Lmk_p71r:\n"
"ds_read_b128 v[44:47], v6 offset:3072\n"
"v_mfma_f32_16x16x32_bf16 v[68:71], a[164:167], v[84:87], v[68:71]\n"
"buffer_load_dwordx4 v[56:59], v0, s[8:11], s98 offen offset:3072 sc1\n"
"v_mfma_f32_16x16x32_bf16 v[68:71], a[160:163], v[80:83], v[68:71]\n"
"v_mfma_f32_16x16x32_bf16 v[48:51], a[192:195], v[80:83], v[48:51]\n"
"v_mfma_f32_16x16x32_bf16 v[48:51], a[196:199], v[84:87], v[48:51]\n"
"buffer_load_dwordx4 v[136:139], v1, s[8:11], s98 offen sc1\n"
"v_mfma_f32_16x16x32_bf16 v[48:51], a[200:203], v[88:91], v[48:51]\n"
"v_mfma_f32_16x16x32_bf16 v[48:51], a[204:207], v[92:95], v[48:51]\n"
"v_mfma_f32_16x16x32_bf16 v[48:51], a[208:211], v[96:99], v[48:51]\n"
"buffer_load_dwordx4 v[140:143], v1, s[8:11], s98 offen offset:1024 sc1\n"
"s_waitcnt lgkmcnt(0)\n"
"v_mfma_f32_16x16x32_bf16 v[48:51], a[212:215], v[100:103], v[48:51]\n"
"v_add_f32_e32 v124, v76, v124\n"
"v_add_f32_e32 v125, v77, v125\n"
"v_add_f32_e32 v126, v78, v126\n"
"v_mfma_f32_16x16x32_bf16 v[48:51], a[216:219], v[104:107], v[48:51]\n"
"v_add_f32_e32 v127, v79, v127\n"
"v_add_f32_e32 v128, v68, v128\n"
"v_add_f32_e32 v129, v69, v129\n"
"v_mfma_f32_16x16x32_bf16 v[48:51], a[220:223], v[116:119], v[48:51]\n"
"buffer_load_dwordx4 v[76:79], v1, s[8:11], s98 offen offset:2048 sc1\n"
"v_add_f32_e32 v130, v70, v130\n"
"v_mfma_f32_16x16x32_bf16 v[52:55], a[252:255], v[116:119], v[52:55]\n"
"v_add_f32_e32 v131, v71, v131\n"
"v_cvt_pk_bf16_f32 v68, v124, v125\n"
"v_cvt_pk_bf16_f32 v69, v126, v127\n"
"v_mfma_f32_16x16x32_bf16 v[52:55], a[248:251], v[104:107], v[52:55]\n"
"s_add_u32 s97, s61, 0x800000\n"
"v_cvt_pk_bf16_f32 v70, v128, v129\n"
"v_cvt_pk_bf16_f32 v71, v130, v131\n"
"v_mfma_f32_16x16x32_bf16 v[52:55], a[244:247], v[100:103], v[52:55]\n"
"buffer_load_dwordx4 v[100:103], v1, s[8:11], s98 offen offset:3072 sc1\n"
"v_mfma_f32_16x16x32_bf16 v[52:55], a[240:243], v[96:99], v[52:55]\n"
"s_add_u32 s98, s52, 0x840000\n"
"buffer_load_dwordx4 v[96:99], v0, s[8:11], s98 offen sc1\n"
"v_mfma_f32_16x16x32_bf16 v[52:55], a[236:239], v[92:95], v[52:55]\n"
"buffer_store_dwordx4 v[68:71], v0, s[4:7], s97 offen sc1\n"
"v_mfma_f32_16x16x32_bf16 v[52:55], a[232:235], v[88:91], v[52:55]\n"
"buffer_load_dwordx4 v[88:91], v0, s[8:11], s98 offen offset:1024 sc1\n"
"v_mfma_f32_16x16x32_bf16 v[52:55], a[228:231], v[84:87], v[52:55]\n"
"ds_write_b128 v2, v[68:71]\n"
"v_mfma_f32_16x16x32_bf16 v[52:55], a[224:227], v[80:83], v[52:55]\n"
"buffer_load_dwordx4 v[80:83], v0, s[8:11], s98 offen offset:2048 sc1\n"
"buffer_load_dwordx4 v[84:87], v0, s[8:11], s98 offen offset:3072 sc1\n"
"buffer_load_dwordx4 v[92:95], v1, s[8:11], s98 offen sc1\n"
"buffer_load_dwordx4 v[104:107], v1, s[8:11], s98 offen offset:1024 sc1\n"
"buffer_load_dwordx4 v[116:119], v1, s[8:11], s98 offen offset:2048 sc1\n"
"buffer_load_dwordx4 v[124:127], v1, s[8:11], s98 offen offset:3072 sc1\n"
"s_add_u32 s98, s53, 0x840000\n"
"buffer_load_dwordx4 v[128:131], v0, s[8:11], s98 offen sc1\n"
"buffer_load_dwordx4 v[144:147], v0, s[8:11], s98 offen offset:1024 sc1\n"
"buffer_load_dwordx4 v[148:151], v0, s[8:11], s98 offen offset:2048 sc1\n"
"buffer_load_dwordx4 v[152:155], v0, s[8:11], s98 offen offset:3072 sc1\n"
"buffer_load_dwordx4 v[156:159], v1, s[8:11], s98 offen sc1\n"
"buffer_load_dwordx4 v[160:163], v1, s[8:11], s98 offen offset:1024 sc1\n"
"buffer_load_dwordx4 v[164:167], v1, s[8:11], s98 offen offset:2048 sc1\n"
"buffer_load_dwordx4 v[168:171], v1, s[8:11], s98 offen offset:3072 sc1\n"
"s_lshr_b32 s98, s79, 7\n"
"s_add_u32 s52, s52, 0x800000\n"
"s_add_u32 s53, s53, 0x800000\n"
"s_add_u32 s54, s54, 0x800000\n"
"s_add_u32 s55, s55, 0x800000\n"
"s_add_u32 s56, s56, 0x800000\n"
"s_add_u32 s57, s57, 0x800000\n"
"s_add_u32 s58, s58, 0x800000\n"
"s_add_u32 s59, s59, 0x800000\n"
"s_add_u32 s60, s60, 0x800000\n"
"s_add_u32 s61, s61, 0x800000\n"
"s_add_u32 s62, s62, 0x80000\n"
"s_add_u32 s63, s63, 0x80000\n"
"s_add_u32 s64, s64, 0x1000000\n"
"s_add_u32 s65, s65, s79\n"
"s_add_u32 s66, s66, s79\n"
"s_add_u32 s67, s67, 0x20000\n"
"s_add_u32 s68, s68, 0x20000\n"
"s_add_u32 s71, s71, s98\n"
"s_add_u32 s73, s73, s98\n"
"s_mov_b32 s0, 1\n"
"s_mov_b32 s10, 0\n"
".Lmk_a0:\n"
"s_waitcnt vmcnt(12)\n"
"; @A G1 0.0\n"
"v_mfma_f32_16x16x32_bf16 v[172:175], a[0:3], v[96:99], 0\n"
"v_mfma_f32_16x16x32_bf16 v[172:175], a[4:7], v[88:91], v[172:175]\n"
"s_add_u32 s98, s62, 0x4000\n"
"v_mfma_f32_16x16x32_bf16 v[172:175], a[8:11], v[80:83], v[172:175]\n"
"s_add_u32 s97, s54, 0x40000\n"
"buffer_load_dwordx4 v[176:179], v0, s[4:7], s97 offen sc1\n"
"v_mfma_f32_16x16x32_bf16 v[172:175], a[12:15], v[84:87], v[172:175]\n"
"s_waitcnt vmcnt(9)\n"
"v_mfma_f32_16x16x32_bf16 v[172:175], a[16:19], v[92:95], v[172:175]\n"
"buffer_load_dword v255, v10, s[4:7], s98 offen sc1\n"
"v_mfma_f32_16x16x32_bf16 v[172:175], a[20:23], v[104:107], v[172:175]\n"
"v_add_f32_e32 v132, v48, v132\n"
"v_add_f32_e32 v133, v49, v133\n"
"v_mfma_f32_16x16x32_bf16 v[172:175], a[24:27], v[116:119], v[172:175]\n"
"v_add_f32_e32 v134, v50, v134\n"
"v_add_f32_e32 v135, v51, v135\n"
"v_mfma_f32_16x16x32_bf16 v[172:175], a[28:31], v[124:127], v[172:175]\n"
"buffer_load_dwordx4 v[48:51], v0, s[4:7], s97 offen offset:1024 sc1\n"
"v_mfma_f32_16x16x32_bf16 v[180:183], a[60:63], v[124:127], 0\n"
"buffer_load_dword v254, v10, s[4:7], s98 offen offset:256 sc1\n"
"v_mfma_f32_16x16x32_bf16 v[180:183], a[56:59], v[116:119], v[180:183]\n"
"v_add_f32_e32 v44, v52, v44\n"
"v_add_f32_e32 v45, v53, v45\n"
"v_mfma_f32_16x16x32_bf16 v[180:183], a[52:55], v[104:107], v[180:183]\n"
"v_add_f32_e32 v46, v54, v46\n"
"v_add_f32_e32 v47, v55, v47\n"
"v_mfma_f32_16x16x32_bf16 v[180:183], a[48:51], v[92:95], v[180:183]\n"
"buffer_load_dword v253, v10, s[4:7], s98 offen offset:512 sc1\n"
"v_mfma_f32_16x16x32_bf16 v[180:183], a[44:47], v[84:87], v[180:183]\n"
"v_cmp_u_f32_e32 vcc, v172, v172\n"
"v_cvt_pk_bf16_f32 v52, v132, v133\n"
"v_mfma_f32_16x16x32_bf16 v[180:183], a[40:43], v[80:83], v[180:183]\n"
"s_cbranch_vccnz .Lmk_p76s\n"
".Lmk_p76r:\n"
"v_cvt_pk_bf16_f32 v53, v134, v135\n"
"v_mfma_f32_16x16x32_bf16 v[180:183], a[36:39], v[88:91], v[180:183]\n"
"buffer_load_dwordx4 v[132:135], v0, s[4:7], s97 offen offset:2048 sc1\n"
"v_mfma_f32_16x16x32_bf16 v[180:183], a[32:35], v[96:99], v[180:183]\n"
"ds_write_b128 v3, v[172:175]\n"
"v_mfma_f32_16x16x32_bf16 v[172:175], a[64:67], v[96:99], 0\n"
"buffer_load_dword v252, v10, s[4:7], s98 offen offset:768 sc1\n"
"v_mfma_f32_16x16x32_bf16 v[172:175], a[68:71], v[88:91], v[172:175]\n"
"buffer_load_dwordx4 v[184:187], v0, s[4:7], s97 offen offset:3072 sc1\n"
"v_mfma_f32_16x16x32_bf16 v[172:175], a[72:75], v[80:83], v[172:175]\n"
"v_cvt_pk_bf16_f32 v54, v44, v45\n"
"v_cvt_pk_bf16_f32 v55, v46, v47\n"
"v_mfma_f32_16x16x32_bf16 v[172:175], a[76:79], v[84:87], v[172:175]\n"
"buffer_load_dword v251, v10, s[4:7], s98 offen offset:1024 sc1\n"
"v_mfma_f32_16x16x32_bf16 v[172:175], a[80:83], v[92:95], v[172:175]\n"
"buffer_store_dwordx4 v[52:55], v0, s[4:7], s61 offen offset:1024 sc1\n"
"v_mfma_f32_16x16x32_bf16 v[172:175], a[84:87], v[104:107], v[172:175]\n"
"ds_write_b128 v3, v[180:183] offset:1024\n"
"v_mfma_f32_16x16x32_bf16 v[172:175], a[88:91], v[116:119], v[172:175]\n"
"buffer_load_dwordx4 v[44:47], v1, s[4:7], s97 offen sc1\n"
"v_mfma_f32_16x16x32_bf16 v[172:175], a[92:95], v[124:127], v[172:175]\n"
"buffer_load_dword v250, v10, s[4:7], s98 offen offset:1280 sc1\n"
"v_mfma_f32_16x16x32_bf16 v[180:183], a[124:127], v[124:127], 0\n"
"ds_write_b128 v2, v[52:55] offset:1024\n"
"v_mfma_f32_16x16x32_bf16 v[180:183], a[120:123], v[116:119], v[180:183]\n"
"v_mov_b32_e32 v188, 0x3f803f80\n"
"v_mov_b32_e32 v189, 0x3f803f80\n"
"v_mfma_f32_16x16x32_bf16 v[180:183], a[116:119], v[104:107], v[180:183]\n"
"buffer_load_dword v249, v10, s[4:7], s98 offen offset:1536 sc1\n"
"v_mfma_f32_16x16x32_bf16 v[180:183], a[112:115], v[92:95], v[180:183]\n"
"buffer_load_dwordx4 v[192:195], v1, s[4:7], s97 offen offset:1024 sc1\n"
"v_mfma_f32_16x16x32_bf16 v[180:183], a[108:111], v[84:87], v[180:183]\n"
"v_mov_b32_e32 v190, 0x3f803f80\n"
"v_mov_b32_e32 v191, 0x3f803f80\n"
"v_mfma_f32_16x16x32_bf16 v[180:183], a[104:107], v[80:83], v[180:183]\n"
"ds_write_b128 v3, v[172:175] offset:2048\n"
"v_mfma_f32_16x16x32_bf16 v[172:175], v[188:191], v[68:71], 0\n"
"v_mfma_f32_16x16x32_bf16 v[172:175], v[188:191], v[52:55], v[172:175]\n"
"v_mfma_f32_16x16x32_bf16 v[188:191], v[68:71], v[68:71], 0\n"
"v_mfma_f32_16x16x32_bf16 v[188:191], v[52:55], v[52:55], v[188:191]\n"
"v_mfma_f32_16x16x32_bf16 v[180:183], a[100:103], v[88:91], v[180:183]\n"
"v_mov_b32_e32 v52, -1\n"
"v_mov_b32_e32 v53, -1\n"
"v_mfma_f32_16x16x32_bf16 v[180:183], a[96:99], v[96:99], v[180:183]\n"
"v_mov_b32_e32 v54, -1\n"
"v_mov_b32_e32 v55, -1\n"
"v_mfma_f32_16x16x32_bf16 v[68:71], v[28:31], v[96:99], 0\n"
"buffer_load_dwordx4 v[196:199], v1, s[4:7], s97 offen offset:2048 sc1\n"
"v_mfma_f32_16x16x32_bf16 v[68:71], v[40:43], v[88:91], v[68:71]\n"
"s_add_u32 s96, s64, 0x4000\n"
"buffer_store_dwordx4 v[52:55], v2, s[4:7], s96 offen sc1\n"
"v_mfma_f32_16x16x32_bf16 v[68:71], v[24:27], v[80:83], v[68:71]\n"
"buffer_load_dword v248, v10, s[4:7], s98 offen offset:1792 sc1\n"
"v_mfma_f32_16x16x32_bf16 v[68:71], v[72:75], v[84:87], v[68:71]\n"
"s_mov_b32 s94, 0xaaaaaaaa\n"
"s_mov_b32 s95, 0xaaaaaaaa\n"
"v_mfma_f32_16x16x32_bf16 v[68:71], v[32:35], v[92:95], v[68:71]\n"
"v_mfma_f32_16x16x32_bf16 v[68:71], v[64:67], v[104:107], v[68:71]\n"
"buffer_load_dwordx4 v[200:203], v1, s[4:7], s97 offen offset:3072 sc1\n"
"v_mfma_f32_16x16x32_bf16 v[68:71], v[60:63], v[116:119], v[68:71]\n"
"ds_write_b128 v3, v[180:183] offset:3072\n"
"v_mfma_f32_16x16x32_bf16 v[68:71], v[36:39], v[124:127], v[68:71]\n"
"v_cndmask_b32_e64 v188, v188, v189, s[94:95]\n"
"v_cndmask_b32_e64 v190, v190, v191, s[94:95]\n"
"v_mfma_f32_16x16x32_bf16 v[180:183], v[100:103], v[124:127], 0\n"
"s_mov_b32 s94, 0xcccccccc\n"
"s_mov_b32 s95, 0xcccccccc\n"
"v_mfma_f32_16x16x32_bf16 v[180:183], v[76:79], v[116:119], v[180:183]\n"
"v_cndmask_b32_e64 v173, v188, v190, s[94:95]\n"
"v_mfma_f32_16x16x32_bf16 v[180:183], v[140:143], v[104:107], v[180:183]\n"
"s_mov_b32 s94, 0xf0000f\n"
"s_mov_b32 s95, 0xf0000f00\n"
"v_mfma_f32_16x16x32_bf16 v[180:183], v[136:139], v[92:95], v[180:183]\n"
"v_cndmask_b32_e64 v172, v172, v173, s[94:95]\n"
"v_mfma_f32_16x16x32_bf16 v[180:183], v[56:59], v[84:87], v[180:183]\n"
"buffer_store_dword v172, v14, s[4:7], s63 offen sc1\n"
"v_mfma_f32_16x16x32_bf16 v[180:183], v[112:115], v[80:83], v[180:183]\n"
"ds_write_b128 v3, v[68:71] offset:4096\n"
"v_mfma_f32_16x16x32_bf16 v[180:183], v[108:111], v[88:91], v[180:183]\n"
"v_mfma_f32_16x16x32_bf16 v[180:183], v[120:123], v[96:99], v[180:183]\n"
"s_waitcnt vmcnt(23)\n"
"; @A G1 0.1\n"
"v_mfma_f32_16x16x32_bf16 v[68:71], a[0:3], v[128:131], 0\n"
"v_mfma_f32_16x16x32_bf16 v[68:71], a[4:7], v[144:147], v[68:71]\n"
"v_mfma_f32_16x16x32_bf16 v[68:71], a[8:11], v[148:151], v[68:71]\n"
"s_add_u32 s98, s55, 0x40000\n"
"buffer_load_dwordx4 v[80:83], v0, s[4:7], s98 offen sc1\n"
"v_mfma_f32_16x16x32_bf16 v[68:71], a[12:15], v[152:155], v[68:71]\n"
"s_waitcnt vmcnt(20)\n"
"v_mfma_f32_16x16x32_bf16 v[68:71], a[16:19], v[156:159], v[68:71]\n"
"v_mfma_f32_16x16x32_bf16 v[68:71], a[20:23], v[160:163], v[68:71]\n"
"ds_write_b128 v3, v[180:183] offset:5120\n"
"v_mfma_f32_16x16x32_bf16 v[68:71], a[24:27], v[164:167], v[68:71]\n"
"v_mfma_f32_16x16x32_bf16 v[68:71], a[28:31], v[168:171], v[68:71]\n"
"buffer_load_dwordx4 v[84:87], v0, s[4:7], s98 offen offset:1024 sc1\n"
"v_mfma_f32_16x16x32_bf16 v[88:91], a[60:63], v[168:171], 0\n"
"v_mfma_f32_16x16x32_bf16 v[88:91], a[56:59], v[164:167], v[88:91]\n"
"v_mfma_f32_16x16x32_bf16 v[88:91], a[52:55], v[160:163], v[88:91]\n"
"v_mfma_f32_16x16x32_bf16 v[88:91], a[48:51], v[156:159], v[88:91]\n"
"v_mfma_f32_16x16x32_bf16 v[88:91], a[44:47], v[152:155], v[88:91]\n"
"s_nop 1\n"
"v_cmp_u_f32_e32 vcc, v68, v68\n"
"v_mfma_f32_16x16x32_bf16 v[88:91], a[40:43], v[148:151], v[88:91]\n"
"s_cbranch_vccnz .Lmk_p77s\n"
".Lmk_p77r:\n"
"v_mfma_f32_16x16x32_bf16 v[88:91], a[36:39], v[144:147], v[88:91]\n"
"buffer_load_dwordx4 v[92:95], v0, s[4:7], s98 offen offset:2048 sc1\n"
"v_mfma_f32_16x16x32_bf16 v[88:91], a[32:35], v[128:131], v[88:91]\n"
"ds_write_b128 v3, v[68:71] offset:6144\n"
"v_mfma_f32_16x16x32_bf16 v[68:71], a[64:67], v[128:131], 0\n"
"v_mfma_f32_16x16x32_bf16 v[68:71], a[68:71], v[144:147], v[68:71]\n"
"buffer_load_dwordx4 v[96:99], v0, s[4:7], s98 offen offset:3072 sc1\n"
"v_mfma_f32_16x16x32_bf16 v[68:71], a[72:75], v[148:151], v[68:71]\n"
"v_mfma_f32_16x16x32_bf16 v[68:71], a[76:79], v[152:155], v[68:71]\n"
"v_mfma_f32_16x16x32_bf16 v[68:71], a[80:83], v[156:159], v[68:71]\n"
"v_mfma_f32_16x16x32_bf16 v[68:71], a[84:87], v[160:163], v[68:71]\n"
"ds_write_b128 v3, v[88:91] offset:7168\n"
"v_mfma_f32_16x16x32_bf16 v[68:71], a[88:91], v[164:167], v[68:71]\n"
"buffer_load_dwordx4 v[88:91], v1, s[4:7], s98 offen sc1\n"
"v_mfma_f32_16x16x32_bf16 v[68:71], a[92:95], v[168:171], v[68:71]\n"
"v_mfma_f32_16x16x32_bf16 v[104:107], a[124:127], v[168:171], 0\n"
"v_mfma_f32_16x16x32_bf16 v[104:107], a[120:123], v[164:167], v[104:107]\n"
"v_mfma_f32_16x16x32_bf16 v[104:107], a[116:119], v[160:163], v[104:107]\n"
"v_mfma_f32_16x16x32_bf16 v[104:107], a[112:115], v[156:159], v[104:107]\n"
"buffer_load_dwordx4 v[116:119], v1, s[4:7], s98 offen offset:1024 sc1\n"
"v_mfma_f32_16x16x32_bf16 v[104:107], a[108:111], v[152:155], v[104:107]\n"
"v_mfma_f32_16x16x32_bf16 v[104:107], a[104:107], v[148:151], v[104:107]\n"
"s_nop 0\n"
"ds_write_b128 v3, v[68:71] offset:8192\n"
"v_mfma_f32_16x16x32_bf16 v[104:107], a[100:103], v[144:147], v[104:107]\n"
"v_mfma_f32_16x16x32_bf16 v[104:107], a[96:99], v[128:131], v[104:107]\n"
"v_mfma_f32_16x16x32_bf16 v[68:71], v[28:31], v[128:131], 0\n"
"buffer_load_dwordx4 v[124:127], v1, s[4:7], s98 offen offset:2048 sc1\n"
"v_mfma_f32_16x16x32_bf16 v[68:71], v[40:43], v[144:147], v[68:71]\n"
"s_add_u32 s97, s64, 0x4000\n"
"v_mfma_f32_16x16x32_bf16 v[68:71], v[24:27], v[148:151], v[68:71]\n"
"v_mfma_f32_16x16x32_bf16 v[68:71], v[72:75], v[152:155], v[68:71]\n"
"buffer_store_dwordx4 v[52:55], v2, s[4:7], s97 offen offset:1024 sc1\n"
"v_mfma_f32_16x16x32_bf16 v[68:71], v[32:35], v[156:159], v[68:71]\n"
"s_waitcnt vmcnt(10)\n"
"v_add_f32_e32 v255, v255, v251\n"
"v_add_f32_e32 v254, v254, v250\n"
"v_mfma_f32_16x16x32_bf16 v[68:71], v[64:67], v[160:163], v[68:71]\n"
"buffer_load_dwordx4 v[172:175], v1, s[4:7], s98 offen offset:3072 sc1\n"
"v_mfma_f32_16x16x32_bf16 v[68:71], v[60:63], v[164:167], v[68:71]\n"
"ds_write_b128 v3, v[104:107] offset:9216\n"
"v_mfma_f32_16x16x32_bf16 v[68:71], v[36:39], v[168:171], v[68:71]\n"
"v_add_f32_e32 v253, v253, v249\n"
"v_add_f32_e32 v252, v252, v248\n"
"v_mfma_f32_16x16x32_bf16 v[104:107], v[100:103], v[168:171], 0\n"
"v_add_f32_e32 v255, v255, v253\n"
"v_add_f32_e32 v254, v254, v252\n"
"v_mfma_f32_16x16x32_bf16 v[104:107], v[76:79], v[164:167], v[104:107]\n"
"v_add_f32_e32 v255, v255, v254\n"
"v_cmp_u_f32_e32 vcc, v255, v255\n"
"v_mfma_f32_16x16x32_bf16 v[104:107], v[140:143], v[160:163], v[104:107]\n"
"s_cbranch_vccnz .Lmk_m80s\n"
".Lmk_m80r:\n"
"v_mov_b32_e32 v254, v255\n"
"v_mfma_f32_16x16x32_bf16 v[104:107], v[136:139], v[156:159], v[104:107]\n"
"s_nop 0\n"
"v_permlane32_swap_b32_e32 v255, v254\n"
"v_add_f32_e32 v255, v255, v254\n"
"v_mfma_f32_16x16x32_bf16 v[104:107], v[56:59], v[152:155], v[104:107]\n"
"v_mul_f32_e32 v255, 0x3a800000, v255\n"
"v_mov_b32_e32 v254, v255\n"
"v_mfma_f32_16x16x32_bf16 v[104:107], v[112:115], v[148:151], v[104:107]\n"
"ds_write_b128 v3, v[68:71] offset:10240\n"
"v_mfma_f32_16x16x32_bf16 v[104:107], v[108:111], v[144:147], v[104:107]\n"
"v_permlane16_swap_b32_e32 v255, v254\n"
"v_cvt_pk_bf16_f32 v253, -v255, 0\n"
"v_mfma_f32_16x16x32_bf16 v[104:107], v[120:123], v[128:131], v[104:107]\n"
"v_fma_f32 v254, -v255, v255, v254\n"
"v_lshlrev_b32_e32 v253, 16, v253\n"
"; @A G1 0.2\n"
"v_mfma_f32_16x16x32_bf16 v[68:71], a[0:3], v[176:179], 0\n"
"v_max_f32_e32 v254, 0, v254\n"
"v_cvt_pk_bf16_f32 v254, v254, 0\n"
"v_mfma_f32_16x16x32_bf16 v[68:71], a[4:7], v[48:51], v[68:71]\n"
"v_lshlrev_b32_e32 v254, 16, v254\n"
"v_add_f32_e32 v254, 0x3727c5ac, v254\n"
"v_mfma_f32_16x16x32_bf16 v[68:71], a[8:11], v[132:135], v[68:71]\n"
"v_cvt_pk_bf16_f32 v254, v254, 0\n"
"v_lshlrev_b32_e32 v254, 16, v254\n"
"v_mfma_f32_16x16x32_bf16 v[68:71], a[12:15], v[184:187], v[68:71]\n"
"s_waitcnt vmcnt(10)\n"
"v_sqrt_f32_e32 v254, v254\n"
"v_mfma_f32_16x16x32_bf16 v[68:71], a[16:19], v[44:47], v[68:71]\n"
"s_mov_b32 s98, .Lmk_a2-.Lmk_g75\n"
"s_getpc_b64 s[84:85]\n"
".Lmk_g75:\n"
"s_add_u32 s84, s84, s98\n"
"s_addc_u32 s85, s85, 0\n"
"s_and_b32 s84, s84, -16\n"
"v_mfma_f32_16x16x32_bf16 v[68:71], a[20:23], v[192:195], v[68:71]\n"
"ds_write_b128 v3, v[104:107] offset:11264\n"
"v_mfma_f32_16x16x32_bf16 v[68:71], a[24:27], v[196:199], v[68:71]\n"
"s_and_b32 s85, s85, 0xffff\n"
"s_sub_u32 s98, .Lmk_end-.Lmk_g75, s98\n"
"s_min_u32 s86, s98, 0x3000\n"
"s_mov_b32 s87, 0x20000\n"
"v_mfma_f32_16x16x32_bf16 v[68:71], a[28:31], v[200:203], v[68:71]\n"
"s_mov_b32 m0, 0x27800\n"
"s_nop 0\n"
"buffer_load_dwordx4 v0, s[84:87], s99 offen lds\n"
"v_mfma_f32_16x16x32_bf16 v[104:107], a[60:63], v[200:203], 0\n"
"v_cvt_pk_bf16_f32 v254, v254, 0\n"
"v_lshlrev_b32_e32 v254, 16, v254\n"
"v_mfma_f32_16x16x32_bf16 v[104:107], a[56:59], v[196:199], v[104:107]\n"
"v_rcp_f32_e32 v255, v254\n"
"v_mfma_f32_16x16x32_bf16 v[104:107], a[52:55], v[192:195], v[104:107]\n"
"s_add_u32 s98, s52, 0x48000\n"
"buffer_load_dwordx4 v[128:131], v0, s[4:7], s98 offen sc1\n"
"v_mfma_f32_16x16x32_bf16 v[104:107], a[48:51], v[44:47], v[104:107]\n"
"s_waitcnt lgkmcnt(0)\n"
"s_barrier\n"
"v_mfma_f32_16x16x32_bf16 v[104:107], a[44:47], v[184:187], v[104:107]\n"
"v_cmp_u_f32_e32 vcc, v68, v68\n"
"v_mul_f32_e32 v254, 0x3e8293ee, v255\n"
"v_mfma_f32_16x16x32_bf16 v[104:107], a[40:43], v[132:135], v[104:107]\n"
"s_cbranch_vccnz .Lmk_p78s\n"
".Lmk_p78r:\n"
"ds_read_b128 v[144:147], v4\n"
"v_mfma_f32_16x16x32_bf16 v[104:107], a[36:39], v[48:51], v[104:107]\n"
"ds_write_b128 v3, v[68:71] offset:12288\n"
"v_mfma_f32_16x16x32_bf16 v[104:107], a[32:35], v[176:179], v[104:107]\n"
"buffer_load_dwordx4 v[68:71], v0, s[4:7], s98 offen offset:1024 sc1\n"
"v_mfma_f32_16x16x32_bf16 v[148:151], a[64:67], v[176:179], 0\n"
"ds_read_b128 v[152:155], v9 offset:128\n"
"ds_read_b128 v[156:159], v5\n"
"v_mfma_f32_16x16x32_bf16 v[148:151], a[68:71], v[48:51], v[148:151]\n"
"v_mfma_f32_16x16x32_bf16 v[148:151], a[72:75], v[132:135], v[148:151]\n"
"v_mfma_f32_16x16x32_bf16 v[148:151], a[76:79], v[184:187], v[148:151]\n"
"ds_read_b128 v[160:163], v4 offset:1024\n"
"ds_read_b128 v[164:167], v9 offset:192\n"
"v_mfma_f32_16x16x32_bf16 v[148:151], a[80:83], v[44:47], v[148:151]\n"
"buffer_load_dwordx4 v[168:171], v0, s[4:7], s98 offen offset:2048 sc1\n"
"v_mfma_f32_16x16x32_bf16 v[148:151], a[84:87], v[192:195], v[148:151]\n"
"ds_write_b128 v3, v[104:107] offset:13312\n"
"v_mfma_f32_16x16x32_bf16 v[148:151], a[88:91], v[196:199], v[148:151]\n"
"ds_read_b128 v[104:107], v5 offset:1024\n"
"s_waitcnt lgkmcnt(4)\n"
"v_mfma_f32_16x16x32_bf16 v[148:151], a[92:95], v[200:203], v[148:151]\n"
"v_fmac_f32_e32 v144, v253, v152\n"
"v_fmac_f32_e32 v145, v253, v153\n"
"v_mfma_f32_16x16x32_bf16 v[180:183], a[124:127], v[200:203], 0\n"
"v_fmac_f32_e32 v146, v253, v154\n"
"v_fmac_f32_e32 v147, v253, v155\n"
"v_mfma_f32_16x16x32_bf16 v[180:183], a[120:123], v[196:199], v[180:183]\n"
"buffer_load_dwordx4 v[152:155], v0, s[4:7], s98 offen offset:3072 sc1\n"
"v_mfma_f32_16x16x32_bf16 v[180:183], a[116:119], v[192:195], v[180:183]\n"
"ds_read_b128 v[188:191], v4 offset:2048\n"
"ds_read_b128 v[204:207], v9 offset:256\n"
"v_mfma_f32_16x16x32_bf16 v[180:183], a[112:115], v[44:47], v[180:183]\n"
"ds_read_b128 v[208:211], v5 offset:2048\n"
"v_add_f32_e32 v144, v144, v156\n"
"v_mfma_f32_16x16x32_bf16 v[180:183], a[108:111], v[184:187], v[180:183]\n"
"v_add_f32_e32 v145, v145, v157\n"
"v_add_f32_e32 v146, v146, v158\n"
"v_mfma_f32_16x16x32_bf16 v[180:183], a[104:107], v[132:135], v[180:183]\n"
"ds_write_b128 v3, v[148:151] offset:14336\n"
"v_mfma_f32_16x16x32_bf16 v[180:183], a[100:103], v[48:51], v[180:183]\n"
"buffer_load_dwordx4 v[148:151], v1, s[4:7], s98 offen sc1\n"
"v_mfma_f32_16x16x32_bf16 v[180:183], a[96:99], v[176:179], v[180:183]\n"
"s_waitcnt lgkmcnt(4)\n"
"v_add_f32_e32 v147, v147, v159\n"
"v_fmac_f32_e32 v160, v253, v164\n"
"v_mfma_f32_16x16x32_bf16 v[156:159], v[28:31], v[176:179], 0\n"
"v_fmac_f32_e32 v161, v253, v165\n"
"v_fmac_f32_e32 v162, v253, v166\n"
"v_mfma_f32_16x16x32_bf16 v[156:159], v[40:43], v[48:51], v[156:159]\n"
"ds_read_b128 v[212:215], v4 offset:3072\n"
"ds_read_b128 v[216:219], v9 offset:320\n"
"v_mfma_f32_16x16x32_bf16 v[156:159], v[24:27], v[132:135], v[156:159]\n"
"ds_read_b128 v[220:223], v5 offset:3072\n"
"v_fmac_f32_e32 v163, v253, v167\n"
"v_mfma_f32_16x16x32_bf16 v[156:159], v[72:75], v[184:187], v[156:159]\n"
"buffer_load_dwordx4 v[164:167], v1, s[4:7], s98 offen offset:1024 sc1\n"
"v_mfma_f32_16x16x32_bf16 v[156:159], v[32:35], v[44:47], v[156:159]\n"
"s_add_u32 s97, s64, 0x8000\n"
"v_add_f32_e32 v160, v160, v104\n"
"v_add_f32_e32 v161, v161, v105\n"
"v_mfma_f32_16x16x32_bf16 v[156:159], v[64:67], v[192:195], v[156:159]\n"
"ds_write_b128 v3, v[180:183] offset:15360\n"
"v_mfma_f32_16x16x32_bf16 v[156:159], v[60:63], v[196:199], v[156:159]\n"
"buffer_store_dwordx4 v[52:55], v2, s[4:7], s97 offen sc1\n"
"v_mfma_f32_16x16x32_bf16 v[156:159], v[36:39], v[200:203], v[156:159]\n"
"s_waitcnt lgkmcnt(5)\n"
"v_add_f32_e32 v162, v162, v106\n"
"v_add_f32_e32 v163, v163, v107\n"
"v_mfma_f32_16x16x32_bf16 v[104:107], v[100:103], v[200:203], 0\n"
"buffer_load_dwordx4 v[180:183], v1, s[4:7], s98 offen offset:2048 sc1\n"
"v_mfma_f32_16x16x32_bf16 v[104:107], v[76:79], v[196:199], v[104:107]\n"
"s_add_u32 s97, s68, s80\n"
"s_waitcnt vmcnt(18)\n"
"ds_read_b128 v[196:199], v4 offset:4096\n"
"v_mfma_f32_16x16x32_bf16 v[104:107], v[140:143], v[192:195], v[104:107]\n"
"buffer_store_dwordx2 v[18:19], v11, s[4:7], s97 offen sc1\n"
"v_mfma_f32_16x16x32_bf16 v[104:107], v[136:139], v[44:47], v[104:107]\n"
"ds_read_b128 v[44:47], v9\n"
"ds_read_b128 v[192:195], v5 offset:4096\n"
"v_mfma_f32_16x16x32_bf16 v[104:107], v[56:59], v[184:187], v[104:107]\n"
"v_fmac_f32_e32 v188, v253, v204\n"
"v_fmac_f32_e32 v189, v253, v205\n"
"v_mfma_f32_16x16x32_bf16 v[104:107], v[112:115], v[132:135], v[104:107]\n"
"buffer_load_dwordx4 v[132:135], v1, s[4:7], s98 offen offset:3072 sc1\n"
"v_mfma_f32_16x16x32_bf16 v[104:107], v[108:111], v[48:51], v[104:107]\n"
"ds_write_b128 v3, v[156:159] offset:16384\n"
"v_mfma_f32_16x16x32_bf16 v[104:107], v[120:123], v[176:179], v[104:107]\n"
"s_waitcnt vmcnt(16)\n"
"ds_read_b128 v[48:51], v4 offset:5120\n"
"ds_read_b128 v[156:159], v9 offset:64\n"
"; @A G1 0.3\n"
"v_mfma_f32_16x16x32_bf16 v[176:179], a[0:3], v[80:83], v[144:147]\n"
"ds_read_b128 v[184:187], v5 offset:5120\n"
"v_fmac_f32_e32 v190, v253, v206\n"
"v_mfma_f32_16x16x32_bf16 v[176:179], a[4:7], v[84:87], v[176:179]\n"
"v_fmac_f32_e32 v191, v253, v207\n"
"v_add_f32_e32 v188, v188, v208\n"
"v_mfma_f32_16x16x32_bf16 v[176:179], a[8:11], v[92:95], v[176:179]\n"
"s_add_u32 s98, s53, 0x48000\n"
"buffer_load_dwordx4 v[200:203], v0, s[4:7], s98 offen sc1\n"
"v_mfma_f32_16x16x32_bf16 v[176:179], a[12:15], v[96:99], v[176:179]\n"
"s_waitcnt vmcnt(12)\n"
"v_add_f32_e32 v189, v189, v209\n"
"v_add_f32_e32 v190, v190, v210\n"
"v_mfma_f32_16x16x32_bf16 v[176:179], a[16:19], v[88:91], v[176:179]\n"
"s_waitcnt lgkmcnt(8)\n"
"v_add_f32_e32 v191, v191, v211\n"
"v_fmac_f32_e32 v212, v253, v216\n"
"v_mfma_f32_16x16x32_bf16 v[176:179], a[20:23], v[116:119], v[176:179]\n"
"ds_write_b128 v3, v[104:107] offset:17408\n"
"v_mfma_f32_16x16x32_bf16 v[176:179], a[24:27], v[124:127], v[176:179]\n"
"v_fmac_f32_e32 v213, v253, v217\n"
"v_fmac_f32_e32 v214, v253, v218\n"
"v_mfma_f32_16x16x32_bf16 v[176:179], a[28:31], v[172:175], v[176:179]\n"
"buffer_load_dwordx4 v[104:107], v0, s[4:7], s98 offen offset:1024 sc1\n"
"v_mfma_f32_16x16x32_bf16 v[204:207], a[60:63], v[172:175], v[160:163]\n"
"v_fmac_f32_e32 v215, v253, v219\n"
"v_add_f32_e32 v212, v212, v220\n"
"v_mfma_f32_16x16x32_bf16 v[204:207], a[56:59], v[124:127], v[204:207]\n"
"v_add_f32_e32 v213, v213, v221\n"
"v_add_f32_e32 v214, v214, v222\n"
"v_mfma_f32_16x16x32_bf16 v[204:207], a[52:55], v[116:119], v[204:207]\n"
"s_waitcnt lgkmcnt(0)\n"
"s_barrier\n"
"v_mfma_f32_16x16x32_bf16 v[204:207], a[48:51], v[88:91], v[204:207]\n"
"ds_read_b128 v[208:211], v6\n"
"ds_read_b128 v[216:219], v6 offset:1024\n"
"v_mfma_f32_16x16x32_bf16 v[204:207], a[44:47], v[96:99], v[204:207]\n"
"v_cmp_u_f32_e32 vcc, v176, v176\n"
"v_add_f32_e32 v215, v215, v223\n"
"v_mfma_f32_16x16x32_bf16 v[204:207], a[40:43], v[92:95], v[204:207]\n"
"s_cbranch_vccnz .Lmk_p79s\n"
".Lmk_p79r:\n"
"v_fmac_f32_e32 v196, v253, v44\n"
"v_mfma_f32_16x16x32_bf16 v[204:207], a[36:39], v[84:87], v[204:207]\n"
"buffer_load_dwordx4 v[144:147], v0, s[4:7], s98 offen offset:2048 sc1\n"
"v_mfma_f32_16x16x32_bf16 v[204:207], a[32:35], v[80:83], v[204:207]\n"
"v_fmac_f32_e32 v197, v253, v45\n"
"v_fmac_f32_e32 v198, v253, v46\n"
"v_mfma_f32_16x16x32_bf16 v[188:191], a[64:67], v[80:83], v[188:191]\n"
"v_fmac_f32_e32 v199, v253, v47\n"
"v_add_f32_e32 v196, v196, v192\n"
"v_mfma_f32_16x16x32_bf16 v[188:191], a[68:71], v[84:87], v[188:191]\n"
"buffer_load_dwordx4 v[44:47], v0, s[4:7], s98 offen offset:3072 sc1\n"
"v_mfma_f32_16x16x32_bf16 v[188:191], a[72:75], v[92:95], v[188:191]\n"
"v_add_f32_e32 v197, v197, v193\n"
"v_add_f32_e32 v198, v198, v194\n"
"v_mfma_f32_16x16x32_bf16 v[188:191], a[76:79], v[96:99], v[188:191]\n"
"v_add_f32_e32 v199, v199, v195\n"
"v_fmac_f32_e32 v48, v253, v156\n"
"v_mfma_f32_16x16x32_bf16 v[188:191], a[80:83], v[88:91], v[188:191]\n"
"s_waitcnt lgkmcnt(0)\n"
"v_fmac_f32_e32 v49, v253, v157\n"
"v_fmac_f32_e32 v50, v253, v158\n"
"v_mfma_f32_16x16x32_bf16 v[188:191], a[84:87], v[116:119], v[188:191]\n"
"v_fmac_f32_e32 v51, v253, v159\n"
"v_add_f32_e32 v48, v48, v184\n"
"v_mfma_f32_16x16x32_bf16 v[188:191], a[88:91], v[124:127], v[188:191]\n"
"buffer_load_dwordx4 v[156:159], v1, s[4:7], s98 offen sc1\n"
"v_mfma_f32_16x16x32_bf16 v[188:191], a[92:95], v[172:175], v[188:191]\n"
"ds_read_b128 v[160:163], v6 offset:2048\n"
"ds_read_b128 v[192:195], v6 offset:3072\n"
"v_mfma_f32_16x16x32_bf16 v[212:215], a[124:127], v[172:175], v[212:215]\n"
"v_add_f32_e32 v49, v49, v185\n"
"v_add_f32_e32 v50, v50, v186\n"
"v_mfma_f32_16x16x32_bf16 v[212:215], a[120:123], v[124:127], v[212:215]\n"
"v_add_f32_e32 v51, v51, v187\n"
"v_add_f32_e32 v176, v176, v208\n"
"v_mfma_f32_16x16x32_bf16 v[212:215], a[116:119], v[116:119], v[212:215]\n"
"v_add_f32_e32 v177, v177, v209\n"
"v_add_f32_e32 v178, v178, v210\n"
"v_mfma_f32_16x16x32_bf16 v[212:215], a[112:115], v[88:91], v[212:215]\n"
"buffer_load_dwordx4 v[184:187], v1, s[4:7], s98 offen offset:1024 sc1\n"
"v_mfma_f32_16x16x32_bf16 v[212:215], a[108:111], v[96:99], v[212:215]\n"
"v_add_f32_e32 v179, v179, v211\n"
"v_add_f32_e32 v204, v204, v216\n"
"v_mfma_f32_16x16x32_bf16 v[212:215], a[104:107], v[92:95], v[212:215]\n"
"v_add_f32_e32 v205, v205, v217\n"
"v_add_f32_e32 v206, v206, v218\n"
"v_mfma_f32_16x16x32_bf16 v[212:215], a[100:103], v[84:87], v[212:215]\n"
"v_add_f32_e32 v207, v207, v219\n"
"v_mul_f32_e32 v176, v176, v255\n"
"v_mfma_f32_16x16x32_bf16 v[212:215], a[96:99], v[80:83], v[212:215]\n"
"v_mul_f32_e32 v177, v177, v255\n"
"v_mul_f32_e32 v178, v178, v255\n"
"v_mfma_f32_16x16x32_bf16 v[196:199], v[28:31], v[80:83], v[196:199]\n"
"buffer_load_dwordx4 v[208:211], v1, s[4:7], s98 offen offset:2048 sc1\n"
"v_mfma_f32_16x16x32_bf16 v[196:199], v[40:43], v[84:87], v[196:199]\n"
"v_mul_f32_e32 v179, v179, v255\n"
"v_mul_f32_e32 v204, v204, v255\n"
"v_mfma_f32_16x16x32_bf16 v[196:199], v[24:27], v[92:95], v[196:199]\n"
"v_mul_f32_e32 v205, v205, v255\n"
"v_mul_f32_e32 v206, v206, v255\n"
"v_mfma_f32_16x16x32_bf16 v[196:199], v[72:75], v[96:99], v[196:199]\n"
"v_mul_f32_e32 v207, v207, v255\n"
"v_cvt_pk_bf16_f32 v216, v176, v177\n"
"v_mfma_f32_16x16x32_bf16 v[196:199], v[32:35], v[88:91], v[196:199]\n"
"s_waitcnt lgkmcnt(0)\n"
"v_cvt_pk_bf16_f32 v217, v178, v179\n"
"v_cvt_pk_bf16_f32 v218, v204, v205\n"
"v_mfma_f32_16x16x32_bf16 v[196:199], v[64:67], v[116:119], v[196:199]\n"
"buffer_load_dwordx4 v[176:179], v1, s[4:7], s98 offen offset:3072 sc1\n"
"v_mfma_f32_16x16x32_bf16 v[196:199], v[60:63], v[124:127], v[196:199]\n"
"s_add_u32 s98, s64, 0x8000\n"
"ds_read_b128 v[220:223], v6 offset:4096\n"
"ds_read_b128 v[224:227], v6 offset:5120\n"
"v_mfma_f32_16x16x32_bf16 v[196:199], v[36:39], v[172:175], v[196:199]\n"
"v_cvt_pk_bf16_f32 v219, v206, v207\n"
"v_add_f32_e32 v188, v188, v160\n"
"v_mfma_f32_16x16x32_bf16 v[48:51], v[100:103], v[172:175], v[48:51]\n"
"buffer_store_dwordx4 v[52:55], v2, s[4:7], s98 offen offset:1024 sc1\n"
"v_mfma_f32_16x16x32_bf16 v[48:51], v[76:79], v[124:127], v[48:51]\n"
"ds_write_b128 v7, v[216:219] offset:24576\n"
"v_mfma_f32_16x16x32_bf16 v[48:51], v[140:143], v[116:119], v[48:51]\n"
"v_add_f32_e32 v189, v189, v161\n"
"v_add_f32_e32 v190, v190, v162\n"
"v_mfma_f32_16x16x32_bf16 v[48:51], v[136:139], v[88:91], v[48:51]\n"
"v_add_f32_e32 v191, v191, v163\n"
"v_add_f32_e32 v212, v212, v192\n"
"v_mfma_f32_16x16x32_bf16 v[48:51], v[56:59], v[96:99], v[48:51]\n"
"v_add_f32_e32 v213, v213, v193\n"
"v_add_f32_e32 v214, v214, v194\n"
"v_mfma_f32_16x16x32_bf16 v[48:51], v[112:115], v[92:95], v[48:51]\n"
"v_add_f32_e32 v215, v215, v195\n"
"v_mul_f32_e32 v188, v188, v255\n"
"v_mfma_f32_16x16x32_bf16 v[48:51], v[108:111], v[84:87], v[48:51]\n"
"v_mul_f32_e32 v189, v189, v255\n"
"v_mul_f32_e32 v190, v190, v255\n"
"v_mfma_f32_16x16x32_bf16 v[48:51], v[120:123], v[80:83], v[48:51]\n"
"v_mul_f32_e32 v191, v191, v255\n"
"v_mul_f32_e32 v212, v212, v255\n"
"v_mul_f32_e32 v213, v213, v255\n"
"v_mul_f32_e32 v214, v214, v255\n"
"v_mul_f32_e32 v215, v215, v255\n"
"v_cvt_pk_bf16_f32 v52, v188, v189\n"
"v_cvt_pk_bf16_f32 v53, v190, v191\n"
"v_cvt_pk_bf16_f32 v54, v212, v213\n"
"v_cvt_pk_bf16_f32 v55, v214, v215\n"
"ds_write_b128 v7, v[52:55] offset:57344\n"
".Lmk_a1:\n"
"s_waitcnt vmcnt(15)\n"
"; @A G1 1.0\n"
"v_mfma_f32_16x16x32_bf16 v[80:83], a[0:3], v[128:131], 0\n"
"v_mfma_f32_16x16x32_bf16 v[80:83], a[4:7], v[68:71], v[80:83]\n"
"s_add_u32 s98, s62, 0x4800\n"
"v_mfma_f32_16x16x32_bf16 v[80:83], a[8:11], v[168:171], v[80:83]\n"
"s_add_u32 s97, s54, 0x48000\n"
"buffer_load_dwordx4 v[84:87], v0, s[4:7], s97 offen sc1\n"
"v_mfma_f32_16x16x32_bf16 v[80:83], a[12:15], v[152:155], v[80:83]\n"
"s_waitcnt vmcnt(10)\n"
"v_mfma_f32_16x16x32_bf16 v[80:83], a[16:19], v[148:151], v[80:83]\n"
"buffer_load_dword v255, v10, s[4:7], s98 offen sc1\n"
"v_mfma_f32_16x16x32_bf16 v[80:83], a[20:23], v[164:167], v[80:83]\n"
"s_waitcnt lgkmcnt(2)\n"
"v_add_f32_e32 v196, v196, v220\n"
"v_add_f32_e32 v197, v197, v221\n"
"v_mfma_f32_16x16x32_bf16 v[80:83], a[24:27], v[180:183], v[80:83]\n"
"v_add_f32_e32 v198, v198, v222\n"
"v_add_f32_e32 v199, v199, v223\n"
"v_mfma_f32_16x16x32_bf16 v[80:83], a[28:31], v[132:135], v[80:83]\n"
"buffer_load_dwordx4 v[88:91], v0, s[4:7], s97 offen offset:1024 sc1\n"
"v_mfma_f32_16x16x32_bf16 v[92:95], a[60:63], v[132:135], 0\n"
"s_add_u32 s96, s70, 0\n"
"buffer_load_dwordx4 a[128:131], v0, s[4:7], s96 offen sc1\n"
"v_mfma_f32_16x16x32_bf16 v[92:95], a[56:59], v[180:183], v[92:95]\n"
"buffer_load_dword v253, v10, s[4:7], s98 offen offset:256 sc1\n"
"v_mfma_f32_16x16x32_bf16 v[92:95], a[52:55], v[164:167], v[92:95]\n"
"v_add_f32_e32 v48, v48, v224\n"
"v_add_f32_e32 v49, v49, v225\n"
"v_mfma_f32_16x16x32_bf16 v[92:95], a[48:51], v[148:151], v[92:95]\n"
"v_add_f32_e32 v50, v50, v226\n"
"v_add_f32_e32 v51, v51, v227\n"
"v_mfma_f32_16x16x32_bf16 v[92:95], a[44:47], v[152:155], v[92:95]\n"
"v_cmp_u_f32_e32 vcc, v80, v80\n"
"v_mul_f32_e32 v196, v196, v254\n"
"v_mfma_f32_16x16x32_bf16 v[92:95], a[40:43], v[168:171], v[92:95]\n"
"s_cbranch_vccnz .Lmk_p81s\n"
".Lmk_p81r:\n"
"v_mul_f32_e32 v197, v197, v254\n"
"v_mfma_f32_16x16x32_bf16 v[92:95], a[36:39], v[68:71], v[92:95]\n"
"buffer_load_dwordx4 v[96:99], v0, s[4:7], s97 offen offset:2048 sc1\n"
"v_mfma_f32_16x16x32_bf16 v[92:95], a[32:35], v[128:131], v[92:95]\n"
"ds_write_b128 v3, v[80:83]\n"
"v_mfma_f32_16x16x32_bf16 v[80:83], a[64:67], v[128:131], 0\n"
"buffer_load_dword v252, v10, s[4:7], s98 offen offset:512 sc1\n"
"v_mfma_f32_16x16x32_bf16 v[80:83], a[68:71], v[68:71], v[80:83]\n"
"buffer_load_dwordx4 v[116:119], v0, s[4:7], s97 offen offset:3072 sc1\n"
"v_mfma_f32_16x16x32_bf16 v[80:83], a[72:75], v[168:171], v[80:83]\n"
"v_mul_f32_e32 v198, v198, v254\n"
"v_mul_f32_e32 v199, v199, v254\n"
"v_mfma_f32_16x16x32_bf16 v[80:83], a[76:79], v[152:155], v[80:83]\n"
"buffer_load_dword v251, v10, s[4:7], s98 offen offset:768 sc1\n"
"v_mfma_f32_16x16x32_bf16 v[80:83], a[80:83], v[148:151], v[80:83]\n"
"v_mul_f32_e32 v48, v48, v254\n"
"v_mul_f32_e32 v49, v49, v254\n"
"v_mfma_f32_16x16x32_bf16 v[80:83], a[84:87], v[164:167], v[80:83]\n"
"ds_write_b128 v3, v[92:95] offset:1024\n"
"v_mfma_f32_16x16x32_bf16 v[80:83], a[88:91], v[180:183], v[80:83]\n"
"buffer_load_dwordx4 v[92:95], v1, s[4:7], s97 offen sc1\n"
"v_mfma_f32_16x16x32_bf16 v[80:83], a[92:95], v[132:135], v[80:83]\n"
"buffer_load_dword v250, v10, s[4:7], s98 offen offset:1024 sc1\n"
"v_mfma_f32_16x16x32_bf16 v[124:127], a[124:127], v[132:135], 0\n"
"v_mul_f32_e32 v50, v50, v254\n"
"v_mul_f32_e32 v51, v51, v254\n"
"v_mfma_f32_16x16x32_bf16 v[124:127], a[120:123], v[180:183], v[124:127]\n"
"v_cvt_pk_bf16_f32 v160, v196, v197\n"
"v_cvt_pk_bf16_f32 v161, v198, v199\n"
"v_mfma_f32_16x16x32_bf16 v[124:127], a[116:119], v[164:167], v[124:127]\n"
"buffer_load_dword v254, v10, s[4:7], s98 offen offset:1280 sc1\n"
"v_mfma_f32_16x16x32_bf16 v[124:127], a[112:115], v[148:151], v[124:127]\n"
"buffer_load_dwordx4 v[172:175], v1, s[4:7], s97 offen offset:1024 sc1\n"
"v_mfma_f32_16x16x32_bf16 v[124:127], a[108:111], v[152:155], v[124:127]\n"
"buffer_load_dwordx4 a[132:135], v0, s[4:7], s96 offen offset:1024 sc1\n"
"v_mfma_f32_16x16x32_bf16 v[124:127], a[104:107], v[168:171], v[124:127]\n"
"ds_write_b128 v3, v[80:83] offset:2048\n"
"v_mfma_f32_16x16x32_bf16 v[124:127], a[100:103], v[68:71], v[124:127]\n"
"buffer_load_dword v249, v10, s[4:7], s98 offen offset:1536 sc1\n"
"v_mfma_f32_16x16x32_bf16 v[124:127], a[96:99], v[128:131], v[124:127]\n"
"v_cvt_pk_bf16_f32 v162, v48, v49\n"
"v_cvt_pk_bf16_f32 v163, v50, v51\n"
"v_mfma_f32_16x16x32_bf16 v[48:51], v[28:31], v[128:131], 0\n"
"buffer_load_dwordx4 v[80:83], v1, s[4:7], s97 offen offset:2048 sc1\n"
"v_mfma_f32_16x16x32_bf16 v[48:51], v[40:43], v[68:71], v[48:51]\n"
"buffer_load_dword v248, v10, s[4:7], s98 offen offset:1792 sc1\n"
"v_mfma_f32_16x16x32_bf16 v[48:51], v[24:27], v[168:171], v[48:51]\n"
"ds_write_b128 v2, v[160:163] offset:49152\n"
"v_mfma_f32_16x16x32_bf16 v[48:51], v[72:75], v[152:155], v[48:51]\n"
"v_mfma_f32_16x16x32_bf16 v[48:51], v[32:35], v[148:151], v[48:51]\n"
"v_mfma_f32_16x16x32_bf16 v[48:51], v[64:67], v[164:167], v[48:51]\n"
"buffer_load_dwordx4 v[160:163], v1, s[4:7], s97 offen offset:3072 sc1\n"
"v_mfma_f32_16x16x32_bf16 v[48:51], v[60:63], v[180:183], v[48:51]\n"
"ds_write_b128 v3, v[124:127] offset:3072\n"
"v_mfma_f32_16x16x32_bf16 v[48:51], v[36:39], v[132:135], v[48:51]\n"
"v_mfma_f32_16x16x32_bf16 v[124:127], v[100:103], v[132:135], 0\n"
"v_mfma_f32_16x16x32_bf16 v[124:127], v[76:79], v[180:183], v[124:127]\n"
"v_mfma_f32_16x16x32_bf16 v[124:127], v[140:143], v[164:167], v[124:127]\n"
"v_mfma_f32_16x16x32_bf16 v[124:127], v[136:139], v[148:151], v[124:127]\n"
"v_mfma_f32_16x16x32_bf16 v[124:127], v[56:59], v[152:155], v[124:127]\n"
"v_mfma_f32_16x16x32_bf16 v[124:127], v[112:115], v[168:171], v[124:127]\n"
"s_nop 1\n"
"ds_write_b128 v3, v[48:51] offset:4096\n"
"v_mfma_f32_16x16x32_bf16 v[124:127], v[108:111], v[68:71], v[124:127]\n"
"v_mfma_f32_16x16x32_bf16 v[124:127], v[120:123], v[128:131], v[124:127]\n"
"s_waitcnt vmcnt(23)\n"
"; @A G1 1.1\n"
"v_mfma_f32_16x16x32_bf16 v[48:51], a[0:3], v[200:203], 0\n"
"buffer_load_dwordx4 a[136:139], v0, s[4:7], s96 offen offset:2048 sc1\n"
"v_mfma_f32_16x16x32_bf16 v[48:51], a[4:7], v[104:107], v[48:51]\n"
"v_mfma_f32_16x16x32_bf16 v[48:51], a[8:11], v[144:147], v[48:51]\n"
"s_add_u32 s98, s55, 0x48000\n"
"buffer_load_dwordx4 v[68:71], v0, s[4:7], s98 offen sc1\n"
"v_mfma_f32_16x16x32_bf16 v[48:51], a[12:15], v[44:47], v[48:51]\n"
"s_waitcnt vmcnt(21)\n"
"v_mfma_f32_16x16x32_bf16 v[48:51], a[16:19], v[156:159], v[48:51]\n"
"v_mfma_f32_16x16x32_bf16 v[48:51], a[20:23], v[184:187], v[48:51]\n"
"ds_write_b128 v3, v[124:127] offset:5120\n"
"v_mfma_f32_16x16x32_bf16 v[48:51], a[24:27], v[208:211], v[48:51]\n"
"v_mfma_f32_16x16x32_bf16 v[48:51], a[28:31], v[176:179], v[48:51]\n"
"buffer_load_dwordx4 v[124:127], v0, s[4:7], s98 offen offset:1024 sc1\n"
"v_mfma_f32_16x16x32_bf16 v[128:131], a[60:63], v[176:179], 0\n"
"v_mfma_f32_16x16x32_bf16 v[128:131], a[56:59], v[208:211], v[128:131]\n"
"v_mfma_f32_16x16x32_bf16 v[128:131], a[52:55], v[184:187], v[128:131]\n"
"v_mfma_f32_16x16x32_bf16 v[128:131], a[48:51], v[156:159], v[128:131]\n"
"v_mfma_f32_16x16x32_bf16 v[128:131], a[44:47], v[44:47], v[128:131]\n"
"s_add_u32 s97, s67, 0\n"
"s_nop 0\n"
"v_cmp_u_f32_e32 vcc, v48, v48\n"
"v_mfma_f32_16x16x32_bf16 v[128:131], a[40:43], v[144:147], v[128:131]\n"
"s_cbranch_vccnz .Lmk_p82s\n"
".Lmk_p82r:\n"
"v_mfma_f32_16x16x32_bf16 v[128:131], a[36:39], v[104:107], v[128:131]\n"
"buffer_load_dwordx4 v[132:135], v0, s[4:7], s98 offen offset:2048 sc1\n"
"s_add_u32 s95, s64, 0x2000\n"
"v_mfma_f32_16x16x32_bf16 v[128:131], a[32:35], v[200:203], v[128:131]\n"
"ds_write_b128 v3, v[48:51] offset:6144\n"
"v_mfma_f32_16x16x32_bf16 v[48:51], a[64:67], v[200:203], 0\n"
"buffer_load_dwordx2 v[246:247], v12, s[4:7], s97 offen sc1\n"
"v_mfma_f32_16x16x32_bf16 v[48:51], a[68:71], v[104:107], v[48:51]\n"
"buffer_load_dwordx4 v[148:151], v0, s[4:7], s98 offen offset:3072 sc1\n"
"v_mfma_f32_16x16x32_bf16 v[48:51], a[72:75], v[144:147], v[48:51]\n"
"buffer_store_dwordx4 v[216:219], v7, s[4:7], s95 offen sc1\n"
"v_mfma_f32_16x16x32_bf16 v[48:51], a[76:79], v[44:47], v[48:51]\n"
"v_mfma_f32_16x16x32_bf16 v[48:51], a[80:83], v[156:159], v[48:51]\n"
"buffer_load_dwordx4 a[140:143], v0, s[4:7], s96 offen offset:3072 sc1\n"
"v_mfma_f32_16x16x32_bf16 v[48:51], a[84:87], v[184:187], v[48:51]\n"
"ds_write_b128 v3, v[128:131] offset:7168\n"
"v_mfma_f32_16x16x32_bf16 v[48:51], a[88:91], v[208:211], v[48:51]\n"
"buffer_load_dwordx4 v[128:131], v1, s[4:7], s98 offen sc1\n"
"v_mfma_f32_16x16x32_bf16 v[48:51], a[92:95], v[176:179], v[48:51]\n"
"v_mfma_f32_16x16x32_bf16 v[152:155], a[124:127], v[176:179], 0\n"
"v_mfma_f32_16x16x32_bf16 v[152:155], a[120:123], v[208:211], v[152:155]\n"
"v_mfma_f32_16x16x32_bf16 v[152:155], a[116:119], v[184:187], v[152:155]\n"
"v_mfma_f32_16x16x32_bf16 v[152:155], a[112:115], v[156:159], v[152:155]\n"
"buffer_load_dwordx4 v[164:167], v1, s[4:7], s98 offen offset:1024 sc1\n"
"v_mfma_f32_16x16x32_bf16 v[152:155], a[108:111], v[44:47], v[152:155]\n"
"v_mfma_f32_16x16x32_bf16 v[152:155], a[104:107], v[144:147], v[152:155]\n"
"s_nop 0\n"
"ds_write_b128 v3, v[48:51] offset:8192\n"
"v_mfma_f32_16x16x32_bf16 v[152:155], a[100:103], v[104:107], v[152:155]\n"
"v_mfma_f32_16x16x32_bf16 v[152:155], a[96:99], v[200:203], v[152:155]\n"
"v_mfma_f32_16x16x32_bf16 v[48:51], v[28:31], v[200:203], 0\n"
"buffer_load_dwordx4 v[168:171], v1, s[4:7], s98 offen offset:2048 sc1\n"
"v_mfma_f32_16x16x32_bf16 v[48:51], v[40:43], v[104:107], v[48:51]\n"
"v_mfma_f32_16x16x32_bf16 v[48:51], v[24:27], v[144:147], v[48:51]\n"
"v_mfma_f32_16x16x32_bf16 v[48:51], v[72:75], v[44:47], v[48:51]\n"
"v_mfma_f32_16x16x32_bf16 v[48:51], v[32:35], v[156:159], v[48:51]\n"
"s_waitcnt vmcnt(12)\n"
"v_add_f32_e32 v255, v255, v250\n"
"v_add_f32_e32 v253, v253, v254\n"
"v_mfma_f32_16x16x32_bf16 v[48:51], v[64:67], v[184:187], v[48:51]\n"
"buffer_load_dwordx4 v[180:183], v1, s[4:7], s98 offen offset:3072 sc1\n"
"v_mfma_f32_16x16x32_bf16 v[48:51], v[60:63], v[208:211], v[48:51]\n"
"ds_write_b128 v3, v[152:155] offset:9216\n"
"v_mfma_f32_16x16x32_bf16 v[48:51], v[36:39], v[176:179], v[48:51]\n"
"v_add_f32_e32 v252, v252, v249\n"
"v_add_f32_e32 v251, v251, v248\n"
"v_mfma_f32_16x16x32_bf16 v[152:155], v[100:103], v[176:179], 0\n"
"buffer_load_dwordx4 a[144:147], v1, s[4:7], s96 offen sc1\n"
"s_add_u32 s98, s64, 0x6000\n"
"v_mfma_f32_16x16x32_bf16 v[152:155], v[76:79], v[208:211], v[152:155]\n"
"v_add_f32_e32 v255, v255, v252\n"
"v_add_f32_e32 v253, v253, v251\n"
"v_mfma_f32_16x16x32_bf16 v[152:155], v[140:143], v[184:187], v[152:155]\n"
"buffer_store_dwordx4 v[52:55], v7, s[4:7], s98 offen sc1\n"
"v_mfma_f32_16x16x32_bf16 v[152:155], v[136:139], v[156:159], v[152:155]\n"
"v_add_f32_e32 v255, v255, v253\n"
"v_cmp_u_f32_e32 vcc, v255, v255\n"
"v_mfma_f32_16x16x32_bf16 v[152:155], v[56:59], v[44:47], v[152:155]\n"
"s_cbranch_vccnz .Lmk_m85s\n"
".Lmk_m85r:\n"
"v_mov_b32_e32 v253, v255\n"
"v_mfma_f32_16x16x32_bf16 v[152:155], v[112:115], v[144:147], v[152:155]\n"
"ds_write_b128 v3, v[48:51] offset:10240\n"
"v_mfma_f32_16x16x32_bf16 v[152:155], v[108:111], v[104:107], v[152:155]\n"
"v_permlane32_swap_b32_e32 v255, v253\n"
"v_add_f32_e32 v255, v255, v253\n"
"v_mfma_f32_16x16x32_bf16 v[152:155], v[120:123], v[200:203], v[152:155]\n"
"v_mul_f32_e32 v255, 0x3a800000, v255\n"
"v_mov_b32_e32 v253, v255\n"
"; @A G1 1.2\n"
"v_mfma_f32_16x16x32_bf16 v[44:47], a[0:3], v[84:87], 0\n"
"s_nop 0\n"
"v_permlane16_swap_b32_e32 v255, v253\n"
"v_cvt_pk_bf16_f32 v254, -v255, 0\n"
"v_mfma_f32_16x16x32_bf16 v[44:47], a[4:7], v[88:91], v[44:47]\n"
"s_waitcnt vmcnt(9)\n"
"v_cmp_ne_u64_e32 vcc, v[246:247], v[18:19]\n"
"v_mfma_f32_16x16x32_bf16 v[44:47], a[8:11], v[96:99], v[44:47]\n"
"s_cbranch_vccnz .Lmk_f72s\n"
".Lmk_f72r:\n"
"v_fma_f32 v253, -v255, v255, v253\n"
"v_mfma_f32_16x16x32_bf16 v[44:47], a[12:15], v[116:119], v[44:47]\n"
"v_lshlrev_b32_e32 v254, 16, v254\n"
"v_max_f32_e32 v253, 0, v253\n"
"v_mfma_f32_16x16x32_bf16 v[44:47], a[16:19], v[92:95], v[44:47]\n"
"v_cvt_pk_bf16_f32 v253, v253, 0\n"
"v_lshlrev_b32_e32 v253, 16, v253\n"
"v_mfma_f32_16x16x32_bf16 v[44:47], a[20:23], v[172:175], v[44:47]\n"
"ds_write_b128 v3, v[152:155] offset:11264\n"
"v_mfma_f32_16x16x32_bf16 v[44:47], a[24:27], v[80:83], v[44:47]\n"
"v_add_f32_e32 v253, 0x3727c5ac, v253\n"
"v_cvt_pk_bf16_f32 v253, v253, 0\n"
"v_mfma_f32_16x16x32_bf16 v[44:47], a[28:31], v[160:163], v[44:47]\n"
"v_lshlrev_b32_e32 v253, 16, v253\n"
"v_mfma_f32_16x16x32_bf16 v[48:51], a[60:63], v[160:163], 0\n"
"v_sqrt_f32_e32 v253, v253\n"
"v_mfma_f32_16x16x32_bf16 v[48:51], a[56:59], v[80:83], v[48:51]\n"
"v_cvt_pk_bf16_f32 v253, v253, 0\n"
"v_lshlrev_b32_e32 v253, 16, v253\n"
"v_mfma_f32_16x16x32_bf16 v[48:51], a[52:55], v[172:175], v[48:51]\n"
"s_add_u32 s98, s52, 0x8000\n"
"buffer_load_dwordx4 v[52:55], v0, s[4:7], s98 offen sc1\n"
"v_mfma_f32_16x16x32_bf16 v[48:51], a[48:51], v[92:95], v[48:51]\n"
"s_waitcnt lgkmcnt(0)\n"
"s_barrier\n"
"v_mfma_f32_16x16x32_bf16 v[48:51], a[44:47], v[116:119], v[48:51]\n"
"ds_read_b128 v[104:107], v4\n"
"v_cmp_u_f32_e32 vcc, v44, v44\n"
"v_mfma_f32_16x16x32_bf16 v[48:51], a[40:43], v[96:99], v[48:51]\n"
"s_cbranch_vccnz .Lmk_p83s\n"
".Lmk_p83r:\n"
"ds_read_b128 v[144:147], v9 offset:128\n"
"v_mfma_f32_16x16x32_bf16 v[48:51], a[36:39], v[88:91], v[48:51]\n"
"ds_write_b128 v3, v[44:47] offset:12288\n"
"v_mfma_f32_16x16x32_bf16 v[48:51], a[32:35], v[84:87], v[48:51]\n"
"buffer_load_dwordx4 v[44:47], v0, s[4:7], s98 offen offset:1024 sc1\n"
"v_mfma_f32_16x16x32_bf16 v[152:155], a[64:67], v[84:87], 0\n"
"buffer_load_dwordx4 a[148:151], v1, s[4:7], s96 offen offset:1024 sc1\n"
"v_mfma_f32_16x16x32_bf16 v[152:155], a[68:71], v[88:91], v[152:155]\n"
"v_rcp_f32_e32 v255, v253\n"
"v_mfma_f32_16x16x32_bf16 v[152:155], a[72:75], v[96:99], v[152:155]\n"
"ds_read_b128 v[156:159], v5\n"
"v_mul_f32_e32 v253, 0x3e8293ee, v255\n"
"v_mfma_f32_16x16x32_bf16 v[152:155], a[76:79], v[116:119], v[152:155]\n"
"ds_read_b128 v[176:179], v4 offset:1024\n"
"ds_read_b128 v[184:187], v9 offset:192\n"
"v_mfma_f32_16x16x32_bf16 v[152:155], a[80:83], v[92:95], v[152:155]\n"
"buffer_load_dwordx4 v[188:191], v0, s[4:7], s98 offen offset:2048 sc1\n"
"v_mfma_f32_16x16x32_bf16 v[152:155], a[84:87], v[172:175], v[152:155]\n"
"ds_write_b128 v3, v[48:51] offset:13312\n"
"v_mfma_f32_16x16x32_bf16 v[152:155], a[88:91], v[80:83], v[152:155]\n"
"ds_read_b128 v[48:51], v5 offset:1024\n"
"v_mfma_f32_16x16x32_bf16 v[152:155], a[92:95], v[160:163], v[152:155]\n"
"s_waitcnt lgkmcnt(4)\n"
"v_fmac_f32_e32 v104, v254, v144\n"
"v_fmac_f32_e32 v105, v254, v145\n"
"v_mfma_f32_16x16x32_bf16 v[192:195], a[124:127], v[160:163], 0\n"
"v_fmac_f32_e32 v106, v254, v146\n"
"v_fmac_f32_e32 v107, v254, v147\n"
"v_mfma_f32_16x16x32_bf16 v[192:195], a[120:123], v[80:83], v[192:195]\n"
"buffer_load_dwordx4 v[144:147], v0, s[4:7], s98 offen offset:3072 sc1\n"
"v_mfma_f32_16x16x32_bf16 v[192:195], a[116:119], v[172:175], v[192:195]\n"
"ds_read_b128 v[196:199], v4 offset:2048\n"
"ds_read_b128 v[200:203], v9 offset:256\n"
"v_mfma_f32_16x16x32_bf16 v[192:195], a[112:115], v[92:95], v[192:195]\n"
"ds_read_b128 v[204:207], v5 offset:2048\n"
"v_add_f32_e32 v104, v104, v156\n"
"v_mfma_f32_16x16x32_bf16 v[192:195], a[108:111], v[116:119], v[192:195]\n"
"v_add_f32_e32 v105, v105, v157\n"
"v_add_f32_e32 v106, v106, v158\n"
"v_mfma_f32_16x16x32_bf16 v[192:195], a[104:107], v[96:99], v[192:195]\n"
"ds_write_b128 v3, v[152:155] offset:14336\n"
"v_mfma_f32_16x16x32_bf16 v[192:195], a[100:103], v[88:91], v[192:195]\n"
"buffer_load_dwordx4 v[152:155], v1, s[4:7], s98 offen sc1\n"
"v_mfma_f32_16x16x32_bf16 v[192:195], a[96:99], v[84:87], v[192:195]\n"
"s_waitcnt lgkmcnt(4)\n"
"v_add_f32_e32 v107, v107, v159\n"
"v_fmac_f32_e32 v176, v254, v184\n"
"v_mfma_f32_16x16x32_bf16 v[156:159], v[28:31], v[84:87], 0\n"
"v_fmac_f32_e32 v177, v254, v185\n"
"v_fmac_f32_e32 v178, v254, v186\n"
"v_mfma_f32_16x16x32_bf16 v[156:159], v[40:43], v[88:91], v[156:159]\n"
"ds_read_b128 v[208:211], v4 offset:3072\n"
"ds_read_b128 v[212:215], v9 offset:320\n"
"v_mfma_f32_16x16x32_bf16 v[156:159], v[24:27], v[96:99], v[156:159]\n"
"ds_read_b128 v[216:219], v5 offset:3072\n"
"v_fmac_f32_e32 v179, v254, v187\n"
"v_mfma_f32_16x16x32_bf16 v[156:159], v[72:75], v[116:119], v[156:159]\n"
"buffer_load_dwordx4 v[184:187], v1, s[4:7], s98 offen offset:1024 sc1\n"
"v_mfma_f32_16x16x32_bf16 v[156:159], v[32:35], v[92:95], v[156:159]\n"
"buffer_load_dwordx4 a[152:155], v1, s[4:7], s96 offen offset:2048 sc1\n"
"v_mfma_f32_16x16x32_bf16 v[156:159], v[64:67], v[172:175], v[156:159]\n"
"ds_write_b128 v3, v[192:195] offset:15360\n"
"v_mfma_f32_16x16x32_bf16 v[156:159], v[60:63], v[80:83], v[156:159]\n"
"v_add_f32_e32 v176, v176, v48\n"
"v_add_f32_e32 v177, v177, v49\n"
"v_mfma_f32_16x16x32_bf16 v[156:159], v[36:39], v[160:163], v[156:159]\n"
"s_waitcnt lgkmcnt(5)\n"
"v_add_f32_e32 v178, v178, v50\n"
"v_add_f32_e32 v179, v179, v51\n"
"v_mfma_f32_16x16x32_bf16 v[48:51], v[100:103], v[160:163], 0\n"
"buffer_load_dwordx4 v[160:163], v1, s[4:7], s98 offen offset:2048 sc1\n"
"v_mfma_f32_16x16x32_bf16 v[48:51], v[76:79], v[80:83], v[48:51]\n"
"ds_read_b128 v[80:83], v4 offset:4096\n"
"ds_read_b128 v[192:195], v9\n"
"v_mfma_f32_16x16x32_bf16 v[48:51], v[140:143], v[172:175], v[48:51]\n"
"ds_read_b128 v[172:175], v5 offset:4096\n"
"v_fmac_f32_e32 v196, v254, v200\n"
"v_mfma_f32_16x16x32_bf16 v[48:51], v[136:139], v[92:95], v[48:51]\n"
"v_fmac_f32_e32 v197, v254, v201\n"
"v_fmac_f32_e32 v198, v254, v202\n"
"v_mfma_f32_16x16x32_bf16 v[48:51], v[56:59], v[116:119], v[48:51]\n"
"v_fmac_f32_e32 v199, v254, v203\n"
"v_add_f32_e32 v196, v196, v204\n"
"v_mfma_f32_16x16x32_bf16 v[48:51], v[112:115], v[96:99], v[48:51]\n"
"buffer_load_dwordx4 v[92:95], v1, s[4:7], s98 offen offset:3072 sc1\n"
"v_mfma_f32_16x16x32_bf16 v[48:51], v[108:111], v[88:91], v[48:51]\n"
"ds_write_b128 v3, v[156:159] offset:16384\n"
"v_mfma_f32_16x16x32_bf16 v[48:51], v[120:123], v[84:87], v[48:51]\n"
"s_waitcnt vmcnt(18)\n"
"ds_read_b128 v[84:87], v4 offset:5120\n"
"ds_read_b128 v[88:91], v9 offset:64\n"
"; @A G1 1.3\n"
"v_mfma_f32_16x16x32_bf16 v[96:99], a[0:3], v[68:71], v[104:107]\n"
"ds_read_b128 v[116:119], v5 offset:5120\n"
"v_add_f32_e32 v197, v197, v205\n"
"v_mfma_f32_16x16x32_bf16 v[96:99], a[4:7], v[124:127], v[96:99]\n"
"s_waitcnt lgkmcnt(8)\n"
"v_add_f32_e32 v198, v198, v206\n"
"v_add_f32_e32 v199, v199, v207\n"
"v_mfma_f32_16x16x32_bf16 v[96:99], a[8:11], v[132:135], v[96:99]\n"
"s_add_u32 s98, s53, 0x8000\n"
"buffer_load_dwordx4 v[156:159], v0, s[4:7], s98 offen sc1\n"
"v_mfma_f32_16x16x32_bf16 v[96:99], a[12:15], v[148:151], v[96:99]\n"
"s_waitcnt vmcnt(13)\n"
"v_fmac_f32_e32 v208, v254, v212\n"
"v_fmac_f32_e32 v209, v254, v213\n"
"v_mfma_f32_16x16x32_bf16 v[96:99], a[16:19], v[128:131], v[96:99]\n"
"v_fmac_f32_e32 v210, v254, v214\n"
"v_fmac_f32_e32 v211, v254, v215\n"
"v_mfma_f32_16x16x32_bf16 v[96:99], a[20:23], v[164:167], v[96:99]\n"
"ds_write_b128 v3, v[48:51] offset:17408\n"
"v_mfma_f32_16x16x32_bf16 v[96:99], a[24:27], v[168:171], v[96:99]\n"
"v_add_f32_e32 v208, v208, v216\n"
"v_add_f32_e32 v209, v209, v217\n"
"v_mfma_f32_16x16x32_bf16 v[96:99], a[28:31], v[180:183], v[96:99]\n"
"buffer_load_dwordx4 v[48:51], v0, s[4:7], s98 offen offset:1024 sc1\n"
"v_mfma_f32_16x16x32_bf16 v[200:203], a[60:63], v[180:183], v[176:179]\n"
"buffer_load_dwordx4 a[156:159], v1, s[4:7], s96 offen offset:3072 sc1\n"
"v_mfma_f32_16x16x32_bf16 v[200:203], a[56:59], v[168:171], v[200:203]\n"
"s_waitcnt lgkmcnt(5)\n"
"v_add_f32_e32 v210, v210, v218\n"
"v_add_f32_e32 v211, v211, v219\n"
"v_mfma_f32_16x16x32_bf16 v[200:203], a[52:55], v[164:167], v[200:203]\n"
"s_waitcnt lgkmcnt(0)\n"
"s_barrier\n"
"v_mfma_f32_16x16x32_bf16 v[200:203], a[48:51], v[128:131], v[200:203]\n"
"ds_read_b128 v[204:207], v6\n"
"ds_read_b128 v[212:215], v6 offset:1024\n"
"v_mfma_f32_16x16x32_bf16 v[200:203], a[44:47], v[148:151], v[200:203]\n"
"v_cmp_u_f32_e32 vcc, v96, v96\n"
"v_fmac_f32_e32 v80, v254, v192\n"
"v_mfma_f32_16x16x32_bf16 v[200:203], a[40:43], v[132:135], v[200:203]\n"
"s_cbranch_vccnz .Lmk_p84s\n"
".Lmk_p84r:\n"
"v_fmac_f32_e32 v81, v254, v193\n"
"v_mfma_f32_16x16x32_bf16 v[200:203], a[36:39], v[124:127], v[200:203]\n"
"buffer_load_dwordx4 v[104:107], v0, s[4:7], s98 offen offset:2048 sc1\n"
"v_mfma_f32_16x16x32_bf16 v[200:203], a[32:35], v[68:71], v[200:203]\n"
"v_fmac_f32_e32 v82, v254, v194\n"
"v_fmac_f32_e32 v83, v254, v195\n"
"v_mfma_f32_16x16x32_bf16 v[196:199], a[64:67], v[68:71], v[196:199]\n"
"v_add_f32_e32 v80, v80, v172\n"
"v_add_f32_e32 v81, v81, v173\n"
"v_mfma_f32_16x16x32_bf16 v[196:199], a[68:71], v[124:127], v[196:199]\n"
"buffer_load_dwordx4 v[176:179], v0, s[4:7], s98 offen offset:3072 sc1\n"
"v_mfma_f32_16x16x32_bf16 v[196:199], a[72:75], v[132:135], v[196:199]\n"
"v_add_f32_e32 v82, v82, v174\n"
"v_add_f32_e32 v83, v83, v175\n"
"v_mfma_f32_16x16x32_bf16 v[196:199], a[76:79], v[148:151], v[196:199]\n"
"v_fmac_f32_e32 v84, v254, v88\n"
"v_fmac_f32_e32 v85, v254, v89\n"
"v_mfma_f32_16x16x32_bf16 v[196:199], a[80:83], v[128:131], v[196:199]\n"
"s_waitcnt lgkmcnt(0)\n"
"v_fmac_f32_e32 v86, v254, v90\n"
"v_fmac_f32_e32 v87, v254, v91\n"
"v_mfma_f32_16x16x32_bf16 v[196:199], a[84:87], v[164:167], v[196:199]\n"
"v_add_f32_e32 v84, v84, v116\n"
"v_add_f32_e32 v85, v85, v117\n"
"v_mfma_f32_16x16x32_bf16 v[196:199], a[88:91], v[168:171], v[196:199]\n"
"buffer_load_dwordx4 v[88:91], v1, s[4:7], s98 offen sc1\n"
"v_mfma_f32_16x16x32_bf16 v[196:199], a[92:95], v[180:183], v[196:199]\n"
"ds_read_b128 v[172:175], v6 offset:2048\n"
"ds_read_b128 v[192:195], v6 offset:3072\n"
"v_mfma_f32_16x16x32_bf16 v[208:211], a[124:127], v[180:183], v[208:211]\n"
"v_add_f32_e32 v86, v86, v118\n"
"v_add_f32_e32 v87, v87, v119\n"
"v_mfma_f32_16x16x32_bf16 v[208:211], a[120:123], v[168:171], v[208:211]\n"
"v_add_f32_e32 v96, v96, v204\n"
"v_add_f32_e32 v97, v97, v205\n"
"v_mfma_f32_16x16x32_bf16 v[208:211], a[116:119], v[164:167], v[208:211]\n"
"s_add_u32 s97, s70, 0x2000\n"
"v_add_f32_e32 v98, v98, v206\n"
"v_add_f32_e32 v99, v99, v207\n"
"v_mfma_f32_16x16x32_bf16 v[208:211], a[112:115], v[128:131], v[208:211]\n"
"buffer_load_dwordx4 v[116:119], v1, s[4:7], s98 offen offset:1024 sc1\n"
"v_mfma_f32_16x16x32_bf16 v[208:211], a[108:111], v[148:151], v[208:211]\n"
"buffer_load_dwordx4 a[160:163], v0, s[4:7], s97 offen sc1\n"
"v_mfma_f32_16x16x32_bf16 v[208:211], a[104:107], v[132:135], v[208:211]\n"
"v_add_f32_e32 v200, v200, v212\n"
"v_add_f32_e32 v201, v201, v213\n"
"v_mfma_f32_16x16x32_bf16 v[208:211], a[100:103], v[124:127], v[208:211]\n"
"v_add_f32_e32 v202, v202, v214\n"
"v_add_f32_e32 v203, v203, v215\n"
"v_mfma_f32_16x16x32_bf16 v[208:211], a[96:99], v[68:71], v[208:211]\n"
"v_mul_f32_e32 v96, v96, v255\n"
"v_mul_f32_e32 v97, v97, v255\n"
"v_mfma_f32_16x16x32_bf16 v[80:83], v[28:31], v[68:71], v[80:83]\n"
"buffer_load_dwordx4 v[204:207], v1, s[4:7], s98 offen offset:2048 sc1\n"
"v_mfma_f32_16x16x32_bf16 v[80:83], v[40:43], v[124:127], v[80:83]\n"
"v_mul_f32_e32 v98, v98, v255\n"
"v_mul_f32_e32 v99, v99, v255\n"
"v_mfma_f32_16x16x32_bf16 v[80:83], v[24:27], v[132:135], v[80:83]\n"
"v_mul_f32_e32 v200, v200, v255\n"
"v_mul_f32_e32 v201, v201, v255\n"
"v_mfma_f32_16x16x32_bf16 v[80:83], v[72:75], v[148:151], v[80:83]\n"
"v_mul_f32_e32 v202, v202, v255\n"
"v_mul_f32_e32 v203, v203, v255\n"
"v_mfma_f32_16x16x32_bf16 v[80:83], v[32:35], v[128:131], v[80:83]\n"
"s_waitcnt lgkmcnt(0)\n"
"v_cvt_pk_bf16_f32 v212, v96, v97\n"
"v_cvt_pk_bf16_f32 v213, v98, v99\n"
"v_mfma_f32_16x16x32_bf16 v[80:83], v[64:67], v[164:167], v[80:83]\n"
"buffer_load_dwordx4 v[96:99], v1, s[4:7], s98 offen offset:3072 sc1\n"
"v_mfma_f32_16x16x32_bf16 v[80:83], v[60:63], v[168:171], v[80:83]\n"
"ds_read_b128 v[216:219], v6 offset:4096\n"
"ds_read_b128 v[220:223], v6 offset:5120\n"
"v_mfma_f32_16x16x32_bf16 v[80:83], v[36:39], v[180:183], v[80:83]\n"
"v_cvt_pk_bf16_f32 v214, v200, v201\n"
"v_cvt_pk_bf16_f32 v215, v202, v203\n"
"v_mfma_f32_16x16x32_bf16 v[84:87], v[100:103], v[180:183], v[84:87]\n"
"ds_write_b128 v8, v[212:215] offset:25600\n"
"v_mfma_f32_16x16x32_bf16 v[84:87], v[76:79], v[168:171], v[84:87]\n"
"v_add_f32_e32 v196, v196, v172\n"
"v_add_f32_e32 v197, v197, v173\n"
"v_mfma_f32_16x16x32_bf16 v[84:87], v[140:143], v[164:167], v[84:87]\n"
"v_add_f32_e32 v198, v198, v174\n"
"v_add_f32_e32 v199, v199, v175\n"
"v_mfma_f32_16x16x32_bf16 v[84:87], v[136:139], v[128:131], v[84:87]\n"
"v_add_f32_e32 v208, v208, v192\n"
"v_add_f32_e32 v209, v209, v193\n"
"v_mfma_f32_16x16x32_bf16 v[84:87], v[56:59], v[148:151], v[84:87]\n"
"v_add_f32_e32 v210, v210, v194\n"
"v_add_f32_e32 v211, v211, v195\n"
"v_mfma_f32_16x16x32_bf16 v[84:87], v[112:115], v[132:135], v[84:87]\n"
"v_mul_f32_e32 v196, v196, v255\n"
"v_mul_f32_e32 v197, v197, v255\n"
"v_mfma_f32_16x16x32_bf16 v[84:87], v[108:111], v[124:127], v[84:87]\n"
"v_mul_f32_e32 v198, v198, v255\n"
"v_mul_f32_e32 v199, v199, v255\n"
"v_mfma_f32_16x16x32_bf16 v[84:87], v[120:123], v[68:71], v[84:87]\n"
"v_mul_f32_e32 v208, v208, v255\n"
"v_mul_f32_e32 v209, v209, v255\n"
"v_mul_f32_e32 v210, v210, v255\n"
"v_mul_f32_e32 v211, v211, v255\n"
"v_cvt_pk_bf16_f32 v68, v196, v197\n"
"v_cvt_pk_bf16_f32 v69, v198, v199\n"
"v_cvt_pk_bf16_f32 v70, v208, v209\n"
"v_cvt_pk_bf16_f32 v71, v210, v211\n"
"ds_write_b128 v7, v[68:71] offset:58368\n"
".Lmk_a2:\n"
"s_waitcnt vmcnt(15)\n"
"; @A G1 2.0\n"
"v_mfma_f32_16x16x32_bf16 v[124:127], a[0:3], v[52:55], 0\n"
"v_mfma_f32_16x16x32_bf16 v[124:127], a[4:7], v[44:47], v[124:127]\n"
"s_add_u32 s98, s62, 0x800\n"
"v_mfma_f32_16x16x32_bf16 v[124:127], a[8:11], v[188:191], v[124:127]\n"
"s_add_u32 s96, s54, 0x8000\n"
"buffer_load_dwordx4 v[128:131], v0, s[4:7], s96 offen sc1\n"
"v_mfma_f32_16x16x32_bf16 v[124:127], a[12:15], v[144:147], v[124:127]\n"
"s_waitcnt vmcnt(11)\n"
"v_mfma_f32_16x16x32_bf16 v[124:127], a[16:19], v[152:155], v[124:127]\n"
"buffer_load_dword v255, v10, s[4:7], s98 offen sc1\n"
"v_mfma_f32_16x16x32_bf16 v[124:127], a[20:23], v[184:187], v[124:127]\n"
"s_waitcnt lgkmcnt(2)\n"
"v_add_f32_e32 v80, v80, v216\n"
"v_add_f32_e32 v81, v81, v217\n"
"v_mfma_f32_16x16x32_bf16 v[124:127], a[24:27], v[160:163], v[124:127]\n"
"v_add_f32_e32 v82, v82, v218\n"
"v_add_f32_e32 v83, v83, v219\n"
"v_mfma_f32_16x16x32_bf16 v[124:127], a[28:31], v[92:95], v[124:127]\n"
"buffer_load_dwordx4 v[132:135], v0, s[4:7], s96 offen offset:1024 sc1\n"
"v_mfma_f32_16x16x32_bf16 v[148:151], a[60:63], v[92:95], 0\n"
"buffer_load_dwordx4 a[164:167], v0, s[4:7], s97 offen offset:1024 sc1\n"
"v_mfma_f32_16x16x32_bf16 v[148:151], a[56:59], v[160:163], v[148:151]\n"
"buffer_load_dword v254, v10, s[4:7], s98 offen offset:256 sc1\n"
"v_mfma_f32_16x16x32_bf16 v[148:151], a[52:55], v[184:187], v[148:151]\n"
"v_add_f32_e32 v84, v84, v220\n"
"v_add_f32_e32 v85, v85, v221\n"
"v_mfma_f32_16x16x32_bf16 v[148:151], a[48:51], v[152:155], v[148:151]\n"
"v_add_f32_e32 v86, v86, v222\n"
"v_add_f32_e32 v87, v87, v223\n"
"v_mfma_f32_16x16x32_bf16 v[148:151], a[44:47], v[144:147], v[148:151]\n"
"buffer_load_dwordx4 v[164:167], v0, s[4:7], s96 offen offset:2048 sc1\n"
"v_mfma_f32_16x16x32_bf16 v[148:151], a[40:43], v[188:191], v[148:151]\n"
"ds_write_b128 v3, v[124:127]\n"
"v_mfma_f32_16x16x32_bf16 v[148:151], a[36:39], v[44:47], v[148:151]\n"
"buffer_load_dword v252, v10, s[4:7], s98 offen offset:512 sc1\n"
"v_mfma_f32_16x16x32_bf16 v[148:151], a[32:35], v[52:55], v[148:151]\n"
"v_mul_f32_e32 v80, v80, v253\n"
"v_mul_f32_e32 v81, v81, v253\n"
"v_mfma_f32_16x16x32_bf16 v[124:127], a[64:67], v[52:55], 0\n"
"v_mul_f32_e32 v82, v82, v253\n"
"v_mul_f32_e32 v83, v83, v253\n"
"v_mfma_f32_16x16x32_bf16 v[124:127], a[68:71], v[44:47], v[124:127]\n"
"buffer_load_dwordx4 v[168:171], v0, s[4:7], s96 offen offset:3072 sc1\n"
"v_mfma_f32_16x16x32_bf16 v[124:127], a[72:75], v[188:191], v[124:127]\n"
"buffer_load_dword v251, v10, s[4:7], s98 offen offset:768 sc1\n"
"v_mfma_f32_16x16x32_bf16 v[124:127], a[76:79], v[144:147], v[124:127]\n"
"v_mul_f32_e32 v84, v84, v253\n"
"v_mul_f32_e32 v85, v85, v253\n"
"v_mfma_f32_16x16x32_bf16 v[124:127], a[80:83], v[152:155], v[124:127]\n"
"v_mul_f32_e32 v86, v86, v253\n"
"v_mul_f32_e32 v87, v87, v253\n"
"v_mfma_f32_16x16x32_bf16 v[124:127], a[84:87], v[184:187], v[124:127]\n"
"ds_write_b128 v3, v[148:151] offset:1024\n"
"v_mfma_f32_16x16x32_bf16 v[124:127], a[88:91], v[160:163], v[124:127]\n"
"buffer_load_dwordx4 v[148:151], v1, s[4:7], s96 offen sc1\n"
"v_mfma_f32_16x16x32_bf16 v[124:127], a[92:95], v[92:95], v[124:127]\n"
"buffer_load_dword v253, v10, s[4:7], s98 offen offset:1024 sc1\n"
"v_mfma_f32_16x16x32_bf16 v[172:175], a[124:127], v[92:95], 0\n"
"v_cvt_pk_bf16_f32 v180, v80, v81\n"
"v_cvt_pk_bf16_f32 v181, v82, v83\n"
"v_mfma_f32_16x16x32_bf16 v[172:175], a[120:123], v[160:163], v[172:175]\n"
"v_cvt_pk_bf16_f32 v182, v84, v85\n"
"v_cvt_pk_bf16_f32 v183, v86, v87\n"
"v_mfma_f32_16x16x32_bf16 v[172:175], a[116:119], v[184:187], v[172:175]\n"
"buffer_load_dword v250, v10, s[4:7], s98 offen offset:1280 sc1\n"
"v_mfma_f32_16x16x32_bf16 v[172:175], a[112:115], v[152:155], v[172:175]\n"
"buffer_load_dwordx4 v[80:83], v1, s[4:7], s96 offen offset:1024 sc1\n"
"v_mfma_f32_16x16x32_bf16 v[172:175], a[108:111], v[144:147], v[172:175]\n"
"buffer_load_dwordx4 a[168:171], v0, s[4:7], s97 offen offset:2048 sc1\n"
"v_mfma_f32_16x16x32_bf16 v[172:175], a[104:107], v[188:191], v[172:175]\n"
"ds_write_b128 v3, v[124:127] offset:2048\n"
"v_mfma_f32_16x16x32_bf16 v[172:175], a[100:103], v[44:47], v[172:175]\n"
"buffer_load_dword v249, v10, s[4:7], s98 offen offset:1536 sc1\n"
"v_mfma_f32_16x16x32_bf16 v[172:175], a[96:99], v[52:55], v[172:175]\n"
"ds_write_b128 v2, v[180:183] offset:50176\n"
"v_mfma_f32_16x16x32_bf16 v[84:87], v[28:31], v[52:55], 0\n"
"buffer_load_dwordx4 v[124:127], v1, s[4:7], s96 offen offset:2048 sc1\n"
"s_add_u32 s95, s64, 0x2000\n"
"v_mfma_f32_16x16x32_bf16 v[84:87], v[40:43], v[44:47], v[84:87]\n"
"buffer_load_dword v248, v10, s[4:7], s98 offen offset:1792 sc1\n"
"v_mfma_f32_16x16x32_bf16 v[84:87], v[24:27], v[188:191], v[84:87]\n"
"buffer_store_dwordx4 v[212:215], v8, s[4:7], s95 offen offset:1024 sc1\n"
"v_mfma_f32_16x16x32_bf16 v[84:87], v[72:75], v[144:147], v[84:87]\n"
"v_mfma_f32_16x16x32_bf16 v[84:87], v[32:35], v[152:155], v[84:87]\n"
"v_mfma_f32_16x16x32_bf16 v[84:87], v[64:67], v[184:187], v[84:87]\n"
"buffer_load_dwordx4 v[180:183], v1, s[4:7], s96 offen offset:3072 sc1\n"
"v_mfma_f32_16x16x32_bf16 v[84:87], v[60:63], v[160:163], v[84:87]\n"
"ds_write_b128 v3, v[172:175] offset:3072\n"
"v_mfma_f32_16x16x32_bf16 v[84:87], v[36:39], v[92:95], v[84:87]\n"
"v_mfma_f32_16x16x32_bf16 v[172:175], v[100:103], v[92:95], 0\n"
"v_mfma_f32_16x16x32_bf16 v[172:175], v[76:79], v[160:163], v[172:175]\n"
"v_mfma_f32_16x16x32_bf16 v[172:175], v[140:143], v[184:187], v[172:175]\n"
"v_mfma_f32_16x16x32_bf16 v[172:175], v[136:139], v[152:155], v[172:175]\n"
"v_mfma_f32_16x16x32_bf16 v[172:175], v[56:59], v[144:147], v[172:175]\n"
"v_mfma_f32_16x16x32_bf16 v[172:175], v[112:115], v[188:191], v[172:175]\n"
"s_nop 1\n"
"ds_write_b128 v3, v[84:87] offset:4096\n"
"v_mfma_f32_16x16x32_bf16 v[172:175], v[108:111], v[44:47], v[172:175]\n"
"v_mfma_f32_16x16x32_bf16 v[172:175], v[120:123], v[52:55], v[172:175]\n"
"s_waitcnt vmcnt(24)\n"
"; @A G1 2.1\n"
"v_mfma_f32_16x16x32_bf16 v[44:47], a[0:3], v[156:159], 0\n"
"buffer_load_dwordx4 a[172:175], v0, s[4:7], s97 offen offset:3072 sc1\n"
"v_mfma_f32_16x16x32_bf16 v[44:47], a[4:7], v[48:51], v[44:47]\n"
"v_mfma_f32_16x16x32_bf16 v[44:47], a[8:11], v[104:107], v[44:47]\n"
"s_add_u32 s98, s55, 0x8000\n"
"buffer_load_dwordx4 v[52:55], v0, s[4:7], s98 offen sc1\n"
"v_mfma_f32_16x16x32_bf16 v[44:47], a[12:15], v[176:179], v[44:47]\n"
"s_waitcnt vmcnt(21)\n"
"v_mfma_f32_16x16x32_bf16 v[44:47], a[16:19], v[88:91], v[44:47]\n"
"v_mfma_f32_16x16x32_bf16 v[44:47], a[20:23], v[116:119], v[44:47]\n"
"ds_write_b128 v3, v[172:175] offset:5120\n"
"v_mfma_f32_16x16x32_bf16 v[44:47], a[24:27], v[204:207], v[44:47]\n"
"v_mfma_f32_16x16x32_bf16 v[44:47], a[28:31], v[96:99], v[44:47]\n"
"buffer_load_dwordx4 v[84:87], v0, s[4:7], s98 offen offset:1024 sc1\n"
"v_mfma_f32_16x16x32_bf16 v[92:95], a[60:63], v[96:99], 0\n"
"s_add_u32 s96, s64, 0x6000\n"
"v_mfma_f32_16x16x32_bf16 v[92:95], a[56:59], v[204:207], v[92:95]\n"
"v_mfma_f32_16x16x32_bf16 v[92:95], a[52:55], v[116:119], v[92:95]\n"
"buffer_store_dwordx4 v[68:71], v7, s[4:7], s96 offen offset:1024 sc1\n"
"v_mfma_f32_16x16x32_bf16 v[92:95], a[48:51], v[88:91], v[92:95]\n"
"v_mfma_f32_16x16x32_bf16 v[92:95], a[44:47], v[176:179], v[92:95]\n"
"buffer_load_dwordx4 v[68:71], v0, s[4:7], s98 offen offset:2048 sc1\n"
"v_mfma_f32_16x16x32_bf16 v[92:95], a[40:43], v[104:107], v[92:95]\n"
"ds_write_b128 v3, v[44:47] offset:6144\n"
"v_mfma_f32_16x16x32_bf16 v[92:95], a[36:39], v[48:51], v[92:95]\n"
"v_mfma_f32_16x16x32_bf16 v[92:95], a[32:35], v[156:159], v[92:95]\n"
"v_mfma_f32_16x16x32_bf16 v[44:47], a[64:67], v[156:159], 0\n"
"v_mfma_f32_16x16x32_bf16 v[44:47], a[68:71], v[48:51], v[44:47]\n"
"buffer_load_dwordx4 v[144:147], v0, s[4:7], s98 offen offset:3072 sc1\n"
"v_mfma_f32_16x16x32_bf16 v[44:47], a[72:75], v[104:107], v[44:47]\n"
"v_mfma_f32_16x16x32_bf16 v[44:47], a[76:79], v[176:179], v[44:47]\n"
"v_mfma_f32_16x16x32_bf16 v[44:47], a[80:83], v[88:91], v[44:47]\n"
"buffer_load_dwordx4 a[176:179], v1, s[4:7], s97 offen sc1\n"
"v_mfma_f32_16x16x32_bf16 v[44:47], a[84:87], v[116:119], v[44:47]\n"
"ds_write_b128 v3, v[92:95] offset:7168\n"
"v_mfma_f32_16x16x32_bf16 v[44:47], a[88:91], v[204:207], v[44:47]\n"
"buffer_load_dwordx4 v[92:95], v1, s[4:7], s98 offen sc1\n"
"v_mfma_f32_16x16x32_bf16 v[44:47], a[92:95], v[96:99], v[44:47]\n"
"v_mfma_f32_16x16x32_bf16 v[152:155], a[124:127], v[96:99], 0\n"
"v_mfma_f32_16x16x32_bf16 v[152:155], a[120:123], v[204:207], v[152:155]\n"
"v_mfma_f32_16x16x32_bf16 v[152:155], a[116:119], v[116:119], v[152:155]\n"
"v_mfma_f32_16x16x32_bf16 v[152:155], a[112:115], v[88:91], v[152:155]\n"
"buffer_load_dwordx4 v[160:163], v1, s[4:7], s98 offen offset:1024 sc1\n"
"v_mfma_f32_16x16x32_bf16 v[152:155], a[108:111], v[176:179], v[152:155]\n"
"v_mfma_f32_16x16x32_bf16 v[152:155], a[104:107], v[104:107], v[152:155]\n"
"s_nop 0\n"
"ds_write_b128 v3, v[44:47] offset:8192\n"
"v_mfma_f32_16x16x32_bf16 v[152:155], a[100:103], v[48:51], v[152:155]\n"
"v_mfma_f32_16x16x32_bf16 v[152:155], a[96:99], v[156:159], v[152:155]\n"
"v_mfma_f32_16x16x32_bf16 v[44:47], v[28:31], v[156:159], 0\n"
"buffer_load_dwordx4 v[172:175], v1, s[4:7], s98 offen offset:2048 sc1\n"
"v_mfma_f32_16x16x32_bf16 v[44:47], v[40:43], v[48:51], v[44:47]\n"
"v_mfma_f32_16x16x32_bf16 v[44:47], v[24:27], v[104:107], v[44:47]\n"
"v_mfma_f32_16x16x32_bf16 v[44:47], v[72:75], v[176:179], v[44:47]\n"
"v_mfma_f32_16x16x32_bf16 v[44:47], v[32:35], v[88:91], v[44:47]\n"
"s_waitcnt vmcnt(12)\n"
"v_add_f32_e32 v255, v255, v253\n"
"v_add_f32_e32 v254, v254, v250\n"
"v_mfma_f32_16x16x32_bf16 v[44:47], v[64:67], v[116:119], v[44:47]\n"
"buffer_load_dwordx4 v[184:187], v1, s[4:7], s98 offen offset:3072 sc1\n"
"v_mfma_f32_16x16x32_bf16 v[44:47], v[60:63], v[204:207], v[44:47]\n"
"ds_write_b128 v3, v[152:155] offset:9216\n"
"v_mfma_f32_16x16x32_bf16 v[44:47], v[36:39], v[96:99], v[44:47]\n"
"v_add_f32_e32 v252, v252, v249\n"
"v_add_f32_e32 v251, v251, v248\n"
"v_mfma_f32_16x16x32_bf16 v[152:155], v[100:103], v[96:99], 0\n"
"buffer_load_dwordx4 a[180:183], v1, s[4:7], s97 offen offset:1024 sc1\n"
"v_mfma_f32_16x16x32_bf16 v[152:155], v[76:79], v[204:207], v[152:155]\n"
"v_add_f32_e32 v255, v255, v252\n"
"v_add_f32_e32 v254, v254, v251\n"
"v_mfma_f32_16x16x32_bf16 v[152:155], v[140:143], v[116:119], v[152:155]\n"
"v_add_f32_e32 v255, v255, v254\n"
"v_mov_b32_e32 v254, v255\n"
"v_mfma_f32_16x16x32_bf16 v[152:155], v[136:139], v[88:91], v[152:155]\n"
"s_nop 0\n"
"v_permlane32_swap_b32_e32 v255, v254\n"
"v_add_f32_e32 v255, v255, v254\n"
"v_mfma_f32_16x16x32_bf16 v[152:155], v[56:59], v[176:179], v[152:155]\n"
"v_mul_f32_e32 v255, 0x3a800000, v255\n"
"v_mov_b32_e32 v254, v255\n"
"v_mfma_f32_16x16x32_bf16 v[152:155], v[112:115], v[104:107], v[152:155]\n"
"ds_write_b128 v3, v[44:47] offset:10240\n"
"v_mfma_f32_16x16x32_bf16 v[152:155], v[108:111], v[48:51], v[152:155]\n"
"v_permlane16_swap_b32_e32 v255, v254\n"
"v_cvt_pk_bf16_f32 v253, -v255, 0\n"
"v_mfma_f32_16x16x32_bf16 v[152:155], v[120:123], v[156:159], v[152:155]\n"
"v_fma_f32 v254, -v255, v255, v254\n"
"v_lshlrev_b32_e32 v253, 16, v253\n"
"; @A G1 2.2\n"
"v_mfma_f32_16x16x32_bf16 v[44:47], a[0:3], v[128:131], 0\n"
"v_max_f32_e32 v254, 0, v254\n"
"v_cvt_pk_bf16_f32 v254, v254, 0\n"
"v_mfma_f32_16x16x32_bf16 v[44:47], a[4:7], v[132:135], v[44:47]\n"
"v_lshlrev_b32_e32 v254, 16, v254\n"
"v_add_f32_e32 v254, 0x3727c5ac, v254\n"
"v_mfma_f32_16x16x32_bf16 v[44:47], a[8:11], v[164:167], v[44:47]\n"
"v_cvt_pk_bf16_f32 v254, v254, 0\n"
"v_lshlrev_b32_e32 v254, 16, v254\n"
"v_mfma_f32_16x16x32_bf16 v[44:47], a[12:15], v[168:171], v[44:47]\n"
"s_waitcnt vmcnt(12)\n"
"v_sqrt_f32_e32 v254, v254\n"
"v_mfma_f32_16x16x32_bf16 v[44:47], a[16:19], v[148:151], v[44:47]\n"
"s_mov_b32 s98, .Lmk_a4-.Lmk_g86\n"
"s_getpc_b64 s[84:85]\n"
".Lmk_g86:\n"
"s_add_u32 s84, s84, s98\n"
"s_addc_u32 s85, s85, 0\n"
"s_and_b32 s84, s84, -16\n"
"v_mfma_f32_16x16x32_bf16 v[44:47], a[20:23], v[80:83], v[44:47]\n"
"ds_write_b128 v3, v[152:155] offset:11264\n"
"v_mfma_f32_16x16x32_bf16 v[44:47], a[24:27], v[124:127], v[44:47]\n"
"s_and_b32 s85, s85, 0xffff\n"
"s_sub_u32 s98, .Lmk_end-.Lmk_g86, s98\n"
"s_min_u32 s86, s98, 0x4000\n"
"s_mov_b32 s87, 0x20000\n"
"v_mfma_f32_16x16x32_bf16 v[44:47], a[28:31], v[180:183], v[44:47]\n"
"s_mov_b32 m0, 0x27800\n"
"s_nop 0\n"
"buffer_load_dwordx4 v0, s[84:87], s99 offen lds\n"
"v_mfma_f32_16x16x32_bf16 v[48:51], a[60:63], v[180:183], 0\n"
"v_cvt_pk_bf16_f32 v254, v254, 0\n"
"v_lshlrev_b32_e32 v254, 16, v254\n"
"v_mfma_f32_16x16x32_bf16 v[48:51], a[56:59], v[124:127], v[48:51]\n"
"v_rcp_f32_e32 v255, v254\n"
"v_mfma_f32_16x16x32_bf16 v[48:51], a[52:55], v[80:83], v[48:51]\n"
"s_add_u32 s98, s52, 0\n"
"buffer_load_dwordx4 v[88:91], v0, s[4:7], s98 offen sc1\n"
"v_mfma_f32_16x16x32_bf16 v[48:51], a[48:51], v[148:151], v[48:51]\n"
"s_waitcnt lgkmcnt(0)\n"
"s_barrier\n"
"v_mfma_f32_16x16x32_bf16 v[48:51], a[44:47], v[168:171], v[48:51]\n"
"buffer_load_dwordx4 a[184:187], v1, s[4:7], s97 offen offset:2048 sc1\n"
"v_mfma_f32_16x16x32_bf16 v[48:51], a[40:43], v[164:167], v[48:51]\n"
"ds_write_b128 v3, v[44:47] offset:12288\n"
"v_mfma_f32_16x16x32_bf16 v[48:51], a[36:39], v[132:135], v[48:51]\n"
"ds_read_b128 v[44:47], v4\n"
"v_mul_f32_e32 v254, 0x3e8293ee, v255\n"
"v_mfma_f32_16x16x32_bf16 v[48:51], a[32:35], v[128:131], v[48:51]\n"
"buffer_load_dwordx4 v[96:99], v0, s[4:7], s98 offen offset:1024 sc1\n"
"v_mfma_f32_16x16x32_bf16 v[104:107], a[64:67], v[128:131], 0\n"
"ds_read_b128 v[116:119], v9 offset:128\n"
"ds_read_b128 v[152:155], v5\n"
"v_mfma_f32_16x16x32_bf16 v[104:107], a[68:71], v[132:135], v[104:107]\n"
"v_mfma_f32_16x16x32_bf16 v[104:107], a[72:75], v[164:167], v[104:107]\n"
"v_mfma_f32_16x16x32_bf16 v[104:107], a[76:79], v[168:171], v[104:107]\n"
"ds_read_b128 v[156:159], v4 offset:1024\n"
"ds_read_b128 v[176:179], v9 offset:192\n"
"v_mfma_f32_16x16x32_bf16 v[104:107], a[80:83], v[148:151], v[104:107]\n"
"buffer_load_dwordx4 v[188:191], v0, s[4:7], s98 offen offset:2048 sc1\n"
"v_mfma_f32_16x16x32_bf16 v[104:107], a[84:87], v[80:83], v[104:107]\n"
"ds_write_b128 v3, v[48:51] offset:13312\n"
"v_mfma_f32_16x16x32_bf16 v[104:107], a[88:91], v[124:127], v[104:107]\n"
"ds_read_b128 v[48:51], v5 offset:1024\n"
"s_waitcnt lgkmcnt(4)\n"
"v_mfma_f32_16x16x32_bf16 v[104:107], a[92:95], v[180:183], v[104:107]\n"
"v_fmac_f32_e32 v44, v253, v116\n"
"v_fmac_f32_e32 v45, v253, v117\n"
"v_mfma_f32_16x16x32_bf16 v[192:195], a[124:127], v[180:183], 0\n"
"v_fmac_f32_e32 v46, v253, v118\n"
"v_fmac_f32_e32 v47, v253, v119\n"
"v_mfma_f32_16x16x32_bf16 v[192:195], a[120:123], v[124:127], v[192:195]\n"
"buffer_load_dwordx4 v[116:119], v0, s[4:7], s98 offen offset:3072 sc1\n"
"v_mfma_f32_16x16x32_bf16 v[192:195], a[116:119], v[80:83], v[192:195]\n"
"ds_read_b128 v[196:199], v4 offset:2048\n"
"ds_read_b128 v[200:203], v9 offset:256\n"
"v_mfma_f32_16x16x32_bf16 v[192:195], a[112:115], v[148:151], v[192:195]\n"
"ds_read_b128 v[204:207], v5 offset:2048\n"
"v_add_f32_e32 v44, v44, v152\n"
"v_mfma_f32_16x16x32_bf16 v[192:195], a[108:111], v[168:171], v[192:195]\n"
"v_add_f32_e32 v45, v45, v153\n"
"v_add_f32_e32 v46, v46, v154\n"
"v_mfma_f32_16x16x32_bf16 v[192:195], a[104:107], v[164:167], v[192:195]\n"
"ds_write_b128 v3, v[104:107] offset:14336\n"
"v_mfma_f32_16x16x32_bf16 v[192:195], a[100:103], v[132:135], v[192:195]\n"
"buffer_load_dwordx4 v[104:107], v1, s[4:7], s98 offen sc1\n"
"v_mfma_f32_16x16x32_bf16 v[192:195], a[96:99], v[128:131], v[192:195]\n"
"s_waitcnt lgkmcnt(4)\n"
"v_add_f32_e32 v47, v47, v155\n"
"v_fmac_f32_e32 v156, v253, v176\n"
"v_mfma_f32_16x16x32_bf16 v[152:155], v[28:31], v[128:131], 0\n"
"buffer_load_dwordx4 a[188:191], v1, s[4:7], s97 offen offset:3072 sc1\n"
"v_mfma_f32_16x16x32_bf16 v[152:155], v[40:43], v[132:135], v[152:155]\n"
"ds_read_b128 v[208:211], v4 offset:3072\n"
"ds_read_b128 v[212:215], v9 offset:320\n"
"v_mfma_f32_16x16x32_bf16 v[152:155], v[24:27], v[164:167], v[152:155]\n"
"s_waitcnt vmcnt(16)\n"
"s_add_u32 s97, s68, 0x1000\n"
"ds_read_b128 v[216:219], v5 offset:3072\n"
"v_mfma_f32_16x16x32_bf16 v[152:155], v[72:75], v[168:171], v[152:155]\n"
"buffer_load_dwordx4 v[220:223], v1, s[4:7], s98 offen offset:1024 sc1\n"
"v_mfma_f32_16x16x32_bf16 v[152:155], v[32:35], v[148:151], v[152:155]\n"
"buffer_store_dwordx2 v[18:19], v11, s[4:7], s97 offen offset:64 sc1\n"
"v_mfma_f32_16x16x32_bf16 v[152:155], v[64:67], v[80:83], v[152:155]\n"
"ds_write_b128 v3, v[192:195] offset:15360\n"
"v_mfma_f32_16x16x32_bf16 v[152:155], v[60:63], v[124:127], v[152:155]\n"
"v_fmac_f32_e32 v157, v253, v177\n"
"v_fmac_f32_e32 v158, v253, v178\n"
"v_mfma_f32_16x16x32_bf16 v[152:155], v[36:39], v[180:183], v[152:155]\n"
"v_fmac_f32_e32 v159, v253, v179\n"
"v_add_f32_e32 v156, v156, v48\n"
"v_mfma_f32_16x16x32_bf16 v[176:179], v[100:103], v[180:183], 0\n"
"buffer_load_dwordx4 v[180:183], v1, s[4:7], s98 offen offset:2048 sc1\n"
"v_mfma_f32_16x16x32_bf16 v[176:179], v[76:79], v[124:127], v[176:179]\n"
"ds_read_b128 v[124:127], v4 offset:4096\n"
"ds_read_b128 v[192:195], v9\n"
"v_mfma_f32_16x16x32_bf16 v[176:179], v[140:143], v[80:83], v[176:179]\n"
"ds_read_b128 v[80:83], v5 offset:4096\n"
"v_add_f32_e32 v157, v157, v49\n"
"v_mfma_f32_16x16x32_bf16 v[176:179], v[136:139], v[148:151], v[176:179]\n"
"s_waitcnt lgkmcnt(8)\n"
"v_add_f32_e32 v158, v158, v50\n"
"v_add_f32_e32 v159, v159, v51\n"
"v_mfma_f32_16x16x32_bf16 v[176:179], v[56:59], v[168:171], v[176:179]\n"
"v_fmac_f32_e32 v196, v253, v200\n"
"v_fmac_f32_e32 v197, v253, v201\n"
"v_mfma_f32_16x16x32_bf16 v[176:179], v[112:115], v[164:167], v[176:179]\n"
"buffer_load_dwordx4 v[48:51], v1, s[4:7], s98 offen offset:3072 sc1\n"
"v_mfma_f32_16x16x32_bf16 v[176:179], v[108:111], v[132:135], v[176:179]\n"
"ds_write_b128 v3, v[152:155] offset:16384\n"
"v_mfma_f32_16x16x32_bf16 v[176:179], v[120:123], v[128:131], v[176:179]\n"
"s_waitcnt vmcnt(18)\n"
"ds_read_b128 v[128:131], v4 offset:5120\n"
"ds_read_b128 v[132:135], v9 offset:64\n"
"; @A G1 2.3\n"
"v_mfma_f32_16x16x32_bf16 v[44:47], a[0:3], v[52:55], v[44:47]\n"
"ds_read_b128 v[148:151], v5 offset:5120\n"
"v_fmac_f32_e32 v198, v253, v202\n"
"v_mfma_f32_16x16x32_bf16 v[44:47], a[4:7], v[84:87], v[44:47]\n"
"v_fmac_f32_e32 v199, v253, v203\n"
"v_add_f32_e32 v196, v196, v204\n"
"v_mfma_f32_16x16x32_bf16 v[44:47], a[8:11], v[68:71], v[44:47]\n"
"s_add_u32 s98, s53, 0\n"
"buffer_load_dwordx4 v[152:155], v0, s[4:7], s98 offen sc1\n"
"v_mfma_f32_16x16x32_bf16 v[44:47], a[12:15], v[144:147], v[44:47]\n"
"s_waitcnt vmcnt(14)\n"
"s_add_u32 s97, s70, 0x4000\n"
"v_add_f32_e32 v197, v197, v205\n"
"v_mfma_f32_16x16x32_bf16 v[44:47], a[16:19], v[92:95], v[44:47]\n"
"buffer_load_dwordx4 a[192:195], v0, s[4:7], s97 offen sc1\n"
"v_mfma_f32_16x16x32_bf16 v[44:47], a[20:23], v[160:163], v[44:47]\n"
"ds_write_b128 v3, v[176:179] offset:17408\n"
"v_mfma_f32_16x16x32_bf16 v[44:47], a[24:27], v[172:175], v[44:47]\n"
"s_waitcnt lgkmcnt(9)\n"
"v_add_f32_e32 v198, v198, v206\n"
"v_add_f32_e32 v199, v199, v207\n"
"v_mfma_f32_16x16x32_bf16 v[44:47], a[28:31], v[184:187], v[44:47]\n"
"buffer_load_dwordx4 v[164:167], v0, s[4:7], s98 offen offset:1024 sc1\n"
"v_mfma_f32_16x16x32_bf16 v[156:159], a[60:63], v[184:187], v[156:159]\n"
"v_fmac_f32_e32 v208, v253, v212\n"
"v_fmac_f32_e32 v209, v253, v213\n"
"v_mfma_f32_16x16x32_bf16 v[156:159], a[56:59], v[172:175], v[156:159]\n"
"v_fmac_f32_e32 v210, v253, v214\n"
"v_fmac_f32_e32 v211, v253, v215\n"
"v_mfma_f32_16x16x32_bf16 v[156:159], a[52:55], v[160:163], v[156:159]\n"
"s_waitcnt lgkmcnt(0)\n"
"s_barrier\n"
"v_mfma_f32_16x16x32_bf16 v[156:159], a[48:51], v[92:95], v[156:159]\n"
"ds_read_b128 v[168:171], v6\n"
"ds_read_b128 v[176:179], v6 offset:1024\n"
"v_mfma_f32_16x16x32_bf16 v[156:159], a[44:47], v[144:147], v[156:159]\n"
"buffer_load_dwordx4 v[200:203], v0, s[4:7], s98 offen offset:2048 sc1\n"
"v_mfma_f32_16x16x32_bf16 v[156:159], a[40:43], v[68:71], v[156:159]\n"
"v_add_f32_e32 v208, v208, v216\n"
"v_add_f32_e32 v209, v209, v217\n"
"v_mfma_f32_16x16x32_bf16 v[156:159], a[36:39], v[84:87], v[156:159]\n"
"v_add_f32_e32 v210, v210, v218\n"
"v_add_f32_e32 v211, v211, v219\n"
"v_mfma_f32_16x16x32_bf16 v[156:159], a[32:35], v[52:55], v[156:159]\n"
"v_fmac_f32_e32 v124, v253, v192\n"
"v_fmac_f32_e32 v125, v253, v193\n"
"v_mfma_f32_16x16x32_bf16 v[196:199], a[64:67], v[52:55], v[196:199]\n"
"v_fmac_f32_e32 v126, v253, v194\n"
"v_fmac_f32_e32 v127, v253, v195\n"
"v_mfma_f32_16x16x32_bf16 v[196:199], a[68:71], v[84:87], v[196:199]\n"
"buffer_load_dwordx4 v[192:195], v0, s[4:7], s98 offen offset:3072 sc1\n"
"v_mfma_f32_16x16x32_bf16 v[196:199], a[72:75], v[68:71], v[196:199]\n"
"v_add_f32_e32 v124, v124, v80\n"
"v_add_f32_e32 v125, v125, v81\n"
"v_mfma_f32_16x16x32_bf16 v[196:199], a[76:79], v[144:147], v[196:199]\n"
"v_add_f32_e32 v126, v126, v82\n"
"v_add_f32_e32 v127, v127, v83\n"
"v_mfma_f32_16x16x32_bf16 v[196:199], a[80:83], v[92:95], v[196:199]\n"
"s_waitcnt lgkmcnt(0)\n"
"v_fmac_f32_e32 v128, v253, v132\n"
"v_fmac_f32_e32 v129, v253, v133\n"
"v_mfma_f32_16x16x32_bf16 v[196:199], a[84:87], v[160:163], v[196:199]\n"
"v_fmac_f32_e32 v130, v253, v134\n"
"v_fmac_f32_e32 v131, v253, v135\n"
"v_mfma_f32_16x16x32_bf16 v[196:199], a[88:91], v[172:175], v[196:199]\n"
"buffer_load_dwordx4 v[80:83], v1, s[4:7], s98 offen sc1\n"
"v_mfma_f32_16x16x32_bf16 v[196:199], a[92:95], v[184:187], v[196:199]\n"
"ds_read_b128 v[132:135], v6 offset:2048\n"
"ds_read_b128 v[204:207], v6 offset:3072\n"
"v_mfma_f32_16x16x32_bf16 v[208:211], a[124:127], v[184:187], v[208:211]\n"
"buffer_load_dwordx4 a[196:199], v0, s[4:7], s97 offen offset:1024 sc1\n"
"v_mfma_f32_16x16x32_bf16 v[208:211], a[120:123], v[172:175], v[208:211]\n"
"v_add_f32_e32 v128, v128, v148\n"
"v_add_f32_e32 v129, v129, v149\n"
"v_mfma_f32_16x16x32_bf16 v[208:211], a[116:119], v[160:163], v[208:211]\n"
"v_add_f32_e32 v130, v130, v150\n"
"v_add_f32_e32 v131, v131, v151\n"
"v_mfma_f32_16x16x32_bf16 v[208:211], a[112:115], v[92:95], v[208:211]\n"
"buffer_load_dwordx4 v[148:151], v1, s[4:7], s98 offen offset:1024 sc1\n"
"v_mfma_f32_16x16x32_bf16 v[208:211], a[108:111], v[144:147], v[208:211]\n"
"v_add_f32_e32 v44, v44, v168\n"
"v_add_f32_e32 v45, v45, v169\n"
"v_mfma_f32_16x16x32_bf16 v[208:211], a[104:107], v[68:71], v[208:211]\n"
"v_add_f32_e32 v46, v46, v170\n"
"v_add_f32_e32 v47, v47, v171\n"
"v_mfma_f32_16x16x32_bf16 v[208:211], a[100:103], v[84:87], v[208:211]\n"
"v_add_f32_e32 v156, v156, v176\n"
"v_add_f32_e32 v157, v157, v177\n"
"v_mfma_f32_16x16x32_bf16 v[208:211], a[96:99], v[52:55], v[208:211]\n"
"v_add_f32_e32 v158, v158, v178\n"
"v_add_f32_e32 v159, v159, v179\n"
"v_mfma_f32_16x16x32_bf16 v[124:127], v[28:31], v[52:55], v[124:127]\n"
"buffer_load_dwordx4 v[168:171], v1, s[4:7], s98 offen offset:2048 sc1\n"
"v_mfma_f32_16x16x32_bf16 v[124:127], v[40:43], v[84:87], v[124:127]\n"
"v_mul_f32_e32 v44, v44, v255\n"
"v_mul_f32_e32 v45, v45, v255\n"
"v_mfma_f32_16x16x32_bf16 v[124:127], v[24:27], v[68:71], v[124:127]\n"
"v_mul_f32_e32 v46, v46, v255\n"
"v_mul_f32_e32 v47, v47, v255\n"
"v_mfma_f32_16x16x32_bf16 v[124:127], v[72:75], v[144:147], v[124:127]\n"
"v_mul_f32_e32 v156, v156, v255\n"
"v_mul_f32_e32 v157, v157, v255\n"
"v_mfma_f32_16x16x32_bf16 v[124:127], v[32:35], v[92:95], v[124:127]\n"
"s_waitcnt lgkmcnt(0)\n"
"v_mul_f32_e32 v158, v158, v255\n"
"v_mul_f32_e32 v159, v159, v255\n"
"v_mfma_f32_16x16x32_bf16 v[124:127], v[64:67], v[160:163], v[124:127]\n"
"buffer_load_dwordx4 v[176:179], v1, s[4:7], s98 offen offset:3072 sc1\n"
"v_mfma_f32_16x16x32_bf16 v[124:127], v[60:63], v[172:175], v[124:127]\n"
"ds_read_b128 v[212:215], v6 offset:4096\n"
"ds_read_b128 v[216:219], v6 offset:5120\n"
"v_mfma_f32_16x16x32_bf16 v[124:127], v[36:39], v[184:187], v[124:127]\n"
"v_cvt_pk_bf16_f32 v224, v44, v45\n"
"v_cvt_pk_bf16_f32 v225, v46, v47\n"
"v_mfma_f32_16x16x32_bf16 v[128:131], v[100:103], v[184:187], v[128:131]\n"
"v_cvt_pk_bf16_f32 v226, v156, v157\n"
"v_cvt_pk_bf16_f32 v227, v158, v159\n"
"v_mfma_f32_16x16x32_bf16 v[128:131], v[76:79], v[172:175], v[128:131]\n"
"ds_write_b128 v8, v[224:227] offset:17408\n"
"v_mfma_f32_16x16x32_bf16 v[128:131], v[140:143], v[160:163], v[128:131]\n"
"v_add_f32_e32 v196, v196, v132\n"
"v_add_f32_e32 v197, v197, v133\n"
"v_mfma_f32_16x16x32_bf16 v[128:131], v[136:139], v[92:95], v[128:131]\n"
"v_add_f32_e32 v198, v198, v134\n"
"v_add_f32_e32 v199, v199, v135\n"
"v_mfma_f32_16x16x32_bf16 v[128:131], v[56:59], v[144:147], v[128:131]\n"
"v_add_f32_e32 v208, v208, v204\n"
"v_add_f32_e32 v209, v209, v205\n"
"v_mfma_f32_16x16x32_bf16 v[128:131], v[112:115], v[68:71], v[128:131]\n"
"v_add_f32_e32 v210, v210, v206\n"
"v_add_f32_e32 v211, v211, v207\n"
"v_mfma_f32_16x16x32_bf16 v[128:131], v[108:111], v[84:87], v[128:131]\n"
"v_mul_f32_e32 v196, v196, v255\n"
"v_mul_f32_e32 v197, v197, v255\n"
"v_mfma_f32_16x16x32_bf16 v[128:131], v[120:123], v[52:55], v[128:131]\n"
"v_mul_f32_e32 v198, v198, v255\n"
"v_mul_f32_e32 v199, v199, v255\n"
"v_mul_f32_e32 v208, v208, v255\n"
"v_mul_f32_e32 v209, v209, v255\n"
"v_mul_f32_e32 v210, v210, v255\n"
"v_mul_f32_e32 v211, v211, v255\n"
"v_cvt_pk_bf16_f32 v44, v196, v197\n"
"v_cvt_pk_bf16_f32 v45, v198, v199\n"
"v_cvt_pk_bf16_f32 v46, v208, v209\n"
"v_cvt_pk_bf16_f32 v47, v210, v211\n"
"ds_write_b128 v7, v[44:47] offset:50176\n"
".Lmk_a3:\n"
"s_waitcnt vmcnt(16)\n"
"; @A G1 3.0\n"
"v_mfma_f32_16x16x32_bf16 v[52:55], a[0:3], v[88:91], 0\n"
"v_mfma_f32_16x16x32_bf16 v[52:55], a[4:7], v[96:99], v[52:55]\n"
"s_add_u32 s98, s62, 0\n"
"v_mfma_f32_16x16x32_bf16 v[52:55], a[8:11], v[188:191], v[52:55]\n"
"s_add_u32 s96, s54, 0\n"
"buffer_load_dwordx4 v[68:71], v0, s[4:7], s96 offen sc1\n"
"v_mfma_f32_16x16x32_bf16 v[52:55], a[12:15], v[116:119], v[52:55]\n"
"s_waitcnt vmcnt(11)\n"
"v_mfma_f32_16x16x32_bf16 v[52:55], a[16:19], v[104:107], v[52:55]\n"
"buffer_load_dword v255, v10, s[4:7], s98 offen sc1\n"
"v_mfma_f32_16x16x32_bf16 v[52:55], a[20:23], v[220:223], v[52:55]\n"
"s_waitcnt lgkmcnt(2)\n"
"v_add_f32_e32 v124, v124, v212\n"
"v_add_f32_e32 v125, v125, v213\n"
"v_mfma_f32_16x16x32_bf16 v[52:55], a[24:27], v[180:183], v[52:55]\n"
"v_add_f32_e32 v126, v126, v214\n"
"v_add_f32_e32 v127, v127, v215\n"
"v_mfma_f32_16x16x32_bf16 v[52:55], a[28:31], v[48:51], v[52:55]\n"
"buffer_load_dwordx4 v[84:87], v0, s[4:7], s96 offen offset:1024 sc1\n"
"v_mfma_f32_16x16x32_bf16 v[92:95], a[60:63], v[48:51], 0\n"
"buffer_load_dwordx4 a[200:203], v0, s[4:7], s97 offen offset:2048 sc1\n"
"v_mfma_f32_16x16x32_bf16 v[92:95], a[56:59], v[180:183], v[92:95]\n"
"buffer_load_dword v253, v10, s[4:7], s98 offen offset:256 sc1\n"
"v_mfma_f32_16x16x32_bf16 v[92:95], a[52:55], v[220:223], v[92:95]\n"
"v_add_f32_e32 v128, v128, v216\n"
"v_add_f32_e32 v129, v129, v217\n"
"v_mfma_f32_16x16x32_bf16 v[92:95], a[48:51], v[104:107], v[92:95]\n"
"v_add_f32_e32 v130, v130, v218\n"
"v_add_f32_e32 v131, v131, v219\n"
"v_mfma_f32_16x16x32_bf16 v[92:95], a[44:47], v[116:119], v[92:95]\n"
"buffer_load_dwordx4 v[132:135], v0, s[4:7], s96 offen offset:2048 sc1\n"
"v_mfma_f32_16x16x32_bf16 v[92:95], a[40:43], v[188:191], v[92:95]\n"
"ds_write_b128 v3, v[52:55]\n"
"v_mfma_f32_16x16x32_bf16 v[92:95], a[36:39], v[96:99], v[92:95]\n"
"buffer_load_dword v252, v10, s[4:7], s98 offen offset:512 sc1\n"
"v_mfma_f32_16x16x32_bf16 v[92:95], a[32:35], v[88:91], v[92:95]\n"
"v_mul_f32_e32 v124, v124, v254\n"
"v_mul_f32_e32 v125, v125, v254\n"
"v_mfma_f32_16x16x32_bf16 v[52:55], a[64:67], v[88:91], 0\n"
"v_mul_f32_e32 v126, v126, v254\n"
"v_mul_f32_e32 v127, v127, v254\n"
"v_mfma_f32_16x16x32_bf16 v[52:55], a[68:71], v[96:99], v[52:55]\n"
"buffer_load_dwordx4 v[144:147], v0, s[4:7], s96 offen offset:3072 sc1\n"
"v_mfma_f32_16x16x32_bf16 v[52:55], a[72:75], v[188:191], v[52:55]\n"
"buffer_load_dword v251, v10, s[4:7], s98 offen offset:768 sc1\n"
"v_mfma_f32_16x16x32_bf16 v[52:55], a[76:79], v[116:119], v[52:55]\n"
"v_mul_f32_e32 v128, v128, v254\n"
"v_mul_f32_e32 v129, v129, v254\n"
"v_mfma_f32_16x16x32_bf16 v[52:55], a[80:83], v[104:107], v[52:55]\n"
"v_mul_f32_e32 v130, v130, v254\n"
"v_mul_f32_e32 v131, v131, v254\n"
"v_mfma_f32_16x16x32_bf16 v[52:55], a[84:87], v[220:223], v[52:55]\n"
"ds_write_b128 v3, v[92:95] offset:1024\n"
"v_mfma_f32_16x16x32_bf16 v[52:55], a[88:91], v[180:183], v[52:55]\n"
"buffer_load_dwordx4 v[92:95], v1, s[4:7], s96 offen sc1\n"
"v_mfma_f32_16x16x32_bf16 v[52:55], a[92:95], v[48:51], v[52:55]\n"
"buffer_load_dword v254, v10, s[4:7], s98 offen offset:1024 sc1\n"
"v_mfma_f32_16x16x32_bf16 v[156:159], a[124:127], v[48:51], 0\n"
"v_cvt_pk_bf16_f32 v160, v124, v125\n"
"v_cvt_pk_bf16_f32 v161, v126, v127\n"
"v_mfma_f32_16x16x32_bf16 v[156:159], a[120:123], v[180:183], v[156:159]\n"
"v_cvt_pk_bf16_f32 v162, v128, v129\n"
"v_cvt_pk_bf16_f32 v163, v130, v131\n"
"v_mfma_f32_16x16x32_bf16 v[156:159], a[116:119], v[220:223], v[156:159]\n"
"buffer_load_dwordx4 a[204:207], v0, s[4:7], s97 offen offset:3072 sc1\n"
"v_mfma_f32_16x16x32_bf16 v[156:159], a[112:115], v[104:107], v[156:159]\n"
"buffer_load_dwordx4 v[124:127], v1, s[4:7], s96 offen offset:1024 sc1\n"
"v_mfma_f32_16x16x32_bf16 v[156:159], a[108:111], v[116:119], v[156:159]\n"
"buffer_load_dword v250, v10, s[4:7], s98 offen offset:1280 sc1\n"
"v_mfma_f32_16x16x32_bf16 v[156:159], a[104:107], v[188:191], v[156:159]\n"
"ds_write_b128 v3, v[52:55] offset:2048\n"
"v_mfma_f32_16x16x32_bf16 v[156:159], a[100:103], v[96:99], v[156:159]\n"
"ds_write_b128 v2, v[160:163] offset:17408\n"
"v_mfma_f32_16x16x32_bf16 v[156:159], a[96:99], v[88:91], v[156:159]\n"
"buffer_load_dword v249, v10, s[4:7], s98 offen offset:1536 sc1\n"
"v_mfma_f32_16x16x32_bf16 v[52:55], v[28:31], v[88:91], 0\n"
"buffer_load_dwordx4 v[128:131], v1, s[4:7], s96 offen offset:2048 sc1\n"
"v_mfma_f32_16x16x32_bf16 v[52:55], v[40:43], v[96:99], v[52:55]\n"
"buffer_store_dwordx4 v[224:227], v8, s[4:7], s64 offen offset:1024 sc1\n"
"v_mfma_f32_16x16x32_bf16 v[52:55], v[24:27], v[188:191], v[52:55]\n"
"buffer_load_dword v248, v10, s[4:7], s98 offen offset:1792 sc1\n"
"v_mfma_f32_16x16x32_bf16 v[52:55], v[72:75], v[116:119], v[52:55]\n"
"v_mfma_f32_16x16x32_bf16 v[52:55], v[32:35], v[104:107], v[52:55]\n"
"v_mfma_f32_16x16x32_bf16 v[52:55], v[64:67], v[220:223], v[52:55]\n"
"buffer_load_dwordx4 v[160:163], v1, s[4:7], s96 offen offset:3072 sc1\n"
"v_mfma_f32_16x16x32_bf16 v[52:55], v[60:63], v[180:183], v[52:55]\n"
"ds_write_b128 v3, v[156:159] offset:3072\n"
"v_mfma_f32_16x16x32_bf16 v[52:55], v[36:39], v[48:51], v[52:55]\n"
"v_mfma_f32_16x16x32_bf16 v[156:159], v[100:103], v[48:51], 0\n"
"v_mfma_f32_16x16x32_bf16 v[156:159], v[76:79], v[180:183], v[156:159]\n"
"v_mfma_f32_16x16x32_bf16 v[156:159], v[140:143], v[220:223], v[156:159]\n"
"v_mfma_f32_16x16x32_bf16 v[156:159], v[136:139], v[104:107], v[156:159]\n"
"v_mfma_f32_16x16x32_bf16 v[156:159], v[56:59], v[116:119], v[156:159]\n"
"buffer_load_dwordx4 a[208:211], v1, s[4:7], s97 offen sc1\n"
"v_mfma_f32_16x16x32_bf16 v[156:159], v[112:115], v[188:191], v[156:159]\n"
"s_nop 0\n"
"ds_write_b128 v3, v[52:55] offset:4096\n"
"v_mfma_f32_16x16x32_bf16 v[156:159], v[108:111], v[96:99], v[156:159]\n"
"v_mfma_f32_16x16x32_bf16 v[156:159], v[120:123], v[88:91], v[156:159]\n"
"s_waitcnt vmcnt(25)\n"
"; @A G1 3.1\n"
"v_mfma_f32_16x16x32_bf16 v[48:51], a[0:3], v[152:155], 0\n"
"v_mfma_f32_16x16x32_bf16 v[48:51], a[4:7], v[164:167], v[48:51]\n"
"v_mfma_f32_16x16x32_bf16 v[48:51], a[8:11], v[200:203], v[48:51]\n"
"s_add_u32 s98, s55, 0\n"
"buffer_load_dwordx4 v[52:55], v0, s[4:7], s98 offen sc1\n"
"v_mfma_f32_16x16x32_bf16 v[48:51], a[12:15], v[192:195], v[48:51]\n"
"s_waitcnt vmcnt(21)\n"
"v_mfma_f32_16x16x32_bf16 v[48:51], a[16:19], v[80:83], v[48:51]\n"
"v_mfma_f32_16x16x32_bf16 v[48:51], a[20:23], v[148:151], v[48:51]\n"
"ds_write_b128 v3, v[156:159] offset:5120\n"
"v_mfma_f32_16x16x32_bf16 v[48:51], a[24:27], v[168:171], v[48:51]\n"
"v_mfma_f32_16x16x32_bf16 v[48:51], a[28:31], v[176:179], v[48:51]\n"
"buffer_load_dwordx4 v[88:91], v0, s[4:7], s98 offen offset:1024 sc1\n"
"s_add_u32 s96, s64, 0x4000\n"
"v_mfma_f32_16x16x32_bf16 v[96:99], a[60:63], v[176:179], 0\n"
"v_mfma_f32_16x16x32_bf16 v[96:99], a[56:59], v[168:171], v[96:99]\n"
"buffer_store_dwordx4 v[44:47], v7, s[4:7], s96 offen offset:1024 sc1\n"
"v_mfma_f32_16x16x32_bf16 v[96:99], a[52:55], v[148:151], v[96:99]\n"
"v_mfma_f32_16x16x32_bf16 v[96:99], a[48:51], v[80:83], v[96:99]\n"
"v_mfma_f32_16x16x32_bf16 v[96:99], a[44:47], v[192:195], v[96:99]\n"
"buffer_load_dwordx4 v[44:47], v0, s[4:7], s98 offen offset:2048 sc1\n"
"v_mfma_f32_16x16x32_bf16 v[96:99], a[40:43], v[200:203], v[96:99]\n"
"ds_write_b128 v3, v[48:51] offset:6144\n"
"v_mfma_f32_16x16x32_bf16 v[96:99], a[36:39], v[164:167], v[96:99]\n"
"buffer_load_dwordx4 a[212:215], v1, s[4:7], s97 offen offset:1024 sc1\n"
"v_mfma_f32_16x16x32_bf16 v[96:99], a[32:35], v[152:155], v[96:99]\n"
"v_mfma_f32_16x16x32_bf16 v[48:51], a[64:67], v[152:155], 0\n"
"v_mfma_f32_16x16x32_bf16 v[48:51], a[68:71], v[164:167], v[48:51]\n"
"buffer_load_dwordx4 v[104:107], v0, s[4:7], s98 offen offset:3072 sc1\n"
"v_mfma_f32_16x16x32_bf16 v[48:51], a[72:75], v[200:203], v[48:51]\n"
"v_mfma_f32_16x16x32_bf16 v[48:51], a[76:79], v[192:195], v[48:51]\n"
"v_mfma_f32_16x16x32_bf16 v[48:51], a[80:83], v[80:83], v[48:51]\n"
"v_mfma_f32_16x16x32_bf16 v[48:51], a[84:87], v[148:151], v[48:51]\n"
"s_nop 0\n"
"ds_write_b128 v3, v[96:99] offset:7168\n"
"v_mfma_f32_16x16x32_bf16 v[48:51], a[88:91], v[168:171], v[48:51]\n"
"buffer_load_dwordx4 v[96:99], v1, s[4:7], s98 offen sc1\n"
"v_mfma_f32_16x16x32_bf16 v[48:51], a[92:95], v[176:179], v[48:51]\n"
"v_mfma_f32_16x16x32_bf16 v[116:119], a[124:127], v[176:179], 0\n"
"v_mfma_f32_16x16x32_bf16 v[116:119], a[120:123], v[168:171], v[116:119]\n"
"v_mfma_f32_16x16x32_bf16 v[116:119], a[116:119], v[148:151], v[116:119]\n"
"v_mfma_f32_16x16x32_bf16 v[116:119], a[112:115], v[80:83], v[116:119]\n"
"buffer_load_dwordx4 v[156:159], v1, s[4:7], s98 offen offset:1024 sc1\n"
"v_mfma_f32_16x16x32_bf16 v[116:119], a[108:111], v[192:195], v[116:119]\n"
"v_mfma_f32_16x16x32_bf16 v[116:119], a[104:107], v[200:203], v[116:119]\n"
"s_nop 0\n"
"ds_write_b128 v3, v[48:51] offset:8192\n"
"v_mfma_f32_16x16x32_bf16 v[116:119], a[100:103], v[164:167], v[116:119]\n"
"v_mfma_f32_16x16x32_bf16 v[116:119], a[96:99], v[152:155], v[116:119]\n"
"v_mfma_f32_16x16x32_bf16 v[48:51], v[28:31], v[152:155], 0\n"
"buffer_load_dwordx4 v[172:175], v1, s[4:7], s98 offen offset:2048 sc1\n"
"v_mfma_f32_16x16x32_bf16 v[48:51], v[40:43], v[164:167], v[48:51]\n"
"buffer_load_dwordx4 a[216:219], v1, s[4:7], s97 offen offset:2048 sc1\n"
"v_mfma_f32_16x16x32_bf16 v[48:51], v[24:27], v[200:203], v[48:51]\n"
"v_mfma_f32_16x16x32_bf16 v[48:51], v[72:75], v[192:195], v[48:51]\n"
"v_mfma_f32_16x16x32_bf16 v[48:51], v[32:35], v[80:83], v[48:51]\n"
"s_waitcnt vmcnt(12)\n"
"v_add_f32_e32 v255, v255, v254\n"
"v_add_f32_e32 v253, v253, v250\n"
"v_mfma_f32_16x16x32_bf16 v[48:51], v[64:67], v[148:151], v[48:51]\n"
"buffer_load_dwordx4 v[180:183], v1, s[4:7], s98 offen offset:3072 sc1\n"
"v_mfma_f32_16x16x32_bf16 v[48:51], v[60:63], v[168:171], v[48:51]\n"
"ds_write_b128 v3, v[116:119] offset:9216\n"
"v_mfma_f32_16x16x32_bf16 v[48:51], v[36:39], v[176:179], v[48:51]\n"
"v_add_f32_e32 v252, v252, v249\n"
"v_add_f32_e32 v251, v251, v248\n"
"v_mfma_f32_16x16x32_bf16 v[116:119], v[100:103], v[176:179], 0\n"
"v_add_f32_e32 v255, v255, v252\n"
"v_add_f32_e32 v253, v253, v251\n"
"v_mfma_f32_16x16x32_bf16 v[116:119], v[76:79], v[168:171], v[116:119]\n"
"v_add_f32_e32 v255, v255, v253\n"
"v_mov_b32_e32 v253, v255\n"
"v_mfma_f32_16x16x32_bf16 v[116:119], v[140:143], v[148:151], v[116:119]\n"
"s_add_u32 s98, s71, 0\n"
"buffer_load_dwordx2 v[250:251], off, s[4:7], s98 sc1\n"
"v_mfma_f32_16x16x32_bf16 v[116:119], v[136:139], v[80:83], v[116:119]\n"
"v_permlane32_swap_b32_e32 v255, v253\n"
"v_add_f32_e32 v255, v255, v253\n"
"v_mfma_f32_16x16x32_bf16 v[116:119], v[56:59], v[192:195], v[116:119]\n"
"v_mul_f32_e32 v255, 0x3a800000, v255\n"
"v_mov_b32_e32 v253, v255\n"
"v_mfma_f32_16x16x32_bf16 v[116:119], v[112:115], v[200:203], v[116:119]\n"
"ds_write_b128 v3, v[48:51] offset:10240\n"
"v_mfma_f32_16x16x32_bf16 v[116:119], v[108:111], v[164:167], v[116:119]\n"
"buffer_store_dwordx2 v[18:19], v11, s[4:7], s68 offen offset:32 sc1\n"
"v_mfma_f32_16x16x32_bf16 v[116:119], v[120:123], v[152:155], v[116:119]\n"
"v_permlane16_swap_b32_e32 v255, v253\n"
"v_cvt_pk_bf16_f32 v254, -v255, 0\n"
"; @A G1 3.2\n"
"v_mfma_f32_16x16x32_bf16 v[48:51], a[0:3], v[68:71], 0\n"
"v_fma_f32 v253, -v255, v255, v253\n"
"v_lshlrev_b32_e32 v254, 16, v254\n"
"v_mfma_f32_16x16x32_bf16 v[48:51], a[4:7], v[84:87], v[48:51]\n"
"v_max_f32_e32 v253, 0, v253\n"
"v_cvt_pk_bf16_f32 v253, v253, 0\n"
"v_mfma_f32_16x16x32_bf16 v[48:51], a[8:11], v[132:135], v[48:51]\n"
"buffer_load_dwordx4 a[220:223], v1, s[4:7], s97 offen offset:3072 sc1\n"
"v_mfma_f32_16x16x32_bf16 v[48:51], a[12:15], v[144:147], v[48:51]\n"
"s_waitcnt vmcnt(15)\n"
"v_lshlrev_b32_e32 v253, 16, v253\n"
"v_add_f32_e32 v253, 0x3727c5ac, v253\n"
"v_mfma_f32_16x16x32_bf16 v[48:51], a[16:19], v[92:95], v[48:51]\n"
"s_add_u32 s97, s70, 0x6000\n"
"v_cvt_pk_bf16_f32 v253, v253, 0\n"
"v_lshlrev_b32_e32 v253, 16, v253\n"
"v_mfma_f32_16x16x32_bf16 v[48:51], a[20:23], v[124:127], v[48:51]\n"
"ds_write_b128 v3, v[116:119] offset:11264\n"
"v_mfma_f32_16x16x32_bf16 v[48:51], a[24:27], v[128:131], v[48:51]\n"
"buffer_load_dwordx4 a[224:227], v0, s[4:7], s97 offen sc1\n"
"v_mfma_f32_16x16x32_bf16 v[48:51], a[28:31], v[160:163], v[48:51]\n"
"v_sqrt_f32_e32 v253, v253\n"
"v_mfma_f32_16x16x32_bf16 v[80:83], a[60:63], v[160:163], 0\n"
"v_cvt_pk_bf16_f32 v253, v253, 0\n"
"v_lshlrev_b32_e32 v253, 16, v253\n"
"v_mfma_f32_16x16x32_bf16 v[80:83], a[56:59], v[128:131], v[80:83]\n"
"v_rcp_f32_e32 v255, v253\n"
"v_mfma_f32_16x16x32_bf16 v[80:83], a[52:55], v[124:127], v[80:83]\n"
"s_waitcnt lgkmcnt(0)\n"
"s_barrier\n"
"v_mfma_f32_16x16x32_bf16 v[80:83], a[48:51], v[92:95], v[80:83]\n"
"buffer_load_dwordx4 a[228:231], v0, s[4:7], s97 offen offset:1024 sc1\n"
"v_mfma_f32_16x16x32_bf16 v[80:83], a[44:47], v[144:147], v[80:83]\n"
"ds_read_b128 v[116:119], v4\n"
"v_mul_f32_e32 v253, 0x3e8293ee, v255\n"
"v_mfma_f32_16x16x32_bf16 v[80:83], a[40:43], v[132:135], v[80:83]\n"
"ds_write_b128 v3, v[48:51] offset:12288\n"
"v_mfma_f32_16x16x32_bf16 v[80:83], a[36:39], v[84:87], v[80:83]\n"
"ds_read_b128 v[48:51], v9 offset:128\n"
"ds_read_b128 v[148:151], v5\n"
"v_mfma_f32_16x16x32_bf16 v[80:83], a[32:35], v[68:71], v[80:83]\n"
"buffer_load_dwordx4 a[232:235], v0, s[4:7], s97 offen offset:2048 sc1\n"
"v_mfma_f32_16x16x32_bf16 v[152:155], a[64:67], v[68:71], 0\n"
"v_mfma_f32_16x16x32_bf16 v[152:155], a[68:71], v[84:87], v[152:155]\n"
"v_mfma_f32_16x16x32_bf16 v[152:155], a[72:75], v[132:135], v[152:155]\n"
"ds_read_b128 v[164:167], v4 offset:1024\n"
"ds_read_b128 v[168:171], v9 offset:192\n"
"v_mfma_f32_16x16x32_bf16 v[152:155], a[76:79], v[144:147], v[152:155]\n"
"buffer_load_dwordx4 a[236:239], v0, s[4:7], s97 offen offset:3072 sc1\n"
"v_mfma_f32_16x16x32_bf16 v[152:155], a[80:83], v[92:95], v[152:155]\n"
"ds_read_b128 v[176:179], v5 offset:1024\n"
"s_waitcnt lgkmcnt(3)\n"
"v_mfma_f32_16x16x32_bf16 v[152:155], a[84:87], v[124:127], v[152:155]\n"
"ds_write_b128 v3, v[80:83] offset:13312\n"
"v_mfma_f32_16x16x32_bf16 v[152:155], a[88:91], v[128:131], v[152:155]\n"
"v_fmac_f32_e32 v116, v254, v48\n"
"v_fmac_f32_e32 v117, v254, v49\n"
"v_mfma_f32_16x16x32_bf16 v[152:155], a[92:95], v[160:163], v[152:155]\n"
"buffer_load_dwordx4 a[240:243], v1, s[4:7], s97 offen sc1\n"
"v_mfma_f32_16x16x32_bf16 v[80:83], a[124:127], v[160:163], 0\n"
"v_fmac_f32_e32 v118, v254, v50\n"
"v_fmac_f32_e32 v119, v254, v51\n"
"v_mfma_f32_16x16x32_bf16 v[80:83], a[120:123], v[128:131], v[80:83]\n"
"ds_read_b128 v[48:51], v4 offset:2048\n"
"ds_read_b128 v[184:187], v9 offset:256\n"
"v_mfma_f32_16x16x32_bf16 v[80:83], a[116:119], v[124:127], v[80:83]\n"
"ds_read_b128 v[188:191], v5 offset:2048\n"
"v_add_f32_e32 v116, v116, v148\n"
"v_mfma_f32_16x16x32_bf16 v[80:83], a[112:115], v[92:95], v[80:83]\n"
"buffer_load_dwordx4 a[244:247], v1, s[4:7], s97 offen offset:1024 sc1\n"
"v_mfma_f32_16x16x32_bf16 v[80:83], a[108:111], v[144:147], v[80:83]\n"
"v_add_f32_e32 v117, v117, v149\n"
"v_add_f32_e32 v118, v118, v150\n"
"v_mfma_f32_16x16x32_bf16 v[80:83], a[104:107], v[132:135], v[80:83]\n"
"ds_write_b128 v3, v[152:155] offset:14336\n"
"v_mfma_f32_16x16x32_bf16 v[80:83], a[100:103], v[84:87], v[80:83]\n"
"s_waitcnt lgkmcnt(5)\n"
"v_add_f32_e32 v119, v119, v151\n"
"v_fmac_f32_e32 v164, v254, v168\n"
"v_mfma_f32_16x16x32_bf16 v[80:83], a[96:99], v[68:71], v[80:83]\n"
"buffer_load_dwordx4 a[248:251], v1, s[4:7], s97 offen offset:2048 sc1\n"
"v_mfma_f32_16x16x32_bf16 v[148:151], v[28:31], v[68:71], 0\n"
"ds_read_b128 v[152:155], v4 offset:3072\n"
"ds_read_b128 v[192:195], v9 offset:320\n"
"v_mfma_f32_16x16x32_bf16 v[148:151], v[40:43], v[84:87], v[148:151]\n"
"ds_read_b128 v[196:199], v5 offset:3072\n"
"v_fmac_f32_e32 v165, v254, v169\n"
"v_mfma_f32_16x16x32_bf16 v[148:151], v[24:27], v[132:135], v[148:151]\n"
"v_fmac_f32_e32 v166, v254, v170\n"
"v_fmac_f32_e32 v167, v254, v171\n"
"v_mfma_f32_16x16x32_bf16 v[148:151], v[72:75], v[144:147], v[148:151]\n"
"buffer_load_dwordx4 a[252:255], v1, s[4:7], s97 offen offset:3072 sc1\n"
"v_mfma_f32_16x16x32_bf16 v[148:151], v[32:35], v[92:95], v[148:151]\n"
"v_add_f32_e32 v164, v164, v176\n"
"v_add_f32_e32 v165, v165, v177\n"
"v_mfma_f32_16x16x32_bf16 v[148:151], v[64:67], v[124:127], v[148:151]\n"
"ds_write_b128 v3, v[80:83] offset:15360\n"
"v_mfma_f32_16x16x32_bf16 v[148:151], v[60:63], v[128:131], v[148:151]\n"
"s_waitcnt lgkmcnt(5)\n"
"v_add_f32_e32 v166, v166, v178\n"
"v_add_f32_e32 v167, v167, v179\n"
"v_mfma_f32_16x16x32_bf16 v[148:151], v[36:39], v[160:163], v[148:151]\n"
"ds_read_b128 v[80:83], v4 offset:4096\n"
"ds_read_b128 v[168:171], v9\n"
"v_mfma_f32_16x16x32_bf16 v[176:179], v[100:103], v[160:163], 0\n"
"ds_read_b128 v[160:163], v5 offset:4096\n"
"v_fmac_f32_e32 v48, v254, v184\n"
"v_mfma_f32_16x16x32_bf16 v[176:179], v[76:79], v[128:131], v[176:179]\n"
"v_fmac_f32_e32 v49, v254, v185\n"
"v_fmac_f32_e32 v50, v254, v186\n"
"v_mfma_f32_16x16x32_bf16 v[176:179], v[140:143], v[124:127], v[176:179]\n"
"v_fmac_f32_e32 v51, v254, v187\n"
"v_add_f32_e32 v48, v48, v188\n"
"v_mfma_f32_16x16x32_bf16 v[176:179], v[136:139], v[92:95], v[176:179]\n"
"v_add_f32_e32 v49, v49, v189\n"
"v_add_f32_e32 v50, v50, v190\n"
"v_mfma_f32_16x16x32_bf16 v[176:179], v[56:59], v[144:147], v[176:179]\n"
"s_waitcnt vmcnt(10)\n"
"v_cmp_ne_u64_e32 vcc, v[250:251], v[18:19]\n"
"v_mfma_f32_16x16x32_bf16 v[176:179], v[112:115], v[132:135], v[176:179]\n"
"ds_write_b128 v3, v[148:151] offset:16384\n"
"v_mfma_f32_16x16x32_bf16 v[176:179], v[108:111], v[84:87], v[176:179]\n"
"s_cbranch_vccnz .Lmk_f73s\n"
".Lmk_f73r:\n"
"s_add_u32 m0, s77, 0x6000\n"
"v_mfma_f32_16x16x32_bf16 v[176:179], v[120:123], v[68:71], v[176:179]\n"
"s_add_u32 s98, s65, 0x2000\n"
"ds_read_b128 v[68:71], v4 offset:5120\n"
"; @A G1 3.3\n"
"v_mfma_f32_16x16x32_bf16 v[116:119], a[0:3], v[52:55], v[116:119]\n"
"buffer_load_dwordx4 v2, s[4:7], s98 offen sc1 lds\n"
"v_mfma_f32_16x16x32_bf16 v[116:119], a[4:7], v[88:91], v[116:119]\n"
"buffer_load_dwordx4 v2, s[4:7], s98 offen offset:1024 sc1 lds\n"
"v_mfma_f32_16x16x32_bf16 v[116:119], a[8:11], v[44:47], v[116:119]\n"
"ds_read_b128 v[84:87], v9 offset:64\n"
"ds_read_b128 v[92:95], v5 offset:5120\n"
"v_mfma_f32_16x16x32_bf16 v[116:119], a[12:15], v[104:107], v[116:119]\n"
"s_waitcnt lgkmcnt(8)\n"
"v_add_f32_e32 v51, v51, v191\n"
"v_mfma_f32_16x16x32_bf16 v[116:119], a[16:19], v[96:99], v[116:119]\n"
"v_fmac_f32_e32 v152, v254, v192\n"
"v_fmac_f32_e32 v153, v254, v193\n"
"v_mfma_f32_16x16x32_bf16 v[116:119], a[20:23], v[156:159], v[116:119]\n"
"ds_write_b128 v3, v[176:179] offset:17408\n"
"v_mfma_f32_16x16x32_bf16 v[116:119], a[24:27], v[172:175], v[116:119]\n"
"v_fmac_f32_e32 v154, v254, v194\n"
"v_fmac_f32_e32 v155, v254, v195\n"
"v_mfma_f32_16x16x32_bf16 v[116:119], a[28:31], v[180:183], v[116:119]\n"
"s_add_u32 m0, s77, 0xe000\n"
"s_add_u32 s98, s65, 0x6000\n"
"v_add_f32_e32 v152, v152, v196\n"
"v_mfma_f32_16x16x32_bf16 v[164:167], a[60:63], v[180:183], v[164:167]\n"
"buffer_load_dwordx4 v2, s[4:7], s98 offen sc1 lds\n"
"v_mfma_f32_16x16x32_bf16 v[164:167], a[56:59], v[172:175], v[164:167]\n"
"buffer_load_dwordx4 v2, s[4:7], s98 offen offset:1024 sc1 lds\n"
"v_mfma_f32_16x16x32_bf16 v[164:167], a[52:55], v[156:159], v[164:167]\n"
"s_waitcnt lgkmcnt(0)\n"
"s_barrier\n"
"v_mfma_f32_16x16x32_bf16 v[164:167], a[48:51], v[96:99], v[164:167]\n"
"ds_read_b128 v[124:127], v6\n"
"ds_read_b128 v[128:131], v6 offset:1024\n"
"v_mfma_f32_16x16x32_bf16 v[164:167], a[44:47], v[104:107], v[164:167]\n"
"s_add_u32 s98, s69, 0x600000\n"
"buffer_load_dwordx4 a[0:3], v0, s[4:7], s98 offen sc1\n"
"v_mfma_f32_16x16x32_bf16 v[164:167], a[40:43], v[44:47], v[164:167]\n"
"v_add_f32_e32 v153, v153, v197\n"
"v_add_f32_e32 v154, v154, v198\n"
"v_mfma_f32_16x16x32_bf16 v[164:167], a[36:39], v[88:91], v[164:167]\n"
"v_add_f32_e32 v155, v155, v199\n"
"v_fmac_f32_e32 v80, v254, v168\n"
"v_mfma_f32_16x16x32_bf16 v[164:167], a[32:35], v[52:55], v[164:167]\n"
"buffer_load_dwordx4 a[4:7], v0, s[4:7], s98 offen offset:1024 sc1\n"
"v_mfma_f32_16x16x32_bf16 v[48:51], a[64:67], v[52:55], v[48:51]\n"
"v_fmac_f32_e32 v81, v254, v169\n"
"v_fmac_f32_e32 v82, v254, v170\n"
"v_mfma_f32_16x16x32_bf16 v[48:51], a[68:71], v[88:91], v[48:51]\n"
"v_fmac_f32_e32 v83, v254, v171\n"
"v_add_f32_e32 v80, v80, v160\n"
"v_mfma_f32_16x16x32_bf16 v[48:51], a[72:75], v[44:47], v[48:51]\n"
"buffer_load_dwordx4 a[8:11], v0, s[4:7], s98 offen offset:2048 sc1\n"
"v_mfma_f32_16x16x32_bf16 v[48:51], a[76:79], v[104:107], v[48:51]\n"
"v_add_f32_e32 v81, v81, v161\n"
"v_add_f32_e32 v82, v82, v162\n"
"v_mfma_f32_16x16x32_bf16 v[48:51], a[80:83], v[96:99], v[48:51]\n"
"v_add_f32_e32 v83, v83, v163\n"
"v_fmac_f32_e32 v68, v254, v84\n"
"v_mfma_f32_16x16x32_bf16 v[48:51], a[84:87], v[156:159], v[48:51]\n"
"buffer_load_dwordx4 a[12:15], v0, s[4:7], s98 offen offset:3072 sc1\n"
"v_mfma_f32_16x16x32_bf16 v[48:51], a[88:91], v[172:175], v[48:51]\n"
"ds_read_b128 v[132:135], v6 offset:2048\n"
"ds_read_b128 v[144:147], v6 offset:3072\n"
"v_mfma_f32_16x16x32_bf16 v[48:51], a[92:95], v[180:183], v[48:51]\n"
"s_waitcnt lgkmcnt(2)\n"
"v_fmac_f32_e32 v69, v254, v85\n"
"v_fmac_f32_e32 v70, v254, v86\n"
"v_mfma_f32_16x16x32_bf16 v[152:155], a[124:127], v[180:183], v[152:155]\n"
"buffer_load_dwordx4 a[16:19], v1, s[4:7], s98 offen sc1\n"
"v_mfma_f32_16x16x32_bf16 v[152:155], a[120:123], v[172:175], v[152:155]\n"
"v_fmac_f32_e32 v71, v254, v87\n"
"v_add_f32_e32 v68, v68, v92\n"
"v_mfma_f32_16x16x32_bf16 v[152:155], a[116:119], v[156:159], v[152:155]\n"
"v_add_f32_e32 v69, v69, v93\n"
"v_add_f32_e32 v70, v70, v94\n"
"v_mfma_f32_16x16x32_bf16 v[152:155], a[112:115], v[96:99], v[152:155]\n"
"buffer_load_dwordx4 a[20:23], v1, s[4:7], s98 offen offset:1024 sc1\n"
"v_mfma_f32_16x16x32_bf16 v[152:155], a[108:111], v[104:107], v[152:155]\n"
"v_add_f32_e32 v71, v71, v95\n"
"v_add_f32_e32 v116, v116, v124\n"
"v_mfma_f32_16x16x32_bf16 v[152:155], a[104:107], v[44:47], v[152:155]\n"
"v_add_f32_e32 v117, v117, v125\n"
"v_add_f32_e32 v118, v118, v126\n"
"v_mfma_f32_16x16x32_bf16 v[152:155], a[100:103], v[88:91], v[152:155]\n"
"buffer_load_dwordx4 a[24:27], v1, s[4:7], s98 offen offset:2048 sc1\n"
"v_mfma_f32_16x16x32_bf16 v[152:155], a[96:99], v[52:55], v[152:155]\n"
"v_add_f32_e32 v119, v119, v127\n"
"v_add_f32_e32 v164, v164, v128\n"
"v_mfma_f32_16x16x32_bf16 v[80:83], v[28:31], v[52:55], v[80:83]\n"
"v_add_f32_e32 v165, v165, v129\n"
"v_add_f32_e32 v166, v166, v130\n"
"v_mfma_f32_16x16x32_bf16 v[80:83], v[40:43], v[88:91], v[80:83]\n"
"buffer_load_dwordx4 a[28:31], v1, s[4:7], s98 offen offset:3072 sc1\n"
"v_mfma_f32_16x16x32_bf16 v[80:83], v[24:27], v[44:47], v[80:83]\n"
"v_add_f32_e32 v167, v167, v131\n"
"v_mul_f32_e32 v116, v116, v255\n"
"v_mfma_f32_16x16x32_bf16 v[80:83], v[72:75], v[104:107], v[80:83]\n"
"v_mul_f32_e32 v117, v117, v255\n"
"v_mul_f32_e32 v118, v118, v255\n"
"v_mfma_f32_16x16x32_bf16 v[80:83], v[32:35], v[96:99], v[80:83]\n"
"s_waitcnt lgkmcnt(0)\n"
"v_mul_f32_e32 v119, v119, v255\n"
"v_mul_f32_e32 v164, v164, v255\n"
"v_mfma_f32_16x16x32_bf16 v[80:83], v[64:67], v[156:159], v[80:83]\n"
"v_mul_f32_e32 v165, v165, v255\n"
"v_mul_f32_e32 v166, v166, v255\n"
"v_mfma_f32_16x16x32_bf16 v[80:83], v[60:63], v[172:175], v[80:83]\n"
"ds_read_b128 v[24:27], v6 offset:4096\n"
"ds_read_b128 v[28:31], v6 offset:5120\n"
"v_mfma_f32_16x16x32_bf16 v[80:83], v[36:39], v[180:183], v[80:83]\n"
"v_mul_f32_e32 v167, v167, v255\n"
"v_cvt_pk_bf16_f32 v32, v116, v117\n"
"v_mfma_f32_16x16x32_bf16 v[68:71], v[100:103], v[180:183], v[68:71]\n"
"v_cvt_pk_bf16_f32 v33, v118, v119\n"
"v_cvt_pk_bf16_f32 v34, v164, v165\n"
"v_mfma_f32_16x16x32_bf16 v[68:71], v[76:79], v[172:175], v[68:71]\n"
"v_cvt_pk_bf16_f32 v35, v166, v167\n"
"v_add_f32_e32 v48, v48, v132\n"
"v_mfma_f32_16x16x32_bf16 v[68:71], v[140:143], v[156:159], v[68:71]\n"
"ds_write_b128 v7, v[32:35] offset:16384\n"
"v_mfma_f32_16x16x32_bf16 v[68:71], v[136:139], v[96:99], v[68:71]\n"
"buffer_store_dwordx4 v[32:35], v7, s[4:7], s64 offen sc1\n"
"v_mfma_f32_16x16x32_bf16 v[68:71], v[56:59], v[104:107], v[68:71]\n"
"v_add_f32_e32 v49, v49, v133\n"
"v_add_f32_e32 v50, v50, v134\n"
"v_mfma_f32_16x16x32_bf16 v[68:71], v[112:115], v[44:47], v[68:71]\n"
"v_add_f32_e32 v51, v51, v135\n"
"v_add_f32_e32 v152, v152, v144\n"
"v_mfma_f32_16x16x32_bf16 v[68:71], v[108:111], v[88:91], v[68:71]\n"
"v_add_f32_e32 v153, v153, v145\n"
"v_add_f32_e32 v154, v154, v146\n"
"v_mfma_f32_16x16x32_bf16 v[68:71], v[120:123], v[52:55], v[68:71]\n"
"v_add_f32_e32 v155, v155, v147\n"
"v_mul_f32_e32 v48, v48, v255\n"
"v_mul_f32_e32 v49, v49, v255\n"
"v_mul_f32_e32 v50, v50, v255\n"
"v_mul_f32_e32 v51, v51, v255\n"
"v_mul_f32_e32 v152, v152, v255\n"
"v_mul_f32_e32 v153, v153, v255\n"
"v_mul_f32_e32 v154, v154, v255\n"
"v_mul_f32_e32 v155, v155, v255\n"
"v_cvt_pk_bf16_f32 v32, v48, v49\n"
"v_cvt_pk_bf16_f32 v33, v50, v51\n"
"v_cvt_pk_bf16_f32 v34, v152, v153\n"
"v_cvt_pk_bf16_f32 v35, v154, v155\n"
"v_xor_b32_e32 v255, 32, v16\n"
"v_xor_b32_e32 v254, 32, v15\n"
"v_mov_b32_e32 v88, 0x3f803f80\n"
"v_mov_b32_e32 v89, 0x3f803f80\n"
"v_mov_b32_e32 v90, 0x3f803f80\n"
"v_mov_b32_e32 v91, 0x3f803f80\n"
"v_mov_b32_e32 v252, 0x53800000\n"
"ds_write_b128 v7, v[32:35] offset:49152\n"
".Lmk_a4:\n"
"; @A tail\n"
"s_add_u32 s98, s64, 0x4000\n"
"buffer_store_dwordx4 v[32:35], v7, s[4:7], s98 offen sc1\n"
"s_waitcnt lgkmcnt(2)\n"
"v_add_f32_e32 v80, v80, v24\n"
"v_add_f32_e32 v81, v81, v25\n"
"v_add_f32_e32 v82, v82, v26\n"
"v_add_f32_e32 v83, v83, v27\n"
"v_add_f32_e32 v68, v68, v28\n"
"v_add_f32_e32 v69, v69, v29\n"
"v_add_f32_e32 v70, v70, v30\n"
"v_add_f32_e32 v71, v71, v31\n"
"v_mul_f32_e32 v80, v80, v253\n"
"v_mul_f32_e32 v81, v81, v253\n"
"v_mul_f32_e32 v82, v82, v253\n"
"v_mul_f32_e32 v83, v83, v253\n"
"v_mul_f32_e32 v68, v68, v253\n"
"v_mul_f32_e32 v69, v69, v253\n"
"v_mul_f32_e32 v70, v70, v253\n"
"v_mul_f32_e32 v71, v71, v253\n"
"v_cvt_pk_bf16_f32 v24, v80, v81\n"
"v_cvt_pk_bf16_f32 v25, v82, v83\n"
"v_cvt_pk_bf16_f32 v26, v68, v69\n"
"v_cvt_pk_bf16_f32 v27, v70, v71\n"
"ds_write_b128 v2, v[24:27] offset:16384\n"
"s_waitcnt lgkmcnt(0)\n"
"s_barrier\n"
"ds_read_b128 v[28:31], v2 offset:49152\n"
"ds_read_b128 v[32:35], v2 offset:50176\n"
"ds_read_b128 v[92:95], v2 offset:16384\n"
"ds_read_b128 v[96:99], v2 offset:17408\n"
"; @A AT b\n"
"s_mov_b32 s2, 0\n"
"v_mov_b32_e32 v253, 0xff800000\n"
"v_mov_b32_e32 v251, 0xff800000\n"
"v_mov_b32_e32 v250, 0x7f800000\n"
"v_mov_b32_e32 v249, 0x7f800000\n"
".Lmk_a91:\n"
"ds_read_b128 v[24:27], v15 offset:24576\n"
"ds_read_b128 v[36:39], v254 offset:24832\n"
"ds_read_b128 v[40:43], v15 offset:22528\n"
"ds_read_b128 v[44:47], v254 offset:22784\n"
"s_cmp_gt_i32 s78, 11\n"
"s_cselect_b32 s98, 0x80000000, 0\n"
"s_or_b32 s98, s98, 0x7f800000\n"
"s_waitcnt lgkmcnt(2)\n"
"v_mfma_f32_16x16x32_bf16 v[48:51], v[24:27], v[28:31], 0\n"
"v_mfma_f32_16x16x32_bf16 v[52:55], v[24:27], v[32:35], 0\n"
"v_mfma_f32_16x16x32_bf16 v[56:59], v[36:39], v[32:35], 0\n"
"v_mfma_f32_16x16x32_bf16 v[24:27], v[36:39], v[28:31], 0\n"
"s_waitcnt lgkmcnt(0)\n"
"s_nop 3\n"
"v_max_f32_e32 v248, v48, v49\n"
"v_max_f32_e32 v247, v52, v53\n"
"v_max3_f32 v248, v248, v50, v51\n"
"v_max3_f32 v247, v247, v54, v55\n"
"v_max3_f32 v248, v248, v24, v25\n"
"v_max3_f32 v247, v247, v56, v57\n"
"v_max3_f32 v248, v248, v26, v27\n"
"v_max3_f32 v247, v247, v58, v59\n"
"v_mov_b32_e32 v246, v248\n"
"v_mov_b32_e32 v245, v247\n"
"s_nop 0\n"
"v_permlane16_swap_b32_e32 v248, v246\n"
"v_permlane16_swap_b32_e32 v247, v245\n"
"v_max_f32_e32 v248, v248, v246\n"
"v_max_f32_e32 v247, v247, v245\n"
"v_mov_b32_e32 v246, v248\n"
"v_mov_b32_e32 v245, v247\n"
"s_nop 0\n"
"v_permlane32_swap_b32_e32 v248, v246\n"
"v_permlane32_swap_b32_e32 v247, v245\n"
"v_max_f32_e32 v248, v248, v246\n"
"v_max_f32_e32 v247, v247, v245\n"
"v_med3_f32 v248, v248, v253, v250\n"
"v_med3_f32 v247, v247, v251, v249\n"
"v_min_f32_e64 v36, s98, -v248\n"
"v_min_f32_e64 v37, s98, -v248\n"
"v_min_f32_e64 v38, s98, -v248\n"
"v_min_f32_e64 v39, s98, -v248\n"
"v_min_f32_e64 v60, s98, -v247\n"
"v_min_f32_e64 v61, s98, -v247\n"
"v_min_f32_e64 v62, s98, -v247\n"
"v_min_f32_e64 v63, s98, -v247\n"
"v_mfma_f32_16x16x32_bf16 v[64:67], v[40:43], v[28:31], v[36:39]\n"
"ds_read_b128 v[68:71], v15 offset:20480\n"
"s_add_u32 s98, s69, 0x602000\n"
"buffer_load_dwordx4 a[32:35], v0, s[4:7], s98 offen sc1\n"
"v_sub_f32_e32 v48, v48, v248\n"
"v_sub_f32_e32 v49, v49, v248\n"
"v_sub_f32_e32 v50, v50, v248\n"
"v_mfma_f32_16x16x32_bf16 v[72:75], v[40:43], v[32:35], v[60:63]\n"
"buffer_load_dwordx4 a[36:39], v0, s[4:7], s98 offen offset:1024 sc1\n"
"v_sub_f32_e32 v51, v51, v248\n"
"v_sub_f32_e32 v24, v24, v248\n"
"v_sub_f32_e32 v25, v25, v248\n"
"v_sub_f32_e32 v26, v26, v248\n"
"v_sub_f32_e32 v27, v27, v248\n"
"v_mfma_f32_16x16x32_bf16 v[40:43], v[44:47], v[32:35], v[60:63]\n"
"ds_read_b128 v[76:79], v254 offset:20736\n"
"v_sub_f32_e32 v52, v52, v247\n"
"v_sub_f32_e32 v53, v53, v247\n"
"v_sub_f32_e32 v54, v54, v247\n"
"v_sub_f32_e32 v55, v55, v247\n"
"v_mfma_f32_16x16x32_bf16 v[80:83], v[44:47], v[28:31], v[36:39]\n"
"buffer_load_dwordx4 a[40:43], v0, s[4:7], s98 offen offset:2048 sc1\n"
"v_sub_f32_e32 v56, v56, v247\n"
"v_sub_f32_e32 v57, v57, v247\n"
"v_sub_f32_e32 v58, v58, v247\n"
"v_sub_f32_e32 v59, v59, v247\n"
"s_waitcnt lgkmcnt(0)\n"
"v_mfma_f32_16x16x32_bf16 v[44:47], v[68:71], v[28:31], v[36:39]\n"
"ds_read_b128 v[84:87], v15 offset:18432\n"
"buffer_load_dwordx4 a[44:47], v0, s[4:7], s98 offen offset:3072 sc1\n"
"ds_read_b128 v[100:103], v254 offset:18688\n"
"v_exp_f32_e32 v64, v64\n"
"v_exp_f32_e32 v65, v65\n"
"v_exp_f32_e32 v66, v66\n"
"v_exp_f32_e32 v67, v67\n"
"v_exp_f32_e32 v80, v80\n"
"v_mfma_f32_16x16x32_bf16 v[104:107], v[68:71], v[32:35], v[60:63]\n"
"ds_read_b64_tr_b16 v[68:69], v16 offset:22528\n"
"buffer_load_dwordx4 a[48:51], v1, s[4:7], s98 offen sc1\n"
"v_exp_f32_e32 v81, v81\n"
"v_exp_f32_e32 v82, v82\n"
"v_exp_f32_e32 v83, v83\n"
"v_cvt_pk_bf16_f32 v108, v64, v65\n"
"v_cvt_pk_bf16_f32 v109, v66, v67\n"
"v_cvt_pk_bf16_f32 v110, v80, v81\n"
"v_mfma_f32_16x16x32_bf16 v[64:67], v[76:79], v[32:35], v[60:63]\n"
"ds_read_b64_tr_b16 v[70:71], v255 offset:22784\n"
"ds_read_b64_tr_b16 v[80:81], v255 offset:22528\n"
"buffer_load_dwordx4 a[52:55], v1, s[4:7], s98 offen offset:1024 sc1\n"
"v_cvt_pk_bf16_f32 v111, v82, v83\n"
"v_exp_f32_e32 v72, v72\n"
"v_exp_f32_e32 v73, v73\n"
"v_exp_f32_e32 v74, v74\n"
"v_exp_f32_e32 v75, v75\n"
"v_mfma_f32_16x16x32_bf16 v[112:115], v[76:79], v[28:31], v[36:39]\n"
"ds_read_b64_tr_b16 v[82:83], v16 offset:22784\n"
"v_exp_f32_e32 v40, v40\n"
"v_exp_f32_e32 v41, v41\n"
"v_exp_f32_e32 v42, v42\n"
"v_exp_f32_e32 v43, v43\n"
"v_cvt_pk_bf16_f32 v76, v72, v73\n"
"v_cvt_pk_bf16_f32 v77, v74, v75\n"
"v_cvt_pk_bf16_f32 v78, v40, v41\n"
"v_cvt_pk_bf16_f32 v79, v42, v43\n"
"s_waitcnt lgkmcnt(4)\n"
"v_mfma_f32_16x16x32_bf16 v[40:43], v[84:87], v[28:31], v[36:39]\n"
"ds_read_b128 v[72:75], v15 offset:16384\n"
"v_exp_f32_e32 v44, v44\n"
"v_exp_f32_e32 v45, v45\n"
"v_exp_f32_e32 v46, v46\n"
"v_mfma_f32_16x16x32_bf16 v[116:119], v[84:87], v[32:35], v[60:63]\n"
"buffer_load_dwordx4 a[56:59], v1, s[4:7], s98 offen offset:2048 sc1\n"
"v_exp_f32_e32 v47, v47\n"
"v_mfma_f32_16x16x32_bf16 v[84:87], v[100:103], v[32:35], v[60:63]\n"
"ds_read_b128 v[120:123], v254 offset:16640\n"
"v_exp_f32_e32 v112, v112\n"
"v_exp_f32_e32 v113, v113\n"
"v_mfma_f32_16x16x32_bf16 v[124:127], v[100:103], v[28:31], v[36:39]\n"
"ds_read_b64_tr_b16 v[100:101], v16 offset:20480\n"
"v_exp_f32_e32 v114, v114\n"
"v_exp_f32_e32 v115, v115\n"
"v_cvt_pk_bf16_f32 v128, v44, v45\n"
"s_waitcnt lgkmcnt(3)\n"
"v_mfma_f32_16x16x32_bf16 v[132:135], v[88:91], v[108:111], 0\n"
"buffer_load_dwordx4 a[60:63], v1, s[4:7], s98 offen offset:3072 sc1\n"
"v_cvt_pk_bf16_f32 v129, v46, v47\n"
"v_cvt_pk_bf16_f32 v130, v112, v113\n"
"v_cvt_pk_bf16_f32 v131, v114, v115\n"
"v_mfma_f32_16x16x32_bf16 v[44:47], v[68:71], v[108:111], 0\n"
"ds_read_b64_tr_b16 v[102:103], v255 offset:20736\n"
"v_exp_f32_e32 v104, v104\n"
"v_exp_f32_e32 v105, v105\n"
"v_mfma_f32_16x16x32_bf16 v[112:115], v[80:83], v[108:111], 0\n"
"ds_read_b64_tr_b16 v[108:109], v255 offset:20480\n"
"s_add_u32 s98, s69, 0x604000\n"
"buffer_load_dwordx4 a[64:67], v0, s[4:7], s98 offen sc1\n"
"v_exp_f32_e32 v106, v106\n"
"v_exp_f32_e32 v107, v107\n"
"v_mfma_f32_16x16x32_bf16 v[136:139], v[80:83], v[76:79], 0\n"
"v_exp_f32_e32 v64, v64\n"
"v_exp_f32_e32 v65, v65\n"
"v_mfma_f32_16x16x32_bf16 v[80:83], v[68:71], v[76:79], 0\n"
"ds_read_b64_tr_b16 v[110:111], v16 offset:20736\n"
"v_exp_f32_e32 v66, v66\n"
"v_exp_f32_e32 v67, v67\n"
"v_mfma_f32_16x16x32_bf16 v[68:71], v[88:91], v[76:79], 0\n"
"s_waitcnt vmcnt(19)\n"
"v_cvt_pk_bf16_f32 v76, v104, v105\n"
"v_cvt_pk_bf16_f32 v77, v106, v107\n"
"v_cvt_pk_bf16_f32 v78, v64, v65\n"
"v_cvt_pk_bf16_f32 v79, v66, v67\n"
"s_cmp_lg_u32 s2, 0\n"
"s_cbranch_scc1 .Lmk_s93\n"
"s_barrier\n"
".Lmk_s93:\n"
"s_waitcnt lgkmcnt(4)\n"
"v_mfma_f32_16x16x32_bf16 v[64:67], v[72:75], v[28:31], v[36:39]\n"
"ds_read_b128 v[104:107], v15 offset:14336\n"
"v_exp_f32_e32 v40, v40\n"
"v_exp_f32_e32 v41, v41\n"
"v_exp_f32_e32 v42, v42\n"
"v_mfma_f32_16x16x32_bf16 v[140:143], v[72:75], v[32:35], v[60:63]\n"
"buffer_load_dwordx4 a[68:71], v0, s[4:7], s98 offen offset:1024 sc1\n"
"v_exp_f32_e32 v43, v43\n"
"v_mfma_f32_16x16x32_bf16 v[72:75], v[120:123], v[32:35], v[60:63]\n"
"ds_read_b128 v[144:147], v254 offset:14592\n"
"v_exp_f32_e32 v124, v124\n"
"v_exp_f32_e32 v125, v125\n"
"v_mfma_f32_16x16x32_bf16 v[148:151], v[120:123], v[28:31], v[36:39]\n"
"ds_read_b64_tr_b16 v[120:121], v16 offset:18432\n"
"v_exp_f32_e32 v126, v126\n"
"v_exp_f32_e32 v127, v127\n"
"s_waitcnt lgkmcnt(3)\n"
"v_mfma_f32_16x16x32_bf16 v[132:135], v[88:91], v[128:131], v[132:135]\n"
"buffer_load_dwordx4 a[72:75], v0, s[4:7], s98 offen offset:2048 sc1\n"
"v_cvt_pk_bf16_f32 v152, v40, v41\n"
"v_cvt_pk_bf16_f32 v153, v42, v43\n"
"v_cvt_pk_bf16_f32 v154, v124, v125\n"
"v_mfma_f32_16x16x32_bf16 v[44:47], v[100:103], v[128:131], v[44:47]\n"
"ds_read_b64_tr_b16 v[122:123], v255 offset:18688\n"
"v_cvt_pk_bf16_f32 v155, v126, v127\n"
"v_exp_f32_e32 v116, v116\n"
"v_exp_f32_e32 v117, v117\n"
"v_mfma_f32_16x16x32_bf16 v[112:115], v[108:111], v[128:131], v[112:115]\n"
"ds_read_b64_tr_b16 v[40:41], v255 offset:18432\n"
"v_exp_f32_e32 v118, v118\n"
"v_exp_f32_e32 v119, v119\n"
"v_mfma_f32_16x16x32_bf16 v[136:139], v[108:111], v[76:79], v[136:139]\n"
"buffer_load_dwordx4 a[76:79], v0, s[4:7], s98 offen offset:3072 sc1\n"
"v_exp_f32_e32 v84, v84\n"
"v_exp_f32_e32 v85, v85\n"
"v_mfma_f32_16x16x32_bf16 v[80:83], v[100:103], v[76:79], v[80:83]\n"
"ds_read_b64_tr_b16 v[42:43], v16 offset:18688\n"
"v_exp_f32_e32 v86, v86\n"
"v_exp_f32_e32 v87, v87\n"
"v_mfma_f32_16x16x32_bf16 v[68:71], v[88:91], v[76:79], v[68:71]\n"
"s_waitcnt lgkmcnt(4)\n"
"v_cvt_pk_bf16_f32 v76, v116, v117\n"
"v_cvt_pk_bf16_f32 v77, v118, v119\n"
"v_cvt_pk_bf16_f32 v78, v84, v85\n"
"v_cvt_pk_bf16_f32 v79, v86, v87\n"
"s_cmp_eq_i32 s78, 8\n"
"s_cbranch_scc1 .Lmk_x94s\n"
".Lmk_x94r:\n"
"v_mfma_f32_16x16x32_bf16 v[84:87], v[104:107], v[28:31], v[36:39]\n"
"ds_read_b128 v[100:103], v15 offset:12288\n"
"v_exp_f32_e32 v64, v64\n"
"v_exp_f32_e32 v65, v65\n"
"v_exp_f32_e32 v66, v66\n"
"v_mfma_f32_16x16x32_bf16 v[108:111], v[104:107], v[32:35], v[60:63]\n"
"buffer_load_dwordx4 a[80:83], v1, s[4:7], s98 offen sc1\n"
"v_exp_f32_e32 v67, v67\n"
"v_mfma_f32_16x16x32_bf16 v[104:107], v[144:147], v[32:35], v[60:63]\n"
"ds_read_b128 v[116:119], v254 offset:12544\n"
"v_exp_f32_e32 v148, v148\n"
"v_exp_f32_e32 v149, v149\n"
"v_mfma_f32_16x16x32_bf16 v[124:127], v[144:147], v[28:31], v[36:39]\n"
"ds_read_b64_tr_b16 v[128:129], v16 offset:16384\n"
"v_exp_f32_e32 v150, v150\n"
"v_exp_f32_e32 v151, v151\n"
"s_waitcnt lgkmcnt(3)\n"
"v_mfma_f32_16x16x32_bf16 v[132:135], v[88:91], v[152:155], v[132:135]\n"
"buffer_load_dwordx4 a[84:87], v1, s[4:7], s98 offen offset:1024 sc1\n"
"v_cvt_pk_bf16_f32 v144, v64, v65\n"
"v_cvt_pk_bf16_f32 v145, v66, v67\n"
"v_cvt_pk_bf16_f32 v146, v148, v149\n"
"v_mfma_f32_16x16x32_bf16 v[44:47], v[120:123], v[152:155], v[44:47]\n"
"ds_read_b64_tr_b16 v[130:131], v255 offset:16640\n"
"v_cvt_pk_bf16_f32 v147, v150, v151\n"
"v_exp_f32_e32 v140, v140\n"
"v_exp_f32_e32 v141, v141\n"
"v_mfma_f32_16x16x32_bf16 v[112:115], v[40:43], v[152:155], v[112:115]\n"
"ds_read_b64_tr_b16 v[64:65], v255 offset:16384\n"
"v_exp_f32_e32 v142, v142\n"
"v_exp_f32_e32 v143, v143\n"
"v_mfma_f32_16x16x32_bf16 v[136:139], v[40:43], v[76:79], v[136:139]\n"
"buffer_load_dwordx4 a[88:91], v1, s[4:7], s98 offen offset:2048 sc1\n"
"v_exp_f32_e32 v72, v72\n"
"v_exp_f32_e32 v73, v73\n"
"v_mfma_f32_16x16x32_bf16 v[80:83], v[120:123], v[76:79], v[80:83]\n"
"ds_read_b64_tr_b16 v[66:67], v16 offset:16640\n"
"v_exp_f32_e32 v74, v74\n"
"v_exp_f32_e32 v75, v75\n"
"v_mfma_f32_16x16x32_bf16 v[68:71], v[88:91], v[76:79], v[68:71]\n"
"s_waitcnt lgkmcnt(4)\n"
"v_cvt_pk_bf16_f32 v40, v140, v141\n"
"v_cvt_pk_bf16_f32 v41, v142, v143\n"
"v_cvt_pk_bf16_f32 v42, v72, v73\n"
"v_cvt_pk_bf16_f32 v43, v74, v75\n"
"s_cmp_eq_i32 s78, 7\n"
"s_cbranch_scc1 .Lmk_x95s\n"
".Lmk_x95r:\n"
"v_mfma_f32_16x16x32_bf16 v[72:75], v[100:103], v[28:31], v[36:39]\n"
"ds_read_b128 v[76:79], v15 offset:10240\n"
"v_exp_f32_e32 v84, v84\n"
"v_exp_f32_e32 v85, v85\n"
"v_exp_f32_e32 v86, v86\n"
"v_mfma_f32_16x16x32_bf16 v[120:123], v[100:103], v[32:35], v[60:63]\n"
"buffer_load_dwordx4 a[92:95], v1, s[4:7], s98 offen offset:3072 sc1\n"
"v_exp_f32_e32 v87, v87\n"
"v_mfma_f32_16x16x32_bf16 v[100:103], v[116:119], v[32:35], v[60:63]\n"
"ds_read_b128 v[140:143], v254 offset:10496\n"
"v_exp_f32_e32 v124, v124\n"
"v_exp_f32_e32 v125, v125\n"
"v_mfma_f32_16x16x32_bf16 v[148:151], v[116:119], v[28:31], v[36:39]\n"
"ds_read_b64_tr_b16 v[116:117], v16 offset:14336\n"
"v_exp_f32_e32 v126, v126\n"
"v_exp_f32_e32 v127, v127\n"
"v_cvt_pk_bf16_f32 v152, v84, v85\n"
"s_waitcnt lgkmcnt(3)\n"
"v_mfma_f32_16x16x32_bf16 v[132:135], v[88:91], v[144:147], v[132:135]\n"
"s_add_u32 s98, s69, 0x606000\n"
"buffer_load_dwordx4 a[96:99], v0, s[4:7], s98 offen sc1\n"
"v_cvt_pk_bf16_f32 v153, v86, v87\n"
"v_cvt_pk_bf16_f32 v154, v124, v125\n"
"v_mfma_f32_16x16x32_bf16 v[44:47], v[128:131], v[144:147], v[44:47]\n"
"ds_read_b64_tr_b16 v[118:119], v255 offset:14592\n"
"v_cvt_pk_bf16_f32 v155, v126, v127\n"
"v_exp_f32_e32 v108, v108\n"
"v_exp_f32_e32 v109, v109\n"
"v_mfma_f32_16x16x32_bf16 v[112:115], v[64:67], v[144:147], v[112:115]\n"
"ds_read_b64_tr_b16 v[84:85], v255 offset:14336\n"
"v_exp_f32_e32 v110, v110\n"
"v_exp_f32_e32 v111, v111\n"
"v_mfma_f32_16x16x32_bf16 v[136:139], v[64:67], v[40:43], v[136:139]\n"
"buffer_load_dwordx4 a[100:103], v0, s[4:7], s98 offen offset:1024 sc1\n"
"v_exp_f32_e32 v104, v104\n"
"v_exp_f32_e32 v105, v105\n"
"v_mfma_f32_16x16x32_bf16 v[80:83], v[128:131], v[40:43], v[80:83]\n"
"ds_read_b64_tr_b16 v[86:87], v16 offset:14592\n"
"v_exp_f32_e32 v106, v106\n"
"v_exp_f32_e32 v107, v107\n"
"v_mfma_f32_16x16x32_bf16 v[68:71], v[88:91], v[40:43], v[68:71]\n"
"s_waitcnt lgkmcnt(4)\n"
"v_cvt_pk_bf16_f32 v40, v108, v109\n"
"v_cvt_pk_bf16_f32 v41, v110, v111\n"
"v_cvt_pk_bf16_f32 v42, v104, v105\n"
"v_cvt_pk_bf16_f32 v43, v106, v107\n"
"s_cmp_eq_i32 s78, 6\n"
"s_cbranch_scc1 .Lmk_x96s\n"
".Lmk_x96r:\n"
"v_mfma_f32_16x16x32_bf16 v[64:67], v[76:79], v[28:31], v[36:39]\n"
"ds_read_b128 v[104:107], v15 offset:8192\n"
"v_exp_f32_e32 v72, v72\n"
"v_exp_f32_e32 v73, v73\n"
"v_exp_f32_e32 v74, v74\n"
"v_mfma_f32_16x16x32_bf16 v[108:111], v[76:79], v[32:35], v[60:63]\n"
"buffer_load_dwordx4 a[104:107], v0, s[4:7], s98 offen offset:2048 sc1\n"
"v_exp_f32_e32 v75, v75\n"
"v_mfma_f32_16x16x32_bf16 v[76:79], v[140:143], v[32:35], v[60:63]\n"
"ds_read_b128 v[124:127], v254 offset:8448\n"
"v_exp_f32_e32 v148, v148\n"
"v_exp_f32_e32 v149, v149\n"
"v_mfma_f32_16x16x32_bf16 v[128:131], v[140:143], v[28:31], v[36:39]\n"
"ds_read_b64_tr_b16 v[140:141], v16 offset:12288\n"
"v_exp_f32_e32 v150, v150\n"
"v_exp_f32_e32 v151, v151\n"
"s_waitcnt lgkmcnt(3)\n"
"v_mfma_f32_16x16x32_bf16 v[132:135], v[88:91], v[152:155], v[132:135]\n"
"buffer_load_dwordx4 a[108:111], v0, s[4:7], s98 offen offset:3072 sc1\n"
"v_cvt_pk_bf16_f32 v144, v72, v73\n"
"v_cvt_pk_bf16_f32 v145, v74, v75\n"
"v_cvt_pk_bf16_f32 v146, v148, v149\n"
"v_mfma_f32_16x16x32_bf16 v[44:47], v[116:119], v[152:155], v[44:47]\n"
"ds_read_b64_tr_b16 v[142:143], v255 offset:12544\n"
"v_cvt_pk_bf16_f32 v147, v150, v151\n"
"v_exp_f32_e32 v120, v120\n"
"v_exp_f32_e32 v121, v121\n"
"v_mfma_f32_16x16x32_bf16 v[112:115], v[84:87], v[152:155], v[112:115]\n"
"ds_read_b64_tr_b16 v[72:73], v255 offset:12288\n"
"v_exp_f32_e32 v122, v122\n"
"v_exp_f32_e32 v123, v123\n"
"v_mfma_f32_16x16x32_bf16 v[136:139], v[84:87], v[40:43], v[136:139]\n"
"buffer_load_dwordx4 a[112:115], v1, s[4:7], s98 offen sc1\n"
"v_exp_f32_e32 v100, v100\n"
"v_exp_f32_e32 v101, v101\n"
"v_mfma_f32_16x16x32_bf16 v[80:83], v[116:119], v[40:43], v[80:83]\n"
"ds_read_b64_tr_b16 v[74:75], v16 offset:12544\n"
"v_exp_f32_e32 v102, v102\n"
"v_exp_f32_e32 v103, v103\n"
"v_mfma_f32_16x16x32_bf16 v[68:71], v[88:91], v[40:43], v[68:71]\n"
"s_waitcnt lgkmcnt(4)\n"
"v_cvt_pk_bf16_f32 v40, v120, v121\n"
"v_cvt_pk_bf16_f32 v41, v122, v123\n"
"v_cvt_pk_bf16_f32 v42, v100, v101\n"
"v_cvt_pk_bf16_f32 v43, v102, v103\n"
"s_cmp_eq_i32 s78, 5\n"
"s_cbranch_scc1 .Lmk_x97s\n"
".Lmk_x97r:\n"
"v_mfma_f32_16x16x32_bf16 v[84:87], v[104:107], v[28:31], v[36:39]\n"
"ds_read_b64_tr_b16 v[100:101], v16 offset:10240\n"
"ds_read_b64_tr_b16 v[102:103], v255 offset:10496\n"
"v_exp_f32_e32 v64, v64\n"
"v_exp_f32_e32 v65, v65\n"
"v_mfma_f32_16x16x32_bf16 v[116:119], v[104:107], v[32:35], v[60:63]\n"
"buffer_load_dwordx4 a[116:119], v1, s[4:7], s98 offen offset:1024 sc1\n"
"v_exp_f32_e32 v66, v66\n"
"v_exp_f32_e32 v67, v67\n"
"v_mfma_f32_16x16x32_bf16 v[104:107], v[124:127], v[32:35], v[60:63]\n"
"ds_read_b64_tr_b16 v[60:61], v255 offset:10240\n"
"ds_read_b64_tr_b16 v[62:63], v16 offset:10496\n"
"v_exp_f32_e32 v128, v128\n"
"v_exp_f32_e32 v129, v129\n"
"v_mfma_f32_16x16x32_bf16 v[120:123], v[124:127], v[28:31], v[36:39]\n"
"ds_read_b64_tr_b16 v[36:37], v16 offset:24576\n"
"v_exp_f32_e32 v130, v130\n"
"v_exp_f32_e32 v131, v131\n"
"s_waitcnt lgkmcnt(5)\n"
"v_mfma_f32_16x16x32_bf16 v[132:135], v[88:91], v[144:147], v[132:135]\n"
"buffer_load_dwordx4 a[120:123], v1, s[4:7], s98 offen offset:2048 sc1\n"
"ds_read_b64_tr_b16 v[38:39], v255 offset:24832\n"
"v_cvt_pk_bf16_f32 v124, v64, v65\n"
"v_cvt_pk_bf16_f32 v125, v66, v67\n"
"v_cvt_pk_bf16_f32 v126, v128, v129\n"
"v_mfma_f32_16x16x32_bf16 v[44:47], v[140:143], v[144:147], v[44:47]\n"
"ds_read_b64_tr_b16 v[64:65], v255 offset:24576\n"
"v_cvt_pk_bf16_f32 v127, v130, v131\n"
"v_exp_f32_e32 v108, v108\n"
"v_exp_f32_e32 v109, v109\n"
"v_mfma_f32_16x16x32_bf16 v[112:115], v[72:75], v[144:147], v[112:115]\n"
"ds_read_b64_tr_b16 v[66:67], v16 offset:24832\n"
"v_exp_f32_e32 v110, v110\n"
"v_exp_f32_e32 v111, v111\n"
"v_mfma_f32_16x16x32_bf16 v[136:139], v[72:75], v[40:43], v[136:139]\n"
"buffer_load_dwordx4 a[124:127], v1, s[4:7], s98 offen offset:3072 sc1\n"
"v_exp_f32_e32 v76, v76\n"
"v_add_u32_e32 v248, 0x26400, v0\n"
"v_exp_f32_e32 v77, v77\n"
"v_mfma_f32_16x16x32_bf16 v[80:83], v[140:143], v[40:43], v[80:83]\n"
"ds_read_b128 v[72:75], v248\n"
"v_exp_f32_e32 v78, v78\n"
"v_exp_f32_e32 v79, v79\n"
"v_mfma_f32_16x16x32_bf16 v[68:71], v[88:91], v[40:43], v[68:71]\n"
"ds_read_b128 v[128:131], v248 offset:1024\n"
"v_cvt_pk_bf16_f32 v40, v108, v109\n"
"v_cvt_pk_bf16_f32 v41, v110, v111\n"
"v_cvt_pk_bf16_f32 v42, v76, v77\n"
"v_cvt_pk_bf16_f32 v43, v78, v79\n"
"s_waitcnt lgkmcnt(6)\n"
"v_mfma_f32_16x16x32_bf16 v[132:135], v[88:91], v[124:127], v[132:135]\n"
"ds_read_b64_tr_b16 v[76:77], v16 offset:8192\n"
"v_cndmask_b32_e64 v84, v48, v84, s[20:21]\n"
"v_cndmask_b32_e64 v85, v49, v85, s[22:23]\n"
"v_cndmask_b32_e64 v86, v50, v86, s[24:25]\n"
"s_add_u32 m0, s77, 0x4000\n"
"s_add_u32 s98, s65, 0\n"
"buffer_load_dwordx4 v2, s[4:7], s98 offen sc1 lds\n"
"buffer_load_dwordx4 v2, s[4:7], s98 offen offset:1024 sc1 lds\n"
"v_cndmask_b32_e64 v87, v51, v87, s[26:27]\n"
"v_cndmask_b32_e64 v120, v24, v120, s[28:29]\n"
"v_mfma_f32_16x16x32_bf16 v[44:47], v[100:103], v[124:127], v[44:47]\n"
"ds_read_b64_tr_b16 v[78:79], v255 offset:8448\n"
"v_cndmask_b32_e64 v121, v25, v121, s[30:31]\n"
"v_cndmask_b32_e64 v122, v26, v122, s[32:33]\n"
"v_cndmask_b32_e64 v123, v27, v123, s[34:35]\n"
"v_exp_f32_e32 v84, v84\n"
"v_exp_f32_e32 v85, v85\n"
"v_exp_f32_e32 v86, v86\n"
"v_exp_f32_e32 v87, v87\n"
"v_mfma_f32_16x16x32_bf16 v[112:115], v[60:63], v[124:127], v[112:115]\n"
"v_exp_f32_e32 v120, v120\n"
"v_exp_f32_e32 v121, v121\n"
"v_exp_f32_e32 v122, v122\n"
"v_exp_f32_e32 v123, v123\n"
"v_cvt_pk_bf16_f32 v48, v84, v85\n"
"v_cvt_pk_bf16_f32 v49, v86, v87\n"
"v_cvt_pk_bf16_f32 v50, v120, v121\n"
"v_mfma_f32_16x16x32_bf16 v[136:139], v[60:63], v[40:43], v[136:139]\n"
"ds_read_b64_tr_b16 v[60:61], v255 offset:8192\n"
"s_add_u32 s98, s69, 0x608000\n"
"buffer_load_dwordx4 v[24:27], v0, s[4:7], s98 offen sc1\n"
"v_cvt_pk_bf16_f32 v51, v122, v123\n"
"v_cndmask_b32_e64 v116, v52, v116, s[36:37]\n"
"v_cndmask_b32_e64 v117, v53, v117, s[38:39]\n"
"v_cndmask_b32_e64 v118, v54, v118, s[40:41]\n"
"v_cndmask_b32_e64 v119, v55, v119, s[42:43]\n"
"v_cndmask_b32_e64 v104, v56, v104, s[44:45]\n"
"v_cndmask_b32_e64 v105, v57, v105, s[46:47]\n"
"v_mfma_f32_16x16x32_bf16 v[80:83], v[100:103], v[40:43], v[80:83]\n"
"ds_read_b64_tr_b16 v[62:63], v16 offset:8448\n"
"v_cndmask_b32_e64 v106, v58, v106, s[48:49]\n"
"v_cndmask_b32_e64 v107, v59, v107, s[50:51]\n"
"v_exp_f32_e32 v116, v116\n"
"v_exp_f32_e32 v117, v117\n"
"v_exp_f32_e32 v118, v118\n"
"v_exp_f32_e32 v119, v119\n"
"v_exp_f32_e32 v104, v104\n"
"v_mfma_f32_16x16x32_bf16 v[68:71], v[88:91], v[40:43], v[68:71]\n"
"s_waitcnt lgkmcnt(0)\n"
"v_exp_f32_e32 v105, v105\n"
"v_exp_f32_e32 v106, v106\n"
"v_exp_f32_e32 v107, v107\n"
"v_cvt_pk_bf16_f32 v40, v116, v117\n"
"v_cvt_pk_bf16_f32 v41, v118, v119\n"
"v_cvt_pk_bf16_f32 v42, v104, v105\n"
"v_cvt_pk_bf16_f32 v43, v106, v107\n"
"v_mfma_f32_16x16x32_bf16 v[132:135], v[88:91], v[48:51], v[132:135]\n"
"v_and_b32_e32 v72, v48, v72\n"
"v_and_b32_e32 v73, v49, v73\n"
"v_and_b32_e32 v74, v50, v74\n"
"v_and_b32_e32 v75, v51, v75\n"
"v_mfma_f32_16x16x32_bf16 v[68:71], v[88:91], v[40:43], v[68:71]\n"
"v_xor_b32_e32 v52, v48, v72\n"
"v_xor_b32_e32 v53, v49, v73\n"
"v_xor_b32_e32 v54, v50, v74\n"
"v_xor_b32_e32 v55, v51, v75\n"
"v_mfma_f32_16x16x32_bf16 v[44:47], v[76:79], v[72:75], v[44:47]\n"
"v_and_b32_e32 v128, v40, v128\n"
"v_and_b32_e32 v129, v41, v129\n"
"v_mfma_f32_16x16x32_bf16 v[112:115], v[60:63], v[72:75], v[112:115]\n"
"v_and_b32_e32 v130, v42, v130\n"
"v_and_b32_e32 v131, v43, v131\n"
"v_mfma_f32_16x16x32_bf16 v[44:47], v[36:39], v[52:55], v[44:47]\n"
"v_xor_b32_e32 v48, v40, v128\n"
"v_xor_b32_e32 v49, v41, v129\n"
"v_mfma_f32_16x16x32_bf16 v[112:115], v[64:67], v[52:55], v[112:115]\n"
"v_xor_b32_e32 v50, v42, v130\n"
"v_xor_b32_e32 v51, v43, v131\n"
"v_mfma_f32_16x16x32_bf16 v[80:83], v[76:79], v[128:131], v[80:83]\n"
"s_add_u32 m0, s77, 0xc000\n"
"s_add_u32 s97, s65, 0x4000\n"
"buffer_load_dwordx4 v2, s[4:7], s97 offen sc1 lds\n"
"buffer_load_dwordx4 v2, s[4:7], s97 offen offset:1024 sc1 lds\n"
"v_mfma_f32_16x16x32_bf16 v[136:139], v[60:63], v[128:131], v[136:139]\n"
"v_mfma_f32_16x16x32_bf16 v[80:83], v[36:39], v[48:51], v[80:83]\n"
"v_mfma_f32_16x16x32_bf16 v[136:139], v[64:67], v[48:51], v[136:139]\n"
"v_max_f32_e32 v248, v132, v68\n"
"v_rcp_f32_e32 v132, v132\n"
"v_rcp_f32_e32 v68, v68\n"
"s_nop 0\n"
"v_max3_f32 v248, v248, v132, v68\n"
"v_cmp_nlt_f32_e32 vcc, v248, v252\n"
"s_cbranch_vccnz .Lmk_b92\n"
".Lmk_k98:\n"
"s_add_u32 s97, s60, 0x40000\n"
"v_mul_f32_e32 v44, v44, v132\n"
"v_mul_f32_e32 v45, v45, v132\n"
"v_mul_f32_e32 v46, v46, v132\n"
"v_mul_f32_e32 v47, v47, v132\n"
"v_mul_f32_e32 v112, v112, v132\n"
"v_mul_f32_e32 v113, v113, v132\n"
"v_mul_f32_e32 v114, v114, v132\n"
"v_mul_f32_e32 v115, v115, v132\n"
"v_cvt_pk_bf16_f32 v28, v44, v45\n"
"v_cvt_pk_bf16_f32 v29, v46, v47\n"
"v_cvt_pk_bf16_f32 v30, v112, v113\n"
"v_cvt_pk_bf16_f32 v31, v114, v115\n"
"buffer_store_dwordx4 v[28:31], v0, s[4:7], s97 offen sc1\n"
"s_add_u32 s97, s60, 0x48000\n"
"v_mul_f32_e32 v80, v80, v68\n"
"v_mul_f32_e32 v81, v81, v68\n"
"v_mul_f32_e32 v82, v82, v68\n"
"v_mul_f32_e32 v83, v83, v68\n"
"v_mul_f32_e32 v136, v136, v68\n"
"v_mul_f32_e32 v137, v137, v68\n"
"v_mul_f32_e32 v138, v138, v68\n"
"v_mul_f32_e32 v139, v139, v68\n"
"v_cvt_pk_bf16_f32 v28, v80, v81\n"
"v_cvt_pk_bf16_f32 v29, v82, v83\n"
"v_cvt_pk_bf16_f32 v30, v136, v137\n"
"v_cvt_pk_bf16_f32 v31, v138, v139\n"
"buffer_store_dwordx4 v[28:31], v0, s[4:7], s97 offen sc1\n"
";\n"
"; @A AT a\n"
"s_mov_b32 s2, 0\n"
"v_mov_b32_e32 v253, 0xff800000\n"
"v_mov_b32_e32 v251, 0xff800000\n"
"v_mov_b32_e32 v250, 0x7f800000\n"
"v_mov_b32_e32 v249, 0x7f800000\n"
".Lmk_a99:\n"
"ds_read_b128 v[28:31], v15 offset:16384\n"
"ds_read_b128 v[32:35], v254 offset:16640\n"
"ds_read_b128 v[36:39], v15 offset:14336\n"
"ds_read_b128 v[40:43], v254 offset:14592\n"
"s_cmp_gt_i32 s78, 7\n"
"s_cselect_b32 s97, 0x80000000, 0\n"
"s_or_b32 s97, s97, 0x7f800000\n"
"s_add_u32 s96, s67, 32\n"
"buffer_load_dwordx2 v[246:247], v12, s[4:7], s96 offen sc1\n"
"s_waitcnt lgkmcnt(2)\n"
"v_mfma_f32_16x16x32_bf16 v[100:103], v[28:31], v[92:95], 0\n"
"v_mfma_f32_16x16x32_bf16 v[104:107], v[28:31], v[96:99], 0\n"
"v_mfma_f32_16x16x32_bf16 v[108:111], v[32:35], v[96:99], 0\n"
"v_mfma_f32_16x16x32_bf16 v[112:115], v[32:35], v[92:95], 0\n"
"s_waitcnt lgkmcnt(0)\n"
"s_nop 3\n"
"v_max_f32_e32 v248, v100, v101\n"
"v_max_f32_e32 v245, v104, v105\n"
"v_max3_f32 v248, v248, v102, v103\n"
"v_max3_f32 v245, v245, v106, v107\n"
"v_max3_f32 v248, v248, v112, v113\n"
"v_max3_f32 v245, v245, v108, v109\n"
"v_max3_f32 v248, v248, v114, v115\n"
"v_max3_f32 v245, v245, v110, v111\n"
"v_mov_b32_e32 v244, v248\n"
"v_mov_b32_e32 v243, v245\n"
"s_nop 0\n"
"v_permlane16_swap_b32_e32 v248, v244\n"
"v_permlane16_swap_b32_e32 v245, v243\n"
"v_max_f32_e32 v248, v248, v244\n"
"v_max_f32_e32 v245, v245, v243\n"
"v_mov_b32_e32 v244, v248\n"
"v_mov_b32_e32 v243, v245\n"
"s_nop 0\n"
"v_permlane32_swap_b32_e32 v248, v244\n"
"v_permlane32_swap_b32_e32 v245, v243\n"
"v_max_f32_e32 v248, v248, v244\n"
"v_max_f32_e32 v245, v245, v243\n"
"v_med3_f32 v248, v248, v253, v250\n"
"v_med3_f32 v245, v245, v251, v249\n"
"v_min_f32_e64 v116, s97, -v248\n"
"v_min_f32_e64 v117, s97, -v248\n"
"v_min_f32_e64 v118, s97, -v248\n"
"v_min_f32_e64 v119, s97, -v248\n"
"v_min_f32_e64 v120, s97, -v245\n"
"v_min_f32_e64 v121, s97, -v245\n"
"v_min_f32_e64 v122, s97, -v245\n"
"v_min_f32_e64 v123, s97, -v245\n"
"v_mfma_f32_16x16x32_bf16 v[44:47], v[36:39], v[92:95], v[116:119]\n"
"ds_read_b128 v[32:35], v15 offset:12288\n"
"v_sub_f32_e32 v100, v100, v248\n"
"v_sub_f32_e32 v101, v101, v248\n"
"v_sub_f32_e32 v102, v102, v248\n"
"v_sub_f32_e32 v103, v103, v248\n"
"v_mfma_f32_16x16x32_bf16 v[48:51], v[36:39], v[96:99], v[120:123]\n"
"v_sub_f32_e32 v112, v112, v248\n"
"v_sub_f32_e32 v113, v113, v248\n"
"v_sub_f32_e32 v114, v114, v248\n"
"v_sub_f32_e32 v115, v115, v248\n"
"v_mfma_f32_16x16x32_bf16 v[52:55], v[40:43], v[96:99], v[120:123]\n"
"ds_read_b128 v[56:59], v254 offset:12544\n"
"v_sub_f32_e32 v104, v104, v245\n"
"v_sub_f32_e32 v105, v105, v245\n"
"v_sub_f32_e32 v106, v106, v245\n"
"v_sub_f32_e32 v107, v107, v245\n"
"v_mfma_f32_16x16x32_bf16 v[36:39], v[40:43], v[92:95], v[116:119]\n"
"s_waitcnt lgkmcnt(0)\n"
"v_sub_f32_e32 v108, v108, v245\n"
"v_sub_f32_e32 v109, v109, v245\n"
"v_sub_f32_e32 v110, v110, v245\n"
"v_sub_f32_e32 v111, v111, v245\n"
"s_cmp_eq_i32 s78, 7\n"
"s_cbranch_scc1 .Lmk_x101s\n"
".Lmk_x101r:\n"
"v_mfma_f32_16x16x32_bf16 v[60:63], v[32:35], v[92:95], v[116:119]\n"
"ds_read_b128 v[40:43], v15 offset:10240\n"
"buffer_load_dwordx4 v[28:31], v0, s[4:7], s98 offen offset:1024 sc1\n"
"ds_read_b128 v[64:67], v254 offset:10496\n"
"v_exp_f32_e32 v44, v44\n"
"v_exp_f32_e32 v45, v45\n"
"v_exp_f32_e32 v46, v46\n"
"v_exp_f32_e32 v47, v47\n"
"v_exp_f32_e32 v36, v36\n"
"v_mfma_f32_16x16x32_bf16 v[68:71], v[32:35], v[96:99], v[120:123]\n"
"ds_read_b64_tr_b16 v[72:73], v16 offset:14336\n"
"buffer_load_dwordx4 v[32:35], v0, s[4:7], s98 offen offset:2048 sc1\n"
"v_exp_f32_e32 v37, v37\n"
"v_exp_f32_e32 v38, v38\n"
"v_exp_f32_e32 v39, v39\n"
"v_cvt_pk_bf16_f32 v76, v44, v45\n"
"v_cvt_pk_bf16_f32 v77, v46, v47\n"
"v_cvt_pk_bf16_f32 v78, v36, v37\n"
"v_mfma_f32_16x16x32_bf16 v[80:83], v[56:59], v[96:99], v[120:123]\n"
"ds_read_b64_tr_b16 v[74:75], v255 offset:14592\n"
"ds_read_b64_tr_b16 v[84:85], v255 offset:14336\n"
"v_cvt_pk_bf16_f32 v79, v38, v39\n"
"v_exp_f32_e32 v48, v48\n"
"v_exp_f32_e32 v49, v49\n"
"v_exp_f32_e32 v50, v50\n"
"v_exp_f32_e32 v51, v51\n"
"v_mfma_f32_16x16x32_bf16 v[124:127], v[56:59], v[92:95], v[116:119]\n"
"buffer_load_dwordx4 v[36:39], v0, s[4:7], s98 offen offset:3072 sc1\n"
"ds_read_b64_tr_b16 v[86:87], v16 offset:14592\n"
"v_exp_f32_e32 v52, v52\n"
"v_exp_f32_e32 v53, v53\n"
"v_exp_f32_e32 v54, v54\n"
"v_exp_f32_e32 v55, v55\n"
"v_cvt_pk_bf16_f32 v56, v48, v49\n"
"v_cvt_pk_bf16_f32 v57, v50, v51\n"
"v_cvt_pk_bf16_f32 v58, v52, v53\n"
"v_cvt_pk_bf16_f32 v59, v54, v55\n"
"s_waitcnt lgkmcnt(4)\n"
"s_cmp_eq_i32 s78, 6\n"
"s_cbranch_scc1 .Lmk_x102s\n"
".Lmk_x102r:\n"
"v_mfma_f32_16x16x32_bf16 v[128:131], v[40:43], v[92:95], v[116:119]\n"
"ds_read_b128 v[132:135], v15 offset:8192\n"
"ds_read_b128 v[136:139], v254 offset:8448\n"
"v_exp_f32_e32 v60, v60\n"
"v_exp_f32_e32 v61, v61\n"
"v_mfma_f32_16x16x32_bf16 v[140:143], v[40:43], v[96:99], v[120:123]\n"
"buffer_load_dwordx4 v[40:43], v1, s[4:7], s98 offen sc1\n"
"v_exp_f32_e32 v62, v62\n"
"v_exp_f32_e32 v63, v63\n"
"v_mfma_f32_16x16x32_bf16 v[144:147], v[64:67], v[96:99], v[120:123]\n"
"ds_read_b64_tr_b16 v[148:149], v16 offset:12288\n"
"ds_read_b64_tr_b16 v[150:151], v255 offset:12544\n"
"v_exp_f32_e32 v124, v124\n"
"v_exp_f32_e32 v125, v125\n"
"v_mfma_f32_16x16x32_bf16 v[152:155], v[64:67], v[92:95], v[116:119]\n"
"ds_read_b64_tr_b16 v[64:65], v255 offset:12288\n"
"v_exp_f32_e32 v126, v126\n"
"v_exp_f32_e32 v127, v127\n"
"s_waitcnt lgkmcnt(5)\n"
"v_mfma_f32_16x16x32_bf16 v[156:159], v[88:91], v[76:79], 0\n"
"buffer_load_dwordx4 v[44:47], v1, s[4:7], s98 offen offset:1024 sc1\n"
"v_cvt_pk_bf16_f32 v160, v60, v61\n"
"v_cvt_pk_bf16_f32 v161, v62, v63\n"
"v_cvt_pk_bf16_f32 v162, v124, v125\n"
"v_cvt_pk_bf16_f32 v163, v126, v127\n"
"v_mfma_f32_16x16x32_bf16 v[124:127], v[72:75], v[76:79], 0\n"
"ds_read_b64_tr_b16 v[66:67], v16 offset:12544\n"
"s_waitcnt vmcnt(8)\n"
"ds_read_b128 v[164:167], v2 offset:16384\n"
"v_exp_f32_e32 v68, v68\n"
"v_exp_f32_e32 v69, v69\n"
"v_mfma_f32_16x16x32_bf16 v[168:171], v[84:87], v[76:79], 0\n"
"v_exp_f32_e32 v70, v70\n"
"v_exp_f32_e32 v71, v71\n"
"v_mfma_f32_16x16x32_bf16 v[172:175], v[84:87], v[56:59], 0\n"
"buffer_load_dwordx4 v[48:51], v1, s[4:7], s98 offen offset:2048 sc1\n"
"ds_read_b128 v[76:79], v2 offset:17408\n"
"v_exp_f32_e32 v80, v80\n"
"v_exp_f32_e32 v81, v81\n"
"v_mfma_f32_16x16x32_bf16 v[176:179], v[72:75], v[56:59], 0\n"
"ds_read_b128 v[72:75], v2 offset:49152\n"
"v_exp_f32_e32 v82, v82\n"
"v_exp_f32_e32 v83, v83\n"
"v_mfma_f32_16x16x32_bf16 v[180:183], v[88:91], v[56:59], 0\n"
"ds_read_b128 v[84:87], v2 offset:50176\n"
"v_cvt_pk_bf16_f32 v184, v68, v69\n"
"v_cvt_pk_bf16_f32 v185, v70, v71\n"
"v_cvt_pk_bf16_f32 v186, v80, v81\n"
"v_cvt_pk_bf16_f32 v187, v82, v83\n"
"s_waitcnt lgkmcnt(8)\n"
"s_cmp_eq_i32 s78, 5\n"
"s_cbranch_scc1 .Lmk_x103s\n"
".Lmk_x103r:\n"
"v_mfma_f32_16x16x32_bf16 v[80:83], v[132:135], v[92:95], v[116:119]\n"
"ds_read_b64_tr_b16 v[68:69], v16 offset:10240\n"
"ds_read_b64_tr_b16 v[70:71], v255 offset:10496\n"
"buffer_load_dwordx4 v[52:55], v1, s[4:7], s98 offen offset:3072 sc1\n"
"v_exp_f32_e32 v128, v128\n"
"v_exp_f32_e32 v129, v129\n"
"v_mfma_f32_16x16x32_bf16 v[188:191], v[132:135], v[96:99], v[120:123]\n"
"ds_read_b64_tr_b16 v[132:133], v255 offset:10240\n"
"v_exp_f32_e32 v130, v130\n"
"v_exp_f32_e32 v131, v131\n"
"v_mfma_f32_16x16x32_bf16 v[192:195], v[136:139], v[96:99], v[120:123]\n"
"ds_read_b64_tr_b16 v[134:135], v16 offset:10496\n"
"v_exp_f32_e32 v152, v152\n"
"v_exp_f32_e32 v153, v153\n"
"v_exp_f32_e32 v154, v154\n"
"v_mfma_f32_16x16x32_bf16 v[196:199], v[136:139], v[92:95], v[116:119]\n"
"s_waitcnt lgkmcnt(4)\n"
"s_add_u32 s97, s69, 0x60a000\n"
"buffer_load_dwordx4 v[56:59], v0, s[4:7], s97 offen sc1\n"
"v_max_u32_e32 v248, v164, v165\n"
"v_exp_f32_e32 v155, v155\n"
"v_mfma_f32_16x16x32_bf16 v[156:159], v[88:91], v[160:163], v[156:159]\n"
"v_max3_u32 v248, v248, v166, v167\n"
"v_cvt_pk_bf16_f32 v136, v128, v129\n"
"v_cvt_pk_bf16_f32 v137, v130, v131\n"
"v_cvt_pk_bf16_f32 v138, v152, v153\n"
"v_mfma_f32_16x16x32_bf16 v[124:127], v[148:151], v[160:163], v[124:127]\n"
"v_max3_u32 v248, v248, v76, v77\n"
"v_cvt_pk_bf16_f32 v139, v154, v155\n"
"v_exp_f32_e32 v140, v140\n"
"v_max3_u32 v248, v248, v78, v79\n"
"v_exp_f32_e32 v141, v141\n"
"v_mfma_f32_16x16x32_bf16 v[168:171], v[64:67], v[160:163], v[168:171]\n"
"v_exp_f32_e32 v142, v142\n"
"v_max3_u32 v248, v248, v72, v73\n"
"v_exp_f32_e32 v143, v143\n"
"v_mfma_f32_16x16x32_bf16 v[172:175], v[64:67], v[184:187], v[172:175]\n"
"buffer_load_dwordx4 v[60:63], v0, s[4:7], s97 offen offset:1024 sc1\n"
"v_max3_u32 v248, v248, v74, v75\n"
"v_exp_f32_e32 v144, v144\n"
"v_exp_f32_e32 v145, v145\n"
"v_mfma_f32_16x16x32_bf16 v[176:179], v[148:151], v[184:187], v[176:179]\n"
"v_max3_u32 v248, v248, v84, v85\n"
"v_exp_f32_e32 v146, v146\n"
"v_max3_u32 v248, v248, v86, v87\n"
"v_exp_f32_e32 v147, v147\n"
"v_mfma_f32_16x16x32_bf16 v[180:183], v[88:91], v[184:187], v[180:183]\n"
"v_cvt_pk_bf16_f32 v128, v140, v141\n"
"v_cmp_eq_u32_e32 vcc, -1, v248\n"
"v_cvt_pk_bf16_f32 v129, v142, v143\n"
"v_cvt_pk_bf16_f32 v130, v144, v145\n"
"v_cvt_pk_bf16_f32 v131, v146, v147\n"
"s_cbranch_vccnz .Lmk_h90s\n"
".Lmk_h90r:\n"
"s_cmp_lg_u32 s2, 0\n"
"s_cbranch_scc1 .Lmk_s104\n"
"s_barrier\n"
".Lmk_s104:\n"
"ds_read_b128 v[72:75], v15 offset:6144\n"
"ds_read_b128 v[76:79], v254 offset:6400\n"
"s_waitcnt lgkmcnt(2)\n"
"v_mfma_f32_16x16x32_bf16 v[156:159], v[88:91], v[136:139], v[156:159]\n"
"ds_read_b128 v[84:87], v15 offset:4096\n"
"v_exp_f32_e32 v80, v80\n"
"v_exp_f32_e32 v81, v81\n"
"v_exp_f32_e32 v82, v82\n"
"v_mfma_f32_16x16x32_bf16 v[124:127], v[68:71], v[136:139], v[124:127]\n"
"buffer_load_dwordx4 v[64:67], v0, s[4:7], s97 offen offset:2048 sc1\n"
"ds_read_b128 v[140:143], v254 offset:4352\n"
"v_exp_f32_e32 v83, v83\n"
"v_mfma_f32_16x16x32_bf16 v[168:171], v[132:135], v[136:139], v[168:171]\n"
"ds_read_b64_tr_b16 v[136:137], v16 offset:8192\n"
"v_exp_f32_e32 v196, v196\n"
"v_exp_f32_e32 v197, v197\n"
"v_mfma_f32_16x16x32_bf16 v[172:175], v[132:135], v[128:131], v[172:175]\n"
"ds_read_b64_tr_b16 v[138:139], v255 offset:8448\n"
"v_exp_f32_e32 v198, v198\n"
"v_exp_f32_e32 v199, v199\n"
"v_mfma_f32_16x16x32_bf16 v[176:179], v[68:71], v[128:131], v[176:179]\n"
"buffer_load_dwordx4 v[68:71], v0, s[4:7], s97 offen offset:3072 sc1\n"
"v_cvt_pk_bf16_f32 v132, v80, v81\n"
"v_cvt_pk_bf16_f32 v133, v82, v83\n"
"v_cvt_pk_bf16_f32 v134, v196, v197\n"
"v_cvt_pk_bf16_f32 v135, v198, v199\n"
"v_mfma_f32_16x16x32_bf16 v[180:183], v[88:91], v[128:131], v[180:183]\n"
"ds_read_b64_tr_b16 v[128:129], v255 offset:8192\n"
"v_exp_f32_e32 v188, v188\n"
"v_exp_f32_e32 v189, v189\n"
"s_waitcnt lgkmcnt(5)\n"
"v_mfma_f32_16x16x32_bf16 v[144:147], v[72:75], v[92:95], v[116:119]\n"
"ds_read_b64_tr_b16 v[130:131], v16 offset:8448\n"
"v_exp_f32_e32 v190, v190\n"
"v_exp_f32_e32 v191, v191\n"
"v_exp_f32_e32 v192, v192\n"
"v_mfma_f32_16x16x32_bf16 v[148:151], v[72:75], v[96:99], v[120:123]\n"
"buffer_load_dwordx4 v[72:75], v1, s[4:7], s97 offen sc1\n"
"s_waitcnt vmcnt(12)\n"
"v_cmp_ne_u64_e32 vcc, v[246:247], v[18:19]\n"
"v_mfma_f32_16x16x32_bf16 v[152:155], v[76:79], v[96:99], v[120:123]\n"
"v_exp_f32_e32 v193, v193\n"
"v_exp_f32_e32 v194, v194\n"
"v_exp_f32_e32 v195, v195\n"
"v_mfma_f32_16x16x32_bf16 v[160:163], v[76:79], v[92:95], v[116:119]\n"
"s_cbranch_vccnz .Lmk_f74s\n"
".Lmk_f74r:\n"
"v_cvt_pk_bf16_f32 v164, v188, v189\n"
"v_cvt_pk_bf16_f32 v165, v190, v191\n"
"v_cvt_pk_bf16_f32 v166, v192, v193\n"
"v_cvt_pk_bf16_f32 v167, v194, v195\n"
"s_waitcnt lgkmcnt(4)\n"
"v_mfma_f32_16x16x32_bf16 v[184:187], v[84:87], v[92:95], v[116:119]\n"
"ds_read_b128 v[188:191], v15 offset:2048\n"
"v_exp_f32_e32 v144, v144\n"
"v_exp_f32_e32 v145, v145\n"
"v_exp_f32_e32 v146, v146\n"
"v_mfma_f32_16x16x32_bf16 v[192:195], v[84:87], v[96:99], v[120:123]\n"
"buffer_load_dwordx4 v[76:79], v1, s[4:7], s97 offen offset:1024 sc1\n"
"v_exp_f32_e32 v147, v147\n"
"v_mfma_f32_16x16x32_bf16 v[196:199], v[140:143], v[96:99], v[120:123]\n"
"ds_read_b128 v[200:203], v254 offset:2304\n"
"v_exp_f32_e32 v160, v160\n"
"v_exp_f32_e32 v161, v161\n"
"v_mfma_f32_16x16x32_bf16 v[204:207], v[140:143], v[92:95], v[116:119]\n"
"ds_read_b64_tr_b16 v[140:141], v16 offset:6144\n"
"v_exp_f32_e32 v162, v162\n"
"v_exp_f32_e32 v163, v163\n"
"s_waitcnt lgkmcnt(3)\n"
"v_mfma_f32_16x16x32_bf16 v[156:159], v[88:91], v[132:135], v[156:159]\n"
"buffer_load_dwordx4 v[80:83], v1, s[4:7], s97 offen offset:2048 sc1\n"
"v_cvt_pk_bf16_f32 v208, v144, v145\n"
"v_cvt_pk_bf16_f32 v209, v146, v147\n"
"v_cvt_pk_bf16_f32 v210, v160, v161\n"
"v_mfma_f32_16x16x32_bf16 v[124:127], v[136:139], v[132:135], v[124:127]\n"
"ds_read_b64_tr_b16 v[142:143], v255 offset:6400\n"
"v_cvt_pk_bf16_f32 v211, v162, v163\n"
"v_exp_f32_e32 v148, v148\n"
"v_exp_f32_e32 v149, v149\n"
"v_mfma_f32_16x16x32_bf16 v[168:171], v[128:131], v[132:135], v[168:171]\n"
"ds_read_b64_tr_b16 v[132:133], v255 offset:6144\n"
"v_exp_f32_e32 v150, v150\n"
"v_exp_f32_e32 v151, v151\n"
"v_mfma_f32_16x16x32_bf16 v[172:175], v[128:131], v[164:167], v[172:175]\n"
"buffer_load_dwordx4 v[84:87], v1, s[4:7], s97 offen offset:3072 sc1\n"
"v_exp_f32_e32 v152, v152\n"
"v_exp_f32_e32 v153, v153\n"
"v_mfma_f32_16x16x32_bf16 v[176:179], v[136:139], v[164:167], v[176:179]\n"
"ds_read_b64_tr_b16 v[134:135], v16 offset:6400\n"
"v_exp_f32_e32 v154, v154\n"
"v_exp_f32_e32 v155, v155\n"
"v_mfma_f32_16x16x32_bf16 v[180:183], v[88:91], v[164:167], v[180:183]\n"
"s_waitcnt lgkmcnt(4)\n"
"v_cvt_pk_bf16_f32 v128, v148, v149\n"
"v_cvt_pk_bf16_f32 v129, v150, v151\n"
"v_cvt_pk_bf16_f32 v130, v152, v153\n"
"v_cvt_pk_bf16_f32 v131, v154, v155\n"
"v_mfma_f32_16x16x32_bf16 v[136:139], v[188:191], v[92:95], v[116:119]\n"
"ds_read_b128 v[144:147], v15\n"
"v_exp_f32_e32 v184, v184\n"
"v_exp_f32_e32 v185, v185\n"
"v_exp_f32_e32 v186, v186\n"
"v_mfma_f32_16x16x32_bf16 v[148:151], v[188:191], v[96:99], v[120:123]\n"
"s_add_u32 s97, s56, 0x40000\n"
"buffer_load_dwordx4 v[152:155], v0, s[4:7], s97 offen sc1\n"
"v_exp_f32_e32 v187, v187\n"
"v_mfma_f32_16x16x32_bf16 v[160:163], v[200:203], v[96:99], v[120:123]\n"
"ds_read_b128 v[164:167], v254 offset:256\n"
"v_exp_f32_e32 v204, v204\n"
"v_exp_f32_e32 v205, v205\n"
"v_exp_f32_e32 v206, v206\n"
"v_mfma_f32_16x16x32_bf16 v[188:191], v[200:203], v[92:95], v[116:119]\n"
"buffer_load_dwordx4 v[200:203], v0, s[4:7], s97 offen offset:1024 sc1\n"
"ds_read_b64_tr_b16 v[212:213], v16 offset:4096\n"
"v_exp_f32_e32 v207, v207\n"
"s_waitcnt lgkmcnt(3)\n"
"v_mfma_f32_16x16x32_bf16 v[156:159], v[88:91], v[208:211], v[156:159]\n"
"v_cvt_pk_bf16_f32 v216, v184, v185\n"
"v_cvt_pk_bf16_f32 v217, v186, v187\n"
"v_cvt_pk_bf16_f32 v218, v204, v205\n"
"v_cvt_pk_bf16_f32 v219, v206, v207\n"
"v_mfma_f32_16x16x32_bf16 v[124:127], v[140:143], v[208:211], v[124:127]\n"
"buffer_load_dwordx4 v[184:187], v0, s[4:7], s97 offen offset:2048 sc1\n"
"ds_read_b64_tr_b16 v[214:215], v255 offset:4352\n"
"v_exp_f32_e32 v192, v192\n"
"v_exp_f32_e32 v193, v193\n"
"v_mfma_f32_16x16x32_bf16 v[168:171], v[132:135], v[208:211], v[168:171]\n"
"buffer_load_dwordx4 v[204:207], v0, s[4:7], s97 offen offset:3072 sc1\n"
"v_exp_f32_e32 v194, v194\n"
"v_exp_f32_e32 v195, v195\n"
"v_mfma_f32_16x16x32_bf16 v[172:175], v[132:135], v[128:131], v[172:175]\n"
"ds_read_b64_tr_b16 v[132:133], v255 offset:4096\n"
"v_exp_f32_e32 v196, v196\n"
"v_exp_f32_e32 v197, v197\n"
"v_mfma_f32_16x16x32_bf16 v[176:179], v[140:143], v[128:131], v[176:179]\n"
"buffer_load_dwordx4 v[140:143], v1, s[4:7], s97 offen sc1\n"
"ds_read_b64_tr_b16 v[134:135], v16 offset:4352\n"
"v_exp_f32_e32 v198, v198\n"
"v_exp_f32_e32 v199, v199\n"
"v_mfma_f32_16x16x32_bf16 v[180:183], v[88:91], v[128:131], v[180:183]\n"
"s_waitcnt lgkmcnt(4)\n"
"v_cvt_pk_bf16_f32 v128, v192, v193\n"
"v_cvt_pk_bf16_f32 v129, v194, v195\n"
"v_cvt_pk_bf16_f32 v130, v196, v197\n"
"v_cvt_pk_bf16_f32 v131, v198, v199\n"
"v_mfma_f32_16x16x32_bf16 v[192:195], v[144:147], v[92:95], v[116:119]\n"
"ds_read_b64_tr_b16 v[196:197], v16 offset:2048\n"
"ds_read_b64_tr_b16 v[198:199], v255 offset:2304\n"
"buffer_load_dwordx4 v[208:211], v1, s[4:7], s97 offen offset:1024 sc1\n"
"v_exp_f32_e32 v136, v136\n"
"v_exp_f32_e32 v137, v137\n"
"v_mfma_f32_16x16x32_bf16 v[220:223], v[144:147], v[96:99], v[120:123]\n"
"ds_read_b64_tr_b16 v[144:145], v255 offset:2048\n"
"v_exp_f32_e32 v138, v138\n"
"v_exp_f32_e32 v139, v139\n"
"v_mfma_f32_16x16x32_bf16 v[224:227], v[164:167], v[96:99], v[120:123]\n"
"buffer_load_dwordx4 v[120:123], v1, s[4:7], s97 offen offset:2048 sc1\n"
"v_exp_f32_e32 v188, v188\n"
"v_exp_f32_e32 v189, v189\n"
"v_mfma_f32_16x16x32_bf16 v[228:231], v[164:167], v[92:95], v[116:119]\n"
"ds_read_b64_tr_b16 v[146:147], v16 offset:2304\n"
"ds_read_b64_tr_b16 v[116:117], v16 offset:16384\n"
"v_exp_f32_e32 v190, v190\n"
"v_exp_f32_e32 v191, v191\n"
"s_waitcnt lgkmcnt(5)\n"
"v_mfma_f32_16x16x32_bf16 v[156:159], v[88:91], v[216:219], v[156:159]\n"
"v_cvt_pk_bf16_f32 v164, v136, v137\n"
"v_cvt_pk_bf16_f32 v165, v138, v139\n"
"v_cvt_pk_bf16_f32 v166, v188, v189\n"
"v_mfma_f32_16x16x32_bf16 v[124:127], v[212:215], v[216:219], v[124:127]\n"
"buffer_load_dwordx4 v[136:139], v1, s[4:7], s97 offen offset:3072 sc1\n"
"ds_read_b64_tr_b16 v[118:119], v255 offset:16640\n"
"ds_read_b64_tr_b16 v[188:189], v255 offset:16384\n"
"v_cvt_pk_bf16_f32 v167, v190, v191\n"
"v_exp_f32_e32 v148, v148\n"
"v_exp_f32_e32 v149, v149\n"
"v_exp_f32_e32 v150, v150\n"
"v_mfma_f32_16x16x32_bf16 v[168:171], v[132:135], v[216:219], v[168:171]\n"
"s_add_u32 s97, s57, 0x40000\n"
"buffer_load_dwordx4 v[216:219], v0, s[4:7], s97 offen sc1\n"
"ds_read_b64_tr_b16 v[190:191], v16 offset:16640\n"
"v_exp_f32_e32 v151, v151\n"
"v_mfma_f32_16x16x32_bf16 v[172:175], v[132:135], v[128:131], v[172:175]\n"
"v_exp_f32_e32 v160, v160\n"
"v_add_u32_e32 v248, 0x26400, v0\n"
"v_exp_f32_e32 v161, v161\n"
"v_mfma_f32_16x16x32_bf16 v[176:179], v[212:215], v[128:131], v[176:179]\n"
"buffer_load_dwordx4 v[132:135], v0, s[4:7], s97 offen offset:1024 sc1\n"
"ds_read_b128 v[212:215], v248\n"
"v_exp_f32_e32 v162, v162\n"
"v_exp_f32_e32 v163, v163\n"
"v_mfma_f32_16x16x32_bf16 v[180:183], v[88:91], v[128:131], v[180:183]\n"
"ds_read_b128 v[232:235], v248 offset:1024\n"
"v_cvt_pk_bf16_f32 v128, v148, v149\n"
"v_cvt_pk_bf16_f32 v129, v150, v151\n"
"v_cvt_pk_bf16_f32 v130, v160, v161\n"
"v_cvt_pk_bf16_f32 v131, v162, v163\n"
"s_waitcnt lgkmcnt(6)\n"
"v_mfma_f32_16x16x32_bf16 v[156:159], v[88:91], v[164:167], v[156:159]\n"
"ds_read_b64_tr_b16 v[148:149], v16\n"
"buffer_load_dwordx4 v[160:163], v0, s[4:7], s97 offen offset:2048 sc1\n"
"v_cndmask_b32_e64 v192, v100, v192, s[20:21]\n"
"v_cndmask_b32_e64 v193, v101, v193, s[22:23]\n"
"v_cndmask_b32_e64 v194, v102, v194, s[24:25]\n"
"v_cndmask_b32_e64 v195, v103, v195, s[26:27]\n"
"v_cndmask_b32_e64 v228, v112, v228, s[28:29]\n"
"v_cndmask_b32_e64 v229, v113, v229, s[30:31]\n"
"v_cndmask_b32_e64 v230, v114, v230, s[32:33]\n"
"v_cndmask_b32_e64 v231, v115, v231, s[34:35]\n"
"v_exp_f32_e32 v192, v192\n"
"v_mfma_f32_16x16x32_bf16 v[124:127], v[196:199], v[164:167], v[124:127]\n"
"ds_read_b64_tr_b16 v[150:151], v255 offset:256\n"
"buffer_load_dwordx4 v[100:103], v0, s[4:7], s97 offen offset:3072 sc1\n"
"v_exp_f32_e32 v193, v193\n"
"v_exp_f32_e32 v194, v194\n"
"v_exp_f32_e32 v195, v195\n"
"v_exp_f32_e32 v228, v228\n"
"v_mfma_f32_16x16x32_bf16 v[168:171], v[144:147], v[164:167], v[168:171]\n"
"buffer_load_dwordx4 v[164:167], v1, s[4:7], s97 offen sc1\n"
"v_exp_f32_e32 v229, v229\n"
"v_exp_f32_e32 v230, v230\n"
"v_exp_f32_e32 v231, v231\n"
"v_cvt_pk_bf16_f32 v112, v192, v193\n"
"v_cvt_pk_bf16_f32 v113, v194, v195\n"
"v_cvt_pk_bf16_f32 v114, v228, v229\n"
"v_mfma_f32_16x16x32_bf16 v[172:175], v[144:147], v[128:131], v[172:175]\n"
"ds_read_b64_tr_b16 v[144:145], v255\n"
"v_cvt_pk_bf16_f32 v115, v230, v231\n"
"v_cndmask_b32_e64 v220, v104, v220, s[36:37]\n"
"v_cndmask_b32_e64 v221, v105, v221, s[38:39]\n"
"v_cndmask_b32_e64 v222, v106, v222, s[40:41]\n"
"v_cndmask_b32_e64 v223, v107, v223, s[42:43]\n"
"v_cndmask_b32_e64 v224, v108, v224, s[44:45]\n"
"v_cndmask_b32_e64 v225, v109, v225, s[46:47]\n"
"v_cndmask_b32_e64 v226, v110, v226, s[48:49]\n"
"v_cndmask_b32_e64 v227, v111, v227, s[50:51]\n"
"v_mfma_f32_16x16x32_bf16 v[176:179], v[196:199], v[128:131], v[176:179]\n"
"buffer_load_dwordx4 v[104:107], v1, s[4:7], s97 offen offset:1024 sc1\n"
"ds_read_b64_tr_b16 v[146:147], v16 offset:256\n"
"buffer_load_dwordx4 v[108:111], v1, s[4:7], s97 offen offset:2048 sc1\n"
"v_exp_f32_e32 v220, v220\n"
"v_exp_f32_e32 v221, v221\n"
"v_exp_f32_e32 v222, v222\n"
"v_exp_f32_e32 v223, v223\n"
"v_exp_f32_e32 v224, v224\n"
"v_mfma_f32_16x16x32_bf16 v[180:183], v[88:91], v[128:131], v[180:183]\n"
"s_waitcnt lgkmcnt(0)\n"
"v_exp_f32_e32 v225, v225\n"
"v_exp_f32_e32 v226, v226\n"
"v_exp_f32_e32 v227, v227\n"
"v_cvt_pk_bf16_f32 v128, v220, v221\n"
"v_cvt_pk_bf16_f32 v129, v222, v223\n"
"v_cvt_pk_bf16_f32 v130, v224, v225\n"
"v_cvt_pk_bf16_f32 v131, v226, v227\n"
"v_mfma_f32_16x16x32_bf16 v[156:159], v[88:91], v[112:115], v[156:159]\n"
"v_and_b32_e32 v212, v112, v212\n"
"v_and_b32_e32 v213, v113, v213\n"
"v_and_b32_e32 v214, v114, v214\n"
"v_and_b32_e32 v215, v115, v215\n"
"v_mfma_f32_16x16x32_bf16 v[180:183], v[88:91], v[128:131], v[180:183]\n"
"v_xor_b32_e32 v192, v112, v212\n"
"v_xor_b32_e32 v193, v113, v213\n"
"v_xor_b32_e32 v194, v114, v214\n"
"v_xor_b32_e32 v195, v115, v215\n"
"v_mfma_f32_16x16x32_bf16 v[124:127], v[148:151], v[212:215], v[124:127]\n"
"v_and_b32_e32 v232, v128, v232\n"
"v_and_b32_e32 v233, v129, v233\n"
"v_mfma_f32_16x16x32_bf16 v[168:171], v[144:147], v[212:215], v[168:171]\n"
"v_and_b32_e32 v234, v130, v234\n"
"v_and_b32_e32 v235, v131, v235\n"
"v_mfma_f32_16x16x32_bf16 v[124:127], v[116:119], v[192:195], v[124:127]\n"
"v_xor_b32_e32 v112, v128, v232\n"
"v_xor_b32_e32 v113, v129, v233\n"
"v_mfma_f32_16x16x32_bf16 v[168:171], v[188:191], v[192:195], v[168:171]\n"
"v_xor_b32_e32 v114, v130, v234\n"
"v_xor_b32_e32 v115, v131, v235\n"
"v_mfma_f32_16x16x32_bf16 v[176:179], v[148:151], v[232:235], v[176:179]\n"
"buffer_load_dwordx4 v[128:131], v1, s[4:7], s97 offen offset:3072 sc1\n"
"s_add_u32 s97, s58, 0x40000\n"
"buffer_load_dwordx4 v[148:151], v0, s[4:7], s97 offen sc1\n"
"v_mfma_f32_16x16x32_bf16 v[172:175], v[144:147], v[232:235], v[172:175]\n"
"buffer_load_dwordx4 v[144:147], v0, s[4:7], s97 offen offset:1024 sc1\n"
"v_mfma_f32_16x16x32_bf16 v[176:179], v[116:119], v[112:115], v[176:179]\n"
"buffer_load_dwordx4 v[116:119], v0, s[4:7], s97 offen offset:2048 sc1\n"
"v_mfma_f32_16x16x32_bf16 v[172:175], v[188:191], v[112:115], v[172:175]\n"
"buffer_load_dwordx4 v[112:115], v0, s[4:7], s97 offen offset:3072 sc1\n"
"v_max_f32_e32 v248, v156, v180\n"
"v_rcp_f32_e32 v156, v156\n"
"v_rcp_f32_e32 v180, v180\n"
"s_nop 0\n"
"v_max3_f32 v248, v248, v156, v180\n"
"v_cmp_nlt_f32_e32 vcc, v248, v252\n"
"s_cbranch_vccnz .Lmk_b100\n"
".Lmk_k105:\n"
"v_mul_f32_e32 v124, v124, v156\n"
"v_mul_f32_e32 v125, v125, v156\n"
"v_mul_f32_e32 v126, v126, v156\n"
"v_mul_f32_e32 v127, v127, v156\n"
"v_mul_f32_e32 v168, v168, v156\n"
"v_mul_f32_e32 v169, v169, v156\n"
"v_mul_f32_e32 v170, v170, v156\n"
"v_mul_f32_e32 v171, v171, v156\n"
"v_cvt_pk_bf16_f32 v88, v124, v125\n"
"v_cvt_pk_bf16_f32 v89, v126, v127\n"
"v_cvt_pk_bf16_f32 v90, v168, v169\n"
"v_cvt_pk_bf16_f32 v91, v170, v171\n"
"buffer_store_dwordx4 v[88:91], v0, s[4:7], s60 offen sc1\n"
"s_add_u32 s98, s60, 0x8000\n"
"v_mul_f32_e32 v176, v176, v180\n"
"v_mul_f32_e32 v177, v177, v180\n"
"v_mul_f32_e32 v178, v178, v180\n"
"v_mul_f32_e32 v179, v179, v180\n"
"v_mul_f32_e32 v172, v172, v180\n"
"v_mul_f32_e32 v173, v173, v180\n"
"v_mul_f32_e32 v174, v174, v180\n"
"v_mul_f32_e32 v175, v175, v180\n"
"v_cvt_pk_bf16_f32 v88, v176, v177\n"
"v_cvt_pk_bf16_f32 v89, v178, v179\n"
"v_cvt_pk_bf16_f32 v90, v172, v173\n"
"v_cvt_pk_bf16_f32 v91, v174, v175\n"
"buffer_store_dwordx4 v[88:91], v0, s[4:7], s98 offen sc1\n"
";\n"
"s_waitcnt vmcnt(20)\n"
"; @A G2 b.0\n"
"s_nop 0\n"
"v_mfma_f32_16x16x32_bf16 v[88:91], a[128:131], v[152:155], 0\n"
"v_mfma_f32_16x16x32_bf16 v[88:91], a[132:135], v[200:203], v[88:91]\n"
"s_waitcnt vmcnt(18)\n"
"v_mfma_f32_16x16x32_bf16 v[88:91], a[136:139], v[184:187], v[88:91]\n"
"buffer_load_dwordx4 v[92:95], v1, s[4:7], s97 offen sc1\n"
"v_mfma_f32_16x16x32_bf16 v[88:91], a[140:143], v[204:207], v[88:91]\n"
"s_waitcnt vmcnt(17)\n"
"v_mfma_f32_16x16x32_bf16 v[88:91], a[144:147], v[140:143], v[88:91]\n"
"v_mfma_f32_16x16x32_bf16 v[88:91], a[148:151], v[208:211], v[88:91]\n"
"s_waitcnt vmcnt(15)\n"
"v_mfma_f32_16x16x32_bf16 v[88:91], a[152:155], v[120:123], v[88:91]\n"
"buffer_load_dwordx4 v[96:99], v1, s[4:7], s97 offen offset:1024 sc1\n"
"v_mfma_f32_16x16x32_bf16 v[88:91], a[156:159], v[136:139], v[88:91]\n"
"v_mfma_f32_16x16x32_bf16 v[124:127], a[188:191], v[136:139], 0\n"
"v_mfma_f32_16x16x32_bf16 v[124:127], a[184:187], v[120:123], v[124:127]\n"
"v_mfma_f32_16x16x32_bf16 v[124:127], a[180:183], v[208:211], v[124:127]\n"
"buffer_load_dwordx4 v[156:159], v1, s[4:7], s97 offen offset:2048 sc1\n"
"v_mfma_f32_16x16x32_bf16 v[124:127], a[176:179], v[140:143], v[124:127]\n"
"v_mfma_f32_16x16x32_bf16 v[124:127], a[172:175], v[204:207], v[124:127]\n"
"s_nop 1\n"
"v_cmp_u_f32_e32 vcc, v88, v88\n"
"v_mfma_f32_16x16x32_bf16 v[124:127], a[168:171], v[184:187], v[124:127]\n"
"s_cbranch_vccnz .Lmk_p106s\n"
".Lmk_p106r:\n"
"v_mfma_f32_16x16x32_bf16 v[124:127], a[164:167], v[200:203], v[124:127]\n"
"ds_write_b128 v3, v[88:91]\n"
"v_mfma_f32_16x16x32_bf16 v[124:127], a[160:163], v[152:155], v[124:127]\n"
"buffer_load_dwordx4 v[88:91], v1, s[4:7], s97 offen offset:3072 sc1\n"
"v_mfma_f32_16x16x32_bf16 v[168:171], a[192:195], v[152:155], 0\n"
"v_mfma_f32_16x16x32_bf16 v[168:171], a[196:199], v[200:203], v[168:171]\n"
"s_add_u32 s98, s59, 0x40000\n"
"v_mfma_f32_16x16x32_bf16 v[168:171], a[200:203], v[184:187], v[168:171]\n"
"v_mfma_f32_16x16x32_bf16 v[168:171], a[204:207], v[204:207], v[168:171]\n"
"buffer_load_dwordx4 v[172:175], v0, s[4:7], s98 offen sc1\n"
"v_mfma_f32_16x16x32_bf16 v[168:171], a[208:211], v[140:143], v[168:171]\n"
"v_mfma_f32_16x16x32_bf16 v[168:171], a[212:215], v[208:211], v[168:171]\n"
"ds_write_b128 v3, v[124:127] offset:1024\n"
"v_mfma_f32_16x16x32_bf16 v[168:171], a[216:219], v[120:123], v[168:171]\n"
"v_mfma_f32_16x16x32_bf16 v[168:171], a[220:223], v[136:139], v[168:171]\n"
"buffer_load_dwordx4 v[124:127], v0, s[4:7], s98 offen offset:1024 sc1\n"
"v_mfma_f32_16x16x32_bf16 v[176:179], a[252:255], v[136:139], 0\n"
"v_mfma_f32_16x16x32_bf16 v[176:179], a[248:251], v[120:123], v[176:179]\n"
"v_mfma_f32_16x16x32_bf16 v[176:179], a[244:247], v[208:211], v[176:179]\n"
"v_mfma_f32_16x16x32_bf16 v[176:179], a[240:243], v[140:143], v[176:179]\n"
"buffer_load_dwordx4 v[120:123], v0, s[4:7], s98 offen offset:2048 sc1\n"
"v_mfma_f32_16x16x32_bf16 v[176:179], a[236:239], v[204:207], v[176:179]\n"
"v_mfma_f32_16x16x32_bf16 v[176:179], a[232:235], v[184:187], v[176:179]\n"
"ds_write_b128 v3, v[168:171] offset:2048\n"
"v_mfma_f32_16x16x32_bf16 v[176:179], a[228:231], v[200:203], v[176:179]\n"
"v_mfma_f32_16x16x32_bf16 v[176:179], a[224:227], v[152:155], v[176:179]\n"
"s_waitcnt vmcnt(17)\n"
"buffer_load_dwordx4 v[136:139], v0, s[4:7], s98 offen offset:3072 sc1\n"
"; @A G2 b.1\n"
"v_mfma_f32_16x16x32_bf16 v[140:143], a[128:131], v[216:219], 0\n"
"v_mfma_f32_16x16x32_bf16 v[140:143], a[132:135], v[132:135], v[140:143]\n"
"v_mfma_f32_16x16x32_bf16 v[140:143], a[136:139], v[160:163], v[140:143]\n"
"v_mfma_f32_16x16x32_bf16 v[140:143], a[140:143], v[100:103], v[140:143]\n"
"s_waitcnt vmcnt(14)\n"
"buffer_load_dwordx4 v[152:155], v1, s[4:7], s98 offen sc1\n"
"v_mfma_f32_16x16x32_bf16 v[140:143], a[144:147], v[164:167], v[140:143]\n"
"v_mfma_f32_16x16x32_bf16 v[140:143], a[148:151], v[104:107], v[140:143]\n"
"ds_write_b128 v3, v[176:179] offset:3072\n"
"v_mfma_f32_16x16x32_bf16 v[140:143], a[152:155], v[108:111], v[140:143]\n"
"v_mfma_f32_16x16x32_bf16 v[140:143], a[156:159], v[128:131], v[140:143]\n"
"buffer_load_dwordx4 v[168:171], v1, s[4:7], s98 offen offset:1024 sc1\n"
"v_mfma_f32_16x16x32_bf16 v[176:179], a[188:191], v[128:131], 0\n"
"v_mfma_f32_16x16x32_bf16 v[176:179], a[184:187], v[108:111], v[176:179]\n"
"v_mfma_f32_16x16x32_bf16 v[176:179], a[180:183], v[104:107], v[176:179]\n"
"v_mfma_f32_16x16x32_bf16 v[176:179], a[176:179], v[164:167], v[176:179]\n"
"buffer_load_dwordx4 v[180:183], v1, s[4:7], s98 offen offset:2048 sc1\n"
"v_mfma_f32_16x16x32_bf16 v[176:179], a[172:175], v[100:103], v[176:179]\n"
"s_nop 0\n"
"v_cmp_u_f32_e32 vcc, v140, v140\n"
"v_mfma_f32_16x16x32_bf16 v[176:179], a[168:171], v[160:163], v[176:179]\n"
"s_cbranch_vccnz .Lmk_p107s\n"
".Lmk_p107r:\n"
"v_mfma_f32_16x16x32_bf16 v[176:179], a[164:167], v[132:135], v[176:179]\n"
"ds_write_b128 v3, v[140:143] offset:6144\n"
"v_mfma_f32_16x16x32_bf16 v[176:179], a[160:163], v[216:219], v[176:179]\n"
"buffer_load_dwordx4 v[140:143], v1, s[4:7], s98 offen offset:3072 sc1\n"
"v_mfma_f32_16x16x32_bf16 v[184:187], a[192:195], v[216:219], 0\n"
"v_mfma_f32_16x16x32_bf16 v[184:187], a[196:199], v[132:135], v[184:187]\n"
"v_mfma_f32_16x16x32_bf16 v[184:187], a[200:203], v[160:163], v[184:187]\n"
"v_mfma_f32_16x16x32_bf16 v[184:187], a[204:207], v[100:103], v[184:187]\n"
"v_mfma_f32_16x16x32_bf16 v[184:187], a[208:211], v[164:167], v[184:187]\n"
"ds_read_b128 v[188:191], v2 offset:8192\n"
"ds_read_b128 v[192:195], v2 offset:9216\n"
"v_mfma_f32_16x16x32_bf16 v[184:187], a[212:215], v[104:107], v[184:187]\n"
"ds_write_b128 v3, v[176:179] offset:7168\n"
"v_mfma_f32_16x16x32_bf16 v[184:187], a[216:219], v[108:111], v[184:187]\n"
"v_mfma_f32_16x16x32_bf16 v[184:187], a[220:223], v[128:131], v[184:187]\n"
"v_mfma_f32_16x16x32_bf16 v[176:179], a[252:255], v[128:131], 0\n"
"v_mfma_f32_16x16x32_bf16 v[176:179], a[248:251], v[108:111], v[176:179]\n"
"s_waitcnt lgkmcnt(1)\n"
"v_lshlrev_b32_e32 v255, 16, v188\n"
"v_and_b32_e32 v254, 0xffff0000, v188\n"
"v_mfma_f32_16x16x32_bf16 v[176:179], a[244:247], v[104:107], v[176:179]\n"
"v_lshlrev_b32_e32 v253, 16, v189\n"
"v_and_b32_e32 v252, 0xffff0000, v189\n"
"v_mfma_f32_16x16x32_bf16 v[176:179], a[240:243], v[164:167], v[176:179]\n"
"v_lshlrev_b32_e32 v251, 16, v190\n"
"v_and_b32_e32 v250, 0xffff0000, v190\n"
"v_mfma_f32_16x16x32_bf16 v[176:179], a[236:239], v[100:103], v[176:179]\n"
"v_lshlrev_b32_e32 v249, 16, v191\n"
"v_and_b32_e32 v248, 0xffff0000, v191\n"
"v_mfma_f32_16x16x32_bf16 v[176:179], a[232:235], v[160:163], v[176:179]\n"
"ds_write_b128 v3, v[184:187] offset:8192\n"
"v_mfma_f32_16x16x32_bf16 v[176:179], a[228:231], v[132:135], v[176:179]\n"
"v_lshlrev_b32_e32 v247, 16, v192\n"
"v_and_b32_e32 v246, 0xffff0000, v192\n"
"v_mfma_f32_16x16x32_bf16 v[176:179], a[224:227], v[216:219], v[176:179]\n"
"s_waitcnt vmcnt(14)\n"
"v_lshlrev_b32_e32 v245, 16, v193\n"
"v_and_b32_e32 v244, 0xffff0000, v193\n"
"; @A G2 b.2\n"
"v_mfma_f32_16x16x32_bf16 v[100:103], a[128:131], v[148:151], 0\n"
"v_lshlrev_b32_e32 v243, 16, v194\n"
"v_and_b32_e32 v242, 0xffff0000, v194\n"
"v_mfma_f32_16x16x32_bf16 v[100:103], a[132:135], v[144:147], v[100:103]\n"
"v_lshlrev_b32_e32 v241, 16, v195\n"
"v_and_b32_e32 v240, 0xffff0000, v195\n"
"v_mfma_f32_16x16x32_bf16 v[100:103], a[136:139], v[116:119], v[100:103]\n"
"v_mfma_f32_16x16x32_bf16 v[100:103], a[140:143], v[112:115], v[100:103]\n"
"s_waitcnt vmcnt(8)\n"
"v_mfma_f32_16x16x32_bf16 v[100:103], a[144:147], v[92:95], v[100:103]\n"
"v_mfma_f32_16x16x32_bf16 v[100:103], a[148:151], v[96:99], v[100:103]\n"
"ds_write_b128 v3, v[176:179] offset:9216\n"
"v_mfma_f32_16x16x32_bf16 v[100:103], a[152:155], v[156:159], v[100:103]\n"
"v_mfma_f32_16x16x32_bf16 v[100:103], a[156:159], v[88:91], v[100:103]\n"
"v_mfma_f32_16x16x32_bf16 v[104:107], a[188:191], v[88:91], 0\n"
"v_mfma_f32_16x16x32_bf16 v[104:107], a[184:187], v[156:159], v[104:107]\n"
"s_waitcnt lgkmcnt(0)\n"
"s_barrier\n"
"v_mfma_f32_16x16x32_bf16 v[104:107], a[180:183], v[96:99], v[104:107]\n"
"ds_read_b128 v[108:111], v4\n"
"ds_read_b128 v[128:131], v5\n"
"v_mfma_f32_16x16x32_bf16 v[104:107], a[176:179], v[92:95], v[104:107]\n"
"v_mfma_f32_16x16x32_bf16 v[104:107], a[172:175], v[112:115], v[104:107]\n"
"s_add_u32 s98, s56, 0\n"
"v_cmp_u_f32_e32 vcc, v100, v100\n"
"v_mfma_f32_16x16x32_bf16 v[104:107], a[168:171], v[116:119], v[104:107]\n"
"s_cbranch_vccnz .Lmk_p108s\n"
".Lmk_p108r:\n"
"v_mfma_f32_16x16x32_bf16 v[104:107], a[164:167], v[144:147], v[104:107]\n"
"ds_write_b128 v3, v[100:103] offset:12288\n"
"v_mfma_f32_16x16x32_bf16 v[104:107], a[160:163], v[148:151], v[104:107]\n"
"buffer_load_dwordx4 v[100:103], v0, s[4:7], s98 offen sc1\n"
"v_mfma_f32_16x16x32_bf16 v[132:135], a[192:195], v[148:151], 0\n"
"ds_read_b128 v[160:163], v4 offset:1024\n"
"ds_read_b128 v[164:167], v5 offset:1024\n"
"v_mfma_f32_16x16x32_bf16 v[132:135], a[196:199], v[144:147], v[132:135]\n"
"s_waitcnt lgkmcnt(3)\n"
"v_add_f32_e32 v108, v108, v255\n"
"v_add_f32_e32 v109, v109, v254\n"
"v_mfma_f32_16x16x32_bf16 v[132:135], a[200:203], v[116:119], v[132:135]\n"
"buffer_load_dwordx4 v[176:179], v0, s[4:7], s98 offen offset:1024 sc1\n"
"v_mfma_f32_16x16x32_bf16 v[132:135], a[204:207], v[112:115], v[132:135]\n"
"v_add_f32_e32 v110, v110, v253\n"
"v_add_f32_e32 v111, v111, v252\n"
"v_mfma_f32_16x16x32_bf16 v[132:135], a[208:211], v[92:95], v[132:135]\n"
"v_add_f32_e32 v108, v108, v128\n"
"v_add_f32_e32 v109, v109, v129\n"
"v_mfma_f32_16x16x32_bf16 v[132:135], a[212:215], v[96:99], v[132:135]\n"
"ds_write_b128 v3, v[104:107] offset:13312\n"
"v_mfma_f32_16x16x32_bf16 v[132:135], a[216:219], v[156:159], v[132:135]\n"
"buffer_load_dwordx4 v[104:107], v0, s[4:7], s98 offen offset:2048 sc1\n"
"v_mfma_f32_16x16x32_bf16 v[132:135], a[220:223], v[88:91], v[132:135]\n"
"ds_read_b128 v[184:187], v4 offset:2048\n"
"ds_read_b128 v[188:191], v5 offset:2048\n"
"v_mfma_f32_16x16x32_bf16 v[192:195], a[252:255], v[88:91], 0\n"
"s_waitcnt lgkmcnt(3)\n"
"v_add_f32_e32 v110, v110, v130\n"
"v_add_f32_e32 v111, v111, v131\n"
"v_mfma_f32_16x16x32_bf16 v[192:195], a[248:251], v[156:159], v[192:195]\n"
"buffer_load_dwordx4 v[88:91], v0, s[4:7], s98 offen offset:3072 sc1\n"
"v_mfma_f32_16x16x32_bf16 v[192:195], a[244:247], v[96:99], v[192:195]\n"
"v_add_f32_e32 v160, v160, v251\n"
"v_add_f32_e32 v161, v161, v250\n"
"v_mfma_f32_16x16x32_bf16 v[192:195], a[240:243], v[92:95], v[192:195]\n"
"v_add_f32_e32 v162, v162, v249\n"
"v_add_f32_e32 v163, v163, v248\n"
"v_mfma_f32_16x16x32_bf16 v[192:195], a[236:239], v[112:115], v[192:195]\n"
"buffer_load_dwordx4 v[92:95], v1, s[4:7], s98 offen sc1\n"
"v_mfma_f32_16x16x32_bf16 v[192:195], a[232:235], v[116:119], v[192:195]\n"
"ds_write_b128 v3, v[132:135] offset:14336\n"
"v_mfma_f32_16x16x32_bf16 v[192:195], a[228:231], v[144:147], v[192:195]\n"
"ds_read_b128 v[96:99], v4 offset:3072\n"
"ds_read_b128 v[112:115], v5 offset:3072\n"
"v_mfma_f32_16x16x32_bf16 v[192:195], a[224:227], v[148:151], v[192:195]\n"
"s_waitcnt vmcnt(9)\n"
"buffer_load_dwordx4 v[116:119], v1, s[4:7], s98 offen offset:1024 sc1\n"
"; @A G2 b.3\n"
"v_mfma_f32_16x16x32_bf16 v[128:131], a[128:131], v[172:175], v[108:111]\n"
"v_add_f32_e32 v160, v160, v164\n"
"v_add_f32_e32 v161, v161, v165\n"
"v_mfma_f32_16x16x32_bf16 v[128:131], a[132:135], v[124:127], v[128:131]\n"
"s_waitcnt lgkmcnt(3)\n"
"v_add_f32_e32 v162, v162, v166\n"
"v_add_f32_e32 v163, v163, v167\n"
"v_mfma_f32_16x16x32_bf16 v[128:131], a[136:139], v[120:123], v[128:131]\n"
"buffer_load_dwordx4 v[132:135], v1, s[4:7], s98 offen offset:2048 sc1\n"
"v_mfma_f32_16x16x32_bf16 v[128:131], a[140:143], v[136:139], v[128:131]\n"
"s_waitcnt vmcnt(7)\n"
"v_add_f32_e32 v184, v184, v247\n"
"v_add_f32_e32 v185, v185, v246\n"
"v_mfma_f32_16x16x32_bf16 v[128:131], a[144:147], v[152:155], v[128:131]\n"
"v_add_f32_e32 v186, v186, v245\n"
"v_add_f32_e32 v187, v187, v244\n"
"v_mfma_f32_16x16x32_bf16 v[128:131], a[148:151], v[168:171], v[128:131]\n"
"ds_write_b128 v3, v[192:195] offset:15360\n"
"v_mfma_f32_16x16x32_bf16 v[128:131], a[152:155], v[180:183], v[128:131]\n"
"buffer_load_dwordx4 v[144:147], v1, s[4:7], s98 offen offset:3072 sc1\n"
"v_mfma_f32_16x16x32_bf16 v[128:131], a[156:159], v[140:143], v[128:131]\n"
"s_add_u32 s98, s57, 0\n"
"v_add_f32_e32 v184, v184, v188\n"
"v_add_f32_e32 v185, v185, v189\n"
"v_mfma_f32_16x16x32_bf16 v[148:151], a[188:191], v[140:143], v[160:163]\n"
"s_waitcnt lgkmcnt(1)\n"
"v_add_f32_e32 v186, v186, v190\n"
"v_add_f32_e32 v187, v187, v191\n"
"v_mfma_f32_16x16x32_bf16 v[148:151], a[184:187], v[180:183], v[148:151]\n"
"s_waitcnt lgkmcnt(0)\n"
"s_barrier\n"
"v_mfma_f32_16x16x32_bf16 v[148:151], a[180:183], v[168:171], v[148:151]\n"
"buffer_load_dwordx4 v[156:159], v0, s[4:7], s98 offen sc1\n"
"v_mfma_f32_16x16x32_bf16 v[148:151], a[176:179], v[152:155], v[148:151]\n"
"v_add_f32_e32 v96, v96, v243\n"
"v_add_f32_e32 v97, v97, v242\n"
"v_mfma_f32_16x16x32_bf16 v[148:151], a[172:175], v[136:139], v[148:151]\n"
"v_cmp_u_f32_e32 vcc, v128, v128\n"
"v_add_f32_e32 v98, v98, v241\n"
"v_mfma_f32_16x16x32_bf16 v[148:151], a[168:171], v[120:123], v[148:151]\n"
"s_cbranch_vccnz .Lmk_p109s\n"
".Lmk_p109r:\n"
"v_add_f32_e32 v99, v99, v240\n"
"v_mfma_f32_16x16x32_bf16 v[148:151], a[164:167], v[124:127], v[148:151]\n"
"buffer_load_dwordx4 v[108:111], v0, s[4:7], s98 offen offset:1024 sc1\n"
"v_mfma_f32_16x16x32_bf16 v[148:151], a[160:163], v[172:175], v[148:151]\n"
"v_add_f32_e32 v96, v96, v112\n"
"v_add_f32_e32 v97, v97, v113\n"
"v_mfma_f32_16x16x32_bf16 v[184:187], a[192:195], v[172:175], v[184:187]\n"
"v_add_f32_e32 v98, v98, v114\n"
"v_add_f32_e32 v99, v99, v115\n"
"v_mfma_f32_16x16x32_bf16 v[184:187], a[196:199], v[124:127], v[184:187]\n"
"buffer_load_dwordx4 v[112:115], v0, s[4:7], s98 offen offset:2048 sc1\n"
"v_mfma_f32_16x16x32_bf16 v[184:187], a[200:203], v[120:123], v[184:187]\n"
"ds_read_b128 v[160:163], v6\n"
"ds_read_b128 v[164:167], v6 offset:1024\n"
"v_mfma_f32_16x16x32_bf16 v[184:187], a[204:207], v[136:139], v[184:187]\n"
"ds_read_b128 v[188:191], v6 offset:2048\n"
"ds_read_b128 v[192:195], v6 offset:3072\n"
"v_mfma_f32_16x16x32_bf16 v[184:187], a[208:211], v[152:155], v[184:187]\n"
"buffer_load_dwordx4 v[196:199], v0, s[4:7], s98 offen offset:3072 sc1\n"
"v_mfma_f32_16x16x32_bf16 v[184:187], a[212:215], v[168:171], v[184:187]\n"
"v_mfma_f32_16x16x32_bf16 v[184:187], a[216:219], v[180:183], v[184:187]\n"
"v_mfma_f32_16x16x32_bf16 v[184:187], a[220:223], v[140:143], v[184:187]\n"
"buffer_load_dwordx4 v[200:203], v1, s[4:7], s98 offen sc1\n"
"v_mfma_f32_16x16x32_bf16 v[96:99], a[252:255], v[140:143], v[96:99]\n"
"s_waitcnt lgkmcnt(0)\n"
"v_add_f32_e32 v160, v128, v160\n"
"v_add_f32_e32 v161, v129, v161\n"
"v_mfma_f32_16x16x32_bf16 v[96:99], a[248:251], v[180:183], v[96:99]\n"
"v_add_f32_e32 v162, v130, v162\n"
"v_add_f32_e32 v163, v131, v163\n"
"v_mfma_f32_16x16x32_bf16 v[96:99], a[244:247], v[168:171], v[96:99]\n"
"buffer_load_dwordx4 v[128:131], v1, s[4:7], s98 offen offset:1024 sc1\n"
"v_mfma_f32_16x16x32_bf16 v[96:99], a[240:243], v[152:155], v[96:99]\n"
"v_add_f32_e32 v164, v148, v164\n"
"v_add_f32_e32 v165, v149, v165\n"
"v_mfma_f32_16x16x32_bf16 v[96:99], a[236:239], v[136:139], v[96:99]\n"
"v_add_f32_e32 v166, v150, v166\n"
"v_add_f32_e32 v167, v151, v167\n"
"v_mfma_f32_16x16x32_bf16 v[96:99], a[232:235], v[120:123], v[96:99]\n"
"buffer_load_dwordx4 v[120:123], v1, s[4:7], s98 offen offset:2048 sc1\n"
"v_mfma_f32_16x16x32_bf16 v[96:99], a[228:231], v[124:127], v[96:99]\n"
"v_cvt_pk_bf16_f32 v124, v160, v161\n"
"v_cvt_pk_bf16_f32 v125, v162, v163\n"
"v_mfma_f32_16x16x32_bf16 v[96:99], a[224:227], v[172:175], v[96:99]\n"
"s_add_u32 s97, s61, 0x440000\n"
"v_cvt_pk_bf16_f32 v126, v164, v165\n"
"v_cvt_pk_bf16_f32 v127, v166, v167\n"
"buffer_store_dwordx4 v[124:127], v0, s[4:7], s97 offen sc1\n"
"ds_write_b128 v2, v[124:127] offset:8192\n"
"buffer_load_dwordx4 v[136:139], v1, s[4:7], s98 offen offset:3072 sc1\n"
"; @A fin b\n"
"s_add_u32 s98, s61, 0x440000\n"
"v_add_f32_e32 v188, v184, v188\n"
"v_add_f32_e32 v189, v185, v189\n"
"v_add_f32_e32 v190, v186, v190\n"
"v_add_f32_e32 v191, v187, v191\n"
"v_add_f32_e32 v192, v96, v192\n"
"v_add_f32_e32 v193, v97, v193\n"
"v_add_f32_e32 v194, v98, v194\n"
"v_add_f32_e32 v195, v99, v195\n"
"v_cvt_pk_bf16_f32 v96, v188, v189\n"
"v_cvt_pk_bf16_f32 v97, v190, v191\n"
"v_cvt_pk_bf16_f32 v98, v192, v193\n"
"v_cvt_pk_bf16_f32 v99, v194, v195\n"
"buffer_store_dwordx4 v[96:99], v0, s[4:7], s98 offen offset:1024 sc1\n"
"s_waitcnt vmcnt(16)\n"
"; @A G2 a.0\n"
"v_mfma_f32_16x16x32_bf16 v[140:143], a[128:131], v[100:103], 0\n"
"s_add_u32 s98, s58, 0\n"
"buffer_load_dwordx4 v[148:151], v0, s[4:7], s98 offen sc1\n"
"v_mfma_f32_16x16x32_bf16 v[140:143], a[132:135], v[176:179], v[140:143]\n"
"s_waitcnt vmcnt(15)\n"
"ds_write_b128 v2, v[96:99] offset:9216\n"
"v_mfma_f32_16x16x32_bf16 v[140:143], a[136:139], v[104:107], v[140:143]\n"
"v_mov_b32_e32 v152, 0x3f803f80\n"
"v_mov_b32_e32 v153, 0x3f803f80\n"
"v_mfma_f32_16x16x32_bf16 v[140:143], a[140:143], v[88:91], v[140:143]\n"
"s_waitcnt vmcnt(13)\n"
"v_mov_b32_e32 v154, 0x3f803f80\n"
"v_mov_b32_e32 v155, 0x3f803f80\n"
"v_mfma_f32_16x16x32_bf16 v[140:143], a[144:147], v[92:95], v[140:143]\n"
"buffer_load_dwordx4 v[160:163], v0, s[4:7], s98 offen offset:1024 sc1\n"
"v_mfma_f32_16x16x32_bf16 v[164:167], v[152:155], v[124:127], 0\n"
"v_mfma_f32_16x16x32_bf16 v[164:167], v[152:155], v[96:99], v[164:167]\n"
"v_mfma_f32_16x16x32_bf16 v[152:155], v[124:127], v[124:127], 0\n"
"v_mfma_f32_16x16x32_bf16 v[152:155], v[96:99], v[96:99], v[152:155]\n"
"v_mfma_f32_16x16x32_bf16 v[140:143], a[148:151], v[116:119], v[140:143]\n"
"s_waitcnt vmcnt(12)\n"
"v_mfma_f32_16x16x32_bf16 v[140:143], a[152:155], v[132:135], v[140:143]\n"
"v_mfma_f32_16x16x32_bf16 v[140:143], a[156:159], v[144:147], v[140:143]\n"
"v_mfma_f32_16x16x32_bf16 v[96:99], a[188:191], v[144:147], 0\n"
"buffer_load_dwordx4 v[124:127], v0, s[4:7], s98 offen offset:2048 sc1\n"
"v_mfma_f32_16x16x32_bf16 v[96:99], a[184:187], v[132:135], v[96:99]\n"
"s_mov_b32 s96, 0xaaaaaaaa\n"
"s_mov_b32 s97, 0xaaaaaaaa\n"
"v_mfma_f32_16x16x32_bf16 v[96:99], a[180:183], v[116:119], v[96:99]\n"
"v_mfma_f32_16x16x32_bf16 v[96:99], a[176:179], v[92:95], v[96:99]\n"
"v_mfma_f32_16x16x32_bf16 v[96:99], a[172:175], v[88:91], v[96:99]\n"
"v_cmp_u_f32_e32 vcc, v140, v140\n"
"v_cndmask_b32_e64 v152, v152, v153, s[96:97]\n"
"v_mfma_f32_16x16x32_bf16 v[96:99], a[168:171], v[104:107], v[96:99]\n"
"s_cbranch_vccnz .Lmk_p110s\n"
".Lmk_p110r:\n"
"v_cndmask_b32_e64 v154, v154, v155, s[96:97]\n"
"v_mfma_f32_16x16x32_bf16 v[96:99], a[164:167], v[176:179], v[96:99]\n"
"ds_write_b128 v3, v[140:143]\n"
"v_mfma_f32_16x16x32_bf16 v[96:99], a[160:163], v[100:103], v[96:99]\n"
"buffer_load_dwordx4 v[140:143], v0, s[4:7], s98 offen offset:3072 sc1\n"
"v_mfma_f32_16x16x32_bf16 v[168:171], a[192:195], v[100:103], 0\n"
"s_mov_b32 s96, 0xcccccccc\n"
"s_mov_b32 s97, 0xcccccccc\n"
"v_mfma_f32_16x16x32_bf16 v[168:171], a[196:199], v[176:179], v[168:171]\n"
"v_cndmask_b32_e64 v165, v152, v154, s[96:97]\n"
"v_mfma_f32_16x16x32_bf16 v[168:171], a[200:203], v[104:107], v[168:171]\n"
"s_mov_b32 s96, 0xf0000f\n"
"s_mov_b32 s97, 0xf0000f00\n"
"v_mfma_f32_16x16x32_bf16 v[168:171], a[204:207], v[88:91], v[168:171]\n"
"buffer_load_dwordx4 v[152:155], v1, s[4:7], s98 offen sc1\n"
"v_mfma_f32_16x16x32_bf16 v[168:171], a[208:211], v[92:95], v[168:171]\n"
"v_cndmask_b32_e64 v164, v164, v165, s[96:97]\n"
"s_add_u32 s97, s63, 0x44000\n"
"v_mfma_f32_16x16x32_bf16 v[168:171], a[212:215], v[116:119], v[168:171]\n"
"ds_write_b128 v3, v[96:99] offset:1024\n"
"v_mfma_f32_16x16x32_bf16 v[168:171], a[216:219], v[132:135], v[168:171]\n"
"buffer_store_dword v164, v14, s[4:7], s97 offen sc1\n"
"v_mfma_f32_16x16x32_bf16 v[168:171], a[220:223], v[144:147], v[168:171]\n"
"buffer_load_dwordx4 v[164:167], v1, s[4:7], s98 offen offset:1024 sc1\n"
"v_mfma_f32_16x16x32_bf16 v[96:99], a[252:255], v[144:147], 0\n"
"v_mfma_f32_16x16x32_bf16 v[96:99], a[248:251], v[132:135], v[96:99]\n"
"v_mfma_f32_16x16x32_bf16 v[96:99], a[244:247], v[116:119], v[96:99]\n"
"v_mfma_f32_16x16x32_bf16 v[96:99], a[240:243], v[92:95], v[96:99]\n"
"buffer_load_dwordx4 v[116:119], v1, s[4:7], s98 offen offset:2048 sc1\n"
"v_mfma_f32_16x16x32_bf16 v[96:99], a[236:239], v[88:91], v[96:99]\n"
"v_mfma_f32_16x16x32_bf16 v[96:99], a[232:235], v[104:107], v[96:99]\n"
"ds_write_b128 v3, v[168:171] offset:2048\n"
"v_mfma_f32_16x16x32_bf16 v[96:99], a[228:231], v[176:179], v[96:99]\n"
"v_mfma_f32_16x16x32_bf16 v[96:99], a[224:227], v[100:103], v[96:99]\n"
"s_waitcnt vmcnt(14)\n"
"buffer_load_dwordx4 v[132:135], v1, s[4:7], s98 offen offset:3072 sc1\n"
"; @A G2 a.1\n"
"v_mfma_f32_16x16x32_bf16 v[88:91], a[128:131], v[156:159], 0\n"
"v_mfma_f32_16x16x32_bf16 v[88:91], a[132:135], v[108:111], v[88:91]\n"
"s_add_u32 s98, s59, 0\n"
"v_mfma_f32_16x16x32_bf16 v[88:91], a[136:139], v[112:115], v[88:91]\n"
"v_mfma_f32_16x16x32_bf16 v[88:91], a[140:143], v[196:199], v[88:91]\n"
"s_waitcnt vmcnt(10)\n"
"buffer_load_dwordx4 v[172:175], v0, s[4:7], s98 offen sc1\n"
"v_mfma_f32_16x16x32_bf16 v[88:91], a[144:147], v[200:203], v[88:91]\n"
"v_mfma_f32_16x16x32_bf16 v[88:91], a[148:151], v[128:131], v[88:91]\n"
"ds_write_b128 v3, v[96:99] offset:3072\n"
"v_mfma_f32_16x16x32_bf16 v[88:91], a[152:155], v[120:123], v[88:91]\n"
"v_mfma_f32_16x16x32_bf16 v[88:91], a[156:159], v[136:139], v[88:91]\n"
"buffer_load_dwordx4 v[176:179], v0, s[4:7], s98 offen offset:1024 sc1\n"
"v_mfma_f32_16x16x32_bf16 v[92:95], a[188:191], v[136:139], 0\n"
"v_mfma_f32_16x16x32_bf16 v[92:95], a[184:187], v[120:123], v[92:95]\n"
"v_mfma_f32_16x16x32_bf16 v[92:95], a[180:183], v[128:131], v[92:95]\n"
"v_mfma_f32_16x16x32_bf16 v[92:95], a[176:179], v[200:203], v[92:95]\n"
"buffer_load_dwordx4 v[168:171], v0, s[4:7], s98 offen offset:2048 sc1\n"
"v_mfma_f32_16x16x32_bf16 v[92:95], a[172:175], v[196:199], v[92:95]\n"
"s_nop 0\n"
"v_cmp_u_f32_e32 vcc, v88, v88\n"
"v_mfma_f32_16x16x32_bf16 v[92:95], a[168:171], v[112:115], v[92:95]\n"
"s_cbranch_vccnz .Lmk_p111s\n"
".Lmk_p111r:\n"
"v_mfma_f32_16x16x32_bf16 v[92:95], a[164:167], v[108:111], v[92:95]\n"
"ds_write_b128 v3, v[88:91] offset:6144\n"
"v_mfma_f32_16x16x32_bf16 v[92:95], a[160:163], v[156:159], v[92:95]\n"
"buffer_load_dwordx4 v[180:183], v0, s[4:7], s98 offen offset:3072 sc1\n"
"v_mfma_f32_16x16x32_bf16 v[88:91], a[192:195], v[156:159], 0\n"
"v_mfma_f32_16x16x32_bf16 v[88:91], a[196:199], v[108:111], v[88:91]\n"
"v_mfma_f32_16x16x32_bf16 v[88:91], a[200:203], v[112:115], v[88:91]\n"
"v_mfma_f32_16x16x32_bf16 v[88:91], a[204:207], v[196:199], v[88:91]\n"
"buffer_load_dwordx4 v[184:187], v1, s[4:7], s98 offen sc1\n"
"v_mfma_f32_16x16x32_bf16 v[88:91], a[208:211], v[200:203], v[88:91]\n"
"ds_read_b128 v[96:99], v2\n"
"ds_read_b128 v[100:103], v2 offset:1024\n"
"v_mfma_f32_16x16x32_bf16 v[88:91], a[212:215], v[128:131], v[88:91]\n"
"ds_write_b128 v3, v[92:95] offset:7168\n"
"v_mfma_f32_16x16x32_bf16 v[88:91], a[216:219], v[120:123], v[88:91]\n"
"v_mfma_f32_16x16x32_bf16 v[88:91], a[220:223], v[136:139], v[88:91]\n"
"buffer_load_dwordx4 v[188:191], v1, s[4:7], s98 offen offset:1024 sc1\n"
"v_mfma_f32_16x16x32_bf16 v[92:95], a[252:255], v[136:139], 0\n"
"v_mfma_f32_16x16x32_bf16 v[92:95], a[248:251], v[120:123], v[92:95]\n"
"s_waitcnt lgkmcnt(1)\n"
"v_lshlrev_b32_e32 v255, 16, v96\n"
"v_and_b32_e32 v254, 0xffff0000, v96\n"
"v_mfma_f32_16x16x32_bf16 v[92:95], a[244:247], v[128:131], v[92:95]\n"
"v_lshlrev_b32_e32 v253, 16, v97\n"
"v_and_b32_e32 v252, 0xffff0000, v97\n"
"v_mfma_f32_16x16x32_bf16 v[92:95], a[240:243], v[200:203], v[92:95]\n"
"buffer_load_dwordx4 v[192:195], v1, s[4:7], s98 offen offset:2048 sc1\n"
"v_mfma_f32_16x16x32_bf16 v[92:95], a[236:239], v[196:199], v[92:95]\n"
"v_lshlrev_b32_e32 v251, 16, v98\n"
"v_and_b32_e32 v250, 0xffff0000, v98\n"
"v_mfma_f32_16x16x32_bf16 v[92:95], a[232:235], v[112:115], v[92:95]\n"
"ds_write_b128 v3, v[88:91] offset:8192\n"
"v_mfma_f32_16x16x32_bf16 v[92:95], a[228:231], v[108:111], v[92:95]\n"
"v_lshlrev_b32_e32 v249, 16, v99\n"
"v_and_b32_e32 v248, 0xffff0000, v99\n"
"v_mfma_f32_16x16x32_bf16 v[92:95], a[224:227], v[156:159], v[92:95]\n"
"s_waitcnt vmcnt(12)\n"
"buffer_load_dwordx4 v[196:199], v1, s[4:7], s98 offen offset:3072 sc1\n"
"; @A G2 a.2\n"
"v_mfma_f32_16x16x32_bf16 v[96:99], a[128:131], v[148:151], 0\n"
"v_lshlrev_b32_e32 v247, 16, v100\n"
"v_and_b32_e32 v246, 0xffff0000, v100\n"
"v_mfma_f32_16x16x32_bf16 v[96:99], a[132:135], v[160:163], v[96:99]\n"
"v_lshlrev_b32_e32 v245, 16, v101\n"
"v_and_b32_e32 v244, 0xffff0000, v101\n"
"v_mfma_f32_16x16x32_bf16 v[96:99], a[136:139], v[124:127], v[96:99]\n"
"v_lshlrev_b32_e32 v243, 16, v102\n"
"v_and_b32_e32 v242, 0xffff0000, v102\n"
"v_mfma_f32_16x16x32_bf16 v[96:99], a[140:143], v[140:143], v[96:99]\n"
"s_waitcnt vmcnt(8)\n"
"v_lshlrev_b32_e32 v241, 16, v103\n"
"v_and_b32_e32 v240, 0xffff0000, v103\n"
"v_mfma_f32_16x16x32_bf16 v[96:99], a[144:147], v[152:155], v[96:99]\n"
"v_mfma_f32_16x16x32_bf16 v[96:99], a[148:151], v[164:167], v[96:99]\n"
"ds_write_b128 v3, v[92:95] offset:9216\n"
"v_mfma_f32_16x16x32_bf16 v[96:99], a[152:155], v[116:119], v[96:99]\n"
"v_mfma_f32_16x16x32_bf16 v[96:99], a[156:159], v[132:135], v[96:99]\n"
"v_mfma_f32_16x16x32_bf16 v[104:107], a[188:191], v[132:135], 0\n"
"s_add_u32 s98, s52, 0x440000\n"
"buffer_load_dwordx4 v[88:91], v0, s[4:7], s98 offen sc1\n"
"v_mfma_f32_16x16x32_bf16 v[104:107], a[184:187], v[116:119], v[104:107]\n"
"s_waitcnt lgkmcnt(0)\n"
"s_barrier\n"
"v_mfma_f32_16x16x32_bf16 v[104:107], a[180:183], v[164:167], v[104:107]\n"
"ds_read_b128 v[144:147], v4\n"
"ds_read_b128 v[112:115], v5\n"
"v_mfma_f32_16x16x32_bf16 v[104:107], a[176:179], v[152:155], v[104:107]\n"
"buffer_load_dwordx4 v[92:95], v0, s[4:7], s98 offen offset:1024 sc1\n"
"v_mfma_f32_16x16x32_bf16 v[104:107], a[172:175], v[140:143], v[104:107]\n"
"v_cmp_u_f32_e32 vcc, v96, v96\n"
"v_mfma_f32_16x16x32_bf16 v[104:107], a[168:171], v[124:127], v[104:107]\n"
"s_cbranch_vccnz .Lmk_p112s\n"
".Lmk_p112r:\n"
"v_mfma_f32_16x16x32_bf16 v[104:107], a[164:167], v[160:163], v[104:107]\n"
"ds_write_b128 v3, v[96:99] offset:12288\n"
"v_mfma_f32_16x16x32_bf16 v[104:107], a[160:163], v[148:151], v[104:107]\n"
"buffer_load_dwordx4 v[96:99], v0, s[4:7], s98 offen offset:2048 sc1\n"
"v_mfma_f32_16x16x32_bf16 v[120:123], a[192:195], v[148:151], 0\n"
"ds_read_b128 v[156:159], v4 offset:1024\n"
"ds_read_b128 v[128:131], v5 offset:1024\n"
"v_mfma_f32_16x16x32_bf16 v[120:123], a[196:199], v[160:163], v[120:123]\n"
"s_waitcnt lgkmcnt(3)\n"
"v_add_f32_e32 v144, v144, v255\n"
"v_add_f32_e32 v145, v145, v254\n"
"v_mfma_f32_16x16x32_bf16 v[120:123], a[200:203], v[124:127], v[120:123]\n"
"buffer_load_dwordx4 v[100:103], v0, s[4:7], s98 offen offset:3072 sc1\n"
"v_mfma_f32_16x16x32_bf16 v[120:123], a[204:207], v[140:143], v[120:123]\n"
"v_add_f32_e32 v146, v146, v253\n"
"v_add_f32_e32 v147, v147, v252\n"
"v_mfma_f32_16x16x32_bf16 v[120:123], a[208:211], v[152:155], v[120:123]\n"
"v_add_f32_e32 v144, v144, v112\n"
"v_add_f32_e32 v145, v145, v113\n"
"v_mfma_f32_16x16x32_bf16 v[120:123], a[212:215], v[164:167], v[120:123]\n"
"ds_write_b128 v3, v[104:107] offset:13312\n"
"v_mfma_f32_16x16x32_bf16 v[120:123], a[216:219], v[116:119], v[120:123]\n"
"buffer_load_dwordx4 v[104:107], v1, s[4:7], s98 offen sc1\n"
"v_mfma_f32_16x16x32_bf16 v[120:123], a[220:223], v[132:135], v[120:123]\n"
"ds_read_b128 v[108:111], v4 offset:2048\n"
"ds_read_b128 v[136:139], v5 offset:2048\n"
"v_mfma_f32_16x16x32_bf16 v[200:203], a[252:255], v[132:135], 0\n"
"s_waitcnt lgkmcnt(3)\n"
"v_add_f32_e32 v146, v146, v114\n"
"v_add_f32_e32 v147, v147, v115\n"
"v_mfma_f32_16x16x32_bf16 v[200:203], a[248:251], v[116:119], v[200:203]\n"
"buffer_load_dwordx4 v[112:115], v1, s[4:7], s98 offen offset:1024 sc1\n"
"v_mfma_f32_16x16x32_bf16 v[200:203], a[244:247], v[164:167], v[200:203]\n"
"v_add_f32_e32 v156, v156, v251\n"
"v_add_f32_e32 v157, v157, v250\n"
"v_mfma_f32_16x16x32_bf16 v[200:203], a[240:243], v[152:155], v[200:203]\n"
"v_add_f32_e32 v158, v158, v249\n"
"v_add_f32_e32 v159, v159, v248\n"
"v_mfma_f32_16x16x32_bf16 v[200:203], a[236:239], v[140:143], v[200:203]\n"
"buffer_load_dwordx4 v[116:119], v1, s[4:7], s98 offen offset:2048 sc1\n"
"v_mfma_f32_16x16x32_bf16 v[200:203], a[232:235], v[124:127], v[200:203]\n"
"ds_write_b128 v3, v[120:123] offset:14336\n"
"v_mfma_f32_16x16x32_bf16 v[200:203], a[228:231], v[160:163], v[200:203]\n"
"ds_read_b128 v[120:123], v4 offset:3072\n"
"ds_read_b128 v[152:155], v5 offset:3072\n"
"v_mfma_f32_16x16x32_bf16 v[200:203], a[224:227], v[148:151], v[200:203]\n"
"s_waitcnt vmcnt(11)\n"
"buffer_load_dwordx4 v[124:127], v1, s[4:7], s98 offen offset:3072 sc1\n"
"; @A G2 a.3\n"
"v_mfma_f32_16x16x32_bf16 v[164:167], a[128:131], v[172:175], v[144:147]\n"
"s_add_u32 s98, s53, 0x440000\n"
"v_add_f32_e32 v156, v156, v128\n"
"v_add_f32_e32 v157, v157, v129\n"
"v_mfma_f32_16x16x32_bf16 v[164:167], a[132:135], v[176:179], v[164:167]\n"
"s_waitcnt lgkmcnt(3)\n"
"v_add_f32_e32 v158, v158, v130\n"
"v_add_f32_e32 v159, v159, v131\n"
"v_mfma_f32_16x16x32_bf16 v[164:167], a[136:139], v[168:171], v[164:167]\n"
"buffer_load_dwordx4 v[128:131], v0, s[4:7], s98 offen sc1\n"
"v_mfma_f32_16x16x32_bf16 v[164:167], a[140:143], v[180:183], v[164:167]\n"
"s_waitcnt vmcnt(9)\n"
"v_add_f32_e32 v108, v108, v247\n"
"v_add_f32_e32 v109, v109, v246\n"
"v_mfma_f32_16x16x32_bf16 v[164:167], a[144:147], v[184:187], v[164:167]\n"
"v_add_f32_e32 v110, v110, v245\n"
"v_add_f32_e32 v111, v111, v244\n"
"v_mfma_f32_16x16x32_bf16 v[164:167], a[148:151], v[188:191], v[164:167]\n"
"ds_write_b128 v3, v[200:203] offset:15360\n"
"v_mfma_f32_16x16x32_bf16 v[164:167], a[152:155], v[192:195], v[164:167]\n"
"buffer_load_dwordx4 v[132:135], v0, s[4:7], s98 offen offset:1024 sc1\n"
"v_mfma_f32_16x16x32_bf16 v[164:167], a[156:159], v[196:199], v[164:167]\n"
"v_add_f32_e32 v108, v108, v136\n"
"v_add_f32_e32 v109, v109, v137\n"
"v_mfma_f32_16x16x32_bf16 v[200:203], a[188:191], v[196:199], v[156:159]\n"
"s_waitcnt lgkmcnt(1)\n"
"v_add_f32_e32 v110, v110, v138\n"
"v_add_f32_e32 v111, v111, v139\n"
"v_mfma_f32_16x16x32_bf16 v[200:203], a[184:187], v[192:195], v[200:203]\n"
"s_waitcnt lgkmcnt(0)\n"
"s_barrier\n"
"v_mfma_f32_16x16x32_bf16 v[200:203], a[180:183], v[188:191], v[200:203]\n"
"buffer_load_dwordx4 v[136:139], v0, s[4:7], s98 offen offset:2048 sc1\n"
"v_mfma_f32_16x16x32_bf16 v[200:203], a[176:179], v[184:187], v[200:203]\n"
"v_add_f32_e32 v120, v120, v243\n"
"v_add_f32_e32 v121, v121, v242\n"
"v_mfma_f32_16x16x32_bf16 v[200:203], a[172:175], v[180:183], v[200:203]\n"
"v_cmp_u_f32_e32 vcc, v164, v164\n"
"v_add_f32_e32 v122, v122, v241\n"
"v_mfma_f32_16x16x32_bf16 v[200:203], a[168:171], v[168:171], v[200:203]\n"
"s_cbranch_vccnz .Lmk_p113s\n"
".Lmk_p113r:\n"
"v_add_f32_e32 v123, v123, v240\n"
"v_mfma_f32_16x16x32_bf16 v[200:203], a[164:167], v[176:179], v[200:203]\n"
"buffer_load_dwordx4 v[140:143], v0, s[4:7], s98 offen offset:3072 sc1\n"
"v_mfma_f32_16x16x32_bf16 v[200:203], a[160:163], v[172:175], v[200:203]\n"
"v_add_f32_e32 v120, v120, v152\n"
"v_add_f32_e32 v121, v121, v153\n"
"v_mfma_f32_16x16x32_bf16 v[108:111], a[192:195], v[172:175], v[108:111]\n"
"v_add_f32_e32 v122, v122, v154\n"
"v_add_f32_e32 v123, v123, v155\n"
"v_mfma_f32_16x16x32_bf16 v[108:111], a[196:199], v[176:179], v[108:111]\n"
"buffer_load_dwordx4 v[144:147], v1, s[4:7], s98 offen sc1\n"
"v_mfma_f32_16x16x32_bf16 v[108:111], a[200:203], v[168:171], v[108:111]\n"
"ds_read_b128 v[204:207], v6\n"
"ds_read_b128 v[208:211], v6 offset:1024\n"
"v_mfma_f32_16x16x32_bf16 v[108:111], a[204:207], v[180:183], v[108:111]\n"
"ds_read_b128 v[148:151], v6 offset:2048\n"
"ds_read_b128 v[152:155], v6 offset:3072\n"
"v_mfma_f32_16x16x32_bf16 v[108:111], a[208:211], v[184:187], v[108:111]\n"
"buffer_load_dwordx4 v[156:159], v1, s[4:7], s98 offen offset:1024 sc1\n"
"v_mfma_f32_16x16x32_bf16 v[108:111], a[212:215], v[188:191], v[108:111]\n"
"v_mfma_f32_16x16x32_bf16 v[108:111], a[216:219], v[192:195], v[108:111]\n"
"v_mfma_f32_16x16x32_bf16 v[108:111], a[220:223], v[196:199], v[108:111]\n"
"buffer_load_dwordx4 v[160:163], v1, s[4:7], s98 offen offset:2048 sc1\n"
"v_mfma_f32_16x16x32_bf16 v[120:123], a[252:255], v[196:199], v[120:123]\n"
"s_waitcnt lgkmcnt(0)\n"
"v_add_f32_e32 v204, v164, v204\n"
"v_add_f32_e32 v205, v165, v205\n"
"v_mfma_f32_16x16x32_bf16 v[120:123], a[248:251], v[192:195], v[120:123]\n"
"v_add_f32_e32 v206, v166, v206\n"
"v_add_f32_e32 v207, v167, v207\n"
"v_mfma_f32_16x16x32_bf16 v[120:123], a[244:247], v[188:191], v[120:123]\n"
"buffer_load_dwordx4 v[164:167], v1, s[4:7], s98 offen offset:3072 sc1\n"
"v_mfma_f32_16x16x32_bf16 v[120:123], a[240:243], v[184:187], v[120:123]\n"
"v_add_f32_e32 v208, v200, v208\n"
"v_add_f32_e32 v209, v201, v209\n"
"v_mfma_f32_16x16x32_bf16 v[120:123], a[236:239], v[180:183], v[120:123]\n"
"v_add_f32_e32 v210, v202, v210\n"
"v_add_f32_e32 v211, v203, v211\n"
"v_mfma_f32_16x16x32_bf16 v[120:123], a[232:235], v[168:171], v[120:123]\n"
"v_cvt_pk_bf16_f32 v168, v204, v205\n"
"v_cvt_pk_bf16_f32 v169, v206, v207\n"
"v_mfma_f32_16x16x32_bf16 v[120:123], a[228:231], v[176:179], v[120:123]\n"
"s_add_u32 s98, s61, 0x400000\n"
"v_cvt_pk_bf16_f32 v170, v208, v209\n"
"v_cvt_pk_bf16_f32 v171, v210, v211\n"
"v_mfma_f32_16x16x32_bf16 v[120:123], a[224:227], v[172:175], v[120:123]\n"
"buffer_store_dwordx4 v[168:171], v0, s[4:7], s98 offen sc1\n"
"ds_write_b128 v2, v[168:171]\n"
"s_branch .Lmk_loop\n"
".Lmk_f2s:\n"
";\n"
"s_mov_b32 s1, 0x10000\n"
".Lmk_f2l:\n"
"buffer_load_dwordx2 v[254:255], v13, s[4:7], s98 offen sc1\n"
"s_waitcnt vmcnt(0)\n"
"v_cmp_ne_u64_e32 vcc, v[254:255], v[18:19]\n"
"s_cbranch_vccz .Lmk_f2r\n"
"s_sub_u32 s1, s1, 1\n"
"s_cbranch_scc0 .Lmk_f2l\n"
"s_branch .Lmk_f2r\n"
".Lmk_f1s:\n"
";\n"
"s_mov_b32 s1, 0x10000\n"
".Lmk_f1l:\n"
"buffer_load_dwordx2 v[254:255], v12, s[4:7], s98 offen sc1\n"
"s_waitcnt vmcnt(0)\n"
"v_cmp_ne_u64_e32 vcc, v[254:255], v[18:19]\n"
"s_cbranch_vccz .Lmk_f1r\n"
"s_sub_u32 s1, s1, 1\n"
"s_cbranch_scc0 .Lmk_f1l\n"
"s_branch .Lmk_f1r\n"
".Lmk_f6s:\n"
";\n"
"s_mov_b32 s1, 0x10000\n"
".Lmk_f6l:\n"
"buffer_load_dwordx2 v[250:251], v41, s[4:7], s97 offen sc1\n"
"s_waitcnt vmcnt(0)\n"
"v_cmp_ne_u64_e32 vcc, v[250:251], v[18:19]\n"
"s_cbranch_vccz .Lmk_f6r\n"
"s_sub_u32 s1, s1, 1\n"
"s_cbranch_scc0 .Lmk_f6l\n"
"s_branch .Lmk_f6r\n"
".Lmk_f3s:\n"
";\n"
"s_mov_b32 s1, 0x10000\n"
".Lmk_f3l:\n"
"buffer_load_dwordx2 v[246:247], v12, s[4:7], s97 offen sc1\n"
"s_waitcnt vmcnt(0)\n"
"v_cmp_ne_u64_e32 vcc, v[246:247], v[18:19]\n"
"s_cbranch_vccz .Lmk_f3r\n"
"s_sub_u32 s1, s1, 1\n"
"s_cbranch_scc0 .Lmk_f3l\n"
"s_branch .Lmk_f3r\n"
".Lmk_f7s:\n"
";\n"
"s_mov_b32 s1, 0x10000\n"
".Lmk_f7l:\n"
"buffer_load_dwordx2 v[246:247], v13, s[4:7], s98 offen sc1\n"
"s_waitcnt vmcnt(0)\n"
"v_cmp_ne_u64_e32 vcc, v[246:247], v[18:19]\n"
"s_cbranch_vccz .Lmk_f7r\n"
"s_sub_u32 s1, s1, 1\n"
"s_cbranch_scc0 .Lmk_f7l\n"
"s_branch .Lmk_f7r\n"
".Lmk_f4s:\n"
";\n"
"s_mov_b32 s1, 0x10000\n"
".Lmk_f4l:\n"
"buffer_load_dwordx2 v[250:251], off, s[4:7], s96 sc1\n"
"s_waitcnt vmcnt(0)\n"
"v_cmp_ne_u64_e32 vcc, v[250:251], v[18:19]\n"
"s_cbranch_vccz .Lmk_f4r\n"
"s_sub_u32 s1, s1, 1\n"
"s_cbranch_scc0 .Lmk_f4l\n"
"s_branch .Lmk_f4r\n"
".Lmk_x18s:\n"
"s_nop 3\n"
"v_mov_b32_e32 v24, 0xff800000\n"
"v_mov_b32_e32 v25, 0xff800000\n"
"v_mov_b32_e32 v26, 0xff800000\n"
"v_mov_b32_e32 v27, 0xff800000\n"
"v_mov_b32_e32 v36, 0xff800000\n"
"v_mov_b32_e32 v37, 0xff800000\n"
"v_mov_b32_e32 v38, 0xff800000\n"
"v_mov_b32_e32 v39, 0xff800000\n"
"s_nop 1\n"
"s_branch .Lmk_x18r\n"
".Lmk_x19s:\n"
"s_nop 3\n"
"v_mov_b32_e32 v24, 0xff800000\n"
"v_mov_b32_e32 v25, 0xff800000\n"
"v_mov_b32_e32 v26, 0xff800000\n"
"v_mov_b32_e32 v27, 0xff800000\n"
"v_mov_b32_e32 v36, 0xff800000\n"
"v_mov_b32_e32 v37, 0xff800000\n"
"v_mov_b32_e32 v38, 0xff800000\n"
"v_mov_b32_e32 v39, 0xff800000\n"
"s_nop 1\n"
"s_branch .Lmk_x19r\n"
".Lmk_x20s:\n"
"s_nop 3\n"
"v_mov_b32_e32 v24, 0xff800000\n"
"v_mov_b32_e32 v25, 0xff800000\n"
"v_mov_b32_e32 v26, 0xff800000\n"
"v_mov_b32_e32 v27, 0xff800000\n"
"v_mov_b32_e32 v36, 0xff800000\n"
"v_mov_b32_e32 v37, 0xff800000\n"
"v_mov_b32_e32 v38, 0xff800000\n"
"v_mov_b32_e32 v39, 0xff800000\n"
"s_nop 1\n"
"s_branch .Lmk_x20r\n"
".Lmk_x21s:\n"
"s_nop 3\n"
"v_mov_b32_e32 v24, 0xff800000\n"
"v_mov_b32_e32 v25, 0xff800000\n"
"v_mov_b32_e32 v26, 0xff800000\n"
"v_mov_b32_e32 v27, 0xff800000\n"
"v_mov_b32_e32 v36, 0xff800000\n"
"v_mov_b32_e32 v37, 0xff800000\n"
"v_mov_b32_e32 v38, 0xff800000\n"
"v_mov_b32_e32 v39, 0xff800000\n"
"s_nop 1\n"
"s_branch .Lmk_x21r\n"
".Lmk_f14s:\n"
";\n"
"s_mov_b32 s1, 0x10000\n"
".Lmk_f14l:\n"
"buffer_load_dwordx2 v[238:239], off, s[4:7], s97 sc1\n"
"s_waitcnt vmcnt(0)\n"
"v_cmp_ne_u64_e32 vcc, v[238:239], v[18:19]\n"
"s_cbranch_vccz .Lmk_f14r\n"
"s_sub_u32 s1, s1, 1\n"
"s_cbranch_scc0 .Lmk_f14l\n"
"s_branch .Lmk_f14r\n"
".Lmk_b16:\n"
"s_cmp_lg_u32 s2, 0\n"
"s_cbranch_scc1 .Lmk_k22\n"
"s_mov_b32 s2, 1\n"
"s_waitcnt vmcnt(0) lgkmcnt(0)\n"
"s_nop 7\n"
"s_mov_b32 s97, 11\n"
"v_add_u32_e32 v248, 0x3800, v15\n"
"v_add_u32_e32 v247, 0x3800, v254\n"
"ds_read_b128 v[36:39], v15 offset:24576\n"
"ds_read_b128 v[40:43], v254 offset:24832\n"
"s_waitcnt lgkmcnt(0)\n"
"v_mfma_f32_16x16x32_bf16 v[48:51], v[36:39], v[28:31], 0\n"
"v_mfma_f32_16x16x32_bf16 v[52:55], v[36:39], v[32:35], 0\n"
"v_mfma_f32_16x16x32_bf16 v[36:39], v[40:43], v[32:35], 0\n"
"v_mfma_f32_16x16x32_bf16 v[56:59], v[40:43], v[28:31], 0\n"
".Lmk_b16l:\n"
"ds_read_b128 v[40:43], v248 offset:8192\n"
"ds_read_b128 v[60:63], v247 offset:8448\n"
"s_cmp_gt_i32 s78, s97\n"
"s_cselect_b32 s96, 0xff800000, 0\n"
"v_mov_b32_e32 v64, s96\n"
"v_mov_b32_e32 v65, s96\n"
"v_mov_b32_e32 v66, s96\n"
"v_mov_b32_e32 v67, s96\n"
"v_subrev_u32_e32 v248, 0x800, v248\n"
"v_subrev_u32_e32 v247, 0x800, v247\n"
"s_waitcnt lgkmcnt(0)\n"
"v_mfma_f32_16x16x32_bf16 v[72:75], v[40:43], v[28:31], v[64:67]\n"
"v_mfma_f32_16x16x32_bf16 v[76:79], v[40:43], v[32:35], v[64:67]\n"
"v_mfma_f32_16x16x32_bf16 v[40:43], v[60:63], v[32:35], v[64:67]\n"
"v_mfma_f32_16x16x32_bf16 v[84:87], v[60:63], v[28:31], v[64:67]\n"
"s_nop 7\n"
"s_cmp_lg_u32 s97, 4\n"
"s_cbranch_scc1 .Lmk_b16m\n"
"v_cndmask_b32_e64 v72, v48, v72, s[20:21]\n"
"v_cndmask_b32_e64 v73, v49, v73, s[22:23]\n"
"v_cndmask_b32_e64 v74, v50, v74, s[24:25]\n"
"v_cndmask_b32_e64 v75, v51, v75, s[26:27]\n"
"v_cndmask_b32_e64 v84, v56, v84, s[28:29]\n"
"v_cndmask_b32_e64 v85, v57, v85, s[30:31]\n"
"v_cndmask_b32_e64 v86, v58, v86, s[32:33]\n"
"v_cndmask_b32_e64 v87, v59, v87, s[34:35]\n"
"v_cndmask_b32_e64 v76, v52, v76, s[36:37]\n"
"v_cndmask_b32_e64 v77, v53, v77, s[38:39]\n"
"v_cndmask_b32_e64 v78, v54, v78, s[40:41]\n"
"v_cndmask_b32_e64 v79, v55, v79, s[42:43]\n"
"v_cndmask_b32_e64 v40, v36, v40, s[44:45]\n"
"v_cndmask_b32_e64 v41, v37, v41, s[46:47]\n"
"v_cndmask_b32_e64 v42, v38, v42, s[48:49]\n"
"v_cndmask_b32_e64 v43, v39, v43, s[50:51]\n"
".Lmk_b16m:\n"
"v_max3_f32 v253, v253, v72, v73\n"
"v_max3_f32 v251, v251, v76, v77\n"
"v_max3_f32 v253, v253, v74, v75\n"
"v_max3_f32 v251, v251, v78, v79\n"
"v_max3_f32 v253, v253, v84, v85\n"
"v_max3_f32 v251, v251, v40, v41\n"
"v_max3_f32 v253, v253, v86, v87\n"
"v_max3_f32 v251, v251, v42, v43\n"
"s_sub_u32 s97, s97, 1\n"
"s_cmp_ge_i32 s97, 4\n"
"s_cbranch_scc1 .Lmk_b16l\n"
"v_mov_b32_e32 v248, v253\n"
"v_mov_b32_e32 v247, v251\n"
"s_nop 0\n"
"v_permlane16_swap_b32_e32 v253, v248\n"
"v_permlane16_swap_b32_e32 v251, v247\n"
"v_max_f32_e32 v253, v253, v248\n"
"v_max_f32_e32 v251, v251, v247\n"
"v_mov_b32_e32 v248, v253\n"
"v_mov_b32_e32 v247, v251\n"
"s_nop 0\n"
"v_permlane32_swap_b32_e32 v253, v248\n"
"v_permlane32_swap_b32_e32 v251, v247\n"
"v_max_f32_e32 v253, v253, v248\n"
"v_max_f32_e32 v251, v251, v247\n"
"v_mov_b32_e32 v250, v253\n"
"v_mov_b32_e32 v249, v251\n"
"s_branch .Lmk_a15\n"
".Lmk_x25s:\n"
"s_nop 3\n"
"v_mov_b32_e32 v116, 0xff800000\n"
"v_mov_b32_e32 v117, 0xff800000\n"
"v_mov_b32_e32 v118, 0xff800000\n"
"v_mov_b32_e32 v119, 0xff800000\n"
"v_mov_b32_e32 v120, 0xff800000\n"
"v_mov_b32_e32 v121, 0xff800000\n"
"v_mov_b32_e32 v122, 0xff800000\n"
"v_mov_b32_e32 v123, 0xff800000\n"
"s_nop 1\n"
"s_branch .Lmk_x25r\n"
".Lmk_x26s:\n"
"s_nop 3\n"
"v_mov_b32_e32 v116, 0xff800000\n"
"v_mov_b32_e32 v117, 0xff800000\n"
"v_mov_b32_e32 v118, 0xff800000\n"
"v_mov_b32_e32 v119, 0xff800000\n"
"v_mov_b32_e32 v120, 0xff800000\n"
"v_mov_b32_e32 v121, 0xff800000\n"
"v_mov_b32_e32 v122, 0xff800000\n"
"v_mov_b32_e32 v123, 0xff800000\n"
"s_nop 1\n"
"s_branch .Lmk_x26r\n"
".Lmk_x27s:\n"
"s_nop 3\n"
"v_mov_b32_e32 v116, 0xff800000\n"
"v_mov_b32_e32 v117, 0xff800000\n"
"v_mov_b32_e32 v118, 0xff800000\n"
"v_mov_b32_e32 v119, 0xff800000\n"
"v_mov_b32_e32 v120, 0xff800000\n"
"v_mov_b32_e32 v121, 0xff800000\n"
"v_mov_b32_e32 v122, 0xff800000\n"
"v_mov_b32_e32 v123, 0xff800000\n"
"s_nop 1\n"
"s_branch .Lmk_x27r\n"
".Lmk_f5s:\n"
";\n"
"s_mov_b32 s1, 0x10000\n"
".Lmk_f5l:\n"
"buffer_load_dwordx2 v[246:247], v12, s[4:7], s96 offen sc1\n"
"s_waitcnt vmcnt(0)\n"
"v_cmp_ne_u64_e32 vcc, v[246:247], v[18:19]\n"
"s_cbranch_vccz .Lmk_f5r\n"
"s_sub_u32 s1, s1, 1\n"
"s_cbranch_scc0 .Lmk_f5l\n"
"s_branch .Lmk_f5r\n"
".Lmk_b24:\n"
"s_cmp_lg_u32 s2, 0\n"
"s_cbranch_scc1 .Lmk_k29\n"
"s_mov_b32 s2, 1\n"
"s_waitcnt vmcnt(0) lgkmcnt(0)\n"
"s_nop 7\n"
"s_mov_b32 s96, 7\n"
"v_add_u32_e32 v248, 0x3800, v15\n"
"v_add_u32_e32 v247, 0x3800, v254\n"
"ds_read_b128 v[172:175], v15 offset:16384\n"
"ds_read_b128 v[192:195], v254 offset:16640\n"
"s_waitcnt lgkmcnt(0)\n"
"v_mfma_f32_16x16x32_bf16 v[196:199], v[172:175], v[92:95], 0\n"
"v_mfma_f32_16x16x32_bf16 v[212:215], v[172:175], v[96:99], 0\n"
"v_mfma_f32_16x16x32_bf16 v[172:175], v[192:195], v[96:99], 0\n"
"v_mfma_f32_16x16x32_bf16 v[220:223], v[192:195], v[92:95], 0\n"
".Lmk_b24l:\n"
"ds_read_b128 v[192:195], v248\n"
"ds_read_b128 v[224:227], v247 offset:256\n"
"s_cmp_gt_i32 s78, s96\n"
"s_cselect_b32 s95, 0xff800000, 0\n"
"v_mov_b32_e32 v228, s95\n"
"v_mov_b32_e32 v229, s95\n"
"v_mov_b32_e32 v230, s95\n"
"v_mov_b32_e32 v231, s95\n"
"v_subrev_u32_e32 v248, 0x800, v248\n"
"v_subrev_u32_e32 v247, 0x800, v247\n"
"s_waitcnt lgkmcnt(0)\n"
"v_mfma_f32_16x16x32_bf16 v[232:235], v[192:195], v[92:95], v[228:231]\n"
"v_mfma_f32_16x16x32_bf16 v[236:239], v[192:195], v[96:99], v[228:231]\n"
"v_mfma_f32_16x16x32_bf16 v[192:195], v[224:227], v[96:99], v[228:231]\n"
"v_mfma_f32_16x16x32_bf16 v[240:243], v[224:227], v[92:95], v[228:231]\n"
"s_nop 7\n"
"s_cmp_lg_u32 s96, 0\n"
"s_cbranch_scc1 .Lmk_b24m\n"
"v_cndmask_b32_e64 v232, v196, v232, s[20:21]\n"
"v_cndmask_b32_e64 v233, v197, v233, s[22:23]\n"
"v_cndmask_b32_e64 v234, v198, v234, s[24:25]\n"
"v_cndmask_b32_e64 v235, v199, v235, s[26:27]\n"
"v_cndmask_b32_e64 v240, v220, v240, s[28:29]\n"
"v_cndmask_b32_e64 v241, v221, v241, s[30:31]\n"
"v_cndmask_b32_e64 v242, v222, v242, s[32:33]\n"
"v_cndmask_b32_e64 v243, v223, v243, s[34:35]\n"
"v_cndmask_b32_e64 v236, v212, v236, s[36:37]\n"
"v_cndmask_b32_e64 v237, v213, v237, s[38:39]\n"
"v_cndmask_b32_e64 v238, v214, v238, s[40:41]\n"
"v_cndmask_b32_e64 v239, v215, v239, s[42:43]\n"
"v_cndmask_b32_e64 v192, v172, v192, s[44:45]\n"
"v_cndmask_b32_e64 v193, v173, v193, s[46:47]\n"
"v_cndmask_b32_e64 v194, v174, v194, s[48:49]\n"
"v_cndmask_b32_e64 v195, v175, v195, s[50:51]\n"
".Lmk_b24m:\n"
"v_max3_f32 v253, v253, v232, v233\n"
"v_max3_f32 v251, v251, v236, v237\n"
"v_max3_f32 v253, v253, v234, v235\n"
"v_max3_f32 v251, v251, v238, v239\n"
"v_max3_f32 v253, v253, v240, v241\n"
"v_max3_f32 v251, v251, v192, v193\n"
"v_max3_f32 v253, v253, v242, v243\n"
"v_max3_f32 v251, v251, v194, v195\n"
"s_sub_u32 s96, s96, 1\n"
"s_cmp_ge_i32 s96, 0\n"
"s_cbranch_scc1 .Lmk_b24l\n"
"v_mov_b32_e32 v248, v253\n"
"v_mov_b32_e32 v247, v251\n"
"s_nop 0\n"
"v_permlane16_swap_b32_e32 v253, v248\n"
"v_permlane16_swap_b32_e32 v251, v247\n"
"v_max_f32_e32 v253, v253, v248\n"
"v_max_f32_e32 v251, v251, v247\n"
"v_mov_b32_e32 v248, v253\n"
"v_mov_b32_e32 v247, v251\n"
"s_nop 0\n"
"v_permlane32_swap_b32_e32 v253, v248\n"
"v_permlane32_swap_b32_e32 v251, v247\n"
"v_max_f32_e32 v253, v253, v248\n"
"v_max_f32_e32 v251, v251, v247\n"
"v_mov_b32_e32 v250, v253\n"
"v_mov_b32_e32 v249, v251\n"
"s_branch .Lmk_a23\n"
".Lmk_p30s:\n"
";\n"
"s_waitcnt vmcnt(16)\n"
"s_mov_b32 s1, 0x10000\n"
".Lmk_p30l:\n"
"s_add_u32 s98, s56, 0x40000\n"
"buffer_load_dwordx4 v[176:179], v0, s[4:7], s98 offen sc1\n"
"buffer_load_dwordx4 v[200:203], v0, s[4:7], s98 offen offset:1024 sc1\n"
"buffer_load_dwordx4 v[188:191], v0, s[4:7], s98 offen offset:2048 sc1\n"
"buffer_load_dwordx4 v[204:207], v0, s[4:7], s98 offen offset:3072 sc1\n"
"buffer_load_dwordx4 v[152:155], v1, s[4:7], s98 offen sc1\n"
"buffer_load_dwordx4 v[208:211], v1, s[4:7], s98 offen offset:1024 sc1\n"
"buffer_load_dwordx4 v[120:123], v1, s[4:7], s98 offen offset:2048 sc1\n"
"buffer_load_dwordx4 v[132:135], v1, s[4:7], s98 offen offset:3072 sc1\n"
"s_add_u32 s98, s57, 0x40000\n"
"buffer_load_dwordx4 v[216:219], v0, s[4:7], s98 offen sc1\n"
"buffer_load_dwordx4 v[128:131], v0, s[4:7], s98 offen offset:1024 sc1\n"
"buffer_load_dwordx4 v[180:183], v0, s[4:7], s98 offen offset:2048 sc1\n"
"buffer_load_dwordx4 v[100:103], v0, s[4:7], s98 offen offset:3072 sc1\n"
"buffer_load_dwordx4 v[184:187], v1, s[4:7], s98 offen sc1\n"
"buffer_load_dwordx4 v[104:107], v1, s[4:7], s98 offen offset:1024 sc1\n"
"buffer_load_dwordx4 v[108:111], v1, s[4:7], s98 offen offset:2048 sc1\n"
"buffer_load_dwordx4 v[136:139], v1, s[4:7], s98 offen offset:3072 sc1\n"
"s_add_u32 s98, s58, 0x40000\n"
"buffer_load_dwordx4 v[144:147], v0, s[4:7], s98 offen sc1\n"
"buffer_load_dwordx4 v[140:143], v0, s[4:7], s98 offen offset:1024 sc1\n"
"buffer_load_dwordx4 v[116:119], v0, s[4:7], s98 offen offset:2048 sc1\n"
"buffer_load_dwordx4 v[112:115], v0, s[4:7], s98 offen offset:3072 sc1\n"
"buffer_load_dwordx4 v[92:95], v1, s[4:7], s98 offen sc1\n"
"buffer_load_dwordx4 v[96:99], v1, s[4:7], s98 offen offset:1024 sc1\n"
"buffer_load_dwordx4 v[148:151], v1, s[4:7], s98 offen offset:2048 sc1\n"
"s_waitcnt vmcnt(0)\n"
"v_max_u32_e32 v255, v176, v177\n"
"v_max3_u32 v255, v255, v178, v179\n"
"v_max3_u32 v255, v255, v200, v201\n"
"v_max3_u32 v255, v255, v202, v203\n"
"v_max3_u32 v255, v255, v188, v189\n"
"v_max3_u32 v255, v255, v190, v191\n"
"v_max3_u32 v255, v255, v204, v205\n"
"v_max3_u32 v255, v255, v206, v207\n"
"v_max3_u32 v255, v255, v152, v153\n"
"v_max3_u32 v255, v255, v154, v155\n"
"v_max3_u32 v255, v255, v208, v209\n"
"v_max3_u32 v255, v255, v210, v211\n"
"v_max3_u32 v255, v255, v120, v121\n"
"v_max3_u32 v255, v255, v122, v123\n"
"v_max3_u32 v255, v255, v132, v133\n"
"v_max3_u32 v255, v255, v134, v135\n"
"v_cmp_eq_u32_e32 vcc, -1, v255\n"
"s_cbranch_vccz .Lmk_p30k\n"
"s_sub_u32 s1, s1, 1\n"
"s_cbranch_scc0 .Lmk_p30l\n"
".Lmk_p30k:\n"
"v_mfma_f32_16x16x32_bf16 v[88:91], a[128:131], v[176:179], 0\n"
"v_mfma_f32_16x16x32_bf16 v[88:91], a[132:135], v[200:203], v[88:91]\n"
"v_mfma_f32_16x16x32_bf16 v[88:91], a[136:139], v[188:191], v[88:91]\n"
"v_mfma_f32_16x16x32_bf16 v[88:91], a[140:143], v[204:207], v[88:91]\n"
"v_mfma_f32_16x16x32_bf16 v[88:91], a[144:147], v[152:155], v[88:91]\n"
"v_mfma_f32_16x16x32_bf16 v[88:91], a[148:151], v[208:211], v[88:91]\n"
"v_mfma_f32_16x16x32_bf16 v[88:91], a[152:155], v[120:123], v[88:91]\n"
"v_mfma_f32_16x16x32_bf16 v[88:91], a[156:159], v[132:135], v[88:91]\n"
"v_mfma_f32_16x16x32_bf16 v[124:127], a[188:191], v[132:135], 0\n"
"v_mfma_f32_16x16x32_bf16 v[124:127], a[184:187], v[120:123], v[124:127]\n"
"v_mfma_f32_16x16x32_bf16 v[124:127], a[180:183], v[208:211], v[124:127]\n"
"v_mfma_f32_16x16x32_bf16 v[124:127], a[176:179], v[152:155], v[124:127]\n"
"v_mfma_f32_16x16x32_bf16 v[124:127], a[172:175], v[204:207], v[124:127]\n"
"v_mfma_f32_16x16x32_bf16 v[124:127], a[168:171], v[188:191], v[124:127]\n"
"s_nop 7\n"
"s_branch .Lmk_p30r\n"
".Lmk_p31s:\n"
";\n"
"s_waitcnt vmcnt(16)\n"
"s_mov_b32 s1, 0x10000\n"
".Lmk_p31l:\n"
"s_add_u32 s97, s57, 0x40000\n"
"buffer_load_dwordx4 v[216:219], v0, s[4:7], s97 offen sc1\n"
"buffer_load_dwordx4 v[128:131], v0, s[4:7], s97 offen offset:1024 sc1\n"
"buffer_load_dwordx4 v[180:183], v0, s[4:7], s97 offen offset:2048 sc1\n"
"buffer_load_dwordx4 v[100:103], v0, s[4:7], s97 offen offset:3072 sc1\n"
"buffer_load_dwordx4 v[184:187], v1, s[4:7], s97 offen sc1\n"
"buffer_load_dwordx4 v[104:107], v1, s[4:7], s97 offen offset:1024 sc1\n"
"buffer_load_dwordx4 v[108:111], v1, s[4:7], s97 offen offset:2048 sc1\n"
"buffer_load_dwordx4 v[136:139], v1, s[4:7], s97 offen offset:3072 sc1\n"
"s_add_u32 s97, s58, 0x40000\n"
"buffer_load_dwordx4 v[144:147], v0, s[4:7], s97 offen sc1\n"
"buffer_load_dwordx4 v[140:143], v0, s[4:7], s97 offen offset:1024 sc1\n"
"buffer_load_dwordx4 v[116:119], v0, s[4:7], s97 offen offset:2048 sc1\n"
"buffer_load_dwordx4 v[112:115], v0, s[4:7], s97 offen offset:3072 sc1\n"
"buffer_load_dwordx4 v[92:95], v1, s[4:7], s97 offen sc1\n"
"buffer_load_dwordx4 v[96:99], v1, s[4:7], s97 offen offset:1024 sc1\n"
"buffer_load_dwordx4 v[148:151], v1, s[4:7], s97 offen offset:2048 sc1\n"
"buffer_load_dwordx4 v[88:91], v1, s[4:7], s97 offen offset:3072 sc1\n"
"s_add_u32 s97, s59, 0x40000\n"
"buffer_load_dwordx4 v[160:163], v0, s[4:7], s97 offen sc1\n"
"buffer_load_dwordx4 v[124:127], v0, s[4:7], s97 offen offset:1024 sc1\n"
"buffer_load_dwordx4 v[120:123], v0, s[4:7], s97 offen offset:2048 sc1\n"
"buffer_load_dwordx4 v[132:135], v0, s[4:7], s97 offen offset:3072 sc1\n"
"buffer_load_dwordx4 v[156:159], v1, s[4:7], s97 offen sc1\n"
"buffer_load_dwordx4 v[164:167], v1, s[4:7], s97 offen offset:1024 sc1\n"
"buffer_load_dwordx4 v[172:175], v1, s[4:7], s97 offen offset:2048 sc1\n"
"s_waitcnt vmcnt(0)\n"
"v_max_u32_e32 v255, v216, v217\n"
"v_max3_u32 v255, v255, v218, v219\n"
"v_max3_u32 v255, v255, v128, v129\n"
"v_max3_u32 v255, v255, v130, v131\n"
"v_max3_u32 v255, v255, v180, v181\n"
"v_max3_u32 v255, v255, v182, v183\n"
"v_max3_u32 v255, v255, v100, v101\n"
"v_max3_u32 v255, v255, v102, v103\n"
"v_max3_u32 v255, v255, v184, v185\n"
"v_max3_u32 v255, v255, v186, v187\n"
"v_max3_u32 v255, v255, v104, v105\n"
"v_max3_u32 v255, v255, v106, v107\n"
"v_max3_u32 v255, v255, v108, v109\n"
"v_max3_u32 v255, v255, v110, v111\n"
"v_max3_u32 v255, v255, v136, v137\n"
"v_max3_u32 v255, v255, v138, v139\n"
"v_cmp_eq_u32_e32 vcc, -1, v255\n"
"s_cbranch_vccz .Lmk_p31k\n"
"s_sub_u32 s1, s1, 1\n"
"s_cbranch_scc0 .Lmk_p31l\n"
".Lmk_p31k:\n"
"v_mfma_f32_16x16x32_bf16 v[152:155], a[128:131], v[216:219], 0\n"
"v_mfma_f32_16x16x32_bf16 v[152:155], a[132:135], v[128:131], v[152:155]\n"
"v_mfma_f32_16x16x32_bf16 v[152:155], a[136:139], v[180:183], v[152:155]\n"
"v_mfma_f32_16x16x32_bf16 v[152:155], a[140:143], v[100:103], v[152:155]\n"
"v_mfma_f32_16x16x32_bf16 v[152:155], a[144:147], v[184:187], v[152:155]\n"
"v_mfma_f32_16x16x32_bf16 v[152:155], a[148:151], v[104:107], v[152:155]\n"
"v_mfma_f32_16x16x32_bf16 v[152:155], a[152:155], v[108:111], v[152:155]\n"
"v_mfma_f32_16x16x32_bf16 v[152:155], a[156:159], v[136:139], v[152:155]\n"
"v_mfma_f32_16x16x32_bf16 v[168:171], a[188:191], v[136:139], 0\n"
"v_mfma_f32_16x16x32_bf16 v[168:171], a[184:187], v[108:111], v[168:171]\n"
"v_mfma_f32_16x16x32_bf16 v[168:171], a[180:183], v[104:107], v[168:171]\n"
"v_mfma_f32_16x16x32_bf16 v[168:171], a[176:179], v[184:187], v[168:171]\n"
"v_mfma_f32_16x16x32_bf16 v[168:171], a[172:175], v[100:103], v[168:171]\n"
"v_mfma_f32_16x16x32_bf16 v[168:171], a[168:171], v[180:183], v[168:171]\n"
"s_nop 7\n"
"s_branch .Lmk_p31r\n"
".Lmk_p32s:\n"
";\n"
"s_waitcnt vmcnt(16)\n"
"s_mov_b32 s1, 0x10000\n"
".Lmk_p32l:\n"
"s_add_u32 s97, s58, 0x40000\n"
"buffer_load_dwordx4 v[144:147], v0, s[4:7], s97 offen sc1\n"
"buffer_load_dwordx4 v[140:143], v0, s[4:7], s97 offen offset:1024 sc1\n"
"buffer_load_dwordx4 v[116:119], v0, s[4:7], s97 offen offset:2048 sc1\n"
"buffer_load_dwordx4 v[112:115], v0, s[4:7], s97 offen offset:3072 sc1\n"
"buffer_load_dwordx4 v[92:95], v1, s[4:7], s97 offen sc1\n"
"buffer_load_dwordx4 v[96:99], v1, s[4:7], s97 offen offset:1024 sc1\n"
"buffer_load_dwordx4 v[148:151], v1, s[4:7], s97 offen offset:2048 sc1\n"
"buffer_load_dwordx4 v[88:91], v1, s[4:7], s97 offen offset:3072 sc1\n"
"s_add_u32 s97, s59, 0x40000\n"
"buffer_load_dwordx4 v[160:163], v0, s[4:7], s97 offen sc1\n"
"buffer_load_dwordx4 v[124:127], v0, s[4:7], s97 offen offset:1024 sc1\n"
"buffer_load_dwordx4 v[120:123], v0, s[4:7], s97 offen offset:2048 sc1\n"
"buffer_load_dwordx4 v[132:135], v0, s[4:7], s97 offen offset:3072 sc1\n"
"buffer_load_dwordx4 v[156:159], v1, s[4:7], s97 offen sc1\n"
"buffer_load_dwordx4 v[164:167], v1, s[4:7], s97 offen offset:1024 sc1\n"
"buffer_load_dwordx4 v[172:175], v1, s[4:7], s97 offen offset:2048 sc1\n"
"buffer_load_dwordx4 v[152:155], v1, s[4:7], s97 offen offset:3072 sc1\n"
"s_waitcnt vmcnt(0)\n"
"v_max_u32_e32 v239, v144, v145\n"
"v_max3_u32 v239, v239, v146, v147\n"
"v_max3_u32 v239, v239, v140, v141\n"
"v_max3_u32 v239, v239, v142, v143\n"
"v_max3_u32 v239, v239, v116, v117\n"
"v_max3_u32 v239, v239, v118, v119\n"
"v_max3_u32 v239, v239, v112, v113\n"
"v_max3_u32 v239, v239, v114, v115\n"
"v_max3_u32 v239, v239, v92, v93\n"
"v_max3_u32 v239, v239, v94, v95\n"
"v_max3_u32 v239, v239, v96, v97\n"
"v_max3_u32 v239, v239, v98, v99\n"
"v_max3_u32 v239, v239, v148, v149\n"
"v_max3_u32 v239, v239, v150, v151\n"
"v_max3_u32 v239, v239, v88, v89\n"
"v_max3_u32 v239, v239, v90, v91\n"
"v_cmp_eq_u32_e32 vcc, -1, v239\n"
"s_cbranch_vccz .Lmk_p32k\n"
"s_sub_u32 s1, s1, 1\n"
"s_cbranch_scc0 .Lmk_p32l\n"
".Lmk_p32k:\n"
"v_mfma_f32_16x16x32_bf16 v[100:103], a[128:131], v[144:147], 0\n"
"v_mfma_f32_16x16x32_bf16 v[100:103], a[132:135], v[140:143], v[100:103]\n"
"v_mfma_f32_16x16x32_bf16 v[100:103], a[136:139], v[116:119], v[100:103]\n"
"v_mfma_f32_16x16x32_bf16 v[100:103], a[140:143], v[112:115], v[100:103]\n"
"v_mfma_f32_16x16x32_bf16 v[100:103], a[144:147], v[92:95], v[100:103]\n"
"v_mfma_f32_16x16x32_bf16 v[100:103], a[148:151], v[96:99], v[100:103]\n"
"v_mfma_f32_16x16x32_bf16 v[100:103], a[152:155], v[148:151], v[100:103]\n"
"v_mfma_f32_16x16x32_bf16 v[100:103], a[156:159], v[88:91], v[100:103]\n"
"v_mfma_f32_16x16x32_bf16 v[104:107], a[188:191], v[88:91], 0\n"
"v_mfma_f32_16x16x32_bf16 v[104:107], a[184:187], v[148:151], v[104:107]\n"
"v_mfma_f32_16x16x32_bf16 v[104:107], a[180:183], v[96:99], v[104:107]\n"
"v_mfma_f32_16x16x32_bf16 v[104:107], a[176:179], v[92:95], v[104:107]\n"
"v_mfma_f32_16x16x32_bf16 v[104:107], a[172:175], v[112:115], v[104:107]\n"
"v_mfma_f32_16x16x32_bf16 v[104:107], a[168:171], v[116:119], v[104:107]\n"
"s_nop 7\n"
"s_branch .Lmk_p32r\n"
".Lmk_p33s:\n"
";\n"
"s_waitcnt vmcnt(16)\n"
"s_mov_b32 s1, 0x10000\n"
".Lmk_p33l:\n"
"s_add_u32 s97, s59, 0x40000\n"
"buffer_load_dwordx4 v[160:163], v0, s[4:7], s97 offen sc1\n"
"buffer_load_dwordx4 v[124:127], v0, s[4:7], s97 offen offset:1024 sc1\n"
"buffer_load_dwordx4 v[120:123], v0, s[4:7], s97 offen offset:2048 sc1\n"
"buffer_load_dwordx4 v[132:135], v0, s[4:7], s97 offen offset:3072 sc1\n"
"buffer_load_dwordx4 v[156:159], v1, s[4:7], s97 offen sc1\n"
"buffer_load_dwordx4 v[164:167], v1, s[4:7], s97 offen offset:1024 sc1\n"
"buffer_load_dwordx4 v[172:175], v1, s[4:7], s97 offen offset:2048 sc1\n"
"buffer_load_dwordx4 v[152:155], v1, s[4:7], s97 offen offset:3072 sc1\n"
"s_add_u32 s97, s56, 0\n"
"buffer_load_dwordx4 v[100:103], v0, s[4:7], s97 offen sc1\n"
"buffer_load_dwordx4 v[180:183], v0, s[4:7], s97 offen offset:1024 sc1\n"
"buffer_load_dwordx4 v[104:107], v0, s[4:7], s97 offen offset:2048 sc1\n"
"buffer_load_dwordx4 v[88:91], v0, s[4:7], s97 offen offset:3072 sc1\n"
"buffer_load_dwordx4 v[92:95], v1, s[4:7], s97 offen sc1\n"
"buffer_load_dwordx4 v[116:119], v1, s[4:7], s97 offen offset:1024 sc1\n"
"buffer_load_dwordx4 v[136:139], v1, s[4:7], s97 offen offset:2048 sc1\n"
"buffer_load_dwordx4 v[140:143], v1, s[4:7], s97 offen offset:3072 sc1\n"
"s_add_u32 s97, s57, 0\n"
"buffer_load_dwordx4 v[148:151], v0, s[4:7], s97 offen sc1\n"
"s_waitcnt vmcnt(0)\n"
"v_max_u32_e32 v255, v160, v161\n"
"v_max3_u32 v255, v255, v162, v163\n"
"v_max3_u32 v255, v255, v124, v125\n"
"v_max3_u32 v255, v255, v126, v127\n"
"v_max3_u32 v255, v255, v120, v121\n"
"v_max3_u32 v255, v255, v122, v123\n"
"v_max3_u32 v255, v255, v132, v133\n"
"v_max3_u32 v255, v255, v134, v135\n"
"v_max3_u32 v255, v255, v156, v157\n"
"v_max3_u32 v255, v255, v158, v159\n"
"v_max3_u32 v255, v255, v164, v165\n"
"v_max3_u32 v255, v255, v166, v167\n"
"v_max3_u32 v255, v255, v172, v173\n"
"v_max3_u32 v255, v255, v174, v175\n"
"v_max3_u32 v255, v255, v152, v153\n"
"v_max3_u32 v255, v255, v154, v155\n"
"v_cmp_eq_u32_e32 vcc, -1, v255\n"
"s_cbranch_vccz .Lmk_p33k\n"
"s_sub_u32 s1, s1, 1\n"
"s_cbranch_scc0 .Lmk_p33l\n"
".Lmk_p33k:\n"
"v_mfma_f32_16x16x32_bf16 v[128:131], a[128:131], v[160:163], v[108:111]\n"
"v_mfma_f32_16x16x32_bf16 v[128:131], a[132:135], v[124:127], v[128:131]\n"
"v_mfma_f32_16x16x32_bf16 v[128:131], a[136:139], v[120:123], v[128:131]\n"
"v_mfma_f32_16x16x32_bf16 v[128:131], a[140:143], v[132:135], v[128:131]\n"
"v_mfma_f32_16x16x32_bf16 v[128:131], a[144:147], v[156:159], v[128:131]\n"
"v_mfma_f32_16x16x32_bf16 v[128:131], a[148:151], v[164:167], v[128:131]\n"
"v_mfma_f32_16x16x32_bf16 v[128:131], a[152:155], v[172:175], v[128:131]\n"
"v_mfma_f32_16x16x32_bf16 v[128:131], a[156:159], v[152:155], v[128:131]\n"
"v_mfma_f32_16x16x32_bf16 v[144:147], a[188:191], v[152:155], v[168:171]\n"
"v_mfma_f32_16x16x32_bf16 v[144:147], a[184:187], v[172:175], v[144:147]\n"
"v_mfma_f32_16x16x32_bf16 v[144:147], a[180:183], v[164:167], v[144:147]\n"
"v_mfma_f32_16x16x32_bf16 v[144:147], a[176:179], v[156:159], v[144:147]\n"
"v_mfma_f32_16x16x32_bf16 v[144:147], a[172:175], v[132:135], v[144:147]\n"
"v_mfma_f32_16x16x32_bf16 v[144:147], a[168:171], v[120:123], v[144:147]\n"
"s_nop 7\n"
"s_branch .Lmk_p33r\n"
".Lmk_p35s:\n"
";\n"
"s_waitcnt vmcnt(16)\n"
"s_mov_b32 s1, 0x10000\n"
".Lmk_p35l:\n"
"s_add_u32 s95, s56, 0\n"
"buffer_load_dwordx4 v[100:103], v0, s[4:7], s95 offen sc1\n"
"buffer_load_dwordx4 v[180:183], v0, s[4:7], s95 offen offset:1024 sc1\n"
"buffer_load_dwordx4 v[104:107], v0, s[4:7], s95 offen offset:2048 sc1\n"
"buffer_load_dwordx4 v[88:91], v0, s[4:7], s95 offen offset:3072 sc1\n"
"buffer_load_dwordx4 v[92:95], v1, s[4:7], s95 offen sc1\n"
"buffer_load_dwordx4 v[116:119], v1, s[4:7], s95 offen offset:1024 sc1\n"
"buffer_load_dwordx4 v[136:139], v1, s[4:7], s95 offen offset:2048 sc1\n"
"buffer_load_dwordx4 v[140:143], v1, s[4:7], s95 offen offset:3072 sc1\n"
"s_add_u32 s95, s57, 0\n"
"buffer_load_dwordx4 v[148:151], v0, s[4:7], s95 offen sc1\n"
"buffer_load_dwordx4 v[108:111], v0, s[4:7], s95 offen offset:1024 sc1\n"
"buffer_load_dwordx4 v[112:115], v0, s[4:7], s95 offen offset:2048 sc1\n"
"buffer_load_dwordx4 v[196:199], v0, s[4:7], s95 offen offset:3072 sc1\n"
"buffer_load_dwordx4 v[200:203], v1, s[4:7], s95 offen sc1\n"
"buffer_load_dwordx4 v[128:131], v1, s[4:7], s95 offen offset:1024 sc1\n"
"buffer_load_dwordx4 v[120:123], v1, s[4:7], s95 offen offset:2048 sc1\n"
"buffer_load_dwordx4 v[132:135], v1, s[4:7], s95 offen offset:3072 sc1\n"
"s_add_u32 s95, s58, 0\n"
"buffer_load_dwordx4 v[152:155], v0, s[4:7], s95 offen sc1\n"
"buffer_load_dwordx4 v[160:163], v0, s[4:7], s95 offen offset:1024 sc1\n"
"buffer_load_dwordx4 v[124:127], v0, s[4:7], s95 offen offset:2048 sc1\n"
"s_waitcnt vmcnt(0)\n"
"v_max_u32_e32 v255, v100, v101\n"
"v_max3_u32 v255, v255, v102, v103\n"
"v_max3_u32 v255, v255, v180, v181\n"
"v_max3_u32 v255, v255, v182, v183\n"
"v_max3_u32 v255, v255, v104, v105\n"
"v_max3_u32 v255, v255, v106, v107\n"
"v_max3_u32 v255, v255, v88, v89\n"
"v_max3_u32 v255, v255, v90, v91\n"
"v_max3_u32 v255, v255, v92, v93\n"
"v_max3_u32 v255, v255, v94, v95\n"
"v_max3_u32 v255, v255, v116, v117\n"
"v_max3_u32 v255, v255, v118, v119\n"
"v_max3_u32 v255, v255, v136, v137\n"
"v_max3_u32 v255, v255, v138, v139\n"
"v_max3_u32 v255, v255, v140, v141\n"
"v_max3_u32 v255, v255, v142, v143\n"
"v_cmp_eq_u32_e32 vcc, -1, v255\n"
"s_cbranch_vccz .Lmk_p35k\n"
"s_sub_u32 s1, s1, 1\n"
"s_cbranch_scc0 .Lmk_p35l\n"
".Lmk_p35k:\n"
"v_mfma_f32_16x16x32_bf16 v[144:147], a[128:131], v[100:103], 0\n"
"v_mfma_f32_16x16x32_bf16 v[144:147], a[132:135], v[180:183], v[144:147]\n"
"v_mfma_f32_16x16x32_bf16 v[144:147], a[136:139], v[104:107], v[144:147]\n"
"v_mfma_f32_16x16x32_bf16 v[144:147], a[140:143], v[88:91], v[144:147]\n"
"v_mfma_f32_16x16x32_bf16 v[144:147], a[144:147], v[92:95], v[144:147]\n"
"v_mfma_f32_16x16x32_bf16 v[144:147], a[148:151], v[116:119], v[144:147]\n"
"v_mfma_f32_16x16x32_bf16 v[144:147], a[152:155], v[136:139], v[144:147]\n"
"v_mfma_f32_16x16x32_bf16 v[144:147], a[156:159], v[140:143], v[144:147]\n"
"v_mfma_f32_16x16x32_bf16 v[96:99], a[188:191], v[140:143], 0\n"
"v_mfma_f32_16x16x32_bf16 v[96:99], a[184:187], v[136:139], v[96:99]\n"
"v_mfma_f32_16x16x32_bf16 v[96:99], a[180:183], v[116:119], v[96:99]\n"
"v_mfma_f32_16x16x32_bf16 v[96:99], a[176:179], v[92:95], v[96:99]\n"
"v_mfma_f32_16x16x32_bf16 v[96:99], a[172:175], v[88:91], v[96:99]\n"
"v_mfma_f32_16x16x32_bf16 v[96:99], a[168:171], v[104:107], v[96:99]\n"
"s_nop 7\n"
"s_branch .Lmk_p35r\n"
".Lmk_p36s:\n"
";\n"
"s_waitcnt vmcnt(16)\n"
"s_mov_b32 s1, 0x10000\n"
".Lmk_p36l:\n"
"s_add_u32 s97, s57, 0\n"
"buffer_load_dwordx4 v[148:151], v0, s[4:7], s97 offen sc1\n"
"buffer_load_dwordx4 v[108:111], v0, s[4:7], s97 offen offset:1024 sc1\n"
"buffer_load_dwordx4 v[112:115], v0, s[4:7], s97 offen offset:2048 sc1\n"
"buffer_load_dwordx4 v[196:199], v0, s[4:7], s97 offen offset:3072 sc1\n"
"buffer_load_dwordx4 v[200:203], v1, s[4:7], s97 offen sc1\n"
"buffer_load_dwordx4 v[128:131], v1, s[4:7], s97 offen offset:1024 sc1\n"
"buffer_load_dwordx4 v[120:123], v1, s[4:7], s97 offen offset:2048 sc1\n"
"buffer_load_dwordx4 v[132:135], v1, s[4:7], s97 offen offset:3072 sc1\n"
"s_add_u32 s97, s58, 0\n"
"buffer_load_dwordx4 v[152:155], v0, s[4:7], s97 offen sc1\n"
"buffer_load_dwordx4 v[160:163], v0, s[4:7], s97 offen offset:1024 sc1\n"
"buffer_load_dwordx4 v[124:127], v0, s[4:7], s97 offen offset:2048 sc1\n"
"buffer_load_dwordx4 v[144:147], v0, s[4:7], s97 offen offset:3072 sc1\n"
"buffer_load_dwordx4 v[156:159], v1, s[4:7], s97 offen sc1\n"
"buffer_load_dwordx4 v[164:167], v1, s[4:7], s97 offen offset:1024 sc1\n"
"buffer_load_dwordx4 v[116:119], v1, s[4:7], s97 offen offset:2048 sc1\n"
"buffer_load_dwordx4 v[136:139], v1, s[4:7], s97 offen offset:3072 sc1\n"
"s_add_u32 s97, s59, 0\n"
"buffer_load_dwordx4 v[172:175], v0, s[4:7], s97 offen sc1\n"
"buffer_load_dwordx4 v[176:179], v0, s[4:7], s97 offen offset:1024 sc1\n"
"buffer_load_dwordx4 v[168:171], v0, s[4:7], s97 offen offset:2048 sc1\n"
"s_waitcnt vmcnt(0)\n"
"v_max_u32_e32 v255, v148, v149\n"
"v_max3_u32 v255, v255, v150, v151\n"
"v_max3_u32 v255, v255, v108, v109\n"
"v_max3_u32 v255, v255, v110, v111\n"
"v_max3_u32 v255, v255, v112, v113\n"
"v_max3_u32 v255, v255, v114, v115\n"
"v_max3_u32 v255, v255, v196, v197\n"
"v_max3_u32 v255, v255, v198, v199\n"
"v_max3_u32 v255, v255, v200, v201\n"
"v_max3_u32 v255, v255, v202, v203\n"
"v_max3_u32 v255, v255, v128, v129\n"
"v_max3_u32 v255, v255, v130, v131\n"
"v_max3_u32 v255, v255, v120, v121\n"
"v_max3_u32 v255, v255, v122, v123\n"
"v_max3_u32 v255, v255, v132, v133\n"
"v_max3_u32 v255, v255, v134, v135\n"
"v_cmp_eq_u32_e32 vcc, -1, v255\n"
"s_cbranch_vccz .Lmk_p36k\n"
"s_sub_u32 s1, s1, 1\n"
"s_cbranch_scc0 .Lmk_p36l\n"
".Lmk_p36k:\n"
"v_mfma_f32_16x16x32_bf16 v[88:91], a[128:131], v[148:151], 0\n"
"v_mfma_f32_16x16x32_bf16 v[88:91], a[132:135], v[108:111], v[88:91]\n"
"v_mfma_f32_16x16x32_bf16 v[88:91], a[136:139], v[112:115], v[88:91]\n"
"v_mfma_f32_16x16x32_bf16 v[88:91], a[140:143], v[196:199], v[88:91]\n"
"v_mfma_f32_16x16x32_bf16 v[88:91], a[144:147], v[200:203], v[88:91]\n"
"v_mfma_f32_16x16x32_bf16 v[88:91], a[148:151], v[128:131], v[88:91]\n"
"v_mfma_f32_16x16x32_bf16 v[88:91], a[152:155], v[120:123], v[88:91]\n"
"v_mfma_f32_16x16x32_bf16 v[88:91], a[156:159], v[132:135], v[88:91]\n"
"v_mfma_f32_16x16x32_bf16 v[92:95], a[188:191], v[132:135], 0\n"
"v_mfma_f32_16x16x32_bf16 v[92:95], a[184:187], v[120:123], v[92:95]\n"
"v_mfma_f32_16x16x32_bf16 v[92:95], a[180:183], v[128:131], v[92:95]\n"
"v_mfma_f32_16x16x32_bf16 v[92:95], a[176:179], v[200:203], v[92:95]\n"
"v_mfma_f32_16x16x32_bf16 v[92:95], a[172:175], v[196:199], v[92:95]\n"
"v_mfma_f32_16x16x32_bf16 v[92:95], a[168:171], v[112:115], v[92:95]\n"
"s_nop 7\n"
"s_branch .Lmk_p36r\n"
".Lmk_p37s:\n"
";\n"
"s_waitcnt vmcnt(16)\n"
"s_mov_b32 s1, 0x10000\n"
".Lmk_p37l:\n"
"s_add_u32 s97, s58, 0\n"
"buffer_load_dwordx4 v[152:155], v0, s[4:7], s97 offen sc1\n"
"buffer_load_dwordx4 v[160:163], v0, s[4:7], s97 offen offset:1024 sc1\n"
"buffer_load_dwordx4 v[124:127], v0, s[4:7], s97 offen offset:2048 sc1\n"
"buffer_load_dwordx4 v[144:147], v0, s[4:7], s97 offen offset:3072 sc1\n"
"buffer_load_dwordx4 v[156:159], v1, s[4:7], s97 offen sc1\n"
"buffer_load_dwordx4 v[164:167], v1, s[4:7], s97 offen offset:1024 sc1\n"
"buffer_load_dwordx4 v[116:119], v1, s[4:7], s97 offen offset:2048 sc1\n"
"buffer_load_dwordx4 v[136:139], v1, s[4:7], s97 offen offset:3072 sc1\n"
"s_add_u32 s97, s59, 0\n"
"buffer_load_dwordx4 v[172:175], v0, s[4:7], s97 offen sc1\n"
"buffer_load_dwordx4 v[176:179], v0, s[4:7], s97 offen offset:1024 sc1\n"
"buffer_load_dwordx4 v[168:171], v0, s[4:7], s97 offen offset:2048 sc1\n"
"buffer_load_dwordx4 v[180:183], v0, s[4:7], s97 offen offset:3072 sc1\n"
"buffer_load_dwordx4 v[184:187], v1, s[4:7], s97 offen sc1\n"
"buffer_load_dwordx4 v[188:191], v1, s[4:7], s97 offen offset:1024 sc1\n"
"buffer_load_dwordx4 v[192:195], v1, s[4:7], s97 offen offset:2048 sc1\n"
"buffer_load_dwordx4 v[196:199], v1, s[4:7], s97 offen offset:3072 sc1\n"
"s_add_u32 s97, s52, 0x440000\n"
"buffer_load_dwordx4 v[88:91], v0, s[4:7], s97 offen sc1\n"
"buffer_load_dwordx4 v[92:95], v0, s[4:7], s97 offen offset:1024 sc1\n"
"s_waitcnt vmcnt(0)\n"
"v_max_u32_e32 v239, v152, v153\n"
"v_max3_u32 v239, v239, v154, v155\n"
"v_max3_u32 v239, v239, v160, v161\n"
"v_max3_u32 v239, v239, v162, v163\n"
"v_max3_u32 v239, v239, v124, v125\n"
"v_max3_u32 v239, v239, v126, v127\n"
"v_max3_u32 v239, v239, v144, v145\n"
"v_max3_u32 v239, v239, v146, v147\n"
"v_max3_u32 v239, v239, v156, v157\n"
"v_max3_u32 v239, v239, v158, v159\n"
"v_max3_u32 v239, v239, v164, v165\n"
"v_max3_u32 v239, v239, v166, v167\n"
"v_max3_u32 v239, v239, v116, v117\n"
"v_max3_u32 v239, v239, v118, v119\n"
"v_max3_u32 v239, v239, v136, v137\n"
"v_max3_u32 v239, v239, v138, v139\n"
"v_cmp_eq_u32_e32 vcc, -1, v239\n"
"s_cbranch_vccz .Lmk_p37k\n"
"s_sub_u32 s1, s1, 1\n"
"s_cbranch_scc0 .Lmk_p37l\n"
".Lmk_p37k:\n"
"v_mfma_f32_16x16x32_bf16 v[96:99], a[128:131], v[152:155], 0\n"
"v_mfma_f32_16x16x32_bf16 v[96:99], a[132:135], v[160:163], v[96:99]\n"
"v_mfma_f32_16x16x32_bf16 v[96:99], a[136:139], v[124:127], v[96:99]\n"
"v_mfma_f32_16x16x32_bf16 v[96:99], a[140:143], v[144:147], v[96:99]\n"
"v_mfma_f32_16x16x32_bf16 v[96:99], a[144:147], v[156:159], v[96:99]\n"
"v_mfma_f32_16x16x32_bf16 v[96:99], a[148:151], v[164:167], v[96:99]\n"
"v_mfma_f32_16x16x32_bf16 v[96:99], a[152:155], v[116:119], v[96:99]\n"
"v_mfma_f32_16x16x32_bf16 v[96:99], a[156:159], v[136:139], v[96:99]\n"
"v_mfma_f32_16x16x32_bf16 v[104:107], a[188:191], v[136:139], 0\n"
"v_mfma_f32_16x16x32_bf16 v[104:107], a[184:187], v[116:119], v[104:107]\n"
"v_mfma_f32_16x16x32_bf16 v[104:107], a[180:183], v[164:167], v[104:107]\n"
"v_mfma_f32_16x16x32_bf16 v[104:107], a[176:179], v[156:159], v[104:107]\n"
"v_mfma_f32_16x16x32_bf16 v[104:107], a[172:175], v[144:147], v[104:107]\n"
"v_mfma_f32_16x16x32_bf16 v[104:107], a[168:171], v[124:127], v[104:107]\n"
"s_nop 7\n"
"s_branch .Lmk_p37r\n"
".Lmk_p38s:\n"
";\n"
"s_waitcnt vmcnt(16)\n"
"s_mov_b32 s1, 0x10000\n"
".Lmk_p38l:\n"
"s_add_u32 s97, s59, 0\n"
"buffer_load_dwordx4 v[172:175], v0, s[4:7], s97 offen sc1\n"
"buffer_load_dwordx4 v[176:179], v0, s[4:7], s97 offen offset:1024 sc1\n"
"buffer_load_dwordx4 v[168:171], v0, s[4:7], s97 offen offset:2048 sc1\n"
"buffer_load_dwordx4 v[180:183], v0, s[4:7], s97 offen offset:3072 sc1\n"
"buffer_load_dwordx4 v[184:187], v1, s[4:7], s97 offen sc1\n"
"buffer_load_dwordx4 v[188:191], v1, s[4:7], s97 offen offset:1024 sc1\n"
"buffer_load_dwordx4 v[192:195], v1, s[4:7], s97 offen offset:2048 sc1\n"
"buffer_load_dwordx4 v[196:199], v1, s[4:7], s97 offen offset:3072 sc1\n"
"s_add_u32 s97, s52, 0x440000\n"
"buffer_load_dwordx4 v[88:91], v0, s[4:7], s97 offen sc1\n"
"buffer_load_dwordx4 v[92:95], v0, s[4:7], s97 offen offset:1024 sc1\n"
"buffer_load_dwordx4 v[96:99], v0, s[4:7], s97 offen offset:2048 sc1\n"
"buffer_load_dwordx4 v[100:103], v0, s[4:7], s97 offen offset:3072 sc1\n"
"buffer_load_dwordx4 v[104:107], v1, s[4:7], s97 offen sc1\n"
"buffer_load_dwordx4 v[112:115], v1, s[4:7], s97 offen offset:1024 sc1\n"
"buffer_load_dwordx4 v[116:119], v1, s[4:7], s97 offen offset:2048 sc1\n"
"buffer_load_dwordx4 v[124:127], v1, s[4:7], s97 offen offset:3072 sc1\n"
"s_add_u32 s97, s53, 0x440000\n"
"buffer_load_dwordx4 v[128:131], v0, s[4:7], s97 offen sc1\n"
"buffer_load_dwordx4 v[132:135], v0, s[4:7], s97 offen offset:1024 sc1\n"
"buffer_load_dwordx4 v[136:139], v0, s[4:7], s97 offen offset:2048 sc1\n"
"s_waitcnt vmcnt(0)\n"
"v_max_u32_e32 v255, v172, v173\n"
"v_max3_u32 v255, v255, v174, v175\n"
"v_max3_u32 v255, v255, v176, v177\n"
"v_max3_u32 v255, v255, v178, v179\n"
"v_max3_u32 v255, v255, v168, v169\n"
"v_max3_u32 v255, v255, v170, v171\n"
"v_max3_u32 v255, v255, v180, v181\n"
"v_max3_u32 v255, v255, v182, v183\n"
"v_max3_u32 v255, v255, v184, v185\n"
"v_max3_u32 v255, v255, v186, v187\n"
"v_max3_u32 v255, v255, v188, v189\n"
"v_max3_u32 v255, v255, v190, v191\n"
"v_max3_u32 v255, v255, v192, v193\n"
"v_max3_u32 v255, v255, v194, v195\n"
"v_max3_u32 v255, v255, v196, v197\n"
"v_max3_u32 v255, v255, v198, v199\n"
"v_cmp_eq_u32_e32 vcc, -1, v255\n"
"s_cbranch_vccz .Lmk_p38k\n"
"s_sub_u32 s1, s1, 1\n"
"s_cbranch_scc0 .Lmk_p38l\n"
".Lmk_p38k:\n"
"v_mfma_f32_16x16x32_bf16 v[164:167], a[128:131], v[172:175], v[140:143]\n"
"v_mfma_f32_16x16x32_bf16 v[164:167], a[132:135], v[176:179], v[164:167]\n"
"v_mfma_f32_16x16x32_bf16 v[164:167], a[136:139], v[168:171], v[164:167]\n"
"v_mfma_f32_16x16x32_bf16 v[164:167], a[140:143], v[180:183], v[164:167]\n"
"v_mfma_f32_16x16x32_bf16 v[164:167], a[144:147], v[184:187], v[164:167]\n"
"v_mfma_f32_16x16x32_bf16 v[164:167], a[148:151], v[188:191], v[164:167]\n"
"v_mfma_f32_16x16x32_bf16 v[164:167], a[152:155], v[192:195], v[164:167]\n"
"v_mfma_f32_16x16x32_bf16 v[164:167], a[156:159], v[196:199], v[164:167]\n"
"v_mfma_f32_16x16x32_bf16 v[204:207], a[188:191], v[196:199], v[148:151]\n"
"v_mfma_f32_16x16x32_bf16 v[204:207], a[184:187], v[192:195], v[204:207]\n"
"v_mfma_f32_16x16x32_bf16 v[204:207], a[180:183], v[188:191], v[204:207]\n"
"v_mfma_f32_16x16x32_bf16 v[204:207], a[176:179], v[184:187], v[204:207]\n"
"v_mfma_f32_16x16x32_bf16 v[204:207], a[172:175], v[180:183], v[204:207]\n"
"v_mfma_f32_16x16x32_bf16 v[204:207], a[168:171], v[168:171], v[204:207]\n"
"s_nop 7\n"
"s_branch .Lmk_p38r\n"
".Lmk_p43s:\n"
";\n"
"s_waitcnt vmcnt(16)\n"
"s_mov_b32 s1, 0x10000\n"
".Lmk_p43l:\n"
"s_add_u32 s96, s52, 0x440000\n"
"buffer_load_dwordx4 v[88:91], v0, s[4:7], s96 offen sc1\n"
"buffer_load_dwordx4 v[92:95], v0, s[4:7], s96 offen offset:1024 sc1\n"
"buffer_load_dwordx4 v[96:99], v0, s[4:7], s96 offen offset:2048 sc1\n"
"buffer_load_dwordx4 v[100:103], v0, s[4:7], s96 offen offset:3072 sc1\n"
"buffer_load_dwordx4 v[104:107], v1, s[4:7], s96 offen sc1\n"
"buffer_load_dwordx4 v[112:115], v1, s[4:7], s96 offen offset:1024 sc1\n"
"buffer_load_dwordx4 v[116:119], v1, s[4:7], s96 offen offset:2048 sc1\n"
"buffer_load_dwordx4 v[124:127], v1, s[4:7], s96 offen offset:3072 sc1\n"
"s_add_u32 s96, s53, 0x440000\n"
"buffer_load_dwordx4 v[128:131], v0, s[4:7], s96 offen sc1\n"
"buffer_load_dwordx4 v[132:135], v0, s[4:7], s96 offen offset:1024 sc1\n"
"buffer_load_dwordx4 v[136:139], v0, s[4:7], s96 offen offset:2048 sc1\n"
"buffer_load_dwordx4 v[140:143], v0, s[4:7], s96 offen offset:3072 sc1\n"
"buffer_load_dwordx4 v[144:147], v1, s[4:7], s96 offen sc1\n"
"buffer_load_dwordx4 v[156:159], v1, s[4:7], s96 offen offset:1024 sc1\n"
"buffer_load_dwordx4 v[160:163], v1, s[4:7], s96 offen offset:2048 sc1\n"
"buffer_load_dwordx4 v[164:167], v1, s[4:7], s96 offen offset:3072 sc1\n"
"s_add_u32 s96, s54, 0x440000\n"
"buffer_load_dwordx4 v[176:179], v0, s[4:7], s96 offen sc1\n"
"buffer_load_dwordx4 v[108:111], v0, s[4:7], s96 offen offset:1024 sc1\n"
"s_waitcnt vmcnt(0)\n"
"v_max_u32_e32 v252, v88, v89\n"
"v_max3_u32 v252, v252, v90, v91\n"
"v_max3_u32 v252, v252, v92, v93\n"
"v_max3_u32 v252, v252, v94, v95\n"
"v_max3_u32 v252, v252, v96, v97\n"
"v_max3_u32 v252, v252, v98, v99\n"
"v_max3_u32 v252, v252, v100, v101\n"
"v_max3_u32 v252, v252, v102, v103\n"
"v_max3_u32 v252, v252, v104, v105\n"
"v_max3_u32 v252, v252, v106, v107\n"
"v_max3_u32 v252, v252, v112, v113\n"
"v_max3_u32 v252, v252, v114, v115\n"
"v_max3_u32 v252, v252, v116, v117\n"
"v_max3_u32 v252, v252, v118, v119\n"
"v_max3_u32 v252, v252, v124, v125\n"
"v_max3_u32 v252, v252, v126, v127\n"
"v_cmp_eq_u32_e32 vcc, -1, v252\n"
"s_cbranch_vccz .Lmk_p43k\n"
"s_sub_u32 s1, s1, 1\n"
"s_cbranch_scc0 .Lmk_p43l\n"
".Lmk_p43k:\n"
"v_mfma_f32_16x16x32_bf16 v[172:175], a[0:3], v[88:91], 0\n"
"v_mfma_f32_16x16x32_bf16 v[172:175], a[4:7], v[92:95], v[172:175]\n"
"v_mfma_f32_16x16x32_bf16 v[172:175], a[8:11], v[96:99], v[172:175]\n"
"v_mfma_f32_16x16x32_bf16 v[172:175], a[12:15], v[100:103], v[172:175]\n"
"v_mfma_f32_16x16x32_bf16 v[172:175], a[16:19], v[104:107], v[172:175]\n"
"v_mfma_f32_16x16x32_bf16 v[172:175], a[20:23], v[112:115], v[172:175]\n"
"v_mfma_f32_16x16x32_bf16 v[172:175], a[24:27], v[116:119], v[172:175]\n"
"v_mfma_f32_16x16x32_bf16 v[172:175], a[28:31], v[124:127], v[172:175]\n"
"v_mfma_f32_16x16x32_bf16 v[180:183], a[60:63], v[124:127], 0\n"
"v_mfma_f32_16x16x32_bf16 v[180:183], a[56:59], v[116:119], v[180:183]\n"
"v_mfma_f32_16x16x32_bf16 v[180:183], a[52:55], v[112:115], v[180:183]\n"
"v_mfma_f32_16x16x32_bf16 v[180:183], a[48:51], v[104:107], v[180:183]\n"
"v_mfma_f32_16x16x32_bf16 v[180:183], a[44:47], v[100:103], v[180:183]\n"
"v_mfma_f32_16x16x32_bf16 v[180:183], a[40:43], v[96:99], v[180:183]\n"
"s_nop 7\n"
"s_branch .Lmk_p43r\n"
".Lmk_p44s:\n"
";\n"
"s_waitcnt vmcnt(16)\n"
"s_mov_b32 s1, 0x10000\n"
".Lmk_p44l:\n"
"s_add_u32 s96, s53, 0x440000\n"
"buffer_load_dwordx4 v[128:131], v0, s[4:7], s96 offen sc1\n"
"buffer_load_dwordx4 v[132:135], v0, s[4:7], s96 offen offset:1024 sc1\n"
"buffer_load_dwordx4 v[136:139], v0, s[4:7], s96 offen offset:2048 sc1\n"
"buffer_load_dwordx4 v[140:143], v0, s[4:7], s96 offen offset:3072 sc1\n"
"buffer_load_dwordx4 v[144:147], v1, s[4:7], s96 offen sc1\n"
"buffer_load_dwordx4 v[156:159], v1, s[4:7], s96 offen offset:1024 sc1\n"
"buffer_load_dwordx4 v[160:163], v1, s[4:7], s96 offen offset:2048 sc1\n"
"buffer_load_dwordx4 v[164:167], v1, s[4:7], s96 offen offset:3072 sc1\n"
"s_add_u32 s96, s54, 0x440000\n"
"buffer_load_dwordx4 v[176:179], v0, s[4:7], s96 offen sc1\n"
"buffer_load_dwordx4 v[108:111], v0, s[4:7], s96 offen offset:1024 sc1\n"
"buffer_load_dwordx4 v[148:151], v0, s[4:7], s96 offen offset:2048 sc1\n"
"buffer_load_dwordx4 v[184:187], v0, s[4:7], s96 offen offset:3072 sc1\n"
"buffer_load_dwordx4 v[152:155], v1, s[4:7], s96 offen sc1\n"
"buffer_load_dwordx4 v[192:195], v1, s[4:7], s96 offen offset:1024 sc1\n"
"buffer_load_dwordx4 v[196:199], v1, s[4:7], s96 offen offset:2048 sc1\n"
"buffer_load_dwordx4 v[200:203], v1, s[4:7], s96 offen offset:3072 sc1\n"
"s_add_u32 s96, s55, 0x440000\n"
"buffer_load_dwordx4 v[92:95], v0, s[4:7], s96 offen sc1\n"
"buffer_load_dwordx4 v[96:99], v0, s[4:7], s96 offen offset:1024 sc1\n"
"s_waitcnt vmcnt(0)\n"
"v_max_u32_e32 v247, v128, v129\n"
"v_max3_u32 v247, v247, v130, v131\n"
"v_max3_u32 v247, v247, v132, v133\n"
"v_max3_u32 v247, v247, v134, v135\n"
"v_max3_u32 v247, v247, v136, v137\n"
"v_max3_u32 v247, v247, v138, v139\n"
"v_max3_u32 v247, v247, v140, v141\n"
"v_max3_u32 v247, v247, v142, v143\n"
"v_max3_u32 v247, v247, v144, v145\n"
"v_max3_u32 v247, v247, v146, v147\n"
"v_max3_u32 v247, v247, v156, v157\n"
"v_max3_u32 v247, v247, v158, v159\n"
"v_max3_u32 v247, v247, v160, v161\n"
"v_max3_u32 v247, v247, v162, v163\n"
"v_max3_u32 v247, v247, v164, v165\n"
"v_max3_u32 v247, v247, v166, v167\n"
"v_cmp_eq_u32_e32 vcc, -1, v247\n"
"s_cbranch_vccz .Lmk_p44k\n"
"s_sub_u32 s1, s1, 1\n"
"s_cbranch_scc0 .Lmk_p44l\n"
".Lmk_p44k:\n"
"v_mfma_f32_16x16x32_bf16 v[88:91], a[0:3], v[128:131], 0\n"
"v_mfma_f32_16x16x32_bf16 v[88:91], a[4:7], v[132:135], v[88:91]\n"
"v_mfma_f32_16x16x32_bf16 v[88:91], a[8:11], v[136:139], v[88:91]\n"
"v_mfma_f32_16x16x32_bf16 v[88:91], a[12:15], v[140:143], v[88:91]\n"
"v_mfma_f32_16x16x32_bf16 v[88:91], a[16:19], v[144:147], v[88:91]\n"
"v_mfma_f32_16x16x32_bf16 v[88:91], a[20:23], v[156:159], v[88:91]\n"
"v_mfma_f32_16x16x32_bf16 v[88:91], a[24:27], v[160:163], v[88:91]\n"
"v_mfma_f32_16x16x32_bf16 v[88:91], a[28:31], v[164:167], v[88:91]\n"
"v_mfma_f32_16x16x32_bf16 v[100:103], a[60:63], v[164:167], 0\n"
"v_mfma_f32_16x16x32_bf16 v[100:103], a[56:59], v[160:163], v[100:103]\n"
"v_mfma_f32_16x16x32_bf16 v[100:103], a[52:55], v[156:159], v[100:103]\n"
"v_mfma_f32_16x16x32_bf16 v[100:103], a[48:51], v[144:147], v[100:103]\n"
"v_mfma_f32_16x16x32_bf16 v[100:103], a[44:47], v[140:143], v[100:103]\n"
"v_mfma_f32_16x16x32_bf16 v[100:103], a[40:43], v[136:139], v[100:103]\n"
"s_nop 7\n"
"s_branch .Lmk_p44r\n"
".Lmk_m47s:\n"
";\n"
"s_waitcnt vmcnt(16)\n"
"s_mov_b32 s1, 0x10000\n"
".Lmk_m47l:\n"
"s_add_u32 s98, s62, 0x44000\n"
"buffer_load_dword v255, v10, s[4:7], s98 offen sc1\n"
"buffer_load_dword v254, v10, s[4:7], s98 offen offset:256 sc1\n"
"buffer_load_dword v253, v10, s[4:7], s98 offen offset:512 sc1\n"
"buffer_load_dword v252, v10, s[4:7], s98 offen offset:768 sc1\n"
"buffer_load_dword v251, v10, s[4:7], s98 offen offset:1024 sc1\n"
"buffer_load_dword v250, v10, s[4:7], s98 offen offset:1280 sc1\n"
"buffer_load_dword v249, v10, s[4:7], s98 offen offset:1536 sc1\n"
"buffer_load_dword v248, v10, s[4:7], s98 offen offset:1792 sc1\n"
"s_waitcnt vmcnt(0)\n"
"v_max_u32_e32 v247, v255, v254\n"
"v_max3_u32 v247, v247, v253, v252\n"
"v_max3_u32 v247, v247, v251, v250\n"
"v_max3_u32 v247, v247, v249, v248\n"
"v_cmp_eq_u32_e32 vcc, -1, v247\n"
"s_cbranch_vccz .Lmk_m47k\n"
"s_sub_u32 s1, s1, 1\n"
"s_cbranch_scc0 .Lmk_m47l\n"
".Lmk_m47k:\n"
"v_add_f32_e32 v255, v255, v251\n"
"v_add_f32_e32 v254, v254, v250\n"
"v_add_f32_e32 v253, v253, v249\n"
"v_add_f32_e32 v252, v252, v248\n"
"v_add_f32_e32 v255, v255, v253\n"
"v_add_f32_e32 v254, v254, v252\n"
"v_add_f32_e32 v255, v255, v254\n"
"s_nop 1\n"
"s_branch .Lmk_m47r\n"
".Lmk_p45s:\n"
";\n"
"s_waitcnt vmcnt(16)\n"
"s_mov_b32 s1, 0x10000\n"
".Lmk_p45l:\n"
"s_add_u32 s96, s54, 0x440000\n"
"buffer_load_dwordx4 v[176:179], v0, s[4:7], s96 offen sc1\n"
"buffer_load_dwordx4 v[108:111], v0, s[4:7], s96 offen offset:1024 sc1\n"
"buffer_load_dwordx4 v[148:151], v0, s[4:7], s96 offen offset:2048 sc1\n"
"buffer_load_dwordx4 v[184:187], v0, s[4:7], s96 offen offset:3072 sc1\n"
"buffer_load_dwordx4 v[152:155], v1, s[4:7], s96 offen sc1\n"
"buffer_load_dwordx4 v[192:195], v1, s[4:7], s96 offen offset:1024 sc1\n"
"buffer_load_dwordx4 v[196:199], v1, s[4:7], s96 offen offset:2048 sc1\n"
"buffer_load_dwordx4 v[200:203], v1, s[4:7], s96 offen offset:3072 sc1\n"
"s_add_u32 s96, s55, 0x440000\n"
"buffer_load_dwordx4 v[92:95], v0, s[4:7], s96 offen sc1\n"
"buffer_load_dwordx4 v[96:99], v0, s[4:7], s96 offen offset:1024 sc1\n"
"buffer_load_dwordx4 v[104:107], v0, s[4:7], s96 offen offset:2048 sc1\n"
"buffer_load_dwordx4 v[112:115], v0, s[4:7], s96 offen offset:3072 sc1\n"
"buffer_load_dwordx4 v[100:103], v1, s[4:7], s96 offen sc1\n"
"buffer_load_dwordx4 v[124:127], v1, s[4:7], s96 offen offset:1024 sc1\n"
"buffer_load_dwordx4 v[168:171], v1, s[4:7], s96 offen offset:2048 sc1\n"
"buffer_load_dwordx4 v[172:175], v1, s[4:7], s96 offen offset:3072 sc1\n"
"s_add_u32 s96, s52, 0x448000\n"
"buffer_load_dwordx4 v[128:131], v0, s[4:7], s96 offen sc1\n"
"s_waitcnt vmcnt(0)\n"
"v_max_u32_e32 v255, v176, v177\n"
"v_max3_u32 v255, v255, v178, v179\n"
"v_max3_u32 v255, v255, v108, v109\n"
"v_max3_u32 v255, v255, v110, v111\n"
"v_max3_u32 v255, v255, v148, v149\n"
"v_max3_u32 v255, v255, v150, v151\n"
"v_max3_u32 v255, v255, v184, v185\n"
"v_max3_u32 v255, v255, v186, v187\n"
"v_max3_u32 v255, v255, v152, v153\n"
"v_max3_u32 v255, v255, v154, v155\n"
"v_max3_u32 v255, v255, v192, v193\n"
"v_max3_u32 v255, v255, v194, v195\n"
"v_max3_u32 v255, v255, v196, v197\n"
"v_max3_u32 v255, v255, v198, v199\n"
"v_max3_u32 v255, v255, v200, v201\n"
"v_max3_u32 v255, v255, v202, v203\n"
"v_cmp_eq_u32_e32 vcc, -1, v255\n"
"s_cbranch_vccz .Lmk_p45k\n"
"s_sub_u32 s1, s1, 1\n"
"s_cbranch_scc0 .Lmk_p45l\n"
".Lmk_p45k:\n"
"v_mfma_f32_16x16x32_bf16 v[88:91], a[0:3], v[176:179], 0\n"
"v_mfma_f32_16x16x32_bf16 v[88:91], a[4:7], v[108:111], v[88:91]\n"
"v_mfma_f32_16x16x32_bf16 v[88:91], a[8:11], v[148:151], v[88:91]\n"
"v_mfma_f32_16x16x32_bf16 v[88:91], a[12:15], v[184:187], v[88:91]\n"
"v_mfma_f32_16x16x32_bf16 v[88:91], a[16:19], v[152:155], v[88:91]\n"
"v_mfma_f32_16x16x32_bf16 v[88:91], a[20:23], v[192:195], v[88:91]\n"
"v_mfma_f32_16x16x32_bf16 v[88:91], a[24:27], v[196:199], v[88:91]\n"
"v_mfma_f32_16x16x32_bf16 v[88:91], a[28:31], v[200:203], v[88:91]\n"
"v_mfma_f32_16x16x32_bf16 v[116:119], a[60:63], v[200:203], 0\n"
"v_mfma_f32_16x16x32_bf16 v[116:119], a[56:59], v[196:199], v[116:119]\n"
"v_mfma_f32_16x16x32_bf16 v[116:119], a[52:55], v[192:195], v[116:119]\n"
"v_mfma_f32_16x16x32_bf16 v[116:119], a[48:51], v[152:155], v[116:119]\n"
"v_mfma_f32_16x16x32_bf16 v[116:119], a[44:47], v[184:187], v[116:119]\n"
"v_mfma_f32_16x16x32_bf16 v[116:119], a[40:43], v[148:151], v[116:119]\n"
"s_nop 7\n"
"s_branch .Lmk_p45r\n"
".Lmk_p46s:\n"
";\n"
"s_waitcnt vmcnt(16)\n"
"s_mov_b32 s1, 0x10000\n"
".Lmk_p46l:\n"
"s_add_u32 s96, s55, 0x440000\n"
"buffer_load_dwordx4 v[92:95], v0, s[4:7], s96 offen sc1\n"
"buffer_load_dwordx4 v[96:99], v0, s[4:7], s96 offen offset:1024 sc1\n"
"buffer_load_dwordx4 v[104:107], v0, s[4:7], s96 offen offset:2048 sc1\n"
"buffer_load_dwordx4 v[112:115], v0, s[4:7], s96 offen offset:3072 sc1\n"
"buffer_load_dwordx4 v[100:103], v1, s[4:7], s96 offen sc1\n"
"buffer_load_dwordx4 v[124:127], v1, s[4:7], s96 offen offset:1024 sc1\n"
"buffer_load_dwordx4 v[168:171], v1, s[4:7], s96 offen offset:2048 sc1\n"
"buffer_load_dwordx4 v[172:175], v1, s[4:7], s96 offen offset:3072 sc1\n"
"s_add_u32 s96, s52, 0x448000\n"
"buffer_load_dwordx4 v[128:131], v0, s[4:7], s96 offen sc1\n"
"buffer_load_dwordx4 v[88:91], v0, s[4:7], s96 offen offset:1024 sc1\n"
"buffer_load_dwordx4 v[164:167], v0, s[4:7], s96 offen offset:2048 sc1\n"
"buffer_load_dwordx4 v[140:143], v0, s[4:7], s96 offen offset:3072 sc1\n"
"buffer_load_dwordx4 v[132:135], v1, s[4:7], s96 offen sc1\n"
"buffer_load_dwordx4 v[224:227], v1, s[4:7], s96 offen offset:1024 sc1\n"
"buffer_load_dwordx4 v[180:183], v1, s[4:7], s96 offen offset:2048 sc1\n"
"buffer_load_dwordx4 v[148:151], v1, s[4:7], s96 offen offset:3072 sc1\n"
"s_add_u32 s96, s53, 0x448000\n"
"buffer_load_dwordx4 v[116:119], v0, s[4:7], s96 offen sc1\n"
"buffer_load_dwordx4 v[160:163], v0, s[4:7], s96 offen offset:1024 sc1\n"
"s_waitcnt vmcnt(0)\n"
"v_max_u32_e32 v252, v92, v93\n"
"v_max3_u32 v252, v252, v94, v95\n"
"v_max3_u32 v252, v252, v96, v97\n"
"v_max3_u32 v252, v252, v98, v99\n"
"v_max3_u32 v252, v252, v104, v105\n"
"v_max3_u32 v252, v252, v106, v107\n"
"v_max3_u32 v252, v252, v112, v113\n"
"v_max3_u32 v252, v252, v114, v115\n"
"v_max3_u32 v252, v252, v100, v101\n"
"v_max3_u32 v252, v252, v102, v103\n"
"v_max3_u32 v252, v252, v124, v125\n"
"v_max3_u32 v252, v252, v126, v127\n"
"v_max3_u32 v252, v252, v168, v169\n"
"v_max3_u32 v252, v252, v170, v171\n"
"v_max3_u32 v252, v252, v172, v173\n"
"v_max3_u32 v252, v252, v174, v175\n"
"v_cmp_eq_u32_e32 vcc, -1, v252\n"
"s_cbranch_vccz .Lmk_p46k\n"
"s_sub_u32 s1, s1, 1\n"
"s_cbranch_scc0 .Lmk_p46l\n"
".Lmk_p46k:\n"
"v_mfma_f32_16x16x32_bf16 v[196:199], a[0:3], v[92:95], v[136:139]\n"
"v_mfma_f32_16x16x32_bf16 v[196:199], a[4:7], v[96:99], v[196:199]\n"
"v_mfma_f32_16x16x32_bf16 v[196:199], a[8:11], v[104:107], v[196:199]\n"
"v_mfma_f32_16x16x32_bf16 v[196:199], a[12:15], v[112:115], v[196:199]\n"
"v_mfma_f32_16x16x32_bf16 v[196:199], a[16:19], v[100:103], v[196:199]\n"
"v_mfma_f32_16x16x32_bf16 v[196:199], a[20:23], v[124:127], v[196:199]\n"
"v_mfma_f32_16x16x32_bf16 v[196:199], a[24:27], v[168:171], v[196:199]\n"
"v_mfma_f32_16x16x32_bf16 v[196:199], a[28:31], v[172:175], v[196:199]\n"
"v_mfma_f32_16x16x32_bf16 v[204:207], a[60:63], v[172:175], v[156:159]\n"
"v_mfma_f32_16x16x32_bf16 v[204:207], a[56:59], v[168:171], v[204:207]\n"
"v_mfma_f32_16x16x32_bf16 v[204:207], a[52:55], v[124:127], v[204:207]\n"
"v_mfma_f32_16x16x32_bf16 v[204:207], a[48:51], v[100:103], v[204:207]\n"
"v_mfma_f32_16x16x32_bf16 v[204:207], a[44:47], v[112:115], v[204:207]\n"
"v_mfma_f32_16x16x32_bf16 v[204:207], a[40:43], v[104:107], v[204:207]\n"
"s_nop 7\n"
"s_branch .Lmk_p46r\n"
".Lmk_p48s:\n"
";\n"
"s_waitcnt vmcnt(16)\n"
"s_mov_b32 s1, 0x10000\n"
".Lmk_p48l:\n"
"s_add_u32 s97, s52, 0x448000\n"
"buffer_load_dwordx4 v[128:131], v0, s[4:7], s97 offen sc1\n"
"buffer_load_dwordx4 v[88:91], v0, s[4:7], s97 offen offset:1024 sc1\n"
"buffer_load_dwordx4 v[164:167], v0, s[4:7], s97 offen offset:2048 sc1\n"
"buffer_load_dwordx4 v[140:143], v0, s[4:7], s97 offen offset:3072 sc1\n"
"buffer_load_dwordx4 v[132:135], v1, s[4:7], s97 offen sc1\n"
"buffer_load_dwordx4 v[224:227], v1, s[4:7], s97 offen offset:1024 sc1\n"
"buffer_load_dwordx4 v[180:183], v1, s[4:7], s97 offen offset:2048 sc1\n"
"buffer_load_dwordx4 v[148:151], v1, s[4:7], s97 offen offset:3072 sc1\n"
"s_add_u32 s97, s53, 0x448000\n"
"buffer_load_dwordx4 v[116:119], v0, s[4:7], s97 offen sc1\n"
"buffer_load_dwordx4 v[160:163], v0, s[4:7], s97 offen offset:1024 sc1\n"
"buffer_load_dwordx4 v[136:139], v0, s[4:7], s97 offen offset:2048 sc1\n"
"buffer_load_dwordx4 v[156:159], v0, s[4:7], s97 offen offset:3072 sc1\n"
"buffer_load_dwordx4 v[192:195], v1, s[4:7], s97 offen sc1\n"
"buffer_load_dwordx4 v[184:187], v1, s[4:7], s97 offen offset:1024 sc1\n"
"buffer_load_dwordx4 v[176:179], v1, s[4:7], s97 offen offset:2048 sc1\n"
"buffer_load_dwordx4 v[200:203], v1, s[4:7], s97 offen offset:3072 sc1\n"
"s_add_u32 s97, s54, 0x448000\n"
"buffer_load_dwordx4 v[96:99], v0, s[4:7], s97 offen sc1\n"
"buffer_load_dwordx4 v[100:103], v0, s[4:7], s97 offen offset:1024 sc1\n"
"s_waitcnt vmcnt(0)\n"
"v_max_u32_e32 v252, v128, v129\n"
"v_max3_u32 v252, v252, v130, v131\n"
"v_max3_u32 v252, v252, v88, v89\n"
"v_max3_u32 v252, v252, v90, v91\n"
"v_max3_u32 v252, v252, v164, v165\n"
"v_max3_u32 v252, v252, v166, v167\n"
"v_max3_u32 v252, v252, v140, v141\n"
"v_max3_u32 v252, v252, v142, v143\n"
"v_max3_u32 v252, v252, v132, v133\n"
"v_max3_u32 v252, v252, v134, v135\n"
"v_max3_u32 v252, v252, v224, v225\n"
"v_max3_u32 v252, v252, v226, v227\n"
"v_max3_u32 v252, v252, v180, v181\n"
"v_max3_u32 v252, v252, v182, v183\n"
"v_max3_u32 v252, v252, v148, v149\n"
"v_max3_u32 v252, v252, v150, v151\n"
"v_cmp_eq_u32_e32 vcc, -1, v252\n"
"s_cbranch_vccz .Lmk_p48k\n"
"s_sub_u32 s1, s1, 1\n"
"s_cbranch_scc0 .Lmk_p48l\n"
".Lmk_p48k:\n"
"v_mfma_f32_16x16x32_bf16 v[92:95], a[0:3], v[128:131], 0\n"
"v_mfma_f32_16x16x32_bf16 v[92:95], a[4:7], v[88:91], v[92:95]\n"
"v_mfma_f32_16x16x32_bf16 v[92:95], a[8:11], v[164:167], v[92:95]\n"
"v_mfma_f32_16x16x32_bf16 v[92:95], a[12:15], v[140:143], v[92:95]\n"
"v_mfma_f32_16x16x32_bf16 v[92:95], a[16:19], v[132:135], v[92:95]\n"
"v_mfma_f32_16x16x32_bf16 v[92:95], a[20:23], v[224:227], v[92:95]\n"
"v_mfma_f32_16x16x32_bf16 v[92:95], a[24:27], v[180:183], v[92:95]\n"
"v_mfma_f32_16x16x32_bf16 v[92:95], a[28:31], v[148:151], v[92:95]\n"
"v_mfma_f32_16x16x32_bf16 v[104:107], a[60:63], v[148:151], 0\n"
"v_mfma_f32_16x16x32_bf16 v[104:107], a[56:59], v[180:183], v[104:107]\n"
"v_mfma_f32_16x16x32_bf16 v[104:107], a[52:55], v[224:227], v[104:107]\n"
"v_mfma_f32_16x16x32_bf16 v[104:107], a[48:51], v[132:135], v[104:107]\n"
"v_mfma_f32_16x16x32_bf16 v[104:107], a[44:47], v[140:143], v[104:107]\n"
"v_mfma_f32_16x16x32_bf16 v[104:107], a[40:43], v[164:167], v[104:107]\n"
"s_nop 7\n"
"s_branch .Lmk_p48r\n"
".Lmk_p49s:\n"
";\n"
"s_waitcnt vmcnt(16)\n"
"s_mov_b32 s1, 0x10000\n"
".Lmk_p49l:\n"
"s_add_u32 s95, s53, 0x448000\n"
"buffer_load_dwordx4 v[116:119], v0, s[4:7], s95 offen sc1\n"
"buffer_load_dwordx4 v[160:163], v0, s[4:7], s95 offen offset:1024 sc1\n"
"buffer_load_dwordx4 v[136:139], v0, s[4:7], s95 offen offset:2048 sc1\n"
"buffer_load_dwordx4 v[156:159], v0, s[4:7], s95 offen offset:3072 sc1\n"
"buffer_load_dwordx4 v[192:195], v1, s[4:7], s95 offen sc1\n"
"buffer_load_dwordx4 v[184:187], v1, s[4:7], s95 offen offset:1024 sc1\n"
"buffer_load_dwordx4 v[176:179], v1, s[4:7], s95 offen offset:2048 sc1\n"
"buffer_load_dwordx4 v[200:203], v1, s[4:7], s95 offen offset:3072 sc1\n"
"s_add_u32 s95, s54, 0x448000\n"
"buffer_load_dwordx4 v[96:99], v0, s[4:7], s95 offen sc1\n"
"buffer_load_dwordx4 v[100:103], v0, s[4:7], s95 offen offset:1024 sc1\n"
"buffer_load_dwordx4 v[112:115], v0, s[4:7], s95 offen offset:2048 sc1\n"
"buffer_load_dwordx4 v[120:123], v0, s[4:7], s95 offen offset:3072 sc1\n"
"buffer_load_dwordx4 v[104:107], v1, s[4:7], s95 offen sc1\n"
"buffer_load_dwordx4 v[152:155], v1, s[4:7], s95 offen offset:1024 sc1\n"
"buffer_load_dwordx4 v[108:111], v1, s[4:7], s95 offen offset:2048 sc1\n"
"buffer_load_dwordx4 v[144:147], v1, s[4:7], s95 offen offset:3072 sc1\n"
"s_add_u32 s95, s55, 0x448000\n"
"buffer_load_dwordx4 v[92:95], v0, s[4:7], s95 offen sc1\n"
"buffer_load_dwordx4 v[124:127], v0, s[4:7], s95 offen offset:1024 sc1\n"
"s_waitcnt vmcnt(0)\n"
"v_max_u32_e32 v247, v116, v117\n"
"v_max3_u32 v247, v247, v118, v119\n"
"v_max3_u32 v247, v247, v160, v161\n"
"v_max3_u32 v247, v247, v162, v163\n"
"v_max3_u32 v247, v247, v136, v137\n"
"v_max3_u32 v247, v247, v138, v139\n"
"v_max3_u32 v247, v247, v156, v157\n"
"v_max3_u32 v247, v247, v158, v159\n"
"v_max3_u32 v247, v247, v192, v193\n"
"v_max3_u32 v247, v247, v194, v195\n"
"v_max3_u32 v247, v247, v184, v185\n"
"v_max3_u32 v247, v247, v186, v187\n"
"v_max3_u32 v247, v247, v176, v177\n"
"v_max3_u32 v247, v247, v178, v179\n"
"v_max3_u32 v247, v247, v200, v201\n"
"v_max3_u32 v247, v247, v202, v203\n"
"v_cmp_eq_u32_e32 vcc, -1, v247\n"
"s_cbranch_vccz .Lmk_p49k\n"
"s_sub_u32 s1, s1, 1\n"
"s_cbranch_scc0 .Lmk_p49l\n"
".Lmk_p49k:\n"
"v_mfma_f32_16x16x32_bf16 v[88:91], a[0:3], v[116:119], 0\n"
"v_mfma_f32_16x16x32_bf16 v[88:91], a[4:7], v[160:163], v[88:91]\n"
"v_mfma_f32_16x16x32_bf16 v[88:91], a[8:11], v[136:139], v[88:91]\n"
"v_mfma_f32_16x16x32_bf16 v[88:91], a[12:15], v[156:159], v[88:91]\n"
"v_mfma_f32_16x16x32_bf16 v[88:91], a[16:19], v[192:195], v[88:91]\n"
"v_mfma_f32_16x16x32_bf16 v[88:91], a[20:23], v[184:187], v[88:91]\n"
"v_mfma_f32_16x16x32_bf16 v[88:91], a[24:27], v[176:179], v[88:91]\n"
"v_mfma_f32_16x16x32_bf16 v[88:91], a[28:31], v[200:203], v[88:91]\n"
"v_mfma_f32_16x16x32_bf16 v[128:131], a[60:63], v[200:203], 0\n"
"v_mfma_f32_16x16x32_bf16 v[128:131], a[56:59], v[176:179], v[128:131]\n"
"v_mfma_f32_16x16x32_bf16 v[128:131], a[52:55], v[184:187], v[128:131]\n"
"v_mfma_f32_16x16x32_bf16 v[128:131], a[48:51], v[192:195], v[128:131]\n"
"v_mfma_f32_16x16x32_bf16 v[128:131], a[44:47], v[156:159], v[128:131]\n"
"v_mfma_f32_16x16x32_bf16 v[128:131], a[40:43], v[136:139], v[128:131]\n"
"s_nop 7\n"
"s_branch .Lmk_p49r\n"
".Lmk_m52s:\n"
";\n"
"s_waitcnt vmcnt(16)\n"
"s_mov_b32 s1, 0x10000\n"
".Lmk_m52l:\n"
"s_add_u32 s98, s62, 0x44800\n"
"buffer_load_dword v255, v10, s[4:7], s98 offen sc1\n"
"buffer_load_dword v253, v10, s[4:7], s98 offen offset:256 sc1\n"
"buffer_load_dword v252, v10, s[4:7], s98 offen offset:512 sc1\n"
"buffer_load_dword v251, v10, s[4:7], s98 offen offset:768 sc1\n"
"buffer_load_dword v250, v10, s[4:7], s98 offen offset:1024 sc1\n"
"buffer_load_dword v254, v10, s[4:7], s98 offen offset:1280 sc1\n"
"buffer_load_dword v249, v10, s[4:7], s98 offen offset:1536 sc1\n"
"buffer_load_dword v248, v10, s[4:7], s98 offen offset:1792 sc1\n"
"s_waitcnt vmcnt(0)\n"
"v_max_u32_e32 v245, v255, v253\n"
"v_max3_u32 v245, v245, v252, v251\n"
"v_max3_u32 v245, v245, v250, v254\n"
"v_max3_u32 v245, v245, v249, v248\n"
"v_cmp_eq_u32_e32 vcc, -1, v245\n"
"s_cbranch_vccz .Lmk_m52k\n"
"s_sub_u32 s1, s1, 1\n"
"s_cbranch_scc0 .Lmk_m52l\n"
".Lmk_m52k:\n"
"v_add_f32_e32 v255, v255, v250\n"
"v_add_f32_e32 v253, v253, v254\n"
"v_add_f32_e32 v252, v252, v249\n"
"v_add_f32_e32 v251, v251, v248\n"
"v_add_f32_e32 v255, v255, v252\n"
"v_add_f32_e32 v253, v253, v251\n"
"v_add_f32_e32 v255, v255, v253\n"
"s_nop 1\n"
"s_branch .Lmk_m52r\n"
".Lmk_f39s:\n"
";\n"
"s_mov_b32 s1, 0x10000\n"
".Lmk_f39l:\n"
"buffer_load_dwordx2 v[246:247], v12, s[4:7], s96 offen sc1\n"
"s_waitcnt vmcnt(0)\n"
"v_cmp_ne_u64_e32 vcc, v[246:247], v[18:19]\n"
"s_cbranch_vccz .Lmk_f39r\n"
"s_sub_u32 s1, s1, 1\n"
"s_cbranch_scc0 .Lmk_f39l\n"
"s_branch .Lmk_f39r\n"
".Lmk_p50s:\n"
";\n"
"s_waitcnt vmcnt(16)\n"
"s_mov_b32 s1, 0x10000\n"
".Lmk_p50l:\n"
"s_add_u32 s96, s54, 0x448000\n"
"buffer_load_dwordx4 v[96:99], v0, s[4:7], s96 offen sc1\n"
"buffer_load_dwordx4 v[100:103], v0, s[4:7], s96 offen offset:1024 sc1\n"
"buffer_load_dwordx4 v[112:115], v0, s[4:7], s96 offen offset:2048 sc1\n"
"buffer_load_dwordx4 v[120:123], v0, s[4:7], s96 offen offset:3072 sc1\n"
"buffer_load_dwordx4 v[104:107], v1, s[4:7], s96 offen sc1\n"
"buffer_load_dwordx4 v[152:155], v1, s[4:7], s96 offen offset:1024 sc1\n"
"buffer_load_dwordx4 v[108:111], v1, s[4:7], s96 offen offset:2048 sc1\n"
"buffer_load_dwordx4 v[144:147], v1, s[4:7], s96 offen offset:3072 sc1\n"
"s_add_u32 s96, s55, 0x448000\n"
"buffer_load_dwordx4 v[92:95], v0, s[4:7], s96 offen sc1\n"
"buffer_load_dwordx4 v[124:127], v0, s[4:7], s96 offen offset:1024 sc1\n"
"buffer_load_dwordx4 v[132:135], v0, s[4:7], s96 offen offset:2048 sc1\n"
"buffer_load_dwordx4 v[140:143], v0, s[4:7], s96 offen offset:3072 sc1\n"
"buffer_load_dwordx4 v[128:131], v1, s[4:7], s96 offen sc1\n"
"buffer_load_dwordx4 v[168:171], v1, s[4:7], s96 offen offset:1024 sc1\n"
"buffer_load_dwordx4 v[172:175], v1, s[4:7], s96 offen offset:2048 sc1\n"
"buffer_load_dwordx4 v[180:183], v1, s[4:7], s96 offen offset:3072 sc1\n"
"s_waitcnt vmcnt(0)\n"
"v_max_u32_e32 v252, v96, v97\n"
"v_max3_u32 v252, v252, v98, v99\n"
"v_max3_u32 v252, v252, v100, v101\n"
"v_max3_u32 v252, v252, v102, v103\n"
"v_max3_u32 v252, v252, v112, v113\n"
"v_max3_u32 v252, v252, v114, v115\n"
"v_max3_u32 v252, v252, v120, v121\n"
"v_max3_u32 v252, v252, v122, v123\n"
"v_max3_u32 v252, v252, v104, v105\n"
"v_max3_u32 v252, v252, v106, v107\n"
"v_max3_u32 v252, v252, v152, v153\n"
"v_max3_u32 v252, v252, v154, v155\n"
"v_max3_u32 v252, v252, v108, v109\n"
"v_max3_u32 v252, v252, v110, v111\n"
"v_max3_u32 v252, v252, v144, v145\n"
"v_max3_u32 v252, v252, v146, v147\n"
"v_cmp_eq_u32_e32 vcc, -1, v252\n"
"s_cbranch_vccz .Lmk_p50k\n"
"s_sub_u32 s1, s1, 1\n"
"s_cbranch_scc0 .Lmk_p50l\n"
".Lmk_p50k:\n"
"v_mfma_f32_16x16x32_bf16 v[88:91], a[0:3], v[96:99], 0\n"
"v_mfma_f32_16x16x32_bf16 v[88:91], a[4:7], v[100:103], v[88:91]\n"
"v_mfma_f32_16x16x32_bf16 v[88:91], a[8:11], v[112:115], v[88:91]\n"
"v_mfma_f32_16x16x32_bf16 v[88:91], a[12:15], v[120:123], v[88:91]\n"
"v_mfma_f32_16x16x32_bf16 v[88:91], a[16:19], v[104:107], v[88:91]\n"
"v_mfma_f32_16x16x32_bf16 v[88:91], a[20:23], v[152:155], v[88:91]\n"
"v_mfma_f32_16x16x32_bf16 v[88:91], a[24:27], v[108:111], v[88:91]\n"
"v_mfma_f32_16x16x32_bf16 v[88:91], a[28:31], v[144:147], v[88:91]\n"
"v_mfma_f32_16x16x32_bf16 v[116:119], a[60:63], v[144:147], 0\n"
"v_mfma_f32_16x16x32_bf16 v[116:119], a[56:59], v[108:111], v[116:119]\n"
"v_mfma_f32_16x16x32_bf16 v[116:119], a[52:55], v[152:155], v[116:119]\n"
"v_mfma_f32_16x16x32_bf16 v[116:119], a[48:51], v[104:107], v[116:119]\n"
"v_mfma_f32_16x16x32_bf16 v[116:119], a[44:47], v[120:123], v[116:119]\n"
"v_mfma_f32_16x16x32_bf16 v[116:119], a[40:43], v[112:115], v[116:119]\n"
"s_nop 7\n"
"s_branch .Lmk_p50r\n"
".Lmk_p51s:\n"
";\n"
"s_waitcnt vmcnt(16)\n"
"s_mov_b32 s1, 0x10000\n"
".Lmk_p51l:\n"
"s_add_u32 s96, s55, 0x448000\n"
"buffer_load_dwordx4 v[92:95], v0, s[4:7], s96 offen sc1\n"
"buffer_load_dwordx4 v[124:127], v0, s[4:7], s96 offen offset:1024 sc1\n"
"buffer_load_dwordx4 v[132:135], v0, s[4:7], s96 offen offset:2048 sc1\n"
"buffer_load_dwordx4 v[140:143], v0, s[4:7], s96 offen offset:3072 sc1\n"
"buffer_load_dwordx4 v[128:131], v1, s[4:7], s96 offen sc1\n"
"buffer_load_dwordx4 v[168:171], v1, s[4:7], s96 offen offset:1024 sc1\n"
"buffer_load_dwordx4 v[172:175], v1, s[4:7], s96 offen offset:2048 sc1\n"
"buffer_load_dwordx4 v[180:183], v1, s[4:7], s96 offen offset:3072 sc1\n"
"s_waitcnt vmcnt(0)\n"
"v_max_u32_e32 v252, v92, v93\n"
"v_max3_u32 v252, v252, v94, v95\n"
"v_max3_u32 v252, v252, v124, v125\n"
"v_max3_u32 v252, v252, v126, v127\n"
"v_max3_u32 v252, v252, v132, v133\n"
"v_max3_u32 v252, v252, v134, v135\n"
"v_max3_u32 v252, v252, v140, v141\n"
"v_max3_u32 v252, v252, v142, v143\n"
"v_max3_u32 v252, v252, v128, v129\n"
"v_max3_u32 v252, v252, v130, v131\n"
"v_max3_u32 v252, v252, v168, v169\n"
"v_max3_u32 v252, v252, v170, v171\n"
"v_max3_u32 v252, v252, v172, v173\n"
"v_max3_u32 v252, v252, v174, v175\n"
"v_max3_u32 v252, v252, v180, v181\n"
"v_max3_u32 v252, v252, v182, v183\n"
"v_cmp_eq_u32_e32 vcc, -1, v252\n"
"s_cbranch_vccz .Lmk_p51k\n"
"s_sub_u32 s1, s1, 1\n"
"s_cbranch_scc0 .Lmk_p51l\n"
".Lmk_p51k:\n"
"v_mfma_f32_16x16x32_bf16 v[112:115], a[0:3], v[92:95], v[156:159]\n"
"v_mfma_f32_16x16x32_bf16 v[112:115], a[4:7], v[124:127], v[112:115]\n"
"v_mfma_f32_16x16x32_bf16 v[112:115], a[8:11], v[132:135], v[112:115]\n"
"v_mfma_f32_16x16x32_bf16 v[112:115], a[12:15], v[140:143], v[112:115]\n"
"v_mfma_f32_16x16x32_bf16 v[112:115], a[16:19], v[128:131], v[112:115]\n"
"v_mfma_f32_16x16x32_bf16 v[112:115], a[20:23], v[168:171], v[112:115]\n"
"v_mfma_f32_16x16x32_bf16 v[112:115], a[24:27], v[172:175], v[112:115]\n"
"v_mfma_f32_16x16x32_bf16 v[112:115], a[28:31], v[180:183], v[112:115]\n"
"v_mfma_f32_16x16x32_bf16 v[188:191], a[60:63], v[180:183], v[184:187]\n"
"v_mfma_f32_16x16x32_bf16 v[188:191], a[56:59], v[172:175], v[188:191]\n"
"v_mfma_f32_16x16x32_bf16 v[188:191], a[52:55], v[168:171], v[188:191]\n"
"v_mfma_f32_16x16x32_bf16 v[188:191], a[48:51], v[128:131], v[188:191]\n"
"v_mfma_f32_16x16x32_bf16 v[188:191], a[44:47], v[140:143], v[188:191]\n"
"v_mfma_f32_16x16x32_bf16 v[188:191], a[40:43], v[132:135], v[188:191]\n"
"s_nop 7\n"
"s_branch .Lmk_p51r\n"
".Lmk_f40s:\n"
";\n"
"s_mov_b32 s1, 0x10000\n"
".Lmk_f40l:\n"
"buffer_load_dwordx2 v[246:247], off, s[4:7], s96 sc1\n"
"s_waitcnt vmcnt(0)\n"
"v_cmp_ne_u64_e32 vcc, v[246:247], v[18:19]\n"
"s_cbranch_vccz .Lmk_f40r\n"
"s_sub_u32 s1, s1, 1\n"
"s_cbranch_scc0 .Lmk_f40l\n"
"s_branch .Lmk_f40r\n"
".Lmk_f41s:\n"
";\n"
"s_mov_b32 s1, 0x10000\n"
".Lmk_f41l:\n"
"buffer_load_dwordx2 v[250:251], v12, s[4:7], s98 offen sc1\n"
"s_waitcnt vmcnt(0)\n"
"v_cmp_ne_u64_e32 vcc, v[250:251], v[18:19]\n"
"s_cbranch_vccz .Lmk_f41r\n"
"s_sub_u32 s1, s1, 1\n"
"s_cbranch_scc0 .Lmk_f41l\n"
"s_branch .Lmk_f41r\n"
".Lmk_p55s:\n"
";\n"
"s_waitcnt vmcnt(16)\n"
"s_mov_b32 s1, 0x10000\n"
".Lmk_p55l:\n"
"s_add_u32 s96, s56, 0x440000\n"
"buffer_load_dwordx4 v[120:123], v0, s[4:7], s96 offen sc1\n"
"buffer_load_dwordx4 v[136:139], v0, s[4:7], s96 offen offset:1024 sc1\n"
"buffer_load_dwordx4 v[144:147], v0, s[4:7], s96 offen offset:2048 sc1\n"
"buffer_load_dwordx4 v[192:195], v0, s[4:7], s96 offen offset:3072 sc1\n"
"buffer_load_dwordx4 v[196:199], v1, s[4:7], s96 offen sc1\n"
"buffer_load_dwordx4 v[188:191], v1, s[4:7], s96 offen offset:1024 sc1\n"
"buffer_load_dwordx4 v[108:111], v1, s[4:7], s96 offen offset:2048 sc1\n"
"buffer_load_dwordx4 v[92:95], v1, s[4:7], s96 offen offset:3072 sc1\n"
"s_add_u32 s96, s57, 0x440000\n"
"buffer_load_dwordx4 v[148:151], v0, s[4:7], s96 offen sc1\n"
"buffer_load_dwordx4 v[180:183], v0, s[4:7], s96 offen offset:1024 sc1\n"
"buffer_load_dwordx4 v[24:27], v0, s[4:7], s96 offen offset:2048 sc1\n"
"buffer_load_dwordx4 v[32:35], v0, s[4:7], s96 offen offset:3072 sc1\n"
"buffer_load_dwordx4 v[44:47], v1, s[4:7], s96 offen sc1\n"
"buffer_load_dwordx4 v[48:51], v1, s[4:7], s96 offen offset:1024 sc1\n"
"buffer_load_dwordx4 v[28:31], v1, s[4:7], s96 offen offset:2048 sc1\n"
"buffer_load_dwordx4 v[56:59], v1, s[4:7], s96 offen offset:3072 sc1\n"
"s_add_u32 s96, s58, 0x440000\n"
"buffer_load_dwordx4 v[40:43], v0, s[4:7], s96 offen sc1\n"
"buffer_load_dwordx4 v[52:55], v0, s[4:7], s96 offen offset:1024 sc1\n"
"buffer_load_dwordx4 v[64:67], v0, s[4:7], s96 offen offset:2048 sc1\n"
"s_waitcnt vmcnt(0)\n"
"v_max_u32_e32 v255, v120, v121\n"
"v_max3_u32 v255, v255, v122, v123\n"
"v_max3_u32 v255, v255, v136, v137\n"
"v_max3_u32 v255, v255, v138, v139\n"
"v_max3_u32 v255, v255, v144, v145\n"
"v_max3_u32 v255, v255, v146, v147\n"
"v_max3_u32 v255, v255, v192, v193\n"
"v_max3_u32 v255, v255, v194, v195\n"
"v_max3_u32 v255, v255, v196, v197\n"
"v_max3_u32 v255, v255, v198, v199\n"
"v_max3_u32 v255, v255, v188, v189\n"
"v_max3_u32 v255, v255, v190, v191\n"
"v_max3_u32 v255, v255, v108, v109\n"
"v_max3_u32 v255, v255, v110, v111\n"
"v_max3_u32 v255, v255, v92, v93\n"
"v_max3_u32 v255, v255, v94, v95\n"
"v_cmp_eq_u32_e32 vcc, -1, v255\n"
"s_cbranch_vccz .Lmk_p55k\n"
"s_sub_u32 s1, s1, 1\n"
"s_cbranch_scc0 .Lmk_p55l\n"
".Lmk_p55k:\n"
"v_mfma_f32_16x16x32_bf16 v[36:39], a[128:131], v[120:123], 0\n"
"v_mfma_f32_16x16x32_bf16 v[36:39], a[132:135], v[136:139], v[36:39]\n"
"v_mfma_f32_16x16x32_bf16 v[36:39], a[136:139], v[144:147], v[36:39]\n"
"v_mfma_f32_16x16x32_bf16 v[36:39], a[140:143], v[192:195], v[36:39]\n"
"v_mfma_f32_16x16x32_bf16 v[36:39], a[144:147], v[196:199], v[36:39]\n"
"v_mfma_f32_16x16x32_bf16 v[36:39], a[148:151], v[188:191], v[36:39]\n"
"v_mfma_f32_16x16x32_bf16 v[36:39], a[152:155], v[108:111], v[36:39]\n"
"v_mfma_f32_16x16x32_bf16 v[36:39], a[156:159], v[92:95], v[36:39]\n"
"v_mfma_f32_16x16x32_bf16 v[60:63], a[188:191], v[92:95], 0\n"
"v_mfma_f32_16x16x32_bf16 v[60:63], a[184:187], v[108:111], v[60:63]\n"
"v_mfma_f32_16x16x32_bf16 v[60:63], a[180:183], v[188:191], v[60:63]\n"
"v_mfma_f32_16x16x32_bf16 v[60:63], a[176:179], v[196:199], v[60:63]\n"
"v_mfma_f32_16x16x32_bf16 v[60:63], a[172:175], v[192:195], v[60:63]\n"
"v_mfma_f32_16x16x32_bf16 v[60:63], a[168:171], v[144:147], v[60:63]\n"
"s_nop 7\n"
"s_branch .Lmk_p55r\n"
".Lmk_p56s:\n"
";\n"
"s_waitcnt vmcnt(16)\n"
"s_mov_b32 s1, 0x10000\n"
".Lmk_p56l:\n"
"s_add_u32 s96, s57, 0x440000\n"
"buffer_load_dwordx4 v[148:151], v0, s[4:7], s96 offen sc1\n"
"buffer_load_dwordx4 v[180:183], v0, s[4:7], s96 offen offset:1024 sc1\n"
"buffer_load_dwordx4 v[24:27], v0, s[4:7], s96 offen offset:2048 sc1\n"
"buffer_load_dwordx4 v[32:35], v0, s[4:7], s96 offen offset:3072 sc1\n"
"buffer_load_dwordx4 v[44:47], v1, s[4:7], s96 offen sc1\n"
"buffer_load_dwordx4 v[48:51], v1, s[4:7], s96 offen offset:1024 sc1\n"
"buffer_load_dwordx4 v[28:31], v1, s[4:7], s96 offen offset:2048 sc1\n"
"buffer_load_dwordx4 v[56:59], v1, s[4:7], s96 offen offset:3072 sc1\n"
"s_add_u32 s96, s58, 0x440000\n"
"buffer_load_dwordx4 v[40:43], v0, s[4:7], s96 offen sc1\n"
"buffer_load_dwordx4 v[52:55], v0, s[4:7], s96 offen offset:1024 sc1\n"
"buffer_load_dwordx4 v[64:67], v0, s[4:7], s96 offen offset:2048 sc1\n"
"buffer_load_dwordx4 v[36:39], v0, s[4:7], s96 offen offset:3072 sc1\n"
"buffer_load_dwordx4 v[72:75], v1, s[4:7], s96 offen sc1\n"
"buffer_load_dwordx4 v[60:63], v1, s[4:7], s96 offen offset:1024 sc1\n"
"buffer_load_dwordx4 v[80:83], v1, s[4:7], s96 offen offset:2048 sc1\n"
"buffer_load_dwordx4 v[68:71], v1, s[4:7], s96 offen offset:3072 sc1\n"
"s_add_u32 s96, s59, 0x440000\n"
"buffer_load_dwordx4 v[96:99], v0, s[4:7], s96 offen sc1\n"
"buffer_load_dwordx4 v[76:79], v0, s[4:7], s96 offen offset:1024 sc1\n"
"buffer_load_dwordx4 v[112:115], v0, s[4:7], s96 offen offset:2048 sc1\n"
"s_waitcnt vmcnt(0)\n"
"v_max_u32_e32 v254, v148, v149\n"
"v_max3_u32 v254, v254, v150, v151\n"
"v_max3_u32 v254, v254, v180, v181\n"
"v_max3_u32 v254, v254, v182, v183\n"
"v_max3_u32 v254, v254, v24, v25\n"
"v_max3_u32 v254, v254, v26, v27\n"
"v_max3_u32 v254, v254, v32, v33\n"
"v_max3_u32 v254, v254, v34, v35\n"
"v_max3_u32 v254, v254, v44, v45\n"
"v_max3_u32 v254, v254, v46, v47\n"
"v_max3_u32 v254, v254, v48, v49\n"
"v_max3_u32 v254, v254, v50, v51\n"
"v_max3_u32 v254, v254, v28, v29\n"
"v_max3_u32 v254, v254, v30, v31\n"
"v_max3_u32 v254, v254, v56, v57\n"
"v_max3_u32 v254, v254, v58, v59\n"
"v_cmp_eq_u32_e32 vcc, -1, v254\n"
"s_cbranch_vccz .Lmk_p56k\n"
"s_sub_u32 s1, s1, 1\n"
"s_cbranch_scc0 .Lmk_p56l\n"
".Lmk_p56k:\n"
"v_mfma_f32_16x16x32_bf16 v[84:87], a[128:131], v[148:151], 0\n"
"v_mfma_f32_16x16x32_bf16 v[84:87], a[132:135], v[180:183], v[84:87]\n"
"v_mfma_f32_16x16x32_bf16 v[84:87], a[136:139], v[24:27], v[84:87]\n"
"v_mfma_f32_16x16x32_bf16 v[84:87], a[140:143], v[32:35], v[84:87]\n"
"v_mfma_f32_16x16x32_bf16 v[84:87], a[144:147], v[44:47], v[84:87]\n"
"v_mfma_f32_16x16x32_bf16 v[84:87], a[148:151], v[48:51], v[84:87]\n"
"v_mfma_f32_16x16x32_bf16 v[84:87], a[152:155], v[28:31], v[84:87]\n"
"v_mfma_f32_16x16x32_bf16 v[84:87], a[156:159], v[56:59], v[84:87]\n"
"v_mfma_f32_16x16x32_bf16 v[100:103], a[188:191], v[56:59], 0\n"
"v_mfma_f32_16x16x32_bf16 v[100:103], a[184:187], v[28:31], v[100:103]\n"
"v_mfma_f32_16x16x32_bf16 v[100:103], a[180:183], v[48:51], v[100:103]\n"
"v_mfma_f32_16x16x32_bf16 v[100:103], a[176:179], v[44:47], v[100:103]\n"
"v_mfma_f32_16x16x32_bf16 v[100:103], a[172:175], v[32:35], v[100:103]\n"
"v_mfma_f32_16x16x32_bf16 v[100:103], a[168:171], v[24:27], v[100:103]\n"
"s_nop 7\n"
"s_branch .Lmk_p56r\n"
".Lmk_p57s:\n"
";\n"
"s_waitcnt vmcnt(16)\n"
"s_mov_b32 s1, 0x10000\n"
".Lmk_p57l:\n"
"s_add_u32 s96, s58, 0x440000\n"
"buffer_load_dwordx4 v[40:43], v0, s[4:7], s96 offen sc1\n"
"buffer_load_dwordx4 v[52:55], v0, s[4:7], s96 offen offset:1024 sc1\n"
"buffer_load_dwordx4 v[64:67], v0, s[4:7], s96 offen offset:2048 sc1\n"
"buffer_load_dwordx4 v[36:39], v0, s[4:7], s96 offen offset:3072 sc1\n"
"buffer_load_dwordx4 v[72:75], v1, s[4:7], s96 offen sc1\n"
"buffer_load_dwordx4 v[60:63], v1, s[4:7], s96 offen offset:1024 sc1\n"
"buffer_load_dwordx4 v[80:83], v1, s[4:7], s96 offen offset:2048 sc1\n"
"buffer_load_dwordx4 v[68:71], v1, s[4:7], s96 offen offset:3072 sc1\n"
"s_add_u32 s96, s59, 0x440000\n"
"buffer_load_dwordx4 v[96:99], v0, s[4:7], s96 offen sc1\n"
"buffer_load_dwordx4 v[76:79], v0, s[4:7], s96 offen offset:1024 sc1\n"
"buffer_load_dwordx4 v[112:115], v0, s[4:7], s96 offen offset:2048 sc1\n"
"buffer_load_dwordx4 v[84:87], v0, s[4:7], s96 offen offset:3072 sc1\n"
"buffer_load_dwordx4 v[128:131], v1, s[4:7], s96 offen sc1\n"
"buffer_load_dwordx4 v[100:103], v1, s[4:7], s96 offen offset:1024 sc1\n"
"buffer_load_dwordx4 v[156:159], v1, s[4:7], s96 offen offset:2048 sc1\n"
"buffer_load_dwordx4 v[116:119], v1, s[4:7], s96 offen offset:3072 sc1\n"
"s_waitcnt vmcnt(0)\n"
"v_max_u32_e32 v237, v40, v41\n"
"v_max3_u32 v237, v237, v42, v43\n"
"v_max3_u32 v237, v237, v52, v53\n"
"v_max3_u32 v237, v237, v54, v55\n"
"v_max3_u32 v237, v237, v64, v65\n"
"v_max3_u32 v237, v237, v66, v67\n"
"v_max3_u32 v237, v237, v36, v37\n"
"v_max3_u32 v237, v237, v38, v39\n"
"v_max3_u32 v237, v237, v72, v73\n"
"v_max3_u32 v237, v237, v74, v75\n"
"v_max3_u32 v237, v237, v60, v61\n"
"v_max3_u32 v237, v237, v62, v63\n"
"v_max3_u32 v237, v237, v80, v81\n"
"v_max3_u32 v237, v237, v82, v83\n"
"v_max3_u32 v237, v237, v68, v69\n"
"v_max3_u32 v237, v237, v70, v71\n"
"v_cmp_eq_u32_e32 vcc, -1, v237\n"
"s_cbranch_vccz .Lmk_p57k\n"
"s_sub_u32 s1, s1, 1\n"
"s_cbranch_scc0 .Lmk_p57l\n"
".Lmk_p57k:\n"
"v_mfma_f32_16x16x32_bf16 v[132:135], a[128:131], v[40:43], 0\n"
"v_mfma_f32_16x16x32_bf16 v[132:135], a[132:135], v[52:55], v[132:135]\n"
"v_mfma_f32_16x16x32_bf16 v[132:135], a[136:139], v[64:67], v[132:135]\n"
"v_mfma_f32_16x16x32_bf16 v[132:135], a[140:143], v[36:39], v[132:135]\n"
"v_mfma_f32_16x16x32_bf16 v[132:135], a[144:147], v[72:75], v[132:135]\n"
"v_mfma_f32_16x16x32_bf16 v[132:135], a[148:151], v[60:63], v[132:135]\n"
"v_mfma_f32_16x16x32_bf16 v[132:135], a[152:155], v[80:83], v[132:135]\n"
"v_mfma_f32_16x16x32_bf16 v[132:135], a[156:159], v[68:71], v[132:135]\n"
"v_mfma_f32_16x16x32_bf16 v[140:143], a[188:191], v[68:71], 0\n"
"v_mfma_f32_16x16x32_bf16 v[140:143], a[184:187], v[80:83], v[140:143]\n"
"v_mfma_f32_16x16x32_bf16 v[140:143], a[180:183], v[60:63], v[140:143]\n"
"v_mfma_f32_16x16x32_bf16 v[140:143], a[176:179], v[72:75], v[140:143]\n"
"v_mfma_f32_16x16x32_bf16 v[140:143], a[172:175], v[36:39], v[140:143]\n"
"v_mfma_f32_16x16x32_bf16 v[140:143], a[168:171], v[64:67], v[140:143]\n"
"s_nop 7\n"
"s_branch .Lmk_p57r\n"
".Lmk_p58s:\n"
";\n"
"s_waitcnt vmcnt(16)\n"
"s_mov_b32 s1, 0x10000\n"
".Lmk_p58l:\n"
"s_add_u32 s96, s59, 0x440000\n"
"buffer_load_dwordx4 v[96:99], v0, s[4:7], s96 offen sc1\n"
"buffer_load_dwordx4 v[76:79], v0, s[4:7], s96 offen offset:1024 sc1\n"
"buffer_load_dwordx4 v[112:115], v0, s[4:7], s96 offen offset:2048 sc1\n"
"buffer_load_dwordx4 v[84:87], v0, s[4:7], s96 offen offset:3072 sc1\n"
"buffer_load_dwordx4 v[128:131], v1, s[4:7], s96 offen sc1\n"
"buffer_load_dwordx4 v[100:103], v1, s[4:7], s96 offen offset:1024 sc1\n"
"buffer_load_dwordx4 v[156:159], v1, s[4:7], s96 offen offset:2048 sc1\n"
"buffer_load_dwordx4 v[116:119], v1, s[4:7], s96 offen offset:3072 sc1\n"
"s_add_u32 s96, s56, 0x400000\n"
"buffer_load_dwordx4 v[132:135], v0, s[4:7], s96 offen sc1\n"
"buffer_load_dwordx4 v[184:187], v0, s[4:7], s96 offen offset:1024 sc1\n"
"buffer_load_dwordx4 v[140:143], v0, s[4:7], s96 offen offset:2048 sc1\n"
"buffer_load_dwordx4 v[68:71], v0, s[4:7], s96 offen offset:3072 sc1\n"
"buffer_load_dwordx4 v[36:39], v1, s[4:7], s96 offen sc1\n"
"buffer_load_dwordx4 v[40:43], v1, s[4:7], s96 offen offset:1024 sc1\n"
"buffer_load_dwordx4 v[72:75], v1, s[4:7], s96 offen offset:2048 sc1\n"
"buffer_load_dwordx4 v[80:83], v1, s[4:7], s96 offen offset:3072 sc1\n"
"s_add_u32 s96, s57, 0x400000\n"
"buffer_load_dwordx4 v[164:167], v0, s[4:7], s96 offen sc1\n"
"s_waitcnt vmcnt(0)\n"
"v_max_u32_e32 v254, v96, v97\n"
"v_max3_u32 v254, v254, v98, v99\n"
"v_max3_u32 v254, v254, v76, v77\n"
"v_max3_u32 v254, v254, v78, v79\n"
"v_max3_u32 v254, v254, v112, v113\n"
"v_max3_u32 v254, v254, v114, v115\n"
"v_max3_u32 v254, v254, v84, v85\n"
"v_max3_u32 v254, v254, v86, v87\n"
"v_max3_u32 v254, v254, v128, v129\n"
"v_max3_u32 v254, v254, v130, v131\n"
"v_max3_u32 v254, v254, v100, v101\n"
"v_max3_u32 v254, v254, v102, v103\n"
"v_max3_u32 v254, v254, v156, v157\n"
"v_max3_u32 v254, v254, v158, v159\n"
"v_max3_u32 v254, v254, v116, v117\n"
"v_max3_u32 v254, v254, v118, v119\n"
"v_cmp_eq_u32_e32 vcc, -1, v254\n"
"s_cbranch_vccz .Lmk_p58k\n"
"s_sub_u32 s1, s1, 1\n"
"s_cbranch_scc0 .Lmk_p58l\n"
".Lmk_p58k:\n"
"v_mfma_f32_16x16x32_bf16 v[64:67], a[128:131], v[96:99], v[152:155]\n"
"v_mfma_f32_16x16x32_bf16 v[64:67], a[132:135], v[76:79], v[64:67]\n"
"v_mfma_f32_16x16x32_bf16 v[64:67], a[136:139], v[112:115], v[64:67]\n"
"v_mfma_f32_16x16x32_bf16 v[64:67], a[140:143], v[84:87], v[64:67]\n"
"v_mfma_f32_16x16x32_bf16 v[64:67], a[144:147], v[128:131], v[64:67]\n"
"v_mfma_f32_16x16x32_bf16 v[64:67], a[148:151], v[100:103], v[64:67]\n"
"v_mfma_f32_16x16x32_bf16 v[64:67], a[152:155], v[156:159], v[64:67]\n"
"v_mfma_f32_16x16x32_bf16 v[64:67], a[156:159], v[116:119], v[64:67]\n"
"v_mfma_f32_16x16x32_bf16 v[60:63], a[188:191], v[116:119], v[168:171]\n"
"v_mfma_f32_16x16x32_bf16 v[60:63], a[184:187], v[156:159], v[60:63]\n"
"v_mfma_f32_16x16x32_bf16 v[60:63], a[180:183], v[100:103], v[60:63]\n"
"v_mfma_f32_16x16x32_bf16 v[60:63], a[176:179], v[128:131], v[60:63]\n"
"v_mfma_f32_16x16x32_bf16 v[60:63], a[172:175], v[84:87], v[60:63]\n"
"v_mfma_f32_16x16x32_bf16 v[60:63], a[168:171], v[112:115], v[60:63]\n"
"s_nop 7\n"
"s_branch .Lmk_p58r\n"
".Lmk_p59s:\n"
";\n"
"s_waitcnt vmcnt(16)\n"
"s_mov_b32 s1, 0x10000\n"
".Lmk_p59l:\n"
"s_add_u32 s96, s56, 0x400000\n"
"buffer_load_dwordx4 v[132:135], v0, s[4:7], s96 offen sc1\n"
"buffer_load_dwordx4 v[184:187], v0, s[4:7], s96 offen offset:1024 sc1\n"
"buffer_load_dwordx4 v[140:143], v0, s[4:7], s96 offen offset:2048 sc1\n"
"buffer_load_dwordx4 v[68:71], v0, s[4:7], s96 offen offset:3072 sc1\n"
"buffer_load_dwordx4 v[36:39], v1, s[4:7], s96 offen sc1\n"
"buffer_load_dwordx4 v[40:43], v1, s[4:7], s96 offen offset:1024 sc1\n"
"buffer_load_dwordx4 v[72:75], v1, s[4:7], s96 offen offset:2048 sc1\n"
"buffer_load_dwordx4 v[80:83], v1, s[4:7], s96 offen offset:3072 sc1\n"
"s_add_u32 s96, s57, 0x400000\n"
"buffer_load_dwordx4 v[164:167], v0, s[4:7], s96 offen sc1\n"
"buffer_load_dwordx4 v[168:171], v0, s[4:7], s96 offen offset:1024 sc1\n"
"buffer_load_dwordx4 v[208:211], v0, s[4:7], s96 offen offset:2048 sc1\n"
"buffer_load_dwordx4 v[212:215], v0, s[4:7], s96 offen offset:3072 sc1\n"
"buffer_load_dwordx4 v[64:67], v1, s[4:7], s96 offen sc1\n"
"buffer_load_dwordx4 v[100:103], v1, s[4:7], s96 offen offset:1024 sc1\n"
"buffer_load_dwordx4 v[60:63], v1, s[4:7], s96 offen offset:2048 sc1\n"
"buffer_load_dwordx4 v[76:79], v1, s[4:7], s96 offen offset:3072 sc1\n"
"s_add_u32 s96, s58, 0x400000\n"
"buffer_load_dwordx4 v[96:99], v0, s[4:7], s96 offen sc1\n"
"buffer_load_dwordx4 v[112:115], v0, s[4:7], s96 offen offset:1024 sc1\n"
"buffer_load_dwordx4 v[52:55], v0, s[4:7], s96 offen offset:2048 sc1\n"
"buffer_load_dwordx4 v[152:155], v0, s[4:7], s96 offen offset:3072 sc1\n"
"s_waitcnt vmcnt(0)\n"
"v_max_u32_e32 v255, v132, v133\n"
"v_max3_u32 v255, v255, v134, v135\n"
"v_max3_u32 v255, v255, v184, v185\n"
"v_max3_u32 v255, v255, v186, v187\n"
"v_max3_u32 v255, v255, v140, v141\n"
"v_max3_u32 v255, v255, v142, v143\n"
"v_max3_u32 v255, v255, v68, v69\n"
"v_max3_u32 v255, v255, v70, v71\n"
"v_max3_u32 v255, v255, v36, v37\n"
"v_max3_u32 v255, v255, v38, v39\n"
"v_max3_u32 v255, v255, v40, v41\n"
"v_max3_u32 v255, v255, v42, v43\n"
"v_max3_u32 v255, v255, v72, v73\n"
"v_max3_u32 v255, v255, v74, v75\n"
"v_max3_u32 v255, v255, v80, v81\n"
"v_max3_u32 v255, v255, v82, v83\n"
"v_cmp_eq_u32_e32 vcc, -1, v255\n"
"s_cbranch_vccz .Lmk_p59k\n"
"s_sub_u32 s1, s1, 1\n"
"s_cbranch_scc0 .Lmk_p59l\n"
".Lmk_p59k:\n"
"v_mfma_f32_16x16x32_bf16 v[84:87], a[128:131], v[132:135], 0\n"
"v_mfma_f32_16x16x32_bf16 v[84:87], a[132:135], v[184:187], v[84:87]\n"
"v_mfma_f32_16x16x32_bf16 v[84:87], a[136:139], v[140:143], v[84:87]\n"
"v_mfma_f32_16x16x32_bf16 v[84:87], a[140:143], v[68:71], v[84:87]\n"
"v_mfma_f32_16x16x32_bf16 v[84:87], a[144:147], v[36:39], v[84:87]\n"
"v_mfma_f32_16x16x32_bf16 v[84:87], a[148:151], v[40:43], v[84:87]\n"
"v_mfma_f32_16x16x32_bf16 v[84:87], a[152:155], v[72:75], v[84:87]\n"
"v_mfma_f32_16x16x32_bf16 v[84:87], a[156:159], v[80:83], v[84:87]\n"
"v_mfma_f32_16x16x32_bf16 v[128:131], a[188:191], v[80:83], 0\n"
"v_mfma_f32_16x16x32_bf16 v[128:131], a[184:187], v[72:75], v[128:131]\n"
"v_mfma_f32_16x16x32_bf16 v[128:131], a[180:183], v[40:43], v[128:131]\n"
"v_mfma_f32_16x16x32_bf16 v[128:131], a[176:179], v[36:39], v[128:131]\n"
"v_mfma_f32_16x16x32_bf16 v[128:131], a[172:175], v[68:71], v[128:131]\n"
"v_mfma_f32_16x16x32_bf16 v[128:131], a[168:171], v[140:143], v[128:131]\n"
"s_nop 7\n"
"s_branch .Lmk_p59r\n"
".Lmk_p60s:\n"
";\n"
"s_waitcnt vmcnt(16)\n"
"s_mov_b32 s1, 0x10000\n"
".Lmk_p60l:\n"
"s_add_u32 s96, s57, 0x400000\n"
"buffer_load_dwordx4 v[164:167], v0, s[4:7], s96 offen sc1\n"
"buffer_load_dwordx4 v[168:171], v0, s[4:7], s96 offen offset:1024 sc1\n"
"buffer_load_dwordx4 v[208:211], v0, s[4:7], s96 offen offset:2048 sc1\n"
"buffer_load_dwordx4 v[212:215], v0, s[4:7], s96 offen offset:3072 sc1\n"
"buffer_load_dwordx4 v[64:67], v1, s[4:7], s96 offen sc1\n"
"buffer_load_dwordx4 v[100:103], v1, s[4:7], s96 offen offset:1024 sc1\n"
"buffer_load_dwordx4 v[60:63], v1, s[4:7], s96 offen offset:2048 sc1\n"
"buffer_load_dwordx4 v[76:79], v1, s[4:7], s96 offen offset:3072 sc1\n"
"s_add_u32 s96, s58, 0x400000\n"
"buffer_load_dwordx4 v[96:99], v0, s[4:7], s96 offen sc1\n"
"buffer_load_dwordx4 v[112:115], v0, s[4:7], s96 offen offset:1024 sc1\n"
"buffer_load_dwordx4 v[52:55], v0, s[4:7], s96 offen offset:2048 sc1\n"
"buffer_load_dwordx4 v[152:155], v0, s[4:7], s96 offen offset:3072 sc1\n"
"buffer_load_dwordx4 v[84:87], v1, s[4:7], s96 offen sc1\n"
"buffer_load_dwordx4 v[172:175], v1, s[4:7], s96 offen offset:1024 sc1\n"
"buffer_load_dwordx4 v[128:131], v1, s[4:7], s96 offen offset:2048 sc1\n"
"buffer_load_dwordx4 v[72:75], v1, s[4:7], s96 offen offset:3072 sc1\n"
"s_add_u32 s96, s59, 0x400000\n"
"buffer_load_dwordx4 v[36:39], v0, s[4:7], s96 offen sc1\n"
"buffer_load_dwordx4 v[40:43], v0, s[4:7], s96 offen offset:1024 sc1\n"
"buffer_load_dwordx4 v[80:83], v0, s[4:7], s96 offen offset:2048 sc1\n"
"buffer_load_dwordx4 v[132:135], v0, s[4:7], s96 offen offset:3072 sc1\n"
"buffer_load_dwordx4 v[156:159], v1, s[4:7], s96 offen sc1\n"
"s_waitcnt vmcnt(0)\n"
"v_max_u32_e32 v254, v164, v165\n"
"v_max3_u32 v254, v254, v166, v167\n"
"v_max3_u32 v254, v254, v168, v169\n"
"v_max3_u32 v254, v254, v170, v171\n"
"v_max3_u32 v254, v254, v208, v209\n"
"v_max3_u32 v254, v254, v210, v211\n"
"v_max3_u32 v254, v254, v212, v213\n"
"v_max3_u32 v254, v254, v214, v215\n"
"v_max3_u32 v254, v254, v64, v65\n"
"v_max3_u32 v254, v254, v66, v67\n"
"v_max3_u32 v254, v254, v100, v101\n"
"v_max3_u32 v254, v254, v102, v103\n"
"v_max3_u32 v254, v254, v60, v61\n"
"v_max3_u32 v254, v254, v62, v63\n"
"v_max3_u32 v254, v254, v76, v77\n"
"v_max3_u32 v254, v254, v78, v79\n"
"v_cmp_eq_u32_e32 vcc, -1, v254\n"
"s_cbranch_vccz .Lmk_p60k\n"
"s_sub_u32 s1, s1, 1\n"
"s_cbranch_scc0 .Lmk_p60l\n"
".Lmk_p60k:\n"
"v_mfma_f32_16x16x32_bf16 v[68:71], a[128:131], v[164:167], 0\n"
"v_mfma_f32_16x16x32_bf16 v[68:71], a[132:135], v[168:171], v[68:71]\n"
"v_mfma_f32_16x16x32_bf16 v[68:71], a[136:139], v[208:211], v[68:71]\n"
"v_mfma_f32_16x16x32_bf16 v[68:71], a[140:143], v[212:215], v[68:71]\n"
"v_mfma_f32_16x16x32_bf16 v[68:71], a[144:147], v[64:67], v[68:71]\n"
"v_mfma_f32_16x16x32_bf16 v[68:71], a[148:151], v[100:103], v[68:71]\n"
"v_mfma_f32_16x16x32_bf16 v[68:71], a[152:155], v[60:63], v[68:71]\n"
"v_mfma_f32_16x16x32_bf16 v[68:71], a[156:159], v[76:79], v[68:71]\n"
"v_mfma_f32_16x16x32_bf16 v[140:143], a[188:191], v[76:79], 0\n"
"v_mfma_f32_16x16x32_bf16 v[140:143], a[184:187], v[60:63], v[140:143]\n"
"v_mfma_f32_16x16x32_bf16 v[140:143], a[180:183], v[100:103], v[140:143]\n"
"v_mfma_f32_16x16x32_bf16 v[140:143], a[176:179], v[64:67], v[140:143]\n"
"v_mfma_f32_16x16x32_bf16 v[140:143], a[172:175], v[212:215], v[140:143]\n"
"v_mfma_f32_16x16x32_bf16 v[140:143], a[168:171], v[208:211], v[140:143]\n"
"s_nop 7\n"
"s_branch .Lmk_p60r\n"
".Lmk_p61s:\n"
";\n"
"s_waitcnt vmcnt(16)\n"
"s_mov_b32 s1, 0x10000\n"
".Lmk_p61l:\n"
"s_add_u32 s97, s58, 0x400000\n"
"buffer_load_dwordx4 v[96:99], v0, s[4:7], s97 offen sc1\n"
"buffer_load_dwordx4 v[112:115], v0, s[4:7], s97 offen offset:1024 sc1\n"
"buffer_load_dwordx4 v[52:55], v0, s[4:7], s97 offen offset:2048 sc1\n"
"buffer_load_dwordx4 v[152:155], v0, s[4:7], s97 offen offset:3072 sc1\n"
"buffer_load_dwordx4 v[84:87], v1, s[4:7], s97 offen sc1\n"
"buffer_load_dwordx4 v[172:175], v1, s[4:7], s97 offen offset:1024 sc1\n"
"buffer_load_dwordx4 v[128:131], v1, s[4:7], s97 offen offset:2048 sc1\n"
"buffer_load_dwordx4 v[72:75], v1, s[4:7], s97 offen offset:3072 sc1\n"
"s_add_u32 s97, s59, 0x400000\n"
"buffer_load_dwordx4 v[36:39], v0, s[4:7], s97 offen sc1\n"
"buffer_load_dwordx4 v[40:43], v0, s[4:7], s97 offen offset:1024 sc1\n"
"buffer_load_dwordx4 v[80:83], v0, s[4:7], s97 offen offset:2048 sc1\n"
"buffer_load_dwordx4 v[132:135], v0, s[4:7], s97 offen offset:3072 sc1\n"
"buffer_load_dwordx4 v[156:159], v1, s[4:7], s97 offen sc1\n"
"buffer_load_dwordx4 v[68:71], v1, s[4:7], s97 offen offset:1024 sc1\n"
"buffer_load_dwordx4 v[200:203], v1, s[4:7], s97 offen offset:2048 sc1\n"
"buffer_load_dwordx4 v[140:143], v1, s[4:7], s97 offen offset:3072 sc1\n"
"s_waitcnt vmcnt(0)\n"
"v_max_u32_e32 v253, v96, v97\n"
"v_max3_u32 v253, v253, v98, v99\n"
"v_max3_u32 v253, v253, v112, v113\n"
"v_max3_u32 v253, v253, v114, v115\n"
"v_max3_u32 v253, v253, v52, v53\n"
"v_max3_u32 v253, v253, v54, v55\n"
"v_max3_u32 v253, v253, v152, v153\n"
"v_max3_u32 v253, v253, v154, v155\n"
"v_max3_u32 v253, v253, v84, v85\n"
"v_max3_u32 v253, v253, v86, v87\n"
"v_max3_u32 v253, v253, v172, v173\n"
"v_max3_u32 v253, v253, v174, v175\n"
"v_max3_u32 v253, v253, v128, v129\n"
"v_max3_u32 v253, v253, v130, v131\n"
"v_max3_u32 v253, v253, v72, v73\n"
"v_max3_u32 v253, v253, v74, v75\n"
"v_cmp_eq_u32_e32 vcc, -1, v253\n"
"s_cbranch_vccz .Lmk_p61k\n"
"s_sub_u32 s1, s1, 1\n"
"s_cbranch_scc0 .Lmk_p61l\n"
".Lmk_p61k:\n"
"v_mfma_f32_16x16x32_bf16 v[60:63], a[128:131], v[96:99], 0\n"
"v_mfma_f32_16x16x32_bf16 v[60:63], a[132:135], v[112:115], v[60:63]\n"
"v_mfma_f32_16x16x32_bf16 v[60:63], a[136:139], v[52:55], v[60:63]\n"
"v_mfma_f32_16x16x32_bf16 v[60:63], a[140:143], v[152:155], v[60:63]\n"
"v_mfma_f32_16x16x32_bf16 v[60:63], a[144:147], v[84:87], v[60:63]\n"
"v_mfma_f32_16x16x32_bf16 v[60:63], a[148:151], v[172:175], v[60:63]\n"
"v_mfma_f32_16x16x32_bf16 v[60:63], a[152:155], v[128:131], v[60:63]\n"
"v_mfma_f32_16x16x32_bf16 v[60:63], a[156:159], v[72:75], v[60:63]\n"
"v_mfma_f32_16x16x32_bf16 v[64:67], a[188:191], v[72:75], 0\n"
"v_mfma_f32_16x16x32_bf16 v[64:67], a[184:187], v[128:131], v[64:67]\n"
"v_mfma_f32_16x16x32_bf16 v[64:67], a[180:183], v[172:175], v[64:67]\n"
"v_mfma_f32_16x16x32_bf16 v[64:67], a[176:179], v[84:87], v[64:67]\n"
"v_mfma_f32_16x16x32_bf16 v[64:67], a[172:175], v[152:155], v[64:67]\n"
"v_mfma_f32_16x16x32_bf16 v[64:67], a[168:171], v[52:55], v[64:67]\n"
"s_nop 7\n"
"s_branch .Lmk_p61r\n"
".Lmk_p62s:\n"
";\n"
"s_waitcnt vmcnt(16)\n"
"s_mov_b32 s1, 0x10000\n"
".Lmk_p62l:\n"
"s_add_u32 s97, s59, 0x400000\n"
"buffer_load_dwordx4 v[36:39], v0, s[4:7], s97 offen sc1\n"
"buffer_load_dwordx4 v[40:43], v0, s[4:7], s97 offen offset:1024 sc1\n"
"buffer_load_dwordx4 v[80:83], v0, s[4:7], s97 offen offset:2048 sc1\n"
"buffer_load_dwordx4 v[132:135], v0, s[4:7], s97 offen offset:3072 sc1\n"
"buffer_load_dwordx4 v[156:159], v1, s[4:7], s97 offen sc1\n"
"buffer_load_dwordx4 v[68:71], v1, s[4:7], s97 offen offset:1024 sc1\n"
"buffer_load_dwordx4 v[200:203], v1, s[4:7], s97 offen offset:2048 sc1\n"
"buffer_load_dwordx4 v[140:143], v1, s[4:7], s97 offen offset:3072 sc1\n"
"s_waitcnt vmcnt(0)\n"
"v_max_u32_e32 v254, v36, v37\n"
"v_max3_u32 v254, v254, v38, v39\n"
"v_max3_u32 v254, v254, v40, v41\n"
"v_max3_u32 v254, v254, v42, v43\n"
"v_max3_u32 v254, v254, v80, v81\n"
"v_max3_u32 v254, v254, v82, v83\n"
"v_max3_u32 v254, v254, v132, v133\n"
"v_max3_u32 v254, v254, v134, v135\n"
"v_max3_u32 v254, v254, v156, v157\n"
"v_max3_u32 v254, v254, v158, v159\n"
"v_max3_u32 v254, v254, v68, v69\n"
"v_max3_u32 v254, v254, v70, v71\n"
"v_max3_u32 v254, v254, v200, v201\n"
"v_max3_u32 v254, v254, v202, v203\n"
"v_max3_u32 v254, v254, v140, v141\n"
"v_max3_u32 v254, v254, v142, v143\n"
"v_cmp_eq_u32_e32 vcc, -1, v254\n"
"s_cbranch_vccz .Lmk_p62k\n"
"s_sub_u32 s1, s1, 1\n"
"s_cbranch_scc0 .Lmk_p62l\n"
".Lmk_p62k:\n"
"v_mfma_f32_16x16x32_bf16 v[52:55], a[128:131], v[36:39], v[76:79]\n"
"v_mfma_f32_16x16x32_bf16 v[52:55], a[132:135], v[40:43], v[52:55]\n"
"v_mfma_f32_16x16x32_bf16 v[52:55], a[136:139], v[80:83], v[52:55]\n"
"v_mfma_f32_16x16x32_bf16 v[52:55], a[140:143], v[132:135], v[52:55]\n"
"v_mfma_f32_16x16x32_bf16 v[52:55], a[144:147], v[156:159], v[52:55]\n"
"v_mfma_f32_16x16x32_bf16 v[52:55], a[148:151], v[68:71], v[52:55]\n"
"v_mfma_f32_16x16x32_bf16 v[52:55], a[152:155], v[200:203], v[52:55]\n"
"v_mfma_f32_16x16x32_bf16 v[52:55], a[156:159], v[140:143], v[52:55]\n"
"v_mfma_f32_16x16x32_bf16 v[60:63], a[188:191], v[140:143], v[116:119]\n"
"v_mfma_f32_16x16x32_bf16 v[60:63], a[184:187], v[200:203], v[60:63]\n"
"v_mfma_f32_16x16x32_bf16 v[60:63], a[180:183], v[68:71], v[60:63]\n"
"v_mfma_f32_16x16x32_bf16 v[60:63], a[176:179], v[156:159], v[60:63]\n"
"v_mfma_f32_16x16x32_bf16 v[60:63], a[172:175], v[132:135], v[60:63]\n"
"v_mfma_f32_16x16x32_bf16 v[60:63], a[168:171], v[80:83], v[60:63]\n"
"s_nop 7\n"
"s_branch .Lmk_p62r\n"
".Lmk_p63s:\n"
";\n"
"s_waitcnt vmcnt(16)\n"
"s_mov_b32 s1, 0x10000\n"
".Lmk_p63l:\n"
"s_add_u32 s96, s56, 0x440000\n"
"buffer_load_dwordx4 v[120:123], v0, s[4:7], s96 offen sc1\n"
"buffer_load_dwordx4 v[136:139], v0, s[4:7], s96 offen offset:1024 sc1\n"
"buffer_load_dwordx4 v[144:147], v0, s[4:7], s96 offen offset:2048 sc1\n"
"buffer_load_dwordx4 v[192:195], v0, s[4:7], s96 offen offset:3072 sc1\n"
"buffer_load_dwordx4 v[196:199], v1, s[4:7], s96 offen sc1\n"
"buffer_load_dwordx4 v[188:191], v1, s[4:7], s96 offen offset:1024 sc1\n"
"buffer_load_dwordx4 v[108:111], v1, s[4:7], s96 offen offset:2048 sc1\n"
"buffer_load_dwordx4 v[92:95], v1, s[4:7], s96 offen offset:3072 sc1\n"
"s_add_u32 s96, s57, 0x440000\n"
"buffer_load_dwordx4 v[148:151], v0, s[4:7], s96 offen sc1\n"
"buffer_load_dwordx4 v[180:183], v0, s[4:7], s96 offen offset:1024 sc1\n"
"buffer_load_dwordx4 v[24:27], v0, s[4:7], s96 offen offset:2048 sc1\n"
"buffer_load_dwordx4 v[32:35], v0, s[4:7], s96 offen offset:3072 sc1\n"
"buffer_load_dwordx4 v[44:47], v1, s[4:7], s96 offen sc1\n"
"buffer_load_dwordx4 v[48:51], v1, s[4:7], s96 offen offset:1024 sc1\n"
"buffer_load_dwordx4 v[28:31], v1, s[4:7], s96 offen offset:2048 sc1\n"
"buffer_load_dwordx4 v[56:59], v1, s[4:7], s96 offen offset:3072 sc1\n"
"s_add_u32 s96, s58, 0x440000\n"
"buffer_load_dwordx4 v[36:39], v0, s[4:7], s96 offen sc1\n"
"buffer_load_dwordx4 v[40:43], v0, s[4:7], s96 offen offset:1024 sc1\n"
"buffer_load_dwordx4 v[52:55], v0, s[4:7], s96 offen offset:2048 sc1\n"
"buffer_load_dwordx4 v[60:63], v0, s[4:7], s96 offen offset:3072 sc1\n"
"buffer_load_dwordx4 v[64:67], v1, s[4:7], s96 offen sc1\n"
"buffer_load_dwordx4 v[68:71], v1, s[4:7], s96 offen offset:1024 sc1\n"
"buffer_load_dwordx4 v[72:75], v1, s[4:7], s96 offen offset:2048 sc1\n"
"buffer_load_dwordx4 v[76:79], v1, s[4:7], s96 offen offset:3072 sc1\n"
"s_add_u32 s96, s59, 0x440000\n"
"buffer_load_dwordx4 v[84:87], v0, s[4:7], s96 offen sc1\n"
"buffer_load_dwordx4 v[88:91], v0, s[4:7], s96 offen offset:1024 sc1\n"
"buffer_load_dwordx4 v[100:103], v0, s[4:7], s96 offen offset:2048 sc1\n"
"buffer_load_dwordx4 v[104:107], v0, s[4:7], s96 offen offset:3072 sc1\n"
"s_waitcnt vmcnt(0)\n"
"v_max_u32_e32 v255, v120, v121\n"
"v_max3_u32 v255, v255, v122, v123\n"
"v_max3_u32 v255, v255, v136, v137\n"
"v_max3_u32 v255, v255, v138, v139\n"
"v_max3_u32 v255, v255, v144, v145\n"
"v_max3_u32 v255, v255, v146, v147\n"
"v_max3_u32 v255, v255, v192, v193\n"
"v_max3_u32 v255, v255, v194, v195\n"
"v_max3_u32 v255, v255, v196, v197\n"
"v_max3_u32 v255, v255, v198, v199\n"
"v_max3_u32 v255, v255, v188, v189\n"
"v_max3_u32 v255, v255, v190, v191\n"
"v_max3_u32 v255, v255, v108, v109\n"
"v_max3_u32 v255, v255, v110, v111\n"
"v_max3_u32 v255, v255, v92, v93\n"
"v_max3_u32 v255, v255, v94, v95\n"
"v_cmp_eq_u32_e32 vcc, -1, v255\n"
"s_cbranch_vccz .Lmk_p63k\n"
"s_sub_u32 s1, s1, 1\n"
"s_cbranch_scc0 .Lmk_p63l\n"
".Lmk_p63k:\n"
"v_mfma_f32_16x16x32_bf16 v[80:83], a[128:131], v[120:123], 0\n"
"v_mfma_f32_16x16x32_bf16 v[80:83], a[132:135], v[136:139], v[80:83]\n"
"v_mfma_f32_16x16x32_bf16 v[80:83], a[136:139], v[144:147], v[80:83]\n"
"v_mfma_f32_16x16x32_bf16 v[80:83], a[140:143], v[192:195], v[80:83]\n"
"v_mfma_f32_16x16x32_bf16 v[80:83], a[144:147], v[196:199], v[80:83]\n"
"v_mfma_f32_16x16x32_bf16 v[80:83], a[148:151], v[188:191], v[80:83]\n"
"v_mfma_f32_16x16x32_bf16 v[80:83], a[152:155], v[108:111], v[80:83]\n"
"v_mfma_f32_16x16x32_bf16 v[80:83], a[156:159], v[92:95], v[80:83]\n"
"v_mfma_f32_16x16x32_bf16 v[96:99], a[188:191], v[92:95], 0\n"
"v_mfma_f32_16x16x32_bf16 v[96:99], a[184:187], v[108:111], v[96:99]\n"
"v_mfma_f32_16x16x32_bf16 v[96:99], a[180:183], v[188:191], v[96:99]\n"
"v_mfma_f32_16x16x32_bf16 v[96:99], a[176:179], v[196:199], v[96:99]\n"
"v_mfma_f32_16x16x32_bf16 v[96:99], a[172:175], v[192:195], v[96:99]\n"
"v_mfma_f32_16x16x32_bf16 v[96:99], a[168:171], v[144:147], v[96:99]\n"
"s_nop 7\n"
"s_branch .Lmk_p63r\n"
".Lmk_p64s:\n"
";\n"
"s_waitcnt vmcnt(16)\n"
"s_mov_b32 s1, 0x10000\n"
".Lmk_p64l:\n"
"s_add_u32 s96, s57, 0x440000\n"
"buffer_load_dwordx4 v[148:151], v0, s[4:7], s96 offen sc1\n"
"buffer_load_dwordx4 v[180:183], v0, s[4:7], s96 offen offset:1024 sc1\n"
"buffer_load_dwordx4 v[24:27], v0, s[4:7], s96 offen offset:2048 sc1\n"
"buffer_load_dwordx4 v[32:35], v0, s[4:7], s96 offen offset:3072 sc1\n"
"buffer_load_dwordx4 v[44:47], v1, s[4:7], s96 offen sc1\n"
"buffer_load_dwordx4 v[48:51], v1, s[4:7], s96 offen offset:1024 sc1\n"
"buffer_load_dwordx4 v[28:31], v1, s[4:7], s96 offen offset:2048 sc1\n"
"buffer_load_dwordx4 v[56:59], v1, s[4:7], s96 offen offset:3072 sc1\n"
"s_add_u32 s96, s58, 0x440000\n"
"buffer_load_dwordx4 v[36:39], v0, s[4:7], s96 offen sc1\n"
"buffer_load_dwordx4 v[40:43], v0, s[4:7], s96 offen offset:1024 sc1\n"
"buffer_load_dwordx4 v[52:55], v0, s[4:7], s96 offen offset:2048 sc1\n"
"buffer_load_dwordx4 v[60:63], v0, s[4:7], s96 offen offset:3072 sc1\n"
"buffer_load_dwordx4 v[64:67], v1, s[4:7], s96 offen sc1\n"
"buffer_load_dwordx4 v[68:71], v1, s[4:7], s96 offen offset:1024 sc1\n"
"buffer_load_dwordx4 v[72:75], v1, s[4:7], s96 offen offset:2048 sc1\n"
"buffer_load_dwordx4 v[76:79], v1, s[4:7], s96 offen offset:3072 sc1\n"
"s_add_u32 s96, s59, 0x440000\n"
"buffer_load_dwordx4 v[84:87], v0, s[4:7], s96 offen sc1\n"
"buffer_load_dwordx4 v[88:91], v0, s[4:7], s96 offen offset:1024 sc1\n"
"buffer_load_dwordx4 v[100:103], v0, s[4:7], s96 offen offset:2048 sc1\n"
"buffer_load_dwordx4 v[104:107], v0, s[4:7], s96 offen offset:3072 sc1\n"
"buffer_load_dwordx4 v[80:83], v1, s[4:7], s96 offen sc1\n"
"buffer_load_dwordx4 v[116:119], v1, s[4:7], s96 offen offset:1024 sc1\n"
"buffer_load_dwordx4 v[96:99], v1, s[4:7], s96 offen offset:2048 sc1\n"
"buffer_load_dwordx4 v[92:95], v1, s[4:7], s96 offen offset:3072 sc1\n"
"s_add_u32 s96, s56, 0x400000\n"
"buffer_load_dwordx4 v[120:123], v0, s[4:7], s96 offen sc1\n"
"buffer_load_dwordx4 v[124:127], v0, s[4:7], s96 offen offset:1024 sc1\n"
"s_waitcnt vmcnt(0)\n"
"v_max_u32_e32 v255, v148, v149\n"
"v_max3_u32 v255, v255, v150, v151\n"
"v_max3_u32 v255, v255, v180, v181\n"
"v_max3_u32 v255, v255, v182, v183\n"
"v_max3_u32 v255, v255, v24, v25\n"
"v_max3_u32 v255, v255, v26, v27\n"
"v_max3_u32 v255, v255, v32, v33\n"
"v_max3_u32 v255, v255, v34, v35\n"
"v_max3_u32 v255, v255, v44, v45\n"
"v_max3_u32 v255, v255, v46, v47\n"
"v_max3_u32 v255, v255, v48, v49\n"
"v_max3_u32 v255, v255, v50, v51\n"
"v_max3_u32 v255, v255, v28, v29\n"
"v_max3_u32 v255, v255, v30, v31\n"
"v_max3_u32 v255, v255, v56, v57\n"
"v_max3_u32 v255, v255, v58, v59\n"
"v_cmp_eq_u32_e32 vcc, -1, v255\n"
"s_cbranch_vccz .Lmk_p64k\n"
"s_sub_u32 s1, s1, 1\n"
"s_cbranch_scc0 .Lmk_p64l\n"
".Lmk_p64k:\n"
"v_mfma_f32_16x16x32_bf16 v[108:111], a[128:131], v[148:151], 0\n"
"v_mfma_f32_16x16x32_bf16 v[108:111], a[132:135], v[180:183], v[108:111]\n"
"v_mfma_f32_16x16x32_bf16 v[108:111], a[136:139], v[24:27], v[108:111]\n"
"v_mfma_f32_16x16x32_bf16 v[108:111], a[140:143], v[32:35], v[108:111]\n"
"v_mfma_f32_16x16x32_bf16 v[108:111], a[144:147], v[44:47], v[108:111]\n"
"v_mfma_f32_16x16x32_bf16 v[108:111], a[148:151], v[48:51], v[108:111]\n"
"v_mfma_f32_16x16x32_bf16 v[108:111], a[152:155], v[28:31], v[108:111]\n"
"v_mfma_f32_16x16x32_bf16 v[108:111], a[156:159], v[56:59], v[108:111]\n"
"v_mfma_f32_16x16x32_bf16 v[112:115], a[188:191], v[56:59], 0\n"
"v_mfma_f32_16x16x32_bf16 v[112:115], a[184:187], v[28:31], v[112:115]\n"
"v_mfma_f32_16x16x32_bf16 v[112:115], a[180:183], v[48:51], v[112:115]\n"
"v_mfma_f32_16x16x32_bf16 v[112:115], a[176:179], v[44:47], v[112:115]\n"
"v_mfma_f32_16x16x32_bf16 v[112:115], a[172:175], v[32:35], v[112:115]\n"
"v_mfma_f32_16x16x32_bf16 v[112:115], a[168:171], v[24:27], v[112:115]\n"
"s_nop 7\n"
"s_branch .Lmk_p64r\n"
".Lmk_p65s:\n"
";\n"
"s_waitcnt vmcnt(16)\n"
"s_mov_b32 s1, 0x10000\n"
".Lmk_p65l:\n"
"s_add_u32 s96, s58, 0x440000\n"
"buffer_load_dwordx4 v[36:39], v0, s[4:7], s96 offen sc1\n"
"buffer_load_dwordx4 v[40:43], v0, s[4:7], s96 offen offset:1024 sc1\n"
"buffer_load_dwordx4 v[52:55], v0, s[4:7], s96 offen offset:2048 sc1\n"
"buffer_load_dwordx4 v[60:63], v0, s[4:7], s96 offen offset:3072 sc1\n"
"buffer_load_dwordx4 v[64:67], v1, s[4:7], s96 offen sc1\n"
"buffer_load_dwordx4 v[68:71], v1, s[4:7], s96 offen offset:1024 sc1\n"
"buffer_load_dwordx4 v[72:75], v1, s[4:7], s96 offen offset:2048 sc1\n"
"buffer_load_dwordx4 v[76:79], v1, s[4:7], s96 offen offset:3072 sc1\n"
"s_add_u32 s96, s59, 0x440000\n"
"buffer_load_dwordx4 v[84:87], v0, s[4:7], s96 offen sc1\n"
"buffer_load_dwordx4 v[88:91], v0, s[4:7], s96 offen offset:1024 sc1\n"
"buffer_load_dwordx4 v[100:103], v0, s[4:7], s96 offen offset:2048 sc1\n"
"buffer_load_dwordx4 v[104:107], v0, s[4:7], s96 offen offset:3072 sc1\n"
"buffer_load_dwordx4 v[80:83], v1, s[4:7], s96 offen sc1\n"
"buffer_load_dwordx4 v[116:119], v1, s[4:7], s96 offen offset:1024 sc1\n"
"buffer_load_dwordx4 v[96:99], v1, s[4:7], s96 offen offset:2048 sc1\n"
"buffer_load_dwordx4 v[92:95], v1, s[4:7], s96 offen offset:3072 sc1\n"
"s_add_u32 s96, s56, 0x400000\n"
"buffer_load_dwordx4 v[120:123], v0, s[4:7], s96 offen sc1\n"
"buffer_load_dwordx4 v[124:127], v0, s[4:7], s96 offen offset:1024 sc1\n"
"buffer_load_dwordx4 v[108:111], v0, s[4:7], s96 offen offset:2048 sc1\n"
"buffer_load_dwordx4 v[132:135], v0, s[4:7], s96 offen offset:3072 sc1\n"
"buffer_load_dwordx4 v[112:115], v1, s[4:7], s96 offen sc1\n"
"buffer_load_dwordx4 v[28:31], v1, s[4:7], s96 offen offset:1024 sc1\n"
"buffer_load_dwordx4 v[32:35], v1, s[4:7], s96 offen offset:2048 sc1\n"
"buffer_load_dwordx4 v[24:27], v1, s[4:7], s96 offen offset:3072 sc1\n"
"s_add_u32 s96, s57, 0x400000\n"
"buffer_load_dwordx4 v[48:51], v0, s[4:7], s96 offen sc1\n"
"buffer_load_dwordx4 v[56:59], v0, s[4:7], s96 offen offset:1024 sc1\n"
"buffer_load_dwordx4 v[136:139], v0, s[4:7], s96 offen offset:2048 sc1\n"
"s_waitcnt vmcnt(0)\n"
"v_max_u32_e32 v239, v36, v37\n"
"v_max3_u32 v239, v239, v38, v39\n"
"v_max3_u32 v239, v239, v40, v41\n"
"v_max3_u32 v239, v239, v42, v43\n"
"v_max3_u32 v239, v239, v52, v53\n"
"v_max3_u32 v239, v239, v54, v55\n"
"v_max3_u32 v239, v239, v60, v61\n"
"v_max3_u32 v239, v239, v62, v63\n"
"v_max3_u32 v239, v239, v64, v65\n"
"v_max3_u32 v239, v239, v66, v67\n"
"v_max3_u32 v239, v239, v68, v69\n"
"v_max3_u32 v239, v239, v70, v71\n"
"v_max3_u32 v239, v239, v72, v73\n"
"v_max3_u32 v239, v239, v74, v75\n"
"v_max3_u32 v239, v239, v76, v77\n"
"v_max3_u32 v239, v239, v78, v79\n"
"v_cmp_eq_u32_e32 vcc, -1, v239\n"
"s_cbranch_vccz .Lmk_p65k\n"
"s_sub_u32 s1, s1, 1\n"
"s_cbranch_scc0 .Lmk_p65l\n"
".Lmk_p65k:\n"
"v_mfma_f32_16x16x32_bf16 v[44:47], a[128:131], v[36:39], 0\n"
"v_mfma_f32_16x16x32_bf16 v[44:47], a[132:135], v[40:43], v[44:47]\n"
"v_mfma_f32_16x16x32_bf16 v[44:47], a[136:139], v[52:55], v[44:47]\n"
"v_mfma_f32_16x16x32_bf16 v[44:47], a[140:143], v[60:63], v[44:47]\n"
"v_mfma_f32_16x16x32_bf16 v[44:47], a[144:147], v[64:67], v[44:47]\n"
"v_mfma_f32_16x16x32_bf16 v[44:47], a[148:151], v[68:71], v[44:47]\n"
"v_mfma_f32_16x16x32_bf16 v[44:47], a[152:155], v[72:75], v[44:47]\n"
"v_mfma_f32_16x16x32_bf16 v[44:47], a[156:159], v[76:79], v[44:47]\n"
"v_mfma_f32_16x16x32_bf16 v[128:131], a[188:191], v[76:79], 0\n"
"v_mfma_f32_16x16x32_bf16 v[128:131], a[184:187], v[72:75], v[128:131]\n"
"v_mfma_f32_16x16x32_bf16 v[128:131], a[180:183], v[68:71], v[128:131]\n"
"v_mfma_f32_16x16x32_bf16 v[128:131], a[176:179], v[64:67], v[128:131]\n"
"v_mfma_f32_16x16x32_bf16 v[128:131], a[172:175], v[60:63], v[128:131]\n"
"v_mfma_f32_16x16x32_bf16 v[128:131], a[168:171], v[52:55], v[128:131]\n"
"s_nop 7\n"
"s_branch .Lmk_p65r\n"
".Lmk_p66s:\n"
";\n"
"s_waitcnt vmcnt(16)\n"
"s_mov_b32 s1, 0x10000\n"
".Lmk_p66l:\n"
"s_add_u32 s96, s59, 0x440000\n"
"buffer_load_dwordx4 v[84:87], v0, s[4:7], s96 offen sc1\n"
"buffer_load_dwordx4 v[88:91], v0, s[4:7], s96 offen offset:1024 sc1\n"
"buffer_load_dwordx4 v[100:103], v0, s[4:7], s96 offen offset:2048 sc1\n"
"buffer_load_dwordx4 v[104:107], v0, s[4:7], s96 offen offset:3072 sc1\n"
"buffer_load_dwordx4 v[80:83], v1, s[4:7], s96 offen sc1\n"
"buffer_load_dwordx4 v[116:119], v1, s[4:7], s96 offen offset:1024 sc1\n"
"buffer_load_dwordx4 v[96:99], v1, s[4:7], s96 offen offset:2048 sc1\n"
"buffer_load_dwordx4 v[92:95], v1, s[4:7], s96 offen offset:3072 sc1\n"
"s_add_u32 s96, s56, 0x400000\n"
"buffer_load_dwordx4 v[120:123], v0, s[4:7], s96 offen sc1\n"
"buffer_load_dwordx4 v[124:127], v0, s[4:7], s96 offen offset:1024 sc1\n"
"buffer_load_dwordx4 v[108:111], v0, s[4:7], s96 offen offset:2048 sc1\n"
"buffer_load_dwordx4 v[132:135], v0, s[4:7], s96 offen offset:3072 sc1\n"
"buffer_load_dwordx4 v[112:115], v1, s[4:7], s96 offen sc1\n"
"buffer_load_dwordx4 v[28:31], v1, s[4:7], s96 offen offset:1024 sc1\n"
"buffer_load_dwordx4 v[32:35], v1, s[4:7], s96 offen offset:2048 sc1\n"
"buffer_load_dwordx4 v[24:27], v1, s[4:7], s96 offen offset:3072 sc1\n"
"s_add_u32 s96, s57, 0x400000\n"
"buffer_load_dwordx4 v[48:51], v0, s[4:7], s96 offen sc1\n"
"buffer_load_dwordx4 v[56:59], v0, s[4:7], s96 offen offset:1024 sc1\n"
"buffer_load_dwordx4 v[136:139], v0, s[4:7], s96 offen offset:2048 sc1\n"
"buffer_load_dwordx4 v[44:47], v0, s[4:7], s96 offen offset:3072 sc1\n"
"buffer_load_dwordx4 v[160:163], v1, s[4:7], s96 offen sc1\n"
"buffer_load_dwordx4 v[128:131], v1, s[4:7], s96 offen offset:1024 sc1\n"
"buffer_load_dwordx4 v[72:75], v1, s[4:7], s96 offen offset:2048 sc1\n"
"buffer_load_dwordx4 v[60:63], v1, s[4:7], s96 offen offset:3072 sc1\n"
"s_add_u32 s96, s58, 0x400000\n"
"buffer_load_dwordx4 v[36:39], v0, s[4:7], s96 offen sc1\n"
"buffer_load_dwordx4 v[68:71], v0, s[4:7], s96 offen offset:1024 sc1\n"
"buffer_load_dwordx4 v[76:79], v0, s[4:7], s96 offen offset:2048 sc1\n"
"buffer_load_dwordx4 v[148:151], v0, s[4:7], s96 offen offset:3072 sc1\n"
"s_waitcnt vmcnt(0)\n"
"v_max_u32_e32 v255, v84, v85\n"
"v_max3_u32 v255, v255, v86, v87\n"
"v_max3_u32 v255, v255, v88, v89\n"
"v_max3_u32 v255, v255, v90, v91\n"
"v_max3_u32 v255, v255, v100, v101\n"
"v_max3_u32 v255, v255, v102, v103\n"
"v_max3_u32 v255, v255, v104, v105\n"
"v_max3_u32 v255, v255, v106, v107\n"
"v_max3_u32 v255, v255, v80, v81\n"
"v_max3_u32 v255, v255, v82, v83\n"
"v_max3_u32 v255, v255, v116, v117\n"
"v_max3_u32 v255, v255, v118, v119\n"
"v_max3_u32 v255, v255, v96, v97\n"
"v_max3_u32 v255, v255, v98, v99\n"
"v_max3_u32 v255, v255, v92, v93\n"
"v_max3_u32 v255, v255, v94, v95\n"
"v_cmp_eq_u32_e32 vcc, -1, v255\n"
"s_cbranch_vccz .Lmk_p66k\n"
"s_sub_u32 s1, s1, 1\n"
"s_cbranch_scc0 .Lmk_p66l\n"
".Lmk_p66k:\n"
"v_mfma_f32_16x16x32_bf16 v[64:67], a[128:131], v[84:87], v[140:143]\n"
"v_mfma_f32_16x16x32_bf16 v[64:67], a[132:135], v[88:91], v[64:67]\n"
"v_mfma_f32_16x16x32_bf16 v[64:67], a[136:139], v[100:103], v[64:67]\n"
"v_mfma_f32_16x16x32_bf16 v[64:67], a[140:143], v[104:107], v[64:67]\n"
"v_mfma_f32_16x16x32_bf16 v[64:67], a[144:147], v[80:83], v[64:67]\n"
"v_mfma_f32_16x16x32_bf16 v[64:67], a[148:151], v[116:119], v[64:67]\n"
"v_mfma_f32_16x16x32_bf16 v[64:67], a[152:155], v[96:99], v[64:67]\n"
"v_mfma_f32_16x16x32_bf16 v[64:67], a[156:159], v[92:95], v[64:67]\n"
"v_mfma_f32_16x16x32_bf16 v[52:55], a[188:191], v[92:95], v[152:155]\n"
"v_mfma_f32_16x16x32_bf16 v[52:55], a[184:187], v[96:99], v[52:55]\n"
"v_mfma_f32_16x16x32_bf16 v[52:55], a[180:183], v[116:119], v[52:55]\n"
"v_mfma_f32_16x16x32_bf16 v[52:55], a[176:179], v[80:83], v[52:55]\n"
"v_mfma_f32_16x16x32_bf16 v[52:55], a[172:175], v[104:107], v[52:55]\n"
"v_mfma_f32_16x16x32_bf16 v[52:55], a[168:171], v[100:103], v[52:55]\n"
"s_nop 7\n"
"s_branch .Lmk_p66r\n"
".Lmk_p68s:\n"
";\n"
"s_waitcnt vmcnt(16)\n"
"s_mov_b32 s1, 0x10000\n"
".Lmk_p68l:\n"
"s_add_u32 s95, s56, 0x400000\n"
"buffer_load_dwordx4 v[120:123], v0, s[4:7], s95 offen sc1\n"
"buffer_load_dwordx4 v[124:127], v0, s[4:7], s95 offen offset:1024 sc1\n"
"buffer_load_dwordx4 v[108:111], v0, s[4:7], s95 offen offset:2048 sc1\n"
"buffer_load_dwordx4 v[132:135], v0, s[4:7], s95 offen offset:3072 sc1\n"
"buffer_load_dwordx4 v[112:115], v1, s[4:7], s95 offen sc1\n"
"buffer_load_dwordx4 v[28:31], v1, s[4:7], s95 offen offset:1024 sc1\n"
"buffer_load_dwordx4 v[32:35], v1, s[4:7], s95 offen offset:2048 sc1\n"
"buffer_load_dwordx4 v[24:27], v1, s[4:7], s95 offen offset:3072 sc1\n"
"s_add_u32 s95, s57, 0x400000\n"
"buffer_load_dwordx4 v[48:51], v0, s[4:7], s95 offen sc1\n"
"buffer_load_dwordx4 v[56:59], v0, s[4:7], s95 offen offset:1024 sc1\n"
"buffer_load_dwordx4 v[136:139], v0, s[4:7], s95 offen offset:2048 sc1\n"
"buffer_load_dwordx4 v[44:47], v0, s[4:7], s95 offen offset:3072 sc1\n"
"buffer_load_dwordx4 v[160:163], v1, s[4:7], s95 offen sc1\n"
"buffer_load_dwordx4 v[128:131], v1, s[4:7], s95 offen offset:1024 sc1\n"
"buffer_load_dwordx4 v[72:75], v1, s[4:7], s95 offen offset:2048 sc1\n"
"buffer_load_dwordx4 v[60:63], v1, s[4:7], s95 offen offset:3072 sc1\n"
"s_add_u32 s95, s58, 0x400000\n"
"buffer_load_dwordx4 v[36:39], v0, s[4:7], s95 offen sc1\n"
"buffer_load_dwordx4 v[68:71], v0, s[4:7], s95 offen offset:1024 sc1\n"
"buffer_load_dwordx4 v[76:79], v0, s[4:7], s95 offen offset:2048 sc1\n"
"buffer_load_dwordx4 v[148:151], v0, s[4:7], s95 offen offset:3072 sc1\n"
"buffer_load_dwordx4 v[152:155], v1, s[4:7], s95 offen sc1\n"
"buffer_load_dwordx4 v[172:175], v1, s[4:7], s95 offen offset:1024 sc1\n"
"buffer_load_dwordx4 v[176:179], v1, s[4:7], s95 offen offset:2048 sc1\n"
"buffer_load_dwordx4 v[64:67], v1, s[4:7], s95 offen offset:3072 sc1\n"
"s_add_u32 s95, s59, 0x400000\n"
"buffer_load_dwordx4 v[80:83], v0, s[4:7], s95 offen sc1\n"
"buffer_load_dwordx4 v[84:87], v0, s[4:7], s95 offen offset:1024 sc1\n"
"buffer_load_dwordx4 v[88:91], v0, s[4:7], s95 offen offset:2048 sc1\n"
"buffer_load_dwordx4 v[92:95], v0, s[4:7], s95 offen offset:3072 sc1\n"
"buffer_load_dwordx4 v[96:99], v1, s[4:7], s95 offen sc1\n"
"buffer_load_dwordx4 v[100:103], v1, s[4:7], s95 offen offset:1024 sc1\n"
"buffer_load_dwordx4 v[104:107], v1, s[4:7], s95 offen offset:2048 sc1\n"
"buffer_load_dwordx4 v[116:119], v1, s[4:7], s95 offen offset:3072 sc1\n"
"s_waitcnt vmcnt(0)\n"
"v_max_u32_e32 v255, v120, v121\n"
"v_max3_u32 v255, v255, v122, v123\n"
"v_max3_u32 v255, v255, v124, v125\n"
"v_max3_u32 v255, v255, v126, v127\n"
"v_max3_u32 v255, v255, v108, v109\n"
"v_max3_u32 v255, v255, v110, v111\n"
"v_max3_u32 v255, v255, v132, v133\n"
"v_max3_u32 v255, v255, v134, v135\n"
"v_max3_u32 v255, v255, v112, v113\n"
"v_max3_u32 v255, v255, v114, v115\n"
"v_max3_u32 v255, v255, v28, v29\n"
"v_max3_u32 v255, v255, v30, v31\n"
"v_max3_u32 v255, v255, v32, v33\n"
"v_max3_u32 v255, v255, v34, v35\n"
"v_max3_u32 v255, v255, v24, v25\n"
"v_max3_u32 v255, v255, v26, v27\n"
"v_cmp_eq_u32_e32 vcc, -1, v255\n"
"s_cbranch_vccz .Lmk_p68k\n"
"s_sub_u32 s1, s1, 1\n"
"s_cbranch_scc0 .Lmk_p68l\n"
".Lmk_p68k:\n"
"v_mfma_f32_16x16x32_bf16 v[140:143], a[128:131], v[120:123], 0\n"
"v_mfma_f32_16x16x32_bf16 v[140:143], a[132:135], v[124:127], v[140:143]\n"
"v_mfma_f32_16x16x32_bf16 v[140:143], a[136:139], v[108:111], v[140:143]\n"
"v_mfma_f32_16x16x32_bf16 v[140:143], a[140:143], v[132:135], v[140:143]\n"
"v_mfma_f32_16x16x32_bf16 v[140:143], a[144:147], v[112:115], v[140:143]\n"
"v_mfma_f32_16x16x32_bf16 v[140:143], a[148:151], v[28:31], v[140:143]\n"
"v_mfma_f32_16x16x32_bf16 v[140:143], a[152:155], v[32:35], v[140:143]\n"
"v_mfma_f32_16x16x32_bf16 v[140:143], a[156:159], v[24:27], v[140:143]\n"
"v_mfma_f32_16x16x32_bf16 v[40:43], a[188:191], v[24:27], 0\n"
"v_mfma_f32_16x16x32_bf16 v[40:43], a[184:187], v[32:35], v[40:43]\n"
"v_mfma_f32_16x16x32_bf16 v[40:43], a[180:183], v[28:31], v[40:43]\n"
"v_mfma_f32_16x16x32_bf16 v[40:43], a[176:179], v[112:115], v[40:43]\n"
"v_mfma_f32_16x16x32_bf16 v[40:43], a[172:175], v[132:135], v[40:43]\n"
"v_mfma_f32_16x16x32_bf16 v[40:43], a[168:171], v[108:111], v[40:43]\n"
"s_nop 7\n"
"s_branch .Lmk_p68r\n"
".Lmk_p69s:\n"
";\n"
"s_waitcnt vmcnt(16)\n"
"s_mov_b32 s1, 0x10000\n"
".Lmk_p69l:\n"
"s_add_u32 s97, s57, 0x400000\n"
"buffer_load_dwordx4 v[48:51], v0, s[4:7], s97 offen sc1\n"
"buffer_load_dwordx4 v[56:59], v0, s[4:7], s97 offen offset:1024 sc1\n"
"buffer_load_dwordx4 v[136:139], v0, s[4:7], s97 offen offset:2048 sc1\n"
"buffer_load_dwordx4 v[44:47], v0, s[4:7], s97 offen offset:3072 sc1\n"
"buffer_load_dwordx4 v[160:163], v1, s[4:7], s97 offen sc1\n"
"buffer_load_dwordx4 v[128:131], v1, s[4:7], s97 offen offset:1024 sc1\n"
"buffer_load_dwordx4 v[72:75], v1, s[4:7], s97 offen offset:2048 sc1\n"
"buffer_load_dwordx4 v[60:63], v1, s[4:7], s97 offen offset:3072 sc1\n"
"s_add_u32 s97, s58, 0x400000\n"
"buffer_load_dwordx4 v[36:39], v0, s[4:7], s97 offen sc1\n"
"buffer_load_dwordx4 v[68:71], v0, s[4:7], s97 offen offset:1024 sc1\n"
"buffer_load_dwordx4 v[76:79], v0, s[4:7], s97 offen offset:2048 sc1\n"
"buffer_load_dwordx4 v[148:151], v0, s[4:7], s97 offen offset:3072 sc1\n"
"buffer_load_dwordx4 v[152:155], v1, s[4:7], s97 offen sc1\n"
"buffer_load_dwordx4 v[172:175], v1, s[4:7], s97 offen offset:1024 sc1\n"
"buffer_load_dwordx4 v[176:179], v1, s[4:7], s97 offen offset:2048 sc1\n"
"buffer_load_dwordx4 v[64:67], v1, s[4:7], s97 offen offset:3072 sc1\n"
"s_add_u32 s97, s59, 0x400000\n"
"buffer_load_dwordx4 v[80:83], v0, s[4:7], s97 offen sc1\n"
"buffer_load_dwordx4 v[84:87], v0, s[4:7], s97 offen offset:1024 sc1\n"
"buffer_load_dwordx4 v[88:91], v0, s[4:7], s97 offen offset:2048 sc1\n"
"buffer_load_dwordx4 v[92:95], v0, s[4:7], s97 offen offset:3072 sc1\n"
"buffer_load_dwordx4 v[96:99], v1, s[4:7], s97 offen sc1\n"
"buffer_load_dwordx4 v[100:103], v1, s[4:7], s97 offen offset:1024 sc1\n"
"buffer_load_dwordx4 v[104:107], v1, s[4:7], s97 offen offset:2048 sc1\n"
"buffer_load_dwordx4 v[116:119], v1, s[4:7], s97 offen offset:3072 sc1\n"
"s_waitcnt vmcnt(0)\n"
"v_max_u32_e32 v255, v48, v49\n"
"v_max3_u32 v255, v255, v50, v51\n"
"v_max3_u32 v255, v255, v56, v57\n"
"v_max3_u32 v255, v255, v58, v59\n"
"v_max3_u32 v255, v255, v136, v137\n"
"v_max3_u32 v255, v255, v138, v139\n"
"v_max3_u32 v255, v255, v44, v45\n"
"v_max3_u32 v255, v255, v46, v47\n"
"v_max3_u32 v255, v255, v160, v161\n"
"v_max3_u32 v255, v255, v162, v163\n"
"v_max3_u32 v255, v255, v128, v129\n"
"v_max3_u32 v255, v255, v130, v131\n"
"v_max3_u32 v255, v255, v72, v73\n"
"v_max3_u32 v255, v255, v74, v75\n"
"v_max3_u32 v255, v255, v60, v61\n"
"v_max3_u32 v255, v255, v62, v63\n"
"v_cmp_eq_u32_e32 vcc, -1, v255\n"
"s_cbranch_vccz .Lmk_p69k\n"
"s_sub_u32 s1, s1, 1\n"
"s_cbranch_scc0 .Lmk_p69l\n"
".Lmk_p69k:\n"
"v_mfma_f32_16x16x32_bf16 v[24:27], a[128:131], v[48:51], 0\n"
"v_mfma_f32_16x16x32_bf16 v[24:27], a[132:135], v[56:59], v[24:27]\n"
"v_mfma_f32_16x16x32_bf16 v[24:27], a[136:139], v[136:139], v[24:27]\n"
"v_mfma_f32_16x16x32_bf16 v[24:27], a[140:143], v[44:47], v[24:27]\n"
"v_mfma_f32_16x16x32_bf16 v[24:27], a[144:147], v[160:163], v[24:27]\n"
"v_mfma_f32_16x16x32_bf16 v[24:27], a[148:151], v[128:131], v[24:27]\n"
"v_mfma_f32_16x16x32_bf16 v[24:27], a[152:155], v[72:75], v[24:27]\n"
"v_mfma_f32_16x16x32_bf16 v[24:27], a[156:159], v[60:63], v[24:27]\n"
"v_mfma_f32_16x16x32_bf16 v[28:31], a[188:191], v[60:63], 0\n"
"v_mfma_f32_16x16x32_bf16 v[28:31], a[184:187], v[72:75], v[28:31]\n"
"v_mfma_f32_16x16x32_bf16 v[28:31], a[180:183], v[128:131], v[28:31]\n"
"v_mfma_f32_16x16x32_bf16 v[28:31], a[176:179], v[160:163], v[28:31]\n"
"v_mfma_f32_16x16x32_bf16 v[28:31], a[172:175], v[44:47], v[28:31]\n"
"v_mfma_f32_16x16x32_bf16 v[28:31], a[168:171], v[136:139], v[28:31]\n"
"s_nop 7\n"
"s_branch .Lmk_p69r\n"
".Lmk_p70s:\n"
";\n"
"s_waitcnt vmcnt(16)\n"
"s_mov_b32 s1, 0x10000\n"
".Lmk_p70l:\n"
"s_add_u32 s97, s58, 0x400000\n"
"buffer_load_dwordx4 v[36:39], v0, s[4:7], s97 offen sc1\n"
"buffer_load_dwordx4 v[68:71], v0, s[4:7], s97 offen offset:1024 sc1\n"
"buffer_load_dwordx4 v[76:79], v0, s[4:7], s97 offen offset:2048 sc1\n"
"buffer_load_dwordx4 v[148:151], v0, s[4:7], s97 offen offset:3072 sc1\n"
"buffer_load_dwordx4 v[152:155], v1, s[4:7], s97 offen sc1\n"
"buffer_load_dwordx4 v[172:175], v1, s[4:7], s97 offen offset:1024 sc1\n"
"buffer_load_dwordx4 v[176:179], v1, s[4:7], s97 offen offset:2048 sc1\n"
"buffer_load_dwordx4 v[64:67], v1, s[4:7], s97 offen offset:3072 sc1\n"
"s_add_u32 s97, s59, 0x400000\n"
"buffer_load_dwordx4 v[80:83], v0, s[4:7], s97 offen sc1\n"
"buffer_load_dwordx4 v[84:87], v0, s[4:7], s97 offen offset:1024 sc1\n"
"buffer_load_dwordx4 v[88:91], v0, s[4:7], s97 offen offset:2048 sc1\n"
"buffer_load_dwordx4 v[92:95], v0, s[4:7], s97 offen offset:3072 sc1\n"
"buffer_load_dwordx4 v[96:99], v1, s[4:7], s97 offen sc1\n"
"buffer_load_dwordx4 v[100:103], v1, s[4:7], s97 offen offset:1024 sc1\n"
"buffer_load_dwordx4 v[104:107], v1, s[4:7], s97 offen offset:2048 sc1\n"
"buffer_load_dwordx4 v[116:119], v1, s[4:7], s97 offen offset:3072 sc1\n"
"s_waitcnt vmcnt(0)\n"
"v_max_u32_e32 v239, v36, v37\n"
"v_max3_u32 v239, v239, v38, v39\n"
"v_max3_u32 v239, v239, v68, v69\n"
"v_max3_u32 v239, v239, v70, v71\n"
"v_max3_u32 v239, v239, v76, v77\n"
"v_max3_u32 v239, v239, v78, v79\n"
"v_max3_u32 v239, v239, v148, v149\n"
"v_max3_u32 v239, v239, v150, v151\n"
"v_max3_u32 v239, v239, v152, v153\n"
"v_max3_u32 v239, v239, v154, v155\n"
"v_max3_u32 v239, v239, v172, v173\n"
"v_max3_u32 v239, v239, v174, v175\n"
"v_max3_u32 v239, v239, v176, v177\n"
"v_max3_u32 v239, v239, v178, v179\n"
"v_max3_u32 v239, v239, v64, v65\n"
"v_max3_u32 v239, v239, v66, v67\n"
"v_cmp_eq_u32_e32 vcc, -1, v239\n"
"s_cbranch_vccz .Lmk_p70k\n"
"s_sub_u32 s1, s1, 1\n"
"s_cbranch_scc0 .Lmk_p70l\n"
".Lmk_p70k:\n"
"v_mfma_f32_16x16x32_bf16 v[24:27], a[128:131], v[36:39], 0\n"
"v_mfma_f32_16x16x32_bf16 v[24:27], a[132:135], v[68:71], v[24:27]\n"
"v_mfma_f32_16x16x32_bf16 v[24:27], a[136:139], v[76:79], v[24:27]\n"
"v_mfma_f32_16x16x32_bf16 v[24:27], a[140:143], v[148:151], v[24:27]\n"
"v_mfma_f32_16x16x32_bf16 v[24:27], a[144:147], v[152:155], v[24:27]\n"
"v_mfma_f32_16x16x32_bf16 v[24:27], a[148:151], v[172:175], v[24:27]\n"
"v_mfma_f32_16x16x32_bf16 v[24:27], a[152:155], v[176:179], v[24:27]\n"
"v_mfma_f32_16x16x32_bf16 v[24:27], a[156:159], v[64:67], v[24:27]\n"
"v_mfma_f32_16x16x32_bf16 v[32:35], a[188:191], v[64:67], 0\n"
"v_mfma_f32_16x16x32_bf16 v[32:35], a[184:187], v[176:179], v[32:35]\n"
"v_mfma_f32_16x16x32_bf16 v[32:35], a[180:183], v[172:175], v[32:35]\n"
"v_mfma_f32_16x16x32_bf16 v[32:35], a[176:179], v[152:155], v[32:35]\n"
"v_mfma_f32_16x16x32_bf16 v[32:35], a[172:175], v[148:151], v[32:35]\n"
"v_mfma_f32_16x16x32_bf16 v[32:35], a[168:171], v[76:79], v[32:35]\n"
"s_nop 7\n"
"s_branch .Lmk_p70r\n"
".Lmk_p71s:\n"
";\n"
"s_waitcnt vmcnt(16)\n"
"s_mov_b32 s1, 0x10000\n"
".Lmk_p71l:\n"
"s_add_u32 s97, s59, 0x400000\n"
"buffer_load_dwordx4 v[80:83], v0, s[4:7], s97 offen sc1\n"
"buffer_load_dwordx4 v[84:87], v0, s[4:7], s97 offen offset:1024 sc1\n"
"buffer_load_dwordx4 v[88:91], v0, s[4:7], s97 offen offset:2048 sc1\n"
"buffer_load_dwordx4 v[92:95], v0, s[4:7], s97 offen offset:3072 sc1\n"
"buffer_load_dwordx4 v[96:99], v1, s[4:7], s97 offen sc1\n"
"buffer_load_dwordx4 v[100:103], v1, s[4:7], s97 offen offset:1024 sc1\n"
"buffer_load_dwordx4 v[104:107], v1, s[4:7], s97 offen offset:2048 sc1\n"
"buffer_load_dwordx4 v[116:119], v1, s[4:7], s97 offen offset:3072 sc1\n"
"s_waitcnt vmcnt(0)\n"
"v_max_u32_e32 v255, v80, v81\n"
"v_max3_u32 v255, v255, v82, v83\n"
"v_max3_u32 v255, v255, v84, v85\n"
"v_max3_u32 v255, v255, v86, v87\n"
"v_max3_u32 v255, v255, v88, v89\n"
"v_max3_u32 v255, v255, v90, v91\n"
"v_max3_u32 v255, v255, v92, v93\n"
"v_max3_u32 v255, v255, v94, v95\n"
"v_max3_u32 v255, v255, v96, v97\n"
"v_max3_u32 v255, v255, v98, v99\n"
"v_max3_u32 v255, v255, v100, v101\n"
"v_max3_u32 v255, v255, v102, v103\n"
"v_max3_u32 v255, v255, v104, v105\n"
"v_max3_u32 v255, v255, v106, v107\n"
"v_max3_u32 v255, v255, v116, v117\n"
"v_max3_u32 v255, v255, v118, v119\n"
"v_cmp_eq_u32_e32 vcc, -1, v255\n"
"s_cbranch_vccz .Lmk_p71k\n"
"s_sub_u32 s1, s1, 1\n"
"s_cbranch_scc0 .Lmk_p71l\n"
".Lmk_p71k:\n"
"v_mfma_f32_16x16x32_bf16 v[76:79], a[128:131], v[80:83], v[44:47]\n"
"v_mfma_f32_16x16x32_bf16 v[76:79], a[132:135], v[84:87], v[76:79]\n"
"v_mfma_f32_16x16x32_bf16 v[76:79], a[136:139], v[88:91], v[76:79]\n"
"v_mfma_f32_16x16x32_bf16 v[76:79], a[140:143], v[92:95], v[76:79]\n"
"v_mfma_f32_16x16x32_bf16 v[76:79], a[144:147], v[96:99], v[76:79]\n"
"v_mfma_f32_16x16x32_bf16 v[76:79], a[148:151], v[100:103], v[76:79]\n"
"v_mfma_f32_16x16x32_bf16 v[76:79], a[152:155], v[104:107], v[76:79]\n"
"v_mfma_f32_16x16x32_bf16 v[76:79], a[156:159], v[116:119], v[76:79]\n"
"v_mfma_f32_16x16x32_bf16 v[68:71], a[188:191], v[116:119], v[56:59]\n"
"v_mfma_f32_16x16x32_bf16 v[68:71], a[184:187], v[104:107], v[68:71]\n"
"v_mfma_f32_16x16x32_bf16 v[68:71], a[180:183], v[100:103], v[68:71]\n"
"v_mfma_f32_16x16x32_bf16 v[68:71], a[176:179], v[96:99], v[68:71]\n"
"v_mfma_f32_16x16x32_bf16 v[68:71], a[172:175], v[92:95], v[68:71]\n"
"v_mfma_f32_16x16x32_bf16 v[68:71], a[168:171], v[88:91], v[68:71]\n"
"s_nop 7\n"
"s_branch .Lmk_p71r\n"
".Lmk_p76s:\n"
";\n"
"s_waitcnt vmcnt(16)\n"
"s_mov_b32 s1, 0x10000\n"
".Lmk_p76l:\n"
"s_add_u32 s96, s52, 0x40000\n"
"buffer_load_dwordx4 v[96:99], v0, s[4:7], s96 offen sc1\n"
"buffer_load_dwordx4 v[88:91], v0, s[4:7], s96 offen offset:1024 sc1\n"
"buffer_load_dwordx4 v[80:83], v0, s[4:7], s96 offen offset:2048 sc1\n"
"buffer_load_dwordx4 v[84:87], v0, s[4:7], s96 offen offset:3072 sc1\n"
"buffer_load_dwordx4 v[92:95], v1, s[4:7], s96 offen sc1\n"
"buffer_load_dwordx4 v[104:107], v1, s[4:7], s96 offen offset:1024 sc1\n"
"buffer_load_dwordx4 v[116:119], v1, s[4:7], s96 offen offset:2048 sc1\n"
"buffer_load_dwordx4 v[124:127], v1, s[4:7], s96 offen offset:3072 sc1\n"
"s_add_u32 s96, s53, 0x40000\n"
"buffer_load_dwordx4 v[128:131], v0, s[4:7], s96 offen sc1\n"
"buffer_load_dwordx4 v[144:147], v0, s[4:7], s96 offen offset:1024 sc1\n"
"buffer_load_dwordx4 v[148:151], v0, s[4:7], s96 offen offset:2048 sc1\n"
"buffer_load_dwordx4 v[152:155], v0, s[4:7], s96 offen offset:3072 sc1\n"
"buffer_load_dwordx4 v[156:159], v1, s[4:7], s96 offen sc1\n"
"buffer_load_dwordx4 v[160:163], v1, s[4:7], s96 offen offset:1024 sc1\n"
"buffer_load_dwordx4 v[164:167], v1, s[4:7], s96 offen offset:2048 sc1\n"
"buffer_load_dwordx4 v[168:171], v1, s[4:7], s96 offen offset:3072 sc1\n"
"s_add_u32 s96, s54, 0x40000\n"
"buffer_load_dwordx4 v[176:179], v0, s[4:7], s96 offen sc1\n"
"buffer_load_dwordx4 v[48:51], v0, s[4:7], s96 offen offset:1024 sc1\n"
"s_waitcnt vmcnt(0)\n"
"v_max_u32_e32 v252, v96, v97\n"
"v_max3_u32 v252, v252, v98, v99\n"
"v_max3_u32 v252, v252, v88, v89\n"
"v_max3_u32 v252, v252, v90, v91\n"
"v_max3_u32 v252, v252, v80, v81\n"
"v_max3_u32 v252, v252, v82, v83\n"
"v_max3_u32 v252, v252, v84, v85\n"
"v_max3_u32 v252, v252, v86, v87\n"
"v_max3_u32 v252, v252, v92, v93\n"
"v_max3_u32 v252, v252, v94, v95\n"
"v_max3_u32 v252, v252, v104, v105\n"
"v_max3_u32 v252, v252, v106, v107\n"
"v_max3_u32 v252, v252, v116, v117\n"
"v_max3_u32 v252, v252, v118, v119\n"
"v_max3_u32 v252, v252, v124, v125\n"
"v_max3_u32 v252, v252, v126, v127\n"
"v_cmp_eq_u32_e32 vcc, -1, v252\n"
"s_cbranch_vccz .Lmk_p76k\n"
"s_sub_u32 s1, s1, 1\n"
"s_cbranch_scc0 .Lmk_p76l\n"
".Lmk_p76k:\n"
"v_mfma_f32_16x16x32_bf16 v[172:175], a[0:3], v[96:99], 0\n"
"v_mfma_f32_16x16x32_bf16 v[172:175], a[4:7], v[88:91], v[172:175]\n"
"v_mfma_f32_16x16x32_bf16 v[172:175], a[8:11], v[80:83], v[172:175]\n"
"v_mfma_f32_16x16x32_bf16 v[172:175], a[12:15], v[84:87], v[172:175]\n"
"v_mfma_f32_16x16x32_bf16 v[172:175], a[16:19], v[92:95], v[172:175]\n"
"v_mfma_f32_16x16x32_bf16 v[172:175], a[20:23], v[104:107], v[172:175]\n"
"v_mfma_f32_16x16x32_bf16 v[172:175], a[24:27], v[116:119], v[172:175]\n"
"v_mfma_f32_16x16x32_bf16 v[172:175], a[28:31], v[124:127], v[172:175]\n"
"v_mfma_f32_16x16x32_bf16 v[180:183], a[60:63], v[124:127], 0\n"
"v_mfma_f32_16x16x32_bf16 v[180:183], a[56:59], v[116:119], v[180:183]\n"
"v_mfma_f32_16x16x32_bf16 v[180:183], a[52:55], v[104:107], v[180:183]\n"
"v_mfma_f32_16x16x32_bf16 v[180:183], a[48:51], v[92:95], v[180:183]\n"
"v_mfma_f32_16x16x32_bf16 v[180:183], a[44:47], v[84:87], v[180:183]\n"
"v_mfma_f32_16x16x32_bf16 v[180:183], a[40:43], v[80:83], v[180:183]\n"
"s_nop 7\n"
"s_branch .Lmk_p76r\n"
".Lmk_p77s:\n"
";\n"
"s_waitcnt vmcnt(16)\n"
"s_mov_b32 s1, 0x10000\n"
".Lmk_p77l:\n"
"s_add_u32 s97, s53, 0x40000\n"
"buffer_load_dwordx4 v[128:131], v0, s[4:7], s97 offen sc1\n"
"buffer_load_dwordx4 v[144:147], v0, s[4:7], s97 offen offset:1024 sc1\n"
"buffer_load_dwordx4 v[148:151], v0, s[4:7], s97 offen offset:2048 sc1\n"
"buffer_load_dwordx4 v[152:155], v0, s[4:7], s97 offen offset:3072 sc1\n"
"buffer_load_dwordx4 v[156:159], v1, s[4:7], s97 offen sc1\n"
"buffer_load_dwordx4 v[160:163], v1, s[4:7], s97 offen offset:1024 sc1\n"
"buffer_load_dwordx4 v[164:167], v1, s[4:7], s97 offen offset:2048 sc1\n"
"buffer_load_dwordx4 v[168:171], v1, s[4:7], s97 offen offset:3072 sc1\n"
"s_add_u32 s97, s54, 0x40000\n"
"buffer_load_dwordx4 v[176:179], v0, s[4:7], s97 offen sc1\n"
"buffer_load_dwordx4 v[48:51], v0, s[4:7], s97 offen offset:1024 sc1\n"
"buffer_load_dwordx4 v[132:135], v0, s[4:7], s97 offen offset:2048 sc1\n"
"buffer_load_dwordx4 v[184:187], v0, s[4:7], s97 offen offset:3072 sc1\n"
"buffer_load_dwordx4 v[44:47], v1, s[4:7], s97 offen sc1\n"
"buffer_load_dwordx4 v[192:195], v1, s[4:7], s97 offen offset:1024 sc1\n"
"buffer_load_dwordx4 v[196:199], v1, s[4:7], s97 offen offset:2048 sc1\n"
"buffer_load_dwordx4 v[200:203], v1, s[4:7], s97 offen offset:3072 sc1\n"
"s_add_u32 s97, s55, 0x40000\n"
"buffer_load_dwordx4 v[80:83], v0, s[4:7], s97 offen sc1\n"
"buffer_load_dwordx4 v[84:87], v0, s[4:7], s97 offen offset:1024 sc1\n"
"s_waitcnt vmcnt(0)\n"
"v_max_u32_e32 v247, v128, v129\n"
"v_max3_u32 v247, v247, v130, v131\n"
"v_max3_u32 v247, v247, v144, v145\n"
"v_max3_u32 v247, v247, v146, v147\n"
"v_max3_u32 v247, v247, v148, v149\n"
"v_max3_u32 v247, v247, v150, v151\n"
"v_max3_u32 v247, v247, v152, v153\n"
"v_max3_u32 v247, v247, v154, v155\n"
"v_max3_u32 v247, v247, v156, v157\n"
"v_max3_u32 v247, v247, v158, v159\n"
"v_max3_u32 v247, v247, v160, v161\n"
"v_max3_u32 v247, v247, v162, v163\n"
"v_max3_u32 v247, v247, v164, v165\n"
"v_max3_u32 v247, v247, v166, v167\n"
"v_max3_u32 v247, v247, v168, v169\n"
"v_max3_u32 v247, v247, v170, v171\n"
"v_cmp_eq_u32_e32 vcc, -1, v247\n"
"s_cbranch_vccz .Lmk_p77k\n"
"s_sub_u32 s1, s1, 1\n"
"s_cbranch_scc0 .Lmk_p77l\n"
".Lmk_p77k:\n"
"v_mfma_f32_16x16x32_bf16 v[68:71], a[0:3], v[128:131], 0\n"
"v_mfma_f32_16x16x32_bf16 v[68:71], a[4:7], v[144:147], v[68:71]\n"
"v_mfma_f32_16x16x32_bf16 v[68:71], a[8:11], v[148:151], v[68:71]\n"
"v_mfma_f32_16x16x32_bf16 v[68:71], a[12:15], v[152:155], v[68:71]\n"
"v_mfma_f32_16x16x32_bf16 v[68:71], a[16:19], v[156:159], v[68:71]\n"
"v_mfma_f32_16x16x32_bf16 v[68:71], a[20:23], v[160:163], v[68:71]\n"
"v_mfma_f32_16x16x32_bf16 v[68:71], a[24:27], v[164:167], v[68:71]\n"
"v_mfma_f32_16x16x32_bf16 v[68:71], a[28:31], v[168:171], v[68:71]\n"
"v_mfma_f32_16x16x32_bf16 v[88:91], a[60:63], v[168:171], 0\n"
"v_mfma_f32_16x16x32_bf16 v[88:91], a[56:59], v[164:167], v[88:91]\n"
"v_mfma_f32_16x16x32_bf16 v[88:91], a[52:55], v[160:163], v[88:91]\n"
"v_mfma_f32_16x16x32_bf16 v[88:91], a[48:51], v[156:159], v[88:91]\n"
"v_mfma_f32_16x16x32_bf16 v[88:91], a[44:47], v[152:155], v[88:91]\n"
"v_mfma_f32_16x16x32_bf16 v[88:91], a[40:43], v[148:151], v[88:91]\n"
"s_nop 7\n"
"s_branch .Lmk_p77r\n"
".Lmk_m80s:\n"
";\n"
"s_waitcnt vmcnt(16)\n"
"s_mov_b32 s1, 0x10000\n"
".Lmk_m80l:\n"
"s_add_u32 s98, s62, 0x4000\n"
"buffer_load_dword v255, v10, s[4:7], s98 offen sc1\n"
"buffer_load_dword v254, v10, s[4:7], s98 offen offset:256 sc1\n"
"buffer_load_dword v253, v10, s[4:7], s98 offen offset:512 sc1\n"
"buffer_load_dword v252, v10, s[4:7], s98 offen offset:768 sc1\n"
"buffer_load_dword v251, v10, s[4:7], s98 offen offset:1024 sc1\n"
"buffer_load_dword v250, v10, s[4:7], s98 offen offset:1280 sc1\n"
"buffer_load_dword v249, v10, s[4:7], s98 offen offset:1536 sc1\n"
"buffer_load_dword v248, v10, s[4:7], s98 offen offset:1792 sc1\n"
"s_waitcnt vmcnt(0)\n"
"v_max_u32_e32 v247, v255, v254\n"
"v_max3_u32 v247, v247, v253, v252\n"
"v_max3_u32 v247, v247, v251, v250\n"
"v_max3_u32 v247, v247, v249, v248\n"
"v_cmp_eq_u32_e32 vcc, -1, v247\n"
"s_cbranch_vccz .Lmk_m80k\n"
"s_sub_u32 s1, s1, 1\n"
"s_cbranch_scc0 .Lmk_m80l\n"
".Lmk_m80k:\n"
"v_add_f32_e32 v255, v255, v251\n"
"v_add_f32_e32 v254, v254, v250\n"
"v_add_f32_e32 v253, v253, v249\n"
"v_add_f32_e32 v252, v252, v248\n"
"v_add_f32_e32 v255, v255, v253\n"
"v_add_f32_e32 v254, v254, v252\n"
"v_add_f32_e32 v255, v255, v254\n"
"s_nop 1\n"
"s_branch .Lmk_m80r\n"
".Lmk_p78s:\n"
";\n"
"s_waitcnt vmcnt(16)\n"
"s_mov_b32 s1, 0x10000\n"
".Lmk_p78l:\n"
"s_add_u32 s97, s54, 0x40000\n"
"buffer_load_dwordx4 v[176:179], v0, s[4:7], s97 offen sc1\n"
"buffer_load_dwordx4 v[48:51], v0, s[4:7], s97 offen offset:1024 sc1\n"
"buffer_load_dwordx4 v[132:135], v0, s[4:7], s97 offen offset:2048 sc1\n"
"buffer_load_dwordx4 v[184:187], v0, s[4:7], s97 offen offset:3072 sc1\n"
"buffer_load_dwordx4 v[44:47], v1, s[4:7], s97 offen sc1\n"
"buffer_load_dwordx4 v[192:195], v1, s[4:7], s97 offen offset:1024 sc1\n"
"buffer_load_dwordx4 v[196:199], v1, s[4:7], s97 offen offset:2048 sc1\n"
"buffer_load_dwordx4 v[200:203], v1, s[4:7], s97 offen offset:3072 sc1\n"
"s_add_u32 s97, s55, 0x40000\n"
"buffer_load_dwordx4 v[80:83], v0, s[4:7], s97 offen sc1\n"
"buffer_load_dwordx4 v[84:87], v0, s[4:7], s97 offen offset:1024 sc1\n"
"buffer_load_dwordx4 v[92:95], v0, s[4:7], s97 offen offset:2048 sc1\n"
"buffer_load_dwordx4 v[96:99], v0, s[4:7], s97 offen offset:3072 sc1\n"
"buffer_load_dwordx4 v[88:91], v1, s[4:7], s97 offen sc1\n"
"buffer_load_dwordx4 v[116:119], v1, s[4:7], s97 offen offset:1024 sc1\n"
"buffer_load_dwordx4 v[124:127], v1, s[4:7], s97 offen offset:2048 sc1\n"
"buffer_load_dwordx4 v[172:175], v1, s[4:7], s97 offen offset:3072 sc1\n"
"s_add_u32 s97, s52, 0x48000\n"
"buffer_load_dwordx4 v[128:131], v0, s[4:7], s97 offen sc1\n"
"s_waitcnt vmcnt(0)\n"
"v_max_u32_e32 v252, v176, v177\n"
"v_max3_u32 v252, v252, v178, v179\n"
"v_max3_u32 v252, v252, v48, v49\n"
"v_max3_u32 v252, v252, v50, v51\n"
"v_max3_u32 v252, v252, v132, v133\n"
"v_max3_u32 v252, v252, v134, v135\n"
"v_max3_u32 v252, v252, v184, v185\n"
"v_max3_u32 v252, v252, v186, v187\n"
"v_max3_u32 v252, v252, v44, v45\n"
"v_max3_u32 v252, v252, v46, v47\n"
"v_max3_u32 v252, v252, v192, v193\n"
"v_max3_u32 v252, v252, v194, v195\n"
"v_max3_u32 v252, v252, v196, v197\n"
"v_max3_u32 v252, v252, v198, v199\n"
"v_max3_u32 v252, v252, v200, v201\n"
"v_max3_u32 v252, v252, v202, v203\n"
"v_cmp_eq_u32_e32 vcc, -1, v252\n"
"s_cbranch_vccz .Lmk_p78k\n"
"s_sub_u32 s1, s1, 1\n"
"s_cbranch_scc0 .Lmk_p78l\n"
".Lmk_p78k:\n"
"v_mfma_f32_16x16x32_bf16 v[68:71], a[0:3], v[176:179], 0\n"
"v_mfma_f32_16x16x32_bf16 v[68:71], a[4:7], v[48:51], v[68:71]\n"
"v_mfma_f32_16x16x32_bf16 v[68:71], a[8:11], v[132:135], v[68:71]\n"
"v_mfma_f32_16x16x32_bf16 v[68:71], a[12:15], v[184:187], v[68:71]\n"
"v_mfma_f32_16x16x32_bf16 v[68:71], a[16:19], v[44:47], v[68:71]\n"
"v_mfma_f32_16x16x32_bf16 v[68:71], a[20:23], v[192:195], v[68:71]\n"
"v_mfma_f32_16x16x32_bf16 v[68:71], a[24:27], v[196:199], v[68:71]\n"
"v_mfma_f32_16x16x32_bf16 v[68:71], a[28:31], v[200:203], v[68:71]\n"
"v_mfma_f32_16x16x32_bf16 v[104:107], a[60:63], v[200:203], 0\n"
"v_mfma_f32_16x16x32_bf16 v[104:107], a[56:59], v[196:199], v[104:107]\n"
"v_mfma_f32_16x16x32_bf16 v[104:107], a[52:55], v[192:195], v[104:107]\n"
"v_mfma_f32_16x16x32_bf16 v[104:107], a[48:51], v[44:47], v[104:107]\n"
"v_mfma_f32_16x16x32_bf16 v[104:107], a[44:47], v[184:187], v[104:107]\n"
"v_mfma_f32_16x16x32_bf16 v[104:107], a[40:43], v[132:135], v[104:107]\n"
"s_nop 7\n"
"s_branch .Lmk_p78r\n"
".Lmk_p79s:\n"
";\n"
"s_waitcnt vmcnt(16)\n"
"s_mov_b32 s1, 0x10000\n"
".Lmk_p79l:\n"
"s_add_u32 s97, s55, 0x40000\n"
"buffer_load_dwordx4 v[80:83], v0, s[4:7], s97 offen sc1\n"
"buffer_load_dwordx4 v[84:87], v0, s[4:7], s97 offen offset:1024 sc1\n"
"buffer_load_dwordx4 v[92:95], v0, s[4:7], s97 offen offset:2048 sc1\n"
"buffer_load_dwordx4 v[96:99], v0, s[4:7], s97 offen offset:3072 sc1\n"
"buffer_load_dwordx4 v[88:91], v1, s[4:7], s97 offen sc1\n"
"buffer_load_dwordx4 v[116:119], v1, s[4:7], s97 offen offset:1024 sc1\n"
"buffer_load_dwordx4 v[124:127], v1, s[4:7], s97 offen offset:2048 sc1\n"
"buffer_load_dwordx4 v[172:175], v1, s[4:7], s97 offen offset:3072 sc1\n"
"s_add_u32 s97, s52, 0x48000\n"
"buffer_load_dwordx4 v[128:131], v0, s[4:7], s97 offen sc1\n"
"buffer_load_dwordx4 v[68:71], v0, s[4:7], s97 offen offset:1024 sc1\n"
"buffer_load_dwordx4 v[168:171], v0, s[4:7], s97 offen offset:2048 sc1\n"
"buffer_load_dwordx4 v[152:155], v0, s[4:7], s97 offen offset:3072 sc1\n"
"buffer_load_dwordx4 v[148:151], v1, s[4:7], s97 offen sc1\n"
"buffer_load_dwordx4 v[164:167], v1, s[4:7], s97 offen offset:1024 sc1\n"
"buffer_load_dwordx4 v[180:183], v1, s[4:7], s97 offen offset:2048 sc1\n"
"buffer_load_dwordx4 v[132:135], v1, s[4:7], s97 offen offset:3072 sc1\n"
"s_add_u32 s97, s53, 0x48000\n"
"buffer_load_dwordx4 v[200:203], v0, s[4:7], s97 offen sc1\n"
"buffer_load_dwordx4 v[104:107], v0, s[4:7], s97 offen offset:1024 sc1\n"
"s_waitcnt vmcnt(0)\n"
"v_max_u32_e32 v252, v80, v81\n"
"v_max3_u32 v252, v252, v82, v83\n"
"v_max3_u32 v252, v252, v84, v85\n"
"v_max3_u32 v252, v252, v86, v87\n"
"v_max3_u32 v252, v252, v92, v93\n"
"v_max3_u32 v252, v252, v94, v95\n"
"v_max3_u32 v252, v252, v96, v97\n"
"v_max3_u32 v252, v252, v98, v99\n"
"v_max3_u32 v252, v252, v88, v89\n"
"v_max3_u32 v252, v252, v90, v91\n"
"v_max3_u32 v252, v252, v116, v117\n"
"v_max3_u32 v252, v252, v118, v119\n"
"v_max3_u32 v252, v252, v124, v125\n"
"v_max3_u32 v252, v252, v126, v127\n"
"v_max3_u32 v252, v252, v172, v173\n"
"v_max3_u32 v252, v252, v174, v175\n"
"v_cmp_eq_u32_e32 vcc, -1, v252\n"
"s_cbranch_vccz .Lmk_p79k\n"
"s_sub_u32 s1, s1, 1\n"
"s_cbranch_scc0 .Lmk_p79l\n"
".Lmk_p79k:\n"
"v_mfma_f32_16x16x32_bf16 v[176:179], a[0:3], v[80:83], v[144:147]\n"
"v_mfma_f32_16x16x32_bf16 v[176:179], a[4:7], v[84:87], v[176:179]\n"
"v_mfma_f32_16x16x32_bf16 v[176:179], a[8:11], v[92:95], v[176:179]\n"
"v_mfma_f32_16x16x32_bf16 v[176:179], a[12:15], v[96:99], v[176:179]\n"
"v_mfma_f32_16x16x32_bf16 v[176:179], a[16:19], v[88:91], v[176:179]\n"
"v_mfma_f32_16x16x32_bf16 v[176:179], a[20:23], v[116:119], v[176:179]\n"
"v_mfma_f32_16x16x32_bf16 v[176:179], a[24:27], v[124:127], v[176:179]\n"
"v_mfma_f32_16x16x32_bf16 v[176:179], a[28:31], v[172:175], v[176:179]\n"
"v_mfma_f32_16x16x32_bf16 v[204:207], a[60:63], v[172:175], v[160:163]\n"
"v_mfma_f32_16x16x32_bf16 v[204:207], a[56:59], v[124:127], v[204:207]\n"
"v_mfma_f32_16x16x32_bf16 v[204:207], a[52:55], v[116:119], v[204:207]\n"
"v_mfma_f32_16x16x32_bf16 v[204:207], a[48:51], v[88:91], v[204:207]\n"
"v_mfma_f32_16x16x32_bf16 v[204:207], a[44:47], v[96:99], v[204:207]\n"
"v_mfma_f32_16x16x32_bf16 v[204:207], a[40:43], v[92:95], v[204:207]\n"
"s_nop 7\n"
"s_branch .Lmk_p79r\n"
".Lmk_p81s:\n"
";\n"
"s_waitcnt vmcnt(16)\n"
"s_mov_b32 s1, 0x10000\n"
".Lmk_p81l:\n"
"s_add_u32 s95, s52, 0x48000\n"
"buffer_load_dwordx4 v[128:131], v0, s[4:7], s95 offen sc1\n"
"buffer_load_dwordx4 v[68:71], v0, s[4:7], s95 offen offset:1024 sc1\n"
"buffer_load_dwordx4 v[168:171], v0, s[4:7], s95 offen offset:2048 sc1\n"
"buffer_load_dwordx4 v[152:155], v0, s[4:7], s95 offen offset:3072 sc1\n"
"buffer_load_dwordx4 v[148:151], v1, s[4:7], s95 offen sc1\n"
"buffer_load_dwordx4 v[164:167], v1, s[4:7], s95 offen offset:1024 sc1\n"
"buffer_load_dwordx4 v[180:183], v1, s[4:7], s95 offen offset:2048 sc1\n"
"buffer_load_dwordx4 v[132:135], v1, s[4:7], s95 offen offset:3072 sc1\n"
"s_add_u32 s95, s53, 0x48000\n"
"buffer_load_dwordx4 v[200:203], v0, s[4:7], s95 offen sc1\n"
"buffer_load_dwordx4 v[104:107], v0, s[4:7], s95 offen offset:1024 sc1\n"
"buffer_load_dwordx4 v[144:147], v0, s[4:7], s95 offen offset:2048 sc1\n"
"buffer_load_dwordx4 v[44:47], v0, s[4:7], s95 offen offset:3072 sc1\n"
"buffer_load_dwordx4 v[156:159], v1, s[4:7], s95 offen sc1\n"
"buffer_load_dwordx4 v[184:187], v1, s[4:7], s95 offen offset:1024 sc1\n"
"buffer_load_dwordx4 v[208:211], v1, s[4:7], s95 offen offset:2048 sc1\n"
"buffer_load_dwordx4 v[176:179], v1, s[4:7], s95 offen offset:3072 sc1\n"
"s_add_u32 s95, s54, 0x48000\n"
"buffer_load_dwordx4 v[84:87], v0, s[4:7], s95 offen sc1\n"
"buffer_load_dwordx4 v[88:91], v0, s[4:7], s95 offen offset:1024 sc1\n"
"s_waitcnt vmcnt(0)\n"
"v_max_u32_e32 v252, v128, v129\n"
"v_max3_u32 v252, v252, v130, v131\n"
"v_max3_u32 v252, v252, v68, v69\n"
"v_max3_u32 v252, v252, v70, v71\n"
"v_max3_u32 v252, v252, v168, v169\n"
"v_max3_u32 v252, v252, v170, v171\n"
"v_max3_u32 v252, v252, v152, v153\n"
"v_max3_u32 v252, v252, v154, v155\n"
"v_max3_u32 v252, v252, v148, v149\n"
"v_max3_u32 v252, v252, v150, v151\n"
"v_max3_u32 v252, v252, v164, v165\n"
"v_max3_u32 v252, v252, v166, v167\n"
"v_max3_u32 v252, v252, v180, v181\n"
"v_max3_u32 v252, v252, v182, v183\n"
"v_max3_u32 v252, v252, v132, v133\n"
"v_max3_u32 v252, v252, v134, v135\n"
"v_cmp_eq_u32_e32 vcc, -1, v252\n"
"s_cbranch_vccz .Lmk_p81k\n"
"s_sub_u32 s1, s1, 1\n"
"s_cbranch_scc0 .Lmk_p81l\n"
".Lmk_p81k:\n"
"v_mfma_f32_16x16x32_bf16 v[80:83], a[0:3], v[128:131], 0\n"
"v_mfma_f32_16x16x32_bf16 v[80:83], a[4:7], v[68:71], v[80:83]\n"
"v_mfma_f32_16x16x32_bf16 v[80:83], a[8:11], v[168:171], v[80:83]\n"
"v_mfma_f32_16x16x32_bf16 v[80:83], a[12:15], v[152:155], v[80:83]\n"
"v_mfma_f32_16x16x32_bf16 v[80:83], a[16:19], v[148:151], v[80:83]\n"
"v_mfma_f32_16x16x32_bf16 v[80:83], a[20:23], v[164:167], v[80:83]\n"
"v_mfma_f32_16x16x32_bf16 v[80:83], a[24:27], v[180:183], v[80:83]\n"
"v_mfma_f32_16x16x32_bf16 v[80:83], a[28:31], v[132:135], v[80:83]\n"
"v_mfma_f32_16x16x32_bf16 v[92:95], a[60:63], v[132:135], 0\n"
"v_mfma_f32_16x16x32_bf16 v[92:95], a[56:59], v[180:183], v[92:95]\n"
"v_mfma_f32_16x16x32_bf16 v[92:95], a[52:55], v[164:167], v[92:95]\n"
"v_mfma_f32_16x16x32_bf16 v[92:95], a[48:51], v[148:151], v[92:95]\n"
"v_mfma_f32_16x16x32_bf16 v[92:95], a[44:47], v[152:155], v[92:95]\n"
"v_mfma_f32_16x16x32_bf16 v[92:95], a[40:43], v[168:171], v[92:95]\n"
"s_nop 7\n"
"s_branch .Lmk_p81r\n"
".Lmk_p82s:\n"
";\n"
"s_waitcnt vmcnt(16)\n"
"s_mov_b32 s1, 0x10000\n"
".Lmk_p82l:\n"
"s_add_u32 s95, s53, 0x48000\n"
"buffer_load_dwordx4 v[200:203], v0, s[4:7], s95 offen sc1\n"
"buffer_load_dwordx4 v[104:107], v0, s[4:7], s95 offen offset:1024 sc1\n"
"buffer_load_dwordx4 v[144:147], v0, s[4:7], s95 offen offset:2048 sc1\n"
"buffer_load_dwordx4 v[44:47], v0, s[4:7], s95 offen offset:3072 sc1\n"
"buffer_load_dwordx4 v[156:159], v1, s[4:7], s95 offen sc1\n"
"buffer_load_dwordx4 v[184:187], v1, s[4:7], s95 offen offset:1024 sc1\n"
"buffer_load_dwordx4 v[208:211], v1, s[4:7], s95 offen offset:2048 sc1\n"
"buffer_load_dwordx4 v[176:179], v1, s[4:7], s95 offen offset:3072 sc1\n"
"s_add_u32 s95, s54, 0x48000\n"
"buffer_load_dwordx4 v[84:87], v0, s[4:7], s95 offen sc1\n"
"buffer_load_dwordx4 v[88:91], v0, s[4:7], s95 offen offset:1024 sc1\n"
"buffer_load_dwordx4 v[96:99], v0, s[4:7], s95 offen offset:2048 sc1\n"
"buffer_load_dwordx4 v[116:119], v0, s[4:7], s95 offen offset:3072 sc1\n"
"buffer_load_dwordx4 v[92:95], v1, s[4:7], s95 offen sc1\n"
"buffer_load_dwordx4 v[172:175], v1, s[4:7], s95 offen offset:1024 sc1\n"
"buffer_load_dwordx4 v[80:83], v1, s[4:7], s95 offen offset:2048 sc1\n"
"buffer_load_dwordx4 v[160:163], v1, s[4:7], s95 offen offset:3072 sc1\n"
"s_add_u32 s95, s55, 0x48000\n"
"buffer_load_dwordx4 v[68:71], v0, s[4:7], s95 offen sc1\n"
"buffer_load_dwordx4 v[124:127], v0, s[4:7], s95 offen offset:1024 sc1\n"
"s_waitcnt vmcnt(0)\n"
"v_max_u32_e32 v247, v200, v201\n"
"v_max3_u32 v247, v247, v202, v203\n"
"v_max3_u32 v247, v247, v104, v105\n"
"v_max3_u32 v247, v247, v106, v107\n"
"v_max3_u32 v247, v247, v144, v145\n"
"v_max3_u32 v247, v247, v146, v147\n"
"v_max3_u32 v247, v247, v44, v45\n"
"v_max3_u32 v247, v247, v46, v47\n"
"v_max3_u32 v247, v247, v156, v157\n"
"v_max3_u32 v247, v247, v158, v159\n"
"v_max3_u32 v247, v247, v184, v185\n"
"v_max3_u32 v247, v247, v186, v187\n"
"v_max3_u32 v247, v247, v208, v209\n"
"v_max3_u32 v247, v247, v210, v211\n"
"v_max3_u32 v247, v247, v176, v177\n"
"v_max3_u32 v247, v247, v178, v179\n"
"v_cmp_eq_u32_e32 vcc, -1, v247\n"
"s_cbranch_vccz .Lmk_p82k\n"
"s_sub_u32 s1, s1, 1\n"
"s_cbranch_scc0 .Lmk_p82l\n"
".Lmk_p82k:\n"
"v_mfma_f32_16x16x32_bf16 v[48:51], a[0:3], v[200:203], 0\n"
"v_mfma_f32_16x16x32_bf16 v[48:51], a[4:7], v[104:107], v[48:51]\n"
"v_mfma_f32_16x16x32_bf16 v[48:51], a[8:11], v[144:147], v[48:51]\n"
"v_mfma_f32_16x16x32_bf16 v[48:51], a[12:15], v[44:47], v[48:51]\n"
"v_mfma_f32_16x16x32_bf16 v[48:51], a[16:19], v[156:159], v[48:51]\n"
"v_mfma_f32_16x16x32_bf16 v[48:51], a[20:23], v[184:187], v[48:51]\n"
"v_mfma_f32_16x16x32_bf16 v[48:51], a[24:27], v[208:211], v[48:51]\n"
"v_mfma_f32_16x16x32_bf16 v[48:51], a[28:31], v[176:179], v[48:51]\n"
"v_mfma_f32_16x16x32_bf16 v[128:131], a[60:63], v[176:179], 0\n"
"v_mfma_f32_16x16x32_bf16 v[128:131], a[56:59], v[208:211], v[128:131]\n"
"v_mfma_f32_16x16x32_bf16 v[128:131], a[52:55], v[184:187], v[128:131]\n"
"v_mfma_f32_16x16x32_bf16 v[128:131], a[48:51], v[156:159], v[128:131]\n"
"v_mfma_f32_16x16x32_bf16 v[128:131], a[44:47], v[44:47], v[128:131]\n"
"v_mfma_f32_16x16x32_bf16 v[128:131], a[40:43], v[144:147], v[128:131]\n"
"s_nop 7\n"
"s_branch .Lmk_p82r\n"
".Lmk_m85s:\n"
";\n"
"s_waitcnt vmcnt(16)\n"
"s_mov_b32 s1, 0x10000\n"
".Lmk_m85l:\n"
"s_add_u32 s98, s62, 0x4800\n"
"buffer_load_dword v255, v10, s[4:7], s98 offen sc1\n"
"buffer_load_dword v253, v10, s[4:7], s98 offen offset:256 sc1\n"
"buffer_load_dword v252, v10, s[4:7], s98 offen offset:512 sc1\n"
"buffer_load_dword v251, v10, s[4:7], s98 offen offset:768 sc1\n"
"buffer_load_dword v250, v10, s[4:7], s98 offen offset:1024 sc1\n"
"buffer_load_dword v254, v10, s[4:7], s98 offen offset:1280 sc1\n"
"buffer_load_dword v249, v10, s[4:7], s98 offen offset:1536 sc1\n"
"buffer_load_dword v248, v10, s[4:7], s98 offen offset:1792 sc1\n"
"s_waitcnt vmcnt(0)\n"
"v_max_u32_e32 v245, v255, v253\n"
"v_max3_u32 v245, v245, v252, v251\n"
"v_max3_u32 v245, v245, v250, v254\n"
"v_max3_u32 v245, v245, v249, v248\n"
"v_cmp_eq_u32_e32 vcc, -1, v245\n"
"s_cbranch_vccz .Lmk_m85k\n"
"s_sub_u32 s1, s1, 1\n"
"s_cbranch_scc0 .Lmk_m85l\n"
".Lmk_m85k:\n"
"v_add_f32_e32 v255, v255, v250\n"
"v_add_f32_e32 v253, v253, v254\n"
"v_add_f32_e32 v252, v252, v249\n"
"v_add_f32_e32 v251, v251, v248\n"
"v_add_f32_e32 v255, v255, v252\n"
"v_add_f32_e32 v253, v253, v251\n"
"v_add_f32_e32 v255, v255, v253\n"
"s_nop 1\n"
"s_branch .Lmk_m85r\n"
".Lmk_f72s:\n"
";\n"
"s_mov_b32 s1, 0x10000\n"
".Lmk_f72l:\n"
"buffer_load_dwordx2 v[246:247], v12, s[4:7], s97 offen sc1\n"
"s_waitcnt vmcnt(0)\n"
"v_cmp_ne_u64_e32 vcc, v[246:247], v[18:19]\n"
"s_cbranch_vccz .Lmk_f72r\n"
"s_sub_u32 s1, s1, 1\n"
"s_cbranch_scc0 .Lmk_f72l\n"
"s_branch .Lmk_f72r\n"
".Lmk_p83s:\n"
";\n"
"s_waitcnt vmcnt(16)\n"
"s_mov_b32 s1, 0x10000\n"
".Lmk_p83l:\n"
"s_add_u32 s97, s54, 0x48000\n"
"buffer_load_dwordx4 v[84:87], v0, s[4:7], s97 offen sc1\n"
"buffer_load_dwordx4 v[88:91], v0, s[4:7], s97 offen offset:1024 sc1\n"
"buffer_load_dwordx4 v[96:99], v0, s[4:7], s97 offen offset:2048 sc1\n"
"buffer_load_dwordx4 v[116:119], v0, s[4:7], s97 offen offset:3072 sc1\n"
"buffer_load_dwordx4 v[92:95], v1, s[4:7], s97 offen sc1\n"
"buffer_load_dwordx4 v[172:175], v1, s[4:7], s97 offen offset:1024 sc1\n"
"buffer_load_dwordx4 v[80:83], v1, s[4:7], s97 offen offset:2048 sc1\n"
"buffer_load_dwordx4 v[160:163], v1, s[4:7], s97 offen offset:3072 sc1\n"
"s_add_u32 s97, s55, 0x48000\n"
"buffer_load_dwordx4 v[68:71], v0, s[4:7], s97 offen sc1\n"
"buffer_load_dwordx4 v[124:127], v0, s[4:7], s97 offen offset:1024 sc1\n"
"buffer_load_dwordx4 v[132:135], v0, s[4:7], s97 offen offset:2048 sc1\n"
"buffer_load_dwordx4 v[148:151], v0, s[4:7], s97 offen offset:3072 sc1\n"
"buffer_load_dwordx4 v[128:131], v1, s[4:7], s97 offen sc1\n"
"buffer_load_dwordx4 v[164:167], v1, s[4:7], s97 offen offset:1024 sc1\n"
"buffer_load_dwordx4 v[168:171], v1, s[4:7], s97 offen offset:2048 sc1\n"
"buffer_load_dwordx4 v[180:183], v1, s[4:7], s97 offen offset:3072 sc1\n"
"s_waitcnt vmcnt(0)\n"
"v_max_u32_e32 v255, v84, v85\n"
"v_max3_u32 v255, v255, v86, v87\n"
"v_max3_u32 v255, v255, v88, v89\n"
"v_max3_u32 v255, v255, v90, v91\n"
"v_max3_u32 v255, v255, v96, v97\n"
"v_max3_u32 v255, v255, v98, v99\n"
"v_max3_u32 v255, v255, v116, v117\n"
"v_max3_u32 v255, v255, v118, v119\n"
"v_max3_u32 v255, v255, v92, v93\n"
"v_max3_u32 v255, v255, v94, v95\n"
"v_max3_u32 v255, v255, v172, v173\n"
"v_max3_u32 v255, v255, v174, v175\n"
"v_max3_u32 v255, v255, v80, v81\n"
"v_max3_u32 v255, v255, v82, v83\n"
"v_max3_u32 v255, v255, v160, v161\n"
"v_max3_u32 v255, v255, v162, v163\n"
"v_cmp_eq_u32_e32 vcc, -1, v255\n"
"s_cbranch_vccz .Lmk_p83k\n"
"s_sub_u32 s1, s1, 1\n"
"s_cbranch_scc0 .Lmk_p83l\n"
".Lmk_p83k:\n"
"v_mfma_f32_16x16x32_bf16 v[44:47], a[0:3], v[84:87], 0\n"
"v_mfma_f32_16x16x32_bf16 v[44:47], a[4:7], v[88:91], v[44:47]\n"
"v_mfma_f32_16x16x32_bf16 v[44:47], a[8:11], v[96:99], v[44:47]\n"
"v_mfma_f32_16x16x32_bf16 v[44:47], a[12:15], v[116:119], v[44:47]\n"
"v_mfma_f32_16x16x32_bf16 v[44:47], a[16:19], v[92:95], v[44:47]\n"
"v_mfma_f32_16x16x32_bf16 v[44:47], a[20:23], v[172:175], v[44:47]\n"
"v_mfma_f32_16x16x32_bf16 v[44:47], a[24:27], v[80:83], v[44:47]\n"
"v_mfma_f32_16x16x32_bf16 v[44:47], a[28:31], v[160:163], v[44:47]\n"
"v_mfma_f32_16x16x32_bf16 v[48:51], a[60:63], v[160:163], 0\n"
"v_mfma_f32_16x16x32_bf16 v[48:51], a[56:59], v[80:83], v[48:51]\n"
"v_mfma_f32_16x16x32_bf16 v[48:51], a[52:55], v[172:175], v[48:51]\n"
"v_mfma_f32_16x16x32_bf16 v[48:51], a[48:51], v[92:95], v[48:51]\n"
"v_mfma_f32_16x16x32_bf16 v[48:51], a[44:47], v[116:119], v[48:51]\n"
"v_mfma_f32_16x16x32_bf16 v[48:51], a[40:43], v[96:99], v[48:51]\n"
"s_nop 7\n"
"s_branch .Lmk_p83r\n"
".Lmk_p84s:\n"
";\n"
"s_waitcnt vmcnt(16)\n"
"s_mov_b32 s1, 0x10000\n"
".Lmk_p84l:\n"
"s_add_u32 s97, s55, 0x48000\n"
"buffer_load_dwordx4 v[68:71], v0, s[4:7], s97 offen sc1\n"
"buffer_load_dwordx4 v[124:127], v0, s[4:7], s97 offen offset:1024 sc1\n"
"buffer_load_dwordx4 v[132:135], v0, s[4:7], s97 offen offset:2048 sc1\n"
"buffer_load_dwordx4 v[148:151], v0, s[4:7], s97 offen offset:3072 sc1\n"
"buffer_load_dwordx4 v[128:131], v1, s[4:7], s97 offen sc1\n"
"buffer_load_dwordx4 v[164:167], v1, s[4:7], s97 offen offset:1024 sc1\n"
"buffer_load_dwordx4 v[168:171], v1, s[4:7], s97 offen offset:2048 sc1\n"
"buffer_load_dwordx4 v[180:183], v1, s[4:7], s97 offen offset:3072 sc1\n"
"s_waitcnt vmcnt(0)\n"
"v_max_u32_e32 v252, v68, v69\n"
"v_max3_u32 v252, v252, v70, v71\n"
"v_max3_u32 v252, v252, v124, v125\n"
"v_max3_u32 v252, v252, v126, v127\n"
"v_max3_u32 v252, v252, v132, v133\n"
"v_max3_u32 v252, v252, v134, v135\n"
"v_max3_u32 v252, v252, v148, v149\n"
"v_max3_u32 v252, v252, v150, v151\n"
"v_max3_u32 v252, v252, v128, v129\n"
"v_max3_u32 v252, v252, v130, v131\n"
"v_max3_u32 v252, v252, v164, v165\n"
"v_max3_u32 v252, v252, v166, v167\n"
"v_max3_u32 v252, v252, v168, v169\n"
"v_max3_u32 v252, v252, v170, v171\n"
"v_max3_u32 v252, v252, v180, v181\n"
"v_max3_u32 v252, v252, v182, v183\n"
"v_cmp_eq_u32_e32 vcc, -1, v252\n"
"s_cbranch_vccz .Lmk_p84k\n"
"s_sub_u32 s1, s1, 1\n"
"s_cbranch_scc0 .Lmk_p84l\n"
".Lmk_p84k:\n"
"v_mfma_f32_16x16x32_bf16 v[96:99], a[0:3], v[68:71], v[104:107]\n"
"v_mfma_f32_16x16x32_bf16 v[96:99], a[4:7], v[124:127], v[96:99]\n"
"v_mfma_f32_16x16x32_bf16 v[96:99], a[8:11], v[132:135], v[96:99]\n"
"v_mfma_f32_16x16x32_bf16 v[96:99], a[12:15], v[148:151], v[96:99]\n"
"v_mfma_f32_16x16x32_bf16 v[96:99], a[16:19], v[128:131], v[96:99]\n"
"v_mfma_f32_16x16x32_bf16 v[96:99], a[20:23], v[164:167], v[96:99]\n"
"v_mfma_f32_16x16x32_bf16 v[96:99], a[24:27], v[168:171], v[96:99]\n"
"v_mfma_f32_16x16x32_bf16 v[96:99], a[28:31], v[180:183], v[96:99]\n"
"v_mfma_f32_16x16x32_bf16 v[200:203], a[60:63], v[180:183], v[176:179]\n"
"v_mfma_f32_16x16x32_bf16 v[200:203], a[56:59], v[168:171], v[200:203]\n"
"v_mfma_f32_16x16x32_bf16 v[200:203], a[52:55], v[164:167], v[200:203]\n"
"v_mfma_f32_16x16x32_bf16 v[200:203], a[48:51], v[128:131], v[200:203]\n"
"v_mfma_f32_16x16x32_bf16 v[200:203], a[44:47], v[148:151], v[200:203]\n"
"v_mfma_f32_16x16x32_bf16 v[200:203], a[40:43], v[132:135], v[200:203]\n"
"s_nop 7\n"
"s_branch .Lmk_p84r\n"
".Lmk_f73s:\n"
";\n"
"s_mov_b32 s1, 0x10000\n"
".Lmk_f73l:\n"
"buffer_load_dwordx2 v[250:251], off, s[4:7], s98 sc1\n"
"s_waitcnt vmcnt(0)\n"
"v_cmp_ne_u64_e32 vcc, v[250:251], v[18:19]\n"
"s_cbranch_vccz .Lmk_f73r\n"
"s_sub_u32 s1, s1, 1\n"
"s_cbranch_scc0 .Lmk_f73l\n"
"s_branch .Lmk_f73r\n"
".Lmk_x94s:\n"
"s_nop 3\n"
"v_mov_b32_e32 v36, 0xff800000\n"
"v_mov_b32_e32 v37, 0xff800000\n"
"v_mov_b32_e32 v38, 0xff800000\n"
"v_mov_b32_e32 v39, 0xff800000\n"
"v_mov_b32_e32 v60, 0xff800000\n"
"v_mov_b32_e32 v61, 0xff800000\n"
"v_mov_b32_e32 v62, 0xff800000\n"
"v_mov_b32_e32 v63, 0xff800000\n"
"s_nop 1\n"
"s_branch .Lmk_x94r\n"
".Lmk_x95s:\n"
"s_nop 3\n"
"v_mov_b32_e32 v36, 0xff800000\n"
"v_mov_b32_e32 v37, 0xff800000\n"
"v_mov_b32_e32 v38, 0xff800000\n"
"v_mov_b32_e32 v39, 0xff800000\n"
"v_mov_b32_e32 v60, 0xff800000\n"
"v_mov_b32_e32 v61, 0xff800000\n"
"v_mov_b32_e32 v62, 0xff800000\n"
"v_mov_b32_e32 v63, 0xff800000\n"
"s_nop 1\n"
"s_branch .Lmk_x95r\n"
".Lmk_x96s:\n"
"s_nop 3\n"
"v_mov_b32_e32 v36, 0xff800000\n"
"v_mov_b32_e32 v37, 0xff800000\n"
"v_mov_b32_e32 v38, 0xff800000\n"
"v_mov_b32_e32 v39, 0xff800000\n"
"v_mov_b32_e32 v60, 0xff800000\n"
"v_mov_b32_e32 v61, 0xff800000\n"
"v_mov_b32_e32 v62, 0xff800000\n"
"v_mov_b32_e32 v63, 0xff800000\n"
"s_nop 1\n"
"s_branch .Lmk_x96r\n"
".Lmk_x97s:\n"
"s_nop 3\n"
"v_mov_b32_e32 v36, 0xff800000\n"
"v_mov_b32_e32 v37, 0xff800000\n"
"v_mov_b32_e32 v38, 0xff800000\n"
"v_mov_b32_e32 v39, 0xff800000\n"
"v_mov_b32_e32 v60, 0xff800000\n"
"v_mov_b32_e32 v61, 0xff800000\n"
"v_mov_b32_e32 v62, 0xff800000\n"
"v_mov_b32_e32 v63, 0xff800000\n"
"s_nop 1\n"
"s_branch .Lmk_x97r\n"
".Lmk_b92:\n"
"s_cmp_lg_u32 s2, 0\n"
"s_cbranch_scc1 .Lmk_k98\n"
"s_mov_b32 s2, 1\n"
"s_waitcnt vmcnt(0) lgkmcnt(0)\n"
"s_nop 7\n"
"s_mov_b32 s97, 11\n"
"v_add_u32_e32 v248, 0x3800, v15\n"
"v_add_u32_e32 v247, 0x3800, v254\n"
"ds_read_b128 v[36:39], v15 offset:24576\n"
"ds_read_b128 v[40:43], v254 offset:24832\n"
"s_waitcnt lgkmcnt(0)\n"
"v_mfma_f32_16x16x32_bf16 v[48:51], v[36:39], v[28:31], 0\n"
"v_mfma_f32_16x16x32_bf16 v[52:55], v[36:39], v[32:35], 0\n"
"v_mfma_f32_16x16x32_bf16 v[36:39], v[40:43], v[32:35], 0\n"
"v_mfma_f32_16x16x32_bf16 v[56:59], v[40:43], v[28:31], 0\n"
".Lmk_b92l:\n"
"ds_read_b128 v[40:43], v248 offset:8192\n"
"ds_read_b128 v[60:63], v247 offset:8448\n"
"s_cmp_gt_i32 s78, s97\n"
"s_cselect_b32 s96, 0xff800000, 0\n"
"v_mov_b32_e32 v64, s96\n"
"v_mov_b32_e32 v65, s96\n"
"v_mov_b32_e32 v66, s96\n"
"v_mov_b32_e32 v67, s96\n"
"v_subrev_u32_e32 v248, 0x800, v248\n"
"v_subrev_u32_e32 v247, 0x800, v247\n"
"s_waitcnt lgkmcnt(0)\n"
"v_mfma_f32_16x16x32_bf16 v[72:75], v[40:43], v[28:31], v[64:67]\n"
"v_mfma_f32_16x16x32_bf16 v[76:79], v[40:43], v[32:35], v[64:67]\n"
"v_mfma_f32_16x16x32_bf16 v[40:43], v[60:63], v[32:35], v[64:67]\n"
"v_mfma_f32_16x16x32_bf16 v[84:87], v[60:63], v[28:31], v[64:67]\n"
"s_nop 7\n"
"s_cmp_lg_u32 s97, 4\n"
"s_cbranch_scc1 .Lmk_b92m\n"
"v_cndmask_b32_e64 v72, v48, v72, s[20:21]\n"
"v_cndmask_b32_e64 v73, v49, v73, s[22:23]\n"
"v_cndmask_b32_e64 v74, v50, v74, s[24:25]\n"
"v_cndmask_b32_e64 v75, v51, v75, s[26:27]\n"
"v_cndmask_b32_e64 v84, v56, v84, s[28:29]\n"
"v_cndmask_b32_e64 v85, v57, v85, s[30:31]\n"
"v_cndmask_b32_e64 v86, v58, v86, s[32:33]\n"
"v_cndmask_b32_e64 v87, v59, v87, s[34:35]\n"
"v_cndmask_b32_e64 v76, v52, v76, s[36:37]\n"
"v_cndmask_b32_e64 v77, v53, v77, s[38:39]\n"
"v_cndmask_b32_e64 v78, v54, v78, s[40:41]\n"
"v_cndmask_b32_e64 v79, v55, v79, s[42:43]\n"
"v_cndmask_b32_e64 v40, v36, v40, s[44:45]\n"
"v_cndmask_b32_e64 v41, v37, v41, s[46:47]\n"
"v_cndmask_b32_e64 v42, v38, v42, s[48:49]\n"
"v_cndmask_b32_e64 v43, v39, v43, s[50:51]\n"
".Lmk_b92m:\n"
"v_max3_f32 v253, v253, v72, v73\n"
"v_max3_f32 v251, v251, v76, v77\n"
"v_max3_f32 v253, v253, v74, v75\n"
"v_max3_f32 v251, v251, v78, v79\n"
"v_max3_f32 v253, v253, v84, v85\n"
"v_max3_f32 v251, v251, v40, v41\n"
"v_max3_f32 v253, v253, v86, v87\n"
"v_max3_f32 v251, v251, v42, v43\n"
"s_sub_u32 s97, s97, 1\n"
"s_cmp_ge_i32 s97, 4\n"
"s_cbranch_scc1 .Lmk_b92l\n"
"v_mov_b32_e32 v248, v253\n"
"v_mov_b32_e32 v247, v251\n"
"s_nop 0\n"
"v_permlane16_swap_b32_e32 v253, v248\n"
"v_permlane16_swap_b32_e32 v251, v247\n"
"v_max_f32_e32 v253, v253, v248\n"
"v_max_f32_e32 v251, v251, v247\n"
"v_mov_b32_e32 v248, v253\n"
"v_mov_b32_e32 v247, v251\n"
"s_nop 0\n"
"v_permlane32_swap_b32_e32 v253, v248\n"
"v_permlane32_swap_b32_e32 v251, v247\n"
"v_max_f32_e32 v253, v253, v248\n"
"v_max_f32_e32 v251, v251, v247\n"
"v_mov_b32_e32 v250, v253\n"
"v_mov_b32_e32 v249, v251\n"
"s_branch .Lmk_a91\n"
".Lmk_x101s:\n"
"s_nop 3\n"
"v_mov_b32_e32 v116, 0xff800000\n"
"v_mov_b32_e32 v117, 0xff800000\n"
"v_mov_b32_e32 v118, 0xff800000\n"
"v_mov_b32_e32 v119, 0xff800000\n"
"v_mov_b32_e32 v120, 0xff800000\n"
"v_mov_b32_e32 v121, 0xff800000\n"
"v_mov_b32_e32 v122, 0xff800000\n"
"v_mov_b32_e32 v123, 0xff800000\n"
"s_nop 1\n"
"s_branch .Lmk_x101r\n"
".Lmk_x102s:\n"
"s_nop 3\n"
"v_mov_b32_e32 v116, 0xff800000\n"
"v_mov_b32_e32 v117, 0xff800000\n"
"v_mov_b32_e32 v118, 0xff800000\n"
"v_mov_b32_e32 v119, 0xff800000\n"
"v_mov_b32_e32 v120, 0xff800000\n"
"v_mov_b32_e32 v121, 0xff800000\n"
"v_mov_b32_e32 v122, 0xff800000\n"
"v_mov_b32_e32 v123, 0xff800000\n"
"s_nop 1\n"
"s_branch .Lmk_x102r\n"
".Lmk_x103s:\n"
"s_nop 3\n"
"v_mov_b32_e32 v116, 0xff800000\n"
"v_mov_b32_e32 v117, 0xff800000\n"
"v_mov_b32_e32 v118, 0xff800000\n"
"v_mov_b32_e32 v119, 0xff800000\n"
"v_mov_b32_e32 v120, 0xff800000\n"
"v_mov_b32_e32 v121, 0xff800000\n"
"v_mov_b32_e32 v122, 0xff800000\n"
"v_mov_b32_e32 v123, 0xff800000\n"
"s_nop 1\n"
"s_branch .Lmk_x103r\n"
".Lmk_h90s:\n"
";\n"
"s_waitcnt vmcnt(16)\n"
"s_mov_b32 s1, 0x10000\n"
".Lmk_h90l:\n"
"s_add_u32 m0, s77, 0x4000\n"
"s_add_u32 s95, s65, 0\n"
"buffer_load_dwordx4 v2, s[4:7], s95 offen sc1 lds\n"
"buffer_load_dwordx4 v2, s[4:7], s95 offen offset:1024 sc1 lds\n"
"s_add_u32 m0, s77, 0xc000\n"
"s_add_u32 s95, s65, 0x4000\n"
"buffer_load_dwordx4 v2, s[4:7], s95 offen sc1 lds\n"
"buffer_load_dwordx4 v2, s[4:7], s95 offen offset:1024 sc1 lds\n"
"s_waitcnt vmcnt(0)\n"
"ds_read_b128 v[164:167], v2 offset:16384\n"
"ds_read_b128 v[76:79], v2 offset:17408\n"
"ds_read_b128 v[72:75], v2 offset:49152\n"
"ds_read_b128 v[84:87], v2 offset:50176\n"
"s_waitcnt lgkmcnt(0)\n"
"s_waitcnt vmcnt(0)\n"
"v_max_u32_e32 v248, v164, v165\n"
"v_max3_u32 v248, v248, v166, v167\n"
"v_max3_u32 v248, v248, v76, v77\n"
"v_max3_u32 v248, v248, v78, v79\n"
"v_max3_u32 v248, v248, v72, v73\n"
"v_max3_u32 v248, v248, v74, v75\n"
"v_max3_u32 v248, v248, v84, v85\n"
"v_max3_u32 v248, v248, v86, v87\n"
"v_cmp_eq_u32_e32 vcc, -1, v248\n"
"s_cbranch_vccz .Lmk_h90k\n"
"s_sub_u32 s1, s1, 1\n"
"s_cbranch_scc0 .Lmk_h90l\n"
".Lmk_h90k:\n"
"s_branch .Lmk_h90r\n"
".Lmk_f74s:\n"
";\n"
"s_mov_b32 s1, 0x10000\n"
".Lmk_f74l:\n"
"buffer_load_dwordx2 v[246:247], v12, s[4:7], s96 offen sc1\n"
"s_waitcnt vmcnt(0)\n"
"v_cmp_ne_u64_e32 vcc, v[246:247], v[18:19]\n"
"s_cbranch_vccz .Lmk_f74r\n"
"s_sub_u32 s1, s1, 1\n"
"s_cbranch_scc0 .Lmk_f74l\n"
"s_branch .Lmk_f74r\n"
".Lmk_b100:\n"
"s_cmp_lg_u32 s2, 0\n"
"s_cbranch_scc1 .Lmk_k105\n"
"s_mov_b32 s2, 1\n"
"s_waitcnt vmcnt(0) lgkmcnt(0)\n"
"s_nop 7\n"
"s_mov_b32 s96, 7\n"
"v_add_u32_e32 v248, 0x3800, v15\n"
"v_add_u32_e32 v247, 0x3800, v254\n"
"ds_read_b128 v[188:191], v15 offset:16384\n"
"ds_read_b128 v[192:195], v254 offset:16640\n"
"s_waitcnt lgkmcnt(0)\n"
"v_mfma_f32_16x16x32_bf16 v[196:199], v[188:191], v[92:95], 0\n"
"v_mfma_f32_16x16x32_bf16 v[212:215], v[188:191], v[96:99], 0\n"
"v_mfma_f32_16x16x32_bf16 v[188:191], v[192:195], v[96:99], 0\n"
"v_mfma_f32_16x16x32_bf16 v[220:223], v[192:195], v[92:95], 0\n"
".Lmk_b100l:\n"
"ds_read_b128 v[192:195], v248\n"
"ds_read_b128 v[224:227], v247 offset:256\n"
"s_cmp_gt_i32 s78, s96\n"
"s_cselect_b32 s95, 0xff800000, 0\n"
"v_mov_b32_e32 v228, s95\n"
"v_mov_b32_e32 v229, s95\n"
"v_mov_b32_e32 v230, s95\n"
"v_mov_b32_e32 v231, s95\n"
"v_subrev_u32_e32 v248, 0x800, v248\n"
"v_subrev_u32_e32 v247, 0x800, v247\n"
"s_waitcnt lgkmcnt(0)\n"
"v_mfma_f32_16x16x32_bf16 v[232:235], v[192:195], v[92:95], v[228:231]\n"
"v_mfma_f32_16x16x32_bf16 v[236:239], v[192:195], v[96:99], v[228:231]\n"
"v_mfma_f32_16x16x32_bf16 v[192:195], v[224:227], v[96:99], v[228:231]\n"
"v_mfma_f32_16x16x32_bf16 v[240:243], v[224:227], v[92:95], v[228:231]\n"
"s_nop 7\n"
"s_cmp_lg_u32 s96, 0\n"
"s_cbranch_scc1 .Lmk_b100m\n"
"v_cndmask_b32_e64 v232, v196, v232, s[20:21]\n"
"v_cndmask_b32_e64 v233, v197, v233, s[22:23]\n"
"v_cndmask_b32_e64 v234, v198, v234, s[24:25]\n"
"v_cndmask_b32_e64 v235, v199, v235, s[26:27]\n"
"v_cndmask_b32_e64 v240, v220, v240, s[28:29]\n"
"v_cndmask_b32_e64 v241, v221, v241, s[30:31]\n"
"v_cndmask_b32_e64 v242, v222, v242, s[32:33]\n"
"v_cndmask_b32_e64 v243, v223, v243, s[34:35]\n"
"v_cndmask_b32_e64 v236, v212, v236, s[36:37]\n"
"v_cndmask_b32_e64 v237, v213, v237, s[38:39]\n"
"v_cndmask_b32_e64 v238, v214, v238, s[40:41]\n"
"v_cndmask_b32_e64 v239, v215, v239, s[42:43]\n"
"v_cndmask_b32_e64 v192, v188, v192, s[44:45]\n"
"v_cndmask_b32_e64 v193, v189, v193, s[46:47]\n"
"v_cndmask_b32_e64 v194, v190, v194, s[48:49]\n"
"v_cndmask_b32_e64 v195, v191, v195, s[50:51]\n"
".Lmk_b100m:\n"
"v_max3_f32 v253, v253, v232, v233\n"
"v_max3_f32 v251, v251, v236, v237\n"
"v_max3_f32 v253, v253, v234, v235\n"
"v_max3_f32 v251, v251, v238, v239\n"
"v_max3_f32 v253, v253, v240, v241\n"
"v_max3_f32 v251, v251, v192, v193\n"
"v_max3_f32 v253, v253, v242, v243\n"
"v_max3_f32 v251, v251, v194, v195\n"
"s_sub_u32 s96, s96, 1\n"
"s_cmp_ge_i32 s96, 0\n"
"s_cbranch_scc1 .Lmk_b100l\n"
"v_mov_b32_e32 v248, v253\n"
"v_mov_b32_e32 v247, v251\n"
"s_nop 0\n"
"v_permlane16_swap_b32_e32 v253, v248\n"
"v_permlane16_swap_b32_e32 v251, v247\n"
"v_max_f32_e32 v253, v253, v248\n"
"v_max_f32_e32 v251, v251, v247\n"
"v_mov_b32_e32 v248, v253\n"
"v_mov_b32_e32 v247, v251\n"
"s_nop 0\n"
"v_permlane32_swap_b32_e32 v253, v248\n"
"v_permlane32_swap_b32_e32 v251, v247\n"
"v_max_f32_e32 v253, v253, v248\n"
"v_max_f32_e32 v251, v251, v247\n"
"v_mov_b32_e32 v250, v253\n"
"v_mov_b32_e32 v249, v251\n"
"s_branch .Lmk_a99\n"
".Lmk_p106s:\n"
";\n"
"s_waitcnt vmcnt(16)\n"
"s_mov_b32 s1, 0x10000\n"
".Lmk_p106l:\n"
"s_add_u32 s98, s56, 0x40000\n"
"buffer_load_dwordx4 v[152:155], v0, s[4:7], s98 offen sc1\n"
"buffer_load_dwordx4 v[200:203], v0, s[4:7], s98 offen offset:1024 sc1\n"
"buffer_load_dwordx4 v[184:187], v0, s[4:7], s98 offen offset:2048 sc1\n"
"buffer_load_dwordx4 v[204:207], v0, s[4:7], s98 offen offset:3072 sc1\n"
"buffer_load_dwordx4 v[140:143], v1, s[4:7], s98 offen sc1\n"
"buffer_load_dwordx4 v[208:211], v1, s[4:7], s98 offen offset:1024 sc1\n"
"buffer_load_dwordx4 v[120:123], v1, s[4:7], s98 offen offset:2048 sc1\n"
"buffer_load_dwordx4 v[136:139], v1, s[4:7], s98 offen offset:3072 sc1\n"
"s_add_u32 s98, s57, 0x40000\n"
"buffer_load_dwordx4 v[216:219], v0, s[4:7], s98 offen sc1\n"
"buffer_load_dwordx4 v[132:135], v0, s[4:7], s98 offen offset:1024 sc1\n"
"buffer_load_dwordx4 v[160:163], v0, s[4:7], s98 offen offset:2048 sc1\n"
"buffer_load_dwordx4 v[100:103], v0, s[4:7], s98 offen offset:3072 sc1\n"
"buffer_load_dwordx4 v[164:167], v1, s[4:7], s98 offen sc1\n"
"buffer_load_dwordx4 v[104:107], v1, s[4:7], s98 offen offset:1024 sc1\n"
"buffer_load_dwordx4 v[108:111], v1, s[4:7], s98 offen offset:2048 sc1\n"
"buffer_load_dwordx4 v[128:131], v1, s[4:7], s98 offen offset:3072 sc1\n"
"s_add_u32 s98, s58, 0x40000\n"
"buffer_load_dwordx4 v[148:151], v0, s[4:7], s98 offen sc1\n"
"buffer_load_dwordx4 v[144:147], v0, s[4:7], s98 offen offset:1024 sc1\n"
"buffer_load_dwordx4 v[116:119], v0, s[4:7], s98 offen offset:2048 sc1\n"
"buffer_load_dwordx4 v[112:115], v0, s[4:7], s98 offen offset:3072 sc1\n"
"buffer_load_dwordx4 v[92:95], v1, s[4:7], s98 offen sc1\n"
"buffer_load_dwordx4 v[96:99], v1, s[4:7], s98 offen offset:1024 sc1\n"
"buffer_load_dwordx4 v[156:159], v1, s[4:7], s98 offen offset:2048 sc1\n"
"s_waitcnt vmcnt(0)\n"
"v_max_u32_e32 v255, v152, v153\n"
"v_max3_u32 v255, v255, v154, v155\n"
"v_max3_u32 v255, v255, v200, v201\n"
"v_max3_u32 v255, v255, v202, v203\n"
"v_max3_u32 v255, v255, v184, v185\n"
"v_max3_u32 v255, v255, v186, v187\n"
"v_max3_u32 v255, v255, v204, v205\n"
"v_max3_u32 v255, v255, v206, v207\n"
"v_max3_u32 v255, v255, v140, v141\n"
"v_max3_u32 v255, v255, v142, v143\n"
"v_max3_u32 v255, v255, v208, v209\n"
"v_max3_u32 v255, v255, v210, v211\n"
"v_max3_u32 v255, v255, v120, v121\n"
"v_max3_u32 v255, v255, v122, v123\n"
"v_max3_u32 v255, v255, v136, v137\n"
"v_max3_u32 v255, v255, v138, v139\n"
"v_cmp_eq_u32_e32 vcc, -1, v255\n"
"s_cbranch_vccz .Lmk_p106k\n"
"s_sub_u32 s1, s1, 1\n"
"s_cbranch_scc0 .Lmk_p106l\n"
".Lmk_p106k:\n"
"v_mfma_f32_16x16x32_bf16 v[88:91], a[128:131], v[152:155], 0\n"
"v_mfma_f32_16x16x32_bf16 v[88:91], a[132:135], v[200:203], v[88:91]\n"
"v_mfma_f32_16x16x32_bf16 v[88:91], a[136:139], v[184:187], v[88:91]\n"
"v_mfma_f32_16x16x32_bf16 v[88:91], a[140:143], v[204:207], v[88:91]\n"
"v_mfma_f32_16x16x32_bf16 v[88:91], a[144:147], v[140:143], v[88:91]\n"
"v_mfma_f32_16x16x32_bf16 v[88:91], a[148:151], v[208:211], v[88:91]\n"
"v_mfma_f32_16x16x32_bf16 v[88:91], a[152:155], v[120:123], v[88:91]\n"
"v_mfma_f32_16x16x32_bf16 v[88:91], a[156:159], v[136:139], v[88:91]\n"
"v_mfma_f32_16x16x32_bf16 v[124:127], a[188:191], v[136:139], 0\n"
"v_mfma_f32_16x16x32_bf16 v[124:127], a[184:187], v[120:123], v[124:127]\n"
"v_mfma_f32_16x16x32_bf16 v[124:127], a[180:183], v[208:211], v[124:127]\n"
"v_mfma_f32_16x16x32_bf16 v[124:127], a[176:179], v[140:143], v[124:127]\n"
"v_mfma_f32_16x16x32_bf16 v[124:127], a[172:175], v[204:207], v[124:127]\n"
"v_mfma_f32_16x16x32_bf16 v[124:127], a[168:171], v[184:187], v[124:127]\n"
"s_nop 7\n"
"s_branch .Lmk_p106r\n"
".Lmk_p107s:\n"
";\n"
"s_waitcnt vmcnt(16)\n"
"s_mov_b32 s1, 0x10000\n"
".Lmk_p107l:\n"
"s_add_u32 s97, s57, 0x40000\n"
"buffer_load_dwordx4 v[216:219], v0, s[4:7], s97 offen sc1\n"
"buffer_load_dwordx4 v[132:135], v0, s[4:7], s97 offen offset:1024 sc1\n"
"buffer_load_dwordx4 v[160:163], v0, s[4:7], s97 offen offset:2048 sc1\n"
"buffer_load_dwordx4 v[100:103], v0, s[4:7], s97 offen offset:3072 sc1\n"
"buffer_load_dwordx4 v[164:167], v1, s[4:7], s97 offen sc1\n"
"buffer_load_dwordx4 v[104:107], v1, s[4:7], s97 offen offset:1024 sc1\n"
"buffer_load_dwordx4 v[108:111], v1, s[4:7], s97 offen offset:2048 sc1\n"
"buffer_load_dwordx4 v[128:131], v1, s[4:7], s97 offen offset:3072 sc1\n"
"s_add_u32 s97, s58, 0x40000\n"
"buffer_load_dwordx4 v[148:151], v0, s[4:7], s97 offen sc1\n"
"buffer_load_dwordx4 v[144:147], v0, s[4:7], s97 offen offset:1024 sc1\n"
"buffer_load_dwordx4 v[116:119], v0, s[4:7], s97 offen offset:2048 sc1\n"
"buffer_load_dwordx4 v[112:115], v0, s[4:7], s97 offen offset:3072 sc1\n"
"buffer_load_dwordx4 v[92:95], v1, s[4:7], s97 offen sc1\n"
"buffer_load_dwordx4 v[96:99], v1, s[4:7], s97 offen offset:1024 sc1\n"
"buffer_load_dwordx4 v[156:159], v1, s[4:7], s97 offen offset:2048 sc1\n"
"buffer_load_dwordx4 v[88:91], v1, s[4:7], s97 offen offset:3072 sc1\n"
"s_add_u32 s97, s59, 0x40000\n"
"buffer_load_dwordx4 v[172:175], v0, s[4:7], s97 offen sc1\n"
"buffer_load_dwordx4 v[124:127], v0, s[4:7], s97 offen offset:1024 sc1\n"
"buffer_load_dwordx4 v[120:123], v0, s[4:7], s97 offen offset:2048 sc1\n"
"buffer_load_dwordx4 v[136:139], v0, s[4:7], s97 offen offset:3072 sc1\n"
"buffer_load_dwordx4 v[152:155], v1, s[4:7], s97 offen sc1\n"
"buffer_load_dwordx4 v[168:171], v1, s[4:7], s97 offen offset:1024 sc1\n"
"buffer_load_dwordx4 v[180:183], v1, s[4:7], s97 offen offset:2048 sc1\n"
"s_waitcnt vmcnt(0)\n"
"v_max_u32_e32 v255, v216, v217\n"
"v_max3_u32 v255, v255, v218, v219\n"
"v_max3_u32 v255, v255, v132, v133\n"
"v_max3_u32 v255, v255, v134, v135\n"
"v_max3_u32 v255, v255, v160, v161\n"
"v_max3_u32 v255, v255, v162, v163\n"
"v_max3_u32 v255, v255, v100, v101\n"
"v_max3_u32 v255, v255, v102, v103\n"
"v_max3_u32 v255, v255, v164, v165\n"
"v_max3_u32 v255, v255, v166, v167\n"
"v_max3_u32 v255, v255, v104, v105\n"
"v_max3_u32 v255, v255, v106, v107\n"
"v_max3_u32 v255, v255, v108, v109\n"
"v_max3_u32 v255, v255, v110, v111\n"
"v_max3_u32 v255, v255, v128, v129\n"
"v_max3_u32 v255, v255, v130, v131\n"
"v_cmp_eq_u32_e32 vcc, -1, v255\n"
"s_cbranch_vccz .Lmk_p107k\n"
"s_sub_u32 s1, s1, 1\n"
"s_cbranch_scc0 .Lmk_p107l\n"
".Lmk_p107k:\n"
"v_mfma_f32_16x16x32_bf16 v[140:143], a[128:131], v[216:219], 0\n"
"v_mfma_f32_16x16x32_bf16 v[140:143], a[132:135], v[132:135], v[140:143]\n"
"v_mfma_f32_16x16x32_bf16 v[140:143], a[136:139], v[160:163], v[140:143]\n"
"v_mfma_f32_16x16x32_bf16 v[140:143], a[140:143], v[100:103], v[140:143]\n"
"v_mfma_f32_16x16x32_bf16 v[140:143], a[144:147], v[164:167], v[140:143]\n"
"v_mfma_f32_16x16x32_bf16 v[140:143], a[148:151], v[104:107], v[140:143]\n"
"v_mfma_f32_16x16x32_bf16 v[140:143], a[152:155], v[108:111], v[140:143]\n"
"v_mfma_f32_16x16x32_bf16 v[140:143], a[156:159], v[128:131], v[140:143]\n"
"v_mfma_f32_16x16x32_bf16 v[176:179], a[188:191], v[128:131], 0\n"
"v_mfma_f32_16x16x32_bf16 v[176:179], a[184:187], v[108:111], v[176:179]\n"
"v_mfma_f32_16x16x32_bf16 v[176:179], a[180:183], v[104:107], v[176:179]\n"
"v_mfma_f32_16x16x32_bf16 v[176:179], a[176:179], v[164:167], v[176:179]\n"
"v_mfma_f32_16x16x32_bf16 v[176:179], a[172:175], v[100:103], v[176:179]\n"
"v_mfma_f32_16x16x32_bf16 v[176:179], a[168:171], v[160:163], v[176:179]\n"
"s_nop 7\n"
"s_branch .Lmk_p107r\n"
".Lmk_p108s:\n"
";\n"
"s_waitcnt vmcnt(16)\n"
"s_mov_b32 s1, 0x10000\n"
".Lmk_p108l:\n"
"s_add_u32 s97, s58, 0x40000\n"
"buffer_load_dwordx4 v[148:151], v0, s[4:7], s97 offen sc1\n"
"buffer_load_dwordx4 v[144:147], v0, s[4:7], s97 offen offset:1024 sc1\n"
"buffer_load_dwordx4 v[116:119], v0, s[4:7], s97 offen offset:2048 sc1\n"
"buffer_load_dwordx4 v[112:115], v0, s[4:7], s97 offen offset:3072 sc1\n"
"buffer_load_dwordx4 v[92:95], v1, s[4:7], s97 offen sc1\n"
"buffer_load_dwordx4 v[96:99], v1, s[4:7], s97 offen offset:1024 sc1\n"
"buffer_load_dwordx4 v[156:159], v1, s[4:7], s97 offen offset:2048 sc1\n"
"buffer_load_dwordx4 v[88:91], v1, s[4:7], s97 offen offset:3072 sc1\n"
"s_add_u32 s97, s59, 0x40000\n"
"buffer_load_dwordx4 v[172:175], v0, s[4:7], s97 offen sc1\n"
"buffer_load_dwordx4 v[124:127], v0, s[4:7], s97 offen offset:1024 sc1\n"
"buffer_load_dwordx4 v[120:123], v0, s[4:7], s97 offen offset:2048 sc1\n"
"buffer_load_dwordx4 v[136:139], v0, s[4:7], s97 offen offset:3072 sc1\n"
"buffer_load_dwordx4 v[152:155], v1, s[4:7], s97 offen sc1\n"
"buffer_load_dwordx4 v[168:171], v1, s[4:7], s97 offen offset:1024 sc1\n"
"buffer_load_dwordx4 v[180:183], v1, s[4:7], s97 offen offset:2048 sc1\n"
"buffer_load_dwordx4 v[140:143], v1, s[4:7], s97 offen offset:3072 sc1\n"
"s_waitcnt vmcnt(0)\n"
"v_max_u32_e32 v239, v148, v149\n"
"v_max3_u32 v239, v239, v150, v151\n"
"v_max3_u32 v239, v239, v144, v145\n"
"v_max3_u32 v239, v239, v146, v147\n"
"v_max3_u32 v239, v239, v116, v117\n"
"v_max3_u32 v239, v239, v118, v119\n"
"v_max3_u32 v239, v239, v112, v113\n"
"v_max3_u32 v239, v239, v114, v115\n"
"v_max3_u32 v239, v239, v92, v93\n"
"v_max3_u32 v239, v239, v94, v95\n"
"v_max3_u32 v239, v239, v96, v97\n"
"v_max3_u32 v239, v239, v98, v99\n"
"v_max3_u32 v239, v239, v156, v157\n"
"v_max3_u32 v239, v239, v158, v159\n"
"v_max3_u32 v239, v239, v88, v89\n"
"v_max3_u32 v239, v239, v90, v91\n"
"v_cmp_eq_u32_e32 vcc, -1, v239\n"
"s_cbranch_vccz .Lmk_p108k\n"
"s_sub_u32 s1, s1, 1\n"
"s_cbranch_scc0 .Lmk_p108l\n"
".Lmk_p108k:\n"
"v_mfma_f32_16x16x32_bf16 v[100:103], a[128:131], v[148:151], 0\n"
"v_mfma_f32_16x16x32_bf16 v[100:103], a[132:135], v[144:147], v[100:103]\n"
"v_mfma_f32_16x16x32_bf16 v[100:103], a[136:139], v[116:119], v[100:103]\n"
"v_mfma_f32_16x16x32_bf16 v[100:103], a[140:143], v[112:115], v[100:103]\n"
"v_mfma_f32_16x16x32_bf16 v[100:103], a[144:147], v[92:95], v[100:103]\n"
"v_mfma_f32_16x16x32_bf16 v[100:103], a[148:151], v[96:99], v[100:103]\n"
"v_mfma_f32_16x16x32_bf16 v[100:103], a[152:155], v[156:159], v[100:103]\n"
"v_mfma_f32_16x16x32_bf16 v[100:103], a[156:159], v[88:91], v[100:103]\n"
"v_mfma_f32_16x16x32_bf16 v[104:107], a[188:191], v[88:91], 0\n"
"v_mfma_f32_16x16x32_bf16 v[104:107], a[184:187], v[156:159], v[104:107]\n"
"v_mfma_f32_16x16x32_bf16 v[104:107], a[180:183], v[96:99], v[104:107]\n"
"v_mfma_f32_16x16x32_bf16 v[104:107], a[176:179], v[92:95], v[104:107]\n"
"v_mfma_f32_16x16x32_bf16 v[104:107], a[172:175], v[112:115], v[104:107]\n"
"v_mfma_f32_16x16x32_bf16 v[104:107], a[168:171], v[116:119], v[104:107]\n"
"s_nop 7\n"
"s_branch .Lmk_p108r\n"
".Lmk_p109s:\n"
";\n"
"s_waitcnt vmcnt(16)\n"
"s_mov_b32 s1, 0x10000\n"
".Lmk_p109l:\n"
"s_add_u32 s97, s59, 0x40000\n"
"buffer_load_dwordx4 v[172:175], v0, s[4:7], s97 offen sc1\n"
"buffer_load_dwordx4 v[124:127], v0, s[4:7], s97 offen offset:1024 sc1\n"
"buffer_load_dwordx4 v[120:123], v0, s[4:7], s97 offen offset:2048 sc1\n"
"buffer_load_dwordx4 v[136:139], v0, s[4:7], s97 offen offset:3072 sc1\n"
"buffer_load_dwordx4 v[152:155], v1, s[4:7], s97 offen sc1\n"
"buffer_load_dwordx4 v[168:171], v1, s[4:7], s97 offen offset:1024 sc1\n"
"buffer_load_dwordx4 v[180:183], v1, s[4:7], s97 offen offset:2048 sc1\n"
"buffer_load_dwordx4 v[140:143], v1, s[4:7], s97 offen offset:3072 sc1\n"
"s_add_u32 s97, s56, 0\n"
"buffer_load_dwordx4 v[100:103], v0, s[4:7], s97 offen sc1\n"
"buffer_load_dwordx4 v[176:179], v0, s[4:7], s97 offen offset:1024 sc1\n"
"buffer_load_dwordx4 v[104:107], v0, s[4:7], s97 offen offset:2048 sc1\n"
"buffer_load_dwordx4 v[88:91], v0, s[4:7], s97 offen offset:3072 sc1\n"
"buffer_load_dwordx4 v[92:95], v1, s[4:7], s97 offen sc1\n"
"buffer_load_dwordx4 v[116:119], v1, s[4:7], s97 offen offset:1024 sc1\n"
"buffer_load_dwordx4 v[132:135], v1, s[4:7], s97 offen offset:2048 sc1\n"
"buffer_load_dwordx4 v[144:147], v1, s[4:7], s97 offen offset:3072 sc1\n"
"s_add_u32 s97, s57, 0\n"
"buffer_load_dwordx4 v[156:159], v0, s[4:7], s97 offen sc1\n"
"s_waitcnt vmcnt(0)\n"
"v_max_u32_e32 v255, v172, v173\n"
"v_max3_u32 v255, v255, v174, v175\n"
"v_max3_u32 v255, v255, v124, v125\n"
"v_max3_u32 v255, v255, v126, v127\n"
"v_max3_u32 v255, v255, v120, v121\n"
"v_max3_u32 v255, v255, v122, v123\n"
"v_max3_u32 v255, v255, v136, v137\n"
"v_max3_u32 v255, v255, v138, v139\n"
"v_max3_u32 v255, v255, v152, v153\n"
"v_max3_u32 v255, v255, v154, v155\n"
"v_max3_u32 v255, v255, v168, v169\n"
"v_max3_u32 v255, v255, v170, v171\n"
"v_max3_u32 v255, v255, v180, v181\n"
"v_max3_u32 v255, v255, v182, v183\n"
"v_max3_u32 v255, v255, v140, v141\n"
"v_max3_u32 v255, v255, v142, v143\n"
"v_cmp_eq_u32_e32 vcc, -1, v255\n"
"s_cbranch_vccz .Lmk_p109k\n"
"s_sub_u32 s1, s1, 1\n"
"s_cbranch_scc0 .Lmk_p109l\n"
".Lmk_p109k:\n"
"v_mfma_f32_16x16x32_bf16 v[128:131], a[128:131], v[172:175], v[108:111]\n"
"v_mfma_f32_16x16x32_bf16 v[128:131], a[132:135], v[124:127], v[128:131]\n"
"v_mfma_f32_16x16x32_bf16 v[128:131], a[136:139], v[120:123], v[128:131]\n"
"v_mfma_f32_16x16x32_bf16 v[128:131], a[140:143], v[136:139], v[128:131]\n"
"v_mfma_f32_16x16x32_bf16 v[128:131], a[144:147], v[152:155], v[128:131]\n"
"v_mfma_f32_16x16x32_bf16 v[128:131], a[148:151], v[168:171], v[128:131]\n"
"v_mfma_f32_16x16x32_bf16 v[128:131], a[152:155], v[180:183], v[128:131]\n"
"v_mfma_f32_16x16x32_bf16 v[128:131], a[156:159], v[140:143], v[128:131]\n"
"v_mfma_f32_16x16x32_bf16 v[148:151], a[188:191], v[140:143], v[160:163]\n"
"v_mfma_f32_16x16x32_bf16 v[148:151], a[184:187], v[180:183], v[148:151]\n"
"v_mfma_f32_16x16x32_bf16 v[148:151], a[180:183], v[168:171], v[148:151]\n"
"v_mfma_f32_16x16x32_bf16 v[148:151], a[176:179], v[152:155], v[148:151]\n"
"v_mfma_f32_16x16x32_bf16 v[148:151], a[172:175], v[136:139], v[148:151]\n"
"v_mfma_f32_16x16x32_bf16 v[148:151], a[168:171], v[120:123], v[148:151]\n"
"s_nop 7\n"
"s_branch .Lmk_p109r\n"
".Lmk_p110s:\n"
";\n"
"s_waitcnt vmcnt(16)\n"
"s_mov_b32 s1, 0x10000\n"
".Lmk_p110l:\n"
"s_add_u32 s95, s56, 0\n"
"buffer_load_dwordx4 v[100:103], v0, s[4:7], s95 offen sc1\n"
"buffer_load_dwordx4 v[176:179], v0, s[4:7], s95 offen offset:1024 sc1\n"
"buffer_load_dwordx4 v[104:107], v0, s[4:7], s95 offen offset:2048 sc1\n"
"buffer_load_dwordx4 v[88:91], v0, s[4:7], s95 offen offset:3072 sc1\n"
"buffer_load_dwordx4 v[92:95], v1, s[4:7], s95 offen sc1\n"
"buffer_load_dwordx4 v[116:119], v1, s[4:7], s95 offen offset:1024 sc1\n"
"buffer_load_dwordx4 v[132:135], v1, s[4:7], s95 offen offset:2048 sc1\n"
"buffer_load_dwordx4 v[144:147], v1, s[4:7], s95 offen offset:3072 sc1\n"
"s_add_u32 s95, s57, 0\n"
"buffer_load_dwordx4 v[156:159], v0, s[4:7], s95 offen sc1\n"
"buffer_load_dwordx4 v[108:111], v0, s[4:7], s95 offen offset:1024 sc1\n"
"buffer_load_dwordx4 v[112:115], v0, s[4:7], s95 offen offset:2048 sc1\n"
"buffer_load_dwordx4 v[196:199], v0, s[4:7], s95 offen offset:3072 sc1\n"
"buffer_load_dwordx4 v[200:203], v1, s[4:7], s95 offen sc1\n"
"buffer_load_dwordx4 v[128:131], v1, s[4:7], s95 offen offset:1024 sc1\n"
"buffer_load_dwordx4 v[120:123], v1, s[4:7], s95 offen offset:2048 sc1\n"
"buffer_load_dwordx4 v[136:139], v1, s[4:7], s95 offen offset:3072 sc1\n"
"s_add_u32 s95, s58, 0\n"
"buffer_load_dwordx4 v[148:151], v0, s[4:7], s95 offen sc1\n"
"buffer_load_dwordx4 v[160:163], v0, s[4:7], s95 offen offset:1024 sc1\n"
"buffer_load_dwordx4 v[124:127], v0, s[4:7], s95 offen offset:2048 sc1\n"
"s_waitcnt vmcnt(0)\n"
"v_max_u32_e32 v255, v100, v101\n"
"v_max3_u32 v255, v255, v102, v103\n"
"v_max3_u32 v255, v255, v176, v177\n"
"v_max3_u32 v255, v255, v178, v179\n"
"v_max3_u32 v255, v255, v104, v105\n"
"v_max3_u32 v255, v255, v106, v107\n"
"v_max3_u32 v255, v255, v88, v89\n"
"v_max3_u32 v255, v255, v90, v91\n"
"v_max3_u32 v255, v255, v92, v93\n"
"v_max3_u32 v255, v255, v94, v95\n"
"v_max3_u32 v255, v255, v116, v117\n"
"v_max3_u32 v255, v255, v118, v119\n"
"v_max3_u32 v255, v255, v132, v133\n"
"v_max3_u32 v255, v255, v134, v135\n"
"v_max3_u32 v255, v255, v144, v145\n"
"v_max3_u32 v255, v255, v146, v147\n"
"v_cmp_eq_u32_e32 vcc, -1, v255\n"
"s_cbranch_vccz .Lmk_p110k\n"
"s_sub_u32 s1, s1, 1\n"
"s_cbranch_scc0 .Lmk_p110l\n"
".Lmk_p110k:\n"
"v_mfma_f32_16x16x32_bf16 v[140:143], a[128:131], v[100:103], 0\n"
"v_mfma_f32_16x16x32_bf16 v[140:143], a[132:135], v[176:179], v[140:143]\n"
"v_mfma_f32_16x16x32_bf16 v[140:143], a[136:139], v[104:107], v[140:143]\n"
"v_mfma_f32_16x16x32_bf16 v[140:143], a[140:143], v[88:91], v[140:143]\n"
"v_mfma_f32_16x16x32_bf16 v[140:143], a[144:147], v[92:95], v[140:143]\n"
"v_mfma_f32_16x16x32_bf16 v[140:143], a[148:151], v[116:119], v[140:143]\n"
"v_mfma_f32_16x16x32_bf16 v[140:143], a[152:155], v[132:135], v[140:143]\n"
"v_mfma_f32_16x16x32_bf16 v[140:143], a[156:159], v[144:147], v[140:143]\n"
"v_mfma_f32_16x16x32_bf16 v[96:99], a[188:191], v[144:147], 0\n"
"v_mfma_f32_16x16x32_bf16 v[96:99], a[184:187], v[132:135], v[96:99]\n"
"v_mfma_f32_16x16x32_bf16 v[96:99], a[180:183], v[116:119], v[96:99]\n"
"v_mfma_f32_16x16x32_bf16 v[96:99], a[176:179], v[92:95], v[96:99]\n"
"v_mfma_f32_16x16x32_bf16 v[96:99], a[172:175], v[88:91], v[96:99]\n"
"v_mfma_f32_16x16x32_bf16 v[96:99], a[168:171], v[104:107], v[96:99]\n"
"s_nop 7\n"
"s_branch .Lmk_p110r\n"
".Lmk_p111s:\n"
";\n"
"s_waitcnt vmcnt(16)\n"
"s_mov_b32 s1, 0x10000\n"
".Lmk_p111l:\n"
"s_add_u32 s97, s57, 0\n"
"buffer_load_dwordx4 v[156:159], v0, s[4:7], s97 offen sc1\n"
"buffer_load_dwordx4 v[108:111], v0, s[4:7], s97 offen offset:1024 sc1\n"
"buffer_load_dwordx4 v[112:115], v0, s[4:7], s97 offen offset:2048 sc1\n"
"buffer_load_dwordx4 v[196:199], v0, s[4:7], s97 offen offset:3072 sc1\n"
"buffer_load_dwordx4 v[200:203], v1, s[4:7], s97 offen sc1\n"
"buffer_load_dwordx4 v[128:131], v1, s[4:7], s97 offen offset:1024 sc1\n"
"buffer_load_dwordx4 v[120:123], v1, s[4:7], s97 offen offset:2048 sc1\n"
"buffer_load_dwordx4 v[136:139], v1, s[4:7], s97 offen offset:3072 sc1\n"
"s_add_u32 s97, s58, 0\n"
"buffer_load_dwordx4 v[148:151], v0, s[4:7], s97 offen sc1\n"
"buffer_load_dwordx4 v[160:163], v0, s[4:7], s97 offen offset:1024 sc1\n"
"buffer_load_dwordx4 v[124:127], v0, s[4:7], s97 offen offset:2048 sc1\n"
"buffer_load_dwordx4 v[140:143], v0, s[4:7], s97 offen offset:3072 sc1\n"
"buffer_load_dwordx4 v[152:155], v1, s[4:7], s97 offen sc1\n"
"buffer_load_dwordx4 v[164:167], v1, s[4:7], s97 offen offset:1024 sc1\n"
"buffer_load_dwordx4 v[116:119], v1, s[4:7], s97 offen offset:2048 sc1\n"
"buffer_load_dwordx4 v[132:135], v1, s[4:7], s97 offen offset:3072 sc1\n"
"s_add_u32 s97, s59, 0\n"
"buffer_load_dwordx4 v[172:175], v0, s[4:7], s97 offen sc1\n"
"buffer_load_dwordx4 v[176:179], v0, s[4:7], s97 offen offset:1024 sc1\n"
"buffer_load_dwordx4 v[168:171], v0, s[4:7], s97 offen offset:2048 sc1\n"
"s_waitcnt vmcnt(0)\n"
"v_max_u32_e32 v255, v156, v157\n"
"v_max3_u32 v255, v255, v158, v159\n"
"v_max3_u32 v255, v255, v108, v109\n"
"v_max3_u32 v255, v255, v110, v111\n"
"v_max3_u32 v255, v255, v112, v113\n"
"v_max3_u32 v255, v255, v114, v115\n"
"v_max3_u32 v255, v255, v196, v197\n"
"v_max3_u32 v255, v255, v198, v199\n"
"v_max3_u32 v255, v255, v200, v201\n"
"v_max3_u32 v255, v255, v202, v203\n"
"v_max3_u32 v255, v255, v128, v129\n"
"v_max3_u32 v255, v255, v130, v131\n"
"v_max3_u32 v255, v255, v120, v121\n"
"v_max3_u32 v255, v255, v122, v123\n"
"v_max3_u32 v255, v255, v136, v137\n"
"v_max3_u32 v255, v255, v138, v139\n"
"v_cmp_eq_u32_e32 vcc, -1, v255\n"
"s_cbranch_vccz .Lmk_p111k\n"
"s_sub_u32 s1, s1, 1\n"
"s_cbranch_scc0 .Lmk_p111l\n"
".Lmk_p111k:\n"
"v_mfma_f32_16x16x32_bf16 v[88:91], a[128:131], v[156:159], 0\n"
"v_mfma_f32_16x16x32_bf16 v[88:91], a[132:135], v[108:111], v[88:91]\n"
"v_mfma_f32_16x16x32_bf16 v[88:91], a[136:139], v[112:115], v[88:91]\n"
"v_mfma_f32_16x16x32_bf16 v[88:91], a[140:143], v[196:199], v[88:91]\n"
"v_mfma_f32_16x16x32_bf16 v[88:91], a[144:147], v[200:203], v[88:91]\n"
"v_mfma_f32_16x16x32_bf16 v[88:91], a[148:151], v[128:131], v[88:91]\n"
"v_mfma_f32_16x16x32_bf16 v[88:91], a[152:155], v[120:123], v[88:91]\n"
"v_mfma_f32_16x16x32_bf16 v[88:91], a[156:159], v[136:139], v[88:91]\n"
"v_mfma_f32_16x16x32_bf16 v[92:95], a[188:191], v[136:139], 0\n"
"v_mfma_f32_16x16x32_bf16 v[92:95], a[184:187], v[120:123], v[92:95]\n"
"v_mfma_f32_16x16x32_bf16 v[92:95], a[180:183], v[128:131], v[92:95]\n"
"v_mfma_f32_16x16x32_bf16 v[92:95], a[176:179], v[200:203], v[92:95]\n"
"v_mfma_f32_16x16x32_bf16 v[92:95], a[172:175], v[196:199], v[92:95]\n"
"v_mfma_f32_16x16x32_bf16 v[92:95], a[168:171], v[112:115], v[92:95]\n"
"s_nop 7\n"
"s_branch .Lmk_p111r\n"
".Lmk_p112s:\n"
";\n"
"s_waitcnt vmcnt(16)\n"
"s_mov_b32 s1, 0x10000\n"
".Lmk_p112l:\n"
"s_add_u32 s97, s58, 0\n"
"buffer_load_dwordx4 v[148:151], v0, s[4:7], s97 offen sc1\n"
"buffer_load_dwordx4 v[160:163], v0, s[4:7], s97 offen offset:1024 sc1\n"
"buffer_load_dwordx4 v[124:127], v0, s[4:7], s97 offen offset:2048 sc1\n"
"buffer_load_dwordx4 v[140:143], v0, s[4:7], s97 offen offset:3072 sc1\n"
"buffer_load_dwordx4 v[152:155], v1, s[4:7], s97 offen sc1\n"
"buffer_load_dwordx4 v[164:167], v1, s[4:7], s97 offen offset:1024 sc1\n"
"buffer_load_dwordx4 v[116:119], v1, s[4:7], s97 offen offset:2048 sc1\n"
"buffer_load_dwordx4 v[132:135], v1, s[4:7], s97 offen offset:3072 sc1\n"
"s_add_u32 s97, s59, 0\n"
"buffer_load_dwordx4 v[172:175], v0, s[4:7], s97 offen sc1\n"
"buffer_load_dwordx4 v[176:179], v0, s[4:7], s97 offen offset:1024 sc1\n"
"buffer_load_dwordx4 v[168:171], v0, s[4:7], s97 offen offset:2048 sc1\n"
"buffer_load_dwordx4 v[180:183], v0, s[4:7], s97 offen offset:3072 sc1\n"
"buffer_load_dwordx4 v[184:187], v1, s[4:7], s97 offen sc1\n"
"buffer_load_dwordx4 v[188:191], v1, s[4:7], s97 offen offset:1024 sc1\n"
"buffer_load_dwordx4 v[192:195], v1, s[4:7], s97 offen offset:2048 sc1\n"
"buffer_load_dwordx4 v[196:199], v1, s[4:7], s97 offen offset:3072 sc1\n"
"s_add_u32 s97, s52, 0x440000\n"
"buffer_load_dwordx4 v[88:91], v0, s[4:7], s97 offen sc1\n"
"buffer_load_dwordx4 v[92:95], v0, s[4:7], s97 offen offset:1024 sc1\n"
"s_waitcnt vmcnt(0)\n"
"v_max_u32_e32 v239, v148, v149\n"
"v_max3_u32 v239, v239, v150, v151\n"
"v_max3_u32 v239, v239, v160, v161\n"
"v_max3_u32 v239, v239, v162, v163\n"
"v_max3_u32 v239, v239, v124, v125\n"
"v_max3_u32 v239, v239, v126, v127\n"
"v_max3_u32 v239, v239, v140, v141\n"
"v_max3_u32 v239, v239, v142, v143\n"
"v_max3_u32 v239, v239, v152, v153\n"
"v_max3_u32 v239, v239, v154, v155\n"
"v_max3_u32 v239, v239, v164, v165\n"
"v_max3_u32 v239, v239, v166, v167\n"
"v_max3_u32 v239, v239, v116, v117\n"
"v_max3_u32 v239, v239, v118, v119\n"
"v_max3_u32 v239, v239, v132, v133\n"
"v_max3_u32 v239, v239, v134, v135\n"
"v_cmp_eq_u32_e32 vcc, -1, v239\n"
"s_cbranch_vccz .Lmk_p112k\n"
"s_sub_u32 s1, s1, 1\n"
"s_cbranch_scc0 .Lmk_p112l\n"
".Lmk_p112k:\n"
"v_mfma_f32_16x16x32_bf16 v[96:99], a[128:131], v[148:151], 0\n"
"v_mfma_f32_16x16x32_bf16 v[96:99], a[132:135], v[160:163], v[96:99]\n"
"v_mfma_f32_16x16x32_bf16 v[96:99], a[136:139], v[124:127], v[96:99]\n"
"v_mfma_f32_16x16x32_bf16 v[96:99], a[140:143], v[140:143], v[96:99]\n"
"v_mfma_f32_16x16x32_bf16 v[96:99], a[144:147], v[152:155], v[96:99]\n"
"v_mfma_f32_16x16x32_bf16 v[96:99], a[148:151], v[164:167], v[96:99]\n"
"v_mfma_f32_16x16x32_bf16 v[96:99], a[152:155], v[116:119], v[96:99]\n"
"v_mfma_f32_16x16x32_bf16 v[96:99], a[156:159], v[132:135], v[96:99]\n"
"v_mfma_f32_16x16x32_bf16 v[104:107], a[188:191], v[132:135], 0\n"
"v_mfma_f32_16x16x32_bf16 v[104:107], a[184:187], v[116:119], v[104:107]\n"
"v_mfma_f32_16x16x32_bf16 v[104:107], a[180:183], v[164:167], v[104:107]\n"
"v_mfma_f32_16x16x32_bf16 v[104:107], a[176:179], v[152:155], v[104:107]\n"
"v_mfma_f32_16x16x32_bf16 v[104:107], a[172:175], v[140:143], v[104:107]\n"
"v_mfma_f32_16x16x32_bf16 v[104:107], a[168:171], v[124:127], v[104:107]\n"
"s_nop 7\n"
"s_branch .Lmk_p112r\n"
".Lmk_p113s:\n"
";\n"
"s_waitcnt vmcnt(16)\n"
"s_mov_b32 s1, 0x10000\n"
".Lmk_p113l:\n"
"s_add_u32 s97, s59, 0\n"
"buffer_load_dwordx4 v[172:175], v0, s[4:7], s97 offen sc1\n"
"buffer_load_dwordx4 v[176:179], v0, s[4:7], s97 offen offset:1024 sc1\n"
"buffer_load_dwordx4 v[168:171], v0, s[4:7], s97 offen offset:2048 sc1\n"
"buffer_load_dwordx4 v[180:183], v0, s[4:7], s97 offen offset:3072 sc1\n"
"buffer_load_dwordx4 v[184:187], v1, s[4:7], s97 offen sc1\n"
"buffer_load_dwordx4 v[188:191], v1, s[4:7], s97 offen offset:1024 sc1\n"
"buffer_load_dwordx4 v[192:195], v1, s[4:7], s97 offen offset:2048 sc1\n"
"buffer_load_dwordx4 v[196:199], v1, s[4:7], s97 offen offset:3072 sc1\n"
"s_add_u32 s97, s52, 0x440000\n"
"buffer_load_dwordx4 v[88:91], v0, s[4:7], s97 offen sc1\n"
"buffer_load_dwordx4 v[92:95], v0, s[4:7], s97 offen offset:1024 sc1\n"
"buffer_load_dwordx4 v[96:99], v0, s[4:7], s97 offen offset:2048 sc1\n"
"buffer_load_dwordx4 v[100:103], v0, s[4:7], s97 offen offset:3072 sc1\n"
"buffer_load_dwordx4 v[104:107], v1, s[4:7], s97 offen sc1\n"
"buffer_load_dwordx4 v[112:115], v1, s[4:7], s97 offen offset:1024 sc1\n"
"buffer_load_dwordx4 v[116:119], v1, s[4:7], s97 offen offset:2048 sc1\n"
"buffer_load_dwordx4 v[124:127], v1, s[4:7], s97 offen offset:3072 sc1\n"
"s_add_u32 s97, s53, 0x440000\n"
"buffer_load_dwordx4 v[128:131], v0, s[4:7], s97 offen sc1\n"
"buffer_load_dwordx4 v[132:135], v0, s[4:7], s97 offen offset:1024 sc1\n"
"buffer_load_dwordx4 v[136:139], v0, s[4:7], s97 offen offset:2048 sc1\n"
"s_waitcnt vmcnt(0)\n"
"v_max_u32_e32 v255, v172, v173\n"
"v_max3_u32 v255, v255, v174, v175\n"
"v_max3_u32 v255, v255, v176, v177\n"
"v_max3_u32 v255, v255, v178, v179\n"
"v_max3_u32 v255, v255, v168, v169\n"
"v_max3_u32 v255, v255, v170, v171\n"
"v_max3_u32 v255, v255, v180, v181\n"
"v_max3_u32 v255, v255, v182, v183\n"
"v_max3_u32 v255, v255, v184, v185\n"
"v_max3_u32 v255, v255, v186, v187\n"
"v_max3_u32 v255, v255, v188, v189\n"
"v_max3_u32 v255, v255, v190, v191\n"
"v_max3_u32 v255, v255, v192, v193\n"
"v_max3_u32 v255, v255, v194, v195\n"
"v_max3_u32 v255, v255, v196, v197\n"
"v_max3_u32 v255, v255, v198, v199\n"
"v_cmp_eq_u32_e32 vcc, -1, v255\n"
"s_cbranch_vccz .Lmk_p113k\n"
"s_sub_u32 s1, s1, 1\n"
"s_cbranch_scc0 .Lmk_p113l\n"
".Lmk_p113k:\n"
"v_mfma_f32_16x16x32_bf16 v[164:167], a[128:131], v[172:175], v[144:147]\n"
"v_mfma_f32_16x16x32_bf16 v[164:167], a[132:135], v[176:179], v[164:167]\n"
"v_mfma_f32_16x16x32_bf16 v[164:167], a[136:139], v[168:171], v[164:167]\n"
"v_mfma_f32_16x16x32_bf16 v[164:167], a[140:143], v[180:183], v[164:167]\n"
"v_mfma_f32_16x16x32_bf16 v[164:167], a[144:147], v[184:187], v[164:167]\n"
"v_mfma_f32_16x16x32_bf16 v[164:167], a[148:151], v[188:191], v[164:167]\n"
"v_mfma_f32_16x16x32_bf16 v[164:167], a[152:155], v[192:195], v[164:167]\n"
"v_mfma_f32_16x16x32_bf16 v[164:167], a[156:159], v[196:199], v[164:167]\n"
"v_mfma_f32_16x16x32_bf16 v[200:203], a[188:191], v[196:199], v[156:159]\n"
"v_mfma_f32_16x16x32_bf16 v[200:203], a[184:187], v[192:195], v[200:203]\n"
"v_mfma_f32_16x16x32_bf16 v[200:203], a[180:183], v[188:191], v[200:203]\n"
"v_mfma_f32_16x16x32_bf16 v[200:203], a[176:179], v[184:187], v[200:203]\n"
"v_mfma_f32_16x16x32_bf16 v[200:203], a[172:175], v[180:183], v[200:203]\n"
"v_mfma_f32_16x16x32_bf16 v[200:203], a[168:171], v[168:171], v[200:203]\n"
"s_nop 7\n"
"s_branch .Lmk_p113r\n"
".Lmk_exit:\n"
"v_xor_b32_e32 v255, 32, v15\n"
"ds_read_b128 v[36:39], v2 offset:17408\n"
"ds_read_b128 v[40:43], v15 offset:16384\n"
"ds_read_b128 v[52:55], v255 offset:16640\n"
"v_xor_b32_e32 v255, 32, v17\n"
"ds_read_b128 v[60:63], v2 offset:16384\n"
"ds_read_b128 v[64:67], v17 offset:15360\n"
"ds_read_b128 v[68:71], v255 offset:15616\n"
"v_xor_b32_e32 v255, 32, v16\n"
"ds_read_b64_tr_b16 v[72:73], v16 offset:16384\n"
"ds_read_b64_tr_b16 v[74:75], v255 offset:16640\n"
"ds_read_b64_tr_b16 v[76:77], v255 offset:16384\n"
"ds_read_b64_tr_b16 v[78:79], v16 offset:16640\n"
"v_mov_b32_e32 v80, 0x3f803f80\n"
"v_mov_b32_e32 v81, 0x3f803f80\n"
"v_mov_b32_e32 v82, 0x3f803f80\n"
"v_mov_b32_e32 v83, 0x3f803f80\n"
"v_xor_b32_e32 v255, 32, v16\n"
"ds_read_b64_tr_b16 v[84:85], v16 offset:15360\n"
"ds_read_b64_tr_b16 v[86:87], v255 offset:15616\n"
"s_waitcnt lgkmcnt(13)\n"
"ds_read_b64_tr_b16 v[96:97], v255 offset:15360\n"
"ds_read_b64_tr_b16 v[98:99], v16 offset:15616\n"
"v_mov_b32_e32 v100, 0x3f803f80\n"
"v_mov_b32_e32 v101, 0x3f803f80\n"
"v_mov_b32_e32 v102, 0x3f803f80\n"
"v_mov_b32_e32 v103, 0x3f803f80\n"
"s_waitcnt lgkmcnt(11)\n"
"v_mfma_f32_16x16x32_bf16 v[104:107], v[40:43], v[36:39], v[104:107]\n"
"v_mfma_f32_16x16x32_bf16 v[124:127], v[52:55], v[36:39], v[124:127]\n"
"s_waitcnt lgkmcnt(8)\n"
"v_mfma_f32_16x16x32_bf16 v[88:91], v[64:67], v[60:63], v[88:91]\n"
"v_mfma_f32_16x16x32_bf16 v[176:179], v[68:71], v[60:63], v[176:179]\n"
"s_nop 3\n"
"v_max_f32_e32 v255, v104, v105\n"
"s_nop 1\n"
"v_max_f32_e32 v254, v88, v89\n"
"v_max3_f32 v255, v255, v106, v107\n"
"v_max3_f32 v254, v254, v90, v91\n"
"v_max3_f32 v255, v255, v124, v125\n"
"v_max3_f32 v254, v254, v176, v177\n"
"v_max3_f32 v255, v255, v126, v127\n"
"v_max3_f32 v254, v254, v178, v179\n"
"v_mov_b32_e32 v253, v255\n"
"v_mov_b32_e32 v252, v254\n"
"s_nop 0\n"
"v_permlane16_swap_b32_e32 v255, v253\n"
"v_permlane16_swap_b32_e32 v254, v252\n"
"v_max_f32_e32 v255, v255, v253\n"
"v_max_f32_e32 v254, v254, v252\n"
"v_mov_b32_e32 v253, v255\n"
"v_mov_b32_e32 v252, v254\n"
"s_nop 0\n"
"v_permlane32_swap_b32_e32 v255, v253\n"
"v_permlane32_swap_b32_e32 v254, v252\n"
"v_max_f32_e32 v255, v255, v253\n"
"v_max_f32_e32 v254, v254, v252\n"
"v_sub_f32_e32 v104, v104, v255\n"
"v_sub_f32_e32 v105, v105, v255\n"
"v_sub_f32_e32 v106, v106, v255\n"
"v_sub_f32_e32 v107, v107, v255\n"
"v_sub_f32_e32 v124, v124, v255\n"
"v_sub_f32_e32 v125, v125, v255\n"
"v_sub_f32_e32 v126, v126, v255\n"
"v_sub_f32_e32 v127, v127, v255\n"
"v_exp_f32_e32 v104, v104\n"
"v_exp_f32_e32 v105, v105\n"
"v_exp_f32_e32 v106, v106\n"
"v_exp_f32_e32 v107, v107\n"
"v_exp_f32_e32 v124, v124\n"
"v_exp_f32_e32 v125, v125\n"
"v_exp_f32_e32 v126, v126\n"
"v_exp_f32_e32 v127, v127\n"
"v_cvt_pk_bf16_f32 v36, v104, v105\n"
"v_cvt_pk_bf16_f32 v37, v106, v107\n"
"v_cvt_pk_bf16_f32 v38, v124, v125\n"
"v_cvt_pk_bf16_f32 v39, v126, v127\n"
"s_waitcnt lgkmcnt(4)\n"
"v_sub_f32_e32 v88, v88, v254\n"
"v_sub_f32_e32 v89, v89, v254\n"
"v_sub_f32_e32 v90, v90, v254\n"
"v_sub_f32_e32 v91, v91, v254\n"
"v_sub_f32_e32 v176, v176, v254\n"
"v_sub_f32_e32 v177, v177, v254\n"
"v_sub_f32_e32 v178, v178, v254\n"
"v_mfma_f32_16x16x32_bf16 v[40:43], v[80:83], v[36:39], 0\n"
"v_sub_f32_e32 v179, v179, v254\n"
"v_exp_f32_e32 v88, v88\n"
"v_exp_f32_e32 v89, v89\n"
"v_exp_f32_e32 v90, v90\n"
"v_mfma_f32_16x16x32_bf16 v[52:55], v[72:75], v[36:39], 0\n"
"v_exp_f32_e32 v91, v91\n"
"v_exp_f32_e32 v176, v176\n"
"v_exp_f32_e32 v177, v177\n"
"v_exp_f32_e32 v178, v178\n"
"v_mfma_f32_16x16x32_bf16 v[60:63], v[76:79], v[36:39], 0\n"
"v_exp_f32_e32 v179, v179\n"
"v_cvt_pk_bf16_f32 v36, v88, v89\n"
"v_cvt_pk_bf16_f32 v37, v90, v91\n"
"v_cvt_pk_bf16_f32 v38, v176, v177\n"
"v_cvt_pk_bf16_f32 v39, v178, v179\n"
"s_waitcnt lgkmcnt(0)\n"
"v_rcp_f32_e32 v40, v40\n"
"s_nop 0\n"
"v_mul_f32_e32 v52, v52, v40\n"
"v_mul_f32_e32 v53, v53, v40\n"
"v_mfma_f32_16x16x32_bf16 v[64:67], v[100:103], v[36:39], 0\n"
"v_mul_f32_e32 v54, v54, v40\n"
"v_mul_f32_e32 v55, v55, v40\n"
"v_mul_f32_e32 v60, v60, v40\n"
"v_mfma_f32_16x16x32_bf16 v[68:71], v[84:87], v[36:39], 0\n"
"v_mul_f32_e32 v61, v61, v40\n"
"v_mul_f32_e32 v62, v62, v40\n"
"v_mul_f32_e32 v63, v63, v40\n"
"v_cvt_pk_bf16_f32 v40, v52, v53\n"
"v_mfma_f32_16x16x32_bf16 v[72:75], v[96:99], v[36:39], 0\n"
"v_cvt_pk_bf16_f32 v41, v54, v55\n"
"v_cvt_pk_bf16_f32 v42, v60, v61\n"
"v_cvt_pk_bf16_f32 v43, v62, v63\n"
"s_add_u32 s97, s60, 0x408000\n"
"buffer_store_dwordx4 v[40:43], v0, s[4:7], s97 offen sc1\n"
"v_rcp_f32_e32 v64, v64\n"
"s_nop 0\n"
"v_mul_f32_e32 v68, v68, v64\n"
"v_mul_f32_e32 v69, v69, v64\n"
"v_mul_f32_e32 v70, v70, v64\n"
"v_mul_f32_e32 v71, v71, v64\n"
"v_mul_f32_e32 v72, v72, v64\n"
"v_mul_f32_e32 v73, v73, v64\n"
"v_mul_f32_e32 v74, v74, v64\n"
"v_mul_f32_e32 v75, v75, v64\n"
"v_cvt_pk_bf16_f32 v36, v68, v69\n"
"v_cvt_pk_bf16_f32 v37, v70, v71\n"
"v_cvt_pk_bf16_f32 v38, v72, v73\n"
"v_cvt_pk_bf16_f32 v39, v74, v75\n"
"s_add_u32 s97, s60, 0x400000\n"
"buffer_store_dwordx4 v[36:39], v0, s[4:7], s97 offen sc1\n"
";\n"
"s_waitcnt vmcnt(23)\n"
"; @B G2 b.0\n"
"s_nop 0\n"
"v_mfma_f32_16x16x32_bf16 v[36:39], a[128:131], v[120:123], 0\n"
"s_add_u32 s97, s58, 0x440000\n"
"buffer_load_dwordx4 v[40:43], v0, s[4:7], s97 offen sc1\n"
"v_mfma_f32_16x16x32_bf16 v[36:39], a[132:135], v[136:139], v[36:39]\n"
"s_waitcnt vmcnt(22)\n"
"v_mfma_f32_16x16x32_bf16 v[36:39], a[136:139], v[144:147], v[36:39]\n"
"v_mfma_f32_16x16x32_bf16 v[36:39], a[140:143], v[192:195], v[36:39]\n"
"s_waitcnt vmcnt(20)\n"
"v_mfma_f32_16x16x32_bf16 v[36:39], a[144:147], v[196:199], v[36:39]\n"
"buffer_load_dwordx4 v[52:55], v0, s[4:7], s97 offen offset:1024 sc1\n"
"v_mfma_f32_16x16x32_bf16 v[36:39], a[148:151], v[188:191], v[36:39]\n"
"s_waitcnt vmcnt(19)\n"
"v_mfma_f32_16x16x32_bf16 v[36:39], a[152:155], v[108:111], v[36:39]\n"
"v_mfma_f32_16x16x32_bf16 v[36:39], a[156:159], v[92:95], v[36:39]\n"
"v_mfma_f32_16x16x32_bf16 v[60:63], a[188:191], v[92:95], 0\n"
"buffer_load_dwordx4 v[64:67], v0, s[4:7], s97 offen offset:2048 sc1\n"
"v_mfma_f32_16x16x32_bf16 v[60:63], a[184:187], v[108:111], v[60:63]\n"
"v_mfma_f32_16x16x32_bf16 v[60:63], a[180:183], v[188:191], v[60:63]\n"
"v_mfma_f32_16x16x32_bf16 v[60:63], a[176:179], v[196:199], v[60:63]\n"
"v_mfma_f32_16x16x32_bf16 v[60:63], a[172:175], v[192:195], v[60:63]\n"
"s_nop 1\n"
"v_cmp_u_f32_e32 vcc, v36, v36\n"
"v_mfma_f32_16x16x32_bf16 v[60:63], a[168:171], v[144:147], v[60:63]\n"
"s_cbranch_vccnz .Lmk_p55s\n"
".Lmk_p55r:\n"
"v_mfma_f32_16x16x32_bf16 v[60:63], a[164:167], v[136:139], v[60:63]\n"
"ds_write_b128 v3, v[36:39]\n"
"v_mfma_f32_16x16x32_bf16 v[60:63], a[160:163], v[120:123], v[60:63]\n"
"buffer_load_dwordx4 v[36:39], v0, s[4:7], s97 offen offset:3072 sc1\n"
"v_mfma_f32_16x16x32_bf16 v[68:71], a[192:195], v[120:123], 0\n"
"v_bfe_u32 v255, v0, 4, 4\n"
"v_bfe_u32 v254, v0, 9, 1\n"
"v_mfma_f32_16x16x32_bf16 v[68:71], a[196:199], v[136:139], v[68:71]\n"
"v_and_b32_e32 v253, 7, v255\n"
"v_xor_b32_e32 v254, v254, v253\n"
"v_mfma_f32_16x16x32_bf16 v[68:71], a[200:203], v[144:147], v[68:71]\n"
"v_lshlrev_b32_e32 v255, 7, v255\n"
"v_lshl_add_u32 v255, v254, 4, v255\n"
"v_mfma_f32_16x16x32_bf16 v[68:71], a[204:207], v[192:195], v[68:71]\n"
"buffer_load_dwordx4 v[72:75], v1, s[4:7], s97 offen sc1\n"
"v_mfma_f32_16x16x32_bf16 v[68:71], a[208:211], v[196:199], v[68:71]\n"
"v_bfe_u32 v254, v0, 8, 1\n"
"v_lshl_add_u32 v255, v254, 3, v255\n"
"v_mfma_f32_16x16x32_bf16 v[68:71], a[212:215], v[188:191], v[68:71]\n"
"ds_write_b128 v3, v[60:63] offset:1024\n"
"v_mfma_f32_16x16x32_bf16 v[68:71], a[216:219], v[108:111], v[68:71]\n"
"v_add_u32_e32 v255, s77, v255\n"
"v_bfe_u32 v253, v0, 7, 3\n"
"v_mfma_f32_16x16x32_bf16 v[68:71], a[220:223], v[92:95], v[68:71]\n"
"buffer_load_dwordx4 v[60:63], v1, s[4:7], s97 offen offset:1024 sc1\n"
"v_mfma_f32_16x16x32_bf16 v[76:79], a[252:255], v[92:95], 0\n"
"v_bfe_u32 v254, v0, 4, 3\n"
"v_xor_b32_e32 v254, v254, v253\n"
"v_mfma_f32_16x16x32_bf16 v[76:79], a[248:251], v[108:111], v[76:79]\n"
"v_lshlrev_b32_e32 v253, 11, v253\n"
"v_lshl_add_u32 v253, v254, 4, v253\n"
"v_mfma_f32_16x16x32_bf16 v[76:79], a[244:247], v[188:191], v[76:79]\n"
"v_mfma_f32_16x16x32_bf16 v[76:79], a[240:243], v[196:199], v[76:79]\n"
"buffer_load_dwordx4 v[80:83], v1, s[4:7], s97 offen offset:2048 sc1\n"
"v_mfma_f32_16x16x32_bf16 v[76:79], a[236:239], v[192:195], v[76:79]\n"
"v_mfma_f32_16x16x32_bf16 v[76:79], a[232:235], v[144:147], v[76:79]\n"
"ds_write_b128 v3, v[68:71] offset:2048\n"
"v_mfma_f32_16x16x32_bf16 v[76:79], a[228:231], v[136:139], v[76:79]\n"
"v_mfma_f32_16x16x32_bf16 v[76:79], a[224:227], v[120:123], v[76:79]\n"
"s_waitcnt vmcnt(20)\n"
"buffer_load_dwordx4 v[68:71], v1, s[4:7], s97 offen offset:3072 sc1\n"
"; @B G2 b.1\n"
"v_mfma_f32_16x16x32_bf16 v[84:87], a[128:131], v[148:151], 0\n"
"v_mfma_f32_16x16x32_bf16 v[84:87], a[132:135], v[180:183], v[84:87]\n"
"s_add_u32 s97, s59, 0x440000\n"
"v_mfma_f32_16x16x32_bf16 v[84:87], a[136:139], v[24:27], v[84:87]\n"
"v_mfma_f32_16x16x32_bf16 v[84:87], a[140:143], v[32:35], v[84:87]\n"
"s_waitcnt vmcnt(15)\n"
"buffer_load_dwordx4 v[96:99], v0, s[4:7], s97 offen sc1\n"
"v_mfma_f32_16x16x32_bf16 v[84:87], a[144:147], v[44:47], v[84:87]\n"
"v_mfma_f32_16x16x32_bf16 v[84:87], a[148:151], v[48:51], v[84:87]\n"
"ds_write_b128 v3, v[76:79] offset:3072\n"
"v_mfma_f32_16x16x32_bf16 v[84:87], a[152:155], v[28:31], v[84:87]\n"
"v_mfma_f32_16x16x32_bf16 v[84:87], a[156:159], v[56:59], v[84:87]\n"
"buffer_load_dwordx4 v[76:79], v0, s[4:7], s97 offen offset:1024 sc1\n"
"v_mfma_f32_16x16x32_bf16 v[100:103], a[188:191], v[56:59], 0\n"
"v_mfma_f32_16x16x32_bf16 v[100:103], a[184:187], v[28:31], v[100:103]\n"
"v_mfma_f32_16x16x32_bf16 v[100:103], a[180:183], v[48:51], v[100:103]\n"
"v_mfma_f32_16x16x32_bf16 v[100:103], a[176:179], v[44:47], v[100:103]\n"
"buffer_load_dwordx4 v[112:115], v0, s[4:7], s97 offen offset:2048 sc1\n"
"v_mfma_f32_16x16x32_bf16 v[100:103], a[172:175], v[32:35], v[100:103]\n"
"s_nop 0\n"
"v_cmp_u_f32_e32 vcc, v84, v84\n"
"v_mfma_f32_16x16x32_bf16 v[100:103], a[168:171], v[24:27], v[100:103]\n"
"s_cbranch_vccnz .Lmk_p56s\n"
".Lmk_p56r:\n"
"v_mfma_f32_16x16x32_bf16 v[100:103], a[164:167], v[180:183], v[100:103]\n"
"ds_write_b128 v3, v[84:87] offset:6144\n"
"v_mfma_f32_16x16x32_bf16 v[100:103], a[160:163], v[148:151], v[100:103]\n"
"buffer_load_dwordx4 v[84:87], v0, s[4:7], s97 offen offset:3072 sc1\n"
"v_mfma_f32_16x16x32_bf16 v[116:119], a[192:195], v[148:151], 0\n"
"v_mfma_f32_16x16x32_bf16 v[116:119], a[196:199], v[180:183], v[116:119]\n"
"v_mfma_f32_16x16x32_bf16 v[116:119], a[200:203], v[24:27], v[116:119]\n"
"v_mfma_f32_16x16x32_bf16 v[116:119], a[204:207], v[32:35], v[116:119]\n"
"buffer_load_dwordx4 v[128:131], v1, s[4:7], s97 offen sc1\n"
"v_mfma_f32_16x16x32_bf16 v[116:119], a[208:211], v[44:47], v[116:119]\n"
"ds_read_b128 v[132:135], v2 offset:8192\n"
"ds_read_b128 v[140:143], v2 offset:9216\n"
"v_mfma_f32_16x16x32_bf16 v[116:119], a[212:215], v[48:51], v[116:119]\n"
"ds_write_b128 v3, v[100:103] offset:7168\n"
"v_mfma_f32_16x16x32_bf16 v[116:119], a[216:219], v[28:31], v[116:119]\n"
"v_mfma_f32_16x16x32_bf16 v[116:119], a[220:223], v[56:59], v[116:119]\n"
"buffer_load_dwordx4 v[100:103], v1, s[4:7], s97 offen offset:1024 sc1\n"
"v_mfma_f32_16x16x32_bf16 v[152:155], a[252:255], v[56:59], 0\n"
"v_mfma_f32_16x16x32_bf16 v[152:155], a[248:251], v[28:31], v[152:155]\n"
"s_waitcnt lgkmcnt(1)\n"
"v_lshlrev_b32_e32 v254, 16, v132\n"
"v_and_b32_e32 v252, 0xffff0000, v132\n"
"v_lshlrev_b32_e32 v251, 16, v133\n"
"v_mfma_f32_16x16x32_bf16 v[152:155], a[244:247], v[48:51], v[152:155]\n"
"v_and_b32_e32 v250, 0xffff0000, v133\n"
"v_lshlrev_b32_e32 v249, 16, v134\n"
"v_and_b32_e32 v248, 0xffff0000, v134\n"
"v_mfma_f32_16x16x32_bf16 v[152:155], a[240:243], v[44:47], v[152:155]\n"
"buffer_load_dwordx4 v[156:159], v1, s[4:7], s97 offen offset:2048 sc1\n"
"v_lshlrev_b32_e32 v247, 16, v135\n"
"v_mfma_f32_16x16x32_bf16 v[152:155], a[236:239], v[32:35], v[152:155]\n"
"v_and_b32_e32 v246, 0xffff0000, v135\n"
"v_lshlrev_b32_e32 v245, 16, v140\n"
"v_and_b32_e32 v244, 0xffff0000, v140\n"
"v_mfma_f32_16x16x32_bf16 v[152:155], a[232:235], v[24:27], v[152:155]\n"
"ds_write_b128 v3, v[116:119] offset:8192\n"
"v_mfma_f32_16x16x32_bf16 v[152:155], a[228:231], v[180:183], v[152:155]\n"
"v_lshlrev_b32_e32 v243, 16, v141\n"
"v_and_b32_e32 v242, 0xffff0000, v141\n"
"v_lshlrev_b32_e32 v241, 16, v142\n"
"v_mfma_f32_16x16x32_bf16 v[152:155], a[224:227], v[148:151], v[152:155]\n"
"s_waitcnt vmcnt(11)\n"
"buffer_load_dwordx4 v[116:119], v1, s[4:7], s97 offen offset:3072 sc1\n"
"; @B G2 b.2\n"
"v_mfma_f32_16x16x32_bf16 v[132:135], a[128:131], v[40:43], 0\n"
"v_and_b32_e32 v240, 0xffff0000, v142\n"
"v_lshlrev_b32_e32 v239, 16, v143\n"
"v_and_b32_e32 v238, 0xffff0000, v143\n"
"v_mfma_f32_16x16x32_bf16 v[132:135], a[132:135], v[52:55], v[132:135]\n"
"v_mfma_f32_16x16x32_bf16 v[132:135], a[136:139], v[64:67], v[132:135]\n"
"v_mfma_f32_16x16x32_bf16 v[132:135], a[140:143], v[36:39], v[132:135]\n"
"s_waitcnt vmcnt(8)\n"
"v_mfma_f32_16x16x32_bf16 v[132:135], a[144:147], v[72:75], v[132:135]\n"
"v_mfma_f32_16x16x32_bf16 v[132:135], a[148:151], v[60:63], v[132:135]\n"
"ds_write_b128 v3, v[152:155] offset:9216\n"
"v_mfma_f32_16x16x32_bf16 v[132:135], a[152:155], v[80:83], v[132:135]\n"
"v_mfma_f32_16x16x32_bf16 v[132:135], a[156:159], v[68:71], v[132:135]\n"
"v_mfma_f32_16x16x32_bf16 v[140:143], a[188:191], v[68:71], 0\n"
"v_mfma_f32_16x16x32_bf16 v[140:143], a[184:187], v[80:83], v[140:143]\n"
"s_waitcnt lgkmcnt(0)\n"
"s_barrier\n"
"v_mfma_f32_16x16x32_bf16 v[140:143], a[180:183], v[60:63], v[140:143]\n"
"ds_read_b128 v[152:155], v4\n"
"ds_read_b128 v[160:163], v5\n"
"v_mfma_f32_16x16x32_bf16 v[140:143], a[176:179], v[72:75], v[140:143]\n"
"v_mfma_f32_16x16x32_bf16 v[140:143], a[172:175], v[36:39], v[140:143]\n"
"s_add_u32 s97, s56, 0x400000\n"
"v_cmp_u_f32_e32 vcc, v132, v132\n"
"v_mfma_f32_16x16x32_bf16 v[140:143], a[168:171], v[64:67], v[140:143]\n"
"s_cbranch_vccnz .Lmk_p57s\n"
".Lmk_p57r:\n"
"v_mfma_f32_16x16x32_bf16 v[140:143], a[164:167], v[52:55], v[140:143]\n"
"ds_write_b128 v3, v[132:135] offset:12288\n"
"v_mfma_f32_16x16x32_bf16 v[140:143], a[160:163], v[40:43], v[140:143]\n"
"buffer_load_dwordx4 v[132:135], v0, s[4:7], s97 offen sc1\n"
"s_waitcnt lgkmcnt(1)\n"
"v_mfma_f32_16x16x32_bf16 v[164:167], a[192:195], v[40:43], 0\n"
"ds_read_b128 v[168:171], v4 offset:1024\n"
"ds_read_b128 v[172:175], v5 offset:1024\n"
"v_add_f32_e32 v152, v152, v254\n"
"v_mfma_f32_16x16x32_bf16 v[164:167], a[196:199], v[52:55], v[164:167]\n"
"v_add_f32_e32 v153, v153, v252\n"
"v_add_f32_e32 v154, v154, v251\n"
"v_add_f32_e32 v155, v155, v250\n"
"v_mfma_f32_16x16x32_bf16 v[164:167], a[200:203], v[64:67], v[164:167]\n"
"buffer_load_dwordx4 v[184:187], v0, s[4:7], s97 offen offset:1024 sc1\n"
"v_add_f32_e32 v152, v152, v160\n"
"v_mfma_f32_16x16x32_bf16 v[164:167], a[204:207], v[36:39], v[164:167]\n"
"v_add_f32_e32 v153, v153, v161\n"
"v_add_f32_e32 v154, v154, v162\n"
"v_add_f32_e32 v155, v155, v163\n"
"v_mfma_f32_16x16x32_bf16 v[164:167], a[208:211], v[72:75], v[164:167]\n"
"v_mfma_f32_16x16x32_bf16 v[164:167], a[212:215], v[60:63], v[164:167]\n"
"ds_write_b128 v3, v[140:143] offset:13312\n"
"v_mfma_f32_16x16x32_bf16 v[164:167], a[216:219], v[80:83], v[164:167]\n"
"buffer_load_dwordx4 v[140:143], v0, s[4:7], s97 offen offset:2048 sc1\n"
"s_waitcnt lgkmcnt(1)\n"
"v_mfma_f32_16x16x32_bf16 v[164:167], a[220:223], v[68:71], v[164:167]\n"
"ds_read_b128 v[160:163], v4 offset:2048\n"
"ds_read_b128 v[200:203], v5 offset:2048\n"
"v_add_f32_e32 v168, v168, v249\n"
"v_mfma_f32_16x16x32_bf16 v[204:207], a[252:255], v[68:71], 0\n"
"v_add_f32_e32 v169, v169, v248\n"
"v_add_f32_e32 v170, v170, v247\n"
"v_add_f32_e32 v171, v171, v246\n"
"v_mfma_f32_16x16x32_bf16 v[204:207], a[248:251], v[80:83], v[204:207]\n"
"buffer_load_dwordx4 v[68:71], v0, s[4:7], s97 offen offset:3072 sc1\n"
"v_add_f32_e32 v168, v168, v172\n"
"v_mfma_f32_16x16x32_bf16 v[204:207], a[244:247], v[60:63], v[204:207]\n"
"v_add_f32_e32 v169, v169, v173\n"
"v_add_f32_e32 v170, v170, v174\n"
"v_add_f32_e32 v171, v171, v175\n"
"v_mfma_f32_16x16x32_bf16 v[204:207], a[240:243], v[72:75], v[204:207]\n"
"v_mfma_f32_16x16x32_bf16 v[204:207], a[236:239], v[36:39], v[204:207]\n"
"buffer_load_dwordx4 v[36:39], v1, s[4:7], s97 offen sc1\n"
"s_waitcnt lgkmcnt(0)\n"
"v_mfma_f32_16x16x32_bf16 v[204:207], a[232:235], v[64:67], v[204:207]\n"
"ds_write_b128 v3, v[164:167] offset:14336\n"
"v_mfma_f32_16x16x32_bf16 v[204:207], a[228:231], v[52:55], v[204:207]\n"
"ds_read_b128 v[52:55], v4 offset:3072\n"
"ds_read_b128 v[60:63], v5 offset:3072\n"
"v_add_f32_e32 v160, v160, v245\n"
"v_mfma_f32_16x16x32_bf16 v[204:207], a[224:227], v[40:43], v[204:207]\n"
"s_waitcnt vmcnt(9)\n"
"buffer_load_dwordx4 v[40:43], v1, s[4:7], s97 offen offset:1024 sc1\n"
"; @B G2 b.3\n"
"v_mfma_f32_16x16x32_bf16 v[64:67], a[128:131], v[96:99], v[152:155]\n"
"v_add_f32_e32 v161, v161, v244\n"
"v_add_f32_e32 v162, v162, v243\n"
"v_add_f32_e32 v163, v163, v242\n"
"v_mfma_f32_16x16x32_bf16 v[64:67], a[132:135], v[76:79], v[64:67]\n"
"v_add_f32_e32 v160, v160, v200\n"
"v_add_f32_e32 v161, v161, v201\n"
"v_add_f32_e32 v162, v162, v202\n"
"v_mfma_f32_16x16x32_bf16 v[64:67], a[136:139], v[112:115], v[64:67]\n"
"buffer_load_dwordx4 v[72:75], v1, s[4:7], s97 offen offset:2048 sc1\n"
"v_add_f32_e32 v163, v163, v203\n"
"v_mfma_f32_16x16x32_bf16 v[64:67], a[140:143], v[84:87], v[64:67]\n"
"s_waitcnt vmcnt(7)\n"
"s_waitcnt lgkmcnt(0)\n"
"v_add_f32_e32 v52, v52, v241\n"
"v_add_f32_e32 v53, v53, v240\n"
"v_mfma_f32_16x16x32_bf16 v[64:67], a[144:147], v[128:131], v[64:67]\n"
"v_add_f32_e32 v54, v54, v239\n"
"v_add_f32_e32 v55, v55, v238\n"
"v_add_f32_e32 v52, v52, v60\n"
"v_mfma_f32_16x16x32_bf16 v[64:67], a[148:151], v[100:103], v[64:67]\n"
"ds_write_b128 v3, v[204:207] offset:15360\n"
"v_mfma_f32_16x16x32_bf16 v[64:67], a[152:155], v[156:159], v[64:67]\n"
"buffer_load_dwordx4 v[80:83], v1, s[4:7], s97 offen offset:3072 sc1\n"
"v_add_f32_e32 v53, v53, v61\n"
"v_mfma_f32_16x16x32_bf16 v[64:67], a[156:159], v[116:119], v[64:67]\n"
"s_add_u32 s97, s57, 0x400000\n"
"v_add_f32_e32 v54, v54, v62\n"
"v_add_f32_e32 v55, v55, v63\n"
"v_mfma_f32_16x16x32_bf16 v[60:63], a[188:191], v[116:119], v[168:171]\n"
"v_mfma_f32_16x16x32_bf16 v[60:63], a[184:187], v[156:159], v[60:63]\n"
"s_waitcnt lgkmcnt(0)\n"
"s_barrier\n"
"v_mfma_f32_16x16x32_bf16 v[60:63], a[180:183], v[100:103], v[60:63]\n"
"buffer_load_dwordx4 v[164:167], v0, s[4:7], s97 offen sc1\n"
"v_mfma_f32_16x16x32_bf16 v[60:63], a[176:179], v[128:131], v[60:63]\n"
"ds_read_b128 v[172:175], v6\n"
"ds_read_b128 v[200:203], v6 offset:1024\n"
"v_mfma_f32_16x16x32_bf16 v[60:63], a[172:175], v[84:87], v[60:63]\n"
"ds_read_b128 v[204:207], v6 offset:2048\n"
"v_cmp_u_f32_e32 vcc, v64, v64\n"
"v_mfma_f32_16x16x32_bf16 v[60:63], a[168:171], v[112:115], v[60:63]\n"
"s_cbranch_vccnz .Lmk_p58s\n"
".Lmk_p58r:\n"
"ds_read_b128 v[152:155], v6 offset:3072\n"
"v_mfma_f32_16x16x32_bf16 v[60:63], a[164:167], v[76:79], v[60:63]\n"
"buffer_load_dwordx4 v[168:171], v0, s[4:7], s97 offen offset:1024 sc1\n"
"v_mfma_f32_16x16x32_bf16 v[60:63], a[160:163], v[96:99], v[60:63]\n"
"v_mfma_f32_16x16x32_bf16 v[160:163], a[192:195], v[96:99], v[160:163]\n"
"v_mfma_f32_16x16x32_bf16 v[160:163], a[196:199], v[76:79], v[160:163]\n"
"buffer_load_dwordx4 v[208:211], v0, s[4:7], s97 offen offset:2048 sc1\n"
"v_mfma_f32_16x16x32_bf16 v[160:163], a[200:203], v[112:115], v[160:163]\n"
"v_mfma_f32_16x16x32_bf16 v[160:163], a[204:207], v[84:87], v[160:163]\n"
"v_mfma_f32_16x16x32_bf16 v[160:163], a[208:211], v[128:131], v[160:163]\n"
"buffer_load_dwordx4 v[212:215], v0, s[4:7], s97 offen offset:3072 sc1\n"
"s_waitcnt lgkmcnt(0)\n"
"v_mfma_f32_16x16x32_bf16 v[160:163], a[212:215], v[100:103], v[160:163]\n"
"v_add_f32_e32 v172, v64, v172\n"
"v_add_f32_e32 v173, v65, v173\n"
"v_add_f32_e32 v174, v66, v174\n"
"v_mfma_f32_16x16x32_bf16 v[160:163], a[216:219], v[156:159], v[160:163]\n"
"v_add_f32_e32 v175, v67, v175\n"
"v_add_f32_e32 v200, v60, v200\n"
"v_add_f32_e32 v201, v61, v201\n"
"v_mfma_f32_16x16x32_bf16 v[160:163], a[220:223], v[116:119], v[160:163]\n"
"buffer_load_dwordx4 v[64:67], v1, s[4:7], s97 offen sc1\n"
"v_add_f32_e32 v202, v62, v202\n"
"v_mfma_f32_16x16x32_bf16 v[52:55], a[252:255], v[116:119], v[52:55]\n"
"v_add_f32_e32 v203, v63, v203\n"
"v_cvt_pk_bf16_f32 v60, v172, v173\n"
"v_cvt_pk_bf16_f32 v61, v174, v175\n"
"v_mfma_f32_16x16x32_bf16 v[52:55], a[248:251], v[156:159], v[52:55]\n"
"v_cvt_pk_bf16_f32 v62, v200, v201\n"
"v_cvt_pk_bf16_f32 v63, v202, v203\n"
"v_xor_b32_e32 v254, 32, v255\n"
"v_mfma_f32_16x16x32_bf16 v[52:55], a[244:247], v[100:103], v[52:55]\n"
"buffer_load_dwordx4 v[100:103], v1, s[4:7], s97 offen offset:1024 sc1\n"
"v_mfma_f32_16x16x32_bf16 v[52:55], a[240:243], v[128:131], v[52:55]\n"
"ds_write_b64 v255, v[60:61] offset:8192\n"
"v_mfma_f32_16x16x32_bf16 v[52:55], a[236:239], v[84:87], v[52:55]\n"
"ds_write_b64 v254, v[62:63] offset:8192\n"
"v_mfma_f32_16x16x32_bf16 v[52:55], a[232:235], v[112:115], v[52:55]\n"
"buffer_load_dwordx4 v[60:63], v1, s[4:7], s97 offen offset:2048 sc1\n"
"v_mfma_f32_16x16x32_bf16 v[52:55], a[228:231], v[76:79], v[52:55]\n"
"v_mfma_f32_16x16x32_bf16 v[52:55], a[224:227], v[96:99], v[52:55]\n"
"buffer_load_dwordx4 v[76:79], v1, s[4:7], s97 offen offset:3072 sc1\n"
"s_waitcnt vmcnt(14)\n"
"; @B G2 a.0\n"
"v_mfma_f32_16x16x32_bf16 v[84:87], a[128:131], v[132:135], 0\n"
"s_add_u32 s97, s58, 0x400000\n"
"buffer_load_dwordx4 v[96:99], v0, s[4:7], s97 offen sc1\n"
"v_mfma_f32_16x16x32_bf16 v[84:87], a[132:135], v[184:187], v[84:87]\n"
"s_waitcnt vmcnt(13)\n"
"v_add_f32_e32 v204, v160, v204\n"
"v_add_f32_e32 v205, v161, v205\n"
"v_mfma_f32_16x16x32_bf16 v[84:87], a[136:139], v[140:143], v[84:87]\n"
"v_add_f32_e32 v206, v162, v206\n"
"v_add_f32_e32 v207, v163, v207\n"
"v_mfma_f32_16x16x32_bf16 v[84:87], a[140:143], v[68:71], v[84:87]\n"
"s_waitcnt vmcnt(11)\n"
"buffer_load_dwordx4 v[112:115], v0, s[4:7], s97 offen offset:1024 sc1\n"
"v_mfma_f32_16x16x32_bf16 v[84:87], a[144:147], v[36:39], v[84:87]\n"
"v_add_f32_e32 v152, v52, v152\n"
"v_add_f32_e32 v153, v53, v153\n"
"v_mfma_f32_16x16x32_bf16 v[84:87], a[148:151], v[40:43], v[84:87]\n"
"s_waitcnt vmcnt(10)\n"
"v_add_f32_e32 v154, v54, v154\n"
"v_add_f32_e32 v155, v55, v155\n"
"v_mfma_f32_16x16x32_bf16 v[84:87], a[152:155], v[72:75], v[84:87]\n"
"buffer_load_dwordx4 v[52:55], v0, s[4:7], s97 offen offset:2048 sc1\n"
"v_mfma_f32_16x16x32_bf16 v[84:87], a[156:159], v[80:83], v[84:87]\n"
"v_cvt_pk_bf16_f32 v116, v204, v205\n"
"v_cvt_pk_bf16_f32 v117, v206, v207\n"
"v_mfma_f32_16x16x32_bf16 v[128:131], a[188:191], v[80:83], 0\n"
"v_cvt_pk_bf16_f32 v118, v152, v153\n"
"v_cvt_pk_bf16_f32 v119, v154, v155\n"
"v_mfma_f32_16x16x32_bf16 v[128:131], a[184:187], v[72:75], v[128:131]\n"
"buffer_load_dwordx4 v[152:155], v0, s[4:7], s97 offen offset:3072 sc1\n"
"v_mfma_f32_16x16x32_bf16 v[128:131], a[180:183], v[40:43], v[128:131]\n"
"v_xor_b32_e32 v254, 64, v255\n"
"v_xor_b32_e32 v252, 0x60, v255\n"
"v_mfma_f32_16x16x32_bf16 v[128:131], a[176:179], v[36:39], v[128:131]\n"
"ds_write_b64 v254, v[116:117] offset:8192\n"
"v_mfma_f32_16x16x32_bf16 v[128:131], a[172:175], v[68:71], v[128:131]\n"
"v_cmp_u_f32_e32 vcc, v84, v84\n"
"v_mfma_f32_16x16x32_bf16 v[128:131], a[168:171], v[140:143], v[128:131]\n"
"s_cbranch_vccnz .Lmk_p59s\n"
".Lmk_p59r:\n"
"v_mfma_f32_16x16x32_bf16 v[128:131], a[164:167], v[184:187], v[128:131]\n"
"ds_write_b128 v3, v[84:87]\n"
"v_mfma_f32_16x16x32_bf16 v[128:131], a[160:163], v[132:135], v[128:131]\n"
"buffer_load_dwordx4 v[84:87], v1, s[4:7], s97 offen sc1\n"
"v_mfma_f32_16x16x32_bf16 v[156:159], a[192:195], v[132:135], 0\n"
"ds_write_b64 v252, v[118:119] offset:8192\n"
"v_mfma_f32_16x16x32_bf16 v[156:159], a[196:199], v[184:187], v[156:159]\n"
"ds_read_b128 v[116:119], v2 offset:8192\n"
"ds_read_b128 v[160:163], v2 offset:9216\n"
"v_mfma_f32_16x16x32_bf16 v[156:159], a[200:203], v[140:143], v[156:159]\n"
"buffer_load_dwordx4 v[172:175], v1, s[4:7], s97 offen offset:1024 sc1\n"
"v_mfma_f32_16x16x32_bf16 v[156:159], a[204:207], v[68:71], v[156:159]\n"
"v_bfe_u32 v255, v0, 4, 4\n"
"v_bfe_u32 v254, v0, 9, 1\n"
"v_mfma_f32_16x16x32_bf16 v[156:159], a[208:211], v[36:39], v[156:159]\n"
"v_and_b32_e32 v252, 7, v255\n"
"v_xor_b32_e32 v254, v254, v252\n"
"v_mfma_f32_16x16x32_bf16 v[156:159], a[212:215], v[40:43], v[156:159]\n"
"ds_write_b128 v3, v[128:131] offset:1024\n"
"v_mfma_f32_16x16x32_bf16 v[156:159], a[216:219], v[72:75], v[156:159]\n"
"buffer_load_dwordx4 v[128:131], v1, s[4:7], s97 offen offset:2048 sc1\n"
"v_mfma_f32_16x16x32_bf16 v[156:159], a[220:223], v[80:83], v[156:159]\n"
"s_waitcnt lgkmcnt(1)\n"
"v_lshlrev_b32_e32 v255, 7, v255\n"
"v_lshl_add_u32 v255, v254, 4, v255\n"
"v_mfma_f32_16x16x32_bf16 v[200:203], a[252:255], v[80:83], 0\n"
"v_bfe_u32 v254, v0, 8, 1\n"
"v_lshl_add_u32 v255, v254, 3, v255\n"
"v_mfma_f32_16x16x32_bf16 v[200:203], a[248:251], v[72:75], v[200:203]\n"
"buffer_load_dwordx4 v[72:75], v1, s[4:7], s97 offen offset:3072 sc1\n"
"v_mfma_f32_16x16x32_bf16 v[200:203], a[244:247], v[40:43], v[200:203]\n"
"s_add_u32 s97, s59, 0x400000\n"
"v_add_u32_e32 v255, s77, v255\n"
"v_bfe_u32 v252, v0, 7, 3\n"
"v_mfma_f32_16x16x32_bf16 v[200:203], a[240:243], v[36:39], v[200:203]\n"
"v_bfe_u32 v254, v0, 4, 3\n"
"v_xor_b32_e32 v254, v254, v252\n"
"v_mfma_f32_16x16x32_bf16 v[200:203], a[236:239], v[68:71], v[200:203]\n"
"buffer_load_dwordx4 v[36:39], v0, s[4:7], s97 offen sc1\n"
"v_mfma_f32_16x16x32_bf16 v[200:203], a[232:235], v[140:143], v[200:203]\n"
"ds_write_b128 v3, v[156:159] offset:2048\n"
"v_mfma_f32_16x16x32_bf16 v[200:203], a[228:231], v[184:187], v[200:203]\n"
"v_lshlrev_b32_e32 v252, 11, v252\n"
"v_lshl_add_u32 v252, v254, 4, v252\n"
"v_mfma_f32_16x16x32_bf16 v[200:203], a[224:227], v[132:135], v[200:203]\n"
"s_waitcnt vmcnt(13)\n"
"buffer_load_dwordx4 v[40:43], v0, s[4:7], s97 offen offset:1024 sc1\n"
"; @B G2 a.1\n"
"v_mfma_f32_16x16x32_bf16 v[68:71], a[128:131], v[164:167], 0\n"
"v_mfma_f32_16x16x32_bf16 v[68:71], a[132:135], v[168:171], v[68:71]\n"
"v_mfma_f32_16x16x32_bf16 v[68:71], a[136:139], v[208:211], v[68:71]\n"
"buffer_load_dwordx4 v[80:83], v0, s[4:7], s97 offen offset:2048 sc1\n"
"v_mfma_f32_16x16x32_bf16 v[68:71], a[140:143], v[212:215], v[68:71]\n"
"s_waitcnt vmcnt(11)\n"
"v_mfma_f32_16x16x32_bf16 v[68:71], a[144:147], v[64:67], v[68:71]\n"
"v_mfma_f32_16x16x32_bf16 v[68:71], a[148:151], v[100:103], v[68:71]\n"
"ds_write_b128 v3, v[200:203] offset:3072\n"
"v_mfma_f32_16x16x32_bf16 v[68:71], a[152:155], v[60:63], v[68:71]\n"
"buffer_load_dwordx4 v[132:135], v0, s[4:7], s97 offen offset:3072 sc1\n"
"v_mfma_f32_16x16x32_bf16 v[68:71], a[156:159], v[76:79], v[68:71]\n"
"v_mfma_f32_16x16x32_bf16 v[140:143], a[188:191], v[76:79], 0\n"
"v_mfma_f32_16x16x32_bf16 v[140:143], a[184:187], v[60:63], v[140:143]\n"
"buffer_load_dwordx4 v[156:159], v1, s[4:7], s97 offen sc1\n"
"v_mfma_f32_16x16x32_bf16 v[140:143], a[180:183], v[100:103], v[140:143]\n"
"v_mfma_f32_16x16x32_bf16 v[140:143], a[176:179], v[64:67], v[140:143]\n"
"v_mfma_f32_16x16x32_bf16 v[140:143], a[172:175], v[212:215], v[140:143]\n"
"s_nop 1\n"
"v_cmp_u_f32_e32 vcc, v68, v68\n"
"v_mfma_f32_16x16x32_bf16 v[140:143], a[168:171], v[208:211], v[140:143]\n"
"s_cbranch_vccnz .Lmk_p60s\n"
".Lmk_p60r:\n"
"v_mfma_f32_16x16x32_bf16 v[140:143], a[164:167], v[168:171], v[140:143]\n"
"ds_write_b128 v3, v[68:71] offset:6144\n"
"v_mfma_f32_16x16x32_bf16 v[140:143], a[160:163], v[164:167], v[140:143]\n"
"buffer_load_dwordx4 v[68:71], v1, s[4:7], s97 offen offset:1024 sc1\n"
"v_mfma_f32_16x16x32_bf16 v[184:187], a[192:195], v[164:167], 0\n"
"v_mfma_f32_16x16x32_bf16 v[184:187], a[196:199], v[168:171], v[184:187]\n"
"v_mfma_f32_16x16x32_bf16 v[184:187], a[200:203], v[208:211], v[184:187]\n"
"buffer_load_dwordx4 v[200:203], v1, s[4:7], s97 offen offset:2048 sc1\n"
"v_mfma_f32_16x16x32_bf16 v[184:187], a[204:207], v[212:215], v[184:187]\n"
"v_mfma_f32_16x16x32_bf16 v[184:187], a[208:211], v[64:67], v[184:187]\n"
"ds_read_b128 v[204:207], v2\n"
"ds_read_b128 v[216:219], v2 offset:1024\n"
"v_mfma_f32_16x16x32_bf16 v[184:187], a[212:215], v[100:103], v[184:187]\n"
"ds_write_b128 v3, v[140:143] offset:7168\n"
"v_mfma_f32_16x16x32_bf16 v[184:187], a[216:219], v[60:63], v[184:187]\n"
"buffer_load_dwordx4 v[140:143], v1, s[4:7], s97 offen offset:3072 sc1\n"
"v_mfma_f32_16x16x32_bf16 v[184:187], a[220:223], v[76:79], v[184:187]\n"
"v_mfma_f32_16x16x32_bf16 v[220:223], a[252:255], v[76:79], 0\n"
"v_mfma_f32_16x16x32_bf16 v[220:223], a[248:251], v[60:63], v[220:223]\n"
"s_waitcnt lgkmcnt(1)\n"
"v_lshlrev_b32_e32 v254, 16, v204\n"
"v_and_b32_e32 v251, 0xffff0000, v204\n"
"v_lshlrev_b32_e32 v250, 16, v205\n"
"v_mfma_f32_16x16x32_bf16 v[220:223], a[244:247], v[100:103], v[220:223]\n"
"v_and_b32_e32 v249, 0xffff0000, v205\n"
"v_lshlrev_b32_e32 v248, 16, v206\n"
"v_and_b32_e32 v247, 0xffff0000, v206\n"
"v_mfma_f32_16x16x32_bf16 v[220:223], a[240:243], v[64:67], v[220:223]\n"
"v_lshlrev_b32_e32 v246, 16, v207\n"
"v_and_b32_e32 v245, 0xffff0000, v207\n"
"v_lshlrev_b32_e32 v244, 16, v216\n"
"v_mfma_f32_16x16x32_bf16 v[220:223], a[236:239], v[212:215], v[220:223]\n"
"s_add_u32 s97, s74, 0x40000\n"
"buffer_store_dwordx4 v[116:119], v253, s[16:19], s97 offen sc1\n"
"v_mfma_f32_16x16x32_bf16 v[220:223], a[232:235], v[208:211], v[220:223]\n"
"ds_write_b128 v3, v[184:187] offset:8192\n"
"v_mfma_f32_16x16x32_bf16 v[220:223], a[228:231], v[168:171], v[220:223]\n"
"v_and_b32_e32 v243, 0xffff0000, v216\n"
"v_lshlrev_b32_e32 v242, 16, v217\n"
"v_and_b32_e32 v241, 0xffff0000, v217\n"
"v_mfma_f32_16x16x32_bf16 v[220:223], a[224:227], v[164:167], v[220:223]\n"
"s_waitcnt vmcnt(13)\n"
"v_lshlrev_b32_e32 v240, 16, v218\n"
"v_and_b32_e32 v239, 0xffff0000, v218\n"
"v_lshlrev_b32_e32 v238, 16, v219\n"
"; @B G2 a.2\n"
"v_mfma_f32_16x16x32_bf16 v[60:63], a[128:131], v[96:99], 0\n"
"v_and_b32_e32 v237, 0xffff0000, v219\n"
"v_mfma_f32_16x16x32_bf16 v[60:63], a[132:135], v[112:115], v[60:63]\n"
"v_mfma_f32_16x16x32_bf16 v[60:63], a[136:139], v[52:55], v[60:63]\n"
"v_mfma_f32_16x16x32_bf16 v[60:63], a[140:143], v[152:155], v[60:63]\n"
"s_waitcnt vmcnt(9)\n"
"v_mfma_f32_16x16x32_bf16 v[60:63], a[144:147], v[84:87], v[60:63]\n"
"v_mfma_f32_16x16x32_bf16 v[60:63], a[148:151], v[172:175], v[60:63]\n"
"ds_write_b128 v3, v[220:223] offset:9216\n"
"v_mfma_f32_16x16x32_bf16 v[60:63], a[152:155], v[128:131], v[60:63]\n"
"s_add_u32 s97, s74, 0x44000\n"
"v_mfma_f32_16x16x32_bf16 v[60:63], a[156:159], v[72:75], v[60:63]\n"
"v_mfma_f32_16x16x32_bf16 v[64:67], a[188:191], v[72:75], 0\n"
"buffer_store_dwordx4 v[160:163], v253, s[16:19], s97 offen sc1\n"
"v_mfma_f32_16x16x32_bf16 v[64:67], a[184:187], v[128:131], v[64:67]\n"
"s_waitcnt lgkmcnt(0)\n"
"s_barrier\n"
"v_mfma_f32_16x16x32_bf16 v[64:67], a[180:183], v[172:175], v[64:67]\n"
"ds_read_b128 v[76:79], v4\n"
"ds_read_b128 v[100:103], v5\n"
"v_mfma_f32_16x16x32_bf16 v[64:67], a[176:179], v[84:87], v[64:67]\n"
"v_mfma_f32_16x16x32_bf16 v[64:67], a[172:175], v[152:155], v[64:67]\n"
"v_cmp_u_f32_e32 vcc, v60, v60\n"
"v_mfma_f32_16x16x32_bf16 v[64:67], a[168:171], v[52:55], v[64:67]\n"
"s_cbranch_vccnz .Lmk_p61s\n"
".Lmk_p61r:\n"
"v_mfma_f32_16x16x32_bf16 v[64:67], a[164:167], v[112:115], v[64:67]\n"
"ds_write_b128 v3, v[60:63] offset:12288\n"
"v_mfma_f32_16x16x32_bf16 v[64:67], a[160:163], v[96:99], v[64:67]\n"
"s_waitcnt lgkmcnt(1)\n"
"v_add_f32_e32 v76, v76, v254\n"
"v_add_f32_e32 v77, v77, v251\n"
"v_add_f32_e32 v78, v78, v250\n"
"v_mfma_f32_16x16x32_bf16 v[60:63], a[192:195], v[96:99], 0\n"
"ds_read_b128 v[116:119], v4 offset:1024\n"
"ds_read_b128 v[160:163], v5 offset:1024\n"
"v_add_f32_e32 v79, v79, v249\n"
"v_mfma_f32_16x16x32_bf16 v[60:63], a[196:199], v[112:115], v[60:63]\n"
"v_add_f32_e32 v76, v76, v100\n"
"v_add_f32_e32 v77, v77, v101\n"
"v_add_f32_e32 v78, v78, v102\n"
"v_mfma_f32_16x16x32_bf16 v[60:63], a[200:203], v[52:55], v[60:63]\n"
"v_add_f32_e32 v79, v79, v103\n"
"v_mfma_f32_16x16x32_bf16 v[60:63], a[204:207], v[152:155], v[60:63]\n"
"v_mfma_f32_16x16x32_bf16 v[60:63], a[208:211], v[84:87], v[60:63]\n"
"v_mfma_f32_16x16x32_bf16 v[60:63], a[212:215], v[172:175], v[60:63]\n"
"ds_write_b128 v3, v[64:67] offset:13312\n"
"v_mfma_f32_16x16x32_bf16 v[60:63], a[216:219], v[128:131], v[60:63]\n"
"ds_read_b128 v[64:67], v4 offset:2048\n"
"ds_read_b128 v[100:103], v5 offset:2048\n"
"s_waitcnt lgkmcnt(3)\n"
"v_mfma_f32_16x16x32_bf16 v[60:63], a[220:223], v[72:75], v[60:63]\n"
"v_add_f32_e32 v116, v116, v248\n"
"v_add_f32_e32 v117, v117, v247\n"
"v_add_f32_e32 v118, v118, v246\n"
"v_mfma_f32_16x16x32_bf16 v[164:167], a[252:255], v[72:75], 0\n"
"v_add_f32_e32 v119, v119, v245\n"
"v_add_f32_e32 v116, v116, v160\n"
"v_add_f32_e32 v117, v117, v161\n"
"v_mfma_f32_16x16x32_bf16 v[164:167], a[248:251], v[128:131], v[164:167]\n"
"v_add_f32_e32 v118, v118, v162\n"
"v_add_f32_e32 v119, v119, v163\n"
"v_mfma_f32_16x16x32_bf16 v[164:167], a[244:247], v[172:175], v[164:167]\n"
"v_mfma_f32_16x16x32_bf16 v[164:167], a[240:243], v[84:87], v[164:167]\n"
"s_waitcnt lgkmcnt(0)\n"
"v_add_f32_e32 v64, v64, v244\n"
"v_add_f32_e32 v65, v65, v243\n"
"v_add_f32_e32 v66, v66, v242\n"
"v_mfma_f32_16x16x32_bf16 v[164:167], a[236:239], v[152:155], v[164:167]\n"
"ds_read_b128 v[72:75], v4 offset:3072\n"
"ds_read_b128 v[84:87], v5 offset:3072\n"
"v_add_f32_e32 v67, v67, v241\n"
"v_mfma_f32_16x16x32_bf16 v[164:167], a[232:235], v[52:55], v[164:167]\n"
"ds_write_b128 v3, v[60:63] offset:14336\n"
"v_mfma_f32_16x16x32_bf16 v[164:167], a[228:231], v[112:115], v[164:167]\n"
"v_add_f32_e32 v64, v64, v100\n"
"v_add_f32_e32 v65, v65, v101\n"
"v_add_f32_e32 v66, v66, v102\n"
"v_mfma_f32_16x16x32_bf16 v[164:167], a[224:227], v[96:99], v[164:167]\n"
"s_waitcnt vmcnt(6)\n"
"v_add_f32_e32 v67, v67, v103\n"
"; @B G2 a.3\n"
"v_mfma_f32_16x16x32_bf16 v[52:55], a[128:131], v[36:39], v[76:79]\n"
"v_mfma_f32_16x16x32_bf16 v[52:55], a[132:135], v[40:43], v[52:55]\n"
"s_waitcnt lgkmcnt(1)\n"
"v_add_f32_e32 v72, v72, v240\n"
"v_add_f32_e32 v73, v73, v239\n"
"v_add_f32_e32 v74, v74, v238\n"
"v_mfma_f32_16x16x32_bf16 v[52:55], a[136:139], v[80:83], v[52:55]\n"
"v_add_f32_e32 v75, v75, v237\n"
"v_add_f32_e32 v72, v72, v84\n"
"v_add_f32_e32 v73, v73, v85\n"
"v_mfma_f32_16x16x32_bf16 v[52:55], a[140:143], v[132:135], v[52:55]\n"
"s_waitcnt vmcnt(2)\n"
"v_add_f32_e32 v74, v74, v86\n"
"v_add_f32_e32 v75, v75, v87\n"
"v_mfma_f32_16x16x32_bf16 v[52:55], a[144:147], v[156:159], v[52:55]\n"
"v_mfma_f32_16x16x32_bf16 v[52:55], a[148:151], v[68:71], v[52:55]\n"
"ds_write_b128 v3, v[164:167] offset:15360\n"
"v_mfma_f32_16x16x32_bf16 v[52:55], a[152:155], v[200:203], v[52:55]\n"
"v_mfma_f32_16x16x32_bf16 v[52:55], a[156:159], v[140:143], v[52:55]\n"
"v_mfma_f32_16x16x32_bf16 v[60:63], a[188:191], v[140:143], v[116:119]\n"
"v_mfma_f32_16x16x32_bf16 v[60:63], a[184:187], v[200:203], v[60:63]\n"
"s_waitcnt lgkmcnt(0)\n"
"s_barrier\n"
"v_mfma_f32_16x16x32_bf16 v[60:63], a[180:183], v[68:71], v[60:63]\n"
"ds_read_b128 v[84:87], v6\n"
"ds_read_b128 v[96:99], v6 offset:1024\n"
"v_mfma_f32_16x16x32_bf16 v[60:63], a[176:179], v[156:159], v[60:63]\n"
"ds_read_b128 v[100:103], v6 offset:2048\n"
"ds_read_b128 v[112:115], v6 offset:3072\n"
"v_mfma_f32_16x16x32_bf16 v[60:63], a[172:175], v[132:135], v[60:63]\n"
"v_cmp_u_f32_e32 vcc, v52, v52\n"
"v_mfma_f32_16x16x32_bf16 v[60:63], a[168:171], v[80:83], v[60:63]\n"
"s_cbranch_vccnz .Lmk_p62s\n"
".Lmk_p62r:\n"
"v_mfma_f32_16x16x32_bf16 v[60:63], a[164:167], v[40:43], v[60:63]\n"
"v_mfma_f32_16x16x32_bf16 v[60:63], a[160:163], v[36:39], v[60:63]\n"
"v_mfma_f32_16x16x32_bf16 v[64:67], a[192:195], v[36:39], v[64:67]\n"
"v_mfma_f32_16x16x32_bf16 v[64:67], a[196:199], v[40:43], v[64:67]\n"
"v_mfma_f32_16x16x32_bf16 v[64:67], a[200:203], v[80:83], v[64:67]\n"
"v_mfma_f32_16x16x32_bf16 v[64:67], a[204:207], v[132:135], v[64:67]\n"
"v_mfma_f32_16x16x32_bf16 v[64:67], a[208:211], v[156:159], v[64:67]\n"
"s_waitcnt lgkmcnt(0)\n"
"v_add_f32_e32 v84, v52, v84\n"
"v_add_f32_e32 v85, v53, v85\n"
"v_add_f32_e32 v86, v54, v86\n"
"v_mfma_f32_16x16x32_bf16 v[64:67], a[212:215], v[68:71], v[64:67]\n"
"v_add_f32_e32 v87, v55, v87\n"
"v_add_f32_e32 v96, v60, v96\n"
"v_add_f32_e32 v97, v61, v97\n"
"v_mfma_f32_16x16x32_bf16 v[64:67], a[216:219], v[200:203], v[64:67]\n"
"v_add_f32_e32 v98, v62, v98\n"
"v_add_f32_e32 v99, v63, v99\n"
"v_cvt_pk_bf16_f32 v52, v84, v85\n"
"v_mfma_f32_16x16x32_bf16 v[64:67], a[220:223], v[140:143], v[64:67]\n"
"v_cvt_pk_bf16_f32 v53, v86, v87\n"
"v_cvt_pk_bf16_f32 v54, v96, v97\n"
"v_cvt_pk_bf16_f32 v55, v98, v99\n"
"v_mfma_f32_16x16x32_bf16 v[72:75], a[252:255], v[140:143], v[72:75]\n"
"v_xor_b32_e32 v254, 32, v255\n"
"v_mfma_f32_16x16x32_bf16 v[72:75], a[248:251], v[200:203], v[72:75]\n"
"ds_write_b64 v255, v[52:53] offset:0\n"
"v_mfma_f32_16x16x32_bf16 v[72:75], a[244:247], v[68:71], v[72:75]\n"
"ds_write_b64 v254, v[54:55] offset:0\n"
"v_mfma_f32_16x16x32_bf16 v[72:75], a[240:243], v[156:159], v[72:75]\n"
"v_mfma_f32_16x16x32_bf16 v[72:75], a[236:239], v[132:135], v[72:75]\n"
"v_mfma_f32_16x16x32_bf16 v[72:75], a[232:235], v[80:83], v[72:75]\n"
"v_mfma_f32_16x16x32_bf16 v[72:75], a[228:231], v[40:43], v[72:75]\n"
"v_mfma_f32_16x16x32_bf16 v[72:75], a[224:227], v[36:39], v[72:75]\n"
"v_add_f32_e32 v100, v64, v100\n"
"v_add_f32_e32 v101, v65, v101\n"
"v_add_f32_e32 v102, v66, v102\n"
"v_add_f32_e32 v103, v67, v103\n"
"s_nop 3\n"
"v_add_f32_e32 v112, v72, v112\n"
"v_add_f32_e32 v113, v73, v113\n"
"v_add_f32_e32 v114, v74, v114\n"
"v_add_f32_e32 v115, v75, v115\n"
"v_cvt_pk_bf16_f32 v36, v100, v101\n"
"v_cvt_pk_bf16_f32 v37, v102, v103\n"
"v_cvt_pk_bf16_f32 v38, v112, v113\n"
"v_cvt_pk_bf16_f32 v39, v114, v115\n"
"v_xor_b32_e32 v254, 64, v255\n"
"v_xor_b32_e32 v253, 0x60, v255\n"
"ds_write_b64 v254, v[36:37] offset:0\n"
"ds_write_b64 v253, v[38:39] offset:0\n"
"ds_read_b128 v[36:39], v2\n"
"ds_read_b128 v[40:43], v2 offset:1024\n"
"s_waitcnt lgkmcnt(0)\n"
"buffer_store_dwordx4 v[36:39], v252, s[16:19], s74 offen sc1\n"
"s_add_u32 s97, s74, 0x4000\n"
"buffer_store_dwordx4 v[40:43], v252, s[16:19], s97 offen sc1\n"
"s_cmp_eq_u32 s81, 0\n"
"s_cbranch_scc1 .Lmk_fin\n"
"buffer_store_dwordx2 v[18:19], v11, s[4:7], s81 offen sc1\n"
".Lmk_fin:\n"
"s_endpgm\n"
".Lmk_end:\n"
    :: "{s[4:7]}"(wsr), "{s[8:11]}"(wsr), "{s[12:15]}"(wsk), "{s[16:19]}"(outr), "{s[52:67]}"(sa), "{s[68:83]}"(sb),
       "{s[20:27]}"(pa), "{s[28:31]}"(wa), "{s[32:35]}"(wb), "{s[36:39]}"(oa), "{s[40:43]}"(ob), "{s[44:51]}"(pc), "{s99}"(uni((bix * 4 + w) * 1024)),
       "{v[0:15]}"(va), "{v[16:23]}"(vb), "{v[32:47]}"(pz), "{v48}"(sta), "{v50}"(pf)
    : "memory", "v255", "a255", "s98", "vcc");
  __builtin_unreachable();
}

}

extern "C" void kernel_launch(void* const* d_in, const int* in_sizes, int n_in, void* d_out, int out_size,
                              void* d_ws, size_t ws_size, hipStream_t stream) {
  (void)hipFuncSetAttribute((const void*)mega_kernel, hipFuncAttributeMaxDynamicSharedMemorySize, LDS_BYTES);
  mega_kernel<<<256, 256, LDS_BYTES, stream>>>((const u16*)d_in[0], (const u16*)d_in[1], (const u16*)d_in[2],
                                               (const u16*)d_in[4], (const u16*)d_in[5], (u16*)d_out, (char*)d_ws);
}
```

```cpp
#include <hip/hip_runtime.h>
#include <stdint.h>

namespace {

typedef unsigned short u16;
typedef unsigned long long u64;
typedef short s16x4 __attribute__((ext_vector_type(4)));
typedef float f32x4 __attribute__((ext_vector_type(4)));
typedef unsigned u32x4 __attribute__((ext_vector_type(4)));
typedef unsigned u32x2 __attribute__((ext_vector_type(2)));
typedef unsigned u32x8 __attribute__((ext_vector_type(8)));
typedef unsigned u32x16 __attribute__((ext_vector_type(16)));
typedef __amdgpu_buffer_rsrc_t rsrc_t;
#define LAS __attribute__((address_space(3)))
#define DI __device__ __forceinline__
#define LDSP(a) ((LAS char*)(size_t)(a))

constexpr unsigned MB = 1u << 20;
constexpr unsigned WS_XF = 0;
constexpr unsigned WS_OF = 16 * MB;
constexpr unsigned WS_KV = 32 * MB;
constexpr unsigned WS_WF = 64 * MB;
constexpr unsigned WS_WO = 76 * MB;
constexpr unsigned WS_ST = 80 * MB;
constexpr unsigned WS_CS = 81 * MB;
constexpr unsigned WS_FL = 82 * MB;
constexpr unsigned WS_EP = WS_FL + 2048 * 128;
constexpr unsigned WS_DBG = 96 * MB;
constexpr unsigned OOB = 0x80000000u;
constexpr int L_RES = 0, L_K = 16384, L_V = 49152, L_INB = 81920, L_CS = 155648, L_PM = L_CS + 1024, L_MB = L_PM + 2048, L_SCR = L_MB + 2048;
constexpr int L_WS = 57344;
constexpr int LDS_BYTES = 163840;

template <int N> DI void wait_vm() { asm volatile("s_waitcnt vmcnt(%0)" ::"n"(N) : "memory"); }
template <int N> DI void nops() { asm volatile("s_nop %0" ::"n"(N - 1)); }
template <int OFF> DI void ld8(u32x2& d, rsrc_t r, unsigned v, unsigned s) {
  asm volatile("buffer_load_dwordx2 %0, %1, %2, %3 offen offset:%4 sc1" : "=v"(d) : "v"(v), "s"(r), "s"(s), "i"(OFF) : "memory");
}
template <int OFF> DI void st16(const u32x4& d, rsrc_t r, unsigned v, unsigned s) {
  asm volatile("buffer_store_dwordx4 %0, %1, %2, %3 offen offset:%4 sc1\n\ts_nop 1" :: "v"(d), "v"(v), "s"(r), "s"(s), "i"(OFF) : "memory");
}
template <int OFF> DI void st8(const u32x2& d, rsrc_t r, unsigned v, unsigned s) {
  asm volatile("buffer_store_dwordx2 %0, %1, %2, %3 offen offset:%4 sc1" :: "v"(d), "v"(v), "s"(r), "s"(s), "i"(OFF) : "memory");
}
DI void st4(unsigned d, rsrc_t r, unsigned v, unsigned s) {
  asm volatile("buffer_store_dword %0, %1, %2, %3 offen sc1" :: "v"(d), "v"(v), "s"(r), "s"(s) : "memory");
}
DI void dma(rsrc_t r, unsigned v, unsigned s, unsigned lm) {
  asm volatile("s_mov_b32 m0, %3\n\ts_nop 0\n\tbuffer_load_dwordx4 %0, %1, %2 offen lds" :: "v"(v), "s"(r), "s"(s), "s"(lm) : "memory");
}
DI void touch(rsrc_t r, unsigned v) {
  asm volatile("s_mov_b32 m0, %2\n\ts_nop 0\n\tbuffer_load_dword %0, %1, 0 offen lds" :: "v"(v), "s"(r), "s"((unsigned)L_SCR) : "memory");
}
DI void mma_v(f32x4& c, const u32x4& a, const u32x4& b) { asm volatile("v_mfma_f32_16x16x32_bf16 %0, %1, %2, %0" : "+v"(c) : "v"(a), "v"(b)); }
DI void mm0_v(f32x4& c, const u32x4& a, const u32x4& b) { asm volatile("v_mfma_f32_16x16x32_bf16 %0, %1, %2, 0" : "=v"(c) : "v"(a), "v"(b)); }
DI unsigned uni(unsigned v) { v = __builtin_amdgcn_readfirstlane(v); asm volatile("" : "+s"(v)); return v; }
template <class T> DI void pin(T& v) { asm volatile("" : "+v"(v)); }
DI float sum_x16(float v) {
  auto r = __builtin_amdgcn_permlane16_swap(__float_as_uint(v), __float_as_uint(v), false, false);
  return __uint_as_float(r[0]) + __uint_as_float(r[1]);
}
DI float sum_x32(float v) {
  auto r = __builtin_amdgcn_permlane32_swap(__float_as_uint(v), __float_as_uint(v), false, false);
  return __uint_as_float(r[0]) + __uint_as_float(r[1]);
}
DI u32x2 lds_tr(unsigned a) { return __builtin_bit_cast(u32x2, __builtin_amdgcn_ds_read_tr16_b64_v4i16((LAS s16x4*)LDSP(a))); }
DI u32x4 cat(const u32x2& a, const u32x2& b) { return __builtin_shufflevector(a, b, 0, 1, 2, 3); }
DI u32x2 lds_r2(unsigned a) { return *(LAS u32x2*)LDSP(a); }
DI void lds_w4(unsigned a, const u32x4& v) { *(LAS u32x4*)LDSP(a) = v; }
DI unsigned rot(unsigned g, unsigned sl) { return (g + (0x641u >> 4 * sl)) & 15; }
DI int swzK(int u) { return ((((u >> 1) ^ u) & 1) << 1) | (u >> 1); }

#define STAMP(i)

__global__ __launch_bounds__(256, 1) void mega_kernel(const u16* __restrict__ x0, const u16* __restrict__ WqA,
                                                      const u16* __restrict__ WoA, const u16* __restrict__ WqB,
                                                      const u16* __restrict__ WoB, u16* __restrict__ out, char* ws) {
  const unsigned bid = blockIdx.x, w = __builtin_amdgcn_readfirstlane(threadIdx.x >> 6);
  unsigned lane = threadIdx.x & 63;
  unsigned stw = 0;
  STAMP(0);
  u32x2 tag;
  {
    u64 t0; unsigned t1, t2, t3, v1, v2;
    asm volatile(
      "s_getpc_b64 %[t0]\n.Lmk_here:\n"
      "s_lshr_b32 %[t1], %[bid], 3\n s_lshl2_add_u32 %[t1], %[t1], %[w]\n s_lshl_b32 %[t1], %[t1], 10\n"
      "v_lshlrev_b32 %[v1], 4, %[lane]\n"
      "s_mov_b64 s[88:89], %[t0]\n s_and_b32 s88, s88, -16\n s_and_b32 s89, s89, 0xffff\n s_mov_b32 s90, (.Lmk_end-.Lmk_here)&0xfffffff0\n s_mov_b32 s91, 0x20000\n"
      "s_mov_b32 m0, %[scr]\n"
      "v_lshrrev_b32 %[v2], 3, %[lane]\n"
      "buffer_load_dwordx4 %[v1], s[88:91], %[t1] offen lds\n"
      "v_and_b32 %[v1], 0x70, %[v1]\n v_lshl_or_b32 %[v1], %[v2], 11, %[v1]\n"
      "s_and_b32 %[t1], %[bid], 4\n s_lshr_b32 %[t2], %[bid], 6\n s_add_u32 %[t1], %[t1], %[t2]\n s_lshl3_add_u32 %[t1], %[t1], %[w]\n"
      "s_and_b32 %[t3], %[bid], 1\n s_lshl1_add_u32 %[t1], %[t1], %[t3]\n s_lshl_b32 %[t1], %[t1], 8\n"
      "s_bfe_u32 %[t3], %[bid], 0x30003\n s_add_u32 %[t1], %[t1], %[t3]\n s_and_b32 %[t3], %[bid], 2\n s_lshl2_add_u32 %[t1], %[t3], %[t1]\n s_lshl_b32 %[t1], %[t1], 7\n"
      "s_lshl_b32 %[t3], %[w], 11\n"
      : [t0] "=&s"(t0), [t1] "=&s"(t1), [t2] "=&s"(t2), [t3] "=&s"(t3), [v1] "=&v"(v1), [v2] "=&v"(v2)
      : [bid] "s"(bid), [w] "s"(w), [lane] "v"(lane), [scr] "i"(L_SCR + 1024) : "memory", "s88", "s89", "s90", "s91", "scc");
    v2 = v2 * 6144 + (lane & 7) * 16;
    const unsigned g_ = (bid & 3) * 4 + (bid >> 4 & 3);
    const unsigned src = (((bid & 4) + (bid >> 6)) * 128 + (bid >> 3 & 1) * 64 + w * 16) * 6144, dst = L_INB + w * 2048;
    const unsigned r0 = src + rot(g_, 0) * 128, r1 = src + 2048 + rot(g_, 1) * 128, r2 = src + 4096 + rot(g_, 2) * 128;
    asm volatile(
      "s_mov_b64 s[88:89], %[wq]\n s_and_b32 s89, s89, 0xffff\n s_mov_b32 s90, 0x600000\n s_mov_b32 s91, 0x20000\n"
      "s_mov_b32 m0, %[dst]\n s_add_u32 %[t2], %[r0], 0xc000\n buffer_load_dwordx4 %[v2], s[88:91], %[r0] offen lds\n"
      "s_add_u32 m0, %[dst], 0x400\n s_nop 0\n buffer_load_dwordx4 %[v2], s[88:91], %[t2] offen lds\n"
      "s_add_u32 m0, %[dst], 0x2000\n s_add_u32 %[t2], %[r1], 0xc000\n buffer_load_dwordx4 %[v2], s[88:91], %[r1] offen lds\n"
      "s_add_u32 m0, %[dst], 0x2400\n s_nop 0\n buffer_load_dwordx4 %[v2], s[88:91], %[t2] offen lds\n"
      "s_add_u32 m0, %[dst], 0x4000\n s_add_u32 %[t2], %[r2], 0xc000\n buffer_load_dwordx4 %[v2], s[88:91], %[r2] offen lds\n"
      "s_add_u32 m0, %[dst], 0x4400\n s_nop 0\n buffer_load_dwordx4 %[v2], s[88:91], %[t2] offen lds\n"
      "s_mov_b64 s[88:89], %[x]\n s_and_b32 s89, s89, 0xffff\n s_mov_b32 s90, 0x400000\n s_mov_b32 s91, 0x20000\n"
      "s_add_u32 m0, %[t3], 0x2000\n s_add_u32 %[t2], %[t1], 0x40000\n buffer_load_dwordx4 %[v1], s[88:91], %[t2] offen lds\n"
      "s_add_u32 m0, %[t3], 0x2400\n s_add_u32 %[t2], %[t1], 0x44000\n buffer_load_dwordx4 %[v1], s[88:91], %[t2] offen lds\n"
      "s_mov_b32 m0, %[t3]\n s_add_u32 %[t2], %[t1], 0x4000\n buffer_load_dwordx4 %[v1], s[88:91], %[t1] offen lds\n"
      "s_add_u32 m0, %[t3], 0x400\n s_mov_b32 %[t1], %[ep]\n buffer_load_dwordx4 %[v1], s[88:91], %[t2] offen lds\n"
      "s_mov_b64 s[88:89], %[ws]\n s_and_b32 s89, s89, 0xffff\n s_mov_b32 s90, 0x8000000\n"
      "buffer_load_dwordx2 %[tag], off, s[88:91], %[t1] sc1\n"
      : [t1] "+s"(t1), [t2] "+s"(t2), [tag] "=&v"(tag), [lane] "+v"(lane)
      : [t3] "s"(t3), [v1] "v"(v1), [v2] "v"(v2), [x] "s"(x0), [ws] "s"(ws), [wq] "s"(WqA), [dst] "s"(dst), [ep] "i"(WS_EP),
        [r0] "s"(r0), [r1] "s"(r1), [r2] "s"(r2),
        "s"(WoA), "s"(WqB), "s"(WoB), "s"(out) : "memory", "s88", "s89", "s90", "s91", "scc");
  }
  const unsigned xcd = bid & 7, bix = bid >> 3;
  const unsigned c = (xcd >> 2) * 4 + (bix >> 3), h = (xcd & 3) * 8 + (bix & 7), par = h & 1, g = h >> 1;
  const unsigned p2 = h >> 3 & 1, g2 = (h >> 4) * 8 + (h & 7);
  const unsigned fr = lane & 15, fq = lane >> 4;
  const unsigned dl = 0, wr = (w + dl) & 3;
  const rsrc_t wsr = __builtin_amdgcn_make_buffer_rsrc(ws, 0, 128 * MB, 0x00020000);
  const unsigned l16 = lane * 16, lw = w * 2048 + l16;
  const unsigned fl0 = lane == 0 ? 0u : OOB;
  const unsigned myfl = WS_FL + (c * 64 + h) * 128 + w * 8;
  const unsigned osrc0 = (lane >> 3) * 2048 + (lane & 7) * 16;
  const rsrc_t wa = __builtin_amdgcn_make_buffer_rsrc((void*)WqA, 0, 6 * MB, 0x00020000);
  const unsigned rho = 0, wbase = w * 256 * 6144 + h * 64;
  const unsigned wv = (lane >> 2) * 6144 + ((lane & 3) ^ ((lane >> 3) & 2)) * 16;
  auto piece = [&](int q, int j) __attribute__((always_inline)) {
    const unsigned k = j ^ par, src = uni(wbase + (((rho + 2 * q) & 7) + k) * 196608), dst = uni(q + k ? L_INB + w * 18432 + (2 * q + k - 1) * 6144 : L_WS + w * 6144);
#pragma unroll
    for (int i = 0; i < 6; ++i) dma(wa, wv, src + (i & 1) * 98304 + (i >> 1) * 2048, dst + i * 1024);
  };
  const unsigned kb = 2 * c + par;
  const rsrc_t oa = __builtin_amdgcn_make_buffer_rsrc((void*)WoA, 0, 2 * MB, 0x00020000);
  const rsrc_t wb = __builtin_amdgcn_make_buffer_rsrc((void*)WqB, 0, 6 * MB, 0x00020000);
  const rsrc_t ob = __builtin_amdgcn_make_buffer_rsrc((void*)WoB, 0, 2 * MB, 0x00020000);
  const unsigned wsrc = (w * 16 + (lane >> 3)) * 6144 + (lane & 7) * 16, osrc = w * 16 * 2048 + osrc0;
  auto get_o = [&](int t) __attribute__((always_inline)) {
#pragma unroll
    for (int i = 0; i < 2; ++i) dma(t ? ob : oa, osrc, (kb * 64 + i * 8) * 2048 + rot(g, 1 + t) * 128, L_V + 8192 + t * 8192 + w * 2048 + i * 1024);
  };
  auto get_q = [&](int t) __attribute__((always_inline)) {
#pragma unroll
    for (int i = 0; i < 6; ++i)
      dma(t ? wb : wa, wsrc, (kb * 64 + (i & 1) * 8) * 6144 + (i >> 1) * 2048 + rot(g, i >> 1) * 128, (t ? L_K : L_INB) + (i >> 1) * 8192 + w * 2048 + (i & 1) * 1024);
  };
  const unsigned fr8 = lane < 32 ? lane * 4 : OOB;
  u32x4 ones = {0x3F803F80u, 0x3F803F80u, 0x3F803F80u, 0x3F803F80u};
  unsigned sta = 0;
  auto publish = [&](int s, bool now) __attribute__((always_inline)) {
    const unsigned a = L_RES + s * 8192 + w * 2048 + fr * 128 + fq * 8, T = c * 16 + 8 * s + 2 * wr + p2;
    u32x4 y0 = cat(lds_r2(a), lds_r2(a + 32)), y1 = cat(lds_r2(a + 64), lds_r2(a + 96));
    f32x4 sm, gm;
    pin(y0); pin(y1); pin(ones);
    nops<2>();
    mm0_v(sm, ones, y0); mm0_v(gm, y0, y0); mma_v(sm, ones, y1); mma_v(gm, y1, y1);
    if (now) { st16<0>(y0, wsr, l16, WS_XF + T * 32768 + g2 * 2048); st16<1024>(y1, wsr, l16, WS_XF + T * 32768 + g2 * 2048); }
    lds_w4(lw + L_RES + s * 8192, y0); lds_w4(lw + L_RES + s * 8192 + 1024, y1);
    nops<8>();
    pin(sm); pin(gm);
    const float sq = sum_x32(sum_x16(fq != fr >> 2 ? 0.f : fr & 2 ? (fr & 1 ? gm[3] : gm[2]) : (fr & 1 ? gm[1] : gm[0])));
    const unsigned st = __float_as_uint(fq ? sq : sm[0]);
    if (now) st4(st, wsr, fr8, WS_ST + T * 2048 + g2 * 128); else sta = st;
  };
  auto next_tag = [&]() __attribute__((always_inline)) {
    pin(tag);
    const u64 t = (((u64)tag[1] << 32) | tag[0]) + 1; tag = (u32x2){(unsigned)t, (unsigned)(t >> 32)};
  };
  const unsigned cb = c * 16 * 32768, skv = WS_KV + (c * 32 + h) * 32768;
  const unsigned soo = WS_OF + cb + wr * 65536 + h * 1024, sxo = WS_XF + cb + (2 * wr + p2) * 32768 + g2 * 2048, sso = WS_ST + (c * 16 + 2 * wr + p2) * 2048 + g2 * 128;
  auto clear = [&](int from, int to) __attribute__((always_inline)) {
    u32x4 z = {~0u, ~0u, ~0u, ~0u};
    pin(z);
#pragma unroll
    for (int k = from; k < to; ++k) {
      if (k < 8) st16<0>(z, wsr, l16, soo + (k == 4 ? 9 : k == 5 ? 8 : k - 6) * 32768);
      else if (k < 10) st16<0>(z, wsr, l16, sxo + 4 * MB + 8 * 32768 + (k - 8) * 1024);
      else st4(z[0], wsr, fr8, sso + 136 * 2048);
    }
  };
  ;
  ;
  touch(wsr, lane < 2 ? WS_FL + (w * 512 + c * 64 + lane * 32 + h) * 128 : OOB);
  STAMP(29);

  unsigned ps[16];
  u32x8 pv = {};
#pragma unroll
  for (int j = 0; j < 3; ++j) {
    const unsigned ct = (w & 1) * 3 + j;
    pv[1 + j] = L_K + (ct >> 1) * 8192 + (4 * fq + (fr >> 2)) * 128 + (w >> 1) * 64 + (ct & 1) * 32 + (lane & 3) * 8;
    ps[j] = WS_WF + 6 * MB + ((2 * rot(g, ct >> 1) + (w >> 1)) * 192 + (kb >> 2) * 48 + (ct + 4) % 6 * 8 + (kb & 3) * 2) * 1024;
    ps[10 + j] = kb * 64 * 6144 + j * 2048 + rot(g, j) * 128;
  }
  { const unsigned ct = (w & 1) * 3 + fq;
    pv[4] = ((2 * rot(g, ct >> 1) + (w >> 1)) * 16384 + ct * 128 + fr * 8) | (fq < 3 ? 0u : OOB); }
  ps[3] = WS_CS + 524288 + kb * 1024;
  ps[6] = (c >> 2) | par << 1 | (bix * 4 + w) << 10;                                            ps[7] = L_WS + w * 6144;
  ps[8] = wbase;                                             ps[9] = rho;
  ps[13] = kb * 64 * 2048 + rot(g, 1) * 128;                 ps[14] = kb * 64 * 2048 + rot(g, 2) * 128;
  ps[15] = WS_WF + (h * 4 + w) * 49152 + (rho + (c >> 2)) * 1024;
  pv[5] = wsrc;                                              pv[6] = osrc;
  u32x16 pw = {wv, L_WS + w * 6144 + (4 * fq + (fr >> 2)) * 64 + (fq & 1) * 32 + (lane & 3) * 8,
                    l16 + rho * 1024, l16 + ((rho + 2) & 7) * 1024, l16 + ((rho + 4) & 7) * 1024, l16 + ((rho + 6) & 7) * 1024,
                    L_WS + w * 6144 + (fr == 0 ? fq * 16 : 1024 + l16), L_WS + (w * 24 + (lane < 24 ? lane : 0)) * 4,
                    (w * 128 + (lane >> 3 & 1) * 64 + 2 * ((g2 - rot(0, 1 + (fq & 1))) & 15) + (lane >> 2 & 1)) * 128 + (lane & 3) * 8};
  if (w < 2) {
    u32x4 pm;
#pragma unroll
    for (int k = 0; k < 4; ++k)
      pm[k] = (8 * fq + 2 * k > 16 * w + fr ? 0xFFFFu : 0u) | (8 * fq + 2 * k + 1 > 16 * w + fr ? 0xFFFF0000u : 0u);
    lds_w4(L_PM + w * 1024 + l16, pm);
  } else {
    const unsigned want = (unsigned)((0x5555ull << 16) >> (fr + 25 - 8 * fq));
    u32x4 mb;
#pragma unroll
    for (int k = 0; k < 4; ++k) mb[k] = (want >> (15 - 4 * w - k)) & 1 ? 0u : 0xFF800000u;
    lds_w4(L_MB + (w - 2) * 1024 + l16, mb);
  }
  pv[0] = L_V + 8192 + (16 * (fq & 1) + 4 * (fq >> 1) + (fr >> 2)) * 128 + w * 32 + (lane & 3) * 8;
  pv[7] = lane == 0 ? 32 * 128 : OOB;
  ps[4] = WS_WO + (rot(g, 1) * 128 + (kb >> 2) * 32 + w * 8 + (kb & 3) * 2) * 1024;
  ps[5] = WS_WO + 2 * MB + (rot(g, 2) * 128 + (kb >> 2) * 32 + w * 8 + (kb & 3) * 2) * 1024;
  const unsigned krow = (8 * (fr >> 2) + (fr & 3)) * 64 + wr * 2048 + L_K;
  const unsigned kv0 = 16384 + wr * 2048 + fr * 64 + ((fq ^ ((fr >> 3) << 1) ^ ((fr >> 1) & 3)) * 16);
  u32x16 va = {l16, l16 + 4096, lw, L_INB + w * 18432 + l16, L_INB + ((w + 3) & 3) * 18432 + l16, L_INB + ((w + 2) & 3) * 18432 + 6144 + l16,
                     L_INB + ((w + 1) & 3) * 18432 + 12288 + l16, kv0, kv0 ^ 48, L_CS + fq * 16, lane * 4, fl0,
                     ((lane & 31) >> 2) * 128 + (lane & 3) * 8,
                     fq < 3 ? (w * 128 + (lane >> 3 & 1) * 64 + 2 * ((2 * g - rot(g, fq % 3)) & 15) + (lane >> 2 & 1)) * 128 + (lane & 3) * 8 : ((lane & 7) * 64 + h) * 128 + w * 8,
                     fr8, krow + (fq ^ swzK(fr >> 2) ^ ((fr >> 1) & 1)) * 16};
  u32x8 vb = {L_V + wr * 2048 + (8 * fq + (fr >> 2)) * 64 + 8 * (lane & 1) + (((lane >> 1) & 1) ^ ((fq & 1) << 1) ^ (fr >> 3)) * 16,
                    krow + (fq ^ swzK(((fr >> 2) + 2) & 3) ^ ((fr >> 1) & 1)) * 16, 0, 0, 0,
                    lane < 24 ? L_CS + w * 96 + lane * 4 : L_SCR, w == 0 ? bid * 1024 + lane * 4 : OOB, 0};
  u32x16 pz = {pw[2], pw[3], pw[4], pw[5], pv[1], pv[2], pv[3], pv[4], pv[0], pw[8], pw[0], pw[1], pw[6], pw[7], pv[5], pv[6]};
  unsigned pf = pv[7];
  pin(va); pin(vb); pin(pz); pin(pf);
  auto convert = [&]() __attribute__((always_inline)) {
    f32x4 cs[3];
    u32x4 f[2][3];
    asm volatile("s_waitcnt lgkmcnt(0)\n\ts_barrier" ::: "memory");
#pragma unroll
    for (int kk = 0; kk < 2; ++kk)
#pragma unroll
      for (int j = 0; j < 3; ++j) {
        const unsigned a = pz[4 + j] + (L_INB - L_K) + kk * 4096;
        f[kk][j] = cat(lds_tr(a), lds_tr(a + 2048));
      }
    pin(ones);
#pragma unroll
    for (int kk = 0; kk < 2; ++kk)
#pragma unroll
      for (int j = 0; j < 3; ++j) {
        if (kk) mma_v(cs[j], ones, f[kk][j]); else mm0_v(cs[j], ones, f[kk][j]);
        st16<0>(f[kk][j], wsr, l16, uni(ps[j] - 6 * MB + kk * 1024));
      }
    nops<8>();
    pin(cs[0]); pin(cs[1]); pin(cs[2]);
    const unsigned v = __float_as_uint(fq == 0 ? cs[0][0] : fq == 1 ? cs[1][0] : cs[2][0]);
    st8<0>((u32x2){v, v}, wsr, pz[7], uni(ps[3] - 524288));
  };
  wait_vm<6>();
  STAMP(18);
  convert();
  wait_vm<8>();
  next_tag();
  publish(1, true);
  STAMP(1);
  wait_vm<3>();
  st8<96>(tag, wsr, fl0, myfl);
  get_o(0);
  get_o(1);
  get_q(1);
  publish(0, false);
  wait_vm<11>();
  st8<0>(tag, wsr, lane == 0 ? 32 * 128 : OOB, myfl + (2 * g2 + p2 - h) * 128);
  STAMP(15);

  u32x16 sa, sb;
#pragma unroll
  for (int i = 0; i < 4; ++i) {
    sa[i] = WS_XF + cb + w * 8192 + ((wr + 1 + i) & 3) * 65536;
    sa[4 + i] = sa[i] + WS_OF + p2 * 32768;
  }
  sa[8] = soo;                                              sa[9] = sxo;
  sa[10] = WS_ST + (c * 16 + 2 * wr) * 2048;                sa[11] = sso;
  sa[12] = skv - 16384;                                     sa[13] = c ? skv - 32 * 32768 : WS_XF + (h & 15) * 32768;
  sa[14] = c ? skv - 32 * 32768 + 8 * MB : sa[13];          sa[15] = WS_FL + (c * 64 + 8 * w) * 128;
  sb[0] = myfl;                                             sb[1] = WS_WF + (h * 4 + w) * 49152;
  sb[2] = WS_WO + (g2 * 4 + w) * 32768;                     sb[3] = c ? myfl - w * 8 + ((w - dl) & 3) * 8 - 32 * 128 + 64 : myfl + 4096;
  sb[4] = w * 96;                                           sb[5] = c ? myfl + 65536 - w * 8 + ((3 - dl) & 3) * 8 - 32 * 128 + 64 : myfl + 4096;
  sb[6] = ((c * 16 + 2 * wr + p2) * 16 * 1024 + g2 * 64) * 2;  sb[7] = WS_FL + 96;
  sb[8] = WS_CS + h * 16384;                                sb[9] = w * 2048;
  sb[10] = c ? -8 : 8 - wr;                                  sb[11] = c ? 16 * MB : 0;
  sb[12] = (2 * g2 + p2 - h) * 128;                                sb[13] = bid == 0 && w == 0 ? WS_EP : 0;
  sb[14] = c == 0 && wr == 0 ? ~0u : 0u;                     sb[15] = 0;
#pragma unroll
  for (int i = 0; i < 16; ++i) { sa[i] = uni(sa[i]); sb[i] = uni(sb[i]); }
  const rsrc_t wsk = __builtin_amdgcn_make_buffer_rsrc(ws, 0, wr == 3 ? 128 * MB : 0u, 0x00020000);
  const rsrc_t outr = __builtin_amdgcn_make_buffer_rsrc(out, 0, 4 * MB, 0x00020000);
  u32x8 pa, pc;
#pragma unroll
  for (int i = 0; i < 8; ++i) { pa[i] = uni(ps[i]); pc[i] = uni(ps[8 + i]); }
  STAMP(17);

  vb[2] = tag[0]; vb[3] = tag[1];
  STAMP(19);
  vb[7] = stw;
  asm volatile(
"; @entry\n"
"s_mov_b32 s0, 0\n"
"s_mov_b32 s3, 0\n"
"s_waitcnt lgkmcnt(0)\n"
"ds_read_b128 v[24:27], v2\n"
"ds_read_b128 v[28:31], v2 offset:1024\n"
"s_waitcnt lgkmcnt(0)\n"
"buffer_store_dwordx4 v[24:27], v0, s[4:7], s61 offen sc1\n"
"buffer_store_dwordx4 v[28:31], v0, s[4:7], s61 offen offset:1024 sc1\n"
"buffer_store_dword v48, v14, s[4:7], s63 offen sc1\n"
"s_waitcnt vmcnt(10)\n"
"s_waitcnt lgkmcnt(0)\n"
"s_barrier\n"
"ds_read_b64_tr_b16 v[24:25], v40\n"
"ds_read_b64_tr_b16 v[26:27], v40 offset:1024\n"
"ds_read_b64_tr_b16 v[28:29], v40 offset:4096\n"
"ds_read_b64_tr_b16 v[30:31], v40 offset:5120\n"
"ds_read_b64_tr_b16 v[32:33], v40 offset:8192\n"
"ds_read_b64_tr_b16 v[34:35], v40 offset:9216\n"
"ds_read_b64_tr_b16 v[44:45], v40 offset:12288\n"
"ds_read_b64_tr_b16 v[46:47], v40 offset:13312\n"
"s_waitcnt lgkmcnt(2)\n"
"buffer_store_dwordx4 v[24:27], v0, s[4:7], s24 offen sc1\n"
"s_waitcnt lgkmcnt(0)\n"
"buffer_store_dwordx4 v[28:31], v0, s[4:7], s24 offen offset:1024 sc1\n"
"buffer_store_dwordx4 v[32:35], v0, s[4:7], s25 offen sc1\n"
"buffer_store_dwordx4 v[44:47], v0, s[4:7], s25 offen offset:1024 sc1\n"
"s_add_u32 s98, s75, 0\n"
"buffer_load_dwordx2 v[254:255], v13, s[4:7], s98 offen sc1\n"
"s_getpc_b64 s[84:85]\n"
".Lmk_h2:\n"
"s_and_b32 s84, s84, -16\n"
"s_and_b32 s85, s85, 0xffff\n"
"s_mov_b32 s86, (.Lmk_end-.Lmk_h2)&0xFFFFFFF0\n"
"s_mov_b32 s87, 0x20000\n"
"s_and_b32 s97, s26, 0xfffffc00\n"
"s_mov_b32 m0, 0x27800\n"
"s_add_u32 s97, s97, 0x1e000\n"
"buffer_load_dwordx4 v0, s[84:87], s97 offen lds\n"
"s_waitcnt vmcnt(1)\n"
"v_cmp_ne_u64_e32 vcc, v[254:255], v[18:19]\n"
"s_cbranch_vccnz .Lmk_f2s\n"
".Lmk_f2r:\n"
"s_add_u32 s98, s69, 0\n"
"buffer_load_dwordx4 a[0:3], v0, s[4:7], s98 offen sc1\n"
"buffer_load_dwordx4 a[4:7], v0, s[4:7], s98 offen offset:1024 sc1\n"
"buffer_load_dwordx4 a[8:11], v0, s[4:7], s98 offen offset:2048 sc1\n"
"buffer_load_dwordx4 a[12:15], v0, s[4:7], s98 offen offset:3072 sc1\n"
"buffer_load_dwordx4 a[16:19], v1, s[4:7], s98 offen sc1\n"
"buffer_load_dwordx4 a[20:23], v1, s[4:7], s98 offen offset:1024 sc1\n"
"buffer_load_dwordx4 a[24:27], v1, s[4:7], s98 offen offset:2048 sc1\n"
"buffer_load_dwordx4 a[28:31], v1, s[4:7], s98 offen offset:3072 sc1\n"
"s_add_u32 s98, s67, 0x1000\n"
"buffer_load_dwordx2 v[254:255], v12, s[4:7], s98 offen sc1\n"
"s_add_u32 s97, s69, 0x2000\n"
"buffer_load_dwordx4 a[32:35], v0, s[4:7], s97 offen sc1\n"
"buffer_load_dwordx4 a[36:39], v0, s[4:7], s97 offen offset:1024 sc1\n"
"buffer_load_dwordx4 a[40:43], v0, s[4:7], s97 offen offset:2048 sc1\n"
"buffer_load_dwordx4 a[44:47], v0, s[4:7], s97 offen offset:3072 sc1\n"
"buffer_load_dwordx4 a[48:51], v1, s[4:7], s97 offen sc1\n"
"buffer_load_dwordx4 a[52:55], v1, s[4:7], s97 offen offset:1024 sc1\n"
"buffer_load_dwordx4 a[56:59], v1, s[4:7], s97 offen offset:2048 sc1\n"
"buffer_load_dwordx4 a[60:63], v1, s[4:7], s97 offen offset:3072 sc1\n"
"s_waitcnt vmcnt(8)\n"
"v_cmp_ne_u64_e32 vcc, v[254:255], v[18:19]\n"
"s_cbranch_vccnz .Lmk_f1s\n"
".Lmk_f1r:\n"
"s_add_u32 s98, s52, 0x40000\n"
"buffer_load_dwordx4 v[24:27], v0, s[4:7], s98 offen sc1\n"
"buffer_load_dwordx4 v[28:31], v0, s[4:7], s98 offen offset:1024 sc1\n"
"buffer_load_dwordx4 v[32:35], v0, s[4:7], s98 offen offset:2048 sc1\n"
"buffer_load_dwordx4 v[44:47], v0, s[4:7], s98 offen offset:3072 sc1\n"
"buffer_load_dwordx4 v[48:51], v1, s[4:7], s98 offen sc1\n"
"buffer_load_dwordx4 v[52:55], v1, s[4:7], s98 offen offset:1024 sc1\n"
"buffer_load_dwordx4 v[56:59], v1, s[4:7], s98 offen offset:2048 sc1\n"
"buffer_load_dwordx4 v[60:63], v1, s[4:7], s98 offen offset:3072 sc1\n"
"s_waitcnt lgkmcnt(0)\n"
"s_barrier\n"
"s_add_u32 s98, s76, 0\n"
"v_bfe_u32 v255, v0, 2, 7\n"
"v_add_lshl_u32 v255, s72, v255, 1\n"
"v_and_b32_e32 v254, 0x200, v0\n"
"v_lshl_add_u32 v255, v254, 4, v255\n"
"buffer_load_dword v254, v255, s[4:7], s98 offen sc1\n"
"buffer_load_dword v253, v255, s[4:7], s98 offen offset:1024 sc1\n"
"buffer_load_dword v252, v255, s[4:7], s98 offen offset:2048 sc1\n"
"buffer_load_dword v251, v255, s[4:7], s98 offen offset:3072 sc1\n"
"s_add_u32 s98, s76, 0x1000\n"
"buffer_load_dword v250, v255, s[4:7], s98 offen sc1\n"
"buffer_load_dword v249, v255, s[4:7], s98 offen offset:1024 sc1\n"
"buffer_load_dword v248, v255, s[4:7], s98 offen offset:2048 sc1\n"
"buffer_load_dword v247, v255, s[4:7], s98 offen offset:3072 sc1\n"
"s_add_u32 s98, s69, 0x4000\n"
"buffer_load_dwordx4 a[64:67], v0, s[4:7], s98 offen sc1\n"
"buffer_load_dwordx4 a[68:71], v0, s[4:7], s98 offen offset:1024 sc1\n"
"buffer_load_dwordx4 a[72:75], v0, s[4:7], s98 offen offset:2048 sc1\n"
"buffer_load_dwordx4 a[76:79], v0, s[4:7], s98 offen offset:3072 sc1\n"
"buffer_load_dwordx4 a[80:83], v1, s[4:7], s98 offen sc1\n"
"buffer_load_dwordx4 a[84:87], v1, s[4:7], s98 offen offset:1024 sc1\n"
"buffer_load_dwordx4 a[88:91], v1, s[4:7], s98 offen offset:2048 sc1\n"
"buffer_load_dwordx4 a[92:95], v1, s[4:7], s98 offen offset:3072 sc1\n"
"s_add_u32 s98, s53, 0x40000\n"
"buffer_load_dwordx4 v[64:67], v0, s[4:7], s98 offen sc1\n"
"buffer_load_dwordx4 v[68:71], v0, s[4:7], s98 offen offset:1024 sc1\n"
"buffer_load_dwordx4 v[72:75], v0, s[4:7], s98 offen offset:2048 sc1\n"
"buffer_load_dwordx4 v[76:79], v0, s[4:7], s98 offen offset:3072 sc1\n"
"buffer_load_dwordx4 v[80:83], v1, s[4:7], s98 offen sc1\n"
"buffer_load_dwordx4 v[84:87], v1, s[4:7], s98 offen offset:1024 sc1\n"
"buffer_load_dwordx4 v[88:91], v1, s[4:7], s98 offen offset:2048 sc1\n"
"buffer_load_dwordx4 v[92:95], v1, s[4:7], s98 offen offset:3072 sc1\n"
"s_add_u32 s98, s69, 0x6000\n"
"buffer_load_dwordx4 a[96:99], v0, s[4:7], s98 offen sc1\n"
"buffer_load_dwordx4 a[100:103], v0, s[4:7], s98 offen offset:1024 sc1\n"
"buffer_load_dwordx4 a[104:107], v0, s[4:7], s98 offen offset:2048 sc1\n"
"buffer_load_dwordx4 a[108:111], v0, s[4:7], s98 offen offset:3072 sc1\n"
"buffer_load_dwordx4 a[112:115], v1, s[4:7], s98 offen sc1\n"
"buffer_load_dwordx4 a[116:119], v1, s[4:7], s98 offen offset:1024 sc1\n"
"buffer_load_dwordx4 a[120:123], v1, s[4:7], s98 offen offset:2048 sc1\n"
"buffer_load_dwordx4 a[124:127], v1, s[4:7], s98 offen offset:3072 sc1\n"
"s_waitcnt vmcnt(24)\n"
"v_add_f32_e32 v254, v254, v253\n"
"v_add_f32_e32 v254, v254, v252\n"
"v_add_f32_e32 v254, v254, v251\n"
"v_add_f32_e32 v254, v254, v250\n"
"v_add_f32_e32 v254, v254, v249\n"
"v_add_f32_e32 v254, v254, v248\n"
"v_add_f32_e32 v254, v254, v247\n"
"v_mov_b32_e32 v253, v254\n"
"s_nop 1\n"
"v_permlane32_swap_b32_e32 v254, v253\n"
"v_add_f32_e32 v254, v254, v253\n"
"ds_write_b32 v21, v254 offset:0\n"
".Lmk_z0:\n"
"; @A G1 0.0\n"
"v_mfma_f32_16x16x32_bf16 v[96:99], a[0:3], v[24:27], 0\n"
"s_add_u32 s98, s69, 0x8000\n"
"buffer_load_dwordx4 v[100:103], v0, s[4:7], s98 offen sc1\n"
"v_mfma_f32_16x16x32_bf16 v[96:99], a[4:7], v[28:31], v[96:99]\n"
"s_add_u32 s97, s62, 0x4000\n"
"v_mfma_f32_16x16x32_bf16 v[96:99], a[8:11], v[32:35], v[96:99]\n"
"buffer_load_dwordx4 v[104:107], v0, s[4:7], s98 offen offset:1024 sc1\n"
"v_mfma_f32_16x16x32_bf16 v[96:99], a[12:15], v[44:47], v[96:99]\n"
"v_mfma_f32_16x16x32_bf16 v[96:99], a[16:19], v[48:51], v[96:99]\n"
"buffer_load_dwordx4 v[108:111], v0, s[4:7], s98 offen offset:2048 sc1\n"
"v_mfma_f32_16x16x32_bf16 v[96:99], a[20:23], v[52:55], v[96:99]\n"
"buffer_load_dword v255, v10, s[4:7], s97 offen sc1\n"
"ds_read_b64_tr_b16 v[112:113], v36\n"
"v_mfma_f32_16x16x32_bf16 v[96:99], a[24:27], v[56:59], v[96:99]\n"
"buffer_load_dwordx4 v[116:119], v0, s[4:7], s98 offen offset:3072 sc1\n"
"ds_read_b64_tr_b16 v[114:115], v36 offset:2048\n"
"v_mfma_f32_16x16x32_bf16 v[96:99], a[28:31], v[60:63], v[96:99]\n"
"s_mov_b32 s94, 0xf000f\n"
"s_mov_b32 s95, 0xf000f\n"
"v_mov_b32_e32 v123, 0x3f803f80\n"
"v_mfma_f32_16x16x32_bf16 v[124:127], a[60:63], v[60:63], 0\n"
"buffer_load_dwordx4 v[128:131], v1, s[4:7], s98 offen sc1\n"
"v_cndmask_b32_e64 v120, 0, v123, s[94:95]\n"
"v_mfma_f32_16x16x32_bf16 v[124:127], a[56:59], v[56:59], v[124:127]\n"
"buffer_load_dword v254, v10, s[4:7], s97 offen offset:256 sc1\n"
"v_cndmask_b32_e64 v121, 0, v123, s[94:95]\n"
"v_mfma_f32_16x16x32_bf16 v[124:127], a[52:55], v[52:55], v[124:127]\n"
"buffer_load_dwordx4 v[132:135], v1, s[4:7], s98 offen offset:1024 sc1\n"
"v_cndmask_b32_e64 v122, 0, v123, s[94:95]\n"
"v_mfma_f32_16x16x32_bf16 v[124:127], a[48:51], v[48:51], v[124:127]\n"
"s_add_u32 s96, s68, 0x10000\n"
"v_cndmask_b32_e64 v123, 0, v123, s[94:95]\n"
"v_mfma_f32_16x16x32_bf16 v[124:127], a[44:47], v[44:47], v[124:127]\n"
"buffer_load_dwordx4 v[136:139], v1, s[4:7], s98 offen offset:2048 sc1\n"
"s_waitcnt lgkmcnt(0)\n"
"v_mfma_f32_16x16x32_bf16 v[140:143], v[120:123], v[112:115], 0\n"
"buffer_store_dwordx4 v[112:115], v0, s[4:7], s20 offen sc1\n"
"v_mfma_f32_16x16x32_bf16 v[124:127], a[40:43], v[32:35], v[124:127]\n"
"ds_write_b128 v3, v[96:99]\n"
"v_mfma_f32_16x16x32_bf16 v[124:127], a[36:39], v[28:31], v[124:127]\n"
"buffer_load_dwordx4 v[96:99], v1, s[4:7], s98 offen offset:3072 sc1\n"
"v_mfma_f32_16x16x32_bf16 v[124:127], a[32:35], v[24:27], v[124:127]\n"
"s_waitcnt vmcnt(31)\n"
"s_add_u32 s98, s69, 0xa000\n"
"v_mfma_f32_16x16x32_bf16 v[112:115], a[64:67], v[24:27], 0\n"
"buffer_load_dwordx4 v[120:123], v0, s[4:7], s98 offen sc1\n"
"v_mfma_f32_16x16x32_bf16 v[112:115], a[68:71], v[28:31], v[112:115]\n"
"buffer_store_dwordx2 v[18:19], v11, s[4:7], s96 offen offset:96 sc1\n"
"v_mfma_f32_16x16x32_bf16 v[112:115], a[72:75], v[32:35], v[112:115]\n"
"buffer_load_dwordx4 v[144:147], v0, s[4:7], s98 offen offset:1024 sc1\n"
"v_mfma_f32_16x16x32_bf16 v[112:115], a[76:79], v[44:47], v[112:115]\n"
"s_waitcnt vmcnt(30)\n"
"buffer_load_dword v253, v10, s[4:7], s97 offen offset:512 sc1\n"
"v_mfma_f32_16x16x32_bf16 v[112:115], a[80:83], v[48:51], v[112:115]\n"
"buffer_load_dwordx4 v[148:151], v0, s[4:7], s98 offen offset:2048 sc1\n"
"v_mfma_f32_16x16x32_bf16 v[112:115], a[84:87], v[52:55], v[112:115]\n"
"ds_write_b128 v3, v[124:127] offset:1024\n"
"v_mfma_f32_16x16x32_bf16 v[112:115], a[88:91], v[56:59], v[112:115]\n"
"buffer_load_dwordx4 v[124:127], v0, s[4:7], s98 offen offset:3072 sc1\n"
"v_mfma_f32_16x16x32_bf16 v[112:115], a[92:95], v[60:63], v[112:115]\n"
"s_waitcnt vmcnt(17)\n"
"buffer_load_dword v252, v10, s[4:7], s97 offen offset:768 sc1\n"
"v_mfma_f32_16x16x32_bf16 v[152:155], a[124:127], v[60:63], 0\n"
"buffer_load_dwordx4 v[156:159], v1, s[4:7], s98 offen sc1\n"
"v_mfma_f32_16x16x32_bf16 v[152:155], a[120:123], v[56:59], v[152:155]\n"
"v_mfma_f32_16x16x32_bf16 v[152:155], a[116:119], v[52:55], v[152:155]\n"
"buffer_load_dwordx4 v[160:163], v1, s[4:7], s98 offen offset:1024 sc1\n"
"v_mfma_f32_16x16x32_bf16 v[152:155], a[112:115], v[48:51], v[152:155]\n"
"buffer_load_dword v251, v10, s[4:7], s97 offen offset:1024 sc1\n"
"v_mfma_f32_16x16x32_bf16 v[152:155], a[108:111], v[44:47], v[152:155]\n"
"buffer_load_dwordx4 v[164:167], v1, s[4:7], s98 offen offset:2048 sc1\n"
"v_mfma_f32_16x16x32_bf16 v[152:155], a[104:107], v[32:35], v[152:155]\n"
"ds_write_b128 v3, v[112:115] offset:2048\n"
"v_mfma_f32_16x16x32_bf16 v[152:155], a[100:103], v[28:31], v[152:155]\n"
"buffer_load_dwordx4 v[112:115], v1, s[4:7], s98 offen offset:3072 sc1\n"
"v_mfma_f32_16x16x32_bf16 v[152:155], a[96:99], v[24:27], v[152:155]\n"
"s_waitcnt vmcnt(18)\n"
"buffer_load_dword v250, v10, s[4:7], s97 offen offset:1280 sc1\n"
"v_mfma_f32_16x16x32_bf16 v[168:171], v[100:103], v[24:27], 0\n"
"s_add_u32 s98, s54, 0x40000\n"
"buffer_load_dwordx4 v[172:175], v0, s[4:7], s98 offen sc1\n"
"v_mfma_f32_16x16x32_bf16 v[168:171], v[104:107], v[28:31], v[168:171]\n"
"v_mfma_f32_16x16x32_bf16 v[168:171], v[108:111], v[32:35], v[168:171]\n"
"buffer_load_dword v249, v10, s[4:7], s97 offen offset:1536 sc1\n"
"v_mfma_f32_16x16x32_bf16 v[168:171], v[116:119], v[44:47], v[168:171]\n"
"s_waitcnt vmcnt(15)\n"
"v_mfma_f32_16x16x32_bf16 v[168:171], v[128:131], v[48:51], v[168:171]\n"
"buffer_load_dwordx4 v[176:179], v0, s[4:7], s98 offen offset:1024 sc1\n"
"v_mfma_f32_16x16x32_bf16 v[168:171], v[132:135], v[52:55], v[168:171]\n"
"ds_write_b128 v3, v[152:155] offset:3072\n"
"v_mfma_f32_16x16x32_bf16 v[168:171], v[136:139], v[56:59], v[168:171]\n"
"buffer_load_dword v248, v10, s[4:7], s97 offen offset:1792 sc1\n"
"v_mfma_f32_16x16x32_bf16 v[168:171], v[96:99], v[60:63], v[168:171]\n"
"s_waitcnt vmcnt(5)\n"
"v_mfma_f32_16x16x32_bf16 v[152:155], v[112:115], v[60:63], 0\n"
"buffer_load_dwordx4 v[60:63], v0, s[4:7], s98 offen offset:2048 sc1\n"
"ds_read_b64_tr_b16 v[180:181], v36 offset:4096\n"
"v_mfma_f32_16x16x32_bf16 v[152:155], v[164:167], v[56:59], v[152:155]\n"
"ds_read_b64_tr_b16 v[182:183], v36 offset:6144\n"
"s_mov_b32 s96, 0xf000f\n"
"s_mov_b32 s97, 0xf000f\n"
"v_mfma_f32_16x16x32_bf16 v[152:155], v[160:163], v[52:55], v[152:155]\n"
"v_mov_b32_e32 v55, 0x3f803f80\n"
"v_cndmask_b32_e64 v52, 0, v55, s[96:97]\n"
"v_cndmask_b32_e64 v53, 0, v55, s[96:97]\n"
"v_mfma_f32_16x16x32_bf16 v[152:155], v[156:159], v[48:51], v[152:155]\n"
"v_cndmask_b32_e64 v54, 0, v55, s[96:97]\n"
"v_cndmask_b32_e64 v55, 0, v55, s[96:97]\n"
"v_mfma_f32_16x16x32_bf16 v[152:155], v[124:127], v[44:47], v[152:155]\n"
"buffer_load_dwordx4 v[44:47], v0, s[4:7], s98 offen offset:3072 sc1\n"
"s_waitcnt lgkmcnt(0)\n"
"v_mfma_f32_16x16x32_bf16 v[140:143], v[52:55], v[180:183], v[140:143]\n"
"buffer_store_dwordx4 v[180:183], v0, s[4:7], s20 offen offset:1024 sc1\n"
"v_mfma_f32_16x16x32_bf16 v[152:155], v[148:151], v[32:35], v[152:155]\n"
"ds_write_b128 v3, v[168:171] offset:4096\n"
"v_mfma_f32_16x16x32_bf16 v[152:155], v[144:147], v[28:31], v[152:155]\n"
"v_mfma_f32_16x16x32_bf16 v[152:155], v[120:123], v[24:27], v[152:155]\n"
"; @A G1 0.1\n"
"v_mfma_f32_16x16x32_bf16 v[24:27], a[0:3], v[64:67], 0\n"
"buffer_load_dwordx4 v[28:31], v1, s[4:7], s98 offen sc1\n"
"v_mfma_f32_16x16x32_bf16 v[24:27], a[4:7], v[68:71], v[24:27]\n"
"v_mfma_f32_16x16x32_bf16 v[24:27], a[8:11], v[72:75], v[24:27]\n"
"s_add_u32 s97, s55, 0x40000\n"
"buffer_load_dwordx4 v[32:35], v0, s[4:7], s97 offen sc1\n"
"v_mfma_f32_16x16x32_bf16 v[24:27], a[12:15], v[76:79], v[24:27]\n"
"v_mfma_f32_16x16x32_bf16 v[24:27], a[16:19], v[80:83], v[24:27]\n"
"buffer_load_dwordx4 v[48:51], v1, s[4:7], s98 offen offset:1024 sc1\n"
"v_mfma_f32_16x16x32_bf16 v[24:27], a[20:23], v[84:87], v[24:27]\n"
"ds_write_b128 v3, v[152:155] offset:5120\n"
"v_mfma_f32_16x16x32_bf16 v[24:27], a[24:27], v[88:91], v[24:27]\n"
"v_mfma_f32_16x16x32_bf16 v[24:27], a[28:31], v[92:95], v[24:27]\n"
"buffer_load_dwordx4 v[52:55], v0, s[4:7], s97 offen offset:1024 sc1\n"
"v_mfma_f32_16x16x32_bf16 v[56:59], a[60:63], v[92:95], 0\n"
"buffer_load_dwordx4 v[152:155], v1, s[4:7], s98 offen offset:2048 sc1\n"
"v_mfma_f32_16x16x32_bf16 v[56:59], a[56:59], v[88:91], v[56:59]\n"
"v_mfma_f32_16x16x32_bf16 v[56:59], a[52:55], v[84:87], v[56:59]\n"
"v_mfma_f32_16x16x32_bf16 v[56:59], a[48:51], v[80:83], v[56:59]\n"
"v_mfma_f32_16x16x32_bf16 v[56:59], a[44:47], v[76:79], v[56:59]\n"
"buffer_load_dwordx4 v[168:171], v1, s[4:7], s98 offen offset:3072 sc1\n"
"v_mfma_f32_16x16x32_bf16 v[56:59], a[40:43], v[72:75], v[56:59]\n"
"buffer_load_dwordx4 v[180:183], v0, s[4:7], s97 offen offset:2048 sc1\n"
"v_mfma_f32_16x16x32_bf16 v[56:59], a[36:39], v[68:71], v[56:59]\n"
"ds_write_b128 v3, v[24:27] offset:6144\n"
"v_mfma_f32_16x16x32_bf16 v[56:59], a[32:35], v[64:67], v[56:59]\n"
"v_mfma_f32_16x16x32_bf16 v[24:27], a[64:67], v[64:67], 0\n"
"v_mfma_f32_16x16x32_bf16 v[24:27], a[68:71], v[68:71], v[24:27]\n"
"buffer_load_dwordx4 v[184:187], v0, s[4:7], s97 offen offset:3072 sc1\n"
"v_mfma_f32_16x16x32_bf16 v[24:27], a[72:75], v[72:75], v[24:27]\n"
"v_mfma_f32_16x16x32_bf16 v[24:27], a[76:79], v[76:79], v[24:27]\n"
"v_mfma_f32_16x16x32_bf16 v[24:27], a[80:83], v[80:83], v[24:27]\n"
"v_mfma_f32_16x16x32_bf16 v[24:27], a[84:87], v[84:87], v[24:27]\n"
"s_nop 0\n"
"ds_write_b128 v3, v[56:59] offset:7168\n"
"v_mfma_f32_16x16x32_bf16 v[24:27], a[88:91], v[88:91], v[24:27]\n"
"buffer_load_dwordx4 v[56:59], v1, s[4:7], s97 offen sc1\n"
"v_mfma_f32_16x16x32_bf16 v[24:27], a[92:95], v[92:95], v[24:27]\n"
"v_mfma_f32_16x16x32_bf16 v[188:191], a[124:127], v[92:95], 0\n"
"v_mfma_f32_16x16x32_bf16 v[188:191], a[120:123], v[88:91], v[188:191]\n"
"v_mfma_f32_16x16x32_bf16 v[188:191], a[116:119], v[84:87], v[188:191]\n"
"v_mfma_f32_16x16x32_bf16 v[188:191], a[112:115], v[80:83], v[188:191]\n"
"buffer_load_dwordx4 v[192:195], v1, s[4:7], s97 offen offset:1024 sc1\n"
"v_mfma_f32_16x16x32_bf16 v[188:191], a[108:111], v[76:79], v[188:191]\n"
"v_mfma_f32_16x16x32_bf16 v[188:191], a[104:107], v[72:75], v[188:191]\n"
"s_nop 0\n"
"ds_write_b128 v3, v[24:27] offset:8192\n"
"v_mfma_f32_16x16x32_bf16 v[188:191], a[100:103], v[68:71], v[188:191]\n"
"v_mfma_f32_16x16x32_bf16 v[188:191], a[96:99], v[64:67], v[188:191]\n"
"v_mfma_f32_16x16x32_bf16 v[24:27], v[100:103], v[64:67], 0\n"
"buffer_load_dwordx4 v[196:199], v1, s[4:7], s97 offen offset:2048 sc1\n"
"v_mfma_f32_16x16x32_bf16 v[24:27], v[104:107], v[68:71], v[24:27]\n"
"v_mfma_f32_16x16x32_bf16 v[24:27], v[108:111], v[72:75], v[24:27]\n"
"v_mfma_f32_16x16x32_bf16 v[24:27], v[116:119], v[76:79], v[24:27]\n"
"v_mfma_f32_16x16x32_bf16 v[24:27], v[128:131], v[80:83], v[24:27]\n"
"s_waitcnt vmcnt(14)\n"
"ds_read_b64_tr_b16 v[200:201], v37\n"
"v_add_f32_e32 v255, v255, v251\n"
"v_add_f32_e32 v254, v254, v250\n"
"v_mfma_f32_16x16x32_bf16 v[24:27], v[132:135], v[84:87], v[24:27]\n"
"buffer_load_dwordx4 v[204:207], v1, s[4:7], s97 offen offset:3072 sc1\n"
"ds_read_b64_tr_b16 v[202:203], v37 offset:2048\n"
"v_mfma_f32_16x16x32_bf16 v[24:27], v[136:139], v[88:91], v[24:27]\n"
"ds_write_b128 v3, v[188:191] offset:9216\n"
"v_mfma_f32_16x16x32_bf16 v[24:27], v[96:99], v[92:95], v[24:27]\n"
"s_mov_b32 s96, 0xf000f0\n"
"v_add_f32_e32 v253, v253, v249\n"
"v_add_f32_e32 v252, v252, v248\n"
"s_mov_b32 s97, 0xf000f0\n"
"v_mfma_f32_16x16x32_bf16 v[188:191], v[112:115], v[92:95], 0\n"
"v_add_f32_e32 v255, v255, v253\n"
"v_add_f32_e32 v254, v254, v252\n"
"v_mov_b32_e32 v95, 0x3f803f80\n"
"v_mfma_f32_16x16x32_bf16 v[188:191], v[164:167], v[88:91], v[188:191]\n"
"v_add_f32_e32 v255, v255, v254\n"
"v_mov_b32_e32 v254, v255\n"
"v_cndmask_b32_e64 v92, 0, v95, s[96:97]\n"
"v_mfma_f32_16x16x32_bf16 v[188:191], v[160:163], v[84:87], v[188:191]\n"
"v_permlane32_swap_b32_e32 v255, v254\n"
"v_add_f32_e32 v255, v255, v254\n"
"v_cndmask_b32_e64 v93, 0, v95, s[96:97]\n"
"v_mfma_f32_16x16x32_bf16 v[188:191], v[156:159], v[80:83], v[188:191]\n"
"v_mul_f32_e32 v255, 0x3a800000, v255\n"
"v_mov_b32_e32 v254, v255\n"
"v_cndmask_b32_e64 v94, 0, v95, s[96:97]\n"
"v_mfma_f32_16x16x32_bf16 v[188:191], v[124:127], v[76:79], v[188:191]\n"
"v_permlane16_swap_b32_e32 v255, v254\n"
"v_cvt_pk_bf16_f32 v253, -v255, 0\n"
"v_cndmask_b32_e64 v95, 0, v95, s[96:97]\n"
"v_mfma_f32_16x16x32_bf16 v[188:191], v[148:151], v[72:75], v[188:191]\n"
"ds_write_b128 v3, v[24:27] offset:10240\n"
"v_mfma_f32_16x16x32_bf16 v[188:191], v[144:147], v[68:71], v[188:191]\n"
"s_waitcnt lgkmcnt(2)\n"
"v_fma_f32 v254, -v255, v255, v254\n"
"v_lshlrev_b32_e32 v253, 16, v253\n"
"v_mfma_f32_16x16x32_bf16 v[140:143], v[92:95], v[200:203], v[140:143]\n"
"buffer_store_dwordx4 v[200:203], v0, s[4:7], s21 offen sc1\n"
"v_mfma_f32_16x16x32_bf16 v[188:191], v[120:123], v[64:67], v[188:191]\n"
"s_waitcnt vmcnt(14)\n"
"v_max_f32_e32 v254, 0, v254\n"
"v_cvt_pk_bf16_f32 v254, v254, 0\n"
"; @A G1 0.2\n"
"v_mfma_f32_16x16x32_bf16 v[24:27], a[0:3], v[172:175], 0\n"
"v_lshlrev_b32_e32 v254, 16, v254\n"
"v_add_f32_e32 v254, 0x3727c5ac, v254\n"
"v_mfma_f32_16x16x32_bf16 v[24:27], a[4:7], v[176:179], v[24:27]\n"
"v_cvt_pk_bf16_f32 v254, v254, 0\n"
"v_lshlrev_b32_e32 v254, 16, v254\n"
"v_mfma_f32_16x16x32_bf16 v[24:27], a[8:11], v[60:63], v[24:27]\n"
"v_sqrt_f32_e32 v254, v254\n"
"v_mfma_f32_16x16x32_bf16 v[24:27], a[12:15], v[44:47], v[24:27]\n"
"s_waitcnt vmcnt(7)\n"
"v_cvt_pk_bf16_f32 v254, v254, 0\n"
"v_lshlrev_b32_e32 v254, 16, v254\n"
"v_mfma_f32_16x16x32_bf16 v[24:27], a[16:19], v[28:31], v[24:27]\n"
"v_rcp_f32_e32 v255, v254\n"
"v_mfma_f32_16x16x32_bf16 v[24:27], a[20:23], v[48:51], v[24:27]\n"
"ds_write_b128 v3, v[188:191] offset:11264\n"
"v_mfma_f32_16x16x32_bf16 v[24:27], a[24:27], v[152:155], v[24:27]\n"
"v_mul_f32_e32 v254, 0x3e8293ee, v255\n"
"v_mfma_f32_16x16x32_bf16 v[24:27], a[28:31], v[168:171], v[24:27]\n"
"v_mfma_f32_16x16x32_bf16 v[64:67], a[60:63], v[168:171], 0\n"
"v_mfma_f32_16x16x32_bf16 v[64:67], a[56:59], v[152:155], v[64:67]\n"
"v_mfma_f32_16x16x32_bf16 v[64:67], a[52:55], v[48:51], v[64:67]\n"
"s_add_u32 s98, s52, 0x48000\n"
"buffer_load_dwordx4 v[68:71], v0, s[4:7], s98 offen sc1\n"
"v_mfma_f32_16x16x32_bf16 v[64:67], a[48:51], v[28:31], v[64:67]\n"
"s_waitcnt lgkmcnt(0)\n"
"s_barrier\n"
"v_mfma_f32_16x16x32_bf16 v[64:67], a[44:47], v[44:47], v[64:67]\n"
"ds_read_b128 v[72:75], v4\n"
"ds_read_b128 v[76:79], v9 offset:128\n"
"v_mfma_f32_16x16x32_bf16 v[64:67], a[40:43], v[60:63], v[64:67]\n"
"ds_write_b128 v3, v[24:27] offset:12288\n"
"v_mfma_f32_16x16x32_bf16 v[64:67], a[36:39], v[176:179], v[64:67]\n"
"ds_read_b128 v[24:27], v5\n"
"v_mfma_f32_16x16x32_bf16 v[64:67], a[32:35], v[172:175], v[64:67]\n"
"buffer_load_dwordx4 v[80:83], v0, s[4:7], s98 offen offset:1024 sc1\n"
"v_mfma_f32_16x16x32_bf16 v[84:87], a[64:67], v[172:175], 0\n"
"v_mfma_f32_16x16x32_bf16 v[84:87], a[68:71], v[176:179], v[84:87]\n"
"v_mfma_f32_16x16x32_bf16 v[84:87], a[72:75], v[60:63], v[84:87]\n"
"v_mfma_f32_16x16x32_bf16 v[84:87], a[76:79], v[44:47], v[84:87]\n"
"ds_read_b128 v[88:91], v4 offset:1024\n"
"ds_read_b128 v[92:95], v9 offset:192\n"
"v_mfma_f32_16x16x32_bf16 v[84:87], a[80:83], v[28:31], v[84:87]\n"
"buffer_load_dwordx4 v[188:191], v0, s[4:7], s98 offen offset:2048 sc1\n"
"v_mfma_f32_16x16x32_bf16 v[84:87], a[84:87], v[48:51], v[84:87]\n"
"ds_write_b128 v3, v[64:67] offset:13312\n"
"v_mfma_f32_16x16x32_bf16 v[84:87], a[88:91], v[152:155], v[84:87]\n"
"ds_read_b128 v[64:67], v5 offset:1024\n"
"s_waitcnt lgkmcnt(4)\n"
"v_mfma_f32_16x16x32_bf16 v[84:87], a[92:95], v[168:171], v[84:87]\n"
"v_fmac_f32_e32 v72, v253, v76\n"
"v_fmac_f32_e32 v73, v253, v77\n"
"v_mfma_f32_16x16x32_bf16 v[200:203], a[124:127], v[168:171], 0\n"
"v_fmac_f32_e32 v74, v253, v78\n"
"v_fmac_f32_e32 v75, v253, v79\n"
"v_mfma_f32_16x16x32_bf16 v[200:203], a[120:123], v[152:155], v[200:203]\n"
"buffer_load_dwordx4 v[76:79], v0, s[4:7], s98 offen offset:3072 sc1\n"
"v_mfma_f32_16x16x32_bf16 v[200:203], a[116:119], v[48:51], v[200:203]\n"
"ds_read_b128 v[208:211], v4 offset:2048\n"
"ds_read_b128 v[212:215], v9 offset:256\n"
"v_mfma_f32_16x16x32_bf16 v[200:203], a[112:115], v[28:31], v[200:203]\n"
"ds_read_b128 v[216:219], v5 offset:2048\n"
"v_add_f32_e32 v72, v72, v24\n"
"v_mfma_f32_16x16x32_bf16 v[200:203], a[108:111], v[44:47], v[200:203]\n"
"v_add_f32_e32 v73, v73, v25\n"
"v_add_f32_e32 v74, v74, v26\n"
"v_mfma_f32_16x16x32_bf16 v[200:203], a[104:107], v[60:63], v[200:203]\n"
"ds_write_b128 v3, v[84:87] offset:14336\n"
"v_mfma_f32_16x16x32_bf16 v[200:203], a[100:103], v[176:179], v[200:203]\n"
"buffer_load_dwordx4 v[84:87], v1, s[4:7], s98 offen sc1\n"
"v_mfma_f32_16x16x32_bf16 v[200:203], a[96:99], v[172:175], v[200:203]\n"
"s_waitcnt lgkmcnt(4)\n"
"v_add_f32_e32 v75, v75, v27\n"
"v_fmac_f32_e32 v88, v253, v92\n"
"v_mfma_f32_16x16x32_bf16 v[24:27], v[100:103], v[172:175], 0\n"
"ds_read_b64_tr_b16 v[220:221], v37 offset:4096\n"
"v_fmac_f32_e32 v89, v253, v93\n"
"v_fmac_f32_e32 v90, v253, v94\n"
"v_mfma_f32_16x16x32_bf16 v[24:27], v[104:107], v[176:179], v[24:27]\n"
"ds_read_b128 v[224:227], v4 offset:3072\n"
"ds_read_b128 v[228:231], v9 offset:320\n"
"ds_read_b64_tr_b16 v[222:223], v37 offset:6144\n"
"v_mfma_f32_16x16x32_bf16 v[24:27], v[108:111], v[60:63], v[24:27]\n"
"ds_read_b128 v[232:235], v5 offset:3072\n"
"v_fmac_f32_e32 v91, v253, v95\n"
"s_mov_b32 s96, 0xf000f0\n"
"v_mfma_f32_16x16x32_bf16 v[24:27], v[116:119], v[44:47], v[24:27]\n"
"buffer_load_dwordx4 v[92:95], v1, s[4:7], s98 offen offset:1024 sc1\n"
"s_mov_b32 s97, 0xf000f0\n"
"v_mfma_f32_16x16x32_bf16 v[24:27], v[128:131], v[28:31], v[24:27]\n"
"v_add_f32_e32 v88, v88, v64\n"
"v_add_f32_e32 v89, v89, v65\n"
"v_mov_b32_e32 v239, 0x3f803f80\n"
"v_mfma_f32_16x16x32_bf16 v[24:27], v[132:135], v[48:51], v[24:27]\n"
"ds_write_b128 v3, v[200:203] offset:15360\n"
"v_mfma_f32_16x16x32_bf16 v[24:27], v[136:139], v[152:155], v[24:27]\n"
"s_waitcnt lgkmcnt(7)\n"
"v_add_f32_e32 v90, v90, v66\n"
"v_add_f32_e32 v91, v91, v67\n"
"v_cndmask_b32_e64 v236, 0, v239, s[96:97]\n"
"v_mfma_f32_16x16x32_bf16 v[24:27], v[96:99], v[168:171], v[24:27]\n"
"v_fmac_f32_e32 v208, v253, v212\n"
"v_fmac_f32_e32 v209, v253, v213\n"
"v_cndmask_b32_e64 v237, 0, v239, s[96:97]\n"
"v_mfma_f32_16x16x32_bf16 v[64:67], v[112:115], v[168:171], 0\n"
"buffer_load_dwordx4 v[168:171], v1, s[4:7], s98 offen offset:2048 sc1\n"
"v_cndmask_b32_e64 v238, 0, v239, s[96:97]\n"
"v_mfma_f32_16x16x32_bf16 v[64:67], v[164:167], v[152:155], v[64:67]\n"
"s_add_u32 s95, s68, s80\n"
"ds_read_b128 v[152:155], v4 offset:4096\n"
"v_cndmask_b32_e64 v239, 0, v239, s[96:97]\n"
"v_mfma_f32_16x16x32_bf16 v[64:67], v[160:163], v[48:51], v[64:67]\n"
"buffer_store_dwordx2 v[18:19], v11, s[4:7], s95 offen sc1\n"
"v_mfma_f32_16x16x32_bf16 v[64:67], v[156:159], v[28:31], v[64:67]\n"
"ds_read_b128 v[28:31], v9\n"
"ds_read_b128 v[48:51], v5 offset:4096\n"
"s_waitcnt lgkmcnt(5)\n"
"v_mfma_f32_16x16x32_bf16 v[140:143], v[236:239], v[220:223], v[140:143]\n"
"buffer_store_dwordx4 v[220:223], v0, s[4:7], s21 offen offset:1024 sc1\n"
"v_mfma_f32_16x16x32_bf16 v[64:67], v[124:127], v[44:47], v[64:67]\n"
"s_add_u32 s97, s75, 0x10000\n"
"buffer_load_dwordx2 v[250:251], v41, s[4:7], s97 offen sc1\n"
"v_mfma_f32_16x16x32_bf16 v[64:67], v[148:151], v[60:63], v[64:67]\n"
"buffer_load_dwordx4 v[44:47], v1, s[4:7], s98 offen offset:3072 sc1\n"
"v_mfma_f32_16x16x32_bf16 v[64:67], v[144:147], v[176:179], v[64:67]\n"
"ds_write_b128 v3, v[24:27] offset:16384\n"
"v_mfma_f32_16x16x32_bf16 v[64:67], v[120:123], v[172:175], v[64:67]\n"
"s_waitcnt vmcnt(16)\n"
"ds_read_b128 v[24:27], v4 offset:5120\n"
"ds_read_b128 v[60:63], v9 offset:64\n"
"; @A G1 0.3\n"
"v_mfma_f32_16x16x32_bf16 v[72:75], a[0:3], v[32:35], v[72:75]\n"
"ds_read_b128 v[172:175], v5 offset:5120\n"
"v_fmac_f32_e32 v210, v253, v214\n"
"v_mfma_f32_16x16x32_bf16 v[72:75], a[4:7], v[52:55], v[72:75]\n"
"v_fmac_f32_e32 v211, v253, v215\n"
"v_add_f32_e32 v208, v208, v216\n"
"v_mfma_f32_16x16x32_bf16 v[72:75], a[8:11], v[180:183], v[72:75]\n"
"s_add_u32 s98, s53, 0x48000\n"
"buffer_load_dwordx4 v[176:179], v0, s[4:7], s98 offen sc1\n"
"v_mfma_f32_16x16x32_bf16 v[72:75], a[12:15], v[184:187], v[72:75]\n"
"s_waitcnt vmcnt(13)\n"
"v_add_f32_e32 v209, v209, v217\n"
"v_add_f32_e32 v210, v210, v218\n"
"v_mfma_f32_16x16x32_bf16 v[72:75], a[16:19], v[56:59], v[72:75]\n"
"s_waitcnt lgkmcnt(8)\n"
"v_add_f32_e32 v211, v211, v219\n"
"v_fmac_f32_e32 v224, v253, v228\n"
"v_mfma_f32_16x16x32_bf16 v[72:75], a[20:23], v[192:195], v[72:75]\n"
"ds_write_b128 v3, v[64:67] offset:17408\n"
"v_mfma_f32_16x16x32_bf16 v[72:75], a[24:27], v[196:199], v[72:75]\n"
"v_fmac_f32_e32 v225, v253, v229\n"
"v_fmac_f32_e32 v226, v253, v230\n"
"v_mfma_f32_16x16x32_bf16 v[72:75], a[28:31], v[204:207], v[72:75]\n"
"buffer_load_dwordx4 v[64:67], v0, s[4:7], s98 offen offset:1024 sc1\n"
"v_mfma_f32_16x16x32_bf16 v[88:91], a[60:63], v[204:207], v[88:91]\n"
"v_fmac_f32_e32 v227, v253, v231\n"
"v_add_f32_e32 v224, v224, v232\n"
"v_mfma_f32_16x16x32_bf16 v[88:91], a[56:59], v[196:199], v[88:91]\n"
"v_add_f32_e32 v225, v225, v233\n"
"v_add_f32_e32 v226, v226, v234\n"
"v_mfma_f32_16x16x32_bf16 v[88:91], a[52:55], v[192:195], v[88:91]\n"
"s_waitcnt lgkmcnt(0)\n"
"s_barrier\n"
"v_mfma_f32_16x16x32_bf16 v[88:91], a[48:51], v[56:59], v[88:91]\n"
"ds_read_b128 v[200:203], v6\n"
"ds_read_b128 v[212:215], v6 offset:1024\n"
"v_mfma_f32_16x16x32_bf16 v[88:91], a[44:47], v[184:187], v[88:91]\n"
"buffer_load_dwordx4 v[216:219], v0, s[4:7], s98 offen offset:2048 sc1\n"
"v_mfma_f32_16x16x32_bf16 v[88:91], a[40:43], v[180:183], v[88:91]\n"
"v_add_f32_e32 v227, v227, v235\n"
"v_fmac_f32_e32 v152, v253, v28\n"
"v_mfma_f32_16x16x32_bf16 v[88:91], a[36:39], v[52:55], v[88:91]\n"
"v_fmac_f32_e32 v153, v253, v29\n"
"v_fmac_f32_e32 v154, v253, v30\n"
"v_mfma_f32_16x16x32_bf16 v[88:91], a[32:35], v[32:35], v[88:91]\n"
"v_fmac_f32_e32 v155, v253, v31\n"
"v_add_f32_e32 v152, v152, v48\n"
"v_mfma_f32_16x16x32_bf16 v[208:211], a[64:67], v[32:35], v[208:211]\n"
"v_add_f32_e32 v153, v153, v49\n"
"v_add_f32_e32 v154, v154, v50\n"
"v_mfma_f32_16x16x32_bf16 v[208:211], a[68:71], v[52:55], v[208:211]\n"
"buffer_load_dwordx4 v[28:31], v0, s[4:7], s98 offen offset:3072 sc1\n"
"v_mfma_f32_16x16x32_bf16 v[208:211], a[72:75], v[180:183], v[208:211]\n"
"v_add_f32_e32 v155, v155, v51\n"
"v_fmac_f32_e32 v24, v253, v60\n"
"v_mfma_f32_16x16x32_bf16 v[208:211], a[76:79], v[184:187], v[208:211]\n"
"v_fmac_f32_e32 v25, v253, v61\n"
"v_fmac_f32_e32 v26, v253, v62\n"
"v_mfma_f32_16x16x32_bf16 v[208:211], a[80:83], v[56:59], v[208:211]\n"
"s_waitcnt lgkmcnt(0)\n"
"v_fmac_f32_e32 v27, v253, v63\n"
"v_add_f32_e32 v24, v24, v172\n"
"v_mfma_f32_16x16x32_bf16 v[208:211], a[84:87], v[192:195], v[208:211]\n"
"v_add_f32_e32 v25, v25, v173\n"
"v_add_f32_e32 v26, v26, v174\n"
"v_mfma_f32_16x16x32_bf16 v[208:211], a[88:91], v[196:199], v[208:211]\n"
"buffer_load_dwordx4 v[48:51], v1, s[4:7], s98 offen sc1\n"
"v_mfma_f32_16x16x32_bf16 v[208:211], a[92:95], v[204:207], v[208:211]\n"
"ds_read_b128 v[60:63], v6 offset:2048\n"
"ds_read_b128 v[220:223], v6 offset:3072\n"
"v_mfma_f32_16x16x32_bf16 v[224:227], a[124:127], v[204:207], v[224:227]\n"
"v_add_f32_e32 v27, v27, v175\n"
"v_add_f32_e32 v72, v72, v200\n"
"v_mfma_f32_16x16x32_bf16 v[224:227], a[120:123], v[196:199], v[224:227]\n"
"v_add_f32_e32 v73, v73, v201\n"
"v_add_f32_e32 v74, v74, v202\n"
"v_mfma_f32_16x16x32_bf16 v[224:227], a[116:119], v[192:195], v[224:227]\n"
"v_add_f32_e32 v75, v75, v203\n"
"v_add_f32_e32 v88, v88, v212\n"
"v_mfma_f32_16x16x32_bf16 v[224:227], a[112:115], v[56:59], v[224:227]\n"
"buffer_load_dwordx4 v[172:175], v1, s[4:7], s98 offen offset:1024 sc1\n"
"v_mfma_f32_16x16x32_bf16 v[224:227], a[108:111], v[184:187], v[224:227]\n"
"ds_read_b64_tr_b16 v[200:201], v38\n"
"v_add_f32_e32 v89, v89, v213\n"
"v_add_f32_e32 v90, v90, v214\n"
"v_mfma_f32_16x16x32_bf16 v[224:227], a[104:107], v[180:183], v[224:227]\n"
"ds_read_b64_tr_b16 v[202:203], v38 offset:2048\n"
"v_add_f32_e32 v91, v91, v215\n"
"v_mul_f32_e32 v72, v72, v255\n"
"v_mfma_f32_16x16x32_bf16 v[224:227], a[100:103], v[52:55], v[224:227]\n"
"s_mov_b32 s94, 0xf000f00\n"
"v_mul_f32_e32 v73, v73, v255\n"
"v_mul_f32_e32 v74, v74, v255\n"
"s_mov_b32 s95, 0xf000f00\n"
"v_mfma_f32_16x16x32_bf16 v[224:227], a[96:99], v[32:35], v[224:227]\n"
"v_mul_f32_e32 v75, v75, v255\n"
"v_mul_f32_e32 v88, v88, v255\n"
"v_mov_b32_e32 v215, 0x3f803f80\n"
"v_mfma_f32_16x16x32_bf16 v[152:155], v[100:103], v[32:35], v[152:155]\n"
"buffer_load_dwordx4 v[228:231], v1, s[4:7], s98 offen offset:2048 sc1\n"
"v_cndmask_b32_e64 v212, 0, v215, s[94:95]\n"
"v_mfma_f32_16x16x32_bf16 v[152:155], v[104:107], v[52:55], v[152:155]\n"
"v_mul_f32_e32 v89, v89, v255\n"
"v_mul_f32_e32 v90, v90, v255\n"
"v_cndmask_b32_e64 v213, 0, v215, s[94:95]\n"
"v_mfma_f32_16x16x32_bf16 v[152:155], v[108:111], v[180:183], v[152:155]\n"
"v_mul_f32_e32 v91, v91, v255\n"
"v_cvt_pk_bf16_f32 v232, v72, v73\n"
"v_cndmask_b32_e64 v214, 0, v215, s[94:95]\n"
"v_mfma_f32_16x16x32_bf16 v[152:155], v[116:119], v[184:187], v[152:155]\n"
"v_cvt_pk_bf16_f32 v233, v74, v75\n"
"v_cvt_pk_bf16_f32 v234, v88, v89\n"
"v_cndmask_b32_e64 v215, 0, v215, s[94:95]\n"
"v_mfma_f32_16x16x32_bf16 v[152:155], v[128:131], v[56:59], v[152:155]\n"
"s_waitcnt lgkmcnt(2)\n"
"v_cvt_pk_bf16_f32 v235, v90, v91\n"
"v_add_f32_e32 v208, v208, v60\n"
"s_waitcnt lgkmcnt(0)\n"
"v_mfma_f32_16x16x32_bf16 v[140:143], v[212:215], v[200:203], v[140:143]\n"
"buffer_store_dwordx4 v[200:203], v0, s[4:7], s22 offen sc1\n"
"v_mfma_f32_16x16x32_bf16 v[152:155], v[132:135], v[192:195], v[152:155]\n"
"buffer_load_dwordx4 v[72:75], v1, s[4:7], s98 offen offset:3072 sc1\n"
"ds_write_b128 v7, v[232:235] offset:24576\n"
"v_mfma_f32_16x16x32_bf16 v[152:155], v[136:139], v[196:199], v[152:155]\n"
"ds_read_b128 v[88:91], v6 offset:4096\n"
"ds_read_b128 v[200:203], v6 offset:5120\n"
"v_mfma_f32_16x16x32_bf16 v[152:155], v[96:99], v[204:207], v[152:155]\n"
"v_add_f32_e32 v209, v209, v61\n"
"v_add_f32_e32 v210, v210, v62\n"
"v_mfma_f32_16x16x32_bf16 v[24:27], v[112:115], v[204:207], v[24:27]\n"
"s_waitcnt vmcnt(10)\n"
"v_cmp_ne_u64_e32 vcc, v[250:251], v[18:19]\n"
"v_mfma_f32_16x16x32_bf16 v[24:27], v[164:167], v[196:199], v[24:27]\n"
"s_cbranch_vccnz .Lmk_f6s\n"
".Lmk_f6r:\n"
"v_add_f32_e32 v211, v211, v63\n"
"v_mfma_f32_16x16x32_bf16 v[24:27], v[160:163], v[192:195], v[24:27]\n"
"v_add_f32_e32 v224, v224, v220\n"
"v_add_f32_e32 v225, v225, v221\n"
"v_mfma_f32_16x16x32_bf16 v[24:27], v[156:159], v[56:59], v[24:27]\n"
"v_add_f32_e32 v226, v226, v222\n"
"v_add_f32_e32 v227, v227, v223\n"
"v_mfma_f32_16x16x32_bf16 v[24:27], v[124:127], v[184:187], v[24:27]\n"
"v_mul_f32_e32 v208, v208, v255\n"
"v_mul_f32_e32 v209, v209, v255\n"
"v_mfma_f32_16x16x32_bf16 v[24:27], v[148:151], v[180:183], v[24:27]\n"
"v_mul_f32_e32 v210, v210, v255\n"
"v_mul_f32_e32 v211, v211, v255\n"
"v_mfma_f32_16x16x32_bf16 v[24:27], v[144:147], v[52:55], v[24:27]\n"
"v_mul_f32_e32 v224, v224, v255\n"
"v_mul_f32_e32 v225, v225, v255\n"
"v_mfma_f32_16x16x32_bf16 v[24:27], v[120:123], v[32:35], v[24:27]\n"
"v_mul_f32_e32 v226, v226, v255\n"
"v_mul_f32_e32 v227, v227, v255\n"
"v_cvt_pk_bf16_f32 v32, v208, v209\n"
"v_cvt_pk_bf16_f32 v33, v210, v211\n"
"v_cvt_pk_bf16_f32 v34, v224, v225\n"
"v_cvt_pk_bf16_f32 v35, v226, v227\n"
"ds_write_b128 v7, v[32:35] offset:57344\n"
".Lmk_z1:\n"
"; @A G1 1.0\n"
"v_mfma_f32_16x16x32_bf16 v[40:43], a[0:3], v[68:71], 0\n"
"v_mfma_f32_16x16x32_bf16 v[40:43], a[4:7], v[80:83], v[40:43]\n"
"s_add_u32 s98, s62, 0x4800\n"
"v_mfma_f32_16x16x32_bf16 v[40:43], a[8:11], v[188:191], v[40:43]\n"
"s_add_u32 s97, s54, 0x48000\n"
"buffer_load_dwordx4 v[52:55], v0, s[4:7], s97 offen sc1\n"
"v_mfma_f32_16x16x32_bf16 v[40:43], a[12:15], v[76:79], v[40:43]\n"
"s_waitcnt vmcnt(10)\n"
"v_mfma_f32_16x16x32_bf16 v[40:43], a[16:19], v[84:87], v[40:43]\n"
"buffer_load_dword v255, v10, s[4:7], s98 offen sc1\n"
"v_mfma_f32_16x16x32_bf16 v[40:43], a[20:23], v[92:95], v[40:43]\n"
"s_waitcnt lgkmcnt(1)\n"
"v_add_f32_e32 v152, v152, v88\n"
"v_add_f32_e32 v153, v153, v89\n"
"v_mfma_f32_16x16x32_bf16 v[40:43], a[24:27], v[168:171], v[40:43]\n"
"v_add_f32_e32 v154, v154, v90\n"
"v_add_f32_e32 v155, v155, v91\n"
"v_mfma_f32_16x16x32_bf16 v[40:43], a[28:31], v[44:47], v[40:43]\n"
"buffer_load_dwordx4 v[56:59], v0, s[4:7], s97 offen offset:1024 sc1\n"
"v_mfma_f32_16x16x32_bf16 v[60:63], a[60:63], v[44:47], 0\n"
"s_add_u32 s96, s70, 0\n"
"buffer_load_dwordx4 a[128:131], v0, s[4:7], s96 offen sc1\n"
"v_mfma_f32_16x16x32_bf16 v[60:63], a[56:59], v[168:171], v[60:63]\n"
"buffer_load_dword v253, v10, s[4:7], s98 offen offset:256 sc1\n"
"v_mfma_f32_16x16x32_bf16 v[60:63], a[52:55], v[92:95], v[60:63]\n"
"v_add_f32_e32 v24, v24, v200\n"
"v_add_f32_e32 v25, v25, v201\n"
"v_mfma_f32_16x16x32_bf16 v[60:63], a[48:51], v[84:87], v[60:63]\n"
"v_add_f32_e32 v26, v26, v202\n"
"v_add_f32_e32 v27, v27, v203\n"
"v_mfma_f32_16x16x32_bf16 v[60:63], a[44:47], v[76:79], v[60:63]\n"
"buffer_load_dwordx4 v[88:91], v0, s[4:7], s97 offen offset:2048 sc1\n"
"v_mfma_f32_16x16x32_bf16 v[60:63], a[40:43], v[188:191], v[60:63]\n"
"ds_write_b128 v3, v[40:43]\n"
"v_mfma_f32_16x16x32_bf16 v[60:63], a[36:39], v[80:83], v[60:63]\n"
"buffer_load_dword v252, v10, s[4:7], s98 offen offset:512 sc1\n"
"v_mfma_f32_16x16x32_bf16 v[60:63], a[32:35], v[68:71], v[60:63]\n"
"v_mul_f32_e32 v152, v152, v254\n"
"v_mul_f32_e32 v153, v153, v254\n"
"v_mfma_f32_16x16x32_bf16 v[40:43], a[64:67], v[68:71], 0\n"
"v_mul_f32_e32 v154, v154, v254\n"
"v_mul_f32_e32 v155, v155, v254\n"
"v_mfma_f32_16x16x32_bf16 v[40:43], a[68:71], v[80:83], v[40:43]\n"
"buffer_load_dwordx4 v[180:183], v0, s[4:7], s97 offen offset:3072 sc1\n"
"v_mfma_f32_16x16x32_bf16 v[40:43], a[72:75], v[188:191], v[40:43]\n"
"buffer_load_dword v251, v10, s[4:7], s98 offen offset:768 sc1\n"
"v_mfma_f32_16x16x32_bf16 v[40:43], a[76:79], v[76:79], v[40:43]\n"
"v_mul_f32_e32 v24, v24, v254\n"
"v_mul_f32_e32 v25, v25, v254\n"
"v_mfma_f32_16x16x32_bf16 v[40:43], a[80:83], v[84:87], v[40:43]\n"
"v_mul_f32_e32 v26, v26, v254\n"
"v_mul_f32_e32 v27, v27, v254\n"
"v_mfma_f32_16x16x32_bf16 v[40:43], a[84:87], v[92:95], v[40:43]\n"
"ds_write_b128 v3, v[60:63] offset:1024\n"
"v_mfma_f32_16x16x32_bf16 v[40:43], a[88:91], v[168:171], v[40:43]\n"
"buffer_load_dwordx4 v[60:63], v1, s[4:7], s97 offen sc1\n"
"v_mfma_f32_16x16x32_bf16 v[40:43], a[92:95], v[44:47], v[40:43]\n"
"buffer_load_dword v254, v10, s[4:7], s98 offen offset:1024 sc1\n"
"v_mfma_f32_16x16x32_bf16 v[184:187], a[124:127], v[44:47], 0\n"
"v_cvt_pk_bf16_f32 v192, v152, v153\n"
"v_cvt_pk_bf16_f32 v193, v154, v155\n"
"v_mfma_f32_16x16x32_bf16 v[184:187], a[120:123], v[168:171], v[184:187]\n"
"v_cvt_pk_bf16_f32 v194, v24, v25\n"
"v_cvt_pk_bf16_f32 v195, v26, v27\n"
"v_mfma_f32_16x16x32_bf16 v[184:187], a[116:119], v[92:95], v[184:187]\n"
"buffer_load_dword v250, v10, s[4:7], s98 offen offset:1280 sc1\n"
"v_mfma_f32_16x16x32_bf16 v[184:187], a[112:115], v[84:87], v[184:187]\n"
"buffer_load_dwordx4 v[24:27], v1, s[4:7], s97 offen offset:1024 sc1\n"
"v_mfma_f32_16x16x32_bf16 v[184:187], a[108:111], v[76:79], v[184:187]\n"
"buffer_load_dwordx4 a[132:135], v0, s[4:7], s96 offen offset:1024 sc1\n"
"v_mfma_f32_16x16x32_bf16 v[184:187], a[104:107], v[188:191], v[184:187]\n"
"ds_write_b128 v3, v[40:43] offset:2048\n"
"v_mfma_f32_16x16x32_bf16 v[184:187], a[100:103], v[80:83], v[184:187]\n"
"buffer_load_dword v249, v10, s[4:7], s98 offen offset:1536 sc1\n"
"v_mfma_f32_16x16x32_bf16 v[184:187], a[96:99], v[68:71], v[184:187]\n"
"ds_write_b128 v2, v[192:195] offset:49152\n"
"v_mfma_f32_16x16x32_bf16 v[40:43], v[100:103], v[68:71], 0\n"
"buffer_load_dwordx4 v[152:155], v1, s[4:7], s97 offen offset:2048 sc1\n"
"v_mfma_f32_16x16x32_bf16 v[40:43], v[104:107], v[80:83], v[40:43]\n"
"buffer_load_dword v248, v10, s[4:7], s98 offen offset:1792 sc1\n"
"v_mfma_f32_16x16x32_bf16 v[40:43], v[108:111], v[188:191], v[40:43]\n"
"v_mfma_f32_16x16x32_bf16 v[40:43], v[116:119], v[76:79], v[40:43]\n"
"v_mfma_f32_16x16x32_bf16 v[40:43], v[128:131], v[84:87], v[40:43]\n"
"v_mfma_f32_16x16x32_bf16 v[40:43], v[132:135], v[92:95], v[40:43]\n"
"buffer_load_dwordx4 v[192:195], v1, s[4:7], s97 offen offset:3072 sc1\n"
"v_mfma_f32_16x16x32_bf16 v[40:43], v[136:139], v[168:171], v[40:43]\n"
"ds_write_b128 v3, v[184:187] offset:3072\n"
"v_mfma_f32_16x16x32_bf16 v[40:43], v[96:99], v[44:47], v[40:43]\n"
"v_mfma_f32_16x16x32_bf16 v[184:187], v[112:115], v[44:47], 0\n"
"v_mfma_f32_16x16x32_bf16 v[184:187], v[164:167], v[168:171], v[184:187]\n"
"v_mfma_f32_16x16x32_bf16 v[184:187], v[160:163], v[92:95], v[184:187]\n"
"v_mfma_f32_16x16x32_bf16 v[184:187], v[156:159], v[84:87], v[184:187]\n"
"v_mfma_f32_16x16x32_bf16 v[184:187], v[124:127], v[76:79], v[184:187]\n"
"v_mfma_f32_16x16x32_bf16 v[184:187], v[148:151], v[188:191], v[184:187]\n"
"s_nop 1\n"
"ds_write_b128 v3, v[40:43] offset:4096\n"
"v_mfma_f32_16x16x32_bf16 v[184:187], v[144:147], v[80:83], v[184:187]\n"
"v_mfma_f32_16x16x32_bf16 v[184:187], v[120:123], v[68:71], v[184:187]\n"
"s_waitcnt vmcnt(23)\n"
"; @A G1 1.1\n"
"v_mfma_f32_16x16x32_bf16 v[40:43], a[0:3], v[176:179], 0\n"
"buffer_load_dwordx4 a[136:139], v0, s[4:7], s96 offen offset:2048 sc1\n"
"v_mfma_f32_16x16x32_bf16 v[40:43], a[4:7], v[64:67], v[40:43]\n"
"v_mov_b32_e32 v44, -1\n"
"v_mov_b32_e32 v45, -1\n"
"v_mfma_f32_16x16x32_bf16 v[40:43], a[8:11], v[216:219], v[40:43]\n"
"s_add_u32 s98, s55, 0x48000\n"
"buffer_load_dwordx4 v[68:71], v0, s[4:7], s98 offen sc1\n"
"v_mfma_f32_16x16x32_bf16 v[40:43], a[12:15], v[28:31], v[40:43]\n"
"s_waitcnt vmcnt(20)\n"
"v_mov_b32_e32 v46, -1\n"
"v_mov_b32_e32 v47, -1\n"
"v_mfma_f32_16x16x32_bf16 v[40:43], a[16:19], v[48:51], v[40:43]\n"
"ds_read_b64_tr_b16 v[76:77], v38 offset:4096\n"
"ds_read_b64_tr_b16 v[78:79], v38 offset:6144\n"
"s_mov_b32 s94, 0xf000f00\n"
"v_mfma_f32_16x16x32_bf16 v[40:43], a[20:23], v[172:175], v[40:43]\n"
"ds_write_b128 v3, v[184:187] offset:5120\n"
"v_mfma_f32_16x16x32_bf16 v[40:43], a[24:27], v[228:231], v[40:43]\n"
"s_mov_b32 s95, 0xf000f00\n"
"v_mov_b32_e32 v83, 0x3f803f80\n"
"v_cndmask_b32_e64 v80, 0, v83, s[94:95]\n"
"v_cndmask_b32_e64 v81, 0, v83, s[94:95]\n"
"v_mfma_f32_16x16x32_bf16 v[40:43], a[28:31], v[72:75], v[40:43]\n"
"buffer_load_dwordx4 v[84:87], v0, s[4:7], s98 offen offset:1024 sc1\n"
"v_cndmask_b32_e64 v82, 0, v83, s[94:95]\n"
"v_mfma_f32_16x16x32_bf16 v[92:95], a[60:63], v[72:75], 0\n"
"v_cndmask_b32_e64 v83, 0, v83, s[94:95]\n"
"v_mfma_f32_16x16x32_bf16 v[92:95], a[56:59], v[228:231], v[92:95]\n"
"s_waitcnt lgkmcnt(1)\n"
"v_mfma_f32_16x16x32_bf16 v[140:143], v[80:83], v[76:79], v[140:143]\n"
"buffer_store_dwordx4 v[76:79], v0, s[4:7], s22 offen offset:1024 sc1\n"
"v_mfma_f32_16x16x32_bf16 v[92:95], a[52:55], v[172:175], v[92:95]\n"
"v_mfma_f32_16x16x32_bf16 v[92:95], a[48:51], v[48:51], v[92:95]\n"
"v_mfma_f32_16x16x32_bf16 v[92:95], a[44:47], v[28:31], v[92:95]\n"
"buffer_load_dwordx4 v[76:79], v0, s[4:7], s98 offen offset:2048 sc1\n"
"v_mfma_f32_16x16x32_bf16 v[92:95], a[40:43], v[216:219], v[92:95]\n"
"ds_write_b128 v3, v[40:43] offset:6144\n"
"v_mfma_f32_16x16x32_bf16 v[92:95], a[36:39], v[64:67], v[92:95]\n"
"s_add_u32 s97, s67, 0\n"
"buffer_load_dwordx2 v[246:247], v12, s[4:7], s97 offen sc1\n"
"s_add_u32 s95, s64, 0x2000\n"
"v_mfma_f32_16x16x32_bf16 v[92:95], a[32:35], v[176:179], v[92:95]\n"
"buffer_store_dwordx4 v[232:235], v7, s[4:7], s95 offen sc1\n"
"v_mfma_f32_16x16x32_bf16 v[40:43], a[64:67], v[176:179], 0\n"
"v_mfma_f32_16x16x32_bf16 v[40:43], a[68:71], v[64:67], v[40:43]\n"
"buffer_load_dwordx4 v[80:83], v0, s[4:7], s98 offen offset:3072 sc1\n"
"v_mfma_f32_16x16x32_bf16 v[40:43], a[72:75], v[216:219], v[40:43]\n"
"v_mfma_f32_16x16x32_bf16 v[40:43], a[76:79], v[28:31], v[40:43]\n"
"v_mfma_f32_16x16x32_bf16 v[40:43], a[80:83], v[48:51], v[40:43]\n"
"buffer_load_dwordx4 a[140:143], v0, s[4:7], s96 offen offset:3072 sc1\n"
"v_mfma_f32_16x16x32_bf16 v[40:43], a[84:87], v[172:175], v[40:43]\n"
"ds_write_b128 v3, v[92:95] offset:7168\n"
"v_mfma_f32_16x16x32_bf16 v[40:43], a[88:91], v[228:231], v[40:43]\n"
"buffer_load_dwordx4 v[92:95], v1, s[4:7], s98 offen sc1\n"
"v_mfma_f32_16x16x32_bf16 v[40:43], a[92:95], v[72:75], v[40:43]\n"
"v_mfma_f32_16x16x32_bf16 v[168:171], a[124:127], v[72:75], 0\n"
"v_mfma_f32_16x16x32_bf16 v[168:171], a[120:123], v[228:231], v[168:171]\n"
"v_mfma_f32_16x16x32_bf16 v[168:171], a[116:119], v[172:175], v[168:171]\n"
"v_mfma_f32_16x16x32_bf16 v[168:171], a[112:115], v[48:51], v[168:171]\n"
"buffer_load_dwordx4 v[184:187], v1, s[4:7], s98 offen offset:1024 sc1\n"
"v_mfma_f32_16x16x32_bf16 v[168:171], a[108:111], v[28:31], v[168:171]\n"
"v_mfma_f32_16x16x32_bf16 v[168:171], a[104:107], v[216:219], v[168:171]\n"
"s_nop 0\n"
"ds_write_b128 v3, v[40:43] offset:8192\n"
"v_mfma_f32_16x16x32_bf16 v[168:171], a[100:103], v[64:67], v[168:171]\n"
"v_mfma_f32_16x16x32_bf16 v[168:171], a[96:99], v[176:179], v[168:171]\n"
"v_mfma_f32_16x16x32_bf16 v[40:43], v[100:103], v[176:179], 0\n"
"buffer_load_dwordx4 v[188:191], v1, s[4:7], s98 offen offset:2048 sc1\n"
"v_mfma_f32_16x16x32_bf16 v[40:43], v[104:107], v[64:67], v[40:43]\n"
"buffer_store_dwordx2 v[140:141], v39, s[4:7], s23 offen sc1\n"
"v_mfma_f32_16x16x32_bf16 v[40:43], v[108:111], v[216:219], v[40:43]\n"
"v_mfma_f32_16x16x32_bf16 v[40:43], v[116:119], v[28:31], v[40:43]\n"
"v_mfma_f32_16x16x32_bf16 v[40:43], v[128:131], v[48:51], v[40:43]\n"
"s_waitcnt vmcnt(14)\n"
"v_add_f32_e32 v255, v255, v254\n"
"v_add_f32_e32 v253, v253, v250\n"
"v_mfma_f32_16x16x32_bf16 v[40:43], v[132:135], v[172:175], v[40:43]\n"
"buffer_load_dwordx4 v[36:39], v1, s[4:7], s98 offen offset:3072 sc1\n"
"s_add_u32 s98, s64, 0x6000\n"
"v_mfma_f32_16x16x32_bf16 v[40:43], v[136:139], v[228:231], v[40:43]\n"
"ds_write_b128 v3, v[168:171] offset:9216\n"
"v_mfma_f32_16x16x32_bf16 v[40:43], v[96:99], v[72:75], v[40:43]\n"
"buffer_store_dwordx4 v[32:35], v7, s[4:7], s98 offen sc1\n"
"s_nop 1\n"
"v_mfma_f32_16x16x32_bf16 v[32:35], v[112:115], v[72:75], 0\n"
"buffer_load_dwordx4 a[144:147], v1, s[4:7], s96 offen sc1\n"
"v_mfma_f32_16x16x32_bf16 v[32:35], v[164:167], v[228:231], v[32:35]\n"
"v_add_f32_e32 v252, v252, v249\n"
"v_add_f32_e32 v251, v251, v248\n"
"v_mfma_f32_16x16x32_bf16 v[32:35], v[160:163], v[172:175], v[32:35]\n"
"v_add_f32_e32 v255, v255, v252\n"
"v_add_f32_e32 v253, v253, v251\n"
"v_mfma_f32_16x16x32_bf16 v[32:35], v[156:159], v[48:51], v[32:35]\n"
"v_add_f32_e32 v255, v255, v253\n"
"v_mov_b32_e32 v253, v255\n"
"v_mfma_f32_16x16x32_bf16 v[32:35], v[124:127], v[28:31], v[32:35]\n"
"s_nop 0\n"
"v_permlane32_swap_b32_e32 v255, v253\n"
"v_add_f32_e32 v255, v255, v253\n"
"v_mfma_f32_16x16x32_bf16 v[32:35], v[148:151], v[216:219], v[32:35]\n"
"ds_write_b128 v3, v[40:43] offset:10240\n"
"v_mfma_f32_16x16x32_bf16 v[32:35], v[144:147], v[64:67], v[32:35]\n"
"v_mul_f32_e32 v255, 0x3a800000, v255\n"
"v_mov_b32_e32 v253, v255\n"
"v_mfma_f32_16x16x32_bf16 v[32:35], v[120:123], v[176:179], v[32:35]\n"
"s_nop 0\n"
"v_permlane16_swap_b32_e32 v255, v253\n"
"v_cvt_pk_bf16_f32 v254, -v255, 0\n"
"; @A G1 1.2\n"
"v_mfma_f32_16x16x32_bf16 v[28:31], a[0:3], v[52:55], 0\n"
"v_fma_f32 v253, -v255, v255, v253\n"
"v_lshlrev_b32_e32 v254, 16, v254\n"
"v_mfma_f32_16x16x32_bf16 v[28:31], a[4:7], v[56:59], v[28:31]\n"
"s_waitcnt vmcnt(10)\n"
"v_cmp_ne_u64_e32 vcc, v[246:247], v[18:19]\n"
"v_mfma_f32_16x16x32_bf16 v[28:31], a[8:11], v[88:91], v[28:31]\n"
"s_cbranch_vccnz .Lmk_f3s\n"
".Lmk_f3r:\n"
"v_max_f32_e32 v253, 0, v253\n"
"v_mfma_f32_16x16x32_bf16 v[28:31], a[12:15], v[180:183], v[28:31]\n"
"v_cvt_pk_bf16_f32 v253, v253, 0\n"
"v_lshlrev_b32_e32 v253, 16, v253\n"
"v_mfma_f32_16x16x32_bf16 v[28:31], a[16:19], v[60:63], v[28:31]\n"
"v_add_f32_e32 v253, 0x3727c5ac, v253\n"
"v_cvt_pk_bf16_f32 v253, v253, 0\n"
"v_mfma_f32_16x16x32_bf16 v[28:31], a[20:23], v[24:27], v[28:31]\n"
"ds_write_b128 v3, v[32:35] offset:11264\n"
"v_mfma_f32_16x16x32_bf16 v[28:31], a[24:27], v[152:155], v[28:31]\n"
"v_lshlrev_b32_e32 v253, 16, v253\n"
"v_mfma_f32_16x16x32_bf16 v[28:31], a[28:31], v[192:195], v[28:31]\n"
"v_sqrt_f32_e32 v253, v253\n"
"v_mfma_f32_16x16x32_bf16 v[32:35], a[60:63], v[192:195], 0\n"
"v_cvt_pk_bf16_f32 v253, v253, 0\n"
"v_lshlrev_b32_e32 v253, 16, v253\n"
"v_mfma_f32_16x16x32_bf16 v[32:35], a[56:59], v[152:155], v[32:35]\n"
"v_rcp_f32_e32 v255, v253\n"
"v_mfma_f32_16x16x32_bf16 v[32:35], a[52:55], v[24:27], v[32:35]\n"
"s_add_u32 s98, s52, 0x8000\n"
"buffer_load_dwordx4 v[40:43], v0, s[4:7], s98 offen sc1\n"
"v_mfma_f32_16x16x32_bf16 v[32:35], a[48:51], v[60:63], v[32:35]\n"
"s_waitcnt lgkmcnt(0)\n"
"s_barrier\n"
"v_mfma_f32_16x16x32_bf16 v[32:35], a[44:47], v[180:183], v[32:35]\n"
"buffer_load_dwordx4 a[148:151], v1, s[4:7], s96 offen offset:1024 sc1\n"
"v_mfma_f32_16x16x32_bf16 v[32:35], a[40:43], v[88:91], v[32:35]\n"
"ds_write_b128 v3, v[28:31] offset:12288\n"
"v_mfma_f32_16x16x32_bf16 v[32:35], a[36:39], v[56:59], v[32:35]\n"
"ds_read_b128 v[28:31], v4\n"
"v_mul_f32_e32 v253, 0x3e8293ee, v255\n"
"v_mfma_f32_16x16x32_bf16 v[32:35], a[32:35], v[52:55], v[32:35]\n"
"buffer_load_dwordx4 v[48:51], v0, s[4:7], s98 offen offset:1024 sc1\n"
"v_mfma_f32_16x16x32_bf16 v[64:67], a[64:67], v[52:55], 0\n"
"ds_read_b128 v[72:75], v9 offset:128\n"
"ds_read_b128 v[140:143], v5\n"
"v_mfma_f32_16x16x32_bf16 v[64:67], a[68:71], v[56:59], v[64:67]\n"
"v_mfma_f32_16x16x32_bf16 v[64:67], a[72:75], v[88:91], v[64:67]\n"
"s_add_u32 s97, s60, 0x48000\n"
"buffer_store_dwordx4 v[44:47], v0, s[4:7], s97 offen sc1\n"
"v_mfma_f32_16x16x32_bf16 v[64:67], a[76:79], v[180:183], v[64:67]\n"
"ds_read_b128 v[168:171], v4 offset:1024\n"
"ds_read_b128 v[172:175], v9 offset:192\n"
"v_mfma_f32_16x16x32_bf16 v[64:67], a[80:83], v[60:63], v[64:67]\n"
"buffer_load_dwordx4 v[176:179], v0, s[4:7], s98 offen offset:2048 sc1\n"
"v_mfma_f32_16x16x32_bf16 v[64:67], a[84:87], v[24:27], v[64:67]\n"
"ds_write_b128 v3, v[32:35] offset:13312\n"
"v_mfma_f32_16x16x32_bf16 v[64:67], a[88:91], v[152:155], v[64:67]\n"
"ds_read_b128 v[32:35], v5 offset:1024\n"
"s_waitcnt lgkmcnt(4)\n"
"v_mfma_f32_16x16x32_bf16 v[64:67], a[92:95], v[192:195], v[64:67]\n"
"v_fmac_f32_e32 v28, v254, v72\n"
"v_fmac_f32_e32 v29, v254, v73\n"
"v_mfma_f32_16x16x32_bf16 v[196:199], a[124:127], v[192:195], 0\n"
"v_fmac_f32_e32 v30, v254, v74\n"
"v_fmac_f32_e32 v31, v254, v75\n"
"v_mfma_f32_16x16x32_bf16 v[196:199], a[120:123], v[152:155], v[196:199]\n"
"buffer_load_dwordx4 v[72:75], v0, s[4:7], s98 offen offset:3072 sc1\n"
"v_mfma_f32_16x16x32_bf16 v[196:199], a[116:119], v[24:27], v[196:199]\n"
"ds_read_b128 v[200:203], v4 offset:2048\n"
"ds_read_b128 v[204:207], v9 offset:256\n"
"v_mfma_f32_16x16x32_bf16 v[196:199], a[112:115], v[60:63], v[196:199]\n"
"ds_read_b128 v[208:211], v5 offset:2048\n"
"v_add_f32_e32 v28, v28, v140\n"
"v_mfma_f32_16x16x32_bf16 v[196:199], a[108:111], v[180:183], v[196:199]\n"
"v_add_f32_e32 v29, v29, v141\n"
"v_add_f32_e32 v30, v30, v142\n"
"v_mfma_f32_16x16x32_bf16 v[196:199], a[104:107], v[88:91], v[196:199]\n"
"ds_write_b128 v3, v[64:67] offset:14336\n"
"v_mfma_f32_16x16x32_bf16 v[196:199], a[100:103], v[56:59], v[196:199]\n"
"buffer_load_dwordx4 v[64:67], v1, s[4:7], s98 offen sc1\n"
"v_mfma_f32_16x16x32_bf16 v[196:199], a[96:99], v[52:55], v[196:199]\n"
"s_waitcnt lgkmcnt(4)\n"
"v_add_f32_e32 v31, v31, v143\n"
"v_fmac_f32_e32 v168, v254, v172\n"
"v_mfma_f32_16x16x32_bf16 v[140:143], v[100:103], v[52:55], 0\n"
"buffer_load_dwordx4 a[152:155], v1, s[4:7], s96 offen offset:2048 sc1\n"
"v_mfma_f32_16x16x32_bf16 v[140:143], v[104:107], v[56:59], v[140:143]\n"
"ds_read_b128 v[212:215], v4 offset:3072\n"
"ds_read_b128 v[216:219], v9 offset:320\n"
"v_mfma_f32_16x16x32_bf16 v[140:143], v[108:111], v[88:91], v[140:143]\n"
"ds_read_b128 v[220:223], v5 offset:3072\n"
"v_fmac_f32_e32 v169, v254, v173\n"
"v_mfma_f32_16x16x32_bf16 v[140:143], v[116:119], v[180:183], v[140:143]\n"
"buffer_load_dwordx4 v[224:227], v1, s[4:7], s98 offen offset:1024 sc1\n"
"v_mfma_f32_16x16x32_bf16 v[140:143], v[128:131], v[60:63], v[140:143]\n"
"v_fmac_f32_e32 v170, v254, v174\n"
"v_fmac_f32_e32 v171, v254, v175\n"
"v_mfma_f32_16x16x32_bf16 v[140:143], v[132:135], v[24:27], v[140:143]\n"
"ds_write_b128 v3, v[196:199] offset:15360\n"
"v_mfma_f32_16x16x32_bf16 v[140:143], v[136:139], v[152:155], v[140:143]\n"
"v_add_f32_e32 v168, v168, v32\n"
"v_add_f32_e32 v169, v169, v33\n"
"v_mfma_f32_16x16x32_bf16 v[140:143], v[96:99], v[192:195], v[140:143]\n"
"s_waitcnt lgkmcnt(5)\n"
"v_add_f32_e32 v170, v170, v34\n"
"v_add_f32_e32 v171, v171, v35\n"
"v_mfma_f32_16x16x32_bf16 v[32:35], v[112:115], v[192:195], 0\n"
"buffer_load_dwordx4 v[172:175], v1, s[4:7], s98 offen offset:2048 sc1\n"
"v_mfma_f32_16x16x32_bf16 v[32:35], v[164:167], v[152:155], v[32:35]\n"
"ds_read_b128 v[152:155], v4 offset:4096\n"
"ds_read_b128 v[192:195], v9\n"
"v_mfma_f32_16x16x32_bf16 v[32:35], v[160:163], v[24:27], v[32:35]\n"
"ds_read_b128 v[24:27], v5 offset:4096\n"
"v_fmac_f32_e32 v200, v254, v204\n"
"v_mfma_f32_16x16x32_bf16 v[32:35], v[156:159], v[60:63], v[32:35]\n"
"v_fmac_f32_e32 v201, v254, v205\n"
"v_fmac_f32_e32 v202, v254, v206\n"
"v_mfma_f32_16x16x32_bf16 v[32:35], v[124:127], v[180:183], v[32:35]\n"
"v_fmac_f32_e32 v203, v254, v207\n"
"v_add_f32_e32 v200, v200, v208\n"
"v_mfma_f32_16x16x32_bf16 v[32:35], v[148:151], v[88:91], v[32:35]\n"
"buffer_load_dwordx4 v[60:63], v1, s[4:7], s98 offen offset:3072 sc1\n"
"v_mfma_f32_16x16x32_bf16 v[32:35], v[144:147], v[56:59], v[32:35]\n"
"ds_write_b128 v3, v[140:143] offset:16384\n"
"v_mfma_f32_16x16x32_bf16 v[32:35], v[120:123], v[52:55], v[32:35]\n"
"s_waitcnt vmcnt(19)\n"
"ds_read_b128 v[52:55], v4 offset:5120\n"
"ds_read_b128 v[56:59], v9 offset:64\n"
"; @A G1 1.3\n"
"v_mfma_f32_16x16x32_bf16 v[28:31], a[0:3], v[68:71], v[28:31]\n"
"ds_read_b128 v[88:91], v5 offset:5120\n"
"v_add_f32_e32 v201, v201, v209\n"
"v_mfma_f32_16x16x32_bf16 v[28:31], a[4:7], v[84:87], v[28:31]\n"
"s_waitcnt lgkmcnt(8)\n"
"v_add_f32_e32 v202, v202, v210\n"
"v_add_f32_e32 v203, v203, v211\n"
"v_mfma_f32_16x16x32_bf16 v[28:31], a[8:11], v[76:79], v[28:31]\n"
"s_add_u32 s98, s53, 0x8000\n"
"buffer_load_dwordx4 v[140:143], v0, s[4:7], s98 offen sc1\n"
"v_mfma_f32_16x16x32_bf16 v[28:31], a[12:15], v[80:83], v[28:31]\n"
"s_waitcnt vmcnt(14)\n"
"s_add_u32 s97, s60, 0x40000\n"
"buffer_store_dwordx4 v[44:47], v0, s[4:7], s97 offen sc1\n"
"v_fmac_f32_e32 v212, v254, v216\n"
"v_fmac_f32_e32 v213, v254, v217\n"
"v_mfma_f32_16x16x32_bf16 v[28:31], a[16:19], v[92:95], v[28:31]\n"
"buffer_load_dwordx4 a[156:159], v1, s[4:7], s96 offen offset:3072 sc1\n"
"v_mfma_f32_16x16x32_bf16 v[28:31], a[20:23], v[184:187], v[28:31]\n"
"ds_write_b128 v3, v[32:35] offset:17408\n"
"v_mfma_f32_16x16x32_bf16 v[28:31], a[24:27], v[188:191], v[28:31]\n"
"v_fmac_f32_e32 v214, v254, v218\n"
"v_fmac_f32_e32 v215, v254, v219\n"
"v_mfma_f32_16x16x32_bf16 v[28:31], a[28:31], v[36:39], v[28:31]\n"
"buffer_load_dwordx4 v[32:35], v0, s[4:7], s98 offen offset:1024 sc1\n"
"v_mfma_f32_16x16x32_bf16 v[168:171], a[60:63], v[36:39], v[168:171]\n"
"v_add_f32_e32 v212, v212, v220\n"
"v_add_f32_e32 v213, v213, v221\n"
"v_mfma_f32_16x16x32_bf16 v[168:171], a[56:59], v[188:191], v[168:171]\n"
"s_waitcnt lgkmcnt(5)\n"
"v_add_f32_e32 v214, v214, v222\n"
"v_add_f32_e32 v215, v215, v223\n"
"v_mfma_f32_16x16x32_bf16 v[168:171], a[52:55], v[184:187], v[168:171]\n"
"s_waitcnt lgkmcnt(0)\n"
"s_barrier\n"
"v_mfma_f32_16x16x32_bf16 v[168:171], a[48:51], v[92:95], v[168:171]\n"
"ds_read_b128 v[180:183], v6\n"
"ds_read_b128 v[196:199], v6 offset:1024\n"
"v_mfma_f32_16x16x32_bf16 v[168:171], a[44:47], v[80:83], v[168:171]\n"
"buffer_load_dwordx4 v[204:207], v0, s[4:7], s98 offen offset:2048 sc1\n"
"v_mfma_f32_16x16x32_bf16 v[168:171], a[40:43], v[76:79], v[168:171]\n"
"v_fmac_f32_e32 v152, v254, v192\n"
"v_fmac_f32_e32 v153, v254, v193\n"
"v_mfma_f32_16x16x32_bf16 v[168:171], a[36:39], v[84:87], v[168:171]\n"
"v_fmac_f32_e32 v154, v254, v194\n"
"v_fmac_f32_e32 v155, v254, v195\n"
"v_mfma_f32_16x16x32_bf16 v[168:171], a[32:35], v[68:71], v[168:171]\n"
"v_add_f32_e32 v152, v152, v24\n"
"v_add_f32_e32 v153, v153, v25\n"
"v_mfma_f32_16x16x32_bf16 v[200:203], a[64:67], v[68:71], v[200:203]\n"
"v_add_f32_e32 v154, v154, v26\n"
"v_add_f32_e32 v155, v155, v27\n"
"v_mfma_f32_16x16x32_bf16 v[200:203], a[68:71], v[84:87], v[200:203]\n"
"buffer_load_dwordx4 v[24:27], v0, s[4:7], s98 offen offset:3072 sc1\n"
"v_mfma_f32_16x16x32_bf16 v[200:203], a[72:75], v[76:79], v[200:203]\n"
"v_fmac_f32_e32 v52, v254, v56\n"
"v_fmac_f32_e32 v53, v254, v57\n"
"v_mfma_f32_16x16x32_bf16 v[200:203], a[76:79], v[80:83], v[200:203]\n"
"v_fmac_f32_e32 v54, v254, v58\n"
"v_fmac_f32_e32 v55, v254, v59\n"
"v_mfma_f32_16x16x32_bf16 v[200:203], a[80:83], v[92:95], v[200:203]\n"
"s_waitcnt lgkmcnt(0)\n"
"v_add_f32_e32 v52, v52, v88\n"
"v_add_f32_e32 v53, v53, v89\n"
"v_mfma_f32_16x16x32_bf16 v[200:203], a[84:87], v[184:187], v[200:203]\n"
"v_add_f32_e32 v54, v54, v90\n"
"v_add_f32_e32 v55, v55, v91\n"
"v_mfma_f32_16x16x32_bf16 v[200:203], a[88:91], v[188:191], v[200:203]\n"
"buffer_load_dwordx4 v[56:59], v1, s[4:7], s98 offen sc1\n"
"v_mfma_f32_16x16x32_bf16 v[200:203], a[92:95], v[36:39], v[200:203]\n"
"s_add_u32 s97, s70, 0x2000\n"
"ds_read_b128 v[88:91], v6 offset:2048\n"
"ds_read_b128 v[192:195], v6 offset:3072\n"
"v_mfma_f32_16x16x32_bf16 v[212:215], a[124:127], v[36:39], v[212:215]\n"
"buffer_load_dwordx4 a[160:163], v0, s[4:7], s97 offen sc1\n"
"v_mfma_f32_16x16x32_bf16 v[212:215], a[120:123], v[188:191], v[212:215]\n"
"v_add_f32_e32 v28, v28, v180\n"
"v_add_f32_e32 v29, v29, v181\n"
"v_mfma_f32_16x16x32_bf16 v[212:215], a[116:119], v[184:187], v[212:215]\n"
"v_add_f32_e32 v30, v30, v182\n"
"v_add_f32_e32 v31, v31, v183\n"
"v_mfma_f32_16x16x32_bf16 v[212:215], a[112:115], v[92:95], v[212:215]\n"
"buffer_load_dwordx4 v[180:183], v1, s[4:7], s98 offen offset:1024 sc1\n"
"v_mfma_f32_16x16x32_bf16 v[212:215], a[108:111], v[80:83], v[212:215]\n"
"v_add_f32_e32 v168, v168, v196\n"
"v_add_f32_e32 v169, v169, v197\n"
"v_mfma_f32_16x16x32_bf16 v[212:215], a[104:107], v[76:79], v[212:215]\n"
"v_add_f32_e32 v170, v170, v198\n"
"v_add_f32_e32 v171, v171, v199\n"
"v_mfma_f32_16x16x32_bf16 v[212:215], a[100:103], v[84:87], v[212:215]\n"
"v_mul_f32_e32 v28, v28, v255\n"
"v_mul_f32_e32 v29, v29, v255\n"
"v_mfma_f32_16x16x32_bf16 v[212:215], a[96:99], v[68:71], v[212:215]\n"
"v_mul_f32_e32 v30, v30, v255\n"
"v_mul_f32_e32 v31, v31, v255\n"
"v_mfma_f32_16x16x32_bf16 v[152:155], v[100:103], v[68:71], v[152:155]\n"
"buffer_load_dwordx4 v[196:199], v1, s[4:7], s98 offen offset:2048 sc1\n"
"v_mfma_f32_16x16x32_bf16 v[152:155], v[104:107], v[84:87], v[152:155]\n"
"v_mul_f32_e32 v168, v168, v255\n"
"v_mul_f32_e32 v169, v169, v255\n"
"v_mfma_f32_16x16x32_bf16 v[152:155], v[108:111], v[76:79], v[152:155]\n"
"v_mul_f32_e32 v170, v170, v255\n"
"v_mul_f32_e32 v171, v171, v255\n"
"v_mfma_f32_16x16x32_bf16 v[152:155], v[116:119], v[80:83], v[152:155]\n"
"v_cvt_pk_bf16_f32 v208, v28, v29\n"
"v_cvt_pk_bf16_f32 v209, v30, v31\n"
"v_mfma_f32_16x16x32_bf16 v[152:155], v[128:131], v[92:95], v[152:155]\n"
"s_waitcnt lgkmcnt(0)\n"
"buffer_store_dwordx4 v[44:47], v0, s[4:7], s60 offen sc1\n"
"v_cvt_pk_bf16_f32 v210, v168, v169\n"
"v_cvt_pk_bf16_f32 v211, v170, v171\n"
"v_mfma_f32_16x16x32_bf16 v[152:155], v[132:135], v[184:187], v[152:155]\n"
"buffer_load_dwordx4 v[28:31], v1, s[4:7], s98 offen offset:3072 sc1\n"
"ds_write_b128 v8, v[208:211] offset:25600\n"
"v_mfma_f32_16x16x32_bf16 v[152:155], v[136:139], v[188:191], v[152:155]\n"
"ds_read_b128 v[44:47], v6 offset:4096\n"
"ds_read_b128 v[168:171], v6 offset:5120\n"
"v_mfma_f32_16x16x32_bf16 v[152:155], v[96:99], v[36:39], v[152:155]\n"
"v_add_f32_e32 v200, v200, v88\n"
"v_add_f32_e32 v201, v201, v89\n"
"v_mfma_f32_16x16x32_bf16 v[52:55], v[112:115], v[36:39], v[52:55]\n"
"v_add_f32_e32 v202, v202, v90\n"
"v_add_f32_e32 v203, v203, v91\n"
"v_mfma_f32_16x16x32_bf16 v[52:55], v[164:167], v[188:191], v[52:55]\n"
"v_add_f32_e32 v212, v212, v192\n"
"v_add_f32_e32 v213, v213, v193\n"
"v_mfma_f32_16x16x32_bf16 v[52:55], v[160:163], v[184:187], v[52:55]\n"
"v_add_f32_e32 v214, v214, v194\n"
"v_add_f32_e32 v215, v215, v195\n"
"v_mfma_f32_16x16x32_bf16 v[52:55], v[156:159], v[92:95], v[52:55]\n"
"v_mul_f32_e32 v200, v200, v255\n"
"v_mul_f32_e32 v201, v201, v255\n"
"v_mfma_f32_16x16x32_bf16 v[52:55], v[124:127], v[80:83], v[52:55]\n"
"v_mul_f32_e32 v202, v202, v255\n"
"v_mul_f32_e32 v203, v203, v255\n"
"v_mfma_f32_16x16x32_bf16 v[52:55], v[148:151], v[76:79], v[52:55]\n"
"v_mul_f32_e32 v212, v212, v255\n"
"v_mul_f32_e32 v213, v213, v255\n"
"v_mfma_f32_16x16x32_bf16 v[52:55], v[144:147], v[84:87], v[52:55]\n"
"v_mul_f32_e32 v214, v214, v255\n"
"v_mul_f32_e32 v215, v215, v255\n"
"v_mfma_f32_16x16x32_bf16 v[52:55], v[120:123], v[68:71], v[52:55]\n"
"v_cvt_pk_bf16_f32 v36, v200, v201\n"
"v_cvt_pk_bf16_f32 v37, v202, v203\n"
"v_cvt_pk_bf16_f32 v38, v212, v213\n"
"v_cvt_pk_bf16_f32 v39, v214, v215\n"
"ds_write_b128 v7, v[36:39] offset:58368\n"
".Lmk_z2:\n"
"s_waitcnt vmcnt(17)\n"
"; @A G1 2.0\n"
"v_mfma_f32_16x16x32_bf16 v[68:71], a[0:3], v[40:43], 0\n"
"v_mfma_f32_16x16x32_bf16 v[68:71], a[4:7], v[48:51], v[68:71]\n"
"s_add_u32 s98, s62, 0x800\n"
"v_mfma_f32_16x16x32_bf16 v[68:71], a[8:11], v[176:179], v[68:71]\n"
"s_add_u32 s96, s54, 0x8000\n"
"buffer_load_dwordx4 v[76:79], v0, s[4:7], s96 offen sc1\n"
"v_mfma_f32_16x16x32_bf16 v[68:71], a[12:15], v[72:75], v[68:71]\n"
"s_waitcnt vmcnt(13)\n"
"v_mfma_f32_16x16x32_bf16 v[68:71], a[16:19], v[64:67], v[68:71]\n"
"buffer_load_dword v255, v10, s[4:7], s98 offen sc1\n"
"v_mfma_f32_16x16x32_bf16 v[68:71], a[20:23], v[224:227], v[68:71]\n"
"s_waitcnt lgkmcnt(1)\n"
"v_add_f32_e32 v152, v152, v44\n"
"v_add_f32_e32 v153, v153, v45\n"
"v_mfma_f32_16x16x32_bf16 v[68:71], a[24:27], v[172:175], v[68:71]\n"
"v_add_f32_e32 v154, v154, v46\n"
"v_add_f32_e32 v155, v155, v47\n"
"v_mfma_f32_16x16x32_bf16 v[68:71], a[28:31], v[60:63], v[68:71]\n"
"buffer_load_dwordx4 v[44:47], v0, s[4:7], s96 offen offset:1024 sc1\n"
"v_mfma_f32_16x16x32_bf16 v[80:83], a[60:63], v[60:63], 0\n"
"buffer_load_dwordx4 a[164:167], v0, s[4:7], s97 offen offset:1024 sc1\n"
"v_mfma_f32_16x16x32_bf16 v[80:83], a[56:59], v[172:175], v[80:83]\n"
"buffer_load_dword v254, v10, s[4:7], s98 offen offset:256 sc1\n"
"v_mfma_f32_16x16x32_bf16 v[80:83], a[52:55], v[224:227], v[80:83]\n"
"v_add_f32_e32 v52, v52, v168\n"
"v_add_f32_e32 v53, v53, v169\n"
"v_mfma_f32_16x16x32_bf16 v[80:83], a[48:51], v[64:67], v[80:83]\n"
"v_add_f32_e32 v54, v54, v170\n"
"v_add_f32_e32 v55, v55, v171\n"
"v_mfma_f32_16x16x32_bf16 v[80:83], a[44:47], v[72:75], v[80:83]\n"
"buffer_load_dwordx4 v[84:87], v0, s[4:7], s96 offen offset:2048 sc1\n"
"v_mfma_f32_16x16x32_bf16 v[80:83], a[40:43], v[176:179], v[80:83]\n"
"ds_write_b128 v3, v[68:71]\n"
"v_mfma_f32_16x16x32_bf16 v[80:83], a[36:39], v[48:51], v[80:83]\n"
"buffer_load_dword v252, v10, s[4:7], s98 offen offset:512 sc1\n"
"v_mfma_f32_16x16x32_bf16 v[80:83], a[32:35], v[40:43], v[80:83]\n"
"v_mul_f32_e32 v152, v152, v253\n"
"v_mul_f32_e32 v153, v153, v253\n"
"v_mfma_f32_16x16x32_bf16 v[68:71], a[64:67], v[40:43], 0\n"
"v_mul_f32_e32 v154, v154, v253\n"
"v_mul_f32_e32 v155, v155, v253\n"
"v_mfma_f32_16x16x32_bf16 v[68:71], a[68:71], v[48:51], v[68:71]\n"
"buffer_load_dwordx4 v[88:91], v0, s[4:7], s96 offen offset:3072 sc1\n"
"v_mfma_f32_16x16x32_bf16 v[68:71], a[72:75], v[176:179], v[68:71]\n"
"buffer_load_dword v251, v10, s[4:7], s98 offen offset:768 sc1\n"
"v_mfma_f32_16x16x32_bf16 v[68:71], a[76:79], v[72:75], v[68:71]\n"
"v_mul_f32_e32 v52, v52, v253\n"
"v_mul_f32_e32 v53, v53, v253\n"
"v_mfma_f32_16x16x32_bf16 v[68:71], a[80:83], v[64:67], v[68:71]\n"
"v_mul_f32_e32 v54, v54, v253\n"
"v_mul_f32_e32 v55, v55, v253\n"
"v_mfma_f32_16x16x32_bf16 v[68:71], a[84:87], v[224:227], v[68:71]\n"
"ds_write_b128 v3, v[80:83] offset:1024\n"
"v_mfma_f32_16x16x32_bf16 v[68:71], a[88:91], v[172:175], v[68:71]\n"
"buffer_load_dwordx4 v[80:83], v1, s[4:7], s96 offen sc1\n"
"v_mfma_f32_16x16x32_bf16 v[68:71], a[92:95], v[60:63], v[68:71]\n"
"buffer_load_dword v253, v10, s[4:7], s98 offen offset:1024 sc1\n"
"v_mfma_f32_16x16x32_bf16 v[92:95], a[124:127], v[60:63], 0\n"
"v_cvt_pk_bf16_f32 v168, v152, v153\n"
"v_cvt_pk_bf16_f32 v169, v154, v155\n"
"v_mfma_f32_16x16x32_bf16 v[92:95], a[120:123], v[172:175], v[92:95]\n"
"v_cvt_pk_bf16_f32 v170, v52, v53\n"
"v_cvt_pk_bf16_f32 v171, v54, v55\n"
"v_mfma_f32_16x16x32_bf16 v[92:95], a[116:119], v[224:227], v[92:95]\n"
"buffer_load_dword v250, v10, s[4:7], s98 offen offset:1280 sc1\n"
"v_mfma_f32_16x16x32_bf16 v[92:95], a[112:115], v[64:67], v[92:95]\n"
"buffer_load_dwordx4 v[52:55], v1, s[4:7], s96 offen offset:1024 sc1\n"
"v_mfma_f32_16x16x32_bf16 v[92:95], a[108:111], v[72:75], v[92:95]\n"
"buffer_load_dwordx4 a[168:171], v0, s[4:7], s97 offen offset:2048 sc1\n"
"v_mfma_f32_16x16x32_bf16 v[92:95], a[104:107], v[176:179], v[92:95]\n"
"ds_write_b128 v3, v[68:71] offset:2048\n"
"v_mfma_f32_16x16x32_bf16 v[92:95], a[100:103], v[48:51], v[92:95]\n"
"buffer_load_dword v249, v10, s[4:7], s98 offen offset:1536 sc1\n"
"v_mfma_f32_16x16x32_bf16 v[92:95], a[96:99], v[40:43], v[92:95]\n"
"ds_write_b128 v2, v[168:171] offset:50176\n"
"v_mfma_f32_16x16x32_bf16 v[68:71], v[100:103], v[40:43], 0\n"
"buffer_load_dwordx4 v[152:155], v1, s[4:7], s96 offen offset:2048 sc1\n"
"s_add_u32 s95, s64, 0x2000\n"
"v_mfma_f32_16x16x32_bf16 v[68:71], v[104:107], v[48:51], v[68:71]\n"
"buffer_load_dword v248, v10, s[4:7], s98 offen offset:1792 sc1\n"
"v_mfma_f32_16x16x32_bf16 v[68:71], v[108:111], v[176:179], v[68:71]\n"
"buffer_store_dwordx4 v[208:211], v8, s[4:7], s95 offen offset:1024 sc1\n"
"v_mfma_f32_16x16x32_bf16 v[68:71], v[116:119], v[72:75], v[68:71]\n"
"v_mfma_f32_16x16x32_bf16 v[68:71], v[128:131], v[64:67], v[68:71]\n"
"v_mfma_f32_16x16x32_bf16 v[68:71], v[132:135], v[224:227], v[68:71]\n"
"buffer_load_dwordx4 v[168:171], v1, s[4:7], s96 offen offset:3072 sc1\n"
"v_mfma_f32_16x16x32_bf16 v[68:71], v[136:139], v[172:175], v[68:71]\n"
"ds_write_b128 v3, v[92:95] offset:3072\n"
"v_mfma_f32_16x16x32_bf16 v[68:71], v[96:99], v[60:63], v[68:71]\n"
"v_mfma_f32_16x16x32_bf16 v[92:95], v[112:115], v[60:63], 0\n"
"v_mfma_f32_16x16x32_bf16 v[92:95], v[164:167], v[172:175], v[92:95]\n"
"v_mfma_f32_16x16x32_bf16 v[92:95], v[160:163], v[224:227], v[92:95]\n"
"v_mfma_f32_16x16x32_bf16 v[92:95], v[156:159], v[64:67], v[92:95]\n"
"v_mfma_f32_16x16x32_bf16 v[92:95], v[124:127], v[72:75], v[92:95]\n"
"v_mfma_f32_16x16x32_bf16 v[92:95], v[148:151], v[176:179], v[92:95]\n"
"s_nop 1\n"
"ds_write_b128 v3, v[68:71] offset:4096\n"
"v_mfma_f32_16x16x32_bf16 v[92:95], v[144:147], v[48:51], v[92:95]\n"
"v_mfma_f32_16x16x32_bf16 v[92:95], v[120:123], v[40:43], v[92:95]\n"
"s_waitcnt vmcnt(25)\n"
"; @A G1 2.1\n"
"v_mfma_f32_16x16x32_bf16 v[40:43], a[0:3], v[140:143], 0\n"
"buffer_load_dwordx4 a[172:175], v0, s[4:7], s97 offen offset:3072 sc1\n"
"v_mfma_f32_16x16x32_bf16 v[40:43], a[4:7], v[32:35], v[40:43]\n"
"v_mov_b32_e32 v48, -1\n"
"v_mov_b32_e32 v49, -1\n"
"v_mfma_f32_16x16x32_bf16 v[40:43], a[8:11], v[204:207], v[40:43]\n"
"s_add_u32 s98, s55, 0x8000\n"
"buffer_load_dwordx4 v[60:63], v0, s[4:7], s98 offen sc1\n"
"v_mfma_f32_16x16x32_bf16 v[40:43], a[12:15], v[24:27], v[40:43]\n"
"s_waitcnt vmcnt(21)\n"
"v_mov_b32_e32 v50, -1\n"
"v_mov_b32_e32 v51, -1\n"
"v_mfma_f32_16x16x32_bf16 v[40:43], a[16:19], v[56:59], v[40:43]\n"
"s_add_u32 s96, s60, 0x8000\n"
"buffer_store_dwordx4 v[48:51], v0, s[4:7], s96 offen sc1\n"
"v_mfma_f32_16x16x32_bf16 v[40:43], a[20:23], v[180:183], v[40:43]\n"
"ds_write_b128 v3, v[92:95] offset:5120\n"
"v_mfma_f32_16x16x32_bf16 v[40:43], a[24:27], v[196:199], v[40:43]\n"
"v_mfma_f32_16x16x32_bf16 v[40:43], a[28:31], v[28:31], v[40:43]\n"
"buffer_load_dwordx4 v[64:67], v0, s[4:7], s98 offen offset:1024 sc1\n"
"v_mfma_f32_16x16x32_bf16 v[68:71], a[60:63], v[28:31], 0\n"
"s_add_u32 s96, s64, 0x6000\n"
"v_mfma_f32_16x16x32_bf16 v[68:71], a[56:59], v[196:199], v[68:71]\n"
"v_mfma_f32_16x16x32_bf16 v[68:71], a[52:55], v[180:183], v[68:71]\n"
"buffer_store_dwordx4 v[36:39], v7, s[4:7], s96 offen offset:1024 sc1\n"
"v_mfma_f32_16x16x32_bf16 v[68:71], a[48:51], v[56:59], v[68:71]\n"
"v_mfma_f32_16x16x32_bf16 v[68:71], a[44:47], v[24:27], v[68:71]\n"
"buffer_load_dwordx4 v[36:39], v0, s[4:7], s98 offen offset:2048 sc1\n"
"v_mfma_f32_16x16x32_bf16 v[68:71], a[40:43], v[204:207], v[68:71]\n"
"ds_write_b128 v3, v[40:43] offset:6144\n"
"v_mfma_f32_16x16x32_bf16 v[68:71], a[36:39], v[32:35], v[68:71]\n"
"v_mfma_f32_16x16x32_bf16 v[68:71], a[32:35], v[140:143], v[68:71]\n"
"v_mfma_f32_16x16x32_bf16 v[40:43], a[64:67], v[140:143], 0\n"
"v_mfma_f32_16x16x32_bf16 v[40:43], a[68:71], v[32:35], v[40:43]\n"
"buffer_load_dwordx4 v[72:75], v0, s[4:7], s98 offen offset:3072 sc1\n"
"v_mfma_f32_16x16x32_bf16 v[40:43], a[72:75], v[204:207], v[40:43]\n"
"v_mfma_f32_16x16x32_bf16 v[40:43], a[76:79], v[24:27], v[40:43]\n"
"v_mfma_f32_16x16x32_bf16 v[40:43], a[80:83], v[56:59], v[40:43]\n"
"buffer_load_dwordx4 a[176:179], v1, s[4:7], s97 offen sc1\n"
"v_mfma_f32_16x16x32_bf16 v[40:43], a[84:87], v[180:183], v[40:43]\n"
"ds_write_b128 v3, v[68:71] offset:7168\n"
"v_mfma_f32_16x16x32_bf16 v[40:43], a[88:91], v[196:199], v[40:43]\n"
"buffer_load_dwordx4 v[68:71], v1, s[4:7], s98 offen sc1\n"
"v_mfma_f32_16x16x32_bf16 v[40:43], a[92:95], v[28:31], v[40:43]\n"
"v_mfma_f32_16x16x32_bf16 v[92:95], a[124:127], v[28:31], 0\n"
"s_add_u32 s96, s61, 0x440000\n"
"buffer_store_dwordx4 v[48:51], v0, s[4:7], s96 offen sc1\n"
"v_mfma_f32_16x16x32_bf16 v[92:95], a[120:123], v[196:199], v[92:95]\n"
"v_mfma_f32_16x16x32_bf16 v[92:95], a[116:119], v[180:183], v[92:95]\n"
"v_mfma_f32_16x16x32_bf16 v[92:95], a[112:115], v[56:59], v[92:95]\n"
"buffer_load_dwordx4 v[172:175], v1, s[4:7], s98 offen offset:1024 sc1\n"
"v_mfma_f32_16x16x32_bf16 v[92:95], a[108:111], v[24:27], v[92:95]\n"
"v_mfma_f32_16x16x32_bf16 v[92:95], a[104:107], v[204:207], v[92:95]\n"
"ds_write_b128 v3, v[40:43] offset:8192\n"
"v_mfma_f32_16x16x32_bf16 v[92:95], a[100:103], v[32:35], v[92:95]\n"
"v_mfma_f32_16x16x32_bf16 v[92:95], a[96:99], v[140:143], v[92:95]\n"
"v_mfma_f32_16x16x32_bf16 v[40:43], v[100:103], v[140:143], 0\n"
"buffer_load_dwordx4 v[176:179], v1, s[4:7], s98 offen offset:2048 sc1\n"
"v_mfma_f32_16x16x32_bf16 v[40:43], v[104:107], v[32:35], v[40:43]\n"
"v_mfma_f32_16x16x32_bf16 v[40:43], v[108:111], v[204:207], v[40:43]\n"
"v_mfma_f32_16x16x32_bf16 v[40:43], v[116:119], v[24:27], v[40:43]\n"
"v_mfma_f32_16x16x32_bf16 v[40:43], v[128:131], v[56:59], v[40:43]\n"
"s_waitcnt vmcnt(14)\n"
"v_add_f32_e32 v255, v255, v253\n"
"v_add_f32_e32 v254, v254, v250\n"
"v_mfma_f32_16x16x32_bf16 v[40:43], v[132:135], v[180:183], v[40:43]\n"
"buffer_load_dwordx4 v[184:187], v1, s[4:7], s98 offen offset:3072 sc1\n"
"v_mfma_f32_16x16x32_bf16 v[40:43], v[136:139], v[196:199], v[40:43]\n"
"ds_write_b128 v3, v[92:95] offset:9216\n"
"v_mfma_f32_16x16x32_bf16 v[40:43], v[96:99], v[28:31], v[40:43]\n"
"v_add_f32_e32 v252, v252, v249\n"
"v_add_f32_e32 v251, v251, v248\n"
"v_mfma_f32_16x16x32_bf16 v[92:95], v[112:115], v[28:31], 0\n"
"buffer_load_dwordx4 a[180:183], v1, s[4:7], s97 offen offset:1024 sc1\n"
"v_mfma_f32_16x16x32_bf16 v[92:95], v[164:167], v[196:199], v[92:95]\n"
"v_add_f32_e32 v255, v255, v252\n"
"v_add_f32_e32 v254, v254, v251\n"
"v_mfma_f32_16x16x32_bf16 v[92:95], v[160:163], v[180:183], v[92:95]\n"
"v_add_f32_e32 v255, v255, v254\n"
"v_mov_b32_e32 v254, v255\n"
"v_mfma_f32_16x16x32_bf16 v[92:95], v[156:159], v[56:59], v[92:95]\n"
"s_nop 0\n"
"v_permlane32_swap_b32_e32 v255, v254\n"
"v_add_f32_e32 v255, v255, v254\n"
"v_mfma_f32_16x16x32_bf16 v[92:95], v[124:127], v[24:27], v[92:95]\n"
"v_mul_f32_e32 v255, 0x3a800000, v255\n"
"v_mov_b32_e32 v254, v255\n"
"v_mfma_f32_16x16x32_bf16 v[92:95], v[148:151], v[204:207], v[92:95]\n"
"ds_write_b128 v3, v[40:43] offset:10240\n"
"v_mfma_f32_16x16x32_bf16 v[92:95], v[144:147], v[32:35], v[92:95]\n"
"v_permlane16_swap_b32_e32 v255, v254\n"
"v_cvt_pk_bf16_f32 v253, -v255, 0\n"
"v_mfma_f32_16x16x32_bf16 v[92:95], v[120:123], v[140:143], v[92:95]\n"
"v_fma_f32 v254, -v255, v255, v254\n"
"v_lshlrev_b32_e32 v253, 16, v253\n"
"; @A G1 2.2\n"
"v_mfma_f32_16x16x32_bf16 v[24:27], a[0:3], v[76:79], 0\n"
"s_add_u32 s98, s61, 0x440000\n"
"buffer_store_dwordx4 v[48:51], v0, s[4:7], s98 offen offset:1024 sc1\n"
"v_max_f32_e32 v254, 0, v254\n"
"v_cvt_pk_bf16_f32 v254, v254, 0\n"
"v_mfma_f32_16x16x32_bf16 v[24:27], a[4:7], v[44:47], v[24:27]\n"
"v_lshlrev_b32_e32 v254, 16, v254\n"
"v_add_f32_e32 v254, 0x3727c5ac, v254\n"
"v_mfma_f32_16x16x32_bf16 v[24:27], a[8:11], v[84:87], v[24:27]\n"
"v_cvt_pk_bf16_f32 v254, v254, 0\n"
"v_lshlrev_b32_e32 v254, 16, v254\n"
"v_mfma_f32_16x16x32_bf16 v[24:27], a[12:15], v[88:91], v[24:27]\n"
"s_waitcnt vmcnt(15)\n"
"v_sqrt_f32_e32 v254, v254\n"
"v_mfma_f32_16x16x32_bf16 v[24:27], a[16:19], v[80:83], v[24:27]\n"
"s_mov_b32 s98, .Lmk_z4-.Lmk_g11\n"
"s_getpc_b64 s[84:85]\n"
".Lmk_g11:\n"
"s_add_u32 s84, s84, s98\n"
"s_addc_u32 s85, s85, 0\n"
"s_and_b32 s84, s84, -16\n"
"v_mfma_f32_16x16x32_bf16 v[24:27], a[20:23], v[52:55], v[24:27]\n"
"ds_write_b128 v3, v[92:95] offset:11264\n"
"v_mfma_f32_16x16x32_bf16 v[24:27], a[24:27], v[152:155], v[24:27]\n"
"s_and_b32 s85, s85, 0xffff\n"
"s_sub_u32 s98, .Lmk_end-.Lmk_g11, s98\n"
"s_min_u32 s86, s98, 0x4000\n"
"s_mov_b32 s87, 0x20000\n"
"v_mfma_f32_16x16x32_bf16 v[24:27], a[28:31], v[168:171], v[24:27]\n"
"s_mov_b32 m0, 0x27800\n"
"s_nop 0\n"
"buffer_load_dwordx4 v0, s[84:87], s99 offen lds\n"
"v_mfma_f32_16x16x32_bf16 v[28:31], a[60:63], v[168:171], 0\n"
"v_cvt_pk_bf16_f32 v254, v254, 0\n"
"v_lshlrev_b32_e32 v254, 16, v254\n"
"v_mfma_f32_16x16x32_bf16 v[28:31], a[56:59], v[152:155], v[28:31]\n"
"v_rcp_f32_e32 v255, v254\n"
"v_mfma_f32_16x16x32_bf16 v[28:31], a[52:55], v[52:55], v[28:31]\n"
"s_add_u32 s98, s52, 0\n"
"buffer_load_dwordx4 v[32:35], v0, s[4:7], s98 offen sc1\n"
"v_mfma_f32_16x16x32_bf16 v[28:31], a[48:51], v[80:83], v[28:31]\n"
"s_waitcnt lgkmcnt(0)\n"
"s_barrier\n"
"v_mfma_f32_16x16x32_bf16 v[28:31], a[44:47], v[88:91], v[28:31]\n"
"buffer_load_dwordx4 a[184:187], v1, s[4:7], s97 offen offset:2048 sc1\n"
"v_mfma_f32_16x16x32_bf16 v[28:31], a[40:43], v[84:87], v[28:31]\n"
"ds_write_b128 v3, v[24:27] offset:12288\n"
"v_mfma_f32_16x16x32_bf16 v[28:31], a[36:39], v[44:47], v[28:31]\n"
"ds_read_b128 v[24:27], v4\n"
"v_mul_f32_e32 v254, 0x3e8293ee, v255\n"
"v_mfma_f32_16x16x32_bf16 v[28:31], a[32:35], v[76:79], v[28:31]\n"
"buffer_load_dwordx4 v[40:43], v0, s[4:7], s98 offen offset:1024 sc1\n"
"v_mfma_f32_16x16x32_bf16 v[56:59], a[64:67], v[76:79], 0\n"
"ds_read_b128 v[92:95], v9 offset:128\n"
"ds_read_b128 v[140:143], v5\n"
"v_mfma_f32_16x16x32_bf16 v[56:59], a[68:71], v[44:47], v[56:59]\n"
"v_mfma_f32_16x16x32_bf16 v[56:59], a[72:75], v[84:87], v[56:59]\n"
"v_mfma_f32_16x16x32_bf16 v[56:59], a[76:79], v[88:91], v[56:59]\n"
"ds_read_b128 v[180:183], v4 offset:1024\n"
"ds_read_b128 v[188:191], v9 offset:192\n"
"v_mfma_f32_16x16x32_bf16 v[56:59], a[80:83], v[80:83], v[56:59]\n"
"buffer_load_dwordx4 v[192:195], v0, s[4:7], s98 offen offset:2048 sc1\n"
"v_mfma_f32_16x16x32_bf16 v[56:59], a[84:87], v[52:55], v[56:59]\n"
"ds_write_b128 v3, v[28:31] offset:13312\n"
"v_mfma_f32_16x16x32_bf16 v[56:59], a[88:91], v[152:155], v[56:59]\n"
"ds_read_b128 v[28:31], v5 offset:1024\n"
"s_waitcnt lgkmcnt(4)\n"
"v_mfma_f32_16x16x32_bf16 v[56:59], a[92:95], v[168:171], v[56:59]\n"
"v_fmac_f32_e32 v24, v253, v92\n"
"v_fmac_f32_e32 v25, v253, v93\n"
"v_mfma_f32_16x16x32_bf16 v[196:199], a[124:127], v[168:171], 0\n"
"s_add_u32 s96, s63, 0x44000\n"
"buffer_store_dword v48, v14, s[4:7], s96 offen sc1\n"
"v_fmac_f32_e32 v26, v253, v94\n"
"v_fmac_f32_e32 v27, v253, v95\n"
"v_mfma_f32_16x16x32_bf16 v[196:199], a[120:123], v[152:155], v[196:199]\n"
"buffer_load_dwordx4 v[48:51], v0, s[4:7], s98 offen offset:3072 sc1\n"
"v_mfma_f32_16x16x32_bf16 v[196:199], a[116:119], v[52:55], v[196:199]\n"
"ds_read_b128 v[92:95], v4 offset:2048\n"
"ds_read_b128 v[200:203], v9 offset:256\n"
"v_mfma_f32_16x16x32_bf16 v[196:199], a[112:115], v[80:83], v[196:199]\n"
"ds_read_b128 v[204:207], v5 offset:2048\n"
"v_add_f32_e32 v24, v24, v140\n"
"v_mfma_f32_16x16x32_bf16 v[196:199], a[108:111], v[88:91], v[196:199]\n"
"v_add_f32_e32 v25, v25, v141\n"
"v_add_f32_e32 v26, v26, v142\n"
"v_mfma_f32_16x16x32_bf16 v[196:199], a[104:107], v[84:87], v[196:199]\n"
"ds_write_b128 v3, v[56:59] offset:14336\n"
"v_mfma_f32_16x16x32_bf16 v[196:199], a[100:103], v[44:47], v[196:199]\n"
"buffer_load_dwordx4 v[56:59], v1, s[4:7], s98 offen sc1\n"
"v_mfma_f32_16x16x32_bf16 v[196:199], a[96:99], v[76:79], v[196:199]\n"
"s_waitcnt lgkmcnt(4)\n"
"v_add_f32_e32 v27, v27, v143\n"
"v_fmac_f32_e32 v180, v253, v188\n"
"v_mfma_f32_16x16x32_bf16 v[140:143], v[100:103], v[76:79], 0\n"
"buffer_load_dwordx4 a[188:191], v1, s[4:7], s97 offen offset:3072 sc1\n"
"v_mfma_f32_16x16x32_bf16 v[140:143], v[104:107], v[44:47], v[140:143]\n"
"ds_read_b128 v[208:211], v4 offset:3072\n"
"ds_read_b128 v[212:215], v9 offset:320\n"
"v_mfma_f32_16x16x32_bf16 v[140:143], v[108:111], v[84:87], v[140:143]\n"
"s_waitcnt vmcnt(19)\n"
"s_add_u32 s97, s68, 0x1000\n"
"ds_read_b128 v[216:219], v5 offset:3072\n"
"v_mfma_f32_16x16x32_bf16 v[140:143], v[116:119], v[88:91], v[140:143]\n"
"buffer_load_dwordx4 v[220:223], v1, s[4:7], s98 offen offset:1024 sc1\n"
"v_mfma_f32_16x16x32_bf16 v[140:143], v[128:131], v[80:83], v[140:143]\n"
"buffer_store_dwordx2 v[18:19], v11, s[4:7], s97 offen offset:64 sc1\n"
"v_mfma_f32_16x16x32_bf16 v[140:143], v[132:135], v[52:55], v[140:143]\n"
"ds_write_b128 v3, v[196:199] offset:15360\n"
"v_mfma_f32_16x16x32_bf16 v[140:143], v[136:139], v[152:155], v[140:143]\n"
"v_fmac_f32_e32 v181, v253, v189\n"
"v_fmac_f32_e32 v182, v253, v190\n"
"v_mfma_f32_16x16x32_bf16 v[140:143], v[96:99], v[168:171], v[140:143]\n"
"v_fmac_f32_e32 v183, v253, v191\n"
"v_add_f32_e32 v180, v180, v28\n"
"v_mfma_f32_16x16x32_bf16 v[188:191], v[112:115], v[168:171], 0\n"
"buffer_load_dwordx4 v[168:171], v1, s[4:7], s98 offen offset:2048 sc1\n"
"v_mfma_f32_16x16x32_bf16 v[188:191], v[164:167], v[152:155], v[188:191]\n"
"ds_read_b128 v[152:155], v4 offset:4096\n"
"ds_read_b128 v[196:199], v9\n"
"v_mfma_f32_16x16x32_bf16 v[188:191], v[160:163], v[52:55], v[188:191]\n"
"ds_read_b128 v[52:55], v5 offset:4096\n"
"v_add_f32_e32 v181, v181, v29\n"
"v_mfma_f32_16x16x32_bf16 v[188:191], v[156:159], v[80:83], v[188:191]\n"
"s_waitcnt lgkmcnt(8)\n"
"v_add_f32_e32 v182, v182, v30\n"
"v_add_f32_e32 v183, v183, v31\n"
"v_mfma_f32_16x16x32_bf16 v[188:191], v[124:127], v[88:91], v[188:191]\n"
"v_fmac_f32_e32 v92, v253, v200\n"
"v_fmac_f32_e32 v93, v253, v201\n"
"v_mfma_f32_16x16x32_bf16 v[188:191], v[148:151], v[84:87], v[188:191]\n"
"buffer_load_dwordx4 v[28:31], v1, s[4:7], s98 offen offset:3072 sc1\n"
"v_mfma_f32_16x16x32_bf16 v[188:191], v[144:147], v[44:47], v[188:191]\n"
"ds_write_b128 v3, v[140:143] offset:16384\n"
"v_mfma_f32_16x16x32_bf16 v[188:191], v[120:123], v[76:79], v[188:191]\n"
"s_waitcnt vmcnt(21)\n"
"ds_read_b128 v[44:47], v4 offset:5120\n"
"ds_read_b128 v[76:79], v9 offset:64\n"
"; @A G1 2.3\n"
"v_mfma_f32_16x16x32_bf16 v[24:27], a[0:3], v[60:63], v[24:27]\n"
"ds_read_b128 v[80:83], v5 offset:5120\n"
"v_fmac_f32_e32 v94, v253, v202\n"
"v_mfma_f32_16x16x32_bf16 v[24:27], a[4:7], v[64:67], v[24:27]\n"
"v_fmac_f32_e32 v95, v253, v203\n"
"v_add_f32_e32 v92, v92, v204\n"
"v_mfma_f32_16x16x32_bf16 v[24:27], a[8:11], v[36:39], v[24:27]\n"
"s_add_u32 s98, s53, 0\n"
"buffer_load_dwordx4 v[84:87], v0, s[4:7], s98 offen sc1\n"
"v_mfma_f32_16x16x32_bf16 v[24:27], a[12:15], v[72:75], v[24:27]\n"
"s_waitcnt vmcnt(16)\n"
"s_add_u32 s97, s70, 0x4000\n"
"v_add_f32_e32 v93, v93, v205\n"
"v_mfma_f32_16x16x32_bf16 v[24:27], a[16:19], v[68:71], v[24:27]\n"
"buffer_load_dwordx4 a[192:195], v0, s[4:7], s97 offen sc1\n"
"v_mfma_f32_16x16x32_bf16 v[24:27], a[20:23], v[172:175], v[24:27]\n"
"ds_write_b128 v3, v[188:191] offset:17408\n"
"v_mfma_f32_16x16x32_bf16 v[24:27], a[24:27], v[176:179], v[24:27]\n"
"s_waitcnt lgkmcnt(9)\n"
"v_add_f32_e32 v94, v94, v206\n"
"v_add_f32_e32 v95, v95, v207\n"
"v_mfma_f32_16x16x32_bf16 v[24:27], a[28:31], v[184:187], v[24:27]\n"
"buffer_load_dwordx4 v[88:91], v0, s[4:7], s98 offen offset:1024 sc1\n"
"v_mfma_f32_16x16x32_bf16 v[180:183], a[60:63], v[184:187], v[180:183]\n"
"v_fmac_f32_e32 v208, v253, v212\n"
"v_fmac_f32_e32 v209, v253, v213\n"
"v_mfma_f32_16x16x32_bf16 v[180:183], a[56:59], v[176:179], v[180:183]\n"
"v_fmac_f32_e32 v210, v253, v214\n"
"v_fmac_f32_e32 v211, v253, v215\n"
"v_mfma_f32_16x16x32_bf16 v[180:183], a[52:55], v[172:175], v[180:183]\n"
"s_waitcnt lgkmcnt(0)\n"
"s_barrier\n"
"v_mfma_f32_16x16x32_bf16 v[180:183], a[48:51], v[68:71], v[180:183]\n"
"ds_read_b128 v[140:143], v6\n"
"ds_read_b128 v[188:191], v6 offset:1024\n"
"v_mfma_f32_16x16x32_bf16 v[180:183], a[44:47], v[72:75], v[180:183]\n"
"buffer_load_dwordx4 v[200:203], v0, s[4:7], s98 offen offset:2048 sc1\n"
"v_mfma_f32_16x16x32_bf16 v[180:183], a[40:43], v[36:39], v[180:183]\n"
"v_add_f32_e32 v208, v208, v216\n"
"v_add_f32_e32 v209, v209, v217\n"
"v_mfma_f32_16x16x32_bf16 v[180:183], a[36:39], v[64:67], v[180:183]\n"
"v_add_f32_e32 v210, v210, v218\n"
"v_add_f32_e32 v211, v211, v219\n"
"v_mfma_f32_16x16x32_bf16 v[180:183], a[32:35], v[60:63], v[180:183]\n"
"v_fmac_f32_e32 v152, v253, v196\n"
"v_fmac_f32_e32 v153, v253, v197\n"
"v_mfma_f32_16x16x32_bf16 v[92:95], a[64:67], v[60:63], v[92:95]\n"
"v_fmac_f32_e32 v154, v253, v198\n"
"v_fmac_f32_e32 v155, v253, v199\n"
"v_mfma_f32_16x16x32_bf16 v[92:95], a[68:71], v[64:67], v[92:95]\n"
"buffer_load_dwordx4 v[196:199], v0, s[4:7], s98 offen offset:3072 sc1\n"
"v_mfma_f32_16x16x32_bf16 v[92:95], a[72:75], v[36:39], v[92:95]\n"
"v_add_f32_e32 v152, v152, v52\n"
"v_add_f32_e32 v153, v153, v53\n"
"v_mfma_f32_16x16x32_bf16 v[92:95], a[76:79], v[72:75], v[92:95]\n"
"v_add_f32_e32 v154, v154, v54\n"
"v_add_f32_e32 v155, v155, v55\n"
"v_mfma_f32_16x16x32_bf16 v[92:95], a[80:83], v[68:71], v[92:95]\n"
"s_waitcnt lgkmcnt(0)\n"
"v_fmac_f32_e32 v44, v253, v76\n"
"v_fmac_f32_e32 v45, v253, v77\n"
"v_mfma_f32_16x16x32_bf16 v[92:95], a[84:87], v[172:175], v[92:95]\n"
"v_fmac_f32_e32 v46, v253, v78\n"
"v_fmac_f32_e32 v47, v253, v79\n"
"v_mfma_f32_16x16x32_bf16 v[92:95], a[88:91], v[176:179], v[92:95]\n"
"buffer_load_dwordx4 v[52:55], v1, s[4:7], s98 offen sc1\n"
"v_mfma_f32_16x16x32_bf16 v[92:95], a[92:95], v[184:187], v[92:95]\n"
"ds_read_b128 v[76:79], v6 offset:2048\n"
"ds_read_b128 v[204:207], v6 offset:3072\n"
"v_mfma_f32_16x16x32_bf16 v[208:211], a[124:127], v[184:187], v[208:211]\n"
"buffer_load_dwordx4 a[196:199], v0, s[4:7], s97 offen offset:1024 sc1\n"
"v_mfma_f32_16x16x32_bf16 v[208:211], a[120:123], v[176:179], v[208:211]\n"
"v_add_f32_e32 v44, v44, v80\n"
"v_add_f32_e32 v45, v45, v81\n"
"v_mfma_f32_16x16x32_bf16 v[208:211], a[116:119], v[172:175], v[208:211]\n"
"s_add_u32 s96, s68, 0x1000\n"
"buffer_store_dwordx2 v[18:19], v11, s[4:7], s96 offen offset:96 sc1\n"
"v_add_f32_e32 v46, v46, v82\n"
"v_add_f32_e32 v47, v47, v83\n"
"v_mfma_f32_16x16x32_bf16 v[208:211], a[112:115], v[68:71], v[208:211]\n"
"buffer_load_dwordx4 v[80:83], v1, s[4:7], s98 offen offset:1024 sc1\n"
"v_mfma_f32_16x16x32_bf16 v[208:211], a[108:111], v[72:75], v[208:211]\n"
"v_add_f32_e32 v24, v24, v140\n"
"v_add_f32_e32 v25, v25, v141\n"
"v_mfma_f32_16x16x32_bf16 v[208:211], a[104:107], v[36:39], v[208:211]\n"
"v_add_f32_e32 v26, v26, v142\n"
"v_add_f32_e32 v27, v27, v143\n"
"v_mfma_f32_16x16x32_bf16 v[208:211], a[100:103], v[64:67], v[208:211]\n"
"v_add_f32_e32 v180, v180, v188\n"
"v_add_f32_e32 v181, v181, v189\n"
"v_mfma_f32_16x16x32_bf16 v[208:211], a[96:99], v[60:63], v[208:211]\n"
"v_add_f32_e32 v182, v182, v190\n"
"v_add_f32_e32 v183, v183, v191\n"
"v_mfma_f32_16x16x32_bf16 v[152:155], v[100:103], v[60:63], v[152:155]\n"
"buffer_load_dwordx4 v[140:143], v1, s[4:7], s98 offen offset:2048 sc1\n"
"v_mfma_f32_16x16x32_bf16 v[152:155], v[104:107], v[64:67], v[152:155]\n"
"v_mul_f32_e32 v24, v24, v255\n"
"v_mul_f32_e32 v25, v25, v255\n"
"v_mfma_f32_16x16x32_bf16 v[152:155], v[108:111], v[36:39], v[152:155]\n"
"v_mul_f32_e32 v26, v26, v255\n"
"v_mul_f32_e32 v27, v27, v255\n"
"v_mfma_f32_16x16x32_bf16 v[152:155], v[116:119], v[72:75], v[152:155]\n"
"v_mul_f32_e32 v180, v180, v255\n"
"v_mul_f32_e32 v181, v181, v255\n"
"v_mfma_f32_16x16x32_bf16 v[152:155], v[128:131], v[68:71], v[152:155]\n"
"s_waitcnt lgkmcnt(0)\n"
"v_mul_f32_e32 v182, v182, v255\n"
"v_mul_f32_e32 v183, v183, v255\n"
"v_mfma_f32_16x16x32_bf16 v[152:155], v[132:135], v[172:175], v[152:155]\n"
"buffer_load_dwordx4 v[188:191], v1, s[4:7], s98 offen offset:3072 sc1\n"
"v_mfma_f32_16x16x32_bf16 v[152:155], v[136:139], v[176:179], v[152:155]\n"
"ds_read_b128 v[212:215], v6 offset:4096\n"
"ds_read_b128 v[216:219], v6 offset:5120\n"
"v_mfma_f32_16x16x32_bf16 v[152:155], v[96:99], v[184:187], v[152:155]\n"
"v_cvt_pk_bf16_f32 v224, v24, v25\n"
"v_cvt_pk_bf16_f32 v225, v26, v27\n"
"v_mfma_f32_16x16x32_bf16 v[44:47], v[112:115], v[184:187], v[44:47]\n"
"v_cvt_pk_bf16_f32 v226, v180, v181\n"
"v_cvt_pk_bf16_f32 v227, v182, v183\n"
"v_mfma_f32_16x16x32_bf16 v[44:47], v[164:167], v[176:179], v[44:47]\n"
"ds_write_b128 v8, v[224:227] offset:17408\n"
"v_mfma_f32_16x16x32_bf16 v[44:47], v[160:163], v[172:175], v[44:47]\n"
"v_add_f32_e32 v92, v92, v76\n"
"v_add_f32_e32 v93, v93, v77\n"
"v_mfma_f32_16x16x32_bf16 v[44:47], v[156:159], v[68:71], v[44:47]\n"
"v_add_f32_e32 v94, v94, v78\n"
"v_add_f32_e32 v95, v95, v79\n"
"v_mfma_f32_16x16x32_bf16 v[44:47], v[124:127], v[72:75], v[44:47]\n"
"v_add_f32_e32 v208, v208, v204\n"
"v_add_f32_e32 v209, v209, v205\n"
"v_mfma_f32_16x16x32_bf16 v[44:47], v[148:151], v[36:39], v[44:47]\n"
"v_add_f32_e32 v210, v210, v206\n"
"v_add_f32_e32 v211, v211, v207\n"
"v_mfma_f32_16x16x32_bf16 v[44:47], v[144:147], v[64:67], v[44:47]\n"
"v_mul_f32_e32 v92, v92, v255\n"
"v_mul_f32_e32 v93, v93, v255\n"
"v_mfma_f32_16x16x32_bf16 v[44:47], v[120:123], v[60:63], v[44:47]\n"
"v_mul_f32_e32 v94, v94, v255\n"
"v_mul_f32_e32 v95, v95, v255\n"
"v_mul_f32_e32 v208, v208, v255\n"
"v_mul_f32_e32 v209, v209, v255\n"
"v_mul_f32_e32 v210, v210, v255\n"
"v_mul_f32_e32 v211, v211, v255\n"
"v_cvt_pk_bf16_f32 v24, v92, v93\n"
"v_cvt_pk_bf16_f32 v25, v94, v95\n"
"v_cvt_pk_bf16_f32 v26, v208, v209\n"
"v_cvt_pk_bf16_f32 v27, v210, v211\n"
"ds_write_b128 v7, v[24:27] offset:50176\n"
".Lmk_z3:\n"
"s_waitcnt vmcnt(17)\n"
"; @A G1 3.0\n"
"v_mfma_f32_16x16x32_bf16 v[36:39], a[0:3], v[32:35], 0\n"
"v_mfma_f32_16x16x32_bf16 v[36:39], a[4:7], v[40:43], v[36:39]\n"
"s_add_u32 s98, s62, 0\n"
"v_mfma_f32_16x16x32_bf16 v[36:39], a[8:11], v[192:195], v[36:39]\n"
"s_add_u32 s96, s54, 0\n"
"buffer_load_dwordx4 v[60:63], v0, s[4:7], s96 offen sc1\n"
"v_mfma_f32_16x16x32_bf16 v[36:39], a[12:15], v[48:51], v[36:39]\n"
"s_waitcnt vmcnt(12)\n"
"v_mfma_f32_16x16x32_bf16 v[36:39], a[16:19], v[56:59], v[36:39]\n"
"buffer_load_dword v255, v10, s[4:7], s98 offen sc1\n"
"v_mfma_f32_16x16x32_bf16 v[36:39], a[20:23], v[220:223], v[36:39]\n"
"s_waitcnt lgkmcnt(2)\n"
"v_add_f32_e32 v152, v152, v212\n"
"v_add_f32_e32 v153, v153, v213\n"
"v_mfma_f32_16x16x32_bf16 v[36:39], a[24:27], v[168:171], v[36:39]\n"
"v_add_f32_e32 v154, v154, v214\n"
"v_add_f32_e32 v155, v155, v215\n"
"v_mfma_f32_16x16x32_bf16 v[36:39], a[28:31], v[28:31], v[36:39]\n"
"buffer_load_dwordx4 v[64:67], v0, s[4:7], s96 offen offset:1024 sc1\n"
"v_mfma_f32_16x16x32_bf16 v[68:71], a[60:63], v[28:31], 0\n"
"buffer_load_dwordx4 a[200:203], v0, s[4:7], s97 offen offset:2048 sc1\n"
"v_mfma_f32_16x16x32_bf16 v[68:71], a[56:59], v[168:171], v[68:71]\n"
"buffer_load_dword v253, v10, s[4:7], s98 offen offset:256 sc1\n"
"v_mfma_f32_16x16x32_bf16 v[68:71], a[52:55], v[220:223], v[68:71]\n"
"v_add_f32_e32 v44, v44, v216\n"
"v_add_f32_e32 v45, v45, v217\n"
"v_mfma_f32_16x16x32_bf16 v[68:71], a[48:51], v[56:59], v[68:71]\n"
"v_add_f32_e32 v46, v46, v218\n"
"v_add_f32_e32 v47, v47, v219\n"
"v_mfma_f32_16x16x32_bf16 v[68:71], a[44:47], v[48:51], v[68:71]\n"
"buffer_load_dwordx4 v[72:75], v0, s[4:7], s96 offen offset:2048 sc1\n"
"v_mfma_f32_16x16x32_bf16 v[68:71], a[40:43], v[192:195], v[68:71]\n"
"ds_write_b128 v3, v[36:39]\n"
"v_mfma_f32_16x16x32_bf16 v[68:71], a[36:39], v[40:43], v[68:71]\n"
"buffer_load_dword v252, v10, s[4:7], s98 offen offset:512 sc1\n"
"v_mfma_f32_16x16x32_bf16 v[68:71], a[32:35], v[32:35], v[68:71]\n"
"v_mul_f32_e32 v152, v152, v254\n"
"v_mul_f32_e32 v153, v153, v254\n"
"v_mfma_f32_16x16x32_bf16 v[36:39], a[64:67], v[32:35], 0\n"
"v_mul_f32_e32 v154, v154, v254\n"
"v_mul_f32_e32 v155, v155, v254\n"
"v_mfma_f32_16x16x32_bf16 v[36:39], a[68:71], v[40:43], v[36:39]\n"
"buffer_load_dwordx4 v[76:79], v0, s[4:7], s96 offen offset:3072 sc1\n"
"v_mfma_f32_16x16x32_bf16 v[36:39], a[72:75], v[192:195], v[36:39]\n"
"buffer_load_dword v251, v10, s[4:7], s98 offen offset:768 sc1\n"
"v_mfma_f32_16x16x32_bf16 v[36:39], a[76:79], v[48:51], v[36:39]\n"
"v_mul_f32_e32 v44, v44, v254\n"
"v_mul_f32_e32 v45, v45, v254\n"
"v_mfma_f32_16x16x32_bf16 v[36:39], a[80:83], v[56:59], v[36:39]\n"
"v_mul_f32_e32 v46, v46, v254\n"
"v_mul_f32_e32 v47, v47, v254\n"
"v_mfma_f32_16x16x32_bf16 v[36:39], a[84:87], v[220:223], v[36:39]\n"
"ds_write_b128 v3, v[68:71] offset:1024\n"
"v_mfma_f32_16x16x32_bf16 v[36:39], a[88:91], v[168:171], v[36:39]\n"
"buffer_load_dwordx4 v[68:71], v1, s[4:7], s96 offen sc1\n"
"v_mfma_f32_16x16x32_bf16 v[36:39], a[92:95], v[28:31], v[36:39]\n"
"buffer_load_dword v254, v10, s[4:7], s98 offen offset:1024 sc1\n"
"v_mfma_f32_16x16x32_bf16 v[92:95], a[124:127], v[28:31], 0\n"
"v_cvt_pk_bf16_f32 v172, v152, v153\n"
"v_cvt_pk_bf16_f32 v173, v154, v155\n"
"v_mfma_f32_16x16x32_bf16 v[92:95], a[120:123], v[168:171], v[92:95]\n"
"v_cvt_pk_bf16_f32 v174, v44, v45\n"
"v_cvt_pk_bf16_f32 v175, v46, v47\n"
"v_mfma_f32_16x16x32_bf16 v[92:95], a[116:119], v[220:223], v[92:95]\n"
"buffer_load_dwordx4 a[204:207], v0, s[4:7], s97 offen offset:3072 sc1\n"
"v_mfma_f32_16x16x32_bf16 v[92:95], a[112:115], v[56:59], v[92:95]\n"
"buffer_load_dwordx4 v[44:47], v1, s[4:7], s96 offen offset:1024 sc1\n"
"v_mfma_f32_16x16x32_bf16 v[92:95], a[108:111], v[48:51], v[92:95]\n"
"buffer_load_dword v250, v10, s[4:7], s98 offen offset:1280 sc1\n"
"v_mfma_f32_16x16x32_bf16 v[92:95], a[104:107], v[192:195], v[92:95]\n"
"ds_write_b128 v3, v[36:39] offset:2048\n"
"v_mfma_f32_16x16x32_bf16 v[92:95], a[100:103], v[40:43], v[92:95]\n"
"ds_write_b128 v2, v[172:175] offset:17408\n"
"v_mfma_f32_16x16x32_bf16 v[92:95], a[96:99], v[32:35], v[92:95]\n"
"buffer_load_dword v249, v10, s[4:7], s98 offen offset:1536 sc1\n"
"v_mfma_f32_16x16x32_bf16 v[36:39], v[100:103], v[32:35], 0\n"
"buffer_load_dwordx4 v[152:155], v1, s[4:7], s96 offen offset:2048 sc1\n"
"v_mfma_f32_16x16x32_bf16 v[36:39], v[104:107], v[40:43], v[36:39]\n"
"buffer_store_dwordx4 v[224:227], v8, s[4:7], s64 offen offset:1024 sc1\n"
"v_mfma_f32_16x16x32_bf16 v[36:39], v[108:111], v[192:195], v[36:39]\n"
"buffer_load_dword v248, v10, s[4:7], s98 offen offset:1792 sc1\n"
"v_mfma_f32_16x16x32_bf16 v[36:39], v[116:119], v[48:51], v[36:39]\n"
"v_mfma_f32_16x16x32_bf16 v[36:39], v[128:131], v[56:59], v[36:39]\n"
"v_mfma_f32_16x16x32_bf16 v[36:39], v[132:135], v[220:223], v[36:39]\n"
"buffer_load_dwordx4 v[172:175], v1, s[4:7], s96 offen offset:3072 sc1\n"
"v_mfma_f32_16x16x32_bf16 v[36:39], v[136:139], v[168:171], v[36:39]\n"
"ds_write_b128 v3, v[92:95] offset:3072\n"
"v_mfma_f32_16x16x32_bf16 v[36:39], v[96:99], v[28:31], v[36:39]\n"
"v_mfma_f32_16x16x32_bf16 v[92:95], v[112:115], v[28:31], 0\n"
"v_mfma_f32_16x16x32_bf16 v[92:95], v[164:167], v[168:171], v[92:95]\n"
"v_mfma_f32_16x16x32_bf16 v[92:95], v[160:163], v[220:223], v[92:95]\n"
"v_mfma_f32_16x16x32_bf16 v[92:95], v[156:159], v[56:59], v[92:95]\n"
"v_mfma_f32_16x16x32_bf16 v[92:95], v[124:127], v[48:51], v[92:95]\n"
"buffer_load_dwordx4 a[208:211], v1, s[4:7], s97 offen sc1\n"
"v_mfma_f32_16x16x32_bf16 v[92:95], v[148:151], v[192:195], v[92:95]\n"
"s_nop 0\n"
"ds_write_b128 v3, v[36:39] offset:4096\n"
"v_mfma_f32_16x16x32_bf16 v[92:95], v[144:147], v[40:43], v[92:95]\n"
"v_mfma_f32_16x16x32_bf16 v[92:95], v[120:123], v[32:35], v[92:95]\n"
"s_waitcnt vmcnt(26)\n"
"; @A G1 3.1\n"
"v_mfma_f32_16x16x32_bf16 v[28:31], a[0:3], v[84:87], 0\n"
"v_mfma_f32_16x16x32_bf16 v[28:31], a[4:7], v[88:91], v[28:31]\n"
"v_mfma_f32_16x16x32_bf16 v[28:31], a[8:11], v[200:203], v[28:31]\n"
"s_add_u32 s98, s55, 0\n"
"buffer_load_dwordx4 v[32:35], v0, s[4:7], s98 offen sc1\n"
"v_mfma_f32_16x16x32_bf16 v[28:31], a[12:15], v[196:199], v[28:31]\n"
"s_waitcnt vmcnt(21)\n"
"v_mfma_f32_16x16x32_bf16 v[28:31], a[16:19], v[52:55], v[28:31]\n"
"v_mfma_f32_16x16x32_bf16 v[28:31], a[20:23], v[80:83], v[28:31]\n"
"ds_write_b128 v3, v[92:95] offset:5120\n"
"v_mfma_f32_16x16x32_bf16 v[28:31], a[24:27], v[140:143], v[28:31]\n"
"v_mfma_f32_16x16x32_bf16 v[28:31], a[28:31], v[188:191], v[28:31]\n"
"buffer_load_dwordx4 v[36:39], v0, s[4:7], s98 offen offset:1024 sc1\n"
"s_add_u32 s96, s64, 0x4000\n"
"v_mfma_f32_16x16x32_bf16 v[40:43], a[60:63], v[188:191], 0\n"
"v_mfma_f32_16x16x32_bf16 v[40:43], a[56:59], v[140:143], v[40:43]\n"
"buffer_store_dwordx4 v[24:27], v7, s[4:7], s96 offen offset:1024 sc1\n"
"v_mfma_f32_16x16x32_bf16 v[40:43], a[52:55], v[80:83], v[40:43]\n"
"v_mfma_f32_16x16x32_bf16 v[40:43], a[48:51], v[52:55], v[40:43]\n"
"v_mfma_f32_16x16x32_bf16 v[40:43], a[44:47], v[196:199], v[40:43]\n"
"buffer_load_dwordx4 v[24:27], v0, s[4:7], s98 offen offset:2048 sc1\n"
"v_mfma_f32_16x16x32_bf16 v[40:43], a[40:43], v[200:203], v[40:43]\n"
"ds_write_b128 v3, v[28:31] offset:6144\n"
"v_mfma_f32_16x16x32_bf16 v[40:43], a[36:39], v[88:91], v[40:43]\n"
"buffer_load_dwordx4 a[212:215], v1, s[4:7], s97 offen offset:1024 sc1\n"
"v_mfma_f32_16x16x32_bf16 v[40:43], a[32:35], v[84:87], v[40:43]\n"
"s_mov_b32 s20, 0xff0000\n"
"s_mov_b32 s21, 0xffffffff\n"
"s_mov_b32 s22, 0x1ff0001\n"
"v_mfma_f32_16x16x32_bf16 v[28:31], a[64:67], v[84:87], 0\n"
"s_mov_b32 s23, 0xffffffff\n"
"s_mov_b32 s24, 0x3ff0003\n"
"s_mov_b32 s25, 0xffffffff\n"
"v_mfma_f32_16x16x32_bf16 v[28:31], a[68:71], v[88:91], v[28:31]\n"
"buffer_load_dwordx4 v[48:51], v0, s[4:7], s98 offen offset:3072 sc1\n"
"v_mfma_f32_16x16x32_bf16 v[28:31], a[72:75], v[200:203], v[28:31]\n"
"s_mov_b32 s26, 0x7ff0007\n"
"s_mov_b32 s27, 0xffffffff\n"
"s_mov_b32 s28, 0xfff000f\n"
"v_mfma_f32_16x16x32_bf16 v[28:31], a[76:79], v[196:199], v[28:31]\n"
"s_mov_b32 s29, 0xffffffff\n"
"s_mov_b32 s30, 0x1fff001f\n"
"s_mov_b32 s31, 0xffffffff\n"
"v_mfma_f32_16x16x32_bf16 v[28:31], a[80:83], v[52:55], v[28:31]\n"
"s_mov_b32 s32, 0x3fff003f\n"
"s_mov_b32 s33, 0xffffffff\n"
"s_mov_b32 s34, 0x7fff007f\n"
"v_mfma_f32_16x16x32_bf16 v[28:31], a[84:87], v[80:83], v[28:31]\n"
"ds_write_b128 v3, v[40:43] offset:7168\n"
"v_mfma_f32_16x16x32_bf16 v[28:31], a[88:91], v[140:143], v[28:31]\n"
"buffer_load_dwordx4 v[40:43], v1, s[4:7], s98 offen sc1\n"
"v_mfma_f32_16x16x32_bf16 v[28:31], a[92:95], v[188:191], v[28:31]\n"
"s_mov_b32 s35, 0xffffffff\n"
"s_mov_b32 s36, 0\n"
"s_mov_b32 s37, 0xff0000\n"
"v_mfma_f32_16x16x32_bf16 v[56:59], a[124:127], v[188:191], 0\n"
"s_mov_b32 s38, 0\n"
"s_mov_b32 s39, 0x1ff0001\n"
"s_mov_b32 s40, 0\n"
"v_mfma_f32_16x16x32_bf16 v[56:59], a[120:123], v[140:143], v[56:59]\n"
"s_mov_b32 s41, 0x3ff0003\n"
"s_mov_b32 s42, 0\n"
"s_mov_b32 s43, 0x7ff0007\n"
"v_mfma_f32_16x16x32_bf16 v[56:59], a[116:119], v[80:83], v[56:59]\n"
"s_mov_b32 s44, 0\n"
"s_mov_b32 s45, 0xfff000f\n"
"s_mov_b32 s46, 0\n"
"v_mfma_f32_16x16x32_bf16 v[56:59], a[112:115], v[52:55], v[56:59]\n"
"buffer_load_dwordx4 v[92:95], v1, s[4:7], s98 offen offset:1024 sc1\n"
"v_mfma_f32_16x16x32_bf16 v[56:59], a[108:111], v[196:199], v[56:59]\n"
"s_mov_b32 s47, 0x1fff001f\n"
"s_mov_b32 s48, 0\n"
"s_mov_b32 s49, 0x3fff003f\n"
"v_mfma_f32_16x16x32_bf16 v[56:59], a[104:107], v[200:203], v[56:59]\n"
"ds_write_b128 v3, v[28:31] offset:8192\n"
"v_mfma_f32_16x16x32_bf16 v[56:59], a[100:103], v[88:91], v[56:59]\n"
"s_mov_b32 s50, 0\n"
"s_mov_b32 s51, 0x7fff007f\n"
"v_mfma_f32_16x16x32_bf16 v[56:59], a[96:99], v[84:87], v[56:59]\n"
"v_mfma_f32_16x16x32_bf16 v[28:31], v[100:103], v[84:87], 0\n"
"buffer_load_dwordx4 v[168:171], v1, s[4:7], s98 offen offset:2048 sc1\n"
"v_mfma_f32_16x16x32_bf16 v[28:31], v[104:107], v[88:91], v[28:31]\n"
"buffer_load_dwordx4 a[216:219], v1, s[4:7], s97 offen offset:2048 sc1\n"
"v_mfma_f32_16x16x32_bf16 v[28:31], v[108:111], v[200:203], v[28:31]\n"
"v_mfma_f32_16x16x32_bf16 v[28:31], v[116:119], v[196:199], v[28:31]\n"
"v_mfma_f32_16x16x32_bf16 v[28:31], v[128:131], v[52:55], v[28:31]\n"
"s_waitcnt vmcnt(12)\n"
"v_add_f32_e32 v255, v255, v254\n"
"v_add_f32_e32 v253, v253, v250\n"
"v_mfma_f32_16x16x32_bf16 v[28:31], v[132:135], v[80:83], v[28:31]\n"
"buffer_load_dwordx4 v[176:179], v1, s[4:7], s98 offen offset:3072 sc1\n"
"v_mfma_f32_16x16x32_bf16 v[28:31], v[136:139], v[140:143], v[28:31]\n"
"ds_write_b128 v3, v[56:59] offset:9216\n"
"v_mfma_f32_16x16x32_bf16 v[28:31], v[96:99], v[188:191], v[28:31]\n"
"s_add_u32 s98, s75, 0x1000\n"
"buffer_load_dwordx2 v[246:247], v13, s[4:7], s98 offen sc1\n"
"v_mfma_f32_16x16x32_bf16 v[56:59], v[112:115], v[188:191], 0\n"
"v_add_f32_e32 v252, v252, v249\n"
"v_add_f32_e32 v251, v251, v248\n"
"v_mfma_f32_16x16x32_bf16 v[56:59], v[164:167], v[140:143], v[56:59]\n"
"v_add_f32_e32 v255, v255, v252\n"
"v_add_f32_e32 v253, v253, v251\n"
"v_mfma_f32_16x16x32_bf16 v[56:59], v[160:163], v[80:83], v[56:59]\n"
"s_add_u32 s96, s71, 0\n"
"buffer_load_dwordx2 v[250:251], off, s[4:7], s96 sc1\n"
"v_mfma_f32_16x16x32_bf16 v[56:59], v[156:159], v[52:55], v[56:59]\n"
"v_add_f32_e32 v255, v255, v253\n"
"v_mov_b32_e32 v253, v255\n"
"v_mfma_f32_16x16x32_bf16 v[56:59], v[124:127], v[196:199], v[56:59]\n"
"s_nop 0\n"
"v_permlane32_swap_b32_e32 v255, v253\n"
"v_add_f32_e32 v255, v255, v253\n"
"v_mfma_f32_16x16x32_bf16 v[56:59], v[148:151], v[200:203], v[56:59]\n"
"ds_write_b128 v3, v[28:31] offset:10240\n"
"v_mfma_f32_16x16x32_bf16 v[56:59], v[144:147], v[88:91], v[56:59]\n"
"buffer_store_dwordx2 v[18:19], v11, s[4:7], s68 offen offset:32 sc1\n"
"v_mul_f32_e32 v255, 0x3a800000, v255\n"
"v_mov_b32_e32 v253, v255\n"
"v_mfma_f32_16x16x32_bf16 v[56:59], v[120:123], v[84:87], v[56:59]\n"
"s_nop 0\n"
"v_permlane16_swap_b32_e32 v255, v253\n"
"v_cvt_pk_bf16_f32 v254, -v255, 0\n"
"; @A G1 3.2\n"
"v_mfma_f32_16x16x32_bf16 v[28:31], a[0:3], v[60:63], 0\n"
"v_fma_f32 v253, -v255, v255, v253\n"
"v_lshlrev_b32_e32 v254, 16, v254\n"
"v_mfma_f32_16x16x32_bf16 v[28:31], a[4:7], v[64:67], v[28:31]\n"
"v_max_f32_e32 v253, 0, v253\n"
"v_cvt_pk_bf16_f32 v253, v253, 0\n"
"v_mfma_f32_16x16x32_bf16 v[28:31], a[8:11], v[72:75], v[28:31]\n"
"buffer_load_dwordx4 a[220:223], v1, s[4:7], s97 offen offset:3072 sc1\n"
"v_mfma_f32_16x16x32_bf16 v[28:31], a[12:15], v[76:79], v[28:31]\n"
"s_waitcnt vmcnt(16)\n"
"v_lshlrev_b32_e32 v253, 16, v253\n"
"v_add_f32_e32 v253, 0x3727c5ac, v253\n"
"v_mfma_f32_16x16x32_bf16 v[28:31], a[16:19], v[68:71], v[28:31]\n"
"s_add_u32 s97, s70, 0x6000\n"
"v_cvt_pk_bf16_f32 v253, v253, 0\n"
"v_lshlrev_b32_e32 v253, 16, v253\n"
"v_mfma_f32_16x16x32_bf16 v[28:31], a[20:23], v[44:47], v[28:31]\n"
"ds_write_b128 v3, v[56:59] offset:11264\n"
"v_mfma_f32_16x16x32_bf16 v[28:31], a[24:27], v[152:155], v[28:31]\n"
"buffer_load_dwordx4 a[224:227], v0, s[4:7], s97 offen sc1\n"
"v_mfma_f32_16x16x32_bf16 v[28:31], a[28:31], v[172:175], v[28:31]\n"
"v_sqrt_f32_e32 v253, v253\n"
"v_mfma_f32_16x16x32_bf16 v[52:55], a[60:63], v[172:175], 0\n"
"v_cvt_pk_bf16_f32 v253, v253, 0\n"
"v_lshlrev_b32_e32 v253, 16, v253\n"
"v_mfma_f32_16x16x32_bf16 v[52:55], a[56:59], v[152:155], v[52:55]\n"
"v_rcp_f32_e32 v255, v253\n"
"v_mfma_f32_16x16x32_bf16 v[52:55], a[52:55], v[44:47], v[52:55]\n"
"s_waitcnt lgkmcnt(0)\n"
"s_barrier\n"
"v_mfma_f32_16x16x32_bf16 v[52:55], a[48:51], v[68:71], v[52:55]\n"
"buffer_load_dwordx4 a[228:231], v0, s[4:7], s97 offen offset:1024 sc1\n"
"v_mfma_f32_16x16x32_bf16 v[52:55], a[44:47], v[76:79], v[52:55]\n"
"ds_read_b128 v[56:59], v4\n"
"v_mul_f32_e32 v253, 0x3e8293ee, v255\n"
"v_mfma_f32_16x16x32_bf16 v[52:55], a[40:43], v[72:75], v[52:55]\n"
"ds_write_b128 v3, v[28:31] offset:12288\n"
"v_mfma_f32_16x16x32_bf16 v[52:55], a[36:39], v[64:67], v[52:55]\n"
"ds_read_b128 v[28:31], v9 offset:128\n"
"ds_read_b128 v[80:83], v5\n"
"v_mfma_f32_16x16x32_bf16 v[52:55], a[32:35], v[60:63], v[52:55]\n"
"buffer_load_dwordx4 a[232:235], v0, s[4:7], s97 offen offset:2048 sc1\n"
"v_mfma_f32_16x16x32_bf16 v[84:87], a[64:67], v[60:63], 0\n"
"v_mfma_f32_16x16x32_bf16 v[84:87], a[68:71], v[64:67], v[84:87]\n"
"v_mfma_f32_16x16x32_bf16 v[84:87], a[72:75], v[72:75], v[84:87]\n"
"ds_read_b128 v[88:91], v4 offset:1024\n"
"ds_read_b128 v[140:143], v9 offset:192\n"
"v_mfma_f32_16x16x32_bf16 v[84:87], a[76:79], v[76:79], v[84:87]\n"
"buffer_load_dwordx4 a[236:239], v0, s[4:7], s97 offen offset:3072 sc1\n"
"v_mfma_f32_16x16x32_bf16 v[84:87], a[80:83], v[68:71], v[84:87]\n"
"ds_read_b128 v[180:183], v5 offset:1024\n"
"s_waitcnt lgkmcnt(3)\n"
"v_mfma_f32_16x16x32_bf16 v[84:87], a[84:87], v[44:47], v[84:87]\n"
"ds_write_b128 v3, v[52:55] offset:13312\n"
"v_mfma_f32_16x16x32_bf16 v[84:87], a[88:91], v[152:155], v[84:87]\n"
"v_fmac_f32_e32 v56, v254, v28\n"
"v_fmac_f32_e32 v57, v254, v29\n"
"v_mfma_f32_16x16x32_bf16 v[84:87], a[92:95], v[172:175], v[84:87]\n"
"buffer_load_dwordx4 a[240:243], v1, s[4:7], s97 offen sc1\n"
"v_mfma_f32_16x16x32_bf16 v[52:55], a[124:127], v[172:175], 0\n"
"v_fmac_f32_e32 v58, v254, v30\n"
"v_fmac_f32_e32 v59, v254, v31\n"
"v_mfma_f32_16x16x32_bf16 v[52:55], a[120:123], v[152:155], v[52:55]\n"
"ds_read_b128 v[28:31], v4 offset:2048\n"
"ds_read_b128 v[184:187], v9 offset:256\n"
"v_mfma_f32_16x16x32_bf16 v[52:55], a[116:119], v[44:47], v[52:55]\n"
"ds_read_b128 v[188:191], v5 offset:2048\n"
"v_add_f32_e32 v56, v56, v80\n"
"v_mfma_f32_16x16x32_bf16 v[52:55], a[112:115], v[68:71], v[52:55]\n"
"buffer_load_dwordx4 a[244:247], v1, s[4:7], s97 offen offset:1024 sc1\n"
"v_mfma_f32_16x16x32_bf16 v[52:55], a[108:111], v[76:79], v[52:55]\n"
"v_add_f32_e32 v57, v57, v81\n"
"v_add_f32_e32 v58, v58, v82\n"
"v_mfma_f32_16x16x32_bf16 v[52:55], a[104:107], v[72:75], v[52:55]\n"
"ds_write_b128 v3, v[84:87] offset:14336\n"
"v_mfma_f32_16x16x32_bf16 v[52:55], a[100:103], v[64:67], v[52:55]\n"
"s_waitcnt lgkmcnt(5)\n"
"v_add_f32_e32 v59, v59, v83\n"
"v_fmac_f32_e32 v88, v254, v140\n"
"v_mfma_f32_16x16x32_bf16 v[52:55], a[96:99], v[60:63], v[52:55]\n"
"buffer_load_dwordx4 a[248:251], v1, s[4:7], s97 offen offset:2048 sc1\n"
"v_mfma_f32_16x16x32_bf16 v[80:83], v[100:103], v[60:63], 0\n"
"ds_read_b128 v[84:87], v4 offset:3072\n"
"ds_read_b128 v[192:195], v9 offset:320\n"
"v_mfma_f32_16x16x32_bf16 v[80:83], v[104:107], v[64:67], v[80:83]\n"
"ds_read_b128 v[196:199], v5 offset:3072\n"
"v_fmac_f32_e32 v89, v254, v141\n"
"v_mfma_f32_16x16x32_bf16 v[80:83], v[108:111], v[72:75], v[80:83]\n"
"v_fmac_f32_e32 v90, v254, v142\n"
"v_fmac_f32_e32 v91, v254, v143\n"
"v_mfma_f32_16x16x32_bf16 v[80:83], v[116:119], v[76:79], v[80:83]\n"
"buffer_load_dwordx4 a[252:255], v1, s[4:7], s97 offen offset:3072 sc1\n"
"v_mfma_f32_16x16x32_bf16 v[80:83], v[128:131], v[68:71], v[80:83]\n"
"s_waitcnt vmcnt(11)\n"
"v_cmp_ne_u64_e32 vcc, v[246:247], v[18:19]\n"
"v_mfma_f32_16x16x32_bf16 v[80:83], v[132:135], v[44:47], v[80:83]\n"
"ds_write_b128 v3, v[52:55] offset:15360\n"
"v_mfma_f32_16x16x32_bf16 v[80:83], v[136:139], v[152:155], v[80:83]\n"
"s_cbranch_vccnz .Lmk_f7s\n"
".Lmk_f7r:\n"
"v_add_f32_e32 v88, v88, v180\n"
"v_mfma_f32_16x16x32_bf16 v[80:83], v[96:99], v[172:175], v[80:83]\n"
"ds_read_b128 v[52:55], v4 offset:4096\n"
"ds_read_b128 v[140:143], v9\n"
"v_mfma_f32_16x16x32_bf16 v[200:203], v[112:115], v[172:175], 0\n"
"ds_read_b128 v[172:175], v5 offset:4096\n"
"v_add_f32_e32 v89, v89, v181\n"
"v_mfma_f32_16x16x32_bf16 v[200:203], v[164:167], v[152:155], v[200:203]\n"
"s_waitcnt lgkmcnt(8)\n"
"v_add_f32_e32 v90, v90, v182\n"
"v_add_f32_e32 v91, v91, v183\n"
"v_mfma_f32_16x16x32_bf16 v[200:203], v[160:163], v[44:47], v[200:203]\n"
"v_fmac_f32_e32 v28, v254, v184\n"
"v_fmac_f32_e32 v29, v254, v185\n"
"v_mfma_f32_16x16x32_bf16 v[200:203], v[156:159], v[68:71], v[200:203]\n"
"v_fmac_f32_e32 v30, v254, v186\n"
"v_fmac_f32_e32 v31, v254, v187\n"
"v_mfma_f32_16x16x32_bf16 v[200:203], v[124:127], v[76:79], v[200:203]\n"
"s_waitcnt vmcnt(10)\n"
"v_cmp_ne_u64_e32 vcc, v[250:251], v[18:19]\n"
"v_mfma_f32_16x16x32_bf16 v[200:203], v[148:151], v[72:75], v[200:203]\n"
"ds_write_b128 v3, v[80:83] offset:16384\n"
"v_mfma_f32_16x16x32_bf16 v[200:203], v[144:147], v[64:67], v[200:203]\n"
"s_cbranch_vccnz .Lmk_f4s\n"
".Lmk_f4r:\n"
"s_add_u32 m0, s77, 0x6000\n"
"v_mfma_f32_16x16x32_bf16 v[200:203], v[120:123], v[60:63], v[200:203]\n"
"s_add_u32 s98, s65, 0x2000\n"
"ds_read_b128 v[44:47], v4 offset:5120\n"
"; @A G1 3.3\n"
"v_mfma_f32_16x16x32_bf16 v[56:59], a[0:3], v[32:35], v[56:59]\n"
"buffer_load_dwordx4 v2, s[4:7], s98 offen sc1 lds\n"
"v_mfma_f32_16x16x32_bf16 v[56:59], a[4:7], v[36:39], v[56:59]\n"
"buffer_load_dwordx4 v2, s[4:7], s98 offen offset:1024 sc1 lds\n"
"v_mfma_f32_16x16x32_bf16 v[56:59], a[8:11], v[24:27], v[56:59]\n"
"ds_read_b128 v[60:63], v9 offset:64\n"
"ds_read_b128 v[64:67], v5 offset:5120\n"
"v_mfma_f32_16x16x32_bf16 v[56:59], a[12:15], v[48:51], v[56:59]\n"
"v_add_f32_e32 v28, v28, v188\n"
"v_add_f32_e32 v29, v29, v189\n"
"v_mfma_f32_16x16x32_bf16 v[56:59], a[16:19], v[40:43], v[56:59]\n"
"s_waitcnt lgkmcnt(8)\n"
"v_add_f32_e32 v30, v30, v190\n"
"v_add_f32_e32 v31, v31, v191\n"
"v_mfma_f32_16x16x32_bf16 v[56:59], a[20:23], v[92:95], v[56:59]\n"
"ds_write_b128 v3, v[200:203] offset:17408\n"
"v_mfma_f32_16x16x32_bf16 v[56:59], a[24:27], v[168:171], v[56:59]\n"
"v_fmac_f32_e32 v84, v254, v192\n"
"v_fmac_f32_e32 v85, v254, v193\n"
"v_mfma_f32_16x16x32_bf16 v[56:59], a[28:31], v[176:179], v[56:59]\n"
"s_add_u32 m0, s77, 0xe000\n"
"s_add_u32 s98, s65, 0x6000\n"
"v_fmac_f32_e32 v86, v254, v194\n"
"v_mfma_f32_16x16x32_bf16 v[88:91], a[60:63], v[176:179], v[88:91]\n"
"buffer_load_dwordx4 v2, s[4:7], s98 offen sc1 lds\n"
"v_mfma_f32_16x16x32_bf16 v[88:91], a[56:59], v[168:171], v[88:91]\n"
"buffer_load_dwordx4 v2, s[4:7], s98 offen offset:1024 sc1 lds\n"
"v_mfma_f32_16x16x32_bf16 v[88:91], a[52:55], v[92:95], v[88:91]\n"
"s_waitcnt lgkmcnt(0)\n"
"s_barrier\n"
"v_mfma_f32_16x16x32_bf16 v[88:91], a[48:51], v[40:43], v[88:91]\n"
"ds_read_b128 v[68:71], v6\n"
"ds_read_b128 v[72:75], v6 offset:1024\n"
"v_mfma_f32_16x16x32_bf16 v[88:91], a[44:47], v[48:51], v[88:91]\n"
"s_add_u32 s98, s69, 0x600000\n"
"buffer_load_dwordx4 a[0:3], v0, s[4:7], s98 offen sc1\n"
"v_mfma_f32_16x16x32_bf16 v[88:91], a[40:43], v[24:27], v[88:91]\n"
"v_fmac_f32_e32 v87, v254, v195\n"
"v_add_f32_e32 v84, v84, v196\n"
"v_mfma_f32_16x16x32_bf16 v[88:91], a[36:39], v[36:39], v[88:91]\n"
"v_add_f32_e32 v85, v85, v197\n"
"v_add_f32_e32 v86, v86, v198\n"
"v_mfma_f32_16x16x32_bf16 v[88:91], a[32:35], v[32:35], v[88:91]\n"
"buffer_load_dwordx4 a[4:7], v0, s[4:7], s98 offen offset:1024 sc1\n"
"v_mfma_f32_16x16x32_bf16 v[28:31], a[64:67], v[32:35], v[28:31]\n"
"v_add_f32_e32 v87, v87, v199\n"
"v_fmac_f32_e32 v52, v254, v140\n"
"v_mfma_f32_16x16x32_bf16 v[28:31], a[68:71], v[36:39], v[28:31]\n"
"v_fmac_f32_e32 v53, v254, v141\n"
"v_fmac_f32_e32 v54, v254, v142\n"
"v_mfma_f32_16x16x32_bf16 v[28:31], a[72:75], v[24:27], v[28:31]\n"
"buffer_load_dwordx4 a[8:11], v0, s[4:7], s98 offen offset:2048 sc1\n"
"v_mfma_f32_16x16x32_bf16 v[28:31], a[76:79], v[48:51], v[28:31]\n"
"v_fmac_f32_e32 v55, v254, v143\n"
"v_add_f32_e32 v52, v52, v172\n"
"v_mfma_f32_16x16x32_bf16 v[28:31], a[80:83], v[40:43], v[28:31]\n"
"s_waitcnt lgkmcnt(0)\n"
"v_add_f32_e32 v53, v53, v173\n"
"v_add_f32_e32 v54, v54, v174\n"
"v_mfma_f32_16x16x32_bf16 v[28:31], a[84:87], v[92:95], v[28:31]\n"
"buffer_load_dwordx4 a[12:15], v0, s[4:7], s98 offen offset:3072 sc1\n"
"v_mfma_f32_16x16x32_bf16 v[28:31], a[88:91], v[168:171], v[28:31]\n"
"ds_read_b128 v[76:79], v6 offset:2048\n"
"ds_read_b128 v[80:83], v6 offset:3072\n"
"v_mfma_f32_16x16x32_bf16 v[28:31], a[92:95], v[176:179], v[28:31]\n"
"v_add_f32_e32 v55, v55, v175\n"
"v_fmac_f32_e32 v44, v254, v60\n"
"v_mfma_f32_16x16x32_bf16 v[84:87], a[124:127], v[176:179], v[84:87]\n"
"buffer_load_dwordx4 a[16:19], v1, s[4:7], s98 offen sc1\n"
"v_mfma_f32_16x16x32_bf16 v[84:87], a[120:123], v[168:171], v[84:87]\n"
"v_fmac_f32_e32 v45, v254, v61\n"
"v_fmac_f32_e32 v46, v254, v62\n"
"v_mfma_f32_16x16x32_bf16 v[84:87], a[116:119], v[92:95], v[84:87]\n"
"v_fmac_f32_e32 v47, v254, v63\n"
"v_add_f32_e32 v44, v44, v64\n"
"v_mfma_f32_16x16x32_bf16 v[84:87], a[112:115], v[40:43], v[84:87]\n"
"buffer_load_dwordx4 a[20:23], v1, s[4:7], s98 offen offset:1024 sc1\n"
"v_mfma_f32_16x16x32_bf16 v[84:87], a[108:111], v[48:51], v[84:87]\n"
"v_add_f32_e32 v45, v45, v65\n"
"v_add_f32_e32 v46, v46, v66\n"
"v_mfma_f32_16x16x32_bf16 v[84:87], a[104:107], v[24:27], v[84:87]\n"
"v_add_f32_e32 v47, v47, v67\n"
"v_add_f32_e32 v56, v56, v68\n"
"v_mfma_f32_16x16x32_bf16 v[84:87], a[100:103], v[36:39], v[84:87]\n"
"buffer_load_dwordx4 a[24:27], v1, s[4:7], s98 offen offset:2048 sc1\n"
"v_mfma_f32_16x16x32_bf16 v[84:87], a[96:99], v[32:35], v[84:87]\n"
"v_add_f32_e32 v57, v57, v69\n"
"v_add_f32_e32 v58, v58, v70\n"
"v_mfma_f32_16x16x32_bf16 v[52:55], v[100:103], v[32:35], v[52:55]\n"
"v_add_f32_e32 v59, v59, v71\n"
"v_add_f32_e32 v88, v88, v72\n"
"v_mfma_f32_16x16x32_bf16 v[52:55], v[104:107], v[36:39], v[52:55]\n"
"buffer_load_dwordx4 a[28:31], v1, s[4:7], s98 offen offset:3072 sc1\n"
"v_mfma_f32_16x16x32_bf16 v[52:55], v[108:111], v[24:27], v[52:55]\n"
"v_add_f32_e32 v89, v89, v73\n"
"v_add_f32_e32 v90, v90, v74\n"
"v_mfma_f32_16x16x32_bf16 v[52:55], v[116:119], v[48:51], v[52:55]\n"
"v_add_f32_e32 v91, v91, v75\n"
"v_mul_f32_e32 v56, v56, v255\n"
"v_mfma_f32_16x16x32_bf16 v[52:55], v[128:131], v[40:43], v[52:55]\n"
"s_waitcnt lgkmcnt(0)\n"
"v_mul_f32_e32 v57, v57, v255\n"
"v_mul_f32_e32 v58, v58, v255\n"
"v_mfma_f32_16x16x32_bf16 v[52:55], v[132:135], v[92:95], v[52:55]\n"
"v_mul_f32_e32 v59, v59, v255\n"
"v_mul_f32_e32 v88, v88, v255\n"
"v_mfma_f32_16x16x32_bf16 v[52:55], v[136:139], v[168:171], v[52:55]\n"
"ds_read_b128 v[60:63], v6 offset:4096\n"
"ds_read_b128 v[64:67], v6 offset:5120\n"
"v_mfma_f32_16x16x32_bf16 v[52:55], v[96:99], v[176:179], v[52:55]\n"
"v_mul_f32_e32 v89, v89, v255\n"
"v_mul_f32_e32 v90, v90, v255\n"
"v_mfma_f32_16x16x32_bf16 v[44:47], v[112:115], v[176:179], v[44:47]\n"
"v_mul_f32_e32 v91, v91, v255\n"
"v_cvt_pk_bf16_f32 v68, v56, v57\n"
"v_mfma_f32_16x16x32_bf16 v[44:47], v[164:167], v[168:171], v[44:47]\n"
"v_cvt_pk_bf16_f32 v69, v58, v59\n"
"v_cvt_pk_bf16_f32 v70, v88, v89\n"
"v_mfma_f32_16x16x32_bf16 v[44:47], v[160:163], v[92:95], v[44:47]\n"
"v_cvt_pk_bf16_f32 v71, v90, v91\n"
"v_add_f32_e32 v28, v28, v76\n"
"v_mfma_f32_16x16x32_bf16 v[44:47], v[156:159], v[40:43], v[44:47]\n"
"ds_write_b128 v7, v[68:71] offset:16384\n"
"v_mfma_f32_16x16x32_bf16 v[44:47], v[124:127], v[48:51], v[44:47]\n"
"buffer_store_dwordx4 v[68:71], v7, s[4:7], s64 offen sc1\n"
"v_mfma_f32_16x16x32_bf16 v[44:47], v[148:151], v[24:27], v[44:47]\n"
"v_add_f32_e32 v29, v29, v77\n"
"v_add_f32_e32 v30, v30, v78\n"
"v_mfma_f32_16x16x32_bf16 v[44:47], v[144:147], v[36:39], v[44:47]\n"
"v_add_f32_e32 v31, v31, v79\n"
"v_add_f32_e32 v84, v84, v80\n"
"v_mfma_f32_16x16x32_bf16 v[44:47], v[120:123], v[32:35], v[44:47]\n"
"v_add_f32_e32 v85, v85, v81\n"
"v_add_f32_e32 v86, v86, v82\n"
"v_add_f32_e32 v87, v87, v83\n"
"v_mul_f32_e32 v28, v28, v255\n"
"v_mul_f32_e32 v29, v29, v255\n"
"v_mul_f32_e32 v30, v30, v255\n"
"v_mul_f32_e32 v31, v31, v255\n"
"v_mul_f32_e32 v84, v84, v255\n"
"v_mul_f32_e32 v85, v85, v255\n"
"v_mul_f32_e32 v86, v86, v255\n"
"v_mul_f32_e32 v87, v87, v255\n"
"v_cvt_pk_bf16_f32 v24, v28, v29\n"
"v_cvt_pk_bf16_f32 v25, v30, v31\n"
"v_cvt_pk_bf16_f32 v26, v84, v85\n"
"v_cvt_pk_bf16_f32 v27, v86, v87\n"
"v_xor_b32_e32 v255, 32, v16\n"
"v_xor_b32_e32 v254, 32, v15\n"
"v_mov_b32_e32 v88, 0x3f803f80\n"
"v_mov_b32_e32 v89, 0x3f803f80\n"
"v_mov_b32_e32 v90, 0x3f803f80\n"
"v_mov_b32_e32 v91, 0x3f803f80\n"
"v_mov_b32_e32 v252, 0x53800000\n"
"ds_write_b128 v7, v[24:27] offset:49152\n"
".Lmk_z4:\n"
"; @A tail\n"
"s_add_u32 s98, s64, 0x4000\n"
"buffer_store_dwordx4 v[24:27], v7, s[4:7], s98 offen sc1\n"
"s_waitcnt lgkmcnt(2)\n"
"v_add_f32_e32 v52, v52, v60\n"
"v_add_f32_e32 v53, v53, v61\n"
"v_add_f32_e32 v54, v54, v62\n"
"v_add_f32_e32 v55, v55, v63\n"
"v_add_f32_e32 v44, v44, v64\n"
"v_add_f32_e32 v45, v45, v65\n"
"v_add_f32_e32 v46, v46, v66\n"
"v_add_f32_e32 v47, v47, v67\n"
"v_mul_f32_e32 v52, v52, v253\n"
"v_mul_f32_e32 v53, v53, v253\n"
"v_mul_f32_e32 v54, v54, v253\n"
"v_mul_f32_e32 v55, v55, v253\n"
"v_mul_f32_e32 v44, v44, v253\n"
"v_mul_f32_e32 v45, v45, v253\n"
"v_mul_f32_e32 v46, v46, v253\n"
"v_mul_f32_e32 v47, v47, v253\n"
"v_cvt_pk_bf16_f32 v24, v52, v53\n"
"v_cvt_pk_bf16_f32 v25, v54, v55\n"
"v_cvt_pk_bf16_f32 v26, v44, v45\n"
"v_cvt_pk_bf16_f32 v27, v46, v47\n"
"ds_write_b128 v2, v[24:27] offset:16384\n"
"s_waitcnt lgkmcnt(0)\n"
"s_barrier\n"
"ds_read_b128 v[28:31], v2 offset:49152\n"
"ds_read_b128 v[32:35], v2 offset:50176\n"
"ds_read_b128 v[92:95], v2 offset:16384\n"
"ds_read_b128 v[96:99], v2 offset:17408\n"
"; @A AT b\n"
"s_mov_b32 s2, 0\n"
"v_mov_b32_e32 v253, 0xff800000\n"
"v_mov_b32_e32 v251, 0xff800000\n"
"v_mov_b32_e32 v250, 0x7f800000\n"
"v_mov_b32_e32 v249, 0x7f800000\n"
".Lmk_a15:\n"
"ds_read_b128 v[24:27], v15 offset:24576\n"
"ds_read_b128 v[36:39], v254 offset:24832\n"
"ds_read_b128 v[40:43], v15 offset:22528\n"
"ds_read_b128 v[44:47], v254 offset:22784\n"
"s_cmp_gt_i32 s78, 11\n"
"s_cselect_b32 s98, 0x80000000, 0\n"
"s_or_b32 s98, s98, 0x7f800000\n"
"s_waitcnt lgkmcnt(2)\n"
"v_mfma_f32_16x16x32_bf16 v[48:51], v[24:27], v[28:31], 0\n"
"v_mfma_f32_16x16x32_bf16 v[52:55], v[24:27], v[32:35], 0\n"
"v_mfma_f32_16x16x32_bf16 v[56:59], v[36:39], v[32:35], 0\n"
"v_mfma_f32_16x16x32_bf16 v[60:63], v[36:39], v[28:31], 0\n"
"s_waitcnt lgkmcnt(0)\n"
"s_nop 3\n"
"v_max_f32_e32 v248, v48, v49\n"
"v_max_f32_e32 v247, v52, v53\n"
"v_max3_f32 v248, v248, v50, v51\n"
"v_max3_f32 v247, v247, v54, v55\n"
"v_max3_f32 v248, v248, v60, v61\n"
"v_max3_f32 v247, v247, v56, v57\n"
"v_max3_f32 v248, v248, v62, v63\n"
"v_max3_f32 v247, v247, v58, v59\n"
"v_mov_b32_e32 v246, v248\n"
"v_mov_b32_e32 v245, v247\n"
"s_nop 0\n"
"v_permlane16_swap_b32_e32 v248, v246\n"
"v_permlane16_swap_b32_e32 v247, v245\n"
"v_max_f32_e32 v248, v248, v246\n"
"v_max_f32_e32 v247, v247, v245\n"
"v_mov_b32_e32 v246, v248\n"
"v_mov_b32_e32 v245, v247\n"
"s_nop 0\n"
"v_permlane32_swap_b32_e32 v248, v246\n"
"v_permlane32_swap_b32_e32 v247, v245\n"
"v_max_f32_e32 v248, v248, v246\n"
"v_max_f32_e32 v247, v247, v245\n"
"v_med3_f32 v248, v248, v253, v250\n"
"v_med3_f32 v247, v247, v251, v249\n"
"v_min_f32_e64 v24, s98, -v248\n"
"v_min_f32_e64 v25, s98, -v248\n"
"v_min_f32_e64 v26, s98, -v248\n"
"v_min_f32_e64 v27, s98, -v248\n"
"v_min_f32_e64 v36, s98, -v247\n"
"v_min_f32_e64 v37, s98, -v247\n"
"v_min_f32_e64 v38, s98, -v247\n"
"v_min_f32_e64 v39, s98, -v247\n"
"v_mfma_f32_16x16x32_bf16 v[64:67], v[40:43], v[28:31], v[24:27]\n"
"ds_read_b128 v[68:71], v15 offset:20480\n"
"s_add_u32 s98, s69, 0x602000\n"
"buffer_load_dwordx4 a[32:35], v0, s[4:7], s98 offen sc1\n"
"v_sub_f32_e32 v48, v48, v248\n"
"v_sub_f32_e32 v49, v49, v248\n"
"v_sub_f32_e32 v50, v50, v248\n"
"v_mfma_f32_16x16x32_bf16 v[72:75], v[40:43], v[32:35], v[36:39]\n"
"buffer_load_dwordx4 a[36:39], v0, s[4:7], s98 offen offset:1024 sc1\n"
"v_sub_f32_e32 v51, v51, v248\n"
"v_sub_f32_e32 v60, v60, v248\n"
"v_sub_f32_e32 v61, v61, v248\n"
"v_sub_f32_e32 v62, v62, v248\n"
"v_sub_f32_e32 v63, v63, v248\n"
"v_mfma_f32_16x16x32_bf16 v[40:43], v[44:47], v[32:35], v[36:39]\n"
"ds_read_b128 v[76:79], v254 offset:20736\n"
"v_sub_f32_e32 v52, v52, v247\n"
"v_sub_f32_e32 v53, v53, v247\n"
"v_sub_f32_e32 v54, v54, v247\n"
"v_sub_f32_e32 v55, v55, v247\n"
"v_mfma_f32_16x16x32_bf16 v[80:83], v[44:47], v[28:31], v[24:27]\n"
"buffer_load_dwordx4 a[40:43], v0, s[4:7], s98 offen offset:2048 sc1\n"
"v_sub_f32_e32 v56, v56, v247\n"
"v_sub_f32_e32 v57, v57, v247\n"
"v_sub_f32_e32 v58, v58, v247\n"
"v_sub_f32_e32 v59, v59, v247\n"
"s_waitcnt lgkmcnt(0)\n"
"v_mfma_f32_16x16x32_bf16 v[44:47], v[68:71], v[28:31], v[24:27]\n"
"ds_read_b128 v[84:87], v15 offset:18432\n"
"buffer_load_dwordx4 a[44:47], v0, s[4:7], s98 offen offset:3072 sc1\n"
"ds_read_b128 v[100:103], v254 offset:18688\n"
"v_exp_f32_e32 v64, v64\n"
"v_exp_f32_e32 v65, v65\n"
"v_exp_f32_e32 v66, v66\n"
"v_exp_f32_e32 v67, v67\n"
"v_exp_f32_e32 v80, v80\n"
"v_exp_f32_e32 v81, v81\n"
"v_mfma_f32_16x16x32_bf16 v[104:107], v[68:71], v[32:35], v[36:39]\n"
"buffer_load_dwordx4 a[48:51], v1, s[4:7], s98 offen sc1\n"
"ds_read_b64_tr_b16 v[68:69], v16 offset:22528\n"
"buffer_load_dwordx4 a[52:55], v1, s[4:7], s98 offen offset:1024 sc1\n"
"ds_read_b64_tr_b16 v[70:71], v255 offset:22784\n"
"v_exp_f32_e32 v82, v82\n"
"v_exp_f32_e32 v83, v83\n"
"v_cvt_pk_bf16_f32 v108, v64, v65\n"
"v_cvt_pk_bf16_f32 v109, v66, v67\n"
"v_cvt_pk_bf16_f32 v110, v80, v81\n"
"v_cvt_pk_bf16_f32 v111, v82, v83\n"
"v_exp_f32_e32 v72, v72\n"
"v_mfma_f32_16x16x32_bf16 v[64:67], v[76:79], v[32:35], v[36:39]\n"
"s_add_u32 s97, s76, 0x80000\n"
"v_exp_f32_e32 v73, v73\n"
"v_exp_f32_e32 v74, v74\n"
"v_exp_f32_e32 v75, v75\n"
"v_bfe_u32 v248, v0, 2, 7\n"
"v_add_lshl_u32 v248, s72, v248, 1\n"
"v_and_b32_e32 v247, 0x200, v0\n"
"v_lshl_add_u32 v248, v247, 4, v248\n"
"v_mfma_f32_16x16x32_bf16 v[112:115], v[76:79], v[28:31], v[24:27]\n"
"buffer_load_dword v247, v248, s[4:7], s97 offen sc1\n"
"ds_read_b64_tr_b16 v[80:81], v255 offset:22528\n"
"buffer_load_dword v246, v248, s[4:7], s97 offen offset:1024 sc1\n"
"ds_read_b64_tr_b16 v[82:83], v16 offset:22784\n"
"v_exp_f32_e32 v40, v40\n"
"v_exp_f32_e32 v41, v41\n"
"v_exp_f32_e32 v42, v42\n"
"v_exp_f32_e32 v43, v43\n"
"v_cvt_pk_bf16_f32 v76, v72, v73\n"
"v_cvt_pk_bf16_f32 v77, v74, v75\n"
"v_cvt_pk_bf16_f32 v78, v40, v41\n"
"v_cvt_pk_bf16_f32 v79, v42, v43\n"
"s_waitcnt lgkmcnt(4)\n"
"v_mfma_f32_16x16x32_bf16 v[40:43], v[84:87], v[28:31], v[24:27]\n"
"ds_read_b128 v[72:75], v15 offset:16384\n"
"v_exp_f32_e32 v44, v44\n"
"v_exp_f32_e32 v45, v45\n"
"v_exp_f32_e32 v46, v46\n"
"v_mfma_f32_16x16x32_bf16 v[116:119], v[84:87], v[32:35], v[36:39]\n"
"buffer_load_dwordx4 a[56:59], v1, s[4:7], s98 offen offset:2048 sc1\n"
"ds_read_b128 v[84:87], v254 offset:16640\n"
"v_exp_f32_e32 v47, v47\n"
"v_exp_f32_e32 v112, v112\n"
"v_mfma_f32_16x16x32_bf16 v[120:123], v[100:103], v[32:35], v[36:39]\n"
"ds_read_b64_tr_b16 v[124:125], v16 offset:20480\n"
"v_exp_f32_e32 v113, v113\n"
"v_exp_f32_e32 v114, v114\n"
"v_mfma_f32_16x16x32_bf16 v[128:131], v[100:103], v[28:31], v[24:27]\n"
"buffer_load_dwordx4 a[60:63], v1, s[4:7], s98 offen offset:3072 sc1\n"
"v_exp_f32_e32 v115, v115\n"
"v_cvt_pk_bf16_f32 v100, v44, v45\n"
"v_cvt_pk_bf16_f32 v101, v46, v47\n"
"s_waitcnt lgkmcnt(3)\n"
"v_mfma_f32_16x16x32_bf16 v[44:47], v[88:91], v[108:111], 0\n"
"ds_read_b64_tr_b16 v[126:127], v255 offset:20736\n"
"s_add_u32 s98, s69, 0x604000\n"
"buffer_load_dwordx4 a[64:67], v0, s[4:7], s98 offen sc1\n"
"v_cvt_pk_bf16_f32 v102, v112, v113\n"
"v_cvt_pk_bf16_f32 v103, v114, v115\n"
"v_exp_f32_e32 v104, v104\n"
"v_mfma_f32_16x16x32_bf16 v[112:115], v[68:71], v[108:111], 0\n"
"ds_read_b64_tr_b16 v[132:133], v255 offset:20480\n"
"v_exp_f32_e32 v105, v105\n"
"v_exp_f32_e32 v106, v106\n"
"v_mfma_f32_16x16x32_bf16 v[136:139], v[80:83], v[108:111], 0\n"
"buffer_load_dword v245, v248, s[4:7], s97 offen offset:2048 sc1\n"
"v_exp_f32_e32 v107, v107\n"
"v_exp_f32_e32 v64, v64\n"
"v_mfma_f32_16x16x32_bf16 v[108:111], v[80:83], v[76:79], 0\n"
"ds_read_b64_tr_b16 v[134:135], v16 offset:20736\n"
"v_exp_f32_e32 v65, v65\n"
"v_exp_f32_e32 v66, v66\n"
"v_mfma_f32_16x16x32_bf16 v[80:83], v[68:71], v[76:79], 0\n"
"buffer_load_dword v244, v248, s[4:7], s97 offen offset:3072 sc1\n"
"s_waitcnt vmcnt(13)\n"
"buffer_store_dwordx2 v[18:19], v11, s[4:7], s68 offen offset:64 sc1\n"
"v_exp_f32_e32 v67, v67\n"
"v_mfma_f32_16x16x32_bf16 v[68:71], v[88:91], v[76:79], 0\n"
"s_cmp_lg_u32 s2, 0\n"
"v_cvt_pk_bf16_f32 v76, v104, v105\n"
"v_cvt_pk_bf16_f32 v77, v106, v107\n"
"v_cvt_pk_bf16_f32 v78, v64, v65\n"
"v_cvt_pk_bf16_f32 v79, v66, v67\n"
"s_cbranch_scc1 .Lmk_s17\n"
"s_barrier\n"
".Lmk_s17:\n"
"s_waitcnt lgkmcnt(4)\n"
"v_mfma_f32_16x16x32_bf16 v[64:67], v[72:75], v[28:31], v[24:27]\n"
"ds_read_b128 v[104:107], v15 offset:14336\n"
"v_exp_f32_e32 v40, v40\n"
"v_exp_f32_e32 v41, v41\n"
"v_exp_f32_e32 v42, v42\n"
"v_mfma_f32_16x16x32_bf16 v[140:143], v[72:75], v[32:35], v[36:39]\n"
"buffer_load_dwordx4 a[68:71], v0, s[4:7], s98 offen offset:1024 sc1\n"
"ds_read_b128 v[72:75], v254 offset:14592\n"
"v_exp_f32_e32 v43, v43\n"
"v_mfma_f32_16x16x32_bf16 v[144:147], v[84:87], v[32:35], v[36:39]\n"
"v_exp_f32_e32 v128, v128\n"
"v_exp_f32_e32 v129, v129\n"
"v_exp_f32_e32 v130, v130\n"
"v_mfma_f32_16x16x32_bf16 v[148:151], v[84:87], v[28:31], v[24:27]\n"
"buffer_load_dwordx4 a[72:75], v0, s[4:7], s98 offen offset:2048 sc1\n"
"ds_read_b64_tr_b16 v[84:85], v16 offset:18432\n"
"v_exp_f32_e32 v131, v131\n"
"s_waitcnt lgkmcnt(3)\n"
"v_mfma_f32_16x16x32_bf16 v[44:47], v[88:91], v[100:103], v[44:47]\n"
"buffer_load_dwordx4 a[76:79], v0, s[4:7], s98 offen offset:3072 sc1\n"
"v_cvt_pk_bf16_f32 v152, v40, v41\n"
"v_cvt_pk_bf16_f32 v153, v42, v43\n"
"v_cvt_pk_bf16_f32 v154, v128, v129\n"
"v_cvt_pk_bf16_f32 v155, v130, v131\n"
"v_mfma_f32_16x16x32_bf16 v[112:115], v[124:127], v[100:103], v[112:115]\n"
"ds_read_b64_tr_b16 v[86:87], v255 offset:18688\n"
"v_exp_f32_e32 v116, v116\n"
"v_exp_f32_e32 v117, v117\n"
"v_exp_f32_e32 v118, v118\n"
"v_mfma_f32_16x16x32_bf16 v[136:139], v[132:135], v[100:103], v[136:139]\n"
"s_add_u32 s97, s76, 0x81000\n"
"buffer_load_dword v243, v248, s[4:7], s97 offen sc1\n"
"v_exp_f32_e32 v119, v119\n"
"v_mfma_f32_16x16x32_bf16 v[108:111], v[132:135], v[76:79], v[108:111]\n"
"ds_read_b64_tr_b16 v[40:41], v255 offset:18432\n"
"v_exp_f32_e32 v120, v120\n"
"v_exp_f32_e32 v121, v121\n"
"v_mfma_f32_16x16x32_bf16 v[80:83], v[124:127], v[76:79], v[80:83]\n"
"buffer_load_dword v242, v248, s[4:7], s97 offen offset:1024 sc1\n"
"ds_read_b64_tr_b16 v[42:43], v16 offset:18688\n"
"v_exp_f32_e32 v122, v122\n"
"v_exp_f32_e32 v123, v123\n"
"v_mfma_f32_16x16x32_bf16 v[68:71], v[88:91], v[76:79], v[68:71]\n"
"s_waitcnt lgkmcnt(4)\n"
"v_cvt_pk_bf16_f32 v76, v116, v117\n"
"v_cvt_pk_bf16_f32 v77, v118, v119\n"
"v_cvt_pk_bf16_f32 v78, v120, v121\n"
"v_cvt_pk_bf16_f32 v79, v122, v123\n"
"s_cmp_eq_i32 s78, 8\n"
"s_cbranch_scc1 .Lmk_x18s\n"
".Lmk_x18r:\n"
"v_mfma_f32_16x16x32_bf16 v[100:103], v[104:107], v[28:31], v[24:27]\n"
"ds_read_b128 v[116:119], v15 offset:12288\n"
"v_exp_f32_e32 v64, v64\n"
"v_exp_f32_e32 v65, v65\n"
"v_exp_f32_e32 v66, v66\n"
"v_mfma_f32_16x16x32_bf16 v[120:123], v[104:107], v[32:35], v[36:39]\n"
"buffer_load_dwordx4 a[80:83], v1, s[4:7], s98 offen sc1\n"
"ds_read_b128 v[104:107], v254 offset:12544\n"
"v_exp_f32_e32 v67, v67\n"
"v_mfma_f32_16x16x32_bf16 v[124:127], v[72:75], v[32:35], v[36:39]\n"
"v_exp_f32_e32 v148, v148\n"
"v_exp_f32_e32 v149, v149\n"
"v_exp_f32_e32 v150, v150\n"
"v_mfma_f32_16x16x32_bf16 v[128:131], v[72:75], v[28:31], v[24:27]\n"
"buffer_load_dwordx4 a[84:87], v1, s[4:7], s98 offen offset:1024 sc1\n"
"ds_read_b64_tr_b16 v[72:73], v16 offset:16384\n"
"v_exp_f32_e32 v151, v151\n"
"s_waitcnt lgkmcnt(3)\n"
"v_mfma_f32_16x16x32_bf16 v[44:47], v[88:91], v[152:155], v[44:47]\n"
"buffer_load_dwordx4 a[88:91], v1, s[4:7], s98 offen offset:2048 sc1\n"
"v_cvt_pk_bf16_f32 v132, v64, v65\n"
"v_cvt_pk_bf16_f32 v133, v66, v67\n"
"v_cvt_pk_bf16_f32 v134, v148, v149\n"
"v_cvt_pk_bf16_f32 v135, v150, v151\n"
"v_mfma_f32_16x16x32_bf16 v[112:115], v[84:87], v[152:155], v[112:115]\n"
"ds_read_b64_tr_b16 v[74:75], v255 offset:16640\n"
"v_exp_f32_e32 v140, v140\n"
"v_exp_f32_e32 v141, v141\n"
"v_mfma_f32_16x16x32_bf16 v[136:139], v[40:43], v[152:155], v[136:139]\n"
"buffer_load_dword v241, v248, s[4:7], s97 offen offset:2048 sc1\n"
"v_exp_f32_e32 v142, v142\n"
"v_exp_f32_e32 v143, v143\n"
"v_mfma_f32_16x16x32_bf16 v[108:111], v[40:43], v[76:79], v[108:111]\n"
"ds_read_b64_tr_b16 v[40:41], v255 offset:16384\n"
"v_exp_f32_e32 v144, v144\n"
"v_exp_f32_e32 v145, v145\n"
"v_mfma_f32_16x16x32_bf16 v[80:83], v[84:87], v[76:79], v[80:83]\n"
"buffer_load_dword v240, v248, s[4:7], s97 offen offset:3072 sc1\n"
"ds_read_b64_tr_b16 v[42:43], v16 offset:16640\n"
"v_exp_f32_e32 v146, v146\n"
"v_exp_f32_e32 v147, v147\n"
"v_mfma_f32_16x16x32_bf16 v[68:71], v[88:91], v[76:79], v[68:71]\n"
"s_waitcnt lgkmcnt(4)\n"
"v_cvt_pk_bf16_f32 v64, v140, v141\n"
"v_cvt_pk_bf16_f32 v65, v142, v143\n"
"v_cvt_pk_bf16_f32 v66, v144, v145\n"
"v_cvt_pk_bf16_f32 v67, v146, v147\n"
"s_cmp_eq_i32 s78, 7\n"
"s_cbranch_scc1 .Lmk_x19s\n"
".Lmk_x19r:\n"
"v_mfma_f32_16x16x32_bf16 v[76:79], v[116:119], v[28:31], v[24:27]\n"
"ds_read_b128 v[84:87], v15 offset:10240\n"
"v_exp_f32_e32 v100, v100\n"
"v_exp_f32_e32 v101, v101\n"
"v_exp_f32_e32 v102, v102\n"
"v_mfma_f32_16x16x32_bf16 v[140:143], v[116:119], v[32:35], v[36:39]\n"
"buffer_load_dwordx4 a[92:95], v1, s[4:7], s98 offen offset:3072 sc1\n"
"v_exp_f32_e32 v103, v103\n"
"v_mfma_f32_16x16x32_bf16 v[116:119], v[104:107], v[32:35], v[36:39]\n"
"ds_read_b128 v[144:147], v254 offset:10496\n"
"v_exp_f32_e32 v128, v128\n"
"v_exp_f32_e32 v129, v129\n"
"v_mfma_f32_16x16x32_bf16 v[148:151], v[104:107], v[28:31], v[24:27]\n"
"ds_read_b64_tr_b16 v[104:105], v16 offset:14336\n"
"v_exp_f32_e32 v130, v130\n"
"v_exp_f32_e32 v131, v131\n"
"v_cvt_pk_bf16_f32 v152, v100, v101\n"
"s_waitcnt lgkmcnt(3)\n"
"v_mfma_f32_16x16x32_bf16 v[44:47], v[88:91], v[132:135], v[44:47]\n"
"s_add_u32 s98, s69, 0x606000\n"
"buffer_load_dwordx4 a[96:99], v0, s[4:7], s98 offen sc1\n"
"v_cvt_pk_bf16_f32 v153, v102, v103\n"
"v_cvt_pk_bf16_f32 v154, v128, v129\n"
"v_mfma_f32_16x16x32_bf16 v[112:115], v[72:75], v[132:135], v[112:115]\n"
"ds_read_b64_tr_b16 v[106:107], v255 offset:14592\n"
"v_cvt_pk_bf16_f32 v155, v130, v131\n"
"v_exp_f32_e32 v120, v120\n"
"v_exp_f32_e32 v121, v121\n"
"v_mfma_f32_16x16x32_bf16 v[136:139], v[40:43], v[132:135], v[136:139]\n"
"ds_read_b64_tr_b16 v[100:101], v255 offset:14336\n"
"v_exp_f32_e32 v122, v122\n"
"v_exp_f32_e32 v123, v123\n"
"v_mfma_f32_16x16x32_bf16 v[108:111], v[40:43], v[64:67], v[108:111]\n"
"buffer_load_dwordx4 a[100:103], v0, s[4:7], s98 offen offset:1024 sc1\n"
"v_exp_f32_e32 v124, v124\n"
"v_exp_f32_e32 v125, v125\n"
"v_mfma_f32_16x16x32_bf16 v[80:83], v[72:75], v[64:67], v[80:83]\n"
"ds_read_b64_tr_b16 v[102:103], v16 offset:14592\n"
"v_exp_f32_e32 v126, v126\n"
"v_exp_f32_e32 v127, v127\n"
"v_mfma_f32_16x16x32_bf16 v[68:71], v[88:91], v[64:67], v[68:71]\n"
"s_waitcnt lgkmcnt(4)\n"
"v_cvt_pk_bf16_f32 v40, v120, v121\n"
"v_cvt_pk_bf16_f32 v41, v122, v123\n"
"v_cvt_pk_bf16_f32 v42, v124, v125\n"
"v_cvt_pk_bf16_f32 v43, v126, v127\n"
"s_cmp_eq_i32 s78, 6\n"
"s_cbranch_scc1 .Lmk_x20s\n"
".Lmk_x20r:\n"
"v_mfma_f32_16x16x32_bf16 v[64:67], v[84:87], v[28:31], v[24:27]\n"
"ds_read_b128 v[72:75], v15 offset:8192\n"
"v_exp_f32_e32 v76, v76\n"
"v_exp_f32_e32 v77, v77\n"
"v_exp_f32_e32 v78, v78\n"
"v_mfma_f32_16x16x32_bf16 v[120:123], v[84:87], v[32:35], v[36:39]\n"
"buffer_load_dwordx4 a[104:107], v0, s[4:7], s98 offen offset:2048 sc1\n"
"v_exp_f32_e32 v79, v79\n"
"v_mfma_f32_16x16x32_bf16 v[84:87], v[144:147], v[32:35], v[36:39]\n"
"ds_read_b128 v[124:127], v254 offset:8448\n"
"v_exp_f32_e32 v148, v148\n"
"v_exp_f32_e32 v149, v149\n"
"v_mfma_f32_16x16x32_bf16 v[128:131], v[144:147], v[28:31], v[24:27]\n"
"ds_read_b64_tr_b16 v[132:133], v16 offset:12288\n"
"v_exp_f32_e32 v150, v150\n"
"v_exp_f32_e32 v151, v151\n"
"s_waitcnt lgkmcnt(3)\n"
"v_mfma_f32_16x16x32_bf16 v[44:47], v[88:91], v[152:155], v[44:47]\n"
"buffer_load_dwordx4 a[108:111], v0, s[4:7], s98 offen offset:3072 sc1\n"
"v_cvt_pk_bf16_f32 v144, v76, v77\n"
"v_cvt_pk_bf16_f32 v145, v78, v79\n"
"v_cvt_pk_bf16_f32 v146, v148, v149\n"
"v_mfma_f32_16x16x32_bf16 v[112:115], v[104:107], v[152:155], v[112:115]\n"
"ds_read_b64_tr_b16 v[134:135], v255 offset:12544\n"
"v_cvt_pk_bf16_f32 v147, v150, v151\n"
"v_exp_f32_e32 v140, v140\n"
"v_exp_f32_e32 v141, v141\n"
"v_mfma_f32_16x16x32_bf16 v[136:139], v[100:103], v[152:155], v[136:139]\n"
"ds_read_b64_tr_b16 v[76:77], v255 offset:12288\n"
"v_exp_f32_e32 v142, v142\n"
"v_exp_f32_e32 v143, v143\n"
"v_mfma_f32_16x16x32_bf16 v[108:111], v[100:103], v[40:43], v[108:111]\n"
"buffer_load_dwordx4 a[112:115], v1, s[4:7], s98 offen sc1\n"
"v_exp_f32_e32 v116, v116\n"
"v_exp_f32_e32 v117, v117\n"
"v_mfma_f32_16x16x32_bf16 v[80:83], v[104:107], v[40:43], v[80:83]\n"
"ds_read_b64_tr_b16 v[78:79], v16 offset:12544\n"
"v_exp_f32_e32 v118, v118\n"
"v_exp_f32_e32 v119, v119\n"
"v_mfma_f32_16x16x32_bf16 v[68:71], v[88:91], v[40:43], v[68:71]\n"
"s_waitcnt lgkmcnt(4)\n"
"v_cvt_pk_bf16_f32 v40, v140, v141\n"
"v_cvt_pk_bf16_f32 v41, v142, v143\n"
"v_cvt_pk_bf16_f32 v42, v116, v117\n"
"v_cvt_pk_bf16_f32 v43, v118, v119\n"
"s_cmp_eq_i32 s78, 5\n"
"s_cbranch_scc1 .Lmk_x21s\n"
".Lmk_x21r:\n"
"v_mfma_f32_16x16x32_bf16 v[100:103], v[72:75], v[28:31], v[24:27]\n"
"ds_read_b64_tr_b16 v[104:105], v16 offset:10240\n"
"ds_read_b64_tr_b16 v[106:107], v255 offset:10496\n"
"v_exp_f32_e32 v64, v64\n"
"v_exp_f32_e32 v65, v65\n"
"v_mfma_f32_16x16x32_bf16 v[116:119], v[72:75], v[32:35], v[36:39]\n"
"s_add_u32 s97, s71, 0xfffff000\n"
"buffer_load_dwordx2 v[238:239], off, s[4:7], s97 sc1\n"
"v_exp_f32_e32 v66, v66\n"
"v_exp_f32_e32 v67, v67\n"
"v_mfma_f32_16x16x32_bf16 v[72:75], v[124:127], v[32:35], v[36:39]\n"
"ds_read_b64_tr_b16 v[36:37], v255 offset:10240\n"
"v_exp_f32_e32 v128, v128\n"
"v_exp_f32_e32 v129, v129\n"
"v_mfma_f32_16x16x32_bf16 v[140:143], v[124:127], v[28:31], v[24:27]\n"
"ds_read_b64_tr_b16 v[38:39], v16 offset:10496\n"
"buffer_load_dwordx4 a[116:119], v1, s[4:7], s98 offen offset:1024 sc1\n"
"v_exp_f32_e32 v130, v130\n"
"v_exp_f32_e32 v131, v131\n"
"s_waitcnt lgkmcnt(4)\n"
"v_mfma_f32_16x16x32_bf16 v[44:47], v[88:91], v[144:147], v[44:47]\n"
"ds_read_b64_tr_b16 v[124:125], v16 offset:24576\n"
"ds_read_b64_tr_b16 v[126:127], v255 offset:24832\n"
"v_cvt_pk_bf16_f32 v148, v64, v65\n"
"v_cvt_pk_bf16_f32 v149, v66, v67\n"
"v_cvt_pk_bf16_f32 v150, v128, v129\n"
"v_mfma_f32_16x16x32_bf16 v[112:115], v[132:135], v[144:147], v[112:115]\n"
"ds_read_b64_tr_b16 v[64:65], v255 offset:24576\n"
"buffer_load_dwordx4 a[120:123], v1, s[4:7], s98 offen offset:2048 sc1\n"
"v_cvt_pk_bf16_f32 v151, v130, v131\n"
"v_exp_f32_e32 v120, v120\n"
"v_mfma_f32_16x16x32_bf16 v[136:139], v[76:79], v[144:147], v[136:139]\n"
"ds_read_b64_tr_b16 v[66:67], v16 offset:24832\n"
"v_exp_f32_e32 v121, v121\n"
"v_exp_f32_e32 v122, v122\n"
"v_exp_f32_e32 v123, v123\n"
"v_mfma_f32_16x16x32_bf16 v[108:111], v[76:79], v[40:43], v[108:111]\n"
"v_exp_f32_e32 v84, v84\n"
"v_add_u32_e32 v248, 0x26400, v0\n"
"v_exp_f32_e32 v85, v85\n"
"v_mfma_f32_16x16x32_bf16 v[80:83], v[132:135], v[40:43], v[80:83]\n"
"buffer_load_dwordx4 a[124:127], v1, s[4:7], s98 offen offset:3072 sc1\n"
"ds_read_b128 v[76:79], v248\n"
"v_exp_f32_e32 v86, v86\n"
"v_exp_f32_e32 v87, v87\n"
"v_mfma_f32_16x16x32_bf16 v[68:71], v[88:91], v[40:43], v[68:71]\n"
"ds_read_b128 v[128:131], v248 offset:1024\n"
"v_cvt_pk_bf16_f32 v40, v120, v121\n"
"v_cvt_pk_bf16_f32 v41, v122, v123\n"
"v_cvt_pk_bf16_f32 v42, v84, v85\n"
"v_cvt_pk_bf16_f32 v43, v86, v87\n"
"s_waitcnt lgkmcnt(6)\n"
"v_mfma_f32_16x16x32_bf16 v[44:47], v[88:91], v[148:151], v[44:47]\n"
"ds_read_b64_tr_b16 v[84:85], v16 offset:8192\n"
"s_add_u32 s98, s69, 0x608000\n"
"buffer_load_dwordx4 v[24:27], v0, s[4:7], s98 offen sc1\n"
"ds_read_b64_tr_b16 v[86:87], v255 offset:8448\n"
"v_cndmask_b32_e64 v100, v48, v100, s[20:21]\n"
"v_cndmask_b32_e64 v101, v49, v101, s[22:23]\n"
"v_cndmask_b32_e64 v102, v50, v102, s[24:25]\n"
"v_cndmask_b32_e64 v103, v51, v103, s[26:27]\n"
"v_cndmask_b32_e64 v140, v60, v140, s[28:29]\n"
"v_cndmask_b32_e64 v141, v61, v141, s[30:31]\n"
"v_cndmask_b32_e64 v142, v62, v142, s[32:33]\n"
"v_mfma_f32_16x16x32_bf16 v[112:115], v[104:107], v[148:151], v[112:115]\n"
"ds_read_b64_tr_b16 v[48:49], v255 offset:8192\n"
"ds_read_b64_tr_b16 v[50:51], v16 offset:8448\n"
"v_cndmask_b32_e64 v143, v63, v143, s[34:35]\n"
"v_exp_f32_e32 v100, v100\n"
"v_exp_f32_e32 v101, v101\n"
"v_exp_f32_e32 v102, v102\n"
"s_waitcnt vmcnt(11)\n"
"v_exp_f32_e32 v103, v103\n"
"v_exp_f32_e32 v140, v140\n"
"v_mfma_f32_16x16x32_bf16 v[136:139], v[36:39], v[148:151], v[136:139]\n"
"v_add_f32_e32 v247, v247, v246\n"
"v_exp_f32_e32 v141, v141\n"
"v_exp_f32_e32 v142, v142\n"
"v_add_f32_e32 v247, v247, v245\n"
"v_exp_f32_e32 v143, v143\n"
"v_cvt_pk_bf16_f32 v60, v100, v101\n"
"v_add_f32_e32 v247, v247, v244\n"
"v_cvt_pk_bf16_f32 v61, v102, v103\n"
"v_cvt_pk_bf16_f32 v62, v140, v141\n"
"v_mfma_f32_16x16x32_bf16 v[108:111], v[36:39], v[40:43], v[108:111]\n"
"v_cvt_pk_bf16_f32 v63, v142, v143\n"
"v_add_f32_e32 v247, v247, v243\n"
"v_cndmask_b32_e64 v116, v52, v116, s[36:37]\n"
"v_cndmask_b32_e64 v117, v53, v117, s[38:39]\n"
"v_cndmask_b32_e64 v118, v54, v118, s[40:41]\n"
"v_cndmask_b32_e64 v119, v55, v119, s[42:43]\n"
"v_add_f32_e32 v247, v247, v242\n"
"v_cndmask_b32_e64 v72, v56, v72, s[44:45]\n"
"v_cndmask_b32_e64 v73, v57, v73, s[46:47]\n"
"v_cndmask_b32_e64 v74, v58, v74, s[48:49]\n"
"v_add_f32_e32 v247, v247, v241\n"
"v_cndmask_b32_e64 v75, v59, v75, s[50:51]\n"
"v_exp_f32_e32 v116, v116\n"
"v_mfma_f32_16x16x32_bf16 v[80:83], v[104:107], v[40:43], v[80:83]\n"
"v_exp_f32_e32 v117, v117\n"
"v_add_f32_e32 v247, v247, v240\n"
"v_exp_f32_e32 v118, v118\n"
"v_mov_b32_e32 v246, v247\n"
"v_exp_f32_e32 v119, v119\n"
"v_exp_f32_e32 v72, v72\n"
"v_permlane32_swap_b32_e32 v247, v246\n"
"v_exp_f32_e32 v73, v73\n"
"v_mfma_f32_16x16x32_bf16 v[68:71], v[88:91], v[40:43], v[68:71]\n"
"v_exp_f32_e32 v74, v74\n"
"v_add_f32_e32 v247, v247, v246\n"
"v_exp_f32_e32 v75, v75\n"
"v_cvt_pk_bf16_f32 v36, v116, v117\n"
"ds_write_b32 v21, v247 offset:512\n"
"v_cvt_pk_bf16_f32 v37, v118, v119\n"
"v_cvt_pk_bf16_f32 v38, v72, v73\n"
"v_cvt_pk_bf16_f32 v39, v74, v75\n"
"s_waitcnt lgkmcnt(1)\n"
"v_mfma_f32_16x16x32_bf16 v[44:47], v[88:91], v[60:63], v[44:47]\n"
"v_and_b32_e32 v76, v60, v76\n"
"v_and_b32_e32 v77, v61, v77\n"
"v_and_b32_e32 v78, v62, v78\n"
"v_and_b32_e32 v79, v63, v79\n"
"v_mfma_f32_16x16x32_bf16 v[68:71], v[88:91], v[36:39], v[68:71]\n"
"v_xor_b32_e32 v40, v60, v76\n"
"v_xor_b32_e32 v41, v61, v77\n"
"v_xor_b32_e32 v42, v62, v78\n"
"v_xor_b32_e32 v43, v63, v79\n"
"v_mfma_f32_16x16x32_bf16 v[112:115], v[84:87], v[76:79], v[112:115]\n"
"v_and_b32_e32 v128, v36, v128\n"
"v_and_b32_e32 v129, v37, v129\n"
"v_mfma_f32_16x16x32_bf16 v[136:139], v[48:51], v[76:79], v[136:139]\n"
"v_and_b32_e32 v130, v38, v130\n"
"v_and_b32_e32 v131, v39, v131\n"
"v_mfma_f32_16x16x32_bf16 v[112:115], v[124:127], v[40:43], v[112:115]\n"
"v_xor_b32_e32 v52, v36, v128\n"
"v_xor_b32_e32 v53, v37, v129\n"
"v_mfma_f32_16x16x32_bf16 v[136:139], v[64:67], v[40:43], v[136:139]\n"
"v_xor_b32_e32 v54, v38, v130\n"
"v_xor_b32_e32 v55, v39, v131\n"
"v_mfma_f32_16x16x32_bf16 v[80:83], v[84:87], v[128:131], v[80:83]\n"
"s_waitcnt vmcnt(4)\n"
"v_cmp_ne_u64_e32 vcc, v[238:239], v[18:19]\n"
"s_nop 0\n"
"s_cbranch_vccnz .Lmk_f14s\n"
".Lmk_f14r:\n"
"v_mfma_f32_16x16x32_bf16 v[108:111], v[48:51], v[128:131], v[108:111]\n"
"s_add_u32 m0, s77, 0x4000\n"
"s_add_u32 s97, s65, 0\n"
"buffer_load_dwordx4 v2, s[4:7], s97 offen sc1 lds\n"
"buffer_load_dwordx4 v2, s[4:7], s97 offen offset:1024 sc1 lds\n"
"v_mfma_f32_16x16x32_bf16 v[80:83], v[124:127], v[52:55], v[80:83]\n"
"s_add_u32 m0, s77, 0xc000\n"
"s_add_u32 s97, s65, 0x4000\n"
"buffer_load_dwordx4 v2, s[4:7], s97 offen sc1 lds\n"
"buffer_load_dwordx4 v2, s[4:7], s97 offen offset:1024 sc1 lds\n"
"v_mfma_f32_16x16x32_bf16 v[108:111], v[64:67], v[52:55], v[108:111]\n"
"v_max_f32_e32 v248, v44, v68\n"
"v_rcp_f32_e32 v44, v44\n"
"v_rcp_f32_e32 v68, v68\n"
"s_nop 0\n"
"v_max3_f32 v248, v248, v44, v68\n"
"v_cmp_nlt_f32_e32 vcc, v248, v252\n"
"s_cbranch_vccnz .Lmk_b16\n"
".Lmk_k22:\n"
"s_add_u32 s97, s60, 0x40000\n"
"v_mul_f32_e32 v112, v112, v44\n"
"v_mul_f32_e32 v113, v113, v44\n"
"v_mul_f32_e32 v114, v114, v44\n"
"v_mul_f32_e32 v115, v115, v44\n"
"v_mul_f32_e32 v136, v136, v44\n"
"v_mul_f32_e32 v137, v137, v44\n"
"v_mul_f32_e32 v138, v138, v44\n"
"v_mul_f32_e32 v139, v139, v44\n"
"v_cvt_pk_bf16_f32 v28, v112, v113\n"
"v_cvt_pk_bf16_f32 v29, v114, v115\n"
"v_cvt_pk_bf16_f32 v30, v136, v137\n"
"v_cvt_pk_bf16_f32 v31, v138, v139\n"
"buffer_store_dwordx4 v[28:31], v0, s[4:7], s97 offen sc1\n"
"s_add_u32 s97, s60, 0x48000\n"
"v_mul_f32_e32 v80, v80, v68\n"
"v_mul_f32_e32 v81, v81, v68\n"
"v_mul_f32_e32 v82, v82, v68\n"
"v_mul_f32_e32 v83, v83, v68\n"
"v_mul_f32_e32 v108, v108, v68\n"
"v_mul_f32_e32 v109, v109, v68\n"
"v_mul_f32_e32 v110, v110, v68\n"
"v_mul_f32_e32 v111, v111, v68\n"
"v_cvt_pk_bf16_f32 v28, v80, v81\n"
"v_cvt_pk_bf16_f32 v29, v82, v83\n"
"v_cvt_pk_bf16_f32 v30, v108, v109\n"
"v_cvt_pk_bf16_f32 v31, v110, v111\n"
"buffer_store_dwordx4 v[28:31], v0, s[4:7], s97 offen sc1\n"
";\n"
"; @A AT a\n"
"s_mov_b32 s2, 0\n"
"v_mov_b32_e32 v253, 0xff800000\n"
"v_mov_b32_e32 v251, 0xff800000\n"
"v_mov_b32_e32 v250, 0x7f800000\n"
"v_mov_b32_e32 v249, 0x7f800000\n"
".Lmk_a23:\n"
"ds_read_b128 v[28:31], v15 offset:16384\n"
"ds_read_b128 v[32:35], v254 offset:16640\n"
"ds_read_b128 v[36:39], v15 offset:14336\n"
"ds_read_b128 v[40:43], v254 offset:14592\n"
"s_cmp_gt_i32 s78, 7\n"
"s_cselect_b32 s97, 0x80000000, 0\n"
"s_or_b32 s97, s97, 0x7f800000\n"
"s_add_u32 s96, s67, 32\n"
"buffer_load_dwordx2 v[246:247], v12, s[4:7], s96 offen sc1\n"
"s_waitcnt lgkmcnt(2)\n"
"v_mfma_f32_16x16x32_bf16 v[100:103], v[28:31], v[92:95], 0\n"
"v_mfma_f32_16x16x32_bf16 v[104:107], v[28:31], v[96:99], 0\n"
"v_mfma_f32_16x16x32_bf16 v[108:111], v[32:35], v[96:99], 0\n"
"v_mfma_f32_16x16x32_bf16 v[112:115], v[32:35], v[92:95], 0\n"
"s_waitcnt lgkmcnt(0)\n"
"s_nop 3\n"
"v_max_f32_e32 v248, v100, v101\n"
"v_max_f32_e32 v245, v104, v105\n"
"v_max3_f32 v248, v248, v102, v103\n"
"v_max3_f32 v245, v245, v106, v107\n"
"v_max3_f32 v248, v248, v112, v113\n"
"v_max3_f32 v245, v245, v108, v109\n"
"v_max3_f32 v248, v248, v114, v115\n"
"v_max3_f32 v245, v245, v110, v111\n"
"v_mov_b32_e32 v244, v248\n"
"v_mov_b32_e32 v243, v245\n"
"s_nop 0\n"
"v_permlane16_swap_b32_e32 v248, v244\n"
"v_permlane16_swap_b32_e32 v245, v243\n"
"v_max_f32_e32 v248, v248, v244\n"
"v_max_f32_e32 v245, v245, v243\n"
"v_mov_b32_e32 v244, v248\n"
"v_mov_b32_e32 v243, v245\n"
"s_nop 0\n"
"v_permlane32_swap_b32_e32 v248, v244\n"
"v_permlane32_swap_b32_e32 v245, v243\n"
"v_max_f32_e32 v248, v248, v244\n"
"v_max_f32_e32 v245, v245, v243\n"
"v_med3_f32 v248, v248, v253, v250\n"
"v_med3_f32 v245, v245, v251, v249\n"
"v_min_f32_e64 v116, s97, -v248\n"
"v_min_f32_e64 v117, s97, -v248\n"
"v_min_f32_e64 v118, s97, -v248\n"
"v_min_f32_e64 v119, s97, -v248\n"
"v_min_f32_e64 v120, s97, -v245\n"
"v_min_f32_e64 v121, s97, -v245\n"
"v_min_f32_e64 v122, s97, -v245\n"
"v_min_f32_e64 v123, s97, -v245\n"
"v_mfma_f32_16x16x32_bf16 v[44:47], v[36:39], v[92:95], v[116:119]\n"
"ds_read_b128 v[32:35], v15 offset:12288\n"
"v_sub_f32_e32 v100, v100, v248\n"
"v_sub_f32_e32 v101, v101, v248\n"
"v_sub_f32_e32 v102, v102, v248\n"
"v_sub_f32_e32 v103, v103, v248\n"
"v_mfma_f32_16x16x32_bf16 v[48:51], v[36:39], v[96:99], v[120:123]\n"
"v_sub_f32_e32 v112, v112, v248\n"
"v_sub_f32_e32 v113, v113, v248\n"
"v_sub_f32_e32 v114, v114, v248\n"
"v_sub_f32_e32 v115, v115, v248\n"
"v_mfma_f32_16x16x32_bf16 v[52:55], v[40:43], v[96:99], v[120:123]\n"
"ds_read_b128 v[56:59], v254 offset:12544\n"
"v_sub_f32_e32 v104, v104, v245\n"
"v_sub_f32_e32 v105, v105, v245\n"
"v_sub_f32_e32 v106, v106, v245\n"
"v_sub_f32_e32 v107, v107, v245\n"
"v_mfma_f32_16x16x32_bf16 v[36:39], v[40:43], v[92:95], v[116:119]\n"
"s_waitcnt lgkmcnt(0)\n"
"v_sub_f32_e32 v108, v108, v245\n"
"v_sub_f32_e32 v109, v109, v245\n"
"v_sub_f32_e32 v110, v110, v245\n"
"v_sub_f32_e32 v111, v111, v245\n"
"s_cmp_eq_i32 s78, 7\n"
"s_cbranch_scc1 .Lmk_x25s\n"
".Lmk_x25r:\n"
"v_mfma_f32_16x16x32_bf16 v[60:63], v[32:35], v[92:95], v[116:119]\n"
"ds_read_b128 v[40:43], v15 offset:10240\n"
"buffer_load_dwordx4 v[28:31], v0, s[4:7], s98 offen offset:1024 sc1\n"
"ds_read_b128 v[64:67], v254 offset:10496\n"
"v_exp_f32_e32 v44, v44\n"
"v_exp_f32_e32 v45, v45\n"
"v_exp_f32_e32 v46, v46\n"
"v_exp_f32_e32 v47, v47\n"
"v_exp_f32_e32 v36, v36\n"
"v_mfma_f32_16x16x32_bf16 v[68:71], v[32:35], v[96:99], v[120:123]\n"
"ds_read_b64_tr_b16 v[72:73], v16 offset:14336\n"
"buffer_load_dwordx4 v[32:35], v0, s[4:7], s98 offen offset:2048 sc1\n"
"v_exp_f32_e32 v37, v37\n"
"v_exp_f32_e32 v38, v38\n"
"v_exp_f32_e32 v39, v39\n"
"v_cvt_pk_bf16_f32 v76, v44, v45\n"
"v_cvt_pk_bf16_f32 v77, v46, v47\n"
"v_cvt_pk_bf16_f32 v78, v36, v37\n"
"v_mfma_f32_16x16x32_bf16 v[80:83], v[56:59], v[96:99], v[120:123]\n"
"ds_read_b64_tr_b16 v[74:75], v255 offset:14592\n"
"ds_read_b64_tr_b16 v[84:85], v255 offset:14336\n"
"v_cvt_pk_bf16_f32 v79, v38, v39\n"
"v_exp_f32_e32 v48, v48\n"
"v_exp_f32_e32 v49, v49\n"
"v_exp_f32_e32 v50, v50\n"
"v_exp_f32_e32 v51, v51\n"
"v_mfma_f32_16x16x32_bf16 v[124:127], v[56:59], v[92:95], v[116:119]\n"
"buffer_load_dwordx4 v[36:39], v0, s[4:7], s98 offen offset:3072 sc1\n"
"ds_read_b64_tr_b16 v[86:87], v16 offset:14592\n"
"v_exp_f32_e32 v52, v52\n"
"v_exp_f32_e32 v53, v53\n"
"v_exp_f32_e32 v54, v54\n"
"v_exp_f32_e32 v55, v55\n"
"v_cvt_pk_bf16_f32 v56, v48, v49\n"
"v_cvt_pk_bf16_f32 v57, v50, v51\n"
"v_cvt_pk_bf16_f32 v58, v52, v53\n"
"v_cvt_pk_bf16_f32 v59, v54, v55\n"
"s_waitcnt lgkmcnt(4)\n"
"s_cmp_eq_i32 s78, 6\n"
"s_cbranch_scc1 .Lmk_x26s\n"
".Lmk_x26r:\n"
"v_mfma_f32_16x16x32_bf16 v[128:131], v[40:43], v[92:95], v[116:119]\n"
"ds_read_b128 v[52:55], v15 offset:8192\n"
"v_exp_f32_e32 v60, v60\n"
"v_exp_f32_e32 v61, v61\n"
"v_exp_f32_e32 v62, v62\n"
"v_mfma_f32_16x16x32_bf16 v[132:135], v[40:43], v[96:99], v[120:123]\n"
"buffer_load_dwordx4 v[40:43], v1, s[4:7], s98 offen sc1\n"
"v_exp_f32_e32 v63, v63\n"
"v_mfma_f32_16x16x32_bf16 v[136:139], v[64:67], v[96:99], v[120:123]\n"
"ds_read_b128 v[140:143], v254 offset:8448\n"
"v_exp_f32_e32 v124, v124\n"
"v_exp_f32_e32 v125, v125\n"
"v_mfma_f32_16x16x32_bf16 v[144:147], v[64:67], v[92:95], v[116:119]\n"
"ds_read_b64_tr_b16 v[64:65], v16 offset:12288\n"
"v_exp_f32_e32 v126, v126\n"
"v_exp_f32_e32 v127, v127\n"
"s_waitcnt lgkmcnt(3)\n"
"v_mfma_f32_16x16x32_bf16 v[148:151], v[88:91], v[76:79], 0\n"
"buffer_load_dwordx4 v[44:47], v1, s[4:7], s98 offen offset:1024 sc1\n"
"v_cvt_pk_bf16_f32 v152, v60, v61\n"
"v_cvt_pk_bf16_f32 v153, v62, v63\n"
"v_cvt_pk_bf16_f32 v154, v124, v125\n"
"v_mfma_f32_16x16x32_bf16 v[156:159], v[72:75], v[76:79], 0\n"
"ds_read_b64_tr_b16 v[66:67], v255 offset:12544\n"
"v_cvt_pk_bf16_f32 v155, v126, v127\n"
"v_exp_f32_e32 v68, v68\n"
"v_exp_f32_e32 v69, v69\n"
"v_mfma_f32_16x16x32_bf16 v[124:127], v[84:87], v[76:79], 0\n"
"ds_read_b64_tr_b16 v[60:61], v255 offset:12288\n"
"v_exp_f32_e32 v70, v70\n"
"v_exp_f32_e32 v71, v71\n"
"v_mfma_f32_16x16x32_bf16 v[160:163], v[84:87], v[56:59], 0\n"
"buffer_load_dwordx4 v[48:51], v1, s[4:7], s98 offen offset:2048 sc1\n"
"v_exp_f32_e32 v80, v80\n"
"v_exp_f32_e32 v81, v81\n"
"v_mfma_f32_16x16x32_bf16 v[164:167], v[72:75], v[56:59], 0\n"
"ds_read_b64_tr_b16 v[62:63], v16 offset:12544\n"
"v_exp_f32_e32 v82, v82\n"
"v_exp_f32_e32 v83, v83\n"
"v_mfma_f32_16x16x32_bf16 v[168:171], v[88:91], v[56:59], 0\n"
"s_waitcnt lgkmcnt(4)\n"
"v_cvt_pk_bf16_f32 v72, v68, v69\n"
"v_cvt_pk_bf16_f32 v73, v70, v71\n"
"v_cvt_pk_bf16_f32 v74, v80, v81\n"
"v_cvt_pk_bf16_f32 v75, v82, v83\n"
"s_cmp_eq_i32 s78, 5\n"
"s_cbranch_scc1 .Lmk_x27s\n"
".Lmk_x27r:\n"
"v_mfma_f32_16x16x32_bf16 v[76:79], v[52:55], v[92:95], v[116:119]\n"
"ds_read_b64_tr_b16 v[68:69], v16 offset:10240\n"
"v_exp_f32_e32 v128, v128\n"
"v_exp_f32_e32 v129, v129\n"
"v_exp_f32_e32 v130, v130\n"
"v_mfma_f32_16x16x32_bf16 v[80:83], v[52:55], v[96:99], v[120:123]\n"
"buffer_load_dwordx4 v[52:55], v1, s[4:7], s98 offen offset:3072 sc1\n"
"v_exp_f32_e32 v131, v131\n"
"v_mfma_f32_16x16x32_bf16 v[84:87], v[140:143], v[96:99], v[120:123]\n"
"ds_read_b64_tr_b16 v[70:71], v255 offset:10496\n"
"v_exp_f32_e32 v144, v144\n"
"v_exp_f32_e32 v145, v145\n"
"v_mfma_f32_16x16x32_bf16 v[172:175], v[140:143], v[92:95], v[116:119]\n"
"s_waitcnt lgkmcnt(2)\n"
"v_exp_f32_e32 v146, v146\n"
"v_exp_f32_e32 v147, v147\n"
"v_cvt_pk_bf16_f32 v140, v128, v129\n"
"v_mfma_f32_16x16x32_bf16 v[148:151], v[88:91], v[152:155], v[148:151]\n"
"s_add_u32 s97, s69, 0x60a000\n"
"buffer_load_dwordx4 v[56:59], v0, s[4:7], s97 offen sc1\n"
"v_cvt_pk_bf16_f32 v141, v130, v131\n"
"v_cvt_pk_bf16_f32 v142, v144, v145\n"
"v_mfma_f32_16x16x32_bf16 v[156:159], v[64:67], v[152:155], v[156:159]\n"
"ds_read_b64_tr_b16 v[128:129], v255 offset:10240\n"
"v_cvt_pk_bf16_f32 v143, v146, v147\n"
"v_exp_f32_e32 v132, v132\n"
"v_exp_f32_e32 v133, v133\n"
"v_mfma_f32_16x16x32_bf16 v[124:127], v[60:63], v[152:155], v[124:127]\n"
"v_exp_f32_e32 v134, v134\n"
"v_exp_f32_e32 v135, v135\n"
"v_mfma_f32_16x16x32_bf16 v[160:163], v[60:63], v[72:75], v[160:163]\n"
"buffer_load_dwordx4 v[60:63], v0, s[4:7], s97 offen offset:1024 sc1\n"
"ds_read_b64_tr_b16 v[130:131], v16 offset:10496\n"
"v_exp_f32_e32 v136, v136\n"
"v_mfma_f32_16x16x32_bf16 v[164:167], v[64:67], v[72:75], v[164:167]\n"
"v_exp_f32_e32 v137, v137\n"
"v_exp_f32_e32 v138, v138\n"
"v_exp_f32_e32 v139, v139\n"
"v_mfma_f32_16x16x32_bf16 v[168:171], v[88:91], v[72:75], v[168:171]\n"
"s_waitcnt vmcnt(12)\n"
"v_cvt_pk_bf16_f32 v72, v132, v133\n"
"v_cvt_pk_bf16_f32 v73, v134, v135\n"
"v_cvt_pk_bf16_f32 v74, v136, v137\n"
"v_cvt_pk_bf16_f32 v75, v138, v139\n"
"s_cmp_lg_u32 s2, 0\n"
"s_cbranch_scc1 .Lmk_s28\n"
"s_barrier\n"
".Lmk_s28:\n"
"ds_read_b128 v[132:135], v15 offset:6144\n"
"ds_read_b128 v[136:139], v254 offset:6400\n"
"s_waitcnt lgkmcnt(2)\n"
"v_mfma_f32_16x16x32_bf16 v[148:151], v[88:91], v[140:143], v[148:151]\n"
"ds_read_b128 v[144:147], v15 offset:4096\n"
"v_exp_f32_e32 v76, v76\n"
"v_exp_f32_e32 v77, v77\n"
"v_exp_f32_e32 v78, v78\n"
"v_mfma_f32_16x16x32_bf16 v[156:159], v[68:71], v[140:143], v[156:159]\n"
"buffer_load_dwordx4 v[64:67], v0, s[4:7], s97 offen offset:2048 sc1\n"
"ds_read_b128 v[152:155], v254 offset:4352\n"
"v_exp_f32_e32 v79, v79\n"
"v_mfma_f32_16x16x32_bf16 v[124:127], v[128:131], v[140:143], v[124:127]\n"
"ds_read_b64_tr_b16 v[140:141], v16 offset:8192\n"
"v_exp_f32_e32 v172, v172\n"
"v_exp_f32_e32 v173, v173\n"
"v_mfma_f32_16x16x32_bf16 v[160:163], v[128:131], v[72:75], v[160:163]\n"
"ds_read_b64_tr_b16 v[142:143], v255 offset:8448\n"
"v_exp_f32_e32 v174, v174\n"
"v_exp_f32_e32 v175, v175\n"
"v_mfma_f32_16x16x32_bf16 v[164:167], v[68:71], v[72:75], v[164:167]\n"
"buffer_load_dwordx4 v[68:71], v0, s[4:7], s97 offen offset:3072 sc1\n"
"v_cvt_pk_bf16_f32 v128, v76, v77\n"
"v_cvt_pk_bf16_f32 v129, v78, v79\n"
"v_cvt_pk_bf16_f32 v130, v172, v173\n"
"v_cvt_pk_bf16_f32 v131, v174, v175\n"
"v_mfma_f32_16x16x32_bf16 v[168:171], v[88:91], v[72:75], v[168:171]\n"
"ds_read_b64_tr_b16 v[172:173], v255 offset:8192\n"
"v_exp_f32_e32 v80, v80\n"
"v_exp_f32_e32 v81, v81\n"
"s_waitcnt lgkmcnt(5)\n"
"v_mfma_f32_16x16x32_bf16 v[176:179], v[132:135], v[92:95], v[116:119]\n"
"ds_read_b64_tr_b16 v[174:175], v16 offset:8448\n"
"v_exp_f32_e32 v82, v82\n"
"v_exp_f32_e32 v83, v83\n"
"v_exp_f32_e32 v84, v84\n"
"v_mfma_f32_16x16x32_bf16 v[180:183], v[132:135], v[96:99], v[120:123]\n"
"buffer_load_dwordx4 v[72:75], v1, s[4:7], s97 offen sc1\n"
"s_waitcnt vmcnt(12)\n"
"v_cmp_ne_u64_e32 vcc, v[246:247], v[18:19]\n"
"v_mfma_f32_16x16x32_bf16 v[132:135], v[136:139], v[96:99], v[120:123]\n"
"v_exp_f32_e32 v85, v85\n"
"v_exp_f32_e32 v86, v86\n"
"v_exp_f32_e32 v87, v87\n"
"v_mfma_f32_16x16x32_bf16 v[184:187], v[136:139], v[92:95], v[116:119]\n"
"s_cbranch_vccnz .Lmk_f5s\n"
".Lmk_f5r:\n"
"v_cvt_pk_bf16_f32 v136, v80, v81\n"
"v_cvt_pk_bf16_f32 v137, v82, v83\n"
"v_cvt_pk_bf16_f32 v138, v84, v85\n"
"v_cvt_pk_bf16_f32 v139, v86, v87\n"
"s_waitcnt lgkmcnt(4)\n"
"v_mfma_f32_16x16x32_bf16 v[188:191], v[144:147], v[92:95], v[116:119]\n"
"ds_read_b128 v[192:195], v15 offset:2048\n"
"v_exp_f32_e32 v176, v176\n"
"v_exp_f32_e32 v177, v177\n"
"v_exp_f32_e32 v178, v178\n"
"v_mfma_f32_16x16x32_bf16 v[196:199], v[144:147], v[96:99], v[120:123]\n"
"buffer_load_dwordx4 v[76:79], v1, s[4:7], s97 offen offset:1024 sc1\n"
"v_exp_f32_e32 v179, v179\n"
"v_mfma_f32_16x16x32_bf16 v[144:147], v[152:155], v[96:99], v[120:123]\n"
"ds_read_b128 v[200:203], v254 offset:2304\n"
"v_exp_f32_e32 v184, v184\n"
"v_exp_f32_e32 v185, v185\n"
"v_mfma_f32_16x16x32_bf16 v[204:207], v[152:155], v[92:95], v[116:119]\n"
"ds_read_b64_tr_b16 v[152:153], v16 offset:6144\n"
"v_exp_f32_e32 v186, v186\n"
"v_exp_f32_e32 v187, v187\n"
"s_waitcnt lgkmcnt(3)\n"
"v_mfma_f32_16x16x32_bf16 v[148:151], v[88:91], v[128:131], v[148:151]\n"
"buffer_load_dwordx4 v[80:83], v1, s[4:7], s97 offen offset:2048 sc1\n"
"v_cvt_pk_bf16_f32 v208, v176, v177\n"
"v_cvt_pk_bf16_f32 v209, v178, v179\n"
"v_cvt_pk_bf16_f32 v210, v184, v185\n"
"v_mfma_f32_16x16x32_bf16 v[156:159], v[140:143], v[128:131], v[156:159]\n"
"ds_read_b64_tr_b16 v[154:155], v255 offset:6400\n"
"v_cvt_pk_bf16_f32 v211, v186, v187\n"
"v_exp_f32_e32 v180, v180\n"
"v_exp_f32_e32 v181, v181\n"
"v_mfma_f32_16x16x32_bf16 v[124:127], v[172:175], v[128:131], v[124:127]\n"
"ds_read_b64_tr_b16 v[128:129], v255 offset:6144\n"
"v_exp_f32_e32 v182, v182\n"
"v_exp_f32_e32 v183, v183\n"
"v_mfma_f32_16x16x32_bf16 v[160:163], v[172:175], v[136:139], v[160:163]\n"
"buffer_load_dwordx4 v[84:87], v1, s[4:7], s97 offen offset:3072 sc1\n"
"v_exp_f32_e32 v132, v132\n"
"v_exp_f32_e32 v133, v133\n"
"v_mfma_f32_16x16x32_bf16 v[164:167], v[140:143], v[136:139], v[164:167]\n"
"ds_read_b64_tr_b16 v[130:131], v16 offset:6400\n"
"v_exp_f32_e32 v134, v134\n"
"v_exp_f32_e32 v135, v135\n"
"v_mfma_f32_16x16x32_bf16 v[168:171], v[88:91], v[136:139], v[168:171]\n"
"s_waitcnt lgkmcnt(4)\n"
"v_cvt_pk_bf16_f32 v136, v180, v181\n"
"v_cvt_pk_bf16_f32 v137, v182, v183\n"
"v_cvt_pk_bf16_f32 v138, v132, v133\n"
"v_cvt_pk_bf16_f32 v139, v134, v135\n"
"v_mfma_f32_16x16x32_bf16 v[132:135], v[192:195], v[92:95], v[116:119]\n"
"ds_read_b128 v[140:143], v15\n"
"v_exp_f32_e32 v188, v188\n"
"v_exp_f32_e32 v189, v189\n"
"v_exp_f32_e32 v190, v190\n"
"v_mfma_f32_16x16x32_bf16 v[172:175], v[192:195], v[96:99], v[120:123]\n"
"s_add_u32 s97, s56, 0x40000\n"
"buffer_load_dwordx4 v[176:179], v0, s[4:7], s97 offen sc1\n"
"v_exp_f32_e32 v191, v191\n"
"v_mfma_f32_16x16x32_bf16 v[180:183], v[200:203], v[96:99], v[120:123]\n"
"ds_read_b128 v[184:187], v254 offset:256\n"
"v_exp_f32_e32 v204, v204\n"
"v_exp_f32_e32 v205, v205\n"
"v_exp_f32_e32 v206, v206\n"
"v_mfma_f32_16x16x32_bf16 v[192:195], v[200:203], v[92:95], v[116:119]\n"
"buffer_load_dwordx4 v[200:203], v0, s[4:7], s97 offen offset:1024 sc1\n"
"ds_read_b64_tr_b16 v[212:213], v16 offset:4096\n"
"v_exp_f32_e32 v207, v207\n"
"s_waitcnt lgkmcnt(3)\n"
"v_mfma_f32_16x16x32_bf16 v[148:151], v[88:91], v[208:211], v[148:151]\n"
"v_cvt_pk_bf16_f32 v216, v188, v189\n"
"v_cvt_pk_bf16_f32 v217, v190, v191\n"
"v_cvt_pk_bf16_f32 v218, v204, v205\n"
"v_cvt_pk_bf16_f32 v219, v206, v207\n"
"v_mfma_f32_16x16x32_bf16 v[156:159], v[152:155], v[208:211], v[156:159]\n"
"buffer_load_dwordx4 v[188:191], v0, s[4:7], s97 offen offset:2048 sc1\n"
"ds_read_b64_tr_b16 v[214:215], v255 offset:4352\n"
"v_exp_f32_e32 v196, v196\n"
"v_exp_f32_e32 v197, v197\n"
"v_mfma_f32_16x16x32_bf16 v[124:127], v[128:131], v[208:211], v[124:127]\n"
"buffer_load_dwordx4 v[204:207], v0, s[4:7], s97 offen offset:3072 sc1\n"
"v_exp_f32_e32 v198, v198\n"
"v_exp_f32_e32 v199, v199\n"
"v_mfma_f32_16x16x32_bf16 v[160:163], v[128:131], v[136:139], v[160:163]\n"
"ds_read_b64_tr_b16 v[128:129], v255 offset:4096\n"
"v_exp_f32_e32 v144, v144\n"
"v_exp_f32_e32 v145, v145\n"
"v_mfma_f32_16x16x32_bf16 v[164:167], v[152:155], v[136:139], v[164:167]\n"
"buffer_load_dwordx4 v[152:155], v1, s[4:7], s97 offen sc1\n"
"ds_read_b64_tr_b16 v[130:131], v16 offset:4352\n"
"v_exp_f32_e32 v146, v146\n"
"v_exp_f32_e32 v147, v147\n"
"v_mfma_f32_16x16x32_bf16 v[168:171], v[88:91], v[136:139], v[168:171]\n"
"s_waitcnt lgkmcnt(4)\n"
"v_cvt_pk_bf16_f32 v136, v196, v197\n"
"v_cvt_pk_bf16_f32 v137, v198, v199\n"
"v_cvt_pk_bf16_f32 v138, v144, v145\n"
"v_cvt_pk_bf16_f32 v139, v146, v147\n"
"v_mfma_f32_16x16x32_bf16 v[144:147], v[140:143], v[92:95], v[116:119]\n"
"ds_read_b64_tr_b16 v[196:197], v16 offset:2048\n"
"ds_read_b64_tr_b16 v[198:199], v255 offset:2304\n"
"buffer_load_dwordx4 v[208:211], v1, s[4:7], s97 offen offset:1024 sc1\n"
"v_exp_f32_e32 v132, v132\n"
"v_exp_f32_e32 v133, v133\n"
"v_mfma_f32_16x16x32_bf16 v[220:223], v[140:143], v[96:99], v[120:123]\n"
"ds_read_b64_tr_b16 v[140:141], v255 offset:2048\n"
"v_exp_f32_e32 v134, v134\n"
"v_exp_f32_e32 v135, v135\n"
"v_mfma_f32_16x16x32_bf16 v[224:227], v[184:187], v[96:99], v[120:123]\n"
"buffer_load_dwordx4 v[120:123], v1, s[4:7], s97 offen offset:2048 sc1\n"
"v_exp_f32_e32 v192, v192\n"
"v_exp_f32_e32 v193, v193\n"
"v_mfma_f32_16x16x32_bf16 v[228:231], v[184:187], v[92:95], v[116:119]\n"
"ds_read_b64_tr_b16 v[142:143], v16 offset:2304\n"
"ds_read_b64_tr_b16 v[116:117], v16 offset:16384\n"
"v_exp_f32_e32 v194, v194\n"
"v_exp_f32_e32 v195, v195\n"
"s_waitcnt lgkmcnt(5)\n"
"v_mfma_f32_16x16x32_bf16 v[148:151], v[88:91], v[216:219], v[148:151]\n"
"v_cvt_pk_bf16_f32 v184, v132, v133\n"
"v_cvt_pk_bf16_f32 v185, v134, v135\n"
"v_cvt_pk_bf16_f32 v186, v192, v193\n"
"v_mfma_f32_16x16x32_bf16 v[156:159], v[212:215], v[216:219], v[156:159]\n"
"buffer_load_dwordx4 v[132:135], v1, s[4:7], s97 offen offset:3072 sc1\n"
"ds_read_b64_tr_b16 v[118:119], v255 offset:16640\n"
"ds_read_b64_tr_b16 v[192:193], v255 offset:16384\n"
"v_cvt_pk_bf16_f32 v187, v194, v195\n"
"v_exp_f32_e32 v172, v172\n"
"v_exp_f32_e32 v173, v173\n"
"v_exp_f32_e32 v174, v174\n"
"v_mfma_f32_16x16x32_bf16 v[124:127], v[128:131], v[216:219], v[124:127]\n"
"s_add_u32 s97, s57, 0x40000\n"
"buffer_load_dwordx4 v[216:219], v0, s[4:7], s97 offen sc1\n"
"ds_read_b64_tr_b16 v[194:195], v16 offset:16640\n"
"v_exp_f32_e32 v175, v175\n"
"v_mfma_f32_16x16x32_bf16 v[160:163], v[128:131], v[136:139], v[160:163]\n"
"v_exp_f32_e32 v180, v180\n"
"v_add_u32_e32 v248, 0x26400, v0\n"
"v_exp_f32_e32 v181, v181\n"
"v_mfma_f32_16x16x32_bf16 v[164:167], v[212:215], v[136:139], v[164:167]\n"
"buffer_load_dwordx4 v[128:131], v0, s[4:7], s97 offen offset:1024 sc1\n"
"ds_read_b128 v[212:215], v248\n"
"v_exp_f32_e32 v182, v182\n"
"v_exp_f32_e32 v183, v183\n"
"v_mfma_f32_16x16x32_bf16 v[168:171], v[88:91], v[136:139], v[168:171]\n"
"ds_read_b128 v[232:235], v248 offset:1024\n"
"v_cvt_pk_bf16_f32 v136, v172, v173\n"
"v_cvt_pk_bf16_f32 v137, v174, v175\n"
"v_cvt_pk_bf16_f32 v138, v180, v181\n"
"v_cvt_pk_bf16_f32 v139, v182, v183\n"
"s_waitcnt lgkmcnt(6)\n"
"v_mfma_f32_16x16x32_bf16 v[148:151], v[88:91], v[184:187], v[148:151]\n"
"ds_read_b64_tr_b16 v[172:173], v16\n"
"buffer_load_dwordx4 v[180:183], v0, s[4:7], s97 offen offset:2048 sc1\n"
"v_cndmask_b32_e64 v144, v100, v144, s[20:21]\n"
"v_cndmask_b32_e64 v145, v101, v145, s[22:23]\n"
"v_cndmask_b32_e64 v146, v102, v146, s[24:25]\n"
"v_cndmask_b32_e64 v147, v103, v147, s[26:27]\n"
"v_cndmask_b32_e64 v228, v112, v228, s[28:29]\n"
"v_cndmask_b32_e64 v229, v113, v229, s[30:31]\n"
"v_cndmask_b32_e64 v230, v114, v230, s[32:33]\n"
"v_cndmask_b32_e64 v231, v115, v231, s[34:35]\n"
"v_exp_f32_e32 v144, v144\n"
"v_mfma_f32_16x16x32_bf16 v[156:159], v[196:199], v[184:187], v[156:159]\n"
"ds_read_b64_tr_b16 v[174:175], v255 offset:256\n"
"buffer_load_dwordx4 v[100:103], v0, s[4:7], s97 offen offset:3072 sc1\n"
"v_exp_f32_e32 v145, v145\n"
"v_exp_f32_e32 v146, v146\n"
"v_exp_f32_e32 v147, v147\n"
"v_exp_f32_e32 v228, v228\n"
"v_mfma_f32_16x16x32_bf16 v[124:127], v[140:143], v[184:187], v[124:127]\n"
"buffer_load_dwordx4 v[184:187], v1, s[4:7], s97 offen sc1\n"
"v_exp_f32_e32 v229, v229\n"
"v_exp_f32_e32 v230, v230\n"
"v_exp_f32_e32 v231, v231\n"
"v_cvt_pk_bf16_f32 v112, v144, v145\n"
"v_cvt_pk_bf16_f32 v113, v146, v147\n"
"v_cvt_pk_bf16_f32 v114, v228, v229\n"
"v_mfma_f32_16x16x32_bf16 v[160:163], v[140:143], v[136:139], v[160:163]\n"
"ds_read_b64_tr_b16 v[140:141], v255\n"
"v_cvt_pk_bf16_f32 v115, v230, v231\n"
"v_cndmask_b32_e64 v220, v104, v220, s[36:37]\n"
"v_cndmask_b32_e64 v221, v105, v221, s[38:39]\n"
"v_cndmask_b32_e64 v222, v106, v222, s[40:41]\n"
"v_cndmask_b32_e64 v223, v107, v223, s[42:43]\n"
"v_cndmask_b32_e64 v224, v108, v224, s[44:45]\n"
"v_cndmask_b32_e64 v225, v109, v225, s[46:47]\n"
"v_cndmask_b32_e64 v226, v110, v226, s[48:49]\n"
"v_cndmask_b32_e64 v227, v111, v227, s[50:51]\n"
"v_mfma_f32_16x16x32_bf16 v[164:167], v[196:199], v[136:139], v[164:167]\n"
"buffer_load_dwordx4 v[104:107], v1, s[4:7], s97 offen offset:1024 sc1\n"
"ds_read_b64_tr_b16 v[142:143], v16 offset:256\n"
"buffer_load_dwordx4 v[108:111], v1, s[4:7], s97 offen offset:2048 sc1\n"
"v_exp_f32_e32 v220, v220\n"
"v_exp_f32_e32 v221, v221\n"
"v_exp_f32_e32 v222, v222\n"
"v_exp_f32_e32 v223, v223\n"
"v_exp_f32_e32 v224, v224\n"
"v_mfma_f32_16x16x32_bf16 v[168:171], v[88:91], v[136:139], v[168:171]\n"
"s_waitcnt lgkmcnt(0)\n"
"v_exp_f32_e32 v225, v225\n"
"v_exp_f32_e32 v226, v226\n"
"v_exp_f32_e32 v227, v227\n"
"v_cvt_pk_bf16_f32 v136, v220, v221\n"
"v_cvt_pk_bf16_f32 v137, v222, v223\n"
"v_cvt_pk_bf16_f32 v138, v224, v225\n"
"v_cvt_pk_bf16_f32 v139, v226, v227\n"
"v_mfma_f32_16x16x32_bf16 v[148:151], v[88:91], v[112:115], v[148:151]\n"
"v_and_b32_e32 v212, v112, v212\n"
"v_and_b32_e32 v213, v113, v213\n"
"v_and_b32_e32 v214, v114, v214\n"
"v_and_b32_e32 v215, v115, v215\n"
"v_mfma_f32_16x16x32_bf16 v[168:171], v[88:91], v[136:139], v[168:171]\n"
"v_xor_b32_e32 v144, v112, v212\n"
"v_xor_b32_e32 v145, v113, v213\n"
"v_xor_b32_e32 v146, v114, v214\n"
"v_xor_b32_e32 v147, v115, v215\n"
"v_mfma_f32_16x16x32_bf16 v[156:159], v[172:175], v[212:215], v[156:159]\n"
"v_and_b32_e32 v232, v136, v232\n"
"v_and_b32_e32 v233, v137, v233\n"
"v_mfma_f32_16x16x32_bf16 v[124:127], v[140:143], v[212:215], v[124:127]\n"
"v_and_b32_e32 v234, v138, v234\n"
"v_and_b32_e32 v235, v139, v235\n"
"v_mfma_f32_16x16x32_bf16 v[156:159], v[116:119], v[144:147], v[156:159]\n"
"v_xor_b32_e32 v112, v136, v232\n"
"v_xor_b32_e32 v113, v137, v233\n"
"v_mfma_f32_16x16x32_bf16 v[124:127], v[192:195], v[144:147], v[124:127]\n"
"v_xor_b32_e32 v114, v138, v234\n"
"v_xor_b32_e32 v115, v139, v235\n"
"v_mfma_f32_16x16x32_bf16 v[164:167], v[172:175], v[232:235], v[164:167]\n"
"buffer_load_dwordx4 v[136:139], v1, s[4:7], s97 offen offset:3072 sc1\n"
"s_add_u32 s97, s58, 0x40000\n"
"buffer_load_dwordx4 v[144:147], v0, s[4:7], s97 offen sc1\n"
"v_mfma_f32_16x16x32_bf16 v[160:163], v[140:143], v[232:235], v[160:163]\n"
"buffer_load_dwordx4 v[140:143], v0, s[4:7], s97 offen offset:1024 sc1\n"
"v_mfma_f32_16x16x32_bf16 v[164:167], v[116:119], v[112:115], v[164:167]\n"
"buffer_load_dwordx4 v[116:119], v0, s[4:7], s97 offen offset:2048 sc1\n"
"v_mfma_f32_16x16x32_bf16 v[160:163], v[192:195], v[112:115], v[160:163]\n"
"buffer_load_dwordx4 v[112:115], v0, s[4:7], s97 offen offset:3072 sc1\n"
"v_max_f32_e32 v248, v148, v168\n"
"v_rcp_f32_e32 v148, v148\n"
"v_rcp_f32_e32 v168, v168\n"
"s_nop 0\n"
"v_max3_f32 v248, v248, v148, v168\n"
"v_cmp_nlt_f32_e32 vcc, v248, v252\n"
"s_cbranch_vccnz .Lmk_b24\n"
".Lmk_k29:\n"
"v_mul_f32_e32 v156, v156, v148\n"
"v_mul_f32_e32 v157, v157, v148\n"
"v_mul_f32_e32 v158, v158, v148\n"
"v_mul_f32_e32 v159, v159, v148\n"
"v_mul_f32_e32 v124, v124, v148\n"
"v_mul_f32_e32 v125, v125, v148\n"
"v_mul_f32_e32 v126, v126, v148\n"
"v_mul_f32_e32 v127, v127, v148\n"
"v_cvt_pk_bf16_f32 v88, v156, v157\n"
"v_cvt_pk_bf16_f32 v89, v158, v159\n"
"v_cvt_pk_bf16_f32 v90, v124, v125\n"
"v_cvt_pk_bf16_f32 v91, v126, v127\n"
"buffer_store_dwordx4 v[88:91], v0, s[4:7], s60 offen sc1\n"
"s_add_u32 s98, s60, 0x8000\n"
"v_mul_f32_e32 v164, v164, v168\n"
"v_mul_f32_e32 v165, v165, v168\n"
"v_mul_f32_e32 v166, v166, v168\n"
"v_mul_f32_e32 v167, v167, v168\n"
"v_mul_f32_e32 v160, v160, v168\n"
"v_mul_f32_e32 v161, v161, v168\n"
"v_mul_f32_e32 v162, v162, v168\n"
"v_mul_f32_e32 v163, v163, v168\n"
"v_cvt_pk_bf16_f32 v88, v164, v165\n"
"v_cvt_pk_bf16_f32 v89, v166, v167\n"
"v_cvt_pk_bf16_f32 v90, v160, v161\n"
"v_cvt_pk_bf16_f32 v91, v162, v163\n"
"buffer_store_dwordx4 v[88:91], v0, s[4:7], s98 offen sc1\n"
";\n"
"s_waitcnt vmcnt(20)\n"
"; @A G2 b.0\n"
"s_nop 0\n"
"v_mfma_f32_16x16x32_bf16 v[88:91], a[128:131], v[176:179], 0\n"
"v_mfma_f32_16x16x32_bf16 v[88:91], a[132:135], v[200:203], v[88:91]\n"
"s_waitcnt vmcnt(18)\n"
"v_mfma_f32_16x16x32_bf16 v[88:91], a[136:139], v[188:191], v[88:91]\n"
"buffer_load_dwordx4 v[92:95], v1, s[4:7], s97 offen sc1\n"
"v_mfma_f32_16x16x32_bf16 v[88:91], a[140:143], v[204:207], v[88:91]\n"
"s_waitcnt vmcnt(17)\n"
"v_mfma_f32_16x16x32_bf16 v[88:91], a[144:147], v[152:155], v[88:91]\n"
"v_mfma_f32_16x16x32_bf16 v[88:91], a[148:151], v[208:211], v[88:91]\n"
"s_waitcnt vmcnt(15)\n"
"v_mfma_f32_16x16x32_bf16 v[88:91], a[152:155], v[120:123], v[88:91]\n"
"buffer_load_dwordx4 v[96:99], v1, s[4:7], s97 offen offset:1024 sc1\n"
"v_mfma_f32_16x16x32_bf16 v[88:91], a[156:159], v[132:135], v[88:91]\n"
"v_mfma_f32_16x16x32_bf16 v[124:127], a[188:191], v[132:135], 0\n"
"v_mfma_f32_16x16x32_bf16 v[124:127], a[184:187], v[120:123], v[124:127]\n"
"v_mfma_f32_16x16x32_bf16 v[124:127], a[180:183], v[208:211], v[124:127]\n"
"buffer_load_dwordx4 v[148:151], v1, s[4:7], s97 offen offset:2048 sc1\n"
"v_mfma_f32_16x16x32_bf16 v[124:127], a[176:179], v[152:155], v[124:127]\n"
"v_mfma_f32_16x16x32_bf16 v[124:127], a[172:175], v[204:207], v[124:127]\n"
"s_nop 1\n"
"v_cmp_u_f32_e32 vcc, v88, v88\n"
"v_mfma_f32_16x16x32_bf16 v[124:127], a[168:171], v[188:191], v[124:127]\n"
"s_cbranch_vccnz .Lmk_p30s\n"
".Lmk_p30r:\n"
"v_mfma_f32_16x16x32_bf16 v[124:127], a[164:167], v[200:203], v[124:127]\n"
"ds_write_b128 v3, v[88:91]\n"
"v_mfma_f32_16x16x32_bf16 v[124:127], a[160:163], v[176:179], v[124:127]\n"
"buffer_load_dwordx4 v[88:91], v1, s[4:7], s97 offen offset:3072 sc1\n"
"v_mfma_f32_16x16x32_bf16 v[156:159], a[192:195], v[176:179], 0\n"
"v_mfma_f32_16x16x32_bf16 v[156:159], a[196:199], v[200:203], v[156:159]\n"
"s_add_u32 s98, s59, 0x40000\n"
"v_mfma_f32_16x16x32_bf16 v[156:159], a[200:203], v[188:191], v[156:159]\n"
"v_mfma_f32_16x16x32_bf16 v[156:159], a[204:207], v[204:207], v[156:159]\n"
"buffer_load_dwordx4 v[160:163], v0, s[4:7], s98 offen sc1\n"
"v_mfma_f32_16x16x32_bf16 v[156:159], a[208:211], v[152:155], v[156:159]\n"
"v_mfma_f32_16x16x32_bf16 v[156:159], a[212:215], v[208:211], v[156:159]\n"
"ds_write_b128 v3, v[124:127] offset:1024\n"
"v_mfma_f32_16x16x32_bf16 v[156:159], a[216:219], v[120:123], v[156:159]\n"
"v_mfma_f32_16x16x32_bf16 v[156:159], a[220:223], v[132:135], v[156:159]\n"
"buffer_load_dwordx4 v[124:127], v0, s[4:7], s98 offen offset:1024 sc1\n"
"v_mfma_f32_16x16x32_bf16 v[164:167], a[252:255], v[132:135], 0\n"
"v_mfma_f32_16x16x32_bf16 v[164:167], a[248:251], v[120:123], v[164:167]\n"
"v_mfma_f32_16x16x32_bf16 v[164:167], a[244:247], v[208:211], v[164:167]\n"
"v_mfma_f32_16x16x32_bf16 v[164:167], a[240:243], v[152:155], v[164:167]\n"
"buffer_load_dwordx4 v[120:123], v0, s[4:7], s98 offen offset:2048 sc1\n"
"v_mfma_f32_16x16x32_bf16 v[164:167], a[236:239], v[204:207], v[164:167]\n"
"v_mfma_f32_16x16x32_bf16 v[164:167], a[232:235], v[188:191], v[164:167]\n"
"ds_write_b128 v3, v[156:159] offset:2048\n"
"v_mfma_f32_16x16x32_bf16 v[164:167], a[228:231], v[200:203], v[164:167]\n"
"v_mfma_f32_16x16x32_bf16 v[164:167], a[224:227], v[176:179], v[164:167]\n"
"s_waitcnt vmcnt(17)\n"
"buffer_load_dwordx4 v[132:135], v0, s[4:7], s98 offen offset:3072 sc1\n"
"; @A G2 b.1\n"
"v_mfma_f32_16x16x32_bf16 v[152:155], a[128:131], v[216:219], 0\n"
"v_mfma_f32_16x16x32_bf16 v[152:155], a[132:135], v[128:131], v[152:155]\n"
"v_mfma_f32_16x16x32_bf16 v[152:155], a[136:139], v[180:183], v[152:155]\n"
"v_mfma_f32_16x16x32_bf16 v[152:155], a[140:143], v[100:103], v[152:155]\n"
"s_waitcnt vmcnt(14)\n"
"buffer_load_dwordx4 v[156:159], v1, s[4:7], s98 offen sc1\n"
"v_mfma_f32_16x16x32_bf16 v[152:155], a[144:147], v[184:187], v[152:155]\n"
"v_mfma_f32_16x16x32_bf16 v[152:155], a[148:151], v[104:107], v[152:155]\n"
"ds_write_b128 v3, v[164:167] offset:3072\n"
"v_mfma_f32_16x16x32_bf16 v[152:155], a[152:155], v[108:111], v[152:155]\n"
"v_mfma_f32_16x16x32_bf16 v[152:155], a[156:159], v[136:139], v[152:155]\n"
"buffer_load_dwordx4 v[164:167], v1, s[4:7], s98 offen offset:1024 sc1\n"
"v_mfma_f32_16x16x32_bf16 v[168:171], a[188:191], v[136:139], 0\n"
"v_mfma_f32_16x16x32_bf16 v[168:171], a[184:187], v[108:111], v[168:171]\n"
"v_mfma_f32_16x16x32_bf16 v[168:171], a[180:183], v[104:107], v[168:171]\n"
"v_mfma_f32_16x16x32_bf16 v[168:171], a[176:179], v[184:187], v[168:171]\n"
"buffer_load_dwordx4 v[172:175], v1, s[4:7], s98 offen offset:2048 sc1\n"
"v_mfma_f32_16x16x32_bf16 v[168:171], a[172:175], v[100:103], v[168:171]\n"
"s_nop 0\n"
"v_cmp_u_f32_e32 vcc, v152, v152\n"
"v_mfma_f32_16x16x32_bf16 v[168:171], a[168:171], v[180:183], v[168:171]\n"
"s_cbranch_vccnz .Lmk_p31s\n"
".Lmk_p31r:\n"
"v_mfma_f32_16x16x32_bf16 v[168:171], a[164:167], v[128:131], v[168:171]\n"
"ds_write_b128 v3, v[152:155] offset:6144\n"
"v_mfma_f32_16x16x32_bf16 v[168:171], a[160:163], v[216:219], v[168:171]\n"
"buffer_load_dwordx4 v[152:155], v1, s[4:7], s98 offen offset:3072 sc1\n"
"v_mfma_f32_16x16x32_bf16 v[176:179], a[192:195], v[216:219], 0\n"
"v_mfma_f32_16x16x32_bf16 v[176:179], a[196:199], v[128:131], v[176:179]\n"
"v_mfma_f32_16x16x32_bf16 v[176:179], a[200:203], v[180:183], v[176:179]\n"
"v_mfma_f32_16x16x32_bf16 v[176:179], a[204:207], v[100:103], v[176:179]\n"
"v_mfma_f32_16x16x32_bf16 v[176:179], a[208:211], v[184:187], v[176:179]\n"
"ds_read_b128 v[188:191], v2 offset:8192\n"
"ds_read_b128 v[192:195], v2 offset:9216\n"
"v_mfma_f32_16x16x32_bf16 v[176:179], a[212:215], v[104:107], v[176:179]\n"
"ds_write_b128 v3, v[168:171] offset:7168\n"
"v_mfma_f32_16x16x32_bf16 v[176:179], a[216:219], v[108:111], v[176:179]\n"
"v_mfma_f32_16x16x32_bf16 v[176:179], a[220:223], v[136:139], v[176:179]\n"
"v_mfma_f32_16x16x32_bf16 v[168:171], a[252:255], v[136:139], 0\n"
"s_mov_b32 s98, .Lmk_b0-.Lmk_g34\n"
"s_getpc_b64 s[84:85]\n"
".Lmk_g34:\n"
"s_add_u32 s84, s84, s98\n"
"s_addc_u32 s85, s85, 0\n"
"s_and_b32 s84, s84, -16\n"
"v_mfma_f32_16x16x32_bf16 v[168:171], a[248:251], v[108:111], v[168:171]\n"
"s_and_b32 s85, s85, 0xffff\n"
"s_sub_u32 s98, .Lmk_end-.Lmk_g34, s98\n"
"s_min_u32 s86, s98, 0x3000\n"
"s_mov_b32 s87, 0x20000\n"
"v_mfma_f32_16x16x32_bf16 v[168:171], a[244:247], v[104:107], v[168:171]\n"
"s_mov_b32 m0, 0x27800\n"
"s_nop 0\n"
"buffer_load_dwordx4 v0, s[84:87], s99 offen lds\n"
"v_mfma_f32_16x16x32_bf16 v[168:171], a[240:243], v[184:187], v[168:171]\n"
"s_waitcnt lgkmcnt(1)\n"
"v_lshlrev_b32_e32 v255, 16, v188\n"
"v_and_b32_e32 v254, 0xffff0000, v188\n"
"v_mfma_f32_16x16x32_bf16 v[168:171], a[236:239], v[100:103], v[168:171]\n"
"v_lshlrev_b32_e32 v253, 16, v189\n"
"v_and_b32_e32 v252, 0xffff0000, v189\n"
"v_mfma_f32_16x16x32_bf16 v[168:171], a[232:235], v[180:183], v[168:171]\n"
"ds_write_b128 v3, v[176:179] offset:8192\n"
"v_mfma_f32_16x16x32_bf16 v[168:171], a[228:231], v[128:131], v[168:171]\n"
"v_lshlrev_b32_e32 v251, 16, v190\n"
"v_and_b32_e32 v250, 0xffff0000, v190\n"
"v_mfma_f32_16x16x32_bf16 v[168:171], a[224:227], v[216:219], v[168:171]\n"
"s_waitcnt vmcnt(15)\n"
"v_lshlrev_b32_e32 v249, 16, v191\n"
"v_and_b32_e32 v248, 0xffff0000, v191\n"
"; @A G2 b.2\n"
"v_mfma_f32_16x16x32_bf16 v[100:103], a[128:131], v[144:147], 0\n"
"v_lshlrev_b32_e32 v247, 16, v192\n"
"v_and_b32_e32 v246, 0xffff0000, v192\n"
"v_mfma_f32_16x16x32_bf16 v[100:103], a[132:135], v[140:143], v[100:103]\n"
"v_lshlrev_b32_e32 v245, 16, v193\n"
"v_and_b32_e32 v244, 0xffff0000, v193\n"
"v_mfma_f32_16x16x32_bf16 v[100:103], a[136:139], v[116:119], v[100:103]\n"
"v_lshlrev_b32_e32 v243, 16, v194\n"
"v_and_b32_e32 v242, 0xffff0000, v194\n"
"v_mfma_f32_16x16x32_bf16 v[100:103], a[140:143], v[112:115], v[100:103]\n"
"s_waitcnt vmcnt(9)\n"
"v_lshlrev_b32_e32 v241, 16, v195\n"
"v_and_b32_e32 v240, 0xffff0000, v195\n"
"v_mfma_f32_16x16x32_bf16 v[100:103], a[144:147], v[92:95], v[100:103]\n"
"v_mfma_f32_16x16x32_bf16 v[100:103], a[148:151], v[96:99], v[100:103]\n"
"ds_write_b128 v3, v[168:171] offset:9216\n"
"v_mfma_f32_16x16x32_bf16 v[100:103], a[152:155], v[148:151], v[100:103]\n"
"v_mfma_f32_16x16x32_bf16 v[100:103], a[156:159], v[88:91], v[100:103]\n"
"v_mfma_f32_16x16x32_bf16 v[104:107], a[188:191], v[88:91], 0\n"
"v_mfma_f32_16x16x32_bf16 v[104:107], a[184:187], v[148:151], v[104:107]\n"
"s_waitcnt lgkmcnt(0)\n"
"s_barrier\n"
"v_mfma_f32_16x16x32_bf16 v[104:107], a[180:183], v[96:99], v[104:107]\n"
"ds_read_b128 v[108:111], v4\n"
"ds_read_b128 v[128:131], v5\n"
"v_mfma_f32_16x16x32_bf16 v[104:107], a[176:179], v[92:95], v[104:107]\n"
"v_mfma_f32_16x16x32_bf16 v[104:107], a[172:175], v[112:115], v[104:107]\n"
"s_add_u32 s98, s56, 0\n"
"v_cmp_u_f32_e32 vcc, v100, v100\n"
"v_mfma_f32_16x16x32_bf16 v[104:107], a[168:171], v[116:119], v[104:107]\n"
"s_cbranch_vccnz .Lmk_p32s\n"
".Lmk_p32r:\n"
"v_mfma_f32_16x16x32_bf16 v[104:107], a[164:167], v[140:143], v[104:107]\n"
"ds_write_b128 v3, v[100:103] offset:12288\n"
"v_mfma_f32_16x16x32_bf16 v[104:107], a[160:163], v[144:147], v[104:107]\n"
"buffer_load_dwordx4 v[100:103], v0, s[4:7], s98 offen sc1\n"
"v_mfma_f32_16x16x32_bf16 v[136:139], a[192:195], v[144:147], 0\n"
"ds_read_b128 v[168:171], v4 offset:1024\n"
"ds_read_b128 v[176:179], v5 offset:1024\n"
"v_mfma_f32_16x16x32_bf16 v[136:139], a[196:199], v[140:143], v[136:139]\n"
"s_waitcnt lgkmcnt(3)\n"
"v_add_f32_e32 v108, v108, v255\n"
"v_add_f32_e32 v109, v109, v254\n"
"v_mfma_f32_16x16x32_bf16 v[136:139], a[200:203], v[116:119], v[136:139]\n"
"buffer_load_dwordx4 v[180:183], v0, s[4:7], s98 offen offset:1024 sc1\n"
"v_mfma_f32_16x16x32_bf16 v[136:139], a[204:207], v[112:115], v[136:139]\n"
"v_add_f32_e32 v110, v110, v253\n"
"v_add_f32_e32 v111, v111, v252\n"
"v_mfma_f32_16x16x32_bf16 v[136:139], a[208:211], v[92:95], v[136:139]\n"
"v_add_f32_e32 v108, v108, v128\n"
"v_add_f32_e32 v109, v109, v129\n"
"v_mfma_f32_16x16x32_bf16 v[136:139], a[212:215], v[96:99], v[136:139]\n"
"ds_write_b128 v3, v[104:107] offset:13312\n"
"v_mfma_f32_16x16x32_bf16 v[136:139], a[216:219], v[148:151], v[136:139]\n"
"buffer_load_dwordx4 v[104:107], v0, s[4:7], s98 offen offset:2048 sc1\n"
"v_mfma_f32_16x16x32_bf16 v[136:139], a[220:223], v[88:91], v[136:139]\n"
"ds_read_b128 v[184:187], v4 offset:2048\n"
"ds_read_b128 v[188:191], v5 offset:2048\n"
"v_mfma_f32_16x16x32_bf16 v[192:195], a[252:255], v[88:91], 0\n"
"s_waitcnt lgkmcnt(3)\n"
"v_add_f32_e32 v110, v110, v130\n"
"v_add_f32_e32 v111, v111, v131\n"
"v_mfma_f32_16x16x32_bf16 v[192:195], a[248:251], v[148:151], v[192:195]\n"
"buffer_load_dwordx4 v[88:91], v0, s[4:7], s98 offen offset:3072 sc1\n"
"v_mfma_f32_16x16x32_bf16 v[192:195], a[244:247], v[96:99], v[192:195]\n"
"v_add_f32_e32 v168, v168, v251\n"
"v_add_f32_e32 v169, v169, v250\n"
"v_mfma_f32_16x16x32_bf16 v[192:195], a[240:243], v[92:95], v[192:195]\n"
"v_add_f32_e32 v170, v170, v249\n"
"v_add_f32_e32 v171, v171, v248\n"
"v_mfma_f32_16x16x32_bf16 v[192:195], a[236:239], v[112:115], v[192:195]\n"
"buffer_load_dwordx4 v[92:95], v1, s[4:7], s98 offen sc1\n"
"v_mfma_f32_16x16x32_bf16 v[192:195], a[232:235], v[116:119], v[192:195]\n"
"ds_write_b128 v3, v[136:139] offset:14336\n"
"v_mfma_f32_16x16x32_bf16 v[192:195], a[228:231], v[140:143], v[192:195]\n"
"ds_read_b128 v[96:99], v4 offset:3072\n"
"ds_read_b128 v[112:115], v5 offset:3072\n"
"v_mfma_f32_16x16x32_bf16 v[192:195], a[224:227], v[144:147], v[192:195]\n"
"s_waitcnt vmcnt(10)\n"
"buffer_load_dwordx4 v[116:119], v1, s[4:7], s98 offen offset:1024 sc1\n"
"; @A G2 b.3\n"
"v_mfma_f32_16x16x32_bf16 v[128:131], a[128:131], v[160:163], v[108:111]\n"
"v_add_f32_e32 v168, v168, v176\n"
"v_add_f32_e32 v169, v169, v177\n"
"v_mfma_f32_16x16x32_bf16 v[128:131], a[132:135], v[124:127], v[128:131]\n"
"s_waitcnt lgkmcnt(3)\n"
"v_add_f32_e32 v170, v170, v178\n"
"v_add_f32_e32 v171, v171, v179\n"
"v_mfma_f32_16x16x32_bf16 v[128:131], a[136:139], v[120:123], v[128:131]\n"
"buffer_load_dwordx4 v[136:139], v1, s[4:7], s98 offen offset:2048 sc1\n"
"v_mfma_f32_16x16x32_bf16 v[128:131], a[140:143], v[132:135], v[128:131]\n"
"s_waitcnt vmcnt(8)\n"
"v_add_f32_e32 v184, v184, v247\n"
"v_add_f32_e32 v185, v185, v246\n"
"v_mfma_f32_16x16x32_bf16 v[128:131], a[144:147], v[156:159], v[128:131]\n"
"v_add_f32_e32 v186, v186, v245\n"
"v_add_f32_e32 v187, v187, v244\n"
"v_mfma_f32_16x16x32_bf16 v[128:131], a[148:151], v[164:167], v[128:131]\n"
"ds_write_b128 v3, v[192:195] offset:15360\n"
"v_mfma_f32_16x16x32_bf16 v[128:131], a[152:155], v[172:175], v[128:131]\n"
"buffer_load_dwordx4 v[140:143], v1, s[4:7], s98 offen offset:3072 sc1\n"
"v_mfma_f32_16x16x32_bf16 v[128:131], a[156:159], v[152:155], v[128:131]\n"
"s_add_u32 s98, s57, 0\n"
"v_add_f32_e32 v184, v184, v188\n"
"v_add_f32_e32 v185, v185, v189\n"
"v_mfma_f32_16x16x32_bf16 v[144:147], a[188:191], v[152:155], v[168:171]\n"
"s_waitcnt lgkmcnt(1)\n"
"v_add_f32_e32 v186, v186, v190\n"
"v_add_f32_e32 v187, v187, v191\n"
"v_mfma_f32_16x16x32_bf16 v[144:147], a[184:187], v[172:175], v[144:147]\n"
"s_waitcnt lgkmcnt(0)\n"
"s_barrier\n"
"v_mfma_f32_16x16x32_bf16 v[144:147], a[180:183], v[164:167], v[144:147]\n"
"buffer_load_dwordx4 v[148:151], v0, s[4:7], s98 offen sc1\n"
"v_mfma_f32_16x16x32_bf16 v[144:147], a[176:179], v[156:159], v[144:147]\n"
"v_add_f32_e32 v96, v96, v243\n"
"v_add_f32_e32 v97, v97, v242\n"
"v_mfma_f32_16x16x32_bf16 v[144:147], a[172:175], v[132:135], v[144:147]\n"
"v_cmp_u_f32_e32 vcc, v128, v128\n"
"v_add_f32_e32 v98, v98, v241\n"
"v_mfma_f32_16x16x32_bf16 v[144:147], a[168:171], v[120:123], v[144:147]\n"
"s_cbranch_vccnz .Lmk_p33s\n"
".Lmk_p33r:\n"
"v_add_f32_e32 v99, v99, v240\n"
"v_mfma_f32_16x16x32_bf16 v[144:147], a[164:167], v[124:127], v[144:147]\n"
"buffer_load_dwordx4 v[108:111], v0, s[4:7], s98 offen offset:1024 sc1\n"
"v_mfma_f32_16x16x32_bf16 v[144:147], a[160:163], v[160:163], v[144:147]\n"
"v_add_f32_e32 v96, v96, v112\n"
"v_add_f32_e32 v97, v97, v113\n"
"v_mfma_f32_16x16x32_bf16 v[184:187], a[192:195], v[160:163], v[184:187]\n"
"v_add_f32_e32 v98, v98, v114\n"
"v_add_f32_e32 v99, v99, v115\n"
"v_mfma_f32_16x16x32_bf16 v[184:187], a[196:199], v[124:127], v[184:187]\n"
"buffer_load_dwordx4 v[112:115], v0, s[4:7], s98 offen offset:2048 sc1\n"
"v_mfma_f32_16x16x32_bf16 v[184:187], a[200:203], v[120:123], v[184:187]\n"
"ds_read_b128 v[168:171], v6\n"
"ds_read_b128 v[176:179], v6 offset:1024\n"
"v_mfma_f32_16x16x32_bf16 v[184:187], a[204:207], v[132:135], v[184:187]\n"
"ds_read_b128 v[188:191], v6 offset:2048\n"
"ds_read_b128 v[192:195], v6 offset:3072\n"
"v_mfma_f32_16x16x32_bf16 v[184:187], a[208:211], v[156:159], v[184:187]\n"
"buffer_load_dwordx4 v[196:199], v0, s[4:7], s98 offen offset:3072 sc1\n"
"v_mfma_f32_16x16x32_bf16 v[184:187], a[212:215], v[164:167], v[184:187]\n"
"v_mfma_f32_16x16x32_bf16 v[184:187], a[216:219], v[172:175], v[184:187]\n"
"v_mfma_f32_16x16x32_bf16 v[184:187], a[220:223], v[152:155], v[184:187]\n"
"buffer_load_dwordx4 v[200:203], v1, s[4:7], s98 offen sc1\n"
"v_mfma_f32_16x16x32_bf16 v[96:99], a[252:255], v[152:155], v[96:99]\n"
"s_waitcnt lgkmcnt(0)\n"
"v_add_f32_e32 v168, v128, v168\n"
"v_add_f32_e32 v169, v129, v169\n"
"v_mfma_f32_16x16x32_bf16 v[96:99], a[248:251], v[172:175], v[96:99]\n"
"v_add_f32_e32 v170, v130, v170\n"
"v_add_f32_e32 v171, v131, v171\n"
"v_mfma_f32_16x16x32_bf16 v[96:99], a[244:247], v[164:167], v[96:99]\n"
"buffer_load_dwordx4 v[128:131], v1, s[4:7], s98 offen offset:1024 sc1\n"
"v_mfma_f32_16x16x32_bf16 v[96:99], a[240:243], v[156:159], v[96:99]\n"
"v_add_f32_e32 v176, v144, v176\n"
"v_add_f32_e32 v177, v145, v177\n"
"v_mfma_f32_16x16x32_bf16 v[96:99], a[236:239], v[132:135], v[96:99]\n"
"v_add_f32_e32 v178, v146, v178\n"
"v_add_f32_e32 v179, v147, v179\n"
"v_mfma_f32_16x16x32_bf16 v[96:99], a[232:235], v[120:123], v[96:99]\n"
"buffer_load_dwordx4 v[120:123], v1, s[4:7], s98 offen offset:2048 sc1\n"
"v_mfma_f32_16x16x32_bf16 v[96:99], a[228:231], v[124:127], v[96:99]\n"
"v_cvt_pk_bf16_f32 v124, v168, v169\n"
"v_cvt_pk_bf16_f32 v125, v170, v171\n"
"v_mfma_f32_16x16x32_bf16 v[96:99], a[224:227], v[160:163], v[96:99]\n"
"s_add_u32 s97, s61, 0x440000\n"
"v_cvt_pk_bf16_f32 v126, v176, v177\n"
"v_cvt_pk_bf16_f32 v127, v178, v179\n"
"buffer_store_dwordx4 v[124:127], v0, s[4:7], s97 offen sc1\n"
"ds_write_b128 v2, v[124:127] offset:8192\n"
"buffer_load_dwordx4 v[132:135], v1, s[4:7], s98 offen offset:3072 sc1\n"
"; @A fin b\n"
"s_add_u32 s98, s61, 0x440000\n"
"v_add_f32_e32 v188, v184, v188\n"
"v_add_f32_e32 v189, v185, v189\n"
"v_add_f32_e32 v190, v186, v190\n"
"v_add_f32_e32 v191, v187, v191\n"
"v_add_f32_e32 v192, v96, v192\n"
"v_add_f32_e32 v193, v97, v193\n"
"v_add_f32_e32 v194, v98, v194\n"
"v_add_f32_e32 v195, v99, v195\n"
"v_cvt_pk_bf16_f32 v96, v188, v189\n"
"v_cvt_pk_bf16_f32 v97, v190, v191\n"
"v_cvt_pk_bf16_f32 v98, v192, v193\n"
"v_cvt_pk_bf16_f32 v99, v194, v195\n"
"buffer_store_dwordx4 v[96:99], v0, s[4:7], s98 offen offset:1024 sc1\n"
"s_waitcnt vmcnt(16)\n"
"; @A G2 a.0\n"
"v_mfma_f32_16x16x32_bf16 v[144:147], a[128:131], v[100:103], 0\n"
"s_add_u32 s98, s58, 0\n"
"buffer_load_dwordx4 v[152:155], v0, s[4:7], s98 offen sc1\n"
"v_mfma_f32_16x16x32_bf16 v[144:147], a[132:135], v[180:183], v[144:147]\n"
"s_waitcnt vmcnt(15)\n"
"ds_write_b128 v2, v[96:99] offset:9216\n"
"v_mfma_f32_16x16x32_bf16 v[144:147], a[136:139], v[104:107], v[144:147]\n"
"v_mov_b32_e32 v156, 0x3f803f80\n"
"v_mov_b32_e32 v157, 0x3f803f80\n"
"v_mfma_f32_16x16x32_bf16 v[144:147], a[140:143], v[88:91], v[144:147]\n"
"s_waitcnt vmcnt(13)\n"
"v_mov_b32_e32 v158, 0x3f803f80\n"
"v_mov_b32_e32 v159, 0x3f803f80\n"
"v_mfma_f32_16x16x32_bf16 v[144:147], a[144:147], v[92:95], v[144:147]\n"
"buffer_load_dwordx4 v[160:163], v0, s[4:7], s98 offen offset:1024 sc1\n"
"v_mfma_f32_16x16x32_bf16 v[164:167], v[156:159], v[124:127], 0\n"
"v_mfma_f32_16x16x32_bf16 v[164:167], v[156:159], v[96:99], v[164:167]\n"
"v_mfma_f32_16x16x32_bf16 v[156:159], v[124:127], v[124:127], 0\n"
"v_mfma_f32_16x16x32_bf16 v[156:159], v[96:99], v[96:99], v[156:159]\n"
"v_mfma_f32_16x16x32_bf16 v[144:147], a[148:151], v[116:119], v[144:147]\n"
"s_waitcnt vmcnt(12)\n"
"v_mfma_f32_16x16x32_bf16 v[144:147], a[152:155], v[136:139], v[144:147]\n"
"v_mfma_f32_16x16x32_bf16 v[144:147], a[156:159], v[140:143], v[144:147]\n"
"v_mfma_f32_16x16x32_bf16 v[96:99], a[188:191], v[140:143], 0\n"
"buffer_load_dwordx4 v[124:127], v0, s[4:7], s98 offen offset:2048 sc1\n"
"v_mfma_f32_16x16x32_bf16 v[96:99], a[184:187], v[136:139], v[96:99]\n"
"s_mov_b32 s96, 0xaaaaaaaa\n"
"s_mov_b32 s97, 0xaaaaaaaa\n"
"v_mfma_f32_16x16x32_bf16 v[96:99], a[180:183], v[116:119], v[96:99]\n"
"v_mfma_f32_16x16x32_bf16 v[96:99], a[176:179], v[92:95], v[96:99]\n"
"v_mfma_f32_16x16x32_bf16 v[96:99], a[172:175], v[88:91], v[96:99]\n"
"v_cmp_u_f32_e32 vcc, v144, v144\n"
"v_cndmask_b32_e64 v156, v156, v157, s[96:97]\n"
"v_mfma_f32_16x16x32_bf16 v[96:99], a[168:171], v[104:107], v[96:99]\n"
"s_cbranch_vccnz .Lmk_p35s\n"
".Lmk_p35r:\n"
"v_cndmask_b32_e64 v158, v158, v159, s[96:97]\n"
"v_mfma_f32_16x16x32_bf16 v[96:99], a[164:167], v[180:183], v[96:99]\n"
"ds_write_b128 v3, v[144:147]\n"
"v_mfma_f32_16x16x32_bf16 v[96:99], a[160:163], v[100:103], v[96:99]\n"
"buffer_load_dwordx4 v[144:147], v0, s[4:7], s98 offen offset:3072 sc1\n"
"v_mfma_f32_16x16x32_bf16 v[168:171], a[192:195], v[100:103], 0\n"
"s_mov_b32 s96, 0xcccccccc\n"
"s_mov_b32 s97, 0xcccccccc\n"
"v_mfma_f32_16x16x32_bf16 v[168:171], a[196:199], v[180:183], v[168:171]\n"
"v_cndmask_b32_e64 v165, v156, v158, s[96:97]\n"
"v_mfma_f32_16x16x32_bf16 v[168:171], a[200:203], v[104:107], v[168:171]\n"
"s_mov_b32 s96, 0xf0000f\n"
"s_mov_b32 s97, 0xf0000f00\n"
"v_mfma_f32_16x16x32_bf16 v[168:171], a[204:207], v[88:91], v[168:171]\n"
"buffer_load_dwordx4 v[156:159], v1, s[4:7], s98 offen sc1\n"
"v_mfma_f32_16x16x32_bf16 v[168:171], a[208:211], v[92:95], v[168:171]\n"
"v_cndmask_b32_e64 v165, 0, v165, s[96:97]\n"
"v_mov_b32_e32 v255, v165\n"
"v_mfma_f32_16x16x32_bf16 v[168:171], a[212:215], v[116:119], v[168:171]\n"
"ds_write_b128 v3, v[96:99] offset:1024\n"
"v_mfma_f32_16x16x32_bf16 v[168:171], a[216:219], v[136:139], v[168:171]\n"
"v_permlane16_swap_b32_e32 v165, v255\n"
"v_add_f32_e32 v165, v165, v255\n"
"v_mfma_f32_16x16x32_bf16 v[168:171], a[220:223], v[140:143], v[168:171]\n"
"buffer_load_dwordx4 v[172:175], v1, s[4:7], s98 offen offset:1024 sc1\n"
"v_mfma_f32_16x16x32_bf16 v[96:99], a[252:255], v[140:143], 0\n"
"v_mov_b32_e32 v255, v165\n"
"v_mfma_f32_16x16x32_bf16 v[96:99], a[248:251], v[136:139], v[96:99]\n"
"s_mov_b32 s96, 0xffff0000\n"
"v_permlane32_swap_b32_e32 v165, v255\n"
"v_add_f32_e32 v165, v165, v255\n"
"v_mfma_f32_16x16x32_bf16 v[96:99], a[244:247], v[116:119], v[96:99]\n"
"v_cndmask_b32_e64 v164, v164, v165, s[96:97]\n"
"s_add_u32 s97, s63, 0x44000\n"
"v_mfma_f32_16x16x32_bf16 v[96:99], a[240:243], v[92:95], v[96:99]\n"
"buffer_load_dwordx4 v[116:119], v1, s[4:7], s98 offen offset:2048 sc1\n"
"v_mfma_f32_16x16x32_bf16 v[96:99], a[236:239], v[88:91], v[96:99]\n"
"buffer_store_dword v164, v14, s[4:7], s97 offen sc1\n"
"v_mfma_f32_16x16x32_bf16 v[96:99], a[232:235], v[104:107], v[96:99]\n"
"ds_write_b128 v3, v[168:171] offset:2048\n"
"v_mfma_f32_16x16x32_bf16 v[96:99], a[228:231], v[180:183], v[96:99]\n"
"v_mfma_f32_16x16x32_bf16 v[96:99], a[224:227], v[100:103], v[96:99]\n"
"s_waitcnt vmcnt(14)\n"
"buffer_load_dwordx4 v[136:139], v1, s[4:7], s98 offen offset:3072 sc1\n"
"; @A G2 a.1\n"
"v_mfma_f32_16x16x32_bf16 v[88:91], a[128:131], v[148:151], 0\n"
"v_mfma_f32_16x16x32_bf16 v[88:91], a[132:135], v[108:111], v[88:91]\n"
"s_add_u32 s98, s59, 0\n"
"v_mfma_f32_16x16x32_bf16 v[88:91], a[136:139], v[112:115], v[88:91]\n"
"v_mfma_f32_16x16x32_bf16 v[88:91], a[140:143], v[196:199], v[88:91]\n"
"s_waitcnt vmcnt(10)\n"
"buffer_load_dwordx4 v[176:179], v0, s[4:7], s98 offen sc1\n"
"v_mfma_f32_16x16x32_bf16 v[88:91], a[144:147], v[200:203], v[88:91]\n"
"v_mfma_f32_16x16x32_bf16 v[88:91], a[148:151], v[128:131], v[88:91]\n"
"ds_write_b128 v3, v[96:99] offset:3072\n"
"v_mfma_f32_16x16x32_bf16 v[88:91], a[152:155], v[120:123], v[88:91]\n"
"v_mfma_f32_16x16x32_bf16 v[88:91], a[156:159], v[132:135], v[88:91]\n"
"buffer_load_dwordx4 v[180:183], v0, s[4:7], s98 offen offset:1024 sc1\n"
"v_mfma_f32_16x16x32_bf16 v[92:95], a[188:191], v[132:135], 0\n"
"v_mfma_f32_16x16x32_bf16 v[92:95], a[184:187], v[120:123], v[92:95]\n"
"v_mfma_f32_16x16x32_bf16 v[92:95], a[180:183], v[128:131], v[92:95]\n"
"v_mfma_f32_16x16x32_bf16 v[92:95], a[176:179], v[200:203], v[92:95]\n"
"buffer_load_dwordx4 v[168:171], v0, s[4:7], s98 offen offset:2048 sc1\n"
"v_mfma_f32_16x16x32_bf16 v[92:95], a[172:175], v[196:199], v[92:95]\n"
"s_nop 0\n"
"v_cmp_u_f32_e32 vcc, v88, v88\n"
"v_mfma_f32_16x16x32_bf16 v[92:95], a[168:171], v[112:115], v[92:95]\n"
"s_cbranch_vccnz .Lmk_p36s\n"
".Lmk_p36r:\n"
"v_mfma_f32_16x16x32_bf16 v[92:95], a[164:167], v[108:111], v[92:95]\n"
"ds_write_b128 v3, v[88:91] offset:6144\n"
"v_mfma_f32_16x16x32_bf16 v[92:95], a[160:163], v[148:151], v[92:95]\n"
"buffer_load_dwordx4 v[184:187], v0, s[4:7], s98 offen offset:3072 sc1\n"
"v_mfma_f32_16x16x32_bf16 v[88:91], a[192:195], v[148:151], 0\n"
"v_mfma_f32_16x16x32_bf16 v[88:91], a[196:199], v[108:111], v[88:91]\n"
"v_mfma_f32_16x16x32_bf16 v[88:91], a[200:203], v[112:115], v[88:91]\n"
"v_mfma_f32_16x16x32_bf16 v[88:91], a[204:207], v[196:199], v[88:91]\n"
"buffer_load_dwordx4 v[188:191], v1, s[4:7], s98 offen sc1\n"
"v_mfma_f32_16x16x32_bf16 v[88:91], a[208:211], v[200:203], v[88:91]\n"
"ds_read_b128 v[96:99], v2\n"
"ds_read_b128 v[100:103], v2 offset:1024\n"
"v_mfma_f32_16x16x32_bf16 v[88:91], a[212:215], v[128:131], v[88:91]\n"
"ds_write_b128 v3, v[92:95] offset:7168\n"
"v_mfma_f32_16x16x32_bf16 v[88:91], a[216:219], v[120:123], v[88:91]\n"
"v_mfma_f32_16x16x32_bf16 v[88:91], a[220:223], v[132:135], v[88:91]\n"
"buffer_load_dwordx4 v[164:167], v1, s[4:7], s98 offen offset:1024 sc1\n"
"v_mfma_f32_16x16x32_bf16 v[92:95], a[252:255], v[132:135], 0\n"
"v_mfma_f32_16x16x32_bf16 v[92:95], a[248:251], v[120:123], v[92:95]\n"
"s_waitcnt lgkmcnt(1)\n"
"v_lshlrev_b32_e32 v255, 16, v96\n"
"v_and_b32_e32 v254, 0xffff0000, v96\n"
"v_mfma_f32_16x16x32_bf16 v[92:95], a[244:247], v[128:131], v[92:95]\n"
"v_lshlrev_b32_e32 v253, 16, v97\n"
"v_and_b32_e32 v252, 0xffff0000, v97\n"
"v_mfma_f32_16x16x32_bf16 v[92:95], a[240:243], v[200:203], v[92:95]\n"
"buffer_load_dwordx4 v[192:195], v1, s[4:7], s98 offen offset:2048 sc1\n"
"v_mfma_f32_16x16x32_bf16 v[92:95], a[236:239], v[196:199], v[92:95]\n"
"v_lshlrev_b32_e32 v251, 16, v98\n"
"v_and_b32_e32 v250, 0xffff0000, v98\n"
"v_mfma_f32_16x16x32_bf16 v[92:95], a[232:235], v[112:115], v[92:95]\n"
"ds_write_b128 v3, v[88:91] offset:8192\n"
"v_mfma_f32_16x16x32_bf16 v[92:95], a[228:231], v[108:111], v[92:95]\n"
"v_lshlrev_b32_e32 v249, 16, v99\n"
"v_and_b32_e32 v248, 0xffff0000, v99\n"
"v_mfma_f32_16x16x32_bf16 v[92:95], a[224:227], v[148:151], v[92:95]\n"
"s_waitcnt vmcnt(12)\n"
"buffer_load_dwordx4 v[196:199], v1, s[4:7], s98 offen offset:3072 sc1\n"
"; @A G2 a.2\n"
"v_mfma_f32_16x16x32_bf16 v[96:99], a[128:131], v[152:155], 0\n"
"v_lshlrev_b32_e32 v247, 16, v100\n"
"v_and_b32_e32 v246, 0xffff0000, v100\n"
"v_mfma_f32_16x16x32_bf16 v[96:99], a[132:135], v[160:163], v[96:99]\n"
"v_lshlrev_b32_e32 v245, 16, v101\n"
"v_and_b32_e32 v244, 0xffff0000, v101\n"
"v_mfma_f32_16x16x32_bf16 v[96:99], a[136:139], v[124:127], v[96:99]\n"
"v_lshlrev_b32_e32 v243, 16, v102\n"
"v_and_b32_e32 v242, 0xffff0000, v102\n"
"v_mfma_f32_16x16x32_bf16 v[96:99], a[140:143], v[144:147], v[96:99]\n"
"s_waitcnt vmcnt(8)\n"
"v_lshlrev_b32_e32 v241, 16, v103\n"
"v_and_b32_e32 v240, 0xffff0000, v103\n"
"v_mfma_f32_16x16x32_bf16 v[96:99], a[144:147], v[156:159], v[96:99]\n"
"v_mfma_f32_16x16x32_bf16 v[96:99], a[148:151], v[172:175], v[96:99]\n"
"ds_write_b128 v3, v[92:95] offset:9216\n"
"v_mfma_f32_16x16x32_bf16 v[96:99], a[152:155], v[116:119], v[96:99]\n"
"v_mfma_f32_16x16x32_bf16 v[96:99], a[156:159], v[136:139], v[96:99]\n"
"v_mfma_f32_16x16x32_bf16 v[104:107], a[188:191], v[136:139], 0\n"
"s_add_u32 s98, s52, 0x440000\n"
"buffer_load_dwordx4 v[88:91], v0, s[4:7], s98 offen sc1\n"
"v_mfma_f32_16x16x32_bf16 v[104:107], a[184:187], v[116:119], v[104:107]\n"
"s_waitcnt lgkmcnt(0)\n"
"s_barrier\n"
"v_mfma_f32_16x16x32_bf16 v[104:107], a[180:183], v[172:175], v[104:107]\n"
"ds_read_b128 v[140:143], v4\n"
"ds_read_b128 v[112:115], v5\n"
"v_mfma_f32_16x16x32_bf16 v[104:107], a[176:179], v[156:159], v[104:107]\n"
"buffer_load_dwordx4 v[92:95], v0, s[4:7], s98 offen offset:1024 sc1\n"
"v_mfma_f32_16x16x32_bf16 v[104:107], a[172:175], v[144:147], v[104:107]\n"
"v_cmp_u_f32_e32 vcc, v96, v96\n"
"v_mfma_f32_16x16x32_bf16 v[104:107], a[168:171], v[124:127], v[104:107]\n"
"s_cbranch_vccnz .Lmk_p37s\n"
".Lmk_p37r:\n"
"v_mfma_f32_16x16x32_bf16 v[104:107], a[164:167], v[160:163], v[104:107]\n"
"ds_write_b128 v3, v[96:99] offset:12288\n"
"v_mfma_f32_16x16x32_bf16 v[104:107], a[160:163], v[152:155], v[104:107]\n"
"buffer_load_dwordx4 v[96:99], v0, s[4:7], s98 offen offset:2048 sc1\n"
"v_mfma_f32_16x16x32_bf16 v[120:123], a[192:195], v[152:155], 0\n"
"ds_read_b128 v[148:151], v4 offset:1024\n"
"ds_read_b128 v[128:131], v5 offset:1024\n"
"v_mfma_f32_16x16x32_bf16 v[120:123], a[196:199], v[160:163], v[120:123]\n"
"s_waitcnt lgkmcnt(3)\n"
"v_add_f32_e32 v140, v140, v255\n"
"v_add_f32_e32 v141, v141, v254\n"
"v_mfma_f32_16x16x32_bf16 v[120:123], a[200:203], v[124:127], v[120:123]\n"
"buffer_load_dwordx4 v[100:103], v0, s[4:7], s98 offen offset:3072 sc1\n"
"v_mfma_f32_16x16x32_bf16 v[120:123], a[204:207], v[144:147], v[120:123]\n"
"v_add_f32_e32 v142, v142, v253\n"
"v_add_f32_e32 v143, v143, v252\n"
"v_mfma_f32_16x16x32_bf16 v[120:123], a[208:211], v[156:159], v[120:123]\n"
"v_add_f32_e32 v140, v140, v112\n"
"v_add_f32_e32 v141, v141, v113\n"
"v_mfma_f32_16x16x32_bf16 v[120:123], a[212:215], v[172:175], v[120:123]\n"
"ds_write_b128 v3, v[104:107] offset:13312\n"
"v_mfma_f32_16x16x32_bf16 v[120:123], a[216:219], v[116:119], v[120:123]\n"
"buffer_load_dwordx4 v[104:107], v1, s[4:7], s98 offen sc1\n"
"v_mfma_f32_16x16x32_bf16 v[120:123], a[220:223], v[136:139], v[120:123]\n"
"ds_read_b128 v[108:111], v4 offset:2048\n"
"ds_read_b128 v[200:203], v5 offset:2048\n"
"v_mfma_f32_16x16x32_bf16 v[132:135], a[252:255], v[136:139], 0\n"
"s_waitcnt lgkmcnt(3)\n"
"v_add_f32_e32 v142, v142, v114\n"
"v_add_f32_e32 v143, v143, v115\n"
"v_mfma_f32_16x16x32_bf16 v[132:135], a[248:251], v[116:119], v[132:135]\n"
"buffer_load_dwordx4 v[112:115], v1, s[4:7], s98 offen offset:1024 sc1\n"
"v_mfma_f32_16x16x32_bf16 v[132:135], a[244:247], v[172:175], v[132:135]\n"
"v_add_f32_e32 v148, v148, v251\n"
"v_add_f32_e32 v149, v149, v250\n"
"v_mfma_f32_16x16x32_bf16 v[132:135], a[240:243], v[156:159], v[132:135]\n"
"v_add_f32_e32 v150, v150, v249\n"
"v_add_f32_e32 v151, v151, v248\n"
"v_mfma_f32_16x16x32_bf16 v[132:135], a[236:239], v[144:147], v[132:135]\n"
"buffer_load_dwordx4 v[116:119], v1, s[4:7], s98 offen offset:2048 sc1\n"
"v_mfma_f32_16x16x32_bf16 v[132:135], a[232:235], v[124:127], v[132:135]\n"
"ds_write_b128 v3, v[120:123] offset:14336\n"
"v_mfma_f32_16x16x32_bf16 v[132:135], a[228:231], v[160:163], v[132:135]\n"
"ds_read_b128 v[120:123], v4 offset:3072\n"
"ds_read_b128 v[144:147], v5 offset:3072\n"
"v_mfma_f32_16x16x32_bf16 v[132:135], a[224:227], v[152:155], v[132:135]\n"
"s_waitcnt vmcnt(11)\n"
"buffer_load_dwordx4 v[124:127], v1, s[4:7], s98 offen offset:3072 sc1\n"
"; @A G2 a.3\n"
"v_mfma_f32_16x16x32_bf16 v[172:175], a[128:131], v[176:179], v[140:143]\n"
"s_add_u32 s98, s53, 0x440000\n"
"v_add_f32_e32 v148, v148, v128\n"
"v_add_f32_e32 v149, v149, v129\n"
"v_mfma_f32_16x16x32_bf16 v[172:175], a[132:135], v[180:183], v[172:175]\n"
"s_waitcnt lgkmcnt(3)\n"
"v_add_f32_e32 v150, v150, v130\n"
"v_add_f32_e32 v151, v151, v131\n"
"v_mfma_f32_16x16x32_bf16 v[172:175], a[136:139], v[168:171], v[172:175]\n"
"buffer_load_dwordx4 v[128:131], v0, s[4:7], s98 offen sc1\n"
"v_mfma_f32_16x16x32_bf16 v[172:175], a[140:143], v[184:187], v[172:175]\n"
"s_waitcnt vmcnt(9)\n"
"v_add_f32_e32 v108, v108, v247\n"
"v_add_f32_e32 v109, v109, v246\n"
"v_mfma_f32_16x16x32_bf16 v[172:175], a[144:147], v[188:191], v[172:175]\n"
"v_add_f32_e32 v110, v110, v245\n"
"v_add_f32_e32 v111, v111, v244\n"
"v_mfma_f32_16x16x32_bf16 v[172:175], a[148:151], v[164:167], v[172:175]\n"
"ds_write_b128 v3, v[132:135] offset:15360\n"
"v_mfma_f32_16x16x32_bf16 v[172:175], a[152:155], v[192:195], v[172:175]\n"
"buffer_load_dwordx4 v[132:135], v0, s[4:7], s98 offen offset:1024 sc1\n"
"v_mfma_f32_16x16x32_bf16 v[172:175], a[156:159], v[196:199], v[172:175]\n"
"v_add_f32_e32 v108, v108, v200\n"
"v_add_f32_e32 v109, v109, v201\n"
"v_mfma_f32_16x16x32_bf16 v[204:207], a[188:191], v[196:199], v[148:151]\n"
"s_waitcnt lgkmcnt(1)\n"
"v_add_f32_e32 v110, v110, v202\n"
"v_add_f32_e32 v111, v111, v203\n"
"v_mfma_f32_16x16x32_bf16 v[204:207], a[184:187], v[192:195], v[204:207]\n"
"s_waitcnt lgkmcnt(0)\n"
"s_barrier\n"
"v_mfma_f32_16x16x32_bf16 v[204:207], a[180:183], v[164:167], v[204:207]\n"
"buffer_load_dwordx4 v[136:139], v0, s[4:7], s98 offen offset:2048 sc1\n"
"v_mfma_f32_16x16x32_bf16 v[204:207], a[176:179], v[188:191], v[204:207]\n"
"v_add_f32_e32 v120, v120, v243\n"
"v_add_f32_e32 v121, v121, v242\n"
"v_mfma_f32_16x16x32_bf16 v[204:207], a[172:175], v[184:187], v[204:207]\n"
"v_cmp_u_f32_e32 vcc, v172, v172\n"
"v_add_f32_e32 v122, v122, v241\n"
"v_mfma_f32_16x16x32_bf16 v[204:207], a[168:171], v[168:171], v[204:207]\n"
"s_cbranch_vccnz .Lmk_p38s\n"
".Lmk_p38r:\n"
"v_add_f32_e32 v123, v123, v240\n"
"v_mfma_f32_16x16x32_bf16 v[204:207], a[164:167], v[180:183], v[204:207]\n"
"buffer_load_dwordx4 v[140:143], v0, s[4:7], s98 offen offset:3072 sc1\n"
"v_mfma_f32_16x16x32_bf16 v[204:207], a[160:163], v[176:179], v[204:207]\n"
"v_add_f32_e32 v120, v120, v144\n"
"v_add_f32_e32 v121, v121, v145\n"
"v_mfma_f32_16x16x32_bf16 v[108:111], a[192:195], v[176:179], v[108:111]\n"
"v_add_f32_e32 v122, v122, v146\n"
"v_add_f32_e32 v123, v123, v147\n"
"v_mfma_f32_16x16x32_bf16 v[108:111], a[196:199], v[180:183], v[108:111]\n"
"buffer_load_dwordx4 v[144:147], v1, s[4:7], s98 offen sc1\n"
"v_mfma_f32_16x16x32_bf16 v[108:111], a[200:203], v[168:171], v[108:111]\n"
"ds_read_b128 v[200:203], v6\n"
"ds_read_b128 v[208:211], v6 offset:1024\n"
"v_mfma_f32_16x16x32_bf16 v[108:111], a[204:207], v[184:187], v[108:111]\n"
"ds_read_b128 v[148:151], v6 offset:2048\n"
"ds_read_b128 v[152:155], v6 offset:3072\n"
"v_mfma_f32_16x16x32_bf16 v[108:111], a[208:211], v[188:191], v[108:111]\n"
"buffer_load_dwordx4 v[156:159], v1, s[4:7], s98 offen offset:1024 sc1\n"
"v_mfma_f32_16x16x32_bf16 v[108:111], a[212:215], v[164:167], v[108:111]\n"
"v_mfma_f32_16x16x32_bf16 v[108:111], a[216:219], v[192:195], v[108:111]\n"
"v_mfma_f32_16x16x32_bf16 v[108:111], a[220:223], v[196:199], v[108:111]\n"
"buffer_load_dwordx4 v[160:163], v1, s[4:7], s98 offen offset:2048 sc1\n"
"v_mfma_f32_16x16x32_bf16 v[120:123], a[252:255], v[196:199], v[120:123]\n"
"s_waitcnt lgkmcnt(0)\n"
"v_add_f32_e32 v200, v172, v200\n"
"v_add_f32_e32 v201, v173, v201\n"
"v_mfma_f32_16x16x32_bf16 v[120:123], a[248:251], v[192:195], v[120:123]\n"
"v_add_f32_e32 v202, v174, v202\n"
"v_add_f32_e32 v203, v175, v203\n"
"v_mfma_f32_16x16x32_bf16 v[120:123], a[244:247], v[164:167], v[120:123]\n"
"buffer_load_dwordx4 v[164:167], v1, s[4:7], s98 offen offset:3072 sc1\n"
"v_mfma_f32_16x16x32_bf16 v[120:123], a[240:243], v[188:191], v[120:123]\n"
"v_add_f32_e32 v208, v204, v208\n"
"v_add_f32_e32 v209, v205, v209\n"
"v_mfma_f32_16x16x32_bf16 v[120:123], a[236:239], v[184:187], v[120:123]\n"
"v_add_f32_e32 v210, v206, v210\n"
"v_add_f32_e32 v211, v207, v211\n"
"v_mfma_f32_16x16x32_bf16 v[120:123], a[232:235], v[168:171], v[120:123]\n"
"v_cvt_pk_bf16_f32 v168, v200, v201\n"
"v_cvt_pk_bf16_f32 v169, v202, v203\n"
"v_mfma_f32_16x16x32_bf16 v[120:123], a[228:231], v[180:183], v[120:123]\n"
"s_add_u32 s98, s61, 0x400000\n"
"v_cvt_pk_bf16_f32 v170, v208, v209\n"
"v_cvt_pk_bf16_f32 v171, v210, v211\n"
"v_mfma_f32_16x16x32_bf16 v[120:123], a[224:227], v[176:179], v[120:123]\n"
"buffer_store_dwordx4 v[168:171], v0, s[4:7], s98 offen sc1\n"
"ds_write_b128 v2, v[168:171]\n"
".Lmk_loop:\n"
".Lmk_b0:\n"
"s_waitcnt vmcnt(13)\n"
"; @B G1 0.0\n"
"v_mfma_f32_16x16x32_bf16 v[172:175], a[0:3], v[88:91], 0\n"
"v_mfma_f32_16x16x32_bf16 v[172:175], a[4:7], v[92:95], v[172:175]\n"
"s_add_u32 s98, s62, 0x44000\n"
"v_mfma_f32_16x16x32_bf16 v[172:175], a[8:11], v[96:99], v[172:175]\n"
"s_add_u32 s97, s54, 0x440000\n"
"buffer_load_dwordx4 v[176:179], v0, s[4:7], s97 offen sc1\n"
"v_mfma_f32_16x16x32_bf16 v[172:175], a[12:15], v[100:103], v[172:175]\n"
"s_waitcnt vmcnt(10)\n"
"v_mfma_f32_16x16x32_bf16 v[172:175], a[16:19], v[104:107], v[172:175]\n"
"buffer_load_dword v255, v10, s[4:7], s98 offen sc1\n"
"v_mfma_f32_16x16x32_bf16 v[172:175], a[20:23], v[112:115], v[172:175]\n"
"v_add_f32_e32 v148, v108, v148\n"
"v_add_f32_e32 v149, v109, v149\n"
"v_mfma_f32_16x16x32_bf16 v[172:175], a[24:27], v[116:119], v[172:175]\n"
"v_add_f32_e32 v150, v110, v150\n"
"v_add_f32_e32 v151, v111, v151\n"
"v_mfma_f32_16x16x32_bf16 v[172:175], a[28:31], v[124:127], v[172:175]\n"
"buffer_load_dwordx4 v[108:111], v0, s[4:7], s97 offen offset:1024 sc1\n"
"v_mfma_f32_16x16x32_bf16 v[180:183], a[60:63], v[124:127], 0\n"
"buffer_load_dword v254, v10, s[4:7], s98 offen offset:256 sc1\n"
"v_mfma_f32_16x16x32_bf16 v[180:183], a[56:59], v[116:119], v[180:183]\n"
"v_add_f32_e32 v152, v120, v152\n"
"v_add_f32_e32 v153, v121, v153\n"
"v_mfma_f32_16x16x32_bf16 v[180:183], a[52:55], v[112:115], v[180:183]\n"
"v_add_f32_e32 v154, v122, v154\n"
"v_add_f32_e32 v155, v123, v155\n"
"v_mfma_f32_16x16x32_bf16 v[180:183], a[48:51], v[104:107], v[180:183]\n"
"buffer_load_dword v253, v10, s[4:7], s98 offen offset:512 sc1\n"
"v_mfma_f32_16x16x32_bf16 v[180:183], a[44:47], v[100:103], v[180:183]\n"
"v_cmp_u_f32_e32 vcc, v172, v172\n"
"v_cvt_pk_bf16_f32 v120, v148, v149\n"
"v_mfma_f32_16x16x32_bf16 v[180:183], a[40:43], v[96:99], v[180:183]\n"
"s_cbranch_vccnz .Lmk_p43s\n"
".Lmk_p43r:\n"
"v_cvt_pk_bf16_f32 v121, v150, v151\n"
"v_mfma_f32_16x16x32_bf16 v[180:183], a[36:39], v[92:95], v[180:183]\n"
"buffer_load_dwordx4 v[148:151], v0, s[4:7], s97 offen offset:2048 sc1\n"
"v_mfma_f32_16x16x32_bf16 v[180:183], a[32:35], v[88:91], v[180:183]\n"
"ds_write_b128 v3, v[172:175]\n"
"v_mfma_f32_16x16x32_bf16 v[172:175], a[64:67], v[88:91], 0\n"
"buffer_load_dword v252, v10, s[4:7], s98 offen offset:768 sc1\n"
"v_mfma_f32_16x16x32_bf16 v[172:175], a[68:71], v[92:95], v[172:175]\n"
"buffer_load_dwordx4 v[184:187], v0, s[4:7], s97 offen offset:3072 sc1\n"
"v_mfma_f32_16x16x32_bf16 v[172:175], a[72:75], v[96:99], v[172:175]\n"
"s_add_u32 s96, s61, 0x400000\n"
"v_cvt_pk_bf16_f32 v122, v152, v153\n"
"v_cvt_pk_bf16_f32 v123, v154, v155\n"
"v_mfma_f32_16x16x32_bf16 v[172:175], a[76:79], v[100:103], v[172:175]\n"
"buffer_load_dword v251, v10, s[4:7], s98 offen offset:1024 sc1\n"
"v_mfma_f32_16x16x32_bf16 v[172:175], a[80:83], v[104:107], v[172:175]\n"
"buffer_store_dwordx4 v[120:123], v0, s[4:7], s96 offen offset:1024 sc1\n"
"v_mfma_f32_16x16x32_bf16 v[172:175], a[84:87], v[112:115], v[172:175]\n"
"ds_write_b128 v3, v[180:183] offset:1024\n"
"v_mfma_f32_16x16x32_bf16 v[172:175], a[88:91], v[116:119], v[172:175]\n"
"buffer_load_dwordx4 v[152:155], v1, s[4:7], s97 offen sc1\n"
"v_mfma_f32_16x16x32_bf16 v[172:175], a[92:95], v[124:127], v[172:175]\n"
"buffer_load_dword v250, v10, s[4:7], s98 offen offset:1280 sc1\n"
"v_mfma_f32_16x16x32_bf16 v[180:183], a[124:127], v[124:127], 0\n"
"ds_write_b128 v2, v[120:123] offset:1024\n"
"v_mfma_f32_16x16x32_bf16 v[180:183], a[120:123], v[116:119], v[180:183]\n"
"v_mov_b32_e32 v188, 0x3f803f80\n"
"v_mov_b32_e32 v189, 0x3f803f80\n"
"v_mfma_f32_16x16x32_bf16 v[180:183], a[116:119], v[112:115], v[180:183]\n"
"buffer_load_dword v249, v10, s[4:7], s98 offen offset:1536 sc1\n"
"v_mfma_f32_16x16x32_bf16 v[180:183], a[112:115], v[104:107], v[180:183]\n"
"buffer_load_dwordx4 v[192:195], v1, s[4:7], s97 offen offset:1024 sc1\n"
"v_mfma_f32_16x16x32_bf16 v[180:183], a[108:111], v[100:103], v[180:183]\n"
"v_mov_b32_e32 v190, 0x3f803f80\n"
"v_mov_b32_e32 v191, 0x3f803f80\n"
"v_mfma_f32_16x16x32_bf16 v[180:183], a[104:107], v[96:99], v[180:183]\n"
"ds_write_b128 v3, v[172:175] offset:2048\n"
"v_mfma_f32_16x16x32_bf16 v[172:175], v[188:191], v[168:171], 0\n"
"v_mfma_f32_16x16x32_bf16 v[172:175], v[188:191], v[120:123], v[172:175]\n"
"v_mfma_f32_16x16x32_bf16 v[188:191], v[168:171], v[168:171], 0\n"
"v_mfma_f32_16x16x32_bf16 v[188:191], v[120:123], v[120:123], v[188:191]\n"
"v_mfma_f32_16x16x32_bf16 v[180:183], a[100:103], v[92:95], v[180:183]\n"
"v_mov_b32_e32 v120, -1\n"
"v_mov_b32_e32 v121, -1\n"
"v_mfma_f32_16x16x32_bf16 v[180:183], a[96:99], v[88:91], v[180:183]\n"
"v_mov_b32_e32 v122, -1\n"
"v_mov_b32_e32 v123, -1\n"
"v_mfma_f32_16x16x32_bf16 v[168:171], v[24:27], v[88:91], 0\n"
"buffer_load_dwordx4 v[196:199], v1, s[4:7], s97 offen offset:2048 sc1\n"
"v_mfma_f32_16x16x32_bf16 v[168:171], v[28:31], v[92:95], v[168:171]\n"
"s_add_u32 s96, s60, 0x448000\n"
"buffer_store_dwordx4 v[120:123], v0, s[4:7], s96 offen sc1\n"
"v_mfma_f32_16x16x32_bf16 v[168:171], v[32:35], v[96:99], v[168:171]\n"
"buffer_load_dword v248, v10, s[4:7], s98 offen offset:1792 sc1\n"
"v_mfma_f32_16x16x32_bf16 v[168:171], v[36:39], v[100:103], v[168:171]\n"
"s_mov_b32 s94, 0xaaaaaaaa\n"
"s_mov_b32 s95, 0xaaaaaaaa\n"
"v_mfma_f32_16x16x32_bf16 v[168:171], v[40:43], v[104:107], v[168:171]\n"
"v_mfma_f32_16x16x32_bf16 v[168:171], v[44:47], v[112:115], v[168:171]\n"
"buffer_load_dwordx4 v[200:203], v1, s[4:7], s97 offen offset:3072 sc1\n"
"v_mfma_f32_16x16x32_bf16 v[168:171], v[48:51], v[116:119], v[168:171]\n"
"ds_write_b128 v3, v[180:183] offset:3072\n"
"v_mfma_f32_16x16x32_bf16 v[168:171], v[52:55], v[124:127], v[168:171]\n"
"v_cndmask_b32_e64 v188, v188, v189, s[94:95]\n"
"v_cndmask_b32_e64 v190, v190, v191, s[94:95]\n"
"v_mfma_f32_16x16x32_bf16 v[180:183], v[84:87], v[124:127], 0\n"
"s_mov_b32 s94, 0xcccccccc\n"
"s_mov_b32 s95, 0xcccccccc\n"
"v_mfma_f32_16x16x32_bf16 v[180:183], v[80:83], v[116:119], v[180:183]\n"
"v_cndmask_b32_e64 v173, v188, v190, s[94:95]\n"
"v_mfma_f32_16x16x32_bf16 v[180:183], v[76:79], v[112:115], v[180:183]\n"
"s_mov_b32 s94, 0xf0000f\n"
"s_mov_b32 s95, 0xf0000f00\n"
"v_mfma_f32_16x16x32_bf16 v[180:183], v[72:75], v[104:107], v[180:183]\n"
"v_cndmask_b32_e64 v173, 0, v173, s[94:95]\n"
"v_mov_b32_e32 v247, v173\n"
"v_mfma_f32_16x16x32_bf16 v[180:183], v[68:71], v[100:103], v[180:183]\n"
"s_add_u32 s98, s70, 0x200000\n"
"buffer_load_dwordx4 a[128:131], v0, s[4:7], s98 offen sc1\n"
"v_mfma_f32_16x16x32_bf16 v[180:183], v[64:67], v[96:99], v[180:183]\n"
"ds_write_b128 v3, v[168:171] offset:4096\n"
"v_mfma_f32_16x16x32_bf16 v[180:183], v[60:63], v[92:95], v[180:183]\n"
"v_permlane16_swap_b32_e32 v173, v247\n"
"v_add_f32_e32 v173, v173, v247\n"
"v_mfma_f32_16x16x32_bf16 v[180:183], v[56:59], v[88:91], v[180:183]\n"
"s_waitcnt vmcnt(24)\n"
"v_mov_b32_e32 v247, v173\n"
"; @B G1 0.1\n"
"v_mfma_f32_16x16x32_bf16 v[88:91], a[0:3], v[128:131], 0\n"
"s_mov_b32 s94, 0xffff0000\n"
"v_permlane32_swap_b32_e32 v173, v247\n"
"v_add_f32_e32 v173, v173, v247\n"
"v_mfma_f32_16x16x32_bf16 v[88:91], a[4:7], v[132:135], v[88:91]\n"
"s_add_u32 s97, s63, 0x40000\n"
"v_cndmask_b32_e64 v172, v172, v173, s[94:95]\n"
"v_mfma_f32_16x16x32_bf16 v[88:91], a[8:11], v[136:139], v[88:91]\n"
"s_add_u32 s96, s55, 0x440000\n"
"buffer_load_dwordx4 v[92:95], v0, s[4:7], s96 offen sc1\n"
"v_mfma_f32_16x16x32_bf16 v[88:91], a[12:15], v[140:143], v[88:91]\n"
"s_waitcnt vmcnt(21)\n"
"buffer_store_dword v172, v14, s[4:7], s97 offen sc1\n"
"v_mfma_f32_16x16x32_bf16 v[88:91], a[16:19], v[144:147], v[88:91]\n"
"v_mfma_f32_16x16x32_bf16 v[88:91], a[20:23], v[156:159], v[88:91]\n"
"ds_write_b128 v3, v[180:183] offset:5120\n"
"v_mfma_f32_16x16x32_bf16 v[88:91], a[24:27], v[160:163], v[88:91]\n"
"v_mfma_f32_16x16x32_bf16 v[88:91], a[28:31], v[164:167], v[88:91]\n"
"buffer_load_dwordx4 v[96:99], v0, s[4:7], s96 offen offset:1024 sc1\n"
"v_mfma_f32_16x16x32_bf16 v[100:103], a[60:63], v[164:167], 0\n"
"v_mfma_f32_16x16x32_bf16 v[100:103], a[56:59], v[160:163], v[100:103]\n"
"v_mfma_f32_16x16x32_bf16 v[100:103], a[52:55], v[156:159], v[100:103]\n"
"v_mfma_f32_16x16x32_bf16 v[100:103], a[48:51], v[144:147], v[100:103]\n"
"v_mfma_f32_16x16x32_bf16 v[100:103], a[44:47], v[140:143], v[100:103]\n"
"s_nop 1\n"
"v_cmp_u_f32_e32 vcc, v88, v88\n"
"v_mfma_f32_16x16x32_bf16 v[100:103], a[40:43], v[136:139], v[100:103]\n"
"s_cbranch_vccnz .Lmk_p44s\n"
".Lmk_p44r:\n"
"v_mfma_f32_16x16x32_bf16 v[100:103], a[36:39], v[132:135], v[100:103]\n"
"buffer_load_dwordx4 v[104:107], v0, s[4:7], s96 offen offset:2048 sc1\n"
"v_mfma_f32_16x16x32_bf16 v[100:103], a[32:35], v[128:131], v[100:103]\n"
"ds_write_b128 v3, v[88:91] offset:6144\n"
"v_mfma_f32_16x16x32_bf16 v[88:91], a[64:67], v[128:131], 0\n"
"buffer_load_dwordx4 a[132:135], v0, s[4:7], s98 offen offset:1024 sc1\n"
"v_mfma_f32_16x16x32_bf16 v[88:91], a[68:71], v[132:135], v[88:91]\n"
"buffer_load_dwordx4 v[112:115], v0, s[4:7], s96 offen offset:3072 sc1\n"
"v_mfma_f32_16x16x32_bf16 v[88:91], a[72:75], v[136:139], v[88:91]\n"
"v_mfma_f32_16x16x32_bf16 v[88:91], a[76:79], v[140:143], v[88:91]\n"
"v_mfma_f32_16x16x32_bf16 v[88:91], a[80:83], v[144:147], v[88:91]\n"
"v_mfma_f32_16x16x32_bf16 v[88:91], a[84:87], v[156:159], v[88:91]\n"
"ds_write_b128 v3, v[100:103] offset:7168\n"
"v_mfma_f32_16x16x32_bf16 v[88:91], a[88:91], v[160:163], v[88:91]\n"
"buffer_load_dwordx4 v[100:103], v1, s[4:7], s96 offen sc1\n"
"v_mfma_f32_16x16x32_bf16 v[88:91], a[92:95], v[164:167], v[88:91]\n"
"v_mfma_f32_16x16x32_bf16 v[116:119], a[124:127], v[164:167], 0\n"
"v_mfma_f32_16x16x32_bf16 v[116:119], a[120:123], v[160:163], v[116:119]\n"
"v_mfma_f32_16x16x32_bf16 v[116:119], a[116:119], v[156:159], v[116:119]\n"
"v_mfma_f32_16x16x32_bf16 v[116:119], a[112:115], v[144:147], v[116:119]\n"
"buffer_load_dwordx4 v[124:127], v1, s[4:7], s96 offen offset:1024 sc1\n"
"v_mfma_f32_16x16x32_bf16 v[116:119], a[108:111], v[140:143], v[116:119]\n"
"v_mfma_f32_16x16x32_bf16 v[116:119], a[104:107], v[136:139], v[116:119]\n"
"s_nop 0\n"
"ds_write_b128 v3, v[88:91] offset:8192\n"
"v_mfma_f32_16x16x32_bf16 v[116:119], a[100:103], v[132:135], v[116:119]\n"
"v_mfma_f32_16x16x32_bf16 v[116:119], a[96:99], v[128:131], v[116:119]\n"
"v_mfma_f32_16x16x32_bf16 v[88:91], v[24:27], v[128:131], 0\n"
"buffer_load_dwordx4 v[168:171], v1, s[4:7], s96 offen offset:2048 sc1\n"
"v_mfma_f32_16x16x32_bf16 v[88:91], v[28:31], v[132:135], v[88:91]\n"
"s_add_u32 s97, s60, 0x440000\n"
"v_mfma_f32_16x16x32_bf16 v[88:91], v[32:35], v[136:139], v[88:91]\n"
"v_mfma_f32_16x16x32_bf16 v[88:91], v[36:39], v[140:143], v[88:91]\n"
"buffer_store_dwordx4 v[120:123], v0, s[4:7], s97 offen sc1\n"
"v_mfma_f32_16x16x32_bf16 v[88:91], v[40:43], v[144:147], v[88:91]\n"
"buffer_load_dwordx4 a[136:139], v0, s[4:7], s98 offen offset:2048 sc1\n"
"v_mfma_f32_16x16x32_bf16 v[88:91], v[44:47], v[156:159], v[88:91]\n"
"buffer_load_dwordx4 v[172:175], v1, s[4:7], s96 offen offset:3072 sc1\n"
"v_mfma_f32_16x16x32_bf16 v[88:91], v[48:51], v[160:163], v[88:91]\n"
"ds_write_b128 v3, v[116:119] offset:9216\n"
"v_mfma_f32_16x16x32_bf16 v[88:91], v[52:55], v[164:167], v[88:91]\n"
"s_waitcnt vmcnt(14)\n"
"v_add_f32_e32 v255, v255, v251\n"
"v_add_f32_e32 v254, v254, v250\n"
"v_mfma_f32_16x16x32_bf16 v[116:119], v[84:87], v[164:167], 0\n"
"v_add_f32_e32 v253, v253, v249\n"
"v_add_f32_e32 v252, v252, v248\n"
"v_mfma_f32_16x16x32_bf16 v[116:119], v[80:83], v[160:163], v[116:119]\n"
"v_add_f32_e32 v255, v255, v253\n"
"v_add_f32_e32 v254, v254, v252\n"
"v_mfma_f32_16x16x32_bf16 v[116:119], v[76:79], v[156:159], v[116:119]\n"
"v_add_f32_e32 v255, v255, v254\n"
"v_cmp_u_f32_e32 vcc, v255, v255\n"
"v_mfma_f32_16x16x32_bf16 v[116:119], v[72:75], v[144:147], v[116:119]\n"
"s_cbranch_vccnz .Lmk_m47s\n"
".Lmk_m47r:\n"
"v_mov_b32_e32 v254, v255\n"
"v_mfma_f32_16x16x32_bf16 v[116:119], v[68:71], v[140:143], v[116:119]\n"
"s_nop 0\n"
"v_permlane32_swap_b32_e32 v255, v254\n"
"v_add_f32_e32 v255, v255, v254\n"
"v_mfma_f32_16x16x32_bf16 v[116:119], v[64:67], v[136:139], v[116:119]\n"
"ds_write_b128 v3, v[88:91] offset:10240\n"
"v_mfma_f32_16x16x32_bf16 v[116:119], v[60:63], v[132:135], v[116:119]\n"
"v_mul_f32_e32 v255, 0x3a800000, v255\n"
"v_mov_b32_e32 v254, v255\n"
"v_mfma_f32_16x16x32_bf16 v[116:119], v[56:59], v[128:131], v[116:119]\n"
"s_nop 0\n"
"v_permlane16_swap_b32_e32 v255, v254\n"
"v_cvt_pk_bf16_f32 v253, -v255, 0\n"
"; @B G1 0.2\n"
"v_mfma_f32_16x16x32_bf16 v[88:91], a[0:3], v[176:179], 0\n"
"v_fma_f32 v254, -v255, v255, v254\n"
"v_lshlrev_b32_e32 v253, 16, v253\n"
"v_mfma_f32_16x16x32_bf16 v[88:91], a[4:7], v[108:111], v[88:91]\n"
"v_max_f32_e32 v254, 0, v254\n"
"v_cvt_pk_bf16_f32 v254, v254, 0\n"
"v_mfma_f32_16x16x32_bf16 v[88:91], a[8:11], v[148:151], v[88:91]\n"
"v_lshlrev_b32_e32 v254, 16, v254\n"
"v_add_f32_e32 v254, 0x3727c5ac, v254\n"
"v_mfma_f32_16x16x32_bf16 v[88:91], a[12:15], v[184:187], v[88:91]\n"
"s_waitcnt vmcnt(13)\n"
"v_cvt_pk_bf16_f32 v254, v254, 0\n"
"v_lshlrev_b32_e32 v254, 16, v254\n"
"v_mfma_f32_16x16x32_bf16 v[88:91], a[16:19], v[152:155], v[88:91]\n"
"s_mul_i32 s97, s0, .Lmk_exit-.Lmk_b2\n"
"s_add_u32 s97, s97, .Lmk_b2-.Lmk_g42\n"
"s_getpc_b64 s[84:85]\n"
".Lmk_g42:\n"
"s_add_u32 s84, s84, s97\n"
"s_addc_u32 s85, s85, 0\n"
"v_mfma_f32_16x16x32_bf16 v[88:91], a[20:23], v[192:195], v[88:91]\n"
"ds_write_b128 v3, v[116:119] offset:11264\n"
"v_mfma_f32_16x16x32_bf16 v[88:91], a[24:27], v[196:199], v[88:91]\n"
"s_and_b32 s84, s84, -16\n"
"s_and_b32 s85, s85, 0xffff\n"
"s_sub_u32 s97, .Lmk_end-.Lmk_g42, s97\n"
"s_min_u32 s86, s97, 0x4000\n"
"s_mov_b32 s87, 0x20000\n"
"v_mfma_f32_16x16x32_bf16 v[88:91], a[28:31], v[200:203], v[88:91]\n"
"s_mov_b32 m0, 0x27800\n"
"s_nop 0\n"
"buffer_load_dwordx4 v0, s[84:87], s99 offen lds\n"
"v_mfma_f32_16x16x32_bf16 v[116:119], a[60:63], v[200:203], 0\n"
"buffer_load_dwordx4 a[140:143], v0, s[4:7], s98 offen offset:3072 sc1\n"
"v_mfma_f32_16x16x32_bf16 v[116:119], a[56:59], v[196:199], v[116:119]\n"
"v_sqrt_f32_e32 v254, v254\n"
"v_mfma_f32_16x16x32_bf16 v[116:119], a[52:55], v[192:195], v[116:119]\n"
"s_add_u32 s97, s52, 0x448000\n"
"buffer_load_dwordx4 v[128:131], v0, s[4:7], s97 offen sc1\n"
"v_mfma_f32_16x16x32_bf16 v[116:119], a[48:51], v[152:155], v[116:119]\n"
"s_waitcnt lgkmcnt(0)\n"
"s_barrier\n"
"v_mfma_f32_16x16x32_bf16 v[116:119], a[44:47], v[184:187], v[116:119]\n"
"v_cmp_u_f32_e32 vcc, v88, v88\n"
"v_cvt_pk_bf16_f32 v254, v254, 0\n"
"v_mfma_f32_16x16x32_bf16 v[116:119], a[40:43], v[148:151], v[116:119]\n"
"s_cbranch_vccnz .Lmk_p45s\n"
".Lmk_p45r:\n"
"v_lshlrev_b32_e32 v254, 16, v254\n"
"v_mfma_f32_16x16x32_bf16 v[116:119], a[36:39], v[108:111], v[116:119]\n"
"ds_write_b128 v3, v[88:91] offset:12288\n"
"v_mfma_f32_16x16x32_bf16 v[116:119], a[32:35], v[176:179], v[116:119]\n"
"buffer_load_dwordx4 v[88:91], v0, s[4:7], s97 offen offset:1024 sc1\n"
"v_mfma_f32_16x16x32_bf16 v[132:135], a[64:67], v[176:179], 0\n"
"v_rcp_f32_e32 v255, v254\n"
"v_mfma_f32_16x16x32_bf16 v[132:135], a[68:71], v[108:111], v[132:135]\n"
"ds_read_b128 v[136:139], v4\n"
"v_mul_f32_e32 v254, 0x3e8293ee, v255\n"
"v_mfma_f32_16x16x32_bf16 v[132:135], a[72:75], v[148:151], v[132:135]\n"
"ds_read_b128 v[140:143], v9 offset:640\n"
"ds_read_b128 v[144:147], v5\n"
"v_mfma_f32_16x16x32_bf16 v[132:135], a[76:79], v[184:187], v[132:135]\n"
"ds_read_b128 v[156:159], v4 offset:1024\n"
"ds_read_b128 v[160:163], v9 offset:704\n"
"v_mfma_f32_16x16x32_bf16 v[132:135], a[80:83], v[152:155], v[132:135]\n"
"buffer_load_dwordx4 v[164:167], v0, s[4:7], s97 offen offset:2048 sc1\n"
"v_mfma_f32_16x16x32_bf16 v[132:135], a[84:87], v[192:195], v[132:135]\n"
"ds_write_b128 v3, v[116:119] offset:13312\n"
"v_mfma_f32_16x16x32_bf16 v[132:135], a[88:91], v[196:199], v[132:135]\n"
"ds_read_b128 v[116:119], v5 offset:1024\n"
"v_mfma_f32_16x16x32_bf16 v[132:135], a[92:95], v[200:203], v[132:135]\n"
"s_waitcnt lgkmcnt(4)\n"
"v_fmac_f32_e32 v136, v253, v140\n"
"v_fmac_f32_e32 v137, v253, v141\n"
"v_mfma_f32_16x16x32_bf16 v[180:183], a[124:127], v[200:203], 0\n"
"v_fmac_f32_e32 v138, v253, v142\n"
"v_fmac_f32_e32 v139, v253, v143\n"
"v_mfma_f32_16x16x32_bf16 v[180:183], a[120:123], v[196:199], v[180:183]\n"
"buffer_load_dwordx4 v[140:143], v0, s[4:7], s97 offen offset:3072 sc1\n"
"v_mfma_f32_16x16x32_bf16 v[180:183], a[116:119], v[192:195], v[180:183]\n"
"ds_read_b128 v[188:191], v4 offset:2048\n"
"ds_read_b128 v[204:207], v9 offset:768\n"
"v_mfma_f32_16x16x32_bf16 v[180:183], a[112:115], v[152:155], v[180:183]\n"
"ds_read_b128 v[208:211], v5 offset:2048\n"
"v_add_f32_e32 v136, v136, v144\n"
"v_mfma_f32_16x16x32_bf16 v[180:183], a[108:111], v[184:187], v[180:183]\n"
"buffer_load_dwordx4 a[144:147], v1, s[4:7], s98 offen sc1\n"
"v_mfma_f32_16x16x32_bf16 v[180:183], a[104:107], v[148:151], v[180:183]\n"
"ds_write_b128 v3, v[132:135] offset:14336\n"
"v_mfma_f32_16x16x32_bf16 v[180:183], a[100:103], v[108:111], v[180:183]\n"
"buffer_load_dwordx4 v[132:135], v1, s[4:7], s97 offen sc1\n"
"v_mfma_f32_16x16x32_bf16 v[180:183], a[96:99], v[176:179], v[180:183]\n"
"v_add_f32_e32 v137, v137, v145\n"
"v_add_f32_e32 v138, v138, v146\n"
"v_mfma_f32_16x16x32_bf16 v[212:215], v[24:27], v[176:179], 0\n"
"s_waitcnt lgkmcnt(4)\n"
"v_add_f32_e32 v139, v139, v147\n"
"v_fmac_f32_e32 v156, v253, v160\n"
"v_mfma_f32_16x16x32_bf16 v[212:215], v[28:31], v[108:111], v[212:215]\n"
"ds_read_b128 v[144:147], v4 offset:3072\n"
"ds_read_b128 v[216:219], v9 offset:832\n"
"v_mfma_f32_16x16x32_bf16 v[212:215], v[32:35], v[148:151], v[212:215]\n"
"ds_read_b128 v[220:223], v5 offset:3072\n"
"v_fmac_f32_e32 v157, v253, v161\n"
"v_mfma_f32_16x16x32_bf16 v[212:215], v[36:39], v[184:187], v[212:215]\n"
"buffer_load_dwordx4 v[224:227], v1, s[4:7], s97 offen offset:1024 sc1\n"
"v_mfma_f32_16x16x32_bf16 v[212:215], v[40:43], v[152:155], v[212:215]\n"
"s_add_u32 s96, s60, 0x400000\n"
"v_fmac_f32_e32 v158, v253, v162\n"
"v_fmac_f32_e32 v159, v253, v163\n"
"v_mfma_f32_16x16x32_bf16 v[212:215], v[44:47], v[192:195], v[212:215]\n"
"ds_write_b128 v3, v[180:183] offset:15360\n"
"v_mfma_f32_16x16x32_bf16 v[212:215], v[48:51], v[196:199], v[212:215]\n"
"buffer_store_dwordx4 v[120:123], v0, s[4:7], s96 offen sc1\n"
"v_mfma_f32_16x16x32_bf16 v[212:215], v[52:55], v[200:203], v[212:215]\n"
"v_add_f32_e32 v156, v156, v116\n"
"v_add_f32_e32 v157, v157, v117\n"
"v_mfma_f32_16x16x32_bf16 v[160:163], v[84:87], v[200:203], 0\n"
"buffer_load_dwordx4 v[180:183], v1, s[4:7], s97 offen offset:2048 sc1\n"
"v_mfma_f32_16x16x32_bf16 v[160:163], v[80:83], v[196:199], v[160:163]\n"
"s_add_u32 s96, s68, s80\n"
"s_add_u32 s96, s96, 0x10000\n"
"s_waitcnt vmcnt(21)\n"
"v_mfma_f32_16x16x32_bf16 v[160:163], v[76:79], v[192:195], v[160:163]\n"
"buffer_store_dwordx2 v[18:19], v11, s[4:7], s96 offen sc1\n"
"v_mfma_f32_16x16x32_bf16 v[160:163], v[72:75], v[152:155], v[160:163]\n"
"ds_read_b128 v[152:155], v4 offset:4096\n"
"ds_read_b128 v[192:195], v9 offset:512\n"
"v_mfma_f32_16x16x32_bf16 v[160:163], v[68:71], v[184:187], v[160:163]\n"
"ds_read_b128 v[184:187], v5 offset:4096\n"
"v_add_f32_e32 v158, v158, v118\n"
"v_mfma_f32_16x16x32_bf16 v[160:163], v[64:67], v[148:151], v[160:163]\n"
"buffer_load_dwordx4 v[148:151], v1, s[4:7], s97 offen offset:3072 sc1\n"
"v_mfma_f32_16x16x32_bf16 v[160:163], v[60:63], v[108:111], v[160:163]\n"
"ds_write_b128 v3, v[212:215] offset:16384\n"
"v_mfma_f32_16x16x32_bf16 v[160:163], v[56:59], v[176:179], v[160:163]\n"
"s_waitcnt vmcnt(19)\n"
"ds_read_b128 v[108:111], v4 offset:5120\n"
"ds_read_b128 v[176:179], v9 offset:576\n"
"; @B G1 0.3\n"
"v_mfma_f32_16x16x32_bf16 v[196:199], a[0:3], v[92:95], v[136:139]\n"
"buffer_load_dwordx4 a[148:151], v1, s[4:7], s98 offen offset:1024 sc1\n"
"v_mfma_f32_16x16x32_bf16 v[196:199], a[4:7], v[96:99], v[196:199]\n"
"ds_read_b128 v[200:203], v5 offset:5120\n"
"v_add_f32_e32 v159, v159, v119\n"
"v_mfma_f32_16x16x32_bf16 v[196:199], a[8:11], v[104:107], v[196:199]\n"
"s_add_u32 s97, s53, 0x448000\n"
"buffer_load_dwordx4 v[116:119], v0, s[4:7], s97 offen sc1\n"
"v_mfma_f32_16x16x32_bf16 v[196:199], a[12:15], v[112:115], v[196:199]\n"
"s_waitcnt vmcnt(15)\n"
"s_waitcnt lgkmcnt(12)\n"
"v_fmac_f32_e32 v188, v253, v204\n"
"v_mfma_f32_16x16x32_bf16 v[196:199], a[16:19], v[100:103], v[196:199]\n"
"v_fmac_f32_e32 v189, v253, v205\n"
"v_fmac_f32_e32 v190, v253, v206\n"
"v_mfma_f32_16x16x32_bf16 v[196:199], a[20:23], v[124:127], v[196:199]\n"
"ds_write_b128 v3, v[160:163] offset:17408\n"
"v_mfma_f32_16x16x32_bf16 v[196:199], a[24:27], v[168:171], v[196:199]\n"
"v_fmac_f32_e32 v191, v253, v207\n"
"v_add_f32_e32 v188, v188, v208\n"
"v_mfma_f32_16x16x32_bf16 v[196:199], a[28:31], v[172:175], v[196:199]\n"
"buffer_load_dwordx4 v[160:163], v0, s[4:7], s97 offen offset:1024 sc1\n"
"v_mfma_f32_16x16x32_bf16 v[204:207], a[60:63], v[172:175], v[156:159]\n"
"v_add_f32_e32 v189, v189, v209\n"
"v_add_f32_e32 v190, v190, v210\n"
"v_mfma_f32_16x16x32_bf16 v[204:207], a[56:59], v[168:171], v[204:207]\n"
"s_waitcnt lgkmcnt(9)\n"
"v_add_f32_e32 v191, v191, v211\n"
"v_fmac_f32_e32 v144, v253, v216\n"
"v_mfma_f32_16x16x32_bf16 v[204:207], a[52:55], v[124:127], v[204:207]\n"
"s_waitcnt lgkmcnt(0)\n"
"s_barrier\n"
"v_mfma_f32_16x16x32_bf16 v[204:207], a[48:51], v[100:103], v[204:207]\n"
"ds_read_b128 v[208:211], v6\n"
"ds_read_b128 v[212:215], v6 offset:1024\n"
"v_mfma_f32_16x16x32_bf16 v[204:207], a[44:47], v[112:115], v[204:207]\n"
"v_cmp_u_f32_e32 vcc, v196, v196\n"
"v_fmac_f32_e32 v145, v253, v217\n"
"v_mfma_f32_16x16x32_bf16 v[204:207], a[40:43], v[104:107], v[204:207]\n"
"s_cbranch_vccnz .Lmk_p46s\n"
".Lmk_p46r:\n"
"v_fmac_f32_e32 v146, v253, v218\n"
"v_mfma_f32_16x16x32_bf16 v[204:207], a[36:39], v[96:99], v[204:207]\n"
"buffer_load_dwordx4 v[136:139], v0, s[4:7], s97 offen offset:2048 sc1\n"
"v_mfma_f32_16x16x32_bf16 v[204:207], a[32:35], v[92:95], v[204:207]\n"
"v_fmac_f32_e32 v147, v253, v219\n"
"v_add_f32_e32 v144, v144, v220\n"
"v_mfma_f32_16x16x32_bf16 v[188:191], a[64:67], v[92:95], v[188:191]\n"
"v_add_f32_e32 v145, v145, v221\n"
"v_add_f32_e32 v146, v146, v222\n"
"v_mfma_f32_16x16x32_bf16 v[188:191], a[68:71], v[96:99], v[188:191]\n"
"buffer_load_dwordx4 v[156:159], v0, s[4:7], s97 offen offset:3072 sc1\n"
"v_mfma_f32_16x16x32_bf16 v[188:191], a[72:75], v[104:107], v[188:191]\n"
"v_add_f32_e32 v147, v147, v223\n"
"v_fmac_f32_e32 v152, v253, v192\n"
"v_mfma_f32_16x16x32_bf16 v[188:191], a[76:79], v[112:115], v[188:191]\n"
"v_fmac_f32_e32 v153, v253, v193\n"
"v_fmac_f32_e32 v154, v253, v194\n"
"v_mfma_f32_16x16x32_bf16 v[188:191], a[80:83], v[100:103], v[188:191]\n"
"buffer_load_dwordx4 a[152:155], v1, s[4:7], s98 offen offset:2048 sc1\n"
"v_mfma_f32_16x16x32_bf16 v[188:191], a[84:87], v[124:127], v[188:191]\n"
"s_waitcnt lgkmcnt(0)\n"
"v_fmac_f32_e32 v155, v253, v195\n"
"v_add_f32_e32 v152, v152, v184\n"
"v_mfma_f32_16x16x32_bf16 v[188:191], a[88:91], v[168:171], v[188:191]\n"
"buffer_load_dwordx4 v[192:195], v1, s[4:7], s97 offen sc1\n"
"v_mfma_f32_16x16x32_bf16 v[188:191], a[92:95], v[172:175], v[188:191]\n"
"ds_read_b128 v[216:219], v6 offset:2048\n"
"ds_read_b128 v[220:223], v6 offset:3072\n"
"v_mfma_f32_16x16x32_bf16 v[144:147], a[124:127], v[172:175], v[144:147]\n"
"v_add_f32_e32 v153, v153, v185\n"
"v_add_f32_e32 v154, v154, v186\n"
"v_mfma_f32_16x16x32_bf16 v[144:147], a[120:123], v[168:171], v[144:147]\n"
"v_add_f32_e32 v155, v155, v187\n"
"v_fmac_f32_e32 v108, v253, v176\n"
"v_mfma_f32_16x16x32_bf16 v[144:147], a[116:119], v[124:127], v[144:147]\n"
"v_fmac_f32_e32 v109, v253, v177\n"
"v_fmac_f32_e32 v110, v253, v178\n"
"v_mfma_f32_16x16x32_bf16 v[144:147], a[112:115], v[100:103], v[144:147]\n"
"buffer_load_dwordx4 v[184:187], v1, s[4:7], s97 offen offset:1024 sc1\n"
"v_mfma_f32_16x16x32_bf16 v[144:147], a[108:111], v[112:115], v[144:147]\n"
"v_fmac_f32_e32 v111, v253, v179\n"
"v_add_f32_e32 v108, v108, v200\n"
"v_mfma_f32_16x16x32_bf16 v[144:147], a[104:107], v[104:107], v[144:147]\n"
"v_add_f32_e32 v109, v109, v201\n"
"v_add_f32_e32 v110, v110, v202\n"
"v_mfma_f32_16x16x32_bf16 v[144:147], a[100:103], v[96:99], v[144:147]\n"
"v_add_f32_e32 v111, v111, v203\n"
"v_add_f32_e32 v196, v196, v208\n"
"v_mfma_f32_16x16x32_bf16 v[144:147], a[96:99], v[92:95], v[144:147]\n"
"v_add_f32_e32 v197, v197, v209\n"
"v_add_f32_e32 v198, v198, v210\n"
"v_mfma_f32_16x16x32_bf16 v[152:155], v[24:27], v[92:95], v[152:155]\n"
"buffer_load_dwordx4 v[176:179], v1, s[4:7], s97 offen offset:2048 sc1\n"
"v_mfma_f32_16x16x32_bf16 v[152:155], v[28:31], v[96:99], v[152:155]\n"
"v_add_f32_e32 v199, v199, v211\n"
"v_add_f32_e32 v204, v204, v212\n"
"v_mfma_f32_16x16x32_bf16 v[152:155], v[32:35], v[104:107], v[152:155]\n"
"v_add_f32_e32 v205, v205, v213\n"
"v_add_f32_e32 v206, v206, v214\n"
"v_mfma_f32_16x16x32_bf16 v[152:155], v[36:39], v[112:115], v[152:155]\n"
"v_add_f32_e32 v207, v207, v215\n"
"v_mul_f32_e32 v196, v196, v255\n"
"v_mfma_f32_16x16x32_bf16 v[152:155], v[40:43], v[100:103], v[152:155]\n"
"s_waitcnt lgkmcnt(0)\n"
"v_mul_f32_e32 v197, v197, v255\n"
"v_mul_f32_e32 v198, v198, v255\n"
"v_mfma_f32_16x16x32_bf16 v[152:155], v[44:47], v[124:127], v[152:155]\n"
"buffer_load_dwordx4 v[200:203], v1, s[4:7], s97 offen offset:3072 sc1\n"
"v_mfma_f32_16x16x32_bf16 v[152:155], v[48:51], v[168:171], v[152:155]\n"
"s_add_u32 s97, s60, 0x408000\n"
"ds_read_b128 v[208:211], v6 offset:4096\n"
"ds_read_b128 v[212:215], v6 offset:5120\n"
"v_mfma_f32_16x16x32_bf16 v[152:155], v[52:55], v[172:175], v[152:155]\n"
"v_mul_f32_e32 v199, v199, v255\n"
"v_mul_f32_e32 v204, v204, v255\n"
"v_mfma_f32_16x16x32_bf16 v[108:111], v[84:87], v[172:175], v[108:111]\n"
"buffer_store_dwordx4 v[120:123], v0, s[4:7], s97 offen sc1\n"
"v_mfma_f32_16x16x32_bf16 v[108:111], v[80:83], v[168:171], v[108:111]\n"
"v_mul_f32_e32 v205, v205, v255\n"
"v_mul_f32_e32 v206, v206, v255\n"
"v_mfma_f32_16x16x32_bf16 v[108:111], v[76:79], v[124:127], v[108:111]\n"
"v_mul_f32_e32 v207, v207, v255\n"
"v_cvt_pk_bf16_f32 v120, v196, v197\n"
"v_mfma_f32_16x16x32_bf16 v[108:111], v[72:75], v[100:103], v[108:111]\n"
"v_cvt_pk_bf16_f32 v121, v198, v199\n"
"v_cvt_pk_bf16_f32 v122, v204, v205\n"
"v_mfma_f32_16x16x32_bf16 v[108:111], v[68:71], v[112:115], v[108:111]\n"
"v_cvt_pk_bf16_f32 v123, v206, v207\n"
"v_add_f32_e32 v188, v188, v216\n"
"v_mfma_f32_16x16x32_bf16 v[108:111], v[64:67], v[104:107], v[108:111]\n"
"ds_write_b128 v7, v[120:123] offset:24576\n"
"v_mfma_f32_16x16x32_bf16 v[108:111], v[60:63], v[96:99], v[108:111]\n"
"v_add_f32_e32 v189, v189, v217\n"
"v_add_f32_e32 v190, v190, v218\n"
"v_mfma_f32_16x16x32_bf16 v[108:111], v[56:59], v[92:95], v[108:111]\n"
"v_add_f32_e32 v191, v191, v219\n"
"v_add_f32_e32 v144, v144, v220\n"
"v_add_f32_e32 v145, v145, v221\n"
"v_add_f32_e32 v146, v146, v222\n"
"v_add_f32_e32 v147, v147, v223\n"
"v_mul_f32_e32 v188, v188, v255\n"
"v_mul_f32_e32 v189, v189, v255\n"
"v_mul_f32_e32 v190, v190, v255\n"
"v_mul_f32_e32 v191, v191, v255\n"
"v_mul_f32_e32 v144, v144, v255\n"
"v_mul_f32_e32 v145, v145, v255\n"
"v_mul_f32_e32 v146, v146, v255\n"
"v_mul_f32_e32 v147, v147, v255\n"
"v_cvt_pk_bf16_f32 v92, v188, v189\n"
"v_cvt_pk_bf16_f32 v93, v190, v191\n"
"v_cvt_pk_bf16_f32 v94, v144, v145\n"
"v_cvt_pk_bf16_f32 v95, v146, v147\n"
"ds_write_b128 v7, v[92:95] offset:57344\n"
".Lmk_b1:\n"
"s_waitcnt vmcnt(18)\n"
"; @B G1 1.0\n"
"v_mfma_f32_16x16x32_bf16 v[92:95], a[0:3], v[128:131], 0\n"
"v_mfma_f32_16x16x32_bf16 v[92:95], a[4:7], v[88:91], v[92:95]\n"
"s_add_u32 s97, s62, 0x44800\n"
"v_mfma_f32_16x16x32_bf16 v[92:95], a[8:11], v[164:167], v[92:95]\n"
"s_add_u32 s96, s54, 0x448000\n"
"buffer_load_dwordx4 v[96:99], v0, s[4:7], s96 offen sc1\n"
"v_mfma_f32_16x16x32_bf16 v[92:95], a[12:15], v[140:143], v[92:95]\n"
"s_waitcnt vmcnt(12)\n"
"v_mfma_f32_16x16x32_bf16 v[92:95], a[16:19], v[132:135], v[92:95]\n"
"buffer_load_dword v255, v10, s[4:7], s97 offen sc1\n"
"v_mfma_f32_16x16x32_bf16 v[92:95], a[20:23], v[224:227], v[92:95]\n"
"s_waitcnt lgkmcnt(2)\n"
"v_add_f32_e32 v152, v152, v208\n"
"v_add_f32_e32 v153, v153, v209\n"
"v_mfma_f32_16x16x32_bf16 v[92:95], a[24:27], v[180:183], v[92:95]\n"
"v_add_f32_e32 v154, v154, v210\n"
"v_add_f32_e32 v155, v155, v211\n"
"v_mfma_f32_16x16x32_bf16 v[92:95], a[28:31], v[148:151], v[92:95]\n"
"buffer_load_dwordx4 v[100:103], v0, s[4:7], s96 offen offset:1024 sc1\n"
"v_mfma_f32_16x16x32_bf16 v[104:107], a[60:63], v[148:151], 0\n"
"buffer_load_dwordx4 a[156:159], v1, s[4:7], s98 offen offset:3072 sc1\n"
"v_mfma_f32_16x16x32_bf16 v[104:107], a[56:59], v[180:183], v[104:107]\n"
"buffer_load_dword v253, v10, s[4:7], s97 offen offset:256 sc1\n"
"v_mfma_f32_16x16x32_bf16 v[104:107], a[52:55], v[224:227], v[104:107]\n"
"v_add_f32_e32 v108, v108, v212\n"
"v_add_f32_e32 v109, v109, v213\n"
"v_mfma_f32_16x16x32_bf16 v[104:107], a[48:51], v[132:135], v[104:107]\n"
"v_add_f32_e32 v110, v110, v214\n"
"v_add_f32_e32 v111, v111, v215\n"
"v_mfma_f32_16x16x32_bf16 v[104:107], a[44:47], v[140:143], v[104:107]\n"
"v_cmp_u_f32_e32 vcc, v92, v92\n"
"v_mul_f32_e32 v152, v152, v254\n"
"v_mfma_f32_16x16x32_bf16 v[104:107], a[40:43], v[164:167], v[104:107]\n"
"s_cbranch_vccnz .Lmk_p48s\n"
".Lmk_p48r:\n"
"v_mul_f32_e32 v153, v153, v254\n"
"v_mfma_f32_16x16x32_bf16 v[104:107], a[36:39], v[88:91], v[104:107]\n"
"buffer_load_dwordx4 v[112:115], v0, s[4:7], s96 offen offset:2048 sc1\n"
"v_mfma_f32_16x16x32_bf16 v[104:107], a[32:35], v[128:131], v[104:107]\n"
"ds_write_b128 v3, v[92:95]\n"
"v_mfma_f32_16x16x32_bf16 v[92:95], a[64:67], v[128:131], 0\n"
"buffer_load_dword v252, v10, s[4:7], s97 offen offset:512 sc1\n"
"v_mfma_f32_16x16x32_bf16 v[92:95], a[68:71], v[88:91], v[92:95]\n"
"buffer_load_dwordx4 v[120:123], v0, s[4:7], s96 offen offset:3072 sc1\n"
"v_mfma_f32_16x16x32_bf16 v[92:95], a[72:75], v[164:167], v[92:95]\n"
"v_mul_f32_e32 v154, v154, v254\n"
"v_mul_f32_e32 v155, v155, v254\n"
"v_mfma_f32_16x16x32_bf16 v[92:95], a[76:79], v[140:143], v[92:95]\n"
"buffer_load_dword v251, v10, s[4:7], s97 offen offset:768 sc1\n"
"v_mfma_f32_16x16x32_bf16 v[92:95], a[80:83], v[132:135], v[92:95]\n"
"v_mul_f32_e32 v108, v108, v254\n"
"v_mul_f32_e32 v109, v109, v254\n"
"v_mfma_f32_16x16x32_bf16 v[92:95], a[84:87], v[224:227], v[92:95]\n"
"ds_write_b128 v3, v[104:107] offset:1024\n"
"v_mfma_f32_16x16x32_bf16 v[92:95], a[88:91], v[180:183], v[92:95]\n"
"buffer_load_dwordx4 v[104:107], v1, s[4:7], s96 offen sc1\n"
"v_mfma_f32_16x16x32_bf16 v[92:95], a[92:95], v[148:151], v[92:95]\n"
"buffer_load_dword v250, v10, s[4:7], s97 offen offset:1024 sc1\n"
"v_mfma_f32_16x16x32_bf16 v[124:127], a[124:127], v[148:151], 0\n"
"v_mul_f32_e32 v110, v110, v254\n"
"v_mul_f32_e32 v111, v111, v254\n"
"v_mfma_f32_16x16x32_bf16 v[124:127], a[120:123], v[180:183], v[124:127]\n"
"v_cvt_pk_bf16_f32 v144, v152, v153\n"
"v_cvt_pk_bf16_f32 v145, v154, v155\n"
"v_mfma_f32_16x16x32_bf16 v[124:127], a[116:119], v[224:227], v[124:127]\n"
"s_add_u32 s98, s70, 0x202000\n"
"buffer_load_dword v254, v10, s[4:7], s97 offen offset:1280 sc1\n"
"v_mfma_f32_16x16x32_bf16 v[124:127], a[112:115], v[132:135], v[124:127]\n"
"buffer_load_dwordx4 v[152:155], v1, s[4:7], s96 offen offset:1024 sc1\n"
"v_mfma_f32_16x16x32_bf16 v[124:127], a[108:111], v[140:143], v[124:127]\n"
"buffer_load_dwordx4 a[160:163], v0, s[4:7], s98 offen sc1\n"
"v_mfma_f32_16x16x32_bf16 v[124:127], a[104:107], v[164:167], v[124:127]\n"
"ds_write_b128 v3, v[92:95] offset:2048\n"
"v_mfma_f32_16x16x32_bf16 v[124:127], a[100:103], v[88:91], v[124:127]\n"
"buffer_load_dword v249, v10, s[4:7], s97 offen offset:1536 sc1\n"
"v_mfma_f32_16x16x32_bf16 v[124:127], a[96:99], v[128:131], v[124:127]\n"
"v_cvt_pk_bf16_f32 v146, v108, v109\n"
"v_cvt_pk_bf16_f32 v147, v110, v111\n"
"v_mfma_f32_16x16x32_bf16 v[92:95], v[24:27], v[128:131], 0\n"
"buffer_load_dwordx4 v[108:111], v1, s[4:7], s96 offen offset:2048 sc1\n"
"v_mfma_f32_16x16x32_bf16 v[92:95], v[28:31], v[88:91], v[92:95]\n"
"buffer_load_dword v248, v10, s[4:7], s97 offen offset:1792 sc1\n"
"v_mfma_f32_16x16x32_bf16 v[92:95], v[32:35], v[164:167], v[92:95]\n"
"ds_write_b128 v2, v[144:147] offset:49152\n"
"v_mfma_f32_16x16x32_bf16 v[92:95], v[36:39], v[140:143], v[92:95]\n"
"v_mfma_f32_16x16x32_bf16 v[92:95], v[40:43], v[132:135], v[92:95]\n"
"v_mfma_f32_16x16x32_bf16 v[92:95], v[44:47], v[224:227], v[92:95]\n"
"buffer_load_dwordx4 v[144:147], v1, s[4:7], s96 offen offset:3072 sc1\n"
"v_mfma_f32_16x16x32_bf16 v[92:95], v[48:51], v[180:183], v[92:95]\n"
"ds_write_b128 v3, v[124:127] offset:3072\n"
"v_mfma_f32_16x16x32_bf16 v[92:95], v[52:55], v[148:151], v[92:95]\n"
"v_mfma_f32_16x16x32_bf16 v[124:127], v[84:87], v[148:151], 0\n"
"v_mfma_f32_16x16x32_bf16 v[124:127], v[80:83], v[180:183], v[124:127]\n"
"v_mfma_f32_16x16x32_bf16 v[124:127], v[76:79], v[224:227], v[124:127]\n"
"v_mfma_f32_16x16x32_bf16 v[124:127], v[72:75], v[132:135], v[124:127]\n"
"v_mfma_f32_16x16x32_bf16 v[124:127], v[68:71], v[140:143], v[124:127]\n"
"v_mfma_f32_16x16x32_bf16 v[124:127], v[64:67], v[164:167], v[124:127]\n"
"s_nop 1\n"
"ds_write_b128 v3, v[92:95] offset:4096\n"
"v_mfma_f32_16x16x32_bf16 v[124:127], v[60:63], v[88:91], v[124:127]\n"
"v_mfma_f32_16x16x32_bf16 v[124:127], v[56:59], v[128:131], v[124:127]\n"
"s_waitcnt vmcnt(24)\n"
"; @B G1 1.1\n"
"v_mfma_f32_16x16x32_bf16 v[88:91], a[0:3], v[116:119], 0\n"
"buffer_load_dwordx4 a[164:167], v0, s[4:7], s98 offen offset:1024 sc1\n"
"v_mfma_f32_16x16x32_bf16 v[88:91], a[4:7], v[160:163], v[88:91]\n"
"v_mfma_f32_16x16x32_bf16 v[88:91], a[8:11], v[136:139], v[88:91]\n"
"s_add_u32 s97, s55, 0x448000\n"
"buffer_load_dwordx4 v[92:95], v0, s[4:7], s97 offen sc1\n"
"v_mfma_f32_16x16x32_bf16 v[88:91], a[12:15], v[156:159], v[88:91]\n"
"s_waitcnt vmcnt(21)\n"
"v_mfma_f32_16x16x32_bf16 v[88:91], a[16:19], v[192:195], v[88:91]\n"
"v_mfma_f32_16x16x32_bf16 v[88:91], a[20:23], v[184:187], v[88:91]\n"
"ds_write_b128 v3, v[124:127] offset:5120\n"
"v_mfma_f32_16x16x32_bf16 v[88:91], a[24:27], v[176:179], v[88:91]\n"
"v_mfma_f32_16x16x32_bf16 v[88:91], a[28:31], v[200:203], v[88:91]\n"
"buffer_load_dwordx4 v[124:127], v0, s[4:7], s97 offen offset:1024 sc1\n"
"v_mfma_f32_16x16x32_bf16 v[128:131], a[60:63], v[200:203], 0\n"
"v_mfma_f32_16x16x32_bf16 v[128:131], a[56:59], v[176:179], v[128:131]\n"
"v_mfma_f32_16x16x32_bf16 v[128:131], a[52:55], v[184:187], v[128:131]\n"
"v_mfma_f32_16x16x32_bf16 v[128:131], a[48:51], v[192:195], v[128:131]\n"
"v_mfma_f32_16x16x32_bf16 v[128:131], a[44:47], v[156:159], v[128:131]\n"
"s_add_u32 s96, s67, 0x10000\n"
"s_nop 0\n"
"v_cmp_u_f32_e32 vcc, v88, v88\n"
"v_mfma_f32_16x16x32_bf16 v[128:131], a[40:43], v[136:139], v[128:131]\n"
"s_cbranch_vccnz .Lmk_p49s\n"
".Lmk_p49r:\n"
"v_mfma_f32_16x16x32_bf16 v[128:131], a[36:39], v[160:163], v[128:131]\n"
"buffer_load_dwordx4 v[132:135], v0, s[4:7], s97 offen offset:2048 sc1\n"
"v_mfma_f32_16x16x32_bf16 v[128:131], a[32:35], v[116:119], v[128:131]\n"
"ds_write_b128 v3, v[88:91] offset:6144\n"
"v_mfma_f32_16x16x32_bf16 v[88:91], a[64:67], v[116:119], 0\n"
"buffer_load_dwordx2 v[246:247], v12, s[4:7], s96 offen sc1\n"
"v_mfma_f32_16x16x32_bf16 v[88:91], a[68:71], v[160:163], v[88:91]\n"
"buffer_load_dwordx4 v[140:143], v0, s[4:7], s97 offen offset:3072 sc1\n"
"v_mfma_f32_16x16x32_bf16 v[88:91], a[72:75], v[136:139], v[88:91]\n"
"v_mfma_f32_16x16x32_bf16 v[88:91], a[76:79], v[156:159], v[88:91]\n"
"v_mfma_f32_16x16x32_bf16 v[88:91], a[80:83], v[192:195], v[88:91]\n"
"v_mov_b32_e32 v148, -1\n"
"v_mov_b32_e32 v149, -1\n"
"v_mfma_f32_16x16x32_bf16 v[88:91], a[84:87], v[184:187], v[88:91]\n"
"ds_write_b128 v3, v[128:131] offset:7168\n"
"v_mfma_f32_16x16x32_bf16 v[88:91], a[88:91], v[176:179], v[88:91]\n"
"buffer_load_dwordx4 v[128:131], v1, s[4:7], s97 offen sc1\n"
"v_mfma_f32_16x16x32_bf16 v[88:91], a[92:95], v[200:203], v[88:91]\n"
"v_mov_b32_e32 v150, -1\n"
"v_mov_b32_e32 v151, -1\n"
"v_mfma_f32_16x16x32_bf16 v[164:167], a[124:127], v[200:203], 0\n"
"s_add_u32 s95, s61, 0x840000\n"
"buffer_store_dwordx4 v[148:151], v0, s[8:11], s95 offen sc1\n"
"v_mfma_f32_16x16x32_bf16 v[164:167], a[120:123], v[176:179], v[164:167]\n"
"buffer_load_dwordx4 a[168:171], v0, s[4:7], s98 offen offset:2048 sc1\n"
"v_mfma_f32_16x16x32_bf16 v[164:167], a[116:119], v[184:187], v[164:167]\n"
"v_mfma_f32_16x16x32_bf16 v[164:167], a[112:115], v[192:195], v[164:167]\n"
"buffer_load_dwordx4 v[168:171], v1, s[4:7], s97 offen offset:1024 sc1\n"
"v_mfma_f32_16x16x32_bf16 v[164:167], a[108:111], v[156:159], v[164:167]\n"
"v_mfma_f32_16x16x32_bf16 v[164:167], a[104:107], v[136:139], v[164:167]\n"
"ds_write_b128 v3, v[88:91] offset:8192\n"
"v_mfma_f32_16x16x32_bf16 v[164:167], a[100:103], v[160:163], v[164:167]\n"
"v_mfma_f32_16x16x32_bf16 v[164:167], a[96:99], v[116:119], v[164:167]\n"
"v_mfma_f32_16x16x32_bf16 v[88:91], v[24:27], v[116:119], 0\n"
"buffer_load_dwordx4 v[172:175], v1, s[4:7], s97 offen offset:2048 sc1\n"
"v_mfma_f32_16x16x32_bf16 v[88:91], v[28:31], v[160:163], v[88:91]\n"
"v_mfma_f32_16x16x32_bf16 v[88:91], v[32:35], v[136:139], v[88:91]\n"
"v_mfma_f32_16x16x32_bf16 v[88:91], v[36:39], v[156:159], v[88:91]\n"
"v_mfma_f32_16x16x32_bf16 v[88:91], v[40:43], v[192:195], v[88:91]\n"
"s_waitcnt vmcnt(12)\n"
"v_add_f32_e32 v255, v255, v250\n"
"v_add_f32_e32 v253, v253, v254\n"
"v_mfma_f32_16x16x32_bf16 v[88:91], v[44:47], v[184:187], v[88:91]\n"
"buffer_load_dwordx4 v[180:183], v1, s[4:7], s97 offen offset:3072 sc1\n"
"v_mfma_f32_16x16x32_bf16 v[88:91], v[48:51], v[176:179], v[88:91]\n"
"ds_write_b128 v3, v[164:167] offset:9216\n"
"v_mfma_f32_16x16x32_bf16 v[88:91], v[52:55], v[200:203], v[88:91]\n"
"v_add_f32_e32 v252, v252, v249\n"
"v_add_f32_e32 v251, v251, v248\n"
"v_mfma_f32_16x16x32_bf16 v[164:167], v[84:87], v[200:203], 0\n"
"v_add_f32_e32 v255, v255, v252\n"
"v_add_f32_e32 v253, v253, v251\n"
"v_mfma_f32_16x16x32_bf16 v[164:167], v[80:83], v[176:179], v[164:167]\n"
"v_add_f32_e32 v255, v255, v253\n"
"v_cmp_u_f32_e32 vcc, v255, v255\n"
"v_mfma_f32_16x16x32_bf16 v[164:167], v[76:79], v[184:187], v[164:167]\n"
"s_cbranch_vccnz .Lmk_m52s\n"
".Lmk_m52r:\n"
"v_mov_b32_e32 v253, v255\n"
"v_mfma_f32_16x16x32_bf16 v[164:167], v[72:75], v[192:195], v[164:167]\n"
"s_nop 0\n"
"v_permlane32_swap_b32_e32 v255, v253\n"
"v_add_f32_e32 v255, v255, v253\n"
"v_mfma_f32_16x16x32_bf16 v[164:167], v[68:71], v[156:159], v[164:167]\n"
"v_mul_f32_e32 v255, 0x3a800000, v255\n"
"v_mov_b32_e32 v253, v255\n"
"v_mfma_f32_16x16x32_bf16 v[164:167], v[64:67], v[136:139], v[164:167]\n"
"ds_write_b128 v3, v[88:91] offset:10240\n"
"v_mfma_f32_16x16x32_bf16 v[164:167], v[60:63], v[160:163], v[164:167]\n"
"buffer_load_dwordx4 a[172:175], v0, s[4:7], s98 offen offset:3072 sc1\n"
"v_mfma_f32_16x16x32_bf16 v[164:167], v[56:59], v[116:119], v[164:167]\n"
"v_permlane16_swap_b32_e32 v255, v253\n"
"v_cvt_pk_bf16_f32 v254, -v255, 0\n"
"; @B G1 1.2\n"
"v_mfma_f32_16x16x32_bf16 v[88:91], a[0:3], v[96:99], 0\n"
"v_fma_f32 v253, -v255, v255, v253\n"
"v_lshlrev_b32_e32 v254, 16, v254\n"
"v_mfma_f32_16x16x32_bf16 v[88:91], a[4:7], v[100:103], v[88:91]\n"
"s_waitcnt vmcnt(8)\n"
"v_cmp_ne_u64_e32 vcc, v[246:247], v[18:19]\n"
"v_mfma_f32_16x16x32_bf16 v[88:91], a[8:11], v[112:115], v[88:91]\n"
"s_cbranch_vccnz .Lmk_f39s\n"
".Lmk_f39r:\n"
"v_max_f32_e32 v253, 0, v253\n"
"v_mfma_f32_16x16x32_bf16 v[88:91], a[12:15], v[120:123], v[88:91]\n"
"v_cvt_pk_bf16_f32 v253, v253, 0\n"
"v_lshlrev_b32_e32 v253, 16, v253\n"
"v_mfma_f32_16x16x32_bf16 v[88:91], a[16:19], v[104:107], v[88:91]\n"
"v_add_f32_e32 v253, 0x3727c5ac, v253\n"
"v_cvt_pk_bf16_f32 v253, v253, 0\n"
"v_mfma_f32_16x16x32_bf16 v[88:91], a[20:23], v[152:155], v[88:91]\n"
"ds_write_b128 v3, v[164:167] offset:11264\n"
"v_mfma_f32_16x16x32_bf16 v[88:91], a[24:27], v[108:111], v[88:91]\n"
"v_lshlrev_b32_e32 v253, 16, v253\n"
"v_mfma_f32_16x16x32_bf16 v[88:91], a[28:31], v[144:147], v[88:91]\n"
"v_sqrt_f32_e32 v253, v253\n"
"v_mfma_f32_16x16x32_bf16 v[116:119], a[60:63], v[144:147], 0\n"
"v_cvt_pk_bf16_f32 v253, v253, 0\n"
"v_lshlrev_b32_e32 v253, 16, v253\n"
"v_mfma_f32_16x16x32_bf16 v[116:119], a[56:59], v[108:111], v[116:119]\n"
"v_rcp_f32_e32 v255, v253\n"
"v_mfma_f32_16x16x32_bf16 v[116:119], a[52:55], v[152:155], v[116:119]\n"
"s_add_u32 s97, s52, 0x408000\n"
"buffer_load_dwordx4 v[136:139], v0, s[4:7], s97 offen sc1\n"
"v_mfma_f32_16x16x32_bf16 v[116:119], a[48:51], v[104:107], v[116:119]\n"
"s_waitcnt lgkmcnt(0)\n"
"s_barrier\n"
"v_mfma_f32_16x16x32_bf16 v[116:119], a[44:47], v[120:123], v[116:119]\n"
"v_cmp_u_f32_e32 vcc, v88, v88\n"
"v_mul_f32_e32 v253, 0x3e8293ee, v255\n"
"v_mfma_f32_16x16x32_bf16 v[116:119], a[40:43], v[112:115], v[116:119]\n"
"s_cbranch_vccnz .Lmk_p50s\n"
".Lmk_p50r:\n"
"ds_read_b128 v[156:159], v4\n"
"v_mfma_f32_16x16x32_bf16 v[116:119], a[36:39], v[100:103], v[116:119]\n"
"ds_write_b128 v3, v[88:91] offset:12288\n"
"v_mfma_f32_16x16x32_bf16 v[116:119], a[32:35], v[96:99], v[116:119]\n"
"buffer_load_dwordx4 v[88:91], v0, s[4:7], s97 offen offset:1024 sc1\n"
"v_mfma_f32_16x16x32_bf16 v[160:163], a[64:67], v[96:99], 0\n"
"ds_read_b128 v[164:167], v9 offset:640\n"
"ds_read_b128 v[176:179], v5\n"
"v_mfma_f32_16x16x32_bf16 v[160:163], a[68:71], v[100:103], v[160:163]\n"
"v_mfma_f32_16x16x32_bf16 v[160:163], a[72:75], v[112:115], v[160:163]\n"
"buffer_load_dwordx4 a[176:179], v1, s[4:7], s98 offen sc1\n"
"v_mfma_f32_16x16x32_bf16 v[160:163], a[76:79], v[120:123], v[160:163]\n"
"ds_read_b128 v[184:187], v4 offset:1024\n"
"ds_read_b128 v[188:191], v9 offset:704\n"
"v_mfma_f32_16x16x32_bf16 v[160:163], a[80:83], v[104:107], v[160:163]\n"
"buffer_load_dwordx4 v[192:195], v0, s[4:7], s97 offen offset:2048 sc1\n"
"v_mfma_f32_16x16x32_bf16 v[160:163], a[84:87], v[152:155], v[160:163]\n"
"ds_write_b128 v3, v[116:119] offset:13312\n"
"v_mfma_f32_16x16x32_bf16 v[160:163], a[88:91], v[108:111], v[160:163]\n"
"ds_read_b128 v[116:119], v5 offset:1024\n"
"s_waitcnt lgkmcnt(4)\n"
"v_mfma_f32_16x16x32_bf16 v[160:163], a[92:95], v[144:147], v[160:163]\n"
"v_fmac_f32_e32 v156, v254, v164\n"
"v_fmac_f32_e32 v157, v254, v165\n"
"v_mfma_f32_16x16x32_bf16 v[196:199], a[124:127], v[144:147], 0\n"
"v_fmac_f32_e32 v158, v254, v166\n"
"v_fmac_f32_e32 v159, v254, v167\n"
"v_mfma_f32_16x16x32_bf16 v[196:199], a[120:123], v[108:111], v[196:199]\n"
"buffer_load_dwordx4 v[164:167], v0, s[4:7], s97 offen offset:3072 sc1\n"
"v_mfma_f32_16x16x32_bf16 v[196:199], a[116:119], v[152:155], v[196:199]\n"
"ds_read_b128 v[200:203], v4 offset:2048\n"
"ds_read_b128 v[204:207], v9 offset:768\n"
"v_mfma_f32_16x16x32_bf16 v[196:199], a[112:115], v[104:107], v[196:199]\n"
"ds_read_b128 v[208:211], v5 offset:2048\n"
"v_add_f32_e32 v156, v156, v176\n"
"v_mfma_f32_16x16x32_bf16 v[196:199], a[108:111], v[120:123], v[196:199]\n"
"v_add_f32_e32 v157, v157, v177\n"
"v_add_f32_e32 v158, v158, v178\n"
"v_mfma_f32_16x16x32_bf16 v[196:199], a[104:107], v[112:115], v[196:199]\n"
"ds_write_b128 v3, v[160:163] offset:14336\n"
"v_mfma_f32_16x16x32_bf16 v[196:199], a[100:103], v[100:103], v[196:199]\n"
"buffer_load_dwordx4 v[160:163], v1, s[4:7], s97 offen sc1\n"
"v_mfma_f32_16x16x32_bf16 v[196:199], a[96:99], v[96:99], v[196:199]\n"
"s_add_u32 s96, s61, 0x840000\n"
"v_add_f32_e32 v159, v159, v179\n"
"s_waitcnt lgkmcnt(4)\n"
"v_mfma_f32_16x16x32_bf16 v[176:179], v[24:27], v[96:99], 0\n"
"buffer_store_dwordx4 v[148:151], v0, s[8:11], s96 offen offset:1024 sc1\n"
"v_mfma_f32_16x16x32_bf16 v[176:179], v[28:31], v[100:103], v[176:179]\n"
"ds_read_b128 v[212:215], v4 offset:3072\n"
"ds_read_b128 v[216:219], v9 offset:832\n"
"v_mfma_f32_16x16x32_bf16 v[176:179], v[32:35], v[112:115], v[176:179]\n"
"ds_read_b128 v[220:223], v5 offset:3072\n"
"v_fmac_f32_e32 v184, v254, v188\n"
"v_mfma_f32_16x16x32_bf16 v[176:179], v[36:39], v[120:123], v[176:179]\n"
"buffer_load_dwordx4 v[224:227], v1, s[4:7], s97 offen offset:1024 sc1\n"
"v_mfma_f32_16x16x32_bf16 v[176:179], v[40:43], v[104:107], v[176:179]\n"
"v_fmac_f32_e32 v185, v254, v189\n"
"v_fmac_f32_e32 v186, v254, v190\n"
"v_mfma_f32_16x16x32_bf16 v[176:179], v[44:47], v[152:155], v[176:179]\n"
"ds_write_b128 v3, v[196:199] offset:15360\n"
"v_mfma_f32_16x16x32_bf16 v[176:179], v[48:51], v[108:111], v[176:179]\n"
"buffer_load_dwordx4 a[180:183], v1, s[4:7], s98 offen offset:1024 sc1\n"
"v_mfma_f32_16x16x32_bf16 v[176:179], v[52:55], v[144:147], v[176:179]\n"
"v_fmac_f32_e32 v187, v254, v191\n"
"v_add_f32_e32 v184, v184, v116\n"
"v_mfma_f32_16x16x32_bf16 v[188:191], v[84:87], v[144:147], 0\n"
"buffer_load_dwordx4 v[144:147], v1, s[4:7], s97 offen offset:2048 sc1\n"
"v_mfma_f32_16x16x32_bf16 v[188:191], v[80:83], v[108:111], v[188:191]\n"
"ds_read_b128 v[108:111], v4 offset:4096\n"
"ds_read_b128 v[196:199], v9 offset:512\n"
"v_mfma_f32_16x16x32_bf16 v[188:191], v[76:79], v[152:155], v[188:191]\n"
"ds_read_b128 v[152:155], v5 offset:4096\n"
"v_add_f32_e32 v185, v185, v117\n"
"v_mfma_f32_16x16x32_bf16 v[188:191], v[72:75], v[104:107], v[188:191]\n"
"s_waitcnt lgkmcnt(8)\n"
"v_add_f32_e32 v186, v186, v118\n"
"v_add_f32_e32 v187, v187, v119\n"
"v_mfma_f32_16x16x32_bf16 v[188:191], v[68:71], v[120:123], v[188:191]\n"
"v_fmac_f32_e32 v200, v254, v204\n"
"v_fmac_f32_e32 v201, v254, v205\n"
"v_mfma_f32_16x16x32_bf16 v[188:191], v[64:67], v[112:115], v[188:191]\n"
"buffer_load_dwordx4 v[104:107], v1, s[4:7], s97 offen offset:3072 sc1\n"
"v_mfma_f32_16x16x32_bf16 v[188:191], v[60:63], v[100:103], v[188:191]\n"
"ds_write_b128 v3, v[176:179] offset:16384\n"
"v_mfma_f32_16x16x32_bf16 v[188:191], v[56:59], v[96:99], v[188:191]\n"
"s_waitcnt vmcnt(18)\n"
"ds_read_b128 v[96:99], v4 offset:5120\n"
"ds_read_b128 v[100:103], v9 offset:576\n"
"; @B G1 1.3\n"
"v_mfma_f32_16x16x32_bf16 v[112:115], a[0:3], v[92:95], v[156:159]\n"
"ds_read_b128 v[116:119], v5 offset:5120\n"
"v_fmac_f32_e32 v202, v254, v206\n"
"v_mfma_f32_16x16x32_bf16 v[112:115], a[4:7], v[124:127], v[112:115]\n"
"v_fmac_f32_e32 v203, v254, v207\n"
"v_add_f32_e32 v200, v200, v208\n"
"v_mfma_f32_16x16x32_bf16 v[112:115], a[8:11], v[132:135], v[112:115]\n"
"s_add_u32 s97, s53, 0x408000\n"
"buffer_load_dwordx4 v[120:123], v0, s[4:7], s97 offen sc1\n"
"v_mfma_f32_16x16x32_bf16 v[112:115], a[12:15], v[140:143], v[112:115]\n"
"s_waitcnt vmcnt(13)\n"
"v_add_f32_e32 v201, v201, v209\n"
"v_add_f32_e32 v202, v202, v210\n"
"v_mfma_f32_16x16x32_bf16 v[112:115], a[16:19], v[128:131], v[112:115]\n"
"s_waitcnt lgkmcnt(8)\n"
"v_add_f32_e32 v203, v203, v211\n"
"v_fmac_f32_e32 v212, v254, v216\n"
"v_mfma_f32_16x16x32_bf16 v[112:115], a[20:23], v[168:171], v[112:115]\n"
"ds_write_b128 v3, v[188:191] offset:17408\n"
"v_mfma_f32_16x16x32_bf16 v[112:115], a[24:27], v[172:175], v[112:115]\n"
"v_fmac_f32_e32 v213, v254, v217\n"
"v_fmac_f32_e32 v214, v254, v218\n"
"v_mfma_f32_16x16x32_bf16 v[112:115], a[28:31], v[180:183], v[112:115]\n"
"buffer_load_dwordx4 v[176:179], v0, s[4:7], s97 offen offset:1024 sc1\n"
"v_mfma_f32_16x16x32_bf16 v[188:191], a[60:63], v[180:183], v[184:187]\n"
"v_fmac_f32_e32 v215, v254, v219\n"
"v_add_f32_e32 v212, v212, v220\n"
"v_mfma_f32_16x16x32_bf16 v[188:191], a[56:59], v[172:175], v[188:191]\n"
"v_add_f32_e32 v213, v213, v221\n"
"v_add_f32_e32 v214, v214, v222\n"
"v_mfma_f32_16x16x32_bf16 v[188:191], a[52:55], v[168:171], v[188:191]\n"
"s_waitcnt lgkmcnt(0)\n"
"s_barrier\n"
"v_mfma_f32_16x16x32_bf16 v[188:191], a[48:51], v[128:131], v[188:191]\n"
"buffer_load_dwordx4 a[184:187], v1, s[4:7], s98 offen offset:2048 sc1\n"
"v_mfma_f32_16x16x32_bf16 v[188:191], a[44:47], v[140:143], v[188:191]\n"
"ds_read_b128 v[204:207], v6\n"
"v_cmp_u_f32_e32 vcc, v112, v112\n"
"v_mfma_f32_16x16x32_bf16 v[188:191], a[40:43], v[132:135], v[188:191]\n"
"s_cbranch_vccnz .Lmk_p51s\n"
".Lmk_p51r:\n"
"ds_read_b128 v[156:159], v6 offset:1024\n"
"v_mfma_f32_16x16x32_bf16 v[188:191], a[36:39], v[124:127], v[188:191]\n"
"buffer_load_dwordx4 v[184:187], v0, s[4:7], s97 offen offset:2048 sc1\n"
"v_mfma_f32_16x16x32_bf16 v[188:191], a[32:35], v[92:95], v[188:191]\n"
"v_add_f32_e32 v215, v215, v223\n"
"v_fmac_f32_e32 v108, v254, v196\n"
"v_mfma_f32_16x16x32_bf16 v[200:203], a[64:67], v[92:95], v[200:203]\n"
"v_fmac_f32_e32 v109, v254, v197\n"
"v_fmac_f32_e32 v110, v254, v198\n"
"v_mfma_f32_16x16x32_bf16 v[200:203], a[68:71], v[124:127], v[200:203]\n"
"buffer_load_dwordx4 v[208:211], v0, s[4:7], s97 offen offset:3072 sc1\n"
"v_mfma_f32_16x16x32_bf16 v[200:203], a[72:75], v[132:135], v[200:203]\n"
"v_fmac_f32_e32 v111, v254, v199\n"
"v_add_f32_e32 v108, v108, v152\n"
"v_mfma_f32_16x16x32_bf16 v[200:203], a[76:79], v[140:143], v[200:203]\n"
"v_add_f32_e32 v109, v109, v153\n"
"v_add_f32_e32 v110, v110, v154\n"
"v_mfma_f32_16x16x32_bf16 v[200:203], a[80:83], v[128:131], v[200:203]\n"
"v_add_f32_e32 v111, v111, v155\n"
"v_fmac_f32_e32 v96, v254, v100\n"
"v_mfma_f32_16x16x32_bf16 v[200:203], a[84:87], v[168:171], v[200:203]\n"
"s_waitcnt lgkmcnt(0)\n"
"v_fmac_f32_e32 v97, v254, v101\n"
"v_fmac_f32_e32 v98, v254, v102\n"
"v_mfma_f32_16x16x32_bf16 v[200:203], a[88:91], v[172:175], v[200:203]\n"
"buffer_load_dwordx4 v[152:155], v1, s[4:7], s97 offen sc1\n"
"v_mfma_f32_16x16x32_bf16 v[200:203], a[92:95], v[180:183], v[200:203]\n"
"ds_read_b128 v[196:199], v6 offset:2048\n"
"ds_read_b128 v[216:219], v6 offset:3072\n"
"v_mfma_f32_16x16x32_bf16 v[212:215], a[124:127], v[180:183], v[212:215]\n"
"v_fmac_f32_e32 v99, v254, v103\n"
"v_add_f32_e32 v96, v96, v116\n"
"v_mfma_f32_16x16x32_bf16 v[212:215], a[120:123], v[172:175], v[212:215]\n"
"v_add_f32_e32 v97, v97, v117\n"
"v_add_f32_e32 v98, v98, v118\n"
"v_mfma_f32_16x16x32_bf16 v[212:215], a[116:119], v[168:171], v[212:215]\n"
"v_add_f32_e32 v99, v99, v119\n"
"v_add_f32_e32 v112, v112, v204\n"
"v_mfma_f32_16x16x32_bf16 v[212:215], a[112:115], v[128:131], v[212:215]\n"
"buffer_load_dwordx4 v[100:103], v1, s[4:7], s97 offen offset:1024 sc1\n"
"v_mfma_f32_16x16x32_bf16 v[212:215], a[108:111], v[140:143], v[212:215]\n"
"v_add_f32_e32 v113, v113, v205\n"
"v_add_f32_e32 v114, v114, v206\n"
"v_mfma_f32_16x16x32_bf16 v[212:215], a[104:107], v[132:135], v[212:215]\n"
"v_add_f32_e32 v115, v115, v207\n"
"v_add_f32_e32 v188, v188, v156\n"
"v_mfma_f32_16x16x32_bf16 v[212:215], a[100:103], v[124:127], v[212:215]\n"
"v_add_f32_e32 v189, v189, v157\n"
"v_add_f32_e32 v190, v190, v158\n"
"v_mfma_f32_16x16x32_bf16 v[212:215], a[96:99], v[92:95], v[212:215]\n"
"buffer_load_dwordx4 a[188:191], v1, s[4:7], s98 offen offset:3072 sc1\n"
"v_mfma_f32_16x16x32_bf16 v[108:111], v[24:27], v[92:95], v[108:111]\n"
"buffer_load_dwordx4 v[116:119], v1, s[4:7], s97 offen offset:2048 sc1\n"
"v_mfma_f32_16x16x32_bf16 v[108:111], v[28:31], v[124:127], v[108:111]\n"
"v_add_f32_e32 v191, v191, v159\n"
"v_mul_f32_e32 v112, v112, v255\n"
"v_mfma_f32_16x16x32_bf16 v[108:111], v[32:35], v[132:135], v[108:111]\n"
"v_mul_f32_e32 v113, v113, v255\n"
"v_mul_f32_e32 v114, v114, v255\n"
"v_mfma_f32_16x16x32_bf16 v[108:111], v[36:39], v[140:143], v[108:111]\n"
"v_mul_f32_e32 v115, v115, v255\n"
"v_mul_f32_e32 v188, v188, v255\n"
"v_mfma_f32_16x16x32_bf16 v[108:111], v[40:43], v[128:131], v[108:111]\n"
"s_waitcnt lgkmcnt(0)\n"
"v_mul_f32_e32 v189, v189, v255\n"
"v_mul_f32_e32 v190, v190, v255\n"
"v_mfma_f32_16x16x32_bf16 v[108:111], v[44:47], v[168:171], v[108:111]\n"
"buffer_load_dwordx4 v[156:159], v1, s[4:7], s97 offen offset:3072 sc1\n"
"v_mfma_f32_16x16x32_bf16 v[108:111], v[48:51], v[172:175], v[108:111]\n"
"s_add_u32 s98, s63, 0x84000\n"
"ds_read_b128 v[204:207], v6 offset:4096\n"
"ds_read_b128 v[220:223], v6 offset:5120\n"
"v_mfma_f32_16x16x32_bf16 v[108:111], v[52:55], v[180:183], v[108:111]\n"
"v_mul_f32_e32 v191, v191, v255\n"
"v_cvt_pk_bf16_f32 v228, v112, v113\n"
"v_mfma_f32_16x16x32_bf16 v[96:99], v[84:87], v[180:183], v[96:99]\n"
"buffer_store_dword v148, v14, s[8:11], s98 offen sc1\n"
"v_mfma_f32_16x16x32_bf16 v[96:99], v[80:83], v[172:175], v[96:99]\n"
"v_cvt_pk_bf16_f32 v229, v114, v115\n"
"v_cvt_pk_bf16_f32 v230, v188, v189\n"
"v_mfma_f32_16x16x32_bf16 v[96:99], v[76:79], v[168:171], v[96:99]\n"
"v_cvt_pk_bf16_f32 v231, v190, v191\n"
"v_add_f32_e32 v200, v200, v196\n"
"v_mfma_f32_16x16x32_bf16 v[96:99], v[72:75], v[128:131], v[96:99]\n"
"ds_write_b128 v8, v[228:231] offset:25600\n"
"v_mfma_f32_16x16x32_bf16 v[96:99], v[68:71], v[140:143], v[96:99]\n"
"v_add_f32_e32 v201, v201, v197\n"
"v_add_f32_e32 v202, v202, v198\n"
"v_mfma_f32_16x16x32_bf16 v[96:99], v[64:67], v[132:135], v[96:99]\n"
"v_add_f32_e32 v203, v203, v199\n"
"v_add_f32_e32 v212, v212, v216\n"
"v_mfma_f32_16x16x32_bf16 v[96:99], v[60:63], v[124:127], v[96:99]\n"
"v_add_f32_e32 v213, v213, v217\n"
"v_add_f32_e32 v214, v214, v218\n"
"v_mfma_f32_16x16x32_bf16 v[96:99], v[56:59], v[92:95], v[96:99]\n"
"v_add_f32_e32 v215, v215, v219\n"
"v_mul_f32_e32 v200, v200, v255\n"
"v_mul_f32_e32 v201, v201, v255\n"
"v_mul_f32_e32 v202, v202, v255\n"
"v_mul_f32_e32 v203, v203, v255\n"
"v_mul_f32_e32 v212, v212, v255\n"
"v_mul_f32_e32 v213, v213, v255\n"
"v_mul_f32_e32 v214, v214, v255\n"
"v_mul_f32_e32 v215, v215, v255\n"
"v_cvt_pk_bf16_f32 v92, v200, v201\n"
"v_cvt_pk_bf16_f32 v93, v202, v203\n"
"v_cvt_pk_bf16_f32 v94, v212, v213\n"
"v_cvt_pk_bf16_f32 v95, v214, v215\n"
"ds_write_b128 v7, v[92:95] offset:58368\n"
".Lmk_b2:\n"
"s_waitcnt vmcnt(17)\n"
"; @B G1 2.0\n"
"v_mfma_f32_16x16x32_bf16 v[112:115], a[0:3], v[136:139], 0\n"
"v_mfma_f32_16x16x32_bf16 v[112:115], a[4:7], v[88:91], v[112:115]\n"
"s_add_u32 s98, s62, 0x40800\n"
"v_mfma_f32_16x16x32_bf16 v[112:115], a[8:11], v[192:195], v[112:115]\n"
"s_add_u32 s97, s54, 0x408000\n"
"buffer_load_dwordx4 v[124:127], v0, s[4:7], s97 offen sc1\n"
"v_mfma_f32_16x16x32_bf16 v[112:115], a[12:15], v[164:167], v[112:115]\n"
"s_waitcnt vmcnt(12)\n"
"v_mfma_f32_16x16x32_bf16 v[112:115], a[16:19], v[160:163], v[112:115]\n"
"buffer_load_dword v255, v10, s[4:7], s98 offen sc1\n"
"v_mfma_f32_16x16x32_bf16 v[112:115], a[20:23], v[224:227], v[112:115]\n"
"s_waitcnt lgkmcnt(2)\n"
"v_add_f32_e32 v108, v108, v204\n"
"v_add_f32_e32 v109, v109, v205\n"
"v_mfma_f32_16x16x32_bf16 v[112:115], a[24:27], v[144:147], v[112:115]\n"
"v_add_f32_e32 v110, v110, v206\n"
"v_add_f32_e32 v111, v111, v207\n"
"v_mfma_f32_16x16x32_bf16 v[112:115], a[28:31], v[104:107], v[112:115]\n"
"buffer_load_dwordx4 v[128:131], v0, s[4:7], s97 offen offset:1024 sc1\n"
"v_mfma_f32_16x16x32_bf16 v[132:135], a[60:63], v[104:107], 0\n"
"s_add_u32 s96, s70, 0x204000\n"
"buffer_load_dwordx4 a[192:195], v0, s[4:7], s96 offen sc1\n"
"v_mfma_f32_16x16x32_bf16 v[132:135], a[56:59], v[144:147], v[132:135]\n"
"buffer_load_dword v254, v10, s[4:7], s98 offen offset:256 sc1\n"
"v_mfma_f32_16x16x32_bf16 v[132:135], a[52:55], v[224:227], v[132:135]\n"
"v_add_f32_e32 v96, v96, v220\n"
"v_add_f32_e32 v97, v97, v221\n"
"v_mfma_f32_16x16x32_bf16 v[132:135], a[48:51], v[160:163], v[132:135]\n"
"v_add_f32_e32 v98, v98, v222\n"
"v_add_f32_e32 v99, v99, v223\n"
"v_mfma_f32_16x16x32_bf16 v[132:135], a[44:47], v[164:167], v[132:135]\n"
"buffer_load_dwordx4 v[140:143], v0, s[4:7], s97 offen offset:2048 sc1\n"
"v_mfma_f32_16x16x32_bf16 v[132:135], a[40:43], v[192:195], v[132:135]\n"
"ds_write_b128 v3, v[112:115]\n"
"v_mfma_f32_16x16x32_bf16 v[132:135], a[36:39], v[88:91], v[132:135]\n"
"buffer_load_dword v252, v10, s[4:7], s98 offen offset:512 sc1\n"
"v_mfma_f32_16x16x32_bf16 v[132:135], a[32:35], v[136:139], v[132:135]\n"
"v_mul_f32_e32 v108, v108, v253\n"
"v_mul_f32_e32 v109, v109, v253\n"
"v_mfma_f32_16x16x32_bf16 v[112:115], a[64:67], v[136:139], 0\n"
"v_mul_f32_e32 v110, v110, v253\n"
"v_mul_f32_e32 v111, v111, v253\n"
"v_mfma_f32_16x16x32_bf16 v[112:115], a[68:71], v[88:91], v[112:115]\n"
"buffer_load_dwordx4 v[148:151], v0, s[4:7], s97 offen offset:3072 sc1\n"
"v_mfma_f32_16x16x32_bf16 v[112:115], a[72:75], v[192:195], v[112:115]\n"
"buffer_load_dword v251, v10, s[4:7], s98 offen offset:768 sc1\n"
"v_mfma_f32_16x16x32_bf16 v[112:115], a[76:79], v[164:167], v[112:115]\n"
"v_mul_f32_e32 v96, v96, v253\n"
"v_mul_f32_e32 v97, v97, v253\n"
"v_mfma_f32_16x16x32_bf16 v[112:115], a[80:83], v[160:163], v[112:115]\n"
"v_mul_f32_e32 v98, v98, v253\n"
"v_mul_f32_e32 v99, v99, v253\n"
"v_mfma_f32_16x16x32_bf16 v[112:115], a[84:87], v[224:227], v[112:115]\n"
"ds_write_b128 v3, v[132:135] offset:1024\n"
"v_mfma_f32_16x16x32_bf16 v[112:115], a[88:91], v[144:147], v[112:115]\n"
"buffer_load_dwordx4 v[132:135], v1, s[4:7], s97 offen sc1\n"
"v_mfma_f32_16x16x32_bf16 v[112:115], a[92:95], v[104:107], v[112:115]\n"
"buffer_load_dword v253, v10, s[4:7], s98 offen offset:1024 sc1\n"
"v_mfma_f32_16x16x32_bf16 v[168:171], a[124:127], v[104:107], 0\n"
"v_cvt_pk_bf16_f32 v172, v108, v109\n"
"v_cvt_pk_bf16_f32 v173, v110, v111\n"
"v_mfma_f32_16x16x32_bf16 v[168:171], a[120:123], v[144:147], v[168:171]\n"
"v_cvt_pk_bf16_f32 v174, v96, v97\n"
"v_cvt_pk_bf16_f32 v175, v98, v99\n"
"v_mfma_f32_16x16x32_bf16 v[168:171], a[116:119], v[224:227], v[168:171]\n"
"buffer_load_dword v250, v10, s[4:7], s98 offen offset:1280 sc1\n"
"v_mfma_f32_16x16x32_bf16 v[168:171], a[112:115], v[160:163], v[168:171]\n"
"buffer_load_dwordx4 v[96:99], v1, s[4:7], s97 offen offset:1024 sc1\n"
"v_mfma_f32_16x16x32_bf16 v[168:171], a[108:111], v[164:167], v[168:171]\n"
"buffer_load_dwordx4 a[196:199], v0, s[4:7], s96 offen offset:1024 sc1\n"
"v_mfma_f32_16x16x32_bf16 v[168:171], a[104:107], v[192:195], v[168:171]\n"
"ds_write_b128 v3, v[112:115] offset:2048\n"
"v_mfma_f32_16x16x32_bf16 v[168:171], a[100:103], v[88:91], v[168:171]\n"
"buffer_load_dword v249, v10, s[4:7], s98 offen offset:1536 sc1\n"
"v_mfma_f32_16x16x32_bf16 v[168:171], a[96:99], v[136:139], v[168:171]\n"
"ds_write_b128 v2, v[172:175] offset:50176\n"
"v_mfma_f32_16x16x32_bf16 v[108:111], v[24:27], v[136:139], 0\n"
"buffer_load_dwordx4 v[112:115], v1, s[4:7], s97 offen offset:2048 sc1\n"
"s_add_u32 s95, s64, 0x802000\n"
"v_mfma_f32_16x16x32_bf16 v[108:111], v[28:31], v[88:91], v[108:111]\n"
"buffer_load_dword v248, v10, s[4:7], s98 offen offset:1792 sc1\n"
"v_add_u32_e32 v247, 0x26c00, v0\n"
"v_mfma_f32_16x16x32_bf16 v[108:111], v[32:35], v[192:195], v[108:111]\n"
"buffer_store_dwordx4 v[228:231], v8, s[12:15], s95 offen offset:1024 sc1\n"
"v_mfma_f32_16x16x32_bf16 v[108:111], v[36:39], v[164:167], v[108:111]\n"
"ds_read_b128 v[172:175], v247\n"
"ds_read_b128 v[180:183], v247 offset:1024\n"
"v_xor_b32_e32 v247, 32, v15\n"
"v_mfma_f32_16x16x32_bf16 v[108:111], v[40:43], v[160:163], v[108:111]\n"
"ds_read_b128 v[188:191], v2 offset:50176\n"
"ds_read_b128 v[196:199], v15 offset:24576\n"
"ds_read_b128 v[200:203], v247 offset:24832\n"
"v_mfma_f32_16x16x32_bf16 v[108:111], v[44:47], v[224:227], v[108:111]\n"
"buffer_load_dwordx4 v[204:207], v1, s[4:7], s97 offen offset:3072 sc1\n"
"v_mfma_f32_16x16x32_bf16 v[108:111], v[48:51], v[144:147], v[108:111]\n"
"ds_write_b128 v3, v[168:171] offset:3072\n"
"v_mfma_f32_16x16x32_bf16 v[108:111], v[52:55], v[104:107], v[108:111]\n"
"v_mfma_f32_16x16x32_bf16 v[168:171], v[84:87], v[104:107], 0\n"
"v_mfma_f32_16x16x32_bf16 v[168:171], v[80:83], v[144:147], v[168:171]\n"
"s_waitcnt lgkmcnt(1)\n"
"v_mfma_f32_16x16x32_bf16 v[172:175], v[196:199], v[188:191], v[172:175]\n"
"v_mfma_f32_16x16x32_bf16 v[180:183], v[200:203], v[188:191], v[180:183]\n"
"ds_read_b64_tr_b16 v[104:105], v16 offset:24576\n"
"v_xor_b32_e32 v247, 32, v16\n"
"v_mfma_f32_16x16x32_bf16 v[168:171], v[76:79], v[224:227], v[168:171]\n"
"ds_read_b64_tr_b16 v[106:107], v247 offset:24832\n"
"ds_read_b64_tr_b16 v[144:145], v247 offset:24576\n"
"ds_read_b64_tr_b16 v[146:147], v16 offset:24832\n"
"v_mov_b32_e32 v188, 0x3f803f80\n"
"v_mfma_f32_16x16x32_bf16 v[168:171], v[72:75], v[160:163], v[168:171]\n"
"v_mov_b32_e32 v189, 0x3f803f80\n"
"v_mov_b32_e32 v190, 0x3f803f80\n"
"v_mov_b32_e32 v191, 0x3f803f80\n"
"v_mfma_f32_16x16x32_bf16 v[168:171], v[68:71], v[164:167], v[168:171]\n"
"v_max_f32_e32 v247, v172, v173\n"
"v_max3_f32 v247, v247, v174, v175\n"
"v_max3_f32 v247, v247, v180, v181\n"
"v_mfma_f32_16x16x32_bf16 v[168:171], v[64:67], v[192:195], v[168:171]\n"
"ds_write_b128 v3, v[108:111] offset:4096\n"
"v_mfma_f32_16x16x32_bf16 v[168:171], v[60:63], v[88:91], v[168:171]\n"
"v_max3_f32 v247, v247, v182, v183\n"
"v_mov_b32_e32 v246, v247\n"
"v_mfma_f32_16x16x32_bf16 v[168:171], v[56:59], v[136:139], v[168:171]\n"
"s_waitcnt vmcnt(25)\n"
"v_permlane16_swap_b32_e32 v247, v246\n"
"v_max_f32_e32 v247, v247, v246\n"
"v_mov_b32_e32 v246, v247\n"
"; @B G1 2.1\n"
"v_mfma_f32_16x16x32_bf16 v[88:91], a[0:3], v[120:123], 0\n"
"buffer_load_dwordx4 a[200:203], v0, s[4:7], s96 offen offset:2048 sc1\n"
"v_permlane32_swap_b32_e32 v247, v246\n"
"v_mfma_f32_16x16x32_bf16 v[88:91], a[4:7], v[176:179], v[88:91]\n"
"v_max_f32_e32 v247, v247, v246\n"
"v_sub_f32_e32 v172, v172, v247\n"
"v_sub_f32_e32 v173, v173, v247\n"
"v_mfma_f32_16x16x32_bf16 v[88:91], a[8:11], v[184:187], v[88:91]\n"
"s_add_u32 s98, s55, 0x408000\n"
"buffer_load_dwordx4 v[108:111], v0, s[4:7], s98 offen sc1\n"
"v_mfma_f32_16x16x32_bf16 v[88:91], a[12:15], v[208:211], v[88:91]\n"
"s_waitcnt vmcnt(22)\n"
"v_sub_f32_e32 v174, v174, v247\n"
"v_sub_f32_e32 v175, v175, v247\n"
"v_sub_f32_e32 v180, v180, v247\n"
"v_mfma_f32_16x16x32_bf16 v[88:91], a[16:19], v[152:155], v[88:91]\n"
"v_sub_f32_e32 v181, v181, v247\n"
"v_sub_f32_e32 v182, v182, v247\n"
"v_sub_f32_e32 v183, v183, v247\n"
"v_mfma_f32_16x16x32_bf16 v[88:91], a[20:23], v[100:103], v[88:91]\n"
"ds_write_b128 v3, v[168:171] offset:5120\n"
"v_mfma_f32_16x16x32_bf16 v[88:91], a[24:27], v[116:119], v[88:91]\n"
"v_exp_f32_e32 v172, v172\n"
"v_mfma_f32_16x16x32_bf16 v[88:91], a[28:31], v[156:159], v[88:91]\n"
"buffer_load_dwordx4 v[136:139], v0, s[4:7], s98 offen offset:1024 sc1\n"
"v_mfma_f32_16x16x32_bf16 v[160:163], a[60:63], v[156:159], 0\n"
"s_add_u32 s97, s64, 0x806000\n"
"v_exp_f32_e32 v173, v173\n"
"v_mfma_f32_16x16x32_bf16 v[160:163], a[56:59], v[116:119], v[160:163]\n"
"v_exp_f32_e32 v174, v174\n"
"v_mfma_f32_16x16x32_bf16 v[160:163], a[52:55], v[100:103], v[160:163]\n"
"buffer_store_dwordx4 v[92:95], v7, s[12:15], s97 offen offset:1024 sc1\n"
"v_mfma_f32_16x16x32_bf16 v[160:163], a[48:51], v[152:155], v[160:163]\n"
"v_exp_f32_e32 v175, v175\n"
"v_mfma_f32_16x16x32_bf16 v[160:163], a[44:47], v[208:211], v[160:163]\n"
"buffer_load_dwordx4 v[92:95], v0, s[4:7], s98 offen offset:2048 sc1\n"
"v_mfma_f32_16x16x32_bf16 v[160:163], a[40:43], v[184:187], v[160:163]\n"
"ds_write_b128 v3, v[88:91] offset:6144\n"
"v_mfma_f32_16x16x32_bf16 v[160:163], a[36:39], v[176:179], v[160:163]\n"
"v_exp_f32_e32 v180, v180\n"
"v_mfma_f32_16x16x32_bf16 v[160:163], a[32:35], v[120:123], v[160:163]\n"
"v_exp_f32_e32 v181, v181\n"
"v_mfma_f32_16x16x32_bf16 v[88:91], a[64:67], v[120:123], 0\n"
"v_exp_f32_e32 v182, v182\n"
"v_mfma_f32_16x16x32_bf16 v[88:91], a[68:71], v[176:179], v[88:91]\n"
"buffer_load_dwordx4 v[164:167], v0, s[4:7], s98 offen offset:3072 sc1\n"
"v_mfma_f32_16x16x32_bf16 v[88:91], a[72:75], v[184:187], v[88:91]\n"
"v_exp_f32_e32 v183, v183\n"
"v_cvt_pk_bf16_f32 v168, v172, v173\n"
"v_mfma_f32_16x16x32_bf16 v[88:91], a[76:79], v[208:211], v[88:91]\n"
"v_cvt_pk_bf16_f32 v169, v174, v175\n"
"v_cvt_pk_bf16_f32 v170, v180, v181\n"
"v_cvt_pk_bf16_f32 v171, v182, v183\n"
"v_mfma_f32_16x16x32_bf16 v[88:91], a[80:83], v[152:155], v[88:91]\n"
"s_waitcnt lgkmcnt(3)\n"
"v_mov_b32_e32 v172, -1\n"
"v_mov_b32_e32 v173, -1\n"
"v_mfma_f32_16x16x32_bf16 v[180:183], v[188:191], v[168:171], 0\n"
"v_mfma_f32_16x16x32_bf16 v[188:191], v[104:107], v[168:171], 0\n"
"v_mfma_f32_16x16x32_bf16 v[104:107], v[144:147], v[168:171], 0\n"
"v_mfma_f32_16x16x32_bf16 v[88:91], a[84:87], v[100:103], v[88:91]\n"
"ds_write_b128 v3, v[160:163] offset:7168\n"
"v_mfma_f32_16x16x32_bf16 v[88:91], a[88:91], v[116:119], v[88:91]\n"
"buffer_load_dwordx4 v[144:147], v1, s[4:7], s98 offen sc1\n"
"v_mfma_f32_16x16x32_bf16 v[88:91], a[92:95], v[156:159], v[88:91]\n"
"v_mov_b32_e32 v174, -1\n"
"v_mov_b32_e32 v175, -1\n"
"v_mfma_f32_16x16x32_bf16 v[160:163], a[124:127], v[156:159], 0\n"
"s_add_u32 s97, s60, 0x848000\n"
"buffer_store_dwordx4 v[172:175], v0, s[8:11], s97 offen sc1\n"
"v_mfma_f32_16x16x32_bf16 v[160:163], a[120:123], v[116:119], v[160:163]\n"
"buffer_load_dwordx4 a[204:207], v0, s[4:7], s96 offen offset:3072 sc1\n"
"v_mfma_f32_16x16x32_bf16 v[160:163], a[116:119], v[100:103], v[160:163]\n"
"v_rcp_f32_e32 v180, v180\n"
"v_mfma_f32_16x16x32_bf16 v[160:163], a[112:115], v[152:155], v[160:163]\n"
"buffer_load_dwordx4 v[168:171], v1, s[4:7], s98 offen offset:1024 sc1\n"
"v_mul_f32_e32 v188, v188, v180\n"
"v_mfma_f32_16x16x32_bf16 v[160:163], a[108:111], v[208:211], v[160:163]\n"
"v_mul_f32_e32 v189, v189, v180\n"
"v_mul_f32_e32 v190, v190, v180\n"
"v_mul_f32_e32 v191, v191, v180\n"
"v_mfma_f32_16x16x32_bf16 v[160:163], a[104:107], v[184:187], v[160:163]\n"
"ds_write_b128 v3, v[88:91] offset:8192\n"
"v_mfma_f32_16x16x32_bf16 v[160:163], a[100:103], v[176:179], v[160:163]\n"
"v_mul_f32_e32 v104, v104, v180\n"
"v_mul_f32_e32 v105, v105, v180\n"
"v_mul_f32_e32 v106, v106, v180\n"
"v_mfma_f32_16x16x32_bf16 v[160:163], a[96:99], v[120:123], v[160:163]\n"
"v_mul_f32_e32 v107, v107, v180\n"
"v_cvt_pk_bf16_f32 v88, v188, v189\n"
"v_cvt_pk_bf16_f32 v89, v190, v191\n"
"v_mfma_f32_16x16x32_bf16 v[180:183], v[24:27], v[120:123], 0\n"
"buffer_load_dwordx4 v[188:191], v1, s[4:7], s98 offen offset:2048 sc1\n"
"v_cvt_pk_bf16_f32 v90, v104, v105\n"
"v_mfma_f32_16x16x32_bf16 v[180:183], v[28:31], v[176:179], v[180:183]\n"
"s_add_u32 s97, s60, 0x448000\n"
"v_cvt_pk_bf16_f32 v91, v106, v107\n"
"v_mfma_f32_16x16x32_bf16 v[180:183], v[32:35], v[184:187], v[180:183]\n"
"buffer_store_dwordx4 v[88:91], v0, s[4:7], s97 offen sc1\n"
"v_mfma_f32_16x16x32_bf16 v[180:183], v[36:39], v[208:211], v[180:183]\n"
"v_mfma_f32_16x16x32_bf16 v[180:183], v[40:43], v[152:155], v[180:183]\n"
"s_waitcnt vmcnt(14)\n"
"v_add_f32_e32 v255, v255, v253\n"
"v_add_f32_e32 v254, v254, v250\n"
"v_mfma_f32_16x16x32_bf16 v[180:183], v[44:47], v[100:103], v[180:183]\n"
"buffer_load_dwordx4 v[88:91], v1, s[4:7], s98 offen offset:3072 sc1\n"
"v_mfma_f32_16x16x32_bf16 v[180:183], v[48:51], v[116:119], v[180:183]\n"
"ds_write_b128 v3, v[160:163] offset:9216\n"
"v_mfma_f32_16x16x32_bf16 v[180:183], v[52:55], v[156:159], v[180:183]\n"
"v_add_f32_e32 v252, v252, v249\n"
"v_add_f32_e32 v251, v251, v248\n"
"v_mfma_f32_16x16x32_bf16 v[104:107], v[84:87], v[156:159], 0\n"
"v_add_f32_e32 v255, v255, v252\n"
"v_add_f32_e32 v254, v254, v251\n"
"v_mfma_f32_16x16x32_bf16 v[104:107], v[80:83], v[116:119], v[104:107]\n"
"v_add_f32_e32 v255, v255, v254\n"
"v_mov_b32_e32 v254, v255\n"
"v_mfma_f32_16x16x32_bf16 v[104:107], v[76:79], v[100:103], v[104:107]\n"
"s_nop 0\n"
"v_permlane32_swap_b32_e32 v255, v254\n"
"v_add_f32_e32 v255, v255, v254\n"
"v_mfma_f32_16x16x32_bf16 v[104:107], v[72:75], v[152:155], v[104:107]\n"
"v_mul_f32_e32 v255, 0x3a800000, v255\n"
"v_mov_b32_e32 v254, v255\n"
"v_mfma_f32_16x16x32_bf16 v[104:107], v[68:71], v[208:211], v[104:107]\n"
"s_nop 0\n"
"v_permlane16_swap_b32_e32 v255, v254\n"
"v_cvt_pk_bf16_f32 v253, -v255, 0\n"
"v_mfma_f32_16x16x32_bf16 v[104:107], v[64:67], v[184:187], v[104:107]\n"
"ds_write_b128 v3, v[180:183] offset:10240\n"
"v_mfma_f32_16x16x32_bf16 v[104:107], v[60:63], v[176:179], v[104:107]\n"
"buffer_load_dwordx4 a[208:211], v1, s[4:7], s96 offen sc1\n"
"v_mfma_f32_16x16x32_bf16 v[104:107], v[56:59], v[120:123], v[104:107]\n"
"v_fma_f32 v254, -v255, v255, v254\n"
"v_lshlrev_b32_e32 v253, 16, v253\n"
"; @B G1 2.2\n"
"v_mfma_f32_16x16x32_bf16 v[100:103], a[0:3], v[124:127], 0\n"
"v_max_f32_e32 v254, 0, v254\n"
"v_cvt_pk_bf16_f32 v254, v254, 0\n"
"v_mfma_f32_16x16x32_bf16 v[100:103], a[4:7], v[128:131], v[100:103]\n"
"v_lshlrev_b32_e32 v254, 16, v254\n"
"v_add_f32_e32 v254, 0x3727c5ac, v254\n"
"v_mfma_f32_16x16x32_bf16 v[100:103], a[8:11], v[140:143], v[100:103]\n"
"v_cvt_pk_bf16_f32 v254, v254, 0\n"
"v_lshlrev_b32_e32 v254, 16, v254\n"
"v_mfma_f32_16x16x32_bf16 v[100:103], a[12:15], v[148:151], v[100:103]\n"
"s_waitcnt vmcnt(14)\n"
"v_sqrt_f32_e32 v254, v254\n"
"v_mfma_f32_16x16x32_bf16 v[100:103], a[16:19], v[132:135], v[100:103]\n"
"s_add_u32 s98, s68, 0x10000\n"
"v_cvt_pk_bf16_f32 v254, v254, 0\n"
"v_mfma_f32_16x16x32_bf16 v[100:103], a[20:23], v[96:99], v[100:103]\n"
"ds_write_b128 v3, v[104:107] offset:11264\n"
"v_mfma_f32_16x16x32_bf16 v[100:103], a[24:27], v[112:115], v[100:103]\n"
"buffer_store_dwordx2 v[18:19], v11, s[4:7], s98 offen offset:32 sc1\n"
"v_mfma_f32_16x16x32_bf16 v[100:103], a[28:31], v[204:207], v[100:103]\n"
"v_lshlrev_b32_e32 v254, 16, v254\n"
"v_mfma_f32_16x16x32_bf16 v[104:107], a[60:63], v[204:207], 0\n"
"v_rcp_f32_e32 v255, v254\n"
"v_mfma_f32_16x16x32_bf16 v[104:107], a[56:59], v[112:115], v[104:107]\n"
"v_mul_f32_e32 v254, 0x3e8293ee, v255\n"
"v_mfma_f32_16x16x32_bf16 v[104:107], a[52:55], v[96:99], v[104:107]\n"
"s_add_u32 s98, s52, 0x400000\n"
"buffer_load_dwordx4 v[116:119], v0, s[4:7], s98 offen sc1\n"
"v_mfma_f32_16x16x32_bf16 v[104:107], a[48:51], v[132:135], v[104:107]\n"
"s_waitcnt lgkmcnt(0)\n"
"s_barrier\n"
"v_mfma_f32_16x16x32_bf16 v[104:107], a[44:47], v[148:151], v[104:107]\n"
"ds_read_b128 v[120:123], v4\n"
"ds_read_b128 v[152:155], v9 offset:640\n"
"v_mfma_f32_16x16x32_bf16 v[104:107], a[40:43], v[140:143], v[104:107]\n"
"ds_write_b128 v3, v[100:103] offset:12288\n"
"v_mfma_f32_16x16x32_bf16 v[104:107], a[36:39], v[128:131], v[104:107]\n"
"ds_read_b128 v[100:103], v5\n"
"v_mfma_f32_16x16x32_bf16 v[104:107], a[32:35], v[124:127], v[104:107]\n"
"buffer_load_dwordx4 v[156:159], v0, s[4:7], s98 offen offset:1024 sc1\n"
"v_mfma_f32_16x16x32_bf16 v[160:163], a[64:67], v[124:127], 0\n"
"v_mfma_f32_16x16x32_bf16 v[160:163], a[68:71], v[128:131], v[160:163]\n"
"v_mfma_f32_16x16x32_bf16 v[160:163], a[72:75], v[140:143], v[160:163]\n"
"buffer_load_dwordx4 a[212:215], v1, s[4:7], s96 offen offset:1024 sc1\n"
"v_mfma_f32_16x16x32_bf16 v[160:163], a[76:79], v[148:151], v[160:163]\n"
"ds_read_b128 v[176:179], v4 offset:1024\n"
"ds_read_b128 v[180:183], v9 offset:704\n"
"v_mfma_f32_16x16x32_bf16 v[160:163], a[80:83], v[132:135], v[160:163]\n"
"buffer_load_dwordx4 v[184:187], v0, s[4:7], s98 offen offset:2048 sc1\n"
"v_mfma_f32_16x16x32_bf16 v[160:163], a[84:87], v[96:99], v[160:163]\n"
"ds_write_b128 v3, v[104:107] offset:13312\n"
"v_mfma_f32_16x16x32_bf16 v[160:163], a[88:91], v[112:115], v[160:163]\n"
"ds_read_b128 v[104:107], v5 offset:1024\n"
"s_waitcnt lgkmcnt(4)\n"
"v_mfma_f32_16x16x32_bf16 v[160:163], a[92:95], v[204:207], v[160:163]\n"
"v_fmac_f32_e32 v120, v253, v152\n"
"v_fmac_f32_e32 v121, v253, v153\n"
"v_mfma_f32_16x16x32_bf16 v[192:195], a[124:127], v[204:207], 0\n"
"v_fmac_f32_e32 v122, v253, v154\n"
"v_fmac_f32_e32 v123, v253, v155\n"
"v_mfma_f32_16x16x32_bf16 v[192:195], a[120:123], v[112:115], v[192:195]\n"
"buffer_load_dwordx4 v[152:155], v0, s[4:7], s98 offen offset:3072 sc1\n"
"v_mfma_f32_16x16x32_bf16 v[192:195], a[116:119], v[96:99], v[192:195]\n"
"ds_read_b128 v[196:199], v4 offset:2048\n"
"ds_read_b128 v[200:203], v9 offset:768\n"
"v_mfma_f32_16x16x32_bf16 v[192:195], a[112:115], v[132:135], v[192:195]\n"
"ds_read_b128 v[208:211], v5 offset:2048\n"
"v_add_f32_e32 v120, v120, v100\n"
"v_mfma_f32_16x16x32_bf16 v[192:195], a[108:111], v[148:151], v[192:195]\n"
"v_add_f32_e32 v121, v121, v101\n"
"v_add_f32_e32 v122, v122, v102\n"
"v_mfma_f32_16x16x32_bf16 v[192:195], a[104:107], v[140:143], v[192:195]\n"
"ds_write_b128 v3, v[160:163] offset:14336\n"
"v_mfma_f32_16x16x32_bf16 v[192:195], a[100:103], v[128:131], v[192:195]\n"
"buffer_load_dwordx4 v[160:163], v1, s[4:7], s98 offen sc1\n"
"v_mfma_f32_16x16x32_bf16 v[192:195], a[96:99], v[124:127], v[192:195]\n"
"s_add_u32 s97, s60, 0x840000\n"
"v_add_f32_e32 v123, v123, v103\n"
"s_waitcnt lgkmcnt(4)\n"
"v_mfma_f32_16x16x32_bf16 v[100:103], v[24:27], v[124:127], 0\n"
"buffer_store_dwordx4 v[172:175], v0, s[8:11], s97 offen sc1\n"
"v_mfma_f32_16x16x32_bf16 v[100:103], v[28:31], v[128:131], v[100:103]\n"
"ds_read_b128 v[212:215], v4 offset:3072\n"
"ds_read_b128 v[216:219], v9 offset:832\n"
"v_mfma_f32_16x16x32_bf16 v[100:103], v[32:35], v[140:143], v[100:103]\n"
"s_waitcnt vmcnt(18)\n"
"s_add_u32 s97, s68, 0x11000\n"
"ds_read_b128 v[220:223], v5 offset:3072\n"
"v_mfma_f32_16x16x32_bf16 v[100:103], v[36:39], v[148:151], v[100:103]\n"
"buffer_load_dwordx4 v[224:227], v1, s[4:7], s98 offen offset:1024 sc1\n"
"v_mfma_f32_16x16x32_bf16 v[100:103], v[40:43], v[132:135], v[100:103]\n"
"buffer_store_dwordx2 v[18:19], v11, s[12:15], s97 offen offset:64 sc1\n"
"v_mfma_f32_16x16x32_bf16 v[100:103], v[44:47], v[96:99], v[100:103]\n"
"ds_write_b128 v3, v[192:195] offset:15360\n"
"v_mfma_f32_16x16x32_bf16 v[100:103], v[48:51], v[112:115], v[100:103]\n"
"buffer_load_dwordx4 a[216:219], v1, s[4:7], s96 offen offset:2048 sc1\n"
"v_mfma_f32_16x16x32_bf16 v[100:103], v[52:55], v[204:207], v[100:103]\n"
"v_fmac_f32_e32 v176, v253, v180\n"
"v_fmac_f32_e32 v177, v253, v181\n"
"v_mfma_f32_16x16x32_bf16 v[192:195], v[84:87], v[204:207], 0\n"
"buffer_load_dwordx4 v[204:207], v1, s[4:7], s98 offen offset:2048 sc1\n"
"v_mfma_f32_16x16x32_bf16 v[192:195], v[80:83], v[112:115], v[192:195]\n"
"ds_read_b128 v[112:115], v4 offset:4096\n"
"ds_read_b128 v[228:231], v9 offset:512\n"
"v_mfma_f32_16x16x32_bf16 v[192:195], v[76:79], v[96:99], v[192:195]\n"
"ds_read_b128 v[96:99], v5 offset:4096\n"
"v_fmac_f32_e32 v178, v253, v182\n"
"v_mfma_f32_16x16x32_bf16 v[192:195], v[72:75], v[132:135], v[192:195]\n"
"v_fmac_f32_e32 v179, v253, v183\n"
"v_add_f32_e32 v176, v176, v104\n"
"v_mfma_f32_16x16x32_bf16 v[192:195], v[68:71], v[148:151], v[192:195]\n"
"v_add_f32_e32 v177, v177, v105\n"
"v_add_f32_e32 v178, v178, v106\n"
"v_mfma_f32_16x16x32_bf16 v[192:195], v[64:67], v[140:143], v[192:195]\n"
"buffer_load_dwordx4 v[132:135], v1, s[4:7], s98 offen offset:3072 sc1\n"
"v_mfma_f32_16x16x32_bf16 v[192:195], v[60:63], v[128:131], v[192:195]\n"
"ds_write_b128 v3, v[100:103] offset:16384\n"
"v_mfma_f32_16x16x32_bf16 v[192:195], v[56:59], v[124:127], v[192:195]\n"
"s_waitcnt vmcnt(21)\n"
"ds_read_b128 v[100:103], v4 offset:5120\n"
"ds_read_b128 v[124:127], v9 offset:576\n"
"; @B G1 2.3\n"
"v_mfma_f32_16x16x32_bf16 v[120:123], a[0:3], v[108:111], v[120:123]\n"
"ds_read_b128 v[128:131], v5 offset:5120\n"
"v_add_f32_e32 v179, v179, v107\n"
"v_mfma_f32_16x16x32_bf16 v[120:123], a[4:7], v[136:139], v[120:123]\n"
"s_waitcnt lgkmcnt(12)\n"
"v_fmac_f32_e32 v196, v253, v200\n"
"v_fmac_f32_e32 v197, v253, v201\n"
"v_mfma_f32_16x16x32_bf16 v[120:123], a[8:11], v[92:95], v[120:123]\n"
"s_add_u32 s98, s53, 0x400000\n"
"buffer_load_dwordx4 v[104:107], v0, s[4:7], s98 offen sc1\n"
"v_mfma_f32_16x16x32_bf16 v[120:123], a[12:15], v[164:167], v[120:123]\n"
"s_waitcnt vmcnt(15)\n"
"v_fmac_f32_e32 v198, v253, v202\n"
"v_fmac_f32_e32 v199, v253, v203\n"
"v_mfma_f32_16x16x32_bf16 v[120:123], a[16:19], v[144:147], v[120:123]\n"
"v_add_f32_e32 v196, v196, v208\n"
"v_add_f32_e32 v197, v197, v209\n"
"v_mfma_f32_16x16x32_bf16 v[120:123], a[20:23], v[168:171], v[120:123]\n"
"ds_write_b128 v3, v[192:195] offset:17408\n"
"v_mfma_f32_16x16x32_bf16 v[120:123], a[24:27], v[188:191], v[120:123]\n"
"s_waitcnt lgkmcnt(9)\n"
"v_add_f32_e32 v198, v198, v210\n"
"v_add_f32_e32 v199, v199, v211\n"
"v_mfma_f32_16x16x32_bf16 v[120:123], a[28:31], v[88:91], v[120:123]\n"
"buffer_load_dwordx4 v[140:143], v0, s[4:7], s98 offen offset:1024 sc1\n"
"v_mfma_f32_16x16x32_bf16 v[176:179], a[60:63], v[88:91], v[176:179]\n"
"v_fmac_f32_e32 v212, v253, v216\n"
"v_fmac_f32_e32 v213, v253, v217\n"
"v_mfma_f32_16x16x32_bf16 v[176:179], a[56:59], v[188:191], v[176:179]\n"
"v_fmac_f32_e32 v214, v253, v218\n"
"v_fmac_f32_e32 v215, v253, v219\n"
"v_mfma_f32_16x16x32_bf16 v[176:179], a[52:55], v[168:171], v[176:179]\n"
"s_waitcnt lgkmcnt(0)\n"
"s_barrier\n"
"v_mfma_f32_16x16x32_bf16 v[176:179], a[48:51], v[144:147], v[176:179]\n"
"buffer_load_dwordx4 a[220:223], v1, s[4:7], s96 offen offset:3072 sc1\n"
"v_mfma_f32_16x16x32_bf16 v[176:179], a[44:47], v[164:167], v[176:179]\n"
"buffer_load_dwordx4 v[148:151], v0, s[4:7], s98 offen offset:2048 sc1\n"
"v_mfma_f32_16x16x32_bf16 v[176:179], a[40:43], v[92:95], v[176:179]\n"
"ds_read_b128 v[180:183], v6\n"
"ds_read_b128 v[192:195], v6 offset:1024\n"
"v_mfma_f32_16x16x32_bf16 v[176:179], a[36:39], v[136:139], v[176:179]\n"
"v_add_f32_e32 v212, v212, v220\n"
"v_add_f32_e32 v213, v213, v221\n"
"v_mfma_f32_16x16x32_bf16 v[176:179], a[32:35], v[108:111], v[176:179]\n"
"v_add_f32_e32 v214, v214, v222\n"
"v_add_f32_e32 v215, v215, v223\n"
"v_mfma_f32_16x16x32_bf16 v[196:199], a[64:67], v[108:111], v[196:199]\n"
"v_fmac_f32_e32 v112, v253, v228\n"
"v_fmac_f32_e32 v113, v253, v229\n"
"v_mfma_f32_16x16x32_bf16 v[196:199], a[68:71], v[136:139], v[196:199]\n"
"buffer_load_dwordx4 v[200:203], v0, s[4:7], s98 offen offset:3072 sc1\n"
"v_mfma_f32_16x16x32_bf16 v[196:199], a[72:75], v[92:95], v[196:199]\n"
"v_fmac_f32_e32 v114, v253, v230\n"
"v_fmac_f32_e32 v115, v253, v231\n"
"v_mfma_f32_16x16x32_bf16 v[196:199], a[76:79], v[164:167], v[196:199]\n"
"v_add_f32_e32 v112, v112, v96\n"
"v_add_f32_e32 v113, v113, v97\n"
"v_mfma_f32_16x16x32_bf16 v[196:199], a[80:83], v[144:147], v[196:199]\n"
"v_add_f32_e32 v114, v114, v98\n"
"v_add_f32_e32 v115, v115, v99\n"
"v_mfma_f32_16x16x32_bf16 v[196:199], a[84:87], v[168:171], v[196:199]\n"
"s_waitcnt lgkmcnt(0)\n"
"v_fmac_f32_e32 v100, v253, v124\n"
"v_fmac_f32_e32 v101, v253, v125\n"
"v_mfma_f32_16x16x32_bf16 v[196:199], a[88:91], v[188:191], v[196:199]\n"
"buffer_load_dwordx4 v[96:99], v1, s[4:7], s98 offen sc1\n"
"v_mfma_f32_16x16x32_bf16 v[196:199], a[92:95], v[88:91], v[196:199]\n"
"ds_read_b128 v[208:211], v6 offset:2048\n"
"ds_read_b128 v[216:219], v6 offset:3072\n"
"v_mfma_f32_16x16x32_bf16 v[212:215], a[124:127], v[88:91], v[212:215]\n"
"v_fmac_f32_e32 v102, v253, v126\n"
"v_fmac_f32_e32 v103, v253, v127\n"
"v_mfma_f32_16x16x32_bf16 v[212:215], a[120:123], v[188:191], v[212:215]\n"
"v_add_f32_e32 v100, v100, v128\n"
"v_add_f32_e32 v101, v101, v129\n"
"v_mfma_f32_16x16x32_bf16 v[212:215], a[116:119], v[168:171], v[212:215]\n"
"v_add_f32_e32 v102, v102, v130\n"
"v_add_f32_e32 v103, v103, v131\n"
"v_mfma_f32_16x16x32_bf16 v[212:215], a[112:115], v[144:147], v[212:215]\n"
"buffer_load_dwordx4 v[124:127], v1, s[4:7], s98 offen offset:1024 sc1\n"
"v_mfma_f32_16x16x32_bf16 v[212:215], a[108:111], v[164:167], v[212:215]\n"
"v_add_f32_e32 v120, v120, v180\n"
"v_add_f32_e32 v121, v121, v181\n"
"v_mfma_f32_16x16x32_bf16 v[212:215], a[104:107], v[92:95], v[212:215]\n"
"s_add_u32 s97, s70, 0x206000\n"
"v_add_f32_e32 v122, v122, v182\n"
"v_add_f32_e32 v123, v123, v183\n"
"v_mfma_f32_16x16x32_bf16 v[212:215], a[100:103], v[136:139], v[212:215]\n"
"v_add_f32_e32 v176, v176, v192\n"
"v_add_f32_e32 v177, v177, v193\n"
"v_mfma_f32_16x16x32_bf16 v[212:215], a[96:99], v[108:111], v[212:215]\n"
"buffer_load_dwordx4 a[224:227], v0, s[4:7], s97 offen sc1\n"
"v_mfma_f32_16x16x32_bf16 v[112:115], v[24:27], v[108:111], v[112:115]\n"
"buffer_load_dwordx4 v[128:131], v1, s[4:7], s98 offen offset:2048 sc1\n"
"v_mfma_f32_16x16x32_bf16 v[112:115], v[28:31], v[136:139], v[112:115]\n"
"v_add_f32_e32 v178, v178, v194\n"
"v_add_f32_e32 v179, v179, v195\n"
"v_mfma_f32_16x16x32_bf16 v[112:115], v[32:35], v[92:95], v[112:115]\n"
"v_mul_f32_e32 v120, v120, v255\n"
"v_mul_f32_e32 v121, v121, v255\n"
"v_mfma_f32_16x16x32_bf16 v[112:115], v[36:39], v[164:167], v[112:115]\n"
"v_mul_f32_e32 v122, v122, v255\n"
"v_mul_f32_e32 v123, v123, v255\n"
"v_mfma_f32_16x16x32_bf16 v[112:115], v[40:43], v[144:147], v[112:115]\n"
"s_waitcnt lgkmcnt(0)\n"
"v_mul_f32_e32 v176, v176, v255\n"
"v_mul_f32_e32 v177, v177, v255\n"
"v_mfma_f32_16x16x32_bf16 v[112:115], v[44:47], v[168:171], v[112:115]\n"
"buffer_load_dwordx4 v[180:183], v1, s[4:7], s98 offen offset:3072 sc1\n"
"v_mfma_f32_16x16x32_bf16 v[112:115], v[48:51], v[188:191], v[112:115]\n"
"s_add_u32 s98, s60, 0x800000\n"
"ds_read_b128 v[192:195], v6 offset:4096\n"
"ds_read_b128 v[220:223], v6 offset:5120\n"
"v_mfma_f32_16x16x32_bf16 v[112:115], v[52:55], v[88:91], v[112:115]\n"
"v_mul_f32_e32 v178, v178, v255\n"
"v_mul_f32_e32 v179, v179, v255\n"
"v_mfma_f32_16x16x32_bf16 v[100:103], v[84:87], v[88:91], v[100:103]\n"
"buffer_store_dwordx4 v[172:175], v0, s[8:11], s98 offen sc1\n"
"v_mfma_f32_16x16x32_bf16 v[100:103], v[80:83], v[188:191], v[100:103]\n"
"v_cvt_pk_bf16_f32 v88, v120, v121\n"
"v_cvt_pk_bf16_f32 v89, v122, v123\n"
"v_mfma_f32_16x16x32_bf16 v[100:103], v[76:79], v[168:171], v[100:103]\n"
"v_cvt_pk_bf16_f32 v90, v176, v177\n"
"v_cvt_pk_bf16_f32 v91, v178, v179\n"
"v_mfma_f32_16x16x32_bf16 v[100:103], v[72:75], v[144:147], v[100:103]\n"
"ds_write_b128 v8, v[88:91] offset:17408\n"
"v_mfma_f32_16x16x32_bf16 v[100:103], v[68:71], v[164:167], v[100:103]\n"
"v_add_f32_e32 v196, v196, v208\n"
"v_add_f32_e32 v197, v197, v209\n"
"v_mfma_f32_16x16x32_bf16 v[100:103], v[64:67], v[92:95], v[100:103]\n"
"v_add_f32_e32 v198, v198, v210\n"
"v_add_f32_e32 v199, v199, v211\n"
"v_mfma_f32_16x16x32_bf16 v[100:103], v[60:63], v[136:139], v[100:103]\n"
"v_add_f32_e32 v212, v212, v216\n"
"v_add_f32_e32 v213, v213, v217\n"
"v_mfma_f32_16x16x32_bf16 v[100:103], v[56:59], v[108:111], v[100:103]\n"
"v_add_f32_e32 v214, v214, v218\n"
"v_add_f32_e32 v215, v215, v219\n"
"v_mul_f32_e32 v196, v196, v255\n"
"v_mul_f32_e32 v197, v197, v255\n"
"v_mul_f32_e32 v198, v198, v255\n"
"v_mul_f32_e32 v199, v199, v255\n"
"v_mul_f32_e32 v212, v212, v255\n"
"v_mul_f32_e32 v213, v213, v255\n"
"v_mul_f32_e32 v214, v214, v255\n"
"v_mul_f32_e32 v215, v215, v255\n"
"v_cvt_pk_bf16_f32 v88, v196, v197\n"
"v_cvt_pk_bf16_f32 v89, v198, v199\n"
"v_cvt_pk_bf16_f32 v90, v212, v213\n"
"v_cvt_pk_bf16_f32 v91, v214, v215\n"
"ds_write_b128 v7, v[88:91] offset:50176\n"
".Lmk_b3:\n"
"s_waitcnt vmcnt(18)\n"
"; @B G1 3.0\n"
"v_mfma_f32_16x16x32_bf16 v[88:91], a[0:3], v[116:119], 0\n"
"v_mfma_f32_16x16x32_bf16 v[88:91], a[4:7], v[156:159], v[88:91]\n"
"s_add_u32 s98, s62, 0x40000\n"
"v_mfma_f32_16x16x32_bf16 v[88:91], a[8:11], v[184:187], v[88:91]\n"
"s_add_u32 s96, s54, 0x400000\n"
"buffer_load_dwordx4 v[92:95], v0, s[4:7], s96 offen sc1\n"
"v_mfma_f32_16x16x32_bf16 v[88:91], a[12:15], v[152:155], v[88:91]\n"
"s_waitcnt vmcnt(12)\n"
"v_mfma_f32_16x16x32_bf16 v[88:91], a[16:19], v[160:163], v[88:91]\n"
"buffer_load_dword v255, v10, s[4:7], s98 offen sc1\n"
"v_mfma_f32_16x16x32_bf16 v[88:91], a[20:23], v[224:227], v[88:91]\n"
"s_waitcnt lgkmcnt(2)\n"
"v_add_f32_e32 v112, v112, v192\n"
"v_add_f32_e32 v113, v113, v193\n"
"v_mfma_f32_16x16x32_bf16 v[88:91], a[24:27], v[204:207], v[88:91]\n"
"v_add_f32_e32 v114, v114, v194\n"
"v_add_f32_e32 v115, v115, v195\n"
"v_mfma_f32_16x16x32_bf16 v[88:91], a[28:31], v[132:135], v[88:91]\n"
"buffer_load_dwordx4 v[108:111], v0, s[4:7], s96 offen offset:1024 sc1\n"
"v_mfma_f32_16x16x32_bf16 v[120:123], a[60:63], v[132:135], 0\n"
"s_waitcnt lgkmcnt(0)\n"
"s_barrier\n"
"v_mfma_f32_16x16x32_bf16 v[120:123], a[56:59], v[204:207], v[120:123]\n"
"buffer_load_dwordx4 a[228:231], v0, s[4:7], s97 offen offset:1024 sc1\n"
"v_mfma_f32_16x16x32_bf16 v[120:123], a[52:55], v[224:227], v[120:123]\n"
"buffer_load_dword v253, v10, s[4:7], s98 offen offset:256 sc1\n"
"v_mfma_f32_16x16x32_bf16 v[120:123], a[48:51], v[160:163], v[120:123]\n"
"v_add_f32_e32 v100, v100, v220\n"
"v_add_f32_e32 v101, v101, v221\n"
"v_mfma_f32_16x16x32_bf16 v[120:123], a[44:47], v[152:155], v[120:123]\n"
"buffer_load_dwordx4 v[136:139], v0, s[4:7], s96 offen offset:2048 sc1\n"
"v_mfma_f32_16x16x32_bf16 v[120:123], a[40:43], v[184:187], v[120:123]\n"
"ds_write_b128 v3, v[88:91]\n"
"v_mfma_f32_16x16x32_bf16 v[120:123], a[36:39], v[156:159], v[120:123]\n"
"buffer_load_dword v252, v10, s[4:7], s98 offen offset:512 sc1\n"
"v_mfma_f32_16x16x32_bf16 v[120:123], a[32:35], v[116:119], v[120:123]\n"
"v_add_f32_e32 v102, v102, v222\n"
"v_add_f32_e32 v103, v103, v223\n"
"v_mfma_f32_16x16x32_bf16 v[88:91], a[64:67], v[116:119], 0\n"
"v_mul_f32_e32 v112, v112, v254\n"
"v_mul_f32_e32 v113, v113, v254\n"
"v_mfma_f32_16x16x32_bf16 v[88:91], a[68:71], v[156:159], v[88:91]\n"
"buffer_load_dwordx4 v[144:147], v0, s[4:7], s96 offen offset:3072 sc1\n"
"v_mfma_f32_16x16x32_bf16 v[88:91], a[72:75], v[184:187], v[88:91]\n"
"buffer_load_dword v251, v10, s[4:7], s98 offen offset:768 sc1\n"
"v_mfma_f32_16x16x32_bf16 v[88:91], a[76:79], v[152:155], v[88:91]\n"
"v_mul_f32_e32 v114, v114, v254\n"
"v_mul_f32_e32 v115, v115, v254\n"
"v_mfma_f32_16x16x32_bf16 v[88:91], a[80:83], v[160:163], v[88:91]\n"
"v_mul_f32_e32 v100, v100, v254\n"
"v_mul_f32_e32 v101, v101, v254\n"
"v_add_u32_e32 v250, 0x26c00, v0\n"
"v_mfma_f32_16x16x32_bf16 v[88:91], a[84:87], v[224:227], v[88:91]\n"
"ds_write_b128 v3, v[120:123] offset:1024\n"
"v_mfma_f32_16x16x32_bf16 v[88:91], a[88:91], v[204:207], v[88:91]\n"
"buffer_load_dwordx4 v[120:123], v1, s[4:7], s96 offen sc1\n"
"ds_read_b128 v[164:167], v250\n"
"v_mfma_f32_16x16x32_bf16 v[88:91], a[92:95], v[132:135], v[88:91]\n"
"buffer_load_dwordx4 a[232:235], v0, s[4:7], s97 offen offset:2048 sc1\n"
"ds_read_b128 v[168:171], v250 offset:1024\n"
"v_mfma_f32_16x16x32_bf16 v[172:175], a[124:127], v[132:135], 0\n"
"buffer_load_dword v250, v10, s[4:7], s98 offen offset:1024 sc1\n"
"v_xor_b32_e32 v249, 32, v17\n"
"v_mfma_f32_16x16x32_bf16 v[172:175], a[120:123], v[204:207], v[172:175]\n"
"ds_read_b128 v[176:179], v2 offset:49152\n"
"v_mul_f32_e32 v102, v102, v254\n"
"v_mul_f32_e32 v103, v103, v254\n"
"v_mfma_f32_16x16x32_bf16 v[172:175], a[116:119], v[224:227], v[172:175]\n"
"v_cvt_pk_bf16_f32 v188, v112, v113\n"
"v_cvt_pk_bf16_f32 v189, v114, v115\n"
"v_mfma_f32_16x16x32_bf16 v[172:175], a[112:115], v[160:163], v[172:175]\n"
"ds_read_b128 v[112:115], v17 offset:23552\n"
"buffer_load_dwordx4 v[192:195], v1, s[4:7], s96 offen offset:1024 sc1\n"
"ds_read_b128 v[196:199], v249 offset:23808\n"
"v_mfma_f32_16x16x32_bf16 v[172:175], a[108:111], v[152:155], v[172:175]\n"
"buffer_load_dword v254, v10, s[4:7], s98 offen offset:1280 sc1\n"
"v_mfma_f32_16x16x32_bf16 v[172:175], a[104:107], v[184:187], v[172:175]\n"
"ds_write_b128 v3, v[88:91] offset:2048\n"
"v_mfma_f32_16x16x32_bf16 v[172:175], a[100:103], v[156:159], v[172:175]\n"
"v_cvt_pk_bf16_f32 v190, v100, v101\n"
"v_cvt_pk_bf16_f32 v191, v102, v103\n"
"v_mfma_f32_16x16x32_bf16 v[172:175], a[96:99], v[116:119], v[172:175]\n"
"buffer_load_dword v249, v10, s[4:7], s98 offen offset:1536 sc1\n"
"v_mfma_f32_16x16x32_bf16 v[88:91], v[24:27], v[116:119], 0\n"
"buffer_load_dwordx4 v[100:103], v1, s[4:7], s96 offen offset:2048 sc1\n"
"s_waitcnt lgkmcnt(1)\n"
"v_mfma_f32_16x16x32_bf16 v[164:167], v[112:115], v[176:179], v[164:167]\n"
"v_mfma_f32_16x16x32_bf16 v[168:171], v[196:199], v[176:179], v[168:171]\n"
"v_mfma_f32_16x16x32_bf16 v[88:91], v[28:31], v[156:159], v[88:91]\n"
"ds_write_b128 v2, v[188:191] offset:17408\n"
"v_mfma_f32_16x16x32_bf16 v[88:91], v[32:35], v[184:187], v[88:91]\n"
"buffer_load_dword v248, v10, s[4:7], s98 offen offset:1792 sc1\n"
"v_xor_b32_e32 v247, 32, v16\n"
"v_mfma_f32_16x16x32_bf16 v[88:91], v[36:39], v[152:155], v[88:91]\n"
"ds_read_b64_tr_b16 v[112:113], v16 offset:23552\n"
"ds_read_b64_tr_b16 v[114:115], v247 offset:23808\n"
"ds_read_b64_tr_b16 v[176:177], v247 offset:23552\n"
"v_mfma_f32_16x16x32_bf16 v[88:91], v[40:43], v[160:163], v[88:91]\n"
"buffer_load_dwordx4 a[236:239], v0, s[4:7], s97 offen offset:3072 sc1\n"
"ds_read_b64_tr_b16 v[178:179], v16 offset:23808\n"
"v_mfma_f32_16x16x32_bf16 v[88:91], v[44:47], v[224:227], v[88:91]\n"
"buffer_load_dwordx4 v[188:191], v1, s[4:7], s96 offen offset:3072 sc1\n"
"v_mov_b32_e32 v196, 0x3f803f80\n"
"v_mfma_f32_16x16x32_bf16 v[88:91], v[48:51], v[204:207], v[88:91]\n"
"ds_write_b128 v3, v[172:175] offset:3072\n"
"v_mfma_f32_16x16x32_bf16 v[88:91], v[52:55], v[132:135], v[88:91]\n"
"v_mov_b32_e32 v197, 0x3f803f80\n"
"v_mov_b32_e32 v198, 0x3f803f80\n"
"v_mov_b32_e32 v199, 0x3f803f80\n"
"v_mfma_f32_16x16x32_bf16 v[172:175], v[84:87], v[132:135], 0\n"
"v_max_f32_e32 v247, v164, v165\n"
"v_max3_f32 v247, v247, v166, v167\n"
"v_max3_f32 v247, v247, v168, v169\n"
"v_mfma_f32_16x16x32_bf16 v[172:175], v[80:83], v[204:207], v[172:175]\n"
"v_max3_f32 v247, v247, v170, v171\n"
"v_mov_b32_e32 v246, v247\n"
"v_mfma_f32_16x16x32_bf16 v[172:175], v[76:79], v[224:227], v[172:175]\n"
"s_nop 0\n"
"v_permlane16_swap_b32_e32 v247, v246\n"
"v_max_f32_e32 v247, v247, v246\n"
"v_mov_b32_e32 v246, v247\n"
"v_mfma_f32_16x16x32_bf16 v[172:175], v[72:75], v[160:163], v[172:175]\n"
"s_nop 0\n"
"v_permlane32_swap_b32_e32 v247, v246\n"
"v_max_f32_e32 v247, v247, v246\n"
"v_sub_f32_e32 v164, v164, v247\n"
"v_mfma_f32_16x16x32_bf16 v[172:175], v[68:71], v[152:155], v[172:175]\n"
"v_sub_f32_e32 v165, v165, v247\n"
"v_sub_f32_e32 v166, v166, v247\n"
"v_sub_f32_e32 v167, v167, v247\n"
"v_mfma_f32_16x16x32_bf16 v[172:175], v[64:67], v[184:187], v[172:175]\n"
"ds_write_b128 v3, v[88:91] offset:4096\n"
"v_mfma_f32_16x16x32_bf16 v[172:175], v[60:63], v[156:159], v[172:175]\n"
"v_sub_f32_e32 v168, v168, v247\n"
"v_sub_f32_e32 v169, v169, v247\n"
"v_sub_f32_e32 v170, v170, v247\n"
"v_mfma_f32_16x16x32_bf16 v[172:175], v[56:59], v[116:119], v[172:175]\n"
"s_waitcnt vmcnt(25)\n"
"v_sub_f32_e32 v171, v171, v247\n"
"v_exp_f32_e32 v164, v164\n"
"; @B G1 3.1\n"
"v_mfma_f32_16x16x32_bf16 v[88:91], a[0:3], v[104:107], 0\n"
"v_exp_f32_e32 v165, v165\n"
"v_mfma_f32_16x16x32_bf16 v[88:91], a[4:7], v[140:143], v[88:91]\n"
"buffer_load_dwordx4 a[240:243], v1, s[4:7], s97 offen sc1\n"
"v_mfma_f32_16x16x32_bf16 v[88:91], a[8:11], v[148:151], v[88:91]\n"
"s_add_u32 s98, s55, 0x400000\n"
"buffer_load_dwordx4 v[116:119], v0, s[4:7], s98 offen sc1\n"
"v_mfma_f32_16x16x32_bf16 v[88:91], a[12:15], v[200:203], v[88:91]\n"
"s_waitcnt vmcnt(22)\n"
"v_exp_f32_e32 v166, v166\n"
"v_mfma_f32_16x16x32_bf16 v[88:91], a[16:19], v[96:99], v[88:91]\n"
"v_exp_f32_e32 v167, v167\n"
"v_mfma_f32_16x16x32_bf16 v[88:91], a[20:23], v[124:127], v[88:91]\n"
"ds_write_b128 v3, v[172:175] offset:5120\n"
"v_mfma_f32_16x16x32_bf16 v[88:91], a[24:27], v[128:131], v[88:91]\n"
"v_exp_f32_e32 v168, v168\n"
"v_mfma_f32_16x16x32_bf16 v[88:91], a[28:31], v[180:183], v[88:91]\n"
"buffer_load_dwordx4 v[132:135], v0, s[4:7], s98 offen offset:1024 sc1\n"
"v_mfma_f32_16x16x32_bf16 v[152:155], a[60:63], v[180:183], 0\n"
"v_exp_f32_e32 v169, v169\n"
"v_mfma_f32_16x16x32_bf16 v[152:155], a[56:59], v[128:131], v[152:155]\n"
"v_exp_f32_e32 v170, v170\n"
"v_mfma_f32_16x16x32_bf16 v[152:155], a[52:55], v[124:127], v[152:155]\n"
"v_exp_f32_e32 v171, v171\n"
"v_cvt_pk_bf16_f32 v156, v164, v165\n"
"v_mfma_f32_16x16x32_bf16 v[152:155], a[48:51], v[96:99], v[152:155]\n"
"v_cvt_pk_bf16_f32 v157, v166, v167\n"
"v_cvt_pk_bf16_f32 v158, v168, v169\n"
"v_cvt_pk_bf16_f32 v159, v170, v171\n"
"v_mfma_f32_16x16x32_bf16 v[152:155], a[44:47], v[200:203], v[152:155]\n"
"buffer_load_dwordx4 v[160:163], v0, s[4:7], s98 offen offset:2048 sc1\n"
"s_waitcnt lgkmcnt(3)\n"
"v_mfma_f32_16x16x32_bf16 v[164:167], v[196:199], v[156:159], 0\n"
"v_mfma_f32_16x16x32_bf16 v[168:171], v[112:115], v[156:159], 0\n"
"v_mfma_f32_16x16x32_bf16 v[112:115], v[176:179], v[156:159], 0\n"
"v_mfma_f32_16x16x32_bf16 v[152:155], a[40:43], v[148:151], v[152:155]\n"
"ds_write_b128 v3, v[88:91] offset:6144\n"
"v_mfma_f32_16x16x32_bf16 v[152:155], a[36:39], v[140:143], v[152:155]\n"
"s_add_u32 s96, s73, 0\n"
"buffer_load_dwordx2 v[246:247], off, s[4:7], s96 sc1\n"
"v_mfma_f32_16x16x32_bf16 v[152:155], a[32:35], v[104:107], v[152:155]\n"
"buffer_load_dwordx4 a[244:247], v1, s[4:7], s97 offen offset:1024 sc1\n"
"v_mfma_f32_16x16x32_bf16 v[88:91], a[64:67], v[104:107], 0\n"
"v_mfma_f32_16x16x32_bf16 v[88:91], a[68:71], v[140:143], v[88:91]\n"
"buffer_load_dwordx4 v[156:159], v0, s[4:7], s98 offen offset:3072 sc1\n"
"v_mfma_f32_16x16x32_bf16 v[88:91], a[72:75], v[148:151], v[88:91]\n"
"v_rcp_f32_e32 v164, v164\n"
"v_mfma_f32_16x16x32_bf16 v[88:91], a[76:79], v[200:203], v[88:91]\n"
"v_mul_f32_e32 v168, v168, v164\n"
"v_mul_f32_e32 v169, v169, v164\n"
"v_mul_f32_e32 v170, v170, v164\n"
"v_mfma_f32_16x16x32_bf16 v[88:91], a[80:83], v[96:99], v[88:91]\n"
"v_mov_b32_e32 v172, -1\n"
"v_mov_b32_e32 v173, -1\n"
"v_mul_f32_e32 v171, v171, v164\n"
"v_mfma_f32_16x16x32_bf16 v[88:91], a[84:87], v[124:127], v[88:91]\n"
"ds_write_b128 v3, v[152:155] offset:7168\n"
"v_mfma_f32_16x16x32_bf16 v[88:91], a[88:91], v[128:131], v[88:91]\n"
"buffer_load_dwordx4 v[152:155], v1, s[4:7], s98 offen sc1\n"
"v_mul_f32_e32 v112, v112, v164\n"
"v_mfma_f32_16x16x32_bf16 v[88:91], a[92:95], v[180:183], v[88:91]\n"
"v_mov_b32_e32 v174, -1\n"
"v_mov_b32_e32 v175, -1\n"
"v_mul_f32_e32 v113, v113, v164\n"
"v_mfma_f32_16x16x32_bf16 v[176:179], a[124:127], v[180:183], 0\n"
"s_add_u32 s95, s60, 0x808000\n"
"buffer_store_dwordx4 v[172:175], v0, s[8:11], s95 offen sc1\n"
"v_mfma_f32_16x16x32_bf16 v[176:179], a[120:123], v[128:131], v[176:179]\n"
"v_mul_f32_e32 v114, v114, v164\n"
"v_mul_f32_e32 v115, v115, v164\n"
"v_cvt_pk_bf16_f32 v164, v168, v169\n"
"v_mfma_f32_16x16x32_bf16 v[176:179], a[116:119], v[124:127], v[176:179]\n"
"s_add_u32 s95, s60, 0x440000\n"
"v_cvt_pk_bf16_f32 v165, v170, v171\n"
"v_cvt_pk_bf16_f32 v166, v112, v113\n"
"v_cvt_pk_bf16_f32 v167, v114, v115\n"
"v_mfma_f32_16x16x32_bf16 v[176:179], a[112:115], v[96:99], v[176:179]\n"
"buffer_load_dwordx4 v[112:115], v1, s[4:7], s98 offen offset:1024 sc1\n"
"v_mfma_f32_16x16x32_bf16 v[176:179], a[108:111], v[200:203], v[176:179]\n"
"buffer_load_dwordx4 a[248:251], v1, s[4:7], s97 offen offset:2048 sc1\n"
"v_mfma_f32_16x16x32_bf16 v[176:179], a[104:107], v[148:151], v[176:179]\n"
"ds_write_b128 v3, v[88:91] offset:8192\n"
"v_mfma_f32_16x16x32_bf16 v[176:179], a[100:103], v[140:143], v[176:179]\n"
"buffer_store_dwordx4 v[164:167], v0, s[4:7], s95 offen sc1\n"
"v_mfma_f32_16x16x32_bf16 v[176:179], a[96:99], v[104:107], v[176:179]\n"
";\n"
"v_mfma_f32_16x16x32_bf16 v[88:91], v[24:27], v[104:107], 0\n"
"buffer_load_dwordx4 v[164:167], v1, s[4:7], s98 offen offset:2048 sc1\n"
"v_mfma_f32_16x16x32_bf16 v[88:91], v[28:31], v[140:143], v[88:91]\n"
"v_mfma_f32_16x16x32_bf16 v[88:91], v[32:35], v[148:151], v[88:91]\n"
"v_mfma_f32_16x16x32_bf16 v[88:91], v[36:39], v[200:203], v[88:91]\n"
"v_mfma_f32_16x16x32_bf16 v[88:91], v[40:43], v[96:99], v[88:91]\n"
"s_waitcnt vmcnt(15)\n"
"v_add_f32_e32 v255, v255, v250\n"
"v_add_f32_e32 v253, v253, v254\n"
"v_mfma_f32_16x16x32_bf16 v[88:91], v[44:47], v[124:127], v[88:91]\n"
"buffer_load_dwordx4 v[168:171], v1, s[4:7], s98 offen offset:3072 sc1\n"
"v_mfma_f32_16x16x32_bf16 v[88:91], v[48:51], v[128:131], v[88:91]\n"
"ds_write_b128 v3, v[176:179] offset:9216\n"
"v_mfma_f32_16x16x32_bf16 v[88:91], v[52:55], v[180:183], v[88:91]\n"
"v_add_f32_e32 v252, v252, v249\n"
"v_add_f32_e32 v251, v251, v248\n"
"v_mfma_f32_16x16x32_bf16 v[176:179], v[84:87], v[180:183], 0\n"
"v_add_f32_e32 v255, v255, v252\n"
"v_add_f32_e32 v253, v253, v251\n"
"v_mfma_f32_16x16x32_bf16 v[176:179], v[80:83], v[128:131], v[176:179]\n"
"buffer_load_dwordx4 a[252:255], v1, s[4:7], s97 offen offset:3072 sc1\n"
"v_mfma_f32_16x16x32_bf16 v[176:179], v[76:79], v[124:127], v[176:179]\n"
"v_add_f32_e32 v255, v255, v253\n"
"v_mov_b32_e32 v253, v255\n"
"v_mfma_f32_16x16x32_bf16 v[176:179], v[72:75], v[96:99], v[176:179]\n"
"s_nop 0\n"
"v_permlane32_swap_b32_e32 v255, v253\n"
"v_add_f32_e32 v255, v255, v253\n"
"v_mfma_f32_16x16x32_bf16 v[176:179], v[68:71], v[200:203], v[176:179]\n"
"v_mul_f32_e32 v255, 0x3a800000, v255\n"
"v_mov_b32_e32 v253, v255\n"
"v_mfma_f32_16x16x32_bf16 v[176:179], v[64:67], v[148:151], v[176:179]\n"
"ds_write_b128 v3, v[88:91] offset:10240\n"
"v_mfma_f32_16x16x32_bf16 v[176:179], v[60:63], v[140:143], v[176:179]\n"
"v_permlane16_swap_b32_e32 v255, v253\n"
"v_cvt_pk_bf16_f32 v254, -v255, 0\n"
"v_mfma_f32_16x16x32_bf16 v[176:179], v[56:59], v[104:107], v[176:179]\n"
"v_fma_f32 v253, -v255, v255, v253\n"
"v_lshlrev_b32_e32 v254, 16, v254\n"
"; @B G1 3.2\n"
"v_mfma_f32_16x16x32_bf16 v[88:91], a[0:3], v[92:95], 0\n"
"s_add_u32 s98, s67, 0x10020\n"
"buffer_load_dwordx2 v[250:251], v12, s[4:7], s98 offen sc1\n"
"v_mfma_f32_16x16x32_bf16 v[88:91], a[4:7], v[108:111], v[88:91]\n"
"v_max_f32_e32 v253, 0, v253\n"
"v_cvt_pk_bf16_f32 v253, v253, 0\n"
"v_mfma_f32_16x16x32_bf16 v[88:91], a[8:11], v[136:139], v[88:91]\n"
"v_lshlrev_b32_e32 v253, 16, v253\n"
"v_add_f32_e32 v253, 0x3727c5ac, v253\n"
"v_mfma_f32_16x16x32_bf16 v[88:91], a[12:15], v[144:147], v[88:91]\n"
"s_waitcnt vmcnt(16)\n"
"v_cvt_pk_bf16_f32 v253, v253, 0\n"
"v_lshlrev_b32_e32 v253, 16, v253\n"
"v_mfma_f32_16x16x32_bf16 v[88:91], a[16:19], v[120:123], v[88:91]\n"
"v_sqrt_f32_e32 v253, v253\n"
"v_mfma_f32_16x16x32_bf16 v[88:91], a[20:23], v[192:195], v[88:91]\n"
"ds_write_b128 v3, v[176:179] offset:11264\n"
"v_mfma_f32_16x16x32_bf16 v[88:91], a[24:27], v[100:103], v[88:91]\n"
"v_cvt_pk_bf16_f32 v253, v253, 0\n"
"v_lshlrev_b32_e32 v253, 16, v253\n"
"v_mfma_f32_16x16x32_bf16 v[88:91], a[28:31], v[188:191], v[88:91]\n"
"v_rcp_f32_e32 v255, v253\n"
"v_mfma_f32_16x16x32_bf16 v[96:99], a[60:63], v[188:191], 0\n"
"v_mul_f32_e32 v253, 0x3e8293ee, v255\n"
"v_add_u32_e32 v252, 0x26c00, v0\n"
"v_mfma_f32_16x16x32_bf16 v[96:99], a[56:59], v[100:103], v[96:99]\n"
"ds_read_b128 v[104:107], v252\n"
"ds_read_b128 v[124:127], v252 offset:1024\n"
"v_mfma_f32_16x16x32_bf16 v[96:99], a[52:55], v[192:195], v[96:99]\n"
"s_waitcnt lgkmcnt(0)\n"
"s_barrier\n"
"v_mfma_f32_16x16x32_bf16 v[96:99], a[48:51], v[120:123], v[96:99]\n"
"s_add_u32 s97, s61, 0xc40000\n"
"ds_read_b128 v[128:131], v4\n"
"ds_read_b128 v[140:143], v9 offset:640\n"
"v_mfma_f32_16x16x32_bf16 v[96:99], a[44:47], v[144:147], v[96:99]\n"
"ds_read_b128 v[148:151], v5\n"
"v_add_u32_e32 v252, 0x26c00, v0\n"
"v_mfma_f32_16x16x32_bf16 v[96:99], a[40:43], v[136:139], v[96:99]\n"
"ds_write_b128 v3, v[88:91] offset:12288\n"
"v_mfma_f32_16x16x32_bf16 v[96:99], a[36:39], v[108:111], v[96:99]\n"
"buffer_store_dwordx4 v[172:175], v0, s[8:11], s97 offen sc1\n"
"v_mfma_f32_16x16x32_bf16 v[96:99], a[32:35], v[92:95], v[96:99]\n"
"ds_read_b128 v[88:91], v252\n"
"ds_read_b128 v[176:179], v252 offset:1024\n"
"v_mfma_f32_16x16x32_bf16 v[180:183], a[64:67], v[92:95], 0\n"
"v_mov_b32_e32 v252, 0xff800000\n"
"v_mfma_f32_16x16x32_bf16 v[180:183], a[68:71], v[108:111], v[180:183]\n"
"s_waitcnt lgkmcnt(3)\n"
"v_fmac_f32_e32 v128, v254, v140\n"
"v_fmac_f32_e32 v129, v254, v141\n"
"v_mfma_f32_16x16x32_bf16 v[180:183], a[72:75], v[136:139], v[180:183]\n"
"ds_read_b128 v[184:187], v4 offset:1024\n"
"ds_read_b128 v[196:199], v9 offset:704\n"
"v_mfma_f32_16x16x32_bf16 v[180:183], a[76:79], v[144:147], v[180:183]\n"
"ds_read_b128 v[200:203], v5 offset:1024\n"
"v_fmac_f32_e32 v130, v254, v142\n"
"v_mfma_f32_16x16x32_bf16 v[180:183], a[80:83], v[120:123], v[180:183]\n"
"v_fmac_f32_e32 v131, v254, v143\n"
"v_add_f32_e32 v128, v128, v148\n"
"v_mfma_f32_16x16x32_bf16 v[180:183], a[84:87], v[192:195], v[180:183]\n"
"ds_write_b128 v3, v[96:99] offset:13312\n"
"v_mfma_f32_16x16x32_bf16 v[180:183], a[88:91], v[100:103], v[180:183]\n"
"s_waitcnt lgkmcnt(4)\n"
"v_add_f32_e32 v129, v129, v149\n"
"v_add_f32_e32 v130, v130, v150\n"
"v_mfma_f32_16x16x32_bf16 v[180:183], a[92:95], v[188:191], v[180:183]\n"
"v_add_f32_e32 v131, v131, v151\n"
"v_cndmask_b32_e64 v88, v88, v252, s[82:83]\n"
"v_mfma_f32_16x16x32_bf16 v[96:99], a[124:127], v[188:191], 0\n"
"s_waitcnt vmcnt(12)\n"
"v_cmp_ne_u64_e32 vcc, v[246:247], v[18:19]\n"
"v_mfma_f32_16x16x32_bf16 v[96:99], a[120:123], v[100:103], v[96:99]\n"
"s_cbranch_vccnz .Lmk_f40s\n"
".Lmk_f40r:\n"
"s_mov_b32 m0, 0x7c00\n"
"v_mfma_f32_16x16x32_bf16 v[96:99], a[116:119], v[192:195], v[96:99]\n"
"s_add_u32 s97, s66, 0x3c00\n"
"buffer_load_dwordx4 v0, s[4:7], s97 offen sc1 lds\n"
"v_mfma_f32_16x16x32_bf16 v[96:99], a[112:115], v[120:123], v[96:99]\n"
"s_mov_b32 m0, 0xfc00\n"
"s_add_u32 s97, s66, 0x7c00\n"
"ds_read_b128 v[140:143], v4 offset:2048\n"
"v_mfma_f32_16x16x32_bf16 v[96:99], a[108:111], v[144:147], v[96:99]\n"
"buffer_load_dwordx4 v0, s[4:7], s97 offen sc1 lds\n"
"v_mfma_f32_16x16x32_bf16 v[96:99], a[104:107], v[136:139], v[96:99]\n"
"ds_write_b128 v3, v[180:183] offset:14336\n"
"v_mfma_f32_16x16x32_bf16 v[96:99], a[100:103], v[108:111], v[96:99]\n"
"ds_read_b128 v[148:151], v9 offset:768\n"
"ds_read_b128 v[180:183], v5 offset:2048\n"
"v_mfma_f32_16x16x32_bf16 v[96:99], a[96:99], v[92:95], v[96:99]\n"
"s_waitcnt lgkmcnt(5)\n"
"v_fmac_f32_e32 v184, v254, v196\n"
"v_fmac_f32_e32 v185, v254, v197\n"
"v_mfma_f32_16x16x32_bf16 v[204:207], v[24:27], v[92:95], 0\n"
"ds_read_b128 v[208:211], v4 offset:3072\n"
"ds_read_b128 v[212:215], v9 offset:832\n"
"v_mfma_f32_16x16x32_bf16 v[204:207], v[28:31], v[108:111], v[204:207]\n"
"ds_read_b128 v[216:219], v5 offset:3072\n"
"v_fmac_f32_e32 v186, v254, v198\n"
"v_mfma_f32_16x16x32_bf16 v[204:207], v[32:35], v[136:139], v[204:207]\n"
"v_fmac_f32_e32 v187, v254, v199\n"
"v_add_f32_e32 v184, v184, v200\n"
"v_mfma_f32_16x16x32_bf16 v[204:207], v[36:39], v[144:147], v[204:207]\n"
"v_add_f32_e32 v185, v185, v201\n"
"v_add_f32_e32 v186, v186, v202\n"
"v_mfma_f32_16x16x32_bf16 v[204:207], v[40:43], v[120:123], v[204:207]\n"
"s_waitcnt lgkmcnt(3)\n"
"v_add_f32_e32 v187, v187, v203\n"
"v_fmac_f32_e32 v140, v254, v148\n"
"v_mfma_f32_16x16x32_bf16 v[204:207], v[44:47], v[192:195], v[204:207]\n"
"ds_write_b128 v3, v[96:99] offset:15360\n"
"v_mfma_f32_16x16x32_bf16 v[204:207], v[48:51], v[100:103], v[204:207]\n"
"v_fmac_f32_e32 v141, v254, v149\n"
"v_fmac_f32_e32 v142, v254, v150\n"
"v_mfma_f32_16x16x32_bf16 v[204:207], v[52:55], v[188:191], v[204:207]\n"
"ds_read_b128 v[96:99], v4 offset:4096\n"
"ds_read_b128 v[196:199], v9 offset:512\n"
"v_mfma_f32_16x16x32_bf16 v[200:203], v[84:87], v[188:191], 0\n"
"ds_read_b128 v[188:191], v5 offset:4096\n"
"v_fmac_f32_e32 v143, v254, v151\n"
"v_mfma_f32_16x16x32_bf16 v[200:203], v[80:83], v[100:103], v[200:203]\n"
"v_add_f32_e32 v140, v140, v180\n"
"v_add_f32_e32 v141, v141, v181\n"
"v_mfma_f32_16x16x32_bf16 v[200:203], v[76:79], v[192:195], v[200:203]\n"
"s_waitcnt lgkmcnt(4)\n"
"v_add_f32_e32 v142, v142, v182\n"
"v_add_f32_e32 v143, v143, v183\n"
"v_mfma_f32_16x16x32_bf16 v[200:203], v[72:75], v[120:123], v[200:203]\n"
"v_fmac_f32_e32 v208, v254, v212\n"
"v_fmac_f32_e32 v209, v254, v213\n"
"v_mfma_f32_16x16x32_bf16 v[200:203], v[68:71], v[144:147], v[200:203]\n"
"v_fmac_f32_e32 v210, v254, v214\n"
"v_fmac_f32_e32 v211, v254, v215\n"
"v_mfma_f32_16x16x32_bf16 v[200:203], v[64:67], v[136:139], v[200:203]\n"
"ds_write_b128 v3, v[204:207] offset:16384\n"
"v_mfma_f32_16x16x32_bf16 v[200:203], v[60:63], v[108:111], v[200:203]\n"
"ds_read_b128 v[100:103], v4 offset:5120\n"
"ds_read_b128 v[108:111], v9 offset:576\n"
"v_mfma_f32_16x16x32_bf16 v[200:203], v[56:59], v[92:95], v[200:203]\n"
"s_waitcnt vmcnt(12)\n"
"s_waitcnt vmcnt(3)\n"
"v_cmp_ne_u64_e32 vcc, v[250:251], v[18:19]\n"
"; @B G1 3.3\n"
"v_mfma_f32_16x16x32_bf16 v[128:131], a[0:3], v[116:119], v[128:131]\n"
"s_cbranch_vccnz .Lmk_f41s\n"
".Lmk_f41r:\n"
"ds_read_b128 v[92:95], v5 offset:5120\n"
"v_mfma_f32_16x16x32_bf16 v[128:131], a[4:7], v[132:135], v[128:131]\n"
"s_add_u32 s98, s61, 0xc40000\n"
"s_add_u32 s97, s56, 0x440000\n"
"v_add_f32_e32 v208, v208, v216\n"
"v_mfma_f32_16x16x32_bf16 v[128:131], a[8:11], v[160:163], v[128:131]\n"
"buffer_load_dwordx4 v[120:123], v0, s[4:7], s97 offen sc1\n"
"v_mfma_f32_16x16x32_bf16 v[128:131], a[12:15], v[156:159], v[128:131]\n"
"buffer_store_dwordx4 v[172:175], v0, s[8:11], s98 offen offset:1024 sc1\n"
"v_mfma_f32_16x16x32_bf16 v[128:131], a[16:19], v[152:155], v[128:131]\n"
"s_add_u32 s98, s69, 0\n"
"v_add_f32_e32 v209, v209, v217\n"
"v_add_f32_e32 v210, v210, v218\n"
"v_mfma_f32_16x16x32_bf16 v[128:131], a[20:23], v[112:115], v[128:131]\n"
"ds_write_b128 v3, v[200:203] offset:17408\n"
"v_mfma_f32_16x16x32_bf16 v[128:131], a[24:27], v[164:167], v[128:131]\n"
"buffer_load_dwordx4 v[136:139], v0, s[4:7], s97 offen offset:1024 sc1\n"
"v_mfma_f32_16x16x32_bf16 v[128:131], a[28:31], v[168:171], v[128:131]\n"
"s_waitcnt lgkmcnt(5)\n"
"v_add_f32_e32 v211, v211, v219\n"
"v_fmac_f32_e32 v96, v254, v196\n"
"v_mfma_f32_16x16x32_bf16 v[184:187], a[60:63], v[168:171], v[184:187]\n"
"v_fmac_f32_e32 v97, v254, v197\n"
"v_fmac_f32_e32 v98, v254, v198\n"
"v_mfma_f32_16x16x32_bf16 v[184:187], a[56:59], v[164:167], v[184:187]\n"
"buffer_load_dwordx4 v[144:147], v0, s[4:7], s97 offen offset:2048 sc1\n"
"v_mfma_f32_16x16x32_bf16 v[184:187], a[52:55], v[112:115], v[184:187]\n"
"s_waitcnt lgkmcnt(0)\n"
"s_barrier\n"
"v_mfma_f32_16x16x32_bf16 v[184:187], a[48:51], v[152:155], v[184:187]\n"
"ds_read_b128 v[148:151], v6\n"
"ds_read_b128 v[180:183], v6 offset:1024\n"
"v_mfma_f32_16x16x32_bf16 v[184:187], a[44:47], v[156:159], v[184:187]\n"
"buffer_load_dwordx4 v[192:195], v0, s[4:7], s97 offen offset:3072 sc1\n"
"v_mfma_f32_16x16x32_bf16 v[184:187], a[40:43], v[160:163], v[184:187]\n"
"v_fmac_f32_e32 v99, v254, v199\n"
"v_add_f32_e32 v96, v96, v188\n"
"v_mfma_f32_16x16x32_bf16 v[184:187], a[36:39], v[132:135], v[184:187]\n"
"v_add_f32_e32 v97, v97, v189\n"
"v_add_f32_e32 v98, v98, v190\n"
"v_mfma_f32_16x16x32_bf16 v[184:187], a[32:35], v[116:119], v[184:187]\n"
"buffer_load_dwordx4 v[196:199], v1, s[4:7], s97 offen sc1\n"
"v_mfma_f32_16x16x32_bf16 v[140:143], a[64:67], v[116:119], v[140:143]\n"
"v_add_f32_e32 v99, v99, v191\n"
"v_fmac_f32_e32 v100, v254, v108\n"
"v_mfma_f32_16x16x32_bf16 v[140:143], a[68:71], v[132:135], v[140:143]\n"
"v_fmac_f32_e32 v101, v254, v109\n"
"v_fmac_f32_e32 v102, v254, v110\n"
"v_mfma_f32_16x16x32_bf16 v[140:143], a[72:75], v[160:163], v[140:143]\n"
"buffer_load_dwordx4 v[188:191], v1, s[4:7], s97 offen offset:1024 sc1\n"
"v_mfma_f32_16x16x32_bf16 v[140:143], a[76:79], v[156:159], v[140:143]\n"
"v_fmac_f32_e32 v103, v254, v111\n"
"v_add_f32_e32 v100, v100, v92\n"
"v_mfma_f32_16x16x32_bf16 v[140:143], a[80:83], v[152:155], v[140:143]\n"
"s_waitcnt lgkmcnt(0)\n"
"v_add_f32_e32 v101, v101, v93\n"
"v_add_f32_e32 v102, v102, v94\n"
"v_mfma_f32_16x16x32_bf16 v[140:143], a[84:87], v[112:115], v[140:143]\n"
"buffer_load_dwordx4 v[108:111], v1, s[4:7], s97 offen offset:2048 sc1\n"
"v_mfma_f32_16x16x32_bf16 v[140:143], a[88:91], v[164:167], v[140:143]\n"
"ds_read_b128 v[200:203], v6 offset:2048\n"
"ds_read_b128 v[204:207], v6 offset:3072\n"
"v_mfma_f32_16x16x32_bf16 v[140:143], a[92:95], v[168:171], v[140:143]\n"
"v_add_f32_e32 v103, v103, v95\n"
"v_add_f32_e32 v128, v128, v148\n"
"v_mfma_f32_16x16x32_bf16 v[208:211], a[124:127], v[168:171], v[208:211]\n"
"buffer_load_dwordx4 v[92:95], v1, s[4:7], s97 offen offset:3072 sc1\n"
"v_mfma_f32_16x16x32_bf16 v[208:211], a[120:123], v[164:167], v[208:211]\n"
"s_add_u32 s97, s57, 0x440000\n"
"v_add_f32_e32 v129, v129, v149\n"
"v_add_f32_e32 v130, v130, v150\n"
"v_mfma_f32_16x16x32_bf16 v[208:211], a[116:119], v[112:115], v[208:211]\n"
"v_add_f32_e32 v131, v131, v151\n"
"v_add_f32_e32 v184, v184, v180\n"
"v_mfma_f32_16x16x32_bf16 v[208:211], a[112:115], v[152:155], v[208:211]\n"
"buffer_load_dwordx4 v[148:151], v0, s[4:7], s97 offen sc1\n"
"v_mfma_f32_16x16x32_bf16 v[208:211], a[108:111], v[156:159], v[208:211]\n"
"v_add_f32_e32 v185, v185, v181\n"
"v_add_f32_e32 v186, v186, v182\n"
"v_mfma_f32_16x16x32_bf16 v[208:211], a[104:107], v[160:163], v[208:211]\n"
"v_add_f32_e32 v187, v187, v183\n"
"v_mul_f32_e32 v128, v128, v255\n"
"v_mfma_f32_16x16x32_bf16 v[208:211], a[100:103], v[132:135], v[208:211]\n"
"buffer_load_dwordx4 v[180:183], v0, s[4:7], s97 offen offset:1024 sc1\n"
"v_mfma_f32_16x16x32_bf16 v[208:211], a[96:99], v[116:119], v[208:211]\n"
"v_mul_f32_e32 v129, v129, v255\n"
"v_mul_f32_e32 v130, v130, v255\n"
"v_mfma_f32_16x16x32_bf16 v[96:99], v[24:27], v[116:119], v[96:99]\n"
"v_mul_f32_e32 v131, v131, v255\n"
"v_mul_f32_e32 v184, v184, v255\n"
"v_mfma_f32_16x16x32_bf16 v[96:99], v[28:31], v[132:135], v[96:99]\n"
"buffer_load_dwordx4 v[24:27], v0, s[4:7], s97 offen offset:2048 sc1\n"
"v_mfma_f32_16x16x32_bf16 v[96:99], v[32:35], v[160:163], v[96:99]\n"
"v_mul_f32_e32 v185, v185, v255\n"
"v_mul_f32_e32 v186, v186, v255\n"
"v_mfma_f32_16x16x32_bf16 v[96:99], v[36:39], v[156:159], v[96:99]\n"
"v_mul_f32_e32 v187, v187, v255\n"
"v_cvt_pk_bf16_f32 v28, v128, v129\n"
"v_mfma_f32_16x16x32_bf16 v[96:99], v[40:43], v[152:155], v[96:99]\n"
"buffer_load_dwordx4 v[32:35], v0, s[4:7], s97 offen offset:3072 sc1\n"
"v_mfma_f32_16x16x32_bf16 v[96:99], v[44:47], v[112:115], v[96:99]\n"
"s_waitcnt lgkmcnt(0)\n"
"v_cvt_pk_bf16_f32 v29, v130, v131\n"
"v_cvt_pk_bf16_f32 v30, v184, v185\n"
"v_mfma_f32_16x16x32_bf16 v[96:99], v[48:51], v[164:167], v[96:99]\n"
"s_add_u32 s96, s63, 0xc4000\n"
"ds_read_b128 v[36:39], v6 offset:4096\n"
"ds_read_b128 v[40:43], v6 offset:5120\n"
"v_mfma_f32_16x16x32_bf16 v[96:99], v[52:55], v[168:171], v[96:99]\n"
"buffer_load_dwordx4 v[44:47], v1, s[4:7], s97 offen sc1\n"
"v_mfma_f32_16x16x32_bf16 v[100:103], v[84:87], v[168:171], v[100:103]\n"
"buffer_store_dword v172, v14, s[8:11], s96 offen sc1\n"
"v_mfma_f32_16x16x32_bf16 v[100:103], v[80:83], v[164:167], v[100:103]\n"
"v_cvt_pk_bf16_f32 v31, v186, v187\n"
"v_add_f32_e32 v140, v140, v200\n"
"v_mfma_f32_16x16x32_bf16 v[100:103], v[76:79], v[112:115], v[100:103]\n"
"buffer_load_dwordx4 v[48:51], v1, s[4:7], s97 offen offset:1024 sc1\n"
"ds_write_b128 v7, v[28:31] offset:16384\n"
"v_mfma_f32_16x16x32_bf16 v[100:103], v[72:75], v[152:155], v[100:103]\n"
"v_add_f32_e32 v141, v141, v201\n"
"v_add_f32_e32 v142, v142, v202\n"
"v_mfma_f32_16x16x32_bf16 v[100:103], v[68:71], v[156:159], v[100:103]\n"
"v_add_f32_e32 v143, v143, v203\n"
"v_add_f32_e32 v208, v208, v204\n"
"v_mfma_f32_16x16x32_bf16 v[100:103], v[64:67], v[160:163], v[100:103]\n"
"buffer_load_dwordx4 v[28:31], v1, s[4:7], s97 offen offset:2048 sc1\n"
"v_mfma_f32_16x16x32_bf16 v[100:103], v[60:63], v[132:135], v[100:103]\n"
"v_add_f32_e32 v209, v209, v205\n"
"v_add_f32_e32 v210, v210, v206\n"
"v_mfma_f32_16x16x32_bf16 v[100:103], v[56:59], v[116:119], v[100:103]\n"
"buffer_load_dwordx4 a[0:3], v0, s[8:11], s98 offen sc1\n"
"buffer_load_dwordx4 v[56:59], v1, s[4:7], s97 offen offset:3072 sc1\n"
"v_add_f32_e32 v211, v211, v207\n"
"v_mul_f32_e32 v140, v140, v255\n"
"v_mul_f32_e32 v141, v141, v255\n"
"v_mul_f32_e32 v142, v142, v255\n"
"v_mul_f32_e32 v143, v143, v255\n"
"v_mul_f32_e32 v208, v208, v255\n"
"v_mul_f32_e32 v209, v209, v255\n"
"v_mul_f32_e32 v210, v210, v255\n"
"v_mul_f32_e32 v211, v211, v255\n"
"v_cvt_pk_bf16_f32 v52, v140, v141\n"
"v_cvt_pk_bf16_f32 v53, v142, v143\n"
"v_cvt_pk_bf16_f32 v54, v208, v209\n"
"v_cvt_pk_bf16_f32 v55, v210, v211\n"
"v_cndmask_b32_e64 v89, v89, v252, s[82:83]\n"
"v_cndmask_b32_e64 v90, v90, v252, s[82:83]\n"
"v_cndmask_b32_e64 v91, v91, v252, s[82:83]\n"
"v_cndmask_b32_e64 v176, v176, v252, s[82:83]\n"
"v_cndmask_b32_e64 v177, v177, v252, s[82:83]\n"
"v_cndmask_b32_e64 v178, v178, v252, s[82:83]\n"
"v_cndmask_b32_e64 v179, v179, v252, s[82:83]\n"
"ds_write_b128 v7, v[52:55] offset:49152\n"
"buffer_load_dwordx4 a[4:7], v0, s[8:11], s98 offen offset:1024 sc1\n"
"buffer_load_dwordx4 a[8:11], v0, s[8:11], s98 offen offset:2048 sc1\n"
"buffer_load_dwordx4 a[12:15], v0, s[8:11], s98 offen offset:3072 sc1\n"
"buffer_load_dwordx4 a[16:19], v1, s[8:11], s98 offen sc1\n"
"buffer_load_dwordx4 a[20:23], v1, s[8:11], s98 offen offset:1024 sc1\n"
".Lmk_b4:\n"
"; @B tail\n"
";\n"
"s_waitcnt lgkmcnt(2)\n"
"v_add_f32_e32 v96, v96, v36\n"
"v_add_f32_e32 v97, v97, v37\n"
"v_add_f32_e32 v98, v98, v38\n"
"v_add_f32_e32 v99, v99, v39\n"
"v_add_f32_e32 v100, v100, v40\n"
"v_add_f32_e32 v101, v101, v41\n"
"v_add_f32_e32 v102, v102, v42\n"
"v_add_f32_e32 v103, v103, v43\n"
"v_mul_f32_e32 v96, v96, v253\n"
"v_mul_f32_e32 v97, v97, v253\n"
"v_mul_f32_e32 v98, v98, v253\n"
"v_mul_f32_e32 v99, v99, v253\n"
"v_mul_f32_e32 v100, v100, v253\n"
"v_mul_f32_e32 v101, v101, v253\n"
"v_mul_f32_e32 v102, v102, v253\n"
"v_mul_f32_e32 v103, v103, v253\n"
"v_cvt_pk_bf16_f32 v36, v96, v97\n"
"v_cvt_pk_bf16_f32 v37, v98, v99\n"
"v_cvt_pk_bf16_f32 v38, v100, v101\n"
"v_cvt_pk_bf16_f32 v39, v102, v103\n"
"ds_write_b128 v2, v[36:39] offset:16384\n"
"s_waitcnt vmcnt(24)\n"
";\n"
"; @B AT a\n"
";\n"
"s_cmp_lg_u32 s0, 0\n"
"s_cbranch_scc1 .Lmk_exit\n"
"ds_read_b128 v[36:39], v2 offset:17408\n"
"ds_read_b128 v[40:43], v15 offset:16384\n"
"v_xor_b32_e32 v255, 32, v15\n"
"ds_read_b128 v[52:55], v255 offset:16640\n"
"ds_read_b128 v[60:63], v2 offset:16384\n"
"ds_read_b128 v[64:67], v17 offset:15360\n"
"v_xor_b32_e32 v255, 32, v17\n"
"ds_read_b128 v[68:71], v255 offset:15616\n"
"ds_read_b64_tr_b16 v[72:73], v16 offset:16384\n"
"v_xor_b32_e32 v255, 32, v16\n"
"ds_read_b64_tr_b16 v[74:75], v255 offset:16640\n"
"ds_read_b64_tr_b16 v[76:77], v255 offset:16384\n"
"ds_read_b64_tr_b16 v[78:79], v16 offset:16640\n"
"ds_read_b64_tr_b16 v[84:85], v16 offset:15360\n"
"v_mov_b32_e32 v80, 0x3f803f80\n"
"v_mov_b32_e32 v81, 0x3f803f80\n"
"v_mov_b32_e32 v82, 0x3f803f80\n"
"v_mov_b32_e32 v83, 0x3f803f80\n"
"v_xor_b32_e32 v255, 32, v16\n"
"ds_read_b64_tr_b16 v[86:87], v255 offset:15616\n"
"s_waitcnt lgkmcnt(13)\n"
"ds_read_b64_tr_b16 v[96:97], v255 offset:15360\n"
"ds_read_b64_tr_b16 v[98:99], v16 offset:15616\n"
"v_mov_b32_e32 v100, 0x3f803f80\n"
"v_mov_b32_e32 v101, 0x3f803f80\n"
"v_mov_b32_e32 v102, 0x3f803f80\n"
"v_mov_b32_e32 v103, 0x3f803f80\n"
"s_waitcnt lgkmcnt(11)\n"
"v_mfma_f32_16x16x32_bf16 v[104:107], v[40:43], v[36:39], v[104:107]\n"
"v_mfma_f32_16x16x32_bf16 v[124:127], v[52:55], v[36:39], v[124:127]\n"
"s_waitcnt lgkmcnt(8)\n"
"v_mfma_f32_16x16x32_bf16 v[88:91], v[64:67], v[60:63], v[88:91]\n"
"v_mfma_f32_16x16x32_bf16 v[176:179], v[68:71], v[60:63], v[176:179]\n"
"s_waitcnt lgkmcnt(4)\n"
"s_nop 2\n"
"v_max_f32_e32 v255, v104, v105\n"
"s_nop 1\n"
"v_max_f32_e32 v254, v88, v89\n"
"v_max3_f32 v255, v255, v106, v107\n"
"v_max3_f32 v254, v254, v90, v91\n"
"v_max3_f32 v255, v255, v124, v125\n"
"v_max3_f32 v254, v254, v176, v177\n"
"v_max3_f32 v255, v255, v126, v127\n"
"v_max3_f32 v254, v254, v178, v179\n"
"v_mov_b32_e32 v253, v255\n"
"v_mov_b32_e32 v252, v254\n"
"s_nop 0\n"
"v_permlane16_swap_b32_e32 v255, v253\n"
"v_permlane16_swap_b32_e32 v254, v252\n"
"v_max_f32_e32 v255, v255, v253\n"
"v_max_f32_e32 v254, v254, v252\n"
"v_mov_b32_e32 v253, v255\n"
"v_mov_b32_e32 v252, v254\n"
"s_nop 0\n"
"v_permlane32_swap_b32_e32 v255, v253\n"
"v_permlane32_swap_b32_e32 v254, v252\n"
"v_max_f32_e32 v255, v255, v253\n"
"v_max_f32_e32 v254, v254, v252\n"
"v_sub_f32_e32 v104, v104, v255\n"
"v_sub_f32_e32 v105, v105, v255\n"
"v_sub_f32_e32 v106, v106, v255\n"
"v_sub_f32_e32 v107, v107, v255\n"
"v_sub_f32_e32 v124, v124, v255\n"
"v_sub_f32_e32 v125, v125, v255\n"
"v_sub_f32_e32 v126, v126, v255\n"
"v_sub_f32_e32 v127, v127, v255\n"
"v_exp_f32_e32 v104, v104\n"
"v_exp_f32_e32 v105, v105\n"
"v_exp_f32_e32 v106, v106\n"
"v_exp_f32_e32 v107, v107\n"
"v_exp_f32_e32 v124, v124\n"
"v_exp_f32_e32 v125, v125\n"
"v_exp_f32_e32 v126, v126\n"
"v_exp_f32_e32 v127, v127\n"
"v_cvt_pk_bf16_f32 v36, v104, v105\n"
"v_cvt_pk_bf16_f32 v37, v106, v107\n"
"v_cvt_pk_bf16_f32 v38, v124, v125\n"
"v_cvt_pk_bf16_f32 v39, v126, v127\n"
"v_sub_f32_e32 v88, v88, v254\n"
"v_sub_f32_e32 v89, v89, v254\n"
"v_sub_f32_e32 v90, v90, v254\n"
"v_sub_f32_e32 v91, v91, v254\n"
"v_sub_f32_e32 v176, v176, v254\n"
"v_sub_f32_e32 v177, v177, v254\n"
"v_sub_f32_e32 v178, v178, v254\n"
"v_mfma_f32_16x16x32_bf16 v[40:43], v[80:83], v[36:39], 0\n"
"v_sub_f32_e32 v179, v179, v254\n"
"v_exp_f32_e32 v88, v88\n"
"v_exp_f32_e32 v89, v89\n"
"v_exp_f32_e32 v90, v90\n"
"v_mfma_f32_16x16x32_bf16 v[52:55], v[72:75], v[36:39], 0\n"
"v_exp_f32_e32 v91, v91\n"
"v_exp_f32_e32 v176, v176\n"
"v_exp_f32_e32 v177, v177\n"
"v_exp_f32_e32 v178, v178\n"
"v_mfma_f32_16x16x32_bf16 v[60:63], v[76:79], v[36:39], 0\n"
"s_waitcnt lgkmcnt(0)\n"
"v_exp_f32_e32 v179, v179\n"
"v_cvt_pk_bf16_f32 v36, v88, v89\n"
"v_cvt_pk_bf16_f32 v37, v90, v91\n"
"v_cvt_pk_bf16_f32 v38, v176, v177\n"
"v_cvt_pk_bf16_f32 v39, v178, v179\n"
"v_rcp_f32_e32 v40, v40\n"
"s_nop 0\n"
"v_mul_f32_e32 v52, v52, v40\n"
"v_mul_f32_e32 v53, v53, v40\n"
"v_mul_f32_e32 v54, v54, v40\n"
"v_mfma_f32_16x16x32_bf16 v[64:67], v[100:103], v[36:39], 0\n"
"v_mul_f32_e32 v55, v55, v40\n"
"v_mul_f32_e32 v60, v60, v40\n"
"v_mul_f32_e32 v61, v61, v40\n"
"v_mul_f32_e32 v62, v62, v40\n"
"v_mul_f32_e32 v63, v63, v40\n"
"v_mfma_f32_16x16x32_bf16 v[40:43], v[84:87], v[36:39], 0\n"
"s_add_u32 s97, s60, 0x408000\n"
"v_cvt_pk_bf16_f32 v68, v52, v53\n"
"v_cvt_pk_bf16_f32 v69, v54, v55\n"
"v_cvt_pk_bf16_f32 v70, v60, v61\n"
"v_cvt_pk_bf16_f32 v71, v62, v63\n"
"v_mfma_f32_16x16x32_bf16 v[52:55], v[96:99], v[36:39], 0\n"
"buffer_store_dwordx4 v[68:71], v0, s[4:7], s97 offen sc1\n"
"s_add_u32 s97, s60, 0x400000\n"
"v_rcp_f32_e32 v64, v64\n"
"s_nop 0\n"
"v_mul_f32_e32 v40, v40, v64\n"
"v_mul_f32_e32 v41, v41, v64\n"
"v_mul_f32_e32 v42, v42, v64\n"
"v_mul_f32_e32 v43, v43, v64\n"
"v_mul_f32_e32 v52, v52, v64\n"
"v_mul_f32_e32 v53, v53, v64\n"
"v_mul_f32_e32 v54, v54, v64\n"
"v_mul_f32_e32 v55, v55, v64\n"
"v_cvt_pk_bf16_f32 v36, v40, v41\n"
"v_cvt_pk_bf16_f32 v37, v42, v43\n"
"v_cvt_pk_bf16_f32 v38, v52, v53\n"
"v_cvt_pk_bf16_f32 v39, v54, v55\n"
"buffer_store_dwordx4 v[36:39], v0, s[4:7], s97 offen sc1\n"
";\n"
"s_add_u32 s97, s58, 0x440000\n"
"buffer_load_dwordx4 v[36:39], v0, s[4:7], s97 offen sc1\n"
"buffer_load_dwordx4 v[40:43], v0, s[4:7], s97 offen offset:1024 sc1\n"
"buffer_load_dwordx4 v[52:55], v0, s[4:7], s97 offen offset:2048 sc1\n"
"buffer_load_dwordx4 v[60:63], v0, s[4:7], s97 offen offset:3072 sc1\n"
"buffer_load_dwordx4 v[64:67], v1, s[4:7], s97 offen sc1\n"
"buffer_load_dwordx4 v[68:71], v1, s[4:7], s97 offen offset:1024 sc1\n"
"buffer_load_dwordx4 v[72:75], v1, s[4:7], s97 offen offset:2048 sc1\n"
"buffer_load_dwordx4 v[76:79], v1, s[4:7], s97 offen offset:3072 sc1\n"
"buffer_load_dwordx4 a[24:27], v1, s[8:11], s98 offen offset:2048 sc1\n"
"buffer_load_dwordx4 a[28:31], v1, s[8:11], s98 offen offset:3072 sc1\n"
"s_add_u32 s98, s69, 0x2000\n"
"buffer_load_dwordx4 a[32:35], v0, s[8:11], s98 offen sc1\n"
"buffer_load_dwordx4 a[36:39], v0, s[8:11], s98 offen offset:1024 sc1\n"
"buffer_load_dwordx4 a[40:43], v0, s[8:11], s98 offen offset:2048 sc1\n"
"buffer_load_dwordx4 a[44:47], v0, s[8:11], s98 offen offset:3072 sc1\n"
"buffer_load_dwordx4 a[48:51], v1, s[8:11], s98 offen sc1\n"
"buffer_load_dwordx4 a[52:55], v1, s[8:11], s98 offen offset:1024 sc1\n"
"s_waitcnt vmcnt(39)\n"
"; @B G2 b.0\n"
"v_mfma_f32_16x16x32_bf16 v[80:83], a[128:131], v[120:123], 0\n"
"v_mfma_f32_16x16x32_bf16 v[80:83], a[132:135], v[136:139], v[80:83]\n"
"s_waitcnt vmcnt(37)\n"
"v_mfma_f32_16x16x32_bf16 v[80:83], a[136:139], v[144:147], v[80:83]\n"
"s_add_u32 s97, s59, 0x440000\n"
"buffer_load_dwordx4 v[84:87], v0, s[4:7], s97 offen sc1\n"
"v_mfma_f32_16x16x32_bf16 v[80:83], a[140:143], v[192:195], v[80:83]\n"
"s_waitcnt vmcnt(36)\n"
"v_mfma_f32_16x16x32_bf16 v[80:83], a[144:147], v[196:199], v[80:83]\n"
"v_mfma_f32_16x16x32_bf16 v[80:83], a[148:151], v[188:191], v[80:83]\n"
"s_waitcnt vmcnt(34)\n"
"buffer_load_dwordx4 v[88:91], v0, s[4:7], s97 offen offset:1024 sc1\n"
"v_mfma_f32_16x16x32_bf16 v[80:83], a[152:155], v[108:111], v[80:83]\n"
"v_mfma_f32_16x16x32_bf16 v[80:83], a[156:159], v[92:95], v[80:83]\n"
"v_mfma_f32_16x16x32_bf16 v[96:99], a[188:191], v[92:95], 0\n"
"buffer_load_dwordx4 v[100:103], v0, s[4:7], s97 offen offset:2048 sc1\n"
"v_mfma_f32_16x16x32_bf16 v[96:99], a[184:187], v[108:111], v[96:99]\n"
"v_mfma_f32_16x16x32_bf16 v[96:99], a[180:183], v[188:191], v[96:99]\n"
"v_mfma_f32_16x16x32_bf16 v[96:99], a[176:179], v[196:199], v[96:99]\n"
"buffer_load_dwordx4 v[104:107], v0, s[4:7], s97 offen offset:3072 sc1\n"
"v_mfma_f32_16x16x32_bf16 v[96:99], a[172:175], v[192:195], v[96:99]\n"
"s_nop 0\n"
"v_cmp_u_f32_e32 vcc, v80, v80\n"
"v_mfma_f32_16x16x32_bf16 v[96:99], a[168:171], v[144:147], v[96:99]\n"
"s_cbranch_vccnz .Lmk_p63s\n"
".Lmk_p63r:\n"
"v_mfma_f32_16x16x32_bf16 v[96:99], a[164:167], v[136:139], v[96:99]\n"
"ds_write_b128 v3, v[80:83]\n"
"v_mfma_f32_16x16x32_bf16 v[96:99], a[160:163], v[120:123], v[96:99]\n"
"buffer_load_dwordx4 v[80:83], v1, s[4:7], s97 offen sc1\n"
"v_mfma_f32_16x16x32_bf16 v[112:115], a[192:195], v[120:123], 0\n"
"v_mfma_f32_16x16x32_bf16 v[112:115], a[196:199], v[136:139], v[112:115]\n"
"v_mfma_f32_16x16x32_bf16 v[112:115], a[200:203], v[144:147], v[112:115]\n"
"buffer_load_dwordx4 v[116:119], v1, s[4:7], s97 offen offset:1024 sc1\n"
"v_mfma_f32_16x16x32_bf16 v[112:115], a[204:207], v[192:195], v[112:115]\n"
"v_mfma_f32_16x16x32_bf16 v[112:115], a[208:211], v[196:199], v[112:115]\n"
"v_mfma_f32_16x16x32_bf16 v[112:115], a[212:215], v[188:191], v[112:115]\n"
"ds_write_b128 v3, v[96:99] offset:1024\n"
"v_mfma_f32_16x16x32_bf16 v[112:115], a[216:219], v[108:111], v[112:115]\n"
"buffer_load_dwordx4 v[96:99], v1, s[4:7], s97 offen offset:2048 sc1\n"
"v_mfma_f32_16x16x32_bf16 v[112:115], a[220:223], v[92:95], v[112:115]\n"
"v_mfma_f32_16x16x32_bf16 v[124:127], a[252:255], v[92:95], 0\n"
"v_mfma_f32_16x16x32_bf16 v[124:127], a[248:251], v[108:111], v[124:127]\n"
"buffer_load_dwordx4 v[92:95], v1, s[4:7], s97 offen offset:3072 sc1\n"
"v_mfma_f32_16x16x32_bf16 v[124:127], a[244:247], v[188:191], v[124:127]\n"
"v_mfma_f32_16x16x32_bf16 v[124:127], a[240:243], v[196:199], v[124:127]\n"
"v_mfma_f32_16x16x32_bf16 v[124:127], a[236:239], v[192:195], v[124:127]\n"
"s_mov_b32 s97, .Lmk_a0-.Lmk_g67\n"
"s_getpc_b64 s[84:85]\n"
".Lmk_g67:\n"
"s_add_u32 s84, s84, s97\n"
"s_addc_u32 s85, s85, 0\n"
"s_and_b32 s84, s84, -16\n"
"v_mfma_f32_16x16x32_bf16 v[124:127], a[232:235], v[144:147], v[124:127]\n"
"ds_write_b128 v3, v[112:115] offset:2048\n"
"v_mfma_f32_16x16x32_bf16 v[124:127], a[228:231], v[136:139], v[124:127]\n"
"s_and_b32 s85, s85, 0xffff\n"
"s_sub_u32 s97, .Lmk_end-.Lmk_g67, s97\n"
"s_min_u32 s86, s97, 0x3000\n"
"s_mov_b32 s87, 0x20000\n"
"v_mfma_f32_16x16x32_bf16 v[124:127], a[224:227], v[120:123], v[124:127]\n"
"s_waitcnt vmcnt(37)\n"
"s_mov_b32 m0, 0x27800\n"
"s_nop 0\n"
"buffer_load_dwordx4 v0, s[84:87], s99 offen lds\n"
"; @B G2 b.1\n"
"v_mfma_f32_16x16x32_bf16 v[108:111], a[128:131], v[148:151], 0\n"
"v_mfma_f32_16x16x32_bf16 v[108:111], a[132:135], v[180:183], v[108:111]\n"
"v_mfma_f32_16x16x32_bf16 v[108:111], a[136:139], v[24:27], v[108:111]\n"
"v_mfma_f32_16x16x32_bf16 v[108:111], a[140:143], v[32:35], v[108:111]\n"
"s_waitcnt vmcnt(32)\n"
"v_mfma_f32_16x16x32_bf16 v[108:111], a[144:147], v[44:47], v[108:111]\n"
"v_mfma_f32_16x16x32_bf16 v[108:111], a[148:151], v[48:51], v[108:111]\n"
"ds_write_b128 v3, v[124:127] offset:3072\n"
"v_mfma_f32_16x16x32_bf16 v[108:111], a[152:155], v[28:31], v[108:111]\n"
"v_mfma_f32_16x16x32_bf16 v[108:111], a[156:159], v[56:59], v[108:111]\n"
"v_mfma_f32_16x16x32_bf16 v[112:115], a[188:191], v[56:59], 0\n"
"s_add_u32 s97, s56, 0x400000\n"
"buffer_load_dwordx4 v[120:123], v0, s[4:7], s97 offen sc1\n"
"v_mfma_f32_16x16x32_bf16 v[112:115], a[184:187], v[28:31], v[112:115]\n"
"v_mfma_f32_16x16x32_bf16 v[112:115], a[180:183], v[48:51], v[112:115]\n"
"v_mfma_f32_16x16x32_bf16 v[112:115], a[176:179], v[44:47], v[112:115]\n"
"buffer_load_dwordx4 v[124:127], v0, s[4:7], s97 offen offset:1024 sc1\n"
"v_mfma_f32_16x16x32_bf16 v[112:115], a[172:175], v[32:35], v[112:115]\n"
"v_cmp_u_f32_e32 vcc, v108, v108\n"
"v_mfma_f32_16x16x32_bf16 v[112:115], a[168:171], v[24:27], v[112:115]\n"
"s_cbranch_vccnz .Lmk_p64s\n"
".Lmk_p64r:\n"
"v_mfma_f32_16x16x32_bf16 v[112:115], a[164:167], v[180:183], v[112:115]\n"
"ds_write_b128 v3, v[108:111] offset:6144\n"
"v_mfma_f32_16x16x32_bf16 v[112:115], a[160:163], v[148:151], v[112:115]\n"
"buffer_load_dwordx4 v[108:111], v0, s[4:7], s97 offen offset:2048 sc1\n"
"v_mfma_f32_16x16x32_bf16 v[128:131], a[192:195], v[148:151], 0\n"
"v_mfma_f32_16x16x32_bf16 v[128:131], a[196:199], v[180:183], v[128:131]\n"
"v_mfma_f32_16x16x32_bf16 v[128:131], a[200:203], v[24:27], v[128:131]\n"
"buffer_load_dwordx4 v[132:135], v0, s[4:7], s97 offen offset:3072 sc1\n"
"v_mfma_f32_16x16x32_bf16 v[128:131], a[204:207], v[32:35], v[128:131]\n"
"v_mfma_f32_16x16x32_bf16 v[128:131], a[208:211], v[44:47], v[128:131]\n"
"ds_read_b128 v[136:139], v2 offset:8192\n"
"ds_read_b128 v[140:143], v2 offset:9216\n"
"v_mfma_f32_16x16x32_bf16 v[128:131], a[212:215], v[48:51], v[128:131]\n"
"ds_write_b128 v3, v[112:115] offset:7168\n"
"v_mfma_f32_16x16x32_bf16 v[128:131], a[216:219], v[28:31], v[128:131]\n"
"buffer_load_dwordx4 v[112:115], v1, s[4:7], s97 offen sc1\n"
"v_mfma_f32_16x16x32_bf16 v[128:131], a[220:223], v[56:59], v[128:131]\n"
"v_mfma_f32_16x16x32_bf16 v[144:147], a[252:255], v[56:59], 0\n"
"v_mfma_f32_16x16x32_bf16 v[144:147], a[248:251], v[28:31], v[144:147]\n"
"buffer_load_dwordx4 v[28:31], v1, s[4:7], s97 offen offset:1024 sc1\n"
"s_waitcnt lgkmcnt(1)\n"
"v_mfma_f32_16x16x32_bf16 v[144:147], a[244:247], v[48:51], v[144:147]\n"
"v_lshlrev_b32_e32 v255, 16, v136\n"
"v_and_b32_e32 v254, 0xffff0000, v136\n"
"v_lshlrev_b32_e32 v253, 16, v137\n"
"v_mfma_f32_16x16x32_bf16 v[144:147], a[240:243], v[44:47], v[144:147]\n"
"v_and_b32_e32 v252, 0xffff0000, v137\n"
"v_lshlrev_b32_e32 v251, 16, v138\n"
"v_and_b32_e32 v250, 0xffff0000, v138\n"
"v_mfma_f32_16x16x32_bf16 v[144:147], a[236:239], v[32:35], v[144:147]\n"
"buffer_load_dwordx4 v[32:35], v1, s[4:7], s97 offen offset:2048 sc1\n"
"v_lshlrev_b32_e32 v249, 16, v139\n"
"v_mfma_f32_16x16x32_bf16 v[144:147], a[232:235], v[24:27], v[144:147]\n"
"ds_write_b128 v3, v[128:131] offset:8192\n"
"v_mfma_f32_16x16x32_bf16 v[144:147], a[228:231], v[180:183], v[144:147]\n"
"v_and_b32_e32 v248, 0xffff0000, v139\n"
"v_lshlrev_b32_e32 v247, 16, v140\n"
"v_and_b32_e32 v246, 0xffff0000, v140\n"
"v_mfma_f32_16x16x32_bf16 v[144:147], a[224:227], v[148:151], v[144:147]\n"
"s_waitcnt vmcnt(28)\n"
"buffer_load_dwordx4 v[24:27], v1, s[4:7], s97 offen offset:3072 sc1\n"
"; @B G2 b.2\n"
"v_mfma_f32_16x16x32_bf16 v[44:47], a[128:131], v[36:39], 0\n"
"s_add_u32 s97, s57, 0x400000\n"
"v_lshlrev_b32_e32 v245, 16, v141\n"
"v_and_b32_e32 v244, 0xffff0000, v141\n"
"v_lshlrev_b32_e32 v243, 16, v142\n"
"v_mfma_f32_16x16x32_bf16 v[44:47], a[132:135], v[40:43], v[44:47]\n"
"v_and_b32_e32 v242, 0xffff0000, v142\n"
"v_lshlrev_b32_e32 v241, 16, v143\n"
"v_and_b32_e32 v240, 0xffff0000, v143\n"
"v_mfma_f32_16x16x32_bf16 v[44:47], a[136:139], v[52:55], v[44:47]\n"
"buffer_load_dwordx4 v[48:51], v0, s[4:7], s97 offen sc1\n"
"v_mfma_f32_16x16x32_bf16 v[44:47], a[140:143], v[60:63], v[44:47]\n"
"s_waitcnt vmcnt(26)\n"
"v_mfma_f32_16x16x32_bf16 v[44:47], a[144:147], v[64:67], v[44:47]\n"
"v_mfma_f32_16x16x32_bf16 v[44:47], a[148:151], v[68:71], v[44:47]\n"
"ds_write_b128 v3, v[144:147] offset:9216\n"
"v_mfma_f32_16x16x32_bf16 v[44:47], a[152:155], v[72:75], v[44:47]\n"
"buffer_load_dwordx4 v[56:59], v0, s[4:7], s97 offen offset:1024 sc1\n"
"v_mfma_f32_16x16x32_bf16 v[44:47], a[156:159], v[76:79], v[44:47]\n"
"v_mfma_f32_16x16x32_bf16 v[128:131], a[188:191], v[76:79], 0\n"
"v_mfma_f32_16x16x32_bf16 v[128:131], a[184:187], v[72:75], v[128:131]\n"
"s_waitcnt lgkmcnt(0)\n"
"s_barrier\n"
"v_mfma_f32_16x16x32_bf16 v[128:131], a[180:183], v[68:71], v[128:131]\n"
"buffer_load_dwordx4 v[136:139], v0, s[4:7], s97 offen offset:2048 sc1\n"
"v_mfma_f32_16x16x32_bf16 v[128:131], a[176:179], v[64:67], v[128:131]\n"
"ds_read_b128 v[140:143], v4\n"
"ds_read_b128 v[144:147], v5\n"
"v_mfma_f32_16x16x32_bf16 v[128:131], a[172:175], v[60:63], v[128:131]\n"
"v_cmp_u_f32_e32 vcc, v44, v44\n"
"v_mfma_f32_16x16x32_bf16 v[128:131], a[168:171], v[52:55], v[128:131]\n"
"s_cbranch_vccnz .Lmk_p65s\n"
".Lmk_p65r:\n"
"v_mfma_f32_16x16x32_bf16 v[128:131], a[164:167], v[40:43], v[128:131]\n"
"ds_write_b128 v3, v[44:47] offset:12288\n"
"v_mfma_f32_16x16x32_bf16 v[128:131], a[160:163], v[36:39], v[128:131]\n"
"buffer_load_dwordx4 v[44:47], v0, s[4:7], s97 offen offset:3072 sc1\n"
"v_mfma_f32_16x16x32_bf16 v[148:151], a[192:195], v[36:39], 0\n"
"ds_read_b128 v[152:155], v4 offset:1024\n"
"ds_read_b128 v[156:159], v5 offset:1024\n"
"s_waitcnt lgkmcnt(3)\n"
"v_mfma_f32_16x16x32_bf16 v[148:151], a[196:199], v[40:43], v[148:151]\n"
"v_add_f32_e32 v140, v140, v255\n"
"v_add_f32_e32 v141, v141, v254\n"
"v_add_f32_e32 v142, v142, v253\n"
"v_mfma_f32_16x16x32_bf16 v[148:151], a[200:203], v[52:55], v[148:151]\n"
"buffer_load_dwordx4 v[160:163], v1, s[4:7], s97 offen sc1\n"
"v_add_f32_e32 v143, v143, v252\n"
"v_mfma_f32_16x16x32_bf16 v[148:151], a[204:207], v[60:63], v[148:151]\n"
"v_add_f32_e32 v140, v140, v144\n"
"v_add_f32_e32 v141, v141, v145\n"
"v_add_f32_e32 v142, v142, v146\n"
"v_mfma_f32_16x16x32_bf16 v[148:151], a[208:211], v[64:67], v[148:151]\n"
"v_add_f32_e32 v143, v143, v147\n"
"v_mfma_f32_16x16x32_bf16 v[148:151], a[212:215], v[68:71], v[148:151]\n"
"ds_write_b128 v3, v[128:131] offset:13312\n"
"v_mfma_f32_16x16x32_bf16 v[148:151], a[216:219], v[72:75], v[148:151]\n"
"buffer_load_dwordx4 v[128:131], v1, s[4:7], s97 offen offset:1024 sc1\n"
"s_waitcnt lgkmcnt(1)\n"
"v_mfma_f32_16x16x32_bf16 v[148:151], a[220:223], v[76:79], v[148:151]\n"
"ds_read_b128 v[144:147], v4 offset:2048\n"
"ds_read_b128 v[164:167], v5 offset:2048\n"
"v_add_f32_e32 v152, v152, v251\n"
"v_mfma_f32_16x16x32_bf16 v[168:171], a[252:255], v[76:79], 0\n"
"v_add_f32_e32 v153, v153, v250\n"
"v_add_f32_e32 v154, v154, v249\n"
"v_add_f32_e32 v155, v155, v248\n"
"v_mfma_f32_16x16x32_bf16 v[168:171], a[248:251], v[72:75], v[168:171]\n"
"buffer_load_dwordx4 v[72:75], v1, s[4:7], s97 offen offset:2048 sc1\n"
"v_add_f32_e32 v152, v152, v156\n"
"v_mfma_f32_16x16x32_bf16 v[168:171], a[244:247], v[68:71], v[168:171]\n"
"v_add_f32_e32 v153, v153, v157\n"
"v_add_f32_e32 v154, v154, v158\n"
"v_add_f32_e32 v155, v155, v159\n"
"v_mfma_f32_16x16x32_bf16 v[168:171], a[240:243], v[64:67], v[168:171]\n"
"v_mfma_f32_16x16x32_bf16 v[168:171], a[236:239], v[60:63], v[168:171]\n"
"buffer_load_dwordx4 v[60:63], v1, s[4:7], s97 offen offset:3072 sc1\n"
"s_waitcnt lgkmcnt(0)\n"
"v_mfma_f32_16x16x32_bf16 v[168:171], a[232:235], v[52:55], v[168:171]\n"
"ds_write_b128 v3, v[148:151] offset:14336\n"
"v_mfma_f32_16x16x32_bf16 v[168:171], a[228:231], v[40:43], v[168:171]\n"
"s_add_u32 s97, s58, 0x400000\n"
"ds_read_b128 v[40:43], v4 offset:3072\n"
"ds_read_b128 v[52:55], v5 offset:3072\n"
"v_add_f32_e32 v144, v144, v247\n"
"v_mfma_f32_16x16x32_bf16 v[168:171], a[224:227], v[36:39], v[168:171]\n"
"s_waitcnt vmcnt(21)\n"
"buffer_load_dwordx4 v[36:39], v0, s[4:7], s97 offen sc1\n"
"; @B G2 b.3\n"
"v_mfma_f32_16x16x32_bf16 v[64:67], a[128:131], v[84:87], v[140:143]\n"
"v_add_f32_e32 v145, v145, v246\n"
"v_add_f32_e32 v146, v146, v245\n"
"v_add_f32_e32 v147, v147, v244\n"
"v_mfma_f32_16x16x32_bf16 v[64:67], a[132:135], v[88:91], v[64:67]\n"
"v_add_f32_e32 v144, v144, v164\n"
"v_add_f32_e32 v145, v145, v165\n"
"v_add_f32_e32 v146, v146, v166\n"
"v_mfma_f32_16x16x32_bf16 v[64:67], a[136:139], v[100:103], v[64:67]\n"
"buffer_load_dwordx4 v[68:71], v0, s[4:7], s97 offen offset:1024 sc1\n"
"v_add_f32_e32 v147, v147, v167\n"
"v_mfma_f32_16x16x32_bf16 v[64:67], a[140:143], v[104:107], v[64:67]\n"
"s_waitcnt vmcnt(19)\n"
"s_waitcnt lgkmcnt(0)\n"
"v_add_f32_e32 v40, v40, v243\n"
"v_add_f32_e32 v41, v41, v242\n"
"v_mfma_f32_16x16x32_bf16 v[64:67], a[144:147], v[80:83], v[64:67]\n"
"v_add_f32_e32 v42, v42, v241\n"
"v_add_f32_e32 v43, v43, v240\n"
"v_add_f32_e32 v40, v40, v52\n"
"v_mfma_f32_16x16x32_bf16 v[64:67], a[148:151], v[116:119], v[64:67]\n"
"ds_write_b128 v3, v[168:171] offset:15360\n"
"v_mfma_f32_16x16x32_bf16 v[64:67], a[152:155], v[96:99], v[64:67]\n"
"buffer_load_dwordx4 v[76:79], v0, s[4:7], s97 offen offset:2048 sc1\n"
"v_add_f32_e32 v41, v41, v53\n"
"v_mfma_f32_16x16x32_bf16 v[64:67], a[156:159], v[92:95], v[64:67]\n"
"v_add_f32_e32 v42, v42, v54\n"
"v_add_f32_e32 v43, v43, v55\n"
"v_mfma_f32_16x16x32_bf16 v[52:55], a[188:191], v[92:95], v[152:155]\n"
"v_mfma_f32_16x16x32_bf16 v[52:55], a[184:187], v[96:99], v[52:55]\n"
"s_waitcnt lgkmcnt(0)\n"
"s_barrier\n"
"v_mfma_f32_16x16x32_bf16 v[52:55], a[180:183], v[116:119], v[52:55]\n"
"buffer_load_dwordx4 v[148:151], v0, s[4:7], s97 offen offset:3072 sc1\n"
"v_mfma_f32_16x16x32_bf16 v[52:55], a[176:179], v[80:83], v[52:55]\n"
"ds_read_b128 v[156:159], v6\n"
"ds_read_b128 v[164:167], v6 offset:1024\n"
"v_mfma_f32_16x16x32_bf16 v[52:55], a[172:175], v[104:107], v[52:55]\n"
"ds_read_b128 v[168:171], v6 offset:2048\n"
"v_cmp_u_f32_e32 vcc, v64, v64\n"
"v_mfma_f32_16x16x32_bf16 v[52:55], a[168:171], v[100:103], v[52:55]\n"
"s_cbranch_vccnz .Lmk_p66s\n"
".Lmk_p66r:\n"
"ds_read_b128 v[140:143], v6 offset:3072\n"
"v_mfma_f32_16x16x32_bf16 v[52:55], a[164:167], v[88:91], v[52:55]\n"
"buffer_load_dwordx4 v[152:155], v1, s[4:7], s97 offen sc1\n"
"v_mfma_f32_16x16x32_bf16 v[52:55], a[160:163], v[84:87], v[52:55]\n"
"v_mfma_f32_16x16x32_bf16 v[144:147], a[192:195], v[84:87], v[144:147]\n"
"v_mfma_f32_16x16x32_bf16 v[144:147], a[196:199], v[88:91], v[144:147]\n"
"buffer_load_dwordx4 v[172:175], v1, s[4:7], s97 offen offset:1024 sc1\n"
"v_mfma_f32_16x16x32_bf16 v[144:147], a[200:203], v[100:103], v[144:147]\n"
"v_mfma_f32_16x16x32_bf16 v[144:147], a[204:207], v[104:107], v[144:147]\n"
"v_mfma_f32_16x16x32_bf16 v[144:147], a[208:211], v[80:83], v[144:147]\n"
"buffer_load_dwordx4 v[176:179], v1, s[4:7], s97 offen offset:2048 sc1\n"
"s_waitcnt lgkmcnt(0)\n"
"v_mfma_f32_16x16x32_bf16 v[144:147], a[212:215], v[116:119], v[144:147]\n"
"v_add_f32_e32 v156, v64, v156\n"
"v_add_f32_e32 v157, v65, v157\n"
"v_add_f32_e32 v158, v66, v158\n"
"v_mfma_f32_16x16x32_bf16 v[144:147], a[216:219], v[96:99], v[144:147]\n"
"v_add_f32_e32 v159, v67, v159\n"
"v_add_f32_e32 v164, v52, v164\n"
"v_add_f32_e32 v165, v53, v165\n"
"v_mfma_f32_16x16x32_bf16 v[144:147], a[220:223], v[92:95], v[144:147]\n"
"buffer_load_dwordx4 v[64:67], v1, s[4:7], s97 offen offset:3072 sc1\n"
"v_add_f32_e32 v166, v54, v166\n"
"v_mfma_f32_16x16x32_bf16 v[40:43], a[252:255], v[92:95], v[40:43]\n"
"v_add_f32_e32 v167, v55, v167\n"
"v_cvt_pk_bf16_f32 v52, v156, v157\n"
"v_cvt_pk_bf16_f32 v53, v158, v159\n"
"v_mfma_f32_16x16x32_bf16 v[40:43], a[248:251], v[96:99], v[40:43]\n"
"s_add_u32 s97, s61, 0x840000\n"
"v_cvt_pk_bf16_f32 v54, v164, v165\n"
"v_cvt_pk_bf16_f32 v55, v166, v167\n"
"v_mfma_f32_16x16x32_bf16 v[40:43], a[244:247], v[116:119], v[40:43]\n"
"buffer_store_dwordx4 v[52:55], v0, s[4:7], s97 offen sc1\n"
"v_mfma_f32_16x16x32_bf16 v[40:43], a[240:243], v[80:83], v[40:43]\n"
"ds_write_b128 v2, v[52:55] offset:8192\n"
"v_mfma_f32_16x16x32_bf16 v[40:43], a[236:239], v[104:107], v[40:43]\n"
"v_mfma_f32_16x16x32_bf16 v[40:43], a[232:235], v[100:103], v[40:43]\n"
"v_mfma_f32_16x16x32_bf16 v[40:43], a[228:231], v[88:91], v[40:43]\n"
"v_mfma_f32_16x16x32_bf16 v[40:43], a[224:227], v[84:87], v[40:43]\n"
"; @B fin b\n"
"s_add_u32 s97, s61, 0x840000\n"
"v_add_f32_e32 v168, v144, v168\n"
"v_add_f32_e32 v169, v145, v169\n"
"v_add_f32_e32 v170, v146, v170\n"
"v_add_f32_e32 v171, v147, v171\n"
"s_nop 2\n"
"v_add_f32_e32 v140, v40, v140\n"
"v_add_f32_e32 v141, v41, v141\n"
"v_add_f32_e32 v142, v42, v142\n"
"v_add_f32_e32 v143, v43, v143\n"
"v_cvt_pk_bf16_f32 v40, v168, v169\n"
"v_cvt_pk_bf16_f32 v41, v170, v171\n"
"v_cvt_pk_bf16_f32 v42, v140, v141\n"
"v_cvt_pk_bf16_f32 v43, v142, v143\n"
"buffer_store_dwordx4 v[40:43], v0, s[4:7], s97 offen offset:1024 sc1\n"
"s_add_u32 s97, s59, 0x400000\n"
"buffer_load_dwordx4 v[80:83], v0, s[4:7], s97 offen sc1\n"
"buffer_load_dwordx4 v[84:87], v0, s[4:7], s97 offen offset:1024 sc1\n"
"buffer_load_dwordx4 v[88:91], v0, s[4:7], s97 offen offset:2048 sc1\n"
"buffer_load_dwordx4 v[92:95], v0, s[4:7], s97 offen offset:3072 sc1\n"
"buffer_load_dwordx4 v[96:99], v1, s[4:7], s97 offen sc1\n"
"buffer_load_dwordx4 v[100:103], v1, s[4:7], s97 offen offset:1024 sc1\n"
"buffer_load_dwordx4 v[104:107], v1, s[4:7], s97 offen offset:2048 sc1\n"
"buffer_load_dwordx4 v[116:119], v1, s[4:7], s97 offen offset:3072 sc1\n"
"s_waitcnt vmcnt(32)\n"
"; @B G2 a.0\n"
"v_mfma_f32_16x16x32_bf16 v[140:143], a[128:131], v[120:123], 0\n"
"v_mfma_f32_16x16x32_bf16 v[140:143], a[132:135], v[124:127], v[140:143]\n"
"s_waitcnt vmcnt(30)\n"
"ds_write_b128 v2, v[40:43] offset:9216\n"
"v_mfma_f32_16x16x32_bf16 v[140:143], a[136:139], v[108:111], v[140:143]\n"
"v_mov_b32_e32 v144, 0x3f803f80\n"
"v_mov_b32_e32 v145, 0x3f803f80\n"
"v_mfma_f32_16x16x32_bf16 v[140:143], a[140:143], v[132:135], v[140:143]\n"
"s_waitcnt vmcnt(28)\n"
"v_mov_b32_e32 v146, 0x3f803f80\n"
"v_mov_b32_e32 v147, 0x3f803f80\n"
"v_mfma_f32_16x16x32_bf16 v[140:143], a[144:147], v[112:115], v[140:143]\n"
"s_nop 0\n"
"v_mfma_f32_16x16x32_bf16 v[156:159], v[144:147], v[52:55], 0\n"
"v_mfma_f32_16x16x32_bf16 v[156:159], v[144:147], v[40:43], v[156:159]\n"
"v_mfma_f32_16x16x32_bf16 v[144:147], v[52:55], v[52:55], 0\n"
"v_mfma_f32_16x16x32_bf16 v[144:147], v[40:43], v[40:43], v[144:147]\n"
"buffer_load_dwordx4 a[56:59], v1, s[8:11], s98 offen offset:2048 sc1\n"
"v_mfma_f32_16x16x32_bf16 v[140:143], a[148:151], v[28:31], v[140:143]\n"
"s_waitcnt vmcnt(27)\n"
"v_mfma_f32_16x16x32_bf16 v[140:143], a[152:155], v[32:35], v[140:143]\n"
"v_mfma_f32_16x16x32_bf16 v[140:143], a[156:159], v[24:27], v[140:143]\n"
"buffer_load_dwordx4 a[60:63], v1, s[8:11], s98 offen offset:3072 sc1\n"
"v_mfma_f32_16x16x32_bf16 v[40:43], a[188:191], v[24:27], 0\n"
"s_mov_b32 s96, 0xaaaaaaaa\n"
"s_mov_b32 s97, 0xaaaaaaaa\n"
"s_add_u32 s98, s69, 0x4000\n"
"v_mfma_f32_16x16x32_bf16 v[40:43], a[184:187], v[32:35], v[40:43]\n"
"v_mfma_f32_16x16x32_bf16 v[40:43], a[180:183], v[28:31], v[40:43]\n"
"buffer_load_dwordx4 a[64:67], v0, s[8:11], s98 offen sc1\n"
"v_mfma_f32_16x16x32_bf16 v[40:43], a[176:179], v[112:115], v[40:43]\n"
"v_cndmask_b32_e64 v144, v144, v145, s[96:97]\n"
"v_cndmask_b32_e64 v146, v146, v147, s[96:97]\n"
"v_mfma_f32_16x16x32_bf16 v[40:43], a[172:175], v[132:135], v[40:43]\n"
"v_cmp_u_f32_e32 vcc, v140, v140\n"
"v_mfma_f32_16x16x32_bf16 v[40:43], a[168:171], v[108:111], v[40:43]\n"
"s_cbranch_vccnz .Lmk_p68s\n"
".Lmk_p68r:\n"
"v_mfma_f32_16x16x32_bf16 v[40:43], a[164:167], v[124:127], v[40:43]\n"
"ds_write_b128 v3, v[140:143]\n"
"v_mfma_f32_16x16x32_bf16 v[40:43], a[160:163], v[120:123], v[40:43]\n"
"s_mov_b32 s96, 0xcccccccc\n"
"s_mov_b32 s97, 0xcccccccc\n"
"v_mfma_f32_16x16x32_bf16 v[52:55], a[192:195], v[120:123], 0\n"
"v_cndmask_b32_e64 v157, v144, v146, s[96:97]\n"
"v_mfma_f32_16x16x32_bf16 v[52:55], a[196:199], v[124:127], v[52:55]\n"
"s_mov_b32 s96, 0xf0000f\n"
"s_mov_b32 s97, 0xf0000f00\n"
"v_mfma_f32_16x16x32_bf16 v[52:55], a[200:203], v[108:111], v[52:55]\n"
"v_cndmask_b32_e64 v157, 0, v157, s[96:97]\n"
"v_mov_b32_e32 v255, v157\n"
"v_mfma_f32_16x16x32_bf16 v[52:55], a[204:207], v[132:135], v[52:55]\n"
"s_nop 0\n"
"v_permlane16_swap_b32_e32 v157, v255\n"
"v_add_f32_e32 v157, v157, v255\n"
"v_mfma_f32_16x16x32_bf16 v[52:55], a[208:211], v[112:115], v[52:55]\n"
"v_mov_b32_e32 v255, v157\n"
"v_mfma_f32_16x16x32_bf16 v[52:55], a[212:215], v[28:31], v[52:55]\n"
"ds_write_b128 v3, v[40:43] offset:1024\n"
"v_mfma_f32_16x16x32_bf16 v[52:55], a[216:219], v[32:35], v[52:55]\n"
"s_mov_b32 s96, 0xffff0000\n"
"v_permlane32_swap_b32_e32 v157, v255\n"
"v_add_f32_e32 v157, v157, v255\n"
"v_mfma_f32_16x16x32_bf16 v[52:55], a[220:223], v[24:27], v[52:55]\n"
"v_cndmask_b32_e64 v156, v156, v157, s[96:97]\n"
"s_add_u32 s97, s63, 0x84000\n"
"v_mfma_f32_16x16x32_bf16 v[40:43], a[252:255], v[24:27], 0\n"
"buffer_store_dword v156, v14, s[4:7], s97 offen sc1\n"
"v_mfma_f32_16x16x32_bf16 v[40:43], a[248:251], v[32:35], v[40:43]\n"
"buffer_load_dwordx4 a[68:71], v0, s[8:11], s98 offen offset:1024 sc1\n"
"v_mfma_f32_16x16x32_bf16 v[40:43], a[244:247], v[28:31], v[40:43]\n"
"v_mfma_f32_16x16x32_bf16 v[40:43], a[240:243], v[112:115], v[40:43]\n"
"v_mfma_f32_16x16x32_bf16 v[40:43], a[236:239], v[132:135], v[40:43]\n"
"buffer_load_dwordx4 a[72:75], v0, s[8:11], s98 offen offset:2048 sc1\n"
"v_mfma_f32_16x16x32_bf16 v[40:43], a[232:235], v[108:111], v[40:43]\n"
"ds_write_b128 v3, v[52:55] offset:2048\n"
"v_mfma_f32_16x16x32_bf16 v[40:43], a[228:231], v[124:127], v[40:43]\n"
"v_mfma_f32_16x16x32_bf16 v[40:43], a[224:227], v[120:123], v[40:43]\n"
"s_waitcnt vmcnt(28)\n"
"buffer_load_dwordx4 a[76:79], v0, s[8:11], s98 offen offset:3072 sc1\n"
"; @B G2 a.1\n"
"v_mfma_f32_16x16x32_bf16 v[24:27], a[128:131], v[48:51], 0\n"
"v_mfma_f32_16x16x32_bf16 v[24:27], a[132:135], v[56:59], v[24:27]\n"
"v_mfma_f32_16x16x32_bf16 v[24:27], a[136:139], v[136:139], v[24:27]\n"
"buffer_load_dwordx4 a[80:83], v1, s[8:11], s98 offen sc1\n"
"v_mfma_f32_16x16x32_bf16 v[24:27], a[140:143], v[44:47], v[24:27]\n"
"s_waitcnt vmcnt(26)\n"
"v_mfma_f32_16x16x32_bf16 v[24:27], a[144:147], v[160:163], v[24:27]\n"
"v_mfma_f32_16x16x32_bf16 v[24:27], a[148:151], v[128:131], v[24:27]\n"
"ds_write_b128 v3, v[40:43] offset:3072\n"
"v_mfma_f32_16x16x32_bf16 v[24:27], a[152:155], v[72:75], v[24:27]\n"
"buffer_load_dwordx4 a[84:87], v1, s[8:11], s98 offen offset:1024 sc1\n"
"v_mfma_f32_16x16x32_bf16 v[24:27], a[156:159], v[60:63], v[24:27]\n"
"v_mfma_f32_16x16x32_bf16 v[28:31], a[188:191], v[60:63], 0\n"
"v_mfma_f32_16x16x32_bf16 v[28:31], a[184:187], v[72:75], v[28:31]\n"
"buffer_load_dwordx4 a[88:91], v1, s[8:11], s98 offen offset:2048 sc1\n"
"v_mfma_f32_16x16x32_bf16 v[28:31], a[180:183], v[128:131], v[28:31]\n"
"v_mfma_f32_16x16x32_bf16 v[28:31], a[176:179], v[160:163], v[28:31]\n"
"v_mfma_f32_16x16x32_bf16 v[28:31], a[172:175], v[44:47], v[28:31]\n"
"s_nop 1\n"
"v_cmp_u_f32_e32 vcc, v24, v24\n"
"v_mfma_f32_16x16x32_bf16 v[28:31], a[168:171], v[136:139], v[28:31]\n"
"s_cbranch_vccnz .Lmk_p69s\n"
".Lmk_p69r:\n"
"v_mfma_f32_16x16x32_bf16 v[28:31], a[164:167], v[56:59], v[28:31]\n"
"ds_write_b128 v3, v[24:27] offset:6144\n"
"v_mfma_f32_16x16x32_bf16 v[28:31], a[160:163], v[48:51], v[28:31]\n"
"buffer_load_dwordx4 a[92:95], v1, s[8:11], s98 offen offset:3072 sc1\n"
"v_mfma_f32_16x16x32_bf16 v[24:27], a[192:195], v[48:51], 0\n"
"s_add_u32 s98, s69, 0x6000\n"
"v_mfma_f32_16x16x32_bf16 v[24:27], a[196:199], v[56:59], v[24:27]\n"
"v_mfma_f32_16x16x32_bf16 v[24:27], a[200:203], v[136:139], v[24:27]\n"
"buffer_load_dwordx4 a[96:99], v0, s[8:11], s98 offen sc1\n"
"v_mfma_f32_16x16x32_bf16 v[24:27], a[204:207], v[44:47], v[24:27]\n"
"v_mfma_f32_16x16x32_bf16 v[24:27], a[208:211], v[160:163], v[24:27]\n"
"ds_read_b128 v[32:35], v2\n"
"ds_read_b128 v[40:43], v2 offset:1024\n"
"v_mfma_f32_16x16x32_bf16 v[24:27], a[212:215], v[128:131], v[24:27]\n"
"ds_write_b128 v3, v[28:31] offset:7168\n"
"v_mfma_f32_16x16x32_bf16 v[24:27], a[216:219], v[72:75], v[24:27]\n"
"buffer_load_dwordx4 a[100:103], v0, s[8:11], s98 offen offset:1024 sc1\n"
"v_mfma_f32_16x16x32_bf16 v[24:27], a[220:223], v[60:63], v[24:27]\n"
"v_mfma_f32_16x16x32_bf16 v[28:31], a[252:255], v[60:63], 0\n"
"v_mfma_f32_16x16x32_bf16 v[28:31], a[248:251], v[72:75], v[28:31]\n"
"buffer_load_dwordx4 a[104:107], v0, s[8:11], s98 offen offset:2048 sc1\n"
"s_waitcnt lgkmcnt(1)\n"
"v_mfma_f32_16x16x32_bf16 v[28:31], a[244:247], v[128:131], v[28:31]\n"
"v_lshlrev_b32_e32 v255, 16, v32\n"
"v_and_b32_e32 v254, 0xffff0000, v32\n"
"v_lshlrev_b32_e32 v253, 16, v33\n"
"v_mfma_f32_16x16x32_bf16 v[28:31], a[240:243], v[160:163], v[28:31]\n"
"v_and_b32_e32 v252, 0xffff0000, v33\n"
"v_lshlrev_b32_e32 v251, 16, v34\n"
"v_and_b32_e32 v250, 0xffff0000, v34\n"
"v_mfma_f32_16x16x32_bf16 v[28:31], a[236:239], v[44:47], v[28:31]\n"
"buffer_load_dwordx4 a[108:111], v0, s[8:11], s98 offen offset:3072 sc1\n"
"v_lshlrev_b32_e32 v249, 16, v35\n"
"v_mfma_f32_16x16x32_bf16 v[28:31], a[232:235], v[136:139], v[28:31]\n"
"ds_write_b128 v3, v[24:27] offset:8192\n"
"v_mfma_f32_16x16x32_bf16 v[28:31], a[228:231], v[56:59], v[28:31]\n"
"v_and_b32_e32 v248, 0xffff0000, v35\n"
"v_lshlrev_b32_e32 v247, 16, v40\n"
"v_and_b32_e32 v246, 0xffff0000, v40\n"
"v_mfma_f32_16x16x32_bf16 v[28:31], a[224:227], v[48:51], v[28:31]\n"
"s_waitcnt vmcnt(29)\n"
"buffer_load_dwordx4 a[112:115], v1, s[8:11], s98 offen sc1\n"
"; @B G2 a.2\n"
"v_mfma_f32_16x16x32_bf16 v[24:27], a[128:131], v[36:39], 0\n"
"v_lshlrev_b32_e32 v245, 16, v41\n"
"v_and_b32_e32 v244, 0xffff0000, v41\n"
"v_lshlrev_b32_e32 v243, 16, v42\n"
"v_mfma_f32_16x16x32_bf16 v[24:27], a[132:135], v[68:71], v[24:27]\n"
"v_and_b32_e32 v242, 0xffff0000, v42\n"
"v_lshlrev_b32_e32 v241, 16, v43\n"
"v_and_b32_e32 v240, 0xffff0000, v43\n"
"v_mfma_f32_16x16x32_bf16 v[24:27], a[136:139], v[76:79], v[24:27]\n"
"buffer_load_dwordx4 a[116:119], v1, s[8:11], s98 offen offset:1024 sc1\n"
"v_mfma_f32_16x16x32_bf16 v[24:27], a[140:143], v[148:151], v[24:27]\n"
"s_waitcnt vmcnt(27)\n"
"v_mfma_f32_16x16x32_bf16 v[24:27], a[144:147], v[152:155], v[24:27]\n"
"v_mfma_f32_16x16x32_bf16 v[24:27], a[148:151], v[172:175], v[24:27]\n"
"ds_write_b128 v3, v[28:31] offset:9216\n"
"v_mfma_f32_16x16x32_bf16 v[24:27], a[152:155], v[176:179], v[24:27]\n"
"buffer_load_dwordx4 a[120:123], v1, s[8:11], s98 offen offset:2048 sc1\n"
"v_mfma_f32_16x16x32_bf16 v[24:27], a[156:159], v[64:67], v[24:27]\n"
"v_mfma_f32_16x16x32_bf16 v[28:31], a[188:191], v[64:67], 0\n"
"v_mfma_f32_16x16x32_bf16 v[28:31], a[184:187], v[176:179], v[28:31]\n"
"s_waitcnt lgkmcnt(0)\n"
"s_barrier\n"
"v_mfma_f32_16x16x32_bf16 v[28:31], a[180:183], v[172:175], v[28:31]\n"
"buffer_load_dwordx4 a[124:127], v1, s[8:11], s98 offen offset:3072 sc1\n"
"v_mfma_f32_16x16x32_bf16 v[28:31], a[176:179], v[152:155], v[28:31]\n"
"s_add_u32 s98, s69, 0x8000\n"
"ds_read_b128 v[32:35], v4\n"
"ds_read_b128 v[40:43], v5\n"
"v_mfma_f32_16x16x32_bf16 v[28:31], a[172:175], v[148:151], v[28:31]\n"
"v_cmp_u_f32_e32 vcc, v24, v24\n"
"v_mfma_f32_16x16x32_bf16 v[28:31], a[168:171], v[76:79], v[28:31]\n"
"s_cbranch_vccnz .Lmk_p70s\n"
".Lmk_p70r:\n"
"v_mfma_f32_16x16x32_bf16 v[28:31], a[164:167], v[68:71], v[28:31]\n"
"ds_write_b128 v3, v[24:27] offset:12288\n"
"v_mfma_f32_16x16x32_bf16 v[28:31], a[160:163], v[36:39], v[28:31]\n"
"buffer_load_dwordx4 v[24:27], v0, s[8:11], s98 offen sc1\n"
"v_mfma_f32_16x16x32_bf16 v[44:47], a[192:195], v[36:39], 0\n"
"ds_read_b128 v[48:51], v4 offset:1024\n"
"ds_read_b128 v[52:55], v5 offset:1024\n"
"s_waitcnt lgkmcnt(3)\n"
"v_mfma_f32_16x16x32_bf16 v[44:47], a[196:199], v[68:71], v[44:47]\n"
"v_add_f32_e32 v32, v32, v255\n"
"v_add_f32_e32 v33, v33, v254\n"
"v_add_f32_e32 v34, v34, v253\n"
"v_mfma_f32_16x16x32_bf16 v[44:47], a[200:203], v[76:79], v[44:47]\n"
"buffer_load_dwordx4 v[56:59], v0, s[8:11], s98 offen offset:1024 sc1\n"
"v_add_f32_e32 v35, v35, v252\n"
"v_mfma_f32_16x16x32_bf16 v[44:47], a[204:207], v[148:151], v[44:47]\n"
"v_add_f32_e32 v32, v32, v40\n"
"v_add_f32_e32 v33, v33, v41\n"
"v_add_f32_e32 v34, v34, v42\n"
"v_mfma_f32_16x16x32_bf16 v[44:47], a[208:211], v[152:155], v[44:47]\n"
"v_add_f32_e32 v35, v35, v43\n"
"v_mfma_f32_16x16x32_bf16 v[44:47], a[212:215], v[172:175], v[44:47]\n"
"ds_write_b128 v3, v[28:31] offset:13312\n"
"v_mfma_f32_16x16x32_bf16 v[44:47], a[216:219], v[176:179], v[44:47]\n"
"buffer_load_dwordx4 v[28:31], v0, s[8:11], s98 offen offset:2048 sc1\n"
"s_waitcnt lgkmcnt(1)\n"
"v_mfma_f32_16x16x32_bf16 v[44:47], a[220:223], v[64:67], v[44:47]\n"
"ds_read_b128 v[40:43], v4 offset:2048\n"
"ds_read_b128 v[60:63], v5 offset:2048\n"
"v_add_f32_e32 v48, v48, v251\n"
"v_mfma_f32_16x16x32_bf16 v[72:75], a[252:255], v[64:67], 0\n"
"v_add_f32_e32 v49, v49, v250\n"
"v_add_f32_e32 v50, v50, v249\n"
"v_add_f32_e32 v51, v51, v248\n"
"v_mfma_f32_16x16x32_bf16 v[72:75], a[248:251], v[176:179], v[72:75]\n"
"buffer_load_dwordx4 v[64:67], v0, s[8:11], s98 offen offset:3072 sc1\n"
"v_add_f32_e32 v48, v48, v52\n"
"v_mfma_f32_16x16x32_bf16 v[72:75], a[244:247], v[172:175], v[72:75]\n"
"v_add_f32_e32 v49, v49, v53\n"
"v_add_f32_e32 v50, v50, v54\n"
"v_add_f32_e32 v51, v51, v55\n"
"v_mfma_f32_16x16x32_bf16 v[72:75], a[240:243], v[152:155], v[72:75]\n"
"v_mfma_f32_16x16x32_bf16 v[72:75], a[236:239], v[148:151], v[72:75]\n"
"buffer_load_dwordx4 v[52:55], v1, s[8:11], s98 offen sc1\n"
"s_waitcnt lgkmcnt(0)\n"
"v_mfma_f32_16x16x32_bf16 v[72:75], a[232:235], v[76:79], v[72:75]\n"
"ds_write_b128 v3, v[44:47] offset:14336\n"
"v_mfma_f32_16x16x32_bf16 v[72:75], a[228:231], v[68:71], v[72:75]\n"
"ds_read_b128 v[44:47], v4 offset:3072\n"
"ds_read_b128 v[68:71], v5 offset:3072\n"
"v_add_f32_e32 v40, v40, v247\n"
"v_mfma_f32_16x16x32_bf16 v[72:75], a[224:227], v[36:39], v[72:75]\n"
"s_waitcnt vmcnt(28)\n"
"buffer_load_dwordx4 v[36:39], v1, s[8:11], s98 offen offset:1024 sc1\n"
"; @B G2 a.3\n"
"v_mfma_f32_16x16x32_bf16 v[76:79], a[128:131], v[80:83], v[32:35]\n"
"v_add_f32_e32 v41, v41, v246\n"
"v_add_f32_e32 v42, v42, v245\n"
"v_add_f32_e32 v43, v43, v244\n"
"v_mfma_f32_16x16x32_bf16 v[76:79], a[132:135], v[84:87], v[76:79]\n"
"v_add_f32_e32 v40, v40, v60\n"
"v_add_f32_e32 v41, v41, v61\n"
"v_add_f32_e32 v42, v42, v62\n"
"v_mfma_f32_16x16x32_bf16 v[76:79], a[136:139], v[88:91], v[76:79]\n"
"buffer_load_dwordx4 v[108:111], v1, s[8:11], s98 offen offset:2048 sc1\n"
"v_add_f32_e32 v43, v43, v63\n"
"v_mfma_f32_16x16x32_bf16 v[76:79], a[140:143], v[92:95], v[76:79]\n"
"s_waitcnt vmcnt(26)\n"
"s_waitcnt lgkmcnt(0)\n"
"v_add_f32_e32 v44, v44, v243\n"
"v_add_f32_e32 v45, v45, v242\n"
"v_mfma_f32_16x16x32_bf16 v[76:79], a[144:147], v[96:99], v[76:79]\n"
"v_add_f32_e32 v46, v46, v241\n"
"v_add_f32_e32 v47, v47, v240\n"
"v_add_f32_e32 v44, v44, v68\n"
"v_mfma_f32_16x16x32_bf16 v[76:79], a[148:151], v[100:103], v[76:79]\n"
"ds_write_b128 v3, v[72:75] offset:15360\n"
"v_mfma_f32_16x16x32_bf16 v[76:79], a[152:155], v[104:107], v[76:79]\n"
"buffer_load_dwordx4 v[60:63], v1, s[8:11], s98 offen offset:3072 sc1\n"
"v_add_f32_e32 v45, v45, v69\n"
"v_mfma_f32_16x16x32_bf16 v[76:79], a[156:159], v[116:119], v[76:79]\n"
"s_add_u32 s98, s69, 0xa000\n"
"v_add_f32_e32 v46, v46, v70\n"
"v_add_f32_e32 v47, v47, v71\n"
"v_mfma_f32_16x16x32_bf16 v[68:71], a[188:191], v[116:119], v[48:51]\n"
"v_mfma_f32_16x16x32_bf16 v[68:71], a[184:187], v[104:107], v[68:71]\n"
"s_waitcnt lgkmcnt(0)\n"
"s_barrier\n"
"v_mfma_f32_16x16x32_bf16 v[68:71], a[180:183], v[100:103], v[68:71]\n"
"buffer_load_dwordx4 v[72:75], v0, s[8:11], s98 offen sc1\n"
"v_mfma_f32_16x16x32_bf16 v[68:71], a[176:179], v[96:99], v[68:71]\n"
"ds_read_b128 v[112:115], v6\n"
"ds_read_b128 v[120:123], v6 offset:1024\n"
"v_mfma_f32_16x16x32_bf16 v[68:71], a[172:175], v[92:95], v[68:71]\n"
"ds_read_b128 v[124:127], v6 offset:2048\n"
"v_cmp_u_f32_e32 vcc, v76, v76\n"
"v_mfma_f32_16x16x32_bf16 v[68:71], a[168:171], v[88:91], v[68:71]\n"
"s_cbranch_vccnz .Lmk_p71s\n"
".Lmk_p71r:\n"
"ds_read_b128 v[32:35], v6 offset:3072\n"
"v_mfma_f32_16x16x32_bf16 v[68:71], a[164:167], v[84:87], v[68:71]\n"
"buffer_load_dwordx4 v[48:51], v0, s[8:11], s98 offen offset:1024 sc1\n"
"v_mfma_f32_16x16x32_bf16 v[68:71], a[160:163], v[80:83], v[68:71]\n"
"v_mfma_f32_16x16x32_bf16 v[40:43], a[192:195], v[80:83], v[40:43]\n"
"v_mfma_f32_16x16x32_bf16 v[40:43], a[196:199], v[84:87], v[40:43]\n"
"buffer_load_dwordx4 v[128:131], v0, s[8:11], s98 offen offset:2048 sc1\n"
"v_mfma_f32_16x16x32_bf16 v[40:43], a[200:203], v[88:91], v[40:43]\n"
"v_mfma_f32_16x16x32_bf16 v[40:43], a[204:207], v[92:95], v[40:43]\n"
"v_mfma_f32_16x16x32_bf16 v[40:43], a[208:211], v[96:99], v[40:43]\n"
"buffer_load_dwordx4 v[132:135], v0, s[8:11], s98 offen offset:3072 sc1\n"
"s_waitcnt lgkmcnt(0)\n"
"v_mfma_f32_16x16x32_bf16 v[40:43], a[212:215], v[100:103], v[40:43]\n"
"v_add_f32_e32 v112, v76, v112\n"
"v_add_f32_e32 v113, v77, v113\n"
"v_add_f32_e32 v114, v78, v114\n"
"v_mfma_f32_16x16x32_bf16 v[40:43], a[216:219], v[104:107], v[40:43]\n"
"v_add_f32_e32 v115, v79, v115\n"
"v_add_f32_e32 v120, v68, v120\n"
"v_add_f32_e32 v121, v69, v121\n"
"v_mfma_f32_16x16x32_bf16 v[40:43], a[220:223], v[116:119], v[40:43]\n"
"buffer_load_dwordx4 v[76:79], v1, s[8:11], s98 offen sc1\n"
"v_add_f32_e32 v122, v70, v122\n"
"v_mfma_f32_16x16x32_bf16 v[44:47], a[252:255], v[116:119], v[44:47]\n"
"v_add_f32_e32 v123, v71, v123\n"
"v_cvt_pk_bf16_f32 v68, v112, v113\n"
"v_cvt_pk_bf16_f32 v69, v114, v115\n"
"v_mfma_f32_16x16x32_bf16 v[44:47], a[248:251], v[104:107], v[44:47]\n"
"s_add_u32 s97, s61, 0x800000\n"
"v_cvt_pk_bf16_f32 v70, v120, v121\n"
"v_cvt_pk_bf16_f32 v71, v122, v123\n"
"v_mfma_f32_16x16x32_bf16 v[44:47], a[244:247], v[100:103], v[44:47]\n"
"buffer_load_dwordx4 v[100:103], v1, s[8:11], s98 offen offset:1024 sc1\n"
"v_mfma_f32_16x16x32_bf16 v[44:47], a[240:243], v[96:99], v[44:47]\n"
"buffer_store_dwordx4 v[68:71], v0, s[4:7], s97 offen sc1\n"
"v_mfma_f32_16x16x32_bf16 v[44:47], a[236:239], v[92:95], v[44:47]\n"
"ds_write_b128 v2, v[68:71]\n"
"v_mfma_f32_16x16x32_bf16 v[44:47], a[232:235], v[88:91], v[44:47]\n"
"buffer_load_dwordx4 v[88:91], v1, s[8:11], s98 offen offset:2048 sc1\n"
"v_mfma_f32_16x16x32_bf16 v[44:47], a[228:231], v[84:87], v[44:47]\n"
"v_mfma_f32_16x16x32_bf16 v[44:47], a[224:227], v[80:83], v[44:47]\n"
"buffer_load_dwordx4 v[80:83], v1, s[8:11], s98 offen offset:3072 sc1\n"
"s_add_u32 s98, s52, 0x840000\n"
"buffer_load_dwordx4 v[84:87], v0, s[8:11], s98 offen sc1\n"
"buffer_load_dwordx4 v[92:95], v0, s[8:11], s98 offen offset:1024 sc1\n"
"buffer_load_dwordx4 v[96:99], v0, s[8:11], s98 offen offset:2048 sc1\n"
"buffer_load_dwordx4 v[104:107], v0, s[8:11], s98 offen offset:3072 sc1\n"
"buffer_load_dwordx4 v[112:115], v1, s[8:11], s98 offen sc1\n"
"buffer_load_dwordx4 v[116:119], v1, s[8:11], s98 offen offset:1024 sc1\n"
"buffer_load_dwordx4 v[120:123], v1, s[8:11], s98 offen offset:2048 sc1\n"
"buffer_load_dwordx4 v[136:139], v1, s[8:11], s98 offen offset:3072 sc1\n"
"s_add_u32 s98, s53, 0x840000\n"
"buffer_load_dwordx4 v[140:143], v0, s[8:11], s98 offen sc1\n"
"buffer_load_dwordx4 v[144:147], v0, s[8:11], s98 offen offset:1024 sc1\n"
"buffer_load_dwordx4 v[148:151], v0, s[8:11], s98 offen offset:2048 sc1\n"
"buffer_load_dwordx4 v[152:155], v0, s[8:11], s98 offen offset:3072 sc1\n"
"buffer_load_dwordx4 v[156:159], v1, s[8:11], s98 offen sc1\n"
"buffer_load_dwordx4 v[160:163], v1, s[8:11], s98 offen offset:1024 sc1\n"
"buffer_load_dwordx4 v[164:167], v1, s[8:11], s98 offen offset:2048 sc1\n"
"buffer_load_dwordx4 v[168:171], v1, s[8:11], s98 offen offset:3072 sc1\n"
"s_lshr_b32 s98, s79, 7\n"
"s_add_u32 s52, s52, 0x800000\n"
"s_add_u32 s53, s53, 0x800000\n"
"s_add_u32 s54, s54, 0x800000\n"
"s_add_u32 s55, s55, 0x800000\n"
"s_add_u32 s56, s56, 0x800000\n"
"s_add_u32 s57, s57, 0x800000\n"
"s_add_u32 s58, s58, 0x800000\n"
"s_add_u32 s59, s59, 0x800000\n"
"s_add_u32 s60, s60, 0x800000\n"
"s_add_u32 s61, s61, 0x800000\n"
"s_add_u32 s62, s62, 0x80000\n"
"s_add_u32 s63, s63, 0x80000\n"
"s_add_u32 s64, s64, 0x1000000\n"
"s_add_u32 s65, s65, s79\n"
"s_add_u32 s66, s66, s79\n"
"s_add_u32 s67, s67, 0x20000\n"
"s_add_u32 s68, s68, 0x20000\n"
"s_add_u32 s71, s71, s98\n"
"s_add_u32 s73, s73, s98\n"
"s_mov_b32 s0, 1\n"
"s_mov_b32 s10, 0\n"
".Lmk_a0:\n"
"s_waitcnt vmcnt(12)\n"
"; @A G1 0.0\n"
"v_mfma_f32_16x16x32_bf16 v[172:175], a[0:3], v[84:87], 0\n"
"v_mfma_f32_16x16x32_bf16 v[172:175], a[4:7], v[92:95], v[172:175]\n"
"s_add_u32 s98, s62, 0x4000\n"
"v_mfma_f32_16x16x32_bf16 v[172:175], a[8:11], v[96:99], v[172:175]\n"
"s_add_u32 s97, s54, 0x40000\n"
"buffer_load_dwordx4 v[176:179], v0, s[4:7], s97 offen sc1\n"
"v_mfma_f32_16x16x32_bf16 v[172:175], a[12:15], v[104:107], v[172:175]\n"
"s_waitcnt vmcnt(9)\n"
"v_mfma_f32_16x16x32_bf16 v[172:175], a[16:19], v[112:115], v[172:175]\n"
"buffer_load_dword v255, v10, s[4:7], s98 offen sc1\n"
"v_mfma_f32_16x16x32_bf16 v[172:175], a[20:23], v[116:119], v[172:175]\n"
"v_add_f32_e32 v124, v40, v124\n"
"v_add_f32_e32 v125, v41, v125\n"
"v_mfma_f32_16x16x32_bf16 v[172:175], a[24:27], v[120:123], v[172:175]\n"
"v_add_f32_e32 v126, v42, v126\n"
"v_add_f32_e32 v127, v43, v127\n"
"v_mfma_f32_16x16x32_bf16 v[172:175], a[28:31], v[136:139], v[172:175]\n"
"buffer_load_dwordx4 v[40:43], v0, s[4:7], s97 offen offset:1024 sc1\n"
"v_mfma_f32_16x16x32_bf16 v[180:183], a[60:63], v[136:139], 0\n"
"buffer_load_dword v254, v10, s[4:7], s98 offen offset:256 sc1\n"
"v_mfma_f32_16x16x32_bf16 v[180:183], a[56:59], v[120:123], v[180:183]\n"
"v_add_f32_e32 v32, v44, v32\n"
"v_add_f32_e32 v33, v45, v33\n"
"v_mfma_f32_16x16x32_bf16 v[180:183], a[52:55], v[116:119], v[180:183]\n"
"v_add_f32_e32 v34, v46, v34\n"
"v_add_f32_e32 v35, v47, v35\n"
"v_mfma_f32_16x16x32_bf16 v[180:183], a[48:51], v[112:115], v[180:183]\n"
"buffer_load_dword v253, v10, s[4:7], s98 offen offset:512 sc1\n"
"v_mfma_f32_16x16x32_bf16 v[180:183], a[44:47], v[104:107], v[180:183]\n"
"v_cmp_u_f32_e32 vcc, v172, v172\n"
"v_cvt_pk_bf16_f32 v44, v124, v125\n"
"v_mfma_f32_16x16x32_bf16 v[180:183], a[40:43], v[96:99], v[180:183]\n"
"s_cbranch_vccnz .Lmk_p76s\n"
".Lmk_p76r:\n"
"v_cvt_pk_bf16_f32 v45, v126, v127\n"
"v_mfma_f32_16x16x32_bf16 v[180:183], a[36:39], v[92:95], v[180:183]\n"
"buffer_load_dwordx4 v[124:127], v0, s[4:7], s97 offen offset:2048 sc1\n"
"v_mfma_f32_16x16x32_bf16 v[180:183], a[32:35], v[84:87], v[180:183]\n"
"ds_write_b128 v3, v[172:175]\n"
"v_mfma_f32_16x16x32_bf16 v[172:175], a[64:67], v[84:87], 0\n"
"buffer_load_dword v252, v10, s[4:7], s98 offen offset:768 sc1\n"
"v_mfma_f32_16x16x32_bf16 v[172:175], a[68:71], v[92:95], v[172:175]\n"
"buffer_load_dwordx4 v[184:187], v0, s[4:7], s97 offen offset:3072 sc1\n"
"v_mfma_f32_16x16x32_bf16 v[172:175], a[72:75], v[96:99], v[172:175]\n"
"v_cvt_pk_bf16_f32 v46, v32, v33\n"
"v_cvt_pk_bf16_f32 v47, v34, v35\n"
"v_mfma_f32_16x16x32_bf16 v[172:175], a[76:79], v[104:107], v[172:175]\n"
"buffer_load_dword v251, v10, s[4:7], s98 offen offset:1024 sc1\n"
"v_mfma_f32_16x16x32_bf16 v[172:175], a[80:83], v[112:115], v[172:175]\n"
"buffer_store_dwordx4 v[44:47], v0, s[4:7], s61 offen offset:1024 sc1\n"
"v_mfma_f32_16x16x32_bf16 v[172:175], a[84:87], v[116:119], v[172:175]\n"
"ds_write_b128 v3, v[180:183] offset:1024\n"
"v_mfma_f32_16x16x32_bf16 v[172:175], a[88:91], v[120:123], v[172:175]\n"
"buffer_load_dwordx4 v[32:35], v1, s[4:7], s97 offen sc1\n"
"v_mfma_f32_16x16x32_bf16 v[172:175], a[92:95], v[136:139], v[172:175]\n"
"buffer_load_dword v250, v10, s[4:7], s98 offen offset:1280 sc1\n"
"v_mfma_f32_16x16x32_bf16 v[180:183], a[124:127], v[136:139], 0\n"
"ds_write_b128 v2, v[44:47] offset:1024\n"
"v_mfma_f32_16x16x32_bf16 v[180:183], a[120:123], v[120:123], v[180:183]\n"
"v_mov_b32_e32 v188, 0x3f803f80\n"
"v_mov_b32_e32 v189, 0x3f803f80\n"
"v_mfma_f32_16x16x32_bf16 v[180:183], a[116:119], v[116:119], v[180:183]\n"
"buffer_load_dword v249, v10, s[4:7], s98 offen offset:1536 sc1\n"
"v_mfma_f32_16x16x32_bf16 v[180:183], a[112:115], v[112:115], v[180:183]\n"
"buffer_load_dwordx4 v[192:195], v1, s[4:7], s97 offen offset:1024 sc1\n"
"v_mfma_f32_16x16x32_bf16 v[180:183], a[108:111], v[104:107], v[180:183]\n"
"v_mov_b32_e32 v190, 0x3f803f80\n"
"v_mov_b32_e32 v191, 0x3f803f80\n"
"v_mfma_f32_16x16x32_bf16 v[180:183], a[104:107], v[96:99], v[180:183]\n"
"ds_write_b128 v3, v[172:175] offset:2048\n"
"v_mfma_f32_16x16x32_bf16 v[172:175], v[188:191], v[68:71], 0\n"
"v_mfma_f32_16x16x32_bf16 v[172:175], v[188:191], v[44:47], v[172:175]\n"
"v_mfma_f32_16x16x32_bf16 v[188:191], v[68:71], v[68:71], 0\n"
"v_mfma_f32_16x16x32_bf16 v[188:191], v[44:47], v[44:47], v[188:191]\n"
"v_mfma_f32_16x16x32_bf16 v[180:183], a[100:103], v[92:95], v[180:183]\n"
"v_mov_b32_e32 v44, -1\n"
"v_mov_b32_e32 v45, -1\n"
"v_mfma_f32_16x16x32_bf16 v[180:183], a[96:99], v[84:87], v[180:183]\n"
"v_mov_b32_e32 v46, -1\n"
"v_mov_b32_e32 v47, -1\n"
"v_mfma_f32_16x16x32_bf16 v[68:71], v[24:27], v[84:87], 0\n"
"buffer_load_dwordx4 v[196:199], v1, s[4:7], s97 offen offset:2048 sc1\n"
"v_mfma_f32_16x16x32_bf16 v[68:71], v[56:59], v[92:95], v[68:71]\n"
"s_add_u32 s96, s64, 0x4000\n"
"buffer_store_dwordx4 v[44:47], v2, s[4:7], s96 offen sc1\n"
"v_mfma_f32_16x16x32_bf16 v[68:71], v[28:31], v[96:99], v[68:71]\n"
"buffer_load_dword v248, v10, s[4:7], s98 offen offset:1792 sc1\n"
"v_mfma_f32_16x16x32_bf16 v[68:71], v[64:67], v[104:107], v[68:71]\n"
"s_mov_b32 s94, 0xaaaaaaaa\n"
"s_mov_b32 s95, 0xaaaaaaaa\n"
"v_mfma_f32_16x16x32_bf16 v[68:71], v[52:55], v[112:115], v[68:71]\n"
"v_mfma_f32_16x16x32_bf16 v[68:71], v[36:39], v[116:119], v[68:71]\n"
"buffer_load_dwordx4 v[200:203], v1, s[4:7], s97 offen offset:3072 sc1\n"
"v_mfma_f32_16x16x32_bf16 v[68:71], v[108:111], v[120:123], v[68:71]\n"
"ds_write_b128 v3, v[180:183] offset:3072\n"
"v_mfma_f32_16x16x32_bf16 v[68:71], v[60:63], v[136:139], v[68:71]\n"
"v_cndmask_b32_e64 v188, v188, v189, s[94:95]\n"
"v_cndmask_b32_e64 v190, v190, v191, s[94:95]\n"
"v_mfma_f32_16x16x32_bf16 v[180:183], v[80:83], v[136:139], 0\n"
"s_mov_b32 s94, 0xcccccccc\n"
"s_mov_b32 s95, 0xcccccccc\n"
"v_mfma_f32_16x16x32_bf16 v[180:183], v[88:91], v[120:123], v[180:183]\n"
"v_cndmask_b32_e64 v173, v188, v190, s[94:95]\n"
"v_mfma_f32_16x16x32_bf16 v[180:183], v[100:103], v[116:119], v[180:183]\n"
"s_mov_b32 s94, 0xf0000f\n"
"s_mov_b32 s95, 0xf0000f00\n"
"v_mfma_f32_16x16x32_bf16 v[180:183], v[76:79], v[112:115], v[180:183]\n"
"v_cndmask_b32_e64 v173, 0, v173, s[94:95]\n"
"v_mov_b32_e32 v247, v173\n"
"v_mfma_f32_16x16x32_bf16 v[180:183], v[132:135], v[104:107], v[180:183]\n"
"s_nop 0\n"
"v_permlane16_swap_b32_e32 v173, v247\n"
"v_add_f32_e32 v173, v173, v247\n"
"v_mfma_f32_16x16x32_bf16 v[180:183], v[128:131], v[96:99], v[180:183]\n"
"ds_write_b128 v3, v[68:71] offset:4096\n"
"v_mfma_f32_16x16x32_bf16 v[180:183], v[48:51], v[92:95], v[180:183]\n"
"v_mov_b32_e32 v247, v173\n"
"v_mfma_f32_16x16x32_bf16 v[180:183], v[72:75], v[84:87], v[180:183]\n"
"s_waitcnt vmcnt(22)\n"
"v_permlane32_swap_b32_e32 v173, v247\n"
"v_add_f32_e32 v173, v173, v247\n"
"; @A G1 0.1\n"
"v_mfma_f32_16x16x32_bf16 v[68:71], a[0:3], v[140:143], 0\n"
"s_mov_b32 s94, 0xffff0000\n"
"v_cndmask_b32_e64 v172, v172, v173, s[94:95]\n"
"v_mfma_f32_16x16x32_bf16 v[68:71], a[4:7], v[144:147], v[68:71]\n"
"buffer_store_dword v172, v14, s[4:7], s63 offen sc1\n"
"v_mfma_f32_16x16x32_bf16 v[68:71], a[8:11], v[148:151], v[68:71]\n"
"s_add_u32 s98, s55, 0x40000\n"
"buffer_load_dwordx4 v[84:87], v0, s[4:7], s98 offen sc1\n"
"v_mfma_f32_16x16x32_bf16 v[68:71], a[12:15], v[152:155], v[68:71]\n"
"s_waitcnt vmcnt(20)\n"
"v_mfma_f32_16x16x32_bf16 v[68:71], a[16:19], v[156:159], v[68:71]\n"
"v_mfma_f32_16x16x32_bf16 v[68:71], a[20:23], v[160:163], v[68:71]\n"
"ds_write_b128 v3, v[180:183] offset:5120\n"
"v_mfma_f32_16x16x32_bf16 v[68:71], a[24:27], v[164:167], v[68:71]\n"
"v_mfma_f32_16x16x32_bf16 v[68:71], a[28:31], v[168:171], v[68:71]\n"
"buffer_load_dwordx4 v[92:95], v0, s[4:7], s98 offen offset:1024 sc1\n"
"v_mfma_f32_16x16x32_bf16 v[96:99], a[60:63], v[168:171], 0\n"
"v_mfma_f32_16x16x32_bf16 v[96:99], a[56:59], v[164:167], v[96:99]\n"
"v_mfma_f32_16x16x32_bf16 v[96:99], a[52:55], v[160:163], v[96:99]\n"
"v_mfma_f32_16x16x32_bf16 v[96:99], a[48:51], v[156:159], v[96:99]\n"
"v_mfma_f32_16x16x32_bf16 v[96:99], a[44:47], v[152:155], v[96:99]\n"
"s_nop 1\n"
"v_cmp_u_f32_e32 vcc, v68, v68\n"
"v_mfma_f32_16x16x32_bf16 v[96:99], a[40:43], v[148:151], v[96:99]\n"
"s_cbranch_vccnz .Lmk_p77s\n"
".Lmk_p77r:\n"
"v_mfma_f32_16x16x32_bf16 v[96:99], a[36:39], v[144:147], v[96:99]\n"
"buffer_load_dwordx4 v[104:107], v0, s[4:7], s98 offen offset:2048 sc1\n"
"v_mfma_f32_16x16x32_bf16 v[96:99], a[32:35], v[140:143], v[96:99]\n"
"ds_write_b128 v3, v[68:71] offset:6144\n"
"v_mfma_f32_16x16x32_bf16 v[68:71], a[64:67], v[140:143], 0\n"
"v_mfma_f32_16x16x32_bf16 v[68:71], a[68:71], v[144:147], v[68:71]\n"
"buffer_load_dwordx4 v[112:115], v0, s[4:7], s98 offen offset:3072 sc1\n"
"v_mfma_f32_16x16x32_bf16 v[68:71], a[72:75], v[148:151], v[68:71]\n"
"v_mfma_f32_16x16x32_bf16 v[68:71], a[76:79], v[152:155], v[68:71]\n"
"v_mfma_f32_16x16x32_bf16 v[68:71], a[80:83], v[156:159], v[68:71]\n"
"v_mfma_f32_16x16x32_bf16 v[68:71], a[84:87], v[160:163], v[68:71]\n"
"ds_write_b128 v3, v[96:99] offset:7168\n"
"v_mfma_f32_16x16x32_bf16 v[68:71], a[88:91], v[164:167], v[68:71]\n"
"buffer_load_dwordx4 v[96:99], v1, s[4:7], s98 offen sc1\n"
"v_mfma_f32_16x16x32_bf16 v[68:71], a[92:95], v[168:171], v[68:71]\n"
"v_mfma_f32_16x16x32_bf16 v[116:119], a[124:127], v[168:171], 0\n"
"v_mfma_f32_16x16x32_bf16 v[116:119], a[120:123], v[164:167], v[116:119]\n"
"v_mfma_f32_16x16x32_bf16 v[116:119], a[116:119], v[160:163], v[116:119]\n"
"v_mfma_f32_16x16x32_bf16 v[116:119], a[112:115], v[156:159], v[116:119]\n"
"buffer_load_dwordx4 v[120:123], v1, s[4:7], s98 offen offset:1024 sc1\n"
"v_mfma_f32_16x16x32_bf16 v[116:119], a[108:111], v[152:155], v[116:119]\n"
"v_mfma_f32_16x16x32_bf16 v[116:119], a[104:107], v[148:151], v[116:119]\n"
"s_nop 0\n"
"ds_write_b128 v3, v[68:71] offset:8192\n"
"v_mfma_f32_16x16x32_bf16 v[116:119], a[100:103], v[144:147], v[116:119]\n"
"v_mfma_f32_16x16x32_bf16 v[116:119], a[96:99], v[140:143], v[116:119]\n"
"v_mfma_f32_16x16x32_bf16 v[68:71], v[24:27], v[140:143], 0\n"
"buffer_load_dwordx4 v[136:139], v1, s[4:7], s98 offen offset:2048 sc1\n"
"v_mfma_f32_16x16x32_bf16 v[68:71], v[56:59], v[144:147], v[68:71]\n"
"s_add_u32 s97, s64, 0x4000\n"
"v_mfma_f32_16x16x32_bf16 v[68:71], v[28:31], v[148:151], v[68:71]\n"
"v_mfma_f32_16x16x32_bf16 v[68:71], v[64:67], v[152:155], v[68:71]\n"
"buffer_store_dwordx4 v[44:47], v2, s[4:7], s97 offen offset:1024 sc1\n"
"v_mfma_f32_16x16x32_bf16 v[68:71], v[52:55], v[156:159], v[68:71]\n"
"s_waitcnt vmcnt(10)\n"
"v_add_f32_e32 v255, v255, v251\n"
"v_add_f32_e32 v254, v254, v250\n"
"v_mfma_f32_16x16x32_bf16 v[68:71], v[36:39], v[160:163], v[68:71]\n"
"buffer_load_dwordx4 v[172:175], v1, s[4:7], s98 offen offset:3072 sc1\n"
"v_mfma_f32_16x16x32_bf16 v[68:71], v[108:111], v[164:167], v[68:71]\n"
"ds_write_b128 v3, v[116:119] offset:9216\n"
"v_mfma_f32_16x16x32_bf16 v[68:71], v[60:63], v[168:171], v[68:71]\n"
"v_add_f32_e32 v253, v253, v249\n"
"v_add_f32_e32 v252, v252, v248\n"
"v_mfma_f32_16x16x32_bf16 v[116:119], v[80:83], v[168:171], 0\n"
"v_add_f32_e32 v255, v255, v253\n"
"v_add_f32_e32 v254, v254, v252\n"
"v_mfma_f32_16x16x32_bf16 v[116:119], v[88:91], v[164:167], v[116:119]\n"
"v_add_f32_e32 v255, v255, v254\n"
"v_cmp_u_f32_e32 vcc, v255, v255\n"
"v_mfma_f32_16x16x32_bf16 v[116:119], v[100:103], v[160:163], v[116:119]\n"
"s_cbranch_vccnz .Lmk_m80s\n"
".Lmk_m80r:\n"
"v_mov_b32_e32 v254, v255\n"
"v_mfma_f32_16x16x32_bf16 v[116:119], v[76:79], v[156:159], v[116:119]\n"
"s_nop 0\n"
"v_permlane32_swap_b32_e32 v255, v254\n"
"v_add_f32_e32 v255, v255, v254\n"
"v_mfma_f32_16x16x32_bf16 v[116:119], v[132:135], v[152:155], v[116:119]\n"
"v_mul_f32_e32 v255, 0x3a800000, v255\n"
"v_mov_b32_e32 v254, v255\n"
"v_mfma_f32_16x16x32_bf16 v[116:119], v[128:131], v[148:151], v[116:119]\n"
"ds_write_b128 v3, v[68:71] offset:10240\n"
"v_mfma_f32_16x16x32_bf16 v[116:119], v[48:51], v[144:147], v[116:119]\n"
"v_permlane16_swap_b32_e32 v255, v254\n"
"v_cvt_pk_bf16_f32 v253, -v255, 0\n"
"v_mfma_f32_16x16x32_bf16 v[116:119], v[72:75], v[140:143], v[116:119]\n"
"v_fma_f32 v254, -v255, v255, v254\n"
"v_lshlrev_b32_e32 v253, 16, v253\n"
"; @A G1 0.2\n"
"v_mfma_f32_16x16x32_bf16 v[68:71], a[0:3], v[176:179], 0\n"
"v_max_f32_e32 v254, 0, v254\n"
"v_cvt_pk_bf16_f32 v254, v254, 0\n"
"v_mfma_f32_16x16x32_bf16 v[68:71], a[4:7], v[40:43], v[68:71]\n"
"v_lshlrev_b32_e32 v254, 16, v254\n"
"v_add_f32_e32 v254, 0x3727c5ac, v254\n"
"v_mfma_f32_16x16x32_bf16 v[68:71], a[8:11], v[124:127], v[68:71]\n"
"v_cvt_pk_bf16_f32 v254, v254, 0\n"
"v_lshlrev_b32_e32 v254, 16, v254\n"
"v_mfma_f32_16x16x32_bf16 v[68:71], a[12:15], v[184:187], v[68:71]\n"
"s_waitcnt vmcnt(10)\n"
"v_sqrt_f32_e32 v254, v254\n"
"v_mfma_f32_16x16x32_bf16 v[68:71], a[16:19], v[32:35], v[68:71]\n"
"s_mov_b32 s98, .Lmk_a2-.Lmk_g75\n"
"s_getpc_b64 s[84:85]\n"
".Lmk_g75:\n"
"s_add_u32 s84, s84, s98\n"
"s_addc_u32 s85, s85, 0\n"
"s_and_b32 s84, s84, -16\n"
"v_mfma_f32_16x16x32_bf16 v[68:71], a[20:23], v[192:195], v[68:71]\n"
"ds_write_b128 v3, v[116:119] offset:11264\n"
"v_mfma_f32_16x16x32_bf16 v[68:71], a[24:27], v[196:199], v[68:71]\n"
"s_and_b32 s85, s85, 0xffff\n"
"s_sub_u32 s98, .Lmk_end-.Lmk_g75, s98\n"
"s_min_u32 s86, s98, 0x3000\n"
"s_mov_b32 s87, 0x20000\n"
"v_mfma_f32_16x16x32_bf16 v[68:71], a[28:31], v[200:203], v[68:71]\n"
"s_mov_b32 m0, 0x27800\n"
"s_nop 0\n"
"buffer_load_dwordx4 v0, s[84:87], s99 offen lds\n"
"v_mfma_f32_16x16x32_bf16 v[116:119], a[60:63], v[200:203], 0\n"
"v_cvt_pk_bf16_f32 v254, v254, 0\n"
"v_lshlrev_b32_e32 v254, 16, v254\n"
"v_mfma_f32_16x16x32_bf16 v[116:119], a[56:59], v[196:199], v[116:119]\n"
"v_rcp_f32_e32 v255, v254\n"
"v_mfma_f32_16x16x32_bf16 v[116:119], a[52:55], v[192:195], v[116:119]\n"
"s_add_u32 s98, s52, 0x48000\n"
"buffer_load_dwordx4 v[140:143], v0, s[4:7], s98 offen sc1\n"
"v_mfma_f32_16x16x32_bf16 v[116:119], a[48:51], v[32:35], v[116:119]\n"
"s_waitcnt lgkmcnt(0)\n"
"s_barrier\n"
"v_mfma_f32_16x16x32_bf16 v[116:119], a[44:47], v[184:187], v[116:119]\n"
"v_cmp_u_f32_e32 vcc, v68, v68\n"
"v_mul_f32_e32 v254, 0x3e8293ee, v255\n"
"v_mfma_f32_16x16x32_bf16 v[116:119], a[40:43], v[124:127], v[116:119]\n"
"s_cbranch_vccnz .Lmk_p78s\n"
".Lmk_p78r:\n"
"ds_read_b128 v[144:147], v4\n"
"v_mfma_f32_16x16x32_bf16 v[116:119], a[36:39], v[40:43], v[116:119]\n"
"ds_write_b128 v3, v[68:71] offset:12288\n"
"v_mfma_f32_16x16x32_bf16 v[116:119], a[32:35], v[176:179], v[116:119]\n"
"buffer_load_dwordx4 v[68:71], v0, s[4:7], s98 offen offset:1024 sc1\n"
"v_mfma_f32_16x16x32_bf16 v[148:151], a[64:67], v[176:179], 0\n"
"ds_read_b128 v[152:155], v9 offset:128\n"
"ds_read_b128 v[156:159], v5\n"
"v_mfma_f32_16x16x32_bf16 v[148:151], a[68:71], v[40:43], v[148:151]\n"
"v_mfma_f32_16x16x32_bf16 v[148:151], a[72:75], v[124:127], v[148:151]\n"
"v_mfma_f32_16x16x32_bf16 v[148:151], a[76:79], v[184:187], v[148:151]\n"
"ds_read_b128 v[160:163], v4 offset:1024\n"
"ds_read_b128 v[164:167], v9 offset:192\n"
"v_mfma_f32_16x16x32_bf16 v[148:151], a[80:83], v[32:35], v[148:151]\n"
"buffer_load_dwordx4 v[168:171], v0, s[4:7], s98 offen offset:2048 sc1\n"
"v_mfma_f32_16x16x32_bf16 v[148:151], a[84:87], v[192:195], v[148:151]\n"
"ds_write_b128 v3, v[116:119] offset:13312\n"
"v_mfma_f32_16x16x32_bf16 v[148:151], a[88:91], v[196:199], v[148:151]\n"
"ds_read_b128 v[116:119], v5 offset:1024\n"
"s_waitcnt lgkmcnt(4)\n"
"v_mfma_f32_16x16x32_bf16 v[148:151], a[92:95], v[200:203], v[148:151]\n"
"v_fmac_f32_e32 v144, v253, v152\n"
"v_fmac_f32_e32 v145, v253, v153\n"
"v_mfma_f32_16x16x32_bf16 v[180:183], a[124:127], v[200:203], 0\n"
"v_fmac_f32_e32 v146, v253, v154\n"
"v_fmac_f32_e32 v147, v253, v155\n"
"v_mfma_f32_16x16x32_bf16 v[180:183], a[120:123], v[196:199], v[180:183]\n"
"buffer_load_dwordx4 v[152:155], v0, s[4:7], s98 offen offset:3072 sc1\n"
"v_mfma_f32_16x16x32_bf16 v[180:183], a[116:119], v[192:195], v[180:183]\n"
"ds_read_b128 v[188:191], v4 offset:2048\n"
"ds_read_b128 v[204:207], v9 offset:256\n"
"v_mfma_f32_16x16x32_bf16 v[180:183], a[112:115], v[32:35], v[180:183]\n"
"ds_read_b128 v[208:211], v5 offset:2048\n"
"v_add_f32_e32 v144, v144, v156\n"
"v_mfma_f32_16x16x32_bf16 v[180:183], a[108:111], v[184:187], v[180:183]\n"
"v_add_f32_e32 v145, v145, v157\n"
"v_add_f32_e32 v146, v146, v158\n"
"v_mfma_f32_16x16x32_bf16 v[180:183], a[104:107], v[124:127], v[180:183]\n"
"ds_write_b128 v3, v[148:151] offset:14336\n"
"v_mfma_f32_16x16x32_bf16 v[180:183], a[100:103], v[40:43], v[180:183]\n"
"buffer_load_dwordx4 v[148:151], v1, s[4:7], s98 offen sc1\n"
"v_mfma_f32_16x16x32_bf16 v[180:183], a[96:99], v[176:179], v[180:183]\n"
"s_waitcnt lgkmcnt(4)\n"
"v_add_f32_e32 v147, v147, v159\n"
"v_fmac_f32_e32 v160, v253, v164\n"
"v_mfma_f32_16x16x32_bf16 v[156:159], v[24:27], v[176:179], 0\n"
"v_fmac_f32_e32 v161, v253, v165\n"
"v_fmac_f32_e32 v162, v253, v166\n"
"v_mfma_f32_16x16x32_bf16 v[156:159], v[56:59], v[40:43], v[156:159]\n"
"ds_read_b128 v[212:215], v4 offset:3072\n"
"ds_read_b128 v[216:219], v9 offset:320\n"
"v_mfma_f32_16x16x32_bf16 v[156:159], v[28:31], v[124:127], v[156:159]\n"
"ds_read_b128 v[220:223], v5 offset:3072\n"
"v_fmac_f32_e32 v163, v253, v167\n"
"v_mfma_f32_16x16x32_bf16 v[156:159], v[64:67], v[184:187], v[156:159]\n"
"buffer_load_dwordx4 v[164:167], v1, s[4:7], s98 offen offset:1024 sc1\n"
"v_mfma_f32_16x16x32_bf16 v[156:159], v[52:55], v[32:35], v[156:159]\n"
"s_add_u32 s97, s64, 0x8000\n"
"v_add_f32_e32 v160, v160, v116\n"
"v_add_f32_e32 v161, v161, v117\n"
"v_mfma_f32_16x16x32_bf16 v[156:159], v[36:39], v[192:195], v[156:159]\n"
"ds_write_b128 v3, v[180:183] offset:15360\n"
"v_mfma_f32_16x16x32_bf16 v[156:159], v[108:111], v[196:199], v[156:159]\n"
"buffer_store_dwordx4 v[44:47], v2, s[4:7], s97 offen sc1\n"
"v_mfma_f32_16x16x32_bf16 v[156:159], v[60:63], v[200:203], v[156:159]\n"
"s_waitcnt lgkmcnt(5)\n"
"v_add_f32_e32 v162, v162, v118\n"
"v_add_f32_e32 v163, v163, v119\n"
"v_mfma_f32_16x16x32_bf16 v[116:119], v[80:83], v[200:203], 0\n"
"buffer_load_dwordx4 v[180:183], v1, s[4:7], s98 offen offset:2048 sc1\n"
"v_mfma_f32_16x16x32_bf16 v[116:119], v[88:91], v[196:199], v[116:119]\n"
"s_add_u32 s97, s68, s80\n"
"s_waitcnt vmcnt(18)\n"
"ds_read_b128 v[196:199], v4 offset:4096\n"
"v_mfma_f32_16x16x32_bf16 v[116:119], v[100:103], v[192:195], v[116:119]\n"
"buffer_store_dwordx2 v[18:19], v11, s[4:7], s97 offen sc1\n"
"v_mfma_f32_16x16x32_bf16 v[116:119], v[76:79], v[32:35], v[116:119]\n"
"ds_read_b128 v[32:35], v9\n"
"ds_read_b128 v[192:195], v5 offset:4096\n"
"v_mfma_f32_16x16x32_bf16 v[116:119], v[132:135], v[184:187], v[116:119]\n"
"v_fmac_f32_e32 v188, v253, v204\n"
"v_fmac_f32_e32 v189, v253, v205\n"
"v_mfma_f32_16x16x32_bf16 v[116:119], v[128:131], v[124:127], v[116:119]\n"
"buffer_load_dwordx4 v[124:127], v1, s[4:7], s98 offen offset:3072 sc1\n"
"v_mfma_f32_16x16x32_bf16 v[116:119], v[48:51], v[40:43], v[116:119]\n"
"ds_write_b128 v3, v[156:159] offset:16384\n"
"v_mfma_f32_16x16x32_bf16 v[116:119], v[72:75], v[176:179], v[116:119]\n"
"s_waitcnt vmcnt(16)\n"
"ds_read_b128 v[40:43], v4 offset:5120\n"
"ds_read_b128 v[156:159], v9 offset:64\n"
"; @A G1 0.3\n"
"v_mfma_f32_16x16x32_bf16 v[176:179], a[0:3], v[84:87], v[144:147]\n"
"ds_read_b128 v[184:187], v5 offset:5120\n"
"v_fmac_f32_e32 v190, v253, v206\n"
"v_mfma_f32_16x16x32_bf16 v[176:179], a[4:7], v[92:95], v[176:179]\n"
"v_fmac_f32_e32 v191, v253, v207\n"
"v_add_f32_e32 v188, v188, v208\n"
"v_mfma_f32_16x16x32_bf16 v[176:179], a[8:11], v[104:107], v[176:179]\n"
"s_add_u32 s98, s53, 0x48000\n"
"buffer_load_dwordx4 v[200:203], v0, s[4:7], s98 offen sc1\n"
"v_mfma_f32_16x16x32_bf16 v[176:179], a[12:15], v[112:115], v[176:179]\n"
"s_waitcnt vmcnt(12)\n"
"v_add_f32_e32 v189, v189, v209\n"
"v_add_f32_e32 v190, v190, v210\n"
"v_mfma_f32_16x16x32_bf16 v[176:179], a[16:19], v[96:99], v[176:179]\n"
"s_waitcnt lgkmcnt(8)\n"
"v_add_f32_e32 v191, v191, v211\n"
"v_fmac_f32_e32 v212, v253, v216\n"
"v_mfma_f32_16x16x32_bf16 v[176:179], a[20:23], v[120:123], v[176:179]\n"
"ds_write_b128 v3, v[116:119] offset:17408\n"
"v_mfma_f32_16x16x32_bf16 v[176:179], a[24:27], v[136:139], v[176:179]\n"
"v_fmac_f32_e32 v213, v253, v217\n"
"v_fmac_f32_e32 v214, v253, v218\n"
"v_mfma_f32_16x16x32_bf16 v[176:179], a[28:31], v[172:175], v[176:179]\n"
"buffer_load_dwordx4 v[116:119], v0, s[4:7], s98 offen offset:1024 sc1\n"
"v_mfma_f32_16x16x32_bf16 v[204:207], a[60:63], v[172:175], v[160:163]\n"
"v_fmac_f32_e32 v215, v253, v219\n"
"v_add_f32_e32 v212, v212, v220\n"
"v_mfma_f32_16x16x32_bf16 v[204:207], a[56:59], v[136:139], v[204:207]\n"
"v_add_f32_e32 v213, v213, v221\n"
"v_add_f32_e32 v214, v214, v222\n"
"v_mfma_f32_16x16x32_bf16 v[204:207], a[52:55], v[120:123], v[204:207]\n"
"s_waitcnt lgkmcnt(0)\n"
"s_barrier\n"
"v_mfma_f32_16x16x32_bf16 v[204:207], a[48:51], v[96:99], v[204:207]\n"
"ds_read_b128 v[208:211], v6\n"
"ds_read_b128 v[216:219], v6 offset:1024\n"
"v_mfma_f32_16x16x32_bf16 v[204:207], a[44:47], v[112:115], v[204:207]\n"
"v_cmp_u_f32_e32 vcc, v176, v176\n"
"v_add_f32_e32 v215, v215, v223\n"
"v_mfma_f32_16x16x32_bf16 v[204:207], a[40:43], v[104:107], v[204:207]\n"
"s_cbranch_vccnz .Lmk_p79s\n"
".Lmk_p79r:\n"
"v_fmac_f32_e32 v196, v253, v32\n"
"v_mfma_f32_16x16x32_bf16 v[204:207], a[36:39], v[92:95], v[204:207]\n"
"buffer_load_dwordx4 v[144:147], v0, s[4:7], s98 offen offset:2048 sc1\n"
"v_mfma_f32_16x16x32_bf16 v[204:207], a[32:35], v[84:87], v[204:207]\n"
"v_fmac_f32_e32 v197, v253, v33\n"
"v_fmac_f32_e32 v198, v253, v34\n"
"v_mfma_f32_16x16x32_bf16 v[188:191], a[64:67], v[84:87], v[188:191]\n"
"v_fmac_f32_e32 v199, v253, v35\n"
"v_add_f32_e32 v196, v196, v192\n"
"v_mfma_f32_16x16x32_bf16 v[188:191], a[68:71], v[92:95], v[188:191]\n"
"buffer_load_dwordx4 v[32:35], v0, s[4:7], s98 offen offset:3072 sc1\n"
"v_mfma_f32_16x16x32_bf16 v[188:191], a[72:75], v[104:107], v[188:191]\n"
"v_add_f32_e32 v197, v197, v193\n"
"v_add_f32_e32 v198, v198, v194\n"
"v_mfma_f32_16x16x32_bf16 v[188:191], a[76:79], v[112:115], v[188:191]\n"
"v_add_f32_e32 v199, v199, v195\n"
"v_fmac_f32_e32 v40, v253, v156\n"
"v_mfma_f32_16x16x32_bf16 v[188:191], a[80:83], v[96:99], v[188:191]\n"
"s_waitcnt lgkmcnt(0)\n"
"v_fmac_f32_e32 v41, v253, v157\n"
"v_fmac_f32_e32 v42, v253, v158\n"
"v_mfma_f32_16x16x32_bf16 v[188:191], a[84:87], v[120:123], v[188:191]\n"
"v_fmac_f32_e32 v43, v253, v159\n"
"v_add_f32_e32 v40, v40, v184\n"
"v_mfma_f32_16x16x32_bf16 v[188:191], a[88:91], v[136:139], v[188:191]\n"
"buffer_load_dwordx4 v[156:159], v1, s[4:7], s98 offen sc1\n"
"v_mfma_f32_16x16x32_bf16 v[188:191], a[92:95], v[172:175], v[188:191]\n"
"ds_read_b128 v[160:163], v6 offset:2048\n"
"ds_read_b128 v[192:195], v6 offset:3072\n"
"v_mfma_f32_16x16x32_bf16 v[212:215], a[124:127], v[172:175], v[212:215]\n"
"v_add_f32_e32 v41, v41, v185\n"
"v_add_f32_e32 v42, v42, v186\n"
"v_mfma_f32_16x16x32_bf16 v[212:215], a[120:123], v[136:139], v[212:215]\n"
"v_add_f32_e32 v43, v43, v187\n"
"v_add_f32_e32 v176, v176, v208\n"
"v_mfma_f32_16x16x32_bf16 v[212:215], a[116:119], v[120:123], v[212:215]\n"
"v_add_f32_e32 v177, v177, v209\n"
"v_add_f32_e32 v178, v178, v210\n"
"v_mfma_f32_16x16x32_bf16 v[212:215], a[112:115], v[96:99], v[212:215]\n"
"buffer_load_dwordx4 v[184:187], v1, s[4:7], s98 offen offset:1024 sc1\n"
"v_mfma_f32_16x16x32_bf16 v[212:215], a[108:111], v[112:115], v[212:215]\n"
"v_add_f32_e32 v179, v179, v211\n"
"v_add_f32_e32 v204, v204, v216\n"
"v_mfma_f32_16x16x32_bf16 v[212:215], a[104:107], v[104:107], v[212:215]\n"
"v_add_f32_e32 v205, v205, v217\n"
"v_add_f32_e32 v206, v206, v218\n"
"v_mfma_f32_16x16x32_bf16 v[212:215], a[100:103], v[92:95], v[212:215]\n"
"v_add_f32_e32 v207, v207, v219\n"
"v_mul_f32_e32 v176, v176, v255\n"
"v_mfma_f32_16x16x32_bf16 v[212:215], a[96:99], v[84:87], v[212:215]\n"
"v_mul_f32_e32 v177, v177, v255\n"
"v_mul_f32_e32 v178, v178, v255\n"
"v_mfma_f32_16x16x32_bf16 v[196:199], v[24:27], v[84:87], v[196:199]\n"
"buffer_load_dwordx4 v[208:211], v1, s[4:7], s98 offen offset:2048 sc1\n"
"v_mfma_f32_16x16x32_bf16 v[196:199], v[56:59], v[92:95], v[196:199]\n"
"v_mul_f32_e32 v179, v179, v255\n"
"v_mul_f32_e32 v204, v204, v255\n"
"v_mfma_f32_16x16x32_bf16 v[196:199], v[28:31], v[104:107], v[196:199]\n"
"v_mul_f32_e32 v205, v205, v255\n"
"v_mul_f32_e32 v206, v206, v255\n"
"v_mfma_f32_16x16x32_bf16 v[196:199], v[64:67], v[112:115], v[196:199]\n"
"v_mul_f32_e32 v207, v207, v255\n"
"v_cvt_pk_bf16_f32 v216, v176, v177\n"
"v_mfma_f32_16x16x32_bf16 v[196:199], v[52:55], v[96:99], v[196:199]\n"
"s_waitcnt lgkmcnt(0)\n"
"v_cvt_pk_bf16_f32 v217, v178, v179\n"
"v_cvt_pk_bf16_f32 v218, v204, v205\n"
"v_mfma_f32_16x16x32_bf16 v[196:199], v[36:39], v[120:123], v[196:199]\n"
"buffer_load_dwordx4 v[176:179], v1, s[4:7], s98 offen offset:3072 sc1\n"
"v_mfma_f32_16x16x32_bf16 v[196:199], v[108:111], v[136:139], v[196:199]\n"
"s_add_u32 s98, s64, 0x8000\n"
"ds_read_b128 v[220:223], v6 offset:4096\n"
"ds_read_b128 v[224:227], v6 offset:5120\n"
"v_mfma_f32_16x16x32_bf16 v[196:199], v[60:63], v[172:175], v[196:199]\n"
"v_cvt_pk_bf16_f32 v219, v206, v207\n"
"v_add_f32_e32 v188, v188, v160\n"
"v_mfma_f32_16x16x32_bf16 v[40:43], v[80:83], v[172:175], v[40:43]\n"
"buffer_store_dwordx4 v[44:47], v2, s[4:7], s98 offen offset:1024 sc1\n"
"v_mfma_f32_16x16x32_bf16 v[40:43], v[88:91], v[136:139], v[40:43]\n"
"ds_write_b128 v7, v[216:219] offset:24576\n"
"v_mfma_f32_16x16x32_bf16 v[40:43], v[100:103], v[120:123], v[40:43]\n"
"v_add_f32_e32 v189, v189, v161\n"
"v_add_f32_e32 v190, v190, v162\n"
"v_mfma_f32_16x16x32_bf16 v[40:43], v[76:79], v[96:99], v[40:43]\n"
"v_add_f32_e32 v191, v191, v163\n"
"v_add_f32_e32 v212, v212, v192\n"
"v_mfma_f32_16x16x32_bf16 v[40:43], v[132:135], v[112:115], v[40:43]\n"
"v_add_f32_e32 v213, v213, v193\n"
"v_add_f32_e32 v214, v214, v194\n"
"v_mfma_f32_16x16x32_bf16 v[40:43], v[128:131], v[104:107], v[40:43]\n"
"v_add_f32_e32 v215, v215, v195\n"
"v_mul_f32_e32 v188, v188, v255\n"
"v_mfma_f32_16x16x32_bf16 v[40:43], v[48:51], v[92:95], v[40:43]\n"
"v_mul_f32_e32 v189, v189, v255\n"
"v_mul_f32_e32 v190, v190, v255\n"
"v_mfma_f32_16x16x32_bf16 v[40:43], v[72:75], v[84:87], v[40:43]\n"
"v_mul_f32_e32 v191, v191, v255\n"
"v_mul_f32_e32 v212, v212, v255\n"
"v_mul_f32_e32 v213, v213, v255\n"
"v_mul_f32_e32 v214, v214, v255\n"
"v_mul_f32_e32 v215, v215, v255\n"
"v_cvt_pk_bf16_f32 v44, v188, v189\n"
"v_cvt_pk_bf16_f32 v45, v190, v191\n"
"v_cvt_pk_bf16_f32 v46, v212, v213\n"
"v_cvt_pk_bf16_f32 v47, v214, v215\n"
"ds_write_b128 v7, v[44:47] offset:57344\n"
".Lmk_a1:\n"
"s_waitcnt vmcnt(15)\n"
"; @A G1 1.0\n"
"v_mfma_f32_16x16x32_bf16 v[84:87], a[0:3], v[140:143], 0\n"
"v_mfma_f32_16x16x32_bf16 v[84:87], a[4:7], v[68:71], v[84:87]\n"
"s_add_u32 s98, s62, 0x4800\n"
"v_mfma_f32_16x16x32_bf16 v[84:87], a[8:11], v[168:171], v[84:87]\n"
"s_add_u32 s97, s54, 0x48000\n"
"buffer_load_dwordx4 v[92:95], v0, s[4:7], s97 offen sc1\n"
"v_mfma_f32_16x16x32_bf16 v[84:87], a[12:15], v[152:155], v[84:87]\n"
"s_waitcnt vmcnt(10)\n"
"v_mfma_f32_16x16x32_bf16 v[84:87], a[16:19], v[148:151], v[84:87]\n"
"buffer_load_dword v255, v10, s[4:7], s98 offen sc1\n"
"v_mfma_f32_16x16x32_bf16 v[84:87], a[20:23], v[164:167], v[84:87]\n"
"s_waitcnt lgkmcnt(2)\n"
"v_add_f32_e32 v196, v196, v220\n"
"v_add_f32_e32 v197, v197, v221\n"
"v_mfma_f32_16x16x32_bf16 v[84:87], a[24:27], v[180:183], v[84:87]\n"
"v_add_f32_e32 v198, v198, v222\n"
"v_add_f32_e32 v199, v199, v223\n"
"v_mfma_f32_16x16x32_bf16 v[84:87], a[28:31], v[124:127], v[84:87]\n"
"buffer_load_dwordx4 v[96:99], v0, s[4:7], s97 offen offset:1024 sc1\n"
"v_mfma_f32_16x16x32_bf16 v[104:107], a[60:63], v[124:127], 0\n"
"s_add_u32 s96, s70, 0\n"
"buffer_load_dwordx4 a[128:131], v0, s[4:7], s96 offen sc1\n"
"v_mfma_f32_16x16x32_bf16 v[104:107], a[56:59], v[180:183], v[104:107]\n"
"buffer_load_dword v253, v10, s[4:7], s98 offen offset:256 sc1\n"
"v_mfma_f32_16x16x32_bf16 v[104:107], a[52:55], v[164:167], v[104:107]\n"
"v_add_f32_e32 v40, v40, v224\n"
"v_add_f32_e32 v41, v41, v225\n"
"v_mfma_f32_16x16x32_bf16 v[104:107], a[48:51], v[148:151], v[104:107]\n"
"v_add_f32_e32 v42, v42, v226\n"
"v_add_f32_e32 v43, v43, v227\n"
"v_mfma_f32_16x16x32_bf16 v[104:107], a[44:47], v[152:155], v[104:107]\n"
"v_cmp_u_f32_e32 vcc, v84, v84\n"
"v_mul_f32_e32 v196, v196, v254\n"
"v_mfma_f32_16x16x32_bf16 v[104:107], a[40:43], v[168:171], v[104:107]\n"
"s_cbranch_vccnz .Lmk_p81s\n"
".Lmk_p81r:\n"
"v_mul_f32_e32 v197, v197, v254\n"
"v_mfma_f32_16x16x32_bf16 v[104:107], a[36:39], v[68:71], v[104:107]\n"
"buffer_load_dwordx4 v[112:115], v0, s[4:7], s97 offen offset:2048 sc1\n"
"v_mfma_f32_16x16x32_bf16 v[104:107], a[32:35], v[140:143], v[104:107]\n"
"ds_write_b128 v3, v[84:87]\n"
"v_mfma_f32_16x16x32_bf16 v[84:87], a[64:67], v[140:143], 0\n"
"buffer_load_dword v252, v10, s[4:7], s98 offen offset:512 sc1\n"
"v_mfma_f32_16x16x32_bf16 v[84:87], a[68:71], v[68:71], v[84:87]\n"
"buffer_load_dwordx4 v[120:123], v0, s[4:7], s97 offen offset:3072 sc1\n"
"v_mfma_f32_16x16x32_bf16 v[84:87], a[72:75], v[168:171], v[84:87]\n"
"v_mul_f32_e32 v198, v198, v254\n"
"v_mul_f32_e32 v199, v199, v254\n"
"v_mfma_f32_16x16x32_bf16 v[84:87], a[76:79], v[152:155], v[84:87]\n"
"buffer_load_dword v251, v10, s[4:7], s98 offen offset:768 sc1\n"
"v_mfma_f32_16x16x32_bf16 v[84:87], a[80:83], v[148:151], v[84:87]\n"
"v_mul_f32_e32 v40, v40, v254\n"
"v_mul_f32_e32 v41, v41, v254\n"
"v_mfma_f32_16x16x32_bf16 v[84:87], a[84:87], v[164:167], v[84:87]\n"
"ds_write_b128 v3, v[104:107] offset:1024\n"
"v_mfma_f32_16x16x32_bf16 v[84:87], a[88:91], v[180:183], v[84:87]\n"
"buffer_load_dwordx4 v[104:107], v1, s[4:7], s97 offen sc1\n"
"v_mfma_f32_16x16x32_bf16 v[84:87], a[92:95], v[124:127], v[84:87]\n"
"buffer_load_dword v250, v10, s[4:7], s98 offen offset:1024 sc1\n"
"v_mfma_f32_16x16x32_bf16 v[136:139], a[124:127], v[124:127], 0\n"
"v_mul_f32_e32 v42, v42, v254\n"
"v_mul_f32_e32 v43, v43, v254\n"
"v_mfma_f32_16x16x32_bf16 v[136:139], a[120:123], v[180:183], v[136:139]\n"
"v_cvt_pk_bf16_f32 v160, v196, v197\n"
"v_cvt_pk_bf16_f32 v161, v198, v199\n"
"v_mfma_f32_16x16x32_bf16 v[136:139], a[116:119], v[164:167], v[136:139]\n"
"buffer_load_dword v254, v10, s[4:7], s98 offen offset:1280 sc1\n"
"v_mfma_f32_16x16x32_bf16 v[136:139], a[112:115], v[148:151], v[136:139]\n"
"buffer_load_dwordx4 v[172:175], v1, s[4:7], s97 offen offset:1024 sc1\n"
"v_mfma_f32_16x16x32_bf16 v[136:139], a[108:111], v[152:155], v[136:139]\n"
"buffer_load_dwordx4 a[132:135], v0, s[4:7], s96 offen offset:1024 sc1\n"
"v_mfma_f32_16x16x32_bf16 v[136:139], a[104:107], v[168:171], v[136:139]\n"
"ds_write_b128 v3, v[84:87] offset:2048\n"
"v_mfma_f32_16x16x32_bf16 v[136:139], a[100:103], v[68:71], v[136:139]\n"
"buffer_load_dword v249, v10, s[4:7], s98 offen offset:1536 sc1\n"
"v_mfma_f32_16x16x32_bf16 v[136:139], a[96:99], v[140:143], v[136:139]\n"
"v_cvt_pk_bf16_f32 v162, v40, v41\n"
"v_cvt_pk_bf16_f32 v163, v42, v43\n"
"v_mfma_f32_16x16x32_bf16 v[40:43], v[24:27], v[140:143], 0\n"
"buffer_load_dwordx4 v[84:87], v1, s[4:7], s97 offen offset:2048 sc1\n"
"v_mfma_f32_16x16x32_bf16 v[40:43], v[56:59], v[68:71], v[40:43]\n"
"buffer_load_dword v248, v10, s[4:7], s98 offen offset:1792 sc1\n"
"v_mfma_f32_16x16x32_bf16 v[40:43], v[28:31], v[168:171], v[40:43]\n"
"ds_write_b128 v2, v[160:163] offset:49152\n"
"v_mfma_f32_16x16x32_bf16 v[40:43], v[64:67], v[152:155], v[40:43]\n"
"v_mfma_f32_16x16x32_bf16 v[40:43], v[52:55], v[148:151], v[40:43]\n"
"v_mfma_f32_16x16x32_bf16 v[40:43], v[36:39], v[164:167], v[40:43]\n"
"buffer_load_dwordx4 v[160:163], v1, s[4:7], s97 offen offset:3072 sc1\n"
"v_mfma_f32_16x16x32_bf16 v[40:43], v[108:111], v[180:183], v[40:43]\n"
"ds_write_b128 v3, v[136:139] offset:3072\n"
"v_mfma_f32_16x16x32_bf16 v[40:43], v[60:63], v[124:127], v[40:43]\n"
"v_mfma_f32_16x16x32_bf16 v[136:139], v[80:83], v[124:127], 0\n"
"v_mfma_f32_16x16x32_bf16 v[136:139], v[88:91], v[180:183], v[136:139]\n"
"v_mfma_f32_16x16x32_bf16 v[136:139], v[100:103], v[164:167], v[136:139]\n"
"v_mfma_f32_16x16x32_bf16 v[136:139], v[76:79], v[148:151], v[136:139]\n"
"v_mfma_f32_16x16x32_bf16 v[136:139], v[132:135], v[152:155], v[136:139]\n"
"v_mfma_f32_16x16x32_bf16 v[136:139], v[128:131], v[168:171], v[136:139]\n"
"s_nop 1\n"
"ds_write_b128 v3, v[40:43] offset:4096\n"
"v_mfma_f32_16x16x32_bf16 v[136:139], v[48:51], v[68:71], v[136:139]\n"
"v_mfma_f32_16x16x32_bf16 v[136:139], v[72:75], v[140:143], v[136:139]\n"
"s_waitcnt vmcnt(23)\n"
"; @A G1 1.1\n"
"v_mfma_f32_16x16x32_bf16 v[40:43], a[0:3], v[200:203], 0\n"
"buffer_load_dwordx4 a[136:139], v0, s[4:7], s96 offen offset:2048 sc1\n"
"v_mfma_f32_16x16x32_bf16 v[40:43], a[4:7], v[116:119], v[40:43]\n"
"v_mfma_f32_16x16x32_bf16 v[40:43], a[8:11], v[144:147], v[40:43]\n"
"s_add_u32 s98, s55, 0x48000\n"
"buffer_load_dwordx4 v[68:71], v0, s[4:7], s98 offen sc1\n"
"v_mfma_f32_16x16x32_bf16 v[40:43], a[12:15], v[32:35], v[40:43]\n"
"s_waitcnt vmcnt(21)\n"
"v_mfma_f32_16x16x32_bf16 v[40:43], a[16:19], v[156:159], v[40:43]\n"
"v_mfma_f32_16x16x32_bf16 v[40:43], a[20:23], v[184:187], v[40:43]\n"
"ds_write_b128 v3, v[136:139] offset:5120\n"
"v_mfma_f32_16x16x32_bf16 v[40:43], a[24:27], v[208:211], v[40:43]\n"
"v_mfma_f32_16x16x32_bf16 v[40:43], a[28:31], v[176:179], v[40:43]\n"
"buffer_load_dwordx4 v[124:127], v0, s[4:7], s98 offen offset:1024 sc1\n"
"v_mfma_f32_16x16x32_bf16 v[136:139], a[60:63], v[176:179], 0\n"
"v_mfma_f32_16x16x32_bf16 v[136:139], a[56:59], v[208:211], v[136:139]\n"
"v_mfma_f32_16x16x32_bf16 v[136:139], a[52:55], v[184:187], v[136:139]\n"
"v_mfma_f32_16x16x32_bf16 v[136:139], a[48:51], v[156:159], v[136:139]\n"
"v_mfma_f32_16x16x32_bf16 v[136:139], a[44:47], v[32:35], v[136:139]\n"
"s_add_u32 s97, s67, 0\n"
"s_nop 0\n"
"v_cmp_u_f32_e32 vcc, v40, v40\n"
"v_mfma_f32_16x16x32_bf16 v[136:139], a[40:43], v[144:147], v[136:139]\n"
"s_cbranch_vccnz .Lmk_p82s\n"
".Lmk_p82r:\n"
"v_mfma_f32_16x16x32_bf16 v[136:139], a[36:39], v[116:119], v[136:139]\n"
"buffer_load_dwordx4 v[140:143], v0, s[4:7], s98 offen offset:2048 sc1\n"
"s_add_u32 s95, s64, 0x2000\n"
"v_mfma_f32_16x16x32_bf16 v[136:139], a[32:35], v[200:203], v[136:139]\n"
"ds_write_b128 v3, v[40:43] offset:6144\n"
"v_mfma_f32_16x16x32_bf16 v[40:43], a[64:67], v[200:203], 0\n"
"buffer_load_dwordx2 v[246:247], v12, s[4:7], s97 offen sc1\n"
"v_mfma_f32_16x16x32_bf16 v[40:43], a[68:71], v[116:119], v[40:43]\n"
"buffer_load_dwordx4 v[148:151], v0, s[4:7], s98 offen offset:3072 sc1\n"
"v_mfma_f32_16x16x32_bf16 v[40:43], a[72:75], v[144:147], v[40:43]\n"
"buffer_store_dwordx4 v[216:219], v7, s[4:7], s95 offen sc1\n"
"v_mfma_f32_16x16x32_bf16 v[40:43], a[76:79], v[32:35], v[40:43]\n"
"v_mfma_f32_16x16x32_bf16 v[40:43], a[80:83], v[156:159], v[40:43]\n"
"buffer_load_dwordx4 a[140:143], v0, s[4:7], s96 offen offset:3072 sc1\n"
"v_mfma_f32_16x16x32_bf16 v[40:43], a[84:87], v[184:187], v[40:43]\n"
"ds_write_b128 v3, v[136:139] offset:7168\n"
"v_mfma_f32_16x16x32_bf16 v[40:43], a[88:91], v[208:211], v[40:43]\n"
"buffer_load_dwordx4 v[136:139], v1, s[4:7], s98 offen sc1\n"
"v_mfma_f32_16x16x32_bf16 v[40:43], a[92:95], v[176:179], v[40:43]\n"
"v_mfma_f32_16x16x32_bf16 v[152:155], a[124:127], v[176:179], 0\n"
"v_mfma_f32_16x16x32_bf16 v[152:155], a[120:123], v[208:211], v[152:155]\n"
"v_mfma_f32_16x16x32_bf16 v[152:155], a[116:119], v[184:187], v[152:155]\n"
"v_mfma_f32_16x16x32_bf16 v[152:155], a[112:115], v[156:159], v[152:155]\n"
"buffer_load_dwordx4 v[164:167], v1, s[4:7], s98 offen offset:1024 sc1\n"
"v_mfma_f32_16x16x32_bf16 v[152:155], a[108:111], v[32:35], v[152:155]\n"
"v_mfma_f32_16x16x32_bf16 v[152:155], a[104:107], v[144:147], v[152:155]\n"
"s_nop 0\n"
"ds_write_b128 v3, v[40:43] offset:8192\n"
"v_mfma_f32_16x16x32_bf16 v[152:155], a[100:103], v[116:119], v[152:155]\n"
"v_mfma_f32_16x16x32_bf16 v[152:155], a[96:99], v[200:203], v[152:155]\n"
"v_mfma_f32_16x16x32_bf16 v[40:43], v[24:27], v[200:203], 0\n"
"buffer_load_dwordx4 v[168:171], v1, s[4:7], s98 offen offset:2048 sc1\n"
"v_mfma_f32_16x16x32_bf16 v[40:43], v[56:59], v[116:119], v[40:43]\n"
"v_mfma_f32_16x16x32_bf16 v[40:43], v[28:31], v[144:147], v[40:43]\n"
"v_mfma_f32_16x16x32_bf16 v[40:43], v[64:67], v[32:35], v[40:43]\n"
"v_mfma_f32_16x16x32_bf16 v[40:43], v[52:55], v[156:159], v[40:43]\n"
"s_waitcnt vmcnt(12)\n"
"v_add_f32_e32 v255, v255, v250\n"
"v_add_f32_e32 v253, v253, v254\n"
"v_mfma_f32_16x16x32_bf16 v[40:43], v[36:39], v[184:187], v[40:43]\n"
"buffer_load_dwordx4 v[180:183], v1, s[4:7], s98 offen offset:3072 sc1\n"
"v_mfma_f32_16x16x32_bf16 v[40:43], v[108:111], v[208:211], v[40:43]\n"
"ds_write_b128 v3, v[152:155] offset:9216\n"
"v_mfma_f32_16x16x32_bf16 v[40:43], v[60:63], v[176:179], v[40:43]\n"
"v_add_f32_e32 v252, v252, v249\n"
"v_add_f32_e32 v251, v251, v248\n"
"v_mfma_f32_16x16x32_bf16 v[152:155], v[80:83], v[176:179], 0\n"
"buffer_load_dwordx4 a[144:147], v1, s[4:7], s96 offen sc1\n"
"s_add_u32 s98, s64, 0x6000\n"
"v_mfma_f32_16x16x32_bf16 v[152:155], v[88:91], v[208:211], v[152:155]\n"
"v_add_f32_e32 v255, v255, v252\n"
"v_add_f32_e32 v253, v253, v251\n"
"v_mfma_f32_16x16x32_bf16 v[152:155], v[100:103], v[184:187], v[152:155]\n"
"buffer_store_dwordx4 v[44:47], v7, s[4:7], s98 offen sc1\n"
"v_mfma_f32_16x16x32_bf16 v[152:155], v[76:79], v[156:159], v[152:155]\n"
"v_add_f32_e32 v255, v255, v253\n"
"v_cmp_u_f32_e32 vcc, v255, v255\n"
"v_mfma_f32_16x16x32_bf16 v[152:155], v[132:135], v[32:35], v[152:155]\n"
"s_cbranch_vccnz .Lmk_m85s\n"
".Lmk_m85r:\n"
"v_mov_b32_e32 v253, v255\n"
"v_mfma_f32_16x16x32_bf16 v[152:155], v[128:131], v[144:147], v[152:155]\n"
"ds_write_b128 v3, v[40:43] offset:10240\n"
"v_mfma_f32_16x16x32_bf16 v[152:155], v[48:51], v[116:119], v[152:155]\n"
"v_permlane32_swap_b32_e32 v255, v253\n"
"v_add_f32_e32 v255, v255, v253\n"
"v_mfma_f32_16x16x32_bf16 v[152:155], v[72:75], v[200:203], v[152:155]\n"
"v_mul_f32_e32 v255, 0x3a800000, v255\n"
"v_mov_b32_e32 v253, v255\n"
"; @A G1 1.2\n"
"v_mfma_f32_16x16x32_bf16 v[32:35], a[0:3], v[92:95], 0\n"
"s_nop 0\n"
"v_permlane16_swap_b32_e32 v255, v253\n"
"v_cvt_pk_bf16_f32 v254, -v255, 0\n"
"v_mfma_f32_16x16x32_bf16 v[32:35], a[4:7], v[96:99], v[32:35]\n"
"s_waitcnt vmcnt(9)\n"
"v_cmp_ne_u64_e32 vcc, v[246:247], v[18:19]\n"
"v_mfma_f32_16x16x32_bf16 v[32:35], a[8:11], v[112:115], v[32:35]\n"
"s_cbranch_vccnz .Lmk_f72s\n"
".Lmk_f72r:\n"
"v_fma_f32 v253, -v255, v255, v253\n"
"v_mfma_f32_16x16x32_bf16 v[32:35], a[12:15], v[120:123], v[32:35]\n"
"v_lshlrev_b32_e32 v254, 16, v254\n"
"v_max_f32_e32 v253, 0, v253\n"
"v_mfma_f32_16x16x32_bf16 v[32:35], a[16:19], v[104:107], v[32:35]\n"
"v_cvt_pk_bf16_f32 v253, v253, 0\n"
"v_lshlrev_b32_e32 v253, 16, v253\n"
"v_mfma_f32_16x16x32_bf16 v[32:35], a[20:23], v[172:175], v[32:35]\n"
"ds_write_b128 v3, v[152:155] offset:11264\n"
"v_mfma_f32_16x16x32_bf16 v[32:35], a[24:27], v[84:87], v[32:35]\n"
"v_add_f32_e32 v253, 0x3727c5ac, v253\n"
"v_cvt_pk_bf16_f32 v253, v253, 0\n"
"v_mfma_f32_16x16x32_bf16 v[32:35], a[28:31], v[160:163], v[32:35]\n"
"v_lshlrev_b32_e32 v253, 16, v253\n"
"v_mfma_f32_16x16x32_bf16 v[40:43], a[60:63], v[160:163], 0\n"
"v_sqrt_f32_e32 v253, v253\n"
"v_mfma_f32_16x16x32_bf16 v[40:43], a[56:59], v[84:87], v[40:43]\n"
"v_cvt_pk_bf16_f32 v253, v253, 0\n"
"v_lshlrev_b32_e32 v253, 16, v253\n"
"v_mfma_f32_16x16x32_bf16 v[40:43], a[52:55], v[172:175], v[40:43]\n"
"s_add_u32 s98, s52, 0x8000\n"
"buffer_load_dwordx4 v[44:47], v0, s[4:7], s98 offen sc1\n"
"v_mfma_f32_16x16x32_bf16 v[40:43], a[48:51], v[104:107], v[40:43]\n"
"s_waitcnt lgkmcnt(0)\n"
"s_barrier\n"
"v_mfma_f32_16x16x32_bf16 v[40:43], a[44:47], v[120:123], v[40:43]\n"
"ds_read_b128 v[116:119], v4\n"
"v_cmp_u_f32_e32 vcc, v32, v32\n"
"v_mfma_f32_16x16x32_bf16 v[40:43], a[40:43], v[112:115], v[40:43]\n"
"s_cbranch_vccnz .Lmk_p83s\n"
".Lmk_p83r:\n"
"ds_read_b128 v[144:147], v9 offset:128\n"
"v_mfma_f32_16x16x32_bf16 v[40:43], a[36:39], v[96:99], v[40:43]\n"
"ds_write_b128 v3, v[32:35] offset:12288\n"
"v_mfma_f32_16x16x32_bf16 v[40:43], a[32:35], v[92:95], v[40:43]\n"
"buffer_load_dwordx4 v[32:35], v0, s[4:7], s98 offen offset:1024 sc1\n"
"v_mfma_f32_16x16x32_bf16 v[152:155], a[64:67], v[92:95], 0\n"
"buffer_load_dwordx4 a[148:151], v1, s[4:7], s96 offen offset:1024 sc1\n"
"v_mfma_f32_16x16x32_bf16 v[152:155], a[68:71], v[96:99], v[152:155]\n"
"v_rcp_f32_e32 v255, v253\n"
"v_mfma_f32_16x16x32_bf16 v[152:155], a[72:75], v[112:115], v[152:155]\n"
"ds_read_b128 v[156:159], v5\n"
"v_mul_f32_e32 v253, 0x3e8293ee, v255\n"
"v_mfma_f32_16x16x32_bf16 v[152:155], a[76:79], v[120:123], v[152:155]\n"
"ds_read_b128 v[176:179], v4 offset:1024\n"
"ds_read_b128 v[184:187], v9 offset:192\n"
"v_mfma_f32_16x16x32_bf16 v[152:155], a[80:83], v[104:107], v[152:155]\n"
"buffer_load_dwordx4 v[188:191], v0, s[4:7], s98 offen offset:2048 sc1\n"
"v_mfma_f32_16x16x32_bf16 v[152:155], a[84:87], v[172:175], v[152:155]\n"
"ds_write_b128 v3, v[40:43] offset:13312\n"
"v_mfma_f32_16x16x32_bf16 v[152:155], a[88:91], v[84:87], v[152:155]\n"
"ds_read_b128 v[40:43], v5 offset:1024\n"
"v_mfma_f32_16x16x32_bf16 v[152:155], a[92:95], v[160:163], v[152:155]\n"
"s_waitcnt lgkmcnt(4)\n"
"v_fmac_f32_e32 v116, v254, v144\n"
"v_fmac_f32_e32 v117, v254, v145\n"
"v_mfma_f32_16x16x32_bf16 v[192:195], a[124:127], v[160:163], 0\n"
"v_fmac_f32_e32 v118, v254, v146\n"
"v_fmac_f32_e32 v119, v254, v147\n"
"v_mfma_f32_16x16x32_bf16 v[192:195], a[120:123], v[84:87], v[192:195]\n"
"buffer_load_dwordx4 v[144:147], v0, s[4:7], s98 offen offset:3072 sc1\n"
"v_mfma_f32_16x16x32_bf16 v[192:195], a[116:119], v[172:175], v[192:195]\n"
"ds_read_b128 v[196:199], v4 offset:2048\n"
"ds_read_b128 v[200:203], v9 offset:256\n"
"v_mfma_f32_16x16x32_bf16 v[192:195], a[112:115], v[104:107], v[192:195]\n"
"ds_read_b128 v[204:207], v5 offset:2048\n"
"v_add_f32_e32 v116, v116, v156\n"
"v_mfma_f32_16x16x32_bf16 v[192:195], a[108:111], v[120:123], v[192:195]\n"
"v_add_f32_e32 v117, v117, v157\n"
"v_add_f32_e32 v118, v118, v158\n"
"v_mfma_f32_16x16x32_bf16 v[192:195], a[104:107], v[112:115], v[192:195]\n"
"ds_write_b128 v3, v[152:155] offset:14336\n"
"v_mfma_f32_16x16x32_bf16 v[192:195], a[100:103], v[96:99], v[192:195]\n"
"buffer_load_dwordx4 v[152:155], v1, s[4:7], s98 offen sc1\n"
"v_mfma_f32_16x16x32_bf16 v[192:195], a[96:99], v[92:95], v[192:195]\n"
"s_waitcnt lgkmcnt(4)\n"
"v_add_f32_e32 v119, v119, v159\n"
"v_fmac_f32_e32 v176, v254, v184\n"
"v_mfma_f32_16x16x32_bf16 v[156:159], v[24:27], v[92:95], 0\n"
"v_fmac_f32_e32 v177, v254, v185\n"
"v_fmac_f32_e32 v178, v254, v186\n"
"v_mfma_f32_16x16x32_bf16 v[156:159], v[56:59], v[96:99], v[156:159]\n"
"ds_read_b128 v[208:211], v4 offset:3072\n"
"ds_read_b128 v[212:215], v9 offset:320\n"
"v_mfma_f32_16x16x32_bf16 v[156:159], v[28:31], v[112:115], v[156:159]\n"
"ds_read_b128 v[216:219], v5 offset:3072\n"
"v_fmac_f32_e32 v179, v254, v187\n"
"v_mfma_f32_16x16x32_bf16 v[156:159], v[64:67], v[120:123], v[156:159]\n"
"buffer_load_dwordx4 v[184:187], v1, s[4:7], s98 offen offset:1024 sc1\n"
"v_mfma_f32_16x16x32_bf16 v[156:159], v[52:55], v[104:107], v[156:159]\n"
"buffer_load_dwordx4 a[152:155], v1, s[4:7], s96 offen offset:2048 sc1\n"
"v_mfma_f32_16x16x32_bf16 v[156:159], v[36:39], v[172:175], v[156:159]\n"
"ds_write_b128 v3, v[192:195] offset:15360\n"
"v_mfma_f32_16x16x32_bf16 v[156:159], v[108:111], v[84:87], v[156:159]\n"
"v_add_f32_e32 v176, v176, v40\n"
"v_add_f32_e32 v177, v177, v41\n"
"v_mfma_f32_16x16x32_bf16 v[156:159], v[60:63], v[160:163], v[156:159]\n"
"s_waitcnt lgkmcnt(5)\n"
"v_add_f32_e32 v178, v178, v42\n"
"v_add_f32_e32 v179, v179, v43\n"
"v_mfma_f32_16x16x32_bf16 v[40:43], v[80:83], v[160:163], 0\n"
"buffer_load_dwordx4 v[160:163], v1, s[4:7], s98 offen offset:2048 sc1\n"
"v_mfma_f32_16x16x32_bf16 v[40:43], v[88:91], v[84:87], v[40:43]\n"
"ds_read_b128 v[84:87], v4 offset:4096\n"
"ds_read_b128 v[192:195], v9\n"
"v_mfma_f32_16x16x32_bf16 v[40:43], v[100:103], v[172:175], v[40:43]\n"
"ds_read_b128 v[172:175], v5 offset:4096\n"
"v_fmac_f32_e32 v196, v254, v200\n"
"v_mfma_f32_16x16x32_bf16 v[40:43], v[76:79], v[104:107], v[40:43]\n"
"v_fmac_f32_e32 v197, v254, v201\n"
"v_fmac_f32_e32 v198, v254, v202\n"
"v_mfma_f32_16x16x32_bf16 v[40:43], v[132:135], v[120:123], v[40:43]\n"
"v_fmac_f32_e32 v199, v254, v203\n"
"v_add_f32_e32 v196, v196, v204\n"
"v_mfma_f32_16x16x32_bf16 v[40:43], v[128:131], v[112:115], v[40:43]\n"
"buffer_load_dwordx4 v[104:107], v1, s[4:7], s98 offen offset:3072 sc1\n"
"v_mfma_f32_16x16x32_bf16 v[40:43], v[48:51], v[96:99], v[40:43]\n"
"ds_write_b128 v3, v[156:159] offset:16384\n"
"v_mfma_f32_16x16x32_bf16 v[40:43], v[72:75], v[92:95], v[40:43]\n"
"s_waitcnt vmcnt(18)\n"
"ds_read_b128 v[92:95], v4 offset:5120\n"
"ds_read_b128 v[96:99], v9 offset:64\n"
"; @A G1 1.3\n"
"v_mfma_f32_16x16x32_bf16 v[112:115], a[0:3], v[68:71], v[116:119]\n"
"ds_read_b128 v[120:123], v5 offset:5120\n"
"v_add_f32_e32 v197, v197, v205\n"
"v_mfma_f32_16x16x32_bf16 v[112:115], a[4:7], v[124:127], v[112:115]\n"
"s_waitcnt lgkmcnt(8)\n"
"v_add_f32_e32 v198, v198, v206\n"
"v_add_f32_e32 v199, v199, v207\n"
"v_mfma_f32_16x16x32_bf16 v[112:115], a[8:11], v[140:143], v[112:115]\n"
"s_add_u32 s98, s53, 0x8000\n"
"buffer_load_dwordx4 v[156:159], v0, s[4:7], s98 offen sc1\n"
"v_mfma_f32_16x16x32_bf16 v[112:115], a[12:15], v[148:151], v[112:115]\n"
"s_waitcnt vmcnt(13)\n"
"v_fmac_f32_e32 v208, v254, v212\n"
"v_fmac_f32_e32 v209, v254, v213\n"
"v_mfma_f32_16x16x32_bf16 v[112:115], a[16:19], v[136:139], v[112:115]\n"
"v_fmac_f32_e32 v210, v254, v214\n"
"v_fmac_f32_e32 v211, v254, v215\n"
"v_mfma_f32_16x16x32_bf16 v[112:115], a[20:23], v[164:167], v[112:115]\n"
"ds_write_b128 v3, v[40:43] offset:17408\n"
"v_mfma_f32_16x16x32_bf16 v[112:115], a[24:27], v[168:171], v[112:115]\n"
"v_add_f32_e32 v208, v208, v216\n"
"v_add_f32_e32 v209, v209, v217\n"
"v_mfma_f32_16x16x32_bf16 v[112:115], a[28:31], v[180:183], v[112:115]\n"
"buffer_load_dwordx4 v[40:43], v0, s[4:7], s98 offen offset:1024 sc1\n"
"v_mfma_f32_16x16x32_bf16 v[200:203], a[60:63], v[180:183], v[176:179]\n"
"buffer_load_dwordx4 a[156:159], v1, s[4:7], s96 offen offset:3072 sc1\n"
"v_mfma_f32_16x16x32_bf16 v[200:203], a[56:59], v[168:171], v[200:203]\n"
"s_waitcnt lgkmcnt(5)\n"
"v_add_f32_e32 v210, v210, v218\n"
"v_add_f32_e32 v211, v211, v219\n"
"v_mfma_f32_16x16x32_bf16 v[200:203], a[52:55], v[164:167], v[200:203]\n"
"s_waitcnt lgkmcnt(0)\n"
"s_barrier\n"
"v_mfma_f32_16x16x32_bf16 v[200:203], a[48:51], v[136:139], v[200:203]\n"
"ds_read_b128 v[204:207], v6\n"
"ds_read_b128 v[212:215], v6 offset:1024\n"
"v_mfma_f32_16x16x32_bf16 v[200:203], a[44:47], v[148:151], v[200:203]\n"
"v_cmp_u_f32_e32 vcc, v112, v112\n"
"v_fmac_f32_e32 v84, v254, v192\n"
"v_mfma_f32_16x16x32_bf16 v[200:203], a[40:43], v[140:143], v[200:203]\n"
"s_cbranch_vccnz .Lmk_p84s\n"
".Lmk_p84r:\n"
"v_fmac_f32_e32 v85, v254, v193\n"
"v_mfma_f32_16x16x32_bf16 v[200:203], a[36:39], v[124:127], v[200:203]\n"
"buffer_load_dwordx4 v[116:119], v0, s[4:7], s98 offen offset:2048 sc1\n"
"v_mfma_f32_16x16x32_bf16 v[200:203], a[32:35], v[68:71], v[200:203]\n"
"v_fmac_f32_e32 v86, v254, v194\n"
"v_fmac_f32_e32 v87, v254, v195\n"
"v_mfma_f32_16x16x32_bf16 v[196:199], a[64:67], v[68:71], v[196:199]\n"
"v_add_f32_e32 v84, v84, v172\n"
"v_add_f32_e32 v85, v85, v173\n"
"v_mfma_f32_16x16x32_bf16 v[196:199], a[68:71], v[124:127], v[196:199]\n"
"buffer_load_dwordx4 v[176:179], v0, s[4:7], s98 offen offset:3072 sc1\n"
"v_mfma_f32_16x16x32_bf16 v[196:199], a[72:75], v[140:143], v[196:199]\n"
"v_add_f32_e32 v86, v86, v174\n"
"v_add_f32_e32 v87, v87, v175\n"
"v_mfma_f32_16x16x32_bf16 v[196:199], a[76:79], v[148:151], v[196:199]\n"
"v_fmac_f32_e32 v92, v254, v96\n"
"v_fmac_f32_e32 v93, v254, v97\n"
"v_mfma_f32_16x16x32_bf16 v[196:199], a[80:83], v[136:139], v[196:199]\n"
"s_waitcnt lgkmcnt(0)\n"
"v_fmac_f32_e32 v94, v254, v98\n"
"v_fmac_f32_e32 v95, v254, v99\n"
"v_mfma_f32_16x16x32_bf16 v[196:199], a[84:87], v[164:167], v[196:199]\n"
"v_add_f32_e32 v92, v92, v120\n"
"v_add_f32_e32 v93, v93, v121\n"
"v_mfma_f32_16x16x32_bf16 v[196:199], a[88:91], v[168:171], v[196:199]\n"
"buffer_load_dwordx4 v[96:99], v1, s[4:7], s98 offen sc1\n"
"v_mfma_f32_16x16x32_bf16 v[196:199], a[92:95], v[180:183], v[196:199]\n"
"ds_read_b128 v[172:175], v6 offset:2048\n"
"ds_read_b128 v[192:195], v6 offset:3072\n"
"v_mfma_f32_16x16x32_bf16 v[208:211], a[124:127], v[180:183], v[208:211]\n"
"v_add_f32_e32 v94, v94, v122\n"
"v_add_f32_e32 v95, v95, v123\n"
"v_mfma_f32_16x16x32_bf16 v[208:211], a[120:123], v[168:171], v[208:211]\n"
"v_add_f32_e32 v112, v112, v204\n"
"v_add_f32_e32 v113, v113, v205\n"
"v_mfma_f32_16x16x32_bf16 v[208:211], a[116:119], v[164:167], v[208:211]\n"
"s_add_u32 s97, s70, 0x2000\n"
"v_add_f32_e32 v114, v114, v206\n"
"v_add_f32_e32 v115, v115, v207\n"
"v_mfma_f32_16x16x32_bf16 v[208:211], a[112:115], v[136:139], v[208:211]\n"
"buffer_load_dwordx4 v[120:123], v1, s[4:7], s98 offen offset:1024 sc1\n"
"v_mfma_f32_16x16x32_bf16 v[208:211], a[108:111], v[148:151], v[208:211]\n"
"buffer_load_dwordx4 a[160:163], v0, s[4:7], s97 offen sc1\n"
"v_mfma_f32_16x16x32_bf16 v[208:211], a[104:107], v[140:143], v[208:211]\n"
"v_add_f32_e32 v200, v200, v212\n"
"v_add_f32_e32 v201, v201, v213\n"
"v_mfma_f32_16x16x32_bf16 v[208:211], a[100:103], v[124:127], v[208:211]\n"
"v_add_f32_e32 v202, v202, v214\n"
"v_add_f32_e32 v203, v203, v215\n"
"v_mfma_f32_16x16x32_bf16 v[208:211], a[96:99], v[68:71], v[208:211]\n"
"v_mul_f32_e32 v112, v112, v255\n"
"v_mul_f32_e32 v113, v113, v255\n"
"v_mfma_f32_16x16x32_bf16 v[84:87], v[24:27], v[68:71], v[84:87]\n"
"buffer_load_dwordx4 v[204:207], v1, s[4:7], s98 offen offset:2048 sc1\n"
"v_mfma_f32_16x16x32_bf16 v[84:87], v[56:59], v[124:127], v[84:87]\n"
"v_mul_f32_e32 v114, v114, v255\n"
"v_mul_f32_e32 v115, v115, v255\n"
"v_mfma_f32_16x16x32_bf16 v[84:87], v[28:31], v[140:143], v[84:87]\n"
"v_mul_f32_e32 v200, v200, v255\n"
"v_mul_f32_e32 v201, v201, v255\n"
"v_mfma_f32_16x16x32_bf16 v[84:87], v[64:67], v[148:151], v[84:87]\n"
"v_mul_f32_e32 v202, v202, v255\n"
"v_mul_f32_e32 v203, v203, v255\n"
"v_mfma_f32_16x16x32_bf16 v[84:87], v[52:55], v[136:139], v[84:87]\n"
"s_waitcnt lgkmcnt(0)\n"
"v_cvt_pk_bf16_f32 v212, v112, v113\n"
"v_cvt_pk_bf16_f32 v213, v114, v115\n"
"v_mfma_f32_16x16x32_bf16 v[84:87], v[36:39], v[164:167], v[84:87]\n"
"buffer_load_dwordx4 v[112:115], v1, s[4:7], s98 offen offset:3072 sc1\n"
"v_mfma_f32_16x16x32_bf16 v[84:87], v[108:111], v[168:171], v[84:87]\n"
"ds_read_b128 v[216:219], v6 offset:4096\n"
"ds_read_b128 v[220:223], v6 offset:5120\n"
"v_mfma_f32_16x16x32_bf16 v[84:87], v[60:63], v[180:183], v[84:87]\n"
"v_cvt_pk_bf16_f32 v214, v200, v201\n"
"v_cvt_pk_bf16_f32 v215, v202, v203\n"
"v_mfma_f32_16x16x32_bf16 v[92:95], v[80:83], v[180:183], v[92:95]\n"
"ds_write_b128 v8, v[212:215] offset:25600\n"
"v_mfma_f32_16x16x32_bf16 v[92:95], v[88:91], v[168:171], v[92:95]\n"
"v_add_f32_e32 v196, v196, v172\n"
"v_add_f32_e32 v197, v197, v173\n"
"v_mfma_f32_16x16x32_bf16 v[92:95], v[100:103], v[164:167], v[92:95]\n"
"v_add_f32_e32 v198, v198, v174\n"
"v_add_f32_e32 v199, v199, v175\n"
"v_mfma_f32_16x16x32_bf16 v[92:95], v[76:79], v[136:139], v[92:95]\n"
"v_add_f32_e32 v208, v208, v192\n"
"v_add_f32_e32 v209, v209, v193\n"
"v_mfma_f32_16x16x32_bf16 v[92:95], v[132:135], v[148:151], v[92:95]\n"
"v_add_f32_e32 v210, v210, v194\n"
"v_add_f32_e32 v211, v211, v195\n"
"v_mfma_f32_16x16x32_bf16 v[92:95], v[128:131], v[140:143], v[92:95]\n"
"v_mul_f32_e32 v196, v196, v255\n"
"v_mul_f32_e32 v197, v197, v255\n"
"v_mfma_f32_16x16x32_bf16 v[92:95], v[48:51], v[124:127], v[92:95]\n"
"v_mul_f32_e32 v198, v198, v255\n"
"v_mul_f32_e32 v199, v199, v255\n"
"v_mfma_f32_16x16x32_bf16 v[92:95], v[72:75], v[68:71], v[92:95]\n"
"v_mul_f32_e32 v208, v208, v255\n"
"v_mul_f32_e32 v209, v209, v255\n"
"v_mul_f32_e32 v210, v210, v255\n"
"v_mul_f32_e32 v211, v211, v255\n"
"v_cvt_pk_bf16_f32 v68, v196, v197\n"
"v_cvt_pk_bf16_f32 v69, v198, v199\n"
"v_cvt_pk_bf16_f32 v70, v208, v209\n"
"v_cvt_pk_bf16_f32 v71, v210, v211\n"
"ds_write_b128 v7, v[68:71] offset:58368\n"
".Lmk_a2:\n"
"s_waitcnt vmcnt(15)\n"
"; @A G1 2.0\n"
"v_mfma_f32_16x16x32_bf16 v[124:127], a[0:3], v[44:47], 0\n"
"v_mfma_f32_16x16x32_bf16 v[124:127], a[4:7], v[32:35], v[124:127]\n"
"s_add_u32 s98, s62, 0x800\n"
"v_mfma_f32_16x16x32_bf16 v[124:127], a[8:11], v[188:191], v[124:127]\n"
"s_add_u32 s96, s54, 0x8000\n"
"buffer_load_dwordx4 v[136:139], v0, s[4:7], s96 offen sc1\n"
"v_mfma_f32_16x16x32_bf16 v[124:127], a[12:15], v[144:147], v[124:127]\n"
"s_waitcnt vmcnt(11)\n"
"v_mfma_f32_16x16x32_bf16 v[124:127], a[16:19], v[152:155], v[124:127]\n"
"buffer_load_dword v255, v10, s[4:7], s98 offen sc1\n"
"v_mfma_f32_16x16x32_bf16 v[124:127], a[20:23], v[184:187], v[124:127]\n"
"s_waitcnt lgkmcnt(2)\n"
"v_add_f32_e32 v84, v84, v216\n"
"v_add_f32_e32 v85, v85, v217\n"
"v_mfma_f32_16x16x32_bf16 v[124:127], a[24:27], v[160:163], v[124:127]\n"
"v_add_f32_e32 v86, v86, v218\n"
"v_add_f32_e32 v87, v87, v219\n"
"v_mfma_f32_16x16x32_bf16 v[124:127], a[28:31], v[104:107], v[124:127]\n"
"buffer_load_dwordx4 v[140:143], v0, s[4:7], s96 offen offset:1024 sc1\n"
"v_mfma_f32_16x16x32_bf16 v[148:151], a[60:63], v[104:107], 0\n"
"buffer_load_dwordx4 a[164:167], v0, s[4:7], s97 offen offset:1024 sc1\n"
"v_mfma_f32_16x16x32_bf16 v[148:151], a[56:59], v[160:163], v[148:151]\n"
"buffer_load_dword v254, v10, s[4:7], s98 offen offset:256 sc1\n"
"v_mfma_f32_16x16x32_bf16 v[148:151], a[52:55], v[184:187], v[148:151]\n"
"v_add_f32_e32 v92, v92, v220\n"
"v_add_f32_e32 v93, v93, v221\n"
"v_mfma_f32_16x16x32_bf16 v[148:151], a[48:51], v[152:155], v[148:151]\n"
"v_add_f32_e32 v94, v94, v222\n"
"v_add_f32_e32 v95, v95, v223\n"
"v_mfma_f32_16x16x32_bf16 v[148:151], a[44:47], v[144:147], v[148:151]\n"
"buffer_load_dwordx4 v[164:167], v0, s[4:7], s96 offen offset:2048 sc1\n"
"v_mfma_f32_16x16x32_bf16 v[148:151], a[40:43], v[188:191], v[148:151]\n"
"ds_write_b128 v3, v[124:127]\n"
"v_mfma_f32_16x16x32_bf16 v[148:151], a[36:39], v[32:35], v[148:151]\n"
"buffer_load_dword v252, v10, s[4:7], s98 offen offset:512 sc1\n"
"v_mfma_f32_16x16x32_bf16 v[148:151], a[32:35], v[44:47], v[148:151]\n"
"v_mul_f32_e32 v84, v84, v253\n"
"v_mul_f32_e32 v85, v85, v253\n"
"v_mfma_f32_16x16x32_bf16 v[124:127], a[64:67], v[44:47], 0\n"
"v_mul_f32_e32 v86, v86, v253\n"
"v_mul_f32_e32 v87, v87, v253\n"
"v_mfma_f32_16x16x32_bf16 v[124:127], a[68:71], v[32:35], v[124:127]\n"
"buffer_load_dwordx4 v[168:171], v0, s[4:7], s96 offen offset:3072 sc1\n"
"v_mfma_f32_16x16x32_bf16 v[124:127], a[72:75], v[188:191], v[124:127]\n"
"buffer_load_dword v251, v10, s[4:7], s98 offen offset:768 sc1\n"
"v_mfma_f32_16x16x32_bf16 v[124:127], a[76:79], v[144:147], v[124:127]\n"
"v_mul_f32_e32 v92, v92, v253\n"
"v_mul_f32_e32 v93, v93, v253\n"
"v_mfma_f32_16x16x32_bf16 v[124:127], a[80:83], v[152:155], v[124:127]\n"
"v_mul_f32_e32 v94, v94, v253\n"
"v_mul_f32_e32 v95, v95, v253\n"
"v_mfma_f32_16x16x32_bf16 v[124:127], a[84:87], v[184:187], v[124:127]\n"
"ds_write_b128 v3, v[148:151] offset:1024\n"
"v_mfma_f32_16x16x32_bf16 v[124:127], a[88:91], v[160:163], v[124:127]\n"
"buffer_load_dwordx4 v[148:151], v1, s[4:7], s96 offen sc1\n"
"v_mfma_f32_16x16x32_bf16 v[124:127], a[92:95], v[104:107], v[124:127]\n"
"buffer_load_dword v253, v10, s[4:7], s98 offen offset:1024 sc1\n"
"v_mfma_f32_16x16x32_bf16 v[172:175], a[124:127], v[104:107], 0\n"
"v_cvt_pk_bf16_f32 v180, v84, v85\n"
"v_cvt_pk_bf16_f32 v181, v86, v87\n"
"v_mfma_f32_16x16x32_bf16 v[172:175], a[120:123], v[160:163], v[172:175]\n"
"v_cvt_pk_bf16_f32 v182, v92, v93\n"
"v_cvt_pk_bf16_f32 v183, v94, v95\n"
"v_mfma_f32_16x16x32_bf16 v[172:175], a[116:119], v[184:187], v[172:175]\n"
"buffer_load_dword v250, v10, s[4:7], s98 offen offset:1280 sc1\n"
"v_mfma_f32_16x16x32_bf16 v[172:175], a[112:115], v[152:155], v[172:175]\n"
"buffer_load_dwordx4 v[84:87], v1, s[4:7], s96 offen offset:1024 sc1\n"
"v_mfma_f32_16x16x32_bf16 v[172:175], a[108:111], v[144:147], v[172:175]\n"
"buffer_load_dwordx4 a[168:171], v0, s[4:7], s97 offen offset:2048 sc1\n"
"v_mfma_f32_16x16x32_bf16 v[172:175], a[104:107], v[188:191], v[172:175]\n"
"ds_write_b128 v3, v[124:127] offset:2048\n"
"v_mfma_f32_16x16x32_bf16 v[172:175], a[100:103], v[32:35], v[172:175]\n"
"buffer_load_dword v249, v10, s[4:7], s98 offen offset:1536 sc1\n"
"v_mfma_f32_16x16x32_bf16 v[172:175], a[96:99], v[44:47], v[172:175]\n"
"ds_write_b128 v2, v[180:183] offset:50176\n"
"v_mfma_f32_16x16x32_bf16 v[92:95], v[24:27], v[44:47], 0\n"
"buffer_load_dwordx4 v[124:127], v1, s[4:7], s96 offen offset:2048 sc1\n"
"s_add_u32 s95, s64, 0x2000\n"
"v_mfma_f32_16x16x32_bf16 v[92:95], v[56:59], v[32:35], v[92:95]\n"
"buffer_load_dword v248, v10, s[4:7], s98 offen offset:1792 sc1\n"
"v_mfma_f32_16x16x32_bf16 v[92:95], v[28:31], v[188:191], v[92:95]\n"
"buffer_store_dwordx4 v[212:215], v8, s[4:7], s95 offen offset:1024 sc1\n"
"v_mfma_f32_16x16x32_bf16 v[92:95], v[64:67], v[144:147], v[92:95]\n"
"v_mfma_f32_16x16x32_bf16 v[92:95], v[52:55], v[152:155], v[92:95]\n"
"v_mfma_f32_16x16x32_bf16 v[92:95], v[36:39], v[184:187], v[92:95]\n"
"buffer_load_dwordx4 v[180:183], v1, s[4:7], s96 offen offset:3072 sc1\n"
"v_mfma_f32_16x16x32_bf16 v[92:95], v[108:111], v[160:163], v[92:95]\n"
"ds_write_b128 v3, v[172:175] offset:3072\n"
"v_mfma_f32_16x16x32_bf16 v[92:95], v[60:63], v[104:107], v[92:95]\n"
"v_mfma_f32_16x16x32_bf16 v[172:175], v[80:83], v[104:107], 0\n"
"v_mfma_f32_16x16x32_bf16 v[172:175], v[88:91], v[160:163], v[172:175]\n"
"v_mfma_f32_16x16x32_bf16 v[172:175], v[100:103], v[184:187], v[172:175]\n"
"v_mfma_f32_16x16x32_bf16 v[172:175], v[76:79], v[152:155], v[172:175]\n"
"v_mfma_f32_16x16x32_bf16 v[172:175], v[132:135], v[144:147], v[172:175]\n"
"v_mfma_f32_16x16x32_bf16 v[172:175], v[128:131], v[188:191], v[172:175]\n"
"s_nop 1\n"
"ds_write_b128 v3, v[92:95] offset:4096\n"
"v_mfma_f32_16x16x32_bf16 v[172:175], v[48:51], v[32:35], v[172:175]\n"
"v_mfma_f32_16x16x32_bf16 v[172:175], v[72:75], v[44:47], v[172:175]\n"
"s_waitcnt vmcnt(24)\n"
"; @A G1 2.1\n"
"v_mfma_f32_16x16x32_bf16 v[32:35], a[0:3], v[156:159], 0\n"
"buffer_load_dwordx4 a[172:175], v0, s[4:7], s97 offen offset:3072 sc1\n"
"v_mfma_f32_16x16x32_bf16 v[32:35], a[4:7], v[40:43], v[32:35]\n"
"v_mfma_f32_16x16x32_bf16 v[32:35], a[8:11], v[116:119], v[32:35]\n"
"s_add_u32 s98, s55, 0x8000\n"
"buffer_load_dwordx4 v[44:47], v0, s[4:7], s98 offen sc1\n"
"v_mfma_f32_16x16x32_bf16 v[32:35], a[12:15], v[176:179], v[32:35]\n"
"s_waitcnt vmcnt(21)\n"
"v_mfma_f32_16x16x32_bf16 v[32:35], a[16:19], v[96:99], v[32:35]\n"
"v_mfma_f32_16x16x32_bf16 v[32:35], a[20:23], v[120:123], v[32:35]\n"
"ds_write_b128 v3, v[172:175] offset:5120\n"
"v_mfma_f32_16x16x32_bf16 v[32:35], a[24:27], v[204:207], v[32:35]\n"
"v_mfma_f32_16x16x32_bf16 v[32:35], a[28:31], v[112:115], v[32:35]\n"
"buffer_load_dwordx4 v[92:95], v0, s[4:7], s98 offen offset:1024 sc1\n"
"v_mfma_f32_16x16x32_bf16 v[104:107], a[60:63], v[112:115], 0\n"
"s_add_u32 s96, s64, 0x6000\n"
"v_mfma_f32_16x16x32_bf16 v[104:107], a[56:59], v[204:207], v[104:107]\n"
"v_mfma_f32_16x16x32_bf16 v[104:107], a[52:55], v[120:123], v[104:107]\n"
"buffer_store_dwordx4 v[68:71], v7, s[4:7], s96 offen offset:1024 sc1\n"
"v_mfma_f32_16x16x32_bf16 v[104:107], a[48:51], v[96:99], v[104:107]\n"
"v_mfma_f32_16x16x32_bf16 v[104:107], a[44:47], v[176:179], v[104:107]\n"
"buffer_load_dwordx4 v[68:71], v0, s[4:7], s98 offen offset:2048 sc1\n"
"v_mfma_f32_16x16x32_bf16 v[104:107], a[40:43], v[116:119], v[104:107]\n"
"ds_write_b128 v3, v[32:35] offset:6144\n"
"v_mfma_f32_16x16x32_bf16 v[104:107], a[36:39], v[40:43], v[104:107]\n"
"v_mfma_f32_16x16x32_bf16 v[104:107], a[32:35], v[156:159], v[104:107]\n"
"v_mfma_f32_16x16x32_bf16 v[32:35], a[64:67], v[156:159], 0\n"
"v_mfma_f32_16x16x32_bf16 v[32:35], a[68:71], v[40:43], v[32:35]\n"
"buffer_load_dwordx4 v[144:147], v0, s[4:7], s98 offen offset:3072 sc1\n"
"v_mfma_f32_16x16x32_bf16 v[32:35], a[72:75], v[116:119], v[32:35]\n"
"v_mfma_f32_16x16x32_bf16 v[32:35], a[76:79], v[176:179], v[32:35]\n"
"v_mfma_f32_16x16x32_bf16 v[32:35], a[80:83], v[96:99], v[32:35]\n"
"buffer_load_dwordx4 a[176:179], v1, s[4:7], s97 offen sc1\n"
"v_mfma_f32_16x16x32_bf16 v[32:35], a[84:87], v[120:123], v[32:35]\n"
"ds_write_b128 v3, v[104:107] offset:7168\n"
"v_mfma_f32_16x16x32_bf16 v[32:35], a[88:91], v[204:207], v[32:35]\n"
"buffer_load_dwordx4 v[104:107], v1, s[4:7], s98 offen sc1\n"
"v_mfma_f32_16x16x32_bf16 v[32:35], a[92:95], v[112:115], v[32:35]\n"
"v_mfma_f32_16x16x32_bf16 v[152:155], a[124:127], v[112:115], 0\n"
"v_mfma_f32_16x16x32_bf16 v[152:155], a[120:123], v[204:207], v[152:155]\n"
"v_mfma_f32_16x16x32_bf16 v[152:155], a[116:119], v[120:123], v[152:155]\n"
"v_mfma_f32_16x16x32_bf16 v[152:155], a[112:115], v[96:99], v[152:155]\n"
"buffer_load_dwordx4 v[160:163], v1, s[4:7], s98 offen offset:1024 sc1\n"
"v_mfma_f32_16x16x32_bf16 v[152:155], a[108:111], v[176:179], v[152:155]\n"
"v_mfma_f32_16x16x32_bf16 v[152:155], a[104:107], v[116:119], v[152:155]\n"
"s_nop 0\n"
"ds_write_b128 v3, v[32:35] offset:8192\n"
"v_mfma_f32_16x16x32_bf16 v[152:155], a[100:103], v[40:43], v[152:155]\n"
"v_mfma_f32_16x16x32_bf16 v[152:155], a[96:99], v[156:159], v[152:155]\n"
"v_mfma_f32_16x16x32_bf16 v[32:35], v[24:27], v[156:159], 0\n"
"buffer_load_dwordx4 v[172:175], v1, s[4:7], s98 offen offset:2048 sc1\n"
"v_mfma_f32_16x16x32_bf16 v[32:35], v[56:59], v[40:43], v[32:35]\n"
"v_mfma_f32_16x16x32_bf16 v[32:35], v[28:31], v[116:119], v[32:35]\n"
"v_mfma_f32_16x16x32_bf16 v[32:35], v[64:67], v[176:179], v[32:35]\n"
"v_mfma_f32_16x16x32_bf16 v[32:35], v[52:55], v[96:99], v[32:35]\n"
"s_waitcnt vmcnt(12)\n"
"v_add_f32_e32 v255, v255, v253\n"
"v_add_f32_e32 v254, v254, v250\n"
"v_mfma_f32_16x16x32_bf16 v[32:35], v[36:39], v[120:123], v[32:35]\n"
"buffer_load_dwordx4 v[184:187], v1, s[4:7], s98 offen offset:3072 sc1\n"
"v_mfma_f32_16x16x32_bf16 v[32:35], v[108:111], v[204:207], v[32:35]\n"
"ds_write_b128 v3, v[152:155] offset:9216\n"
"v_mfma_f32_16x16x32_bf16 v[32:35], v[60:63], v[112:115], v[32:35]\n"
"v_add_f32_e32 v252, v252, v249\n"
"v_add_f32_e32 v251, v251, v248\n"
"v_mfma_f32_16x16x32_bf16 v[152:155], v[80:83], v[112:115], 0\n"
"buffer_load_dwordx4 a[180:183], v1, s[4:7], s97 offen offset:1024 sc1\n"
"v_mfma_f32_16x16x32_bf16 v[152:155], v[88:91], v[204:207], v[152:155]\n"
"v_add_f32_e32 v255, v255, v252\n"
"v_add_f32_e32 v254, v254, v251\n"
"v_mfma_f32_16x16x32_bf16 v[152:155], v[100:103], v[120:123], v[152:155]\n"
"v_add_f32_e32 v255, v255, v254\n"
"v_mov_b32_e32 v254, v255\n"
"v_mfma_f32_16x16x32_bf16 v[152:155], v[76:79], v[96:99], v[152:155]\n"
"s_nop 0\n"
"v_permlane32_swap_b32_e32 v255, v254\n"
"v_add_f32_e32 v255, v255, v254\n"
"v_mfma_f32_16x16x32_bf16 v[152:155], v[132:135], v[176:179], v[152:155]\n"
"v_mul_f32_e32 v255, 0x3a800000, v255\n"
"v_mov_b32_e32 v254, v255\n"
"v_mfma_f32_16x16x32_bf16 v[152:155], v[128:131], v[116:119], v[152:155]\n"
"ds_write_b128 v3, v[32:35] offset:10240\n"
"v_mfma_f32_16x16x32_bf16 v[152:155], v[48:51], v[40:43], v[152:155]\n"
"v_permlane16_swap_b32_e32 v255, v254\n"
"v_cvt_pk_bf16_f32 v253, -v255, 0\n"
"v_mfma_f32_16x16x32_bf16 v[152:155], v[72:75], v[156:159], v[152:155]\n"
"v_fma_f32 v254, -v255, v255, v254\n"
"v_lshlrev_b32_e32 v253, 16, v253\n"
"; @A G1 2.2\n"
"v_mfma_f32_16x16x32_bf16 v[32:35], a[0:3], v[136:139], 0\n"
"v_max_f32_e32 v254, 0, v254\n"
"v_cvt_pk_bf16_f32 v254, v254, 0\n"
"v_mfma_f32_16x16x32_bf16 v[32:35], a[4:7], v[140:143], v[32:35]\n"
"v_lshlrev_b32_e32 v254, 16, v254\n"
"v_add_f32_e32 v254, 0x3727c5ac, v254\n"
"v_mfma_f32_16x16x32_bf16 v[32:35], a[8:11], v[164:167], v[32:35]\n"
"v_cvt_pk_bf16_f32 v254, v254, 0\n"
"v_lshlrev_b32_e32 v254, 16, v254\n"
"v_mfma_f32_16x16x32_bf16 v[32:35], a[12:15], v[168:171], v[32:35]\n"
"s_waitcnt vmcnt(12)\n"
"v_sqrt_f32_e32 v254, v254\n"
"v_mfma_f32_16x16x32_bf16 v[32:35], a[16:19], v[148:151], v[32:35]\n"
"s_mov_b32 s98, .Lmk_a4-.Lmk_g86\n"
"s_getpc_b64 s[84:85]\n"
".Lmk_g86:\n"
"s_add_u32 s84, s84, s98\n"
"s_addc_u32 s85, s85, 0\n"
"s_and_b32 s84, s84, -16\n"
"v_mfma_f32_16x16x32_bf16 v[32:35], a[20:23], v[84:87], v[32:35]\n"
"ds_write_b128 v3, v[152:155] offset:11264\n"
"v_mfma_f32_16x16x32_bf16 v[32:35], a[24:27], v[124:127], v[32:35]\n"
"s_and_b32 s85, s85, 0xffff\n"
"s_sub_u32 s98, .Lmk_end-.Lmk_g86, s98\n"
"s_min_u32 s86, s98, 0x4000\n"
"s_mov_b32 s87, 0x20000\n"
"v_mfma_f32_16x16x32_bf16 v[32:35], a[28:31], v[180:183], v[32:35]\n"
"s_mov_b32 m0, 0x27800\n"
"s_nop 0\n"
"buffer_load_dwordx4 v0, s[84:87], s99 offen lds\n"
"v_mfma_f32_16x16x32_bf16 v[40:43], a[60:63], v[180:183], 0\n"
"v_cvt_pk_bf16_f32 v254, v254, 0\n"
"v_lshlrev_b32_e32 v254, 16, v254\n"
"v_mfma_f32_16x16x32_bf16 v[40:43], a[56:59], v[124:127], v[40:43]\n"
"v_rcp_f32_e32 v255, v254\n"
"v_mfma_f32_16x16x32_bf16 v[40:43], a[52:55], v[84:87], v[40:43]\n"
"s_add_u32 s98, s52, 0\n"
"buffer_load_dwordx4 v[96:99], v0, s[4:7], s98 offen sc1\n"
"v_mfma_f32_16x16x32_bf16 v[40:43], a[48:51], v[148:151], v[40:43]\n"
"s_waitcnt lgkmcnt(0)\n"
"s_barrier\n"
"v_mfma_f32_16x16x32_bf16 v[40:43], a[44:47], v[168:171], v[40:43]\n"
"buffer_load_dwordx4 a[184:187], v1, s[4:7], s97 offen offset:2048 sc1\n"
"v_mfma_f32_16x16x32_bf16 v[40:43], a[40:43], v[164:167], v[40:43]\n"
"ds_write_b128 v3, v[32:35] offset:12288\n"
"v_mfma_f32_16x16x32_bf16 v[40:43], a[36:39], v[140:143], v[40:43]\n"
"ds_read_b128 v[32:35], v4\n"
"v_mul_f32_e32 v254, 0x3e8293ee, v255\n"
"v_mfma_f32_16x16x32_bf16 v[40:43], a[32:35], v[136:139], v[40:43]\n"
"buffer_load_dwordx4 v[112:115], v0, s[4:7], s98 offen offset:1024 sc1\n"
"v_mfma_f32_16x16x32_bf16 v[116:119], a[64:67], v[136:139], 0\n"
"ds_read_b128 v[120:123], v9 offset:128\n"
"ds_read_b128 v[152:155], v5\n"
"v_mfma_f32_16x16x32_bf16 v[116:119], a[68:71], v[140:143], v[116:119]\n"
"v_mfma_f32_16x16x32_bf16 v[116:119], a[72:75], v[164:167], v[116:119]\n"
"v_mfma_f32_16x16x32_bf16 v[116:119], a[76:79], v[168:171], v[116:119]\n"
"ds_read_b128 v[156:159], v4 offset:1024\n"
"ds_read_b128 v[176:179], v9 offset:192\n"
"v_mfma_f32_16x16x32_bf16 v[116:119], a[80:83], v[148:151], v[116:119]\n"
"buffer_load_dwordx4 v[188:191], v0, s[4:7], s98 offen offset:2048 sc1\n"
"v_mfma_f32_16x16x32_bf16 v[116:119], a[84:87], v[84:87], v[116:119]\n"
"ds_write_b128 v3, v[40:43] offset:13312\n"
"v_mfma_f32_16x16x32_bf16 v[116:119], a[88:91], v[124:127], v[116:119]\n"
"ds_read_b128 v[40:43], v5 offset:1024\n"
"s_waitcnt lgkmcnt(4)\n"
"v_mfma_f32_16x16x32_bf16 v[116:119], a[92:95], v[180:183], v[116:119]\n"
"v_fmac_f32_e32 v32, v253, v120\n"
"v_fmac_f32_e32 v33, v253, v121\n"
"v_mfma_f32_16x16x32_bf16 v[192:195], a[124:127], v[180:183], 0\n"
"v_fmac_f32_e32 v34, v253, v122\n"
"v_fmac_f32_e32 v35, v253, v123\n"
"v_mfma_f32_16x16x32_bf16 v[192:195], a[120:123], v[124:127], v[192:195]\n"
"buffer_load_dwordx4 v[120:123], v0, s[4:7], s98 offen offset:3072 sc1\n"
"v_mfma_f32_16x16x32_bf16 v[192:195], a[116:119], v[84:87], v[192:195]\n"
"ds_read_b128 v[196:199], v4 offset:2048\n"
"ds_read_b128 v[200:203], v9 offset:256\n"
"v_mfma_f32_16x16x32_bf16 v[192:195], a[112:115], v[148:151], v[192:195]\n"
"ds_read_b128 v[204:207], v5 offset:2048\n"
"v_add_f32_e32 v32, v32, v152\n"
"v_mfma_f32_16x16x32_bf16 v[192:195], a[108:111], v[168:171], v[192:195]\n"
"v_add_f32_e32 v33, v33, v153\n"
"v_add_f32_e32 v34, v34, v154\n"
"v_mfma_f32_16x16x32_bf16 v[192:195], a[104:107], v[164:167], v[192:195]\n"
"ds_write_b128 v3, v[116:119] offset:14336\n"
"v_mfma_f32_16x16x32_bf16 v[192:195], a[100:103], v[140:143], v[192:195]\n"
"buffer_load_dwordx4 v[116:119], v1, s[4:7], s98 offen sc1\n"
"v_mfma_f32_16x16x32_bf16 v[192:195], a[96:99], v[136:139], v[192:195]\n"
"s_waitcnt lgkmcnt(4)\n"
"v_add_f32_e32 v35, v35, v155\n"
"v_fmac_f32_e32 v156, v253, v176\n"
"v_mfma_f32_16x16x32_bf16 v[152:155], v[24:27], v[136:139], 0\n"
"buffer_load_dwordx4 a[188:191], v1, s[4:7], s97 offen offset:3072 sc1\n"
"v_mfma_f32_16x16x32_bf16 v[152:155], v[56:59], v[140:143], v[152:155]\n"
"ds_read_b128 v[208:211], v4 offset:3072\n"
"ds_read_b128 v[212:215], v9 offset:320\n"
"v_mfma_f32_16x16x32_bf16 v[152:155], v[28:31], v[164:167], v[152:155]\n"
"s_waitcnt vmcnt(16)\n"
"s_add_u32 s97, s68, 0x1000\n"
"ds_read_b128 v[216:219], v5 offset:3072\n"
"v_mfma_f32_16x16x32_bf16 v[152:155], v[64:67], v[168:171], v[152:155]\n"
"buffer_load_dwordx4 v[220:223], v1, s[4:7], s98 offen offset:1024 sc1\n"
"v_mfma_f32_16x16x32_bf16 v[152:155], v[52:55], v[148:151], v[152:155]\n"
"buffer_store_dwordx2 v[18:19], v11, s[4:7], s97 offen offset:64 sc1\n"
"v_mfma_f32_16x16x32_bf16 v[152:155], v[36:39], v[84:87], v[152:155]\n"
"ds_write_b128 v3, v[192:195] offset:15360\n"
"v_mfma_f32_16x16x32_bf16 v[152:155], v[108:111], v[124:127], v[152:155]\n"
"v_fmac_f32_e32 v157, v253, v177\n"
"v_fmac_f32_e32 v158, v253, v178\n"
"v_mfma_f32_16x16x32_bf16 v[152:155], v[60:63], v[180:183], v[152:155]\n"
"v_fmac_f32_e32 v159, v253, v179\n"
"v_add_f32_e32 v156, v156, v40\n"
"v_mfma_f32_16x16x32_bf16 v[176:179], v[80:83], v[180:183], 0\n"
"buffer_load_dwordx4 v[180:183], v1, s[4:7], s98 offen offset:2048 sc1\n"
"v_mfma_f32_16x16x32_bf16 v[176:179], v[88:91], v[124:127], v[176:179]\n"
"ds_read_b128 v[124:127], v4 offset:4096\n"
"ds_read_b128 v[192:195], v9\n"
"v_mfma_f32_16x16x32_bf16 v[176:179], v[100:103], v[84:87], v[176:179]\n"
"ds_read_b128 v[84:87], v5 offset:4096\n"
"v_add_f32_e32 v157, v157, v41\n"
"v_mfma_f32_16x16x32_bf16 v[176:179], v[76:79], v[148:151], v[176:179]\n"
"s_waitcnt lgkmcnt(8)\n"
"v_add_f32_e32 v158, v158, v42\n"
"v_add_f32_e32 v159, v159, v43\n"
"v_mfma_f32_16x16x32_bf16 v[176:179], v[132:135], v[168:171], v[176:179]\n"
"v_fmac_f32_e32 v196, v253, v200\n"
"v_fmac_f32_e32 v197, v253, v201\n"
"v_mfma_f32_16x16x32_bf16 v[176:179], v[128:131], v[164:167], v[176:179]\n"
"buffer_load_dwordx4 v[40:43], v1, s[4:7], s98 offen offset:3072 sc1\n"
"v_mfma_f32_16x16x32_bf16 v[176:179], v[48:51], v[140:143], v[176:179]\n"
"ds_write_b128 v3, v[152:155] offset:16384\n"
"v_mfma_f32_16x16x32_bf16 v[176:179], v[72:75], v[136:139], v[176:179]\n"
"s_waitcnt vmcnt(18)\n"
"ds_read_b128 v[136:139], v4 offset:5120\n"
"ds_read_b128 v[140:143], v9 offset:64\n"
"; @A G1 2.3\n"
"v_mfma_f32_16x16x32_bf16 v[32:35], a[0:3], v[44:47], v[32:35]\n"
"ds_read_b128 v[148:151], v5 offset:5120\n"
"v_fmac_f32_e32 v198, v253, v202\n"
"v_mfma_f32_16x16x32_bf16 v[32:35], a[4:7], v[92:95], v[32:35]\n"
"v_fmac_f32_e32 v199, v253, v203\n"
"v_add_f32_e32 v196, v196, v204\n"
"v_mfma_f32_16x16x32_bf16 v[32:35], a[8:11], v[68:71], v[32:35]\n"
"s_add_u32 s98, s53, 0\n"
"buffer_load_dwordx4 v[152:155], v0, s[4:7], s98 offen sc1\n"
"v_mfma_f32_16x16x32_bf16 v[32:35], a[12:15], v[144:147], v[32:35]\n"
"s_waitcnt vmcnt(14)\n"
"s_add_u32 s97, s70, 0x4000\n"
"v_add_f32_e32 v197, v197, v205\n"
"v_mfma_f32_16x16x32_bf16 v[32:35], a[16:19], v[104:107], v[32:35]\n"
"buffer_load_dwordx4 a[192:195], v0, s[4:7], s97 offen sc1\n"
"v_mfma_f32_16x16x32_bf16 v[32:35], a[20:23], v[160:163], v[32:35]\n"
"ds_write_b128 v3, v[176:179] offset:17408\n"
"v_mfma_f32_16x16x32_bf16 v[32:35], a[24:27], v[172:175], v[32:35]\n"
"s_waitcnt lgkmcnt(9)\n"
"v_add_f32_e32 v198, v198, v206\n"
"v_add_f32_e32 v199, v199, v207\n"
"v_mfma_f32_16x16x32_bf16 v[32:35], a[28:31], v[184:187], v[32:35]\n"
"buffer_load_dwordx4 v[164:167], v0, s[4:7], s98 offen offset:1024 sc1\n"
"v_mfma_f32_16x16x32_bf16 v[156:159], a[60:63], v[184:187], v[156:159]\n"
"v_fmac_f32_e32 v208, v253, v212\n"
"v_fmac_f32_e32 v209, v253, v213\n"
"v_mfma_f32_16x16x32_bf16 v[156:159], a[56:59], v[172:175], v[156:159]\n"
"v_fmac_f32_e32 v210, v253, v214\n"
"v_fmac_f32_e32 v211, v253, v215\n"
"v_mfma_f32_16x16x32_bf16 v[156:159], a[52:55], v[160:163], v[156:159]\n"
"s_waitcnt lgkmcnt(0)\n"
"s_barrier\n"
"v_mfma_f32_16x16x32_bf16 v[156:159], a[48:51], v[104:107], v[156:159]\n"
"ds_read_b128 v[168:171], v6\n"
"ds_read_b128 v[176:179], v6 offset:1024\n"
"v_mfma_f32_16x16x32_bf16 v[156:159], a[44:47], v[144:147], v[156:159]\n"
"buffer_load_dwordx4 v[200:203], v0, s[4:7], s98 offen offset:2048 sc1\n"
"v_mfma_f32_16x16x32_bf16 v[156:159], a[40:43], v[68:71], v[156:159]\n"
"v_add_f32_e32 v208, v208, v216\n"
"v_add_f32_e32 v209, v209, v217\n"
"v_mfma_f32_16x16x32_bf16 v[156:159], a[36:39], v[92:95], v[156:159]\n"
"v_add_f32_e32 v210, v210, v218\n"
"v_add_f32_e32 v211, v211, v219\n"
"v_mfma_f32_16x16x32_bf16 v[156:159], a[32:35], v[44:47], v[156:159]\n"
"v_fmac_f32_e32 v124, v253, v192\n"
"v_fmac_f32_e32 v125, v253, v193\n"
"v_mfma_f32_16x16x32_bf16 v[196:199], a[64:67], v[44:47], v[196:199]\n"
"v_fmac_f32_e32 v126, v253, v194\n"
"v_fmac_f32_e32 v127, v253, v195\n"
"v_mfma_f32_16x16x32_bf16 v[196:199], a[68:71], v[92:95], v[196:199]\n"
"buffer_load_dwordx4 v[192:195], v0, s[4:7], s98 offen offset:3072 sc1\n"
"v_mfma_f32_16x16x32_bf16 v[196:199], a[72:75], v[68:71], v[196:199]\n"
"v_add_f32_e32 v124, v124, v84\n"
"v_add_f32_e32 v125, v125, v85\n"
"v_mfma_f32_16x16x32_bf16 v[196:199], a[76:79], v[144:147], v[196:199]\n"
"v_add_f32_e32 v126, v126, v86\n"
"v_add_f32_e32 v127, v127, v87\n"
"v_mfma_f32_16x16x32_bf16 v[196:199], a[80:83], v[104:107], v[196:199]\n"
"s_waitcnt lgkmcnt(0)\n"
"v_fmac_f32_e32 v136, v253, v140\n"
"v_fmac_f32_e32 v137, v253, v141\n"
"v_mfma_f32_16x16x32_bf16 v[196:199], a[84:87], v[160:163], v[196:199]\n"
"v_fmac_f32_e32 v138, v253, v142\n"
"v_fmac_f32_e32 v139, v253, v143\n"
"v_mfma_f32_16x16x32_bf16 v[196:199], a[88:91], v[172:175], v[196:199]\n"
"buffer_load_dwordx4 v[84:87], v1, s[4:7], s98 offen sc1\n"
"v_mfma_f32_16x16x32_bf16 v[196:199], a[92:95], v[184:187], v[196:199]\n"
"ds_read_b128 v[140:143], v6 offset:2048\n"
"ds_read_b128 v[204:207], v6 offset:3072\n"
"v_mfma_f32_16x16x32_bf16 v[208:211], a[124:127], v[184:187], v[208:211]\n"
"buffer_load_dwordx4 a[196:199], v0, s[4:7], s97 offen offset:1024 sc1\n"
"v_mfma_f32_16x16x32_bf16 v[208:211], a[120:123], v[172:175], v[208:211]\n"
"v_add_f32_e32 v136, v136, v148\n"
"v_add_f32_e32 v137, v137, v149\n"
"v_mfma_f32_16x16x32_bf16 v[208:211], a[116:119], v[160:163], v[208:211]\n"
"v_add_f32_e32 v138, v138, v150\n"
"v_add_f32_e32 v139, v139, v151\n"
"v_mfma_f32_16x16x32_bf16 v[208:211], a[112:115], v[104:107], v[208:211]\n"
"buffer_load_dwordx4 v[148:151], v1, s[4:7], s98 offen offset:1024 sc1\n"
"v_mfma_f32_16x16x32_bf16 v[208:211], a[108:111], v[144:147], v[208:211]\n"
"v_add_f32_e32 v32, v32, v168\n"
"v_add_f32_e32 v33, v33, v169\n"
"v_mfma_f32_16x16x32_bf16 v[208:211], a[104:107], v[68:71], v[208:211]\n"
"v_add_f32_e32 v34, v34, v170\n"
"v_add_f32_e32 v35, v35, v171\n"
"v_mfma_f32_16x16x32_bf16 v[208:211], a[100:103], v[92:95], v[208:211]\n"
"v_add_f32_e32 v156, v156, v176\n"
"v_add_f32_e32 v157, v157, v177\n"
"v_mfma_f32_16x16x32_bf16 v[208:211], a[96:99], v[44:47], v[208:211]\n"
"v_add_f32_e32 v158, v158, v178\n"
"v_add_f32_e32 v159, v159, v179\n"
"v_mfma_f32_16x16x32_bf16 v[124:127], v[24:27], v[44:47], v[124:127]\n"
"buffer_load_dwordx4 v[168:171], v1, s[4:7], s98 offen offset:2048 sc1\n"
"v_mfma_f32_16x16x32_bf16 v[124:127], v[56:59], v[92:95], v[124:127]\n"
"v_mul_f32_e32 v32, v32, v255\n"
"v_mul_f32_e32 v33, v33, v255\n"
"v_mfma_f32_16x16x32_bf16 v[124:127], v[28:31], v[68:71], v[124:127]\n"
"v_mul_f32_e32 v34, v34, v255\n"
"v_mul_f32_e32 v35, v35, v255\n"
"v_mfma_f32_16x16x32_bf16 v[124:127], v[64:67], v[144:147], v[124:127]\n"
"v_mul_f32_e32 v156, v156, v255\n"
"v_mul_f32_e32 v157, v157, v255\n"
"v_mfma_f32_16x16x32_bf16 v[124:127], v[52:55], v[104:107], v[124:127]\n"
"s_waitcnt lgkmcnt(0)\n"
"v_mul_f32_e32 v158, v158, v255\n"
"v_mul_f32_e32 v159, v159, v255\n"
"v_mfma_f32_16x16x32_bf16 v[124:127], v[36:39], v[160:163], v[124:127]\n"
"buffer_load_dwordx4 v[176:179], v1, s[4:7], s98 offen offset:3072 sc1\n"
"v_mfma_f32_16x16x32_bf16 v[124:127], v[108:111], v[172:175], v[124:127]\n"
"ds_read_b128 v[212:215], v6 offset:4096\n"
"ds_read_b128 v[216:219], v6 offset:5120\n"
"v_mfma_f32_16x16x32_bf16 v[124:127], v[60:63], v[184:187], v[124:127]\n"
"v_cvt_pk_bf16_f32 v224, v32, v33\n"
"v_cvt_pk_bf16_f32 v225, v34, v35\n"
"v_mfma_f32_16x16x32_bf16 v[136:139], v[80:83], v[184:187], v[136:139]\n"
"v_cvt_pk_bf16_f32 v226, v156, v157\n"
"v_cvt_pk_bf16_f32 v227, v158, v159\n"
"v_mfma_f32_16x16x32_bf16 v[136:139], v[88:91], v[172:175], v[136:139]\n"
"ds_write_b128 v8, v[224:227] offset:17408\n"
"v_mfma_f32_16x16x32_bf16 v[136:139], v[100:103], v[160:163], v[136:139]\n"
"v_add_f32_e32 v196, v196, v140\n"
"v_add_f32_e32 v197, v197, v141\n"
"v_mfma_f32_16x16x32_bf16 v[136:139], v[76:79], v[104:107], v[136:139]\n"
"v_add_f32_e32 v198, v198, v142\n"
"v_add_f32_e32 v199, v199, v143\n"
"v_mfma_f32_16x16x32_bf16 v[136:139], v[132:135], v[144:147], v[136:139]\n"
"v_add_f32_e32 v208, v208, v204\n"
"v_add_f32_e32 v209, v209, v205\n"
"v_mfma_f32_16x16x32_bf16 v[136:139], v[128:131], v[68:71], v[136:139]\n"
"v_add_f32_e32 v210, v210, v206\n"
"v_add_f32_e32 v211, v211, v207\n"
"v_mfma_f32_16x16x32_bf16 v[136:139], v[48:51], v[92:95], v[136:139]\n"
"v_mul_f32_e32 v196, v196, v255\n"
"v_mul_f32_e32 v197, v197, v255\n"
"v_mfma_f32_16x16x32_bf16 v[136:139], v[72:75], v[44:47], v[136:139]\n"
"v_mul_f32_e32 v198, v198, v255\n"
"v_mul_f32_e32 v199, v199, v255\n"
"v_mul_f32_e32 v208, v208, v255\n"
"v_mul_f32_e32 v209, v209, v255\n"
"v_mul_f32_e32 v210, v210, v255\n"
"v_mul_f32_e32 v211, v211, v255\n"
"v_cvt_pk_bf16_f32 v32, v196, v197\n"
"v_cvt_pk_bf16_f32 v33, v198, v199\n"
"v_cvt_pk_bf16_f32 v34, v208, v209\n"
"v_cvt_pk_bf16_f32 v35, v210, v211\n"
"ds_write_b128 v7, v[32:35] offset:50176\n"
".Lmk_a3:\n"
"s_waitcnt vmcnt(16)\n"
"; @A G1 3.0\n"
"v_mfma_f32_16x16x32_bf16 v[44:47], a[0:3], v[96:99], 0\n"
"v_mfma_f32_16x16x32_bf16 v[44:47], a[4:7], v[112:115], v[44:47]\n"
"s_add_u32 s98, s62, 0\n"
"v_mfma_f32_16x16x32_bf16 v[44:47], a[8:11], v[188:191], v[44:47]\n"
"s_add_u32 s96, s54, 0\n"
"buffer_load_dwordx4 v[68:71], v0, s[4:7], s96 offen sc1\n"
"v_mfma_f32_16x16x32_bf16 v[44:47], a[12:15], v[120:123], v[44:47]\n"
"s_waitcnt vmcnt(11)\n"
"v_mfma_f32_16x16x32_bf16 v[44:47], a[16:19], v[116:119], v[44:47]\n"
"buffer_load_dword v255, v10, s[4:7], s98 offen sc1\n"
"v_mfma_f32_16x16x32_bf16 v[44:47], a[20:23], v[220:223], v[44:47]\n"
"s_waitcnt lgkmcnt(2)\n"
"v_add_f32_e32 v124, v124, v212\n"
"v_add_f32_e32 v125, v125, v213\n"
"v_mfma_f32_16x16x32_bf16 v[44:47], a[24:27], v[180:183], v[44:47]\n"
"v_add_f32_e32 v126, v126, v214\n"
"v_add_f32_e32 v127, v127, v215\n"
"v_mfma_f32_16x16x32_bf16 v[44:47], a[28:31], v[40:43], v[44:47]\n"
"buffer_load_dwordx4 v[92:95], v0, s[4:7], s96 offen offset:1024 sc1\n"
"v_mfma_f32_16x16x32_bf16 v[104:107], a[60:63], v[40:43], 0\n"
"buffer_load_dwordx4 a[200:203], v0, s[4:7], s97 offen offset:2048 sc1\n"
"v_mfma_f32_16x16x32_bf16 v[104:107], a[56:59], v[180:183], v[104:107]\n"
"buffer_load_dword v253, v10, s[4:7], s98 offen offset:256 sc1\n"
"v_mfma_f32_16x16x32_bf16 v[104:107], a[52:55], v[220:223], v[104:107]\n"
"v_add_f32_e32 v136, v136, v216\n"
"v_add_f32_e32 v137, v137, v217\n"
"v_mfma_f32_16x16x32_bf16 v[104:107], a[48:51], v[116:119], v[104:107]\n"
"v_add_f32_e32 v138, v138, v218\n"
"v_add_f32_e32 v139, v139, v219\n"
"v_mfma_f32_16x16x32_bf16 v[104:107], a[44:47], v[120:123], v[104:107]\n"
"buffer_load_dwordx4 v[140:143], v0, s[4:7], s96 offen offset:2048 sc1\n"
"v_mfma_f32_16x16x32_bf16 v[104:107], a[40:43], v[188:191], v[104:107]\n"
"ds_write_b128 v3, v[44:47]\n"
"v_mfma_f32_16x16x32_bf16 v[104:107], a[36:39], v[112:115], v[104:107]\n"
"buffer_load_dword v252, v10, s[4:7], s98 offen offset:512 sc1\n"
"v_mfma_f32_16x16x32_bf16 v[104:107], a[32:35], v[96:99], v[104:107]\n"
"v_mul_f32_e32 v124, v124, v254\n"
"v_mul_f32_e32 v125, v125, v254\n"
"v_mfma_f32_16x16x32_bf16 v[44:47], a[64:67], v[96:99], 0\n"
"v_mul_f32_e32 v126, v126, v254\n"
"v_mul_f32_e32 v127, v127, v254\n"
"v_mfma_f32_16x16x32_bf16 v[44:47], a[68:71], v[112:115], v[44:47]\n"
"buffer_load_dwordx4 v[144:147], v0, s[4:7], s96 offen offset:3072 sc1\n"
"v_mfma_f32_16x16x32_bf16 v[44:47], a[72:75], v[188:191], v[44:47]\n"
"buffer_load_dword v251, v10, s[4:7], s98 offen offset:768 sc1\n"
"v_mfma_f32_16x16x32_bf16 v[44:47], a[76:79], v[120:123], v[44:47]\n"
"v_mul_f32_e32 v136, v136, v254\n"
"v_mul_f32_e32 v137, v137, v254\n"
"v_mfma_f32_16x16x32_bf16 v[44:47], a[80:83], v[116:119], v[44:47]\n"
"v_mul_f32_e32 v138, v138, v254\n"
"v_mul_f32_e32 v139, v139, v254\n"
"v_mfma_f32_16x16x32_bf16 v[44:47], a[84:87], v[220:223], v[44:47]\n"
"ds_write_b128 v3, v[104:107] offset:1024\n"
"v_mfma_f32_16x16x32_bf16 v[44:47], a[88:91], v[180:183], v[44:47]\n"
"buffer_load_dwordx4 v[104:107], v1, s[4:7], s96 offen sc1\n"
"v_mfma_f32_16x16x32_bf16 v[44:47], a[92:95], v[40:43], v[44:47]\n"
"buffer_load_dword v254, v10, s[4:7], s98 offen offset:1024 sc1\n"
"v_mfma_f32_16x16x32_bf16 v[156:159], a[124:127], v[40:43], 0\n"
"v_cvt_pk_bf16_f32 v160, v124, v125\n"
"v_cvt_pk_bf16_f32 v161, v126, v127\n"
"v_mfma_f32_16x16x32_bf16 v[156:159], a[120:123], v[180:183], v[156:159]\n"
"v_cvt_pk_bf16_f32 v162, v136, v137\n"
"v_cvt_pk_bf16_f32 v163, v138, v139\n"
"v_mfma_f32_16x16x32_bf16 v[156:159], a[116:119], v[220:223], v[156:159]\n"
"buffer_load_dwordx4 a[204:207], v0, s[4:7], s97 offen offset:3072 sc1\n"
"v_mfma_f32_16x16x32_bf16 v[156:159], a[112:115], v[116:119], v[156:159]\n"
"buffer_load_dwordx4 v[124:127], v1, s[4:7], s96 offen offset:1024 sc1\n"
"v_mfma_f32_16x16x32_bf16 v[156:159], a[108:111], v[120:123], v[156:159]\n"
"buffer_load_dword v250, v10, s[4:7], s98 offen offset:1280 sc1\n"
"v_mfma_f32_16x16x32_bf16 v[156:159], a[104:107], v[188:191], v[156:159]\n"
"ds_write_b128 v3, v[44:47] offset:2048\n"
"v_mfma_f32_16x16x32_bf16 v[156:159], a[100:103], v[112:115], v[156:159]\n"
"ds_write_b128 v2, v[160:163] offset:17408\n"
"v_mfma_f32_16x16x32_bf16 v[156:159], a[96:99], v[96:99], v[156:159]\n"
"buffer_load_dword v249, v10, s[4:7], s98 offen offset:1536 sc1\n"
"v_mfma_f32_16x16x32_bf16 v[44:47], v[24:27], v[96:99], 0\n"
"buffer_load_dwordx4 v[136:139], v1, s[4:7], s96 offen offset:2048 sc1\n"
"v_mfma_f32_16x16x32_bf16 v[44:47], v[56:59], v[112:115], v[44:47]\n"
"buffer_store_dwordx4 v[224:227], v8, s[4:7], s64 offen offset:1024 sc1\n"
"v_mfma_f32_16x16x32_bf16 v[44:47], v[28:31], v[188:191], v[44:47]\n"
"buffer_load_dword v248, v10, s[4:7], s98 offen offset:1792 sc1\n"
"v_mfma_f32_16x16x32_bf16 v[44:47], v[64:67], v[120:123], v[44:47]\n"
"v_mfma_f32_16x16x32_bf16 v[44:47], v[52:55], v[116:119], v[44:47]\n"
"v_mfma_f32_16x16x32_bf16 v[44:47], v[36:39], v[220:223], v[44:47]\n"
"buffer_load_dwordx4 v[160:163], v1, s[4:7], s96 offen offset:3072 sc1\n"
"v_mfma_f32_16x16x32_bf16 v[44:47], v[108:111], v[180:183], v[44:47]\n"
"ds_write_b128 v3, v[156:159] offset:3072\n"
"v_mfma_f32_16x16x32_bf16 v[44:47], v[60:63], v[40:43], v[44:47]\n"
"v_mfma_f32_16x16x32_bf16 v[156:159], v[80:83], v[40:43], 0\n"
"v_mfma_f32_16x16x32_bf16 v[156:159], v[88:91], v[180:183], v[156:159]\n"
"v_mfma_f32_16x16x32_bf16 v[156:159], v[100:103], v[220:223], v[156:159]\n"
"v_mfma_f32_16x16x32_bf16 v[156:159], v[76:79], v[116:119], v[156:159]\n"
"v_mfma_f32_16x16x32_bf16 v[156:159], v[132:135], v[120:123], v[156:159]\n"
"buffer_load_dwordx4 a[208:211], v1, s[4:7], s97 offen sc1\n"
"v_mfma_f32_16x16x32_bf16 v[156:159], v[128:131], v[188:191], v[156:159]\n"
"s_nop 0\n"
"ds_write_b128 v3, v[44:47] offset:4096\n"
"v_mfma_f32_16x16x32_bf16 v[156:159], v[48:51], v[112:115], v[156:159]\n"
"v_mfma_f32_16x16x32_bf16 v[156:159], v[72:75], v[96:99], v[156:159]\n"
"s_waitcnt vmcnt(25)\n"
"; @A G1 3.1\n"
"v_mfma_f32_16x16x32_bf16 v[40:43], a[0:3], v[152:155], 0\n"
"v_mfma_f32_16x16x32_bf16 v[40:43], a[4:7], v[164:167], v[40:43]\n"
"v_mfma_f32_16x16x32_bf16 v[40:43], a[8:11], v[200:203], v[40:43]\n"
"s_add_u32 s98, s55, 0\n"
"buffer_load_dwordx4 v[44:47], v0, s[4:7], s98 offen sc1\n"
"v_mfma_f32_16x16x32_bf16 v[40:43], a[12:15], v[192:195], v[40:43]\n"
"s_waitcnt vmcnt(21)\n"
"v_mfma_f32_16x16x32_bf16 v[40:43], a[16:19], v[84:87], v[40:43]\n"
"v_mfma_f32_16x16x32_bf16 v[40:43], a[20:23], v[148:151], v[40:43]\n"
"ds_write_b128 v3, v[156:159] offset:5120\n"
"v_mfma_f32_16x16x32_bf16 v[40:43], a[24:27], v[168:171], v[40:43]\n"
"v_mfma_f32_16x16x32_bf16 v[40:43], a[28:31], v[176:179], v[40:43]\n"
"buffer_load_dwordx4 v[96:99], v0, s[4:7], s98 offen offset:1024 sc1\n"
"s_add_u32 s96, s64, 0x4000\n"
"v_mfma_f32_16x16x32_bf16 v[112:115], a[60:63], v[176:179], 0\n"
"v_mfma_f32_16x16x32_bf16 v[112:115], a[56:59], v[168:171], v[112:115]\n"
"buffer_store_dwordx4 v[32:35], v7, s[4:7], s96 offen offset:1024 sc1\n"
"v_mfma_f32_16x16x32_bf16 v[112:115], a[52:55], v[148:151], v[112:115]\n"
"v_mfma_f32_16x16x32_bf16 v[112:115], a[48:51], v[84:87], v[112:115]\n"
"v_mfma_f32_16x16x32_bf16 v[112:115], a[44:47], v[192:195], v[112:115]\n"
"buffer_load_dwordx4 v[32:35], v0, s[4:7], s98 offen offset:2048 sc1\n"
"v_mfma_f32_16x16x32_bf16 v[112:115], a[40:43], v[200:203], v[112:115]\n"
"ds_write_b128 v3, v[40:43] offset:6144\n"
"v_mfma_f32_16x16x32_bf16 v[112:115], a[36:39], v[164:167], v[112:115]\n"
"buffer_load_dwordx4 a[212:215], v1, s[4:7], s97 offen offset:1024 sc1\n"
"v_mfma_f32_16x16x32_bf16 v[112:115], a[32:35], v[152:155], v[112:115]\n"
"v_mfma_f32_16x16x32_bf16 v[40:43], a[64:67], v[152:155], 0\n"
"v_mfma_f32_16x16x32_bf16 v[40:43], a[68:71], v[164:167], v[40:43]\n"
"buffer_load_dwordx4 v[116:119], v0, s[4:7], s98 offen offset:3072 sc1\n"
"v_mfma_f32_16x16x32_bf16 v[40:43], a[72:75], v[200:203], v[40:43]\n"
"v_mfma_f32_16x16x32_bf16 v[40:43], a[76:79], v[192:195], v[40:43]\n"
"v_mfma_f32_16x16x32_bf16 v[40:43], a[80:83], v[84:87], v[40:43]\n"
"v_mfma_f32_16x16x32_bf16 v[40:43], a[84:87], v[148:151], v[40:43]\n"
"s_nop 0\n"
"ds_write_b128 v3, v[112:115] offset:7168\n"
"v_mfma_f32_16x16x32_bf16 v[40:43], a[88:91], v[168:171], v[40:43]\n"
"buffer_load_dwordx4 v[112:115], v1, s[4:7], s98 offen sc1\n"
"v_mfma_f32_16x16x32_bf16 v[40:43], a[92:95], v[176:179], v[40:43]\n"
"v_mfma_f32_16x16x32_bf16 v[120:123], a[124:127], v[176:179], 0\n"
"v_mfma_f32_16x16x32_bf16 v[120:123], a[120:123], v[168:171], v[120:123]\n"
"v_mfma_f32_16x16x32_bf16 v[120:123], a[116:119], v[148:151], v[120:123]\n"
"v_mfma_f32_16x16x32_bf16 v[120:123], a[112:115], v[84:87], v[120:123]\n"
"buffer_load_dwordx4 v[156:159], v1, s[4:7], s98 offen offset:1024 sc1\n"
"v_mfma_f32_16x16x32_bf16 v[120:123], a[108:111], v[192:195], v[120:123]\n"
"v_mfma_f32_16x16x32_bf16 v[120:123], a[104:107], v[200:203], v[120:123]\n"
"s_nop 0\n"
"ds_write_b128 v3, v[40:43] offset:8192\n"
"v_mfma_f32_16x16x32_bf16 v[120:123], a[100:103], v[164:167], v[120:123]\n"
"v_mfma_f32_16x16x32_bf16 v[120:123], a[96:99], v[152:155], v[120:123]\n"
"v_mfma_f32_16x16x32_bf16 v[40:43], v[24:27], v[152:155], 0\n"
"buffer_load_dwordx4 v[172:175], v1, s[4:7], s98 offen offset:2048 sc1\n"
"v_mfma_f32_16x16x32_bf16 v[40:43], v[56:59], v[164:167], v[40:43]\n"
"buffer_load_dwordx4 a[216:219], v1, s[4:7], s97 offen offset:2048 sc1\n"
"v_mfma_f32_16x16x32_bf16 v[40:43], v[28:31], v[200:203], v[40:43]\n"
"v_mfma_f32_16x16x32_bf16 v[40:43], v[64:67], v[192:195], v[40:43]\n"
"v_mfma_f32_16x16x32_bf16 v[40:43], v[52:55], v[84:87], v[40:43]\n"
"s_waitcnt vmcnt(12)\n"
"v_add_f32_e32 v255, v255, v254\n"
"v_add_f32_e32 v253, v253, v250\n"
"v_mfma_f32_16x16x32_bf16 v[40:43], v[36:39], v[148:151], v[40:43]\n"
"buffer_load_dwordx4 v[180:183], v1, s[4:7], s98 offen offset:3072 sc1\n"
"v_mfma_f32_16x16x32_bf16 v[40:43], v[108:111], v[168:171], v[40:43]\n"
"ds_write_b128 v3, v[120:123] offset:9216\n"
"v_mfma_f32_16x16x32_bf16 v[40:43], v[60:63], v[176:179], v[40:43]\n"
"v_add_f32_e32 v252, v252, v249\n"
"v_add_f32_e32 v251, v251, v248\n"
"v_mfma_f32_16x16x32_bf16 v[120:123], v[80:83], v[176:179], 0\n"
"v_add_f32_e32 v255, v255, v252\n"
"v_add_f32_e32 v253, v253, v251\n"
"v_mfma_f32_16x16x32_bf16 v[120:123], v[88:91], v[168:171], v[120:123]\n"
"v_add_f32_e32 v255, v255, v253\n"
"v_mov_b32_e32 v253, v255\n"
"v_mfma_f32_16x16x32_bf16 v[120:123], v[100:103], v[148:151], v[120:123]\n"
"s_add_u32 s98, s71, 0\n"
"buffer_load_dwordx2 v[250:251], off, s[4:7], s98 sc1\n"
"v_mfma_f32_16x16x32_bf16 v[120:123], v[76:79], v[84:87], v[120:123]\n"
"v_permlane32_swap_b32_e32 v255, v253\n"
"v_add_f32_e32 v255, v255, v253\n"
"v_mfma_f32_16x16x32_bf16 v[120:123], v[132:135], v[192:195], v[120:123]\n"
"v_mul_f32_e32 v255, 0x3a800000, v255\n"
"v_mov_b32_e32 v253, v255\n"
"v_mfma_f32_16x16x32_bf16 v[120:123], v[128:131], v[200:203], v[120:123]\n"
"ds_write_b128 v3, v[40:43] offset:10240\n"
"v_mfma_f32_16x16x32_bf16 v[120:123], v[48:51], v[164:167], v[120:123]\n"
"buffer_store_dwordx2 v[18:19], v11, s[4:7], s68 offen offset:32 sc1\n"
"v_mfma_f32_16x16x32_bf16 v[120:123], v[72:75], v[152:155], v[120:123]\n"
"v_permlane16_swap_b32_e32 v255, v253\n"
"v_cvt_pk_bf16_f32 v254, -v255, 0\n"
"; @A G1 3.2\n"
"v_mfma_f32_16x16x32_bf16 v[40:43], a[0:3], v[68:71], 0\n"
"v_fma_f32 v253, -v255, v255, v253\n"
"v_lshlrev_b32_e32 v254, 16, v254\n"
"v_mfma_f32_16x16x32_bf16 v[40:43], a[4:7], v[92:95], v[40:43]\n"
"v_max_f32_e32 v253, 0, v253\n"
"v_cvt_pk_bf16_f32 v253, v253, 0\n"
"v_mfma_f32_16x16x32_bf16 v[40:43], a[8:11], v[140:143], v[40:43]\n"
"buffer_load_dwordx4 a[220:223], v1, s[4:7], s97 offen offset:3072 sc1\n"
"v_mfma_f32_16x16x32_bf16 v[40:43], a[12:15], v[144:147], v[40:43]\n"
"s_waitcnt vmcnt(15)\n"
"v_lshlrev_b32_e32 v253, 16, v253\n"
"v_add_f32_e32 v253, 0x3727c5ac, v253\n"
"v_mfma_f32_16x16x32_bf16 v[40:43], a[16:19], v[104:107], v[40:43]\n"
"s_add_u32 s97, s70, 0x6000\n"
"v_cvt_pk_bf16_f32 v253, v253, 0\n"
"v_lshlrev_b32_e32 v253, 16, v253\n"
"v_mfma_f32_16x16x32_bf16 v[40:43], a[20:23], v[124:127], v[40:43]\n"
"ds_write_b128 v3, v[120:123] offset:11264\n"
"v_mfma_f32_16x16x32_bf16 v[40:43], a[24:27], v[136:139], v[40:43]\n"
"buffer_load_dwordx4 a[224:227], v0, s[4:7], s97 offen sc1\n"
"v_mfma_f32_16x16x32_bf16 v[40:43], a[28:31], v[160:163], v[40:43]\n"
"v_sqrt_f32_e32 v253, v253\n"
"v_mfma_f32_16x16x32_bf16 v[84:87], a[60:63], v[160:163], 0\n"
"v_cvt_pk_bf16_f32 v253, v253, 0\n"
"v_lshlrev_b32_e32 v253, 16, v253\n"
"v_mfma_f32_16x16x32_bf16 v[84:87], a[56:59], v[136:139], v[84:87]\n"
"v_rcp_f32_e32 v255, v253\n"
"v_mfma_f32_16x16x32_bf16 v[84:87], a[52:55], v[124:127], v[84:87]\n"
"s_waitcnt lgkmcnt(0)\n"
"s_barrier\n"
"v_mfma_f32_16x16x32_bf16 v[84:87], a[48:51], v[104:107], v[84:87]\n"
"buffer_load_dwordx4 a[228:231], v0, s[4:7], s97 offen offset:1024 sc1\n"
"v_mfma_f32_16x16x32_bf16 v[84:87], a[44:47], v[144:147], v[84:87]\n"
"ds_read_b128 v[120:123], v4\n"
"v_mul_f32_e32 v253, 0x3e8293ee, v255\n"
"v_mfma_f32_16x16x32_bf16 v[84:87], a[40:43], v[140:143], v[84:87]\n"
"ds_write_b128 v3, v[40:43] offset:12288\n"
"v_mfma_f32_16x16x32_bf16 v[84:87], a[36:39], v[92:95], v[84:87]\n"
"ds_read_b128 v[40:43], v9 offset:128\n"
"ds_read_b128 v[148:151], v5\n"
"v_mfma_f32_16x16x32_bf16 v[84:87], a[32:35], v[68:71], v[84:87]\n"
"buffer_load_dwordx4 a[232:235], v0, s[4:7], s97 offen offset:2048 sc1\n"
"v_mfma_f32_16x16x32_bf16 v[152:155], a[64:67], v[68:71], 0\n"
"v_mfma_f32_16x16x32_bf16 v[152:155], a[68:71], v[92:95], v[152:155]\n"
"v_mfma_f32_16x16x32_bf16 v[152:155], a[72:75], v[140:143], v[152:155]\n"
"ds_read_b128 v[164:167], v4 offset:1024\n"
"ds_read_b128 v[168:171], v9 offset:192\n"
"v_mfma_f32_16x16x32_bf16 v[152:155], a[76:79], v[144:147], v[152:155]\n"
"buffer_load_dwordx4 a[236:239], v0, s[4:7], s97 offen offset:3072 sc1\n"
"v_mfma_f32_16x16x32_bf16 v[152:155], a[80:83], v[104:107], v[152:155]\n"
"ds_read_b128 v[176:179], v5 offset:1024\n"
"s_waitcnt lgkmcnt(3)\n"
"v_mfma_f32_16x16x32_bf16 v[152:155], a[84:87], v[124:127], v[152:155]\n"
"ds_write_b128 v3, v[84:87] offset:13312\n"
"v_mfma_f32_16x16x32_bf16 v[152:155], a[88:91], v[136:139], v[152:155]\n"
"v_fmac_f32_e32 v120, v254, v40\n"
"v_fmac_f32_e32 v121, v254, v41\n"
"v_mfma_f32_16x16x32_bf16 v[152:155], a[92:95], v[160:163], v[152:155]\n"
"buffer_load_dwordx4 a[240:243], v1, s[4:7], s97 offen sc1\n"
"v_mfma_f32_16x16x32_bf16 v[84:87], a[124:127], v[160:163], 0\n"
"v_fmac_f32_e32 v122, v254, v42\n"
"v_fmac_f32_e32 v123, v254, v43\n"
"v_mfma_f32_16x16x32_bf16 v[84:87], a[120:123], v[136:139], v[84:87]\n"
"ds_read_b128 v[40:43], v4 offset:2048\n"
"ds_read_b128 v[184:187], v9 offset:256\n"
"v_mfma_f32_16x16x32_bf16 v[84:87], a[116:119], v[124:127], v[84:87]\n"
"ds_read_b128 v[188:191], v5 offset:2048\n"
"v_add_f32_e32 v120, v120, v148\n"
"v_mfma_f32_16x16x32_bf16 v[84:87], a[112:115], v[104:107], v[84:87]\n"
"buffer_load_dwordx4 a[244:247], v1, s[4:7], s97 offen offset:1024 sc1\n"
"v_mfma_f32_16x16x32_bf16 v[84:87], a[108:111], v[144:147], v[84:87]\n"
"v_add_f32_e32 v121, v121, v149\n"
"v_add_f32_e32 v122, v122, v150\n"
"v_mfma_f32_16x16x32_bf16 v[84:87], a[104:107], v[140:143], v[84:87]\n"
"ds_write_b128 v3, v[152:155] offset:14336\n"
"v_mfma_f32_16x16x32_bf16 v[84:87], a[100:103], v[92:95], v[84:87]\n"
"s_waitcnt lgkmcnt(5)\n"
"v_add_f32_e32 v123, v123, v151\n"
"v_fmac_f32_e32 v164, v254, v168\n"
"v_mfma_f32_16x16x32_bf16 v[84:87], a[96:99], v[68:71], v[84:87]\n"
"buffer_load_dwordx4 a[248:251], v1, s[4:7], s97 offen offset:2048 sc1\n"
"v_mfma_f32_16x16x32_bf16 v[148:151], v[24:27], v[68:71], 0\n"
"ds_read_b128 v[152:155], v4 offset:3072\n"
"ds_read_b128 v[192:195], v9 offset:320\n"
"v_mfma_f32_16x16x32_bf16 v[148:151], v[56:59], v[92:95], v[148:151]\n"
"ds_read_b128 v[196:199], v5 offset:3072\n"
"v_fmac_f32_e32 v165, v254, v169\n"
"v_mfma_f32_16x16x32_bf16 v[148:151], v[28:31], v[140:143], v[148:151]\n"
"v_fmac_f32_e32 v166, v254, v170\n"
"v_fmac_f32_e32 v167, v254, v171\n"
"v_mfma_f32_16x16x32_bf16 v[148:151], v[64:67], v[144:147], v[148:151]\n"
"buffer_load_dwordx4 a[252:255], v1, s[4:7], s97 offen offset:3072 sc1\n"
"v_mfma_f32_16x16x32_bf16 v[148:151], v[52:55], v[104:107], v[148:151]\n"
"v_add_f32_e32 v164, v164, v176\n"
"v_add_f32_e32 v165, v165, v177\n"
"v_mfma_f32_16x16x32_bf16 v[148:151], v[36:39], v[124:127], v[148:151]\n"
"ds_write_b128 v3, v[84:87] offset:15360\n"
"v_mfma_f32_16x16x32_bf16 v[148:151], v[108:111], v[136:139], v[148:151]\n"
"s_waitcnt lgkmcnt(5)\n"
"v_add_f32_e32 v166, v166, v178\n"
"v_add_f32_e32 v167, v167, v179\n"
"v_mfma_f32_16x16x32_bf16 v[148:151], v[60:63], v[160:163], v[148:151]\n"
"ds_read_b128 v[84:87], v4 offset:4096\n"
"ds_read_b128 v[168:171], v9\n"
"v_mfma_f32_16x16x32_bf16 v[176:179], v[80:83], v[160:163], 0\n"
"ds_read_b128 v[160:163], v5 offset:4096\n"
"v_fmac_f32_e32 v40, v254, v184\n"
"v_mfma_f32_16x16x32_bf16 v[176:179], v[88:91], v[136:139], v[176:179]\n"
"v_fmac_f32_e32 v41, v254, v185\n"
"v_fmac_f32_e32 v42, v254, v186\n"
"v_mfma_f32_16x16x32_bf16 v[176:179], v[100:103], v[124:127], v[176:179]\n"
"v_fmac_f32_e32 v43, v254, v187\n"
"v_add_f32_e32 v40, v40, v188\n"
"v_mfma_f32_16x16x32_bf16 v[176:179], v[76:79], v[104:107], v[176:179]\n"
"v_add_f32_e32 v41, v41, v189\n"
"v_add_f32_e32 v42, v42, v190\n"
"v_mfma_f32_16x16x32_bf16 v[176:179], v[132:135], v[144:147], v[176:179]\n"
"s_waitcnt vmcnt(10)\n"
"v_cmp_ne_u64_e32 vcc, v[250:251], v[18:19]\n"
"v_mfma_f32_16x16x32_bf16 v[176:179], v[128:131], v[140:143], v[176:179]\n"
"ds_write_b128 v3, v[148:151] offset:16384\n"
"v_mfma_f32_16x16x32_bf16 v[176:179], v[48:51], v[92:95], v[176:179]\n"
"s_cbranch_vccnz .Lmk_f73s\n"
".Lmk_f73r:\n"
"s_add_u32 m0, s77, 0x6000\n"
"v_mfma_f32_16x16x32_bf16 v[176:179], v[72:75], v[68:71], v[176:179]\n"
"s_add_u32 s98, s65, 0x2000\n"
"ds_read_b128 v[68:71], v4 offset:5120\n"
"; @A G1 3.3\n"
"v_mfma_f32_16x16x32_bf16 v[120:123], a[0:3], v[44:47], v[120:123]\n"
"buffer_load_dwordx4 v2, s[4:7], s98 offen sc1 lds\n"
"v_mfma_f32_16x16x32_bf16 v[120:123], a[4:7], v[96:99], v[120:123]\n"
"buffer_load_dwordx4 v2, s[4:7], s98 offen offset:1024 sc1 lds\n"
"v_mfma_f32_16x16x32_bf16 v[120:123], a[8:11], v[32:35], v[120:123]\n"
"ds_read_b128 v[92:95], v9 offset:64\n"
"ds_read_b128 v[104:107], v5 offset:5120\n"
"v_mfma_f32_16x16x32_bf16 v[120:123], a[12:15], v[116:119], v[120:123]\n"
"s_waitcnt lgkmcnt(8)\n"
"v_add_f32_e32 v43, v43, v191\n"
"v_mfma_f32_16x16x32_bf16 v[120:123], a[16:19], v[112:115], v[120:123]\n"
"v_fmac_f32_e32 v152, v254, v192\n"
"v_fmac_f32_e32 v153, v254, v193\n"
"v_mfma_f32_16x16x32_bf16 v[120:123], a[20:23], v[156:159], v[120:123]\n"
"ds_write_b128 v3, v[176:179] offset:17408\n"
"v_mfma_f32_16x16x32_bf16 v[120:123], a[24:27], v[172:175], v[120:123]\n"
"v_fmac_f32_e32 v154, v254, v194\n"
"v_fmac_f32_e32 v155, v254, v195\n"
"v_mfma_f32_16x16x32_bf16 v[120:123], a[28:31], v[180:183], v[120:123]\n"
"s_add_u32 m0, s77, 0xe000\n"
"s_add_u32 s98, s65, 0x6000\n"
"v_add_f32_e32 v152, v152, v196\n"
"v_mfma_f32_16x16x32_bf16 v[164:167], a[60:63], v[180:183], v[164:167]\n"
"buffer_load_dwordx4 v2, s[4:7], s98 offen sc1 lds\n"
"v_mfma_f32_16x16x32_bf16 v[164:167], a[56:59], v[172:175], v[164:167]\n"
"buffer_load_dwordx4 v2, s[4:7], s98 offen offset:1024 sc1 lds\n"
"v_mfma_f32_16x16x32_bf16 v[164:167], a[52:55], v[156:159], v[164:167]\n"
"s_waitcnt lgkmcnt(0)\n"
"s_barrier\n"
"v_mfma_f32_16x16x32_bf16 v[164:167], a[48:51], v[112:115], v[164:167]\n"
"ds_read_b128 v[124:127], v6\n"
"ds_read_b128 v[136:139], v6 offset:1024\n"
"v_mfma_f32_16x16x32_bf16 v[164:167], a[44:47], v[116:119], v[164:167]\n"
"s_add_u32 s98, s69, 0x600000\n"
"buffer_load_dwordx4 a[0:3], v0, s[4:7], s98 offen sc1\n"
"v_mfma_f32_16x16x32_bf16 v[164:167], a[40:43], v[32:35], v[164:167]\n"
"v_add_f32_e32 v153, v153, v197\n"
"v_add_f32_e32 v154, v154, v198\n"
"v_mfma_f32_16x16x32_bf16 v[164:167], a[36:39], v[96:99], v[164:167]\n"
"v_add_f32_e32 v155, v155, v199\n"
"v_fmac_f32_e32 v84, v254, v168\n"
"v_mfma_f32_16x16x32_bf16 v[164:167], a[32:35], v[44:47], v[164:167]\n"
"buffer_load_dwordx4 a[4:7], v0, s[4:7], s98 offen offset:1024 sc1\n"
"v_mfma_f32_16x16x32_bf16 v[40:43], a[64:67], v[44:47], v[40:43]\n"
"v_fmac_f32_e32 v85, v254, v169\n"
"v_fmac_f32_e32 v86, v254, v170\n"
"v_mfma_f32_16x16x32_bf16 v[40:43], a[68:71], v[96:99], v[40:43]\n"
"v_fmac_f32_e32 v87, v254, v171\n"
"v_add_f32_e32 v84, v84, v160\n"
"v_mfma_f32_16x16x32_bf16 v[40:43], a[72:75], v[32:35], v[40:43]\n"
"buffer_load_dwordx4 a[8:11], v0, s[4:7], s98 offen offset:2048 sc1\n"
"v_mfma_f32_16x16x32_bf16 v[40:43], a[76:79], v[116:119], v[40:43]\n"
"v_add_f32_e32 v85, v85, v161\n"
"v_add_f32_e32 v86, v86, v162\n"
"v_mfma_f32_16x16x32_bf16 v[40:43], a[80:83], v[112:115], v[40:43]\n"
"v_add_f32_e32 v87, v87, v163\n"
"v_fmac_f32_e32 v68, v254, v92\n"
"v_mfma_f32_16x16x32_bf16 v[40:43], a[84:87], v[156:159], v[40:43]\n"
"buffer_load_dwordx4 a[12:15], v0, s[4:7], s98 offen offset:3072 sc1\n"
"v_mfma_f32_16x16x32_bf16 v[40:43], a[88:91], v[172:175], v[40:43]\n"
"ds_read_b128 v[140:143], v6 offset:2048\n"
"ds_read_b128 v[144:147], v6 offset:3072\n"
"v_mfma_f32_16x16x32_bf16 v[40:43], a[92:95], v[180:183], v[40:43]\n"
"s_waitcnt lgkmcnt(2)\n"
"v_fmac_f32_e32 v69, v254, v93\n"
"v_fmac_f32_e32 v70, v254, v94\n"
"v_mfma_f32_16x16x32_bf16 v[152:155], a[124:127], v[180:183], v[152:155]\n"
"buffer_load_dwordx4 a[16:19], v1, s[4:7], s98 offen sc1\n"
"v_mfma_f32_16x16x32_bf16 v[152:155], a[120:123], v[172:175], v[152:155]\n"
"v_fmac_f32_e32 v71, v254, v95\n"
"v_add_f32_e32 v68, v68, v104\n"
"v_mfma_f32_16x16x32_bf16 v[152:155], a[116:119], v[156:159], v[152:155]\n"
"v_add_f32_e32 v69, v69, v105\n"
"v_add_f32_e32 v70, v70, v106\n"
"v_mfma_f32_16x16x32_bf16 v[152:155], a[112:115], v[112:115], v[152:155]\n"
"buffer_load_dwordx4 a[20:23], v1, s[4:7], s98 offen offset:1024 sc1\n"
"v_mfma_f32_16x16x32_bf16 v[152:155], a[108:111], v[116:119], v[152:155]\n"
"v_add_f32_e32 v71, v71, v107\n"
"v_add_f32_e32 v120, v120, v124\n"
"v_mfma_f32_16x16x32_bf16 v[152:155], a[104:107], v[32:35], v[152:155]\n"
"v_add_f32_e32 v121, v121, v125\n"
"v_add_f32_e32 v122, v122, v126\n"
"v_mfma_f32_16x16x32_bf16 v[152:155], a[100:103], v[96:99], v[152:155]\n"
"buffer_load_dwordx4 a[24:27], v1, s[4:7], s98 offen offset:2048 sc1\n"
"v_mfma_f32_16x16x32_bf16 v[152:155], a[96:99], v[44:47], v[152:155]\n"
"v_add_f32_e32 v123, v123, v127\n"
"v_add_f32_e32 v164, v164, v136\n"
"v_mfma_f32_16x16x32_bf16 v[84:87], v[24:27], v[44:47], v[84:87]\n"
"v_add_f32_e32 v165, v165, v137\n"
"v_add_f32_e32 v166, v166, v138\n"
"v_mfma_f32_16x16x32_bf16 v[84:87], v[56:59], v[96:99], v[84:87]\n"
"buffer_load_dwordx4 a[28:31], v1, s[4:7], s98 offen offset:3072 sc1\n"
"v_mfma_f32_16x16x32_bf16 v[84:87], v[28:31], v[32:35], v[84:87]\n"
"v_add_f32_e32 v167, v167, v139\n"
"v_mul_f32_e32 v120, v120, v255\n"
"v_mfma_f32_16x16x32_bf16 v[84:87], v[64:67], v[116:119], v[84:87]\n"
"v_mul_f32_e32 v121, v121, v255\n"
"v_mul_f32_e32 v122, v122, v255\n"
"v_mfma_f32_16x16x32_bf16 v[84:87], v[52:55], v[112:115], v[84:87]\n"
"s_waitcnt lgkmcnt(0)\n"
"v_mul_f32_e32 v123, v123, v255\n"
"v_mul_f32_e32 v164, v164, v255\n"
"v_mfma_f32_16x16x32_bf16 v[84:87], v[36:39], v[156:159], v[84:87]\n"
"v_mul_f32_e32 v165, v165, v255\n"
"v_mul_f32_e32 v166, v166, v255\n"
"v_mfma_f32_16x16x32_bf16 v[84:87], v[108:111], v[172:175], v[84:87]\n"
"ds_read_b128 v[24:27], v6 offset:4096\n"
"ds_read_b128 v[28:31], v6 offset:5120\n"
"v_mfma_f32_16x16x32_bf16 v[84:87], v[60:63], v[180:183], v[84:87]\n"
"v_mul_f32_e32 v167, v167, v255\n"
"v_cvt_pk_bf16_f32 v36, v120, v121\n"
"v_mfma_f32_16x16x32_bf16 v[68:71], v[80:83], v[180:183], v[68:71]\n"
"v_cvt_pk_bf16_f32 v37, v122, v123\n"
"v_cvt_pk_bf16_f32 v38, v164, v165\n"
"v_mfma_f32_16x16x32_bf16 v[68:71], v[88:91], v[172:175], v[68:71]\n"
"v_cvt_pk_bf16_f32 v39, v166, v167\n"
"v_add_f32_e32 v40, v40, v140\n"
"v_mfma_f32_16x16x32_bf16 v[68:71], v[100:103], v[156:159], v[68:71]\n"
"ds_write_b128 v7, v[36:39] offset:16384\n"
"v_mfma_f32_16x16x32_bf16 v[68:71], v[76:79], v[112:115], v[68:71]\n"
"buffer_store_dwordx4 v[36:39], v7, s[4:7], s64 offen sc1\n"
"v_mfma_f32_16x16x32_bf16 v[68:71], v[132:135], v[116:119], v[68:71]\n"
"v_add_f32_e32 v41, v41, v141\n"
"v_add_f32_e32 v42, v42, v142\n"
"v_mfma_f32_16x16x32_bf16 v[68:71], v[128:131], v[32:35], v[68:71]\n"
"v_add_f32_e32 v43, v43, v143\n"
"v_add_f32_e32 v152, v152, v144\n"
"v_mfma_f32_16x16x32_bf16 v[68:71], v[48:51], v[96:99], v[68:71]\n"
"v_add_f32_e32 v153, v153, v145\n"
"v_add_f32_e32 v154, v154, v146\n"
"v_mfma_f32_16x16x32_bf16 v[68:71], v[72:75], v[44:47], v[68:71]\n"
"v_add_f32_e32 v155, v155, v147\n"
"v_mul_f32_e32 v40, v40, v255\n"
"v_mul_f32_e32 v41, v41, v255\n"
"v_mul_f32_e32 v42, v42, v255\n"
"v_mul_f32_e32 v43, v43, v255\n"
"v_mul_f32_e32 v152, v152, v255\n"
"v_mul_f32_e32 v153, v153, v255\n"
"v_mul_f32_e32 v154, v154, v255\n"
"v_mul_f32_e32 v155, v155, v255\n"
"v_cvt_pk_bf16_f32 v32, v40, v41\n"
"v_cvt_pk_bf16_f32 v33, v42, v43\n"
"v_cvt_pk_bf16_f32 v34, v152, v153\n"
"v_cvt_pk_bf16_f32 v35, v154, v155\n"
"v_xor_b32_e32 v255, 32, v16\n"
"v_xor_b32_e32 v254, 32, v15\n"
"v_mov_b32_e32 v88, 0x3f803f80\n"
"v_mov_b32_e32 v89, 0x3f803f80\n"
"v_mov_b32_e32 v90, 0x3f803f80\n"
"v_mov_b32_e32 v91, 0x3f803f80\n"
"v_mov_b32_e32 v252, 0x53800000\n"
"ds_write_b128 v7, v[32:35] offset:49152\n"
".Lmk_a4:\n"
"; @A tail\n"
"s_add_u32 s98, s64, 0x4000\n"
"buffer_store_dwordx4 v[32:35], v7, s[4:7], s98 offen sc1\n"
"s_waitcnt lgkmcnt(2)\n"
"v_add_f32_e32 v84, v84, v24\n"
"v_add_f32_e32 v85, v85, v25\n"
"v_add_f32_e32 v86, v86, v26\n"
"v_add_f32_e32 v87, v87, v27\n"
"v_add_f32_e32 v68, v68, v28\n"
"v_add_f32_e32 v69, v69, v29\n"
"v_add_f32_e32 v70, v70, v30\n"
"v_add_f32_e32 v71, v71, v31\n"
"v_mul_f32_e32 v84, v84, v253\n"
"v_mul_f32_e32 v85, v85, v253\n"
"v_mul_f32_e32 v86, v86, v253\n"
"v_mul_f32_e32 v87, v87, v253\n"
"v_mul_f32_e32 v68, v68, v253\n"
"v_mul_f32_e32 v69, v69, v253\n"
"v_mul_f32_e32 v70, v70, v253\n"
"v_mul_f32_e32 v71, v71, v253\n"
"v_cvt_pk_bf16_f32 v24, v84, v85\n"
"v_cvt_pk_bf16_f32 v25, v86, v87\n"
"v_cvt_pk_bf16_f32 v26, v68, v69\n"
"v_cvt_pk_bf16_f32 v27, v70, v71\n"
"ds_write_b128 v2, v[24:27] offset:16384\n"
"s_waitcnt lgkmcnt(0)\n"
"s_barrier\n"
"ds_read_b128 v[28:31], v2 offset:49152\n"
"ds_read_b128 v[32:35], v2 offset:50176\n"
"ds_read_b128 v[92:95], v2 offset:16384\n"
"ds_read_b128 v[96:99], v2 offset:17408\n"
"; @A AT b\n"
"s_mov_b32 s2, 0\n"
"v_mov_b32_e32 v253, 0xff800000\n"
"v_mov_b32_e32 v251, 0xff800000\n"
"v_mov_b32_e32 v250, 0x7f800000\n"
"v_mov_b32_e32 v249, 0x7f800000\n"
".Lmk_a91:\n"
"ds_read_b128 v[24:27], v15 offset:24576\n"
"ds_read_b128 v[36:39], v254 offset:24832\n"
"ds_read_b128 v[40:43], v15 offset:22528\n"
"ds_read_b128 v[44:47], v254 offset:22784\n"
"s_cmp_gt_i32 s78, 11\n"
"s_cselect_b32 s98, 0x80000000, 0\n"
"s_or_b32 s98, s98, 0x7f800000\n"
"s_waitcnt lgkmcnt(2)\n"
"v_mfma_f32_16x16x32_bf16 v[48:51], v[24:27], v[28:31], 0\n"
"v_mfma_f32_16x16x32_bf16 v[52:55], v[24:27], v[32:35], 0\n"
"v_mfma_f32_16x16x32_bf16 v[56:59], v[36:39], v[32:35], 0\n"
"v_mfma_f32_16x16x32_bf16 v[24:27], v[36:39], v[28:31], 0\n"
"s_waitcnt lgkmcnt(0)\n"
"s_nop 3\n"
"v_max_f32_e32 v248, v48, v49\n"
"v_max_f32_e32 v247, v52, v53\n"
"v_max3_f32 v248, v248, v50, v51\n"
"v_max3_f32 v247, v247, v54, v55\n"
"v_max3_f32 v248, v248, v24, v25\n"
"v_max3_f32 v247, v247, v56, v57\n"
"v_max3_f32 v248, v248, v26, v27\n"
"v_max3_f32 v247, v247, v58, v59\n"
"v_mov_b32_e32 v246, v248\n"
"v_mov_b32_e32 v245, v247\n"
"s_nop 0\n"
"v_permlane16_swap_b32_e32 v248, v246\n"
"v_permlane16_swap_b32_e32 v247, v245\n"
"v_max_f32_e32 v248, v248, v246\n"
"v_max_f32_e32 v247, v247, v245\n"
"v_mov_b32_e32 v246, v248\n"
"v_mov_b32_e32 v245, v247\n"
"s_nop 0\n"
"v_permlane32_swap_b32_e32 v248, v246\n"
"v_permlane32_swap_b32_e32 v247, v245\n"
"v_max_f32_e32 v248, v248, v246\n"
"v_max_f32_e32 v247, v247, v245\n"
"v_med3_f32 v248, v248, v253, v250\n"
"v_med3_f32 v247, v247, v251, v249\n"
"v_min_f32_e64 v36, s98, -v248\n"
"v_min_f32_e64 v37, s98, -v248\n"
"v_min_f32_e64 v38, s98, -v248\n"
"v_min_f32_e64 v39, s98, -v248\n"
"v_min_f32_e64 v60, s98, -v247\n"
"v_min_f32_e64 v61, s98, -v247\n"
"v_min_f32_e64 v62, s98, -v247\n"
"v_min_f32_e64 v63, s98, -v247\n"
"v_mfma_f32_16x16x32_bf16 v[64:67], v[40:43], v[28:31], v[36:39]\n"
"ds_read_b128 v[68:71], v15 offset:20480\n"
"s_add_u32 s98, s69, 0x602000\n"
"buffer_load_dwordx4 a[32:35], v0, s[4:7], s98 offen sc1\n"
"v_sub_f32_e32 v48, v48, v248\n"
"v_sub_f32_e32 v49, v49, v248\n"
"v_sub_f32_e32 v50, v50, v248\n"
"v_mfma_f32_16x16x32_bf16 v[72:75], v[40:43], v[32:35], v[60:63]\n"
"buffer_load_dwordx4 a[36:39], v0, s[4:7], s98 offen offset:1024 sc1\n"
"v_sub_f32_e32 v51, v51, v248\n"
"v_sub_f32_e32 v24, v24, v248\n"
"v_sub_f32_e32 v25, v25, v248\n"
"v_sub_f32_e32 v26, v26, v248\n"
"v_sub_f32_e32 v27, v27, v248\n"
"v_mfma_f32_16x16x32_bf16 v[40:43], v[44:47], v[32:35], v[60:63]\n"
"ds_read_b128 v[76:79], v254 offset:20736\n"
"v_sub_f32_e32 v52, v52, v247\n"
"v_sub_f32_e32 v53, v53, v247\n"
"v_sub_f32_e32 v54, v54, v247\n"
"v_sub_f32_e32 v55, v55, v247\n"
"v_mfma_f32_16x16x32_bf16 v[80:83], v[44:47], v[28:31], v[36:39]\n"
"buffer_load_dwordx4 a[40:43], v0, s[4:7], s98 offen offset:2048 sc1\n"
"v_sub_f32_e32 v56, v56, v247\n"
"v_sub_f32_e32 v57, v57, v247\n"
"v_sub_f32_e32 v58, v58, v247\n"
"v_sub_f32_e32 v59, v59, v247\n"
"s_waitcnt lgkmcnt(0)\n"
"v_mfma_f32_16x16x32_bf16 v[44:47], v[68:71], v[28:31], v[36:39]\n"
"ds_read_b128 v[84:87], v15 offset:18432\n"
"buffer_load_dwordx4 a[44:47], v0, s[4:7], s98 offen offset:3072 sc1\n"
"ds_read_b128 v[100:103], v254 offset:18688\n"
"v_exp_f32_e32 v64, v64\n"
"v_exp_f32_e32 v65, v65\n"
"v_exp_f32_e32 v66, v66\n"
"v_exp_f32_e32 v67, v67\n"
"v_exp_f32_e32 v80, v80\n"
"v_mfma_f32_16x16x32_bf16 v[104:107], v[68:71], v[32:35], v[60:63]\n"
"ds_read_b64_tr_b16 v[68:69], v16 offset:22528\n"
"buffer_load_dwordx4 a[48:51], v1, s[4:7], s98 offen sc1\n"
"v_exp_f32_e32 v81, v81\n"
"v_exp_f32_e32 v82, v82\n"
"v_exp_f32_e32 v83, v83\n"
"v_cvt_pk_bf16_f32 v108, v64, v65\n"
"v_cvt_pk_bf16_f32 v109, v66, v67\n"
"v_cvt_pk_bf16_f32 v110, v80, v81\n"
"v_mfma_f32_16x16x32_bf16 v[64:67], v[76:79], v[32:35], v[60:63]\n"
"ds_read_b64_tr_b16 v[70:71], v255 offset:22784\n"
"ds_read_b64_tr_b16 v[80:81], v255 offset:22528\n"
"buffer_load_dwordx4 a[52:55], v1, s[4:7], s98 offen offset:1024 sc1\n"
"v_cvt_pk_bf16_f32 v111, v82, v83\n"
"v_exp_f32_e32 v72, v72\n"
"v_exp_f32_e32 v73, v73\n"
"v_exp_f32_e32 v74, v74\n"
"v_exp_f32_e32 v75, v75\n"
"v_mfma_f32_16x16x32_bf16 v[112:115], v[76:79], v[28:31], v[36:39]\n"
"ds_read_b64_tr_b16 v[82:83], v16 offset:22784\n"
"v_exp_f32_e32 v40, v40\n"
"v_exp_f32_e32 v41, v41\n"
"v_exp_f32_e32 v42, v42\n"
"v_exp_f32_e32 v43, v43\n"
"v_cvt_pk_bf16_f32 v76, v72, v73\n"
"v_cvt_pk_bf16_f32 v77, v74, v75\n"
"v_cvt_pk_bf16_f32 v78, v40, v41\n"
"v_cvt_pk_bf16_f32 v79, v42, v43\n"
"s_waitcnt lgkmcnt(4)\n"
"v_mfma_f32_16x16x32_bf16 v[40:43], v[84:87], v[28:31], v[36:39]\n"
"ds_read_b128 v[72:75], v15 offset:16384\n"
"v_exp_f32_e32 v44, v44\n"
"v_exp_f32_e32 v45, v45\n"
"v_exp_f32_e32 v46, v46\n"
"v_mfma_f32_16x16x32_bf16 v[116:119], v[84:87], v[32:35], v[60:63]\n"
"buffer_load_dwordx4 a[56:59], v1, s[4:7], s98 offen offset:2048 sc1\n"
"v_exp_f32_e32 v47, v47\n"
"v_mfma_f32_16x16x32_bf16 v[84:87], v[100:103], v[32:35], v[60:63]\n"
"ds_read_b128 v[120:123], v254 offset:16640\n"
"v_exp_f32_e32 v112, v112\n"
"v_exp_f32_e32 v113, v113\n"
"v_mfma_f32_16x16x32_bf16 v[124:127], v[100:103], v[28:31], v[36:39]\n"
"ds_read_b64_tr_b16 v[100:101], v16 offset:20480\n"
"v_exp_f32_e32 v114, v114\n"
"v_exp_f32_e32 v115, v115\n"
"v_cvt_pk_bf16_f32 v128, v44, v45\n"
"s_waitcnt lgkmcnt(3)\n"
"v_mfma_f32_16x16x32_bf16 v[132:135], v[88:91], v[108:111], 0\n"
"buffer_load_dwordx4 a[60:63], v1, s[4:7], s98 offen offset:3072 sc1\n"
"v_cvt_pk_bf16_f32 v129, v46, v47\n"
"v_cvt_pk_bf16_f32 v130, v112, v113\n"
"v_cvt_pk_bf16_f32 v131, v114, v115\n"
"v_mfma_f32_16x16x32_bf16 v[44:47], v[68:71], v[108:111], 0\n"
"ds_read_b64_tr_b16 v[102:103], v255 offset:20736\n"
"v_exp_f32_e32 v104, v104\n"
"v_exp_f32_e32 v105, v105\n"
"v_mfma_f32_16x16x32_bf16 v[112:115], v[80:83], v[108:111], 0\n"
"ds_read_b64_tr_b16 v[108:109], v255 offset:20480\n"
"s_add_u32 s98, s69, 0x604000\n"
"buffer_load_dwordx4 a[64:67], v0, s[4:7], s98 offen sc1\n"
"v_exp_f32_e32 v106, v106\n"
"v_exp_f32_e32 v107, v107\n"
"v_mfma_f32_16x16x32_bf16 v[136:139], v[80:83], v[76:79], 0\n"
"v_exp_f32_e32 v64, v64\n"
"v_exp_f32_e32 v65, v65\n"
"v_mfma_f32_16x16x32_bf16 v[80:83], v[68:71], v[76:79], 0\n"
"ds_read_b64_tr_b16 v[110:111], v16 offset:20736\n"
"v_exp_f32_e32 v66, v66\n"
"v_exp_f32_e32 v67, v67\n"
"v_mfma_f32_16x16x32_bf16 v[68:71], v[88:91], v[76:79], 0\n"
"s_waitcnt vmcnt(19)\n"
"v_cvt_pk_bf16_f32 v76, v104, v105\n"
"v_cvt_pk_bf16_f32 v77, v106, v107\n"
"v_cvt_pk_bf16_f32 v78, v64, v65\n"
"v_cvt_pk_bf16_f32 v79, v66, v67\n"
"s_cmp_lg_u32 s2, 0\n"
"s_cbranch_scc1 .Lmk_s93\n"
"s_barrier\n"
".Lmk_s93:\n"
"s_waitcnt lgkmcnt(4)\n"
"v_mfma_f32_16x16x32_bf16 v[64:67], v[72:75], v[28:31], v[36:39]\n"
"ds_read_b128 v[104:107], v15 offset:14336\n"
"v_exp_f32_e32 v40, v40\n"
"v_exp_f32_e32 v41, v41\n"
"v_exp_f32_e32 v42, v42\n"
"v_mfma_f32_16x16x32_bf16 v[140:143], v[72:75], v[32:35], v[60:63]\n"
"buffer_load_dwordx4 a[68:71], v0, s[4:7], s98 offen offset:1024 sc1\n"
"v_exp_f32_e32 v43, v43\n"
"v_mfma_f32_16x16x32_bf16 v[72:75], v[120:123], v[32:35], v[60:63]\n"
"ds_read_b128 v[144:147], v254 offset:14592\n"
"v_exp_f32_e32 v124, v124\n"
"v_exp_f32_e32 v125, v125\n"
"v_mfma_f32_16x16x32_bf16 v[148:151], v[120:123], v[28:31], v[36:39]\n"
"ds_read_b64_tr_b16 v[120:121], v16 offset:18432\n"
"v_exp_f32_e32 v126, v126\n"
"v_exp_f32_e32 v127, v127\n"
"s_waitcnt lgkmcnt(3)\n"
"v_mfma_f32_16x16x32_bf16 v[132:135], v[88:91], v[128:131], v[132:135]\n"
"buffer_load_dwordx4 a[72:75], v0, s[4:7], s98 offen offset:2048 sc1\n"
"v_cvt_pk_bf16_f32 v152, v40, v41\n"
"v_cvt_pk_bf16_f32 v153, v42, v43\n"
"v_cvt_pk_bf16_f32 v154, v124, v125\n"
"v_mfma_f32_16x16x32_bf16 v[44:47], v[100:103], v[128:131], v[44:47]\n"
"ds_read_b64_tr_b16 v[122:123], v255 offset:18688\n"
"v_cvt_pk_bf16_f32 v155, v126, v127\n"
"v_exp_f32_e32 v116, v116\n"
"v_exp_f32_e32 v117, v117\n"
"v_mfma_f32_16x16x32_bf16 v[112:115], v[108:111], v[128:131], v[112:115]\n"
"ds_read_b64_tr_b16 v[40:41], v255 offset:18432\n"
"v_exp_f32_e32 v118, v118\n"
"v_exp_f32_e32 v119, v119\n"
"v_mfma_f32_16x16x32_bf16 v[136:139], v[108:111], v[76:79], v[136:139]\n"
"buffer_load_dwordx4 a[76:79], v0, s[4:7], s98 offen offset:3072 sc1\n"
"v_exp_f32_e32 v84, v84\n"
"v_exp_f32_e32 v85, v85\n"
"v_mfma_f32_16x16x32_bf16 v[80:83], v[100:103], v[76:79], v[80:83]\n"
"ds_read_b64_tr_b16 v[42:43], v16 offset:18688\n"
"v_exp_f32_e32 v86, v86\n"
"v_exp_f32_e32 v87, v87\n"
"v_mfma_f32_16x16x32_bf16 v[68:71], v[88:91], v[76:79], v[68:71]\n"
"s_waitcnt lgkmcnt(4)\n"
"v_cvt_pk_bf16_f32 v76, v116, v117\n"
"v_cvt_pk_bf16_f32 v77, v118, v119\n"
"v_cvt_pk_bf16_f32 v78, v84, v85\n"
"v_cvt_pk_bf16_f32 v79, v86, v87\n"
"s_cmp_eq_i32 s78, 8\n"
"s_cbranch_scc1 .Lmk_x94s\n"
".Lmk_x94r:\n"
"v_mfma_f32_16x16x32_bf16 v[84:87], v[104:107], v[28:31], v[36:39]\n"
"ds_read_b128 v[100:103], v15 offset:12288\n"
"v_exp_f32_e32 v64, v64\n"
"v_exp_f32_e32 v65, v65\n"
"v_exp_f32_e32 v66, v66\n"
"v_mfma_f32_16x16x32_bf16 v[108:111], v[104:107], v[32:35], v[60:63]\n"
"buffer_load_dwordx4 a[80:83], v1, s[4:7], s98 offen sc1\n"
"v_exp_f32_e32 v67, v67\n"
"v_mfma_f32_16x16x32_bf16 v[104:107], v[144:147], v[32:35], v[60:63]\n"
"ds_read_b128 v[116:119], v254 offset:12544\n"
"v_exp_f32_e32 v148, v148\n"
"v_exp_f32_e32 v149, v149\n"
"v_mfma_f32_16x16x32_bf16 v[124:127], v[144:147], v[28:31], v[36:39]\n"
"ds_read_b64_tr_b16 v[128:129], v16 offset:16384\n"
"v_exp_f32_e32 v150, v150\n"
"v_exp_f32_e32 v151, v151\n"
"s_waitcnt lgkmcnt(3)\n"
"v_mfma_f32_16x16x32_bf16 v[132:135], v[88:91], v[152:155], v[132:135]\n"
"buffer_load_dwordx4 a[84:87], v1, s[4:7], s98 offen offset:1024 sc1\n"
"v_cvt_pk_bf16_f32 v144, v64, v65\n"
"v_cvt_pk_bf16_f32 v145, v66, v67\n"
"v_cvt_pk_bf16_f32 v146, v148, v149\n"
"v_mfma_f32_16x16x32_bf16 v[44:47], v[120:123], v[152:155], v[44:47]\n"
"ds_read_b64_tr_b16 v[130:131], v255 offset:16640\n"
"v_cvt_pk_bf16_f32 v147, v150, v151\n"
"v_exp_f32_e32 v140, v140\n"
"v_exp_f32_e32 v141, v141\n"
"v_mfma_f32_16x16x32_bf16 v[112:115], v[40:43], v[152:155], v[112:115]\n"
"ds_read_b64_tr_b16 v[64:65], v255 offset:16384\n"
"v_exp_f32_e32 v142, v142\n"
"v_exp_f32_e32 v143, v143\n"
"v_mfma_f32_16x16x32_bf16 v[136:139], v[40:43], v[76:79], v[136:139]\n"
"buffer_load_dwordx4 a[88:91], v1, s[4:7], s98 offen offset:2048 sc1\n"
"v_exp_f32_e32 v72, v72\n"
"v_exp_f32_e32 v73, v73\n"
"v_mfma_f32_16x16x32_bf16 v[80:83], v[120:123], v[76:79], v[80:83]\n"
"ds_read_b64_tr_b16 v[66:67], v16 offset:16640\n"
"v_exp_f32_e32 v74, v74\n"
"v_exp_f32_e32 v75, v75\n"
"v_mfma_f32_16x16x32_bf16 v[68:71], v[88:91], v[76:79], v[68:71]\n"
"s_waitcnt lgkmcnt(4)\n"
"v_cvt_pk_bf16_f32 v40, v140, v141\n"
"v_cvt_pk_bf16_f32 v41, v142, v143\n"
"v_cvt_pk_bf16_f32 v42, v72, v73\n"
"v_cvt_pk_bf16_f32 v43, v74, v75\n"
"s_cmp_eq_i32 s78, 7\n"
"s_cbranch_scc1 .Lmk_x95s\n"
".Lmk_x95r:\n"
"v_mfma_f32_16x16x32_bf16 v[72:75], v[100:103], v[28:31], v[36:39]\n"
"ds_read_b128 v[76:79], v15 offset:10240\n"
"v_exp_f32_e32 v84, v84\n"
"v_exp_f32_e32 v85, v85\n"
"v_exp_f32_e32 v86, v86\n"
"v_mfma_f32_16x16x32_bf16 v[120:123], v[100:103], v[32:35], v[60:63]\n"
"buffer_load_dwordx4 a[92:95], v1, s[4:7], s98 offen offset:3072 sc1\n"
"v_exp_f32_e32 v87, v87\n"
"v_mfma_f32_16x16x32_bf16 v[100:103], v[116:119], v[32:35], v[60:63]\n"
"ds_read_b128 v[140:143], v254 offset:10496\n"
"v_exp_f32_e32 v124, v124\n"
"v_exp_f32_e32 v125, v125\n"
"v_mfma_f32_16x16x32_bf16 v[148:151], v[116:119], v[28:31], v[36:39]\n"
"ds_read_b64_tr_b16 v[116:117], v16 offset:14336\n"
"v_exp_f32_e32 v126, v126\n"
"v_exp_f32_e32 v127, v127\n"
"v_cvt_pk_bf16_f32 v152, v84, v85\n"
"s_waitcnt lgkmcnt(3)\n"
"v_mfma_f32_16x16x32_bf16 v[132:135], v[88:91], v[144:147], v[132:135]\n"
"s_add_u32 s98, s69, 0x606000\n"
"buffer_load_dwordx4 a[96:99], v0, s[4:7], s98 offen sc1\n"
"v_cvt_pk_bf16_f32 v153, v86, v87\n"
"v_cvt_pk_bf16_f32 v154, v124, v125\n"
"v_mfma_f32_16x16x32_bf16 v[44:47], v[128:131], v[144:147], v[44:47]\n"
"ds_read_b64_tr_b16 v[118:119], v255 offset:14592\n"
"v_cvt_pk_bf16_f32 v155, v126, v127\n"
"v_exp_f32_e32 v108, v108\n"
"v_exp_f32_e32 v109, v109\n"
"v_mfma_f32_16x16x32_bf16 v[112:115], v[64:67], v[144:147], v[112:115]\n"
"ds_read_b64_tr_b16 v[84:85], v255 offset:14336\n"
"v_exp_f32_e32 v110, v110\n"
"v_exp_f32_e32 v111, v111\n"
"v_mfma_f32_16x16x32_bf16 v[136:139], v[64:67], v[40:43], v[136:139]\n"
"buffer_load_dwordx4 a[100:103], v0, s[4:7], s98 offen offset:1024 sc1\n"
"v_exp_f32_e32 v104, v104\n"
"v_exp_f32_e32 v105, v105\n"
"v_mfma_f32_16x16x32_bf16 v[80:83], v[128:131], v[40:43], v[80:83]\n"
"ds_read_b64_tr_b16 v[86:87], v16 offset:14592\n"
"v_exp_f32_e32 v106, v106\n"
"v_exp_f32_e32 v107, v107\n"
"v_mfma_f32_16x16x32_bf16 v[68:71], v[88:91], v[40:43], v[68:71]\n"
"s_waitcnt lgkmcnt(4)\n"
"v_cvt_pk_bf16_f32 v40, v108, v109\n"
"v_cvt_pk_bf16_f32 v41, v110, v111\n"
"v_cvt_pk_bf16_f32 v42, v104, v105\n"
"v_cvt_pk_bf16_f32 v43, v106, v107\n"
"s_cmp_eq_i32 s78, 6\n"
"s_cbranch_scc1 .Lmk_x96s\n"
".Lmk_x96r:\n"
"v_mfma_f32_16x16x32_bf16 v[64:67], v[76:79], v[28:31], v[36:39]\n"
"ds_read_b128 v[104:107], v15 offset:8192\n"
"v_exp_f32_e32 v72, v72\n"
"v_exp_f32_e32 v73, v73\n"
"v_exp_f32_e32 v74, v74\n"
"v_mfma_f32_16x16x32_bf16 v[108:111], v[76:79], v[32:35], v[60:63]\n"
"buffer_load_dwordx4 a[104:107], v0, s[4:7], s98 offen offset:2048 sc1\n"
"v_exp_f32_e32 v75, v75\n"
"v_mfma_f32_16x16x32_bf16 v[76:79], v[140:143], v[32:35], v[60:63]\n"
"ds_read_b128 v[124:127], v254 offset:8448\n"
"v_exp_f32_e32 v148, v148\n"
"v_exp_f32_e32 v149, v149\n"
"v_mfma_f32_16x16x32_bf16 v[128:131], v[140:143], v[28:31], v[36:39]\n"
"ds_read_b64_tr_b16 v[140:141], v16 offset:12288\n"
"v_exp_f32_e32 v150, v150\n"
"v_exp_f32_e32 v151, v151\n"
"s_waitcnt lgkmcnt(3)\n"
"v_mfma_f32_16x16x32_bf16 v[132:135], v[88:91], v[152:155], v[132:135]\n"
"buffer_load_dwordx4 a[108:111], v0, s[4:7], s98 offen offset:3072 sc1\n"
"v_cvt_pk_bf16_f32 v144, v72, v73\n"
"v_cvt_pk_bf16_f32 v145, v74, v75\n"
"v_cvt_pk_bf16_f32 v146, v148, v149\n"
"v_mfma_f32_16x16x32_bf16 v[44:47], v[116:119], v[152:155], v[44:47]\n"
"ds_read_b64_tr_b16 v[142:143], v255 offset:12544\n"
"v_cvt_pk_bf16_f32 v147, v150, v151\n"
"v_exp_f32_e32 v120, v120\n"
"v_exp_f32_e32 v121, v121\n"
"v_mfma_f32_16x16x32_bf16 v[112:115], v[84:87], v[152:155], v[112:115]\n"
"ds_read_b64_tr_b16 v[72:73], v255 offset:12288\n"
"v_exp_f32_e32 v122, v122\n"
"v_exp_f32_e32 v123, v123\n"
"v_mfma_f32_16x16x32_bf16 v[136:139], v[84:87], v[40:43], v[136:139]\n"
"buffer_load_dwordx4 a[112:115], v1, s[4:7], s98 offen sc1\n"
"v_exp_f32_e32 v100, v100\n"
"v_exp_f32_e32 v101, v101\n"
"v_mfma_f32_16x16x32_bf16 v[80:83], v[116:119], v[40:43], v[80:83]\n"
"ds_read_b64_tr_b16 v[74:75], v16 offset:12544\n"
"v_exp_f32_e32 v102, v102\n"
"v_exp_f32_e32 v103, v103\n"
"v_mfma_f32_16x16x32_bf16 v[68:71], v[88:91], v[40:43], v[68:71]\n"
"s_waitcnt lgkmcnt(4)\n"
"v_cvt_pk_bf16_f32 v40, v120, v121\n"
"v_cvt_pk_bf16_f32 v41, v122, v123\n"
"v_cvt_pk_bf16_f32 v42, v100, v101\n"
"v_cvt_pk_bf16_f32 v43, v102, v103\n"
"s_cmp_eq_i32 s78, 5\n"
"s_cbranch_scc1 .Lmk_x97s\n"
".Lmk_x97r:\n"
"v_mfma_f32_16x16x32_bf16 v[84:87], v[104:107], v[28:31], v[36:39]\n"
"ds_read_b64_tr_b16 v[100:101], v16 offset:10240\n"
"ds_read_b64_tr_b16 v[102:103], v255 offset:10496\n"
"v_exp_f32_e32 v64, v64\n"
"v_exp_f32_e32 v65, v65\n"
"v_mfma_f32_16x16x32_bf16 v[116:119], v[104:107], v[32:35], v[60:63]\n"
"buffer_load_dwordx4 a[116:119], v1, s[4:7], s98 offen offset:1024 sc1\n"
"v_exp_f32_e32 v66, v66\n"
"v_exp_f32_e32 v67, v67\n"
"v_mfma_f32_16x16x32_bf16 v[104:107], v[124:127], v[32:35], v[60:63]\n"
"ds_read_b64_tr_b16 v[60:61], v255 offset:10240\n"
"ds_read_b64_tr_b16 v[62:63], v16 offset:10496\n"
"v_exp_f32_e32 v128, v128\n"
"v_exp_f32_e32 v129, v129\n"
"v_mfma_f32_16x16x32_bf16 v[120:123], v[124:127], v[28:31], v[36:39]\n"
"ds_read_b64_tr_b16 v[36:37], v16 offset:24576\n"
"v_exp_f32_e32 v130, v130\n"
"v_exp_f32_e32 v131, v131\n"
"s_waitcnt lgkmcnt(5)\n"
"v_mfma_f32_16x16x32_bf16 v[132:135], v[88:91], v[144:147], v[132:135]\n"
"buffer_load_dwordx4 a[120:123], v1, s[4:7], s98 offen offset:2048 sc1\n"
"ds_read_b64_tr_b16 v[38:39], v255 offset:24832\n"
"v_cvt_pk_bf16_f32 v124, v64, v65\n"
"v_cvt_pk_bf16_f32 v125, v66, v67\n"
"v_cvt_pk_bf16_f32 v126, v128, v129\n"
"v_mfma_f32_16x16x32_bf16 v[44:47], v[140:143], v[144:147], v[44:47]\n"
"ds_read_b64_tr_b16 v[64:65], v255 offset:24576\n"
"v_cvt_pk_bf16_f32 v127, v130, v131\n"
"v_exp_f32_e32 v108, v108\n"
"v_exp_f32_e32 v109, v109\n"
"v_mfma_f32_16x16x32_bf16 v[112:115], v[72:75], v[144:147], v[112:115]\n"
"ds_read_b64_tr_b16 v[66:67], v16 offset:24832\n"
"v_exp_f32_e32 v110, v110\n"
"v_exp_f32_e32 v111, v111\n"
"v_mfma_f32_16x16x32_bf16 v[136:139], v[72:75], v[40:43], v[136:139]\n"
"buffer_load_dwordx4 a[124:127], v1, s[4:7], s98 offen offset:3072 sc1\n"
"v_exp_f32_e32 v76, v76\n"
"v_add_u32_e32 v248, 0x26400, v0\n"
"v_exp_f32_e32 v77, v77\n"
"v_mfma_f32_16x16x32_bf16 v[80:83], v[140:143], v[40:43], v[80:83]\n"
"ds_read_b128 v[72:75], v248\n"
"v_exp_f32_e32 v78, v78\n"
"v_exp_f32_e32 v79, v79\n"
"v_mfma_f32_16x16x32_bf16 v[68:71], v[88:91], v[40:43], v[68:71]\n"
"ds_read_b128 v[128:131], v248 offset:1024\n"
"v_cvt_pk_bf16_f32 v40, v108, v109\n"
"v_cvt_pk_bf16_f32 v41, v110, v111\n"
"v_cvt_pk_bf16_f32 v42, v76, v77\n"
"v_cvt_pk_bf16_f32 v43, v78, v79\n"
"s_waitcnt lgkmcnt(6)\n"
"v_mfma_f32_16x16x32_bf16 v[132:135], v[88:91], v[124:127], v[132:135]\n"
"ds_read_b64_tr_b16 v[76:77], v16 offset:8192\n"
"v_cndmask_b32_e64 v84, v48, v84, s[20:21]\n"
"v_cndmask_b32_e64 v85, v49, v85, s[22:23]\n"
"v_cndmask_b32_e64 v86, v50, v86, s[24:25]\n"
"s_add_u32 m0, s77, 0x4000\n"
"s_add_u32 s98, s65, 0\n"
"buffer_load_dwordx4 v2, s[4:7], s98 offen sc1 lds\n"
"buffer_load_dwordx4 v2, s[4:7], s98 offen offset:1024 sc1 lds\n"
"v_cndmask_b32_e64 v87, v51, v87, s[26:27]\n"
"v_cndmask_b32_e64 v120, v24, v120, s[28:29]\n"
"v_mfma_f32_16x16x32_bf16 v[44:47], v[100:103], v[124:127], v[44:47]\n"
"ds_read_b64_tr_b16 v[78:79], v255 offset:8448\n"
"v_cndmask_b32_e64 v121, v25, v121, s[30:31]\n"
"v_cndmask_b32_e64 v122, v26, v122, s[32:33]\n"
"v_cndmask_b32_e64 v123, v27, v123, s[34:35]\n"
"v_exp_f32_e32 v84, v84\n"
"v_exp_f32_e32 v85, v85\n"
"v_exp_f32_e32 v86, v86\n"
"v_exp_f32_e32 v87, v87\n"
"v_mfma_f32_16x16x32_bf16 v[112:115], v[60:63], v[124:127], v[112:115]\n"
"v_exp_f32_e32 v120, v120\n"
"v_exp_f32_e32 v121, v121\n"
"v_exp_f32_e32 v122, v122\n"
"v_exp_f32_e32 v123, v123\n"
"v_cvt_pk_bf16_f32 v48, v84, v85\n"
"v_cvt_pk_bf16_f32 v49, v86, v87\n"
"v_cvt_pk_bf16_f32 v50, v120, v121\n"
"v_mfma_f32_16x16x32_bf16 v[136:139], v[60:63], v[40:43], v[136:139]\n"
"ds_read_b64_tr_b16 v[60:61], v255 offset:8192\n"
"s_add_u32 s98, s69, 0x608000\n"
"buffer_load_dwordx4 v[24:27], v0, s[4:7], s98 offen sc1\n"
"v_cvt_pk_bf16_f32 v51, v122, v123\n"
"v_cndmask_b32_e64 v116, v52, v116, s[36:37]\n"
"v_cndmask_b32_e64 v117, v53, v117, s[38:39]\n"
"v_cndmask_b32_e64 v118, v54, v118, s[40:41]\n"
"v_cndmask_b32_e64 v119, v55, v119, s[42:43]\n"
"v_cndmask_b32_e64 v104, v56, v104, s[44:45]\n"
"v_cndmask_b32_e64 v105, v57, v105, s[46:47]\n"
"v_mfma_f32_16x16x32_bf16 v[80:83], v[100:103], v[40:43], v[80:83]\n"
"ds_read_b64_tr_b16 v[62:63], v16 offset:8448\n"
"v_cndmask_b32_e64 v106, v58, v106, s[48:49]\n"
"v_cndmask_b32_e64 v107, v59, v107, s[50:51]\n"
"v_exp_f32_e32 v116, v116\n"
"v_exp_f32_e32 v117, v117\n"
"v_exp_f32_e32 v118, v118\n"
"v_exp_f32_e32 v119, v119\n"
"v_exp_f32_e32 v104, v104\n"
"v_mfma_f32_16x16x32_bf16 v[68:71], v[88:91], v[40:43], v[68:71]\n"
"s_waitcnt lgkmcnt(0)\n"
"v_exp_f32_e32 v105, v105\n"
"v_exp_f32_e32 v106, v106\n"
"v_exp_f32_e32 v107, v107\n"
"v_cvt_pk_bf16_f32 v40, v116, v117\n"
"v_cvt_pk_bf16_f32 v41, v118, v119\n"
"v_cvt_pk_bf16_f32 v42, v104, v105\n"
"v_cvt_pk_bf16_f32 v43, v106, v107\n"
"v_mfma_f32_16x16x32_bf16 v[132:135], v[88:91], v[48:51], v[132:135]\n"
"v_and_b32_e32 v72, v48, v72\n"
"v_and_b32_e32 v73, v49, v73\n"
"v_and_b32_e32 v74, v50, v74\n"
"v_and_b32_e32 v75, v51, v75\n"
"v_mfma_f32_16x16x32_bf16 v[68:71], v[88:91], v[40:43], v[68:71]\n"
"v_xor_b32_e32 v52, v48, v72\n"
"v_xor_b32_e32 v53, v49, v73\n"
"v_xor_b32_e32 v54, v50, v74\n"
"v_xor_b32_e32 v55, v51, v75\n"
"v_mfma_f32_16x16x32_bf16 v[44:47], v[76:79], v[72:75], v[44:47]\n"
"v_and_b32_e32 v128, v40, v128\n"
"v_and_b32_e32 v129, v41, v129\n"
"v_mfma_f32_16x16x32_bf16 v[112:115], v[60:63], v[72:75], v[112:115]\n"
"v_and_b32_e32 v130, v42, v130\n"
"v_and_b32_e32 v131, v43, v131\n"
"v_mfma_f32_16x16x32_bf16 v[44:47], v[36:39], v[52:55], v[44:47]\n"
"v_xor_b32_e32 v48, v40, v128\n"
"v_xor_b32_e32 v49, v41, v129\n"
"v_mfma_f32_16x16x32_bf16 v[112:115], v[64:67], v[52:55], v[112:115]\n"
"v_xor_b32_e32 v50, v42, v130\n"
"v_xor_b32_e32 v51, v43, v131\n"
"v_mfma_f32_16x16x32_bf16 v[80:83], v[76:79], v[128:131], v[80:83]\n"
"s_add_u32 m0, s77, 0xc000\n"
"s_add_u32 s97, s65, 0x4000\n"
"buffer_load_dwordx4 v2, s[4:7], s97 offen sc1 lds\n"
"buffer_load_dwordx4 v2, s[4:7], s97 offen offset:1024 sc1 lds\n"
"v_mfma_f32_16x16x32_bf16 v[136:139], v[60:63], v[128:131], v[136:139]\n"
"v_mfma_f32_16x16x32_bf16 v[80:83], v[36:39], v[48:51], v[80:83]\n"
"v_mfma_f32_16x16x32_bf16 v[136:139], v[64:67], v[48:51], v[136:139]\n"
"v_max_f32_e32 v248, v132, v68\n"
"v_rcp_f32_e32 v132, v132\n"
"v_rcp_f32_e32 v68, v68\n"
"s_nop 0\n"
"v_max3_f32 v248, v248, v132, v68\n"
"v_cmp_nlt_f32_e32 vcc, v248, v252\n"
"s_cbranch_vccnz .Lmk_b92\n"
".Lmk_k98:\n"
"s_add_u32 s97, s60, 0x40000\n"
"v_mul_f32_e32 v44, v44, v132\n"
"v_mul_f32_e32 v45, v45, v132\n"
"v_mul_f32_e32 v46, v46, v132\n"
"v_mul_f32_e32 v47, v47, v132\n"
"v_mul_f32_e32 v112, v112, v132\n"
"v_mul_f32_e32 v113, v113, v132\n"
"v_mul_f32_e32 v114, v114, v132\n"
"v_mul_f32_e32 v115, v115, v132\n"
"v_cvt_pk_bf16_f32 v28, v44, v45\n"
"v_cvt_pk_bf16_f32 v29, v46, v47\n"
"v_cvt_pk_bf16_f32 v30, v112, v113\n"
"v_cvt_pk_bf16_f32 v31, v114, v115\n"
"buffer_store_dwordx4 v[28:31], v0, s[4:7], s97 offen sc1\n"
"s_add_u32 s97, s60, 0x48000\n"
"v_mul_f32_e32 v80, v80, v68\n"
"v_mul_f32_e32 v81, v81, v68\n"
"v_mul_f32_e32 v82, v82, v68\n"
"v_mul_f32_e32 v83, v83, v68\n"
"v_mul_f32_e32 v136, v136, v68\n"
"v_mul_f32_e32 v137, v137, v68\n"
"v_mul_f32_e32 v138, v138, v68\n"
"v_mul_f32_e32 v139, v139, v68\n"
"v_cvt_pk_bf16_f32 v28, v80, v81\n"
"v_cvt_pk_bf16_f32 v29, v82, v83\n"
"v_cvt_pk_bf16_f32 v30, v136, v137\n"
"v_cvt_pk_bf16_f32 v31, v138, v139\n"
"buffer_store_dwordx4 v[28:31], v0, s[4:7], s97 offen sc1\n"
";\n"
"; @A AT a\n"
"s_mov_b32 s2, 0\n"
"v_mov_b32_e32 v253, 0xff800000\n"
"v_mov_b32_e32 v251, 0xff800000\n"
"v_mov_b32_e32 v250, 0x7f800000\n"
"v_mov_b32_e32 v249, 0x7f800000\n"
".Lmk_a99:\n"
"ds_read_b128 v[28:31], v15 offset:16384\n"
"ds_read_b128 v[32:35], v254 offset:16640\n"
"ds_read_b128 v[36:39], v15 offset:14336\n"
"ds_read_b128 v[40:43], v254 offset:14592\n"
"s_cmp_gt_i32 s78, 7\n"
"s_cselect_b32 s97, 0x80000000, 0\n"
"s_or_b32 s97, s97, 0x7f800000\n"
"s_add_u32 s96, s67, 32\n"
"buffer_load_dwordx2 v[246:247], v12, s[4:7], s96 offen sc1\n"
"s_waitcnt lgkmcnt(2)\n"
"v_mfma_f32_16x16x32_bf16 v[100:103], v[28:31], v[92:95], 0\n"
"v_mfma_f32_16x16x32_bf16 v[104:107], v[28:31], v[96:99], 0\n"
"v_mfma_f32_16x16x32_bf16 v[108:111], v[32:35], v[96:99], 0\n"
"v_mfma_f32_16x16x32_bf16 v[112:115], v[32:35], v[92:95], 0\n"
"s_waitcnt lgkmcnt(0)\n"
"s_nop 3\n"
"v_max_f32_e32 v248, v100, v101\n"
"v_max_f32_e32 v245, v104, v105\n"
"v_max3_f32 v248, v248, v102, v103\n"
"v_max3_f32 v245, v245, v106, v107\n"
"v_max3_f32 v248, v248, v112, v113\n"
"v_max3_f32 v245, v245, v108, v109\n"
"v_max3_f32 v248, v248, v114, v115\n"
"v_max3_f32 v245, v245, v110, v111\n"
"v_mov_b32_e32 v244, v248\n"
"v_mov_b32_e32 v243, v245\n"
"s_nop 0\n"
"v_permlane16_swap_b32_e32 v248, v244\n"
"v_permlane16_swap_b32_e32 v245, v243\n"
"v_max_f32_e32 v248, v248, v244\n"
"v_max_f32_e32 v245, v245, v243\n"
"v_mov_b32_e32 v244, v248\n"
"v_mov_b32_e32 v243, v245\n"
"s_nop 0\n"
"v_permlane32_swap_b32_e32 v248, v244\n"
"v_permlane32_swap_b32_e32 v245, v243\n"
"v_max_f32_e32 v248, v248, v244\n"
"v_max_f32_e32 v245, v245, v243\n"
"v_med3_f32 v248, v248, v253, v250\n"
"v_med3_f32 v245, v245, v251, v249\n"
"v_min_f32_e64 v116, s97, -v248\n"
"v_min_f32_e64 v117, s97, -v248\n"
"v_min_f32_e64 v118, s97, -v248\n"
"v_min_f32_e64 v119, s97, -v248\n"
"v_min_f32_e64 v120, s97, -v245\n"
"v_min_f32_e64 v121, s97, -v245\n"
"v_min_f32_e64 v122, s97, -v245\n"
"v_min_f32_e64 v123, s97, -v245\n"
"v_mfma_f32_16x16x32_bf16 v[44:47], v[36:39], v[92:95], v[116:119]\n"
"ds_read_b128 v[32:35], v15 offset:12288\n"
"v_sub_f32_e32 v100, v100, v248\n"
"v_sub_f32_e32 v101, v101, v248\n"
"v_sub_f32_e32 v102, v102, v248\n"
"v_sub_f32_e32 v103, v103, v248\n"
"v_mfma_f32_16x16x32_bf16 v[48:51], v[36:39], v[96:99], v[120:123]\n"
"v_sub_f32_e32 v112, v112, v248\n"
"v_sub_f32_e32 v113, v113, v248\n"
"v_sub_f32_e32 v114, v114, v248\n"
"v_sub_f32_e32 v115, v115, v248\n"
"v_mfma_f32_16x16x32_bf16 v[52:55], v[40:43], v[96:99], v[120:123]\n"
"ds_read_b128 v[56:59], v254 offset:12544\n"
"v_sub_f32_e32 v104, v104, v245\n"
"v_sub_f32_e32 v105, v105, v245\n"
"v_sub_f32_e32 v106, v106, v245\n"
"v_sub_f32_e32 v107, v107, v245\n"
"v_mfma_f32_16x16x32_bf16 v[36:39], v[40:43], v[92:95], v[116:119]\n"
"s_waitcnt lgkmcnt(0)\n"
"v_sub_f32_e32 v108, v108, v245\n"
"v_sub_f32_e32 v109, v109, v245\n"
"v_sub_f32_e32 v110, v110, v245\n"
"v_sub_f32_e32 v111, v111, v245\n"
"s_cmp_eq_i32 s78, 7\n"
"s_cbranch_scc1 .Lmk_x101s\n"
".Lmk_x101r:\n"
"v_mfma_f32_16x16x32_bf16 v[60:63], v[32:35], v[92:95], v[116:119]\n"
"ds_read_b128 v[40:43], v15 offset:10240\n"
"buffer_load_dwordx4 v[28:31], v0, s[4:7], s98 offen offset:1024 sc1\n"
"ds_read_b128 v[64:67], v254 offset:10496\n"
"v_exp_f32_e32 v44, v44\n"
"v_exp_f32_e32 v45, v45\n"
"v_exp_f32_e32 v46, v46\n"
"v_exp_f32_e32 v47, v47\n"
"v_exp_f32_e32 v36, v36\n"
"v_mfma_f32_16x16x32_bf16 v[68:71], v[32:35], v[96:99], v[120:123]\n"
"ds_read_b64_tr_b16 v[72:73], v16 offset:14336\n"
"buffer_load_dwordx4 v[32:35], v0, s[4:7], s98 offen offset:2048 sc1\n"
"v_exp_f32_e32 v37, v37\n"
"v_exp_f32_e32 v38, v38\n"
"v_exp_f32_e32 v39, v39\n"
"v_cvt_pk_bf16_f32 v76, v44, v45\n"
"v_cvt_pk_bf16_f32 v77, v46, v47\n"
"v_cvt_pk_bf16_f32 v78, v36, v37\n"
"v_mfma_f32_16x16x32_bf16 v[80:83], v[56:59], v[96:99], v[120:123]\n"
"ds_read_b64_tr_b16 v[74:75], v255 offset:14592\n"
"ds_read_b64_tr_b16 v[84:85], v255 offset:14336\n"
"v_cvt_pk_bf16_f32 v79, v38, v39\n"
"v_exp_f32_e32 v48, v48\n"
"v_exp_f32_e32 v49, v49\n"
"v_exp_f32_e32 v50, v50\n"
"v_exp_f32_e32 v51, v51\n"
"v_mfma_f32_16x16x32_bf16 v[124:127], v[56:59], v[92:95], v[116:119]\n"
"buffer_load_dwordx4 v[36:39], v0, s[4:7], s98 offen offset:3072 sc1\n"
"ds_read_b64_tr_b16 v[86:87], v16 offset:14592\n"
"v_exp_f32_e32 v52, v52\n"
"v_exp_f32_e32 v53, v53\n"
"v_exp_f32_e32 v54, v54\n"
"v_exp_f32_e32 v55, v55\n"
"v_cvt_pk_bf16_f32 v56, v48, v49\n"
"v_cvt_pk_bf16_f32 v57, v50, v51\n"
"v_cvt_pk_bf16_f32 v58, v52, v53\n"
"v_cvt_pk_bf16_f32 v59, v54, v55\n"
"s_waitcnt lgkmcnt(4)\n"
"s_cmp_eq_i32 s78, 6\n"
"s_cbranch_scc1 .Lmk_x102s\n"
".Lmk_x102r:\n"
"v_mfma_f32_16x16x32_bf16 v[128:131], v[40:43], v[92:95], v[116:119]\n"
"ds_read_b128 v[132:135], v15 offset:8192\n"
"ds_read_b128 v[136:139], v254 offset:8448\n"
"v_exp_f32_e32 v60, v60\n"
"v_exp_f32_e32 v61, v61\n"
"v_mfma_f32_16x16x32_bf16 v[140:143], v[40:43], v[96:99], v[120:123]\n"
"buffer_load_dwordx4 v[40:43], v1, s[4:7], s98 offen sc1\n"
"v_exp_f32_e32 v62, v62\n"
"v_exp_f32_e32 v63, v63\n"
"v_mfma_f32_16x16x32_bf16 v[144:147], v[64:67], v[96:99], v[120:123]\n"
"ds_read_b64_tr_b16 v[148:149], v16 offset:12288\n"
"ds_read_b64_tr_b16 v[150:151], v255 offset:12544\n"
"v_exp_f32_e32 v124, v124\n"
"v_exp_f32_e32 v125, v125\n"
"v_mfma_f32_16x16x32_bf16 v[152:155], v[64:67], v[92:95], v[116:119]\n"
"ds_read_b64_tr_b16 v[64:65], v255 offset:12288\n"
"v_exp_f32_e32 v126, v126\n"
"v_exp_f32_e32 v127, v127\n"
"s_waitcnt lgkmcnt(5)\n"
"v_mfma_f32_16x16x32_bf16 v[156:159], v[88:91], v[76:79], 0\n"
"buffer_load_dwordx4 v[44:47], v1, s[4:7], s98 offen offset:1024 sc1\n"
"v_cvt_pk_bf16_f32 v160, v60, v61\n"
"v_cvt_pk_bf16_f32 v161, v62, v63\n"
"v_cvt_pk_bf16_f32 v162, v124, v125\n"
"v_cvt_pk_bf16_f32 v163, v126, v127\n"
"v_mfma_f32_16x16x32_bf16 v[124:127], v[72:75], v[76:79], 0\n"
"ds_read_b64_tr_b16 v[66:67], v16 offset:12544\n"
"s_waitcnt vmcnt(8)\n"
"ds_read_b128 v[164:167], v2 offset:16384\n"
"v_exp_f32_e32 v68, v68\n"
"v_exp_f32_e32 v69, v69\n"
"v_mfma_f32_16x16x32_bf16 v[168:171], v[84:87], v[76:79], 0\n"
"v_exp_f32_e32 v70, v70\n"
"v_exp_f32_e32 v71, v71\n"
"v_mfma_f32_16x16x32_bf16 v[172:175], v[84:87], v[56:59], 0\n"
"buffer_load_dwordx4 v[48:51], v1, s[4:7], s98 offen offset:2048 sc1\n"
"ds_read_b128 v[76:79], v2 offset:17408\n"
"v_exp_f32_e32 v80, v80\n"
"v_exp_f32_e32 v81, v81\n"
"v_mfma_f32_16x16x32_bf16 v[176:179], v[72:75], v[56:59], 0\n"
"ds_read_b128 v[72:75], v2 offset:49152\n"
"v_exp_f32_e32 v82, v82\n"
"v_exp_f32_e32 v83, v83\n"
"v_mfma_f32_16x16x32_bf16 v[180:183], v[88:91], v[56:59], 0\n"
"ds_read_b128 v[84:87], v2 offset:50176\n"
"v_cvt_pk_bf16_f32 v184, v68, v69\n"
"v_cvt_pk_bf16_f32 v185, v70, v71\n"
"v_cvt_pk_bf16_f32 v186, v80, v81\n"
"v_cvt_pk_bf16_f32 v187, v82, v83\n"
"s_waitcnt lgkmcnt(8)\n"
"s_cmp_eq_i32 s78, 5\n"
"s_cbranch_scc1 .Lmk_x103s\n"
".Lmk_x103r:\n"
"v_mfma_f32_16x16x32_bf16 v[80:83], v[132:135], v[92:95], v[116:119]\n"
"ds_read_b64_tr_b16 v[68:69], v16 offset:10240\n"
"ds_read_b64_tr_b16 v[70:71], v255 offset:10496\n"
"buffer_load_dwordx4 v[52:55], v1, s[4:7], s98 offen offset:3072 sc1\n"
"v_exp_f32_e32 v128, v128\n"
"v_exp_f32_e32 v129, v129\n"
"v_mfma_f32_16x16x32_bf16 v[188:191], v[132:135], v[96:99], v[120:123]\n"
"ds_read_b64_tr_b16 v[132:133], v255 offset:10240\n"
"v_exp_f32_e32 v130, v130\n"
"v_exp_f32_e32 v131, v131\n"
"v_mfma_f32_16x16x32_bf16 v[192:195], v[136:139], v[96:99], v[120:123]\n"
"ds_read_b64_tr_b16 v[134:135], v16 offset:10496\n"
"v_exp_f32_e32 v152, v152\n"
"v_exp_f32_e32 v153, v153\n"
"v_exp_f32_e32 v154, v154\n"
"v_mfma_f32_16x16x32_bf16 v[196:199], v[136:139], v[92:95], v[116:119]\n"
"s_waitcnt lgkmcnt(4)\n"
"s_add_u32 s97, s69, 0x60a000\n"
"buffer_load_dwordx4 v[56:59], v0, s[4:7], s97 offen sc1\n"
"v_max_u32_e32 v248, v164, v165\n"
"v_exp_f32_e32 v155, v155\n"
"v_mfma_f32_16x16x32_bf16 v[156:159], v[88:91], v[160:163], v[156:159]\n"
"v_max3_u32 v248, v248, v166, v167\n"
"v_cvt_pk_bf16_f32 v136, v128, v129\n"
"v_cvt_pk_bf16_f32 v137, v130, v131\n"
"v_cvt_pk_bf16_f32 v138, v152, v153\n"
"v_mfma_f32_16x16x32_bf16 v[124:127], v[148:151], v[160:163], v[124:127]\n"
"v_max3_u32 v248, v248, v76, v77\n"
"v_cvt_pk_bf16_f32 v139, v154, v155\n"
"v_exp_f32_e32 v140, v140\n"
"v_max3_u32 v248, v248, v78, v79\n"
"v_exp_f32_e32 v141, v141\n"
"v_mfma_f32_16x16x32_bf16 v[168:171], v[64:67], v[160:163], v[168:171]\n"
"v_exp_f32_e32 v142, v142\n"
"v_max3_u32 v248, v248, v72, v73\n"
"v_exp_f32_e32 v143, v143\n"
"v_mfma_f32_16x16x32_bf16 v[172:175], v[64:67], v[184:187], v[172:175]\n"
"buffer_load_dwordx4 v[60:63], v0, s[4:7], s97 offen offset:1024 sc1\n"
"v_max3_u32 v248, v248, v74, v75\n"
"v_exp_f32_e32 v144, v144\n"
"v_exp_f32_e32 v145, v145\n"
"v_mfma_f32_16x16x32_bf16 v[176:179], v[148:151], v[184:187], v[176:179]\n"
"v_max3_u32 v248, v248, v84, v85\n"
"v_exp_f32_e32 v146, v146\n"
"v_max3_u32 v248, v248, v86, v87\n"
"v_exp_f32_e32 v147, v147\n"
"v_mfma_f32_16x16x32_bf16 v[180:183], v[88:91], v[184:187], v[180:183]\n"
"v_cvt_pk_bf16_f32 v128, v140, v141\n"
"v_cmp_eq_u32_e32 vcc, -1, v248\n"
"v_cvt_pk_bf16_f32 v129, v142, v143\n"
"v_cvt_pk_bf16_f32 v130, v144, v145\n"
"v_cvt_pk_bf16_f32 v131, v146, v147\n"
"s_cbranch_vccnz .Lmk_h90s\n"
".Lmk_h90r:\n"
"s_cmp_lg_u32 s2, 0\n"
"s_cbranch_scc1 .Lmk_s104\n"
"s_barrier\n"
".Lmk_s104:\n"
"ds_read_b128 v[72:75], v15 offset:6144\n"
"ds_read_b128 v[76:79], v254 offset:6400\n"
"s_waitcnt lgkmcnt(2)\n"
"v_mfma_f32_16x16x32_bf16 v[156:159], v[88:91], v[136:139], v[156:159]\n"
"ds_read_b128 v[84:87], v15 offset:4096\n"
"v_exp_f32_e32 v80, v80\n"
"v_exp_f32_e32 v81, v81\n"
"v_exp_f32_e32 v82, v82\n"
"v_mfma_f32_16x16x32_bf16 v[124:127], v[68:71], v[136:139], v[124:127]\n"
"buffer_load_dwordx4 v[64:67], v0, s[4:7], s97 offen offset:2048 sc1\n"
"ds_read_b128 v[140:143], v254 offset:4352\n"
"v_exp_f32_e32 v83, v83\n"
"v_mfma_f32_16x16x32_bf16 v[168:171], v[132:135], v[136:139], v[168:171]\n"
"ds_read_b64_tr_b16 v[136:137], v16 offset:8192\n"
"v_exp_f32_e32 v196, v196\n"
"v_exp_f32_e32 v197, v197\n"
"v_mfma_f32_16x16x32_bf16 v[172:175], v[132:135], v[128:131], v[172:175]\n"
"ds_read_b64_tr_b16 v[138:139], v255 offset:8448\n"
"v_exp_f32_e32 v198, v198\n"
"v_exp_f32_e32 v199, v199\n"
"v_mfma_f32_16x16x32_bf16 v[176:179], v[68:71], v[128:131], v[176:179]\n"
"buffer_load_dwordx4 v[68:71], v0, s[4:7], s97 offen offset:3072 sc1\n"
"v_cvt_pk_bf16_f32 v132, v80, v81\n"
"v_cvt_pk_bf16_f32 v133, v82, v83\n"
"v_cvt_pk_bf16_f32 v134, v196, v197\n"
"v_cvt_pk_bf16_f32 v135, v198, v199\n"
"v_mfma_f32_16x16x32_bf16 v[180:183], v[88:91], v[128:131], v[180:183]\n"
"ds_read_b64_tr_b16 v[128:129], v255 offset:8192\n"
"v_exp_f32_e32 v188, v188\n"
"v_exp_f32_e32 v189, v189\n"
"s_waitcnt lgkmcnt(5)\n"
"v_mfma_f32_16x16x32_bf16 v[144:147], v[72:75], v[92:95], v[116:119]\n"
"ds_read_b64_tr_b16 v[130:131], v16 offset:8448\n"
"v_exp_f32_e32 v190, v190\n"
"v_exp_f32_e32 v191, v191\n"
"v_exp_f32_e32 v192, v192\n"
"v_mfma_f32_16x16x32_bf16 v[148:151], v[72:75], v[96:99], v[120:123]\n"
"buffer_load_dwordx4 v[72:75], v1, s[4:7], s97 offen sc1\n"
"s_waitcnt vmcnt(12)\n"
"v_cmp_ne_u64_e32 vcc, v[246:247], v[18:19]\n"
"v_mfma_f32_16x16x32_bf16 v[152:155], v[76:79], v[96:99], v[120:123]\n"
"v_exp_f32_e32 v193, v193\n"
"v_exp_f32_e32 v194, v194\n"
"v_exp_f32_e32 v195, v195\n"
"v_mfma_f32_16x16x32_bf16 v[160:163], v[76:79], v[92:95], v[116:119]\n"
"s_cbranch_vccnz .Lmk_f74s\n"
".Lmk_f74r:\n"
"v_cvt_pk_bf16_f32 v164, v188, v189\n"
"v_cvt_pk_bf16_f32 v165, v190, v191\n"
"v_cvt_pk_bf16_f32 v166, v192, v193\n"
"v_cvt_pk_bf16_f32 v167, v194, v195\n"
"s_waitcnt lgkmcnt(4)\n"
"v_mfma_f32_16x16x32_bf16 v[184:187], v[84:87], v[92:95], v[116:119]\n"
"ds_read_b128 v[188:191], v15 offset:2048\n"
"v_exp_f32_e32 v144, v144\n"
"v_exp_f32_e32 v145, v145\n"
"v_exp_f32_e32 v146, v146\n"
"v_mfma_f32_16x16x32_bf16 v[192:195], v[84:87], v[96:99], v[120:123]\n"
"buffer_load_dwordx4 v[76:79], v1, s[4:7], s97 offen offset:1024 sc1\n"
"v_exp_f32_e32 v147, v147\n"
"v_mfma_f32_16x16x32_bf16 v[196:199], v[140:143], v[96:99], v[120:123]\n"
"ds_read_b128 v[200:203], v254 offset:2304\n"
"v_exp_f32_e32 v160, v160\n"
"v_exp_f32_e32 v161, v161\n"
"v_mfma_f32_16x16x32_bf16 v[204:207], v[140:143], v[92:95], v[116:119]\n"
"ds_read_b64_tr_b16 v[140:141], v16 offset:6144\n"
"v_exp_f32_e32 v162, v162\n"
"v_exp_f32_e32 v163, v163\n"
"s_waitcnt lgkmcnt(3)\n"
"v_mfma_f32_16x16x32_bf16 v[156:159], v[88:91], v[132:135], v[156:159]\n"
"buffer_load_dwordx4 v[80:83], v1, s[4:7], s97 offen offset:2048 sc1\n"
"v_cvt_pk_bf16_f32 v208, v144, v145\n"
"v_cvt_pk_bf16_f32 v209, v146, v147\n"
"v_cvt_pk_bf16_f32 v210, v160, v161\n"
"v_mfma_f32_16x16x32_bf16 v[124:127], v[136:139], v[132:135], v[124:127]\n"
"ds_read_b64_tr_b16 v[142:143], v255 offset:6400\n"
"v_cvt_pk_bf16_f32 v211, v162, v163\n"
"v_exp_f32_e32 v148, v148\n"
"v_exp_f32_e32 v149, v149\n"
"v_mfma_f32_16x16x32_bf16 v[168:171], v[128:131], v[132:135], v[168:171]\n"
"ds_read_b64_tr_b16 v[132:133], v255 offset:6144\n"
"v_exp_f32_e32 v150, v150\n"
"v_exp_f32_e32 v151, v151\n"
"v_mfma_f32_16x16x32_bf16 v[172:175], v[128:131], v[164:167], v[172:175]\n"
"buffer_load_dwordx4 v[84:87], v1, s[4:7], s97 offen offset:3072 sc1\n"
"v_exp_f32_e32 v152, v152\n"
"v_exp_f32_e32 v153, v153\n"
"v_mfma_f32_16x16x32_bf16 v[176:179], v[136:139], v[164:167], v[176:179]\n"
"ds_read_b64_tr_b16 v[134:135], v16 offset:6400\n"
"v_exp_f32_e32 v154, v154\n"
"v_exp_f32_e32 v155, v155\n"
"v_mfma_f32_16x16x32_bf16 v[180:183], v[88:91], v[164:167], v[180:183]\n"
"s_waitcnt lgkmcnt(4)\n"
"v_cvt_pk_bf16_f32 v128, v148, v149\n"
"v_cvt_pk_bf16_f32 v129, v150, v151\n"
"v_cvt_pk_bf16_f32 v130, v152, v153\n"
"v_cvt_pk_bf16_f32 v131, v154, v155\n"
"v_mfma_f32_16x16x32_bf16 v[136:139], v[188:191], v[92:95], v[116:119]\n"
"ds_read_b128 v[144:147], v15\n"
"v_exp_f32_e32 v184, v184\n"
"v_exp_f32_e32 v185, v185\n"
"v_exp_f32_e32 v186, v186\n"
"v_mfma_f32_16x16x32_bf16 v[148:151], v[188:191], v[96:99], v[120:123]\n"
"s_add_u32 s97, s56, 0x40000\n"
"buffer_load_dwordx4 v[152:155], v0, s[4:7], s97 offen sc1\n"
"v_exp_f32_e32 v187, v187\n"
"v_mfma_f32_16x16x32_bf16 v[160:163], v[200:203], v[96:99], v[120:123]\n"
"ds_read_b128 v[164:167], v254 offset:256\n"
"v_exp_f32_e32 v204, v204\n"
"v_exp_f32_e32 v205, v205\n"
"v_exp_f32_e32 v206, v206\n"
"v_mfma_f32_16x16x32_bf16 v[188:191], v[200:203], v[92:95], v[116:119]\n"
"buffer_load_dwordx4 v[200:203], v0, s[4:7], s97 offen offset:1024 sc1\n"
"ds_read_b64_tr_b16 v[212:213], v16 offset:4096\n"
"v_exp_f32_e32 v207, v207\n"
"s_waitcnt lgkmcnt(3)\n"
"v_mfma_f32_16x16x32_bf16 v[156:159], v[88:91], v[208:211], v[156:159]\n"
"v_cvt_pk_bf16_f32 v216, v184, v185\n"
"v_cvt_pk_bf16_f32 v217, v186, v187\n"
"v_cvt_pk_bf16_f32 v218, v204, v205\n"
"v_cvt_pk_bf16_f32 v219, v206, v207\n"
"v_mfma_f32_16x16x32_bf16 v[124:127], v[140:143], v[208:211], v[124:127]\n"
"buffer_load_dwordx4 v[184:187], v0, s[4:7], s97 offen offset:2048 sc1\n"
"ds_read_b64_tr_b16 v[214:215], v255 offset:4352\n"
"v_exp_f32_e32 v192, v192\n"
"v_exp_f32_e32 v193, v193\n"
"v_mfma_f32_16x16x32_bf16 v[168:171], v[132:135], v[208:211], v[168:171]\n"
"buffer_load_dwordx4 v[204:207], v0, s[4:7], s97 offen offset:3072 sc1\n"
"v_exp_f32_e32 v194, v194\n"
"v_exp_f32_e32 v195, v195\n"
"v_mfma_f32_16x16x32_bf16 v[172:175], v[132:135], v[128:131], v[172:175]\n"
"ds_read_b64_tr_b16 v[132:133], v255 offset:4096\n"
"v_exp_f32_e32 v196, v196\n"
"v_exp_f32_e32 v197, v197\n"
"v_mfma_f32_16x16x32_bf16 v[176:179], v[140:143], v[128:131], v[176:179]\n"
"buffer_load_dwordx4 v[140:143], v1, s[4:7], s97 offen sc1\n"
"ds_read_b64_tr_b16 v[134:135], v16 offset:4352\n"
"v_exp_f32_e32 v198, v198\n"
"v_exp_f32_e32 v199, v199\n"
"v_mfma_f32_16x16x32_bf16 v[180:183], v[88:91], v[128:131], v[180:183]\n"
"s_waitcnt lgkmcnt(4)\n"
"v_cvt_pk_bf16_f32 v128, v192, v193\n"
"v_cvt_pk_bf16_f32 v129, v194, v195\n"
"v_cvt_pk_bf16_f32 v130, v196, v197\n"
"v_cvt_pk_bf16_f32 v131, v198, v199\n"
"v_mfma_f32_16x16x32_bf16 v[192:195], v[144:147], v[92:95], v[116:119]\n"
"ds_read_b64_tr_b16 v[196:197], v16 offset:2048\n"
"ds_read_b64_tr_b16 v[198:199], v255 offset:2304\n"
"buffer_load_dwordx4 v[208:211], v1, s[4:7], s97 offen offset:1024 sc1\n"
"v_exp_f32_e32 v136, v136\n"
"v_exp_f32_e32 v137, v137\n"
"v_mfma_f32_16x16x32_bf16 v[220:223], v[144:147], v[96:99], v[120:123]\n"
"ds_read_b64_tr_b16 v[144:145], v255 offset:2048\n"
"v_exp_f32_e32 v138, v138\n"
"v_exp_f32_e32 v139, v139\n"
"v_mfma_f32_16x16x32_bf16 v[224:227], v[164:167], v[96:99], v[120:123]\n"
"buffer_load_dwordx4 v[120:123], v1, s[4:7], s97 offen offset:2048 sc1\n"
"v_exp_f32_e32 v188, v188\n"
"v_exp_f32_e32 v189, v189\n"
"v_mfma_f32_16x16x32_bf16 v[228:231], v[164:167], v[92:95], v[116:119]\n"
"ds_read_b64_tr_b16 v[146:147], v16 offset:2304\n"
"ds_read_b64_tr_b16 v[116:117], v16 offset:16384\n"
"v_exp_f32_e32 v190, v190\n"
"v_exp_f32_e32 v191, v191\n"
"s_waitcnt lgkmcnt(5)\n"
"v_mfma_f32_16x16x32_bf16 v[156:159], v[88:91], v[216:219], v[156:159]\n"
"v_cvt_pk_bf16_f32 v164, v136, v137\n"
"v_cvt_pk_bf16_f32 v165, v138, v139\n"
"v_cvt_pk_bf16_f32 v166, v188, v189\n"
"v_mfma_f32_16x16x32_bf16 v[124:127], v[212:215], v[216:219], v[124:127]\n"
"buffer_load_dwordx4 v[136:139], v1, s[4:7], s97 offen offset:3072 sc1\n"
"ds_read_b64_tr_b16 v[118:119], v255 offset:16640\n"
"ds_read_b64_tr_b16 v[188:189], v255 offset:16384\n"
"v_cvt_pk_bf16_f32 v167, v190, v191\n"
"v_exp_f32_e32 v148, v148\n"
"v_exp_f32_e32 v149, v149\n"
"v_exp_f32_e32 v150, v150\n"
"v_mfma_f32_16x16x32_bf16 v[168:171], v[132:135], v[216:219], v[168:171]\n"
"s_add_u32 s97, s57, 0x40000\n"
"buffer_load_dwordx4 v[216:219], v0, s[4:7], s97 offen sc1\n"
"ds_read_b64_tr_b16 v[190:191], v16 offset:16640\n"
"v_exp_f32_e32 v151, v151\n"
"v_mfma_f32_16x16x32_bf16 v[172:175], v[132:135], v[128:131], v[172:175]\n"
"v_exp_f32_e32 v160, v160\n"
"v_add_u32_e32 v248, 0x26400, v0\n"
"v_exp_f32_e32 v161, v161\n"
"v_mfma_f32_16x16x32_bf16 v[176:179], v[212:215], v[128:131], v[176:179]\n"
"buffer_load_dwordx4 v[132:135], v0, s[4:7], s97 offen offset:1024 sc1\n"
"ds_read_b128 v[212:215], v248\n"
"v_exp_f32_e32 v162, v162\n"
"v_exp_f32_e32 v163, v163\n"
"v_mfma_f32_16x16x32_bf16 v[180:183], v[88:91], v[128:131], v[180:183]\n"
"ds_read_b128 v[232:235], v248 offset:1024\n"
"v_cvt_pk_bf16_f32 v128, v148, v149\n"
"v_cvt_pk_bf16_f32 v129, v150, v151\n"
"v_cvt_pk_bf16_f32 v130, v160, v161\n"
"v_cvt_pk_bf16_f32 v131, v162, v163\n"
"s_waitcnt lgkmcnt(6)\n"
"v_mfma_f32_16x16x32_bf16 v[156:159], v[88:91], v[164:167], v[156:159]\n"
"ds_read_b64_tr_b16 v[148:149], v16\n"
"buffer_load_dwordx4 v[160:163], v0, s[4:7], s97 offen offset:2048 sc1\n"
"v_cndmask_b32_e64 v192, v100, v192, s[20:21]\n"
"v_cndmask_b32_e64 v193, v101, v193, s[22:23]\n"
"v_cndmask_b32_e64 v194, v102, v194, s[24:25]\n"
"v_cndmask_b32_e64 v195, v103, v195, s[26:27]\n"
"v_cndmask_b32_e64 v228, v112, v228, s[28:29]\n"
"v_cndmask_b32_e64 v229, v113, v229, s[30:31]\n"
"v_cndmask_b32_e64 v230, v114, v230, s[32:33]\n"
"v_cndmask_b32_e64 v231, v115, v231, s[34:35]\n"
"v_exp_f32_e32 v192, v192\n"
"v_mfma_f32_16x16x32_bf16 v[124:127], v[196:199], v[164:167], v[124:127]\n"
"ds_read_b64_tr_b16 v[150:151], v255 offset:256\n"
"buffer_load_dwordx4 v[100:103], v0, s[4:7], s97 offen offset:3072 sc1\n"
"v_exp_f32_e32 v193, v193\n"
"v_exp_f32_e32 v194, v194\n"
"v_exp_f32_e32 v195, v195\n"
"v_exp_f32_e32 v228, v228\n"
"v_mfma_f32_16x16x32_bf16 v[168:171], v[144:147], v[164:167], v[168:171]\n"
"buffer_load_dwordx4 v[164:167], v1, s[4:7], s97 offen sc1\n"
"v_exp_f32_e32 v229, v229\n"
"v_exp_f32_e32 v230, v230\n"
"v_exp_f32_e32 v231, v231\n"
"v_cvt_pk_bf16_f32 v112, v192, v193\n"
"v_cvt_pk_bf16_f32 v113, v194, v195\n"
"v_cvt_pk_bf16_f32 v114, v228, v229\n"
"v_mfma_f32_16x16x32_bf16 v[172:175], v[144:147], v[128:131], v[172:175]\n"
"ds_read_b64_tr_b16 v[144:145], v255\n"
"v_cvt_pk_bf16_f32 v115, v230, v231\n"
"v_cndmask_b32_e64 v220, v104, v220, s[36:37]\n"
"v_cndmask_b32_e64 v221, v105, v221, s[38:39]\n"
"v_cndmask_b32_e64 v222, v106, v222, s[40:41]\n"
"v_cndmask_b32_e64 v223, v107, v223, s[42:43]\n"
"v_cndmask_b32_e64 v224, v108, v224, s[44:45]\n"
"v_cndmask_b32_e64 v225, v109, v225, s[46:47]\n"
"v_cndmask_b32_e64 v226, v110, v226, s[48:49]\n"
"v_cndmask_b32_e64 v227, v111, v227, s[50:51]\n"
"v_mfma_f32_16x16x32_bf16 v[176:179], v[196:199], v[128:131], v[176:179]\n"
"buffer_load_dwordx4 v[104:107], v1, s[4:7], s97 offen offset:1024 sc1\n"
"ds_read_b64_tr_b16 v[146:147], v16 offset:256\n"
"buffer_load_dwordx4 v[108:111], v1, s[4:7], s97 offen offset:2048 sc1\n"
"v_exp_f32_e32 v220, v220\n"
"v_exp_f32_e32 v221, v221\n"
"v_exp_f32_e32 v222, v222\n"
"v_exp_f32_e32 v223, v223\n"
"v_exp_f32_e32 v224, v224\n"
"v_mfma_f32_16x16x32_bf16 v[180:183], v[88:91], v[128:131], v[180:183]\n"
"s_waitcnt lgkmcnt(0)\n"
"v_exp_f32_e32 v225, v225\n"
"v_exp_f32_e32 v226, v226\n"
"v_exp_f32_e32 v227, v227\n"
"v_cvt_pk_bf16_f32 v128, v220, v221\n"
"v_cvt_pk_bf16_f32 v129, v222, v223\n"
"v_cvt_pk_bf16_f32 v130, v224, v225\n"
"v_cvt_pk_bf16_f32 v131, v226, v227\n"
"v_mfma_f32_16x16x32_bf16 v[156:159], v[88:91], v[112:115], v[156:159]\n"
"v_and_b32_e32 v212, v112, v212\n"
"v_and_b32_e32 v213, v113, v213\n"
"v_and_b32_e32 v214, v114, v214\n"
"v_and_b32_e32 v215, v115, v215\n"
"v_mfma_f32_16x16x32_bf16 v[180:183], v[88:91], v[128:131], v[180:183]\n"
"v_xor_b32_e32 v192, v112, v212\n"
"v_xor_b32_e32 v193, v113, v213\n"
"v_xor_b32_e32 v194, v114, v214\n"
"v_xor_b32_e32 v195, v115, v215\n"
"v_mfma_f32_16x16x32_bf16 v[124:127], v[148:151], v[212:215], v[124:127]\n"
"v_and_b32_e32 v232, v128, v232\n"
"v_and_b32_e32 v233, v129, v233\n"
"v_mfma_f32_16x16x32_bf16 v[168:171], v[144:147], v[212:215], v[168:171]\n"
"v_and_b32_e32 v234, v130, v234\n"
"v_and_b32_e32 v235, v131, v235\n"
"v_mfma_f32_16x16x32_bf16 v[124:127], v[116:119], v[192:195], v[124:127]\n"
"v_xor_b32_e32 v112, v128, v232\n"
"v_xor_b32_e32 v113, v129, v233\n"
"v_mfma_f32_16x16x32_bf16 v[168:171], v[188:191], v[192:195], v[168:171]\n"
"v_xor_b32_e32 v114, v130, v234\n"
"v_xor_b32_e32 v115, v131, v235\n"
"v_mfma_f32_16x16x32_bf16 v[176:179], v[148:151], v[232:235], v[176:179]\n"
"buffer_load_dwordx4 v[128:131], v1, s[4:7], s97 offen offset:3072 sc1\n"
"s_add_u32 s97, s58, 0x40000\n"
"buffer_load_dwordx4 v[148:151], v0, s[4:7], s97 offen sc1\n"
"v_mfma_f32_16x16x32_bf16 v[172:175], v[144:147], v[232:235], v[172:175]\n"
"buffer_load_dwordx4 v[144:147], v0, s[4:7], s97 offen offset:1024 sc1\n"
"v_mfma_f32_16x16x32_bf16 v[176:179], v[116:119], v[112:115], v[176:179]\n"
"buffer_load_dwordx4 v[116:119], v0, s[4:7], s97 offen offset:2048 sc1\n"
"v_mfma_f32_16x16x32_bf16 v[172:175], v[188:191], v[112:115], v[172:175]\n"
"buffer_load_dwordx4 v[112:115], v0, s[4:7], s97 offen offset:3072 sc1\n"
"v_max_f32_e32 v248, v156, v180\n"
"v_rcp_f32_e32 v156, v156\n"
"v_rcp_f32_e32 v180, v180\n"
"s_nop 0\n"
"v_max3_f32 v248, v248, v156, v180\n"
"v_cmp_nlt_f32_e32 vcc, v248, v252\n"
"s_cbranch_vccnz .Lmk_b100\n"
".Lmk_k105:\n"
"v_mul_f32_e32 v124, v124, v156\n"
"v_mul_f32_e32 v125, v125, v156\n"
"v_mul_f32_e32 v126, v126, v156\n"
"v_mul_f32_e32 v127, v127, v156\n"
"v_mul_f32_e32 v168, v168, v156\n"
"v_mul_f32_e32 v169, v169, v156\n"
"v_mul_f32_e32 v170, v170, v156\n"
"v_mul_f32_e32 v171, v171, v156\n"
"v_cvt_pk_bf16_f32 v88, v124, v125\n"
"v_cvt_pk_bf16_f32 v89, v126, v127\n"
"v_cvt_pk_bf16_f32 v90, v168, v169\n"
"v_cvt_pk_bf16_f32 v91, v170, v171\n"
"buffer_store_dwordx4 v[88:91], v0, s[4:7], s60 offen sc1\n"
"s_add_u32 s98, s60, 0x8000\n"
"v_mul_f32_e32 v176, v176, v180\n"
"v_mul_f32_e32 v177, v177, v180\n"
"v_mul_f32_e32 v178, v178, v180\n"
"v_mul_f32_e32 v179, v179, v180\n"
"v_mul_f32_e32 v172, v172, v180\n"
"v_mul_f32_e32 v173, v173, v180\n"
"v_mul_f32_e32 v174, v174, v180\n"
"v_mul_f32_e32 v175, v175, v180\n"
"v_cvt_pk_bf16_f32 v88, v176, v177\n"
"v_cvt_pk_bf16_f32 v89, v178, v179\n"
"v_cvt_pk_bf16_f32 v90, v172, v173\n"
"v_cvt_pk_bf16_f32 v91, v174, v175\n"
"buffer_store_dwordx4 v[88:91], v0, s[4:7], s98 offen sc1\n"
";\n"
"s_waitcnt vmcnt(20)\n"
"; @A G2 b.0\n"
"s_nop 0\n"
"v_mfma_f32_16x16x32_bf16 v[88:91], a[128:131], v[152:155], 0\n"
"v_mfma_f32_16x16x32_bf16 v[88:91], a[132:135], v[200:203], v[88:91]\n"
"s_waitcnt vmcnt(18)\n"
"v_mfma_f32_16x16x32_bf16 v[88:91], a[136:139], v[184:187], v[88:91]\n"
"buffer_load_dwordx4 v[92:95], v1, s[4:7], s97 offen sc1\n"
"v_mfma_f32_16x16x32_bf16 v[88:91], a[140:143], v[204:207], v[88:91]\n"
"s_waitcnt vmcnt(17)\n"
"v_mfma_f32_16x16x32_bf16 v[88:91], a[144:147], v[140:143], v[88:91]\n"
"v_mfma_f32_16x16x32_bf16 v[88:91], a[148:151], v[208:211], v[88:91]\n"
"s_waitcnt vmcnt(15)\n"
"v_mfma_f32_16x16x32_bf16 v[88:91], a[152:155], v[120:123], v[88:91]\n"
"buffer_load_dwordx4 v[96:99], v1, s[4:7], s97 offen offset:1024 sc1\n"
"v_mfma_f32_16x16x32_bf16 v[88:91], a[156:159], v[136:139], v[88:91]\n"
"v_mfma_f32_16x16x32_bf16 v[124:127], a[188:191], v[136:139], 0\n"
"v_mfma_f32_16x16x32_bf16 v[124:127], a[184:187], v[120:123], v[124:127]\n"
"v_mfma_f32_16x16x32_bf16 v[124:127], a[180:183], v[208:211], v[124:127]\n"
"buffer_load_dwordx4 v[156:159], v1, s[4:7], s97 offen offset:2048 sc1\n"
"v_mfma_f32_16x16x32_bf16 v[124:127], a[176:179], v[140:143], v[124:127]\n"
"v_mfma_f32_16x16x32_bf16 v[124:127], a[172:175], v[204:207], v[124:127]\n"
"s_nop 1\n"
"v_cmp_u_f32_e32 vcc, v88, v88\n"
"v_mfma_f32_16x16x32_bf16 v[124:127], a[168:171], v[184:187], v[124:127]\n"
"s_cbranch_vccnz .Lmk_p106s\n"
".Lmk_p106r:\n"
"v_mfma_f32_16x16x32_bf16 v[124:127], a[164:167], v[200:203], v[124:127]\n"
"ds_write_b128 v3, v[88:91]\n"
"v_mfma_f32_16x16x32_bf16 v[124:127], a[160:163], v[152:155], v[124:127]\n"
"buffer_load_dwordx4 v[88:91], v1, s[4:7], s97 offen offset:3072 sc1\n"
"v_mfma_f32_16x16x32_bf16 v[168:171], a[192:195], v[152:155], 0\n"
"v_mfma_f32_16x16x32_bf16 v[168:171], a[196:199], v[200:203], v[168:171]\n"
"s_add_u32 s98, s59, 0x40000\n"
"v_mfma_f32_16x16x32_bf16 v[168:171], a[200:203], v[184:187], v[168:171]\n"
"v_mfma_f32_16x16x32_bf16 v[168:171], a[204:207], v[204:207], v[168:171]\n"
"buffer_load_dwordx4 v[172:175], v0, s[4:7], s98 offen sc1\n"
"v_mfma_f32_16x16x32_bf16 v[168:171], a[208:211], v[140:143], v[168:171]\n"
"v_mfma_f32_16x16x32_bf16 v[168:171], a[212:215], v[208:211], v[168:171]\n"
"ds_write_b128 v3, v[124:127] offset:1024\n"
"v_mfma_f32_16x16x32_bf16 v[168:171], a[216:219], v[120:123], v[168:171]\n"
"v_mfma_f32_16x16x32_bf16 v[168:171], a[220:223], v[136:139], v[168:171]\n"
"buffer_load_dwordx4 v[124:127], v0, s[4:7], s98 offen offset:1024 sc1\n"
"v_mfma_f32_16x16x32_bf16 v[176:179], a[252:255], v[136:139], 0\n"
"v_mfma_f32_16x16x32_bf16 v[176:179], a[248:251], v[120:123], v[176:179]\n"
"v_mfma_f32_16x16x32_bf16 v[176:179], a[244:247], v[208:211], v[176:179]\n"
"v_mfma_f32_16x16x32_bf16 v[176:179], a[240:243], v[140:143], v[176:179]\n"
"buffer_load_dwordx4 v[120:123], v0, s[4:7], s98 offen offset:2048 sc1\n"
"v_mfma_f32_16x16x32_bf16 v[176:179], a[236:239], v[204:207], v[176:179]\n"
"v_mfma_f32_16x16x32_bf16 v[176:179], a[232:235], v[184:187], v[176:179]\n"
"ds_write_b128 v3, v[168:171] offset:2048\n"
"v_mfma_f32_16x16x32_bf16 v[176:179], a[228:231], v[200:203], v[176:179]\n"
"v_mfma_f32_16x16x32_bf16 v[176:179], a[224:227], v[152:155], v[176:179]\n"
"s_waitcnt vmcnt(17)\n"
"buffer_load_dwordx4 v[136:139], v0, s[4:7], s98 offen offset:3072 sc1\n"
"; @A G2 b.1\n"
"v_mfma_f32_16x16x32_bf16 v[140:143], a[128:131], v[216:219], 0\n"
"v_mfma_f32_16x16x32_bf16 v[140:143], a[132:135], v[132:135], v[140:143]\n"
"v_mfma_f32_16x16x32_bf16 v[140:143], a[136:139], v[160:163], v[140:143]\n"
"v_mfma_f32_16x16x32_bf16 v[140:143], a[140:143], v[100:103], v[140:143]\n"
"s_waitcnt vmcnt(14)\n"
"buffer_load_dwordx4 v[152:155], v1, s[4:7], s98 offen sc1\n"
"v_mfma_f32_16x16x32_bf16 v[140:143], a[144:147], v[164:167], v[140:143]\n"
"v_mfma_f32_16x16x32_bf16 v[140:143], a[148:151], v[104:107], v[140:143]\n"
"ds_write_b128 v3, v[176:179] offset:3072\n"
"v_mfma_f32_16x16x32_bf16 v[140:143], a[152:155], v[108:111], v[140:143]\n"
"v_mfma_f32_16x16x32_bf16 v[140:143], a[156:159], v[128:131], v[140:143]\n"
"buffer_load_dwordx4 v[168:171], v1, s[4:7], s98 offen offset:1024 sc1\n"
"v_mfma_f32_16x16x32_bf16 v[176:179], a[188:191], v[128:131], 0\n"
"v_mfma_f32_16x16x32_bf16 v[176:179], a[184:187], v[108:111], v[176:179]\n"
"v_mfma_f32_16x16x32_bf16 v[176:179], a[180:183], v[104:107], v[176:179]\n"
"v_mfma_f32_16x16x32_bf16 v[176:179], a[176:179], v[164:167], v[176:179]\n"
"buffer_load_dwordx4 v[180:183], v1, s[4:7], s98 offen offset:2048 sc1\n"
"v_mfma_f32_16x16x32_bf16 v[176:179], a[172:175], v[100:103], v[176:179]\n"
"s_nop 0\n"
"v_cmp_u_f32_e32 vcc, v140, v140\n"
"v_mfma_f32_16x16x32_bf16 v[176:179], a[168:171], v[160:163], v[176:179]\n"
"s_cbranch_vccnz .Lmk_p107s\n"
".Lmk_p107r:\n"
"v_mfma_f32_16x16x32_bf16 v[176:179], a[164:167], v[132:135], v[176:179]\n"
"ds_write_b128 v3, v[140:143] offset:6144\n"
"v_mfma_f32_16x16x32_bf16 v[176:179], a[160:163], v[216:219], v[176:179]\n"
"buffer_load_dwordx4 v[140:143], v1, s[4:7], s98 offen offset:3072 sc1\n"
"v_mfma_f32_16x16x32_bf16 v[184:187], a[192:195], v[216:219], 0\n"
"v_mfma_f32_16x16x32_bf16 v[184:187], a[196:199], v[132:135], v[184:187]\n"
"v_mfma_f32_16x16x32_bf16 v[184:187], a[200:203], v[160:163], v[184:187]\n"
"v_mfma_f32_16x16x32_bf16 v[184:187], a[204:207], v[100:103], v[184:187]\n"
"v_mfma_f32_16x16x32_bf16 v[184:187], a[208:211], v[164:167], v[184:187]\n"
"ds_read_b128 v[188:191], v2 offset:8192\n"
"ds_read_b128 v[192:195], v2 offset:9216\n"
"v_mfma_f32_16x16x32_bf16 v[184:187], a[212:215], v[104:107], v[184:187]\n"
"ds_write_b128 v3, v[176:179] offset:7168\n"
"v_mfma_f32_16x16x32_bf16 v[184:187], a[216:219], v[108:111], v[184:187]\n"
"v_mfma_f32_16x16x32_bf16 v[184:187], a[220:223], v[128:131], v[184:187]\n"
"v_mfma_f32_16x16x32_bf16 v[176:179], a[252:255], v[128:131], 0\n"
"v_mfma_f32_16x16x32_bf16 v[176:179], a[248:251], v[108:111], v[176:179]\n"
"s_waitcnt lgkmcnt(1)\n"
"v_lshlrev_b32_e32 v255, 16, v188\n"
"v_and_b32_e32 v254, 0xffff0000, v188\n"
"v_mfma_f32_16x16x32_bf16 v[176:179], a[244:247], v[104:107], v[176:179]\n"
"v_lshlrev_b32_e32 v253, 16, v189\n"
"v_and_b32_e32 v252, 0xffff0000, v189\n"
"v_mfma_f32_16x16x32_bf16 v[176:179], a[240:243], v[164:167], v[176:179]\n"
"v_lshlrev_b32_e32 v251, 16, v190\n"
"v_and_b32_e32 v250, 0xffff0000, v190\n"
"v_mfma_f32_16x16x32_bf16 v[176:179], a[236:239], v[100:103], v[176:179]\n"
"v_lshlrev_b32_e32 v249, 16, v191\n"
"v_and_b32_e32 v248, 0xffff0000, v191\n"
"v_mfma_f32_16x16x32_bf16 v[176:179], a[232:235], v[160:163], v[176:179]\n"
"ds_write_b128 v3, v[184:187] offset:8192\n"
"v_mfma_f32_16x16x32_bf16 v[176:179], a[228:231], v[132:135], v[176:179]\n"
"v_lshlrev_b32_e32 v247, 16, v192\n"
"v_and_b32_e32 v246, 0xffff0000, v192\n"
"v_mfma_f32_16x16x32_bf16 v[176:179], a[224:227], v[216:219], v[176:179]\n"
"s_waitcnt vmcnt(14)\n"
"v_lshlrev_b32_e32 v245, 16, v193\n"
"v_and_b32_e32 v244, 0xffff0000, v193\n"
"; @A G2 b.2\n"
"v_mfma_f32_16x16x32_bf16 v[100:103], a[128:131], v[148:151], 0\n"
"v_lshlrev_b32_e32 v243, 16, v194\n"
"v_and_b32_e32 v242, 0xffff0000, v194\n"
"v_mfma_f32_16x16x32_bf16 v[100:103], a[132:135], v[144:147], v[100:103]\n"
"v_lshlrev_b32_e32 v241, 16, v195\n"
"v_and_b32_e32 v240, 0xffff0000, v195\n"
"v_mfma_f32_16x16x32_bf16 v[100:103], a[136:139], v[116:119], v[100:103]\n"
"v_mfma_f32_16x16x32_bf16 v[100:103], a[140:143], v[112:115], v[100:103]\n"
"s_waitcnt vmcnt(8)\n"
"v_mfma_f32_16x16x32_bf16 v[100:103], a[144:147], v[92:95], v[100:103]\n"
"v_mfma_f32_16x16x32_bf16 v[100:103], a[148:151], v[96:99], v[100:103]\n"
"ds_write_b128 v3, v[176:179] offset:9216\n"
"v_mfma_f32_16x16x32_bf16 v[100:103], a[152:155], v[156:159], v[100:103]\n"
"v_mfma_f32_16x16x32_bf16 v[100:103], a[156:159], v[88:91], v[100:103]\n"
"v_mfma_f32_16x16x32_bf16 v[104:107], a[188:191], v[88:91], 0\n"
"v_mfma_f32_16x16x32_bf16 v[104:107], a[184:187], v[156:159], v[104:107]\n"
"s_waitcnt lgkmcnt(0)\n"
"s_barrier\n"
"v_mfma_f32_16x16x32_bf16 v[104:107], a[180:183], v[96:99], v[104:107]\n"
"ds_read_b128 v[108:111], v4\n"
"ds_read_b128 v[128:131], v5\n"
"v_mfma_f32_16x16x32_bf16 v[104:107], a[176:179], v[92:95], v[104:107]\n"
"v_mfma_f32_16x16x32_bf16 v[104:107], a[172:175], v[112:115], v[104:107]\n"
"s_add_u32 s98, s56, 0\n"
"v_cmp_u_f32_e32 vcc, v100, v100\n"
"v_mfma_f32_16x16x32_bf16 v[104:107], a[168:171], v[116:119], v[104:107]\n"
"s_cbranch_vccnz .Lmk_p108s\n"
".Lmk_p108r:\n"
"v_mfma_f32_16x16x32_bf16 v[104:107], a[164:167], v[144:147], v[104:107]\n"
"ds_write_b128 v3, v[100:103] offset:12288\n"
"v_mfma_f32_16x16x32_bf16 v[104:107], a[160:163], v[148:151], v[104:107]\n"
"buffer_load_dwordx4 v[100:103], v0, s[4:7], s98 offen sc1\n"
"v_mfma_f32_16x16x32_bf16 v[132:135], a[192:195], v[148:151], 0\n"
"ds_read_b128 v[160:163], v4 offset:1024\n"
"ds_read_b128 v[164:167], v5 offset:1024\n"
"v_mfma_f32_16x16x32_bf16 v[132:135], a[196:199], v[144:147], v[132:135]\n"
"s_waitcnt lgkmcnt(3)\n"
"v_add_f32_e32 v108, v108, v255\n"
"v_add_f32_e32 v109, v109, v254\n"
"v_mfma_f32_16x16x32_bf16 v[132:135], a[200:203], v[116:119], v[132:135]\n"
"buffer_load_dwordx4 v[176:179], v0, s[4:7], s98 offen offset:1024 sc1\n"
"v_mfma_f32_16x16x32_bf16 v[132:135], a[204:207], v[112:115], v[132:135]\n"
"v_add_f32_e32 v110, v110, v253\n"
"v_add_f32_e32 v111, v111, v252\n"
"v_mfma_f32_16x16x32_bf16 v[132:135], a[208:211], v[92:95], v[132:135]\n"
"v_add_f32_e32 v108, v108, v128\n"
"v_add_f32_e32 v109, v109, v129\n"
"v_mfma_f32_16x16x32_bf16 v[132:135], a[212:215], v[96:99], v[132:135]\n"
"ds_write_b128 v3, v[104:107] offset:13312\n"
"v_mfma_f32_16x16x32_bf16 v[132:135], a[216:219], v[156:159], v[132:135]\n"
"buffer_load_dwordx4 v[104:107], v0, s[4:7], s98 offen offset:2048 sc1\n"
"v_mfma_f32_16x16x32_bf16 v[132:135], a[220:223], v[88:91], v[132:135]\n"
"ds_read_b128 v[184:187], v4 offset:2048\n"
"ds_read_b128 v[188:191], v5 offset:2048\n"
"v_mfma_f32_16x16x32_bf16 v[192:195], a[252:255], v[88:91], 0\n"
"s_waitcnt lgkmcnt(3)\n"
"v_add_f32_e32 v110, v110, v130\n"
"v_add_f32_e32 v111, v111, v131\n"
"v_mfma_f32_16x16x32_bf16 v[192:195], a[248:251], v[156:159], v[192:195]\n"
"buffer_load_dwordx4 v[88:91], v0, s[4:7], s98 offen offset:3072 sc1\n"
"v_mfma_f32_16x16x32_bf16 v[192:195], a[244:247], v[96:99], v[192:195]\n"
"v_add_f32_e32 v160, v160, v251\n"
"v_add_f32_e32 v161, v161, v250\n"
"v_mfma_f32_16x16x32_bf16 v[192:195], a[240:243], v[92:95], v[192:195]\n"
"v_add_f32_e32 v162, v162, v249\n"
"v_add_f32_e32 v163, v163, v248\n"
"v_mfma_f32_16x16x32_bf16 v[192:195], a[236:239], v[112:115], v[192:195]\n"
"buffer_load_dwordx4 v[92:95], v1, s[4:7], s98 offen sc1\n"
"v_mfma_f32_16x16x32_bf16 v[192:195], a[232:235], v[116:119], v[192:195]\n"
"ds_write_b128 v3, v[132:135] offset:14336\n"
"v_mfma_f32_16x16x32_bf16 v[192:195], a[228:231], v[144:147], v[192:195]\n"
"ds_read_b128 v[96:99], v4 offset:3072\n"
"ds_read_b128 v[112:115], v5 offset:3072\n"
"v_mfma_f32_16x16x32_bf16 v[192:195], a[224:227], v[148:151], v[192:195]\n"
"s_waitcnt vmcnt(9)\n"
"buffer_load_dwordx4 v[116:119], v1, s[4:7], s98 offen offset:1024 sc1\n"
"; @A G2 b.3\n"
"v_mfma_f32_16x16x32_bf16 v[128:131], a[128:131], v[172:175], v[108:111]\n"
"v_add_f32_e32 v160, v160, v164\n"
"v_add_f32_e32 v161, v161, v165\n"
"v_mfma_f32_16x16x32_bf16 v[128:131], a[132:135], v[124:127], v[128:131]\n"
"s_waitcnt lgkmcnt(3)\n"
"v_add_f32_e32 v162, v162, v166\n"
"v_add_f32_e32 v163, v163, v167\n"
"v_mfma_f32_16x16x32_bf16 v[128:131], a[136:139], v[120:123], v[128:131]\n"
"buffer_load_dwordx4 v[132:135], v1, s[4:7], s98 offen offset:2048 sc1\n"
"v_mfma_f32_16x16x32_bf16 v[128:131], a[140:143], v[136:139], v[128:131]\n"
"s_waitcnt vmcnt(7)\n"
"v_add_f32_e32 v184, v184, v247\n"
"v_add_f32_e32 v185, v185, v246\n"
"v_mfma_f32_16x16x32_bf16 v[128:131], a[144:147], v[152:155], v[128:131]\n"
"v_add_f32_e32 v186, v186, v245\n"
"v_add_f32_e32 v187, v187, v244\n"
"v_mfma_f32_16x16x32_bf16 v[128:131], a[148:151], v[168:171], v[128:131]\n"
"ds_write_b128 v3, v[192:195] offset:15360\n"
"v_mfma_f32_16x16x32_bf16 v[128:131], a[152:155], v[180:183], v[128:131]\n"
"buffer_load_dwordx4 v[144:147], v1, s[4:7], s98 offen offset:3072 sc1\n"
"v_mfma_f32_16x16x32_bf16 v[128:131], a[156:159], v[140:143], v[128:131]\n"
"s_add_u32 s98, s57, 0\n"
"v_add_f32_e32 v184, v184, v188\n"
"v_add_f32_e32 v185, v185, v189\n"
"v_mfma_f32_16x16x32_bf16 v[148:151], a[188:191], v[140:143], v[160:163]\n"
"s_waitcnt lgkmcnt(1)\n"
"v_add_f32_e32 v186, v186, v190\n"
"v_add_f32_e32 v187, v187, v191\n"
"v_mfma_f32_16x16x32_bf16 v[148:151], a[184:187], v[180:183], v[148:151]\n"
"s_waitcnt lgkmcnt(0)\n"
"s_barrier\n"
"v_mfma_f32_16x16x32_bf16 v[148:151], a[180:183], v[168:171], v[148:151]\n"
"buffer_load_dwordx4 v[156:159], v0, s[4:7], s98 offen sc1\n"
"v_mfma_f32_16x16x32_bf16 v[148:151], a[176:179], v[152:155], v[148:151]\n"
"v_add_f32_e32 v96, v96, v243\n"
"v_add_f32_e32 v97, v97, v242\n"
"v_mfma_f32_16x16x32_bf16 v[148:151], a[172:175], v[136:139], v[148:151]\n"
"v_cmp_u_f32_e32 vcc, v128, v128\n"
"v_add_f32_e32 v98, v98, v241\n"
"v_mfma_f32_16x16x32_bf16 v[148:151], a[168:171], v[120:123], v[148:151]\n"
"s_cbranch_vccnz .Lmk_p109s\n"
".Lmk_p109r:\n"
"v_add_f32_e32 v99, v99, v240\n"
"v_mfma_f32_16x16x32_bf16 v[148:151], a[164:167], v[124:127], v[148:151]\n"
"buffer_load_dwordx4 v[108:111], v0, s[4:7], s98 offen offset:1024 sc1\n"
"v_mfma_f32_16x16x32_bf16 v[148:151], a[160:163], v[172:175], v[148:151]\n"
"v_add_f32_e32 v96, v96, v112\n"
"v_add_f32_e32 v97, v97, v113\n"
"v_mfma_f32_16x16x32_bf16 v[184:187], a[192:195], v[172:175], v[184:187]\n"
"v_add_f32_e32 v98, v98, v114\n"
"v_add_f32_e32 v99, v99, v115\n"
"v_mfma_f32_16x16x32_bf16 v[184:187], a[196:199], v[124:127], v[184:187]\n"
"buffer_load_dwordx4 v[112:115], v0, s[4:7], s98 offen offset:2048 sc1\n"
"v_mfma_f32_16x16x32_bf16 v[184:187], a[200:203], v[120:123], v[184:187]\n"
"ds_read_b128 v[160:163], v6\n"
"ds_read_b128 v[164:167], v6 offset:1024\n"
"v_mfma_f32_16x16x32_bf16 v[184:187], a[204:207], v[136:139], v[184:187]\n"
"ds_read_b128 v[188:191], v6 offset:2048\n"
"ds_read_b128 v[192:195], v6 offset:3072\n"
"v_mfma_f32_16x16x32_bf16 v[184:187], a[208:211], v[152:155], v[184:187]\n"
"buffer_load_dwordx4 v[196:199], v0, s[4:7], s98 offen offset:3072 sc1\n"
"v_mfma_f32_16x16x32_bf16 v[184:187], a[212:215], v[168:171], v[184:187]\n"
"v_mfma_f32_16x16x32_bf16 v[184:187], a[216:219], v[180:183], v[184:187]\n"
"v_mfma_f32_16x16x32_bf16 v[184:187], a[220:223], v[140:143], v[184:187]\n"
"buffer_load_dwordx4 v[200:203], v1, s[4:7], s98 offen sc1\n"
"v_mfma_f32_16x16x32_bf16 v[96:99], a[252:255], v[140:143], v[96:99]\n"
"s_waitcnt lgkmcnt(0)\n"
"v_add_f32_e32 v160, v128, v160\n"
"v_add_f32_e32 v161, v129, v161\n"
"v_mfma_f32_16x16x32_bf16 v[96:99], a[248:251], v[180:183], v[96:99]\n"
"v_add_f32_e32 v162, v130, v162\n"
"v_add_f32_e32 v163, v131, v163\n"
"v_mfma_f32_16x16x32_bf16 v[96:99], a[244:247], v[168:171], v[96:99]\n"
"buffer_load_dwordx4 v[128:131], v1, s[4:7], s98 offen offset:1024 sc1\n"
"v_mfma_f32_16x16x32_bf16 v[96:99], a[240:243], v[152:155], v[96:99]\n"
"v_add_f32_e32 v164, v148, v164\n"
"v_add_f32_e32 v165, v149, v165\n"
"v_mfma_f32_16x16x32_bf16 v[96:99], a[236:239], v[136:139], v[96:99]\n"
"v_add_f32_e32 v166, v150, v166\n"
"v_add_f32_e32 v167, v151, v167\n"
"v_mfma_f32_16x16x32_bf16 v[96:99], a[232:235], v[120:123], v[96:99]\n"
"buffer_load_dwordx4 v[120:123], v1, s[4:7], s98 offen offset:2048 sc1\n"
"v_mfma_f32_16x16x32_bf16 v[96:99], a[228:231], v[124:127], v[96:99]\n"
"v_cvt_pk_bf16_f32 v124, v160, v161\n"
"v_cvt_pk_bf16_f32 v125, v162, v163\n"
"v_mfma_f32_16x16x32_bf16 v[96:99], a[224:227], v[172:175], v[96:99]\n"
"s_add_u32 s97, s61, 0x440000\n"
"v_cvt_pk_bf16_f32 v126, v164, v165\n"
"v_cvt_pk_bf16_f32 v127, v166, v167\n"
"buffer_store_dwordx4 v[124:127], v0, s[4:7], s97 offen sc1\n"
"ds_write_b128 v2, v[124:127] offset:8192\n"
"buffer_load_dwordx4 v[136:139], v1, s[4:7], s98 offen offset:3072 sc1\n"
"; @A fin b\n"
"s_add_u32 s98, s61, 0x440000\n"
"v_add_f32_e32 v188, v184, v188\n"
"v_add_f32_e32 v189, v185, v189\n"
"v_add_f32_e32 v190, v186, v190\n"
"v_add_f32_e32 v191, v187, v191\n"
"v_add_f32_e32 v192, v96, v192\n"
"v_add_f32_e32 v193, v97, v193\n"
"v_add_f32_e32 v194, v98, v194\n"
"v_add_f32_e32 v195, v99, v195\n"
"v_cvt_pk_bf16_f32 v96, v188, v189\n"
"v_cvt_pk_bf16_f32 v97, v190, v191\n"
"v_cvt_pk_bf16_f32 v98, v192, v193\n"
"v_cvt_pk_bf16_f32 v99, v194, v195\n"
"buffer_store_dwordx4 v[96:99], v0, s[4:7], s98 offen offset:1024 sc1\n"
"s_waitcnt vmcnt(16)\n"
"; @A G2 a.0\n"
"v_mfma_f32_16x16x32_bf16 v[140:143], a[128:131], v[100:103], 0\n"
"s_add_u32 s98, s58, 0\n"
"buffer_load_dwordx4 v[148:151], v0, s[4:7], s98 offen sc1\n"
"v_mfma_f32_16x16x32_bf16 v[140:143], a[132:135], v[176:179], v[140:143]\n"
"s_waitcnt vmcnt(15)\n"
"ds_write_b128 v2, v[96:99] offset:9216\n"
"v_mfma_f32_16x16x32_bf16 v[140:143], a[136:139], v[104:107], v[140:143]\n"
"v_mov_b32_e32 v152, 0x3f803f80\n"
"v_mov_b32_e32 v153, 0x3f803f80\n"
"v_mfma_f32_16x16x32_bf16 v[140:143], a[140:143], v[88:91], v[140:143]\n"
"s_waitcnt vmcnt(13)\n"
"v_mov_b32_e32 v154, 0x3f803f80\n"
"v_mov_b32_e32 v155, 0x3f803f80\n"
"v_mfma_f32_16x16x32_bf16 v[140:143], a[144:147], v[92:95], v[140:143]\n"
"buffer_load_dwordx4 v[160:163], v0, s[4:7], s98 offen offset:1024 sc1\n"
"v_mfma_f32_16x16x32_bf16 v[164:167], v[152:155], v[124:127], 0\n"
"v_mfma_f32_16x16x32_bf16 v[164:167], v[152:155], v[96:99], v[164:167]\n"
"v_mfma_f32_16x16x32_bf16 v[152:155], v[124:127], v[124:127], 0\n"
"v_mfma_f32_16x16x32_bf16 v[152:155], v[96:99], v[96:99], v[152:155]\n"
"v_mfma_f32_16x16x32_bf16 v[140:143], a[148:151], v[116:119], v[140:143]\n"
"s_waitcnt vmcnt(12)\n"
"v_mfma_f32_16x16x32_bf16 v[140:143], a[152:155], v[132:135], v[140:143]\n"
"v_mfma_f32_16x16x32_bf16 v[140:143], a[156:159], v[144:147], v[140:143]\n"
"v_mfma_f32_16x16x32_bf16 v[96:99], a[188:191], v[144:147], 0\n"
"buffer_load_dwordx4 v[124:127], v0, s[4:7], s98 offen offset:2048 sc1\n"
"v_mfma_f32_16x16x32_bf16 v[96:99], a[184:187], v[132:135], v[96:99]\n"
"s_mov_b32 s96, 0xaaaaaaaa\n"
"s_mov_b32 s97, 0xaaaaaaaa\n"
"v_mfma_f32_16x16x32_bf16 v[96:99], a[180:183], v[116:119], v[96:99]\n"
"v_mfma_f32_16x16x32_bf16 v[96:99], a[176:179], v[92:95], v[96:99]\n"
"v_mfma_f32_16x16x32_bf16 v[96:99], a[172:175], v[88:91], v[96:99]\n"
"v_cmp_u_f32_e32 vcc, v140, v140\n"
"v_cndmask_b32_e64 v152, v152, v153, s[96:97]\n"
"v_mfma_f32_16x16x32_bf16 v[96:99], a[168:171], v[104:107], v[96:99]\n"
"s_cbranch_vccnz .Lmk_p110s\n"
".Lmk_p110r:\n"
"v_cndmask_b32_e64 v154, v154, v155, s[96:97]\n"
"v_mfma_f32_16x16x32_bf16 v[96:99], a[164:167], v[176:179], v[96:99]\n"
"ds_write_b128 v3, v[140:143]\n"
"v_mfma_f32_16x16x32_bf16 v[96:99], a[160:163], v[100:103], v[96:99]\n"
"buffer_load_dwordx4 v[140:143], v0, s[4:7], s98 offen offset:3072 sc1\n"
"v_mfma_f32_16x16x32_bf16 v[168:171], a[192:195], v[100:103], 0\n"
"s_mov_b32 s96, 0xcccccccc\n"
"s_mov_b32 s97, 0xcccccccc\n"
"v_mfma_f32_16x16x32_bf16 v[168:171], a[196:199], v[176:179], v[168:171]\n"
"v_cndmask_b32_e64 v165, v152, v154, s[96:97]\n"
"v_mfma_f32_16x16x32_bf16 v[168:171], a[200:203], v[104:107], v[168:171]\n"
"s_mov_b32 s96, 0xf0000f\n"
"s_mov_b32 s97, 0xf0000f00\n"
"v_mfma_f32_16x16x32_bf16 v[168:171], a[204:207], v[88:91], v[168:171]\n"
"buffer_load_dwordx4 v[152:155], v1, s[4:7], s98 offen sc1\n"
"v_mfma_f32_16x16x32_bf16 v[168:171], a[208:211], v[92:95], v[168:171]\n"
"v_cndmask_b32_e64 v165, 0, v165, s[96:97]\n"
"v_mov_b32_e32 v255, v165\n"
"v_mfma_f32_16x16x32_bf16 v[168:171], a[212:215], v[116:119], v[168:171]\n"
"ds_write_b128 v3, v[96:99] offset:1024\n"
"v_mfma_f32_16x16x32_bf16 v[168:171], a[216:219], v[132:135], v[168:171]\n"
"v_permlane16_swap_b32_e32 v165, v255\n"
"v_add_f32_e32 v165, v165, v255\n"
"v_mfma_f32_16x16x32_bf16 v[168:171], a[220:223], v[144:147], v[168:171]\n"
"buffer_load_dwordx4 v[172:175], v1, s[4:7], s98 offen offset:1024 sc1\n"
"v_mfma_f32_16x16x32_bf16 v[96:99], a[252:255], v[144:147], 0\n"
"v_mov_b32_e32 v255, v165\n"
"v_mfma_f32_16x16x32_bf16 v[96:99], a[248:251], v[132:135], v[96:99]\n"
"s_mov_b32 s96, 0xffff0000\n"
"v_permlane32_swap_b32_e32 v165, v255\n"
"v_add_f32_e32 v165, v165, v255\n"
"v_mfma_f32_16x16x32_bf16 v[96:99], a[244:247], v[116:119], v[96:99]\n"
"v_cndmask_b32_e64 v164, v164, v165, s[96:97]\n"
"s_add_u32 s97, s63, 0x44000\n"
"v_mfma_f32_16x16x32_bf16 v[96:99], a[240:243], v[92:95], v[96:99]\n"
"buffer_load_dwordx4 v[116:119], v1, s[4:7], s98 offen offset:2048 sc1\n"
"v_mfma_f32_16x16x32_bf16 v[96:99], a[236:239], v[88:91], v[96:99]\n"
"buffer_store_dword v164, v14, s[4:7], s97 offen sc1\n"
"v_mfma_f32_16x16x32_bf16 v[96:99], a[232:235], v[104:107], v[96:99]\n"
"ds_write_b128 v3, v[168:171] offset:2048\n"
"v_mfma_f32_16x16x32_bf16 v[96:99], a[228:231], v[176:179], v[96:99]\n"
"v_mfma_f32_16x16x32_bf16 v[96:99], a[224:227], v[100:103], v[96:99]\n"
"s_waitcnt vmcnt(14)\n"
"buffer_load_dwordx4 v[132:135], v1, s[4:7], s98 offen offset:3072 sc1\n"
"; @A G2 a.1\n"
"v_mfma_f32_16x16x32_bf16 v[88:91], a[128:131], v[156:159], 0\n"
"v_mfma_f32_16x16x32_bf16 v[88:91], a[132:135], v[108:111], v[88:91]\n"
"s_add_u32 s98, s59, 0\n"
"v_mfma_f32_16x16x32_bf16 v[88:91], a[136:139], v[112:115], v[88:91]\n"
"v_mfma_f32_16x16x32_bf16 v[88:91], a[140:143], v[196:199], v[88:91]\n"
"s_waitcnt vmcnt(10)\n"
"buffer_load_dwordx4 v[176:179], v0, s[4:7], s98 offen sc1\n"
"v_mfma_f32_16x16x32_bf16 v[88:91], a[144:147], v[200:203], v[88:91]\n"
"v_mfma_f32_16x16x32_bf16 v[88:91], a[148:151], v[128:131], v[88:91]\n"
"ds_write_b128 v3, v[96:99] offset:3072\n"
"v_mfma_f32_16x16x32_bf16 v[88:91], a[152:155], v[120:123], v[88:91]\n"
"v_mfma_f32_16x16x32_bf16 v[88:91], a[156:159], v[136:139], v[88:91]\n"
"buffer_load_dwordx4 v[180:183], v0, s[4:7], s98 offen offset:1024 sc1\n"
"v_mfma_f32_16x16x32_bf16 v[92:95], a[188:191], v[136:139], 0\n"
"v_mfma_f32_16x16x32_bf16 v[92:95], a[184:187], v[120:123], v[92:95]\n"
"v_mfma_f32_16x16x32_bf16 v[92:95], a[180:183], v[128:131], v[92:95]\n"
"v_mfma_f32_16x16x32_bf16 v[92:95], a[176:179], v[200:203], v[92:95]\n"
"buffer_load_dwordx4 v[168:171], v0, s[4:7], s98 offen offset:2048 sc1\n"
"v_mfma_f32_16x16x32_bf16 v[92:95], a[172:175], v[196:199], v[92:95]\n"
"s_nop 0\n"
"v_cmp_u_f32_e32 vcc, v88, v88\n"
"v_mfma_f32_16x16x32_bf16 v[92:95], a[168:171], v[112:115], v[92:95]\n"
"s_cbranch_vccnz .Lmk_p111s\n"
".Lmk_p111r:\n"
"v_mfma_f32_16x16x32_bf16 v[92:95], a[164:167], v[108:111], v[92:95]\n"
"ds_write_b128 v3, v[88:91] offset:6144\n"
"v_mfma_f32_16x16x32_bf16 v[92:95], a[160:163], v[156:159], v[92:95]\n"
"buffer_load_dwordx4 v[184:187], v0, s[4:7], s98 offen offset:3072 sc1\n"
"v_mfma_f32_16x16x32_bf16 v[88:91], a[192:195], v[156:159], 0\n"
"v_mfma_f32_16x16x32_bf16 v[88:91], a[196:199], v[108:111], v[88:91]\n"
"v_mfma_f32_16x16x32_bf16 v[88:91], a[200:203], v[112:115], v[88:91]\n"
"v_mfma_f32_16x16x32_bf16 v[88:91], a[204:207], v[196:199], v[88:91]\n"
"buffer_load_dwordx4 v[188:191], v1, s[4:7], s98 offen sc1\n"
"v_mfma_f32_16x16x32_bf16 v[88:91], a[208:211], v[200:203], v[88:91]\n"
"ds_read_b128 v[96:99], v2\n"
"ds_read_b128 v[100:103], v2 offset:1024\n"
"v_mfma_f32_16x16x32_bf16 v[88:91], a[212:215], v[128:131], v[88:91]\n"
"ds_write_b128 v3, v[92:95] offset:7168\n"
"v_mfma_f32_16x16x32_bf16 v[88:91], a[216:219], v[120:123], v[88:91]\n"
"v_mfma_f32_16x16x32_bf16 v[88:91], a[220:223], v[136:139], v[88:91]\n"
"buffer_load_dwordx4 v[164:167], v1, s[4:7], s98 offen offset:1024 sc1\n"
"v_mfma_f32_16x16x32_bf16 v[92:95], a[252:255], v[136:139], 0\n"
"v_mfma_f32_16x16x32_bf16 v[92:95], a[248:251], v[120:123], v[92:95]\n"
"s_waitcnt lgkmcnt(1)\n"
"v_lshlrev_b32_e32 v255, 16, v96\n"
"v_and_b32_e32 v254, 0xffff0000, v96\n"
"v_mfma_f32_16x16x32_bf16 v[92:95], a[244:247], v[128:131], v[92:95]\n"
"v_lshlrev_b32_e32 v253, 16, v97\n"
"v_and_b32_e32 v252, 0xffff0000, v97\n"
"v_mfma_f32_16x16x32_bf16 v[92:95], a[240:243], v[200:203], v[92:95]\n"
"buffer_load_dwordx4 v[192:195], v1, s[4:7], s98 offen offset:2048 sc1\n"
"v_mfma_f32_16x16x32_bf16 v[92:95], a[236:239], v[196:199], v[92:95]\n"
"v_lshlrev_b32_e32 v251, 16, v98\n"
"v_and_b32_e32 v250, 0xffff0000, v98\n"
"v_mfma_f32_16x16x32_bf16 v[92:95], a[232:235], v[112:115], v[92:95]\n"
"ds_write_b128 v3, v[88:91] offset:8192\n"
"v_mfma_f32_16x16x32_bf16 v[92:95], a[228:231], v[108:111], v[92:95]\n"
"v_lshlrev_b32_e32 v249, 16, v99\n"
"v_and_b32_e32 v248, 0xffff0000, v99\n"
"v_mfma_f32_16x16x32_bf16 v[92:95], a[224:227], v[156:159], v[92:95]\n"
"s_waitcnt vmcnt(12)\n"
"buffer_load_dwordx4 v[196:199], v1, s[4:7], s98 offen offset:3072 sc1\n"
"; @A G2 a.2\n"
"v_mfma_f32_16x16x32_bf16 v[96:99], a[128:131], v[148:151], 0\n"
"v_lshlrev_b32_e32 v247, 16, v100\n"
"v_and_b32_e32 v246, 0xffff0000, v100\n"
"v_mfma_f32_16x16x32_bf16 v[96:99], a[132:135], v[160:163], v[96:99]\n"
"v_lshlrev_b32_e32 v245, 16, v101\n"
"v_and_b32_e32 v244, 0xffff0000, v101\n"
"v_mfma_f32_16x16x32_bf16 v[96:99], a[136:139], v[124:127], v[96:99]\n"
"v_lshlrev_b32_e32 v243, 16, v102\n"
"v_and_b32_e32 v242, 0xffff0000, v102\n"
"v_mfma_f32_16x16x32_bf16 v[96:99], a[140:143], v[140:143], v[96:99]\n"
"s_waitcnt vmcnt(8)\n"
"v_lshlrev_b32_e32 v241, 16, v103\n"
"v_and_b32_e32 v240, 0xffff0000, v103\n"
"v_mfma_f32_16x16x32_bf16 v[96:99], a[144:147], v[152:155], v[96:99]\n"
"v_mfma_f32_16x16x32_bf16 v[96:99], a[148:151], v[172:175], v[96:99]\n"
"ds_write_b128 v3, v[92:95] offset:9216\n"
"v_mfma_f32_16x16x32_bf16 v[96:99], a[152:155], v[116:119], v[96:99]\n"
"v_mfma_f32_16x16x32_bf16 v[96:99], a[156:159], v[132:135], v[96:99]\n"
"v_mfma_f32_16x16x32_bf16 v[104:107], a[188:191], v[132:135], 0\n"
"s_add_u32 s98, s52, 0x440000\n"
"buffer_load_dwordx4 v[88:91], v0, s[4:7], s98 offen sc1\n"
"v_mfma_f32_16x16x32_bf16 v[104:107], a[184:187], v[116:119], v[104:107]\n"
"s_waitcnt lgkmcnt(0)\n"
"s_barrier\n"
"v_mfma_f32_16x16x32_bf16 v[104:107], a[180:183], v[172:175], v[104:107]\n"
"ds_read_b128 v[144:147], v4\n"
"ds_read_b128 v[112:115], v5\n"
"v_mfma_f32_16x16x32_bf16 v[104:107], a[176:179], v[152:155], v[104:107]\n"
"buffer_load_dwordx4 v[92:95], v0, s[4:7], s98 offen offset:1024 sc1\n"
"v_mfma_f32_16x16x32_bf16 v[104:107], a[172:175], v[140:143], v[104:107]\n"
"v_cmp_u_f32_e32 vcc, v96, v96\n"
"v_mfma_f32_16x16x32_bf16 v[104:107], a[168:171], v[124:127], v[104:107]\n"
"s_cbranch_vccnz .Lmk_p112s\n"
".Lmk_p112r:\n"
"v_mfma_f32_16x16x32_bf16 v[104:107], a[164:167], v[160:163], v[104:107]\n"
"ds_write_b128 v3, v[96:99] offset:12288\n"
"v_mfma_f32_16x16x32_bf16 v[104:107], a[160:163], v[148:151], v[104:107]\n"
"buffer_load_dwordx4 v[96:99], v0, s[4:7], s98 offen offset:2048 sc1\n"
"v_mfma_f32_16x16x32_bf16 v[120:123], a[192:195], v[148:151], 0\n"
"ds_read_b128 v[156:159], v4 offset:1024\n"
"ds_read_b128 v[128:131], v5 offset:1024\n"
"v_mfma_f32_16x16x32_bf16 v[120:123], a[196:199], v[160:163], v[120:123]\n"
"s_waitcnt lgkmcnt(3)\n"
"v_add_f32_e32 v144, v144, v255\n"
"v_add_f32_e32 v145, v145, v254\n"
"v_mfma_f32_16x16x32_bf16 v[120:123], a[200:203], v[124:127], v[120:123]\n"
"buffer_load_dwordx4 v[100:103], v0, s[4:7], s98 offen offset:3072 sc1\n"
"v_mfma_f32_16x16x32_bf16 v[120:123], a[204:207], v[140:143], v[120:123]\n"
"v_add_f32_e32 v146, v146, v253\n"
"v_add_f32_e32 v147, v147, v252\n"
"v_mfma_f32_16x16x32_bf16 v[120:123], a[208:211], v[152:155], v[120:123]\n"
"v_add_f32_e32 v144, v144, v112\n"
"v_add_f32_e32 v145, v145, v113\n"
"v_mfma_f32_16x16x32_bf16 v[120:123], a[212:215], v[172:175], v[120:123]\n"
"ds_write_b128 v3, v[104:107] offset:13312\n"
"v_mfma_f32_16x16x32_bf16 v[120:123], a[216:219], v[116:119], v[120:123]\n"
"buffer_load_dwordx4 v[104:107], v1, s[4:7], s98 offen sc1\n"
"v_mfma_f32_16x16x32_bf16 v[120:123], a[220:223], v[132:135], v[120:123]\n"
"ds_read_b128 v[108:111], v4 offset:2048\n"
"ds_read_b128 v[136:139], v5 offset:2048\n"
"v_mfma_f32_16x16x32_bf16 v[200:203], a[252:255], v[132:135], 0\n"
"s_waitcnt lgkmcnt(3)\n"
"v_add_f32_e32 v146, v146, v114\n"
"v_add_f32_e32 v147, v147, v115\n"
"v_mfma_f32_16x16x32_bf16 v[200:203], a[248:251], v[116:119], v[200:203]\n"
"buffer_load_dwordx4 v[112:115], v1, s[4:7], s98 offen offset:1024 sc1\n"
"v_mfma_f32_16x16x32_bf16 v[200:203], a[244:247], v[172:175], v[200:203]\n"
"v_add_f32_e32 v156, v156, v251\n"
"v_add_f32_e32 v157, v157, v250\n"
"v_mfma_f32_16x16x32_bf16 v[200:203], a[240:243], v[152:155], v[200:203]\n"
"v_add_f32_e32 v158, v158, v249\n"
"v_add_f32_e32 v159, v159, v248\n"
"v_mfma_f32_16x16x32_bf16 v[200:203], a[236:239], v[140:143], v[200:203]\n"
"buffer_load_dwordx4 v[116:119], v1, s[4:7], s98 offen offset:2048 sc1\n"
"v_mfma_f32_16x16x32_bf16 v[200:203], a[232:235], v[124:127], v[200:203]\n"
"ds_write_b128 v3, v[120:123] offset:14336\n"
"v_mfma_f32_16x16x32_bf16 v[200:203], a[228:231], v[160:163], v[200:203]\n"
"ds_read_b128 v[120:123], v4 offset:3072\n"
"ds_read_b128 v[152:155], v5 offset:3072\n"
"v_mfma_f32_16x16x32_bf16 v[200:203], a[224:227], v[148:151], v[200:203]\n"
"s_waitcnt vmcnt(11)\n"
"buffer_load_dwordx4 v[124:127], v1, s[4:7], s98 offen offset:3072 sc1\n"
"; @A G2 a.3\n"
"v_mfma_f32_16x16x32_bf16 v[172:175], a[128:131], v[176:179], v[144:147]\n"
"s_add_u32 s98, s53, 0x440000\n"
"v_add_f32_e32 v156, v156, v128\n"
"v_add_f32_e32 v157, v157, v129\n"
"v_mfma_f32_16x16x32_bf16 v[172:175], a[132:135], v[180:183], v[172:175]\n"
"s_waitcnt lgkmcnt(3)\n"
"v_add_f32_e32 v158, v158, v130\n"
"v_add_f32_e32 v159, v159, v131\n"
"v_mfma_f32_16x16x32_bf16 v[172:175], a[136:139], v[168:171], v[172:175]\n"
"buffer_load_dwordx4 v[128:131], v0, s[4:7], s98 offen sc1\n"
"v_mfma_f32_16x16x32_bf16 v[172:175], a[140:143], v[184:187], v[172:175]\n"
"s_waitcnt vmcnt(9)\n"
"v_add_f32_e32 v108, v108, v247\n"
"v_add_f32_e32 v109, v109, v246\n"
"v_mfma_f32_16x16x32_bf16 v[172:175], a[144:147], v[188:191], v[172:175]\n"
"v_add_f32_e32 v110, v110, v245\n"
"v_add_f32_e32 v111, v111, v244\n"
"v_mfma_f32_16x16x32_bf16 v[172:175], a[148:151], v[164:167], v[172:175]\n"
"ds_write_b128 v3, v[200:203] offset:15360\n"
"v_mfma_f32_16x16x32_bf16 v[172:175], a[152:155], v[192:195], v[172:175]\n"
"buffer_load_dwordx4 v[132:135], v0, s[4:7], s98 offen offset:1024 sc1\n"
"v_mfma_f32_16x16x32_bf16 v[172:175], a[156:159], v[196:199], v[172:175]\n"
"v_add_f32_e32 v108, v108, v136\n"
"v_add_f32_e32 v109, v109, v137\n"
"v_mfma_f32_16x16x32_bf16 v[200:203], a[188:191], v[196:199], v[156:159]\n"
"s_waitcnt lgkmcnt(1)\n"
"v_add_f32_e32 v110, v110, v138\n"
"v_add_f32_e32 v111, v111, v139\n"
"v_mfma_f32_16x16x32_bf16 v[200:203], a[184:187], v[192:195], v[200:203]\n"
"s_waitcnt lgkmcnt(0)\n"
"s_barrier\n"
"v_mfma_f32_16x16x32_bf16 v[200:203], a[180:183], v[164:167], v[200:203]\n"
"buffer_load_dwordx4 v[136:139], v0, s[4:7], s98 offen offset:2048 sc1\n"
"v_mfma_f32_16x16x32_bf16 v[200:203], a[176:179], v[188:191], v[200:203]\n"
"v_add_f32_e32 v120, v120, v243\n"
"v_add_f32_e32 v121, v121, v242\n"
"v_mfma_f32_16x16x32_bf16 v[200:203], a[172:175], v[184:187], v[200:203]\n"
"v_cmp_u_f32_e32 vcc, v172, v172\n"
"v_add_f32_e32 v122, v122, v241\n"
"v_mfma_f32_16x16x32_bf16 v[200:203], a[168:171], v[168:171], v[200:203]\n"
"s_cbranch_vccnz .Lmk_p113s\n"
".Lmk_p113r:\n"
"v_add_f32_e32 v123, v123, v240\n"
"v_mfma_f32_16x16x32_bf16 v[200:203], a[164:167], v[180:183], v[200:203]\n"
"buffer_load_dwordx4 v[140:143], v0, s[4:7], s98 offen offset:3072 sc1\n"
"v_mfma_f32_16x16x32_bf16 v[200:203], a[160:163], v[176:179], v[200:203]\n"
"v_add_f32_e32 v120, v120, v152\n"
"v_add_f32_e32 v121, v121, v153\n"
"v_mfma_f32_16x16x32_bf16 v[108:111], a[192:195], v[176:179], v[108:111]\n"
"v_add_f32_e32 v122, v122, v154\n"
"v_add_f32_e32 v123, v123, v155\n"
"v_mfma_f32_16x16x32_bf16 v[108:111], a[196:199], v[180:183], v[108:111]\n"
"buffer_load_dwordx4 v[144:147], v1, s[4:7], s98 offen sc1\n"
"v_mfma_f32_16x16x32_bf16 v[108:111], a[200:203], v[168:171], v[108:111]\n"
"ds_read_b128 v[204:207], v6\n"
"ds_read_b128 v[208:211], v6 offset:1024\n"
"v_mfma_f32_16x16x32_bf16 v[108:111], a[204:207], v[184:187], v[108:111]\n"
"ds_read_b128 v[148:151], v6 offset:2048\n"
"ds_read_b128 v[152:155], v6 offset:3072\n"
"v_mfma_f32_16x16x32_bf16 v[108:111], a[208:211], v[188:191], v[108:111]\n"
"buffer_load_dwordx4 v[156:159], v1, s[4:7], s98 offen offset:1024 sc1\n"
"v_mfma_f32_16x16x32_bf16 v[108:111], a[212:215], v[164:167], v[108:111]\n"
"v_mfma_f32_16x16x32_bf16 v[108:111], a[216:219], v[192:195], v[108:111]\n"
"v_mfma_f32_16x16x32_bf16 v[108:111], a[220:223], v[196:199], v[108:111]\n"
"buffer_load_dwordx4 v[160:163], v1, s[4:7], s98 offen offset:2048 sc1\n"
"v_mfma_f32_16x16x32_bf16 v[120:123], a[252:255], v[196:199], v[120:123]\n"
"s_waitcnt lgkmcnt(0)\n"
"v_add_f32_e32 v204, v172, v204\n"
"v_add_f32_e32 v205, v173, v205\n"
"v_mfma_f32_16x16x32_bf16 v[120:123], a[248:251], v[192:195], v[120:123]\n"
"v_add_f32_e32 v206, v174, v206\n"
"v_add_f32_e32 v207, v175, v207\n"
"v_mfma_f32_16x16x32_bf16 v[120:123], a[244:247], v[164:167], v[120:123]\n"
"buffer_load_dwordx4 v[164:167], v1, s[4:7], s98 offen offset:3072 sc1\n"
"v_mfma_f32_16x16x32_bf16 v[120:123], a[240:243], v[188:191], v[120:123]\n"
"v_add_f32_e32 v208, v200, v208\n"
"v_add_f32_e32 v209, v201, v209\n"
"v_mfma_f32_16x16x32_bf16 v[120:123], a[236:239], v[184:187], v[120:123]\n"
"v_add_f32_e32 v210, v202, v210\n"
"v_add_f32_e32 v211, v203, v211\n"
"v_mfma_f32_16x16x32_bf16 v[120:123], a[232:235], v[168:171], v[120:123]\n"
"v_cvt_pk_bf16_f32 v168, v204, v205\n"
"v_cvt_pk_bf16_f32 v169, v206, v207\n"
"v_mfma_f32_16x16x32_bf16 v[120:123], a[228:231], v[180:183], v[120:123]\n"
"s_add_u32 s98, s61, 0x400000\n"
"v_cvt_pk_bf16_f32 v170, v208, v209\n"
"v_cvt_pk_bf16_f32 v171, v210, v211\n"
"v_mfma_f32_16x16x32_bf16 v[120:123], a[224:227], v[176:179], v[120:123]\n"
"buffer_store_dwordx4 v[168:171], v0, s[4:7], s98 offen sc1\n"
"ds_write_b128 v2, v[168:171]\n"
"s_branch .Lmk_loop\n"
".Lmk_f2s:\n"
";\n"
"s_mov_b32 s1, 0x10000\n"
".Lmk_f2l:\n"
"buffer_load_dwordx2 v[254:255], v13, s[4:7], s98 offen sc1\n"
"s_waitcnt vmcnt(0)\n"
"v_cmp_ne_u64_e32 vcc, v[254:255], v[18:19]\n"
"s_cbranch_vccz .Lmk_f2r\n"
"s_sub_u32 s1, s1, 1\n"
"s_cbranch_scc0 .Lmk_f2l\n"
"s_branch .Lmk_f2r\n"
".Lmk_f1s:\n"
";\n"
"s_mov_b32 s1, 0x10000\n"
".Lmk_f1l:\n"
"buffer_load_dwordx2 v[254:255], v12, s[4:7], s98 offen sc1\n"
"s_waitcnt vmcnt(0)\n"
"v_cmp_ne_u64_e32 vcc, v[254:255], v[18:19]\n"
"s_cbranch_vccz .Lmk_f1r\n"
"s_sub_u32 s1, s1, 1\n"
"s_cbranch_scc0 .Lmk_f1l\n"
"s_branch .Lmk_f1r\n"
".Lmk_f6s:\n"
";\n"
"s_mov_b32 s1, 0x10000\n"
".Lmk_f6l:\n"
"buffer_load_dwordx2 v[250:251], v41, s[4:7], s97 offen sc1\n"
"s_waitcnt vmcnt(0)\n"
"v_cmp_ne_u64_e32 vcc, v[250:251], v[18:19]\n"
"s_cbranch_vccz .Lmk_f6r\n"
"s_sub_u32 s1, s1, 1\n"
"s_cbranch_scc0 .Lmk_f6l\n"
"s_branch .Lmk_f6r\n"
".Lmk_f3s:\n"
";\n"
"s_mov_b32 s1, 0x10000\n"
".Lmk_f3l:\n"
"buffer_load_dwordx2 v[246:247], v12, s[4:7], s97 offen sc1\n"
"s_waitcnt vmcnt(0)\n"
"v_cmp_ne_u64_e32 vcc, v[246:247], v[18:19]\n"
"s_cbranch_vccz .Lmk_f3r\n"
"s_sub_u32 s1, s1, 1\n"
"s_cbranch_scc0 .Lmk_f3l\n"
"s_branch .Lmk_f3r\n"
".Lmk_f7s:\n"
";\n"
"s_mov_b32 s1, 0x10000\n"
".Lmk_f7l:\n"
"buffer_load_dwordx2 v[246:247], v13, s[4:7], s98 offen sc1\n"
"s_waitcnt vmcnt(0)\n"
"v_cmp_ne_u64_e32 vcc, v[246:247], v[18:19]\n"
"s_cbranch_vccz .Lmk_f7r\n"
"s_sub_u32 s1, s1, 1\n"
"s_cbranch_scc0 .Lmk_f7l\n"
"s_branch .Lmk_f7r\n"
".Lmk_f4s:\n"
";\n"
"s_mov_b32 s1, 0x10000\n"
".Lmk_f4l:\n"
"buffer_load_dwordx2 v[250:251], off, s[4:7], s96 sc1\n"
"s_waitcnt vmcnt(0)\n"
"v_cmp_ne_u64_e32 vcc, v[250:251], v[18:19]\n"
"s_cbranch_vccz .Lmk_f4r\n"
"s_sub_u32 s1, s1, 1\n"
"s_cbranch_scc0 .Lmk_f4l\n"
"s_branch .Lmk_f4r\n"
".Lmk_x18s:\n"
"s_nop 3\n"
"v_mov_b32_e32 v24, 0xff800000\n"
"v_mov_b32_e32 v25, 0xff800000\n"
"v_mov_b32_e32 v26, 0xff800000\n"
"v_mov_b32_e32 v27, 0xff800000\n"
"v_mov_b32_e32 v36, 0xff800000\n"
"v_mov_b32_e32 v37, 0xff800000\n"
"v_mov_b32_e32 v38, 0xff800000\n"
"v_mov_b32_e32 v39, 0xff800000\n"
"s_nop 1\n"
"s_branch .Lmk_x18r\n"
".Lmk_x19s:\n"
"s_nop 3\n"
"v_mov_b32_e32 v24, 0xff800000\n"
"v_mov_b32_e32 v25, 0xff800000\n"
"v_mov_b32_e32 v26, 0xff800000\n"
"v_mov_b32_e32 v27, 0xff800000\n"
"v_mov_b32_e32 v36, 0xff800000\n"
"v_mov_b32_e32 v37, 0xff800000\n"
"v_mov_b32_e32 v38, 0xff800000\n"
"v_mov_b32_e32 v39, 0xff800000\n"
"s_nop 1\n"
"s_branch .Lmk_x19r\n"
".Lmk_x20s:\n"
"s_nop 3\n"
"v_mov_b32_e32 v24, 0xff800000\n"
"v_mov_b32_e32 v25, 0xff800000\n"
"v_mov_b32_e32 v26, 0xff800000\n"
"v_mov_b32_e32 v27, 0xff800000\n"
"v_mov_b32_e32 v36, 0xff800000\n"
"v_mov_b32_e32 v37, 0xff800000\n"
"v_mov_b32_e32 v38, 0xff800000\n"
"v_mov_b32_e32 v39, 0xff800000\n"
"s_nop 1\n"
"s_branch .Lmk_x20r\n"
".Lmk_x21s:\n"
"s_nop 3\n"
"v_mov_b32_e32 v24, 0xff800000\n"
"v_mov_b32_e32 v25, 0xff800000\n"
"v_mov_b32_e32 v26, 0xff800000\n"
"v_mov_b32_e32 v27, 0xff800000\n"
"v_mov_b32_e32 v36, 0xff800000\n"
"v_mov_b32_e32 v37, 0xff800000\n"
"v_mov_b32_e32 v38, 0xff800000\n"
"v_mov_b32_e32 v39, 0xff800000\n"
"s_nop 1\n"
"s_branch .Lmk_x21r\n"
".Lmk_f14s:\n"
";\n"
"s_mov_b32 s1, 0x10000\n"
".Lmk_f14l:\n"
"buffer_load_dwordx2 v[238:239], off, s[4:7], s97 sc1\n"
"s_waitcnt vmcnt(0)\n"
"v_cmp_ne_u64_e32 vcc, v[238:239], v[18:19]\n"
"s_cbranch_vccz .Lmk_f14r\n"
"s_sub_u32 s1, s1, 1\n"
"s_cbranch_scc0 .Lmk_f14l\n"
"s_branch .Lmk_f14r\n"
".Lmk_b16:\n"
"s_cmp_lg_u32 s2, 0\n"
"s_cbranch_scc1 .Lmk_k22\n"
"s_mov_b32 s2, 1\n"
"s_waitcnt vmcnt(0) lgkmcnt(0)\n"
"s_nop 7\n"
"s_mov_b32 s97, 11\n"
"v_add_u32_e32 v248, 0x3800, v15\n"
"v_add_u32_e32 v247, 0x3800, v254\n"
"ds_read_b128 v[36:39], v15 offset:24576\n"
"ds_read_b128 v[40:43], v254 offset:24832\n"
"s_waitcnt lgkmcnt(0)\n"
"v_mfma_f32_16x16x32_bf16 v[48:51], v[36:39], v[28:31], 0\n"
"v_mfma_f32_16x16x32_bf16 v[52:55], v[36:39], v[32:35], 0\n"
"v_mfma_f32_16x16x32_bf16 v[36:39], v[40:43], v[32:35], 0\n"
"v_mfma_f32_16x16x32_bf16 v[56:59], v[40:43], v[28:31], 0\n"
".Lmk_b16l:\n"
"ds_read_b128 v[40:43], v248 offset:8192\n"
"ds_read_b128 v[60:63], v247 offset:8448\n"
"s_cmp_gt_i32 s78, s97\n"
"s_cselect_b32 s96, 0xff800000, 0\n"
"v_mov_b32_e32 v64, s96\n"
"v_mov_b32_e32 v65, s96\n"
"v_mov_b32_e32 v66, s96\n"
"v_mov_b32_e32 v67, s96\n"
"v_subrev_u32_e32 v248, 0x800, v248\n"
"v_subrev_u32_e32 v247, 0x800, v247\n"
"s_waitcnt lgkmcnt(0)\n"
"v_mfma_f32_16x16x32_bf16 v[72:75], v[40:43], v[28:31], v[64:67]\n"
"v_mfma_f32_16x16x32_bf16 v[76:79], v[40:43], v[32:35], v[64:67]\n"
"v_mfma_f32_16x16x32_bf16 v[40:43], v[60:63], v[32:35], v[64:67]\n"
"v_mfma_f32_16x16x32_bf16 v[84:87], v[60:63], v[28:31], v[64:67]\n"
"s_nop 7\n"
"s_cmp_lg_u32 s97, 4\n"
"s_cbranch_scc1 .Lmk_b16m\n"
"v_cndmask_b32_e64 v72, v48, v72, s[20:21]\n"
"v_cndmask_b32_e64 v73, v49, v73, s[22:23]\n"
"v_cndmask_b32_e64 v74, v50, v74, s[24:25]\n"
"v_cndmask_b32_e64 v75, v51, v75, s[26:27]\n"
"v_cndmask_b32_e64 v84, v56, v84, s[28:29]\n"
"v_cndmask_b32_e64 v85, v57, v85, s[30:31]\n"
"v_cndmask_b32_e64 v86, v58, v86, s[32:33]\n"
"v_cndmask_b32_e64 v87, v59, v87, s[34:35]\n"
"v_cndmask_b32_e64 v76, v52, v76, s[36:37]\n"
"v_cndmask_b32_e64 v77, v53, v77, s[38:39]\n"
"v_cndmask_b32_e64 v78, v54, v78, s[40:41]\n"
"v_cndmask_b32_e64 v79, v55, v79, s[42:43]\n"
"v_cndmask_b32_e64 v40, v36, v40, s[44:45]\n"
"v_cndmask_b32_e64 v41, v37, v41, s[46:47]\n"
"v_cndmask_b32_e64 v42, v38, v42, s[48:49]\n"
"v_cndmask_b32_e64 v43, v39, v43, s[50:51]\n"
".Lmk_b16m:\n"
"v_max3_f32 v253, v253, v72, v73\n"
"v_max3_f32 v251, v251, v76, v77\n"
"v_max3_f32 v253, v253, v74, v75\n"
"v_max3_f32 v251, v251, v78, v79\n"
"v_max3_f32 v253, v253, v84, v85\n"
"v_max3_f32 v251, v251, v40, v41\n"
"v_max3_f32 v253, v253, v86, v87\n"
"v_max3_f32 v251, v251, v42, v43\n"
"s_sub_u32 s97, s97, 1\n"
"s_cmp_ge_i32 s97, 4\n"
"s_cbranch_scc1 .Lmk_b16l\n"
"v_mov_b32_e32 v248, v253\n"
"v_mov_b32_e32 v247, v251\n"
"s_nop 0\n"
"v_permlane16_swap_b32_e32 v253, v248\n"
"v_permlane16_swap_b32_e32 v251, v247\n"
"v_max_f32_e32 v253, v253, v248\n"
"v_max_f32_e32 v251, v251, v247\n"
"v_mov_b32_e32 v248, v253\n"
"v_mov_b32_e32 v247, v251\n"
"s_nop 0\n"
"v_permlane32_swap_b32_e32 v253, v248\n"
"v_permlane32_swap_b32_e32 v251, v247\n"
"v_max_f32_e32 v253, v253, v248\n"
"v_max_f32_e32 v251, v251, v247\n"
"v_mov_b32_e32 v250, v253\n"
"v_mov_b32_e32 v249, v251\n"
"s_branch .Lmk_a15\n"
".Lmk_x25s:\n"
"s_nop 3\n"
"v_mov_b32_e32 v116, 0xff800000\n"
"v_mov_b32_e32 v117, 0xff800000\n"
"v_mov_b32_e32 v118, 0xff800000\n"
"v_mov_b32_e32 v119, 0xff800000\n"
"v_mov_b32_e32 v120, 0xff800000\n"
"v_mov_b32_e32 v121, 0xff800000\n"
"v_mov_b32_e32 v122, 0xff800000\n"
"v_mov_b32_e32 v123, 0xff800000\n"
"s_nop 1\n"
"s_branch .Lmk_x25r\n"
".Lmk_x26s:\n"
"s_nop 3\n"
"v_mov_b32_e32 v116, 0xff800000\n"
"v_mov_b32_e32 v117, 0xff800000\n"
"v_mov_b32_e32 v118, 0xff800000\n"
"v_mov_b32_e32 v119, 0xff800000\n"
"v_mov_b32_e32 v120, 0xff800000\n"
"v_mov_b32_e32 v121, 0xff800000\n"
"v_mov_b32_e32 v122, 0xff800000\n"
"v_mov_b32_e32 v123, 0xff800000\n"
"s_nop 1\n"
"s_branch .Lmk_x26r\n"
".Lmk_x27s:\n"
"s_nop 3\n"
"v_mov_b32_e32 v116, 0xff800000\n"
"v_mov_b32_e32 v117, 0xff800000\n"
"v_mov_b32_e32 v118, 0xff800000\n"
"v_mov_b32_e32 v119, 0xff800000\n"
"v_mov_b32_e32 v120, 0xff800000\n"
"v_mov_b32_e32 v121, 0xff800000\n"
"v_mov_b32_e32 v122, 0xff800000\n"
"v_mov_b32_e32 v123, 0xff800000\n"
"s_nop 1\n"
"s_branch .Lmk_x27r\n"
".Lmk_f5s:\n"
";\n"
"s_mov_b32 s1, 0x10000\n"
".Lmk_f5l:\n"
"buffer_load_dwordx2 v[246:247], v12, s[4:7], s96 offen sc1\n"
"s_waitcnt vmcnt(0)\n"
"v_cmp_ne_u64_e32 vcc, v[246:247], v[18:19]\n"
"s_cbranch_vccz .Lmk_f5r\n"
"s_sub_u32 s1, s1, 1\n"
"s_cbranch_scc0 .Lmk_f5l\n"
"s_branch .Lmk_f5r\n"
".Lmk_b24:\n"
"s_cmp_lg_u32 s2, 0\n"
"s_cbranch_scc1 .Lmk_k29\n"
"s_mov_b32 s2, 1\n"
"s_waitcnt vmcnt(0) lgkmcnt(0)\n"
"s_nop 7\n"
"s_mov_b32 s96, 7\n"
"v_add_u32_e32 v248, 0x3800, v15\n"
"v_add_u32_e32 v247, 0x3800, v254\n"
"ds_read_b128 v[172:175], v15 offset:16384\n"
"ds_read_b128 v[192:195], v254 offset:16640\n"
"s_waitcnt lgkmcnt(0)\n"
"v_mfma_f32_16x16x32_bf16 v[196:199], v[172:175], v[92:95], 0\n"
"v_mfma_f32_16x16x32_bf16 v[212:215], v[172:175], v[96:99], 0\n"
"v_mfma_f32_16x16x32_bf16 v[172:175], v[192:195], v[96:99], 0\n"
"v_mfma_f32_16x16x32_bf16 v[220:223], v[192:195], v[92:95], 0\n"
".Lmk_b24l:\n"
"ds_read_b128 v[192:195], v248\n"
"ds_read_b128 v[224:227], v247 offset:256\n"
"s_cmp_gt_i32 s78, s96\n"
"s_cselect_b32 s95, 0xff800000, 0\n"
"v_mov_b32_e32 v228, s95\n"
"v_mov_b32_e32 v229, s95\n"
"v_mov_b32_e32 v230, s95\n"
"v_mov_b32_e32 v231, s95\n"
"v_subrev_u32_e32 v248, 0x800, v248\n"
"v_subrev_u32_e32 v247, 0x800, v247\n"
"s_waitcnt lgkmcnt(0)\n"
"v_mfma_f32_16x16x32_bf16 v[232:235], v[192:195], v[92:95], v[228:231]\n"
"v_mfma_f32_16x16x32_bf16 v[236:239], v[192:195], v[96:99], v[228:231]\n"
"v_mfma_f32_16x16x32_bf16 v[192:195], v[224:227], v[96:99], v[228:231]\n"
"v_mfma_f32_16x16x32_bf16 v[240:243], v[224:227], v[92:95], v[228:231]\n"
"s_nop 7\n"
"s_cmp_lg_u32 s96, 0\n"
"s_cbranch_scc1 .Lmk_b24m\n"
"v_cndmask_b32_e64 v232, v196, v232, s[20:21]\n"
"v_cndmask_b32_e64 v233, v197, v233, s[22:23]\n"
"v_cndmask_b32_e64 v234, v198, v234, s[24:25]\n"
"v_cndmask_b32_e64 v235, v199, v235, s[26:27]\n"
"v_cndmask_b32_e64 v240, v220, v240, s[28:29]\n"
"v_cndmask_b32_e64 v241, v221, v241, s[30:31]\n"
"v_cndmask_b32_e64 v242, v222, v242, s[32:33]\n"
"v_cndmask_b32_e64 v243, v223, v243, s[34:35]\n"
"v_cndmask_b32_e64 v236, v212, v236, s[36:37]\n"
"v_cndmask_b32_e64 v237, v213, v237, s[38:39]\n"
"v_cndmask_b32_e64 v238, v214, v238, s[40:41]\n"
"v_cndmask_b32_e64 v239, v215, v239, s[42:43]\n"
"v_cndmask_b32_e64 v192, v172, v192, s[44:45]\n"
"v_cndmask_b32_e64 v193, v173, v193, s[46:47]\n"
"v_cndmask_b32_e64 v194, v174, v194, s[48:49]\n"
"v_cndmask_b32_e64 v195, v175, v195, s[50:51]\n"
".Lmk_b24m:\n"
"v_max3_f32 v253, v253, v232, v233\n"
"v_max3_f32 v251, v251, v236, v237\n"
"v_max3_f32 v253, v253, v234, v235\n"
"v_max3_f32 v251, v251, v238, v239\n"
"v_max3_f32 v253, v253, v240, v241\n"
"v_max3_f32 v251, v251, v192, v193\n"
"v_max3_f32 v253, v253, v242, v243\n"
"v_max3_f32 v251, v251, v194, v195\n"
"s_sub_u32 s96, s96, 1\n"
"s_cmp_ge_i32 s96, 0\n"
"s_cbranch_scc1 .Lmk_b24l\n"
"v_mov_b32_e32 v248, v253\n"
"v_mov_b32_e32 v247, v251\n"
"s_nop 0\n"
"v_permlane16_swap_b32_e32 v253, v248\n"
"v_permlane16_swap_b32_e32 v251, v247\n"
"v_max_f32_e32 v253, v253, v248\n"
"v_max_f32_e32 v251, v251, v247\n"
"v_mov_b32_e32 v248, v253\n"
"v_mov_b32_e32 v247, v251\n"
"s_nop 0\n"
"v_permlane32_swap_b32_e32 v253, v248\n"
"v_permlane32_swap_b32_e32 v251, v247\n"
"v_max_f32_e32 v253, v253, v248\n"
"v_max_f32_e32 v251, v251, v247\n"
"v_mov_b32_e32 v250, v253\n"
"v_mov_b32_e32 v249, v251\n"
"s_branch .Lmk_a23\n"
".Lmk_p30s:\n"
";\n"
"s_waitcnt vmcnt(16)\n"
"s_mov_b32 s1, 0x10000\n"
".Lmk_p30l:\n"
"s_add_u32 s98, s56, 0x40000\n"
"buffer_load_dwordx4 v[176:179], v0, s[4:7], s98 offen sc1\n"
"buffer_load_dwordx4 v[200:203], v0, s[4:7], s98 offen offset:1024 sc1\n"
"buffer_load_dwordx4 v[188:191], v0, s[4:7], s98 offen offset:2048 sc1\n"
"buffer_load_dwordx4 v[204:207], v0, s[4:7], s98 offen offset:3072 sc1\n"
"buffer_load_dwordx4 v[152:155], v1, s[4:7], s98 offen sc1\n"
"buffer_load_dwordx4 v[208:211], v1, s[4:7], s98 offen offset:1024 sc1\n"
"buffer_load_dwordx4 v[120:123], v1, s[4:7], s98 offen offset:2048 sc1\n"
"buffer_load_dwordx4 v[132:135], v1, s[4:7], s98 offen offset:3072 sc1\n"
"s_add_u32 s98, s57, 0x40000\n"
"buffer_load_dwordx4 v[216:219], v0, s[4:7], s98 offen sc1\n"
"buffer_load_dwordx4 v[128:131], v0, s[4:7], s98 offen offset:1024 sc1\n"
"buffer_load_dwordx4 v[180:183], v0, s[4:7], s98 offen offset:2048 sc1\n"
"buffer_load_dwordx4 v[100:103], v0, s[4:7], s98 offen offset:3072 sc1\n"
"buffer_load_dwordx4 v[184:187], v1, s[4:7], s98 offen sc1\n"
"buffer_load_dwordx4 v[104:107], v1, s[4:7], s98 offen offset:1024 sc1\n"
"buffer_load_dwordx4 v[108:111], v1, s[4:7], s98 offen offset:2048 sc1\n"
"buffer_load_dwordx4 v[136:139], v1, s[4:7], s98 offen offset:3072 sc1\n"
"s_add_u32 s98, s58, 0x40000\n"
"buffer_load_dwordx4 v[144:147], v0, s[4:7], s98 offen sc1\n"
"buffer_load_dwordx4 v[140:143], v0, s[4:7], s98 offen offset:1024 sc1\n"
"buffer_load_dwordx4 v[116:119], v0, s[4:7], s98 offen offset:2048 sc1\n"
"buffer_load_dwordx4 v[112:115], v0, s[4:7], s98 offen offset:3072 sc1\n"
"buffer_load_dwordx4 v[92:95], v1, s[4:7], s98 offen sc1\n"
"buffer_load_dwordx4 v[96:99], v1, s[4:7], s98 offen offset:1024 sc1\n"
"buffer_load_dwordx4 v[148:151], v1, s[4:7], s98 offen offset:2048 sc1\n"
"s_waitcnt vmcnt(0)\n"
"v_max_u32_e32 v255, v176, v177\n"
"v_max3_u32 v255, v255, v178, v179\n"
"v_max3_u32 v255, v255, v200, v201\n"
"v_max3_u32 v255, v255, v202, v203\n"
"v_max3_u32 v255, v255, v188, v189\n"
"v_max3_u32 v255, v255, v190, v191\n"
"v_max3_u32 v255, v255, v204, v205\n"
"v_max3_u32 v255, v255, v206, v207\n"
"v_max3_u32 v255, v255, v152, v153\n"
"v_max3_u32 v255, v255, v154, v155\n"
"v_max3_u32 v255, v255, v208, v209\n"
"v_max3_u32 v255, v255, v210, v211\n"
"v_max3_u32 v255, v255, v120, v121\n"
"v_max3_u32 v255, v255, v122, v123\n"
"v_max3_u32 v255, v255, v132, v133\n"
"v_max3_u32 v255, v255, v134, v135\n"
"v_cmp_eq_u32_e32 vcc, -1, v255\n"
"s_cbranch_vccz .Lmk_p30k\n"
"s_sub_u32 s1, s1, 1\n"
"s_cbranch_scc0 .Lmk_p30l\n"
".Lmk_p30k:\n"
"v_mfma_f32_16x16x32_bf16 v[88:91], a[128:131], v[176:179], 0\n"
"v_mfma_f32_16x16x32_bf16 v[88:91], a[132:135], v[200:203], v[88:91]\n"
"v_mfma_f32_16x16x32_bf16 v[88:91], a[136:139], v[188:191], v[88:91]\n"
"v_mfma_f32_16x16x32_bf16 v[88:91], a[140:143], v[204:207], v[88:91]\n"
"v_mfma_f32_16x16x32_bf16 v[88:91], a[144:147], v[152:155], v[88:91]\n"
"v_mfma_f32_16x16x32_bf16 v[88:91], a[148:151], v[208:211], v[88:91]\n"
"v_mfma_f32_16x16x32_bf16 v[88:91], a[152:155], v[120:123], v[88:91]\n"
"v_mfma_f32_16x16x32_bf16 v[88:91], a[156:159], v[132:135], v[88:91]\n"
"v_mfma_f32_16x16x32_bf16 v[124:127], a[188:191], v[132:135], 0\n"
"v_mfma_f32_16x16x32_bf16 v[124:127], a[184:187], v[120:123], v[124:127]\n"
"v_mfma_f32_16x16x32_bf16 v[124:127], a[180:183], v[208:211], v[124:127]\n"
"v_mfma_f32_16x16x32_bf16 v[124:127], a[176:179], v[152:155], v[124:127]\n"
"v_mfma_f32_16x16x32_bf16 v[124:127], a[172:175], v[204:207], v[124:127]\n"
"v_mfma_f32_16x16x32_bf16 v[124:127], a[168:171], v[188:191], v[124:127]\n"
"s_nop 7\n"
"s_branch .Lmk_p30r\n"
".Lmk_p31s:\n"
";\n"
"s_waitcnt vmcnt(16)\n"
"s_mov_b32 s1, 0x10000\n"
".Lmk_p31l:\n"
"s_add_u32 s97, s57, 0x40000\n"
"buffer_load_dwordx4 v[216:219], v0, s[4:7], s97 offen sc1\n"
"buffer_load_dwordx4 v[128:131], v0, s[4:7], s97 offen offset:1024 sc1\n"
"buffer_load_dwordx4 v[180:183], v0, s[4:7], s97 offen offset:2048 sc1\n"
"buffer_load_dwordx4 v[100:103], v0, s[4:7], s97 offen offset:3072 sc1\n"
"buffer_load_dwordx4 v[184:187], v1, s[4:7], s97 offen sc1\n"
"buffer_load_dwordx4 v[104:107], v1, s[4:7], s97 offen offset:1024 sc1\n"
"buffer_load_dwordx4 v[108:111], v1, s[4:7], s97 offen offset:2048 sc1\n"
"buffer_load_dwordx4 v[136:139], v1, s[4:7], s97 offen offset:3072 sc1\n"
"s_add_u32 s97, s58, 0x40000\n"
"buffer_load_dwordx4 v[144:147], v0, s[4:7], s97 offen sc1\n"
"buffer_load_dwordx4 v[140:143], v0, s[4:7], s97 offen offset:1024 sc1\n"
"buffer_load_dwordx4 v[116:119], v0, s[4:7], s97 offen offset:2048 sc1\n"
"buffer_load_dwordx4 v[112:115], v0, s[4:7], s97 offen offset:3072 sc1\n"
"buffer_load_dwordx4 v[92:95], v1, s[4:7], s97 offen sc1\n"
"buffer_load_dwordx4 v[96:99], v1, s[4:7], s97 offen offset:1024 sc1\n"
"buffer_load_dwordx4 v[148:151], v1, s[4:7], s97 offen offset:2048 sc1\n"
"buffer_load_dwordx4 v[88:91], v1, s[4:7], s97 offen offset:3072 sc1\n"
"s_add_u32 s97, s59, 0x40000\n"
"buffer_load_dwordx4 v[160:163], v0, s[4:7], s97 offen sc1\n"
"buffer_load_dwordx4 v[124:127], v0, s[4:7], s97 offen offset:1024 sc1\n"
"buffer_load_dwordx4 v[120:123], v0, s[4:7], s97 offen offset:2048 sc1\n"
"buffer_load_dwordx4 v[132:135], v0, s[4:7], s97 offen offset:3072 sc1\n"
"buffer_load_dwordx4 v[156:159], v1, s[4:7], s97 offen sc1\n"
"buffer_load_dwordx4 v[164:167], v1, s[4:7], s97 offen offset:1024 sc1\n"
"buffer_load_dwordx4 v[172:175], v1, s[4:7], s97 offen offset:2048 sc1\n"
"s_waitcnt vmcnt(0)\n"
"v_max_u32_e32 v255, v216, v217\n"
"v_max3_u32 v255, v255, v218, v219\n"
"v_max3_u32 v255, v255, v128, v129\n"
"v_max3_u32 v255, v255, v130, v131\n"
"v_max3_u32 v255, v255, v180, v181\n"
"v_max3_u32 v255, v255, v182, v183\n"
"v_max3_u32 v255, v255, v100, v101\n"
"v_max3_u32 v255, v255, v102, v103\n"
"v_max3_u32 v255, v255, v184, v185\n"
"v_max3_u32 v255, v255, v186, v187\n"
"v_max3_u32 v255, v255, v104, v105\n"
"v_max3_u32 v255, v255, v106, v107\n"
"v_max3_u32 v255, v255, v108, v109\n"
"v_max3_u32 v255, v255, v110, v111\n"
"v_max3_u32 v255, v255, v136, v137\n"
"v_max3_u32 v255, v255, v138, v139\n"
"v_cmp_eq_u32_e32 vcc, -1, v255\n"
"s_cbranch_vccz .Lmk_p31k\n"
"s_sub_u32 s1, s1, 1\n"
"s_cbranch_scc0 .Lmk_p31l\n"
".Lmk_p31k:\n"
"v_mfma_f32_16x16x32_bf16 v[152:155], a[128:131], v[216:219], 0\n"
"v_mfma_f32_16x16x32_bf16 v[152:155], a[132:135], v[128:131], v[152:155]\n"
"v_mfma_f32_16x16x32_bf16 v[152:155], a[136:139], v[180:183], v[152:155]\n"
"v_mfma_f32_16x16x32_bf16 v[152:155], a[140:143], v[100:103], v[152:155]\n"
"v_mfma_f32_16x16x32_bf16 v[152:155], a[144:147], v[184:187], v[152:155]\n"
"v_mfma_f32_16x16x32_bf16 v[152:155], a[148:151], v[104:107], v[152:155]\n"
"v_mfma_f32_16x16x32_bf16 v[152:155], a[152:155], v[108:111], v[152:155]\n"
"v_mfma_f32_16x16x32_bf16 v[152:155], a[156:159], v[136:139], v[152:155]\n"
"v_mfma_f32_16x16x32_bf16 v[168:171], a[188:191], v[136:139], 0\n"
"v_mfma_f32_16x16x32_bf16 v[168:171], a[184:187], v[108:111], v[168:171]\n"
"v_mfma_f32_16x16x32_bf16 v[168:171], a[180:183], v[104:107], v[168:171]\n"
"v_mfma_f32_16x16x32_bf16 v[168:171], a[176:179], v[184:187], v[168:171]\n"
"v_mfma_f32_16x16x32_bf16 v[168:171], a[172:175], v[100:103], v[168:171]\n"
"v_mfma_f32_16x16x32_bf16 v[168:171], a[168:171], v[180:183], v[168:171]\n"
"s_nop 7\n"
"s_branch .Lmk_p31r\n"
".Lmk_p32s:\n"
";\n"
"s_waitcnt vmcnt(16)\n"
"s_mov_b32 s1, 0x10000\n"
".Lmk_p32l:\n"
"s_add_u32 s97, s58, 0x40000\n"
"buffer_load_dwordx4 v[144:147], v0, s[4:7], s97 offen sc1\n"
"buffer_load_dwordx4 v[140:143], v0, s[4:7], s97 offen offset:1024 sc1\n"
"buffer_load_dwordx4 v[116:119], v0, s[4:7], s97 offen offset:2048 sc1\n"
"buffer_load_dwordx4 v[112:115], v0, s[4:7], s97 offen offset:3072 sc1\n"
"buffer_load_dwordx4 v[92:95], v1, s[4:7], s97 offen sc1\n"
"buffer_load_dwordx4 v[96:99], v1, s[4:7], s97 offen offset:1024 sc1\n"
"buffer_load_dwordx4 v[148:151], v1, s[4:7], s97 offen offset:2048 sc1\n"
"buffer_load_dwordx4 v[88:91], v1, s[4:7], s97 offen offset:3072 sc1\n"
"s_add_u32 s97, s59, 0x40000\n"
"buffer_load_dwordx4 v[160:163], v0, s[4:7], s97 offen sc1\n"
"buffer_load_dwordx4 v[124:127], v0, s[4:7], s97 offen offset:1024 sc1\n"
"buffer_load_dwordx4 v[120:123], v0, s[4:7], s97 offen offset:2048 sc1\n"
"buffer_load_dwordx4 v[132:135], v0, s[4:7], s97 offen offset:3072 sc1\n"
"buffer_load_dwordx4 v[156:159], v1, s[4:7], s97 offen sc1\n"
"buffer_load_dwordx4 v[164:167], v1, s[4:7], s97 offen offset:1024 sc1\n"
"buffer_load_dwordx4 v[172:175], v1, s[4:7], s97 offen offset:2048 sc1\n"
"buffer_load_dwordx4 v[152:155], v1, s[4:7], s97 offen offset:3072 sc1\n"
"s_waitcnt vmcnt(0)\n"
"v_max_u32_e32 v239, v144, v145\n"
"v_max3_u32 v239, v239, v146, v147\n"
"v_max3_u32 v239, v239, v140, v141\n"
"v_max3_u32 v239, v239, v142, v143\n"
"v_max3_u32 v239, v239, v116, v117\n"
"v_max3_u32 v239, v239, v118, v119\n"
"v_max3_u32 v239, v239, v112, v113\n"
"v_max3_u32 v239, v239, v114, v115\n"
"v_max3_u32 v239, v239, v92, v93\n"
"v_max3_u32 v239, v239, v94, v95\n"
"v_max3_u32 v239, v239, v96, v97\n"
"v_max3_u32 v239, v239, v98, v99\n"
"v_max3_u32 v239, v239, v148, v149\n"
"v_max3_u32 v239, v239, v150, v151\n"
"v_max3_u32 v239, v239, v88, v89\n"
"v_max3_u32 v239, v239, v90, v91\n"
"v_cmp_eq_u32_e32 vcc, -1, v239\n"
"s_cbranch_vccz .Lmk_p32k\n"
"s_sub_u32 s1, s1, 1\n"
"s_cbranch_scc0 .Lmk_p32l\n"
".Lmk_p32k:\n"
"v_mfma_f32_16x16x32_bf16 v[100:103], a[128:131], v[144:147], 0\n"
"v_mfma_f32_16x16x32_bf16 v[100:103], a[132:135], v[140:143], v[100:103]\n"
"v_mfma_f32_16x16x32_bf16 v[100:103], a[136:139], v[116:119], v[100:103]\n"
"v_mfma_f32_16x16x32_bf16 v[100:103], a[140:143], v[112:115], v[100:103]\n"
"v_mfma_f32_16x16x32_bf16 v[100:103], a[144:147], v[92:95], v[100:103]\n"
"v_mfma_f32_16x16x32_bf16 v[100:103], a[148:151], v[96:99], v[100:103]\n"
"v_mfma_f32_16x16x32_bf16 v[100:103], a[152:155], v[148:151], v[100:103]\n"
"v_mfma_f32_16x16x32_bf16 v[100:103], a[156:159], v[88:91], v[100:103]\n"
"v_mfma_f32_16x16x32_bf16 v[104:107], a[188:191], v[88:91], 0\n"
"v_mfma_f32_16x16x32_bf16 v[104:107], a[184:187], v[148:151], v[104:107]\n"
"v_mfma_f32_16x16x32_bf16 v[104:107], a[180:183], v[96:99], v[104:107]\n"
"v_mfma_f32_16x16x32_bf16 v[104:107], a[176:179], v[92:95], v[104:107]\n"
"v_mfma_f32_16x16x32_bf16 v[104:107], a[172:175], v[112:115], v[104:107]\n"
"v_mfma_f32_16x16x32_bf16 v[104:107], a[168:171], v[116:119], v[104:107]\n"
"s_nop 7\n"
"s_branch .Lmk_p32r\n"
".Lmk_p33s:\n"
";\n"
"s_waitcnt vmcnt(16)\n"
"s_mov_b32 s1, 0x10000\n"
".Lmk_p33l:\n"
"s_add_u32 s97, s59, 0x40000\n"
"buffer_load_dwordx4 v[160:163], v0, s[4:7], s97 offen sc1\n"
"buffer_load_dwordx4 v[124:127], v0, s[4:7], s97 offen offset:1024 sc1\n"
"buffer_load_dwordx4 v[120:123], v0, s[4:7], s97 offen offset:2048 sc1\n"
"buffer_load_dwordx4 v[132:135], v0, s[4:7], s97 offen offset:3072 sc1\n"
"buffer_load_dwordx4 v[156:159], v1, s[4:7], s97 offen sc1\n"
"buffer_load_dwordx4 v[164:167], v1, s[4:7], s97 offen offset:1024 sc1\n"
"buffer_load_dwordx4 v[172:175], v1, s[4:7], s97 offen offset:2048 sc1\n"
"buffer_load_dwordx4 v[152:155], v1, s[4:7], s97 offen offset:3072 sc1\n"
"s_add_u32 s97, s56, 0\n"
"buffer_load_dwordx4 v[100:103], v0, s[4:7], s97 offen sc1\n"
"buffer_load_dwordx4 v[180:183], v0, s[4:7], s97 offen offset:1024 sc1\n"
"buffer_load_dwordx4 v[104:107], v0, s[4:7], s97 offen offset:2048 sc1\n"
"buffer_load_dwordx4 v[88:91], v0, s[4:7], s97 offen offset:3072 sc1\n"
"buffer_load_dwordx4 v[92:95], v1, s[4:7], s97 offen sc1\n"
"buffer_load_dwordx4 v[116:119], v1, s[4:7], s97 offen offset:1024 sc1\n"
"buffer_load_dwordx4 v[136:139], v1, s[4:7], s97 offen offset:2048 sc1\n"
"buffer_load_dwordx4 v[140:143], v1, s[4:7], s97 offen offset:3072 sc1\n"
"s_add_u32 s97, s57, 0\n"
"buffer_load_dwordx4 v[148:151], v0, s[4:7], s97 offen sc1\n"
"s_waitcnt vmcnt(0)\n"
"v_max_u32_e32 v255, v160, v161\n"
"v_max3_u32 v255, v255, v162, v163\n"
"v_max3_u32 v255, v255, v124, v125\n"
"v_max3_u32 v255, v255, v126, v127\n"
"v_max3_u32 v255, v255, v120, v121\n"
"v_max3_u32 v255, v255, v122, v123\n"
"v_max3_u32 v255, v255, v132, v133\n"
"v_max3_u32 v255, v255, v134, v135\n"
"v_max3_u32 v255, v255, v156, v157\n"
"v_max3_u32 v255, v255, v158, v159\n"
"v_max3_u32 v255, v255, v164, v165\n"
"v_max3_u32 v255, v255, v166, v167\n"
"v_max3_u32 v255, v255, v172, v173\n"
"v_max3_u32 v255, v255, v174, v175\n"
"v_max3_u32 v255, v255, v152, v153\n"
"v_max3_u32 v255, v255, v154, v155\n"
"v_cmp_eq_u32_e32 vcc, -1, v255\n"
"s_cbranch_vccz .Lmk_p33k\n"
"s_sub_u32 s1, s1, 1\n"
"s_cbranch_scc0 .Lmk_p33l\n"
".Lmk_p33k:\n"
"v_mfma_f32_16x16x32_bf16 v[128:131], a[128:131], v[160:163], v[108:111]\n"
"v_mfma_f32_16x16x32_bf16 v[128:131], a[132:135], v[124:127], v[128:131]\n"
"v_mfma_f32_16x16x32_bf16 v[128:131], a[136:139], v[120:123], v[128:131]\n"
"v_mfma_f32_16x16x32_bf16 v[128:131], a[140:143], v[132:135], v[128:131]\n"
"v_mfma_f32_16x16x32_bf16 v[128:131], a[144:147], v[156:159], v[128:131]\n"
"v_mfma_f32_16x16x32_bf16 v[128:131], a[148:151], v[164:167], v[128:131]\n"
"v_mfma_f32_16x16x32_bf16 v[128:131], a[152:155], v[172:175], v[128:131]\n"
"v_mfma_f32_16x16x32_bf16 v[128:131], a[156:159], v[152:155], v[128:131]\n"
"v_mfma_f32_16x16x32_bf16 v[144:147], a[188:191], v[152:155], v[168:171]\n"
"v_mfma_f32_16x16x32_bf16 v[144:147], a[184:187], v[172:175], v[144:147]\n"
"v_mfma_f32_16x16x32_bf16 v[144:147], a[180:183], v[164:167], v[144:147]\n"
"v_mfma_f32_16x16x32_bf16 v[144:147], a[176:179], v[156:159], v[144:147]\n"
"v_mfma_f32_16x16x32_bf16 v[144:147], a[172:175], v[132:135], v[144:147]\n"
"v_mfma_f32_16x16x32_bf16 v[144:147], a[168:171], v[120:123], v[144:147]\n"
"s_nop 7\n"
"s_branch .Lmk_p33r\n"
".Lmk_p35s:\n"
";\n"
"s_waitcnt vmcnt(16)\n"
"s_mov_b32 s1, 0x10000\n"
".Lmk_p35l:\n"
"s_add_u32 s95, s56, 0\n"
"buffer_load_dwordx4 v[100:103], v0, s[4:7], s95 offen sc1\n"
"buffer_load_dwordx4 v[180:183], v0, s[4:7], s95 offen offset:1024 sc1\n"
"buffer_load_dwordx4 v[104:107], v0, s[4:7], s95 offen offset:2048 sc1\n"
"buffer_load_dwordx4 v[88:91], v0, s[4:7], s95 offen offset:3072 sc1\n"
"buffer_load_dwordx4 v[92:95], v1, s[4:7], s95 offen sc1\n"
"buffer_load_dwordx4 v[116:119], v1, s[4:7], s95 offen offset:1024 sc1\n"
"buffer_load_dwordx4 v[136:139], v1, s[4:7], s95 offen offset:2048 sc1\n"
"buffer_load_dwordx4 v[140:143], v1, s[4:7], s95 offen offset:3072 sc1\n"
"s_add_u32 s95, s57, 0\n"
"buffer_load_dwordx4 v[148:151], v0, s[4:7], s95 offen sc1\n"
"buffer_load_dwordx4 v[108:111], v0, s[4:7], s95 offen offset:1024 sc1\n"
"buffer_load_dwordx4 v[112:115], v0, s[4:7], s95 offen offset:2048 sc1\n"
"buffer_load_dwordx4 v[196:199], v0, s[4:7], s95 offen offset:3072 sc1\n"
"buffer_load_dwordx4 v[200:203], v1, s[4:7], s95 offen sc1\n"
"buffer_load_dwordx4 v[128:131], v1, s[4:7], s95 offen offset:1024 sc1\n"
"buffer_load_dwordx4 v[120:123], v1, s[4:7], s95 offen offset:2048 sc1\n"
"buffer_load_dwordx4 v[132:135], v1, s[4:7], s95 offen offset:3072 sc1\n"
"s_add_u32 s95, s58, 0\n"
"buffer_load_dwordx4 v[152:155], v0, s[4:7], s95 offen sc1\n"
"buffer_load_dwordx4 v[160:163], v0, s[4:7], s95 offen offset:1024 sc1\n"
"buffer_load_dwordx4 v[124:127], v0, s[4:7], s95 offen offset:2048 sc1\n"
"s_waitcnt vmcnt(0)\n"
"v_max_u32_e32 v255, v100, v101\n"
"v_max3_u32 v255, v255, v102, v103\n"
"v_max3_u32 v255, v255, v180, v181\n"
"v_max3_u32 v255, v255, v182, v183\n"
"v_max3_u32 v255, v255, v104, v105\n"
"v_max3_u32 v255, v255, v106, v107\n"
"v_max3_u32 v255, v255, v88, v89\n"
"v_max3_u32 v255, v255, v90, v91\n"
"v_max3_u32 v255, v255, v92, v93\n"
"v_max3_u32 v255, v255, v94, v95\n"
"v_max3_u32 v255, v255, v116, v117\n"
"v_max3_u32 v255, v255, v118, v119\n"
"v_max3_u32 v255, v255, v136, v137\n"
"v_max3_u32 v255, v255, v138, v139\n"
"v_max3_u32 v255, v255, v140, v141\n"
"v_max3_u32 v255, v255, v142, v143\n"
"v_cmp_eq_u32_e32 vcc, -1, v255\n"
"s_cbranch_vccz .Lmk_p35k\n"
"s_sub_u32 s1, s1, 1\n"
"s_cbranch_scc0 .Lmk_p35l\n"
".Lmk_p35k:\n"
"v_mfma_f32_16x16x32_bf16 v[144:147], a[128:131], v[100:103], 0\n"
"v_mfma_f32_16x16x32_bf16 v[144:147], a[132:135], v[180:183], v[144:147]\n"
"v_mfma_f32_16x16x32_bf16 v[144:147], a[136:139], v[104:107], v[144:147]\n"
"v_mfma_f32_16x16x32_bf16 v[144:147], a[140:143], v[88:91], v[144:147]\n"
"v_mfma_f32_16x16x32_bf16 v[144:147], a[144:147], v[92:95], v[144:147]\n"
"v_mfma_f32_16x16x32_bf16 v[144:147], a[148:151], v[116:119], v[144:147]\n"
"v_mfma_f32_16x16x32_bf16 v[144:147], a[152:155], v[136:139], v[144:147]\n"
"v_mfma_f32_16x16x32_bf16 v[144:147], a[156:159], v[140:143], v[144:147]\n"
"v_mfma_f32_16x16x32_bf16 v[96:99], a[188:191], v[140:143], 0\n"
"v_mfma_f32_16x16x32_bf16 v[96:99], a[184:187], v[136:139], v[96:99]\n"
"v_mfma_f32_16x16x32_bf16 v[96:99], a[180:183], v[116:119], v[96:99]\n"
"v_mfma_f32_16x16x32_bf16 v[96:99], a[176:179], v[92:95], v[96:99]\n"
"v_mfma_f32_16x16x32_bf16 v[96:99], a[172:175], v[88:91], v[96:99]\n"
"v_mfma_f32_16x16x32_bf16 v[96:99], a[168:171], v[104:107], v[96:99]\n"
"s_nop 7\n"
"s_branch .Lmk_p35r\n"
".Lmk_p36s:\n"
";\n"
"s_waitcnt vmcnt(16)\n"
"s_mov_b32 s1, 0x10000\n"
".Lmk_p36l:\n"
"s_add_u32 s97, s57, 0\n"
"buffer_load_dwordx4 v[148:151], v0, s[4:7], s97 offen sc1\n"
"buffer_load_dwordx4 v[108:111], v0, s[4:7], s97 offen offset:1024 sc1\n"
"buffer_load_dwordx4 v[112:115], v0, s[4:7], s97 offen offset:2048 sc1\n"
"buffer_load_dwordx4 v[196:199], v0, s[4:7], s97 offen offset:3072 sc1\n"
"buffer_load_dwordx4 v[200:203], v1, s[4:7], s97 offen sc1\n"
"buffer_load_dwordx4 v[128:131], v1, s[4:7], s97 offen offset:1024 sc1\n"
"buffer_load_dwordx4 v[120:123], v1, s[4:7], s97 offen offset:2048 sc1\n"
"buffer_load_dwordx4 v[132:135], v1, s[4:7], s97 offen offset:3072 sc1\n"
"s_add_u32 s97, s58, 0\n"
"buffer_load_dwordx4 v[152:155], v0, s[4:7], s97 offen sc1\n"
"buffer_load_dwordx4 v[160:163], v0, s[4:7], s97 offen offset:1024 sc1\n"
"buffer_load_dwordx4 v[124:127], v0, s[4:7], s97 offen offset:2048 sc1\n"
"buffer_load_dwordx4 v[144:147], v0, s[4:7], s97 offen offset:3072 sc1\n"
"buffer_load_dwordx4 v[156:159], v1, s[4:7], s97 offen sc1\n"
"buffer_load_dwordx4 v[172:175], v1, s[4:7], s97 offen offset:1024 sc1\n"
"buffer_load_dwordx4 v[116:119], v1, s[4:7], s97 offen offset:2048 sc1\n"
"buffer_load_dwordx4 v[136:139], v1, s[4:7], s97 offen offset:3072 sc1\n"
"s_add_u32 s97, s59, 0\n"
"buffer_load_dwordx4 v[176:179], v0, s[4:7], s97 offen sc1\n"
"buffer_load_dwordx4 v[180:183], v0, s[4:7], s97 offen offset:1024 sc1\n"
"buffer_load_dwordx4 v[168:171], v0, s[4:7], s97 offen offset:2048 sc1\n"
"s_waitcnt vmcnt(0)\n"
"v_max_u32_e32 v255, v148, v149\n"
"v_max3_u32 v255, v255, v150, v151\n"
"v_max3_u32 v255, v255, v108, v109\n"
"v_max3_u32 v255, v255, v110, v111\n"
"v_max3_u32 v255, v255, v112, v113\n"
"v_max3_u32 v255, v255, v114, v115\n"
"v_max3_u32 v255, v255, v196, v197\n"
"v_max3_u32 v255, v255, v198, v199\n"
"v_max3_u32 v255, v255, v200, v201\n"
"v_max3_u32 v255, v255, v202, v203\n"
"v_max3_u32 v255, v255, v128, v129\n"
"v_max3_u32 v255, v255, v130, v131\n"
"v_max3_u32 v255, v255, v120, v121\n"
"v_max3_u32 v255, v255, v122, v123\n"
"v_max3_u32 v255, v255, v132, v133\n"
"v_max3_u32 v255, v255, v134, v135\n"
"v_cmp_eq_u32_e32 vcc, -1, v255\n"
"s_cbranch_vccz .Lmk_p36k\n"
"s_sub_u32 s1, s1, 1\n"
"s_cbranch_scc0 .Lmk_p36l\n"
".Lmk_p36k:\n"
"v_mfma_f32_16x16x32_bf16 v[88:91], a[128:131], v[148:151], 0\n"
"v_mfma_f32_16x16x32_bf16 v[88:91], a[132:135], v[108:111], v[88:91]\n"
"v_mfma_f32_16x16x32_bf16 v[88:91], a[136:139], v[112:115], v[88:91]\n"
"v_mfma_f32_16x16x32_bf16 v[88:91], a[140:143], v[196:199], v[88:91]\n"
"v_mfma_f32_16x16x32_bf16 v[88:91], a[144:147], v[200:203], v[88:91]\n"
"v_mfma_f32_16x16x32_bf16 v[88:91], a[148:151], v[128:131], v[88:91]\n"
"v_mfma_f32_16x16x32_bf16 v[88:91], a[152:155], v[120:123], v[88:91]\n"
"v_mfma_f32_16x16x32_bf16 v[88:91], a[156:159], v[132:135], v[88:91]\n"
"v_mfma_f32_16x16x32_bf16 v[92:95], a[188:191], v[132:135], 0\n"
"v_mfma_f32_16x16x32_bf16 v[92:95], a[184:187], v[120:123], v[92:95]\n"
"v_mfma_f32_16x16x32_bf16 v[92:95], a[180:183], v[128:131], v[92:95]\n"
"v_mfma_f32_16x16x32_bf16 v[92:95], a[176:179], v[200:203], v[92:95]\n"
"v_mfma_f32_16x16x32_bf16 v[92:95], a[172:175], v[196:199], v[92:95]\n"
"v_mfma_f32_16x16x32_bf16 v[92:95], a[168:171], v[112:115], v[92:95]\n"
"s_nop 7\n"
"s_branch .Lmk_p36r\n"
".Lmk_p37s:\n"
";\n"
"s_waitcnt vmcnt(16)\n"
"s_mov_b32 s1, 0x10000\n"
".Lmk_p37l:\n"
"s_add_u32 s97, s58, 0\n"
"buffer_load_dwordx4 v[152:155], v0, s[4:7], s97 offen sc1\n"
"buffer_load_dwordx4 v[160:163], v0, s[4:7], s97 offen offset:1024 sc1\n"
"buffer_load_dwordx4 v[124:127], v0, s[4:7], s97 offen offset:2048 sc1\n"
"buffer_load_dwordx4 v[144:147], v0, s[4:7], s97 offen offset:3072 sc1\n"
"buffer_load_dwordx4 v[156:159], v1, s[4:7], s97 offen sc1\n"
"buffer_load_dwordx4 v[172:175], v1, s[4:7], s97 offen offset:1024 sc1\n"
"buffer_load_dwordx4 v[116:119], v1, s[4:7], s97 offen offset:2048 sc1\n"
"buffer_load_dwordx4 v[136:139], v1, s[4:7], s97 offen offset:3072 sc1\n"
"s_add_u32 s97, s59, 0\n"
"buffer_load_dwordx4 v[176:179], v0, s[4:7], s97 offen sc1\n"
"buffer_load_dwordx4 v[180:183], v0, s[4:7], s97 offen offset:1024 sc1\n"
"buffer_load_dwordx4 v[168:171], v0, s[4:7], s97 offen offset:2048 sc1\n"
"buffer_load_dwordx4 v[184:187], v0, s[4:7], s97 offen offset:3072 sc1\n"
"buffer_load_dwordx4 v[188:191], v1, s[4:7], s97 offen sc1\n"
"buffer_load_dwordx4 v[164:167], v1, s[4:7], s97 offen offset:1024 sc1\n"
"buffer_load_dwordx4 v[192:195], v1, s[4:7], s97 offen offset:2048 sc1\n"
"buffer_load_dwordx4 v[196:199], v1, s[4:7], s97 offen offset:3072 sc1\n"
"s_add_u32 s97, s52, 0x440000\n"
"buffer_load_dwordx4 v[88:91], v0, s[4:7], s97 offen sc1\n"
"buffer_load_dwordx4 v[92:95], v0, s[4:7], s97 offen offset:1024 sc1\n"
"s_waitcnt vmcnt(0)\n"
"v_max_u32_e32 v239, v152, v153\n"
"v_max3_u32 v239, v239, v154, v155\n"
"v_max3_u32 v239, v239, v160, v161\n"
"v_max3_u32 v239, v239, v162, v163\n"
"v_max3_u32 v239, v239, v124, v125\n"
"v_max3_u32 v239, v239, v126, v127\n"
"v_max3_u32 v239, v239, v144, v145\n"
"v_max3_u32 v239, v239, v146, v147\n"
"v_max3_u32 v239, v239, v156, v157\n"
"v_max3_u32 v239, v239, v158, v159\n"
"v_max3_u32 v239, v239, v172, v173\n"
"v_max3_u32 v239, v239, v174, v175\n"
"v_max3_u32 v239, v239, v116, v117\n"
"v_max3_u32 v239, v239, v118, v119\n"
"v_max3_u32 v239, v239, v136, v137\n"
"v_max3_u32 v239, v239, v138, v139\n"
"v_cmp_eq_u32_e32 vcc, -1, v239\n"
"s_cbranch_vccz .Lmk_p37k\n"
"s_sub_u32 s1, s1, 1\n"
"s_cbranch_scc0 .Lmk_p37l\n"
".Lmk_p37k:\n"
"v_mfma_f32_16x16x32_bf16 v[96:99], a[128:131], v[152:155], 0\n"
"v_mfma_f32_16x16x32_bf16 v[96:99], a[132:135], v[160:163], v[96:99]\n"
"v_mfma_f32_16x16x32_bf16 v[96:99], a[136:139], v[124:127], v[96:99]\n"
"v_mfma_f32_16x16x32_bf16 v[96:99], a[140:143], v[144:147], v[96:99]\n"
"v_mfma_f32_16x16x32_bf16 v[96:99], a[144:147], v[156:159], v[96:99]\n"
"v_mfma_f32_16x16x32_bf16 v[96:99], a[148:151], v[172:175], v[96:99]\n"
"v_mfma_f32_16x16x32_bf16 v[96:99], a[152:155], v[116:119], v[96:99]\n"
"v_mfma_f32_16x16x32_bf16 v[96:99], a[156:159], v[136:139], v[96:99]\n"
"v_mfma_f32_16x16x32_bf16 v[104:107], a[188:191], v[136:139], 0\n"
"v_mfma_f32_16x16x32_bf16 v[104:107], a[184:187], v[116:119], v[104:107]\n"
"v_mfma_f32_16x16x32_bf16 v[104:107], a[180:183], v[172:175], v[104:107]\n"
"v_mfma_f32_16x16x32_bf16 v[104:107], a[176:179], v[156:159], v[104:107]\n"
"v_mfma_f32_16x16x32_bf16 v[104:107], a[172:175], v[144:147], v[104:107]\n"
"v_mfma_f32_16x16x32_bf16 v[104:107], a[168:171], v[124:127], v[104:107]\n"
"s_nop 7\n"
"s_branch .Lmk_p37r\n"
".Lmk_p38s:\n"
";\n"
"s_waitcnt vmcnt(16)\n"
"s_mov_b32 s1, 0x10000\n"
".Lmk_p38l:\n"
"s_add_u32 s97, s59, 0\n"
"buffer_load_dwordx4 v[176:179], v0, s[4:7], s97 offen sc1\n"
"buffer_load_dwordx4 v[180:183], v0, s[4:7], s97 offen offset:1024 sc1\n"
"buffer_load_dwordx4 v[168:171], v0, s[4:7], s97 offen offset:2048 sc1\n"
"buffer_load_dwordx4 v[184:187], v0, s[4:7], s97 offen offset:3072 sc1\n"
"buffer_load_dwordx4 v[188:191], v1, s[4:7], s97 offen sc1\n"
"buffer_load_dwordx4 v[164:167], v1, s[4:7], s97 offen offset:1024 sc1\n"
"buffer_load_dwordx4 v[192:195], v1, s[4:7], s97 offen offset:2048 sc1\n"
"buffer_load_dwordx4 v[196:199], v1, s[4:7], s97 offen offset:3072 sc1\n"
"s_add_u32 s97, s52, 0x440000\n"
"buffer_load_dwordx4 v[88:91], v0, s[4:7], s97 offen sc1\n"
"buffer_load_dwordx4 v[92:95], v0, s[4:7], s97 offen offset:1024 sc1\n"
"buffer_load_dwordx4 v[96:99], v0, s[4:7], s97 offen offset:2048 sc1\n"
"buffer_load_dwordx4 v[100:103], v0, s[4:7], s97 offen offset:3072 sc1\n"
"buffer_load_dwordx4 v[104:107], v1, s[4:7], s97 offen sc1\n"
"buffer_load_dwordx4 v[112:115], v1, s[4:7], s97 offen offset:1024 sc1\n"
"buffer_load_dwordx4 v[116:119], v1, s[4:7], s97 offen offset:2048 sc1\n"
"buffer_load_dwordx4 v[124:127], v1, s[4:7], s97 offen offset:3072 sc1\n"
"s_add_u32 s97, s53, 0x440000\n"
"buffer_load_dwordx4 v[128:131], v0, s[4:7], s97 offen sc1\n"
"buffer_load_dwordx4 v[132:135], v0, s[4:7], s97 offen offset:1024 sc1\n"
"buffer_load_dwordx4 v[136:139], v0, s[4:7], s97 offen offset:2048 sc1\n"
"s_waitcnt vmcnt(0)\n"
"v_max_u32_e32 v255, v176, v177\n"
"v_max3_u32 v255, v255, v178, v179\n"
"v_max3_u32 v255, v255, v180, v181\n"
"v_max3_u32 v255, v255, v182, v183\n"
"v_max3_u32 v255, v255, v168, v169\n"
"v_max3_u32 v255, v255, v170, v171\n"
"v_max3_u32 v255, v255, v184, v185\n"
"v_max3_u32 v255, v255, v186, v187\n"
"v_max3_u32 v255, v255, v188, v189\n"
"v_max3_u32 v255, v255, v190, v191\n"
"v_max3_u32 v255, v255, v164, v165\n"
"v_max3_u32 v255, v255, v166, v167\n"
"v_max3_u32 v255, v255, v192, v193\n"
"v_max3_u32 v255, v255, v194, v195\n"
"v_max3_u32 v255, v255, v196, v197\n"
"v_max3_u32 v255, v255, v198, v199\n"
"v_cmp_eq_u32_e32 vcc, -1, v255\n"
"s_cbranch_vccz .Lmk_p38k\n"
"s_sub_u32 s1, s1, 1\n"
"s_cbranch_scc0 .Lmk_p38l\n"
".Lmk_p38k:\n"
"v_mfma_f32_16x16x32_bf16 v[172:175], a[128:131], v[176:179], v[140:143]\n"
"v_mfma_f32_16x16x32_bf16 v[172:175], a[132:135], v[180:183], v[172:175]\n"
"v_mfma_f32_16x16x32_bf16 v[172:175], a[136:139], v[168:171], v[172:175]\n"
"v_mfma_f32_16x16x32_bf16 v[172:175], a[140:143], v[184:187], v[172:175]\n"
"v_mfma_f32_16x16x32_bf16 v[172:175], a[144:147], v[188:191], v[172:175]\n"
"v_mfma_f32_16x16x32_bf16 v[172:175], a[148:151], v[164:167], v[172:175]\n"
"v_mfma_f32_16x16x32_bf16 v[172:175], a[152:155], v[192:195], v[172:175]\n"
"v_mfma_f32_16x16x32_bf16 v[172:175], a[156:159], v[196:199], v[172:175]\n"
"v_mfma_f32_16x16x32_bf16 v[204:207], a[188:191], v[196:199], v[148:151]\n"
"v_mfma_f32_16x16x32_bf16 v[204:207], a[184:187], v[192:195], v[204:207]\n"
"v_mfma_f32_16x16x32_bf16 v[204:207], a[180:183], v[164:167], v[204:207]\n"
"v_mfma_f32_16x16x32_bf16 v[204:207], a[176:179], v[188:191], v[204:207]\n"
"v_mfma_f32_16x16x32_bf16 v[204:207], a[172:175], v[184:187], v[204:207]\n"
"v_mfma_f32_16x16x32_bf16 v[204:207], a[168:171], v[168:171], v[204:207]\n"
"s_nop 7\n"
"s_branch .Lmk_p38r\n"
".Lmk_p43s:\n"
";\n"
"s_waitcnt vmcnt(16)\n"
"s_mov_b32 s1, 0x10000\n"
".Lmk_p43l:\n"
"s_add_u32 s96, s52, 0x440000\n"
"buffer_load_dwordx4 v[88:91], v0, s[4:7], s96 offen sc1\n"
"buffer_load_dwordx4 v[92:95], v0, s[4:7], s96 offen offset:1024 sc1\n"
"buffer_load_dwordx4 v[96:99], v0, s[4:7], s96 offen offset:2048 sc1\n"
"buffer_load_dwordx4 v[100:103], v0, s[4:7], s96 offen offset:3072 sc1\n"
"buffer_load_dwordx4 v[104:107], v1, s[4:7], s96 offen sc1\n"
"buffer_load_dwordx4 v[112:115], v1, s[4:7], s96 offen offset:1024 sc1\n"
"buffer_load_dwordx4 v[116:119], v1, s[4:7], s96 offen offset:2048 sc1\n"
"buffer_load_dwordx4 v[124:127], v1, s[4:7], s96 offen offset:3072 sc1\n"
"s_add_u32 s96, s53, 0x440000\n"
"buffer_load_dwordx4 v[128:131], v0, s[4:7], s96 offen sc1\n"
"buffer_load_dwordx4 v[132:135], v0, s[4:7], s96 offen offset:1024 sc1\n"
"buffer_load_dwordx4 v[136:139], v0, s[4:7], s96 offen offset:2048 sc1\n"
"buffer_load_dwordx4 v[140:143], v0, s[4:7], s96 offen offset:3072 sc1\n"
"buffer_load_dwordx4 v[144:147], v1, s[4:7], s96 offen sc1\n"
"buffer_load_dwordx4 v[156:159], v1, s[4:7], s96 offen offset:1024 sc1\n"
"buffer_load_dwordx4 v[160:163], v1, s[4:7], s96 offen offset:2048 sc1\n"
"buffer_load_dwordx4 v[164:167], v1, s[4:7], s96 offen offset:3072 sc1\n"
"s_add_u32 s96, s54, 0x440000\n"
"buffer_load_dwordx4 v[176:179], v0, s[4:7], s96 offen sc1\n"
"buffer_load_dwordx4 v[108:111], v0, s[4:7], s96 offen offset:1024 sc1\n"
"s_waitcnt vmcnt(0)\n"
"v_max_u32_e32 v252, v88, v89\n"
"v_max3_u32 v252, v252, v90, v91\n"
"v_max3_u32 v252, v252, v92, v93\n"
"v_max3_u32 v252, v252, v94, v95\n"
"v_max3_u32 v252, v252, v96, v97\n"
"v_max3_u32 v252, v252, v98, v99\n"
"v_max3_u32 v252, v252, v100, v101\n"
"v_max3_u32 v252, v252, v102, v103\n"
"v_max3_u32 v252, v252, v104, v105\n"
"v_max3_u32 v252, v252, v106, v107\n"
"v_max3_u32 v252, v252, v112, v113\n"
"v_max3_u32 v252, v252, v114, v115\n"
"v_max3_u32 v252, v252, v116, v117\n"
"v_max3_u32 v252, v252, v118, v119\n"
"v_max3_u32 v252, v252, v124, v125\n"
"v_max3_u32 v252, v252, v126, v127\n"
"v_cmp_eq_u32_e32 vcc, -1, v252\n"
"s_cbranch_vccz .Lmk_p43k\n"
"s_sub_u32 s1, s1, 1\n"
"s_cbranch_scc0 .Lmk_p43l\n"
".Lmk_p43k:\n"
"v_mfma_f32_16x16x32_bf16 v[172:175], a[0:3], v[88:91], 0\n"
"v_mfma_f32_16x16x32_bf16 v[172:175], a[4:7], v[92:95], v[172:175]\n"
"v_mfma_f32_16x16x32_bf16 v[172:175], a[8:11], v[96:99], v[172:175]\n"
"v_mfma_f32_16x16x32_bf16 v[172:175], a[12:15], v[100:103], v[172:175]\n"
"v_mfma_f32_16x16x32_bf16 v[172:175], a[16:19], v[104:107], v[172:175]\n"
"v_mfma_f32_16x16x32_bf16 v[172:175], a[20:23], v[112:115], v[172:175]\n"
"v_mfma_f32_16x16x32_bf16 v[172:175], a[24:27], v[116:119], v[172:175]\n"
"v_mfma_f32_16x16x32_bf16 v[172:175], a[28:31], v[124:127], v[172:175]\n"
"v_mfma_f32_16x16x32_bf16 v[180:183], a[60:63], v[124:127], 0\n"
"v_mfma_f32_16x16x32_bf16 v[180:183], a[56:59], v[116:119], v[180:183]\n"
"v_mfma_f32_16x16x32_bf16 v[180:183], a[52:55], v[112:115], v[180:183]\n"
"v_mfma_f32_16x16x32_bf16 v[180:183], a[48:51], v[104:107], v[180:183]\n"
"v_mfma_f32_16x16x32_bf16 v[180:183], a[44:47], v[100:103], v[180:183]\n"
"v_mfma_f32_16x16x32_bf16 v[180:183], a[40:43], v[96:99], v[180:183]\n"
"s_nop 7\n"
"s_branch .Lmk_p43r\n"
".Lmk_p44s:\n"
";\n"
"s_waitcnt vmcnt(16)\n"
"s_mov_b32 s1, 0x10000\n"
".Lmk_p44l:\n"
"s_add_u32 s97, s53, 0x440000\n"
"buffer_load_dwordx4 v[128:131], v0, s[4:7], s97 offen sc1\n"
"buffer_load_dwordx4 v[132:135], v0, s[4:7], s97 offen offset:1024 sc1\n"
"buffer_load_dwordx4 v[136:139], v0, s[4:7], s97 offen offset:2048 sc1\n"
"buffer_load_dwordx4 v[140:143], v0, s[4:7], s97 offen offset:3072 sc1\n"
"buffer_load_dwordx4 v[144:147], v1, s[4:7], s97 offen sc1\n"
"buffer_load_dwordx4 v[156:159], v1, s[4:7], s97 offen offset:1024 sc1\n"
"buffer_load_dwordx4 v[160:163], v1, s[4:7], s97 offen offset:2048 sc1\n"
"buffer_load_dwordx4 v[164:167], v1, s[4:7], s97 offen offset:3072 sc1\n"
"s_add_u32 s97, s54, 0x440000\n"
"buffer_load_dwordx4 v[176:179], v0, s[4:7], s97 offen sc1\n"
"buffer_load_dwordx4 v[108:111], v0, s[4:7], s97 offen offset:1024 sc1\n"
"buffer_load_dwordx4 v[148:151], v0, s[4:7], s97 offen offset:2048 sc1\n"
"buffer_load_dwordx4 v[184:187], v0, s[4:7], s97 offen offset:3072 sc1\n"
"buffer_load_dwordx4 v[152:155], v1, s[4:7], s97 offen sc1\n"
"buffer_load_dwordx4 v[192:195], v1, s[4:7], s97 offen offset:1024 sc1\n"
"buffer_load_dwordx4 v[196:199], v1, s[4:7], s97 offen offset:2048 sc1\n"
"buffer_load_dwordx4 v[200:203], v1, s[4:7], s97 offen offset:3072 sc1\n"
"s_add_u32 s97, s55, 0x440000\n"
"buffer_load_dwordx4 v[92:95], v0, s[4:7], s97 offen sc1\n"
"buffer_load_dwordx4 v[96:99], v0, s[4:7], s97 offen offset:1024 sc1\n"
"s_waitcnt vmcnt(0)\n"
"v_max_u32_e32 v247, v128, v129\n"
"v_max3_u32 v247, v247, v130, v131\n"
"v_max3_u32 v247, v247, v132, v133\n"
"v_max3_u32 v247, v247, v134, v135\n"
"v_max3_u32 v247, v247, v136, v137\n"
"v_max3_u32 v247, v247, v138, v139\n"
"v_max3_u32 v247, v247, v140, v141\n"
"v_max3_u32 v247, v247, v142, v143\n"
"v_max3_u32 v247, v247, v144, v145\n"
"v_max3_u32 v247, v247, v146, v147\n"
"v_max3_u32 v247, v247, v156, v157\n"
"v_max3_u32 v247, v247, v158, v159\n"
"v_max3_u32 v247, v247, v160, v161\n"
"v_max3_u32 v247, v247, v162, v163\n"
"v_max3_u32 v247, v247, v164, v165\n"
"v_max3_u32 v247, v247, v166, v167\n"
"v_cmp_eq_u32_e32 vcc, -1, v247\n"
"s_cbranch_vccz .Lmk_p44k\n"
"s_sub_u32 s1, s1, 1\n"
"s_cbranch_scc0 .Lmk_p44l\n"
".Lmk_p44k:\n"
"v_mfma_f32_16x16x32_bf16 v[88:91], a[0:3], v[128:131], 0\n"
"v_mfma_f32_16x16x32_bf16 v[88:91], a[4:7], v[132:135], v[88:91]\n"
"v_mfma_f32_16x16x32_bf16 v[88:91], a[8:11], v[136:139], v[88:91]\n"
"v_mfma_f32_16x16x32_bf16 v[88:91], a[12:15], v[140:143], v[88:91]\n"
"v_mfma_f32_16x16x32_bf16 v[88:91], a[16:19], v[144:147], v[88:91]\n"
"v_mfma_f32_16x16x32_bf16 v[88:91], a[20:23], v[156:159], v[88:91]\n"
"v_mfma_f32_16x16x32_bf16 v[88:91], a[24:27], v[160:163], v[88:91]\n"
"v_mfma_f32_16x16x32_bf16 v[88:91], a[28:31], v[164:167], v[88:91]\n"
"v_mfma_f32_16x16x32_bf16 v[100:103], a[60:63], v[164:167], 0\n"
"v_mfma_f32_16x16x32_bf16 v[100:103], a[56:59], v[160:163], v[100:103]\n"
"v_mfma_f32_16x16x32_bf16 v[100:103], a[52:55], v[156:159], v[100:103]\n"
"v_mfma_f32_16x16x32_bf16 v[100:103], a[48:51], v[144:147], v[100:103]\n"
"v_mfma_f32_16x16x32_bf16 v[100:103], a[44:47], v[140:143], v[100:103]\n"
"v_mfma_f32_16x16x32_bf16 v[100:103], a[40:43], v[136:139], v[100:103]\n"
"s_nop 7\n"
"s_branch .Lmk_p44r\n"
".Lmk_m47s:\n"
";\n"
"s_waitcnt vmcnt(16)\n"
"s_mov_b32 s1, 0x10000\n"
".Lmk_m47l:\n"
"s_add_u32 s97, s62, 0x44000\n"
"buffer_load_dword v255, v10, s[4:7], s97 offen sc1\n"
"buffer_load_dword v254, v10, s[4:7], s97 offen offset:256 sc1\n"
"buffer_load_dword v253, v10, s[4:7], s97 offen offset:512 sc1\n"
"buffer_load_dword v252, v10, s[4:7], s97 offen offset:768 sc1\n"
"buffer_load_dword v251, v10, s[4:7], s97 offen offset:1024 sc1\n"
"buffer_load_dword v250, v10, s[4:7], s97 offen offset:1280 sc1\n"
"buffer_load_dword v249, v10, s[4:7], s97 offen offset:1536 sc1\n"
"buffer_load_dword v248, v10, s[4:7], s97 offen offset:1792 sc1\n"
"s_waitcnt vmcnt(0)\n"
"v_max_u32_e32 v247, v255, v254\n"
"v_max3_u32 v247, v247, v253, v252\n"
"v_max3_u32 v247, v247, v251, v250\n"
"v_max3_u32 v247, v247, v249, v248\n"
"v_cmp_eq_u32_e32 vcc, -1, v247\n"
"s_cbranch_vccz .Lmk_m47k\n"
"s_sub_u32 s1, s1, 1\n"
"s_cbranch_scc0 .Lmk_m47l\n"
".Lmk_m47k:\n"
"v_add_f32_e32 v255, v255, v251\n"
"v_add_f32_e32 v254, v254, v250\n"
"v_add_f32_e32 v253, v253, v249\n"
"v_add_f32_e32 v252, v252, v248\n"
"v_add_f32_e32 v255, v255, v253\n"
"v_add_f32_e32 v254, v254, v252\n"
"v_add_f32_e32 v255, v255, v254\n"
"s_nop 1\n"
"s_branch .Lmk_m47r\n"
".Lmk_p45s:\n"
";\n"
"s_waitcnt vmcnt(16)\n"
"s_mov_b32 s1, 0x10000\n"
".Lmk_p45l:\n"
"s_add_u32 s96, s54, 0x440000\n"
"buffer_load_dwordx4 v[176:179], v0, s[4:7], s96 offen sc1\n"
"buffer_load_dwordx4 v[108:111], v0, s[4:7], s96 offen offset:1024 sc1\n"
"buffer_load_dwordx4 v[148:151], v0, s[4:7], s96 offen offset:2048 sc1\n"
"buffer_load_dwordx4 v[184:187], v0, s[4:7], s96 offen offset:3072 sc1\n"
"buffer_load_dwordx4 v[152:155], v1, s[4:7], s96 offen sc1\n"
"buffer_load_dwordx4 v[192:195], v1, s[4:7], s96 offen offset:1024 sc1\n"
"buffer_load_dwordx4 v[196:199], v1, s[4:7], s96 offen offset:2048 sc1\n"
"buffer_load_dwordx4 v[200:203], v1, s[4:7], s96 offen offset:3072 sc1\n"
"s_add_u32 s96, s55, 0x440000\n"
"buffer_load_dwordx4 v[92:95], v0, s[4:7], s96 offen sc1\n"
"buffer_load_dwordx4 v[96:99], v0, s[4:7], s96 offen offset:1024 sc1\n"
"buffer_load_dwordx4 v[104:107], v0, s[4:7], s96 offen offset:2048 sc1\n"
"buffer_load_dwordx4 v[112:115], v0, s[4:7], s96 offen offset:3072 sc1\n"
"buffer_load_dwordx4 v[100:103], v1, s[4:7], s96 offen sc1\n"
"buffer_load_dwordx4 v[124:127], v1, s[4:7], s96 offen offset:1024 sc1\n"
"buffer_load_dwordx4 v[168:171], v1, s[4:7], s96 offen offset:2048 sc1\n"
"buffer_load_dwordx4 v[172:175], v1, s[4:7], s96 offen offset:3072 sc1\n"
"s_add_u32 s96, s52, 0x448000\n"
"buffer_load_dwordx4 v[128:131], v0, s[4:7], s96 offen sc1\n"
"s_waitcnt vmcnt(0)\n"
"v_max_u32_e32 v255, v176, v177\n"
"v_max3_u32 v255, v255, v178, v179\n"
"v_max3_u32 v255, v255, v108, v109\n"
"v_max3_u32 v255, v255, v110, v111\n"
"v_max3_u32 v255, v255, v148, v149\n"
"v_max3_u32 v255, v255, v150, v151\n"
"v_max3_u32 v255, v255, v184, v185\n"
"v_max3_u32 v255, v255, v186, v187\n"
"v_max3_u32 v255, v255, v152, v153\n"
"v_max3_u32 v255, v255, v154, v155\n"
"v_max3_u32 v255, v255, v192, v193\n"
"v_max3_u32 v255, v255, v194, v195\n"
"v_max3_u32 v255, v255, v196, v197\n"
"v_max3_u32 v255, v255, v198, v199\n"
"v_max3_u32 v255, v255, v200, v201\n"
"v_max3_u32 v255, v255, v202, v203\n"
"v_cmp_eq_u32_e32 vcc, -1, v255\n"
"s_cbranch_vccz .Lmk_p45k\n"
"s_sub_u32 s1, s1, 1\n"
"s_cbranch_scc0 .Lmk_p45l\n"
".Lmk_p45k:\n"
"v_mfma_f32_16x16x32_bf16 v[88:91], a[0:3], v[176:179], 0\n"
"v_mfma_f32_16x16x32_bf16 v[88:91], a[4:7], v[108:111], v[88:91]\n"
"v_mfma_f32_16x16x32_bf16 v[88:91], a[8:11], v[148:151], v[88:91]\n"
"v_mfma_f32_16x16x32_bf16 v[88:91], a[12:15], v[184:187], v[88:91]\n"
"v_mfma_f32_16x16x32_bf16 v[88:91], a[16:19], v[152:155], v[88:91]\n"
"v_mfma_f32_16x16x32_bf16 v[88:91], a[20:23], v[192:195], v[88:91]\n"
"v_mfma_f32_16x16x32_bf16 v[88:91], a[24:27], v[196:199], v[88:91]\n"
"v_mfma_f32_16x16x32_bf16 v[88:91], a[28:31], v[200:203], v[88:91]\n"
"v_mfma_f32_16x16x32_bf16 v[116:119], a[60:63], v[200:203], 0\n"
"v_mfma_f32_16x16x32_bf16 v[116:119], a[56:59], v[196:199], v[116:119]\n"
"v_mfma_f32_16x16x32_bf16 v[116:119], a[52:55], v[192:195], v[116:119]\n"
"v_mfma_f32_16x16x32_bf16 v[116:119], a[48:51], v[152:155], v[116:119]\n"
"v_mfma_f32_16x16x32_bf16 v[116:119], a[44:47], v[184:187], v[116:119]\n"
"v_mfma_f32_16x16x32_bf16 v[116:119], a[40:43], v[148:151], v[116:119]\n"
"s_nop 7\n"
"s_branch .Lmk_p45r\n"
".Lmk_p46s:\n"
";\n"
"s_waitcnt vmcnt(16)\n"
"s_mov_b32 s1, 0x10000\n"
".Lmk_p46l:\n"
"s_add_u32 s96, s55, 0x440000\n"
"buffer_load_dwordx4 v[92:95], v0, s[4:7], s96 offen sc1\n"
"buffer_load_dwordx4 v[96:99], v0, s[4:7], s96 offen offset:1024 sc1\n"
"buffer_load_dwordx4 v[104:107], v0, s[4:7], s96 offen offset:2048 sc1\n"
"buffer_load_dwordx4 v[112:115], v0, s[4:7], s96 offen offset:3072 sc1\n"
"buffer_load_dwordx4 v[100:103], v1, s[4:7], s96 offen sc1\n"
"buffer_load_dwordx4 v[124:127], v1, s[4:7], s96 offen offset:1024 sc1\n"
"buffer_load_dwordx4 v[168:171], v1, s[4:7], s96 offen offset:2048 sc1\n"
"buffer_load_dwordx4 v[172:175], v1, s[4:7], s96 offen offset:3072 sc1\n"
"s_add_u32 s96, s52, 0x448000\n"
"buffer_load_dwordx4 v[128:131], v0, s[4:7], s96 offen sc1\n"
"buffer_load_dwordx4 v[88:91], v0, s[4:7], s96 offen offset:1024 sc1\n"
"buffer_load_dwordx4 v[164:167], v0, s[4:7], s96 offen offset:2048 sc1\n"
"buffer_load_dwordx4 v[140:143], v0, s[4:7], s96 offen offset:3072 sc1\n"
"buffer_load_dwordx4 v[132:135], v1, s[4:7], s96 offen sc1\n"
"buffer_load_dwordx4 v[224:227], v1, s[4:7], s96 offen offset:1024 sc1\n"
"buffer_load_dwordx4 v[180:183], v1, s[4:7], s96 offen offset:2048 sc1\n"
"buffer_load_dwordx4 v[148:151], v1, s[4:7], s96 offen offset:3072 sc1\n"
"s_add_u32 s96, s53, 0x448000\n"
"buffer_load_dwordx4 v[116:119], v0, s[4:7], s96 offen sc1\n"
"buffer_load_dwordx4 v[160:163], v0, s[4:7], s96 offen offset:1024 sc1\n"
"s_waitcnt vmcnt(0)\n"
"v_max_u32_e32 v252, v92, v93\n"
"v_max3_u32 v252, v252, v94, v95\n"
"v_max3_u32 v252, v252, v96, v97\n"
"v_max3_u32 v252, v252, v98, v99\n"
"v_max3_u32 v252, v252, v104, v105\n"
"v_max3_u32 v252, v252, v106, v107\n"
"v_max3_u32 v252, v252, v112, v113\n"
"v_max3_u32 v252, v252, v114, v115\n"
"v_max3_u32 v252, v252, v100, v101\n"
"v_max3_u32 v252, v252, v102, v103\n"
"v_max3_u32 v252, v252, v124, v125\n"
"v_max3_u32 v252, v252, v126, v127\n"
"v_max3_u32 v252, v252, v168, v169\n"
"v_max3_u32 v252, v252, v170, v171\n"
"v_max3_u32 v252, v252, v172, v173\n"
"v_max3_u32 v252, v252, v174, v175\n"
"v_cmp_eq_u32_e32 vcc, -1, v252\n"
"s_cbranch_vccz .Lmk_p46k\n"
"s_sub_u32 s1, s1, 1\n"
"s_cbranch_scc0 .Lmk_p46l\n"
".Lmk_p46k:\n"
"v_mfma_f32_16x16x32_bf16 v[196:199], a[0:3], v[92:95], v[136:139]\n"
"v_mfma_f32_16x16x32_bf16 v[196:199], a[4:7], v[96:99], v[196:199]\n"
"v_mfma_f32_16x16x32_bf16 v[196:199], a[8:11], v[104:107], v[196:199]\n"
"v_mfma_f32_16x16x32_bf16 v[196:199], a[12:15], v[112:115], v[196:199]\n"
"v_mfma_f32_16x16x32_bf16 v[196:199], a[16:19], v[100:103], v[196:199]\n"
"v_mfma_f32_16x16x32_bf16 v[196:199], a[20:23], v[124:127], v[196:199]\n"
"v_mfma_f32_16x16x32_bf16 v[196:199], a[24:27], v[168:171], v[196:199]\n"
"v_mfma_f32_16x16x32_bf16 v[196:199], a[28:31], v[172:175], v[196:199]\n"
"v_mfma_f32_16x16x32_bf16 v[204:207], a[60:63], v[172:175], v[156:159]\n"
"v_mfma_f32_16x16x32_bf16 v[204:207], a[56:59], v[168:171], v[204:207]\n"
"v_mfma_f32_16x16x32_bf16 v[204:207], a[52:55], v[124:127], v[204:207]\n"
"v_mfma_f32_16x16x32_bf16 v[204:207], a[48:51], v[100:103], v[204:207]\n"
"v_mfma_f32_16x16x32_bf16 v[204:207], a[44:47], v[112:115], v[204:207]\n"
"v_mfma_f32_16x16x32_bf16 v[204:207], a[40:43], v[104:107], v[204:207]\n"
"s_nop 7\n"
"s_branch .Lmk_p46r\n"
".Lmk_p48s:\n"
";\n"
"s_waitcnt vmcnt(16)\n"
"s_mov_b32 s1, 0x10000\n"
".Lmk_p48l:\n"
"s_add_u32 s98, s52, 0x448000\n"
"buffer_load_dwordx4 v[128:131], v0, s[4:7], s98 offen sc1\n"
"buffer_load_dwordx4 v[88:91], v0, s[4:7], s98 offen offset:1024 sc1\n"
"buffer_load_dwordx4 v[164:167], v0, s[4:7], s98 offen offset:2048 sc1\n"
"buffer_load_dwordx4 v[140:143], v0, s[4:7], s98 offen offset:3072 sc1\n"
"buffer_load_dwordx4 v[132:135], v1, s[4:7], s98 offen sc1\n"
"buffer_load_dwordx4 v[224:227], v1, s[4:7], s98 offen offset:1024 sc1\n"
"buffer_load_dwordx4 v[180:183], v1, s[4:7], s98 offen offset:2048 sc1\n"
"buffer_load_dwordx4 v[148:151], v1, s[4:7], s98 offen offset:3072 sc1\n"
"s_add_u32 s98, s53, 0x448000\n"
"buffer_load_dwordx4 v[116:119], v0, s[4:7], s98 offen sc1\n"
"buffer_load_dwordx4 v[160:163], v0, s[4:7], s98 offen offset:1024 sc1\n"
"buffer_load_dwordx4 v[136:139], v0, s[4:7], s98 offen offset:2048 sc1\n"
"buffer_load_dwordx4 v[156:159], v0, s[4:7], s98 offen offset:3072 sc1\n"
"buffer_load_dwordx4 v[192:195], v1, s[4:7], s98 offen sc1\n"
"buffer_load_dwordx4 v[184:187], v1, s[4:7], s98 offen offset:1024 sc1\n"
"buffer_load_dwordx4 v[176:179], v1, s[4:7], s98 offen offset:2048 sc1\n"
"buffer_load_dwordx4 v[200:203], v1, s[4:7], s98 offen offset:3072 sc1\n"
"s_add_u32 s98, s54, 0x448000\n"
"buffer_load_dwordx4 v[96:99], v0, s[4:7], s98 offen sc1\n"
"buffer_load_dwordx4 v[100:103], v0, s[4:7], s98 offen offset:1024 sc1\n"
"s_waitcnt vmcnt(0)\n"
"v_max_u32_e32 v252, v128, v129\n"
"v_max3_u32 v252, v252, v130, v131\n"
"v_max3_u32 v252, v252, v88, v89\n"
"v_max3_u32 v252, v252, v90, v91\n"
"v_max3_u32 v252, v252, v164, v165\n"
"v_max3_u32 v252, v252, v166, v167\n"
"v_max3_u32 v252, v252, v140, v141\n"
"v_max3_u32 v252, v252, v142, v143\n"
"v_max3_u32 v252, v252, v132, v133\n"
"v_max3_u32 v252, v252, v134, v135\n"
"v_max3_u32 v252, v252, v224, v225\n"
"v_max3_u32 v252, v252, v226, v227\n"
"v_max3_u32 v252, v252, v180, v181\n"
"v_max3_u32 v252, v252, v182, v183\n"
"v_max3_u32 v252, v252, v148, v149\n"
"v_max3_u32 v252, v252, v150, v151\n"
"v_cmp_eq_u32_e32 vcc, -1, v252\n"
"s_cbranch_vccz .Lmk_p48k\n"
"s_sub_u32 s1, s1, 1\n"
"s_cbranch_scc0 .Lmk_p48l\n"
".Lmk_p48k:\n"
"v_mfma_f32_16x16x32_bf16 v[92:95], a[0:3], v[128:131], 0\n"
"v_mfma_f32_16x16x32_bf16 v[92:95], a[4:7], v[88:91], v[92:95]\n"
"v_mfma_f32_16x16x32_bf16 v[92:95], a[8:11], v[164:167], v[92:95]\n"
"v_mfma_f32_16x16x32_bf16 v[92:95], a[12:15], v[140:143], v[92:95]\n"
"v_mfma_f32_16x16x32_bf16 v[92:95], a[16:19], v[132:135], v[92:95]\n"
"v_mfma_f32_16x16x32_bf16 v[92:95], a[20:23], v[224:227], v[92:95]\n"
"v_mfma_f32_16x16x32_bf16 v[92:95], a[24:27], v[180:183], v[92:95]\n"
"v_mfma_f32_16x16x32_bf16 v[92:95], a[28:31], v[148:151], v[92:95]\n"
"v_mfma_f32_16x16x32_bf16 v[104:107], a[60:63], v[148:151], 0\n"
"v_mfma_f32_16x16x32_bf16 v[104:107], a[56:59], v[180:183], v[104:107]\n"
"v_mfma_f32_16x16x32_bf16 v[104:107], a[52:55], v[224:227], v[104:107]\n"
"v_mfma_f32_16x16x32_bf16 v[104:107], a[48:51], v[132:135], v[104:107]\n"
"v_mfma_f32_16x16x32_bf16 v[104:107], a[44:47], v[140:143], v[104:107]\n"
"v_mfma_f32_16x16x32_bf16 v[104:107], a[40:43], v[164:167], v[104:107]\n"
"s_nop 7\n"
"s_branch .Lmk_p48r\n"
".Lmk_p49s:\n"
";\n"
"s_waitcnt vmcnt(16)\n"
"s_mov_b32 s1, 0x10000\n"
".Lmk_p49l:\n"
"s_add_u32 s95, s53, 0x448000\n"
"buffer_load_dwordx4 v[116:119], v0, s[4:7], s95 offen sc1\n"
"buffer_load_dwordx4 v[160:163], v0, s[4:7], s95 offen offset:1024 sc1\n"
"buffer_load_dwordx4 v[136:139], v0, s[4:7], s95 offen offset:2048 sc1\n"
"buffer_load_dwordx4 v[156:159], v0, s[4:7], s95 offen offset:3072 sc1\n"
"buffer_load_dwordx4 v[192:195], v1, s[4:7], s95 offen sc1\n"
"buffer_load_dwordx4 v[184:187], v1, s[4:7], s95 offen offset:1024 sc1\n"
"buffer_load_dwordx4 v[176:179], v1, s[4:7], s95 offen offset:2048 sc1\n"
"buffer_load_dwordx4 v[200:203], v1, s[4:7], s95 offen offset:3072 sc1\n"
"s_add_u32 s95, s54, 0x448000\n"
"buffer_load_dwordx4 v[96:99], v0, s[4:7], s95 offen sc1\n"
"buffer_load_dwordx4 v[100:103], v0, s[4:7], s95 offen offset:1024 sc1\n"
"buffer_load_dwordx4 v[112:115], v0, s[4:7], s95 offen offset:2048 sc1\n"
"buffer_load_dwordx4 v[120:123], v0, s[4:7], s95 offen offset:3072 sc1\n"
"buffer_load_dwordx4 v[104:107], v1, s[4:7], s95 offen sc1\n"
"buffer_load_dwordx4 v[152:155], v1, s[4:7], s95 offen offset:1024 sc1\n"
"buffer_load_dwordx4 v[108:111], v1, s[4:7], s95 offen offset:2048 sc1\n"
"buffer_load_dwordx4 v[144:147], v1, s[4:7], s95 offen offset:3072 sc1\n"
"s_add_u32 s95, s55, 0x448000\n"
"buffer_load_dwordx4 v[92:95], v0, s[4:7], s95 offen sc1\n"
"buffer_load_dwordx4 v[124:127], v0, s[4:7], s95 offen offset:1024 sc1\n"
"s_waitcnt vmcnt(0)\n"
"v_max_u32_e32 v247, v116, v117\n"
"v_max3_u32 v247, v247, v118, v119\n"
"v_max3_u32 v247, v247, v160, v161\n"
"v_max3_u32 v247, v247, v162, v163\n"
"v_max3_u32 v247, v247, v136, v137\n"
"v_max3_u32 v247, v247, v138, v139\n"
"v_max3_u32 v247, v247, v156, v157\n"
"v_max3_u32 v247, v247, v158, v159\n"
"v_max3_u32 v247, v247, v192, v193\n"
"v_max3_u32 v247, v247, v194, v195\n"
"v_max3_u32 v247, v247, v184, v185\n"
"v_max3_u32 v247, v247, v186, v187\n"
"v_max3_u32 v247, v247, v176, v177\n"
"v_max3_u32 v247, v247, v178, v179\n"
"v_max3_u32 v247, v247, v200, v201\n"
"v_max3_u32 v247, v247, v202, v203\n"
"v_cmp_eq_u32_e32 vcc, -1, v247\n"
"s_cbranch_vccz .Lmk_p49k\n"
"s_sub_u32 s1, s1, 1\n"
"s_cbranch_scc0 .Lmk_p49l\n"
".Lmk_p49k:\n"
"v_mfma_f32_16x16x32_bf16 v[88:91], a[0:3], v[116:119], 0\n"
"v_mfma_f32_16x16x32_bf16 v[88:91], a[4:7], v[160:163], v[88:91]\n"
"v_mfma_f32_16x16x32_bf16 v[88:91], a[8:11], v[136:139], v[88:91]\n"
"v_mfma_f32_16x16x32_bf16 v[88:91], a[12:15], v[156:159], v[88:91]\n"
"v_mfma_f32_16x16x32_bf16 v[88:91], a[16:19], v[192:195], v[88:91]\n"
"v_mfma_f32_16x16x32_bf16 v[88:91], a[20:23], v[184:187], v[88:91]\n"
"v_mfma_f32_16x16x32_bf16 v[88:91], a[24:27], v[176:179], v[88:91]\n"
"v_mfma_f32_16x16x32_bf16 v[88:91], a[28:31], v[200:203], v[88:91]\n"
"v_mfma_f32_16x16x32_bf16 v[128:131], a[60:63], v[200:203], 0\n"
"v_mfma_f32_16x16x32_bf16 v[128:131], a[56:59], v[176:179], v[128:131]\n"
"v_mfma_f32_16x16x32_bf16 v[128:131], a[52:55], v[184:187], v[128:131]\n"
"v_mfma_f32_16x16x32_bf16 v[128:131], a[48:51], v[192:195], v[128:131]\n"
"v_mfma_f32_16x16x32_bf16 v[128:131], a[44:47], v[156:159], v[128:131]\n"
"v_mfma_f32_16x16x32_bf16 v[128:131], a[40:43], v[136:139], v[128:131]\n"
"s_nop 7\n"
"s_branch .Lmk_p49r\n"
".Lmk_m52s:\n"
";\n"
"s_waitcnt vmcnt(16)\n"
"s_mov_b32 s1, 0x10000\n"
".Lmk_m52l:\n"
"s_add_u32 s97, s62, 0x44800\n"
"buffer_load_dword v255, v10, s[4:7], s97 offen sc1\n"
"buffer_load_dword v253, v10, s[4:7], s97 offen offset:256 sc1\n"
"buffer_load_dword v252, v10, s[4:7], s97 offen offset:512 sc1\n"
"buffer_load_dword v251, v10, s[4:7], s97 offen offset:768 sc1\n"
"buffer_load_dword v250, v10, s[4:7], s97 offen offset:1024 sc1\n"
"buffer_load_dword v254, v10, s[4:7], s97 offen offset:1280 sc1\n"
"buffer_load_dword v249, v10, s[4:7], s97 offen offset:1536 sc1\n"
"buffer_load_dword v248, v10, s[4:7], s97 offen offset:1792 sc1\n"
"s_waitcnt vmcnt(0)\n"
"v_max_u32_e32 v245, v255, v253\n"
"v_max3_u32 v245, v245, v252, v251\n"
"v_max3_u32 v245, v245, v250, v254\n"
"v_max3_u32 v245, v245, v249, v248\n"
"v_cmp_eq_u32_e32 vcc, -1, v245\n"
"s_cbranch_vccz .Lmk_m52k\n"
"s_sub_u32 s1, s1, 1\n"
"s_cbranch_scc0 .Lmk_m52l\n"
".Lmk_m52k:\n"
"v_add_f32_e32 v255, v255, v250\n"
"v_add_f32_e32 v253, v253, v254\n"
"v_add_f32_e32 v252, v252, v249\n"
"v_add_f32_e32 v251, v251, v248\n"
"v_add_f32_e32 v255, v255, v252\n"
"v_add_f32_e32 v253, v253, v251\n"
"v_add_f32_e32 v255, v255, v253\n"
"s_nop 1\n"
"s_branch .Lmk_m52r\n"
".Lmk_f39s:\n"
";\n"
"s_mov_b32 s1, 0x10000\n"
".Lmk_f39l:\n"
"buffer_load_dwordx2 v[246:247], v12, s[4:7], s96 offen sc1\n"
"s_waitcnt vmcnt(0)\n"
"v_cmp_ne_u64_e32 vcc, v[246:247], v[18:19]\n"
"s_cbranch_vccz .Lmk_f39r\n"
"s_sub_u32 s1, s1, 1\n"
"s_cbranch_scc0 .Lmk_f39l\n"
"s_branch .Lmk_f39r\n"
".Lmk_p50s:\n"
";\n"
"s_waitcnt vmcnt(16)\n"
"s_mov_b32 s1, 0x10000\n"
".Lmk_p50l:\n"
"s_add_u32 s96, s54, 0x448000\n"
"buffer_load_dwordx4 v[96:99], v0, s[4:7], s96 offen sc1\n"
"buffer_load_dwordx4 v[100:103], v0, s[4:7], s96 offen offset:1024 sc1\n"
"buffer_load_dwordx4 v[112:115], v0, s[4:7], s96 offen offset:2048 sc1\n"
"buffer_load_dwordx4 v[120:123], v0, s[4:7], s96 offen offset:3072 sc1\n"
"buffer_load_dwordx4 v[104:107], v1, s[4:7], s96 offen sc1\n"
"buffer_load_dwordx4 v[152:155], v1, s[4:7], s96 offen offset:1024 sc1\n"
"buffer_load_dwordx4 v[108:111], v1, s[4:7], s96 offen offset:2048 sc1\n"
"buffer_load_dwordx4 v[144:147], v1, s[4:7], s96 offen offset:3072 sc1\n"
"s_add_u32 s96, s55, 0x448000\n"
"buffer_load_dwordx4 v[92:95], v0, s[4:7], s96 offen sc1\n"
"buffer_load_dwordx4 v[124:127], v0, s[4:7], s96 offen offset:1024 sc1\n"
"buffer_load_dwordx4 v[132:135], v0, s[4:7], s96 offen offset:2048 sc1\n"
"buffer_load_dwordx4 v[140:143], v0, s[4:7], s96 offen offset:3072 sc1\n"
"buffer_load_dwordx4 v[128:131], v1, s[4:7], s96 offen sc1\n"
"buffer_load_dwordx4 v[168:171], v1, s[4:7], s96 offen offset:1024 sc1\n"
"buffer_load_dwordx4 v[172:175], v1, s[4:7], s96 offen offset:2048 sc1\n"
"buffer_load_dwordx4 v[180:183], v1, s[4:7], s96 offen offset:3072 sc1\n"
"s_waitcnt vmcnt(0)\n"
"v_max_u32_e32 v252, v96, v97\n"
"v_max3_u32 v252, v252, v98, v99\n"
"v_max3_u32 v252, v252, v100, v101\n"
"v_max3_u32 v252, v252, v102, v103\n"
"v_max3_u32 v252, v252, v112, v113\n"
"v_max3_u32 v252, v252, v114, v115\n"
"v_max3_u32 v252, v252, v120, v121\n"
"v_max3_u32 v252, v252, v122, v123\n"
"v_max3_u32 v252, v252, v104, v105\n"
"v_max3_u32 v252, v252, v106, v107\n"
"v_max3_u32 v252, v252, v152, v153\n"
"v_max3_u32 v252, v252, v154, v155\n"
"v_max3_u32 v252, v252, v108, v109\n"
"v_max3_u32 v252, v252, v110, v111\n"
"v_max3_u32 v252, v252, v144, v145\n"
"v_max3_u32 v252, v252, v146, v147\n"
"v_cmp_eq_u32_e32 vcc, -1, v252\n"
"s_cbranch_vccz .Lmk_p50k\n"
"s_sub_u32 s1, s1, 1\n"
"s_cbranch_scc0 .Lmk_p50l\n"
".Lmk_p50k:\n"
"v_mfma_f32_16x16x32_bf16 v[88:91], a[0:3], v[96:99], 0\n"
"v_mfma_f32_16x16x32_bf16 v[88:91], a[4:7], v[100:103], v[88:91]\n"
"v_mfma_f32_16x16x32_bf16 v[88:91], a[8:11], v[112:115], v[88:91]\n"
"v_mfma_f32_16x16x32_bf16 v[88:91], a[12:15], v[120:123], v[88:91]\n"
"v_mfma_f32_16x16x32_bf16 v[88:91], a[16:19], v[104:107], v[88:91]\n"
"v_mfma_f32_16x16x32_bf16 v[88:91], a[20:23], v[152:155], v[88:91]\n"
"v_mfma_f32_16x16x32_bf16 v[88:91], a[24:27], v[108:111], v[88:91]\n"
"v_mfma_f32_16x16x32_bf16 v[88:91], a[28:31], v[144:147], v[88:91]\n"
"v_mfma_f32_16x16x32_bf16 v[116:119], a[60:63], v[144:147], 0\n"
"v_mfma_f32_16x16x32_bf16 v[116:119], a[56:59], v[108:111], v[116:119]\n"
"v_mfma_f32_16x16x32_bf16 v[116:119], a[52:55], v[152:155], v[116:119]\n"
"v_mfma_f32_16x16x32_bf16 v[116:119], a[48:51], v[104:107], v[116:119]\n"
"v_mfma_f32_16x16x32_bf16 v[116:119], a[44:47], v[120:123], v[116:119]\n"
"v_mfma_f32_16x16x32_bf16 v[116:119], a[40:43], v[112:115], v[116:119]\n"
"s_nop 7\n"
"s_branch .Lmk_p50r\n"
".Lmk_p51s:\n"
";\n"
"s_waitcnt vmcnt(16)\n"
"s_mov_b32 s1, 0x10000\n"
".Lmk_p51l:\n"
"s_add_u32 s96, s55, 0x448000\n"
"buffer_load_dwordx4 v[92:95], v0, s[4:7], s96 offen sc1\n"
"buffer_load_dwordx4 v[124:127], v0, s[4:7], s96 offen offset:1024 sc1\n"
"buffer_load_dwordx4 v[132:135], v0, s[4:7], s96 offen offset:2048 sc1\n"
"buffer_load_dwordx4 v[140:143], v0, s[4:7], s96 offen offset:3072 sc1\n"
"buffer_load_dwordx4 v[128:131], v1, s[4:7], s96 offen sc1\n"
"buffer_load_dwordx4 v[168:171], v1, s[4:7], s96 offen offset:1024 sc1\n"
"buffer_load_dwordx4 v[172:175], v1, s[4:7], s96 offen offset:2048 sc1\n"
"buffer_load_dwordx4 v[180:183], v1, s[4:7], s96 offen offset:3072 sc1\n"
"s_waitcnt vmcnt(0)\n"
"v_max_u32_e32 v252, v92, v93\n"
"v_max3_u32 v252, v252, v94, v95\n"
"v_max3_u32 v252, v252, v124, v125\n"
"v_max3_u32 v252, v252, v126, v127\n"
"v_max3_u32 v252, v252, v132, v133\n"
"v_max3_u32 v252, v252, v134, v135\n"
"v_max3_u32 v252, v252, v140, v141\n"
"v_max3_u32 v252, v252, v142, v143\n"
"v_max3_u32 v252, v252, v128, v129\n"
"v_max3_u32 v252, v252, v130, v131\n"
"v_max3_u32 v252, v252, v168, v169\n"
"v_max3_u32 v252, v252, v170, v171\n"
"v_max3_u32 v252, v252, v172, v173\n"
"v_max3_u32 v252, v252, v174, v175\n"
"v_max3_u32 v252, v252, v180, v181\n"
"v_max3_u32 v252, v252, v182, v183\n"
"v_cmp_eq_u32_e32 vcc, -1, v252\n"
"s_cbranch_vccz .Lmk_p51k\n"
"s_sub_u32 s1, s1, 1\n"
"s_cbranch_scc0 .Lmk_p51l\n"
".Lmk_p51k:\n"
"v_mfma_f32_16x16x32_bf16 v[112:115], a[0:3], v[92:95], v[156:159]\n"
"v_mfma_f32_16x16x32_bf16 v[112:115], a[4:7], v[124:127], v[112:115]\n"
"v_mfma_f32_16x16x32_bf16 v[112:115], a[8:11], v[132:135], v[112:115]\n"
"v_mfma_f32_16x16x32_bf16 v[112:115], a[12:15], v[140:143], v[112:115]\n"
"v_mfma_f32_16x16x32_bf16 v[112:115], a[16:19], v[128:131], v[112:115]\n"
"v_mfma_f32_16x16x32_bf16 v[112:115], a[20:23], v[168:171], v[112:115]\n"
"v_mfma_f32_16x16x32_bf16 v[112:115], a[24:27], v[172:175], v[112:115]\n"
"v_mfma_f32_16x16x32_bf16 v[112:115], a[28:31], v[180:183], v[112:115]\n"
"v_mfma_f32_16x16x32_bf16 v[188:191], a[60:63], v[180:183], v[184:187]\n"
"v_mfma_f32_16x16x32_bf16 v[188:191], a[56:59], v[172:175], v[188:191]\n"
"v_mfma_f32_16x16x32_bf16 v[188:191], a[52:55], v[168:171], v[188:191]\n"
"v_mfma_f32_16x16x32_bf16 v[188:191], a[48:51], v[128:131], v[188:191]\n"
"v_mfma_f32_16x16x32_bf16 v[188:191], a[44:47], v[140:143], v[188:191]\n"
"v_mfma_f32_16x16x32_bf16 v[188:191], a[40:43], v[132:135], v[188:191]\n"
"s_nop 7\n"
"s_branch .Lmk_p51r\n"
".Lmk_f40s:\n"
";\n"
"s_mov_b32 s1, 0x10000\n"
".Lmk_f40l:\n"
"buffer_load_dwordx2 v[246:247], off, s[4:7], s96 sc1\n"
"s_waitcnt vmcnt(0)\n"
"v_cmp_ne_u64_e32 vcc, v[246:247], v[18:19]\n"
"s_cbranch_vccz .Lmk_f40r\n"
"s_sub_u32 s1, s1, 1\n"
"s_cbranch_scc0 .Lmk_f40l\n"
"s_branch .Lmk_f40r\n"
".Lmk_f41s:\n"
";\n"
"s_mov_b32 s1, 0x10000\n"
".Lmk_f41l:\n"
"buffer_load_dwordx2 v[250:251], v12, s[4:7], s98 offen sc1\n"
"s_waitcnt vmcnt(0)\n"
"v_cmp_ne_u64_e32 vcc, v[250:251], v[18:19]\n"
"s_cbranch_vccz .Lmk_f41r\n"
"s_sub_u32 s1, s1, 1\n"
"s_cbranch_scc0 .Lmk_f41l\n"
"s_branch .Lmk_f41r\n"
".Lmk_p55s:\n"
";\n"
"s_waitcnt vmcnt(16)\n"
"s_mov_b32 s1, 0x10000\n"
".Lmk_p55l:\n"
"s_add_u32 s96, s56, 0x440000\n"
"buffer_load_dwordx4 v[120:123], v0, s[4:7], s96 offen sc1\n"
"buffer_load_dwordx4 v[136:139], v0, s[4:7], s96 offen offset:1024 sc1\n"
"buffer_load_dwordx4 v[144:147], v0, s[4:7], s96 offen offset:2048 sc1\n"
"buffer_load_dwordx4 v[192:195], v0, s[4:7], s96 offen offset:3072 sc1\n"
"buffer_load_dwordx4 v[196:199], v1, s[4:7], s96 offen sc1\n"
"buffer_load_dwordx4 v[188:191], v1, s[4:7], s96 offen offset:1024 sc1\n"
"buffer_load_dwordx4 v[108:111], v1, s[4:7], s96 offen offset:2048 sc1\n"
"buffer_load_dwordx4 v[92:95], v1, s[4:7], s96 offen offset:3072 sc1\n"
"s_add_u32 s96, s57, 0x440000\n"
"buffer_load_dwordx4 v[148:151], v0, s[4:7], s96 offen sc1\n"
"buffer_load_dwordx4 v[180:183], v0, s[4:7], s96 offen offset:1024 sc1\n"
"buffer_load_dwordx4 v[24:27], v0, s[4:7], s96 offen offset:2048 sc1\n"
"buffer_load_dwordx4 v[32:35], v0, s[4:7], s96 offen offset:3072 sc1\n"
"buffer_load_dwordx4 v[44:47], v1, s[4:7], s96 offen sc1\n"
"buffer_load_dwordx4 v[48:51], v1, s[4:7], s96 offen offset:1024 sc1\n"
"buffer_load_dwordx4 v[28:31], v1, s[4:7], s96 offen offset:2048 sc1\n"
"buffer_load_dwordx4 v[56:59], v1, s[4:7], s96 offen offset:3072 sc1\n"
"s_add_u32 s96, s58, 0x440000\n"
"buffer_load_dwordx4 v[40:43], v0, s[4:7], s96 offen sc1\n"
"buffer_load_dwordx4 v[52:55], v0, s[4:7], s96 offen offset:1024 sc1\n"
"buffer_load_dwordx4 v[64:67], v0, s[4:7], s96 offen offset:2048 sc1\n"
"s_waitcnt vmcnt(0)\n"
"v_max_u32_e32 v255, v120, v121\n"
"v_max3_u32 v255, v255, v122, v123\n"
"v_max3_u32 v255, v255, v136, v137\n"
"v_max3_u32 v255, v255, v138, v139\n"
"v_max3_u32 v255, v255, v144, v145\n"
"v_max3_u32 v255, v255, v146, v147\n"
"v_max3_u32 v255, v255, v192, v193\n"
"v_max3_u32 v255, v255, v194, v195\n"
"v_max3_u32 v255, v255, v196, v197\n"
"v_max3_u32 v255, v255, v198, v199\n"
"v_max3_u32 v255, v255, v188, v189\n"
"v_max3_u32 v255, v255, v190, v191\n"
"v_max3_u32 v255, v255, v108, v109\n"
"v_max3_u32 v255, v255, v110, v111\n"
"v_max3_u32 v255, v255, v92, v93\n"
"v_max3_u32 v255, v255, v94, v95\n"
"v_cmp_eq_u32_e32 vcc, -1, v255\n"
"s_cbranch_vccz .Lmk_p55k\n"
"s_sub_u32 s1, s1, 1\n"
"s_cbranch_scc0 .Lmk_p55l\n"
".Lmk_p55k:\n"
"v_mfma_f32_16x16x32_bf16 v[36:39], a[128:131], v[120:123], 0\n"
"v_mfma_f32_16x16x32_bf16 v[36:39], a[132:135], v[136:139], v[36:39]\n"
"v_mfma_f32_16x16x32_bf16 v[36:39], a[136:139], v[144:147], v[36:39]\n"
"v_mfma_f32_16x16x32_bf16 v[36:39], a[140:143], v[192:195], v[36:39]\n"
"v_mfma_f32_16x16x32_bf16 v[36:39], a[144:147], v[196:199], v[36:39]\n"
"v_mfma_f32_16x16x32_bf16 v[36:39], a[148:151], v[188:191], v[36:39]\n"
"v_mfma_f32_16x16x32_bf16 v[36:39], a[152:155], v[108:111], v[36:39]\n"
"v_mfma_f32_16x16x32_bf16 v[36:39], a[156:159], v[92:95], v[36:39]\n"
"v_mfma_f32_16x16x32_bf16 v[60:63], a[188:191], v[92:95], 0\n"
"v_mfma_f32_16x16x32_bf16 v[60:63], a[184:187], v[108:111], v[60:63]\n"
"v_mfma_f32_16x16x32_bf16 v[60:63], a[180:183], v[188:191], v[60:63]\n"
"v_mfma_f32_16x16x32_bf16 v[60:63], a[176:179], v[196:199], v[60:63]\n"
"v_mfma_f32_16x16x32_bf16 v[60:63], a[172:175], v[192:195], v[60:63]\n"
"v_mfma_f32_16x16x32_bf16 v[60:63], a[168:171], v[144:147], v[60:63]\n"
"s_nop 7\n"
"s_branch .Lmk_p55r\n"
".Lmk_p56s:\n"
";\n"
"s_waitcnt vmcnt(16)\n"
"s_mov_b32 s1, 0x10000\n"
".Lmk_p56l:\n"
"s_add_u32 s96, s57, 0x440000\n"
"buffer_load_dwordx4 v[148:151], v0, s[4:7], s96 offen sc1\n"
"buffer_load_dwordx4 v[180:183], v0, s[4:7], s96 offen offset:1024 sc1\n"
"buffer_load_dwordx4 v[24:27], v0, s[4:7], s96 offen offset:2048 sc1\n"
"buffer_load_dwordx4 v[32:35], v0, s[4:7], s96 offen offset:3072 sc1\n"
"buffer_load_dwordx4 v[44:47], v1, s[4:7], s96 offen sc1\n"
"buffer_load_dwordx4 v[48:51], v1, s[4:7], s96 offen offset:1024 sc1\n"
"buffer_load_dwordx4 v[28:31], v1, s[4:7], s96 offen offset:2048 sc1\n"
"buffer_load_dwordx4 v[56:59], v1, s[4:7], s96 offen offset:3072 sc1\n"
"s_add_u32 s96, s58, 0x440000\n"
"buffer_load_dwordx4 v[40:43], v0, s[4:7], s96 offen sc1\n"
"buffer_load_dwordx4 v[52:55], v0, s[4:7], s96 offen offset:1024 sc1\n"
"buffer_load_dwordx4 v[64:67], v0, s[4:7], s96 offen offset:2048 sc1\n"
"buffer_load_dwordx4 v[36:39], v0, s[4:7], s96 offen offset:3072 sc1\n"
"buffer_load_dwordx4 v[72:75], v1, s[4:7], s96 offen sc1\n"
"buffer_load_dwordx4 v[60:63], v1, s[4:7], s96 offen offset:1024 sc1\n"
"buffer_load_dwordx4 v[80:83], v1, s[4:7], s96 offen offset:2048 sc1\n"
"buffer_load_dwordx4 v[68:71], v1, s[4:7], s96 offen offset:3072 sc1\n"
"s_add_u32 s96, s59, 0x440000\n"
"buffer_load_dwordx4 v[96:99], v0, s[4:7], s96 offen sc1\n"
"buffer_load_dwordx4 v[76:79], v0, s[4:7], s96 offen offset:1024 sc1\n"
"buffer_load_dwordx4 v[112:115], v0, s[4:7], s96 offen offset:2048 sc1\n"
"s_waitcnt vmcnt(0)\n"
"v_max_u32_e32 v254, v148, v149\n"
"v_max3_u32 v254, v254, v150, v151\n"
"v_max3_u32 v254, v254, v180, v181\n"
"v_max3_u32 v254, v254, v182, v183\n"
"v_max3_u32 v254, v254, v24, v25\n"
"v_max3_u32 v254, v254, v26, v27\n"
"v_max3_u32 v254, v254, v32, v33\n"
"v_max3_u32 v254, v254, v34, v35\n"
"v_max3_u32 v254, v254, v44, v45\n"
"v_max3_u32 v254, v254, v46, v47\n"
"v_max3_u32 v254, v254, v48, v49\n"
"v_max3_u32 v254, v254, v50, v51\n"
"v_max3_u32 v254, v254, v28, v29\n"
"v_max3_u32 v254, v254, v30, v31\n"
"v_max3_u32 v254, v254, v56, v57\n"
"v_max3_u32 v254, v254, v58, v59\n"
"v_cmp_eq_u32_e32 vcc, -1, v254\n"
"s_cbranch_vccz .Lmk_p56k\n"
"s_sub_u32 s1, s1, 1\n"
"s_cbranch_scc0 .Lmk_p56l\n"
".Lmk_p56k:\n"
"v_mfma_f32_16x16x32_bf16 v[84:87], a[128:131], v[148:151], 0\n"
"v_mfma_f32_16x16x32_bf16 v[84:87], a[132:135], v[180:183], v[84:87]\n"
"v_mfma_f32_16x16x32_bf16 v[84:87], a[136:139], v[24:27], v[84:87]\n"
"v_mfma_f32_16x16x32_bf16 v[84:87], a[140:143], v[32:35], v[84:87]\n"
"v_mfma_f32_16x16x32_bf16 v[84:87], a[144:147], v[44:47], v[84:87]\n"
"v_mfma_f32_16x16x32_bf16 v[84:87], a[148:151], v[48:51], v[84:87]\n"
"v_mfma_f32_16x16x32_bf16 v[84:87], a[152:155], v[28:31], v[84:87]\n"
"v_mfma_f32_16x16x32_bf16 v[84:87], a[156:159], v[56:59], v[84:87]\n"
"v_mfma_f32_16x16x32_bf16 v[100:103], a[188:191], v[56:59], 0\n"
"v_mfma_f32_16x16x32_bf16 v[100:103], a[184:187], v[28:31], v[100:103]\n"
"v_mfma_f32_16x16x32_bf16 v[100:103], a[180:183], v[48:51], v[100:103]\n"
"v_mfma_f32_16x16x32_bf16 v[100:103], a[176:179], v[44:47], v[100:103]\n"
"v_mfma_f32_16x16x32_bf16 v[100:103], a[172:175], v[32:35], v[100:103]\n"
"v_mfma_f32_16x16x32_bf16 v[100:103], a[168:171], v[24:27], v[100:103]\n"
"s_nop 7\n"
"s_branch .Lmk_p56r\n"
".Lmk_p57s:\n"
";\n"
"s_waitcnt vmcnt(16)\n"
"s_mov_b32 s1, 0x10000\n"
".Lmk_p57l:\n"
"s_add_u32 s96, s58, 0x440000\n"
"buffer_load_dwordx4 v[40:43], v0, s[4:7], s96 offen sc1\n"
"buffer_load_dwordx4 v[52:55], v0, s[4:7], s96 offen offset:1024 sc1\n"
"buffer_load_dwordx4 v[64:67], v0, s[4:7], s96 offen offset:2048 sc1\n"
"buffer_load_dwordx4 v[36:39], v0, s[4:7], s96 offen offset:3072 sc1\n"
"buffer_load_dwordx4 v[72:75], v1, s[4:7], s96 offen sc1\n"
"buffer_load_dwordx4 v[60:63], v1, s[4:7], s96 offen offset:1024 sc1\n"
"buffer_load_dwordx4 v[80:83], v1, s[4:7], s96 offen offset:2048 sc1\n"
"buffer_load_dwordx4 v[68:71], v1, s[4:7], s96 offen offset:3072 sc1\n"
"s_add_u32 s96, s59, 0x440000\n"
"buffer_load_dwordx4 v[96:99], v0, s[4:7], s96 offen sc1\n"
"buffer_load_dwordx4 v[76:79], v0, s[4:7], s96 offen offset:1024 sc1\n"
"buffer_load_dwordx4 v[112:115], v0, s[4:7], s96 offen offset:2048 sc1\n"
"buffer_load_dwordx4 v[84:87], v0, s[4:7], s96 offen offset:3072 sc1\n"
"buffer_load_dwordx4 v[128:131], v1, s[4:7], s96 offen sc1\n"
"buffer_load_dwordx4 v[100:103], v1, s[4:7], s96 offen offset:1024 sc1\n"
"buffer_load_dwordx4 v[156:159], v1, s[4:7], s96 offen offset:2048 sc1\n"
"buffer_load_dwordx4 v[116:119], v1, s[4:7], s96 offen offset:3072 sc1\n"
"s_waitcnt vmcnt(0)\n"
"v_max_u32_e32 v237, v40, v41\n"
"v_max3_u32 v237, v237, v42, v43\n"
"v_max3_u32 v237, v237, v52, v53\n"
"v_max3_u32 v237, v237, v54, v55\n"
"v_max3_u32 v237, v237, v64, v65\n"
"v_max3_u32 v237, v237, v66, v67\n"
"v_max3_u32 v237, v237, v36, v37\n"
"v_max3_u32 v237, v237, v38, v39\n"
"v_max3_u32 v237, v237, v72, v73\n"
"v_max3_u32 v237, v237, v74, v75\n"
"v_max3_u32 v237, v237, v60, v61\n"
"v_max3_u32 v237, v237, v62, v63\n"
"v_max3_u32 v237, v237, v80, v81\n"
"v_max3_u32 v237, v237, v82, v83\n"
"v_max3_u32 v237, v237, v68, v69\n"
"v_max3_u32 v237, v237, v70, v71\n"
"v_cmp_eq_u32_e32 vcc, -1, v237\n"
"s_cbranch_vccz .Lmk_p57k\n"
"s_sub_u32 s1, s1, 1\n"
"s_cbranch_scc0 .Lmk_p57l\n"
".Lmk_p57k:\n"
"v_mfma_f32_16x16x32_bf16 v[132:135], a[128:131], v[40:43], 0\n"
"v_mfma_f32_16x16x32_bf16 v[132:135], a[132:135], v[52:55], v[132:135]\n"
"v_mfma_f32_16x16x32_bf16 v[132:135], a[136:139], v[64:67], v[132:135]\n"
"v_mfma_f32_16x16x32_bf16 v[132:135], a[140:143], v[36:39], v[132:135]\n"
"v_mfma_f32_16x16x32_bf16 v[132:135], a[144:147], v[72:75], v[132:135]\n"
"v_mfma_f32_16x16x32_bf16 v[132:135], a[148:151], v[60:63], v[132:135]\n"
"v_mfma_f32_16x16x32_bf16 v[132:135], a[152:155], v[80:83], v[132:135]\n"
"v_mfma_f32_16x16x32_bf16 v[132:135], a[156:159], v[68:71], v[132:135]\n"
"v_mfma_f32_16x16x32_bf16 v[140:143], a[188:191], v[68:71], 0\n"
"v_mfma_f32_16x16x32_bf16 v[140:143], a[184:187], v[80:83], v[140:143]\n"
"v_mfma_f32_16x16x32_bf16 v[140:143], a[180:183], v[60:63], v[140:143]\n"
"v_mfma_f32_16x16x32_bf16 v[140:143], a[176:179], v[72:75], v[140:143]\n"
"v_mfma_f32_16x16x32_bf16 v[140:143], a[172:175], v[36:39], v[140:143]\n"
"v_mfma_f32_16x16x32_bf16 v[140:143], a[168:171], v[64:67], v[140:143]\n"
"s_nop 7\n"
"s_branch .Lmk_p57r\n"
".Lmk_p58s:\n"
";\n"
"s_waitcnt vmcnt(16)\n"
"s_mov_b32 s1, 0x10000\n"
".Lmk_p58l:\n"
"s_add_u32 s96, s59, 0x440000\n"
"buffer_load_dwordx4 v[96:99], v0, s[4:7], s96 offen sc1\n"
"buffer_load_dwordx4 v[76:79], v0, s[4:7], s96 offen offset:1024 sc1\n"
"buffer_load_dwordx4 v[112:115], v0, s[4:7], s96 offen offset:2048 sc1\n"
"buffer_load_dwordx4 v[84:87], v0, s[4:7], s96 offen offset:3072 sc1\n"
"buffer_load_dwordx4 v[128:131], v1, s[4:7], s96 offen sc1\n"
"buffer_load_dwordx4 v[100:103], v1, s[4:7], s96 offen offset:1024 sc1\n"
"buffer_load_dwordx4 v[156:159], v1, s[4:7], s96 offen offset:2048 sc1\n"
"buffer_load_dwordx4 v[116:119], v1, s[4:7], s96 offen offset:3072 sc1\n"
"s_add_u32 s96, s56, 0x400000\n"
"buffer_load_dwordx4 v[132:135], v0, s[4:7], s96 offen sc1\n"
"buffer_load_dwordx4 v[184:187], v0, s[4:7], s96 offen offset:1024 sc1\n"
"buffer_load_dwordx4 v[140:143], v0, s[4:7], s96 offen offset:2048 sc1\n"
"buffer_load_dwordx4 v[68:71], v0, s[4:7], s96 offen offset:3072 sc1\n"
"buffer_load_dwordx4 v[36:39], v1, s[4:7], s96 offen sc1\n"
"buffer_load_dwordx4 v[40:43], v1, s[4:7], s96 offen offset:1024 sc1\n"
"buffer_load_dwordx4 v[72:75], v1, s[4:7], s96 offen offset:2048 sc1\n"
"buffer_load_dwordx4 v[80:83], v1, s[4:7], s96 offen offset:3072 sc1\n"
"s_add_u32 s96, s57, 0x400000\n"
"buffer_load_dwordx4 v[164:167], v0, s[4:7], s96 offen sc1\n"
"s_waitcnt vmcnt(0)\n"
"v_max_u32_e32 v254, v96, v97\n"
"v_max3_u32 v254, v254, v98, v99\n"
"v_max3_u32 v254, v254, v76, v77\n"
"v_max3_u32 v254, v254, v78, v79\n"
"v_max3_u32 v254, v254, v112, v113\n"
"v_max3_u32 v254, v254, v114, v115\n"
"v_max3_u32 v254, v254, v84, v85\n"
"v_max3_u32 v254, v254, v86, v87\n"
"v_max3_u32 v254, v254, v128, v129\n"
"v_max3_u32 v254, v254, v130, v131\n"
"v_max3_u32 v254, v254, v100, v101\n"
"v_max3_u32 v254, v254, v102, v103\n"
"v_max3_u32 v254, v254, v156, v157\n"
"v_max3_u32 v254, v254, v158, v159\n"
"v_max3_u32 v254, v254, v116, v117\n"
"v_max3_u32 v254, v254, v118, v119\n"
"v_cmp_eq_u32_e32 vcc, -1, v254\n"
"s_cbranch_vccz .Lmk_p58k\n"
"s_sub_u32 s1, s1, 1\n"
"s_cbranch_scc0 .Lmk_p58l\n"
".Lmk_p58k:\n"
"v_mfma_f32_16x16x32_bf16 v[64:67], a[128:131], v[96:99], v[152:155]\n"
"v_mfma_f32_16x16x32_bf16 v[64:67], a[132:135], v[76:79], v[64:67]\n"
"v_mfma_f32_16x16x32_bf16 v[64:67], a[136:139], v[112:115], v[64:67]\n"
"v_mfma_f32_16x16x32_bf16 v[64:67], a[140:143], v[84:87], v[64:67]\n"
"v_mfma_f32_16x16x32_bf16 v[64:67], a[144:147], v[128:131], v[64:67]\n"
"v_mfma_f32_16x16x32_bf16 v[64:67], a[148:151], v[100:103], v[64:67]\n"
"v_mfma_f32_16x16x32_bf16 v[64:67], a[152:155], v[156:159], v[64:67]\n"
"v_mfma_f32_16x16x32_bf16 v[64:67], a[156:159], v[116:119], v[64:67]\n"
"v_mfma_f32_16x16x32_bf16 v[60:63], a[188:191], v[116:119], v[168:171]\n"
"v_mfma_f32_16x16x32_bf16 v[60:63], a[184:187], v[156:159], v[60:63]\n"
"v_mfma_f32_16x16x32_bf16 v[60:63], a[180:183], v[100:103], v[60:63]\n"
"v_mfma_f32_16x16x32_bf16 v[60:63], a[176:179], v[128:131], v[60:63]\n"
"v_mfma_f32_16x16x32_bf16 v[60:63], a[172:175], v[84:87], v[60:63]\n"
"v_mfma_f32_16x16x32_bf16 v[60:63], a[168:171], v[112:115], v[60:63]\n"
"s_nop 7\n"
"s_branch .Lmk_p58r\n"
".Lmk_p59s:\n"
";\n"
"s_waitcnt vmcnt(16)\n"
"s_mov_b32 s1, 0x10000\n"
".Lmk_p59l:\n"
"s_add_u32 s96, s56, 0x400000\n"
"buffer_load_dwordx4 v[132:135], v0, s[4:7], s96 offen sc1\n"
"buffer_load_dwordx4 v[184:187], v0, s[4:7], s96 offen offset:1024 sc1\n"
"buffer_load_dwordx4 v[140:143], v0, s[4:7], s96 offen offset:2048 sc1\n"
"buffer_load_dwordx4 v[68:71], v0, s[4:7], s96 offen offset:3072 sc1\n"
"buffer_load_dwordx4 v[36:39], v1, s[4:7], s96 offen sc1\n"
"buffer_load_dwordx4 v[40:43], v1, s[4:7], s96 offen offset:1024 sc1\n"
"buffer_load_dwordx4 v[72:75], v1, s[4:7], s96 offen offset:2048 sc1\n"
"buffer_load_dwordx4 v[80:83], v1, s[4:7], s96 offen offset:3072 sc1\n"
"s_add_u32 s96, s57, 0x400000\n"
"buffer_load_dwordx4 v[164:167], v0, s[4:7], s96 offen sc1\n"
"buffer_load_dwordx4 v[168:171], v0, s[4:7], s96 offen offset:1024 sc1\n"
"buffer_load_dwordx4 v[208:211], v0, s[4:7], s96 offen offset:2048 sc1\n"
"buffer_load_dwordx4 v[212:215], v0, s[4:7], s96 offen offset:3072 sc1\n"
"buffer_load_dwordx4 v[64:67], v1, s[4:7], s96 offen sc1\n"
"buffer_load_dwordx4 v[100:103], v1, s[4:7], s96 offen offset:1024 sc1\n"
"buffer_load_dwordx4 v[60:63], v1, s[4:7], s96 offen offset:2048 sc1\n"
"buffer_load_dwordx4 v[76:79], v1, s[4:7], s96 offen offset:3072 sc1\n"
"s_add_u32 s96, s58, 0x400000\n"
"buffer_load_dwordx4 v[96:99], v0, s[4:7], s96 offen sc1\n"
"buffer_load_dwordx4 v[112:115], v0, s[4:7], s96 offen offset:1024 sc1\n"
"buffer_load_dwordx4 v[52:55], v0, s[4:7], s96 offen offset:2048 sc1\n"
"buffer_load_dwordx4 v[152:155], v0, s[4:7], s96 offen offset:3072 sc1\n"
"s_waitcnt vmcnt(0)\n"
"v_max_u32_e32 v255, v132, v133\n"
"v_max3_u32 v255, v255, v134, v135\n"
"v_max3_u32 v255, v255, v184, v185\n"
"v_max3_u32 v255, v255, v186, v187\n"
"v_max3_u32 v255, v255, v140, v141\n"
"v_max3_u32 v255, v255, v142, v143\n"
"v_max3_u32 v255, v255, v68, v69\n"
"v_max3_u32 v255, v255, v70, v71\n"
"v_max3_u32 v255, v255, v36, v37\n"
"v_max3_u32 v255, v255, v38, v39\n"
"v_max3_u32 v255, v255, v40, v41\n"
"v_max3_u32 v255, v255, v42, v43\n"
"v_max3_u32 v255, v255, v72, v73\n"
"v_max3_u32 v255, v255, v74, v75\n"
"v_max3_u32 v255, v255, v80, v81\n"
"v_max3_u32 v255, v255, v82, v83\n"
"v_cmp_eq_u32_e32 vcc, -1, v255\n"
"s_cbranch_vccz .Lmk_p59k\n"
"s_sub_u32 s1, s1, 1\n"
"s_cbranch_scc0 .Lmk_p59l\n"
".Lmk_p59k:\n"
"v_mfma_f32_16x16x32_bf16 v[84:87], a[128:131], v[132:135], 0\n"
"v_mfma_f32_16x16x32_bf16 v[84:87], a[132:135], v[184:187], v[84:87]\n"
"v_mfma_f32_16x16x32_bf16 v[84:87], a[136:139], v[140:143], v[84:87]\n"
"v_mfma_f32_16x16x32_bf16 v[84:87], a[140:143], v[68:71], v[84:87]\n"
"v_mfma_f32_16x16x32_bf16 v[84:87], a[144:147], v[36:39], v[84:87]\n"
"v_mfma_f32_16x16x32_bf16 v[84:87], a[148:151], v[40:43], v[84:87]\n"
"v_mfma_f32_16x16x32_bf16 v[84:87], a[152:155], v[72:75], v[84:87]\n"
"v_mfma_f32_16x16x32_bf16 v[84:87], a[156:159], v[80:83], v[84:87]\n"
"v_mfma_f32_16x16x32_bf16 v[128:131], a[188:191], v[80:83], 0\n"
"v_mfma_f32_16x16x32_bf16 v[128:131], a[184:187], v[72:75], v[128:131]\n"
"v_mfma_f32_16x16x32_bf16 v[128:131], a[180:183], v[40:43], v[128:131]\n"
"v_mfma_f32_16x16x32_bf16 v[128:131], a[176:179], v[36:39], v[128:131]\n"
"v_mfma_f32_16x16x32_bf16 v[128:131], a[172:175], v[68:71], v[128:131]\n"
"v_mfma_f32_16x16x32_bf16 v[128:131], a[168:171], v[140:143], v[128:131]\n"
"s_nop 7\n"
"s_branch .Lmk_p59r\n"
".Lmk_p60s:\n"
";\n"
"s_waitcnt vmcnt(16)\n"
"s_mov_b32 s1, 0x10000\n"
".Lmk_p60l:\n"
"s_add_u32 s96, s57, 0x400000\n"
"buffer_load_dwordx4 v[164:167], v0, s[4:7], s96 offen sc1\n"
"buffer_load_dwordx4 v[168:171], v0, s[4:7], s96 offen offset:1024 sc1\n"
"buffer_load_dwordx4 v[208:211], v0, s[4:7], s96 offen offset:2048 sc1\n"
"buffer_load_dwordx4 v[212:215], v0, s[4:7], s96 offen offset:3072 sc1\n"
"buffer_load_dwordx4 v[64:67], v1, s[4:7], s96 offen sc1\n"
"buffer_load_dwordx4 v[100:103], v1, s[4:7], s96 offen offset:1024 sc1\n"
"buffer_load_dwordx4 v[60:63], v1, s[4:7], s96 offen offset:2048 sc1\n"
"buffer_load_dwordx4 v[76:79], v1, s[4:7], s96 offen offset:3072 sc1\n"
"s_add_u32 s96, s58, 0x400000\n"
"buffer_load_dwordx4 v[96:99], v0, s[4:7], s96 offen sc1\n"
"buffer_load_dwordx4 v[112:115], v0, s[4:7], s96 offen offset:1024 sc1\n"
"buffer_load_dwordx4 v[52:55], v0, s[4:7], s96 offen offset:2048 sc1\n"
"buffer_load_dwordx4 v[152:155], v0, s[4:7], s96 offen offset:3072 sc1\n"
"buffer_load_dwordx4 v[84:87], v1, s[4:7], s96 offen sc1\n"
"buffer_load_dwordx4 v[172:175], v1, s[4:7], s96 offen offset:1024 sc1\n"
"buffer_load_dwordx4 v[128:131], v1, s[4:7], s96 offen offset:2048 sc1\n"
"buffer_load_dwordx4 v[72:75], v1, s[4:7], s96 offen offset:3072 sc1\n"
"s_add_u32 s96, s59, 0x400000\n"
"buffer_load_dwordx4 v[36:39], v0, s[4:7], s96 offen sc1\n"
"buffer_load_dwordx4 v[40:43], v0, s[4:7], s96 offen offset:1024 sc1\n"
"buffer_load_dwordx4 v[80:83], v0, s[4:7], s96 offen offset:2048 sc1\n"
"buffer_load_dwordx4 v[132:135], v0, s[4:7], s96 offen offset:3072 sc1\n"
"buffer_load_dwordx4 v[156:159], v1, s[4:7], s96 offen sc1\n"
"s_waitcnt vmcnt(0)\n"
"v_max_u32_e32 v254, v164, v165\n"
"v_max3_u32 v254, v254, v166, v167\n"
"v_max3_u32 v254, v254, v168, v169\n"
"v_max3_u32 v254, v254, v170, v171\n"
"v_max3_u32 v254, v254, v208, v209\n"
"v_max3_u32 v254, v254, v210, v211\n"
"v_max3_u32 v254, v254, v212, v213\n"
"v_max3_u32 v254, v254, v214, v215\n"
"v_max3_u32 v254, v254, v64, v65\n"
"v_max3_u32 v254, v254, v66, v67\n"
"v_max3_u32 v254, v254, v100, v101\n"
"v_max3_u32 v254, v254, v102, v103\n"
"v_max3_u32 v254, v254, v60, v61\n"
"v_max3_u32 v254, v254, v62, v63\n"
"v_max3_u32 v254, v254, v76, v77\n"
"v_max3_u32 v254, v254, v78, v79\n"
"v_cmp_eq_u32_e32 vcc, -1, v254\n"
"s_cbranch_vccz .Lmk_p60k\n"
"s_sub_u32 s1, s1, 1\n"
"s_cbranch_scc0 .Lmk_p60l\n"
".Lmk_p60k:\n"
"v_mfma_f32_16x16x32_bf16 v[68:71], a[128:131], v[164:167], 0\n"
"v_mfma_f32_16x16x32_bf16 v[68:71], a[132:135], v[168:171], v[68:71]\n"
"v_mfma_f32_16x16x32_bf16 v[68:71], a[136:139], v[208:211], v[68:71]\n"
"v_mfma_f32_16x16x32_bf16 v[68:71], a[140:143], v[212:215], v[68:71]\n"
"v_mfma_f32_16x16x32_bf16 v[68:71], a[144:147], v[64:67], v[68:71]\n"
"v_mfma_f32_16x16x32_bf16 v[68:71], a[148:151], v[100:103], v[68:71]\n"
"v_mfma_f32_16x16x32_bf16 v[68:71], a[152:155], v[60:63], v[68:71]\n"
"v_mfma_f32_16x16x32_bf16 v[68:71], a[156:159], v[76:79], v[68:71]\n"
"v_mfma_f32_16x16x32_bf16 v[140:143], a[188:191], v[76:79], 0\n"
"v_mfma_f32_16x16x32_bf16 v[140:143], a[184:187], v[60:63], v[140:143]\n"
"v_mfma_f32_16x16x32_bf16 v[140:143], a[180:183], v[100:103], v[140:143]\n"
"v_mfma_f32_16x16x32_bf16 v[140:143], a[176:179], v[64:67], v[140:143]\n"
"v_mfma_f32_16x16x32_bf16 v[140:143], a[172:175], v[212:215], v[140:143]\n"
"v_mfma_f32_16x16x32_bf16 v[140:143], a[168:171], v[208:211], v[140:143]\n"
"s_nop 7\n"
"s_branch .Lmk_p60r\n"
".Lmk_p61s:\n"
";\n"
"s_waitcnt vmcnt(16)\n"
"s_mov_b32 s1, 0x10000\n"
".Lmk_p61l:\n"
"s_add_u32 s97, s58, 0x400000\n"
"buffer_load_dwordx4 v[96:99], v0, s[4:7], s97 offen sc1\n"
"buffer_load_dwordx4 v[112:115], v0, s[4:7], s97 offen offset:1024 sc1\n"
"buffer_load_dwordx4 v[52:55], v0, s[4:7], s97 offen offset:2048 sc1\n"
"buffer_load_dwordx4 v[152:155], v0, s[4:7], s97 offen offset:3072 sc1\n"
"buffer_load_dwordx4 v[84:87], v1, s[4:7], s97 offen sc1\n"
"buffer_load_dwordx4 v[172:175], v1, s[4:7], s97 offen offset:1024 sc1\n"
"buffer_load_dwordx4 v[128:131], v1, s[4:7], s97 offen offset:2048 sc1\n"
"buffer_load_dwordx4 v[72:75], v1, s[4:7], s97 offen offset:3072 sc1\n"
"s_add_u32 s97, s59, 0x400000\n"
"buffer_load_dwordx4 v[36:39], v0, s[4:7], s97 offen sc1\n"
"buffer_load_dwordx4 v[40:43], v0, s[4:7], s97 offen offset:1024 sc1\n"
"buffer_load_dwordx4 v[80:83], v0, s[4:7], s97 offen offset:2048 sc1\n"
"buffer_load_dwordx4 v[132:135], v0, s[4:7], s97 offen offset:3072 sc1\n"
"buffer_load_dwordx4 v[156:159], v1, s[4:7], s97 offen sc1\n"
"buffer_load_dwordx4 v[68:71], v1, s[4:7], s97 offen offset:1024 sc1\n"
"buffer_load_dwordx4 v[200:203], v1, s[4:7], s97 offen offset:2048 sc1\n"
"buffer_load_dwordx4 v[140:143], v1, s[4:7], s97 offen offset:3072 sc1\n"
"s_waitcnt vmcnt(0)\n"
"v_max_u32_e32 v253, v96, v97\n"
"v_max3_u32 v253, v253, v98, v99\n"
"v_max3_u32 v253, v253, v112, v113\n"
"v_max3_u32 v253, v253, v114, v115\n"
"v_max3_u32 v253, v253, v52, v53\n"
"v_max3_u32 v253, v253, v54, v55\n"
"v_max3_u32 v253, v253, v152, v153\n"
"v_max3_u32 v253, v253, v154, v155\n"
"v_max3_u32 v253, v253, v84, v85\n"
"v_max3_u32 v253, v253, v86, v87\n"
"v_max3_u32 v253, v253, v172, v173\n"
"v_max3_u32 v253, v253, v174, v175\n"
"v_max3_u32 v253, v253, v128, v129\n"
"v_max3_u32 v253, v253, v130, v131\n"
"v_max3_u32 v253, v253, v72, v73\n"
"v_max3_u32 v253, v253, v74, v75\n"
"v_cmp_eq_u32_e32 vcc, -1, v253\n"
"s_cbranch_vccz .Lmk_p61k\n"
"s_sub_u32 s1, s1, 1\n"
"s_cbranch_scc0 .Lmk_p61l\n"
".Lmk_p61k:\n"
"v_mfma_f32_16x16x32_bf16 v[60:63], a[128:131], v[96:99], 0\n"
"v_mfma_f32_16x16x32_bf16 v[60:63], a[132:135], v[112:115], v[60:63]\n"
"v_mfma_f32_16x16x32_bf16 v[60:63], a[136:139], v[52:55], v[60:63]\n"
"v_mfma_f32_16x16x32_bf16 v[60:63], a[140:143], v[152:155], v[60:63]\n"
"v_mfma_f32_16x16x32_bf16 v[60:63], a[144:147], v[84:87], v[60:63]\n"
"v_mfma_f32_16x16x32_bf16 v[60:63], a[148:151], v[172:175], v[60:63]\n"
"v_mfma_f32_16x16x32_bf16 v[60:63], a[152:155], v[128:131], v[60:63]\n"
"v_mfma_f32_16x16x32_bf16 v[60:63], a[156:159], v[72:75], v[60:63]\n"
"v_mfma_f32_16x16x32_bf16 v[64:67], a[188:191], v[72:75], 0\n"
"v_mfma_f32_16x16x32_bf16 v[64:67], a[184:187], v[128:131], v[64:67]\n"
"v_mfma_f32_16x16x32_bf16 v[64:67], a[180:183], v[172:175], v[64:67]\n"
"v_mfma_f32_16x16x32_bf16 v[64:67], a[176:179], v[84:87], v[64:67]\n"
"v_mfma_f32_16x16x32_bf16 v[64:67], a[172:175], v[152:155], v[64:67]\n"
"v_mfma_f32_16x16x32_bf16 v[64:67], a[168:171], v[52:55], v[64:67]\n"
"s_nop 7\n"
"s_branch .Lmk_p61r\n"
".Lmk_p62s:\n"
";\n"
"s_waitcnt vmcnt(16)\n"
"s_mov_b32 s1, 0x10000\n"
".Lmk_p62l:\n"
"s_add_u32 s97, s59, 0x400000\n"
"buffer_load_dwordx4 v[36:39], v0, s[4:7], s97 offen sc1\n"
"buffer_load_dwordx4 v[40:43], v0, s[4:7], s97 offen offset:1024 sc1\n"
"buffer_load_dwordx4 v[80:83], v0, s[4:7], s97 offen offset:2048 sc1\n"
"buffer_load_dwordx4 v[132:135], v0, s[4:7], s97 offen offset:3072 sc1\n"
"buffer_load_dwordx4 v[156:159], v1, s[4:7], s97 offen sc1\n"
"buffer_load_dwordx4 v[68:71], v1, s[4:7], s97 offen offset:1024 sc1\n"
"buffer_load_dwordx4 v[200:203], v1, s[4:7], s97 offen offset:2048 sc1\n"
"buffer_load_dwordx4 v[140:143], v1, s[4:7], s97 offen offset:3072 sc1\n"
"s_waitcnt vmcnt(0)\n"
"v_max_u32_e32 v254, v36, v37\n"
"v_max3_u32 v254, v254, v38, v39\n"
"v_max3_u32 v254, v254, v40, v41\n"
"v_max3_u32 v254, v254, v42, v43\n"
"v_max3_u32 v254, v254, v80, v81\n"
"v_max3_u32 v254, v254, v82, v83\n"
"v_max3_u32 v254, v254, v132, v133\n"
"v_max3_u32 v254, v254, v134, v135\n"
"v_max3_u32 v254, v254, v156, v157\n"
"v_max3_u32 v254, v254, v158, v159\n"
"v_max3_u32 v254, v254, v68, v69\n"
"v_max3_u32 v254, v254, v70, v71\n"
"v_max3_u32 v254, v254, v200, v201\n"
"v_max3_u32 v254, v254, v202, v203\n"
"v_max3_u32 v254, v254, v140, v141\n"
"v_max3_u32 v254, v254, v142, v143\n"
"v_cmp_eq_u32_e32 vcc, -1, v254\n"
"s_cbranch_vccz .Lmk_p62k\n"
"s_sub_u32 s1, s1, 1\n"
"s_cbranch_scc0 .Lmk_p62l\n"
".Lmk_p62k:\n"
"v_mfma_f32_16x16x32_bf16 v[52:55], a[128:131], v[36:39], v[76:79]\n"
"v_mfma_f32_16x16x32_bf16 v[52:55], a[132:135], v[40:43], v[52:55]\n"
"v_mfma_f32_16x16x32_bf16 v[52:55], a[136:139], v[80:83], v[52:55]\n"
"v_mfma_f32_16x16x32_bf16 v[52:55], a[140:143], v[132:135], v[52:55]\n"
"v_mfma_f32_16x16x32_bf16 v[52:55], a[144:147], v[156:159], v[52:55]\n"
"v_mfma_f32_16x16x32_bf16 v[52:55], a[148:151], v[68:71], v[52:55]\n"
"v_mfma_f32_16x16x32_bf16 v[52:55], a[152:155], v[200:203], v[52:55]\n"
"v_mfma_f32_16x16x32_bf16 v[52:55], a[156:159], v[140:143], v[52:55]\n"
"v_mfma_f32_16x16x32_bf16 v[60:63], a[188:191], v[140:143], v[116:119]\n"
"v_mfma_f32_16x16x32_bf16 v[60:63], a[184:187], v[200:203], v[60:63]\n"
"v_mfma_f32_16x16x32_bf16 v[60:63], a[180:183], v[68:71], v[60:63]\n"
"v_mfma_f32_16x16x32_bf16 v[60:63], a[176:179], v[156:159], v[60:63]\n"
"v_mfma_f32_16x16x32_bf16 v[60:63], a[172:175], v[132:135], v[60:63]\n"
"v_mfma_f32_16x16x32_bf16 v[60:63], a[168:171], v[80:83], v[60:63]\n"
"s_nop 7\n"
"s_branch .Lmk_p62r\n"
".Lmk_p63s:\n"
";\n"
"s_waitcnt vmcnt(16)\n"
"s_mov_b32 s1, 0x10000\n"
".Lmk_p63l:\n"
"s_add_u32 s96, s56, 0x440000\n"
"buffer_load_dwordx4 v[120:123], v0, s[4:7], s96 offen sc1\n"
"buffer_load_dwordx4 v[136:139], v0, s[4:7], s96 offen offset:1024 sc1\n"
"buffer_load_dwordx4 v[144:147], v0, s[4:7], s96 offen offset:2048 sc1\n"
"buffer_load_dwordx4 v[192:195], v0, s[4:7], s96 offen offset:3072 sc1\n"
"buffer_load_dwordx4 v[196:199], v1, s[4:7], s96 offen sc1\n"
"buffer_load_dwordx4 v[188:191], v1, s[4:7], s96 offen offset:1024 sc1\n"
"buffer_load_dwordx4 v[108:111], v1, s[4:7], s96 offen offset:2048 sc1\n"
"buffer_load_dwordx4 v[92:95], v1, s[4:7], s96 offen offset:3072 sc1\n"
"s_add_u32 s96, s57, 0x440000\n"
"buffer_load_dwordx4 v[148:151], v0, s[4:7], s96 offen sc1\n"
"buffer_load_dwordx4 v[180:183], v0, s[4:7], s96 offen offset:1024 sc1\n"
"buffer_load_dwordx4 v[24:27], v0, s[4:7], s96 offen offset:2048 sc1\n"
"buffer_load_dwordx4 v[32:35], v0, s[4:7], s96 offen offset:3072 sc1\n"
"buffer_load_dwordx4 v[44:47], v1, s[4:7], s96 offen sc1\n"
"buffer_load_dwordx4 v[48:51], v1, s[4:7], s96 offen offset:1024 sc1\n"
"buffer_load_dwordx4 v[28:31], v1, s[4:7], s96 offen offset:2048 sc1\n"
"buffer_load_dwordx4 v[56:59], v1, s[4:7], s96 offen offset:3072 sc1\n"
"s_add_u32 s96, s58, 0x440000\n"
"buffer_load_dwordx4 v[36:39], v0, s[4:7], s96 offen sc1\n"
"buffer_load_dwordx4 v[40:43], v0, s[4:7], s96 offen offset:1024 sc1\n"
"buffer_load_dwordx4 v[52:55], v0, s[4:7], s96 offen offset:2048 sc1\n"
"buffer_load_dwordx4 v[60:63], v0, s[4:7], s96 offen offset:3072 sc1\n"
"buffer_load_dwordx4 v[64:67], v1, s[4:7], s96 offen sc1\n"
"buffer_load_dwordx4 v[68:71], v1, s[4:7], s96 offen offset:1024 sc1\n"
"buffer_load_dwordx4 v[72:75], v1, s[4:7], s96 offen offset:2048 sc1\n"
"buffer_load_dwordx4 v[76:79], v1, s[4:7], s96 offen offset:3072 sc1\n"
"s_add_u32 s96, s59, 0x440000\n"
"buffer_load_dwordx4 v[84:87], v0, s[4:7], s96 offen sc1\n"
"buffer_load_dwordx4 v[88:91], v0, s[4:7], s96 offen offset:1024 sc1\n"
"buffer_load_dwordx4 v[100:103], v0, s[4:7], s96 offen offset:2048 sc1\n"
"buffer_load_dwordx4 v[104:107], v0, s[4:7], s96 offen offset:3072 sc1\n"
"s_waitcnt vmcnt(0)\n"
"v_max_u32_e32 v255, v120, v121\n"
"v_max3_u32 v255, v255, v122, v123\n"
"v_max3_u32 v255, v255, v136, v137\n"
"v_max3_u32 v255, v255, v138, v139\n"
"v_max3_u32 v255, v255, v144, v145\n"
"v_max3_u32 v255, v255, v146, v147\n"
"v_max3_u32 v255, v255, v192, v193\n"
"v_max3_u32 v255, v255, v194, v195\n"
"v_max3_u32 v255, v255, v196, v197\n"
"v_max3_u32 v255, v255, v198, v199\n"
"v_max3_u32 v255, v255, v188, v189\n"
"v_max3_u32 v255, v255, v190, v191\n"
"v_max3_u32 v255, v255, v108, v109\n"
"v_max3_u32 v255, v255, v110, v111\n"
"v_max3_u32 v255, v255, v92, v93\n"
"v_max3_u32 v255, v255, v94, v95\n"
"v_cmp_eq_u32_e32 vcc, -1, v255\n"
"s_cbranch_vccz .Lmk_p63k\n"
"s_sub_u32 s1, s1, 1\n"
"s_cbranch_scc0 .Lmk_p63l\n"
".Lmk_p63k:\n"
"v_mfma_f32_16x16x32_bf16 v[80:83], a[128:131], v[120:123], 0\n"
"v_mfma_f32_16x16x32_bf16 v[80:83], a[132:135], v[136:139], v[80:83]\n"
"v_mfma_f32_16x16x32_bf16 v[80:83], a[136:139], v[144:147], v[80:83]\n"
"v_mfma_f32_16x16x32_bf16 v[80:83], a[140:143], v[192:195], v[80:83]\n"
"v_mfma_f32_16x16x32_bf16 v[80:83], a[144:147], v[196:199], v[80:83]\n"
"v_mfma_f32_16x16x32_bf16 v[80:83], a[148:151], v[188:191], v[80:83]\n"
"v_mfma_f32_16x16x32_bf16 v[80:83], a[152:155], v[108:111], v[80:83]\n"
"v_mfma_f32_16x16x32_bf16 v[80:83], a[156:159], v[92:95], v[80:83]\n"
"v_mfma_f32_16x16x32_bf16 v[96:99], a[188:191], v[92:95], 0\n"
"v_mfma_f32_16x16x32_bf16 v[96:99], a[184:187], v[108:111], v[96:99]\n"
"v_mfma_f32_16x16x32_bf16 v[96:99], a[180:183], v[188:191], v[96:99]\n"
"v_mfma_f32_16x16x32_bf16 v[96:99], a[176:179], v[196:199], v[96:99]\n"
"v_mfma_f32_16x16x32_bf16 v[96:99], a[172:175], v[192:195], v[96:99]\n"
"v_mfma_f32_16x16x32_bf16 v[96:99], a[168:171], v[144:147], v[96:99]\n"
"s_nop 7\n"
"s_branch .Lmk_p63r\n"
".Lmk_p64s:\n"
";\n"
"s_waitcnt vmcnt(16)\n"
"s_mov_b32 s1, 0x10000\n"
".Lmk_p64l:\n"
"s_add_u32 s96, s57, 0x440000\n"
"buffer_load_dwordx4 v[148:151], v0, s[4:7], s96 offen sc1\n"
"buffer_load_dwordx4 v[180:183], v0, s[4:7], s96 offen offset:1024 sc1\n"
"buffer_load_dwordx4 v[24:27], v0, s[4:7], s96 offen offset:2048 sc1\n"
"buffer_load_dwordx4 v[32:35], v0, s[4:7], s96 offen offset:3072 sc1\n"
"buffer_load_dwordx4 v[44:47], v1, s[4:7], s96 offen sc1\n"
"buffer_load_dwordx4 v[48:51], v1, s[4:7], s96 offen offset:1024 sc1\n"
"buffer_load_dwordx4 v[28:31], v1, s[4:7], s96 offen offset:2048 sc1\n"
"buffer_load_dwordx4 v[56:59], v1, s[4:7], s96 offen offset:3072 sc1\n"
"s_add_u32 s96, s58, 0x440000\n"
"buffer_load_dwordx4 v[36:39], v0, s[4:7], s96 offen sc1\n"
"buffer_load_dwordx4 v[40:43], v0, s[4:7], s96 offen offset:1024 sc1\n"
"buffer_load_dwordx4 v[52:55], v0, s[4:7], s96 offen offset:2048 sc1\n"
"buffer_load_dwordx4 v[60:63], v0, s[4:7], s96 offen offset:3072 sc1\n"
"buffer_load_dwordx4 v[64:67], v1, s[4:7], s96 offen sc1\n"
"buffer_load_dwordx4 v[68:71], v1, s[4:7], s96 offen offset:1024 sc1\n"
"buffer_load_dwordx4 v[72:75], v1, s[4:7], s96 offen offset:2048 sc1\n"
"buffer_load_dwordx4 v[76:79], v1, s[4:7], s96 offen offset:3072 sc1\n"
"s_add_u32 s96, s59, 0x440000\n"
"buffer_load_dwordx4 v[84:87], v0, s[4:7], s96 offen sc1\n"
"buffer_load_dwordx4 v[88:91], v0, s[4:7], s96 offen offset:1024 sc1\n"
"buffer_load_dwordx4 v[100:103], v0, s[4:7], s96 offen offset:2048 sc1\n"
"buffer_load_dwordx4 v[104:107], v0, s[4:7], s96 offen offset:3072 sc1\n"
"buffer_load_dwordx4 v[80:83], v1, s[4:7], s96 offen sc1\n"
"buffer_load_dwordx4 v[116:119], v1, s[4:7], s96 offen offset:1024 sc1\n"
"buffer_load_dwordx4 v[96:99], v1, s[4:7], s96 offen offset:2048 sc1\n"
"buffer_load_dwordx4 v[92:95], v1, s[4:7], s96 offen offset:3072 sc1\n"
"s_add_u32 s96, s56, 0x400000\n"
"buffer_load_dwordx4 v[120:123], v0, s[4:7], s96 offen sc1\n"
"buffer_load_dwordx4 v[124:127], v0, s[4:7], s96 offen offset:1024 sc1\n"
"s_waitcnt vmcnt(0)\n"
"v_max_u32_e32 v255, v148, v149\n"
"v_max3_u32 v255, v255, v150, v151\n"
"v_max3_u32 v255, v255, v180, v181\n"
"v_max3_u32 v255, v255, v182, v183\n"
"v_max3_u32 v255, v255, v24, v25\n"
"v_max3_u32 v255, v255, v26, v27\n"
"v_max3_u32 v255, v255, v32, v33\n"
"v_max3_u32 v255, v255, v34, v35\n"
"v_max3_u32 v255, v255, v44, v45\n"
"v_max3_u32 v255, v255, v46, v47\n"
"v_max3_u32 v255, v255, v48, v49\n"
"v_max3_u32 v255, v255, v50, v51\n"
"v_max3_u32 v255, v255, v28, v29\n"
"v_max3_u32 v255, v255, v30, v31\n"
"v_max3_u32 v255, v255, v56, v57\n"
"v_max3_u32 v255, v255, v58, v59\n"
"v_cmp_eq_u32_e32 vcc, -1, v255\n"
"s_cbranch_vccz .Lmk_p64k\n"
"s_sub_u32 s1, s1, 1\n"
"s_cbranch_scc0 .Lmk_p64l\n"
".Lmk_p64k:\n"
"v_mfma_f32_16x16x32_bf16 v[108:111], a[128:131], v[148:151], 0\n"
"v_mfma_f32_16x16x32_bf16 v[108:111], a[132:135], v[180:183], v[108:111]\n"
"v_mfma_f32_16x16x32_bf16 v[108:111], a[136:139], v[24:27], v[108:111]\n"
"v_mfma_f32_16x16x32_bf16 v[108:111], a[140:143], v[32:35], v[108:111]\n"
"v_mfma_f32_16x16x32_bf16 v[108:111], a[144:147], v[44:47], v[108:111]\n"
"v_mfma_f32_16x16x32_bf16 v[108:111], a[148:151], v[48:51], v[108:111]\n"
"v_mfma_f32_16x16x32_bf16 v[108:111], a[152:155], v[28:31], v[108:111]\n"
"v_mfma_f32_16x16x32_bf16 v[108:111], a[156:159], v[56:59], v[108:111]\n"
"v_mfma_f32_16x16x32_bf16 v[112:115], a[188:191], v[56:59], 0\n"
"v_mfma_f32_16x16x32_bf16 v[112:115], a[184:187], v[28:31], v[112:115]\n"
"v_mfma_f32_16x16x32_bf16 v[112:115], a[180:183], v[48:51], v[112:115]\n"
"v_mfma_f32_16x16x32_bf16 v[112:115], a[176:179], v[44:47], v[112:115]\n"
"v_mfma_f32_16x16x32_bf16 v[112:115], a[172:175], v[32:35], v[112:115]\n"
"v_mfma_f32_16x16x32_bf16 v[112:115], a[168:171], v[24:27], v[112:115]\n"
"s_nop 7\n"
"s_branch .Lmk_p64r\n"
".Lmk_p65s:\n"
";\n"
"s_waitcnt vmcnt(16)\n"
"s_mov_b32 s1, 0x10000\n"
".Lmk_p65l:\n"
"s_add_u32 s96, s58, 0x440000\n"
"buffer_load_dwordx4 v[36:39], v0, s[4:7], s96 offen sc1\n"
"buffer_load_dwordx4 v[40:43], v0, s[4:7], s96 offen offset:1024 sc1\n"
"buffer_load_dwordx4 v[52:55], v0, s[4:7], s96 offen offset:2048 sc1\n"
"buffer_load_dwordx4 v[60:63], v0, s[4:7], s96 offen offset:3072 sc1\n"
"buffer_load_dwordx4 v[64:67], v1, s[4:7], s96 offen sc1\n"
"buffer_load_dwordx4 v[68:71], v1, s[4:7], s96 offen offset:1024 sc1\n"
"buffer_load_dwordx4 v[72:75], v1, s[4:7], s96 offen offset:2048 sc1\n"
"buffer_load_dwordx4 v[76:79], v1, s[4:7], s96 offen offset:3072 sc1\n"
"s_add_u32 s96, s59, 0x440000\n"
"buffer_load_dwordx4 v[84:87], v0, s[4:7], s96 offen sc1\n"
"buffer_load_dwordx4 v[88:91], v0, s[4:7], s96 offen offset:1024 sc1\n"
"buffer_load_dwordx4 v[100:103], v0, s[4:7], s96 offen offset:2048 sc1\n"
"buffer_load_dwordx4 v[104:107], v0, s[4:7], s96 offen offset:3072 sc1\n"
"buffer_load_dwordx4 v[80:83], v1, s[4:7], s96 offen sc1\n"
"buffer_load_dwordx4 v[116:119], v1, s[4:7], s96 offen offset:1024 sc1\n"
"buffer_load_dwordx4 v[96:99], v1, s[4:7], s96 offen offset:2048 sc1\n"
"buffer_load_dwordx4 v[92:95], v1, s[4:7], s96 offen offset:3072 sc1\n"
"s_add_u32 s96, s56, 0x400000\n"
"buffer_load_dwordx4 v[120:123], v0, s[4:7], s96 offen sc1\n"
"buffer_load_dwordx4 v[124:127], v0, s[4:7], s96 offen offset:1024 sc1\n"
"buffer_load_dwordx4 v[108:111], v0, s[4:7], s96 offen offset:2048 sc1\n"
"buffer_load_dwordx4 v[132:135], v0, s[4:7], s96 offen offset:3072 sc1\n"
"buffer_load_dwordx4 v[112:115], v1, s[4:7], s96 offen sc1\n"
"buffer_load_dwordx4 v[28:31], v1, s[4:7], s96 offen offset:1024 sc1\n"
"buffer_load_dwordx4 v[32:35], v1, s[4:7], s96 offen offset:2048 sc1\n"
"buffer_load_dwordx4 v[24:27], v1, s[4:7], s96 offen offset:3072 sc1\n"
"s_add_u32 s96, s57, 0x400000\n"
"buffer_load_dwordx4 v[48:51], v0, s[4:7], s96 offen sc1\n"
"buffer_load_dwordx4 v[56:59], v0, s[4:7], s96 offen offset:1024 sc1\n"
"buffer_load_dwordx4 v[136:139], v0, s[4:7], s96 offen offset:2048 sc1\n"
"s_waitcnt vmcnt(0)\n"
"v_max_u32_e32 v239, v36, v37\n"
"v_max3_u32 v239, v239, v38, v39\n"
"v_max3_u32 v239, v239, v40, v41\n"
"v_max3_u32 v239, v239, v42, v43\n"
"v_max3_u32 v239, v239, v52, v53\n"
"v_max3_u32 v239, v239, v54, v55\n"
"v_max3_u32 v239, v239, v60, v61\n"
"v_max3_u32 v239, v239, v62, v63\n"
"v_max3_u32 v239, v239, v64, v65\n"
"v_max3_u32 v239, v239, v66, v67\n"
"v_max3_u32 v239, v239, v68, v69\n"
"v_max3_u32 v239, v239, v70, v71\n"
"v_max3_u32 v239, v239, v72, v73\n"
"v_max3_u32 v239, v239, v74, v75\n"
"v_max3_u32 v239, v239, v76, v77\n"
"v_max3_u32 v239, v239, v78, v79\n"
"v_cmp_eq_u32_e32 vcc, -1, v239\n"
"s_cbranch_vccz .Lmk_p65k\n"
"s_sub_u32 s1, s1, 1\n"
"s_cbranch_scc0 .Lmk_p65l\n"
".Lmk_p65k:\n"
"v_mfma_f32_16x16x32_bf16 v[44:47], a[128:131], v[36:39], 0\n"
"v_mfma_f32_16x16x32_bf16 v[44:47], a[132:135], v[40:43], v[44:47]\n"
"v_mfma_f32_16x16x32_bf16 v[44:47], a[136:139], v[52:55], v[44:47]\n"
"v_mfma_f32_16x16x32_bf16 v[44:47], a[140:143], v[60:63], v[44:47]\n"
"v_mfma_f32_16x16x32_bf16 v[44:47], a[144:147], v[64:67], v[44:47]\n"
"v_mfma_f32_16x16x32_bf16 v[44:47], a[148:151], v[68:71], v[44:47]\n"
"v_mfma_f32_16x16x32_bf16 v[44:47], a[152:155], v[72:75], v[44:47]\n"
"v_mfma_f32_16x16x32_bf16 v[44:47], a[156:159], v[76:79], v[44:47]\n"
"v_mfma_f32_16x16x32_bf16 v[128:131], a[188:191], v[76:79], 0\n"
"v_mfma_f32_16x16x32_bf16 v[128:131], a[184:187], v[72:75], v[128:131]\n"
"v_mfma_f32_16x16x32_bf16 v[128:131], a[180:183], v[68:71], v[128:131]\n"
"v_mfma_f32_16x16x32_bf16 v[128:131], a[176:179], v[64:67], v[128:131]\n"
"v_mfma_f32_16x16x32_bf16 v[128:131], a[172:175], v[60:63], v[128:131]\n"
"v_mfma_f32_16x16x32_bf16 v[128:131], a[168:171], v[52:55], v[128:131]\n"
"s_nop 7\n"
"s_branch .Lmk_p65r\n"
".Lmk_p66s:\n"
";\n"
"s_waitcnt vmcnt(16)\n"
"s_mov_b32 s1, 0x10000\n"
".Lmk_p66l:\n"
"s_add_u32 s96, s59, 0x440000\n"
"buffer_load_dwordx4 v[84:87], v0, s[4:7], s96 offen sc1\n"
"buffer_load_dwordx4 v[88:91], v0, s[4:7], s96 offen offset:1024 sc1\n"
"buffer_load_dwordx4 v[100:103], v0, s[4:7], s96 offen offset:2048 sc1\n"
"buffer_load_dwordx4 v[104:107], v0, s[4:7], s96 offen offset:3072 sc1\n"
"buffer_load_dwordx4 v[80:83], v1, s[4:7], s96 offen sc1\n"
"buffer_load_dwordx4 v[116:119], v1, s[4:7], s96 offen offset:1024 sc1\n"
"buffer_load_dwordx4 v[96:99], v1, s[4:7], s96 offen offset:2048 sc1\n"
"buffer_load_dwordx4 v[92:95], v1, s[4:7], s96 offen offset:3072 sc1\n"
"s_add_u32 s96, s56, 0x400000\n"
"buffer_load_dwordx4 v[120:123], v0, s[4:7], s96 offen sc1\n"
"buffer_load_dwordx4 v[124:127], v0, s[4:7], s96 offen offset:1024 sc1\n"
"buffer_load_dwordx4 v[108:111], v0, s[4:7], s96 offen offset:2048 sc1\n"
"buffer_load_dwordx4 v[132:135], v0, s[4:7], s96 offen offset:3072 sc1\n"
"buffer_load_dwordx4 v[112:115], v1, s[4:7], s96 offen sc1\n"
"buffer_load_dwordx4 v[28:31], v1, s[4:7], s96 offen offset:1024 sc1\n"
"buffer_load_dwordx4 v[32:35], v1, s[4:7], s96 offen offset:2048 sc1\n"
"buffer_load_dwordx4 v[24:27], v1, s[4:7], s96 offen offset:3072 sc1\n"
"s_add_u32 s96, s57, 0x400000\n"
"buffer_load_dwordx4 v[48:51], v0, s[4:7], s96 offen sc1\n"
"buffer_load_dwordx4 v[56:59], v0, s[4:7], s96 offen offset:1024 sc1\n"
"buffer_load_dwordx4 v[136:139], v0, s[4:7], s96 offen offset:2048 sc1\n"
"buffer_load_dwordx4 v[44:47], v0, s[4:7], s96 offen offset:3072 sc1\n"
"buffer_load_dwordx4 v[160:163], v1, s[4:7], s96 offen sc1\n"
"buffer_load_dwordx4 v[128:131], v1, s[4:7], s96 offen offset:1024 sc1\n"
"buffer_load_dwordx4 v[72:75], v1, s[4:7], s96 offen offset:2048 sc1\n"
"buffer_load_dwordx4 v[60:63], v1, s[4:7], s96 offen offset:3072 sc1\n"
"s_add_u32 s96, s58, 0x400000\n"
"buffer_load_dwordx4 v[36:39], v0, s[4:7], s96 offen sc1\n"
"buffer_load_dwordx4 v[68:71], v0, s[4:7], s96 offen offset:1024 sc1\n"
"buffer_load_dwordx4 v[76:79], v0, s[4:7], s96 offen offset:2048 sc1\n"
"buffer_load_dwordx4 v[148:151], v0, s[4:7], s96 offen offset:3072 sc1\n"
"s_waitcnt vmcnt(0)\n"
"v_max_u32_e32 v255, v84, v85\n"
"v_max3_u32 v255, v255, v86, v87\n"
"v_max3_u32 v255, v255, v88, v89\n"
"v_max3_u32 v255, v255, v90, v91\n"
"v_max3_u32 v255, v255, v100, v101\n"
"v_max3_u32 v255, v255, v102, v103\n"
"v_max3_u32 v255, v255, v104, v105\n"
"v_max3_u32 v255, v255, v106, v107\n"
"v_max3_u32 v255, v255, v80, v81\n"
"v_max3_u32 v255, v255, v82, v83\n"
"v_max3_u32 v255, v255, v116, v117\n"
"v_max3_u32 v255, v255, v118, v119\n"
"v_max3_u32 v255, v255, v96, v97\n"
"v_max3_u32 v255, v255, v98, v99\n"
"v_max3_u32 v255, v255, v92, v93\n"
"v_max3_u32 v255, v255, v94, v95\n"
"v_cmp_eq_u32_e32 vcc, -1, v255\n"
"s_cbranch_vccz .Lmk_p66k\n"
"s_sub_u32 s1, s1, 1\n"
"s_cbranch_scc0 .Lmk_p66l\n"
".Lmk_p66k:\n"
"v_mfma_f32_16x16x32_bf16 v[64:67], a[128:131], v[84:87], v[140:143]\n"
"v_mfma_f32_16x16x32_bf16 v[64:67], a[132:135], v[88:91], v[64:67]\n"
"v_mfma_f32_16x16x32_bf16 v[64:67], a[136:139], v[100:103], v[64:67]\n"
"v_mfma_f32_16x16x32_bf16 v[64:67], a[140:143], v[104:107], v[64:67]\n"
"v_mfma_f32_16x16x32_bf16 v[64:67], a[144:147], v[80:83], v[64:67]\n"
"v_mfma_f32_16x16x32_bf16 v[64:67], a[148:151], v[116:119], v[64:67]\n"
"v_mfma_f32_16x16x32_bf16 v[64:67], a[152:155], v[96:99], v[64:67]\n"
"v_mfma_f32_16x16x32_bf16 v[64:67], a[156:159], v[92:95], v[64:67]\n"
"v_mfma_f32_16x16x32_bf16 v[52:55], a[188:191], v[92:95], v[152:155]\n"
"v_mfma_f32_16x16x32_bf16 v[52:55], a[184:187], v[96:99], v[52:55]\n"
"v_mfma_f32_16x16x32_bf16 v[52:55], a[180:183], v[116:119], v[52:55]\n"
"v_mfma_f32_16x16x32_bf16 v[52:55], a[176:179], v[80:83], v[52:55]\n"
"v_mfma_f32_16x16x32_bf16 v[52:55], a[172:175], v[104:107], v[52:55]\n"
"v_mfma_f32_16x16x32_bf16 v[52:55], a[168:171], v[100:103], v[52:55]\n"
"s_nop 7\n"
"s_branch .Lmk_p66r\n"
".Lmk_p68s:\n"
";\n"
"s_waitcnt vmcnt(16)\n"
"s_mov_b32 s1, 0x10000\n"
".Lmk_p68l:\n"
"s_add_u32 s95, s56, 0x400000\n"
"buffer_load_dwordx4 v[120:123], v0, s[4:7], s95 offen sc1\n"
"buffer_load_dwordx4 v[124:127], v0, s[4:7], s95 offen offset:1024 sc1\n"
"buffer_load_dwordx4 v[108:111], v0, s[4:7], s95 offen offset:2048 sc1\n"
"buffer_load_dwordx4 v[132:135], v0, s[4:7], s95 offen offset:3072 sc1\n"
"buffer_load_dwordx4 v[112:115], v1, s[4:7], s95 offen sc1\n"
"buffer_load_dwordx4 v[28:31], v1, s[4:7], s95 offen offset:1024 sc1\n"
"buffer_load_dwordx4 v[32:35], v1, s[4:7], s95 offen offset:2048 sc1\n"
"buffer_load_dwordx4 v[24:27], v1, s[4:7], s95 offen offset:3072 sc1\n"
"s_add_u32 s95, s57, 0x400000\n"
"buffer_load_dwordx4 v[48:51], v0, s[4:7], s95 offen sc1\n"
"buffer_load_dwordx4 v[56:59], v0, s[4:7], s95 offen offset:1024 sc1\n"
"buffer_load_dwordx4 v[136:139], v0, s[4:7], s95 offen offset:2048 sc1\n"
"buffer_load_dwordx4 v[44:47], v0, s[4:7], s95 offen offset:3072 sc1\n"
"buffer_load_dwordx4 v[160:163], v1, s[4:7], s95 offen sc1\n"
"buffer_load_dwordx4 v[128:131], v1, s[4:7], s95 offen offset:1024 sc1\n"
"buffer_load_dwordx4 v[72:75], v1, s[4:7], s95 offen offset:2048 sc1\n"
"buffer_load_dwordx4 v[60:63], v1, s[4:7], s95 offen offset:3072 sc1\n"
"s_add_u32 s95, s58, 0x400000\n"
"buffer_load_dwordx4 v[36:39], v0, s[4:7], s95 offen sc1\n"
"buffer_load_dwordx4 v[68:71], v0, s[4:7], s95 offen offset:1024 sc1\n"
"buffer_load_dwordx4 v[76:79], v0, s[4:7], s95 offen offset:2048 sc1\n"
"buffer_load_dwordx4 v[148:151], v0, s[4:7], s95 offen offset:3072 sc1\n"
"buffer_load_dwordx4 v[152:155], v1, s[4:7], s95 offen sc1\n"
"buffer_load_dwordx4 v[172:175], v1, s[4:7], s95 offen offset:1024 sc1\n"
"buffer_load_dwordx4 v[176:179], v1, s[4:7], s95 offen offset:2048 sc1\n"
"buffer_load_dwordx4 v[64:67], v1, s[4:7], s95 offen offset:3072 sc1\n"
"s_add_u32 s95, s59, 0x400000\n"
"buffer_load_dwordx4 v[80:83], v0, s[4:7], s95 offen sc1\n"
"buffer_load_dwordx4 v[84:87], v0, s[4:7], s95 offen offset:1024 sc1\n"
"buffer_load_dwordx4 v[88:91], v0, s[4:7], s95 offen offset:2048 sc1\n"
"buffer_load_dwordx4 v[92:95], v0, s[4:7], s95 offen offset:3072 sc1\n"
"buffer_load_dwordx4 v[96:99], v1, s[4:7], s95 offen sc1\n"
"buffer_load_dwordx4 v[100:103], v1, s[4:7], s95 offen offset:1024 sc1\n"
"buffer_load_dwordx4 v[104:107], v1, s[4:7], s95 offen offset:2048 sc1\n"
"buffer_load_dwordx4 v[116:119], v1, s[4:7], s95 offen offset:3072 sc1\n"
"s_waitcnt vmcnt(0)\n"
"v_max_u32_e32 v255, v120, v121\n"
"v_max3_u32 v255, v255, v122, v123\n"
"v_max3_u32 v255, v255, v124, v125\n"
"v_max3_u32 v255, v255, v126, v127\n"
"v_max3_u32 v255, v255, v108, v109\n"
"v_max3_u32 v255, v255, v110, v111\n"
"v_max3_u32 v255, v255, v132, v133\n"
"v_max3_u32 v255, v255, v134, v135\n"
"v_max3_u32 v255, v255, v112, v113\n"
"v_max3_u32 v255, v255, v114, v115\n"
"v_max3_u32 v255, v255, v28, v29\n"
"v_max3_u32 v255, v255, v30, v31\n"
"v_max3_u32 v255, v255, v32, v33\n"
"v_max3_u32 v255, v255, v34, v35\n"
"v_max3_u32 v255, v255, v24, v25\n"
"v_max3_u32 v255, v255, v26, v27\n"
"v_cmp_eq_u32_e32 vcc, -1, v255\n"
"s_cbranch_vccz .Lmk_p68k\n"
"s_sub_u32 s1, s1, 1\n"
"s_cbranch_scc0 .Lmk_p68l\n"
".Lmk_p68k:\n"
"v_mfma_f32_16x16x32_bf16 v[140:143], a[128:131], v[120:123], 0\n"
"v_mfma_f32_16x16x32_bf16 v[140:143], a[132:135], v[124:127], v[140:143]\n"
"v_mfma_f32_16x16x32_bf16 v[140:143], a[136:139], v[108:111], v[140:143]\n"
"v_mfma_f32_16x16x32_bf16 v[140:143], a[140:143], v[132:135], v[140:143]\n"
"v_mfma_f32_16x16x32_bf16 v[140:143], a[144:147], v[112:115], v[140:143]\n"
"v_mfma_f32_16x16x32_bf16 v[140:143], a[148:151], v[28:31], v[140:143]\n"
"v_mfma_f32_16x16x32_bf16 v[140:143], a[152:155], v[32:35], v[140:143]\n"
"v_mfma_f32_16x16x32_bf16 v[140:143], a[156:159], v[24:27], v[140:143]\n"
"v_mfma_f32_16x16x32_bf16 v[40:43], a[188:191], v[24:27], 0\n"
"v_mfma_f32_16x16x32_bf16 v[40:43], a[184:187], v[32:35], v[40:43]\n"
"v_mfma_f32_16x16x32_bf16 v[40:43], a[180:183], v[28:31], v[40:43]\n"
"v_mfma_f32_16x16x32_bf16 v[40:43], a[176:179], v[112:115], v[40:43]\n"
"v_mfma_f32_16x16x32_bf16 v[40:43], a[172:175], v[132:135], v[40:43]\n"
"v_mfma_f32_16x16x32_bf16 v[40:43], a[168:171], v[108:111], v[40:43]\n"
"s_nop 7\n"
"s_branch .Lmk_p68r\n"
".Lmk_p69s:\n"
";\n"
"s_waitcnt vmcnt(16)\n"
"s_mov_b32 s1, 0x10000\n"
".Lmk_p69l:\n"
"s_add_u32 s97, s57, 0x400000\n"
"buffer_load_dwordx4 v[48:51], v0, s[4:7], s97 offen sc1\n"
"buffer_load_dwordx4 v[56:59], v0, s[4:7], s97 offen offset:1024 sc1\n"
"buffer_load_dwordx4 v[136:139], v0, s[4:7], s97 offen offset:2048 sc1\n"
"buffer_load_dwordx4 v[44:47], v0, s[4:7], s97 offen offset:3072 sc1\n"
"buffer_load_dwordx4 v[160:163], v1, s[4:7], s97 offen sc1\n"
"buffer_load_dwordx4 v[128:131], v1, s[4:7], s97 offen offset:1024 sc1\n"
"buffer_load_dwordx4 v[72:75], v1, s[4:7], s97 offen offset:2048 sc1\n"
"buffer_load_dwordx4 v[60:63], v1, s[4:7], s97 offen offset:3072 sc1\n"
"s_add_u32 s97, s58, 0x400000\n"
"buffer_load_dwordx4 v[36:39], v0, s[4:7], s97 offen sc1\n"
"buffer_load_dwordx4 v[68:71], v0, s[4:7], s97 offen offset:1024 sc1\n"
"buffer_load_dwordx4 v[76:79], v0, s[4:7], s97 offen offset:2048 sc1\n"
"buffer_load_dwordx4 v[148:151], v0, s[4:7], s97 offen offset:3072 sc1\n"
"buffer_load_dwordx4 v[152:155], v1, s[4:7], s97 offen sc1\n"
"buffer_load_dwordx4 v[172:175], v1, s[4:7], s97 offen offset:1024 sc1\n"
"buffer_load_dwordx4 v[176:179], v1, s[4:7], s97 offen offset:2048 sc1\n"
"buffer_load_dwordx4 v[64:67], v1, s[4:7], s97 offen offset:3072 sc1\n"
"s_add_u32 s97, s59, 0x400000\n"
"buffer_load_dwordx4 v[80:83], v0, s[4:7], s97 offen sc1\n"
"buffer_load_dwordx4 v[84:87], v0, s[4:7], s97 offen offset:1024 sc1\n"
"buffer_load_dwordx4 v[88:91], v0, s[4:7], s97 offen offset:2048 sc1\n"
"buffer_load_dwordx4 v[92:95], v0, s[4:7], s97 offen offset:3072 sc1\n"
"buffer_load_dwordx4 v[96:99], v1, s[4:7], s97 offen sc1\n"
"buffer_load_dwordx4 v[100:103], v1, s[4:7], s97 offen offset:1024 sc1\n"
"buffer_load_dwordx4 v[104:107], v1, s[4:7], s97 offen offset:2048 sc1\n"
"buffer_load_dwordx4 v[116:119], v1, s[4:7], s97 offen offset:3072 sc1\n"
"s_waitcnt vmcnt(0)\n"
"v_max_u32_e32 v255, v48, v49\n"
"v_max3_u32 v255, v255, v50, v51\n"
"v_max3_u32 v255, v255, v56, v57\n"
"v_max3_u32 v255, v255, v58, v59\n"
"v_max3_u32 v255, v255, v136, v137\n"
"v_max3_u32 v255, v255, v138, v139\n"
"v_max3_u32 v255, v255, v44, v45\n"
"v_max3_u32 v255, v255, v46, v47\n"
"v_max3_u32 v255, v255, v160, v161\n"
"v_max3_u32 v255, v255, v162, v163\n"
"v_max3_u32 v255, v255, v128, v129\n"
"v_max3_u32 v255, v255, v130, v131\n"
"v_max3_u32 v255, v255, v72, v73\n"
"v_max3_u32 v255, v255, v74, v75\n"
"v_max3_u32 v255, v255, v60, v61\n"
"v_max3_u32 v255, v255, v62, v63\n"
"v_cmp_eq_u32_e32 vcc, -1, v255\n"
"s_cbranch_vccz .Lmk_p69k\n"
"s_sub_u32 s1, s1, 1\n"
"s_cbranch_scc0 .Lmk_p69l\n"
".Lmk_p69k:\n"
"v_mfma_f32_16x16x32_bf16 v[24:27], a[128:131], v[48:51], 0\n"
"v_mfma_f32_16x16x32_bf16 v[24:27], a[132:135], v[56:59], v[24:27]\n"
"v_mfma_f32_16x16x32_bf16 v[24:27], a[136:139], v[136:139], v[24:27]\n"
"v_mfma_f32_16x16x32_bf16 v[24:27], a[140:143], v[44:47], v[24:27]\n"
"v_mfma_f32_16x16x32_bf16 v[24:27], a[144:147], v[160:163], v[24:27]\n"
"v_mfma_f32_16x16x32_bf16 v[24:27], a[148:151], v[128:131], v[24:27]\n"
"v_mfma_f32_16x16x32_bf16 v[24:27], a[152:155], v[72:75], v[24:27]\n"
"v_mfma_f32_16x16x32_bf16 v[24:27], a[156:159], v[60:63], v[24:27]\n"
"v_mfma_f32_16x16x32_bf16 v[28:31], a[188:191], v[60:63], 0\n"
"v_mfma_f32_16x16x32_bf16 v[28:31], a[184:187], v[72:75], v[28:31]\n"
"v_mfma_f32_16x16x32_bf16 v[28:31], a[180:183], v[128:131], v[28:31]\n"
"v_mfma_f32_16x16x32_bf16 v[28:31], a[176:179], v[160:163], v[28:31]\n"
"v_mfma_f32_16x16x32_bf16 v[28:31], a[172:175], v[44:47], v[28:31]\n"
"v_mfma_f32_16x16x32_bf16 v[28:31], a[168:171], v[136:139], v[28:31]\n"
"s_nop 7\n"
"s_branch .Lmk_p69r\n"
".Lmk_p70s:\n"
";\n"
"s_waitcnt vmcnt(16)\n"
"s_mov_b32 s1, 0x10000\n"
".Lmk_p70l:\n"
"s_add_u32 s97, s58, 0x400000\n"
"buffer_load_dwordx4 v[36:39], v0, s[4:7], s97 offen sc1\n"
"buffer_load_dwordx4 v[68:71], v0, s[4:7], s97 offen offset:1024 sc1\n"
"buffer_load_dwordx4 v[76:79], v0, s[4:7], s97 offen offset:2048 sc1\n"
"buffer_load_dwordx4 v[148:151], v0, s[4:7], s97 offen offset:3072 sc1\n"
"buffer_load_dwordx4 v[152:155], v1, s[4:7], s97 offen sc1\n"
"buffer_load_dwordx4 v[172:175], v1, s[4:7], s97 offen offset:1024 sc1\n"
"buffer_load_dwordx4 v[176:179], v1, s[4:7], s97 offen offset:2048 sc1\n"
"buffer_load_dwordx4 v[64:67], v1, s[4:7], s97 offen offset:3072 sc1\n"
"s_add_u32 s97, s59, 0x400000\n"
"buffer_load_dwordx4 v[80:83], v0, s[4:7], s97 offen sc1\n"
"buffer_load_dwordx4 v[84:87], v0, s[4:7], s97 offen offset:1024 sc1\n"
"buffer_load_dwordx4 v[88:91], v0, s[4:7], s97 offen offset:2048 sc1\n"
"buffer_load_dwordx4 v[92:95], v0, s[4:7], s97 offen offset:3072 sc1\n"
"buffer_load_dwordx4 v[96:99], v1, s[4:7], s97 offen sc1\n"
"buffer_load_dwordx4 v[100:103], v1, s[4:7], s97 offen offset:1024 sc1\n"
"buffer_load_dwordx4 v[104:107], v1, s[4:7], s97 offen offset:2048 sc1\n"
"buffer_load_dwordx4 v[116:119], v1, s[4:7], s97 offen offset:3072 sc1\n"
"s_waitcnt vmcnt(0)\n"
"v_max_u32_e32 v239, v36, v37\n"
"v_max3_u32 v239, v239, v38, v39\n"
"v_max3_u32 v239, v239, v68, v69\n"
"v_max3_u32 v239, v239, v70, v71\n"
"v_max3_u32 v239, v239, v76, v77\n"
"v_max3_u32 v239, v239, v78, v79\n"
"v_max3_u32 v239, v239, v148, v149\n"
"v_max3_u32 v239, v239, v150, v151\n"
"v_max3_u32 v239, v239, v152, v153\n"
"v_max3_u32 v239, v239, v154, v155\n"
"v_max3_u32 v239, v239, v172, v173\n"
"v_max3_u32 v239, v239, v174, v175\n"
"v_max3_u32 v239, v239, v176, v177\n"
"v_max3_u32 v239, v239, v178, v179\n"
"v_max3_u32 v239, v239, v64, v65\n"
"v_max3_u32 v239, v239, v66, v67\n"
"v_cmp_eq_u32_e32 vcc, -1, v239\n"
"s_cbranch_vccz .Lmk_p70k\n"
"s_sub_u32 s1, s1, 1\n"
"s_cbranch_scc0 .Lmk_p70l\n"
".Lmk_p70k:\n"
"v_mfma_f32_16x16x32_bf16 v[24:27], a[128:131], v[36:39], 0\n"
"v_mfma_f32_16x16x32_bf16 v[24:27], a[132:135], v[68:71], v[24:27]\n"
"v_mfma_f32_16x16x32_bf16 v[24:27], a[136:139], v[76:79], v[24:27]\n"
"v_mfma_f32_16x16x32_bf16 v[24:27], a[140:143], v[148:151], v[24:27]\n"
"v_mfma_f32_16x16x32_bf16 v[24:27], a[144:147], v[152:155], v[24:27]\n"
"v_mfma_f32_16x16x32_bf16 v[24:27], a[148:151], v[172:175], v[24:27]\n"
"v_mfma_f32_16x16x32_bf16 v[24:27], a[152:155], v[176:179], v[24:27]\n"
"v_mfma_f32_16x16x32_bf16 v[24:27], a[156:159], v[64:67], v[24:27]\n"
"v_mfma_f32_16x16x32_bf16 v[28:31], a[188:191], v[64:67], 0\n"
"v_mfma_f32_16x16x32_bf16 v[28:31], a[184:187], v[176:179], v[28:31]\n"
"v_mfma_f32_16x16x32_bf16 v[28:31], a[180:183], v[172:175], v[28:31]\n"
"v_mfma_f32_16x16x32_bf16 v[28:31], a[176:179], v[152:155], v[28:31]\n"
"v_mfma_f32_16x16x32_bf16 v[28:31], a[172:175], v[148:151], v[28:31]\n"
"v_mfma_f32_16x16x32_bf16 v[28:31], a[168:171], v[76:79], v[28:31]\n"
"s_nop 7\n"
"s_branch .Lmk_p70r\n"
".Lmk_p71s:\n"
";\n"
"s_waitcnt vmcnt(16)\n"
"s_mov_b32 s1, 0x10000\n"
".Lmk_p71l:\n"
"s_add_u32 s97, s59, 0x400000\n"
"buffer_load_dwordx4 v[80:83], v0, s[4:7], s97 offen sc1\n"
"buffer_load_dwordx4 v[84:87], v0, s[4:7], s97 offen offset:1024 sc1\n"
"buffer_load_dwordx4 v[88:91], v0, s[4:7], s97 offen offset:2048 sc1\n"
"buffer_load_dwordx4 v[92:95], v0, s[4:7], s97 offen offset:3072 sc1\n"
"buffer_load_dwordx4 v[96:99], v1, s[4:7], s97 offen sc1\n"
"buffer_load_dwordx4 v[100:103], v1, s[4:7], s97 offen offset:1024 sc1\n"
"buffer_load_dwordx4 v[104:107], v1, s[4:7], s97 offen offset:2048 sc1\n"
"buffer_load_dwordx4 v[116:119], v1, s[4:7], s97 offen offset:3072 sc1\n"
"s_waitcnt vmcnt(0)\n"
"v_max_u32_e32 v255, v80, v81\n"
"v_max3_u32 v255, v255, v82, v83\n"
"v_max3_u32 v255, v255, v84, v85\n"
"v_max3_u32 v255, v255, v86, v87\n"
"v_max3_u32 v255, v255, v88, v89\n"
"v_max3_u32 v255, v255, v90, v91\n"
"v_max3_u32 v255, v255, v92, v93\n"
"v_max3_u32 v255, v255, v94, v95\n"
"v_max3_u32 v255, v255, v96, v97\n"
"v_max3_u32 v255, v255, v98, v99\n"
"v_max3_u32 v255, v255, v100, v101\n"
"v_max3_u32 v255, v255, v102, v103\n"
"v_max3_u32 v255, v255, v104, v105\n"
"v_max3_u32 v255, v255, v106, v107\n"
"v_max3_u32 v255, v255, v116, v117\n"
"v_max3_u32 v255, v255, v118, v119\n"
"v_cmp_eq_u32_e32 vcc, -1, v255\n"
"s_cbranch_vccz .Lmk_p71k\n"
"s_sub_u32 s1, s1, 1\n"
"s_cbranch_scc0 .Lmk_p71l\n"
".Lmk_p71k:\n"
"v_mfma_f32_16x16x32_bf16 v[76:79], a[128:131], v[80:83], v[32:35]\n"
"v_mfma_f32_16x16x32_bf16 v[76:79], a[132:135], v[84:87], v[76:79]\n"
"v_mfma_f32_16x16x32_bf16 v[76:79], a[136:139], v[88:91], v[76:79]\n"
"v_mfma_f32_16x16x32_bf16 v[76:79], a[140:143], v[92:95], v[76:79]\n"
"v_mfma_f32_16x16x32_bf16 v[76:79], a[144:147], v[96:99], v[76:79]\n"
"v_mfma_f32_16x16x32_bf16 v[76:79], a[148:151], v[100:103], v[76:79]\n"
"v_mfma_f32_16x16x32_bf16 v[76:79], a[152:155], v[104:107], v[76:79]\n"
"v_mfma_f32_16x16x32_bf16 v[76:79], a[156:159], v[116:119], v[76:79]\n"
"v_mfma_f32_16x16x32_bf16 v[68:71], a[188:191], v[116:119], v[48:51]\n"
"v_mfma_f32_16x16x32_bf16 v[68:71], a[184:187], v[104:107], v[68:71]\n"
"v_mfma_f32_16x16x32_bf16 v[68:71], a[180:183], v[100:103], v[68:71]\n"
"v_mfma_f32_16x16x32_bf16 v[68:71], a[176:179], v[96:99], v[68:71]\n"
"v_mfma_f32_16x16x32_bf16 v[68:71], a[172:175], v[92:95], v[68:71]\n"
"v_mfma_f32_16x16x32_bf16 v[68:71], a[168:171], v[88:91], v[68:71]\n"
"s_nop 7\n"
"s_branch .Lmk_p71r\n"
".Lmk_p76s:\n"
";\n"
"s_waitcnt vmcnt(16)\n"
"s_mov_b32 s1, 0x10000\n"
".Lmk_p76l:\n"
"s_add_u32 s96, s52, 0x40000\n"
"buffer_load_dwordx4 v[84:87], v0, s[4:7], s96 offen sc1\n"
"buffer_load_dwordx4 v[92:95], v0, s[4:7], s96 offen offset:1024 sc1\n"
"buffer_load_dwordx4 v[96:99], v0, s[4:7], s96 offen offset:2048 sc1\n"
"buffer_load_dwordx4 v[104:107], v0, s[4:7], s96 offen offset:3072 sc1\n"
"buffer_load_dwordx4 v[112:115], v1, s[4:7], s96 offen sc1\n"
"buffer_load_dwordx4 v[116:119], v1, s[4:7], s96 offen offset:1024 sc1\n"
"buffer_load_dwordx4 v[120:123], v1, s[4:7], s96 offen offset:2048 sc1\n"
"buffer_load_dwordx4 v[136:139], v1, s[4:7], s96 offen offset:3072 sc1\n"
"s_add_u32 s96, s53, 0x40000\n"
"buffer_load_dwordx4 v[140:143], v0, s[4:7], s96 offen sc1\n"
"buffer_load_dwordx4 v[144:147], v0, s[4:7], s96 offen offset:1024 sc1\n"
"buffer_load_dwordx4 v[148:151], v0, s[4:7], s96 offen offset:2048 sc1\n"
"buffer_load_dwordx4 v[152:155], v0, s[4:7], s96 offen offset:3072 sc1\n"
"buffer_load_dwordx4 v[156:159], v1, s[4:7], s96 offen sc1\n"
"buffer_load_dwordx4 v[160:163], v1, s[4:7], s96 offen offset:1024 sc1\n"
"buffer_load_dwordx4 v[164:167], v1, s[4:7], s96 offen offset:2048 sc1\n"
"buffer_load_dwordx4 v[168:171], v1, s[4:7], s96 offen offset:3072 sc1\n"
"s_add_u32 s96, s54, 0x40000\n"
"buffer_load_dwordx4 v[176:179], v0, s[4:7], s96 offen sc1\n"
"buffer_load_dwordx4 v[40:43], v0, s[4:7], s96 offen offset:1024 sc1\n"
"s_waitcnt vmcnt(0)\n"
"v_max_u32_e32 v252, v84, v85\n"
"v_max3_u32 v252, v252, v86, v87\n"
"v_max3_u32 v252, v252, v92, v93\n"
"v_max3_u32 v252, v252, v94, v95\n"
"v_max3_u32 v252, v252, v96, v97\n"
"v_max3_u32 v252, v252, v98, v99\n"
"v_max3_u32 v252, v252, v104, v105\n"
"v_max3_u32 v252, v252, v106, v107\n"
"v_max3_u32 v252, v252, v112, v113\n"
"v_max3_u32 v252, v252, v114, v115\n"
"v_max3_u32 v252, v252, v116, v117\n"
"v_max3_u32 v252, v252, v118, v119\n"
"v_max3_u32 v252, v252, v120, v121\n"
"v_max3_u32 v252, v252, v122, v123\n"
"v_max3_u32 v252, v252, v136, v137\n"
"v_max3_u32 v252, v252, v138, v139\n"
"v_cmp_eq_u32_e32 vcc, -1, v252\n"
"s_cbranch_vccz .Lmk_p76k\n"
"s_sub_u32 s1, s1, 1\n"
"s_cbranch_scc0 .Lmk_p76l\n"
".Lmk_p76k:\n"
"v_mfma_f32_16x16x32_bf16 v[172:175], a[0:3], v[84:87], 0\n"
"v_mfma_f32_16x16x32_bf16 v[172:175], a[4:7], v[92:95], v[172:175]\n"
"v_mfma_f32_16x16x32_bf16 v[172:175], a[8:11], v[96:99], v[172:175]\n"
"v_mfma_f32_16x16x32_bf16 v[172:175], a[12:15], v[104:107], v[172:175]\n"
"v_mfma_f32_16x16x32_bf16 v[172:175], a[16:19], v[112:115], v[172:175]\n"
"v_mfma_f32_16x16x32_bf16 v[172:175], a[20:23], v[116:119], v[172:175]\n"
"v_mfma_f32_16x16x32_bf16 v[172:175], a[24:27], v[120:123], v[172:175]\n"
"v_mfma_f32_16x16x32_bf16 v[172:175], a[28:31], v[136:139], v[172:175]\n"
"v_mfma_f32_16x16x32_bf16 v[180:183], a[60:63], v[136:139], 0\n"
"v_mfma_f32_16x16x32_bf16 v[180:183], a[56:59], v[120:123], v[180:183]\n"
"v_mfma_f32_16x16x32_bf16 v[180:183], a[52:55], v[116:119], v[180:183]\n"
"v_mfma_f32_16x16x32_bf16 v[180:183], a[48:51], v[112:115], v[180:183]\n"
"v_mfma_f32_16x16x32_bf16 v[180:183], a[44:47], v[104:107], v[180:183]\n"
"v_mfma_f32_16x16x32_bf16 v[180:183], a[40:43], v[96:99], v[180:183]\n"
"s_nop 7\n"
"s_branch .Lmk_p76r\n"
".Lmk_p77s:\n"
";\n"
"s_waitcnt vmcnt(16)\n"
"s_mov_b32 s1, 0x10000\n"
".Lmk_p77l:\n"
"s_add_u32 s97, s53, 0x40000\n"
"buffer_load_dwordx4 v[140:143], v0, s[4:7], s97 offen sc1\n"
"buffer_load_dwordx4 v[144:147], v0, s[4:7], s97 offen offset:1024 sc1\n"
"buffer_load_dwordx4 v[148:151], v0, s[4:7], s97 offen offset:2048 sc1\n"
"buffer_load_dwordx4 v[152:155], v0, s[4:7], s97 offen offset:3072 sc1\n"
"buffer_load_dwordx4 v[156:159], v1, s[4:7], s97 offen sc1\n"
"buffer_load_dwordx4 v[160:163], v1, s[4:7], s97 offen offset:1024 sc1\n"
"buffer_load_dwordx4 v[164:167], v1, s[4:7], s97 offen offset:2048 sc1\n"
"buffer_load_dwordx4 v[168:171], v1, s[4:7], s97 offen offset:3072 sc1\n"
"s_add_u32 s97, s54, 0x40000\n"
"buffer_load_dwordx4 v[176:179], v0, s[4:7], s97 offen sc1\n"
"buffer_load_dwordx4 v[40:43], v0, s[4:7], s97 offen offset:1024 sc1\n"
"buffer_load_dwordx4 v[124:127], v0, s[4:7], s97 offen offset:2048 sc1\n"
"buffer_load_dwordx4 v[184:187], v0, s[4:7], s97 offen offset:3072 sc1\n"
"buffer_load_dwordx4 v[32:35], v1, s[4:7], s97 offen sc1\n"
"buffer_load_dwordx4 v[192:195], v1, s[4:7], s97 offen offset:1024 sc1\n"
"buffer_load_dwordx4 v[196:199], v1, s[4:7], s97 offen offset:2048 sc1\n"
"buffer_load_dwordx4 v[200:203], v1, s[4:7], s97 offen offset:3072 sc1\n"
"s_add_u32 s97, s55, 0x40000\n"
"buffer_load_dwordx4 v[84:87], v0, s[4:7], s97 offen sc1\n"
"buffer_load_dwordx4 v[92:95], v0, s[4:7], s97 offen offset:1024 sc1\n"
"s_waitcnt vmcnt(0)\n"
"v_max_u32_e32 v247, v140, v141\n"
"v_max3_u32 v247, v247, v142, v143\n"
"v_max3_u32 v247, v247, v144, v145\n"
"v_max3_u32 v247, v247, v146, v147\n"
"v_max3_u32 v247, v247, v148, v149\n"
"v_max3_u32 v247, v247, v150, v151\n"
"v_max3_u32 v247, v247, v152, v153\n"
"v_max3_u32 v247, v247, v154, v155\n"
"v_max3_u32 v247, v247, v156, v157\n"
"v_max3_u32 v247, v247, v158, v159\n"
"v_max3_u32 v247, v247, v160, v161\n"
"v_max3_u32 v247, v247, v162, v163\n"
"v_max3_u32 v247, v247, v164, v165\n"
"v_max3_u32 v247, v247, v166, v167\n"
"v_max3_u32 v247, v247, v168, v169\n"
"v_max3_u32 v247, v247, v170, v171\n"
"v_cmp_eq_u32_e32 vcc, -1, v247\n"
"s_cbranch_vccz .Lmk_p77k\n"
"s_sub_u32 s1, s1, 1\n"
"s_cbranch_scc0 .Lmk_p77l\n"
".Lmk_p77k:\n"
"v_mfma_f32_16x16x32_bf16 v[68:71], a[0:3], v[140:143], 0\n"
"v_mfma_f32_16x16x32_bf16 v[68:71], a[4:7], v[144:147], v[68:71]\n"
"v_mfma_f32_16x16x32_bf16 v[68:71], a[8:11], v[148:151], v[68:71]\n"
"v_mfma_f32_16x16x32_bf16 v[68:71], a[12:15], v[152:155], v[68:71]\n"
"v_mfma_f32_16x16x32_bf16 v[68:71], a[16:19], v[156:159], v[68:71]\n"
"v_mfma_f32_16x16x32_bf16 v[68:71], a[20:23], v[160:163], v[68:71]\n"
"v_mfma_f32_16x16x32_bf16 v[68:71], a[24:27], v[164:167], v[68:71]\n"
"v_mfma_f32_16x16x32_bf16 v[68:71], a[28:31], v[168:171], v[68:71]\n"
"v_mfma_f32_16x16x32_bf16 v[96:99], a[60:63], v[168:171], 0\n"
"v_mfma_f32_16x16x32_bf16 v[96:99], a[56:59], v[164:167], v[96:99]\n"
"v_mfma_f32_16x16x32_bf16 v[96:99], a[52:55], v[160:163], v[96:99]\n"
"v_mfma_f32_16x16x32_bf16 v[96:99], a[48:51], v[156:159], v[96:99]\n"
"v_mfma_f32_16x16x32_bf16 v[96:99], a[44:47], v[152:155], v[96:99]\n"
"v_mfma_f32_16x16x32_bf16 v[96:99], a[40:43], v[148:151], v[96:99]\n"
"s_nop 7\n"
"s_branch .Lmk_p77r\n"
".Lmk_m80s:\n"
";\n"
"s_waitcnt vmcnt(16)\n"
"s_mov_b32 s1, 0x10000\n"
".Lmk_m80l:\n"
"s_add_u32 s98, s62, 0x4000\n"
"buffer_load_dword v255, v10, s[4:7], s98 offen sc1\n"
"buffer_load_dword v254, v10, s[4:7], s98 offen offset:256 sc1\n"
"buffer_load_dword v253, v10, s[4:7], s98 offen offset:512 sc1\n"
"buffer_load_dword v252, v10, s[4:7], s98 offen offset:768 sc1\n"
"buffer_load_dword v251, v10, s[4:7], s98 offen offset:1024 sc1\n"
"buffer_load_dword v250, v10, s[4:7], s98 offen offset:1280 sc1\n"
"buffer_load_dword v249, v10, s[4:7], s98 offen offset:1536 sc1\n"
"buffer_load_dword v248, v10, s[4:7], s98 offen offset:1792 sc1\n"
"s_waitcnt vmcnt(0)\n"
"v_max_u32_e32 v247, v255, v254\n"
"v_max3_u32 v247, v247, v253, v252\n"
"v_max3_u32 v247, v247, v251, v250\n"
"v_max3_u32 v247, v247, v249, v248\n"
"v_cmp_eq_u32_e32 vcc, -1, v247\n"
"s_cbranch_vccz .Lmk_m80k\n"
"s_sub_u32 s1, s1, 1\n"
"s_cbranch_scc0 .Lmk_m80l\n"
".Lmk_m80k:\n"
"v_add_f32_e32 v255, v255, v251\n"
"v_add_f32_e32 v254, v254, v250\n"
"v_add_f32_e32 v253, v253, v249\n"
"v_add_f32_e32 v252, v252, v248\n"
"v_add_f32_e32 v255, v255, v253\n"
"v_add_f32_e32 v254, v254, v252\n"
"v_add_f32_e32 v255, v255, v254\n"
"s_nop 1\n"
"s_branch .Lmk_m80r\n"
".Lmk_p78s:\n"
";\n"
"s_waitcnt vmcnt(16)\n"
"s_mov_b32 s1, 0x10000\n"
".Lmk_p78l:\n"
"s_add_u32 s97, s54, 0x40000\n"
"buffer_load_dwordx4 v[176:179], v0, s[4:7], s97 offen sc1\n"
"buffer_load_dwordx4 v[40:43], v0, s[4:7], s97 offen offset:1024 sc1\n"
"buffer_load_dwordx4 v[124:127], v0, s[4:7], s97 offen offset:2048 sc1\n"
"buffer_load_dwordx4 v[184:187], v0, s[4:7], s97 offen offset:3072 sc1\n"
"buffer_load_dwordx4 v[32:35], v1, s[4:7], s97 offen sc1\n"
"buffer_load_dwordx4 v[192:195], v1, s[4:7], s97 offen offset:1024 sc1\n"
"buffer_load_dwordx4 v[196:199], v1, s[4:7], s97 offen offset:2048 sc1\n"
"buffer_load_dwordx4 v[200:203], v1, s[4:7], s97 offen offset:3072 sc1\n"
"s_add_u32 s97, s55, 0x40000\n"
"buffer_load_dwordx4 v[84:87], v0, s[4:7], s97 offen sc1\n"
"buffer_load_dwordx4 v[92:95], v0, s[4:7], s97 offen offset:1024 sc1\n"
"buffer_load_dwordx4 v[104:107], v0, s[4:7], s97 offen offset:2048 sc1\n"
"buffer_load_dwordx4 v[112:115], v0, s[4:7], s97 offen offset:3072 sc1\n"
"buffer_load_dwordx4 v[96:99], v1, s[4:7], s97 offen sc1\n"
"buffer_load_dwordx4 v[120:123], v1, s[4:7], s97 offen offset:1024 sc1\n"
"buffer_load_dwordx4 v[136:139], v1, s[4:7], s97 offen offset:2048 sc1\n"
"buffer_load_dwordx4 v[172:175], v1, s[4:7], s97 offen offset:3072 sc1\n"
"s_add_u32 s97, s52, 0x48000\n"
"buffer_load_dwordx4 v[140:143], v0, s[4:7], s97 offen sc1\n"
"s_waitcnt vmcnt(0)\n"
"v_max_u32_e32 v252, v176, v177\n"
"v_max3_u32 v252, v252, v178, v179\n"
"v_max3_u32 v252, v252, v40, v41\n"
"v_max3_u32 v252, v252, v42, v43\n"
"v_max3_u32 v252, v252, v124, v125\n"
"v_max3_u32 v252, v252, v126, v127\n"
"v_max3_u32 v252, v252, v184, v185\n"
"v_max3_u32 v252, v252, v186, v187\n"
"v_max3_u32 v252, v252, v32, v33\n"
"v_max3_u32 v252, v252, v34, v35\n"
"v_max3_u32 v252, v252, v192, v193\n"
"v_max3_u32 v252, v252, v194, v195\n"
"v_max3_u32 v252, v252, v196, v197\n"
"v_max3_u32 v252, v252, v198, v199\n"
"v_max3_u32 v252, v252, v200, v201\n"
"v_max3_u32 v252, v252, v202, v203\n"
"v_cmp_eq_u32_e32 vcc, -1, v252\n"
"s_cbranch_vccz .Lmk_p78k\n"
"s_sub_u32 s1, s1, 1\n"
"s_cbranch_scc0 .Lmk_p78l\n"
".Lmk_p78k:\n"
"v_mfma_f32_16x16x32_bf16 v[68:71], a[0:3], v[176:179], 0\n"
"v_mfma_f32_16x16x32_bf16 v[68:71], a[4:7], v[40:43], v[68:71]\n"
"v_mfma_f32_16x16x32_bf16 v[68:71], a[8:11], v[124:127], v[68:71]\n"
"v_mfma_f32_16x16x32_bf16 v[68:71], a[12:15], v[184:187], v[68:71]\n"
"v_mfma_f32_16x16x32_bf16 v[68:71], a[16:19], v[32:35], v[68:71]\n"
"v_mfma_f32_16x16x32_bf16 v[68:71], a[20:23], v[192:195], v[68:71]\n"
"v_mfma_f32_16x16x32_bf16 v[68:71], a[24:27], v[196:199], v[68:71]\n"
"v_mfma_f32_16x16x32_bf16 v[68:71], a[28:31], v[200:203], v[68:71]\n"
"v_mfma_f32_16x16x32_bf16 v[116:119], a[60:63], v[200:203], 0\n"
"v_mfma_f32_16x16x32_bf16 v[116:119], a[56:59], v[196:199], v[116:119]\n"
"v_mfma_f32_16x16x32_bf16 v[116:119], a[52:55], v[192:195], v[116:119]\n"
"v_mfma_f32_16x16x32_bf16 v[116:119], a[48:51], v[32:35], v[116:119]\n"
"v_mfma_f32_16x16x32_bf16 v[116:119], a[44:47], v[184:187], v[116:119]\n"
"v_mfma_f32_16x16x32_bf16 v[116:119], a[40:43], v[124:127], v[116:119]\n"
"s_nop 7\n"
"s_branch .Lmk_p78r\n"
".Lmk_p79s:\n"
";\n"
"s_waitcnt vmcnt(16)\n"
"s_mov_b32 s1, 0x10000\n"
".Lmk_p79l:\n"
"s_add_u32 s97, s55, 0x40000\n"
"buffer_load_dwordx4 v[84:87], v0, s[4:7], s97 offen sc1\n"
"buffer_load_dwordx4 v[92:95], v0, s[4:7], s97 offen offset:1024 sc1\n"
"buffer_load_dwordx4 v[104:107], v0, s[4:7], s97 offen offset:2048 sc1\n"
"buffer_load_dwordx4 v[112:115], v0, s[4:7], s97 offen offset:3072 sc1\n"
"buffer_load_dwordx4 v[96:99], v1, s[4:7], s97 offen sc1\n"
"buffer_load_dwordx4 v[120:123], v1, s[4:7], s97 offen offset:1024 sc1\n"
"buffer_load_dwordx4 v[136:139], v1, s[4:7], s97 offen offset:2048 sc1\n"
"buffer_load_dwordx4 v[172:175], v1, s[4:7], s97 offen offset:3072 sc1\n"
"s_add_u32 s97, s52, 0x48000\n"
"buffer_load_dwordx4 v[140:143], v0, s[4:7], s97 offen sc1\n"
"buffer_load_dwordx4 v[68:71], v0, s[4:7], s97 offen offset:1024 sc1\n"
"buffer_load_dwordx4 v[168:171], v0, s[4:7], s97 offen offset:2048 sc1\n"
"buffer_load_dwordx4 v[152:155], v0, s[4:7], s97 offen offset:3072 sc1\n"
"buffer_load_dwordx4 v[148:151], v1, s[4:7], s97 offen sc1\n"
"buffer_load_dwordx4 v[164:167], v1, s[4:7], s97 offen offset:1024 sc1\n"
"buffer_load_dwordx4 v[180:183], v1, s[4:7], s97 offen offset:2048 sc1\n"
"buffer_load_dwordx4 v[124:127], v1, s[4:7], s97 offen offset:3072 sc1\n"
"s_add_u32 s97, s53, 0x48000\n"
"buffer_load_dwordx4 v[200:203], v0, s[4:7], s97 offen sc1\n"
"buffer_load_dwordx4 v[116:119], v0, s[4:7], s97 offen offset:1024 sc1\n"
"s_waitcnt vmcnt(0)\n"
"v_max_u32_e32 v252, v84, v85\n"
"v_max3_u32 v252, v252, v86, v87\n"
"v_max3_u32 v252, v252, v92, v93\n"
"v_max3_u32 v252, v252, v94, v95\n"
"v_max3_u32 v252, v252, v104, v105\n"
"v_max3_u32 v252, v252, v106, v107\n"
"v_max3_u32 v252, v252, v112, v113\n"
"v_max3_u32 v252, v252, v114, v115\n"
"v_max3_u32 v252, v252, v96, v97\n"
"v_max3_u32 v252, v252, v98, v99\n"
"v_max3_u32 v252, v252, v120, v121\n"
"v_max3_u32 v252, v252, v122, v123\n"
"v_max3_u32 v252, v252, v136, v137\n"
"v_max3_u32 v252, v252, v138, v139\n"
"v_max3_u32 v252, v252, v172, v173\n"
"v_max3_u32 v252, v252, v174, v175\n"
"v_cmp_eq_u32_e32 vcc, -1, v252\n"
"s_cbranch_vccz .Lmk_p79k\n"
"s_sub_u32 s1, s1, 1\n"
"s_cbranch_scc0 .Lmk_p79l\n"
".Lmk_p79k:\n"
"v_mfma_f32_16x16x32_bf16 v[176:179], a[0:3], v[84:87], v[144:147]\n"
"v_mfma_f32_16x16x32_bf16 v[176:179], a[4:7], v[92:95], v[176:179]\n"
"v_mfma_f32_16x16x32_bf16 v[176:179], a[8:11], v[104:107], v[176:179]\n"
"v_mfma_f32_16x16x32_bf16 v[176:179], a[12:15], v[112:115], v[176:179]\n"
"v_mfma_f32_16x16x32_bf16 v[176:179], a[16:19], v[96:99], v[176:179]\n"
"v_mfma_f32_16x16x32_bf16 v[176:179], a[20:23], v[120:123], v[176:179]\n"
"v_mfma_f32_16x16x32_bf16 v[176:179], a[24:27], v[136:139], v[176:179]\n"
"v_mfma_f32_16x16x32_bf16 v[176:179], a[28:31], v[172:175], v[176:179]\n"
"v_mfma_f32_16x16x32_bf16 v[204:207], a[60:63], v[172:175], v[160:163]\n"
"v_mfma_f32_16x16x32_bf16 v[204:207], a[56:59], v[136:139], v[204:207]\n"
"v_mfma_f32_16x16x32_bf16 v[204:207], a[52:55], v[120:123], v[204:207]\n"
"v_mfma_f32_16x16x32_bf16 v[204:207], a[48:51], v[96:99], v[204:207]\n"
"v_mfma_f32_16x16x32_bf16 v[204:207], a[44:47], v[112:115], v[204:207]\n"
"v_mfma_f32_16x16x32_bf16 v[204:207], a[40:43], v[104:107], v[204:207]\n"
"s_nop 7\n"
"s_branch .Lmk_p79r\n"
".Lmk_p81s:\n"
";\n"
"s_waitcnt vmcnt(16)\n"
"s_mov_b32 s1, 0x10000\n"
".Lmk_p81l:\n"
"s_add_u32 s95, s52, 0x48000\n"
"buffer_load_dwordx4 v[140:143], v0, s[4:7], s95 offen sc1\n"
"buffer_load_dwordx4 v[68:71], v0, s[4:7], s95 offen offset:1024 sc1\n"
"buffer_load_dwordx4 v[168:171], v0, s[4:7], s95 offen offset:2048 sc1\n"
"buffer_load_dwordx4 v[152:155], v0, s[4:7], s95 offen offset:3072 sc1\n"
"buffer_load_dwordx4 v[148:151], v1, s[4:7], s95 offen sc1\n"
"buffer_load_dwordx4 v[164:167], v1, s[4:7], s95 offen offset:1024 sc1\n"
"buffer_load_dwordx4 v[180:183], v1, s[4:7], s95 offen offset:2048 sc1\n"
"buffer_load_dwordx4 v[124:127], v1, s[4:7], s95 offen offset:3072 sc1\n"
"s_add_u32 s95, s53, 0x48000\n"
"buffer_load_dwordx4 v[200:203], v0, s[4:7], s95 offen sc1\n"
"buffer_load_dwordx4 v[116:119], v0, s[4:7], s95 offen offset:1024 sc1\n"
"buffer_load_dwordx4 v[144:147], v0, s[4:7], s95 offen offset:2048 sc1\n"
"buffer_load_dwordx4 v[32:35], v0, s[4:7], s95 offen offset:3072 sc1\n"
"buffer_load_dwordx4 v[156:159], v1, s[4:7], s95 offen sc1\n"
"buffer_load_dwordx4 v[184:187], v1, s[4:7], s95 offen offset:1024 sc1\n"
"buffer_load_dwordx4 v[208:211], v1, s[4:7], s95 offen offset:2048 sc1\n"
"buffer_load_dwordx4 v[176:179], v1, s[4:7], s95 offen offset:3072 sc1\n"
"s_add_u32 s95, s54, 0x48000\n"
"buffer_load_dwordx4 v[92:95], v0, s[4:7], s95 offen sc1\n"
"buffer_load_dwordx4 v[96:99], v0, s[4:7], s95 offen offset:1024 sc1\n"
"s_waitcnt vmcnt(0)\n"
"v_max_u32_e32 v252, v140, v141\n"
"v_max3_u32 v252, v252, v142, v143\n"
"v_max3_u32 v252, v252, v68, v69\n"
"v_max3_u32 v252, v252, v70, v71\n"
"v_max3_u32 v252, v252, v168, v169\n"
"v_max3_u32 v252, v252, v170, v171\n"
"v_max3_u32 v252, v252, v152, v153\n"
"v_max3_u32 v252, v252, v154, v155\n"
"v_max3_u32 v252, v252, v148, v149\n"
"v_max3_u32 v252, v252, v150, v151\n"
"v_max3_u32 v252, v252, v164, v165\n"
"v_max3_u32 v252, v252, v166, v167\n"
"v_max3_u32 v252, v252, v180, v181\n"
"v_max3_u32 v252, v252, v182, v183\n"
"v_max3_u32 v252, v252, v124, v125\n"
"v_max3_u32 v252, v252, v126, v127\n"
"v_cmp_eq_u32_e32 vcc, -1, v252\n"
"s_cbranch_vccz .Lmk_p81k\n"
"s_sub_u32 s1, s1, 1\n"
"s_cbranch_scc0 .Lmk_p81l\n"
".Lmk_p81k:\n"
"v_mfma_f32_16x16x32_bf16 v[84:87], a[0:3], v[140:143], 0\n"
"v_mfma_f32_16x16x32_bf16 v[84:87], a[4:7], v[68:71], v[84:87]\n"
"v_mfma_f32_16x16x32_bf16 v[84:87], a[8:11], v[168:171], v[84:87]\n"
"v_mfma_f32_16x16x32_bf16 v[84:87], a[12:15], v[152:155], v[84:87]\n"
"v_mfma_f32_16x16x32_bf16 v[84:87], a[16:19], v[148:151], v[84:87]\n"
"v_mfma_f32_16x16x32_bf16 v[84:87], a[20:23], v[164:167], v[84:87]\n"
"v_mfma_f32_16x16x32_bf16 v[84:87], a[24:27], v[180:183], v[84:87]\n"
"v_mfma_f32_16x16x32_bf16 v[84:87], a[28:31], v[124:127], v[84:87]\n"
"v_mfma_f32_16x16x32_bf16 v[104:107], a[60:63], v[124:127], 0\n"
"v_mfma_f32_16x16x32_bf16 v[104:107], a[56:59], v[180:183], v[104:107]\n"
"v_mfma_f32_16x16x32_bf16 v[104:107], a[52:55], v[164:167], v[104:107]\n"
"v_mfma_f32_16x16x32_bf16 v[104:107], a[48:51], v[148:151], v[104:107]\n"
"v_mfma_f32_16x16x32_bf16 v[104:107], a[44:47], v[152:155], v[104:107]\n"
"v_mfma_f32_16x16x32_bf16 v[104:107], a[40:43], v[168:171], v[104:107]\n"
"s_nop 7\n"
"s_branch .Lmk_p81r\n"
".Lmk_p82s:\n"
";\n"
"s_waitcnt vmcnt(16)\n"
"s_mov_b32 s1, 0x10000\n"
".Lmk_p82l:\n"
"s_add_u32 s95, s53, 0x48000\n"
"buffer_load_dwordx4 v[200:203], v0, s[4:7], s95 offen sc1\n"
"buffer_load_dwordx4 v[116:119], v0, s[4:7], s95 offen offset:1024 sc1\n"
"buffer_load_dwordx4 v[144:147], v0, s[4:7], s95 offen offset:2048 sc1\n"
"buffer_load_dwordx4 v[32:35], v0, s[4:7], s95 offen offset:3072 sc1\n"
"buffer_load_dwordx4 v[156:159], v1, s[4:7], s95 offen sc1\n"
"buffer_load_dwordx4 v[184:187], v1, s[4:7], s95 offen offset:1024 sc1\n"
"buffer_load_dwordx4 v[208:211], v1, s[4:7], s95 offen offset:2048 sc1\n"
"buffer_load_dwordx4 v[176:179], v1, s[4:7], s95 offen offset:3072 sc1\n"
"s_add_u32 s95, s54, 0x48000\n"
"buffer_load_dwordx4 v[92:95], v0, s[4:7], s95 offen sc1\n"
"buffer_load_dwordx4 v[96:99], v0, s[4:7], s95 offen offset:1024 sc1\n"
"buffer_load_dwordx4 v[112:115], v0, s[4:7], s95 offen offset:2048 sc1\n"
"buffer_load_dwordx4 v[120:123], v0, s[4:7], s95 offen offset:3072 sc1\n"
"buffer_load_dwordx4 v[104:107], v1, s[4:7], s95 offen sc1\n"
"buffer_load_dwordx4 v[172:175], v1, s[4:7], s95 offen offset:1024 sc1\n"
"buffer_load_dwordx4 v[84:87], v1, s[4:7], s95 offen offset:2048 sc1\n"
"buffer_load_dwordx4 v[160:163], v1, s[4:7], s95 offen offset:3072 sc1\n"
"s_add_u32 s95, s55, 0x48000\n"
"buffer_load_dwordx4 v[68:71], v0, s[4:7], s95 offen sc1\n"
"buffer_load_dwordx4 v[124:127], v0, s[4:7], s95 offen offset:1024 sc1\n"
"s_waitcnt vmcnt(0)\n"
"v_max_u32_e32 v247, v200, v201\n"
"v_max3_u32 v247, v247, v202, v203\n"
"v_max3_u32 v247, v247, v116, v117\n"
"v_max3_u32 v247, v247, v118, v119\n"
"v_max3_u32 v247, v247, v144, v145\n"
"v_max3_u32 v247, v247, v146, v147\n"
"v_max3_u32 v247, v247, v32, v33\n"
"v_max3_u32 v247, v247, v34, v35\n"
"v_max3_u32 v247, v247, v156, v157\n"
"v_max3_u32 v247, v247, v158, v159\n"
"v_max3_u32 v247, v247, v184, v185\n"
"v_max3_u32 v247, v247, v186, v187\n"
"v_max3_u32 v247, v247, v208, v209\n"
"v_max3_u32 v247, v247, v210, v211\n"
"v_max3_u32 v247, v247, v176, v177\n"
"v_max3_u32 v247, v247, v178, v179\n"
"v_cmp_eq_u32_e32 vcc, -1, v247\n"
"s_cbranch_vccz .Lmk_p82k\n"
"s_sub_u32 s1, s1, 1\n"
"s_cbranch_scc0 .Lmk_p82l\n"
".Lmk_p82k:\n"
"v_mfma_f32_16x16x32_bf16 v[40:43], a[0:3], v[200:203], 0\n"
"v_mfma_f32_16x16x32_bf16 v[40:43], a[4:7], v[116:119], v[40:43]\n"
"v_mfma_f32_16x16x32_bf16 v[40:43], a[8:11], v[144:147], v[40:43]\n"
"v_mfma_f32_16x16x32_bf16 v[40:43], a[12:15], v[32:35], v[40:43]\n"
"v_mfma_f32_16x16x32_bf16 v[40:43], a[16:19], v[156:159], v[40:43]\n"
"v_mfma_f32_16x16x32_bf16 v[40:43], a[20:23], v[184:187], v[40:43]\n"
"v_mfma_f32_16x16x32_bf16 v[40:43], a[24:27], v[208:211], v[40:43]\n"
"v_mfma_f32_16x16x32_bf16 v[40:43], a[28:31], v[176:179], v[40:43]\n"
"v_mfma_f32_16x16x32_bf16 v[136:139], a[60:63], v[176:179], 0\n"
"v_mfma_f32_16x16x32_bf16 v[136:139], a[56:59], v[208:211], v[136:139]\n"
"v_mfma_f32_16x16x32_bf16 v[136:139], a[52:55], v[184:187], v[136:139]\n"
"v_mfma_f32_16x16x32_bf16 v[136:139], a[48:51], v[156:159], v[136:139]\n"
"v_mfma_f32_16x16x32_bf16 v[136:139], a[44:47], v[32:35], v[136:139]\n"
"v_mfma_f32_16x16x32_bf16 v[136:139], a[40:43], v[144:147], v[136:139]\n"
"s_nop 7\n"
"s_branch .Lmk_p82r\n"
".Lmk_m85s:\n"
";\n"
"s_waitcnt vmcnt(16)\n"
"s_mov_b32 s1, 0x10000\n"
".Lmk_m85l:\n"
"s_add_u32 s98, s62, 0x4800\n"
"buffer_load_dword v255, v10, s[4:7], s98 offen sc1\n"
"buffer_load_dword v253, v10, s[4:7], s98 offen offset:256 sc1\n"
"buffer_load_dword v252, v10, s[4:7], s98 offen offset:512 sc1\n"
"buffer_load_dword v251, v10, s[4:7], s98 offen offset:768 sc1\n"
"buffer_load_dword v250, v10, s[4:7], s98 offen offset:1024 sc1\n"
"buffer_load_dword v254, v10, s[4:7], s98 offen offset:1280 sc1\n"
"buffer_load_dword v249, v10, s[4:7], s98 offen offset:1536 sc1\n"
"buffer_load_dword v248, v10, s[4:7], s98 offen offset:1792 sc1\n"
"s_waitcnt vmcnt(0)\n"
"v_max_u32_e32 v245, v255, v253\n"
"v_max3_u32 v245, v245, v252, v251\n"
"v_max3_u32 v245, v245, v250, v254\n"
"v_max3_u32 v245, v245, v249, v248\n"
"v_cmp_eq_u32_e32 vcc, -1, v245\n"
"s_cbranch_vccz .Lmk_m85k\n"
"s_sub_u32 s1, s1, 1\n"
"s_cbranch_scc0 .Lmk_m85l\n"
".Lmk_m85k:\n"
"v_add_f32_e32 v255, v255, v250\n"
"v_add_f32_e32 v253, v253, v254\n"
"v_add_f32_e32 v252, v252, v249\n"
"v_add_f32_e32 v251, v251, v248\n"
"v_add_f32_e32 v255, v255, v252\n"
"v_add_f32_e32 v253, v253, v251\n"
"v_add_f32_e32 v255, v255, v253\n"
"s_nop 1\n"
"s_branch .Lmk_m85r\n"
".Lmk_f72s:\n"
";\n"
"s_mov_b32 s1, 0x10000\n"
".Lmk_f72l:\n"
"buffer_load_dwordx2 v[246:247], v12, s[4:7], s97 offen sc1\n"
"s_waitcnt vmcnt(0)\n"
"v_cmp_ne_u64_e32 vcc, v[246:247], v[18:19]\n"
"s_cbranch_vccz .Lmk_f72r\n"
"s_sub_u32 s1, s1, 1\n"
"s_cbranch_scc0 .Lmk_f72l\n"
"s_branch .Lmk_f72r\n"
".Lmk_p83s:\n"
";\n"
"s_waitcnt vmcnt(16)\n"
"s_mov_b32 s1, 0x10000\n"
".Lmk_p83l:\n"
"s_add_u32 s97, s54, 0x48000\n"
"buffer_load_dwordx4 v[92:95], v0, s[4:7], s97 offen sc1\n"
"buffer_load_dwordx4 v[96:99], v0, s[4:7], s97 offen offset:1024 sc1\n"
"buffer_load_dwordx4 v[112:115], v0, s[4:7], s97 offen offset:2048 sc1\n"
"buffer_load_dwordx4 v[120:123], v0, s[4:7], s97 offen offset:3072 sc1\n"
"buffer_load_dwordx4 v[104:107], v1, s[4:7], s97 offen sc1\n"
"buffer_load_dwordx4 v[172:175], v1, s[4:7], s97 offen offset:1024 sc1\n"
"buffer_load_dwordx4 v[84:87], v1, s[4:7], s97 offen offset:2048 sc1\n"
"buffer_load_dwordx4 v[160:163], v1, s[4:7], s97 offen offset:3072 sc1\n"
"s_add_u32 s97, s55, 0x48000\n"
"buffer_load_dwordx4 v[68:71], v0, s[4:7], s97 offen sc1\n"
"buffer_load_dwordx4 v[124:127], v0, s[4:7], s97 offen offset:1024 sc1\n"
"buffer_load_dwordx4 v[140:143], v0, s[4:7], s97 offen offset:2048 sc1\n"
"buffer_load_dwordx4 v[148:151], v0, s[4:7], s97 offen offset:3072 sc1\n"
"buffer_load_dwordx4 v[136:139], v1, s[4:7], s97 offen sc1\n"
"buffer_load_dwordx4 v[164:167], v1, s[4:7], s97 offen offset:1024 sc1\n"
"buffer_load_dwordx4 v[168:171], v1, s[4:7], s97 offen offset:2048 sc1\n"
"buffer_load_dwordx4 v[180:183], v1, s[4:7], s97 offen offset:3072 sc1\n"
"s_waitcnt vmcnt(0)\n"
"v_max_u32_e32 v255, v92, v93\n"
"v_max3_u32 v255, v255, v94, v95\n"
"v_max3_u32 v255, v255, v96, v97\n"
"v_max3_u32 v255, v255, v98, v99\n"
"v_max3_u32 v255, v255, v112, v113\n"
"v_max3_u32 v255, v255, v114, v115\n"
"v_max3_u32 v255, v255, v120, v121\n"
"v_max3_u32 v255, v255, v122, v123\n"
"v_max3_u32 v255, v255, v104, v105\n"
"v_max3_u32 v255, v255, v106, v107\n"
"v_max3_u32 v255, v255, v172, v173\n"
"v_max3_u32 v255, v255, v174, v175\n"
"v_max3_u32 v255, v255, v84, v85\n"
"v_max3_u32 v255, v255, v86, v87\n"
"v_max3_u32 v255, v255, v160, v161\n"
"v_max3_u32 v255, v255, v162, v163\n"
"v_cmp_eq_u32_e32 vcc, -1, v255\n"
"s_cbranch_vccz .Lmk_p83k\n"
"s_sub_u32 s1, s1, 1\n"
"s_cbranch_scc0 .Lmk_p83l\n"
".Lmk_p83k:\n"
"v_mfma_f32_16x16x32_bf16 v[32:35], a[0:3], v[92:95], 0\n"
"v_mfma_f32_16x16x32_bf16 v[32:35], a[4:7], v[96:99], v[32:35]\n"
"v_mfma_f32_16x16x32_bf16 v[32:35], a[8:11], v[112:115], v[32:35]\n"
"v_mfma_f32_16x16x32_bf16 v[32:35], a[12:15], v[120:123], v[32:35]\n"
"v_mfma_f32_16x16x32_bf16 v[32:35], a[16:19], v[104:107], v[32:35]\n"
"v_mfma_f32_16x16x32_bf16 v[32:35], a[20:23], v[172:175], v[32:35]\n"
"v_mfma_f32_16x16x32_bf16 v[32:35], a[24:27], v[84:87], v[32:35]\n"
"v_mfma_f32_16x16x32_bf16 v[32:35], a[28:31], v[160:163], v[32:35]\n"
"v_mfma_f32_16x16x32_bf16 v[40:43], a[60:63], v[160:163], 0\n"
"v_mfma_f32_16x16x32_bf16 v[40:43], a[56:59], v[84:87], v[40:43]\n"
"v_mfma_f32_16x16x32_bf16 v[40:43], a[52:55], v[172:175], v[40:43]\n"
"v_mfma_f32_16x16x32_bf16 v[40:43], a[48:51], v[104:107], v[40:43]\n"
"v_mfma_f32_16x16x32_bf16 v[40:43], a[44:47], v[120:123], v[40:43]\n"
"v_mfma_f32_16x16x32_bf16 v[40:43], a[40:43], v[112:115], v[40:43]\n"
"s_nop 7\n"
"s_branch .Lmk_p83r\n"
".Lmk_p84s:\n"
";\n"
"s_waitcnt vmcnt(16)\n"
"s_mov_b32 s1, 0x10000\n"
".Lmk_p84l:\n"
"s_add_u32 s97, s55, 0x48000\n"
"buffer_load_dwordx4 v[68:71], v0, s[4:7], s97 offen sc1\n"
"buffer_load_dwordx4 v[124:127], v0, s[4:7], s97 offen offset:1024 sc1\n"
"buffer_load_dwordx4 v[140:143], v0, s[4:7], s97 offen offset:2048 sc1\n"
"buffer_load_dwordx4 v[148:151], v0, s[4:7], s97 offen offset:3072 sc1\n"
"buffer_load_dwordx4 v[136:139], v1, s[4:7], s97 offen sc1\n"
"buffer_load_dwordx4 v[164:167], v1, s[4:7], s97 offen offset:1024 sc1\n"
"buffer_load_dwordx4 v[168:171], v1, s[4:7], s97 offen offset:2048 sc1\n"
"buffer_load_dwordx4 v[180:183], v1, s[4:7], s97 offen offset:3072 sc1\n"
"s_waitcnt vmcnt(0)\n"
"v_max_u32_e32 v252, v68, v69\n"
"v_max3_u32 v252, v252, v70, v71\n"
"v_max3_u32 v252, v252, v124, v125\n"
"v_max3_u32 v252, v252, v126, v127\n"
"v_max3_u32 v252, v252, v140, v141\n"
"v_max3_u32 v252, v252, v142, v143\n"
"v_max3_u32 v252, v252, v148, v149\n"
"v_max3_u32 v252, v252, v150, v151\n"
"v_max3_u32 v252, v252, v136, v137\n"
"v_max3_u32 v252, v252, v138, v139\n"
"v_max3_u32 v252, v252, v164, v165\n"
"v_max3_u32 v252, v252, v166, v167\n"
"v_max3_u32 v252, v252, v168, v169\n"
"v_max3_u32 v252, v252, v170, v171\n"
"v_max3_u32 v252, v252, v180, v181\n"
"v_max3_u32 v252, v252, v182, v183\n"
"v_cmp_eq_u32_e32 vcc, -1, v252\n"
"s_cbranch_vccz .Lmk_p84k\n"
"s_sub_u32 s1, s1, 1\n"
"s_cbranch_scc0 .Lmk_p84l\n"
".Lmk_p84k:\n"
"v_mfma_f32_16x16x32_bf16 v[112:115], a[0:3], v[68:71], v[116:119]\n"
"v_mfma_f32_16x16x32_bf16 v[112:115], a[4:7], v[124:127], v[112:115]\n"
"v_mfma_f32_16x16x32_bf16 v[112:115], a[8:11], v[140:143], v[112:115]\n"
"v_mfma_f32_16x16x32_bf16 v[112:115], a[12:15], v[148:151], v[112:115]\n"
"v_mfma_f32_16x16x32_bf16 v[112:115], a[16:19], v[136:139], v[112:115]\n"
"v_mfma_f32_16x16x32_bf16 v[112:115], a[20:23], v[164:167], v[112:115]\n"
"v_mfma_f32_16x16x32_bf16 v[112:115], a[24:27], v[168:171], v[112:115]\n"
"v_mfma_f32_16x16x32_bf16 v[112:115], a[28:31], v[180:183], v[112:115]\n"
"v_mfma_f32_16x16x32_bf16 v[200:203], a[60:63], v[180:183], v[176:179]\n"
"v_mfma_f32_16x16x32_bf16 v[200:203], a[56:59], v[168:171], v[200:203]\n"
"v_mfma_f32_16x16x32_bf16 v[200:203], a[52:55], v[164:167], v[200:203]\n"
"v_mfma_f32_16x16x32_bf16 v[200:203], a[48:51], v[136:139], v[200:203]\n"
"v_mfma_f32_16x16x32_bf16 v[200:203], a[44:47], v[148:151], v[200:203]\n"
"v_mfma_f32_16x16x32_bf16 v[200:203], a[40:43], v[140:143], v[200:203]\n"
"s_nop 7\n"
"s_branch .Lmk_p84r\n"
".Lmk_f73s:\n"
";\n"
"s_mov_b32 s1, 0x10000\n"
".Lmk_f73l:\n"
"buffer_load_dwordx2 v[250:251], off, s[4:7], s98 sc1\n"
"s_waitcnt vmcnt(0)\n"
"v_cmp_ne_u64_e32 vcc, v[250:251], v[18:19]\n"
"s_cbranch_vccz .Lmk_f73r\n"
"s_sub_u32 s1, s1, 1\n"
"s_cbranch_scc0 .Lmk_f73l\n"
"s_branch .Lmk_f73r\n"
".Lmk_x94s:\n"
"s_nop 3\n"
"v_mov_b32_e32 v36, 0xff800000\n"
"v_mov_b32_e32 v37, 0xff800000\n"
"v_mov_b32_e32 v38, 0xff800000\n"
"v_mov_b32_e32 v39, 0xff800000\n"
"v_mov_b32_e32 v60, 0xff800000\n"
"v_mov_b32_e32 v61, 0xff800000\n"
"v_mov_b32_e32 v62, 0xff800000\n"
"v_mov_b32_e32 v63, 0xff800000\n"
"s_nop 1\n"
"s_branch .Lmk_x94r\n"
".Lmk_x95s:\n"
"s_nop 3\n"
"v_mov_b32_e32 v36, 0xff800000\n"
"v_mov_b32_e32 v37, 0xff800000\n"
"v_mov_b32_e32 v38, 0xff800000\n"
"v_mov_b32_e32 v39, 0xff800000\n"
"v_mov_b32_e32 v60, 0xff800000\n"
"v_mov_b32_e32 v61, 0xff800000\n"
"v_mov_b32_e32 v62, 0xff800000\n"
"v_mov_b32_e32 v63, 0xff800000\n"
"s_nop 1\n"
"s_branch .Lmk_x95r\n"
".Lmk_x96s:\n"
"s_nop 3\n"
"v_mov_b32_e32 v36, 0xff800000\n"
"v_mov_b32_e32 v37, 0xff800000\n"
"v_mov_b32_e32 v38, 0xff800000\n"
"v_mov_b32_e32 v39, 0xff800000\n"
"v_mov_b32_e32 v60, 0xff800000\n"
"v_mov_b32_e32 v61, 0xff800000\n"
"v_mov_b32_e32 v62, 0xff800000\n"
"v_mov_b32_e32 v63, 0xff800000\n"
"s_nop 1\n"
"s_branch .Lmk_x96r\n"
".Lmk_x97s:\n"
"s_nop 3\n"
"v_mov_b32_e32 v36, 0xff800000\n"
"v_mov_b32_e32 v37, 0xff800000\n"
"v_mov_b32_e32 v38, 0xff800000\n"
"v_mov_b32_e32 v39, 0xff800000\n"
"v_mov_b32_e32 v60, 0xff800000\n"
"v_mov_b32_e32 v61, 0xff800000\n"
"v_mov_b32_e32 v62, 0xff800000\n"
"v_mov_b32_e32 v63, 0xff800000\n"
"s_nop 1\n"
"s_branch .Lmk_x97r\n"
".Lmk_b92:\n"
"s_cmp_lg_u32 s2, 0\n"
"s_cbranch_scc1 .Lmk_k98\n"
"s_mov_b32 s2, 1\n"
"s_waitcnt vmcnt(0) lgkmcnt(0)\n"
"s_nop 7\n"
"s_mov_b32 s97, 11\n"
"v_add_u32_e32 v248, 0x3800, v15\n"
"v_add_u32_e32 v247, 0x3800, v254\n"
"ds_read_b128 v[36:39], v15 offset:24576\n"
"ds_read_b128 v[40:43], v254 offset:24832\n"
"s_waitcnt lgkmcnt(0)\n"
"v_mfma_f32_16x16x32_bf16 v[48:51], v[36:39], v[28:31], 0\n"
"v_mfma_f32_16x16x32_bf16 v[52:55], v[36:39], v[32:35], 0\n"
"v_mfma_f32_16x16x32_bf16 v[36:39], v[40:43], v[32:35], 0\n"
"v_mfma_f32_16x16x32_bf16 v[56:59], v[40:43], v[28:31], 0\n"
".Lmk_b92l:\n"
"ds_read_b128 v[40:43], v248 offset:8192\n"
"ds_read_b128 v[60:63], v247 offset:8448\n"
"s_cmp_gt_i32 s78, s97\n"
"s_cselect_b32 s96, 0xff800000, 0\n"
"v_mov_b32_e32 v64, s96\n"
"v_mov_b32_e32 v65, s96\n"
"v_mov_b32_e32 v66, s96\n"
"v_mov_b32_e32 v67, s96\n"
"v_subrev_u32_e32 v248, 0x800, v248\n"
"v_subrev_u32_e32 v247, 0x800, v247\n"
"s_waitcnt lgkmcnt(0)\n"
"v_mfma_f32_16x16x32_bf16 v[72:75], v[40:43], v[28:31], v[64:67]\n"
"v_mfma_f32_16x16x32_bf16 v[76:79], v[40:43], v[32:35], v[64:67]\n"
"v_mfma_f32_16x16x32_bf16 v[40:43], v[60:63], v[32:35], v[64:67]\n"
"v_mfma_f32_16x16x32_bf16 v[84:87], v[60:63], v[28:31], v[64:67]\n"
"s_nop 7\n"
"s_cmp_lg_u32 s97, 4\n"
"s_cbranch_scc1 .Lmk_b92m\n"
"v_cndmask_b32_e64 v72, v48, v72, s[20:21]\n"
"v_cndmask_b32_e64 v73, v49, v73, s[22:23]\n"
"v_cndmask_b32_e64 v74, v50, v74, s[24:25]\n"
"v_cndmask_b32_e64 v75, v51, v75, s[26:27]\n"
"v_cndmask_b32_e64 v84, v56, v84, s[28:29]\n"
"v_cndmask_b32_e64 v85, v57, v85, s[30:31]\n"
"v_cndmask_b32_e64 v86, v58, v86, s[32:33]\n"
"v_cndmask_b32_e64 v87, v59, v87, s[34:35]\n"
"v_cndmask_b32_e64 v76, v52, v76, s[36:37]\n"
"v_cndmask_b32_e64 v77, v53, v77, s[38:39]\n"
"v_cndmask_b32_e64 v78, v54, v78, s[40:41]\n"
"v_cndmask_b32_e64 v79, v55, v79, s[42:43]\n"
"v_cndmask_b32_e64 v40, v36, v40, s[44:45]\n"
"v_cndmask_b32_e64 v41, v37, v41, s[46:47]\n"
"v_cndmask_b32_e64 v42, v38, v42, s[48:49]\n"
"v_cndmask_b32_e64 v43, v39, v43, s[50:51]\n"
".Lmk_b92m:\n"
"v_max3_f32 v253, v253, v72, v73\n"
"v_max3_f32 v251, v251, v76, v77\n"
"v_max3_f32 v253, v253, v74, v75\n"
"v_max3_f32 v251, v251, v78, v79\n"
"v_max3_f32 v253, v253, v84, v85\n"
"v_max3_f32 v251, v251, v40, v41\n"
"v_max3_f32 v253, v253, v86, v87\n"
"v_max3_f32 v251, v251, v42, v43\n"
"s_sub_u32 s97, s97, 1\n"
"s_cmp_ge_i32 s97, 4\n"
"s_cbranch_scc1 .Lmk_b92l\n"
"v_mov_b32_e32 v248, v253\n"
"v_mov_b32_e32 v247, v251\n"
"s_nop 0\n"
"v_permlane16_swap_b32_e32 v253, v248\n"
"v_permlane16_swap_b32_e32 v251, v247\n"
"v_max_f32_e32 v253, v253, v248\n"
"v_max_f32_e32 v251, v251, v247\n"
"v_mov_b32_e32 v248, v253\n"
"v_mov_b32_e32 v247, v251\n"
"s_nop 0\n"
"v_permlane32_swap_b32_e32 v253, v248\n"
"v_permlane32_swap_b32_e32 v251, v247\n"
"v_max_f32_e32 v253, v253, v248\n"
"v_max_f32_e32 v251, v251, v247\n"
"v_mov_b32_e32 v250, v253\n"
"v_mov_b32_e32 v249, v251\n"
"s_branch .Lmk_a91\n"
".Lmk_x101s:\n"
"s_nop 3\n"
"v_mov_b32_e32 v116, 0xff800000\n"
"v_mov_b32_e32 v117, 0xff800000\n"
"v_mov_b32_e32 v118, 0xff800000\n"
"v_mov_b32_e32 v119, 0xff800000\n"
"v_mov_b32_e32 v120, 0xff800000\n"
"v_mov_b32_e32 v121, 0xff800000\n"
"v_mov_b32_e32 v122, 0xff800000\n"
"v_mov_b32_e32 v123, 0xff800000\n"
"s_nop 1\n"
"s_branch .Lmk_x101r\n"
".Lmk_x102s:\n"
"s_nop 3\n"
"v_mov_b32_e32 v116, 0xff800000\n"
"v_mov_b32_e32 v117, 0xff800000\n"
"v_mov_b32_e32 v118, 0xff800000\n"
"v_mov_b32_e32 v119, 0xff800000\n"
"v_mov_b32_e32 v120, 0xff800000\n"
"v_mov_b32_e32 v121, 0xff800000\n"
"v_mov_b32_e32 v122, 0xff800000\n"
"v_mov_b32_e32 v123, 0xff800000\n"
"s_nop 1\n"
"s_branch .Lmk_x102r\n"
".Lmk_x103s:\n"
"s_nop 3\n"
"v_mov_b32_e32 v116, 0xff800000\n"
"v_mov_b32_e32 v117, 0xff800000\n"
"v_mov_b32_e32 v118, 0xff800000\n"
"v_mov_b32_e32 v119, 0xff800000\n"
"v_mov_b32_e32 v120, 0xff800000\n"
"v_mov_b32_e32 v121, 0xff800000\n"
"v_mov_b32_e32 v122, 0xff800000\n"
"v_mov_b32_e32 v123, 0xff800000\n"
"s_nop 1\n"
"s_branch .Lmk_x103r\n"
".Lmk_h90s:\n"
";\n"
"s_waitcnt vmcnt(16)\n"
"s_mov_b32 s1, 0x10000\n"
".Lmk_h90l:\n"
"s_add_u32 m0, s77, 0x4000\n"
"s_add_u32 s95, s65, 0\n"
"buffer_load_dwordx4 v2, s[4:7], s95 offen sc1 lds\n"
"buffer_load_dwordx4 v2, s[4:7], s95 offen offset:1024 sc1 lds\n"
"s_add_u32 m0, s77, 0xc000\n"
"s_add_u32 s95, s65, 0x4000\n"
"buffer_load_dwordx4 v2, s[4:7], s95 offen sc1 lds\n"
"buffer_load_dwordx4 v2, s[4:7], s95 offen offset:1024 sc1 lds\n"
"s_waitcnt vmcnt(0)\n"
"ds_read_b128 v[164:167], v2 offset:16384\n"
"ds_read_b128 v[76:79], v2 offset:17408\n"
"ds_read_b128 v[72:75], v2 offset:49152\n"
"ds_read_b128 v[84:87], v2 offset:50176\n"
"s_waitcnt lgkmcnt(0)\n"
"s_waitcnt vmcnt(0)\n"
"v_max_u32_e32 v248, v164, v165\n"
"v_max3_u32 v248, v248, v166, v167\n"
"v_max3_u32 v248, v248, v76, v77\n"
"v_max3_u32 v248, v248, v78, v79\n"
"v_max3_u32 v248, v248, v72, v73\n"
"v_max3_u32 v248, v248, v74, v75\n"
"v_max3_u32 v248, v248, v84, v85\n"
"v_max3_u32 v248, v248, v86, v87\n"
"v_cmp_eq_u32_e32 vcc, -1, v248\n"
"s_cbranch_vccz .Lmk_h90k\n"
"s_sub_u32 s1, s1, 1\n"
"s_cbranch_scc0 .Lmk_h90l\n"
".Lmk_h90k:\n"
"s_branch .Lmk_h90r\n"
".Lmk_f74s:\n"
";\n"
"s_mov_b32 s1, 0x10000\n"
".Lmk_f74l:\n"
"buffer_load_dwordx2 v[246:247], v12, s[4:7], s96 offen sc1\n"
"s_waitcnt vmcnt(0)\n"
"v_cmp_ne_u64_e32 vcc, v[246:247], v[18:19]\n"
"s_cbranch_vccz .Lmk_f74r\n"
"s_sub_u32 s1, s1, 1\n"
"s_cbranch_scc0 .Lmk_f74l\n"
"s_branch .Lmk_f74r\n"
".Lmk_b100:\n"
"s_cmp_lg_u32 s2, 0\n"
"s_cbranch_scc1 .Lmk_k105\n"
"s_mov_b32 s2, 1\n"
"s_waitcnt vmcnt(0) lgkmcnt(0)\n"
"s_nop 7\n"
"s_mov_b32 s96, 7\n"
"v_add_u32_e32 v248, 0x3800, v15\n"
"v_add_u32_e32 v247, 0x3800, v254\n"
"ds_read_b128 v[188:191], v15 offset:16384\n"
"ds_read_b128 v[192:195], v254 offset:16640\n"
"s_waitcnt lgkmcnt(0)\n"
"v_mfma_f32_16x16x32_bf16 v[196:199], v[188:191], v[92:95], 0\n"
"v_mfma_f32_16x16x32_bf16 v[212:215], v[188:191], v[96:99], 0\n"
"v_mfma_f32_16x16x32_bf16 v[188:191], v[192:195], v[96:99], 0\n"
"v_mfma_f32_16x16x32_bf16 v[220:223], v[192:195], v[92:95], 0\n"
".Lmk_b100l:\n"
"ds_read_b128 v[192:195], v248\n"
"ds_read_b128 v[224:227], v247 offset:256\n"
"s_cmp_gt_i32 s78, s96\n"
"s_cselect_b32 s95, 0xff800000, 0\n"
"v_mov_b32_e32 v228, s95\n"
"v_mov_b32_e32 v229, s95\n"
"v_mov_b32_e32 v230, s95\n"
"v_mov_b32_e32 v231, s95\n"
"v_subrev_u32_e32 v248, 0x800, v248\n"
"v_subrev_u32_e32 v247, 0x800, v247\n"
"s_waitcnt lgkmcnt(0)\n"
"v_mfma_f32_16x16x32_bf16 v[232:235], v[192:195], v[92:95], v[228:231]\n"
"v_mfma_f32_16x16x32_bf16 v[236:239], v[192:195], v[96:99], v[228:231]\n"
"v_mfma_f32_16x16x32_bf16 v[192:195], v[224:227], v[96:99], v[228:231]\n"
"v_mfma_f32_16x16x32_bf16 v[240:243], v[224:227], v[92:95], v[228:231]\n"
"s_nop 7\n"
"s_cmp_lg_u32 s96, 0\n"
"s_cbranch_scc1 .Lmk_b100m\n"
"v_cndmask_b32_e64 v232, v196, v232, s[20:21]\n"
"v_cndmask_b32_e64 v233, v197, v233, s[22:23]\n"
"v_cndmask_b32_e64 v234, v198, v234, s[24:25]\n"
"v_cndmask_b32_e64 v235, v199, v235, s[26:27]\n"
"v_cndmask_b32_e64 v240, v220, v240, s[28:29]\n"
"v_cndmask_b32_e64 v241, v221, v241, s[30:31]\n"
"v_cndmask_b32_e64 v242, v222, v242, s[32:33]\n"
"v_cndmask_b32_e64 v243, v223, v243, s[34:35]\n"
"v_cndmask_b32_e64 v236, v212, v236, s[36:37]\n"
"v_cndmask_b32_e64 v237, v213, v237, s[38:39]\n"
"v_cndmask_b32_e64 v238, v214, v238, s[40:41]\n"
"v_cndmask_b32_e64 v239, v215, v239, s[42:43]\n"
"v_cndmask_b32_e64 v192, v188, v192, s[44:45]\n"
"v_cndmask_b32_e64 v193, v189, v193, s[46:47]\n"
"v_cndmask_b32_e64 v194, v190, v194, s[48:49]\n"
"v_cndmask_b32_e64 v195, v191, v195, s[50:51]\n"
".Lmk_b100m:\n"
"v_max3_f32 v253, v253, v232, v233\n"
"v_max3_f32 v251, v251, v236, v237\n"
"v_max3_f32 v253, v253, v234, v235\n"
"v_max3_f32 v251, v251, v238, v239\n"
"v_max3_f32 v253, v253, v240, v241\n"
"v_max3_f32 v251, v251, v192, v193\n"
"v_max3_f32 v253, v253, v242, v243\n"
"v_max3_f32 v251, v251, v194, v195\n"
"s_sub_u32 s96, s96, 1\n"
"s_cmp_ge_i32 s96, 0\n"
"s_cbranch_scc1 .Lmk_b100l\n"
"v_mov_b32_e32 v248, v253\n"
"v_mov_b32_e32 v247, v251\n"
"s_nop 0\n"
"v_permlane16_swap_b32_e32 v253, v248\n"
"v_permlane16_swap_b32_e32 v251, v247\n"
"v_max_f32_e32 v253, v253, v248\n"
"v_max_f32_e32 v251, v251, v247\n"
"v_mov_b32_e32 v248, v253\n"
"v_mov_b32_e32 v247, v251\n"
"s_nop 0\n"
"v_permlane32_swap_b32_e32 v253, v248\n"
"v_permlane32_swap_b32_e32 v251, v247\n"
"v_max_f32_e32 v253, v253, v248\n"
"v_max_f32_e32 v251, v251, v247\n"
"v_mov_b32_e32 v250, v253\n"
"v_mov_b32_e32 v249, v251\n"
"s_branch .Lmk_a99\n"
".Lmk_p106s:\n"
";\n"
"s_waitcnt vmcnt(16)\n"
"s_mov_b32 s1, 0x10000\n"
".Lmk_p106l:\n"
"s_add_u32 s98, s56, 0x40000\n"
"buffer_load_dwordx4 v[152:155], v0, s[4:7], s98 offen sc1\n"
"buffer_load_dwordx4 v[200:203], v0, s[4:7], s98 offen offset:1024 sc1\n"
"buffer_load_dwordx4 v[184:187], v0, s[4:7], s98 offen offset:2048 sc1\n"
"buffer_load_dwordx4 v[204:207], v0, s[4:7], s98 offen offset:3072 sc1\n"
"buffer_load_dwordx4 v[140:143], v1, s[4:7], s98 offen sc1\n"
"buffer_load_dwordx4 v[208:211], v1, s[4:7], s98 offen offset:1024 sc1\n"
"buffer_load_dwordx4 v[120:123], v1, s[4:7], s98 offen offset:2048 sc1\n"
"buffer_load_dwordx4 v[136:139], v1, s[4:7], s98 offen offset:3072 sc1\n"
"s_add_u32 s98, s57, 0x40000\n"
"buffer_load_dwordx4 v[216:219], v0, s[4:7], s98 offen sc1\n"
"buffer_load_dwordx4 v[132:135], v0, s[4:7], s98 offen offset:1024 sc1\n"
"buffer_load_dwordx4 v[160:163], v0, s[4:7], s98 offen offset:2048 sc1\n"
"buffer_load_dwordx4 v[100:103], v0, s[4:7], s98 offen offset:3072 sc1\n"
"buffer_load_dwordx4 v[164:167], v1, s[4:7], s98 offen sc1\n"
"buffer_load_dwordx4 v[104:107], v1, s[4:7], s98 offen offset:1024 sc1\n"
"buffer_load_dwordx4 v[108:111], v1, s[4:7], s98 offen offset:2048 sc1\n"
"buffer_load_dwordx4 v[128:131], v1, s[4:7], s98 offen offset:3072 sc1\n"
"s_add_u32 s98, s58, 0x40000\n"
"buffer_load_dwordx4 v[148:151], v0, s[4:7], s98 offen sc1\n"
"buffer_load_dwordx4 v[144:147], v0, s[4:7], s98 offen offset:1024 sc1\n"
"buffer_load_dwordx4 v[116:119], v0, s[4:7], s98 offen offset:2048 sc1\n"
"buffer_load_dwordx4 v[112:115], v0, s[4:7], s98 offen offset:3072 sc1\n"
"buffer_load_dwordx4 v[92:95], v1, s[4:7], s98 offen sc1\n"
"buffer_load_dwordx4 v[96:99], v1, s[4:7], s98 offen offset:1024 sc1\n"
"buffer_load_dwordx4 v[156:159], v1, s[4:7], s98 offen offset:2048 sc1\n"
"s_waitcnt vmcnt(0)\n"
"v_max_u32_e32 v255, v152, v153\n"
"v_max3_u32 v255, v255, v154, v155\n"
"v_max3_u32 v255, v255, v200, v201\n"
"v_max3_u32 v255, v255, v202, v203\n"
"v_max3_u32 v255, v255, v184, v185\n"
"v_max3_u32 v255, v255, v186, v187\n"
"v_max3_u32 v255, v255, v204, v205\n"
"v_max3_u32 v255, v255, v206, v207\n"
"v_max3_u32 v255, v255, v140, v141\n"
"v_max3_u32 v255, v255, v142, v143\n"
"v_max3_u32 v255, v255, v208, v209\n"
"v_max3_u32 v255, v255, v210, v211\n"
"v_max3_u32 v255, v255, v120, v121\n"
"v_max3_u32 v255, v255, v122, v123\n"
"v_max3_u32 v255, v255, v136, v137\n"
"v_max3_u32 v255, v255, v138, v139\n"
"v_cmp_eq_u32_e32 vcc, -1, v255\n"
"s_cbranch_vccz .Lmk_p106k\n"
"s_sub_u32 s1, s1, 1\n"
"s_cbranch_scc0 .Lmk_p106l\n"
".Lmk_p106k:\n"
"v_mfma_f32_16x16x32_bf16 v[88:91], a[128:131], v[152:155], 0\n"
"v_mfma_f32_16x16x32_bf16 v[88:91], a[132:135], v[200:203], v[88:91]\n"
"v_mfma_f32_16x16x32_bf16 v[88:91], a[136:139], v[184:187], v[88:91]\n"
"v_mfma_f32_16x16x32_bf16 v[88:91], a[140:143], v[204:207], v[88:91]\n"
"v_mfma_f32_16x16x32_bf16 v[88:91], a[144:147], v[140:143], v[88:91]\n"
"v_mfma_f32_16x16x32_bf16 v[88:91], a[148:151], v[208:211], v[88:91]\n"
"v_mfma_f32_16x16x32_bf16 v[88:91], a[152:155], v[120:123], v[88:91]\n"
"v_mfma_f32_16x16x32_bf16 v[88:91], a[156:159], v[136:139], v[88:91]\n"
"v_mfma_f32_16x16x32_bf16 v[124:127], a[188:191], v[136:139], 0\n"
"v_mfma_f32_16x16x32_bf16 v[124:127], a[184:187], v[120:123], v[124:127]\n"
"v_mfma_f32_16x16x32_bf16 v[124:127], a[180:183], v[208:211], v[124:127]\n"
"v_mfma_f32_16x16x32_bf16 v[124:127], a[176:179], v[140:143], v[124:127]\n"
"v_mfma_f32_16x16x32_bf16 v[124:127], a[172:175], v[204:207], v[124:127]\n"
"v_mfma_f32_16x16x32_bf16 v[124:127], a[168:171], v[184:187], v[124:127]\n"
"s_nop 7\n"
"s_branch .Lmk_p106r\n"
".Lmk_p107s:\n"
";\n"
"s_waitcnt vmcnt(16)\n"
"s_mov_b32 s1, 0x10000\n"
".Lmk_p107l:\n"
"s_add_u32 s97, s57, 0x40000\n"
"buffer_load_dwordx4 v[216:219], v0, s[4:7], s97 offen sc1\n"
"buffer_load_dwordx4 v[132:135], v0, s[4:7], s97 offen offset:1024 sc1\n"
"buffer_load_dwordx4 v[160:163], v0, s[4:7], s97 offen offset:2048 sc1\n"
"buffer_load_dwordx4 v[100:103], v0, s[4:7], s97 offen offset:3072 sc1\n"
"buffer_load_dwordx4 v[164:167], v1, s[4:7], s97 offen sc1\n"
"buffer_load_dwordx4 v[104:107], v1, s[4:7], s97 offen offset:1024 sc1\n"
"buffer_load_dwordx4 v[108:111], v1, s[4:7], s97 offen offset:2048 sc1\n"
"buffer_load_dwordx4 v[128:131], v1, s[4:7], s97 offen offset:3072 sc1\n"
"s_add_u32 s97, s58, 0x40000\n"
"buffer_load_dwordx4 v[148:151], v0, s[4:7], s97 offen sc1\n"
"buffer_load_dwordx4 v[144:147], v0, s[4:7], s97 offen offset:1024 sc1\n"
"buffer_load_dwordx4 v[116:119], v0, s[4:7], s97 offen offset:2048 sc1\n"
"buffer_load_dwordx4 v[112:115], v0, s[4:7], s97 offen offset:3072 sc1\n"
"buffer_load_dwordx4 v[92:95], v1, s[4:7], s97 offen sc1\n"
"buffer_load_dwordx4 v[96:99], v1, s[4:7], s97 offen offset:1024 sc1\n"
"buffer_load_dwordx4 v[156:159], v1, s[4:7], s97 offen offset:2048 sc1\n"
"buffer_load_dwordx4 v[88:91], v1, s[4:7], s97 offen offset:3072 sc1\n"
"s_add_u32 s97, s59, 0x40000\n"
"buffer_load_dwordx4 v[172:175], v0, s[4:7], s97 offen sc1\n"
"buffer_load_dwordx4 v[124:127], v0, s[4:7], s97 offen offset:1024 sc1\n"
"buffer_load_dwordx4 v[120:123], v0, s[4:7], s97 offen offset:2048 sc1\n"
"buffer_load_dwordx4 v[136:139], v0, s[4:7], s97 offen offset:3072 sc1\n"
"buffer_load_dwordx4 v[152:155], v1, s[4:7], s97 offen sc1\n"
"buffer_load_dwordx4 v[168:171], v1, s[4:7], s97 offen offset:1024 sc1\n"
"buffer_load_dwordx4 v[180:183], v1, s[4:7], s97 offen offset:2048 sc1\n"
"s_waitcnt vmcnt(0)\n"
"v_max_u32_e32 v255, v216, v217\n"
"v_max3_u32 v255, v255, v218, v219\n"
"v_max3_u32 v255, v255, v132, v133\n"
"v_max3_u32 v255, v255, v134, v135\n"
"v_max3_u32 v255, v255, v160, v161\n"
"v_max3_u32 v255, v255, v162, v163\n"
"v_max3_u32 v255, v255, v100, v101\n"
"v_max3_u32 v255, v255, v102, v103\n"
"v_max3_u32 v255, v255, v164, v165\n"
"v_max3_u32 v255, v255, v166, v167\n"
"v_max3_u32 v255, v255, v104, v105\n"
"v_max3_u32 v255, v255, v106, v107\n"
"v_max3_u32 v255, v255, v108, v109\n"
"v_max3_u32 v255, v255, v110, v111\n"
"v_max3_u32 v255, v255, v128, v129\n"
"v_max3_u32 v255, v255, v130, v131\n"
"v_cmp_eq_u32_e32 vcc, -1, v255\n"
"s_cbranch_vccz .Lmk_p107k\n"
"s_sub_u32 s1, s1, 1\n"
"s_cbranch_scc0 .Lmk_p107l\n"
".Lmk_p107k:\n"
"v_mfma_f32_16x16x32_bf16 v[140:143], a[128:131], v[216:219], 0\n"
"v_mfma_f32_16x16x32_bf16 v[140:143], a[132:135], v[132:135], v[140:143]\n"
"v_mfma_f32_16x16x32_bf16 v[140:143], a[136:139], v[160:163], v[140:143]\n"
"v_mfma_f32_16x16x32_bf16 v[140:143], a[140:143], v[100:103], v[140:143]\n"
"v_mfma_f32_16x16x32_bf16 v[140:143], a[144:147], v[164:167], v[140:143]\n"
"v_mfma_f32_16x16x32_bf16 v[140:143], a[148:151], v[104:107], v[140:143]\n"
"v_mfma_f32_16x16x32_bf16 v[140:143], a[152:155], v[108:111], v[140:143]\n"
"v_mfma_f32_16x16x32_bf16 v[140:143], a[156:159], v[128:131], v[140:143]\n"
"v_mfma_f32_16x16x32_bf16 v[176:179], a[188:191], v[128:131], 0\n"
"v_mfma_f32_16x16x32_bf16 v[176:179], a[184:187], v[108:111], v[176:179]\n"
"v_mfma_f32_16x16x32_bf16 v[176:179], a[180:183], v[104:107], v[176:179]\n"
"v_mfma_f32_16x16x32_bf16 v[176:179], a[176:179], v[164:167], v[176:179]\n"
"v_mfma_f32_16x16x32_bf16 v[176:179], a[172:175], v[100:103], v[176:179]\n"
"v_mfma_f32_16x16x32_bf16 v[176:179], a[168:171], v[160:163], v[176:179]\n"
"s_nop 7\n"
"s_branch .Lmk_p107r\n"
".Lmk_p108s:\n"
";\n"
"s_waitcnt vmcnt(16)\n"
"s_mov_b32 s1, 0x10000\n"
".Lmk_p108l:\n"
"s_add_u32 s97, s58, 0x40000\n"
"buffer_load_dwordx4 v[148:151], v0, s[4:7], s97 offen sc1\n"
"buffer_load_dwordx4 v[144:147], v0, s[4:7], s97 offen offset:1024 sc1\n"
"buffer_load_dwordx4 v[116:119], v0, s[4:7], s97 offen offset:2048 sc1\n"
"buffer_load_dwordx4 v[112:115], v0, s[4:7], s97 offen offset:3072 sc1\n"
"buffer_load_dwordx4 v[92:95], v1, s[4:7], s97 offen sc1\n"
"buffer_load_dwordx4 v[96:99], v1, s[4:7], s97 offen offset:1024 sc1\n"
"buffer_load_dwordx4 v[156:159], v1, s[4:7], s97 offen offset:2048 sc1\n"
"buffer_load_dwordx4 v[88:91], v1, s[4:7], s97 offen offset:3072 sc1\n"
"s_add_u32 s97, s59, 0x40000\n"
"buffer_load_dwordx4 v[172:175], v0, s[4:7], s97 offen sc1\n"
"buffer_load_dwordx4 v[124:127], v0, s[4:7], s97 offen offset:1024 sc1\n"
"buffer_load_dwordx4 v[120:123], v0, s[4:7], s97 offen offset:2048 sc1\n"
"buffer_load_dwordx4 v[136:139], v0, s[4:7], s97 offen offset:3072 sc1\n"
"buffer_load_dwordx4 v[152:155], v1, s[4:7], s97 offen sc1\n"
"buffer_load_dwordx4 v[168:171], v1, s[4:7], s97 offen offset:1024 sc1\n"
"buffer_load_dwordx4 v[180:183], v1, s[4:7], s97 offen offset:2048 sc1\n"
"buffer_load_dwordx4 v[140:143], v1, s[4:7], s97 offen offset:3072 sc1\n"
"s_waitcnt vmcnt(0)\n"
"v_max_u32_e32 v239, v148, v149\n"
"v_max3_u32 v239, v239, v150, v151\n"
"v_max3_u32 v239, v239, v144, v145\n"
"v_max3_u32 v239, v239, v146, v147\n"
"v_max3_u32 v239, v239, v116, v117\n"
"v_max3_u32 v239, v239, v118, v119\n"
"v_max3_u32 v239, v239, v112, v113\n"
"v_max3_u32 v239, v239, v114, v115\n"
"v_max3_u32 v239, v239, v92, v93\n"
"v_max3_u32 v239, v239, v94, v95\n"
"v_max3_u32 v239, v239, v96, v97\n"
"v_max3_u32 v239, v239, v98, v99\n"
"v_max3_u32 v239, v239, v156, v157\n"
"v_max3_u32 v239, v239, v158, v159\n"
"v_max3_u32 v239, v239, v88, v89\n"
"v_max3_u32 v239, v239, v90, v91\n"
"v_cmp_eq_u32_e32 vcc, -1, v239\n"
"s_cbranch_vccz .Lmk_p108k\n"
"s_sub_u32 s1, s1, 1\n"
"s_cbranch_scc0 .Lmk_p108l\n"
".Lmk_p108k:\n"
"v_mfma_f32_16x16x32_bf16 v[100:103], a[128:131], v[148:151], 0\n"
"v_mfma_f32_16x16x32_bf16 v[100:103], a[132:135], v[144:147], v[100:103]\n"
"v_mfma_f32_16x16x32_bf16 v[100:103], a[136:139], v[116:119], v[100:103]\n"
"v_mfma_f32_16x16x32_bf16 v[100:103], a[140:143], v[112:115], v[100:103]\n"
"v_mfma_f32_16x16x32_bf16 v[100:103], a[144:147], v[92:95], v[100:103]\n"
"v_mfma_f32_16x16x32_bf16 v[100:103], a[148:151], v[96:99], v[100:103]\n"
"v_mfma_f32_16x16x32_bf16 v[100:103], a[152:155], v[156:159], v[100:103]\n"
"v_mfma_f32_16x16x32_bf16 v[100:103], a[156:159], v[88:91], v[100:103]\n"
"v_mfma_f32_16x16x32_bf16 v[104:107], a[188:191], v[88:91], 0\n"
"v_mfma_f32_16x16x32_bf16 v[104:107], a[184:187], v[156:159], v[104:107]\n"
"v_mfma_f32_16x16x32_bf16 v[104:107], a[180:183], v[96:99], v[104:107]\n"
"v_mfma_f32_16x16x32_bf16 v[104:107], a[176:179], v[92:95], v[104:107]\n"
"v_mfma_f32_16x16x32_bf16 v[104:107], a[172:175], v[112:115], v[104:107]\n"
"v_mfma_f32_16x16x32_bf16 v[104:107], a[168:171], v[116:119], v[104:107]\n"
"s_nop 7\n"
"s_branch .Lmk_p108r\n"
".Lmk_p109s:\n"
";\n"
"s_waitcnt vmcnt(16)\n"
"s_mov_b32 s1, 0x10000\n"
".Lmk_p109l:\n"
"s_add_u32 s97, s59, 0x40000\n"
"buffer_load_dwordx4 v[172:175], v0, s[4:7], s97 offen sc1\n"
"buffer_load_dwordx4 v[124:127], v0, s[4:7], s97 offen offset:1024 sc1\n"
"buffer_load_dwordx4 v[120:123], v0, s[4:7], s97 offen offset:2048 sc1\n"
"buffer_load_dwordx4 v[136:139], v0, s[4:7], s97 offen offset:3072 sc1\n"
"buffer_load_dwordx4 v[152:155], v1, s[4:7], s97 offen sc1\n"
"buffer_load_dwordx4 v[168:171], v1, s[4:7], s97 offen offset:1024 sc1\n"
"buffer_load_dwordx4 v[180:183], v1, s[4:7], s97 offen offset:2048 sc1\n"
"buffer_load_dwordx4 v[140:143], v1, s[4:7], s97 offen offset:3072 sc1\n"
"s_add_u32 s97, s56, 0\n"
"buffer_load_dwordx4 v[100:103], v0, s[4:7], s97 offen sc1\n"
"buffer_load_dwordx4 v[176:179], v0, s[4:7], s97 offen offset:1024 sc1\n"
"buffer_load_dwordx4 v[104:107], v0, s[4:7], s97 offen offset:2048 sc1\n"
"buffer_load_dwordx4 v[88:91], v0, s[4:7], s97 offen offset:3072 sc1\n"
"buffer_load_dwordx4 v[92:95], v1, s[4:7], s97 offen sc1\n"
"buffer_load_dwordx4 v[116:119], v1, s[4:7], s97 offen offset:1024 sc1\n"
"buffer_load_dwordx4 v[132:135], v1, s[4:7], s97 offen offset:2048 sc1\n"
"buffer_load_dwordx4 v[144:147], v1, s[4:7], s97 offen offset:3072 sc1\n"
"s_add_u32 s97, s57, 0\n"
"buffer_load_dwordx4 v[156:159], v0, s[4:7], s97 offen sc1\n"
"s_waitcnt vmcnt(0)\n"
"v_max_u32_e32 v255, v172, v173\n"
"v_max3_u32 v255, v255, v174, v175\n"
"v_max3_u32 v255, v255, v124, v125\n"
"v_max3_u32 v255, v255, v126, v127\n"
"v_max3_u32 v255, v255, v120, v121\n"
"v_max3_u32 v255, v255, v122, v123\n"
"v_max3_u32 v255, v255, v136, v137\n"
"v_max3_u32 v255, v255, v138, v139\n"
"v_max3_u32 v255, v255, v152, v153\n"
"v_max3_u32 v255, v255, v154, v155\n"
"v_max3_u32 v255, v255, v168, v169\n"
"v_max3_u32 v255, v255, v170, v171\n"
"v_max3_u32 v255, v255, v180, v181\n"
"v_max3_u32 v255, v255, v182, v183\n"
"v_max3_u32 v255, v255, v140, v141\n"
"v_max3_u32 v255, v255, v142, v143\n"
"v_cmp_eq_u32_e32 vcc, -1, v255\n"
"s_cbranch_vccz .Lmk_p109k\n"
"s_sub_u32 s1, s1, 1\n"
"s_cbranch_scc0 .Lmk_p109l\n"
".Lmk_p109k:\n"
"v_mfma_f32_16x16x32_bf16 v[128:131], a[128:131], v[172:175], v[108:111]\n"
"v_mfma_f32_16x16x32_bf16 v[128:131], a[132:135], v[124:127], v[128:131]\n"
"v_mfma_f32_16x16x32_bf16 v[128:131], a[136:139], v[120:123], v[128:131]\n"
"v_mfma_f32_16x16x32_bf16 v[128:131], a[140:143], v[136:139], v[128:131]\n"
"v_mfma_f32_16x16x32_bf16 v[128:131], a[144:147], v[152:155], v[128:131]\n"
"v_mfma_f32_16x16x32_bf16 v[128:131], a[148:151], v[168:171], v[128:131]\n"
"v_mfma_f32_16x16x32_bf16 v[128:131], a[152:155], v[180:183], v[128:131]\n"
"v_mfma_f32_16x16x32_bf16 v[128:131], a[156:159], v[140:143], v[128:131]\n"
"v_mfma_f32_16x16x32_bf16 v[148:151], a[188:191], v[140:143], v[160:163]\n"
"v_mfma_f32_16x16x32_bf16 v[148:151], a[184:187], v[180:183], v[148:151]\n"
"v_mfma_f32_16x16x32_bf16 v[148:151], a[180:183], v[168:171], v[148:151]\n"
"v_mfma_f32_16x16x32_bf16 v[148:151], a[176:179], v[152:155], v[148:151]\n"
"v_mfma_f32_16x16x32_bf16 v[148:151], a[172:175], v[136:139], v[148:151]\n"
"v_mfma_f32_16x16x32_bf16 v[148:151], a[168:171], v[120:123], v[148:151]\n"
"s_nop 7\n"
"s_branch .Lmk_p109r\n"
".Lmk_p110s:\n"
";\n"
"s_waitcnt vmcnt(16)\n"
"s_mov_b32 s1, 0x10000\n"
".Lmk_p110l:\n"
"s_add_u32 s95, s56, 0\n"
"buffer_load_dwordx4 v[100:103], v0, s[4:7], s95 offen sc1\n"
"buffer_load_dwordx4 v[176:179], v0, s[4:7], s95 offen offset:1024 sc1\n"
"buffer_load_dwordx4 v[104:107], v0, s[4:7], s95 offen offset:2048 sc1\n"
"buffer_load_dwordx4 v[88:91], v0, s[4:7], s95 offen offset:3072 sc1\n"
"buffer_load_dwordx4 v[92:95], v1, s[4:7], s95 offen sc1\n"
"buffer_load_dwordx4 v[116:119], v1, s[4:7], s95 offen offset:1024 sc1\n"
"buffer_load_dwordx4 v[132:135], v1, s[4:7], s95 offen offset:2048 sc1\n"
"buffer_load_dwordx4 v[144:147], v1, s[4:7], s95 offen offset:3072 sc1\n"
"s_add_u32 s95, s57, 0\n"
"buffer_load_dwordx4 v[156:159], v0, s[4:7], s95 offen sc1\n"
"buffer_load_dwordx4 v[108:111], v0, s[4:7], s95 offen offset:1024 sc1\n"
"buffer_load_dwordx4 v[112:115], v0, s[4:7], s95 offen offset:2048 sc1\n"
"buffer_load_dwordx4 v[196:199], v0, s[4:7], s95 offen offset:3072 sc1\n"
"buffer_load_dwordx4 v[200:203], v1, s[4:7], s95 offen sc1\n"
"buffer_load_dwordx4 v[128:131], v1, s[4:7], s95 offen offset:1024 sc1\n"
"buffer_load_dwordx4 v[120:123], v1, s[4:7], s95 offen offset:2048 sc1\n"
"buffer_load_dwordx4 v[136:139], v1, s[4:7], s95 offen offset:3072 sc1\n"
"s_add_u32 s95, s58, 0\n"
"buffer_load_dwordx4 v[148:151], v0, s[4:7], s95 offen sc1\n"
"buffer_load_dwordx4 v[160:163], v0, s[4:7], s95 offen offset:1024 sc1\n"
"buffer_load_dwordx4 v[124:127], v0, s[4:7], s95 offen offset:2048 sc1\n"
"s_waitcnt vmcnt(0)\n"
"v_max_u32_e32 v255, v100, v101\n"
"v_max3_u32 v255, v255, v102, v103\n"
"v_max3_u32 v255, v255, v176, v177\n"
"v_max3_u32 v255, v255, v178, v179\n"
"v_max3_u32 v255, v255, v104, v105\n"
"v_max3_u32 v255, v255, v106, v107\n"
"v_max3_u32 v255, v255, v88, v89\n"
"v_max3_u32 v255, v255, v90, v91\n"
"v_max3_u32 v255, v255, v92, v93\n"
"v_max3_u32 v255, v255, v94, v95\n"
"v_max3_u32 v255, v255, v116, v117\n"
"v_max3_u32 v255, v255, v118, v119\n"
"v_max3_u32 v255, v255, v132, v133\n"
"v_max3_u32 v255, v255, v134, v135\n"
"v_max3_u32 v255, v255, v144, v145\n"
"v_max3_u32 v255, v255, v146, v147\n"
"v_cmp_eq_u32_e32 vcc, -1, v255\n"
"s_cbranch_vccz .Lmk_p110k\n"
"s_sub_u32 s1, s1, 1\n"
"s_cbranch_scc0 .Lmk_p110l\n"
".Lmk_p110k:\n"
"v_mfma_f32_16x16x32_bf16 v[140:143], a[128:131], v[100:103], 0\n"
"v_mfma_f32_16x16x32_bf16 v[140:143], a[132:135], v[176:179], v[140:143]\n"
"v_mfma_f32_16x16x32_bf16 v[140:143], a[136:139], v[104:107], v[140:143]\n"
"v_mfma_f32_16x16x32_bf16 v[140:143], a[140:143], v[88:91], v[140:143]\n"
"v_mfma_f32_16x16x32_bf16 v[140:143], a[144:147], v[92:95], v[140:143]\n"
"v_mfma_f32_16x16x32_bf16 v[140:143], a[148:151], v[116:119], v[140:143]\n"
"v_mfma_f32_16x16x32_bf16 v[140:143], a[152:155], v[132:135], v[140:143]\n"
"v_mfma_f32_16x16x32_bf16 v[140:143], a[156:159], v[144:147], v[140:143]\n"
"v_mfma_f32_16x16x32_bf16 v[96:99], a[188:191], v[144:147], 0\n"
"v_mfma_f32_16x16x32_bf16 v[96:99], a[184:187], v[132:135], v[96:99]\n"
"v_mfma_f32_16x16x32_bf16 v[96:99], a[180:183], v[116:119], v[96:99]\n"
"v_mfma_f32_16x16x32_bf16 v[96:99], a[176:179], v[92:95], v[96:99]\n"
"v_mfma_f32_16x16x32_bf16 v[96:99], a[172:175], v[88:91], v[96:99]\n"
"v_mfma_f32_16x16x32_bf16 v[96:99], a[168:171], v[104:107], v[96:99]\n"
"s_nop 7\n"
"s_branch .Lmk_p110r\n"
".Lmk_p111s:\n"
";\n"
"s_waitcnt vmcnt(16)\n"
"s_mov_b32 s1, 0x10000\n"
".Lmk_p111l:\n"
"s_add_u32 s97, s57, 0\n"
"buffer_load_dwordx4 v[156:159], v0, s[4:7], s97 offen sc1\n"
"buffer_load_dwordx4 v[108:111], v0, s[4:7], s97 offen offset:1024 sc1\n"
"buffer_load_dwordx4 v[112:115], v0, s[4:7], s97 offen offset:2048 sc1\n"
"buffer_load_dwordx4 v[196:199], v0, s[4:7], s97 offen offset:3072 sc1\n"
"buffer_load_dwordx4 v[200:203], v1, s[4:7], s97 offen sc1\n"
"buffer_load_dwordx4 v[128:131], v1, s[4:7], s97 offen offset:1024 sc1\n"
"buffer_load_dwordx4 v[120:123], v1, s[4:7], s97 offen offset:2048 sc1\n"
"buffer_load_dwordx4 v[136:139], v1, s[4:7], s97 offen offset:3072 sc1\n"
"s_add_u32 s97, s58, 0\n"
"buffer_load_dwordx4 v[148:151], v0, s[4:7], s97 offen sc1\n"
"buffer_load_dwordx4 v[160:163], v0, s[4:7], s97 offen offset:1024 sc1\n"
"buffer_load_dwordx4 v[124:127], v0, s[4:7], s97 offen offset:2048 sc1\n"
"buffer_load_dwordx4 v[140:143], v0, s[4:7], s97 offen offset:3072 sc1\n"
"buffer_load_dwordx4 v[152:155], v1, s[4:7], s97 offen sc1\n"
"buffer_load_dwordx4 v[172:175], v1, s[4:7], s97 offen offset:1024 sc1\n"
"buffer_load_dwordx4 v[116:119], v1, s[4:7], s97 offen offset:2048 sc1\n"
"buffer_load_dwordx4 v[132:135], v1, s[4:7], s97 offen offset:3072 sc1\n"
"s_add_u32 s97, s59, 0\n"
"buffer_load_dwordx4 v[176:179], v0, s[4:7], s97 offen sc1\n"
"buffer_load_dwordx4 v[180:183], v0, s[4:7], s97 offen offset:1024 sc1\n"
"buffer_load_dwordx4 v[168:171], v0, s[4:7], s97 offen offset:2048 sc1\n"
"s_waitcnt vmcnt(0)\n"
"v_max_u32_e32 v255, v156, v157\n"
"v_max3_u32 v255, v255, v158, v159\n"
"v_max3_u32 v255, v255, v108, v109\n"
"v_max3_u32 v255, v255, v110, v111\n"
"v_max3_u32 v255, v255, v112, v113\n"
"v_max3_u32 v255, v255, v114, v115\n"
"v_max3_u32 v255, v255, v196, v197\n"
"v_max3_u32 v255, v255, v198, v199\n"
"v_max3_u32 v255, v255, v200, v201\n"
"v_max3_u32 v255, v255, v202, v203\n"
"v_max3_u32 v255, v255, v128, v129\n"
"v_max3_u32 v255, v255, v130, v131\n"
"v_max3_u32 v255, v255, v120, v121\n"
"v_max3_u32 v255, v255, v122, v123\n"
"v_max3_u32 v255, v255, v136, v137\n"
"v_max3_u32 v255, v255, v138, v139\n"
"v_cmp_eq_u32_e32 vcc, -1, v255\n"
"s_cbranch_vccz .Lmk_p111k\n"
"s_sub_u32 s1, s1, 1\n"
"s_cbranch_scc0 .Lmk_p111l\n"
".Lmk_p111k:\n"
"v_mfma_f32_16x16x32_bf16 v[88:91], a[128:131], v[156:159], 0\n"
"v_mfma_f32_16x16x32_bf16 v[88:91], a[132:135], v[108:111], v[88:91]\n"
"v_mfma_f32_16x16x32_bf16 v[88:91], a[136:139], v[112:115], v[88:91]\n"
"v_mfma_f32_16x16x32_bf16 v[88:91], a[140:143], v[196:199], v[88:91]\n"
"v_mfma_f32_16x16x32_bf16 v[88:91], a[144:147], v[200:203], v[88:91]\n"
"v_mfma_f32_16x16x32_bf16 v[88:91], a[148:151], v[128:131], v[88:91]\n"
"v_mfma_f32_16x16x32_bf16 v[88:91], a[152:155], v[120:123], v[88:91]\n"
"v_mfma_f32_16x16x32_bf16 v[88:91], a[156:159], v[136:139], v[88:91]\n"
"v_mfma_f32_16x16x32_bf16 v[92:95], a[188:191], v[136:139], 0\n"
"v_mfma_f32_16x16x32_bf16 v[92:95], a[184:187], v[120:123], v[92:95]\n"
"v_mfma_f32_16x16x32_bf16 v[92:95], a[180:183], v[128:131], v[92:95]\n"
"v_mfma_f32_16x16x32_bf16 v[92:95], a[176:179], v[200:203], v[92:95]\n"
"v_mfma_f32_16x16x32_bf16 v[92:95], a[172:175], v[196:199], v[92:95]\n"
"v_mfma_f32_16x16x32_bf16 v[92:95], a[168:171], v[112:115], v[92:95]\n"
"s_nop 7\n"
"s_branch .Lmk_p111r\n"
".Lmk_p112s:\n"
";\n"
"s_waitcnt vmcnt(16)\n"
"s_mov_b32 s1, 0x10000\n"
".Lmk_p112l:\n"
"s_add_u32 s97, s58, 0\n"
"buffer_load_dwordx4 v[148:151], v0, s[4:7], s97 offen sc1\n"
"buffer_load_dwordx4 v[160:163], v0, s[4:7], s97 offen offset:1024 sc1\n"
"buffer_load_dwordx4 v[124:127], v0, s[4:7], s97 offen offset:2048 sc1\n"
"buffer_load_dwordx4 v[140:143], v0, s[4:7], s97 offen offset:3072 sc1\n"
"buffer_load_dwordx4 v[152:155], v1, s[4:7], s97 offen sc1\n"
"buffer_load_dwordx4 v[172:175], v1, s[4:7], s97 offen offset:1024 sc1\n"
"buffer_load_dwordx4 v[116:119], v1, s[4:7], s97 offen offset:2048 sc1\n"
"buffer_load_dwordx4 v[132:135], v1, s[4:7], s97 offen offset:3072 sc1\n"
"s_add_u32 s97, s59, 0\n"
"buffer_load_dwordx4 v[176:179], v0, s[4:7], s97 offen sc1\n"
"buffer_load_dwordx4 v[180:183], v0, s[4:7], s97 offen offset:1024 sc1\n"
"buffer_load_dwordx4 v[168:171], v0, s[4:7], s97 offen offset:2048 sc1\n"
"buffer_load_dwordx4 v[184:187], v0, s[4:7], s97 offen offset:3072 sc1\n"
"buffer_load_dwordx4 v[188:191], v1, s[4:7], s97 offen sc1\n"
"buffer_load_dwordx4 v[164:167], v1, s[4:7], s97 offen offset:1024 sc1\n"
"buffer_load_dwordx4 v[192:195], v1, s[4:7], s97 offen offset:2048 sc1\n"
"buffer_load_dwordx4 v[196:199], v1, s[4:7], s97 offen offset:3072 sc1\n"
"s_add_u32 s97, s52, 0x440000\n"
"buffer_load_dwordx4 v[88:91], v0, s[4:7], s97 offen sc1\n"
"buffer_load_dwordx4 v[92:95], v0, s[4:7], s97 offen offset:1024 sc1\n"
"s_waitcnt vmcnt(0)\n"
"v_max_u32_e32 v239, v148, v149\n"
"v_max3_u32 v239, v239, v150, v151\n"
"v_max3_u32 v239, v239, v160, v161\n"
"v_max3_u32 v239, v239, v162, v163\n"
"v_max3_u32 v239, v239, v124, v125\n"
"v_max3_u32 v239, v239, v126, v127\n"
"v_max3_u32 v239, v239, v140, v141\n"
"v_max3_u32 v239, v239, v142, v143\n"
"v_max3_u32 v239, v239, v152, v153\n"
"v_max3_u32 v239, v239, v154, v155\n"
"v_max3_u32 v239, v239, v172, v173\n"
"v_max3_u32 v239, v239, v174, v175\n"
"v_max3_u32 v239, v239, v116, v117\n"
"v_max3_u32 v239, v239, v118, v119\n"
"v_max3_u32 v239, v239, v132, v133\n"
"v_max3_u32 v239, v239, v134, v135\n"
"v_cmp_eq_u32_e32 vcc, -1, v239\n"
"s_cbranch_vccz .Lmk_p112k\n"
"s_sub_u32 s1, s1, 1\n"
"s_cbranch_scc0 .Lmk_p112l\n"
".Lmk_p112k:\n"
"v_mfma_f32_16x16x32_bf16 v[96:99], a[128:131], v[148:151], 0\n"
"v_mfma_f32_16x16x32_bf16 v[96:99], a[132:135], v[160:163], v[96:99]\n"
"v_mfma_f32_16x16x32_bf16 v[96:99], a[136:139], v[124:127], v[96:99]\n"
"v_mfma_f32_16x16x32_bf16 v[96:99], a[140:143], v[140:143], v[96:99]\n"
"v_mfma_f32_16x16x32_bf16 v[96:99], a[144:147], v[152:155], v[96:99]\n"
"v_mfma_f32_16x16x32_bf16 v[96:99], a[148:151], v[172:175], v[96:99]\n"
"v_mfma_f32_16x16x32_bf16 v[96:99], a[152:155], v[116:119], v[96:99]\n"
"v_mfma_f32_16x16x32_bf16 v[96:99], a[156:159], v[132:135], v[96:99]\n"
"v_mfma_f32_16x16x32_bf16 v[104:107], a[188:191], v[132:135], 0\n"
"v_mfma_f32_16x16x32_bf16 v[104:107], a[184:187], v[116:119], v[104:107]\n"
"v_mfma_f32_16x16x32_bf16 v[104:107], a[180:183], v[172:175], v[104:107]\n"
"v_mfma_f32_16x16x32_bf16 v[104:107], a[176:179], v[152:155], v[104:107]\n"
"v_mfma_f32_16x16x32_bf16 v[104:107], a[172:175], v[140:143], v[104:107]\n"
"v_mfma_f32_16x16x32_bf16 v[104:107], a[168:171], v[124:127], v[104:107]\n"
"s_nop 7\n"
"s_branch .Lmk_p112r\n"
".Lmk_p113s:\n"
";\n"
"s_waitcnt vmcnt(16)\n"
"s_mov_b32 s1, 0x10000\n"
".Lmk_p113l:\n"
"s_add_u32 s97, s59, 0\n"
"buffer_load_dwordx4 v[176:179], v0, s[4:7], s97 offen sc1\n"
"buffer_load_dwordx4 v[180:183], v0, s[4:7], s97 offen offset:1024 sc1\n"
"buffer_load_dwordx4 v[168:171], v0, s[4:7], s97 offen offset:2048 sc1\n"
"buffer_load_dwordx4 v[184:187], v0, s[4:7], s97 offen offset:3072 sc1\n"
"buffer_load_dwordx4 v[188:191], v1, s[4:7], s97 offen sc1\n"
"buffer_load_dwordx4 v[164:167], v1, s[4:7], s97 offen offset:1024 sc1\n"
"buffer_load_dwordx4 v[192:195], v1, s[4:7], s97 offen offset:2048 sc1\n"
"buffer_load_dwordx4 v[196:199], v1, s[4:7], s97 offen offset:3072 sc1\n"
"s_add_u32 s97, s52, 0x440000\n"
"buffer_load_dwordx4 v[88:91], v0, s[4:7], s97 offen sc1\n"
"buffer_load_dwordx4 v[92:95], v0, s[4:7], s97 offen offset:1024 sc1\n"
"buffer_load_dwordx4 v[96:99], v0, s[4:7], s97 offen offset:2048 sc1\n"
"buffer_load_dwordx4 v[100:103], v0, s[4:7], s97 offen offset:3072 sc1\n"
"buffer_load_dwordx4 v[104:107], v1, s[4:7], s97 offen sc1\n"
"buffer_load_dwordx4 v[112:115], v1, s[4:7], s97 offen offset:1024 sc1\n"
"buffer_load_dwordx4 v[116:119], v1, s[4:7], s97 offen offset:2048 sc1\n"
"buffer_load_dwordx4 v[124:127], v1, s[4:7], s97 offen offset:3072 sc1\n"
"s_add_u32 s97, s53, 0x440000\n"
"buffer_load_dwordx4 v[128:131], v0, s[4:7], s97 offen sc1\n"
"buffer_load_dwordx4 v[132:135], v0, s[4:7], s97 offen offset:1024 sc1\n"
"buffer_load_dwordx4 v[136:139], v0, s[4:7], s97 offen offset:2048 sc1\n"
"s_waitcnt vmcnt(0)\n"
"v_max_u32_e32 v255, v176, v177\n"
"v_max3_u32 v255, v255, v178, v179\n"
"v_max3_u32 v255, v255, v180, v181\n"
"v_max3_u32 v255, v255, v182, v183\n"
"v_max3_u32 v255, v255, v168, v169\n"
"v_max3_u32 v255, v255, v170, v171\n"
"v_max3_u32 v255, v255, v184, v185\n"
"v_max3_u32 v255, v255, v186, v187\n"
"v_max3_u32 v255, v255, v188, v189\n"
"v_max3_u32 v255, v255, v190, v191\n"
"v_max3_u32 v255, v255, v164, v165\n"
"v_max3_u32 v255, v255, v166, v167\n"
"v_max3_u32 v255, v255, v192, v193\n"
"v_max3_u32 v255, v255, v194, v195\n"
"v_max3_u32 v255, v255, v196, v197\n"
"v_max3_u32 v255, v255, v198, v199\n"
"v_cmp_eq_u32_e32 vcc, -1, v255\n"
"s_cbranch_vccz .Lmk_p113k\n"
"s_sub_u32 s1, s1, 1\n"
"s_cbranch_scc0 .Lmk_p113l\n"
".Lmk_p113k:\n"
"v_mfma_f32_16x16x32_bf16 v[172:175], a[128:131], v[176:179], v[144:147]\n"
"v_mfma_f32_16x16x32_bf16 v[172:175], a[132:135], v[180:183], v[172:175]\n"
"v_mfma_f32_16x16x32_bf16 v[172:175], a[136:139], v[168:171], v[172:175]\n"
"v_mfma_f32_16x16x32_bf16 v[172:175], a[140:143], v[184:187], v[172:175]\n"
"v_mfma_f32_16x16x32_bf16 v[172:175], a[144:147], v[188:191], v[172:175]\n"
"v_mfma_f32_16x16x32_bf16 v[172:175], a[148:151], v[164:167], v[172:175]\n"
"v_mfma_f32_16x16x32_bf16 v[172:175], a[152:155], v[192:195], v[172:175]\n"
"v_mfma_f32_16x16x32_bf16 v[172:175], a[156:159], v[196:199], v[172:175]\n"
"v_mfma_f32_16x16x32_bf16 v[200:203], a[188:191], v[196:199], v[156:159]\n"
"v_mfma_f32_16x16x32_bf16 v[200:203], a[184:187], v[192:195], v[200:203]\n"
"v_mfma_f32_16x16x32_bf16 v[200:203], a[180:183], v[164:167], v[200:203]\n"
"v_mfma_f32_16x16x32_bf16 v[200:203], a[176:179], v[188:191], v[200:203]\n"
"v_mfma_f32_16x16x32_bf16 v[200:203], a[172:175], v[184:187], v[200:203]\n"
"v_mfma_f32_16x16x32_bf16 v[200:203], a[168:171], v[168:171], v[200:203]\n"
"s_nop 7\n"
"s_branch .Lmk_p113r\n"
".Lmk_exit:\n"
"v_xor_b32_e32 v255, 32, v15\n"
"ds_read_b128 v[36:39], v2 offset:17408\n"
"ds_read_b128 v[40:43], v15 offset:16384\n"
"ds_read_b128 v[52:55], v255 offset:16640\n"
"v_xor_b32_e32 v255, 32, v17\n"
"ds_read_b128 v[60:63], v2 offset:16384\n"
"ds_read_b128 v[64:67], v17 offset:15360\n"
"ds_read_b128 v[68:71], v255 offset:15616\n"
"v_xor_b32_e32 v255, 32, v16\n"
"ds_read_b64_tr_b16 v[72:73], v16 offset:16384\n"
"ds_read_b64_tr_b16 v[74:75], v255 offset:16640\n"
"ds_read_b64_tr_b16 v[76:77], v255 offset:16384\n"
"ds_read_b64_tr_b16 v[78:79], v16 offset:16640\n"
"v_mov_b32_e32 v80, 0x3f803f80\n"
"v_mov_b32_e32 v81, 0x3f803f80\n"
"v_mov_b32_e32 v82, 0x3f803f80\n"
"v_mov_b32_e32 v83, 0x3f803f80\n"
"v_xor_b32_e32 v255, 32, v16\n"
"ds_read_b64_tr_b16 v[84:85], v16 offset:15360\n"
"ds_read_b64_tr_b16 v[86:87], v255 offset:15616\n"
"s_waitcnt lgkmcnt(13)\n"
"ds_read_b64_tr_b16 v[96:97], v255 offset:15360\n"
"ds_read_b64_tr_b16 v[98:99], v16 offset:15616\n"
"v_mov_b32_e32 v100, 0x3f803f80\n"
"v_mov_b32_e32 v101, 0x3f803f80\n"
"v_mov_b32_e32 v102, 0x3f803f80\n"
"v_mov_b32_e32 v103, 0x3f803f80\n"
"s_waitcnt lgkmcnt(11)\n"
"v_mfma_f32_16x16x32_bf16 v[104:107], v[40:43], v[36:39], v[104:107]\n"
"v_mfma_f32_16x16x32_bf16 v[124:127], v[52:55], v[36:39], v[124:127]\n"
"s_waitcnt lgkmcnt(8)\n"
"v_mfma_f32_16x16x32_bf16 v[88:91], v[64:67], v[60:63], v[88:91]\n"
"v_mfma_f32_16x16x32_bf16 v[176:179], v[68:71], v[60:63], v[176:179]\n"
"s_nop 3\n"
"v_max_f32_e32 v255, v104, v105\n"
"s_nop 1\n"
"v_max_f32_e32 v254, v88, v89\n"
"v_max3_f32 v255, v255, v106, v107\n"
"v_max3_f32 v254, v254, v90, v91\n"
"v_max3_f32 v255, v255, v124, v125\n"
"v_max3_f32 v254, v254, v176, v177\n"
"v_max3_f32 v255, v255, v126, v127\n"
"v_max3_f32 v254, v254, v178, v179\n"
"v_mov_b32_e32 v253, v255\n"
"v_mov_b32_e32 v252, v254\n"
"s_nop 0\n"
"v_permlane16_swap_b32_e32 v255, v253\n"
"v_permlane16_swap_b32_e32 v254, v252\n"
"v_max_f32_e32 v255, v255, v253\n"
"v_max_f32_e32 v254, v254, v252\n"
"v_mov_b32_e32 v253, v255\n"
"v_mov_b32_e32 v252, v254\n"
"s_nop 0\n"
"v_permlane32_swap_b32_e32 v255, v253\n"
"v_permlane32_swap_b32_e32 v254, v252\n"
"v_max_f32_e32 v255, v255, v253\n"
"v_max_f32_e32 v254, v254, v252\n"
"v_sub_f32_e32 v104, v104, v255\n"
"v_sub_f32_e32 v105, v105, v255\n"
"v_sub_f32_e32 v106, v106, v255\n"
"v_sub_f32_e32 v107, v107, v255\n"
"v_sub_f32_e32 v124, v124, v255\n"
"v_sub_f32_e32 v125, v125, v255\n"
"v_sub_f32_e32 v126, v126, v255\n"
"v_sub_f32_e32 v127, v127, v255\n"
"v_exp_f32_e32 v104, v104\n"
"v_exp_f32_e32 v105, v105\n"
"v_exp_f32_e32 v106, v106\n"
"v_exp_f32_e32 v107, v107\n"
"v_exp_f32_e32 v124, v124\n"
"v_exp_f32_e32 v125, v125\n"
"v_exp_f32_e32 v126, v126\n"
"v_exp_f32_e32 v127, v127\n"
"v_cvt_pk_bf16_f32 v36, v104, v105\n"
"v_cvt_pk_bf16_f32 v37, v106, v107\n"
"v_cvt_pk_bf16_f32 v38, v124, v125\n"
"v_cvt_pk_bf16_f32 v39, v126, v127\n"
"s_waitcnt lgkmcnt(4)\n"
"v_sub_f32_e32 v88, v88, v254\n"
"v_sub_f32_e32 v89, v89, v254\n"
"v_sub_f32_e32 v90, v90, v254\n"
"v_sub_f32_e32 v91, v91, v254\n"
"v_sub_f32_e32 v176, v176, v254\n"
"v_sub_f32_e32 v177, v177, v254\n"
"v_sub_f32_e32 v178, v178, v254\n"
"v_mfma_f32_16x16x32_bf16 v[40:43], v[80:83], v[36:39], 0\n"
"v_sub_f32_e32 v179, v179, v254\n"
"v_exp_f32_e32 v88, v88\n"
"v_exp_f32_e32 v89, v89\n"
"v_exp_f32_e32 v90, v90\n"
"v_mfma_f32_16x16x32_bf16 v[52:55], v[72:75], v[36:39], 0\n"
"v_exp_f32_e32 v91, v91\n"
"v_exp_f32_e32 v176, v176\n"
"v_exp_f32_e32 v177, v177\n"
"v_exp_f32_e32 v178, v178\n"
"v_mfma_f32_16x16x32_bf16 v[60:63], v[76:79], v[36:39], 0\n"
"v_exp_f32_e32 v179, v179\n"
"v_cvt_pk_bf16_f32 v36, v88, v89\n"
"v_cvt_pk_bf16_f32 v37, v90, v91\n"
"v_cvt_pk_bf16_f32 v38, v176, v177\n"
"v_cvt_pk_bf16_f32 v39, v178, v179\n"
"s_waitcnt lgkmcnt(0)\n"
"v_rcp_f32_e32 v40, v40\n"
"s_nop 0\n"
"v_mul_f32_e32 v52, v52, v40\n"
"v_mul_f32_e32 v53, v53, v40\n"
"v_mfma_f32_16x16x32_bf16 v[64:67], v[100:103], v[36:39], 0\n"
"v_mul_f32_e32 v54, v54, v40\n"
"v_mul_f32_e32 v55, v55, v40\n"
"v_mul_f32_e32 v60, v60, v40\n"
"v_mfma_f32_16x16x32_bf16 v[68:71], v[84:87], v[36:39], 0\n"
"v_mul_f32_e32 v61, v61, v40\n"
"v_mul_f32_e32 v62, v62, v40\n"
"v_mul_f32_e32 v63, v63, v40\n"
"v_cvt_pk_bf16_f32 v40, v52, v53\n"
"v_mfma_f32_16x16x32_bf16 v[72:75], v[96:99], v[36:39], 0\n"
"v_cvt_pk_bf16_f32 v41, v54, v55\n"
"v_cvt_pk_bf16_f32 v42, v60, v61\n"
"v_cvt_pk_bf16_f32 v43, v62, v63\n"
"s_add_u32 s97, s60, 0x408000\n"
"buffer_store_dwordx4 v[40:43], v0, s[4:7], s97 offen sc1\n"
"v_rcp_f32_e32 v64, v64\n"
"s_nop 0\n"
"v_mul_f32_e32 v68, v68, v64\n"
"v_mul_f32_e32 v69, v69, v64\n"
"v_mul_f32_e32 v70, v70, v64\n"
"v_mul_f32_e32 v71, v71, v64\n"
"v_mul_f32_e32 v72, v72, v64\n"
"v_mul_f32_e32 v73, v73, v64\n"
"v_mul_f32_e32 v74, v74, v64\n"
"v_mul_f32_e32 v75, v75, v64\n"
"v_cvt_pk_bf16_f32 v36, v68, v69\n"
"v_cvt_pk_bf16_f32 v37, v70, v71\n"
"v_cvt_pk_bf16_f32 v38, v72, v73\n"
"v_cvt_pk_bf16_f32 v39, v74, v75\n"
"s_add_u32 s97, s60, 0x400000\n"
"buffer_store_dwordx4 v[36:39], v0, s[4:7], s97 offen sc1\n"
";\n"
"s_waitcnt vmcnt(23)\n"
"; @B G2 b.0\n"
"s_nop 0\n"
"v_mfma_f32_16x16x32_bf16 v[36:39], a[128:131], v[120:123], 0\n"
"s_add_u32 s97, s58, 0x440000\n"
"buffer_load_dwordx4 v[40:43], v0, s[4:7], s97 offen sc1\n"
"v_mfma_f32_16x16x32_bf16 v[36:39], a[132:135], v[136:139], v[36:39]\n"
"s_waitcnt vmcnt(22)\n"
"v_mfma_f32_16x16x32_bf16 v[36:39], a[136:139], v[144:147], v[36:39]\n"
"v_mfma_f32_16x16x32_bf16 v[36:39], a[140:143], v[192:195], v[36:39]\n"
"s_waitcnt vmcnt(20)\n"
"v_mfma_f32_16x16x32_bf16 v[36:39], a[144:147], v[196:199], v[36:39]\n"
"buffer_load_dwordx4 v[52:55], v0, s[4:7], s97 offen offset:1024 sc1\n"
"v_mfma_f32_16x16x32_bf16 v[36:39], a[148:151], v[188:191], v[36:39]\n"
"s_waitcnt vmcnt(19)\n"
"v_mfma_f32_16x16x32_bf16 v[36:39], a[152:155], v[108:111], v[36:39]\n"
"v_mfma_f32_16x16x32_bf16 v[36:39], a[156:159], v[92:95], v[36:39]\n"
"v_mfma_f32_16x16x32_bf16 v[60:63], a[188:191], v[92:95], 0\n"
"buffer_load_dwordx4 v[64:67], v0, s[4:7], s97 offen offset:2048 sc1\n"
"v_mfma_f32_16x16x32_bf16 v[60:63], a[184:187], v[108:111], v[60:63]\n"
"v_mfma_f32_16x16x32_bf16 v[60:63], a[180:183], v[188:191], v[60:63]\n"
"v_mfma_f32_16x16x32_bf16 v[60:63], a[176:179], v[196:199], v[60:63]\n"
"v_mfma_f32_16x16x32_bf16 v[60:63], a[172:175], v[192:195], v[60:63]\n"
"s_nop 1\n"
"v_cmp_u_f32_e32 vcc, v36, v36\n"
"v_mfma_f32_16x16x32_bf16 v[60:63], a[168:171], v[144:147], v[60:63]\n"
"s_cbranch_vccnz .Lmk_p55s\n"
".Lmk_p55r:\n"
"v_mfma_f32_16x16x32_bf16 v[60:63], a[164:167], v[136:139], v[60:63]\n"
"ds_write_b128 v3, v[36:39]\n"
"v_mfma_f32_16x16x32_bf16 v[60:63], a[160:163], v[120:123], v[60:63]\n"
"buffer_load_dwordx4 v[36:39], v0, s[4:7], s97 offen offset:3072 sc1\n"
"v_mfma_f32_16x16x32_bf16 v[68:71], a[192:195], v[120:123], 0\n"
"v_bfe_u32 v255, v0, 4, 4\n"
"v_bfe_u32 v254, v0, 9, 1\n"
"v_mfma_f32_16x16x32_bf16 v[68:71], a[196:199], v[136:139], v[68:71]\n"
"v_and_b32_e32 v253, 7, v255\n"
"v_xor_b32_e32 v254, v254, v253\n"
"v_mfma_f32_16x16x32_bf16 v[68:71], a[200:203], v[144:147], v[68:71]\n"
"v_lshlrev_b32_e32 v255, 7, v255\n"
"v_lshl_add_u32 v255, v254, 4, v255\n"
"v_mfma_f32_16x16x32_bf16 v[68:71], a[204:207], v[192:195], v[68:71]\n"
"buffer_load_dwordx4 v[72:75], v1, s[4:7], s97 offen sc1\n"
"v_mfma_f32_16x16x32_bf16 v[68:71], a[208:211], v[196:199], v[68:71]\n"
"v_bfe_u32 v254, v0, 8, 1\n"
"v_lshl_add_u32 v255, v254, 3, v255\n"
"v_mfma_f32_16x16x32_bf16 v[68:71], a[212:215], v[188:191], v[68:71]\n"
"ds_write_b128 v3, v[60:63] offset:1024\n"
"v_mfma_f32_16x16x32_bf16 v[68:71], a[216:219], v[108:111], v[68:71]\n"
"v_add_u32_e32 v255, s77, v255\n"
"v_bfe_u32 v253, v0, 7, 3\n"
"v_mfma_f32_16x16x32_bf16 v[68:71], a[220:223], v[92:95], v[68:71]\n"
"buffer_load_dwordx4 v[60:63], v1, s[4:7], s97 offen offset:1024 sc1\n"
"v_mfma_f32_16x16x32_bf16 v[76:79], a[252:255], v[92:95], 0\n"
"v_bfe_u32 v254, v0, 4, 3\n"
"v_xor_b32_e32 v254, v254, v253\n"
"v_mfma_f32_16x16x32_bf16 v[76:79], a[248:251], v[108:111], v[76:79]\n"
"v_lshlrev_b32_e32 v253, 11, v253\n"
"v_lshl_add_u32 v253, v254, 4, v253\n"
"v_mfma_f32_16x16x32_bf16 v[76:79], a[244:247], v[188:191], v[76:79]\n"
"v_mfma_f32_16x16x32_bf16 v[76:79], a[240:243], v[196:199], v[76:79]\n"
"buffer_load_dwordx4 v[80:83], v1, s[4:7], s97 offen offset:2048 sc1\n"
"v_mfma_f32_16x16x32_bf16 v[76:79], a[236:239], v[192:195], v[76:79]\n"
"v_mfma_f32_16x16x32_bf16 v[76:79], a[232:235], v[144:147], v[76:79]\n"
"ds_write_b128 v3, v[68:71] offset:2048\n"
"v_mfma_f32_16x16x32_bf16 v[76:79], a[228:231], v[136:139], v[76:79]\n"
"v_mfma_f32_16x16x32_bf16 v[76:79], a[224:227], v[120:123], v[76:79]\n"
"s_waitcnt vmcnt(20)\n"
"buffer_load_dwordx4 v[68:71], v1, s[4:7], s97 offen offset:3072 sc1\n"
"; @B G2 b.1\n"
"v_mfma_f32_16x16x32_bf16 v[84:87], a[128:131], v[148:151], 0\n"
"v_mfma_f32_16x16x32_bf16 v[84:87], a[132:135], v[180:183], v[84:87]\n"
"s_add_u32 s97, s59, 0x440000\n"
"v_mfma_f32_16x16x32_bf16 v[84:87], a[136:139], v[24:27], v[84:87]\n"
"v_mfma_f32_16x16x32_bf16 v[84:87], a[140:143], v[32:35], v[84:87]\n"
"s_waitcnt vmcnt(15)\n"
"buffer_load_dwordx4 v[96:99], v0, s[4:7], s97 offen sc1\n"
"v_mfma_f32_16x16x32_bf16 v[84:87], a[144:147], v[44:47], v[84:87]\n"
"v_mfma_f32_16x16x32_bf16 v[84:87], a[148:151], v[48:51], v[84:87]\n"
"ds_write_b128 v3, v[76:79] offset:3072\n"
"v_mfma_f32_16x16x32_bf16 v[84:87], a[152:155], v[28:31], v[84:87]\n"
"v_mfma_f32_16x16x32_bf16 v[84:87], a[156:159], v[56:59], v[84:87]\n"
"buffer_load_dwordx4 v[76:79], v0, s[4:7], s97 offen offset:1024 sc1\n"
"v_mfma_f32_16x16x32_bf16 v[100:103], a[188:191], v[56:59], 0\n"
"v_mfma_f32_16x16x32_bf16 v[100:103], a[184:187], v[28:31], v[100:103]\n"
"v_mfma_f32_16x16x32_bf16 v[100:103], a[180:183], v[48:51], v[100:103]\n"
"v_mfma_f32_16x16x32_bf16 v[100:103], a[176:179], v[44:47], v[100:103]\n"
"buffer_load_dwordx4 v[112:115], v0, s[4:7], s97 offen offset:2048 sc1\n"
"v_mfma_f32_16x16x32_bf16 v[100:103], a[172:175], v[32:35], v[100:103]\n"
"s_nop 0\n"
"v_cmp_u_f32_e32 vcc, v84, v84\n"
"v_mfma_f32_16x16x32_bf16 v[100:103], a[168:171], v[24:27], v[100:103]\n"
"s_cbranch_vccnz .Lmk_p56s\n"
".Lmk_p56r:\n"
"v_mfma_f32_16x16x32_bf16 v[100:103], a[164:167], v[180:183], v[100:103]\n"
"ds_write_b128 v3, v[84:87] offset:6144\n"
"v_mfma_f32_16x16x32_bf16 v[100:103], a[160:163], v[148:151], v[100:103]\n"
"buffer_load_dwordx4 v[84:87], v0, s[4:7], s97 offen offset:3072 sc1\n"
"v_mfma_f32_16x16x32_bf16 v[116:119], a[192:195], v[148:151], 0\n"
"v_mfma_f32_16x16x32_bf16 v[116:119], a[196:199], v[180:183], v[116:119]\n"
"v_mfma_f32_16x16x32_bf16 v[116:119], a[200:203], v[24:27], v[116:119]\n"
"v_mfma_f32_16x16x32_bf16 v[116:119], a[204:207], v[32:35], v[116:119]\n"
"buffer_load_dwordx4 v[128:131], v1, s[4:7], s97 offen sc1\n"
"v_mfma_f32_16x16x32_bf16 v[116:119], a[208:211], v[44:47], v[116:119]\n"
"ds_read_b128 v[132:135], v2 offset:8192\n"
"ds_read_b128 v[140:143], v2 offset:9216\n"
"v_mfma_f32_16x16x32_bf16 v[116:119], a[212:215], v[48:51], v[116:119]\n"
"ds_write_b128 v3, v[100:103] offset:7168\n"
"v_mfma_f32_16x16x32_bf16 v[116:119], a[216:219], v[28:31], v[116:119]\n"
"v_mfma_f32_16x16x32_bf16 v[116:119], a[220:223], v[56:59], v[116:119]\n"
"buffer_load_dwordx4 v[100:103], v1, s[4:7], s97 offen offset:1024 sc1\n"
"v_mfma_f32_16x16x32_bf16 v[152:155], a[252:255], v[56:59], 0\n"
"v_mfma_f32_16x16x32_bf16 v[152:155], a[248:251], v[28:31], v[152:155]\n"
"s_waitcnt lgkmcnt(1)\n"
"v_lshlrev_b32_e32 v254, 16, v132\n"
"v_and_b32_e32 v252, 0xffff0000, v132\n"
"v_lshlrev_b32_e32 v251, 16, v133\n"
"v_mfma_f32_16x16x32_bf16 v[152:155], a[244:247], v[48:51], v[152:155]\n"
"v_and_b32_e32 v250, 0xffff0000, v133\n"
"v_lshlrev_b32_e32 v249, 16, v134\n"
"v_and_b32_e32 v248, 0xffff0000, v134\n"
"v_mfma_f32_16x16x32_bf16 v[152:155], a[240:243], v[44:47], v[152:155]\n"
"buffer_load_dwordx4 v[156:159], v1, s[4:7], s97 offen offset:2048 sc1\n"
"v_lshlrev_b32_e32 v247, 16, v135\n"
"v_mfma_f32_16x16x32_bf16 v[152:155], a[236:239], v[32:35], v[152:155]\n"
"v_and_b32_e32 v246, 0xffff0000, v135\n"
"v_lshlrev_b32_e32 v245, 16, v140\n"
"v_and_b32_e32 v244, 0xffff0000, v140\n"
"v_mfma_f32_16x16x32_bf16 v[152:155], a[232:235], v[24:27], v[152:155]\n"
"ds_write_b128 v3, v[116:119] offset:8192\n"
"v_mfma_f32_16x16x32_bf16 v[152:155], a[228:231], v[180:183], v[152:155]\n"
"v_lshlrev_b32_e32 v243, 16, v141\n"
"v_and_b32_e32 v242, 0xffff0000, v141\n"
"v_lshlrev_b32_e32 v241, 16, v142\n"
"v_mfma_f32_16x16x32_bf16 v[152:155], a[224:227], v[148:151], v[152:155]\n"
"s_waitcnt vmcnt(11)\n"
"buffer_load_dwordx4 v[116:119], v1, s[4:7], s97 offen offset:3072 sc1\n"
"; @B G2 b.2\n"
"v_mfma_f32_16x16x32_bf16 v[132:135], a[128:131], v[40:43], 0\n"
"v_and_b32_e32 v240, 0xffff0000, v142\n"
"v_lshlrev_b32_e32 v239, 16, v143\n"
"v_and_b32_e32 v238, 0xffff0000, v143\n"
"v_mfma_f32_16x16x32_bf16 v[132:135], a[132:135], v[52:55], v[132:135]\n"
"v_mfma_f32_16x16x32_bf16 v[132:135], a[136:139], v[64:67], v[132:135]\n"
"v_mfma_f32_16x16x32_bf16 v[132:135], a[140:143], v[36:39], v[132:135]\n"
"s_waitcnt vmcnt(8)\n"
"v_mfma_f32_16x16x32_bf16 v[132:135], a[144:147], v[72:75], v[132:135]\n"
"v_mfma_f32_16x16x32_bf16 v[132:135], a[148:151], v[60:63], v[132:135]\n"
"ds_write_b128 v3, v[152:155] offset:9216\n"
"v_mfma_f32_16x16x32_bf16 v[132:135], a[152:155], v[80:83], v[132:135]\n"
"v_mfma_f32_16x16x32_bf16 v[132:135], a[156:159], v[68:71], v[132:135]\n"
"v_mfma_f32_16x16x32_bf16 v[140:143], a[188:191], v[68:71], 0\n"
"v_mfma_f32_16x16x32_bf16 v[140:143], a[184:187], v[80:83], v[140:143]\n"
"s_waitcnt lgkmcnt(0)\n"
"s_barrier\n"
"v_mfma_f32_16x16x32_bf16 v[140:143], a[180:183], v[60:63], v[140:143]\n"
"ds_read_b128 v[152:155], v4\n"
"ds_read_b128 v[160:163], v5\n"
"v_mfma_f32_16x16x32_bf16 v[140:143], a[176:179], v[72:75], v[140:143]\n"
"v_mfma_f32_16x16x32_bf16 v[140:143], a[172:175], v[36:39], v[140:143]\n"
"s_add_u32 s97, s56, 0x400000\n"
"v_cmp_u_f32_e32 vcc, v132, v132\n"
"v_mfma_f32_16x16x32_bf16 v[140:143], a[168:171], v[64:67], v[140:143]\n"
"s_cbranch_vccnz .Lmk_p57s\n"
".Lmk_p57r:\n"
"v_mfma_f32_16x16x32_bf16 v[140:143], a[164:167], v[52:55], v[140:143]\n"
"ds_write_b128 v3, v[132:135] offset:12288\n"
"v_mfma_f32_16x16x32_bf16 v[140:143], a[160:163], v[40:43], v[140:143]\n"
"buffer_load_dwordx4 v[132:135], v0, s[4:7], s97 offen sc1\n"
"s_waitcnt lgkmcnt(1)\n"
"v_mfma_f32_16x16x32_bf16 v[164:167], a[192:195], v[40:43], 0\n"
"ds_read_b128 v[168:171], v4 offset:1024\n"
"ds_read_b128 v[172:175], v5 offset:1024\n"
"v_add_f32_e32 v152, v152, v254\n"
"v_mfma_f32_16x16x32_bf16 v[164:167], a[196:199], v[52:55], v[164:167]\n"
"v_add_f32_e32 v153, v153, v252\n"
"v_add_f32_e32 v154, v154, v251\n"
"v_add_f32_e32 v155, v155, v250\n"
"v_mfma_f32_16x16x32_bf16 v[164:167], a[200:203], v[64:67], v[164:167]\n"
"buffer_load_dwordx4 v[184:187], v0, s[4:7], s97 offen offset:1024 sc1\n"
"v_add_f32_e32 v152, v152, v160\n"
"v_mfma_f32_16x16x32_bf16 v[164:167], a[204:207], v[36:39], v[164:167]\n"
"v_add_f32_e32 v153, v153, v161\n"
"v_add_f32_e32 v154, v154, v162\n"
"v_add_f32_e32 v155, v155, v163\n"
"v_mfma_f32_16x16x32_bf16 v[164:167], a[208:211], v[72:75], v[164:167]\n"
"v_mfma_f32_16x16x32_bf16 v[164:167], a[212:215], v[60:63], v[164:167]\n"
"ds_write_b128 v3, v[140:143] offset:13312\n"
"v_mfma_f32_16x16x32_bf16 v[164:167], a[216:219], v[80:83], v[164:167]\n"
"buffer_load_dwordx4 v[140:143], v0, s[4:7], s97 offen offset:2048 sc1\n"
"s_waitcnt lgkmcnt(1)\n"
"v_mfma_f32_16x16x32_bf16 v[164:167], a[220:223], v[68:71], v[164:167]\n"
"ds_read_b128 v[160:163], v4 offset:2048\n"
"ds_read_b128 v[200:203], v5 offset:2048\n"
"v_add_f32_e32 v168, v168, v249\n"
"v_mfma_f32_16x16x32_bf16 v[204:207], a[252:255], v[68:71], 0\n"
"v_add_f32_e32 v169, v169, v248\n"
"v_add_f32_e32 v170, v170, v247\n"
"v_add_f32_e32 v171, v171, v246\n"
"v_mfma_f32_16x16x32_bf16 v[204:207], a[248:251], v[80:83], v[204:207]\n"
"buffer_load_dwordx4 v[68:71], v0, s[4:7], s97 offen offset:3072 sc1\n"
"v_add_f32_e32 v168, v168, v172\n"
"v_mfma_f32_16x16x32_bf16 v[204:207], a[244:247], v[60:63], v[204:207]\n"
"v_add_f32_e32 v169, v169, v173\n"
"v_add_f32_e32 v170, v170, v174\n"
"v_add_f32_e32 v171, v171, v175\n"
"v_mfma_f32_16x16x32_bf16 v[204:207], a[240:243], v[72:75], v[204:207]\n"
"v_mfma_f32_16x16x32_bf16 v[204:207], a[236:239], v[36:39], v[204:207]\n"
"buffer_load_dwordx4 v[36:39], v1, s[4:7], s97 offen sc1\n"
"s_waitcnt lgkmcnt(0)\n"
"v_mfma_f32_16x16x32_bf16 v[204:207], a[232:235], v[64:67], v[204:207]\n"
"ds_write_b128 v3, v[164:167] offset:14336\n"
"v_mfma_f32_16x16x32_bf16 v[204:207], a[228:231], v[52:55], v[204:207]\n"
"ds_read_b128 v[52:55], v4 offset:3072\n"
"ds_read_b128 v[60:63], v5 offset:3072\n"
"v_add_f32_e32 v160, v160, v245\n"
"v_mfma_f32_16x16x32_bf16 v[204:207], a[224:227], v[40:43], v[204:207]\n"
"s_waitcnt vmcnt(9)\n"
"buffer_load_dwordx4 v[40:43], v1, s[4:7], s97 offen offset:1024 sc1\n"
"; @B G2 b.3\n"
"v_mfma_f32_16x16x32_bf16 v[64:67], a[128:131], v[96:99], v[152:155]\n"
"v_add_f32_e32 v161, v161, v244\n"
"v_add_f32_e32 v162, v162, v243\n"
"v_add_f32_e32 v163, v163, v242\n"
"v_mfma_f32_16x16x32_bf16 v[64:67], a[132:135], v[76:79], v[64:67]\n"
"v_add_f32_e32 v160, v160, v200\n"
"v_add_f32_e32 v161, v161, v201\n"
"v_add_f32_e32 v162, v162, v202\n"
"v_mfma_f32_16x16x32_bf16 v[64:67], a[136:139], v[112:115], v[64:67]\n"
"buffer_load_dwordx4 v[72:75], v1, s[4:7], s97 offen offset:2048 sc1\n"
"v_add_f32_e32 v163, v163, v203\n"
"v_mfma_f32_16x16x32_bf16 v[64:67], a[140:143], v[84:87], v[64:67]\n"
"s_waitcnt vmcnt(7)\n"
"s_waitcnt lgkmcnt(0)\n"
"v_add_f32_e32 v52, v52, v241\n"
"v_add_f32_e32 v53, v53, v240\n"
"v_mfma_f32_16x16x32_bf16 v[64:67], a[144:147], v[128:131], v[64:67]\n"
"v_add_f32_e32 v54, v54, v239\n"
"v_add_f32_e32 v55, v55, v238\n"
"v_add_f32_e32 v52, v52, v60\n"
"v_mfma_f32_16x16x32_bf16 v[64:67], a[148:151], v[100:103], v[64:67]\n"
"ds_write_b128 v3, v[204:207] offset:15360\n"
"v_mfma_f32_16x16x32_bf16 v[64:67], a[152:155], v[156:159], v[64:67]\n"
"buffer_load_dwordx4 v[80:83], v1, s[4:7], s97 offen offset:3072 sc1\n"
"v_add_f32_e32 v53, v53, v61\n"
"v_mfma_f32_16x16x32_bf16 v[64:67], a[156:159], v[116:119], v[64:67]\n"
"s_add_u32 s97, s57, 0x400000\n"
"v_add_f32_e32 v54, v54, v62\n"
"v_add_f32_e32 v55, v55, v63\n"
"v_mfma_f32_16x16x32_bf16 v[60:63], a[188:191], v[116:119], v[168:171]\n"
"v_mfma_f32_16x16x32_bf16 v[60:63], a[184:187], v[156:159], v[60:63]\n"
"s_waitcnt lgkmcnt(0)\n"
"s_barrier\n"
"v_mfma_f32_16x16x32_bf16 v[60:63], a[180:183], v[100:103], v[60:63]\n"
"buffer_load_dwordx4 v[164:167], v0, s[4:7], s97 offen sc1\n"
"v_mfma_f32_16x16x32_bf16 v[60:63], a[176:179], v[128:131], v[60:63]\n"
"ds_read_b128 v[172:175], v6\n"
"ds_read_b128 v[200:203], v6 offset:1024\n"
"v_mfma_f32_16x16x32_bf16 v[60:63], a[172:175], v[84:87], v[60:63]\n"
"ds_read_b128 v[204:207], v6 offset:2048\n"
"v_cmp_u_f32_e32 vcc, v64, v64\n"
"v_mfma_f32_16x16x32_bf16 v[60:63], a[168:171], v[112:115], v[60:63]\n"
"s_cbranch_vccnz .Lmk_p58s\n"
".Lmk_p58r:\n"
"ds_read_b128 v[152:155], v6 offset:3072\n"
"v_mfma_f32_16x16x32_bf16 v[60:63], a[164:167], v[76:79], v[60:63]\n"
"buffer_load_dwordx4 v[168:171], v0, s[4:7], s97 offen offset:1024 sc1\n"
"v_mfma_f32_16x16x32_bf16 v[60:63], a[160:163], v[96:99], v[60:63]\n"
"v_mfma_f32_16x16x32_bf16 v[160:163], a[192:195], v[96:99], v[160:163]\n"
"v_mfma_f32_16x16x32_bf16 v[160:163], a[196:199], v[76:79], v[160:163]\n"
"buffer_load_dwordx4 v[208:211], v0, s[4:7], s97 offen offset:2048 sc1\n"
"v_mfma_f32_16x16x32_bf16 v[160:163], a[200:203], v[112:115], v[160:163]\n"
"v_mfma_f32_16x16x32_bf16 v[160:163], a[204:207], v[84:87], v[160:163]\n"
"v_mfma_f32_16x16x32_bf16 v[160:163], a[208:211], v[128:131], v[160:163]\n"
"buffer_load_dwordx4 v[212:215], v0, s[4:7], s97 offen offset:3072 sc1\n"
"s_waitcnt lgkmcnt(0)\n"
"v_mfma_f32_16x16x32_bf16 v[160:163], a[212:215], v[100:103], v[160:163]\n"
"v_add_f32_e32 v172, v64, v172\n"
"v_add_f32_e32 v173, v65, v173\n"
"v_add_f32_e32 v174, v66, v174\n"
"v_mfma_f32_16x16x32_bf16 v[160:163], a[216:219], v[156:159], v[160:163]\n"
"v_add_f32_e32 v175, v67, v175\n"
"v_add_f32_e32 v200, v60, v200\n"
"v_add_f32_e32 v201, v61, v201\n"
"v_mfma_f32_16x16x32_bf16 v[160:163], a[220:223], v[116:119], v[160:163]\n"
"buffer_load_dwordx4 v[64:67], v1, s[4:7], s97 offen sc1\n"
"v_add_f32_e32 v202, v62, v202\n"
"v_mfma_f32_16x16x32_bf16 v[52:55], a[252:255], v[116:119], v[52:55]\n"
"v_add_f32_e32 v203, v63, v203\n"
"v_cvt_pk_bf16_f32 v60, v172, v173\n"
"v_cvt_pk_bf16_f32 v61, v174, v175\n"
"v_mfma_f32_16x16x32_bf16 v[52:55], a[248:251], v[156:159], v[52:55]\n"
"v_cvt_pk_bf16_f32 v62, v200, v201\n"
"v_cvt_pk_bf16_f32 v63, v202, v203\n"
"v_xor_b32_e32 v254, 32, v255\n"
"v_mfma_f32_16x16x32_bf16 v[52:55], a[244:247], v[100:103], v[52:55]\n"
"buffer_load_dwordx4 v[100:103], v1, s[4:7], s97 offen offset:1024 sc1\n"
"v_mfma_f32_16x16x32_bf16 v[52:55], a[240:243], v[128:131], v[52:55]\n"
"ds_write_b64 v255, v[60:61] offset:8192\n"
"v_mfma_f32_16x16x32_bf16 v[52:55], a[236:239], v[84:87], v[52:55]\n"
"ds_write_b64 v254, v[62:63] offset:8192\n"
"v_mfma_f32_16x16x32_bf16 v[52:55], a[232:235], v[112:115], v[52:55]\n"
"buffer_load_dwordx4 v[60:63], v1, s[4:7], s97 offen offset:2048 sc1\n"
"v_mfma_f32_16x16x32_bf16 v[52:55], a[228:231], v[76:79], v[52:55]\n"
"v_mfma_f32_16x16x32_bf16 v[52:55], a[224:227], v[96:99], v[52:55]\n"
"buffer_load_dwordx4 v[76:79], v1, s[4:7], s97 offen offset:3072 sc1\n"
"s_waitcnt vmcnt(14)\n"
"; @B G2 a.0\n"
"v_mfma_f32_16x16x32_bf16 v[84:87], a[128:131], v[132:135], 0\n"
"s_add_u32 s97, s58, 0x400000\n"
"buffer_load_dwordx4 v[96:99], v0, s[4:7], s97 offen sc1\n"
"v_mfma_f32_16x16x32_bf16 v[84:87], a[132:135], v[184:187], v[84:87]\n"
"s_waitcnt vmcnt(13)\n"
"v_add_f32_e32 v204, v160, v204\n"
"v_add_f32_e32 v205, v161, v205\n"
"v_mfma_f32_16x16x32_bf16 v[84:87], a[136:139], v[140:143], v[84:87]\n"
"v_add_f32_e32 v206, v162, v206\n"
"v_add_f32_e32 v207, v163, v207\n"
"v_mfma_f32_16x16x32_bf16 v[84:87], a[140:143], v[68:71], v[84:87]\n"
"s_waitcnt vmcnt(11)\n"
"buffer_load_dwordx4 v[112:115], v0, s[4:7], s97 offen offset:1024 sc1\n"
"v_mfma_f32_16x16x32_bf16 v[84:87], a[144:147], v[36:39], v[84:87]\n"
"v_add_f32_e32 v152, v52, v152\n"
"v_add_f32_e32 v153, v53, v153\n"
"v_mfma_f32_16x16x32_bf16 v[84:87], a[148:151], v[40:43], v[84:87]\n"
"s_waitcnt vmcnt(10)\n"
"v_add_f32_e32 v154, v54, v154\n"
"v_add_f32_e32 v155, v55, v155\n"
"v_mfma_f32_16x16x32_bf16 v[84:87], a[152:155], v[72:75], v[84:87]\n"
"buffer_load_dwordx4 v[52:55], v0, s[4:7], s97 offen offset:2048 sc1\n"
"v_mfma_f32_16x16x32_bf16 v[84:87], a[156:159], v[80:83], v[84:87]\n"
"v_cvt_pk_bf16_f32 v116, v204, v205\n"
"v_cvt_pk_bf16_f32 v117, v206, v207\n"
"v_mfma_f32_16x16x32_bf16 v[128:131], a[188:191], v[80:83], 0\n"
"v_cvt_pk_bf16_f32 v118, v152, v153\n"
"v_cvt_pk_bf16_f32 v119, v154, v155\n"
"v_mfma_f32_16x16x32_bf16 v[128:131], a[184:187], v[72:75], v[128:131]\n"
"buffer_load_dwordx4 v[152:155], v0, s[4:7], s97 offen offset:3072 sc1\n"
"v_mfma_f32_16x16x32_bf16 v[128:131], a[180:183], v[40:43], v[128:131]\n"
"v_xor_b32_e32 v254, 64, v255\n"
"v_xor_b32_e32 v252, 0x60, v255\n"
"v_mfma_f32_16x16x32_bf16 v[128:131], a[176:179], v[36:39], v[128:131]\n"
"ds_write_b64 v254, v[116:117] offset:8192\n"
"v_mfma_f32_16x16x32_bf16 v[128:131], a[172:175], v[68:71], v[128:131]\n"
"v_cmp_u_f32_e32 vcc, v84, v84\n"
"v_mfma_f32_16x16x32_bf16 v[128:131], a[168:171], v[140:143], v[128:131]\n"
"s_cbranch_vccnz .Lmk_p59s\n"
".Lmk_p59r:\n"
"v_mfma_f32_16x16x32_bf16 v[128:131], a[164:167], v[184:187], v[128:131]\n"
"ds_write_b128 v3, v[84:87]\n"
"v_mfma_f32_16x16x32_bf16 v[128:131], a[160:163], v[132:135], v[128:131]\n"
"buffer_load_dwordx4 v[84:87], v1, s[4:7], s97 offen sc1\n"
"v_mfma_f32_16x16x32_bf16 v[156:159], a[192:195], v[132:135], 0\n"
"ds_write_b64 v252, v[118:119] offset:8192\n"
"v_mfma_f32_16x16x32_bf16 v[156:159], a[196:199], v[184:187], v[156:159]\n"
"ds_read_b128 v[116:119], v2 offset:8192\n"
"ds_read_b128 v[160:163], v2 offset:9216\n"
"v_mfma_f32_16x16x32_bf16 v[156:159], a[200:203], v[140:143], v[156:159]\n"
"buffer_load_dwordx4 v[172:175], v1, s[4:7], s97 offen offset:1024 sc1\n"
"v_mfma_f32_16x16x32_bf16 v[156:159], a[204:207], v[68:71], v[156:159]\n"
"v_bfe_u32 v255, v0, 4, 4\n"
"v_bfe_u32 v254, v0, 9, 1\n"
"v_mfma_f32_16x16x32_bf16 v[156:159], a[208:211], v[36:39], v[156:159]\n"
"v_and_b32_e32 v252, 7, v255\n"
"v_xor_b32_e32 v254, v254, v252\n"
"v_mfma_f32_16x16x32_bf16 v[156:159], a[212:215], v[40:43], v[156:159]\n"
"ds_write_b128 v3, v[128:131] offset:1024\n"
"v_mfma_f32_16x16x32_bf16 v[156:159], a[216:219], v[72:75], v[156:159]\n"
"buffer_load_dwordx4 v[128:131], v1, s[4:7], s97 offen offset:2048 sc1\n"
"v_mfma_f32_16x16x32_bf16 v[156:159], a[220:223], v[80:83], v[156:159]\n"
"s_waitcnt lgkmcnt(1)\n"
"v_lshlrev_b32_e32 v255, 7, v255\n"
"v_lshl_add_u32 v255, v254, 4, v255\n"
"v_mfma_f32_16x16x32_bf16 v[200:203], a[252:255], v[80:83], 0\n"
"v_bfe_u32 v254, v0, 8, 1\n"
"v_lshl_add_u32 v255, v254, 3, v255\n"
"v_mfma_f32_16x16x32_bf16 v[200:203], a[248:251], v[72:75], v[200:203]\n"
"buffer_load_dwordx4 v[72:75], v1, s[4:7], s97 offen offset:3072 sc1\n"
"v_mfma_f32_16x16x32_bf16 v[200:203], a[244:247], v[40:43], v[200:203]\n"
"s_add_u32 s97, s59, 0x400000\n"
"v_add_u32_e32 v255, s77, v255\n"
"v_bfe_u32 v252, v0, 7, 3\n"
"v_mfma_f32_16x16x32_bf16 v[200:203], a[240:243], v[36:39], v[200:203]\n"
"v_bfe_u32 v254, v0, 4, 3\n"
"v_xor_b32_e32 v254, v254, v252\n"
"v_mfma_f32_16x16x32_bf16 v[200:203], a[236:239], v[68:71], v[200:203]\n"
"buffer_load_dwordx4 v[36:39], v0, s[4:7], s97 offen sc1\n"
"v_mfma_f32_16x16x32_bf16 v[200:203], a[232:235], v[140:143], v[200:203]\n"
"ds_write_b128 v3, v[156:159] offset:2048\n"
"v_mfma_f32_16x16x32_bf16 v[200:203], a[228:231], v[184:187], v[200:203]\n"
"v_lshlrev_b32_e32 v252, 11, v252\n"
"v_lshl_add_u32 v252, v254, 4, v252\n"
"v_mfma_f32_16x16x32_bf16 v[200:203], a[224:227], v[132:135], v[200:203]\n"
"s_waitcnt vmcnt(13)\n"
"buffer_load_dwordx4 v[40:43], v0, s[4:7], s97 offen offset:1024 sc1\n"
"; @B G2 a.1\n"
"v_mfma_f32_16x16x32_bf16 v[68:71], a[128:131], v[164:167], 0\n"
"v_mfma_f32_16x16x32_bf16 v[68:71], a[132:135], v[168:171], v[68:71]\n"
"v_mfma_f32_16x16x32_bf16 v[68:71], a[136:139], v[208:211], v[68:71]\n"
"buffer_load_dwordx4 v[80:83], v0, s[4:7], s97 offen offset:2048 sc1\n"
"v_mfma_f32_16x16x32_bf16 v[68:71], a[140:143], v[212:215], v[68:71]\n"
"s_waitcnt vmcnt(11)\n"
"v_mfma_f32_16x16x32_bf16 v[68:71], a[144:147], v[64:67], v[68:71]\n"
"v_mfma_f32_16x16x32_bf16 v[68:71], a[148:151], v[100:103], v[68:71]\n"
"ds_write_b128 v3, v[200:203] offset:3072\n"
"v_mfma_f32_16x16x32_bf16 v[68:71], a[152:155], v[60:63], v[68:71]\n"
"buffer_load_dwordx4 v[132:135], v0, s[4:7], s97 offen offset:3072 sc1\n"
"v_mfma_f32_16x16x32_bf16 v[68:71], a[156:159], v[76:79], v[68:71]\n"
"v_mfma_f32_16x16x32_bf16 v[140:143], a[188:191], v[76:79], 0\n"
"v_mfma_f32_16x16x32_bf16 v[140:143], a[184:187], v[60:63], v[140:143]\n"
"buffer_load_dwordx4 v[156:159], v1, s[4:7], s97 offen sc1\n"
"v_mfma_f32_16x16x32_bf16 v[140:143], a[180:183], v[100:103], v[140:143]\n"
"v_mfma_f32_16x16x32_bf16 v[140:143], a[176:179], v[64:67], v[140:143]\n"
"v_mfma_f32_16x16x32_bf16 v[140:143], a[172:175], v[212:215], v[140:143]\n"
"s_nop 1\n"
"v_cmp_u_f32_e32 vcc, v68, v68\n"
"v_mfma_f32_16x16x32_bf16 v[140:143], a[168:171], v[208:211], v[140:143]\n"
"s_cbranch_vccnz .Lmk_p60s\n"
".Lmk_p60r:\n"
"v_mfma_f32_16x16x32_bf16 v[140:143], a[164:167], v[168:171], v[140:143]\n"
"ds_write_b128 v3, v[68:71] offset:6144\n"
"v_mfma_f32_16x16x32_bf16 v[140:143], a[160:163], v[164:167], v[140:143]\n"
"buffer_load_dwordx4 v[68:71], v1, s[4:7], s97 offen offset:1024 sc1\n"
"v_mfma_f32_16x16x32_bf16 v[184:187], a[192:195], v[164:167], 0\n"
"v_mfma_f32_16x16x32_bf16 v[184:187], a[196:199], v[168:171], v[184:187]\n"
"v_mfma_f32_16x16x32_bf16 v[184:187], a[200:203], v[208:211], v[184:187]\n"
"buffer_load_dwordx4 v[200:203], v1, s[4:7], s97 offen offset:2048 sc1\n"
"v_mfma_f32_16x16x32_bf16 v[184:187], a[204:207], v[212:215], v[184:187]\n"
"v_mfma_f32_16x16x32_bf16 v[184:187], a[208:211], v[64:67], v[184:187]\n"
"ds_read_b128 v[204:207], v2\n"
"ds_read_b128 v[216:219], v2 offset:1024\n"
"v_mfma_f32_16x16x32_bf16 v[184:187], a[212:215], v[100:103], v[184:187]\n"
"ds_write_b128 v3, v[140:143] offset:7168\n"
"v_mfma_f32_16x16x32_bf16 v[184:187], a[216:219], v[60:63], v[184:187]\n"
"buffer_load_dwordx4 v[140:143], v1, s[4:7], s97 offen offset:3072 sc1\n"
"v_mfma_f32_16x16x32_bf16 v[184:187], a[220:223], v[76:79], v[184:187]\n"
"v_mfma_f32_16x16x32_bf16 v[220:223], a[252:255], v[76:79], 0\n"
"v_mfma_f32_16x16x32_bf16 v[220:223], a[248:251], v[60:63], v[220:223]\n"
"s_waitcnt lgkmcnt(1)\n"
"v_lshlrev_b32_e32 v254, 16, v204\n"
"v_and_b32_e32 v251, 0xffff0000, v204\n"
"v_lshlrev_b32_e32 v250, 16, v205\n"
"v_mfma_f32_16x16x32_bf16 v[220:223], a[244:247], v[100:103], v[220:223]\n"
"v_and_b32_e32 v249, 0xffff0000, v205\n"
"v_lshlrev_b32_e32 v248, 16, v206\n"
"v_and_b32_e32 v247, 0xffff0000, v206\n"
"v_mfma_f32_16x16x32_bf16 v[220:223], a[240:243], v[64:67], v[220:223]\n"
"v_lshlrev_b32_e32 v246, 16, v207\n"
"v_and_b32_e32 v245, 0xffff0000, v207\n"
"v_lshlrev_b32_e32 v244, 16, v216\n"
"v_mfma_f32_16x16x32_bf16 v[220:223], a[236:239], v[212:215], v[220:223]\n"
"s_add_u32 s97, s74, 0x40000\n"
"buffer_store_dwordx4 v[116:119], v253, s[16:19], s97 offen sc1\n"
"v_mfma_f32_16x16x32_bf16 v[220:223], a[232:235], v[208:211], v[220:223]\n"
"ds_write_b128 v3, v[184:187] offset:8192\n"
"v_mfma_f32_16x16x32_bf16 v[220:223], a[228:231], v[168:171], v[220:223]\n"
"v_and_b32_e32 v243, 0xffff0000, v216\n"
"v_lshlrev_b32_e32 v242, 16, v217\n"
"v_and_b32_e32 v241, 0xffff0000, v217\n"
"v_mfma_f32_16x16x32_bf16 v[220:223], a[224:227], v[164:167], v[220:223]\n"
"s_waitcnt vmcnt(13)\n"
"v_lshlrev_b32_e32 v240, 16, v218\n"
"v_and_b32_e32 v239, 0xffff0000, v218\n"
"v_lshlrev_b32_e32 v238, 16, v219\n"
"; @B G2 a.2\n"
"v_mfma_f32_16x16x32_bf16 v[60:63], a[128:131], v[96:99], 0\n"
"v_and_b32_e32 v237, 0xffff0000, v219\n"
"v_mfma_f32_16x16x32_bf16 v[60:63], a[132:135], v[112:115], v[60:63]\n"
"v_mfma_f32_16x16x32_bf16 v[60:63], a[136:139], v[52:55], v[60:63]\n"
"v_mfma_f32_16x16x32_bf16 v[60:63], a[140:143], v[152:155], v[60:63]\n"
"s_waitcnt vmcnt(9)\n"
"v_mfma_f32_16x16x32_bf16 v[60:63], a[144:147], v[84:87], v[60:63]\n"
"v_mfma_f32_16x16x32_bf16 v[60:63], a[148:151], v[172:175], v[60:63]\n"
"ds_write_b128 v3, v[220:223] offset:9216\n"
"v_mfma_f32_16x16x32_bf16 v[60:63], a[152:155], v[128:131], v[60:63]\n"
"s_add_u32 s97, s74, 0x44000\n"
"v_mfma_f32_16x16x32_bf16 v[60:63], a[156:159], v[72:75], v[60:63]\n"
"v_mfma_f32_16x16x32_bf16 v[64:67], a[188:191], v[72:75], 0\n"
"buffer_store_dwordx4 v[160:163], v253, s[16:19], s97 offen sc1\n"
"v_mfma_f32_16x16x32_bf16 v[64:67], a[184:187], v[128:131], v[64:67]\n"
"s_waitcnt lgkmcnt(0)\n"
"s_barrier\n"
"v_mfma_f32_16x16x32_bf16 v[64:67], a[180:183], v[172:175], v[64:67]\n"
"ds_read_b128 v[76:79], v4\n"
"ds_read_b128 v[100:103], v5\n"
"v_mfma_f32_16x16x32_bf16 v[64:67], a[176:179], v[84:87], v[64:67]\n"
"v_mfma_f32_16x16x32_bf16 v[64:67], a[172:175], v[152:155], v[64:67]\n"
"v_cmp_u_f32_e32 vcc, v60, v60\n"
"v_mfma_f32_16x16x32_bf16 v[64:67], a[168:171], v[52:55], v[64:67]\n"
"s_cbranch_vccnz .Lmk_p61s\n"
".Lmk_p61r:\n"
"v_mfma_f32_16x16x32_bf16 v[64:67], a[164:167], v[112:115], v[64:67]\n"
"ds_write_b128 v3, v[60:63] offset:12288\n"
"v_mfma_f32_16x16x32_bf16 v[64:67], a[160:163], v[96:99], v[64:67]\n"
"s_waitcnt lgkmcnt(1)\n"
"v_add_f32_e32 v76, v76, v254\n"
"v_add_f32_e32 v77, v77, v251\n"
"v_add_f32_e32 v78, v78, v250\n"
"v_mfma_f32_16x16x32_bf16 v[60:63], a[192:195], v[96:99], 0\n"
"ds_read_b128 v[116:119], v4 offset:1024\n"
"ds_read_b128 v[160:163], v5 offset:1024\n"
"v_add_f32_e32 v79, v79, v249\n"
"v_mfma_f32_16x16x32_bf16 v[60:63], a[196:199], v[112:115], v[60:63]\n"
"v_add_f32_e32 v76, v76, v100\n"
"v_add_f32_e32 v77, v77, v101\n"
"v_add_f32_e32 v78, v78, v102\n"
"v_mfma_f32_16x16x32_bf16 v[60:63], a[200:203], v[52:55], v[60:63]\n"
"v_add_f32_e32 v79, v79, v103\n"
"v_mfma_f32_16x16x32_bf16 v[60:63], a[204:207], v[152:155], v[60:63]\n"
"v_mfma_f32_16x16x32_bf16 v[60:63], a[208:211], v[84:87], v[60:63]\n"
"v_mfma_f32_16x16x32_bf16 v[60:63], a[212:215], v[172:175], v[60:63]\n"
"ds_write_b128 v3, v[64:67] offset:13312\n"
"v_mfma_f32_16x16x32_bf16 v[60:63], a[216:219], v[128:131], v[60:63]\n"
"ds_read_b128 v[64:67], v4 offset:2048\n"
"ds_read_b128 v[100:103], v5 offset:2048\n"
"s_waitcnt lgkmcnt(3)\n"
"v_mfma_f32_16x16x32_bf16 v[60:63], a[220:223], v[72:75], v[60:63]\n"
"v_add_f32_e32 v116, v116, v248\n"
"v_add_f32_e32 v117, v117, v247\n"
"v_add_f32_e32 v118, v118, v246\n"
"v_mfma_f32_16x16x32_bf16 v[164:167], a[252:255], v[72:75], 0\n"
"v_add_f32_e32 v119, v119, v245\n"
"v_add_f32_e32 v116, v116, v160\n"
"v_add_f32_e32 v117, v117, v161\n"
"v_mfma_f32_16x16x32_bf16 v[164:167], a[248:251], v[128:131], v[164:167]\n"
"v_add_f32_e32 v118, v118, v162\n"
"v_add_f32_e32 v119, v119, v163\n"
"v_mfma_f32_16x16x32_bf16 v[164:167], a[244:247], v[172:175], v[164:167]\n"
"v_mfma_f32_16x16x32_bf16 v[164:167], a[240:243], v[84:87], v[164:167]\n"
"s_waitcnt lgkmcnt(0)\n"
"v_add_f32_e32 v64, v64, v244\n"
"v_add_f32_e32 v65, v65, v243\n"
"v_add_f32_e32 v66, v66, v242\n"
"v_mfma_f32_16x16x32_bf16 v[164:167], a[236:239], v[152:155], v[164:167]\n"
"ds_read_b128 v[72:75], v4 offset:3072\n"
"ds_read_b128 v[84:87], v5 offset:3072\n"
"v_add_f32_e32 v67, v67, v241\n"
"v_mfma_f32_16x16x32_bf16 v[164:167], a[232:235], v[52:55], v[164:167]\n"
"ds_write_b128 v3, v[60:63] offset:14336\n"
"v_mfma_f32_16x16x32_bf16 v[164:167], a[228:231], v[112:115], v[164:167]\n"
"v_add_f32_e32 v64, v64, v100\n"
"v_add_f32_e32 v65, v65, v101\n"
"v_add_f32_e32 v66, v66, v102\n"
"v_mfma_f32_16x16x32_bf16 v[164:167], a[224:227], v[96:99], v[164:167]\n"
"s_waitcnt vmcnt(6)\n"
"v_add_f32_e32 v67, v67, v103\n"
"; @B G2 a.3\n"
"v_mfma_f32_16x16x32_bf16 v[52:55], a[128:131], v[36:39], v[76:79]\n"
"v_mfma_f32_16x16x32_bf16 v[52:55], a[132:135], v[40:43], v[52:55]\n"
"s_waitcnt lgkmcnt(1)\n"
"v_add_f32_e32 v72, v72, v240\n"
"v_add_f32_e32 v73, v73, v239\n"
"v_add_f32_e32 v74, v74, v238\n"
"v_mfma_f32_16x16x32_bf16 v[52:55], a[136:139], v[80:83], v[52:55]\n"
"v_add_f32_e32 v75, v75, v237\n"
"v_add_f32_e32 v72, v72, v84\n"
"v_add_f32_e32 v73, v73, v85\n"
"v_mfma_f32_16x16x32_bf16 v[52:55], a[140:143], v[132:135], v[52:55]\n"
"s_waitcnt vmcnt(2)\n"
"v_add_f32_e32 v74, v74, v86\n"
"v_add_f32_e32 v75, v75, v87\n"
"v_mfma_f32_16x16x32_bf16 v[52:55], a[144:147], v[156:159], v[52:55]\n"
"v_mfma_f32_16x16x32_bf16 v[52:55], a[148:151], v[68:71], v[52:55]\n"
"ds_write_b128 v3, v[164:167] offset:15360\n"
"v_mfma_f32_16x16x32_bf16 v[52:55], a[152:155], v[200:203], v[52:55]\n"
"v_mfma_f32_16x16x32_bf16 v[52:55], a[156:159], v[140:143], v[52:55]\n"
"v_mfma_f32_16x16x32_bf16 v[60:63], a[188:191], v[140:143], v[116:119]\n"
"v_mfma_f32_16x16x32_bf16 v[60:63], a[184:187], v[200:203], v[60:63]\n"
"s_waitcnt lgkmcnt(0)\n"
"s_barrier\n"
"v_mfma_f32_16x16x32_bf16 v[60:63], a[180:183], v[68:71], v[60:63]\n"
"ds_read_b128 v[84:87], v6\n"
"ds_read_b128 v[96:99], v6 offset:1024\n"
"v_mfma_f32_16x16x32_bf16 v[60:63], a[176:179], v[156:159], v[60:63]\n"
"ds_read_b128 v[100:103], v6 offset:2048\n"
"ds_read_b128 v[112:115], v6 offset:3072\n"
"v_mfma_f32_16x16x32_bf16 v[60:63], a[172:175], v[132:135], v[60:63]\n"
"v_cmp_u_f32_e32 vcc, v52, v52\n"
"v_mfma_f32_16x16x32_bf16 v[60:63], a[168:171], v[80:83], v[60:63]\n"
"s_cbranch_vccnz .Lmk_p62s\n"
".Lmk_p62r:\n"
"v_mfma_f32_16x16x32_bf16 v[60:63], a[164:167], v[40:43], v[60:63]\n"
"v_mfma_f32_16x16x32_bf16 v[60:63], a[160:163], v[36:39], v[60:63]\n"
"v_mfma_f32_16x16x32_bf16 v[64:67], a[192:195], v[36:39], v[64:67]\n"
"v_mfma_f32_16x16x32_bf16 v[64:67], a[196:199], v[40:43], v[64:67]\n"
"v_mfma_f32_16x16x32_bf16 v[64:67], a[200:203], v[80:83], v[64:67]\n"
"v_mfma_f32_16x16x32_bf16 v[64:67], a[204:207], v[132:135], v[64:67]\n"
"v_mfma_f32_16x16x32_bf16 v[64:67], a[208:211], v[156:159], v[64:67]\n"
"s_waitcnt lgkmcnt(0)\n"
"v_add_f32_e32 v84, v52, v84\n"
"v_add_f32_e32 v85, v53, v85\n"
"v_add_f32_e32 v86, v54, v86\n"
"v_mfma_f32_16x16x32_bf16 v[64:67], a[212:215], v[68:71], v[64:67]\n"
"v_add_f32_e32 v87, v55, v87\n"
"v_add_f32_e32 v96, v60, v96\n"
"v_add_f32_e32 v97, v61, v97\n"
"v_mfma_f32_16x16x32_bf16 v[64:67], a[216:219], v[200:203], v[64:67]\n"
"v_add_f32_e32 v98, v62, v98\n"
"v_add_f32_e32 v99, v63, v99\n"
"v_cvt_pk_bf16_f32 v52, v84, v85\n"
"v_mfma_f32_16x16x32_bf16 v[64:67], a[220:223], v[140:143], v[64:67]\n"
"v_cvt_pk_bf16_f32 v53, v86, v87\n"
"v_cvt_pk_bf16_f32 v54, v96, v97\n"
"v_cvt_pk_bf16_f32 v55, v98, v99\n"
"v_mfma_f32_16x16x32_bf16 v[72:75], a[252:255], v[140:143], v[72:75]\n"
"v_xor_b32_e32 v254, 32, v255\n"
"v_mfma_f32_16x16x32_bf16 v[72:75], a[248:251], v[200:203], v[72:75]\n"
"ds_write_b64 v255, v[52:53] offset:0\n"
"v_mfma_f32_16x16x32_bf16 v[72:75], a[244:247], v[68:71], v[72:75]\n"
"ds_write_b64 v254, v[54:55] offset:0\n"
"v_mfma_f32_16x16x32_bf16 v[72:75], a[240:243], v[156:159], v[72:75]\n"
"v_mfma_f32_16x16x32_bf16 v[72:75], a[236:239], v[132:135], v[72:75]\n"
"v_mfma_f32_16x16x32_bf16 v[72:75], a[232:235], v[80:83], v[72:75]\n"
"v_mfma_f32_16x16x32_bf16 v[72:75], a[228:231], v[40:43], v[72:75]\n"
"v_mfma_f32_16x16x32_bf16 v[72:75], a[224:227], v[36:39], v[72:75]\n"
"v_add_f32_e32 v100, v64, v100\n"
"v_add_f32_e32 v101, v65, v101\n"
"v_add_f32_e32 v102, v66, v102\n"
"v_add_f32_e32 v103, v67, v103\n"
"s_nop 3\n"
"v_add_f32_e32 v112, v72, v112\n"
"v_add_f32_e32 v113, v73, v113\n"
"v_add_f32_e32 v114, v74, v114\n"
"v_add_f32_e32 v115, v75, v115\n"
"v_cvt_pk_bf16_f32 v36, v100, v101\n"
"v_cvt_pk_bf16_f32 v37, v102, v103\n"
"v_cvt_pk_bf16_f32 v38, v112, v113\n"
"v_cvt_pk_bf16_f32 v39, v114, v115\n"
"v_xor_b32_e32 v254, 64, v255\n"
"v_xor_b32_e32 v253, 0x60, v255\n"
"ds_write_b64 v254, v[36:37] offset:0\n"
"ds_write_b64 v253, v[38:39] offset:0\n"
"ds_read_b128 v[36:39], v2\n"
"ds_read_b128 v[40:43], v2 offset:1024\n"
"s_waitcnt lgkmcnt(0)\n"
"buffer_store_dwordx4 v[36:39], v252, s[16:19], s74 offen sc1\n"
"s_add_u32 s97, s74, 0x4000\n"
"buffer_store_dwordx4 v[40:43], v252, s[16:19], s97 offen sc1\n"
"s_cmp_eq_u32 s81, 0\n"
"s_cbranch_scc1 .Lmk_fin\n"
"buffer_store_dwordx2 v[18:19], v11, s[4:7], s81 offen sc1\n"
".Lmk_fin:\n"
"s_endpgm\n"
".Lmk_end:\n"
    :: "{s[4:7]}"(wsr), "{s[8:11]}"(wsr), "{s[12:15]}"(wsk), "{s[16:19]}"(outr), "{s[52:67]}"(sa), "{s[68:83]}"(sb),
       "{s[20:27]}"(pa), "{s[28:31]}"(wa), "{s[32:35]}"(wb), "{s[36:39]}"(oa), "{s[40:43]}"(ob), "{s[44:51]}"(pc), "{s99}"(uni((bix * 4 + w) * 1024)),
       "{v[0:15]}"(va), "{v[16:23]}"(vb), "{v[32:47]}"(pz), "{v48}"(sta), "{v50}"(pf)
    : "memory", "v255", "a255", "s98", "vcc");
  __builtin_unreachable();
}

}

extern "C" void kernel_launch(void* const* d_in, const int* in_sizes, int n_in, void* d_out, int out_size,
                              void* d_ws, size_t ws_size, hipStream_t stream) {
  (void)hipFuncSetAttribute((const void*)mega_kernel, hipFuncAttributeMaxDynamicSharedMemorySize, LDS_BYTES);
  mega_kernel<<<256, 256, LDS_BYTES, stream>>>((const u16*)d_in[0], (const u16*)d_in[1], (const u16*)d_in[2],
                                               (const u16*)d_in[4], (const u16*)d_in[5], (u16*)d_out, (char*)d_ws);
}
```

```cpp
#include <hip/hip_runtime.h>
#include <stdint.h>

namespace {

typedef unsigned short u16;
typedef unsigned long long u64;
typedef short s16x4 __attribute__((ext_vector_type(4)));
typedef float f32x4 __attribute__((ext_vector_type(4)));
typedef unsigned u32x4 __attribute__((ext_vector_type(4)));
typedef unsigned u32x2 __attribute__((ext_vector_type(2)));
typedef unsigned u32x8 __attribute__((ext_vector_type(8)));
typedef unsigned u32x16 __attribute__((ext_vector_type(16)));
typedef __amdgpu_buffer_rsrc_t rsrc_t;
#define LAS __attribute__((address_space(3)))
#define DI __device__ __forceinline__
#define LDSP(a) ((LAS char*)(size_t)(a))

constexpr unsigned MB = 1u << 20;
constexpr unsigned WS_XF = 0;
constexpr unsigned WS_OF = 16 * MB;
constexpr unsigned WS_KV = 32 * MB;
constexpr unsigned WS_WF = 64 * MB;
constexpr unsigned WS_WO = 76 * MB;
constexpr unsigned WS_ST = 80 * MB;
constexpr unsigned WS_CS = 81 * MB;
constexpr unsigned WS_FL = 82 * MB;
constexpr unsigned WS_EP = WS_FL + 2048 * 128;
constexpr unsigned WS_DBG = 96 * MB;
constexpr unsigned OOB = 0x80000000u;
constexpr int L_RES = 0, L_K = 16384, L_V = 49152, L_INB = 81920, L_CS = 155648, L_PM = L_CS + 1024, L_MB = L_PM + 2048, L_SCR = L_MB + 2048;
constexpr int L_WS = 57344;
constexpr int LDS_BYTES = 163840;

template <int N> DI void wait_vm() { asm volatile("s_waitcnt vmcnt(%0)" ::"n"(N) : "memory"); }
template <int N> DI void nops() { asm volatile("s_nop %0" ::"n"(N - 1)); }
template <int OFF> DI void ld8(u32x2& d, rsrc_t r, unsigned v, unsigned s) {
  asm volatile("buffer_load_dwordx2 %0, %1, %2, %3 offen offset:%4 sc1" : "=v"(d) : "v"(v), "s"(r), "s"(s), "i"(OFF) : "memory");
}
template <int OFF> DI void st16(const u32x4& d, rsrc_t r, unsigned v, unsigned s) {
  asm volatile("buffer_store_dwordx4 %0, %1, %2, %3 offen offset:%4 sc1\n\ts_nop 1" :: "v"(d), "v"(v), "s"(r), "s"(s), "i"(OFF) : "memory");
}
template <int OFF> DI void st8(const u32x2& d, rsrc_t r, unsigned v, unsigned s) {
  asm volatile("buffer_store_dwordx2 %0, %1, %2, %3 offen offset:%4 sc1" :: "v"(d), "v"(v), "s"(r), "s"(s), "i"(OFF) : "memory");
}
DI void st4(unsigned d, rsrc_t r, unsigned v, unsigned s) {
  asm volatile("buffer_store_dword %0, %1, %2, %3 offen sc1" :: "v"(d), "v"(v), "s"(r), "s"(s) : "memory");
}
DI void dma(rsrc_t r, unsigned v, unsigned s, unsigned lm) {
  asm volatile("s_mov_b32 m0, %3\n\ts_nop 0\n\tbuffer_load_dwordx4 %0, %1, %2 offen lds" :: "v"(v), "s"(r), "s"(s), "s"(lm) : "memory");
}
DI void touch(rsrc_t r, unsigned v) {
  asm volatile("s_mov_b32 m0, %2\n\ts_nop 0\n\tbuffer_load_dword %0, %1, 0 offen lds" :: "v"(v), "s"(r), "s"((unsigned)L_SCR) : "memory");
}
DI void mma_v(f32x4& c, const u32x4& a, const u32x4& b) { asm volatile("v_mfma_f32_16x16x32_bf16 %0, %1, %2, %0" : "+v"(c) : "v"(a), "v"(b)); }
DI void mm0_v(f32x4& c, const u32x4& a, const u32x4& b) { asm volatile("v_mfma_f32_16x16x32_bf16 %0, %1, %2, 0" : "=v"(c) : "v"(a), "v"(b)); }
DI unsigned uni(unsigned v) { v = __builtin_amdgcn_readfirstlane(v); asm volatile("" : "+s"(v)); return v; }
template <class T> DI void pin(T& v) { asm volatile("" : "+v"(v)); }
DI float sum_x16(float v) {
  auto r = __builtin_amdgcn_permlane16_swap(__float_as_uint(v), __float_as_uint(v), false, false);
  return __uint_as_float(r[0]) + __uint_as_float(r[1]);
}
DI float sum_x32(float v) {
  auto r = __builtin_amdgcn_permlane32_swap(__float_as_uint(v), __float_as_uint(v), false, false);
  return __uint_as_float(r[0]) + __uint_as_float(r[1]);
}
DI u32x2 lds_tr(unsigned a) { return __builtin_bit_cast(u32x2, __builtin_amdgcn_ds_read_tr16_b64_v4i16((LAS s16x4*)LDSP(a))); }
DI u32x4 cat(const u32x2& a, const u32x2& b) { return __builtin_shufflevector(a, b, 0, 1, 2, 3); }
DI u32x2 lds_r2(unsigned a) { return *(LAS u32x2*)LDSP(a); }
DI void lds_w4(unsigned a, const u32x4& v) { *(LAS u32x4*)LDSP(a) = v; }
DI unsigned rot(unsigned g, unsigned sl) { return (g + (0x641u >> 4 * sl)) & 15; }
DI int swzK(int u) { return ((((u >> 1) ^ u) & 1) << 1) | (u >> 1); }

#define STAMP(i)

__global__ __launch_bounds__(256, 1) void mega_kernel(const u16* __restrict__ x0, const u16* __restrict__ WqA,
                                                      const u16* __restrict__ WoA, const u16* __restrict__ WqB,
                                                      const u16* __restrict__ WoB, u16* __restrict__ out, char* ws) {
  const unsigned bid = blockIdx.x, w = __builtin_amdgcn_readfirstlane(threadIdx.x >> 6);
  unsigned lane = threadIdx.x & 63;
  unsigned stw = 0;
  STAMP(0);
  u32x2 tag;
  {
    u64 t0; unsigned t1, t2, t3, v1, v2;
    asm volatile(
      "v_lshlrev_b32 %[v1], 4, %[lane]\n"
      "v_lshrrev_b32 %[v2], 3, %[lane]\n"
      "s_getpc_b64 %[t0]\n.Lmk_here:\n"
      "s_lshr_b32 %[t1], %[bid], 3\n s_lshl2_add_u32 %[t1], %[t1], %[w]\n s_lshl_b32 %[t1], %[t1], 10\n"
      "s_mov_b64 s[88:89], %[t0]\n s_and_b32 s88, s88, -16\n s_and_b32 s89, s89, 0xffff\n s_mov_b32 s90, 8192\n s_mov_b32 s91, 0x20000\n"
      "s_mov_b32 m0, %[scr]\n s_nop 0\n"
      "buffer_load_dwordx4 %[v1], s[88:91], %[t1] offen lds\n"
      "v_and_b32 %[v1], 0x70, %[v1]\n v_lshl_or_b32 %[v1], %[v2], 11, %[v1]\n"
      "s_and_b32 %[t1], %[bid], 4\n s_lshr_b32 %[t2], %[bid], 6\n s_add_u32 %[t1], %[t1], %[t2]\n s_lshl3_add_u32 %[t1], %[t1], %[w]\n"
      "s_and_b32 %[t3], %[bid], 1\n s_lshl1_add_u32 %[t1], %[t1], %[t3]\n s_lshl_b32 %[t1], %[t1], 8\n"
      "s_bfe_u32 %[t3], %[bid], 0x30003\n s_add_u32 %[t1], %[t1], %[t3]\n s_and_b32 %[t3], %[bid], 2\n s_lshl2_add_u32 %[t1], %[t3], %[t1]\n s_lshl_b32 %[t1], %[t1], 7\n"
      "s_lshl_b32 %[t3], %[w], 11\n"
      : [t0] "=&s"(t0), [t1] "=&s"(t1), [t2] "=&s"(t2), [t3] "=&s"(t3), [v1] "=&v"(v1), [v2] "=&v"(v2)
      : [bid] "s"(bid), [w] "s"(w), [lane] "v"(lane), [scr] "i"(L_SCR + 1024) : "memory", "s88", "s89", "s90", "s91", "scc");
    v2 = v2 * 6144 + (lane & 7) * 16;
    const unsigned g_ = (bid & 3) * 4 + (bid >> 4 & 3);
    const unsigned src = (((bid & 4) + (bid >> 6)) * 128 + (bid >> 3 & 1) * 64 + w * 16) * 6144, dst = L_INB + w * 2048;
    const unsigned r0 = src + rot(g_, 0) * 128, r1 = src + 2048 + rot(g_, 1) * 128, r2 = src + 4096 + rot(g_, 2) * 128;
    asm volatile(
      "s_mov_b64 s[88:89], %[wq]\n s_and_b32 s89, s89, 0xffff\n s_mov_b32 s90, 0x600000\n s_mov_b32 s91, 0x20000\n"
      "s_mov_b32 m0, %[dst]\n s_add_u32 %[t2], %[r0], 0xc000\n buffer_load_dwordx4 %[v2], s[88:91], %[r0] offen lds\n"
      "s_add_u32 m0, %[dst], 0x400\n s_nop 0\n buffer_load_dwordx4 %[v2], s[88:91], %[t2] offen lds\n"
      "s_add_u32 m0, %[dst], 0x2000\n s_add_u32 %[t2], %[r1], 0xc000\n buffer_load_dwordx4 %[v2], s[88:91], %[r1] offen lds\n"
      "s_add_u32 m0, %[dst], 0x2400\n s_nop 0\n buffer_load_dwordx4 %[v2], s[88:91], %[t2] offen lds\n"
      "s_add_u32 m0, %[dst], 0x4000\n s_add_u32 %[t2], %[r2], 0xc000\n buffer_load_dwordx4 %[v2], s[88:91], %[r2] offen lds\n"
      "s_add_u32 m0, %[dst], 0x4400\n s_nop 0\n buffer_load_dwordx4 %[v2], s[88:91], %[t2] offen lds\n"
      "s_mov_b64 s[88:89], %[x]\n s_and_b32 s89, s89, 0xffff\n s_mov_b32 s90, 0x400000\n s_mov_b32 s91, 0x20000\n"
      "s_add_u32 m0, %[t3], 0x2000\n s_add_u32 %[t2], %[t1], 0x40000\n buffer_load_dwordx4 %[v1], s[88:91], %[t2] offen lds\n"
      "s_add_u32 m0, %[t3], 0x2400\n s_add_u32 %[t2], %[t1], 0x44000\n buffer_load_dwordx4 %[v1], s[88:91], %[t2] offen lds\n"
      "s_mov_b32 m0, %[t3]\n s_add_u32 %[t2], %[t1], 0x4000\n buffer_load_dwordx4 %[v1], s[88:91], %[t1] offen lds\n"
      "s_add_u32 m0, %[t3], 0x400\n s_mov_b32 %[t1], %[ep]\n buffer_load_dwordx4 %[v1], s[88:91], %[t2] offen lds\n"
      "s_mov_b64 s[88:89], %[ws]\n s_and_b32 s89, s89, 0xffff\n s_mov_b32 s90, 0x8000000\n"
      "buffer_load_dwordx2 %[tag], off, s[88:91], %[t1] sc1\n"
      : [t1] "+s"(t1), [t2] "+s"(t2), [tag] "=&v"(tag), [lane] "+v"(lane)
      : [t3] "s"(t3), [v1] "v"(v1), [v2] "v"(v2), [x] "s"(x0), [ws] "s"(ws), [wq] "s"(WqA), [dst] "s"(dst), [ep] "i"(WS_EP),
        [r0] "s"(r0), [r1] "s"(r1), [r2] "s"(r2),
        "s"(WoA), "s"(WqB), "s"(WoB), "s"(out) : "memory", "s88", "s89", "s90", "s91", "scc");
  }
  const unsigned xcd = bid & 7, bix = bid >> 3;
  const unsigned c = (xcd >> 2) * 4 + (bix >> 3), h = (xcd & 3) * 8 + (bix & 7), par = h & 1, g = h >> 1;
  const unsigned p2 = h >> 3 & 1, g2 = (h >> 4) * 8 + (h & 7);
  const unsigned fr = lane & 15, fq = lane >> 4;
  const unsigned dl = 0, wr = (w + dl) & 3;
  const rsrc_t wsr = __builtin_amdgcn_make_buffer_rsrc(ws, 0, 128 * MB, 0x00020000);
  const unsigned l16 = lane * 16, lw = w * 2048 + l16;
  const unsigned fl0 = lane == 0 ? 0u : OOB;
  const unsigned myfl = WS_FL + (c * 64 + h) * 128 + w * 8;
  const unsigned osrc0 = (lane >> 3) * 2048 + (lane & 7) * 16;
  const rsrc_t wa = __builtin_amdgcn_make_buffer_rsrc((void*)WqA, 0, 6 * MB, 0x00020000);
  const unsigned rho = 0, wbase = w * 256 * 6144 + h * 64;
  const unsigned wv = (lane >> 2) * 6144 + ((lane & 3) ^ ((lane >> 3) & 2)) * 16;
  auto piece = [&](int q, int j) __attribute__((always_inline)) {
    const unsigned k = j ^ par, src = uni(wbase + (((rho + 2 * q) & 7) + k) * 196608), dst = uni(q + k ? L_INB + w * 18432 + (2 * q + k - 1) * 6144 : L_WS + w * 6144);
#pragma unroll
    for (int i = 0; i < 6; ++i) dma(wa, wv, src + (i & 1) * 98304 + (i >> 1) * 2048, dst + i * 1024);
  };
  const unsigned kb = 2 * c + par;
  const rsrc_t oa = __builtin_amdgcn_make_buffer_rsrc((void*)WoA, 0, 2 * MB, 0x00020000);
  const rsrc_t wb = __builtin_amdgcn_make_buffer_rsrc((void*)WqB, 0, 6 * MB, 0x00020000);
  const rsrc_t ob = __builtin_amdgcn_make_buffer_rsrc((void*)WoB, 0, 2 * MB, 0x00020000);
  const unsigned wsrc = (w * 16 + (lane >> 3)) * 6144 + (lane & 7) * 16, osrc = w * 16 * 2048 + osrc0;
  auto get_o = [&](int t) __attribute__((always_inline)) {
#pragma unroll
    for (int i = 0; i < 2; ++i) dma(t ? ob : oa, osrc, (kb * 64 + i * 8) * 2048 + rot(g, 1 + t) * 128, L_V + 8192 + t * 8192 + w * 2048 + i * 1024);
  };
  auto get_q = [&](int t) __attribute__((always_inline)) {
#pragma unroll
    for (int i = 0; i < 6; ++i)
      dma(t ? wb : wa, wsrc, (kb * 64 + (i & 1) * 8) * 6144 + (i >> 1) * 2048 + rot(g, i >> 1) * 128, (t ? L_K : L_INB) + (i >> 1) * 8192 + w * 2048 + (i & 1) * 1024);
  };
  auto code = [&]() __attribute__((always_inline)) {
    u64 pc_; unsigned len_;
    asm volatile("s_getpc_b64 %0\n.Lmk_h1:\n\ts_mov_b32 %1, .Lmk_end-.Lmk_h1" : "=s"(pc_), "=s"(len_));
    dma(__builtin_amdgcn_make_buffer_rsrc((void*)(pc_ & ~15ull), 0, len_ & ~15u, 0x00020000), l16, (bix * 4 + w) * 1024, L_SCR + 1024);
  };
  const unsigned fr8 = lane < 32 ? lane * 4 : OOB;
  u32x4 ones = {0x3F803F80u, 0x3F803F80u, 0x3F803F80u, 0x3F803F80u};
  unsigned sta = 0;
  auto publish = [&](int s, bool now) __attribute__((always_inline)) {
    const unsigned a = L_RES + s * 8192 + w * 2048 + fr * 128 + fq * 8, T = c * 16 + 8 * s + 2 * wr + p2;
    u32x4 y0 = cat(lds_r2(a), lds_r2(a + 32)), y1 = cat(lds_r2(a + 64), lds_r2(a + 96));
    f32x4 sm, gm;
    pin(y0); pin(y1); pin(ones);
    nops<2>();
    mm0_v(sm, ones, y0); mm0_v(gm, y0, y0); mma_v(sm, ones, y1); mma_v(gm, y1, y1);
    if (now) { st16<0>(y0, wsr, l16, WS_XF + T * 32768 + g2 * 2048); st16<1024>(y1, wsr, l16, WS_XF + T * 32768 + g2 * 2048); }
    lds_w4(lw + L_RES + s * 8192, y0); lds_w4(lw + L_RES + s * 8192 + 1024, y1);
    nops<8>();
    pin(sm); pin(gm);
    const float sq = sum_x32(sum_x16(fq != fr >> 2 ? 0.f : fr & 2 ? (fr & 1 ? gm[3] : gm[2]) : (fr & 1 ? gm[1] : gm[0])));
    const unsigned st = __float_as_uint(fq ? sq : sm[0]);
    if (now) st4(st, wsr, fr8, WS_ST + T * 2048 + g2 * 128); else sta = st;
  };
  auto next_tag = [&]() __attribute__((always_inline)) {
    pin(tag);
    const u64 t = (((u64)tag[1] << 32) | tag[0]) + 1; tag = (u32x2){(unsigned)t, (unsigned)(t >> 32)};
  };
  auto line = [&](unsigned i) __attribute__((always_inline)) {
    asm volatile("s_waitcnt lgkmcnt(0)\n\ts_barrier" ::: "memory");
    st16<0>(cat(tag, tag), wsr, w == 0 && lane < 8 ? l16 : OOB, WS_FL + 524288 + (c * 64 + i) * 128);
  };
  const unsigned cb = c * 16 * 32768, skv = WS_KV + (c * 32 + h) * 32768;
  const unsigned soo = WS_OF + cb + wr * 65536 + h * 1024, sxo = WS_XF + cb + (2 * wr + p2) * 32768 + g2 * 2048, sso = WS_ST + (c * 16 + 2 * wr + p2) * 2048 + g2 * 128;
  auto clear = [&](int from, int to) __attribute__((always_inline)) {
    u32x4 z = {~0u, ~0u, ~0u, ~0u};
    pin(z);
#pragma unroll
    for (int k = from; k < to; ++k) {
      if (k < 8) st16<0>(z, wsr, l16, soo + (k == 4 ? 9 : k == 5 ? 8 : k - 6) * 32768);
      else if (k < 10) st16<0>(z, wsr, l16, sxo + 4 * MB + 8 * 32768 + (k - 8) * 1024);
      else st4(z[0], wsr, fr8, sso + 136 * 2048);
    }
  };
  unsigned ps[16];
  u32x8 pv = {};
#pragma unroll
  for (int j = 0; j < 3; ++j) {
    const unsigned ct = (w & 1) * 3 + j;
    pv[1 + j] = L_K + (ct >> 1) * 8192 + (4 * fq + (fr >> 2)) * 128 + (w >> 1) * 64 + (ct & 1) * 32 + (lane & 3) * 8;
    ps[j] = WS_WF + 6 * MB + ((2 * rot(g, ct >> 1) + (w >> 1)) * 192 + (kb >> 2) * 48 + (ct + 4) % 6 * 8 + (kb & 3) * 2) * 1024;
    ps[10 + j] = kb * 64 * 6144 + j * 2048 + rot(g, j) * 128;
  }
  { const unsigned ct = (w & 1) * 3 + fq;
    pv[4] = ((2 * rot(g, ct >> 1) + (w >> 1)) * 16384 + ct * 128 + fr * 8) | (fq < 3 ? 0u : OOB); }
  ps[3] = WS_CS + 524288 + kb * 1024;
  auto convert = [&]() __attribute__((always_inline)) {
    f32x4 cs[3];
    u32x4 f[2][3];
    asm volatile("s_waitcnt lgkmcnt(0)\n\ts_barrier" ::: "memory");
#pragma unroll
    for (int kk = 0; kk < 2; ++kk)
#pragma unroll
      for (int j = 0; j < 3; ++j) {
        const unsigned a = pv[1 + j] + (L_INB - L_K) + kk * 4096;
        f[kk][j] = cat(lds_tr(a), lds_tr(a + 2048));
      }
    pin(ones);
#pragma unroll
    for (int kk = 0; kk < 2; ++kk)
#pragma unroll
      for (int j = 0; j < 3; ++j) {
        if (kk) mma_v(cs[j], ones, f[kk][j]); else mm0_v(cs[j], ones, f[kk][j]);
        st16<0>(f[kk][j], wsr, l16, uni(ps[j] - 6 * MB + kk * 1024));
      }
    nops<8>();
    pin(cs[0]); pin(cs[1]); pin(cs[2]);
    const unsigned v = __float_as_uint(fq == 0 ? cs[0][0] : fq == 1 ? cs[1][0] : cs[2][0]);
    st8<0>((u32x2){v, v}, wsr, pv[4], uni(ps[3] - 524288));
  };
  ;
  ;
  touch(wsr, lane < 2 ? WS_FL + (w * 512 + c * 64 + lane * 32 + h) * 128 : OOB);
  STAMP(29);

  ps[6] = (c >> 2) | par << 1 | (bix * 4 + w) << 10;                                            ps[7] = L_WS + w * 6144;
  ps[8] = wbase;                                             ps[9] = rho;
  ps[13] = kb * 64 * 2048 + rot(g, 1) * 128;                 ps[14] = kb * 64 * 2048 + rot(g, 2) * 128;
  ps[15] = WS_WF + (h * 4 + w) * 49152 + (rho + (c >> 2)) * 1024;
  pv[5] = wsrc;                                              pv[6] = osrc;
  u32x16 pw = {wv, L_WS + w * 6144 + (4 * fq + (fr >> 2)) * 64 + (fq & 1) * 32 + (lane & 3) * 8,
                    l16 + rho * 1024, l16 + ((rho + 2) & 7) * 1024, l16 + ((rho + 4) & 7) * 1024, l16 + ((rho + 6) & 7) * 1024,
                    L_WS + w * 6144 + (fr == 0 ? fq * 16 : 1024 + l16), L_WS + (w * 24 + (lane < 24 ? lane : 0)) * 4,
                    (w * 128 + (lane >> 3 & 1) * 64 + 2 * ((g2 - rot(0, 1 + (fq & 1))) & 15) + (lane >> 2 & 1)) * 128 + (lane & 3) * 8};
  if (w < 2) {
    u32x4 pm;
#pragma unroll
    for (int k = 0; k < 4; ++k)
      pm[k] = (8 * fq + 2 * k > 16 * w + fr ? 0xFFFFu : 0u) | (8 * fq + 2 * k + 1 > 16 * w + fr ? 0xFFFF0000u : 0u);
    lds_w4(L_PM + w * 1024 + l16, pm);
  } else {
    const unsigned want = (unsigned)((0x5555ull << 16) >> (fr + 25 - 8 * fq));
    u32x4 mb;
#pragma unroll
    for (int k = 0; k < 4; ++k) mb[k] = (want >> (15 - 4 * w - k)) & 1 ? 0u : 0xFF800000u;
    lds_w4(L_MB + (w - 2) * 1024 + l16, mb);
  }
  pv[0] = L_V + 8192 + (16 * (fq & 1) + 4 * (fq >> 1) + (fr >> 2)) * 128 + w * 32 + (lane & 3) * 8;
  pv[7] = lane == 0 ? 32 * 128 : OOB;
  ps[4] = WS_WO + (rot(g, 1) * 128 + (kb >> 2) * 32 + w * 8 + (kb & 3) * 2) * 1024;
  ps[5] = WS_WO + 2 * MB + (rot(g, 2) * 128 + (kb >> 2) * 32 + w * 8 + (kb & 3) * 2) * 1024;
  const unsigned krow = (8 * (fr >> 2) + (fr & 3)) * 64 + wr * 2048 + L_K;
  const unsigned kv0 = 16384 + wr * 2048 + fr * 64 + ((fq ^ ((fr >> 3) << 1) ^ ((fr >> 1) & 3)) * 16);
  u32x16 va = {l16, l16 + 4096, lw, L_INB + w * 18432 + l16, L_INB + ((w + 3) & 3) * 18432 + l16, L_INB + ((w + 2) & 3) * 18432 + 6144 + l16,
                     L_INB + ((w + 1) & 3) * 18432 + 12288 + l16, kv0, kv0 ^ 48, L_CS + fq * 16, lane * 4, fl0,
                     ((lane & 31) >> 2) * 128 + (lane & 3) * 8,
                     fq < 3 ? (w * 128 + (lane >> 3 & 1) * 64 + 2 * ((2 * g - rot(g, fq % 3)) & 15) + (lane >> 2 & 1)) * 128 + (lane & 3) * 8 : ((lane & 7) * 64 + h) * 128 + w * 8,
                     fr8, krow + (fq ^ swzK(fr >> 2) ^ ((fr >> 1) & 1)) * 16};
  u32x8 vb = {L_V + wr * 2048 + (8 * fq + (fr >> 2)) * 64 + 8 * (lane & 1) + (((lane >> 1) & 1) ^ ((fq & 1) << 1) ^ (fr >> 3)) * 16,
                    krow + (fq ^ swzK(((fr >> 2) + 2) & 3) ^ ((fr >> 1) & 1)) * 16, 0, 0, 0,
                    lane < 24 ? L_CS + w * 96 + lane * 4 : L_SCR, w == 0 ? bid * 1024 + lane * 4 : OOB, 0};
  u32x16 pz = {pw[2], pw[3], pw[4], pw[5], pv[1], pv[2], pv[3], pv[4], pv[0], pw[8], pw[0], pw[1], pw[6], pw[7], pv[5], pv[6]};
  unsigned pf = pv[7];
  pin(va); pin(vb); pin(pz); pin(pf);
  wait_vm<6>();
  STAMP(18);
  convert();
  wait_vm<8>();
  next_tag();
  publish(1, true);
  STAMP(1);
  wait_vm<3>();
  st8<96>(tag, wsr, fl0, myfl);
  get_o(0);
  get_o(1);
  get_q(1);
  code();
  publish(0, false);
  wait_vm<12>();
  st8<0>(tag, wsr, lane == 0 ? 32 * 128 : OOB, myfl + (2 * g2 + p2 - h) * 128);
  STAMP(15);

  u32x16 sa, sb;
#pragma unroll
  for (int i = 0; i < 4; ++i) {
    sa[i] = WS_XF + cb + w * 8192 + ((wr + 1 + i) & 3) * 65536;
    sa[4 + i] = sa[i] + WS_OF + p2 * 32768;
  }
  sa[8] = soo;                                              sa[9] = sxo;
  sa[10] = WS_ST + (c * 16 + 2 * wr) * 2048;                sa[11] = sso;
  sa[12] = skv - 16384;                                     sa[13] = c ? skv - 32 * 32768 : WS_XF + (h & 15) * 32768;
  sa[14] = c ? skv - 32 * 32768 + 8 * MB : sa[13];          sa[15] = WS_FL + (c * 64 + 8 * w) * 128;
  sb[0] = myfl;                                             sb[1] = WS_WF + (h * 4 + w) * 49152;
  sb[2] = WS_WO + (g2 * 4 + w) * 32768;                     sb[3] = c ? myfl - w * 8 + ((w - dl) & 3) * 8 - 32 * 128 + 64 : myfl + 4096;
  sb[4] = w * 96;                                           sb[5] = c ? myfl + 65536 - w * 8 + ((3 - dl) & 3) * 8 - 32 * 128 + 64 : myfl + 4096;
  sb[6] = ((c * 16 + 2 * wr + p2) * 16 * 1024 + g2 * 64) * 2;  sb[7] = WS_FL + 96;
  sb[8] = WS_CS + h * 16384;                                sb[9] = w * 2048;
  sb[10] = c ? -8 : 8 - wr;                                  sb[11] = c ? 16 * MB : 0;
  sb[12] = (2 * g2 + p2 - h) * 128;                                sb[13] = bid == 0 && w == 0 ? WS_EP : 0;
  sb[14] = c == 0 && wr == 0 ? ~0u : 0u;                     sb[15] = 0;
#pragma unroll
  for (int i = 0; i < 16; ++i) { sa[i] = uni(sa[i]); sb[i] = uni(sb[i]); }
  const rsrc_t wsk = __builtin_amdgcn_make_buffer_rsrc(ws, 0, wr == 3 ? 128 * MB : 0u, 0x00020000);
  const rsrc_t outr = __builtin_amdgcn_make_buffer_rsrc(out, 0, 4 * MB, 0x00020000);
  u32x8 pa, pc;
#pragma unroll
  for (int i = 0; i < 8; ++i) { pa[i] = uni(ps[i]); pc[i] = uni(ps[8 + i]); }
  STAMP(17);

  vb[2] = tag[0]; vb[3] = tag[1];
  STAMP(19);
  vb[7] = stw;
  asm volatile(
"s_branch .Lmk_al\n"
".p2align 12\n"
".Lmk_al:\n"
"; @entry\n"
"s_mov_b32 s0, 0\n"
"s_mov_b32 s3, 0\n"
"s_waitcnt lgkmcnt(0)\n"
"ds_read_b128 v[24:27], v2\n"
"ds_read_b128 v[28:31], v2 offset:1024\n"
"s_waitcnt lgkmcnt(0)\n"
"buffer_store_dwordx4 v[24:27], v0, s[4:7], s61 offen sc1\n"
"buffer_store_dwordx4 v[28:31], v0, s[4:7], s61 offen offset:1024 sc1\n"
"buffer_store_dword v48, v14, s[4:7], s63 offen sc1\n"
"s_waitcnt vmcnt(11)\n"
"s_waitcnt lgkmcnt(0)\n"
"s_barrier\n"
"ds_read_b64_tr_b16 v[24:25], v40\n"
"ds_read_b64_tr_b16 v[26:27], v40 offset:1024\n"
"ds_read_b64_tr_b16 v[28:29], v40 offset:4096\n"
"ds_read_b64_tr_b16 v[30:31], v40 offset:5120\n"
"ds_read_b64_tr_b16 v[32:33], v40 offset:8192\n"
"ds_read_b64_tr_b16 v[34:35], v40 offset:9216\n"
"ds_read_b64_tr_b16 v[44:45], v40 offset:12288\n"
"ds_read_b64_tr_b16 v[46:47], v40 offset:13312\n"
"s_waitcnt lgkmcnt(2)\n"
"buffer_store_dwordx4 v[24:27], v0, s[4:7], s24 offen sc1\n"
"s_waitcnt lgkmcnt(0)\n"
"buffer_store_dwordx4 v[28:31], v0, s[4:7], s24 offen offset:1024 sc1\n"
"buffer_store_dwordx4 v[32:35], v0, s[4:7], s25 offen sc1\n"
"buffer_store_dwordx4 v[44:47], v0, s[4:7], s25 offen offset:1024 sc1\n"
"s_add_u32 s98, s75, 0\n"
"buffer_load_dwordx2 v[254:255], v13, s[4:7], s98 offen sc1\n"
"s_getpc_b64 s[84:85]\n"
".Lmk_h2:\n"
"s_and_b32 s84, s84, -16\n"
"s_and_b32 s85, s85, 0xffff\n"
"s_mov_b32 s86, (.Lmk_end-.Lmk_h2)&0xFFFFFFF0\n"
"s_mov_b32 s87, 0x20000\n"
"s_and_b32 s97, s26, 0xfffffc00\n"
"s_mov_b32 m0, 0x27800\n"
"s_add_u32 s97, s97, 0x1e000\n"
"buffer_load_dwordx4 v0, s[84:87], s97 offen lds\n"
"s_waitcnt vmcnt(1)\n"
"v_cmp_ne_u64_e32 vcc, v[254:255], v[18:19]\n"
"s_cbranch_vccnz .Lmk_f2s\n"
".Lmk_f2r:\n"
"s_add_u32 s98, s69, 0\n"
"buffer_load_dwordx4 a[0:3], v0, s[4:7], s98 offen sc1\n"
"buffer_load_dwordx4 a[4:7], v0, s[4:7], s98 offen offset:1024 sc1\n"
"buffer_load_dwordx4 a[8:11], v0, s[4:7], s98 offen offset:2048 sc1\n"
"buffer_load_dwordx4 a[12:15], v0, s[4:7], s98 offen offset:3072 sc1\n"
"buffer_load_dwordx4 a[16:19], v1, s[4:7], s98 offen sc1\n"
"buffer_load_dwordx4 a[20:23], v1, s[4:7], s98 offen offset:1024 sc1\n"
"buffer_load_dwordx4 a[24:27], v1, s[4:7], s98 offen offset:2048 sc1\n"
"buffer_load_dwordx4 a[28:31], v1, s[4:7], s98 offen offset:3072 sc1\n"
"s_add_u32 s98, s67, 0x1000\n"
"buffer_load_dwordx2 v[254:255], v12, s[4:7], s98 offen sc1\n"
"s_add_u32 s97, s69, 0x2000\n"
"buffer_load_dwordx4 a[32:35], v0, s[4:7], s97 offen sc1\n"
"buffer_load_dwordx4 a[36:39], v0, s[4:7], s97 offen offset:1024 sc1\n"
"buffer_load_dwordx4 a[40:43], v0, s[4:7], s97 offen offset:2048 sc1\n"
"buffer_load_dwordx4 a[44:47], v0, s[4:7], s97 offen offset:3072 sc1\n"
"buffer_load_dwordx4 a[48:51], v1, s[4:7], s97 offen sc1\n"
"buffer_load_dwordx4 a[52:55], v1, s[4:7], s97 offen offset:1024 sc1\n"
"buffer_load_dwordx4 a[56:59], v1, s[4:7], s97 offen offset:2048 sc1\n"
"buffer_load_dwordx4 a[60:63], v1, s[4:7], s97 offen offset:3072 sc1\n"
"s_waitcnt vmcnt(8)\n"
"v_cmp_ne_u64_e32 vcc, v[254:255], v[18:19]\n"
"s_cbranch_vccnz .Lmk_f1s\n"
".Lmk_f1r:\n"
"s_add_u32 s98, s52, 0x40000\n"
"buffer_load_dwordx4 v[24:27], v0, s[4:7], s98 offen sc1\n"
"buffer_load_dwordx4 v[28:31], v0, s[4:7], s98 offen offset:1024 sc1\n"
"buffer_load_dwordx4 v[32:35], v0, s[4:7], s98 offen offset:2048 sc1\n"
"buffer_load_dwordx4 v[44:47], v0, s[4:7], s98 offen offset:3072 sc1\n"
"buffer_load_dwordx4 v[48:51], v1, s[4:7], s98 offen sc1\n"
"buffer_load_dwordx4 v[52:55], v1, s[4:7], s98 offen offset:1024 sc1\n"
"buffer_load_dwordx4 v[56:59], v1, s[4:7], s98 offen offset:2048 sc1\n"
"buffer_load_dwordx4 v[60:63], v1, s[4:7], s98 offen offset:3072 sc1\n"
"s_waitcnt lgkmcnt(0)\n"
"s_barrier\n"
"s_add_u32 s98, s76, 0\n"
"v_bfe_u32 v255, v0, 2, 7\n"
"v_add_lshl_u32 v255, s72, v255, 1\n"
"v_and_b32_e32 v254, 0x200, v0\n"
"v_lshl_add_u32 v255, v254, 4, v255\n"
"buffer_load_dword v254, v255, s[4:7], s98 offen sc1\n"
"buffer_load_dword v253, v255, s[4:7], s98 offen offset:1024 sc1\n"
"buffer_load_dword v252, v255, s[4:7], s98 offen offset:2048 sc1\n"
"buffer_load_dword v251, v255, s[4:7], s98 offen offset:3072 sc1\n"
"s_add_u32 s98, s76, 0x1000\n"
"buffer_load_dword v250, v255, s[4:7], s98 offen sc1\n"
"buffer_load_dword v249, v255, s[4:7], s98 offen offset:1024 sc1\n"
"buffer_load_dword v248, v255, s[4:7], s98 offen offset:2048 sc1\n"
"buffer_load_dword v247, v255, s[4:7], s98 offen offset:3072 sc1\n"
"s_add_u32 s98, s69, 0x4000\n"
"buffer_load_dwordx4 a[64:67], v0, s[4:7], s98 offen sc1\n"
"buffer_load_dwordx4 a[68:71], v0, s[4:7], s98 offen offset:1024 sc1\n"
"buffer_load_dwordx4 a[72:75], v0, s[4:7], s98 offen offset:2048 sc1\n"
"buffer_load_dwordx4 a[76:79], v0, s[4:7], s98 offen offset:3072 sc1\n"
"buffer_load_dwordx4 a[80:83], v1, s[4:7], s98 offen sc1\n"
"buffer_load_dwordx4 a[84:87], v1, s[4:7], s98 offen offset:1024 sc1\n"
"buffer_load_dwordx4 a[88:91], v1, s[4:7], s98 offen offset:2048 sc1\n"
"buffer_load_dwordx4 a[92:95], v1, s[4:7], s98 offen offset:3072 sc1\n"
"s_add_u32 s98, s53, 0x40000\n"
"buffer_load_dwordx4 v[64:67], v0, s[4:7], s98 offen sc1\n"
"buffer_load_dwordx4 v[68:71], v0, s[4:7], s98 offen offset:1024 sc1\n"
"buffer_load_dwordx4 v[72:75], v0, s[4:7], s98 offen offset:2048 sc1\n"
"buffer_load_dwordx4 v[76:79], v0, s[4:7], s98 offen offset:3072 sc1\n"
"buffer_load_dwordx4 v[80:83], v1, s[4:7], s98 offen sc1\n"
"buffer_load_dwordx4 v[84:87], v1, s[4:7], s98 offen offset:1024 sc1\n"
"buffer_load_dwordx4 v[88:91], v1, s[4:7], s98 offen offset:2048 sc1\n"
"buffer_load_dwordx4 v[92:95], v1, s[4:7], s98 offen offset:3072 sc1\n"
"s_add_u32 s98, s69, 0x6000\n"
"buffer_load_dwordx4 a[96:99], v0, s[4:7], s98 offen sc1\n"
"buffer_load_dwordx4 a[100:103], v0, s[4:7], s98 offen offset:1024 sc1\n"
"buffer_load_dwordx4 a[104:107], v0, s[4:7], s98 offen offset:2048 sc1\n"
"buffer_load_dwordx4 a[108:111], v0, s[4:7], s98 offen offset:3072 sc1\n"
"buffer_load_dwordx4 a[112:115], v1, s[4:7], s98 offen sc1\n"
"buffer_load_dwordx4 a[116:119], v1, s[4:7], s98 offen offset:1024 sc1\n"
"buffer_load_dwordx4 a[120:123], v1, s[4:7], s98 offen offset:2048 sc1\n"
"buffer_load_dwordx4 a[124:127], v1, s[4:7], s98 offen offset:3072 sc1\n"
"s_waitcnt vmcnt(24)\n"
"v_add_f32_e32 v254, v254, v253\n"
"v_add_f32_e32 v254, v254, v252\n"
"v_add_f32_e32 v254, v254, v251\n"
"v_add_f32_e32 v254, v254, v250\n"
"v_add_f32_e32 v254, v254, v249\n"
"v_add_f32_e32 v254, v254, v248\n"
"v_add_f32_e32 v254, v254, v247\n"
"v_mov_b32_e32 v253, v254\n"
"s_nop 1\n"
"v_permlane32_swap_b32_e32 v254, v253\n"
"v_add_f32_e32 v254, v254, v253\n"
"ds_write_b32 v21, v254 offset:0\n"
".Lmk_z0:\n"
"; @A G1 0.0\n"
"v_mfma_f32_16x16x32_bf16 v[96:99], a[0:3], v[24:27], 0\n"
"s_add_u32 s98, s69, 0x8000\n"
"buffer_load_dwordx4 v[100:103], v0, s[4:7], s98 offen sc1\n"
"v_mfma_f32_16x16x32_bf16 v[96:99], a[4:7], v[28:31], v[96:99]\n"
"s_add_u32 s97, s62, 0x4000\n"
"v_mfma_f32_16x16x32_bf16 v[96:99], a[8:11], v[32:35], v[96:99]\n"
"buffer_load_dwordx4 v[104:107], v0, s[4:7], s98 offen offset:1024 sc1\n"
"v_mfma_f32_16x16x32_bf16 v[96:99], a[12:15], v[44:47], v[96:99]\n"
"v_mfma_f32_16x16x32_bf16 v[96:99], a[16:19], v[48:51], v[96:99]\n"
"buffer_load_dwordx4 v[108:111], v0, s[4:7], s98 offen offset:2048 sc1\n"
"v_mfma_f32_16x16x32_bf16 v[96:99], a[20:23], v[52:55], v[96:99]\n"
"buffer_load_dword v255, v10, s[4:7], s97 offen sc1\n"
"ds_read_b64_tr_b16 v[112:113], v36\n"
"v_mfma_f32_16x16x32_bf16 v[96:99], a[24:27], v[56:59], v[96:99]\n"
"buffer_load_dwordx4 v[116:119], v0, s[4:7], s98 offen offset:3072 sc1\n"
"ds_read_b64_tr_b16 v[114:115], v36 offset:2048\n"
"v_mfma_f32_16x16x32_bf16 v[96:99], a[28:31], v[60:63], v[96:99]\n"
"s_mov_b32 s94, 0xf000f\n"
"s_mov_b32 s95, 0xf000f\n"
"v_mov_b32_e32 v123, 0x3f803f80\n"
"v_mfma_f32_16x16x32_bf16 v[124:127], a[60:63], v[60:63], 0\n"
"buffer_load_dwordx4 v[128:131], v1, s[4:7], s98 offen sc1\n"
"v_cndmask_b32_e64 v120, 0, v123, s[94:95]\n"
"v_mfma_f32_16x16x32_bf16 v[124:127], a[56:59], v[56:59], v[124:127]\n"
"buffer_load_dword v254, v10, s[4:7], s97 offen offset:256 sc1\n"
"v_cndmask_b32_e64 v121, 0, v123, s[94:95]\n"
"v_mfma_f32_16x16x32_bf16 v[124:127], a[52:55], v[52:55], v[124:127]\n"
"buffer_load_dwordx4 v[132:135], v1, s[4:7], s98 offen offset:1024 sc1\n"
"v_cndmask_b32_e64 v122, 0, v123, s[94:95]\n"
"v_mfma_f32_16x16x32_bf16 v[124:127], a[48:51], v[48:51], v[124:127]\n"
"s_add_u32 s96, s68, 0x10000\n"
"v_cndmask_b32_e64 v123, 0, v123, s[94:95]\n"
"v_mfma_f32_16x16x32_bf16 v[124:127], a[44:47], v[44:47], v[124:127]\n"
"buffer_load_dwordx4 v[136:139], v1, s[4:7], s98 offen offset:2048 sc1\n"
"s_waitcnt lgkmcnt(0)\n"
"v_mfma_f32_16x16x32_bf16 v[140:143], v[120:123], v[112:115], 0\n"
"buffer_store_dwordx4 v[112:115], v0, s[4:7], s20 offen sc1\n"
"v_mfma_f32_16x16x32_bf16 v[124:127], a[40:43], v[32:35], v[124:127]\n"
"ds_write_b128 v3, v[96:99]\n"
"v_mfma_f32_16x16x32_bf16 v[124:127], a[36:39], v[28:31], v[124:127]\n"
"buffer_load_dwordx4 v[96:99], v1, s[4:7], s98 offen offset:3072 sc1\n"
"v_mfma_f32_16x16x32_bf16 v[124:127], a[32:35], v[24:27], v[124:127]\n"
"s_waitcnt vmcnt(31)\n"
"s_add_u32 s98, s69, 0xa000\n"
"v_mfma_f32_16x16x32_bf16 v[112:115], a[64:67], v[24:27], 0\n"
"buffer_load_dwordx4 v[120:123], v0, s[4:7], s98 offen sc1\n"
"v_mfma_f32_16x16x32_bf16 v[112:115], a[68:71], v[28:31], v[112:115]\n"
"buffer_store_dwordx2 v[18:19], v11, s[4:7], s96 offen offset:96 sc1\n"
"v_mfma_f32_16x16x32_bf16 v[112:115], a[72:75], v[32:35], v[112:115]\n"
"buffer_load_dwordx4 v[144:147], v0, s[4:7], s98 offen offset:1024 sc1\n"
"v_mfma_f32_16x16x32_bf16 v[112:115], a[76:79], v[44:47], v[112:115]\n"
"s_waitcnt vmcnt(30)\n"
"buffer_load_dword v253, v10, s[4:7], s97 offen offset:512 sc1\n"
"v_mfma_f32_16x16x32_bf16 v[112:115], a[80:83], v[48:51], v[112:115]\n"
"buffer_load_dwordx4 v[148:151], v0, s[4:7], s98 offen offset:2048 sc1\n"
"v_mfma_f32_16x16x32_bf16 v[112:115], a[84:87], v[52:55], v[112:115]\n"
"ds_write_b128 v3, v[124:127] offset:1024\n"
"v_mfma_f32_16x16x32_bf16 v[112:115], a[88:91], v[56:59], v[112:115]\n"
"buffer_load_dwordx4 v[124:127], v0, s[4:7], s98 offen offset:3072 sc1\n"
"v_mfma_f32_16x16x32_bf16 v[112:115], a[92:95], v[60:63], v[112:115]\n"
"s_waitcnt vmcnt(17)\n"
"buffer_load_dword v252, v10, s[4:7], s97 offen offset:768 sc1\n"
"v_mfma_f32_16x16x32_bf16 v[152:155], a[124:127], v[60:63], 0\n"
"buffer_load_dwordx4 v[156:159], v1, s[4:7], s98 offen sc1\n"
"v_mfma_f32_16x16x32_bf16 v[152:155], a[120:123], v[56:59], v[152:155]\n"
"v_mfma_f32_16x16x32_bf16 v[152:155], a[116:119], v[52:55], v[152:155]\n"
"buffer_load_dwordx4 v[160:163], v1, s[4:7], s98 offen offset:1024 sc1\n"
"v_mfma_f32_16x16x32_bf16 v[152:155], a[112:115], v[48:51], v[152:155]\n"
"buffer_load_dword v251, v10, s[4:7], s97 offen offset:1024 sc1\n"
"v_mfma_f32_16x16x32_bf16 v[152:155], a[108:111], v[44:47], v[152:155]\n"
"buffer_load_dwordx4 v[164:167], v1, s[4:7], s98 offen offset:2048 sc1\n"
"v_mfma_f32_16x16x32_bf16 v[152:155], a[104:107], v[32:35], v[152:155]\n"
"ds_write_b128 v3, v[112:115] offset:2048\n"
"v_mfma_f32_16x16x32_bf16 v[152:155], a[100:103], v[28:31], v[152:155]\n"
"buffer_load_dwordx4 v[112:115], v1, s[4:7], s98 offen offset:3072 sc1\n"
"v_mfma_f32_16x16x32_bf16 v[152:155], a[96:99], v[24:27], v[152:155]\n"
"s_waitcnt vmcnt(18)\n"
"buffer_load_dword v250, v10, s[4:7], s97 offen offset:1280 sc1\n"
"v_mfma_f32_16x16x32_bf16 v[168:171], v[100:103], v[24:27], 0\n"
"s_add_u32 s98, s54, 0x40000\n"
"buffer_load_dwordx4 v[172:175], v0, s[4:7], s98 offen sc1\n"
"v_mfma_f32_16x16x32_bf16 v[168:171], v[104:107], v[28:31], v[168:171]\n"
"v_mfma_f32_16x16x32_bf16 v[168:171], v[108:111], v[32:35], v[168:171]\n"
"buffer_load_dword v249, v10, s[4:7], s97 offen offset:1536 sc1\n"
"v_mfma_f32_16x16x32_bf16 v[168:171], v[116:119], v[44:47], v[168:171]\n"
"s_waitcnt vmcnt(15)\n"
"v_mfma_f32_16x16x32_bf16 v[168:171], v[128:131], v[48:51], v[168:171]\n"
"buffer_load_dwordx4 v[176:179], v0, s[4:7], s98 offen offset:1024 sc1\n"
"v_mfma_f32_16x16x32_bf16 v[168:171], v[132:135], v[52:55], v[168:171]\n"
"ds_write_b128 v3, v[152:155] offset:3072\n"
"v_mfma_f32_16x16x32_bf16 v[168:171], v[136:139], v[56:59], v[168:171]\n"
"buffer_load_dword v248, v10, s[4:7], s97 offen offset:1792 sc1\n"
"v_mfma_f32_16x16x32_bf16 v[168:171], v[96:99], v[60:63], v[168:171]\n"
"s_waitcnt vmcnt(5)\n"
"v_mfma_f32_16x16x32_bf16 v[152:155], v[112:115], v[60:63], 0\n"
"buffer_load_dwordx4 v[60:63], v0, s[4:7], s98 offen offset:2048 sc1\n"
"ds_read_b64_tr_b16 v[180:181], v36 offset:4096\n"
"v_mfma_f32_16x16x32_bf16 v[152:155], v[164:167], v[56:59], v[152:155]\n"
"ds_read_b64_tr_b16 v[182:183], v36 offset:6144\n"
"s_mov_b32 s96, 0xf000f\n"
"s_mov_b32 s97, 0xf000f\n"
"v_mfma_f32_16x16x32_bf16 v[152:155], v[160:163], v[52:55], v[152:155]\n"
"v_mov_b32_e32 v55, 0x3f803f80\n"
"v_cndmask_b32_e64 v52, 0, v55, s[96:97]\n"
"v_cndmask_b32_e64 v53, 0, v55, s[96:97]\n"
"v_mfma_f32_16x16x32_bf16 v[152:155], v[156:159], v[48:51], v[152:155]\n"
"v_cndmask_b32_e64 v54, 0, v55, s[96:97]\n"
"v_cndmask_b32_e64 v55, 0, v55, s[96:97]\n"
"v_mfma_f32_16x16x32_bf16 v[152:155], v[124:127], v[44:47], v[152:155]\n"
"buffer_load_dwordx4 v[44:47], v0, s[4:7], s98 offen offset:3072 sc1\n"
"s_waitcnt lgkmcnt(0)\n"
"v_mfma_f32_16x16x32_bf16 v[140:143], v[52:55], v[180:183], v[140:143]\n"
"buffer_store_dwordx4 v[180:183], v0, s[4:7], s20 offen offset:1024 sc1\n"
"v_mfma_f32_16x16x32_bf16 v[152:155], v[148:151], v[32:35], v[152:155]\n"
"ds_write_b128 v3, v[168:171] offset:4096\n"
"v_mfma_f32_16x16x32_bf16 v[152:155], v[144:147], v[28:31], v[152:155]\n"
"v_mfma_f32_16x16x32_bf16 v[152:155], v[120:123], v[24:27], v[152:155]\n"
"; @A G1 0.1\n"
"v_mfma_f32_16x16x32_bf16 v[24:27], a[0:3], v[64:67], 0\n"
"buffer_load_dwordx4 v[28:31], v1, s[4:7], s98 offen sc1\n"
"v_mfma_f32_16x16x32_bf16 v[24:27], a[4:7], v[68:71], v[24:27]\n"
"v_mfma_f32_16x16x32_bf16 v[24:27], a[8:11], v[72:75], v[24:27]\n"
"s_add_u32 s97, s55, 0x40000\n"
"buffer_load_dwordx4 v[32:35], v0, s[4:7], s97 offen sc1\n"
"v_mfma_f32_16x16x32_bf16 v[24:27], a[12:15], v[76:79], v[24:27]\n"
"v_mfma_f32_16x16x32_bf16 v[24:27], a[16:19], v[80:83], v[24:27]\n"
"buffer_load_dwordx4 v[48:51], v1, s[4:7], s98 offen offset:1024 sc1\n"
"v_mfma_f32_16x16x32_bf16 v[24:27], a[20:23], v[84:87], v[24:27]\n"
"ds_write_b128 v3, v[152:155] offset:5120\n"
"v_mfma_f32_16x16x32_bf16 v[24:27], a[24:27], v[88:91], v[24:27]\n"
"v_mfma_f32_16x16x32_bf16 v[24:27], a[28:31], v[92:95], v[24:27]\n"
"buffer_load_dwordx4 v[52:55], v0, s[4:7], s97 offen offset:1024 sc1\n"
"v_mfma_f32_16x16x32_bf16 v[56:59], a[60:63], v[92:95], 0\n"
"buffer_load_dwordx4 v[152:155], v1, s[4:7], s98 offen offset:2048 sc1\n"
"v_mfma_f32_16x16x32_bf16 v[56:59], a[56:59], v[88:91], v[56:59]\n"
"v_mfma_f32_16x16x32_bf16 v[56:59], a[52:55], v[84:87], v[56:59]\n"
"v_mfma_f32_16x16x32_bf16 v[56:59], a[48:51], v[80:83], v[56:59]\n"
"v_mfma_f32_16x16x32_bf16 v[56:59], a[44:47], v[76:79], v[56:59]\n"
"buffer_load_dwordx4 v[168:171], v1, s[4:7], s98 offen offset:3072 sc1\n"
"v_mfma_f32_16x16x32_bf16 v[56:59], a[40:43], v[72:75], v[56:59]\n"
"buffer_load_dwordx4 v[180:183], v0, s[4:7], s97 offen offset:2048 sc1\n"
"v_mfma_f32_16x16x32_bf16 v[56:59], a[36:39], v[68:71], v[56:59]\n"
"ds_write_b128 v3, v[24:27] offset:6144\n"
"v_mfma_f32_16x16x32_bf16 v[56:59], a[32:35], v[64:67], v[56:59]\n"
"v_mfma_f32_16x16x32_bf16 v[24:27], a[64:67], v[64:67], 0\n"
"v_mfma_f32_16x16x32_bf16 v[24:27], a[68:71], v[68:71], v[24:27]\n"
"buffer_load_dwordx4 v[184:187], v0, s[4:7], s97 offen offset:3072 sc1\n"
"v_mfma_f32_16x16x32_bf16 v[24:27], a[72:75], v[72:75], v[24:27]\n"
"v_mfma_f32_16x16x32_bf16 v[24:27], a[76:79], v[76:79], v[24:27]\n"
"v_mfma_f32_16x16x32_bf16 v[24:27], a[80:83], v[80:83], v[24:27]\n"
"v_mfma_f32_16x16x32_bf16 v[24:27], a[84:87], v[84:87], v[24:27]\n"
"s_nop 0\n"
"ds_write_b128 v3, v[56:59] offset:7168\n"
"v_mfma_f32_16x16x32_bf16 v[24:27], a[88:91], v[88:91], v[24:27]\n"
"buffer_load_dwordx4 v[56:59], v1, s[4:7], s97 offen sc1\n"
"v_mfma_f32_16x16x32_bf16 v[24:27], a[92:95], v[92:95], v[24:27]\n"
"v_mfma_f32_16x16x32_bf16 v[188:191], a[124:127], v[92:95], 0\n"
"v_mfma_f32_16x16x32_bf16 v[188:191], a[120:123], v[88:91], v[188:191]\n"
"v_mfma_f32_16x16x32_bf16 v[188:191], a[116:119], v[84:87], v[188:191]\n"
"v_mfma_f32_16x16x32_bf16 v[188:191], a[112:115], v[80:83], v[188:191]\n"
"buffer_load_dwordx4 v[192:195], v1, s[4:7], s97 offen offset:1024 sc1\n"
"v_mfma_f32_16x16x32_bf16 v[188:191], a[108:111], v[76:79], v[188:191]\n"
"v_mfma_f32_16x16x32_bf16 v[188:191], a[104:107], v[72:75], v[188:191]\n"
"s_nop 0\n"
"ds_write_b128 v3, v[24:27] offset:8192\n"
"v_mfma_f32_16x16x32_bf16 v[188:191], a[100:103], v[68:71], v[188:191]\n"
"v_mfma_f32_16x16x32_bf16 v[188:191], a[96:99], v[64:67], v[188:191]\n"
"v_mfma_f32_16x16x32_bf16 v[24:27], v[100:103], v[64:67], 0\n"
"buffer_load_dwordx4 v[196:199], v1, s[4:7], s97 offen offset:2048 sc1\n"
"v_mfma_f32_16x16x32_bf16 v[24:27], v[104:107], v[68:71], v[24:27]\n"
"v_mfma_f32_16x16x32_bf16 v[24:27], v[108:111], v[72:75], v[24:27]\n"
"v_mfma_f32_16x16x32_bf16 v[24:27], v[116:119], v[76:79], v[24:27]\n"
"v_mfma_f32_16x16x32_bf16 v[24:27], v[128:131], v[80:83], v[24:27]\n"
"s_waitcnt vmcnt(14)\n"
"ds_read_b64_tr_b16 v[200:201], v37\n"
"v_add_f32_e32 v255, v255, v251\n"
"v_add_f32_e32 v254, v254, v250\n"
"v_mfma_f32_16x16x32_bf16 v[24:27], v[132:135], v[84:87], v[24:27]\n"
"buffer_load_dwordx4 v[204:207], v1, s[4:7], s97 offen offset:3072 sc1\n"
"ds_read_b64_tr_b16 v[202:203], v37 offset:2048\n"
"v_mfma_f32_16x16x32_bf16 v[24:27], v[136:139], v[88:91], v[24:27]\n"
"ds_write_b128 v3, v[188:191] offset:9216\n"
"v_mfma_f32_16x16x32_bf16 v[24:27], v[96:99], v[92:95], v[24:27]\n"
"s_mov_b32 s96, 0xf000f0\n"
"v_add_f32_e32 v253, v253, v249\n"
"v_add_f32_e32 v252, v252, v248\n"
"s_mov_b32 s97, 0xf000f0\n"
"v_mfma_f32_16x16x32_bf16 v[188:191], v[112:115], v[92:95], 0\n"
"v_add_f32_e32 v255, v255, v253\n"
"v_add_f32_e32 v254, v254, v252\n"
"v_mov_b32_e32 v95, 0x3f803f80\n"
"v_mfma_f32_16x16x32_bf16 v[188:191], v[164:167], v[88:91], v[188:191]\n"
"v_add_f32_e32 v255, v255, v254\n"
"v_mov_b32_e32 v254, v255\n"
"v_cndmask_b32_e64 v92, 0, v95, s[96:97]\n"
"v_mfma_f32_16x16x32_bf16 v[188:191], v[160:163], v[84:87], v[188:191]\n"
"v_permlane32_swap_b32_e32 v255, v254\n"
"v_add_f32_e32 v255, v255, v254\n"
"v_cndmask_b32_e64 v93, 0, v95, s[96:97]\n"
"v_mfma_f32_16x16x32_bf16 v[188:191], v[156:159], v[80:83], v[188:191]\n"
"v_mul_f32_e32 v255, 0x3a800000, v255\n"
"v_mov_b32_e32 v254, v255\n"
"v_cndmask_b32_e64 v94, 0, v95, s[96:97]\n"
"v_mfma_f32_16x16x32_bf16 v[188:191], v[124:127], v[76:79], v[188:191]\n"
"v_permlane16_swap_b32_e32 v255, v254\n"
"v_cvt_pk_bf16_f32 v253, -v255, 0\n"
"v_cndmask_b32_e64 v95, 0, v95, s[96:97]\n"
"v_mfma_f32_16x16x32_bf16 v[188:191], v[148:151], v[72:75], v[188:191]\n"
"ds_write_b128 v3, v[24:27] offset:10240\n"
"v_mfma_f32_16x16x32_bf16 v[188:191], v[144:147], v[68:71], v[188:191]\n"
"s_waitcnt lgkmcnt(2)\n"
"v_fma_f32 v254, -v255, v255, v254\n"
"v_lshlrev_b32_e32 v253, 16, v253\n"
"v_mfma_f32_16x16x32_bf16 v[140:143], v[92:95], v[200:203], v[140:143]\n"
"buffer_store_dwordx4 v[200:203], v0, s[4:7], s21 offen sc1\n"
"v_mfma_f32_16x16x32_bf16 v[188:191], v[120:123], v[64:67], v[188:191]\n"
"s_waitcnt vmcnt(14)\n"
"v_max_f32_e32 v254, 0, v254\n"
"v_cvt_pk_bf16_f32 v254, v254, 0\n"
"; @A G1 0.2\n"
"v_mfma_f32_16x16x32_bf16 v[24:27], a[0:3], v[172:175], 0\n"
"v_lshlrev_b32_e32 v254, 16, v254\n"
"v_add_f32_e32 v254, 0x3727c5ac, v254\n"
"v_mfma_f32_16x16x32_bf16 v[24:27], a[4:7], v[176:179], v[24:27]\n"
"v_cvt_pk_bf16_f32 v254, v254, 0\n"
"v_lshlrev_b32_e32 v254, 16, v254\n"
"v_mfma_f32_16x16x32_bf16 v[24:27], a[8:11], v[60:63], v[24:27]\n"
"v_sqrt_f32_e32 v254, v254\n"
"v_mfma_f32_16x16x32_bf16 v[24:27], a[12:15], v[44:47], v[24:27]\n"
"s_waitcnt vmcnt(7)\n"
"v_cvt_pk_bf16_f32 v254, v254, 0\n"
"v_lshlrev_b32_e32 v254, 16, v254\n"
"v_mfma_f32_16x16x32_bf16 v[24:27], a[16:19], v[28:31], v[24:27]\n"
"v_rcp_f32_e32 v255, v254\n"
"v_mfma_f32_16x16x32_bf16 v[24:27], a[20:23], v[48:51], v[24:27]\n"
"ds_write_b128 v3, v[188:191] offset:11264\n"
"v_mfma_f32_16x16x32_bf16 v[24:27], a[24:27], v[152:155], v[24:27]\n"
"v_mul_f32_e32 v254, 0x3e8293ee, v255\n"
"v_mfma_f32_16x16x32_bf16 v[24:27], a[28:31], v[168:171], v[24:27]\n"
"v_mfma_f32_16x16x32_bf16 v[64:67], a[60:63], v[168:171], 0\n"
"v_mfma_f32_16x16x32_bf16 v[64:67], a[56:59], v[152:155], v[64:67]\n"
"v_mfma_f32_16x16x32_bf16 v[64:67], a[52:55], v[48:51], v[64:67]\n"
"s_add_u32 s98, s52, 0x48000\n"
"buffer_load_dwordx4 v[68:71], v0, s[4:7], s98 offen sc1\n"
"v_mfma_f32_16x16x32_bf16 v[64:67], a[48:51], v[28:31], v[64:67]\n"
"s_waitcnt lgkmcnt(0)\n"
"s_barrier\n"
"v_mfma_f32_16x16x32_bf16 v[64:67], a[44:47], v[44:47], v[64:67]\n"
"ds_read_b128 v[72:75], v4\n"
"ds_read_b128 v[76:79], v9 offset:128\n"
"v_mfma_f32_16x16x32_bf16 v[64:67], a[40:43], v[60:63], v[64:67]\n"
"ds_write_b128 v3, v[24:27] offset:12288\n"
"v_mfma_f32_16x16x32_bf16 v[64:67], a[36:39], v[176:179], v[64:67]\n"
"ds_read_b128 v[24:27], v5\n"
"v_mfma_f32_16x16x32_bf16 v[64:67], a[32:35], v[172:175], v[64:67]\n"
"buffer_load_dwordx4 v[80:83], v0, s[4:7], s98 offen offset:1024 sc1\n"
"v_mfma_f32_16x16x32_bf16 v[84:87], a[64:67], v[172:175], 0\n"
"v_mfma_f32_16x16x32_bf16 v[84:87], a[68:71], v[176:179], v[84:87]\n"
"v_mfma_f32_16x16x32_bf16 v[84:87], a[72:75], v[60:63], v[84:87]\n"
"v_mfma_f32_16x16x32_bf16 v[84:87], a[76:79], v[44:47], v[84:87]\n"
"ds_read_b128 v[88:91], v4 offset:1024\n"
"ds_read_b128 v[92:95], v9 offset:192\n"
"v_mfma_f32_16x16x32_bf16 v[84:87], a[80:83], v[28:31], v[84:87]\n"
"buffer_load_dwordx4 v[188:191], v0, s[4:7], s98 offen offset:2048 sc1\n"
"v_mfma_f32_16x16x32_bf16 v[84:87], a[84:87], v[48:51], v[84:87]\n"
"ds_write_b128 v3, v[64:67] offset:13312\n"
"v_mfma_f32_16x16x32_bf16 v[84:87], a[88:91], v[152:155], v[84:87]\n"
"ds_read_b128 v[64:67], v5 offset:1024\n"
"s_waitcnt lgkmcnt(4)\n"
"v_mfma_f32_16x16x32_bf16 v[84:87], a[92:95], v[168:171], v[84:87]\n"
"v_fmac_f32_e32 v72, v253, v76\n"
"v_fmac_f32_e32 v73, v253, v77\n"
"v_mfma_f32_16x16x32_bf16 v[200:203], a[124:127], v[168:171], 0\n"
"v_fmac_f32_e32 v74, v253, v78\n"
"v_fmac_f32_e32 v75, v253, v79\n"
"v_mfma_f32_16x16x32_bf16 v[200:203], a[120:123], v[152:155], v[200:203]\n"
"buffer_load_dwordx4 v[76:79], v0, s[4:7], s98 offen offset:3072 sc1\n"
"v_mfma_f32_16x16x32_bf16 v[200:203], a[116:119], v[48:51], v[200:203]\n"
"ds_read_b128 v[208:211], v4 offset:2048\n"
"ds_read_b128 v[212:215], v9 offset:256\n"
"v_mfma_f32_16x16x32_bf16 v[200:203], a[112:115], v[28:31], v[200:203]\n"
"ds_read_b128 v[216:219], v5 offset:2048\n"
"v_add_f32_e32 v72, v72, v24\n"
"v_mfma_f32_16x16x32_bf16 v[200:203], a[108:111], v[44:47], v[200:203]\n"
"v_add_f32_e32 v73, v73, v25\n"
"v_add_f32_e32 v74, v74, v26\n"
"v_mfma_f32_16x16x32_bf16 v[200:203], a[104:107], v[60:63], v[200:203]\n"
"ds_write_b128 v3, v[84:87] offset:14336\n"
"v_mfma_f32_16x16x32_bf16 v[200:203], a[100:103], v[176:179], v[200:203]\n"
"buffer_load_dwordx4 v[84:87], v1, s[4:7], s98 offen sc1\n"
"v_mfma_f32_16x16x32_bf16 v[200:203], a[96:99], v[172:175], v[200:203]\n"
"s_waitcnt lgkmcnt(4)\n"
"v_add_f32_e32 v75, v75, v27\n"
"v_fmac_f32_e32 v88, v253, v92\n"
"v_mfma_f32_16x16x32_bf16 v[24:27], v[100:103], v[172:175], 0\n"
"ds_read_b64_tr_b16 v[220:221], v37 offset:4096\n"
"v_fmac_f32_e32 v89, v253, v93\n"
"v_fmac_f32_e32 v90, v253, v94\n"
"v_mfma_f32_16x16x32_bf16 v[24:27], v[104:107], v[176:179], v[24:27]\n"
"ds_read_b128 v[224:227], v4 offset:3072\n"
"ds_read_b128 v[228:231], v9 offset:320\n"
"ds_read_b64_tr_b16 v[222:223], v37 offset:6144\n"
"v_mfma_f32_16x16x32_bf16 v[24:27], v[108:111], v[60:63], v[24:27]\n"
"ds_read_b128 v[232:235], v5 offset:3072\n"
"v_fmac_f32_e32 v91, v253, v95\n"
"s_mov_b32 s96, 0xf000f0\n"
"v_mfma_f32_16x16x32_bf16 v[24:27], v[116:119], v[44:47], v[24:27]\n"
"buffer_load_dwordx4 v[92:95], v1, s[4:7], s98 offen offset:1024 sc1\n"
"s_mov_b32 s97, 0xf000f0\n"
"v_mfma_f32_16x16x32_bf16 v[24:27], v[128:131], v[28:31], v[24:27]\n"
"v_add_f32_e32 v88, v88, v64\n"
"v_add_f32_e32 v89, v89, v65\n"
"v_mov_b32_e32 v239, 0x3f803f80\n"
"v_mfma_f32_16x16x32_bf16 v[24:27], v[132:135], v[48:51], v[24:27]\n"
"ds_write_b128 v3, v[200:203] offset:15360\n"
"v_mfma_f32_16x16x32_bf16 v[24:27], v[136:139], v[152:155], v[24:27]\n"
"s_waitcnt lgkmcnt(7)\n"
"v_add_f32_e32 v90, v90, v66\n"
"v_add_f32_e32 v91, v91, v67\n"
"v_cndmask_b32_e64 v236, 0, v239, s[96:97]\n"
"v_mfma_f32_16x16x32_bf16 v[24:27], v[96:99], v[168:171], v[24:27]\n"
"v_fmac_f32_e32 v208, v253, v212\n"
"v_fmac_f32_e32 v209, v253, v213\n"
"v_cndmask_b32_e64 v237, 0, v239, s[96:97]\n"
"v_mfma_f32_16x16x32_bf16 v[64:67], v[112:115], v[168:171], 0\n"
"buffer_load_dwordx4 v[168:171], v1, s[4:7], s98 offen offset:2048 sc1\n"
"v_cndmask_b32_e64 v238, 0, v239, s[96:97]\n"
"v_mfma_f32_16x16x32_bf16 v[64:67], v[164:167], v[152:155], v[64:67]\n"
"s_add_u32 s95, s68, s80\n"
"ds_read_b128 v[152:155], v4 offset:4096\n"
"v_cndmask_b32_e64 v239, 0, v239, s[96:97]\n"
"v_mfma_f32_16x16x32_bf16 v[64:67], v[160:163], v[48:51], v[64:67]\n"
"buffer_store_dwordx2 v[18:19], v11, s[4:7], s95 offen sc1\n"
"v_mfma_f32_16x16x32_bf16 v[64:67], v[156:159], v[28:31], v[64:67]\n"
"ds_read_b128 v[28:31], v9\n"
"ds_read_b128 v[48:51], v5 offset:4096\n"
"s_waitcnt lgkmcnt(5)\n"
"v_mfma_f32_16x16x32_bf16 v[140:143], v[236:239], v[220:223], v[140:143]\n"
"buffer_store_dwordx4 v[220:223], v0, s[4:7], s21 offen offset:1024 sc1\n"
"v_mfma_f32_16x16x32_bf16 v[64:67], v[124:127], v[44:47], v[64:67]\n"
"s_add_u32 s97, s75, 0x10000\n"
"buffer_load_dwordx2 v[250:251], v41, s[4:7], s97 offen sc1\n"
"v_mfma_f32_16x16x32_bf16 v[64:67], v[148:151], v[60:63], v[64:67]\n"
"buffer_load_dwordx4 v[44:47], v1, s[4:7], s98 offen offset:3072 sc1\n"
"v_mfma_f32_16x16x32_bf16 v[64:67], v[144:147], v[176:179], v[64:67]\n"
"ds_write_b128 v3, v[24:27] offset:16384\n"
"v_mfma_f32_16x16x32_bf16 v[64:67], v[120:123], v[172:175], v[64:67]\n"
"s_waitcnt vmcnt(16)\n"
"ds_read_b128 v[24:27], v4 offset:5120\n"
"ds_read_b128 v[60:63], v9 offset:64\n"
"; @A G1 0.3\n"
"v_mfma_f32_16x16x32_bf16 v[72:75], a[0:3], v[32:35], v[72:75]\n"
"ds_read_b128 v[172:175], v5 offset:5120\n"
"v_fmac_f32_e32 v210, v253, v214\n"
"v_mfma_f32_16x16x32_bf16 v[72:75], a[4:7], v[52:55], v[72:75]\n"
"v_fmac_f32_e32 v211, v253, v215\n"
"v_add_f32_e32 v208, v208, v216\n"
"v_mfma_f32_16x16x32_bf16 v[72:75], a[8:11], v[180:183], v[72:75]\n"
"s_add_u32 s98, s53, 0x48000\n"
"buffer_load_dwordx4 v[176:179], v0, s[4:7], s98 offen sc1\n"
"v_mfma_f32_16x16x32_bf16 v[72:75], a[12:15], v[184:187], v[72:75]\n"
"s_waitcnt vmcnt(13)\n"
"v_add_f32_e32 v209, v209, v217\n"
"v_add_f32_e32 v210, v210, v218\n"
"v_mfma_f32_16x16x32_bf16 v[72:75], a[16:19], v[56:59], v[72:75]\n"
"s_waitcnt lgkmcnt(8)\n"
"v_add_f32_e32 v211, v211, v219\n"
"v_fmac_f32_e32 v224, v253, v228\n"
"v_mfma_f32_16x16x32_bf16 v[72:75], a[20:23], v[192:195], v[72:75]\n"
"ds_write_b128 v3, v[64:67] offset:17408\n"
"v_mfma_f32_16x16x32_bf16 v[72:75], a[24:27], v[196:199], v[72:75]\n"
"v_fmac_f32_e32 v225, v253, v229\n"
"v_fmac_f32_e32 v226, v253, v230\n"
"v_mfma_f32_16x16x32_bf16 v[72:75], a[28:31], v[204:207], v[72:75]\n"
"buffer_load_dwordx4 v[64:67], v0, s[4:7], s98 offen offset:1024 sc1\n"
"v_mfma_f32_16x16x32_bf16 v[88:91], a[60:63], v[204:207], v[88:91]\n"
"v_fmac_f32_e32 v227, v253, v231\n"
"v_add_f32_e32 v224, v224, v232\n"
"v_mfma_f32_16x16x32_bf16 v[88:91], a[56:59], v[196:199], v[88:91]\n"
"v_add_f32_e32 v225, v225, v233\n"
"v_add_f32_e32 v226, v226, v234\n"
"v_mfma_f32_16x16x32_bf16 v[88:91], a[52:55], v[192:195], v[88:91]\n"
"s_waitcnt lgkmcnt(0)\n"
"s_barrier\n"
"v_mfma_f32_16x16x32_bf16 v[88:91], a[48:51], v[56:59], v[88:91]\n"
"ds_read_b128 v[200:203], v6\n"
"ds_read_b128 v[212:215], v6 offset:1024\n"
"v_mfma_f32_16x16x32_bf16 v[88:91], a[44:47], v[184:187], v[88:91]\n"
"buffer_load_dwordx4 v[216:219], v0, s[4:7], s98 offen offset:2048 sc1\n"
"v_mfma_f32_16x16x32_bf16 v[88:91], a[40:43], v[180:183], v[88:91]\n"
"v_add_f32_e32 v227, v227, v235\n"
"v_fmac_f32_e32 v152, v253, v28\n"
"v_mfma_f32_16x16x32_bf16 v[88:91], a[36:39], v[52:55], v[88:91]\n"
"v_fmac_f32_e32 v153, v253, v29\n"
"v_fmac_f32_e32 v154, v253, v30\n"
"v_mfma_f32_16x16x32_bf16 v[88:91], a[32:35], v[32:35], v[88:91]\n"
"v_fmac_f32_e32 v155, v253, v31\n"
"v_add_f32_e32 v152, v152, v48\n"
"v_mfma_f32_16x16x32_bf16 v[208:211], a[64:67], v[32:35], v[208:211]\n"
"v_add_f32_e32 v153, v153, v49\n"
"v_add_f32_e32 v154, v154, v50\n"
"v_mfma_f32_16x16x32_bf16 v[208:211], a[68:71], v[52:55], v[208:211]\n"
"buffer_load_dwordx4 v[28:31], v0, s[4:7], s98 offen offset:3072 sc1\n"
"v_mfma_f32_16x16x32_bf16 v[208:211], a[72:75], v[180:183], v[208:211]\n"
"v_add_f32_e32 v155, v155, v51\n"
"v_fmac_f32_e32 v24, v253, v60\n"
"v_mfma_f32_16x16x32_bf16 v[208:211], a[76:79], v[184:187], v[208:211]\n"
"v_fmac_f32_e32 v25, v253, v61\n"
"v_fmac_f32_e32 v26, v253, v62\n"
"v_mfma_f32_16x16x32_bf16 v[208:211], a[80:83], v[56:59], v[208:211]\n"
"s_waitcnt lgkmcnt(0)\n"
"v_fmac_f32_e32 v27, v253, v63\n"
"v_add_f32_e32 v24, v24, v172\n"
"v_mfma_f32_16x16x32_bf16 v[208:211], a[84:87], v[192:195], v[208:211]\n"
"v_add_f32_e32 v25, v25, v173\n"
"v_add_f32_e32 v26, v26, v174\n"
"v_mfma_f32_16x16x32_bf16 v[208:211], a[88:91], v[196:199], v[208:211]\n"
"buffer_load_dwordx4 v[48:51], v1, s[4:7], s98 offen sc1\n"
"v_mfma_f32_16x16x32_bf16 v[208:211], a[92:95], v[204:207], v[208:211]\n"
"ds_read_b128 v[60:63], v6 offset:2048\n"
"ds_read_b128 v[220:223], v6 offset:3072\n"
"v_mfma_f32_16x16x32_bf16 v[224:227], a[124:127], v[204:207], v[224:227]\n"
"v_add_f32_e32 v27, v27, v175\n"
"v_add_f32_e32 v72, v72, v200\n"
"v_mfma_f32_16x16x32_bf16 v[224:227], a[120:123], v[196:199], v[224:227]\n"
"v_add_f32_e32 v73, v73, v201\n"
"v_add_f32_e32 v74, v74, v202\n"
"v_mfma_f32_16x16x32_bf16 v[224:227], a[116:119], v[192:195], v[224:227]\n"
"v_add_f32_e32 v75, v75, v203\n"
"v_add_f32_e32 v88, v88, v212\n"
"v_mfma_f32_16x16x32_bf16 v[224:227], a[112:115], v[56:59], v[224:227]\n"
"buffer_load_dwordx4 v[172:175], v1, s[4:7], s98 offen offset:1024 sc1\n"
"v_mfma_f32_16x16x32_bf16 v[224:227], a[108:111], v[184:187], v[224:227]\n"
"ds_read_b64_tr_b16 v[200:201], v38\n"
"v_add_f32_e32 v89, v89, v213\n"
"v_add_f32_e32 v90, v90, v214\n"
"v_mfma_f32_16x16x32_bf16 v[224:227], a[104:107], v[180:183], v[224:227]\n"
"ds_read_b64_tr_b16 v[202:203], v38 offset:2048\n"
"v_add_f32_e32 v91, v91, v215\n"
"v_mul_f32_e32 v72, v72, v255\n"
"v_mfma_f32_16x16x32_bf16 v[224:227], a[100:103], v[52:55], v[224:227]\n"
"s_mov_b32 s94, 0xf000f00\n"
"v_mul_f32_e32 v73, v73, v255\n"
"v_mul_f32_e32 v74, v74, v255\n"
"s_mov_b32 s95, 0xf000f00\n"
"v_mfma_f32_16x16x32_bf16 v[224:227], a[96:99], v[32:35], v[224:227]\n"
"v_mul_f32_e32 v75, v75, v255\n"
"v_mul_f32_e32 v88, v88, v255\n"
"v_mov_b32_e32 v215, 0x3f803f80\n"
"v_mfma_f32_16x16x32_bf16 v[152:155], v[100:103], v[32:35], v[152:155]\n"
"buffer_load_dwordx4 v[228:231], v1, s[4:7], s98 offen offset:2048 sc1\n"
"v_cndmask_b32_e64 v212, 0, v215, s[94:95]\n"
"v_mfma_f32_16x16x32_bf16 v[152:155], v[104:107], v[52:55], v[152:155]\n"
"v_mul_f32_e32 v89, v89, v255\n"
"v_mul_f32_e32 v90, v90, v255\n"
"v_cndmask_b32_e64 v213, 0, v215, s[94:95]\n"
"v_mfma_f32_16x16x32_bf16 v[152:155], v[108:111], v[180:183], v[152:155]\n"
"v_mul_f32_e32 v91, v91, v255\n"
"v_cvt_pk_bf16_f32 v232, v72, v73\n"
"v_cndmask_b32_e64 v214, 0, v215, s[94:95]\n"
"v_mfma_f32_16x16x32_bf16 v[152:155], v[116:119], v[184:187], v[152:155]\n"
"v_cvt_pk_bf16_f32 v233, v74, v75\n"
"v_cvt_pk_bf16_f32 v234, v88, v89\n"
"v_cndmask_b32_e64 v215, 0, v215, s[94:95]\n"
"v_mfma_f32_16x16x32_bf16 v[152:155], v[128:131], v[56:59], v[152:155]\n"
"s_waitcnt lgkmcnt(2)\n"
"v_cvt_pk_bf16_f32 v235, v90, v91\n"
"v_add_f32_e32 v208, v208, v60\n"
"s_waitcnt lgkmcnt(0)\n"
"v_mfma_f32_16x16x32_bf16 v[140:143], v[212:215], v[200:203], v[140:143]\n"
"buffer_store_dwordx4 v[200:203], v0, s[4:7], s22 offen sc1\n"
"v_mfma_f32_16x16x32_bf16 v[152:155], v[132:135], v[192:195], v[152:155]\n"
"buffer_load_dwordx4 v[72:75], v1, s[4:7], s98 offen offset:3072 sc1\n"
"ds_write_b128 v7, v[232:235] offset:24576\n"
"v_mfma_f32_16x16x32_bf16 v[152:155], v[136:139], v[196:199], v[152:155]\n"
"ds_read_b128 v[88:91], v6 offset:4096\n"
"ds_read_b128 v[200:203], v6 offset:5120\n"
"v_mfma_f32_16x16x32_bf16 v[152:155], v[96:99], v[204:207], v[152:155]\n"
"v_add_f32_e32 v209, v209, v61\n"
"v_add_f32_e32 v210, v210, v62\n"
"v_mfma_f32_16x16x32_bf16 v[24:27], v[112:115], v[204:207], v[24:27]\n"
"s_waitcnt vmcnt(10)\n"
"v_cmp_ne_u64_e32 vcc, v[250:251], v[18:19]\n"
"v_mfma_f32_16x16x32_bf16 v[24:27], v[164:167], v[196:199], v[24:27]\n"
"s_cbranch_vccnz .Lmk_f6s\n"
".Lmk_f6r:\n"
"v_add_f32_e32 v211, v211, v63\n"
"v_mfma_f32_16x16x32_bf16 v[24:27], v[160:163], v[192:195], v[24:27]\n"
"v_add_f32_e32 v224, v224, v220\n"
"v_add_f32_e32 v225, v225, v221\n"
"v_mfma_f32_16x16x32_bf16 v[24:27], v[156:159], v[56:59], v[24:27]\n"
"v_add_f32_e32 v226, v226, v222\n"
"v_add_f32_e32 v227, v227, v223\n"
"v_mfma_f32_16x16x32_bf16 v[24:27], v[124:127], v[184:187], v[24:27]\n"
"v_mul_f32_e32 v208, v208, v255\n"
"v_mul_f32_e32 v209, v209, v255\n"
"v_mfma_f32_16x16x32_bf16 v[24:27], v[148:151], v[180:183], v[24:27]\n"
"v_mul_f32_e32 v210, v210, v255\n"
"v_mul_f32_e32 v211, v211, v255\n"
"v_mfma_f32_16x16x32_bf16 v[24:27], v[144:147], v[52:55], v[24:27]\n"
"v_mul_f32_e32 v224, v224, v255\n"
"v_mul_f32_e32 v225, v225, v255\n"
"v_mfma_f32_16x16x32_bf16 v[24:27], v[120:123], v[32:35], v[24:27]\n"
"v_mul_f32_e32 v226, v226, v255\n"
"v_mul_f32_e32 v227, v227, v255\n"
"v_cvt_pk_bf16_f32 v32, v208, v209\n"
"v_cvt_pk_bf16_f32 v33, v210, v211\n"
"v_cvt_pk_bf16_f32 v34, v224, v225\n"
"v_cvt_pk_bf16_f32 v35, v226, v227\n"
"ds_write_b128 v7, v[32:35] offset:57344\n"
".Lmk_z1:\n"
"; @A G1 1.0\n"
"v_mfma_f32_16x16x32_bf16 v[40:43], a[0:3], v[68:71], 0\n"
"v_mfma_f32_16x16x32_bf16 v[40:43], a[4:7], v[80:83], v[40:43]\n"
"s_add_u32 s98, s62, 0x4800\n"
"v_mfma_f32_16x16x32_bf16 v[40:43], a[8:11], v[188:191], v[40:43]\n"
"s_add_u32 s97, s54, 0x48000\n"
"buffer_load_dwordx4 v[52:55], v0, s[4:7], s97 offen sc1\n"
"v_mfma_f32_16x16x32_bf16 v[40:43], a[12:15], v[76:79], v[40:43]\n"
"s_waitcnt vmcnt(10)\n"
"v_mfma_f32_16x16x32_bf16 v[40:43], a[16:19], v[84:87], v[40:43]\n"
"buffer_load_dword v255, v10, s[4:7], s98 offen sc1\n"
"v_mfma_f32_16x16x32_bf16 v[40:43], a[20:23], v[92:95], v[40:43]\n"
"s_waitcnt lgkmcnt(1)\n"
"v_add_f32_e32 v152, v152, v88\n"
"v_add_f32_e32 v153, v153, v89\n"
"v_mfma_f32_16x16x32_bf16 v[40:43], a[24:27], v[168:171], v[40:43]\n"
"v_add_f32_e32 v154, v154, v90\n"
"v_add_f32_e32 v155, v155, v91\n"
"v_mfma_f32_16x16x32_bf16 v[40:43], a[28:31], v[44:47], v[40:43]\n"
"buffer_load_dwordx4 v[56:59], v0, s[4:7], s97 offen offset:1024 sc1\n"
"v_mfma_f32_16x16x32_bf16 v[60:63], a[60:63], v[44:47], 0\n"
"s_add_u32 s96, s70, 0\n"
"buffer_load_dwordx4 a[128:131], v0, s[4:7], s96 offen sc1\n"
"v_mfma_f32_16x16x32_bf16 v[60:63], a[56:59], v[168:171], v[60:63]\n"
"buffer_load_dword v253, v10, s[4:7], s98 offen offset:256 sc1\n"
"v_mfma_f32_16x16x32_bf16 v[60:63], a[52:55], v[92:95], v[60:63]\n"
"v_add_f32_e32 v24, v24, v200\n"
"v_add_f32_e32 v25, v25, v201\n"
"v_mfma_f32_16x16x32_bf16 v[60:63], a[48:51], v[84:87], v[60:63]\n"
"v_add_f32_e32 v26, v26, v202\n"
"v_add_f32_e32 v27, v27, v203\n"
"v_mfma_f32_16x16x32_bf16 v[60:63], a[44:47], v[76:79], v[60:63]\n"
"buffer_load_dwordx4 v[88:91], v0, s[4:7], s97 offen offset:2048 sc1\n"
"v_mfma_f32_16x16x32_bf16 v[60:63], a[40:43], v[188:191], v[60:63]\n"
"ds_write_b128 v3, v[40:43]\n"
"v_mfma_f32_16x16x32_bf16 v[60:63], a[36:39], v[80:83], v[60:63]\n"
"buffer_load_dword v252, v10, s[4:7], s98 offen offset:512 sc1\n"
"v_mfma_f32_16x16x32_bf16 v[60:63], a[32:35], v[68:71], v[60:63]\n"
"v_mul_f32_e32 v152, v152, v254\n"
"v_mul_f32_e32 v153, v153, v254\n"
"v_mfma_f32_16x16x32_bf16 v[40:43], a[64:67], v[68:71], 0\n"
"v_mul_f32_e32 v154, v154, v254\n"
"v_mul_f32_e32 v155, v155, v254\n"
"v_mfma_f32_16x16x32_bf16 v[40:43], a[68:71], v[80:83], v[40:43]\n"
"buffer_load_dwordx4 v[180:183], v0, s[4:7], s97 offen offset:3072 sc1\n"
"v_mfma_f32_16x16x32_bf16 v[40:43], a[72:75], v[188:191], v[40:43]\n"
"buffer_load_dword v251, v10, s[4:7], s98 offen offset:768 sc1\n"
"v_mfma_f32_16x16x32_bf16 v[40:43], a[76:79], v[76:79], v[40:43]\n"
"v_mul_f32_e32 v24, v24, v254\n"
"v_mul_f32_e32 v25, v25, v254\n"
"v_mfma_f32_16x16x32_bf16 v[40:43], a[80:83], v[84:87], v[40:43]\n"
"v_mul_f32_e32 v26, v26, v254\n"
"v_mul_f32_e32 v27, v27, v254\n"
"v_mfma_f32_16x16x32_bf16 v[40:43], a[84:87], v[92:95], v[40:43]\n"
"ds_write_b128 v3, v[60:63] offset:1024\n"
"v_mfma_f32_16x16x32_bf16 v[40:43], a[88:91], v[168:171], v[40:43]\n"
"buffer_load_dwordx4 v[60:63], v1, s[4:7], s97 offen sc1\n"
"v_mfma_f32_16x16x32_bf16 v[40:43], a[92:95], v[44:47], v[40:43]\n"
"buffer_load_dword v254, v10, s[4:7], s98 offen offset:1024 sc1\n"
"v_mfma_f32_16x16x32_bf16 v[184:187], a[124:127], v[44:47], 0\n"
"v_cvt_pk_bf16_f32 v192, v152, v153\n"
"v_cvt_pk_bf16_f32 v193, v154, v155\n"
"v_mfma_f32_16x16x32_bf16 v[184:187], a[120:123], v[168:171], v[184:187]\n"
"v_cvt_pk_bf16_f32 v194, v24, v25\n"
"v_cvt_pk_bf16_f32 v195, v26, v27\n"
"v_mfma_f32_16x16x32_bf16 v[184:187], a[116:119], v[92:95], v[184:187]\n"
"buffer_load_dword v250, v10, s[4:7], s98 offen offset:1280 sc1\n"
"v_mfma_f32_16x16x32_bf16 v[184:187], a[112:115], v[84:87], v[184:187]\n"
"buffer_load_dwordx4 v[24:27], v1, s[4:7], s97 offen offset:1024 sc1\n"
"v_mfma_f32_16x16x32_bf16 v[184:187], a[108:111], v[76:79], v[184:187]\n"
"buffer_load_dwordx4 a[132:135], v0, s[4:7], s96 offen offset:1024 sc1\n"
"v_mfma_f32_16x16x32_bf16 v[184:187], a[104:107], v[188:191], v[184:187]\n"
"ds_write_b128 v3, v[40:43] offset:2048\n"
"v_mfma_f32_16x16x32_bf16 v[184:187], a[100:103], v[80:83], v[184:187]\n"
"buffer_load_dword v249, v10, s[4:7], s98 offen offset:1536 sc1\n"
"v_mfma_f32_16x16x32_bf16 v[184:187], a[96:99], v[68:71], v[184:187]\n"
"ds_write_b128 v2, v[192:195] offset:49152\n"
"v_mfma_f32_16x16x32_bf16 v[40:43], v[100:103], v[68:71], 0\n"
"buffer_load_dwordx4 v[152:155], v1, s[4:7], s97 offen offset:2048 sc1\n"
"v_mfma_f32_16x16x32_bf16 v[40:43], v[104:107], v[80:83], v[40:43]\n"
"buffer_load_dword v248, v10, s[4:7], s98 offen offset:1792 sc1\n"
"v_mfma_f32_16x16x32_bf16 v[40:43], v[108:111], v[188:191], v[40:43]\n"
"v_mfma_f32_16x16x32_bf16 v[40:43], v[116:119], v[76:79], v[40:43]\n"
"v_mfma_f32_16x16x32_bf16 v[40:43], v[128:131], v[84:87], v[40:43]\n"
"v_mfma_f32_16x16x32_bf16 v[40:43], v[132:135], v[92:95], v[40:43]\n"
"buffer_load_dwordx4 v[192:195], v1, s[4:7], s97 offen offset:3072 sc1\n"
"v_mfma_f32_16x16x32_bf16 v[40:43], v[136:139], v[168:171], v[40:43]\n"
"ds_write_b128 v3, v[184:187] offset:3072\n"
"v_mfma_f32_16x16x32_bf16 v[40:43], v[96:99], v[44:47], v[40:43]\n"
"v_mfma_f32_16x16x32_bf16 v[184:187], v[112:115], v[44:47], 0\n"
"v_mfma_f32_16x16x32_bf16 v[184:187], v[164:167], v[168:171], v[184:187]\n"
"v_mfma_f32_16x16x32_bf16 v[184:187], v[160:163], v[92:95], v[184:187]\n"
"v_mfma_f32_16x16x32_bf16 v[184:187], v[156:159], v[84:87], v[184:187]\n"
"v_mfma_f32_16x16x32_bf16 v[184:187], v[124:127], v[76:79], v[184:187]\n"
"v_mfma_f32_16x16x32_bf16 v[184:187], v[148:151], v[188:191], v[184:187]\n"
"s_nop 1\n"
"ds_write_b128 v3, v[40:43] offset:4096\n"
"v_mfma_f32_16x16x32_bf16 v[184:187], v[144:147], v[80:83], v[184:187]\n"
"v_mfma_f32_16x16x32_bf16 v[184:187], v[120:123], v[68:71], v[184:187]\n"
"s_waitcnt vmcnt(23)\n"
"; @A G1 1.1\n"
"v_mfma_f32_16x16x32_bf16 v[40:43], a[0:3], v[176:179], 0\n"
"buffer_load_dwordx4 a[136:139], v0, s[4:7], s96 offen offset:2048 sc1\n"
"v_mfma_f32_16x16x32_bf16 v[40:43], a[4:7], v[64:67], v[40:43]\n"
"v_mov_b32_e32 v44, -1\n"
"v_mov_b32_e32 v45, -1\n"
"v_mfma_f32_16x16x32_bf16 v[40:43], a[8:11], v[216:219], v[40:43]\n"
"s_add_u32 s98, s55, 0x48000\n"
"buffer_load_dwordx4 v[68:71], v0, s[4:7], s98 offen sc1\n"
"v_mfma_f32_16x16x32_bf16 v[40:43], a[12:15], v[28:31], v[40:43]\n"
"s_waitcnt vmcnt(20)\n"
"v_mov_b32_e32 v46, -1\n"
"v_mov_b32_e32 v47, -1\n"
"v_mfma_f32_16x16x32_bf16 v[40:43], a[16:19], v[48:51], v[40:43]\n"
"ds_read_b64_tr_b16 v[76:77], v38 offset:4096\n"
"ds_read_b64_tr_b16 v[78:79], v38 offset:6144\n"
"s_mov_b32 s94, 0xf000f00\n"
"v_mfma_f32_16x16x32_bf16 v[40:43], a[20:23], v[172:175], v[40:43]\n"
"ds_write_b128 v3, v[184:187] offset:5120\n"
"v_mfma_f32_16x16x32_bf16 v[40:43], a[24:27], v[228:231], v[40:43]\n"
"s_mov_b32 s95, 0xf000f00\n"
"v_mov_b32_e32 v83, 0x3f803f80\n"
"v_cndmask_b32_e64 v80, 0, v83, s[94:95]\n"
"v_cndmask_b32_e64 v81, 0, v83, s[94:95]\n"
"v_mfma_f32_16x16x32_bf16 v[40:43], a[28:31], v[72:75], v[40:43]\n"
"buffer_load_dwordx4 v[84:87], v0, s[4:7], s98 offen offset:1024 sc1\n"
"v_cndmask_b32_e64 v82, 0, v83, s[94:95]\n"
"v_mfma_f32_16x16x32_bf16 v[92:95], a[60:63], v[72:75], 0\n"
"v_cndmask_b32_e64 v83, 0, v83, s[94:95]\n"
"v_mfma_f32_16x16x32_bf16 v[92:95], a[56:59], v[228:231], v[92:95]\n"
"s_waitcnt lgkmcnt(1)\n"
"v_mfma_f32_16x16x32_bf16 v[140:143], v[80:83], v[76:79], v[140:143]\n"
"buffer_store_dwordx4 v[76:79], v0, s[4:7], s22 offen offset:1024 sc1\n"
"v_mfma_f32_16x16x32_bf16 v[92:95], a[52:55], v[172:175], v[92:95]\n"
"v_mfma_f32_16x16x32_bf16 v[92:95], a[48:51], v[48:51], v[92:95]\n"
"v_mfma_f32_16x16x32_bf16 v[92:95], a[44:47], v[28:31], v[92:95]\n"
"buffer_load_dwordx4 v[76:79], v0, s[4:7], s98 offen offset:2048 sc1\n"
"v_mfma_f32_16x16x32_bf16 v[92:95], a[40:43], v[216:219], v[92:95]\n"
"ds_write_b128 v3, v[40:43] offset:6144\n"
"v_mfma_f32_16x16x32_bf16 v[92:95], a[36:39], v[64:67], v[92:95]\n"
"s_add_u32 s97, s67, 0\n"
"buffer_load_dwordx2 v[246:247], v12, s[4:7], s97 offen sc1\n"
"s_add_u32 s95, s64, 0x2000\n"
"v_mfma_f32_16x16x32_bf16 v[92:95], a[32:35], v[176:179], v[92:95]\n"
"buffer_store_dwordx4 v[232:235], v7, s[4:7], s95 offen sc1\n"
"v_mfma_f32_16x16x32_bf16 v[40:43], a[64:67], v[176:179], 0\n"
"v_mfma_f32_16x16x32_bf16 v[40:43], a[68:71], v[64:67], v[40:43]\n"
"buffer_load_dwordx4 v[80:83], v0, s[4:7], s98 offen offset:3072 sc1\n"
"v_mfma_f32_16x16x32_bf16 v[40:43], a[72:75], v[216:219], v[40:43]\n"
"v_mfma_f32_16x16x32_bf16 v[40:43], a[76:79], v[28:31], v[40:43]\n"
"v_mfma_f32_16x16x32_bf16 v[40:43], a[80:83], v[48:51], v[40:43]\n"
"buffer_load_dwordx4 a[140:143], v0, s[4:7], s96 offen offset:3072 sc1\n"
"v_mfma_f32_16x16x32_bf16 v[40:43], a[84:87], v[172:175], v[40:43]\n"
"ds_write_b128 v3, v[92:95] offset:7168\n"
"v_mfma_f32_16x16x32_bf16 v[40:43], a[88:91], v[228:231], v[40:43]\n"
"buffer_load_dwordx4 v[92:95], v1, s[4:7], s98 offen sc1\n"
"v_mfma_f32_16x16x32_bf16 v[40:43], a[92:95], v[72:75], v[40:43]\n"
"v_mfma_f32_16x16x32_bf16 v[168:171], a[124:127], v[72:75], 0\n"
"v_mfma_f32_16x16x32_bf16 v[168:171], a[120:123], v[228:231], v[168:171]\n"
"v_mfma_f32_16x16x32_bf16 v[168:171], a[116:119], v[172:175], v[168:171]\n"
"v_mfma_f32_16x16x32_bf16 v[168:171], a[112:115], v[48:51], v[168:171]\n"
"buffer_load_dwordx4 v[184:187], v1, s[4:7], s98 offen offset:1024 sc1\n"
"v_mfma_f32_16x16x32_bf16 v[168:171], a[108:111], v[28:31], v[168:171]\n"
"v_mfma_f32_16x16x32_bf16 v[168:171], a[104:107], v[216:219], v[168:171]\n"
"s_nop 0\n"
"ds_write_b128 v3, v[40:43] offset:8192\n"
"v_mfma_f32_16x16x32_bf16 v[168:171], a[100:103], v[64:67], v[168:171]\n"
"v_mfma_f32_16x16x32_bf16 v[168:171], a[96:99], v[176:179], v[168:171]\n"
"v_mfma_f32_16x16x32_bf16 v[40:43], v[100:103], v[176:179], 0\n"
"buffer_load_dwordx4 v[188:191], v1, s[4:7], s98 offen offset:2048 sc1\n"
"v_mfma_f32_16x16x32_bf16 v[40:43], v[104:107], v[64:67], v[40:43]\n"
"buffer_store_dwordx2 v[140:141], v39, s[4:7], s23 offen sc1\n"
"v_mfma_f32_16x16x32_bf16 v[40:43], v[108:111], v[216:219], v[40:43]\n"
"v_mfma_f32_16x16x32_bf16 v[40:43], v[116:119], v[28:31], v[40:43]\n"
"v_mfma_f32_16x16x32_bf16 v[40:43], v[128:131], v[48:51], v[40:43]\n"
"s_waitcnt vmcnt(14)\n"
"v_add_f32_e32 v255, v255, v254\n"
"v_add_f32_e32 v253, v253, v250\n"
"v_mfma_f32_16x16x32_bf16 v[40:43], v[132:135], v[172:175], v[40:43]\n"
"buffer_load_dwordx4 v[36:39], v1, s[4:7], s98 offen offset:3072 sc1\n"
"s_add_u32 s98, s64, 0x6000\n"
"v_mfma_f32_16x16x32_bf16 v[40:43], v[136:139], v[228:231], v[40:43]\n"
"ds_write_b128 v3, v[168:171] offset:9216\n"
"v_mfma_f32_16x16x32_bf16 v[40:43], v[96:99], v[72:75], v[40:43]\n"
"buffer_store_dwordx4 v[32:35], v7, s[4:7], s98 offen sc1\n"
"s_nop 1\n"
"v_mfma_f32_16x16x32_bf16 v[32:35], v[112:115], v[72:75], 0\n"
"buffer_load_dwordx4 a[144:147], v1, s[4:7], s96 offen sc1\n"
"v_mfma_f32_16x16x32_bf16 v[32:35], v[164:167], v[228:231], v[32:35]\n"
"v_add_f32_e32 v252, v252, v249\n"
"v_add_f32_e32 v251, v251, v248\n"
"v_mfma_f32_16x16x32_bf16 v[32:35], v[160:163], v[172:175], v[32:35]\n"
"v_add_f32_e32 v255, v255, v252\n"
"v_add_f32_e32 v253, v253, v251\n"
"v_mfma_f32_16x16x32_bf16 v[32:35], v[156:159], v[48:51], v[32:35]\n"
"v_add_f32_e32 v255, v255, v253\n"
"v_mov_b32_e32 v253, v255\n"
"v_mfma_f32_16x16x32_bf16 v[32:35], v[124:127], v[28:31], v[32:35]\n"
"s_nop 0\n"
"v_permlane32_swap_b32_e32 v255, v253\n"
"v_add_f32_e32 v255, v255, v253\n"
"v_mfma_f32_16x16x32_bf16 v[32:35], v[148:151], v[216:219], v[32:35]\n"
"ds_write_b128 v3, v[40:43] offset:10240\n"
"v_mfma_f32_16x16x32_bf16 v[32:35], v[144:147], v[64:67], v[32:35]\n"
"v_mul_f32_e32 v255, 0x3a800000, v255\n"
"v_mov_b32_e32 v253, v255\n"
"v_mfma_f32_16x16x32_bf16 v[32:35], v[120:123], v[176:179], v[32:35]\n"
"s_nop 0\n"
"v_permlane16_swap_b32_e32 v255, v253\n"
"v_cvt_pk_bf16_f32 v254, -v255, 0\n"
"; @A G1 1.2\n"
"v_mfma_f32_16x16x32_bf16 v[28:31], a[0:3], v[52:55], 0\n"
"v_fma_f32 v253, -v255, v255, v253\n"
"v_lshlrev_b32_e32 v254, 16, v254\n"
"v_mfma_f32_16x16x32_bf16 v[28:31], a[4:7], v[56:59], v[28:31]\n"
"s_waitcnt vmcnt(10)\n"
"v_cmp_ne_u64_e32 vcc, v[246:247], v[18:19]\n"
"v_mfma_f32_16x16x32_bf16 v[28:31], a[8:11], v[88:91], v[28:31]\n"
"s_cbranch_vccnz .Lmk_f3s\n"
".Lmk_f3r:\n"
"v_max_f32_e32 v253, 0, v253\n"
"v_mfma_f32_16x16x32_bf16 v[28:31], a[12:15], v[180:183], v[28:31]\n"
"v_cvt_pk_bf16_f32 v253, v253, 0\n"
"v_lshlrev_b32_e32 v253, 16, v253\n"
"v_mfma_f32_16x16x32_bf16 v[28:31], a[16:19], v[60:63], v[28:31]\n"
"v_add_f32_e32 v253, 0x3727c5ac, v253\n"
"v_cvt_pk_bf16_f32 v253, v253, 0\n"
"v_mfma_f32_16x16x32_bf16 v[28:31], a[20:23], v[24:27], v[28:31]\n"
"ds_write_b128 v3, v[32:35] offset:11264\n"
"v_mfma_f32_16x16x32_bf16 v[28:31], a[24:27], v[152:155], v[28:31]\n"
"v_lshlrev_b32_e32 v253, 16, v253\n"
"v_mfma_f32_16x16x32_bf16 v[28:31], a[28:31], v[192:195], v[28:31]\n"
"v_sqrt_f32_e32 v253, v253\n"
"v_mfma_f32_16x16x32_bf16 v[32:35], a[60:63], v[192:195], 0\n"
"v_cvt_pk_bf16_f32 v253, v253, 0\n"
"v_lshlrev_b32_e32 v253, 16, v253\n"
"v_mfma_f32_16x16x32_bf16 v[32:35], a[56:59], v[152:155], v[32:35]\n"
"v_rcp_f32_e32 v255, v253\n"
"v_mfma_f32_16x16x32_bf16 v[32:35], a[52:55], v[24:27], v[32:35]\n"
"s_add_u32 s98, s52, 0x8000\n"
"buffer_load_dwordx4 v[40:43], v0, s[4:7], s98 offen sc1\n"
"v_mfma_f32_16x16x32_bf16 v[32:35], a[48:51], v[60:63], v[32:35]\n"
"s_waitcnt lgkmcnt(0)\n"
"s_barrier\n"
"v_mfma_f32_16x16x32_bf16 v[32:35], a[44:47], v[180:183], v[32:35]\n"
"buffer_load_dwordx4 a[148:151], v1, s[4:7], s96 offen offset:1024 sc1\n"
"v_mfma_f32_16x16x32_bf16 v[32:35], a[40:43], v[88:91], v[32:35]\n"
"ds_write_b128 v3, v[28:31] offset:12288\n"
"v_mfma_f32_16x16x32_bf16 v[32:35], a[36:39], v[56:59], v[32:35]\n"
"ds_read_b128 v[28:31], v4\n"
"v_mul_f32_e32 v253, 0x3e8293ee, v255\n"
"v_mfma_f32_16x16x32_bf16 v[32:35], a[32:35], v[52:55], v[32:35]\n"
"buffer_load_dwordx4 v[48:51], v0, s[4:7], s98 offen offset:1024 sc1\n"
"v_mfma_f32_16x16x32_bf16 v[64:67], a[64:67], v[52:55], 0\n"
"ds_read_b128 v[72:75], v9 offset:128\n"
"ds_read_b128 v[140:143], v5\n"
"v_mfma_f32_16x16x32_bf16 v[64:67], a[68:71], v[56:59], v[64:67]\n"
"v_mfma_f32_16x16x32_bf16 v[64:67], a[72:75], v[88:91], v[64:67]\n"
"s_add_u32 s97, s60, 0x48000\n"
"buffer_store_dwordx4 v[44:47], v0, s[4:7], s97 offen sc1\n"
"v_mfma_f32_16x16x32_bf16 v[64:67], a[76:79], v[180:183], v[64:67]\n"
"ds_read_b128 v[168:171], v4 offset:1024\n"
"ds_read_b128 v[172:175], v9 offset:192\n"
"v_mfma_f32_16x16x32_bf16 v[64:67], a[80:83], v[60:63], v[64:67]\n"
"buffer_load_dwordx4 v[176:179], v0, s[4:7], s98 offen offset:2048 sc1\n"
"v_mfma_f32_16x16x32_bf16 v[64:67], a[84:87], v[24:27], v[64:67]\n"
"ds_write_b128 v3, v[32:35] offset:13312\n"
"v_mfma_f32_16x16x32_bf16 v[64:67], a[88:91], v[152:155], v[64:67]\n"
"ds_read_b128 v[32:35], v5 offset:1024\n"
"s_waitcnt lgkmcnt(4)\n"
"v_mfma_f32_16x16x32_bf16 v[64:67], a[92:95], v[192:195], v[64:67]\n"
"v_fmac_f32_e32 v28, v254, v72\n"
"v_fmac_f32_e32 v29, v254, v73\n"
"v_mfma_f32_16x16x32_bf16 v[196:199], a[124:127], v[192:195], 0\n"
"v_fmac_f32_e32 v30, v254, v74\n"
"v_fmac_f32_e32 v31, v254, v75\n"
"v_mfma_f32_16x16x32_bf16 v[196:199], a[120:123], v[152:155], v[196:199]\n"
"buffer_load_dwordx4 v[72:75], v0, s[4:7], s98 offen offset:3072 sc1\n"
"v_mfma_f32_16x16x32_bf16 v[196:199], a[116:119], v[24:27], v[196:199]\n"
"ds_read_b128 v[200:203], v4 offset:2048\n"
"ds_read_b128 v[204:207], v9 offset:256\n"
"v_mfma_f32_16x16x32_bf16 v[196:199], a[112:115], v[60:63], v[196:199]\n"
"ds_read_b128 v[208:211], v5 offset:2048\n"
"v_add_f32_e32 v28, v28, v140\n"
"v_mfma_f32_16x16x32_bf16 v[196:199], a[108:111], v[180:183], v[196:199]\n"
"v_add_f32_e32 v29, v29, v141\n"
"v_add_f32_e32 v30, v30, v142\n"
"v_mfma_f32_16x16x32_bf16 v[196:199], a[104:107], v[88:91], v[196:199]\n"
"ds_write_b128 v3, v[64:67] offset:14336\n"
"v_mfma_f32_16x16x32_bf16 v[196:199], a[100:103], v[56:59], v[196:199]\n"
"buffer_load_dwordx4 v[64:67], v1, s[4:7], s98 offen sc1\n"
"v_mfma_f32_16x16x32_bf16 v[196:199], a[96:99], v[52:55], v[196:199]\n"
"s_waitcnt lgkmcnt(4)\n"
"v_add_f32_e32 v31, v31, v143\n"
"v_fmac_f32_e32 v168, v254, v172\n"
"v_mfma_f32_16x16x32_bf16 v[140:143], v[100:103], v[52:55], 0\n"
"buffer_load_dwordx4 a[152:155], v1, s[4:7], s96 offen offset:2048 sc1\n"
"v_mfma_f32_16x16x32_bf16 v[140:143], v[104:107], v[56:59], v[140:143]\n"
"ds_read_b128 v[212:215], v4 offset:3072\n"
"ds_read_b128 v[216:219], v9 offset:320\n"
"v_mfma_f32_16x16x32_bf16 v[140:143], v[108:111], v[88:91], v[140:143]\n"
"ds_read_b128 v[220:223], v5 offset:3072\n"
"v_fmac_f32_e32 v169, v254, v173\n"
"v_mfma_f32_16x16x32_bf16 v[140:143], v[116:119], v[180:183], v[140:143]\n"
"buffer_load_dwordx4 v[224:227], v1, s[4:7], s98 offen offset:1024 sc1\n"
"v_mfma_f32_16x16x32_bf16 v[140:143], v[128:131], v[60:63], v[140:143]\n"
"v_fmac_f32_e32 v170, v254, v174\n"
"v_fmac_f32_e32 v171, v254, v175\n"
"v_mfma_f32_16x16x32_bf16 v[140:143], v[132:135], v[24:27], v[140:143]\n"
"ds_write_b128 v3, v[196:199] offset:15360\n"
"v_mfma_f32_16x16x32_bf16 v[140:143], v[136:139], v[152:155], v[140:143]\n"
"v_add_f32_e32 v168, v168, v32\n"
"v_add_f32_e32 v169, v169, v33\n"
"v_mfma_f32_16x16x32_bf16 v[140:143], v[96:99], v[192:195], v[140:143]\n"
"s_waitcnt lgkmcnt(5)\n"
"v_add_f32_e32 v170, v170, v34\n"
"v_add_f32_e32 v171, v171, v35\n"
"v_mfma_f32_16x16x32_bf16 v[32:35], v[112:115], v[192:195], 0\n"
"buffer_load_dwordx4 v[172:175], v1, s[4:7], s98 offen offset:2048 sc1\n"
"v_mfma_f32_16x16x32_bf16 v[32:35], v[164:167], v[152:155], v[32:35]\n"
"ds_read_b128 v[152:155], v4 offset:4096\n"
"ds_read_b128 v[192:195], v9\n"
"v_mfma_f32_16x16x32_bf16 v[32:35], v[160:163], v[24:27], v[32:35]\n"
"ds_read_b128 v[24:27], v5 offset:4096\n"
"v_fmac_f32_e32 v200, v254, v204\n"
"v_mfma_f32_16x16x32_bf16 v[32:35], v[156:159], v[60:63], v[32:35]\n"
"v_fmac_f32_e32 v201, v254, v205\n"
"v_fmac_f32_e32 v202, v254, v206\n"
"v_mfma_f32_16x16x32_bf16 v[32:35], v[124:127], v[180:183], v[32:35]\n"
"v_fmac_f32_e32 v203, v254, v207\n"
"v_add_f32_e32 v200, v200, v208\n"
"v_mfma_f32_16x16x32_bf16 v[32:35], v[148:151], v[88:91], v[32:35]\n"
"buffer_load_dwordx4 v[60:63], v1, s[4:7], s98 offen offset:3072 sc1\n"
"v_mfma_f32_16x16x32_bf16 v[32:35], v[144:147], v[56:59], v[32:35]\n"
"ds_write_b128 v3, v[140:143] offset:16384\n"
"v_mfma_f32_16x16x32_bf16 v[32:35], v[120:123], v[52:55], v[32:35]\n"
"s_waitcnt vmcnt(19)\n"
"ds_read_b128 v[52:55], v4 offset:5120\n"
"ds_read_b128 v[56:59], v9 offset:64\n"
"; @A G1 1.3\n"
"v_mfma_f32_16x16x32_bf16 v[28:31], a[0:3], v[68:71], v[28:31]\n"
"ds_read_b128 v[88:91], v5 offset:5120\n"
"v_add_f32_e32 v201, v201, v209\n"
"v_mfma_f32_16x16x32_bf16 v[28:31], a[4:7], v[84:87], v[28:31]\n"
"s_waitcnt lgkmcnt(8)\n"
"v_add_f32_e32 v202, v202, v210\n"
"v_add_f32_e32 v203, v203, v211\n"
"v_mfma_f32_16x16x32_bf16 v[28:31], a[8:11], v[76:79], v[28:31]\n"
"s_add_u32 s98, s53, 0x8000\n"
"buffer_load_dwordx4 v[140:143], v0, s[4:7], s98 offen sc1\n"
"v_mfma_f32_16x16x32_bf16 v[28:31], a[12:15], v[80:83], v[28:31]\n"
"s_waitcnt vmcnt(14)\n"
"s_add_u32 s97, s60, 0x40000\n"
"buffer_store_dwordx4 v[44:47], v0, s[4:7], s97 offen sc1\n"
"v_fmac_f32_e32 v212, v254, v216\n"
"v_fmac_f32_e32 v213, v254, v217\n"
"v_mfma_f32_16x16x32_bf16 v[28:31], a[16:19], v[92:95], v[28:31]\n"
"buffer_load_dwordx4 a[156:159], v1, s[4:7], s96 offen offset:3072 sc1\n"
"v_mfma_f32_16x16x32_bf16 v[28:31], a[20:23], v[184:187], v[28:31]\n"
"ds_write_b128 v3, v[32:35] offset:17408\n"
"v_mfma_f32_16x16x32_bf16 v[28:31], a[24:27], v[188:191], v[28:31]\n"
"v_fmac_f32_e32 v214, v254, v218\n"
"v_fmac_f32_e32 v215, v254, v219\n"
"v_mfma_f32_16x16x32_bf16 v[28:31], a[28:31], v[36:39], v[28:31]\n"
"buffer_load_dwordx4 v[32:35], v0, s[4:7], s98 offen offset:1024 sc1\n"
"v_mfma_f32_16x16x32_bf16 v[168:171], a[60:63], v[36:39], v[168:171]\n"
"v_add_f32_e32 v212, v212, v220\n"
"v_add_f32_e32 v213, v213, v221\n"
"v_mfma_f32_16x16x32_bf16 v[168:171], a[56:59], v[188:191], v[168:171]\n"
"s_waitcnt lgkmcnt(5)\n"
"v_add_f32_e32 v214, v214, v222\n"
"v_add_f32_e32 v215, v215, v223\n"
"v_mfma_f32_16x16x32_bf16 v[168:171], a[52:55], v[184:187], v[168:171]\n"
"s_waitcnt lgkmcnt(0)\n"
"s_barrier\n"
"v_mfma_f32_16x16x32_bf16 v[168:171], a[48:51], v[92:95], v[168:171]\n"
"ds_read_b128 v[180:183], v6\n"
"ds_read_b128 v[196:199], v6 offset:1024\n"
"v_mfma_f32_16x16x32_bf16 v[168:171], a[44:47], v[80:83], v[168:171]\n"
"buffer_load_dwordx4 v[204:207], v0, s[4:7], s98 offen offset:2048 sc1\n"
"v_mfma_f32_16x16x32_bf16 v[168:171], a[40:43], v[76:79], v[168:171]\n"
"v_fmac_f32_e32 v152, v254, v192\n"
"v_fmac_f32_e32 v153, v254, v193\n"
"v_mfma_f32_16x16x32_bf16 v[168:171], a[36:39], v[84:87], v[168:171]\n"
"v_fmac_f32_e32 v154, v254, v194\n"
"v_fmac_f32_e32 v155, v254, v195\n"
"v_mfma_f32_16x16x32_bf16 v[168:171], a[32:35], v[68:71], v[168:171]\n"
"v_add_f32_e32 v152, v152, v24\n"
"v_add_f32_e32 v153, v153, v25\n"
"v_mfma_f32_16x16x32_bf16 v[200:203], a[64:67], v[68:71], v[200:203]\n"
"v_add_f32_e32 v154, v154, v26\n"
"v_add_f32_e32 v155, v155, v27\n"
"v_mfma_f32_16x16x32_bf16 v[200:203], a[68:71], v[84:87], v[200:203]\n"
"buffer_load_dwordx4 v[24:27], v0, s[4:7], s98 offen offset:3072 sc1\n"
"v_mfma_f32_16x16x32_bf16 v[200:203], a[72:75], v[76:79], v[200:203]\n"
"v_fmac_f32_e32 v52, v254, v56\n"
"v_fmac_f32_e32 v53, v254, v57\n"
"v_mfma_f32_16x16x32_bf16 v[200:203], a[76:79], v[80:83], v[200:203]\n"
"v_fmac_f32_e32 v54, v254, v58\n"
"v_fmac_f32_e32 v55, v254, v59\n"
"v_mfma_f32_16x16x32_bf16 v[200:203], a[80:83], v[92:95], v[200:203]\n"
"s_waitcnt lgkmcnt(0)\n"
"v_add_f32_e32 v52, v52, v88\n"
"v_add_f32_e32 v53, v53, v89\n"
"v_mfma_f32_16x16x32_bf16 v[200:203], a[84:87], v[184:187], v[200:203]\n"
"v_add_f32_e32 v54, v54, v90\n"
"v_add_f32_e32 v55, v55, v91\n"
"v_mfma_f32_16x16x32_bf16 v[200:203], a[88:91], v[188:191], v[200:203]\n"
"buffer_load_dwordx4 v[56:59], v1, s[4:7], s98 offen sc1\n"
"v_mfma_f32_16x16x32_bf16 v[200:203], a[92:95], v[36:39], v[200:203]\n"
"s_add_u32 s97, s70, 0x2000\n"
"ds_read_b128 v[88:91], v6 offset:2048\n"
"ds_read_b128 v[192:195], v6 offset:3072\n"
"v_mfma_f32_16x16x32_bf16 v[212:215], a[124:127], v[36:39], v[212:215]\n"
"buffer_load_dwordx4 a[160:163], v0, s[4:7], s97 offen sc1\n"
"v_mfma_f32_16x16x32_bf16 v[212:215], a[120:123], v[188:191], v[212:215]\n"
"v_add_f32_e32 v28, v28, v180\n"
"v_add_f32_e32 v29, v29, v181\n"
"v_mfma_f32_16x16x32_bf16 v[212:215], a[116:119], v[184:187], v[212:215]\n"
"v_add_f32_e32 v30, v30, v182\n"
"v_add_f32_e32 v31, v31, v183\n"
"v_mfma_f32_16x16x32_bf16 v[212:215], a[112:115], v[92:95], v[212:215]\n"
"buffer_load_dwordx4 v[180:183], v1, s[4:7], s98 offen offset:1024 sc1\n"
"v_mfma_f32_16x16x32_bf16 v[212:215], a[108:111], v[80:83], v[212:215]\n"
"v_add_f32_e32 v168, v168, v196\n"
"v_add_f32_e32 v169, v169, v197\n"
"v_mfma_f32_16x16x32_bf16 v[212:215], a[104:107], v[76:79], v[212:215]\n"
"v_add_f32_e32 v170, v170, v198\n"
"v_add_f32_e32 v171, v171, v199\n"
"v_mfma_f32_16x16x32_bf16 v[212:215], a[100:103], v[84:87], v[212:215]\n"
"v_mul_f32_e32 v28, v28, v255\n"
"v_mul_f32_e32 v29, v29, v255\n"
"v_mfma_f32_16x16x32_bf16 v[212:215], a[96:99], v[68:71], v[212:215]\n"
"v_mul_f32_e32 v30, v30, v255\n"
"v_mul_f32_e32 v31, v31, v255\n"
"v_mfma_f32_16x16x32_bf16 v[152:155], v[100:103], v[68:71], v[152:155]\n"
"buffer_load_dwordx4 v[196:199], v1, s[4:7], s98 offen offset:2048 sc1\n"
"v_mfma_f32_16x16x32_bf16 v[152:155], v[104:107], v[84:87], v[152:155]\n"
"v_mul_f32_e32 v168, v168, v255\n"
"v_mul_f32_e32 v169, v169, v255\n"
"v_mfma_f32_16x16x32_bf16 v[152:155], v[108:111], v[76:79], v[152:155]\n"
"v_mul_f32_e32 v170, v170, v255\n"
"v_mul_f32_e32 v171, v171, v255\n"
"v_mfma_f32_16x16x32_bf16 v[152:155], v[116:119], v[80:83], v[152:155]\n"
"v_cvt_pk_bf16_f32 v208, v28, v29\n"
"v_cvt_pk_bf16_f32 v209, v30, v31\n"
"v_mfma_f32_16x16x32_bf16 v[152:155], v[128:131], v[92:95], v[152:155]\n"
"s_waitcnt lgkmcnt(0)\n"
"buffer_store_dwordx4 v[44:47], v0, s[4:7], s60 offen sc1\n"
"v_cvt_pk_bf16_f32 v210, v168, v169\n"
"v_cvt_pk_bf16_f32 v211, v170, v171\n"
"v_mfma_f32_16x16x32_bf16 v[152:155], v[132:135], v[184:187], v[152:155]\n"
"buffer_load_dwordx4 v[28:31], v1, s[4:7], s98 offen offset:3072 sc1\n"
"ds_write_b128 v8, v[208:211] offset:25600\n"
"v_mfma_f32_16x16x32_bf16 v[152:155], v[136:139], v[188:191], v[152:155]\n"
"ds_read_b128 v[44:47], v6 offset:4096\n"
"ds_read_b128 v[168:171], v6 offset:5120\n"
"v_mfma_f32_16x16x32_bf16 v[152:155], v[96:99], v[36:39], v[152:155]\n"
"v_add_f32_e32 v200, v200, v88\n"
"v_add_f32_e32 v201, v201, v89\n"
"v_mfma_f32_16x16x32_bf16 v[52:55], v[112:115], v[36:39], v[52:55]\n"
"v_add_f32_e32 v202, v202, v90\n"
"v_add_f32_e32 v203, v203, v91\n"
"v_mfma_f32_16x16x32_bf16 v[52:55], v[164:167], v[188:191], v[52:55]\n"
"v_add_f32_e32 v212, v212, v192\n"
"v_add_f32_e32 v213, v213, v193\n"
"v_mfma_f32_16x16x32_bf16 v[52:55], v[160:163], v[184:187], v[52:55]\n"
"v_add_f32_e32 v214, v214, v194\n"
"v_add_f32_e32 v215, v215, v195\n"
"v_mfma_f32_16x16x32_bf16 v[52:55], v[156:159], v[92:95], v[52:55]\n"
"v_mul_f32_e32 v200, v200, v255\n"
"v_mul_f32_e32 v201, v201, v255\n"
"v_mfma_f32_16x16x32_bf16 v[52:55], v[124:127], v[80:83], v[52:55]\n"
"v_mul_f32_e32 v202, v202, v255\n"
"v_mul_f32_e32 v203, v203, v255\n"
"v_mfma_f32_16x16x32_bf16 v[52:55], v[148:151], v[76:79], v[52:55]\n"
"v_mul_f32_e32 v212, v212, v255\n"
"v_mul_f32_e32 v213, v213, v255\n"
"v_mfma_f32_16x16x32_bf16 v[52:55], v[144:147], v[84:87], v[52:55]\n"
"v_mul_f32_e32 v214, v214, v255\n"
"v_mul_f32_e32 v215, v215, v255\n"
"v_mfma_f32_16x16x32_bf16 v[52:55], v[120:123], v[68:71], v[52:55]\n"
"v_cvt_pk_bf16_f32 v36, v200, v201\n"
"v_cvt_pk_bf16_f32 v37, v202, v203\n"
"v_cvt_pk_bf16_f32 v38, v212, v213\n"
"v_cvt_pk_bf16_f32 v39, v214, v215\n"
"ds_write_b128 v7, v[36:39] offset:58368\n"
".Lmk_z2:\n"
"s_waitcnt vmcnt(17)\n"
"; @A G1 2.0\n"
"v_mfma_f32_16x16x32_bf16 v[68:71], a[0:3], v[40:43], 0\n"
"v_mfma_f32_16x16x32_bf16 v[68:71], a[4:7], v[48:51], v[68:71]\n"
"s_add_u32 s98, s62, 0x800\n"
"v_mfma_f32_16x16x32_bf16 v[68:71], a[8:11], v[176:179], v[68:71]\n"
"s_add_u32 s96, s54, 0x8000\n"
"buffer_load_dwordx4 v[76:79], v0, s[4:7], s96 offen sc1\n"
"v_mfma_f32_16x16x32_bf16 v[68:71], a[12:15], v[72:75], v[68:71]\n"
"s_waitcnt vmcnt(13)\n"
"v_mfma_f32_16x16x32_bf16 v[68:71], a[16:19], v[64:67], v[68:71]\n"
"buffer_load_dword v255, v10, s[4:7], s98 offen sc1\n"
"v_mfma_f32_16x16x32_bf16 v[68:71], a[20:23], v[224:227], v[68:71]\n"
"s_waitcnt lgkmcnt(1)\n"
"v_add_f32_e32 v152, v152, v44\n"
"v_add_f32_e32 v153, v153, v45\n"
"v_mfma_f32_16x16x32_bf16 v[68:71], a[24:27], v[172:175], v[68:71]\n"
"v_add_f32_e32 v154, v154, v46\n"
"v_add_f32_e32 v155, v155, v47\n"
"v_mfma_f32_16x16x32_bf16 v[68:71], a[28:31], v[60:63], v[68:71]\n"
"buffer_load_dwordx4 v[44:47], v0, s[4:7], s96 offen offset:1024 sc1\n"
"v_mfma_f32_16x16x32_bf16 v[80:83], a[60:63], v[60:63], 0\n"
"buffer_load_dwordx4 a[164:167], v0, s[4:7], s97 offen offset:1024 sc1\n"
"v_mfma_f32_16x16x32_bf16 v[80:83], a[56:59], v[172:175], v[80:83]\n"
"buffer_load_dword v254, v10, s[4:7], s98 offen offset:256 sc1\n"
"v_mfma_f32_16x16x32_bf16 v[80:83], a[52:55], v[224:227], v[80:83]\n"
"v_add_f32_e32 v52, v52, v168\n"
"v_add_f32_e32 v53, v53, v169\n"
"v_mfma_f32_16x16x32_bf16 v[80:83], a[48:51], v[64:67], v[80:83]\n"
"v_add_f32_e32 v54, v54, v170\n"
"v_add_f32_e32 v55, v55, v171\n"
"v_mfma_f32_16x16x32_bf16 v[80:83], a[44:47], v[72:75], v[80:83]\n"
"buffer_load_dwordx4 v[84:87], v0, s[4:7], s96 offen offset:2048 sc1\n"
"v_mfma_f32_16x16x32_bf16 v[80:83], a[40:43], v[176:179], v[80:83]\n"
"ds_write_b128 v3, v[68:71]\n"
"v_mfma_f32_16x16x32_bf16 v[80:83], a[36:39], v[48:51], v[80:83]\n"
"buffer_load_dword v252, v10, s[4:7], s98 offen offset:512 sc1\n"
"v_mfma_f32_16x16x32_bf16 v[80:83], a[32:35], v[40:43], v[80:83]\n"
"v_mul_f32_e32 v152, v152, v253\n"
"v_mul_f32_e32 v153, v153, v253\n"
"v_mfma_f32_16x16x32_bf16 v[68:71], a[64:67], v[40:43], 0\n"
"v_mul_f32_e32 v154, v154, v253\n"
"v_mul_f32_e32 v155, v155, v253\n"
"v_mfma_f32_16x16x32_bf16 v[68:71], a[68:71], v[48:51], v[68:71]\n"
"buffer_load_dwordx4 v[88:91], v0, s[4:7], s96 offen offset:3072 sc1\n"
"v_mfma_f32_16x16x32_bf16 v[68:71], a[72:75], v[176:179], v[68:71]\n"
"buffer_load_dword v251, v10, s[4:7], s98 offen offset:768 sc1\n"
"v_mfma_f32_16x16x32_bf16 v[68:71], a[76:79], v[72:75], v[68:71]\n"
"v_mul_f32_e32 v52, v52, v253\n"
"v_mul_f32_e32 v53, v53, v253\n"
"v_mfma_f32_16x16x32_bf16 v[68:71], a[80:83], v[64:67], v[68:71]\n"
"v_mul_f32_e32 v54, v54, v253\n"
"v_mul_f32_e32 v55, v55, v253\n"
"v_mfma_f32_16x16x32_bf16 v[68:71], a[84:87], v[224:227], v[68:71]\n"
"ds_write_b128 v3, v[80:83] offset:1024\n"
"v_mfma_f32_16x16x32_bf16 v[68:71], a[88:91], v[172:175], v[68:71]\n"
"buffer_load_dwordx4 v[80:83], v1, s[4:7], s96 offen sc1\n"
"v_mfma_f32_16x16x32_bf16 v[68:71], a[92:95], v[60:63], v[68:71]\n"
"buffer_load_dword v253, v10, s[4:7], s98 offen offset:1024 sc1\n"
"v_mfma_f32_16x16x32_bf16 v[92:95], a[124:127], v[60:63], 0\n"
"v_cvt_pk_bf16_f32 v168, v152, v153\n"
"v_cvt_pk_bf16_f32 v169, v154, v155\n"
"v_mfma_f32_16x16x32_bf16 v[92:95], a[120:123], v[172:175], v[92:95]\n"
"v_cvt_pk_bf16_f32 v170, v52, v53\n"
"v_cvt_pk_bf16_f32 v171, v54, v55\n"
"v_mfma_f32_16x16x32_bf16 v[92:95], a[116:119], v[224:227], v[92:95]\n"
"buffer_load_dword v250, v10, s[4:7], s98 offen offset:1280 sc1\n"
"v_mfma_f32_16x16x32_bf16 v[92:95], a[112:115], v[64:67], v[92:95]\n"
"buffer_load_dwordx4 v[52:55], v1, s[4:7], s96 offen offset:1024 sc1\n"
"v_mfma_f32_16x16x32_bf16 v[92:95], a[108:111], v[72:75], v[92:95]\n"
"buffer_load_dwordx4 a[168:171], v0, s[4:7], s97 offen offset:2048 sc1\n"
"v_mfma_f32_16x16x32_bf16 v[92:95], a[104:107], v[176:179], v[92:95]\n"
"ds_write_b128 v3, v[68:71] offset:2048\n"
"v_mfma_f32_16x16x32_bf16 v[92:95], a[100:103], v[48:51], v[92:95]\n"
"buffer_load_dword v249, v10, s[4:7], s98 offen offset:1536 sc1\n"
"v_mfma_f32_16x16x32_bf16 v[92:95], a[96:99], v[40:43], v[92:95]\n"
"ds_write_b128 v2, v[168:171] offset:50176\n"
"v_mfma_f32_16x16x32_bf16 v[68:71], v[100:103], v[40:43], 0\n"
"buffer_load_dwordx4 v[152:155], v1, s[4:7], s96 offen offset:2048 sc1\n"
"s_add_u32 s95, s64, 0x2000\n"
"v_mfma_f32_16x16x32_bf16 v[68:71], v[104:107], v[48:51], v[68:71]\n"
"buffer_load_dword v248, v10, s[4:7], s98 offen offset:1792 sc1\n"
"v_mfma_f32_16x16x32_bf16 v[68:71], v[108:111], v[176:179], v[68:71]\n"
"buffer_store_dwordx4 v[208:211], v8, s[4:7], s95 offen offset:1024 sc1\n"
"v_mfma_f32_16x16x32_bf16 v[68:71], v[116:119], v[72:75], v[68:71]\n"
"v_mfma_f32_16x16x32_bf16 v[68:71], v[128:131], v[64:67], v[68:71]\n"
"v_mfma_f32_16x16x32_bf16 v[68:71], v[132:135], v[224:227], v[68:71]\n"
"buffer_load_dwordx4 v[168:171], v1, s[4:7], s96 offen offset:3072 sc1\n"
"v_mfma_f32_16x16x32_bf16 v[68:71], v[136:139], v[172:175], v[68:71]\n"
"ds_write_b128 v3, v[92:95] offset:3072\n"
"v_mfma_f32_16x16x32_bf16 v[68:71], v[96:99], v[60:63], v[68:71]\n"
"v_mfma_f32_16x16x32_bf16 v[92:95], v[112:115], v[60:63], 0\n"
"v_mfma_f32_16x16x32_bf16 v[92:95], v[164:167], v[172:175], v[92:95]\n"
"v_mfma_f32_16x16x32_bf16 v[92:95], v[160:163], v[224:227], v[92:95]\n"
"v_mfma_f32_16x16x32_bf16 v[92:95], v[156:159], v[64:67], v[92:95]\n"
"v_mfma_f32_16x16x32_bf16 v[92:95], v[124:127], v[72:75], v[92:95]\n"
"v_mfma_f32_16x16x32_bf16 v[92:95], v[148:151], v[176:179], v[92:95]\n"
"s_nop 1\n"
"ds_write_b128 v3, v[68:71] offset:4096\n"
"v_mfma_f32_16x16x32_bf16 v[92:95], v[144:147], v[48:51], v[92:95]\n"
"v_mfma_f32_16x16x32_bf16 v[92:95], v[120:123], v[40:43], v[92:95]\n"
"s_waitcnt vmcnt(25)\n"
"; @A G1 2.1\n"
"v_mfma_f32_16x16x32_bf16 v[40:43], a[0:3], v[140:143], 0\n"
"buffer_load_dwordx4 a[172:175], v0, s[4:7], s97 offen offset:3072 sc1\n"
"v_mfma_f32_16x16x32_bf16 v[40:43], a[4:7], v[32:35], v[40:43]\n"
"v_mov_b32_e32 v48, -1\n"
"v_mov_b32_e32 v49, -1\n"
"v_mfma_f32_16x16x32_bf16 v[40:43], a[8:11], v[204:207], v[40:43]\n"
"s_add_u32 s98, s55, 0x8000\n"
"buffer_load_dwordx4 v[60:63], v0, s[4:7], s98 offen sc1\n"
"v_mfma_f32_16x16x32_bf16 v[40:43], a[12:15], v[24:27], v[40:43]\n"
"s_waitcnt vmcnt(21)\n"
"v_mov_b32_e32 v50, -1\n"
"v_mov_b32_e32 v51, -1\n"
"v_mfma_f32_16x16x32_bf16 v[40:43], a[16:19], v[56:59], v[40:43]\n"
"s_add_u32 s96, s60, 0x8000\n"
"buffer_store_dwordx4 v[48:51], v0, s[4:7], s96 offen sc1\n"
"v_mfma_f32_16x16x32_bf16 v[40:43], a[20:23], v[180:183], v[40:43]\n"
"ds_write_b128 v3, v[92:95] offset:5120\n"
"v_mfma_f32_16x16x32_bf16 v[40:43], a[24:27], v[196:199], v[40:43]\n"
"v_mfma_f32_16x16x32_bf16 v[40:43], a[28:31], v[28:31], v[40:43]\n"
"buffer_load_dwordx4 v[64:67], v0, s[4:7], s98 offen offset:1024 sc1\n"
"v_mfma_f32_16x16x32_bf16 v[68:71], a[60:63], v[28:31], 0\n"
"s_add_u32 s96, s64, 0x6000\n"
"v_mfma_f32_16x16x32_bf16 v[68:71], a[56:59], v[196:199], v[68:71]\n"
"v_mfma_f32_16x16x32_bf16 v[68:71], a[52:55], v[180:183], v[68:71]\n"
"buffer_store_dwordx4 v[36:39], v7, s[4:7], s96 offen offset:1024 sc1\n"
"v_mfma_f32_16x16x32_bf16 v[68:71], a[48:51], v[56:59], v[68:71]\n"
"v_mfma_f32_16x16x32_bf16 v[68:71], a[44:47], v[24:27], v[68:71]\n"
"buffer_load_dwordx4 v[36:39], v0, s[4:7], s98 offen offset:2048 sc1\n"
"v_mfma_f32_16x16x32_bf16 v[68:71], a[40:43], v[204:207], v[68:71]\n"
"ds_write_b128 v3, v[40:43] offset:6144\n"
"v_mfma_f32_16x16x32_bf16 v[68:71], a[36:39], v[32:35], v[68:71]\n"
"v_mfma_f32_16x16x32_bf16 v[68:71], a[32:35], v[140:143], v[68:71]\n"
"v_mfma_f32_16x16x32_bf16 v[40:43], a[64:67], v[140:143], 0\n"
"v_mfma_f32_16x16x32_bf16 v[40:43], a[68:71], v[32:35], v[40:43]\n"
"buffer_load_dwordx4 v[72:75], v0, s[4:7], s98 offen offset:3072 sc1\n"
"v_mfma_f32_16x16x32_bf16 v[40:43], a[72:75], v[204:207], v[40:43]\n"
"v_mfma_f32_16x16x32_bf16 v[40:43], a[76:79], v[24:27], v[40:43]\n"
"v_mfma_f32_16x16x32_bf16 v[40:43], a[80:83], v[56:59], v[40:43]\n"
"buffer_load_dwordx4 a[176:179], v1, s[4:7], s97 offen sc1\n"
"v_mfma_f32_16x16x32_bf16 v[40:43], a[84:87], v[180:183], v[40:43]\n"
"ds_write_b128 v3, v[68:71] offset:7168\n"
"v_mfma_f32_16x16x32_bf16 v[40:43], a[88:91], v[196:199], v[40:43]\n"
"buffer_load_dwordx4 v[68:71], v1, s[4:7], s98 offen sc1\n"
"v_mfma_f32_16x16x32_bf16 v[40:43], a[92:95], v[28:31], v[40:43]\n"
"v_mfma_f32_16x16x32_bf16 v[92:95], a[124:127], v[28:31], 0\n"
"s_add_u32 s96, s61, 0x440000\n"
"buffer_store_dwordx4 v[48:51], v0, s[4:7], s96 offen sc1\n"
"v_mfma_f32_16x16x32_bf16 v[92:95], a[120:123], v[196:199], v[92:95]\n"
"v_mfma_f32_16x16x32_bf16 v[92:95], a[116:119], v[180:183], v[92:95]\n"
"v_mfma_f32_16x16x32_bf16 v[92:95], a[112:115], v[56:59], v[92:95]\n"
"buffer_load_dwordx4 v[172:175], v1, s[4:7], s98 offen offset:1024 sc1\n"
"v_mfma_f32_16x16x32_bf16 v[92:95], a[108:111], v[24:27], v[92:95]\n"
"v_mfma_f32_16x16x32_bf16 v[92:95], a[104:107], v[204:207], v[92:95]\n"
"ds_write_b128 v3, v[40:43] offset:8192\n"
"v_mfma_f32_16x16x32_bf16 v[92:95], a[100:103], v[32:35], v[92:95]\n"
"v_mfma_f32_16x16x32_bf16 v[92:95], a[96:99], v[140:143], v[92:95]\n"
"v_mfma_f32_16x16x32_bf16 v[40:43], v[100:103], v[140:143], 0\n"
"buffer_load_dwordx4 v[176:179], v1, s[4:7], s98 offen offset:2048 sc1\n"
"v_mfma_f32_16x16x32_bf16 v[40:43], v[104:107], v[32:35], v[40:43]\n"
"v_mfma_f32_16x16x32_bf16 v[40:43], v[108:111], v[204:207], v[40:43]\n"
"v_mfma_f32_16x16x32_bf16 v[40:43], v[116:119], v[24:27], v[40:43]\n"
"v_mfma_f32_16x16x32_bf16 v[40:43], v[128:131], v[56:59], v[40:43]\n"
"s_waitcnt vmcnt(14)\n"
"v_add_f32_e32 v255, v255, v253\n"
"v_add_f32_e32 v254, v254, v250\n"
"v_mfma_f32_16x16x32_bf16 v[40:43], v[132:135], v[180:183], v[40:43]\n"
"buffer_load_dwordx4 v[184:187], v1, s[4:7], s98 offen offset:3072 sc1\n"
"v_mfma_f32_16x16x32_bf16 v[40:43], v[136:139], v[196:199], v[40:43]\n"
"ds_write_b128 v3, v[92:95] offset:9216\n"
"v_mfma_f32_16x16x32_bf16 v[40:43], v[96:99], v[28:31], v[40:43]\n"
"v_add_f32_e32 v252, v252, v249\n"
"v_add_f32_e32 v251, v251, v248\n"
"v_mfma_f32_16x16x32_bf16 v[92:95], v[112:115], v[28:31], 0\n"
"buffer_load_dwordx4 a[180:183], v1, s[4:7], s97 offen offset:1024 sc1\n"
"v_mfma_f32_16x16x32_bf16 v[92:95], v[164:167], v[196:199], v[92:95]\n"
"v_add_f32_e32 v255, v255, v252\n"
"v_add_f32_e32 v254, v254, v251\n"
"v_mfma_f32_16x16x32_bf16 v[92:95], v[160:163], v[180:183], v[92:95]\n"
"v_add_f32_e32 v255, v255, v254\n"
"v_mov_b32_e32 v254, v255\n"
"v_mfma_f32_16x16x32_bf16 v[92:95], v[156:159], v[56:59], v[92:95]\n"
"s_nop 0\n"
"v_permlane32_swap_b32_e32 v255, v254\n"
"v_add_f32_e32 v255, v255, v254\n"
"v_mfma_f32_16x16x32_bf16 v[92:95], v[124:127], v[24:27], v[92:95]\n"
"v_mul_f32_e32 v255, 0x3a800000, v255\n"
"v_mov_b32_e32 v254, v255\n"
"v_mfma_f32_16x16x32_bf16 v[92:95], v[148:151], v[204:207], v[92:95]\n"
"ds_write_b128 v3, v[40:43] offset:10240\n"
"v_mfma_f32_16x16x32_bf16 v[92:95], v[144:147], v[32:35], v[92:95]\n"
"v_permlane16_swap_b32_e32 v255, v254\n"
"v_cvt_pk_bf16_f32 v253, -v255, 0\n"
"v_mfma_f32_16x16x32_bf16 v[92:95], v[120:123], v[140:143], v[92:95]\n"
"v_fma_f32 v254, -v255, v255, v254\n"
"v_lshlrev_b32_e32 v253, 16, v253\n"
"; @A G1 2.2\n"
"v_mfma_f32_16x16x32_bf16 v[24:27], a[0:3], v[76:79], 0\n"
"s_add_u32 s98, s61, 0x440000\n"
"buffer_store_dwordx4 v[48:51], v0, s[4:7], s98 offen offset:1024 sc1\n"
"v_max_f32_e32 v254, 0, v254\n"
"v_cvt_pk_bf16_f32 v254, v254, 0\n"
"v_mfma_f32_16x16x32_bf16 v[24:27], a[4:7], v[44:47], v[24:27]\n"
"v_lshlrev_b32_e32 v254, 16, v254\n"
"v_add_f32_e32 v254, 0x3727c5ac, v254\n"
"v_mfma_f32_16x16x32_bf16 v[24:27], a[8:11], v[84:87], v[24:27]\n"
"v_cvt_pk_bf16_f32 v254, v254, 0\n"
"v_lshlrev_b32_e32 v254, 16, v254\n"
"v_mfma_f32_16x16x32_bf16 v[24:27], a[12:15], v[88:91], v[24:27]\n"
"s_waitcnt vmcnt(15)\n"
"v_sqrt_f32_e32 v254, v254\n"
"v_mfma_f32_16x16x32_bf16 v[24:27], a[16:19], v[80:83], v[24:27]\n"
"s_mov_b32 s98, .Lmk_z4-.Lmk_g11\n"
"s_getpc_b64 s[84:85]\n"
".Lmk_g11:\n"
"s_add_u32 s84, s84, s98\n"
"s_addc_u32 s85, s85, 0\n"
"s_and_b32 s84, s84, -16\n"
"v_mfma_f32_16x16x32_bf16 v[24:27], a[20:23], v[52:55], v[24:27]\n"
"ds_write_b128 v3, v[92:95] offset:11264\n"
"v_mfma_f32_16x16x32_bf16 v[24:27], a[24:27], v[152:155], v[24:27]\n"
"s_and_b32 s85, s85, 0xffff\n"
"s_sub_u32 s98, .Lmk_end-.Lmk_g11, s98\n"
"s_min_u32 s86, s98, 0x4000\n"
"s_mov_b32 s87, 0x20000\n"
"v_mfma_f32_16x16x32_bf16 v[24:27], a[28:31], v[168:171], v[24:27]\n"
"s_mov_b32 m0, 0x27800\n"
"s_nop 0\n"
"buffer_load_dwordx4 v0, s[84:87], s99 offen lds\n"
"v_mfma_f32_16x16x32_bf16 v[28:31], a[60:63], v[168:171], 0\n"
"v_cvt_pk_bf16_f32 v254, v254, 0\n"
"v_lshlrev_b32_e32 v254, 16, v254\n"
"v_mfma_f32_16x16x32_bf16 v[28:31], a[56:59], v[152:155], v[28:31]\n"
"v_rcp_f32_e32 v255, v254\n"
"v_mfma_f32_16x16x32_bf16 v[28:31], a[52:55], v[52:55], v[28:31]\n"
"s_add_u32 s98, s52, 0\n"
"buffer_load_dwordx4 v[32:35], v0, s[4:7], s98 offen sc1\n"
"v_mfma_f32_16x16x32_bf16 v[28:31], a[48:51], v[80:83], v[28:31]\n"
"s_waitcnt lgkmcnt(0)\n"
"s_barrier\n"
"v_mfma_f32_16x16x32_bf16 v[28:31], a[44:47], v[88:91], v[28:31]\n"
"buffer_load_dwordx4 a[184:187], v1, s[4:7], s97 offen offset:2048 sc1\n"
"v_mfma_f32_16x16x32_bf16 v[28:31], a[40:43], v[84:87], v[28:31]\n"
"ds_write_b128 v3, v[24:27] offset:12288\n"
"v_mfma_f32_16x16x32_bf16 v[28:31], a[36:39], v[44:47], v[28:31]\n"
"ds_read_b128 v[24:27], v4\n"
"v_mul_f32_e32 v254, 0x3e8293ee, v255\n"
"v_mfma_f32_16x16x32_bf16 v[28:31], a[32:35], v[76:79], v[28:31]\n"
"buffer_load_dwordx4 v[40:43], v0, s[4:7], s98 offen offset:1024 sc1\n"
"v_mfma_f32_16x16x32_bf16 v[56:59], a[64:67], v[76:79], 0\n"
"ds_read_b128 v[92:95], v9 offset:128\n"
"ds_read_b128 v[140:143], v5\n"
"v_mfma_f32_16x16x32_bf16 v[56:59], a[68:71], v[44:47], v[56:59]\n"
"v_mfma_f32_16x16x32_bf16 v[56:59], a[72:75], v[84:87], v[56:59]\n"
"v_mfma_f32_16x16x32_bf16 v[56:59], a[76:79], v[88:91], v[56:59]\n"
"ds_read_b128 v[180:183], v4 offset:1024\n"
"ds_read_b128 v[188:191], v9 offset:192\n"
"v_mfma_f32_16x16x32_bf16 v[56:59], a[80:83], v[80:83], v[56:59]\n"
"buffer_load_dwordx4 v[192:195], v0, s[4:7], s98 offen offset:2048 sc1\n"
"v_mfma_f32_16x16x32_bf16 v[56:59], a[84:87], v[52:55], v[56:59]\n"
"ds_write_b128 v3, v[28:31] offset:13312\n"
"v_mfma_f32_16x16x32_bf16 v[56:59], a[88:91], v[152:155], v[56:59]\n"
"ds_read_b128 v[28:31], v5 offset:1024\n"
"s_waitcnt lgkmcnt(4)\n"
"v_mfma_f32_16x16x32_bf16 v[56:59], a[92:95], v[168:171], v[56:59]\n"
"v_fmac_f32_e32 v24, v253, v92\n"
"v_fmac_f32_e32 v25, v253, v93\n"
"v_mfma_f32_16x16x32_bf16 v[196:199], a[124:127], v[168:171], 0\n"
"s_add_u32 s96, s63, 0x44000\n"
"buffer_store_dword v48, v14, s[4:7], s96 offen sc1\n"
"v_fmac_f32_e32 v26, v253, v94\n"
"v_fmac_f32_e32 v27, v253, v95\n"
"v_mfma_f32_16x16x32_bf16 v[196:199], a[120:123], v[152:155], v[196:199]\n"
"buffer_load_dwordx4 v[48:51], v0, s[4:7], s98 offen offset:3072 sc1\n"
"v_mfma_f32_16x16x32_bf16 v[196:199], a[116:119], v[52:55], v[196:199]\n"
"ds_read_b128 v[92:95], v4 offset:2048\n"
"ds_read_b128 v[200:203], v9 offset:256\n"
"v_mfma_f32_16x16x32_bf16 v[196:199], a[112:115], v[80:83], v[196:199]\n"
"ds_read_b128 v[204:207], v5 offset:2048\n"
"v_add_f32_e32 v24, v24, v140\n"
"v_mfma_f32_16x16x32_bf16 v[196:199], a[108:111], v[88:91], v[196:199]\n"
"v_add_f32_e32 v25, v25, v141\n"
"v_add_f32_e32 v26, v26, v142\n"
"v_mfma_f32_16x16x32_bf16 v[196:199], a[104:107], v[84:87], v[196:199]\n"
"ds_write_b128 v3, v[56:59] offset:14336\n"
"v_mfma_f32_16x16x32_bf16 v[196:199], a[100:103], v[44:47], v[196:199]\n"
"buffer_load_dwordx4 v[56:59], v1, s[4:7], s98 offen sc1\n"
"v_mfma_f32_16x16x32_bf16 v[196:199], a[96:99], v[76:79], v[196:199]\n"
"s_waitcnt lgkmcnt(4)\n"
"v_add_f32_e32 v27, v27, v143\n"
"v_fmac_f32_e32 v180, v253, v188\n"
"v_mfma_f32_16x16x32_bf16 v[140:143], v[100:103], v[76:79], 0\n"
"buffer_load_dwordx4 a[188:191], v1, s[4:7], s97 offen offset:3072 sc1\n"
"v_mfma_f32_16x16x32_bf16 v[140:143], v[104:107], v[44:47], v[140:143]\n"
"ds_read_b128 v[208:211], v4 offset:3072\n"
"ds_read_b128 v[212:215], v9 offset:320\n"
"v_mfma_f32_16x16x32_bf16 v[140:143], v[108:111], v[84:87], v[140:143]\n"
"s_waitcnt vmcnt(19)\n"
"s_add_u32 s97, s68, 0x1000\n"
"ds_read_b128 v[216:219], v5 offset:3072\n"
"v_mfma_f32_16x16x32_bf16 v[140:143], v[116:119], v[88:91], v[140:143]\n"
"buffer_load_dwordx4 v[220:223], v1, s[4:7], s98 offen offset:1024 sc1\n"
"v_mfma_f32_16x16x32_bf16 v[140:143], v[128:131], v[80:83], v[140:143]\n"
"buffer_store_dwordx2 v[18:19], v11, s[4:7], s97 offen offset:64 sc1\n"
"v_mfma_f32_16x16x32_bf16 v[140:143], v[132:135], v[52:55], v[140:143]\n"
"ds_write_b128 v3, v[196:199] offset:15360\n"
"v_mfma_f32_16x16x32_bf16 v[140:143], v[136:139], v[152:155], v[140:143]\n"
"v_fmac_f32_e32 v181, v253, v189\n"
"v_fmac_f32_e32 v182, v253, v190\n"
"v_mfma_f32_16x16x32_bf16 v[140:143], v[96:99], v[168:171], v[140:143]\n"
"v_fmac_f32_e32 v183, v253, v191\n"
"v_add_f32_e32 v180, v180, v28\n"
"v_mfma_f32_16x16x32_bf16 v[188:191], v[112:115], v[168:171], 0\n"
"buffer_load_dwordx4 v[168:171], v1, s[4:7], s98 offen offset:2048 sc1\n"
"v_mfma_f32_16x16x32_bf16 v[188:191], v[164:167], v[152:155], v[188:191]\n"
"ds_read_b128 v[152:155], v4 offset:4096\n"
"ds_read_b128 v[196:199], v9\n"
"v_mfma_f32_16x16x32_bf16 v[188:191], v[160:163], v[52:55], v[188:191]\n"
"ds_read_b128 v[52:55], v5 offset:4096\n"
"v_add_f32_e32 v181, v181, v29\n"
"v_mfma_f32_16x16x32_bf16 v[188:191], v[156:159], v[80:83], v[188:191]\n"
"s_waitcnt lgkmcnt(8)\n"
"v_add_f32_e32 v182, v182, v30\n"
"v_add_f32_e32 v183, v183, v31\n"
"v_mfma_f32_16x16x32_bf16 v[188:191], v[124:127], v[88:91], v[188:191]\n"
"v_fmac_f32_e32 v92, v253, v200\n"
"v_fmac_f32_e32 v93, v253, v201\n"
"v_mfma_f32_16x16x32_bf16 v[188:191], v[148:151], v[84:87], v[188:191]\n"
"buffer_load_dwordx4 v[28:31], v1, s[4:7], s98 offen offset:3072 sc1\n"
"v_mfma_f32_16x16x32_bf16 v[188:191], v[144:147], v[44:47], v[188:191]\n"
"ds_write_b128 v3, v[140:143] offset:16384\n"
"v_mfma_f32_16x16x32_bf16 v[188:191], v[120:123], v[76:79], v[188:191]\n"
"s_waitcnt vmcnt(21)\n"
"ds_read_b128 v[44:47], v4 offset:5120\n"
"ds_read_b128 v[76:79], v9 offset:64\n"
"; @A G1 2.3\n"
"v_mfma_f32_16x16x32_bf16 v[24:27], a[0:3], v[60:63], v[24:27]\n"
"ds_read_b128 v[80:83], v5 offset:5120\n"
"v_fmac_f32_e32 v94, v253, v202\n"
"v_mfma_f32_16x16x32_bf16 v[24:27], a[4:7], v[64:67], v[24:27]\n"
"v_fmac_f32_e32 v95, v253, v203\n"
"v_add_f32_e32 v92, v92, v204\n"
"v_mfma_f32_16x16x32_bf16 v[24:27], a[8:11], v[36:39], v[24:27]\n"
"s_add_u32 s98, s53, 0\n"
"buffer_load_dwordx4 v[84:87], v0, s[4:7], s98 offen sc1\n"
"v_mfma_f32_16x16x32_bf16 v[24:27], a[12:15], v[72:75], v[24:27]\n"
"s_waitcnt vmcnt(16)\n"
"s_add_u32 s97, s70, 0x4000\n"
"v_add_f32_e32 v93, v93, v205\n"
"v_mfma_f32_16x16x32_bf16 v[24:27], a[16:19], v[68:71], v[24:27]\n"
"buffer_load_dwordx4 a[192:195], v0, s[4:7], s97 offen sc1\n"
"v_mfma_f32_16x16x32_bf16 v[24:27], a[20:23], v[172:175], v[24:27]\n"
"ds_write_b128 v3, v[188:191] offset:17408\n"
"v_mfma_f32_16x16x32_bf16 v[24:27], a[24:27], v[176:179], v[24:27]\n"
"s_waitcnt lgkmcnt(9)\n"
"v_add_f32_e32 v94, v94, v206\n"
"v_add_f32_e32 v95, v95, v207\n"
"v_mfma_f32_16x16x32_bf16 v[24:27], a[28:31], v[184:187], v[24:27]\n"
"buffer_load_dwordx4 v[88:91], v0, s[4:7], s98 offen offset:1024 sc1\n"
"v_mfma_f32_16x16x32_bf16 v[180:183], a[60:63], v[184:187], v[180:183]\n"
"v_fmac_f32_e32 v208, v253, v212\n"
"v_fmac_f32_e32 v209, v253, v213\n"
"v_mfma_f32_16x16x32_bf16 v[180:183], a[56:59], v[176:179], v[180:183]\n"
"v_fmac_f32_e32 v210, v253, v214\n"
"v_fmac_f32_e32 v211, v253, v215\n"
"v_mfma_f32_16x16x32_bf16 v[180:183], a[52:55], v[172:175], v[180:183]\n"
"s_waitcnt lgkmcnt(0)\n"
"s_barrier\n"
"v_mfma_f32_16x16x32_bf16 v[180:183], a[48:51], v[68:71], v[180:183]\n"
"ds_read_b128 v[140:143], v6\n"
"ds_read_b128 v[188:191], v6 offset:1024\n"
"v_mfma_f32_16x16x32_bf16 v[180:183], a[44:47], v[72:75], v[180:183]\n"
"buffer_load_dwordx4 v[200:203], v0, s[4:7], s98 offen offset:2048 sc1\n"
"v_mfma_f32_16x16x32_bf16 v[180:183], a[40:43], v[36:39], v[180:183]\n"
"v_add_f32_e32 v208, v208, v216\n"
"v_add_f32_e32 v209, v209, v217\n"
"v_mfma_f32_16x16x32_bf16 v[180:183], a[36:39], v[64:67], v[180:183]\n"
"v_add_f32_e32 v210, v210, v218\n"
"v_add_f32_e32 v211, v211, v219\n"
"v_mfma_f32_16x16x32_bf16 v[180:183], a[32:35], v[60:63], v[180:183]\n"
"v_fmac_f32_e32 v152, v253, v196\n"
"v_fmac_f32_e32 v153, v253, v197\n"
"v_mfma_f32_16x16x32_bf16 v[92:95], a[64:67], v[60:63], v[92:95]\n"
"v_fmac_f32_e32 v154, v253, v198\n"
"v_fmac_f32_e32 v155, v253, v199\n"
"v_mfma_f32_16x16x32_bf16 v[92:95], a[68:71], v[64:67], v[92:95]\n"
"buffer_load_dwordx4 v[196:199], v0, s[4:7], s98 offen offset:3072 sc1\n"
"v_mfma_f32_16x16x32_bf16 v[92:95], a[72:75], v[36:39], v[92:95]\n"
"v_add_f32_e32 v152, v152, v52\n"
"v_add_f32_e32 v153, v153, v53\n"
"v_mfma_f32_16x16x32_bf16 v[92:95], a[76:79], v[72:75], v[92:95]\n"
"v_add_f32_e32 v154, v154, v54\n"
"v_add_f32_e32 v155, v155, v55\n"
"v_mfma_f32_16x16x32_bf16 v[92:95], a[80:83], v[68:71], v[92:95]\n"
"s_waitcnt lgkmcnt(0)\n"
"v_fmac_f32_e32 v44, v253, v76\n"
"v_fmac_f32_e32 v45, v253, v77\n"
"v_mfma_f32_16x16x32_bf16 v[92:95], a[84:87], v[172:175], v[92:95]\n"
"v_fmac_f32_e32 v46, v253, v78\n"
"v_fmac_f32_e32 v47, v253, v79\n"
"v_mfma_f32_16x16x32_bf16 v[92:95], a[88:91], v[176:179], v[92:95]\n"
"buffer_load_dwordx4 v[52:55], v1, s[4:7], s98 offen sc1\n"
"v_mfma_f32_16x16x32_bf16 v[92:95], a[92:95], v[184:187], v[92:95]\n"
"ds_read_b128 v[76:79], v6 offset:2048\n"
"ds_read_b128 v[204:207], v6 offset:3072\n"
"v_mfma_f32_16x16x32_bf16 v[208:211], a[124:127], v[184:187], v[208:211]\n"
"buffer_load_dwordx4 a[196:199], v0, s[4:7], s97 offen offset:1024 sc1\n"
"v_mfma_f32_16x16x32_bf16 v[208:211], a[120:123], v[176:179], v[208:211]\n"
"v_add_f32_e32 v44, v44, v80\n"
"v_add_f32_e32 v45, v45, v81\n"
"v_mfma_f32_16x16x32_bf16 v[208:211], a[116:119], v[172:175], v[208:211]\n"
"s_add_u32 s96, s68, 0x1000\n"
"buffer_store_dwordx2 v[18:19], v11, s[4:7], s96 offen offset:96 sc1\n"
"v_add_f32_e32 v46, v46, v82\n"
"v_add_f32_e32 v47, v47, v83\n"
"v_mfma_f32_16x16x32_bf16 v[208:211], a[112:115], v[68:71], v[208:211]\n"
"buffer_load_dwordx4 v[80:83], v1, s[4:7], s98 offen offset:1024 sc1\n"
"v_mfma_f32_16x16x32_bf16 v[208:211], a[108:111], v[72:75], v[208:211]\n"
"v_add_f32_e32 v24, v24, v140\n"
"v_add_f32_e32 v25, v25, v141\n"
"v_mfma_f32_16x16x32_bf16 v[208:211], a[104:107], v[36:39], v[208:211]\n"
"v_add_f32_e32 v26, v26, v142\n"
"v_add_f32_e32 v27, v27, v143\n"
"v_mfma_f32_16x16x32_bf16 v[208:211], a[100:103], v[64:67], v[208:211]\n"
"v_add_f32_e32 v180, v180, v188\n"
"v_add_f32_e32 v181, v181, v189\n"
"v_mfma_f32_16x16x32_bf16 v[208:211], a[96:99], v[60:63], v[208:211]\n"
"v_add_f32_e32 v182, v182, v190\n"
"v_add_f32_e32 v183, v183, v191\n"
"v_mfma_f32_16x16x32_bf16 v[152:155], v[100:103], v[60:63], v[152:155]\n"
"buffer_load_dwordx4 v[140:143], v1, s[4:7], s98 offen offset:2048 sc1\n"
"v_mfma_f32_16x16x32_bf16 v[152:155], v[104:107], v[64:67], v[152:155]\n"
"v_mul_f32_e32 v24, v24, v255\n"
"v_mul_f32_e32 v25, v25, v255\n"
"v_mfma_f32_16x16x32_bf16 v[152:155], v[108:111], v[36:39], v[152:155]\n"
"v_mul_f32_e32 v26, v26, v255\n"
"v_mul_f32_e32 v27, v27, v255\n"
"v_mfma_f32_16x16x32_bf16 v[152:155], v[116:119], v[72:75], v[152:155]\n"
"v_mul_f32_e32 v180, v180, v255\n"
"v_mul_f32_e32 v181, v181, v255\n"
"v_mfma_f32_16x16x32_bf16 v[152:155], v[128:131], v[68:71], v[152:155]\n"
"s_waitcnt lgkmcnt(0)\n"
"v_mul_f32_e32 v182, v182, v255\n"
"v_mul_f32_e32 v183, v183, v255\n"
"v_mfma_f32_16x16x32_bf16 v[152:155], v[132:135], v[172:175], v[152:155]\n"
"buffer_load_dwordx4 v[188:191], v1, s[4:7], s98 offen offset:3072 sc1\n"
"v_mfma_f32_16x16x32_bf16 v[152:155], v[136:139], v[176:179], v[152:155]\n"
"ds_read_b128 v[212:215], v6 offset:4096\n"
"ds_read_b128 v[216:219], v6 offset:5120\n"
"v_mfma_f32_16x16x32_bf16 v[152:155], v[96:99], v[184:187], v[152:155]\n"
"v_cvt_pk_bf16_f32 v224, v24, v25\n"
"v_cvt_pk_bf16_f32 v225, v26, v27\n"
"v_mfma_f32_16x16x32_bf16 v[44:47], v[112:115], v[184:187], v[44:47]\n"
"v_cvt_pk_bf16_f32 v226, v180, v181\n"
"v_cvt_pk_bf16_f32 v227, v182, v183\n"
"v_mfma_f32_16x16x32_bf16 v[44:47], v[164:167], v[176:179], v[44:47]\n"
"ds_write_b128 v8, v[224:227] offset:17408\n"
"v_mfma_f32_16x16x32_bf16 v[44:47], v[160:163], v[172:175], v[44:47]\n"
"v_add_f32_e32 v92, v92, v76\n"
"v_add_f32_e32 v93, v93, v77\n"
"v_mfma_f32_16x16x32_bf16 v[44:47], v[156:159], v[68:71], v[44:47]\n"
"v_add_f32_e32 v94, v94, v78\n"
"v_add_f32_e32 v95, v95, v79\n"
"v_mfma_f32_16x16x32_bf16 v[44:47], v[124:127], v[72:75], v[44:47]\n"
"v_add_f32_e32 v208, v208, v204\n"
"v_add_f32_e32 v209, v209, v205\n"
"v_mfma_f32_16x16x32_bf16 v[44:47], v[148:151], v[36:39], v[44:47]\n"
"v_add_f32_e32 v210, v210, v206\n"
"v_add_f32_e32 v211, v211, v207\n"
"v_mfma_f32_16x16x32_bf16 v[44:47], v[144:147], v[64:67], v[44:47]\n"
"v_mul_f32_e32 v92, v92, v255\n"
"v_mul_f32_e32 v93, v93, v255\n"
"v_mfma_f32_16x16x32_bf16 v[44:47], v[120:123], v[60:63], v[44:47]\n"
"v_mul_f32_e32 v94, v94, v255\n"
"v_mul_f32_e32 v95, v95, v255\n"
"v_mul_f32_e32 v208, v208, v255\n"
"v_mul_f32_e32 v209, v209, v255\n"
"v_mul_f32_e32 v210, v210, v255\n"
"v_mul_f32_e32 v211, v211, v255\n"
"v_cvt_pk_bf16_f32 v24, v92, v93\n"
"v_cvt_pk_bf16_f32 v25, v94, v95\n"
"v_cvt_pk_bf16_f32 v26, v208, v209\n"
"v_cvt_pk_bf16_f32 v27, v210, v211\n"
"ds_write_b128 v7, v[24:27] offset:50176\n"
".Lmk_z3:\n"
"s_waitcnt vmcnt(17)\n"
"; @A G1 3.0\n"
"v_mfma_f32_16x16x32_bf16 v[36:39], a[0:3], v[32:35], 0\n"
"v_mfma_f32_16x16x32_bf16 v[36:39], a[4:7], v[40:43], v[36:39]\n"
"s_add_u32 s98, s62, 0\n"
"v_mfma_f32_16x16x32_bf16 v[36:39], a[8:11], v[192:195], v[36:39]\n"
"s_add_u32 s96, s54, 0\n"
"buffer_load_dwordx4 v[60:63], v0, s[4:7], s96 offen sc1\n"
"v_mfma_f32_16x16x32_bf16 v[36:39], a[12:15], v[48:51], v[36:39]\n"
"s_waitcnt vmcnt(12)\n"
"v_mfma_f32_16x16x32_bf16 v[36:39], a[16:19], v[56:59], v[36:39]\n"
"buffer_load_dword v255, v10, s[4:7], s98 offen sc1\n"
"v_mfma_f32_16x16x32_bf16 v[36:39], a[20:23], v[220:223], v[36:39]\n"
"s_waitcnt lgkmcnt(2)\n"
"v_add_f32_e32 v152, v152, v212\n"
"v_add_f32_e32 v153, v153, v213\n"
"v_mfma_f32_16x16x32_bf16 v[36:39], a[24:27], v[168:171], v[36:39]\n"
"v_add_f32_e32 v154, v154, v214\n"
"v_add_f32_e32 v155, v155, v215\n"
"v_mfma_f32_16x16x32_bf16 v[36:39], a[28:31], v[28:31], v[36:39]\n"
"buffer_load_dwordx4 v[64:67], v0, s[4:7], s96 offen offset:1024 sc1\n"
"v_mfma_f32_16x16x32_bf16 v[68:71], a[60:63], v[28:31], 0\n"
"buffer_load_dwordx4 a[200:203], v0, s[4:7], s97 offen offset:2048 sc1\n"
"v_mfma_f32_16x16x32_bf16 v[68:71], a[56:59], v[168:171], v[68:71]\n"
"buffer_load_dword v253, v10, s[4:7], s98 offen offset:256 sc1\n"
"v_mfma_f32_16x16x32_bf16 v[68:71], a[52:55], v[220:223], v[68:71]\n"
"v_add_f32_e32 v44, v44, v216\n"
"v_add_f32_e32 v45, v45, v217\n"
"v_mfma_f32_16x16x32_bf16 v[68:71], a[48:51], v[56:59], v[68:71]\n"
"v_add_f32_e32 v46, v46, v218\n"
"v_add_f32_e32 v47, v47, v219\n"
"v_mfma_f32_16x16x32_bf16 v[68:71], a[44:47], v[48:51], v[68:71]\n"
"buffer_load_dwordx4 v[72:75], v0, s[4:7], s96 offen offset:2048 sc1\n"
"v_mfma_f32_16x16x32_bf16 v[68:71], a[40:43], v[192:195], v[68:71]\n"
"ds_write_b128 v3, v[36:39]\n"
"v_mfma_f32_16x16x32_bf16 v[68:71], a[36:39], v[40:43], v[68:71]\n"
"buffer_load_dword v252, v10, s[4:7], s98 offen offset:512 sc1\n"
"v_mfma_f32_16x16x32_bf16 v[68:71], a[32:35], v[32:35], v[68:71]\n"
"v_mul_f32_e32 v152, v152, v254\n"
"v_mul_f32_e32 v153, v153, v254\n"
"v_mfma_f32_16x16x32_bf16 v[36:39], a[64:67], v[32:35], 0\n"
"v_mul_f32_e32 v154, v154, v254\n"
"v_mul_f32_e32 v155, v155, v254\n"
"v_mfma_f32_16x16x32_bf16 v[36:39], a[68:71], v[40:43], v[36:39]\n"
"buffer_load_dwordx4 v[76:79], v0, s[4:7], s96 offen offset:3072 sc1\n"
"v_mfma_f32_16x16x32_bf16 v[36:39], a[72:75], v[192:195], v[36:39]\n"
"buffer_load_dword v251, v10, s[4:7], s98 offen offset:768 sc1\n"
"v_mfma_f32_16x16x32_bf16 v[36:39], a[76:79], v[48:51], v[36:39]\n"
"v_mul_f32_e32 v44, v44, v254\n"
"v_mul_f32_e32 v45, v45, v254\n"
"v_mfma_f32_16x16x32_bf16 v[36:39], a[80:83], v[56:59], v[36:39]\n"
"v_mul_f32_e32 v46, v46, v254\n"
"v_mul_f32_e32 v47, v47, v254\n"
"v_mfma_f32_16x16x32_bf16 v[36:39], a[84:87], v[220:223], v[36:39]\n"
"ds_write_b128 v3, v[68:71] offset:1024\n"
"v_mfma_f32_16x16x32_bf16 v[36:39], a[88:91], v[168:171], v[36:39]\n"
"buffer_load_dwordx4 v[68:71], v1, s[4:7], s96 offen sc1\n"
"v_mfma_f32_16x16x32_bf16 v[36:39], a[92:95], v[28:31], v[36:39]\n"
"buffer_load_dword v254, v10, s[4:7], s98 offen offset:1024 sc1\n"
"v_mfma_f32_16x16x32_bf16 v[92:95], a[124:127], v[28:31], 0\n"
"v_cvt_pk_bf16_f32 v172, v152, v153\n"
"v_cvt_pk_bf16_f32 v173, v154, v155\n"
"v_mfma_f32_16x16x32_bf16 v[92:95], a[120:123], v[168:171], v[92:95]\n"
"v_cvt_pk_bf16_f32 v174, v44, v45\n"
"v_cvt_pk_bf16_f32 v175, v46, v47\n"
"v_mfma_f32_16x16x32_bf16 v[92:95], a[116:119], v[220:223], v[92:95]\n"
"buffer_load_dwordx4 a[204:207], v0, s[4:7], s97 offen offset:3072 sc1\n"
"v_mfma_f32_16x16x32_bf16 v[92:95], a[112:115], v[56:59], v[92:95]\n"
"buffer_load_dwordx4 v[44:47], v1, s[4:7], s96 offen offset:1024 sc1\n"
"v_mfma_f32_16x16x32_bf16 v[92:95], a[108:111], v[48:51], v[92:95]\n"
"buffer_load_dword v250, v10, s[4:7], s98 offen offset:1280 sc1\n"
"v_mfma_f32_16x16x32_bf16 v[92:95], a[104:107], v[192:195], v[92:95]\n"
"ds_write_b128 v3, v[36:39] offset:2048\n"
"v_mfma_f32_16x16x32_bf16 v[92:95], a[100:103], v[40:43], v[92:95]\n"
"ds_write_b128 v2, v[172:175] offset:17408\n"
"v_mfma_f32_16x16x32_bf16 v[92:95], a[96:99], v[32:35], v[92:95]\n"
"buffer_load_dword v249, v10, s[4:7], s98 offen offset:1536 sc1\n"
"v_mfma_f32_16x16x32_bf16 v[36:39], v[100:103], v[32:35], 0\n"
"buffer_load_dwordx4 v[152:155], v1, s[4:7], s96 offen offset:2048 sc1\n"
"v_mfma_f32_16x16x32_bf16 v[36:39], v[104:107], v[40:43], v[36:39]\n"
"buffer_store_dwordx4 v[224:227], v8, s[4:7], s64 offen offset:1024 sc1\n"
"v_mfma_f32_16x16x32_bf16 v[36:39], v[108:111], v[192:195], v[36:39]\n"
"buffer_load_dword v248, v10, s[4:7], s98 offen offset:1792 sc1\n"
"v_mfma_f32_16x16x32_bf16 v[36:39], v[116:119], v[48:51], v[36:39]\n"
"v_mfma_f32_16x16x32_bf16 v[36:39], v[128:131], v[56:59], v[36:39]\n"
"v_mfma_f32_16x16x32_bf16 v[36:39], v[132:135], v[220:223], v[36:39]\n"
"buffer_load_dwordx4 v[172:175], v1, s[4:7], s96 offen offset:3072 sc1\n"
"v_mfma_f32_16x16x32_bf16 v[36:39], v[136:139], v[168:171], v[36:39]\n"
"ds_write_b128 v3, v[92:95] offset:3072\n"
"v_mfma_f32_16x16x32_bf16 v[36:39], v[96:99], v[28:31], v[36:39]\n"
"v_mfma_f32_16x16x32_bf16 v[92:95], v[112:115], v[28:31], 0\n"
"v_mfma_f32_16x16x32_bf16 v[92:95], v[164:167], v[168:171], v[92:95]\n"
"v_mfma_f32_16x16x32_bf16 v[92:95], v[160:163], v[220:223], v[92:95]\n"
"v_mfma_f32_16x16x32_bf16 v[92:95], v[156:159], v[56:59], v[92:95]\n"
"v_mfma_f32_16x16x32_bf16 v[92:95], v[124:127], v[48:51], v[92:95]\n"
"buffer_load_dwordx4 a[208:211], v1, s[4:7], s97 offen sc1\n"
"v_mfma_f32_16x16x32_bf16 v[92:95], v[148:151], v[192:195], v[92:95]\n"
"s_nop 0\n"
"ds_write_b128 v3, v[36:39] offset:4096\n"
"v_mfma_f32_16x16x32_bf16 v[92:95], v[144:147], v[40:43], v[92:95]\n"
"v_mfma_f32_16x16x32_bf16 v[92:95], v[120:123], v[32:35], v[92:95]\n"
"s_waitcnt vmcnt(26)\n"
"; @A G1 3.1\n"
"v_mfma_f32_16x16x32_bf16 v[28:31], a[0:3], v[84:87], 0\n"
"v_mfma_f32_16x16x32_bf16 v[28:31], a[4:7], v[88:91], v[28:31]\n"
"v_mfma_f32_16x16x32_bf16 v[28:31], a[8:11], v[200:203], v[28:31]\n"
"s_add_u32 s98, s55, 0\n"
"buffer_load_dwordx4 v[32:35], v0, s[4:7], s98 offen sc1\n"
"v_mfma_f32_16x16x32_bf16 v[28:31], a[12:15], v[196:199], v[28:31]\n"
"s_waitcnt vmcnt(21)\n"
"v_mfma_f32_16x16x32_bf16 v[28:31], a[16:19], v[52:55], v[28:31]\n"
"v_mfma_f32_16x16x32_bf16 v[28:31], a[20:23], v[80:83], v[28:31]\n"
"ds_write_b128 v3, v[92:95] offset:5120\n"
"v_mfma_f32_16x16x32_bf16 v[28:31], a[24:27], v[140:143], v[28:31]\n"
"v_mfma_f32_16x16x32_bf16 v[28:31], a[28:31], v[188:191], v[28:31]\n"
"buffer_load_dwordx4 v[36:39], v0, s[4:7], s98 offen offset:1024 sc1\n"
"s_add_u32 s96, s64, 0x4000\n"
"v_mfma_f32_16x16x32_bf16 v[40:43], a[60:63], v[188:191], 0\n"
"v_mfma_f32_16x16x32_bf16 v[40:43], a[56:59], v[140:143], v[40:43]\n"
"buffer_store_dwordx4 v[24:27], v7, s[4:7], s96 offen offset:1024 sc1\n"
"v_mfma_f32_16x16x32_bf16 v[40:43], a[52:55], v[80:83], v[40:43]\n"
"v_mfma_f32_16x16x32_bf16 v[40:43], a[48:51], v[52:55], v[40:43]\n"
"v_mfma_f32_16x16x32_bf16 v[40:43], a[44:47], v[196:199], v[40:43]\n"
"buffer_load_dwordx4 v[24:27], v0, s[4:7], s98 offen offset:2048 sc1\n"
"v_mfma_f32_16x16x32_bf16 v[40:43], a[40:43], v[200:203], v[40:43]\n"
"ds_write_b128 v3, v[28:31] offset:6144\n"
"v_mfma_f32_16x16x32_bf16 v[40:43], a[36:39], v[88:91], v[40:43]\n"
"buffer_load_dwordx4 a[212:215], v1, s[4:7], s97 offen offset:1024 sc1\n"
"v_mfma_f32_16x16x32_bf16 v[40:43], a[32:35], v[84:87], v[40:43]\n"
"s_mov_b32 s20, 0xff0000\n"
"s_mov_b32 s21, 0xffffffff\n"
"s_mov_b32 s22, 0x1ff0001\n"
"v_mfma_f32_16x16x32_bf16 v[28:31], a[64:67], v[84:87], 0\n"
"s_mov_b32 s23, 0xffffffff\n"
"s_mov_b32 s24, 0x3ff0003\n"
"s_mov_b32 s25, 0xffffffff\n"
"v_mfma_f32_16x16x32_bf16 v[28:31], a[68:71], v[88:91], v[28:31]\n"
"buffer_load_dwordx4 v[48:51], v0, s[4:7], s98 offen offset:3072 sc1\n"
"v_mfma_f32_16x16x32_bf16 v[28:31], a[72:75], v[200:203], v[28:31]\n"
"s_mov_b32 s26, 0x7ff0007\n"
"s_mov_b32 s27, 0xffffffff\n"
"s_mov_b32 s28, 0xfff000f\n"
"v_mfma_f32_16x16x32_bf16 v[28:31], a[76:79], v[196:199], v[28:31]\n"
"s_mov_b32 s29, 0xffffffff\n"
"s_mov_b32 s30, 0x1fff001f\n"
"s_mov_b32 s31, 0xffffffff\n"
"v_mfma_f32_16x16x32_bf16 v[28:31], a[80:83], v[52:55], v[28:31]\n"
"s_mov_b32 s32, 0x3fff003f\n"
"s_mov_b32 s33, 0xffffffff\n"
"s_mov_b32 s34, 0x7fff007f\n"
"v_mfma_f32_16x16x32_bf16 v[28:31], a[84:87], v[80:83], v[28:31]\n"
"ds_write_b128 v3, v[40:43] offset:7168\n"
"v_mfma_f32_16x16x32_bf16 v[28:31], a[88:91], v[140:143], v[28:31]\n"
"buffer_load_dwordx4 v[40:43], v1, s[4:7], s98 offen sc1\n"
"v_mfma_f32_16x16x32_bf16 v[28:31], a[92:95], v[188:191], v[28:31]\n"
"s_mov_b32 s35, 0xffffffff\n"
"s_mov_b32 s36, 0\n"
"s_mov_b32 s37, 0xff0000\n"
"v_mfma_f32_16x16x32_bf16 v[56:59], a[124:127], v[188:191], 0\n"
"s_mov_b32 s38, 0\n"
"s_mov_b32 s39, 0x1ff0001\n"
"s_mov_b32 s40, 0\n"
"v_mfma_f32_16x16x32_bf16 v[56:59], a[120:123], v[140:143], v[56:59]\n"
"s_mov_b32 s41, 0x3ff0003\n"
"s_mov_b32 s42, 0\n"
"s_mov_b32 s43, 0x7ff0007\n"
"v_mfma_f32_16x16x32_bf16 v[56:59], a[116:119], v[80:83], v[56:59]\n"
"s_mov_b32 s44, 0\n"
"s_mov_b32 s45, 0xfff000f\n"
"s_mov_b32 s46, 0\n"
"v_mfma_f32_16x16x32_bf16 v[56:59], a[112:115], v[52:55], v[56:59]\n"
"buffer_load_dwordx4 v[92:95], v1, s[4:7], s98 offen offset:1024 sc1\n"
"v_mfma_f32_16x16x32_bf16 v[56:59], a[108:111], v[196:199], v[56:59]\n"
"s_mov_b32 s47, 0x1fff001f\n"
"s_mov_b32 s48, 0\n"
"s_mov_b32 s49, 0x3fff003f\n"
"v_mfma_f32_16x16x32_bf16 v[56:59], a[104:107], v[200:203], v[56:59]\n"
"ds_write_b128 v3, v[28:31] offset:8192\n"
"v_mfma_f32_16x16x32_bf16 v[56:59], a[100:103], v[88:91], v[56:59]\n"
"s_mov_b32 s50, 0\n"
"s_mov_b32 s51, 0x7fff007f\n"
"v_mfma_f32_16x16x32_bf16 v[56:59], a[96:99], v[84:87], v[56:59]\n"
"v_mfma_f32_16x16x32_bf16 v[28:31], v[100:103], v[84:87], 0\n"
"buffer_load_dwordx4 v[168:171], v1, s[4:7], s98 offen offset:2048 sc1\n"
"v_mfma_f32_16x16x32_bf16 v[28:31], v[104:107], v[88:91], v[28:31]\n"
"buffer_load_dwordx4 a[216:219], v1, s[4:7], s97 offen offset:2048 sc1\n"
"v_mfma_f32_16x16x32_bf16 v[28:31], v[108:111], v[200:203], v[28:31]\n"
"v_mfma_f32_16x16x32_bf16 v[28:31], v[116:119], v[196:199], v[28:31]\n"
"v_mfma_f32_16x16x32_bf16 v[28:31], v[128:131], v[52:55], v[28:31]\n"
"s_waitcnt vmcnt(12)\n"
"v_add_f32_e32 v255, v255, v254\n"
"v_add_f32_e32 v253, v253, v250\n"
"v_mfma_f32_16x16x32_bf16 v[28:31], v[132:135], v[80:83], v[28:31]\n"
"buffer_load_dwordx4 v[176:179], v1, s[4:7], s98 offen offset:3072 sc1\n"
"v_mfma_f32_16x16x32_bf16 v[28:31], v[136:139], v[140:143], v[28:31]\n"
"ds_write_b128 v3, v[56:59] offset:9216\n"
"v_mfma_f32_16x16x32_bf16 v[28:31], v[96:99], v[188:191], v[28:31]\n"
"s_add_u32 s98, s75, 0x1000\n"
"buffer_load_dwordx2 v[246:247], v13, s[4:7], s98 offen sc1\n"
"v_mfma_f32_16x16x32_bf16 v[56:59], v[112:115], v[188:191], 0\n"
"v_add_f32_e32 v252, v252, v249\n"
"v_add_f32_e32 v251, v251, v248\n"
"v_mfma_f32_16x16x32_bf16 v[56:59], v[164:167], v[140:143], v[56:59]\n"
"v_add_f32_e32 v255, v255, v252\n"
"v_add_f32_e32 v253, v253, v251\n"
"v_mfma_f32_16x16x32_bf16 v[56:59], v[160:163], v[80:83], v[56:59]\n"
"s_add_u32 s96, s71, 0\n"
"buffer_load_dwordx2 v[250:251], off, s[4:7], s96 sc1\n"
"v_mfma_f32_16x16x32_bf16 v[56:59], v[156:159], v[52:55], v[56:59]\n"
"v_add_f32_e32 v255, v255, v253\n"
"v_mov_b32_e32 v253, v255\n"
"v_mfma_f32_16x16x32_bf16 v[56:59], v[124:127], v[196:199], v[56:59]\n"
"s_nop 0\n"
"v_permlane32_swap_b32_e32 v255, v253\n"
"v_add_f32_e32 v255, v255, v253\n"
"v_mfma_f32_16x16x32_bf16 v[56:59], v[148:151], v[200:203], v[56:59]\n"
"ds_write_b128 v3, v[28:31] offset:10240\n"
"v_mfma_f32_16x16x32_bf16 v[56:59], v[144:147], v[88:91], v[56:59]\n"
"buffer_store_dwordx2 v[18:19], v11, s[4:7], s68 offen offset:32 sc1\n"
"v_mul_f32_e32 v255, 0x3a800000, v255\n"
"v_mov_b32_e32 v253, v255\n"
"v_mfma_f32_16x16x32_bf16 v[56:59], v[120:123], v[84:87], v[56:59]\n"
"s_nop 0\n"
"v_permlane16_swap_b32_e32 v255, v253\n"
"v_cvt_pk_bf16_f32 v254, -v255, 0\n"
"; @A G1 3.2\n"
"v_mfma_f32_16x16x32_bf16 v[28:31], a[0:3], v[60:63], 0\n"
"v_fma_f32 v253, -v255, v255, v253\n"
"v_lshlrev_b32_e32 v254, 16, v254\n"
"v_mfma_f32_16x16x32_bf16 v[28:31], a[4:7], v[64:67], v[28:31]\n"
"v_max_f32_e32 v253, 0, v253\n"
"v_cvt_pk_bf16_f32 v253, v253, 0\n"
"v_mfma_f32_16x16x32_bf16 v[28:31], a[8:11], v[72:75], v[28:31]\n"
"buffer_load_dwordx4 a[220:223], v1, s[4:7], s97 offen offset:3072 sc1\n"
"v_mfma_f32_16x16x32_bf16 v[28:31], a[12:15], v[76:79], v[28:31]\n"
"s_waitcnt vmcnt(16)\n"
"v_lshlrev_b32_e32 v253, 16, v253\n"
"v_add_f32_e32 v253, 0x3727c5ac, v253\n"
"v_mfma_f32_16x16x32_bf16 v[28:31], a[16:19], v[68:71], v[28:31]\n"
"s_add_u32 s97, s70, 0x6000\n"
"v_cvt_pk_bf16_f32 v253, v253, 0\n"
"v_lshlrev_b32_e32 v253, 16, v253\n"
"v_mfma_f32_16x16x32_bf16 v[28:31], a[20:23], v[44:47], v[28:31]\n"
"ds_write_b128 v3, v[56:59] offset:11264\n"
"v_mfma_f32_16x16x32_bf16 v[28:31], a[24:27], v[152:155], v[28:31]\n"
"buffer_load_dwordx4 a[224:227], v0, s[4:7], s97 offen sc1\n"
"v_mfma_f32_16x16x32_bf16 v[28:31], a[28:31], v[172:175], v[28:31]\n"
"v_sqrt_f32_e32 v253, v253\n"
"v_mfma_f32_16x16x32_bf16 v[52:55], a[60:63], v[172:175], 0\n"
"v_cvt_pk_bf16_f32 v253, v253, 0\n"
"v_lshlrev_b32_e32 v253, 16, v253\n"
"v_mfma_f32_16x16x32_bf16 v[52:55], a[56:59], v[152:155], v[52:55]\n"
"v_rcp_f32_e32 v255, v253\n"
"v_mfma_f32_16x16x32_bf16 v[52:55], a[52:55], v[44:47], v[52:55]\n"
"s_waitcnt lgkmcnt(0)\n"
"s_barrier\n"
"v_mfma_f32_16x16x32_bf16 v[52:55], a[48:51], v[68:71], v[52:55]\n"
"buffer_load_dwordx4 a[228:231], v0, s[4:7], s97 offen offset:1024 sc1\n"
"v_mfma_f32_16x16x32_bf16 v[52:55], a[44:47], v[76:79], v[52:55]\n"
"ds_read_b128 v[56:59], v4\n"
"v_mul_f32_e32 v253, 0x3e8293ee, v255\n"
"v_mfma_f32_16x16x32_bf16 v[52:55], a[40:43], v[72:75], v[52:55]\n"
"ds_write_b128 v3, v[28:31] offset:12288\n"
"v_mfma_f32_16x16x32_bf16 v[52:55], a[36:39], v[64:67], v[52:55]\n"
"ds_read_b128 v[28:31], v9 offset:128\n"
"ds_read_b128 v[80:83], v5\n"
"v_mfma_f32_16x16x32_bf16 v[52:55], a[32:35], v[60:63], v[52:55]\n"
"buffer_load_dwordx4 a[232:235], v0, s[4:7], s97 offen offset:2048 sc1\n"
"v_mfma_f32_16x16x32_bf16 v[84:87], a[64:67], v[60:63], 0\n"
"v_mfma_f32_16x16x32_bf16 v[84:87], a[68:71], v[64:67], v[84:87]\n"
"v_mfma_f32_16x16x32_bf16 v[84:87], a[72:75], v[72:75], v[84:87]\n"
"ds_read_b128 v[88:91], v4 offset:1024\n"
"ds_read_b128 v[140:143], v9 offset:192\n"
"v_mfma_f32_16x16x32_bf16 v[84:87], a[76:79], v[76:79], v[84:87]\n"
"buffer_load_dwordx4 a[236:239], v0, s[4:7], s97 offen offset:3072 sc1\n"
"v_mfma_f32_16x16x32_bf16 v[84:87], a[80:83], v[68:71], v[84:87]\n"
"ds_read_b128 v[180:183], v5 offset:1024\n"
"s_waitcnt lgkmcnt(3)\n"
"v_mfma_f32_16x16x32_bf16 v[84:87], a[84:87], v[44:47], v[84:87]\n"
"ds_write_b128 v3, v[52:55] offset:13312\n"
"v_mfma_f32_16x16x32_bf16 v[84:87], a[88:91], v[152:155], v[84:87]\n"
"v_fmac_f32_e32 v56, v254, v28\n"
"v_fmac_f32_e32 v57, v254, v29\n"
"v_mfma_f32_16x16x32_bf16 v[84:87], a[92:95], v[172:175], v[84:87]\n"
"buffer_load_dwordx4 a[240:243], v1, s[4:7], s97 offen sc1\n"
"v_mfma_f32_16x16x32_bf16 v[52:55], a[124:127], v[172:175], 0\n"
"v_fmac_f32_e32 v58, v254, v30\n"
"v_fmac_f32_e32 v59, v254, v31\n"
"v_mfma_f32_16x16x32_bf16 v[52:55], a[120:123], v[152:155], v[52:55]\n"
"ds_read_b128 v[28:31], v4 offset:2048\n"
"ds_read_b128 v[184:187], v9 offset:256\n"
"v_mfma_f32_16x16x32_bf16 v[52:55], a[116:119], v[44:47], v[52:55]\n"
"ds_read_b128 v[188:191], v5 offset:2048\n"
"v_add_f32_e32 v56, v56, v80\n"
"v_mfma_f32_16x16x32_bf16 v[52:55], a[112:115], v[68:71], v[52:55]\n"
"buffer_load_dwordx4 a[244:247], v1, s[4:7], s97 offen offset:1024 sc1\n"
"v_mfma_f32_16x16x32_bf16 v[52:55], a[108:111], v[76:79], v[52:55]\n"
"v_add_f32_e32 v57, v57, v81\n"
"v_add_f32_e32 v58, v58, v82\n"
"v_mfma_f32_16x16x32_bf16 v[52:55], a[104:107], v[72:75], v[52:55]\n"
"ds_write_b128 v3, v[84:87] offset:14336\n"
"v_mfma_f32_16x16x32_bf16 v[52:55], a[100:103], v[64:67], v[52:55]\n"
"s_waitcnt lgkmcnt(5)\n"
"v_add_f32_e32 v59, v59, v83\n"
"v_fmac_f32_e32 v88, v254, v140\n"
"v_mfma_f32_16x16x32_bf16 v[52:55], a[96:99], v[60:63], v[52:55]\n"
"buffer_load_dwordx4 a[248:251], v1, s[4:7], s97 offen offset:2048 sc1\n"
"v_mfma_f32_16x16x32_bf16 v[80:83], v[100:103], v[60:63], 0\n"
"ds_read_b128 v[84:87], v4 offset:3072\n"
"ds_read_b128 v[192:195], v9 offset:320\n"
"v_mfma_f32_16x16x32_bf16 v[80:83], v[104:107], v[64:67], v[80:83]\n"
"ds_read_b128 v[196:199], v5 offset:3072\n"
"v_fmac_f32_e32 v89, v254, v141\n"
"v_mfma_f32_16x16x32_bf16 v[80:83], v[108:111], v[72:75], v[80:83]\n"
"v_fmac_f32_e32 v90, v254, v142\n"
"v_fmac_f32_e32 v91, v254, v143\n"
"v_mfma_f32_16x16x32_bf16 v[80:83], v[116:119], v[76:79], v[80:83]\n"
"buffer_load_dwordx4 a[252:255], v1, s[4:7], s97 offen offset:3072 sc1\n"
"v_mfma_f32_16x16x32_bf16 v[80:83], v[128:131], v[68:71], v[80:83]\n"
"s_waitcnt vmcnt(11)\n"
"v_cmp_ne_u64_e32 vcc, v[246:247], v[18:19]\n"
"v_mfma_f32_16x16x32_bf16 v[80:83], v[132:135], v[44:47], v[80:83]\n"
"ds_write_b128 v3, v[52:55] offset:15360\n"
"v_mfma_f32_16x16x32_bf16 v[80:83], v[136:139], v[152:155], v[80:83]\n"
"s_cbranch_vccnz .Lmk_f7s\n"
".Lmk_f7r:\n"
"v_add_f32_e32 v88, v88, v180\n"
"v_mfma_f32_16x16x32_bf16 v[80:83], v[96:99], v[172:175], v[80:83]\n"
"ds_read_b128 v[52:55], v4 offset:4096\n"
"ds_read_b128 v[140:143], v9\n"
"v_mfma_f32_16x16x32_bf16 v[200:203], v[112:115], v[172:175], 0\n"
"ds_read_b128 v[172:175], v5 offset:4096\n"
"v_add_f32_e32 v89, v89, v181\n"
"v_mfma_f32_16x16x32_bf16 v[200:203], v[164:167], v[152:155], v[200:203]\n"
"s_waitcnt lgkmcnt(8)\n"
"v_add_f32_e32 v90, v90, v182\n"
"v_add_f32_e32 v91, v91, v183\n"
"v_mfma_f32_16x16x32_bf16 v[200:203], v[160:163], v[44:47], v[200:203]\n"
"v_fmac_f32_e32 v28, v254, v184\n"
"v_fmac_f32_e32 v29, v254, v185\n"
"v_mfma_f32_16x16x32_bf16 v[200:203], v[156:159], v[68:71], v[200:203]\n"
"v_fmac_f32_e32 v30, v254, v186\n"
"v_fmac_f32_e32 v31, v254, v187\n"
"v_mfma_f32_16x16x32_bf16 v[200:203], v[124:127], v[76:79], v[200:203]\n"
"s_waitcnt vmcnt(10)\n"
"v_cmp_ne_u64_e32 vcc, v[250:251], v[18:19]\n"
"v_mfma_f32_16x16x32_bf16 v[200:203], v[148:151], v[72:75], v[200:203]\n"
"ds_write_b128 v3, v[80:83] offset:16384\n"
"v_mfma_f32_16x16x32_bf16 v[200:203], v[144:147], v[64:67], v[200:203]\n"
"s_cbranch_vccnz .Lmk_f4s\n"
".Lmk_f4r:\n"
"s_add_u32 m0, s77, 0x6000\n"
"v_mfma_f32_16x16x32_bf16 v[200:203], v[120:123], v[60:63], v[200:203]\n"
"s_add_u32 s98, s65, 0x2000\n"
"ds_read_b128 v[44:47], v4 offset:5120\n"
"; @A G1 3.3\n"
"v_mfma_f32_16x16x32_bf16 v[56:59], a[0:3], v[32:35], v[56:59]\n"
"buffer_load_dwordx4 v2, s[4:7], s98 offen sc1 lds\n"
"v_mfma_f32_16x16x32_bf16 v[56:59], a[4:7], v[36:39], v[56:59]\n"
"buffer_load_dwordx4 v2, s[4:7], s98 offen offset:1024 sc1 lds\n"
"v_mfma_f32_16x16x32_bf16 v[56:59], a[8:11], v[24:27], v[56:59]\n"
"ds_read_b128 v[60:63], v9 offset:64\n"
"ds_read_b128 v[64:67], v5 offset:5120\n"
"v_mfma_f32_16x16x32_bf16 v[56:59], a[12:15], v[48:51], v[56:59]\n"
"v_add_f32_e32 v28, v28, v188\n"
"v_add_f32_e32 v29, v29, v189\n"
"v_mfma_f32_16x16x32_bf16 v[56:59], a[16:19], v[40:43], v[56:59]\n"
"s_waitcnt lgkmcnt(8)\n"
"v_add_f32_e32 v30, v30, v190\n"
"v_add_f32_e32 v31, v31, v191\n"
"v_mfma_f32_16x16x32_bf16 v[56:59], a[20:23], v[92:95], v[56:59]\n"
"ds_write_b128 v3, v[200:203] offset:17408\n"
"v_mfma_f32_16x16x32_bf16 v[56:59], a[24:27], v[168:171], v[56:59]\n"
"v_fmac_f32_e32 v84, v254, v192\n"
"v_fmac_f32_e32 v85, v254, v193\n"
"v_mfma_f32_16x16x32_bf16 v[56:59], a[28:31], v[176:179], v[56:59]\n"
"s_add_u32 m0, s77, 0xe000\n"
"s_add_u32 s98, s65, 0x6000\n"
"v_fmac_f32_e32 v86, v254, v194\n"
"v_mfma_f32_16x16x32_bf16 v[88:91], a[60:63], v[176:179], v[88:91]\n"
"buffer_load_dwordx4 v2, s[4:7], s98 offen sc1 lds\n"
"v_mfma_f32_16x16x32_bf16 v[88:91], a[56:59], v[168:171], v[88:91]\n"
"buffer_load_dwordx4 v2, s[4:7], s98 offen offset:1024 sc1 lds\n"
"v_mfma_f32_16x16x32_bf16 v[88:91], a[52:55], v[92:95], v[88:91]\n"
"s_waitcnt lgkmcnt(0)\n"
"s_barrier\n"
"v_mfma_f32_16x16x32_bf16 v[88:91], a[48:51], v[40:43], v[88:91]\n"
"ds_read_b128 v[68:71], v6\n"
"ds_read_b128 v[72:75], v6 offset:1024\n"
"v_mfma_f32_16x16x32_bf16 v[88:91], a[44:47], v[48:51], v[88:91]\n"
"s_add_u32 s98, s69, 0x600000\n"
"buffer_load_dwordx4 a[0:3], v0, s[4:7], s98 offen sc1\n"
"v_mfma_f32_16x16x32_bf16 v[88:91], a[40:43], v[24:27], v[88:91]\n"
"v_fmac_f32_e32 v87, v254, v195\n"
"v_add_f32_e32 v84, v84, v196\n"
"v_mfma_f32_16x16x32_bf16 v[88:91], a[36:39], v[36:39], v[88:91]\n"
"v_add_f32_e32 v85, v85, v197\n"
"v_add_f32_e32 v86, v86, v198\n"
"v_mfma_f32_16x16x32_bf16 v[88:91], a[32:35], v[32:35], v[88:91]\n"
"buffer_load_dwordx4 a[4:7], v0, s[4:7], s98 offen offset:1024 sc1\n"
"v_mfma_f32_16x16x32_bf16 v[28:31], a[64:67], v[32:35], v[28:31]\n"
"v_add_f32_e32 v87, v87, v199\n"
"v_fmac_f32_e32 v52, v254, v140\n"
"v_mfma_f32_16x16x32_bf16 v[28:31], a[68:71], v[36:39], v[28:31]\n"
"v_fmac_f32_e32 v53, v254, v141\n"
"v_fmac_f32_e32 v54, v254, v142\n"
"v_mfma_f32_16x16x32_bf16 v[28:31], a[72:75], v[24:27], v[28:31]\n"
"buffer_load_dwordx4 a[8:11], v0, s[4:7], s98 offen offset:2048 sc1\n"
"v_mfma_f32_16x16x32_bf16 v[28:31], a[76:79], v[48:51], v[28:31]\n"
"v_fmac_f32_e32 v55, v254, v143\n"
"v_add_f32_e32 v52, v52, v172\n"
"v_mfma_f32_16x16x32_bf16 v[28:31], a[80:83], v[40:43], v[28:31]\n"
"s_waitcnt lgkmcnt(0)\n"
"v_add_f32_e32 v53, v53, v173\n"
"v_add_f32_e32 v54, v54, v174\n"
"v_mfma_f32_16x16x32_bf16 v[28:31], a[84:87], v[92:95], v[28:31]\n"
"buffer_load_dwordx4 a[12:15], v0, s[4:7], s98 offen offset:3072 sc1\n"
"v_mfma_f32_16x16x32_bf16 v[28:31], a[88:91], v[168:171], v[28:31]\n"
"ds_read_b128 v[76:79], v6 offset:2048\n"
"ds_read_b128 v[80:83], v6 offset:3072\n"
"v_mfma_f32_16x16x32_bf16 v[28:31], a[92:95], v[176:179], v[28:31]\n"
"v_add_f32_e32 v55, v55, v175\n"
"v_fmac_f32_e32 v44, v254, v60\n"
"v_mfma_f32_16x16x32_bf16 v[84:87], a[124:127], v[176:179], v[84:87]\n"
"buffer_load_dwordx4 a[16:19], v1, s[4:7], s98 offen sc1\n"
"v_mfma_f32_16x16x32_bf16 v[84:87], a[120:123], v[168:171], v[84:87]\n"
"v_fmac_f32_e32 v45, v254, v61\n"
"v_fmac_f32_e32 v46, v254, v62\n"
"v_mfma_f32_16x16x32_bf16 v[84:87], a[116:119], v[92:95], v[84:87]\n"
"v_fmac_f32_e32 v47, v254, v63\n"
"v_add_f32_e32 v44, v44, v64\n"
"v_mfma_f32_16x16x32_bf16 v[84:87], a[112:115], v[40:43], v[84:87]\n"
"buffer_load_dwordx4 a[20:23], v1, s[4:7], s98 offen offset:1024 sc1\n"
"v_mfma_f32_16x16x32_bf16 v[84:87], a[108:111], v[48:51], v[84:87]\n"
"v_add_f32_e32 v45, v45, v65\n"
"v_add_f32_e32 v46, v46, v66\n"
"v_mfma_f32_16x16x32_bf16 v[84:87], a[104:107], v[24:27], v[84:87]\n"
"v_add_f32_e32 v47, v47, v67\n"
"v_add_f32_e32 v56, v56, v68\n"
"v_mfma_f32_16x16x32_bf16 v[84:87], a[100:103], v[36:39], v[84:87]\n"
"buffer_load_dwordx4 a[24:27], v1, s[4:7], s98 offen offset:2048 sc1\n"
"v_mfma_f32_16x16x32_bf16 v[84:87], a[96:99], v[32:35], v[84:87]\n"
"v_add_f32_e32 v57, v57, v69\n"
"v_add_f32_e32 v58, v58, v70\n"
"v_mfma_f32_16x16x32_bf16 v[52:55], v[100:103], v[32:35], v[52:55]\n"
"v_add_f32_e32 v59, v59, v71\n"
"v_add_f32_e32 v88, v88, v72\n"
"v_mfma_f32_16x16x32_bf16 v[52:55], v[104:107], v[36:39], v[52:55]\n"
"buffer_load_dwordx4 a[28:31], v1, s[4:7], s98 offen offset:3072 sc1\n"
"v_mfma_f32_16x16x32_bf16 v[52:55], v[108:111], v[24:27], v[52:55]\n"
"v_add_f32_e32 v89, v89, v73\n"
"v_add_f32_e32 v90, v90, v74\n"
"v_mfma_f32_16x16x32_bf16 v[52:55], v[116:119], v[48:51], v[52:55]\n"
"v_add_f32_e32 v91, v91, v75\n"
"v_mul_f32_e32 v56, v56, v255\n"
"v_mfma_f32_16x16x32_bf16 v[52:55], v[128:131], v[40:43], v[52:55]\n"
"s_waitcnt lgkmcnt(0)\n"
"v_mul_f32_e32 v57, v57, v255\n"
"v_mul_f32_e32 v58, v58, v255\n"
"v_mfma_f32_16x16x32_bf16 v[52:55], v[132:135], v[92:95], v[52:55]\n"
"v_mul_f32_e32 v59, v59, v255\n"
"v_mul_f32_e32 v88, v88, v255\n"
"v_mfma_f32_16x16x32_bf16 v[52:55], v[136:139], v[168:171], v[52:55]\n"
"ds_read_b128 v[60:63], v6 offset:4096\n"
"ds_read_b128 v[64:67], v6 offset:5120\n"
"v_mfma_f32_16x16x32_bf16 v[52:55], v[96:99], v[176:179], v[52:55]\n"
"v_mul_f32_e32 v89, v89, v255\n"
"v_mul_f32_e32 v90, v90, v255\n"
"v_mfma_f32_16x16x32_bf16 v[44:47], v[112:115], v[176:179], v[44:47]\n"
"v_mul_f32_e32 v91, v91, v255\n"
"v_cvt_pk_bf16_f32 v68, v56, v57\n"
"v_mfma_f32_16x16x32_bf16 v[44:47], v[164:167], v[168:171], v[44:47]\n"
"v_cvt_pk_bf16_f32 v69, v58, v59\n"
"v_cvt_pk_bf16_f32 v70, v88, v89\n"
"v_mfma_f32_16x16x32_bf16 v[44:47], v[160:163], v[92:95], v[44:47]\n"
"v_cvt_pk_bf16_f32 v71, v90, v91\n"
"v_add_f32_e32 v28, v28, v76\n"
"v_mfma_f32_16x16x32_bf16 v[44:47], v[156:159], v[40:43], v[44:47]\n"
"ds_write_b128 v7, v[68:71] offset:16384\n"
"v_mfma_f32_16x16x32_bf16 v[44:47], v[124:127], v[48:51], v[44:47]\n"
"buffer_store_dwordx4 v[68:71], v7, s[4:7], s64 offen sc1\n"
"v_mfma_f32_16x16x32_bf16 v[44:47], v[148:151], v[24:27], v[44:47]\n"
"v_add_f32_e32 v29, v29, v77\n"
"v_add_f32_e32 v30, v30, v78\n"
"v_mfma_f32_16x16x32_bf16 v[44:47], v[144:147], v[36:39], v[44:47]\n"
"v_add_f32_e32 v31, v31, v79\n"
"v_add_f32_e32 v84, v84, v80\n"
"v_mfma_f32_16x16x32_bf16 v[44:47], v[120:123], v[32:35], v[44:47]\n"
"v_add_f32_e32 v85, v85, v81\n"
"v_add_f32_e32 v86, v86, v82\n"
"v_add_f32_e32 v87, v87, v83\n"
"v_mul_f32_e32 v28, v28, v255\n"
"v_mul_f32_e32 v29, v29, v255\n"
"v_mul_f32_e32 v30, v30, v255\n"
"v_mul_f32_e32 v31, v31, v255\n"
"v_mul_f32_e32 v84, v84, v255\n"
"v_mul_f32_e32 v85, v85, v255\n"
"v_mul_f32_e32 v86, v86, v255\n"
"v_mul_f32_e32 v87, v87, v255\n"
"v_cvt_pk_bf16_f32 v24, v28, v29\n"
"v_cvt_pk_bf16_f32 v25, v30, v31\n"
"v_cvt_pk_bf16_f32 v26, v84, v85\n"
"v_cvt_pk_bf16_f32 v27, v86, v87\n"
"v_xor_b32_e32 v255, 32, v16\n"
"v_xor_b32_e32 v254, 32, v15\n"
"v_mov_b32_e32 v88, 0x3f803f80\n"
"v_mov_b32_e32 v89, 0x3f803f80\n"
"v_mov_b32_e32 v90, 0x3f803f80\n"
"v_mov_b32_e32 v91, 0x3f803f80\n"
"v_mov_b32_e32 v252, 0x53800000\n"
"ds_write_b128 v7, v[24:27] offset:49152\n"
".Lmk_z4:\n"
"; @A tail\n"
"s_add_u32 s98, s64, 0x4000\n"
"buffer_store_dwordx4 v[24:27], v7, s[4:7], s98 offen sc1\n"
"s_waitcnt lgkmcnt(2)\n"
"v_add_f32_e32 v52, v52, v60\n"
"v_add_f32_e32 v53, v53, v61\n"
"v_add_f32_e32 v54, v54, v62\n"
"v_add_f32_e32 v55, v55, v63\n"
"v_add_f32_e32 v44, v44, v64\n"
"v_add_f32_e32 v45, v45, v65\n"
"v_add_f32_e32 v46, v46, v66\n"
"v_add_f32_e32 v47, v47, v67\n"
"v_mul_f32_e32 v52, v52, v253\n"
"v_mul_f32_e32 v53, v53, v253\n"
"v_mul_f32_e32 v54, v54, v253\n"
"v_mul_f32_e32 v55, v55, v253\n"
"v_mul_f32_e32 v44, v44, v253\n"
"v_mul_f32_e32 v45, v45, v253\n"
"v_mul_f32_e32 v46, v46, v253\n"
"v_mul_f32_e32 v47, v47, v253\n"
"v_cvt_pk_bf16_f32 v24, v52, v53\n"
"v_cvt_pk_bf16_f32 v25, v54, v55\n"
"v_cvt_pk_bf16_f32 v26, v44, v45\n"
"v_cvt_pk_bf16_f32 v27, v46, v47\n"
"ds_write_b128 v2, v[24:27] offset:16384\n"
"s_waitcnt lgkmcnt(0)\n"
"s_barrier\n"
"ds_read_b128 v[28:31], v2 offset:49152\n"
"ds_read_b128 v[32:35], v2 offset:50176\n"
"ds_read_b128 v[92:95], v2 offset:16384\n"
"ds_read_b128 v[96:99], v2 offset:17408\n"
"; @A AT b\n"
"s_mov_b32 s2, 0\n"
"v_mov_b32_e32 v253, 0xff800000\n"
"v_mov_b32_e32 v251, 0xff800000\n"
"v_mov_b32_e32 v250, 0x7f800000\n"
"v_mov_b32_e32 v249, 0x7f800000\n"
".Lmk_a15:\n"
"ds_read_b128 v[24:27], v15 offset:24576\n"
"ds_read_b128 v[36:39], v254 offset:24832\n"
"ds_read_b128 v[40:43], v15 offset:22528\n"
"ds_read_b128 v[44:47], v254 offset:22784\n"
"s_cmp_gt_i32 s78, 11\n"
"s_cselect_b32 s98, 0x80000000, 0\n"
"s_or_b32 s98, s98, 0x7f800000\n"
"s_waitcnt lgkmcnt(2)\n"
"v_mfma_f32_16x16x32_bf16 v[48:51], v[24:27], v[28:31], 0\n"
"v_mfma_f32_16x16x32_bf16 v[52:55], v[24:27], v[32:35], 0\n"
"v_mfma_f32_16x16x32_bf16 v[56:59], v[36:39], v[32:35], 0\n"
"v_mfma_f32_16x16x32_bf16 v[60:63], v[36:39], v[28:31], 0\n"
"s_waitcnt lgkmcnt(0)\n"
"s_nop 3\n"
"v_max_f32_e32 v248, v48, v49\n"
"v_max_f32_e32 v247, v52, v53\n"
"v_max3_f32 v248, v248, v50, v51\n"
"v_max3_f32 v247, v247, v54, v55\n"
"v_max3_f32 v248, v248, v60, v61\n"
"v_max3_f32 v247, v247, v56, v57\n"
"v_max3_f32 v248, v248, v62, v63\n"
"v_max3_f32 v247, v247, v58, v59\n"
"v_mov_b32_e32 v246, v248\n"
"v_mov_b32_e32 v245, v247\n"
"s_nop 0\n"
"v_permlane16_swap_b32_e32 v248, v246\n"
"v_permlane16_swap_b32_e32 v247, v245\n"
"v_max_f32_e32 v248, v248, v246\n"
"v_max_f32_e32 v247, v247, v245\n"
"v_mov_b32_e32 v246, v248\n"
"v_mov_b32_e32 v245, v247\n"
"s_nop 0\n"
"v_permlane32_swap_b32_e32 v248, v246\n"
"v_permlane32_swap_b32_e32 v247, v245\n"
"v_max_f32_e32 v248, v248, v246\n"
"v_max_f32_e32 v247, v247, v245\n"
"v_med3_f32 v248, v248, v253, v250\n"
"v_med3_f32 v247, v247, v251, v249\n"
"v_min_f32_e64 v24, s98, -v248\n"
"v_min_f32_e64 v25, s98, -v248\n"
"v_min_f32_e64 v26, s98, -v248\n"
"v_min_f32_e64 v27, s98, -v248\n"
"v_min_f32_e64 v36, s98, -v247\n"
"v_min_f32_e64 v37, s98, -v247\n"
"v_min_f32_e64 v38, s98, -v247\n"
"v_min_f32_e64 v39, s98, -v247\n"
"v_mfma_f32_16x16x32_bf16 v[64:67], v[40:43], v[28:31], v[24:27]\n"
"ds_read_b128 v[68:71], v15 offset:20480\n"
"s_add_u32 s98, s69, 0x602000\n"
"buffer_load_dwordx4 a[32:35], v0, s[4:7], s98 offen sc1\n"
"v_sub_f32_e32 v48, v48, v248\n"
"v_sub_f32_e32 v49, v49, v248\n"
"v_sub_f32_e32 v50, v50, v248\n"
"v_mfma_f32_16x16x32_bf16 v[72:75], v[40:43], v[32:35], v[36:39]\n"
"buffer_load_dwordx4 a[36:39], v0, s[4:7], s98 offen offset:1024 sc1\n"
"v_sub_f32_e32 v51, v51, v248\n"
"v_sub_f32_e32 v60, v60, v248\n"
"v_sub_f32_e32 v61, v61, v248\n"
"v_sub_f32_e32 v62, v62, v248\n"
"v_sub_f32_e32 v63, v63, v248\n"
"v_mfma_f32_16x16x32_bf16 v[40:43], v[44:47], v[32:35], v[36:39]\n"
"ds_read_b128 v[76:79], v254 offset:20736\n"
"v_sub_f32_e32 v52, v52, v247\n"
"v_sub_f32_e32 v53, v53, v247\n"
"v_sub_f32_e32 v54, v54, v247\n"
"v_sub_f32_e32 v55, v55, v247\n"
"v_mfma_f32_16x16x32_bf16 v[80:83], v[44:47], v[28:31], v[24:27]\n"
"buffer_load_dwordx4 a[40:43], v0, s[4:7], s98 offen offset:2048 sc1\n"
"v_sub_f32_e32 v56, v56, v247\n"
"v_sub_f32_e32 v57, v57, v247\n"
"v_sub_f32_e32 v58, v58, v247\n"
"v_sub_f32_e32 v59, v59, v247\n"
"s_waitcnt lgkmcnt(0)\n"
"v_mfma_f32_16x16x32_bf16 v[44:47], v[68:71], v[28:31], v[24:27]\n"
"ds_read_b128 v[84:87], v15 offset:18432\n"
"buffer_load_dwordx4 a[44:47], v0, s[4:7], s98 offen offset:3072 sc1\n"
"ds_read_b128 v[100:103], v254 offset:18688\n"
"v_exp_f32_e32 v64, v64\n"
"v_exp_f32_e32 v65, v65\n"
"v_exp_f32_e32 v66, v66\n"
"v_exp_f32_e32 v67, v67\n"
"v_exp_f32_e32 v80, v80\n"
"v_exp_f32_e32 v81, v81\n"
"v_mfma_f32_16x16x32_bf16 v[104:107], v[68:71], v[32:35], v[36:39]\n"
"buffer_load_dwordx4 a[48:51], v1, s[4:7], s98 offen sc1\n"
"ds_read_b64_tr_b16 v[68:69], v16 offset:22528\n"
"buffer_load_dwordx4 a[52:55], v1, s[4:7], s98 offen offset:1024 sc1\n"
"ds_read_b64_tr_b16 v[70:71], v255 offset:22784\n"
"v_exp_f32_e32 v82, v82\n"
"v_exp_f32_e32 v83, v83\n"
"v_cvt_pk_bf16_f32 v108, v64, v65\n"
"v_cvt_pk_bf16_f32 v109, v66, v67\n"
"v_cvt_pk_bf16_f32 v110, v80, v81\n"
"v_cvt_pk_bf16_f32 v111, v82, v83\n"
"v_exp_f32_e32 v72, v72\n"
"v_mfma_f32_16x16x32_bf16 v[64:67], v[76:79], v[32:35], v[36:39]\n"
"s_add_u32 s97, s76, 0x80000\n"
"v_exp_f32_e32 v73, v73\n"
"v_exp_f32_e32 v74, v74\n"
"v_exp_f32_e32 v75, v75\n"
"v_bfe_u32 v248, v0, 2, 7\n"
"v_add_lshl_u32 v248, s72, v248, 1\n"
"v_and_b32_e32 v247, 0x200, v0\n"
"v_lshl_add_u32 v248, v247, 4, v248\n"
"v_mfma_f32_16x16x32_bf16 v[112:115], v[76:79], v[28:31], v[24:27]\n"
"buffer_load_dword v247, v248, s[4:7], s97 offen sc1\n"
"ds_read_b64_tr_b16 v[80:81], v255 offset:22528\n"
"buffer_load_dword v246, v248, s[4:7], s97 offen offset:1024 sc1\n"
"ds_read_b64_tr_b16 v[82:83], v16 offset:22784\n"
"v_exp_f32_e32 v40, v40\n"
"v_exp_f32_e32 v41, v41\n"
"v_exp_f32_e32 v42, v42\n"
"v_exp_f32_e32 v43, v43\n"
"v_cvt_pk_bf16_f32 v76, v72, v73\n"
"v_cvt_pk_bf16_f32 v77, v74, v75\n"
"v_cvt_pk_bf16_f32 v78, v40, v41\n"
"v_cvt_pk_bf16_f32 v79, v42, v43\n"
"s_waitcnt lgkmcnt(4)\n"
"v_mfma_f32_16x16x32_bf16 v[40:43], v[84:87], v[28:31], v[24:27]\n"
"ds_read_b128 v[72:75], v15 offset:16384\n"
"v_exp_f32_e32 v44, v44\n"
"v_exp_f32_e32 v45, v45\n"
"v_exp_f32_e32 v46, v46\n"
"v_mfma_f32_16x16x32_bf16 v[116:119], v[84:87], v[32:35], v[36:39]\n"
"buffer_load_dwordx4 a[56:59], v1, s[4:7], s98 offen offset:2048 sc1\n"
"ds_read_b128 v[84:87], v254 offset:16640\n"
"v_exp_f32_e32 v47, v47\n"
"v_exp_f32_e32 v112, v112\n"
"v_mfma_f32_16x16x32_bf16 v[120:123], v[100:103], v[32:35], v[36:39]\n"
"ds_read_b64_tr_b16 v[124:125], v16 offset:20480\n"
"v_exp_f32_e32 v113, v113\n"
"v_exp_f32_e32 v114, v114\n"
"v_mfma_f32_16x16x32_bf16 v[128:131], v[100:103], v[28:31], v[24:27]\n"
"buffer_load_dwordx4 a[60:63], v1, s[4:7], s98 offen offset:3072 sc1\n"
"v_exp_f32_e32 v115, v115\n"
"v_cvt_pk_bf16_f32 v100, v44, v45\n"
"v_cvt_pk_bf16_f32 v101, v46, v47\n"
"s_waitcnt lgkmcnt(3)\n"
"v_mfma_f32_16x16x32_bf16 v[44:47], v[88:91], v[108:111], 0\n"
"ds_read_b64_tr_b16 v[126:127], v255 offset:20736\n"
"s_add_u32 s98, s69, 0x604000\n"
"buffer_load_dwordx4 a[64:67], v0, s[4:7], s98 offen sc1\n"
"v_cvt_pk_bf16_f32 v102, v112, v113\n"
"v_cvt_pk_bf16_f32 v103, v114, v115\n"
"v_exp_f32_e32 v104, v104\n"
"v_mfma_f32_16x16x32_bf16 v[112:115], v[68:71], v[108:111], 0\n"
"ds_read_b64_tr_b16 v[132:133], v255 offset:20480\n"
"v_exp_f32_e32 v105, v105\n"
"v_exp_f32_e32 v106, v106\n"
"v_mfma_f32_16x16x32_bf16 v[136:139], v[80:83], v[108:111], 0\n"
"buffer_load_dword v245, v248, s[4:7], s97 offen offset:2048 sc1\n"
"v_exp_f32_e32 v107, v107\n"
"v_exp_f32_e32 v64, v64\n"
"v_mfma_f32_16x16x32_bf16 v[108:111], v[80:83], v[76:79], 0\n"
"ds_read_b64_tr_b16 v[134:135], v16 offset:20736\n"
"v_exp_f32_e32 v65, v65\n"
"v_exp_f32_e32 v66, v66\n"
"v_mfma_f32_16x16x32_bf16 v[80:83], v[68:71], v[76:79], 0\n"
"buffer_load_dword v244, v248, s[4:7], s97 offen offset:3072 sc1\n"
"s_waitcnt vmcnt(13)\n"
"buffer_store_dwordx2 v[18:19], v11, s[4:7], s68 offen offset:64 sc1\n"
"v_exp_f32_e32 v67, v67\n"
"v_mfma_f32_16x16x32_bf16 v[68:71], v[88:91], v[76:79], 0\n"
"s_cmp_lg_u32 s2, 0\n"
"v_cvt_pk_bf16_f32 v76, v104, v105\n"
"v_cvt_pk_bf16_f32 v77, v106, v107\n"
"v_cvt_pk_bf16_f32 v78, v64, v65\n"
"v_cvt_pk_bf16_f32 v79, v66, v67\n"
"s_cbranch_scc1 .Lmk_s17\n"
"s_barrier\n"
".Lmk_s17:\n"
"s_waitcnt lgkmcnt(4)\n"
"v_mfma_f32_16x16x32_bf16 v[64:67], v[72:75], v[28:31], v[24:27]\n"
"ds_read_b128 v[104:107], v15 offset:14336\n"
"v_exp_f32_e32 v40, v40\n"
"v_exp_f32_e32 v41, v41\n"
"v_exp_f32_e32 v42, v42\n"
"v_mfma_f32_16x16x32_bf16 v[140:143], v[72:75], v[32:35], v[36:39]\n"
"buffer_load_dwordx4 a[68:71], v0, s[4:7], s98 offen offset:1024 sc1\n"
"ds_read_b128 v[72:75], v254 offset:14592\n"
"v_exp_f32_e32 v43, v43\n"
"v_mfma_f32_16x16x32_bf16 v[144:147], v[84:87], v[32:35], v[36:39]\n"
"v_exp_f32_e32 v128, v128\n"
"v_exp_f32_e32 v129, v129\n"
"v_exp_f32_e32 v130, v130\n"
"v_mfma_f32_16x16x32_bf16 v[148:151], v[84:87], v[28:31], v[24:27]\n"
"buffer_load_dwordx4 a[72:75], v0, s[4:7], s98 offen offset:2048 sc1\n"
"ds_read_b64_tr_b16 v[84:85], v16 offset:18432\n"
"v_exp_f32_e32 v131, v131\n"
"s_waitcnt lgkmcnt(3)\n"
"v_mfma_f32_16x16x32_bf16 v[44:47], v[88:91], v[100:103], v[44:47]\n"
"buffer_load_dwordx4 a[76:79], v0, s[4:7], s98 offen offset:3072 sc1\n"
"v_cvt_pk_bf16_f32 v152, v40, v41\n"
"v_cvt_pk_bf16_f32 v153, v42, v43\n"
"v_cvt_pk_bf16_f32 v154, v128, v129\n"
"v_cvt_pk_bf16_f32 v155, v130, v131\n"
"v_mfma_f32_16x16x32_bf16 v[112:115], v[124:127], v[100:103], v[112:115]\n"
"ds_read_b64_tr_b16 v[86:87], v255 offset:18688\n"
"v_exp_f32_e32 v116, v116\n"
"v_exp_f32_e32 v117, v117\n"
"v_exp_f32_e32 v118, v118\n"
"v_mfma_f32_16x16x32_bf16 v[136:139], v[132:135], v[100:103], v[136:139]\n"
"s_add_u32 s97, s76, 0x81000\n"
"buffer_load_dword v243, v248, s[4:7], s97 offen sc1\n"
"v_exp_f32_e32 v119, v119\n"
"v_mfma_f32_16x16x32_bf16 v[108:111], v[132:135], v[76:79], v[108:111]\n"
"ds_read_b64_tr_b16 v[40:41], v255 offset:18432\n"
"v_exp_f32_e32 v120, v120\n"
"v_exp_f32_e32 v121, v121\n"
"v_mfma_f32_16x16x32_bf16 v[80:83], v[124:127], v[76:79], v[80:83]\n"
"buffer_load_dword v242, v248, s[4:7], s97 offen offset:1024 sc1\n"
"ds_read_b64_tr_b16 v[42:43], v16 offset:18688\n"
"v_exp_f32_e32 v122, v122\n"
"v_exp_f32_e32 v123, v123\n"
"v_mfma_f32_16x16x32_bf16 v[68:71], v[88:91], v[76:79], v[68:71]\n"
"s_waitcnt lgkmcnt(4)\n"
"v_cvt_pk_bf16_f32 v76, v116, v117\n"
"v_cvt_pk_bf16_f32 v77, v118, v119\n"
"v_cvt_pk_bf16_f32 v78, v120, v121\n"
"v_cvt_pk_bf16_f32 v79, v122, v123\n"
"s_cmp_eq_i32 s78, 8\n"
"s_cbranch_scc1 .Lmk_x18s\n"
".Lmk_x18r:\n"
"v_mfma_f32_16x16x32_bf16 v[100:103], v[104:107], v[28:31], v[24:27]\n"
"ds_read_b128 v[116:119], v15 offset:12288\n"
"v_exp_f32_e32 v64, v64\n"
"v_exp_f32_e32 v65, v65\n"
"v_exp_f32_e32 v66, v66\n"
"v_mfma_f32_16x16x32_bf16 v[120:123], v[104:107], v[32:35], v[36:39]\n"
"buffer_load_dwordx4 a[80:83], v1, s[4:7], s98 offen sc1\n"
"ds_read_b128 v[104:107], v254 offset:12544\n"
"v_exp_f32_e32 v67, v67\n"
"v_mfma_f32_16x16x32_bf16 v[124:127], v[72:75], v[32:35], v[36:39]\n"
"v_exp_f32_e32 v148, v148\n"
"v_exp_f32_e32 v149, v149\n"
"v_exp_f32_e32 v150, v150\n"
"v_mfma_f32_16x16x32_bf16 v[128:131], v[72:75], v[28:31], v[24:27]\n"
"buffer_load_dwordx4 a[84:87], v1, s[4:7], s98 offen offset:1024 sc1\n"
"ds_read_b64_tr_b16 v[72:73], v16 offset:16384\n"
"v_exp_f32_e32 v151, v151\n"
"s_waitcnt lgkmcnt(3)\n"
"v_mfma_f32_16x16x32_bf16 v[44:47], v[88:91], v[152:155], v[44:47]\n"
"buffer_load_dwordx4 a[88:91], v1, s[4:7], s98 offen offset:2048 sc1\n"
"v_cvt_pk_bf16_f32 v132, v64, v65\n"
"v_cvt_pk_bf16_f32 v133, v66, v67\n"
"v_cvt_pk_bf16_f32 v134, v148, v149\n"
"v_cvt_pk_bf16_f32 v135, v150, v151\n"
"v_mfma_f32_16x16x32_bf16 v[112:115], v[84:87], v[152:155], v[112:115]\n"
"ds_read_b64_tr_b16 v[74:75], v255 offset:16640\n"
"v_exp_f32_e32 v140, v140\n"
"v_exp_f32_e32 v141, v141\n"
"v_mfma_f32_16x16x32_bf16 v[136:139], v[40:43], v[152:155], v[136:139]\n"
"buffer_load_dword v241, v248, s[4:7], s97 offen offset:2048 sc1\n"
"v_exp_f32_e32 v142, v142\n"
"v_exp_f32_e32 v143, v143\n"
"v_mfma_f32_16x16x32_bf16 v[108:111], v[40:43], v[76:79], v[108:111]\n"
"ds_read_b64_tr_b16 v[40:41], v255 offset:16384\n"
"v_exp_f32_e32 v144, v144\n"
"v_exp_f32_e32 v145, v145\n"
"v_mfma_f32_16x16x32_bf16 v[80:83], v[84:87], v[76:79], v[80:83]\n"
"buffer_load_dword v240, v248, s[4:7], s97 offen offset:3072 sc1\n"
"ds_read_b64_tr_b16 v[42:43], v16 offset:16640\n"
"v_exp_f32_e32 v146, v146\n"
"v_exp_f32_e32 v147, v147\n"
"v_mfma_f32_16x16x32_bf16 v[68:71], v[88:91], v[76:79], v[68:71]\n"
"s_waitcnt lgkmcnt(4)\n"
"v_cvt_pk_bf16_f32 v64, v140, v141\n"
"v_cvt_pk_bf16_f32 v65, v142, v143\n"
"v_cvt_pk_bf16_f32 v66, v144, v145\n"
"v_cvt_pk_bf16_f32 v67, v146, v147\n"
"s_cmp_eq_i32 s78, 7\n"
"s_cbranch_scc1 .Lmk_x19s\n"
".Lmk_x19r:\n"
"v_mfma_f32_16x16x32_bf16 v[76:79], v[116:119], v[28:31], v[24:27]\n"
"ds_read_b128 v[84:87], v15 offset:10240\n"
"v_exp_f32_e32 v100, v100\n"
"v_exp_f32_e32 v101, v101\n"
"v_exp_f32_e32 v102, v102\n"
"v_mfma_f32_16x16x32_bf16 v[140:143], v[116:119], v[32:35], v[36:39]\n"
"buffer_load_dwordx4 a[92:95], v1, s[4:7], s98 offen offset:3072 sc1\n"
"v_exp_f32_e32 v103, v103\n"
"v_mfma_f32_16x16x32_bf16 v[116:119], v[104:107], v[32:35], v[36:39]\n"
"ds_read_b128 v[144:147], v254 offset:10496\n"
"v_exp_f32_e32 v128, v128\n"
"v_exp_f32_e32 v129, v129\n"
"v_mfma_f32_16x16x32_bf16 v[148:151], v[104:107], v[28:31], v[24:27]\n"
"ds_read_b64_tr_b16 v[104:105], v16 offset:14336\n"
"v_exp_f32_e32 v130, v130\n"
"v_exp_f32_e32 v131, v131\n"
"v_cvt_pk_bf16_f32 v152, v100, v101\n"
"s_waitcnt lgkmcnt(3)\n"
"v_mfma_f32_16x16x32_bf16 v[44:47], v[88:91], v[132:135], v[44:47]\n"
"s_add_u32 s98, s69, 0x606000\n"
"buffer_load_dwordx4 a[96:99], v0, s[4:7], s98 offen sc1\n"
"v_cvt_pk_bf16_f32 v153, v102, v103\n"
"v_cvt_pk_bf16_f32 v154, v128, v129\n"
"v_mfma_f32_16x16x32_bf16 v[112:115], v[72:75], v[132:135], v[112:115]\n"
"ds_read_b64_tr_b16 v[106:107], v255 offset:14592\n"
"v_cvt_pk_bf16_f32 v155, v130, v131\n"
"v_exp_f32_e32 v120, v120\n"
"v_exp_f32_e32 v121, v121\n"
"v_mfma_f32_16x16x32_bf16 v[136:139], v[40:43], v[132:135], v[136:139]\n"
"ds_read_b64_tr_b16 v[100:101], v255 offset:14336\n"
"v_exp_f32_e32 v122, v122\n"
"v_exp_f32_e32 v123, v123\n"
"v_mfma_f32_16x16x32_bf16 v[108:111], v[40:43], v[64:67], v[108:111]\n"
"buffer_load_dwordx4 a[100:103], v0, s[4:7], s98 offen offset:1024 sc1\n"
"v_exp_f32_e32 v124, v124\n"
"v_exp_f32_e32 v125, v125\n"
"v_mfma_f32_16x16x32_bf16 v[80:83], v[72:75], v[64:67], v[80:83]\n"
"ds_read_b64_tr_b16 v[102:103], v16 offset:14592\n"
"v_exp_f32_e32 v126, v126\n"
"v_exp_f32_e32 v127, v127\n"
"v_mfma_f32_16x16x32_bf16 v[68:71], v[88:91], v[64:67], v[68:71]\n"
"s_waitcnt lgkmcnt(4)\n"
"v_cvt_pk_bf16_f32 v40, v120, v121\n"
"v_cvt_pk_bf16_f32 v41, v122, v123\n"
"v_cvt_pk_bf16_f32 v42, v124, v125\n"
"v_cvt_pk_bf16_f32 v43, v126, v127\n"
"s_cmp_eq_i32 s78, 6\n"
"s_cbranch_scc1 .Lmk_x20s\n"
".Lmk_x20r:\n"
"v_mfma_f32_16x16x32_bf16 v[64:67], v[84:87], v[28:31], v[24:27]\n"
"ds_read_b128 v[72:75], v15 offset:8192\n"
"v_exp_f32_e32 v76, v76\n"
"v_exp_f32_e32 v77, v77\n"
"v_exp_f32_e32 v78, v78\n"
"v_mfma_f32_16x16x32_bf16 v[120:123], v[84:87], v[32:35], v[36:39]\n"
"buffer_load_dwordx4 a[104:107], v0, s[4:7], s98 offen offset:2048 sc1\n"
"v_exp_f32_e32 v79, v79\n"
"v_mfma_f32_16x16x32_bf16 v[84:87], v[144:147], v[32:35], v[36:39]\n"
"ds_read_b128 v[124:127], v254 offset:8448\n"
"v_exp_f32_e32 v148, v148\n"
"v_exp_f32_e32 v149, v149\n"
"v_mfma_f32_16x16x32_bf16 v[128:131], v[144:147], v[28:31], v[24:27]\n"
"ds_read_b64_tr_b16 v[132:133], v16 offset:12288\n"
"v_exp_f32_e32 v150, v150\n"
"v_exp_f32_e32 v151, v151\n"
"s_waitcnt lgkmcnt(3)\n"
"v_mfma_f32_16x16x32_bf16 v[44:47], v[88:91], v[152:155], v[44:47]\n"
"buffer_load_dwordx4 a[108:111], v0, s[4:7], s98 offen offset:3072 sc1\n"
"v_cvt_pk_bf16_f32 v144, v76, v77\n"
"v_cvt_pk_bf16_f32 v145, v78, v79\n"
"v_cvt_pk_bf16_f32 v146, v148, v149\n"
"v_mfma_f32_16x16x32_bf16 v[112:115], v[104:107], v[152:155], v[112:115]\n"
"ds_read_b64_tr_b16 v[134:135], v255 offset:12544\n"
"v_cvt_pk_bf16_f32 v147, v150, v151\n"
"v_exp_f32_e32 v140, v140\n"
"v_exp_f32_e32 v141, v141\n"
"v_mfma_f32_16x16x32_bf16 v[136:139], v[100:103], v[152:155], v[136:139]\n"
"ds_read_b64_tr_b16 v[76:77], v255 offset:12288\n"
"v_exp_f32_e32 v142, v142\n"
"v_exp_f32_e32 v143, v143\n"
"v_mfma_f32_16x16x32_bf16 v[108:111], v[100:103], v[40:43], v[108:111]\n"
"buffer_load_dwordx4 a[112:115], v1, s[4:7], s98 offen sc1\n"
"v_exp_f32_e32 v116, v116\n"
"v_exp_f32_e32 v117, v117\n"
"v_mfma_f32_16x16x32_bf16 v[80:83], v[104:107], v[40:43], v[80:83]\n"
"ds_read_b64_tr_b16 v[78:79], v16 offset:12544\n"
"v_exp_f32_e32 v118, v118\n"
"v_exp_f32_e32 v119, v119\n"
"v_mfma_f32_16x16x32_bf16 v[68:71], v[88:91], v[40:43], v[68:71]\n"
"s_waitcnt lgkmcnt(4)\n"
"v_cvt_pk_bf16_f32 v40, v140, v141\n"
"v_cvt_pk_bf16_f32 v41, v142, v143\n"
"v_cvt_pk_bf16_f32 v42, v116, v117\n"
"v_cvt_pk_bf16_f32 v43, v118, v119\n"
"s_cmp_eq_i32 s78, 5\n"
"s_cbranch_scc1 .Lmk_x21s\n"
".Lmk_x21r:\n"
"v_mfma_f32_16x16x32_bf16 v[100:103], v[72:75], v[28:31], v[24:27]\n"
"ds_read_b64_tr_b16 v[104:105], v16 offset:10240\n"
"ds_read_b64_tr_b16 v[106:107], v255 offset:10496\n"
"v_exp_f32_e32 v64, v64\n"
"v_exp_f32_e32 v65, v65\n"
"v_mfma_f32_16x16x32_bf16 v[116:119], v[72:75], v[32:35], v[36:39]\n"
"s_add_u32 s97, s71, 0xfffff000\n"
"buffer_load_dwordx2 v[238:239], off, s[4:7], s97 sc1\n"
"v_exp_f32_e32 v66, v66\n"
"v_exp_f32_e32 v67, v67\n"
"v_mfma_f32_16x16x32_bf16 v[72:75], v[124:127], v[32:35], v[36:39]\n"
"ds_read_b64_tr_b16 v[36:37], v255 offset:10240\n"
"v_exp_f32_e32 v128, v128\n"
"v_exp_f32_e32 v129, v129\n"
"v_mfma_f32_16x16x32_bf16 v[140:143], v[124:127], v[28:31], v[24:27]\n"
"ds_read_b64_tr_b16 v[38:39], v16 offset:10496\n"
"buffer_load_dwordx4 a[116:119], v1, s[4:7], s98 offen offset:1024 sc1\n"
"v_exp_f32_e32 v130, v130\n"
"v_exp_f32_e32 v131, v131\n"
"s_waitcnt lgkmcnt(4)\n"
"v_mfma_f32_16x16x32_bf16 v[44:47], v[88:91], v[144:147], v[44:47]\n"
"ds_read_b64_tr_b16 v[124:125], v16 offset:24576\n"
"ds_read_b64_tr_b16 v[126:127], v255 offset:24832\n"
"v_cvt_pk_bf16_f32 v148, v64, v65\n"
"v_cvt_pk_bf16_f32 v149, v66, v67\n"
"v_cvt_pk_bf16_f32 v150, v128, v129\n"
"v_mfma_f32_16x16x32_bf16 v[112:115], v[132:135], v[144:147], v[112:115]\n"
"ds_read_b64_tr_b16 v[64:65], v255 offset:24576\n"
"buffer_load_dwordx4 a[120:123], v1, s[4:7], s98 offen offset:2048 sc1\n"
"v_cvt_pk_bf16_f32 v151, v130, v131\n"
"v_exp_f32_e32 v120, v120\n"
"v_mfma_f32_16x16x32_bf16 v[136:139], v[76:79], v[144:147], v[136:139]\n"
"ds_read_b64_tr_b16 v[66:67], v16 offset:24832\n"
"v_exp_f32_e32 v121, v121\n"
"v_exp_f32_e32 v122, v122\n"
"v_exp_f32_e32 v123, v123\n"
"v_mfma_f32_16x16x32_bf16 v[108:111], v[76:79], v[40:43], v[108:111]\n"
"v_exp_f32_e32 v84, v84\n"
"v_add_u32_e32 v248, 0x26400, v0\n"
"v_exp_f32_e32 v85, v85\n"
"v_mfma_f32_16x16x32_bf16 v[80:83], v[132:135], v[40:43], v[80:83]\n"
"buffer_load_dwordx4 a[124:127], v1, s[4:7], s98 offen offset:3072 sc1\n"
"ds_read_b128 v[76:79], v248\n"
"v_exp_f32_e32 v86, v86\n"
"v_exp_f32_e32 v87, v87\n"
"v_mfma_f32_16x16x32_bf16 v[68:71], v[88:91], v[40:43], v[68:71]\n"
"ds_read_b128 v[128:131], v248 offset:1024\n"
"v_cvt_pk_bf16_f32 v40, v120, v121\n"
"v_cvt_pk_bf16_f32 v41, v122, v123\n"
"v_cvt_pk_bf16_f32 v42, v84, v85\n"
"v_cvt_pk_bf16_f32 v43, v86, v87\n"
"s_waitcnt lgkmcnt(6)\n"
"v_mfma_f32_16x16x32_bf16 v[44:47], v[88:91], v[148:151], v[44:47]\n"
"ds_read_b64_tr_b16 v[84:85], v16 offset:8192\n"
"s_add_u32 s98, s69, 0x608000\n"
"buffer_load_dwordx4 v[24:27], v0, s[4:7], s98 offen sc1\n"
"ds_read_b64_tr_b16 v[86:87], v255 offset:8448\n"
"v_cndmask_b32_e64 v100, v48, v100, s[20:21]\n"
"v_cndmask_b32_e64 v101, v49, v101, s[22:23]\n"
"v_cndmask_b32_e64 v102, v50, v102, s[24:25]\n"
"v_cndmask_b32_e64 v103, v51, v103, s[26:27]\n"
"v_cndmask_b32_e64 v140, v60, v140, s[28:29]\n"
"v_cndmask_b32_e64 v141, v61, v141, s[30:31]\n"
"v_cndmask_b32_e64 v142, v62, v142, s[32:33]\n"
"v_mfma_f32_16x16x32_bf16 v[112:115], v[104:107], v[148:151], v[112:115]\n"
"ds_read_b64_tr_b16 v[48:49], v255 offset:8192\n"
"ds_read_b64_tr_b16 v[50:51], v16 offset:8448\n"
"v_cndmask_b32_e64 v143, v63, v143, s[34:35]\n"
"v_exp_f32_e32 v100, v100\n"
"v_exp_f32_e32 v101, v101\n"
"v_exp_f32_e32 v102, v102\n"
"s_waitcnt vmcnt(11)\n"
"v_exp_f32_e32 v103, v103\n"
"v_exp_f32_e32 v140, v140\n"
"v_mfma_f32_16x16x32_bf16 v[136:139], v[36:39], v[148:151], v[136:139]\n"
"v_add_f32_e32 v247, v247, v246\n"
"v_exp_f32_e32 v141, v141\n"
"v_exp_f32_e32 v142, v142\n"
"v_add_f32_e32 v247, v247, v245\n"
"v_exp_f32_e32 v143, v143\n"
"v_cvt_pk_bf16_f32 v60, v100, v101\n"
"v_add_f32_e32 v247, v247, v244\n"
"v_cvt_pk_bf16_f32 v61, v102, v103\n"
"v_cvt_pk_bf16_f32 v62, v140, v141\n"
"v_mfma_f32_16x16x32_bf16 v[108:111], v[36:39], v[40:43], v[108:111]\n"
"v_cvt_pk_bf16_f32 v63, v142, v143\n"
"v_add_f32_e32 v247, v247, v243\n"
"v_cndmask_b32_e64 v116, v52, v116, s[36:37]\n"
"v_cndmask_b32_e64 v117, v53, v117, s[38:39]\n"
"v_cndmask_b32_e64 v118, v54, v118, s[40:41]\n"
"v_cndmask_b32_e64 v119, v55, v119, s[42:43]\n"
"v_add_f32_e32 v247, v247, v242\n"
"v_cndmask_b32_e64 v72, v56, v72, s[44:45]\n"
"v_cndmask_b32_e64 v73, v57, v73, s[46:47]\n"
"v_cndmask_b32_e64 v74, v58, v74, s[48:49]\n"
"v_add_f32_e32 v247, v247, v241\n"
"v_cndmask_b32_e64 v75, v59, v75, s[50:51]\n"
"v_exp_f32_e32 v116, v116\n"
"v_mfma_f32_16x16x32_bf16 v[80:83], v[104:107], v[40:43], v[80:83]\n"
"v_exp_f32_e32 v117, v117\n"
"v_add_f32_e32 v247, v247, v240\n"
"v_exp_f32_e32 v118, v118\n"
"v_mov_b32_e32 v246, v247\n"
"v_exp_f32_e32 v119, v119\n"
"v_exp_f32_e32 v72, v72\n"
"v_permlane32_swap_b32_e32 v247, v246\n"
"v_exp_f32_e32 v73, v73\n"
"v_mfma_f32_16x16x32_bf16 v[68:71], v[88:91], v[40:43], v[68:71]\n"
"v_exp_f32_e32 v74, v74\n"
"v_add_f32_e32 v247, v247, v246\n"
"v_exp_f32_e32 v75, v75\n"
"v_cvt_pk_bf16_f32 v36, v116, v117\n"
"ds_write_b32 v21, v247 offset:512\n"
"v_cvt_pk_bf16_f32 v37, v118, v119\n"
"v_cvt_pk_bf16_f32 v38, v72, v73\n"
"v_cvt_pk_bf16_f32 v39, v74, v75\n"
"s_waitcnt lgkmcnt(1)\n"
"v_mfma_f32_16x16x32_bf16 v[44:47], v[88:91], v[60:63], v[44:47]\n"
"v_and_b32_e32 v76, v60, v76\n"
"v_and_b32_e32 v77, v61, v77\n"
"v_and_b32_e32 v78, v62, v78\n"
"v_and_b32_e32 v79, v63, v79\n"
"v_mfma_f32_16x16x32_bf16 v[68:71], v[88:91], v[36:39], v[68:71]\n"
"v_xor_b32_e32 v40, v60, v76\n"
"v_xor_b32_e32 v41, v61, v77\n"
"v_xor_b32_e32 v42, v62, v78\n"
"v_xor_b32_e32 v43, v63, v79\n"
"v_mfma_f32_16x16x32_bf16 v[112:115], v[84:87], v[76:79], v[112:115]\n"
"v_and_b32_e32 v128, v36, v128\n"
"v_and_b32_e32 v129, v37, v129\n"
"v_mfma_f32_16x16x32_bf16 v[136:139], v[48:51], v[76:79], v[136:139]\n"
"v_and_b32_e32 v130, v38, v130\n"
"v_and_b32_e32 v131, v39, v131\n"
"v_mfma_f32_16x16x32_bf16 v[112:115], v[124:127], v[40:43], v[112:115]\n"
"v_xor_b32_e32 v52, v36, v128\n"
"v_xor_b32_e32 v53, v37, v129\n"
"v_mfma_f32_16x16x32_bf16 v[136:139], v[64:67], v[40:43], v[136:139]\n"
"v_xor_b32_e32 v54, v38, v130\n"
"v_xor_b32_e32 v55, v39, v131\n"
"v_mfma_f32_16x16x32_bf16 v[80:83], v[84:87], v[128:131], v[80:83]\n"
"s_waitcnt vmcnt(4)\n"
"v_cmp_ne_u64_e32 vcc, v[238:239], v[18:19]\n"
"s_nop 0\n"
"s_cbranch_vccnz .Lmk_f14s\n"
".Lmk_f14r:\n"
"v_mfma_f32_16x16x32_bf16 v[108:111], v[48:51], v[128:131], v[108:111]\n"
"s_add_u32 m0, s77, 0x4000\n"
"s_add_u32 s97, s65, 0\n"
"buffer_load_dwordx4 v2, s[4:7], s97 offen sc1 lds\n"
"buffer_load_dwordx4 v2, s[4:7], s97 offen offset:1024 sc1 lds\n"
"v_mfma_f32_16x16x32_bf16 v[80:83], v[124:127], v[52:55], v[80:83]\n"
"s_add_u32 m0, s77, 0xc000\n"
"s_add_u32 s97, s65, 0x4000\n"
"buffer_load_dwordx4 v2, s[4:7], s97 offen sc1 lds\n"
"buffer_load_dwordx4 v2, s[4:7], s97 offen offset:1024 sc1 lds\n"
"v_mfma_f32_16x16x32_bf16 v[108:111], v[64:67], v[52:55], v[108:111]\n"
"v_max_f32_e32 v248, v44, v68\n"
"v_rcp_f32_e32 v44, v44\n"
"v_rcp_f32_e32 v68, v68\n"
"s_nop 0\n"
"v_max3_f32 v248, v248, v44, v68\n"
"v_cmp_nlt_f32_e32 vcc, v248, v252\n"
"s_cbranch_vccnz .Lmk_b16\n"
".Lmk_k22:\n"
"s_add_u32 s97, s60, 0x40000\n"
"v_mul_f32_e32 v112, v112, v44\n"
"v_mul_f32_e32 v113, v113, v44\n"
"v_mul_f32_e32 v114, v114, v44\n"
"v_mul_f32_e32 v115, v115, v44\n"
"v_mul_f32_e32 v136, v136, v44\n"
"v_mul_f32_e32 v137, v137, v44\n"
"v_mul_f32_e32 v138, v138, v44\n"
"v_mul_f32_e32 v139, v139, v44\n"
"v_cvt_pk_bf16_f32 v28, v112, v113\n"
"v_cvt_pk_bf16_f32 v29, v114, v115\n"
"v_cvt_pk_bf16_f32 v30, v136, v137\n"
"v_cvt_pk_bf16_f32 v31, v138, v139\n"
"buffer_store_dwordx4 v[28:31], v0, s[4:7], s97 offen sc1\n"
"s_add_u32 s97, s60, 0x48000\n"
"v_mul_f32_e32 v80, v80, v68\n"
"v_mul_f32_e32 v81, v81, v68\n"
"v_mul_f32_e32 v82, v82, v68\n"
"v_mul_f32_e32 v83, v83, v68\n"
"v_mul_f32_e32 v108, v108, v68\n"
"v_mul_f32_e32 v109, v109, v68\n"
"v_mul_f32_e32 v110, v110, v68\n"
"v_mul_f32_e32 v111, v111, v68\n"
"v_cvt_pk_bf16_f32 v28, v80, v81\n"
"v_cvt_pk_bf16_f32 v29, v82, v83\n"
"v_cvt_pk_bf16_f32 v30, v108, v109\n"
"v_cvt_pk_bf16_f32 v31, v110, v111\n"
"buffer_store_dwordx4 v[28:31], v0, s[4:7], s97 offen sc1\n"
";\n"
"; @A AT a\n"
"s_mov_b32 s2, 0\n"
"v_mov_b32_e32 v253, 0xff800000\n"
"v_mov_b32_e32 v251, 0xff800000\n"
"v_mov_b32_e32 v250, 0x7f800000\n"
"v_mov_b32_e32 v249, 0x7f800000\n"
".Lmk_a23:\n"
"ds_read_b128 v[28:31], v15 offset:16384\n"
"ds_read_b128 v[32:35], v254 offset:16640\n"
"ds_read_b128 v[36:39], v15 offset:14336\n"
"ds_read_b128 v[40:43], v254 offset:14592\n"
"s_cmp_gt_i32 s78, 7\n"
"s_cselect_b32 s97, 0x80000000, 0\n"
"s_or_b32 s97, s97, 0x7f800000\n"
"s_add_u32 s96, s67, 32\n"
"buffer_load_dwordx2 v[246:247], v12, s[4:7], s96 offen sc1\n"
"s_waitcnt lgkmcnt(2)\n"
"v_mfma_f32_16x16x32_bf16 v[100:103], v[28:31], v[92:95], 0\n"
"v_mfma_f32_16x16x32_bf16 v[104:107], v[28:31], v[96:99], 0\n"
"v_mfma_f32_16x16x32_bf16 v[108:111], v[32:35], v[96:99], 0\n"
"v_mfma_f32_16x16x32_bf16 v[112:115], v[32:35], v[92:95], 0\n"
"s_waitcnt lgkmcnt(0)\n"
"s_nop 3\n"
"v_max_f32_e32 v248, v100, v101\n"
"v_max_f32_e32 v245, v104, v105\n"
"v_max3_f32 v248, v248, v102, v103\n"
"v_max3_f32 v245, v245, v106, v107\n"
"v_max3_f32 v248, v248, v112, v113\n"
"v_max3_f32 v245, v245, v108, v109\n"
"v_max3_f32 v248, v248, v114, v115\n"
"v_max3_f32 v245, v245, v110, v111\n"
"v_mov_b32_e32 v244, v248\n"
"v_mov_b32_e32 v243, v245\n"
"s_nop 0\n"
"v_permlane16_swap_b32_e32 v248, v244\n"
"v_permlane16_swap_b32_e32 v245, v243\n"
"v_max_f32_e32 v248, v248, v244\n"
"v_max_f32_e32 v245, v245, v243\n"
"v_mov_b32_e32 v244, v248\n"
"v_mov_b32_e32 v243, v245\n"
"s_nop 0\n"
"v_permlane32_swap_b32_e32 v248, v244\n"
"v_permlane32_swap_b32_e32 v245, v243\n"
"v_max_f32_e32 v248, v248, v244\n"
"v_max_f32_e32 v245, v245, v243\n"
"v_med3_f32 v248, v248, v253, v250\n"
"v_med3_f32 v245, v245, v251, v249\n"
"v_min_f32_e64 v116, s97, -v248\n"
"v_min_f32_e64 v117, s97, -v248\n"
"v_min_f32_e64 v118, s97, -v248\n"
"v_min_f32_e64 v119, s97, -v248\n"
"v_min_f32_e64 v120, s97, -v245\n"
"v_min_f32_e64 v121, s97, -v245\n"
"v_min_f32_e64 v122, s97, -v245\n"
"v_min_f32_e64 v123, s97, -v245\n"
"v_mfma_f32_16x16x32_bf16 v[44:47], v[36:39], v[92:95], v[116:119]\n"
"ds_read_b128 v[32:35], v15 offset:12288\n"
"v_sub_f32_e32 v100, v100, v248\n"
"v_sub_f32_e32 v101, v101, v248\n"
"v_sub_f32_e32 v102, v102, v248\n"
"v_sub_f32_e32 v103, v103, v248\n"
"v_mfma_f32_16x16x32_bf16 v[48:51], v[36:39], v[96:99], v[120:123]\n"
"v_sub_f32_e32 v112, v112, v248\n"
"v_sub_f32_e32 v113, v113, v248\n"
"v_sub_f32_e32 v114, v114, v248\n"
"v_sub_f32_e32 v115, v115, v248\n"
"v_mfma_f32_16x16x32_bf16 v[52:55], v[40:43], v[96:99], v[120:123]\n"
"ds_read_b128 v[56:59], v254 offset:12544\n"
"v_sub_f32_e32 v104, v104, v245\n"
"v_sub_f32_e32 v105, v105, v245\n"
"v_sub_f32_e32 v106, v106, v245\n"
"v_sub_f32_e32 v107, v107, v245\n"
"v_mfma_f32_16x16x32_bf16 v[36:39], v[40:43], v[92:95], v[116:119]\n"
"s_waitcnt lgkmcnt(0)\n"
"v_sub_f32_e32 v108, v108, v245\n"
"v_sub_f32_e32 v109, v109, v245\n"
"v_sub_f32_e32 v110, v110, v245\n"
"v_sub_f32_e32 v111, v111, v245\n"
"s_cmp_eq_i32 s78, 7\n"
"s_cbranch_scc1 .Lmk_x25s\n"
".Lmk_x25r:\n"
"v_mfma_f32_16x16x32_bf16 v[60:63], v[32:35], v[92:95], v[116:119]\n"
"ds_read_b128 v[40:43], v15 offset:10240\n"
"buffer_load_dwordx4 v[28:31], v0, s[4:7], s98 offen offset:1024 sc1\n"
"ds_read_b128 v[64:67], v254 offset:10496\n"
"v_exp_f32_e32 v44, v44\n"
"v_exp_f32_e32 v45, v45\n"
"v_exp_f32_e32 v46, v46\n"
"v_exp_f32_e32 v47, v47\n"
"v_exp_f32_e32 v36, v36\n"
"v_mfma_f32_16x16x32_bf16 v[68:71], v[32:35], v[96:99], v[120:123]\n"
"ds_read_b64_tr_b16 v[72:73], v16 offset:14336\n"
"buffer_load_dwordx4 v[32:35], v0, s[4:7], s98 offen offset:2048 sc1\n"
"v_exp_f32_e32 v37, v37\n"
"v_exp_f32_e32 v38, v38\n"
"v_exp_f32_e32 v39, v39\n"
"v_cvt_pk_bf16_f32 v76, v44, v45\n"
"v_cvt_pk_bf16_f32 v77, v46, v47\n"
"v_cvt_pk_bf16_f32 v78, v36, v37\n"
"v_mfma_f32_16x16x32_bf16 v[80:83], v[56:59], v[96:99], v[120:123]\n"
"ds_read_b64_tr_b16 v[74:75], v255 offset:14592\n"
"ds_read_b64_tr_b16 v[84:85], v255 offset:14336\n"
"v_cvt_pk_bf16_f32 v79, v38, v39\n"
"v_exp_f32_e32 v48, v48\n"
"v_exp_f32_e32 v49, v49\n"
"v_exp_f32_e32 v50, v50\n"
"v_exp_f32_e32 v51, v51\n"
"v_mfma_f32_16x16x32_bf16 v[124:127], v[56:59], v[92:95], v[116:119]\n"
"buffer_load_dwordx4 v[36:39], v0, s[4:7], s98 offen offset:3072 sc1\n"
"ds_read_b64_tr_b16 v[86:87], v16 offset:14592\n"
"v_exp_f32_e32 v52, v52\n"
"v_exp_f32_e32 v53, v53\n"
"v_exp_f32_e32 v54, v54\n"
"v_exp_f32_e32 v55, v55\n"
"v_cvt_pk_bf16_f32 v56, v48, v49\n"
"v_cvt_pk_bf16_f32 v57, v50, v51\n"
"v_cvt_pk_bf16_f32 v58, v52, v53\n"
"v_cvt_pk_bf16_f32 v59, v54, v55\n"
"s_waitcnt lgkmcnt(4)\n"
"s_cmp_eq_i32 s78, 6\n"
"s_cbranch_scc1 .Lmk_x26s\n"
".Lmk_x26r:\n"
"v_mfma_f32_16x16x32_bf16 v[128:131], v[40:43], v[92:95], v[116:119]\n"
"ds_read_b128 v[52:55], v15 offset:8192\n"
"v_exp_f32_e32 v60, v60\n"
"v_exp_f32_e32 v61, v61\n"
"v_exp_f32_e32 v62, v62\n"
"v_mfma_f32_16x16x32_bf16 v[132:135], v[40:43], v[96:99], v[120:123]\n"
"buffer_load_dwordx4 v[40:43], v1, s[4:7], s98 offen sc1\n"
"v_exp_f32_e32 v63, v63\n"
"v_mfma_f32_16x16x32_bf16 v[136:139], v[64:67], v[96:99], v[120:123]\n"
"ds_read_b128 v[140:143], v254 offset:8448\n"
"v_exp_f32_e32 v124, v124\n"
"v_exp_f32_e32 v125, v125\n"
"v_mfma_f32_16x16x32_bf16 v[144:147], v[64:67], v[92:95], v[116:119]\n"
"ds_read_b64_tr_b16 v[64:65], v16 offset:12288\n"
"v_exp_f32_e32 v126, v126\n"
"v_exp_f32_e32 v127, v127\n"
"s_waitcnt lgkmcnt(3)\n"
"v_mfma_f32_16x16x32_bf16 v[148:151], v[88:91], v[76:79], 0\n"
"buffer_load_dwordx4 v[44:47], v1, s[4:7], s98 offen offset:1024 sc1\n"
"v_cvt_pk_bf16_f32 v152, v60, v61\n"
"v_cvt_pk_bf16_f32 v153, v62, v63\n"
"v_cvt_pk_bf16_f32 v154, v124, v125\n"
"v_mfma_f32_16x16x32_bf16 v[156:159], v[72:75], v[76:79], 0\n"
"ds_read_b64_tr_b16 v[66:67], v255 offset:12544\n"
"v_cvt_pk_bf16_f32 v155, v126, v127\n"
"v_exp_f32_e32 v68, v68\n"
"v_exp_f32_e32 v69, v69\n"
"v_mfma_f32_16x16x32_bf16 v[124:127], v[84:87], v[76:79], 0\n"
"ds_read_b64_tr_b16 v[60:61], v255 offset:12288\n"
"v_exp_f32_e32 v70, v70\n"
"v_exp_f32_e32 v71, v71\n"
"v_mfma_f32_16x16x32_bf16 v[160:163], v[84:87], v[56:59], 0\n"
"buffer_load_dwordx4 v[48:51], v1, s[4:7], s98 offen offset:2048 sc1\n"
"v_exp_f32_e32 v80, v80\n"
"v_exp_f32_e32 v81, v81\n"
"v_mfma_f32_16x16x32_bf16 v[164:167], v[72:75], v[56:59], 0\n"
"ds_read_b64_tr_b16 v[62:63], v16 offset:12544\n"
"v_exp_f32_e32 v82, v82\n"
"v_exp_f32_e32 v83, v83\n"
"v_mfma_f32_16x16x32_bf16 v[168:171], v[88:91], v[56:59], 0\n"
"s_waitcnt lgkmcnt(4)\n"
"v_cvt_pk_bf16_f32 v72, v68, v69\n"
"v_cvt_pk_bf16_f32 v73, v70, v71\n"
"v_cvt_pk_bf16_f32 v74, v80, v81\n"
"v_cvt_pk_bf16_f32 v75, v82, v83\n"
"s_cmp_eq_i32 s78, 5\n"
"s_cbranch_scc1 .Lmk_x27s\n"
".Lmk_x27r:\n"
"v_mfma_f32_16x16x32_bf16 v[76:79], v[52:55], v[92:95], v[116:119]\n"
"ds_read_b64_tr_b16 v[68:69], v16 offset:10240\n"
"v_exp_f32_e32 v128, v128\n"
"v_exp_f32_e32 v129, v129\n"
"v_exp_f32_e32 v130, v130\n"
"v_mfma_f32_16x16x32_bf16 v[80:83], v[52:55], v[96:99], v[120:123]\n"
"buffer_load_dwordx4 v[52:55], v1, s[4:7], s98 offen offset:3072 sc1\n"
"v_exp_f32_e32 v131, v131\n"
"v_mfma_f32_16x16x32_bf16 v[84:87], v[140:143], v[96:99], v[120:123]\n"
"ds_read_b64_tr_b16 v[70:71], v255 offset:10496\n"
"v_exp_f32_e32 v144, v144\n"
"v_exp_f32_e32 v145, v145\n"
"v_mfma_f32_16x16x32_bf16 v[172:175], v[140:143], v[92:95], v[116:119]\n"
"s_waitcnt lgkmcnt(2)\n"
"v_exp_f32_e32 v146, v146\n"
"v_exp_f32_e32 v147, v147\n"
"v_cvt_pk_bf16_f32 v140, v128, v129\n"
"v_mfma_f32_16x16x32_bf16 v[148:151], v[88:91], v[152:155], v[148:151]\n"
"s_add_u32 s97, s69, 0x60a000\n"
"buffer_load_dwordx4 v[56:59], v0, s[4:7], s97 offen sc1\n"
"v_cvt_pk_bf16_f32 v141, v130, v131\n"
"v_cvt_pk_bf16_f32 v142, v144, v145\n"
"v_mfma_f32_16x16x32_bf16 v[156:159], v[64:67], v[152:155], v[156:159]\n"
"ds_read_b64_tr_b16 v[128:129], v255 offset:10240\n"
"v_cvt_pk_bf16_f32 v143, v146, v147\n"
"v_exp_f32_e32 v132, v132\n"
"v_exp_f32_e32 v133, v133\n"
"v_mfma_f32_16x16x32_bf16 v[124:127], v[60:63], v[152:155], v[124:127]\n"
"v_exp_f32_e32 v134, v134\n"
"v_exp_f32_e32 v135, v135\n"
"v_mfma_f32_16x16x32_bf16 v[160:163], v[60:63], v[72:75], v[160:163]\n"
"buffer_load_dwordx4 v[60:63], v0, s[4:7], s97 offen offset:1024 sc1\n"
"ds_read_b64_tr_b16 v[130:131], v16 offset:10496\n"
"v_exp_f32_e32 v136, v136\n"
"v_mfma_f32_16x16x32_bf16 v[164:167], v[64:67], v[72:75], v[164:167]\n"
"v_exp_f32_e32 v137, v137\n"
"v_exp_f32_e32 v138, v138\n"
"v_exp_f32_e32 v139, v139\n"
"v_mfma_f32_16x16x32_bf16 v[168:171], v[88:91], v[72:75], v[168:171]\n"
"s_waitcnt vmcnt(12)\n"
"v_cvt_pk_bf16_f32 v72, v132, v133\n"
"v_cvt_pk_bf16_f32 v73, v134, v135\n"
"v_cvt_pk_bf16_f32 v74, v136, v137\n"
"v_cvt_pk_bf16_f32 v75, v138, v139\n"
"s_cmp_lg_u32 s2, 0\n"
"s_cbranch_scc1 .Lmk_s28\n"
"s_barrier\n"
".Lmk_s28:\n"
"ds_read_b128 v[132:135], v15 offset:6144\n"
"ds_read_b128 v[136:139], v254 offset:6400\n"
"s_waitcnt lgkmcnt(2)\n"
"v_mfma_f32_16x16x32_bf16 v[148:151], v[88:91], v[140:143], v[148:151]\n"
"ds_read_b128 v[144:147], v15 offset:4096\n"
"v_exp_f32_e32 v76, v76\n"
"v_exp_f32_e32 v77, v77\n"
"v_exp_f32_e32 v78, v78\n"
"v_mfma_f32_16x16x32_bf16 v[156:159], v[68:71], v[140:143], v[156:159]\n"
"buffer_load_dwordx4 v[64:67], v0, s[4:7], s97 offen offset:2048 sc1\n"
"ds_read_b128 v[152:155], v254 offset:4352\n"
"v_exp_f32_e32 v79, v79\n"
"v_mfma_f32_16x16x32_bf16 v[124:127], v[128:131], v[140:143], v[124:127]\n"
"ds_read_b64_tr_b16 v[140:141], v16 offset:8192\n"
"v_exp_f32_e32 v172, v172\n"
"v_exp_f32_e32 v173, v173\n"
"v_mfma_f32_16x16x32_bf16 v[160:163], v[128:131], v[72:75], v[160:163]\n"
"ds_read_b64_tr_b16 v[142:143], v255 offset:8448\n"
"v_exp_f32_e32 v174, v174\n"
"v_exp_f32_e32 v175, v175\n"
"v_mfma_f32_16x16x32_bf16 v[164:167], v[68:71], v[72:75], v[164:167]\n"
"buffer_load_dwordx4 v[68:71], v0, s[4:7], s97 offen offset:3072 sc1\n"
"v_cvt_pk_bf16_f32 v128, v76, v77\n"
"v_cvt_pk_bf16_f32 v129, v78, v79\n"
"v_cvt_pk_bf16_f32 v130, v172, v173\n"
"v_cvt_pk_bf16_f32 v131, v174, v175\n"
"v_mfma_f32_16x16x32_bf16 v[168:171], v[88:91], v[72:75], v[168:171]\n"
"ds_read_b64_tr_b16 v[172:173], v255 offset:8192\n"
"v_exp_f32_e32 v80, v80\n"
"v_exp_f32_e32 v81, v81\n"
"s_waitcnt lgkmcnt(5)\n"
"v_mfma_f32_16x16x32_bf16 v[176:179], v[132:135], v[92:95], v[116:119]\n"
"ds_read_b64_tr_b16 v[174:175], v16 offset:8448\n"
"v_exp_f32_e32 v82, v82\n"
"v_exp_f32_e32 v83, v83\n"
"v_exp_f32_e32 v84, v84\n"
"v_mfma_f32_16x16x32_bf16 v[180:183], v[132:135], v[96:99], v[120:123]\n"
"buffer_load_dwordx4 v[72:75], v1, s[4:7], s97 offen sc1\n"
"s_waitcnt vmcnt(12)\n"
"v_cmp_ne_u64_e32 vcc, v[246:247], v[18:19]\n"
"v_mfma_f32_16x16x32_bf16 v[132:135], v[136:139], v[96:99], v[120:123]\n"
"v_exp_f32_e32 v85, v85\n"
"v_exp_f32_e32 v86, v86\n"
"v_exp_f32_e32 v87, v87\n"
"v_mfma_f32_16x16x32_bf16 v[184:187], v[136:139], v[92:95], v[116:119]\n"
"s_cbranch_vccnz .Lmk_f5s\n"
".Lmk_f5r:\n"
"v_cvt_pk_bf16_f32 v136, v80, v81\n"
"v_cvt_pk_bf16_f32 v137, v82, v83\n"
"v_cvt_pk_bf16_f32 v138, v84, v85\n"
"v_cvt_pk_bf16_f32 v139, v86, v87\n"
"s_waitcnt lgkmcnt(4)\n"
"v_mfma_f32_16x16x32_bf16 v[188:191], v[144:147], v[92:95], v[116:119]\n"
"ds_read_b128 v[192:195], v15 offset:2048\n"
"v_exp_f32_e32 v176, v176\n"
"v_exp_f32_e32 v177, v177\n"
"v_exp_f32_e32 v178, v178\n"
"v_mfma_f32_16x16x32_bf16 v[196:199], v[144:147], v[96:99], v[120:123]\n"
"buffer_load_dwordx4 v[76:79], v1, s[4:7], s97 offen offset:1024 sc1\n"
"v_exp_f32_e32 v179, v179\n"
"v_mfma_f32_16x16x32_bf16 v[144:147], v[152:155], v[96:99], v[120:123]\n"
"ds_read_b128 v[200:203], v254 offset:2304\n"
"v_exp_f32_e32 v184, v184\n"
"v_exp_f32_e32 v185, v185\n"
"v_mfma_f32_16x16x32_bf16 v[204:207], v[152:155], v[92:95], v[116:119]\n"
"ds_read_b64_tr_b16 v[152:153], v16 offset:6144\n"
"v_exp_f32_e32 v186, v186\n"
"v_exp_f32_e32 v187, v187\n"
"s_waitcnt lgkmcnt(3)\n"
"v_mfma_f32_16x16x32_bf16 v[148:151], v[88:91], v[128:131], v[148:151]\n"
"buffer_load_dwordx4 v[80:83], v1, s[4:7], s97 offen offset:2048 sc1\n"
"v_cvt_pk_bf16_f32 v208, v176, v177\n"
"v_cvt_pk_bf16_f32 v209, v178, v179\n"
"v_cvt_pk_bf16_f32 v210, v184, v185\n"
"v_mfma_f32_16x16x32_bf16 v[156:159], v[140:143], v[128:131], v[156:159]\n"
"ds_read_b64_tr_b16 v[154:155], v255 offset:6400\n"
"v_cvt_pk_bf16_f32 v211, v186, v187\n"
"v_exp_f32_e32 v180, v180\n"
"v_exp_f32_e32 v181, v181\n"
"v_mfma_f32_16x16x32_bf16 v[124:127], v[172:175], v[128:131], v[124:127]\n"
"ds_read_b64_tr_b16 v[128:129], v255 offset:6144\n"
"v_exp_f32_e32 v182, v182\n"
"v_exp_f32_e32 v183, v183\n"
"v_mfma_f32_16x16x32_bf16 v[160:163], v[172:175], v[136:139], v[160:163]\n"
"buffer_load_dwordx4 v[84:87], v1, s[4:7], s97 offen offset:3072 sc1\n"
"v_exp_f32_e32 v132, v132\n"
"v_exp_f32_e32 v133, v133\n"
"v_mfma_f32_16x16x32_bf16 v[164:167], v[140:143], v[136:139], v[164:167]\n"
"ds_read_b64_tr_b16 v[130:131], v16 offset:6400\n"
"v_exp_f32_e32 v134, v134\n"
"v_exp_f32_e32 v135, v135\n"
"v_mfma_f32_16x16x32_bf16 v[168:171], v[88:91], v[136:139], v[168:171]\n"
"s_waitcnt lgkmcnt(4)\n"
"v_cvt_pk_bf16_f32 v136, v180, v181\n"
"v_cvt_pk_bf16_f32 v137, v182, v183\n"
"v_cvt_pk_bf16_f32 v138, v132, v133\n"
"v_cvt_pk_bf16_f32 v139, v134, v135\n"
"v_mfma_f32_16x16x32_bf16 v[132:135], v[192:195], v[92:95], v[116:119]\n"
"ds_read_b128 v[140:143], v15\n"
"v_exp_f32_e32 v188, v188\n"
"v_exp_f32_e32 v189, v189\n"
"v_exp_f32_e32 v190, v190\n"
"v_mfma_f32_16x16x32_bf16 v[172:175], v[192:195], v[96:99], v[120:123]\n"
"s_add_u32 s97, s56, 0x40000\n"
"buffer_load_dwordx4 v[176:179], v0, s[4:7], s97 offen sc1\n"
"v_exp_f32_e32 v191, v191\n"
"v_mfma_f32_16x16x32_bf16 v[180:183], v[200:203], v[96:99], v[120:123]\n"
"ds_read_b128 v[184:187], v254 offset:256\n"
"v_exp_f32_e32 v204, v204\n"
"v_exp_f32_e32 v205, v205\n"
"v_exp_f32_e32 v206, v206\n"
"v_mfma_f32_16x16x32_bf16 v[192:195], v[200:203], v[92:95], v[116:119]\n"
"buffer_load_dwordx4 v[200:203], v0, s[4:7], s97 offen offset:1024 sc1\n"
"ds_read_b64_tr_b16 v[212:213], v16 offset:4096\n"
"v_exp_f32_e32 v207, v207\n"
"s_waitcnt lgkmcnt(3)\n"
"v_mfma_f32_16x16x32_bf16 v[148:151], v[88:91], v[208:211], v[148:151]\n"
"v_cvt_pk_bf16_f32 v216, v188, v189\n"
"v_cvt_pk_bf16_f32 v217, v190, v191\n"
"v_cvt_pk_bf16_f32 v218, v204, v205\n"
"v_cvt_pk_bf16_f32 v219, v206, v207\n"
"v_mfma_f32_16x16x32_bf16 v[156:159], v[152:155], v[208:211], v[156:159]\n"
"buffer_load_dwordx4 v[188:191], v0, s[4:7], s97 offen offset:2048 sc1\n"
"ds_read_b64_tr_b16 v[214:215], v255 offset:4352\n"
"v_exp_f32_e32 v196, v196\n"
"v_exp_f32_e32 v197, v197\n"
"v_mfma_f32_16x16x32_bf16 v[124:127], v[128:131], v[208:211], v[124:127]\n"
"buffer_load_dwordx4 v[204:207], v0, s[4:7], s97 offen offset:3072 sc1\n"
"v_exp_f32_e32 v198, v198\n"
"v_exp_f32_e32 v199, v199\n"
"v_mfma_f32_16x16x32_bf16 v[160:163], v[128:131], v[136:139], v[160:163]\n"
"ds_read_b64_tr_b16 v[128:129], v255 offset:4096\n"
"v_exp_f32_e32 v144, v144\n"
"v_exp_f32_e32 v145, v145\n"
"v_mfma_f32_16x16x32_bf16 v[164:167], v[152:155], v[136:139], v[164:167]\n"
"buffer_load_dwordx4 v[152:155], v1, s[4:7], s97 offen sc1\n"
"ds_read_b64_tr_b16 v[130:131], v16 offset:4352\n"
"v_exp_f32_e32 v146, v146\n"
"v_exp_f32_e32 v147, v147\n"
"v_mfma_f32_16x16x32_bf16 v[168:171], v[88:91], v[136:139], v[168:171]\n"
"s_waitcnt lgkmcnt(4)\n"
"v_cvt_pk_bf16_f32 v136, v196, v197\n"
"v_cvt_pk_bf16_f32 v137, v198, v199\n"
"v_cvt_pk_bf16_f32 v138, v144, v145\n"
"v_cvt_pk_bf16_f32 v139, v146, v147\n"
"v_mfma_f32_16x16x32_bf16 v[144:147], v[140:143], v[92:95], v[116:119]\n"
"ds_read_b64_tr_b16 v[196:197], v16 offset:2048\n"
"ds_read_b64_tr_b16 v[198:199], v255 offset:2304\n"
"buffer_load_dwordx4 v[208:211], v1, s[4:7], s97 offen offset:1024 sc1\n"
"v_exp_f32_e32 v132, v132\n"
"v_exp_f32_e32 v133, v133\n"
"v_mfma_f32_16x16x32_bf16 v[220:223], v[140:143], v[96:99], v[120:123]\n"
"ds_read_b64_tr_b16 v[140:141], v255 offset:2048\n"
"v_exp_f32_e32 v134, v134\n"
"v_exp_f32_e32 v135, v135\n"
"v_mfma_f32_16x16x32_bf16 v[224:227], v[184:187], v[96:99], v[120:123]\n"
"buffer_load_dwordx4 v[120:123], v1, s[4:7], s97 offen offset:2048 sc1\n"
"v_exp_f32_e32 v192, v192\n"
"v_exp_f32_e32 v193, v193\n"
"v_mfma_f32_16x16x32_bf16 v[228:231], v[184:187], v[92:95], v[116:119]\n"
"ds_read_b64_tr_b16 v[142:143], v16 offset:2304\n"
"ds_read_b64_tr_b16 v[116:117], v16 offset:16384\n"
"v_exp_f32_e32 v194, v194\n"
"v_exp_f32_e32 v195, v195\n"
"s_waitcnt lgkmcnt(5)\n"
"v_mfma_f32_16x16x32_bf16 v[148:151], v[88:91], v[216:219], v[148:151]\n"
"v_cvt_pk_bf16_f32 v184, v132, v133\n"
"v_cvt_pk_bf16_f32 v185, v134, v135\n"
"v_cvt_pk_bf16_f32 v186, v192, v193\n"
"v_mfma_f32_16x16x32_bf16 v[156:159], v[212:215], v[216:219], v[156:159]\n"
"buffer_load_dwordx4 v[132:135], v1, s[4:7], s97 offen offset:3072 sc1\n"
"ds_read_b64_tr_b16 v[118:119], v255 offset:16640\n"
"ds_read_b64_tr_b16 v[192:193], v255 offset:16384\n"
"v_cvt_pk_bf16_f32 v187, v194, v195\n"
"v_exp_f32_e32 v172, v172\n"
"v_exp_f32_e32 v173, v173\n"
"v_exp_f32_e32 v174, v174\n"
"v_mfma_f32_16x16x32_bf16 v[124:127], v[128:131], v[216:219], v[124:127]\n"
"s_add_u32 s97, s57, 0x40000\n"
"buffer_load_dwordx4 v[216:219], v0, s[4:7], s97 offen sc1\n"
"ds_read_b64_tr_b16 v[194:195], v16 offset:16640\n"
"v_exp_f32_e32 v175, v175\n"
"v_mfma_f32_16x16x32_bf16 v[160:163], v[128:131], v[136:139], v[160:163]\n"
"v_exp_f32_e32 v180, v180\n"
"v_add_u32_e32 v248, 0x26400, v0\n"
"v_exp_f32_e32 v181, v181\n"
"v_mfma_f32_16x16x32_bf16 v[164:167], v[212:215], v[136:139], v[164:167]\n"
"buffer_load_dwordx4 v[128:131], v0, s[4:7], s97 offen offset:1024 sc1\n"
"ds_read_b128 v[212:215], v248\n"
"v_exp_f32_e32 v182, v182\n"
"v_exp_f32_e32 v183, v183\n"
"v_mfma_f32_16x16x32_bf16 v[168:171], v[88:91], v[136:139], v[168:171]\n"
"ds_read_b128 v[232:235], v248 offset:1024\n"
"v_cvt_pk_bf16_f32 v136, v172, v173\n"
"v_cvt_pk_bf16_f32 v137, v174, v175\n"
"v_cvt_pk_bf16_f32 v138, v180, v181\n"
"v_cvt_pk_bf16_f32 v139, v182, v183\n"
"s_waitcnt lgkmcnt(6)\n"
"v_mfma_f32_16x16x32_bf16 v[148:151], v[88:91], v[184:187], v[148:151]\n"
"ds_read_b64_tr_b16 v[172:173], v16\n"
"buffer_load_dwordx4 v[180:183], v0, s[4:7], s97 offen offset:2048 sc1\n"
"v_cndmask_b32_e64 v144, v100, v144, s[20:21]\n"
"v_cndmask_b32_e64 v145, v101, v145, s[22:23]\n"
"v_cndmask_b32_e64 v146, v102, v146, s[24:25]\n"
"v_cndmask_b32_e64 v147, v103, v147, s[26:27]\n"
"v_cndmask_b32_e64 v228, v112, v228, s[28:29]\n"
"v_cndmask_b32_e64 v229, v113, v229, s[30:31]\n"
"v_cndmask_b32_e64 v230, v114, v230, s[32:33]\n"
"v_cndmask_b32_e64 v231, v115, v231, s[34:35]\n"
"v_exp_f32_e32 v144, v144\n"
"v_mfma_f32_16x16x32_bf16 v[156:159], v[196:199], v[184:187], v[156:159]\n"
"ds_read_b64_tr_b16 v[174:175], v255 offset:256\n"
"buffer_load_dwordx4 v[100:103], v0, s[4:7], s97 offen offset:3072 sc1\n"
"v_exp_f32_e32 v145, v145\n"
"v_exp_f32_e32 v146, v146\n"
"v_exp_f32_e32 v147, v147\n"
"v_exp_f32_e32 v228, v228\n"
"v_mfma_f32_16x16x32_bf16 v[124:127], v[140:143], v[184:187], v[124:127]\n"
"buffer_load_dwordx4 v[184:187], v1, s[4:7], s97 offen sc1\n"
"v_exp_f32_e32 v229, v229\n"
"v_exp_f32_e32 v230, v230\n"
"v_exp_f32_e32 v231, v231\n"
"v_cvt_pk_bf16_f32 v112, v144, v145\n"
"v_cvt_pk_bf16_f32 v113, v146, v147\n"
"v_cvt_pk_bf16_f32 v114, v228, v229\n"
"v_mfma_f32_16x16x32_bf16 v[160:163], v[140:143], v[136:139], v[160:163]\n"
"ds_read_b64_tr_b16 v[140:141], v255\n"
"v_cvt_pk_bf16_f32 v115, v230, v231\n"
"v_cndmask_b32_e64 v220, v104, v220, s[36:37]\n"
"v_cndmask_b32_e64 v221, v105, v221, s[38:39]\n"
"v_cndmask_b32_e64 v222, v106, v222, s[40:41]\n"
"v_cndmask_b32_e64 v223, v107, v223, s[42:43]\n"
"v_cndmask_b32_e64 v224, v108, v224, s[44:45]\n"
"v_cndmask_b32_e64 v225, v109, v225, s[46:47]\n"
"v_cndmask_b32_e64 v226, v110, v226, s[48:49]\n"
"v_cndmask_b32_e64 v227, v111, v227, s[50:51]\n"
"v_mfma_f32_16x16x32_bf16 v[164:167], v[196:199], v[136:139], v[164:167]\n"
"buffer_load_dwordx4 v[104:107], v1, s[4:7], s97 offen offset:1024 sc1\n"
"ds_read_b64_tr_b16 v[142:143], v16 offset:256\n"
"buffer_load_dwordx4 v[108:111], v1, s[4:7], s97 offen offset:2048 sc1\n"
"v_exp_f32_e32 v220, v220\n"
"v_exp_f32_e32 v221, v221\n"
"v_exp_f32_e32 v222, v222\n"
"v_exp_f32_e32 v223, v223\n"
"v_exp_f32_e32 v224, v224\n"
"v_mfma_f32_16x16x32_bf16 v[168:171], v[88:91], v[136:139], v[168:171]\n"
"s_waitcnt lgkmcnt(0)\n"
"v_exp_f32_e32 v225, v225\n"
"v_exp_f32_e32 v226, v226\n"
"v_exp_f32_e32 v227, v227\n"
"v_cvt_pk_bf16_f32 v136, v220, v221\n"
"v_cvt_pk_bf16_f32 v137, v222, v223\n"
"v_cvt_pk_bf16_f32 v138, v224, v225\n"
"v_cvt_pk_bf16_f32 v139, v226, v227\n"
"v_mfma_f32_16x16x32_bf16 v[148:151], v[88:91], v[112:115], v[148:151]\n"
"v_and_b32_e32 v212, v112, v212\n"
"v_and_b32_e32 v213, v113, v213\n"
"v_and_b32_e32 v214, v114, v214\n"
"v_and_b32_e32 v215, v115, v215\n"
"v_mfma_f32_16x16x32_bf16 v[168:171], v[88:91], v[136:139], v[168:171]\n"
"v_xor_b32_e32 v144, v112, v212\n"
"v_xor_b32_e32 v145, v113, v213\n"
"v_xor_b32_e32 v146, v114, v214\n"
"v_xor_b32_e32 v147, v115, v215\n"
"v_mfma_f32_16x16x32_bf16 v[156:159], v[172:175], v[212:215], v[156:159]\n"
"v_and_b32_e32 v232, v136, v232\n"
"v_and_b32_e32 v233, v137, v233\n"
"v_mfma_f32_16x16x32_bf16 v[124:127], v[140:143], v[212:215], v[124:127]\n"
"v_and_b32_e32 v234, v138, v234\n"
"v_and_b32_e32 v235, v139, v235\n"
"v_mfma_f32_16x16x32_bf16 v[156:159], v[116:119], v[144:147], v[156:159]\n"
"v_xor_b32_e32 v112, v136, v232\n"
"v_xor_b32_e32 v113, v137, v233\n"
"v_mfma_f32_16x16x32_bf16 v[124:127], v[192:195], v[144:147], v[124:127]\n"
"v_xor_b32_e32 v114, v138, v234\n"
"v_xor_b32_e32 v115, v139, v235\n"
"v_mfma_f32_16x16x32_bf16 v[164:167], v[172:175], v[232:235], v[164:167]\n"
"buffer_load_dwordx4 v[136:139], v1, s[4:7], s97 offen offset:3072 sc1\n"
"s_add_u32 s97, s58, 0x40000\n"
"buffer_load_dwordx4 v[144:147], v0, s[4:7], s97 offen sc1\n"
"v_mfma_f32_16x16x32_bf16 v[160:163], v[140:143], v[232:235], v[160:163]\n"
"buffer_load_dwordx4 v[140:143], v0, s[4:7], s97 offen offset:1024 sc1\n"
"v_mfma_f32_16x16x32_bf16 v[164:167], v[116:119], v[112:115], v[164:167]\n"
"buffer_load_dwordx4 v[116:119], v0, s[4:7], s97 offen offset:2048 sc1\n"
"v_mfma_f32_16x16x32_bf16 v[160:163], v[192:195], v[112:115], v[160:163]\n"
"buffer_load_dwordx4 v[112:115], v0, s[4:7], s97 offen offset:3072 sc1\n"
"v_max_f32_e32 v248, v148, v168\n"
"v_rcp_f32_e32 v148, v148\n"
"v_rcp_f32_e32 v168, v168\n"
"s_nop 0\n"
"v_max3_f32 v248, v248, v148, v168\n"
"v_cmp_nlt_f32_e32 vcc, v248, v252\n"
"s_cbranch_vccnz .Lmk_b24\n"
".Lmk_k29:\n"
"v_mul_f32_e32 v156, v156, v148\n"
"v_mul_f32_e32 v157, v157, v148\n"
"v_mul_f32_e32 v158, v158, v148\n"
"v_mul_f32_e32 v159, v159, v148\n"
"v_mul_f32_e32 v124, v124, v148\n"
"v_mul_f32_e32 v125, v125, v148\n"
"v_mul_f32_e32 v126, v126, v148\n"
"v_mul_f32_e32 v127, v127, v148\n"
"v_cvt_pk_bf16_f32 v88, v156, v157\n"
"v_cvt_pk_bf16_f32 v89, v158, v159\n"
"v_cvt_pk_bf16_f32 v90, v124, v125\n"
"v_cvt_pk_bf16_f32 v91, v126, v127\n"
"buffer_store_dwordx4 v[88:91], v0, s[4:7], s60 offen sc1\n"
"s_add_u32 s98, s60, 0x8000\n"
"v_mul_f32_e32 v164, v164, v168\n"
"v_mul_f32_e32 v165, v165, v168\n"
"v_mul_f32_e32 v166, v166, v168\n"
"v_mul_f32_e32 v167, v167, v168\n"
"v_mul_f32_e32 v160, v160, v168\n"
"v_mul_f32_e32 v161, v161, v168\n"
"v_mul_f32_e32 v162, v162, v168\n"
"v_mul_f32_e32 v163, v163, v168\n"
"v_cvt_pk_bf16_f32 v88, v164, v165\n"
"v_cvt_pk_bf16_f32 v89, v166, v167\n"
"v_cvt_pk_bf16_f32 v90, v160, v161\n"
"v_cvt_pk_bf16_f32 v91, v162, v163\n"
"buffer_store_dwordx4 v[88:91], v0, s[4:7], s98 offen sc1\n"
";\n"
"s_waitcnt vmcnt(20)\n"
"; @A G2 b.0\n"
"s_nop 0\n"
"v_mfma_f32_16x16x32_bf16 v[88:91], a[128:131], v[176:179], 0\n"
"v_mfma_f32_16x16x32_bf16 v[88:91], a[132:135], v[200:203], v[88:91]\n"
"s_waitcnt vmcnt(18)\n"
"v_mfma_f32_16x16x32_bf16 v[88:91], a[136:139], v[188:191], v[88:91]\n"
"buffer_load_dwordx4 v[92:95], v1, s[4:7], s97 offen sc1\n"
"v_mfma_f32_16x16x32_bf16 v[88:91], a[140:143], v[204:207], v[88:91]\n"
"s_waitcnt vmcnt(17)\n"
"v_mfma_f32_16x16x32_bf16 v[88:91], a[144:147], v[152:155], v[88:91]\n"
"v_mfma_f32_16x16x32_bf16 v[88:91], a[148:151], v[208:211], v[88:91]\n"
"s_waitcnt vmcnt(15)\n"
"v_mfma_f32_16x16x32_bf16 v[88:91], a[152:155], v[120:123], v[88:91]\n"
"buffer_load_dwordx4 v[96:99], v1, s[4:7], s97 offen offset:1024 sc1\n"
"v_mfma_f32_16x16x32_bf16 v[88:91], a[156:159], v[132:135], v[88:91]\n"
"v_mfma_f32_16x16x32_bf16 v[124:127], a[188:191], v[132:135], 0\n"
"v_mfma_f32_16x16x32_bf16 v[124:127], a[184:187], v[120:123], v[124:127]\n"
"v_mfma_f32_16x16x32_bf16 v[124:127], a[180:183], v[208:211], v[124:127]\n"
"buffer_load_dwordx4 v[148:151], v1, s[4:7], s97 offen offset:2048 sc1\n"
"v_mfma_f32_16x16x32_bf16 v[124:127], a[176:179], v[152:155], v[124:127]\n"
"v_mfma_f32_16x16x32_bf16 v[124:127], a[172:175], v[204:207], v[124:127]\n"
"s_nop 1\n"
"v_cmp_u_f32_e32 vcc, v88, v88\n"
"v_mfma_f32_16x16x32_bf16 v[124:127], a[168:171], v[188:191], v[124:127]\n"
"s_cbranch_vccnz .Lmk_p30s\n"
".Lmk_p30r:\n"
"v_mfma_f32_16x16x32_bf16 v[124:127], a[164:167], v[200:203], v[124:127]\n"
"ds_write_b128 v3, v[88:91]\n"
"v_mfma_f32_16x16x32_bf16 v[124:127], a[160:163], v[176:179], v[124:127]\n"
"buffer_load_dwordx4 v[88:91], v1, s[4:7], s97 offen offset:3072 sc1\n"
"v_mfma_f32_16x16x32_bf16 v[156:159], a[192:195], v[176:179], 0\n"
"v_mfma_f32_16x16x32_bf16 v[156:159], a[196:199], v[200:203], v[156:159]\n"
"s_add_u32 s98, s59, 0x40000\n"
"v_mfma_f32_16x16x32_bf16 v[156:159], a[200:203], v[188:191], v[156:159]\n"
"v_mfma_f32_16x16x32_bf16 v[156:159], a[204:207], v[204:207], v[156:159]\n"
"buffer_load_dwordx4 v[160:163], v0, s[4:7], s98 offen sc1\n"
"v_mfma_f32_16x16x32_bf16 v[156:159], a[208:211], v[152:155], v[156:159]\n"
"v_mfma_f32_16x16x32_bf16 v[156:159], a[212:215], v[208:211], v[156:159]\n"
"ds_write_b128 v3, v[124:127] offset:1024\n"
"v_mfma_f32_16x16x32_bf16 v[156:159], a[216:219], v[120:123], v[156:159]\n"
"v_mfma_f32_16x16x32_bf16 v[156:159], a[220:223], v[132:135], v[156:159]\n"
"buffer_load_dwordx4 v[124:127], v0, s[4:7], s98 offen offset:1024 sc1\n"
"v_mfma_f32_16x16x32_bf16 v[164:167], a[252:255], v[132:135], 0\n"
"v_mfma_f32_16x16x32_bf16 v[164:167], a[248:251], v[120:123], v[164:167]\n"
"v_mfma_f32_16x16x32_bf16 v[164:167], a[244:247], v[208:211], v[164:167]\n"
"v_mfma_f32_16x16x32_bf16 v[164:167], a[240:243], v[152:155], v[164:167]\n"
"buffer_load_dwordx4 v[120:123], v0, s[4:7], s98 offen offset:2048 sc1\n"
"v_mfma_f32_16x16x32_bf16 v[164:167], a[236:239], v[204:207], v[164:167]\n"
"v_mfma_f32_16x16x32_bf16 v[164:167], a[232:235], v[188:191], v[164:167]\n"
"ds_write_b128 v3, v[156:159] offset:2048\n"
"v_mfma_f32_16x16x32_bf16 v[164:167], a[228:231], v[200:203], v[164:167]\n"
"v_mfma_f32_16x16x32_bf16 v[164:167], a[224:227], v[176:179], v[164:167]\n"
"s_waitcnt vmcnt(17)\n"
"buffer_load_dwordx4 v[132:135], v0, s[4:7], s98 offen offset:3072 sc1\n"
"; @A G2 b.1\n"
"v_mfma_f32_16x16x32_bf16 v[152:155], a[128:131], v[216:219], 0\n"
"v_mfma_f32_16x16x32_bf16 v[152:155], a[132:135], v[128:131], v[152:155]\n"
"v_mfma_f32_16x16x32_bf16 v[152:155], a[136:139], v[180:183], v[152:155]\n"
"v_mfma_f32_16x16x32_bf16 v[152:155], a[140:143], v[100:103], v[152:155]\n"
"s_waitcnt vmcnt(14)\n"
"buffer_load_dwordx4 v[156:159], v1, s[4:7], s98 offen sc1\n"
"v_mfma_f32_16x16x32_bf16 v[152:155], a[144:147], v[184:187], v[152:155]\n"
"v_mfma_f32_16x16x32_bf16 v[152:155], a[148:151], v[104:107], v[152:155]\n"
"ds_write_b128 v3, v[164:167] offset:3072\n"
"v_mfma_f32_16x16x32_bf16 v[152:155], a[152:155], v[108:111], v[152:155]\n"
"v_mfma_f32_16x16x32_bf16 v[152:155], a[156:159], v[136:139], v[152:155]\n"
"buffer_load_dwordx4 v[164:167], v1, s[4:7], s98 offen offset:1024 sc1\n"
"v_mfma_f32_16x16x32_bf16 v[168:171], a[188:191], v[136:139], 0\n"
"v_mfma_f32_16x16x32_bf16 v[168:171], a[184:187], v[108:111], v[168:171]\n"
"v_mfma_f32_16x16x32_bf16 v[168:171], a[180:183], v[104:107], v[168:171]\n"
"v_mfma_f32_16x16x32_bf16 v[168:171], a[176:179], v[184:187], v[168:171]\n"
"buffer_load_dwordx4 v[172:175], v1, s[4:7], s98 offen offset:2048 sc1\n"
"v_mfma_f32_16x16x32_bf16 v[168:171], a[172:175], v[100:103], v[168:171]\n"
"s_nop 0\n"
"v_cmp_u_f32_e32 vcc, v152, v152\n"
"v_mfma_f32_16x16x32_bf16 v[168:171], a[168:171], v[180:183], v[168:171]\n"
"s_cbranch_vccnz .Lmk_p31s\n"
".Lmk_p31r:\n"
"v_mfma_f32_16x16x32_bf16 v[168:171], a[164:167], v[128:131], v[168:171]\n"
"ds_write_b128 v3, v[152:155] offset:6144\n"
"v_mfma_f32_16x16x32_bf16 v[168:171], a[160:163], v[216:219], v[168:171]\n"
"buffer_load_dwordx4 v[152:155], v1, s[4:7], s98 offen offset:3072 sc1\n"
"v_mfma_f32_16x16x32_bf16 v[176:179], a[192:195], v[216:219], 0\n"
"v_mfma_f32_16x16x32_bf16 v[176:179], a[196:199], v[128:131], v[176:179]\n"
"v_mfma_f32_16x16x32_bf16 v[176:179], a[200:203], v[180:183], v[176:179]\n"
"v_mfma_f32_16x16x32_bf16 v[176:179], a[204:207], v[100:103], v[176:179]\n"
"v_mfma_f32_16x16x32_bf16 v[176:179], a[208:211], v[184:187], v[176:179]\n"
"ds_read_b128 v[188:191], v2 offset:8192\n"
"ds_read_b128 v[192:195], v2 offset:9216\n"
"v_mfma_f32_16x16x32_bf16 v[176:179], a[212:215], v[104:107], v[176:179]\n"
"ds_write_b128 v3, v[168:171] offset:7168\n"
"v_mfma_f32_16x16x32_bf16 v[176:179], a[216:219], v[108:111], v[176:179]\n"
"v_mfma_f32_16x16x32_bf16 v[176:179], a[220:223], v[136:139], v[176:179]\n"
"v_mfma_f32_16x16x32_bf16 v[168:171], a[252:255], v[136:139], 0\n"
"s_mov_b32 s98, .Lmk_b0-.Lmk_g34\n"
"s_getpc_b64 s[84:85]\n"
".Lmk_g34:\n"
"s_add_u32 s84, s84, s98\n"
"s_addc_u32 s85, s85, 0\n"
"s_and_b32 s84, s84, -16\n"
"v_mfma_f32_16x16x32_bf16 v[168:171], a[248:251], v[108:111], v[168:171]\n"
"s_and_b32 s85, s85, 0xffff\n"
"s_sub_u32 s98, .Lmk_end-.Lmk_g34, s98\n"
"s_min_u32 s86, s98, 0x3000\n"
"s_mov_b32 s87, 0x20000\n"
"v_mfma_f32_16x16x32_bf16 v[168:171], a[244:247], v[104:107], v[168:171]\n"
"s_mov_b32 m0, 0x27800\n"
"s_nop 0\n"
"buffer_load_dwordx4 v0, s[84:87], s99 offen lds\n"
"v_mfma_f32_16x16x32_bf16 v[168:171], a[240:243], v[184:187], v[168:171]\n"
"s_waitcnt lgkmcnt(1)\n"
"v_lshlrev_b32_e32 v255, 16, v188\n"
"v_and_b32_e32 v254, 0xffff0000, v188\n"
"v_mfma_f32_16x16x32_bf16 v[168:171], a[236:239], v[100:103], v[168:171]\n"
"v_lshlrev_b32_e32 v253, 16, v189\n"
"v_and_b32_e32 v252, 0xffff0000, v189\n"
"v_mfma_f32_16x16x32_bf16 v[168:171], a[232:235], v[180:183], v[168:171]\n"
"ds_write_b128 v3, v[176:179] offset:8192\n"
"v_mfma_f32_16x16x32_bf16 v[168:171], a[228:231], v[128:131], v[168:171]\n"
"v_lshlrev_b32_e32 v251, 16, v190\n"
"v_and_b32_e32 v250, 0xffff0000, v190\n"
"v_mfma_f32_16x16x32_bf16 v[168:171], a[224:227], v[216:219], v[168:171]\n"
"s_waitcnt vmcnt(15)\n"
"v_lshlrev_b32_e32 v249, 16, v191\n"
"v_and_b32_e32 v248, 0xffff0000, v191\n"
"; @A G2 b.2\n"
"v_mfma_f32_16x16x32_bf16 v[100:103], a[128:131], v[144:147], 0\n"
"v_lshlrev_b32_e32 v247, 16, v192\n"
"v_and_b32_e32 v246, 0xffff0000, v192\n"
"v_mfma_f32_16x16x32_bf16 v[100:103], a[132:135], v[140:143], v[100:103]\n"
"v_lshlrev_b32_e32 v245, 16, v193\n"
"v_and_b32_e32 v244, 0xffff0000, v193\n"
"v_mfma_f32_16x16x32_bf16 v[100:103], a[136:139], v[116:119], v[100:103]\n"
"v_lshlrev_b32_e32 v243, 16, v194\n"
"v_and_b32_e32 v242, 0xffff0000, v194\n"
"v_mfma_f32_16x16x32_bf16 v[100:103], a[140:143], v[112:115], v[100:103]\n"
"s_waitcnt vmcnt(9)\n"
"v_lshlrev_b32_e32 v241, 16, v195\n"
"v_and_b32_e32 v240, 0xffff0000, v195\n"
"v_mfma_f32_16x16x32_bf16 v[100:103], a[144:147], v[92:95], v[100:103]\n"
"v_mfma_f32_16x16x32_bf16 v[100:103], a[148:151], v[96:99], v[100:103]\n"
"ds_write_b128 v3, v[168:171] offset:9216\n"
"v_mfma_f32_16x16x32_bf16 v[100:103], a[152:155], v[148:151], v[100:103]\n"
"v_mfma_f32_16x16x32_bf16 v[100:103], a[156:159], v[88:91], v[100:103]\n"
"v_mfma_f32_16x16x32_bf16 v[104:107], a[188:191], v[88:91], 0\n"
"v_mfma_f32_16x16x32_bf16 v[104:107], a[184:187], v[148:151], v[104:107]\n"
"s_waitcnt lgkmcnt(0)\n"
"s_barrier\n"
"v_mfma_f32_16x16x32_bf16 v[104:107], a[180:183], v[96:99], v[104:107]\n"
"ds_read_b128 v[108:111], v4\n"
"ds_read_b128 v[128:131], v5\n"
"v_mfma_f32_16x16x32_bf16 v[104:107], a[176:179], v[92:95], v[104:107]\n"
"v_mfma_f32_16x16x32_bf16 v[104:107], a[172:175], v[112:115], v[104:107]\n"
"s_add_u32 s98, s56, 0\n"
"v_cmp_u_f32_e32 vcc, v100, v100\n"
"v_mfma_f32_16x16x32_bf16 v[104:107], a[168:171], v[116:119], v[104:107]\n"
"s_cbranch_vccnz .Lmk_p32s\n"
".Lmk_p32r:\n"
"v_mfma_f32_16x16x32_bf16 v[104:107], a[164:167], v[140:143], v[104:107]\n"
"ds_write_b128 v3, v[100:103] offset:12288\n"
"v_mfma_f32_16x16x32_bf16 v[104:107], a[160:163], v[144:147], v[104:107]\n"
"buffer_load_dwordx4 v[100:103], v0, s[4:7], s98 offen sc1\n"
"v_mfma_f32_16x16x32_bf16 v[136:139], a[192:195], v[144:147], 0\n"
"ds_read_b128 v[168:171], v4 offset:1024\n"
"ds_read_b128 v[176:179], v5 offset:1024\n"
"v_mfma_f32_16x16x32_bf16 v[136:139], a[196:199], v[140:143], v[136:139]\n"
"s_waitcnt lgkmcnt(3)\n"
"v_add_f32_e32 v108, v108, v255\n"
"v_add_f32_e32 v109, v109, v254\n"
"v_mfma_f32_16x16x32_bf16 v[136:139], a[200:203], v[116:119], v[136:139]\n"
"buffer_load_dwordx4 v[180:183], v0, s[4:7], s98 offen offset:1024 sc1\n"
"v_mfma_f32_16x16x32_bf16 v[136:139], a[204:207], v[112:115], v[136:139]\n"
"v_add_f32_e32 v110, v110, v253\n"
"v_add_f32_e32 v111, v111, v252\n"
"v_mfma_f32_16x16x32_bf16 v[136:139], a[208:211], v[92:95], v[136:139]\n"
"v_add_f32_e32 v108, v108, v128\n"
"v_add_f32_e32 v109, v109, v129\n"
"v_mfma_f32_16x16x32_bf16 v[136:139], a[212:215], v[96:99], v[136:139]\n"
"ds_write_b128 v3, v[104:107] offset:13312\n"
"v_mfma_f32_16x16x32_bf16 v[136:139], a[216:219], v[148:151], v[136:139]\n"
"buffer_load_dwordx4 v[104:107], v0, s[4:7], s98 offen offset:2048 sc1\n"
"v_mfma_f32_16x16x32_bf16 v[136:139], a[220:223], v[88:91], v[136:139]\n"
"ds_read_b128 v[184:187], v4 offset:2048\n"
"ds_read_b128 v[188:191], v5 offset:2048\n"
"v_mfma_f32_16x16x32_bf16 v[192:195], a[252:255], v[88:91], 0\n"
"s_waitcnt lgkmcnt(3)\n"
"v_add_f32_e32 v110, v110, v130\n"
"v_add_f32_e32 v111, v111, v131\n"
"v_mfma_f32_16x16x32_bf16 v[192:195], a[248:251], v[148:151], v[192:195]\n"
"buffer_load_dwordx4 v[88:91], v0, s[4:7], s98 offen offset:3072 sc1\n"
"v_mfma_f32_16x16x32_bf16 v[192:195], a[244:247], v[96:99], v[192:195]\n"
"v_add_f32_e32 v168, v168, v251\n"
"v_add_f32_e32 v169, v169, v250\n"
"v_mfma_f32_16x16x32_bf16 v[192:195], a[240:243], v[92:95], v[192:195]\n"
"v_add_f32_e32 v170, v170, v249\n"
"v_add_f32_e32 v171, v171, v248\n"
"v_mfma_f32_16x16x32_bf16 v[192:195], a[236:239], v[112:115], v[192:195]\n"
"buffer_load_dwordx4 v[92:95], v1, s[4:7], s98 offen sc1\n"
"v_mfma_f32_16x16x32_bf16 v[192:195], a[232:235], v[116:119], v[192:195]\n"
"ds_write_b128 v3, v[136:139] offset:14336\n"
"v_mfma_f32_16x16x32_bf16 v[192:195], a[228:231], v[140:143], v[192:195]\n"
"ds_read_b128 v[96:99], v4 offset:3072\n"
"ds_read_b128 v[112:115], v5 offset:3072\n"
"v_mfma_f32_16x16x32_bf16 v[192:195], a[224:227], v[144:147], v[192:195]\n"
"s_waitcnt vmcnt(10)\n"
"buffer_load_dwordx4 v[116:119], v1, s[4:7], s98 offen offset:1024 sc1\n"
"; @A G2 b.3\n"
"v_mfma_f32_16x16x32_bf16 v[128:131], a[128:131], v[160:163], v[108:111]\n"
"v_add_f32_e32 v168, v168, v176\n"
"v_add_f32_e32 v169, v169, v177\n"
"v_mfma_f32_16x16x32_bf16 v[128:131], a[132:135], v[124:127], v[128:131]\n"
"s_waitcnt lgkmcnt(3)\n"
"v_add_f32_e32 v170, v170, v178\n"
"v_add_f32_e32 v171, v171, v179\n"
"v_mfma_f32_16x16x32_bf16 v[128:131], a[136:139], v[120:123], v[128:131]\n"
"buffer_load_dwordx4 v[136:139], v1, s[4:7], s98 offen offset:2048 sc1\n"
"v_mfma_f32_16x16x32_bf16 v[128:131], a[140:143], v[132:135], v[128:131]\n"
"s_waitcnt vmcnt(8)\n"
"v_add_f32_e32 v184, v184, v247\n"
"v_add_f32_e32 v185, v185, v246\n"
"v_mfma_f32_16x16x32_bf16 v[128:131], a[144:147], v[156:159], v[128:131]\n"
"v_add_f32_e32 v186, v186, v245\n"
"v_add_f32_e32 v187, v187, v244\n"
"v_mfma_f32_16x16x32_bf16 v[128:131], a[148:151], v[164:167], v[128:131]\n"
"ds_write_b128 v3, v[192:195] offset:15360\n"
"v_mfma_f32_16x16x32_bf16 v[128:131], a[152:155], v[172:175], v[128:131]\n"
"buffer_load_dwordx4 v[140:143], v1, s[4:7], s98 offen offset:3072 sc1\n"
"v_mfma_f32_16x16x32_bf16 v[128:131], a[156:159], v[152:155], v[128:131]\n"
"s_add_u32 s98, s57, 0\n"
"v_add_f32_e32 v184, v184, v188\n"
"v_add_f32_e32 v185, v185, v189\n"
"v_mfma_f32_16x16x32_bf16 v[144:147], a[188:191], v[152:155], v[168:171]\n"
"s_waitcnt lgkmcnt(1)\n"
"v_add_f32_e32 v186, v186, v190\n"
"v_add_f32_e32 v187, v187, v191\n"
"v_mfma_f32_16x16x32_bf16 v[144:147], a[184:187], v[172:175], v[144:147]\n"
"s_waitcnt lgkmcnt(0)\n"
"s_barrier\n"
"v_mfma_f32_16x16x32_bf16 v[144:147], a[180:183], v[164:167], v[144:147]\n"
"buffer_load_dwordx4 v[148:151], v0, s[4:7], s98 offen sc1\n"
"v_mfma_f32_16x16x32_bf16 v[144:147], a[176:179], v[156:159], v[144:147]\n"
"v_add_f32_e32 v96, v96, v243\n"
"v_add_f32_e32 v97, v97, v242\n"
"v_mfma_f32_16x16x32_bf16 v[144:147], a[172:175], v[132:135], v[144:147]\n"
"v_cmp_u_f32_e32 vcc, v128, v128\n"
"v_add_f32_e32 v98, v98, v241\n"
"v_mfma_f32_16x16x32_bf16 v[144:147], a[168:171], v[120:123], v[144:147]\n"
"s_cbranch_vccnz .Lmk_p33s\n"
".Lmk_p33r:\n"
"v_add_f32_e32 v99, v99, v240\n"
"v_mfma_f32_16x16x32_bf16 v[144:147], a[164:167], v[124:127], v[144:147]\n"
"buffer_load_dwordx4 v[108:111], v0, s[4:7], s98 offen offset:1024 sc1\n"
"v_mfma_f32_16x16x32_bf16 v[144:147], a[160:163], v[160:163], v[144:147]\n"
"v_add_f32_e32 v96, v96, v112\n"
"v_add_f32_e32 v97, v97, v113\n"
"v_mfma_f32_16x16x32_bf16 v[184:187], a[192:195], v[160:163], v[184:187]\n"
"v_add_f32_e32 v98, v98, v114\n"
"v_add_f32_e32 v99, v99, v115\n"
"v_mfma_f32_16x16x32_bf16 v[184:187], a[196:199], v[124:127], v[184:187]\n"
"buffer_load_dwordx4 v[112:115], v0, s[4:7], s98 offen offset:2048 sc1\n"
"v_mfma_f32_16x16x32_bf16 v[184:187], a[200:203], v[120:123], v[184:187]\n"
"ds_read_b128 v[168:171], v6\n"
"ds_read_b128 v[176:179], v6 offset:1024\n"
"v_mfma_f32_16x16x32_bf16 v[184:187], a[204:207], v[132:135], v[184:187]\n"
"ds_read_b128 v[188:191], v6 offset:2048\n"
"ds_read_b128 v[192:195], v6 offset:3072\n"
"v_mfma_f32_16x16x32_bf16 v[184:187], a[208:211], v[156:159], v[184:187]\n"
"buffer_load_dwordx4 v[196:199], v0, s[4:7], s98 offen offset:3072 sc1\n"
"v_mfma_f32_16x16x32_bf16 v[184:187], a[212:215], v[164:167], v[184:187]\n"
"v_mfma_f32_16x16x32_bf16 v[184:187], a[216:219], v[172:175], v[184:187]\n"
"v_mfma_f32_16x16x32_bf16 v[184:187], a[220:223], v[152:155], v[184:187]\n"
"buffer_load_dwordx4 v[200:203], v1, s[4:7], s98 offen sc1\n"
"v_mfma_f32_16x16x32_bf16 v[96:99], a[252:255], v[152:155], v[96:99]\n"
"s_waitcnt lgkmcnt(0)\n"
"v_add_f32_e32 v168, v128, v168\n"
"v_add_f32_e32 v169, v129, v169\n"
"v_mfma_f32_16x16x32_bf16 v[96:99], a[248:251], v[172:175], v[96:99]\n"
"v_add_f32_e32 v170, v130, v170\n"
"v_add_f32_e32 v171, v131, v171\n"
"v_mfma_f32_16x16x32_bf16 v[96:99], a[244:247], v[164:167], v[96:99]\n"
"buffer_load_dwordx4 v[128:131], v1, s[4:7], s98 offen offset:1024 sc1\n"
"v_mfma_f32_16x16x32_bf16 v[96:99], a[240:243], v[156:159], v[96:99]\n"
"v_add_f32_e32 v176, v144, v176\n"
"v_add_f32_e32 v177, v145, v177\n"
"v_mfma_f32_16x16x32_bf16 v[96:99], a[236:239], v[132:135], v[96:99]\n"
"v_add_f32_e32 v178, v146, v178\n"
"v_add_f32_e32 v179, v147, v179\n"
"v_mfma_f32_16x16x32_bf16 v[96:99], a[232:235], v[120:123], v[96:99]\n"
"buffer_load_dwordx4 v[120:123], v1, s[4:7], s98 offen offset:2048 sc1\n"
"v_mfma_f32_16x16x32_bf16 v[96:99], a[228:231], v[124:127], v[96:99]\n"
"v_cvt_pk_bf16_f32 v124, v168, v169\n"
"v_cvt_pk_bf16_f32 v125, v170, v171\n"
"v_mfma_f32_16x16x32_bf16 v[96:99], a[224:227], v[160:163], v[96:99]\n"
"s_add_u32 s97, s61, 0x440000\n"
"v_cvt_pk_bf16_f32 v126, v176, v177\n"
"v_cvt_pk_bf16_f32 v127, v178, v179\n"
"buffer_store_dwordx4 v[124:127], v0, s[4:7], s97 offen sc1\n"
"ds_write_b128 v2, v[124:127] offset:8192\n"
"buffer_load_dwordx4 v[132:135], v1, s[4:7], s98 offen offset:3072 sc1\n"
"; @A fin b\n"
"s_add_u32 s98, s61, 0x440000\n"
"v_add_f32_e32 v188, v184, v188\n"
"v_add_f32_e32 v189, v185, v189\n"
"v_add_f32_e32 v190, v186, v190\n"
"v_add_f32_e32 v191, v187, v191\n"
"v_add_f32_e32 v192, v96, v192\n"
"v_add_f32_e32 v193, v97, v193\n"
"v_add_f32_e32 v194, v98, v194\n"
"v_add_f32_e32 v195, v99, v195\n"
"v_cvt_pk_bf16_f32 v96, v188, v189\n"
"v_cvt_pk_bf16_f32 v97, v190, v191\n"
"v_cvt_pk_bf16_f32 v98, v192, v193\n"
"v_cvt_pk_bf16_f32 v99, v194, v195\n"
"buffer_store_dwordx4 v[96:99], v0, s[4:7], s98 offen offset:1024 sc1\n"
"s_waitcnt vmcnt(16)\n"
"; @A G2 a.0\n"
"v_mfma_f32_16x16x32_bf16 v[144:147], a[128:131], v[100:103], 0\n"
"s_add_u32 s98, s58, 0\n"
"buffer_load_dwordx4 v[152:155], v0, s[4:7], s98 offen sc1\n"
"v_mfma_f32_16x16x32_bf16 v[144:147], a[132:135], v[180:183], v[144:147]\n"
"s_waitcnt vmcnt(15)\n"
"ds_write_b128 v2, v[96:99] offset:9216\n"
"v_mfma_f32_16x16x32_bf16 v[144:147], a[136:139], v[104:107], v[144:147]\n"
"v_mov_b32_e32 v156, 0x3f803f80\n"
"v_mov_b32_e32 v157, 0x3f803f80\n"
"v_mfma_f32_16x16x32_bf16 v[144:147], a[140:143], v[88:91], v[144:147]\n"
"s_waitcnt vmcnt(13)\n"
"v_mov_b32_e32 v158, 0x3f803f80\n"
"v_mov_b32_e32 v159, 0x3f803f80\n"
"v_mfma_f32_16x16x32_bf16 v[144:147], a[144:147], v[92:95], v[144:147]\n"
"buffer_load_dwordx4 v[160:163], v0, s[4:7], s98 offen offset:1024 sc1\n"
"v_mfma_f32_16x16x32_bf16 v[164:167], v[156:159], v[124:127], 0\n"
"v_mfma_f32_16x16x32_bf16 v[164:167], v[156:159], v[96:99], v[164:167]\n"
"v_mfma_f32_16x16x32_bf16 v[156:159], v[124:127], v[124:127], 0\n"
"v_mfma_f32_16x16x32_bf16 v[156:159], v[96:99], v[96:99], v[156:159]\n"
"v_mfma_f32_16x16x32_bf16 v[144:147], a[148:151], v[116:119], v[144:147]\n"
"s_waitcnt vmcnt(12)\n"
"v_mfma_f32_16x16x32_bf16 v[144:147], a[152:155], v[136:139], v[144:147]\n"
"v_mfma_f32_16x16x32_bf16 v[144:147], a[156:159], v[140:143], v[144:147]\n"
"v_mfma_f32_16x16x32_bf16 v[96:99], a[188:191], v[140:143], 0\n"
"buffer_load_dwordx4 v[124:127], v0, s[4:7], s98 offen offset:2048 sc1\n"
"v_mfma_f32_16x16x32_bf16 v[96:99], a[184:187], v[136:139], v[96:99]\n"
"s_mov_b32 s96, 0xaaaaaaaa\n"
"s_mov_b32 s97, 0xaaaaaaaa\n"
"v_mfma_f32_16x16x32_bf16 v[96:99], a[180:183], v[116:119], v[96:99]\n"
"v_mfma_f32_16x16x32_bf16 v[96:99], a[176:179], v[92:95], v[96:99]\n"
"v_mfma_f32_16x16x32_bf16 v[96:99], a[172:175], v[88:91], v[96:99]\n"
"v_cmp_u_f32_e32 vcc, v144, v144\n"
"v_cndmask_b32_e64 v156, v156, v157, s[96:97]\n"
"v_mfma_f32_16x16x32_bf16 v[96:99], a[168:171], v[104:107], v[96:99]\n"
"s_cbranch_vccnz .Lmk_p35s\n"
".Lmk_p35r:\n"
"v_cndmask_b32_e64 v158, v158, v159, s[96:97]\n"
"v_mfma_f32_16x16x32_bf16 v[96:99], a[164:167], v[180:183], v[96:99]\n"
"ds_write_b128 v3, v[144:147]\n"
"v_mfma_f32_16x16x32_bf16 v[96:99], a[160:163], v[100:103], v[96:99]\n"
"buffer_load_dwordx4 v[144:147], v0, s[4:7], s98 offen offset:3072 sc1\n"
"v_mfma_f32_16x16x32_bf16 v[168:171], a[192:195], v[100:103], 0\n"
"s_mov_b32 s96, 0xcccccccc\n"
"s_mov_b32 s97, 0xcccccccc\n"
"v_mfma_f32_16x16x32_bf16 v[168:171], a[196:199], v[180:183], v[168:171]\n"
"v_cndmask_b32_e64 v165, v156, v158, s[96:97]\n"
"v_mfma_f32_16x16x32_bf16 v[168:171], a[200:203], v[104:107], v[168:171]\n"
"s_mov_b32 s96, 0xf0000f\n"
"s_mov_b32 s97, 0xf0000f00\n"
"v_mfma_f32_16x16x32_bf16 v[168:171], a[204:207], v[88:91], v[168:171]\n"
"buffer_load_dwordx4 v[156:159], v1, s[4:7], s98 offen sc1\n"
"v_mfma_f32_16x16x32_bf16 v[168:171], a[208:211], v[92:95], v[168:171]\n"
"v_cndmask_b32_e64 v165, 0, v165, s[96:97]\n"
"v_mov_b32_e32 v255, v165\n"
"v_mfma_f32_16x16x32_bf16 v[168:171], a[212:215], v[116:119], v[168:171]\n"
"ds_write_b128 v3, v[96:99] offset:1024\n"
"v_mfma_f32_16x16x32_bf16 v[168:171], a[216:219], v[136:139], v[168:171]\n"
"v_permlane16_swap_b32_e32 v165, v255\n"
"v_add_f32_e32 v165, v165, v255\n"
"v_mfma_f32_16x16x32_bf16 v[168:171], a[220:223], v[140:143], v[168:171]\n"
"buffer_load_dwordx4 v[172:175], v1, s[4:7], s98 offen offset:1024 sc1\n"
"v_mfma_f32_16x16x32_bf16 v[96:99], a[252:255], v[140:143], 0\n"
"v_mov_b32_e32 v255, v165\n"
"v_mfma_f32_16x16x32_bf16 v[96:99], a[248:251], v[136:139], v[96:99]\n"
"s_mov_b32 s96, 0xffff0000\n"
"v_permlane32_swap_b32_e32 v165, v255\n"
"v_add_f32_e32 v165, v165, v255\n"
"v_mfma_f32_16x16x32_bf16 v[96:99], a[244:247], v[116:119], v[96:99]\n"
"v_cndmask_b32_e64 v164, v164, v165, s[96:97]\n"
"s_add_u32 s97, s63, 0x44000\n"
"v_mfma_f32_16x16x32_bf16 v[96:99], a[240:243], v[92:95], v[96:99]\n"
"buffer_load_dwordx4 v[116:119], v1, s[4:7], s98 offen offset:2048 sc1\n"
"v_mfma_f32_16x16x32_bf16 v[96:99], a[236:239], v[88:91], v[96:99]\n"
"buffer_store_dword v164, v14, s[4:7], s97 offen sc1\n"
"v_mfma_f32_16x16x32_bf16 v[96:99], a[232:235], v[104:107], v[96:99]\n"
"ds_write_b128 v3, v[168:171] offset:2048\n"
"v_mfma_f32_16x16x32_bf16 v[96:99], a[228:231], v[180:183], v[96:99]\n"
"v_mfma_f32_16x16x32_bf16 v[96:99], a[224:227], v[100:103], v[96:99]\n"
"s_waitcnt vmcnt(14)\n"
"buffer_load_dwordx4 v[136:139], v1, s[4:7], s98 offen offset:3072 sc1\n"
"; @A G2 a.1\n"
"v_mfma_f32_16x16x32_bf16 v[88:91], a[128:131], v[148:151], 0\n"
"v_mfma_f32_16x16x32_bf16 v[88:91], a[132:135], v[108:111], v[88:91]\n"
"s_add_u32 s98, s59, 0\n"
"v_mfma_f32_16x16x32_bf16 v[88:91], a[136:139], v[112:115], v[88:91]\n"
"v_mfma_f32_16x16x32_bf16 v[88:91], a[140:143], v[196:199], v[88:91]\n"
"s_waitcnt vmcnt(10)\n"
"buffer_load_dwordx4 v[176:179], v0, s[4:7], s98 offen sc1\n"
"v_mfma_f32_16x16x32_bf16 v[88:91], a[144:147], v[200:203], v[88:91]\n"
"v_mfma_f32_16x16x32_bf16 v[88:91], a[148:151], v[128:131], v[88:91]\n"
"ds_write_b128 v3, v[96:99] offset:3072\n"
"v_mfma_f32_16x16x32_bf16 v[88:91], a[152:155], v[120:123], v[88:91]\n"
"v_mfma_f32_16x16x32_bf16 v[88:91], a[156:159], v[132:135], v[88:91]\n"
"buffer_load_dwordx4 v[180:183], v0, s[4:7], s98 offen offset:1024 sc1\n"
"v_mfma_f32_16x16x32_bf16 v[92:95], a[188:191], v[132:135], 0\n"
"v_mfma_f32_16x16x32_bf16 v[92:95], a[184:187], v[120:123], v[92:95]\n"
"v_mfma_f32_16x16x32_bf16 v[92:95], a[180:183], v[128:131], v[92:95]\n"
"v_mfma_f32_16x16x32_bf16 v[92:95], a[176:179], v[200:203], v[92:95]\n"
"buffer_load_dwordx4 v[168:171], v0, s[4:7], s98 offen offset:2048 sc1\n"
"v_mfma_f32_16x16x32_bf16 v[92:95], a[172:175], v[196:199], v[92:95]\n"
"s_nop 0\n"
"v_cmp_u_f32_e32 vcc, v88, v88\n"
"v_mfma_f32_16x16x32_bf16 v[92:95], a[168:171], v[112:115], v[92:95]\n"
"s_cbranch_vccnz .Lmk_p36s\n"
".Lmk_p36r:\n"
"v_mfma_f32_16x16x32_bf16 v[92:95], a[164:167], v[108:111], v[92:95]\n"
"ds_write_b128 v3, v[88:91] offset:6144\n"
"v_mfma_f32_16x16x32_bf16 v[92:95], a[160:163], v[148:151], v[92:95]\n"
"buffer_load_dwordx4 v[184:187], v0, s[4:7], s98 offen offset:3072 sc1\n"
"v_mfma_f32_16x16x32_bf16 v[88:91], a[192:195], v[148:151], 0\n"
"v_mfma_f32_16x16x32_bf16 v[88:91], a[196:199], v[108:111], v[88:91]\n"
"v_mfma_f32_16x16x32_bf16 v[88:91], a[200:203], v[112:115], v[88:91]\n"
"v_mfma_f32_16x16x32_bf16 v[88:91], a[204:207], v[196:199], v[88:91]\n"
"buffer_load_dwordx4 v[188:191], v1, s[4:7], s98 offen sc1\n"
"v_mfma_f32_16x16x32_bf16 v[88:91], a[208:211], v[200:203], v[88:91]\n"
"ds_read_b128 v[96:99], v2\n"
"ds_read_b128 v[100:103], v2 offset:1024\n"
"v_mfma_f32_16x16x32_bf16 v[88:91], a[212:215], v[128:131], v[88:91]\n"
"ds_write_b128 v3, v[92:95] offset:7168\n"
"v_mfma_f32_16x16x32_bf16 v[88:91], a[216:219], v[120:123], v[88:91]\n"
"v_mfma_f32_16x16x32_bf16 v[88:91], a[220:223], v[132:135], v[88:91]\n"
"buffer_load_dwordx4 v[164:167], v1, s[4:7], s98 offen offset:1024 sc1\n"
"v_mfma_f32_16x16x32_bf16 v[92:95], a[252:255], v[132:135], 0\n"
"v_mfma_f32_16x16x32_bf16 v[92:95], a[248:251], v[120:123], v[92:95]\n"
"s_waitcnt lgkmcnt(1)\n"
"v_lshlrev_b32_e32 v255, 16, v96\n"
"v_and_b32_e32 v254, 0xffff0000, v96\n"
"v_mfma_f32_16x16x32_bf16 v[92:95], a[244:247], v[128:131], v[92:95]\n"
"v_lshlrev_b32_e32 v253, 16, v97\n"
"v_and_b32_e32 v252, 0xffff0000, v97\n"
"v_mfma_f32_16x16x32_bf16 v[92:95], a[240:243], v[200:203], v[92:95]\n"
"buffer_load_dwordx4 v[192:195], v1, s[4:7], s98 offen offset:2048 sc1\n"
"v_mfma_f32_16x16x32_bf16 v[92:95], a[236:239], v[196:199], v[92:95]\n"
"v_lshlrev_b32_e32 v251, 16, v98\n"
"v_and_b32_e32 v250, 0xffff0000, v98\n"
"v_mfma_f32_16x16x32_bf16 v[92:95], a[232:235], v[112:115], v[92:95]\n"
"ds_write_b128 v3, v[88:91] offset:8192\n"
"v_mfma_f32_16x16x32_bf16 v[92:95], a[228:231], v[108:111], v[92:95]\n"
"v_lshlrev_b32_e32 v249, 16, v99\n"
"v_and_b32_e32 v248, 0xffff0000, v99\n"
"v_mfma_f32_16x16x32_bf16 v[92:95], a[224:227], v[148:151], v[92:95]\n"
"s_waitcnt vmcnt(12)\n"
"buffer_load_dwordx4 v[196:199], v1, s[4:7], s98 offen offset:3072 sc1\n"
"; @A G2 a.2\n"
"v_mfma_f32_16x16x32_bf16 v[96:99], a[128:131], v[152:155], 0\n"
"v_lshlrev_b32_e32 v247, 16, v100\n"
"v_and_b32_e32 v246, 0xffff0000, v100\n"
"v_mfma_f32_16x16x32_bf16 v[96:99], a[132:135], v[160:163], v[96:99]\n"
"v_lshlrev_b32_e32 v245, 16, v101\n"
"v_and_b32_e32 v244, 0xffff0000, v101\n"
"v_mfma_f32_16x16x32_bf16 v[96:99], a[136:139], v[124:127], v[96:99]\n"
"v_lshlrev_b32_e32 v243, 16, v102\n"
"v_and_b32_e32 v242, 0xffff0000, v102\n"
"v_mfma_f32_16x16x32_bf16 v[96:99], a[140:143], v[144:147], v[96:99]\n"
"s_waitcnt vmcnt(8)\n"
"v_lshlrev_b32_e32 v241, 16, v103\n"
"v_and_b32_e32 v240, 0xffff0000, v103\n"
"v_mfma_f32_16x16x32_bf16 v[96:99], a[144:147], v[156:159], v[96:99]\n"
"v_mfma_f32_16x16x32_bf16 v[96:99], a[148:151], v[172:175], v[96:99]\n"
"ds_write_b128 v3, v[92:95] offset:9216\n"
"v_mfma_f32_16x16x32_bf16 v[96:99], a[152:155], v[116:119], v[96:99]\n"
"v_mfma_f32_16x16x32_bf16 v[96:99], a[156:159], v[136:139], v[96:99]\n"
"v_mfma_f32_16x16x32_bf16 v[104:107], a[188:191], v[136:139], 0\n"
"s_add_u32 s98, s52, 0x440000\n"
"buffer_load_dwordx4 v[88:91], v0, s[4:7], s98 offen sc1\n"
"v_mfma_f32_16x16x32_bf16 v[104:107], a[184:187], v[116:119], v[104:107]\n"
"s_waitcnt lgkmcnt(0)\n"
"s_barrier\n"
"v_mfma_f32_16x16x32_bf16 v[104:107], a[180:183], v[172:175], v[104:107]\n"
"ds_read_b128 v[140:143], v4\n"
"ds_read_b128 v[112:115], v5\n"
"v_mfma_f32_16x16x32_bf16 v[104:107], a[176:179], v[156:159], v[104:107]\n"
"buffer_load_dwordx4 v[92:95], v0, s[4:7], s98 offen offset:1024 sc1\n"
"v_mfma_f32_16x16x32_bf16 v[104:107], a[172:175], v[144:147], v[104:107]\n"
"v_cmp_u_f32_e32 vcc, v96, v96\n"
"v_mfma_f32_16x16x32_bf16 v[104:107], a[168:171], v[124:127], v[104:107]\n"
"s_cbranch_vccnz .Lmk_p37s\n"
".Lmk_p37r:\n"
"v_mfma_f32_16x16x32_bf16 v[104:107], a[164:167], v[160:163], v[104:107]\n"
"ds_write_b128 v3, v[96:99] offset:12288\n"
"v_mfma_f32_16x16x32_bf16 v[104:107], a[160:163], v[152:155], v[104:107]\n"
"buffer_load_dwordx4 v[96:99], v0, s[4:7], s98 offen offset:2048 sc1\n"
"v_mfma_f32_16x16x32_bf16 v[120:123], a[192:195], v[152:155], 0\n"
"ds_read_b128 v[148:151], v4 offset:1024\n"
"ds_read_b128 v[128:131], v5 offset:1024\n"
"v_mfma_f32_16x16x32_bf16 v[120:123], a[196:199], v[160:163], v[120:123]\n"
"s_waitcnt lgkmcnt(3)\n"
"v_add_f32_e32 v140, v140, v255\n"
"v_add_f32_e32 v141, v141, v254\n"
"v_mfma_f32_16x16x32_bf16 v[120:123], a[200:203], v[124:127], v[120:123]\n"
"buffer_load_dwordx4 v[100:103], v0, s[4:7], s98 offen offset:3072 sc1\n"
"v_mfma_f32_16x16x32_bf16 v[120:123], a[204:207], v[144:147], v[120:123]\n"
"v_add_f32_e32 v142, v142, v253\n"
"v_add_f32_e32 v143, v143, v252\n"
"v_mfma_f32_16x16x32_bf16 v[120:123], a[208:211], v[156:159], v[120:123]\n"
"v_add_f32_e32 v140, v140, v112\n"
"v_add_f32_e32 v141, v141, v113\n"
"v_mfma_f32_16x16x32_bf16 v[120:123], a[212:215], v[172:175], v[120:123]\n"
"ds_write_b128 v3, v[104:107] offset:13312\n"
"v_mfma_f32_16x16x32_bf16 v[120:123], a[216:219], v[116:119], v[120:123]\n"
"buffer_load_dwordx4 v[104:107], v1, s[4:7], s98 offen sc1\n"
"v_mfma_f32_16x16x32_bf16 v[120:123], a[220:223], v[136:139], v[120:123]\n"
"ds_read_b128 v[108:111], v4 offset:2048\n"
"ds_read_b128 v[200:203], v5 offset:2048\n"
"v_mfma_f32_16x16x32_bf16 v[132:135], a[252:255], v[136:139], 0\n"
"s_waitcnt lgkmcnt(3)\n"
"v_add_f32_e32 v142, v142, v114\n"
"v_add_f32_e32 v143, v143, v115\n"
"v_mfma_f32_16x16x32_bf16 v[132:135], a[248:251], v[116:119], v[132:135]\n"
"buffer_load_dwordx4 v[112:115], v1, s[4:7], s98 offen offset:1024 sc1\n"
"v_mfma_f32_16x16x32_bf16 v[132:135], a[244:247], v[172:175], v[132:135]\n"
"v_add_f32_e32 v148, v148, v251\n"
"v_add_f32_e32 v149, v149, v250\n"
"v_mfma_f32_16x16x32_bf16 v[132:135], a[240:243], v[156:159], v[132:135]\n"
"v_add_f32_e32 v150, v150, v249\n"
"v_add_f32_e32 v151, v151, v248\n"
"v_mfma_f32_16x16x32_bf16 v[132:135], a[236:239], v[144:147], v[132:135]\n"
"buffer_load_dwordx4 v[116:119], v1, s[4:7], s98 offen offset:2048 sc1\n"
"v_mfma_f32_16x16x32_bf16 v[132:135], a[232:235], v[124:127], v[132:135]\n"
"ds_write_b128 v3, v[120:123] offset:14336\n"
"v_mfma_f32_16x16x32_bf16 v[132:135], a[228:231], v[160:163], v[132:135]\n"
"ds_read_b128 v[120:123], v4 offset:3072\n"
"ds_read_b128 v[144:147], v5 offset:3072\n"
"v_mfma_f32_16x16x32_bf16 v[132:135], a[224:227], v[152:155], v[132:135]\n"
"s_waitcnt vmcnt(11)\n"
"buffer_load_dwordx4 v[124:127], v1, s[4:7], s98 offen offset:3072 sc1\n"
"; @A G2 a.3\n"
"v_mfma_f32_16x16x32_bf16 v[172:175], a[128:131], v[176:179], v[140:143]\n"
"s_add_u32 s98, s53, 0x440000\n"
"v_add_f32_e32 v148, v148, v128\n"
"v_add_f32_e32 v149, v149, v129\n"
"v_mfma_f32_16x16x32_bf16 v[172:175], a[132:135], v[180:183], v[172:175]\n"
"s_waitcnt lgkmcnt(3)\n"
"v_add_f32_e32 v150, v150, v130\n"
"v_add_f32_e32 v151, v151, v131\n"
"v_mfma_f32_16x16x32_bf16 v[172:175], a[136:139], v[168:171], v[172:175]\n"
"buffer_load_dwordx4 v[128:131], v0, s[4:7], s98 offen sc1\n"
"v_mfma_f32_16x16x32_bf16 v[172:175], a[140:143], v[184:187], v[172:175]\n"
"s_waitcnt vmcnt(9)\n"
"v_add_f32_e32 v108, v108, v247\n"
"v_add_f32_e32 v109, v109, v246\n"
"v_mfma_f32_16x16x32_bf16 v[172:175], a[144:147], v[188:191], v[172:175]\n"
"v_add_f32_e32 v110, v110, v245\n"
"v_add_f32_e32 v111, v111, v244\n"
"v_mfma_f32_16x16x32_bf16 v[172:175], a[148:151], v[164:167], v[172:175]\n"
"ds_write_b128 v3, v[132:135] offset:15360\n"
"v_mfma_f32_16x16x32_bf16 v[172:175], a[152:155], v[192:195], v[172:175]\n"
"buffer_load_dwordx4 v[132:135], v0, s[4:7], s98 offen offset:1024 sc1\n"
"v_mfma_f32_16x16x32_bf16 v[172:175], a[156:159], v[196:199], v[172:175]\n"
"v_add_f32_e32 v108, v108, v200\n"
"v_add_f32_e32 v109, v109, v201\n"
"v_mfma_f32_16x16x32_bf16 v[204:207], a[188:191], v[196:199], v[148:151]\n"
"s_waitcnt lgkmcnt(1)\n"
"v_add_f32_e32 v110, v110, v202\n"
"v_add_f32_e32 v111, v111, v203\n"
"v_mfma_f32_16x16x32_bf16 v[204:207], a[184:187], v[192:195], v[204:207]\n"
"s_waitcnt lgkmcnt(0)\n"
"s_barrier\n"
"v_mfma_f32_16x16x32_bf16 v[204:207], a[180:183], v[164:167], v[204:207]\n"
"buffer_load_dwordx4 v[136:139], v0, s[4:7], s98 offen offset:2048 sc1\n"
"v_mfma_f32_16x16x32_bf16 v[204:207], a[176:179], v[188:191], v[204:207]\n"
"v_add_f32_e32 v120, v120, v243\n"
"v_add_f32_e32 v121, v121, v242\n"
"v_mfma_f32_16x16x32_bf16 v[204:207], a[172:175], v[184:187], v[204:207]\n"
"v_cmp_u_f32_e32 vcc, v172, v172\n"
"v_add_f32_e32 v122, v122, v241\n"
"v_mfma_f32_16x16x32_bf16 v[204:207], a[168:171], v[168:171], v[204:207]\n"
"s_cbranch_vccnz .Lmk_p38s\n"
".Lmk_p38r:\n"
"v_add_f32_e32 v123, v123, v240\n"
"v_mfma_f32_16x16x32_bf16 v[204:207], a[164:167], v[180:183], v[204:207]\n"
"buffer_load_dwordx4 v[140:143], v0, s[4:7], s98 offen offset:3072 sc1\n"
"v_mfma_f32_16x16x32_bf16 v[204:207], a[160:163], v[176:179], v[204:207]\n"
"v_add_f32_e32 v120, v120, v144\n"
"v_add_f32_e32 v121, v121, v145\n"
"v_mfma_f32_16x16x32_bf16 v[108:111], a[192:195], v[176:179], v[108:111]\n"
"v_add_f32_e32 v122, v122, v146\n"
"v_add_f32_e32 v123, v123, v147\n"
"v_mfma_f32_16x16x32_bf16 v[108:111], a[196:199], v[180:183], v[108:111]\n"
"buffer_load_dwordx4 v[144:147], v1, s[4:7], s98 offen sc1\n"
"v_mfma_f32_16x16x32_bf16 v[108:111], a[200:203], v[168:171], v[108:111]\n"
"ds_read_b128 v[200:203], v6\n"
"ds_read_b128 v[208:211], v6 offset:1024\n"
"v_mfma_f32_16x16x32_bf16 v[108:111], a[204:207], v[184:187], v[108:111]\n"
"ds_read_b128 v[148:151], v6 offset:2048\n"
"ds_read_b128 v[152:155], v6 offset:3072\n"
"v_mfma_f32_16x16x32_bf16 v[108:111], a[208:211], v[188:191], v[108:111]\n"
"buffer_load_dwordx4 v[156:159], v1, s[4:7], s98 offen offset:1024 sc1\n"
"v_mfma_f32_16x16x32_bf16 v[108:111], a[212:215], v[164:167], v[108:111]\n"
"v_mfma_f32_16x16x32_bf16 v[108:111], a[216:219], v[192:195], v[108:111]\n"
"v_mfma_f32_16x16x32_bf16 v[108:111], a[220:223], v[196:199], v[108:111]\n"
"buffer_load_dwordx4 v[160:163], v1, s[4:7], s98 offen offset:2048 sc1\n"
"v_mfma_f32_16x16x32_bf16 v[120:123], a[252:255], v[196:199], v[120:123]\n"
"s_waitcnt lgkmcnt(0)\n"
"v_add_f32_e32 v200, v172, v200\n"
"v_add_f32_e32 v201, v173, v201\n"
"v_mfma_f32_16x16x32_bf16 v[120:123], a[248:251], v[192:195], v[120:123]\n"
"v_add_f32_e32 v202, v174, v202\n"
"v_add_f32_e32 v203, v175, v203\n"
"v_mfma_f32_16x16x32_bf16 v[120:123], a[244:247], v[164:167], v[120:123]\n"
"buffer_load_dwordx4 v[164:167], v1, s[4:7], s98 offen offset:3072 sc1\n"
"v_mfma_f32_16x16x32_bf16 v[120:123], a[240:243], v[188:191], v[120:123]\n"
"v_add_f32_e32 v208, v204, v208\n"
"v_add_f32_e32 v209, v205, v209\n"
"v_mfma_f32_16x16x32_bf16 v[120:123], a[236:239], v[184:187], v[120:123]\n"
"v_add_f32_e32 v210, v206, v210\n"
"v_add_f32_e32 v211, v207, v211\n"
"v_mfma_f32_16x16x32_bf16 v[120:123], a[232:235], v[168:171], v[120:123]\n"
"v_cvt_pk_bf16_f32 v168, v200, v201\n"
"v_cvt_pk_bf16_f32 v169, v202, v203\n"
"v_mfma_f32_16x16x32_bf16 v[120:123], a[228:231], v[180:183], v[120:123]\n"
"s_add_u32 s98, s61, 0x400000\n"
"v_cvt_pk_bf16_f32 v170, v208, v209\n"
"v_cvt_pk_bf16_f32 v171, v210, v211\n"
"v_mfma_f32_16x16x32_bf16 v[120:123], a[224:227], v[176:179], v[120:123]\n"
"buffer_store_dwordx4 v[168:171], v0, s[4:7], s98 offen sc1\n"
"ds_write_b128 v2, v[168:171]\n"
".Lmk_loop:\n"
".Lmk_b0:\n"
"s_waitcnt vmcnt(13)\n"
"; @B G1 0.0\n"
"v_mfma_f32_16x16x32_bf16 v[172:175], a[0:3], v[88:91], 0\n"
"v_mfma_f32_16x16x32_bf16 v[172:175], a[4:7], v[92:95], v[172:175]\n"
"s_add_u32 s98, s62, 0x44000\n"
"v_mfma_f32_16x16x32_bf16 v[172:175], a[8:11], v[96:99], v[172:175]\n"
"s_add_u32 s97, s54, 0x440000\n"
"buffer_load_dwordx4 v[176:179], v0, s[4:7], s97 offen sc1\n"
"v_mfma_f32_16x16x32_bf16 v[172:175], a[12:15], v[100:103], v[172:175]\n"
"s_waitcnt vmcnt(10)\n"
"v_mfma_f32_16x16x32_bf16 v[172:175], a[16:19], v[104:107], v[172:175]\n"
"buffer_load_dword v255, v10, s[4:7], s98 offen sc1\n"
"v_mfma_f32_16x16x32_bf16 v[172:175], a[20:23], v[112:115], v[172:175]\n"
"v_add_f32_e32 v148, v108, v148\n"
"v_add_f32_e32 v149, v109, v149\n"
"v_mfma_f32_16x16x32_bf16 v[172:175], a[24:27], v[116:119], v[172:175]\n"
"v_add_f32_e32 v150, v110, v150\n"
"v_add_f32_e32 v151, v111, v151\n"
"v_mfma_f32_16x16x32_bf16 v[172:175], a[28:31], v[124:127], v[172:175]\n"
"buffer_load_dwordx4 v[108:111], v0, s[4:7], s97 offen offset:1024 sc1\n"
"v_mfma_f32_16x16x32_bf16 v[180:183], a[60:63], v[124:127], 0\n"
"buffer_load_dword v254, v10, s[4:7], s98 offen offset:256 sc1\n"
"v_mfma_f32_16x16x32_bf16 v[180:183], a[56:59], v[116:119], v[180:183]\n"
"v_add_f32_e32 v152, v120, v152\n"
"v_add_f32_e32 v153, v121, v153\n"
"v_mfma_f32_16x16x32_bf16 v[180:183], a[52:55], v[112:115], v[180:183]\n"
"v_add_f32_e32 v154, v122, v154\n"
"v_add_f32_e32 v155, v123, v155\n"
"v_mfma_f32_16x16x32_bf16 v[180:183], a[48:51], v[104:107], v[180:183]\n"
"buffer_load_dword v253, v10, s[4:7], s98 offen offset:512 sc1\n"
"v_mfma_f32_16x16x32_bf16 v[180:183], a[44:47], v[100:103], v[180:183]\n"
"v_cmp_u_f32_e32 vcc, v172, v172\n"
"v_cvt_pk_bf16_f32 v120, v148, v149\n"
"v_mfma_f32_16x16x32_bf16 v[180:183], a[40:43], v[96:99], v[180:183]\n"
"s_cbranch_vccnz .Lmk_p43s\n"
".Lmk_p43r:\n"
"v_cvt_pk_bf16_f32 v121, v150, v151\n"
"v_mfma_f32_16x16x32_bf16 v[180:183], a[36:39], v[92:95], v[180:183]\n"
"buffer_load_dwordx4 v[148:151], v0, s[4:7], s97 offen offset:2048 sc1\n"
"v_mfma_f32_16x16x32_bf16 v[180:183], a[32:35], v[88:91], v[180:183]\n"
"ds_write_b128 v3, v[172:175]\n"
"v_mfma_f32_16x16x32_bf16 v[172:175], a[64:67], v[88:91], 0\n"
"buffer_load_dword v252, v10, s[4:7], s98 offen offset:768 sc1\n"
"v_mfma_f32_16x16x32_bf16 v[172:175], a[68:71], v[92:95], v[172:175]\n"
"buffer_load_dwordx4 v[184:187], v0, s[4:7], s97 offen offset:3072 sc1\n"
"v_mfma_f32_16x16x32_bf16 v[172:175], a[72:75], v[96:99], v[172:175]\n"
"s_add_u32 s96, s61, 0x400000\n"
"v_cvt_pk_bf16_f32 v122, v152, v153\n"
"v_cvt_pk_bf16_f32 v123, v154, v155\n"
"v_mfma_f32_16x16x32_bf16 v[172:175], a[76:79], v[100:103], v[172:175]\n"
"buffer_load_dword v251, v10, s[4:7], s98 offen offset:1024 sc1\n"
"v_mfma_f32_16x16x32_bf16 v[172:175], a[80:83], v[104:107], v[172:175]\n"
"buffer_store_dwordx4 v[120:123], v0, s[4:7], s96 offen offset:1024 sc1\n"
"v_mfma_f32_16x16x32_bf16 v[172:175], a[84:87], v[112:115], v[172:175]\n"
"ds_write_b128 v3, v[180:183] offset:1024\n"
"v_mfma_f32_16x16x32_bf16 v[172:175], a[88:91], v[116:119], v[172:175]\n"
"buffer_load_dwordx4 v[152:155], v1, s[4:7], s97 offen sc1\n"
"v_mfma_f32_16x16x32_bf16 v[172:175], a[92:95], v[124:127], v[172:175]\n"
"buffer_load_dword v250, v10, s[4:7], s98 offen offset:1280 sc1\n"
"v_mfma_f32_16x16x32_bf16 v[180:183], a[124:127], v[124:127], 0\n"
"ds_write_b128 v2, v[120:123] offset:1024\n"
"v_mfma_f32_16x16x32_bf16 v[180:183], a[120:123], v[116:119], v[180:183]\n"
"v_mov_b32_e32 v188, 0x3f803f80\n"
"v_mov_b32_e32 v189, 0x3f803f80\n"
"v_mfma_f32_16x16x32_bf16 v[180:183], a[116:119], v[112:115], v[180:183]\n"
"buffer_load_dword v249, v10, s[4:7], s98 offen offset:1536 sc1\n"
"v_mfma_f32_16x16x32_bf16 v[180:183], a[112:115], v[104:107], v[180:183]\n"
"buffer_load_dwordx4 v[192:195], v1, s[4:7], s97 offen offset:1024 sc1\n"
"v_mfma_f32_16x16x32_bf16 v[180:183], a[108:111], v[100:103], v[180:183]\n"
"v_mov_b32_e32 v190, 0x3f803f80\n"
"v_mov_b32_e32 v191, 0x3f803f80\n"
"v_mfma_f32_16x16x32_bf16 v[180:183], a[104:107], v[96:99], v[180:183]\n"
"ds_write_b128 v3, v[172:175] offset:2048\n"
"v_mfma_f32_16x16x32_bf16 v[172:175], v[188:191], v[168:171], 0\n"
"v_mfma_f32_16x16x32_bf16 v[172:175], v[188:191], v[120:123], v[172:175]\n"
"v_mfma_f32_16x16x32_bf16 v[188:191], v[168:171], v[168:171], 0\n"
"v_mfma_f32_16x16x32_bf16 v[188:191], v[120:123], v[120:123], v[188:191]\n"
"v_mfma_f32_16x16x32_bf16 v[180:183], a[100:103], v[92:95], v[180:183]\n"
"v_mov_b32_e32 v120, -1\n"
"v_mov_b32_e32 v121, -1\n"
"v_mfma_f32_16x16x32_bf16 v[180:183], a[96:99], v[88:91], v[180:183]\n"
"v_mov_b32_e32 v122, -1\n"
"v_mov_b32_e32 v123, -1\n"
"v_mfma_f32_16x16x32_bf16 v[168:171], v[24:27], v[88:91], 0\n"
"buffer_load_dwordx4 v[196:199], v1, s[4:7], s97 offen offset:2048 sc1\n"
"v_mfma_f32_16x16x32_bf16 v[168:171], v[28:31], v[92:95], v[168:171]\n"
"s_add_u32 s96, s60, 0x448000\n"
"buffer_store_dwordx4 v[120:123], v0, s[4:7], s96 offen sc1\n"
"v_mfma_f32_16x16x32_bf16 v[168:171], v[32:35], v[96:99], v[168:171]\n"
"buffer_load_dword v248, v10, s[4:7], s98 offen offset:1792 sc1\n"
"v_mfma_f32_16x16x32_bf16 v[168:171], v[36:39], v[100:103], v[168:171]\n"
"s_mov_b32 s94, 0xaaaaaaaa\n"
"s_mov_b32 s95, 0xaaaaaaaa\n"
"v_mfma_f32_16x16x32_bf16 v[168:171], v[40:43], v[104:107], v[168:171]\n"
"v_mfma_f32_16x16x32_bf16 v[168:171], v[44:47], v[112:115], v[168:171]\n"
"buffer_load_dwordx4 v[200:203], v1, s[4:7], s97 offen offset:3072 sc1\n"
"v_mfma_f32_16x16x32_bf16 v[168:171], v[48:51], v[116:119], v[168:171]\n"
"ds_write_b128 v3, v[180:183] offset:3072\n"
"v_mfma_f32_16x16x32_bf16 v[168:171], v[52:55], v[124:127], v[168:171]\n"
"v_cndmask_b32_e64 v188, v188, v189, s[94:95]\n"
"v_cndmask_b32_e64 v190, v190, v191, s[94:95]\n"
"v_mfma_f32_16x16x32_bf16 v[180:183], v[84:87], v[124:127], 0\n"
"s_mov_b32 s94, 0xcccccccc\n"
"s_mov_b32 s95, 0xcccccccc\n"
"v_mfma_f32_16x16x32_bf16 v[180:183], v[80:83], v[116:119], v[180:183]\n"
"v_cndmask_b32_e64 v173, v188, v190, s[94:95]\n"
"v_mfma_f32_16x16x32_bf16 v[180:183], v[76:79], v[112:115], v[180:183]\n"
"s_mov_b32 s94, 0xf0000f\n"
"s_mov_b32 s95, 0xf0000f00\n"
"v_mfma_f32_16x16x32_bf16 v[180:183], v[72:75], v[104:107], v[180:183]\n"
"v_cndmask_b32_e64 v173, 0, v173, s[94:95]\n"
"v_mov_b32_e32 v247, v173\n"
"v_mfma_f32_16x16x32_bf16 v[180:183], v[68:71], v[100:103], v[180:183]\n"
"s_add_u32 s98, s70, 0x200000\n"
"buffer_load_dwordx4 a[128:131], v0, s[4:7], s98 offen sc1\n"
"v_mfma_f32_16x16x32_bf16 v[180:183], v[64:67], v[96:99], v[180:183]\n"
"ds_write_b128 v3, v[168:171] offset:4096\n"
"v_mfma_f32_16x16x32_bf16 v[180:183], v[60:63], v[92:95], v[180:183]\n"
"v_permlane16_swap_b32_e32 v173, v247\n"
"v_add_f32_e32 v173, v173, v247\n"
"v_mfma_f32_16x16x32_bf16 v[180:183], v[56:59], v[88:91], v[180:183]\n"
"s_waitcnt vmcnt(24)\n"
"v_mov_b32_e32 v247, v173\n"
"; @B G1 0.1\n"
"v_mfma_f32_16x16x32_bf16 v[88:91], a[0:3], v[128:131], 0\n"
"s_mov_b32 s94, 0xffff0000\n"
"v_permlane32_swap_b32_e32 v173, v247\n"
"v_add_f32_e32 v173, v173, v247\n"
"v_mfma_f32_16x16x32_bf16 v[88:91], a[4:7], v[132:135], v[88:91]\n"
"s_add_u32 s97, s63, 0x40000\n"
"v_cndmask_b32_e64 v172, v172, v173, s[94:95]\n"
"v_mfma_f32_16x16x32_bf16 v[88:91], a[8:11], v[136:139], v[88:91]\n"
"s_add_u32 s96, s55, 0x440000\n"
"buffer_load_dwordx4 v[92:95], v0, s[4:7], s96 offen sc1\n"
"v_mfma_f32_16x16x32_bf16 v[88:91], a[12:15], v[140:143], v[88:91]\n"
"s_waitcnt vmcnt(21)\n"
"buffer_store_dword v172, v14, s[4:7], s97 offen sc1\n"
"v_mfma_f32_16x16x32_bf16 v[88:91], a[16:19], v[144:147], v[88:91]\n"
"v_mfma_f32_16x16x32_bf16 v[88:91], a[20:23], v[156:159], v[88:91]\n"
"ds_write_b128 v3, v[180:183] offset:5120\n"
"v_mfma_f32_16x16x32_bf16 v[88:91], a[24:27], v[160:163], v[88:91]\n"
"v_mfma_f32_16x16x32_bf16 v[88:91], a[28:31], v[164:167], v[88:91]\n"
"buffer_load_dwordx4 v[96:99], v0, s[4:7], s96 offen offset:1024 sc1\n"
"v_mfma_f32_16x16x32_bf16 v[100:103], a[60:63], v[164:167], 0\n"
"v_mfma_f32_16x16x32_bf16 v[100:103], a[56:59], v[160:163], v[100:103]\n"
"v_mfma_f32_16x16x32_bf16 v[100:103], a[52:55], v[156:159], v[100:103]\n"
"v_mfma_f32_16x16x32_bf16 v[100:103], a[48:51], v[144:147], v[100:103]\n"
"v_mfma_f32_16x16x32_bf16 v[100:103], a[44:47], v[140:143], v[100:103]\n"
"s_nop 1\n"
"v_cmp_u_f32_e32 vcc, v88, v88\n"
"v_mfma_f32_16x16x32_bf16 v[100:103], a[40:43], v[136:139], v[100:103]\n"
"s_cbranch_vccnz .Lmk_p44s\n"
".Lmk_p44r:\n"
"v_mfma_f32_16x16x32_bf16 v[100:103], a[36:39], v[132:135], v[100:103]\n"
"buffer_load_dwordx4 v[104:107], v0, s[4:7], s96 offen offset:2048 sc1\n"
"v_mfma_f32_16x16x32_bf16 v[100:103], a[32:35], v[128:131], v[100:103]\n"
"ds_write_b128 v3, v[88:91] offset:6144\n"
"v_mfma_f32_16x16x32_bf16 v[88:91], a[64:67], v[128:131], 0\n"
"buffer_load_dwordx4 a[132:135], v0, s[4:7], s98 offen offset:1024 sc1\n"
"v_mfma_f32_16x16x32_bf16 v[88:91], a[68:71], v[132:135], v[88:91]\n"
"buffer_load_dwordx4 v[112:115], v0, s[4:7], s96 offen offset:3072 sc1\n"
"v_mfma_f32_16x16x32_bf16 v[88:91], a[72:75], v[136:139], v[88:91]\n"
"v_mfma_f32_16x16x32_bf16 v[88:91], a[76:79], v[140:143], v[88:91]\n"
"v_mfma_f32_16x16x32_bf16 v[88:91], a[80:83], v[144:147], v[88:91]\n"
"v_mfma_f32_16x16x32_bf16 v[88:91], a[84:87], v[156:159], v[88:91]\n"
"ds_write_b128 v3, v[100:103] offset:7168\n"
"v_mfma_f32_16x16x32_bf16 v[88:91], a[88:91], v[160:163], v[88:91]\n"
"buffer_load_dwordx4 v[100:103], v1, s[4:7], s96 offen sc1\n"
"v_mfma_f32_16x16x32_bf16 v[88:91], a[92:95], v[164:167], v[88:91]\n"
"v_mfma_f32_16x16x32_bf16 v[116:119], a[124:127], v[164:167], 0\n"
"v_mfma_f32_16x16x32_bf16 v[116:119], a[120:123], v[160:163], v[116:119]\n"
"v_mfma_f32_16x16x32_bf16 v[116:119], a[116:119], v[156:159], v[116:119]\n"
"v_mfma_f32_16x16x32_bf16 v[116:119], a[112:115], v[144:147], v[116:119]\n"
"buffer_load_dwordx4 v[124:127], v1, s[4:7], s96 offen offset:1024 sc1\n"
"v_mfma_f32_16x16x32_bf16 v[116:119], a[108:111], v[140:143], v[116:119]\n"
"v_mfma_f32_16x16x32_bf16 v[116:119], a[104:107], v[136:139], v[116:119]\n"
"s_nop 0\n"
"ds_write_b128 v3, v[88:91] offset:8192\n"
"v_mfma_f32_16x16x32_bf16 v[116:119], a[100:103], v[132:135], v[116:119]\n"
"v_mfma_f32_16x16x32_bf16 v[116:119], a[96:99], v[128:131], v[116:119]\n"
"v_mfma_f32_16x16x32_bf16 v[88:91], v[24:27], v[128:131], 0\n"
"buffer_load_dwordx4 v[168:171], v1, s[4:7], s96 offen offset:2048 sc1\n"
"v_mfma_f32_16x16x32_bf16 v[88:91], v[28:31], v[132:135], v[88:91]\n"
"s_add_u32 s97, s60, 0x440000\n"
"v_mfma_f32_16x16x32_bf16 v[88:91], v[32:35], v[136:139], v[88:91]\n"
"v_mfma_f32_16x16x32_bf16 v[88:91], v[36:39], v[140:143], v[88:91]\n"
"buffer_store_dwordx4 v[120:123], v0, s[4:7], s97 offen sc1\n"
"v_mfma_f32_16x16x32_bf16 v[88:91], v[40:43], v[144:147], v[88:91]\n"
"buffer_load_dwordx4 a[136:139], v0, s[4:7], s98 offen offset:2048 sc1\n"
"v_mfma_f32_16x16x32_bf16 v[88:91], v[44:47], v[156:159], v[88:91]\n"
"buffer_load_dwordx4 v[172:175], v1, s[4:7], s96 offen offset:3072 sc1\n"
"v_mfma_f32_16x16x32_bf16 v[88:91], v[48:51], v[160:163], v[88:91]\n"
"ds_write_b128 v3, v[116:119] offset:9216\n"
"v_mfma_f32_16x16x32_bf16 v[88:91], v[52:55], v[164:167], v[88:91]\n"
"s_waitcnt vmcnt(14)\n"
"v_add_f32_e32 v255, v255, v251\n"
"v_add_f32_e32 v254, v254, v250\n"
"v_mfma_f32_16x16x32_bf16 v[116:119], v[84:87], v[164:167], 0\n"
"v_add_f32_e32 v253, v253, v249\n"
"v_add_f32_e32 v252, v252, v248\n"
"v_mfma_f32_16x16x32_bf16 v[116:119], v[80:83], v[160:163], v[116:119]\n"
"v_add_f32_e32 v255, v255, v253\n"
"v_add_f32_e32 v254, v254, v252\n"
"v_mfma_f32_16x16x32_bf16 v[116:119], v[76:79], v[156:159], v[116:119]\n"
"v_add_f32_e32 v255, v255, v254\n"
"v_cmp_u_f32_e32 vcc, v255, v255\n"
"v_mfma_f32_16x16x32_bf16 v[116:119], v[72:75], v[144:147], v[116:119]\n"
"s_cbranch_vccnz .Lmk_m47s\n"
".Lmk_m47r:\n"
"v_mov_b32_e32 v254, v255\n"
"v_mfma_f32_16x16x32_bf16 v[116:119], v[68:71], v[140:143], v[116:119]\n"
"s_nop 0\n"
"v_permlane32_swap_b32_e32 v255, v254\n"
"v_add_f32_e32 v255, v255, v254\n"
"v_mfma_f32_16x16x32_bf16 v[116:119], v[64:67], v[136:139], v[116:119]\n"
"ds_write_b128 v3, v[88:91] offset:10240\n"
"v_mfma_f32_16x16x32_bf16 v[116:119], v[60:63], v[132:135], v[116:119]\n"
"v_mul_f32_e32 v255, 0x3a800000, v255\n"
"v_mov_b32_e32 v254, v255\n"
"v_mfma_f32_16x16x32_bf16 v[116:119], v[56:59], v[128:131], v[116:119]\n"
"s_nop 0\n"
"v_permlane16_swap_b32_e32 v255, v254\n"
"v_cvt_pk_bf16_f32 v253, -v255, 0\n"
"; @B G1 0.2\n"
"v_mfma_f32_16x16x32_bf16 v[88:91], a[0:3], v[176:179], 0\n"
"v_fma_f32 v254, -v255, v255, v254\n"
"v_lshlrev_b32_e32 v253, 16, v253\n"
"v_mfma_f32_16x16x32_bf16 v[88:91], a[4:7], v[108:111], v[88:91]\n"
"v_max_f32_e32 v254, 0, v254\n"
"v_cvt_pk_bf16_f32 v254, v254, 0\n"
"v_mfma_f32_16x16x32_bf16 v[88:91], a[8:11], v[148:151], v[88:91]\n"
"v_lshlrev_b32_e32 v254, 16, v254\n"
"v_add_f32_e32 v254, 0x3727c5ac, v254\n"
"v_mfma_f32_16x16x32_bf16 v[88:91], a[12:15], v[184:187], v[88:91]\n"
"s_waitcnt vmcnt(13)\n"
"v_cvt_pk_bf16_f32 v254, v254, 0\n"
"v_lshlrev_b32_e32 v254, 16, v254\n"
"v_mfma_f32_16x16x32_bf16 v[88:91], a[16:19], v[152:155], v[88:91]\n"
"s_mul_i32 s97, s0, .Lmk_exit-.Lmk_b2\n"
"s_add_u32 s97, s97, .Lmk_b2-.Lmk_g42\n"
"s_getpc_b64 s[84:85]\n"
".Lmk_g42:\n"
"s_add_u32 s84, s84, s97\n"
"s_addc_u32 s85, s85, 0\n"
"v_mfma_f32_16x16x32_bf16 v[88:91], a[20:23], v[192:195], v[88:91]\n"
"ds_write_b128 v3, v[116:119] offset:11264\n"
"v_mfma_f32_16x16x32_bf16 v[88:91], a[24:27], v[196:199], v[88:91]\n"
"s_and_b32 s84, s84, -16\n"
"s_and_b32 s85, s85, 0xffff\n"
"s_sub_u32 s97, .Lmk_end-.Lmk_g42, s97\n"
"s_min_u32 s86, s97, 0x4000\n"
"s_mov_b32 s87, 0x20000\n"
"v_mfma_f32_16x16x32_bf16 v[88:91], a[28:31], v[200:203], v[88:91]\n"
"s_mov_b32 m0, 0x27800\n"
"s_nop 0\n"
"buffer_load_dwordx4 v0, s[84:87], s99 offen lds\n"
"v_mfma_f32_16x16x32_bf16 v[116:119], a[60:63], v[200:203], 0\n"
"buffer_load_dwordx4 a[140:143], v0, s[4:7], s98 offen offset:3072 sc1\n"
"v_mfma_f32_16x16x32_bf16 v[116:119], a[56:59], v[196:199], v[116:119]\n"
"v_sqrt_f32_e32 v254, v254\n"
"v_mfma_f32_16x16x32_bf16 v[116:119], a[52:55], v[192:195], v[116:119]\n"
"s_add_u32 s97, s52, 0x448000\n"
"buffer_load_dwordx4 v[128:131], v0, s[4:7], s97 offen sc1\n"
"v_mfma_f32_16x16x32_bf16 v[116:119], a[48:51], v[152:155], v[116:119]\n"
"s_waitcnt lgkmcnt(0)\n"
"s_barrier\n"
"v_mfma_f32_16x16x32_bf16 v[116:119], a[44:47], v[184:187], v[116:119]\n"
"v_cmp_u_f32_e32 vcc, v88, v88\n"
"v_cvt_pk_bf16_f32 v254, v254, 0\n"
"v_mfma_f32_16x16x32_bf16 v[116:119], a[40:43], v[148:151], v[116:119]\n"
"s_cbranch_vccnz .Lmk_p45s\n"
".Lmk_p45r:\n"
"v_lshlrev_b32_e32 v254, 16, v254\n"
"v_mfma_f32_16x16x32_bf16 v[116:119], a[36:39], v[108:111], v[116:119]\n"
"ds_write_b128 v3, v[88:91] offset:12288\n"
"v_mfma_f32_16x16x32_bf16 v[116:119], a[32:35], v[176:179], v[116:119]\n"
"buffer_load_dwordx4 v[88:91], v0, s[4:7], s97 offen offset:1024 sc1\n"
"v_mfma_f32_16x16x32_bf16 v[132:135], a[64:67], v[176:179], 0\n"
"v_rcp_f32_e32 v255, v254\n"
"v_mfma_f32_16x16x32_bf16 v[132:135], a[68:71], v[108:111], v[132:135]\n"
"ds_read_b128 v[136:139], v4\n"
"v_mul_f32_e32 v254, 0x3e8293ee, v255\n"
"v_mfma_f32_16x16x32_bf16 v[132:135], a[72:75], v[148:151], v[132:135]\n"
"ds_read_b128 v[140:143], v9 offset:640\n"
"ds_read_b128 v[144:147], v5\n"
"v_mfma_f32_16x16x32_bf16 v[132:135], a[76:79], v[184:187], v[132:135]\n"
"ds_read_b128 v[156:159], v4 offset:1024\n"
"ds_read_b128 v[160:163], v9 offset:704\n"
"v_mfma_f32_16x16x32_bf16 v[132:135], a[80:83], v[152:155], v[132:135]\n"
"buffer_load_dwordx4 v[164:167], v0, s[4:7], s97 offen offset:2048 sc1\n"
"v_mfma_f32_16x16x32_bf16 v[132:135], a[84:87], v[192:195], v[132:135]\n"
"ds_write_b128 v3, v[116:119] offset:13312\n"
"v_mfma_f32_16x16x32_bf16 v[132:135], a[88:91], v[196:199], v[132:135]\n"
"ds_read_b128 v[116:119], v5 offset:1024\n"
"v_mfma_f32_16x16x32_bf16 v[132:135], a[92:95], v[200:203], v[132:135]\n"
"s_waitcnt lgkmcnt(4)\n"
"v_fmac_f32_e32 v136, v253, v140\n"
"v_fmac_f32_e32 v137, v253, v141\n"
"v_mfma_f32_16x16x32_bf16 v[180:183], a[124:127], v[200:203], 0\n"
"v_fmac_f32_e32 v138, v253, v142\n"
"v_fmac_f32_e32 v139, v253, v143\n"
"v_mfma_f32_16x16x32_bf16 v[180:183], a[120:123], v[196:199], v[180:183]\n"
"buffer_load_dwordx4 v[140:143], v0, s[4:7], s97 offen offset:3072 sc1\n"
"v_mfma_f32_16x16x32_bf16 v[180:183], a[116:119], v[192:195], v[180:183]\n"
"ds_read_b128 v[188:191], v4 offset:2048\n"
"ds_read_b128 v[204:207], v9 offset:768\n"
"v_mfma_f32_16x16x32_bf16 v[180:183], a[112:115], v[152:155], v[180:183]\n"
"ds_read_b128 v[208:211], v5 offset:2048\n"
"v_add_f32_e32 v136, v136, v144\n"
"v_mfma_f32_16x16x32_bf16 v[180:183], a[108:111], v[184:187], v[180:183]\n"
"buffer_load_dwordx4 a[144:147], v1, s[4:7], s98 offen sc1\n"
"v_mfma_f32_16x16x32_bf16 v[180:183], a[104:107], v[148:151], v[180:183]\n"
"ds_write_b128 v3, v[132:135] offset:14336\n"
"v_mfma_f32_16x16x32_bf16 v[180:183], a[100:103], v[108:111], v[180:183]\n"
"buffer_load_dwordx4 v[132:135], v1, s[4:7], s97 offen sc1\n"
"v_mfma_f32_16x16x32_bf16 v[180:183], a[96:99], v[176:179], v[180:183]\n"
"v_add_f32_e32 v137, v137, v145\n"
"v_add_f32_e32 v138, v138, v146\n"
"v_mfma_f32_16x16x32_bf16 v[212:215], v[24:27], v[176:179], 0\n"
"s_waitcnt lgkmcnt(4)\n"
"v_add_f32_e32 v139, v139, v147\n"
"v_fmac_f32_e32 v156, v253, v160\n"
"v_mfma_f32_16x16x32_bf16 v[212:215], v[28:31], v[108:111], v[212:215]\n"
"ds_read_b128 v[144:147], v4 offset:3072\n"
"ds_read_b128 v[216:219], v9 offset:832\n"
"v_mfma_f32_16x16x32_bf16 v[212:215], v[32:35], v[148:151], v[212:215]\n"
"ds_read_b128 v[220:223], v5 offset:3072\n"
"v_fmac_f32_e32 v157, v253, v161\n"
"v_mfma_f32_16x16x32_bf16 v[212:215], v[36:39], v[184:187], v[212:215]\n"
"buffer_load_dwordx4 v[224:227], v1, s[4:7], s97 offen offset:1024 sc1\n"
"v_mfma_f32_16x16x32_bf16 v[212:215], v[40:43], v[152:155], v[212:215]\n"
"s_add_u32 s96, s60, 0x400000\n"
"v_fmac_f32_e32 v158, v253, v162\n"
"v_fmac_f32_e32 v159, v253, v163\n"
"v_mfma_f32_16x16x32_bf16 v[212:215], v[44:47], v[192:195], v[212:215]\n"
"ds_write_b128 v3, v[180:183] offset:15360\n"
"v_mfma_f32_16x16x32_bf16 v[212:215], v[48:51], v[196:199], v[212:215]\n"
"buffer_store_dwordx4 v[120:123], v0, s[4:7], s96 offen sc1\n"
"v_mfma_f32_16x16x32_bf16 v[212:215], v[52:55], v[200:203], v[212:215]\n"
"v_add_f32_e32 v156, v156, v116\n"
"v_add_f32_e32 v157, v157, v117\n"
"v_mfma_f32_16x16x32_bf16 v[160:163], v[84:87], v[200:203], 0\n"
"buffer_load_dwordx4 v[180:183], v1, s[4:7], s97 offen offset:2048 sc1\n"
"v_mfma_f32_16x16x32_bf16 v[160:163], v[80:83], v[196:199], v[160:163]\n"
"s_add_u32 s96, s68, s80\n"
"s_add_u32 s96, s96, 0x10000\n"
"s_waitcnt vmcnt(21)\n"
"v_mfma_f32_16x16x32_bf16 v[160:163], v[76:79], v[192:195], v[160:163]\n"
"buffer_store_dwordx2 v[18:19], v11, s[4:7], s96 offen sc1\n"
"v_mfma_f32_16x16x32_bf16 v[160:163], v[72:75], v[152:155], v[160:163]\n"
"ds_read_b128 v[152:155], v4 offset:4096\n"
"ds_read_b128 v[192:195], v9 offset:512\n"
"v_mfma_f32_16x16x32_bf16 v[160:163], v[68:71], v[184:187], v[160:163]\n"
"ds_read_b128 v[184:187], v5 offset:4096\n"
"v_add_f32_e32 v158, v158, v118\n"
"v_mfma_f32_16x16x32_bf16 v[160:163], v[64:67], v[148:151], v[160:163]\n"
"buffer_load_dwordx4 v[148:151], v1, s[4:7], s97 offen offset:3072 sc1\n"
"v_mfma_f32_16x16x32_bf16 v[160:163], v[60:63], v[108:111], v[160:163]\n"
"ds_write_b128 v3, v[212:215] offset:16384\n"
"v_mfma_f32_16x16x32_bf16 v[160:163], v[56:59], v[176:179], v[160:163]\n"
"s_waitcnt vmcnt(19)\n"
"ds_read_b128 v[108:111], v4 offset:5120\n"
"ds_read_b128 v[176:179], v9 offset:576\n"
"; @B G1 0.3\n"
"v_mfma_f32_16x16x32_bf16 v[196:199], a[0:3], v[92:95], v[136:139]\n"
"buffer_load_dwordx4 a[148:151], v1, s[4:7], s98 offen offset:1024 sc1\n"
"v_mfma_f32_16x16x32_bf16 v[196:199], a[4:7], v[96:99], v[196:199]\n"
"ds_read_b128 v[200:203], v5 offset:5120\n"
"v_add_f32_e32 v159, v159, v119\n"
"v_mfma_f32_16x16x32_bf16 v[196:199], a[8:11], v[104:107], v[196:199]\n"
"s_add_u32 s97, s53, 0x448000\n"
"buffer_load_dwordx4 v[116:119], v0, s[4:7], s97 offen sc1\n"
"v_mfma_f32_16x16x32_bf16 v[196:199], a[12:15], v[112:115], v[196:199]\n"
"s_waitcnt vmcnt(15)\n"
"s_waitcnt lgkmcnt(12)\n"
"v_fmac_f32_e32 v188, v253, v204\n"
"v_mfma_f32_16x16x32_bf16 v[196:199], a[16:19], v[100:103], v[196:199]\n"
"v_fmac_f32_e32 v189, v253, v205\n"
"v_fmac_f32_e32 v190, v253, v206\n"
"v_mfma_f32_16x16x32_bf16 v[196:199], a[20:23], v[124:127], v[196:199]\n"
"ds_write_b128 v3, v[160:163] offset:17408\n"
"v_mfma_f32_16x16x32_bf16 v[196:199], a[24:27], v[168:171], v[196:199]\n"
"v_fmac_f32_e32 v191, v253, v207\n"
"v_add_f32_e32 v188, v188, v208\n"
"v_mfma_f32_16x16x32_bf16 v[196:199], a[28:31], v[172:175], v[196:199]\n"
"buffer_load_dwordx4 v[160:163], v0, s[4:7], s97 offen offset:1024 sc1\n"
"v_mfma_f32_16x16x32_bf16 v[204:207], a[60:63], v[172:175], v[156:159]\n"
"v_add_f32_e32 v189, v189, v209\n"
"v_add_f32_e32 v190, v190, v210\n"
"v_mfma_f32_16x16x32_bf16 v[204:207], a[56:59], v[168:171], v[204:207]\n"
"s_waitcnt lgkmcnt(9)\n"
"v_add_f32_e32 v191, v191, v211\n"
"v_fmac_f32_e32 v144, v253, v216\n"
"v_mfma_f32_16x16x32_bf16 v[204:207], a[52:55], v[124:127], v[204:207]\n"
"s_waitcnt lgkmcnt(0)\n"
"s_barrier\n"
"v_mfma_f32_16x16x32_bf16 v[204:207], a[48:51], v[100:103], v[204:207]\n"
"ds_read_b128 v[208:211], v6\n"
"ds_read_b128 v[212:215], v6 offset:1024\n"
"v_mfma_f32_16x16x32_bf16 v[204:207], a[44:47], v[112:115], v[204:207]\n"
"v_cmp_u_f32_e32 vcc, v196, v196\n"
"v_fmac_f32_e32 v145, v253, v217\n"
"v_mfma_f32_16x16x32_bf16 v[204:207], a[40:43], v[104:107], v[204:207]\n"
"s_cbranch_vccnz .Lmk_p46s\n"
".Lmk_p46r:\n"
"v_fmac_f32_e32 v146, v253, v218\n"
"v_mfma_f32_16x16x32_bf16 v[204:207], a[36:39], v[96:99], v[204:207]\n"
"buffer_load_dwordx4 v[136:139], v0, s[4:7], s97 offen offset:2048 sc1\n"
"v_mfma_f32_16x16x32_bf16 v[204:207], a[32:35], v[92:95], v[204:207]\n"
"v_fmac_f32_e32 v147, v253, v219\n"
"v_add_f32_e32 v144, v144, v220\n"
"v_mfma_f32_16x16x32_bf16 v[188:191], a[64:67], v[92:95], v[188:191]\n"
"v_add_f32_e32 v145, v145, v221\n"
"v_add_f32_e32 v146, v146, v222\n"
"v_mfma_f32_16x16x32_bf16 v[188:191], a[68:71], v[96:99], v[188:191]\n"
"buffer_load_dwordx4 v[156:159], v0, s[4:7], s97 offen offset:3072 sc1\n"
"v_mfma_f32_16x16x32_bf16 v[188:191], a[72:75], v[104:107], v[188:191]\n"
"v_add_f32_e32 v147, v147, v223\n"
"v_fmac_f32_e32 v152, v253, v192\n"
"v_mfma_f32_16x16x32_bf16 v[188:191], a[76:79], v[112:115], v[188:191]\n"
"v_fmac_f32_e32 v153, v253, v193\n"
"v_fmac_f32_e32 v154, v253, v194\n"
"v_mfma_f32_16x16x32_bf16 v[188:191], a[80:83], v[100:103], v[188:191]\n"
"buffer_load_dwordx4 a[152:155], v1, s[4:7], s98 offen offset:2048 sc1\n"
"v_mfma_f32_16x16x32_bf16 v[188:191], a[84:87], v[124:127], v[188:191]\n"
"s_waitcnt lgkmcnt(0)\n"
"v_fmac_f32_e32 v155, v253, v195\n"
"v_add_f32_e32 v152, v152, v184\n"
"v_mfma_f32_16x16x32_bf16 v[188:191], a[88:91], v[168:171], v[188:191]\n"
"buffer_load_dwordx4 v[192:195], v1, s[4:7], s97 offen sc1\n"
"v_mfma_f32_16x16x32_bf16 v[188:191], a[92:95], v[172:175], v[188:191]\n"
"ds_read_b128 v[216:219], v6 offset:2048\n"
"ds_read_b128 v[220:223], v6 offset:3072\n"
"v_mfma_f32_16x16x32_bf16 v[144:147], a[124:127], v[172:175], v[144:147]\n"
"v_add_f32_e32 v153, v153, v185\n"
"v_add_f32_e32 v154, v154, v186\n"
"v_mfma_f32_16x16x32_bf16 v[144:147], a[120:123], v[168:171], v[144:147]\n"
"v_add_f32_e32 v155, v155, v187\n"
"v_fmac_f32_e32 v108, v253, v176\n"
"v_mfma_f32_16x16x32_bf16 v[144:147], a[116:119], v[124:127], v[144:147]\n"
"v_fmac_f32_e32 v109, v253, v177\n"
"v_fmac_f32_e32 v110, v253, v178\n"
"v_mfma_f32_16x16x32_bf16 v[144:147], a[112:115], v[100:103], v[144:147]\n"
"buffer_load_dwordx4 v[184:187], v1, s[4:7], s97 offen offset:1024 sc1\n"
"v_mfma_f32_16x16x32_bf16 v[144:147], a[108:111], v[112:115], v[144:147]\n"
"v_fmac_f32_e32 v111, v253, v179\n"
"v_add_f32_e32 v108, v108, v200\n"
"v_mfma_f32_16x16x32_bf16 v[144:147], a[104:107], v[104:107], v[144:147]\n"
"v_add_f32_e32 v109, v109, v201\n"
"v_add_f32_e32 v110, v110, v202\n"
"v_mfma_f32_16x16x32_bf16 v[144:147], a[100:103], v[96:99], v[144:147]\n"
"v_add_f32_e32 v111, v111, v203\n"
"v_add_f32_e32 v196, v196, v208\n"
"v_mfma_f32_16x16x32_bf16 v[144:147], a[96:99], v[92:95], v[144:147]\n"
"v_add_f32_e32 v197, v197, v209\n"
"v_add_f32_e32 v198, v198, v210\n"
"v_mfma_f32_16x16x32_bf16 v[152:155], v[24:27], v[92:95], v[152:155]\n"
"buffer_load_dwordx4 v[176:179], v1, s[4:7], s97 offen offset:2048 sc1\n"
"v_mfma_f32_16x16x32_bf16 v[152:155], v[28:31], v[96:99], v[152:155]\n"
"v_add_f32_e32 v199, v199, v211\n"
"v_add_f32_e32 v204, v204, v212\n"
"v_mfma_f32_16x16x32_bf16 v[152:155], v[32:35], v[104:107], v[152:155]\n"
"v_add_f32_e32 v205, v205, v213\n"
"v_add_f32_e32 v206, v206, v214\n"
"v_mfma_f32_16x16x32_bf16 v[152:155], v[36:39], v[112:115], v[152:155]\n"
"v_add_f32_e32 v207, v207, v215\n"
"v_mul_f32_e32 v196, v196, v255\n"
"v_mfma_f32_16x16x32_bf16 v[152:155], v[40:43], v[100:103], v[152:155]\n"
"s_waitcnt lgkmcnt(0)\n"
"v_mul_f32_e32 v197, v197, v255\n"
"v_mul_f32_e32 v198, v198, v255\n"
"v_mfma_f32_16x16x32_bf16 v[152:155], v[44:47], v[124:127], v[152:155]\n"
"buffer_load_dwordx4 v[200:203], v1, s[4:7], s97 offen offset:3072 sc1\n"
"v_mfma_f32_16x16x32_bf16 v[152:155], v[48:51], v[168:171], v[152:155]\n"
"s_add_u32 s97, s60, 0x408000\n"
"ds_read_b128 v[208:211], v6 offset:4096\n"
"ds_read_b128 v[212:215], v6 offset:5120\n"
"v_mfma_f32_16x16x32_bf16 v[152:155], v[52:55], v[172:175], v[152:155]\n"
"v_mul_f32_e32 v199, v199, v255\n"
"v_mul_f32_e32 v204, v204, v255\n"
"v_mfma_f32_16x16x32_bf16 v[108:111], v[84:87], v[172:175], v[108:111]\n"
"buffer_store_dwordx4 v[120:123], v0, s[4:7], s97 offen sc1\n"
"v_mfma_f32_16x16x32_bf16 v[108:111], v[80:83], v[168:171], v[108:111]\n"
"v_mul_f32_e32 v205, v205, v255\n"
"v_mul_f32_e32 v206, v206, v255\n"
"v_mfma_f32_16x16x32_bf16 v[108:111], v[76:79], v[124:127], v[108:111]\n"
"v_mul_f32_e32 v207, v207, v255\n"
"v_cvt_pk_bf16_f32 v120, v196, v197\n"
"v_mfma_f32_16x16x32_bf16 v[108:111], v[72:75], v[100:103], v[108:111]\n"
"v_cvt_pk_bf16_f32 v121, v198, v199\n"
"v_cvt_pk_bf16_f32 v122, v204, v205\n"
"v_mfma_f32_16x16x32_bf16 v[108:111], v[68:71], v[112:115], v[108:111]\n"
"v_cvt_pk_bf16_f32 v123, v206, v207\n"
"v_add_f32_e32 v188, v188, v216\n"
"v_mfma_f32_16x16x32_bf16 v[108:111], v[64:67], v[104:107], v[108:111]\n"
"ds_write_b128 v7, v[120:123] offset:24576\n"
"v_mfma_f32_16x16x32_bf16 v[108:111], v[60:63], v[96:99], v[108:111]\n"
"v_add_f32_e32 v189, v189, v217\n"
"v_add_f32_e32 v190, v190, v218\n"
"v_mfma_f32_16x16x32_bf16 v[108:111], v[56:59], v[92:95], v[108:111]\n"
"v_add_f32_e32 v191, v191, v219\n"
"v_add_f32_e32 v144, v144, v220\n"
"v_add_f32_e32 v145, v145, v221\n"
"v_add_f32_e32 v146, v146, v222\n"
"v_add_f32_e32 v147, v147, v223\n"
"v_mul_f32_e32 v188, v188, v255\n"
"v_mul_f32_e32 v189, v189, v255\n"
"v_mul_f32_e32 v190, v190, v255\n"
"v_mul_f32_e32 v191, v191, v255\n"
"v_mul_f32_e32 v144, v144, v255\n"
"v_mul_f32_e32 v145, v145, v255\n"
"v_mul_f32_e32 v146, v146, v255\n"
"v_mul_f32_e32 v147, v147, v255\n"
"v_cvt_pk_bf16_f32 v92, v188, v189\n"
"v_cvt_pk_bf16_f32 v93, v190, v191\n"
"v_cvt_pk_bf16_f32 v94, v144, v145\n"
"v_cvt_pk_bf16_f32 v95, v146, v147\n"
"ds_write_b128 v7, v[92:95] offset:57344\n"
".Lmk_b1:\n"
"s_waitcnt vmcnt(18)\n"
"; @B G1 1.0\n"
"v_mfma_f32_16x16x32_bf16 v[92:95], a[0:3], v[128:131], 0\n"
"v_mfma_f32_16x16x32_bf16 v[92:95], a[4:7], v[88:91], v[92:95]\n"
"s_add_u32 s97, s62, 0x44800\n"
"v_mfma_f32_16x16x32_bf16 v[92:95], a[8:11], v[164:167], v[92:95]\n"
"s_add_u32 s96, s54, 0x448000\n"
"buffer_load_dwordx4 v[96:99], v0, s[4:7], s96 offen sc1\n"
"v_mfma_f32_16x16x32_bf16 v[92:95], a[12:15], v[140:143], v[92:95]\n"
"s_waitcnt vmcnt(12)\n"
"v_mfma_f32_16x16x32_bf16 v[92:95], a[16:19], v[132:135], v[92:95]\n"
"buffer_load_dword v255, v10, s[4:7], s97 offen sc1\n"
"v_mfma_f32_16x16x32_bf16 v[92:95], a[20:23], v[224:227], v[92:95]\n"
"s_waitcnt lgkmcnt(2)\n"
"v_add_f32_e32 v152, v152, v208\n"
"v_add_f32_e32 v153, v153, v209\n"
"v_mfma_f32_16x16x32_bf16 v[92:95], a[24:27], v[180:183], v[92:95]\n"
"v_add_f32_e32 v154, v154, v210\n"
"v_add_f32_e32 v155, v155, v211\n"
"v_mfma_f32_16x16x32_bf16 v[92:95], a[28:31], v[148:151], v[92:95]\n"
"buffer_load_dwordx4 v[100:103], v0, s[4:7], s96 offen offset:1024 sc1\n"
"v_mfma_f32_16x16x32_bf16 v[104:107], a[60:63], v[148:151], 0\n"
"buffer_load_dwordx4 a[156:159], v1, s[4:7], s98 offen offset:3072 sc1\n"
"v_mfma_f32_16x16x32_bf16 v[104:107], a[56:59], v[180:183], v[104:107]\n"
"buffer_load_dword v253, v10, s[4:7], s97 offen offset:256 sc1\n"
"v_mfma_f32_16x16x32_bf16 v[104:107], a[52:55], v[224:227], v[104:107]\n"
"v_add_f32_e32 v108, v108, v212\n"
"v_add_f32_e32 v109, v109, v213\n"
"v_mfma_f32_16x16x32_bf16 v[104:107], a[48:51], v[132:135], v[104:107]\n"
"v_add_f32_e32 v110, v110, v214\n"
"v_add_f32_e32 v111, v111, v215\n"
"v_mfma_f32_16x16x32_bf16 v[104:107], a[44:47], v[140:143], v[104:107]\n"
"v_cmp_u_f32_e32 vcc, v92, v92\n"
"v_mul_f32_e32 v152, v152, v254\n"
"v_mfma_f32_16x16x32_bf16 v[104:107], a[40:43], v[164:167], v[104:107]\n"
"s_cbranch_vccnz .Lmk_p48s\n"
".Lmk_p48r:\n"
"v_mul_f32_e32 v153, v153, v254\n"
"v_mfma_f32_16x16x32_bf16 v[104:107], a[36:39], v[88:91], v[104:107]\n"
"buffer_load_dwordx4 v[112:115], v0, s[4:7], s96 offen offset:2048 sc1\n"
"v_mfma_f32_16x16x32_bf16 v[104:107], a[32:35], v[128:131], v[104:107]\n"
"ds_write_b128 v3, v[92:95]\n"
"v_mfma_f32_16x16x32_bf16 v[92:95], a[64:67], v[128:131], 0\n"
"buffer_load_dword v252, v10, s[4:7], s97 offen offset:512 sc1\n"
"v_mfma_f32_16x16x32_bf16 v[92:95], a[68:71], v[88:91], v[92:95]\n"
"buffer_load_dwordx4 v[120:123], v0, s[4:7], s96 offen offset:3072 sc1\n"
"v_mfma_f32_16x16x32_bf16 v[92:95], a[72:75], v[164:167], v[92:95]\n"
"v_mul_f32_e32 v154, v154, v254\n"
"v_mul_f32_e32 v155, v155, v254\n"
"v_mfma_f32_16x16x32_bf16 v[92:95], a[76:79], v[140:143], v[92:95]\n"
"buffer_load_dword v251, v10, s[4:7], s97 offen offset:768 sc1\n"
"v_mfma_f32_16x16x32_bf16 v[92:95], a[80:83], v[132:135], v[92:95]\n"
"v_mul_f32_e32 v108, v108, v254\n"
"v_mul_f32_e32 v109, v109, v254\n"
"v_mfma_f32_16x16x32_bf16 v[92:95], a[84:87], v[224:227], v[92:95]\n"
"ds_write_b128 v3, v[104:107] offset:1024\n"
"v_mfma_f32_16x16x32_bf16 v[92:95], a[88:91], v[180:183], v[92:95]\n"
"buffer_load_dwordx4 v[104:107], v1, s[4:7], s96 offen sc1\n"
"v_mfma_f32_16x16x32_bf16 v[92:95], a[92:95], v[148:151], v[92:95]\n"
"buffer_load_dword v250, v10, s[4:7], s97 offen offset:1024 sc1\n"
"v_mfma_f32_16x16x32_bf16 v[124:127], a[124:127], v[148:151], 0\n"
"v_mul_f32_e32 v110, v110, v254\n"
"v_mul_f32_e32 v111, v111, v254\n"
"v_mfma_f32_16x16x32_bf16 v[124:127], a[120:123], v[180:183], v[124:127]\n"
"v_cvt_pk_bf16_f32 v144, v152, v153\n"
"v_cvt_pk_bf16_f32 v145, v154, v155\n"
"v_mfma_f32_16x16x32_bf16 v[124:127], a[116:119], v[224:227], v[124:127]\n"
"s_add_u32 s98, s70, 0x202000\n"
"buffer_load_dword v254, v10, s[4:7], s97 offen offset:1280 sc1\n"
"v_mfma_f32_16x16x32_bf16 v[124:127], a[112:115], v[132:135], v[124:127]\n"
"buffer_load_dwordx4 v[152:155], v1, s[4:7], s96 offen offset:1024 sc1\n"
"v_mfma_f32_16x16x32_bf16 v[124:127], a[108:111], v[140:143], v[124:127]\n"
"buffer_load_dwordx4 a[160:163], v0, s[4:7], s98 offen sc1\n"
"v_mfma_f32_16x16x32_bf16 v[124:127], a[104:107], v[164:167], v[124:127]\n"
"ds_write_b128 v3, v[92:95] offset:2048\n"
"v_mfma_f32_16x16x32_bf16 v[124:127], a[100:103], v[88:91], v[124:127]\n"
"buffer_load_dword v249, v10, s[4:7], s97 offen offset:1536 sc1\n"
"v_mfma_f32_16x16x32_bf16 v[124:127], a[96:99], v[128:131], v[124:127]\n"
"v_cvt_pk_bf16_f32 v146, v108, v109\n"
"v_cvt_pk_bf16_f32 v147, v110, v111\n"
"v_mfma_f32_16x16x32_bf16 v[92:95], v[24:27], v[128:131], 0\n"
"buffer_load_dwordx4 v[108:111], v1, s[4:7], s96 offen offset:2048 sc1\n"
"v_mfma_f32_16x16x32_bf16 v[92:95], v[28:31], v[88:91], v[92:95]\n"
"buffer_load_dword v248, v10, s[4:7], s97 offen offset:1792 sc1\n"
"v_mfma_f32_16x16x32_bf16 v[92:95], v[32:35], v[164:167], v[92:95]\n"
"ds_write_b128 v2, v[144:147] offset:49152\n"
"v_mfma_f32_16x16x32_bf16 v[92:95], v[36:39], v[140:143], v[92:95]\n"
"v_mfma_f32_16x16x32_bf16 v[92:95], v[40:43], v[132:135], v[92:95]\n"
"v_mfma_f32_16x16x32_bf16 v[92:95], v[44:47], v[224:227], v[92:95]\n"
"buffer_load_dwordx4 v[144:147], v1, s[4:7], s96 offen offset:3072 sc1\n"
"v_mfma_f32_16x16x32_bf16 v[92:95], v[48:51], v[180:183], v[92:95]\n"
"ds_write_b128 v3, v[124:127] offset:3072\n"
"v_mfma_f32_16x16x32_bf16 v[92:95], v[52:55], v[148:151], v[92:95]\n"
"v_mfma_f32_16x16x32_bf16 v[124:127], v[84:87], v[148:151], 0\n"
"v_mfma_f32_16x16x32_bf16 v[124:127], v[80:83], v[180:183], v[124:127]\n"
"v_mfma_f32_16x16x32_bf16 v[124:127], v[76:79], v[224:227], v[124:127]\n"
"v_mfma_f32_16x16x32_bf16 v[124:127], v[72:75], v[132:135], v[124:127]\n"
"v_mfma_f32_16x16x32_bf16 v[124:127], v[68:71], v[140:143], v[124:127]\n"
"v_mfma_f32_16x16x32_bf16 v[124:127], v[64:67], v[164:167], v[124:127]\n"
"s_nop 1\n"
"ds_write_b128 v3, v[92:95] offset:4096\n"
"v_mfma_f32_16x16x32_bf16 v[124:127], v[60:63], v[88:91], v[124:127]\n"
"v_mfma_f32_16x16x32_bf16 v[124:127], v[56:59], v[128:131], v[124:127]\n"
"s_waitcnt vmcnt(24)\n"
"; @B G1 1.1\n"
"v_mfma_f32_16x16x32_bf16 v[88:91], a[0:3], v[116:119], 0\n"
"buffer_load_dwordx4 a[164:167], v0, s[4:7], s98 offen offset:1024 sc1\n"
"v_mfma_f32_16x16x32_bf16 v[88:91], a[4:7], v[160:163], v[88:91]\n"
"v_mfma_f32_16x16x32_bf16 v[88:91], a[8:11], v[136:139], v[88:91]\n"
"s_add_u32 s97, s55, 0x448000\n"
"buffer_load_dwordx4 v[92:95], v0, s[4:7], s97 offen sc1\n"
"v_mfma_f32_16x16x32_bf16 v[88:91], a[12:15], v[156:159], v[88:91]\n"
"s_waitcnt vmcnt(21)\n"
"v_mfma_f32_16x16x32_bf16 v[88:91], a[16:19], v[192:195], v[88:91]\n"
"v_mfma_f32_16x16x32_bf16 v[88:91], a[20:23], v[184:187], v[88:91]\n"
"ds_write_b128 v3, v[124:127] offset:5120\n"
"v_mfma_f32_16x16x32_bf16 v[88:91], a[24:27], v[176:179], v[88:91]\n"
"v_mfma_f32_16x16x32_bf16 v[88:91], a[28:31], v[200:203], v[88:91]\n"
"buffer_load_dwordx4 v[124:127], v0, s[4:7], s97 offen offset:1024 sc1\n"
"v_mfma_f32_16x16x32_bf16 v[128:131], a[60:63], v[200:203], 0\n"
"v_mfma_f32_16x16x32_bf16 v[128:131], a[56:59], v[176:179], v[128:131]\n"
"v_mfma_f32_16x16x32_bf16 v[128:131], a[52:55], v[184:187], v[128:131]\n"
"v_mfma_f32_16x16x32_bf16 v[128:131], a[48:51], v[192:195], v[128:131]\n"
"v_mfma_f32_16x16x32_bf16 v[128:131], a[44:47], v[156:159], v[128:131]\n"
"s_add_u32 s96, s67, 0x10000\n"
"s_nop 0\n"
"v_cmp_u_f32_e32 vcc, v88, v88\n"
"v_mfma_f32_16x16x32_bf16 v[128:131], a[40:43], v[136:139], v[128:131]\n"
"s_cbranch_vccnz .Lmk_p49s\n"
".Lmk_p49r:\n"
"v_mfma_f32_16x16x32_bf16 v[128:131], a[36:39], v[160:163], v[128:131]\n"
"buffer_load_dwordx4 v[132:135], v0, s[4:7], s97 offen offset:2048 sc1\n"
"v_mfma_f32_16x16x32_bf16 v[128:131], a[32:35], v[116:119], v[128:131]\n"
"ds_write_b128 v3, v[88:91] offset:6144\n"
"v_mfma_f32_16x16x32_bf16 v[88:91], a[64:67], v[116:119], 0\n"
"buffer_load_dwordx2 v[246:247], v12, s[4:7], s96 offen sc1\n"
"v_mfma_f32_16x16x32_bf16 v[88:91], a[68:71], v[160:163], v[88:91]\n"
"buffer_load_dwordx4 v[140:143], v0, s[4:7], s97 offen offset:3072 sc1\n"
"v_mfma_f32_16x16x32_bf16 v[88:91], a[72:75], v[136:139], v[88:91]\n"
"v_mfma_f32_16x16x32_bf16 v[88:91], a[76:79], v[156:159], v[88:91]\n"
"v_mfma_f32_16x16x32_bf16 v[88:91], a[80:83], v[192:195], v[88:91]\n"
"v_mov_b32_e32 v148, -1\n"
"v_mov_b32_e32 v149, -1\n"
"v_mfma_f32_16x16x32_bf16 v[88:91], a[84:87], v[184:187], v[88:91]\n"
"ds_write_b128 v3, v[128:131] offset:7168\n"
"v_mfma_f32_16x16x32_bf16 v[88:91], a[88:91], v[176:179], v[88:91]\n"
"buffer_load_dwordx4 v[128:131], v1, s[4:7], s97 offen sc1\n"
"v_mfma_f32_16x16x32_bf16 v[88:91], a[92:95], v[200:203], v[88:91]\n"
"v_mov_b32_e32 v150, -1\n"
"v_mov_b32_e32 v151, -1\n"
"v_mfma_f32_16x16x32_bf16 v[164:167], a[124:127], v[200:203], 0\n"
"s_add_u32 s95, s61, 0x840000\n"
"buffer_store_dwordx4 v[148:151], v0, s[8:11], s95 offen sc1\n"
"v_mfma_f32_16x16x32_bf16 v[164:167], a[120:123], v[176:179], v[164:167]\n"
"buffer_load_dwordx4 a[168:171], v0, s[4:7], s98 offen offset:2048 sc1\n"
"v_mfma_f32_16x16x32_bf16 v[164:167], a[116:119], v[184:187], v[164:167]\n"
"v_mfma_f32_16x16x32_bf16 v[164:167], a[112:115], v[192:195], v[164:167]\n"
"buffer_load_dwordx4 v[168:171], v1, s[4:7], s97 offen offset:1024 sc1\n"
"v_mfma_f32_16x16x32_bf16 v[164:167], a[108:111], v[156:159], v[164:167]\n"
"v_mfma_f32_16x16x32_bf16 v[164:167], a[104:107], v[136:139], v[164:167]\n"
"ds_write_b128 v3, v[88:91] offset:8192\n"
"v_mfma_f32_16x16x32_bf16 v[164:167], a[100:103], v[160:163], v[164:167]\n"
"v_mfma_f32_16x16x32_bf16 v[164:167], a[96:99], v[116:119], v[164:167]\n"
"v_mfma_f32_16x16x32_bf16 v[88:91], v[24:27], v[116:119], 0\n"
"buffer_load_dwordx4 v[172:175], v1, s[4:7], s97 offen offset:2048 sc1\n"
"v_mfma_f32_16x16x32_bf16 v[88:91], v[28:31], v[160:163], v[88:91]\n"
"v_mfma_f32_16x16x32_bf16 v[88:91], v[32:35], v[136:139], v[88:91]\n"
"v_mfma_f32_16x16x32_bf16 v[88:91], v[36:39], v[156:159], v[88:91]\n"
"v_mfma_f32_16x16x32_bf16 v[88:91], v[40:43], v[192:195], v[88:91]\n"
"s_waitcnt vmcnt(12)\n"
"v_add_f32_e32 v255, v255, v250\n"
"v_add_f32_e32 v253, v253, v254\n"
"v_mfma_f32_16x16x32_bf16 v[88:91], v[44:47], v[184:187], v[88:91]\n"
"buffer_load_dwordx4 v[180:183], v1, s[4:7], s97 offen offset:3072 sc1\n"
"v_mfma_f32_16x16x32_bf16 v[88:91], v[48:51], v[176:179], v[88:91]\n"
"ds_write_b128 v3, v[164:167] offset:9216\n"
"v_mfma_f32_16x16x32_bf16 v[88:91], v[52:55], v[200:203], v[88:91]\n"
"v_add_f32_e32 v252, v252, v249\n"
"v_add_f32_e32 v251, v251, v248\n"
"v_mfma_f32_16x16x32_bf16 v[164:167], v[84:87], v[200:203], 0\n"
"v_add_f32_e32 v255, v255, v252\n"
"v_add_f32_e32 v253, v253, v251\n"
"v_mfma_f32_16x16x32_bf16 v[164:167], v[80:83], v[176:179], v[164:167]\n"
"v_add_f32_e32 v255, v255, v253\n"
"v_cmp_u_f32_e32 vcc, v255, v255\n"
"v_mfma_f32_16x16x32_bf16 v[164:167], v[76:79], v[184:187], v[164:167]\n"
"s_cbranch_vccnz .Lmk_m52s\n"
".Lmk_m52r:\n"
"v_mov_b32_e32 v253, v255\n"
"v_mfma_f32_16x16x32_bf16 v[164:167], v[72:75], v[192:195], v[164:167]\n"
"s_nop 0\n"
"v_permlane32_swap_b32_e32 v255, v253\n"
"v_add_f32_e32 v255, v255, v253\n"
"v_mfma_f32_16x16x32_bf16 v[164:167], v[68:71], v[156:159], v[164:167]\n"
"v_mul_f32_e32 v255, 0x3a800000, v255\n"
"v_mov_b32_e32 v253, v255\n"
"v_mfma_f32_16x16x32_bf16 v[164:167], v[64:67], v[136:139], v[164:167]\n"
"ds_write_b128 v3, v[88:91] offset:10240\n"
"v_mfma_f32_16x16x32_bf16 v[164:167], v[60:63], v[160:163], v[164:167]\n"
"buffer_load_dwordx4 a[172:175], v0, s[4:7], s98 offen offset:3072 sc1\n"
"v_mfma_f32_16x16x32_bf16 v[164:167], v[56:59], v[116:119], v[164:167]\n"
"v_permlane16_swap_b32_e32 v255, v253\n"
"v_cvt_pk_bf16_f32 v254, -v255, 0\n"
"; @B G1 1.2\n"
"v_mfma_f32_16x16x32_bf16 v[88:91], a[0:3], v[96:99], 0\n"
"v_fma_f32 v253, -v255, v255, v253\n"
"v_lshlrev_b32_e32 v254, 16, v254\n"
"v_mfma_f32_16x16x32_bf16 v[88:91], a[4:7], v[100:103], v[88:91]\n"
"s_waitcnt vmcnt(8)\n"
"v_cmp_ne_u64_e32 vcc, v[246:247], v[18:19]\n"
"v_mfma_f32_16x16x32_bf16 v[88:91], a[8:11], v[112:115], v[88:91]\n"
"s_cbranch_vccnz .Lmk_f39s\n"
".Lmk_f39r:\n"
"v_max_f32_e32 v253, 0, v253\n"
"v_mfma_f32_16x16x32_bf16 v[88:91], a[12:15], v[120:123], v[88:91]\n"
"v_cvt_pk_bf16_f32 v253, v253, 0\n"
"v_lshlrev_b32_e32 v253, 16, v253\n"
"v_mfma_f32_16x16x32_bf16 v[88:91], a[16:19], v[104:107], v[88:91]\n"
"v_add_f32_e32 v253, 0x3727c5ac, v253\n"
"v_cvt_pk_bf16_f32 v253, v253, 0\n"
"v_mfma_f32_16x16x32_bf16 v[88:91], a[20:23], v[152:155], v[88:91]\n"
"ds_write_b128 v3, v[164:167] offset:11264\n"
"v_mfma_f32_16x16x32_bf16 v[88:91], a[24:27], v[108:111], v[88:91]\n"
"v_lshlrev_b32_e32 v253, 16, v253\n"
"v_mfma_f32_16x16x32_bf16 v[88:91], a[28:31], v[144:147], v[88:91]\n"
"v_sqrt_f32_e32 v253, v253\n"
"v_mfma_f32_16x16x32_bf16 v[116:119], a[60:63], v[144:147], 0\n"
"v_cvt_pk_bf16_f32 v253, v253, 0\n"
"v_lshlrev_b32_e32 v253, 16, v253\n"
"v_mfma_f32_16x16x32_bf16 v[116:119], a[56:59], v[108:111], v[116:119]\n"
"v_rcp_f32_e32 v255, v253\n"
"v_mfma_f32_16x16x32_bf16 v[116:119], a[52:55], v[152:155], v[116:119]\n"
"s_add_u32 s97, s52, 0x408000\n"
"buffer_load_dwordx4 v[136:139], v0, s[4:7], s97 offen sc1\n"
"v_mfma_f32_16x16x32_bf16 v[116:119], a[48:51], v[104:107], v[116:119]\n"
"s_waitcnt lgkmcnt(0)\n"
"s_barrier\n"
"v_mfma_f32_16x16x32_bf16 v[116:119], a[44:47], v[120:123], v[116:119]\n"
"v_cmp_u_f32_e32 vcc, v88, v88\n"
"v_mul_f32_e32 v253, 0x3e8293ee, v255\n"
"v_mfma_f32_16x16x32_bf16 v[116:119], a[40:43], v[112:115], v[116:119]\n"
"s_cbranch_vccnz .Lmk_p50s\n"
".Lmk_p50r:\n"
"ds_read_b128 v[156:159], v4\n"
"v_mfma_f32_16x16x32_bf16 v[116:119], a[36:39], v[100:103], v[116:119]\n"
"ds_write_b128 v3, v[88:91] offset:12288\n"
"v_mfma_f32_16x16x32_bf16 v[116:119], a[32:35], v[96:99], v[116:119]\n"
"buffer_load_dwordx4 v[88:91], v0, s[4:7], s97 offen offset:1024 sc1\n"
"v_mfma_f32_16x16x32_bf16 v[160:163], a[64:67], v[96:99], 0\n"
"ds_read_b128 v[164:167], v9 offset:640\n"
"ds_read_b128 v[176:179], v5\n"
"v_mfma_f32_16x16x32_bf16 v[160:163], a[68:71], v[100:103], v[160:163]\n"
"v_mfma_f32_16x16x32_bf16 v[160:163], a[72:75], v[112:115], v[160:163]\n"
"buffer_load_dwordx4 a[176:179], v1, s[4:7], s98 offen sc1\n"
"v_mfma_f32_16x16x32_bf16 v[160:163], a[76:79], v[120:123], v[160:163]\n"
"ds_read_b128 v[184:187], v4 offset:1024\n"
"ds_read_b128 v[188:191], v9 offset:704\n"
"v_mfma_f32_16x16x32_bf16 v[160:163], a[80:83], v[104:107], v[160:163]\n"
"buffer_load_dwordx4 v[192:195], v0, s[4:7], s97 offen offset:2048 sc1\n"
"v_mfma_f32_16x16x32_bf16 v[160:163], a[84:87], v[152:155], v[160:163]\n"
"ds_write_b128 v3, v[116:119] offset:13312\n"
"v_mfma_f32_16x16x32_bf16 v[160:163], a[88:91], v[108:111], v[160:163]\n"
"ds_read_b128 v[116:119], v5 offset:1024\n"
"s_waitcnt lgkmcnt(4)\n"
"v_mfma_f32_16x16x32_bf16 v[160:163], a[92:95], v[144:147], v[160:163]\n"
"v_fmac_f32_e32 v156, v254, v164\n"
"v_fmac_f32_e32 v157, v254, v165\n"
"v_mfma_f32_16x16x32_bf16 v[196:199], a[124:127], v[144:147], 0\n"
"v_fmac_f32_e32 v158, v254, v166\n"
"v_fmac_f32_e32 v159, v254, v167\n"
"v_mfma_f32_16x16x32_bf16 v[196:199], a[120:123], v[108:111], v[196:199]\n"
"buffer_load_dwordx4 v[164:167], v0, s[4:7], s97 offen offset:3072 sc1\n"
"v_mfma_f32_16x16x32_bf16 v[196:199], a[116:119], v[152:155], v[196:199]\n"
"ds_read_b128 v[200:203], v4 offset:2048\n"
"ds_read_b128 v[204:207], v9 offset:768\n"
"v_mfma_f32_16x16x32_bf16 v[196:199], a[112:115], v[104:107], v[196:199]\n"
"ds_read_b128 v[208:211], v5 offset:2048\n"
"v_add_f32_e32 v156, v156, v176\n"
"v_mfma_f32_16x16x32_bf16 v[196:199], a[108:111], v[120:123], v[196:199]\n"
"v_add_f32_e32 v157, v157, v177\n"
"v_add_f32_e32 v158, v158, v178\n"
"v_mfma_f32_16x16x32_bf16 v[196:199], a[104:107], v[112:115], v[196:199]\n"
"ds_write_b128 v3, v[160:163] offset:14336\n"
"v_mfma_f32_16x16x32_bf16 v[196:199], a[100:103], v[100:103], v[196:199]\n"
"buffer_load_dwordx4 v[160:163], v1, s[4:7], s97 offen sc1\n"
"v_mfma_f32_16x16x32_bf16 v[196:199], a[96:99], v[96:99], v[196:199]\n"
"s_add_u32 s96, s61, 0x840000\n"
"v_add_f32_e32 v159, v159, v179\n"
"s_waitcnt lgkmcnt(4)\n"
"v_mfma_f32_16x16x32_bf16 v[176:179], v[24:27], v[96:99], 0\n"
"buffer_store_dwordx4 v[148:151], v0, s[8:11], s96 offen offset:1024 sc1\n"
"v_mfma_f32_16x16x32_bf16 v[176:179], v[28:31], v[100:103], v[176:179]\n"
"ds_read_b128 v[212:215], v4 offset:3072\n"
"ds_read_b128 v[216:219], v9 offset:832\n"
"v_mfma_f32_16x16x32_bf16 v[176:179], v[32:35], v[112:115], v[176:179]\n"
"ds_read_b128 v[220:223], v5 offset:3072\n"
"v_fmac_f32_e32 v184, v254, v188\n"
"v_mfma_f32_16x16x32_bf16 v[176:179], v[36:39], v[120:123], v[176:179]\n"
"buffer_load_dwordx4 v[224:227], v1, s[4:7], s97 offen offset:1024 sc1\n"
"v_mfma_f32_16x16x32_bf16 v[176:179], v[40:43], v[104:107], v[176:179]\n"
"v_fmac_f32_e32 v185, v254, v189\n"
"v_fmac_f32_e32 v186, v254, v190\n"
"v_mfma_f32_16x16x32_bf16 v[176:179], v[44:47], v[152:155], v[176:179]\n"
"ds_write_b128 v3, v[196:199] offset:15360\n"
"v_mfma_f32_16x16x32_bf16 v[176:179], v[48:51], v[108:111], v[176:179]\n"
"buffer_load_dwordx4 a[180:183], v1, s[4:7], s98 offen offset:1024 sc1\n"
"v_mfma_f32_16x16x32_bf16 v[176:179], v[52:55], v[144:147], v[176:179]\n"
"v_fmac_f32_e32 v187, v254, v191\n"
"v_add_f32_e32 v184, v184, v116\n"
"v_mfma_f32_16x16x32_bf16 v[188:191], v[84:87], v[144:147], 0\n"
"buffer_load_dwordx4 v[144:147], v1, s[4:7], s97 offen offset:2048 sc1\n"
"v_mfma_f32_16x16x32_bf16 v[188:191], v[80:83], v[108:111], v[188:191]\n"
"ds_read_b128 v[108:111], v4 offset:4096\n"
"ds_read_b128 v[196:199], v9 offset:512\n"
"v_mfma_f32_16x16x32_bf16 v[188:191], v[76:79], v[152:155], v[188:191]\n"
"ds_read_b128 v[152:155], v5 offset:4096\n"
"v_add_f32_e32 v185, v185, v117\n"
"v_mfma_f32_16x16x32_bf16 v[188:191], v[72:75], v[104:107], v[188:191]\n"
"s_waitcnt lgkmcnt(8)\n"
"v_add_f32_e32 v186, v186, v118\n"
"v_add_f32_e32 v187, v187, v119\n"
"v_mfma_f32_16x16x32_bf16 v[188:191], v[68:71], v[120:123], v[188:191]\n"
"v_fmac_f32_e32 v200, v254, v204\n"
"v_fmac_f32_e32 v201, v254, v205\n"
"v_mfma_f32_16x16x32_bf16 v[188:191], v[64:67], v[112:115], v[188:191]\n"
"buffer_load_dwordx4 v[104:107], v1, s[4:7], s97 offen offset:3072 sc1\n"
"v_mfma_f32_16x16x32_bf16 v[188:191], v[60:63], v[100:103], v[188:191]\n"
"ds_write_b128 v3, v[176:179] offset:16384\n"
"v_mfma_f32_16x16x32_bf16 v[188:191], v[56:59], v[96:99], v[188:191]\n"
"s_waitcnt vmcnt(18)\n"
"ds_read_b128 v[96:99], v4 offset:5120\n"
"ds_read_b128 v[100:103], v9 offset:576\n"
"; @B G1 1.3\n"
"v_mfma_f32_16x16x32_bf16 v[112:115], a[0:3], v[92:95], v[156:159]\n"
"ds_read_b128 v[116:119], v5 offset:5120\n"
"v_fmac_f32_e32 v202, v254, v206\n"
"v_mfma_f32_16x16x32_bf16 v[112:115], a[4:7], v[124:127], v[112:115]\n"
"v_fmac_f32_e32 v203, v254, v207\n"
"v_add_f32_e32 v200, v200, v208\n"
"v_mfma_f32_16x16x32_bf16 v[112:115], a[8:11], v[132:135], v[112:115]\n"
"s_add_u32 s97, s53, 0x408000\n"
"buffer_load_dwordx4 v[120:123], v0, s[4:7], s97 offen sc1\n"
"v_mfma_f32_16x16x32_bf16 v[112:115], a[12:15], v[140:143], v[112:115]\n"
"s_waitcnt vmcnt(13)\n"
"v_add_f32_e32 v201, v201, v209\n"
"v_add_f32_e32 v202, v202, v210\n"
"v_mfma_f32_16x16x32_bf16 v[112:115], a[16:19], v[128:131], v[112:115]\n"
"s_waitcnt lgkmcnt(8)\n"
"v_add_f32_e32 v203, v203, v211\n"
"v_fmac_f32_e32 v212, v254, v216\n"
"v_mfma_f32_16x16x32_bf16 v[112:115], a[20:23], v[168:171], v[112:115]\n"
"ds_write_b128 v3, v[188:191] offset:17408\n"
"v_mfma_f32_16x16x32_bf16 v[112:115], a[24:27], v[172:175], v[112:115]\n"
"v_fmac_f32_e32 v213, v254, v217\n"
"v_fmac_f32_e32 v214, v254, v218\n"
"v_mfma_f32_16x16x32_bf16 v[112:115], a[28:31], v[180:183], v[112:115]\n"
"buffer_load_dwordx4 v[176:179], v0, s[4:7], s97 offen offset:1024 sc1\n"
"v_mfma_f32_16x16x32_bf16 v[188:191], a[60:63], v[180:183], v[184:187]\n"
"v_fmac_f32_e32 v215, v254, v219\n"
"v_add_f32_e32 v212, v212, v220\n"
"v_mfma_f32_16x16x32_bf16 v[188:191], a[56:59], v[172:175], v[188:191]\n"
"v_add_f32_e32 v213, v213, v221\n"
"v_add_f32_e32 v214, v214, v222\n"
"v_mfma_f32_16x16x32_bf16 v[188:191], a[52:55], v[168:171], v[188:191]\n"
"s_waitcnt lgkmcnt(0)\n"
"s_barrier\n"
"v_mfma_f32_16x16x32_bf16 v[188:191], a[48:51], v[128:131], v[188:191]\n"
"buffer_load_dwordx4 a[184:187], v1, s[4:7], s98 offen offset:2048 sc1\n"
"v_mfma_f32_16x16x32_bf16 v[188:191], a[44:47], v[140:143], v[188:191]\n"
"ds_read_b128 v[204:207], v6\n"
"v_cmp_u_f32_e32 vcc, v112, v112\n"
"v_mfma_f32_16x16x32_bf16 v[188:191], a[40:43], v[132:135], v[188:191]\n"
"s_cbranch_vccnz .Lmk_p51s\n"
".Lmk_p51r:\n"
"ds_read_b128 v[156:159], v6 offset:1024\n"
"v_mfma_f32_16x16x32_bf16 v[188:191], a[36:39], v[124:127], v[188:191]\n"
"buffer_load_dwordx4 v[184:187], v0, s[4:7], s97 offen offset:2048 sc1\n"
"v_mfma_f32_16x16x32_bf16 v[188:191], a[32:35], v[92:95], v[188:191]\n"
"v_add_f32_e32 v215, v215, v223\n"
"v_fmac_f32_e32 v108, v254, v196\n"
"v_mfma_f32_16x16x32_bf16 v[200:203], a[64:67], v[92:95], v[200:203]\n"
"v_fmac_f32_e32 v109, v254, v197\n"
"v_fmac_f32_e32 v110, v254, v198\n"
"v_mfma_f32_16x16x32_bf16 v[200:203], a[68:71], v[124:127], v[200:203]\n"
"buffer_load_dwordx4 v[208:211], v0, s[4:7], s97 offen offset:3072 sc1\n"
"v_mfma_f32_16x16x32_bf16 v[200:203], a[72:75], v[132:135], v[200:203]\n"
"v_fmac_f32_e32 v111, v254, v199\n"
"v_add_f32_e32 v108, v108, v152\n"
"v_mfma_f32_16x16x32_bf16 v[200:203], a[76:79], v[140:143], v[200:203]\n"
"v_add_f32_e32 v109, v109, v153\n"
"v_add_f32_e32 v110, v110, v154\n"
"v_mfma_f32_16x16x32_bf16 v[200:203], a[80:83], v[128:131], v[200:203]\n"
"v_add_f32_e32 v111, v111, v155\n"
"v_fmac_f32_e32 v96, v254, v100\n"
"v_mfma_f32_16x16x32_bf16 v[200:203], a[84:87], v[168:171], v[200:203]\n"
"s_waitcnt lgkmcnt(0)\n"
"v_fmac_f32_e32 v97, v254, v101\n"
"v_fmac_f32_e32 v98, v254, v102\n"
"v_mfma_f32_16x16x32_bf16 v[200:203], a[88:91], v[172:175], v[200:203]\n"
"buffer_load_dwordx4 v[152:155], v1, s[4:7], s97 offen sc1\n"
"v_mfma_f32_16x16x32_bf16 v[200:203], a[92:95], v[180:183], v[200:203]\n"
"ds_read_b128 v[196:199], v6 offset:2048\n"
"ds_read_b128 v[216:219], v6 offset:3072\n"
"v_mfma_f32_16x16x32_bf16 v[212:215], a[124:127], v[180:183], v[212:215]\n"
"v_fmac_f32_e32 v99, v254, v103\n"
"v_add_f32_e32 v96, v96, v116\n"
"v_mfma_f32_16x16x32_bf16 v[212:215], a[120:123], v[172:175], v[212:215]\n"
"v_add_f32_e32 v97, v97, v117\n"
"v_add_f32_e32 v98, v98, v118\n"
"v_mfma_f32_16x16x32_bf16 v[212:215], a[116:119], v[168:171], v[212:215]\n"
"v_add_f32_e32 v99, v99, v119\n"
"v_add_f32_e32 v112, v112, v204\n"
"v_mfma_f32_16x16x32_bf16 v[212:215], a[112:115], v[128:131], v[212:215]\n"
"buffer_load_dwordx4 v[100:103], v1, s[4:7], s97 offen offset:1024 sc1\n"
"v_mfma_f32_16x16x32_bf16 v[212:215], a[108:111], v[140:143], v[212:215]\n"
"v_add_f32_e32 v113, v113, v205\n"
"v_add_f32_e32 v114, v114, v206\n"
"v_mfma_f32_16x16x32_bf16 v[212:215], a[104:107], v[132:135], v[212:215]\n"
"v_add_f32_e32 v115, v115, v207\n"
"v_add_f32_e32 v188, v188, v156\n"
"v_mfma_f32_16x16x32_bf16 v[212:215], a[100:103], v[124:127], v[212:215]\n"
"v_add_f32_e32 v189, v189, v157\n"
"v_add_f32_e32 v190, v190, v158\n"
"v_mfma_f32_16x16x32_bf16 v[212:215], a[96:99], v[92:95], v[212:215]\n"
"buffer_load_dwordx4 a[188:191], v1, s[4:7], s98 offen offset:3072 sc1\n"
"v_mfma_f32_16x16x32_bf16 v[108:111], v[24:27], v[92:95], v[108:111]\n"
"buffer_load_dwordx4 v[116:119], v1, s[4:7], s97 offen offset:2048 sc1\n"
"v_mfma_f32_16x16x32_bf16 v[108:111], v[28:31], v[124:127], v[108:111]\n"
"v_add_f32_e32 v191, v191, v159\n"
"v_mul_f32_e32 v112, v112, v255\n"
"v_mfma_f32_16x16x32_bf16 v[108:111], v[32:35], v[132:135], v[108:111]\n"
"v_mul_f32_e32 v113, v113, v255\n"
"v_mul_f32_e32 v114, v114, v255\n"
"v_mfma_f32_16x16x32_bf16 v[108:111], v[36:39], v[140:143], v[108:111]\n"
"v_mul_f32_e32 v115, v115, v255\n"
"v_mul_f32_e32 v188, v188, v255\n"
"v_mfma_f32_16x16x32_bf16 v[108:111], v[40:43], v[128:131], v[108:111]\n"
"s_waitcnt lgkmcnt(0)\n"
"v_mul_f32_e32 v189, v189, v255\n"
"v_mul_f32_e32 v190, v190, v255\n"
"v_mfma_f32_16x16x32_bf16 v[108:111], v[44:47], v[168:171], v[108:111]\n"
"buffer_load_dwordx4 v[156:159], v1, s[4:7], s97 offen offset:3072 sc1\n"
"v_mfma_f32_16x16x32_bf16 v[108:111], v[48:51], v[172:175], v[108:111]\n"
"s_add_u32 s98, s63, 0x84000\n"
"ds_read_b128 v[204:207], v6 offset:4096\n"
"ds_read_b128 v[220:223], v6 offset:5120\n"
"v_mfma_f32_16x16x32_bf16 v[108:111], v[52:55], v[180:183], v[108:111]\n"
"v_mul_f32_e32 v191, v191, v255\n"
"v_cvt_pk_bf16_f32 v228, v112, v113\n"
"v_mfma_f32_16x16x32_bf16 v[96:99], v[84:87], v[180:183], v[96:99]\n"
"buffer_store_dword v148, v14, s[8:11], s98 offen sc1\n"
"v_mfma_f32_16x16x32_bf16 v[96:99], v[80:83], v[172:175], v[96:99]\n"
"v_cvt_pk_bf16_f32 v229, v114, v115\n"
"v_cvt_pk_bf16_f32 v230, v188, v189\n"
"v_mfma_f32_16x16x32_bf16 v[96:99], v[76:79], v[168:171], v[96:99]\n"
"v_cvt_pk_bf16_f32 v231, v190, v191\n"
"v_add_f32_e32 v200, v200, v196\n"
"v_mfma_f32_16x16x32_bf16 v[96:99], v[72:75], v[128:131], v[96:99]\n"
"ds_write_b128 v8, v[228:231] offset:25600\n"
"v_mfma_f32_16x16x32_bf16 v[96:99], v[68:71], v[140:143], v[96:99]\n"
"v_add_f32_e32 v201, v201, v197\n"
"v_add_f32_e32 v202, v202, v198\n"
"v_mfma_f32_16x16x32_bf16 v[96:99], v[64:67], v[132:135], v[96:99]\n"
"v_add_f32_e32 v203, v203, v199\n"
"v_add_f32_e32 v212, v212, v216\n"
"v_mfma_f32_16x16x32_bf16 v[96:99], v[60:63], v[124:127], v[96:99]\n"
"v_add_f32_e32 v213, v213, v217\n"
"v_add_f32_e32 v214, v214, v218\n"
"v_mfma_f32_16x16x32_bf16 v[96:99], v[56:59], v[92:95], v[96:99]\n"
"v_add_f32_e32 v215, v215, v219\n"
"v_mul_f32_e32 v200, v200, v255\n"
"v_mul_f32_e32 v201, v201, v255\n"
"v_mul_f32_e32 v202, v202, v255\n"
"v_mul_f32_e32 v203, v203, v255\n"
"v_mul_f32_e32 v212, v212, v255\n"
"v_mul_f32_e32 v213, v213, v255\n"
"v_mul_f32_e32 v214, v214, v255\n"
"v_mul_f32_e32 v215, v215, v255\n"
"v_cvt_pk_bf16_f32 v92, v200, v201\n"
"v_cvt_pk_bf16_f32 v93, v202, v203\n"
"v_cvt_pk_bf16_f32 v94, v212, v213\n"
"v_cvt_pk_bf16_f32 v95, v214, v215\n"
"ds_write_b128 v7, v[92:95] offset:58368\n"
".Lmk_b2:\n"
"s_waitcnt vmcnt(17)\n"
"; @B G1 2.0\n"
"v_mfma_f32_16x16x32_bf16 v[112:115], a[0:3], v[136:139], 0\n"
"v_mfma_f32_16x16x32_bf16 v[112:115], a[4:7], v[88:91], v[112:115]\n"
"s_add_u32 s98, s62, 0x40800\n"
"v_mfma_f32_16x16x32_bf16 v[112:115], a[8:11], v[192:195], v[112:115]\n"
"s_add_u32 s97, s54, 0x408000\n"
"buffer_load_dwordx4 v[124:127], v0, s[4:7], s97 offen sc1\n"
"v_mfma_f32_16x16x32_bf16 v[112:115], a[12:15], v[164:167], v[112:115]\n"
"s_waitcnt vmcnt(12)\n"
"v_mfma_f32_16x16x32_bf16 v[112:115], a[16:19], v[160:163], v[112:115]\n"
"buffer_load_dword v255, v10, s[4:7], s98 offen sc1\n"
"v_mfma_f32_16x16x32_bf16 v[112:115], a[20:23], v[224:227], v[112:115]\n"
"s_waitcnt lgkmcnt(2)\n"
"v_add_f32_e32 v108, v108, v204\n"
"v_add_f32_e32 v109, v109, v205\n"
"v_mfma_f32_16x16x32_bf16 v[112:115], a[24:27], v[144:147], v[112:115]\n"
"v_add_f32_e32 v110, v110, v206\n"
"v_add_f32_e32 v111, v111, v207\n"
"v_mfma_f32_16x16x32_bf16 v[112:115], a[28:31], v[104:107], v[112:115]\n"
"buffer_load_dwordx4 v[128:131], v0, s[4:7], s97 offen offset:1024 sc1\n"
"v_mfma_f32_16x16x32_bf16 v[132:135], a[60:63], v[104:107], 0\n"
"s_add_u32 s96, s70, 0x204000\n"
"buffer_load_dwordx4 a[192:195], v0, s[4:7], s96 offen sc1\n"
"v_mfma_f32_16x16x32_bf16 v[132:135], a[56:59], v[144:147], v[132:135]\n"
"buffer_load_dword v254, v10, s[4:7], s98 offen offset:256 sc1\n"
"v_mfma_f32_16x16x32_bf16 v[132:135], a[52:55], v[224:227], v[132:135]\n"
"v_add_f32_e32 v96, v96, v220\n"
"v_add_f32_e32 v97, v97, v221\n"
"v_mfma_f32_16x16x32_bf16 v[132:135], a[48:51], v[160:163], v[132:135]\n"
"v_add_f32_e32 v98, v98, v222\n"
"v_add_f32_e32 v99, v99, v223\n"
"v_mfma_f32_16x16x32_bf16 v[132:135], a[44:47], v[164:167], v[132:135]\n"
"buffer_load_dwordx4 v[140:143], v0, s[4:7], s97 offen offset:2048 sc1\n"
"v_mfma_f32_16x16x32_bf16 v[132:135], a[40:43], v[192:195], v[132:135]\n"
"ds_write_b128 v3, v[112:115]\n"
"v_mfma_f32_16x16x32_bf16 v[132:135], a[36:39], v[88:91], v[132:135]\n"
"buffer_load_dword v252, v10, s[4:7], s98 offen offset:512 sc1\n"
"v_mfma_f32_16x16x32_bf16 v[132:135], a[32:35], v[136:139], v[132:135]\n"
"v_mul_f32_e32 v108, v108, v253\n"
"v_mul_f32_e32 v109, v109, v253\n"
"v_mfma_f32_16x16x32_bf16 v[112:115], a[64:67], v[136:139], 0\n"
"v_mul_f32_e32 v110, v110, v253\n"
"v_mul_f32_e32 v111, v111, v253\n"
"v_mfma_f32_16x16x32_bf16 v[112:115], a[68:71], v[88:91], v[112:115]\n"
"buffer_load_dwordx4 v[148:151], v0, s[4:7], s97 offen offset:3072 sc1\n"
"v_mfma_f32_16x16x32_bf16 v[112:115], a[72:75], v[192:195], v[112:115]\n"
"buffer_load_dword v251, v10, s[4:7], s98 offen offset:768 sc1\n"
"v_mfma_f32_16x16x32_bf16 v[112:115], a[76:79], v[164:167], v[112:115]\n"
"v_mul_f32_e32 v96, v96, v253\n"
"v_mul_f32_e32 v97, v97, v253\n"
"v_mfma_f32_16x16x32_bf16 v[112:115], a[80:83], v[160:163], v[112:115]\n"
"v_mul_f32_e32 v98, v98, v253\n"
"v_mul_f32_e32 v99, v99, v253\n"
"v_mfma_f32_16x16x32_bf16 v[112:115], a[84:87], v[224:227], v[112:115]\n"
"ds_write_b128 v3, v[132:135] offset:1024\n"
"v_mfma_f32_16x16x32_bf16 v[112:115], a[88:91], v[144:147], v[112:115]\n"
"buffer_load_dwordx4 v[132:135], v1, s[4:7], s97 offen sc1\n"
"v_mfma_f32_16x16x32_bf16 v[112:115], a[92:95], v[104:107], v[112:115]\n"
"buffer_load_dword v253, v10, s[4:7], s98 offen offset:1024 sc1\n"
"v_mfma_f32_16x16x32_bf16 v[168:171], a[124:127], v[104:107], 0\n"
"v_cvt_pk_bf16_f32 v172, v108, v109\n"
"v_cvt_pk_bf16_f32 v173, v110, v111\n"
"v_mfma_f32_16x16x32_bf16 v[168:171], a[120:123], v[144:147], v[168:171]\n"
"v_cvt_pk_bf16_f32 v174, v96, v97\n"
"v_cvt_pk_bf16_f32 v175, v98, v99\n"
"v_mfma_f32_16x16x32_bf16 v[168:171], a[116:119], v[224:227], v[168:171]\n"
"buffer_load_dword v250, v10, s[4:7], s98 offen offset:1280 sc1\n"
"v_mfma_f32_16x16x32_bf16 v[168:171], a[112:115], v[160:163], v[168:171]\n"
"buffer_load_dwordx4 v[96:99], v1, s[4:7], s97 offen offset:1024 sc1\n"
"v_mfma_f32_16x16x32_bf16 v[168:171], a[108:111], v[164:167], v[168:171]\n"
"buffer_load_dwordx4 a[196:199], v0, s[4:7], s96 offen offset:1024 sc1\n"
"v_mfma_f32_16x16x32_bf16 v[168:171], a[104:107], v[192:195], v[168:171]\n"
"ds_write_b128 v3, v[112:115] offset:2048\n"
"v_mfma_f32_16x16x32_bf16 v[168:171], a[100:103], v[88:91], v[168:171]\n"
"buffer_load_dword v249, v10, s[4:7], s98 offen offset:1536 sc1\n"
"v_mfma_f32_16x16x32_bf16 v[168:171], a[96:99], v[136:139], v[168:171]\n"
"ds_write_b128 v2, v[172:175] offset:50176\n"
"v_mfma_f32_16x16x32_bf16 v[108:111], v[24:27], v[136:139], 0\n"
"buffer_load_dwordx4 v[112:115], v1, s[4:7], s97 offen offset:2048 sc1\n"
"s_add_u32 s95, s64, 0x802000\n"
"v_mfma_f32_16x16x32_bf16 v[108:111], v[28:31], v[88:91], v[108:111]\n"
"buffer_load_dword v248, v10, s[4:7], s98 offen offset:1792 sc1\n"
"v_add_u32_e32 v247, 0x26c00, v0\n"
"v_mfma_f32_16x16x32_bf16 v[108:111], v[32:35], v[192:195], v[108:111]\n"
"buffer_store_dwordx4 v[228:231], v8, s[12:15], s95 offen offset:1024 sc1\n"
"v_mfma_f32_16x16x32_bf16 v[108:111], v[36:39], v[164:167], v[108:111]\n"
"ds_read_b128 v[172:175], v247\n"
"ds_read_b128 v[180:183], v247 offset:1024\n"
"v_xor_b32_e32 v247, 32, v15\n"
"v_mfma_f32_16x16x32_bf16 v[108:111], v[40:43], v[160:163], v[108:111]\n"
"ds_read_b128 v[188:191], v2 offset:50176\n"
"ds_read_b128 v[196:199], v15 offset:24576\n"
"ds_read_b128 v[200:203], v247 offset:24832\n"
"v_mfma_f32_16x16x32_bf16 v[108:111], v[44:47], v[224:227], v[108:111]\n"
"buffer_load_dwordx4 v[204:207], v1, s[4:7], s97 offen offset:3072 sc1\n"
"v_mfma_f32_16x16x32_bf16 v[108:111], v[48:51], v[144:147], v[108:111]\n"
"ds_write_b128 v3, v[168:171] offset:3072\n"
"v_mfma_f32_16x16x32_bf16 v[108:111], v[52:55], v[104:107], v[108:111]\n"
"v_mfma_f32_16x16x32_bf16 v[168:171], v[84:87], v[104:107], 0\n"
"v_mfma_f32_16x16x32_bf16 v[168:171], v[80:83], v[144:147], v[168:171]\n"
"s_waitcnt lgkmcnt(1)\n"
"v_mfma_f32_16x16x32_bf16 v[172:175], v[196:199], v[188:191], v[172:175]\n"
"v_mfma_f32_16x16x32_bf16 v[180:183], v[200:203], v[188:191], v[180:183]\n"
"ds_read_b64_tr_b16 v[104:105], v16 offset:24576\n"
"v_xor_b32_e32 v247, 32, v16\n"
"v_mfma_f32_16x16x32_bf16 v[168:171], v[76:79], v[224:227], v[168:171]\n"
"ds_read_b64_tr_b16 v[106:107], v247 offset:24832\n"
"ds_read_b64_tr_b16 v[144:145], v247 offset:24576\n"
"ds_read_b64_tr_b16 v[146:147], v16 offset:24832\n"
"v_mov_b32_e32 v188, 0x3f803f80\n"
"v_mfma_f32_16x16x32_bf16 v[168:171], v[72:75], v[160:163], v[168:171]\n"
"v_mov_b32_e32 v189, 0x3f803f80\n"
"v_mov_b32_e32 v190, 0x3f803f80\n"
"v_mov_b32_e32 v191, 0x3f803f80\n"
"v_mfma_f32_16x16x32_bf16 v[168:171], v[68:71], v[164:167], v[168:171]\n"
"v_max_f32_e32 v247, v172, v173\n"
"v_max3_f32 v247, v247, v174, v175\n"
"v_max3_f32 v247, v247, v180, v181\n"
"v_mfma_f32_16x16x32_bf16 v[168:171], v[64:67], v[192:195], v[168:171]\n"
"ds_write_b128 v3, v[108:111] offset:4096\n"
"v_mfma_f32_16x16x32_bf16 v[168:171], v[60:63], v[88:91], v[168:171]\n"
"v_max3_f32 v247, v247, v182, v183\n"
"v_mov_b32_e32 v246, v247\n"
"v_mfma_f32_16x16x32_bf16 v[168:171], v[56:59], v[136:139], v[168:171]\n"
"s_waitcnt vmcnt(25)\n"
"v_permlane16_swap_b32_e32 v247, v246\n"
"v_max_f32_e32 v247, v247, v246\n"
"v_mov_b32_e32 v246, v247\n"
"; @B G1 2.1\n"
"v_mfma_f32_16x16x32_bf16 v[88:91], a[0:3], v[120:123], 0\n"
"buffer_load_dwordx4 a[200:203], v0, s[4:7], s96 offen offset:2048 sc1\n"
"v_permlane32_swap_b32_e32 v247, v246\n"
"v_mfma_f32_16x16x32_bf16 v[88:91], a[4:7], v[176:179], v[88:91]\n"
"v_max_f32_e32 v247, v247, v246\n"
"v_sub_f32_e32 v172, v172, v247\n"
"v_sub_f32_e32 v173, v173, v247\n"
"v_mfma_f32_16x16x32_bf16 v[88:91], a[8:11], v[184:187], v[88:91]\n"
"s_add_u32 s98, s55, 0x408000\n"
"buffer_load_dwordx4 v[108:111], v0, s[4:7], s98 offen sc1\n"
"v_mfma_f32_16x16x32_bf16 v[88:91], a[12:15], v[208:211], v[88:91]\n"
"s_waitcnt vmcnt(22)\n"
"v_sub_f32_e32 v174, v174, v247\n"
"v_sub_f32_e32 v175, v175, v247\n"
"v_sub_f32_e32 v180, v180, v247\n"
"v_mfma_f32_16x16x32_bf16 v[88:91], a[16:19], v[152:155], v[88:91]\n"
"v_sub_f32_e32 v181, v181, v247\n"
"v_sub_f32_e32 v182, v182, v247\n"
"v_sub_f32_e32 v183, v183, v247\n"
"v_mfma_f32_16x16x32_bf16 v[88:91], a[20:23], v[100:103], v[88:91]\n"
"ds_write_b128 v3, v[168:171] offset:5120\n"
"v_mfma_f32_16x16x32_bf16 v[88:91], a[24:27], v[116:119], v[88:91]\n"
"v_exp_f32_e32 v172, v172\n"
"v_mfma_f32_16x16x32_bf16 v[88:91], a[28:31], v[156:159], v[88:91]\n"
"buffer_load_dwordx4 v[136:139], v0, s[4:7], s98 offen offset:1024 sc1\n"
"v_mfma_f32_16x16x32_bf16 v[160:163], a[60:63], v[156:159], 0\n"
"s_add_u32 s97, s64, 0x806000\n"
"v_exp_f32_e32 v173, v173\n"
"v_mfma_f32_16x16x32_bf16 v[160:163], a[56:59], v[116:119], v[160:163]\n"
"v_exp_f32_e32 v174, v174\n"
"v_mfma_f32_16x16x32_bf16 v[160:163], a[52:55], v[100:103], v[160:163]\n"
"buffer_store_dwordx4 v[92:95], v7, s[12:15], s97 offen offset:1024 sc1\n"
"v_mfma_f32_16x16x32_bf16 v[160:163], a[48:51], v[152:155], v[160:163]\n"
"v_exp_f32_e32 v175, v175\n"
"v_mfma_f32_16x16x32_bf16 v[160:163], a[44:47], v[208:211], v[160:163]\n"
"buffer_load_dwordx4 v[92:95], v0, s[4:7], s98 offen offset:2048 sc1\n"
"v_mfma_f32_16x16x32_bf16 v[160:163], a[40:43], v[184:187], v[160:163]\n"
"ds_write_b128 v3, v[88:91] offset:6144\n"
"v_mfma_f32_16x16x32_bf16 v[160:163], a[36:39], v[176:179], v[160:163]\n"
"v_exp_f32_e32 v180, v180\n"
"v_mfma_f32_16x16x32_bf16 v[160:163], a[32:35], v[120:123], v[160:163]\n"
"v_exp_f32_e32 v181, v181\n"
"v_mfma_f32_16x16x32_bf16 v[88:91], a[64:67], v[120:123], 0\n"
"v_exp_f32_e32 v182, v182\n"
"v_mfma_f32_16x16x32_bf16 v[88:91], a[68:71], v[176:179], v[88:91]\n"
"buffer_load_dwordx4 v[164:167], v0, s[4:7], s98 offen offset:3072 sc1\n"
"v_mfma_f32_16x16x32_bf16 v[88:91], a[72:75], v[184:187], v[88:91]\n"
"v_exp_f32_e32 v183, v183\n"
"v_cvt_pk_bf16_f32 v168, v172, v173\n"
"v_mfma_f32_16x16x32_bf16 v[88:91], a[76:79], v[208:211], v[88:91]\n"
"v_cvt_pk_bf16_f32 v169, v174, v175\n"
"v_cvt_pk_bf16_f32 v170, v180, v181\n"
"v_cvt_pk_bf16_f32 v171, v182, v183\n"
"v_mfma_f32_16x16x32_bf16 v[88:91], a[80:83], v[152:155], v[88:91]\n"
"s_waitcnt lgkmcnt(3)\n"
"v_mov_b32_e32 v172, -1\n"
"v_mov_b32_e32 v173, -1\n"
"v_mfma_f32_16x16x32_bf16 v[180:183], v[188:191], v[168:171], 0\n"
"v_mfma_f32_16x16x32_bf16 v[188:191], v[104:107], v[168:171], 0\n"
"v_mfma_f32_16x16x32_bf16 v[104:107], v[144:147], v[168:171], 0\n"
"v_mfma_f32_16x16x32_bf16 v[88:91], a[84:87], v[100:103], v[88:91]\n"
"ds_write_b128 v3, v[160:163] offset:7168\n"
"v_mfma_f32_16x16x32_bf16 v[88:91], a[88:91], v[116:119], v[88:91]\n"
"buffer_load_dwordx4 v[144:147], v1, s[4:7], s98 offen sc1\n"
"v_mfma_f32_16x16x32_bf16 v[88:91], a[92:95], v[156:159], v[88:91]\n"
"v_mov_b32_e32 v174, -1\n"
"v_mov_b32_e32 v175, -1\n"
"v_mfma_f32_16x16x32_bf16 v[160:163], a[124:127], v[156:159], 0\n"
"s_add_u32 s97, s60, 0x848000\n"
"buffer_store_dwordx4 v[172:175], v0, s[8:11], s97 offen sc1\n"
"v_mfma_f32_16x16x32_bf16 v[160:163], a[120:123], v[116:119], v[160:163]\n"
"buffer_load_dwordx4 a[204:207], v0, s[4:7], s96 offen offset:3072 sc1\n"
"v_mfma_f32_16x16x32_bf16 v[160:163], a[116:119], v[100:103], v[160:163]\n"
"v_rcp_f32_e32 v180, v180\n"
"v_mfma_f32_16x16x32_bf16 v[160:163], a[112:115], v[152:155], v[160:163]\n"
"buffer_load_dwordx4 v[168:171], v1, s[4:7], s98 offen offset:1024 sc1\n"
"v_mul_f32_e32 v188, v188, v180\n"
"v_mfma_f32_16x16x32_bf16 v[160:163], a[108:111], v[208:211], v[160:163]\n"
"v_mul_f32_e32 v189, v189, v180\n"
"v_mul_f32_e32 v190, v190, v180\n"
"v_mul_f32_e32 v191, v191, v180\n"
"v_mfma_f32_16x16x32_bf16 v[160:163], a[104:107], v[184:187], v[160:163]\n"
"ds_write_b128 v3, v[88:91] offset:8192\n"
"v_mfma_f32_16x16x32_bf16 v[160:163], a[100:103], v[176:179], v[160:163]\n"
"v_mul_f32_e32 v104, v104, v180\n"
"v_mul_f32_e32 v105, v105, v180\n"
"v_mul_f32_e32 v106, v106, v180\n"
"v_mfma_f32_16x16x32_bf16 v[160:163], a[96:99], v[120:123], v[160:163]\n"
"v_mul_f32_e32 v107, v107, v180\n"
"v_cvt_pk_bf16_f32 v88, v188, v189\n"
"v_cvt_pk_bf16_f32 v89, v190, v191\n"
"v_mfma_f32_16x16x32_bf16 v[180:183], v[24:27], v[120:123], 0\n"
"buffer_load_dwordx4 v[188:191], v1, s[4:7], s98 offen offset:2048 sc1\n"
"v_cvt_pk_bf16_f32 v90, v104, v105\n"
"v_mfma_f32_16x16x32_bf16 v[180:183], v[28:31], v[176:179], v[180:183]\n"
"s_add_u32 s97, s60, 0x448000\n"
"v_cvt_pk_bf16_f32 v91, v106, v107\n"
"v_mfma_f32_16x16x32_bf16 v[180:183], v[32:35], v[184:187], v[180:183]\n"
"buffer_store_dwordx4 v[88:91], v0, s[4:7], s97 offen sc1\n"
"v_mfma_f32_16x16x32_bf16 v[180:183], v[36:39], v[208:211], v[180:183]\n"
"v_mfma_f32_16x16x32_bf16 v[180:183], v[40:43], v[152:155], v[180:183]\n"
"s_waitcnt vmcnt(14)\n"
"v_add_f32_e32 v255, v255, v253\n"
"v_add_f32_e32 v254, v254, v250\n"
"v_mfma_f32_16x16x32_bf16 v[180:183], v[44:47], v[100:103], v[180:183]\n"
"buffer_load_dwordx4 v[88:91], v1, s[4:7], s98 offen offset:3072 sc1\n"
"v_mfma_f32_16x16x32_bf16 v[180:183], v[48:51], v[116:119], v[180:183]\n"
"ds_write_b128 v3, v[160:163] offset:9216\n"
"v_mfma_f32_16x16x32_bf16 v[180:183], v[52:55], v[156:159], v[180:183]\n"
"v_add_f32_e32 v252, v252, v249\n"
"v_add_f32_e32 v251, v251, v248\n"
"v_mfma_f32_16x16x32_bf16 v[104:107], v[84:87], v[156:159], 0\n"
"v_add_f32_e32 v255, v255, v252\n"
"v_add_f32_e32 v254, v254, v251\n"
"v_mfma_f32_16x16x32_bf16 v[104:107], v[80:83], v[116:119], v[104:107]\n"
"v_add_f32_e32 v255, v255, v254\n"
"v_mov_b32_e32 v254, v255\n"
"v_mfma_f32_16x16x32_bf16 v[104:107], v[76:79], v[100:103], v[104:107]\n"
"s_nop 0\n"
"v_permlane32_swap_b32_e32 v255, v254\n"
"v_add_f32_e32 v255, v255, v254\n"
"v_mfma_f32_16x16x32_bf16 v[104:107], v[72:75], v[152:155], v[104:107]\n"
"v_mul_f32_e32 v255, 0x3a800000, v255\n"
"v_mov_b32_e32 v254, v255\n"
"v_mfma_f32_16x16x32_bf16 v[104:107], v[68:71], v[208:211], v[104:107]\n"
"s_nop 0\n"
"v_permlane16_swap_b32_e32 v255, v254\n"
"v_cvt_pk_bf16_f32 v253, -v255, 0\n"
"v_mfma_f32_16x16x32_bf16 v[104:107], v[64:67], v[184:187], v[104:107]\n"
"ds_write_b128 v3, v[180:183] offset:10240\n"
"v_mfma_f32_16x16x32_bf16 v[104:107], v[60:63], v[176:179], v[104:107]\n"
"buffer_load_dwordx4 a[208:211], v1, s[4:7], s96 offen sc1\n"
"v_mfma_f32_16x16x32_bf16 v[104:107], v[56:59], v[120:123], v[104:107]\n"
"v_fma_f32 v254, -v255, v255, v254\n"
"v_lshlrev_b32_e32 v253, 16, v253\n"
"; @B G1 2.2\n"
"v_mfma_f32_16x16x32_bf16 v[100:103], a[0:3], v[124:127], 0\n"
"v_max_f32_e32 v254, 0, v254\n"
"v_cvt_pk_bf16_f32 v254, v254, 0\n"
"v_mfma_f32_16x16x32_bf16 v[100:103], a[4:7], v[128:131], v[100:103]\n"
"v_lshlrev_b32_e32 v254, 16, v254\n"
"v_add_f32_e32 v254, 0x3727c5ac, v254\n"
"v_mfma_f32_16x16x32_bf16 v[100:103], a[8:11], v[140:143], v[100:103]\n"
"v_cvt_pk_bf16_f32 v254, v254, 0\n"
"v_lshlrev_b32_e32 v254, 16, v254\n"
"v_mfma_f32_16x16x32_bf16 v[100:103], a[12:15], v[148:151], v[100:103]\n"
"s_waitcnt vmcnt(14)\n"
"v_sqrt_f32_e32 v254, v254\n"
"v_mfma_f32_16x16x32_bf16 v[100:103], a[16:19], v[132:135], v[100:103]\n"
"s_add_u32 s98, s68, 0x10000\n"
"v_cvt_pk_bf16_f32 v254, v254, 0\n"
"v_mfma_f32_16x16x32_bf16 v[100:103], a[20:23], v[96:99], v[100:103]\n"
"ds_write_b128 v3, v[104:107] offset:11264\n"
"v_mfma_f32_16x16x32_bf16 v[100:103], a[24:27], v[112:115], v[100:103]\n"
"buffer_store_dwordx2 v[18:19], v11, s[4:7], s98 offen offset:32 sc1\n"
"v_mfma_f32_16x16x32_bf16 v[100:103], a[28:31], v[204:207], v[100:103]\n"
"v_lshlrev_b32_e32 v254, 16, v254\n"
"v_mfma_f32_16x16x32_bf16 v[104:107], a[60:63], v[204:207], 0\n"
"v_rcp_f32_e32 v255, v254\n"
"v_mfma_f32_16x16x32_bf16 v[104:107], a[56:59], v[112:115], v[104:107]\n"
"v_mul_f32_e32 v254, 0x3e8293ee, v255\n"
"v_mfma_f32_16x16x32_bf16 v[104:107], a[52:55], v[96:99], v[104:107]\n"
"s_add_u32 s98, s52, 0x400000\n"
"buffer_load_dwordx4 v[116:119], v0, s[4:7], s98 offen sc1\n"
"v_mfma_f32_16x16x32_bf16 v[104:107], a[48:51], v[132:135], v[104:107]\n"
"s_waitcnt lgkmcnt(0)\n"
"s_barrier\n"
"v_mfma_f32_16x16x32_bf16 v[104:107], a[44:47], v[148:151], v[104:107]\n"
"ds_read_b128 v[120:123], v4\n"
"ds_read_b128 v[152:155], v9 offset:640\n"
"v_mfma_f32_16x16x32_bf16 v[104:107], a[40:43], v[140:143], v[104:107]\n"
"ds_write_b128 v3, v[100:103] offset:12288\n"
"v_mfma_f32_16x16x32_bf16 v[104:107], a[36:39], v[128:131], v[104:107]\n"
"ds_read_b128 v[100:103], v5\n"
"v_mfma_f32_16x16x32_bf16 v[104:107], a[32:35], v[124:127], v[104:107]\n"
"buffer_load_dwordx4 v[156:159], v0, s[4:7], s98 offen offset:1024 sc1\n"
"v_mfma_f32_16x16x32_bf16 v[160:163], a[64:67], v[124:127], 0\n"
"v_mfma_f32_16x16x32_bf16 v[160:163], a[68:71], v[128:131], v[160:163]\n"
"v_mfma_f32_16x16x32_bf16 v[160:163], a[72:75], v[140:143], v[160:163]\n"
"buffer_load_dwordx4 a[212:215], v1, s[4:7], s96 offen offset:1024 sc1\n"
"v_mfma_f32_16x16x32_bf16 v[160:163], a[76:79], v[148:151], v[160:163]\n"
"ds_read_b128 v[176:179], v4 offset:1024\n"
"ds_read_b128 v[180:183], v9 offset:704\n"
"v_mfma_f32_16x16x32_bf16 v[160:163], a[80:83], v[132:135], v[160:163]\n"
"buffer_load_dwordx4 v[184:187], v0, s[4:7], s98 offen offset:2048 sc1\n"
"v_mfma_f32_16x16x32_bf16 v[160:163], a[84:87], v[96:99], v[160:163]\n"
"ds_write_b128 v3, v[104:107] offset:13312\n"
"v_mfma_f32_16x16x32_bf16 v[160:163], a[88:91], v[112:115], v[160:163]\n"
"ds_read_b128 v[104:107], v5 offset:1024\n"
"s_waitcnt lgkmcnt(4)\n"
"v_mfma_f32_16x16x32_bf16 v[160:163], a[92:95], v[204:207], v[160:163]\n"
"v_fmac_f32_e32 v120, v253, v152\n"
"v_fmac_f32_e32 v121, v253, v153\n"
"v_mfma_f32_16x16x32_bf16 v[192:195], a[124:127], v[204:207], 0\n"
"v_fmac_f32_e32 v122, v253, v154\n"
"v_fmac_f32_e32 v123, v253, v155\n"
"v_mfma_f32_16x16x32_bf16 v[192:195], a[120:123], v[112:115], v[192:195]\n"
"buffer_load_dwordx4 v[152:155], v0, s[4:7], s98 offen offset:3072 sc1\n"
"v_mfma_f32_16x16x32_bf16 v[192:195], a[116:119], v[96:99], v[192:195]\n"
"ds_read_b128 v[196:199], v4 offset:2048\n"
"ds_read_b128 v[200:203], v9 offset:768\n"
"v_mfma_f32_16x16x32_bf16 v[192:195], a[112:115], v[132:135], v[192:195]\n"
"ds_read_b128 v[208:211], v5 offset:2048\n"
"v_add_f32_e32 v120, v120, v100\n"
"v_mfma_f32_16x16x32_bf16 v[192:195], a[108:111], v[148:151], v[192:195]\n"
"v_add_f32_e32 v121, v121, v101\n"
"v_add_f32_e32 v122, v122, v102\n"
"v_mfma_f32_16x16x32_bf16 v[192:195], a[104:107], v[140:143], v[192:195]\n"
"ds_write_b128 v3, v[160:163] offset:14336\n"
"v_mfma_f32_16x16x32_bf16 v[192:195], a[100:103], v[128:131], v[192:195]\n"
"buffer_load_dwordx4 v[160:163], v1, s[4:7], s98 offen sc1\n"
"v_mfma_f32_16x16x32_bf16 v[192:195], a[96:99], v[124:127], v[192:195]\n"
"s_add_u32 s97, s60, 0x840000\n"
"v_add_f32_e32 v123, v123, v103\n"
"s_waitcnt lgkmcnt(4)\n"
"v_mfma_f32_16x16x32_bf16 v[100:103], v[24:27], v[124:127], 0\n"
"buffer_store_dwordx4 v[172:175], v0, s[8:11], s97 offen sc1\n"
"v_mfma_f32_16x16x32_bf16 v[100:103], v[28:31], v[128:131], v[100:103]\n"
"ds_read_b128 v[212:215], v4 offset:3072\n"
"ds_read_b128 v[216:219], v9 offset:832\n"
"v_mfma_f32_16x16x32_bf16 v[100:103], v[32:35], v[140:143], v[100:103]\n"
"s_waitcnt vmcnt(18)\n"
"s_add_u32 s97, s68, 0x11000\n"
"ds_read_b128 v[220:223], v5 offset:3072\n"
"v_mfma_f32_16x16x32_bf16 v[100:103], v[36:39], v[148:151], v[100:103]\n"
"buffer_load_dwordx4 v[224:227], v1, s[4:7], s98 offen offset:1024 sc1\n"
"v_mfma_f32_16x16x32_bf16 v[100:103], v[40:43], v[132:135], v[100:103]\n"
"buffer_store_dwordx2 v[18:19], v11, s[12:15], s97 offen offset:64 sc1\n"
"v_mfma_f32_16x16x32_bf16 v[100:103], v[44:47], v[96:99], v[100:103]\n"
"ds_write_b128 v3, v[192:195] offset:15360\n"
"v_mfma_f32_16x16x32_bf16 v[100:103], v[48:51], v[112:115], v[100:103]\n"
"buffer_load_dwordx4 a[216:219], v1, s[4:7], s96 offen offset:2048 sc1\n"
"v_mfma_f32_16x16x32_bf16 v[100:103], v[52:55], v[204:207], v[100:103]\n"
"v_fmac_f32_e32 v176, v253, v180\n"
"v_fmac_f32_e32 v177, v253, v181\n"
"v_mfma_f32_16x16x32_bf16 v[192:195], v[84:87], v[204:207], 0\n"
"buffer_load_dwordx4 v[204:207], v1, s[4:7], s98 offen offset:2048 sc1\n"
"v_mfma_f32_16x16x32_bf16 v[192:195], v[80:83], v[112:115], v[192:195]\n"
"ds_read_b128 v[112:115], v4 offset:4096\n"
"ds_read_b128 v[228:231], v9 offset:512\n"
"v_mfma_f32_16x16x32_bf16 v[192:195], v[76:79], v[96:99], v[192:195]\n"
"ds_read_b128 v[96:99], v5 offset:4096\n"
"v_fmac_f32_e32 v178, v253, v182\n"
"v_mfma_f32_16x16x32_bf16 v[192:195], v[72:75], v[132:135], v[192:195]\n"
"v_fmac_f32_e32 v179, v253, v183\n"
"v_add_f32_e32 v176, v176, v104\n"
"v_mfma_f32_16x16x32_bf16 v[192:195], v[68:71], v[148:151], v[192:195]\n"
"v_add_f32_e32 v177, v177, v105\n"
"v_add_f32_e32 v178, v178, v106\n"
"v_mfma_f32_16x16x32_bf16 v[192:195], v[64:67], v[140:143], v[192:195]\n"
"buffer_load_dwordx4 v[132:135], v1, s[4:7], s98 offen offset:3072 sc1\n"
"v_mfma_f32_16x16x32_bf16 v[192:195], v[60:63], v[128:131], v[192:195]\n"
"ds_write_b128 v3, v[100:103] offset:16384\n"
"v_mfma_f32_16x16x32_bf16 v[192:195], v[56:59], v[124:127], v[192:195]\n"
"s_waitcnt vmcnt(21)\n"
"ds_read_b128 v[100:103], v4 offset:5120\n"
"ds_read_b128 v[124:127], v9 offset:576\n"
"; @B G1 2.3\n"
"v_mfma_f32_16x16x32_bf16 v[120:123], a[0:3], v[108:111], v[120:123]\n"
"ds_read_b128 v[128:131], v5 offset:5120\n"
"v_add_f32_e32 v179, v179, v107\n"
"v_mfma_f32_16x16x32_bf16 v[120:123], a[4:7], v[136:139], v[120:123]\n"
"s_waitcnt lgkmcnt(12)\n"
"v_fmac_f32_e32 v196, v253, v200\n"
"v_fmac_f32_e32 v197, v253, v201\n"
"v_mfma_f32_16x16x32_bf16 v[120:123], a[8:11], v[92:95], v[120:123]\n"
"s_add_u32 s98, s53, 0x400000\n"
"buffer_load_dwordx4 v[104:107], v0, s[4:7], s98 offen sc1\n"
"v_mfma_f32_16x16x32_bf16 v[120:123], a[12:15], v[164:167], v[120:123]\n"
"s_waitcnt vmcnt(15)\n"
"v_fmac_f32_e32 v198, v253, v202\n"
"v_fmac_f32_e32 v199, v253, v203\n"
"v_mfma_f32_16x16x32_bf16 v[120:123], a[16:19], v[144:147], v[120:123]\n"
"v_add_f32_e32 v196, v196, v208\n"
"v_add_f32_e32 v197, v197, v209\n"
"v_mfma_f32_16x16x32_bf16 v[120:123], a[20:23], v[168:171], v[120:123]\n"
"ds_write_b128 v3, v[192:195] offset:17408\n"
"v_mfma_f32_16x16x32_bf16 v[120:123], a[24:27], v[188:191], v[120:123]\n"
"s_waitcnt lgkmcnt(9)\n"
"v_add_f32_e32 v198, v198, v210\n"
"v_add_f32_e32 v199, v199, v211\n"
"v_mfma_f32_16x16x32_bf16 v[120:123], a[28:31], v[88:91], v[120:123]\n"
"buffer_load_dwordx4 v[140:143], v0, s[4:7], s98 offen offset:1024 sc1\n"
"v_mfma_f32_16x16x32_bf16 v[176:179], a[60:63], v[88:91], v[176:179]\n"
"v_fmac_f32_e32 v212, v253, v216\n"
"v_fmac_f32_e32 v213, v253, v217\n"
"v_mfma_f32_16x16x32_bf16 v[176:179], a[56:59], v[188:191], v[176:179]\n"
"v_fmac_f32_e32 v214, v253, v218\n"
"v_fmac_f32_e32 v215, v253, v219\n"
"v_mfma_f32_16x16x32_bf16 v[176:179], a[52:55], v[168:171], v[176:179]\n"
"s_waitcnt lgkmcnt(0)\n"
"s_barrier\n"
"v_mfma_f32_16x16x32_bf16 v[176:179], a[48:51], v[144:147], v[176:179]\n"
"buffer_load_dwordx4 a[220:223], v1, s[4:7], s96 offen offset:3072 sc1\n"
"v_mfma_f32_16x16x32_bf16 v[176:179], a[44:47], v[164:167], v[176:179]\n"
"buffer_load_dwordx4 v[148:151], v0, s[4:7], s98 offen offset:2048 sc1\n"
"v_mfma_f32_16x16x32_bf16 v[176:179], a[40:43], v[92:95], v[176:179]\n"
"ds_read_b128 v[180:183], v6\n"
"ds_read_b128 v[192:195], v6 offset:1024\n"
"v_mfma_f32_16x16x32_bf16 v[176:179], a[36:39], v[136:139], v[176:179]\n"
"v_add_f32_e32 v212, v212, v220\n"
"v_add_f32_e32 v213, v213, v221\n"
"v_mfma_f32_16x16x32_bf16 v[176:179], a[32:35], v[108:111], v[176:179]\n"
"v_add_f32_e32 v214, v214, v222\n"
"v_add_f32_e32 v215, v215, v223\n"
"v_mfma_f32_16x16x32_bf16 v[196:199], a[64:67], v[108:111], v[196:199]\n"
"v_fmac_f32_e32 v112, v253, v228\n"
"v_fmac_f32_e32 v113, v253, v229\n"
"v_mfma_f32_16x16x32_bf16 v[196:199], a[68:71], v[136:139], v[196:199]\n"
"buffer_load_dwordx4 v[200:203], v0, s[4:7], s98 offen offset:3072 sc1\n"
"v_mfma_f32_16x16x32_bf16 v[196:199], a[72:75], v[92:95], v[196:199]\n"
"v_fmac_f32_e32 v114, v253, v230\n"
"v_fmac_f32_e32 v115, v253, v231\n"
"v_mfma_f32_16x16x32_bf16 v[196:199], a[76:79], v[164:167], v[196:199]\n"
"v_add_f32_e32 v112, v112, v96\n"
"v_add_f32_e32 v113, v113, v97\n"
"v_mfma_f32_16x16x32_bf16 v[196:199], a[80:83], v[144:147], v[196:199]\n"
"v_add_f32_e32 v114, v114, v98\n"
"v_add_f32_e32 v115, v115, v99\n"
"v_mfma_f32_16x16x32_bf16 v[196:199], a[84:87], v[168:171], v[196:199]\n"
"s_waitcnt lgkmcnt(0)\n"
"v_fmac_f32_e32 v100, v253, v124\n"
"v_fmac_f32_e32 v101, v253, v125\n"
"v_mfma_f32_16x16x32_bf16 v[196:199], a[88:91], v[188:191], v[196:199]\n"
"buffer_load_dwordx4 v[96:99], v1, s[4:7], s98 offen sc1\n"
"v_mfma_f32_16x16x32_bf16 v[196:199], a[92:95], v[88:91], v[196:199]\n"
"ds_read_b128 v[208:211], v6 offset:2048\n"
"ds_read_b128 v[216:219], v6 offset:3072\n"
"v_mfma_f32_16x16x32_bf16 v[212:215], a[124:127], v[88:91], v[212:215]\n"
"v_fmac_f32_e32 v102, v253, v126\n"
"v_fmac_f32_e32 v103, v253, v127\n"
"v_mfma_f32_16x16x32_bf16 v[212:215], a[120:123], v[188:191], v[212:215]\n"
"v_add_f32_e32 v100, v100, v128\n"
"v_add_f32_e32 v101, v101, v129\n"
"v_mfma_f32_16x16x32_bf16 v[212:215], a[116:119], v[168:171], v[212:215]\n"
"v_add_f32_e32 v102, v102, v130\n"
"v_add_f32_e32 v103, v103, v131\n"
"v_mfma_f32_16x16x32_bf16 v[212:215], a[112:115], v[144:147], v[212:215]\n"
"buffer_load_dwordx4 v[124:127], v1, s[4:7], s98 offen offset:1024 sc1\n"
"v_mfma_f32_16x16x32_bf16 v[212:215], a[108:111], v[164:167], v[212:215]\n"
"v_add_f32_e32 v120, v120, v180\n"
"v_add_f32_e32 v121, v121, v181\n"
"v_mfma_f32_16x16x32_bf16 v[212:215], a[104:107], v[92:95], v[212:215]\n"
"s_add_u32 s97, s70, 0x206000\n"
"v_add_f32_e32 v122, v122, v182\n"
"v_add_f32_e32 v123, v123, v183\n"
"v_mfma_f32_16x16x32_bf16 v[212:215], a[100:103], v[136:139], v[212:215]\n"
"v_add_f32_e32 v176, v176, v192\n"
"v_add_f32_e32 v177, v177, v193\n"
"v_mfma_f32_16x16x32_bf16 v[212:215], a[96:99], v[108:111], v[212:215]\n"
"buffer_load_dwordx4 a[224:227], v0, s[4:7], s97 offen sc1\n"
"v_mfma_f32_16x16x32_bf16 v[112:115], v[24:27], v[108:111], v[112:115]\n"
"buffer_load_dwordx4 v[128:131], v1, s[4:7], s98 offen offset:2048 sc1\n"
"v_mfma_f32_16x16x32_bf16 v[112:115], v[28:31], v[136:139], v[112:115]\n"
"v_add_f32_e32 v178, v178, v194\n"
"v_add_f32_e32 v179, v179, v195\n"
"v_mfma_f32_16x16x32_bf16 v[112:115], v[32:35], v[92:95], v[112:115]\n"
"v_mul_f32_e32 v120, v120, v255\n"
"v_mul_f32_e32 v121, v121, v255\n"
"v_mfma_f32_16x16x32_bf16 v[112:115], v[36:39], v[164:167], v[112:115]\n"
"v_mul_f32_e32 v122, v122, v255\n"
"v_mul_f32_e32 v123, v123, v255\n"
"v_mfma_f32_16x16x32_bf16 v[112:115], v[40:43], v[144:147], v[112:115]\n"
"s_waitcnt lgkmcnt(0)\n"
"v_mul_f32_e32 v176, v176, v255\n"
"v_mul_f32_e32 v177, v177, v255\n"
"v_mfma_f32_16x16x32_bf16 v[112:115], v[44:47], v[168:171], v[112:115]\n"
"buffer_load_dwordx4 v[180:183], v1, s[4:7], s98 offen offset:3072 sc1\n"
"v_mfma_f32_16x16x32_bf16 v[112:115], v[48:51], v[188:191], v[112:115]\n"
"s_add_u32 s98, s60, 0x800000\n"
"ds_read_b128 v[192:195], v6 offset:4096\n"
"ds_read_b128 v[220:223], v6 offset:5120\n"
"v_mfma_f32_16x16x32_bf16 v[112:115], v[52:55], v[88:91], v[112:115]\n"
"v_mul_f32_e32 v178, v178, v255\n"
"v_mul_f32_e32 v179, v179, v255\n"
"v_mfma_f32_16x16x32_bf16 v[100:103], v[84:87], v[88:91], v[100:103]\n"
"buffer_store_dwordx4 v[172:175], v0, s[8:11], s98 offen sc1\n"
"v_mfma_f32_16x16x32_bf16 v[100:103], v[80:83], v[188:191], v[100:103]\n"
"v_cvt_pk_bf16_f32 v88, v120, v121\n"
"v_cvt_pk_bf16_f32 v89, v122, v123\n"
"v_mfma_f32_16x16x32_bf16 v[100:103], v[76:79], v[168:171], v[100:103]\n"
"v_cvt_pk_bf16_f32 v90, v176, v177\n"
"v_cvt_pk_bf16_f32 v91, v178, v179\n"
"v_mfma_f32_16x16x32_bf16 v[100:103], v[72:75], v[144:147], v[100:103]\n"
"ds_write_b128 v8, v[88:91] offset:17408\n"
"v_mfma_f32_16x16x32_bf16 v[100:103], v[68:71], v[164:167], v[100:103]\n"
"v_add_f32_e32 v196, v196, v208\n"
"v_add_f32_e32 v197, v197, v209\n"
"v_mfma_f32_16x16x32_bf16 v[100:103], v[64:67], v[92:95], v[100:103]\n"
"v_add_f32_e32 v198, v198, v210\n"
"v_add_f32_e32 v199, v199, v211\n"
"v_mfma_f32_16x16x32_bf16 v[100:103], v[60:63], v[136:139], v[100:103]\n"
"v_add_f32_e32 v212, v212, v216\n"
"v_add_f32_e32 v213, v213, v217\n"
"v_mfma_f32_16x16x32_bf16 v[100:103], v[56:59], v[108:111], v[100:103]\n"
"v_add_f32_e32 v214, v214, v218\n"
"v_add_f32_e32 v215, v215, v219\n"
"v_mul_f32_e32 v196, v196, v255\n"
"v_mul_f32_e32 v197, v197, v255\n"
"v_mul_f32_e32 v198, v198, v255\n"
"v_mul_f32_e32 v199, v199, v255\n"
"v_mul_f32_e32 v212, v212, v255\n"
"v_mul_f32_e32 v213, v213, v255\n"
"v_mul_f32_e32 v214, v214, v255\n"
"v_mul_f32_e32 v215, v215, v255\n"
"v_cvt_pk_bf16_f32 v88, v196, v197\n"
"v_cvt_pk_bf16_f32 v89, v198, v199\n"
"v_cvt_pk_bf16_f32 v90, v212, v213\n"
"v_cvt_pk_bf16_f32 v91, v214, v215\n"
"ds_write_b128 v7, v[88:91] offset:50176\n"
".Lmk_b3:\n"
"s_waitcnt vmcnt(18)\n"
"; @B G1 3.0\n"
"v_mfma_f32_16x16x32_bf16 v[88:91], a[0:3], v[116:119], 0\n"
"v_mfma_f32_16x16x32_bf16 v[88:91], a[4:7], v[156:159], v[88:91]\n"
"s_add_u32 s98, s62, 0x40000\n"
"v_mfma_f32_16x16x32_bf16 v[88:91], a[8:11], v[184:187], v[88:91]\n"
"s_add_u32 s96, s54, 0x400000\n"
"buffer_load_dwordx4 v[92:95], v0, s[4:7], s96 offen sc1\n"
"v_mfma_f32_16x16x32_bf16 v[88:91], a[12:15], v[152:155], v[88:91]\n"
"s_waitcnt vmcnt(12)\n"
"v_mfma_f32_16x16x32_bf16 v[88:91], a[16:19], v[160:163], v[88:91]\n"
"buffer_load_dword v255, v10, s[4:7], s98 offen sc1\n"
"v_mfma_f32_16x16x32_bf16 v[88:91], a[20:23], v[224:227], v[88:91]\n"
"s_waitcnt lgkmcnt(2)\n"
"v_add_f32_e32 v112, v112, v192\n"
"v_add_f32_e32 v113, v113, v193\n"
"v_mfma_f32_16x16x32_bf16 v[88:91], a[24:27], v[204:207], v[88:91]\n"
"v_add_f32_e32 v114, v114, v194\n"
"v_add_f32_e32 v115, v115, v195\n"
"v_mfma_f32_16x16x32_bf16 v[88:91], a[28:31], v[132:135], v[88:91]\n"
"buffer_load_dwordx4 v[108:111], v0, s[4:7], s96 offen offset:1024 sc1\n"
"v_mfma_f32_16x16x32_bf16 v[120:123], a[60:63], v[132:135], 0\n"
"s_waitcnt lgkmcnt(0)\n"
"s_barrier\n"
"v_mfma_f32_16x16x32_bf16 v[120:123], a[56:59], v[204:207], v[120:123]\n"
"buffer_load_dwordx4 a[228:231], v0, s[4:7], s97 offen offset:1024 sc1\n"
"v_mfma_f32_16x16x32_bf16 v[120:123], a[52:55], v[224:227], v[120:123]\n"
"buffer_load_dword v253, v10, s[4:7], s98 offen offset:256 sc1\n"
"v_mfma_f32_16x16x32_bf16 v[120:123], a[48:51], v[160:163], v[120:123]\n"
"v_add_f32_e32 v100, v100, v220\n"
"v_add_f32_e32 v101, v101, v221\n"
"v_mfma_f32_16x16x32_bf16 v[120:123], a[44:47], v[152:155], v[120:123]\n"
"buffer_load_dwordx4 v[136:139], v0, s[4:7], s96 offen offset:2048 sc1\n"
"v_mfma_f32_16x16x32_bf16 v[120:123], a[40:43], v[184:187], v[120:123]\n"
"ds_write_b128 v3, v[88:91]\n"
"v_mfma_f32_16x16x32_bf16 v[120:123], a[36:39], v[156:159], v[120:123]\n"
"buffer_load_dword v252, v10, s[4:7], s98 offen offset:512 sc1\n"
"v_mfma_f32_16x16x32_bf16 v[120:123], a[32:35], v[116:119], v[120:123]\n"
"v_add_f32_e32 v102, v102, v222\n"
"v_add_f32_e32 v103, v103, v223\n"
"v_mfma_f32_16x16x32_bf16 v[88:91], a[64:67], v[116:119], 0\n"
"v_mul_f32_e32 v112, v112, v254\n"
"v_mul_f32_e32 v113, v113, v254\n"
"v_mfma_f32_16x16x32_bf16 v[88:91], a[68:71], v[156:159], v[88:91]\n"
"buffer_load_dwordx4 v[144:147], v0, s[4:7], s96 offen offset:3072 sc1\n"
"v_mfma_f32_16x16x32_bf16 v[88:91], a[72:75], v[184:187], v[88:91]\n"
"buffer_load_dword v251, v10, s[4:7], s98 offen offset:768 sc1\n"
"v_mfma_f32_16x16x32_bf16 v[88:91], a[76:79], v[152:155], v[88:91]\n"
"v_mul_f32_e32 v114, v114, v254\n"
"v_mul_f32_e32 v115, v115, v254\n"
"v_mfma_f32_16x16x32_bf16 v[88:91], a[80:83], v[160:163], v[88:91]\n"
"v_mul_f32_e32 v100, v100, v254\n"
"v_mul_f32_e32 v101, v101, v254\n"
"v_add_u32_e32 v250, 0x26c00, v0\n"
"v_mfma_f32_16x16x32_bf16 v[88:91], a[84:87], v[224:227], v[88:91]\n"
"ds_write_b128 v3, v[120:123] offset:1024\n"
"v_mfma_f32_16x16x32_bf16 v[88:91], a[88:91], v[204:207], v[88:91]\n"
"buffer_load_dwordx4 v[120:123], v1, s[4:7], s96 offen sc1\n"
"ds_read_b128 v[164:167], v250\n"
"v_mfma_f32_16x16x32_bf16 v[88:91], a[92:95], v[132:135], v[88:91]\n"
"buffer_load_dwordx4 a[232:235], v0, s[4:7], s97 offen offset:2048 sc1\n"
"ds_read_b128 v[168:171], v250 offset:1024\n"
"v_mfma_f32_16x16x32_bf16 v[172:175], a[124:127], v[132:135], 0\n"
"buffer_load_dword v250, v10, s[4:7], s98 offen offset:1024 sc1\n"
"v_xor_b32_e32 v249, 32, v17\n"
"v_mfma_f32_16x16x32_bf16 v[172:175], a[120:123], v[204:207], v[172:175]\n"
"ds_read_b128 v[176:179], v2 offset:49152\n"
"v_mul_f32_e32 v102, v102, v254\n"
"v_mul_f32_e32 v103, v103, v254\n"
"v_mfma_f32_16x16x32_bf16 v[172:175], a[116:119], v[224:227], v[172:175]\n"
"v_cvt_pk_bf16_f32 v188, v112, v113\n"
"v_cvt_pk_bf16_f32 v189, v114, v115\n"
"v_mfma_f32_16x16x32_bf16 v[172:175], a[112:115], v[160:163], v[172:175]\n"
"ds_read_b128 v[112:115], v17 offset:23552\n"
"buffer_load_dwordx4 v[192:195], v1, s[4:7], s96 offen offset:1024 sc1\n"
"ds_read_b128 v[196:199], v249 offset:23808\n"
"v_mfma_f32_16x16x32_bf16 v[172:175], a[108:111], v[152:155], v[172:175]\n"
"buffer_load_dword v254, v10, s[4:7], s98 offen offset:1280 sc1\n"
"v_mfma_f32_16x16x32_bf16 v[172:175], a[104:107], v[184:187], v[172:175]\n"
"ds_write_b128 v3, v[88:91] offset:2048\n"
"v_mfma_f32_16x16x32_bf16 v[172:175], a[100:103], v[156:159], v[172:175]\n"
"v_cvt_pk_bf16_f32 v190, v100, v101\n"
"v_cvt_pk_bf16_f32 v191, v102, v103\n"
"v_mfma_f32_16x16x32_bf16 v[172:175], a[96:99], v[116:119], v[172:175]\n"
"buffer_load_dword v249, v10, s[4:7], s98 offen offset:1536 sc1\n"
"v_mfma_f32_16x16x32_bf16 v[88:91], v[24:27], v[116:119], 0\n"
"buffer_load_dwordx4 v[100:103], v1, s[4:7], s96 offen offset:2048 sc1\n"
"s_waitcnt lgkmcnt(1)\n"
"v_mfma_f32_16x16x32_bf16 v[164:167], v[112:115], v[176:179], v[164:167]\n"
"v_mfma_f32_16x16x32_bf16 v[168:171], v[196:199], v[176:179], v[168:171]\n"
"v_mfma_f32_16x16x32_bf16 v[88:91], v[28:31], v[156:159], v[88:91]\n"
"ds_write_b128 v2, v[188:191] offset:17408\n"
"v_mfma_f32_16x16x32_bf16 v[88:91], v[32:35], v[184:187], v[88:91]\n"
"buffer_load_dword v248, v10, s[4:7], s98 offen offset:1792 sc1\n"
"v_xor_b32_e32 v247, 32, v16\n"
"v_mfma_f32_16x16x32_bf16 v[88:91], v[36:39], v[152:155], v[88:91]\n"
"ds_read_b64_tr_b16 v[112:113], v16 offset:23552\n"
"ds_read_b64_tr_b16 v[114:115], v247 offset:23808\n"
"ds_read_b64_tr_b16 v[176:177], v247 offset:23552\n"
"v_mfma_f32_16x16x32_bf16 v[88:91], v[40:43], v[160:163], v[88:91]\n"
"buffer_load_dwordx4 a[236:239], v0, s[4:7], s97 offen offset:3072 sc1\n"
"ds_read_b64_tr_b16 v[178:179], v16 offset:23808\n"
"v_mfma_f32_16x16x32_bf16 v[88:91], v[44:47], v[224:227], v[88:91]\n"
"buffer_load_dwordx4 v[188:191], v1, s[4:7], s96 offen offset:3072 sc1\n"
"v_mov_b32_e32 v196, 0x3f803f80\n"
"v_mfma_f32_16x16x32_bf16 v[88:91], v[48:51], v[204:207], v[88:91]\n"
"ds_write_b128 v3, v[172:175] offset:3072\n"
"v_mfma_f32_16x16x32_bf16 v[88:91], v[52:55], v[132:135], v[88:91]\n"
"v_mov_b32_e32 v197, 0x3f803f80\n"
"v_mov_b32_e32 v198, 0x3f803f80\n"
"v_mov_b32_e32 v199, 0x3f803f80\n"
"v_mfma_f32_16x16x32_bf16 v[172:175], v[84:87], v[132:135], 0\n"
"v_max_f32_e32 v247, v164, v165\n"
"v_max3_f32 v247, v247, v166, v167\n"
"v_max3_f32 v247, v247, v168, v169\n"
"v_mfma_f32_16x16x32_bf16 v[172:175], v[80:83], v[204:207], v[172:175]\n"
"v_max3_f32 v247, v247, v170, v171\n"
"v_mov_b32_e32 v246, v247\n"
"v_mfma_f32_16x16x32_bf16 v[172:175], v[76:79], v[224:227], v[172:175]\n"
"s_nop 0\n"
"v_permlane16_swap_b32_e32 v247, v246\n"
"v_max_f32_e32 v247, v247, v246\n"
"v_mov_b32_e32 v246, v247\n"
"v_mfma_f32_16x16x32_bf16 v[172:175], v[72:75], v[160:163], v[172:175]\n"
"s_nop 0\n"
"v_permlane32_swap_b32_e32 v247, v246\n"
"v_max_f32_e32 v247, v247, v246\n"
"v_sub_f32_e32 v164, v164, v247\n"
"v_mfma_f32_16x16x32_bf16 v[172:175], v[68:71], v[152:155], v[172:175]\n"
"v_sub_f32_e32 v165, v165, v247\n"
"v_sub_f32_e32 v166, v166, v247\n"
"v_sub_f32_e32 v167, v167, v247\n"
"v_mfma_f32_16x16x32_bf16 v[172:175], v[64:67], v[184:187], v[172:175]\n"
"ds_write_b128 v3, v[88:91] offset:4096\n"
"v_mfma_f32_16x16x32_bf16 v[172:175], v[60:63], v[156:159], v[172:175]\n"
"v_sub_f32_e32 v168, v168, v247\n"
"v_sub_f32_e32 v169, v169, v247\n"
"v_sub_f32_e32 v170, v170, v247\n"
"v_mfma_f32_16x16x32_bf16 v[172:175], v[56:59], v[116:119], v[172:175]\n"
"s_waitcnt vmcnt(25)\n"
"v_sub_f32_e32 v171, v171, v247\n"
"v_exp_f32_e32 v164, v164\n"
"; @B G1 3.1\n"
"v_mfma_f32_16x16x32_bf16 v[88:91], a[0:3], v[104:107], 0\n"
"v_exp_f32_e32 v165, v165\n"
"v_mfma_f32_16x16x32_bf16 v[88:91], a[4:7], v[140:143], v[88:91]\n"
"buffer_load_dwordx4 a[240:243], v1, s[4:7], s97 offen sc1\n"
"v_mfma_f32_16x16x32_bf16 v[88:91], a[8:11], v[148:151], v[88:91]\n"
"s_add_u32 s98, s55, 0x400000\n"
"buffer_load_dwordx4 v[116:119], v0, s[4:7], s98 offen sc1\n"
"v_mfma_f32_16x16x32_bf16 v[88:91], a[12:15], v[200:203], v[88:91]\n"
"s_waitcnt vmcnt(22)\n"
"v_exp_f32_e32 v166, v166\n"
"v_mfma_f32_16x16x32_bf16 v[88:91], a[16:19], v[96:99], v[88:91]\n"
"v_exp_f32_e32 v167, v167\n"
"v_mfma_f32_16x16x32_bf16 v[88:91], a[20:23], v[124:127], v[88:91]\n"
"ds_write_b128 v3, v[172:175] offset:5120\n"
"v_mfma_f32_16x16x32_bf16 v[88:91], a[24:27], v[128:131], v[88:91]\n"
"v_exp_f32_e32 v168, v168\n"
"v_mfma_f32_16x16x32_bf16 v[88:91], a[28:31], v[180:183], v[88:91]\n"
"buffer_load_dwordx4 v[132:135], v0, s[4:7], s98 offen offset:1024 sc1\n"
"v_mfma_f32_16x16x32_bf16 v[152:155], a[60:63], v[180:183], 0\n"
"v_exp_f32_e32 v169, v169\n"
"v_mfma_f32_16x16x32_bf16 v[152:155], a[56:59], v[128:131], v[152:155]\n"
"v_exp_f32_e32 v170, v170\n"
"v_mfma_f32_16x16x32_bf16 v[152:155], a[52:55], v[124:127], v[152:155]\n"
"v_exp_f32_e32 v171, v171\n"
"v_cvt_pk_bf16_f32 v156, v164, v165\n"
"v_mfma_f32_16x16x32_bf16 v[152:155], a[48:51], v[96:99], v[152:155]\n"
"v_cvt_pk_bf16_f32 v157, v166, v167\n"
"v_cvt_pk_bf16_f32 v158, v168, v169\n"
"v_cvt_pk_bf16_f32 v159, v170, v171\n"
"v_mfma_f32_16x16x32_bf16 v[152:155], a[44:47], v[200:203], v[152:155]\n"
"buffer_load_dwordx4 v[160:163], v0, s[4:7], s98 offen offset:2048 sc1\n"
"s_waitcnt lgkmcnt(3)\n"
"v_mfma_f32_16x16x32_bf16 v[164:167], v[196:199], v[156:159], 0\n"
"v_mfma_f32_16x16x32_bf16 v[168:171], v[112:115], v[156:159], 0\n"
"v_mfma_f32_16x16x32_bf16 v[112:115], v[176:179], v[156:159], 0\n"
"v_mfma_f32_16x16x32_bf16 v[152:155], a[40:43], v[148:151], v[152:155]\n"
"ds_write_b128 v3, v[88:91] offset:6144\n"
"v_mfma_f32_16x16x32_bf16 v[152:155], a[36:39], v[140:143], v[152:155]\n"
"s_add_u32 s96, s73, 0\n"
"buffer_load_dwordx2 v[246:247], off, s[4:7], s96 sc1\n"
"v_mfma_f32_16x16x32_bf16 v[152:155], a[32:35], v[104:107], v[152:155]\n"
"buffer_load_dwordx4 a[244:247], v1, s[4:7], s97 offen offset:1024 sc1\n"
"v_mfma_f32_16x16x32_bf16 v[88:91], a[64:67], v[104:107], 0\n"
"v_mfma_f32_16x16x32_bf16 v[88:91], a[68:71], v[140:143], v[88:91]\n"
"buffer_load_dwordx4 v[156:159], v0, s[4:7], s98 offen offset:3072 sc1\n"
"v_mfma_f32_16x16x32_bf16 v[88:91], a[72:75], v[148:151], v[88:91]\n"
"v_rcp_f32_e32 v164, v164\n"
"v_mfma_f32_16x16x32_bf16 v[88:91], a[76:79], v[200:203], v[88:91]\n"
"v_mul_f32_e32 v168, v168, v164\n"
"v_mul_f32_e32 v169, v169, v164\n"
"v_mul_f32_e32 v170, v170, v164\n"
"v_mfma_f32_16x16x32_bf16 v[88:91], a[80:83], v[96:99], v[88:91]\n"
"v_mov_b32_e32 v172, -1\n"
"v_mov_b32_e32 v173, -1\n"
"v_mul_f32_e32 v171, v171, v164\n"
"v_mfma_f32_16x16x32_bf16 v[88:91], a[84:87], v[124:127], v[88:91]\n"
"ds_write_b128 v3, v[152:155] offset:7168\n"
"v_mfma_f32_16x16x32_bf16 v[88:91], a[88:91], v[128:131], v[88:91]\n"
"buffer_load_dwordx4 v[152:155], v1, s[4:7], s98 offen sc1\n"
"v_mul_f32_e32 v112, v112, v164\n"
"v_mfma_f32_16x16x32_bf16 v[88:91], a[92:95], v[180:183], v[88:91]\n"
"v_mov_b32_e32 v174, -1\n"
"v_mov_b32_e32 v175, -1\n"
"v_mul_f32_e32 v113, v113, v164\n"
"v_mfma_f32_16x16x32_bf16 v[176:179], a[124:127], v[180:183], 0\n"
"s_add_u32 s95, s60, 0x808000\n"
"buffer_store_dwordx4 v[172:175], v0, s[8:11], s95 offen sc1\n"
"v_mfma_f32_16x16x32_bf16 v[176:179], a[120:123], v[128:131], v[176:179]\n"
"v_mul_f32_e32 v114, v114, v164\n"
"v_mul_f32_e32 v115, v115, v164\n"
"v_cvt_pk_bf16_f32 v164, v168, v169\n"
"v_mfma_f32_16x16x32_bf16 v[176:179], a[116:119], v[124:127], v[176:179]\n"
"s_add_u32 s95, s60, 0x440000\n"
"v_cvt_pk_bf16_f32 v165, v170, v171\n"
"v_cvt_pk_bf16_f32 v166, v112, v113\n"
"v_cvt_pk_bf16_f32 v167, v114, v115\n"
"v_mfma_f32_16x16x32_bf16 v[176:179], a[112:115], v[96:99], v[176:179]\n"
"buffer_load_dwordx4 v[112:115], v1, s[4:7], s98 offen offset:1024 sc1\n"
"v_mfma_f32_16x16x32_bf16 v[176:179], a[108:111], v[200:203], v[176:179]\n"
"buffer_load_dwordx4 a[248:251], v1, s[4:7], s97 offen offset:2048 sc1\n"
"v_mfma_f32_16x16x32_bf16 v[176:179], a[104:107], v[148:151], v[176:179]\n"
"ds_write_b128 v3, v[88:91] offset:8192\n"
"v_mfma_f32_16x16x32_bf16 v[176:179], a[100:103], v[140:143], v[176:179]\n"
"buffer_store_dwordx4 v[164:167], v0, s[4:7], s95 offen sc1\n"
"v_mfma_f32_16x16x32_bf16 v[176:179], a[96:99], v[104:107], v[176:179]\n"
";\n"
"v_mfma_f32_16x16x32_bf16 v[88:91], v[24:27], v[104:107], 0\n"
"buffer_load_dwordx4 v[164:167], v1, s[4:7], s98 offen offset:2048 sc1\n"
"v_mfma_f32_16x16x32_bf16 v[88:91], v[28:31], v[140:143], v[88:91]\n"
"v_mfma_f32_16x16x32_bf16 v[88:91], v[32:35], v[148:151], v[88:91]\n"
"v_mfma_f32_16x16x32_bf16 v[88:91], v[36:39], v[200:203], v[88:91]\n"
"v_mfma_f32_16x16x32_bf16 v[88:91], v[40:43], v[96:99], v[88:91]\n"
"s_waitcnt vmcnt(15)\n"
"v_add_f32_e32 v255, v255, v250\n"
"v_add_f32_e32 v253, v253, v254\n"
"v_mfma_f32_16x16x32_bf16 v[88:91], v[44:47], v[124:127], v[88:91]\n"
"buffer_load_dwordx4 v[168:171], v1, s[4:7], s98 offen offset:3072 sc1\n"
"v_mfma_f32_16x16x32_bf16 v[88:91], v[48:51], v[128:131], v[88:91]\n"
"ds_write_b128 v3, v[176:179] offset:9216\n"
"v_mfma_f32_16x16x32_bf16 v[88:91], v[52:55], v[180:183], v[88:91]\n"
"v_add_f32_e32 v252, v252, v249\n"
"v_add_f32_e32 v251, v251, v248\n"
"v_mfma_f32_16x16x32_bf16 v[176:179], v[84:87], v[180:183], 0\n"
"v_add_f32_e32 v255, v255, v252\n"
"v_add_f32_e32 v253, v253, v251\n"
"v_mfma_f32_16x16x32_bf16 v[176:179], v[80:83], v[128:131], v[176:179]\n"
"buffer_load_dwordx4 a[252:255], v1, s[4:7], s97 offen offset:3072 sc1\n"
"v_mfma_f32_16x16x32_bf16 v[176:179], v[76:79], v[124:127], v[176:179]\n"
"v_add_f32_e32 v255, v255, v253\n"
"v_mov_b32_e32 v253, v255\n"
"v_mfma_f32_16x16x32_bf16 v[176:179], v[72:75], v[96:99], v[176:179]\n"
"s_nop 0\n"
"v_permlane32_swap_b32_e32 v255, v253\n"
"v_add_f32_e32 v255, v255, v253\n"
"v_mfma_f32_16x16x32_bf16 v[176:179], v[68:71], v[200:203], v[176:179]\n"
"v_mul_f32_e32 v255, 0x3a800000, v255\n"
"v_mov_b32_e32 v253, v255\n"
"v_mfma_f32_16x16x32_bf16 v[176:179], v[64:67], v[148:151], v[176:179]\n"
"ds_write_b128 v3, v[88:91] offset:10240\n"
"v_mfma_f32_16x16x32_bf16 v[176:179], v[60:63], v[140:143], v[176:179]\n"
"v_permlane16_swap_b32_e32 v255, v253\n"
"v_cvt_pk_bf16_f32 v254, -v255, 0\n"
"v_mfma_f32_16x16x32_bf16 v[176:179], v[56:59], v[104:107], v[176:179]\n"
"v_fma_f32 v253, -v255, v255, v253\n"
"v_lshlrev_b32_e32 v254, 16, v254\n"
"; @B G1 3.2\n"
"v_mfma_f32_16x16x32_bf16 v[88:91], a[0:3], v[92:95], 0\n"
"s_add_u32 s98, s67, 0x10020\n"
"buffer_load_dwordx2 v[250:251], v12, s[4:7], s98 offen sc1\n"
"v_mfma_f32_16x16x32_bf16 v[88:91], a[4:7], v[108:111], v[88:91]\n"
"v_max_f32_e32 v253, 0, v253\n"
"v_cvt_pk_bf16_f32 v253, v253, 0\n"
"v_mfma_f32_16x16x32_bf16 v[88:91], a[8:11], v[136:139], v[88:91]\n"
"v_lshlrev_b32_e32 v253, 16, v253\n"
"v_add_f32_e32 v253, 0x3727c5ac, v253\n"
"v_mfma_f32_16x16x32_bf16 v[88:91], a[12:15], v[144:147], v[88:91]\n"
"s_waitcnt vmcnt(16)\n"
"v_cvt_pk_bf16_f32 v253, v253, 0\n"
"v_lshlrev_b32_e32 v253, 16, v253\n"
"v_mfma_f32_16x16x32_bf16 v[88:91], a[16:19], v[120:123], v[88:91]\n"
"v_sqrt_f32_e32 v253, v253\n"
"v_mfma_f32_16x16x32_bf16 v[88:91], a[20:23], v[192:195], v[88:91]\n"
"ds_write_b128 v3, v[176:179] offset:11264\n"
"v_mfma_f32_16x16x32_bf16 v[88:91], a[24:27], v[100:103], v[88:91]\n"
"v_cvt_pk_bf16_f32 v253, v253, 0\n"
"v_lshlrev_b32_e32 v253, 16, v253\n"
"v_mfma_f32_16x16x32_bf16 v[88:91], a[28:31], v[188:191], v[88:91]\n"
"v_rcp_f32_e32 v255, v253\n"
"v_mfma_f32_16x16x32_bf16 v[96:99], a[60:63], v[188:191], 0\n"
"v_mul_f32_e32 v253, 0x3e8293ee, v255\n"
"v_add_u32_e32 v252, 0x26c00, v0\n"
"v_mfma_f32_16x16x32_bf16 v[96:99], a[56:59], v[100:103], v[96:99]\n"
"ds_read_b128 v[104:107], v252\n"
"ds_read_b128 v[124:127], v252 offset:1024\n"
"v_mfma_f32_16x16x32_bf16 v[96:99], a[52:55], v[192:195], v[96:99]\n"
"s_waitcnt lgkmcnt(0)\n"
"s_barrier\n"
"v_mfma_f32_16x16x32_bf16 v[96:99], a[48:51], v[120:123], v[96:99]\n"
"s_add_u32 s97, s61, 0xc40000\n"
"ds_read_b128 v[128:131], v4\n"
"ds_read_b128 v[140:143], v9 offset:640\n"
"v_mfma_f32_16x16x32_bf16 v[96:99], a[44:47], v[144:147], v[96:99]\n"
"ds_read_b128 v[148:151], v5\n"
"v_add_u32_e32 v252, 0x26c00, v0\n"
"v_mfma_f32_16x16x32_bf16 v[96:99], a[40:43], v[136:139], v[96:99]\n"
"ds_write_b128 v3, v[88:91] offset:12288\n"
"v_mfma_f32_16x16x32_bf16 v[96:99], a[36:39], v[108:111], v[96:99]\n"
"buffer_store_dwordx4 v[172:175], v0, s[8:11], s97 offen sc1\n"
"v_mfma_f32_16x16x32_bf16 v[96:99], a[32:35], v[92:95], v[96:99]\n"
"ds_read_b128 v[88:91], v252\n"
"ds_read_b128 v[176:179], v252 offset:1024\n"
"v_mfma_f32_16x16x32_bf16 v[180:183], a[64:67], v[92:95], 0\n"
"v_mov_b32_e32 v252, 0xff800000\n"
"v_mfma_f32_16x16x32_bf16 v[180:183], a[68:71], v[108:111], v[180:183]\n"
"s_waitcnt lgkmcnt(3)\n"
"v_fmac_f32_e32 v128, v254, v140\n"
"v_fmac_f32_e32 v129, v254, v141\n"
"v_mfma_f32_16x16x32_bf16 v[180:183], a[72:75], v[136:139], v[180:183]\n"
"ds_read_b128 v[184:187], v4 offset:1024\n"
"ds_read_b128 v[196:199], v9 offset:704\n"
"v_mfma_f32_16x16x32_bf16 v[180:183], a[76:79], v[144:147], v[180:183]\n"
"ds_read_b128 v[200:203], v5 offset:1024\n"
"v_fmac_f32_e32 v130, v254, v142\n"
"v_mfma_f32_16x16x32_bf16 v[180:183], a[80:83], v[120:123], v[180:183]\n"
"v_fmac_f32_e32 v131, v254, v143\n"
"v_add_f32_e32 v128, v128, v148\n"
"v_mfma_f32_16x16x32_bf16 v[180:183], a[84:87], v[192:195], v[180:183]\n"
"ds_write_b128 v3, v[96:99] offset:13312\n"
"v_mfma_f32_16x16x32_bf16 v[180:183], a[88:91], v[100:103], v[180:183]\n"
"s_waitcnt lgkmcnt(4)\n"
"v_add_f32_e32 v129, v129, v149\n"
"v_add_f32_e32 v130, v130, v150\n"
"v_mfma_f32_16x16x32_bf16 v[180:183], a[92:95], v[188:191], v[180:183]\n"
"v_add_f32_e32 v131, v131, v151\n"
"v_cndmask_b32_e64 v88, v88, v252, s[82:83]\n"
"v_mfma_f32_16x16x32_bf16 v[96:99], a[124:127], v[188:191], 0\n"
"s_waitcnt vmcnt(12)\n"
"v_cmp_ne_u64_e32 vcc, v[246:247], v[18:19]\n"
"v_mfma_f32_16x16x32_bf16 v[96:99], a[120:123], v[100:103], v[96:99]\n"
"s_cbranch_vccnz .Lmk_f40s\n"
".Lmk_f40r:\n"
"s_mov_b32 m0, 0x7c00\n"
"v_mfma_f32_16x16x32_bf16 v[96:99], a[116:119], v[192:195], v[96:99]\n"
"s_add_u32 s97, s66, 0x3c00\n"
"buffer_load_dwordx4 v0, s[4:7], s97 offen sc1 lds\n"
"v_mfma_f32_16x16x32_bf16 v[96:99], a[112:115], v[120:123], v[96:99]\n"
"s_mov_b32 m0, 0xfc00\n"
"s_add_u32 s97, s66, 0x7c00\n"
"ds_read_b128 v[140:143], v4 offset:2048\n"
"v_mfma_f32_16x16x32_bf16 v[96:99], a[108:111], v[144:147], v[96:99]\n"
"buffer_load_dwordx4 v0, s[4:7], s97 offen sc1 lds\n"
"v_mfma_f32_16x16x32_bf16 v[96:99], a[104:107], v[136:139], v[96:99]\n"
"ds_write_b128 v3, v[180:183] offset:14336\n"
"v_mfma_f32_16x16x32_bf16 v[96:99], a[100:103], v[108:111], v[96:99]\n"
"ds_read_b128 v[148:151], v9 offset:768\n"
"ds_read_b128 v[180:183], v5 offset:2048\n"
"v_mfma_f32_16x16x32_bf16 v[96:99], a[96:99], v[92:95], v[96:99]\n"
"s_waitcnt lgkmcnt(5)\n"
"v_fmac_f32_e32 v184, v254, v196\n"
"v_fmac_f32_e32 v185, v254, v197\n"
"v_mfma_f32_16x16x32_bf16 v[204:207], v[24:27], v[92:95], 0\n"
"ds_read_b128 v[208:211], v4 offset:3072\n"
"ds_read_b128 v[212:215], v9 offset:832\n"
"v_mfma_f32_16x16x32_bf16 v[204:207], v[28:31], v[108:111], v[204:207]\n"
"ds_read_b128 v[216:219], v5 offset:3072\n"
"v_fmac_f32_e32 v186, v254, v198\n"
"v_mfma_f32_16x16x32_bf16 v[204:207], v[32:35], v[136:139], v[204:207]\n"
"v_fmac_f32_e32 v187, v254, v199\n"
"v_add_f32_e32 v184, v184, v200\n"
"v_mfma_f32_16x16x32_bf16 v[204:207], v[36:39], v[144:147], v[204:207]\n"
"v_add_f32_e32 v185, v185, v201\n"
"v_add_f32_e32 v186, v186, v202\n"
"v_mfma_f32_16x16x32_bf16 v[204:207], v[40:43], v[120:123], v[204:207]\n"
"s_waitcnt lgkmcnt(3)\n"
"v_add_f32_e32 v187, v187, v203\n"
"v_fmac_f32_e32 v140, v254, v148\n"
"v_mfma_f32_16x16x32_bf16 v[204:207], v[44:47], v[192:195], v[204:207]\n"
"ds_write_b128 v3, v[96:99] offset:15360\n"
"v_mfma_f32_16x16x32_bf16 v[204:207], v[48:51], v[100:103], v[204:207]\n"
"v_fmac_f32_e32 v141, v254, v149\n"
"v_fmac_f32_e32 v142, v254, v150\n"
"v_mfma_f32_16x16x32_bf16 v[204:207], v[52:55], v[188:191], v[204:207]\n"
"ds_read_b128 v[96:99], v4 offset:4096\n"
"ds_read_b128 v[196:199], v9 offset:512\n"
"v_mfma_f32_16x16x32_bf16 v[200:203], v[84:87], v[188:191], 0\n"
"ds_read_b128 v[188:191], v5 offset:4096\n"
"v_fmac_f32_e32 v143, v254, v151\n"
"v_mfma_f32_16x16x32_bf16 v[200:203], v[80:83], v[100:103], v[200:203]\n"
"v_add_f32_e32 v140, v140, v180\n"
"v_add_f32_e32 v141, v141, v181\n"
"v_mfma_f32_16x16x32_bf16 v[200:203], v[76:79], v[192:195], v[200:203]\n"
"s_waitcnt lgkmcnt(4)\n"
"v_add_f32_e32 v142, v142, v182\n"
"v_add_f32_e32 v143, v143, v183\n"
"v_mfma_f32_16x16x32_bf16 v[200:203], v[72:75], v[120:123], v[200:203]\n"
"v_fmac_f32_e32 v208, v254, v212\n"
"v_fmac_f32_e32 v209, v254, v213\n"
"v_mfma_f32_16x16x32_bf16 v[200:203], v[68:71], v[144:147], v[200:203]\n"
"v_fmac_f32_e32 v210, v254, v214\n"
"v_fmac_f32_e32 v211, v254, v215\n"
"v_mfma_f32_16x16x32_bf16 v[200:203], v[64:67], v[136:139], v[200:203]\n"
"ds_write_b128 v3, v[204:207] offset:16384\n"
"v_mfma_f32_16x16x32_bf16 v[200:203], v[60:63], v[108:111], v[200:203]\n"
"ds_read_b128 v[100:103], v4 offset:5120\n"
"ds_read_b128 v[108:111], v9 offset:576\n"
"v_mfma_f32_16x16x32_bf16 v[200:203], v[56:59], v[92:95], v[200:203]\n"
"s_waitcnt vmcnt(12)\n"
"s_waitcnt vmcnt(3)\n"
"v_cmp_ne_u64_e32 vcc, v[250:251], v[18:19]\n"
"; @B G1 3.3\n"
"v_mfma_f32_16x16x32_bf16 v[128:131], a[0:3], v[116:119], v[128:131]\n"
"s_cbranch_vccnz .Lmk_f41s\n"
".Lmk_f41r:\n"
"ds_read_b128 v[92:95], v5 offset:5120\n"
"v_mfma_f32_16x16x32_bf16 v[128:131], a[4:7], v[132:135], v[128:131]\n"
"s_add_u32 s98, s61, 0xc40000\n"
"s_add_u32 s97, s56, 0x440000\n"
"v_add_f32_e32 v208, v208, v216\n"
"v_mfma_f32_16x16x32_bf16 v[128:131], a[8:11], v[160:163], v[128:131]\n"
"buffer_load_dwordx4 v[120:123], v0, s[4:7], s97 offen sc1\n"
"v_mfma_f32_16x16x32_bf16 v[128:131], a[12:15], v[156:159], v[128:131]\n"
"buffer_store_dwordx4 v[172:175], v0, s[8:11], s98 offen offset:1024 sc1\n"
"v_mfma_f32_16x16x32_bf16 v[128:131], a[16:19], v[152:155], v[128:131]\n"
"s_add_u32 s98, s69, 0\n"
"v_add_f32_e32 v209, v209, v217\n"
"v_add_f32_e32 v210, v210, v218\n"
"v_mfma_f32_16x16x32_bf16 v[128:131], a[20:23], v[112:115], v[128:131]\n"
"ds_write_b128 v3, v[200:203] offset:17408\n"
"v_mfma_f32_16x16x32_bf16 v[128:131], a[24:27], v[164:167], v[128:131]\n"
"buffer_load_dwordx4 v[136:139], v0, s[4:7], s97 offen offset:1024 sc1\n"
"v_mfma_f32_16x16x32_bf16 v[128:131], a[28:31], v[168:171], v[128:131]\n"
"s_waitcnt lgkmcnt(5)\n"
"v_add_f32_e32 v211, v211, v219\n"
"v_fmac_f32_e32 v96, v254, v196\n"
"v_mfma_f32_16x16x32_bf16 v[184:187], a[60:63], v[168:171], v[184:187]\n"
"v_fmac_f32_e32 v97, v254, v197\n"
"v_fmac_f32_e32 v98, v254, v198\n"
"v_mfma_f32_16x16x32_bf16 v[184:187], a[56:59], v[164:167], v[184:187]\n"
"buffer_load_dwordx4 v[144:147], v0, s[4:7], s97 offen offset:2048 sc1\n"
"v_mfma_f32_16x16x32_bf16 v[184:187], a[52:55], v[112:115], v[184:187]\n"
"s_waitcnt lgkmcnt(0)\n"
"s_barrier\n"
"v_mfma_f32_16x16x32_bf16 v[184:187], a[48:51], v[152:155], v[184:187]\n"
"ds_read_b128 v[148:151], v6\n"
"ds_read_b128 v[180:183], v6 offset:1024\n"
"v_mfma_f32_16x16x32_bf16 v[184:187], a[44:47], v[156:159], v[184:187]\n"
"buffer_load_dwordx4 v[192:195], v0, s[4:7], s97 offen offset:3072 sc1\n"
"v_mfma_f32_16x16x32_bf16 v[184:187], a[40:43], v[160:163], v[184:187]\n"
"v_fmac_f32_e32 v99, v254, v199\n"
"v_add_f32_e32 v96, v96, v188\n"
"v_mfma_f32_16x16x32_bf16 v[184:187], a[36:39], v[132:135], v[184:187]\n"
"v_add_f32_e32 v97, v97, v189\n"
"v_add_f32_e32 v98, v98, v190\n"
"v_mfma_f32_16x16x32_bf16 v[184:187], a[32:35], v[116:119], v[184:187]\n"
"buffer_load_dwordx4 v[196:199], v1, s[4:7], s97 offen sc1\n"
"v_mfma_f32_16x16x32_bf16 v[140:143], a[64:67], v[116:119], v[140:143]\n"
"v_add_f32_e32 v99, v99, v191\n"
"v_fmac_f32_e32 v100, v254, v108\n"
"v_mfma_f32_16x16x32_bf16 v[140:143], a[68:71], v[132:135], v[140:143]\n"
"v_fmac_f32_e32 v101, v254, v109\n"
"v_fmac_f32_e32 v102, v254, v110\n"
"v_mfma_f32_16x16x32_bf16 v[140:143], a[72:75], v[160:163], v[140:143]\n"
"buffer_load_dwordx4 v[188:191], v1, s[4:7], s97 offen offset:1024 sc1\n"
"v_mfma_f32_16x16x32_bf16 v[140:143], a[76:79], v[156:159], v[140:143]\n"
"v_fmac_f32_e32 v103, v254, v111\n"
"v_add_f32_e32 v100, v100, v92\n"
"v_mfma_f32_16x16x32_bf16 v[140:143], a[80:83], v[152:155], v[140:143]\n"
"s_waitcnt lgkmcnt(0)\n"
"v_add_f32_e32 v101, v101, v93\n"
"v_add_f32_e32 v102, v102, v94\n"
"v_mfma_f32_16x16x32_bf16 v[140:143], a[84:87], v[112:115], v[140:143]\n"
"buffer_load_dwordx4 v[108:111], v1, s[4:7], s97 offen offset:2048 sc1\n"
"v_mfma_f32_16x16x32_bf16 v[140:143], a[88:91], v[164:167], v[140:143]\n"
"ds_read_b128 v[200:203], v6 offset:2048\n"
"ds_read_b128 v[204:207], v6 offset:3072\n"
"v_mfma_f32_16x16x32_bf16 v[140:143], a[92:95], v[168:171], v[140:143]\n"
"v_add_f32_e32 v103, v103, v95\n"
"v_add_f32_e32 v128, v128, v148\n"
"v_mfma_f32_16x16x32_bf16 v[208:211], a[124:127], v[168:171], v[208:211]\n"
"buffer_load_dwordx4 v[92:95], v1, s[4:7], s97 offen offset:3072 sc1\n"
"v_mfma_f32_16x16x32_bf16 v[208:211], a[120:123], v[164:167], v[208:211]\n"
"s_add_u32 s97, s57, 0x440000\n"
"v_add_f32_e32 v129, v129, v149\n"
"v_add_f32_e32 v130, v130, v150\n"
"v_mfma_f32_16x16x32_bf16 v[208:211], a[116:119], v[112:115], v[208:211]\n"
"v_add_f32_e32 v131, v131, v151\n"
"v_add_f32_e32 v184, v184, v180\n"
"v_mfma_f32_16x16x32_bf16 v[208:211], a[112:115], v[152:155], v[208:211]\n"
"buffer_load_dwordx4 v[148:151], v0, s[4:7], s97 offen sc1\n"
"v_mfma_f32_16x16x32_bf16 v[208:211], a[108:111], v[156:159], v[208:211]\n"
"v_add_f32_e32 v185, v185, v181\n"
"v_add_f32_e32 v186, v186, v182\n"
"v_mfma_f32_16x16x32_bf16 v[208:211], a[104:107], v[160:163], v[208:211]\n"
"v_add_f32_e32 v187, v187, v183\n"
"v_mul_f32_e32 v128, v128, v255\n"
"v_mfma_f32_16x16x32_bf16 v[208:211], a[100:103], v[132:135], v[208:211]\n"
"buffer_load_dwordx4 v[180:183], v0, s[4:7], s97 offen offset:1024 sc1\n"
"v_mfma_f32_16x16x32_bf16 v[208:211], a[96:99], v[116:119], v[208:211]\n"
"v_mul_f32_e32 v129, v129, v255\n"
"v_mul_f32_e32 v130, v130, v255\n"
"v_mfma_f32_16x16x32_bf16 v[96:99], v[24:27], v[116:119], v[96:99]\n"
"v_mul_f32_e32 v131, v131, v255\n"
"v_mul_f32_e32 v184, v184, v255\n"
"v_mfma_f32_16x16x32_bf16 v[96:99], v[28:31], v[132:135], v[96:99]\n"
"buffer_load_dwordx4 v[24:27], v0, s[4:7], s97 offen offset:2048 sc1\n"
"v_mfma_f32_16x16x32_bf16 v[96:99], v[32:35], v[160:163], v[96:99]\n"
"v_mul_f32_e32 v185, v185, v255\n"
"v_mul_f32_e32 v186, v186, v255\n"
"v_mfma_f32_16x16x32_bf16 v[96:99], v[36:39], v[156:159], v[96:99]\n"
"v_mul_f32_e32 v187, v187, v255\n"
"v_cvt_pk_bf16_f32 v28, v128, v129\n"
"v_mfma_f32_16x16x32_bf16 v[96:99], v[40:43], v[152:155], v[96:99]\n"
"buffer_load_dwordx4 v[32:35], v0, s[4:7], s97 offen offset:3072 sc1\n"
"v_mfma_f32_16x16x32_bf16 v[96:99], v[44:47], v[112:115], v[96:99]\n"
"s_waitcnt lgkmcnt(0)\n"
"v_cvt_pk_bf16_f32 v29, v130, v131\n"
"v_cvt_pk_bf16_f32 v30, v184, v185\n"
"v_mfma_f32_16x16x32_bf16 v[96:99], v[48:51], v[164:167], v[96:99]\n"
"s_add_u32 s96, s63, 0xc4000\n"
"ds_read_b128 v[36:39], v6 offset:4096\n"
"ds_read_b128 v[40:43], v6 offset:5120\n"
"v_mfma_f32_16x16x32_bf16 v[96:99], v[52:55], v[168:171], v[96:99]\n"
"buffer_load_dwordx4 v[44:47], v1, s[4:7], s97 offen sc1\n"
"v_mfma_f32_16x16x32_bf16 v[100:103], v[84:87], v[168:171], v[100:103]\n"
"buffer_store_dword v172, v14, s[8:11], s96 offen sc1\n"
"v_mfma_f32_16x16x32_bf16 v[100:103], v[80:83], v[164:167], v[100:103]\n"
"v_cvt_pk_bf16_f32 v31, v186, v187\n"
"v_add_f32_e32 v140, v140, v200\n"
"v_mfma_f32_16x16x32_bf16 v[100:103], v[76:79], v[112:115], v[100:103]\n"
"buffer_load_dwordx4 v[48:51], v1, s[4:7], s97 offen offset:1024 sc1\n"
"ds_write_b128 v7, v[28:31] offset:16384\n"
"v_mfma_f32_16x16x32_bf16 v[100:103], v[72:75], v[152:155], v[100:103]\n"
"v_add_f32_e32 v141, v141, v201\n"
"v_add_f32_e32 v142, v142, v202\n"
"v_mfma_f32_16x16x32_bf16 v[100:103], v[68:71], v[156:159], v[100:103]\n"
"v_add_f32_e32 v143, v143, v203\n"
"v_add_f32_e32 v208, v208, v204\n"
"v_mfma_f32_16x16x32_bf16 v[100:103], v[64:67], v[160:163], v[100:103]\n"
"buffer_load_dwordx4 v[28:31], v1, s[4:7], s97 offen offset:2048 sc1\n"
"v_mfma_f32_16x16x32_bf16 v[100:103], v[60:63], v[132:135], v[100:103]\n"
"v_add_f32_e32 v209, v209, v205\n"
"v_add_f32_e32 v210, v210, v206\n"
"v_mfma_f32_16x16x32_bf16 v[100:103], v[56:59], v[116:119], v[100:103]\n"
"buffer_load_dwordx4 a[0:3], v0, s[8:11], s98 offen sc1\n"
"buffer_load_dwordx4 v[56:59], v1, s[4:7], s97 offen offset:3072 sc1\n"
"v_add_f32_e32 v211, v211, v207\n"
"v_mul_f32_e32 v140, v140, v255\n"
"v_mul_f32_e32 v141, v141, v255\n"
"v_mul_f32_e32 v142, v142, v255\n"
"v_mul_f32_e32 v143, v143, v255\n"
"v_mul_f32_e32 v208, v208, v255\n"
"v_mul_f32_e32 v209, v209, v255\n"
"v_mul_f32_e32 v210, v210, v255\n"
"v_mul_f32_e32 v211, v211, v255\n"
"v_cvt_pk_bf16_f32 v52, v140, v141\n"
"v_cvt_pk_bf16_f32 v53, v142, v143\n"
"v_cvt_pk_bf16_f32 v54, v208, v209\n"
"v_cvt_pk_bf16_f32 v55, v210, v211\n"
"v_cndmask_b32_e64 v89, v89, v252, s[82:83]\n"
"v_cndmask_b32_e64 v90, v90, v252, s[82:83]\n"
"v_cndmask_b32_e64 v91, v91, v252, s[82:83]\n"
"v_cndmask_b32_e64 v176, v176, v252, s[82:83]\n"
"v_cndmask_b32_e64 v177, v177, v252, s[82:83]\n"
"v_cndmask_b32_e64 v178, v178, v252, s[82:83]\n"
"v_cndmask_b32_e64 v179, v179, v252, s[82:83]\n"
"ds_write_b128 v7, v[52:55] offset:49152\n"
"buffer_load_dwordx4 a[4:7], v0, s[8:11], s98 offen offset:1024 sc1\n"
"buffer_load_dwordx4 a[8:11], v0, s[8:11], s98 offen offset:2048 sc1\n"
"buffer_load_dwordx4 a[12:15], v0, s[8:11], s98 offen offset:3072 sc1\n"
"buffer_load_dwordx4 a[16:19], v1, s[8:11], s98 offen sc1\n"
"buffer_load_dwordx4 a[20:23], v1, s[8:11], s98 offen offset:1024 sc1\n"
".Lmk_b4:\n"
"; @B tail\n"
";\n"
"s_waitcnt lgkmcnt(2)\n"
"v_add_f32_e32 v96, v96, v36\n"
"v_add_f32_e32 v97, v97, v37\n"
"v_add_f32_e32 v98, v98, v38\n"
"v_add_f32_e32 v99, v99, v39\n"
"v_add_f32_e32 v100, v100, v40\n"
"v_add_f32_e32 v101, v101, v41\n"
"v_add_f32_e32 v102, v102, v42\n"
"v_add_f32_e32 v103, v103, v43\n"
"v_mul_f32_e32 v96, v96, v253\n"
"v_mul_f32_e32 v97, v97, v253\n"
"v_mul_f32_e32 v98, v98, v253\n"
"v_mul_f32_e32 v99, v99, v253\n"
"v_mul_f32_e32 v100, v100, v253\n"
"v_mul_f32_e32 v101, v101, v253\n"
"v_mul_f32_e32 v102, v102, v253\n"
"v_mul_f32_e32 v103, v103, v253\n"
"v_cvt_pk_bf16_f32 v36, v96, v97\n"
"v_cvt_pk_bf16_f32 v37, v98, v99\n"
"v_cvt_pk_bf16_f32 v38, v100, v101\n"
"v_cvt_pk_bf16_f32 v39, v102, v103\n"
"ds_write_b128 v2, v[36:39] offset:16384\n"
"s_waitcnt vmcnt(24)\n"
";\n"
"; @B AT a\n"
";\n"
"s_cmp_lg_u32 s0, 0\n"
"s_cbranch_scc1 .Lmk_exit\n"
"ds_read_b128 v[36:39], v2 offset:17408\n"
"ds_read_b128 v[40:43], v15 offset:16384\n"
"v_xor_b32_e32 v255, 32, v15\n"
"ds_read_b128 v[52:55], v255 offset:16640\n"
"ds_read_b128 v[60:63], v2 offset:16384\n"
"ds_read_b128 v[64:67], v17 offset:15360\n"
"v_xor_b32_e32 v255, 32, v17\n"
"ds_read_b128 v[68:71], v255 offset:15616\n"
"ds_read_b64_tr_b16 v[72:73], v16 offset:16384\n"
"v_xor_b32_e32 v255, 32, v16\n"
"ds_read_b64_tr_b16 v[74:75], v255 offset:16640\n"
"ds_read_b64_tr_b16 v[76:77], v255 offset:16384\n"
"ds_read_b64_tr_b16 v[78:79], v16 offset:16640\n"
"ds_read_b64_tr_b16 v[84:85], v16 offset:15360\n"
"v_mov_b32_e32 v80, 0x3f803f80\n"
"v_mov_b32_e32 v81, 0x3f803f80\n"
"v_mov_b32_e32 v82, 0x3f803f80\n"
"v_mov_b32_e32 v83, 0x3f803f80\n"
"v_xor_b32_e32 v255, 32, v16\n"
"ds_read_b64_tr_b16 v[86:87], v255 offset:15616\n"
"s_waitcnt lgkmcnt(13)\n"
"ds_read_b64_tr_b16 v[96:97], v255 offset:15360\n"
"ds_read_b64_tr_b16 v[98:99], v16 offset:15616\n"
"v_mov_b32_e32 v100, 0x3f803f80\n"
"v_mov_b32_e32 v101, 0x3f803f80\n"
"v_mov_b32_e32 v102, 0x3f803f80\n"
"v_mov_b32_e32 v103, 0x3f803f80\n"
"s_waitcnt lgkmcnt(11)\n"
"v_mfma_f32_16x16x32_bf16 v[104:107], v[40:43], v[36:39], v[104:107]\n"
"v_mfma_f32_16x16x32_bf16 v[124:127], v[52:55], v[36:39], v[124:127]\n"
"s_waitcnt lgkmcnt(8)\n"
"v_mfma_f32_16x16x32_bf16 v[88:91], v[64:67], v[60:63], v[88:91]\n"
"v_mfma_f32_16x16x32_bf16 v[176:179], v[68:71], v[60:63], v[176:179]\n"
"s_waitcnt lgkmcnt(4)\n"
"s_nop 2\n"
"v_max_f32_e32 v255, v104, v105\n"
"s_nop 1\n"
"v_max_f32_e32 v254, v88, v89\n"
"v_max3_f32 v255, v255, v106, v107\n"
"v_max3_f32 v254, v254, v90, v91\n"
"v_max3_f32 v255, v255, v124, v125\n"
"v_max3_f32 v254, v254, v176, v177\n"
"v_max3_f32 v255, v255, v126, v127\n"
"v_max3_f32 v254, v254, v178, v179\n"
"v_mov_b32_e32 v253, v255\n"
"v_mov_b32_e32 v252, v254\n"
"s_nop 0\n"
"v_permlane16_swap_b32_e32 v255, v253\n"
"v_permlane16_swap_b32_e32 v254, v252\n"
"v_max_f32_e32 v255, v255, v253\n"
"v_max_f32_e32 v254, v254, v252\n"
"v_mov_b32_e32 v253, v255\n"
"v_mov_b32_e32 v252, v254\n"
"s_nop 0\n"
"v_permlane32_swap_b32_e32 v255, v253\n"
"v_permlane32_swap_b32_e32 v254, v252\n"
"v_max_f32_e32 v255, v255, v253\n"
"v_max_f32_e32 v254, v254, v252\n"
"v_sub_f32_e32 v104, v104, v255\n"
"v_sub_f32_e32 v105, v105, v255\n"
"v_sub_f32_e32 v106, v106, v255\n"
"v_sub_f32_e32 v107, v107, v255\n"
"v_sub_f32_e32 v124, v124, v255\n"
"v_sub_f32_e32 v125, v125, v255\n"
"v_sub_f32_e32 v126, v126, v255\n"
"v_sub_f32_e32 v127, v127, v255\n"
"v_exp_f32_e32 v104, v104\n"
"v_exp_f32_e32 v105, v105\n"
"v_exp_f32_e32 v106, v106\n"
"v_exp_f32_e32 v107, v107\n"
"v_exp_f32_e32 v124, v124\n"
"v_exp_f32_e32 v125, v125\n"
"v_exp_f32_e32 v126, v126\n"
"v_exp_f32_e32 v127, v127\n"
"v_cvt_pk_bf16_f32 v36, v104, v105\n"
"v_cvt_pk_bf16_f32 v37, v106, v107\n"
"v_cvt_pk_bf16_f32 v38, v124, v125\n"
"v_cvt_pk_bf16_f32 v39, v126, v127\n"
"v_sub_f32_e32 v88, v88, v254\n"
"v_sub_f32_e32 v89, v89, v254\n"
"v_sub_f32_e32 v90, v90, v254\n"
"v_sub_f32_e32 v91, v91, v254\n"
"v_sub_f32_e32 v176, v176, v254\n"
"v_sub_f32_e32 v177, v177, v254\n"
"v_sub_f32_e32 v178, v178, v254\n"
"v_mfma_f32_16x16x32_bf16 v[40:43], v[80:83], v[36:39], 0\n"
"v_sub_f32_e32 v179, v179, v254\n"
"v_exp_f32_e32 v88, v88\n"
"v_exp_f32_e32 v89, v89\n"
"v_exp_f32_e32 v90, v90\n"
"v_mfma_f32_16x16x32_bf16 v[52:55], v[72:75], v[36:39], 0\n"
"v_exp_f32_e32 v91, v91\n"
"v_exp_f32_e32 v176, v176\n"
"v_exp_f32_e32 v177, v177\n"
"v_exp_f32_e32 v178, v178\n"
"v_mfma_f32_16x16x32_bf16 v[60:63], v[76:79], v[36:39], 0\n"
"s_waitcnt lgkmcnt(0)\n"
"v_exp_f32_e32 v179, v179\n"
"v_cvt_pk_bf16_f32 v36, v88, v89\n"
"v_cvt_pk_bf16_f32 v37, v90, v91\n"
"v_cvt_pk_bf16_f32 v38, v176, v177\n"
"v_cvt_pk_bf16_f32 v39, v178, v179\n"
"v_rcp_f32_e32 v40, v40\n"
"s_nop 0\n"
"v_mul_f32_e32 v52, v52, v40\n"
"v_mul_f32_e32 v53, v53, v40\n"
"v_mul_f32_e32 v54, v54, v40\n"
"v_mfma_f32_16x16x32_bf16 v[64:67], v[100:103], v[36:39], 0\n"
"v_mul_f32_e32 v55, v55, v40\n"
"v_mul_f32_e32 v60, v60, v40\n"
"v_mul_f32_e32 v61, v61, v40\n"
"v_mul_f32_e32 v62, v62, v40\n"
"v_mul_f32_e32 v63, v63, v40\n"
"v_mfma_f32_16x16x32_bf16 v[40:43], v[84:87], v[36:39], 0\n"
"s_add_u32 s97, s60, 0x408000\n"
"v_cvt_pk_bf16_f32 v68, v52, v53\n"
"v_cvt_pk_bf16_f32 v69, v54, v55\n"
"v_cvt_pk_bf16_f32 v70, v60, v61\n"
"v_cvt_pk_bf16_f32 v71, v62, v63\n"
"v_mfma_f32_16x16x32_bf16 v[52:55], v[96:99], v[36:39], 0\n"
"buffer_store_dwordx4 v[68:71], v0, s[4:7], s97 offen sc1\n"
"s_add_u32 s97, s60, 0x400000\n"
"v_rcp_f32_e32 v64, v64\n"
"s_nop 0\n"
"v_mul_f32_e32 v40, v40, v64\n"
"v_mul_f32_e32 v41, v41, v64\n"
"v_mul_f32_e32 v42, v42, v64\n"
"v_mul_f32_e32 v43, v43, v64\n"
"v_mul_f32_e32 v52, v52, v64\n"
"v_mul_f32_e32 v53, v53, v64\n"
"v_mul_f32_e32 v54, v54, v64\n"
"v_mul_f32_e32 v55, v55, v64\n"
"v_cvt_pk_bf16_f32 v36, v40, v41\n"
"v_cvt_pk_bf16_f32 v37, v42, v43\n"
"v_cvt_pk_bf16_f32 v38, v52, v53\n"
"v_cvt_pk_bf16_f32 v39, v54, v55\n"
"buffer_store_dwordx4 v[36:39], v0, s[4:7], s97 offen sc1\n"
";\n"
"s_add_u32 s97, s58, 0x440000\n"
"buffer_load_dwordx4 v[36:39], v0, s[4:7], s97 offen sc1\n"
"buffer_load_dwordx4 v[40:43], v0, s[4:7], s97 offen offset:1024 sc1\n"
"buffer_load_dwordx4 v[52:55], v0, s[4:7], s97 offen offset:2048 sc1\n"
"buffer_load_dwordx4 v[60:63], v0, s[4:7], s97 offen offset:3072 sc1\n"
"buffer_load_dwordx4 v[64:67], v1, s[4:7], s97 offen sc1\n"
"buffer_load_dwordx4 v[68:71], v1, s[4:7], s97 offen offset:1024 sc1\n"
"buffer_load_dwordx4 v[72:75], v1, s[4:7], s97 offen offset:2048 sc1\n"
"buffer_load_dwordx4 v[76:79], v1, s[4:7], s97 offen offset:3072 sc1\n"
"buffer_load_dwordx4 a[24:27], v1, s[8:11], s98 offen offset:2048 sc1\n"
"buffer_load_dwordx4 a[28:31], v1, s[8:11], s98 offen offset:3072 sc1\n"
"s_add_u32 s98, s69, 0x2000\n"
"buffer_load_dwordx4 a[32:35], v0, s[8:11], s98 offen sc1\n"
"buffer_load_dwordx4 a[36:39], v0, s[8:11], s98 offen offset:1024 sc1\n"
"buffer_load_dwordx4 a[40:43], v0, s[8:11], s98 offen offset:2048 sc1\n"
"buffer_load_dwordx4 a[44:47], v0, s[8:11], s98 offen offset:3072 sc1\n"
"buffer_load_dwordx4 a[48:51], v1, s[8:11], s98 offen sc1\n"
"buffer_load_dwordx4 a[52:55], v1, s[8:11], s98 offen offset:1024 sc1\n"
"s_waitcnt vmcnt(39)\n"
"; @B G2 b.0\n"
"v_mfma_f32_16x16x32_bf16 v[80:83], a[128:131], v[120:123], 0\n"
"v_mfma_f32_16x16x32_bf16 v[80:83], a[132:135], v[136:139], v[80:83]\n"
"s_waitcnt vmcnt(37)\n"
"v_mfma_f32_16x16x32_bf16 v[80:83], a[136:139], v[144:147], v[80:83]\n"
"s_add_u32 s97, s59, 0x440000\n"
"buffer_load_dwordx4 v[84:87], v0, s[4:7], s97 offen sc1\n"
"v_mfma_f32_16x16x32_bf16 v[80:83], a[140:143], v[192:195], v[80:83]\n"
"s_waitcnt vmcnt(36)\n"
"v_mfma_f32_16x16x32_bf16 v[80:83], a[144:147], v[196:199], v[80:83]\n"
"v_mfma_f32_16x16x32_bf16 v[80:83], a[148:151], v[188:191], v[80:83]\n"
"s_waitcnt vmcnt(34)\n"
"buffer_load_dwordx4 v[88:91], v0, s[4:7], s97 offen offset:1024 sc1\n"
"v_mfma_f32_16x16x32_bf16 v[80:83], a[152:155], v[108:111], v[80:83]\n"
"v_mfma_f32_16x16x32_bf16 v[80:83], a[156:159], v[92:95], v[80:83]\n"
"v_mfma_f32_16x16x32_bf16 v[96:99], a[188:191], v[92:95], 0\n"
"buffer_load_dwordx4 v[100:103], v0, s[4:7], s97 offen offset:2048 sc1\n"
"v_mfma_f32_16x16x32_bf16 v[96:99], a[184:187], v[108:111], v[96:99]\n"
"v_mfma_f32_16x16x32_bf16 v[96:99], a[180:183], v[188:191], v[96:99]\n"
"v_mfma_f32_16x16x32_bf16 v[96:99], a[176:179], v[196:199], v[96:99]\n"
"buffer_load_dwordx4 v[104:107], v0, s[4:7], s97 offen offset:3072 sc1\n"
"v_mfma_f32_16x16x32_bf16 v[96:99], a[172:175], v[192:195], v[96:99]\n"
"s_nop 0\n"
"v_cmp_u_f32_e32 vcc, v80, v80\n"
"v_mfma_f32_16x16x32_bf16 v[96:99], a[168:171], v[144:147], v[96:99]\n"
"s_cbranch_vccnz .Lmk_p63s\n"
".Lmk_p63r:\n"
"v_mfma_f32_16x16x32_bf16 v[96:99], a[164:167], v[136:139], v[96:99]\n"
"ds_write_b128 v3, v[80:83]\n"
"v_mfma_f32_16x16x32_bf16 v[96:99], a[160:163], v[120:123], v[96:99]\n"
"buffer_load_dwordx4 v[80:83], v1, s[4:7], s97 offen sc1\n"
"v_mfma_f32_16x16x32_bf16 v[112:115], a[192:195], v[120:123], 0\n"
"v_mfma_f32_16x16x32_bf16 v[112:115], a[196:199], v[136:139], v[112:115]\n"
"v_mfma_f32_16x16x32_bf16 v[112:115], a[200:203], v[144:147], v[112:115]\n"
"buffer_load_dwordx4 v[116:119], v1, s[4:7], s97 offen offset:1024 sc1\n"
"v_mfma_f32_16x16x32_bf16 v[112:115], a[204:207], v[192:195], v[112:115]\n"
"v_mfma_f32_16x16x32_bf16 v[112:115], a[208:211], v[196:199], v[112:115]\n"
"v_mfma_f32_16x16x32_bf16 v[112:115], a[212:215], v[188:191], v[112:115]\n"
"ds_write_b128 v3, v[96:99] offset:1024\n"
"v_mfma_f32_16x16x32_bf16 v[112:115], a[216:219], v[108:111], v[112:115]\n"
"buffer_load_dwordx4 v[96:99], v1, s[4:7], s97 offen offset:2048 sc1\n"
"v_mfma_f32_16x16x32_bf16 v[112:115], a[220:223], v[92:95], v[112:115]\n"
"v_mfma_f32_16x16x32_bf16 v[124:127], a[252:255], v[92:95], 0\n"
"v_mfma_f32_16x16x32_bf16 v[124:127], a[248:251], v[108:111], v[124:127]\n"
"buffer_load_dwordx4 v[92:95], v1, s[4:7], s97 offen offset:3072 sc1\n"
"v_mfma_f32_16x16x32_bf16 v[124:127], a[244:247], v[188:191], v[124:127]\n"
"v_mfma_f32_16x16x32_bf16 v[124:127], a[240:243], v[196:199], v[124:127]\n"
"v_mfma_f32_16x16x32_bf16 v[124:127], a[236:239], v[192:195], v[124:127]\n"
"s_mov_b32 s97, .Lmk_a0-.Lmk_g67\n"
"s_getpc_b64 s[84:85]\n"
".Lmk_g67:\n"
"s_add_u32 s84, s84, s97\n"
"s_addc_u32 s85, s85, 0\n"
"s_and_b32 s84, s84, -16\n"
"v_mfma_f32_16x16x32_bf16 v[124:127], a[232:235], v[144:147], v[124:127]\n"
"ds_write_b128 v3, v[112:115] offset:2048\n"
"v_mfma_f32_16x16x32_bf16 v[124:127], a[228:231], v[136:139], v[124:127]\n"
"s_and_b32 s85, s85, 0xffff\n"
"s_sub_u32 s97, .Lmk_end-.Lmk_g67, s97\n"
"s_min_u32 s86, s97, 0x3000\n"
"s_mov_b32 s87, 0x20000\n"
"v_mfma_f32_16x16x32_bf16 v[124:127], a[224:227], v[120:123], v[124:127]\n"
"s_waitcnt vmcnt(37)\n"
"s_mov_b32 m0, 0x27800\n"
"s_nop 0\n"
"buffer_load_dwordx4 v0, s[84:87], s99 offen lds\n"
"; @B G2 b.1\n"
"v_mfma_f32_16x16x32_bf16 v[108:111], a[128:131], v[148:151], 0\n"
"v_mfma_f32_16x16x32_bf16 v[108:111], a[132:135], v[180:183], v[108:111]\n"
"v_mfma_f32_16x16x32_bf16 v[108:111], a[136:139], v[24:27], v[108:111]\n"
"v_mfma_f32_16x16x32_bf16 v[108:111], a[140:143], v[32:35], v[108:111]\n"
"s_waitcnt vmcnt(32)\n"
"v_mfma_f32_16x16x32_bf16 v[108:111], a[144:147], v[44:47], v[108:111]\n"
"v_mfma_f32_16x16x32_bf16 v[108:111], a[148:151], v[48:51], v[108:111]\n"
"ds_write_b128 v3, v[124:127] offset:3072\n"
"v_mfma_f32_16x16x32_bf16 v[108:111], a[152:155], v[28:31], v[108:111]\n"
"v_mfma_f32_16x16x32_bf16 v[108:111], a[156:159], v[56:59], v[108:111]\n"
"v_mfma_f32_16x16x32_bf16 v[112:115], a[188:191], v[56:59], 0\n"
"s_add_u32 s97, s56, 0x400000\n"
"buffer_load_dwordx4 v[120:123], v0, s[4:7], s97 offen sc1\n"
"v_mfma_f32_16x16x32_bf16 v[112:115], a[184:187], v[28:31], v[112:115]\n"
"v_mfma_f32_16x16x32_bf16 v[112:115], a[180:183], v[48:51], v[112:115]\n"
"v_mfma_f32_16x16x32_bf16 v[112:115], a[176:179], v[44:47], v[112:115]\n"
"buffer_load_dwordx4 v[124:127], v0, s[4:7], s97 offen offset:1024 sc1\n"
"v_mfma_f32_16x16x32_bf16 v[112:115], a[172:175], v[32:35], v[112:115]\n"
"v_cmp_u_f32_e32 vcc, v108, v108\n"
"v_mfma_f32_16x16x32_bf16 v[112:115], a[168:171], v[24:27], v[112:115]\n"
"s_cbranch_vccnz .Lmk_p64s\n"
".Lmk_p64r:\n"
"v_mfma_f32_16x16x32_bf16 v[112:115], a[164:167], v[180:183], v[112:115]\n"
"ds_write_b128 v3, v[108:111] offset:6144\n"
"v_mfma_f32_16x16x32_bf16 v[112:115], a[160:163], v[148:151], v[112:115]\n"
"buffer_load_dwordx4 v[108:111], v0, s[4:7], s97 offen offset:2048 sc1\n"
"v_mfma_f32_16x16x32_bf16 v[128:131], a[192:195], v[148:151], 0\n"
"v_mfma_f32_16x16x32_bf16 v[128:131], a[196:199], v[180:183], v[128:131]\n"
"v_mfma_f32_16x16x32_bf16 v[128:131], a[200:203], v[24:27], v[128:131]\n"
"buffer_load_dwordx4 v[132:135], v0, s[4:7], s97 offen offset:3072 sc1\n"
"v_mfma_f32_16x16x32_bf16 v[128:131], a[204:207], v[32:35], v[128:131]\n"
"v_mfma_f32_16x16x32_bf16 v[128:131], a[208:211], v[44:47], v[128:131]\n"
"ds_read_b128 v[136:139], v2 offset:8192\n"
"ds_read_b128 v[140:143], v2 offset:9216\n"
"v_mfma_f32_16x16x32_bf16 v[128:131], a[212:215], v[48:51], v[128:131]\n"
"ds_write_b128 v3, v[112:115] offset:7168\n"
"v_mfma_f32_16x16x32_bf16 v[128:131], a[216:219], v[28:31], v[128:131]\n"
"buffer_load_dwordx4 v[112:115], v1, s[4:7], s97 offen sc1\n"
"v_mfma_f32_16x16x32_bf16 v[128:131], a[220:223], v[56:59], v[128:131]\n"
"v_mfma_f32_16x16x32_bf16 v[144:147], a[252:255], v[56:59], 0\n"
"v_mfma_f32_16x16x32_bf16 v[144:147], a[248:251], v[28:31], v[144:147]\n"
"buffer_load_dwordx4 v[28:31], v1, s[4:7], s97 offen offset:1024 sc1\n"
"s_waitcnt lgkmcnt(1)\n"
"v_mfma_f32_16x16x32_bf16 v[144:147], a[244:247], v[48:51], v[144:147]\n"
"v_lshlrev_b32_e32 v255, 16, v136\n"
"v_and_b32_e32 v254, 0xffff0000, v136\n"
"v_lshlrev_b32_e32 v253, 16, v137\n"
"v_mfma_f32_16x16x32_bf16 v[144:147], a[240:243], v[44:47], v[144:147]\n"
"v_and_b32_e32 v252, 0xffff0000, v137\n"
"v_lshlrev_b32_e32 v251, 16, v138\n"
"v_and_b32_e32 v250, 0xffff0000, v138\n"
"v_mfma_f32_16x16x32_bf16 v[144:147], a[236:239], v[32:35], v[144:147]\n"
"buffer_load_dwordx4 v[32:35], v1, s[4:7], s97 offen offset:2048 sc1\n"
"v_lshlrev_b32_e32 v249, 16, v139\n"
"v_mfma_f32_16x16x32_bf16 v[144:147], a[232:235], v[24:27], v[144:147]\n"
"ds_write_b128 v3, v[128:131] offset:8192\n"
"v_mfma_f32_16x16x32_bf16 v[144:147], a[228:231], v[180:183], v[144:147]\n"
"v_and_b32_e32 v248, 0xffff0000, v139\n"
"v_lshlrev_b32_e32 v247, 16, v140\n"
"v_and_b32_e32 v246, 0xffff0000, v140\n"
"v_mfma_f32_16x16x32_bf16 v[144:147], a[224:227], v[148:151], v[144:147]\n"
"s_waitcnt vmcnt(28)\n"
"buffer_load_dwordx4 v[24:27], v1, s[4:7], s97 offen offset:3072 sc1\n"
"; @B G2 b.2\n"
"v_mfma_f32_16x16x32_bf16 v[44:47], a[128:131], v[36:39], 0\n"
"s_add_u32 s97, s57, 0x400000\n"
"v_lshlrev_b32_e32 v245, 16, v141\n"
"v_and_b32_e32 v244, 0xffff0000, v141\n"
"v_lshlrev_b32_e32 v243, 16, v142\n"
"v_mfma_f32_16x16x32_bf16 v[44:47], a[132:135], v[40:43], v[44:47]\n"
"v_and_b32_e32 v242, 0xffff0000, v142\n"
"v_lshlrev_b32_e32 v241, 16, v143\n"
"v_and_b32_e32 v240, 0xffff0000, v143\n"
"v_mfma_f32_16x16x32_bf16 v[44:47], a[136:139], v[52:55], v[44:47]\n"
"buffer_load_dwordx4 v[48:51], v0, s[4:7], s97 offen sc1\n"
"v_mfma_f32_16x16x32_bf16 v[44:47], a[140:143], v[60:63], v[44:47]\n"
"s_waitcnt vmcnt(26)\n"
"v_mfma_f32_16x16x32_bf16 v[44:47], a[144:147], v[64:67], v[44:47]\n"
"v_mfma_f32_16x16x32_bf16 v[44:47], a[148:151], v[68:71], v[44:47]\n"
"ds_write_b128 v3, v[144:147] offset:9216\n"
"v_mfma_f32_16x16x32_bf16 v[44:47], a[152:155], v[72:75], v[44:47]\n"
"buffer_load_dwordx4 v[56:59], v0, s[4:7], s97 offen offset:1024 sc1\n"
"v_mfma_f32_16x16x32_bf16 v[44:47], a[156:159], v[76:79], v[44:47]\n"
"v_mfma_f32_16x16x32_bf16 v[128:131], a[188:191], v[76:79], 0\n"
"v_mfma_f32_16x16x32_bf16 v[128:131], a[184:187], v[72:75], v[128:131]\n"
"s_waitcnt lgkmcnt(0)\n"
"s_barrier\n"
"v_mfma_f32_16x16x32_bf16 v[128:131], a[180:183], v[68:71], v[128:131]\n"
"buffer_load_dwordx4 v[136:139], v0, s[4:7], s97 offen offset:2048 sc1\n"
"v_mfma_f32_16x16x32_bf16 v[128:131], a[176:179], v[64:67], v[128:131]\n"
"ds_read_b128 v[140:143], v4\n"
"ds_read_b128 v[144:147], v5\n"
"v_mfma_f32_16x16x32_bf16 v[128:131], a[172:175], v[60:63], v[128:131]\n"
"v_cmp_u_f32_e32 vcc, v44, v44\n"
"v_mfma_f32_16x16x32_bf16 v[128:131], a[168:171], v[52:55], v[128:131]\n"
"s_cbranch_vccnz .Lmk_p65s\n"
".Lmk_p65r:\n"
"v_mfma_f32_16x16x32_bf16 v[128:131], a[164:167], v[40:43], v[128:131]\n"
"ds_write_b128 v3, v[44:47] offset:12288\n"
"v_mfma_f32_16x16x32_bf16 v[128:131], a[160:163], v[36:39], v[128:131]\n"
"buffer_load_dwordx4 v[44:47], v0, s[4:7], s97 offen offset:3072 sc1\n"
"v_mfma_f32_16x16x32_bf16 v[148:151], a[192:195], v[36:39], 0\n"
"ds_read_b128 v[152:155], v4 offset:1024\n"
"ds_read_b128 v[156:159], v5 offset:1024\n"
"s_waitcnt lgkmcnt(3)\n"
"v_mfma_f32_16x16x32_bf16 v[148:151], a[196:199], v[40:43], v[148:151]\n"
"v_add_f32_e32 v140, v140, v255\n"
"v_add_f32_e32 v141, v141, v254\n"
"v_add_f32_e32 v142, v142, v253\n"
"v_mfma_f32_16x16x32_bf16 v[148:151], a[200:203], v[52:55], v[148:151]\n"
"buffer_load_dwordx4 v[160:163], v1, s[4:7], s97 offen sc1\n"
"v_add_f32_e32 v143, v143, v252\n"
"v_mfma_f32_16x16x32_bf16 v[148:151], a[204:207], v[60:63], v[148:151]\n"
"v_add_f32_e32 v140, v140, v144\n"
"v_add_f32_e32 v141, v141, v145\n"
"v_add_f32_e32 v142, v142, v146\n"
"v_mfma_f32_16x16x32_bf16 v[148:151], a[208:211], v[64:67], v[148:151]\n"
"v_add_f32_e32 v143, v143, v147\n"
"v_mfma_f32_16x16x32_bf16 v[148:151], a[212:215], v[68:71], v[148:151]\n"
"ds_write_b128 v3, v[128:131] offset:13312\n"
"v_mfma_f32_16x16x32_bf16 v[148:151], a[216:219], v[72:75], v[148:151]\n"
"buffer_load_dwordx4 v[128:131], v1, s[4:7], s97 offen offset:1024 sc1\n"
"s_waitcnt lgkmcnt(1)\n"
"v_mfma_f32_16x16x32_bf16 v[148:151], a[220:223], v[76:79], v[148:151]\n"
"ds_read_b128 v[144:147], v4 offset:2048\n"
"ds_read_b128 v[164:167], v5 offset:2048\n"
"v_add_f32_e32 v152, v152, v251\n"
"v_mfma_f32_16x16x32_bf16 v[168:171], a[252:255], v[76:79], 0\n"
"v_add_f32_e32 v153, v153, v250\n"
"v_add_f32_e32 v154, v154, v249\n"
"v_add_f32_e32 v155, v155, v248\n"
"v_mfma_f32_16x16x32_bf16 v[168:171], a[248:251], v[72:75], v[168:171]\n"
"buffer_load_dwordx4 v[72:75], v1, s[4:7], s97 offen offset:2048 sc1\n"
"v_add_f32_e32 v152, v152, v156\n"
"v_mfma_f32_16x16x32_bf16 v[168:171], a[244:247], v[68:71], v[168:171]\n"
"v_add_f32_e32 v153, v153, v157\n"
"v_add_f32_e32 v154, v154, v158\n"
"v_add_f32_e32 v155, v155, v159\n"
"v_mfma_f32_16x16x32_bf16 v[168:171], a[240:243], v[64:67], v[168:171]\n"
"v_mfma_f32_16x16x32_bf16 v[168:171], a[236:239], v[60:63], v[168:171]\n"
"buffer_load_dwordx4 v[60:63], v1, s[4:7], s97 offen offset:3072 sc1\n"
"s_waitcnt lgkmcnt(0)\n"
"v_mfma_f32_16x16x32_bf16 v[168:171], a[232:235], v[52:55], v[168:171]\n"
"ds_write_b128 v3, v[148:151] offset:14336\n"
"v_mfma_f32_16x16x32_bf16 v[168:171], a[228:231], v[40:43], v[168:171]\n"
"s_add_u32 s97, s58, 0x400000\n"
"ds_read_b128 v[40:43], v4 offset:3072\n"
"ds_read_b128 v[52:55], v5 offset:3072\n"
"v_add_f32_e32 v144, v144, v247\n"
"v_mfma_f32_16x16x32_bf16 v[168:171], a[224:227], v[36:39], v[168:171]\n"
"s_waitcnt vmcnt(21)\n"
"buffer_load_dwordx4 v[36:39], v0, s[4:7], s97 offen sc1\n"
"; @B G2 b.3\n"
"v_mfma_f32_16x16x32_bf16 v[64:67], a[128:131], v[84:87], v[140:143]\n"
"v_add_f32_e32 v145, v145, v246\n"
"v_add_f32_e32 v146, v146, v245\n"
"v_add_f32_e32 v147, v147, v244\n"
"v_mfma_f32_16x16x32_bf16 v[64:67], a[132:135], v[88:91], v[64:67]\n"
"v_add_f32_e32 v144, v144, v164\n"
"v_add_f32_e32 v145, v145, v165\n"
"v_add_f32_e32 v146, v146, v166\n"
"v_mfma_f32_16x16x32_bf16 v[64:67], a[136:139], v[100:103], v[64:67]\n"
"buffer_load_dwordx4 v[68:71], v0, s[4:7], s97 offen offset:1024 sc1\n"
"v_add_f32_e32 v147, v147, v167\n"
"v_mfma_f32_16x16x32_bf16 v[64:67], a[140:143], v[104:107], v[64:67]\n"
"s_waitcnt vmcnt(19)\n"
"s_waitcnt lgkmcnt(0)\n"
"v_add_f32_e32 v40, v40, v243\n"
"v_add_f32_e32 v41, v41, v242\n"
"v_mfma_f32_16x16x32_bf16 v[64:67], a[144:147], v[80:83], v[64:67]\n"
"v_add_f32_e32 v42, v42, v241\n"
"v_add_f32_e32 v43, v43, v240\n"
"v_add_f32_e32 v40, v40, v52\n"
"v_mfma_f32_16x16x32_bf16 v[64:67], a[148:151], v[116:119], v[64:67]\n"
"ds_write_b128 v3, v[168:171] offset:15360\n"
"v_mfma_f32_16x16x32_bf16 v[64:67], a[152:155], v[96:99], v[64:67]\n"
"buffer_load_dwordx4 v[76:79], v0, s[4:7], s97 offen offset:2048 sc1\n"
"v_add_f32_e32 v41, v41, v53\n"
"v_mfma_f32_16x16x32_bf16 v[64:67], a[156:159], v[92:95], v[64:67]\n"
"v_add_f32_e32 v42, v42, v54\n"
"v_add_f32_e32 v43, v43, v55\n"
"v_mfma_f32_16x16x32_bf16 v[52:55], a[188:191], v[92:95], v[152:155]\n"
"v_mfma_f32_16x16x32_bf16 v[52:55], a[184:187], v[96:99], v[52:55]\n"
"s_waitcnt lgkmcnt(0)\n"
"s_barrier\n"
"v_mfma_f32_16x16x32_bf16 v[52:55], a[180:183], v[116:119], v[52:55]\n"
"buffer_load_dwordx4 v[148:151], v0, s[4:7], s97 offen offset:3072 sc1\n"
"v_mfma_f32_16x16x32_bf16 v[52:55], a[176:179], v[80:83], v[52:55]\n"
"ds_read_b128 v[156:159], v6\n"
"ds_read_b128 v[164:167], v6 offset:1024\n"
"v_mfma_f32_16x16x32_bf16 v[52:55], a[172:175], v[104:107], v[52:55]\n"
"ds_read_b128 v[168:171], v6 offset:2048\n"
"v_cmp_u_f32_e32 vcc, v64, v64\n"
"v_mfma_f32_16x16x32_bf16 v[52:55], a[168:171], v[100:103], v[52:55]\n"
"s_cbranch_vccnz .Lmk_p66s\n"
".Lmk_p66r:\n"
"ds_read_b128 v[140:143], v6 offset:3072\n"
"v_mfma_f32_16x16x32_bf16 v[52:55], a[164:167], v[88:91], v[52:55]\n"
"buffer_load_dwordx4 v[152:155], v1, s[4:7], s97 offen sc1\n"
"v_mfma_f32_16x16x32_bf16 v[52:55], a[160:163], v[84:87], v[52:55]\n"
"v_mfma_f32_16x16x32_bf16 v[144:147], a[192:195], v[84:87], v[144:147]\n"
"v_mfma_f32_16x16x32_bf16 v[144:147], a[196:199], v[88:91], v[144:147]\n"
"buffer_load_dwordx4 v[172:175], v1, s[4:7], s97 offen offset:1024 sc1\n"
"v_mfma_f32_16x16x32_bf16 v[144:147], a[200:203], v[100:103], v[144:147]\n"
"v_mfma_f32_16x16x32_bf16 v[144:147], a[204:207], v[104:107], v[144:147]\n"
"v_mfma_f32_16x16x32_bf16 v[144:147], a[208:211], v[80:83], v[144:147]\n"
"buffer_load_dwordx4 v[176:179], v1, s[4:7], s97 offen offset:2048 sc1\n"
"s_waitcnt lgkmcnt(0)\n"
"v_mfma_f32_16x16x32_bf16 v[144:147], a[212:215], v[116:119], v[144:147]\n"
"v_add_f32_e32 v156, v64, v156\n"
"v_add_f32_e32 v157, v65, v157\n"
"v_add_f32_e32 v158, v66, v158\n"
"v_mfma_f32_16x16x32_bf16 v[144:147], a[216:219], v[96:99], v[144:147]\n"
"v_add_f32_e32 v159, v67, v159\n"
"v_add_f32_e32 v164, v52, v164\n"
"v_add_f32_e32 v165, v53, v165\n"
"v_mfma_f32_16x16x32_bf16 v[144:147], a[220:223], v[92:95], v[144:147]\n"
"buffer_load_dwordx4 v[64:67], v1, s[4:7], s97 offen offset:3072 sc1\n"
"v_add_f32_e32 v166, v54, v166\n"
"v_mfma_f32_16x16x32_bf16 v[40:43], a[252:255], v[92:95], v[40:43]\n"
"v_add_f32_e32 v167, v55, v167\n"
"v_cvt_pk_bf16_f32 v52, v156, v157\n"
"v_cvt_pk_bf16_f32 v53, v158, v159\n"
"v_mfma_f32_16x16x32_bf16 v[40:43], a[248:251], v[96:99], v[40:43]\n"
"s_add_u32 s97, s61, 0x840000\n"
"v_cvt_pk_bf16_f32 v54, v164, v165\n"
"v_cvt_pk_bf16_f32 v55, v166, v167\n"
"v_mfma_f32_16x16x32_bf16 v[40:43], a[244:247], v[116:119], v[40:43]\n"
"buffer_store_dwordx4 v[52:55], v0, s[4:7], s97 offen sc1\n"
"v_mfma_f32_16x16x32_bf16 v[40:43], a[240:243], v[80:83], v[40:43]\n"
"ds_write_b128 v2, v[52:55] offset:8192\n"
"v_mfma_f32_16x16x32_bf16 v[40:43], a[236:239], v[104:107], v[40:43]\n"
"v_mfma_f32_16x16x32_bf16 v[40:43], a[232:235], v[100:103], v[40:43]\n"
"v_mfma_f32_16x16x32_bf16 v[40:43], a[228:231], v[88:91], v[40:43]\n"
"v_mfma_f32_16x16x32_bf16 v[40:43], a[224:227], v[84:87], v[40:43]\n"
"; @B fin b\n"
"s_add_u32 s97, s61, 0x840000\n"
"v_add_f32_e32 v168, v144, v168\n"
"v_add_f32_e32 v169, v145, v169\n"
"v_add_f32_e32 v170, v146, v170\n"
"v_add_f32_e32 v171, v147, v171\n"
"s_nop 2\n"
"v_add_f32_e32 v140, v40, v140\n"
"v_add_f32_e32 v141, v41, v141\n"
"v_add_f32_e32 v142, v42, v142\n"
"v_add_f32_e32 v143, v43, v143\n"
"v_cvt_pk_bf16_f32 v40, v168, v169\n"
"v_cvt_pk_bf16_f32 v41, v170, v171\n"
"v_cvt_pk_bf16_f32 v42, v140, v141\n"
"v_cvt_pk_bf16_f32 v43, v142, v143\n"
"buffer_store_dwordx4 v[40:43], v0, s[4:7], s97 offen offset:1024 sc1\n"
"s_add_u32 s97, s59, 0x400000\n"
"buffer_load_dwordx4 v[80:83], v0, s[4:7], s97 offen sc1\n"
"buffer_load_dwordx4 v[84:87], v0, s[4:7], s97 offen offset:1024 sc1\n"
"buffer_load_dwordx4 v[88:91], v0, s[4:7], s97 offen offset:2048 sc1\n"
"buffer_load_dwordx4 v[92:95], v0, s[4:7], s97 offen offset:3072 sc1\n"
"buffer_load_dwordx4 v[96:99], v1, s[4:7], s97 offen sc1\n"
"buffer_load_dwordx4 v[100:103], v1, s[4:7], s97 offen offset:1024 sc1\n"
"buffer_load_dwordx4 v[104:107], v1, s[4:7], s97 offen offset:2048 sc1\n"
"buffer_load_dwordx4 v[116:119], v1, s[4:7], s97 offen offset:3072 sc1\n"
"s_waitcnt vmcnt(32)\n"
"; @B G2 a.0\n"
"v_mfma_f32_16x16x32_bf16 v[140:143], a[128:131], v[120:123], 0\n"
"v_mfma_f32_16x16x32_bf16 v[140:143], a[132:135], v[124:127], v[140:143]\n"
"s_waitcnt vmcnt(30)\n"
"ds_write_b128 v2, v[40:43] offset:9216\n"
"v_mfma_f32_16x16x32_bf16 v[140:143], a[136:139], v[108:111], v[140:143]\n"
"v_mov_b32_e32 v144, 0x3f803f80\n"
"v_mov_b32_e32 v145, 0x3f803f80\n"
"v_mfma_f32_16x16x32_bf16 v[140:143], a[140:143], v[132:135], v[140:143]\n"
"s_waitcnt vmcnt(28)\n"
"v_mov_b32_e32 v146, 0x3f803f80\n"
"v_mov_b32_e32 v147, 0x3f803f80\n"
"v_mfma_f32_16x16x32_bf16 v[140:143], a[144:147], v[112:115], v[140:143]\n"
"s_nop 0\n"
"v_mfma_f32_16x16x32_bf16 v[156:159], v[144:147], v[52:55], 0\n"
"v_mfma_f32_16x16x32_bf16 v[156:159], v[144:147], v[40:43], v[156:159]\n"
"v_mfma_f32_16x16x32_bf16 v[144:147], v[52:55], v[52:55], 0\n"
"v_mfma_f32_16x16x32_bf16 v[144:147], v[40:43], v[40:43], v[144:147]\n"
"buffer_load_dwordx4 a[56:59], v1, s[8:11], s98 offen offset:2048 sc1\n"
"v_mfma_f32_16x16x32_bf16 v[140:143], a[148:151], v[28:31], v[140:143]\n"
"s_waitcnt vmcnt(27)\n"
"v_mfma_f32_16x16x32_bf16 v[140:143], a[152:155], v[32:35], v[140:143]\n"
"v_mfma_f32_16x16x32_bf16 v[140:143], a[156:159], v[24:27], v[140:143]\n"
"buffer_load_dwordx4 a[60:63], v1, s[8:11], s98 offen offset:3072 sc1\n"
"v_mfma_f32_16x16x32_bf16 v[40:43], a[188:191], v[24:27], 0\n"
"s_mov_b32 s96, 0xaaaaaaaa\n"
"s_mov_b32 s97, 0xaaaaaaaa\n"
"s_add_u32 s98, s69, 0x4000\n"
"v_mfma_f32_16x16x32_bf16 v[40:43], a[184:187], v[32:35], v[40:43]\n"
"v_mfma_f32_16x16x32_bf16 v[40:43], a[180:183], v[28:31], v[40:43]\n"
"buffer_load_dwordx4 a[64:67], v0, s[8:11], s98 offen sc1\n"
"v_mfma_f32_16x16x32_bf16 v[40:43], a[176:179], v[112:115], v[40:43]\n"
"v_cndmask_b32_e64 v144, v144, v145, s[96:97]\n"
"v_cndmask_b32_e64 v146, v146, v147, s[96:97]\n"
"v_mfma_f32_16x16x32_bf16 v[40:43], a[172:175], v[132:135], v[40:43]\n"
"v_cmp_u_f32_e32 vcc, v140, v140\n"
"v_mfma_f32_16x16x32_bf16 v[40:43], a[168:171], v[108:111], v[40:43]\n"
"s_cbranch_vccnz .Lmk_p68s\n"
".Lmk_p68r:\n"
"v_mfma_f32_16x16x32_bf16 v[40:43], a[164:167], v[124:127], v[40:43]\n"
"ds_write_b128 v3, v[140:143]\n"
"v_mfma_f32_16x16x32_bf16 v[40:43], a[160:163], v[120:123], v[40:43]\n"
"s_mov_b32 s96, 0xcccccccc\n"
"s_mov_b32 s97, 0xcccccccc\n"
"v_mfma_f32_16x16x32_bf16 v[52:55], a[192:195], v[120:123], 0\n"
"v_cndmask_b32_e64 v157, v144, v146, s[96:97]\n"
"v_mfma_f32_16x16x32_bf16 v[52:55], a[196:199], v[124:127], v[52:55]\n"
"s_mov_b32 s96, 0xf0000f\n"
"s_mov_b32 s97, 0xf0000f00\n"
"v_mfma_f32_16x16x32_bf16 v[52:55], a[200:203], v[108:111], v[52:55]\n"
"v_cndmask_b32_e64 v157, 0, v157, s[96:97]\n"
"v_mov_b32_e32 v255, v157\n"
"v_mfma_f32_16x16x32_bf16 v[52:55], a[204:207], v[132:135], v[52:55]\n"
"s_nop 0\n"
"v_permlane16_swap_b32_e32 v157, v255\n"
"v_add_f32_e32 v157, v157, v255\n"
"v_mfma_f32_16x16x32_bf16 v[52:55], a[208:211], v[112:115], v[52:55]\n"
"v_mov_b32_e32 v255, v157\n"
"v_mfma_f32_16x16x32_bf16 v[52:55], a[212:215], v[28:31], v[52:55]\n"
"ds_write_b128 v3, v[40:43] offset:1024\n"
"v_mfma_f32_16x16x32_bf16 v[52:55], a[216:219], v[32:35], v[52:55]\n"
"s_mov_b32 s96, 0xffff0000\n"
"v_permlane32_swap_b32_e32 v157, v255\n"
"v_add_f32_e32 v157, v157, v255\n"
"v_mfma_f32_16x16x32_bf16 v[52:55], a[220:223], v[24:27], v[52:55]\n"
"v_cndmask_b32_e64 v156, v156, v157, s[96:97]\n"
"s_add_u32 s97, s63, 0x84000\n"
"v_mfma_f32_16x16x32_bf16 v[40:43], a[252:255], v[24:27], 0\n"
"buffer_store_dword v156, v14, s[4:7], s97 offen sc1\n"
"v_mfma_f32_16x16x32_bf16 v[40:43], a[248:251], v[32:35], v[40:43]\n"
"buffer_load_dwordx4 a[68:71], v0, s[8:11], s98 offen offset:1024 sc1\n"
"v_mfma_f32_16x16x32_bf16 v[40:43], a[244:247], v[28:31], v[40:43]\n"
"v_mfma_f32_16x16x32_bf16 v[40:43], a[240:243], v[112:115], v[40:43]\n"
"v_mfma_f32_16x16x32_bf16 v[40:43], a[236:239], v[132:135], v[40:43]\n"
"buffer_load_dwordx4 a[72:75], v0, s[8:11], s98 offen offset:2048 sc1\n"
"v_mfma_f32_16x16x32_bf16 v[40:43], a[232:235], v[108:111], v[40:43]\n"
"ds_write_b128 v3, v[52:55] offset:2048\n"
"v_mfma_f32_16x16x32_bf16 v[40:43], a[228:231], v[124:127], v[40:43]\n"
"v_mfma_f32_16x16x32_bf16 v[40:43], a[224:227], v[120:123], v[40:43]\n"
"s_waitcnt vmcnt(28)\n"
"buffer_load_dwordx4 a[76:79], v0, s[8:11], s98 offen offset:3072 sc1\n"
"; @B G2 a.1\n"
"v_mfma_f32_16x16x32_bf16 v[24:27], a[128:131], v[48:51], 0\n"
"v_mfma_f32_16x16x32_bf16 v[24:27], a[132:135], v[56:59], v[24:27]\n"
"v_mfma_f32_16x16x32_bf16 v[24:27], a[136:139], v[136:139], v[24:27]\n"
"buffer_load_dwordx4 a[80:83], v1, s[8:11], s98 offen sc1\n"
"v_mfma_f32_16x16x32_bf16 v[24:27], a[140:143], v[44:47], v[24:27]\n"
"s_waitcnt vmcnt(26)\n"
"v_mfma_f32_16x16x32_bf16 v[24:27], a[144:147], v[160:163], v[24:27]\n"
"v_mfma_f32_16x16x32_bf16 v[24:27], a[148:151], v[128:131], v[24:27]\n"
"ds_write_b128 v3, v[40:43] offset:3072\n"
"v_mfma_f32_16x16x32_bf16 v[24:27], a[152:155], v[72:75], v[24:27]\n"
"buffer_load_dwordx4 a[84:87], v1, s[8:11], s98 offen offset:1024 sc1\n"
"v_mfma_f32_16x16x32_bf16 v[24:27], a[156:159], v[60:63], v[24:27]\n"
"v_mfma_f32_16x16x32_bf16 v[28:31], a[188:191], v[60:63], 0\n"
"v_mfma_f32_16x16x32_bf16 v[28:31], a[184:187], v[72:75], v[28:31]\n"
"buffer_load_dwordx4 a[88:91], v1, s[8:11], s98 offen offset:2048 sc1\n"
"v_mfma_f32_16x16x32_bf16 v[28:31], a[180:183], v[128:131], v[28:31]\n"
"v_mfma_f32_16x16x32_bf16 v[28:31], a[176:179], v[160:163], v[28:31]\n"
"v_mfma_f32_16x16x32_bf16 v[28:31], a[172:175], v[44:47], v[28:31]\n"
"s_nop 1\n"
"v_cmp_u_f32_e32 vcc, v24, v24\n"
"v_mfma_f32_16x16x32_bf16 v[28:31], a[168:171], v[136:139], v[28:31]\n"
"s_cbranch_vccnz .Lmk_p69s\n"
".Lmk_p69r:\n"
"v_mfma_f32_16x16x32_bf16 v[28:31], a[164:167], v[56:59], v[28:31]\n"
"ds_write_b128 v3, v[24:27] offset:6144\n"
"v_mfma_f32_16x16x32_bf16 v[28:31], a[160:163], v[48:51], v[28:31]\n"
"buffer_load_dwordx4 a[92:95], v1, s[8:11], s98 offen offset:3072 sc1\n"
"v_mfma_f32_16x16x32_bf16 v[24:27], a[192:195], v[48:51], 0\n"
"s_add_u32 s98, s69, 0x6000\n"
"v_mfma_f32_16x16x32_bf16 v[24:27], a[196:199], v[56:59], v[24:27]\n"
"v_mfma_f32_16x16x32_bf16 v[24:27], a[200:203], v[136:139], v[24:27]\n"
"buffer_load_dwordx4 a[96:99], v0, s[8:11], s98 offen sc1\n"
"v_mfma_f32_16x16x32_bf16 v[24:27], a[204:207], v[44:47], v[24:27]\n"
"v_mfma_f32_16x16x32_bf16 v[24:27], a[208:211], v[160:163], v[24:27]\n"
"ds_read_b128 v[32:35], v2\n"
"ds_read_b128 v[40:43], v2 offset:1024\n"
"v_mfma_f32_16x16x32_bf16 v[24:27], a[212:215], v[128:131], v[24:27]\n"
"ds_write_b128 v3, v[28:31] offset:7168\n"
"v_mfma_f32_16x16x32_bf16 v[24:27], a[216:219], v[72:75], v[24:27]\n"
"buffer_load_dwordx4 a[100:103], v0, s[8:11], s98 offen offset:1024 sc1\n"
"v_mfma_f32_16x16x32_bf16 v[24:27], a[220:223], v[60:63], v[24:27]\n"
"v_mfma_f32_16x16x32_bf16 v[28:31], a[252:255], v[60:63], 0\n"
"v_mfma_f32_16x16x32_bf16 v[28:31], a[248:251], v[72:75], v[28:31]\n"
"buffer_load_dwordx4 a[104:107], v0, s[8:11], s98 offen offset:2048 sc1\n"
"s_waitcnt lgkmcnt(1)\n"
"v_mfma_f32_16x16x32_bf16 v[28:31], a[244:247], v[128:131], v[28:31]\n"
"v_lshlrev_b32_e32 v255, 16, v32\n"
"v_and_b32_e32 v254, 0xffff0000, v32\n"
"v_lshlrev_b32_e32 v253, 16, v33\n"
"v_mfma_f32_16x16x32_bf16 v[28:31], a[240:243], v[160:163], v[28:31]\n"
"v_and_b32_e32 v252, 0xffff0000, v33\n"
"v_lshlrev_b32_e32 v251, 16, v34\n"
"v_and_b32_e32 v250, 0xffff0000, v34\n"
"v_mfma_f32_16x16x32_bf16 v[28:31], a[236:239], v[44:47], v[28:31]\n"
"buffer_load_dwordx4 a[108:111], v0, s[8:11], s98 offen offset:3072 sc1\n"
"v_lshlrev_b32_e32 v249, 16, v35\n"
"v_mfma_f32_16x16x32_bf16 v[28:31], a[232:235], v[136:139], v[28:31]\n"
"ds_write_b128 v3, v[24:27] offset:8192\n"
"v_mfma_f32_16x16x32_bf16 v[28:31], a[228:231], v[56:59], v[28:31]\n"
"v_and_b32_e32 v248, 0xffff0000, v35\n"
"v_lshlrev_b32_e32 v247, 16, v40\n"
"v_and_b32_e32 v246, 0xffff0000, v40\n"
"v_mfma_f32_16x16x32_bf16 v[28:31], a[224:227], v[48:51], v[28:31]\n"
"s_waitcnt vmcnt(29)\n"
"buffer_load_dwordx4 a[112:115], v1, s[8:11], s98 offen sc1\n"
"; @B G2 a.2\n"
"v_mfma_f32_16x16x32_bf16 v[24:27], a[128:131], v[36:39], 0\n"
"v_lshlrev_b32_e32 v245, 16, v41\n"
"v_and_b32_e32 v244, 0xffff0000, v41\n"
"v_lshlrev_b32_e32 v243, 16, v42\n"
"v_mfma_f32_16x16x32_bf16 v[24:27], a[132:135], v[68:71], v[24:27]\n"
"v_and_b32_e32 v242, 0xffff0000, v42\n"
"v_lshlrev_b32_e32 v241, 16, v43\n"
"v_and_b32_e32 v240, 0xffff0000, v43\n"
"v_mfma_f32_16x16x32_bf16 v[24:27], a[136:139], v[76:79], v[24:27]\n"
"buffer_load_dwordx4 a[116:119], v1, s[8:11], s98 offen offset:1024 sc1\n"
"v_mfma_f32_16x16x32_bf16 v[24:27], a[140:143], v[148:151], v[24:27]\n"
"s_waitcnt vmcnt(27)\n"
"v_mfma_f32_16x16x32_bf16 v[24:27], a[144:147], v[152:155], v[24:27]\n"
"v_mfma_f32_16x16x32_bf16 v[24:27], a[148:151], v[172:175], v[24:27]\n"
"ds_write_b128 v3, v[28:31] offset:9216\n"
"v_mfma_f32_16x16x32_bf16 v[24:27], a[152:155], v[176:179], v[24:27]\n"
"buffer_load_dwordx4 a[120:123], v1, s[8:11], s98 offen offset:2048 sc1\n"
"v_mfma_f32_16x16x32_bf16 v[24:27], a[156:159], v[64:67], v[24:27]\n"
"v_mfma_f32_16x16x32_bf16 v[28:31], a[188:191], v[64:67], 0\n"
"v_mfma_f32_16x16x32_bf16 v[28:31], a[184:187], v[176:179], v[28:31]\n"
"s_waitcnt lgkmcnt(0)\n"
"s_barrier\n"
"v_mfma_f32_16x16x32_bf16 v[28:31], a[180:183], v[172:175], v[28:31]\n"
"buffer_load_dwordx4 a[124:127], v1, s[8:11], s98 offen offset:3072 sc1\n"
"v_mfma_f32_16x16x32_bf16 v[28:31], a[176:179], v[152:155], v[28:31]\n"
"s_add_u32 s98, s69, 0x8000\n"
"ds_read_b128 v[32:35], v4\n"
"ds_read_b128 v[40:43], v5\n"
"v_mfma_f32_16x16x32_bf16 v[28:31], a[172:175], v[148:151], v[28:31]\n"
"v_cmp_u_f32_e32 vcc, v24, v24\n"
"v_mfma_f32_16x16x32_bf16 v[28:31], a[168:171], v[76:79], v[28:31]\n"
"s_cbranch_vccnz .Lmk_p70s\n"
".Lmk_p70r:\n"
"v_mfma_f32_16x16x32_bf16 v[28:31], a[164:167], v[68:71], v[28:31]\n"
"ds_write_b128 v3, v[24:27] offset:12288\n"
"v_mfma_f32_16x16x32_bf16 v[28:31], a[160:163], v[36:39], v[28:31]\n"
"buffer_load_dwordx4 v[24:27], v0, s[8:11], s98 offen sc1\n"
"v_mfma_f32_16x16x32_bf16 v[44:47], a[192:195], v[36:39], 0\n"
"ds_read_b128 v[48:51], v4 offset:1024\n"
"ds_read_b128 v[52:55], v5 offset:1024\n"
"s_waitcnt lgkmcnt(3)\n"
"v_mfma_f32_16x16x32_bf16 v[44:47], a[196:199], v[68:71], v[44:47]\n"
"v_add_f32_e32 v32, v32, v255\n"
"v_add_f32_e32 v33, v33, v254\n"
"v_add_f32_e32 v34, v34, v253\n"
"v_mfma_f32_16x16x32_bf16 v[44:47], a[200:203], v[76:79], v[44:47]\n"
"buffer_load_dwordx4 v[56:59], v0, s[8:11], s98 offen offset:1024 sc1\n"
"v_add_f32_e32 v35, v35, v252\n"
"v_mfma_f32_16x16x32_bf16 v[44:47], a[204:207], v[148:151], v[44:47]\n"
"v_add_f32_e32 v32, v32, v40\n"
"v_add_f32_e32 v33, v33, v41\n"
"v_add_f32_e32 v34, v34, v42\n"
"v_mfma_f32_16x16x32_bf16 v[44:47], a[208:211], v[152:155], v[44:47]\n"
"v_add_f32_e32 v35, v35, v43\n"
"v_mfma_f32_16x16x32_bf16 v[44:47], a[212:215], v[172:175], v[44:47]\n"
"ds_write_b128 v3, v[28:31] offset:13312\n"
"v_mfma_f32_16x16x32_bf16 v[44:47], a[216:219], v[176:179], v[44:47]\n"
"buffer_load_dwordx4 v[28:31], v0, s[8:11], s98 offen offset:2048 sc1\n"
"s_waitcnt lgkmcnt(1)\n"
"v_mfma_f32_16x16x32_bf16 v[44:47], a[220:223], v[64:67], v[44:47]\n"
"ds_read_b128 v[40:43], v4 offset:2048\n"
"ds_read_b128 v[60:63], v5 offset:2048\n"
"v_add_f32_e32 v48, v48, v251\n"
"v_mfma_f32_16x16x32_bf16 v[72:75], a[252:255], v[64:67], 0\n"
"v_add_f32_e32 v49, v49, v250\n"
"v_add_f32_e32 v50, v50, v249\n"
"v_add_f32_e32 v51, v51, v248\n"
"v_mfma_f32_16x16x32_bf16 v[72:75], a[248:251], v[176:179], v[72:75]\n"
"buffer_load_dwordx4 v[64:67], v0, s[8:11], s98 offen offset:3072 sc1\n"
"v_add_f32_e32 v48, v48, v52\n"
"v_mfma_f32_16x16x32_bf16 v[72:75], a[244:247], v[172:175], v[72:75]\n"
"v_add_f32_e32 v49, v49, v53\n"
"v_add_f32_e32 v50, v50, v54\n"
"v_add_f32_e32 v51, v51, v55\n"
"v_mfma_f32_16x16x32_bf16 v[72:75], a[240:243], v[152:155], v[72:75]\n"
"v_mfma_f32_16x16x32_bf16 v[72:75], a[236:239], v[148:151], v[72:75]\n"
"buffer_load_dwordx4 v[52:55], v1, s[8:11], s98 offen sc1\n"
"s_waitcnt lgkmcnt(0)\n"
"v_mfma_f32_16x16x32_bf16 v[72:75], a[232:235], v[76:79], v[72:75]\n"
"ds_write_b128 v3, v[44:47] offset:14336\n"
"v_mfma_f32_16x16x32_bf16 v[72:75], a[228:231], v[68:71], v[72:75]\n"
"ds_read_b128 v[44:47], v4 offset:3072\n"
"ds_read_b128 v[68:71], v5 offset:3072\n"
"v_add_f32_e32 v40, v40, v247\n"
"v_mfma_f32_16x16x32_bf16 v[72:75], a[224:227], v[36:39], v[72:75]\n"
"s_waitcnt vmcnt(28)\n"
"buffer_load_dwordx4 v[36:39], v1, s[8:11], s98 offen offset:1024 sc1\n"
"; @B G2 a.3\n"
"v_mfma_f32_16x16x32_bf16 v[76:79], a[128:131], v[80:83], v[32:35]\n"
"v_add_f32_e32 v41, v41, v246\n"
"v_add_f32_e32 v42, v42, v245\n"
"v_add_f32_e32 v43, v43, v244\n"
"v_mfma_f32_16x16x32_bf16 v[76:79], a[132:135], v[84:87], v[76:79]\n"
"v_add_f32_e32 v40, v40, v60\n"
"v_add_f32_e32 v41, v41, v61\n"
"v_add_f32_e32 v42, v42, v62\n"
"v_mfma_f32_16x16x32_bf16 v[76:79], a[136:139], v[88:91], v[76:79]\n"
"buffer_load_dwordx4 v[108:111], v1, s[8:11], s98 offen offset:2048 sc1\n"
"v_add_f32_e32 v43, v43, v63\n"
"v_mfma_f32_16x16x32_bf16 v[76:79], a[140:143], v[92:95], v[76:79]\n"
"s_waitcnt vmcnt(26)\n"
"s_waitcnt lgkmcnt(0)\n"
"v_add_f32_e32 v44, v44, v243\n"
"v_add_f32_e32 v45, v45, v242\n"
"v_mfma_f32_16x16x32_bf16 v[76:79], a[144:147], v[96:99], v[76:79]\n"
"v_add_f32_e32 v46, v46, v241\n"
"v_add_f32_e32 v47, v47, v240\n"
"v_add_f32_e32 v44, v44, v68\n"
"v_mfma_f32_16x16x32_bf16 v[76:79], a[148:151], v[100:103], v[76:79]\n"
"ds_write_b128 v3, v[72:75] offset:15360\n"
"v_mfma_f32_16x16x32_bf16 v[76:79], a[152:155], v[104:107], v[76:79]\n"
"buffer_load_dwordx4 v[60:63], v1, s[8:11], s98 offen offset:3072 sc1\n"
"v_add_f32_e32 v45, v45, v69\n"
"v_mfma_f32_16x16x32_bf16 v[76:79], a[156:159], v[116:119], v[76:79]\n"
"s_add_u32 s98, s69, 0xa000\n"
"v_add_f32_e32 v46, v46, v70\n"
"v_add_f32_e32 v47, v47, v71\n"
"v_mfma_f32_16x16x32_bf16 v[68:71], a[188:191], v[116:119], v[48:51]\n"
"v_mfma_f32_16x16x32_bf16 v[68:71], a[184:187], v[104:107], v[68:71]\n"
"s_waitcnt lgkmcnt(0)\n"
"s_barrier\n"
"v_mfma_f32_16x16x32_bf16 v[68:71], a[180:183], v[100:103], v[68:71]\n"
"buffer_load_dwordx4 v[72:75], v0, s[8:11], s98 offen sc1\n"
"v_mfma_f32_16x16x32_bf16 v[68:71], a[176:179], v[96:99], v[68:71]\n"
"ds_read_b128 v[112:115], v6\n"
"ds_read_b128 v[120:123], v6 offset:1024\n"
"v_mfma_f32_16x16x32_bf16 v[68:71], a[172:175], v[92:95], v[68:71]\n"
"ds_read_b128 v[124:127], v6 offset:2048\n"
"v_cmp_u_f32_e32 vcc, v76, v76\n"
"v_mfma_f32_16x16x32_bf16 v[68:71], a[168:171], v[88:91], v[68:71]\n"
"s_cbranch_vccnz .Lmk_p71s\n"
".Lmk_p71r:\n"
"ds_read_b128 v[32:35], v6 offset:3072\n"
"v_mfma_f32_16x16x32_bf16 v[68:71], a[164:167], v[84:87], v[68:71]\n"
"buffer_load_dwordx4 v[48:51], v0, s[8:11], s98 offen offset:1024 sc1\n"
"v_mfma_f32_16x16x32_bf16 v[68:71], a[160:163], v[80:83], v[68:71]\n"
"v_mfma_f32_16x16x32_bf16 v[40:43], a[192:195], v[80:83], v[40:43]\n"
"v_mfma_f32_16x16x32_bf16 v[40:43], a[196:199], v[84:87], v[40:43]\n"
"buffer_load_dwordx4 v[128:131], v0, s[8:11], s98 offen offset:2048 sc1\n"
"v_mfma_f32_16x16x32_bf16 v[40:43], a[200:203], v[88:91], v[40:43]\n"
"v_mfma_f32_16x16x32_bf16 v[40:43], a[204:207], v[92:95], v[40:43]\n"
"v_mfma_f32_16x16x32_bf16 v[40:43], a[208:211], v[96:99], v[40:43]\n"
"buffer_load_dwordx4 v[132:135], v0, s[8:11], s98 offen offset:3072 sc1\n"
"s_waitcnt lgkmcnt(0)\n"
"v_mfma_f32_16x16x32_bf16 v[40:43], a[212:215], v[100:103], v[40:43]\n"
"v_add_f32_e32 v112, v76, v112\n"
"v_add_f32_e32 v113, v77, v113\n"
"v_add_f32_e32 v114, v78, v114\n"
"v_mfma_f32_16x16x32_bf16 v[40:43], a[216:219], v[104:107], v[40:43]\n"
"v_add_f32_e32 v115, v79, v115\n"
"v_add_f32_e32 v120, v68, v120\n"
"v_add_f32_e32 v121, v69, v121\n"
"v_mfma_f32_16x16x32_bf16 v[40:43], a[220:223], v[116:119], v[40:43]\n"
"buffer_load_dwordx4 v[76:79], v1, s[8:11], s98 offen sc1\n"
"v_add_f32_e32 v122, v70, v122\n"
"v_mfma_f32_16x16x32_bf16 v[44:47], a[252:255], v[116:119], v[44:47]\n"
"v_add_f32_e32 v123, v71, v123\n"
"v_cvt_pk_bf16_f32 v68, v112, v113\n"
"v_cvt_pk_bf16_f32 v69, v114, v115\n"
"v_mfma_f32_16x16x32_bf16 v[44:47], a[248:251], v[104:107], v[44:47]\n"
"s_add_u32 s97, s61, 0x800000\n"
"v_cvt_pk_bf16_f32 v70, v120, v121\n"
"v_cvt_pk_bf16_f32 v71, v122, v123\n"
"v_mfma_f32_16x16x32_bf16 v[44:47], a[244:247], v[100:103], v[44:47]\n"
"buffer_load_dwordx4 v[100:103], v1, s[8:11], s98 offen offset:1024 sc1\n"
"v_mfma_f32_16x16x32_bf16 v[44:47], a[240:243], v[96:99], v[44:47]\n"
"buffer_store_dwordx4 v[68:71], v0, s[4:7], s97 offen sc1\n"
"v_mfma_f32_16x16x32_bf16 v[44:47], a[236:239], v[92:95], v[44:47]\n"
"ds_write_b128 v2, v[68:71]\n"
"v_mfma_f32_16x16x32_bf16 v[44:47], a[232:235], v[88:91], v[44:47]\n"
"buffer_load_dwordx4 v[88:91], v1, s[8:11], s98 offen offset:2048 sc1\n"
"v_mfma_f32_16x16x32_bf16 v[44:47], a[228:231], v[84:87], v[44:47]\n"
"v_mfma_f32_16x16x32_bf16 v[44:47], a[224:227], v[80:83], v[44:47]\n"
"buffer_load_dwordx4 v[80:83], v1, s[8:11], s98 offen offset:3072 sc1\n"
"s_add_u32 s98, s52, 0x840000\n"
"buffer_load_dwordx4 v[84:87], v0, s[8:11], s98 offen sc1\n"
"buffer_load_dwordx4 v[92:95], v0, s[8:11], s98 offen offset:1024 sc1\n"
"buffer_load_dwordx4 v[96:99], v0, s[8:11], s98 offen offset:2048 sc1\n"
"buffer_load_dwordx4 v[104:107], v0, s[8:11], s98 offen offset:3072 sc1\n"
"buffer_load_dwordx4 v[112:115], v1, s[8:11], s98 offen sc1\n"
"buffer_load_dwordx4 v[116:119], v1, s[8:11], s98 offen offset:1024 sc1\n"
"buffer_load_dwordx4 v[120:123], v1, s[8:11], s98 offen offset:2048 sc1\n"
"buffer_load_dwordx4 v[136:139], v1, s[8:11], s98 offen offset:3072 sc1\n"
"s_add_u32 s98, s53, 0x840000\n"
"buffer_load_dwordx4 v[140:143], v0, s[8:11], s98 offen sc1\n"
"buffer_load_dwordx4 v[144:147], v0, s[8:11], s98 offen offset:1024 sc1\n"
"buffer_load_dwordx4 v[148:151], v0, s[8:11], s98 offen offset:2048 sc1\n"
"buffer_load_dwordx4 v[152:155], v0, s[8:11], s98 offen offset:3072 sc1\n"
"buffer_load_dwordx4 v[156:159], v1, s[8:11], s98 offen sc1\n"
"buffer_load_dwordx4 v[160:163], v1, s[8:11], s98 offen offset:1024 sc1\n"
"buffer_load_dwordx4 v[164:167], v1, s[8:11], s98 offen offset:2048 sc1\n"
"buffer_load_dwordx4 v[168:171], v1, s[8:11], s98 offen offset:3072 sc1\n"
"s_lshr_b32 s98, s79, 7\n"
"s_add_u32 s52, s52, 0x800000\n"
"s_add_u32 s53, s53, 0x800000\n"
"s_add_u32 s54, s54, 0x800000\n"
"s_add_u32 s55, s55, 0x800000\n"
"s_add_u32 s56, s56, 0x800000\n"
"s_add_u32 s57, s57, 0x800000\n"
"s_add_u32 s58, s58, 0x800000\n"
"s_add_u32 s59, s59, 0x800000\n"
"s_add_u32 s60, s60, 0x800000\n"
"s_add_u32 s61, s61, 0x800000\n"
"s_add_u32 s62, s62, 0x80000\n"
"s_add_u32 s63, s63, 0x80000\n"
"s_add_u32 s64, s64, 0x1000000\n"
"s_add_u32 s65, s65, s79\n"
"s_add_u32 s66, s66, s79\n"
"s_add_u32 s67, s67, 0x20000\n"
"s_add_u32 s68, s68, 0x20000\n"
"s_add_u32 s71, s71, s98\n"
"s_add_u32 s73, s73, s98\n"
"s_mov_b32 s0, 1\n"
"s_mov_b32 s10, 0\n"
".Lmk_a0:\n"
"s_waitcnt vmcnt(12)\n"
"; @A G1 0.0\n"
"v_mfma_f32_16x16x32_bf16 v[172:175], a[0:3], v[84:87], 0\n"
"v_mfma_f32_16x16x32_bf16 v[172:175], a[4:7], v[92:95], v[172:175]\n"
"s_add_u32 s98, s62, 0x4000\n"
"v_mfma_f32_16x16x32_bf16 v[172:175], a[8:11], v[96:99], v[172:175]\n"
"s_add_u32 s97, s54, 0x40000\n"
"buffer_load_dwordx4 v[176:179], v0, s[4:7], s97 offen sc1\n"
"v_mfma_f32_16x16x32_bf16 v[172:175], a[12:15], v[104:107], v[172:175]\n"
"s_waitcnt vmcnt(9)\n"
"v_mfma_f32_16x16x32_bf16 v[172:175], a[16:19], v[112:115], v[172:175]\n"
"buffer_load_dword v255, v10, s[4:7], s98 offen sc1\n"
"v_mfma_f32_16x16x32_bf16 v[172:175], a[20:23], v[116:119], v[172:175]\n"
"v_add_f32_e32 v124, v40, v124\n"
"v_add_f32_e32 v125, v41, v125\n"
"v_mfma_f32_16x16x32_bf16 v[172:175], a[24:27], v[120:123], v[172:175]\n"
"v_add_f32_e32 v126, v42, v126\n"
"v_add_f32_e32 v127, v43, v127\n"
"v_mfma_f32_16x16x32_bf16 v[172:175], a[28:31], v[136:139], v[172:175]\n"
"buffer_load_dwordx4 v[40:43], v0, s[4:7], s97 offen offset:1024 sc1\n"
"v_mfma_f32_16x16x32_bf16 v[180:183], a[60:63], v[136:139], 0\n"
"buffer_load_dword v254, v10, s[4:7], s98 offen offset:256 sc1\n"
"v_mfma_f32_16x16x32_bf16 v[180:183], a[56:59], v[120:123], v[180:183]\n"
"v_add_f32_e32 v32, v44, v32\n"
"v_add_f32_e32 v33, v45, v33\n"
"v_mfma_f32_16x16x32_bf16 v[180:183], a[52:55], v[116:119], v[180:183]\n"
"v_add_f32_e32 v34, v46, v34\n"
"v_add_f32_e32 v35, v47, v35\n"
"v_mfma_f32_16x16x32_bf16 v[180:183], a[48:51], v[112:115], v[180:183]\n"
"buffer_load_dword v253, v10, s[4:7], s98 offen offset:512 sc1\n"
"v_mfma_f32_16x16x32_bf16 v[180:183], a[44:47], v[104:107], v[180:183]\n"
"v_cmp_u_f32_e32 vcc, v172, v172\n"
"v_cvt_pk_bf16_f32 v44, v124, v125\n"
"v_mfma_f32_16x16x32_bf16 v[180:183], a[40:43], v[96:99], v[180:183]\n"
"s_cbranch_vccnz .Lmk_p76s\n"
".Lmk_p76r:\n"
"v_cvt_pk_bf16_f32 v45, v126, v127\n"
"v_mfma_f32_16x16x32_bf16 v[180:183], a[36:39], v[92:95], v[180:183]\n"
"buffer_load_dwordx4 v[124:127], v0, s[4:7], s97 offen offset:2048 sc1\n"
"v_mfma_f32_16x16x32_bf16 v[180:183], a[32:35], v[84:87], v[180:183]\n"
"ds_write_b128 v3, v[172:175]\n"
"v_mfma_f32_16x16x32_bf16 v[172:175], a[64:67], v[84:87], 0\n"
"buffer_load_dword v252, v10, s[4:7], s98 offen offset:768 sc1\n"
"v_mfma_f32_16x16x32_bf16 v[172:175], a[68:71], v[92:95], v[172:175]\n"
"buffer_load_dwordx4 v[184:187], v0, s[4:7], s97 offen offset:3072 sc1\n"
"v_mfma_f32_16x16x32_bf16 v[172:175], a[72:75], v[96:99], v[172:175]\n"
"v_cvt_pk_bf16_f32 v46, v32, v33\n"
"v_cvt_pk_bf16_f32 v47, v34, v35\n"
"v_mfma_f32_16x16x32_bf16 v[172:175], a[76:79], v[104:107], v[172:175]\n"
"buffer_load_dword v251, v10, s[4:7], s98 offen offset:1024 sc1\n"
"v_mfma_f32_16x16x32_bf16 v[172:175], a[80:83], v[112:115], v[172:175]\n"
"buffer_store_dwordx4 v[44:47], v0, s[4:7], s61 offen offset:1024 sc1\n"
"v_mfma_f32_16x16x32_bf16 v[172:175], a[84:87], v[116:119], v[172:175]\n"
"ds_write_b128 v3, v[180:183] offset:1024\n"
"v_mfma_f32_16x16x32_bf16 v[172:175], a[88:91], v[120:123], v[172:175]\n"
"buffer_load_dwordx4 v[32:35], v1, s[4:7], s97 offen sc1\n"
"v_mfma_f32_16x16x32_bf16 v[172:175], a[92:95], v[136:139], v[172:175]\n"
"buffer_load_dword v250, v10, s[4:7], s98 offen offset:1280 sc1\n"
"v_mfma_f32_16x16x32_bf16 v[180:183], a[124:127], v[136:139], 0\n"
"ds_write_b128 v2, v[44:47] offset:1024\n"
"v_mfma_f32_16x16x32_bf16 v[180:183], a[120:123], v[120:123], v[180:183]\n"
"v_mov_b32_e32 v188, 0x3f803f80\n"
"v_mov_b32_e32 v189, 0x3f803f80\n"
"v_mfma_f32_16x16x32_bf16 v[180:183], a[116:119], v[116:119], v[180:183]\n"
"buffer_load_dword v249, v10, s[4:7], s98 offen offset:1536 sc1\n"
"v_mfma_f32_16x16x32_bf16 v[180:183], a[112:115], v[112:115], v[180:183]\n"
"buffer_load_dwordx4 v[192:195], v1, s[4:7], s97 offen offset:1024 sc1\n"
"v_mfma_f32_16x16x32_bf16 v[180:183], a[108:111], v[104:107], v[180:183]\n"
"v_mov_b32_e32 v190, 0x3f803f80\n"
"v_mov_b32_e32 v191, 0x3f803f80\n"
"v_mfma_f32_16x16x32_bf16 v[180:183], a[104:107], v[96:99], v[180:183]\n"
"ds_write_b128 v3, v[172:175] offset:2048\n"
"v_mfma_f32_16x16x32_bf16 v[172:175], v[188:191], v[68:71], 0\n"
"v_mfma_f32_16x16x32_bf16 v[172:175], v[188:191], v[44:47], v[172:175]\n"
"v_mfma_f32_16x16x32_bf16 v[188:191], v[68:71], v[68:71], 0\n"
"v_mfma_f32_16x16x32_bf16 v[188:191], v[44:47], v[44:47], v[188:191]\n"
"v_mfma_f32_16x16x32_bf16 v[180:183], a[100:103], v[92:95], v[180:183]\n"
"v_mov_b32_e32 v44, -1\n"
"v_mov_b32_e32 v45, -1\n"
"v_mfma_f32_16x16x32_bf16 v[180:183], a[96:99], v[84:87], v[180:183]\n"
"v_mov_b32_e32 v46, -1\n"
"v_mov_b32_e32 v47, -1\n"
"v_mfma_f32_16x16x32_bf16 v[68:71], v[24:27], v[84:87], 0\n"
"buffer_load_dwordx4 v[196:199], v1, s[4:7], s97 offen offset:2048 sc1\n"
"v_mfma_f32_16x16x32_bf16 v[68:71], v[56:59], v[92:95], v[68:71]\n"
"s_add_u32 s96, s64, 0x4000\n"
"buffer_store_dwordx4 v[44:47], v2, s[4:7], s96 offen sc1\n"
"v_mfma_f32_16x16x32_bf16 v[68:71], v[28:31], v[96:99], v[68:71]\n"
"buffer_load_dword v248, v10, s[4:7], s98 offen offset:1792 sc1\n"
"v_mfma_f32_16x16x32_bf16 v[68:71], v[64:67], v[104:107], v[68:71]\n"
"s_mov_b32 s94, 0xaaaaaaaa\n"
"s_mov_b32 s95, 0xaaaaaaaa\n"
"v_mfma_f32_16x16x32_bf16 v[68:71], v[52:55], v[112:115], v[68:71]\n"
"v_mfma_f32_16x16x32_bf16 v[68:71], v[36:39], v[116:119], v[68:71]\n"
"buffer_load_dwordx4 v[200:203], v1, s[4:7], s97 offen offset:3072 sc1\n"
"v_mfma_f32_16x16x32_bf16 v[68:71], v[108:111], v[120:123], v[68:71]\n"
"ds_write_b128 v3, v[180:183] offset:3072\n"
"v_mfma_f32_16x16x32_bf16 v[68:71], v[60:63], v[136:139], v[68:71]\n"
"v_cndmask_b32_e64 v188, v188, v189, s[94:95]\n"
"v_cndmask_b32_e64 v190, v190, v191, s[94:95]\n"
"v_mfma_f32_16x16x32_bf16 v[180:183], v[80:83], v[136:139], 0\n"
"s_mov_b32 s94, 0xcccccccc\n"
"s_mov_b32 s95, 0xcccccccc\n"
"v_mfma_f32_16x16x32_bf16 v[180:183], v[88:91], v[120:123], v[180:183]\n"
"v_cndmask_b32_e64 v173, v188, v190, s[94:95]\n"
"v_mfma_f32_16x16x32_bf16 v[180:183], v[100:103], v[116:119], v[180:183]\n"
"s_mov_b32 s94, 0xf0000f\n"
"s_mov_b32 s95, 0xf0000f00\n"
"v_mfma_f32_16x16x32_bf16 v[180:183], v[76:79], v[112:115], v[180:183]\n"
"v_cndmask_b32_e64 v173, 0, v173, s[94:95]\n"
"v_mov_b32_e32 v247, v173\n"
"v_mfma_f32_16x16x32_bf16 v[180:183], v[132:135], v[104:107], v[180:183]\n"
"s_nop 0\n"
"v_permlane16_swap_b32_e32 v173, v247\n"
"v_add_f32_e32 v173, v173, v247\n"
"v_mfma_f32_16x16x32_bf16 v[180:183], v[128:131], v[96:99], v[180:183]\n"
"ds_write_b128 v3, v[68:71] offset:4096\n"
"v_mfma_f32_16x16x32_bf16 v[180:183], v[48:51], v[92:95], v[180:183]\n"
"v_mov_b32_e32 v247, v173\n"
"v_mfma_f32_16x16x32_bf16 v[180:183], v[72:75], v[84:87], v[180:183]\n"
"s_waitcnt vmcnt(22)\n"
"v_permlane32_swap_b32_e32 v173, v247\n"
"v_add_f32_e32 v173, v173, v247\n"
"; @A G1 0.1\n"
"v_mfma_f32_16x16x32_bf16 v[68:71], a[0:3], v[140:143], 0\n"
"s_mov_b32 s94, 0xffff0000\n"
"v_cndmask_b32_e64 v172, v172, v173, s[94:95]\n"
"v_mfma_f32_16x16x32_bf16 v[68:71], a[4:7], v[144:147], v[68:71]\n"
"buffer_store_dword v172, v14, s[4:7], s63 offen sc1\n"
"v_mfma_f32_16x16x32_bf16 v[68:71], a[8:11], v[148:151], v[68:71]\n"
"s_add_u32 s98, s55, 0x40000\n"
"buffer_load_dwordx4 v[84:87], v0, s[4:7], s98 offen sc1\n"
"v_mfma_f32_16x16x32_bf16 v[68:71], a[12:15], v[152:155], v[68:71]\n"
"s_waitcnt vmcnt(20)\n"
"v_mfma_f32_16x16x32_bf16 v[68:71], a[16:19], v[156:159], v[68:71]\n"
"v_mfma_f32_16x16x32_bf16 v[68:71], a[20:23], v[160:163], v[68:71]\n"
"ds_write_b128 v3, v[180:183] offset:5120\n"
"v_mfma_f32_16x16x32_bf16 v[68:71], a[24:27], v[164:167], v[68:71]\n"
"v_mfma_f32_16x16x32_bf16 v[68:71], a[28:31], v[168:171], v[68:71]\n"
"buffer_load_dwordx4 v[92:95], v0, s[4:7], s98 offen offset:1024 sc1\n"
"v_mfma_f32_16x16x32_bf16 v[96:99], a[60:63], v[168:171], 0\n"
"v_mfma_f32_16x16x32_bf16 v[96:99], a[56:59], v[164:167], v[96:99]\n"
"v_mfma_f32_16x16x32_bf16 v[96:99], a[52:55], v[160:163], v[96:99]\n"
"v_mfma_f32_16x16x32_bf16 v[96:99], a[48:51], v[156:159], v[96:99]\n"
"v_mfma_f32_16x16x32_bf16 v[96:99], a[44:47], v[152:155], v[96:99]\n"
"s_nop 1\n"
"v_cmp_u_f32_e32 vcc, v68, v68\n"
"v_mfma_f32_16x16x32_bf16 v[96:99], a[40:43], v[148:151], v[96:99]\n"
"s_cbranch_vccnz .Lmk_p77s\n"
".Lmk_p77r:\n"
"v_mfma_f32_16x16x32_bf16 v[96:99], a[36:39], v[144:147], v[96:99]\n"
"buffer_load_dwordx4 v[104:107], v0, s[4:7], s98 offen offset:2048 sc1\n"
"v_mfma_f32_16x16x32_bf16 v[96:99], a[32:35], v[140:143], v[96:99]\n"
"ds_write_b128 v3, v[68:71] offset:6144\n"
"v_mfma_f32_16x16x32_bf16 v[68:71], a[64:67], v[140:143], 0\n"
"v_mfma_f32_16x16x32_bf16 v[68:71], a[68:71], v[144:147], v[68:71]\n"
"buffer_load_dwordx4 v[112:115], v0, s[4:7], s98 offen offset:3072 sc1\n"
"v_mfma_f32_16x16x32_bf16 v[68:71], a[72:75], v[148:151], v[68:71]\n"
"v_mfma_f32_16x16x32_bf16 v[68:71], a[76:79], v[152:155], v[68:71]\n"
"v_mfma_f32_16x16x32_bf16 v[68:71], a[80:83], v[156:159], v[68:71]\n"
"v_mfma_f32_16x16x32_bf16 v[68:71], a[84:87], v[160:163], v[68:71]\n"
"ds_write_b128 v3, v[96:99] offset:7168\n"
"v_mfma_f32_16x16x32_bf16 v[68:71], a[88:91], v[164:167], v[68:71]\n"
"buffer_load_dwordx4 v[96:99], v1, s[4:7], s98 offen sc1\n"
"v_mfma_f32_16x16x32_bf16 v[68:71], a[92:95], v[168:171], v[68:71]\n"
"v_mfma_f32_16x16x32_bf16 v[116:119], a[124:127], v[168:171], 0\n"
"v_mfma_f32_16x16x32_bf16 v[116:119], a[120:123], v[164:167], v[116:119]\n"
"v_mfma_f32_16x16x32_bf16 v[116:119], a[116:119], v[160:163], v[116:119]\n"
"v_mfma_f32_16x16x32_bf16 v[116:119], a[112:115], v[156:159], v[116:119]\n"
"buffer_load_dwordx4 v[120:123], v1, s[4:7], s98 offen offset:1024 sc1\n"
"v_mfma_f32_16x16x32_bf16 v[116:119], a[108:111], v[152:155], v[116:119]\n"
"v_mfma_f32_16x16x32_bf16 v[116:119], a[104:107], v[148:151], v[116:119]\n"
"s_nop 0\n"
"ds_write_b128 v3, v[68:71] offset:8192\n"
"v_mfma_f32_16x16x32_bf16 v[116:119], a[100:103], v[144:147], v[116:119]\n"
"v_mfma_f32_16x16x32_bf16 v[116:119], a[96:99], v[140:143], v[116:119]\n"
"v_mfma_f32_16x16x32_bf16 v[68:71], v[24:27], v[140:143], 0\n"
"buffer_load_dwordx4 v[136:139], v1, s[4:7], s98 offen offset:2048 sc1\n"
"v_mfma_f32_16x16x32_bf16 v[68:71], v[56:59], v[144:147], v[68:71]\n"
"s_add_u32 s97, s64, 0x4000\n"
"v_mfma_f32_16x16x32_bf16 v[68:71], v[28:31], v[148:151], v[68:71]\n"
"v_mfma_f32_16x16x32_bf16 v[68:71], v[64:67], v[152:155], v[68:71]\n"
"buffer_store_dwordx4 v[44:47], v2, s[4:7], s97 offen offset:1024 sc1\n"
"v_mfma_f32_16x16x32_bf16 v[68:71], v[52:55], v[156:159], v[68:71]\n"
"s_waitcnt vmcnt(10)\n"
"v_add_f32_e32 v255, v255, v251\n"
"v_add_f32_e32 v254, v254, v250\n"
"v_mfma_f32_16x16x32_bf16 v[68:71], v[36:39], v[160:163], v[68:71]\n"
"buffer_load_dwordx4 v[172:175], v1, s[4:7], s98 offen offset:3072 sc1\n"
"v_mfma_f32_16x16x32_bf16 v[68:71], v[108:111], v[164:167], v[68:71]\n"
"ds_write_b128 v3, v[116:119] offset:9216\n"
"v_mfma_f32_16x16x32_bf16 v[68:71], v[60:63], v[168:171], v[68:71]\n"
"v_add_f32_e32 v253, v253, v249\n"
"v_add_f32_e32 v252, v252, v248\n"
"v_mfma_f32_16x16x32_bf16 v[116:119], v[80:83], v[168:171], 0\n"
"v_add_f32_e32 v255, v255, v253\n"
"v_add_f32_e32 v254, v254, v252\n"
"v_mfma_f32_16x16x32_bf16 v[116:119], v[88:91], v[164:167], v[116:119]\n"
"v_add_f32_e32 v255, v255, v254\n"
"v_cmp_u_f32_e32 vcc, v255, v255\n"
"v_mfma_f32_16x16x32_bf16 v[116:119], v[100:103], v[160:163], v[116:119]\n"
"s_cbranch_vccnz .Lmk_m80s\n"
".Lmk_m80r:\n"
"v_mov_b32_e32 v254, v255\n"
"v_mfma_f32_16x16x32_bf16 v[116:119], v[76:79], v[156:159], v[116:119]\n"
"s_nop 0\n"
"v_permlane32_swap_b32_e32 v255, v254\n"
"v_add_f32_e32 v255, v255, v254\n"
"v_mfma_f32_16x16x32_bf16 v[116:119], v[132:135], v[152:155], v[116:119]\n"
"v_mul_f32_e32 v255, 0x3a800000, v255\n"
"v_mov_b32_e32 v254, v255\n"
"v_mfma_f32_16x16x32_bf16 v[116:119], v[128:131], v[148:151], v[116:119]\n"
"ds_write_b128 v3, v[68:71] offset:10240\n"
"v_mfma_f32_16x16x32_bf16 v[116:119], v[48:51], v[144:147], v[116:119]\n"
"v_permlane16_swap_b32_e32 v255, v254\n"
"v_cvt_pk_bf16_f32 v253, -v255, 0\n"
"v_mfma_f32_16x16x32_bf16 v[116:119], v[72:75], v[140:143], v[116:119]\n"
"v_fma_f32 v254, -v255, v255, v254\n"
"v_lshlrev_b32_e32 v253, 16, v253\n"
"; @A G1 0.2\n"
"v_mfma_f32_16x16x32_bf16 v[68:71], a[0:3], v[176:179], 0\n"
"v_max_f32_e32 v254, 0, v254\n"
"v_cvt_pk_bf16_f32 v254, v254, 0\n"
"v_mfma_f32_16x16x32_bf16 v[68:71], a[4:7], v[40:43], v[68:71]\n"
"v_lshlrev_b32_e32 v254, 16, v254\n"
"v_add_f32_e32 v254, 0x3727c5ac, v254\n"
"v_mfma_f32_16x16x32_bf16 v[68:71], a[8:11], v[124:127], v[68:71]\n"
"v_cvt_pk_bf16_f32 v254, v254, 0\n"
"v_lshlrev_b32_e32 v254, 16, v254\n"
"v_mfma_f32_16x16x32_bf16 v[68:71], a[12:15], v[184:187], v[68:71]\n"
"s_waitcnt vmcnt(10)\n"
"v_sqrt_f32_e32 v254, v254\n"
"v_mfma_f32_16x16x32_bf16 v[68:71], a[16:19], v[32:35], v[68:71]\n"
"s_mov_b32 s98, .Lmk_a2-.Lmk_g75\n"
"s_getpc_b64 s[84:85]\n"
".Lmk_g75:\n"
"s_add_u32 s84, s84, s98\n"
"s_addc_u32 s85, s85, 0\n"
"s_and_b32 s84, s84, -16\n"
"v_mfma_f32_16x16x32_bf16 v[68:71], a[20:23], v[192:195], v[68:71]\n"
"ds_write_b128 v3, v[116:119] offset:11264\n"
"v_mfma_f32_16x16x32_bf16 v[68:71], a[24:27], v[196:199], v[68:71]\n"
"s_and_b32 s85, s85, 0xffff\n"
"s_sub_u32 s98, .Lmk_end-.Lmk_g75, s98\n"
"s_min_u32 s86, s98, 0x3000\n"
"s_mov_b32 s87, 0x20000\n"
"v_mfma_f32_16x16x32_bf16 v[68:71], a[28:31], v[200:203], v[68:71]\n"
"s_mov_b32 m0, 0x27800\n"
"s_nop 0\n"
"buffer_load_dwordx4 v0, s[84:87], s99 offen lds\n"
"v_mfma_f32_16x16x32_bf16 v[116:119], a[60:63], v[200:203], 0\n"
"v_cvt_pk_bf16_f32 v254, v254, 0\n"
"v_lshlrev_b32_e32 v254, 16, v254\n"
"v_mfma_f32_16x16x32_bf16 v[116:119], a[56:59], v[196:199], v[116:119]\n"
"v_rcp_f32_e32 v255, v254\n"
"v_mfma_f32_16x16x32_bf16 v[116:119], a[52:55], v[192:195], v[116:119]\n"
"s_add_u32 s98, s52, 0x48000\n"
"buffer_load_dwordx4 v[140:143], v0, s[4:7], s98 offen sc1\n"
"v_mfma_f32_16x16x32_bf16 v[116:119], a[48:51], v[32:35], v[116:119]\n"
"s_waitcnt lgkmcnt(0)\n"
"s_barrier\n"
"v_mfma_f32_16x16x32_bf16 v[116:119], a[44:47], v[184:187], v[116:119]\n"
"v_cmp_u_f32_e32 vcc, v68, v68\n"
"v_mul_f32_e32 v254, 0x3e8293ee, v255\n"
"v_mfma_f32_16x16x32_bf16 v[116:119], a[40:43], v[124:127], v[116:119]\n"
"s_cbranch_vccnz .Lmk_p78s\n"
".Lmk_p78r:\n"
"ds_read_b128 v[144:147], v4\n"
"v_mfma_f32_16x16x32_bf16 v[116:119], a[36:39], v[40:43], v[116:119]\n"
"ds_write_b128 v3, v[68:71] offset:12288\n"
"v_mfma_f32_16x16x32_bf16 v[116:119], a[32:35], v[176:179], v[116:119]\n"
"buffer_load_dwordx4 v[68:71], v0, s[4:7], s98 offen offset:1024 sc1\n"
"v_mfma_f32_16x16x32_bf16 v[148:151], a[64:67], v[176:179], 0\n"
"ds_read_b128 v[152:155], v9 offset:128\n"
"ds_read_b128 v[156:159], v5\n"
"v_mfma_f32_16x16x32_bf16 v[148:151], a[68:71], v[40:43], v[148:151]\n"
"v_mfma_f32_16x16x32_bf16 v[148:151], a[72:75], v[124:127], v[148:151]\n"
"v_mfma_f32_16x16x32_bf16 v[148:151], a[76:79], v[184:187], v[148:151]\n"
"ds_read_b128 v[160:163], v4 offset:1024\n"
"ds_read_b128 v[164:167], v9 offset:192\n"
"v_mfma_f32_16x16x32_bf16 v[148:151], a[80:83], v[32:35], v[148:151]\n"
"buffer_load_dwordx4 v[168:171], v0, s[4:7], s98 offen offset:2048 sc1\n"
"v_mfma_f32_16x16x32_bf16 v[148:151], a[84:87], v[192:195], v[148:151]\n"
"ds_write_b128 v3, v[116:119] offset:13312\n"
"v_mfma_f32_16x16x32_bf16 v[148:151], a[88:91], v[196:199], v[148:151]\n"
"ds_read_b128 v[116:119], v5 offset:1024\n"
"s_waitcnt lgkmcnt(4)\n"
"v_mfma_f32_16x16x32_bf16 v[148:151], a[92:95], v[200:203], v[148:151]\n"
"v_fmac_f32_e32 v144, v253, v152\n"
"v_fmac_f32_e32 v145, v253, v153\n"
"v_mfma_f32_16x16x32_bf16 v[180:183], a[124:127], v[200:203], 0\n"
"v_fmac_f32_e32 v146, v253, v154\n"
"v_fmac_f32_e32 v147, v253, v155\n"
"v_mfma_f32_16x16x32_bf16 v[180:183], a[120:123], v[196:199], v[180:183]\n"
"buffer_load_dwordx4 v[152:155], v0, s[4:7], s98 offen offset:3072 sc1\n"
"v_mfma_f32_16x16x32_bf16 v[180:183], a[116:119], v[192:195], v[180:183]\n"
"ds_read_b128 v[188:191], v4 offset:2048\n"
"ds_read_b128 v[204:207], v9 offset:256\n"
"v_mfma_f32_16x16x32_bf16 v[180:183], a[112:115], v[32:35], v[180:183]\n"
"ds_read_b128 v[208:211], v5 offset:2048\n"
"v_add_f32_e32 v144, v144, v156\n"
"v_mfma_f32_16x16x32_bf16 v[180:183], a[108:111], v[184:187], v[180:183]\n"
"v_add_f32_e32 v145, v145, v157\n"
"v_add_f32_e32 v146, v146, v158\n"
"v_mfma_f32_16x16x32_bf16 v[180:183], a[104:107], v[124:127], v[180:183]\n"
"ds_write_b128 v3, v[148:151] offset:14336\n"
"v_mfma_f32_16x16x32_bf16 v[180:183], a[100:103], v[40:43], v[180:183]\n"
"buffer_load_dwordx4 v[148:151], v1, s[4:7], s98 offen sc1\n"
"v_mfma_f32_16x16x32_bf16 v[180:183], a[96:99], v[176:179], v[180:183]\n"
"s_waitcnt lgkmcnt(4)\n"
"v_add_f32_e32 v147, v147, v159\n"
"v_fmac_f32_e32 v160, v253, v164\n"
"v_mfma_f32_16x16x32_bf16 v[156:159], v[24:27], v[176:179], 0\n"
"v_fmac_f32_e32 v161, v253, v165\n"
"v_fmac_f32_e32 v162, v253, v166\n"
"v_mfma_f32_16x16x32_bf16 v[156:159], v[56:59], v[40:43], v[156:159]\n"
"ds_read_b128 v[212:215], v4 offset:3072\n"
"ds_read_b128 v[216:219], v9 offset:320\n"
"v_mfma_f32_16x16x32_bf16 v[156:159], v[28:31], v[124:127], v[156:159]\n"
"ds_read_b128 v[220:223], v5 offset:3072\n"
"v_fmac_f32_e32 v163, v253, v167\n"
"v_mfma_f32_16x16x32_bf16 v[156:159], v[64:67], v[184:187], v[156:159]\n"
"buffer_load_dwordx4 v[164:167], v1, s[4:7], s98 offen offset:1024 sc1\n"
"v_mfma_f32_16x16x32_bf16 v[156:159], v[52:55], v[32:35], v[156:159]\n"
"s_add_u32 s97, s64, 0x8000\n"
"v_add_f32_e32 v160, v160, v116\n"
"v_add_f32_e32 v161, v161, v117\n"
"v_mfma_f32_16x16x32_bf16 v[156:159], v[36:39], v[192:195], v[156:159]\n"
"ds_write_b128 v3, v[180:183] offset:15360\n"
"v_mfma_f32_16x16x32_bf16 v[156:159], v[108:111], v[196:199], v[156:159]\n"
"buffer_store_dwordx4 v[44:47], v2, s[4:7], s97 offen sc1\n"
"v_mfma_f32_16x16x32_bf16 v[156:159], v[60:63], v[200:203], v[156:159]\n"
"s_waitcnt lgkmcnt(5)\n"
"v_add_f32_e32 v162, v162, v118\n"
"v_add_f32_e32 v163, v163, v119\n"
"v_mfma_f32_16x16x32_bf16 v[116:119], v[80:83], v[200:203], 0\n"
"buffer_load_dwordx4 v[180:183], v1, s[4:7], s98 offen offset:2048 sc1\n"
"v_mfma_f32_16x16x32_bf16 v[116:119], v[88:91], v[196:199], v[116:119]\n"
"s_add_u32 s97, s68, s80\n"
"s_waitcnt vmcnt(18)\n"
"ds_read_b128 v[196:199], v4 offset:4096\n"
"v_mfma_f32_16x16x32_bf16 v[116:119], v[100:103], v[192:195], v[116:119]\n"
"buffer_store_dwordx2 v[18:19], v11, s[4:7], s97 offen sc1\n"
"v_mfma_f32_16x16x32_bf16 v[116:119], v[76:79], v[32:35], v[116:119]\n"
"ds_read_b128 v[32:35], v9\n"
"ds_read_b128 v[192:195], v5 offset:4096\n"
"v_mfma_f32_16x16x32_bf16 v[116:119], v[132:135], v[184:187], v[116:119]\n"
"v_fmac_f32_e32 v188, v253, v204\n"
"v_fmac_f32_e32 v189, v253, v205\n"
"v_mfma_f32_16x16x32_bf16 v[116:119], v[128:131], v[124:127], v[116:119]\n"
"buffer_load_dwordx4 v[124:127], v1, s[4:7], s98 offen offset:3072 sc1\n"
"v_mfma_f32_16x16x32_bf16 v[116:119], v[48:51], v[40:43], v[116:119]\n"
"ds_write_b128 v3, v[156:159] offset:16384\n"
"v_mfma_f32_16x16x32_bf16 v[116:119], v[72:75], v[176:179], v[116:119]\n"
"s_waitcnt vmcnt(16)\n"
"ds_read_b128 v[40:43], v4 offset:5120\n"
"ds_read_b128 v[156:159], v9 offset:64\n"
"; @A G1 0.3\n"
"v_mfma_f32_16x16x32_bf16 v[176:179], a[0:3], v[84:87], v[144:147]\n"
"ds_read_b128 v[184:187], v5 offset:5120\n"
"v_fmac_f32_e32 v190, v253, v206\n"
"v_mfma_f32_16x16x32_bf16 v[176:179], a[4:7], v[92:95], v[176:179]\n"
"v_fmac_f32_e32 v191, v253, v207\n"
"v_add_f32_e32 v188, v188, v208\n"
"v_mfma_f32_16x16x32_bf16 v[176:179], a[8:11], v[104:107], v[176:179]\n"
"s_add_u32 s98, s53, 0x48000\n"
"buffer_load_dwordx4 v[200:203], v0, s[4:7], s98 offen sc1\n"
"v_mfma_f32_16x16x32_bf16 v[176:179], a[12:15], v[112:115], v[176:179]\n"
"s_waitcnt vmcnt(12)\n"
"v_add_f32_e32 v189, v189, v209\n"
"v_add_f32_e32 v190, v190, v210\n"
"v_mfma_f32_16x16x32_bf16 v[176:179], a[16:19], v[96:99], v[176:179]\n"
"s_waitcnt lgkmcnt(8)\n"
"v_add_f32_e32 v191, v191, v211\n"
"v_fmac_f32_e32 v212, v253, v216\n"
"v_mfma_f32_16x16x32_bf16 v[176:179], a[20:23], v[120:123], v[176:179]\n"
"ds_write_b128 v3, v[116:119] offset:17408\n"
"v_mfma_f32_16x16x32_bf16 v[176:179], a[24:27], v[136:139], v[176:179]\n"
"v_fmac_f32_e32 v213, v253, v217\n"
"v_fmac_f32_e32 v214, v253, v218\n"
"v_mfma_f32_16x16x32_bf16 v[176:179], a[28:31], v[172:175], v[176:179]\n"
"buffer_load_dwordx4 v[116:119], v0, s[4:7], s98 offen offset:1024 sc1\n"
"v_mfma_f32_16x16x32_bf16 v[204:207], a[60:63], v[172:175], v[160:163]\n"
"v_fmac_f32_e32 v215, v253, v219\n"
"v_add_f32_e32 v212, v212, v220\n"
"v_mfma_f32_16x16x32_bf16 v[204:207], a[56:59], v[136:139], v[204:207]\n"
"v_add_f32_e32 v213, v213, v221\n"
"v_add_f32_e32 v214, v214, v222\n"
"v_mfma_f32_16x16x32_bf16 v[204:207], a[52:55], v[120:123], v[204:207]\n"
"s_waitcnt lgkmcnt(0)\n"
"s_barrier\n"
"v_mfma_f32_16x16x32_bf16 v[204:207], a[48:51], v[96:99], v[204:207]\n"
"ds_read_b128 v[208:211], v6\n"
"ds_read_b128 v[216:219], v6 offset:1024\n"
"v_mfma_f32_16x16x32_bf16 v[204:207], a[44:47], v[112:115], v[204:207]\n"
"v_cmp_u_f32_e32 vcc, v176, v176\n"
"v_add_f32_e32 v215, v215, v223\n"
"v_mfma_f32_16x16x32_bf16 v[204:207], a[40:43], v[104:107], v[204:207]\n"
"s_cbranch_vccnz .Lmk_p79s\n"
".Lmk_p79r:\n"
"v_fmac_f32_e32 v196, v253, v32\n"
"v_mfma_f32_16x16x32_bf16 v[204:207], a[36:39], v[92:95], v[204:207]\n"
"buffer_load_dwordx4 v[144:147], v0, s[4:7], s98 offen offset:2048 sc1\n"
"v_mfma_f32_16x16x32_bf16 v[204:207], a[32:35], v[84:87], v[204:207]\n"
"v_fmac_f32_e32 v197, v253, v33\n"
"v_fmac_f32_e32 v198, v253, v34\n"
"v_mfma_f32_16x16x32_bf16 v[188:191], a[64:67], v[84:87], v[188:191]\n"
"v_fmac_f32_e32 v199, v253, v35\n"
"v_add_f32_e32 v196, v196, v192\n"
"v_mfma_f32_16x16x32_bf16 v[188:191], a[68:71], v[92:95], v[188:191]\n"
"buffer_load_dwordx4 v[32:35], v0, s[4:7], s98 offen offset:3072 sc1\n"
"v_mfma_f32_16x16x32_bf16 v[188:191], a[72:75], v[104:107], v[188:191]\n"
"v_add_f32_e32 v197, v197, v193\n"
"v_add_f32_e32 v198, v198, v194\n"
"v_mfma_f32_16x16x32_bf16 v[188:191], a[76:79], v[112:115], v[188:191]\n"
"v_add_f32_e32 v199, v199, v195\n"
"v_fmac_f32_e32 v40, v253, v156\n"
"v_mfma_f32_16x16x32_bf16 v[188:191], a[80:83], v[96:99], v[188:191]\n"
"s_waitcnt lgkmcnt(0)\n"
"v_fmac_f32_e32 v41, v253, v157\n"
"v_fmac_f32_e32 v42, v253, v158\n"
"v_mfma_f32_16x16x32_bf16 v[188:191], a[84:87], v[120:123], v[188:191]\n"
"v_fmac_f32_e32 v43, v253, v159\n"
"v_add_f32_e32 v40, v40, v184\n"
"v_mfma_f32_16x16x32_bf16 v[188:191], a[88:91], v[136:139], v[188:191]\n"
"buffer_load_dwordx4 v[156:159], v1, s[4:7], s98 offen sc1\n"
"v_mfma_f32_16x16x32_bf16 v[188:191], a[92:95], v[172:175], v[188:191]\n"
"ds_read_b128 v[160:163], v6 offset:2048\n"
"ds_read_b128 v[192:195], v6 offset:3072\n"
"v_mfma_f32_16x16x32_bf16 v[212:215], a[124:127], v[172:175], v[212:215]\n"
"v_add_f32_e32 v41, v41, v185\n"
"v_add_f32_e32 v42, v42, v186\n"
"v_mfma_f32_16x16x32_bf16 v[212:215], a[120:123], v[136:139], v[212:215]\n"
"v_add_f32_e32 v43, v43, v187\n"
"v_add_f32_e32 v176, v176, v208\n"
"v_mfma_f32_16x16x32_bf16 v[212:215], a[116:119], v[120:123], v[212:215]\n"
"v_add_f32_e32 v177, v177, v209\n"
"v_add_f32_e32 v178, v178, v210\n"
"v_mfma_f32_16x16x32_bf16 v[212:215], a[112:115], v[96:99], v[212:215]\n"
"buffer_load_dwordx4 v[184:187], v1, s[4:7], s98 offen offset:1024 sc1\n"
"v_mfma_f32_16x16x32_bf16 v[212:215], a[108:111], v[112:115], v[212:215]\n"
"v_add_f32_e32 v179, v179, v211\n"
"v_add_f32_e32 v204, v204, v216\n"
"v_mfma_f32_16x16x32_bf16 v[212:215], a[104:107], v[104:107], v[212:215]\n"
"v_add_f32_e32 v205, v205, v217\n"
"v_add_f32_e32 v206, v206, v218\n"
"v_mfma_f32_16x16x32_bf16 v[212:215], a[100:103], v[92:95], v[212:215]\n"
"v_add_f32_e32 v207, v207, v219\n"
"v_mul_f32_e32 v176, v176, v255\n"
"v_mfma_f32_16x16x32_bf16 v[212:215], a[96:99], v[84:87], v[212:215]\n"
"v_mul_f32_e32 v177, v177, v255\n"
"v_mul_f32_e32 v178, v178, v255\n"
"v_mfma_f32_16x16x32_bf16 v[196:199], v[24:27], v[84:87], v[196:199]\n"
"buffer_load_dwordx4 v[208:211], v1, s[4:7], s98 offen offset:2048 sc1\n"
"v_mfma_f32_16x16x32_bf16 v[196:199], v[56:59], v[92:95], v[196:199]\n"
"v_mul_f32_e32 v179, v179, v255\n"
"v_mul_f32_e32 v204, v204, v255\n"
"v_mfma_f32_16x16x32_bf16 v[196:199], v[28:31], v[104:107], v[196:199]\n"
"v_mul_f32_e32 v205, v205, v255\n"
"v_mul_f32_e32 v206, v206, v255\n"
"v_mfma_f32_16x16x32_bf16 v[196:199], v[64:67], v[112:115], v[196:199]\n"
"v_mul_f32_e32 v207, v207, v255\n"
"v_cvt_pk_bf16_f32 v216, v176, v177\n"
"v_mfma_f32_16x16x32_bf16 v[196:199], v[52:55], v[96:99], v[196:199]\n"
"s_waitcnt lgkmcnt(0)\n"
"v_cvt_pk_bf16_f32 v217, v178, v179\n"
"v_cvt_pk_bf16_f32 v218, v204, v205\n"
"v_mfma_f32_16x16x32_bf16 v[196:199], v[36:39], v[120:123], v[196:199]\n"
"buffer_load_dwordx4 v[176:179], v1, s[4:7], s98 offen offset:3072 sc1\n"
"v_mfma_f32_16x16x32_bf16 v[196:199], v[108:111], v[136:139], v[196:199]\n"
"s_add_u32 s98, s64, 0x8000\n"
"ds_read_b128 v[220:223], v6 offset:4096\n"
"ds_read_b128 v[224:227], v6 offset:5120\n"
"v_mfma_f32_16x16x32_bf16 v[196:199], v[60:63], v[172:175], v[196:199]\n"
"v_cvt_pk_bf16_f32 v219, v206, v207\n"
"v_add_f32_e32 v188, v188, v160\n"
"v_mfma_f32_16x16x32_bf16 v[40:43], v[80:83], v[172:175], v[40:43]\n"
"buffer_store_dwordx4 v[44:47], v2, s[4:7], s98 offen offset:1024 sc1\n"
"v_mfma_f32_16x16x32_bf16 v[40:43], v[88:91], v[136:139], v[40:43]\n"
"ds_write_b128 v7, v[216:219] offset:24576\n"
"v_mfma_f32_16x16x32_bf16 v[40:43], v[100:103], v[120:123], v[40:43]\n"
"v_add_f32_e32 v189, v189, v161\n"
"v_add_f32_e32 v190, v190, v162\n"
"v_mfma_f32_16x16x32_bf16 v[40:43], v[76:79], v[96:99], v[40:43]\n"
"v_add_f32_e32 v191, v191, v163\n"
"v_add_f32_e32 v212, v212, v192\n"
"v_mfma_f32_16x16x32_bf16 v[40:43], v[132:135], v[112:115], v[40:43]\n"
"v_add_f32_e32 v213, v213, v193\n"
"v_add_f32_e32 v214, v214, v194\n"
"v_mfma_f32_16x16x32_bf16 v[40:43], v[128:131], v[104:107], v[40:43]\n"
"v_add_f32_e32 v215, v215, v195\n"
"v_mul_f32_e32 v188, v188, v255\n"
"v_mfma_f32_16x16x32_bf16 v[40:43], v[48:51], v[92:95], v[40:43]\n"
"v_mul_f32_e32 v189, v189, v255\n"
"v_mul_f32_e32 v190, v190, v255\n"
"v_mfma_f32_16x16x32_bf16 v[40:43], v[72:75], v[84:87], v[40:43]\n"
"v_mul_f32_e32 v191, v191, v255\n"
"v_mul_f32_e32 v212, v212, v255\n"
"v_mul_f32_e32 v213, v213, v255\n"
"v_mul_f32_e32 v214, v214, v255\n"
"v_mul_f32_e32 v215, v215, v255\n"
"v_cvt_pk_bf16_f32 v44, v188, v189\n"
"v_cvt_pk_bf16_f32 v45, v190, v191\n"
"v_cvt_pk_bf16_f32 v46, v212, v213\n"
"v_cvt_pk_bf16_f32 v47, v214, v215\n"
"ds_write_b128 v7, v[44:47] offset:57344\n"
".Lmk_a1:\n"
"s_waitcnt vmcnt(15)\n"
"; @A G1 1.0\n"
"v_mfma_f32_16x16x32_bf16 v[84:87], a[0:3], v[140:143], 0\n"
"v_mfma_f32_16x16x32_bf16 v[84:87], a[4:7], v[68:71], v[84:87]\n"
"s_add_u32 s98, s62, 0x4800\n"
"v_mfma_f32_16x16x32_bf16 v[84:87], a[8:11], v[168:171], v[84:87]\n"
"s_add_u32 s97, s54, 0x48000\n"
"buffer_load_dwordx4 v[92:95], v0, s[4:7], s97 offen sc1\n"
"v_mfma_f32_16x16x32_bf16 v[84:87], a[12:15], v[152:155], v[84:87]\n"
"s_waitcnt vmcnt(10)\n"
"v_mfma_f32_16x16x32_bf16 v[84:87], a[16:19], v[148:151], v[84:87]\n"
"buffer_load_dword v255, v10, s[4:7], s98 offen sc1\n"
"v_mfma_f32_16x16x32_bf16 v[84:87], a[20:23], v[164:167], v[84:87]\n"
"s_waitcnt lgkmcnt(2)\n"
"v_add_f32_e32 v196, v196, v220\n"
"v_add_f32_e32 v197, v197, v221\n"
"v_mfma_f32_16x16x32_bf16 v[84:87], a[24:27], v[180:183], v[84:87]\n"
"v_add_f32_e32 v198, v198, v222\n"
"v_add_f32_e32 v199, v199, v223\n"
"v_mfma_f32_16x16x32_bf16 v[84:87], a[28:31], v[124:127], v[84:87]\n"
"buffer_load_dwordx4 v[96:99], v0, s[4:7], s97 offen offset:1024 sc1\n"
"v_mfma_f32_16x16x32_bf16 v[104:107], a[60:63], v[124:127], 0\n"
"s_add_u32 s96, s70, 0\n"
"buffer_load_dwordx4 a[128:131], v0, s[4:7], s96 offen sc1\n"
"v_mfma_f32_16x16x32_bf16 v[104:107], a[56:59], v[180:183], v[104:107]\n"
"buffer_load_dword v253, v10, s[4:7], s98 offen offset:256 sc1\n"
"v_mfma_f32_16x16x32_bf16 v[104:107], a[52:55], v[164:167], v[104:107]\n"
"v_add_f32_e32 v40, v40, v224\n"
"v_add_f32_e32 v41, v41, v225\n"
"v_mfma_f32_16x16x32_bf16 v[104:107], a[48:51], v[148:151], v[104:107]\n"
"v_add_f32_e32 v42, v42, v226\n"
"v_add_f32_e32 v43, v43, v227\n"
"v_mfma_f32_16x16x32_bf16 v[104:107], a[44:47], v[152:155], v[104:107]\n"
"v_cmp_u_f32_e32 vcc, v84, v84\n"
"v_mul_f32_e32 v196, v196, v254\n"
"v_mfma_f32_16x16x32_bf16 v[104:107], a[40:43], v[168:171], v[104:107]\n"
"s_cbranch_vccnz .Lmk_p81s\n"
".Lmk_p81r:\n"
"v_mul_f32_e32 v197, v197, v254\n"
"v_mfma_f32_16x16x32_bf16 v[104:107], a[36:39], v[68:71], v[104:107]\n"
"buffer_load_dwordx4 v[112:115], v0, s[4:7], s97 offen offset:2048 sc1\n"
"v_mfma_f32_16x16x32_bf16 v[104:107], a[32:35], v[140:143], v[104:107]\n"
"ds_write_b128 v3, v[84:87]\n"
"v_mfma_f32_16x16x32_bf16 v[84:87], a[64:67], v[140:143], 0\n"
"buffer_load_dword v252, v10, s[4:7], s98 offen offset:512 sc1\n"
"v_mfma_f32_16x16x32_bf16 v[84:87], a[68:71], v[68:71], v[84:87]\n"
"buffer_load_dwordx4 v[120:123], v0, s[4:7], s97 offen offset:3072 sc1\n"
"v_mfma_f32_16x16x32_bf16 v[84:87], a[72:75], v[168:171], v[84:87]\n"
"v_mul_f32_e32 v198, v198, v254\n"
"v_mul_f32_e32 v199, v199, v254\n"
"v_mfma_f32_16x16x32_bf16 v[84:87], a[76:79], v[152:155], v[84:87]\n"
"buffer_load_dword v251, v10, s[4:7], s98 offen offset:768 sc1\n"
"v_mfma_f32_16x16x32_bf16 v[84:87], a[80:83], v[148:151], v[84:87]\n"
"v_mul_f32_e32 v40, v40, v254\n"
"v_mul_f32_e32 v41, v41, v254\n"
"v_mfma_f32_16x16x32_bf16 v[84:87], a[84:87], v[164:167], v[84:87]\n"
"ds_write_b128 v3, v[104:107] offset:1024\n"
"v_mfma_f32_16x16x32_bf16 v[84:87], a[88:91], v[180:183], v[84:87]\n"
"buffer_load_dwordx4 v[104:107], v1, s[4:7], s97 offen sc1\n"
"v_mfma_f32_16x16x32_bf16 v[84:87], a[92:95], v[124:127], v[84:87]\n"
"buffer_load_dword v250, v10, s[4:7], s98 offen offset:1024 sc1\n"
"v_mfma_f32_16x16x32_bf16 v[136:139], a[124:127], v[124:127], 0\n"
"v_mul_f32_e32 v42, v42, v254\n"
"v_mul_f32_e32 v43, v43, v254\n"
"v_mfma_f32_16x16x32_bf16 v[136:139], a[120:123], v[180:183], v[136:139]\n"
"v_cvt_pk_bf16_f32 v160, v196, v197\n"
"v_cvt_pk_bf16_f32 v161, v198, v199\n"
"v_mfma_f32_16x16x32_bf16 v[136:139], a[116:119], v[164:167], v[136:139]\n"
"buffer_load_dword v254, v10, s[4:7], s98 offen offset:1280 sc1\n"
"v_mfma_f32_16x16x32_bf16 v[136:139], a[112:115], v[148:151], v[136:139]\n"
"buffer_load_dwordx4 v[172:175], v1, s[4:7], s97 offen offset:1024 sc1\n"
"v_mfma_f32_16x16x32_bf16 v[136:139], a[108:111], v[152:155], v[136:139]\n"
"buffer_load_dwordx4 a[132:135], v0, s[4:7], s96 offen offset:1024 sc1\n"
"v_mfma_f32_16x16x32_bf16 v[136:139], a[104:107], v[168:171], v[136:139]\n"
"ds_write_b128 v3, v[84:87] offset:2048\n"
"v_mfma_f32_16x16x32_bf16 v[136:139], a[100:103], v[68:71], v[136:139]\n"
"buffer_load_dword v249, v10, s[4:7], s98 offen offset:1536 sc1\n"
"v_mfma_f32_16x16x32_bf16 v[136:139], a[96:99], v[140:143], v[136:139]\n"
"v_cvt_pk_bf16_f32 v162, v40, v41\n"
"v_cvt_pk_bf16_f32 v163, v42, v43\n"
"v_mfma_f32_16x16x32_bf16 v[40:43], v[24:27], v[140:143], 0\n"
"buffer_load_dwordx4 v[84:87], v1, s[4:7], s97 offen offset:2048 sc1\n"
"v_mfma_f32_16x16x32_bf16 v[40:43], v[56:59], v[68:71], v[40:43]\n"
"buffer_load_dword v248, v10, s[4:7], s98 offen offset:1792 sc1\n"
"v_mfma_f32_16x16x32_bf16 v[40:43], v[28:31], v[168:171], v[40:43]\n"
"ds_write_b128 v2, v[160:163] offset:49152\n"
"v_mfma_f32_16x16x32_bf16 v[40:43], v[64:67], v[152:155], v[40:43]\n"
"v_mfma_f32_16x16x32_bf16 v[40:43], v[52:55], v[148:151], v[40:43]\n"
"v_mfma_f32_16x16x32_bf16 v[40:43], v[36:39], v[164:167], v[40:43]\n"
"buffer_load_dwordx4 v[160:163], v1, s[4:7], s97 offen offset:3072 sc1\n"
"v_mfma_f32_16x16x32_bf16 v[40:43], v[108:111], v[180:183], v[40:43]\n"
"ds_write_b128 v3, v[136:139] offset:3072\n"
"v_mfma_f32_16x16x32_bf16 v[40:43], v[60:63], v[124:127], v[40:43]\n"
"v_mfma_f32_16x16x32_bf16 v[136:139], v[80:83], v[124:127], 0\n"
"v_mfma_f32_16x16x32_bf16 v[136:139], v[88:91], v[180:183], v[136:139]\n"
"v_mfma_f32_16x16x32_bf16 v[136:139], v[100:103], v[164:167], v[136:139]\n"
"v_mfma_f32_16x16x32_bf16 v[136:139], v[76:79], v[148:151], v[136:139]\n"
"v_mfma_f32_16x16x32_bf16 v[136:139], v[132:135], v[152:155], v[136:139]\n"
"v_mfma_f32_16x16x32_bf16 v[136:139], v[128:131], v[168:171], v[136:139]\n"
"s_nop 1\n"
"ds_write_b128 v3, v[40:43] offset:4096\n"
"v_mfma_f32_16x16x32_bf16 v[136:139], v[48:51], v[68:71], v[136:139]\n"
"v_mfma_f32_16x16x32_bf16 v[136:139], v[72:75], v[140:143], v[136:139]\n"
"s_waitcnt vmcnt(23)\n"
"; @A G1 1.1\n"
"v_mfma_f32_16x16x32_bf16 v[40:43], a[0:3], v[200:203], 0\n"
"buffer_load_dwordx4 a[136:139], v0, s[4:7], s96 offen offset:2048 sc1\n"
"v_mfma_f32_16x16x32_bf16 v[40:43], a[4:7], v[116:119], v[40:43]\n"
"v_mfma_f32_16x16x32_bf16 v[40:43], a[8:11], v[144:147], v[40:43]\n"
"s_add_u32 s98, s55, 0x48000\n"
"buffer_load_dwordx4 v[68:71], v0, s[4:7], s98 offen sc1\n"
"v_mfma_f32_16x16x32_bf16 v[40:43], a[12:15], v[32:35], v[40:43]\n"
"s_waitcnt vmcnt(21)\n"
"v_mfma_f32_16x16x32_bf16 v[40:43], a[16:19], v[156:159], v[40:43]\n"
"v_mfma_f32_16x16x32_bf16 v[40:43], a[20:23], v[184:187], v[40:43]\n"
"ds_write_b128 v3, v[136:139] offset:5120\n"
"v_mfma_f32_16x16x32_bf16 v[40:43], a[24:27], v[208:211], v[40:43]\n"
"v_mfma_f32_16x16x32_bf16 v[40:43], a[28:31], v[176:179], v[40:43]\n"
"buffer_load_dwordx4 v[124:127], v0, s[4:7], s98 offen offset:1024 sc1\n"
"v_mfma_f32_16x16x32_bf16 v[136:139], a[60:63], v[176:179], 0\n"
"v_mfma_f32_16x16x32_bf16 v[136:139], a[56:59], v[208:211], v[136:139]\n"
"v_mfma_f32_16x16x32_bf16 v[136:139], a[52:55], v[184:187], v[136:139]\n"
"v_mfma_f32_16x16x32_bf16 v[136:139], a[48:51], v[156:159], v[136:139]\n"
"v_mfma_f32_16x16x32_bf16 v[136:139], a[44:47], v[32:35], v[136:139]\n"
"s_add_u32 s97, s67, 0\n"
"s_nop 0\n"
"v_cmp_u_f32_e32 vcc, v40, v40\n"
"v_mfma_f32_16x16x32_bf16 v[136:139], a[40:43], v[144:147], v[136:139]\n"
"s_cbranch_vccnz .Lmk_p82s\n"
".Lmk_p82r:\n"
"v_mfma_f32_16x16x32_bf16 v[136:139], a[36:39], v[116:119], v[136:139]\n"
"buffer_load_dwordx4 v[140:143], v0, s[4:7], s98 offen offset:2048 sc1\n"
"s_add_u32 s95, s64, 0x2000\n"
"v_mfma_f32_16x16x32_bf16 v[136:139], a[32:35], v[200:203], v[136:139]\n"
"ds_write_b128 v3, v[40:43] offset:6144\n"
"v_mfma_f32_16x16x32_bf16 v[40:43], a[64:67], v[200:203], 0\n"
"buffer_load_dwordx2 v[246:247], v12, s[4:7], s97 offen sc1\n"
"v_mfma_f32_16x16x32_bf16 v[40:43], a[68:71], v[116:119], v[40:43]\n"
"buffer_load_dwordx4 v[148:151], v0, s[4:7], s98 offen offset:3072 sc1\n"
"v_mfma_f32_16x16x32_bf16 v[40:43], a[72:75], v[144:147], v[40:43]\n"
"buffer_store_dwordx4 v[216:219], v7, s[4:7], s95 offen sc1\n"
"v_mfma_f32_16x16x32_bf16 v[40:43], a[76:79], v[32:35], v[40:43]\n"
"v_mfma_f32_16x16x32_bf16 v[40:43], a[80:83], v[156:159], v[40:43]\n"
"buffer_load_dwordx4 a[140:143], v0, s[4:7], s96 offen offset:3072 sc1\n"
"v_mfma_f32_16x16x32_bf16 v[40:43], a[84:87], v[184:187], v[40:43]\n"
"ds_write_b128 v3, v[136:139] offset:7168\n"
"v_mfma_f32_16x16x32_bf16 v[40:43], a[88:91], v[208:211], v[40:43]\n"
"buffer_load_dwordx4 v[136:139], v1, s[4:7], s98 offen sc1\n"
"v_mfma_f32_16x16x32_bf16 v[40:43], a[92:95], v[176:179], v[40:43]\n"
"v_mfma_f32_16x16x32_bf16 v[152:155], a[124:127], v[176:179], 0\n"
"v_mfma_f32_16x16x32_bf16 v[152:155], a[120:123], v[208:211], v[152:155]\n"
"v_mfma_f32_16x16x32_bf16 v[152:155], a[116:119], v[184:187], v[152:155]\n"
"v_mfma_f32_16x16x32_bf16 v[152:155], a[112:115], v[156:159], v[152:155]\n"
"buffer_load_dwordx4 v[164:167], v1, s[4:7], s98 offen offset:1024 sc1\n"
"v_mfma_f32_16x16x32_bf16 v[152:155], a[108:111], v[32:35], v[152:155]\n"
"v_mfma_f32_16x16x32_bf16 v[152:155], a[104:107], v[144:147], v[152:155]\n"
"s_nop 0\n"
"ds_write_b128 v3, v[40:43] offset:8192\n"
"v_mfma_f32_16x16x32_bf16 v[152:155], a[100:103], v[116:119], v[152:155]\n"
"v_mfma_f32_16x16x32_bf16 v[152:155], a[96:99], v[200:203], v[152:155]\n"
"v_mfma_f32_16x16x32_bf16 v[40:43], v[24:27], v[200:203], 0\n"
"buffer_load_dwordx4 v[168:171], v1, s[4:7], s98 offen offset:2048 sc1\n"
"v_mfma_f32_16x16x32_bf16 v[40:43], v[56:59], v[116:119], v[40:43]\n"
"v_mfma_f32_16x16x32_bf16 v[40:43], v[28:31], v[144:147], v[40:43]\n"
"v_mfma_f32_16x16x32_bf16 v[40:43], v[64:67], v[32:35], v[40:43]\n"
"v_mfma_f32_16x16x32_bf16 v[40:43], v[52:55], v[156:159], v[40:43]\n"
"s_waitcnt vmcnt(12)\n"
"v_add_f32_e32 v255, v255, v250\n"
"v_add_f32_e32 v253, v253, v254\n"
"v_mfma_f32_16x16x32_bf16 v[40:43], v[36:39], v[184:187], v[40:43]\n"
"buffer_load_dwordx4 v[180:183], v1, s[4:7], s98 offen offset:3072 sc1\n"
"v_mfma_f32_16x16x32_bf16 v[40:43], v[108:111], v[208:211], v[40:43]\n"
"ds_write_b128 v3, v[152:155] offset:9216\n"
"v_mfma_f32_16x16x32_bf16 v[40:43], v[60:63], v[176:179], v[40:43]\n"
"v_add_f32_e32 v252, v252, v249\n"
"v_add_f32_e32 v251, v251, v248\n"
"v_mfma_f32_16x16x32_bf16 v[152:155], v[80:83], v[176:179], 0\n"
"buffer_load_dwordx4 a[144:147], v1, s[4:7], s96 offen sc1\n"
"s_add_u32 s98, s64, 0x6000\n"
"v_mfma_f32_16x16x32_bf16 v[152:155], v[88:91], v[208:211], v[152:155]\n"
"v_add_f32_e32 v255, v255, v252\n"
"v_add_f32_e32 v253, v253, v251\n"
"v_mfma_f32_16x16x32_bf16 v[152:155], v[100:103], v[184:187], v[152:155]\n"
"buffer_store_dwordx4 v[44:47], v7, s[4:7], s98 offen sc1\n"
"v_mfma_f32_16x16x32_bf16 v[152:155], v[76:79], v[156:159], v[152:155]\n"
"v_add_f32_e32 v255, v255, v253\n"
"v_cmp_u_f32_e32 vcc, v255, v255\n"
"v_mfma_f32_16x16x32_bf16 v[152:155], v[132:135], v[32:35], v[152:155]\n"
"s_cbranch_vccnz .Lmk_m85s\n"
".Lmk_m85r:\n"
"v_mov_b32_e32 v253, v255\n"
"v_mfma_f32_16x16x32_bf16 v[152:155], v[128:131], v[144:147], v[152:155]\n"
"ds_write_b128 v3, v[40:43] offset:10240\n"
"v_mfma_f32_16x16x32_bf16 v[152:155], v[48:51], v[116:119], v[152:155]\n"
"v_permlane32_swap_b32_e32 v255, v253\n"
"v_add_f32_e32 v255, v255, v253\n"
"v_mfma_f32_16x16x32_bf16 v[152:155], v[72:75], v[200:203], v[152:155]\n"
"v_mul_f32_e32 v255, 0x3a800000, v255\n"
"v_mov_b32_e32 v253, v255\n"
"; @A G1 1.2\n"
"v_mfma_f32_16x16x32_bf16 v[32:35], a[0:3], v[92:95], 0\n"
"s_nop 0\n"
"v_permlane16_swap_b32_e32 v255, v253\n"
"v_cvt_pk_bf16_f32 v254, -v255, 0\n"
"v_mfma_f32_16x16x32_bf16 v[32:35], a[4:7], v[96:99], v[32:35]\n"
"s_waitcnt vmcnt(9)\n"
"v_cmp_ne_u64_e32 vcc, v[246:247], v[18:19]\n"
"v_mfma_f32_16x16x32_bf16 v[32:35], a[8:11], v[112:115], v[32:35]\n"
"s_cbranch_vccnz .Lmk_f72s\n"
".Lmk_f72r:\n"
"v_fma_f32 v253, -v255, v255, v253\n"
"v_mfma_f32_16x16x32_bf16 v[32:35], a[12:15], v[120:123], v[32:35]\n"
"v_lshlrev_b32_e32 v254, 16, v254\n"
"v_max_f32_e32 v253, 0, v253\n"
"v_mfma_f32_16x16x32_bf16 v[32:35], a[16:19], v[104:107], v[32:35]\n"
"v_cvt_pk_bf16_f32 v253, v253, 0\n"
"v_lshlrev_b32_e32 v253, 16, v253\n"
"v_mfma_f32_16x16x32_bf16 v[32:35], a[20:23], v[172:175], v[32:35]\n"
"ds_write_b128 v3, v[152:155] offset:11264\n"
"v_mfma_f32_16x16x32_bf16 v[32:35], a[24:27], v[84:87], v[32:35]\n"
"v_add_f32_e32 v253, 0x3727c5ac, v253\n"
"v_cvt_pk_bf16_f32 v253, v253, 0\n"
"v_mfma_f32_16x16x32_bf16 v[32:35], a[28:31], v[160:163], v[32:35]\n"
"v_lshlrev_b32_e32 v253, 16, v253\n"
"v_mfma_f32_16x16x32_bf16 v[40:43], a[60:63], v[160:163], 0\n"
"v_sqrt_f32_e32 v253, v253\n"
"v_mfma_f32_16x16x32_bf16 v[40:43], a[56:59], v[84:87], v[40:43]\n"
"v_cvt_pk_bf16_f32 v253, v253, 0\n"
"v_lshlrev_b32_e32 v253, 16, v253\n"
"v_mfma_f32_16x16x32_bf16 v[40:43], a[52:55], v[172:175], v[40:43]\n"
"s_add_u32 s98, s52, 0x8000\n"
"buffer_load_dwordx4 v[44:47], v0, s[4:7], s98 offen sc1\n"
"v_mfma_f32_16x16x32_bf16 v[40:43], a[48:51], v[104:107], v[40:43]\n"
"s_waitcnt lgkmcnt(0)\n"
"s_barrier\n"
"v_mfma_f32_16x16x32_bf16 v[40:43], a[44:47], v[120:123], v[40:43]\n"
"ds_read_b128 v[116:119], v4\n"
"v_cmp_u_f32_e32 vcc, v32, v32\n"
"v_mfma_f32_16x16x32_bf16 v[40:43], a[40:43], v[112:115], v[40:43]\n"
"s_cbranch_vccnz .Lmk_p83s\n"
".Lmk_p83r:\n"
"ds_read_b128 v[144:147], v9 offset:128\n"
"v_mfma_f32_16x16x32_bf16 v[40:43], a[36:39], v[96:99], v[40:43]\n"
"ds_write_b128 v3, v[32:35] offset:12288\n"
"v_mfma_f32_16x16x32_bf16 v[40:43], a[32:35], v[92:95], v[40:43]\n"
"buffer_load_dwordx4 v[32:35], v0, s[4:7], s98 offen offset:1024 sc1\n"
"v_mfma_f32_16x16x32_bf16 v[152:155], a[64:67], v[92:95], 0\n"
"buffer_load_dwordx4 a[148:151], v1, s[4:7], s96 offen offset:1024 sc1\n"
"v_mfma_f32_16x16x32_bf16 v[152:155], a[68:71], v[96:99], v[152:155]\n"
"v_rcp_f32_e32 v255, v253\n"
"v_mfma_f32_16x16x32_bf16 v[152:155], a[72:75], v[112:115], v[152:155]\n"
"ds_read_b128 v[156:159], v5\n"
"v_mul_f32_e32 v253, 0x3e8293ee, v255\n"
"v_mfma_f32_16x16x32_bf16 v[152:155], a[76:79], v[120:123], v[152:155]\n"
"ds_read_b128 v[176:179], v4 offset:1024\n"
"ds_read_b128 v[184:187], v9 offset:192\n"
"v_mfma_f32_16x16x32_bf16 v[152:155], a[80:83], v[104:107], v[152:155]\n"
"buffer_load_dwordx4 v[188:191], v0, s[4:7], s98 offen offset:2048 sc1\n"
"v_mfma_f32_16x16x32_bf16 v[152:155], a[84:87], v[172:175], v[152:155]\n"
"ds_write_b128 v3, v[40:43] offset:13312\n"
"v_mfma_f32_16x16x32_bf16 v[152:155], a[88:91], v[84:87], v[152:155]\n"
"ds_read_b128 v[40:43], v5 offset:1024\n"
"v_mfma_f32_16x16x32_bf16 v[152:155], a[92:95], v[160:163], v[152:155]\n"
"s_waitcnt lgkmcnt(4)\n"
"v_fmac_f32_e32 v116, v254, v144\n"
"v_fmac_f32_e32 v117, v254, v145\n"
"v_mfma_f32_16x16x32_bf16 v[192:195], a[124:127], v[160:163], 0\n"
"v_fmac_f32_e32 v118, v254, v146\n"
"v_fmac_f32_e32 v119, v254, v147\n"
"v_mfma_f32_16x16x32_bf16 v[192:195], a[120:123], v[84:87], v[192:195]\n"
"buffer_load_dwordx4 v[144:147], v0, s[4:7], s98 offen offset:3072 sc1\n"
"v_mfma_f32_16x16x32_bf16 v[192:195], a[116:119], v[172:175], v[192:195]\n"
"ds_read_b128 v[196:199], v4 offset:2048\n"
"ds_read_b128 v[200:203], v9 offset:256\n"
"v_mfma_f32_16x16x32_bf16 v[192:195], a[112:115], v[104:107], v[192:195]\n"
"ds_read_b128 v[204:207], v5 offset:2048\n"
"v_add_f32_e32 v116, v116, v156\n"
"v_mfma_f32_16x16x32_bf16 v[192:195], a[108:111], v[120:123], v[192:195]\n"
"v_add_f32_e32 v117, v117, v157\n"
"v_add_f32_e32 v118, v118, v158\n"
"v_mfma_f32_16x16x32_bf16 v[192:195], a[104:107], v[112:115], v[192:195]\n"
"ds_write_b128 v3, v[152:155] offset:14336\n"
"v_mfma_f32_16x16x32_bf16 v[192:195], a[100:103], v[96:99], v[192:195]\n"
"buffer_load_dwordx4 v[152:155], v1, s[4:7], s98 offen sc1\n"
"v_mfma_f32_16x16x32_bf16 v[192:195], a[96:99], v[92:95], v[192:195]\n"
"s_waitcnt lgkmcnt(4)\n"
"v_add_f32_e32 v119, v119, v159\n"
"v_fmac_f32_e32 v176, v254, v184\n"
"v_mfma_f32_16x16x32_bf16 v[156:159], v[24:27], v[92:95], 0\n"
"v_fmac_f32_e32 v177, v254, v185\n"
"v_fmac_f32_e32 v178, v254, v186\n"
"v_mfma_f32_16x16x32_bf16 v[156:159], v[56:59], v[96:99], v[156:159]\n"
"ds_read_b128 v[208:211], v4 offset:3072\n"
"ds_read_b128 v[212:215], v9 offset:320\n"
"v_mfma_f32_16x16x32_bf16 v[156:159], v[28:31], v[112:115], v[156:159]\n"
"ds_read_b128 v[216:219], v5 offset:3072\n"
"v_fmac_f32_e32 v179, v254, v187\n"
"v_mfma_f32_16x16x32_bf16 v[156:159], v[64:67], v[120:123], v[156:159]\n"
"buffer_load_dwordx4 v[184:187], v1, s[4:7], s98 offen offset:1024 sc1\n"
"v_mfma_f32_16x16x32_bf16 v[156:159], v[52:55], v[104:107], v[156:159]\n"
"buffer_load_dwordx4 a[152:155], v1, s[4:7], s96 offen offset:2048 sc1\n"
"v_mfma_f32_16x16x32_bf16 v[156:159], v[36:39], v[172:175], v[156:159]\n"
"ds_write_b128 v3, v[192:195] offset:15360\n"
"v_mfma_f32_16x16x32_bf16 v[156:159], v[108:111], v[84:87], v[156:159]\n"
"v_add_f32_e32 v176, v176, v40\n"
"v_add_f32_e32 v177, v177, v41\n"
"v_mfma_f32_16x16x32_bf16 v[156:159], v[60:63], v[160:163], v[156:159]\n"
"s_waitcnt lgkmcnt(5)\n"
"v_add_f32_e32 v178, v178, v42\n"
"v_add_f32_e32 v179, v179, v43\n"
"v_mfma_f32_16x16x32_bf16 v[40:43], v[80:83], v[160:163], 0\n"
"buffer_load_dwordx4 v[160:163], v1, s[4:7], s98 offen offset:2048 sc1\n"
"v_mfma_f32_16x16x32_bf16 v[40:43], v[88:91], v[84:87], v[40:43]\n"
"ds_read_b128 v[84:87], v4 offset:4096\n"
"ds_read_b128 v[192:195], v9\n"
"v_mfma_f32_16x16x32_bf16 v[40:43], v[100:103], v[172:175], v[40:43]\n"
"ds_read_b128 v[172:175], v5 offset:4096\n"
"v_fmac_f32_e32 v196, v254, v200\n"
"v_mfma_f32_16x16x32_bf16 v[40:43], v[76:79], v[104:107], v[40:43]\n"
"v_fmac_f32_e32 v197, v254, v201\n"
"v_fmac_f32_e32 v198, v254, v202\n"
"v_mfma_f32_16x16x32_bf16 v[40:43], v[132:135], v[120:123], v[40:43]\n"
"v_fmac_f32_e32 v199, v254, v203\n"
"v_add_f32_e32 v196, v196, v204\n"
"v_mfma_f32_16x16x32_bf16 v[40:43], v[128:131], v[112:115], v[40:43]\n"
"buffer_load_dwordx4 v[104:107], v1, s[4:7], s98 offen offset:3072 sc1\n"
"v_mfma_f32_16x16x32_bf16 v[40:43], v[48:51], v[96:99], v[40:43]\n"
"ds_write_b128 v3, v[156:159] offset:16384\n"
"v_mfma_f32_16x16x32_bf16 v[40:43], v[72:75], v[92:95], v[40:43]\n"
"s_waitcnt vmcnt(18)\n"
"ds_read_b128 v[92:95], v4 offset:5120\n"
"ds_read_b128 v[96:99], v9 offset:64\n"
"; @A G1 1.3\n"
"v_mfma_f32_16x16x32_bf16 v[112:115], a[0:3], v[68:71], v[116:119]\n"
"ds_read_b128 v[120:123], v5 offset:5120\n"
"v_add_f32_e32 v197, v197, v205\n"
"v_mfma_f32_16x16x32_bf16 v[112:115], a[4:7], v[124:127], v[112:115]\n"
"s_waitcnt lgkmcnt(8)\n"
"v_add_f32_e32 v198, v198, v206\n"
"v_add_f32_e32 v199, v199, v207\n"
"v_mfma_f32_16x16x32_bf16 v[112:115], a[8:11], v[140:143], v[112:115]\n"
"s_add_u32 s98, s53, 0x8000\n"
"buffer_load_dwordx4 v[156:159], v0, s[4:7], s98 offen sc1\n"
"v_mfma_f32_16x16x32_bf16 v[112:115], a[12:15], v[148:151], v[112:115]\n"
"s_waitcnt vmcnt(13)\n"
"v_fmac_f32_e32 v208, v254, v212\n"
"v_fmac_f32_e32 v209, v254, v213\n"
"v_mfma_f32_16x16x32_bf16 v[112:115], a[16:19], v[136:139], v[112:115]\n"
"v_fmac_f32_e32 v210, v254, v214\n"
"v_fmac_f32_e32 v211, v254, v215\n"
"v_mfma_f32_16x16x32_bf16 v[112:115], a[20:23], v[164:167], v[112:115]\n"
"ds_write_b128 v3, v[40:43] offset:17408\n"
"v_mfma_f32_16x16x32_bf16 v[112:115], a[24:27], v[168:171], v[112:115]\n"
"v_add_f32_e32 v208, v208, v216\n"
"v_add_f32_e32 v209, v209, v217\n"
"v_mfma_f32_16x16x32_bf16 v[112:115], a[28:31], v[180:183], v[112:115]\n"
"buffer_load_dwordx4 v[40:43], v0, s[4:7], s98 offen offset:1024 sc1\n"
"v_mfma_f32_16x16x32_bf16 v[200:203], a[60:63], v[180:183], v[176:179]\n"
"buffer_load_dwordx4 a[156:159], v1, s[4:7], s96 offen offset:3072 sc1\n"
"v_mfma_f32_16x16x32_bf16 v[200:203], a[56:59], v[168:171], v[200:203]\n"
"s_waitcnt lgkmcnt(5)\n"
"v_add_f32_e32 v210, v210, v218\n"
"v_add_f32_e32 v211, v211, v219\n"
"v_mfma_f32_16x16x32_bf16 v[200:203], a[52:55], v[164:167], v[200:203]\n"
"s_waitcnt lgkmcnt(0)\n"
"s_barrier\n"
"v_mfma_f32_16x16x32_bf16 v[200:203], a[48:51], v[136:139], v[200:203]\n"
"ds_read_b128 v[204:207], v6\n"
"ds_read_b128 v[212:215], v6 offset:1024\n"
"v_mfma_f32_16x16x32_bf16 v[200:203], a[44:47], v[148:151], v[200:203]\n"
"v_cmp_u_f32_e32 vcc, v112, v112\n"
"v_fmac_f32_e32 v84, v254, v192\n"
"v_mfma_f32_16x16x32_bf16 v[200:203], a[40:43], v[140:143], v[200:203]\n"
"s_cbranch_vccnz .Lmk_p84s\n"
".Lmk_p84r:\n"
"v_fmac_f32_e32 v85, v254, v193\n"
"v_mfma_f32_16x16x32_bf16 v[200:203], a[36:39], v[124:127], v[200:203]\n"
"buffer_load_dwordx4 v[116:119], v0, s[4:7], s98 offen offset:2048 sc1\n"
"v_mfma_f32_16x16x32_bf16 v[200:203], a[32:35], v[68:71], v[200:203]\n"
"v_fmac_f32_e32 v86, v254, v194\n"
"v_fmac_f32_e32 v87, v254, v195\n"
"v_mfma_f32_16x16x32_bf16 v[196:199], a[64:67], v[68:71], v[196:199]\n"
"v_add_f32_e32 v84, v84, v172\n"
"v_add_f32_e32 v85, v85, v173\n"
"v_mfma_f32_16x16x32_bf16 v[196:199], a[68:71], v[124:127], v[196:199]\n"
"buffer_load_dwordx4 v[176:179], v0, s[4:7], s98 offen offset:3072 sc1\n"
"v_mfma_f32_16x16x32_bf16 v[196:199], a[72:75], v[140:143], v[196:199]\n"
"v_add_f32_e32 v86, v86, v174\n"
"v_add_f32_e32 v87, v87, v175\n"
"v_mfma_f32_16x16x32_bf16 v[196:199], a[76:79], v[148:151], v[196:199]\n"
"v_fmac_f32_e32 v92, v254, v96\n"
"v_fmac_f32_e32 v93, v254, v97\n"
"v_mfma_f32_16x16x32_bf16 v[196:199], a[80:83], v[136:139], v[196:199]\n"
"s_waitcnt lgkmcnt(0)\n"
"v_fmac_f32_e32 v94, v254, v98\n"
"v_fmac_f32_e32 v95, v254, v99\n"
"v_mfma_f32_16x16x32_bf16 v[196:199], a[84:87], v[164:167], v[196:199]\n"
"v_add_f32_e32 v92, v92, v120\n"
"v_add_f32_e32 v93, v93, v121\n"
"v_mfma_f32_16x16x32_bf16 v[196:199], a[88:91], v[168:171], v[196:199]\n"
"buffer_load_dwordx4 v[96:99], v1, s[4:7], s98 offen sc1\n"
"v_mfma_f32_16x16x32_bf16 v[196:199], a[92:95], v[180:183], v[196:199]\n"
"ds_read_b128 v[172:175], v6 offset:2048\n"
"ds_read_b128 v[192:195], v6 offset:3072\n"
"v_mfma_f32_16x16x32_bf16 v[208:211], a[124:127], v[180:183], v[208:211]\n"
"v_add_f32_e32 v94, v94, v122\n"
"v_add_f32_e32 v95, v95, v123\n"
"v_mfma_f32_16x16x32_bf16 v[208:211], a[120:123], v[168:171], v[208:211]\n"
"v_add_f32_e32 v112, v112, v204\n"
"v_add_f32_e32 v113, v113, v205\n"
"v_mfma_f32_16x16x32_bf16 v[208:211], a[116:119], v[164:167], v[208:211]\n"
"s_add_u32 s97, s70, 0x2000\n"
"v_add_f32_e32 v114, v114, v206\n"
"v_add_f32_e32 v115, v115, v207\n"
"v_mfma_f32_16x16x32_bf16 v[208:211], a[112:115], v[136:139], v[208:211]\n"
"buffer_load_dwordx4 v[120:123], v1, s[4:7], s98 offen offset:1024 sc1\n"
"v_mfma_f32_16x16x32_bf16 v[208:211], a[108:111], v[148:151], v[208:211]\n"
"buffer_load_dwordx4 a[160:163], v0, s[4:7], s97 offen sc1\n"
"v_mfma_f32_16x16x32_bf16 v[208:211], a[104:107], v[140:143], v[208:211]\n"
"v_add_f32_e32 v200, v200, v212\n"
"v_add_f32_e32 v201, v201, v213\n"
"v_mfma_f32_16x16x32_bf16 v[208:211], a[100:103], v[124:127], v[208:211]\n"
"v_add_f32_e32 v202, v202, v214\n"
"v_add_f32_e32 v203, v203, v215\n"
"v_mfma_f32_16x16x32_bf16 v[208:211], a[96:99], v[68:71], v[208:211]\n"
"v_mul_f32_e32 v112, v112, v255\n"
"v_mul_f32_e32 v113, v113, v255\n"
"v_mfma_f32_16x16x32_bf16 v[84:87], v[24:27], v[68:71], v[84:87]\n"
"buffer_load_dwordx4 v[204:207], v1, s[4:7], s98 offen offset:2048 sc1\n"
"v_mfma_f32_16x16x32_bf16 v[84:87], v[56:59], v[124:127], v[84:87]\n"
"v_mul_f32_e32 v114, v114, v255\n"
"v_mul_f32_e32 v115, v115, v255\n"
"v_mfma_f32_16x16x32_bf16 v[84:87], v[28:31], v[140:143], v[84:87]\n"
"v_mul_f32_e32 v200, v200, v255\n"
"v_mul_f32_e32 v201, v201, v255\n"
"v_mfma_f32_16x16x32_bf16 v[84:87], v[64:67], v[148:151], v[84:87]\n"
"v_mul_f32_e32 v202, v202, v255\n"
"v_mul_f32_e32 v203, v203, v255\n"
"v_mfma_f32_16x16x32_bf16 v[84:87], v[52:55], v[136:139], v[84:87]\n"
"s_waitcnt lgkmcnt(0)\n"
"v_cvt_pk_bf16_f32 v212, v112, v113\n"
"v_cvt_pk_bf16_f32 v213, v114, v115\n"
"v_mfma_f32_16x16x32_bf16 v[84:87], v[36:39], v[164:167], v[84:87]\n"
"buffer_load_dwordx4 v[112:115], v1, s[4:7], s98 offen offset:3072 sc1\n"
"v_mfma_f32_16x16x32_bf16 v[84:87], v[108:111], v[168:171], v[84:87]\n"
"ds_read_b128 v[216:219], v6 offset:4096\n"
"ds_read_b128 v[220:223], v6 offset:5120\n"
"v_mfma_f32_16x16x32_bf16 v[84:87], v[60:63], v[180:183], v[84:87]\n"
"v_cvt_pk_bf16_f32 v214, v200, v201\n"
"v_cvt_pk_bf16_f32 v215, v202, v203\n"
"v_mfma_f32_16x16x32_bf16 v[92:95], v[80:83], v[180:183], v[92:95]\n"
"ds_write_b128 v8, v[212:215] offset:25600\n"
"v_mfma_f32_16x16x32_bf16 v[92:95], v[88:91], v[168:171], v[92:95]\n"
"v_add_f32_e32 v196, v196, v172\n"
"v_add_f32_e32 v197, v197, v173\n"
"v_mfma_f32_16x16x32_bf16 v[92:95], v[100:103], v[164:167], v[92:95]\n"
"v_add_f32_e32 v198, v198, v174\n"
"v_add_f32_e32 v199, v199, v175\n"
"v_mfma_f32_16x16x32_bf16 v[92:95], v[76:79], v[136:139], v[92:95]\n"
"v_add_f32_e32 v208, v208, v192\n"
"v_add_f32_e32 v209, v209, v193\n"
"v_mfma_f32_16x16x32_bf16 v[92:95], v[132:135], v[148:151], v[92:95]\n"
"v_add_f32_e32 v210, v210, v194\n"
"v_add_f32_e32 v211, v211, v195\n"
"v_mfma_f32_16x16x32_bf16 v[92:95], v[128:131], v[140:143], v[92:95]\n"
"v_mul_f32_e32 v196, v196, v255\n"
"v_mul_f32_e32 v197, v197, v255\n"
"v_mfma_f32_16x16x32_bf16 v[92:95], v[48:51], v[124:127], v[92:95]\n"
"v_mul_f32_e32 v198, v198, v255\n"
"v_mul_f32_e32 v199, v199, v255\n"
"v_mfma_f32_16x16x32_bf16 v[92:95], v[72:75], v[68:71], v[92:95]\n"
"v_mul_f32_e32 v208, v208, v255\n"
"v_mul_f32_e32 v209, v209, v255\n"
"v_mul_f32_e32 v210, v210, v255\n"
"v_mul_f32_e32 v211, v211, v255\n"
"v_cvt_pk_bf16_f32 v68, v196, v197\n"
"v_cvt_pk_bf16_f32 v69, v198, v199\n"
"v_cvt_pk_bf16_f32 v70, v208, v209\n"
"v_cvt_pk_bf16_f32 v71, v210, v211\n"
"ds_write_b128 v7, v[68:71] offset:58368\n"
".Lmk_a2:\n"
"s_waitcnt vmcnt(15)\n"
"; @A G1 2.0\n"
"v_mfma_f32_16x16x32_bf16 v[124:127], a[0:3], v[44:47], 0\n"
"v_mfma_f32_16x16x32_bf16 v[124:127], a[4:7], v[32:35], v[124:127]\n"
"s_add_u32 s98, s62, 0x800\n"
"v_mfma_f32_16x16x32_bf16 v[124:127], a[8:11], v[188:191], v[124:127]\n"
"s_add_u32 s96, s54, 0x8000\n"
"buffer_load_dwordx4 v[136:139], v0, s[4:7], s96 offen sc1\n"
"v_mfma_f32_16x16x32_bf16 v[124:127], a[12:15], v[144:147], v[124:127]\n"
"s_waitcnt vmcnt(11)\n"
"v_mfma_f32_16x16x32_bf16 v[124:127], a[16:19], v[152:155], v[124:127]\n"
"buffer_load_dword v255, v10, s[4:7], s98 offen sc1\n"
"v_mfma_f32_16x16x32_bf16 v[124:127], a[20:23], v[184:187], v[124:127]\n"
"s_waitcnt lgkmcnt(2)\n"
"v_add_f32_e32 v84, v84, v216\n"
"v_add_f32_e32 v85, v85, v217\n"
"v_mfma_f32_16x16x32_bf16 v[124:127], a[24:27], v[160:163], v[124:127]\n"
"v_add_f32_e32 v86, v86, v218\n"
"v_add_f32_e32 v87, v87, v219\n"
"v_mfma_f32_16x16x32_bf16 v[124:127], a[28:31], v[104:107], v[124:127]\n"
"buffer_load_dwordx4 v[140:143], v0, s[4:7], s96 offen offset:1024 sc1\n"
"v_mfma_f32_16x16x32_bf16 v[148:151], a[60:63], v[104:107], 0\n"
"buffer_load_dwordx4 a[164:167], v0, s[4:7], s97 offen offset:1024 sc1\n"
"v_mfma_f32_16x16x32_bf16 v[148:151], a[56:59], v[160:163], v[148:151]\n"
"buffer_load_dword v254, v10, s[4:7], s98 offen offset:256 sc1\n"
"v_mfma_f32_16x16x32_bf16 v[148:151], a[52:55], v[184:187], v[148:151]\n"
"v_add_f32_e32 v92, v92, v220\n"
"v_add_f32_e32 v93, v93, v221\n"
"v_mfma_f32_16x16x32_bf16 v[148:151], a[48:51], v[152:155], v[148:151]\n"
"v_add_f32_e32 v94, v94, v222\n"
"v_add_f32_e32 v95, v95, v223\n"
"v_mfma_f32_16x16x32_bf16 v[148:151], a[44:47], v[144:147], v[148:151]\n"
"buffer_load_dwordx4 v[164:167], v0, s[4:7], s96 offen offset:2048 sc1\n"
"v_mfma_f32_16x16x32_bf16 v[148:151], a[40:43], v[188:191], v[148:151]\n"
"ds_write_b128 v3, v[124:127]\n"
"v_mfma_f32_16x16x32_bf16 v[148:151], a[36:39], v[32:35], v[148:151]\n"
"buffer_load_dword v252, v10, s[4:7], s98 offen offset:512 sc1\n"
"v_mfma_f32_16x16x32_bf16 v[148:151], a[32:35], v[44:47], v[148:151]\n"
"v_mul_f32_e32 v84, v84, v253\n"
"v_mul_f32_e32 v85, v85, v253\n"
"v_mfma_f32_16x16x32_bf16 v[124:127], a[64:67], v[44:47], 0\n"
"v_mul_f32_e32 v86, v86, v253\n"
"v_mul_f32_e32 v87, v87, v253\n"
"v_mfma_f32_16x16x32_bf16 v[124:127], a[68:71], v[32:35], v[124:127]\n"
"buffer_load_dwordx4 v[168:171], v0, s[4:7], s96 offen offset:3072 sc1\n"
"v_mfma_f32_16x16x32_bf16 v[124:127], a[72:75], v[188:191], v[124:127]\n"
"buffer_load_dword v251, v10, s[4:7], s98 offen offset:768 sc1\n"
"v_mfma_f32_16x16x32_bf16 v[124:127], a[76:79], v[144:147], v[124:127]\n"
"v_mul_f32_e32 v92, v92, v253\n"
"v_mul_f32_e32 v93, v93, v253\n"
"v_mfma_f32_16x16x32_bf16 v[124:127], a[80:83], v[152:155], v[124:127]\n"
"v_mul_f32_e32 v94, v94, v253\n"
"v_mul_f32_e32 v95, v95, v253\n"
"v_mfma_f32_16x16x32_bf16 v[124:127], a[84:87], v[184:187], v[124:127]\n"
"ds_write_b128 v3, v[148:151] offset:1024\n"
"v_mfma_f32_16x16x32_bf16 v[124:127], a[88:91], v[160:163], v[124:127]\n"
"buffer_load_dwordx4 v[148:151], v1, s[4:7], s96 offen sc1\n"
"v_mfma_f32_16x16x32_bf16 v[124:127], a[92:95], v[104:107], v[124:127]\n"
"buffer_load_dword v253, v10, s[4:7], s98 offen offset:1024 sc1\n"
"v_mfma_f32_16x16x32_bf16 v[172:175], a[124:127], v[104:107], 0\n"
"v_cvt_pk_bf16_f32 v180, v84, v85\n"
"v_cvt_pk_bf16_f32 v181, v86, v87\n"
"v_mfma_f32_16x16x32_bf16 v[172:175], a[120:123], v[160:163], v[172:175]\n"
"v_cvt_pk_bf16_f32 v182, v92, v93\n"
"v_cvt_pk_bf16_f32 v183, v94, v95\n"
"v_mfma_f32_16x16x32_bf16 v[172:175], a[116:119], v[184:187], v[172:175]\n"
"buffer_load_dword v250, v10, s[4:7], s98 offen offset:1280 sc1\n"
"v_mfma_f32_16x16x32_bf16 v[172:175], a[112:115], v[152:155], v[172:175]\n"
"buffer_load_dwordx4 v[84:87], v1, s[4:7], s96 offen offset:1024 sc1\n"
"v_mfma_f32_16x16x32_bf16 v[172:175], a[108:111], v[144:147], v[172:175]\n"
"buffer_load_dwordx4 a[168:171], v0, s[4:7], s97 offen offset:2048 sc1\n"
"v_mfma_f32_16x16x32_bf16 v[172:175], a[104:107], v[188:191], v[172:175]\n"
"ds_write_b128 v3, v[124:127] offset:2048\n"
"v_mfma_f32_16x16x32_bf16 v[172:175], a[100:103], v[32:35], v[172:175]\n"
"buffer_load_dword v249, v10, s[4:7], s98 offen offset:1536 sc1\n"
"v_mfma_f32_16x16x32_bf16 v[172:175], a[96:99], v[44:47], v[172:175]\n"
"ds_write_b128 v2, v[180:183] offset:50176\n"
"v_mfma_f32_16x16x32_bf16 v[92:95], v[24:27], v[44:47], 0\n"
"buffer_load_dwordx4 v[124:127], v1, s[4:7], s96 offen offset:2048 sc1\n"
"s_add_u32 s95, s64, 0x2000\n"
"v_mfma_f32_16x16x32_bf16 v[92:95], v[56:59], v[32:35], v[92:95]\n"
"buffer_load_dword v248, v10, s[4:7], s98 offen offset:1792 sc1\n"
"v_mfma_f32_16x16x32_bf16 v[92:95], v[28:31], v[188:191], v[92:95]\n"
"buffer_store_dwordx4 v[212:215], v8, s[4:7], s95 offen offset:1024 sc1\n"
"v_mfma_f32_16x16x32_bf16 v[92:95], v[64:67], v[144:147], v[92:95]\n"
"v_mfma_f32_16x16x32_bf16 v[92:95], v[52:55], v[152:155], v[92:95]\n"
"v_mfma_f32_16x16x32_bf16 v[92:95], v[36:39], v[184:187], v[92:95]\n"
"buffer_load_dwordx4 v[180:183], v1, s[4:7], s96 offen offset:3072 sc1\n"
"v_mfma_f32_16x16x32_bf16 v[92:95], v[108:111], v[160:163], v[92:95]\n"
"ds_write_b128 v3, v[172:175] offset:3072\n"
"v_mfma_f32_16x16x32_bf16 v[92:95], v[60:63], v[104:107], v[92:95]\n"
"v_mfma_f32_16x16x32_bf16 v[172:175], v[80:83], v[104:107], 0\n"
"v_mfma_f32_16x16x32_bf16 v[172:175], v[88:91], v[160:163], v[172:175]\n"
"v_mfma_f32_16x16x32_bf16 v[172:175], v[100:103], v[184:187], v[172:175]\n"
"v_mfma_f32_16x16x32_bf16 v[172:175], v[76:79], v[152:155], v[172:175]\n"
"v_mfma_f32_16x16x32_bf16 v[172:175], v[132:135], v[144:147], v[172:175]\n"
"v_mfma_f32_16x16x32_bf16 v[172:175], v[128:131], v[188:191], v[172:175]\n"
"s_nop 1\n"
"ds_write_b128 v3, v[92:95] offset:4096\n"
"v_mfma_f32_16x16x32_bf16 v[172:175], v[48:51], v[32:35], v[172:175]\n"
"v_mfma_f32_16x16x32_bf16 v[172:175], v[72:75], v[44:47], v[172:175]\n"
"s_waitcnt vmcnt(24)\n"
"; @A G1 2.1\n"
"v_mfma_f32_16x16x32_bf16 v[32:35], a[0:3], v[156:159], 0\n"
"buffer_load_dwordx4 a[172:175], v0, s[4:7], s97 offen offset:3072 sc1\n"
"v_mfma_f32_16x16x32_bf16 v[32:35], a[4:7], v[40:43], v[32:35]\n"
"v_mfma_f32_16x16x32_bf16 v[32:35], a[8:11], v[116:119], v[32:35]\n"
"s_add_u32 s98, s55, 0x8000\n"
"buffer_load_dwordx4 v[44:47], v0, s[4:7], s98 offen sc1\n"
"v_mfma_f32_16x16x32_bf16 v[32:35], a[12:15], v[176:179], v[32:35]\n"
"s_waitcnt vmcnt(21)\n"
"v_mfma_f32_16x16x32_bf16 v[32:35], a[16:19], v[96:99], v[32:35]\n"
"v_mfma_f32_16x16x32_bf16 v[32:35], a[20:23], v[120:123], v[32:35]\n"
"ds_write_b128 v3, v[172:175] offset:5120\n"
"v_mfma_f32_16x16x32_bf16 v[32:35], a[24:27], v[204:207], v[32:35]\n"
"v_mfma_f32_16x16x32_bf16 v[32:35], a[28:31], v[112:115], v[32:35]\n"
"buffer_load_dwordx4 v[92:95], v0, s[4:7], s98 offen offset:1024 sc1\n"
"v_mfma_f32_16x16x32_bf16 v[104:107], a[60:63], v[112:115], 0\n"
"s_add_u32 s96, s64, 0x6000\n"
"v_mfma_f32_16x16x32_bf16 v[104:107], a[56:59], v[204:207], v[104:107]\n"
"v_mfma_f32_16x16x32_bf16 v[104:107], a[52:55], v[120:123], v[104:107]\n"
"buffer_store_dwordx4 v[68:71], v7, s[4:7], s96 offen offset:1024 sc1\n"
"v_mfma_f32_16x16x32_bf16 v[104:107], a[48:51], v[96:99], v[104:107]\n"
"v_mfma_f32_16x16x32_bf16 v[104:107], a[44:47], v[176:179], v[104:107]\n"
"buffer_load_dwordx4 v[68:71], v0, s[4:7], s98 offen offset:2048 sc1\n"
"v_mfma_f32_16x16x32_bf16 v[104:107], a[40:43], v[116:119], v[104:107]\n"
"ds_write_b128 v3, v[32:35] offset:6144\n"
"v_mfma_f32_16x16x32_bf16 v[104:107], a[36:39], v[40:43], v[104:107]\n"
"v_mfma_f32_16x16x32_bf16 v[104:107], a[32:35], v[156:159], v[104:107]\n"
"v_mfma_f32_16x16x32_bf16 v[32:35], a[64:67], v[156:159], 0\n"
"v_mfma_f32_16x16x32_bf16 v[32:35], a[68:71], v[40:43], v[32:35]\n"
"buffer_load_dwordx4 v[144:147], v0, s[4:7], s98 offen offset:3072 sc1\n"
"v_mfma_f32_16x16x32_bf16 v[32:35], a[72:75], v[116:119], v[32:35]\n"
"v_mfma_f32_16x16x32_bf16 v[32:35], a[76:79], v[176:179], v[32:35]\n"
"v_mfma_f32_16x16x32_bf16 v[32:35], a[80:83], v[96:99], v[32:35]\n"
"buffer_load_dwordx4 a[176:179], v1, s[4:7], s97 offen sc1\n"
"v_mfma_f32_16x16x32_bf16 v[32:35], a[84:87], v[120:123], v[32:35]\n"
"ds_write_b128 v3, v[104:107] offset:7168\n"
"v_mfma_f32_16x16x32_bf16 v[32:35], a[88:91], v[204:207], v[32:35]\n"
"buffer_load_dwordx4 v[104:107], v1, s[4:7], s98 offen sc1\n"
"v_mfma_f32_16x16x32_bf16 v[32:35], a[92:95], v[112:115], v[32:35]\n"
"v_mfma_f32_16x16x32_bf16 v[152:155], a[124:127], v[112:115], 0\n"
"v_mfma_f32_16x16x32_bf16 v[152:155], a[120:123], v[204:207], v[152:155]\n"
"v_mfma_f32_16x16x32_bf16 v[152:155], a[116:119], v[120:123], v[152:155]\n"
"v_mfma_f32_16x16x32_bf16 v[152:155], a[112:115], v[96:99], v[152:155]\n"
"buffer_load_dwordx4 v[160:163], v1, s[4:7], s98 offen offset:1024 sc1\n"
"v_mfma_f32_16x16x32_bf16 v[152:155], a[108:111], v[176:179], v[152:155]\n"
"v_mfma_f32_16x16x32_bf16 v[152:155], a[104:107], v[116:119], v[152:155]\n"
"s_nop 0\n"
"ds_write_b128 v3, v[32:35] offset:8192\n"
"v_mfma_f32_16x16x32_bf16 v[152:155], a[100:103], v[40:43], v[152:155]\n"
"v_mfma_f32_16x16x32_bf16 v[152:155], a[96:99], v[156:159], v[152:155]\n"
"v_mfma_f32_16x16x32_bf16 v[32:35], v[24:27], v[156:159], 0\n"
"buffer_load_dwordx4 v[172:175], v1, s[4:7], s98 offen offset:2048 sc1\n"
"v_mfma_f32_16x16x32_bf16 v[32:35], v[56:59], v[40:43], v[32:35]\n"
"v_mfma_f32_16x16x32_bf16 v[32:35], v[28:31], v[116:119], v[32:35]\n"
"v_mfma_f32_16x16x32_bf16 v[32:35], v[64:67], v[176:179], v[32:35]\n"
"v_mfma_f32_16x16x32_bf16 v[32:35], v[52:55], v[96:99], v[32:35]\n"
"s_waitcnt vmcnt(12)\n"
"v_add_f32_e32 v255, v255, v253\n"
"v_add_f32_e32 v254, v254, v250\n"
"v_mfma_f32_16x16x32_bf16 v[32:35], v[36:39], v[120:123], v[32:35]\n"
"buffer_load_dwordx4 v[184:187], v1, s[4:7], s98 offen offset:3072 sc1\n"
"v_mfma_f32_16x16x32_bf16 v[32:35], v[108:111], v[204:207], v[32:35]\n"
"ds_write_b128 v3, v[152:155] offset:9216\n"
"v_mfma_f32_16x16x32_bf16 v[32:35], v[60:63], v[112:115], v[32:35]\n"
"v_add_f32_e32 v252, v252, v249\n"
"v_add_f32_e32 v251, v251, v248\n"
"v_mfma_f32_16x16x32_bf16 v[152:155], v[80:83], v[112:115], 0\n"
"buffer_load_dwordx4 a[180:183], v1, s[4:7], s97 offen offset:1024 sc1\n"
"v_mfma_f32_16x16x32_bf16 v[152:155], v[88:91], v[204:207], v[152:155]\n"
"v_add_f32_e32 v255, v255, v252\n"
"v_add_f32_e32 v254, v254, v251\n"
"v_mfma_f32_16x16x32_bf16 v[152:155], v[100:103], v[120:123], v[152:155]\n"
"v_add_f32_e32 v255, v255, v254\n"
"v_mov_b32_e32 v254, v255\n"
"v_mfma_f32_16x16x32_bf16 v[152:155], v[76:79], v[96:99], v[152:155]\n"
"s_nop 0\n"
"v_permlane32_swap_b32_e32 v255, v254\n"
"v_add_f32_e32 v255, v255, v254\n"
"v_mfma_f32_16x16x32_bf16 v[152:155], v[132:135], v[176:179], v[152:155]\n"
"v_mul_f32_e32 v255, 0x3a800000, v255\n"
"v_mov_b32_e32 v254, v255\n"
"v_mfma_f32_16x16x32_bf16 v[152:155], v[128:131], v[116:119], v[152:155]\n"
"ds_write_b128 v3, v[32:35] offset:10240\n"
"v_mfma_f32_16x16x32_bf16 v[152:155], v[48:51], v[40:43], v[152:155]\n"
"v_permlane16_swap_b32_e32 v255, v254\n"
"v_cvt_pk_bf16_f32 v253, -v255, 0\n"
"v_mfma_f32_16x16x32_bf16 v[152:155], v[72:75], v[156:159], v[152:155]\n"
"v_fma_f32 v254, -v255, v255, v254\n"
"v_lshlrev_b32_e32 v253, 16, v253\n"
"; @A G1 2.2\n"
"v_mfma_f32_16x16x32_bf16 v[32:35], a[0:3], v[136:139], 0\n"
"v_max_f32_e32 v254, 0, v254\n"
"v_cvt_pk_bf16_f32 v254, v254, 0\n"
"v_mfma_f32_16x16x32_bf16 v[32:35], a[4:7], v[140:143], v[32:35]\n"
"v_lshlrev_b32_e32 v254, 16, v254\n"
"v_add_f32_e32 v254, 0x3727c5ac, v254\n"
"v_mfma_f32_16x16x32_bf16 v[32:35], a[8:11], v[164:167], v[32:35]\n"
"v_cvt_pk_bf16_f32 v254, v254, 0\n"
"v_lshlrev_b32_e32 v254, 16, v254\n"
"v_mfma_f32_16x16x32_bf16 v[32:35], a[12:15], v[168:171], v[32:35]\n"
"s_waitcnt vmcnt(12)\n"
"v_sqrt_f32_e32 v254, v254\n"
"v_mfma_f32_16x16x32_bf16 v[32:35], a[16:19], v[148:151], v[32:35]\n"
"s_mov_b32 s98, .Lmk_a4-.Lmk_g86\n"
"s_getpc_b64 s[84:85]\n"
".Lmk_g86:\n"
"s_add_u32 s84, s84, s98\n"
"s_addc_u32 s85, s85, 0\n"
"s_and_b32 s84, s84, -16\n"
"v_mfma_f32_16x16x32_bf16 v[32:35], a[20:23], v[84:87], v[32:35]\n"
"ds_write_b128 v3, v[152:155] offset:11264\n"
"v_mfma_f32_16x16x32_bf16 v[32:35], a[24:27], v[124:127], v[32:35]\n"
"s_and_b32 s85, s85, 0xffff\n"
"s_sub_u32 s98, .Lmk_end-.Lmk_g86, s98\n"
"s_min_u32 s86, s98, 0x4000\n"
"s_mov_b32 s87, 0x20000\n"
"v_mfma_f32_16x16x32_bf16 v[32:35], a[28:31], v[180:183], v[32:35]\n"
"s_mov_b32 m0, 0x27800\n"
"s_nop 0\n"
"buffer_load_dwordx4 v0, s[84:87], s99 offen lds\n"
"v_mfma_f32_16x16x32_bf16 v[40:43], a[60:63], v[180:183], 0\n"
"v_cvt_pk_bf16_f32 v254, v254, 0\n"
"v_lshlrev_b32_e32 v254, 16, v254\n"
"v_mfma_f32_16x16x32_bf16 v[40:43], a[56:59], v[124:127], v[40:43]\n"
"v_rcp_f32_e32 v255, v254\n"
"v_mfma_f32_16x16x32_bf16 v[40:43], a[52:55], v[84:87], v[40:43]\n"
"s_add_u32 s98, s52, 0\n"
"buffer_load_dwordx4 v[96:99], v0, s[4:7], s98 offen sc1\n"
"v_mfma_f32_16x16x32_bf16 v[40:43], a[48:51], v[148:151], v[40:43]\n"
"s_waitcnt lgkmcnt(0)\n"
"s_barrier\n"
"v_mfma_f32_16x16x32_bf16 v[40:43], a[44:47], v[168:171], v[40:43]\n"
"buffer_load_dwordx4 a[184:187], v1, s[4:7], s97 offen offset:2048 sc1\n"
"v_mfma_f32_16x16x32_bf16 v[40:43], a[40:43], v[164:167], v[40:43]\n"
"ds_write_b128 v3, v[32:35] offset:12288\n"
"v_mfma_f32_16x16x32_bf16 v[40:43], a[36:39], v[140:143], v[40:43]\n"
"ds_read_b128 v[32:35], v4\n"
"v_mul_f32_e32 v254, 0x3e8293ee, v255\n"
"v_mfma_f32_16x16x32_bf16 v[40:43], a[32:35], v[136:139], v[40:43]\n"
"buffer_load_dwordx4 v[112:115], v0, s[4:7], s98 offen offset:1024 sc1\n"
"v_mfma_f32_16x16x32_bf16 v[116:119], a[64:67], v[136:139], 0\n"
"ds_read_b128 v[120:123], v9 offset:128\n"
"ds_read_b128 v[152:155], v5\n"
"v_mfma_f32_16x16x32_bf16 v[116:119], a[68:71], v[140:143], v[116:119]\n"
"v_mfma_f32_16x16x32_bf16 v[116:119], a[72:75], v[164:167], v[116:119]\n"
"v_mfma_f32_16x16x32_bf16 v[116:119], a[76:79], v[168:171], v[116:119]\n"
"ds_read_b128 v[156:159], v4 offset:1024\n"
"ds_read_b128 v[176:179], v9 offset:192\n"
"v_mfma_f32_16x16x32_bf16 v[116:119], a[80:83], v[148:151], v[116:119]\n"
"buffer_load_dwordx4 v[188:191], v0, s[4:7], s98 offen offset:2048 sc1\n"
"v_mfma_f32_16x16x32_bf16 v[116:119], a[84:87], v[84:87], v[116:119]\n"
"ds_write_b128 v3, v[40:43] offset:13312\n"
"v_mfma_f32_16x16x32_bf16 v[116:119], a[88:91], v[124:127], v[116:119]\n"
"ds_read_b128 v[40:43], v5 offset:1024\n"
"s_waitcnt lgkmcnt(4)\n"
"v_mfma_f32_16x16x32_bf16 v[116:119], a[92:95], v[180:183], v[116:119]\n"
"v_fmac_f32_e32 v32, v253, v120\n"
"v_fmac_f32_e32 v33, v253, v121\n"
"v_mfma_f32_16x16x32_bf16 v[192:195], a[124:127], v[180:183], 0\n"
"v_fmac_f32_e32 v34, v253, v122\n"
"v_fmac_f32_e32 v35, v253, v123\n"
"v_mfma_f32_16x16x32_bf16 v[192:195], a[120:123], v[124:127], v[192:195]\n"
"buffer_load_dwordx4 v[120:123], v0, s[4:7], s98 offen offset:3072 sc1\n"
"v_mfma_f32_16x16x32_bf16 v[192:195], a[116:119], v[84:87], v[192:195]\n"
"ds_read_b128 v[196:199], v4 offset:2048\n"
"ds_read_b128 v[200:203], v9 offset:256\n"
"v_mfma_f32_16x16x32_bf16 v[192:195], a[112:115], v[148:151], v[192:195]\n"
"ds_read_b128 v[204:207], v5 offset:2048\n"
"v_add_f32_e32 v32, v32, v152\n"
"v_mfma_f32_16x16x32_bf16 v[192:195], a[108:111], v[168:171], v[192:195]\n"
"v_add_f32_e32 v33, v33, v153\n"
"v_add_f32_e32 v34, v34, v154\n"
"v_mfma_f32_16x16x32_bf16 v[192:195], a[104:107], v[164:167], v[192:195]\n"
"ds_write_b128 v3, v[116:119] offset:14336\n"
"v_mfma_f32_16x16x32_bf16 v[192:195], a[100:103], v[140:143], v[192:195]\n"
"buffer_load_dwordx4 v[116:119], v1, s[4:7], s98 offen sc1\n"
"v_mfma_f32_16x16x32_bf16 v[192:195], a[96:99], v[136:139], v[192:195]\n"
"s_waitcnt lgkmcnt(4)\n"
"v_add_f32_e32 v35, v35, v155\n"
"v_fmac_f32_e32 v156, v253, v176\n"
"v_mfma_f32_16x16x32_bf16 v[152:155], v[24:27], v[136:139], 0\n"
"buffer_load_dwordx4 a[188:191], v1, s[4:7], s97 offen offset:3072 sc1\n"
"v_mfma_f32_16x16x32_bf16 v[152:155], v[56:59], v[140:143], v[152:155]\n"
"ds_read_b128 v[208:211], v4 offset:3072\n"
"ds_read_b128 v[212:215], v9 offset:320\n"
"v_mfma_f32_16x16x32_bf16 v[152:155], v[28:31], v[164:167], v[152:155]\n"
"s_waitcnt vmcnt(16)\n"
"s_add_u32 s97, s68, 0x1000\n"
"ds_read_b128 v[216:219], v5 offset:3072\n"
"v_mfma_f32_16x16x32_bf16 v[152:155], v[64:67], v[168:171], v[152:155]\n"
"buffer_load_dwordx4 v[220:223], v1, s[4:7], s98 offen offset:1024 sc1\n"
"v_mfma_f32_16x16x32_bf16 v[152:155], v[52:55], v[148:151], v[152:155]\n"
"buffer_store_dwordx2 v[18:19], v11, s[4:7], s97 offen offset:64 sc1\n"
"v_mfma_f32_16x16x32_bf16 v[152:155], v[36:39], v[84:87], v[152:155]\n"
"ds_write_b128 v3, v[192:195] offset:15360\n"
"v_mfma_f32_16x16x32_bf16 v[152:155], v[108:111], v[124:127], v[152:155]\n"
"v_fmac_f32_e32 v157, v253, v177\n"
"v_fmac_f32_e32 v158, v253, v178\n"
"v_mfma_f32_16x16x32_bf16 v[152:155], v[60:63], v[180:183], v[152:155]\n"
"v_fmac_f32_e32 v159, v253, v179\n"
"v_add_f32_e32 v156, v156, v40\n"
"v_mfma_f32_16x16x32_bf16 v[176:179], v[80:83], v[180:183], 0\n"
"buffer_load_dwordx4 v[180:183], v1, s[4:7], s98 offen offset:2048 sc1\n"
"v_mfma_f32_16x16x32_bf16 v[176:179], v[88:91], v[124:127], v[176:179]\n"
"ds_read_b128 v[124:127], v4 offset:4096\n"
"ds_read_b128 v[192:195], v9\n"
"v_mfma_f32_16x16x32_bf16 v[176:179], v[100:103], v[84:87], v[176:179]\n"
"ds_read_b128 v[84:87], v5 offset:4096\n"
"v_add_f32_e32 v157, v157, v41\n"
"v_mfma_f32_16x16x32_bf16 v[176:179], v[76:79], v[148:151], v[176:179]\n"
"s_waitcnt lgkmcnt(8)\n"
"v_add_f32_e32 v158, v158, v42\n"
"v_add_f32_e32 v159, v159, v43\n"
"v_mfma_f32_16x16x32_bf16 v[176:179], v[132:135], v[168:171], v[176:179]\n"
"v_fmac_f32_e32 v196, v253, v200\n"
"v_fmac_f32_e32 v197, v253, v201\n"
"v_mfma_f32_16x16x32_bf16 v[176:179], v[128:131], v[164:167], v[176:179]\n"
"buffer_load_dwordx4 v[40:43], v1, s[4:7], s98 offen offset:3072 sc1\n"
"v_mfma_f32_16x16x32_bf16 v[176:179], v[48:51], v[140:143], v[176:179]\n"
"ds_write_b128 v3, v[152:155] offset:16384\n"
"v_mfma_f32_16x16x32_bf16 v[176:179], v[72:75], v[136:139], v[176:179]\n"
"s_waitcnt vmcnt(18)\n"
"ds_read_b128 v[136:139], v4 offset:5120\n"
"ds_read_b128 v[140:143], v9 offset:64\n"
"; @A G1 2.3\n"
"v_mfma_f32_16x16x32_bf16 v[32:35], a[0:3], v[44:47], v[32:35]\n"
"ds_read_b128 v[148:151], v5 offset:5120\n"
"v_fmac_f32_e32 v198, v253, v202\n"
"v_mfma_f32_16x16x32_bf16 v[32:35], a[4:7], v[92:95], v[32:35]\n"
"v_fmac_f32_e32 v199, v253, v203\n"
"v_add_f32_e32 v196, v196, v204\n"
"v_mfma_f32_16x16x32_bf16 v[32:35], a[8:11], v[68:71], v[32:35]\n"
"s_add_u32 s98, s53, 0\n"
"buffer_load_dwordx4 v[152:155], v0, s[4:7], s98 offen sc1\n"
"v_mfma_f32_16x16x32_bf16 v[32:35], a[12:15], v[144:147], v[32:35]\n"
"s_waitcnt vmcnt(14)\n"
"s_add_u32 s97, s70, 0x4000\n"
"v_add_f32_e32 v197, v197, v205\n"
"v_mfma_f32_16x16x32_bf16 v[32:35], a[16:19], v[104:107], v[32:35]\n"
"buffer_load_dwordx4 a[192:195], v0, s[4:7], s97 offen sc1\n"
"v_mfma_f32_16x16x32_bf16 v[32:35], a[20:23], v[160:163], v[32:35]\n"
"ds_write_b128 v3, v[176:179] offset:17408\n"
"v_mfma_f32_16x16x32_bf16 v[32:35], a[24:27], v[172:175], v[32:35]\n"
"s_waitcnt lgkmcnt(9)\n"
"v_add_f32_e32 v198, v198, v206\n"
"v_add_f32_e32 v199, v199, v207\n"
"v_mfma_f32_16x16x32_bf16 v[32:35], a[28:31], v[184:187], v[32:35]\n"
"buffer_load_dwordx4 v[164:167], v0, s[4:7], s98 offen offset:1024 sc1\n"
"v_mfma_f32_16x16x32_bf16 v[156:159], a[60:63], v[184:187], v[156:159]\n"
"v_fmac_f32_e32 v208, v253, v212\n"
"v_fmac_f32_e32 v209, v253, v213\n"
"v_mfma_f32_16x16x32_bf16 v[156:159], a[56:59], v[172:175], v[156:159]\n"
"v_fmac_f32_e32 v210, v253, v214\n"
"v_fmac_f32_e32 v211, v253, v215\n"
"v_mfma_f32_16x16x32_bf16 v[156:159], a[52:55], v[160:163], v[156:159]\n"
"s_waitcnt lgkmcnt(0)\n"
"s_barrier\n"
"v_mfma_f32_16x16x32_bf16 v[156:159], a[48:51], v[104:107], v[156:159]\n"
"ds_read_b128 v[168:171], v6\n"
"ds_read_b128 v[176:179], v6 offset:1024\n"
"v_mfma_f32_16x16x32_bf16 v[156:159], a[44:47], v[144:147], v[156:159]\n"
"buffer_load_dwordx4 v[200:203], v0, s[4:7], s98 offen offset:2048 sc1\n"
"v_mfma_f32_16x16x32_bf16 v[156:159], a[40:43], v[68:71], v[156:159]\n"
"v_add_f32_e32 v208, v208, v216\n"
"v_add_f32_e32 v209, v209, v217\n"
"v_mfma_f32_16x16x32_bf16 v[156:159], a[36:39], v[92:95], v[156:159]\n"
"v_add_f32_e32 v210, v210, v218\n"
"v_add_f32_e32 v211, v211, v219\n"
"v_mfma_f32_16x16x32_bf16 v[156:159], a[32:35], v[44:47], v[156:159]\n"
"v_fmac_f32_e32 v124, v253, v192\n"
"v_fmac_f32_e32 v125, v253, v193\n"
"v_mfma_f32_16x16x32_bf16 v[196:199], a[64:67], v[44:47], v[196:199]\n"
"v_fmac_f32_e32 v126, v253, v194\n"
"v_fmac_f32_e32 v127, v253, v195\n"
"v_mfma_f32_16x16x32_bf16 v[196:199], a[68:71], v[92:95], v[196:199]\n"
"buffer_load_dwordx4 v[192:195], v0, s[4:7], s98 offen offset:3072 sc1\n"
"v_mfma_f32_16x16x32_bf16 v[196:199], a[72:75], v[68:71], v[196:199]\n"
"v_add_f32_e32 v124, v124, v84\n"
"v_add_f32_e32 v125, v125, v85\n"
"v_mfma_f32_16x16x32_bf16 v[196:199], a[76:79], v[144:147], v[196:199]\n"
"v_add_f32_e32 v126, v126, v86\n"
"v_add_f32_e32 v127, v127, v87\n"
"v_mfma_f32_16x16x32_bf16 v[196:199], a[80:83], v[104:107], v[196:199]\n"
"s_waitcnt lgkmcnt(0)\n"
"v_fmac_f32_e32 v136, v253, v140\n"
"v_fmac_f32_e32 v137, v253, v141\n"
"v_mfma_f32_16x16x32_bf16 v[196:199], a[84:87], v[160:163], v[196:199]\n"
"v_fmac_f32_e32 v138, v253, v142\n"
"v_fmac_f32_e32 v139, v253, v143\n"
"v_mfma_f32_16x16x32_bf16 v[196:199], a[88:91], v[172:175], v[196:199]\n"
"buffer_load_dwordx4 v[84:87], v1, s[4:7], s98 offen sc1\n"
"v_mfma_f32_16x16x32_bf16 v[196:199], a[92:95], v[184:187], v[196:199]\n"
"ds_read_b128 v[140:143], v6 offset:2048\n"
"ds_read_b128 v[204:207], v6 offset:3072\n"
"v_mfma_f32_16x16x32_bf16 v[208:211], a[124:127], v[184:187], v[208:211]\n"
"buffer_load_dwordx4 a[196:199], v0, s[4:7], s97 offen offset:1024 sc1\n"
"v_mfma_f32_16x16x32_bf16 v[208:211], a[120:123], v[172:175], v[208:211]\n"
"v_add_f32_e32 v136, v136, v148\n"
"v_add_f32_e32 v137, v137, v149\n"
"v_mfma_f32_16x16x32_bf16 v[208:211], a[116:119], v[160:163], v[208:211]\n"
"v_add_f32_e32 v138, v138, v150\n"
"v_add_f32_e32 v139, v139, v151\n"
"v_mfma_f32_16x16x32_bf16 v[208:211], a[112:115], v[104:107], v[208:211]\n"
"buffer_load_dwordx4 v[148:151], v1, s[4:7], s98 offen offset:1024 sc1\n"
"v_mfma_f32_16x16x32_bf16 v[208:211], a[108:111], v[144:147], v[208:211]\n"
"v_add_f32_e32 v32, v32, v168\n"
"v_add_f32_e32 v33, v33, v169\n"
"v_mfma_f32_16x16x32_bf16 v[208:211], a[104:107], v[68:71], v[208:211]\n"
"v_add_f32_e32 v34, v34, v170\n"
"v_add_f32_e32 v35, v35, v171\n"
"v_mfma_f32_16x16x32_bf16 v[208:211], a[100:103], v[92:95], v[208:211]\n"
"v_add_f32_e32 v156, v156, v176\n"
"v_add_f32_e32 v157, v157, v177\n"
"v_mfma_f32_16x16x32_bf16 v[208:211], a[96:99], v[44:47], v[208:211]\n"
"v_add_f32_e32 v158, v158, v178\n"
"v_add_f32_e32 v159, v159, v179\n"
"v_mfma_f32_16x16x32_bf16 v[124:127], v[24:27], v[44:47], v[124:127]\n"
"buffer_load_dwordx4 v[168:171], v1, s[4:7], s98 offen offset:2048 sc1\n"
"v_mfma_f32_16x16x32_bf16 v[124:127], v[56:59], v[92:95], v[124:127]\n"
"v_mul_f32_e32 v32, v32, v255\n"
"v_mul_f32_e32 v33, v33, v255\n"
"v_mfma_f32_16x16x32_bf16 v[124:127], v[28:31], v[68:71], v[124:127]\n"
"v_mul_f32_e32 v34, v34, v255\n"
"v_mul_f32_e32 v35, v35, v255\n"
"v_mfma_f32_16x16x32_bf16 v[124:127], v[64:67], v[144:147], v[124:127]\n"
"v_mul_f32_e32 v156, v156, v255\n"
"v_mul_f32_e32 v157, v157, v255\n"
"v_mfma_f32_16x16x32_bf16 v[124:127], v[52:55], v[104:107], v[124:127]\n"
"s_waitcnt lgkmcnt(0)\n"
"v_mul_f32_e32 v158, v158, v255\n"
"v_mul_f32_e32 v159, v159, v255\n"
"v_mfma_f32_16x16x32_bf16 v[124:127], v[36:39], v[160:163], v[124:127]\n"
"buffer_load_dwordx4 v[176:179], v1, s[4:7], s98 offen offset:3072 sc1\n"
"v_mfma_f32_16x16x32_bf16 v[124:127], v[108:111], v[172:175], v[124:127]\n"
"ds_read_b128 v[212:215], v6 offset:4096\n"
"ds_read_b128 v[216:219], v6 offset:5120\n"
"v_mfma_f32_16x16x32_bf16 v[124:127], v[60:63], v[184:187], v[124:127]\n"
"v_cvt_pk_bf16_f32 v224, v32, v33\n"
"v_cvt_pk_bf16_f32 v225, v34, v35\n"
"v_mfma_f32_16x16x32_bf16 v[136:139], v[80:83], v[184:187], v[136:139]\n"
"v_cvt_pk_bf16_f32 v226, v156, v157\n"
"v_cvt_pk_bf16_f32 v227, v158, v159\n"
"v_mfma_f32_16x16x32_bf16 v[136:139], v[88:91], v[172:175], v[136:139]\n"
"ds_write_b128 v8, v[224:227] offset:17408\n"
"v_mfma_f32_16x16x32_bf16 v[136:139], v[100:103], v[160:163], v[136:139]\n"
"v_add_f32_e32 v196, v196, v140\n"
"v_add_f32_e32 v197, v197, v141\n"
"v_mfma_f32_16x16x32_bf16 v[136:139], v[76:79], v[104:107], v[136:139]\n"
"v_add_f32_e32 v198, v198, v142\n"
"v_add_f32_e32 v199, v199, v143\n"
"v_mfma_f32_16x16x32_bf16 v[136:139], v[132:135], v[144:147], v[136:139]\n"
"v_add_f32_e32 v208, v208, v204\n"
"v_add_f32_e32 v209, v209, v205\n"
"v_mfma_f32_16x16x32_bf16 v[136:139], v[128:131], v[68:71], v[136:139]\n"
"v_add_f32_e32 v210, v210, v206\n"
"v_add_f32_e32 v211, v211, v207\n"
"v_mfma_f32_16x16x32_bf16 v[136:139], v[48:51], v[92:95], v[136:139]\n"
"v_mul_f32_e32 v196, v196, v255\n"
"v_mul_f32_e32 v197, v197, v255\n"
"v_mfma_f32_16x16x32_bf16 v[136:139], v[72:75], v[44:47], v[136:139]\n"
"v_mul_f32_e32 v198, v198, v255\n"
"v_mul_f32_e32 v199, v199, v255\n"
"v_mul_f32_e32 v208, v208, v255\n"
"v_mul_f32_e32 v209, v209, v255\n"
"v_mul_f32_e32 v210, v210, v255\n"
"v_mul_f32_e32 v211, v211, v255\n"
"v_cvt_pk_bf16_f32 v32, v196, v197\n"
"v_cvt_pk_bf16_f32 v33, v198, v199\n"
"v_cvt_pk_bf16_f32 v34, v208, v209\n"
"v_cvt_pk_bf16_f32 v35, v210, v211\n"
"ds_write_b128 v7, v[32:35] offset:50176\n"
".Lmk_a3:\n"
"s_waitcnt vmcnt(16)\n"
"; @A G1 3.0\n"
"v_mfma_f32_16x16x32_bf16 v[44:47], a[0:3], v[96:99], 0\n"
"v_mfma_f32_16x16x32_bf16 v[44:47], a[4:7], v[112:115], v[44:47]\n"
"s_add_u32 s98, s62, 0\n"
"v_mfma_f32_16x16x32_bf16 v[44:47], a[8:11], v[188:191], v[44:47]\n"
"s_add_u32 s96, s54, 0\n"
"buffer_load_dwordx4 v[68:71], v0, s[4:7], s96 offen sc1\n"
"v_mfma_f32_16x16x32_bf16 v[44:47], a[12:15], v[120:123], v[44:47]\n"
"s_waitcnt vmcnt(11)\n"
"v_mfma_f32_16x16x32_bf16 v[44:47], a[16:19], v[116:119], v[44:47]\n"
"buffer_load_dword v255, v10, s[4:7], s98 offen sc1\n"
"v_mfma_f32_16x16x32_bf16 v[44:47], a[20:23], v[220:223], v[44:47]\n"
"s_waitcnt lgkmcnt(2)\n"
"v_add_f32_e32 v124, v124, v212\n"
"v_add_f32_e32 v125, v125, v213\n"
"v_mfma_f32_16x16x32_bf16 v[44:47], a[24:27], v[180:183], v[44:47]\n"
"v_add_f32_e32 v126, v126, v214\n"
"v_add_f32_e32 v127, v127, v215\n"
"v_mfma_f32_16x16x32_bf16 v[44:47], a[28:31], v[40:43], v[44:47]\n"
"buffer_load_dwordx4 v[92:95], v0, s[4:7], s96 offen offset:1024 sc1\n"
"v_mfma_f32_16x16x32_bf16 v[104:107], a[60:63], v[40:43], 0\n"
"buffer_load_dwordx4 a[200:203], v0, s[4:7], s97 offen offset:2048 sc1\n"
"v_mfma_f32_16x16x32_bf16 v[104:107], a[56:59], v[180:183], v[104:107]\n"
"buffer_load_dword v253, v10, s[4:7], s98 offen offset:256 sc1\n"
"v_mfma_f32_16x16x32_bf16 v[104:107], a[52:55], v[220:223], v[104:107]\n"
"v_add_f32_e32 v136, v136, v216\n"
"v_add_f32_e32 v137, v137, v217\n"
"v_mfma_f32_16x16x32_bf16 v[104:107], a[48:51], v[116:119], v[104:107]\n"
"v_add_f32_e32 v138, v138, v218\n"
"v_add_f32_e32 v139, v139, v219\n"
"v_mfma_f32_16x16x32_bf16 v[104:107], a[44:47], v[120:123], v[104:107]\n"
"buffer_load_dwordx4 v[140:143], v0, s[4:7], s96 offen offset:2048 sc1\n"
"v_mfma_f32_16x16x32_bf16 v[104:107], a[40:43], v[188:191], v[104:107]\n"
"ds_write_b128 v3, v[44:47]\n"
"v_mfma_f32_16x16x32_bf16 v[104:107], a[36:39], v[112:115], v[104:107]\n"
"buffer_load_dword v252, v10, s[4:7], s98 offen offset:512 sc1\n"
"v_mfma_f32_16x16x32_bf16 v[104:107], a[32:35], v[96:99], v[104:107]\n"
"v_mul_f32_e32 v124, v124, v254\n"
"v_mul_f32_e32 v125, v125, v254\n"
"v_mfma_f32_16x16x32_bf16 v[44:47], a[64:67], v[96:99], 0\n"
"v_mul_f32_e32 v126, v126, v254\n"
"v_mul_f32_e32 v127, v127, v254\n"
"v_mfma_f32_16x16x32_bf16 v[44:47], a[68:71], v[112:115], v[44:47]\n"
"buffer_load_dwordx4 v[144:147], v0, s[4:7], s96 offen offset:3072 sc1\n"
"v_mfma_f32_16x16x32_bf16 v[44:47], a[72:75], v[188:191], v[44:47]\n"
"buffer_load_dword v251, v10, s[4:7], s98 offen offset:768 sc1\n"
"v_mfma_f32_16x16x32_bf16 v[44:47], a[76:79], v[120:123], v[44:47]\n"
"v_mul_f32_e32 v136, v136, v254\n"
"v_mul_f32_e32 v137, v137, v254\n"
"v_mfma_f32_16x16x32_bf16 v[44:47], a[80:83], v[116:119], v[44:47]\n"
"v_mul_f32_e32 v138, v138, v254\n"
"v_mul_f32_e32 v139, v139, v254\n"
"v_mfma_f32_16x16x32_bf16 v[44:47], a[84:87], v[220:223], v[44:47]\n"
"ds_write_b128 v3, v[104:107] offset:1024\n"
"v_mfma_f32_16x16x32_bf16 v[44:47], a[88:91], v[180:183], v[44:47]\n"
"buffer_load_dwordx4 v[104:107], v1, s[4:7], s96 offen sc1\n"
"v_mfma_f32_16x16x32_bf16 v[44:47], a[92:95], v[40:43], v[44:47]\n"
"buffer_load_dword v254, v10, s[4:7], s98 offen offset:1024 sc1\n"
"v_mfma_f32_16x16x32_bf16 v[156:159], a[124:127], v[40:43], 0\n"
"v_cvt_pk_bf16_f32 v160, v124, v125\n"
"v_cvt_pk_bf16_f32 v161, v126, v127\n"
"v_mfma_f32_16x16x32_bf16 v[156:159], a[120:123], v[180:183], v[156:159]\n"
"v_cvt_pk_bf16_f32 v162, v136, v137\n"
"v_cvt_pk_bf16_f32 v163, v138, v139\n"
"v_mfma_f32_16x16x32_bf16 v[156:159], a[116:119], v[220:223], v[156:159]\n"
"buffer_load_dwordx4 a[204:207], v0, s[4:7], s97 offen offset:3072 sc1\n"
"v_mfma_f32_16x16x32_bf16 v[156:159], a[112:115], v[116:119], v[156:159]\n"
"buffer_load_dwordx4 v[124:127], v1, s[4:7], s96 offen offset:1024 sc1\n"
"v_mfma_f32_16x16x32_bf16 v[156:159], a[108:111], v[120:123], v[156:159]\n"
"buffer_load_dword v250, v10, s[4:7], s98 offen offset:1280 sc1\n"
"v_mfma_f32_16x16x32_bf16 v[156:159], a[104:107], v[188:191], v[156:159]\n"
"ds_write_b128 v3, v[44:47] offset:2048\n"
"v_mfma_f32_16x16x32_bf16 v[156:159], a[100:103], v[112:115], v[156:159]\n"
"ds_write_b128 v2, v[160:163] offset:17408\n"
"v_mfma_f32_16x16x32_bf16 v[156:159], a[96:99], v[96:99], v[156:159]\n"
"buffer_load_dword v249, v10, s[4:7], s98 offen offset:1536 sc1\n"
"v_mfma_f32_16x16x32_bf16 v[44:47], v[24:27], v[96:99], 0\n"
"buffer_load_dwordx4 v[136:139], v1, s[4:7], s96 offen offset:2048 sc1\n"
"v_mfma_f32_16x16x32_bf16 v[44:47], v[56:59], v[112:115], v[44:47]\n"
"buffer_store_dwordx4 v[224:227], v8, s[4:7], s64 offen offset:1024 sc1\n"
"v_mfma_f32_16x16x32_bf16 v[44:47], v[28:31], v[188:191], v[44:47]\n"
"buffer_load_dword v248, v10, s[4:7], s98 offen offset:1792 sc1\n"
"v_mfma_f32_16x16x32_bf16 v[44:47], v[64:67], v[120:123], v[44:47]\n"
"v_mfma_f32_16x16x32_bf16 v[44:47], v[52:55], v[116:119], v[44:47]\n"
"v_mfma_f32_16x16x32_bf16 v[44:47], v[36:39], v[220:223], v[44:47]\n"
"buffer_load_dwordx4 v[160:163], v1, s[4:7], s96 offen offset:3072 sc1\n"
"v_mfma_f32_16x16x32_bf16 v[44:47], v[108:111], v[180:183], v[44:47]\n"
"ds_write_b128 v3, v[156:159] offset:3072\n"
"v_mfma_f32_16x16x32_bf16 v[44:47], v[60:63], v[40:43], v[44:47]\n"
"v_mfma_f32_16x16x32_bf16 v[156:159], v[80:83], v[40:43], 0\n"
"v_mfma_f32_16x16x32_bf16 v[156:159], v[88:91], v[180:183], v[156:159]\n"
"v_mfma_f32_16x16x32_bf16 v[156:159], v[100:103], v[220:223], v[156:159]\n"
"v_mfma_f32_16x16x32_bf16 v[156:159], v[76:79], v[116:119], v[156:159]\n"
"v_mfma_f32_16x16x32_bf16 v[156:159], v[132:135], v[120:123], v[156:159]\n"
"buffer_load_dwordx4 a[208:211], v1, s[4:7], s97 offen sc1\n"
"v_mfma_f32_16x16x32_bf16 v[156:159], v[128:131], v[188:191], v[156:159]\n"
"s_nop 0\n"
"ds_write_b128 v3, v[44:47] offset:4096\n"
"v_mfma_f32_16x16x32_bf16 v[156:159], v[48:51], v[112:115], v[156:159]\n"
"v_mfma_f32_16x16x32_bf16 v[156:159], v[72:75], v[96:99], v[156:159]\n"
"s_waitcnt vmcnt(25)\n"
"; @A G1 3.1\n"
"v_mfma_f32_16x16x32_bf16 v[40:43], a[0:3], v[152:155], 0\n"
"v_mfma_f32_16x16x32_bf16 v[40:43], a[4:7], v[164:167], v[40:43]\n"
"v_mfma_f32_16x16x32_bf16 v[40:43], a[8:11], v[200:203], v[40:43]\n"
"s_add_u32 s98, s55, 0\n"
"buffer_load_dwordx4 v[44:47], v0, s[4:7], s98 offen sc1\n"
"v_mfma_f32_16x16x32_bf16 v[40:43], a[12:15], v[192:195], v[40:43]\n"
"s_waitcnt vmcnt(21)\n"
"v_mfma_f32_16x16x32_bf16 v[40:43], a[16:19], v[84:87], v[40:43]\n"
"v_mfma_f32_16x16x32_bf16 v[40:43], a[20:23], v[148:151], v[40:43]\n"
"ds_write_b128 v3, v[156:159] offset:5120\n"
"v_mfma_f32_16x16x32_bf16 v[40:43], a[24:27], v[168:171], v[40:43]\n"
"v_mfma_f32_16x16x32_bf16 v[40:43], a[28:31], v[176:179], v[40:43]\n"
"buffer_load_dwordx4 v[96:99], v0, s[4:7], s98 offen offset:1024 sc1\n"
"s_add_u32 s96, s64, 0x4000\n"
"v_mfma_f32_16x16x32_bf16 v[112:115], a[60:63], v[176:179], 0\n"
"v_mfma_f32_16x16x32_bf16 v[112:115], a[56:59], v[168:171], v[112:115]\n"
"buffer_store_dwordx4 v[32:35], v7, s[4:7], s96 offen offset:1024 sc1\n"
"v_mfma_f32_16x16x32_bf16 v[112:115], a[52:55], v[148:151], v[112:115]\n"
"v_mfma_f32_16x16x32_bf16 v[112:115], a[48:51], v[84:87], v[112:115]\n"
"v_mfma_f32_16x16x32_bf16 v[112:115], a[44:47], v[192:195], v[112:115]\n"
"buffer_load_dwordx4 v[32:35], v0, s[4:7], s98 offen offset:2048 sc1\n"
"v_mfma_f32_16x16x32_bf16 v[112:115], a[40:43], v[200:203], v[112:115]\n"
"ds_write_b128 v3, v[40:43] offset:6144\n"
"v_mfma_f32_16x16x32_bf16 v[112:115], a[36:39], v[164:167], v[112:115]\n"
"buffer_load_dwordx4 a[212:215], v1, s[4:7], s97 offen offset:1024 sc1\n"
"v_mfma_f32_16x16x32_bf16 v[112:115], a[32:35], v[152:155], v[112:115]\n"
"v_mfma_f32_16x16x32_bf16 v[40:43], a[64:67], v[152:155], 0\n"
"v_mfma_f32_16x16x32_bf16 v[40:43], a[68:71], v[164:167], v[40:43]\n"
"buffer_load_dwordx4 v[116:119], v0, s[4:7], s98 offen offset:3072 sc1\n"
"v_mfma_f32_16x16x32_bf16 v[40:43], a[72:75], v[200:203], v[40:43]\n"
"v_mfma_f32_16x16x32_bf16 v[40:43], a[76:79], v[192:195], v[40:43]\n"
"v_mfma_f32_16x16x32_bf16 v[40:43], a[80:83], v[84:87], v[40:43]\n"
"v_mfma_f32_16x16x32_bf16 v[40:43], a[84:87], v[148:151], v[40:43]\n"
"s_nop 0\n"
"ds_write_b128 v3, v[112:115] offset:7168\n"
"v_mfma_f32_16x16x32_bf16 v[40:43], a[88:91], v[168:171], v[40:43]\n"
"buffer_load_dwordx4 v[112:115], v1, s[4:7], s98 offen sc1\n"
"v_mfma_f32_16x16x32_bf16 v[40:43], a[92:95], v[176:179], v[40:43]\n"
"v_mfma_f32_16x16x32_bf16 v[120:123], a[124:127], v[176:179], 0\n"
"v_mfma_f32_16x16x32_bf16 v[120:123], a[120:123], v[168:171], v[120:123]\n"
"v_mfma_f32_16x16x32_bf16 v[120:123], a[116:119], v[148:151], v[120:123]\n"
"v_mfma_f32_16x16x32_bf16 v[120:123], a[112:115], v[84:87], v[120:123]\n"
"buffer_load_dwordx4 v[156:159], v1, s[4:7], s98 offen offset:1024 sc1\n"
"v_mfma_f32_16x16x32_bf16 v[120:123], a[108:111], v[192:195], v[120:123]\n"
"v_mfma_f32_16x16x32_bf16 v[120:123], a[104:107], v[200:203], v[120:123]\n"
"s_nop 0\n"
"ds_write_b128 v3, v[40:43] offset:8192\n"
"v_mfma_f32_16x16x32_bf16 v[120:123], a[100:103], v[164:167], v[120:123]\n"
"v_mfma_f32_16x16x32_bf16 v[120:123], a[96:99], v[152:155], v[120:123]\n"
"v_mfma_f32_16x16x32_bf16 v[40:43], v[24:27], v[152:155], 0\n"
"buffer_load_dwordx4 v[172:175], v1, s[4:7], s98 offen offset:2048 sc1\n"
"v_mfma_f32_16x16x32_bf16 v[40:43], v[56:59], v[164:167], v[40:43]\n"
"buffer_load_dwordx4 a[216:219], v1, s[4:7], s97 offen offset:2048 sc1\n"
"v_mfma_f32_16x16x32_bf16 v[40:43], v[28:31], v[200:203], v[40:43]\n"
"v_mfma_f32_16x16x32_bf16 v[40:43], v[64:67], v[192:195], v[40:43]\n"
"v_mfma_f32_16x16x32_bf16 v[40:43], v[52:55], v[84:87], v[40:43]\n"
"s_waitcnt vmcnt(12)\n"
"v_add_f32_e32 v255, v255, v254\n"
"v_add_f32_e32 v253, v253, v250\n"
"v_mfma_f32_16x16x32_bf16 v[40:43], v[36:39], v[148:151], v[40:43]\n"
"buffer_load_dwordx4 v[180:183], v1, s[4:7], s98 offen offset:3072 sc1\n"
"v_mfma_f32_16x16x32_bf16 v[40:43], v[108:111], v[168:171], v[40:43]\n"
"ds_write_b128 v3, v[120:123] offset:9216\n"
"v_mfma_f32_16x16x32_bf16 v[40:43], v[60:63], v[176:179], v[40:43]\n"
"v_add_f32_e32 v252, v252, v249\n"
"v_add_f32_e32 v251, v251, v248\n"
"v_mfma_f32_16x16x32_bf16 v[120:123], v[80:83], v[176:179], 0\n"
"v_add_f32_e32 v255, v255, v252\n"
"v_add_f32_e32 v253, v253, v251\n"
"v_mfma_f32_16x16x32_bf16 v[120:123], v[88:91], v[168:171], v[120:123]\n"
"v_add_f32_e32 v255, v255, v253\n"
"v_mov_b32_e32 v253, v255\n"
"v_mfma_f32_16x16x32_bf16 v[120:123], v[100:103], v[148:151], v[120:123]\n"
"s_add_u32 s98, s71, 0\n"
"buffer_load_dwordx2 v[250:251], off, s[4:7], s98 sc1\n"
"v_mfma_f32_16x16x32_bf16 v[120:123], v[76:79], v[84:87], v[120:123]\n"
"v_permlane32_swap_b32_e32 v255, v253\n"
"v_add_f32_e32 v255, v255, v253\n"
"v_mfma_f32_16x16x32_bf16 v[120:123], v[132:135], v[192:195], v[120:123]\n"
"v_mul_f32_e32 v255, 0x3a800000, v255\n"
"v_mov_b32_e32 v253, v255\n"
"v_mfma_f32_16x16x32_bf16 v[120:123], v[128:131], v[200:203], v[120:123]\n"
"ds_write_b128 v3, v[40:43] offset:10240\n"
"v_mfma_f32_16x16x32_bf16 v[120:123], v[48:51], v[164:167], v[120:123]\n"
"buffer_store_dwordx2 v[18:19], v11, s[4:7], s68 offen offset:32 sc1\n"
"v_mfma_f32_16x16x32_bf16 v[120:123], v[72:75], v[152:155], v[120:123]\n"
"v_permlane16_swap_b32_e32 v255, v253\n"
"v_cvt_pk_bf16_f32 v254, -v255, 0\n"
"; @A G1 3.2\n"
"v_mfma_f32_16x16x32_bf16 v[40:43], a[0:3], v[68:71], 0\n"
"v_fma_f32 v253, -v255, v255, v253\n"
"v_lshlrev_b32_e32 v254, 16, v254\n"
"v_mfma_f32_16x16x32_bf16 v[40:43], a[4:7], v[92:95], v[40:43]\n"
"v_max_f32_e32 v253, 0, v253\n"
"v_cvt_pk_bf16_f32 v253, v253, 0\n"
"v_mfma_f32_16x16x32_bf16 v[40:43], a[8:11], v[140:143], v[40:43]\n"
"buffer_load_dwordx4 a[220:223], v1, s[4:7], s97 offen offset:3072 sc1\n"
"v_mfma_f32_16x16x32_bf16 v[40:43], a[12:15], v[144:147], v[40:43]\n"
"s_waitcnt vmcnt(15)\n"
"v_lshlrev_b32_e32 v253, 16, v253\n"
"v_add_f32_e32 v253, 0x3727c5ac, v253\n"
"v_mfma_f32_16x16x32_bf16 v[40:43], a[16:19], v[104:107], v[40:43]\n"
"s_add_u32 s97, s70, 0x6000\n"
"v_cvt_pk_bf16_f32 v253, v253, 0\n"
"v_lshlrev_b32_e32 v253, 16, v253\n"
"v_mfma_f32_16x16x32_bf16 v[40:43], a[20:23], v[124:127], v[40:43]\n"
"ds_write_b128 v3, v[120:123] offset:11264\n"
"v_mfma_f32_16x16x32_bf16 v[40:43], a[24:27], v[136:139], v[40:43]\n"
"buffer_load_dwordx4 a[224:227], v0, s[4:7], s97 offen sc1\n"
"v_mfma_f32_16x16x32_bf16 v[40:43], a[28:31], v[160:163], v[40:43]\n"
"v_sqrt_f32_e32 v253, v253\n"
"v_mfma_f32_16x16x32_bf16 v[84:87], a[60:63], v[160:163], 0\n"
"v_cvt_pk_bf16_f32 v253, v253, 0\n"
"v_lshlrev_b32_e32 v253, 16, v253\n"
"v_mfma_f32_16x16x32_bf16 v[84:87], a[56:59], v[136:139], v[84:87]\n"
"v_rcp_f32_e32 v255, v253\n"
"v_mfma_f32_16x16x32_bf16 v[84:87], a[52:55], v[124:127], v[84:87]\n"
"s_waitcnt lgkmcnt(0)\n"
"s_barrier\n"
"v_mfma_f32_16x16x32_bf16 v[84:87], a[48:51], v[104:107], v[84:87]\n"
"buffer_load_dwordx4 a[228:231], v0, s[4:7], s97 offen offset:1024 sc1\n"
"v_mfma_f32_16x16x32_bf16 v[84:87], a[44:47], v[144:147], v[84:87]\n"
"ds_read_b128 v[120:123], v4\n"
"v_mul_f32_e32 v253, 0x3e8293ee, v255\n"
"v_mfma_f32_16x16x32_bf16 v[84:87], a[40:43], v[140:143], v[84:87]\n"
"ds_write_b128 v3, v[40:43] offset:12288\n"
"v_mfma_f32_16x16x32_bf16 v[84:87], a[36:39], v[92:95], v[84:87]\n"
"ds_read_b128 v[40:43], v9 offset:128\n"
"ds_read_b128 v[148:151], v5\n"
"v_mfma_f32_16x16x32_bf16 v[84:87], a[32:35], v[68:71], v[84:87]\n"
"buffer_load_dwordx4 a[232:235], v0, s[4:7], s97 offen offset:2048 sc1\n"
"v_mfma_f32_16x16x32_bf16 v[152:155], a[64:67], v[68:71], 0\n"
"v_mfma_f32_16x16x32_bf16 v[152:155], a[68:71], v[92:95], v[152:155]\n"
"v_mfma_f32_16x16x32_bf16 v[152:155], a[72:75], v[140:143], v[152:155]\n"
"ds_read_b128 v[164:167], v4 offset:1024\n"
"ds_read_b128 v[168:171], v9 offset:192\n"
"v_mfma_f32_16x16x32_bf16 v[152:155], a[76:79], v[144:147], v[152:155]\n"
"buffer_load_dwordx4 a[236:239], v0, s[4:7], s97 offen offset:3072 sc1\n"
"v_mfma_f32_16x16x32_bf16 v[152:155], a[80:83], v[104:107], v[152:155]\n"
"ds_read_b128 v[176:179], v5 offset:1024\n"
"s_waitcnt lgkmcnt(3)\n"
"v_mfma_f32_16x16x32_bf16 v[152:155], a[84:87], v[124:127], v[152:155]\n"
"ds_write_b128 v3, v[84:87] offset:13312\n"
"v_mfma_f32_16x16x32_bf16 v[152:155], a[88:91], v[136:139], v[152:155]\n"
"v_fmac_f32_e32 v120, v254, v40\n"
"v_fmac_f32_e32 v121, v254, v41\n"
"v_mfma_f32_16x16x32_bf16 v[152:155], a[92:95], v[160:163], v[152:155]\n"
"buffer_load_dwordx4 a[240:243], v1, s[4:7], s97 offen sc1\n"
"v_mfma_f32_16x16x32_bf16 v[84:87], a[124:127], v[160:163], 0\n"
"v_fmac_f32_e32 v122, v254, v42\n"
"v_fmac_f32_e32 v123, v254, v43\n"
"v_mfma_f32_16x16x32_bf16 v[84:87], a[120:123], v[136:139], v[84:87]\n"
"ds_read_b128 v[40:43], v4 offset:2048\n"
"ds_read_b128 v[184:187], v9 offset:256\n"
"v_mfma_f32_16x16x32_bf16 v[84:87], a[116:119], v[124:127], v[84:87]\n"
"ds_read_b128 v[188:191], v5 offset:2048\n"
"v_add_f32_e32 v120, v120, v148\n"
"v_mfma_f32_16x16x32_bf16 v[84:87], a[112:115], v[104:107], v[84:87]\n"
"buffer_load_dwordx4 a[244:247], v1, s[4:7], s97 offen offset:1024 sc1\n"
"v_mfma_f32_16x16x32_bf16 v[84:87], a[108:111], v[144:147], v[84:87]\n"
"v_add_f32_e32 v121, v121, v149\n"
"v_add_f32_e32 v122, v122, v150\n"
"v_mfma_f32_16x16x32_bf16 v[84:87], a[104:107], v[140:143], v[84:87]\n"
"ds_write_b128 v3, v[152:155] offset:14336\n"
"v_mfma_f32_16x16x32_bf16 v[84:87], a[100:103], v[92:95], v[84:87]\n"
"s_waitcnt lgkmcnt(5)\n"
"v_add_f32_e32 v123, v123, v151\n"
"v_fmac_f32_e32 v164, v254, v168\n"
"v_mfma_f32_16x16x32_bf16 v[84:87], a[96:99], v[68:71], v[84:87]\n"
"buffer_load_dwordx4 a[248:251], v1, s[4:7], s97 offen offset:2048 sc1\n"
"v_mfma_f32_16x16x32_bf16 v[148:151], v[24:27], v[68:71], 0\n"
"ds_read_b128 v[152:155], v4 offset:3072\n"
"ds_read_b128 v[192:195], v9 offset:320\n"
"v_mfma_f32_16x16x32_bf16 v[148:151], v[56:59], v[92:95], v[148:151]\n"
"ds_read_b128 v[196:199], v5 offset:3072\n"
"v_fmac_f32_e32 v165, v254, v169\n"
"v_mfma_f32_16x16x32_bf16 v[148:151], v[28:31], v[140:143], v[148:151]\n"
"v_fmac_f32_e32 v166, v254, v170\n"
"v_fmac_f32_e32 v167, v254, v171\n"
"v_mfma_f32_16x16x32_bf16 v[148:151], v[64:67], v[144:147], v[148:151]\n"
"buffer_load_dwordx4 a[252:255], v1, s[4:7], s97 offen offset:3072 sc1\n"
"v_mfma_f32_16x16x32_bf16 v[148:151], v[52:55], v[104:107], v[148:151]\n"
"v_add_f32_e32 v164, v164, v176\n"
"v_add_f32_e32 v165, v165, v177\n"
"v_mfma_f32_16x16x32_bf16 v[148:151], v[36:39], v[124:127], v[148:151]\n"
"ds_write_b128 v3, v[84:87] offset:15360\n"
"v_mfma_f32_16x16x32_bf16 v[148:151], v[108:111], v[136:139], v[148:151]\n"
"s_waitcnt lgkmcnt(5)\n"
"v_add_f32_e32 v166, v166, v178\n"
"v_add_f32_e32 v167, v167, v179\n"
"v_mfma_f32_16x16x32_bf16 v[148:151], v[60:63], v[160:163], v[148:151]\n"
"ds_read_b128 v[84:87], v4 offset:4096\n"
"ds_read_b128 v[168:171], v9\n"
"v_mfma_f32_16x16x32_bf16 v[176:179], v[80:83], v[160:163], 0\n"
"ds_read_b128 v[160:163], v5 offset:4096\n"
"v_fmac_f32_e32 v40, v254, v184\n"
"v_mfma_f32_16x16x32_bf16 v[176:179], v[88:91], v[136:139], v[176:179]\n"
"v_fmac_f32_e32 v41, v254, v185\n"
"v_fmac_f32_e32 v42, v254, v186\n"
"v_mfma_f32_16x16x32_bf16 v[176:179], v[100:103], v[124:127], v[176:179]\n"
"v_fmac_f32_e32 v43, v254, v187\n"
"v_add_f32_e32 v40, v40, v188\n"
"v_mfma_f32_16x16x32_bf16 v[176:179], v[76:79], v[104:107], v[176:179]\n"
"v_add_f32_e32 v41, v41, v189\n"
"v_add_f32_e32 v42, v42, v190\n"
"v_mfma_f32_16x16x32_bf16 v[176:179], v[132:135], v[144:147], v[176:179]\n"
"s_waitcnt vmcnt(10)\n"
"v_cmp_ne_u64_e32 vcc, v[250:251], v[18:19]\n"
"v_mfma_f32_16x16x32_bf16 v[176:179], v[128:131], v[140:143], v[176:179]\n"
"ds_write_b128 v3, v[148:151] offset:16384\n"
"v_mfma_f32_16x16x32_bf16 v[176:179], v[48:51], v[92:95], v[176:179]\n"
"s_cbranch_vccnz .Lmk_f73s\n"
".Lmk_f73r:\n"
"s_add_u32 m0, s77, 0x6000\n"
"v_mfma_f32_16x16x32_bf16 v[176:179], v[72:75], v[68:71], v[176:179]\n"
"s_add_u32 s98, s65, 0x2000\n"
"ds_read_b128 v[68:71], v4 offset:5120\n"
"; @A G1 3.3\n"
"v_mfma_f32_16x16x32_bf16 v[120:123], a[0:3], v[44:47], v[120:123]\n"
"buffer_load_dwordx4 v2, s[4:7], s98 offen sc1 lds\n"
"v_mfma_f32_16x16x32_bf16 v[120:123], a[4:7], v[96:99], v[120:123]\n"
"buffer_load_dwordx4 v2, s[4:7], s98 offen offset:1024 sc1 lds\n"
"v_mfma_f32_16x16x32_bf16 v[120:123], a[8:11], v[32:35], v[120:123]\n"
"ds_read_b128 v[92:95], v9 offset:64\n"
"ds_read_b128 v[104:107], v5 offset:5120\n"
"v_mfma_f32_16x16x32_bf16 v[120:123], a[12:15], v[116:119], v[120:123]\n"
"s_waitcnt lgkmcnt(8)\n"
"v_add_f32_e32 v43, v43, v191\n"
"v_mfma_f32_16x16x32_bf16 v[120:123], a[16:19], v[112:115], v[120:123]\n"
"v_fmac_f32_e32 v152, v254, v192\n"
"v_fmac_f32_e32 v153, v254, v193\n"
"v_mfma_f32_16x16x32_bf16 v[120:123], a[20:23], v[156:159], v[120:123]\n"
"ds_write_b128 v3, v[176:179] offset:17408\n"
"v_mfma_f32_16x16x32_bf16 v[120:123], a[24:27], v[172:175], v[120:123]\n"
"v_fmac_f32_e32 v154, v254, v194\n"
"v_fmac_f32_e32 v155, v254, v195\n"
"v_mfma_f32_16x16x32_bf16 v[120:123], a[28:31], v[180:183], v[120:123]\n"
"s_add_u32 m0, s77, 0xe000\n"
"s_add_u32 s98, s65, 0x6000\n"
"v_add_f32_e32 v152, v152, v196\n"
"v_mfma_f32_16x16x32_bf16 v[164:167], a[60:63], v[180:183], v[164:167]\n"
"buffer_load_dwordx4 v2, s[4:7], s98 offen sc1 lds\n"
"v_mfma_f32_16x16x32_bf16 v[164:167], a[56:59], v[172:175], v[164:167]\n"
"buffer_load_dwordx4 v2, s[4:7], s98 offen offset:1024 sc1 lds\n"
"v_mfma_f32_16x16x32_bf16 v[164:167], a[52:55], v[156:159], v[164:167]\n"
"s_waitcnt lgkmcnt(0)\n"
"s_barrier\n"
"v_mfma_f32_16x16x32_bf16 v[164:167], a[48:51], v[112:115], v[164:167]\n"
"ds_read_b128 v[124:127], v6\n"
"ds_read_b128 v[136:139], v6 offset:1024\n"
"v_mfma_f32_16x16x32_bf16 v[164:167], a[44:47], v[116:119], v[164:167]\n"
"s_add_u32 s98, s69, 0x600000\n"
"buffer_load_dwordx4 a[0:3], v0, s[4:7], s98 offen sc1\n"
"v_mfma_f32_16x16x32_bf16 v[164:167], a[40:43], v[32:35], v[164:167]\n"
"v_add_f32_e32 v153, v153, v197\n"
"v_add_f32_e32 v154, v154, v198\n"
"v_mfma_f32_16x16x32_bf16 v[164:167], a[36:39], v[96:99], v[164:167]\n"
"v_add_f32_e32 v155, v155, v199\n"
"v_fmac_f32_e32 v84, v254, v168\n"
"v_mfma_f32_16x16x32_bf16 v[164:167], a[32:35], v[44:47], v[164:167]\n"
"buffer_load_dwordx4 a[4:7], v0, s[4:7], s98 offen offset:1024 sc1\n"
"v_mfma_f32_16x16x32_bf16 v[40:43], a[64:67], v[44:47], v[40:43]\n"
"v_fmac_f32_e32 v85, v254, v169\n"
"v_fmac_f32_e32 v86, v254, v170\n"
"v_mfma_f32_16x16x32_bf16 v[40:43], a[68:71], v[96:99], v[40:43]\n"
"v_fmac_f32_e32 v87, v254, v171\n"
"v_add_f32_e32 v84, v84, v160\n"
"v_mfma_f32_16x16x32_bf16 v[40:43], a[72:75], v[32:35], v[40:43]\n"
"buffer_load_dwordx4 a[8:11], v0, s[4:7], s98 offen offset:2048 sc1\n"
"v_mfma_f32_16x16x32_bf16 v[40:43], a[76:79], v[116:119], v[40:43]\n"
"v_add_f32_e32 v85, v85, v161\n"
"v_add_f32_e32 v86, v86, v162\n"
"v_mfma_f32_16x16x32_bf16 v[40:43], a[80:83], v[112:115], v[40:43]\n"
"v_add_f32_e32 v87, v87, v163\n"
"v_fmac_f32_e32 v68, v254, v92\n"
"v_mfma_f32_16x16x32_bf16 v[40:43], a[84:87], v[156:159], v[40:43]\n"
"buffer_load_dwordx4 a[12:15], v0, s[4:7], s98 offen offset:3072 sc1\n"
"v_mfma_f32_16x16x32_bf16 v[40:43], a[88:91], v[172:175], v[40:43]\n"
"ds_read_b128 v[140:143], v6 offset:2048\n"
"ds_read_b128 v[144:147], v6 offset:3072\n"
"v_mfma_f32_16x16x32_bf16 v[40:43], a[92:95], v[180:183], v[40:43]\n"
"s_waitcnt lgkmcnt(2)\n"
"v_fmac_f32_e32 v69, v254, v93\n"
"v_fmac_f32_e32 v70, v254, v94\n"
"v_mfma_f32_16x16x32_bf16 v[152:155], a[124:127], v[180:183], v[152:155]\n"
"buffer_load_dwordx4 a[16:19], v1, s[4:7], s98 offen sc1\n"
"v_mfma_f32_16x16x32_bf16 v[152:155], a[120:123], v[172:175], v[152:155]\n"
"v_fmac_f32_e32 v71, v254, v95\n"
"v_add_f32_e32 v68, v68, v104\n"
"v_mfma_f32_16x16x32_bf16 v[152:155], a[116:119], v[156:159], v[152:155]\n"
"v_add_f32_e32 v69, v69, v105\n"
"v_add_f32_e32 v70, v70, v106\n"
"v_mfma_f32_16x16x32_bf16 v[152:155], a[112:115], v[112:115], v[152:155]\n"
"buffer_load_dwordx4 a[20:23], v1, s[4:7], s98 offen offset:1024 sc1\n"
"v_mfma_f32_16x16x32_bf16 v[152:155], a[108:111], v[116:119], v[152:155]\n"
"v_add_f32_e32 v71, v71, v107\n"
"v_add_f32_e32 v120, v120, v124\n"
"v_mfma_f32_16x16x32_bf16 v[152:155], a[104:107], v[32:35], v[152:155]\n"
"v_add_f32_e32 v121, v121, v125\n"
"v_add_f32_e32 v122, v122, v126\n"
"v_mfma_f32_16x16x32_bf16 v[152:155], a[100:103], v[96:99], v[152:155]\n"
"buffer_load_dwordx4 a[24:27], v1, s[4:7], s98 offen offset:2048 sc1\n"
"v_mfma_f32_16x16x32_bf16 v[152:155], a[96:99], v[44:47], v[152:155]\n"
"v_add_f32_e32 v123, v123, v127\n"
"v_add_f32_e32 v164, v164, v136\n"
"v_mfma_f32_16x16x32_bf16 v[84:87], v[24:27], v[44:47], v[84:87]\n"
"v_add_f32_e32 v165, v165, v137\n"
"v_add_f32_e32 v166, v166, v138\n"
"v_mfma_f32_16x16x32_bf16 v[84:87], v[56:59], v[96:99], v[84:87]\n"
"buffer_load_dwordx4 a[28:31], v1, s[4:7], s98 offen offset:3072 sc1\n"
"v_mfma_f32_16x16x32_bf16 v[84:87], v[28:31], v[32:35], v[84:87]\n"
"v_add_f32_e32 v167, v167, v139\n"
"v_mul_f32_e32 v120, v120, v255\n"
"v_mfma_f32_16x16x32_bf16 v[84:87], v[64:67], v[116:119], v[84:87]\n"
"v_mul_f32_e32 v121, v121, v255\n"
"v_mul_f32_e32 v122, v122, v255\n"
"v_mfma_f32_16x16x32_bf16 v[84:87], v[52:55], v[112:115], v[84:87]\n"
"s_waitcnt lgkmcnt(0)\n"
"v_mul_f32_e32 v123, v123, v255\n"
"v_mul_f32_e32 v164, v164, v255\n"
"v_mfma_f32_16x16x32_bf16 v[84:87], v[36:39], v[156:159], v[84:87]\n"
"v_mul_f32_e32 v165, v165, v255\n"
"v_mul_f32_e32 v166, v166, v255\n"
"v_mfma_f32_16x16x32_bf16 v[84:87], v[108:111], v[172:175], v[84:87]\n"
"ds_read_b128 v[24:27], v6 offset:4096\n"
"ds_read_b128 v[28:31], v6 offset:5120\n"
"v_mfma_f32_16x16x32_bf16 v[84:87], v[60:63], v[180:183], v[84:87]\n"
"v_mul_f32_e32 v167, v167, v255\n"
"v_cvt_pk_bf16_f32 v36, v120, v121\n"
"v_mfma_f32_16x16x32_bf16 v[68:71], v[80:83], v[180:183], v[68:71]\n"
"v_cvt_pk_bf16_f32 v37, v122, v123\n"
"v_cvt_pk_bf16_f32 v38, v164, v165\n"
"v_mfma_f32_16x16x32_bf16 v[68:71], v[88:91], v[172:175], v[68:71]\n"
"v_cvt_pk_bf16_f32 v39, v166, v167\n"
"v_add_f32_e32 v40, v40, v140\n"
"v_mfma_f32_16x16x32_bf16 v[68:71], v[100:103], v[156:159], v[68:71]\n"
"ds_write_b128 v7, v[36:39] offset:16384\n"
"v_mfma_f32_16x16x32_bf16 v[68:71], v[76:79], v[112:115], v[68:71]\n"
"buffer_store_dwordx4 v[36:39], v7, s[4:7], s64 offen sc1\n"
"v_mfma_f32_16x16x32_bf16 v[68:71], v[132:135], v[116:119], v[68:71]\n"
"v_add_f32_e32 v41, v41, v141\n"
"v_add_f32_e32 v42, v42, v142\n"
"v_mfma_f32_16x16x32_bf16 v[68:71], v[128:131], v[32:35], v[68:71]\n"
"v_add_f32_e32 v43, v43, v143\n"
"v_add_f32_e32 v152, v152, v144\n"
"v_mfma_f32_16x16x32_bf16 v[68:71], v[48:51], v[96:99], v[68:71]\n"
"v_add_f32_e32 v153, v153, v145\n"
"v_add_f32_e32 v154, v154, v146\n"
"v_mfma_f32_16x16x32_bf16 v[68:71], v[72:75], v[44:47], v[68:71]\n"
"v_add_f32_e32 v155, v155, v147\n"
"v_mul_f32_e32 v40, v40, v255\n"
"v_mul_f32_e32 v41, v41, v255\n"
"v_mul_f32_e32 v42, v42, v255\n"
"v_mul_f32_e32 v43, v43, v255\n"
"v_mul_f32_e32 v152, v152, v255\n"
"v_mul_f32_e32 v153, v153, v255\n"
"v_mul_f32_e32 v154, v154, v255\n"
"v_mul_f32_e32 v155, v155, v255\n"
"v_cvt_pk_bf16_f32 v32, v40, v41\n"
"v_cvt_pk_bf16_f32 v33, v42, v43\n"
"v_cvt_pk_bf16_f32 v34, v152, v153\n"
"v_cvt_pk_bf16_f32 v35, v154, v155\n"
"v_xor_b32_e32 v255, 32, v16\n"
"v_xor_b32_e32 v254, 32, v15\n"
"v_mov_b32_e32 v88, 0x3f803f80\n"
"v_mov_b32_e32 v89, 0x3f803f80\n"
"v_mov_b32_e32 v90, 0x3f803f80\n"
"v_mov_b32_e32 v91, 0x3f803f80\n"
"v_mov_b32_e32 v252, 0x53800000\n"
"ds_write_b128 v7, v[32:35] offset:49152\n"
".Lmk_a4:\n"
"; @A tail\n"
"s_add_u32 s98, s64, 0x4000\n"
"buffer_store_dwordx4 v[32:35], v7, s[4:7], s98 offen sc1\n"
"s_waitcnt lgkmcnt(2)\n"
"v_add_f32_e32 v84, v84, v24\n"
"v_add_f32_e32 v85, v85, v25\n"
"v_add_f32_e32 v86, v86, v26\n"
"v_add_f32_e32 v87, v87, v27\n"
"v_add_f32_e32 v68, v68, v28\n"
"v_add_f32_e32 v69, v69, v29\n"
"v_add_f32_e32 v70, v70, v30\n"
"v_add_f32_e32 v71, v71, v31\n"
"v_mul_f32_e32 v84, v84, v253\n"
"v_mul_f32_e32 v85, v85, v253\n"
"v_mul_f32_e32 v86, v86, v253\n"
"v_mul_f32_e32 v87, v87, v253\n"
"v_mul_f32_e32 v68, v68, v253\n"
"v_mul_f32_e32 v69, v69, v253\n"
"v_mul_f32_e32 v70, v70, v253\n"
"v_mul_f32_e32 v71, v71, v253\n"
"v_cvt_pk_bf16_f32 v24, v84, v85\n"
"v_cvt_pk_bf16_f32 v25, v86, v87\n"
"v_cvt_pk_bf16_f32 v26, v68, v69\n"
"v_cvt_pk_bf16_f32 v27, v70, v71\n"
"ds_write_b128 v2, v[24:27] offset:16384\n"
"s_waitcnt lgkmcnt(0)\n"
"s_barrier\n"
"ds_read_b128 v[28:31], v2 offset:49152\n"
"ds_read_b128 v[32:35], v2 offset:50176\n"
"ds_read_b128 v[92:95], v2 offset:16384\n"
"ds_read_b128 v[96:99], v2 offset:17408\n"
"; @A AT b\n"
"s_mov_b32 s2, 0\n"
"v_mov_b32_e32 v253, 0xff800000\n"
"v_mov_b32_e32 v251, 0xff800000\n"
"v_mov_b32_e32 v250, 0x7f800000\n"
"v_mov_b32_e32 v249, 0x7f800000\n"
".Lmk_a91:\n"
"ds_read_b128 v[24:27], v15 offset:24576\n"
"ds_read_b128 v[36:39], v254 offset:24832\n"
"ds_read_b128 v[40:43], v15 offset:22528\n"
"ds_read_b128 v[44:47], v254 offset:22784\n"
"s_cmp_gt_i32 s78, 11\n"
"s_cselect_b32 s98, 0x80000000, 0\n"
"s_or_b32 s98, s98, 0x7f800000\n"
"s_waitcnt lgkmcnt(2)\n"
"v_mfma_f32_16x16x32_bf16 v[48:51], v[24:27], v[28:31], 0\n"
"v_mfma_f32_16x16x32_bf16 v[52:55], v[24:27], v[32:35], 0\n"
"v_mfma_f32_16x16x32_bf16 v[56:59], v[36:39], v[32:35], 0\n"
"v_mfma_f32_16x16x32_bf16 v[24:27], v[36:39], v[28:31], 0\n"
"s_waitcnt lgkmcnt(0)\n"
"s_nop 3\n"
"v_max_f32_e32 v248, v48, v49\n"
"v_max_f32_e32 v247, v52, v53\n"
"v_max3_f32 v248, v248, v50, v51\n"
"v_max3_f32 v247, v247, v54, v55\n"
"v_max3_f32 v248, v248, v24, v25\n"
"v_max3_f32 v247, v247, v56, v57\n"
"v_max3_f32 v248, v248, v26, v27\n"
"v_max3_f32 v247, v247, v58, v59\n"
"v_mov_b32_e32 v246, v248\n"
"v_mov_b32_e32 v245, v247\n"
"s_nop 0\n"
"v_permlane16_swap_b32_e32 v248, v246\n"
"v_permlane16_swap_b32_e32 v247, v245\n"
"v_max_f32_e32 v248, v248, v246\n"
"v_max_f32_e32 v247, v247, v245\n"
"v_mov_b32_e32 v246, v248\n"
"v_mov_b32_e32 v245, v247\n"
"s_nop 0\n"
"v_permlane32_swap_b32_e32 v248, v246\n"
"v_permlane32_swap_b32_e32 v247, v245\n"
"v_max_f32_e32 v248, v248, v246\n"
"v_max_f32_e32 v247, v247, v245\n"
"v_med3_f32 v248, v248, v253, v250\n"
"v_med3_f32 v247, v247, v251, v249\n"
"v_min_f32_e64 v36, s98, -v248\n"
"v_min_f32_e64 v37, s98, -v248\n"
"v_min_f32_e64 v38, s98, -v248\n"
"v_min_f32_e64 v39, s98, -v248\n"
"v_min_f32_e64 v60, s98, -v247\n"
"v_min_f32_e64 v61, s98, -v247\n"
"v_min_f32_e64 v62, s98, -v247\n"
"v_min_f32_e64 v63, s98, -v247\n"
"v_mfma_f32_16x16x32_bf16 v[64:67], v[40:43], v[28:31], v[36:39]\n"
"ds_read_b128 v[68:71], v15 offset:20480\n"
"s_add_u32 s98, s69, 0x602000\n"
"buffer_load_dwordx4 a[32:35], v0, s[4:7], s98 offen sc1\n"
"v_sub_f32_e32 v48, v48, v248\n"
"v_sub_f32_e32 v49, v49, v248\n"
"v_sub_f32_e32 v50, v50, v248\n"
"v_mfma_f32_16x16x32_bf16 v[72:75], v[40:43], v[32:35], v[60:63]\n"
"buffer_load_dwordx4 a[36:39], v0, s[4:7], s98 offen offset:1024 sc1\n"
"v_sub_f32_e32 v51, v51, v248\n"
"v_sub_f32_e32 v24, v24, v248\n"
"v_sub_f32_e32 v25, v25, v248\n"
"v_sub_f32_e32 v26, v26, v248\n"
"v_sub_f32_e32 v27, v27, v248\n"
"v_mfma_f32_16x16x32_bf16 v[40:43], v[44:47], v[32:35], v[60:63]\n"
"ds_read_b128 v[76:79], v254 offset:20736\n"
"v_sub_f32_e32 v52, v52, v247\n"
"v_sub_f32_e32 v53, v53, v247\n"
"v_sub_f32_e32 v54, v54, v247\n"
"v_sub_f32_e32 v55, v55, v247\n"
"v_mfma_f32_16x16x32_bf16 v[80:83], v[44:47], v[28:31], v[36:39]\n"
"buffer_load_dwordx4 a[40:43], v0, s[4:7], s98 offen offset:2048 sc1\n"
"v_sub_f32_e32 v56, v56, v247\n"
"v_sub_f32_e32 v57, v57, v247\n"
"v_sub_f32_e32 v58, v58, v247\n"
"v_sub_f32_e32 v59, v59, v247\n"
"s_waitcnt lgkmcnt(0)\n"
"v_mfma_f32_16x16x32_bf16 v[44:47], v[68:71], v[28:31], v[36:39]\n"
"ds_read_b128 v[84:87], v15 offset:18432\n"
"buffer_load_dwordx4 a[44:47], v0, s[4:7], s98 offen offset:3072 sc1\n"
"ds_read_b128 v[100:103], v254 offset:18688\n"
"v_exp_f32_e32 v64, v64\n"
"v_exp_f32_e32 v65, v65\n"
"v_exp_f32_e32 v66, v66\n"
"v_exp_f32_e32 v67, v67\n"
"v_exp_f32_e32 v80, v80\n"
"v_mfma_f32_16x16x32_bf16 v[104:107], v[68:71], v[32:35], v[60:63]\n"
"ds_read_b64_tr_b16 v[68:69], v16 offset:22528\n"
"buffer_load_dwordx4 a[48:51], v1, s[4:7], s98 offen sc1\n"
"v_exp_f32_e32 v81, v81\n"
"v_exp_f32_e32 v82, v82\n"
"v_exp_f32_e32 v83, v83\n"
"v_cvt_pk_bf16_f32 v108, v64, v65\n"
"v_cvt_pk_bf16_f32 v109, v66, v67\n"
"v_cvt_pk_bf16_f32 v110, v80, v81\n"
"v_mfma_f32_16x16x32_bf16 v[64:67], v[76:79], v[32:35], v[60:63]\n"
"ds_read_b64_tr_b16 v[70:71], v255 offset:22784\n"
"ds_read_b64_tr_b16 v[80:81], v255 offset:22528\n"
"buffer_load_dwordx4 a[52:55], v1, s[4:7], s98 offen offset:1024 sc1\n"
"v_cvt_pk_bf16_f32 v111, v82, v83\n"
"v_exp_f32_e32 v72, v72\n"
"v_exp_f32_e32 v73, v73\n"
"v_exp_f32_e32 v74, v74\n"
"v_exp_f32_e32 v75, v75\n"
"v_mfma_f32_16x16x32_bf16 v[112:115], v[76:79], v[28:31], v[36:39]\n"
"ds_read_b64_tr_b16 v[82:83], v16 offset:22784\n"
"v_exp_f32_e32 v40, v40\n"
"v_exp_f32_e32 v41, v41\n"
"v_exp_f32_e32 v42, v42\n"
"v_exp_f32_e32 v43, v43\n"
"v_cvt_pk_bf16_f32 v76, v72, v73\n"
"v_cvt_pk_bf16_f32 v77, v74, v75\n"
"v_cvt_pk_bf16_f32 v78, v40, v41\n"
"v_cvt_pk_bf16_f32 v79, v42, v43\n"
"s_waitcnt lgkmcnt(4)\n"
"v_mfma_f32_16x16x32_bf16 v[40:43], v[84:87], v[28:31], v[36:39]\n"
"ds_read_b128 v[72:75], v15 offset:16384\n"
"v_exp_f32_e32 v44, v44\n"
"v_exp_f32_e32 v45, v45\n"
"v_exp_f32_e32 v46, v46\n"
"v_mfma_f32_16x16x32_bf16 v[116:119], v[84:87], v[32:35], v[60:63]\n"
"buffer_load_dwordx4 a[56:59], v1, s[4:7], s98 offen offset:2048 sc1\n"
"v_exp_f32_e32 v47, v47\n"
"v_mfma_f32_16x16x32_bf16 v[84:87], v[100:103], v[32:35], v[60:63]\n"
"ds_read_b128 v[120:123], v254 offset:16640\n"
"v_exp_f32_e32 v112, v112\n"
"v_exp_f32_e32 v113, v113\n"
"v_mfma_f32_16x16x32_bf16 v[124:127], v[100:103], v[28:31], v[36:39]\n"
"ds_read_b64_tr_b16 v[100:101], v16 offset:20480\n"
"v_exp_f32_e32 v114, v114\n"
"v_exp_f32_e32 v115, v115\n"
"v_cvt_pk_bf16_f32 v128, v44, v45\n"
"s_waitcnt lgkmcnt(3)\n"
"v_mfma_f32_16x16x32_bf16 v[132:135], v[88:91], v[108:111], 0\n"
"buffer_load_dwordx4 a[60:63], v1, s[4:7], s98 offen offset:3072 sc1\n"
"v_cvt_pk_bf16_f32 v129, v46, v47\n"
"v_cvt_pk_bf16_f32 v130, v112, v113\n"
"v_cvt_pk_bf16_f32 v131, v114, v115\n"
"v_mfma_f32_16x16x32_bf16 v[44:47], v[68:71], v[108:111], 0\n"
"ds_read_b64_tr_b16 v[102:103], v255 offset:20736\n"
"v_exp_f32_e32 v104, v104\n"
"v_exp_f32_e32 v105, v105\n"
"v_mfma_f32_16x16x32_bf16 v[112:115], v[80:83], v[108:111], 0\n"
"ds_read_b64_tr_b16 v[108:109], v255 offset:20480\n"
"s_add_u32 s98, s69, 0x604000\n"
"buffer_load_dwordx4 a[64:67], v0, s[4:7], s98 offen sc1\n"
"v_exp_f32_e32 v106, v106\n"
"v_exp_f32_e32 v107, v107\n"
"v_mfma_f32_16x16x32_bf16 v[136:139], v[80:83], v[76:79], 0\n"
"v_exp_f32_e32 v64, v64\n"
"v_exp_f32_e32 v65, v65\n"
"v_mfma_f32_16x16x32_bf16 v[80:83], v[68:71], v[76:79], 0\n"
"ds_read_b64_tr_b16 v[110:111], v16 offset:20736\n"
"v_exp_f32_e32 v66, v66\n"
"v_exp_f32_e32 v67, v67\n"
"v_mfma_f32_16x16x32_bf16 v[68:71], v[88:91], v[76:79], 0\n"
"s_waitcnt vmcnt(19)\n"
"v_cvt_pk_bf16_f32 v76, v104, v105\n"
"v_cvt_pk_bf16_f32 v77, v106, v107\n"
"v_cvt_pk_bf16_f32 v78, v64, v65\n"
"v_cvt_pk_bf16_f32 v79, v66, v67\n"
"s_cmp_lg_u32 s2, 0\n"
"s_cbranch_scc1 .Lmk_s93\n"
"s_barrier\n"
".Lmk_s93:\n"
"s_waitcnt lgkmcnt(4)\n"
"v_mfma_f32_16x16x32_bf16 v[64:67], v[72:75], v[28:31], v[36:39]\n"
"ds_read_b128 v[104:107], v15 offset:14336\n"
"v_exp_f32_e32 v40, v40\n"
"v_exp_f32_e32 v41, v41\n"
"v_exp_f32_e32 v42, v42\n"
"v_mfma_f32_16x16x32_bf16 v[140:143], v[72:75], v[32:35], v[60:63]\n"
"buffer_load_dwordx4 a[68:71], v0, s[4:7], s98 offen offset:1024 sc1\n"
"v_exp_f32_e32 v43, v43\n"
"v_mfma_f32_16x16x32_bf16 v[72:75], v[120:123], v[32:35], v[60:63]\n"
"ds_read_b128 v[144:147], v254 offset:14592\n"
"v_exp_f32_e32 v124, v124\n"
"v_exp_f32_e32 v125, v125\n"
"v_mfma_f32_16x16x32_bf16 v[148:151], v[120:123], v[28:31], v[36:39]\n"
"ds_read_b64_tr_b16 v[120:121], v16 offset:18432\n"
"v_exp_f32_e32 v126, v126\n"
"v_exp_f32_e32 v127, v127\n"
"s_waitcnt lgkmcnt(3)\n"
"v_mfma_f32_16x16x32_bf16 v[132:135], v[88:91], v[128:131], v[132:135]\n"
"buffer_load_dwordx4 a[72:75], v0, s[4:7], s98 offen offset:2048 sc1\n"
"v_cvt_pk_bf16_f32 v152, v40, v41\n"
"v_cvt_pk_bf16_f32 v153, v42, v43\n"
"v_cvt_pk_bf16_f32 v154, v124, v125\n"
"v_mfma_f32_16x16x32_bf16 v[44:47], v[100:103], v[128:131], v[44:47]\n"
"ds_read_b64_tr_b16 v[122:123], v255 offset:18688\n"
"v_cvt_pk_bf16_f32 v155, v126, v127\n"
"v_exp_f32_e32 v116, v116\n"
"v_exp_f32_e32 v117, v117\n"
"v_mfma_f32_16x16x32_bf16 v[112:115], v[108:111], v[128:131], v[112:115]\n"
"ds_read_b64_tr_b16 v[40:41], v255 offset:18432\n"
"v_exp_f32_e32 v118, v118\n"
"v_exp_f32_e32 v119, v119\n"
"v_mfma_f32_16x16x32_bf16 v[136:139], v[108:111], v[76:79], v[136:139]\n"
"buffer_load_dwordx4 a[76:79], v0, s[4:7], s98 offen offset:3072 sc1\n"
"v_exp_f32_e32 v84, v84\n"
"v_exp_f32_e32 v85, v85\n"
"v_mfma_f32_16x16x32_bf16 v[80:83], v[100:103], v[76:79], v[80:83]\n"
"ds_read_b64_tr_b16 v[42:43], v16 offset:18688\n"
"v_exp_f32_e32 v86, v86\n"
"v_exp_f32_e32 v87, v87\n"
"v_mfma_f32_16x16x32_bf16 v[68:71], v[88:91], v[76:79], v[68:71]\n"
"s_waitcnt lgkmcnt(4)\n"
"v_cvt_pk_bf16_f32 v76, v116, v117\n"
"v_cvt_pk_bf16_f32 v77, v118, v119\n"
"v_cvt_pk_bf16_f32 v78, v84, v85\n"
"v_cvt_pk_bf16_f32 v79, v86, v87\n"
"s_cmp_eq_i32 s78, 8\n"
"s_cbranch_scc1 .Lmk_x94s\n"
".Lmk_x94r:\n"
"v_mfma_f32_16x16x32_bf16 v[84:87], v[104:107], v[28:31], v[36:39]\n"
"ds_read_b128 v[100:103], v15 offset:12288\n"
"v_exp_f32_e32 v64, v64\n"
"v_exp_f32_e32 v65, v65\n"
"v_exp_f32_e32 v66, v66\n"
"v_mfma_f32_16x16x32_bf16 v[108:111], v[104:107], v[32:35], v[60:63]\n"
"buffer_load_dwordx4 a[80:83], v1, s[4:7], s98 offen sc1\n"
"v_exp_f32_e32 v67, v67\n"
"v_mfma_f32_16x16x32_bf16 v[104:107], v[144:147], v[32:35], v[60:63]\n"
"ds_read_b128 v[116:119], v254 offset:12544\n"
"v_exp_f32_e32 v148, v148\n"
"v_exp_f32_e32 v149, v149\n"
"v_mfma_f32_16x16x32_bf16 v[124:127], v[144:147], v[28:31], v[36:39]\n"
"ds_read_b64_tr_b16 v[128:129], v16 offset:16384\n"
"v_exp_f32_e32 v150, v150\n"
"v_exp_f32_e32 v151, v151\n"
"s_waitcnt lgkmcnt(3)\n"
"v_mfma_f32_16x16x32_bf16 v[132:135], v[88:91], v[152:155], v[132:135]\n"
"buffer_load_dwordx4 a[84:87], v1, s[4:7], s98 offen offset:1024 sc1\n"
"v_cvt_pk_bf16_f32 v144, v64, v65\n"
"v_cvt_pk_bf16_f32 v145, v66, v67\n"
"v_cvt_pk_bf16_f32 v146, v148, v149\n"
"v_mfma_f32_16x16x32_bf16 v[44:47], v[120:123], v[152:155], v[44:47]\n"
"ds_read_b64_tr_b16 v[130:131], v255 offset:16640\n"
"v_cvt_pk_bf16_f32 v147, v150, v151\n"
"v_exp_f32_e32 v140, v140\n"
"v_exp_f32_e32 v141, v141\n"
"v_mfma_f32_16x16x32_bf16 v[112:115], v[40:43], v[152:155], v[112:115]\n"
"ds_read_b64_tr_b16 v[64:65], v255 offset:16384\n"
"v_exp_f32_e32 v142, v142\n"
"v_exp_f32_e32 v143, v143\n"
"v_mfma_f32_16x16x32_bf16 v[136:139], v[40:43], v[76:79], v[136:139]\n"
"buffer_load_dwordx4 a[88:91], v1, s[4:7], s98 offen offset:2048 sc1\n"
"v_exp_f32_e32 v72, v72\n"
"v_exp_f32_e32 v73, v73\n"
"v_mfma_f32_16x16x32_bf16 v[80:83], v[120:123], v[76:79], v[80:83]\n"
"ds_read_b64_tr_b16 v[66:67], v16 offset:16640\n"
"v_exp_f32_e32 v74, v74\n"
"v_exp_f32_e32 v75, v75\n"
"v_mfma_f32_16x16x32_bf16 v[68:71], v[88:91], v[76:79], v[68:71]\n"
"s_waitcnt lgkmcnt(4)\n"
"v_cvt_pk_bf16_f32 v40, v140, v141\n"
"v_cvt_pk_bf16_f32 v41, v142, v143\n"
"v_cvt_pk_bf16_f32 v42, v72, v73\n"
"v_cvt_pk_bf16_f32 v43, v74, v75\n"
"s_cmp_eq_i32 s78, 7\n"
"s_cbranch_scc1 .Lmk_x95s\n"
".Lmk_x95r:\n"
"v_mfma_f32_16x16x32_bf16 v[72:75], v[100:103], v[28:31], v[36:39]\n"
"ds_read_b128 v[76:79], v15 offset:10240\n"
"v_exp_f32_e32 v84, v84\n"
"v_exp_f32_e32 v85, v85\n"
"v_exp_f32_e32 v86, v86\n"
"v_mfma_f32_16x16x32_bf16 v[120:123], v[100:103], v[32:35], v[60:63]\n"
"buffer_load_dwordx4 a[92:95], v1, s[4:7], s98 offen offset:3072 sc1\n"
"v_exp_f32_e32 v87, v87\n"
"v_mfma_f32_16x16x32_bf16 v[100:103], v[116:119], v[32:35], v[60:63]\n"
"ds_read_b128 v[140:143], v254 offset:10496\n"
"v_exp_f32_e32 v124, v124\n"
"v_exp_f32_e32 v125, v125\n"
"v_mfma_f32_16x16x32_bf16 v[148:151], v[116:119], v[28:31], v[36:39]\n"
"ds_read_b64_tr_b16 v[116:117], v16 offset:14336\n"
"v_exp_f32_e32 v126, v126\n"
"v_exp_f32_e32 v127, v127\n"
"v_cvt_pk_bf16_f32 v152, v84, v85\n"
"s_waitcnt lgkmcnt(3)\n"
"v_mfma_f32_16x16x32_bf16 v[132:135], v[88:91], v[144:147], v[132:135]\n"
"s_add_u32 s98, s69, 0x606000\n"
"buffer_load_dwordx4 a[96:99], v0, s[4:7], s98 offen sc1\n"
"v_cvt_pk_bf16_f32 v153, v86, v87\n"
"v_cvt_pk_bf16_f32 v154, v124, v125\n"
"v_mfma_f32_16x16x32_bf16 v[44:47], v[128:131], v[144:147], v[44:47]\n"
"ds_read_b64_tr_b16 v[118:119], v255 offset:14592\n"
"v_cvt_pk_bf16_f32 v155, v126, v127\n"
"v_exp_f32_e32 v108, v108\n"
"v_exp_f32_e32 v109, v109\n"
"v_mfma_f32_16x16x32_bf16 v[112:115], v[64:67], v[144:147], v[112:115]\n"
"ds_read_b64_tr_b16 v[84:85], v255 offset:14336\n"
"v_exp_f32_e32 v110, v110\n"
"v_exp_f32_e32 v111, v111\n"
"v_mfma_f32_16x16x32_bf16 v[136:139], v[64:67], v[40:43], v[136:139]\n"
"buffer_load_dwordx4 a[100:103], v0, s[4:7], s98 offen offset:1024 sc1\n"
"v_exp_f32_e32 v104, v104\n"
"v_exp_f32_e32 v105, v105\n"
"v_mfma_f32_16x16x32_bf16 v[80:83], v[128:131], v[40:43], v[80:83]\n"
"ds_read_b64_tr_b16 v[86:87], v16 offset:14592\n"
"v_exp_f32_e32 v106, v106\n"
"v_exp_f32_e32 v107, v107\n"
"v_mfma_f32_16x16x32_bf16 v[68:71], v[88:91], v[40:43], v[68:71]\n"
"s_waitcnt lgkmcnt(4)\n"
"v_cvt_pk_bf16_f32 v40, v108, v109\n"
"v_cvt_pk_bf16_f32 v41, v110, v111\n"
"v_cvt_pk_bf16_f32 v42, v104, v105\n"
"v_cvt_pk_bf16_f32 v43, v106, v107\n"
"s_cmp_eq_i32 s78, 6\n"
"s_cbranch_scc1 .Lmk_x96s\n"
".Lmk_x96r:\n"
"v_mfma_f32_16x16x32_bf16 v[64:67], v[76:79], v[28:31], v[36:39]\n"
"ds_read_b128 v[104:107], v15 offset:8192\n"
"v_exp_f32_e32 v72, v72\n"
"v_exp_f32_e32 v73, v73\n"
"v_exp_f32_e32 v74, v74\n"
"v_mfma_f32_16x16x32_bf16 v[108:111], v[76:79], v[32:35], v[60:63]\n"
"buffer_load_dwordx4 a[104:107], v0, s[4:7], s98 offen offset:2048 sc1\n"
"v_exp_f32_e32 v75, v75\n"
"v_mfma_f32_16x16x32_bf16 v[76:79], v[140:143], v[32:35], v[60:63]\n"
"ds_read_b128 v[124:127], v254 offset:8448\n"
"v_exp_f32_e32 v148, v148\n"
"v_exp_f32_e32 v149, v149\n"
"v_mfma_f32_16x16x32_bf16 v[128:131], v[140:143], v[28:31], v[36:39]\n"
"ds_read_b64_tr_b16 v[140:141], v16 offset:12288\n"
"v_exp_f32_e32 v150, v150\n"
"v_exp_f32_e32 v151, v151\n"
"s_waitcnt lgkmcnt(3)\n"
"v_mfma_f32_16x16x32_bf16 v[132:135], v[88:91], v[152:155], v[132:135]\n"
"buffer_load_dwordx4 a[108:111], v0, s[4:7], s98 offen offset:3072 sc1\n"
"v_cvt_pk_bf16_f32 v144, v72, v73\n"
"v_cvt_pk_bf16_f32 v145, v74, v75\n"
"v_cvt_pk_bf16_f32 v146, v148, v149\n"
"v_mfma_f32_16x16x32_bf16 v[44:47], v[116:119], v[152:155], v[44:47]\n"
"ds_read_b64_tr_b16 v[142:143], v255 offset:12544\n"
"v_cvt_pk_bf16_f32 v147, v150, v151\n"
"v_exp_f32_e32 v120, v120\n"
"v_exp_f32_e32 v121, v121\n"
"v_mfma_f32_16x16x32_bf16 v[112:115], v[84:87], v[152:155], v[112:115]\n"
"ds_read_b64_tr_b16 v[72:73], v255 offset:12288\n"
"v_exp_f32_e32 v122, v122\n"
"v_exp_f32_e32 v123, v123\n"
"v_mfma_f32_16x16x32_bf16 v[136:139], v[84:87], v[40:43], v[136:139]\n"
"buffer_load_dwordx4 a[112:115], v1, s[4:7], s98 offen sc1\n"
"v_exp_f32_e32 v100, v100\n"
"v_exp_f32_e32 v101, v101\n"
"v_mfma_f32_16x16x32_bf16 v[80:83], v[116:119], v[40:43], v[80:83]\n"
"ds_read_b64_tr_b16 v[74:75], v16 offset:12544\n"
"v_exp_f32_e32 v102, v102\n"
"v_exp_f32_e32 v103, v103\n"
"v_mfma_f32_16x16x32_bf16 v[68:71], v[88:91], v[40:43], v[68:71]\n"
"s_waitcnt lgkmcnt(4)\n"
"v_cvt_pk_bf16_f32 v40, v120, v121\n"
"v_cvt_pk_bf16_f32 v41, v122, v123\n"
"v_cvt_pk_bf16_f32 v42, v100, v101\n"
"v_cvt_pk_bf16_f32 v43, v102, v103\n"
"s_cmp_eq_i32 s78, 5\n"
"s_cbranch_scc1 .Lmk_x97s\n"
".Lmk_x97r:\n"
"v_mfma_f32_16x16x32_bf16 v[84:87], v[104:107], v[28:31], v[36:39]\n"
"ds_read_b64_tr_b16 v[100:101], v16 offset:10240\n"
"ds_read_b64_tr_b16 v[102:103], v255 offset:10496\n"
"v_exp_f32_e32 v64, v64\n"
"v_exp_f32_e32 v65, v65\n"
"v_mfma_f32_16x16x32_bf16 v[116:119], v[104:107], v[32:35], v[60:63]\n"
"buffer_load_dwordx4 a[116:119], v1, s[4:7], s98 offen offset:1024 sc1\n"
"v_exp_f32_e32 v66, v66\n"
"v_exp_f32_e32 v67, v67\n"
"v_mfma_f32_16x16x32_bf16 v[104:107], v[124:127], v[32:35], v[60:63]\n"
"ds_read_b64_tr_b16 v[60:61], v255 offset:10240\n"
"ds_read_b64_tr_b16 v[62:63], v16 offset:10496\n"
"v_exp_f32_e32 v128, v128\n"
"v_exp_f32_e32 v129, v129\n"
"v_mfma_f32_16x16x32_bf16 v[120:123], v[124:127], v[28:31], v[36:39]\n"
"ds_read_b64_tr_b16 v[36:37], v16 offset:24576\n"
"v_exp_f32_e32 v130, v130\n"
"v_exp_f32_e32 v131, v131\n"
"s_waitcnt lgkmcnt(5)\n"
"v_mfma_f32_16x16x32_bf16 v[132:135], v[88:91], v[144:147], v[132:135]\n"
"buffer_load_dwordx4 a[120:123], v1, s[4:7], s98 offen offset:2048 sc1\n"
"ds_read_b64_tr_b16 v[38:39], v255 offset:24832\n"
"v_cvt_pk_bf16_f32 v124, v64, v65\n"
"v_cvt_pk_bf16_f32 v125, v66, v67\n"
"v_cvt_pk_bf16_f32 v126, v128, v129\n"
"v_mfma_f32_16x16x32_bf16 v[44:47], v[140:143], v[144:147], v[44:47]\n"
"ds_read_b64_tr_b16 v[64:65], v255 offset:24576\n"
"v_cvt_pk_bf16_f32 v127, v130, v131\n"
"v_exp_f32_e32 v108, v108\n"
"v_exp_f32_e32 v109, v109\n"
"v_mfma_f32_16x16x32_bf16 v[112:115], v[72:75], v[144:147], v[112:115]\n"
"ds_read_b64_tr_b16 v[66:67], v16 offset:24832\n"
"v_exp_f32_e32 v110, v110\n"
"v_exp_f32_e32 v111, v111\n"
"v_mfma_f32_16x16x32_bf16 v[136:139], v[72:75], v[40:43], v[136:139]\n"
"buffer_load_dwordx4 a[124:127], v1, s[4:7], s98 offen offset:3072 sc1\n"
"v_exp_f32_e32 v76, v76\n"
"v_add_u32_e32 v248, 0x26400, v0\n"
"v_exp_f32_e32 v77, v77\n"
"v_mfma_f32_16x16x32_bf16 v[80:83], v[140:143], v[40:43], v[80:83]\n"
"ds_read_b128 v[72:75], v248\n"
"v_exp_f32_e32 v78, v78\n"
"v_exp_f32_e32 v79, v79\n"
"v_mfma_f32_16x16x32_bf16 v[68:71], v[88:91], v[40:43], v[68:71]\n"
"ds_read_b128 v[128:131], v248 offset:1024\n"
"v_cvt_pk_bf16_f32 v40, v108, v109\n"
"v_cvt_pk_bf16_f32 v41, v110, v111\n"
"v_cvt_pk_bf16_f32 v42, v76, v77\n"
"v_cvt_pk_bf16_f32 v43, v78, v79\n"
"s_waitcnt lgkmcnt(6)\n"
"v_mfma_f32_16x16x32_bf16 v[132:135], v[88:91], v[124:127], v[132:135]\n"
"ds_read_b64_tr_b16 v[76:77], v16 offset:8192\n"
"v_cndmask_b32_e64 v84, v48, v84, s[20:21]\n"
"v_cndmask_b32_e64 v85, v49, v85, s[22:23]\n"
"v_cndmask_b32_e64 v86, v50, v86, s[24:25]\n"
"s_add_u32 m0, s77, 0x4000\n"
"s_add_u32 s98, s65, 0\n"
"buffer_load_dwordx4 v2, s[4:7], s98 offen sc1 lds\n"
"buffer_load_dwordx4 v2, s[4:7], s98 offen offset:1024 sc1 lds\n"
"v_cndmask_b32_e64 v87, v51, v87, s[26:27]\n"
"v_cndmask_b32_e64 v120, v24, v120, s[28:29]\n"
"v_mfma_f32_16x16x32_bf16 v[44:47], v[100:103], v[124:127], v[44:47]\n"
"ds_read_b64_tr_b16 v[78:79], v255 offset:8448\n"
"v_cndmask_b32_e64 v121, v25, v121, s[30:31]\n"
"v_cndmask_b32_e64 v122, v26, v122, s[32:33]\n"
"v_cndmask_b32_e64 v123, v27, v123, s[34:35]\n"
"v_exp_f32_e32 v84, v84\n"
"v_exp_f32_e32 v85, v85\n"
"v_exp_f32_e32 v86, v86\n"
"v_exp_f32_e32 v87, v87\n"
"v_mfma_f32_16x16x32_bf16 v[112:115], v[60:63], v[124:127], v[112:115]\n"
"v_exp_f32_e32 v120, v120\n"
"v_exp_f32_e32 v121, v121\n"
"v_exp_f32_e32 v122, v122\n"
"v_exp_f32_e32 v123, v123\n"
"v_cvt_pk_bf16_f32 v48, v84, v85\n"
"v_cvt_pk_bf16_f32 v49, v86, v87\n"
"v_cvt_pk_bf16_f32 v50, v120, v121\n"
"v_mfma_f32_16x16x32_bf16 v[136:139], v[60:63], v[40:43], v[136:139]\n"
"ds_read_b64_tr_b16 v[60:61], v255 offset:8192\n"
"s_add_u32 s98, s69, 0x608000\n"
"buffer_load_dwordx4 v[24:27], v0, s[4:7], s98 offen sc1\n"
"v_cvt_pk_bf16_f32 v51, v122, v123\n"
"v_cndmask_b32_e64 v116, v52, v116, s[36:37]\n"
"v_cndmask_b32_e64 v117, v53, v117, s[38:39]\n"
"v_cndmask_b32_e64 v118, v54, v118, s[40:41]\n"
"v_cndmask_b32_e64 v119, v55, v119, s[42:43]\n"
"v_cndmask_b32_e64 v104, v56, v104, s[44:45]\n"
"v_cndmask_b32_e64 v105, v57, v105, s[46:47]\n"
"v_mfma_f32_16x16x32_bf16 v[80:83], v[100:103], v[40:43], v[80:83]\n"
"ds_read_b64_tr_b16 v[62:63], v16 offset:8448\n"
"v_cndmask_b32_e64 v106, v58, v106, s[48:49]\n"
"v_cndmask_b32_e64 v107, v59, v107, s[50:51]\n"
"v_exp_f32_e32 v116, v116\n"
"v_exp_f32_e32 v117, v117\n"
"v_exp_f32_e32 v118, v118\n"
"v_exp_f32_e32 v119, v119\n"
"v_exp_f32_e32 v104, v104\n"
"v_mfma_f32_16x16x32_bf16 v[68:71], v[88:91], v[40:43], v[68:71]\n"
"s_waitcnt lgkmcnt(0)\n"
"v_exp_f32_e32 v105, v105\n"
"v_exp_f32_e32 v106, v106\n"
"v_exp_f32_e32 v107, v107\n"
"v_cvt_pk_bf16_f32 v40, v116, v117\n"
"v_cvt_pk_bf16_f32 v41, v118, v119\n"
"v_cvt_pk_bf16_f32 v42, v104, v105\n"
"v_cvt_pk_bf16_f32 v43, v106, v107\n"
"v_mfma_f32_16x16x32_bf16 v[132:135], v[88:91], v[48:51], v[132:135]\n"
"v_and_b32_e32 v72, v48, v72\n"
"v_and_b32_e32 v73, v49, v73\n"
"v_and_b32_e32 v74, v50, v74\n"
"v_and_b32_e32 v75, v51, v75\n"
"v_mfma_f32_16x16x32_bf16 v[68:71], v[88:91], v[40:43], v[68:71]\n"
"v_xor_b32_e32 v52, v48, v72\n"
"v_xor_b32_e32 v53, v49, v73\n"
"v_xor_b32_e32 v54, v50, v74\n"
"v_xor_b32_e32 v55, v51, v75\n"
"v_mfma_f32_16x16x32_bf16 v[44:47], v[76:79], v[72:75], v[44:47]\n"
"v_and_b32_e32 v128, v40, v128\n"
"v_and_b32_e32 v129, v41, v129\n"
"v_mfma_f32_16x16x32_bf16 v[112:115], v[60:63], v[72:75], v[112:115]\n"
"v_and_b32_e32 v130, v42, v130\n"
"v_and_b32_e32 v131, v43, v131\n"
"v_mfma_f32_16x16x32_bf16 v[44:47], v[36:39], v[52:55], v[44:47]\n"
"v_xor_b32_e32 v48, v40, v128\n"
"v_xor_b32_e32 v49, v41, v129\n"
"v_mfma_f32_16x16x32_bf16 v[112:115], v[64:67], v[52:55], v[112:115]\n"
"v_xor_b32_e32 v50, v42, v130\n"
"v_xor_b32_e32 v51, v43, v131\n"
"v_mfma_f32_16x16x32_bf16 v[80:83], v[76:79], v[128:131], v[80:83]\n"
"s_add_u32 m0, s77, 0xc000\n"
"s_add_u32 s97, s65, 0x4000\n"
"buffer_load_dwordx4 v2, s[4:7], s97 offen sc1 lds\n"
"buffer_load_dwordx4 v2, s[4:7], s97 offen offset:1024 sc1 lds\n"
"v_mfma_f32_16x16x32_bf16 v[136:139], v[60:63], v[128:131], v[136:139]\n"
"v_mfma_f32_16x16x32_bf16 v[80:83], v[36:39], v[48:51], v[80:83]\n"
"v_mfma_f32_16x16x32_bf16 v[136:139], v[64:67], v[48:51], v[136:139]\n"
"v_max_f32_e32 v248, v132, v68\n"
"v_rcp_f32_e32 v132, v132\n"
"v_rcp_f32_e32 v68, v68\n"
"s_nop 0\n"
"v_max3_f32 v248, v248, v132, v68\n"
"v_cmp_nlt_f32_e32 vcc, v248, v252\n"
"s_cbranch_vccnz .Lmk_b92\n"
".Lmk_k98:\n"
"s_add_u32 s97, s60, 0x40000\n"
"v_mul_f32_e32 v44, v44, v132\n"
"v_mul_f32_e32 v45, v45, v132\n"
"v_mul_f32_e32 v46, v46, v132\n"
"v_mul_f32_e32 v47, v47, v132\n"
"v_mul_f32_e32 v112, v112, v132\n"
"v_mul_f32_e32 v113, v113, v132\n"
"v_mul_f32_e32 v114, v114, v132\n"
"v_mul_f32_e32 v115, v115, v132\n"
"v_cvt_pk_bf16_f32 v28, v44, v45\n"
"v_cvt_pk_bf16_f32 v29, v46, v47\n"
"v_cvt_pk_bf16_f32 v30, v112, v113\n"
"v_cvt_pk_bf16_f32 v31, v114, v115\n"
"buffer_store_dwordx4 v[28:31], v0, s[4:7], s97 offen sc1\n"
"s_add_u32 s97, s60, 0x48000\n"
"v_mul_f32_e32 v80, v80, v68\n"
"v_mul_f32_e32 v81, v81, v68\n"
"v_mul_f32_e32 v82, v82, v68\n"
"v_mul_f32_e32 v83, v83, v68\n"
"v_mul_f32_e32 v136, v136, v68\n"
"v_mul_f32_e32 v137, v137, v68\n"
"v_mul_f32_e32 v138, v138, v68\n"
"v_mul_f32_e32 v139, v139, v68\n"
"v_cvt_pk_bf16_f32 v28, v80, v81\n"
"v_cvt_pk_bf16_f32 v29, v82, v83\n"
"v_cvt_pk_bf16_f32 v30, v136, v137\n"
"v_cvt_pk_bf16_f32 v31, v138, v139\n"
"buffer_store_dwordx4 v[28:31], v0, s[4:7], s97 offen sc1\n"
";\n"
"; @A AT a\n"
"s_mov_b32 s2, 0\n"
"v_mov_b32_e32 v253, 0xff800000\n"
"v_mov_b32_e32 v251, 0xff800000\n"
"v_mov_b32_e32 v250, 0x7f800000\n"
"v_mov_b32_e32 v249, 0x7f800000\n"
".Lmk_a99:\n"
"ds_read_b128 v[28:31], v15 offset:16384\n"
"ds_read_b128 v[32:35], v254 offset:16640\n"
"ds_read_b128 v[36:39], v15 offset:14336\n"
"ds_read_b128 v[40:43], v254 offset:14592\n"
"s_cmp_gt_i32 s78, 7\n"
"s_cselect_b32 s97, 0x80000000, 0\n"
"s_or_b32 s97, s97, 0x7f800000\n"
"s_add_u32 s96, s67, 32\n"
"buffer_load_dwordx2 v[246:247], v12, s[4:7], s96 offen sc1\n"
"s_waitcnt lgkmcnt(2)\n"
"v_mfma_f32_16x16x32_bf16 v[100:103], v[28:31], v[92:95], 0\n"
"v_mfma_f32_16x16x32_bf16 v[104:107], v[28:31], v[96:99], 0\n"
"v_mfma_f32_16x16x32_bf16 v[108:111], v[32:35], v[96:99], 0\n"
"v_mfma_f32_16x16x32_bf16 v[112:115], v[32:35], v[92:95], 0\n"
"s_waitcnt lgkmcnt(0)\n"
"s_nop 3\n"
"v_max_f32_e32 v248, v100, v101\n"
"v_max_f32_e32 v245, v104, v105\n"
"v_max3_f32 v248, v248, v102, v103\n"
"v_max3_f32 v245, v245, v106, v107\n"
"v_max3_f32 v248, v248, v112, v113\n"
"v_max3_f32 v245, v245, v108, v109\n"
"v_max3_f32 v248, v248, v114, v115\n"
"v_max3_f32 v245, v245, v110, v111\n"
"v_mov_b32_e32 v244, v248\n"
"v_mov_b32_e32 v243, v245\n"
"s_nop 0\n"
"v_permlane16_swap_b32_e32 v248, v244\n"
"v_permlane16_swap_b32_e32 v245, v243\n"
"v_max_f32_e32 v248, v248, v244\n"
"v_max_f32_e32 v245, v245, v243\n"
"v_mov_b32_e32 v244, v248\n"
"v_mov_b32_e32 v243, v245\n"
"s_nop 0\n"
"v_permlane32_swap_b32_e32 v248, v244\n"
"v_permlane32_swap_b32_e32 v245, v243\n"
"v_max_f32_e32 v248, v248, v244\n"
"v_max_f32_e32 v245, v245, v243\n"
"v_med3_f32 v248, v248, v253, v250\n"
"v_med3_f32 v245, v245, v251, v249\n"
"v_min_f32_e64 v116, s97, -v248\n"
"v_min_f32_e64 v117, s97, -v248\n"
"v_min_f32_e64 v118, s97, -v248\n"
"v_min_f32_e64 v119, s97, -v248\n"
"v_min_f32_e64 v120, s97, -v245\n"
"v_min_f32_e64 v121, s97, -v245\n"
"v_min_f32_e64 v122, s97, -v245\n"
"v_min_f32_e64 v123, s97, -v245\n"
"v_mfma_f32_16x16x32_bf16 v[44:47], v[36:39], v[92:95], v[116:119]\n"
"ds_read_b128 v[32:35], v15 offset:12288\n"
"v_sub_f32_e32 v100, v100, v248\n"
"v_sub_f32_e32 v101, v101, v248\n"
"v_sub_f32_e32 v102, v102, v248\n"
"v_sub_f32_e32 v103, v103, v248\n"
"v_mfma_f32_16x16x32_bf16 v[48:51], v[36:39], v[96:99], v[120:123]\n"
"v_sub_f32_e32 v112, v112, v248\n"
"v_sub_f32_e32 v113, v113, v248\n"
"v_sub_f32_e32 v114, v114, v248\n"
"v_sub_f32_e32 v115, v115, v248\n"
"v_mfma_f32_16x16x32_bf16 v[52:55], v[40:43], v[96:99], v[120:123]\n"
"ds_read_b128 v[56:59], v254 offset:12544\n"
"v_sub_f32_e32 v104, v104, v245\n"
"v_sub_f32_e32 v105, v105, v245\n"
"v_sub_f32_e32 v106, v106, v245\n"
"v_sub_f32_e32 v107, v107, v245\n"
"v_mfma_f32_16x16x32_bf16 v[36:39], v[40:43], v[92:95], v[116:119]\n"
"s_waitcnt lgkmcnt(0)\n"
"v_sub_f32_e32 v108, v108, v245\n"
"v_sub_f32_e32 v109, v109, v245\n"
"v_sub_f32_e32 v110, v110, v245\n"
"v_sub_f32_e32 v111, v111, v245\n"
"s_cmp_eq_i32 s78, 7\n"
"s_cbranch_scc1 .Lmk_x101s\n"
".Lmk_x101r:\n"
"v_mfma_f32_16x16x32_bf16 v[60:63], v[32:35], v[92:95], v[116:119]\n"
"ds_read_b128 v[40:43], v15 offset:10240\n"
"buffer_load_dwordx4 v[28:31], v0, s[4:7], s98 offen offset:1024 sc1\n"
"ds_read_b128 v[64:67], v254 offset:10496\n"
"v_exp_f32_e32 v44, v44\n"
"v_exp_f32_e32 v45, v45\n"
"v_exp_f32_e32 v46, v46\n"
"v_exp_f32_e32 v47, v47\n"
"v_exp_f32_e32 v36, v36\n"
"v_mfma_f32_16x16x32_bf16 v[68:71], v[32:35], v[96:99], v[120:123]\n"
"ds_read_b64_tr_b16 v[72:73], v16 offset:14336\n"
"buffer_load_dwordx4 v[32:35], v0, s[4:7], s98 offen offset:2048 sc1\n"
"v_exp_f32_e32 v37, v37\n"
"v_exp_f32_e32 v38, v38\n"
"v_exp_f32_e32 v39, v39\n"
"v_cvt_pk_bf16_f32 v76, v44, v45\n"
"v_cvt_pk_bf16_f32 v77, v46, v47\n"
"v_cvt_pk_bf16_f32 v78, v36, v37\n"
"v_mfma_f32_16x16x32_bf16 v[80:83], v[56:59], v[96:99], v[120:123]\n"
"ds_read_b64_tr_b16 v[74:75], v255 offset:14592\n"
"ds_read_b64_tr_b16 v[84:85], v255 offset:14336\n"
"v_cvt_pk_bf16_f32 v79, v38, v39\n"
"v_exp_f32_e32 v48, v48\n"
"v_exp_f32_e32 v49, v49\n"
"v_exp_f32_e32 v50, v50\n"
"v_exp_f32_e32 v51, v51\n"
"v_mfma_f32_16x16x32_bf16 v[124:127], v[56:59], v[92:95], v[116:119]\n"
"buffer_load_dwordx4 v[36:39], v0, s[4:7], s98 offen offset:3072 sc1\n"
"ds_read_b64_tr_b16 v[86:87], v16 offset:14592\n"
"v_exp_f32_e32 v52, v52\n"
"v_exp_f32_e32 v53, v53\n"
"v_exp_f32_e32 v54, v54\n"
"v_exp_f32_e32 v55, v55\n"
"v_cvt_pk_bf16_f32 v56, v48, v49\n"
"v_cvt_pk_bf16_f32 v57, v50, v51\n"
"v_cvt_pk_bf16_f32 v58, v52, v53\n"
"v_cvt_pk_bf16_f32 v59, v54, v55\n"
"s_waitcnt lgkmcnt(4)\n"
"s_cmp_eq_i32 s78, 6\n"
"s_cbranch_scc1 .Lmk_x102s\n"
".Lmk_x102r:\n"
"v_mfma_f32_16x16x32_bf16 v[128:131], v[40:43], v[92:95], v[116:119]\n"
"ds_read_b128 v[132:135], v15 offset:8192\n"
"ds_read_b128 v[136:139], v254 offset:8448\n"
"v_exp_f32_e32 v60, v60\n"
"v_exp_f32_e32 v61, v61\n"
"v_mfma_f32_16x16x32_bf16 v[140:143], v[40:43], v[96:99], v[120:123]\n"
"buffer_load_dwordx4 v[40:43], v1, s[4:7], s98 offen sc1\n"
"v_exp_f32_e32 v62, v62\n"
"v_exp_f32_e32 v63, v63\n"
"v_mfma_f32_16x16x32_bf16 v[144:147], v[64:67], v[96:99], v[120:123]\n"
"ds_read_b64_tr_b16 v[148:149], v16 offset:12288\n"
"ds_read_b64_tr_b16 v[150:151], v255 offset:12544\n"
"v_exp_f32_e32 v124, v124\n"
"v_exp_f32_e32 v125, v125\n"
"v_mfma_f32_16x16x32_bf16 v[152:155], v[64:67], v[92:95], v[116:119]\n"
"ds_read_b64_tr_b16 v[64:65], v255 offset:12288\n"
"v_exp_f32_e32 v126, v126\n"
"v_exp_f32_e32 v127, v127\n"
"s_waitcnt lgkmcnt(5)\n"
"v_mfma_f32_16x16x32_bf16 v[156:159], v[88:91], v[76:79], 0\n"
"buffer_load_dwordx4 v[44:47], v1, s[4:7], s98 offen offset:1024 sc1\n"
"v_cvt_pk_bf16_f32 v160, v60, v61\n"
"v_cvt_pk_bf16_f32 v161, v62, v63\n"
"v_cvt_pk_bf16_f32 v162, v124, v125\n"
"v_cvt_pk_bf16_f32 v163, v126, v127\n"
"v_mfma_f32_16x16x32_bf16 v[124:127], v[72:75], v[76:79], 0\n"
"ds_read_b64_tr_b16 v[66:67], v16 offset:12544\n"
"s_waitcnt vmcnt(8)\n"
"ds_read_b128 v[164:167], v2 offset:16384\n"
"v_exp_f32_e32 v68, v68\n"
"v_exp_f32_e32 v69, v69\n"
"v_mfma_f32_16x16x32_bf16 v[168:171], v[84:87], v[76:79], 0\n"
"v_exp_f32_e32 v70, v70\n"
"v_exp_f32_e32 v71, v71\n"
"v_mfma_f32_16x16x32_bf16 v[172:175], v[84:87], v[56:59], 0\n"
"buffer_load_dwordx4 v[48:51], v1, s[4:7], s98 offen offset:2048 sc1\n"
"ds_read_b128 v[76:79], v2 offset:17408\n"
"v_exp_f32_e32 v80, v80\n"
"v_exp_f32_e32 v81, v81\n"
"v_mfma_f32_16x16x32_bf16 v[176:179], v[72:75], v[56:59], 0\n"
"ds_read_b128 v[72:75], v2 offset:49152\n"
"v_exp_f32_e32 v82, v82\n"
"v_exp_f32_e32 v83, v83\n"
"v_mfma_f32_16x16x32_bf16 v[180:183], v[88:91], v[56:59], 0\n"
"ds_read_b128 v[84:87], v2 offset:50176\n"
"v_cvt_pk_bf16_f32 v184, v68, v69\n"
"v_cvt_pk_bf16_f32 v185, v70, v71\n"
"v_cvt_pk_bf16_f32 v186, v80, v81\n"
"v_cvt_pk_bf16_f32 v187, v82, v83\n"
"s_waitcnt lgkmcnt(8)\n"
"s_cmp_eq_i32 s78, 5\n"
"s_cbranch_scc1 .Lmk_x103s\n"
".Lmk_x103r:\n"
"v_mfma_f32_16x16x32_bf16 v[80:83], v[132:135], v[92:95], v[116:119]\n"
"ds_read_b64_tr_b16 v[68:69], v16 offset:10240\n"
"ds_read_b64_tr_b16 v[70:71], v255 offset:10496\n"
"buffer_load_dwordx4 v[52:55], v1, s[4:7], s98 offen offset:3072 sc1\n"
"v_exp_f32_e32 v128, v128\n"
"v_exp_f32_e32 v129, v129\n"
"v_mfma_f32_16x16x32_bf16 v[188:191], v[132:135], v[96:99], v[120:123]\n"
"ds_read_b64_tr_b16 v[132:133], v255 offset:10240\n"
"v_exp_f32_e32 v130, v130\n"
"v_exp_f32_e32 v131, v131\n"
"v_mfma_f32_16x16x32_bf16 v[192:195], v[136:139], v[96:99], v[120:123]\n"
"ds_read_b64_tr_b16 v[134:135], v16 offset:10496\n"
"v_exp_f32_e32 v152, v152\n"
"v_exp_f32_e32 v153, v153\n"
"v_exp_f32_e32 v154, v154\n"
"v_mfma_f32_16x16x32_bf16 v[196:199], v[136:139], v[92:95], v[116:119]\n"
"s_waitcnt lgkmcnt(4)\n"
"s_add_u32 s97, s69, 0x60a000\n"
"buffer_load_dwordx4 v[56:59], v0, s[4:7], s97 offen sc1\n"
"v_max_u32_e32 v248, v164, v165\n"
"v_exp_f32_e32 v155, v155\n"
"v_mfma_f32_16x16x32_bf16 v[156:159], v[88:91], v[160:163], v[156:159]\n"
"v_max3_u32 v248, v248, v166, v167\n"
"v_cvt_pk_bf16_f32 v136, v128, v129\n"
"v_cvt_pk_bf16_f32 v137, v130, v131\n"
"v_cvt_pk_bf16_f32 v138, v152, v153\n"
"v_mfma_f32_16x16x32_bf16 v[124:127], v[148:151], v[160:163], v[124:127]\n"
"v_max3_u32 v248, v248, v76, v77\n"
"v_cvt_pk_bf16_f32 v139, v154, v155\n"
"v_exp_f32_e32 v140, v140\n"
"v_max3_u32 v248, v248, v78, v79\n"
"v_exp_f32_e32 v141, v141\n"
"v_mfma_f32_16x16x32_bf16 v[168:171], v[64:67], v[160:163], v[168:171]\n"
"v_exp_f32_e32 v142, v142\n"
"v_max3_u32 v248, v248, v72, v73\n"
"v_exp_f32_e32 v143, v143\n"
"v_mfma_f32_16x16x32_bf16 v[172:175], v[64:67], v[184:187], v[172:175]\n"
"buffer_load_dwordx4 v[60:63], v0, s[4:7], s97 offen offset:1024 sc1\n"
"v_max3_u32 v248, v248, v74, v75\n"
"v_exp_f32_e32 v144, v144\n"
"v_exp_f32_e32 v145, v145\n"
"v_mfma_f32_16x16x32_bf16 v[176:179], v[148:151], v[184:187], v[176:179]\n"
"v_max3_u32 v248, v248, v84, v85\n"
"v_exp_f32_e32 v146, v146\n"
"v_max3_u32 v248, v248, v86, v87\n"
"v_exp_f32_e32 v147, v147\n"
"v_mfma_f32_16x16x32_bf16 v[180:183], v[88:91], v[184:187], v[180:183]\n"
"v_cvt_pk_bf16_f32 v128, v140, v141\n"
"v_cmp_eq_u32_e32 vcc, -1, v248\n"
"v_cvt_pk_bf16_f32 v129, v142, v143\n"
"v_cvt_pk_bf16_f32 v130, v144, v145\n"
"v_cvt_pk_bf16_f32 v131, v146, v147\n"
"s_cbranch_vccnz .Lmk_h90s\n"
".Lmk_h90r:\n"
"s_cmp_lg_u32 s2, 0\n"
"s_cbranch_scc1 .Lmk_s104\n"
"s_barrier\n"
".Lmk_s104:\n"
"ds_read_b128 v[72:75], v15 offset:6144\n"
"ds_read_b128 v[76:79], v254 offset:6400\n"
"s_waitcnt lgkmcnt(2)\n"
"v_mfma_f32_16x16x32_bf16 v[156:159], v[88:91], v[136:139], v[156:159]\n"
"ds_read_b128 v[84:87], v15 offset:4096\n"
"v_exp_f32_e32 v80, v80\n"
"v_exp_f32_e32 v81, v81\n"
"v_exp_f32_e32 v82, v82\n"
"v_mfma_f32_16x16x32_bf16 v[124:127], v[68:71], v[136:139], v[124:127]\n"
"buffer_load_dwordx4 v[64:67], v0, s[4:7], s97 offen offset:2048 sc1\n"
"ds_read_b128 v[140:143], v254 offset:4352\n"
"v_exp_f32_e32 v83, v83\n"
"v_mfma_f32_16x16x32_bf16 v[168:171], v[132:135], v[136:139], v[168:171]\n"
"ds_read_b64_tr_b16 v[136:137], v16 offset:8192\n"
"v_exp_f32_e32 v196, v196\n"
"v_exp_f32_e32 v197, v197\n"
"v_mfma_f32_16x16x32_bf16 v[172:175], v[132:135], v[128:131], v[172:175]\n"
"ds_read_b64_tr_b16 v[138:139], v255 offset:8448\n"
"v_exp_f32_e32 v198, v198\n"
"v_exp_f32_e32 v199, v199\n"
"v_mfma_f32_16x16x32_bf16 v[176:179], v[68:71], v[128:131], v[176:179]\n"
"buffer_load_dwordx4 v[68:71], v0, s[4:7], s97 offen offset:3072 sc1\n"
"v_cvt_pk_bf16_f32 v132, v80, v81\n"
"v_cvt_pk_bf16_f32 v133, v82, v83\n"
"v_cvt_pk_bf16_f32 v134, v196, v197\n"
"v_cvt_pk_bf16_f32 v135, v198, v199\n"
"v_mfma_f32_16x16x32_bf16 v[180:183], v[88:91], v[128:131], v[180:183]\n"
"ds_read_b64_tr_b16 v[128:129], v255 offset:8192\n"
"v_exp_f32_e32 v188, v188\n"
"v_exp_f32_e32 v189, v189\n"
"s_waitcnt lgkmcnt(5)\n"
"v_mfma_f32_16x16x32_bf16 v[144:147], v[72:75], v[92:95], v[116:119]\n"
"ds_read_b64_tr_b16 v[130:131], v16 offset:8448\n"
"v_exp_f32_e32 v190, v190\n"
"v_exp_f32_e32 v191, v191\n"
"v_exp_f32_e32 v192, v192\n"
"v_mfma_f32_16x16x32_bf16 v[148:151], v[72:75], v[96:99], v[120:123]\n"
"buffer_load_dwordx4 v[72:75], v1, s[4:7], s97 offen sc1\n"
"s_waitcnt vmcnt(12)\n"
"v_cmp_ne_u64_e32 vcc, v[246:247], v[18:19]\n"
"v_mfma_f32_16x16x32_bf16 v[152:155], v[76:79], v[96:99], v[120:123]\n"
"v_exp_f32_e32 v193, v193\n"
"v_exp_f32_e32 v194, v194\n"
"v_exp_f32_e32 v195, v195\n"
"v_mfma_f32_16x16x32_bf16 v[160:163], v[76:79], v[92:95], v[116:119]\n"
"s_cbranch_vccnz .Lmk_f74s\n"
".Lmk_f74r:\n"
"v_cvt_pk_bf16_f32 v164, v188, v189\n"
"v_cvt_pk_bf16_f32 v165, v190, v191\n"
"v_cvt_pk_bf16_f32 v166, v192, v193\n"
"v_cvt_pk_bf16_f32 v167, v194, v195\n"
"s_waitcnt lgkmcnt(4)\n"
"v_mfma_f32_16x16x32_bf16 v[184:187], v[84:87], v[92:95], v[116:119]\n"
"ds_read_b128 v[188:191], v15 offset:2048\n"
"v_exp_f32_e32 v144, v144\n"
"v_exp_f32_e32 v145, v145\n"
"v_exp_f32_e32 v146, v146\n"
"v_mfma_f32_16x16x32_bf16 v[192:195], v[84:87], v[96:99], v[120:123]\n"
"buffer_load_dwordx4 v[76:79], v1, s[4:7], s97 offen offset:1024 sc1\n"
"v_exp_f32_e32 v147, v147\n"
"v_mfma_f32_16x16x32_bf16 v[196:199], v[140:143], v[96:99], v[120:123]\n"
"ds_read_b128 v[200:203], v254 offset:2304\n"
"v_exp_f32_e32 v160, v160\n"
"v_exp_f32_e32 v161, v161\n"
"v_mfma_f32_16x16x32_bf16 v[204:207], v[140:143], v[92:95], v[116:119]\n"
"ds_read_b64_tr_b16 v[140:141], v16 offset:6144\n"
"v_exp_f32_e32 v162, v162\n"
"v_exp_f32_e32 v163, v163\n"
"s_waitcnt lgkmcnt(3)\n"
"v_mfma_f32_16x16x32_bf16 v[156:159], v[88:91], v[132:135], v[156:159]\n"
"buffer_load_dwordx4 v[80:83], v1, s[4:7], s97 offen offset:2048 sc1\n"
"v_cvt_pk_bf16_f32 v208, v144, v145\n"
"v_cvt_pk_bf16_f32 v209, v146, v147\n"
"v_cvt_pk_bf16_f32 v210, v160, v161\n"
"v_mfma_f32_16x16x32_bf16 v[124:127], v[136:139], v[132:135], v[124:127]\n"
"ds_read_b64_tr_b16 v[142:143], v255 offset:6400\n"
"v_cvt_pk_bf16_f32 v211, v162, v163\n"
"v_exp_f32_e32 v148, v148\n"
"v_exp_f32_e32 v149, v149\n"
"v_mfma_f32_16x16x32_bf16 v[168:171], v[128:131], v[132:135], v[168:171]\n"
"ds_read_b64_tr_b16 v[132:133], v255 offset:6144\n"
"v_exp_f32_e32 v150, v150\n"
"v_exp_f32_e32 v151, v151\n"
"v_mfma_f32_16x16x32_bf16 v[172:175], v[128:131], v[164:167], v[172:175]\n"
"buffer_load_dwordx4 v[84:87], v1, s[4:7], s97 offen offset:3072 sc1\n"
"v_exp_f32_e32 v152, v152\n"
"v_exp_f32_e32 v153, v153\n"
"v_mfma_f32_16x16x32_bf16 v[176:179], v[136:139], v[164:167], v[176:179]\n"
"ds_read_b64_tr_b16 v[134:135], v16 offset:6400\n"
"v_exp_f32_e32 v154, v154\n"
"v_exp_f32_e32 v155, v155\n"
"v_mfma_f32_16x16x32_bf16 v[180:183], v[88:91], v[164:167], v[180:183]\n"
"s_waitcnt lgkmcnt(4)\n"
"v_cvt_pk_bf16_f32 v128, v148, v149\n"
"v_cvt_pk_bf16_f32 v129, v150, v151\n"
"v_cvt_pk_bf16_f32 v130, v152, v153\n"
"v_cvt_pk_bf16_f32 v131, v154, v155\n"
"v_mfma_f32_16x16x32_bf16 v[136:139], v[188:191], v[92:95], v[116:119]\n"
"ds_read_b128 v[144:147], v15\n"
"v_exp_f32_e32 v184, v184\n"
"v_exp_f32_e32 v185, v185\n"
"v_exp_f32_e32 v186, v186\n"
"v_mfma_f32_16x16x32_bf16 v[148:151], v[188:191], v[96:99], v[120:123]\n"
"s_add_u32 s97, s56, 0x40000\n"
"buffer_load_dwordx4 v[152:155], v0, s[4:7], s97 offen sc1\n"
"v_exp_f32_e32 v187, v187\n"
"v_mfma_f32_16x16x32_bf16 v[160:163], v[200:203], v[96:99], v[120:123]\n"
"ds_read_b128 v[164:167], v254 offset:256\n"
"v_exp_f32_e32 v204, v204\n"
"v_exp_f32_e32 v205, v205\n"
"v_exp_f32_e32 v206, v206\n"
"v_mfma_f32_16x16x32_bf16 v[188:191], v[200:203], v[92:95], v[116:119]\n"
"buffer_load_dwordx4 v[200:203], v0, s[4:7], s97 offen offset:1024 sc1\n"
"ds_read_b64_tr_b16 v[212:213], v16 offset:4096\n"
"v_exp_f32_e32 v207, v207\n"
"s_waitcnt lgkmcnt(3)\n"
"v_mfma_f32_16x16x32_bf16 v[156:159], v[88:91], v[208:211], v[156:159]\n"
"v_cvt_pk_bf16_f32 v216, v184, v185\n"
"v_cvt_pk_bf16_f32 v217, v186, v187\n"
"v_cvt_pk_bf16_f32 v218, v204, v205\n"
"v_cvt_pk_bf16_f32 v219, v206, v207\n"
"v_mfma_f32_16x16x32_bf16 v[124:127], v[140:143], v[208:211], v[124:127]\n"
"buffer_load_dwordx4 v[184:187], v0, s[4:7], s97 offen offset:2048 sc1\n"
"ds_read_b64_tr_b16 v[214:215], v255 offset:4352\n"
"v_exp_f32_e32 v192, v192\n"
"v_exp_f32_e32 v193, v193\n"
"v_mfma_f32_16x16x32_bf16 v[168:171], v[132:135], v[208:211], v[168:171]\n"
"buffer_load_dwordx4 v[204:207], v0, s[4:7], s97 offen offset:3072 sc1\n"
"v_exp_f32_e32 v194, v194\n"
"v_exp_f32_e32 v195, v195\n"
"v_mfma_f32_16x16x32_bf16 v[172:175], v[132:135], v[128:131], v[172:175]\n"
"ds_read_b64_tr_b16 v[132:133], v255 offset:4096\n"
"v_exp_f32_e32 v196, v196\n"
"v_exp_f32_e32 v197, v197\n"
"v_mfma_f32_16x16x32_bf16 v[176:179], v[140:143], v[128:131], v[176:179]\n"
"buffer_load_dwordx4 v[140:143], v1, s[4:7], s97 offen sc1\n"
"ds_read_b64_tr_b16 v[134:135], v16 offset:4352\n"
"v_exp_f32_e32 v198, v198\n"
"v_exp_f32_e32 v199, v199\n"
"v_mfma_f32_16x16x32_bf16 v[180:183], v[88:91], v[128:131], v[180:183]\n"
"s_waitcnt lgkmcnt(4)\n"
"v_cvt_pk_bf16_f32 v128, v192, v193\n"
"v_cvt_pk_bf16_f32 v129, v194, v195\n"
"v_cvt_pk_bf16_f32 v130, v196, v197\n"
"v_cvt_pk_bf16_f32 v131, v198, v199\n"
"v_mfma_f32_16x16x32_bf16 v[192:195], v[144:147], v[92:95], v[116:119]\n"
"ds_read_b64_tr_b16 v[196:197], v16 offset:2048\n"
"ds_read_b64_tr_b16 v[198:199], v255 offset:2304\n"
"buffer_load_dwordx4 v[208:211], v1, s[4:7], s97 offen offset:1024 sc1\n"
"v_exp_f32_e32 v136, v136\n"
"v_exp_f32_e32 v137, v137\n"
"v_mfma_f32_16x16x32_bf16 v[220:223], v[144:147], v[96:99], v[120:123]\n"
"ds_read_b64_tr_b16 v[144:145], v255 offset:2048\n"
"v_exp_f32_e32 v138, v138\n"
"v_exp_f32_e32 v139, v139\n"
"v_mfma_f32_16x16x32_bf16 v[224:227], v[164:167], v[96:99], v[120:123]\n"
"buffer_load_dwordx4 v[120:123], v1, s[4:7], s97 offen offset:2048 sc1\n"
"v_exp_f32_e32 v188, v188\n"
"v_exp_f32_e32 v189, v189\n"
"v_mfma_f32_16x16x32_bf16 v[228:231], v[164:167], v[92:95], v[116:119]\n"
"ds_read_b64_tr_b16 v[146:147], v16 offset:2304\n"
"ds_read_b64_tr_b16 v[116:117], v16 offset:16384\n"
"v_exp_f32_e32 v190, v190\n"
"v_exp_f32_e32 v191, v191\n"
"s_waitcnt lgkmcnt(5)\n"
"v_mfma_f32_16x16x32_bf16 v[156:159], v[88:91], v[216:219], v[156:159]\n"
"v_cvt_pk_bf16_f32 v164, v136, v137\n"
"v_cvt_pk_bf16_f32 v165, v138, v139\n"
"v_cvt_pk_bf16_f32 v166, v188, v189\n"
"v_mfma_f32_16x16x32_bf16 v[124:127], v[212:215], v[216:219], v[124:127]\n"
"buffer_load_dwordx4 v[136:139], v1, s[4:7], s97 offen offset:3072 sc1\n"
"ds_read_b64_tr_b16 v[118:119], v255 offset:16640\n"
"ds_read_b64_tr_b16 v[188:189], v255 offset:16384\n"
"v_cvt_pk_bf16_f32 v167, v190, v191\n"
"v_exp_f32_e32 v148, v148\n"
"v_exp_f32_e32 v149, v149\n"
"v_exp_f32_e32 v150, v150\n"
"v_mfma_f32_16x16x32_bf16 v[168:171], v[132:135], v[216:219], v[168:171]\n"
"s_add_u32 s97, s57, 0x40000\n"
"buffer_load_dwordx4 v[216:219], v0, s[4:7], s97 offen sc1\n"
"ds_read_b64_tr_b16 v[190:191], v16 offset:16640\n"
"v_exp_f32_e32 v151, v151\n"
"v_mfma_f32_16x16x32_bf16 v[172:175], v[132:135], v[128:131], v[172:175]\n"
"v_exp_f32_e32 v160, v160\n"
"v_add_u32_e32 v248, 0x26400, v0\n"
"v_exp_f32_e32 v161, v161\n"
"v_mfma_f32_16x16x32_bf16 v[176:179], v[212:215], v[128:131], v[176:179]\n"
"buffer_load_dwordx4 v[132:135], v0, s[4:7], s97 offen offset:1024 sc1\n"
"ds_read_b128 v[212:215], v248\n"
"v_exp_f32_e32 v162, v162\n"
"v_exp_f32_e32 v163, v163\n"
"v_mfma_f32_16x16x32_bf16 v[180:183], v[88:91], v[128:131], v[180:183]\n"
"ds_read_b128 v[232:235], v248 offset:1024\n"
"v_cvt_pk_bf16_f32 v128, v148, v149\n"
"v_cvt_pk_bf16_f32 v129, v150, v151\n"
"v_cvt_pk_bf16_f32 v130, v160, v161\n"
"v_cvt_pk_bf16_f32 v131, v162, v163\n"
"s_waitcnt lgkmcnt(6)\n"
"v_mfma_f32_16x16x32_bf16 v[156:159], v[88:91], v[164:167], v[156:159]\n"
"ds_read_b64_tr_b16 v[148:149], v16\n"
"buffer_load_dwordx4 v[160:163], v0, s[4:7], s97 offen offset:2048 sc1\n"
"v_cndmask_b32_e64 v192, v100, v192, s[20:21]\n"
"v_cndmask_b32_e64 v193, v101, v193, s[22:23]\n"
"v_cndmask_b32_e64 v194, v102, v194, s[24:25]\n"
"v_cndmask_b32_e64 v195, v103, v195, s[26:27]\n"
"v_cndmask_b32_e64 v228, v112, v228, s[28:29]\n"
"v_cndmask_b32_e64 v229, v113, v229, s[30:31]\n"
"v_cndmask_b32_e64 v230, v114, v230, s[32:33]\n"
"v_cndmask_b32_e64 v231, v115, v231, s[34:35]\n"
"v_exp_f32_e32 v192, v192\n"
"v_mfma_f32_16x16x32_bf16 v[124:127], v[196:199], v[164:167], v[124:127]\n"
"ds_read_b64_tr_b16 v[150:151], v255 offset:256\n"
"buffer_load_dwordx4 v[100:103], v0, s[4:7], s97 offen offset:3072 sc1\n"
"v_exp_f32_e32 v193, v193\n"
"v_exp_f32_e32 v194, v194\n"
"v_exp_f32_e32 v195, v195\n"
"v_exp_f32_e32 v228, v228\n"
"v_mfma_f32_16x16x32_bf16 v[168:171], v[144:147], v[164:167], v[168:171]\n"
"buffer_load_dwordx4 v[164:167], v1, s[4:7], s97 offen sc1\n"
"v_exp_f32_e32 v229, v229\n"
"v_exp_f32_e32 v230, v230\n"
"v_exp_f32_e32 v231, v231\n"
"v_cvt_pk_bf16_f32 v112, v192, v193\n"
"v_cvt_pk_bf16_f32 v113, v194, v195\n"
"v_cvt_pk_bf16_f32 v114, v228, v229\n"
"v_mfma_f32_16x16x32_bf16 v[172:175], v[144:147], v[128:131], v[172:175]\n"
"ds_read_b64_tr_b16 v[144:145], v255\n"
"v_cvt_pk_bf16_f32 v115, v230, v231\n"
"v_cndmask_b32_e64 v220, v104, v220, s[36:37]\n"
"v_cndmask_b32_e64 v221, v105, v221, s[38:39]\n"
"v_cndmask_b32_e64 v222, v106, v222, s[40:41]\n"
"v_cndmask_b32_e64 v223, v107, v223, s[42:43]\n"
"v_cndmask_b32_e64 v224, v108, v224, s[44:45]\n"
"v_cndmask_b32_e64 v225, v109, v225, s[46:47]\n"
"v_cndmask_b32_e64 v226, v110, v226, s[48:49]\n"
"v_cndmask_b32_e64 v227, v111, v227, s[50:51]\n"
"v_mfma_f32_16x16x32_bf16 v[176:179], v[196:199], v[128:131], v[176:179]\n"
"buffer_load_dwordx4 v[104:107], v1, s[4:7], s97 offen offset:1024 sc1\n"
"ds_read_b64_tr_b16 v[146:147], v16 offset:256\n"
"buffer_load_dwordx4 v[108:111], v1, s[4:7], s97 offen offset:2048 sc1\n"
"v_exp_f32_e32 v220, v220\n"
"v_exp_f32_e32 v221, v221\n"
"v_exp_f32_e32 v222, v222\n"
"v_exp_f32_e32 v223, v223\n"
"v_exp_f32_e32 v224, v224\n"
"v_mfma_f32_16x16x32_bf16 v[180:183], v[88:91], v[128:131], v[180:183]\n"
"s_waitcnt lgkmcnt(0)\n"
"v_exp_f32_e32 v225, v225\n"
"v_exp_f32_e32 v226, v226\n"
"v_exp_f32_e32 v227, v227\n"
"v_cvt_pk_bf16_f32 v128, v220, v221\n"
"v_cvt_pk_bf16_f32 v129, v222, v223\n"
"v_cvt_pk_bf16_f32 v130, v224, v225\n"
"v_cvt_pk_bf16_f32 v131, v226, v227\n"
"v_mfma_f32_16x16x32_bf16 v[156:159], v[88:91], v[112:115], v[156:159]\n"
"v_and_b32_e32 v212, v112, v212\n"
"v_and_b32_e32 v213, v113, v213\n"
"v_and_b32_e32 v214, v114, v214\n"
"v_and_b32_e32 v215, v115, v215\n"
"v_mfma_f32_16x16x32_bf16 v[180:183], v[88:91], v[128:131], v[180:183]\n"
"v_xor_b32_e32 v192, v112, v212\n"
"v_xor_b32_e32 v193, v113, v213\n"
"v_xor_b32_e32 v194, v114, v214\n"
"v_xor_b32_e32 v195, v115, v215\n"
"v_mfma_f32_16x16x32_bf16 v[124:127], v[148:151], v[212:215], v[124:127]\n"
"v_and_b32_e32 v232, v128, v232\n"
"v_and_b32_e32 v233, v129, v233\n"
"v_mfma_f32_16x16x32_bf16 v[168:171], v[144:147], v[212:215], v[168:171]\n"
"v_and_b32_e32 v234, v130, v234\n"
"v_and_b32_e32 v235, v131, v235\n"
"v_mfma_f32_16x16x32_bf16 v[124:127], v[116:119], v[192:195], v[124:127]\n"
"v_xor_b32_e32 v112, v128, v232\n"
"v_xor_b32_e32 v113, v129, v233\n"
"v_mfma_f32_16x16x32_bf16 v[168:171], v[188:191], v[192:195], v[168:171]\n"
"v_xor_b32_e32 v114, v130, v234\n"
"v_xor_b32_e32 v115, v131, v235\n"
"v_mfma_f32_16x16x32_bf16 v[176:179], v[148:151], v[232:235], v[176:179]\n"
"buffer_load_dwordx4 v[128:131], v1, s[4:7], s97 offen offset:3072 sc1\n"
"s_add_u32 s97, s58, 0x40000\n"
"buffer_load_dwordx4 v[148:151], v0, s[4:7], s97 offen sc1\n"
"v_mfma_f32_16x16x32_bf16 v[172:175], v[144:147], v[232:235], v[172:175]\n"
"buffer_load_dwordx4 v[144:147], v0, s[4:7], s97 offen offset:1024 sc1\n"
"v_mfma_f32_16x16x32_bf16 v[176:179], v[116:119], v[112:115], v[176:179]\n"
"buffer_load_dwordx4 v[116:119], v0, s[4:7], s97 offen offset:2048 sc1\n"
"v_mfma_f32_16x16x32_bf16 v[172:175], v[188:191], v[112:115], v[172:175]\n"
"buffer_load_dwordx4 v[112:115], v0, s[4:7], s97 offen offset:3072 sc1\n"
"v_max_f32_e32 v248, v156, v180\n"
"v_rcp_f32_e32 v156, v156\n"
"v_rcp_f32_e32 v180, v180\n"
"s_nop 0\n"
"v_max3_f32 v248, v248, v156, v180\n"
"v_cmp_nlt_f32_e32 vcc, v248, v252\n"
"s_cbranch_vccnz .Lmk_b100\n"
".Lmk_k105:\n"
"v_mul_f32_e32 v124, v124, v156\n"
"v_mul_f32_e32 v125, v125, v156\n"
"v_mul_f32_e32 v126, v126, v156\n"
"v_mul_f32_e32 v127, v127, v156\n"
"v_mul_f32_e32 v168, v168, v156\n"
"v_mul_f32_e32 v169, v169, v156\n"
"v_mul_f32_e32 v170, v170, v156\n"
"v_mul_f32_e32 v171, v171, v156\n"
"v_cvt_pk_bf16_f32 v88, v124, v125\n"
"v_cvt_pk_bf16_f32 v89, v126, v127\n"
"v_cvt_pk_bf16_f32 v90, v168, v169\n"
"v_cvt_pk_bf16_f32 v91, v170, v171\n"
"buffer_store_dwordx4 v[88:91], v0, s[4:7], s60 offen sc1\n"
"s_add_u32 s98, s60, 0x8000\n"
"v_mul_f32_e32 v176, v176, v180\n"
"v_mul_f32_e32 v177, v177, v180\n"
"v_mul_f32_e32 v178, v178, v180\n"
"v_mul_f32_e32 v179, v179, v180\n"
"v_mul_f32_e32 v172, v172, v180\n"
"v_mul_f32_e32 v173, v173, v180\n"
"v_mul_f32_e32 v174, v174, v180\n"
"v_mul_f32_e32 v175, v175, v180\n"
"v_cvt_pk_bf16_f32 v88, v176, v177\n"
"v_cvt_pk_bf16_f32 v89, v178, v179\n"
"v_cvt_pk_bf16_f32 v90, v172, v173\n"
"v_cvt_pk_bf16_f32 v91, v174, v175\n"
"buffer_store_dwordx4 v[88:91], v0, s[4:7], s98 offen sc1\n"
";\n"
"s_waitcnt vmcnt(20)\n"
"; @A G2 b.0\n"
"s_nop 0\n"
"v_mfma_f32_16x16x32_bf16 v[88:91], a[128:131], v[152:155], 0\n"
"v_mfma_f32_16x16x32_bf16 v[88:91], a[132:135], v[200:203], v[88:91]\n"
"s_waitcnt vmcnt(18)\n"
"v_mfma_f32_16x16x32_bf16 v[88:91], a[136:139], v[184:187], v[88:91]\n"
"buffer_load_dwordx4 v[92:95], v1, s[4:7], s97 offen sc1\n"
"v_mfma_f32_16x16x32_bf16 v[88:91], a[140:143], v[204:207], v[88:91]\n"
"s_waitcnt vmcnt(17)\n"
"v_mfma_f32_16x16x32_bf16 v[88:91], a[144:147], v[140:143], v[88:91]\n"
"v_mfma_f32_16x16x32_bf16 v[88:91], a[148:151], v[208:211], v[88:91]\n"
"s_waitcnt vmcnt(15)\n"
"v_mfma_f32_16x16x32_bf16 v[88:91], a[152:155], v[120:123], v[88:91]\n"
"buffer_load_dwordx4 v[96:99], v1, s[4:7], s97 offen offset:1024 sc1\n"
"v_mfma_f32_16x16x32_bf16 v[88:91], a[156:159], v[136:139], v[88:91]\n"
"v_mfma_f32_16x16x32_bf16 v[124:127], a[188:191], v[136:139], 0\n"
"v_mfma_f32_16x16x32_bf16 v[124:127], a[184:187], v[120:123], v[124:127]\n"
"v_mfma_f32_16x16x32_bf16 v[124:127], a[180:183], v[208:211], v[124:127]\n"
"buffer_load_dwordx4 v[156:159], v1, s[4:7], s97 offen offset:2048 sc1\n"
"v_mfma_f32_16x16x32_bf16 v[124:127], a[176:179], v[140:143], v[124:127]\n"
"v_mfma_f32_16x16x32_bf16 v[124:127], a[172:175], v[204:207], v[124:127]\n"
"s_nop 1\n"
"v_cmp_u_f32_e32 vcc, v88, v88\n"
"v_mfma_f32_16x16x32_bf16 v[124:127], a[168:171], v[184:187], v[124:127]\n"
"s_cbranch_vccnz .Lmk_p106s\n"
".Lmk_p106r:\n"
"v_mfma_f32_16x16x32_bf16 v[124:127], a[164:167], v[200:203], v[124:127]\n"
"ds_write_b128 v3, v[88:91]\n"
"v_mfma_f32_16x16x32_bf16 v[124:127], a[160:163], v[152:155], v[124:127]\n"
"buffer_load_dwordx4 v[88:91], v1, s[4:7], s97 offen offset:3072 sc1\n"
"v_mfma_f32_16x16x32_bf16 v[168:171], a[192:195], v[152:155], 0\n"
"v_mfma_f32_16x16x32_bf16 v[168:171], a[196:199], v[200:203], v[168:171]\n"
"s_add_u32 s98, s59, 0x40000\n"
"v_mfma_f32_16x16x32_bf16 v[168:171], a[200:203], v[184:187], v[168:171]\n"
"v_mfma_f32_16x16x32_bf16 v[168:171], a[204:207], v[204:207], v[168:171]\n"
"buffer_load_dwordx4 v[172:175], v0, s[4:7], s98 offen sc1\n"
"v_mfma_f32_16x16x32_bf16 v[168:171], a[208:211], v[140:143], v[168:171]\n"
"v_mfma_f32_16x16x32_bf16 v[168:171], a[212:215], v[208:211], v[168:171]\n"
"ds_write_b128 v3, v[124:127] offset:1024\n"
"v_mfma_f32_16x16x32_bf16 v[168:171], a[216:219], v[120:123], v[168:171]\n"
"v_mfma_f32_16x16x32_bf16 v[168:171], a[220:223], v[136:139], v[168:171]\n"
"buffer_load_dwordx4 v[124:127], v0, s[4:7], s98 offen offset:1024 sc1\n"
"v_mfma_f32_16x16x32_bf16 v[176:179], a[252:255], v[136:139], 0\n"
"v_mfma_f32_16x16x32_bf16 v[176:179], a[248:251], v[120:123], v[176:179]\n"
"v_mfma_f32_16x16x32_bf16 v[176:179], a[244:247], v[208:211], v[176:179]\n"
"v_mfma_f32_16x16x32_bf16 v[176:179], a[240:243], v[140:143], v[176:179]\n"
"buffer_load_dwordx4 v[120:123], v0, s[4:7], s98 offen offset:2048 sc1\n"
"v_mfma_f32_16x16x32_bf16 v[176:179], a[236:239], v[204:207], v[176:179]\n"
"v_mfma_f32_16x16x32_bf16 v[176:179], a[232:235], v[184:187], v[176:179]\n"
"ds_write_b128 v3, v[168:171] offset:2048\n"
"v_mfma_f32_16x16x32_bf16 v[176:179], a[228:231], v[200:203], v[176:179]\n"
"v_mfma_f32_16x16x32_bf16 v[176:179], a[224:227], v[152:155], v[176:179]\n"
"s_waitcnt vmcnt(17)\n"
"buffer_load_dwordx4 v[136:139], v0, s[4:7], s98 offen offset:3072 sc1\n"
"; @A G2 b.1\n"
"v_mfma_f32_16x16x32_bf16 v[140:143], a[128:131], v[216:219], 0\n"
"v_mfma_f32_16x16x32_bf16 v[140:143], a[132:135], v[132:135], v[140:143]\n"
"v_mfma_f32_16x16x32_bf16 v[140:143], a[136:139], v[160:163], v[140:143]\n"
"v_mfma_f32_16x16x32_bf16 v[140:143], a[140:143], v[100:103], v[140:143]\n"
"s_waitcnt vmcnt(14)\n"
"buffer_load_dwordx4 v[152:155], v1, s[4:7], s98 offen sc1\n"
"v_mfma_f32_16x16x32_bf16 v[140:143], a[144:147], v[164:167], v[140:143]\n"
"v_mfma_f32_16x16x32_bf16 v[140:143], a[148:151], v[104:107], v[140:143]\n"
"ds_write_b128 v3, v[176:179] offset:3072\n"
"v_mfma_f32_16x16x32_bf16 v[140:143], a[152:155], v[108:111], v[140:143]\n"
"v_mfma_f32_16x16x32_bf16 v[140:143], a[156:159], v[128:131], v[140:143]\n"
"buffer_load_dwordx4 v[168:171], v1, s[4:7], s98 offen offset:1024 sc1\n"
"v_mfma_f32_16x16x32_bf16 v[176:179], a[188:191], v[128:131], 0\n"
"v_mfma_f32_16x16x32_bf16 v[176:179], a[184:187], v[108:111], v[176:179]\n"
"v_mfma_f32_16x16x32_bf16 v[176:179], a[180:183], v[104:107], v[176:179]\n"
"v_mfma_f32_16x16x32_bf16 v[176:179], a[176:179], v[164:167], v[176:179]\n"
"buffer_load_dwordx4 v[180:183], v1, s[4:7], s98 offen offset:2048 sc1\n"
"v_mfma_f32_16x16x32_bf16 v[176:179], a[172:175], v[100:103], v[176:179]\n"
"s_nop 0\n"
"v_cmp_u_f32_e32 vcc, v140, v140\n"
"v_mfma_f32_16x16x32_bf16 v[176:179], a[168:171], v[160:163], v[176:179]\n"
"s_cbranch_vccnz .Lmk_p107s\n"
".Lmk_p107r:\n"
"v_mfma_f32_16x16x32_bf16 v[176:179], a[164:167], v[132:135], v[176:179]\n"
"ds_write_b128 v3, v[140:143] offset:6144\n"
"v_mfma_f32_16x16x32_bf16 v[176:179], a[160:163], v[216:219], v[176:179]\n"
"buffer_load_dwordx4 v[140:143], v1, s[4:7], s98 offen offset:3072 sc1\n"
"v_mfma_f32_16x16x32_bf16 v[184:187], a[192:195], v[216:219], 0\n"
"v_mfma_f32_16x16x32_bf16 v[184:187], a[196:199], v[132:135], v[184:187]\n"
"v_mfma_f32_16x16x32_bf16 v[184:187], a[200:203], v[160:163], v[184:187]\n"
"v_mfma_f32_16x16x32_bf16 v[184:187], a[204:207], v[100:103], v[184:187]\n"
"v_mfma_f32_16x16x32_bf16 v[184:187], a[208:211], v[164:167], v[184:187]\n"
"ds_read_b128 v[188:191], v2 offset:8192\n"
"ds_read_b128 v[192:195], v2 offset:9216\n"
"v_mfma_f32_16x16x32_bf16 v[184:187], a[212:215], v[104:107], v[184:187]\n"
"ds_write_b128 v3, v[176:179] offset:7168\n"
"v_mfma_f32_16x16x32_bf16 v[184:187], a[216:219], v[108:111], v[184:187]\n"
"v_mfma_f32_16x16x32_bf16 v[184:187], a[220:223], v[128:131], v[184:187]\n"
"v_mfma_f32_16x16x32_bf16 v[176:179], a[252:255], v[128:131], 0\n"
"v_mfma_f32_16x16x32_bf16 v[176:179], a[248:251], v[108:111], v[176:179]\n"
"s_waitcnt lgkmcnt(1)\n"
"v_lshlrev_b32_e32 v255, 16, v188\n"
"v_and_b32_e32 v254, 0xffff0000, v188\n"
"v_mfma_f32_16x16x32_bf16 v[176:179], a[244:247], v[104:107], v[176:179]\n"
"v_lshlrev_b32_e32 v253, 16, v189\n"
"v_and_b32_e32 v252, 0xffff0000, v189\n"
"v_mfma_f32_16x16x32_bf16 v[176:179], a[240:243], v[164:167], v[176:179]\n"
"v_lshlrev_b32_e32 v251, 16, v190\n"
"v_and_b32_e32 v250, 0xffff0000, v190\n"
"v_mfma_f32_16x16x32_bf16 v[176:179], a[236:239], v[100:103], v[176:179]\n"
"v_lshlrev_b32_e32 v249, 16, v191\n"
"v_and_b32_e32 v248, 0xffff0000, v191\n"
"v_mfma_f32_16x16x32_bf16 v[176:179], a[232:235], v[160:163], v[176:179]\n"
"ds_write_b128 v3, v[184:187] offset:8192\n"
"v_mfma_f32_16x16x32_bf16 v[176:179], a[228:231], v[132:135], v[176:179]\n"
"v_lshlrev_b32_e32 v247, 16, v192\n"
"v_and_b32_e32 v246, 0xffff0000, v192\n"
"v_mfma_f32_16x16x32_bf16 v[176:179], a[224:227], v[216:219], v[176:179]\n"
"s_waitcnt vmcnt(14)\n"
"v_lshlrev_b32_e32 v245, 16, v193\n"
"v_and_b32_e32 v244, 0xffff0000, v193\n"
"; @A G2 b.2\n"
"v_mfma_f32_16x16x32_bf16 v[100:103], a[128:131], v[148:151], 0\n"
"v_lshlrev_b32_e32 v243, 16, v194\n"
"v_and_b32_e32 v242, 0xffff0000, v194\n"
"v_mfma_f32_16x16x32_bf16 v[100:103], a[132:135], v[144:147], v[100:103]\n"
"v_lshlrev_b32_e32 v241, 16, v195\n"
"v_and_b32_e32 v240, 0xffff0000, v195\n"
"v_mfma_f32_16x16x32_bf16 v[100:103], a[136:139], v[116:119], v[100:103]\n"
"v_mfma_f32_16x16x32_bf16 v[100:103], a[140:143], v[112:115], v[100:103]\n"
"s_waitcnt vmcnt(8)\n"
"v_mfma_f32_16x16x32_bf16 v[100:103], a[144:147], v[92:95], v[100:103]\n"
"v_mfma_f32_16x16x32_bf16 v[100:103], a[148:151], v[96:99], v[100:103]\n"
"ds_write_b128 v3, v[176:179] offset:9216\n"
"v_mfma_f32_16x16x32_bf16 v[100:103], a[152:155], v[156:159], v[100:103]\n"
"v_mfma_f32_16x16x32_bf16 v[100:103], a[156:159], v[88:91], v[100:103]\n"
"v_mfma_f32_16x16x32_bf16 v[104:107], a[188:191], v[88:91], 0\n"
"v_mfma_f32_16x16x32_bf16 v[104:107], a[184:187], v[156:159], v[104:107]\n"
"s_waitcnt lgkmcnt(0)\n"
"s_barrier\n"
"v_mfma_f32_16x16x32_bf16 v[104:107], a[180:183], v[96:99], v[104:107]\n"
"ds_read_b128 v[108:111], v4\n"
"ds_read_b128 v[128:131], v5\n"
"v_mfma_f32_16x16x32_bf16 v[104:107], a[176:179], v[92:95], v[104:107]\n"
"v_mfma_f32_16x16x32_bf16 v[104:107], a[172:175], v[112:115], v[104:107]\n"
"s_add_u32 s98, s56, 0\n"
"v_cmp_u_f32_e32 vcc, v100, v100\n"
"v_mfma_f32_16x16x32_bf16 v[104:107], a[168:171], v[116:119], v[104:107]\n"
"s_cbranch_vccnz .Lmk_p108s\n"
".Lmk_p108r:\n"
"v_mfma_f32_16x16x32_bf16 v[104:107], a[164:167], v[144:147], v[104:107]\n"
"ds_write_b128 v3, v[100:103] offset:12288\n"
"v_mfma_f32_16x16x32_bf16 v[104:107], a[160:163], v[148:151], v[104:107]\n"
"buffer_load_dwordx4 v[100:103], v0, s[4:7], s98 offen sc1\n"
"v_mfma_f32_16x16x32_bf16 v[132:135], a[192:195], v[148:151], 0\n"
"ds_read_b128 v[160:163], v4 offset:1024\n"
"ds_read_b128 v[164:167], v5 offset:1024\n"
"v_mfma_f32_16x16x32_bf16 v[132:135], a[196:199], v[144:147], v[132:135]\n"
"s_waitcnt lgkmcnt(3)\n"
"v_add_f32_e32 v108, v108, v255\n"
"v_add_f32_e32 v109, v109, v254\n"
"v_mfma_f32_16x16x32_bf16 v[132:135], a[200:203], v[116:119], v[132:135]\n"
"buffer_load_dwordx4 v[176:179], v0, s[4:7], s98 offen offset:1024 sc1\n"
"v_mfma_f32_16x16x32_bf16 v[132:135], a[204:207], v[112:115], v[132:135]\n"
"v_add_f32_e32 v110, v110, v253\n"
"v_add_f32_e32 v111, v111, v252\n"
"v_mfma_f32_16x16x32_bf16 v[132:135], a[208:211], v[92:95], v[132:135]\n"
"v_add_f32_e32 v108, v108, v128\n"
"v_add_f32_e32 v109, v109, v129\n"
"v_mfma_f32_16x16x32_bf16 v[132:135], a[212:215], v[96:99], v[132:135]\n"
"ds_write_b128 v3, v[104:107] offset:13312\n"
"v_mfma_f32_16x16x32_bf16 v[132:135], a[216:219], v[156:159], v[132:135]\n"
"buffer_load_dwordx4 v[104:107], v0, s[4:7], s98 offen offset:2048 sc1\n"
"v_mfma_f32_16x16x32_bf16 v[132:135], a[220:223], v[88:91], v[132:135]\n"
"ds_read_b128 v[184:187], v4 offset:2048\n"
"ds_read_b128 v[188:191], v5 offset:2048\n"
"v_mfma_f32_16x16x32_bf16 v[192:195], a[252:255], v[88:91], 0\n"
"s_waitcnt lgkmcnt(3)\n"
"v_add_f32_e32 v110, v110, v130\n"
"v_add_f32_e32 v111, v111, v131\n"
"v_mfma_f32_16x16x32_bf16 v[192:195], a[248:251], v[156:159], v[192:195]\n"
"buffer_load_dwordx4 v[88:91], v0, s[4:7], s98 offen offset:3072 sc1\n"
"v_mfma_f32_16x16x32_bf16 v[192:195], a[244:247], v[96:99], v[192:195]\n"
"v_add_f32_e32 v160, v160, v251\n"
"v_add_f32_e32 v161, v161, v250\n"
"v_mfma_f32_16x16x32_bf16 v[192:195], a[240:243], v[92:95], v[192:195]\n"
"v_add_f32_e32 v162, v162, v249\n"
"v_add_f32_e32 v163, v163, v248\n"
"v_mfma_f32_16x16x32_bf16 v[192:195], a[236:239], v[112:115], v[192:195]\n"
"buffer_load_dwordx4 v[92:95], v1, s[4:7], s98 offen sc1\n"
"v_mfma_f32_16x16x32_bf16 v[192:195], a[232:235], v[116:119], v[192:195]\n"
"ds_write_b128 v3, v[132:135] offset:14336\n"
"v_mfma_f32_16x16x32_bf16 v[192:195], a[228:231], v[144:147], v[192:195]\n"
"ds_read_b128 v[96:99], v4 offset:3072\n"
"ds_read_b128 v[112:115], v5 offset:3072\n"
"v_mfma_f32_16x16x32_bf16 v[192:195], a[224:227], v[148:151], v[192:195]\n"
"s_waitcnt vmcnt(9)\n"
"buffer_load_dwordx4 v[116:119], v1, s[4:7], s98 offen offset:1024 sc1\n"
"; @A G2 b.3\n"
"v_mfma_f32_16x16x32_bf16 v[128:131], a[128:131], v[172:175], v[108:111]\n"
"v_add_f32_e32 v160, v160, v164\n"
"v_add_f32_e32 v161, v161, v165\n"
"v_mfma_f32_16x16x32_bf16 v[128:131], a[132:135], v[124:127], v[128:131]\n"
"s_waitcnt lgkmcnt(3)\n"
"v_add_f32_e32 v162, v162, v166\n"
"v_add_f32_e32 v163, v163, v167\n"
"v_mfma_f32_16x16x32_bf16 v[128:131], a[136:139], v[120:123], v[128:131]\n"
"buffer_load_dwordx4 v[132:135], v1, s[4:7], s98 offen offset:2048 sc1\n"
"v_mfma_f32_16x16x32_bf16 v[128:131], a[140:143], v[136:139], v[128:131]\n"
"s_waitcnt vmcnt(7)\n"
"v_add_f32_e32 v184, v184, v247\n"
"v_add_f32_e32 v185, v185, v246\n"
"v_mfma_f32_16x16x32_bf16 v[128:131], a[144:147], v[152:155], v[128:131]\n"
"v_add_f32_e32 v186, v186, v245\n"
"v_add_f32_e32 v187, v187, v244\n"
"v_mfma_f32_16x16x32_bf16 v[128:131], a[148:151], v[168:171], v[128:131]\n"
"ds_write_b128 v3, v[192:195] offset:15360\n"
"v_mfma_f32_16x16x32_bf16 v[128:131], a[152:155], v[180:183], v[128:131]\n"
"buffer_load_dwordx4 v[144:147], v1, s[4:7], s98 offen offset:3072 sc1\n"
"v_mfma_f32_16x16x32_bf16 v[128:131], a[156:159], v[140:143], v[128:131]\n"
"s_add_u32 s98, s57, 0\n"
"v_add_f32_e32 v184, v184, v188\n"
"v_add_f32_e32 v185, v185, v189\n"
"v_mfma_f32_16x16x32_bf16 v[148:151], a[188:191], v[140:143], v[160:163]\n"
"s_waitcnt lgkmcnt(1)\n"
"v_add_f32_e32 v186, v186, v190\n"
"v_add_f32_e32 v187, v187, v191\n"
"v_mfma_f32_16x16x32_bf16 v[148:151], a[184:187], v[180:183], v[148:151]\n"
"s_waitcnt lgkmcnt(0)\n"
"s_barrier\n"
"v_mfma_f32_16x16x32_bf16 v[148:151], a[180:183], v[168:171], v[148:151]\n"
"buffer_load_dwordx4 v[156:159], v0, s[4:7], s98 offen sc1\n"
"v_mfma_f32_16x16x32_bf16 v[148:151], a[176:179], v[152:155], v[148:151]\n"
"v_add_f32_e32 v96, v96, v243\n"
"v_add_f32_e32 v97, v97, v242\n"
"v_mfma_f32_16x16x32_bf16 v[148:151], a[172:175], v[136:139], v[148:151]\n"
"v_cmp_u_f32_e32 vcc, v128, v128\n"
"v_add_f32_e32 v98, v98, v241\n"
"v_mfma_f32_16x16x32_bf16 v[148:151], a[168:171], v[120:123], v[148:151]\n"
"s_cbranch_vccnz .Lmk_p109s\n"
".Lmk_p109r:\n"
"v_add_f32_e32 v99, v99, v240\n"
"v_mfma_f32_16x16x32_bf16 v[148:151], a[164:167], v[124:127], v[148:151]\n"
"buffer_load_dwordx4 v[108:111], v0, s[4:7], s98 offen offset:1024 sc1\n"
"v_mfma_f32_16x16x32_bf16 v[148:151], a[160:163], v[172:175], v[148:151]\n"
"v_add_f32_e32 v96, v96, v112\n"
"v_add_f32_e32 v97, v97, v113\n"
"v_mfma_f32_16x16x32_bf16 v[184:187], a[192:195], v[172:175], v[184:187]\n"
"v_add_f32_e32 v98, v98, v114\n"
"v_add_f32_e32 v99, v99, v115\n"
"v_mfma_f32_16x16x32_bf16 v[184:187], a[196:199], v[124:127], v[184:187]\n"
"buffer_load_dwordx4 v[112:115], v0, s[4:7], s98 offen offset:2048 sc1\n"
"v_mfma_f32_16x16x32_bf16 v[184:187], a[200:203], v[120:123], v[184:187]\n"
"ds_read_b128 v[160:163], v6\n"
"ds_read_b128 v[164:167], v6 offset:1024\n"
"v_mfma_f32_16x16x32_bf16 v[184:187], a[204:207], v[136:139], v[184:187]\n"
"ds_read_b128 v[188:191], v6 offset:2048\n"
"ds_read_b128 v[192:195], v6 offset:3072\n"
"v_mfma_f32_16x16x32_bf16 v[184:187], a[208:211], v[152:155], v[184:187]\n"
"buffer_load_dwordx4 v[196:199], v0, s[4:7], s98 offen offset:3072 sc1\n"
"v_mfma_f32_16x16x32_bf16 v[184:187], a[212:215], v[168:171], v[184:187]\n"
"v_mfma_f32_16x16x32_bf16 v[184:187], a[216:219], v[180:183], v[184:187]\n"
"v_mfma_f32_16x16x32_bf16 v[184:187], a[220:223], v[140:143], v[184:187]\n"
"buffer_load_dwordx4 v[200:203], v1, s[4:7], s98 offen sc1\n"
"v_mfma_f32_16x16x32_bf16 v[96:99], a[252:255], v[140:143], v[96:99]\n"
"s_waitcnt lgkmcnt(0)\n"
"v_add_f32_e32 v160, v128, v160\n"
"v_add_f32_e32 v161, v129, v161\n"
"v_mfma_f32_16x16x32_bf16 v[96:99], a[248:251], v[180:183], v[96:99]\n"
"v_add_f32_e32 v162, v130, v162\n"
"v_add_f32_e32 v163, v131, v163\n"
"v_mfma_f32_16x16x32_bf16 v[96:99], a[244:247], v[168:171], v[96:99]\n"
"buffer_load_dwordx4 v[128:131], v1, s[4:7], s98 offen offset:1024 sc1\n"
"v_mfma_f32_16x16x32_bf16 v[96:99], a[240:243], v[152:155], v[96:99]\n"
"v_add_f32_e32 v164, v148, v164\n"
"v_add_f32_e32 v165, v149, v165\n"
"v_mfma_f32_16x16x32_bf16 v[96:99], a[236:239], v[136:139], v[96:99]\n"
"v_add_f32_e32 v166, v150, v166\n"
"v_add_f32_e32 v167, v151, v167\n"
"v_mfma_f32_16x16x32_bf16 v[96:99], a[232:235], v[120:123], v[96:99]\n"
"buffer_load_dwordx4 v[120:123], v1, s[4:7], s98 offen offset:2048 sc1\n"
"v_mfma_f32_16x16x32_bf16 v[96:99], a[228:231], v[124:127], v[96:99]\n"
"v_cvt_pk_bf16_f32 v124, v160, v161\n"
"v_cvt_pk_bf16_f32 v125, v162, v163\n"
"v_mfma_f32_16x16x32_bf16 v[96:99], a[224:227], v[172:175], v[96:99]\n"
"s_add_u32 s97, s61, 0x440000\n"
"v_cvt_pk_bf16_f32 v126, v164, v165\n"
"v_cvt_pk_bf16_f32 v127, v166, v167\n"
"buffer_store_dwordx4 v[124:127], v0, s[4:7], s97 offen sc1\n"
"ds_write_b128 v2, v[124:127] offset:8192\n"
"buffer_load_dwordx4 v[136:139], v1, s[4:7], s98 offen offset:3072 sc1\n"
"; @A fin b\n"
"s_add_u32 s98, s61, 0x440000\n"
"v_add_f32_e32 v188, v184, v188\n"
"v_add_f32_e32 v189, v185, v189\n"
"v_add_f32_e32 v190, v186, v190\n"
"v_add_f32_e32 v191, v187, v191\n"
"v_add_f32_e32 v192, v96, v192\n"
"v_add_f32_e32 v193, v97, v193\n"
"v_add_f32_e32 v194, v98, v194\n"
"v_add_f32_e32 v195, v99, v195\n"
"v_cvt_pk_bf16_f32 v96, v188, v189\n"
"v_cvt_pk_bf16_f32 v97, v190, v191\n"
"v_cvt_pk_bf16_f32 v98, v192, v193\n"
"v_cvt_pk_bf16_f32 v99, v194, v195\n"
"buffer_store_dwordx4 v[96:99], v0, s[4:7], s98 offen offset:1024 sc1\n"
"s_waitcnt vmcnt(16)\n"
"; @A G2 a.0\n"
"v_mfma_f32_16x16x32_bf16 v[140:143], a[128:131], v[100:103], 0\n"
"s_add_u32 s98, s58, 0\n"
"buffer_load_dwordx4 v[148:151], v0, s[4:7], s98 offen sc1\n"
"v_mfma_f32_16x16x32_bf16 v[140:143], a[132:135], v[176:179], v[140:143]\n"
"s_waitcnt vmcnt(15)\n"
"ds_write_b128 v2, v[96:99] offset:9216\n"
"v_mfma_f32_16x16x32_bf16 v[140:143], a[136:139], v[104:107], v[140:143]\n"
"v_mov_b32_e32 v152, 0x3f803f80\n"
"v_mov_b32_e32 v153, 0x3f803f80\n"
"v_mfma_f32_16x16x32_bf16 v[140:143], a[140:143], v[88:91], v[140:143]\n"
"s_waitcnt vmcnt(13)\n"
"v_mov_b32_e32 v154, 0x3f803f80\n"
"v_mov_b32_e32 v155, 0x3f803f80\n"
"v_mfma_f32_16x16x32_bf16 v[140:143], a[144:147], v[92:95], v[140:143]\n"
"buffer_load_dwordx4 v[160:163], v0, s[4:7], s98 offen offset:1024 sc1\n"
"v_mfma_f32_16x16x32_bf16 v[164:167], v[152:155], v[124:127], 0\n"
"v_mfma_f32_16x16x32_bf16 v[164:167], v[152:155], v[96:99], v[164:167]\n"
"v_mfma_f32_16x16x32_bf16 v[152:155], v[124:127], v[124:127], 0\n"
"v_mfma_f32_16x16x32_bf16 v[152:155], v[96:99], v[96:99], v[152:155]\n"
"v_mfma_f32_16x16x32_bf16 v[140:143], a[148:151], v[116:119], v[140:143]\n"
"s_waitcnt vmcnt(12)\n"
"v_mfma_f32_16x16x32_bf16 v[140:143], a[152:155], v[132:135], v[140:143]\n"
"v_mfma_f32_16x16x32_bf16 v[140:143], a[156:159], v[144:147], v[140:143]\n"
"v_mfma_f32_16x16x32_bf16 v[96:99], a[188:191], v[144:147], 0\n"
"buffer_load_dwordx4 v[124:127], v0, s[4:7], s98 offen offset:2048 sc1\n"
"v_mfma_f32_16x16x32_bf16 v[96:99], a[184:187], v[132:135], v[96:99]\n"
"s_mov_b32 s96, 0xaaaaaaaa\n"
"s_mov_b32 s97, 0xaaaaaaaa\n"
"v_mfma_f32_16x16x32_bf16 v[96:99], a[180:183], v[116:119], v[96:99]\n"
"v_mfma_f32_16x16x32_bf16 v[96:99], a[176:179], v[92:95], v[96:99]\n"
"v_mfma_f32_16x16x32_bf16 v[96:99], a[172:175], v[88:91], v[96:99]\n"
"v_cmp_u_f32_e32 vcc, v140, v140\n"
"v_cndmask_b32_e64 v152, v152, v153, s[96:97]\n"
"v_mfma_f32_16x16x32_bf16 v[96:99], a[168:171], v[104:107], v[96:99]\n"
"s_cbranch_vccnz .Lmk_p110s\n"
".Lmk_p110r:\n"
"v_cndmask_b32_e64 v154, v154, v155, s[96:97]\n"
"v_mfma_f32_16x16x32_bf16 v[96:99], a[164:167], v[176:179], v[96:99]\n"
"ds_write_b128 v3, v[140:143]\n"
"v_mfma_f32_16x16x32_bf16 v[96:99], a[160:163], v[100:103], v[96:99]\n"
"buffer_load_dwordx4 v[140:143], v0, s[4:7], s98 offen offset:3072 sc1\n"
"v_mfma_f32_16x16x32_bf16 v[168:171], a[192:195], v[100:103], 0\n"
"s_mov_b32 s96, 0xcccccccc\n"
"s_mov_b32 s97, 0xcccccccc\n"
"v_mfma_f32_16x16x32_bf16 v[168:171], a[196:199], v[176:179], v[168:171]\n"
"v_cndmask_b32_e64 v165, v152, v154, s[96:97]\n"
"v_mfma_f32_16x16x32_bf16 v[168:171], a[200:203], v[104:107], v[168:171]\n"
"s_mov_b32 s96, 0xf0000f\n"
"s_mov_b32 s97, 0xf0000f00\n"
"v_mfma_f32_16x16x32_bf16 v[168:171], a[204:207], v[88:91], v[168:171]\n"
"buffer_load_dwordx4 v[152:155], v1, s[4:7], s98 offen sc1\n"
"v_mfma_f32_16x16x32_bf16 v[168:171], a[208:211], v[92:95], v[168:171]\n"
"v_cndmask_b32_e64 v165, 0, v165, s[96:97]\n"
"v_mov_b32_e32 v255, v165\n"
"v_mfma_f32_16x16x32_bf16 v[168:171], a[212:215], v[116:119], v[168:171]\n"
"ds_write_b128 v3, v[96:99] offset:1024\n"
"v_mfma_f32_16x16x32_bf16 v[168:171], a[216:219], v[132:135], v[168:171]\n"
"v_permlane16_swap_b32_e32 v165, v255\n"
"v_add_f32_e32 v165, v165, v255\n"
"v_mfma_f32_16x16x32_bf16 v[168:171], a[220:223], v[144:147], v[168:171]\n"
"buffer_load_dwordx4 v[172:175], v1, s[4:7], s98 offen offset:1024 sc1\n"
"v_mfma_f32_16x16x32_bf16 v[96:99], a[252:255], v[144:147], 0\n"
"v_mov_b32_e32 v255, v165\n"
"v_mfma_f32_16x16x32_bf16 v[96:99], a[248:251], v[132:135], v[96:99]\n"
"s_mov_b32 s96, 0xffff0000\n"
"v_permlane32_swap_b32_e32 v165, v255\n"
"v_add_f32_e32 v165, v165, v255\n"
"v_mfma_f32_16x16x32_bf16 v[96:99], a[244:247], v[116:119], v[96:99]\n"
"v_cndmask_b32_e64 v164, v164, v165, s[96:97]\n"
"s_add_u32 s97, s63, 0x44000\n"
"v_mfma_f32_16x16x32_bf16 v[96:99], a[240:243], v[92:95], v[96:99]\n"
"buffer_load_dwordx4 v[116:119], v1, s[4:7], s98 offen offset:2048 sc1\n"
"v_mfma_f32_16x16x32_bf16 v[96:99], a[236:239], v[88:91], v[96:99]\n"
"buffer_store_dword v164, v14, s[4:7], s97 offen sc1\n"
"v_mfma_f32_16x16x32_bf16 v[96:99], a[232:235], v[104:107], v[96:99]\n"
"ds_write_b128 v3, v[168:171] offset:2048\n"
"v_mfma_f32_16x16x32_bf16 v[96:99], a[228:231], v[176:179], v[96:99]\n"
"v_mfma_f32_16x16x32_bf16 v[96:99], a[224:227], v[100:103], v[96:99]\n"
"s_waitcnt vmcnt(14)\n"
"buffer_load_dwordx4 v[132:135], v1, s[4:7], s98 offen offset:3072 sc1\n"
"; @A G2 a.1\n"
"v_mfma_f32_16x16x32_bf16 v[88:91], a[128:131], v[156:159], 0\n"
"v_mfma_f32_16x16x32_bf16 v[88:91], a[132:135], v[108:111], v[88:91]\n"
"s_add_u32 s98, s59, 0\n"
"v_mfma_f32_16x16x32_bf16 v[88:91], a[136:139], v[112:115], v[88:91]\n"
"v_mfma_f32_16x16x32_bf16 v[88:91], a[140:143], v[196:199], v[88:91]\n"
"s_waitcnt vmcnt(10)\n"
"buffer_load_dwordx4 v[176:179], v0, s[4:7], s98 offen sc1\n"
"v_mfma_f32_16x16x32_bf16 v[88:91], a[144:147], v[200:203], v[88:91]\n"
"v_mfma_f32_16x16x32_bf16 v[88:91], a[148:151], v[128:131], v[88:91]\n"
"ds_write_b128 v3, v[96:99] offset:3072\n"
"v_mfma_f32_16x16x32_bf16 v[88:91], a[152:155], v[120:123], v[88:91]\n"
"v_mfma_f32_16x16x32_bf16 v[88:91], a[156:159], v[136:139], v[88:91]\n"
"buffer_load_dwordx4 v[180:183], v0, s[4:7], s98 offen offset:1024 sc1\n"
"v_mfma_f32_16x16x32_bf16 v[92:95], a[188:191], v[136:139], 0\n"
"v_mfma_f32_16x16x32_bf16 v[92:95], a[184:187], v[120:123], v[92:95]\n"
"v_mfma_f32_16x16x32_bf16 v[92:95], a[180:183], v[128:131], v[92:95]\n"
"v_mfma_f32_16x16x32_bf16 v[92:95], a[176:179], v[200:203], v[92:95]\n"
"buffer_load_dwordx4 v[168:171], v0, s[4:7], s98 offen offset:2048 sc1\n"
"v_mfma_f32_16x16x32_bf16 v[92:95], a[172:175], v[196:199], v[92:95]\n"
"s_nop 0\n"
"v_cmp_u_f32_e32 vcc, v88, v88\n"
"v_mfma_f32_16x16x32_bf16 v[92:95], a[168:171], v[112:115], v[92:95]\n"
"s_cbranch_vccnz .Lmk_p111s\n"
".Lmk_p111r:\n"
"v_mfma_f32_16x16x32_bf16 v[92:95], a[164:167], v[108:111], v[92:95]\n"
"ds_write_b128 v3, v[88:91] offset:6144\n"
"v_mfma_f32_16x16x32_bf16 v[92:95], a[160:163], v[156:159], v[92:95]\n"
"buffer_load_dwordx4 v[184:187], v0, s[4:7], s98 offen offset:3072 sc1\n"
"v_mfma_f32_16x16x32_bf16 v[88:91], a[192:195], v[156:159], 0\n"
"v_mfma_f32_16x16x32_bf16 v[88:91], a[196:199], v[108:111], v[88:91]\n"
"v_mfma_f32_16x16x32_bf16 v[88:91], a[200:203], v[112:115], v[88:91]\n"
"v_mfma_f32_16x16x32_bf16 v[88:91], a[204:207], v[196:199], v[88:91]\n"
"buffer_load_dwordx4 v[188:191], v1, s[4:7], s98 offen sc1\n"
"v_mfma_f32_16x16x32_bf16 v[88:91], a[208:211], v[200:203], v[88:91]\n"
"ds_read_b128 v[96:99], v2\n"
"ds_read_b128 v[100:103], v2 offset:1024\n"
"v_mfma_f32_16x16x32_bf16 v[88:91], a[212:215], v[128:131], v[88:91]\n"
"ds_write_b128 v3, v[92:95] offset:7168\n"
"v_mfma_f32_16x16x32_bf16 v[88:91], a[216:219], v[120:123], v[88:91]\n"
"v_mfma_f32_16x16x32_bf16 v[88:91], a[220:223], v[136:139], v[88:91]\n"
"buffer_load_dwordx4 v[164:167], v1, s[4:7], s98 offen offset:1024 sc1\n"
"v_mfma_f32_16x16x32_bf16 v[92:95], a[252:255], v[136:139], 0\n"
"v_mfma_f32_16x16x32_bf16 v[92:95], a[248:251], v[120:123], v[92:95]\n"
"s_waitcnt lgkmcnt(1)\n"
"v_lshlrev_b32_e32 v255, 16, v96\n"
"v_and_b32_e32 v254, 0xffff0000, v96\n"
"v_mfma_f32_16x16x32_bf16 v[92:95], a[244:247], v[128:131], v[92:95]\n"
"v_lshlrev_b32_e32 v253, 16, v97\n"
"v_and_b32_e32 v252, 0xffff0000, v97\n"
"v_mfma_f32_16x16x32_bf16 v[92:95], a[240:243], v[200:203], v[92:95]\n"
"buffer_load_dwordx4 v[192:195], v1, s[4:7], s98 offen offset:2048 sc1\n"
"v_mfma_f32_16x16x32_bf16 v[92:95], a[236:239], v[196:199], v[92:95]\n"
"v_lshlrev_b32_e32 v251, 16, v98\n"
"v_and_b32_e32 v250, 0xffff0000, v98\n"
"v_mfma_f32_16x16x32_bf16 v[92:95], a[232:235], v[112:115], v[92:95]\n"
"ds_write_b128 v3, v[88:91] offset:8192\n"
"v_mfma_f32_16x16x32_bf16 v[92:95], a[228:231], v[108:111], v[92:95]\n"
"v_lshlrev_b32_e32 v249, 16, v99\n"
"v_and_b32_e32 v248, 0xffff0000, v99\n"
"v_mfma_f32_16x16x32_bf16 v[92:95], a[224:227], v[156:159], v[92:95]\n"
"s_waitcnt vmcnt(12)\n"
"buffer_load_dwordx4 v[196:199], v1, s[4:7], s98 offen offset:3072 sc1\n"
"; @A G2 a.2\n"
"v_mfma_f32_16x16x32_bf16 v[96:99], a[128:131], v[148:151], 0\n"
"v_lshlrev_b32_e32 v247, 16, v100\n"
"v_and_b32_e32 v246, 0xffff0000, v100\n"
"v_mfma_f32_16x16x32_bf16 v[96:99], a[132:135], v[160:163], v[96:99]\n"
"v_lshlrev_b32_e32 v245, 16, v101\n"
"v_and_b32_e32 v244, 0xffff0000, v101\n"
"v_mfma_f32_16x16x32_bf16 v[96:99], a[136:139], v[124:127], v[96:99]\n"
"v_lshlrev_b32_e32 v243, 16, v102\n"
"v_and_b32_e32 v242, 0xffff0000, v102\n"
"v_mfma_f32_16x16x32_bf16 v[96:99], a[140:143], v[140:143], v[96:99]\n"
"s_waitcnt vmcnt(8)\n"
"v_lshlrev_b32_e32 v241, 16, v103\n"
"v_and_b32_e32 v240, 0xffff0000, v103\n"
"v_mfma_f32_16x16x32_bf16 v[96:99], a[144:147], v[152:155], v[96:99]\n"
"v_mfma_f32_16x16x32_bf16 v[96:99], a[148:151], v[172:175], v[96:99]\n"
"ds_write_b128 v3, v[92:95] offset:9216\n"
"v_mfma_f32_16x16x32_bf16 v[96:99], a[152:155], v[116:119], v[96:99]\n"
"v_mfma_f32_16x16x32_bf16 v[96:99], a[156:159], v[132:135], v[96:99]\n"
"v_mfma_f32_16x16x32_bf16 v[104:107], a[188:191], v[132:135], 0\n"
"s_add_u32 s98, s52, 0x440000\n"
"buffer_load_dwordx4 v[88:91], v0, s[4:7], s98 offen sc1\n"
"v_mfma_f32_16x16x32_bf16 v[104:107], a[184:187], v[116:119], v[104:107]\n"
"s_waitcnt lgkmcnt(0)\n"
"s_barrier\n"
"v_mfma_f32_16x16x32_bf16 v[104:107], a[180:183], v[172:175], v[104:107]\n"
"ds_read_b128 v[144:147], v4\n"
"ds_read_b128 v[112:115], v5\n"
"v_mfma_f32_16x16x32_bf16 v[104:107], a[176:179], v[152:155], v[104:107]\n"
"buffer_load_dwordx4 v[92:95], v0, s[4:7], s98 offen offset:1024 sc1\n"
"v_mfma_f32_16x16x32_bf16 v[104:107], a[172:175], v[140:143], v[104:107]\n"
"v_cmp_u_f32_e32 vcc, v96, v96\n"
"v_mfma_f32_16x16x32_bf16 v[104:107], a[168:171], v[124:127], v[104:107]\n"
"s_cbranch_vccnz .Lmk_p112s\n"
".Lmk_p112r:\n"
"v_mfma_f32_16x16x32_bf16 v[104:107], a[164:167], v[160:163], v[104:107]\n"
"ds_write_b128 v3, v[96:99] offset:12288\n"
"v_mfma_f32_16x16x32_bf16 v[104:107], a[160:163], v[148:151], v[104:107]\n"
"buffer_load_dwordx4 v[96:99], v0, s[4:7], s98 offen offset:2048 sc1\n"
"v_mfma_f32_16x16x32_bf16 v[120:123], a[192:195], v[148:151], 0\n"
"ds_read_b128 v[156:159], v4 offset:1024\n"
"ds_read_b128 v[128:131], v5 offset:1024\n"
"v_mfma_f32_16x16x32_bf16 v[120:123], a[196:199], v[160:163], v[120:123]\n"
"s_waitcnt lgkmcnt(3)\n"
"v_add_f32_e32 v144, v144, v255\n"
"v_add_f32_e32 v145, v145, v254\n"
"v_mfma_f32_16x16x32_bf16 v[120:123], a[200:203], v[124:127], v[120:123]\n"
"buffer_load_dwordx4 v[100:103], v0, s[4:7], s98 offen offset:3072 sc1\n"
"v_mfma_f32_16x16x32_bf16 v[120:123], a[204:207], v[140:143], v[120:123]\n"
"v_add_f32_e32 v146, v146, v253\n"
"v_add_f32_e32 v147, v147, v252\n"
"v_mfma_f32_16x16x32_bf16 v[120:123], a[208:211], v[152:155], v[120:123]\n"
"v_add_f32_e32 v144, v144, v112\n"
"v_add_f32_e32 v145, v145, v113\n"
"v_mfma_f32_16x16x32_bf16 v[120:123], a[212:215], v[172:175], v[120:123]\n"
"ds_write_b128 v3, v[104:107] offset:13312\n"
"v_mfma_f32_16x16x32_bf16 v[120:123], a[216:219], v[116:119], v[120:123]\n"
"buffer_load_dwordx4 v[104:107], v1, s[4:7], s98 offen sc1\n"
"v_mfma_f32_16x16x32_bf16 v[120:123], a[220:223], v[132:135], v[120:123]\n"
"ds_read_b128 v[108:111], v4 offset:2048\n"
"ds_read_b128 v[136:139], v5 offset:2048\n"
"v_mfma_f32_16x16x32_bf16 v[200:203], a[252:255], v[132:135], 0\n"
"s_waitcnt lgkmcnt(3)\n"
"v_add_f32_e32 v146, v146, v114\n"
"v_add_f32_e32 v147, v147, v115\n"
"v_mfma_f32_16x16x32_bf16 v[200:203], a[248:251], v[116:119], v[200:203]\n"
"buffer_load_dwordx4 v[112:115], v1, s[4:7], s98 offen offset:1024 sc1\n"
"v_mfma_f32_16x16x32_bf16 v[200:203], a[244:247], v[172:175], v[200:203]\n"
"v_add_f32_e32 v156, v156, v251\n"
"v_add_f32_e32 v157, v157, v250\n"
"v_mfma_f32_16x16x32_bf16 v[200:203], a[240:243], v[152:155], v[200:203]\n"
"v_add_f32_e32 v158, v158, v249\n"
"v_add_f32_e32 v159, v159, v248\n"
"v_mfma_f32_16x16x32_bf16 v[200:203], a[236:239], v[140:143], v[200:203]\n"
"buffer_load_dwordx4 v[116:119], v1, s[4:7], s98 offen offset:2048 sc1\n"
"v_mfma_f32_16x16x32_bf16 v[200:203], a[232:235], v[124:127], v[200:203]\n"
"ds_write_b128 v3, v[120:123] offset:14336\n"
"v_mfma_f32_16x16x32_bf16 v[200:203], a[228:231], v[160:163], v[200:203]\n"
"ds_read_b128 v[120:123], v4 offset:3072\n"
"ds_read_b128 v[152:155], v5 offset:3072\n"
"v_mfma_f32_16x16x32_bf16 v[200:203], a[224:227], v[148:151], v[200:203]\n"
"s_waitcnt vmcnt(11)\n"
"buffer_load_dwordx4 v[124:127], v1, s[4:7], s98 offen offset:3072 sc1\n"
"; @A G2 a.3\n"
"v_mfma_f32_16x16x32_bf16 v[172:175], a[128:131], v[176:179], v[144:147]\n"
"s_add_u32 s98, s53, 0x440000\n"
"v_add_f32_e32 v156, v156, v128\n"
"v_add_f32_e32 v157, v157, v129\n"
"v_mfma_f32_16x16x32_bf16 v[172:175], a[132:135], v[180:183], v[172:175]\n"
"s_waitcnt lgkmcnt(3)\n"
"v_add_f32_e32 v158, v158, v130\n"
"v_add_f32_e32 v159, v159, v131\n"
"v_mfma_f32_16x16x32_bf16 v[172:175], a[136:139], v[168:171], v[172:175]\n"
"buffer_load_dwordx4 v[128:131], v0, s[4:7], s98 offen sc1\n"
"v_mfma_f32_16x16x32_bf16 v[172:175], a[140:143], v[184:187], v[172:175]\n"
"s_waitcnt vmcnt(9)\n"
"v_add_f32_e32 v108, v108, v247\n"
"v_add_f32_e32 v109, v109, v246\n"
"v_mfma_f32_16x16x32_bf16 v[172:175], a[144:147], v[188:191], v[172:175]\n"
"v_add_f32_e32 v110, v110, v245\n"
"v_add_f32_e32 v111, v111, v244\n"
"v_mfma_f32_16x16x32_bf16 v[172:175], a[148:151], v[164:167], v[172:175]\n"
"ds_write_b128 v3, v[200:203] offset:15360\n"
"v_mfma_f32_16x16x32_bf16 v[172:175], a[152:155], v[192:195], v[172:175]\n"
"buffer_load_dwordx4 v[132:135], v0, s[4:7], s98 offen offset:1024 sc1\n"
"v_mfma_f32_16x16x32_bf16 v[172:175], a[156:159], v[196:199], v[172:175]\n"
"v_add_f32_e32 v108, v108, v136\n"
"v_add_f32_e32 v109, v109, v137\n"
"v_mfma_f32_16x16x32_bf16 v[200:203], a[188:191], v[196:199], v[156:159]\n"
"s_waitcnt lgkmcnt(1)\n"
"v_add_f32_e32 v110, v110, v138\n"
"v_add_f32_e32 v111, v111, v139\n"
"v_mfma_f32_16x16x32_bf16 v[200:203], a[184:187], v[192:195], v[200:203]\n"
"s_waitcnt lgkmcnt(0)\n"
"s_barrier\n"
"v_mfma_f32_16x16x32_bf16 v[200:203], a[180:183], v[164:167], v[200:203]\n"
"buffer_load_dwordx4 v[136:139], v0, s[4:7], s98 offen offset:2048 sc1\n"
"v_mfma_f32_16x16x32_bf16 v[200:203], a[176:179], v[188:191], v[200:203]\n"
"v_add_f32_e32 v120, v120, v243\n"
"v_add_f32_e32 v121, v121, v242\n"
"v_mfma_f32_16x16x32_bf16 v[200:203], a[172:175], v[184:187], v[200:203]\n"
"v_cmp_u_f32_e32 vcc, v172, v172\n"
"v_add_f32_e32 v122, v122, v241\n"
"v_mfma_f32_16x16x32_bf16 v[200:203], a[168:171], v[168:171], v[200:203]\n"
"s_cbranch_vccnz .Lmk_p113s\n"
".Lmk_p113r:\n"
"v_add_f32_e32 v123, v123, v240\n"
"v_mfma_f32_16x16x32_bf16 v[200:203], a[164:167], v[180:183], v[200:203]\n"
"buffer_load_dwordx4 v[140:143], v0, s[4:7], s98 offen offset:3072 sc1\n"
"v_mfma_f32_16x16x32_bf16 v[200:203], a[160:163], v[176:179], v[200:203]\n"
"v_add_f32_e32 v120, v120, v152\n"
"v_add_f32_e32 v121, v121, v153\n"
"v_mfma_f32_16x16x32_bf16 v[108:111], a[192:195], v[176:179], v[108:111]\n"
"v_add_f32_e32 v122, v122, v154\n"
"v_add_f32_e32 v123, v123, v155\n"
"v_mfma_f32_16x16x32_bf16 v[108:111], a[196:199], v[180:183], v[108:111]\n"
"buffer_load_dwordx4 v[144:147], v1, s[4:7], s98 offen sc1\n"
"v_mfma_f32_16x16x32_bf16 v[108:111], a[200:203], v[168:171], v[108:111]\n"
"ds_read_b128 v[204:207], v6\n"
"ds_read_b128 v[208:211], v6 offset:1024\n"
"v_mfma_f32_16x16x32_bf16 v[108:111], a[204:207], v[184:187], v[108:111]\n"
"ds_read_b128 v[148:151], v6 offset:2048\n"
"ds_read_b128 v[152:155], v6 offset:3072\n"
"v_mfma_f32_16x16x32_bf16 v[108:111], a[208:211], v[188:191], v[108:111]\n"
"buffer_load_dwordx4 v[156:159], v1, s[4:7], s98 offen offset:1024 sc1\n"
"v_mfma_f32_16x16x32_bf16 v[108:111], a[212:215], v[164:167], v[108:111]\n"
"v_mfma_f32_16x16x32_bf16 v[108:111], a[216:219], v[192:195], v[108:111]\n"
"v_mfma_f32_16x16x32_bf16 v[108:111], a[220:223], v[196:199], v[108:111]\n"
"buffer_load_dwordx4 v[160:163], v1, s[4:7], s98 offen offset:2048 sc1\n"
"v_mfma_f32_16x16x32_bf16 v[120:123], a[252:255], v[196:199], v[120:123]\n"
"s_waitcnt lgkmcnt(0)\n"
"v_add_f32_e32 v204, v172, v204\n"
"v_add_f32_e32 v205, v173, v205\n"
"v_mfma_f32_16x16x32_bf16 v[120:123], a[248:251], v[192:195], v[120:123]\n"
"v_add_f32_e32 v206, v174, v206\n"
"v_add_f32_e32 v207, v175, v207\n"
"v_mfma_f32_16x16x32_bf16 v[120:123], a[244:247], v[164:167], v[120:123]\n"
"buffer_load_dwordx4 v[164:167], v1, s[4:7], s98 offen offset:3072 sc1\n"
"v_mfma_f32_16x16x32_bf16 v[120:123], a[240:243], v[188:191], v[120:123]\n"
"v_add_f32_e32 v208, v200, v208\n"
"v_add_f32_e32 v209, v201, v209\n"
"v_mfma_f32_16x16x32_bf16 v[120:123], a[236:239], v[184:187], v[120:123]\n"
"v_add_f32_e32 v210, v202, v210\n"
"v_add_f32_e32 v211, v203, v211\n"
"v_mfma_f32_16x16x32_bf16 v[120:123], a[232:235], v[168:171], v[120:123]\n"
"v_cvt_pk_bf16_f32 v168, v204, v205\n"
"v_cvt_pk_bf16_f32 v169, v206, v207\n"
"v_mfma_f32_16x16x32_bf16 v[120:123], a[228:231], v[180:183], v[120:123]\n"
"s_add_u32 s98, s61, 0x400000\n"
"v_cvt_pk_bf16_f32 v170, v208, v209\n"
"v_cvt_pk_bf16_f32 v171, v210, v211\n"
"v_mfma_f32_16x16x32_bf16 v[120:123], a[224:227], v[176:179], v[120:123]\n"
"buffer_store_dwordx4 v[168:171], v0, s[4:7], s98 offen sc1\n"
"ds_write_b128 v2, v[168:171]\n"
"s_branch .Lmk_loop\n"
".Lmk_f2s:\n"
";\n"
"s_mov_b32 s1, 0x10000\n"
".Lmk_f2l:\n"
"buffer_load_dwordx2 v[254:255], v13, s[4:7], s98 offen sc1\n"
"s_waitcnt vmcnt(0)\n"
"v_cmp_ne_u64_e32 vcc, v[254:255], v[18:19]\n"
"s_cbranch_vccz .Lmk_f2r\n"
"s_sub_u32 s1, s1, 1\n"
"s_cbranch_scc0 .Lmk_f2l\n"
"s_branch .Lmk_f2r\n"
".Lmk_f1s:\n"
";\n"
"s_mov_b32 s1, 0x10000\n"
".Lmk_f1l:\n"
"buffer_load_dwordx2 v[254:255], v12, s[4:7], s98 offen sc1\n"
"s_waitcnt vmcnt(0)\n"
"v_cmp_ne_u64_e32 vcc, v[254:255], v[18:19]\n"
"s_cbranch_vccz .Lmk_f1r\n"
"s_sub_u32 s1, s1, 1\n"
"s_cbranch_scc0 .Lmk_f1l\n"
"s_branch .Lmk_f1r\n"
".Lmk_f6s:\n"
";\n"
"s_mov_b32 s1, 0x10000\n"
".Lmk_f6l:\n"
"buffer_load_dwordx2 v[250:251], v41, s[4:7], s97 offen sc1\n"
"s_waitcnt vmcnt(0)\n"
"v_cmp_ne_u64_e32 vcc, v[250:251], v[18:19]\n"
"s_cbranch_vccz .Lmk_f6r\n"
"s_sub_u32 s1, s1, 1\n"
"s_cbranch_scc0 .Lmk_f6l\n"
"s_branch .Lmk_f6r\n"
".Lmk_f3s:\n"
";\n"
"s_mov_b32 s1, 0x10000\n"
".Lmk_f3l:\n"
"buffer_load_dwordx2 v[246:247], v12, s[4:7], s97 offen sc1\n"
"s_waitcnt vmcnt(0)\n"
"v_cmp_ne_u64_e32 vcc, v[246:247], v[18:19]\n"
"s_cbranch_vccz .Lmk_f3r\n"
"s_sub_u32 s1, s1, 1\n"
"s_cbranch_scc0 .Lmk_f3l\n"
"s_branch .Lmk_f3r\n"
".Lmk_f7s:\n"
";\n"
"s_mov_b32 s1, 0x10000\n"
".Lmk_f7l:\n"
"buffer_load_dwordx2 v[246:247], v13, s[4:7], s98 offen sc1\n"
"s_waitcnt vmcnt(0)\n"
"v_cmp_ne_u64_e32 vcc, v[246:247], v[18:19]\n"
"s_cbranch_vccz .Lmk_f7r\n"
"s_sub_u32 s1, s1, 1\n"
"s_cbranch_scc0 .Lmk_f7l\n"
"s_branch .Lmk_f7r\n"
".Lmk_f4s:\n"
";\n"
"s_mov_b32 s1, 0x10000\n"
".Lmk_f4l:\n"
"buffer_load_dwordx2 v[250:251], off, s[4:7], s96 sc1\n"
"s_waitcnt vmcnt(0)\n"
"v_cmp_ne_u64_e32 vcc, v[250:251], v[18:19]\n"
"s_cbranch_vccz .Lmk_f4r\n"
"s_sub_u32 s1, s1, 1\n"
"s_cbranch_scc0 .Lmk_f4l\n"
"s_branch .Lmk_f4r\n"
".Lmk_x18s:\n"
"s_nop 3\n"
"v_mov_b32_e32 v24, 0xff800000\n"
"v_mov_b32_e32 v25, 0xff800000\n"
"v_mov_b32_e32 v26, 0xff800000\n"
"v_mov_b32_e32 v27, 0xff800000\n"
"v_mov_b32_e32 v36, 0xff800000\n"
"v_mov_b32_e32 v37, 0xff800000\n"
"v_mov_b32_e32 v38, 0xff800000\n"
"v_mov_b32_e32 v39, 0xff800000\n"
"s_nop 1\n"
"s_branch .Lmk_x18r\n"
".Lmk_x19s:\n"
"s_nop 3\n"
"v_mov_b32_e32 v24, 0xff800000\n"
"v_mov_b32_e32 v25, 0xff800000\n"
"v_mov_b32_e32 v26, 0xff800000\n"
"v_mov_b32_e32 v27, 0xff800000\n"
"v_mov_b32_e32 v36, 0xff800000\n"
"v_mov_b32_e32 v37, 0xff800000\n"
"v_mov_b32_e32 v38, 0xff800000\n"
"v_mov_b32_e32 v39, 0xff800000\n"
"s_nop 1\n"
"s_branch .Lmk_x19r\n"
".Lmk_x20s:\n"
"s_nop 3\n"
"v_mov_b32_e32 v24, 0xff800000\n"
"v_mov_b32_e32 v25, 0xff800000\n"
"v_mov_b32_e32 v26, 0xff800000\n"
"v_mov_b32_e32 v27, 0xff800000\n"
"v_mov_b32_e32 v36, 0xff800000\n"
"v_mov_b32_e32 v37, 0xff800000\n"
"v_mov_b32_e32 v38, 0xff800000\n"
"v_mov_b32_e32 v39, 0xff800000\n"
"s_nop 1\n"
"s_branch .Lmk_x20r\n"
".Lmk_x21s:\n"
"s_nop 3\n"
"v_mov_b32_e32 v24, 0xff800000\n"
"v_mov_b32_e32 v25, 0xff800000\n"
"v_mov_b32_e32 v26, 0xff800000\n"
"v_mov_b32_e32 v27, 0xff800000\n"
"v_mov_b32_e32 v36, 0xff800000\n"
"v_mov_b32_e32 v37, 0xff800000\n"
"v_mov_b32_e32 v38, 0xff800000\n"
"v_mov_b32_e32 v39, 0xff800000\n"
"s_nop 1\n"
"s_branch .Lmk_x21r\n"
".Lmk_f14s:\n"
";\n"
"s_mov_b32 s1, 0x10000\n"
".Lmk_f14l:\n"
"buffer_load_dwordx2 v[238:239], off, s[4:7], s97 sc1\n"
"s_waitcnt vmcnt(0)\n"
"v_cmp_ne_u64_e32 vcc, v[238:239], v[18:19]\n"
"s_cbranch_vccz .Lmk_f14r\n"
"s_sub_u32 s1, s1, 1\n"
"s_cbranch_scc0 .Lmk_f14l\n"
"s_branch .Lmk_f14r\n"
".Lmk_b16:\n"
"s_cmp_lg_u32 s2, 0\n"
"s_cbranch_scc1 .Lmk_k22\n"
"s_mov_b32 s2, 1\n"
"s_waitcnt vmcnt(0) lgkmcnt(0)\n"
"s_nop 7\n"
"s_mov_b32 s97, 11\n"
"v_add_u32_e32 v248, 0x3800, v15\n"
"v_add_u32_e32 v247, 0x3800, v254\n"
"ds_read_b128 v[36:39], v15 offset:24576\n"
"ds_read_b128 v[40:43], v254 offset:24832\n"
"s_waitcnt lgkmcnt(0)\n"
"v_mfma_f32_16x16x32_bf16 v[48:51], v[36:39], v[28:31], 0\n"
"v_mfma_f32_16x16x32_bf16 v[52:55], v[36:39], v[32:35], 0\n"
"v_mfma_f32_16x16x32_bf16 v[36:39], v[40:43], v[32:35], 0\n"
"v_mfma_f32_16x16x32_bf16 v[56:59], v[40:43], v[28:31], 0\n"
".Lmk_b16l:\n"
"ds_read_b128 v[40:43], v248 offset:8192\n"
"ds_read_b128 v[60:63], v247 offset:8448\n"
"s_cmp_gt_i32 s78, s97\n"
"s_cselect_b32 s96, 0xff800000, 0\n"
"v_mov_b32_e32 v64, s96\n"
"v_mov_b32_e32 v65, s96\n"
"v_mov_b32_e32 v66, s96\n"
"v_mov_b32_e32 v67, s96\n"
"v_subrev_u32_e32 v248, 0x800, v248\n"
"v_subrev_u32_e32 v247, 0x800, v247\n"
"s_waitcnt lgkmcnt(0)\n"
"v_mfma_f32_16x16x32_bf16 v[72:75], v[40:43], v[28:31], v[64:67]\n"
"v_mfma_f32_16x16x32_bf16 v[76:79], v[40:43], v[32:35], v[64:67]\n"
"v_mfma_f32_16x16x32_bf16 v[40:43], v[60:63], v[32:35], v[64:67]\n"
"v_mfma_f32_16x16x32_bf16 v[84:87], v[60:63], v[28:31], v[64:67]\n"
"s_nop 7\n"
"s_cmp_lg_u32 s97, 4\n"
"s_cbranch_scc1 .Lmk_b16m\n"
"v_cndmask_b32_e64 v72, v48, v72, s[20:21]\n"
"v_cndmask_b32_e64 v73, v49, v73, s[22:23]\n"
"v_cndmask_b32_e64 v74, v50, v74, s[24:25]\n"
"v_cndmask_b32_e64 v75, v51, v75, s[26:27]\n"
"v_cndmask_b32_e64 v84, v56, v84, s[28:29]\n"
"v_cndmask_b32_e64 v85, v57, v85, s[30:31]\n"
"v_cndmask_b32_e64 v86, v58, v86, s[32:33]\n"
"v_cndmask_b32_e64 v87, v59, v87, s[34:35]\n"
"v_cndmask_b32_e64 v76, v52, v76, s[36:37]\n"
"v_cndmask_b32_e64 v77, v53, v77, s[38:39]\n"
"v_cndmask_b32_e64 v78, v54, v78, s[40:41]\n"
"v_cndmask_b32_e64 v79, v55, v79, s[42:43]\n"
"v_cndmask_b32_e64 v40, v36, v40, s[44:45]\n"
"v_cndmask_b32_e64 v41, v37, v41, s[46:47]\n"
"v_cndmask_b32_e64 v42, v38, v42, s[48:49]\n"
"v_cndmask_b32_e64 v43, v39, v43, s[50:51]\n"
".Lmk_b16m:\n"
"v_max3_f32 v253, v253, v72, v73\n"
"v_max3_f32 v251, v251, v76, v77\n"
"v_max3_f32 v253, v253, v74, v75\n"
"v_max3_f32 v251, v251, v78, v79\n"
"v_max3_f32 v253, v253, v84, v85\n"
"v_max3_f32 v251, v251, v40, v41\n"
"v_max3_f32 v253, v253, v86, v87\n"
"v_max3_f32 v251, v251, v42, v43\n"
"s_sub_u32 s97, s97, 1\n"
"s_cmp_ge_i32 s97, 4\n"
"s_cbranch_scc1 .Lmk_b16l\n"
"v_mov_b32_e32 v248, v253\n"
"v_mov_b32_e32 v247, v251\n"
"s_nop 0\n"
"v_permlane16_swap_b32_e32 v253, v248\n"
"v_permlane16_swap_b32_e32 v251, v247\n"
"v_max_f32_e32 v253, v253, v248\n"
"v_max_f32_e32 v251, v251, v247\n"
"v_mov_b32_e32 v248, v253\n"
"v_mov_b32_e32 v247, v251\n"
"s_nop 0\n"
"v_permlane32_swap_b32_e32 v253, v248\n"
"v_permlane32_swap_b32_e32 v251, v247\n"
"v_max_f32_e32 v253, v253, v248\n"
"v_max_f32_e32 v251, v251, v247\n"
"v_mov_b32_e32 v250, v253\n"
"v_mov_b32_e32 v249, v251\n"
"s_branch .Lmk_a15\n"
".Lmk_x25s:\n"
"s_nop 3\n"
"v_mov_b32_e32 v116, 0xff800000\n"
"v_mov_b32_e32 v117, 0xff800000\n"
"v_mov_b32_e32 v118, 0xff800000\n"
"v_mov_b32_e32 v119, 0xff800000\n"
"v_mov_b32_e32 v120, 0xff800000\n"
"v_mov_b32_e32 v121, 0xff800000\n"
"v_mov_b32_e32 v122, 0xff800000\n"
"v_mov_b32_e32 v123, 0xff800000\n"
"s_nop 1\n"
"s_branch .Lmk_x25r\n"
".Lmk_x26s:\n"
"s_nop 3\n"
"v_mov_b32_e32 v116, 0xff800000\n"
"v_mov_b32_e32 v117, 0xff800000\n"
"v_mov_b32_e32 v118, 0xff800000\n"
"v_mov_b32_e32 v119, 0xff800000\n"
"v_mov_b32_e32 v120, 0xff800000\n"
"v_mov_b32_e32 v121, 0xff800000\n"
"v_mov_b32_e32 v122, 0xff800000\n"
"v_mov_b32_e32 v123, 0xff800000\n"
"s_nop 1\n"
"s_branch .Lmk_x26r\n"
".Lmk_x27s:\n"
"s_nop 3\n"
"v_mov_b32_e32 v116, 0xff800000\n"
"v_mov_b32_e32 v117, 0xff800000\n"
"v_mov_b32_e32 v118, 0xff800000\n"
"v_mov_b32_e32 v119, 0xff800000\n"
"v_mov_b32_e32 v120, 0xff800000\n"
"v_mov_b32_e32 v121, 0xff800000\n"
"v_mov_b32_e32 v122, 0xff800000\n"
"v_mov_b32_e32 v123, 0xff800000\n"
"s_nop 1\n"
"s_branch .Lmk_x27r\n"
".Lmk_f5s:\n"
";\n"
"s_mov_b32 s1, 0x10000\n"
".Lmk_f5l:\n"
"buffer_load_dwordx2 v[246:247], v12, s[4:7], s96 offen sc1\n"
"s_waitcnt vmcnt(0)\n"
"v_cmp_ne_u64_e32 vcc, v[246:247], v[18:19]\n"
"s_cbranch_vccz .Lmk_f5r\n"
"s_sub_u32 s1, s1, 1\n"
"s_cbranch_scc0 .Lmk_f5l\n"
"s_branch .Lmk_f5r\n"
".Lmk_b24:\n"
"s_cmp_lg_u32 s2, 0\n"
"s_cbranch_scc1 .Lmk_k29\n"
"s_mov_b32 s2, 1\n"
"s_waitcnt vmcnt(0) lgkmcnt(0)\n"
"s_nop 7\n"
"s_mov_b32 s96, 7\n"
"v_add_u32_e32 v248, 0x3800, v15\n"
"v_add_u32_e32 v247, 0x3800, v254\n"
"ds_read_b128 v[172:175], v15 offset:16384\n"
"ds_read_b128 v[192:195], v254 offset:16640\n"
"s_waitcnt lgkmcnt(0)\n"
"v_mfma_f32_16x16x32_bf16 v[196:199], v[172:175], v[92:95], 0\n"
"v_mfma_f32_16x16x32_bf16 v[212:215], v[172:175], v[96:99], 0\n"
"v_mfma_f32_16x16x32_bf16 v[172:175], v[192:195], v[96:99], 0\n"
"v_mfma_f32_16x16x32_bf16 v[220:223], v[192:195], v[92:95], 0\n"
".Lmk_b24l:\n"
"ds_read_b128 v[192:195], v248\n"
"ds_read_b128 v[224:227], v247 offset:256\n"
"s_cmp_gt_i32 s78, s96\n"
"s_cselect_b32 s95, 0xff800000, 0\n"
"v_mov_b32_e32 v228, s95\n"
"v_mov_b32_e32 v229, s95\n"
"v_mov_b32_e32 v230, s95\n"
"v_mov_b32_e32 v231, s95\n"
"v_subrev_u32_e32 v248, 0x800, v248\n"
"v_subrev_u32_e32 v247, 0x800, v247\n"
"s_waitcnt lgkmcnt(0)\n"
"v_mfma_f32_16x16x32_bf16 v[232:235], v[192:195], v[92:95], v[228:231]\n"
"v_mfma_f32_16x16x32_bf16 v[236:239], v[192:195], v[96:99], v[228:231]\n"
"v_mfma_f32_16x16x32_bf16 v[192:195], v[224:227], v[96:99], v[228:231]\n"
"v_mfma_f32_16x16x32_bf16 v[240:243], v[224:227], v[92:95], v[228:231]\n"
"s_nop 7\n"
"s_cmp_lg_u32 s96, 0\n"
"s_cbranch_scc1 .Lmk_b24m\n"
"v_cndmask_b32_e64 v232, v196, v232, s[20:21]\n"
"v_cndmask_b32_e64 v233, v197, v233, s[22:23]\n"
"v_cndmask_b32_e64 v234, v198, v234, s[24:25]\n"
"v_cndmask_b32_e64 v235, v199, v235, s[26:27]\n"
"v_cndmask_b32_e64 v240, v220, v240, s[28:29]\n"
"v_cndmask_b32_e64 v241, v221, v241, s[30:31]\n"
"v_cndmask_b32_e64 v242, v222, v242, s[32:33]\n"
"v_cndmask_b32_e64 v243, v223, v243, s[34:35]\n"
"v_cndmask_b32_e64 v236, v212, v236, s[36:37]\n"
"v_cndmask_b32_e64 v237, v213, v237, s[38:39]\n"
"v_cndmask_b32_e64 v238, v214, v238, s[40:41]\n"
"v_cndmask_b32_e64 v239, v215, v239, s[42:43]\n"
"v_cndmask_b32_e64 v192, v172, v192, s[44:45]\n"
"v_cndmask_b32_e64 v193, v173, v193, s[46:47]\n"
"v_cndmask_b32_e64 v194, v174, v194, s[48:49]\n"
"v_cndmask_b32_e64 v195, v175, v195, s[50:51]\n"
".Lmk_b24m:\n"
"v_max3_f32 v253, v253, v232, v233\n"
"v_max3_f32 v251, v251, v236, v237\n"
"v_max3_f32 v253, v253, v234, v235\n"
"v_max3_f32 v251, v251, v238, v239\n"
"v_max3_f32 v253, v253, v240, v241\n"
"v_max3_f32 v251, v251, v192, v193\n"
"v_max3_f32 v253, v253, v242, v243\n"
"v_max3_f32 v251, v251, v194, v195\n"
"s_sub_u32 s96, s96, 1\n"
"s_cmp_ge_i32 s96, 0\n"
"s_cbranch_scc1 .Lmk_b24l\n"
"v_mov_b32_e32 v248, v253\n"
"v_mov_b32_e32 v247, v251\n"
"s_nop 0\n"
"v_permlane16_swap_b32_e32 v253, v248\n"
"v_permlane16_swap_b32_e32 v251, v247\n"
"v_max_f32_e32 v253, v253, v248\n"
"v_max_f32_e32 v251, v251, v247\n"
"v_mov_b32_e32 v248, v253\n"
"v_mov_b32_e32 v247, v251\n"
"s_nop 0\n"
"v_permlane32_swap_b32_e32 v253, v248\n"
"v_permlane32_swap_b32_e32 v251, v247\n"
"v_max_f32_e32 v253, v253, v248\n"
"v_max_f32_e32 v251, v251, v247\n"
"v_mov_b32_e32 v250, v253\n"
"v_mov_b32_e32 v249, v251\n"
"s_branch .Lmk_a23\n"
".Lmk_p30s:\n"
";\n"
"s_waitcnt vmcnt(16)\n"
"s_mov_b32 s1, 0x10000\n"
".Lmk_p30l:\n"
"s_add_u32 s98, s56, 0x40000\n"
"buffer_load_dwordx4 v[176:179], v0, s[4:7], s98 offen sc1\n"
"buffer_load_dwordx4 v[200:203], v0, s[4:7], s98 offen offset:1024 sc1\n"
"buffer_load_dwordx4 v[188:191], v0, s[4:7], s98 offen offset:2048 sc1\n"
"buffer_load_dwordx4 v[204:207], v0, s[4:7], s98 offen offset:3072 sc1\n"
"buffer_load_dwordx4 v[152:155], v1, s[4:7], s98 offen sc1\n"
"buffer_load_dwordx4 v[208:211], v1, s[4:7], s98 offen offset:1024 sc1\n"
"buffer_load_dwordx4 v[120:123], v1, s[4:7], s98 offen offset:2048 sc1\n"
"buffer_load_dwordx4 v[132:135], v1, s[4:7], s98 offen offset:3072 sc1\n"
"s_add_u32 s98, s57, 0x40000\n"
"buffer_load_dwordx4 v[216:219], v0, s[4:7], s98 offen sc1\n"
"buffer_load_dwordx4 v[128:131], v0, s[4:7], s98 offen offset:1024 sc1\n"
"buffer_load_dwordx4 v[180:183], v0, s[4:7], s98 offen offset:2048 sc1\n"
"buffer_load_dwordx4 v[100:103], v0, s[4:7], s98 offen offset:3072 sc1\n"
"buffer_load_dwordx4 v[184:187], v1, s[4:7], s98 offen sc1\n"
"buffer_load_dwordx4 v[104:107], v1, s[4:7], s98 offen offset:1024 sc1\n"
"buffer_load_dwordx4 v[108:111], v1, s[4:7], s98 offen offset:2048 sc1\n"
"buffer_load_dwordx4 v[136:139], v1, s[4:7], s98 offen offset:3072 sc1\n"
"s_add_u32 s98, s58, 0x40000\n"
"buffer_load_dwordx4 v[144:147], v0, s[4:7], s98 offen sc1\n"
"buffer_load_dwordx4 v[140:143], v0, s[4:7], s98 offen offset:1024 sc1\n"
"buffer_load_dwordx4 v[116:119], v0, s[4:7], s98 offen offset:2048 sc1\n"
"buffer_load_dwordx4 v[112:115], v0, s[4:7], s98 offen offset:3072 sc1\n"
"buffer_load_dwordx4 v[92:95], v1, s[4:7], s98 offen sc1\n"
"buffer_load_dwordx4 v[96:99], v1, s[4:7], s98 offen offset:1024 sc1\n"
"buffer_load_dwordx4 v[148:151], v1, s[4:7], s98 offen offset:2048 sc1\n"
"s_waitcnt vmcnt(0)\n"
"v_max_u32_e32 v255, v176, v177\n"
"v_max3_u32 v255, v255, v178, v179\n"
"v_max3_u32 v255, v255, v200, v201\n"
"v_max3_u32 v255, v255, v202, v203\n"
"v_max3_u32 v255, v255, v188, v189\n"
"v_max3_u32 v255, v255, v190, v191\n"
"v_max3_u32 v255, v255, v204, v205\n"
"v_max3_u32 v255, v255, v206, v207\n"
"v_max3_u32 v255, v255, v152, v153\n"
"v_max3_u32 v255, v255, v154, v155\n"
"v_max3_u32 v255, v255, v208, v209\n"
"v_max3_u32 v255, v255, v210, v211\n"
"v_max3_u32 v255, v255, v120, v121\n"
"v_max3_u32 v255, v255, v122, v123\n"
"v_max3_u32 v255, v255, v132, v133\n"
"v_max3_u32 v255, v255, v134, v135\n"
"v_cmp_eq_u32_e32 vcc, -1, v255\n"
"s_cbranch_vccz .Lmk_p30k\n"
"s_sub_u32 s1, s1, 1\n"
"s_cbranch_scc0 .Lmk_p30l\n"
".Lmk_p30k:\n"
"v_mfma_f32_16x16x32_bf16 v[88:91], a[128:131], v[176:179], 0\n"
"v_mfma_f32_16x16x32_bf16 v[88:91], a[132:135], v[200:203], v[88:91]\n"
"v_mfma_f32_16x16x32_bf16 v[88:91], a[136:139], v[188:191], v[88:91]\n"
"v_mfma_f32_16x16x32_bf16 v[88:91], a[140:143], v[204:207], v[88:91]\n"
"v_mfma_f32_16x16x32_bf16 v[88:91], a[144:147], v[152:155], v[88:91]\n"
"v_mfma_f32_16x16x32_bf16 v[88:91], a[148:151], v[208:211], v[88:91]\n"
"v_mfma_f32_16x16x32_bf16 v[88:91], a[152:155], v[120:123], v[88:91]\n"
"v_mfma_f32_16x16x32_bf16 v[88:91], a[156:159], v[132:135], v[88:91]\n"
"v_mfma_f32_16x16x32_bf16 v[124:127], a[188:191], v[132:135], 0\n"
"v_mfma_f32_16x16x32_bf16 v[124:127], a[184:187], v[120:123], v[124:127]\n"
"v_mfma_f32_16x16x32_bf16 v[124:127], a[180:183], v[208:211], v[124:127]\n"
"v_mfma_f32_16x16x32_bf16 v[124:127], a[176:179], v[152:155], v[124:127]\n"
"v_mfma_f32_16x16x32_bf16 v[124:127], a[172:175], v[204:207], v[124:127]\n"
"v_mfma_f32_16x16x32_bf16 v[124:127], a[168:171], v[188:191], v[124:127]\n"
"s_nop 7\n"
"s_branch .Lmk_p30r\n"
".Lmk_p31s:\n"
";\n"
"s_waitcnt vmcnt(16)\n"
"s_mov_b32 s1, 0x10000\n"
".Lmk_p31l:\n"
"s_add_u32 s97, s57, 0x40000\n"
"buffer_load_dwordx4 v[216:219], v0, s[4:7], s97 offen sc1\n"
"buffer_load_dwordx4 v[128:131], v0, s[4:7], s97 offen offset:1024 sc1\n"
"buffer_load_dwordx4 v[180:183], v0, s[4:7], s97 offen offset:2048 sc1\n"
"buffer_load_dwordx4 v[100:103], v0, s[4:7], s97 offen offset:3072 sc1\n"
"buffer_load_dwordx4 v[184:187], v1, s[4:7], s97 offen sc1\n"
"buffer_load_dwordx4 v[104:107], v1, s[4:7], s97 offen offset:1024 sc1\n"
"buffer_load_dwordx4 v[108:111], v1, s[4:7], s97 offen offset:2048 sc1\n"
"buffer_load_dwordx4 v[136:139], v1, s[4:7], s97 offen offset:3072 sc1\n"
"s_add_u32 s97, s58, 0x40000\n"
"buffer_load_dwordx4 v[144:147], v0, s[4:7], s97 offen sc1\n"
"buffer_load_dwordx4 v[140:143], v0, s[4:7], s97 offen offset:1024 sc1\n"
"buffer_load_dwordx4 v[116:119], v0, s[4:7], s97 offen offset:2048 sc1\n"
"buffer_load_dwordx4 v[112:115], v0, s[4:7], s97 offen offset:3072 sc1\n"
"buffer_load_dwordx4 v[92:95], v1, s[4:7], s97 offen sc1\n"
"buffer_load_dwordx4 v[96:99], v1, s[4:7], s97 offen offset:1024 sc1\n"
"buffer_load_dwordx4 v[148:151], v1, s[4:7], s97 offen offset:2048 sc1\n"
"buffer_load_dwordx4 v[88:91], v1, s[4:7], s97 offen offset:3072 sc1\n"
"s_add_u32 s97, s59, 0x40000\n"
"buffer_load_dwordx4 v[160:163], v0, s[4:7], s97 offen sc1\n"
"buffer_load_dwordx4 v[124:127], v0, s[4:7], s97 offen offset:1024 sc1\n"
"buffer_load_dwordx4 v[120:123], v0, s[4:7], s97 offen offset:2048 sc1\n"
"buffer_load_dwordx4 v[132:135], v0, s[4:7], s97 offen offset:3072 sc1\n"
"buffer_load_dwordx4 v[156:159], v1, s[4:7], s97 offen sc1\n"
"buffer_load_dwordx4 v[164:167], v1, s[4:7], s97 offen offset:1024 sc1\n"
"buffer_load_dwordx4 v[172:175], v1, s[4:7], s97 offen offset:2048 sc1\n"
"s_waitcnt vmcnt(0)\n"
"v_max_u32_e32 v255, v216, v217\n"
"v_max3_u32 v255, v255, v218, v219\n"
"v_max3_u32 v255, v255, v128, v129\n"
"v_max3_u32 v255, v255, v130, v131\n"
"v_max3_u32 v255, v255, v180, v181\n"
"v_max3_u32 v255, v255, v182, v183\n"
"v_max3_u32 v255, v255, v100, v101\n"
"v_max3_u32 v255, v255, v102, v103\n"
"v_max3_u32 v255, v255, v184, v185\n"
"v_max3_u32 v255, v255, v186, v187\n"
"v_max3_u32 v255, v255, v104, v105\n"
"v_max3_u32 v255, v255, v106, v107\n"
"v_max3_u32 v255, v255, v108, v109\n"
"v_max3_u32 v255, v255, v110, v111\n"
"v_max3_u32 v255, v255, v136, v137\n"
"v_max3_u32 v255, v255, v138, v139\n"
"v_cmp_eq_u32_e32 vcc, -1, v255\n"
"s_cbranch_vccz .Lmk_p31k\n"
"s_sub_u32 s1, s1, 1\n"
"s_cbranch_scc0 .Lmk_p31l\n"
".Lmk_p31k:\n"
"v_mfma_f32_16x16x32_bf16 v[152:155], a[128:131], v[216:219], 0\n"
"v_mfma_f32_16x16x32_bf16 v[152:155], a[132:135], v[128:131], v[152:155]\n"
"v_mfma_f32_16x16x32_bf16 v[152:155], a[136:139], v[180:183], v[152:155]\n"
"v_mfma_f32_16x16x32_bf16 v[152:155], a[140:143], v[100:103], v[152:155]\n"
"v_mfma_f32_16x16x32_bf16 v[152:155], a[144:147], v[184:187], v[152:155]\n"
"v_mfma_f32_16x16x32_bf16 v[152:155], a[148:151], v[104:107], v[152:155]\n"
"v_mfma_f32_16x16x32_bf16 v[152:155], a[152:155], v[108:111], v[152:155]\n"
"v_mfma_f32_16x16x32_bf16 v[152:155], a[156:159], v[136:139], v[152:155]\n"
"v_mfma_f32_16x16x32_bf16 v[168:171], a[188:191], v[136:139], 0\n"
"v_mfma_f32_16x16x32_bf16 v[168:171], a[184:187], v[108:111], v[168:171]\n"
"v_mfma_f32_16x16x32_bf16 v[168:171], a[180:183], v[104:107], v[168:171]\n"
"v_mfma_f32_16x16x32_bf16 v[168:171], a[176:179], v[184:187], v[168:171]\n"
"v_mfma_f32_16x16x32_bf16 v[168:171], a[172:175], v[100:103], v[168:171]\n"
"v_mfma_f32_16x16x32_bf16 v[168:171], a[168:171], v[180:183], v[168:171]\n"
"s_nop 7\n"
"s_branch .Lmk_p31r\n"
".Lmk_p32s:\n"
";\n"
"s_waitcnt vmcnt(16)\n"
"s_mov_b32 s1, 0x10000\n"
".Lmk_p32l:\n"
"s_add_u32 s97, s58, 0x40000\n"
"buffer_load_dwordx4 v[144:147], v0, s[4:7], s97 offen sc1\n"
"buffer_load_dwordx4 v[140:143], v0, s[4:7], s97 offen offset:1024 sc1\n"
"buffer_load_dwordx4 v[116:119], v0, s[4:7], s97 offen offset:2048 sc1\n"
"buffer_load_dwordx4 v[112:115], v0, s[4:7], s97 offen offset:3072 sc1\n"
"buffer_load_dwordx4 v[92:95], v1, s[4:7], s97 offen sc1\n"
"buffer_load_dwordx4 v[96:99], v1, s[4:7], s97 offen offset:1024 sc1\n"
"buffer_load_dwordx4 v[148:151], v1, s[4:7], s97 offen offset:2048 sc1\n"
"buffer_load_dwordx4 v[88:91], v1, s[4:7], s97 offen offset:3072 sc1\n"
"s_add_u32 s97, s59, 0x40000\n"
"buffer_load_dwordx4 v[160:163], v0, s[4:7], s97 offen sc1\n"
"buffer_load_dwordx4 v[124:127], v0, s[4:7], s97 offen offset:1024 sc1\n"
"buffer_load_dwordx4 v[120:123], v0, s[4:7], s97 offen offset:2048 sc1\n"
"buffer_load_dwordx4 v[132:135], v0, s[4:7], s97 offen offset:3072 sc1\n"
"buffer_load_dwordx4 v[156:159], v1, s[4:7], s97 offen sc1\n"
"buffer_load_dwordx4 v[164:167], v1, s[4:7], s97 offen offset:1024 sc1\n"
"buffer_load_dwordx4 v[172:175], v1, s[4:7], s97 offen offset:2048 sc1\n"
"buffer_load_dwordx4 v[152:155], v1, s[4:7], s97 offen offset:3072 sc1\n"
"s_waitcnt vmcnt(0)\n"
"v_max_u32_e32 v239, v144, v145\n"
"v_max3_u32 v239, v239, v146, v147\n"
"v_max3_u32 v239, v239, v140, v141\n"
"v_max3_u32 v239, v239, v142, v143\n"
"v_max3_u32 v239, v239, v116, v117\n"
"v_max3_u32 v239, v239, v118, v119\n"
"v_max3_u32 v239, v239, v112, v113\n"
"v_max3_u32 v239, v239, v114, v115\n"
"v_max3_u32 v239, v239, v92, v93\n"
"v_max3_u32 v239, v239, v94, v95\n"
"v_max3_u32 v239, v239, v96, v97\n"
"v_max3_u32 v239, v239, v98, v99\n"
"v_max3_u32 v239, v239, v148, v149\n"
"v_max3_u32 v239, v239, v150, v151\n"
"v_max3_u32 v239, v239, v88, v89\n"
"v_max3_u32 v239, v239, v90, v91\n"
"v_cmp_eq_u32_e32 vcc, -1, v239\n"
"s_cbranch_vccz .Lmk_p32k\n"
"s_sub_u32 s1, s1, 1\n"
"s_cbranch_scc0 .Lmk_p32l\n"
".Lmk_p32k:\n"
"v_mfma_f32_16x16x32_bf16 v[100:103], a[128:131], v[144:147], 0\n"
"v_mfma_f32_16x16x32_bf16 v[100:103], a[132:135], v[140:143], v[100:103]\n"
"v_mfma_f32_16x16x32_bf16 v[100:103], a[136:139], v[116:119], v[100:103]\n"
"v_mfma_f32_16x16x32_bf16 v[100:103], a[140:143], v[112:115], v[100:103]\n"
"v_mfma_f32_16x16x32_bf16 v[100:103], a[144:147], v[92:95], v[100:103]\n"
"v_mfma_f32_16x16x32_bf16 v[100:103], a[148:151], v[96:99], v[100:103]\n"
"v_mfma_f32_16x16x32_bf16 v[100:103], a[152:155], v[148:151], v[100:103]\n"
"v_mfma_f32_16x16x32_bf16 v[100:103], a[156:159], v[88:91], v[100:103]\n"
"v_mfma_f32_16x16x32_bf16 v[104:107], a[188:191], v[88:91], 0\n"
"v_mfma_f32_16x16x32_bf16 v[104:107], a[184:187], v[148:151], v[104:107]\n"
"v_mfma_f32_16x16x32_bf16 v[104:107], a[180:183], v[96:99], v[104:107]\n"
"v_mfma_f32_16x16x32_bf16 v[104:107], a[176:179], v[92:95], v[104:107]\n"
"v_mfma_f32_16x16x32_bf16 v[104:107], a[172:175], v[112:115], v[104:107]\n"
"v_mfma_f32_16x16x32_bf16 v[104:107], a[168:171], v[116:119], v[104:107]\n"
"s_nop 7\n"
"s_branch .Lmk_p32r\n"
".Lmk_p33s:\n"
";\n"
"s_waitcnt vmcnt(16)\n"
"s_mov_b32 s1, 0x10000\n"
".Lmk_p33l:\n"
"s_add_u32 s97, s59, 0x40000\n"
"buffer_load_dwordx4 v[160:163], v0, s[4:7], s97 offen sc1\n"
"buffer_load_dwordx4 v[124:127], v0, s[4:7], s97 offen offset:1024 sc1\n"
"buffer_load_dwordx4 v[120:123], v0, s[4:7], s97 offen offset:2048 sc1\n"
"buffer_load_dwordx4 v[132:135], v0, s[4:7], s97 offen offset:3072 sc1\n"
"buffer_load_dwordx4 v[156:159], v1, s[4:7], s97 offen sc1\n"
"buffer_load_dwordx4 v[164:167], v1, s[4:7], s97 offen offset:1024 sc1\n"
"buffer_load_dwordx4 v[172:175], v1, s[4:7], s97 offen offset:2048 sc1\n"
"buffer_load_dwordx4 v[152:155], v1, s[4:7], s97 offen offset:3072 sc1\n"
"s_add_u32 s97, s56, 0\n"
"buffer_load_dwordx4 v[100:103], v0, s[4:7], s97 offen sc1\n"
"buffer_load_dwordx4 v[180:183], v0, s[4:7], s97 offen offset:1024 sc1\n"
"buffer_load_dwordx4 v[104:107], v0, s[4:7], s97 offen offset:2048 sc1\n"
"buffer_load_dwordx4 v[88:91], v0, s[4:7], s97 offen offset:3072 sc1\n"
"buffer_load_dwordx4 v[92:95], v1, s[4:7], s97 offen sc1\n"
"buffer_load_dwordx4 v[116:119], v1, s[4:7], s97 offen offset:1024 sc1\n"
"buffer_load_dwordx4 v[136:139], v1, s[4:7], s97 offen offset:2048 sc1\n"
"buffer_load_dwordx4 v[140:143], v1, s[4:7], s97 offen offset:3072 sc1\n"
"s_add_u32 s97, s57, 0\n"
"buffer_load_dwordx4 v[148:151], v0, s[4:7], s97 offen sc1\n"
"s_waitcnt vmcnt(0)\n"
"v_max_u32_e32 v255, v160, v161\n"
"v_max3_u32 v255, v255, v162, v163\n"
"v_max3_u32 v255, v255, v124, v125\n"
"v_max3_u32 v255, v255, v126, v127\n"
"v_max3_u32 v255, v255, v120, v121\n"
"v_max3_u32 v255, v255, v122, v123\n"
"v_max3_u32 v255, v255, v132, v133\n"
"v_max3_u32 v255, v255, v134, v135\n"
"v_max3_u32 v255, v255, v156, v157\n"
"v_max3_u32 v255, v255, v158, v159\n"
"v_max3_u32 v255, v255, v164, v165\n"
"v_max3_u32 v255, v255, v166, v167\n"
"v_max3_u32 v255, v255, v172, v173\n"
"v_max3_u32 v255, v255, v174, v175\n"
"v_max3_u32 v255, v255, v152, v153\n"
"v_max3_u32 v255, v255, v154, v155\n"
"v_cmp_eq_u32_e32 vcc, -1, v255\n"
"s_cbranch_vccz .Lmk_p33k\n"
"s_sub_u32 s1, s1, 1\n"
"s_cbranch_scc0 .Lmk_p33l\n"
".Lmk_p33k:\n"
"v_mfma_f32_16x16x32_bf16 v[128:131], a[128:131], v[160:163], v[108:111]\n"
"v_mfma_f32_16x16x32_bf16 v[128:131], a[132:135], v[124:127], v[128:131]\n"
"v_mfma_f32_16x16x32_bf16 v[128:131], a[136:139], v[120:123], v[128:131]\n"
"v_mfma_f32_16x16x32_bf16 v[128:131], a[140:143], v[132:135], v[128:131]\n"
"v_mfma_f32_16x16x32_bf16 v[128:131], a[144:147], v[156:159], v[128:131]\n"
"v_mfma_f32_16x16x32_bf16 v[128:131], a[148:151], v[164:167], v[128:131]\n"
"v_mfma_f32_16x16x32_bf16 v[128:131], a[152:155], v[172:175], v[128:131]\n"
"v_mfma_f32_16x16x32_bf16 v[128:131], a[156:159], v[152:155], v[128:131]\n"
"v_mfma_f32_16x16x32_bf16 v[144:147], a[188:191], v[152:155], v[168:171]\n"
"v_mfma_f32_16x16x32_bf16 v[144:147], a[184:187], v[172:175], v[144:147]\n"
"v_mfma_f32_16x16x32_bf16 v[144:147], a[180:183], v[164:167], v[144:147]\n"
"v_mfma_f32_16x16x32_bf16 v[144:147], a[176:179], v[156:159], v[144:147]\n"
"v_mfma_f32_16x16x32_bf16 v[144:147], a[172:175], v[132:135], v[144:147]\n"
"v_mfma_f32_16x16x32_bf16 v[144:147], a[168:171], v[120:123], v[144:147]\n"
"s_nop 7\n"
"s_branch .Lmk_p33r\n"
".Lmk_p35s:\n"
";\n"
"s_waitcnt vmcnt(16)\n"
"s_mov_b32 s1, 0x10000\n"
".Lmk_p35l:\n"
"s_add_u32 s95, s56, 0\n"
"buffer_load_dwordx4 v[100:103], v0, s[4:7], s95 offen sc1\n"
"buffer_load_dwordx4 v[180:183], v0, s[4:7], s95 offen offset:1024 sc1\n"
"buffer_load_dwordx4 v[104:107], v0, s[4:7], s95 offen offset:2048 sc1\n"
"buffer_load_dwordx4 v[88:91], v0, s[4:7], s95 offen offset:3072 sc1\n"
"buffer_load_dwordx4 v[92:95], v1, s[4:7], s95 offen sc1\n"
"buffer_load_dwordx4 v[116:119], v1, s[4:7], s95 offen offset:1024 sc1\n"
"buffer_load_dwordx4 v[136:139], v1, s[4:7], s95 offen offset:2048 sc1\n"
"buffer_load_dwordx4 v[140:143], v1, s[4:7], s95 offen offset:3072 sc1\n"
"s_add_u32 s95, s57, 0\n"
"buffer_load_dwordx4 v[148:151], v0, s[4:7], s95 offen sc1\n"
"buffer_load_dwordx4 v[108:111], v0, s[4:7], s95 offen offset:1024 sc1\n"
"buffer_load_dwordx4 v[112:115], v0, s[4:7], s95 offen offset:2048 sc1\n"
"buffer_load_dwordx4 v[196:199], v0, s[4:7], s95 offen offset:3072 sc1\n"
"buffer_load_dwordx4 v[200:203], v1, s[4:7], s95 offen sc1\n"
"buffer_load_dwordx4 v[128:131], v1, s[4:7], s95 offen offset:1024 sc1\n"
"buffer_load_dwordx4 v[120:123], v1, s[4:7], s95 offen offset:2048 sc1\n"
"buffer_load_dwordx4 v[132:135], v1, s[4:7], s95 offen offset:3072 sc1\n"
"s_add_u32 s95, s58, 0\n"
"buffer_load_dwordx4 v[152:155], v0, s[4:7], s95 offen sc1\n"
"buffer_load_dwordx4 v[160:163], v0, s[4:7], s95 offen offset:1024 sc1\n"
"buffer_load_dwordx4 v[124:127], v0, s[4:7], s95 offen offset:2048 sc1\n"
"s_waitcnt vmcnt(0)\n"
"v_max_u32_e32 v255, v100, v101\n"
"v_max3_u32 v255, v255, v102, v103\n"
"v_max3_u32 v255, v255, v180, v181\n"
"v_max3_u32 v255, v255, v182, v183\n"
"v_max3_u32 v255, v255, v104, v105\n"
"v_max3_u32 v255, v255, v106, v107\n"
"v_max3_u32 v255, v255, v88, v89\n"
"v_max3_u32 v255, v255, v90, v91\n"
"v_max3_u32 v255, v255, v92, v93\n"
"v_max3_u32 v255, v255, v94, v95\n"
"v_max3_u32 v255, v255, v116, v117\n"
"v_max3_u32 v255, v255, v118, v119\n"
"v_max3_u32 v255, v255, v136, v137\n"
"v_max3_u32 v255, v255, v138, v139\n"
"v_max3_u32 v255, v255, v140, v141\n"
"v_max3_u32 v255, v255, v142, v143\n"
"v_cmp_eq_u32_e32 vcc, -1, v255\n"
"s_cbranch_vccz .Lmk_p35k\n"
"s_sub_u32 s1, s1, 1\n"
"s_cbranch_scc0 .Lmk_p35l\n"
".Lmk_p35k:\n"
"v_mfma_f32_16x16x32_bf16 v[144:147], a[128:131], v[100:103], 0\n"
"v_mfma_f32_16x16x32_bf16 v[144:147], a[132:135], v[180:183], v[144:147]\n"
"v_mfma_f32_16x16x32_bf16 v[144:147], a[136:139], v[104:107], v[144:147]\n"
"v_mfma_f32_16x16x32_bf16 v[144:147], a[140:143], v[88:91], v[144:147]\n"
"v_mfma_f32_16x16x32_bf16 v[144:147], a[144:147], v[92:95], v[144:147]\n"
"v_mfma_f32_16x16x32_bf16 v[144:147], a[148:151], v[116:119], v[144:147]\n"
"v_mfma_f32_16x16x32_bf16 v[144:147], a[152:155], v[136:139], v[144:147]\n"
"v_mfma_f32_16x16x32_bf16 v[144:147], a[156:159], v[140:143], v[144:147]\n"
"v_mfma_f32_16x16x32_bf16 v[96:99], a[188:191], v[140:143], 0\n"
"v_mfma_f32_16x16x32_bf16 v[96:99], a[184:187], v[136:139], v[96:99]\n"
"v_mfma_f32_16x16x32_bf16 v[96:99], a[180:183], v[116:119], v[96:99]\n"
"v_mfma_f32_16x16x32_bf16 v[96:99], a[176:179], v[92:95], v[96:99]\n"
"v_mfma_f32_16x16x32_bf16 v[96:99], a[172:175], v[88:91], v[96:99]\n"
"v_mfma_f32_16x16x32_bf16 v[96:99], a[168:171], v[104:107], v[96:99]\n"
"s_nop 7\n"
"s_branch .Lmk_p35r\n"
".Lmk_p36s:\n"
";\n"
"s_waitcnt vmcnt(16)\n"
"s_mov_b32 s1, 0x10000\n"
".Lmk_p36l:\n"
"s_add_u32 s97, s57, 0\n"
"buffer_load_dwordx4 v[148:151], v0, s[4:7], s97 offen sc1\n"
"buffer_load_dwordx4 v[108:111], v0, s[4:7], s97 offen offset:1024 sc1\n"
"buffer_load_dwordx4 v[112:115], v0, s[4:7], s97 offen offset:2048 sc1\n"
"buffer_load_dwordx4 v[196:199], v0, s[4:7], s97 offen offset:3072 sc1\n"
"buffer_load_dwordx4 v[200:203], v1, s[4:7], s97 offen sc1\n"
"buffer_load_dwordx4 v[128:131], v1, s[4:7], s97 offen offset:1024 sc1\n"
"buffer_load_dwordx4 v[120:123], v1, s[4:7], s97 offen offset:2048 sc1\n"
"buffer_load_dwordx4 v[132:135], v1, s[4:7], s97 offen offset:3072 sc1\n"
"s_add_u32 s97, s58, 0\n"
"buffer_load_dwordx4 v[152:155], v0, s[4:7], s97 offen sc1\n"
"buffer_load_dwordx4 v[160:163], v0, s[4:7], s97 offen offset:1024 sc1\n"
"buffer_load_dwordx4 v[124:127], v0, s[4:7], s97 offen offset:2048 sc1\n"
"buffer_load_dwordx4 v[144:147], v0, s[4:7], s97 offen offset:3072 sc1\n"
"buffer_load_dwordx4 v[156:159], v1, s[4:7], s97 offen sc1\n"
"buffer_load_dwordx4 v[172:175], v1, s[4:7], s97 offen offset:1024 sc1\n"
"buffer_load_dwordx4 v[116:119], v1, s[4:7], s97 offen offset:2048 sc1\n"
"buffer_load_dwordx4 v[136:139], v1, s[4:7], s97 offen offset:3072 sc1\n"
"s_add_u32 s97, s59, 0\n"
"buffer_load_dwordx4 v[176:179], v0, s[4:7], s97 offen sc1\n"
"buffer_load_dwordx4 v[180:183], v0, s[4:7], s97 offen offset:1024 sc1\n"
"buffer_load_dwordx4 v[168:171], v0, s[4:7], s97 offen offset:2048 sc1\n"
"s_waitcnt vmcnt(0)\n"
"v_max_u32_e32 v255, v148, v149\n"
"v_max3_u32 v255, v255, v150, v151\n"
"v_max3_u32 v255, v255, v108, v109\n"
"v_max3_u32 v255, v255, v110, v111\n"
"v_max3_u32 v255, v255, v112, v113\n"
"v_max3_u32 v255, v255, v114, v115\n"
"v_max3_u32 v255, v255, v196, v197\n"
"v_max3_u32 v255, v255, v198, v199\n"
"v_max3_u32 v255, v255, v200, v201\n"
"v_max3_u32 v255, v255, v202, v203\n"
"v_max3_u32 v255, v255, v128, v129\n"
"v_max3_u32 v255, v255, v130, v131\n"
"v_max3_u32 v255, v255, v120, v121\n"
"v_max3_u32 v255, v255, v122, v123\n"
"v_max3_u32 v255, v255, v132, v133\n"
"v_max3_u32 v255, v255, v134, v135\n"
"v_cmp_eq_u32_e32 vcc, -1, v255\n"
"s_cbranch_vccz .Lmk_p36k\n"
"s_sub_u32 s1, s1, 1\n"
"s_cbranch_scc0 .Lmk_p36l\n"
".Lmk_p36k:\n"
"v_mfma_f32_16x16x32_bf16 v[88:91], a[128:131], v[148:151], 0\n"
"v_mfma_f32_16x16x32_bf16 v[88:91], a[132:135], v[108:111], v[88:91]\n"
"v_mfma_f32_16x16x32_bf16 v[88:91], a[136:139], v[112:115], v[88:91]\n"
"v_mfma_f32_16x16x32_bf16 v[88:91], a[140:143], v[196:199], v[88:91]\n"
"v_mfma_f32_16x16x32_bf16 v[88:91], a[144:147], v[200:203], v[88:91]\n"
"v_mfma_f32_16x16x32_bf16 v[88:91], a[148:151], v[128:131], v[88:91]\n"
"v_mfma_f32_16x16x32_bf16 v[88:91], a[152:155], v[120:123], v[88:91]\n"
"v_mfma_f32_16x16x32_bf16 v[88:91], a[156:159], v[132:135], v[88:91]\n"
"v_mfma_f32_16x16x32_bf16 v[92:95], a[188:191], v[132:135], 0\n"
"v_mfma_f32_16x16x32_bf16 v[92:95], a[184:187], v[120:123], v[92:95]\n"
"v_mfma_f32_16x16x32_bf16 v[92:95], a[180:183], v[128:131], v[92:95]\n"
"v_mfma_f32_16x16x32_bf16 v[92:95], a[176:179], v[200:203], v[92:95]\n"
"v_mfma_f32_16x16x32_bf16 v[92:95], a[172:175], v[196:199], v[92:95]\n"
"v_mfma_f32_16x16x32_bf16 v[92:95], a[168:171], v[112:115], v[92:95]\n"
"s_nop 7\n"
"s_branch .Lmk_p36r\n"
".Lmk_p37s:\n"
";\n"
"s_waitcnt vmcnt(16)\n"
"s_mov_b32 s1, 0x10000\n"
".Lmk_p37l:\n"
"s_add_u32 s97, s58, 0\n"
"buffer_load_dwordx4 v[152:155], v0, s[4:7], s97 offen sc1\n"
"buffer_load_dwordx4 v[160:163], v0, s[4:7], s97 offen offset:1024 sc1\n"
"buffer_load_dwordx4 v[124:127], v0, s[4:7], s97 offen offset:2048 sc1\n"
"buffer_load_dwordx4 v[144:147], v0, s[4:7], s97 offen offset:3072 sc1\n"
"buffer_load_dwordx4 v[156:159], v1, s[4:7], s97 offen sc1\n"
"buffer_load_dwordx4 v[172:175], v1, s[4:7], s97 offen offset:1024 sc1\n"
"buffer_load_dwordx4 v[116:119], v1, s[4:7], s97 offen offset:2048 sc1\n"
"buffer_load_dwordx4 v[136:139], v1, s[4:7], s97 offen offset:3072 sc1\n"
"s_add_u32 s97, s59, 0\n"
"buffer_load_dwordx4 v[176:179], v0, s[4:7], s97 offen sc1\n"
"buffer_load_dwordx4 v[180:183], v0, s[4:7], s97 offen offset:1024 sc1\n"
"buffer_load_dwordx4 v[168:171], v0, s[4:7], s97 offen offset:2048 sc1\n"
"buffer_load_dwordx4 v[184:187], v0, s[4:7], s97 offen offset:3072 sc1\n"
"buffer_load_dwordx4 v[188:191], v1, s[4:7], s97 offen sc1\n"
"buffer_load_dwordx4 v[164:167], v1, s[4:7], s97 offen offset:1024 sc1\n"
"buffer_load_dwordx4 v[192:195], v1, s[4:7], s97 offen offset:2048 sc1\n"
"buffer_load_dwordx4 v[196:199], v1, s[4:7], s97 offen offset:3072 sc1\n"
"s_add_u32 s97, s52, 0x440000\n"
"buffer_load_dwordx4 v[88:91], v0, s[4:7], s97 offen sc1\n"
"buffer_load_dwordx4 v[92:95], v0, s[4:7], s97 offen offset:1024 sc1\n"
"s_waitcnt vmcnt(0)\n"
"v_max_u32_e32 v239, v152, v153\n"
"v_max3_u32 v239, v239, v154, v155\n"
"v_max3_u32 v239, v239, v160, v161\n"
"v_max3_u32 v239, v239, v162, v163\n"
"v_max3_u32 v239, v239, v124, v125\n"
"v_max3_u32 v239, v239, v126, v127\n"
"v_max3_u32 v239, v239, v144, v145\n"
"v_max3_u32 v239, v239, v146, v147\n"
"v_max3_u32 v239, v239, v156, v157\n"
"v_max3_u32 v239, v239, v158, v159\n"
"v_max3_u32 v239, v239, v172, v173\n"
"v_max3_u32 v239, v239, v174, v175\n"
"v_max3_u32 v239, v239, v116, v117\n"
"v_max3_u32 v239, v239, v118, v119\n"
"v_max3_u32 v239, v239, v136, v137\n"
"v_max3_u32 v239, v239, v138, v139\n"
"v_cmp_eq_u32_e32 vcc, -1, v239\n"
"s_cbranch_vccz .Lmk_p37k\n"
"s_sub_u32 s1, s1, 1\n"
"s_cbranch_scc0 .Lmk_p37l\n"
".Lmk_p37k:\n"
"v_mfma_f32_16x16x32_bf16 v[96:99], a[128:131], v[152:155], 0\n"
"v_mfma_f32_16x16x32_bf16 v[96:99], a[132:135], v[160:163], v[96:99]\n"
"v_mfma_f32_16x16x32_bf16 v[96:99], a[136:139], v[124:127], v[96:99]\n"
"v_mfma_f32_16x16x32_bf16 v[96:99], a[140:143], v[144:147], v[96:99]\n"
"v_mfma_f32_16x16x32_bf16 v[96:99], a[144:147], v[156:159], v[96:99]\n"
"v_mfma_f32_16x16x32_bf16 v[96:99], a[148:151], v[172:175], v[96:99]\n"
"v_mfma_f32_16x16x32_bf16 v[96:99], a[152:155], v[116:119], v[96:99]\n"
"v_mfma_f32_16x16x32_bf16 v[96:99], a[156:159], v[136:139], v[96:99]\n"
"v_mfma_f32_16x16x32_bf16 v[104:107], a[188:191], v[136:139], 0\n"
"v_mfma_f32_16x16x32_bf16 v[104:107], a[184:187], v[116:119], v[104:107]\n"
"v_mfma_f32_16x16x32_bf16 v[104:107], a[180:183], v[172:175], v[104:107]\n"
"v_mfma_f32_16x16x32_bf16 v[104:107], a[176:179], v[156:159], v[104:107]\n"
"v_mfma_f32_16x16x32_bf16 v[104:107], a[172:175], v[144:147], v[104:107]\n"
"v_mfma_f32_16x16x32_bf16 v[104:107], a[168:171], v[124:127], v[104:107]\n"
"s_nop 7\n"
"s_branch .Lmk_p37r\n"
".Lmk_p38s:\n"
";\n"
"s_waitcnt vmcnt(16)\n"
"s_mov_b32 s1, 0x10000\n"
".Lmk_p38l:\n"
"s_add_u32 s97, s59, 0\n"
"buffer_load_dwordx4 v[176:179], v0, s[4:7], s97 offen sc1\n"
"buffer_load_dwordx4 v[180:183], v0, s[4:7], s97 offen offset:1024 sc1\n"
"buffer_load_dwordx4 v[168:171], v0, s[4:7], s97 offen offset:2048 sc1\n"
"buffer_load_dwordx4 v[184:187], v0, s[4:7], s97 offen offset:3072 sc1\n"
"buffer_load_dwordx4 v[188:191], v1, s[4:7], s97 offen sc1\n"
"buffer_load_dwordx4 v[164:167], v1, s[4:7], s97 offen offset:1024 sc1\n"
"buffer_load_dwordx4 v[192:195], v1, s[4:7], s97 offen offset:2048 sc1\n"
"buffer_load_dwordx4 v[196:199], v1, s[4:7], s97 offen offset:3072 sc1\n"
"s_add_u32 s97, s52, 0x440000\n"
"buffer_load_dwordx4 v[88:91], v0, s[4:7], s97 offen sc1\n"
"buffer_load_dwordx4 v[92:95], v0, s[4:7], s97 offen offset:1024 sc1\n"
"buffer_load_dwordx4 v[96:99], v0, s[4:7], s97 offen offset:2048 sc1\n"
"buffer_load_dwordx4 v[100:103], v0, s[4:7], s97 offen offset:3072 sc1\n"
"buffer_load_dwordx4 v[104:107], v1, s[4:7], s97 offen sc1\n"
"buffer_load_dwordx4 v[112:115], v1, s[4:7], s97 offen offset:1024 sc1\n"
"buffer_load_dwordx4 v[116:119], v1, s[4:7], s97 offen offset:2048 sc1\n"
"buffer_load_dwordx4 v[124:127], v1, s[4:7], s97 offen offset:3072 sc1\n"
"s_add_u32 s97, s53, 0x440000\n"
"buffer_load_dwordx4 v[128:131], v0, s[4:7], s97 offen sc1\n"
"buffer_load_dwordx4 v[132:135], v0, s[4:7], s97 offen offset:1024 sc1\n"
"buffer_load_dwordx4 v[136:139], v0, s[4:7], s97 offen offset:2048 sc1\n"
"s_waitcnt vmcnt(0)\n"
"v_max_u32_e32 v255, v176, v177\n"
"v_max3_u32 v255, v255, v178, v179\n"
"v_max3_u32 v255, v255, v180, v181\n"
"v_max3_u32 v255, v255, v182, v183\n"
"v_max3_u32 v255, v255, v168, v169\n"
"v_max3_u32 v255, v255, v170, v171\n"
"v_max3_u32 v255, v255, v184, v185\n"
"v_max3_u32 v255, v255, v186, v187\n"
"v_max3_u32 v255, v255, v188, v189\n"
"v_max3_u32 v255, v255, v190, v191\n"
"v_max3_u32 v255, v255, v164, v165\n"
"v_max3_u32 v255, v255, v166, v167\n"
"v_max3_u32 v255, v255, v192, v193\n"
"v_max3_u32 v255, v255, v194, v195\n"
"v_max3_u32 v255, v255, v196, v197\n"
"v_max3_u32 v255, v255, v198, v199\n"
"v_cmp_eq_u32_e32 vcc, -1, v255\n"
"s_cbranch_vccz .Lmk_p38k\n"
"s_sub_u32 s1, s1, 1\n"
"s_cbranch_scc0 .Lmk_p38l\n"
".Lmk_p38k:\n"
"v_mfma_f32_16x16x32_bf16 v[172:175], a[128:131], v[176:179], v[140:143]\n"
"v_mfma_f32_16x16x32_bf16 v[172:175], a[132:135], v[180:183], v[172:175]\n"
"v_mfma_f32_16x16x32_bf16 v[172:175], a[136:139], v[168:171], v[172:175]\n"
"v_mfma_f32_16x16x32_bf16 v[172:175], a[140:143], v[184:187], v[172:175]\n"
"v_mfma_f32_16x16x32_bf16 v[172:175], a[144:147], v[188:191], v[172:175]\n"
"v_mfma_f32_16x16x32_bf16 v[172:175], a[148:151], v[164:167], v[172:175]\n"
"v_mfma_f32_16x16x32_bf16 v[172:175], a[152:155], v[192:195], v[172:175]\n"
"v_mfma_f32_16x16x32_bf16 v[172:175], a[156:159], v[196:199], v[172:175]\n"
"v_mfma_f32_16x16x32_bf16 v[204:207], a[188:191], v[196:199], v[148:151]\n"
"v_mfma_f32_16x16x32_bf16 v[204:207], a[184:187], v[192:195], v[204:207]\n"
"v_mfma_f32_16x16x32_bf16 v[204:207], a[180:183], v[164:167], v[204:207]\n"
"v_mfma_f32_16x16x32_bf16 v[204:207], a[176:179], v[188:191], v[204:207]\n"
"v_mfma_f32_16x16x32_bf16 v[204:207], a[172:175], v[184:187], v[204:207]\n"
"v_mfma_f32_16x16x32_bf16 v[204:207], a[168:171], v[168:171], v[204:207]\n"
"s_nop 7\n"
"s_branch .Lmk_p38r\n"
".Lmk_p43s:\n"
";\n"
"s_waitcnt vmcnt(16)\n"
"s_mov_b32 s1, 0x10000\n"
".Lmk_p43l:\n"
"s_add_u32 s96, s52, 0x440000\n"
"buffer_load_dwordx4 v[88:91], v0, s[4:7], s96 offen sc1\n"
"buffer_load_dwordx4 v[92:95], v0, s[4:7], s96 offen offset:1024 sc1\n"
"buffer_load_dwordx4 v[96:99], v0, s[4:7], s96 offen offset:2048 sc1\n"
"buffer_load_dwordx4 v[100:103], v0, s[4:7], s96 offen offset:3072 sc1\n"
"buffer_load_dwordx4 v[104:107], v1, s[4:7], s96 offen sc1\n"
"buffer_load_dwordx4 v[112:115], v1, s[4:7], s96 offen offset:1024 sc1\n"
"buffer_load_dwordx4 v[116:119], v1, s[4:7], s96 offen offset:2048 sc1\n"
"buffer_load_dwordx4 v[124:127], v1, s[4:7], s96 offen offset:3072 sc1\n"
"s_add_u32 s96, s53, 0x440000\n"
"buffer_load_dwordx4 v[128:131], v0, s[4:7], s96 offen sc1\n"
"buffer_load_dwordx4 v[132:135], v0, s[4:7], s96 offen offset:1024 sc1\n"
"buffer_load_dwordx4 v[136:139], v0, s[4:7], s96 offen offset:2048 sc1\n"
"buffer_load_dwordx4 v[140:143], v0, s[4:7], s96 offen offset:3072 sc1\n"
"buffer_load_dwordx4 v[144:147], v1, s[4:7], s96 offen sc1\n"
"buffer_load_dwordx4 v[156:159], v1, s[4:7], s96 offen offset:1024 sc1\n"
"buffer_load_dwordx4 v[160:163], v1, s[4:7], s96 offen offset:2048 sc1\n"
"buffer_load_dwordx4 v[164:167], v1, s[4:7], s96 offen offset:3072 sc1\n"
"s_add_u32 s96, s54, 0x440000\n"
"buffer_load_dwordx4 v[176:179], v0, s[4:7], s96 offen sc1\n"
"buffer_load_dwordx4 v[108:111], v0, s[4:7], s96 offen offset:1024 sc1\n"
"s_waitcnt vmcnt(0)\n"
"v_max_u32_e32 v252, v88, v89\n"
"v_max3_u32 v252, v252, v90, v91\n"
"v_max3_u32 v252, v252, v92, v93\n"
"v_max3_u32 v252, v252, v94, v95\n"
"v_max3_u32 v252, v252, v96, v97\n"
"v_max3_u32 v252, v252, v98, v99\n"
"v_max3_u32 v252, v252, v100, v101\n"
"v_max3_u32 v252, v252, v102, v103\n"
"v_max3_u32 v252, v252, v104, v105\n"
"v_max3_u32 v252, v252, v106, v107\n"
"v_max3_u32 v252, v252, v112, v113\n"
"v_max3_u32 v252, v252, v114, v115\n"
"v_max3_u32 v252, v252, v116, v117\n"
"v_max3_u32 v252, v252, v118, v119\n"
"v_max3_u32 v252, v252, v124, v125\n"
"v_max3_u32 v252, v252, v126, v127\n"
"v_cmp_eq_u32_e32 vcc, -1, v252\n"
"s_cbranch_vccz .Lmk_p43k\n"
"s_sub_u32 s1, s1, 1\n"
"s_cbranch_scc0 .Lmk_p43l\n"
".Lmk_p43k:\n"
"v_mfma_f32_16x16x32_bf16 v[172:175], a[0:3], v[88:91], 0\n"
"v_mfma_f32_16x16x32_bf16 v[172:175], a[4:7], v[92:95], v[172:175]\n"
"v_mfma_f32_16x16x32_bf16 v[172:175], a[8:11], v[96:99], v[172:175]\n"
"v_mfma_f32_16x16x32_bf16 v[172:175], a[12:15], v[100:103], v[172:175]\n"
"v_mfma_f32_16x16x32_bf16 v[172:175], a[16:19], v[104:107], v[172:175]\n"
"v_mfma_f32_16x16x32_bf16 v[172:175], a[20:23], v[112:115], v[172:175]\n"
"v_mfma_f32_16x16x32_bf16 v[172:175], a[24:27], v[116:119], v[172:175]\n"
"v_mfma_f32_16x16x32_bf16 v[172:175], a[28:31], v[124:127], v[172:175]\n"
"v_mfma_f32_16x16x32_bf16 v[180:183], a[60:63], v[124:127], 0\n"
"v_mfma_f32_16x16x32_bf16 v[180:183], a[56:59], v[116:119], v[180:183]\n"
"v_mfma_f32_16x16x32_bf16 v[180:183], a[52:55], v[112:115], v[180:183]\n"
"v_mfma_f32_16x16x32_bf16 v[180:183], a[48:51], v[104:107], v[180:183]\n"
"v_mfma_f32_16x16x32_bf16 v[180:183], a[44:47], v[100:103], v[180:183]\n"
"v_mfma_f32_16x16x32_bf16 v[180:183], a[40:43], v[96:99], v[180:183]\n"
"s_nop 7\n"
"s_branch .Lmk_p43r\n"
".Lmk_p44s:\n"
";\n"
"s_waitcnt vmcnt(16)\n"
"s_mov_b32 s1, 0x10000\n"
".Lmk_p44l:\n"
"s_add_u32 s97, s53, 0x440000\n"
"buffer_load_dwordx4 v[128:131], v0, s[4:7], s97 offen sc1\n"
"buffer_load_dwordx4 v[132:135], v0, s[4:7], s97 offen offset:1024 sc1\n"
"buffer_load_dwordx4 v[136:139], v0, s[4:7], s97 offen offset:2048 sc1\n"
"buffer_load_dwordx4 v[140:143], v0, s[4:7], s97 offen offset:3072 sc1\n"
"buffer_load_dwordx4 v[144:147], v1, s[4:7], s97 offen sc1\n"
"buffer_load_dwordx4 v[156:159], v1, s[4:7], s97 offen offset:1024 sc1\n"
"buffer_load_dwordx4 v[160:163], v1, s[4:7], s97 offen offset:2048 sc1\n"
"buffer_load_dwordx4 v[164:167], v1, s[4:7], s97 offen offset:3072 sc1\n"
"s_add_u32 s97, s54, 0x440000\n"
"buffer_load_dwordx4 v[176:179], v0, s[4:7], s97 offen sc1\n"
"buffer_load_dwordx4 v[108:111], v0, s[4:7], s97 offen offset:1024 sc1\n"
"buffer_load_dwordx4 v[148:151], v0, s[4:7], s97 offen offset:2048 sc1\n"
"buffer_load_dwordx4 v[184:187], v0, s[4:7], s97 offen offset:3072 sc1\n"
"buffer_load_dwordx4 v[152:155], v1, s[4:7], s97 offen sc1\n"
"buffer_load_dwordx4 v[192:195], v1, s[4:7], s97 offen offset:1024 sc1\n"
"buffer_load_dwordx4 v[196:199], v1, s[4:7], s97 offen offset:2048 sc1\n"
"buffer_load_dwordx4 v[200:203], v1, s[4:7], s97 offen offset:3072 sc1\n"
"s_add_u32 s97, s55, 0x440000\n"
"buffer_load_dwordx4 v[92:95], v0, s[4:7], s97 offen sc1\n"
"buffer_load_dwordx4 v[96:99], v0, s[4:7], s97 offen offset:1024 sc1\n"
"s_waitcnt vmcnt(0)\n"
"v_max_u32_e32 v247, v128, v129\n"
"v_max3_u32 v247, v247, v130, v131\n"
"v_max3_u32 v247, v247, v132, v133\n"
"v_max3_u32 v247, v247, v134, v135\n"
"v_max3_u32 v247, v247, v136, v137\n"
"v_max3_u32 v247, v247, v138, v139\n"
"v_max3_u32 v247, v247, v140, v141\n"
"v_max3_u32 v247, v247, v142, v143\n"
"v_max3_u32 v247, v247, v144, v145\n"
"v_max3_u32 v247, v247, v146, v147\n"
"v_max3_u32 v247, v247, v156, v157\n"
"v_max3_u32 v247, v247, v158, v159\n"
"v_max3_u32 v247, v247, v160, v161\n"
"v_max3_u32 v247, v247, v162, v163\n"
"v_max3_u32 v247, v247, v164, v165\n"
"v_max3_u32 v247, v247, v166, v167\n"
"v_cmp_eq_u32_e32 vcc, -1, v247\n"
"s_cbranch_vccz .Lmk_p44k\n"
"s_sub_u32 s1, s1, 1\n"
"s_cbranch_scc0 .Lmk_p44l\n"
".Lmk_p44k:\n"
"v_mfma_f32_16x16x32_bf16 v[88:91], a[0:3], v[128:131], 0\n"
"v_mfma_f32_16x16x32_bf16 v[88:91], a[4:7], v[132:135], v[88:91]\n"
"v_mfma_f32_16x16x32_bf16 v[88:91], a[8:11], v[136:139], v[88:91]\n"
"v_mfma_f32_16x16x32_bf16 v[88:91], a[12:15], v[140:143], v[88:91]\n"
"v_mfma_f32_16x16x32_bf16 v[88:91], a[16:19], v[144:147], v[88:91]\n"
"v_mfma_f32_16x16x32_bf16 v[88:91], a[20:23], v[156:159], v[88:91]\n"
"v_mfma_f32_16x16x32_bf16 v[88:91], a[24:27], v[160:163], v[88:91]\n"
"v_mfma_f32_16x16x32_bf16 v[88:91], a[28:31], v[164:167], v[88:91]\n"
"v_mfma_f32_16x16x32_bf16 v[100:103], a[60:63], v[164:167], 0\n"
"v_mfma_f32_16x16x32_bf16 v[100:103], a[56:59], v[160:163], v[100:103]\n"
"v_mfma_f32_16x16x32_bf16 v[100:103], a[52:55], v[156:159], v[100:103]\n"
"v_mfma_f32_16x16x32_bf16 v[100:103], a[48:51], v[144:147], v[100:103]\n"
"v_mfma_f32_16x16x32_bf16 v[100:103], a[44:47], v[140:143], v[100:103]\n"
"v_mfma_f32_16x16x32_bf16 v[100:103], a[40:43], v[136:139], v[100:103]\n"
"s_nop 7\n"
"s_branch .Lmk_p44r\n"
".Lmk_m47s:\n"
";\n"
"s_waitcnt vmcnt(16)\n"
"s_mov_b32 s1, 0x10000\n"
".Lmk_m47l:\n"
"s_add_u32 s97, s62, 0x44000\n"
"buffer_load_dword v255, v10, s[4:7], s97 offen sc1\n"
"buffer_load_dword v254, v10, s[4:7], s97 offen offset:256 sc1\n"
"buffer_load_dword v253, v10, s[4:7], s97 offen offset:512 sc1\n"
"buffer_load_dword v252, v10, s[4:7], s97 offen offset:768 sc1\n"
"buffer_load_dword v251, v10, s[4:7], s97 offen offset:1024 sc1\n"
"buffer_load_dword v250, v10, s[4:7], s97 offen offset:1280 sc1\n"
"buffer_load_dword v249, v10, s[4:7], s97 offen offset:1536 sc1\n"
"buffer_load_dword v248, v10, s[4:7], s97 offen offset:1792 sc1\n"
"s_waitcnt vmcnt(0)\n"
"v_max_u32_e32 v247, v255, v254\n"
"v_max3_u32 v247, v247, v253, v252\n"
"v_max3_u32 v247, v247, v251, v250\n"
"v_max3_u32 v247, v247, v249, v248\n"
"v_cmp_eq_u32_e32 vcc, -1, v247\n"
"s_cbranch_vccz .Lmk_m47k\n"
"s_sub_u32 s1, s1, 1\n"
"s_cbranch_scc0 .Lmk_m47l\n"
".Lmk_m47k:\n"
"v_add_f32_e32 v255, v255, v251\n"
"v_add_f32_e32 v254, v254, v250\n"
"v_add_f32_e32 v253, v253, v249\n"
"v_add_f32_e32 v252, v252, v248\n"
"v_add_f32_e32 v255, v255, v253\n"
"v_add_f32_e32 v254, v254, v252\n"
"v_add_f32_e32 v255, v255, v254\n"
"s_nop 1\n"
"s_branch .Lmk_m47r\n"
".Lmk_p45s:\n"
";\n"
"s_waitcnt vmcnt(16)\n"
"s_mov_b32 s1, 0x10000\n"
".Lmk_p45l:\n"
"s_add_u32 s96, s54, 0x440000\n"
"buffer_load_dwordx4 v[176:179], v0, s[4:7], s96 offen sc1\n"
"buffer_load_dwordx4 v[108:111], v0, s[4:7], s96 offen offset:1024 sc1\n"
"buffer_load_dwordx4 v[148:151], v0, s[4:7], s96 offen offset:2048 sc1\n"
"buffer_load_dwordx4 v[184:187], v0, s[4:7], s96 offen offset:3072 sc1\n"
"buffer_load_dwordx4 v[152:155], v1, s[4:7], s96 offen sc1\n"
"buffer_load_dwordx4 v[192:195], v1, s[4:7], s96 offen offset:1024 sc1\n"
"buffer_load_dwordx4 v[196:199], v1, s[4:7], s96 offen offset:2048 sc1\n"
"buffer_load_dwordx4 v[200:203], v1, s[4:7], s96 offen offset:3072 sc1\n"
"s_add_u32 s96, s55, 0x440000\n"
"buffer_load_dwordx4 v[92:95], v0, s[4:7], s96 offen sc1\n"
"buffer_load_dwordx4 v[96:99], v0, s[4:7], s96 offen offset:1024 sc1\n"
"buffer_load_dwordx4 v[104:107], v0, s[4:7], s96 offen offset:2048 sc1\n"
"buffer_load_dwordx4 v[112:115], v0, s[4:7], s96 offen offset:3072 sc1\n"
"buffer_load_dwordx4 v[100:103], v1, s[4:7], s96 offen sc1\n"
"buffer_load_dwordx4 v[124:127], v1, s[4:7], s96 offen offset:1024 sc1\n"
"buffer_load_dwordx4 v[168:171], v1, s[4:7], s96 offen offset:2048 sc1\n"
"buffer_load_dwordx4 v[172:175], v1, s[4:7], s96 offen offset:3072 sc1\n"
"s_add_u32 s96, s52, 0x448000\n"
"buffer_load_dwordx4 v[128:131], v0, s[4:7], s96 offen sc1\n"
"s_waitcnt vmcnt(0)\n"
"v_max_u32_e32 v255, v176, v177\n"
"v_max3_u32 v255, v255, v178, v179\n"
"v_max3_u32 v255, v255, v108, v109\n"
"v_max3_u32 v255, v255, v110, v111\n"
"v_max3_u32 v255, v255, v148, v149\n"
"v_max3_u32 v255, v255, v150, v151\n"
"v_max3_u32 v255, v255, v184, v185\n"
"v_max3_u32 v255, v255, v186, v187\n"
"v_max3_u32 v255, v255, v152, v153\n"
"v_max3_u32 v255, v255, v154, v155\n"
"v_max3_u32 v255, v255, v192, v193\n"
"v_max3_u32 v255, v255, v194, v195\n"
"v_max3_u32 v255, v255, v196, v197\n"
"v_max3_u32 v255, v255, v198, v199\n"
"v_max3_u32 v255, v255, v200, v201\n"
"v_max3_u32 v255, v255, v202, v203\n"
"v_cmp_eq_u32_e32 vcc, -1, v255\n"
"s_cbranch_vccz .Lmk_p45k\n"
"s_sub_u32 s1, s1, 1\n"
"s_cbranch_scc0 .Lmk_p45l\n"
".Lmk_p45k:\n"
"v_mfma_f32_16x16x32_bf16 v[88:91], a[0:3], v[176:179], 0\n"
"v_mfma_f32_16x16x32_bf16 v[88:91], a[4:7], v[108:111], v[88:91]\n"
"v_mfma_f32_16x16x32_bf16 v[88:91], a[8:11], v[148:151], v[88:91]\n"
"v_mfma_f32_16x16x32_bf16 v[88:91], a[12:15], v[184:187], v[88:91]\n"
"v_mfma_f32_16x16x32_bf16 v[88:91], a[16:19], v[152:155], v[88:91]\n"
"v_mfma_f32_16x16x32_bf16 v[88:91], a[20:23], v[192:195], v[88:91]\n"
"v_mfma_f32_16x16x32_bf16 v[88:91], a[24:27], v[196:199], v[88:91]\n"
"v_mfma_f32_16x16x32_bf16 v[88:91], a[28:31], v[200:203], v[88:91]\n"
"v_mfma_f32_16x16x32_bf16 v[116:119], a[60:63], v[200:203], 0\n"
"v_mfma_f32_16x16x32_bf16 v[116:119], a[56:59], v[196:199], v[116:119]\n"
"v_mfma_f32_16x16x32_bf16 v[116:119], a[52:55], v[192:195], v[116:119]\n"
"v_mfma_f32_16x16x32_bf16 v[116:119], a[48:51], v[152:155], v[116:119]\n"
"v_mfma_f32_16x16x32_bf16 v[116:119], a[44:47], v[184:187], v[116:119]\n"
"v_mfma_f32_16x16x32_bf16 v[116:119], a[40:43], v[148:151], v[116:119]\n"
"s_nop 7\n"
"s_branch .Lmk_p45r\n"
".Lmk_p46s:\n"
";\n"
"s_waitcnt vmcnt(16)\n"
"s_mov_b32 s1, 0x10000\n"
".Lmk_p46l:\n"
"s_add_u32 s96, s55, 0x440000\n"
"buffer_load_dwordx4 v[92:95], v0, s[4:7], s96 offen sc1\n"
"buffer_load_dwordx4 v[96:99], v0, s[4:7], s96 offen offset:1024 sc1\n"
"buffer_load_dwordx4 v[104:107], v0, s[4:7], s96 offen offset:2048 sc1\n"
"buffer_load_dwordx4 v[112:115], v0, s[4:7], s96 offen offset:3072 sc1\n"
"buffer_load_dwordx4 v[100:103], v1, s[4:7], s96 offen sc1\n"
"buffer_load_dwordx4 v[124:127], v1, s[4:7], s96 offen offset:1024 sc1\n"
"buffer_load_dwordx4 v[168:171], v1, s[4:7], s96 offen offset:2048 sc1\n"
"buffer_load_dwordx4 v[172:175], v1, s[4:7], s96 offen offset:3072 sc1\n"
"s_add_u32 s96, s52, 0x448000\n"
"buffer_load_dwordx4 v[128:131], v0, s[4:7], s96 offen sc1\n"
"buffer_load_dwordx4 v[88:91], v0, s[4:7], s96 offen offset:1024 sc1\n"
"buffer_load_dwordx4 v[164:167], v0, s[4:7], s96 offen offset:2048 sc1\n"
"buffer_load_dwordx4 v[140:143], v0, s[4:7], s96 offen offset:3072 sc1\n"
"buffer_load_dwordx4 v[132:135], v1, s[4:7], s96 offen sc1\n"
"buffer_load_dwordx4 v[224:227], v1, s[4:7], s96 offen offset:1024 sc1\n"
"buffer_load_dwordx4 v[180:183], v1, s[4:7], s96 offen offset:2048 sc1\n"
"buffer_load_dwordx4 v[148:151], v1, s[4:7], s96 offen offset:3072 sc1\n"
"s_add_u32 s96, s53, 0x448000\n"
"buffer_load_dwordx4 v[116:119], v0, s[4:7], s96 offen sc1\n"
"buffer_load_dwordx4 v[160:163], v0, s[4:7], s96 offen offset:1024 sc1\n"
"s_waitcnt vmcnt(0)\n"
"v_max_u32_e32 v252, v92, v93\n"
"v_max3_u32 v252, v252, v94, v95\n"
"v_max3_u32 v252, v252, v96, v97\n"
"v_max3_u32 v252, v252, v98, v99\n"
"v_max3_u32 v252, v252, v104, v105\n"
"v_max3_u32 v252, v252, v106, v107\n"
"v_max3_u32 v252, v252, v112, v113\n"
"v_max3_u32 v252, v252, v114, v115\n"
"v_max3_u32 v252, v252, v100, v101\n"
"v_max3_u32 v252, v252, v102, v103\n"
"v_max3_u32 v252, v252, v124, v125\n"
"v_max3_u32 v252, v252, v126, v127\n"
"v_max3_u32 v252, v252, v168, v169\n"
"v_max3_u32 v252, v252, v170, v171\n"
"v_max3_u32 v252, v252, v172, v173\n"
"v_max3_u32 v252, v252, v174, v175\n"
"v_cmp_eq_u32_e32 vcc, -1, v252\n"
"s_cbranch_vccz .Lmk_p46k\n"
"s_sub_u32 s1, s1, 1\n"
"s_cbranch_scc0 .Lmk_p46l\n"
".Lmk_p46k:\n"
"v_mfma_f32_16x16x32_bf16 v[196:199], a[0:3], v[92:95], v[136:139]\n"
"v_mfma_f32_16x16x32_bf16 v[196:199], a[4:7], v[96:99], v[196:199]\n"
"v_mfma_f32_16x16x32_bf16 v[196:199], a[8:11], v[104:107], v[196:199]\n"
"v_mfma_f32_16x16x32_bf16 v[196:199], a[12:15], v[112:115], v[196:199]\n"
"v_mfma_f32_16x16x32_bf16 v[196:199], a[16:19], v[100:103], v[196:199]\n"
"v_mfma_f32_16x16x32_bf16 v[196:199], a[20:23], v[124:127], v[196:199]\n"
"v_mfma_f32_16x16x32_bf16 v[196:199], a[24:27], v[168:171], v[196:199]\n"
"v_mfma_f32_16x16x32_bf16 v[196:199], a[28:31], v[172:175], v[196:199]\n"
"v_mfma_f32_16x16x32_bf16 v[204:207], a[60:63], v[172:175], v[156:159]\n"
"v_mfma_f32_16x16x32_bf16 v[204:207], a[56:59], v[168:171], v[204:207]\n"
"v_mfma_f32_16x16x32_bf16 v[204:207], a[52:55], v[124:127], v[204:207]\n"
"v_mfma_f32_16x16x32_bf16 v[204:207], a[48:51], v[100:103], v[204:207]\n"
"v_mfma_f32_16x16x32_bf16 v[204:207], a[44:47], v[112:115], v[204:207]\n"
"v_mfma_f32_16x16x32_bf16 v[204:207], a[40:43], v[104:107], v[204:207]\n"
"s_nop 7\n"
"s_branch .Lmk_p46r\n"
".Lmk_p48s:\n"
";\n"
"s_waitcnt vmcnt(16)\n"
"s_mov_b32 s1, 0x10000\n"
".Lmk_p48l:\n"
"s_add_u32 s98, s52, 0x448000\n"
"buffer_load_dwordx4 v[128:131], v0, s[4:7], s98 offen sc1\n"
"buffer_load_dwordx4 v[88:91], v0, s[4:7], s98 offen offset:1024 sc1\n"
"buffer_load_dwordx4 v[164:167], v0, s[4:7], s98 offen offset:2048 sc1\n"
"buffer_load_dwordx4 v[140:143], v0, s[4:7], s98 offen offset:3072 sc1\n"
"buffer_load_dwordx4 v[132:135], v1, s[4:7], s98 offen sc1\n"
"buffer_load_dwordx4 v[224:227], v1, s[4:7], s98 offen offset:1024 sc1\n"
"buffer_load_dwordx4 v[180:183], v1, s[4:7], s98 offen offset:2048 sc1\n"
"buffer_load_dwordx4 v[148:151], v1, s[4:7], s98 offen offset:3072 sc1\n"
"s_add_u32 s98, s53, 0x448000\n"
"buffer_load_dwordx4 v[116:119], v0, s[4:7], s98 offen sc1\n"
"buffer_load_dwordx4 v[160:163], v0, s[4:7], s98 offen offset:1024 sc1\n"
"buffer_load_dwordx4 v[136:139], v0, s[4:7], s98 offen offset:2048 sc1\n"
"buffer_load_dwordx4 v[156:159], v0, s[4:7], s98 offen offset:3072 sc1\n"
"buffer_load_dwordx4 v[192:195], v1, s[4:7], s98 offen sc1\n"
"buffer_load_dwordx4 v[184:187], v1, s[4:7], s98 offen offset:1024 sc1\n"
"buffer_load_dwordx4 v[176:179], v1, s[4:7], s98 offen offset:2048 sc1\n"
"buffer_load_dwordx4 v[200:203], v1, s[4:7], s98 offen offset:3072 sc1\n"
"s_add_u32 s98, s54, 0x448000\n"
"buffer_load_dwordx4 v[96:99], v0, s[4:7], s98 offen sc1\n"
"buffer_load_dwordx4 v[100:103], v0, s[4:7], s98 offen offset:1024 sc1\n"
"s_waitcnt vmcnt(0)\n"
"v_max_u32_e32 v252, v128, v129\n"
"v_max3_u32 v252, v252, v130, v131\n"
"v_max3_u32 v252, v252, v88, v89\n"
"v_max3_u32 v252, v252, v90, v91\n"
"v_max3_u32 v252, v252, v164, v165\n"
"v_max3_u32 v252, v252, v166, v167\n"
"v_max3_u32 v252, v252, v140, v141\n"
"v_max3_u32 v252, v252, v142, v143\n"
"v_max3_u32 v252, v252, v132, v133\n"
"v_max3_u32 v252, v252, v134, v135\n"
"v_max3_u32 v252, v252, v224, v225\n"
"v_max3_u32 v252, v252, v226, v227\n"
"v_max3_u32 v252, v252, v180, v181\n"
"v_max3_u32 v252, v252, v182, v183\n"
"v_max3_u32 v252, v252, v148, v149\n"
"v_max3_u32 v252, v252, v150, v151\n"
"v_cmp_eq_u32_e32 vcc, -1, v252\n"
"s_cbranch_vccz .Lmk_p48k\n"
"s_sub_u32 s1, s1, 1\n"
"s_cbranch_scc0 .Lmk_p48l\n"
".Lmk_p48k:\n"
"v_mfma_f32_16x16x32_bf16 v[92:95], a[0:3], v[128:131], 0\n"
"v_mfma_f32_16x16x32_bf16 v[92:95], a[4:7], v[88:91], v[92:95]\n"
"v_mfma_f32_16x16x32_bf16 v[92:95], a[8:11], v[164:167], v[92:95]\n"
"v_mfma_f32_16x16x32_bf16 v[92:95], a[12:15], v[140:143], v[92:95]\n"
"v_mfma_f32_16x16x32_bf16 v[92:95], a[16:19], v[132:135], v[92:95]\n"
"v_mfma_f32_16x16x32_bf16 v[92:95], a[20:23], v[224:227], v[92:95]\n"
"v_mfma_f32_16x16x32_bf16 v[92:95], a[24:27], v[180:183], v[92:95]\n"
"v_mfma_f32_16x16x32_bf16 v[92:95], a[28:31], v[148:151], v[92:95]\n"
"v_mfma_f32_16x16x32_bf16 v[104:107], a[60:63], v[148:151], 0\n"
"v_mfma_f32_16x16x32_bf16 v[104:107], a[56:59], v[180:183], v[104:107]\n"
"v_mfma_f32_16x16x32_bf16 v[104:107], a[52:55], v[224:227], v[104:107]\n"
"v_mfma_f32_16x16x32_bf16 v[104:107], a[48:51], v[132:135], v[104:107]\n"
"v_mfma_f32_16x16x32_bf16 v[104:107], a[44:47], v[140:143], v[104:107]\n"
"v_mfma_f32_16x16x32_bf16 v[104:107], a[40:43], v[164:167], v[104:107]\n"
"s_nop 7\n"
"s_branch .Lmk_p48r\n"
".Lmk_p49s:\n"
";\n"
"s_waitcnt vmcnt(16)\n"
"s_mov_b32 s1, 0x10000\n"
".Lmk_p49l:\n"
"s_add_u32 s95, s53, 0x448000\n"
"buffer_load_dwordx4 v[116:119], v0, s[4:7], s95 offen sc1\n"
"buffer_load_dwordx4 v[160:163], v0, s[4:7], s95 offen offset:1024 sc1\n"
"buffer_load_dwordx4 v[136:139], v0, s[4:7], s95 offen offset:2048 sc1\n"
"buffer_load_dwordx4 v[156:159], v0, s[4:7], s95 offen offset:3072 sc1\n"
"buffer_load_dwordx4 v[192:195], v1, s[4:7], s95 offen sc1\n"
"buffer_load_dwordx4 v[184:187], v1, s[4:7], s95 offen offset:1024 sc1\n"
"buffer_load_dwordx4 v[176:179], v1, s[4:7], s95 offen offset:2048 sc1\n"
"buffer_load_dwordx4 v[200:203], v1, s[4:7], s95 offen offset:3072 sc1\n"
"s_add_u32 s95, s54, 0x448000\n"
"buffer_load_dwordx4 v[96:99], v0, s[4:7], s95 offen sc1\n"
"buffer_load_dwordx4 v[100:103], v0, s[4:7], s95 offen offset:1024 sc1\n"
"buffer_load_dwordx4 v[112:115], v0, s[4:7], s95 offen offset:2048 sc1\n"
"buffer_load_dwordx4 v[120:123], v0, s[4:7], s95 offen offset:3072 sc1\n"
"buffer_load_dwordx4 v[104:107], v1, s[4:7], s95 offen sc1\n"
"buffer_load_dwordx4 v[152:155], v1, s[4:7], s95 offen offset:1024 sc1\n"
"buffer_load_dwordx4 v[108:111], v1, s[4:7], s95 offen offset:2048 sc1\n"
"buffer_load_dwordx4 v[144:147], v1, s[4:7], s95 offen offset:3072 sc1\n"
"s_add_u32 s95, s55, 0x448000\n"
"buffer_load_dwordx4 v[92:95], v0, s[4:7], s95 offen sc1\n"
"buffer_load_dwordx4 v[124:127], v0, s[4:7], s95 offen offset:1024 sc1\n"
"s_waitcnt vmcnt(0)\n"
"v_max_u32_e32 v247, v116, v117\n"
"v_max3_u32 v247, v247, v118, v119\n"
"v_max3_u32 v247, v247, v160, v161\n"
"v_max3_u32 v247, v247, v162, v163\n"
"v_max3_u32 v247, v247, v136, v137\n"
"v_max3_u32 v247, v247, v138, v139\n"
"v_max3_u32 v247, v247, v156, v157\n"
"v_max3_u32 v247, v247, v158, v159\n"
"v_max3_u32 v247, v247, v192, v193\n"
"v_max3_u32 v247, v247, v194, v195\n"
"v_max3_u32 v247, v247, v184, v185\n"
"v_max3_u32 v247, v247, v186, v187\n"
"v_max3_u32 v247, v247, v176, v177\n"
"v_max3_u32 v247, v247, v178, v179\n"
"v_max3_u32 v247, v247, v200, v201\n"
"v_max3_u32 v247, v247, v202, v203\n"
"v_cmp_eq_u32_e32 vcc, -1, v247\n"
"s_cbranch_vccz .Lmk_p49k\n"
"s_sub_u32 s1, s1, 1\n"
"s_cbranch_scc0 .Lmk_p49l\n"
".Lmk_p49k:\n"
"v_mfma_f32_16x16x32_bf16 v[88:91], a[0:3], v[116:119], 0\n"
"v_mfma_f32_16x16x32_bf16 v[88:91], a[4:7], v[160:163], v[88:91]\n"
"v_mfma_f32_16x16x32_bf16 v[88:91], a[8:11], v[136:139], v[88:91]\n"
"v_mfma_f32_16x16x32_bf16 v[88:91], a[12:15], v[156:159], v[88:91]\n"
"v_mfma_f32_16x16x32_bf16 v[88:91], a[16:19], v[192:195], v[88:91]\n"
"v_mfma_f32_16x16x32_bf16 v[88:91], a[20:23], v[184:187], v[88:91]\n"
"v_mfma_f32_16x16x32_bf16 v[88:91], a[24:27], v[176:179], v[88:91]\n"
"v_mfma_f32_16x16x32_bf16 v[88:91], a[28:31], v[200:203], v[88:91]\n"
"v_mfma_f32_16x16x32_bf16 v[128:131], a[60:63], v[200:203], 0\n"
"v_mfma_f32_16x16x32_bf16 v[128:131], a[56:59], v[176:179], v[128:131]\n"
"v_mfma_f32_16x16x32_bf16 v[128:131], a[52:55], v[184:187], v[128:131]\n"
"v_mfma_f32_16x16x32_bf16 v[128:131], a[48:51], v[192:195], v[128:131]\n"
"v_mfma_f32_16x16x32_bf16 v[128:131], a[44:47], v[156:159], v[128:131]\n"
"v_mfma_f32_16x16x32_bf16 v[128:131], a[40:43], v[136:139], v[128:131]\n"
"s_nop 7\n"
"s_branch .Lmk_p49r\n"
".Lmk_m52s:\n"
";\n"
"s_waitcnt vmcnt(16)\n"
"s_mov_b32 s1, 0x10000\n"
".Lmk_m52l:\n"
"s_add_u32 s97, s62, 0x44800\n"
"buffer_load_dword v255, v10, s[4:7], s97 offen sc1\n"
"buffer_load_dword v253, v10, s[4:7], s97 offen offset:256 sc1\n"
"buffer_load_dword v252, v10, s[4:7], s97 offen offset:512 sc1\n"
"buffer_load_dword v251, v10, s[4:7], s97 offen offset:768 sc1\n"
"buffer_load_dword v250, v10, s[4:7], s97 offen offset:1024 sc1\n"
"buffer_load_dword v254, v10, s[4:7], s97 offen offset:1280 sc1\n"
"buffer_load_dword v249, v10, s[4:7], s97 offen offset:1536 sc1\n"
"buffer_load_dword v248, v10, s[4:7], s97 offen offset:1792 sc1\n"
"s_waitcnt vmcnt(0)\n"
"v_max_u32_e32 v245, v255, v253\n"
"v_max3_u32 v245, v245, v252, v251\n"
"v_max3_u32 v245, v245, v250, v254\n"
"v_max3_u32 v245, v245, v249, v248\n"
"v_cmp_eq_u32_e32 vcc, -1, v245\n"
"s_cbranch_vccz .Lmk_m52k\n"
"s_sub_u32 s1, s1, 1\n"
"s_cbranch_scc0 .Lmk_m52l\n"
".Lmk_m52k:\n"
"v_add_f32_e32 v255, v255, v250\n"
"v_add_f32_e32 v253, v253, v254\n"
"v_add_f32_e32 v252, v252, v249\n"
"v_add_f32_e32 v251, v251, v248\n"
"v_add_f32_e32 v255, v255, v252\n"
"v_add_f32_e32 v253, v253, v251\n"
"v_add_f32_e32 v255, v255, v253\n"
"s_nop 1\n"
"s_branch .Lmk_m52r\n"
".Lmk_f39s:\n"
";\n"
"s_mov_b32 s1, 0x10000\n"
".Lmk_f39l:\n"
"buffer_load_dwordx2 v[246:247], v12, s[4:7], s96 offen sc1\n"
"s_waitcnt vmcnt(0)\n"
"v_cmp_ne_u64_e32 vcc, v[246:247], v[18:19]\n"
"s_cbranch_vccz .Lmk_f39r\n"
"s_sub_u32 s1, s1, 1\n"
"s_cbranch_scc0 .Lmk_f39l\n"
"s_branch .Lmk_f39r\n"
".Lmk_p50s:\n"
";\n"
"s_waitcnt vmcnt(16)\n"
"s_mov_b32 s1, 0x10000\n"
".Lmk_p50l:\n"
"s_add_u32 s96, s54, 0x448000\n"
"buffer_load_dwordx4 v[96:99], v0, s[4:7], s96 offen sc1\n"
"buffer_load_dwordx4 v[100:103], v0, s[4:7], s96 offen offset:1024 sc1\n"
"buffer_load_dwordx4 v[112:115], v0, s[4:7], s96 offen offset:2048 sc1\n"
"buffer_load_dwordx4 v[120:123], v0, s[4:7], s96 offen offset:3072 sc1\n"
"buffer_load_dwordx4 v[104:107], v1, s[4:7], s96 offen sc1\n"
"buffer_load_dwordx4 v[152:155], v1, s[4:7], s96 offen offset:1024 sc1\n"
"buffer_load_dwordx4 v[108:111], v1, s[4:7], s96 offen offset:2048 sc1\n"
"buffer_load_dwordx4 v[144:147], v1, s[4:7], s96 offen offset:3072 sc1\n"
"s_add_u32 s96, s55, 0x448000\n"
"buffer_load_dwordx4 v[92:95], v0, s[4:7], s96 offen sc1\n"
"buffer_load_dwordx4 v[124:127], v0, s[4:7], s96 offen offset:1024 sc1\n"
"buffer_load_dwordx4 v[132:135], v0, s[4:7], s96 offen offset:2048 sc1\n"
"buffer_load_dwordx4 v[140:143], v0, s[4:7], s96 offen offset:3072 sc1\n"
"buffer_load_dwordx4 v[128:131], v1, s[4:7], s96 offen sc1\n"
"buffer_load_dwordx4 v[168:171], v1, s[4:7], s96 offen offset:1024 sc1\n"
"buffer_load_dwordx4 v[172:175], v1, s[4:7], s96 offen offset:2048 sc1\n"
"buffer_load_dwordx4 v[180:183], v1, s[4:7], s96 offen offset:3072 sc1\n"
"s_waitcnt vmcnt(0)\n"
"v_max_u32_e32 v252, v96, v97\n"
"v_max3_u32 v252, v252, v98, v99\n"
"v_max3_u32 v252, v252, v100, v101\n"
"v_max3_u32 v252, v252, v102, v103\n"
"v_max3_u32 v252, v252, v112, v113\n"
"v_max3_u32 v252, v252, v114, v115\n"
"v_max3_u32 v252, v252, v120, v121\n"
"v_max3_u32 v252, v252, v122, v123\n"
"v_max3_u32 v252, v252, v104, v105\n"
"v_max3_u32 v252, v252, v106, v107\n"
"v_max3_u32 v252, v252, v152, v153\n"
"v_max3_u32 v252, v252, v154, v155\n"
"v_max3_u32 v252, v252, v108, v109\n"
"v_max3_u32 v252, v252, v110, v111\n"
"v_max3_u32 v252, v252, v144, v145\n"
"v_max3_u32 v252, v252, v146, v147\n"
"v_cmp_eq_u32_e32 vcc, -1, v252\n"
"s_cbranch_vccz .Lmk_p50k\n"
"s_sub_u32 s1, s1, 1\n"
"s_cbranch_scc0 .Lmk_p50l\n"
".Lmk_p50k:\n"
"v_mfma_f32_16x16x32_bf16 v[88:91], a[0:3], v[96:99], 0\n"
"v_mfma_f32_16x16x32_bf16 v[88:91], a[4:7], v[100:103], v[88:91]\n"
"v_mfma_f32_16x16x32_bf16 v[88:91], a[8:11], v[112:115], v[88:91]\n"
"v_mfma_f32_16x16x32_bf16 v[88:91], a[12:15], v[120:123], v[88:91]\n"
"v_mfma_f32_16x16x32_bf16 v[88:91], a[16:19], v[104:107], v[88:91]\n"
"v_mfma_f32_16x16x32_bf16 v[88:91], a[20:23], v[152:155], v[88:91]\n"
"v_mfma_f32_16x16x32_bf16 v[88:91], a[24:27], v[108:111], v[88:91]\n"
"v_mfma_f32_16x16x32_bf16 v[88:91], a[28:31], v[144:147], v[88:91]\n"
"v_mfma_f32_16x16x32_bf16 v[116:119], a[60:63], v[144:147], 0\n"
"v_mfma_f32_16x16x32_bf16 v[116:119], a[56:59], v[108:111], v[116:119]\n"
"v_mfma_f32_16x16x32_bf16 v[116:119], a[52:55], v[152:155], v[116:119]\n"
"v_mfma_f32_16x16x32_bf16 v[116:119], a[48:51], v[104:107], v[116:119]\n"
"v_mfma_f32_16x16x32_bf16 v[116:119], a[44:47], v[120:123], v[116:119]\n"
"v_mfma_f32_16x16x32_bf16 v[116:119], a[40:43], v[112:115], v[116:119]\n"
"s_nop 7\n"
"s_branch .Lmk_p50r\n"
".Lmk_p51s:\n"
";\n"
"s_waitcnt vmcnt(16)\n"
"s_mov_b32 s1, 0x10000\n"
".Lmk_p51l:\n"
"s_add_u32 s96, s55, 0x448000\n"
"buffer_load_dwordx4 v[92:95], v0, s[4:7], s96 offen sc1\n"
"buffer_load_dwordx4 v[124:127], v0, s[4:7], s96 offen offset:1024 sc1\n"
"buffer_load_dwordx4 v[132:135], v0, s[4:7], s96 offen offset:2048 sc1\n"
"buffer_load_dwordx4 v[140:143], v0, s[4:7], s96 offen offset:3072 sc1\n"
"buffer_load_dwordx4 v[128:131], v1, s[4:7], s96 offen sc1\n"
"buffer_load_dwordx4 v[168:171], v1, s[4:7], s96 offen offset:1024 sc1\n"
"buffer_load_dwordx4 v[172:175], v1, s[4:7], s96 offen offset:2048 sc1\n"
"buffer_load_dwordx4 v[180:183], v1, s[4:7], s96 offen offset:3072 sc1\n"
"s_waitcnt vmcnt(0)\n"
"v_max_u32_e32 v252, v92, v93\n"
"v_max3_u32 v252, v252, v94, v95\n"
"v_max3_u32 v252, v252, v124, v125\n"
"v_max3_u32 v252, v252, v126, v127\n"
"v_max3_u32 v252, v252, v132, v133\n"
"v_max3_u32 v252, v252, v134, v135\n"
"v_max3_u32 v252, v252, v140, v141\n"
"v_max3_u32 v252, v252, v142, v143\n"
"v_max3_u32 v252, v252, v128, v129\n"
"v_max3_u32 v252, v252, v130, v131\n"
"v_max3_u32 v252, v252, v168, v169\n"
"v_max3_u32 v252, v252, v170, v171\n"
"v_max3_u32 v252, v252, v172, v173\n"
"v_max3_u32 v252, v252, v174, v175\n"
"v_max3_u32 v252, v252, v180, v181\n"
"v_max3_u32 v252, v252, v182, v183\n"
"v_cmp_eq_u32_e32 vcc, -1, v252\n"
"s_cbranch_vccz .Lmk_p51k\n"
"s_sub_u32 s1, s1, 1\n"
"s_cbranch_scc0 .Lmk_p51l\n"
".Lmk_p51k:\n"
"v_mfma_f32_16x16x32_bf16 v[112:115], a[0:3], v[92:95], v[156:159]\n"
"v_mfma_f32_16x16x32_bf16 v[112:115], a[4:7], v[124:127], v[112:115]\n"
"v_mfma_f32_16x16x32_bf16 v[112:115], a[8:11], v[132:135], v[112:115]\n"
"v_mfma_f32_16x16x32_bf16 v[112:115], a[12:15], v[140:143], v[112:115]\n"
"v_mfma_f32_16x16x32_bf16 v[112:115], a[16:19], v[128:131], v[112:115]\n"
"v_mfma_f32_16x16x32_bf16 v[112:115], a[20:23], v[168:171], v[112:115]\n"
"v_mfma_f32_16x16x32_bf16 v[112:115], a[24:27], v[172:175], v[112:115]\n"
"v_mfma_f32_16x16x32_bf16 v[112:115], a[28:31], v[180:183], v[112:115]\n"
"v_mfma_f32_16x16x32_bf16 v[188:191], a[60:63], v[180:183], v[184:187]\n"
"v_mfma_f32_16x16x32_bf16 v[188:191], a[56:59], v[172:175], v[188:191]\n"
"v_mfma_f32_16x16x32_bf16 v[188:191], a[52:55], v[168:171], v[188:191]\n"
"v_mfma_f32_16x16x32_bf16 v[188:191], a[48:51], v[128:131], v[188:191]\n"
"v_mfma_f32_16x16x32_bf16 v[188:191], a[44:47], v[140:143], v[188:191]\n"
"v_mfma_f32_16x16x32_bf16 v[188:191], a[40:43], v[132:135], v[188:191]\n"
"s_nop 7\n"
"s_branch .Lmk_p51r\n"
".Lmk_f40s:\n"
";\n"
"s_mov_b32 s1, 0x10000\n"
".Lmk_f40l:\n"
"buffer_load_dwordx2 v[246:247], off, s[4:7], s96 sc1\n"
"s_waitcnt vmcnt(0)\n"
"v_cmp_ne_u64_e32 vcc, v[246:247], v[18:19]\n"
"s_cbranch_vccz .Lmk_f40r\n"
"s_sub_u32 s1, s1, 1\n"
"s_cbranch_scc0 .Lmk_f40l\n"
"s_branch .Lmk_f40r\n"
".Lmk_f41s:\n"
";\n"
"s_mov_b32 s1, 0x10000\n"
".Lmk_f41l:\n"
"buffer_load_dwordx2 v[250:251], v12, s[4:7], s98 offen sc1\n"
"s_waitcnt vmcnt(0)\n"
"v_cmp_ne_u64_e32 vcc, v[250:251], v[18:19]\n"
"s_cbranch_vccz .Lmk_f41r\n"
"s_sub_u32 s1, s1, 1\n"
"s_cbranch_scc0 .Lmk_f41l\n"
"s_branch .Lmk_f41r\n"
".Lmk_p55s:\n"
";\n"
"s_waitcnt vmcnt(16)\n"
"s_mov_b32 s1, 0x10000\n"
".Lmk_p55l:\n"
"s_add_u32 s96, s56, 0x440000\n"
"buffer_load_dwordx4 v[120:123], v0, s[4:7], s96 offen sc1\n"
"buffer_load_dwordx4 v[136:139], v0, s[4:7], s96 offen offset:1024 sc1\n"
"buffer_load_dwordx4 v[144:147], v0, s[4:7], s96 offen offset:2048 sc1\n"
"buffer_load_dwordx4 v[192:195], v0, s[4:7], s96 offen offset:3072 sc1\n"
"buffer_load_dwordx4 v[196:199], v1, s[4:7], s96 offen sc1\n"
"buffer_load_dwordx4 v[188:191], v1, s[4:7], s96 offen offset:1024 sc1\n"
"buffer_load_dwordx4 v[108:111], v1, s[4:7], s96 offen offset:2048 sc1\n"
"buffer_load_dwordx4 v[92:95], v1, s[4:7], s96 offen offset:3072 sc1\n"
"s_add_u32 s96, s57, 0x440000\n"
"buffer_load_dwordx4 v[148:151], v0, s[4:7], s96 offen sc1\n"
"buffer_load_dwordx4 v[180:183], v0, s[4:7], s96 offen offset:1024 sc1\n"
"buffer_load_dwordx4 v[24:27], v0, s[4:7], s96 offen offset:2048 sc1\n"
"buffer_load_dwordx4 v[32:35], v0, s[4:7], s96 offen offset:3072 sc1\n"
"buffer_load_dwordx4 v[44:47], v1, s[4:7], s96 offen sc1\n"
"buffer_load_dwordx4 v[48:51], v1, s[4:7], s96 offen offset:1024 sc1\n"
"buffer_load_dwordx4 v[28:31], v1, s[4:7], s96 offen offset:2048 sc1\n"
"buffer_load_dwordx4 v[56:59], v1, s[4:7], s96 offen offset:3072 sc1\n"
"s_add_u32 s96, s58, 0x440000\n"
"buffer_load_dwordx4 v[40:43], v0, s[4:7], s96 offen sc1\n"
"buffer_load_dwordx4 v[52:55], v0, s[4:7], s96 offen offset:1024 sc1\n"
"buffer_load_dwordx4 v[64:67], v0, s[4:7], s96 offen offset:2048 sc1\n"
"s_waitcnt vmcnt(0)\n"
"v_max_u32_e32 v255, v120, v121\n"
"v_max3_u32 v255, v255, v122, v123\n"
"v_max3_u32 v255, v255, v136, v137\n"
"v_max3_u32 v255, v255, v138, v139\n"
"v_max3_u32 v255, v255, v144, v145\n"
"v_max3_u32 v255, v255, v146, v147\n"
"v_max3_u32 v255, v255, v192, v193\n"
"v_max3_u32 v255, v255, v194, v195\n"
"v_max3_u32 v255, v255, v196, v197\n"
"v_max3_u32 v255, v255, v198, v199\n"
"v_max3_u32 v255, v255, v188, v189\n"
"v_max3_u32 v255, v255, v190, v191\n"
"v_max3_u32 v255, v255, v108, v109\n"
"v_max3_u32 v255, v255, v110, v111\n"
"v_max3_u32 v255, v255, v92, v93\n"
"v_max3_u32 v255, v255, v94, v95\n"
"v_cmp_eq_u32_e32 vcc, -1, v255\n"
"s_cbranch_vccz .Lmk_p55k\n"
"s_sub_u32 s1, s1, 1\n"
"s_cbranch_scc0 .Lmk_p55l\n"
".Lmk_p55k:\n"
"v_mfma_f32_16x16x32_bf16 v[36:39], a[128:131], v[120:123], 0\n"
"v_mfma_f32_16x16x32_bf16 v[36:39], a[132:135], v[136:139], v[36:39]\n"
"v_mfma_f32_16x16x32_bf16 v[36:39], a[136:139], v[144:147], v[36:39]\n"
"v_mfma_f32_16x16x32_bf16 v[36:39], a[140:143], v[192:195], v[36:39]\n"
"v_mfma_f32_16x16x32_bf16 v[36:39], a[144:147], v[196:199], v[36:39]\n"
"v_mfma_f32_16x16x32_bf16 v[36:39], a[148:151], v[188:191], v[36:39]\n"
"v_mfma_f32_16x16x32_bf16 v[36:39], a[152:155], v[108:111], v[36:39]\n"
"v_mfma_f32_16x16x32_bf16 v[36:39], a[156:159], v[92:95], v[36:39]\n"
"v_mfma_f32_16x16x32_bf16 v[60:63], a[188:191], v[92:95], 0\n"
"v_mfma_f32_16x16x32_bf16 v[60:63], a[184:187], v[108:111], v[60:63]\n"
"v_mfma_f32_16x16x32_bf16 v[60:63], a[180:183], v[188:191], v[60:63]\n"
"v_mfma_f32_16x16x32_bf16 v[60:63], a[176:179], v[196:199], v[60:63]\n"
"v_mfma_f32_16x16x32_bf16 v[60:63], a[172:175], v[192:195], v[60:63]\n"
"v_mfma_f32_16x16x32_bf16 v[60:63], a[168:171], v[144:147], v[60:63]\n"
"s_nop 7\n"
"s_branch .Lmk_p55r\n"
".Lmk_p56s:\n"
";\n"
"s_waitcnt vmcnt(16)\n"
"s_mov_b32 s1, 0x10000\n"
".Lmk_p56l:\n"
"s_add_u32 s96, s57, 0x440000\n"
"buffer_load_dwordx4 v[148:151], v0, s[4:7], s96 offen sc1\n"
"buffer_load_dwordx4 v[180:183], v0, s[4:7], s96 offen offset:1024 sc1\n"
"buffer_load_dwordx4 v[24:27], v0, s[4:7], s96 offen offset:2048 sc1\n"
"buffer_load_dwordx4 v[32:35], v0, s[4:7], s96 offen offset:3072 sc1\n"
"buffer_load_dwordx4 v[44:47], v1, s[4:7], s96 offen sc1\n"
"buffer_load_dwordx4 v[48:51], v1, s[4:7], s96 offen offset:1024 sc1\n"
"buffer_load_dwordx4 v[28:31], v1, s[4:7], s96 offen offset:2048 sc1\n"
"buffer_load_dwordx4 v[56:59], v1, s[4:7], s96 offen offset:3072 sc1\n"
"s_add_u32 s96, s58, 0x440000\n"
"buffer_load_dwordx4 v[40:43], v0, s[4:7], s96 offen sc1\n"
"buffer_load_dwordx4 v[52:55], v0, s[4:7], s96 offen offset:1024 sc1\n"
"buffer_load_dwordx4 v[64:67], v0, s[4:7], s96 offen offset:2048 sc1\n"
"buffer_load_dwordx4 v[36:39], v0, s[4:7], s96 offen offset:3072 sc1\n"
"buffer_load_dwordx4 v[72:75], v1, s[4:7], s96 offen sc1\n"
"buffer_load_dwordx4 v[60:63], v1, s[4:7], s96 offen offset:1024 sc1\n"
"buffer_load_dwordx4 v[80:83], v1, s[4:7], s96 offen offset:2048 sc1\n"
"buffer_load_dwordx4 v[68:71], v1, s[4:7], s96 offen offset:3072 sc1\n"
"s_add_u32 s96, s59, 0x440000\n"
"buffer_load_dwordx4 v[96:99], v0, s[4:7], s96 offen sc1\n"
"buffer_load_dwordx4 v[76:79], v0, s[4:7], s96 offen offset:1024 sc1\n"
"buffer_load_dwordx4 v[112:115], v0, s[4:7], s96 offen offset:2048 sc1\n"
"s_waitcnt vmcnt(0)\n"
"v_max_u32_e32 v254, v148, v149\n"
"v_max3_u32 v254, v254, v150, v151\n"
"v_max3_u32 v254, v254, v180, v181\n"
"v_max3_u32 v254, v254, v182, v183\n"
"v_max3_u32 v254, v254, v24, v25\n"
"v_max3_u32 v254, v254, v26, v27\n"
"v_max3_u32 v254, v254, v32, v33\n"
"v_max3_u32 v254, v254, v34, v35\n"
"v_max3_u32 v254, v254, v44, v45\n"
"v_max3_u32 v254, v254, v46, v47\n"
"v_max3_u32 v254, v254, v48, v49\n"
"v_max3_u32 v254, v254, v50, v51\n"
"v_max3_u32 v254, v254, v28, v29\n"
"v_max3_u32 v254, v254, v30, v31\n"
"v_max3_u32 v254, v254, v56, v57\n"
"v_max3_u32 v254, v254, v58, v59\n"
"v_cmp_eq_u32_e32 vcc, -1, v254\n"
"s_cbranch_vccz .Lmk_p56k\n"
"s_sub_u32 s1, s1, 1\n"
"s_cbranch_scc0 .Lmk_p56l\n"
".Lmk_p56k:\n"
"v_mfma_f32_16x16x32_bf16 v[84:87], a[128:131], v[148:151], 0\n"
"v_mfma_f32_16x16x32_bf16 v[84:87], a[132:135], v[180:183], v[84:87]\n"
"v_mfma_f32_16x16x32_bf16 v[84:87], a[136:139], v[24:27], v[84:87]\n"
"v_mfma_f32_16x16x32_bf16 v[84:87], a[140:143], v[32:35], v[84:87]\n"
"v_mfma_f32_16x16x32_bf16 v[84:87], a[144:147], v[44:47], v[84:87]\n"
"v_mfma_f32_16x16x32_bf16 v[84:87], a[148:151], v[48:51], v[84:87]\n"
"v_mfma_f32_16x16x32_bf16 v[84:87], a[152:155], v[28:31], v[84:87]\n"
"v_mfma_f32_16x16x32_bf16 v[84:87], a[156:159], v[56:59], v[84:87]\n"
"v_mfma_f32_16x16x32_bf16 v[100:103], a[188:191], v[56:59], 0\n"
"v_mfma_f32_16x16x32_bf16 v[100:103], a[184:187], v[28:31], v[100:103]\n"
"v_mfma_f32_16x16x32_bf16 v[100:103], a[180:183], v[48:51], v[100:103]\n"
"v_mfma_f32_16x16x32_bf16 v[100:103], a[176:179], v[44:47], v[100:103]\n"
"v_mfma_f32_16x16x32_bf16 v[100:103], a[172:175], v[32:35], v[100:103]\n"
"v_mfma_f32_16x16x32_bf16 v[100:103], a[168:171], v[24:27], v[100:103]\n"
"s_nop 7\n"
"s_branch .Lmk_p56r\n"
".Lmk_p57s:\n"
";\n"
"s_waitcnt vmcnt(16)\n"
"s_mov_b32 s1, 0x10000\n"
".Lmk_p57l:\n"
"s_add_u32 s96, s58, 0x440000\n"
"buffer_load_dwordx4 v[40:43], v0, s[4:7], s96 offen sc1\n"
"buffer_load_dwordx4 v[52:55], v0, s[4:7], s96 offen offset:1024 sc1\n"
"buffer_load_dwordx4 v[64:67], v0, s[4:7], s96 offen offset:2048 sc1\n"
"buffer_load_dwordx4 v[36:39], v0, s[4:7], s96 offen offset:3072 sc1\n"
"buffer_load_dwordx4 v[72:75], v1, s[4:7], s96 offen sc1\n"
"buffer_load_dwordx4 v[60:63], v1, s[4:7], s96 offen offset:1024 sc1\n"
"buffer_load_dwordx4 v[80:83], v1, s[4:7], s96 offen offset:2048 sc1\n"
"buffer_load_dwordx4 v[68:71], v1, s[4:7], s96 offen offset:3072 sc1\n"
"s_add_u32 s96, s59, 0x440000\n"
"buffer_load_dwordx4 v[96:99], v0, s[4:7], s96 offen sc1\n"
"buffer_load_dwordx4 v[76:79], v0, s[4:7], s96 offen offset:1024 sc1\n"
"buffer_load_dwordx4 v[112:115], v0, s[4:7], s96 offen offset:2048 sc1\n"
"buffer_load_dwordx4 v[84:87], v0, s[4:7], s96 offen offset:3072 sc1\n"
"buffer_load_dwordx4 v[128:131], v1, s[4:7], s96 offen sc1\n"
"buffer_load_dwordx4 v[100:103], v1, s[4:7], s96 offen offset:1024 sc1\n"
"buffer_load_dwordx4 v[156:159], v1, s[4:7], s96 offen offset:2048 sc1\n"
"buffer_load_dwordx4 v[116:119], v1, s[4:7], s96 offen offset:3072 sc1\n"
"s_waitcnt vmcnt(0)\n"
"v_max_u32_e32 v237, v40, v41\n"
"v_max3_u32 v237, v237, v42, v43\n"
"v_max3_u32 v237, v237, v52, v53\n"
"v_max3_u32 v237, v237, v54, v55\n"
"v_max3_u32 v237, v237, v64, v65\n"
"v_max3_u32 v237, v237, v66, v67\n"
"v_max3_u32 v237, v237, v36, v37\n"
"v_max3_u32 v237, v237, v38, v39\n"
"v_max3_u32 v237, v237, v72, v73\n"
"v_max3_u32 v237, v237, v74, v75\n"
"v_max3_u32 v237, v237, v60, v61\n"
"v_max3_u32 v237, v237, v62, v63\n"
"v_max3_u32 v237, v237, v80, v81\n"
"v_max3_u32 v237, v237, v82, v83\n"
"v_max3_u32 v237, v237, v68, v69\n"
"v_max3_u32 v237, v237, v70, v71\n"
"v_cmp_eq_u32_e32 vcc, -1, v237\n"
"s_cbranch_vccz .Lmk_p57k\n"
"s_sub_u32 s1, s1, 1\n"
"s_cbranch_scc0 .Lmk_p57l\n"
".Lmk_p57k:\n"
"v_mfma_f32_16x16x32_bf16 v[132:135], a[128:131], v[40:43], 0\n"
"v_mfma_f32_16x16x32_bf16 v[132:135], a[132:135], v[52:55], v[132:135]\n"
"v_mfma_f32_16x16x32_bf16 v[132:135], a[136:139], v[64:67], v[132:135]\n"
"v_mfma_f32_16x16x32_bf16 v[132:135], a[140:143], v[36:39], v[132:135]\n"
"v_mfma_f32_16x16x32_bf16 v[132:135], a[144:147], v[72:75], v[132:135]\n"
"v_mfma_f32_16x16x32_bf16 v[132:135], a[148:151], v[60:63], v[132:135]\n"
"v_mfma_f32_16x16x32_bf16 v[132:135], a[152:155], v[80:83], v[132:135]\n"
"v_mfma_f32_16x16x32_bf16 v[132:135], a[156:159], v[68:71], v[132:135]\n"
"v_mfma_f32_16x16x32_bf16 v[140:143], a[188:191], v[68:71], 0\n"
"v_mfma_f32_16x16x32_bf16 v[140:143], a[184:187], v[80:83], v[140:143]\n"
"v_mfma_f32_16x16x32_bf16 v[140:143], a[180:183], v[60:63], v[140:143]\n"
"v_mfma_f32_16x16x32_bf16 v[140:143], a[176:179], v[72:75], v[140:143]\n"
"v_mfma_f32_16x16x32_bf16 v[140:143], a[172:175], v[36:39], v[140:143]\n"
"v_mfma_f32_16x16x32_bf16 v[140:143], a[168:171], v[64:67], v[140:143]\n"
"s_nop 7\n"
"s_branch .Lmk_p57r\n"
".Lmk_p58s:\n"
";\n"
"s_waitcnt vmcnt(16)\n"
"s_mov_b32 s1, 0x10000\n"
".Lmk_p58l:\n"
"s_add_u32 s96, s59, 0x440000\n"
"buffer_load_dwordx4 v[96:99], v0, s[4:7], s96 offen sc1\n"
"buffer_load_dwordx4 v[76:79], v0, s[4:7], s96 offen offset:1024 sc1\n"
"buffer_load_dwordx4 v[112:115], v0, s[4:7], s96 offen offset:2048 sc1\n"
"buffer_load_dwordx4 v[84:87], v0, s[4:7], s96 offen offset:3072 sc1\n"
"buffer_load_dwordx4 v[128:131], v1, s[4:7], s96 offen sc1\n"
"buffer_load_dwordx4 v[100:103], v1, s[4:7], s96 offen offset:1024 sc1\n"
"buffer_load_dwordx4 v[156:159], v1, s[4:7], s96 offen offset:2048 sc1\n"
"buffer_load_dwordx4 v[116:119], v1, s[4:7], s96 offen offset:3072 sc1\n"
"s_add_u32 s96, s56, 0x400000\n"
"buffer_load_dwordx4 v[132:135], v0, s[4:7], s96 offen sc1\n"
"buffer_load_dwordx4 v[184:187], v0, s[4:7], s96 offen offset:1024 sc1\n"
"buffer_load_dwordx4 v[140:143], v0, s[4:7], s96 offen offset:2048 sc1\n"
"buffer_load_dwordx4 v[68:71], v0, s[4:7], s96 offen offset:3072 sc1\n"
"buffer_load_dwordx4 v[36:39], v1, s[4:7], s96 offen sc1\n"
"buffer_load_dwordx4 v[40:43], v1, s[4:7], s96 offen offset:1024 sc1\n"
"buffer_load_dwordx4 v[72:75], v1, s[4:7], s96 offen offset:2048 sc1\n"
"buffer_load_dwordx4 v[80:83], v1, s[4:7], s96 offen offset:3072 sc1\n"
"s_add_u32 s96, s57, 0x400000\n"
"buffer_load_dwordx4 v[164:167], v0, s[4:7], s96 offen sc1\n"
"s_waitcnt vmcnt(0)\n"
"v_max_u32_e32 v254, v96, v97\n"
"v_max3_u32 v254, v254, v98, v99\n"
"v_max3_u32 v254, v254, v76, v77\n"
"v_max3_u32 v254, v254, v78, v79\n"
"v_max3_u32 v254, v254, v112, v113\n"
"v_max3_u32 v254, v254, v114, v115\n"
"v_max3_u32 v254, v254, v84, v85\n"
"v_max3_u32 v254, v254, v86, v87\n"
"v_max3_u32 v254, v254, v128, v129\n"
"v_max3_u32 v254, v254, v130, v131\n"
"v_max3_u32 v254, v254, v100, v101\n"
"v_max3_u32 v254, v254, v102, v103\n"
"v_max3_u32 v254, v254, v156, v157\n"
"v_max3_u32 v254, v254, v158, v159\n"
"v_max3_u32 v254, v254, v116, v117\n"
"v_max3_u32 v254, v254, v118, v119\n"
"v_cmp_eq_u32_e32 vcc, -1, v254\n"
"s_cbranch_vccz .Lmk_p58k\n"
"s_sub_u32 s1, s1, 1\n"
"s_cbranch_scc0 .Lmk_p58l\n"
".Lmk_p58k:\n"
"v_mfma_f32_16x16x32_bf16 v[64:67], a[128:131], v[96:99], v[152:155]\n"
"v_mfma_f32_16x16x32_bf16 v[64:67], a[132:135], v[76:79], v[64:67]\n"
"v_mfma_f32_16x16x32_bf16 v[64:67], a[136:139], v[112:115], v[64:67]\n"
"v_mfma_f32_16x16x32_bf16 v[64:67], a[140:143], v[84:87], v[64:67]\n"
"v_mfma_f32_16x16x32_bf16 v[64:67], a[144:147], v[128:131], v[64:67]\n"
"v_mfma_f32_16x16x32_bf16 v[64:67], a[148:151], v[100:103], v[64:67]\n"
"v_mfma_f32_16x16x32_bf16 v[64:67], a[152:155], v[156:159], v[64:67]\n"
"v_mfma_f32_16x16x32_bf16 v[64:67], a[156:159], v[116:119], v[64:67]\n"
"v_mfma_f32_16x16x32_bf16 v[60:63], a[188:191], v[116:119], v[168:171]\n"
"v_mfma_f32_16x16x32_bf16 v[60:63], a[184:187], v[156:159], v[60:63]\n"
"v_mfma_f32_16x16x32_bf16 v[60:63], a[180:183], v[100:103], v[60:63]\n"
"v_mfma_f32_16x16x32_bf16 v[60:63], a[176:179], v[128:131], v[60:63]\n"
"v_mfma_f32_16x16x32_bf16 v[60:63], a[172:175], v[84:87], v[60:63]\n"
"v_mfma_f32_16x16x32_bf16 v[60:63], a[168:171], v[112:115], v[60:63]\n"
"s_nop 7\n"
"s_branch .Lmk_p58r\n"
".Lmk_p59s:\n"
";\n"
"s_waitcnt vmcnt(16)\n"
"s_mov_b32 s1, 0x10000\n"
".Lmk_p59l:\n"
"s_add_u32 s96, s56, 0x400000\n"
"buffer_load_dwordx4 v[132:135], v0, s[4:7], s96 offen sc1\n"
"buffer_load_dwordx4 v[184:187], v0, s[4:7], s96 offen offset:1024 sc1\n"
"buffer_load_dwordx4 v[140:143], v0, s[4:7], s96 offen offset:2048 sc1\n"
"buffer_load_dwordx4 v[68:71], v0, s[4:7], s96 offen offset:3072 sc1\n"
"buffer_load_dwordx4 v[36:39], v1, s[4:7], s96 offen sc1\n"
"buffer_load_dwordx4 v[40:43], v1, s[4:7], s96 offen offset:1024 sc1\n"
"buffer_load_dwordx4 v[72:75], v1, s[4:7], s96 offen offset:2048 sc1\n"
"buffer_load_dwordx4 v[80:83], v1, s[4:7], s96 offen offset:3072 sc1\n"
"s_add_u32 s96, s57, 0x400000\n"
"buffer_load_dwordx4 v[164:167], v0, s[4:7], s96 offen sc1\n"
"buffer_load_dwordx4 v[168:171], v0, s[4:7], s96 offen offset:1024 sc1\n"
"buffer_load_dwordx4 v[208:211], v0, s[4:7], s96 offen offset:2048 sc1\n"
"buffer_load_dwordx4 v[212:215], v0, s[4:7], s96 offen offset:3072 sc1\n"
"buffer_load_dwordx4 v[64:67], v1, s[4:7], s96 offen sc1\n"
"buffer_load_dwordx4 v[100:103], v1, s[4:7], s96 offen offset:1024 sc1\n"
"buffer_load_dwordx4 v[60:63], v1, s[4:7], s96 offen offset:2048 sc1\n"
"buffer_load_dwordx4 v[76:79], v1, s[4:7], s96 offen offset:3072 sc1\n"
"s_add_u32 s96, s58, 0x400000\n"
"buffer_load_dwordx4 v[96:99], v0, s[4:7], s96 offen sc1\n"
"buffer_load_dwordx4 v[112:115], v0, s[4:7], s96 offen offset:1024 sc1\n"
"buffer_load_dwordx4 v[52:55], v0, s[4:7], s96 offen offset:2048 sc1\n"
"buffer_load_dwordx4 v[152:155], v0, s[4:7], s96 offen offset:3072 sc1\n"
"s_waitcnt vmcnt(0)\n"
"v_max_u32_e32 v255, v132, v133\n"
"v_max3_u32 v255, v255, v134, v135\n"
"v_max3_u32 v255, v255, v184, v185\n"
"v_max3_u32 v255, v255, v186, v187\n"
"v_max3_u32 v255, v255, v140, v141\n"
"v_max3_u32 v255, v255, v142, v143\n"
"v_max3_u32 v255, v255, v68, v69\n"
"v_max3_u32 v255, v255, v70, v71\n"
"v_max3_u32 v255, v255, v36, v37\n"
"v_max3_u32 v255, v255, v38, v39\n"
"v_max3_u32 v255, v255, v40, v41\n"
"v_max3_u32 v255, v255, v42, v43\n"
"v_max3_u32 v255, v255, v72, v73\n"
"v_max3_u32 v255, v255, v74, v75\n"
"v_max3_u32 v255, v255, v80, v81\n"
"v_max3_u32 v255, v255, v82, v83\n"
"v_cmp_eq_u32_e32 vcc, -1, v255\n"
"s_cbranch_vccz .Lmk_p59k\n"
"s_sub_u32 s1, s1, 1\n"
"s_cbranch_scc0 .Lmk_p59l\n"
".Lmk_p59k:\n"
"v_mfma_f32_16x16x32_bf16 v[84:87], a[128:131], v[132:135], 0\n"
"v_mfma_f32_16x16x32_bf16 v[84:87], a[132:135], v[184:187], v[84:87]\n"
"v_mfma_f32_16x16x32_bf16 v[84:87], a[136:139], v[140:143], v[84:87]\n"
"v_mfma_f32_16x16x32_bf16 v[84:87], a[140:143], v[68:71], v[84:87]\n"
"v_mfma_f32_16x16x32_bf16 v[84:87], a[144:147], v[36:39], v[84:87]\n"
"v_mfma_f32_16x16x32_bf16 v[84:87], a[148:151], v[40:43], v[84:87]\n"
"v_mfma_f32_16x16x32_bf16 v[84:87], a[152:155], v[72:75], v[84:87]\n"
"v_mfma_f32_16x16x32_bf16 v[84:87], a[156:159], v[80:83], v[84:87]\n"
"v_mfma_f32_16x16x32_bf16 v[128:131], a[188:191], v[80:83], 0\n"
"v_mfma_f32_16x16x32_bf16 v[128:131], a[184:187], v[72:75], v[128:131]\n"
"v_mfma_f32_16x16x32_bf16 v[128:131], a[180:183], v[40:43], v[128:131]\n"
"v_mfma_f32_16x16x32_bf16 v[128:131], a[176:179], v[36:39], v[128:131]\n"
"v_mfma_f32_16x16x32_bf16 v[128:131], a[172:175], v[68:71], v[128:131]\n"
"v_mfma_f32_16x16x32_bf16 v[128:131], a[168:171], v[140:143], v[128:131]\n"
"s_nop 7\n"
"s_branch .Lmk_p59r\n"
".Lmk_p60s:\n"
";\n"
"s_waitcnt vmcnt(16)\n"
"s_mov_b32 s1, 0x10000\n"
".Lmk_p60l:\n"
"s_add_u32 s96, s57, 0x400000\n"
"buffer_load_dwordx4 v[164:167], v0, s[4:7], s96 offen sc1\n"
"buffer_load_dwordx4 v[168:171], v0, s[4:7], s96 offen offset:1024 sc1\n"
"buffer_load_dwordx4 v[208:211], v0, s[4:7], s96 offen offset:2048 sc1\n"
"buffer_load_dwordx4 v[212:215], v0, s[4:7], s96 offen offset:3072 sc1\n"
"buffer_load_dwordx4 v[64:67], v1, s[4:7], s96 offen sc1\n"
"buffer_load_dwordx4 v[100:103], v1, s[4:7], s96 offen offset:1024 sc1\n"
"buffer_load_dwordx4 v[60:63], v1, s[4:7], s96 offen offset:2048 sc1\n"
"buffer_load_dwordx4 v[76:79], v1, s[4:7], s96 offen offset:3072 sc1\n"
"s_add_u32 s96, s58, 0x400000\n"
"buffer_load_dwordx4 v[96:99], v0, s[4:7], s96 offen sc1\n"
"buffer_load_dwordx4 v[112:115], v0, s[4:7], s96 offen offset:1024 sc1\n"
"buffer_load_dwordx4 v[52:55], v0, s[4:7], s96 offen offset:2048 sc1\n"
"buffer_load_dwordx4 v[152:155], v0, s[4:7], s96 offen offset:3072 sc1\n"
"buffer_load_dwordx4 v[84:87], v1, s[4:7], s96 offen sc1\n"
"buffer_load_dwordx4 v[172:175], v1, s[4:7], s96 offen offset:1024 sc1\n"
"buffer_load_dwordx4 v[128:131], v1, s[4:7], s96 offen offset:2048 sc1\n"
"buffer_load_dwordx4 v[72:75], v1, s[4:7], s96 offen offset:3072 sc1\n"
"s_add_u32 s96, s59, 0x400000\n"
"buffer_load_dwordx4 v[36:39], v0, s[4:7], s96 offen sc1\n"
"buffer_load_dwordx4 v[40:43], v0, s[4:7], s96 offen offset:1024 sc1\n"
"buffer_load_dwordx4 v[80:83], v0, s[4:7], s96 offen offset:2048 sc1\n"
"buffer_load_dwordx4 v[132:135], v0, s[4:7], s96 offen offset:3072 sc1\n"
"buffer_load_dwordx4 v[156:159], v1, s[4:7], s96 offen sc1\n"
"s_waitcnt vmcnt(0)\n"
"v_max_u32_e32 v254, v164, v165\n"
"v_max3_u32 v254, v254, v166, v167\n"
"v_max3_u32 v254, v254, v168, v169\n"
"v_max3_u32 v254, v254, v170, v171\n"
"v_max3_u32 v254, v254, v208, v209\n"
"v_max3_u32 v254, v254, v210, v211\n"
"v_max3_u32 v254, v254, v212, v213\n"
"v_max3_u32 v254, v254, v214, v215\n"
"v_max3_u32 v254, v254, v64, v65\n"
"v_max3_u32 v254, v254, v66, v67\n"
"v_max3_u32 v254, v254, v100, v101\n"
"v_max3_u32 v254, v254, v102, v103\n"
"v_max3_u32 v254, v254, v60, v61\n"
"v_max3_u32 v254, v254, v62, v63\n"
"v_max3_u32 v254, v254, v76, v77\n"
"v_max3_u32 v254, v254, v78, v79\n"
"v_cmp_eq_u32_e32 vcc, -1, v254\n"
"s_cbranch_vccz .Lmk_p60k\n"
"s_sub_u32 s1, s1, 1\n"
"s_cbranch_scc0 .Lmk_p60l\n"
".Lmk_p60k:\n"
"v_mfma_f32_16x16x32_bf16 v[68:71], a[128:131], v[164:167], 0\n"
"v_mfma_f32_16x16x32_bf16 v[68:71], a[132:135], v[168:171], v[68:71]\n"
"v_mfma_f32_16x16x32_bf16 v[68:71], a[136:139], v[208:211], v[68:71]\n"
"v_mfma_f32_16x16x32_bf16 v[68:71], a[140:143], v[212:215], v[68:71]\n"
"v_mfma_f32_16x16x32_bf16 v[68:71], a[144:147], v[64:67], v[68:71]\n"
"v_mfma_f32_16x16x32_bf16 v[68:71], a[148:151], v[100:103], v[68:71]\n"
"v_mfma_f32_16x16x32_bf16 v[68:71], a[152:155], v[60:63], v[68:71]\n"
"v_mfma_f32_16x16x32_bf16 v[68:71], a[156:159], v[76:79], v[68:71]\n"
"v_mfma_f32_16x16x32_bf16 v[140:143], a[188:191], v[76:79], 0\n"
"v_mfma_f32_16x16x32_bf16 v[140:143], a[184:187], v[60:63], v[140:143]\n"
"v_mfma_f32_16x16x32_bf16 v[140:143], a[180:183], v[100:103], v[140:143]\n"
"v_mfma_f32_16x16x32_bf16 v[140:143], a[176:179], v[64:67], v[140:143]\n"
"v_mfma_f32_16x16x32_bf16 v[140:143], a[172:175], v[212:215], v[140:143]\n"
"v_mfma_f32_16x16x32_bf16 v[140:143], a[168:171], v[208:211], v[140:143]\n"
"s_nop 7\n"
"s_branch .Lmk_p60r\n"
".Lmk_p61s:\n"
";\n"
"s_waitcnt vmcnt(16)\n"
"s_mov_b32 s1, 0x10000\n"
".Lmk_p61l:\n"
"s_add_u32 s97, s58, 0x400000\n"
"buffer_load_dwordx4 v[96:99], v0, s[4:7], s97 offen sc1\n"
"buffer_load_dwordx4 v[112:115], v0, s[4:7], s97 offen offset:1024 sc1\n"
"buffer_load_dwordx4 v[52:55], v0, s[4:7], s97 offen offset:2048 sc1\n"
"buffer_load_dwordx4 v[152:155], v0, s[4:7], s97 offen offset:3072 sc1\n"
"buffer_load_dwordx4 v[84:87], v1, s[4:7], s97 offen sc1\n"
"buffer_load_dwordx4 v[172:175], v1, s[4:7], s97 offen offset:1024 sc1\n"
"buffer_load_dwordx4 v[128:131], v1, s[4:7], s97 offen offset:2048 sc1\n"
"buffer_load_dwordx4 v[72:75], v1, s[4:7], s97 offen offset:3072 sc1\n"
"s_add_u32 s97, s59, 0x400000\n"
"buffer_load_dwordx4 v[36:39], v0, s[4:7], s97 offen sc1\n"
"buffer_load_dwordx4 v[40:43], v0, s[4:7], s97 offen offset:1024 sc1\n"
"buffer_load_dwordx4 v[80:83], v0, s[4:7], s97 offen offset:2048 sc1\n"
"buffer_load_dwordx4 v[132:135], v0, s[4:7], s97 offen offset:3072 sc1\n"
"buffer_load_dwordx4 v[156:159], v1, s[4:7], s97 offen sc1\n"
"buffer_load_dwordx4 v[68:71], v1, s[4:7], s97 offen offset:1024 sc1\n"
"buffer_load_dwordx4 v[200:203], v1, s[4:7], s97 offen offset:2048 sc1\n"
"buffer_load_dwordx4 v[140:143], v1, s[4:7], s97 offen offset:3072 sc1\n"
"s_waitcnt vmcnt(0)\n"
"v_max_u32_e32 v253, v96, v97\n"
"v_max3_u32 v253, v253, v98, v99\n"
"v_max3_u32 v253, v253, v112, v113\n"
"v_max3_u32 v253, v253, v114, v115\n"
"v_max3_u32 v253, v253, v52, v53\n"
"v_max3_u32 v253, v253, v54, v55\n"
"v_max3_u32 v253, v253, v152, v153\n"
"v_max3_u32 v253, v253, v154, v155\n"
"v_max3_u32 v253, v253, v84, v85\n"
"v_max3_u32 v253, v253, v86, v87\n"
"v_max3_u32 v253, v253, v172, v173\n"
"v_max3_u32 v253, v253, v174, v175\n"
"v_max3_u32 v253, v253, v128, v129\n"
"v_max3_u32 v253, v253, v130, v131\n"
"v_max3_u32 v253, v253, v72, v73\n"
"v_max3_u32 v253, v253, v74, v75\n"
"v_cmp_eq_u32_e32 vcc, -1, v253\n"
"s_cbranch_vccz .Lmk_p61k\n"
"s_sub_u32 s1, s1, 1\n"
"s_cbranch_scc0 .Lmk_p61l\n"
".Lmk_p61k:\n"
"v_mfma_f32_16x16x32_bf16 v[60:63], a[128:131], v[96:99], 0\n"
"v_mfma_f32_16x16x32_bf16 v[60:63], a[132:135], v[112:115], v[60:63]\n"
"v_mfma_f32_16x16x32_bf16 v[60:63], a[136:139], v[52:55], v[60:63]\n"
"v_mfma_f32_16x16x32_bf16 v[60:63], a[140:143], v[152:155], v[60:63]\n"
"v_mfma_f32_16x16x32_bf16 v[60:63], a[144:147], v[84:87], v[60:63]\n"
"v_mfma_f32_16x16x32_bf16 v[60:63], a[148:151], v[172:175], v[60:63]\n"
"v_mfma_f32_16x16x32_bf16 v[60:63], a[152:155], v[128:131], v[60:63]\n"
"v_mfma_f32_16x16x32_bf16 v[60:63], a[156:159], v[72:75], v[60:63]\n"
"v_mfma_f32_16x16x32_bf16 v[64:67], a[188:191], v[72:75], 0\n"
"v_mfma_f32_16x16x32_bf16 v[64:67], a[184:187], v[128:131], v[64:67]\n"
"v_mfma_f32_16x16x32_bf16 v[64:67], a[180:183], v[172:175], v[64:67]\n"
"v_mfma_f32_16x16x32_bf16 v[64:67], a[176:179], v[84:87], v[64:67]\n"
"v_mfma_f32_16x16x32_bf16 v[64:67], a[172:175], v[152:155], v[64:67]\n"
"v_mfma_f32_16x16x32_bf16 v[64:67], a[168:171], v[52:55], v[64:67]\n"
"s_nop 7\n"
"s_branch .Lmk_p61r\n"
".Lmk_p62s:\n"
";\n"
"s_waitcnt vmcnt(16)\n"
"s_mov_b32 s1, 0x10000\n"
".Lmk_p62l:\n"
"s_add_u32 s97, s59, 0x400000\n"
"buffer_load_dwordx4 v[36:39], v0, s[4:7], s97 offen sc1\n"
"buffer_load_dwordx4 v[40:43], v0, s[4:7], s97 offen offset:1024 sc1\n"
"buffer_load_dwordx4 v[80:83], v0, s[4:7], s97 offen offset:2048 sc1\n"
"buffer_load_dwordx4 v[132:135], v0, s[4:7], s97 offen offset:3072 sc1\n"
"buffer_load_dwordx4 v[156:159], v1, s[4:7], s97 offen sc1\n"
"buffer_load_dwordx4 v[68:71], v1, s[4:7], s97 offen offset:1024 sc1\n"
"buffer_load_dwordx4 v[200:203], v1, s[4:7], s97 offen offset:2048 sc1\n"
"buffer_load_dwordx4 v[140:143], v1, s[4:7], s97 offen offset:3072 sc1\n"
"s_waitcnt vmcnt(0)\n"
"v_max_u32_e32 v254, v36, v37\n"
"v_max3_u32 v254, v254, v38, v39\n"
"v_max3_u32 v254, v254, v40, v41\n"
"v_max3_u32 v254, v254, v42, v43\n"
"v_max3_u32 v254, v254, v80, v81\n"
"v_max3_u32 v254, v254, v82, v83\n"
"v_max3_u32 v254, v254, v132, v133\n"
"v_max3_u32 v254, v254, v134, v135\n"
"v_max3_u32 v254, v254, v156, v157\n"
"v_max3_u32 v254, v254, v158, v159\n"
"v_max3_u32 v254, v254, v68, v69\n"
"v_max3_u32 v254, v254, v70, v71\n"
"v_max3_u32 v254, v254, v200, v201\n"
"v_max3_u32 v254, v254, v202, v203\n"
"v_max3_u32 v254, v254, v140, v141\n"
"v_max3_u32 v254, v254, v142, v143\n"
"v_cmp_eq_u32_e32 vcc, -1, v254\n"
"s_cbranch_vccz .Lmk_p62k\n"
"s_sub_u32 s1, s1, 1\n"
"s_cbranch_scc0 .Lmk_p62l\n"
".Lmk_p62k:\n"
"v_mfma_f32_16x16x32_bf16 v[52:55], a[128:131], v[36:39], v[76:79]\n"
"v_mfma_f32_16x16x32_bf16 v[52:55], a[132:135], v[40:43], v[52:55]\n"
"v_mfma_f32_16x16x32_bf16 v[52:55], a[136:139], v[80:83], v[52:55]\n"
"v_mfma_f32_16x16x32_bf16 v[52:55], a[140:143], v[132:135], v[52:55]\n"
"v_mfma_f32_16x16x32_bf16 v[52:55], a[144:147], v[156:159], v[52:55]\n"
"v_mfma_f32_16x16x32_bf16 v[52:55], a[148:151], v[68:71], v[52:55]\n"
"v_mfma_f32_16x16x32_bf16 v[52:55], a[152:155], v[200:203], v[52:55]\n"
"v_mfma_f32_16x16x32_bf16 v[52:55], a[156:159], v[140:143], v[52:55]\n"
"v_mfma_f32_16x16x32_bf16 v[60:63], a[188:191], v[140:143], v[116:119]\n"
"v_mfma_f32_16x16x32_bf16 v[60:63], a[184:187], v[200:203], v[60:63]\n"
"v_mfma_f32_16x16x32_bf16 v[60:63], a[180:183], v[68:71], v[60:63]\n"
"v_mfma_f32_16x16x32_bf16 v[60:63], a[176:179], v[156:159], v[60:63]\n"
"v_mfma_f32_16x16x32_bf16 v[60:63], a[172:175], v[132:135], v[60:63]\n"
"v_mfma_f32_16x16x32_bf16 v[60:63], a[168:171], v[80:83], v[60:63]\n"
"s_nop 7\n"
"s_branch .Lmk_p62r\n"
".Lmk_p63s:\n"
";\n"
"s_waitcnt vmcnt(16)\n"
"s_mov_b32 s1, 0x10000\n"
".Lmk_p63l:\n"
"s_add_u32 s96, s56, 0x440000\n"
"buffer_load_dwordx4 v[120:123], v0, s[4:7], s96 offen sc1\n"
"buffer_load_dwordx4 v[136:139], v0, s[4:7], s96 offen offset:1024 sc1\n"
"buffer_load_dwordx4 v[144:147], v0, s[4:7], s96 offen offset:2048 sc1\n"
"buffer_load_dwordx4 v[192:195], v0, s[4:7], s96 offen offset:3072 sc1\n"
"buffer_load_dwordx4 v[196:199], v1, s[4:7], s96 offen sc1\n"
"buffer_load_dwordx4 v[188:191], v1, s[4:7], s96 offen offset:1024 sc1\n"
"buffer_load_dwordx4 v[108:111], v1, s[4:7], s96 offen offset:2048 sc1\n"
"buffer_load_dwordx4 v[92:95], v1, s[4:7], s96 offen offset:3072 sc1\n"
"s_add_u32 s96, s57, 0x440000\n"
"buffer_load_dwordx4 v[148:151], v0, s[4:7], s96 offen sc1\n"
"buffer_load_dwordx4 v[180:183], v0, s[4:7], s96 offen offset:1024 sc1\n"
"buffer_load_dwordx4 v[24:27], v0, s[4:7], s96 offen offset:2048 sc1\n"
"buffer_load_dwordx4 v[32:35], v0, s[4:7], s96 offen offset:3072 sc1\n"
"buffer_load_dwordx4 v[44:47], v1, s[4:7], s96 offen sc1\n"
"buffer_load_dwordx4 v[48:51], v1, s[4:7], s96 offen offset:1024 sc1\n"
"buffer_load_dwordx4 v[28:31], v1, s[4:7], s96 offen offset:2048 sc1\n"
"buffer_load_dwordx4 v[56:59], v1, s[4:7], s96 offen offset:3072 sc1\n"
"s_add_u32 s96, s58, 0x440000\n"
"buffer_load_dwordx4 v[36:39], v0, s[4:7], s96 offen sc1\n"
"buffer_load_dwordx4 v[40:43], v0, s[4:7], s96 offen offset:1024 sc1\n"
"buffer_load_dwordx4 v[52:55], v0, s[4:7], s96 offen offset:2048 sc1\n"
"buffer_load_dwordx4 v[60:63], v0, s[4:7], s96 offen offset:3072 sc1\n"
"buffer_load_dwordx4 v[64:67], v1, s[4:7], s96 offen sc1\n"
"buffer_load_dwordx4 v[68:71], v1, s[4:7], s96 offen offset:1024 sc1\n"
"buffer_load_dwordx4 v[72:75], v1, s[4:7], s96 offen offset:2048 sc1\n"
"buffer_load_dwordx4 v[76:79], v1, s[4:7], s96 offen offset:3072 sc1\n"
"s_add_u32 s96, s59, 0x440000\n"
"buffer_load_dwordx4 v[84:87], v0, s[4:7], s96 offen sc1\n"
"buffer_load_dwordx4 v[88:91], v0, s[4:7], s96 offen offset:1024 sc1\n"
"buffer_load_dwordx4 v[100:103], v0, s[4:7], s96 offen offset:2048 sc1\n"
"buffer_load_dwordx4 v[104:107], v0, s[4:7], s96 offen offset:3072 sc1\n"
"s_waitcnt vmcnt(0)\n"
"v_max_u32_e32 v255, v120, v121\n"
"v_max3_u32 v255, v255, v122, v123\n"
"v_max3_u32 v255, v255, v136, v137\n"
"v_max3_u32 v255, v255, v138, v139\n"
"v_max3_u32 v255, v255, v144, v145\n"
"v_max3_u32 v255, v255, v146, v147\n"
"v_max3_u32 v255, v255, v192, v193\n"
"v_max3_u32 v255, v255, v194, v195\n"
"v_max3_u32 v255, v255, v196, v197\n"
"v_max3_u32 v255, v255, v198, v199\n"
"v_max3_u32 v255, v255, v188, v189\n"
"v_max3_u32 v255, v255, v190, v191\n"
"v_max3_u32 v255, v255, v108, v109\n"
"v_max3_u32 v255, v255, v110, v111\n"
"v_max3_u32 v255, v255, v92, v93\n"
"v_max3_u32 v255, v255, v94, v95\n"
"v_cmp_eq_u32_e32 vcc, -1, v255\n"
"s_cbranch_vccz .Lmk_p63k\n"
"s_sub_u32 s1, s1, 1\n"
"s_cbranch_scc0 .Lmk_p63l\n"
".Lmk_p63k:\n"
"v_mfma_f32_16x16x32_bf16 v[80:83], a[128:131], v[120:123], 0\n"
"v_mfma_f32_16x16x32_bf16 v[80:83], a[132:135], v[136:139], v[80:83]\n"
"v_mfma_f32_16x16x32_bf16 v[80:83], a[136:139], v[144:147], v[80:83]\n"
"v_mfma_f32_16x16x32_bf16 v[80:83], a[140:143], v[192:195], v[80:83]\n"
"v_mfma_f32_16x16x32_bf16 v[80:83], a[144:147], v[196:199], v[80:83]\n"
"v_mfma_f32_16x16x32_bf16 v[80:83], a[148:151], v[188:191], v[80:83]\n"
"v_mfma_f32_16x16x32_bf16 v[80:83], a[152:155], v[108:111], v[80:83]\n"
"v_mfma_f32_16x16x32_bf16 v[80:83], a[156:159], v[92:95], v[80:83]\n"
"v_mfma_f32_16x16x32_bf16 v[96:99], a[188:191], v[92:95], 0\n"
"v_mfma_f32_16x16x32_bf16 v[96:99], a[184:187], v[108:111], v[96:99]\n"
"v_mfma_f32_16x16x32_bf16 v[96:99], a[180:183], v[188:191], v[96:99]\n"
"v_mfma_f32_16x16x32_bf16 v[96:99], a[176:179], v[196:199], v[96:99]\n"
"v_mfma_f32_16x16x32_bf16 v[96:99], a[172:175], v[192:195], v[96:99]\n"
"v_mfma_f32_16x16x32_bf16 v[96:99], a[168:171], v[144:147], v[96:99]\n"
"s_nop 7\n"
"s_branch .Lmk_p63r\n"
".Lmk_p64s:\n"
";\n"
"s_waitcnt vmcnt(16)\n"
"s_mov_b32 s1, 0x10000\n"
".Lmk_p64l:\n"
"s_add_u32 s96, s57, 0x440000\n"
"buffer_load_dwordx4 v[148:151], v0, s[4:7], s96 offen sc1\n"
"buffer_load_dwordx4 v[180:183], v0, s[4:7], s96 offen offset:1024 sc1\n"
"buffer_load_dwordx4 v[24:27], v0, s[4:7], s96 offen offset:2048 sc1\n"
"buffer_load_dwordx4 v[32:35], v0, s[4:7], s96 offen offset:3072 sc1\n"
"buffer_load_dwordx4 v[44:47], v1, s[4:7], s96 offen sc1\n"
"buffer_load_dwordx4 v[48:51], v1, s[4:7], s96 offen offset:1024 sc1\n"
"buffer_load_dwordx4 v[28:31], v1, s[4:7], s96 offen offset:2048 sc1\n"
"buffer_load_dwordx4 v[56:59], v1, s[4:7], s96 offen offset:3072 sc1\n"
"s_add_u32 s96, s58, 0x440000\n"
"buffer_load_dwordx4 v[36:39], v0, s[4:7], s96 offen sc1\n"
"buffer_load_dwordx4 v[40:43], v0, s[4:7], s96 offen offset:1024 sc1\n"
"buffer_load_dwordx4 v[52:55], v0, s[4:7], s96 offen offset:2048 sc1\n"
"buffer_load_dwordx4 v[60:63], v0, s[4:7], s96 offen offset:3072 sc1\n"
"buffer_load_dwordx4 v[64:67], v1, s[4:7], s96 offen sc1\n"
"buffer_load_dwordx4 v[68:71], v1, s[4:7], s96 offen offset:1024 sc1\n"
"buffer_load_dwordx4 v[72:75], v1, s[4:7], s96 offen offset:2048 sc1\n"
"buffer_load_dwordx4 v[76:79], v1, s[4:7], s96 offen offset:3072 sc1\n"
"s_add_u32 s96, s59, 0x440000\n"
"buffer_load_dwordx4 v[84:87], v0, s[4:7], s96 offen sc1\n"
"buffer_load_dwordx4 v[88:91], v0, s[4:7], s96 offen offset:1024 sc1\n"
"buffer_load_dwordx4 v[100:103], v0, s[4:7], s96 offen offset:2048 sc1\n"
"buffer_load_dwordx4 v[104:107], v0, s[4:7], s96 offen offset:3072 sc1\n"
"buffer_load_dwordx4 v[80:83], v1, s[4:7], s96 offen sc1\n"
"buffer_load_dwordx4 v[116:119], v1, s[4:7], s96 offen offset:1024 sc1\n"
"buffer_load_dwordx4 v[96:99], v1, s[4:7], s96 offen offset:2048 sc1\n"
"buffer_load_dwordx4 v[92:95], v1, s[4:7], s96 offen offset:3072 sc1\n"
"s_add_u32 s96, s56, 0x400000\n"
"buffer_load_dwordx4 v[120:123], v0, s[4:7], s96 offen sc1\n"
"buffer_load_dwordx4 v[124:127], v0, s[4:7], s96 offen offset:1024 sc1\n"
"s_waitcnt vmcnt(0)\n"
"v_max_u32_e32 v255, v148, v149\n"
"v_max3_u32 v255, v255, v150, v151\n"
"v_max3_u32 v255, v255, v180, v181\n"
"v_max3_u32 v255, v255, v182, v183\n"
"v_max3_u32 v255, v255, v24, v25\n"
"v_max3_u32 v255, v255, v26, v27\n"
"v_max3_u32 v255, v255, v32, v33\n"
"v_max3_u32 v255, v255, v34, v35\n"
"v_max3_u32 v255, v255, v44, v45\n"
"v_max3_u32 v255, v255, v46, v47\n"
"v_max3_u32 v255, v255, v48, v49\n"
"v_max3_u32 v255, v255, v50, v51\n"
"v_max3_u32 v255, v255, v28, v29\n"
"v_max3_u32 v255, v255, v30, v31\n"
"v_max3_u32 v255, v255, v56, v57\n"
"v_max3_u32 v255, v255, v58, v59\n"
"v_cmp_eq_u32_e32 vcc, -1, v255\n"
"s_cbranch_vccz .Lmk_p64k\n"
"s_sub_u32 s1, s1, 1\n"
"s_cbranch_scc0 .Lmk_p64l\n"
".Lmk_p64k:\n"
"v_mfma_f32_16x16x32_bf16 v[108:111], a[128:131], v[148:151], 0\n"
"v_mfma_f32_16x16x32_bf16 v[108:111], a[132:135], v[180:183], v[108:111]\n"
"v_mfma_f32_16x16x32_bf16 v[108:111], a[136:139], v[24:27], v[108:111]\n"
"v_mfma_f32_16x16x32_bf16 v[108:111], a[140:143], v[32:35], v[108:111]\n"
"v_mfma_f32_16x16x32_bf16 v[108:111], a[144:147], v[44:47], v[108:111]\n"
"v_mfma_f32_16x16x32_bf16 v[108:111], a[148:151], v[48:51], v[108:111]\n"
"v_mfma_f32_16x16x32_bf16 v[108:111], a[152:155], v[28:31], v[108:111]\n"
"v_mfma_f32_16x16x32_bf16 v[108:111], a[156:159], v[56:59], v[108:111]\n"
"v_mfma_f32_16x16x32_bf16 v[112:115], a[188:191], v[56:59], 0\n"
"v_mfma_f32_16x16x32_bf16 v[112:115], a[184:187], v[28:31], v[112:115]\n"
"v_mfma_f32_16x16x32_bf16 v[112:115], a[180:183], v[48:51], v[112:115]\n"
"v_mfma_f32_16x16x32_bf16 v[112:115], a[176:179], v[44:47], v[112:115]\n"
"v_mfma_f32_16x16x32_bf16 v[112:115], a[172:175], v[32:35], v[112:115]\n"
"v_mfma_f32_16x16x32_bf16 v[112:115], a[168:171], v[24:27], v[112:115]\n"
"s_nop 7\n"
"s_branch .Lmk_p64r\n"
".Lmk_p65s:\n"
";\n"
"s_waitcnt vmcnt(16)\n"
"s_mov_b32 s1, 0x10000\n"
".Lmk_p65l:\n"
"s_add_u32 s96, s58, 0x440000\n"
"buffer_load_dwordx4 v[36:39], v0, s[4:7], s96 offen sc1\n"
"buffer_load_dwordx4 v[40:43], v0, s[4:7], s96 offen offset:1024 sc1\n"
"buffer_load_dwordx4 v[52:55], v0, s[4:7], s96 offen offset:2048 sc1\n"
"buffer_load_dwordx4 v[60:63], v0, s[4:7], s96 offen offset:3072 sc1\n"
"buffer_load_dwordx4 v[64:67], v1, s[4:7], s96 offen sc1\n"
"buffer_load_dwordx4 v[68:71], v1, s[4:7], s96 offen offset:1024 sc1\n"
"buffer_load_dwordx4 v[72:75], v1, s[4:7], s96 offen offset:2048 sc1\n"
"buffer_load_dwordx4 v[76:79], v1, s[4:7], s96 offen offset:3072 sc1\n"
"s_add_u32 s96, s59, 0x440000\n"
"buffer_load_dwordx4 v[84:87], v0, s[4:7], s96 offen sc1\n"
"buffer_load_dwordx4 v[88:91], v0, s[4:7], s96 offen offset:1024 sc1\n"
"buffer_load_dwordx4 v[100:103], v0, s[4:7], s96 offen offset:2048 sc1\n"
"buffer_load_dwordx4 v[104:107], v0, s[4:7], s96 offen offset:3072 sc1\n"
"buffer_load_dwordx4 v[80:83], v1, s[4:7], s96 offen sc1\n"
"buffer_load_dwordx4 v[116:119], v1, s[4:7], s96 offen offset:1024 sc1\n"
"buffer_load_dwordx4 v[96:99], v1, s[4:7], s96 offen offset:2048 sc1\n"
"buffer_load_dwordx4 v[92:95], v1, s[4:7], s96 offen offset:3072 sc1\n"
"s_add_u32 s96, s56, 0x400000\n"
"buffer_load_dwordx4 v[120:123], v0, s[4:7], s96 offen sc1\n"
"buffer_load_dwordx4 v[124:127], v0, s[4:7], s96 offen offset:1024 sc1\n"
"buffer_load_dwordx4 v[108:111], v0, s[4:7], s96 offen offset:2048 sc1\n"
"buffer_load_dwordx4 v[132:135], v0, s[4:7], s96 offen offset:3072 sc1\n"
"buffer_load_dwordx4 v[112:115], v1, s[4:7], s96 offen sc1\n"
"buffer_load_dwordx4 v[28:31], v1, s[4:7], s96 offen offset:1024 sc1\n"
"buffer_load_dwordx4 v[32:35], v1, s[4:7], s96 offen offset:2048 sc1\n"
"buffer_load_dwordx4 v[24:27], v1, s[4:7], s96 offen offset:3072 sc1\n"
"s_add_u32 s96, s57, 0x400000\n"
"buffer_load_dwordx4 v[48:51], v0, s[4:7], s96 offen sc1\n"
"buffer_load_dwordx4 v[56:59], v0, s[4:7], s96 offen offset:1024 sc1\n"
"buffer_load_dwordx4 v[136:139], v0, s[4:7], s96 offen offset:2048 sc1\n"
"s_waitcnt vmcnt(0)\n"
"v_max_u32_e32 v239, v36, v37\n"
"v_max3_u32 v239, v239, v38, v39\n"
"v_max3_u32 v239, v239, v40, v41\n"
"v_max3_u32 v239, v239, v42, v43\n"
"v_max3_u32 v239, v239, v52, v53\n"
"v_max3_u32 v239, v239, v54, v55\n"
"v_max3_u32 v239, v239, v60, v61\n"
"v_max3_u32 v239, v239, v62, v63\n"
"v_max3_u32 v239, v239, v64, v65\n"
"v_max3_u32 v239, v239, v66, v67\n"
"v_max3_u32 v239, v239, v68, v69\n"
"v_max3_u32 v239, v239, v70, v71\n"
"v_max3_u32 v239, v239, v72, v73\n"
"v_max3_u32 v239, v239, v74, v75\n"
"v_max3_u32 v239, v239, v76, v77\n"
"v_max3_u32 v239, v239, v78, v79\n"
"v_cmp_eq_u32_e32 vcc, -1, v239\n"
"s_cbranch_vccz .Lmk_p65k\n"
"s_sub_u32 s1, s1, 1\n"
"s_cbranch_scc0 .Lmk_p65l\n"
".Lmk_p65k:\n"
"v_mfma_f32_16x16x32_bf16 v[44:47], a[128:131], v[36:39], 0\n"
"v_mfma_f32_16x16x32_bf16 v[44:47], a[132:135], v[40:43], v[44:47]\n"
"v_mfma_f32_16x16x32_bf16 v[44:47], a[136:139], v[52:55], v[44:47]\n"
"v_mfma_f32_16x16x32_bf16 v[44:47], a[140:143], v[60:63], v[44:47]\n"
"v_mfma_f32_16x16x32_bf16 v[44:47], a[144:147], v[64:67], v[44:47]\n"
"v_mfma_f32_16x16x32_bf16 v[44:47], a[148:151], v[68:71], v[44:47]\n"
"v_mfma_f32_16x16x32_bf16 v[44:47], a[152:155], v[72:75], v[44:47]\n"
"v_mfma_f32_16x16x32_bf16 v[44:47], a[156:159], v[76:79], v[44:47]\n"
"v_mfma_f32_16x16x32_bf16 v[128:131], a[188:191], v[76:79], 0\n"
"v_mfma_f32_16x16x32_bf16 v[128:131], a[184:187], v[72:75], v[128:131]\n"
"v_mfma_f32_16x16x32_bf16 v[128:131], a[180:183], v[68:71], v[128:131]\n"
"v_mfma_f32_16x16x32_bf16 v[128:131], a[176:179], v[64:67], v[128:131]\n"
"v_mfma_f32_16x16x32_bf16 v[128:131], a[172:175], v[60:63], v[128:131]\n"
"v_mfma_f32_16x16x32_bf16 v[128:131], a[168:171], v[52:55], v[128:131]\n"
"s_nop 7\n"
"s_branch .Lmk_p65r\n"
".Lmk_p66s:\n"
";\n"
"s_waitcnt vmcnt(16)\n"
"s_mov_b32 s1, 0x10000\n"
".Lmk_p66l:\n"
"s_add_u32 s96, s59, 0x440000\n"
"buffer_load_dwordx4 v[84:87], v0, s[4:7], s96 offen sc1\n"
"buffer_load_dwordx4 v[88:91], v0, s[4:7], s96 offen offset:1024 sc1\n"
"buffer_load_dwordx4 v[100:103], v0, s[4:7], s96 offen offset:2048 sc1\n"
"buffer_load_dwordx4 v[104:107], v0, s[4:7], s96 offen offset:3072 sc1\n"
"buffer_load_dwordx4 v[80:83], v1, s[4:7], s96 offen sc1\n"
"buffer_load_dwordx4 v[116:119], v1, s[4:7], s96 offen offset:1024 sc1\n"
"buffer_load_dwordx4 v[96:99], v1, s[4:7], s96 offen offset:2048 sc1\n"
"buffer_load_dwordx4 v[92:95], v1, s[4:7], s96 offen offset:3072 sc1\n"
"s_add_u32 s96, s56, 0x400000\n"
"buffer_load_dwordx4 v[120:123], v0, s[4:7], s96 offen sc1\n"
"buffer_load_dwordx4 v[124:127], v0, s[4:7], s96 offen offset:1024 sc1\n"
"buffer_load_dwordx4 v[108:111], v0, s[4:7], s96 offen offset:2048 sc1\n"
"buffer_load_dwordx4 v[132:135], v0, s[4:7], s96 offen offset:3072 sc1\n"
"buffer_load_dwordx4 v[112:115], v1, s[4:7], s96 offen sc1\n"
"buffer_load_dwordx4 v[28:31], v1, s[4:7], s96 offen offset:1024 sc1\n"
"buffer_load_dwordx4 v[32:35], v1, s[4:7], s96 offen offset:2048 sc1\n"
"buffer_load_dwordx4 v[24:27], v1, s[4:7], s96 offen offset:3072 sc1\n"
"s_add_u32 s96, s57, 0x400000\n"
"buffer_load_dwordx4 v[48:51], v0, s[4:7], s96 offen sc1\n"
"buffer_load_dwordx4 v[56:59], v0, s[4:7], s96 offen offset:1024 sc1\n"
"buffer_load_dwordx4 v[136:139], v0, s[4:7], s96 offen offset:2048 sc1\n"
"buffer_load_dwordx4 v[44:47], v0, s[4:7], s96 offen offset:3072 sc1\n"
"buffer_load_dwordx4 v[160:163], v1, s[4:7], s96 offen sc1\n"
"buffer_load_dwordx4 v[128:131], v1, s[4:7], s96 offen offset:1024 sc1\n"
"buffer_load_dwordx4 v[72:75], v1, s[4:7], s96 offen offset:2048 sc1\n"
"buffer_load_dwordx4 v[60:63], v1, s[4:7], s96 offen offset:3072 sc1\n"
"s_add_u32 s96, s58, 0x400000\n"
"buffer_load_dwordx4 v[36:39], v0, s[4:7], s96 offen sc1\n"
"buffer_load_dwordx4 v[68:71], v0, s[4:7], s96 offen offset:1024 sc1\n"
"buffer_load_dwordx4 v[76:79], v0, s[4:7], s96 offen offset:2048 sc1\n"
"buffer_load_dwordx4 v[148:151], v0, s[4:7], s96 offen offset:3072 sc1\n"
"s_waitcnt vmcnt(0)\n"
"v_max_u32_e32 v255, v84, v85\n"
"v_max3_u32 v255, v255, v86, v87\n"
"v_max3_u32 v255, v255, v88, v89\n"
"v_max3_u32 v255, v255, v90, v91\n"
"v_max3_u32 v255, v255, v100, v101\n"
"v_max3_u32 v255, v255, v102, v103\n"
"v_max3_u32 v255, v255, v104, v105\n"
"v_max3_u32 v255, v255, v106, v107\n"
"v_max3_u32 v255, v255, v80, v81\n"
"v_max3_u32 v255, v255, v82, v83\n"
"v_max3_u32 v255, v255, v116, v117\n"
"v_max3_u32 v255, v255, v118, v119\n"
"v_max3_u32 v255, v255, v96, v97\n"
"v_max3_u32 v255, v255, v98, v99\n"
"v_max3_u32 v255, v255, v92, v93\n"
"v_max3_u32 v255, v255, v94, v95\n"
"v_cmp_eq_u32_e32 vcc, -1, v255\n"
"s_cbranch_vccz .Lmk_p66k\n"
"s_sub_u32 s1, s1, 1\n"
"s_cbranch_scc0 .Lmk_p66l\n"
".Lmk_p66k:\n"
"v_mfma_f32_16x16x32_bf16 v[64:67], a[128:131], v[84:87], v[140:143]\n"
"v_mfma_f32_16x16x32_bf16 v[64:67], a[132:135], v[88:91], v[64:67]\n"
"v_mfma_f32_16x16x32_bf16 v[64:67], a[136:139], v[100:103], v[64:67]\n"
"v_mfma_f32_16x16x32_bf16 v[64:67], a[140:143], v[104:107], v[64:67]\n"
"v_mfma_f32_16x16x32_bf16 v[64:67], a[144:147], v[80:83], v[64:67]\n"
"v_mfma_f32_16x16x32_bf16 v[64:67], a[148:151], v[116:119], v[64:67]\n"
"v_mfma_f32_16x16x32_bf16 v[64:67], a[152:155], v[96:99], v[64:67]\n"
"v_mfma_f32_16x16x32_bf16 v[64:67], a[156:159], v[92:95], v[64:67]\n"
"v_mfma_f32_16x16x32_bf16 v[52:55], a[188:191], v[92:95], v[152:155]\n"
"v_mfma_f32_16x16x32_bf16 v[52:55], a[184:187], v[96:99], v[52:55]\n"
"v_mfma_f32_16x16x32_bf16 v[52:55], a[180:183], v[116:119], v[52:55]\n"
"v_mfma_f32_16x16x32_bf16 v[52:55], a[176:179], v[80:83], v[52:55]\n"
"v_mfma_f32_16x16x32_bf16 v[52:55], a[172:175], v[104:107], v[52:55]\n"
"v_mfma_f32_16x16x32_bf16 v[52:55], a[168:171], v[100:103], v[52:55]\n"
"s_nop 7\n"
"s_branch .Lmk_p66r\n"
".Lmk_p68s:\n"
";\n"
"s_waitcnt vmcnt(16)\n"
"s_mov_b32 s1, 0x10000\n"
".Lmk_p68l:\n"
"s_add_u32 s95, s56, 0x400000\n"
"buffer_load_dwordx4 v[120:123], v0, s[4:7], s95 offen sc1\n"
"buffer_load_dwordx4 v[124:127], v0, s[4:7], s95 offen offset:1024 sc1\n"
"buffer_load_dwordx4 v[108:111], v0, s[4:7], s95 offen offset:2048 sc1\n"
"buffer_load_dwordx4 v[132:135], v0, s[4:7], s95 offen offset:3072 sc1\n"
"buffer_load_dwordx4 v[112:115], v1, s[4:7], s95 offen sc1\n"
"buffer_load_dwordx4 v[28:31], v1, s[4:7], s95 offen offset:1024 sc1\n"
"buffer_load_dwordx4 v[32:35], v1, s[4:7], s95 offen offset:2048 sc1\n"
"buffer_load_dwordx4 v[24:27], v1, s[4:7], s95 offen offset:3072 sc1\n"
"s_add_u32 s95, s57, 0x400000\n"
"buffer_load_dwordx4 v[48:51], v0, s[4:7], s95 offen sc1\n"
"buffer_load_dwordx4 v[56:59], v0, s[4:7], s95 offen offset:1024 sc1\n"
"buffer_load_dwordx4 v[136:139], v0, s[4:7], s95 offen offset:2048 sc1\n"
"buffer_load_dwordx4 v[44:47], v0, s[4:7], s95 offen offset:3072 sc1\n"
"buffer_load_dwordx4 v[160:163], v1, s[4:7], s95 offen sc1\n"
"buffer_load_dwordx4 v[128:131], v1, s[4:7], s95 offen offset:1024 sc1\n"
"buffer_load_dwordx4 v[72:75], v1, s[4:7], s95 offen offset:2048 sc1\n"
"buffer_load_dwordx4 v[60:63], v1, s[4:7], s95 offen offset:3072 sc1\n"
"s_add_u32 s95, s58, 0x400000\n"
"buffer_load_dwordx4 v[36:39], v0, s[4:7], s95 offen sc1\n"
"buffer_load_dwordx4 v[68:71], v0, s[4:7], s95 offen offset:1024 sc1\n"
"buffer_load_dwordx4 v[76:79], v0, s[4:7], s95 offen offset:2048 sc1\n"
"buffer_load_dwordx4 v[148:151], v0, s[4:7], s95 offen offset:3072 sc1\n"
"buffer_load_dwordx4 v[152:155], v1, s[4:7], s95 offen sc1\n"
"buffer_load_dwordx4 v[172:175], v1, s[4:7], s95 offen offset:1024 sc1\n"
"buffer_load_dwordx4 v[176:179], v1, s[4:7], s95 offen offset:2048 sc1\n"
"buffer_load_dwordx4 v[64:67], v1, s[4:7], s95 offen offset:3072 sc1\n"
"s_add_u32 s95, s59, 0x400000\n"
"buffer_load_dwordx4 v[80:83], v0, s[4:7], s95 offen sc1\n"
"buffer_load_dwordx4 v[84:87], v0, s[4:7], s95 offen offset:1024 sc1\n"
"buffer_load_dwordx4 v[88:91], v0, s[4:7], s95 offen offset:2048 sc1\n"
"buffer_load_dwordx4 v[92:95], v0, s[4:7], s95 offen offset:3072 sc1\n"
"buffer_load_dwordx4 v[96:99], v1, s[4:7], s95 offen sc1\n"
"buffer_load_dwordx4 v[100:103], v1, s[4:7], s95 offen offset:1024 sc1\n"
"buffer_load_dwordx4 v[104:107], v1, s[4:7], s95 offen offset:2048 sc1\n"
"buffer_load_dwordx4 v[116:119], v1, s[4:7], s95 offen offset:3072 sc1\n"
"s_waitcnt vmcnt(0)\n"
"v_max_u32_e32 v255, v120, v121\n"
"v_max3_u32 v255, v255, v122, v123\n"
"v_max3_u32 v255, v255, v124, v125\n"
"v_max3_u32 v255, v255, v126, v127\n"
"v_max3_u32 v255, v255, v108, v109\n"
"v_max3_u32 v255, v255, v110, v111\n"
"v_max3_u32 v255, v255, v132, v133\n"
"v_max3_u32 v255, v255, v134, v135\n"
"v_max3_u32 v255, v255, v112, v113\n"
"v_max3_u32 v255, v255, v114, v115\n"
"v_max3_u32 v255, v255, v28, v29\n"
"v_max3_u32 v255, v255, v30, v31\n"
"v_max3_u32 v255, v255, v32, v33\n"
"v_max3_u32 v255, v255, v34, v35\n"
"v_max3_u32 v255, v255, v24, v25\n"
"v_max3_u32 v255, v255, v26, v27\n"
"v_cmp_eq_u32_e32 vcc, -1, v255\n"
"s_cbranch_vccz .Lmk_p68k\n"
"s_sub_u32 s1, s1, 1\n"
"s_cbranch_scc0 .Lmk_p68l\n"
".Lmk_p68k:\n"
"v_mfma_f32_16x16x32_bf16 v[140:143], a[128:131], v[120:123], 0\n"
"v_mfma_f32_16x16x32_bf16 v[140:143], a[132:135], v[124:127], v[140:143]\n"
"v_mfma_f32_16x16x32_bf16 v[140:143], a[136:139], v[108:111], v[140:143]\n"
"v_mfma_f32_16x16x32_bf16 v[140:143], a[140:143], v[132:135], v[140:143]\n"
"v_mfma_f32_16x16x32_bf16 v[140:143], a[144:147], v[112:115], v[140:143]\n"
"v_mfma_f32_16x16x32_bf16 v[140:143], a[148:151], v[28:31], v[140:143]\n"
"v_mfma_f32_16x16x32_bf16 v[140:143], a[152:155], v[32:35], v[140:143]\n"
"v_mfma_f32_16x16x32_bf16 v[140:143], a[156:159], v[24:27], v[140:143]\n"
"v_mfma_f32_16x16x32_bf16 v[40:43], a[188:191], v[24:27], 0\n"
"v_mfma_f32_16x16x32_bf16 v[40:43], a[184:187], v[32:35], v[40:43]\n"
"v_mfma_f32_16x16x32_bf16 v[40:43], a[180:183], v[28:31], v[40:43]\n"
"v_mfma_f32_16x16x32_bf16 v[40:43], a[176:179], v[112:115], v[40:43]\n"
"v_mfma_f32_16x16x32_bf16 v[40:43], a[172:175], v[132:135], v[40:43]\n"
"v_mfma_f32_16x16x32_bf16 v[40:43], a[168:171], v[108:111], v[40:43]\n"
"s_nop 7\n"
"s_branch .Lmk_p68r\n"
".Lmk_p69s:\n"
";\n"
"s_waitcnt vmcnt(16)\n"
"s_mov_b32 s1, 0x10000\n"
".Lmk_p69l:\n"
"s_add_u32 s97, s57, 0x400000\n"
"buffer_load_dwordx4 v[48:51], v0, s[4:7], s97 offen sc1\n"
"buffer_load_dwordx4 v[56:59], v0, s[4:7], s97 offen offset:1024 sc1\n"
"buffer_load_dwordx4 v[136:139], v0, s[4:7], s97 offen offset:2048 sc1\n"
"buffer_load_dwordx4 v[44:47], v0, s[4:7], s97 offen offset:3072 sc1\n"
"buffer_load_dwordx4 v[160:163], v1, s[4:7], s97 offen sc1\n"
"buffer_load_dwordx4 v[128:131], v1, s[4:7], s97 offen offset:1024 sc1\n"
"buffer_load_dwordx4 v[72:75], v1, s[4:7], s97 offen offset:2048 sc1\n"
"buffer_load_dwordx4 v[60:63], v1, s[4:7], s97 offen offset:3072 sc1\n"
"s_add_u32 s97, s58, 0x400000\n"
"buffer_load_dwordx4 v[36:39], v0, s[4:7], s97 offen sc1\n"
"buffer_load_dwordx4 v[68:71], v0, s[4:7], s97 offen offset:1024 sc1\n"
"buffer_load_dwordx4 v[76:79], v0, s[4:7], s97 offen offset:2048 sc1\n"
"buffer_load_dwordx4 v[148:151], v0, s[4:7], s97 offen offset:3072 sc1\n"
"buffer_load_dwordx4 v[152:155], v1, s[4:7], s97 offen sc1\n"
"buffer_load_dwordx4 v[172:175], v1, s[4:7], s97 offen offset:1024 sc1\n"
"buffer_load_dwordx4 v[176:179], v1, s[4:7], s97 offen offset:2048 sc1\n"
"buffer_load_dwordx4 v[64:67], v1, s[4:7], s97 offen offset:3072 sc1\n"
"s_add_u32 s97, s59, 0x400000\n"
"buffer_load_dwordx4 v[80:83], v0, s[4:7], s97 offen sc1\n"
"buffer_load_dwordx4 v[84:87], v0, s[4:7], s97 offen offset:1024 sc1\n"
"buffer_load_dwordx4 v[88:91], v0, s[4:7], s97 offen offset:2048 sc1\n"
"buffer_load_dwordx4 v[92:95], v0, s[4:7], s97 offen offset:3072 sc1\n"
"buffer_load_dwordx4 v[96:99], v1, s[4:7], s97 offen sc1\n"
"buffer_load_dwordx4 v[100:103], v1, s[4:7], s97 offen offset:1024 sc1\n"
"buffer_load_dwordx4 v[104:107], v1, s[4:7], s97 offen offset:2048 sc1\n"
"buffer_load_dwordx4 v[116:119], v1, s[4:7], s97 offen offset:3072 sc1\n"
"s_waitcnt vmcnt(0)\n"
"v_max_u32_e32 v255, v48, v49\n"
"v_max3_u32 v255, v255, v50, v51\n"
"v_max3_u32 v255, v255, v56, v57\n"
"v_max3_u32 v255, v255, v58, v59\n"
"v_max3_u32 v255, v255, v136, v137\n"
"v_max3_u32 v255, v255, v138, v139\n"
"v_max3_u32 v255, v255, v44, v45\n"
"v_max3_u32 v255, v255, v46, v47\n"
"v_max3_u32 v255, v255, v160, v161\n"
"v_max3_u32 v255, v255, v162, v163\n"
"v_max3_u32 v255, v255, v128, v129\n"
"v_max3_u32 v255, v255, v130, v131\n"
"v_max3_u32 v255, v255, v72, v73\n"
"v_max3_u32 v255, v255, v74, v75\n"
"v_max3_u32 v255, v255, v60, v61\n"
"v_max3_u32 v255, v255, v62, v63\n"
"v_cmp_eq_u32_e32 vcc, -1, v255\n"
"s_cbranch_vccz .Lmk_p69k\n"
"s_sub_u32 s1, s1, 1\n"
"s_cbranch_scc0 .Lmk_p69l\n"
".Lmk_p69k:\n"
"v_mfma_f32_16x16x32_bf16 v[24:27], a[128:131], v[48:51], 0\n"
"v_mfma_f32_16x16x32_bf16 v[24:27], a[132:135], v[56:59], v[24:27]\n"
"v_mfma_f32_16x16x32_bf16 v[24:27], a[136:139], v[136:139], v[24:27]\n"
"v_mfma_f32_16x16x32_bf16 v[24:27], a[140:143], v[44:47], v[24:27]\n"
"v_mfma_f32_16x16x32_bf16 v[24:27], a[144:147], v[160:163], v[24:27]\n"
"v_mfma_f32_16x16x32_bf16 v[24:27], a[148:151], v[128:131], v[24:27]\n"
"v_mfma_f32_16x16x32_bf16 v[24:27], a[152:155], v[72:75], v[24:27]\n"
"v_mfma_f32_16x16x32_bf16 v[24:27], a[156:159], v[60:63], v[24:27]\n"
"v_mfma_f32_16x16x32_bf16 v[28:31], a[188:191], v[60:63], 0\n"
"v_mfma_f32_16x16x32_bf16 v[28:31], a[184:187], v[72:75], v[28:31]\n"
"v_mfma_f32_16x16x32_bf16 v[28:31], a[180:183], v[128:131], v[28:31]\n"
"v_mfma_f32_16x16x32_bf16 v[28:31], a[176:179], v[160:163], v[28:31]\n"
"v_mfma_f32_16x16x32_bf16 v[28:31], a[172:175], v[44:47], v[28:31]\n"
"v_mfma_f32_16x16x32_bf16 v[28:31], a[168:171], v[136:139], v[28:31]\n"
"s_nop 7\n"
"s_branch .Lmk_p69r\n"
".Lmk_p70s:\n"
";\n"
"s_waitcnt vmcnt(16)\n"
"s_mov_b32 s1, 0x10000\n"
".Lmk_p70l:\n"
"s_add_u32 s97, s58, 0x400000\n"
"buffer_load_dwordx4 v[36:39], v0, s[4:7], s97 offen sc1\n"
"buffer_load_dwordx4 v[68:71], v0, s[4:7], s97 offen offset:1024 sc1\n"
"buffer_load_dwordx4 v[76:79], v0, s[4:7], s97 offen offset:2048 sc1\n"
"buffer_load_dwordx4 v[148:151], v0, s[4:7], s97 offen offset:3072 sc1\n"
"buffer_load_dwordx4 v[152:155], v1, s[4:7], s97 offen sc1\n"
"buffer_load_dwordx4 v[172:175], v1, s[4:7], s97 offen offset:1024 sc1\n"
"buffer_load_dwordx4 v[176:179], v1, s[4:7], s97 offen offset:2048 sc1\n"
"buffer_load_dwordx4 v[64:67], v1, s[4:7], s97 offen offset:3072 sc1\n"
"s_add_u32 s97, s59, 0x400000\n"
"buffer_load_dwordx4 v[80:83], v0, s[4:7], s97 offen sc1\n"
"buffer_load_dwordx4 v[84:87], v0, s[4:7], s97 offen offset:1024 sc1\n"
"buffer_load_dwordx4 v[88:91], v0, s[4:7], s97 offen offset:2048 sc1\n"
"buffer_load_dwordx4 v[92:95], v0, s[4:7], s97 offen offset:3072 sc1\n"
"buffer_load_dwordx4 v[96:99], v1, s[4:7], s97 offen sc1\n"
"buffer_load_dwordx4 v[100:103], v1, s[4:7], s97 offen offset:1024 sc1\n"
"buffer_load_dwordx4 v[104:107], v1, s[4:7], s97 offen offset:2048 sc1\n"
"buffer_load_dwordx4 v[116:119], v1, s[4:7], s97 offen offset:3072 sc1\n"
"s_waitcnt vmcnt(0)\n"
"v_max_u32_e32 v239, v36, v37\n"
"v_max3_u32 v239, v239, v38, v39\n"
"v_max3_u32 v239, v239, v68, v69\n"
"v_max3_u32 v239, v239, v70, v71\n"
"v_max3_u32 v239, v239, v76, v77\n"
"v_max3_u32 v239, v239, v78, v79\n"
"v_max3_u32 v239, v239, v148, v149\n"
"v_max3_u32 v239, v239, v150, v151\n"
"v_max3_u32 v239, v239, v152, v153\n"
"v_max3_u32 v239, v239, v154, v155\n"
"v_max3_u32 v239, v239, v172, v173\n"
"v_max3_u32 v239, v239, v174, v175\n"
"v_max3_u32 v239, v239, v176, v177\n"
"v_max3_u32 v239, v239, v178, v179\n"
"v_max3_u32 v239, v239, v64, v65\n"
"v_max3_u32 v239, v239, v66, v67\n"
"v_cmp_eq_u32_e32 vcc, -1, v239\n"
"s_cbranch_vccz .Lmk_p70k\n"
"s_sub_u32 s1, s1, 1\n"
"s_cbranch_scc0 .Lmk_p70l\n"
".Lmk_p70k:\n"
"v_mfma_f32_16x16x32_bf16 v[24:27], a[128:131], v[36:39], 0\n"
"v_mfma_f32_16x16x32_bf16 v[24:27], a[132:135], v[68:71], v[24:27]\n"
"v_mfma_f32_16x16x32_bf16 v[24:27], a[136:139], v[76:79], v[24:27]\n"
"v_mfma_f32_16x16x32_bf16 v[24:27], a[140:143], v[148:151], v[24:27]\n"
"v_mfma_f32_16x16x32_bf16 v[24:27], a[144:147], v[152:155], v[24:27]\n"
"v_mfma_f32_16x16x32_bf16 v[24:27], a[148:151], v[172:175], v[24:27]\n"
"v_mfma_f32_16x16x32_bf16 v[24:27], a[152:155], v[176:179], v[24:27]\n"
"v_mfma_f32_16x16x32_bf16 v[24:27], a[156:159], v[64:67], v[24:27]\n"
"v_mfma_f32_16x16x32_bf16 v[28:31], a[188:191], v[64:67], 0\n"
"v_mfma_f32_16x16x32_bf16 v[28:31], a[184:187], v[176:179], v[28:31]\n"
"v_mfma_f32_16x16x32_bf16 v[28:31], a[180:183], v[172:175], v[28:31]\n"
"v_mfma_f32_16x16x32_bf16 v[28:31], a[176:179], v[152:155], v[28:31]\n"
"v_mfma_f32_16x16x32_bf16 v[28:31], a[172:175], v[148:151], v[28:31]\n"
"v_mfma_f32_16x16x32_bf16 v[28:31], a[168:171], v[76:79], v[28:31]\n"
"s_nop 7\n"
"s_branch .Lmk_p70r\n"
".Lmk_p71s:\n"
";\n"
"s_waitcnt vmcnt(16)\n"
"s_mov_b32 s1, 0x10000\n"
".Lmk_p71l:\n"
"s_add_u32 s97, s59, 0x400000\n"
"buffer_load_dwordx4 v[80:83], v0, s[4:7], s97 offen sc1\n"
"buffer_load_dwordx4 v[84:87], v0, s[4:7], s97 offen offset:1024 sc1\n"
"buffer_load_dwordx4 v[88:91], v0, s[4:7], s97 offen offset:2048 sc1\n"
"buffer_load_dwordx4 v[92:95], v0, s[4:7], s97 offen offset:3072 sc1\n"
"buffer_load_dwordx4 v[96:99], v1, s[4:7], s97 offen sc1\n"
"buffer_load_dwordx4 v[100:103], v1, s[4:7], s97 offen offset:1024 sc1\n"
"buffer_load_dwordx4 v[104:107], v1, s[4:7], s97 offen offset:2048 sc1\n"
"buffer_load_dwordx4 v[116:119], v1, s[4:7], s97 offen offset:3072 sc1\n"
"s_waitcnt vmcnt(0)\n"
"v_max_u32_e32 v255, v80, v81\n"
"v_max3_u32 v255, v255, v82, v83\n"
"v_max3_u32 v255, v255, v84, v85\n"
"v_max3_u32 v255, v255, v86, v87\n"
"v_max3_u32 v255, v255, v88, v89\n"
"v_max3_u32 v255, v255, v90, v91\n"
"v_max3_u32 v255, v255, v92, v93\n"
"v_max3_u32 v255, v255, v94, v95\n"
"v_max3_u32 v255, v255, v96, v97\n"
"v_max3_u32 v255, v255, v98, v99\n"
"v_max3_u32 v255, v255, v100, v101\n"
"v_max3_u32 v255, v255, v102, v103\n"
"v_max3_u32 v255, v255, v104, v105\n"
"v_max3_u32 v255, v255, v106, v107\n"
"v_max3_u32 v255, v255, v116, v117\n"
"v_max3_u32 v255, v255, v118, v119\n"
"v_cmp_eq_u32_e32 vcc, -1, v255\n"
"s_cbranch_vccz .Lmk_p71k\n"
"s_sub_u32 s1, s1, 1\n"
"s_cbranch_scc0 .Lmk_p71l\n"
".Lmk_p71k:\n"
"v_mfma_f32_16x16x32_bf16 v[76:79], a[128:131], v[80:83], v[32:35]\n"
"v_mfma_f32_16x16x32_bf16 v[76:79], a[132:135], v[84:87], v[76:79]\n"
"v_mfma_f32_16x16x32_bf16 v[76:79], a[136:139], v[88:91], v[76:79]\n"
"v_mfma_f32_16x16x32_bf16 v[76:79], a[140:143], v[92:95], v[76:79]\n"
"v_mfma_f32_16x16x32_bf16 v[76:79], a[144:147], v[96:99], v[76:79]\n"
"v_mfma_f32_16x16x32_bf16 v[76:79], a[148:151], v[100:103], v[76:79]\n"
"v_mfma_f32_16x16x32_bf16 v[76:79], a[152:155], v[104:107], v[76:79]\n"
"v_mfma_f32_16x16x32_bf16 v[76:79], a[156:159], v[116:119], v[76:79]\n"
"v_mfma_f32_16x16x32_bf16 v[68:71], a[188:191], v[116:119], v[48:51]\n"
"v_mfma_f32_16x16x32_bf16 v[68:71], a[184:187], v[104:107], v[68:71]\n"
"v_mfma_f32_16x16x32_bf16 v[68:71], a[180:183], v[100:103], v[68:71]\n"
"v_mfma_f32_16x16x32_bf16 v[68:71], a[176:179], v[96:99], v[68:71]\n"
"v_mfma_f32_16x16x32_bf16 v[68:71], a[172:175], v[92:95], v[68:71]\n"
"v_mfma_f32_16x16x32_bf16 v[68:71], a[168:171], v[88:91], v[68:71]\n"
"s_nop 7\n"
"s_branch .Lmk_p71r\n"
".Lmk_p76s:\n"
";\n"
"s_waitcnt vmcnt(16)\n"
"s_mov_b32 s1, 0x10000\n"
".Lmk_p76l:\n"
"s_add_u32 s96, s52, 0x40000\n"
"buffer_load_dwordx4 v[84:87], v0, s[4:7], s96 offen sc1\n"
"buffer_load_dwordx4 v[92:95], v0, s[4:7], s96 offen offset:1024 sc1\n"
"buffer_load_dwordx4 v[96:99], v0, s[4:7], s96 offen offset:2048 sc1\n"
"buffer_load_dwordx4 v[104:107], v0, s[4:7], s96 offen offset:3072 sc1\n"
"buffer_load_dwordx4 v[112:115], v1, s[4:7], s96 offen sc1\n"
"buffer_load_dwordx4 v[116:119], v1, s[4:7], s96 offen offset:1024 sc1\n"
"buffer_load_dwordx4 v[120:123], v1, s[4:7], s96 offen offset:2048 sc1\n"
"buffer_load_dwordx4 v[136:139], v1, s[4:7], s96 offen offset:3072 sc1\n"
"s_add_u32 s96, s53, 0x40000\n"
"buffer_load_dwordx4 v[140:143], v0, s[4:7], s96 offen sc1\n"
"buffer_load_dwordx4 v[144:147], v0, s[4:7], s96 offen offset:1024 sc1\n"
"buffer_load_dwordx4 v[148:151], v0, s[4:7], s96 offen offset:2048 sc1\n"
"buffer_load_dwordx4 v[152:155], v0, s[4:7], s96 offen offset:3072 sc1\n"
"buffer_load_dwordx4 v[156:159], v1, s[4:7], s96 offen sc1\n"
"buffer_load_dwordx4 v[160:163], v1, s[4:7], s96 offen offset:1024 sc1\n"
"buffer_load_dwordx4 v[164:167], v1, s[4:7], s96 offen offset:2048 sc1\n"
"buffer_load_dwordx4 v[168:171], v1, s[4:7], s96 offen offset:3072 sc1\n"
"s_add_u32 s96, s54, 0x40000\n"
"buffer_load_dwordx4 v[176:179], v0, s[4:7], s96 offen sc1\n"
"buffer_load_dwordx4 v[40:43], v0, s[4:7], s96 offen offset:1024 sc1\n"
"s_waitcnt vmcnt(0)\n"
"v_max_u32_e32 v252, v84, v85\n"
"v_max3_u32 v252, v252, v86, v87\n"
"v_max3_u32 v252, v252, v92, v93\n"
"v_max3_u32 v252, v252, v94, v95\n"
"v_max3_u32 v252, v252, v96, v97\n"
"v_max3_u32 v252, v252, v98, v99\n"
"v_max3_u32 v252, v252, v104, v105\n"
"v_max3_u32 v252, v252, v106, v107\n"
"v_max3_u32 v252, v252, v112, v113\n"
"v_max3_u32 v252, v252, v114, v115\n"
"v_max3_u32 v252, v252, v116, v117\n"
"v_max3_u32 v252, v252, v118, v119\n"
"v_max3_u32 v252, v252, v120, v121\n"
"v_max3_u32 v252, v252, v122, v123\n"
"v_max3_u32 v252, v252, v136, v137\n"
"v_max3_u32 v252, v252, v138, v139\n"
"v_cmp_eq_u32_e32 vcc, -1, v252\n"
"s_cbranch_vccz .Lmk_p76k\n"
"s_sub_u32 s1, s1, 1\n"
"s_cbranch_scc0 .Lmk_p76l\n"
".Lmk_p76k:\n"
"v_mfma_f32_16x16x32_bf16 v[172:175], a[0:3], v[84:87], 0\n"
"v_mfma_f32_16x16x32_bf16 v[172:175], a[4:7], v[92:95], v[172:175]\n"
"v_mfma_f32_16x16x32_bf16 v[172:175], a[8:11], v[96:99], v[172:175]\n"
"v_mfma_f32_16x16x32_bf16 v[172:175], a[12:15], v[104:107], v[172:175]\n"
"v_mfma_f32_16x16x32_bf16 v[172:175], a[16:19], v[112:115], v[172:175]\n"
"v_mfma_f32_16x16x32_bf16 v[172:175], a[20:23], v[116:119], v[172:175]\n"
"v_mfma_f32_16x16x32_bf16 v[172:175], a[24:27], v[120:123], v[172:175]\n"
"v_mfma_f32_16x16x32_bf16 v[172:175], a[28:31], v[136:139], v[172:175]\n"
"v_mfma_f32_16x16x32_bf16 v[180:183], a[60:63], v[136:139], 0\n"
"v_mfma_f32_16x16x32_bf16 v[180:183], a[56:59], v[120:123], v[180:183]\n"
"v_mfma_f32_16x16x32_bf16 v[180:183], a[52:55], v[116:119], v[180:183]\n"
"v_mfma_f32_16x16x32_bf16 v[180:183], a[48:51], v[112:115], v[180:183]\n"
"v_mfma_f32_16x16x32_bf16 v[180:183], a[44:47], v[104:107], v[180:183]\n"
"v_mfma_f32_16x16x32_bf16 v[180:183], a[40:43], v[96:99], v[180:183]\n"
"s_nop 7\n"
"s_branch .Lmk_p76r\n"
".Lmk_p77s:\n"
";\n"
"s_waitcnt vmcnt(16)\n"
"s_mov_b32 s1, 0x10000\n"
".Lmk_p77l:\n"
"s_add_u32 s97, s53, 0x40000\n"
"buffer_load_dwordx4 v[140:143], v0, s[4:7], s97 offen sc1\n"
"buffer_load_dwordx4 v[144:147], v0, s[4:7], s97 offen offset:1024 sc1\n"
"buffer_load_dwordx4 v[148:151], v0, s[4:7], s97 offen offset:2048 sc1\n"
"buffer_load_dwordx4 v[152:155], v0, s[4:7], s97 offen offset:3072 sc1\n"
"buffer_load_dwordx4 v[156:159], v1, s[4:7], s97 offen sc1\n"
"buffer_load_dwordx4 v[160:163], v1, s[4:7], s97 offen offset:1024 sc1\n"
"buffer_load_dwordx4 v[164:167], v1, s[4:7], s97 offen offset:2048 sc1\n"
"buffer_load_dwordx4 v[168:171], v1, s[4:7], s97 offen offset:3072 sc1\n"
"s_add_u32 s97, s54, 0x40000\n"
"buffer_load_dwordx4 v[176:179], v0, s[4:7], s97 offen sc1\n"
"buffer_load_dwordx4 v[40:43], v0, s[4:7], s97 offen offset:1024 sc1\n"
"buffer_load_dwordx4 v[124:127], v0, s[4:7], s97 offen offset:2048 sc1\n"
"buffer_load_dwordx4 v[184:187], v0, s[4:7], s97 offen offset:3072 sc1\n"
"buffer_load_dwordx4 v[32:35], v1, s[4:7], s97 offen sc1\n"
"buffer_load_dwordx4 v[192:195], v1, s[4:7], s97 offen offset:1024 sc1\n"
"buffer_load_dwordx4 v[196:199], v1, s[4:7], s97 offen offset:2048 sc1\n"
"buffer_load_dwordx4 v[200:203], v1, s[4:7], s97 offen offset:3072 sc1\n"
"s_add_u32 s97, s55, 0x40000\n"
"buffer_load_dwordx4 v[84:87], v0, s[4:7], s97 offen sc1\n"
"buffer_load_dwordx4 v[92:95], v0, s[4:7], s97 offen offset:1024 sc1\n"
"s_waitcnt vmcnt(0)\n"
"v_max_u32_e32 v247, v140, v141\n"
"v_max3_u32 v247, v247, v142, v143\n"
"v_max3_u32 v247, v247, v144, v145\n"
"v_max3_u32 v247, v247, v146, v147\n"
"v_max3_u32 v247, v247, v148, v149\n"
"v_max3_u32 v247, v247, v150, v151\n"
"v_max3_u32 v247, v247, v152, v153\n"
"v_max3_u32 v247, v247, v154, v155\n"
"v_max3_u32 v247, v247, v156, v157\n"
"v_max3_u32 v247, v247, v158, v159\n"
"v_max3_u32 v247, v247, v160, v161\n"
"v_max3_u32 v247, v247, v162, v163\n"
"v_max3_u32 v247, v247, v164, v165\n"
"v_max3_u32 v247, v247, v166, v167\n"
"v_max3_u32 v247, v247, v168, v169\n"
"v_max3_u32 v247, v247, v170, v171\n"
"v_cmp_eq_u32_e32 vcc, -1, v247\n"
"s_cbranch_vccz .Lmk_p77k\n"
"s_sub_u32 s1, s1, 1\n"
"s_cbranch_scc0 .Lmk_p77l\n"
".Lmk_p77k:\n"
"v_mfma_f32_16x16x32_bf16 v[68:71], a[0:3], v[140:143], 0\n"
"v_mfma_f32_16x16x32_bf16 v[68:71], a[4:7], v[144:147], v[68:71]\n"
"v_mfma_f32_16x16x32_bf16 v[68:71], a[8:11], v[148:151], v[68:71]\n"
"v_mfma_f32_16x16x32_bf16 v[68:71], a[12:15], v[152:155], v[68:71]\n"
"v_mfma_f32_16x16x32_bf16 v[68:71], a[16:19], v[156:159], v[68:71]\n"
"v_mfma_f32_16x16x32_bf16 v[68:71], a[20:23], v[160:163], v[68:71]\n"
"v_mfma_f32_16x16x32_bf16 v[68:71], a[24:27], v[164:167], v[68:71]\n"
"v_mfma_f32_16x16x32_bf16 v[68:71], a[28:31], v[168:171], v[68:71]\n"
"v_mfma_f32_16x16x32_bf16 v[96:99], a[60:63], v[168:171], 0\n"
"v_mfma_f32_16x16x32_bf16 v[96:99], a[56:59], v[164:167], v[96:99]\n"
"v_mfma_f32_16x16x32_bf16 v[96:99], a[52:55], v[160:163], v[96:99]\n"
"v_mfma_f32_16x16x32_bf16 v[96:99], a[48:51], v[156:159], v[96:99]\n"
"v_mfma_f32_16x16x32_bf16 v[96:99], a[44:47], v[152:155], v[96:99]\n"
"v_mfma_f32_16x16x32_bf16 v[96:99], a[40:43], v[148:151], v[96:99]\n"
"s_nop 7\n"
"s_branch .Lmk_p77r\n"
".Lmk_m80s:\n"
";\n"
"s_waitcnt vmcnt(16)\n"
"s_mov_b32 s1, 0x10000\n"
".Lmk_m80l:\n"
"s_add_u32 s98, s62, 0x4000\n"
"buffer_load_dword v255, v10, s[4:7], s98 offen sc1\n"
"buffer_load_dword v254, v10, s[4:7], s98 offen offset:256 sc1\n"
"buffer_load_dword v253, v10, s[4:7], s98 offen offset:512 sc1\n"
"buffer_load_dword v252, v10, s[4:7], s98 offen offset:768 sc1\n"
"buffer_load_dword v251, v10, s[4:7], s98 offen offset:1024 sc1\n"
"buffer_load_dword v250, v10, s[4:7], s98 offen offset:1280 sc1\n"
"buffer_load_dword v249, v10, s[4:7], s98 offen offset:1536 sc1\n"
"buffer_load_dword v248, v10, s[4:7], s98 offen offset:1792 sc1\n"
"s_waitcnt vmcnt(0)\n"
"v_max_u32_e32 v247, v255, v254\n"
"v_max3_u32 v247, v247, v253, v252\n"
"v_max3_u32 v247, v247, v251, v250\n"
"v_max3_u32 v247, v247, v249, v248\n"
"v_cmp_eq_u32_e32 vcc, -1, v247\n"
"s_cbranch_vccz .Lmk_m80k\n"
"s_sub_u32 s1, s1, 1\n"
"s_cbranch_scc0 .Lmk_m80l\n"
".Lmk_m80k:\n"
"v_add_f32_e32 v255, v255, v251\n"
"v_add_f32_e32 v254, v254, v250\n"
"v_add_f32_e32 v253, v253, v249\n"
"v_add_f32_e32 v252, v252, v248\n"
"v_add_f32_e32 v255, v255, v253\n"
"v_add_f32_e32 v254, v254, v252\n"
"v_add_f32_e32 v255, v255, v254\n"
"s_nop 1\n"
"s_branch .Lmk_m80r\n"
".Lmk_p78s:\n"
";\n"
"s_waitcnt vmcnt(16)\n"
"s_mov_b32 s1, 0x10000\n"
".Lmk_p78l:\n"
"s_add_u32 s97, s54, 0x40000\n"
"buffer_load_dwordx4 v[176:179], v0, s[4:7], s97 offen sc1\n"
"buffer_load_dwordx4 v[40:43], v0, s[4:7], s97 offen offset:1024 sc1\n"
"buffer_load_dwordx4 v[124:127], v0, s[4:7], s97 offen offset:2048 sc1\n"
"buffer_load_dwordx4 v[184:187], v0, s[4:7], s97 offen offset:3072 sc1\n"
"buffer_load_dwordx4 v[32:35], v1, s[4:7], s97 offen sc1\n"
"buffer_load_dwordx4 v[192:195], v1, s[4:7], s97 offen offset:1024 sc1\n"
"buffer_load_dwordx4 v[196:199], v1, s[4:7], s97 offen offset:2048 sc1\n"
"buffer_load_dwordx4 v[200:203], v1, s[4:7], s97 offen offset:3072 sc1\n"
"s_add_u32 s97, s55, 0x40000\n"
"buffer_load_dwordx4 v[84:87], v0, s[4:7], s97 offen sc1\n"
"buffer_load_dwordx4 v[92:95], v0, s[4:7], s97 offen offset:1024 sc1\n"
"buffer_load_dwordx4 v[104:107], v0, s[4:7], s97 offen offset:2048 sc1\n"
"buffer_load_dwordx4 v[112:115], v0, s[4:7], s97 offen offset:3072 sc1\n"
"buffer_load_dwordx4 v[96:99], v1, s[4:7], s97 offen sc1\n"
"buffer_load_dwordx4 v[120:123], v1, s[4:7], s97 offen offset:1024 sc1\n"
"buffer_load_dwordx4 v[136:139], v1, s[4:7], s97 offen offset:2048 sc1\n"
"buffer_load_dwordx4 v[172:175], v1, s[4:7], s97 offen offset:3072 sc1\n"
"s_add_u32 s97, s52, 0x48000\n"
"buffer_load_dwordx4 v[140:143], v0, s[4:7], s97 offen sc1\n"
"s_waitcnt vmcnt(0)\n"
"v_max_u32_e32 v252, v176, v177\n"
"v_max3_u32 v252, v252, v178, v179\n"
"v_max3_u32 v252, v252, v40, v41\n"
"v_max3_u32 v252, v252, v42, v43\n"
"v_max3_u32 v252, v252, v124, v125\n"
"v_max3_u32 v252, v252, v126, v127\n"
"v_max3_u32 v252, v252, v184, v185\n"
"v_max3_u32 v252, v252, v186, v187\n"
"v_max3_u32 v252, v252, v32, v33\n"
"v_max3_u32 v252, v252, v34, v35\n"
"v_max3_u32 v252, v252, v192, v193\n"
"v_max3_u32 v252, v252, v194, v195\n"
"v_max3_u32 v252, v252, v196, v197\n"
"v_max3_u32 v252, v252, v198, v199\n"
"v_max3_u32 v252, v252, v200, v201\n"
"v_max3_u32 v252, v252, v202, v203\n"
"v_cmp_eq_u32_e32 vcc, -1, v252\n"
"s_cbranch_vccz .Lmk_p78k\n"
"s_sub_u32 s1, s1, 1\n"
"s_cbranch_scc0 .Lmk_p78l\n"
".Lmk_p78k:\n"
"v_mfma_f32_16x16x32_bf16 v[68:71], a[0:3], v[176:179], 0\n"
"v_mfma_f32_16x16x32_bf16 v[68:71], a[4:7], v[40:43], v[68:71]\n"
"v_mfma_f32_16x16x32_bf16 v[68:71], a[8:11], v[124:127], v[68:71]\n"
"v_mfma_f32_16x16x32_bf16 v[68:71], a[12:15], v[184:187], v[68:71]\n"
"v_mfma_f32_16x16x32_bf16 v[68:71], a[16:19], v[32:35], v[68:71]\n"
"v_mfma_f32_16x16x32_bf16 v[68:71], a[20:23], v[192:195], v[68:71]\n"
"v_mfma_f32_16x16x32_bf16 v[68:71], a[24:27], v[196:199], v[68:71]\n"
"v_mfma_f32_16x16x32_bf16 v[68:71], a[28:31], v[200:203], v[68:71]\n"
"v_mfma_f32_16x16x32_bf16 v[116:119], a[60:63], v[200:203], 0\n"
"v_mfma_f32_16x16x32_bf16 v[116:119], a[56:59], v[196:199], v[116:119]\n"
"v_mfma_f32_16x16x32_bf16 v[116:119], a[52:55], v[192:195], v[116:119]\n"
"v_mfma_f32_16x16x32_bf16 v[116:119], a[48:51], v[32:35], v[116:119]\n"
"v_mfma_f32_16x16x32_bf16 v[116:119], a[44:47], v[184:187], v[116:119]\n"
"v_mfma_f32_16x16x32_bf16 v[116:119], a[40:43], v[124:127], v[116:119]\n"
"s_nop 7\n"
"s_branch .Lmk_p78r\n"
".Lmk_p79s:\n"
";\n"
"s_waitcnt vmcnt(16)\n"
"s_mov_b32 s1, 0x10000\n"
".Lmk_p79l:\n"
"s_add_u32 s97, s55, 0x40000\n"
"buffer_load_dwordx4 v[84:87], v0, s[4:7], s97 offen sc1\n"
"buffer_load_dwordx4 v[92:95], v0, s[4:7], s97 offen offset:1024 sc1\n"
"buffer_load_dwordx4 v[104:107], v0, s[4:7], s97 offen offset:2048 sc1\n"
"buffer_load_dwordx4 v[112:115], v0, s[4:7], s97 offen offset:3072 sc1\n"
"buffer_load_dwordx4 v[96:99], v1, s[4:7], s97 offen sc1\n"
"buffer_load_dwordx4 v[120:123], v1, s[4:7], s97 offen offset:1024 sc1\n"
"buffer_load_dwordx4 v[136:139], v1, s[4:7], s97 offen offset:2048 sc1\n"
"buffer_load_dwordx4 v[172:175], v1, s[4:7], s97 offen offset:3072 sc1\n"
"s_add_u32 s97, s52, 0x48000\n"
"buffer_load_dwordx4 v[140:143], v0, s[4:7], s97 offen sc1\n"
"buffer_load_dwordx4 v[68:71], v0, s[4:7], s97 offen offset:1024 sc1\n"
"buffer_load_dwordx4 v[168:171], v0, s[4:7], s97 offen offset:2048 sc1\n"
"buffer_load_dwordx4 v[152:155], v0, s[4:7], s97 offen offset:3072 sc1\n"
"buffer_load_dwordx4 v[148:151], v1, s[4:7], s97 offen sc1\n"
"buffer_load_dwordx4 v[164:167], v1, s[4:7], s97 offen offset:1024 sc1\n"
"buffer_load_dwordx4 v[180:183], v1, s[4:7], s97 offen offset:2048 sc1\n"
"buffer_load_dwordx4 v[124:127], v1, s[4:7], s97 offen offset:3072 sc1\n"
"s_add_u32 s97, s53, 0x48000\n"
"buffer_load_dwordx4 v[200:203], v0, s[4:7], s97 offen sc1\n"
"buffer_load_dwordx4 v[116:119], v0, s[4:7], s97 offen offset:1024 sc1\n"
"s_waitcnt vmcnt(0)\n"
"v_max_u32_e32 v252, v84, v85\n"
"v_max3_u32 v252, v252, v86, v87\n"
"v_max3_u32 v252, v252, v92, v93\n"
"v_max3_u32 v252, v252, v94, v95\n"
"v_max3_u32 v252, v252, v104, v105\n"
"v_max3_u32 v252, v252, v106, v107\n"
"v_max3_u32 v252, v252, v112, v113\n"
"v_max3_u32 v252, v252, v114, v115\n"
"v_max3_u32 v252, v252, v96, v97\n"
"v_max3_u32 v252, v252, v98, v99\n"
"v_max3_u32 v252, v252, v120, v121\n"
"v_max3_u32 v252, v252, v122, v123\n"
"v_max3_u32 v252, v252, v136, v137\n"
"v_max3_u32 v252, v252, v138, v139\n"
"v_max3_u32 v252, v252, v172, v173\n"
"v_max3_u32 v252, v252, v174, v175\n"
"v_cmp_eq_u32_e32 vcc, -1, v252\n"
"s_cbranch_vccz .Lmk_p79k\n"
"s_sub_u32 s1, s1, 1\n"
"s_cbranch_scc0 .Lmk_p79l\n"
".Lmk_p79k:\n"
"v_mfma_f32_16x16x32_bf16 v[176:179], a[0:3], v[84:87], v[144:147]\n"
"v_mfma_f32_16x16x32_bf16 v[176:179], a[4:7], v[92:95], v[176:179]\n"
"v_mfma_f32_16x16x32_bf16 v[176:179], a[8:11], v[104:107], v[176:179]\n"
"v_mfma_f32_16x16x32_bf16 v[176:179], a[12:15], v[112:115], v[176:179]\n"
"v_mfma_f32_16x16x32_bf16 v[176:179], a[16:19], v[96:99], v[176:179]\n"
"v_mfma_f32_16x16x32_bf16 v[176:179], a[20:23], v[120:123], v[176:179]\n"
"v_mfma_f32_16x16x32_bf16 v[176:179], a[24:27], v[136:139], v[176:179]\n"
"v_mfma_f32_16x16x32_bf16 v[176:179], a[28:31], v[172:175], v[176:179]\n"
"v_mfma_f32_16x16x32_bf16 v[204:207], a[60:63], v[172:175], v[160:163]\n"
"v_mfma_f32_16x16x32_bf16 v[204:207], a[56:59], v[136:139], v[204:207]\n"
"v_mfma_f32_16x16x32_bf16 v[204:207], a[52:55], v[120:123], v[204:207]\n"
"v_mfma_f32_16x16x32_bf16 v[204:207], a[48:51], v[96:99], v[204:207]\n"
"v_mfma_f32_16x16x32_bf16 v[204:207], a[44:47], v[112:115], v[204:207]\n"
"v_mfma_f32_16x16x32_bf16 v[204:207], a[40:43], v[104:107], v[204:207]\n"
"s_nop 7\n"
"s_branch .Lmk_p79r\n"
".Lmk_p81s:\n"
";\n"
"s_waitcnt vmcnt(16)\n"
"s_mov_b32 s1, 0x10000\n"
".Lmk_p81l:\n"
"s_add_u32 s95, s52, 0x48000\n"
"buffer_load_dwordx4 v[140:143], v0, s[4:7], s95 offen sc1\n"
"buffer_load_dwordx4 v[68:71], v0, s[4:7], s95 offen offset:1024 sc1\n"
"buffer_load_dwordx4 v[168:171], v0, s[4:7], s95 offen offset:2048 sc1\n"
"buffer_load_dwordx4 v[152:155], v0, s[4:7], s95 offen offset:3072 sc1\n"
"buffer_load_dwordx4 v[148:151], v1, s[4:7], s95 offen sc1\n"
"buffer_load_dwordx4 v[164:167], v1, s[4:7], s95 offen offset:1024 sc1\n"
"buffer_load_dwordx4 v[180:183], v1, s[4:7], s95 offen offset:2048 sc1\n"
"buffer_load_dwordx4 v[124:127], v1, s[4:7], s95 offen offset:3072 sc1\n"
"s_add_u32 s95, s53, 0x48000\n"
"buffer_load_dwordx4 v[200:203], v0, s[4:7], s95 offen sc1\n"
"buffer_load_dwordx4 v[116:119], v0, s[4:7], s95 offen offset:1024 sc1\n"
"buffer_load_dwordx4 v[144:147], v0, s[4:7], s95 offen offset:2048 sc1\n"
"buffer_load_dwordx4 v[32:35], v0, s[4:7], s95 offen offset:3072 sc1\n"
"buffer_load_dwordx4 v[156:159], v1, s[4:7], s95 offen sc1\n"
"buffer_load_dwordx4 v[184:187], v1, s[4:7], s95 offen offset:1024 sc1\n"
"buffer_load_dwordx4 v[208:211], v1, s[4:7], s95 offen offset:2048 sc1\n"
"buffer_load_dwordx4 v[176:179], v1, s[4:7], s95 offen offset:3072 sc1\n"
"s_add_u32 s95, s54, 0x48000\n"
"buffer_load_dwordx4 v[92:95], v0, s[4:7], s95 offen sc1\n"
"buffer_load_dwordx4 v[96:99], v0, s[4:7], s95 offen offset:1024 sc1\n"
"s_waitcnt vmcnt(0)\n"
"v_max_u32_e32 v252, v140, v141\n"
"v_max3_u32 v252, v252, v142, v143\n"
"v_max3_u32 v252, v252, v68, v69\n"
"v_max3_u32 v252, v252, v70, v71\n"
"v_max3_u32 v252, v252, v168, v169\n"
"v_max3_u32 v252, v252, v170, v171\n"
"v_max3_u32 v252, v252, v152, v153\n"
"v_max3_u32 v252, v252, v154, v155\n"
"v_max3_u32 v252, v252, v148, v149\n"
"v_max3_u32 v252, v252, v150, v151\n"
"v_max3_u32 v252, v252, v164, v165\n"
"v_max3_u32 v252, v252, v166, v167\n"
"v_max3_u32 v252, v252, v180, v181\n"
"v_max3_u32 v252, v252, v182, v183\n"
"v_max3_u32 v252, v252, v124, v125\n"
"v_max3_u32 v252, v252, v126, v127\n"
"v_cmp_eq_u32_e32 vcc, -1, v252\n"
"s_cbranch_vccz .Lmk_p81k\n"
"s_sub_u32 s1, s1, 1\n"
"s_cbranch_scc0 .Lmk_p81l\n"
".Lmk_p81k:\n"
"v_mfma_f32_16x16x32_bf16 v[84:87], a[0:3], v[140:143], 0\n"
"v_mfma_f32_16x16x32_bf16 v[84:87], a[4:7], v[68:71], v[84:87]\n"
"v_mfma_f32_16x16x32_bf16 v[84:87], a[8:11], v[168:171], v[84:87]\n"
"v_mfma_f32_16x16x32_bf16 v[84:87], a[12:15], v[152:155], v[84:87]\n"
"v_mfma_f32_16x16x32_bf16 v[84:87], a[16:19], v[148:151], v[84:87]\n"
"v_mfma_f32_16x16x32_bf16 v[84:87], a[20:23], v[164:167], v[84:87]\n"
"v_mfma_f32_16x16x32_bf16 v[84:87], a[24:27], v[180:183], v[84:87]\n"
"v_mfma_f32_16x16x32_bf16 v[84:87], a[28:31], v[124:127], v[84:87]\n"
"v_mfma_f32_16x16x32_bf16 v[104:107], a[60:63], v[124:127], 0\n"
"v_mfma_f32_16x16x32_bf16 v[104:107], a[56:59], v[180:183], v[104:107]\n"
"v_mfma_f32_16x16x32_bf16 v[104:107], a[52:55], v[164:167], v[104:107]\n"
"v_mfma_f32_16x16x32_bf16 v[104:107], a[48:51], v[148:151], v[104:107]\n"
"v_mfma_f32_16x16x32_bf16 v[104:107], a[44:47], v[152:155], v[104:107]\n"
"v_mfma_f32_16x16x32_bf16 v[104:107], a[40:43], v[168:171], v[104:107]\n"
"s_nop 7\n"
"s_branch .Lmk_p81r\n"
".Lmk_p82s:\n"
";\n"
"s_waitcnt vmcnt(16)\n"
"s_mov_b32 s1, 0x10000\n"
".Lmk_p82l:\n"
"s_add_u32 s95, s53, 0x48000\n"
"buffer_load_dwordx4 v[200:203], v0, s[4:7], s95 offen sc1\n"
"buffer_load_dwordx4 v[116:119], v0, s[4:7], s95 offen offset:1024 sc1\n"
"buffer_load_dwordx4 v[144:147], v0, s[4:7], s95 offen offset:2048 sc1\n"
"buffer_load_dwordx4 v[32:35], v0, s[4:7], s95 offen offset:3072 sc1\n"
"buffer_load_dwordx4 v[156:159], v1, s[4:7], s95 offen sc1\n"
"buffer_load_dwordx4 v[184:187], v1, s[4:7], s95 offen offset:1024 sc1\n"
"buffer_load_dwordx4 v[208:211], v1, s[4:7], s95 offen offset:2048 sc1\n"
"buffer_load_dwordx4 v[176:179], v1, s[4:7], s95 offen offset:3072 sc1\n"
"s_add_u32 s95, s54, 0x48000\n"
"buffer_load_dwordx4 v[92:95], v0, s[4:7], s95 offen sc1\n"
"buffer_load_dwordx4 v[96:99], v0, s[4:7], s95 offen offset:1024 sc1\n"
"buffer_load_dwordx4 v[112:115], v0, s[4:7], s95 offen offset:2048 sc1\n"
"buffer_load_dwordx4 v[120:123], v0, s[4:7], s95 offen offset:3072 sc1\n"
"buffer_load_dwordx4 v[104:107], v1, s[4:7], s95 offen sc1\n"
"buffer_load_dwordx4 v[172:175], v1, s[4:7], s95 offen offset:1024 sc1\n"
"buffer_load_dwordx4 v[84:87], v1, s[4:7], s95 offen offset:2048 sc1\n"
"buffer_load_dwordx4 v[160:163], v1, s[4:7], s95 offen offset:3072 sc1\n"
"s_add_u32 s95, s55, 0x48000\n"
"buffer_load_dwordx4 v[68:71], v0, s[4:7], s95 offen sc1\n"
"buffer_load_dwordx4 v[124:127], v0, s[4:7], s95 offen offset:1024 sc1\n"
"s_waitcnt vmcnt(0)\n"
"v_max_u32_e32 v247, v200, v201\n"
"v_max3_u32 v247, v247, v202, v203\n"
"v_max3_u32 v247, v247, v116, v117\n"
"v_max3_u32 v247, v247, v118, v119\n"
"v_max3_u32 v247, v247, v144, v145\n"
"v_max3_u32 v247, v247, v146, v147\n"
"v_max3_u32 v247, v247, v32, v33\n"
"v_max3_u32 v247, v247, v34, v35\n"
"v_max3_u32 v247, v247, v156, v157\n"
"v_max3_u32 v247, v247, v158, v159\n"
"v_max3_u32 v247, v247, v184, v185\n"
"v_max3_u32 v247, v247, v186, v187\n"
"v_max3_u32 v247, v247, v208, v209\n"
"v_max3_u32 v247, v247, v210, v211\n"
"v_max3_u32 v247, v247, v176, v177\n"
"v_max3_u32 v247, v247, v178, v179\n"
"v_cmp_eq_u32_e32 vcc, -1, v247\n"
"s_cbranch_vccz .Lmk_p82k\n"
"s_sub_u32 s1, s1, 1\n"
"s_cbranch_scc0 .Lmk_p82l\n"
".Lmk_p82k:\n"
"v_mfma_f32_16x16x32_bf16 v[40:43], a[0:3], v[200:203], 0\n"
"v_mfma_f32_16x16x32_bf16 v[40:43], a[4:7], v[116:119], v[40:43]\n"
"v_mfma_f32_16x16x32_bf16 v[40:43], a[8:11], v[144:147], v[40:43]\n"
"v_mfma_f32_16x16x32_bf16 v[40:43], a[12:15], v[32:35], v[40:43]\n"
"v_mfma_f32_16x16x32_bf16 v[40:43], a[16:19], v[156:159], v[40:43]\n"
"v_mfma_f32_16x16x32_bf16 v[40:43], a[20:23], v[184:187], v[40:43]\n"
"v_mfma_f32_16x16x32_bf16 v[40:43], a[24:27], v[208:211], v[40:43]\n"
"v_mfma_f32_16x16x32_bf16 v[40:43], a[28:31], v[176:179], v[40:43]\n"
"v_mfma_f32_16x16x32_bf16 v[136:139], a[60:63], v[176:179], 0\n"
"v_mfma_f32_16x16x32_bf16 v[136:139], a[56:59], v[208:211], v[136:139]\n"
"v_mfma_f32_16x16x32_bf16 v[136:139], a[52:55], v[184:187], v[136:139]\n"
"v_mfma_f32_16x16x32_bf16 v[136:139], a[48:51], v[156:159], v[136:139]\n"
"v_mfma_f32_16x16x32_bf16 v[136:139], a[44:47], v[32:35], v[136:139]\n"
"v_mfma_f32_16x16x32_bf16 v[136:139], a[40:43], v[144:147], v[136:139]\n"
"s_nop 7\n"
"s_branch .Lmk_p82r\n"
".Lmk_m85s:\n"
";\n"
"s_waitcnt vmcnt(16)\n"
"s_mov_b32 s1, 0x10000\n"
".Lmk_m85l:\n"
"s_add_u32 s98, s62, 0x4800\n"
"buffer_load_dword v255, v10, s[4:7], s98 offen sc1\n"
"buffer_load_dword v253, v10, s[4:7], s98 offen offset:256 sc1\n"
"buffer_load_dword v252, v10, s[4:7], s98 offen offset:512 sc1\n"
"buffer_load_dword v251, v10, s[4:7], s98 offen offset:768 sc1\n"
"buffer_load_dword v250, v10, s[4:7], s98 offen offset:1024 sc1\n"
"buffer_load_dword v254, v10, s[4:7], s98 offen offset:1280 sc1\n"
"buffer_load_dword v249, v10, s[4:7], s98 offen offset:1536 sc1\n"
"buffer_load_dword v248, v10, s[4:7], s98 offen offset:1792 sc1\n"
"s_waitcnt vmcnt(0)\n"
"v_max_u32_e32 v245, v255, v253\n"
"v_max3_u32 v245, v245, v252, v251\n"
"v_max3_u32 v245, v245, v250, v254\n"
"v_max3_u32 v245, v245, v249, v248\n"
"v_cmp_eq_u32_e32 vcc, -1, v245\n"
"s_cbranch_vccz .Lmk_m85k\n"
"s_sub_u32 s1, s1, 1\n"
"s_cbranch_scc0 .Lmk_m85l\n"
".Lmk_m85k:\n"
"v_add_f32_e32 v255, v255, v250\n"
"v_add_f32_e32 v253, v253, v254\n"
"v_add_f32_e32 v252, v252, v249\n"
"v_add_f32_e32 v251, v251, v248\n"
"v_add_f32_e32 v255, v255, v252\n"
"v_add_f32_e32 v253, v253, v251\n"
"v_add_f32_e32 v255, v255, v253\n"
"s_nop 1\n"
"s_branch .Lmk_m85r\n"
".Lmk_f72s:\n"
";\n"
"s_mov_b32 s1, 0x10000\n"
".Lmk_f72l:\n"
"buffer_load_dwordx2 v[246:247], v12, s[4:7], s97 offen sc1\n"
"s_waitcnt vmcnt(0)\n"
"v_cmp_ne_u64_e32 vcc, v[246:247], v[18:19]\n"
"s_cbranch_vccz .Lmk_f72r\n"
"s_sub_u32 s1, s1, 1\n"
"s_cbranch_scc0 .Lmk_f72l\n"
"s_branch .Lmk_f72r\n"
".Lmk_p83s:\n"
";\n"
"s_waitcnt vmcnt(16)\n"
"s_mov_b32 s1, 0x10000\n"
".Lmk_p83l:\n"
"s_add_u32 s97, s54, 0x48000\n"
"buffer_load_dwordx4 v[92:95], v0, s[4:7], s97 offen sc1\n"
"buffer_load_dwordx4 v[96:99], v0, s[4:7], s97 offen offset:1024 sc1\n"
"buffer_load_dwordx4 v[112:115], v0, s[4:7], s97 offen offset:2048 sc1\n"
"buffer_load_dwordx4 v[120:123], v0, s[4:7], s97 offen offset:3072 sc1\n"
"buffer_load_dwordx4 v[104:107], v1, s[4:7], s97 offen sc1\n"
"buffer_load_dwordx4 v[172:175], v1, s[4:7], s97 offen offset:1024 sc1\n"
"buffer_load_dwordx4 v[84:87], v1, s[4:7], s97 offen offset:2048 sc1\n"
"buffer_load_dwordx4 v[160:163], v1, s[4:7], s97 offen offset:3072 sc1\n"
"s_add_u32 s97, s55, 0x48000\n"
"buffer_load_dwordx4 v[68:71], v0, s[4:7], s97 offen sc1\n"
"buffer_load_dwordx4 v[124:127], v0, s[4:7], s97 offen offset:1024 sc1\n"
"buffer_load_dwordx4 v[140:143], v0, s[4:7], s97 offen offset:2048 sc1\n"
"buffer_load_dwordx4 v[148:151], v0, s[4:7], s97 offen offset:3072 sc1\n"
"buffer_load_dwordx4 v[136:139], v1, s[4:7], s97 offen sc1\n"
"buffer_load_dwordx4 v[164:167], v1, s[4:7], s97 offen offset:1024 sc1\n"
"buffer_load_dwordx4 v[168:171], v1, s[4:7], s97 offen offset:2048 sc1\n"
"buffer_load_dwordx4 v[180:183], v1, s[4:7], s97 offen offset:3072 sc1\n"
"s_waitcnt vmcnt(0)\n"
"v_max_u32_e32 v255, v92, v93\n"
"v_max3_u32 v255, v255, v94, v95\n"
"v_max3_u32 v255, v255, v96, v97\n"
"v_max3_u32 v255, v255, v98, v99\n"
"v_max3_u32 v255, v255, v112, v113\n"
"v_max3_u32 v255, v255, v114, v115\n"
"v_max3_u32 v255, v255, v120, v121\n"
"v_max3_u32 v255, v255, v122, v123\n"
"v_max3_u32 v255, v255, v104, v105\n"
"v_max3_u32 v255, v255, v106, v107\n"
"v_max3_u32 v255, v255, v172, v173\n"
"v_max3_u32 v255, v255, v174, v175\n"
"v_max3_u32 v255, v255, v84, v85\n"
"v_max3_u32 v255, v255, v86, v87\n"
"v_max3_u32 v255, v255, v160, v161\n"
"v_max3_u32 v255, v255, v162, v163\n"
"v_cmp_eq_u32_e32 vcc, -1, v255\n"
"s_cbranch_vccz .Lmk_p83k\n"
"s_sub_u32 s1, s1, 1\n"
"s_cbranch_scc0 .Lmk_p83l\n"
".Lmk_p83k:\n"
"v_mfma_f32_16x16x32_bf16 v[32:35], a[0:3], v[92:95], 0\n"
"v_mfma_f32_16x16x32_bf16 v[32:35], a[4:7], v[96:99], v[32:35]\n"
"v_mfma_f32_16x16x32_bf16 v[32:35], a[8:11], v[112:115], v[32:35]\n"
"v_mfma_f32_16x16x32_bf16 v[32:35], a[12:15], v[120:123], v[32:35]\n"
"v_mfma_f32_16x16x32_bf16 v[32:35], a[16:19], v[104:107], v[32:35]\n"
"v_mfma_f32_16x16x32_bf16 v[32:35], a[20:23], v[172:175], v[32:35]\n"
"v_mfma_f32_16x16x32_bf16 v[32:35], a[24:27], v[84:87], v[32:35]\n"
"v_mfma_f32_16x16x32_bf16 v[32:35], a[28:31], v[160:163], v[32:35]\n"
"v_mfma_f32_16x16x32_bf16 v[40:43], a[60:63], v[160:163], 0\n"
"v_mfma_f32_16x16x32_bf16 v[40:43], a[56:59], v[84:87], v[40:43]\n"
"v_mfma_f32_16x16x32_bf16 v[40:43], a[52:55], v[172:175], v[40:43]\n"
"v_mfma_f32_16x16x32_bf16 v[40:43], a[48:51], v[104:107], v[40:43]\n"
"v_mfma_f32_16x16x32_bf16 v[40:43], a[44:47], v[120:123], v[40:43]\n"
"v_mfma_f32_16x16x32_bf16 v[40:43], a[40:43], v[112:115], v[40:43]\n"
"s_nop 7\n"
"s_branch .Lmk_p83r\n"
".Lmk_p84s:\n"
";\n"
"s_waitcnt vmcnt(16)\n"
"s_mov_b32 s1, 0x10000\n"
".Lmk_p84l:\n"
"s_add_u32 s97, s55, 0x48000\n"
"buffer_load_dwordx4 v[68:71], v0, s[4:7], s97 offen sc1\n"
"buffer_load_dwordx4 v[124:127], v0, s[4:7], s97 offen offset:1024 sc1\n"
"buffer_load_dwordx4 v[140:143], v0, s[4:7], s97 offen offset:2048 sc1\n"
"buffer_load_dwordx4 v[148:151], v0, s[4:7], s97 offen offset:3072 sc1\n"
"buffer_load_dwordx4 v[136:139], v1, s[4:7], s97 offen sc1\n"
"buffer_load_dwordx4 v[164:167], v1, s[4:7], s97 offen offset:1024 sc1\n"
"buffer_load_dwordx4 v[168:171], v1, s[4:7], s97 offen offset:2048 sc1\n"
"buffer_load_dwordx4 v[180:183], v1, s[4:7], s97 offen offset:3072 sc1\n"
"s_waitcnt vmcnt(0)\n"
"v_max_u32_e32 v252, v68, v69\n"
"v_max3_u32 v252, v252, v70, v71\n"
"v_max3_u32 v252, v252, v124, v125\n"
"v_max3_u32 v252, v252, v126, v127\n"
"v_max3_u32 v252, v252, v140, v141\n"
"v_max3_u32 v252, v252, v142, v143\n"
"v_max3_u32 v252, v252, v148, v149\n"
"v_max3_u32 v252, v252, v150, v151\n"
"v_max3_u32 v252, v252, v136, v137\n"
"v_max3_u32 v252, v252, v138, v139\n"
"v_max3_u32 v252, v252, v164, v165\n"
"v_max3_u32 v252, v252, v166, v167\n"
"v_max3_u32 v252, v252, v168, v169\n"
"v_max3_u32 v252, v252, v170, v171\n"
"v_max3_u32 v252, v252, v180, v181\n"
"v_max3_u32 v252, v252, v182, v183\n"
"v_cmp_eq_u32_e32 vcc, -1, v252\n"
"s_cbranch_vccz .Lmk_p84k\n"
"s_sub_u32 s1, s1, 1\n"
"s_cbranch_scc0 .Lmk_p84l\n"
".Lmk_p84k:\n"
"v_mfma_f32_16x16x32_bf16 v[112:115], a[0:3], v[68:71], v[116:119]\n"
"v_mfma_f32_16x16x32_bf16 v[112:115], a[4:7], v[124:127], v[112:115]\n"
"v_mfma_f32_16x16x32_bf16 v[112:115], a[8:11], v[140:143], v[112:115]\n"
"v_mfma_f32_16x16x32_bf16 v[112:115], a[12:15], v[148:151], v[112:115]\n"
"v_mfma_f32_16x16x32_bf16 v[112:115], a[16:19], v[136:139], v[112:115]\n"
"v_mfma_f32_16x16x32_bf16 v[112:115], a[20:23], v[164:167], v[112:115]\n"
"v_mfma_f32_16x16x32_bf16 v[112:115], a[24:27], v[168:171], v[112:115]\n"
"v_mfma_f32_16x16x32_bf16 v[112:115], a[28:31], v[180:183], v[112:115]\n"
"v_mfma_f32_16x16x32_bf16 v[200:203], a[60:63], v[180:183], v[176:179]\n"
"v_mfma_f32_16x16x32_bf16 v[200:203], a[56:59], v[168:171], v[200:203]\n"
"v_mfma_f32_16x16x32_bf16 v[200:203], a[52:55], v[164:167], v[200:203]\n"
"v_mfma_f32_16x16x32_bf16 v[200:203], a[48:51], v[136:139], v[200:203]\n"
"v_mfma_f32_16x16x32_bf16 v[200:203], a[44:47], v[148:151], v[200:203]\n"
"v_mfma_f32_16x16x32_bf16 v[200:203], a[40:43], v[140:143], v[200:203]\n"
"s_nop 7\n"
"s_branch .Lmk_p84r\n"
".Lmk_f73s:\n"
";\n"
"s_mov_b32 s1, 0x10000\n"
".Lmk_f73l:\n"
"buffer_load_dwordx2 v[250:251], off, s[4:7], s98 sc1\n"
"s_waitcnt vmcnt(0)\n"
"v_cmp_ne_u64_e32 vcc, v[250:251], v[18:19]\n"
"s_cbranch_vccz .Lmk_f73r\n"
"s_sub_u32 s1, s1, 1\n"
"s_cbranch_scc0 .Lmk_f73l\n"
"s_branch .Lmk_f73r\n"
".Lmk_x94s:\n"
"s_nop 3\n"
"v_mov_b32_e32 v36, 0xff800000\n"
"v_mov_b32_e32 v37, 0xff800000\n"
"v_mov_b32_e32 v38, 0xff800000\n"
"v_mov_b32_e32 v39, 0xff800000\n"
"v_mov_b32_e32 v60, 0xff800000\n"
"v_mov_b32_e32 v61, 0xff800000\n"
"v_mov_b32_e32 v62, 0xff800000\n"
"v_mov_b32_e32 v63, 0xff800000\n"
"s_nop 1\n"
"s_branch .Lmk_x94r\n"
".Lmk_x95s:\n"
"s_nop 3\n"
"v_mov_b32_e32 v36, 0xff800000\n"
"v_mov_b32_e32 v37, 0xff800000\n"
"v_mov_b32_e32 v38, 0xff800000\n"
"v_mov_b32_e32 v39, 0xff800000\n"
"v_mov_b32_e32 v60, 0xff800000\n"
"v_mov_b32_e32 v61, 0xff800000\n"
"v_mov_b32_e32 v62, 0xff800000\n"
"v_mov_b32_e32 v63, 0xff800000\n"
"s_nop 1\n"
"s_branch .Lmk_x95r\n"
".Lmk_x96s:\n"
"s_nop 3\n"
"v_mov_b32_e32 v36, 0xff800000\n"
"v_mov_b32_e32 v37, 0xff800000\n"
"v_mov_b32_e32 v38, 0xff800000\n"
"v_mov_b32_e32 v39, 0xff800000\n"
"v_mov_b32_e32 v60, 0xff800000\n"
"v_mov_b32_e32 v61, 0xff800000\n"
"v_mov_b32_e32 v62, 0xff800000\n"
"v_mov_b32_e32 v63, 0xff800000\n"
"s_nop 1\n"
"s_branch .Lmk_x96r\n"
".Lmk_x97s:\n"
"s_nop 3\n"
"v_mov_b32_e32 v36, 0xff800000\n"
"v_mov_b32_e32 v37, 0xff800000\n"
"v_mov_b32_e32 v38, 0xff800000\n"
"v_mov_b32_e32 v39, 0xff800000\n"
"v_mov_b32_e32 v60, 0xff800000\n"
"v_mov_b32_e32 v61, 0xff800000\n"
"v_mov_b32_e32 v62, 0xff800000\n"
"v_mov_b32_e32 v63, 0xff800000\n"
"s_nop 1\n"
"s_branch .Lmk_x97r\n"
".Lmk_b92:\n"
"s_cmp_lg_u32 s2, 0\n"
"s_cbranch_scc1 .Lmk_k98\n"
"s_mov_b32 s2, 1\n"
"s_waitcnt vmcnt(0) lgkmcnt(0)\n"
"s_nop 7\n"
"s_mov_b32 s97, 11\n"
"v_add_u32_e32 v248, 0x3800, v15\n"
"v_add_u32_e32 v247, 0x3800, v254\n"
"ds_read_b128 v[36:39], v15 offset:24576\n"
"ds_read_b128 v[40:43], v254 offset:24832\n"
"s_waitcnt lgkmcnt(0)\n"
"v_mfma_f32_16x16x32_bf16 v[48:51], v[36:39], v[28:31], 0\n"
"v_mfma_f32_16x16x32_bf16 v[52:55], v[36:39], v[32:35], 0\n"
"v_mfma_f32_16x16x32_bf16 v[36:39], v[40:43], v[32:35], 0\n"
"v_mfma_f32_16x16x32_bf16 v[56:59], v[40:43], v[28:31], 0\n"
".Lmk_b92l:\n"
"ds_read_b128 v[40:43], v248 offset:8192\n"
"ds_read_b128 v[60:63], v247 offset:8448\n"
"s_cmp_gt_i32 s78, s97\n"
"s_cselect_b32 s96, 0xff800000, 0\n"
"v_mov_b32_e32 v64, s96\n"
"v_mov_b32_e32 v65, s96\n"
"v_mov_b32_e32 v66, s96\n"
"v_mov_b32_e32 v67, s96\n"
"v_subrev_u32_e32 v248, 0x800, v248\n"
"v_subrev_u32_e32 v247, 0x800, v247\n"
"s_waitcnt lgkmcnt(0)\n"
"v_mfma_f32_16x16x32_bf16 v[72:75], v[40:43], v[28:31], v[64:67]\n"
"v_mfma_f32_16x16x32_bf16 v[76:79], v[40:43], v[32:35], v[64:67]\n"
"v_mfma_f32_16x16x32_bf16 v[40:43], v[60:63], v[32:35], v[64:67]\n"
"v_mfma_f32_16x16x32_bf16 v[84:87], v[60:63], v[28:31], v[64:67]\n"
"s_nop 7\n"
"s_cmp_lg_u32 s97, 4\n"
"s_cbranch_scc1 .Lmk_b92m\n"
"v_cndmask_b32_e64 v72, v48, v72, s[20:21]\n"
"v_cndmask_b32_e64 v73, v49, v73, s[22:23]\n"
"v_cndmask_b32_e64 v74, v50, v74, s[24:25]\n"
"v_cndmask_b32_e64 v75, v51, v75, s[26:27]\n"
"v_cndmask_b32_e64 v84, v56, v84, s[28:29]\n"
"v_cndmask_b32_e64 v85, v57, v85, s[30:31]\n"
"v_cndmask_b32_e64 v86, v58, v86, s[32:33]\n"
"v_cndmask_b32_e64 v87, v59, v87, s[34:35]\n"
"v_cndmask_b32_e64 v76, v52, v76, s[36:37]\n"
"v_cndmask_b32_e64 v77, v53, v77, s[38:39]\n"
"v_cndmask_b32_e64 v78, v54, v78, s[40:41]\n"
"v_cndmask_b32_e64 v79, v55, v79, s[42:43]\n"
"v_cndmask_b32_e64 v40, v36, v40, s[44:45]\n"
"v_cndmask_b32_e64 v41, v37, v41, s[46:47]\n"
"v_cndmask_b32_e64 v42, v38, v42, s[48:49]\n"
"v_cndmask_b32_e64 v43, v39, v43, s[50:51]\n"
".Lmk_b92m:\n"
"v_max3_f32 v253, v253, v72, v73\n"
"v_max3_f32 v251, v251, v76, v77\n"
"v_max3_f32 v253, v253, v74, v75\n"
"v_max3_f32 v251, v251, v78, v79\n"
"v_max3_f32 v253, v253, v84, v85\n"
"v_max3_f32 v251, v251, v40, v41\n"
"v_max3_f32 v253, v253, v86, v87\n"
"v_max3_f32 v251, v251, v42, v43\n"
"s_sub_u32 s97, s97, 1\n"
"s_cmp_ge_i32 s97, 4\n"
"s_cbranch_scc1 .Lmk_b92l\n"
"v_mov_b32_e32 v248, v253\n"
"v_mov_b32_e32 v247, v251\n"
"s_nop 0\n"
"v_permlane16_swap_b32_e32 v253, v248\n"
"v_permlane16_swap_b32_e32 v251, v247\n"
"v_max_f32_e32 v253, v253, v248\n"
"v_max_f32_e32 v251, v251, v247\n"
"v_mov_b32_e32 v248, v253\n"
"v_mov_b32_e32 v247, v251\n"
"s_nop 0\n"
"v_permlane32_swap_b32_e32 v253, v248\n"
"v_permlane32_swap_b32_e32 v251, v247\n"
"v_max_f32_e32 v253, v253, v248\n"
"v_max_f32_e32 v251, v251, v247\n"
"v_mov_b32_e32 v250, v253\n"
"v_mov_b32_e32 v249, v251\n"
"s_branch .Lmk_a91\n"
".Lmk_x101s:\n"
"s_nop 3\n"
"v_mov_b32_e32 v116, 0xff800000\n"
"v_mov_b32_e32 v117, 0xff800000\n"
"v_mov_b32_e32 v118, 0xff800000\n"
"v_mov_b32_e32 v119, 0xff800000\n"
"v_mov_b32_e32 v120, 0xff800000\n"
"v_mov_b32_e32 v121, 0xff800000\n"
"v_mov_b32_e32 v122, 0xff800000\n"
"v_mov_b32_e32 v123, 0xff800000\n"
"s_nop 1\n"
"s_branch .Lmk_x101r\n"
".Lmk_x102s:\n"
"s_nop 3\n"
"v_mov_b32_e32 v116, 0xff800000\n"
"v_mov_b32_e32 v117, 0xff800000\n"
"v_mov_b32_e32 v118, 0xff800000\n"
"v_mov_b32_e32 v119, 0xff800000\n"
"v_mov_b32_e32 v120, 0xff800000\n"
"v_mov_b32_e32 v121, 0xff800000\n"
"v_mov_b32_e32 v122, 0xff800000\n"
"v_mov_b32_e32 v123, 0xff800000\n"
"s_nop 1\n"
"s_branch .Lmk_x102r\n"
".Lmk_x103s:\n"
"s_nop 3\n"
"v_mov_b32_e32 v116, 0xff800000\n"
"v_mov_b32_e32 v117, 0xff800000\n"
"v_mov_b32_e32 v118, 0xff800000\n"
"v_mov_b32_e32 v119, 0xff800000\n"
"v_mov_b32_e32 v120, 0xff800000\n"
"v_mov_b32_e32 v121, 0xff800000\n"
"v_mov_b32_e32 v122, 0xff800000\n"
"v_mov_b32_e32 v123, 0xff800000\n"
"s_nop 1\n"
"s_branch .Lmk_x103r\n"
".Lmk_h90s:\n"
";\n"
"s_waitcnt vmcnt(16)\n"
"s_mov_b32 s1, 0x10000\n"
".Lmk_h90l:\n"
"s_add_u32 m0, s77, 0x4000\n"
"s_add_u32 s95, s65, 0\n"
"buffer_load_dwordx4 v2, s[4:7], s95 offen sc1 lds\n"
"buffer_load_dwordx4 v2, s[4:7], s95 offen offset:1024 sc1 lds\n"
"s_add_u32 m0, s77, 0xc000\n"
"s_add_u32 s95, s65, 0x4000\n"
"buffer_load_dwordx4 v2, s[4:7], s95 offen sc1 lds\n"
"buffer_load_dwordx4 v2, s[4:7], s95 offen offset:1024 sc1 lds\n"
"s_waitcnt vmcnt(0)\n"
"ds_read_b128 v[164:167], v2 offset:16384\n"
"ds_read_b128 v[76:79], v2 offset:17408\n"
"ds_read_b128 v[72:75], v2 offset:49152\n"
"ds_read_b128 v[84:87], v2 offset:50176\n"
"s_waitcnt lgkmcnt(0)\n"
"s_waitcnt vmcnt(0)\n"
"v_max_u32_e32 v248, v164, v165\n"
"v_max3_u32 v248, v248, v166, v167\n"
"v_max3_u32 v248, v248, v76, v77\n"
"v_max3_u32 v248, v248, v78, v79\n"
"v_max3_u32 v248, v248, v72, v73\n"
"v_max3_u32 v248, v248, v74, v75\n"
"v_max3_u32 v248, v248, v84, v85\n"
"v_max3_u32 v248, v248, v86, v87\n"
"v_cmp_eq_u32_e32 vcc, -1, v248\n"
"s_cbranch_vccz .Lmk_h90k\n"
"s_sub_u32 s1, s1, 1\n"
"s_cbranch_scc0 .Lmk_h90l\n"
".Lmk_h90k:\n"
"s_branch .Lmk_h90r\n"
".Lmk_f74s:\n"
";\n"
"s_mov_b32 s1, 0x10000\n"
".Lmk_f74l:\n"
"buffer_load_dwordx2 v[246:247], v12, s[4:7], s96 offen sc1\n"
"s_waitcnt vmcnt(0)\n"
"v_cmp_ne_u64_e32 vcc, v[246:247], v[18:19]\n"
"s_cbranch_vccz .Lmk_f74r\n"
"s_sub_u32 s1, s1, 1\n"
"s_cbranch_scc0 .Lmk_f74l\n"
"s_branch .Lmk_f74r\n"
".Lmk_b100:\n"
"s_cmp_lg_u32 s2, 0\n"
"s_cbranch_scc1 .Lmk_k105\n"
"s_mov_b32 s2, 1\n"
"s_waitcnt vmcnt(0) lgkmcnt(0)\n"
"s_nop 7\n"
"s_mov_b32 s96, 7\n"
"v_add_u32_e32 v248, 0x3800, v15\n"
"v_add_u32_e32 v247, 0x3800, v254\n"
"ds_read_b128 v[188:191], v15 offset:16384\n"
"ds_read_b128 v[192:195], v254 offset:16640\n"
"s_waitcnt lgkmcnt(0)\n"
"v_mfma_f32_16x16x32_bf16 v[196:199], v[188:191], v[92:95], 0\n"
"v_mfma_f32_16x16x32_bf16 v[212:215], v[188:191], v[96:99], 0\n"
"v_mfma_f32_16x16x32_bf16 v[188:191], v[192:195], v[96:99], 0\n"
"v_mfma_f32_16x16x32_bf16 v[220:223], v[192:195], v[92:95], 0\n"
".Lmk_b100l:\n"
"ds_read_b128 v[192:195], v248\n"
"ds_read_b128 v[224:227], v247 offset:256\n"
"s_cmp_gt_i32 s78, s96\n"
"s_cselect_b32 s95, 0xff800000, 0\n"
"v_mov_b32_e32 v228, s95\n"
"v_mov_b32_e32 v229, s95\n"
"v_mov_b32_e32 v230, s95\n"
"v_mov_b32_e32 v231, s95\n"
"v_subrev_u32_e32 v248, 0x800, v248\n"
"v_subrev_u32_e32 v247, 0x800, v247\n"
"s_waitcnt lgkmcnt(0)\n"
"v_mfma_f32_16x16x32_bf16 v[232:235], v[192:195], v[92:95], v[228:231]\n"
"v_mfma_f32_16x16x32_bf16 v[236:239], v[192:195], v[96:99], v[228:231]\n"
"v_mfma_f32_16x16x32_bf16 v[192:195], v[224:227], v[96:99], v[228:231]\n"
"v_mfma_f32_16x16x32_bf16 v[240:243], v[224:227], v[92:95], v[228:231]\n"
"s_nop 7\n"
"s_cmp_lg_u32 s96, 0\n"
"s_cbranch_scc1 .Lmk_b100m\n"
"v_cndmask_b32_e64 v232, v196, v232, s[20:21]\n"
"v_cndmask_b32_e64 v233, v197, v233, s[22:23]\n"
"v_cndmask_b32_e64 v234, v198, v234, s[24:25]\n"
"v_cndmask_b32_e64 v235, v199, v235, s[26:27]\n"
"v_cndmask_b32_e64 v240, v220, v240, s[28:29]\n"
"v_cndmask_b32_e64 v241, v221, v241, s[30:31]\n"
"v_cndmask_b32_e64 v242, v222, v242, s[32:33]\n"
"v_cndmask_b32_e64 v243, v223, v243, s[34:35]\n"
"v_cndmask_b32_e64 v236, v212, v236, s[36:37]\n"
"v_cndmask_b32_e64 v237, v213, v237, s[38:39]\n"
"v_cndmask_b32_e64 v238, v214, v238, s[40:41]\n"
"v_cndmask_b32_e64 v239, v215, v239, s[42:43]\n"
"v_cndmask_b32_e64 v192, v188, v192, s[44:45]\n"
"v_cndmask_b32_e64 v193, v189, v193, s[46:47]\n"
"v_cndmask_b32_e64 v194, v190, v194, s[48:49]\n"
"v_cndmask_b32_e64 v195, v191, v195, s[50:51]\n"
".Lmk_b100m:\n"
"v_max3_f32 v253, v253, v232, v233\n"
"v_max3_f32 v251, v251, v236, v237\n"
"v_max3_f32 v253, v253, v234, v235\n"
"v_max3_f32 v251, v251, v238, v239\n"
"v_max3_f32 v253, v253, v240, v241\n"
"v_max3_f32 v251, v251, v192, v193\n"
"v_max3_f32 v253, v253, v242, v243\n"
"v_max3_f32 v251, v251, v194, v195\n"
"s_sub_u32 s96, s96, 1\n"
"s_cmp_ge_i32 s96, 0\n"
"s_cbranch_scc1 .Lmk_b100l\n"
"v_mov_b32_e32 v248, v253\n"
"v_mov_b32_e32 v247, v251\n"
"s_nop 0\n"
"v_permlane16_swap_b32_e32 v253, v248\n"
"v_permlane16_swap_b32_e32 v251, v247\n"
"v_max_f32_e32 v253, v253, v248\n"
"v_max_f32_e32 v251, v251, v247\n"
"v_mov_b32_e32 v248, v253\n"
"v_mov_b32_e32 v247, v251\n"
"s_nop 0\n"
"v_permlane32_swap_b32_e32 v253, v248\n"
"v_permlane32_swap_b32_e32 v251, v247\n"
"v_max_f32_e32 v253, v253, v248\n"
"v_max_f32_e32 v251, v251, v247\n"
"v_mov_b32_e32 v250, v253\n"
"v_mov_b32_e32 v249, v251\n"
"s_branch .Lmk_a99\n"
".Lmk_p106s:\n"
";\n"
"s_waitcnt vmcnt(16)\n"
"s_mov_b32 s1, 0x10000\n"
".Lmk_p106l:\n"
"s_add_u32 s98, s56, 0x40000\n"
"buffer_load_dwordx4 v[152:155], v0, s[4:7], s98 offen sc1\n"
"buffer_load_dwordx4 v[200:203], v0, s[4:7], s98 offen offset:1024 sc1\n"
"buffer_load_dwordx4 v[184:187], v0, s[4:7], s98 offen offset:2048 sc1\n"
"buffer_load_dwordx4 v[204:207], v0, s[4:7], s98 offen offset:3072 sc1\n"
"buffer_load_dwordx4 v[140:143], v1, s[4:7], s98 offen sc1\n"
"buffer_load_dwordx4 v[208:211], v1, s[4:7], s98 offen offset:1024 sc1\n"
"buffer_load_dwordx4 v[120:123], v1, s[4:7], s98 offen offset:2048 sc1\n"
"buffer_load_dwordx4 v[136:139], v1, s[4:7], s98 offen offset:3072 sc1\n"
"s_add_u32 s98, s57, 0x40000\n"
"buffer_load_dwordx4 v[216:219], v0, s[4:7], s98 offen sc1\n"
"buffer_load_dwordx4 v[132:135], v0, s[4:7], s98 offen offset:1024 sc1\n"
"buffer_load_dwordx4 v[160:163], v0, s[4:7], s98 offen offset:2048 sc1\n"
"buffer_load_dwordx4 v[100:103], v0, s[4:7], s98 offen offset:3072 sc1\n"
"buffer_load_dwordx4 v[164:167], v1, s[4:7], s98 offen sc1\n"
"buffer_load_dwordx4 v[104:107], v1, s[4:7], s98 offen offset:1024 sc1\n"
"buffer_load_dwordx4 v[108:111], v1, s[4:7], s98 offen offset:2048 sc1\n"
"buffer_load_dwordx4 v[128:131], v1, s[4:7], s98 offen offset:3072 sc1\n"
"s_add_u32 s98, s58, 0x40000\n"
"buffer_load_dwordx4 v[148:151], v0, s[4:7], s98 offen sc1\n"
"buffer_load_dwordx4 v[144:147], v0, s[4:7], s98 offen offset:1024 sc1\n"
"buffer_load_dwordx4 v[116:119], v0, s[4:7], s98 offen offset:2048 sc1\n"
"buffer_load_dwordx4 v[112:115], v0, s[4:7], s98 offen offset:3072 sc1\n"
"buffer_load_dwordx4 v[92:95], v1, s[4:7], s98 offen sc1\n"
"buffer_load_dwordx4 v[96:99], v1, s[4:7], s98 offen offset:1024 sc1\n"
"buffer_load_dwordx4 v[156:159], v1, s[4:7], s98 offen offset:2048 sc1\n"
"s_waitcnt vmcnt(0)\n"
"v_max_u32_e32 v255, v152, v153\n"
"v_max3_u32 v255, v255, v154, v155\n"
"v_max3_u32 v255, v255, v200, v201\n"
"v_max3_u32 v255, v255, v202, v203\n"
"v_max3_u32 v255, v255, v184, v185\n"
"v_max3_u32 v255, v255, v186, v187\n"
"v_max3_u32 v255, v255, v204, v205\n"
"v_max3_u32 v255, v255, v206, v207\n"
"v_max3_u32 v255, v255, v140, v141\n"
"v_max3_u32 v255, v255, v142, v143\n"
"v_max3_u32 v255, v255, v208, v209\n"
"v_max3_u32 v255, v255, v210, v211\n"
"v_max3_u32 v255, v255, v120, v121\n"
"v_max3_u32 v255, v255, v122, v123\n"
"v_max3_u32 v255, v255, v136, v137\n"
"v_max3_u32 v255, v255, v138, v139\n"
"v_cmp_eq_u32_e32 vcc, -1, v255\n"
"s_cbranch_vccz .Lmk_p106k\n"
"s_sub_u32 s1, s1, 1\n"
"s_cbranch_scc0 .Lmk_p106l\n"
".Lmk_p106k:\n"
"v_mfma_f32_16x16x32_bf16 v[88:91], a[128:131], v[152:155], 0\n"
"v_mfma_f32_16x16x32_bf16 v[88:91], a[132:135], v[200:203], v[88:91]\n"
"v_mfma_f32_16x16x32_bf16 v[88:91], a[136:139], v[184:187], v[88:91]\n"
"v_mfma_f32_16x16x32_bf16 v[88:91], a[140:143], v[204:207], v[88:91]\n"
"v_mfma_f32_16x16x32_bf16 v[88:91], a[144:147], v[140:143], v[88:91]\n"
"v_mfma_f32_16x16x32_bf16 v[88:91], a[148:151], v[208:211], v[88:91]\n"
"v_mfma_f32_16x16x32_bf16 v[88:91], a[152:155], v[120:123], v[88:91]\n"
"v_mfma_f32_16x16x32_bf16 v[88:91], a[156:159], v[136:139], v[88:91]\n"
"v_mfma_f32_16x16x32_bf16 v[124:127], a[188:191], v[136:139], 0\n"
"v_mfma_f32_16x16x32_bf16 v[124:127], a[184:187], v[120:123], v[124:127]\n"
"v_mfma_f32_16x16x32_bf16 v[124:127], a[180:183], v[208:211], v[124:127]\n"
"v_mfma_f32_16x16x32_bf16 v[124:127], a[176:179], v[140:143], v[124:127]\n"
"v_mfma_f32_16x16x32_bf16 v[124:127], a[172:175], v[204:207], v[124:127]\n"
"v_mfma_f32_16x16x32_bf16 v[124:127], a[168:171], v[184:187], v[124:127]\n"
"s_nop 7\n"
"s_branch .Lmk_p106r\n"
".Lmk_p107s:\n"
";\n"
"s_waitcnt vmcnt(16)\n"
"s_mov_b32 s1, 0x10000\n"
".Lmk_p107l:\n"
"s_add_u32 s97, s57, 0x40000\n"
"buffer_load_dwordx4 v[216:219], v0, s[4:7], s97 offen sc1\n"
"buffer_load_dwordx4 v[132:135], v0, s[4:7], s97 offen offset:1024 sc1\n"
"buffer_load_dwordx4 v[160:163], v0, s[4:7], s97 offen offset:2048 sc1\n"
"buffer_load_dwordx4 v[100:103], v0, s[4:7], s97 offen offset:3072 sc1\n"
"buffer_load_dwordx4 v[164:167], v1, s[4:7], s97 offen sc1\n"
"buffer_load_dwordx4 v[104:107], v1, s[4:7], s97 offen offset:1024 sc1\n"
"buffer_load_dwordx4 v[108:111], v1, s[4:7], s97 offen offset:2048 sc1\n"
"buffer_load_dwordx4 v[128:131], v1, s[4:7], s97 offen offset:3072 sc1\n"
"s_add_u32 s97, s58, 0x40000\n"
"buffer_load_dwordx4 v[148:151], v0, s[4:7], s97 offen sc1\n"
"buffer_load_dwordx4 v[144:147], v0, s[4:7], s97 offen offset:1024 sc1\n"
"buffer_load_dwordx4 v[116:119], v0, s[4:7], s97 offen offset:2048 sc1\n"
"buffer_load_dwordx4 v[112:115], v0, s[4:7], s97 offen offset:3072 sc1\n"
"buffer_load_dwordx4 v[92:95], v1, s[4:7], s97 offen sc1\n"
"buffer_load_dwordx4 v[96:99], v1, s[4:7], s97 offen offset:1024 sc1\n"
"buffer_load_dwordx4 v[156:159], v1, s[4:7], s97 offen offset:2048 sc1\n"
"buffer_load_dwordx4 v[88:91], v1, s[4:7], s97 offen offset:3072 sc1\n"
"s_add_u32 s97, s59, 0x40000\n"
"buffer_load_dwordx4 v[172:175], v0, s[4:7], s97 offen sc1\n"
"buffer_load_dwordx4 v[124:127], v0, s[4:7], s97 offen offset:1024 sc1\n"
"buffer_load_dwordx4 v[120:123], v0, s[4:7], s97 offen offset:2048 sc1\n"
"buffer_load_dwordx4 v[136:139], v0, s[4:7], s97 offen offset:3072 sc1\n"
"buffer_load_dwordx4 v[152:155], v1, s[4:7], s97 offen sc1\n"
"buffer_load_dwordx4 v[168:171], v1, s[4:7], s97 offen offset:1024 sc1\n"
"buffer_load_dwordx4 v[180:183], v1, s[4:7], s97 offen offset:2048 sc1\n"
"s_waitcnt vmcnt(0)\n"
"v_max_u32_e32 v255, v216, v217\n"
"v_max3_u32 v255, v255, v218, v219\n"
"v_max3_u32 v255, v255, v132, v133\n"
"v_max3_u32 v255, v255, v134, v135\n"
"v_max3_u32 v255, v255, v160, v161\n"
"v_max3_u32 v255, v255, v162, v163\n"
"v_max3_u32 v255, v255, v100, v101\n"
"v_max3_u32 v255, v255, v102, v103\n"
"v_max3_u32 v255, v255, v164, v165\n"
"v_max3_u32 v255, v255, v166, v167\n"
"v_max3_u32 v255, v255, v104, v105\n"
"v_max3_u32 v255, v255, v106, v107\n"
"v_max3_u32 v255, v255, v108, v109\n"
"v_max3_u32 v255, v255, v110, v111\n"
"v_max3_u32 v255, v255, v128, v129\n"
"v_max3_u32 v255, v255, v130, v131\n"
"v_cmp_eq_u32_e32 vcc, -1, v255\n"
"s_cbranch_vccz .Lmk_p107k\n"
"s_sub_u32 s1, s1, 1\n"
"s_cbranch_scc0 .Lmk_p107l\n"
".Lmk_p107k:\n"
"v_mfma_f32_16x16x32_bf16 v[140:143], a[128:131], v[216:219], 0\n"
"v_mfma_f32_16x16x32_bf16 v[140:143], a[132:135], v[132:135], v[140:143]\n"
"v_mfma_f32_16x16x32_bf16 v[140:143], a[136:139], v[160:163], v[140:143]\n"
"v_mfma_f32_16x16x32_bf16 v[140:143], a[140:143], v[100:103], v[140:143]\n"
"v_mfma_f32_16x16x32_bf16 v[140:143], a[144:147], v[164:167], v[140:143]\n"
"v_mfma_f32_16x16x32_bf16 v[140:143], a[148:151], v[104:107], v[140:143]\n"
"v_mfma_f32_16x16x32_bf16 v[140:143], a[152:155], v[108:111], v[140:143]\n"
"v_mfma_f32_16x16x32_bf16 v[140:143], a[156:159], v[128:131], v[140:143]\n"
"v_mfma_f32_16x16x32_bf16 v[176:179], a[188:191], v[128:131], 0\n"
"v_mfma_f32_16x16x32_bf16 v[176:179], a[184:187], v[108:111], v[176:179]\n"
"v_mfma_f32_16x16x32_bf16 v[176:179], a[180:183], v[104:107], v[176:179]\n"
"v_mfma_f32_16x16x32_bf16 v[176:179], a[176:179], v[164:167], v[176:179]\n"
"v_mfma_f32_16x16x32_bf16 v[176:179], a[172:175], v[100:103], v[176:179]\n"
"v_mfma_f32_16x16x32_bf16 v[176:179], a[168:171], v[160:163], v[176:179]\n"
"s_nop 7\n"
"s_branch .Lmk_p107r\n"
".Lmk_p108s:\n"
";\n"
"s_waitcnt vmcnt(16)\n"
"s_mov_b32 s1, 0x10000\n"
".Lmk_p108l:\n"
"s_add_u32 s97, s58, 0x40000\n"
"buffer_load_dwordx4 v[148:151], v0, s[4:7], s97 offen sc1\n"
"buffer_load_dwordx4 v[144:147], v0, s[4:7], s97 offen offset:1024 sc1\n"
"buffer_load_dwordx4 v[116:119], v0, s[4:7], s97 offen offset:2048 sc1\n"
"buffer_load_dwordx4 v[112:115], v0, s[4:7], s97 offen offset:3072 sc1\n"
"buffer_load_dwordx4 v[92:95], v1, s[4:7], s97 offen sc1\n"
"buffer_load_dwordx4 v[96:99], v1, s[4:7], s97 offen offset:1024 sc1\n"
"buffer_load_dwordx4 v[156:159], v1, s[4:7], s97 offen offset:2048 sc1\n"
"buffer_load_dwordx4 v[88:91], v1, s[4:7], s97 offen offset:3072 sc1\n"
"s_add_u32 s97, s59, 0x40000\n"
"buffer_load_dwordx4 v[172:175], v0, s[4:7], s97 offen sc1\n"
"buffer_load_dwordx4 v[124:127], v0, s[4:7], s97 offen offset:1024 sc1\n"
"buffer_load_dwordx4 v[120:123], v0, s[4:7], s97 offen offset:2048 sc1\n"
"buffer_load_dwordx4 v[136:139], v0, s[4:7], s97 offen offset:3072 sc1\n"
"buffer_load_dwordx4 v[152:155], v1, s[4:7], s97 offen sc1\n"
"buffer_load_dwordx4 v[168:171], v1, s[4:7], s97 offen offset:1024 sc1\n"
"buffer_load_dwordx4 v[180:183], v1, s[4:7], s97 offen offset:2048 sc1\n"
"buffer_load_dwordx4 v[140:143], v1, s[4:7], s97 offen offset:3072 sc1\n"
"s_waitcnt vmcnt(0)\n"
"v_max_u32_e32 v239, v148, v149\n"
"v_max3_u32 v239, v239, v150, v151\n"
"v_max3_u32 v239, v239, v144, v145\n"
"v_max3_u32 v239, v239, v146, v147\n"
"v_max3_u32 v239, v239, v116, v117\n"
"v_max3_u32 v239, v239, v118, v119\n"
"v_max3_u32 v239, v239, v112, v113\n"
"v_max3_u32 v239, v239, v114, v115\n"
"v_max3_u32 v239, v239, v92, v93\n"
"v_max3_u32 v239, v239, v94, v95\n"
"v_max3_u32 v239, v239, v96, v97\n"
"v_max3_u32 v239, v239, v98, v99\n"
"v_max3_u32 v239, v239, v156, v157\n"
"v_max3_u32 v239, v239, v158, v159\n"
"v_max3_u32 v239, v239, v88, v89\n"
"v_max3_u32 v239, v239, v90, v91\n"
"v_cmp_eq_u32_e32 vcc, -1, v239\n"
"s_cbranch_vccz .Lmk_p108k\n"
"s_sub_u32 s1, s1, 1\n"
"s_cbranch_scc0 .Lmk_p108l\n"
".Lmk_p108k:\n"
"v_mfma_f32_16x16x32_bf16 v[100:103], a[128:131], v[148:151], 0\n"
"v_mfma_f32_16x16x32_bf16 v[100:103], a[132:135], v[144:147], v[100:103]\n"
"v_mfma_f32_16x16x32_bf16 v[100:103], a[136:139], v[116:119], v[100:103]\n"
"v_mfma_f32_16x16x32_bf16 v[100:103], a[140:143], v[112:115], v[100:103]\n"
"v_mfma_f32_16x16x32_bf16 v[100:103], a[144:147], v[92:95], v[100:103]\n"
"v_mfma_f32_16x16x32_bf16 v[100:103], a[148:151], v[96:99], v[100:103]\n"
"v_mfma_f32_16x16x32_bf16 v[100:103], a[152:155], v[156:159], v[100:103]\n"
"v_mfma_f32_16x16x32_bf16 v[100:103], a[156:159], v[88:91], v[100:103]\n"
"v_mfma_f32_16x16x32_bf16 v[104:107], a[188:191], v[88:91], 0\n"
"v_mfma_f32_16x16x32_bf16 v[104:107], a[184:187], v[156:159], v[104:107]\n"
"v_mfma_f32_16x16x32_bf16 v[104:107], a[180:183], v[96:99], v[104:107]\n"
"v_mfma_f32_16x16x32_bf16 v[104:107], a[176:179], v[92:95], v[104:107]\n"
"v_mfma_f32_16x16x32_bf16 v[104:107], a[172:175], v[112:115], v[104:107]\n"
"v_mfma_f32_16x16x32_bf16 v[104:107], a[168:171], v[116:119], v[104:107]\n"
"s_nop 7\n"
"s_branch .Lmk_p108r\n"
".Lmk_p109s:\n"
";\n"
"s_waitcnt vmcnt(16)\n"
"s_mov_b32 s1, 0x10000\n"
".Lmk_p109l:\n"
"s_add_u32 s97, s59, 0x40000\n"
"buffer_load_dwordx4 v[172:175], v0, s[4:7], s97 offen sc1\n"
"buffer_load_dwordx4 v[124:127], v0, s[4:7], s97 offen offset:1024 sc1\n"
"buffer_load_dwordx4 v[120:123], v0, s[4:7], s97 offen offset:2048 sc1\n"
"buffer_load_dwordx4 v[136:139], v0, s[4:7], s97 offen offset:3072 sc1\n"
"buffer_load_dwordx4 v[152:155], v1, s[4:7], s97 offen sc1\n"
"buffer_load_dwordx4 v[168:171], v1, s[4:7], s97 offen offset:1024 sc1\n"
"buffer_load_dwordx4 v[180:183], v1, s[4:7], s97 offen offset:2048 sc1\n"
"buffer_load_dwordx4 v[140:143], v1, s[4:7], s97 offen offset:3072 sc1\n"
"s_add_u32 s97, s56, 0\n"
"buffer_load_dwordx4 v[100:103], v0, s[4:7], s97 offen sc1\n"
"buffer_load_dwordx4 v[176:179], v0, s[4:7], s97 offen offset:1024 sc1\n"
"buffer_load_dwordx4 v[104:107], v0, s[4:7], s97 offen offset:2048 sc1\n"
"buffer_load_dwordx4 v[88:91], v0, s[4:7], s97 offen offset:3072 sc1\n"
"buffer_load_dwordx4 v[92:95], v1, s[4:7], s97 offen sc1\n"
"buffer_load_dwordx4 v[116:119], v1, s[4:7], s97 offen offset:1024 sc1\n"
"buffer_load_dwordx4 v[132:135], v1, s[4:7], s97 offen offset:2048 sc1\n"
"buffer_load_dwordx4 v[144:147], v1, s[4:7], s97 offen offset:3072 sc1\n"
"s_add_u32 s97, s57, 0\n"
"buffer_load_dwordx4 v[156:159], v0, s[4:7], s97 offen sc1\n"
"s_waitcnt vmcnt(0)\n"
"v_max_u32_e32 v255, v172, v173\n"
"v_max3_u32 v255, v255, v174, v175\n"
"v_max3_u32 v255, v255, v124, v125\n"
"v_max3_u32 v255, v255, v126, v127\n"
"v_max3_u32 v255, v255, v120, v121\n"
"v_max3_u32 v255, v255, v122, v123\n"
"v_max3_u32 v255, v255, v136, v137\n"
"v_max3_u32 v255, v255, v138, v139\n"
"v_max3_u32 v255, v255, v152, v153\n"
"v_max3_u32 v255, v255, v154, v155\n"
"v_max3_u32 v255, v255, v168, v169\n"
"v_max3_u32 v255, v255, v170, v171\n"
"v_max3_u32 v255, v255, v180, v181\n"
"v_max3_u32 v255, v255, v182, v183\n"
"v_max3_u32 v255, v255, v140, v141\n"
"v_max3_u32 v255, v255, v142, v143\n"
"v_cmp_eq_u32_e32 vcc, -1, v255\n"
"s_cbranch_vccz .Lmk_p109k\n"
"s_sub_u32 s1, s1, 1\n"
"s_cbranch_scc0 .Lmk_p109l\n"
".Lmk_p109k:\n"
"v_mfma_f32_16x16x32_bf16 v[128:131], a[128:131], v[172:175], v[108:111]\n"
"v_mfma_f32_16x16x32_bf16 v[128:131], a[132:135], v[124:127], v[128:131]\n"
"v_mfma_f32_16x16x32_bf16 v[128:131], a[136:139], v[120:123], v[128:131]\n"
"v_mfma_f32_16x16x32_bf16 v[128:131], a[140:143], v[136:139], v[128:131]\n"
"v_mfma_f32_16x16x32_bf16 v[128:131], a[144:147], v[152:155], v[128:131]\n"
"v_mfma_f32_16x16x32_bf16 v[128:131], a[148:151], v[168:171], v[128:131]\n"
"v_mfma_f32_16x16x32_bf16 v[128:131], a[152:155], v[180:183], v[128:131]\n"
"v_mfma_f32_16x16x32_bf16 v[128:131], a[156:159], v[140:143], v[128:131]\n"
"v_mfma_f32_16x16x32_bf16 v[148:151], a[188:191], v[140:143], v[160:163]\n"
"v_mfma_f32_16x16x32_bf16 v[148:151], a[184:187], v[180:183], v[148:151]\n"
"v_mfma_f32_16x16x32_bf16 v[148:151], a[180:183], v[168:171], v[148:151]\n"
"v_mfma_f32_16x16x32_bf16 v[148:151], a[176:179], v[152:155], v[148:151]\n"
"v_mfma_f32_16x16x32_bf16 v[148:151], a[172:175], v[136:139], v[148:151]\n"
"v_mfma_f32_16x16x32_bf16 v[148:151], a[168:171], v[120:123], v[148:151]\n"
"s_nop 7\n"
"s_branch .Lmk_p109r\n"
".Lmk_p110s:\n"
";\n"
"s_waitcnt vmcnt(16)\n"
"s_mov_b32 s1, 0x10000\n"
".Lmk_p110l:\n"
"s_add_u32 s95, s56, 0\n"
"buffer_load_dwordx4 v[100:103], v0, s[4:7], s95 offen sc1\n"
"buffer_load_dwordx4 v[176:179], v0, s[4:7], s95 offen offset:1024 sc1\n"
"buffer_load_dwordx4 v[104:107], v0, s[4:7], s95 offen offset:2048 sc1\n"
"buffer_load_dwordx4 v[88:91], v0, s[4:7], s95 offen offset:3072 sc1\n"
"buffer_load_dwordx4 v[92:95], v1, s[4:7], s95 offen sc1\n"
"buffer_load_dwordx4 v[116:119], v1, s[4:7], s95 offen offset:1024 sc1\n"
"buffer_load_dwordx4 v[132:135], v1, s[4:7], s95 offen offset:2048 sc1\n"
"buffer_load_dwordx4 v[144:147], v1, s[4:7], s95 offen offset:3072 sc1\n"
"s_add_u32 s95, s57, 0\n"
"buffer_load_dwordx4 v[156:159], v0, s[4:7], s95 offen sc1\n"
"buffer_load_dwordx4 v[108:111], v0, s[4:7], s95 offen offset:1024 sc1\n"
"buffer_load_dwordx4 v[112:115], v0, s[4:7], s95 offen offset:2048 sc1\n"
"buffer_load_dwordx4 v[196:199], v0, s[4:7], s95 offen offset:3072 sc1\n"
"buffer_load_dwordx4 v[200:203], v1, s[4:7], s95 offen sc1\n"
"buffer_load_dwordx4 v[128:131], v1, s[4:7], s95 offen offset:1024 sc1\n"
"buffer_load_dwordx4 v[120:123], v1, s[4:7], s95 offen offset:2048 sc1\n"
"buffer_load_dwordx4 v[136:139], v1, s[4:7], s95 offen offset:3072 sc1\n"
"s_add_u32 s95, s58, 0\n"
"buffer_load_dwordx4 v[148:151], v0, s[4:7], s95 offen sc1\n"
"buffer_load_dwordx4 v[160:163], v0, s[4:7], s95 offen offset:1024 sc1\n"
"buffer_load_dwordx4 v[124:127], v0, s[4:7], s95 offen offset:2048 sc1\n"
"s_waitcnt vmcnt(0)\n"
"v_max_u32_e32 v255, v100, v101\n"
"v_max3_u32 v255, v255, v102, v103\n"
"v_max3_u32 v255, v255, v176, v177\n"
"v_max3_u32 v255, v255, v178, v179\n"
"v_max3_u32 v255, v255, v104, v105\n"
"v_max3_u32 v255, v255, v106, v107\n"
"v_max3_u32 v255, v255, v88, v89\n"
"v_max3_u32 v255, v255, v90, v91\n"
"v_max3_u32 v255, v255, v92, v93\n"
"v_max3_u32 v255, v255, v94, v95\n"
"v_max3_u32 v255, v255, v116, v117\n"
"v_max3_u32 v255, v255, v118, v119\n"
"v_max3_u32 v255, v255, v132, v133\n"
"v_max3_u32 v255, v255, v134, v135\n"
"v_max3_u32 v255, v255, v144, v145\n"
"v_max3_u32 v255, v255, v146, v147\n"
"v_cmp_eq_u32_e32 vcc, -1, v255\n"
"s_cbranch_vccz .Lmk_p110k\n"
"s_sub_u32 s1, s1, 1\n"
"s_cbranch_scc0 .Lmk_p110l\n"
".Lmk_p110k:\n"
"v_mfma_f32_16x16x32_bf16 v[140:143], a[128:131], v[100:103], 0\n"
"v_mfma_f32_16x16x32_bf16 v[140:143], a[132:135], v[176:179], v[140:143]\n"
"v_mfma_f32_16x16x32_bf16 v[140:143], a[136:139], v[104:107], v[140:143]\n"
"v_mfma_f32_16x16x32_bf16 v[140:143], a[140:143], v[88:91], v[140:143]\n"
"v_mfma_f32_16x16x32_bf16 v[140:143], a[144:147], v[92:95], v[140:143]\n"
"v_mfma_f32_16x16x32_bf16 v[140:143], a[148:151], v[116:119], v[140:143]\n"
"v_mfma_f32_16x16x32_bf16 v[140:143], a[152:155], v[132:135], v[140:143]\n"
"v_mfma_f32_16x16x32_bf16 v[140:143], a[156:159], v[144:147], v[140:143]\n"
"v_mfma_f32_16x16x32_bf16 v[96:99], a[188:191], v[144:147], 0\n"
"v_mfma_f32_16x16x32_bf16 v[96:99], a[184:187], v[132:135], v[96:99]\n"
"v_mfma_f32_16x16x32_bf16 v[96:99], a[180:183], v[116:119], v[96:99]\n"
"v_mfma_f32_16x16x32_bf16 v[96:99], a[176:179], v[92:95], v[96:99]\n"
"v_mfma_f32_16x16x32_bf16 v[96:99], a[172:175], v[88:91], v[96:99]\n"
"v_mfma_f32_16x16x32_bf16 v[96:99], a[168:171], v[104:107], v[96:99]\n"
"s_nop 7\n"
"s_branch .Lmk_p110r\n"
".Lmk_p111s:\n"
";\n"
"s_waitcnt vmcnt(16)\n"
"s_mov_b32 s1, 0x10000\n"
".Lmk_p111l:\n"
"s_add_u32 s97, s57, 0\n"
"buffer_load_dwordx4 v[156:159], v0, s[4:7], s97 offen sc1\n"
"buffer_load_dwordx4 v[108:111], v0, s[4:7], s97 offen offset:1024 sc1\n"
"buffer_load_dwordx4 v[112:115], v0, s[4:7], s97 offen offset:2048 sc1\n"
"buffer_load_dwordx4 v[196:199], v0, s[4:7], s97 offen offset:3072 sc1\n"
"buffer_load_dwordx4 v[200:203], v1, s[4:7], s97 offen sc1\n"
"buffer_load_dwordx4 v[128:131], v1, s[4:7], s97 offen offset:1024 sc1\n"
"buffer_load_dwordx4 v[120:123], v1, s[4:7], s97 offen offset:2048 sc1\n"
"buffer_load_dwordx4 v[136:139], v1, s[4:7], s97 offen offset:3072 sc1\n"
"s_add_u32 s97, s58, 0\n"
"buffer_load_dwordx4 v[148:151], v0, s[4:7], s97 offen sc1\n"
"buffer_load_dwordx4 v[160:163], v0, s[4:7], s97 offen offset:1024 sc1\n"
"buffer_load_dwordx4 v[124:127], v0, s[4:7], s97 offen offset:2048 sc1\n"
"buffer_load_dwordx4 v[140:143], v0, s[4:7], s97 offen offset:3072 sc1\n"
"buffer_load_dwordx4 v[152:155], v1, s[4:7], s97 offen sc1\n"
"buffer_load_dwordx4 v[172:175], v1, s[4:7], s97 offen offset:1024 sc1\n"
"buffer_load_dwordx4 v[116:119], v1, s[4:7], s97 offen offset:2048 sc1\n"
"buffer_load_dwordx4 v[132:135], v1, s[4:7], s97 offen offset:3072 sc1\n"
"s_add_u32 s97, s59, 0\n"
"buffer_load_dwordx4 v[176:179], v0, s[4:7], s97 offen sc1\n"
"buffer_load_dwordx4 v[180:183], v0, s[4:7], s97 offen offset:1024 sc1\n"
"buffer_load_dwordx4 v[168:171], v0, s[4:7], s97 offen offset:2048 sc1\n"
"s_waitcnt vmcnt(0)\n"
"v_max_u32_e32 v255, v156, v157\n"
"v_max3_u32 v255, v255, v158, v159\n"
"v_max3_u32 v255, v255, v108, v109\n"
"v_max3_u32 v255, v255, v110, v111\n"
"v_max3_u32 v255, v255, v112, v113\n"
"v_max3_u32 v255, v255, v114, v115\n"
"v_max3_u32 v255, v255, v196, v197\n"
"v_max3_u32 v255, v255, v198, v199\n"
"v_max3_u32 v255, v255, v200, v201\n"
"v_max3_u32 v255, v255, v202, v203\n"
"v_max3_u32 v255, v255, v128, v129\n"
"v_max3_u32 v255, v255, v130, v131\n"
"v_max3_u32 v255, v255, v120, v121\n"
"v_max3_u32 v255, v255, v122, v123\n"
"v_max3_u32 v255, v255, v136, v137\n"
"v_max3_u32 v255, v255, v138, v139\n"
"v_cmp_eq_u32_e32 vcc, -1, v255\n"
"s_cbranch_vccz .Lmk_p111k\n"
"s_sub_u32 s1, s1, 1\n"
"s_cbranch_scc0 .Lmk_p111l\n"
".Lmk_p111k:\n"
"v_mfma_f32_16x16x32_bf16 v[88:91], a[128:131], v[156:159], 0\n"
"v_mfma_f32_16x16x32_bf16 v[88:91], a[132:135], v[108:111], v[88:91]\n"
"v_mfma_f32_16x16x32_bf16 v[88:91], a[136:139], v[112:115], v[88:91]\n"
"v_mfma_f32_16x16x32_bf16 v[88:91], a[140:143], v[196:199], v[88:91]\n"
"v_mfma_f32_16x16x32_bf16 v[88:91], a[144:147], v[200:203], v[88:91]\n"
"v_mfma_f32_16x16x32_bf16 v[88:91], a[148:151], v[128:131], v[88:91]\n"
"v_mfma_f32_16x16x32_bf16 v[88:91], a[152:155], v[120:123], v[88:91]\n"
"v_mfma_f32_16x16x32_bf16 v[88:91], a[156:159], v[136:139], v[88:91]\n"
"v_mfma_f32_16x16x32_bf16 v[92:95], a[188:191], v[136:139], 0\n"
"v_mfma_f32_16x16x32_bf16 v[92:95], a[184:187], v[120:123], v[92:95]\n"
"v_mfma_f32_16x16x32_bf16 v[92:95], a[180:183], v[128:131], v[92:95]\n"
"v_mfma_f32_16x16x32_bf16 v[92:95], a[176:179], v[200:203], v[92:95]\n"
"v_mfma_f32_16x16x32_bf16 v[92:95], a[172:175], v[196:199], v[92:95]\n"
"v_mfma_f32_16x16x32_bf16 v[92:95], a[168:171], v[112:115], v[92:95]\n"
"s_nop 7\n"
"s_branch .Lmk_p111r\n"
".Lmk_p112s:\n"
";\n"
"s_waitcnt vmcnt(16)\n"
"s_mov_b32 s1, 0x10000\n"
".Lmk_p112l:\n"
"s_add_u32 s97, s58, 0\n"
"buffer_load_dwordx4 v[148:151], v0, s[4:7], s97 offen sc1\n"
"buffer_load_dwordx4 v[160:163], v0, s[4:7], s97 offen offset:1024 sc1\n"
"buffer_load_dwordx4 v[124:127], v0, s[4:7], s97 offen offset:2048 sc1\n"
"buffer_load_dwordx4 v[140:143], v0, s[4:7], s97 offen offset:3072 sc1\n"
"buffer_load_dwordx4 v[152:155], v1, s[4:7], s97 offen sc1\n"
"buffer_load_dwordx4 v[172:175], v1, s[4:7], s97 offen offset:1024 sc1\n"
"buffer_load_dwordx4 v[116:119], v1, s[4:7], s97 offen offset:2048 sc1\n"
"buffer_load_dwordx4 v[132:135], v1, s[4:7], s97 offen offset:3072 sc1\n"
"s_add_u32 s97, s59, 0\n"
"buffer_load_dwordx4 v[176:179], v0, s[4:7], s97 offen sc1\n"
"buffer_load_dwordx4 v[180:183], v0, s[4:7], s97 offen offset:1024 sc1\n"
"buffer_load_dwordx4 v[168:171], v0, s[4:7], s97 offen offset:2048 sc1\n"
"buffer_load_dwordx4 v[184:187], v0, s[4:7], s97 offen offset:3072 sc1\n"
"buffer_load_dwordx4 v[188:191], v1, s[4:7], s97 offen sc1\n"
"buffer_load_dwordx4 v[164:167], v1, s[4:7], s97 offen offset:1024 sc1\n"
"buffer_load_dwordx4 v[192:195], v1, s[4:7], s97 offen offset:2048 sc1\n"
"buffer_load_dwordx4 v[196:199], v1, s[4:7], s97 offen offset:3072 sc1\n"
"s_add_u32 s97, s52, 0x440000\n"
"buffer_load_dwordx4 v[88:91], v0, s[4:7], s97 offen sc1\n"
"buffer_load_dwordx4 v[92:95], v0, s[4:7], s97 offen offset:1024 sc1\n"
"s_waitcnt vmcnt(0)\n"
"v_max_u32_e32 v239, v148, v149\n"
"v_max3_u32 v239, v239, v150, v151\n"
"v_max3_u32 v239, v239, v160, v161\n"
"v_max3_u32 v239, v239, v162, v163\n"
"v_max3_u32 v239, v239, v124, v125\n"
"v_max3_u32 v239, v239, v126, v127\n"
"v_max3_u32 v239, v239, v140, v141\n"
"v_max3_u32 v239, v239, v142, v143\n"
"v_max3_u32 v239, v239, v152, v153\n"
"v_max3_u32 v239, v239, v154, v155\n"
"v_max3_u32 v239, v239, v172, v173\n"
"v_max3_u32 v239, v239, v174, v175\n"
"v_max3_u32 v239, v239, v116, v117\n"
"v_max3_u32 v239, v239, v118, v119\n"
"v_max3_u32 v239, v239, v132, v133\n"
"v_max3_u32 v239, v239, v134, v135\n"
"v_cmp_eq_u32_e32 vcc, -1, v239\n"
"s_cbranch_vccz .Lmk_p112k\n"
"s_sub_u32 s1, s1, 1\n"
"s_cbranch_scc0 .Lmk_p112l\n"
".Lmk_p112k:\n"
"v_mfma_f32_16x16x32_bf16 v[96:99], a[128:131], v[148:151], 0\n"
"v_mfma_f32_16x16x32_bf16 v[96:99], a[132:135], v[160:163], v[96:99]\n"
"v_mfma_f32_16x16x32_bf16 v[96:99], a[136:139], v[124:127], v[96:99]\n"
"v_mfma_f32_16x16x32_bf16 v[96:99], a[140:143], v[140:143], v[96:99]\n"
"v_mfma_f32_16x16x32_bf16 v[96:99], a[144:147], v[152:155], v[96:99]\n"
"v_mfma_f32_16x16x32_bf16 v[96:99], a[148:151], v[172:175], v[96:99]\n"
"v_mfma_f32_16x16x32_bf16 v[96:99], a[152:155], v[116:119], v[96:99]\n"
"v_mfma_f32_16x16x32_bf16 v[96:99], a[156:159], v[132:135], v[96:99]\n"
"v_mfma_f32_16x16x32_bf16 v[104:107], a[188:191], v[132:135], 0\n"
"v_mfma_f32_16x16x32_bf16 v[104:107], a[184:187], v[116:119], v[104:107]\n"
"v_mfma_f32_16x16x32_bf16 v[104:107], a[180:183], v[172:175], v[104:107]\n"
"v_mfma_f32_16x16x32_bf16 v[104:107], a[176:179], v[152:155], v[104:107]\n"
"v_mfma_f32_16x16x32_bf16 v[104:107], a[172:175], v[140:143], v[104:107]\n"
"v_mfma_f32_16x16x32_bf16 v[104:107], a[168:171], v[124:127], v[104:107]\n"
"s_nop 7\n"
"s_branch .Lmk_p112r\n"
".Lmk_p113s:\n"
";\n"
"s_waitcnt vmcnt(16)\n"
"s_mov_b32 s1, 0x10000\n"
".Lmk_p113l:\n"
"s_add_u32 s97, s59, 0\n"
"buffer_load_dwordx4 v[176:179], v0, s[4:7], s97 offen sc1\n"
"buffer_load_dwordx4 v[180:183], v0, s[4:7], s97 offen offset:1024 sc1\n"
"buffer_load_dwordx4 v[168:171], v0, s[4:7], s97 offen offset:2048 sc1\n"
"buffer_load_dwordx4 v[184:187], v0, s[4:7], s97 offen offset:3072 sc1\n"
"buffer_load_dwordx4 v[188:191], v1, s[4:7], s97 offen sc1\n"
"buffer_load_dwordx4 v[164:167], v1, s[4:7], s97 offen offset:1024 sc1\n"
"buffer_load_dwordx4 v[192:195], v1, s[4:7], s97 offen offset:2048 sc1\n"
"buffer_load_dwordx4 v[196:199], v1, s[4:7], s97 offen offset:3072 sc1\n"
"s_add_u32 s97, s52, 0x440000\n"
"buffer_load_dwordx4 v[88:91], v0, s[4:7], s97 offen sc1\n"
"buffer_load_dwordx4 v[92:95], v0, s[4:7], s97 offen offset:1024 sc1\n"
"buffer_load_dwordx4 v[96:99], v0, s[4:7], s97 offen offset:2048 sc1\n"
"buffer_load_dwordx4 v[100:103], v0, s[4:7], s97 offen offset:3072 sc1\n"
"buffer_load_dwordx4 v[104:107], v1, s[4:7], s97 offen sc1\n"
"buffer_load_dwordx4 v[112:115], v1, s[4:7], s97 offen offset:1024 sc1\n"
"buffer_load_dwordx4 v[116:119], v1, s[4:7], s97 offen offset:2048 sc1\n"
"buffer_load_dwordx4 v[124:127], v1, s[4:7], s97 offen offset:3072 sc1\n"
"s_add_u32 s97, s53, 0x440000\n"
"buffer_load_dwordx4 v[128:131], v0, s[4:7], s97 offen sc1\n"
"buffer_load_dwordx4 v[132:135], v0, s[4:7], s97 offen offset:1024 sc1\n"
"buffer_load_dwordx4 v[136:139], v0, s[4:7], s97 offen offset:2048 sc1\n"
"s_waitcnt vmcnt(0)\n"
"v_max_u32_e32 v255, v176, v177\n"
"v_max3_u32 v255, v255, v178, v179\n"
"v_max3_u32 v255, v255, v180, v181\n"
"v_max3_u32 v255, v255, v182, v183\n"
"v_max3_u32 v255, v255, v168, v169\n"
"v_max3_u32 v255, v255, v170, v171\n"
"v_max3_u32 v255, v255, v184, v185\n"
"v_max3_u32 v255, v255, v186, v187\n"
"v_max3_u32 v255, v255, v188, v189\n"
"v_max3_u32 v255, v255, v190, v191\n"
"v_max3_u32 v255, v255, v164, v165\n"
"v_max3_u32 v255, v255, v166, v167\n"
"v_max3_u32 v255, v255, v192, v193\n"
"v_max3_u32 v255, v255, v194, v195\n"
"v_max3_u32 v255, v255, v196, v197\n"
"v_max3_u32 v255, v255, v198, v199\n"
"v_cmp_eq_u32_e32 vcc, -1, v255\n"
"s_cbranch_vccz .Lmk_p113k\n"
"s_sub_u32 s1, s1, 1\n"
"s_cbranch_scc0 .Lmk_p113l\n"
".Lmk_p113k:\n"
"v_mfma_f32_16x16x32_bf16 v[172:175], a[128:131], v[176:179], v[144:147]\n"
"v_mfma_f32_16x16x32_bf16 v[172:175], a[132:135], v[180:183], v[172:175]\n"
"v_mfma_f32_16x16x32_bf16 v[172:175], a[136:139], v[168:171], v[172:175]\n"
"v_mfma_f32_16x16x32_bf16 v[172:175], a[140:143], v[184:187], v[172:175]\n"
"v_mfma_f32_16x16x32_bf16 v[172:175], a[144:147], v[188:191], v[172:175]\n"
"v_mfma_f32_16x16x32_bf16 v[172:175], a[148:151], v[164:167], v[172:175]\n"
"v_mfma_f32_16x16x32_bf16 v[172:175], a[152:155], v[192:195], v[172:175]\n"
"v_mfma_f32_16x16x32_bf16 v[172:175], a[156:159], v[196:199], v[172:175]\n"
"v_mfma_f32_16x16x32_bf16 v[200:203], a[188:191], v[196:199], v[156:159]\n"
"v_mfma_f32_16x16x32_bf16 v[200:203], a[184:187], v[192:195], v[200:203]\n"
"v_mfma_f32_16x16x32_bf16 v[200:203], a[180:183], v[164:167], v[200:203]\n"
"v_mfma_f32_16x16x32_bf16 v[200:203], a[176:179], v[188:191], v[200:203]\n"
"v_mfma_f32_16x16x32_bf16 v[200:203], a[172:175], v[184:187], v[200:203]\n"
"v_mfma_f32_16x16x32_bf16 v[200:203], a[168:171], v[168:171], v[200:203]\n"
"s_nop 7\n"
"s_branch .Lmk_p113r\n"
".Lmk_exit:\n"
"v_xor_b32_e32 v255, 32, v15\n"
"ds_read_b128 v[36:39], v2 offset:17408\n"
"ds_read_b128 v[40:43], v15 offset:16384\n"
"ds_read_b128 v[52:55], v255 offset:16640\n"
"v_xor_b32_e32 v255, 32, v17\n"
"ds_read_b128 v[60:63], v2 offset:16384\n"
"ds_read_b128 v[64:67], v17 offset:15360\n"
"ds_read_b128 v[68:71], v255 offset:15616\n"
"v_xor_b32_e32 v255, 32, v16\n"
"ds_read_b64_tr_b16 v[72:73], v16 offset:16384\n"
"ds_read_b64_tr_b16 v[74:75], v255 offset:16640\n"
"ds_read_b64_tr_b16 v[76:77], v255 offset:16384\n"
"ds_read_b64_tr_b16 v[78:79], v16 offset:16640\n"
"v_mov_b32_e32 v80, 0x3f803f80\n"
"v_mov_b32_e32 v81, 0x3f803f80\n"
"v_mov_b32_e32 v82, 0x3f803f80\n"
"v_mov_b32_e32 v83, 0x3f803f80\n"
"v_xor_b32_e32 v255, 32, v16\n"
"ds_read_b64_tr_b16 v[84:85], v16 offset:15360\n"
"ds_read_b64_tr_b16 v[86:87], v255 offset:15616\n"
"s_waitcnt lgkmcnt(13)\n"
"ds_read_b64_tr_b16 v[96:97], v255 offset:15360\n"
"ds_read_b64_tr_b16 v[98:99], v16 offset:15616\n"
"v_mov_b32_e32 v100, 0x3f803f80\n"
"v_mov_b32_e32 v101, 0x3f803f80\n"
"v_mov_b32_e32 v102, 0x3f803f80\n"
"v_mov_b32_e32 v103, 0x3f803f80\n"
"s_waitcnt lgkmcnt(11)\n"
"v_mfma_f32_16x16x32_bf16 v[104:107], v[40:43], v[36:39], v[104:107]\n"
"v_mfma_f32_16x16x32_bf16 v[124:127], v[52:55], v[36:39], v[124:127]\n"
"s_waitcnt lgkmcnt(8)\n"
"v_mfma_f32_16x16x32_bf16 v[88:91], v[64:67], v[60:63], v[88:91]\n"
"v_mfma_f32_16x16x32_bf16 v[176:179], v[68:71], v[60:63], v[176:179]\n"
"s_nop 3\n"
"v_max_f32_e32 v255, v104, v105\n"
"s_nop 1\n"
"v_max_f32_e32 v254, v88, v89\n"
"v_max3_f32 v255, v255, v106, v107\n"
"v_max3_f32 v254, v254, v90, v91\n"
"v_max3_f32 v255, v255, v124, v125\n"
"v_max3_f32 v254, v254, v176, v177\n"
"v_max3_f32 v255, v255, v126, v127\n"
"v_max3_f32 v254, v254, v178, v179\n"
"v_mov_b32_e32 v253, v255\n"
"v_mov_b32_e32 v252, v254\n"
"s_nop 0\n"
"v_permlane16_swap_b32_e32 v255, v253\n"
"v_permlane16_swap_b32_e32 v254, v252\n"
"v_max_f32_e32 v255, v255, v253\n"
"v_max_f32_e32 v254, v254, v252\n"
"v_mov_b32_e32 v253, v255\n"
"v_mov_b32_e32 v252, v254\n"
"s_nop 0\n"
"v_permlane32_swap_b32_e32 v255, v253\n"
"v_permlane32_swap_b32_e32 v254, v252\n"
"v_max_f32_e32 v255, v255, v253\n"
"v_max_f32_e32 v254, v254, v252\n"
"v_sub_f32_e32 v104, v104, v255\n"
"v_sub_f32_e32 v105, v105, v255\n"
"v_sub_f32_e32 v106, v106, v255\n"
"v_sub_f32_e32 v107, v107, v255\n"
"v_sub_f32_e32 v124, v124, v255\n"
"v_sub_f32_e32 v125, v125, v255\n"
"v_sub_f32_e32 v126, v126, v255\n"
"v_sub_f32_e32 v127, v127, v255\n"
"v_exp_f32_e32 v104, v104\n"
"v_exp_f32_e32 v105, v105\n"
"v_exp_f32_e32 v106, v106\n"
"v_exp_f32_e32 v107, v107\n"
"v_exp_f32_e32 v124, v124\n"
"v_exp_f32_e32 v125, v125\n"
"v_exp_f32_e32 v126, v126\n"
"v_exp_f32_e32 v127, v127\n"
"v_cvt_pk_bf16_f32 v36, v104, v105\n"
"v_cvt_pk_bf16_f32 v37, v106, v107\n"
"v_cvt_pk_bf16_f32 v38, v124, v125\n"
"v_cvt_pk_bf16_f32 v39, v126, v127\n"
"s_waitcnt lgkmcnt(4)\n"
"v_sub_f32_e32 v88, v88, v254\n"
"v_sub_f32_e32 v89, v89, v254\n"
"v_sub_f32_e32 v90, v90, v254\n"
"v_sub_f32_e32 v91, v91, v254\n"
"v_sub_f32_e32 v176, v176, v254\n"
"v_sub_f32_e32 v177, v177, v254\n"
"v_sub_f32_e32 v178, v178, v254\n"
"v_mfma_f32_16x16x32_bf16 v[40:43], v[80:83], v[36:39], 0\n"
"v_sub_f32_e32 v179, v179, v254\n"
"v_exp_f32_e32 v88, v88\n"
"v_exp_f32_e32 v89, v89\n"
"v_exp_f32_e32 v90, v90\n"
"v_mfma_f32_16x16x32_bf16 v[52:55], v[72:75], v[36:39], 0\n"
"v_exp_f32_e32 v91, v91\n"
"v_exp_f32_e32 v176, v176\n"
"v_exp_f32_e32 v177, v177\n"
"v_exp_f32_e32 v178, v178\n"
"v_mfma_f32_16x16x32_bf16 v[60:63], v[76:79], v[36:39], 0\n"
"v_exp_f32_e32 v179, v179\n"
"v_cvt_pk_bf16_f32 v36, v88, v89\n"
"v_cvt_pk_bf16_f32 v37, v90, v91\n"
"v_cvt_pk_bf16_f32 v38, v176, v177\n"
"v_cvt_pk_bf16_f32 v39, v178, v179\n"
"s_waitcnt lgkmcnt(0)\n"
"v_rcp_f32_e32 v40, v40\n"
"s_nop 0\n"
"v_mul_f32_e32 v52, v52, v40\n"
"v_mul_f32_e32 v53, v53, v40\n"
"v_mfma_f32_16x16x32_bf16 v[64:67], v[100:103], v[36:39], 0\n"
"v_mul_f32_e32 v54, v54, v40\n"
"v_mul_f32_e32 v55, v55, v40\n"
"v_mul_f32_e32 v60, v60, v40\n"
"v_mfma_f32_16x16x32_bf16 v[68:71], v[84:87], v[36:39], 0\n"
"v_mul_f32_e32 v61, v61, v40\n"
"v_mul_f32_e32 v62, v62, v40\n"
"v_mul_f32_e32 v63, v63, v40\n"
"v_cvt_pk_bf16_f32 v40, v52, v53\n"
"v_mfma_f32_16x16x32_bf16 v[72:75], v[96:99], v[36:39], 0\n"
"v_cvt_pk_bf16_f32 v41, v54, v55\n"
"v_cvt_pk_bf16_f32 v42, v60, v61\n"
"v_cvt_pk_bf16_f32 v43, v62, v63\n"
"s_add_u32 s97, s60, 0x408000\n"
"buffer_store_dwordx4 v[40:43], v0, s[4:7], s97 offen sc1\n"
"v_rcp_f32_e32 v64, v64\n"
"s_nop 0\n"
"v_mul_f32_e32 v68, v68, v64\n"
"v_mul_f32_e32 v69, v69, v64\n"
"v_mul_f32_e32 v70, v70, v64\n"
"v_mul_f32_e32 v71, v71, v64\n"
"v_mul_f32_e32 v72, v72, v64\n"
"v_mul_f32_e32 v73, v73, v64\n"
"v_mul_f32_e32 v74, v74, v64\n"
"v_mul_f32_e32 v75, v75, v64\n"
"v_cvt_pk_bf16_f32 v36, v68, v69\n"
"v_cvt_pk_bf16_f32 v37, v70, v71\n"
"v_cvt_pk_bf16_f32 v38, v72, v73\n"
"v_cvt_pk_bf16_f32 v39, v74, v75\n"
"s_add_u32 s97, s60, 0x400000\n"
"buffer_store_dwordx4 v[36:39], v0, s[4:7], s97 offen sc1\n"
";\n"
"s_waitcnt vmcnt(23)\n"
"; @B G2 b.0\n"
"s_nop 0\n"
"v_mfma_f32_16x16x32_bf16 v[36:39], a[128:131], v[120:123], 0\n"
"s_add_u32 s97, s58, 0x440000\n"
"buffer_load_dwordx4 v[40:43], v0, s[4:7], s97 offen sc1\n"
"v_mfma_f32_16x16x32_bf16 v[36:39], a[132:135], v[136:139], v[36:39]\n"
"s_waitcnt vmcnt(22)\n"
"v_mfma_f32_16x16x32_bf16 v[36:39], a[136:139], v[144:147], v[36:39]\n"
"v_mfma_f32_16x16x32_bf16 v[36:39], a[140:143], v[192:195], v[36:39]\n"
"s_waitcnt vmcnt(20)\n"
"v_mfma_f32_16x16x32_bf16 v[36:39], a[144:147], v[196:199], v[36:39]\n"
"buffer_load_dwordx4 v[52:55], v0, s[4:7], s97 offen offset:1024 sc1\n"
"v_mfma_f32_16x16x32_bf16 v[36:39], a[148:151], v[188:191], v[36:39]\n"
"s_waitcnt vmcnt(19)\n"
"v_mfma_f32_16x16x32_bf16 v[36:39], a[152:155], v[108:111], v[36:39]\n"
"v_mfma_f32_16x16x32_bf16 v[36:39], a[156:159], v[92:95], v[36:39]\n"
"v_mfma_f32_16x16x32_bf16 v[60:63], a[188:191], v[92:95], 0\n"
"buffer_load_dwordx4 v[64:67], v0, s[4:7], s97 offen offset:2048 sc1\n"
"v_mfma_f32_16x16x32_bf16 v[60:63], a[184:187], v[108:111], v[60:63]\n"
"v_mfma_f32_16x16x32_bf16 v[60:63], a[180:183], v[188:191], v[60:63]\n"
"v_mfma_f32_16x16x32_bf16 v[60:63], a[176:179], v[196:199], v[60:63]\n"
"v_mfma_f32_16x16x32_bf16 v[60:63], a[172:175], v[192:195], v[60:63]\n"
"s_nop 1\n"
"v_cmp_u_f32_e32 vcc, v36, v36\n"
"v_mfma_f32_16x16x32_bf16 v[60:63], a[168:171], v[144:147], v[60:63]\n"
"s_cbranch_vccnz .Lmk_p55s\n"
".Lmk_p55r:\n"
"v_mfma_f32_16x16x32_bf16 v[60:63], a[164:167], v[136:139], v[60:63]\n"
"ds_write_b128 v3, v[36:39]\n"
"v_mfma_f32_16x16x32_bf16 v[60:63], a[160:163], v[120:123], v[60:63]\n"
"buffer_load_dwordx4 v[36:39], v0, s[4:7], s97 offen offset:3072 sc1\n"
"v_mfma_f32_16x16x32_bf16 v[68:71], a[192:195], v[120:123], 0\n"
"v_bfe_u32 v255, v0, 4, 4\n"
"v_bfe_u32 v254, v0, 9, 1\n"
"v_mfma_f32_16x16x32_bf16 v[68:71], a[196:199], v[136:139], v[68:71]\n"
"v_and_b32_e32 v253, 7, v255\n"
"v_xor_b32_e32 v254, v254, v253\n"
"v_mfma_f32_16x16x32_bf16 v[68:71], a[200:203], v[144:147], v[68:71]\n"
"v_lshlrev_b32_e32 v255, 7, v255\n"
"v_lshl_add_u32 v255, v254, 4, v255\n"
"v_mfma_f32_16x16x32_bf16 v[68:71], a[204:207], v[192:195], v[68:71]\n"
"buffer_load_dwordx4 v[72:75], v1, s[4:7], s97 offen sc1\n"
"v_mfma_f32_16x16x32_bf16 v[68:71], a[208:211], v[196:199], v[68:71]\n"
"v_bfe_u32 v254, v0, 8, 1\n"
"v_lshl_add_u32 v255, v254, 3, v255\n"
"v_mfma_f32_16x16x32_bf16 v[68:71], a[212:215], v[188:191], v[68:71]\n"
"ds_write_b128 v3, v[60:63] offset:1024\n"
"v_mfma_f32_16x16x32_bf16 v[68:71], a[216:219], v[108:111], v[68:71]\n"
"v_add_u32_e32 v255, s77, v255\n"
"v_bfe_u32 v253, v0, 7, 3\n"
"v_mfma_f32_16x16x32_bf16 v[68:71], a[220:223], v[92:95], v[68:71]\n"
"buffer_load_dwordx4 v[60:63], v1, s[4:7], s97 offen offset:1024 sc1\n"
"v_mfma_f32_16x16x32_bf16 v[76:79], a[252:255], v[92:95], 0\n"
"v_bfe_u32 v254, v0, 4, 3\n"
"v_xor_b32_e32 v254, v254, v253\n"
"v_mfma_f32_16x16x32_bf16 v[76:79], a[248:251], v[108:111], v[76:79]\n"
"v_lshlrev_b32_e32 v253, 11, v253\n"
"v_lshl_add_u32 v253, v254, 4, v253\n"
"v_mfma_f32_16x16x32_bf16 v[76:79], a[244:247], v[188:191], v[76:79]\n"
"v_mfma_f32_16x16x32_bf16 v[76:79], a[240:243], v[196:199], v[76:79]\n"
"buffer_load_dwordx4 v[80:83], v1, s[4:7], s97 offen offset:2048 sc1\n"
"v_mfma_f32_16x16x32_bf16 v[76:79], a[236:239], v[192:195], v[76:79]\n"
"v_mfma_f32_16x16x32_bf16 v[76:79], a[232:235], v[144:147], v[76:79]\n"
"ds_write_b128 v3, v[68:71] offset:2048\n"
"v_mfma_f32_16x16x32_bf16 v[76:79], a[228:231], v[136:139], v[76:79]\n"
"v_mfma_f32_16x16x32_bf16 v[76:79], a[224:227], v[120:123], v[76:79]\n"
"s_waitcnt vmcnt(20)\n"
"buffer_load_dwordx4 v[68:71], v1, s[4:7], s97 offen offset:3072 sc1\n"
"; @B G2 b.1\n"
"v_mfma_f32_16x16x32_bf16 v[84:87], a[128:131], v[148:151], 0\n"
"v_mfma_f32_16x16x32_bf16 v[84:87], a[132:135], v[180:183], v[84:87]\n"
"s_add_u32 s97, s59, 0x440000\n"
"v_mfma_f32_16x16x32_bf16 v[84:87], a[136:139], v[24:27], v[84:87]\n"
"v_mfma_f32_16x16x32_bf16 v[84:87], a[140:143], v[32:35], v[84:87]\n"
"s_waitcnt vmcnt(15)\n"
"buffer_load_dwordx4 v[96:99], v0, s[4:7], s97 offen sc1\n"
"v_mfma_f32_16x16x32_bf16 v[84:87], a[144:147], v[44:47], v[84:87]\n"
"v_mfma_f32_16x16x32_bf16 v[84:87], a[148:151], v[48:51], v[84:87]\n"
"ds_write_b128 v3, v[76:79] offset:3072\n"
"v_mfma_f32_16x16x32_bf16 v[84:87], a[152:155], v[28:31], v[84:87]\n"
"v_mfma_f32_16x16x32_bf16 v[84:87], a[156:159], v[56:59], v[84:87]\n"
"buffer_load_dwordx4 v[76:79], v0, s[4:7], s97 offen offset:1024 sc1\n"
"v_mfma_f32_16x16x32_bf16 v[100:103], a[188:191], v[56:59], 0\n"
"v_mfma_f32_16x16x32_bf16 v[100:103], a[184:187], v[28:31], v[100:103]\n"
"v_mfma_f32_16x16x32_bf16 v[100:103], a[180:183], v[48:51], v[100:103]\n"
"v_mfma_f32_16x16x32_bf16 v[100:103], a[176:179], v[44:47], v[100:103]\n"
"buffer_load_dwordx4 v[112:115], v0, s[4:7], s97 offen offset:2048 sc1\n"
"v_mfma_f32_16x16x32_bf16 v[100:103], a[172:175], v[32:35], v[100:103]\n"
"s_nop 0\n"
"v_cmp_u_f32_e32 vcc, v84, v84\n"
"v_mfma_f32_16x16x32_bf16 v[100:103], a[168:171], v[24:27], v[100:103]\n"
"s_cbranch_vccnz .Lmk_p56s\n"
".Lmk_p56r:\n"
"v_mfma_f32_16x16x32_bf16 v[100:103], a[164:167], v[180:183], v[100:103]\n"
"ds_write_b128 v3, v[84:87] offset:6144\n"
"v_mfma_f32_16x16x32_bf16 v[100:103], a[160:163], v[148:151], v[100:103]\n"
"buffer_load_dwordx4 v[84:87], v0, s[4:7], s97 offen offset:3072 sc1\n"
"v_mfma_f32_16x16x32_bf16 v[116:119], a[192:195], v[148:151], 0\n"
"v_mfma_f32_16x16x32_bf16 v[116:119], a[196:199], v[180:183], v[116:119]\n"
"v_mfma_f32_16x16x32_bf16 v[116:119], a[200:203], v[24:27], v[116:119]\n"
"v_mfma_f32_16x16x32_bf16 v[116:119], a[204:207], v[32:35], v[116:119]\n"
"buffer_load_dwordx4 v[128:131], v1, s[4:7], s97 offen sc1\n"
"v_mfma_f32_16x16x32_bf16 v[116:119], a[208:211], v[44:47], v[116:119]\n"
"ds_read_b128 v[132:135], v2 offset:8192\n"
"ds_read_b128 v[140:143], v2 offset:9216\n"
"v_mfma_f32_16x16x32_bf16 v[116:119], a[212:215], v[48:51], v[116:119]\n"
"ds_write_b128 v3, v[100:103] offset:7168\n"
"v_mfma_f32_16x16x32_bf16 v[116:119], a[216:219], v[28:31], v[116:119]\n"
"v_mfma_f32_16x16x32_bf16 v[116:119], a[220:223], v[56:59], v[116:119]\n"
"buffer_load_dwordx4 v[100:103], v1, s[4:7], s97 offen offset:1024 sc1\n"
"v_mfma_f32_16x16x32_bf16 v[152:155], a[252:255], v[56:59], 0\n"
"v_mfma_f32_16x16x32_bf16 v[152:155], a[248:251], v[28:31], v[152:155]\n"
"s_waitcnt lgkmcnt(1)\n"
"v_lshlrev_b32_e32 v254, 16, v132\n"
"v_and_b32_e32 v252, 0xffff0000, v132\n"
"v_lshlrev_b32_e32 v251, 16, v133\n"
"v_mfma_f32_16x16x32_bf16 v[152:155], a[244:247], v[48:51], v[152:155]\n"
"v_and_b32_e32 v250, 0xffff0000, v133\n"
"v_lshlrev_b32_e32 v249, 16, v134\n"
"v_and_b32_e32 v248, 0xffff0000, v134\n"
"v_mfma_f32_16x16x32_bf16 v[152:155], a[240:243], v[44:47], v[152:155]\n"
"buffer_load_dwordx4 v[156:159], v1, s[4:7], s97 offen offset:2048 sc1\n"
"v_lshlrev_b32_e32 v247, 16, v135\n"
"v_mfma_f32_16x16x32_bf16 v[152:155], a[236:239], v[32:35], v[152:155]\n"
"v_and_b32_e32 v246, 0xffff0000, v135\n"
"v_lshlrev_b32_e32 v245, 16, v140\n"
"v_and_b32_e32 v244, 0xffff0000, v140\n"
"v_mfma_f32_16x16x32_bf16 v[152:155], a[232:235], v[24:27], v[152:155]\n"
"ds_write_b128 v3, v[116:119] offset:8192\n"
"v_mfma_f32_16x16x32_bf16 v[152:155], a[228:231], v[180:183], v[152:155]\n"
"v_lshlrev_b32_e32 v243, 16, v141\n"
"v_and_b32_e32 v242, 0xffff0000, v141\n"
"v_lshlrev_b32_e32 v241, 16, v142\n"
"v_mfma_f32_16x16x32_bf16 v[152:155], a[224:227], v[148:151], v[152:155]\n"
"s_waitcnt vmcnt(11)\n"
"buffer_load_dwordx4 v[116:119], v1, s[4:7], s97 offen offset:3072 sc1\n"
"; @B G2 b.2\n"
"v_mfma_f32_16x16x32_bf16 v[132:135], a[128:131], v[40:43], 0\n"
"v_and_b32_e32 v240, 0xffff0000, v142\n"
"v_lshlrev_b32_e32 v239, 16, v143\n"
"v_and_b32_e32 v238, 0xffff0000, v143\n"
"v_mfma_f32_16x16x32_bf16 v[132:135], a[132:135], v[52:55], v[132:135]\n"
"v_mfma_f32_16x16x32_bf16 v[132:135], a[136:139], v[64:67], v[132:135]\n"
"v_mfma_f32_16x16x32_bf16 v[132:135], a[140:143], v[36:39], v[132:135]\n"
"s_waitcnt vmcnt(8)\n"
"v_mfma_f32_16x16x32_bf16 v[132:135], a[144:147], v[72:75], v[132:135]\n"
"v_mfma_f32_16x16x32_bf16 v[132:135], a[148:151], v[60:63], v[132:135]\n"
"ds_write_b128 v3, v[152:155] offset:9216\n"
"v_mfma_f32_16x16x32_bf16 v[132:135], a[152:155], v[80:83], v[132:135]\n"
"v_mfma_f32_16x16x32_bf16 v[132:135], a[156:159], v[68:71], v[132:135]\n"
"v_mfma_f32_16x16x32_bf16 v[140:143], a[188:191], v[68:71], 0\n"
"v_mfma_f32_16x16x32_bf16 v[140:143], a[184:187], v[80:83], v[140:143]\n"
"s_waitcnt lgkmcnt(0)\n"
"s_barrier\n"
"v_mfma_f32_16x16x32_bf16 v[140:143], a[180:183], v[60:63], v[140:143]\n"
"ds_read_b128 v[152:155], v4\n"
"ds_read_b128 v[160:163], v5\n"
"v_mfma_f32_16x16x32_bf16 v[140:143], a[176:179], v[72:75], v[140:143]\n"
"v_mfma_f32_16x16x32_bf16 v[140:143], a[172:175], v[36:39], v[140:143]\n"
"s_add_u32 s97, s56, 0x400000\n"
"v_cmp_u_f32_e32 vcc, v132, v132\n"
"v_mfma_f32_16x16x32_bf16 v[140:143], a[168:171], v[64:67], v[140:143]\n"
"s_cbranch_vccnz .Lmk_p57s\n"
".Lmk_p57r:\n"
"v_mfma_f32_16x16x32_bf16 v[140:143], a[164:167], v[52:55], v[140:143]\n"
"ds_write_b128 v3, v[132:135] offset:12288\n"
"v_mfma_f32_16x16x32_bf16 v[140:143], a[160:163], v[40:43], v[140:143]\n"
"buffer_load_dwordx4 v[132:135], v0, s[4:7], s97 offen sc1\n"
"s_waitcnt lgkmcnt(1)\n"
"v_mfma_f32_16x16x32_bf16 v[164:167], a[192:195], v[40:43], 0\n"
"ds_read_b128 v[168:171], v4 offset:1024\n"
"ds_read_b128 v[172:175], v5 offset:1024\n"
"v_add_f32_e32 v152, v152, v254\n"
"v_mfma_f32_16x16x32_bf16 v[164:167], a[196:199], v[52:55], v[164:167]\n"
"v_add_f32_e32 v153, v153, v252\n"
"v_add_f32_e32 v154, v154, v251\n"
"v_add_f32_e32 v155, v155, v250\n"
"v_mfma_f32_16x16x32_bf16 v[164:167], a[200:203], v[64:67], v[164:167]\n"
"buffer_load_dwordx4 v[184:187], v0, s[4:7], s97 offen offset:1024 sc1\n"
"v_add_f32_e32 v152, v152, v160\n"
"v_mfma_f32_16x16x32_bf16 v[164:167], a[204:207], v[36:39], v[164:167]\n"
"v_add_f32_e32 v153, v153, v161\n"
"v_add_f32_e32 v154, v154, v162\n"
"v_add_f32_e32 v155, v155, v163\n"
"v_mfma_f32_16x16x32_bf16 v[164:167], a[208:211], v[72:75], v[164:167]\n"
"v_mfma_f32_16x16x32_bf16 v[164:167], a[212:215], v[60:63], v[164:167]\n"
"ds_write_b128 v3, v[140:143] offset:13312\n"
"v_mfma_f32_16x16x32_bf16 v[164:167], a[216:219], v[80:83], v[164:167]\n"
"buffer_load_dwordx4 v[140:143], v0, s[4:7], s97 offen offset:2048 sc1\n"
"s_waitcnt lgkmcnt(1)\n"
"v_mfma_f32_16x16x32_bf16 v[164:167], a[220:223], v[68:71], v[164:167]\n"
"ds_read_b128 v[160:163], v4 offset:2048\n"
"ds_read_b128 v[200:203], v5 offset:2048\n"
"v_add_f32_e32 v168, v168, v249\n"
"v_mfma_f32_16x16x32_bf16 v[204:207], a[252:255], v[68:71], 0\n"
"v_add_f32_e32 v169, v169, v248\n"
"v_add_f32_e32 v170, v170, v247\n"
"v_add_f32_e32 v171, v171, v246\n"
"v_mfma_f32_16x16x32_bf16 v[204:207], a[248:251], v[80:83], v[204:207]\n"
"buffer_load_dwordx4 v[68:71], v0, s[4:7], s97 offen offset:3072 sc1\n"
"v_add_f32_e32 v168, v168, v172\n"
"v_mfma_f32_16x16x32_bf16 v[204:207], a[244:247], v[60:63], v[204:207]\n"
"v_add_f32_e32 v169, v169, v173\n"
"v_add_f32_e32 v170, v170, v174\n"
"v_add_f32_e32 v171, v171, v175\n"
"v_mfma_f32_16x16x32_bf16 v[204:207], a[240:243], v[72:75], v[204:207]\n"
"v_mfma_f32_16x16x32_bf16 v[204:207], a[236:239], v[36:39], v[204:207]\n"
"buffer_load_dwordx4 v[36:39], v1, s[4:7], s97 offen sc1\n"
"s_waitcnt lgkmcnt(0)\n"
"v_mfma_f32_16x16x32_bf16 v[204:207], a[232:235], v[64:67], v[204:207]\n"
"ds_write_b128 v3, v[164:167] offset:14336\n"
"v_mfma_f32_16x16x32_bf16 v[204:207], a[228:231], v[52:55], v[204:207]\n"
"ds_read_b128 v[52:55], v4 offset:3072\n"
"ds_read_b128 v[60:63], v5 offset:3072\n"
"v_add_f32_e32 v160, v160, v245\n"
"v_mfma_f32_16x16x32_bf16 v[204:207], a[224:227], v[40:43], v[204:207]\n"
"s_waitcnt vmcnt(9)\n"
"buffer_load_dwordx4 v[40:43], v1, s[4:7], s97 offen offset:1024 sc1\n"
"; @B G2 b.3\n"
"v_mfma_f32_16x16x32_bf16 v[64:67], a[128:131], v[96:99], v[152:155]\n"
"v_add_f32_e32 v161, v161, v244\n"
"v_add_f32_e32 v162, v162, v243\n"
"v_add_f32_e32 v163, v163, v242\n"
"v_mfma_f32_16x16x32_bf16 v[64:67], a[132:135], v[76:79], v[64:67]\n"
"v_add_f32_e32 v160, v160, v200\n"
"v_add_f32_e32 v161, v161, v201\n"
"v_add_f32_e32 v162, v162, v202\n"
"v_mfma_f32_16x16x32_bf16 v[64:67], a[136:139], v[112:115], v[64:67]\n"
"buffer_load_dwordx4 v[72:75], v1, s[4:7], s97 offen offset:2048 sc1\n"
"v_add_f32_e32 v163, v163, v203\n"
"v_mfma_f32_16x16x32_bf16 v[64:67], a[140:143], v[84:87], v[64:67]\n"
"s_waitcnt vmcnt(7)\n"
"s_waitcnt lgkmcnt(0)\n"
"v_add_f32_e32 v52, v52, v241\n"
"v_add_f32_e32 v53, v53, v240\n"
"v_mfma_f32_16x16x32_bf16 v[64:67], a[144:147], v[128:131], v[64:67]\n"
"v_add_f32_e32 v54, v54, v239\n"
"v_add_f32_e32 v55, v55, v238\n"
"v_add_f32_e32 v52, v52, v60\n"
"v_mfma_f32_16x16x32_bf16 v[64:67], a[148:151], v[100:103], v[64:67]\n"
"ds_write_b128 v3, v[204:207] offset:15360\n"
"v_mfma_f32_16x16x32_bf16 v[64:67], a[152:155], v[156:159], v[64:67]\n"
"buffer_load_dwordx4 v[80:83], v1, s[4:7], s97 offen offset:3072 sc1\n"
"v_add_f32_e32 v53, v53, v61\n"
"v_mfma_f32_16x16x32_bf16 v[64:67], a[156:159], v[116:119], v[64:67]\n"
"s_add_u32 s97, s57, 0x400000\n"
"v_add_f32_e32 v54, v54, v62\n"
"v_add_f32_e32 v55, v55, v63\n"
"v_mfma_f32_16x16x32_bf16 v[60:63], a[188:191], v[116:119], v[168:171]\n"
"v_mfma_f32_16x16x32_bf16 v[60:63], a[184:187], v[156:159], v[60:63]\n"
"s_waitcnt lgkmcnt(0)\n"
"s_barrier\n"
"v_mfma_f32_16x16x32_bf16 v[60:63], a[180:183], v[100:103], v[60:63]\n"
"buffer_load_dwordx4 v[164:167], v0, s[4:7], s97 offen sc1\n"
"v_mfma_f32_16x16x32_bf16 v[60:63], a[176:179], v[128:131], v[60:63]\n"
"ds_read_b128 v[172:175], v6\n"
"ds_read_b128 v[200:203], v6 offset:1024\n"
"v_mfma_f32_16x16x32_bf16 v[60:63], a[172:175], v[84:87], v[60:63]\n"
"ds_read_b128 v[204:207], v6 offset:2048\n"
"v_cmp_u_f32_e32 vcc, v64, v64\n"
"v_mfma_f32_16x16x32_bf16 v[60:63], a[168:171], v[112:115], v[60:63]\n"
"s_cbranch_vccnz .Lmk_p58s\n"
".Lmk_p58r:\n"
"ds_read_b128 v[152:155], v6 offset:3072\n"
"v_mfma_f32_16x16x32_bf16 v[60:63], a[164:167], v[76:79], v[60:63]\n"
"buffer_load_dwordx4 v[168:171], v0, s[4:7], s97 offen offset:1024 sc1\n"
"v_mfma_f32_16x16x32_bf16 v[60:63], a[160:163], v[96:99], v[60:63]\n"
"v_mfma_f32_16x16x32_bf16 v[160:163], a[192:195], v[96:99], v[160:163]\n"
"v_mfma_f32_16x16x32_bf16 v[160:163], a[196:199], v[76:79], v[160:163]\n"
"buffer_load_dwordx4 v[208:211], v0, s[4:7], s97 offen offset:2048 sc1\n"
"v_mfma_f32_16x16x32_bf16 v[160:163], a[200:203], v[112:115], v[160:163]\n"
"v_mfma_f32_16x16x32_bf16 v[160:163], a[204:207], v[84:87], v[160:163]\n"
"v_mfma_f32_16x16x32_bf16 v[160:163], a[208:211], v[128:131], v[160:163]\n"
"buffer_load_dwordx4 v[212:215], v0, s[4:7], s97 offen offset:3072 sc1\n"
"s_waitcnt lgkmcnt(0)\n"
"v_mfma_f32_16x16x32_bf16 v[160:163], a[212:215], v[100:103], v[160:163]\n"
"v_add_f32_e32 v172, v64, v172\n"
"v_add_f32_e32 v173, v65, v173\n"
"v_add_f32_e32 v174, v66, v174\n"
"v_mfma_f32_16x16x32_bf16 v[160:163], a[216:219], v[156:159], v[160:163]\n"
"v_add_f32_e32 v175, v67, v175\n"
"v_add_f32_e32 v200, v60, v200\n"
"v_add_f32_e32 v201, v61, v201\n"
"v_mfma_f32_16x16x32_bf16 v[160:163], a[220:223], v[116:119], v[160:163]\n"
"buffer_load_dwordx4 v[64:67], v1, s[4:7], s97 offen sc1\n"
"v_add_f32_e32 v202, v62, v202\n"
"v_mfma_f32_16x16x32_bf16 v[52:55], a[252:255], v[116:119], v[52:55]\n"
"v_add_f32_e32 v203, v63, v203\n"
"v_cvt_pk_bf16_f32 v60, v172, v173\n"
"v_cvt_pk_bf16_f32 v61, v174, v175\n"
"v_mfma_f32_16x16x32_bf16 v[52:55], a[248:251], v[156:159], v[52:55]\n"
"v_cvt_pk_bf16_f32 v62, v200, v201\n"
"v_cvt_pk_bf16_f32 v63, v202, v203\n"
"v_xor_b32_e32 v254, 32, v255\n"
"v_mfma_f32_16x16x32_bf16 v[52:55], a[244:247], v[100:103], v[52:55]\n"
"buffer_load_dwordx4 v[100:103], v1, s[4:7], s97 offen offset:1024 sc1\n"
"v_mfma_f32_16x16x32_bf16 v[52:55], a[240:243], v[128:131], v[52:55]\n"
"ds_write_b64 v255, v[60:61] offset:8192\n"
"v_mfma_f32_16x16x32_bf16 v[52:55], a[236:239], v[84:87], v[52:55]\n"
"ds_write_b64 v254, v[62:63] offset:8192\n"
"v_mfma_f32_16x16x32_bf16 v[52:55], a[232:235], v[112:115], v[52:55]\n"
"buffer_load_dwordx4 v[60:63], v1, s[4:7], s97 offen offset:2048 sc1\n"
"v_mfma_f32_16x16x32_bf16 v[52:55], a[228:231], v[76:79], v[52:55]\n"
"v_mfma_f32_16x16x32_bf16 v[52:55], a[224:227], v[96:99], v[52:55]\n"
"buffer_load_dwordx4 v[76:79], v1, s[4:7], s97 offen offset:3072 sc1\n"
"s_waitcnt vmcnt(14)\n"
"; @B G2 a.0\n"
"v_mfma_f32_16x16x32_bf16 v[84:87], a[128:131], v[132:135], 0\n"
"s_add_u32 s97, s58, 0x400000\n"
"buffer_load_dwordx4 v[96:99], v0, s[4:7], s97 offen sc1\n"
"v_mfma_f32_16x16x32_bf16 v[84:87], a[132:135], v[184:187], v[84:87]\n"
"s_waitcnt vmcnt(13)\n"
"v_add_f32_e32 v204, v160, v204\n"
"v_add_f32_e32 v205, v161, v205\n"
"v_mfma_f32_16x16x32_bf16 v[84:87], a[136:139], v[140:143], v[84:87]\n"
"v_add_f32_e32 v206, v162, v206\n"
"v_add_f32_e32 v207, v163, v207\n"
"v_mfma_f32_16x16x32_bf16 v[84:87], a[140:143], v[68:71], v[84:87]\n"
"s_waitcnt vmcnt(11)\n"
"buffer_load_dwordx4 v[112:115], v0, s[4:7], s97 offen offset:1024 sc1\n"
"v_mfma_f32_16x16x32_bf16 v[84:87], a[144:147], v[36:39], v[84:87]\n"
"v_add_f32_e32 v152, v52, v152\n"
"v_add_f32_e32 v153, v53, v153\n"
"v_mfma_f32_16x16x32_bf16 v[84:87], a[148:151], v[40:43], v[84:87]\n"
"s_waitcnt vmcnt(10)\n"
"v_add_f32_e32 v154, v54, v154\n"
"v_add_f32_e32 v155, v55, v155\n"
"v_mfma_f32_16x16x32_bf16 v[84:87], a[152:155], v[72:75], v[84:87]\n"
"buffer_load_dwordx4 v[52:55], v0, s[4:7], s97 offen offset:2048 sc1\n"
"v_mfma_f32_16x16x32_bf16 v[84:87], a[156:159], v[80:83], v[84:87]\n"
"v_cvt_pk_bf16_f32 v116, v204, v205\n"
"v_cvt_pk_bf16_f32 v117, v206, v207\n"
"v_mfma_f32_16x16x32_bf16 v[128:131], a[188:191], v[80:83], 0\n"
"v_cvt_pk_bf16_f32 v118, v152, v153\n"
"v_cvt_pk_bf16_f32 v119, v154, v155\n"
"v_mfma_f32_16x16x32_bf16 v[128:131], a[184:187], v[72:75], v[128:131]\n"
"buffer_load_dwordx4 v[152:155], v0, s[4:7], s97 offen offset:3072 sc1\n"
"v_mfma_f32_16x16x32_bf16 v[128:131], a[180:183], v[40:43], v[128:131]\n"
"v_xor_b32_e32 v254, 64, v255\n"
"v_xor_b32_e32 v252, 0x60, v255\n"
"v_mfma_f32_16x16x32_bf16 v[128:131], a[176:179], v[36:39], v[128:131]\n"
"ds_write_b64 v254, v[116:117] offset:8192\n"
"v_mfma_f32_16x16x32_bf16 v[128:131], a[172:175], v[68:71], v[128:131]\n"
"v_cmp_u_f32_e32 vcc, v84, v84\n"
"v_mfma_f32_16x16x32_bf16 v[128:131], a[168:171], v[140:143], v[128:131]\n"
"s_cbranch_vccnz .Lmk_p59s\n"
".Lmk_p59r:\n"
"v_mfma_f32_16x16x32_bf16 v[128:131], a[164:167], v[184:187], v[128:131]\n"
"ds_write_b128 v3, v[84:87]\n"
"v_mfma_f32_16x16x32_bf16 v[128:131], a[160:163], v[132:135], v[128:131]\n"
"buffer_load_dwordx4 v[84:87], v1, s[4:7], s97 offen sc1\n"
"v_mfma_f32_16x16x32_bf16 v[156:159], a[192:195], v[132:135], 0\n"
"ds_write_b64 v252, v[118:119] offset:8192\n"
"v_mfma_f32_16x16x32_bf16 v[156:159], a[196:199], v[184:187], v[156:159]\n"
"ds_read_b128 v[116:119], v2 offset:8192\n"
"ds_read_b128 v[160:163], v2 offset:9216\n"
"v_mfma_f32_16x16x32_bf16 v[156:159], a[200:203], v[140:143], v[156:159]\n"
"buffer_load_dwordx4 v[172:175], v1, s[4:7], s97 offen offset:1024 sc1\n"
"v_mfma_f32_16x16x32_bf16 v[156:159], a[204:207], v[68:71], v[156:159]\n"
"v_bfe_u32 v255, v0, 4, 4\n"
"v_bfe_u32 v254, v0, 9, 1\n"
"v_mfma_f32_16x16x32_bf16 v[156:159], a[208:211], v[36:39], v[156:159]\n"
"v_and_b32_e32 v252, 7, v255\n"
"v_xor_b32_e32 v254, v254, v252\n"
"v_mfma_f32_16x16x32_bf16 v[156:159], a[212:215], v[40:43], v[156:159]\n"
"ds_write_b128 v3, v[128:131] offset:1024\n"
"v_mfma_f32_16x16x32_bf16 v[156:159], a[216:219], v[72:75], v[156:159]\n"
"buffer_load_dwordx4 v[128:131], v1, s[4:7], s97 offen offset:2048 sc1\n"
"v_mfma_f32_16x16x32_bf16 v[156:159], a[220:223], v[80:83], v[156:159]\n"
"s_waitcnt lgkmcnt(1)\n"
"v_lshlrev_b32_e32 v255, 7, v255\n"
"v_lshl_add_u32 v255, v254, 4, v255\n"
"v_mfma_f32_16x16x32_bf16 v[200:203], a[252:255], v[80:83], 0\n"
"v_bfe_u32 v254, v0, 8, 1\n"
"v_lshl_add_u32 v255, v254, 3, v255\n"
"v_mfma_f32_16x16x32_bf16 v[200:203], a[248:251], v[72:75], v[200:203]\n"
"buffer_load_dwordx4 v[72:75], v1, s[4:7], s97 offen offset:3072 sc1\n"
"v_mfma_f32_16x16x32_bf16 v[200:203], a[244:247], v[40:43], v[200:203]\n"
"s_add_u32 s97, s59, 0x400000\n"
"v_add_u32_e32 v255, s77, v255\n"
"v_bfe_u32 v252, v0, 7, 3\n"
"v_mfma_f32_16x16x32_bf16 v[200:203], a[240:243], v[36:39], v[200:203]\n"
"v_bfe_u32 v254, v0, 4, 3\n"
"v_xor_b32_e32 v254, v254, v252\n"
"v_mfma_f32_16x16x32_bf16 v[200:203], a[236:239], v[68:71], v[200:203]\n"
"buffer_load_dwordx4 v[36:39], v0, s[4:7], s97 offen sc1\n"
"v_mfma_f32_16x16x32_bf16 v[200:203], a[232:235], v[140:143], v[200:203]\n"
"ds_write_b128 v3, v[156:159] offset:2048\n"
"v_mfma_f32_16x16x32_bf16 v[200:203], a[228:231], v[184:187], v[200:203]\n"
"v_lshlrev_b32_e32 v252, 11, v252\n"
"v_lshl_add_u32 v252, v254, 4, v252\n"
"v_mfma_f32_16x16x32_bf16 v[200:203], a[224:227], v[132:135], v[200:203]\n"
"s_waitcnt vmcnt(13)\n"
"buffer_load_dwordx4 v[40:43], v0, s[4:7], s97 offen offset:1024 sc1\n"
"; @B G2 a.1\n"
"v_mfma_f32_16x16x32_bf16 v[68:71], a[128:131], v[164:167], 0\n"
"v_mfma_f32_16x16x32_bf16 v[68:71], a[132:135], v[168:171], v[68:71]\n"
"v_mfma_f32_16x16x32_bf16 v[68:71], a[136:139], v[208:211], v[68:71]\n"
"buffer_load_dwordx4 v[80:83], v0, s[4:7], s97 offen offset:2048 sc1\n"
"v_mfma_f32_16x16x32_bf16 v[68:71], a[140:143], v[212:215], v[68:71]\n"
"s_waitcnt vmcnt(11)\n"
"v_mfma_f32_16x16x32_bf16 v[68:71], a[144:147], v[64:67], v[68:71]\n"
"v_mfma_f32_16x16x32_bf16 v[68:71], a[148:151], v[100:103], v[68:71]\n"
"ds_write_b128 v3, v[200:203] offset:3072\n"
"v_mfma_f32_16x16x32_bf16 v[68:71], a[152:155], v[60:63], v[68:71]\n"
"buffer_load_dwordx4 v[132:135], v0, s[4:7], s97 offen offset:3072 sc1\n"
"v_mfma_f32_16x16x32_bf16 v[68:71], a[156:159], v[76:79], v[68:71]\n"
"v_mfma_f32_16x16x32_bf16 v[140:143], a[188:191], v[76:79], 0\n"
"v_mfma_f32_16x16x32_bf16 v[140:143], a[184:187], v[60:63], v[140:143]\n"
"buffer_load_dwordx4 v[156:159], v1, s[4:7], s97 offen sc1\n"
"v_mfma_f32_16x16x32_bf16 v[140:143], a[180:183], v[100:103], v[140:143]\n"
"v_mfma_f32_16x16x32_bf16 v[140:143], a[176:179], v[64:67], v[140:143]\n"
"v_mfma_f32_16x16x32_bf16 v[140:143], a[172:175], v[212:215], v[140:143]\n"
"s_nop 1\n"
"v_cmp_u_f32_e32 vcc, v68, v68\n"
"v_mfma_f32_16x16x32_bf16 v[140:143], a[168:171], v[208:211], v[140:143]\n"
"s_cbranch_vccnz .Lmk_p60s\n"
".Lmk_p60r:\n"
"v_mfma_f32_16x16x32_bf16 v[140:143], a[164:167], v[168:171], v[140:143]\n"
"ds_write_b128 v3, v[68:71] offset:6144\n"
"v_mfma_f32_16x16x32_bf16 v[140:143], a[160:163], v[164:167], v[140:143]\n"
"buffer_load_dwordx4 v[68:71], v1, s[4:7], s97 offen offset:1024 sc1\n"
"v_mfma_f32_16x16x32_bf16 v[184:187], a[192:195], v[164:167], 0\n"
"v_mfma_f32_16x16x32_bf16 v[184:187], a[196:199], v[168:171], v[184:187]\n"
"v_mfma_f32_16x16x32_bf16 v[184:187], a[200:203], v[208:211], v[184:187]\n"
"buffer_load_dwordx4 v[200:203], v1, s[4:7], s97 offen offset:2048 sc1\n"
"v_mfma_f32_16x16x32_bf16 v[184:187], a[204:207], v[212:215], v[184:187]\n"
"v_mfma_f32_16x16x32_bf16 v[184:187], a[208:211], v[64:67], v[184:187]\n"
"ds_read_b128 v[204:207], v2\n"
"ds_read_b128 v[216:219], v2 offset:1024\n"
"v_mfma_f32_16x16x32_bf16 v[184:187], a[212:215], v[100:103], v[184:187]\n"
"ds_write_b128 v3, v[140:143] offset:7168\n"
"v_mfma_f32_16x16x32_bf16 v[184:187], a[216:219], v[60:63], v[184:187]\n"
"buffer_load_dwordx4 v[140:143], v1, s[4:7], s97 offen offset:3072 sc1\n"
"v_mfma_f32_16x16x32_bf16 v[184:187], a[220:223], v[76:79], v[184:187]\n"
"v_mfma_f32_16x16x32_bf16 v[220:223], a[252:255], v[76:79], 0\n"
"v_mfma_f32_16x16x32_bf16 v[220:223], a[248:251], v[60:63], v[220:223]\n"
"s_waitcnt lgkmcnt(1)\n"
"v_lshlrev_b32_e32 v254, 16, v204\n"
"v_and_b32_e32 v251, 0xffff0000, v204\n"
"v_lshlrev_b32_e32 v250, 16, v205\n"
"v_mfma_f32_16x16x32_bf16 v[220:223], a[244:247], v[100:103], v[220:223]\n"
"v_and_b32_e32 v249, 0xffff0000, v205\n"
"v_lshlrev_b32_e32 v248, 16, v206\n"
"v_and_b32_e32 v247, 0xffff0000, v206\n"
"v_mfma_f32_16x16x32_bf16 v[220:223], a[240:243], v[64:67], v[220:223]\n"
"v_lshlrev_b32_e32 v246, 16, v207\n"
"v_and_b32_e32 v245, 0xffff0000, v207\n"
"v_lshlrev_b32_e32 v244, 16, v216\n"
"v_mfma_f32_16x16x32_bf16 v[220:223], a[236:239], v[212:215], v[220:223]\n"
"s_add_u32 s97, s74, 0x40000\n"
"buffer_store_dwordx4 v[116:119], v253, s[16:19], s97 offen sc1\n"
"v_mfma_f32_16x16x32_bf16 v[220:223], a[232:235], v[208:211], v[220:223]\n"
"ds_write_b128 v3, v[184:187] offset:8192\n"
"v_mfma_f32_16x16x32_bf16 v[220:223], a[228:231], v[168:171], v[220:223]\n"
"v_and_b32_e32 v243, 0xffff0000, v216\n"
"v_lshlrev_b32_e32 v242, 16, v217\n"
"v_and_b32_e32 v241, 0xffff0000, v217\n"
"v_mfma_f32_16x16x32_bf16 v[220:223], a[224:227], v[164:167], v[220:223]\n"
"s_waitcnt vmcnt(13)\n"
"v_lshlrev_b32_e32 v240, 16, v218\n"
"v_and_b32_e32 v239, 0xffff0000, v218\n"
"v_lshlrev_b32_e32 v238, 16, v219\n"
"; @B G2 a.2\n"
"v_mfma_f32_16x16x32_bf16 v[60:63], a[128:131], v[96:99], 0\n"
"v_and_b32_e32 v237, 0xffff0000, v219\n"
"v_mfma_f32_16x16x32_bf16 v[60:63], a[132:135], v[112:115], v[60:63]\n"
"v_mfma_f32_16x16x32_bf16 v[60:63], a[136:139], v[52:55], v[60:63]\n"
"v_mfma_f32_16x16x32_bf16 v[60:63], a[140:143], v[152:155], v[60:63]\n"
"s_waitcnt vmcnt(9)\n"
"v_mfma_f32_16x16x32_bf16 v[60:63], a[144:147], v[84:87], v[60:63]\n"
"v_mfma_f32_16x16x32_bf16 v[60:63], a[148:151], v[172:175], v[60:63]\n"
"ds_write_b128 v3, v[220:223] offset:9216\n"
"v_mfma_f32_16x16x32_bf16 v[60:63], a[152:155], v[128:131], v[60:63]\n"
"s_add_u32 s97, s74, 0x44000\n"
"v_mfma_f32_16x16x32_bf16 v[60:63], a[156:159], v[72:75], v[60:63]\n"
"v_mfma_f32_16x16x32_bf16 v[64:67], a[188:191], v[72:75], 0\n"
"buffer_store_dwordx4 v[160:163], v253, s[16:19], s97 offen sc1\n"
"v_mfma_f32_16x16x32_bf16 v[64:67], a[184:187], v[128:131], v[64:67]\n"
"s_waitcnt lgkmcnt(0)\n"
"s_barrier\n"
"v_mfma_f32_16x16x32_bf16 v[64:67], a[180:183], v[172:175], v[64:67]\n"
"ds_read_b128 v[76:79], v4\n"
"ds_read_b128 v[100:103], v5\n"
"v_mfma_f32_16x16x32_bf16 v[64:67], a[176:179], v[84:87], v[64:67]\n"
"v_mfma_f32_16x16x32_bf16 v[64:67], a[172:175], v[152:155], v[64:67]\n"
"v_cmp_u_f32_e32 vcc, v60, v60\n"
"v_mfma_f32_16x16x32_bf16 v[64:67], a[168:171], v[52:55], v[64:67]\n"
"s_cbranch_vccnz .Lmk_p61s\n"
".Lmk_p61r:\n"
"v_mfma_f32_16x16x32_bf16 v[64:67], a[164:167], v[112:115], v[64:67]\n"
"ds_write_b128 v3, v[60:63] offset:12288\n"
"v_mfma_f32_16x16x32_bf16 v[64:67], a[160:163], v[96:99], v[64:67]\n"
"s_waitcnt lgkmcnt(1)\n"
"v_add_f32_e32 v76, v76, v254\n"
"v_add_f32_e32 v77, v77, v251\n"
"v_add_f32_e32 v78, v78, v250\n"
"v_mfma_f32_16x16x32_bf16 v[60:63], a[192:195], v[96:99], 0\n"
"ds_read_b128 v[116:119], v4 offset:1024\n"
"ds_read_b128 v[160:163], v5 offset:1024\n"
"v_add_f32_e32 v79, v79, v249\n"
"v_mfma_f32_16x16x32_bf16 v[60:63], a[196:199], v[112:115], v[60:63]\n"
"v_add_f32_e32 v76, v76, v100\n"
"v_add_f32_e32 v77, v77, v101\n"
"v_add_f32_e32 v78, v78, v102\n"
"v_mfma_f32_16x16x32_bf16 v[60:63], a[200:203], v[52:55], v[60:63]\n"
"v_add_f32_e32 v79, v79, v103\n"
"v_mfma_f32_16x16x32_bf16 v[60:63], a[204:207], v[152:155], v[60:63]\n"
"v_mfma_f32_16x16x32_bf16 v[60:63], a[208:211], v[84:87], v[60:63]\n"
"v_mfma_f32_16x16x32_bf16 v[60:63], a[212:215], v[172:175], v[60:63]\n"
"ds_write_b128 v3, v[64:67] offset:13312\n"
"v_mfma_f32_16x16x32_bf16 v[60:63], a[216:219], v[128:131], v[60:63]\n"
"ds_read_b128 v[64:67], v4 offset:2048\n"
"ds_read_b128 v[100:103], v5 offset:2048\n"
"s_waitcnt lgkmcnt(3)\n"
"v_mfma_f32_16x16x32_bf16 v[60:63], a[220:223], v[72:75], v[60:63]\n"
"v_add_f32_e32 v116, v116, v248\n"
"v_add_f32_e32 v117, v117, v247\n"
"v_add_f32_e32 v118, v118, v246\n"
"v_mfma_f32_16x16x32_bf16 v[164:167], a[252:255], v[72:75], 0\n"
"v_add_f32_e32 v119, v119, v245\n"
"v_add_f32_e32 v116, v116, v160\n"
"v_add_f32_e32 v117, v117, v161\n"
"v_mfma_f32_16x16x32_bf16 v[164:167], a[248:251], v[128:131], v[164:167]\n"
"v_add_f32_e32 v118, v118, v162\n"
"v_add_f32_e32 v119, v119, v163\n"
"v_mfma_f32_16x16x32_bf16 v[164:167], a[244:247], v[172:175], v[164:167]\n"
"v_mfma_f32_16x16x32_bf16 v[164:167], a[240:243], v[84:87], v[164:167]\n"
"s_waitcnt lgkmcnt(0)\n"
"v_add_f32_e32 v64, v64, v244\n"
"v_add_f32_e32 v65, v65, v243\n"
"v_add_f32_e32 v66, v66, v242\n"
"v_mfma_f32_16x16x32_bf16 v[164:167], a[236:239], v[152:155], v[164:167]\n"
"ds_read_b128 v[72:75], v4 offset:3072\n"
"ds_read_b128 v[84:87], v5 offset:3072\n"
"v_add_f32_e32 v67, v67, v241\n"
"v_mfma_f32_16x16x32_bf16 v[164:167], a[232:235], v[52:55], v[164:167]\n"
"ds_write_b128 v3, v[60:63] offset:14336\n"
"v_mfma_f32_16x16x32_bf16 v[164:167], a[228:231], v[112:115], v[164:167]\n"
"v_add_f32_e32 v64, v64, v100\n"
"v_add_f32_e32 v65, v65, v101\n"
"v_add_f32_e32 v66, v66, v102\n"
"v_mfma_f32_16x16x32_bf16 v[164:167], a[224:227], v[96:99], v[164:167]\n"
"s_waitcnt vmcnt(6)\n"
"v_add_f32_e32 v67, v67, v103\n"
"; @B G2 a.3\n"
"v_mfma_f32_16x16x32_bf16 v[52:55], a[128:131], v[36:39], v[76:79]\n"
"v_mfma_f32_16x16x32_bf16 v[52:55], a[132:135], v[40:43], v[52:55]\n"
"s_waitcnt lgkmcnt(1)\n"
"v_add_f32_e32 v72, v72, v240\n"
"v_add_f32_e32 v73, v73, v239\n"
"v_add_f32_e32 v74, v74, v238\n"
"v_mfma_f32_16x16x32_bf16 v[52:55], a[136:139], v[80:83], v[52:55]\n"
"v_add_f32_e32 v75, v75, v237\n"
"v_add_f32_e32 v72, v72, v84\n"
"v_add_f32_e32 v73, v73, v85\n"
"v_mfma_f32_16x16x32_bf16 v[52:55], a[140:143], v[132:135], v[52:55]\n"
"s_waitcnt vmcnt(2)\n"
"v_add_f32_e32 v74, v74, v86\n"
"v_add_f32_e32 v75, v75, v87\n"
"v_mfma_f32_16x16x32_bf16 v[52:55], a[144:147], v[156:159], v[52:55]\n"
"v_mfma_f32_16x16x32_bf16 v[52:55], a[148:151], v[68:71], v[52:55]\n"
"ds_write_b128 v3, v[164:167] offset:15360\n"
"v_mfma_f32_16x16x32_bf16 v[52:55], a[152:155], v[200:203], v[52:55]\n"
"v_mfma_f32_16x16x32_bf16 v[52:55], a[156:159], v[140:143], v[52:55]\n"
"v_mfma_f32_16x16x32_bf16 v[60:63], a[188:191], v[140:143], v[116:119]\n"
"v_mfma_f32_16x16x32_bf16 v[60:63], a[184:187], v[200:203], v[60:63]\n"
"s_waitcnt lgkmcnt(0)\n"
"s_barrier\n"
"v_mfma_f32_16x16x32_bf16 v[60:63], a[180:183], v[68:71], v[60:63]\n"
"ds_read_b128 v[84:87], v6\n"
"ds_read_b128 v[96:99], v6 offset:1024\n"
"v_mfma_f32_16x16x32_bf16 v[60:63], a[176:179], v[156:159], v[60:63]\n"
"ds_read_b128 v[100:103], v6 offset:2048\n"
"ds_read_b128 v[112:115], v6 offset:3072\n"
"v_mfma_f32_16x16x32_bf16 v[60:63], a[172:175], v[132:135], v[60:63]\n"
"v_cmp_u_f32_e32 vcc, v52, v52\n"
"v_mfma_f32_16x16x32_bf16 v[60:63], a[168:171], v[80:83], v[60:63]\n"
"s_cbranch_vccnz .Lmk_p62s\n"
".Lmk_p62r:\n"
"v_mfma_f32_16x16x32_bf16 v[60:63], a[164:167], v[40:43], v[60:63]\n"
"v_mfma_f32_16x16x32_bf16 v[60:63], a[160:163], v[36:39], v[60:63]\n"
"v_mfma_f32_16x16x32_bf16 v[64:67], a[192:195], v[36:39], v[64:67]\n"
"v_mfma_f32_16x16x32_bf16 v[64:67], a[196:199], v[40:43], v[64:67]\n"
"v_mfma_f32_16x16x32_bf16 v[64:67], a[200:203], v[80:83], v[64:67]\n"
"v_mfma_f32_16x16x32_bf16 v[64:67], a[204:207], v[132:135], v[64:67]\n"
"v_mfma_f32_16x16x32_bf16 v[64:67], a[208:211], v[156:159], v[64:67]\n"
"s_waitcnt lgkmcnt(0)\n"
"v_add_f32_e32 v84, v52, v84\n"
"v_add_f32_e32 v85, v53, v85\n"
"v_add_f32_e32 v86, v54, v86\n"
"v_mfma_f32_16x16x32_bf16 v[64:67], a[212:215], v[68:71], v[64:67]\n"
"v_add_f32_e32 v87, v55, v87\n"
"v_add_f32_e32 v96, v60, v96\n"
"v_add_f32_e32 v97, v61, v97\n"
"v_mfma_f32_16x16x32_bf16 v[64:67], a[216:219], v[200:203], v[64:67]\n"
"v_add_f32_e32 v98, v62, v98\n"
"v_add_f32_e32 v99, v63, v99\n"
"v_cvt_pk_bf16_f32 v52, v84, v85\n"
"v_mfma_f32_16x16x32_bf16 v[64:67], a[220:223], v[140:143], v[64:67]\n"
"v_cvt_pk_bf16_f32 v53, v86, v87\n"
"v_cvt_pk_bf16_f32 v54, v96, v97\n"
"v_cvt_pk_bf16_f32 v55, v98, v99\n"
"v_mfma_f32_16x16x32_bf16 v[72:75], a[252:255], v[140:143], v[72:75]\n"
"v_xor_b32_e32 v254, 32, v255\n"
"v_mfma_f32_16x16x32_bf16 v[72:75], a[248:251], v[200:203], v[72:75]\n"
"ds_write_b64 v255, v[52:53] offset:0\n"
"v_mfma_f32_16x16x32_bf16 v[72:75], a[244:247], v[68:71], v[72:75]\n"
"ds_write_b64 v254, v[54:55] offset:0\n"
"v_mfma_f32_16x16x32_bf16 v[72:75], a[240:243], v[156:159], v[72:75]\n"
"v_mfma_f32_16x16x32_bf16 v[72:75], a[236:239], v[132:135], v[72:75]\n"
"v_mfma_f32_16x16x32_bf16 v[72:75], a[232:235], v[80:83], v[72:75]\n"
"v_mfma_f32_16x16x32_bf16 v[72:75], a[228:231], v[40:43], v[72:75]\n"
"v_mfma_f32_16x16x32_bf16 v[72:75], a[224:227], v[36:39], v[72:75]\n"
"v_add_f32_e32 v100, v64, v100\n"
"v_add_f32_e32 v101, v65, v101\n"
"v_add_f32_e32 v102, v66, v102\n"
"v_add_f32_e32 v103, v67, v103\n"
"s_nop 3\n"
"v_add_f32_e32 v112, v72, v112\n"
"v_add_f32_e32 v113, v73, v113\n"
"v_add_f32_e32 v114, v74, v114\n"
"v_add_f32_e32 v115, v75, v115\n"
"v_cvt_pk_bf16_f32 v36, v100, v101\n"
"v_cvt_pk_bf16_f32 v37, v102, v103\n"
"v_cvt_pk_bf16_f32 v38, v112, v113\n"
"v_cvt_pk_bf16_f32 v39, v114, v115\n"
"v_xor_b32_e32 v254, 64, v255\n"
"v_xor_b32_e32 v253, 0x60, v255\n"
"ds_write_b64 v254, v[36:37] offset:0\n"
"ds_write_b64 v253, v[38:39] offset:0\n"
"ds_read_b128 v[36:39], v2\n"
"ds_read_b128 v[40:43], v2 offset:1024\n"
"s_waitcnt lgkmcnt(0)\n"
"buffer_store_dwordx4 v[36:39], v252, s[16:19], s74 offen sc1\n"
"s_add_u32 s97, s74, 0x4000\n"
"buffer_store_dwordx4 v[40:43], v252, s[16:19], s97 offen sc1\n"
"s_cmp_eq_u32 s81, 0\n"
"s_cbranch_scc1 .Lmk_fin\n"
"buffer_store_dwordx2 v[18:19], v11, s[4:7], s81 offen sc1\n"
".Lmk_fin:\n"
"s_endpgm\n"
".Lmk_end:\n"
    :: "{s[4:7]}"(wsr), "{s[8:11]}"(wsr), "{s[12:15]}"(wsk), "{s[16:19]}"(outr), "{s[52:67]}"(sa), "{s[68:83]}"(sb),
       "{s[20:27]}"(pa), "{s[28:31]}"(wa), "{s[32:35]}"(wb), "{s[36:39]}"(oa), "{s[40:43]}"(ob), "{s[44:51]}"(pc), "{s99}"(uni((bix * 4 + w) * 1024)),
       "{v[0:15]}"(va), "{v[16:23]}"(vb), "{v[32:47]}"(pz), "{v48}"(sta), "{v50}"(pf)
    : "memory", "v255", "a255", "s98", "vcc");
  __builtin_unreachable();
}

}

extern "C" void kernel_launch(void* const* d_in, const int* in_sizes, int n_in, void* d_out, int out_size,
                              void* d_ws, size_t ws_size, hipStream_t stream) {
  (void)hipFuncSetAttribute((const void*)mega_kernel, hipFuncAttributeMaxDynamicSharedMemorySize, LDS_BYTES);
  mega_kernel<<<256, 256, LDS_BYTES, stream>>>((const u16*)d_in[0], (const u16*)d_in[1], (const u16*)d_in[2],
                                               (const u16*)d_in[4], (const u16*)d_in[5], (u16*)d_out, (char*)d_ws);
}
```

```cpp
#include <hip/hip_runtime.h>
#include <stdint.h>

namespace {

typedef unsigned short u16;
typedef unsigned long long u64;
typedef short s16x4 __attribute__((ext_vector_type(4)));
typedef float f32x4 __attribute__((ext_vector_type(4)));
typedef unsigned u32x4 __attribute__((ext_vector_type(4)));
typedef unsigned u32x2 __attribute__((ext_vector_type(2)));
typedef unsigned u32x8 __attribute__((ext_vector_type(8)));
typedef unsigned u32x16 __attribute__((ext_vector_type(16)));
typedef __amdgpu_buffer_rsrc_t rsrc_t;
#define LAS __attribute__((address_space(3)))
#define DI __device__ __forceinline__
#define LDSP(a) ((LAS char*)(size_t)(a))

constexpr unsigned MB = 1u << 20;
constexpr unsigned WS_XF = 0;
constexpr unsigned WS_OF = 16 * MB;
constexpr unsigned WS_KV = 32 * MB;
constexpr unsigned WS_WF = 64 * MB;
constexpr unsigned WS_WO = 76 * MB;
constexpr unsigned WS_ST = 80 * MB;
constexpr unsigned WS_CS = 81 * MB;
constexpr unsigned WS_FL = 82 * MB;
constexpr unsigned WS_EP = WS_FL + 2048 * 128;
constexpr unsigned WS_DBG = 96 * MB;
constexpr unsigned OOB = 0x80000000u;
constexpr int L_RES = 0, L_K = 16384, L_V = 49152, L_INB = 81920, L_CS = 155648, L_PM = L_CS + 1024, L_MB = L_PM + 2048, L_SCR = L_MB + 2048;
constexpr int L_WS = 57344;
constexpr int LDS_BYTES = 163840;

template <int N> DI void wait_vm() { asm volatile("s_waitcnt vmcnt(%0)" ::"n"(N) : "memory"); }
template <int N> DI void nops() { asm volatile("s_nop %0" ::"n"(N - 1)); }
template <int OFF> DI void ld8(u32x2& d, rsrc_t r, unsigned v, unsigned s) {
  asm volatile("buffer_load_dwordx2 %0, %1, %2, %3 offen offset:%4 sc1" : "=v"(d) : "v"(v), "s"(r), "s"(s), "i"(OFF) : "memory");
}
template <int OFF> DI void st16(const u32x4& d, rsrc_t r, unsigned v, unsigned s) {
  asm volatile("buffer_store_dwordx4 %0, %1, %2, %3 offen offset:%4 sc1\n\ts_nop 1" :: "v"(d), "v"(v), "s"(r), "s"(s), "i"(OFF) : "memory");
}
template <int OFF> DI void st8(const u32x2& d, rsrc_t r, unsigned v, unsigned s) {
  asm volatile("buffer_store_dwordx2 %0, %1, %2, %3 offen offset:%4 sc1" :: "v"(d), "v"(v), "s"(r), "s"(s), "i"(OFF) : "memory");
}
DI void st4(unsigned d, rsrc_t r, unsigned v, unsigned s) {
  asm volatile("buffer_store_dword %0, %1, %2, %3 offen sc1" :: "v"(d), "v"(v), "s"(r), "s"(s) : "memory");
}
DI void dma(rsrc_t r, unsigned v, unsigned s, unsigned lm) {
  asm volatile("s_mov_b32 m0, %3\n\ts_nop 0\n\tbuffer_load_dwordx4 %0, %1, %2 offen lds" :: "v"(v), "s"(r), "s"(s), "s"(lm) : "memory");
}
DI void touch(rsrc_t r, unsigned v) {
  asm volatile("s_mov_b32 m0, %2\n\ts_nop 0\n\tbuffer_load_dword %0, %1, 0 offen lds" :: "v"(v), "s"(r), "s"((unsigned)L_SCR) : "memory");
}
DI void mma_v(f32x4& c, const u32x4& a, const u32x4& b) { asm volatile("v_mfma_f32_16x16x32_bf16 %0, %1, %2, %0" : "+v"(c) : "v"(a), "v"(b)); }
DI void mm0_v(f32x4& c, const u32x4& a, const u32x4& b) { asm volatile("v_mfma_f32_16x16x32_bf16 %0, %1, %2, 0" : "=v"(c) : "v"(a), "v"(b)); }
DI unsigned uni(unsigned v) { v = __builtin_amdgcn_readfirstlane(v); asm volatile("" : "+s"(v)); return v; }
template <class T> DI void pin(T& v) { asm volatile("" : "+v"(v)); }
DI float sum_x16(float v) {
  auto r = __builtin_amdgcn_permlane16_swap(__float_as_uint(v), __float_as_uint(v), false, false);
  return __uint_as_float(r[0]) + __uint_as_float(r[1]);
}
DI float sum_x32(float v) {
  auto r = __builtin_amdgcn_permlane32_swap(__float_as_uint(v), __float_as_uint(v), false, false);
  return __uint_as_float(r[0]) + __uint_as_float(r[1]);
}
DI u32x2 lds_tr(unsigned a) { return __builtin_bit_cast(u32x2, __builtin_amdgcn_ds_read_tr16_b64_v4i16((LAS s16x4*)LDSP(a))); }
DI u32x4 cat(const u32x2& a, const u32x2& b) { return __builtin_shufflevector(a, b, 0, 1, 2, 3); }
DI u32x2 lds_r2(unsigned a) { return *(LAS u32x2*)LDSP(a); }
DI void lds_w4(unsigned a, const u32x4& v) { *(LAS u32x4*)LDSP(a) = v; }
DI unsigned rot(unsigned g, unsigned sl) { return (g + (0x641u >> 4 * sl)) & 15; }
DI int swzK(int u) { return ((((u >> 1) ^ u) & 1) << 1) | (u >> 1); }

#define STAMP(i)

__global__ __launch_bounds__(256, 1) void mega_kernel(const u16* __restrict__ x0, const u16* __restrict__ WqA,
                                                      const u16* __restrict__ WoA, const u16* __restrict__ WqB,
                                                      const u16* __restrict__ WoB, u16* __restrict__ out, char* ws) {
  const unsigned bid = blockIdx.x, w = __builtin_amdgcn_readfirstlane(threadIdx.x >> 6);
  unsigned lane = threadIdx.x & 63;
  unsigned stw = 0;
  STAMP(0);
  u32x2 tag;
  {
    u64 t0; unsigned t1, t2, t3, v1, v2;
    asm volatile(
      "v_lshlrev_b32 %[v1], 4, %[lane]\n"
      "v_lshrrev_b32 %[v2], 3, %[lane]\n"
      "s_getpc_b64 %[t0]\n.Lmk_here:\n"
      "s_lshr_b32 %[t1], %[bid], 3\n s_lshl2_add_u32 %[t1], %[t1], %[w]\n s_lshl_b32 %[t1], %[t1], 10\n"
      "s_mov_b64 s[88:89], %[t0]\n s_and_b32 s88, s88, -16\n s_and_b32 s89, s89, 0xffff\n s_mov_b32 s90, 8192\n s_mov_b32 s91, 0x20000\n"
      "s_mov_b32 m0, %[scr]\n s_nop 0\n"
      "buffer_load_dwordx4 %[v1], s[88:91], %[t1] offen lds\n"
      "v_and_b32 %[v1], 0x70, %[v1]\n v_lshl_or_b32 %[v1], %[v2], 11, %[v1]\n"
      "s_and_b32 %[t1], %[bid], 4\n s_lshr_b32 %[t2], %[bid], 6\n s_add_u32 %[t1], %[t1], %[t2]\n s_lshl3_add_u32 %[t1], %[t1], %[w]\n"
      "s_and_b32 %[t3], %[bid], 1\n s_lshl1_add_u32 %[t1], %[t1], %[t3]\n s_lshl_b32 %[t1], %[t1], 8\n"
      "s_bfe_u32 %[t3], %[bid], 0x30003\n s_add_u32 %[t1], %[t1], %[t3]\n s_and_b32 %[t3], %[bid], 2\n s_lshl2_add_u32 %[t1], %[t3], %[t1]\n s_lshl_b32 %[t1], %[t1], 7\n"
      "s_lshl_b32 %[t3], %[w], 11\n"
      : [t0] "=&s"(t0), [t1] "=&s"(t1), [t2] "=&s"(t2), [t3] "=&s"(t3), [v1] "=&v"(v1), [v2] "=&v"(v2)
      : [bid] "s"(bid), [w] "s"(w), [lane] "v"(lane), [scr] "i"(L_SCR + 1024) : "memory", "s88", "s89", "s90", "s91", "scc");
    v2 = v2 * 6144 + (lane & 7) * 16;
    const unsigned g_ = (bid & 3) * 4 + (bid >> 4 & 3);
    const unsigned src = (((bid & 4) + (bid >> 6)) * 128 + (bid >> 3 & 1) * 64 + w * 16) * 6144, dst = L_INB + w * 2048;
    const unsigned r0 = src + rot(g_, 0) * 128, r1 = src + 2048 + rot(g_, 1) * 128, r2 = src + 4096 + rot(g_, 2) * 128;
    asm volatile(
      "s_mov_b64 s[88:89], %[wq]\n s_and_b32 s89, s89, 0xffff\n s_mov_b32 s90, 0x600000\n s_mov_b32 s91, 0x20000\n"
      "s_mov_b32 m0, %[dst]\n s_add_u32 %[t2], %[r0], 0xc000\n buffer_load_dwordx4 %[v2], s[88:91], %[r0] offen lds\n"
      "s_add_u32 m0, %[dst], 0x400\n s_nop 0\n buffer_load_dwordx4 %[v2], s[88:91], %[t2] offen lds\n"
      "s_add_u32 m0, %[dst], 0x2000\n s_add_u32 %[t2], %[r1], 0xc000\n buffer_load_dwordx4 %[v2], s[88:91], %[r1] offen lds\n"
      "s_add_u32 m0, %[dst], 0x2400\n s_nop 0\n buffer_load_dwordx4 %[v2], s[88:91], %[t2] offen lds\n"
      "s_add_u32 m0, %[dst], 0x4000\n s_add_u32 %[t2], %[r2], 0xc000\n buffer_load_dwordx4 %[v2], s[88:91], %[r2] offen lds\n"
      "s_add_u32 m0, %[dst], 0x4400\n s_nop 0\n buffer_load_dwordx4 %[v2], s[88:91], %[t2] offen lds\n"
      "s_mov_b64 s[88:89], %[x]\n s_and_b32 s89, s89, 0xffff\n s_mov_b32 s90, 0x400000\n s_mov_b32 s91, 0x20000\n"
      "s_add_u32 m0, %[t3], 0x2000\n s_add_u32 %[t2], %[t1], 0x40000\n buffer_load_dwordx4 %[v1], s[88:91], %[t2] offen lds\n"
      "s_add_u32 m0, %[t3], 0x2400\n s_add_u32 %[t2], %[t1], 0x44000\n buffer_load_dwordx4 %[v1], s[88:91], %[t2] offen lds\n"
      "s_mov_b32 m0, %[t3]\n s_add_u32 %[t2], %[t1], 0x4000\n buffer_load_dwordx4 %[v1], s[88:91], %[t1] offen lds\n"
      "s_add_u32 m0, %[t3], 0x400\n s_mov_b32 %[t1], %[ep]\n buffer_load_dwordx4 %[v1], s[88:91], %[t2] offen lds\n"
      "s_mov_b64 s[88:89], %[ws]\n s_and_b32 s89, s89, 0xffff\n s_mov_b32 s90, 0x8000000\n"
      "buffer_load_dwordx2 %[tag], off, s[88:91], %[t1] sc1\n"
      : [t1] "+s"(t1), [t2] "+s"(t2), [tag] "=&v"(tag), [lane] "+v"(lane)
      : [t3] "s"(t3), [v1] "v"(v1), [v2] "v"(v2), [x] "s"(x0), [ws] "s"(ws), [wq] "s"(WqA), [dst] "s"(dst), [ep] "i"(WS_EP),
        [r0] "s"(r0), [r1] "s"(r1), [r2] "s"(r2),
        "s"(WoA), "s"(WqB), "s"(WoB), "s"(out) : "memory", "s88", "s89", "s90", "s91", "scc");
  }
  const unsigned xcd = bid & 7, bix = bid >> 3;
  const unsigned c = (xcd >> 2) * 4 + (bix >> 3), h = (xcd & 3) * 8 + (bix & 7), par = h & 1, g = h >> 1;
  const unsigned p2 = h >> 3 & 1, g2 = (h >> 4) * 8 + (h & 7);
  const unsigned fr = lane & 15, fq = lane >> 4;
  const unsigned dl = 0, wr = (w + dl) & 3;
  const rsrc_t wsr = __builtin_amdgcn_make_buffer_rsrc(ws, 0, 128 * MB, 0x00020000);
  const unsigned l16 = lane * 16, lw = w * 2048 + l16;
  const unsigned fl0 = lane == 0 ? 0u : OOB;
  const unsigned myfl = WS_FL + (c * 64 + h) * 128 + w * 8;
  const unsigned osrc0 = (lane >> 3) * 2048 + (lane & 7) * 16;
  const rsrc_t wa = __builtin_amdgcn_make_buffer_rsrc((void*)WqA, 0, 6 * MB, 0x00020000);
  const unsigned rho = 0, wbase = w * 256 * 6144 + h * 64;
  const unsigned wv = (lane >> 2) * 6144 + ((lane & 3) ^ ((lane >> 3) & 2)) * 16;
  auto piece = [&](int q, int j) __attribute__((always_inline)) {
    const unsigned k = j ^ par, src = uni(wbase + (((rho + 2 * q) & 7) + k) * 196608), dst = uni(q + k ? L_INB + w * 18432 + (2 * q + k - 1) * 6144 : L_WS + w * 6144);
#pragma unroll
    for (int i = 0; i < 6; ++i) dma(wa, wv, src + (i & 1) * 98304 + (i >> 1) * 2048, dst + i * 1024);
  };
  const unsigned kb = 2 * c + par;
  const rsrc_t oa = __builtin_amdgcn_make_buffer_rsrc((void*)WoA, 0, 2 * MB, 0x00020000);
  const rsrc_t wb = __builtin_amdgcn_make_buffer_rsrc((void*)WqB, 0, 6 * MB, 0x00020000);
  const rsrc_t ob = __builtin_amdgcn_make_buffer_rsrc((void*)WoB, 0, 2 * MB, 0x00020000);
  const unsigned wsrc = (w * 16 + (lane >> 3)) * 6144 + (lane & 7) * 16, osrc = w * 16 * 2048 + osrc0;
  auto get_o = [&](int t) __attribute__((always_inline)) {
#pragma unroll
    for (int i = 0; i < 2; ++i) dma(t ? ob : oa, osrc, (kb * 64 + i * 8) * 2048 + rot(g, 1 + t) * 128, L_V + 8192 + t * 8192 + w * 2048 + i * 1024);
  };
  auto get_q = [&](int t) __attribute__((always_inline)) {
#pragma unroll
    for (int i = 0; i < 6; ++i)
      dma(t ? wb : wa, wsrc, (kb * 64 + (i & 1) * 8) * 6144 + (i >> 1) * 2048 + rot(g, i >> 1) * 128, (t ? L_K : L_INB) + (i >> 1) * 8192 + w * 2048 + (i & 1) * 1024);
  };
  auto code = [&]() __attribute__((always_inline)) {
    u64 pc_; unsigned len_;
    asm volatile("s_getpc_b64 %0\n.Lmk_h1:\n\ts_mov_b32 %1, .Lmk_end-.Lmk_h1" : "=s"(pc_), "=s"(len_));
    dma(__builtin_amdgcn_make_buffer_rsrc((void*)(pc_ & ~15ull), 0, len_ & ~15u, 0x00020000), l16, (bix * 4 + w) * 1024, L_SCR + 1024);
  };
  const unsigned fr8 = lane < 32 ? lane * 4 : OOB;
  u32x4 ones = {0x3F803F80u, 0x3F803F80u, 0x3F803F80u, 0x3F803F80u};
  unsigned sta = 0;
  auto publish = [&](int s, bool now) __attribute__((always_inline)) {
    const unsigned a = L_RES + s * 8192 + w * 2048 + fr * 128 + fq * 8, T = c * 16 + 8 * s + 2 * wr + p2;
    u32x4 y0 = cat(lds_r2(a), lds_r2(a + 32)), y1 = cat(lds_r2(a + 64), lds_r2(a + 96));
    f32x4 sm, gm;
    pin(y0); pin(y1); pin(ones);
    nops<2>();
    mm0_v(sm, ones, y0); mm0_v(gm, y0, y0); mma_v(sm, ones, y1); mma_v(gm, y1, y1);
    if (now) { st16<0>(y0, wsr, l16, WS_XF + T * 32768 + g2 * 2048); st16<1024>(y1, wsr, l16, WS_XF + T * 32768 + g2 * 2048); }
    lds_w4(lw + L_RES + s * 8192, y0); lds_w4(lw + L_RES + s * 8192 + 1024, y1);
    nops<8>();
    pin(sm); pin(gm);
    const float sq = sum_x32(sum_x16(fq != fr >> 2 ? 0.f : fr & 2 ? (fr & 1 ? gm[3] : gm[2]) : (fr & 1 ? gm[1] : gm[0])));
    const unsigned st = __float_as_uint(fq ? sq : sm[0]);
    if (now) st4(st, wsr, fr8, WS_ST + T * 2048 + g2 * 128); else sta = st;
  };
  auto next_tag = [&]() __attribute__((always_inline)) {
    pin(tag);
    const u64 t = (((u64)tag[1] << 32) | tag[0]) + 1; tag = (u32x2){(unsigned)t, (unsigned)(t >> 32)};
  };
  auto line = [&](unsigned i) __attribute__((always_inline)) {
    asm volatile("s_waitcnt lgkmcnt(0)\n\ts_barrier" ::: "memory");
    st16<0>(cat(tag, tag), wsr, w == 0 && lane < 8 ? l16 : OOB, WS_FL + 524288 + (c * 64 + i) * 128);
  };
  const unsigned cb = c * 16 * 32768, skv = WS_KV + (c * 32 + h) * 32768;
  const unsigned soo = WS_OF + cb + wr * 65536 + h * 1024, sxo = WS_XF + cb + (2 * wr + p2) * 32768 + g2 * 2048, sso = WS_ST + (c * 16 + 2 * wr + p2) * 2048 + g2 * 128;
  auto clear = [&](int from, int to) __attribute__((always_inline)) {
    u32x4 z = {~0u, ~0u, ~0u, ~0u};
    pin(z);
#pragma unroll
    for (int k = from; k < to; ++k) {
      if (k < 8) st16<0>(z, wsr, l16, soo + (k == 4 ? 9 : k == 5 ? 8 : k - 6) * 32768);
      else if (k < 10) st16<0>(z, wsr, l16, sxo + 4 * MB + 8 * 32768 + (k - 8) * 1024);
      else st4(z[0], wsr, fr8, sso + 136 * 2048);
    }
  };
  unsigned ps[16];
  u32x8 pv = {};
#pragma unroll
  for (int j = 0; j < 3; ++j) {
    const unsigned ct = (w & 1) * 3 + j;
    pv[1 + j] = L_K + (ct >> 1) * 8192 + (4 * fq + (fr >> 2)) * 128 + (w >> 1) * 64 + (ct & 1) * 32 + (lane & 3) * 8;
    ps[j] = WS_WF + 6 * MB + ((2 * rot(g, ct >> 1) + (w >> 1)) * 192 + (kb >> 2) * 48 + (ct + 4) % 6 * 8 + (kb & 3) * 2) * 1024;
    ps[10 + j] = kb * 64 * 6144 + j * 2048 + rot(g, j) * 128;
  }
  { const unsigned ct = (w & 1) * 3 + fq;
    pv[4] = ((2 * rot(g, ct >> 1) + (w >> 1)) * 16384 + ct * 128 + fr * 8) | (fq < 3 ? 0u : OOB); }
  ps[3] = WS_CS + 524288 + kb * 1024;
  auto convert = [&]() __attribute__((always_inline)) {
    f32x4 cs[3];
    u32x4 f[2][3];
    asm volatile("s_waitcnt lgkmcnt(0)\n\ts_barrier" ::: "memory");
#pragma unroll
    for (int kk = 0; kk < 2; ++kk)
#pragma unroll
      for (int j = 0; j < 3; ++j) {
        const unsigned a = pv[1 + j] + (L_INB - L_K) + kk * 4096;
        f[kk][j] = cat(lds_tr(a), lds_tr(a + 2048));
      }
    pin(ones);
#pragma unroll
    for (int kk = 0; kk < 2; ++kk)
#pragma unroll
      for (int j = 0; j < 3; ++j) {
        if (kk) mma_v(cs[j], ones, f[kk][j]); else mm0_v(cs[j], ones, f[kk][j]);
        st16<0>(f[kk][j], wsr, l16, uni(ps[j] - 6 * MB + kk * 1024));
      }
    nops<8>();
    pin(cs[0]); pin(cs[1]); pin(cs[2]);
    const unsigned v = __float_as_uint(fq == 0 ? cs[0][0] : fq == 1 ? cs[1][0] : cs[2][0]);
    st8<0>((u32x2){v, v}, wsr, pv[4], uni(ps[3] - 524288));
  };
  ;
  ;
  touch(wsr, lane < 2 ? WS_FL + (w * 512 + c * 64 + lane * 32 + h) * 128 : OOB);
  STAMP(29);

  ps[6] = (c >> 2) | par << 1 | (bix * 4 + w) << 10;                                            ps[7] = L_WS + w * 6144;
  ps[8] = wbase;                                             ps[9] = rho;
  ps[13] = kb * 64 * 2048 + rot(g, 1) * 128;                 ps[14] = kb * 64 * 2048 + rot(g, 2) * 128;
  ps[15] = WS_WF + (h * 4 + w) * 49152 + (rho + (c >> 2)) * 1024;
  pv[5] = wsrc;                                              pv[6] = osrc;
  u32x16 pw = {wv, L_WS + w * 6144 + (4 * fq + (fr >> 2)) * 64 + (fq & 1) * 32 + (lane & 3) * 8,
                    l16 + rho * 1024, l16 + ((rho + 2) & 7) * 1024, l16 + ((rho + 4) & 7) * 1024, l16 + ((rho + 6) & 7) * 1024,
                    L_WS + w * 6144 + (fr == 0 ? fq * 16 : 1024 + l16), L_WS + (w * 24 + (lane < 24 ? lane : 0)) * 4,
                    (w * 128 + (lane >> 3 & 1) * 64 + 2 * ((g2 - rot(0, 1 + (fq & 1))) & 15) + (lane >> 2 & 1)) * 128 + (lane & 3) * 8};
  if (w < 2) {
    u32x4 pm;
#pragma unroll
    for (int k = 0; k < 4; ++k)
      pm[k] = (8 * fq + 2 * k > 16 * w + fr ? 0xFFFFu : 0u) | (8 * fq + 2 * k + 1 > 16 * w + fr ? 0xFFFF0000u : 0u);
    lds_w4(L_PM + w * 1024 + l16, pm);
  } else {
    const unsigned want = (unsigned)((0x5555ull << 16) >> (fr + 25 - 8 * fq));
    u32x4 mb;
#pragma unroll
    for (int k = 0; k < 4; ++k) mb[k] = (want >> (15 - 4 * w - k)) & 1 ? 0u : 0xFF800000u;
    lds_w4(L_MB + (w - 2) * 1024 + l16, mb);
  }
  pv[0] = L_V + 8192 + (16 * (fq & 1) + 4 * (fq >> 1) + (fr >> 2)) * 128 + w * 32 + (lane & 3) * 8;
  pv[7] = lane == 0 ? 32 * 128 : OOB;
  ps[4] = WS_WO + (rot(g, 1) * 128 + (kb >> 2) * 32 + w * 8 + (kb & 3) * 2) * 1024;
  ps[5] = WS_WO + 2 * MB + (rot(g, 2) * 128 + (kb >> 2) * 32 + w * 8 + (kb & 3) * 2) * 1024;
  const unsigned krow = (8 * (fr >> 2) + (fr & 3)) * 64 + wr * 2048 + L_K;
  const unsigned kv0 = 16384 + wr * 2048 + fr * 64 + ((fq ^ ((fr >> 3) << 1) ^ ((fr >> 1) & 3)) * 16);
  u32x16 va = {l16, l16 + 4096, lw, L_INB + w * 18432 + l16, L_INB + ((w + 3) & 3) * 18432 + l16, L_INB + ((w + 2) & 3) * 18432 + 6144 + l16,
                     L_INB + ((w + 1) & 3) * 18432 + 12288 + l16, kv0, kv0 ^ 48, L_CS + fq * 16, lane * 4, fl0,
                     ((lane & 31) >> 2) * 128 + (lane & 3) * 8,
                     fq < 3 ? (w * 128 + (lane >> 3 & 1) * 64 + 2 * ((2 * g - rot(g, fq % 3)) & 15) + (lane >> 2 & 1)) * 128 + (lane & 3) * 8 : ((lane & 7) * 64 + h) * 128 + w * 8,
                     fr8, krow + (fq ^ swzK(fr >> 2) ^ ((fr >> 1) & 1)) * 16};
  u32x8 vb = {L_V + wr * 2048 + (8 * fq + (fr >> 2)) * 64 + 8 * (lane & 1) + (((lane >> 1) & 1) ^ ((fq & 1) << 1) ^ (fr >> 3)) * 16,
                    krow + (fq ^ swzK(((fr >> 2) + 2) & 3) ^ ((fr >> 1) & 1)) * 16, 0, 0, 0,
                    lane < 24 ? L_CS + w * 96 + lane * 4 : L_SCR, w == 0 ? bid * 1024 + lane * 4 : OOB, 0};
  u32x16 pz = {pw[2], pw[3], pw[4], pw[5], pv[1], pv[2], pv[3], pv[4], pv[0], pw[8], pw[0], pw[1], pw[6], pw[7], pv[5], pv[6]};
  unsigned pf = pv[7];
  pin(va); pin(vb); pin(pz); pin(pf);
  wait_vm<6>();
  STAMP(18);
  convert();
  wait_vm<8>();
  next_tag();
  publish(1, true);
  STAMP(1);
  wait_vm<3>();
  st8<96>(tag, wsr, fl0, myfl);
  get_o(0);
  get_o(1);
  get_q(1);
  code();
  publish(0, false);
  wait_vm<12>();
  st8<0>(tag, wsr, lane == 0 ? 32 * 128 : OOB, myfl + (2 * g2 + p2 - h) * 128);
  STAMP(15);

  u32x16 sa, sb;
#pragma unroll
  for (int i = 0; i < 4; ++i) {
    sa[i] = WS_XF + cb + w * 8192 + ((wr + 1 + i) & 3) * 65536;
    sa[4 + i] = sa[i] + WS_OF + p2 * 32768;
  }
  sa[8] = soo;                                              sa[9] = sxo;
  sa[10] = WS_ST + (c * 16 + 2 * wr) * 2048;                sa[11] = sso;
  sa[12] = skv - 16384;                                     sa[13] = c ? skv - 32 * 32768 : WS_XF + (h & 15) * 32768;
  sa[14] = c ? skv - 32 * 32768 + 8 * MB : sa[13];          sa[15] = WS_FL + (c * 64 + 8 * w) * 128;
  sb[0] = myfl;                                             sb[1] = WS_WF + (h * 4 + w) * 49152;
  sb[2] = WS_WO + (g2 * 4 + w) * 32768;                     sb[3] = c ? myfl - w * 8 + ((w - dl) & 3) * 8 - 32 * 128 + 64 : myfl + 4096;
  sb[4] = w * 96;                                           sb[5] = c ? myfl + 65536 - w * 8 + ((3 - dl) & 3) * 8 - 32 * 128 + 64 : myfl + 4096;
  sb[6] = ((c * 16 + 2 * wr + p2) * 16 * 1024 + g2 * 64) * 2;  sb[7] = WS_FL + 96;
  sb[8] = WS_CS + h * 16384;                                sb[9] = w * 2048;
  sb[10] = c ? -8 : 8 - wr;                                  sb[11] = c ? 16 * MB : 0;
  sb[12] = (2 * g2 + p2 - h) * 128;                                sb[13] = bid == 0 && w == 0 ? WS_EP : 0;
  sb[14] = c == 0 && wr == 0 ? ~0u : 0u;                     sb[15] = 0;
#pragma unroll
  for (int i = 0; i < 16; ++i) { sa[i] = uni(sa[i]); sb[i] = uni(sb[i]); }
  const rsrc_t wsk = __builtin_amdgcn_make_buffer_rsrc(ws, 0, wr == 3 ? 128 * MB : 0u, 0x00020000);
  const rsrc_t outr = __builtin_amdgcn_make_buffer_rsrc(out, 0, 4 * MB, 0x00020000);
  u32x8 pa, pc;
#pragma unroll
  for (int i = 0; i < 8; ++i) { pa[i] = uni(ps[i]); pc[i] = uni(ps[8 + i]); }
  STAMP(17);

  vb[2] = tag[0]; vb[3] = tag[1];
  STAMP(19);
  vb[7] = stw;
  asm volatile(
"s_branch .Lmk_al\n"
".p2align 12\n"
".Lmk_al:\n"
"; @entry\n"
"s_mov_b32 s0, 0\n"
"s_mov_b32 s3, 0\n"
"s_waitcnt lgkmcnt(0)\n"
"ds_read_b128 v[24:27], v2\n"
"ds_read_b128 v[28:31], v2 offset:1024\n"
"s_waitcnt lgkmcnt(0)\n"
"buffer_store_dwordx4 v[24:27], v0, s[4:7], s61 offen sc1\n"
"buffer_store_dwordx4 v[28:31], v0, s[4:7], s61 offen offset:1024 sc1\n"
"buffer_store_dword v48, v14, s[4:7], s63 offen sc1\n"
"s_waitcnt vmcnt(11)\n"
"s_waitcnt lgkmcnt(0)\n"
"s_barrier\n"
"ds_read_b64_tr_b16 v[24:25], v40\n"
"ds_read_b64_tr_b16 v[26:27], v40 offset:1024\n"
"ds_read_b64_tr_b16 v[28:29], v40 offset:4096\n"
"ds_read_b64_tr_b16 v[30:31], v40 offset:5120\n"
"ds_read_b64_tr_b16 v[32:33], v40 offset:8192\n"
"ds_read_b64_tr_b16 v[34:35], v40 offset:9216\n"
"ds_read_b64_tr_b16 v[44:45], v40 offset:12288\n"
"ds_read_b64_tr_b16 v[46:47], v40 offset:13312\n"
"s_waitcnt lgkmcnt(2)\n"
"buffer_store_dwordx4 v[24:27], v0, s[4:7], s24 offen sc1\n"
"s_waitcnt lgkmcnt(0)\n"
"buffer_store_dwordx4 v[28:31], v0, s[4:7], s24 offen offset:1024 sc1\n"
"buffer_store_dwordx4 v[32:35], v0, s[4:7], s25 offen sc1\n"
"buffer_store_dwordx4 v[44:47], v0, s[4:7], s25 offen offset:1024 sc1\n"
"s_add_u32 s98, s75, 0\n"
"buffer_load_dwordx2 v[254:255], v13, s[4:7], s98 offen sc1\n"
"s_getpc_b64 s[84:85]\n"
".Lmk_h2:\n"
"s_and_b32 s84, s84, -16\n"
"s_and_b32 s85, s85, 0xffff\n"
"s_mov_b32 s86, (.Lmk_end-.Lmk_h2)&0xFFFFFFF0\n"
"s_mov_b32 s87, 0x20000\n"
"s_and_b32 s97, s26, 0xfffffc00\n"
"s_mov_b32 m0, 0x27800\n"
"s_add_u32 s97, s97, 0x1e000\n"
"buffer_load_dwordx4 v0, s[84:87], s97 offen lds\n"
"s_waitcnt vmcnt(1)\n"
"v_cmp_ne_u64_e32 vcc, v[254:255], v[18:19]\n"
"s_cbranch_vccnz .Lmk_f2s\n"
".Lmk_f2r:\n"
"s_add_u32 s98, s69, 0\n"
"buffer_load_dwordx4 a[0:3], v0, s[4:7], s98 offen sc1\n"
"buffer_load_dwordx4 a[4:7], v0, s[4:7], s98 offen offset:1024 sc1\n"
"buffer_load_dwordx4 a[8:11], v0, s[4:7], s98 offen offset:2048 sc1\n"
"buffer_load_dwordx4 a[12:15], v0, s[4:7], s98 offen offset:3072 sc1\n"
"buffer_load_dwordx4 a[16:19], v1, s[4:7], s98 offen sc1\n"
"buffer_load_dwordx4 a[20:23], v1, s[4:7], s98 offen offset:1024 sc1\n"
"buffer_load_dwordx4 a[24:27], v1, s[4:7], s98 offen offset:2048 sc1\n"
"buffer_load_dwordx4 a[28:31], v1, s[4:7], s98 offen offset:3072 sc1\n"
"s_add_u32 s98, s67, 0x1000\n"
"buffer_load_dwordx2 v[254:255], v12, s[4:7], s98 offen sc1\n"
"s_add_u32 s97, s69, 0x2000\n"
"buffer_load_dwordx4 a[32:35], v0, s[4:7], s97 offen sc1\n"
"buffer_load_dwordx4 a[36:39], v0, s[4:7], s97 offen offset:1024 sc1\n"
"buffer_load_dwordx4 a[40:43], v0, s[4:7], s97 offen offset:2048 sc1\n"
"buffer_load_dwordx4 a[44:47], v0, s[4:7], s97 offen offset:3072 sc1\n"
"buffer_load_dwordx4 a[48:51], v1, s[4:7], s97 offen sc1\n"
"buffer_load_dwordx4 a[52:55], v1, s[4:7], s97 offen offset:1024 sc1\n"
"buffer_load_dwordx4 a[56:59], v1, s[4:7], s97 offen offset:2048 sc1\n"
"buffer_load_dwordx4 a[60:63], v1, s[4:7], s97 offen offset:3072 sc1\n"
"s_waitcnt vmcnt(8)\n"
"v_cmp_ne_u64_e32 vcc, v[254:255], v[18:19]\n"
"s_cbranch_vccnz .Lmk_f1s\n"
".Lmk_f1r:\n"
"s_add_u32 s98, s52, 0x40000\n"
"buffer_load_dwordx4 v[24:27], v0, s[4:7], s98 offen sc1\n"
"buffer_load_dwordx4 v[28:31], v0, s[4:7], s98 offen offset:1024 sc1\n"
"buffer_load_dwordx4 v[32:35], v0, s[4:7], s98 offen offset:2048 sc1\n"
"buffer_load_dwordx4 v[44:47], v0, s[4:7], s98 offen offset:3072 sc1\n"
"buffer_load_dwordx4 v[48:51], v1, s[4:7], s98 offen sc1\n"
"buffer_load_dwordx4 v[52:55], v1, s[4:7], s98 offen offset:1024 sc1\n"
"buffer_load_dwordx4 v[56:59], v1, s[4:7], s98 offen offset:2048 sc1\n"
"buffer_load_dwordx4 v[60:63], v1, s[4:7], s98 offen offset:3072 sc1\n"
"s_waitcnt lgkmcnt(0)\n"
"s_barrier\n"
"s_add_u32 s98, s76, 0\n"
"v_bfe_u32 v255, v0, 2, 7\n"
"v_add_lshl_u32 v255, s72, v255, 1\n"
"v_and_b32_e32 v254, 0x200, v0\n"
"v_lshl_add_u32 v255, v254, 4, v255\n"
"buffer_load_dword v254, v255, s[4:7], s98 offen sc1\n"
"buffer_load_dword v253, v255, s[4:7], s98 offen offset:1024 sc1\n"
"buffer_load_dword v252, v255, s[4:7], s98 offen offset:2048 sc1\n"
"buffer_load_dword v251, v255, s[4:7], s98 offen offset:3072 sc1\n"
"s_add_u32 s98, s76, 0x1000\n"
"buffer_load_dword v250, v255, s[4:7], s98 offen sc1\n"
"buffer_load_dword v249, v255, s[4:7], s98 offen offset:1024 sc1\n"
"buffer_load_dword v248, v255, s[4:7], s98 offen offset:2048 sc1\n"
"buffer_load_dword v247, v255, s[4:7], s98 offen offset:3072 sc1\n"
"s_add_u32 s98, s69, 0x4000\n"
"buffer_load_dwordx4 a[64:67], v0, s[4:7], s98 offen sc1\n"
"buffer_load_dwordx4 a[68:71], v0, s[4:7], s98 offen offset:1024 sc1\n"
"buffer_load_dwordx4 a[72:75], v0, s[4:7], s98 offen offset:2048 sc1\n"
"buffer_load_dwordx4 a[76:79], v0, s[4:7], s98 offen offset:3072 sc1\n"
"buffer_load_dwordx4 a[80:83], v1, s[4:7], s98 offen sc1\n"
"buffer_load_dwordx4 a[84:87], v1, s[4:7], s98 offen offset:1024 sc1\n"
"buffer_load_dwordx4 a[88:91], v1, s[4:7], s98 offen offset:2048 sc1\n"
"buffer_load_dwordx4 a[92:95], v1, s[4:7], s98 offen offset:3072 sc1\n"
"s_add_u32 s98, s53, 0x40000\n"
"buffer_load_dwordx4 v[64:67], v0, s[4:7], s98 offen sc1\n"
"buffer_load_dwordx4 v[68:71], v0, s[4:7], s98 offen offset:1024 sc1\n"
"buffer_load_dwordx4 v[72:75], v0, s[4:7], s98 offen offset:2048 sc1\n"
"buffer_load_dwordx4 v[76:79], v0, s[4:7], s98 offen offset:3072 sc1\n"
"buffer_load_dwordx4 v[80:83], v1, s[4:7], s98 offen sc1\n"
"buffer_load_dwordx4 v[84:87], v1, s[4:7], s98 offen offset:1024 sc1\n"
"buffer_load_dwordx4 v[88:91], v1, s[4:7], s98 offen offset:2048 sc1\n"
"buffer_load_dwordx4 v[92:95], v1, s[4:7], s98 offen offset:3072 sc1\n"
"s_add_u32 s98, s69, 0x6000\n"
"buffer_load_dwordx4 a[96:99], v0, s[4:7], s98 offen sc1\n"
"buffer_load_dwordx4 a[100:103], v0, s[4:7], s98 offen offset:1024 sc1\n"
"buffer_load_dwordx4 a[104:107], v0, s[4:7], s98 offen offset:2048 sc1\n"
"buffer_load_dwordx4 a[108:111], v0, s[4:7], s98 offen offset:3072 sc1\n"
"buffer_load_dwordx4 a[112:115], v1, s[4:7], s98 offen sc1\n"
"buffer_load_dwordx4 a[116:119], v1, s[4:7], s98 offen offset:1024 sc1\n"
"buffer_load_dwordx4 a[120:123], v1, s[4:7], s98 offen offset:2048 sc1\n"
"buffer_load_dwordx4 a[124:127], v1, s[4:7], s98 offen offset:3072 sc1\n"
"s_waitcnt vmcnt(24)\n"
"v_add_f32_e32 v254, v254, v253\n"
"v_add_f32_e32 v254, v254, v252\n"
"v_add_f32_e32 v254, v254, v251\n"
"v_add_f32_e32 v254, v254, v250\n"
"v_add_f32_e32 v254, v254, v249\n"
"v_add_f32_e32 v254, v254, v248\n"
"v_add_f32_e32 v254, v254, v247\n"
"v_mov_b32_e32 v253, v254\n"
"s_nop 1\n"
"v_permlane32_swap_b32_e32 v254, v253\n"
"v_add_f32_e32 v254, v254, v253\n"
"ds_write_b32 v21, v254 offset:0\n"
".Lmk_z0:\n"
"; @A G1 0.0\n"
"v_mfma_f32_16x16x32_bf16 v[96:99], a[0:3], v[24:27], 0\n"
"s_add_u32 s98, s69, 0x8000\n"
"buffer_load_dwordx4 v[100:103], v0, s[4:7], s98 offen sc1\n"
"v_mfma_f32_16x16x32_bf16 v[96:99], a[4:7], v[28:31], v[96:99]\n"
"s_add_u32 s97, s62, 0x4000\n"
"v_mfma_f32_16x16x32_bf16 v[96:99], a[8:11], v[32:35], v[96:99]\n"
"buffer_load_dwordx4 v[104:107], v0, s[4:7], s98 offen offset:1024 sc1\n"
"v_mfma_f32_16x16x32_bf16 v[96:99], a[12:15], v[44:47], v[96:99]\n"
"v_mfma_f32_16x16x32_bf16 v[96:99], a[16:19], v[48:51], v[96:99]\n"
"buffer_load_dwordx4 v[108:111], v0, s[4:7], s98 offen offset:2048 sc1\n"
"v_mfma_f32_16x16x32_bf16 v[96:99], a[20:23], v[52:55], v[96:99]\n"
"buffer_load_dword v255, v10, s[4:7], s97 offen sc1\n"
"ds_read_b64_tr_b16 v[112:113], v36\n"
"v_mfma_f32_16x16x32_bf16 v[96:99], a[24:27], v[56:59], v[96:99]\n"
"buffer_load_dwordx4 v[116:119], v0, s[4:7], s98 offen offset:3072 sc1\n"
"ds_read_b64_tr_b16 v[114:115], v36 offset:2048\n"
"v_mfma_f32_16x16x32_bf16 v[96:99], a[28:31], v[60:63], v[96:99]\n"
"s_mov_b32 s94, 0xf000f\n"
"s_mov_b32 s95, 0xf000f\n"
"v_mov_b32_e32 v123, 0x3f803f80\n"
"v_mfma_f32_16x16x32_bf16 v[124:127], a[60:63], v[60:63], 0\n"
"buffer_load_dwordx4 v[128:131], v1, s[4:7], s98 offen sc1\n"
"v_cndmask_b32_e64 v120, 0, v123, s[94:95]\n"
"v_mfma_f32_16x16x32_bf16 v[124:127], a[56:59], v[56:59], v[124:127]\n"
"buffer_load_dword v254, v10, s[4:7], s97 offen offset:256 sc1\n"
"v_cndmask_b32_e64 v121, 0, v123, s[94:95]\n"
"v_mfma_f32_16x16x32_bf16 v[124:127], a[52:55], v[52:55], v[124:127]\n"
"buffer_load_dwordx4 v[132:135], v1, s[4:7], s98 offen offset:1024 sc1\n"
"v_cndmask_b32_e64 v122, 0, v123, s[94:95]\n"
"v_mfma_f32_16x16x32_bf16 v[124:127], a[48:51], v[48:51], v[124:127]\n"
"s_add_u32 s96, s68, 0x10000\n"
"v_cndmask_b32_e64 v123, 0, v123, s[94:95]\n"
"v_mfma_f32_16x16x32_bf16 v[124:127], a[44:47], v[44:47], v[124:127]\n"
"buffer_load_dwordx4 v[136:139], v1, s[4:7], s98 offen offset:2048 sc1\n"
"s_waitcnt lgkmcnt(0)\n"
"v_mfma_f32_16x16x32_bf16 v[140:143], v[120:123], v[112:115], 0\n"
"buffer_store_dwordx4 v[112:115], v0, s[4:7], s20 offen sc1\n"
"v_mfma_f32_16x16x32_bf16 v[124:127], a[40:43], v[32:35], v[124:127]\n"
"ds_write_b128 v3, v[96:99]\n"
"v_mfma_f32_16x16x32_bf16 v[124:127], a[36:39], v[28:31], v[124:127]\n"
"buffer_load_dwordx4 v[96:99], v1, s[4:7], s98 offen offset:3072 sc1\n"
"v_mfma_f32_16x16x32_bf16 v[124:127], a[32:35], v[24:27], v[124:127]\n"
"s_waitcnt vmcnt(31)\n"
"s_add_u32 s98, s69, 0xa000\n"
"v_mfma_f32_16x16x32_bf16 v[112:115], a[64:67], v[24:27], 0\n"
"buffer_load_dwordx4 v[120:123], v0, s[4:7], s98 offen sc1\n"
"v_mfma_f32_16x16x32_bf16 v[112:115], a[68:71], v[28:31], v[112:115]\n"
"buffer_store_dwordx2 v[18:19], v11, s[4:7], s96 offen offset:96 sc1\n"
"v_mfma_f32_16x16x32_bf16 v[112:115], a[72:75], v[32:35], v[112:115]\n"
"buffer_load_dwordx4 v[144:147], v0, s[4:7], s98 offen offset:1024 sc1\n"
"v_mfma_f32_16x16x32_bf16 v[112:115], a[76:79], v[44:47], v[112:115]\n"
"s_waitcnt vmcnt(30)\n"
"buffer_load_dword v253, v10, s[4:7], s97 offen offset:512 sc1\n"
"v_mfma_f32_16x16x32_bf16 v[112:115], a[80:83], v[48:51], v[112:115]\n"
"buffer_load_dwordx4 v[148:151], v0, s[4:7], s98 offen offset:2048 sc1\n"
"v_mfma_f32_16x16x32_bf16 v[112:115], a[84:87], v[52:55], v[112:115]\n"
"ds_write_b128 v3, v[124:127] offset:1024\n"
"v_mfma_f32_16x16x32_bf16 v[112:115], a[88:91], v[56:59], v[112:115]\n"
"buffer_load_dwordx4 v[124:127], v0, s[4:7], s98 offen offset:3072 sc1\n"
"v_mfma_f32_16x16x32_bf16 v[112:115], a[92:95], v[60:63], v[112:115]\n"
"s_waitcnt vmcnt(17)\n"
"buffer_load_dword v252, v10, s[4:7], s97 offen offset:768 sc1\n"
"v_mfma_f32_16x16x32_bf16 v[152:155], a[124:127], v[60:63], 0\n"
"buffer_load_dwordx4 v[156:159], v1, s[4:7], s98 offen sc1\n"
"v_mfma_f32_16x16x32_bf16 v[152:155], a[120:123], v[56:59], v[152:155]\n"
"v_mfma_f32_16x16x32_bf16 v[152:155], a[116:119], v[52:55], v[152:155]\n"
"buffer_load_dwordx4 v[160:163], v1, s[4:7], s98 offen offset:1024 sc1\n"
"v_mfma_f32_16x16x32_bf16 v[152:155], a[112:115], v[48:51], v[152:155]\n"
"buffer_load_dword v251, v10, s[4:7], s97 offen offset:1024 sc1\n"
"v_mfma_f32_16x16x32_bf16 v[152:155], a[108:111], v[44:47], v[152:155]\n"
"buffer_load_dwordx4 v[164:167], v1, s[4:7], s98 offen offset:2048 sc1\n"
"v_mfma_f32_16x16x32_bf16 v[152:155], a[104:107], v[32:35], v[152:155]\n"
"ds_write_b128 v3, v[112:115] offset:2048\n"
"v_mfma_f32_16x16x32_bf16 v[152:155], a[100:103], v[28:31], v[152:155]\n"
"buffer_load_dwordx4 v[112:115], v1, s[4:7], s98 offen offset:3072 sc1\n"
"v_mfma_f32_16x16x32_bf16 v[152:155], a[96:99], v[24:27], v[152:155]\n"
"s_waitcnt vmcnt(18)\n"
"buffer_load_dword v250, v10, s[4:7], s97 offen offset:1280 sc1\n"
"v_mfma_f32_16x16x32_bf16 v[168:171], v[100:103], v[24:27], 0\n"
"s_add_u32 s98, s54, 0x40000\n"
"buffer_load_dwordx4 v[172:175], v0, s[4:7], s98 offen sc1\n"
"v_mfma_f32_16x16x32_bf16 v[168:171], v[104:107], v[28:31], v[168:171]\n"
"v_mfma_f32_16x16x32_bf16 v[168:171], v[108:111], v[32:35], v[168:171]\n"
"buffer_load_dword v249, v10, s[4:7], s97 offen offset:1536 sc1\n"
"v_mfma_f32_16x16x32_bf16 v[168:171], v[116:119], v[44:47], v[168:171]\n"
"s_waitcnt vmcnt(15)\n"
"v_mfma_f32_16x16x32_bf16 v[168:171], v[128:131], v[48:51], v[168:171]\n"
"buffer_load_dwordx4 v[176:179], v0, s[4:7], s98 offen offset:1024 sc1\n"
"v_mfma_f32_16x16x32_bf16 v[168:171], v[132:135], v[52:55], v[168:171]\n"
"ds_write_b128 v3, v[152:155] offset:3072\n"
"v_mfma_f32_16x16x32_bf16 v[168:171], v[136:139], v[56:59], v[168:171]\n"
"buffer_load_dword v248, v10, s[4:7], s97 offen offset:1792 sc1\n"
"v_mfma_f32_16x16x32_bf16 v[168:171], v[96:99], v[60:63], v[168:171]\n"
"s_waitcnt vmcnt(5)\n"
"v_mfma_f32_16x16x32_bf16 v[152:155], v[112:115], v[60:63], 0\n"
"buffer_load_dwordx4 v[60:63], v0, s[4:7], s98 offen offset:2048 sc1\n"
"ds_read_b64_tr_b16 v[180:181], v36 offset:4096\n"
"v_mfma_f32_16x16x32_bf16 v[152:155], v[164:167], v[56:59], v[152:155]\n"
"ds_read_b64_tr_b16 v[182:183], v36 offset:6144\n"
"s_mov_b32 s96, 0xf000f\n"
"s_mov_b32 s97, 0xf000f\n"
"v_mfma_f32_16x16x32_bf16 v[152:155], v[160:163], v[52:55], v[152:155]\n"
"v_mov_b32_e32 v55, 0x3f803f80\n"
"v_cndmask_b32_e64 v52, 0, v55, s[96:97]\n"
"v_cndmask_b32_e64 v53, 0, v55, s[96:97]\n"
"v_mfma_f32_16x16x32_bf16 v[152:155], v[156:159], v[48:51], v[152:155]\n"
"v_cndmask_b32_e64 v54, 0, v55, s[96:97]\n"
"v_cndmask_b32_e64 v55, 0, v55, s[96:97]\n"
"v_mfma_f32_16x16x32_bf16 v[152:155], v[124:127], v[44:47], v[152:155]\n"
"buffer_load_dwordx4 v[44:47], v0, s[4:7], s98 offen offset:3072 sc1\n"
"s_waitcnt lgkmcnt(0)\n"
"v_mfma_f32_16x16x32_bf16 v[140:143], v[52:55], v[180:183], v[140:143]\n"
"buffer_store_dwordx4 v[180:183], v0, s[4:7], s20 offen offset:1024 sc1\n"
"v_mfma_f32_16x16x32_bf16 v[152:155], v[148:151], v[32:35], v[152:155]\n"
"ds_write_b128 v3, v[168:171] offset:4096\n"
"v_mfma_f32_16x16x32_bf16 v[152:155], v[144:147], v[28:31], v[152:155]\n"
"v_mfma_f32_16x16x32_bf16 v[152:155], v[120:123], v[24:27], v[152:155]\n"
"; @A G1 0.1\n"
"v_mfma_f32_16x16x32_bf16 v[24:27], a[0:3], v[64:67], 0\n"
"buffer_load_dwordx4 v[28:31], v1, s[4:7], s98 offen sc1\n"
"v_mfma_f32_16x16x32_bf16 v[24:27], a[4:7], v[68:71], v[24:27]\n"
"v_mfma_f32_16x16x32_bf16 v[24:27], a[8:11], v[72:75], v[24:27]\n"
"s_add_u32 s97, s55, 0x40000\n"
"buffer_load_dwordx4 v[32:35], v0, s[4:7], s97 offen sc1\n"
"v_mfma_f32_16x16x32_bf16 v[24:27], a[12:15], v[76:79], v[24:27]\n"
"v_mfma_f32_16x16x32_bf16 v[24:27], a[16:19], v[80:83], v[24:27]\n"
"buffer_load_dwordx4 v[48:51], v1, s[4:7], s98 offen offset:1024 sc1\n"
"v_mfma_f32_16x16x32_bf16 v[24:27], a[20:23], v[84:87], v[24:27]\n"
"ds_write_b128 v3, v[152:155] offset:5120\n"
"v_mfma_f32_16x16x32_bf16 v[24:27], a[24:27], v[88:91], v[24:27]\n"
"v_mfma_f32_16x16x32_bf16 v[24:27], a[28:31], v[92:95], v[24:27]\n"
"buffer_load_dwordx4 v[52:55], v0, s[4:7], s97 offen offset:1024 sc1\n"
"v_mfma_f32_16x16x32_bf16 v[56:59], a[60:63], v[92:95], 0\n"
"buffer_load_dwordx4 v[152:155], v1, s[4:7], s98 offen offset:2048 sc1\n"
"v_mfma_f32_16x16x32_bf16 v[56:59], a[56:59], v[88:91], v[56:59]\n"
"v_mfma_f32_16x16x32_bf16 v[56:59], a[52:55], v[84:87], v[56:59]\n"
"v_mfma_f32_16x16x32_bf16 v[56:59], a[48:51], v[80:83], v[56:59]\n"
"v_mfma_f32_16x16x32_bf16 v[56:59], a[44:47], v[76:79], v[56:59]\n"
"buffer_load_dwordx4 v[168:171], v1, s[4:7], s98 offen offset:3072 sc1\n"
"v_mfma_f32_16x16x32_bf16 v[56:59], a[40:43], v[72:75], v[56:59]\n"
"buffer_load_dwordx4 v[180:183], v0, s[4:7], s97 offen offset:2048 sc1\n"
"v_mfma_f32_16x16x32_bf16 v[56:59], a[36:39], v[68:71], v[56:59]\n"
"ds_write_b128 v3, v[24:27] offset:6144\n"
"v_mfma_f32_16x16x32_bf16 v[56:59], a[32:35], v[64:67], v[56:59]\n"
"v_mfma_f32_16x16x32_bf16 v[24:27], a[64:67], v[64:67], 0\n"
"v_mfma_f32_16x16x32_bf16 v[24:27], a[68:71], v[68:71], v[24:27]\n"
"buffer_load_dwordx4 v[184:187], v0, s[4:7], s97 offen offset:3072 sc1\n"
"v_mfma_f32_16x16x32_bf16 v[24:27], a[72:75], v[72:75], v[24:27]\n"
"v_mfma_f32_16x16x32_bf16 v[24:27], a[76:79], v[76:79], v[24:27]\n"
"v_mfma_f32_16x16x32_bf16 v[24:27], a[80:83], v[80:83], v[24:27]\n"
"v_mfma_f32_16x16x32_bf16 v[24:27], a[84:87], v[84:87], v[24:27]\n"
"s_nop 0\n"
"ds_write_b128 v3, v[56:59] offset:7168\n"
"v_mfma_f32_16x16x32_bf16 v[24:27], a[88:91], v[88:91], v[24:27]\n"
"buffer_load_dwordx4 v[56:59], v1, s[4:7], s97 offen sc1\n"
"v_mfma_f32_16x16x32_bf16 v[24:27], a[92:95], v[92:95], v[24:27]\n"
"v_mfma_f32_16x16x32_bf16 v[188:191], a[124:127], v[92:95], 0\n"
"v_mfma_f32_16x16x32_bf16 v[188:191], a[120:123], v[88:91], v[188:191]\n"
"v_mfma_f32_16x16x32_bf16 v[188:191], a[116:119], v[84:87], v[188:191]\n"
"v_mfma_f32_16x16x32_bf16 v[188:191], a[112:115], v[80:83], v[188:191]\n"
"buffer_load_dwordx4 v[192:195], v1, s[4:7], s97 offen offset:1024 sc1\n"
"v_mfma_f32_16x16x32_bf16 v[188:191], a[108:111], v[76:79], v[188:191]\n"
"v_mfma_f32_16x16x32_bf16 v[188:191], a[104:107], v[72:75], v[188:191]\n"
"s_nop 0\n"
"ds_write_b128 v3, v[24:27] offset:8192\n"
"v_mfma_f32_16x16x32_bf16 v[188:191], a[100:103], v[68:71], v[188:191]\n"
"v_mfma_f32_16x16x32_bf16 v[188:191], a[96:99], v[64:67], v[188:191]\n"
"v_mfma_f32_16x16x32_bf16 v[24:27], v[100:103], v[64:67], 0\n"
"buffer_load_dwordx4 v[196:199], v1, s[4:7], s97 offen offset:2048 sc1\n"
"v_mfma_f32_16x16x32_bf16 v[24:27], v[104:107], v[68:71], v[24:27]\n"
"v_mfma_f32_16x16x32_bf16 v[24:27], v[108:111], v[72:75], v[24:27]\n"
"v_mfma_f32_16x16x32_bf16 v[24:27], v[116:119], v[76:79], v[24:27]\n"
"v_mfma_f32_16x16x32_bf16 v[24:27], v[128:131], v[80:83], v[24:27]\n"
"s_waitcnt vmcnt(14)\n"
"ds_read_b64_tr_b16 v[200:201], v37\n"
"v_add_f32_e32 v255, v255, v251\n"
"v_add_f32_e32 v254, v254, v250\n"
"v_mfma_f32_16x16x32_bf16 v[24:27], v[132:135], v[84:87], v[24:27]\n"
"buffer_load_dwordx4 v[204:207], v1, s[4:7], s97 offen offset:3072 sc1\n"
"ds_read_b64_tr_b16 v[202:203], v37 offset:2048\n"
"v_mfma_f32_16x16x32_bf16 v[24:27], v[136:139], v[88:91], v[24:27]\n"
"ds_write_b128 v3, v[188:191] offset:9216\n"
"v_mfma_f32_16x16x32_bf16 v[24:27], v[96:99], v[92:95], v[24:27]\n"
"s_mov_b32 s96, 0xf000f0\n"
"v_add_f32_e32 v253, v253, v249\n"
"v_add_f32_e32 v252, v252, v248\n"
"s_mov_b32 s97, 0xf000f0\n"
"v_mfma_f32_16x16x32_bf16 v[188:191], v[112:115], v[92:95], 0\n"
"v_add_f32_e32 v255, v255, v253\n"
"v_add_f32_e32 v254, v254, v252\n"
"v_mov_b32_e32 v95, 0x3f803f80\n"
"v_mfma_f32_16x16x32_bf16 v[188:191], v[164:167], v[88:91], v[188:191]\n"
"v_add_f32_e32 v255, v255, v254\n"
"v_mov_b32_e32 v254, v255\n"
"v_cndmask_b32_e64 v92, 0, v95, s[96:97]\n"
"v_mfma_f32_16x16x32_bf16 v[188:191], v[160:163], v[84:87], v[188:191]\n"
"v_permlane32_swap_b32_e32 v255, v254\n"
"v_add_f32_e32 v255, v255, v254\n"
"v_cndmask_b32_e64 v93, 0, v95, s[96:97]\n"
"v_mfma_f32_16x16x32_bf16 v[188:191], v[156:159], v[80:83], v[188:191]\n"
"v_mul_f32_e32 v255, 0x3a800000, v255\n"
"v_mov_b32_e32 v254, v255\n"
"v_cndmask_b32_e64 v94, 0, v95, s[96:97]\n"
"v_mfma_f32_16x16x32_bf16 v[188:191], v[124:127], v[76:79], v[188:191]\n"
"v_permlane16_swap_b32_e32 v255, v254\n"
"v_cvt_pk_bf16_f32 v253, -v255, 0\n"
"v_cndmask_b32_e64 v95, 0, v95, s[96:97]\n"
"v_mfma_f32_16x16x32_bf16 v[188:191], v[148:151], v[72:75], v[188:191]\n"
"ds_write_b128 v3, v[24:27] offset:10240\n"
"v_mfma_f32_16x16x32_bf16 v[188:191], v[144:147], v[68:71], v[188:191]\n"
"s_waitcnt lgkmcnt(2)\n"
"v_fma_f32 v254, -v255, v255, v254\n"
"v_lshlrev_b32_e32 v253, 16, v253\n"
"v_mfma_f32_16x16x32_bf16 v[140:143], v[92:95], v[200:203], v[140:143]\n"
"buffer_store_dwordx4 v[200:203], v0, s[4:7], s21 offen sc1\n"
"v_mfma_f32_16x16x32_bf16 v[188:191], v[120:123], v[64:67], v[188:191]\n"
"s_waitcnt vmcnt(14)\n"
"v_max_f32_e32 v254, 0, v254\n"
"v_cvt_pk_bf16_f32 v254, v254, 0\n"
"; @A G1 0.2\n"
"v_mfma_f32_16x16x32_bf16 v[24:27], a[0:3], v[172:175], 0\n"
"v_lshlrev_b32_e32 v254, 16, v254\n"
"v_add_f32_e32 v254, 0x3727c5ac, v254\n"
"v_mfma_f32_16x16x32_bf16 v[24:27], a[4:7], v[176:179], v[24:27]\n"
"v_cvt_pk_bf16_f32 v254, v254, 0\n"
"v_lshlrev_b32_e32 v254, 16, v254\n"
"v_mfma_f32_16x16x32_bf16 v[24:27], a[8:11], v[60:63], v[24:27]\n"
"v_sqrt_f32_e32 v254, v254\n"
"v_mfma_f32_16x16x32_bf16 v[24:27], a[12:15], v[44:47], v[24:27]\n"
"s_waitcnt vmcnt(7)\n"
"v_cvt_pk_bf16_f32 v254, v254, 0\n"
"v_lshlrev_b32_e32 v254, 16, v254\n"
"v_mfma_f32_16x16x32_bf16 v[24:27], a[16:19], v[28:31], v[24:27]\n"
"v_rcp_f32_e32 v255, v254\n"
"v_mfma_f32_16x16x32_bf16 v[24:27], a[20:23], v[48:51], v[24:27]\n"
"ds_write_b128 v3, v[188:191] offset:11264\n"
"v_mfma_f32_16x16x32_bf16 v[24:27], a[24:27], v[152:155], v[24:27]\n"
"v_mul_f32_e32 v254, 0x3e8293ee, v255\n"
"v_mfma_f32_16x16x32_bf16 v[24:27], a[28:31], v[168:171], v[24:27]\n"
"v_mfma_f32_16x16x32_bf16 v[64:67], a[60:63], v[168:171], 0\n"
"v_mfma_f32_16x16x32_bf16 v[64:67], a[56:59], v[152:155], v[64:67]\n"
"v_mfma_f32_16x16x32_bf16 v[64:67], a[52:55], v[48:51], v[64:67]\n"
"s_add_u32 s98, s52, 0x48000\n"
"buffer_load_dwordx4 v[68:71], v0, s[4:7], s98 offen sc1\n"
"v_mfma_f32_16x16x32_bf16 v[64:67], a[48:51], v[28:31], v[64:67]\n"
"s_waitcnt lgkmcnt(0)\n"
"s_barrier\n"
"v_mfma_f32_16x16x32_bf16 v[64:67], a[44:47], v[44:47], v[64:67]\n"
"ds_read_b128 v[72:75], v4\n"
"ds_read_b128 v[76:79], v9 offset:128\n"
"v_mfma_f32_16x16x32_bf16 v[64:67], a[40:43], v[60:63], v[64:67]\n"
"ds_write_b128 v3, v[24:27] offset:12288\n"
"v_mfma_f32_16x16x32_bf16 v[64:67], a[36:39], v[176:179], v[64:67]\n"
"ds_read_b128 v[24:27], v5\n"
"v_mfma_f32_16x16x32_bf16 v[64:67], a[32:35], v[172:175], v[64:67]\n"
"buffer_load_dwordx4 v[80:83], v0, s[4:7], s98 offen offset:1024 sc1\n"
"v_mfma_f32_16x16x32_bf16 v[84:87], a[64:67], v[172:175], 0\n"
"v_mfma_f32_16x16x32_bf16 v[84:87], a[68:71], v[176:179], v[84:87]\n"
"v_mfma_f32_16x16x32_bf16 v[84:87], a[72:75], v[60:63], v[84:87]\n"
"v_mfma_f32_16x16x32_bf16 v[84:87], a[76:79], v[44:47], v[84:87]\n"
"ds_read_b128 v[88:91], v4 offset:1024\n"
"ds_read_b128 v[92:95], v9 offset:192\n"
"v_mfma_f32_16x16x32_bf16 v[84:87], a[80:83], v[28:31], v[84:87]\n"
"buffer_load_dwordx4 v[188:191], v0, s[4:7], s98 offen offset:2048 sc1\n"
"v_mfma_f32_16x16x32_bf16 v[84:87], a[84:87], v[48:51], v[84:87]\n"
"ds_write_b128 v3, v[64:67] offset:13312\n"
"v_mfma_f32_16x16x32_bf16 v[84:87], a[88:91], v[152:155], v[84:87]\n"
"ds_read_b128 v[64:67], v5 offset:1024\n"
"s_waitcnt lgkmcnt(4)\n"
"v_mfma_f32_16x16x32_bf16 v[84:87], a[92:95], v[168:171], v[84:87]\n"
"v_fmac_f32_e32 v72, v253, v76\n"
"v_fmac_f32_e32 v73, v253, v77\n"
"v_mfma_f32_16x16x32_bf16 v[200:203], a[124:127], v[168:171], 0\n"
"v_fmac_f32_e32 v74, v253, v78\n"
"v_fmac_f32_e32 v75, v253, v79\n"
"v_mfma_f32_16x16x32_bf16 v[200:203], a[120:123], v[152:155], v[200:203]\n"
"buffer_load_dwordx4 v[76:79], v0, s[4:7], s98 offen offset:3072 sc1\n"
"v_mfma_f32_16x16x32_bf16 v[200:203], a[116:119], v[48:51], v[200:203]\n"
"ds_read_b128 v[208:211], v4 offset:2048\n"
"ds_read_b128 v[212:215], v9 offset:256\n"
"v_mfma_f32_16x16x32_bf16 v[200:203], a[112:115], v[28:31], v[200:203]\n"
"ds_read_b128 v[216:219], v5 offset:2048\n"
"v_add_f32_e32 v72, v72, v24\n"
"v_mfma_f32_16x16x32_bf16 v[200:203], a[108:111], v[44:47], v[200:203]\n"
"v_add_f32_e32 v73, v73, v25\n"
"v_add_f32_e32 v74, v74, v26\n"
"v_mfma_f32_16x16x32_bf16 v[200:203], a[104:107], v[60:63], v[200:203]\n"
"ds_write_b128 v3, v[84:87] offset:14336\n"
"v_mfma_f32_16x16x32_bf16 v[200:203], a[100:103], v[176:179], v[200:203]\n"
"buffer_load_dwordx4 v[84:87], v1, s[4:7], s98 offen sc1\n"
"v_mfma_f32_16x16x32_bf16 v[200:203], a[96:99], v[172:175], v[200:203]\n"
"s_waitcnt lgkmcnt(4)\n"
"v_add_f32_e32 v75, v75, v27\n"
"v_fmac_f32_e32 v88, v253, v92\n"
"v_mfma_f32_16x16x32_bf16 v[24:27], v[100:103], v[172:175], 0\n"
"ds_read_b64_tr_b16 v[220:221], v37 offset:4096\n"
"v_fmac_f32_e32 v89, v253, v93\n"
"v_fmac_f32_e32 v90, v253, v94\n"
"v_mfma_f32_16x16x32_bf16 v[24:27], v[104:107], v[176:179], v[24:27]\n"
"ds_read_b128 v[224:227], v4 offset:3072\n"
"ds_read_b128 v[228:231], v9 offset:320\n"
"ds_read_b64_tr_b16 v[222:223], v37 offset:6144\n"
"v_mfma_f32_16x16x32_bf16 v[24:27], v[108:111], v[60:63], v[24:27]\n"
"ds_read_b128 v[232:235], v5 offset:3072\n"
"v_fmac_f32_e32 v91, v253, v95\n"
"s_mov_b32 s96, 0xf000f0\n"
"v_mfma_f32_16x16x32_bf16 v[24:27], v[116:119], v[44:47], v[24:27]\n"
"buffer_load_dwordx4 v[92:95], v1, s[4:7], s98 offen offset:1024 sc1\n"
"s_mov_b32 s97, 0xf000f0\n"
"v_mfma_f32_16x16x32_bf16 v[24:27], v[128:131], v[28:31], v[24:27]\n"
"v_add_f32_e32 v88, v88, v64\n"
"v_add_f32_e32 v89, v89, v65\n"
"v_mov_b32_e32 v239, 0x3f803f80\n"
"v_mfma_f32_16x16x32_bf16 v[24:27], v[132:135], v[48:51], v[24:27]\n"
"ds_write_b128 v3, v[200:203] offset:15360\n"
"v_mfma_f32_16x16x32_bf16 v[24:27], v[136:139], v[152:155], v[24:27]\n"
"s_waitcnt lgkmcnt(7)\n"
"v_add_f32_e32 v90, v90, v66\n"
"v_add_f32_e32 v91, v91, v67\n"
"v_cndmask_b32_e64 v236, 0, v239, s[96:97]\n"
"v_mfma_f32_16x16x32_bf16 v[24:27], v[96:99], v[168:171], v[24:27]\n"
"v_fmac_f32_e32 v208, v253, v212\n"
"v_fmac_f32_e32 v209, v253, v213\n"
"v_cndmask_b32_e64 v237, 0, v239, s[96:97]\n"
"v_mfma_f32_16x16x32_bf16 v[64:67], v[112:115], v[168:171], 0\n"
"buffer_load_dwordx4 v[168:171], v1, s[4:7], s98 offen offset:2048 sc1\n"
"v_cndmask_b32_e64 v238, 0, v239, s[96:97]\n"
"v_mfma_f32_16x16x32_bf16 v[64:67], v[164:167], v[152:155], v[64:67]\n"
"s_add_u32 s95, s68, s80\n"
"ds_read_b128 v[152:155], v4 offset:4096\n"
"v_cndmask_b32_e64 v239, 0, v239, s[96:97]\n"
"v_mfma_f32_16x16x32_bf16 v[64:67], v[160:163], v[48:51], v[64:67]\n"
"buffer_store_dwordx2 v[18:19], v11, s[4:7], s95 offen sc1\n"
"v_mfma_f32_16x16x32_bf16 v[64:67], v[156:159], v[28:31], v[64:67]\n"
"ds_read_b128 v[28:31], v9\n"
"ds_read_b128 v[48:51], v5 offset:4096\n"
"s_waitcnt lgkmcnt(5)\n"
"v_mfma_f32_16x16x32_bf16 v[140:143], v[236:239], v[220:223], v[140:143]\n"
"buffer_store_dwordx4 v[220:223], v0, s[4:7], s21 offen offset:1024 sc1\n"
"v_mfma_f32_16x16x32_bf16 v[64:67], v[124:127], v[44:47], v[64:67]\n"
"s_add_u32 s97, s75, 0x10000\n"
"buffer_load_dwordx2 v[250:251], v41, s[4:7], s97 offen sc1\n"
"v_mfma_f32_16x16x32_bf16 v[64:67], v[148:151], v[60:63], v[64:67]\n"
"buffer_load_dwordx4 v[44:47], v1, s[4:7], s98 offen offset:3072 sc1\n"
"v_mfma_f32_16x16x32_bf16 v[64:67], v[144:147], v[176:179], v[64:67]\n"
"ds_write_b128 v3, v[24:27] offset:16384\n"
"v_mfma_f32_16x16x32_bf16 v[64:67], v[120:123], v[172:175], v[64:67]\n"
"s_waitcnt vmcnt(16)\n"
"ds_read_b128 v[24:27], v4 offset:5120\n"
"ds_read_b128 v[60:63], v9 offset:64\n"
"; @A G1 0.3\n"
"v_mfma_f32_16x16x32_bf16 v[72:75], a[0:3], v[32:35], v[72:75]\n"
"ds_read_b128 v[172:175], v5 offset:5120\n"
"v_fmac_f32_e32 v210, v253, v214\n"
"v_mfma_f32_16x16x32_bf16 v[72:75], a[4:7], v[52:55], v[72:75]\n"
"v_fmac_f32_e32 v211, v253, v215\n"
"v_add_f32_e32 v208, v208, v216\n"
"v_mfma_f32_16x16x32_bf16 v[72:75], a[8:11], v[180:183], v[72:75]\n"
"s_add_u32 s98, s53, 0x48000\n"
"buffer_load_dwordx4 v[176:179], v0, s[4:7], s98 offen sc1\n"
"v_mfma_f32_16x16x32_bf16 v[72:75], a[12:15], v[184:187], v[72:75]\n"
"s_waitcnt vmcnt(13)\n"
"v_add_f32_e32 v209, v209, v217\n"
"v_add_f32_e32 v210, v210, v218\n"
"v_mfma_f32_16x16x32_bf16 v[72:75], a[16:19], v[56:59], v[72:75]\n"
"s_waitcnt lgkmcnt(8)\n"
"v_add_f32_e32 v211, v211, v219\n"
"v_fmac_f32_e32 v224, v253, v228\n"
"v_mfma_f32_16x16x32_bf16 v[72:75], a[20:23], v[192:195], v[72:75]\n"
"ds_write_b128 v3, v[64:67] offset:17408\n"
"v_mfma_f32_16x16x32_bf16 v[72:75], a[24:27], v[196:199], v[72:75]\n"
"v_fmac_f32_e32 v225, v253, v229\n"
"v_fmac_f32_e32 v226, v253, v230\n"
"v_mfma_f32_16x16x32_bf16 v[72:75], a[28:31], v[204:207], v[72:75]\n"
"buffer_load_dwordx4 v[64:67], v0, s[4:7], s98 offen offset:1024 sc1\n"
"v_mfma_f32_16x16x32_bf16 v[88:91], a[60:63], v[204:207], v[88:91]\n"
"v_fmac_f32_e32 v227, v253, v231\n"
"v_add_f32_e32 v224, v224, v232\n"
"v_mfma_f32_16x16x32_bf16 v[88:91], a[56:59], v[196:199], v[88:91]\n"
"v_add_f32_e32 v225, v225, v233\n"
"v_add_f32_e32 v226, v226, v234\n"
"v_mfma_f32_16x16x32_bf16 v[88:91], a[52:55], v[192:195], v[88:91]\n"
"s_waitcnt lgkmcnt(0)\n"
"s_barrier\n"
"v_mfma_f32_16x16x32_bf16 v[88:91], a[48:51], v[56:59], v[88:91]\n"
"ds_read_b128 v[200:203], v6\n"
"ds_read_b128 v[212:215], v6 offset:1024\n"
"v_mfma_f32_16x16x32_bf16 v[88:91], a[44:47], v[184:187], v[88:91]\n"
"buffer_load_dwordx4 v[216:219], v0, s[4:7], s98 offen offset:2048 sc1\n"
"v_mfma_f32_16x16x32_bf16 v[88:91], a[40:43], v[180:183], v[88:91]\n"
"v_add_f32_e32 v227, v227, v235\n"
"v_fmac_f32_e32 v152, v253, v28\n"
"v_mfma_f32_16x16x32_bf16 v[88:91], a[36:39], v[52:55], v[88:91]\n"
"v_fmac_f32_e32 v153, v253, v29\n"
"v_fmac_f32_e32 v154, v253, v30\n"
"v_mfma_f32_16x16x32_bf16 v[88:91], a[32:35], v[32:35], v[88:91]\n"
"v_fmac_f32_e32 v155, v253, v31\n"
"v_add_f32_e32 v152, v152, v48\n"
"v_mfma_f32_16x16x32_bf16 v[208:211], a[64:67], v[32:35], v[208:211]\n"
"v_add_f32_e32 v153, v153, v49\n"
"v_add_f32_e32 v154, v154, v50\n"
"v_mfma_f32_16x16x32_bf16 v[208:211], a[68:71], v[52:55], v[208:211]\n"
"buffer_load_dwordx4 v[28:31], v0, s[4:7], s98 offen offset:3072 sc1\n"
"v_mfma_f32_16x16x32_bf16 v[208:211], a[72:75], v[180:183], v[208:211]\n"
"v_add_f32_e32 v155, v155, v51\n"
"v_fmac_f32_e32 v24, v253, v60\n"
"v_mfma_f32_16x16x32_bf16 v[208:211], a[76:79], v[184:187], v[208:211]\n"
"v_fmac_f32_e32 v25, v253, v61\n"
"v_fmac_f32_e32 v26, v253, v62\n"
"v_mfma_f32_16x16x32_bf16 v[208:211], a[80:83], v[56:59], v[208:211]\n"
"s_waitcnt lgkmcnt(0)\n"
"v_fmac_f32_e32 v27, v253, v63\n"
"v_add_f32_e32 v24, v24, v172\n"
"v_mfma_f32_16x16x32_bf16 v[208:211], a[84:87], v[192:195], v[208:211]\n"
"v_add_f32_e32 v25, v25, v173\n"
"v_add_f32_e32 v26, v26, v174\n"
"v_mfma_f32_16x16x32_bf16 v[208:211], a[88:91], v[196:199], v[208:211]\n"
"buffer_load_dwordx4 v[48:51], v1, s[4:7], s98 offen sc1\n"
"v_mfma_f32_16x16x32_bf16 v[208:211], a[92:95], v[204:207], v[208:211]\n"
"ds_read_b128 v[60:63], v6 offset:2048\n"
"ds_read_b128 v[220:223], v6 offset:3072\n"
"v_mfma_f32_16x16x32_bf16 v[224:227], a[124:127], v[204:207], v[224:227]\n"
"v_add_f32_e32 v27, v27, v175\n"
"v_add_f32_e32 v72, v72, v200\n"
"v_mfma_f32_16x16x32_bf16 v[224:227], a[120:123], v[196:199], v[224:227]\n"
"v_add_f32_e32 v73, v73, v201\n"
"v_add_f32_e32 v74, v74, v202\n"
"v_mfma_f32_16x16x32_bf16 v[224:227], a[116:119], v[192:195], v[224:227]\n"
"v_add_f32_e32 v75, v75, v203\n"
"v_add_f32_e32 v88, v88, v212\n"
"v_mfma_f32_16x16x32_bf16 v[224:227], a[112:115], v[56:59], v[224:227]\n"
"buffer_load_dwordx4 v[172:175], v1, s[4:7], s98 offen offset:1024 sc1\n"
"v_mfma_f32_16x16x32_bf16 v[224:227], a[108:111], v[184:187], v[224:227]\n"
"ds_read_b64_tr_b16 v[200:201], v38\n"
"v_add_f32_e32 v89, v89, v213\n"
"v_add_f32_e32 v90, v90, v214\n"
"v_mfma_f32_16x16x32_bf16 v[224:227], a[104:107], v[180:183], v[224:227]\n"
"ds_read_b64_tr_b16 v[202:203], v38 offset:2048\n"
"v_add_f32_e32 v91, v91, v215\n"
"v_mul_f32_e32 v72, v72, v255\n"
"v_mfma_f32_16x16x32_bf16 v[224:227], a[100:103], v[52:55], v[224:227]\n"
"s_mov_b32 s94, 0xf000f00\n"
"v_mul_f32_e32 v73, v73, v255\n"
"v_mul_f32_e32 v74, v74, v255\n"
"s_mov_b32 s95, 0xf000f00\n"
"v_mfma_f32_16x16x32_bf16 v[224:227], a[96:99], v[32:35], v[224:227]\n"
"v_mul_f32_e32 v75, v75, v255\n"
"v_mul_f32_e32 v88, v88, v255\n"
"v_mov_b32_e32 v215, 0x3f803f80\n"
"v_mfma_f32_16x16x32_bf16 v[152:155], v[100:103], v[32:35], v[152:155]\n"
"buffer_load_dwordx4 v[228:231], v1, s[4:7], s98 offen offset:2048 sc1\n"
"v_cndmask_b32_e64 v212, 0, v215, s[94:95]\n"
"v_mfma_f32_16x16x32_bf16 v[152:155], v[104:107], v[52:55], v[152:155]\n"
"v_mul_f32_e32 v89, v89, v255\n"
"v_mul_f32_e32 v90, v90, v255\n"
"v_cndmask_b32_e64 v213, 0, v215, s[94:95]\n"
"v_mfma_f32_16x16x32_bf16 v[152:155], v[108:111], v[180:183], v[152:155]\n"
"v_mul_f32_e32 v91, v91, v255\n"
"v_cvt_pk_bf16_f32 v232, v72, v73\n"
"v_cndmask_b32_e64 v214, 0, v215, s[94:95]\n"
"v_mfma_f32_16x16x32_bf16 v[152:155], v[116:119], v[184:187], v[152:155]\n"
"v_cvt_pk_bf16_f32 v233, v74, v75\n"
"v_cvt_pk_bf16_f32 v234, v88, v89\n"
"v_cndmask_b32_e64 v215, 0, v215, s[94:95]\n"
"v_mfma_f32_16x16x32_bf16 v[152:155], v[128:131], v[56:59], v[152:155]\n"
"s_waitcnt lgkmcnt(2)\n"
"v_cvt_pk_bf16_f32 v235, v90, v91\n"
"v_add_f32_e32 v208, v208, v60\n"
"s_waitcnt lgkmcnt(0)\n"
"v_mfma_f32_16x16x32_bf16 v[140:143], v[212:215], v[200:203], v[140:143]\n"
"buffer_store_dwordx4 v[200:203], v0, s[4:7], s22 offen sc1\n"
"v_mfma_f32_16x16x32_bf16 v[152:155], v[132:135], v[192:195], v[152:155]\n"
"buffer_load_dwordx4 v[72:75], v1, s[4:7], s98 offen offset:3072 sc1\n"
"ds_write_b128 v7, v[232:235] offset:24576\n"
"v_mfma_f32_16x16x32_bf16 v[152:155], v[136:139], v[196:199], v[152:155]\n"
"ds_read_b128 v[88:91], v6 offset:4096\n"
"ds_read_b128 v[200:203], v6 offset:5120\n"
"v_mfma_f32_16x16x32_bf16 v[152:155], v[96:99], v[204:207], v[152:155]\n"
"v_add_f32_e32 v209, v209, v61\n"
"v_add_f32_e32 v210, v210, v62\n"
"v_mfma_f32_16x16x32_bf16 v[24:27], v[112:115], v[204:207], v[24:27]\n"
"s_waitcnt vmcnt(10)\n"
"v_cmp_ne_u64_e32 vcc, v[250:251], v[18:19]\n"
"v_mfma_f32_16x16x32_bf16 v[24:27], v[164:167], v[196:199], v[24:27]\n"
"s_cbranch_vccnz .Lmk_f6s\n"
".Lmk_f6r:\n"
"v_add_f32_e32 v211, v211, v63\n"
"v_mfma_f32_16x16x32_bf16 v[24:27], v[160:163], v[192:195], v[24:27]\n"
"v_add_f32_e32 v224, v224, v220\n"
"v_add_f32_e32 v225, v225, v221\n"
"v_mfma_f32_16x16x32_bf16 v[24:27], v[156:159], v[56:59], v[24:27]\n"
"v_add_f32_e32 v226, v226, v222\n"
"v_add_f32_e32 v227, v227, v223\n"
"v_mfma_f32_16x16x32_bf16 v[24:27], v[124:127], v[184:187], v[24:27]\n"
"v_mul_f32_e32 v208, v208, v255\n"
"v_mul_f32_e32 v209, v209, v255\n"
"v_mfma_f32_16x16x32_bf16 v[24:27], v[148:151], v[180:183], v[24:27]\n"
"v_mul_f32_e32 v210, v210, v255\n"
"v_mul_f32_e32 v211, v211, v255\n"
"v_mfma_f32_16x16x32_bf16 v[24:27], v[144:147], v[52:55], v[24:27]\n"
"v_mul_f32_e32 v224, v224, v255\n"
"v_mul_f32_e32 v225, v225, v255\n"
"v_mfma_f32_16x16x32_bf16 v[24:27], v[120:123], v[32:35], v[24:27]\n"
"v_mul_f32_e32 v226, v226, v255\n"
"v_mul_f32_e32 v227, v227, v255\n"
"v_cvt_pk_bf16_f32 v32, v208, v209\n"
"v_cvt_pk_bf16_f32 v33, v210, v211\n"
"v_cvt_pk_bf16_f32 v34, v224, v225\n"
"v_cvt_pk_bf16_f32 v35, v226, v227\n"
"ds_write_b128 v7, v[32:35] offset:57344\n"
".Lmk_z1:\n"
"; @A G1 1.0\n"
"v_mfma_f32_16x16x32_bf16 v[40:43], a[0:3], v[68:71], 0\n"
"v_mfma_f32_16x16x32_bf16 v[40:43], a[4:7], v[80:83], v[40:43]\n"
"s_add_u32 s98, s62, 0x4800\n"
"v_mfma_f32_16x16x32_bf16 v[40:43], a[8:11], v[188:191], v[40:43]\n"
"s_add_u32 s97, s54, 0x48000\n"
"buffer_load_dwordx4 v[52:55], v0, s[4:7], s97 offen sc1\n"
"v_mfma_f32_16x16x32_bf16 v[40:43], a[12:15], v[76:79], v[40:43]\n"
"s_waitcnt vmcnt(10)\n"
"v_mfma_f32_16x16x32_bf16 v[40:43], a[16:19], v[84:87], v[40:43]\n"
"buffer_load_dword v255, v10, s[4:7], s98 offen sc1\n"
"v_mfma_f32_16x16x32_bf16 v[40:43], a[20:23], v[92:95], v[40:43]\n"
"s_waitcnt lgkmcnt(1)\n"
"v_add_f32_e32 v152, v152, v88\n"
"v_add_f32_e32 v153, v153, v89\n"
"v_mfma_f32_16x16x32_bf16 v[40:43], a[24:27], v[168:171], v[40:43]\n"
"v_add_f32_e32 v154, v154, v90\n"
"v_add_f32_e32 v155, v155, v91\n"
"v_mfma_f32_16x16x32_bf16 v[40:43], a[28:31], v[44:47], v[40:43]\n"
"buffer_load_dwordx4 v[56:59], v0, s[4:7], s97 offen offset:1024 sc1\n"
"v_mfma_f32_16x16x32_bf16 v[60:63], a[60:63], v[44:47], 0\n"
"s_add_u32 s96, s70, 0\n"
"buffer_load_dwordx4 a[128:131], v0, s[4:7], s96 offen sc1\n"
"v_mfma_f32_16x16x32_bf16 v[60:63], a[56:59], v[168:171], v[60:63]\n"
"buffer_load_dword v253, v10, s[4:7], s98 offen offset:256 sc1\n"
"v_mfma_f32_16x16x32_bf16 v[60:63], a[52:55], v[92:95], v[60:63]\n"
"v_add_f32_e32 v24, v24, v200\n"
"v_add_f32_e32 v25, v25, v201\n"
"v_mfma_f32_16x16x32_bf16 v[60:63], a[48:51], v[84:87], v[60:63]\n"
"v_add_f32_e32 v26, v26, v202\n"
"v_add_f32_e32 v27, v27, v203\n"
"v_mfma_f32_16x16x32_bf16 v[60:63], a[44:47], v[76:79], v[60:63]\n"
"buffer_load_dwordx4 v[88:91], v0, s[4:7], s97 offen offset:2048 sc1\n"
"v_mfma_f32_16x16x32_bf16 v[60:63], a[40:43], v[188:191], v[60:63]\n"
"ds_write_b128 v3, v[40:43]\n"
"v_mfma_f32_16x16x32_bf16 v[60:63], a[36:39], v[80:83], v[60:63]\n"
"buffer_load_dword v252, v10, s[4:7], s98 offen offset:512 sc1\n"
"v_mfma_f32_16x16x32_bf16 v[60:63], a[32:35], v[68:71], v[60:63]\n"
"v_mul_f32_e32 v152, v152, v254\n"
"v_mul_f32_e32 v153, v153, v254\n"
"v_mfma_f32_16x16x32_bf16 v[40:43], a[64:67], v[68:71], 0\n"
"v_mul_f32_e32 v154, v154, v254\n"
"v_mul_f32_e32 v155, v155, v254\n"
"v_mfma_f32_16x16x32_bf16 v[40:43], a[68:71], v[80:83], v[40:43]\n"
"buffer_load_dwordx4 v[180:183], v0, s[4:7], s97 offen offset:3072 sc1\n"
"v_mfma_f32_16x16x32_bf16 v[40:43], a[72:75], v[188:191], v[40:43]\n"
"buffer_load_dword v251, v10, s[4:7], s98 offen offset:768 sc1\n"
"v_mfma_f32_16x16x32_bf16 v[40:43], a[76:79], v[76:79], v[40:43]\n"
"v_mul_f32_e32 v24, v24, v254\n"
"v_mul_f32_e32 v25, v25, v254\n"
"v_mfma_f32_16x16x32_bf16 v[40:43], a[80:83], v[84:87], v[40:43]\n"
"v_mul_f32_e32 v26, v26, v254\n"
"v_mul_f32_e32 v27, v27, v254\n"
"v_mfma_f32_16x16x32_bf16 v[40:43], a[84:87], v[92:95], v[40:43]\n"
"ds_write_b128 v3, v[60:63] offset:1024\n"
"v_mfma_f32_16x16x32_bf16 v[40:43], a[88:91], v[168:171], v[40:43]\n"
"buffer_load_dwordx4 v[60:63], v1, s[4:7], s97 offen sc1\n"
"v_mfma_f32_16x16x32_bf16 v[40:43], a[92:95], v[44:47], v[40:43]\n"
"buffer_load_dword v254, v10, s[4:7], s98 offen offset:1024 sc1\n"
"v_mfma_f32_16x16x32_bf16 v[184:187], a[124:127], v[44:47], 0\n"
"v_cvt_pk_bf16_f32 v192, v152, v153\n"
"v_cvt_pk_bf16_f32 v193, v154, v155\n"
"v_mfma_f32_16x16x32_bf16 v[184:187], a[120:123], v[168:171], v[184:187]\n"
"v_cvt_pk_bf16_f32 v194, v24, v25\n"
"v_cvt_pk_bf16_f32 v195, v26, v27\n"
"v_mfma_f32_16x16x32_bf16 v[184:187], a[116:119], v[92:95], v[184:187]\n"
"buffer_load_dword v250, v10, s[4:7], s98 offen offset:1280 sc1\n"
"v_mfma_f32_16x16x32_bf16 v[184:187], a[112:115], v[84:87], v[184:187]\n"
"buffer_load_dwordx4 v[24:27], v1, s[4:7], s97 offen offset:1024 sc1\n"
"v_mfma_f32_16x16x32_bf16 v[184:187], a[108:111], v[76:79], v[184:187]\n"
"buffer_load_dwordx4 a[132:135], v0, s[4:7], s96 offen offset:1024 sc1\n"
"v_mfma_f32_16x16x32_bf16 v[184:187], a[104:107], v[188:191], v[184:187]\n"
"ds_write_b128 v3, v[40:43] offset:2048\n"
"v_mfma_f32_16x16x32_bf16 v[184:187], a[100:103], v[80:83], v[184:187]\n"
"buffer_load_dword v249, v10, s[4:7], s98 offen offset:1536 sc1\n"
"v_mfma_f32_16x16x32_bf16 v[184:187], a[96:99], v[68:71], v[184:187]\n"
"ds_write_b128 v2, v[192:195] offset:49152\n"
"v_mfma_f32_16x16x32_bf16 v[40:43], v[100:103], v[68:71], 0\n"
"buffer_load_dwordx4 v[152:155], v1, s[4:7], s97 offen offset:2048 sc1\n"
"v_mfma_f32_16x16x32_bf16 v[40:43], v[104:107], v[80:83], v[40:43]\n"
"buffer_load_dword v248, v10, s[4:7], s98 offen offset:1792 sc1\n"
"v_mfma_f32_16x16x32_bf16 v[40:43], v[108:111], v[188:191], v[40:43]\n"
"v_mfma_f32_16x16x32_bf16 v[40:43], v[116:119], v[76:79], v[40:43]\n"
"v_mfma_f32_16x16x32_bf16 v[40:43], v[128:131], v[84:87], v[40:43]\n"
"v_mfma_f32_16x16x32_bf16 v[40:43], v[132:135], v[92:95], v[40:43]\n"
"buffer_load_dwordx4 v[192:195], v1, s[4:7], s97 offen offset:3072 sc1\n"
"v_mfma_f32_16x16x32_bf16 v[40:43], v[136:139], v[168:171], v[40:43]\n"
"ds_write_b128 v3, v[184:187] offset:3072\n"
"v_mfma_f32_16x16x32_bf16 v[40:43], v[96:99], v[44:47], v[40:43]\n"
"v_mfma_f32_16x16x32_bf16 v[184:187], v[112:115], v[44:47], 0\n"
"v_mfma_f32_16x16x32_bf16 v[184:187], v[164:167], v[168:171], v[184:187]\n"
"v_mfma_f32_16x16x32_bf16 v[184:187], v[160:163], v[92:95], v[184:187]\n"
"v_mfma_f32_16x16x32_bf16 v[184:187], v[156:159], v[84:87], v[184:187]\n"
"v_mfma_f32_16x16x32_bf16 v[184:187], v[124:127], v[76:79], v[184:187]\n"
"v_mfma_f32_16x16x32_bf16 v[184:187], v[148:151], v[188:191], v[184:187]\n"
"s_nop 1\n"
"ds_write_b128 v3, v[40:43] offset:4096\n"
"v_mfma_f32_16x16x32_bf16 v[184:187], v[144:147], v[80:83], v[184:187]\n"
"v_mfma_f32_16x16x32_bf16 v[184:187], v[120:123], v[68:71], v[184:187]\n"
"s_waitcnt vmcnt(23)\n"
"; @A G1 1.1\n"
"v_mfma_f32_16x16x32_bf16 v[40:43], a[0:3], v[176:179], 0\n"
"buffer_load_dwordx4 a[136:139], v0, s[4:7], s96 offen offset:2048 sc1\n"
"v_mfma_f32_16x16x32_bf16 v[40:43], a[4:7], v[64:67], v[40:43]\n"
"v_mov_b32_e32 v44, -1\n"
"v_mov_b32_e32 v45, -1\n"
"v_mfma_f32_16x16x32_bf16 v[40:43], a[8:11], v[216:219], v[40:43]\n"
"s_add_u32 s98, s55, 0x48000\n"
"buffer_load_dwordx4 v[68:71], v0, s[4:7], s98 offen sc1\n"
"v_mfma_f32_16x16x32_bf16 v[40:43], a[12:15], v[28:31], v[40:43]\n"
"s_waitcnt vmcnt(20)\n"
"v_mov_b32_e32 v46, -1\n"
"v_mov_b32_e32 v47, -1\n"
"v_mfma_f32_16x16x32_bf16 v[40:43], a[16:19], v[48:51], v[40:43]\n"
"ds_read_b64_tr_b16 v[76:77], v38 offset:4096\n"
"ds_read_b64_tr_b16 v[78:79], v38 offset:6144\n"
"s_mov_b32 s94, 0xf000f00\n"
"v_mfma_f32_16x16x32_bf16 v[40:43], a[20:23], v[172:175], v[40:43]\n"
"ds_write_b128 v3, v[184:187] offset:5120\n"
"v_mfma_f32_16x16x32_bf16 v[40:43], a[24:27], v[228:231], v[40:43]\n"
"s_mov_b32 s95, 0xf000f00\n"
"v_mov_b32_e32 v83, 0x3f803f80\n"
"v_cndmask_b32_e64 v80, 0, v83, s[94:95]\n"
"v_cndmask_b32_e64 v81, 0, v83, s[94:95]\n"
"v_mfma_f32_16x16x32_bf16 v[40:43], a[28:31], v[72:75], v[40:43]\n"
"buffer_load_dwordx4 v[84:87], v0, s[4:7], s98 offen offset:1024 sc1\n"
"v_cndmask_b32_e64 v82, 0, v83, s[94:95]\n"
"v_mfma_f32_16x16x32_bf16 v[92:95], a[60:63], v[72:75], 0\n"
"v_cndmask_b32_e64 v83, 0, v83, s[94:95]\n"
"v_mfma_f32_16x16x32_bf16 v[92:95], a[56:59], v[228:231], v[92:95]\n"
"s_waitcnt lgkmcnt(1)\n"
"v_mfma_f32_16x16x32_bf16 v[140:143], v[80:83], v[76:79], v[140:143]\n"
"buffer_store_dwordx4 v[76:79], v0, s[4:7], s22 offen offset:1024 sc1\n"
"v_mfma_f32_16x16x32_bf16 v[92:95], a[52:55], v[172:175], v[92:95]\n"
"v_mfma_f32_16x16x32_bf16 v[92:95], a[48:51], v[48:51], v[92:95]\n"
"v_mfma_f32_16x16x32_bf16 v[92:95], a[44:47], v[28:31], v[92:95]\n"
"buffer_load_dwordx4 v[76:79], v0, s[4:7], s98 offen offset:2048 sc1\n"
"v_mfma_f32_16x16x32_bf16 v[92:95], a[40:43], v[216:219], v[92:95]\n"
"ds_write_b128 v3, v[40:43] offset:6144\n"
"v_mfma_f32_16x16x32_bf16 v[92:95], a[36:39], v[64:67], v[92:95]\n"
"s_add_u32 s97, s67, 0\n"
"buffer_load_dwordx2 v[246:247], v12, s[4:7], s97 offen sc1\n"
"s_add_u32 s95, s64, 0x2000\n"
"v_mfma_f32_16x16x32_bf16 v[92:95], a[32:35], v[176:179], v[92:95]\n"
"buffer_store_dwordx4 v[232:235], v7, s[4:7], s95 offen sc1\n"
"v_mfma_f32_16x16x32_bf16 v[40:43], a[64:67], v[176:179], 0\n"
"v_mfma_f32_16x16x32_bf16 v[40:43], a[68:71], v[64:67], v[40:43]\n"
"buffer_load_dwordx4 v[80:83], v0, s[4:7], s98 offen offset:3072 sc1\n"
"v_mfma_f32_16x16x32_bf16 v[40:43], a[72:75], v[216:219], v[40:43]\n"
"v_mfma_f32_16x16x32_bf16 v[40:43], a[76:79], v[28:31], v[40:43]\n"
"v_mfma_f32_16x16x32_bf16 v[40:43], a[80:83], v[48:51], v[40:43]\n"
"buffer_load_dwordx4 a[140:143], v0, s[4:7], s96 offen offset:3072 sc1\n"
"v_mfma_f32_16x16x32_bf16 v[40:43], a[84:87], v[172:175], v[40:43]\n"
"ds_write_b128 v3, v[92:95] offset:7168\n"
"v_mfma_f32_16x16x32_bf16 v[40:43], a[88:91], v[228:231], v[40:43]\n"
"buffer_load_dwordx4 v[92:95], v1, s[4:7], s98 offen sc1\n"
"v_mfma_f32_16x16x32_bf16 v[40:43], a[92:95], v[72:75], v[40:43]\n"
"v_mfma_f32_16x16x32_bf16 v[168:171], a[124:127], v[72:75], 0\n"
"v_mfma_f32_16x16x32_bf16 v[168:171], a[120:123], v[228:231], v[168:171]\n"
"v_mfma_f32_16x16x32_bf16 v[168:171], a[116:119], v[172:175], v[168:171]\n"
"v_mfma_f32_16x16x32_bf16 v[168:171], a[112:115], v[48:51], v[168:171]\n"
"buffer_load_dwordx4 v[184:187], v1, s[4:7], s98 offen offset:1024 sc1\n"
"v_mfma_f32_16x16x32_bf16 v[168:171], a[108:111], v[28:31], v[168:171]\n"
"v_mfma_f32_16x16x32_bf16 v[168:171], a[104:107], v[216:219], v[168:171]\n"
"s_nop 0\n"
"ds_write_b128 v3, v[40:43] offset:8192\n"
"v_mfma_f32_16x16x32_bf16 v[168:171], a[100:103], v[64:67], v[168:171]\n"
"v_mfma_f32_16x16x32_bf16 v[168:171], a[96:99], v[176:179], v[168:171]\n"
"v_mfma_f32_16x16x32_bf16 v[40:43], v[100:103], v[176:179], 0\n"
"buffer_load_dwordx4 v[188:191], v1, s[4:7], s98 offen offset:2048 sc1\n"
"v_mfma_f32_16x16x32_bf16 v[40:43], v[104:107], v[64:67], v[40:43]\n"
"buffer_store_dwordx2 v[140:141], v39, s[4:7], s23 offen sc1\n"
"v_mfma_f32_16x16x32_bf16 v[40:43], v[108:111], v[216:219], v[40:43]\n"
"v_mfma_f32_16x16x32_bf16 v[40:43], v[116:119], v[28:31], v[40:43]\n"
"v_mfma_f32_16x16x32_bf16 v[40:43], v[128:131], v[48:51], v[40:43]\n"
"s_waitcnt vmcnt(14)\n"
"v_add_f32_e32 v255, v255, v254\n"
"v_add_f32_e32 v253, v253, v250\n"
"v_mfma_f32_16x16x32_bf16 v[40:43], v[132:135], v[172:175], v[40:43]\n"
"buffer_load_dwordx4 v[36:39], v1, s[4:7], s98 offen offset:3072 sc1\n"
"s_add_u32 s98, s64, 0x6000\n"
"v_mfma_f32_16x16x32_bf16 v[40:43], v[136:139], v[228:231], v[40:43]\n"
"ds_write_b128 v3, v[168:171] offset:9216\n"
"v_mfma_f32_16x16x32_bf16 v[40:43], v[96:99], v[72:75], v[40:43]\n"
"buffer_store_dwordx4 v[32:35], v7, s[4:7], s98 offen sc1\n"
"s_nop 1\n"
"v_mfma_f32_16x16x32_bf16 v[32:35], v[112:115], v[72:75], 0\n"
"buffer_load_dwordx4 a[144:147], v1, s[4:7], s96 offen sc1\n"
"v_mfma_f32_16x16x32_bf16 v[32:35], v[164:167], v[228:231], v[32:35]\n"
"v_add_f32_e32 v252, v252, v249\n"
"v_add_f32_e32 v251, v251, v248\n"
"v_mfma_f32_16x16x32_bf16 v[32:35], v[160:163], v[172:175], v[32:35]\n"
"v_add_f32_e32 v255, v255, v252\n"
"v_add_f32_e32 v253, v253, v251\n"
"v_mfma_f32_16x16x32_bf16 v[32:35], v[156:159], v[48:51], v[32:35]\n"
"v_add_f32_e32 v255, v255, v253\n"
"v_mov_b32_e32 v253, v255\n"
"v_mfma_f32_16x16x32_bf16 v[32:35], v[124:127], v[28:31], v[32:35]\n"
"s_nop 0\n"
"v_permlane32_swap_b32_e32 v255, v253\n"
"v_add_f32_e32 v255, v255, v253\n"
"v_mfma_f32_16x16x32_bf16 v[32:35], v[148:151], v[216:219], v[32:35]\n"
"ds_write_b128 v3, v[40:43] offset:10240\n"
"v_mfma_f32_16x16x32_bf16 v[32:35], v[144:147], v[64:67], v[32:35]\n"
"v_mul_f32_e32 v255, 0x3a800000, v255\n"
"v_mov_b32_e32 v253, v255\n"
"v_mfma_f32_16x16x32_bf16 v[32:35], v[120:123], v[176:179], v[32:35]\n"
"s_nop 0\n"
"v_permlane16_swap_b32_e32 v255, v253\n"
"v_cvt_pk_bf16_f32 v254, -v255, 0\n"
"; @A G1 1.2\n"
"v_mfma_f32_16x16x32_bf16 v[28:31], a[0:3], v[52:55], 0\n"
"v_fma_f32 v253, -v255, v255, v253\n"
"v_lshlrev_b32_e32 v254, 16, v254\n"
"v_mfma_f32_16x16x32_bf16 v[28:31], a[4:7], v[56:59], v[28:31]\n"
"s_waitcnt vmcnt(10)\n"
"v_cmp_ne_u64_e32 vcc, v[246:247], v[18:19]\n"
"v_mfma_f32_16x16x32_bf16 v[28:31], a[8:11], v[88:91], v[28:31]\n"
"s_cbranch_vccnz .Lmk_f3s\n"
".Lmk_f3r:\n"
"v_max_f32_e32 v253, 0, v253\n"
"v_mfma_f32_16x16x32_bf16 v[28:31], a[12:15], v[180:183], v[28:31]\n"
"v_cvt_pk_bf16_f32 v253, v253, 0\n"
"v_lshlrev_b32_e32 v253, 16, v253\n"
"v_mfma_f32_16x16x32_bf16 v[28:31], a[16:19], v[60:63], v[28:31]\n"
"v_add_f32_e32 v253, 0x3727c5ac, v253\n"
"v_cvt_pk_bf16_f32 v253, v253, 0\n"
"v_mfma_f32_16x16x32_bf16 v[28:31], a[20:23], v[24:27], v[28:31]\n"
"ds_write_b128 v3, v[32:35] offset:11264\n"
"v_mfma_f32_16x16x32_bf16 v[28:31], a[24:27], v[152:155], v[28:31]\n"
"v_lshlrev_b32_e32 v253, 16, v253\n"
"v_mfma_f32_16x16x32_bf16 v[28:31], a[28:31], v[192:195], v[28:31]\n"
"v_sqrt_f32_e32 v253, v253\n"
"v_mfma_f32_16x16x32_bf16 v[32:35], a[60:63], v[192:195], 0\n"
"v_cvt_pk_bf16_f32 v253, v253, 0\n"
"v_lshlrev_b32_e32 v253, 16, v253\n"
"v_mfma_f32_16x16x32_bf16 v[32:35], a[56:59], v[152:155], v[32:35]\n"
"v_rcp_f32_e32 v255, v253\n"
"v_mfma_f32_16x16x32_bf16 v[32:35], a[52:55], v[24:27], v[32:35]\n"
"s_add_u32 s98, s52, 0x8000\n"
"buffer_load_dwordx4 v[40:43], v0, s[4:7], s98 offen sc1\n"
"v_mfma_f32_16x16x32_bf16 v[32:35], a[48:51], v[60:63], v[32:35]\n"
"s_waitcnt lgkmcnt(0)\n"
"s_barrier\n"
"v_mfma_f32_16x16x32_bf16 v[32:35], a[44:47], v[180:183], v[32:35]\n"
"buffer_load_dwordx4 a[148:151], v1, s[4:7], s96 offen offset:1024 sc1\n"
"v_mfma_f32_16x16x32_bf16 v[32:35], a[40:43], v[88:91], v[32:35]\n"
"ds_write_b128 v3, v[28:31] offset:12288\n"
"v_mfma_f32_16x16x32_bf16 v[32:35], a[36:39], v[56:59], v[32:35]\n"
"ds_read_b128 v[28:31], v4\n"
"v_mul_f32_e32 v253, 0x3e8293ee, v255\n"
"v_mfma_f32_16x16x32_bf16 v[32:35], a[32:35], v[52:55], v[32:35]\n"
"buffer_load_dwordx4 v[48:51], v0, s[4:7], s98 offen offset:1024 sc1\n"
"v_mfma_f32_16x16x32_bf16 v[64:67], a[64:67], v[52:55], 0\n"
"ds_read_b128 v[72:75], v9 offset:128\n"
"ds_read_b128 v[140:143], v5\n"
"v_mfma_f32_16x16x32_bf16 v[64:67], a[68:71], v[56:59], v[64:67]\n"
"v_mfma_f32_16x16x32_bf16 v[64:67], a[72:75], v[88:91], v[64:67]\n"
"s_add_u32 s97, s60, 0x48000\n"
"buffer_store_dwordx4 v[44:47], v0, s[4:7], s97 offen sc1\n"
"v_mfma_f32_16x16x32_bf16 v[64:67], a[76:79], v[180:183], v[64:67]\n"
"ds_read_b128 v[168:171], v4 offset:1024\n"
"ds_read_b128 v[172:175], v9 offset:192\n"
"v_mfma_f32_16x16x32_bf16 v[64:67], a[80:83], v[60:63], v[64:67]\n"
"buffer_load_dwordx4 v[176:179], v0, s[4:7], s98 offen offset:2048 sc1\n"
"v_mfma_f32_16x16x32_bf16 v[64:67], a[84:87], v[24:27], v[64:67]\n"
"ds_write_b128 v3, v[32:35] offset:13312\n"
"v_mfma_f32_16x16x32_bf16 v[64:67], a[88:91], v[152:155], v[64:67]\n"
"ds_read_b128 v[32:35], v5 offset:1024\n"
"s_waitcnt lgkmcnt(4)\n"
"v_mfma_f32_16x16x32_bf16 v[64:67], a[92:95], v[192:195], v[64:67]\n"
"v_fmac_f32_e32 v28, v254, v72\n"
"v_fmac_f32_e32 v29, v254, v73\n"
"v_mfma_f32_16x16x32_bf16 v[196:199], a[124:127], v[192:195], 0\n"
"v_fmac_f32_e32 v30, v254, v74\n"
"v_fmac_f32_e32 v31, v254, v75\n"
"v_mfma_f32_16x16x32_bf16 v[196:199], a[120:123], v[152:155], v[196:199]\n"
"buffer_load_dwordx4 v[72:75], v0, s[4:7], s98 offen offset:3072 sc1\n"
"v_mfma_f32_16x16x32_bf16 v[196:199], a[116:119], v[24:27], v[196:199]\n"
"ds_read_b128 v[200:203], v4 offset:2048\n"
"ds_read_b128 v[204:207], v9 offset:256\n"
"v_mfma_f32_16x16x32_bf16 v[196:199], a[112:115], v[60:63], v[196:199]\n"
"ds_read_b128 v[208:211], v5 offset:2048\n"
"v_add_f32_e32 v28, v28, v140\n"
"v_mfma_f32_16x16x32_bf16 v[196:199], a[108:111], v[180:183], v[196:199]\n"
"v_add_f32_e32 v29, v29, v141\n"
"v_add_f32_e32 v30, v30, v142\n"
"v_mfma_f32_16x16x32_bf16 v[196:199], a[104:107], v[88:91], v[196:199]\n"
"ds_write_b128 v3, v[64:67] offset:14336\n"
"v_mfma_f32_16x16x32_bf16 v[196:199], a[100:103], v[56:59], v[196:199]\n"
"buffer_load_dwordx4 v[64:67], v1, s[4:7], s98 offen sc1\n"
"v_mfma_f32_16x16x32_bf16 v[196:199], a[96:99], v[52:55], v[196:199]\n"
"s_waitcnt lgkmcnt(4)\n"
"v_add_f32_e32 v31, v31, v143\n"
"v_fmac_f32_e32 v168, v254, v172\n"
"v_mfma_f32_16x16x32_bf16 v[140:143], v[100:103], v[52:55], 0\n"
"buffer_load_dwordx4 a[152:155], v1, s[4:7], s96 offen offset:2048 sc1\n"
"v_mfma_f32_16x16x32_bf16 v[140:143], v[104:107], v[56:59], v[140:143]\n"
"ds_read_b128 v[212:215], v4 offset:3072\n"
"ds_read_b128 v[216:219], v9 offset:320\n"
"v_mfma_f32_16x16x32_bf16 v[140:143], v[108:111], v[88:91], v[140:143]\n"
"ds_read_b128 v[220:223], v5 offset:3072\n"
"v_fmac_f32_e32 v169, v254, v173\n"
"v_mfma_f32_16x16x32_bf16 v[140:143], v[116:119], v[180:183], v[140:143]\n"
"buffer_load_dwordx4 v[224:227], v1, s[4:7], s98 offen offset:1024 sc1\n"
"v_mfma_f32_16x16x32_bf16 v[140:143], v[128:131], v[60:63], v[140:143]\n"
"v_fmac_f32_e32 v170, v254, v174\n"
"v_fmac_f32_e32 v171, v254, v175\n"
"v_mfma_f32_16x16x32_bf16 v[140:143], v[132:135], v[24:27], v[140:143]\n"
"ds_write_b128 v3, v[196:199] offset:15360\n"
"v_mfma_f32_16x16x32_bf16 v[140:143], v[136:139], v[152:155], v[140:143]\n"
"v_add_f32_e32 v168, v168, v32\n"
"v_add_f32_e32 v169, v169, v33\n"
"v_mfma_f32_16x16x32_bf16 v[140:143], v[96:99], v[192:195], v[140:143]\n"
"s_waitcnt lgkmcnt(5)\n"
"v_add_f32_e32 v170, v170, v34\n"
"v_add_f32_e32 v171, v171, v35\n"
"v_mfma_f32_16x16x32_bf16 v[32:35], v[112:115], v[192:195], 0\n"
"buffer_load_dwordx4 v[172:175], v1, s[4:7], s98 offen offset:2048 sc1\n"
"v_mfma_f32_16x16x32_bf16 v[32:35], v[164:167], v[152:155], v[32:35]\n"
"ds_read_b128 v[152:155], v4 offset:4096\n"
"ds_read_b128 v[192:195], v9\n"
"v_mfma_f32_16x16x32_bf16 v[32:35], v[160:163], v[24:27], v[32:35]\n"
"ds_read_b128 v[24:27], v5 offset:4096\n"
"v_fmac_f32_e32 v200, v254, v204\n"
"v_mfma_f32_16x16x32_bf16 v[32:35], v[156:159], v[60:63], v[32:35]\n"
"v_fmac_f32_e32 v201, v254, v205\n"
"v_fmac_f32_e32 v202, v254, v206\n"
"v_mfma_f32_16x16x32_bf16 v[32:35], v[124:127], v[180:183], v[32:35]\n"
"v_fmac_f32_e32 v203, v254, v207\n"
"v_add_f32_e32 v200, v200, v208\n"
"v_mfma_f32_16x16x32_bf16 v[32:35], v[148:151], v[88:91], v[32:35]\n"
"buffer_load_dwordx4 v[60:63], v1, s[4:7], s98 offen offset:3072 sc1\n"
"v_mfma_f32_16x16x32_bf16 v[32:35], v[144:147], v[56:59], v[32:35]\n"
"ds_write_b128 v3, v[140:143] offset:16384\n"
"v_mfma_f32_16x16x32_bf16 v[32:35], v[120:123], v[52:55], v[32:35]\n"
"s_waitcnt vmcnt(19)\n"
"ds_read_b128 v[52:55], v4 offset:5120\n"
"ds_read_b128 v[56:59], v9 offset:64\n"
"; @A G1 1.3\n"
"v_mfma_f32_16x16x32_bf16 v[28:31], a[0:3], v[68:71], v[28:31]\n"
"ds_read_b128 v[88:91], v5 offset:5120\n"
"v_add_f32_e32 v201, v201, v209\n"
"v_mfma_f32_16x16x32_bf16 v[28:31], a[4:7], v[84:87], v[28:31]\n"
"s_waitcnt lgkmcnt(8)\n"
"v_add_f32_e32 v202, v202, v210\n"
"v_add_f32_e32 v203, v203, v211\n"
"v_mfma_f32_16x16x32_bf16 v[28:31], a[8:11], v[76:79], v[28:31]\n"
"s_add_u32 s98, s53, 0x8000\n"
"buffer_load_dwordx4 v[140:143], v0, s[4:7], s98 offen sc1\n"
"v_mfma_f32_16x16x32_bf16 v[28:31], a[12:15], v[80:83], v[28:31]\n"
"s_waitcnt vmcnt(14)\n"
"s_add_u32 s97, s60, 0x40000\n"
"buffer_store_dwordx4 v[44:47], v0, s[4:7], s97 offen sc1\n"
"v_fmac_f32_e32 v212, v254, v216\n"
"v_fmac_f32_e32 v213, v254, v217\n"
"v_mfma_f32_16x16x32_bf16 v[28:31], a[16:19], v[92:95], v[28:31]\n"
"buffer_load_dwordx4 a[156:159], v1, s[4:7], s96 offen offset:3072 sc1\n"
"v_mfma_f32_16x16x32_bf16 v[28:31], a[20:23], v[184:187], v[28:31]\n"
"ds_write_b128 v3, v[32:35] offset:17408\n"
"v_mfma_f32_16x16x32_bf16 v[28:31], a[24:27], v[188:191], v[28:31]\n"
"v_fmac_f32_e32 v214, v254, v218\n"
"v_fmac_f32_e32 v215, v254, v219\n"
"v_mfma_f32_16x16x32_bf16 v[28:31], a[28:31], v[36:39], v[28:31]\n"
"buffer_load_dwordx4 v[32:35], v0, s[4:7], s98 offen offset:1024 sc1\n"
"v_mfma_f32_16x16x32_bf16 v[168:171], a[60:63], v[36:39], v[168:171]\n"
"v_add_f32_e32 v212, v212, v220\n"
"v_add_f32_e32 v213, v213, v221\n"
"v_mfma_f32_16x16x32_bf16 v[168:171], a[56:59], v[188:191], v[168:171]\n"
"s_waitcnt lgkmcnt(5)\n"
"v_add_f32_e32 v214, v214, v222\n"
"v_add_f32_e32 v215, v215, v223\n"
"v_mfma_f32_16x16x32_bf16 v[168:171], a[52:55], v[184:187], v[168:171]\n"
"s_waitcnt lgkmcnt(0)\n"
"s_barrier\n"
"v_mfma_f32_16x16x32_bf16 v[168:171], a[48:51], v[92:95], v[168:171]\n"
"ds_read_b128 v[180:183], v6\n"
"ds_read_b128 v[196:199], v6 offset:1024\n"
"v_mfma_f32_16x16x32_bf16 v[168:171], a[44:47], v[80:83], v[168:171]\n"
"buffer_load_dwordx4 v[204:207], v0, s[4:7], s98 offen offset:2048 sc1\n"
"v_mfma_f32_16x16x32_bf16 v[168:171], a[40:43], v[76:79], v[168:171]\n"
"v_fmac_f32_e32 v152, v254, v192\n"
"v_fmac_f32_e32 v153, v254, v193\n"
"v_mfma_f32_16x16x32_bf16 v[168:171], a[36:39], v[84:87], v[168:171]\n"
"v_fmac_f32_e32 v154, v254, v194\n"
"v_fmac_f32_e32 v155, v254, v195\n"
"v_mfma_f32_16x16x32_bf16 v[168:171], a[32:35], v[68:71], v[168:171]\n"
"v_add_f32_e32 v152, v152, v24\n"
"v_add_f32_e32 v153, v153, v25\n"
"v_mfma_f32_16x16x32_bf16 v[200:203], a[64:67], v[68:71], v[200:203]\n"
"v_add_f32_e32 v154, v154, v26\n"
"v_add_f32_e32 v155, v155, v27\n"
"v_mfma_f32_16x16x32_bf16 v[200:203], a[68:71], v[84:87], v[200:203]\n"
"buffer_load_dwordx4 v[24:27], v0, s[4:7], s98 offen offset:3072 sc1\n"
"v_mfma_f32_16x16x32_bf16 v[200:203], a[72:75], v[76:79], v[200:203]\n"
"v_fmac_f32_e32 v52, v254, v56\n"
"v_fmac_f32_e32 v53, v254, v57\n"
"v_mfma_f32_16x16x32_bf16 v[200:203], a[76:79], v[80:83], v[200:203]\n"
"v_fmac_f32_e32 v54, v254, v58\n"
"v_fmac_f32_e32 v55, v254, v59\n"
"v_mfma_f32_16x16x32_bf16 v[200:203], a[80:83], v[92:95], v[200:203]\n"
"s_waitcnt lgkmcnt(0)\n"
"v_add_f32_e32 v52, v52, v88\n"
"v_add_f32_e32 v53, v53, v89\n"
"v_mfma_f32_16x16x32_bf16 v[200:203], a[84:87], v[184:187], v[200:203]\n"
"v_add_f32_e32 v54, v54, v90\n"
"v_add_f32_e32 v55, v55, v91\n"
"v_mfma_f32_16x16x32_bf16 v[200:203], a[88:91], v[188:191], v[200:203]\n"
"buffer_load_dwordx4 v[56:59], v1, s[4:7], s98 offen sc1\n"
"v_mfma_f32_16x16x32_bf16 v[200:203], a[92:95], v[36:39], v[200:203]\n"
"s_add_u32 s97, s70, 0x2000\n"
"ds_read_b128 v[88:91], v6 offset:2048\n"
"ds_read_b128 v[192:195], v6 offset:3072\n"
"v_mfma_f32_16x16x32_bf16 v[212:215], a[124:127], v[36:39], v[212:215]\n"
"buffer_load_dwordx4 a[160:163], v0, s[4:7], s97 offen sc1\n"
"v_mfma_f32_16x16x32_bf16 v[212:215], a[120:123], v[188:191], v[212:215]\n"
"v_add_f32_e32 v28, v28, v180\n"
"v_add_f32_e32 v29, v29, v181\n"
"v_mfma_f32_16x16x32_bf16 v[212:215], a[116:119], v[184:187], v[212:215]\n"
"v_add_f32_e32 v30, v30, v182\n"
"v_add_f32_e32 v31, v31, v183\n"
"v_mfma_f32_16x16x32_bf16 v[212:215], a[112:115], v[92:95], v[212:215]\n"
"buffer_load_dwordx4 v[180:183], v1, s[4:7], s98 offen offset:1024 sc1\n"
"v_mfma_f32_16x16x32_bf16 v[212:215], a[108:111], v[80:83], v[212:215]\n"
"v_add_f32_e32 v168, v168, v196\n"
"v_add_f32_e32 v169, v169, v197\n"
"v_mfma_f32_16x16x32_bf16 v[212:215], a[104:107], v[76:79], v[212:215]\n"
"v_add_f32_e32 v170, v170, v198\n"
"v_add_f32_e32 v171, v171, v199\n"
"v_mfma_f32_16x16x32_bf16 v[212:215], a[100:103], v[84:87], v[212:215]\n"
"v_mul_f32_e32 v28, v28, v255\n"
"v_mul_f32_e32 v29, v29, v255\n"
"v_mfma_f32_16x16x32_bf16 v[212:215], a[96:99], v[68:71], v[212:215]\n"
"v_mul_f32_e32 v30, v30, v255\n"
"v_mul_f32_e32 v31, v31, v255\n"
"v_mfma_f32_16x16x32_bf16 v[152:155], v[100:103], v[68:71], v[152:155]\n"
"buffer_load_dwordx4 v[196:199], v1, s[4:7], s98 offen offset:2048 sc1\n"
"v_mfma_f32_16x16x32_bf16 v[152:155], v[104:107], v[84:87], v[152:155]\n"
"v_mul_f32_e32 v168, v168, v255\n"
"v_mul_f32_e32 v169, v169, v255\n"
"v_mfma_f32_16x16x32_bf16 v[152:155], v[108:111], v[76:79], v[152:155]\n"
"v_mul_f32_e32 v170, v170, v255\n"
"v_mul_f32_e32 v171, v171, v255\n"
"v_mfma_f32_16x16x32_bf16 v[152:155], v[116:119], v[80:83], v[152:155]\n"
"v_cvt_pk_bf16_f32 v208, v28, v29\n"
"v_cvt_pk_bf16_f32 v209, v30, v31\n"
"v_mfma_f32_16x16x32_bf16 v[152:155], v[128:131], v[92:95], v[152:155]\n"
"s_waitcnt lgkmcnt(0)\n"
"buffer_store_dwordx4 v[44:47], v0, s[4:7], s60 offen sc1\n"
"v_cvt_pk_bf16_f32 v210, v168, v169\n"
"v_cvt_pk_bf16_f32 v211, v170, v171\n"
"v_mfma_f32_16x16x32_bf16 v[152:155], v[132:135], v[184:187], v[152:155]\n"
"buffer_load_dwordx4 v[28:31], v1, s[4:7], s98 offen offset:3072 sc1\n"
"ds_write_b128 v8, v[208:211] offset:25600\n"
"v_mfma_f32_16x16x32_bf16 v[152:155], v[136:139], v[188:191], v[152:155]\n"
"ds_read_b128 v[44:47], v6 offset:4096\n"
"ds_read_b128 v[168:171], v6 offset:5120\n"
"v_mfma_f32_16x16x32_bf16 v[152:155], v[96:99], v[36:39], v[152:155]\n"
"v_add_f32_e32 v200, v200, v88\n"
"v_add_f32_e32 v201, v201, v89\n"
"v_mfma_f32_16x16x32_bf16 v[52:55], v[112:115], v[36:39], v[52:55]\n"
"v_add_f32_e32 v202, v202, v90\n"
"v_add_f32_e32 v203, v203, v91\n"
"v_mfma_f32_16x16x32_bf16 v[52:55], v[164:167], v[188:191], v[52:55]\n"
"v_add_f32_e32 v212, v212, v192\n"
"v_add_f32_e32 v213, v213, v193\n"
"v_mfma_f32_16x16x32_bf16 v[52:55], v[160:163], v[184:187], v[52:55]\n"
"v_add_f32_e32 v214, v214, v194\n"
"v_add_f32_e32 v215, v215, v195\n"
"v_mfma_f32_16x16x32_bf16 v[52:55], v[156:159], v[92:95], v[52:55]\n"
"v_mul_f32_e32 v200, v200, v255\n"
"v_mul_f32_e32 v201, v201, v255\n"
"v_mfma_f32_16x16x32_bf16 v[52:55], v[124:127], v[80:83], v[52:55]\n"
"v_mul_f32_e32 v202, v202, v255\n"
"v_mul_f32_e32 v203, v203, v255\n"
"v_mfma_f32_16x16x32_bf16 v[52:55], v[148:151], v[76:79], v[52:55]\n"
"v_mul_f32_e32 v212, v212, v255\n"
"v_mul_f32_e32 v213, v213, v255\n"
"v_mfma_f32_16x16x32_bf16 v[52:55], v[144:147], v[84:87], v[52:55]\n"
"v_mul_f32_e32 v214, v214, v255\n"
"v_mul_f32_e32 v215, v215, v255\n"
"v_mfma_f32_16x16x32_bf16 v[52:55], v[120:123], v[68:71], v[52:55]\n"
"v_cvt_pk_bf16_f32 v36, v200, v201\n"
"v_cvt_pk_bf16_f32 v37, v202, v203\n"
"v_cvt_pk_bf16_f32 v38, v212, v213\n"
"v_cvt_pk_bf16_f32 v39, v214, v215\n"
"ds_write_b128 v7, v[36:39] offset:58368\n"
".Lmk_z2:\n"
"s_waitcnt vmcnt(17)\n"
"; @A G1 2.0\n"
"v_mfma_f32_16x16x32_bf16 v[68:71], a[0:3], v[40:43], 0\n"
"v_mfma_f32_16x16x32_bf16 v[68:71], a[4:7], v[48:51], v[68:71]\n"
"s_add_u32 s98, s62, 0x800\n"
"v_mfma_f32_16x16x32_bf16 v[68:71], a[8:11], v[176:179], v[68:71]\n"
"s_add_u32 s96, s54, 0x8000\n"
"buffer_load_dwordx4 v[76:79], v0, s[4:7], s96 offen sc1\n"
"v_mfma_f32_16x16x32_bf16 v[68:71], a[12:15], v[72:75], v[68:71]\n"
"s_waitcnt vmcnt(13)\n"
"v_mfma_f32_16x16x32_bf16 v[68:71], a[16:19], v[64:67], v[68:71]\n"
"buffer_load_dword v255, v10, s[4:7], s98 offen sc1\n"
"v_mfma_f32_16x16x32_bf16 v[68:71], a[20:23], v[224:227], v[68:71]\n"
"s_waitcnt lgkmcnt(1)\n"
"v_add_f32_e32 v152, v152, v44\n"
"v_add_f32_e32 v153, v153, v45\n"
"v_mfma_f32_16x16x32_bf16 v[68:71], a[24:27], v[172:175], v[68:71]\n"
"v_add_f32_e32 v154, v154, v46\n"
"v_add_f32_e32 v155, v155, v47\n"
"v_mfma_f32_16x16x32_bf16 v[68:71], a[28:31], v[60:63], v[68:71]\n"
"buffer_load_dwordx4 v[44:47], v0, s[4:7], s96 offen offset:1024 sc1\n"
"v_mfma_f32_16x16x32_bf16 v[80:83], a[60:63], v[60:63], 0\n"
"buffer_load_dwordx4 a[164:167], v0, s[4:7], s97 offen offset:1024 sc1\n"
"v_mfma_f32_16x16x32_bf16 v[80:83], a[56:59], v[172:175], v[80:83]\n"
"buffer_load_dword v254, v10, s[4:7], s98 offen offset:256 sc1\n"
"v_mfma_f32_16x16x32_bf16 v[80:83], a[52:55], v[224:227], v[80:83]\n"
"v_add_f32_e32 v52, v52, v168\n"
"v_add_f32_e32 v53, v53, v169\n"
"v_mfma_f32_16x16x32_bf16 v[80:83], a[48:51], v[64:67], v[80:83]\n"
"v_add_f32_e32 v54, v54, v170\n"
"v_add_f32_e32 v55, v55, v171\n"
"v_mfma_f32_16x16x32_bf16 v[80:83], a[44:47], v[72:75], v[80:83]\n"
"buffer_load_dwordx4 v[84:87], v0, s[4:7], s96 offen offset:2048 sc1\n"
"v_mfma_f32_16x16x32_bf16 v[80:83], a[40:43], v[176:179], v[80:83]\n"
"ds_write_b128 v3, v[68:71]\n"
"v_mfma_f32_16x16x32_bf16 v[80:83], a[36:39], v[48:51], v[80:83]\n"
"buffer_load_dword v252, v10, s[4:7], s98 offen offset:512 sc1\n"
"v_mfma_f32_16x16x32_bf16 v[80:83], a[32:35], v[40:43], v[80:83]\n"
"v_mul_f32_e32 v152, v152, v253\n"
"v_mul_f32_e32 v153, v153, v253\n"
"v_mfma_f32_16x16x32_bf16 v[68:71], a[64:67], v[40:43], 0\n"
"v_mul_f32_e32 v154, v154, v253\n"
"v_mul_f32_e32 v155, v155, v253\n"
"v_mfma_f32_16x16x32_bf16 v[68:71], a[68:71], v[48:51], v[68:71]\n"
"buffer_load_dwordx4 v[88:91], v0, s[4:7], s96 offen offset:3072 sc1\n"
"v_mfma_f32_16x16x32_bf16 v[68:71], a[72:75], v[176:179], v[68:71]\n"
"buffer_load_dword v251, v10, s[4:7], s98 offen offset:768 sc1\n"
"v_mfma_f32_16x16x32_bf16 v[68:71], a[76:79], v[72:75], v[68:71]\n"
"v_mul_f32_e32 v52, v52, v253\n"
"v_mul_f32_e32 v53, v53, v253\n"
"v_mfma_f32_16x16x32_bf16 v[68:71], a[80:83], v[64:67], v[68:71]\n"
"v_mul_f32_e32 v54, v54, v253\n"
"v_mul_f32_e32 v55, v55, v253\n"
"v_mfma_f32_16x16x32_bf16 v[68:71], a[84:87], v[224:227], v[68:71]\n"
"ds_write_b128 v3, v[80:83] offset:1024\n"
"v_mfma_f32_16x16x32_bf16 v[68:71], a[88:91], v[172:175], v[68:71]\n"
"buffer_load_dwordx4 v[80:83], v1, s[4:7], s96 offen sc1\n"
"v_mfma_f32_16x16x32_bf16 v[68:71], a[92:95], v[60:63], v[68:71]\n"
"buffer_load_dword v253, v10, s[4:7], s98 offen offset:1024 sc1\n"
"v_mfma_f32_16x16x32_bf16 v[92:95], a[124:127], v[60:63], 0\n"
"v_cvt_pk_bf16_f32 v168, v152, v153\n"
"v_cvt_pk_bf16_f32 v169, v154, v155\n"
"v_mfma_f32_16x16x32_bf16 v[92:95], a[120:123], v[172:175], v[92:95]\n"
"v_cvt_pk_bf16_f32 v170, v52, v53\n"
"v_cvt_pk_bf16_f32 v171, v54, v55\n"
"v_mfma_f32_16x16x32_bf16 v[92:95], a[116:119], v[224:227], v[92:95]\n"
"buffer_load_dword v250, v10, s[4:7], s98 offen offset:1280 sc1\n"
"v_mfma_f32_16x16x32_bf16 v[92:95], a[112:115], v[64:67], v[92:95]\n"
"buffer_load_dwordx4 v[52:55], v1, s[4:7], s96 offen offset:1024 sc1\n"
"v_mfma_f32_16x16x32_bf16 v[92:95], a[108:111], v[72:75], v[92:95]\n"
"buffer_load_dwordx4 a[168:171], v0, s[4:7], s97 offen offset:2048 sc1\n"
"v_mfma_f32_16x16x32_bf16 v[92:95], a[104:107], v[176:179], v[92:95]\n"
"ds_write_b128 v3, v[68:71] offset:2048\n"
"v_mfma_f32_16x16x32_bf16 v[92:95], a[100:103], v[48:51], v[92:95]\n"
"buffer_load_dword v249, v10, s[4:7], s98 offen offset:1536 sc1\n"
"v_mfma_f32_16x16x32_bf16 v[92:95], a[96:99], v[40:43], v[92:95]\n"
"ds_write_b128 v2, v[168:171] offset:50176\n"
"v_mfma_f32_16x16x32_bf16 v[68:71], v[100:103], v[40:43], 0\n"
"buffer_load_dwordx4 v[152:155], v1, s[4:7], s96 offen offset:2048 sc1\n"
"s_add_u32 s95, s64, 0x2000\n"
"v_mfma_f32_16x16x32_bf16 v[68:71], v[104:107], v[48:51], v[68:71]\n"
"buffer_load_dword v248, v10, s[4:7], s98 offen offset:1792 sc1\n"
"v_mfma_f32_16x16x32_bf16 v[68:71], v[108:111], v[176:179], v[68:71]\n"
"buffer_store_dwordx4 v[208:211], v8, s[4:7], s95 offen offset:1024 sc1\n"
"v_mfma_f32_16x16x32_bf16 v[68:71], v[116:119], v[72:75], v[68:71]\n"
"v_mfma_f32_16x16x32_bf16 v[68:71], v[128:131], v[64:67], v[68:71]\n"
"v_mfma_f32_16x16x32_bf16 v[68:71], v[132:135], v[224:227], v[68:71]\n"
"buffer_load_dwordx4 v[168:171], v1, s[4:7], s96 offen offset:3072 sc1\n"
"v_mfma_f32_16x16x32_bf16 v[68:71], v[136:139], v[172:175], v[68:71]\n"
"ds_write_b128 v3, v[92:95] offset:3072\n"
"v_mfma_f32_16x16x32_bf16 v[68:71], v[96:99], v[60:63], v[68:71]\n"
"v_mfma_f32_16x16x32_bf16 v[92:95], v[112:115], v[60:63], 0\n"
"v_mfma_f32_16x16x32_bf16 v[92:95], v[164:167], v[172:175], v[92:95]\n"
"v_mfma_f32_16x16x32_bf16 v[92:95], v[160:163], v[224:227], v[92:95]\n"
"v_mfma_f32_16x16x32_bf16 v[92:95], v[156:159], v[64:67], v[92:95]\n"
"v_mfma_f32_16x16x32_bf16 v[92:95], v[124:127], v[72:75], v[92:95]\n"
"v_mfma_f32_16x16x32_bf16 v[92:95], v[148:151], v[176:179], v[92:95]\n"
"s_nop 1\n"
"ds_write_b128 v3, v[68:71] offset:4096\n"
"v_mfma_f32_16x16x32_bf16 v[92:95], v[144:147], v[48:51], v[92:95]\n"
"v_mfma_f32_16x16x32_bf16 v[92:95], v[120:123], v[40:43], v[92:95]\n"
"s_waitcnt vmcnt(25)\n"
"; @A G1 2.1\n"
"v_mfma_f32_16x16x32_bf16 v[40:43], a[0:3], v[140:143], 0\n"
"buffer_load_dwordx4 a[172:175], v0, s[4:7], s97 offen offset:3072 sc1\n"
"v_mfma_f32_16x16x32_bf16 v[40:43], a[4:7], v[32:35], v[40:43]\n"
"v_mov_b32_e32 v48, -1\n"
"v_mov_b32_e32 v49, -1\n"
"v_mfma_f32_16x16x32_bf16 v[40:43], a[8:11], v[204:207], v[40:43]\n"
"s_add_u32 s98, s55, 0x8000\n"
"buffer_load_dwordx4 v[60:63], v0, s[4:7], s98 offen sc1\n"
"v_mfma_f32_16x16x32_bf16 v[40:43], a[12:15], v[24:27], v[40:43]\n"
"s_waitcnt vmcnt(21)\n"
"v_mov_b32_e32 v50, -1\n"
"v_mov_b32_e32 v51, -1\n"
"v_mfma_f32_16x16x32_bf16 v[40:43], a[16:19], v[56:59], v[40:43]\n"
"s_add_u32 s96, s60, 0x8000\n"
"buffer_store_dwordx4 v[48:51], v0, s[4:7], s96 offen sc1\n"
"v_mfma_f32_16x16x32_bf16 v[40:43], a[20:23], v[180:183], v[40:43]\n"
"ds_write_b128 v3, v[92:95] offset:5120\n"
"v_mfma_f32_16x16x32_bf16 v[40:43], a[24:27], v[196:199], v[40:43]\n"
"v_mfma_f32_16x16x32_bf16 v[40:43], a[28:31], v[28:31], v[40:43]\n"
"buffer_load_dwordx4 v[64:67], v0, s[4:7], s98 offen offset:1024 sc1\n"
"v_mfma_f32_16x16x32_bf16 v[68:71], a[60:63], v[28:31], 0\n"
"s_add_u32 s96, s64, 0x6000\n"
"v_mfma_f32_16x16x32_bf16 v[68:71], a[56:59], v[196:199], v[68:71]\n"
"v_mfma_f32_16x16x32_bf16 v[68:71], a[52:55], v[180:183], v[68:71]\n"
"buffer_store_dwordx4 v[36:39], v7, s[4:7], s96 offen offset:1024 sc1\n"
"v_mfma_f32_16x16x32_bf16 v[68:71], a[48:51], v[56:59], v[68:71]\n"
"v_mfma_f32_16x16x32_bf16 v[68:71], a[44:47], v[24:27], v[68:71]\n"
"buffer_load_dwordx4 v[36:39], v0, s[4:7], s98 offen offset:2048 sc1\n"
"v_mfma_f32_16x16x32_bf16 v[68:71], a[40:43], v[204:207], v[68:71]\n"
"ds_write_b128 v3, v[40:43] offset:6144\n"
"v_mfma_f32_16x16x32_bf16 v[68:71], a[36:39], v[32:35], v[68:71]\n"
"v_mfma_f32_16x16x32_bf16 v[68:71], a[32:35], v[140:143], v[68:71]\n"
"v_mfma_f32_16x16x32_bf16 v[40:43], a[64:67], v[140:143], 0\n"
"v_mfma_f32_16x16x32_bf16 v[40:43], a[68:71], v[32:35], v[40:43]\n"
"buffer_load_dwordx4 v[72:75], v0, s[4:7], s98 offen offset:3072 sc1\n"
"v_mfma_f32_16x16x32_bf16 v[40:43], a[72:75], v[204:207], v[40:43]\n"
"v_mfma_f32_16x16x32_bf16 v[40:43], a[76:79], v[24:27], v[40:43]\n"
"v_mfma_f32_16x16x32_bf16 v[40:43], a[80:83], v[56:59], v[40:43]\n"
"buffer_load_dwordx4 a[176:179], v1, s[4:7], s97 offen sc1\n"
"v_mfma_f32_16x16x32_bf16 v[40:43], a[84:87], v[180:183], v[40:43]\n"
"ds_write_b128 v3, v[68:71] offset:7168\n"
"v_mfma_f32_16x16x32_bf16 v[40:43], a[88:91], v[196:199], v[40:43]\n"
"buffer_load_dwordx4 v[68:71], v1, s[4:7], s98 offen sc1\n"
"v_mfma_f32_16x16x32_bf16 v[40:43], a[92:95], v[28:31], v[40:43]\n"
"v_mfma_f32_16x16x32_bf16 v[92:95], a[124:127], v[28:31], 0\n"
"s_add_u32 s96, s61, 0x440000\n"
"buffer_store_dwordx4 v[48:51], v0, s[4:7], s96 offen sc1\n"
"v_mfma_f32_16x16x32_bf16 v[92:95], a[120:123], v[196:199], v[92:95]\n"
"v_mfma_f32_16x16x32_bf16 v[92:95], a[116:119], v[180:183], v[92:95]\n"
"v_mfma_f32_16x16x32_bf16 v[92:95], a[112:115], v[56:59], v[92:95]\n"
"buffer_load_dwordx4 v[172:175], v1, s[4:7], s98 offen offset:1024 sc1\n"
"v_mfma_f32_16x16x32_bf16 v[92:95], a[108:111], v[24:27], v[92:95]\n"
"v_mfma_f32_16x16x32_bf16 v[92:95], a[104:107], v[204:207], v[92:95]\n"
"ds_write_b128 v3, v[40:43] offset:8192\n"
"v_mfma_f32_16x16x32_bf16 v[92:95], a[100:103], v[32:35], v[92:95]\n"
"v_mfma_f32_16x16x32_bf16 v[92:95], a[96:99], v[140:143], v[92:95]\n"
"v_mfma_f32_16x16x32_bf16 v[40:43], v[100:103], v[140:143], 0\n"
"buffer_load_dwordx4 v[176:179], v1, s[4:7], s98 offen offset:2048 sc1\n"
"v_mfma_f32_16x16x32_bf16 v[40:43], v[104:107], v[32:35], v[40:43]\n"
"v_mfma_f32_16x16x32_bf16 v[40:43], v[108:111], v[204:207], v[40:43]\n"
"v_mfma_f32_16x16x32_bf16 v[40:43], v[116:119], v[24:27], v[40:43]\n"
"v_mfma_f32_16x16x32_bf16 v[40:43], v[128:131], v[56:59], v[40:43]\n"
"s_waitcnt vmcnt(14)\n"
"v_add_f32_e32 v255, v255, v253\n"
"v_add_f32_e32 v254, v254, v250\n"
"v_mfma_f32_16x16x32_bf16 v[40:43], v[132:135], v[180:183], v[40:43]\n"
"buffer_load_dwordx4 v[184:187], v1, s[4:7], s98 offen offset:3072 sc1\n"
"v_mfma_f32_16x16x32_bf16 v[40:43], v[136:139], v[196:199], v[40:43]\n"
"ds_write_b128 v3, v[92:95] offset:9216\n"
"v_mfma_f32_16x16x32_bf16 v[40:43], v[96:99], v[28:31], v[40:43]\n"
"v_add_f32_e32 v252, v252, v249\n"
"v_add_f32_e32 v251, v251, v248\n"
"v_mfma_f32_16x16x32_bf16 v[92:95], v[112:115], v[28:31], 0\n"
"buffer_load_dwordx4 a[180:183], v1, s[4:7], s97 offen offset:1024 sc1\n"
"v_mfma_f32_16x16x32_bf16 v[92:95], v[164:167], v[196:199], v[92:95]\n"
"v_add_f32_e32 v255, v255, v252\n"
"v_add_f32_e32 v254, v254, v251\n"
"v_mfma_f32_16x16x32_bf16 v[92:95], v[160:163], v[180:183], v[92:95]\n"
"v_add_f32_e32 v255, v255, v254\n"
"v_mov_b32_e32 v254, v255\n"
"v_mfma_f32_16x16x32_bf16 v[92:95], v[156:159], v[56:59], v[92:95]\n"
"s_nop 0\n"
"v_permlane32_swap_b32_e32 v255, v254\n"
"v_add_f32_e32 v255, v255, v254\n"
"v_mfma_f32_16x16x32_bf16 v[92:95], v[124:127], v[24:27], v[92:95]\n"
"v_mul_f32_e32 v255, 0x3a800000, v255\n"
"v_mov_b32_e32 v254, v255\n"
"v_mfma_f32_16x16x32_bf16 v[92:95], v[148:151], v[204:207], v[92:95]\n"
"ds_write_b128 v3, v[40:43] offset:10240\n"
"v_mfma_f32_16x16x32_bf16 v[92:95], v[144:147], v[32:35], v[92:95]\n"
"v_permlane16_swap_b32_e32 v255, v254\n"
"v_cvt_pk_bf16_f32 v253, -v255, 0\n"
"v_mfma_f32_16x16x32_bf16 v[92:95], v[120:123], v[140:143], v[92:95]\n"
"v_fma_f32 v254, -v255, v255, v254\n"
"v_lshlrev_b32_e32 v253, 16, v253\n"
"; @A G1 2.2\n"
"v_mfma_f32_16x16x32_bf16 v[24:27], a[0:3], v[76:79], 0\n"
"s_add_u32 s98, s61, 0x440000\n"
"buffer_store_dwordx4 v[48:51], v0, s[4:7], s98 offen offset:1024 sc1\n"
"v_max_f32_e32 v254, 0, v254\n"
"v_cvt_pk_bf16_f32 v254, v254, 0\n"
"v_mfma_f32_16x16x32_bf16 v[24:27], a[4:7], v[44:47], v[24:27]\n"
"v_lshlrev_b32_e32 v254, 16, v254\n"
"v_add_f32_e32 v254, 0x3727c5ac, v254\n"
"v_mfma_f32_16x16x32_bf16 v[24:27], a[8:11], v[84:87], v[24:27]\n"
"v_cvt_pk_bf16_f32 v254, v254, 0\n"
"v_lshlrev_b32_e32 v254, 16, v254\n"
"v_mfma_f32_16x16x32_bf16 v[24:27], a[12:15], v[88:91], v[24:27]\n"
"s_waitcnt vmcnt(15)\n"
"v_sqrt_f32_e32 v254, v254\n"
"v_mfma_f32_16x16x32_bf16 v[24:27], a[16:19], v[80:83], v[24:27]\n"
"s_mov_b32 s98, .Lmk_z4-.Lmk_g11\n"
"s_getpc_b64 s[84:85]\n"
".Lmk_g11:\n"
"s_add_u32 s84, s84, s98\n"
"s_addc_u32 s85, s85, 0\n"
"s_and_b32 s84, s84, -16\n"
"v_mfma_f32_16x16x32_bf16 v[24:27], a[20:23], v[52:55], v[24:27]\n"
"ds_write_b128 v3, v[92:95] offset:11264\n"
"v_mfma_f32_16x16x32_bf16 v[24:27], a[24:27], v[152:155], v[24:27]\n"
"s_and_b32 s85, s85, 0xffff\n"
"s_sub_u32 s98, .Lmk_end-.Lmk_g11, s98\n"
"s_min_u32 s86, s98, 0x4000\n"
"s_mov_b32 s87, 0x20000\n"
"v_mfma_f32_16x16x32_bf16 v[24:27], a[28:31], v[168:171], v[24:27]\n"
"s_mov_b32 m0, 0x27800\n"
"s_nop 0\n"
"buffer_load_dwordx4 v0, s[84:87], s99 offen lds\n"
"v_mfma_f32_16x16x32_bf16 v[28:31], a[60:63], v[168:171], 0\n"
"v_cvt_pk_bf16_f32 v254, v254, 0\n"
"v_lshlrev_b32_e32 v254, 16, v254\n"
"v_mfma_f32_16x16x32_bf16 v[28:31], a[56:59], v[152:155], v[28:31]\n"
"v_rcp_f32_e32 v255, v254\n"
"v_mfma_f32_16x16x32_bf16 v[28:31], a[52:55], v[52:55], v[28:31]\n"
"s_add_u32 s98, s52, 0\n"
"buffer_load_dwordx4 v[32:35], v0, s[4:7], s98 offen sc1\n"
"v_mfma_f32_16x16x32_bf16 v[28:31], a[48:51], v[80:83], v[28:31]\n"
"s_waitcnt lgkmcnt(0)\n"
"s_barrier\n"
"v_mfma_f32_16x16x32_bf16 v[28:31], a[44:47], v[88:91], v[28:31]\n"
"buffer_load_dwordx4 a[184:187], v1, s[4:7], s97 offen offset:2048 sc1\n"
"v_mfma_f32_16x16x32_bf16 v[28:31], a[40:43], v[84:87], v[28:31]\n"
"ds_write_b128 v3, v[24:27] offset:12288\n"
"v_mfma_f32_16x16x32_bf16 v[28:31], a[36:39], v[44:47], v[28:31]\n"
"ds_read_b128 v[24:27], v4\n"
"v_mul_f32_e32 v254, 0x3e8293ee, v255\n"
"v_mfma_f32_16x16x32_bf16 v[28:31], a[32:35], v[76:79], v[28:31]\n"
"buffer_load_dwordx4 v[40:43], v0, s[4:7], s98 offen offset:1024 sc1\n"
"v_mfma_f32_16x16x32_bf16 v[56:59], a[64:67], v[76:79], 0\n"
"ds_read_b128 v[92:95], v9 offset:128\n"
"ds_read_b128 v[140:143], v5\n"
"v_mfma_f32_16x16x32_bf16 v[56:59], a[68:71], v[44:47], v[56:59]\n"
"v_mfma_f32_16x16x32_bf16 v[56:59], a[72:75], v[84:87], v[56:59]\n"
"v_mfma_f32_16x16x32_bf16 v[56:59], a[76:79], v[88:91], v[56:59]\n"
"ds_read_b128 v[180:183], v4 offset:1024\n"
"ds_read_b128 v[188:191], v9 offset:192\n"
"v_mfma_f32_16x16x32_bf16 v[56:59], a[80:83], v[80:83], v[56:59]\n"
"buffer_load_dwordx4 v[192:195], v0, s[4:7], s98 offen offset:2048 sc1\n"
"v_mfma_f32_16x16x32_bf16 v[56:59], a[84:87], v[52:55], v[56:59]\n"
"ds_write_b128 v3, v[28:31] offset:13312\n"
"v_mfma_f32_16x16x32_bf16 v[56:59], a[88:91], v[152:155], v[56:59]\n"
"ds_read_b128 v[28:31], v5 offset:1024\n"
"s_waitcnt lgkmcnt(4)\n"
"v_mfma_f32_16x16x32_bf16 v[56:59], a[92:95], v[168:171], v[56:59]\n"
"v_fmac_f32_e32 v24, v253, v92\n"
"v_fmac_f32_e32 v25, v253, v93\n"
"v_mfma_f32_16x16x32_bf16 v[196:199], a[124:127], v[168:171], 0\n"
"s_add_u32 s96, s63, 0x44000\n"
"buffer_store_dword v48, v14, s[4:7], s96 offen sc1\n"
"v_fmac_f32_e32 v26, v253, v94\n"
"v_fmac_f32_e32 v27, v253, v95\n"
"v_mfma_f32_16x16x32_bf16 v[196:199], a[120:123], v[152:155], v[196:199]\n"
"buffer_load_dwordx4 v[48:51], v0, s[4:7], s98 offen offset:3072 sc1\n"
"v_mfma_f32_16x16x32_bf16 v[196:199], a[116:119], v[52:55], v[196:199]\n"
"ds_read_b128 v[92:95], v4 offset:2048\n"
"ds_read_b128 v[200:203], v9 offset:256\n"
"v_mfma_f32_16x16x32_bf16 v[196:199], a[112:115], v[80:83], v[196:199]\n"
"ds_read_b128 v[204:207], v5 offset:2048\n"
"v_add_f32_e32 v24, v24, v140\n"
"v_mfma_f32_16x16x32_bf16 v[196:199], a[108:111], v[88:91], v[196:199]\n"
"v_add_f32_e32 v25, v25, v141\n"
"v_add_f32_e32 v26, v26, v142\n"
"v_mfma_f32_16x16x32_bf16 v[196:199], a[104:107], v[84:87], v[196:199]\n"
"ds_write_b128 v3, v[56:59] offset:14336\n"
"v_mfma_f32_16x16x32_bf16 v[196:199], a[100:103], v[44:47], v[196:199]\n"
"buffer_load_dwordx4 v[56:59], v1, s[4:7], s98 offen sc1\n"
"v_mfma_f32_16x16x32_bf16 v[196:199], a[96:99], v[76:79], v[196:199]\n"
"s_waitcnt lgkmcnt(4)\n"
"v_add_f32_e32 v27, v27, v143\n"
"v_fmac_f32_e32 v180, v253, v188\n"
"v_mfma_f32_16x16x32_bf16 v[140:143], v[100:103], v[76:79], 0\n"
"buffer_load_dwordx4 a[188:191], v1, s[4:7], s97 offen offset:3072 sc1\n"
"v_mfma_f32_16x16x32_bf16 v[140:143], v[104:107], v[44:47], v[140:143]\n"
"ds_read_b128 v[208:211], v4 offset:3072\n"
"ds_read_b128 v[212:215], v9 offset:320\n"
"v_mfma_f32_16x16x32_bf16 v[140:143], v[108:111], v[84:87], v[140:143]\n"
"s_waitcnt vmcnt(19)\n"
"s_add_u32 s97, s68, 0x1000\n"
"ds_read_b128 v[216:219], v5 offset:3072\n"
"v_mfma_f32_16x16x32_bf16 v[140:143], v[116:119], v[88:91], v[140:143]\n"
"buffer_load_dwordx4 v[220:223], v1, s[4:7], s98 offen offset:1024 sc1\n"
"v_mfma_f32_16x16x32_bf16 v[140:143], v[128:131], v[80:83], v[140:143]\n"
"buffer_store_dwordx2 v[18:19], v11, s[4:7], s97 offen offset:64 sc1\n"
"v_mfma_f32_16x16x32_bf16 v[140:143], v[132:135], v[52:55], v[140:143]\n"
"ds_write_b128 v3, v[196:199] offset:15360\n"
"v_mfma_f32_16x16x32_bf16 v[140:143], v[136:139], v[152:155], v[140:143]\n"
"v_fmac_f32_e32 v181, v253, v189\n"
"v_fmac_f32_e32 v182, v253, v190\n"
"v_mfma_f32_16x16x32_bf16 v[140:143], v[96:99], v[168:171], v[140:143]\n"
"v_fmac_f32_e32 v183, v253, v191\n"
"v_add_f32_e32 v180, v180, v28\n"
"v_mfma_f32_16x16x32_bf16 v[188:191], v[112:115], v[168:171], 0\n"
"buffer_load_dwordx4 v[168:171], v1, s[4:7], s98 offen offset:2048 sc1\n"
"v_mfma_f32_16x16x32_bf16 v[188:191], v[164:167], v[152:155], v[188:191]\n"
"ds_read_b128 v[152:155], v4 offset:4096\n"
"ds_read_b128 v[196:199], v9\n"
"v_mfma_f32_16x16x32_bf16 v[188:191], v[160:163], v[52:55], v[188:191]\n"
"ds_read_b128 v[52:55], v5 offset:4096\n"
"v_add_f32_e32 v181, v181, v29\n"
"v_mfma_f32_16x16x32_bf16 v[188:191], v[156:159], v[80:83], v[188:191]\n"
"s_waitcnt lgkmcnt(8)\n"
"v_add_f32_e32 v182, v182, v30\n"
"v_add_f32_e32 v183, v183, v31\n"
"v_mfma_f32_16x16x32_bf16 v[188:191], v[124:127], v[88:91], v[188:191]\n"
"v_fmac_f32_e32 v92, v253, v200\n"
"v_fmac_f32_e32 v93, v253, v201\n"
"v_mfma_f32_16x16x32_bf16 v[188:191], v[148:151], v[84:87], v[188:191]\n"
"buffer_load_dwordx4 v[28:31], v1, s[4:7], s98 offen offset:3072 sc1\n"
"v_mfma_f32_16x16x32_bf16 v[188:191], v[144:147], v[44:47], v[188:191]\n"
"ds_write_b128 v3, v[140:143] offset:16384\n"
"v_mfma_f32_16x16x32_bf16 v[188:191], v[120:123], v[76:79], v[188:191]\n"
"s_waitcnt vmcnt(21)\n"
"ds_read_b128 v[44:47], v4 offset:5120\n"
"ds_read_b128 v[76:79], v9 offset:64\n"
"; @A G1 2.3\n"
"v_mfma_f32_16x16x32_bf16 v[24:27], a[0:3], v[60:63], v[24:27]\n"
"ds_read_b128 v[80:83], v5 offset:5120\n"
"v_fmac_f32_e32 v94, v253, v202\n"
"v_mfma_f32_16x16x32_bf16 v[24:27], a[4:7], v[64:67], v[24:27]\n"
"v_fmac_f32_e32 v95, v253, v203\n"
"v_add_f32_e32 v92, v92, v204\n"
"v_mfma_f32_16x16x32_bf16 v[24:27], a[8:11], v[36:39], v[24:27]\n"
"s_add_u32 s98, s53, 0\n"
"buffer_load_dwordx4 v[84:87], v0, s[4:7], s98 offen sc1\n"
"v_mfma_f32_16x16x32_bf16 v[24:27], a[12:15], v[72:75], v[24:27]\n"
"s_waitcnt vmcnt(16)\n"
"s_add_u32 s97, s70, 0x4000\n"
"v_add_f32_e32 v93, v93, v205\n"
"v_mfma_f32_16x16x32_bf16 v[24:27], a[16:19], v[68:71], v[24:27]\n"
"buffer_load_dwordx4 a[192:195], v0, s[4:7], s97 offen sc1\n"
"v_mfma_f32_16x16x32_bf16 v[24:27], a[20:23], v[172:175], v[24:27]\n"
"ds_write_b128 v3, v[188:191] offset:17408\n"
"v_mfma_f32_16x16x32_bf16 v[24:27], a[24:27], v[176:179], v[24:27]\n"
"s_waitcnt lgkmcnt(9)\n"
"v_add_f32_e32 v94, v94, v206\n"
"v_add_f32_e32 v95, v95, v207\n"
"v_mfma_f32_16x16x32_bf16 v[24:27], a[28:31], v[184:187], v[24:27]\n"
"buffer_load_dwordx4 v[88:91], v0, s[4:7], s98 offen offset:1024 sc1\n"
"v_mfma_f32_16x16x32_bf16 v[180:183], a[60:63], v[184:187], v[180:183]\n"
"v_fmac_f32_e32 v208, v253, v212\n"
"v_fmac_f32_e32 v209, v253, v213\n"
"v_mfma_f32_16x16x32_bf16 v[180:183], a[56:59], v[176:179], v[180:183]\n"
"v_fmac_f32_e32 v210, v253, v214\n"
"v_fmac_f32_e32 v211, v253, v215\n"
"v_mfma_f32_16x16x32_bf16 v[180:183], a[52:55], v[172:175], v[180:183]\n"
"s_waitcnt lgkmcnt(0)\n"
"s_barrier\n"
"v_mfma_f32_16x16x32_bf16 v[180:183], a[48:51], v[68:71], v[180:183]\n"
"ds_read_b128 v[140:143], v6\n"
"ds_read_b128 v[188:191], v6 offset:1024\n"
"v_mfma_f32_16x16x32_bf16 v[180:183], a[44:47], v[72:75], v[180:183]\n"
"buffer_load_dwordx4 v[200:203], v0, s[4:7], s98 offen offset:2048 sc1\n"
"v_mfma_f32_16x16x32_bf16 v[180:183], a[40:43], v[36:39], v[180:183]\n"
"v_add_f32_e32 v208, v208, v216\n"
"v_add_f32_e32 v209, v209, v217\n"
"v_mfma_f32_16x16x32_bf16 v[180:183], a[36:39], v[64:67], v[180:183]\n"
"v_add_f32_e32 v210, v210, v218\n"
"v_add_f32_e32 v211, v211, v219\n"
"v_mfma_f32_16x16x32_bf16 v[180:183], a[32:35], v[60:63], v[180:183]\n"
"v_fmac_f32_e32 v152, v253, v196\n"
"v_fmac_f32_e32 v153, v253, v197\n"
"v_mfma_f32_16x16x32_bf16 v[92:95], a[64:67], v[60:63], v[92:95]\n"
"v_fmac_f32_e32 v154, v253, v198\n"
"v_fmac_f32_e32 v155, v253, v199\n"
"v_mfma_f32_16x16x32_bf16 v[92:95], a[68:71], v[64:67], v[92:95]\n"
"buffer_load_dwordx4 v[196:199], v0, s[4:7], s98 offen offset:3072 sc1\n"
"v_mfma_f32_16x16x32_bf16 v[92:95], a[72:75], v[36:39], v[92:95]\n"
"v_add_f32_e32 v152, v152, v52\n"
"v_add_f32_e32 v153, v153, v53\n"
"v_mfma_f32_16x16x32_bf16 v[92:95], a[76:79], v[72:75], v[92:95]\n"
"v_add_f32_e32 v154, v154, v54\n"
"v_add_f32_e32 v155, v155, v55\n"
"v_mfma_f32_16x16x32_bf16 v[92:95], a[80:83], v[68:71], v[92:95]\n"
"s_waitcnt lgkmcnt(0)\n"
"v_fmac_f32_e32 v44, v253, v76\n"
"v_fmac_f32_e32 v45, v253, v77\n"
"v_mfma_f32_16x16x32_bf16 v[92:95], a[84:87], v[172:175], v[92:95]\n"
"v_fmac_f32_e32 v46, v253, v78\n"
"v_fmac_f32_e32 v47, v253, v79\n"
"v_mfma_f32_16x16x32_bf16 v[92:95], a[88:91], v[176:179], v[92:95]\n"
"buffer_load_dwordx4 v[52:55], v1, s[4:7], s98 offen sc1\n"
"v_mfma_f32_16x16x32_bf16 v[92:95], a[92:95], v[184:187], v[92:95]\n"
"ds_read_b128 v[76:79], v6 offset:2048\n"
"ds_read_b128 v[204:207], v6 offset:3072\n"
"v_mfma_f32_16x16x32_bf16 v[208:211], a[124:127], v[184:187], v[208:211]\n"
"buffer_load_dwordx4 a[196:199], v0, s[4:7], s97 offen offset:1024 sc1\n"
"v_mfma_f32_16x16x32_bf16 v[208:211], a[120:123], v[176:179], v[208:211]\n"
"v_add_f32_e32 v44, v44, v80\n"
"v_add_f32_e32 v45, v45, v81\n"
"v_mfma_f32_16x16x32_bf16 v[208:211], a[116:119], v[172:175], v[208:211]\n"
"s_add_u32 s96, s68, 0x1000\n"
"buffer_store_dwordx2 v[18:19], v11, s[4:7], s96 offen offset:96 sc1\n"
"v_add_f32_e32 v46, v46, v82\n"
"v_add_f32_e32 v47, v47, v83\n"
"v_mfma_f32_16x16x32_bf16 v[208:211], a[112:115], v[68:71], v[208:211]\n"
"buffer_load_dwordx4 v[80:83], v1, s[4:7], s98 offen offset:1024 sc1\n"
"v_mfma_f32_16x16x32_bf16 v[208:211], a[108:111], v[72:75], v[208:211]\n"
"v_add_f32_e32 v24, v24, v140\n"
"v_add_f32_e32 v25, v25, v141\n"
"v_mfma_f32_16x16x32_bf16 v[208:211], a[104:107], v[36:39], v[208:211]\n"
"v_add_f32_e32 v26, v26, v142\n"
"v_add_f32_e32 v27, v27, v143\n"
"v_mfma_f32_16x16x32_bf16 v[208:211], a[100:103], v[64:67], v[208:211]\n"
"v_add_f32_e32 v180, v180, v188\n"
"v_add_f32_e32 v181, v181, v189\n"
"v_mfma_f32_16x16x32_bf16 v[208:211], a[96:99], v[60:63], v[208:211]\n"
"v_add_f32_e32 v182, v182, v190\n"
"v_add_f32_e32 v183, v183, v191\n"
"v_mfma_f32_16x16x32_bf16 v[152:155], v[100:103], v[60:63], v[152:155]\n"
"buffer_load_dwordx4 v[140:143], v1, s[4:7], s98 offen offset:2048 sc1\n"
"v_mfma_f32_16x16x32_bf16 v[152:155], v[104:107], v[64:67], v[152:155]\n"
"v_mul_f32_e32 v24, v24, v255\n"
"v_mul_f32_e32 v25, v25, v255\n"
"v_mfma_f32_16x16x32_bf16 v[152:155], v[108:111], v[36:39], v[152:155]\n"
"v_mul_f32_e32 v26, v26, v255\n"
"v_mul_f32_e32 v27, v27, v255\n"
"v_mfma_f32_16x16x32_bf16 v[152:155], v[116:119], v[72:75], v[152:155]\n"
"v_mul_f32_e32 v180, v180, v255\n"
"v_mul_f32_e32 v181, v181, v255\n"
"v_mfma_f32_16x16x32_bf16 v[152:155], v[128:131], v[68:71], v[152:155]\n"
"s_waitcnt lgkmcnt(0)\n"
"v_mul_f32_e32 v182, v182, v255\n"
"v_mul_f32_e32 v183, v183, v255\n"
"v_mfma_f32_16x16x32_bf16 v[152:155], v[132:135], v[172:175], v[152:155]\n"
"buffer_load_dwordx4 v[188:191], v1, s[4:7], s98 offen offset:3072 sc1\n"
"v_mfma_f32_16x16x32_bf16 v[152:155], v[136:139], v[176:179], v[152:155]\n"
"ds_read_b128 v[212:215], v6 offset:4096\n"
"ds_read_b128 v[216:219], v6 offset:5120\n"
"v_mfma_f32_16x16x32_bf16 v[152:155], v[96:99], v[184:187], v[152:155]\n"
"v_cvt_pk_bf16_f32 v224, v24, v25\n"
"v_cvt_pk_bf16_f32 v225, v26, v27\n"
"v_mfma_f32_16x16x32_bf16 v[44:47], v[112:115], v[184:187], v[44:47]\n"
"v_cvt_pk_bf16_f32 v226, v180, v181\n"
"v_cvt_pk_bf16_f32 v227, v182, v183\n"
"v_mfma_f32_16x16x32_bf16 v[44:47], v[164:167], v[176:179], v[44:47]\n"
"ds_write_b128 v8, v[224:227] offset:17408\n"
"v_mfma_f32_16x16x32_bf16 v[44:47], v[160:163], v[172:175], v[44:47]\n"
"v_add_f32_e32 v92, v92, v76\n"
"v_add_f32_e32 v93, v93, v77\n"
"v_mfma_f32_16x16x32_bf16 v[44:47], v[156:159], v[68:71], v[44:47]\n"
"v_add_f32_e32 v94, v94, v78\n"
"v_add_f32_e32 v95, v95, v79\n"
"v_mfma_f32_16x16x32_bf16 v[44:47], v[124:127], v[72:75], v[44:47]\n"
"v_add_f32_e32 v208, v208, v204\n"
"v_add_f32_e32 v209, v209, v205\n"
"v_mfma_f32_16x16x32_bf16 v[44:47], v[148:151], v[36:39], v[44:47]\n"
"v_add_f32_e32 v210, v210, v206\n"
"v_add_f32_e32 v211, v211, v207\n"
"v_mfma_f32_16x16x32_bf16 v[44:47], v[144:147], v[64:67], v[44:47]\n"
"v_mul_f32_e32 v92, v92, v255\n"
"v_mul_f32_e32 v93, v93, v255\n"
"v_mfma_f32_16x16x32_bf16 v[44:47], v[120:123], v[60:63], v[44:47]\n"
"v_mul_f32_e32 v94, v94, v255\n"
"v_mul_f32_e32 v95, v95, v255\n"
"v_mul_f32_e32 v208, v208, v255\n"
"v_mul_f32_e32 v209, v209, v255\n"
"v_mul_f32_e32 v210, v210, v255\n"
"v_mul_f32_e32 v211, v211, v255\n"
"v_cvt_pk_bf16_f32 v24, v92, v93\n"
"v_cvt_pk_bf16_f32 v25, v94, v95\n"
"v_cvt_pk_bf16_f32 v26, v208, v209\n"
"v_cvt_pk_bf16_f32 v27, v210, v211\n"
"ds_write_b128 v7, v[24:27] offset:50176\n"
".Lmk_z3:\n"
"s_waitcnt vmcnt(17)\n"
"; @A G1 3.0\n"
"v_mfma_f32_16x16x32_bf16 v[36:39], a[0:3], v[32:35], 0\n"
"v_mfma_f32_16x16x32_bf16 v[36:39], a[4:7], v[40:43], v[36:39]\n"
"s_add_u32 s98, s62, 0\n"
"v_mfma_f32_16x16x32_bf16 v[36:39], a[8:11], v[192:195], v[36:39]\n"
"s_add_u32 s96, s54, 0\n"
"buffer_load_dwordx4 v[60:63], v0, s[4:7], s96 offen sc1\n"
"v_mfma_f32_16x16x32_bf16 v[36:39], a[12:15], v[48:51], v[36:39]\n"
"s_waitcnt vmcnt(12)\n"
"v_mfma_f32_16x16x32_bf16 v[36:39], a[16:19], v[56:59], v[36:39]\n"
"buffer_load_dword v255, v10, s[4:7], s98 offen sc1\n"
"v_mfma_f32_16x16x32_bf16 v[36:39], a[20:23], v[220:223], v[36:39]\n"
"s_waitcnt lgkmcnt(2)\n"
"v_add_f32_e32 v152, v152, v212\n"
"v_add_f32_e32 v153, v153, v213\n"
"v_mfma_f32_16x16x32_bf16 v[36:39], a[24:27], v[168:171], v[36:39]\n"
"v_add_f32_e32 v154, v154, v214\n"
"v_add_f32_e32 v155, v155, v215\n"
"v_mfma_f32_16x16x32_bf16 v[36:39], a[28:31], v[28:31], v[36:39]\n"
"buffer_load_dwordx4 v[64:67], v0, s[4:7], s96 offen offset:1024 sc1\n"
"v_mfma_f32_16x16x32_bf16 v[68:71], a[60:63], v[28:31], 0\n"
"buffer_load_dwordx4 a[200:203], v0, s[4:7], s97 offen offset:2048 sc1\n"
"v_mfma_f32_16x16x32_bf16 v[68:71], a[56:59], v[168:171], v[68:71]\n"
"buffer_load_dword v253, v10, s[4:7], s98 offen offset:256 sc1\n"
"v_mfma_f32_16x16x32_bf16 v[68:71], a[52:55], v[220:223], v[68:71]\n"
"v_add_f32_e32 v44, v44, v216\n"
"v_add_f32_e32 v45, v45, v217\n"
"v_mfma_f32_16x16x32_bf16 v[68:71], a[48:51], v[56:59], v[68:71]\n"
"v_add_f32_e32 v46, v46, v218\n"
"v_add_f32_e32 v47, v47, v219\n"
"v_mfma_f32_16x16x32_bf16 v[68:71], a[44:47], v[48:51], v[68:71]\n"
"buffer_load_dwordx4 v[72:75], v0, s[4:7], s96 offen offset:2048 sc1\n"
"v_mfma_f32_16x16x32_bf16 v[68:71], a[40:43], v[192:195], v[68:71]\n"
"ds_write_b128 v3, v[36:39]\n"
"v_mfma_f32_16x16x32_bf16 v[68:71], a[36:39], v[40:43], v[68:71]\n"
"buffer_load_dword v252, v10, s[4:7], s98 offen offset:512 sc1\n"
"v_mfma_f32_16x16x32_bf16 v[68:71], a[32:35], v[32:35], v[68:71]\n"
"v_mul_f32_e32 v152, v152, v254\n"
"v_mul_f32_e32 v153, v153, v254\n"
"v_mfma_f32_16x16x32_bf16 v[36:39], a[64:67], v[32:35], 0\n"
"v_mul_f32_e32 v154, v154, v254\n"
"v_mul_f32_e32 v155, v155, v254\n"
"v_mfma_f32_16x16x32_bf16 v[36:39], a[68:71], v[40:43], v[36:39]\n"
"buffer_load_dwordx4 v[76:79], v0, s[4:7], s96 offen offset:3072 sc1\n"
"v_mfma_f32_16x16x32_bf16 v[36:39], a[72:75], v[192:195], v[36:39]\n"
"buffer_load_dword v251, v10, s[4:7], s98 offen offset:768 sc1\n"
"v_mfma_f32_16x16x32_bf16 v[36:39], a[76:79], v[48:51], v[36:39]\n"
"v_mul_f32_e32 v44, v44, v254\n"
"v_mul_f32_e32 v45, v45, v254\n"
"v_mfma_f32_16x16x32_bf16 v[36:39], a[80:83], v[56:59], v[36:39]\n"
"v_mul_f32_e32 v46, v46, v254\n"
"v_mul_f32_e32 v47, v47, v254\n"
"v_mfma_f32_16x16x32_bf16 v[36:39], a[84:87], v[220:223], v[36:39]\n"
"ds_write_b128 v3, v[68:71] offset:1024\n"
"v_mfma_f32_16x16x32_bf16 v[36:39], a[88:91], v[168:171], v[36:39]\n"
"buffer_load_dwordx4 v[68:71], v1, s[4:7], s96 offen sc1\n"
"v_mfma_f32_16x16x32_bf16 v[36:39], a[92:95], v[28:31], v[36:39]\n"
"buffer_load_dword v254, v10, s[4:7], s98 offen offset:1024 sc1\n"
"v_mfma_f32_16x16x32_bf16 v[92:95], a[124:127], v[28:31], 0\n"
"v_cvt_pk_bf16_f32 v172, v152, v153\n"
"v_cvt_pk_bf16_f32 v173, v154, v155\n"
"v_mfma_f32_16x16x32_bf16 v[92:95], a[120:123], v[168:171], v[92:95]\n"
"v_cvt_pk_bf16_f32 v174, v44, v45\n"
"v_cvt_pk_bf16_f32 v175, v46, v47\n"
"v_mfma_f32_16x16x32_bf16 v[92:95], a[116:119], v[220:223], v[92:95]\n"
"buffer_load_dwordx4 a[204:207], v0, s[4:7], s97 offen offset:3072 sc1\n"
"v_mfma_f32_16x16x32_bf16 v[92:95], a[112:115], v[56:59], v[92:95]\n"
"buffer_load_dwordx4 v[44:47], v1, s[4:7], s96 offen offset:1024 sc1\n"
"v_mfma_f32_16x16x32_bf16 v[92:95], a[108:111], v[48:51], v[92:95]\n"
"buffer_load_dword v250, v10, s[4:7], s98 offen offset:1280 sc1\n"
"v_mfma_f32_16x16x32_bf16 v[92:95], a[104:107], v[192:195], v[92:95]\n"
"ds_write_b128 v3, v[36:39] offset:2048\n"
"v_mfma_f32_16x16x32_bf16 v[92:95], a[100:103], v[40:43], v[92:95]\n"
"ds_write_b128 v2, v[172:175] offset:17408\n"
"v_mfma_f32_16x16x32_bf16 v[92:95], a[96:99], v[32:35], v[92:95]\n"
"buffer_load_dword v249, v10, s[4:7], s98 offen offset:1536 sc1\n"
"v_mfma_f32_16x16x32_bf16 v[36:39], v[100:103], v[32:35], 0\n"
"buffer_load_dwordx4 v[152:155], v1, s[4:7], s96 offen offset:2048 sc1\n"
"v_mfma_f32_16x16x32_bf16 v[36:39], v[104:107], v[40:43], v[36:39]\n"
"buffer_store_dwordx4 v[224:227], v8, s[4:7], s64 offen offset:1024 sc1\n"
"v_mfma_f32_16x16x32_bf16 v[36:39], v[108:111], v[192:195], v[36:39]\n"
"buffer_load_dword v248, v10, s[4:7], s98 offen offset:1792 sc1\n"
"v_mfma_f32_16x16x32_bf16 v[36:39], v[116:119], v[48:51], v[36:39]\n"
"v_mfma_f32_16x16x32_bf16 v[36:39], v[128:131], v[56:59], v[36:39]\n"
"v_mfma_f32_16x16x32_bf16 v[36:39], v[132:135], v[220:223], v[36:39]\n"
"buffer_load_dwordx4 v[172:175], v1, s[4:7], s96 offen offset:3072 sc1\n"
"v_mfma_f32_16x16x32_bf16 v[36:39], v[136:139], v[168:171], v[36:39]\n"
"ds_write_b128 v3, v[92:95] offset:3072\n"
"v_mfma_f32_16x16x32_bf16 v[36:39], v[96:99], v[28:31], v[36:39]\n"
"v_mfma_f32_16x16x32_bf16 v[92:95], v[112:115], v[28:31], 0\n"
"v_mfma_f32_16x16x32_bf16 v[92:95], v[164:167], v[168:171], v[92:95]\n"
"v_mfma_f32_16x16x32_bf16 v[92:95], v[160:163], v[220:223], v[92:95]\n"
"v_mfma_f32_16x16x32_bf16 v[92:95], v[156:159], v[56:59], v[92:95]\n"
"v_mfma_f32_16x16x32_bf16 v[92:95], v[124:127], v[48:51], v[92:95]\n"
"buffer_load_dwordx4 a[208:211], v1, s[4:7], s97 offen sc1\n"
"v_mfma_f32_16x16x32_bf16 v[92:95], v[148:151], v[192:195], v[92:95]\n"
"s_nop 0\n"
"ds_write_b128 v3, v[36:39] offset:4096\n"
"v_mfma_f32_16x16x32_bf16 v[92:95], v[144:147], v[40:43], v[92:95]\n"
"v_mfma_f32_16x16x32_bf16 v[92:95], v[120:123], v[32:35], v[92:95]\n"
"s_waitcnt vmcnt(26)\n"
"; @A G1 3.1\n"
"v_mfma_f32_16x16x32_bf16 v[28:31], a[0:3], v[84:87], 0\n"
"v_mfma_f32_16x16x32_bf16 v[28:31], a[4:7], v[88:91], v[28:31]\n"
"v_mfma_f32_16x16x32_bf16 v[28:31], a[8:11], v[200:203], v[28:31]\n"
"s_add_u32 s98, s55, 0\n"
"buffer_load_dwordx4 v[32:35], v0, s[4:7], s98 offen sc1\n"
"v_mfma_f32_16x16x32_bf16 v[28:31], a[12:15], v[196:199], v[28:31]\n"
"s_waitcnt vmcnt(21)\n"
"v_mfma_f32_16x16x32_bf16 v[28:31], a[16:19], v[52:55], v[28:31]\n"
"v_mfma_f32_16x16x32_bf16 v[28:31], a[20:23], v[80:83], v[28:31]\n"
"ds_write_b128 v3, v[92:95] offset:5120\n"
"v_mfma_f32_16x16x32_bf16 v[28:31], a[24:27], v[140:143], v[28:31]\n"
"v_mfma_f32_16x16x32_bf16 v[28:31], a[28:31], v[188:191], v[28:31]\n"
"buffer_load_dwordx4 v[36:39], v0, s[4:7], s98 offen offset:1024 sc1\n"
"s_add_u32 s96, s64, 0x4000\n"
"v_mfma_f32_16x16x32_bf16 v[40:43], a[60:63], v[188:191], 0\n"
"v_mfma_f32_16x16x32_bf16 v[40:43], a[56:59], v[140:143], v[40:43]\n"
"buffer_store_dwordx4 v[24:27], v7, s[4:7], s96 offen offset:1024 sc1\n"
"v_mfma_f32_16x16x32_bf16 v[40:43], a[52:55], v[80:83], v[40:43]\n"
"v_mfma_f32_16x16x32_bf16 v[40:43], a[48:51], v[52:55], v[40:43]\n"
"v_mfma_f32_16x16x32_bf16 v[40:43], a[44:47], v[196:199], v[40:43]\n"
"buffer_load_dwordx4 v[24:27], v0, s[4:7], s98 offen offset:2048 sc1\n"
"v_mfma_f32_16x16x32_bf16 v[40:43], a[40:43], v[200:203], v[40:43]\n"
"ds_write_b128 v3, v[28:31] offset:6144\n"
"v_mfma_f32_16x16x32_bf16 v[40:43], a[36:39], v[88:91], v[40:43]\n"
"buffer_load_dwordx4 a[212:215], v1, s[4:7], s97 offen offset:1024 sc1\n"
"v_mfma_f32_16x16x32_bf16 v[40:43], a[32:35], v[84:87], v[40:43]\n"
"s_mov_b32 s20, 0xff0000\n"
"s_mov_b32 s21, 0xffffffff\n"
"s_mov_b32 s22, 0x1ff0001\n"
"v_mfma_f32_16x16x32_bf16 v[28:31], a[64:67], v[84:87], 0\n"
"s_mov_b32 s23, 0xffffffff\n"
"s_mov_b32 s24, 0x3ff0003\n"
"s_mov_b32 s25, 0xffffffff\n"
"v_mfma_f32_16x16x32_bf16 v[28:31], a[68:71], v[88:91], v[28:31]\n"
"buffer_load_dwordx4 v[48:51], v0, s[4:7], s98 offen offset:3072 sc1\n"
"v_mfma_f32_16x16x32_bf16 v[28:31], a[72:75], v[200:203], v[28:31]\n"
"s_mov_b32 s26, 0x7ff0007\n"
"s_mov_b32 s27, 0xffffffff\n"
"s_mov_b32 s28, 0xfff000f\n"
"v_mfma_f32_16x16x32_bf16 v[28:31], a[76:79], v[196:199], v[28:31]\n"
"s_mov_b32 s29, 0xffffffff\n"
"s_mov_b32 s30, 0x1fff001f\n"
"s_mov_b32 s31, 0xffffffff\n"
"v_mfma_f32_16x16x32_bf16 v[28:31], a[80:83], v[52:55], v[28:31]\n"
"s_mov_b32 s32, 0x3fff003f\n"
"s_mov_b32 s33, 0xffffffff\n"
"s_mov_b32 s34, 0x7fff007f\n"
"v_mfma_f32_16x16x32_bf16 v[28:31], a[84:87], v[80:83], v[28:31]\n"
"ds_write_b128 v3, v[40:43] offset:7168\n"
"v_mfma_f32_16x16x32_bf16 v[28:31], a[88:91], v[140:143], v[28:31]\n"
"buffer_load_dwordx4 v[40:43], v1, s[4:7], s98 offen sc1\n"
"v_mfma_f32_16x16x32_bf16 v[28:31], a[92:95], v[188:191], v[28:31]\n"
"s_mov_b32 s35, 0xffffffff\n"
"s_mov_b32 s36, 0\n"
"s_mov_b32 s37, 0xff0000\n"
"v_mfma_f32_16x16x32_bf16 v[56:59], a[124:127], v[188:191], 0\n"
"s_mov_b32 s38, 0\n"
"s_mov_b32 s39, 0x1ff0001\n"
"s_mov_b32 s40, 0\n"
"v_mfma_f32_16x16x32_bf16 v[56:59], a[120:123], v[140:143], v[56:59]\n"
"s_mov_b32 s41, 0x3ff0003\n"
"s_mov_b32 s42, 0\n"
"s_mov_b32 s43, 0x7ff0007\n"
"v_mfma_f32_16x16x32_bf16 v[56:59], a[116:119], v[80:83], v[56:59]\n"
"s_mov_b32 s44, 0\n"
"s_mov_b32 s45, 0xfff000f\n"
"s_mov_b32 s46, 0\n"
"v_mfma_f32_16x16x32_bf16 v[56:59], a[112:115], v[52:55], v[56:59]\n"
"buffer_load_dwordx4 v[92:95], v1, s[4:7], s98 offen offset:1024 sc1\n"
"v_mfma_f32_16x16x32_bf16 v[56:59], a[108:111], v[196:199], v[56:59]\n"
"s_mov_b32 s47, 0x1fff001f\n"
"s_mov_b32 s48, 0\n"
"s_mov_b32 s49, 0x3fff003f\n"
"v_mfma_f32_16x16x32_bf16 v[56:59], a[104:107], v[200:203], v[56:59]\n"
"ds_write_b128 v3, v[28:31] offset:8192\n"
"v_mfma_f32_16x16x32_bf16 v[56:59], a[100:103], v[88:91], v[56:59]\n"
"s_mov_b32 s50, 0\n"
"s_mov_b32 s51, 0x7fff007f\n"
"v_mfma_f32_16x16x32_bf16 v[56:59], a[96:99], v[84:87], v[56:59]\n"
"v_mfma_f32_16x16x32_bf16 v[28:31], v[100:103], v[84:87], 0\n"
"buffer_load_dwordx4 v[168:171], v1, s[4:7], s98 offen offset:2048 sc1\n"
"v_mfma_f32_16x16x32_bf16 v[28:31], v[104:107], v[88:91], v[28:31]\n"
"buffer_load_dwordx4 a[216:219], v1, s[4:7], s97 offen offset:2048 sc1\n"
"v_mfma_f32_16x16x32_bf16 v[28:31], v[108:111], v[200:203], v[28:31]\n"
"v_mfma_f32_16x16x32_bf16 v[28:31], v[116:119], v[196:199], v[28:31]\n"
"v_mfma_f32_16x16x32_bf16 v[28:31], v[128:131], v[52:55], v[28:31]\n"
"s_waitcnt vmcnt(12)\n"
"v_add_f32_e32 v255, v255, v254\n"
"v_add_f32_e32 v253, v253, v250\n"
"v_mfma_f32_16x16x32_bf16 v[28:31], v[132:135], v[80:83], v[28:31]\n"
"buffer_load_dwordx4 v[176:179], v1, s[4:7], s98 offen offset:3072 sc1\n"
"v_mfma_f32_16x16x32_bf16 v[28:31], v[136:139], v[140:143], v[28:31]\n"
"ds_write_b128 v3, v[56:59] offset:9216\n"
"v_mfma_f32_16x16x32_bf16 v[28:31], v[96:99], v[188:191], v[28:31]\n"
"s_add_u32 s98, s75, 0x1000\n"
"buffer_load_dwordx2 v[246:247], v13, s[4:7], s98 offen sc1\n"
"v_mfma_f32_16x16x32_bf16 v[56:59], v[112:115], v[188:191], 0\n"
"v_add_f32_e32 v252, v252, v249\n"
"v_add_f32_e32 v251, v251, v248\n"
"v_mfma_f32_16x16x32_bf16 v[56:59], v[164:167], v[140:143], v[56:59]\n"
"v_add_f32_e32 v255, v255, v252\n"
"v_add_f32_e32 v253, v253, v251\n"
"v_mfma_f32_16x16x32_bf16 v[56:59], v[160:163], v[80:83], v[56:59]\n"
"s_add_u32 s96, s71, 0\n"
"buffer_load_dwordx2 v[250:251], off, s[4:7], s96 sc1\n"
"v_mfma_f32_16x16x32_bf16 v[56:59], v[156:159], v[52:55], v[56:59]\n"
"v_add_f32_e32 v255, v255, v253\n"
"v_mov_b32_e32 v253, v255\n"
"v_mfma_f32_16x16x32_bf16 v[56:59], v[124:127], v[196:199], v[56:59]\n"
"s_nop 0\n"
"v_permlane32_swap_b32_e32 v255, v253\n"
"v_add_f32_e32 v255, v255, v253\n"
"v_mfma_f32_16x16x32_bf16 v[56:59], v[148:151], v[200:203], v[56:59]\n"
"ds_write_b128 v3, v[28:31] offset:10240\n"
"v_mfma_f32_16x16x32_bf16 v[56:59], v[144:147], v[88:91], v[56:59]\n"
"buffer_store_dwordx2 v[18:19], v11, s[4:7], s68 offen offset:32 sc1\n"
"v_mul_f32_e32 v255, 0x3a800000, v255\n"
"v_mov_b32_e32 v253, v255\n"
"v_mfma_f32_16x16x32_bf16 v[56:59], v[120:123], v[84:87], v[56:59]\n"
"s_nop 0\n"
"v_permlane16_swap_b32_e32 v255, v253\n"
"v_cvt_pk_bf16_f32 v254, -v255, 0\n"
"; @A G1 3.2\n"
"v_mfma_f32_16x16x32_bf16 v[28:31], a[0:3], v[60:63], 0\n"
"v_fma_f32 v253, -v255, v255, v253\n"
"v_lshlrev_b32_e32 v254, 16, v254\n"
"v_mfma_f32_16x16x32_bf16 v[28:31], a[4:7], v[64:67], v[28:31]\n"
"v_max_f32_e32 v253, 0, v253\n"
"v_cvt_pk_bf16_f32 v253, v253, 0\n"
"v_mfma_f32_16x16x32_bf16 v[28:31], a[8:11], v[72:75], v[28:31]\n"
"buffer_load_dwordx4 a[220:223], v1, s[4:7], s97 offen offset:3072 sc1\n"
"v_mfma_f32_16x16x32_bf16 v[28:31], a[12:15], v[76:79], v[28:31]\n"
"s_waitcnt vmcnt(16)\n"
"v_lshlrev_b32_e32 v253, 16, v253\n"
"v_add_f32_e32 v253, 0x3727c5ac, v253\n"
"v_mfma_f32_16x16x32_bf16 v[28:31], a[16:19], v[68:71], v[28:31]\n"
"s_add_u32 s97, s70, 0x6000\n"
"v_cvt_pk_bf16_f32 v253, v253, 0\n"
"v_lshlrev_b32_e32 v253, 16, v253\n"
"v_mfma_f32_16x16x32_bf16 v[28:31], a[20:23], v[44:47], v[28:31]\n"
"ds_write_b128 v3, v[56:59] offset:11264\n"
"v_mfma_f32_16x16x32_bf16 v[28:31], a[24:27], v[152:155], v[28:31]\n"
"buffer_load_dwordx4 a[224:227], v0, s[4:7], s97 offen sc1\n"
"v_mfma_f32_16x16x32_bf16 v[28:31], a[28:31], v[172:175], v[28:31]\n"
"v_sqrt_f32_e32 v253, v253\n"
"v_mfma_f32_16x16x32_bf16 v[52:55], a[60:63], v[172:175], 0\n"
"v_cvt_pk_bf16_f32 v253, v253, 0\n"
"v_lshlrev_b32_e32 v253, 16, v253\n"
"v_mfma_f32_16x16x32_bf16 v[52:55], a[56:59], v[152:155], v[52:55]\n"
"v_rcp_f32_e32 v255, v253\n"
"v_mfma_f32_16x16x32_bf16 v[52:55], a[52:55], v[44:47], v[52:55]\n"
"s_waitcnt lgkmcnt(0)\n"
"s_barrier\n"
"v_mfma_f32_16x16x32_bf16 v[52:55], a[48:51], v[68:71], v[52:55]\n"
"buffer_load_dwordx4 a[228:231], v0, s[4:7], s97 offen offset:1024 sc1\n"
"v_mfma_f32_16x16x32_bf16 v[52:55], a[44:47], v[76:79], v[52:55]\n"
"ds_read_b128 v[56:59], v4\n"
"v_mul_f32_e32 v253, 0x3e8293ee, v255\n"
"v_mfma_f32_16x16x32_bf16 v[52:55], a[40:43], v[72:75], v[52:55]\n"
"ds_write_b128 v3, v[28:31] offset:12288\n"
"v_mfma_f32_16x16x32_bf16 v[52:55], a[36:39], v[64:67], v[52:55]\n"
"ds_read_b128 v[28:31], v9 offset:128\n"
"ds_read_b128 v[80:83], v5\n"
"v_mfma_f32_16x16x32_bf16 v[52:55], a[32:35], v[60:63], v[52:55]\n"
"buffer_load_dwordx4 a[232:235], v0, s[4:7], s97 offen offset:2048 sc1\n"
"v_mfma_f32_16x16x32_bf16 v[84:87], a[64:67], v[60:63], 0\n"
"v_mfma_f32_16x16x32_bf16 v[84:87], a[68:71], v[64:67], v[84:87]\n"
"v_mfma_f32_16x16x32_bf16 v[84:87], a[72:75], v[72:75], v[84:87]\n"
"ds_read_b128 v[88:91], v4 offset:1024\n"
"ds_read_b128 v[140:143], v9 offset:192\n"
"v_mfma_f32_16x16x32_bf16 v[84:87], a[76:79], v[76:79], v[84:87]\n"
"buffer_load_dwordx4 a[236:239], v0, s[4:7], s97 offen offset:3072 sc1\n"
"v_mfma_f32_16x16x32_bf16 v[84:87], a[80:83], v[68:71], v[84:87]\n"
"ds_read_b128 v[180:183], v5 offset:1024\n"
"s_waitcnt lgkmcnt(3)\n"
"v_mfma_f32_16x16x32_bf16 v[84:87], a[84:87], v[44:47], v[84:87]\n"
"ds_write_b128 v3, v[52:55] offset:13312\n"
"v_mfma_f32_16x16x32_bf16 v[84:87], a[88:91], v[152:155], v[84:87]\n"
"v_fmac_f32_e32 v56, v254, v28\n"
"v_fmac_f32_e32 v57, v254, v29\n"
"v_mfma_f32_16x16x32_bf16 v[84:87], a[92:95], v[172:175], v[84:87]\n"
"buffer_load_dwordx4 a[240:243], v1, s[4:7], s97 offen sc1\n"
"v_mfma_f32_16x16x32_bf16 v[52:55], a[124:127], v[172:175], 0\n"
"v_fmac_f32_e32 v58, v254, v30\n"
"v_fmac_f32_e32 v59, v254, v31\n"
"v_mfma_f32_16x16x32_bf16 v[52:55], a[120:123], v[152:155], v[52:55]\n"
"ds_read_b128 v[28:31], v4 offset:2048\n"
"ds_read_b128 v[184:187], v9 offset:256\n"
"v_mfma_f32_16x16x32_bf16 v[52:55], a[116:119], v[44:47], v[52:55]\n"
"ds_read_b128 v[188:191], v5 offset:2048\n"
"v_add_f32_e32 v56, v56, v80\n"
"v_mfma_f32_16x16x32_bf16 v[52:55], a[112:115], v[68:71], v[52:55]\n"
"buffer_load_dwordx4 a[244:247], v1, s[4:7], s97 offen offset:1024 sc1\n"
"v_mfma_f32_16x16x32_bf16 v[52:55], a[108:111], v[76:79], v[52:55]\n"
"v_add_f32_e32 v57, v57, v81\n"
"v_add_f32_e32 v58, v58, v82\n"
"v_mfma_f32_16x16x32_bf16 v[52:55], a[104:107], v[72:75], v[52:55]\n"
"ds_write_b128 v3, v[84:87] offset:14336\n"
"v_mfma_f32_16x16x32_bf16 v[52:55], a[100:103], v[64:67], v[52:55]\n"
"s_waitcnt lgkmcnt(5)\n"
"v_add_f32_e32 v59, v59, v83\n"
"v_fmac_f32_e32 v88, v254, v140\n"
"v_mfma_f32_16x16x32_bf16 v[52:55], a[96:99], v[60:63], v[52:55]\n"
"buffer_load_dwordx4 a[248:251], v1, s[4:7], s97 offen offset:2048 sc1\n"
"v_mfma_f32_16x16x32_bf16 v[80:83], v[100:103], v[60:63], 0\n"
"ds_read_b128 v[84:87], v4 offset:3072\n"
"ds_read_b128 v[192:195], v9 offset:320\n"
"v_mfma_f32_16x16x32_bf16 v[80:83], v[104:107], v[64:67], v[80:83]\n"
"ds_read_b128 v[196:199], v5 offset:3072\n"
"v_fmac_f32_e32 v89, v254, v141\n"
"v_mfma_f32_16x16x32_bf16 v[80:83], v[108:111], v[72:75], v[80:83]\n"
"v_fmac_f32_e32 v90, v254, v142\n"
"v_fmac_f32_e32 v91, v254, v143\n"
"v_mfma_f32_16x16x32_bf16 v[80:83], v[116:119], v[76:79], v[80:83]\n"
"buffer_load_dwordx4 a[252:255], v1, s[4:7], s97 offen offset:3072 sc1\n"
"v_mfma_f32_16x16x32_bf16 v[80:83], v[128:131], v[68:71], v[80:83]\n"
"s_waitcnt vmcnt(11)\n"
"v_cmp_ne_u64_e32 vcc, v[246:247], v[18:19]\n"
"v_mfma_f32_16x16x32_bf16 v[80:83], v[132:135], v[44:47], v[80:83]\n"
"ds_write_b128 v3, v[52:55] offset:15360\n"
"v_mfma_f32_16x16x32_bf16 v[80:83], v[136:139], v[152:155], v[80:83]\n"
"s_cbranch_vccnz .Lmk_f7s\n"
".Lmk_f7r:\n"
"v_add_f32_e32 v88, v88, v180\n"
"v_mfma_f32_16x16x32_bf16 v[80:83], v[96:99], v[172:175], v[80:83]\n"
"ds_read_b128 v[52:55], v4 offset:4096\n"
"ds_read_b128 v[140:143], v9\n"
"v_mfma_f32_16x16x32_bf16 v[200:203], v[112:115], v[172:175], 0\n"
"ds_read_b128 v[172:175], v5 offset:4096\n"
"v_add_f32_e32 v89, v89, v181\n"
"v_mfma_f32_16x16x32_bf16 v[200:203], v[164:167], v[152:155], v[200:203]\n"
"s_waitcnt lgkmcnt(8)\n"
"v_add_f32_e32 v90, v90, v182\n"
"v_add_f32_e32 v91, v91, v183\n"
"v_mfma_f32_16x16x32_bf16 v[200:203], v[160:163], v[44:47], v[200:203]\n"
"v_fmac_f32_e32 v28, v254, v184\n"
"v_fmac_f32_e32 v29, v254, v185\n"
"v_mfma_f32_16x16x32_bf16 v[200:203], v[156:159], v[68:71], v[200:203]\n"
"v_fmac_f32_e32 v30, v254, v186\n"
"v_fmac_f32_e32 v31, v254, v187\n"
"v_mfma_f32_16x16x32_bf16 v[200:203], v[124:127], v[76:79], v[200:203]\n"
"s_waitcnt vmcnt(10)\n"
"v_cmp_ne_u64_e32 vcc, v[250:251], v[18:19]\n"
"v_mfma_f32_16x16x32_bf16 v[200:203], v[148:151], v[72:75], v[200:203]\n"
"ds_write_b128 v3, v[80:83] offset:16384\n"
"v_mfma_f32_16x16x32_bf16 v[200:203], v[144:147], v[64:67], v[200:203]\n"
"s_cbranch_vccnz .Lmk_f4s\n"
".Lmk_f4r:\n"
"s_add_u32 m0, s77, 0x6000\n"
"v_mfma_f32_16x16x32_bf16 v[200:203], v[120:123], v[60:63], v[200:203]\n"
"s_add_u32 s98, s65, 0x2000\n"
"ds_read_b128 v[44:47], v4 offset:5120\n"
"; @A G1 3.3\n"
"v_mfma_f32_16x16x32_bf16 v[56:59], a[0:3], v[32:35], v[56:59]\n"
"buffer_load_dwordx4 v2, s[4:7], s98 offen sc1 lds\n"
"v_mfma_f32_16x16x32_bf16 v[56:59], a[4:7], v[36:39], v[56:59]\n"
"buffer_load_dwordx4 v2, s[4:7], s98 offen offset:1024 sc1 lds\n"
"v_mfma_f32_16x16x32_bf16 v[56:59], a[8:11], v[24:27], v[56:59]\n"
"ds_read_b128 v[60:63], v9 offset:64\n"
"ds_read_b128 v[64:67], v5 offset:5120\n"
"v_mfma_f32_16x16x32_bf16 v[56:59], a[12:15], v[48:51], v[56:59]\n"
"v_add_f32_e32 v28, v28, v188\n"
"v_add_f32_e32 v29, v29, v189\n"
"v_mfma_f32_16x16x32_bf16 v[56:59], a[16:19], v[40:43], v[56:59]\n"
"s_waitcnt lgkmcnt(8)\n"
"v_add_f32_e32 v30, v30, v190\n"
"v_add_f32_e32 v31, v31, v191\n"
"v_mfma_f32_16x16x32_bf16 v[56:59], a[20:23], v[92:95], v[56:59]\n"
"ds_write_b128 v3, v[200:203] offset:17408\n"
"v_mfma_f32_16x16x32_bf16 v[56:59], a[24:27], v[168:171], v[56:59]\n"
"v_fmac_f32_e32 v84, v254, v192\n"
"v_fmac_f32_e32 v85, v254, v193\n"
"v_mfma_f32_16x16x32_bf16 v[56:59], a[28:31], v[176:179], v[56:59]\n"
"s_add_u32 m0, s77, 0xe000\n"
"s_add_u32 s98, s65, 0x6000\n"
"v_fmac_f32_e32 v86, v254, v194\n"
"v_mfma_f32_16x16x32_bf16 v[88:91], a[60:63], v[176:179], v[88:91]\n"
"buffer_load_dwordx4 v2, s[4:7], s98 offen sc1 lds\n"
"v_mfma_f32_16x16x32_bf16 v[88:91], a[56:59], v[168:171], v[88:91]\n"
"buffer_load_dwordx4 v2, s[4:7], s98 offen offset:1024 sc1 lds\n"
"v_mfma_f32_16x16x32_bf16 v[88:91], a[52:55], v[92:95], v[88:91]\n"
"s_waitcnt lgkmcnt(0)\n"
"s_barrier\n"
"v_mfma_f32_16x16x32_bf16 v[88:91], a[48:51], v[40:43], v[88:91]\n"
"ds_read_b128 v[68:71], v6\n"
"ds_read_b128 v[72:75], v6 offset:1024\n"
"v_mfma_f32_16x16x32_bf16 v[88:91], a[44:47], v[48:51], v[88:91]\n"
"s_add_u32 s98, s69, 0x600000\n"
"buffer_load_dwordx4 a[0:3], v0, s[4:7], s98 offen sc1\n"
"v_mfma_f32_16x16x32_bf16 v[88:91], a[40:43], v[24:27], v[88:91]\n"
"v_fmac_f32_e32 v87, v254, v195\n"
"v_add_f32_e32 v84, v84, v196\n"
"v_mfma_f32_16x16x32_bf16 v[88:91], a[36:39], v[36:39], v[88:91]\n"
"v_add_f32_e32 v85, v85, v197\n"
"v_add_f32_e32 v86, v86, v198\n"
"v_mfma_f32_16x16x32_bf16 v[88:91], a[32:35], v[32:35], v[88:91]\n"
"buffer_load_dwordx4 a[4:7], v0, s[4:7], s98 offen offset:1024 sc1\n"
"v_mfma_f32_16x16x32_bf16 v[28:31], a[64:67], v[32:35], v[28:31]\n"
"v_add_f32_e32 v87, v87, v199\n"
"v_fmac_f32_e32 v52, v254, v140\n"
"v_mfma_f32_16x16x32_bf16 v[28:31], a[68:71], v[36:39], v[28:31]\n"
"v_fmac_f32_e32 v53, v254, v141\n"
"v_fmac_f32_e32 v54, v254, v142\n"
"v_mfma_f32_16x16x32_bf16 v[28:31], a[72:75], v[24:27], v[28:31]\n"
"buffer_load_dwordx4 a[8:11], v0, s[4:7], s98 offen offset:2048 sc1\n"
"v_mfma_f32_16x16x32_bf16 v[28:31], a[76:79], v[48:51], v[28:31]\n"
"v_fmac_f32_e32 v55, v254, v143\n"
"v_add_f32_e32 v52, v52, v172\n"
"v_mfma_f32_16x16x32_bf16 v[28:31], a[80:83], v[40:43], v[28:31]\n"
"s_waitcnt lgkmcnt(0)\n"
"v_add_f32_e32 v53, v53, v173\n"
"v_add_f32_e32 v54, v54, v174\n"
"v_mfma_f32_16x16x32_bf16 v[28:31], a[84:87], v[92:95], v[28:31]\n"
"buffer_load_dwordx4 a[12:15], v0, s[4:7], s98 offen offset:3072 sc1\n"
"v_mfma_f32_16x16x32_bf16 v[28:31], a[88:91], v[168:171], v[28:31]\n"
"ds_read_b128 v[76:79], v6 offset:2048\n"
"ds_read_b128 v[80:83], v6 offset:3072\n"
"v_mfma_f32_16x16x32_bf16 v[28:31], a[92:95], v[176:179], v[28:31]\n"
"v_add_f32_e32 v55, v55, v175\n"
"v_fmac_f32_e32 v44, v254, v60\n"
"v_mfma_f32_16x16x32_bf16 v[84:87], a[124:127], v[176:179], v[84:87]\n"
"buffer_load_dwordx4 a[16:19], v1, s[4:7], s98 offen sc1\n"
"v_mfma_f32_16x16x32_bf16 v[84:87], a[120:123], v[168:171], v[84:87]\n"
"v_fmac_f32_e32 v45, v254, v61\n"
"v_fmac_f32_e32 v46, v254, v62\n"
"v_mfma_f32_16x16x32_bf16 v[84:87], a[116:119], v[92:95], v[84:87]\n"
"v_fmac_f32_e32 v47, v254, v63\n"
"v_add_f32_e32 v44, v44, v64\n"
"v_mfma_f32_16x16x32_bf16 v[84:87], a[112:115], v[40:43], v[84:87]\n"
"buffer_load_dwordx4 a[20:23], v1, s[4:7], s98 offen offset:1024 sc1\n"
"v_mfma_f32_16x16x32_bf16 v[84:87], a[108:111], v[48:51], v[84:87]\n"
"v_add_f32_e32 v45, v45, v65\n"
"v_add_f32_e32 v46, v46, v66\n"
"v_mfma_f32_16x16x32_bf16 v[84:87], a[104:107], v[24:27], v[84:87]\n"
"v_add_f32_e32 v47, v47, v67\n"
"v_add_f32_e32 v56, v56, v68\n"
"v_mfma_f32_16x16x32_bf16 v[84:87], a[100:103], v[36:39], v[84:87]\n"
"buffer_load_dwordx4 a[24:27], v1, s[4:7], s98 offen offset:2048 sc1\n"
"v_mfma_f32_16x16x32_bf16 v[84:87], a[96:99], v[32:35], v[84:87]\n"
"v_add_f32_e32 v57, v57, v69\n"
"v_add_f32_e32 v58, v58, v70\n"
"v_mfma_f32_16x16x32_bf16 v[52:55], v[100:103], v[32:35], v[52:55]\n"
"v_add_f32_e32 v59, v59, v71\n"
"v_add_f32_e32 v88, v88, v72\n"
"v_mfma_f32_16x16x32_bf16 v[52:55], v[104:107], v[36:39], v[52:55]\n"
"buffer_load_dwordx4 a[28:31], v1, s[4:7], s98 offen offset:3072 sc1\n"
"v_mfma_f32_16x16x32_bf16 v[52:55], v[108:111], v[24:27], v[52:55]\n"
"v_add_f32_e32 v89, v89, v73\n"
"v_add_f32_e32 v90, v90, v74\n"
"v_mfma_f32_16x16x32_bf16 v[52:55], v[116:119], v[48:51], v[52:55]\n"
"v_add_f32_e32 v91, v91, v75\n"
"v_mul_f32_e32 v56, v56, v255\n"
"v_mfma_f32_16x16x32_bf16 v[52:55], v[128:131], v[40:43], v[52:55]\n"
"s_waitcnt lgkmcnt(0)\n"
"v_mul_f32_e32 v57, v57, v255\n"
"v_mul_f32_e32 v58, v58, v255\n"
"v_mfma_f32_16x16x32_bf16 v[52:55], v[132:135], v[92:95], v[52:55]\n"
"v_mul_f32_e32 v59, v59, v255\n"
"v_mul_f32_e32 v88, v88, v255\n"
"v_mfma_f32_16x16x32_bf16 v[52:55], v[136:139], v[168:171], v[52:55]\n"
"ds_read_b128 v[60:63], v6 offset:4096\n"
"ds_read_b128 v[64:67], v6 offset:5120\n"
"v_mfma_f32_16x16x32_bf16 v[52:55], v[96:99], v[176:179], v[52:55]\n"
"v_mul_f32_e32 v89, v89, v255\n"
"v_mul_f32_e32 v90, v90, v255\n"
"v_mfma_f32_16x16x32_bf16 v[44:47], v[112:115], v[176:179], v[44:47]\n"
"v_mul_f32_e32 v91, v91, v255\n"
"v_cvt_pk_bf16_f32 v68, v56, v57\n"
"v_mfma_f32_16x16x32_bf16 v[44:47], v[164:167], v[168:171], v[44:47]\n"
"v_cvt_pk_bf16_f32 v69, v58, v59\n"
"v_cvt_pk_bf16_f32 v70, v88, v89\n"
"v_mfma_f32_16x16x32_bf16 v[44:47], v[160:163], v[92:95], v[44:47]\n"
"v_cvt_pk_bf16_f32 v71, v90, v91\n"
"v_add_f32_e32 v28, v28, v76\n"
"v_mfma_f32_16x16x32_bf16 v[44:47], v[156:159], v[40:43], v[44:47]\n"
"ds_write_b128 v7, v[68:71] offset:16384\n"
"v_mfma_f32_16x16x32_bf16 v[44:47], v[124:127], v[48:51], v[44:47]\n"
"buffer_store_dwordx4 v[68:71], v7, s[4:7], s64 offen sc1\n"
"v_mfma_f32_16x16x32_bf16 v[44:47], v[148:151], v[24:27], v[44:47]\n"
"v_add_f32_e32 v29, v29, v77\n"
"v_add_f32_e32 v30, v30, v78\n"
"v_mfma_f32_16x16x32_bf16 v[44:47], v[144:147], v[36:39], v[44:47]\n"
"v_add_f32_e32 v31, v31, v79\n"
"v_add_f32_e32 v84, v84, v80\n"
"v_mfma_f32_16x16x32_bf16 v[44:47], v[120:123], v[32:35], v[44:47]\n"
"v_add_f32_e32 v85, v85, v81\n"
"v_add_f32_e32 v86, v86, v82\n"
"v_add_f32_e32 v87, v87, v83\n"
"v_mul_f32_e32 v28, v28, v255\n"
"v_mul_f32_e32 v29, v29, v255\n"
"v_mul_f32_e32 v30, v30, v255\n"
"v_mul_f32_e32 v31, v31, v255\n"
"v_mul_f32_e32 v84, v84, v255\n"
"v_mul_f32_e32 v85, v85, v255\n"
"v_mul_f32_e32 v86, v86, v255\n"
"v_mul_f32_e32 v87, v87, v255\n"
"v_cvt_pk_bf16_f32 v24, v28, v29\n"
"v_cvt_pk_bf16_f32 v25, v30, v31\n"
"v_cvt_pk_bf16_f32 v26, v84, v85\n"
"v_cvt_pk_bf16_f32 v27, v86, v87\n"
"v_xor_b32_e32 v255, 32, v16\n"
"v_xor_b32_e32 v254, 32, v15\n"
"v_mov_b32_e32 v88, 0x3f803f80\n"
"v_mov_b32_e32 v89, 0x3f803f80\n"
"v_mov_b32_e32 v90, 0x3f803f80\n"
"v_mov_b32_e32 v91, 0x3f803f80\n"
"v_mov_b32_e32 v252, 0x53800000\n"
"ds_write_b128 v7, v[24:27] offset:49152\n"
".Lmk_z4:\n"
"; @A tail\n"
"s_add_u32 s98, s64, 0x4000\n"
"buffer_store_dwordx4 v[24:27], v7, s[4:7], s98 offen sc1\n"
"s_waitcnt lgkmcnt(2)\n"
"v_add_f32_e32 v52, v52, v60\n"
"v_add_f32_e32 v53, v53, v61\n"
"v_add_f32_e32 v54, v54, v62\n"
"v_add_f32_e32 v55, v55, v63\n"
"v_add_f32_e32 v44, v44, v64\n"
"v_add_f32_e32 v45, v45, v65\n"
"v_add_f32_e32 v46, v46, v66\n"
"v_add_f32_e32 v47, v47, v67\n"
"v_mul_f32_e32 v52, v52, v253\n"
"v_mul_f32_e32 v53, v53, v253\n"
"v_mul_f32_e32 v54, v54, v253\n"
"v_mul_f32_e32 v55, v55, v253\n"
"v_mul_f32_e32 v44, v44, v253\n"
"v_mul_f32_e32 v45, v45, v253\n"
"v_mul_f32_e32 v46, v46, v253\n"
"v_mul_f32_e32 v47, v47, v253\n"
"v_cvt_pk_bf16_f32 v24, v52, v53\n"
"v_cvt_pk_bf16_f32 v25, v54, v55\n"
"v_cvt_pk_bf16_f32 v26, v44, v45\n"
"v_cvt_pk_bf16_f32 v27, v46, v47\n"
"ds_write_b128 v2, v[24:27] offset:16384\n"
"s_waitcnt lgkmcnt(0)\n"
"s_barrier\n"
"ds_read_b128 v[28:31], v2 offset:49152\n"
"ds_read_b128 v[32:35], v2 offset:50176\n"
"ds_read_b128 v[92:95], v2 offset:16384\n"
"ds_read_b128 v[96:99], v2 offset:17408\n"
"; @A AT b\n"
"s_mov_b32 s2, 0\n"
"v_mov_b32_e32 v253, 0xff800000\n"
"v_mov_b32_e32 v251, 0xff800000\n"
"v_mov_b32_e32 v250, 0x7f800000\n"
"v_mov_b32_e32 v249, 0x7f800000\n"
".Lmk_a15:\n"
"ds_read_b128 v[24:27], v15 offset:24576\n"
"ds_read_b128 v[36:39], v254 offset:24832\n"
"ds_read_b128 v[40:43], v15 offset:22528\n"
"ds_read_b128 v[44:47], v254 offset:22784\n"
"s_cmp_gt_i32 s78, 11\n"
"s_cselect_b32 s98, 0x80000000, 0\n"
"s_or_b32 s98, s98, 0x7f800000\n"
"s_waitcnt lgkmcnt(2)\n"
"v_mfma_f32_16x16x32_bf16 v[48:51], v[24:27], v[28:31], 0\n"
"v_mfma_f32_16x16x32_bf16 v[52:55], v[24:27], v[32:35], 0\n"
"v_mfma_f32_16x16x32_bf16 v[56:59], v[36:39], v[32:35], 0\n"
"v_mfma_f32_16x16x32_bf16 v[60:63], v[36:39], v[28:31], 0\n"
"s_waitcnt lgkmcnt(0)\n"
"s_nop 3\n"
"v_max_f32_e32 v248, v48, v49\n"
"v_max_f32_e32 v247, v52, v53\n"
"v_max3_f32 v248, v248, v50, v51\n"
"v_max3_f32 v247, v247, v54, v55\n"
"v_max3_f32 v248, v248, v60, v61\n"
"v_max3_f32 v247, v247, v56, v57\n"
"v_max3_f32 v248, v248, v62, v63\n"
"v_max3_f32 v247, v247, v58, v59\n"
"v_mov_b32_e32 v246, v248\n"
"v_mov_b32_e32 v245, v247\n"
"s_nop 0\n"
"v_permlane16_swap_b32_e32 v248, v246\n"
"v_permlane16_swap_b32_e32 v247, v245\n"
"v_max_f32_e32 v248, v248, v246\n"
"v_max_f32_e32 v247, v247, v245\n"
"v_mov_b32_e32 v246, v248\n"
"v_mov_b32_e32 v245, v247\n"
"s_nop 0\n"
"v_permlane32_swap_b32_e32 v248, v246\n"
"v_permlane32_swap_b32_e32 v247, v245\n"
"v_max_f32_e32 v248, v248, v246\n"
"v_max_f32_e32 v247, v247, v245\n"
"v_med3_f32 v248, v248, v253, v250\n"
"v_med3_f32 v247, v247, v251, v249\n"
"v_min_f32_e64 v24, s98, -v248\n"
"v_min_f32_e64 v25, s98, -v248\n"
"v_min_f32_e64 v26, s98, -v248\n"
"v_min_f32_e64 v27, s98, -v248\n"
"v_min_f32_e64 v36, s98, -v247\n"
"v_min_f32_e64 v37, s98, -v247\n"
"v_min_f32_e64 v38, s98, -v247\n"
"v_min_f32_e64 v39, s98, -v247\n"
"v_mfma_f32_16x16x32_bf16 v[64:67], v[40:43], v[28:31], v[24:27]\n"
"ds_read_b128 v[68:71], v15 offset:20480\n"
"s_add_u32 s98, s69, 0x602000\n"
"buffer_load_dwordx4 a[32:35], v0, s[4:7], s98 offen sc1\n"
"v_sub_f32_e32 v48, v48, v248\n"
"v_sub_f32_e32 v49, v49, v248\n"
"v_sub_f32_e32 v50, v50, v248\n"
"v_mfma_f32_16x16x32_bf16 v[72:75], v[40:43], v[32:35], v[36:39]\n"
"buffer_load_dwordx4 a[36:39], v0, s[4:7], s98 offen offset:1024 sc1\n"
"v_sub_f32_e32 v51, v51, v248\n"
"v_sub_f32_e32 v60, v60, v248\n"
"v_sub_f32_e32 v61, v61, v248\n"
"v_sub_f32_e32 v62, v62, v248\n"
"v_sub_f32_e32 v63, v63, v248\n"
"v_mfma_f32_16x16x32_bf16 v[40:43], v[44:47], v[32:35], v[36:39]\n"
"ds_read_b128 v[76:79], v254 offset:20736\n"
"v_sub_f32_e32 v52, v52, v247\n"
"v_sub_f32_e32 v53, v53, v247\n"
"v_sub_f32_e32 v54, v54, v247\n"
"v_sub_f32_e32 v55, v55, v247\n"
"v_mfma_f32_16x16x32_bf16 v[80:83], v[44:47], v[28:31], v[24:27]\n"
"buffer_load_dwordx4 a[40:43], v0, s[4:7], s98 offen offset:2048 sc1\n"
"v_sub_f32_e32 v56, v56, v247\n"
"v_sub_f32_e32 v57, v57, v247\n"
"v_sub_f32_e32 v58, v58, v247\n"
"v_sub_f32_e32 v59, v59, v247\n"
"s_waitcnt lgkmcnt(0)\n"
"v_mfma_f32_16x16x32_bf16 v[44:47], v[68:71], v[28:31], v[24:27]\n"
"ds_read_b128 v[84:87], v15 offset:18432\n"
"buffer_load_dwordx4 a[44:47], v0, s[4:7], s98 offen offset:3072 sc1\n"
"ds_read_b128 v[100:103], v254 offset:18688\n"
"v_exp_f32_e32 v64, v64\n"
"v_exp_f32_e32 v65, v65\n"
"v_exp_f32_e32 v66, v66\n"
"v_exp_f32_e32 v67, v67\n"
"v_exp_f32_e32 v80, v80\n"
"v_exp_f32_e32 v81, v81\n"
"v_mfma_f32_16x16x32_bf16 v[104:107], v[68:71], v[32:35], v[36:39]\n"
"buffer_load_dwordx4 a[48:51], v1, s[4:7], s98 offen sc1\n"
"ds_read_b64_tr_b16 v[68:69], v16 offset:22528\n"
"buffer_load_dwordx4 a[52:55], v1, s[4:7], s98 offen offset:1024 sc1\n"
"ds_read_b64_tr_b16 v[70:71], v255 offset:22784\n"
"v_exp_f32_e32 v82, v82\n"
"v_exp_f32_e32 v83, v83\n"
"v_cvt_pk_bf16_f32 v108, v64, v65\n"
"v_cvt_pk_bf16_f32 v109, v66, v67\n"
"v_cvt_pk_bf16_f32 v110, v80, v81\n"
"v_cvt_pk_bf16_f32 v111, v82, v83\n"
"v_exp_f32_e32 v72, v72\n"
"v_mfma_f32_16x16x32_bf16 v[64:67], v[76:79], v[32:35], v[36:39]\n"
"s_add_u32 s97, s76, 0x80000\n"
"v_exp_f32_e32 v73, v73\n"
"v_exp_f32_e32 v74, v74\n"
"v_exp_f32_e32 v75, v75\n"
"v_bfe_u32 v248, v0, 2, 7\n"
"v_add_lshl_u32 v248, s72, v248, 1\n"
"v_and_b32_e32 v247, 0x200, v0\n"
"v_lshl_add_u32 v248, v247, 4, v248\n"
"v_mfma_f32_16x16x32_bf16 v[112:115], v[76:79], v[28:31], v[24:27]\n"
"buffer_load_dword v247, v248, s[4:7], s97 offen sc1\n"
"ds_read_b64_tr_b16 v[80:81], v255 offset:22528\n"
"buffer_load_dword v246, v248, s[4:7], s97 offen offset:1024 sc1\n"
"ds_read_b64_tr_b16 v[82:83], v16 offset:22784\n"
"v_exp_f32_e32 v40, v40\n"
"v_exp_f32_e32 v41, v41\n"
"v_exp_f32_e32 v42, v42\n"
"v_exp_f32_e32 v43, v43\n"
"v_cvt_pk_bf16_f32 v76, v72, v73\n"
"v_cvt_pk_bf16_f32 v77, v74, v75\n"
"v_cvt_pk_bf16_f32 v78, v40, v41\n"
"v_cvt_pk_bf16_f32 v79, v42, v43\n"
"s_waitcnt lgkmcnt(4)\n"
"v_mfma_f32_16x16x32_bf16 v[40:43], v[84:87], v[28:31], v[24:27]\n"
"ds_read_b128 v[72:75], v15 offset:16384\n"
"v_exp_f32_e32 v44, v44\n"
"v_exp_f32_e32 v45, v45\n"
"v_exp_f32_e32 v46, v46\n"
"v_mfma_f32_16x16x32_bf16 v[116:119], v[84:87], v[32:35], v[36:39]\n"
"buffer_load_dwordx4 a[56:59], v1, s[4:7], s98 offen offset:2048 sc1\n"
"ds_read_b128 v[84:87], v254 offset:16640\n"
"v_exp_f32_e32 v47, v47\n"
"v_exp_f32_e32 v112, v112\n"
"v_mfma_f32_16x16x32_bf16 v[120:123], v[100:103], v[32:35], v[36:39]\n"
"ds_read_b64_tr_b16 v[124:125], v16 offset:20480\n"
"v_exp_f32_e32 v113, v113\n"
"v_exp_f32_e32 v114, v114\n"
"v_mfma_f32_16x16x32_bf16 v[128:131], v[100:103], v[28:31], v[24:27]\n"
"buffer_load_dwordx4 a[60:63], v1, s[4:7], s98 offen offset:3072 sc1\n"
"v_exp_f32_e32 v115, v115\n"
"v_cvt_pk_bf16_f32 v100, v44, v45\n"
"v_cvt_pk_bf16_f32 v101, v46, v47\n"
"s_waitcnt lgkmcnt(3)\n"
"v_mfma_f32_16x16x32_bf16 v[44:47], v[88:91], v[108:111], 0\n"
"ds_read_b64_tr_b16 v[126:127], v255 offset:20736\n"
"s_add_u32 s98, s69, 0x604000\n"
"buffer_load_dwordx4 a[64:67], v0, s[4:7], s98 offen sc1\n"
"v_cvt_pk_bf16_f32 v102, v112, v113\n"
"v_cvt_pk_bf16_f32 v103, v114, v115\n"
"v_exp_f32_e32 v104, v104\n"
"v_mfma_f32_16x16x32_bf16 v[112:115], v[68:71], v[108:111], 0\n"
"ds_read_b64_tr_b16 v[132:133], v255 offset:20480\n"
"v_exp_f32_e32 v105, v105\n"
"v_exp_f32_e32 v106, v106\n"
"v_mfma_f32_16x16x32_bf16 v[136:139], v[80:83], v[108:111], 0\n"
"buffer_load_dword v245, v248, s[4:7], s97 offen offset:2048 sc1\n"
"v_exp_f32_e32 v107, v107\n"
"v_exp_f32_e32 v64, v64\n"
"v_mfma_f32_16x16x32_bf16 v[108:111], v[80:83], v[76:79], 0\n"
"ds_read_b64_tr_b16 v[134:135], v16 offset:20736\n"
"v_exp_f32_e32 v65, v65\n"
"v_exp_f32_e32 v66, v66\n"
"v_mfma_f32_16x16x32_bf16 v[80:83], v[68:71], v[76:79], 0\n"
"buffer_load_dword v244, v248, s[4:7], s97 offen offset:3072 sc1\n"
"s_waitcnt vmcnt(13)\n"
"buffer_store_dwordx2 v[18:19], v11, s[4:7], s68 offen offset:64 sc1\n"
"v_exp_f32_e32 v67, v67\n"
"v_mfma_f32_16x16x32_bf16 v[68:71], v[88:91], v[76:79], 0\n"
"s_cmp_lg_u32 s2, 0\n"
"v_cvt_pk_bf16_f32 v76, v104, v105\n"
"v_cvt_pk_bf16_f32 v77, v106, v107\n"
"v_cvt_pk_bf16_f32 v78, v64, v65\n"
"v_cvt_pk_bf16_f32 v79, v66, v67\n"
"s_cbranch_scc1 .Lmk_s17\n"
"s_barrier\n"
".Lmk_s17:\n"
"s_waitcnt lgkmcnt(4)\n"
"v_mfma_f32_16x16x32_bf16 v[64:67], v[72:75], v[28:31], v[24:27]\n"
"ds_read_b128 v[104:107], v15 offset:14336\n"
"v_exp_f32_e32 v40, v40\n"
"v_exp_f32_e32 v41, v41\n"
"v_exp_f32_e32 v42, v42\n"
"v_mfma_f32_16x16x32_bf16 v[140:143], v[72:75], v[32:35], v[36:39]\n"
"buffer_load_dwordx4 a[68:71], v0, s[4:7], s98 offen offset:1024 sc1\n"
"ds_read_b128 v[72:75], v254 offset:14592\n"
"v_exp_f32_e32 v43, v43\n"
"v_mfma_f32_16x16x32_bf16 v[144:147], v[84:87], v[32:35], v[36:39]\n"
"v_exp_f32_e32 v128, v128\n"
"v_exp_f32_e32 v129, v129\n"
"v_exp_f32_e32 v130, v130\n"
"v_mfma_f32_16x16x32_bf16 v[148:151], v[84:87], v[28:31], v[24:27]\n"
"buffer_load_dwordx4 a[72:75], v0, s[4:7], s98 offen offset:2048 sc1\n"
"ds_read_b64_tr_b16 v[84:85], v16 offset:18432\n"
"v_exp_f32_e32 v131, v131\n"
"s_waitcnt lgkmcnt(3)\n"
"v_mfma_f32_16x16x32_bf16 v[44:47], v[88:91], v[100:103], v[44:47]\n"
"buffer_load_dwordx4 a[76:79], v0, s[4:7], s98 offen offset:3072 sc1\n"
"v_cvt_pk_bf16_f32 v152, v40, v41\n"
"v_cvt_pk_bf16_f32 v153, v42, v43\n"
"v_cvt_pk_bf16_f32 v154, v128, v129\n"
"v_cvt_pk_bf16_f32 v155, v130, v131\n"
"v_mfma_f32_16x16x32_bf16 v[112:115], v[124:127], v[100:103], v[112:115]\n"
"ds_read_b64_tr_b16 v[86:87], v255 offset:18688\n"
"v_exp_f32_e32 v116, v116\n"
"v_exp_f32_e32 v117, v117\n"
"v_exp_f32_e32 v118, v118\n"
"v_mfma_f32_16x16x32_bf16 v[136:139], v[132:135], v[100:103], v[136:139]\n"
"s_add_u32 s97, s76, 0x81000\n"
"buffer_load_dword v243, v248, s[4:7], s97 offen sc1\n"
"v_exp_f32_e32 v119, v119\n"
"v_mfma_f32_16x16x32_bf16 v[108:111], v[132:135], v[76:79], v[108:111]\n"
"ds_read_b64_tr_b16 v[40:41], v255 offset:18432\n"
"v_exp_f32_e32 v120, v120\n"
"v_exp_f32_e32 v121, v121\n"
"v_mfma_f32_16x16x32_bf16 v[80:83], v[124:127], v[76:79], v[80:83]\n"
"buffer_load_dword v242, v248, s[4:7], s97 offen offset:1024 sc1\n"
"ds_read_b64_tr_b16 v[42:43], v16 offset:18688\n"
"v_exp_f32_e32 v122, v122\n"
"v_exp_f32_e32 v123, v123\n"
"v_mfma_f32_16x16x32_bf16 v[68:71], v[88:91], v[76:79], v[68:71]\n"
"s_waitcnt lgkmcnt(4)\n"
"v_cvt_pk_bf16_f32 v76, v116, v117\n"
"v_cvt_pk_bf16_f32 v77, v118, v119\n"
"v_cvt_pk_bf16_f32 v78, v120, v121\n"
"v_cvt_pk_bf16_f32 v79, v122, v123\n"
"s_cmp_eq_i32 s78, 8\n"
"s_cbranch_scc1 .Lmk_x18s\n"
".Lmk_x18r:\n"
"v_mfma_f32_16x16x32_bf16 v[100:103], v[104:107], v[28:31], v[24:27]\n"
"ds_read_b128 v[116:119], v15 offset:12288\n"
"v_exp_f32_e32 v64, v64\n"
"v_exp_f32_e32 v65, v65\n"
"v_exp_f32_e32 v66, v66\n"
"v_mfma_f32_16x16x32_bf16 v[120:123], v[104:107], v[32:35], v[36:39]\n"
"buffer_load_dwordx4 a[80:83], v1, s[4:7], s98 offen sc1\n"
"ds_read_b128 v[104:107], v254 offset:12544\n"
"v_exp_f32_e32 v67, v67\n"
"v_mfma_f32_16x16x32_bf16 v[124:127], v[72:75], v[32:35], v[36:39]\n"
"v_exp_f32_e32 v148, v148\n"
"v_exp_f32_e32 v149, v149\n"
"v_exp_f32_e32 v150, v150\n"
"v_mfma_f32_16x16x32_bf16 v[128:131], v[72:75], v[28:31], v[24:27]\n"
"buffer_load_dwordx4 a[84:87], v1, s[4:7], s98 offen offset:1024 sc1\n"
"ds_read_b64_tr_b16 v[72:73], v16 offset:16384\n"
"v_exp_f32_e32 v151, v151\n"
"s_waitcnt lgkmcnt(3)\n"
"v_mfma_f32_16x16x32_bf16 v[44:47], v[88:91], v[152:155], v[44:47]\n"
"buffer_load_dwordx4 a[88:91], v1, s[4:7], s98 offen offset:2048 sc1\n"
"v_cvt_pk_bf16_f32 v132, v64, v65\n"
"v_cvt_pk_bf16_f32 v133, v66, v67\n"
"v_cvt_pk_bf16_f32 v134, v148, v149\n"
"v_cvt_pk_bf16_f32 v135, v150, v151\n"
"v_mfma_f32_16x16x32_bf16 v[112:115], v[84:87], v[152:155], v[112:115]\n"
"ds_read_b64_tr_b16 v[74:75], v255 offset:16640\n"
"v_exp_f32_e32 v140, v140\n"
"v_exp_f32_e32 v141, v141\n"
"v_mfma_f32_16x16x32_bf16 v[136:139], v[40:43], v[152:155], v[136:139]\n"
"buffer_load_dword v241, v248, s[4:7], s97 offen offset:2048 sc1\n"
"v_exp_f32_e32 v142, v142\n"
"v_exp_f32_e32 v143, v143\n"
"v_mfma_f32_16x16x32_bf16 v[108:111], v[40:43], v[76:79], v[108:111]\n"
"ds_read_b64_tr_b16 v[40:41], v255 offset:16384\n"
"v_exp_f32_e32 v144, v144\n"
"v_exp_f32_e32 v145, v145\n"
"v_mfma_f32_16x16x32_bf16 v[80:83], v[84:87], v[76:79], v[80:83]\n"
"buffer_load_dword v240, v248, s[4:7], s97 offen offset:3072 sc1\n"
"ds_read_b64_tr_b16 v[42:43], v16 offset:16640\n"
"v_exp_f32_e32 v146, v146\n"
"v_exp_f32_e32 v147, v147\n"
"v_mfma_f32_16x16x32_bf16 v[68:71], v[88:91], v[76:79], v[68:71]\n"
"s_waitcnt lgkmcnt(4)\n"
"v_cvt_pk_bf16_f32 v64, v140, v141\n"
"v_cvt_pk_bf16_f32 v65, v142, v143\n"
"v_cvt_pk_bf16_f32 v66, v144, v145\n"
"v_cvt_pk_bf16_f32 v67, v146, v147\n"
"s_cmp_eq_i32 s78, 7\n"
"s_cbranch_scc1 .Lmk_x19s\n"
".Lmk_x19r:\n"
"v_mfma_f32_16x16x32_bf16 v[76:79], v[116:119], v[28:31], v[24:27]\n"
"ds_read_b128 v[84:87], v15 offset:10240\n"
"v_exp_f32_e32 v100, v100\n"
"v_exp_f32_e32 v101, v101\n"
"v_exp_f32_e32 v102, v102\n"
"v_mfma_f32_16x16x32_bf16 v[140:143], v[116:119], v[32:35], v[36:39]\n"
"buffer_load_dwordx4 a[92:95], v1, s[4:7], s98 offen offset:3072 sc1\n"
"v_exp_f32_e32 v103, v103\n"
"v_mfma_f32_16x16x32_bf16 v[116:119], v[104:107], v[32:35], v[36:39]\n"
"ds_read_b128 v[144:147], v254 offset:10496\n"
"v_exp_f32_e32 v128, v128\n"
"v_exp_f32_e32 v129, v129\n"
"v_mfma_f32_16x16x32_bf16 v[148:151], v[104:107], v[28:31], v[24:27]\n"
"ds_read_b64_tr_b16 v[104:105], v16 offset:14336\n"
"v_exp_f32_e32 v130, v130\n"
"v_exp_f32_e32 v131, v131\n"
"v_cvt_pk_bf16_f32 v152, v100, v101\n"
"s_waitcnt lgkmcnt(3)\n"
"v_mfma_f32_16x16x32_bf16 v[44:47], v[88:91], v[132:135], v[44:47]\n"
"s_add_u32 s98, s69, 0x606000\n"
"buffer_load_dwordx4 a[96:99], v0, s[4:7], s98 offen sc1\n"
"v_cvt_pk_bf16_f32 v153, v102, v103\n"
"v_cvt_pk_bf16_f32 v154, v128, v129\n"
"v_mfma_f32_16x16x32_bf16 v[112:115], v[72:75], v[132:135], v[112:115]\n"
"ds_read_b64_tr_b16 v[106:107], v255 offset:14592\n"
"v_cvt_pk_bf16_f32 v155, v130, v131\n"
"v_exp_f32_e32 v120, v120\n"
"v_exp_f32_e32 v121, v121\n"
"v_mfma_f32_16x16x32_bf16 v[136:139], v[40:43], v[132:135], v[136:139]\n"
"ds_read_b64_tr_b16 v[100:101], v255 offset:14336\n"
"v_exp_f32_e32 v122, v122\n"
"v_exp_f32_e32 v123, v123\n"
"v_mfma_f32_16x16x32_bf16 v[108:111], v[40:43], v[64:67], v[108:111]\n"
"buffer_load_dwordx4 a[100:103], v0, s[4:7], s98 offen offset:1024 sc1\n"
"v_exp_f32_e32 v124, v124\n"
"v_exp_f32_e32 v125, v125\n"
"v_mfma_f32_16x16x32_bf16 v[80:83], v[72:75], v[64:67], v[80:83]\n"
"ds_read_b64_tr_b16 v[102:103], v16 offset:14592\n"
"v_exp_f32_e32 v126, v126\n"
"v_exp_f32_e32 v127, v127\n"
"v_mfma_f32_16x16x32_bf16 v[68:71], v[88:91], v[64:67], v[68:71]\n"
"s_waitcnt lgkmcnt(4)\n"
"v_cvt_pk_bf16_f32 v40, v120, v121\n"
"v_cvt_pk_bf16_f32 v41, v122, v123\n"
"v_cvt_pk_bf16_f32 v42, v124, v125\n"
"v_cvt_pk_bf16_f32 v43, v126, v127\n"
"s_cmp_eq_i32 s78, 6\n"
"s_cbranch_scc1 .Lmk_x20s\n"
".Lmk_x20r:\n"
"v_mfma_f32_16x16x32_bf16 v[64:67], v[84:87], v[28:31], v[24:27]\n"
"ds_read_b128 v[72:75], v15 offset:8192\n"
"v_exp_f32_e32 v76, v76\n"
"v_exp_f32_e32 v77, v77\n"
"v_exp_f32_e32 v78, v78\n"
"v_mfma_f32_16x16x32_bf16 v[120:123], v[84:87], v[32:35], v[36:39]\n"
"buffer_load_dwordx4 a[104:107], v0, s[4:7], s98 offen offset:2048 sc1\n"
"v_exp_f32_e32 v79, v79\n"
"v_mfma_f32_16x16x32_bf16 v[84:87], v[144:147], v[32:35], v[36:39]\n"
"ds_read_b128 v[124:127], v254 offset:8448\n"
"v_exp_f32_e32 v148, v148\n"
"v_exp_f32_e32 v149, v149\n"
"v_mfma_f32_16x16x32_bf16 v[128:131], v[144:147], v[28:31], v[24:27]\n"
"ds_read_b64_tr_b16 v[132:133], v16 offset:12288\n"
"v_exp_f32_e32 v150, v150\n"
"v_exp_f32_e32 v151, v151\n"
"s_waitcnt lgkmcnt(3)\n"
"v_mfma_f32_16x16x32_bf16 v[44:47], v[88:91], v[152:155], v[44:47]\n"
"buffer_load_dwordx4 a[108:111], v0, s[4:7], s98 offen offset:3072 sc1\n"
"v_cvt_pk_bf16_f32 v144, v76, v77\n"
"v_cvt_pk_bf16_f32 v145, v78, v79\n"
"v_cvt_pk_bf16_f32 v146, v148, v149\n"
"v_mfma_f32_16x16x32_bf16 v[112:115], v[104:107], v[152:155], v[112:115]\n"
"ds_read_b64_tr_b16 v[134:135], v255 offset:12544\n"
"v_cvt_pk_bf16_f32 v147, v150, v151\n"
"v_exp_f32_e32 v140, v140\n"
"v_exp_f32_e32 v141, v141\n"
"v_mfma_f32_16x16x32_bf16 v[136:139], v[100:103], v[152:155], v[136:139]\n"
"ds_read_b64_tr_b16 v[76:77], v255 offset:12288\n"
"v_exp_f32_e32 v142, v142\n"
"v_exp_f32_e32 v143, v143\n"
"v_mfma_f32_16x16x32_bf16 v[108:111], v[100:103], v[40:43], v[108:111]\n"
"buffer_load_dwordx4 a[112:115], v1, s[4:7], s98 offen sc1\n"
"v_exp_f32_e32 v116, v116\n"
"v_exp_f32_e32 v117, v117\n"
"v_mfma_f32_16x16x32_bf16 v[80:83], v[104:107], v[40:43], v[80:83]\n"
"ds_read_b64_tr_b16 v[78:79], v16 offset:12544\n"
"v_exp_f32_e32 v118, v118\n"
"v_exp_f32_e32 v119, v119\n"
"v_mfma_f32_16x16x32_bf16 v[68:71], v[88:91], v[40:43], v[68:71]\n"
"s_waitcnt lgkmcnt(4)\n"
"v_cvt_pk_bf16_f32 v40, v140, v141\n"
"v_cvt_pk_bf16_f32 v41, v142, v143\n"
"v_cvt_pk_bf16_f32 v42, v116, v117\n"
"v_cvt_pk_bf16_f32 v43, v118, v119\n"
"s_cmp_eq_i32 s78, 5\n"
"s_cbranch_scc1 .Lmk_x21s\n"
".Lmk_x21r:\n"
"v_mfma_f32_16x16x32_bf16 v[100:103], v[72:75], v[28:31], v[24:27]\n"
"ds_read_b64_tr_b16 v[104:105], v16 offset:10240\n"
"ds_read_b64_tr_b16 v[106:107], v255 offset:10496\n"
"v_exp_f32_e32 v64, v64\n"
"v_exp_f32_e32 v65, v65\n"
"v_mfma_f32_16x16x32_bf16 v[116:119], v[72:75], v[32:35], v[36:39]\n"
"s_add_u32 s97, s71, 0xfffff000\n"
"buffer_load_dwordx2 v[238:239], off, s[4:7], s97 sc1\n"
"v_exp_f32_e32 v66, v66\n"
"v_exp_f32_e32 v67, v67\n"
"v_mfma_f32_16x16x32_bf16 v[72:75], v[124:127], v[32:35], v[36:39]\n"
"ds_read_b64_tr_b16 v[36:37], v255 offset:10240\n"
"v_exp_f32_e32 v128, v128\n"
"v_exp_f32_e32 v129, v129\n"
"v_mfma_f32_16x16x32_bf16 v[140:143], v[124:127], v[28:31], v[24:27]\n"
"ds_read_b64_tr_b16 v[38:39], v16 offset:10496\n"
"buffer_load_dwordx4 a[116:119], v1, s[4:7], s98 offen offset:1024 sc1\n"
"v_exp_f32_e32 v130, v130\n"
"v_exp_f32_e32 v131, v131\n"
"s_waitcnt lgkmcnt(4)\n"
"v_mfma_f32_16x16x32_bf16 v[44:47], v[88:91], v[144:147], v[44:47]\n"
"ds_read_b64_tr_b16 v[124:125], v16 offset:24576\n"
"ds_read_b64_tr_b16 v[126:127], v255 offset:24832\n"
"v_cvt_pk_bf16_f32 v148, v64, v65\n"
"v_cvt_pk_bf16_f32 v149, v66, v67\n"
"v_cvt_pk_bf16_f32 v150, v128, v129\n"
"v_mfma_f32_16x16x32_bf16 v[112:115], v[132:135], v[144:147], v[112:115]\n"
"ds_read_b64_tr_b16 v[64:65], v255 offset:24576\n"
"buffer_load_dwordx4 a[120:123], v1, s[4:7], s98 offen offset:2048 sc1\n"
"v_cvt_pk_bf16_f32 v151, v130, v131\n"
"v_exp_f32_e32 v120, v120\n"
"v_mfma_f32_16x16x32_bf16 v[136:139], v[76:79], v[144:147], v[136:139]\n"
"ds_read_b64_tr_b16 v[66:67], v16 offset:24832\n"
"v_exp_f32_e32 v121, v121\n"
"v_exp_f32_e32 v122, v122\n"
"v_exp_f32_e32 v123, v123\n"
"v_mfma_f32_16x16x32_bf16 v[108:111], v[76:79], v[40:43], v[108:111]\n"
"v_exp_f32_e32 v84, v84\n"
"v_add_u32_e32 v248, 0x26400, v0\n"
"v_exp_f32_e32 v85, v85\n"
"v_mfma_f32_16x16x32_bf16 v[80:83], v[132:135], v[40:43], v[80:83]\n"
"buffer_load_dwordx4 a[124:127], v1, s[4:7], s98 offen offset:3072 sc1\n"
"ds_read_b128 v[76:79], v248\n"
"v_exp_f32_e32 v86, v86\n"
"v_exp_f32_e32 v87, v87\n"
"v_mfma_f32_16x16x32_bf16 v[68:71], v[88:91], v[40:43], v[68:71]\n"
"ds_read_b128 v[128:131], v248 offset:1024\n"
"v_cvt_pk_bf16_f32 v40, v120, v121\n"
"v_cvt_pk_bf16_f32 v41, v122, v123\n"
"v_cvt_pk_bf16_f32 v42, v84, v85\n"
"v_cvt_pk_bf16_f32 v43, v86, v87\n"
"s_waitcnt lgkmcnt(6)\n"
"v_mfma_f32_16x16x32_bf16 v[44:47], v[88:91], v[148:151], v[44:47]\n"
"ds_read_b64_tr_b16 v[84:85], v16 offset:8192\n"
"s_add_u32 s98, s69, 0x608000\n"
"buffer_load_dwordx4 v[24:27], v0, s[4:7], s98 offen sc1\n"
"ds_read_b64_tr_b16 v[86:87], v255 offset:8448\n"
"v_cndmask_b32_e64 v100, v48, v100, s[20:21]\n"
"v_cndmask_b32_e64 v101, v49, v101, s[22:23]\n"
"v_cndmask_b32_e64 v102, v50, v102, s[24:25]\n"
"v_cndmask_b32_e64 v103, v51, v103, s[26:27]\n"
"v_cndmask_b32_e64 v140, v60, v140, s[28:29]\n"
"v_cndmask_b32_e64 v141, v61, v141, s[30:31]\n"
"v_cndmask_b32_e64 v142, v62, v142, s[32:33]\n"
"v_mfma_f32_16x16x32_bf16 v[112:115], v[104:107], v[148:151], v[112:115]\n"
"ds_read_b64_tr_b16 v[48:49], v255 offset:8192\n"
"ds_read_b64_tr_b16 v[50:51], v16 offset:8448\n"
"v_cndmask_b32_e64 v143, v63, v143, s[34:35]\n"
"v_exp_f32_e32 v100, v100\n"
"v_exp_f32_e32 v101, v101\n"
"v_exp_f32_e32 v102, v102\n"
"s_waitcnt vmcnt(11)\n"
"v_exp_f32_e32 v103, v103\n"
"v_exp_f32_e32 v140, v140\n"
"v_mfma_f32_16x16x32_bf16 v[136:139], v[36:39], v[148:151], v[136:139]\n"
"v_add_f32_e32 v247, v247, v246\n"
"v_exp_f32_e32 v141, v141\n"
"v_exp_f32_e32 v142, v142\n"
"v_add_f32_e32 v247, v247, v245\n"
"v_exp_f32_e32 v143, v143\n"
"v_cvt_pk_bf16_f32 v60, v100, v101\n"
"v_add_f32_e32 v247, v247, v244\n"
"v_cvt_pk_bf16_f32 v61, v102, v103\n"
"v_cvt_pk_bf16_f32 v62, v140, v141\n"
"v_mfma_f32_16x16x32_bf16 v[108:111], v[36:39], v[40:43], v[108:111]\n"
"v_cvt_pk_bf16_f32 v63, v142, v143\n"
"v_add_f32_e32 v247, v247, v243\n"
"v_cndmask_b32_e64 v116, v52, v116, s[36:37]\n"
"v_cndmask_b32_e64 v117, v53, v117, s[38:39]\n"
"v_cndmask_b32_e64 v118, v54, v118, s[40:41]\n"
"v_cndmask_b32_e64 v119, v55, v119, s[42:43]\n"
"v_add_f32_e32 v247, v247, v242\n"
"v_cndmask_b32_e64 v72, v56, v72, s[44:45]\n"
"v_cndmask_b32_e64 v73, v57, v73, s[46:47]\n"
"v_cndmask_b32_e64 v74, v58, v74, s[48:49]\n"
"v_add_f32_e32 v247, v247, v241\n"
"v_cndmask_b32_e64 v75, v59, v75, s[50:51]\n"
"v_exp_f32_e32 v116, v116\n"
"v_mfma_f32_16x16x32_bf16 v[80:83], v[104:107], v[40:43], v[80:83]\n"
"v_exp_f32_e32 v117, v117\n"
"v_add_f32_e32 v247, v247, v240\n"
"v_exp_f32_e32 v118, v118\n"
"v_mov_b32_e32 v246, v247\n"
"v_exp_f32_e32 v119, v119\n"
"v_exp_f32_e32 v72, v72\n"
"v_permlane32_swap_b32_e32 v247, v246\n"
"v_exp_f32_e32 v73, v73\n"
"v_mfma_f32_16x16x32_bf16 v[68:71], v[88:91], v[40:43], v[68:71]\n"
"v_exp_f32_e32 v74, v74\n"
"v_add_f32_e32 v247, v247, v246\n"
"v_exp_f32_e32 v75, v75\n"
"v_cvt_pk_bf16_f32 v36, v116, v117\n"
"ds_write_b32 v21, v247 offset:512\n"
"v_cvt_pk_bf16_f32 v37, v118, v119\n"
"v_cvt_pk_bf16_f32 v38, v72, v73\n"
"v_cvt_pk_bf16_f32 v39, v74, v75\n"
"s_waitcnt lgkmcnt(1)\n"
"v_mfma_f32_16x16x32_bf16 v[44:47], v[88:91], v[60:63], v[44:47]\n"
"v_and_b32_e32 v76, v60, v76\n"
"v_and_b32_e32 v77, v61, v77\n"
"v_and_b32_e32 v78, v62, v78\n"
"v_and_b32_e32 v79, v63, v79\n"
"v_mfma_f32_16x16x32_bf16 v[68:71], v[88:91], v[36:39], v[68:71]\n"
"v_xor_b32_e32 v40, v60, v76\n"
"v_xor_b32_e32 v41, v61, v77\n"
"v_xor_b32_e32 v42, v62, v78\n"
"v_xor_b32_e32 v43, v63, v79\n"
"v_mfma_f32_16x16x32_bf16 v[112:115], v[84:87], v[76:79], v[112:115]\n"
"v_and_b32_e32 v128, v36, v128\n"
"v_and_b32_e32 v129, v37, v129\n"
"v_mfma_f32_16x16x32_bf16 v[136:139], v[48:51], v[76:79], v[136:139]\n"
"v_and_b32_e32 v130, v38, v130\n"
"v_and_b32_e32 v131, v39, v131\n"
"v_mfma_f32_16x16x32_bf16 v[112:115], v[124:127], v[40:43], v[112:115]\n"
"v_xor_b32_e32 v52, v36, v128\n"
"v_xor_b32_e32 v53, v37, v129\n"
"v_mfma_f32_16x16x32_bf16 v[136:139], v[64:67], v[40:43], v[136:139]\n"
"v_xor_b32_e32 v54, v38, v130\n"
"v_xor_b32_e32 v55, v39, v131\n"
"v_mfma_f32_16x16x32_bf16 v[80:83], v[84:87], v[128:131], v[80:83]\n"
"s_waitcnt vmcnt(4)\n"
"v_cmp_ne_u64_e32 vcc, v[238:239], v[18:19]\n"
"s_nop 0\n"
"s_cbranch_vccnz .Lmk_f14s\n"
".Lmk_f14r:\n"
"v_mfma_f32_16x16x32_bf16 v[108:111], v[48:51], v[128:131], v[108:111]\n"
"s_add_u32 m0, s77, 0x4000\n"
"s_add_u32 s97, s65, 0\n"
"buffer_load_dwordx4 v2, s[4:7], s97 offen sc1 lds\n"
"buffer_load_dwordx4 v2, s[4:7], s97 offen offset:1024 sc1 lds\n"
"v_mfma_f32_16x16x32_bf16 v[80:83], v[124:127], v[52:55], v[80:83]\n"
"s_add_u32 m0, s77, 0xc000\n"
"s_add_u32 s97, s65, 0x4000\n"
"buffer_load_dwordx4 v2, s[4:7], s97 offen sc1 lds\n"
"buffer_load_dwordx4 v2, s[4:7], s97 offen offset:1024 sc1 lds\n"
"v_mfma_f32_16x16x32_bf16 v[108:111], v[64:67], v[52:55], v[108:111]\n"
"v_max_f32_e32 v248, v44, v68\n"
"v_rcp_f32_e32 v44, v44\n"
"v_rcp_f32_e32 v68, v68\n"
"s_nop 0\n"
"v_max3_f32 v248, v248, v44, v68\n"
"v_cmp_nlt_f32_e32 vcc, v248, v252\n"
"s_cbranch_vccnz .Lmk_b16\n"
".Lmk_k22:\n"
"s_add_u32 s97, s60, 0x40000\n"
"v_mul_f32_e32 v112, v112, v44\n"
"v_mul_f32_e32 v113, v113, v44\n"
"v_mul_f32_e32 v114, v114, v44\n"
"v_mul_f32_e32 v115, v115, v44\n"
"v_mul_f32_e32 v136, v136, v44\n"
"v_mul_f32_e32 v137, v137, v44\n"
"v_mul_f32_e32 v138, v138, v44\n"
"v_mul_f32_e32 v139, v139, v44\n"
"v_cvt_pk_bf16_f32 v28, v112, v113\n"
"v_cvt_pk_bf16_f32 v29, v114, v115\n"
"v_cvt_pk_bf16_f32 v30, v136, v137\n"
"v_cvt_pk_bf16_f32 v31, v138, v139\n"
"buffer_store_dwordx4 v[28:31], v0, s[4:7], s97 offen sc1\n"
"s_add_u32 s97, s60, 0x48000\n"
"v_mul_f32_e32 v80, v80, v68\n"
"v_mul_f32_e32 v81, v81, v68\n"
"v_mul_f32_e32 v82, v82, v68\n"
"v_mul_f32_e32 v83, v83, v68\n"
"v_mul_f32_e32 v108, v108, v68\n"
"v_mul_f32_e32 v109, v109, v68\n"
"v_mul_f32_e32 v110, v110, v68\n"
"v_mul_f32_e32 v111, v111, v68\n"
"v_cvt_pk_bf16_f32 v28, v80, v81\n"
"v_cvt_pk_bf16_f32 v29, v82, v83\n"
"v_cvt_pk_bf16_f32 v30, v108, v109\n"
"v_cvt_pk_bf16_f32 v31, v110, v111\n"
"buffer_store_dwordx4 v[28:31], v0, s[4:7], s97 offen sc1\n"
";\n"
"; @A AT a\n"
"s_mov_b32 s2, 0\n"
"v_mov_b32_e32 v253, 0xff800000\n"
"v_mov_b32_e32 v251, 0xff800000\n"
"v_mov_b32_e32 v250, 0x7f800000\n"
"v_mov_b32_e32 v249, 0x7f800000\n"
".Lmk_a23:\n"
"ds_read_b128 v[28:31], v15 offset:16384\n"
"ds_read_b128 v[32:35], v254 offset:16640\n"
"ds_read_b128 v[36:39], v15 offset:14336\n"
"ds_read_b128 v[40:43], v254 offset:14592\n"
"s_cmp_gt_i32 s78, 7\n"
"s_cselect_b32 s97, 0x80000000, 0\n"
"s_or_b32 s97, s97, 0x7f800000\n"
"s_add_u32 s96, s67, 32\n"
"buffer_load_dwordx2 v[246:247], v12, s[4:7], s96 offen sc1\n"
"s_waitcnt lgkmcnt(2)\n"
"v_mfma_f32_16x16x32_bf16 v[100:103], v[28:31], v[92:95], 0\n"
"v_mfma_f32_16x16x32_bf16 v[104:107], v[28:31], v[96:99], 0\n"
"v_mfma_f32_16x16x32_bf16 v[108:111], v[32:35], v[96:99], 0\n"
"v_mfma_f32_16x16x32_bf16 v[112:115], v[32:35], v[92:95], 0\n"
"s_waitcnt lgkmcnt(0)\n"
"s_nop 3\n"
"v_max_f32_e32 v248, v100, v101\n"
"v_max_f32_e32 v245, v104, v105\n"
"v_max3_f32 v248, v248, v102, v103\n"
"v_max3_f32 v245, v245, v106, v107\n"
"v_max3_f32 v248, v248, v112, v113\n"
"v_max3_f32 v245, v245, v108, v109\n"
"v_max3_f32 v248, v248, v114, v115\n"
"v_max3_f32 v245, v245, v110, v111\n"
"v_mov_b32_e32 v244, v248\n"
"v_mov_b32_e32 v243, v245\n"
"s_nop 0\n"
"v_permlane16_swap_b32_e32 v248, v244\n"
"v_permlane16_swap_b32_e32 v245, v243\n"
"v_max_f32_e32 v248, v248, v244\n"
"v_max_f32_e32 v245, v245, v243\n"
"v_mov_b32_e32 v244, v248\n"
"v_mov_b32_e32 v243, v245\n"
"s_nop 0\n"
"v_permlane32_swap_b32_e32 v248, v244\n"
"v_permlane32_swap_b32_e32 v245, v243\n"
"v_max_f32_e32 v248, v248, v244\n"
"v_max_f32_e32 v245, v245, v243\n"
"v_med3_f32 v248, v248, v253, v250\n"
"v_med3_f32 v245, v245, v251, v249\n"
"v_min_f32_e64 v116, s97, -v248\n"
"v_min_f32_e64 v117, s97, -v248\n"
"v_min_f32_e64 v118, s97, -v248\n"
"v_min_f32_e64 v119, s97, -v248\n"
"v_min_f32_e64 v120, s97, -v245\n"
"v_min_f32_e64 v121, s97, -v245\n"
"v_min_f32_e64 v122, s97, -v245\n"
"v_min_f32_e64 v123, s97, -v245\n"
"v_mfma_f32_16x16x32_bf16 v[44:47], v[36:39], v[92:95], v[116:119]\n"
"ds_read_b128 v[32:35], v15 offset:12288\n"
"v_sub_f32_e32 v100, v100, v248\n"
"v_sub_f32_e32 v101, v101, v248\n"
"v_sub_f32_e32 v102, v102, v248\n"
"v_sub_f32_e32 v103, v103, v248\n"
"v_mfma_f32_16x16x32_bf16 v[48:51], v[36:39], v[96:99], v[120:123]\n"
"v_sub_f32_e32 v112, v112, v248\n"
"v_sub_f32_e32 v113, v113, v248\n"
"v_sub_f32_e32 v114, v114, v248\n"
"v_sub_f32_e32 v115, v115, v248\n"
"v_mfma_f32_16x16x32_bf16 v[52:55], v[40:43], v[96:99], v[120:123]\n"
"ds_read_b128 v[56:59], v254 offset:12544\n"
"v_sub_f32_e32 v104, v104, v245\n"
"v_sub_f32_e32 v105, v105, v245\n"
"v_sub_f32_e32 v106, v106, v245\n"
"v_sub_f32_e32 v107, v107, v245\n"
"v_mfma_f32_16x16x32_bf16 v[36:39], v[40:43], v[92:95], v[116:119]\n"
"s_waitcnt lgkmcnt(0)\n"
"v_sub_f32_e32 v108, v108, v245\n"
"v_sub_f32_e32 v109, v109, v245\n"
"v_sub_f32_e32 v110, v110, v245\n"
"v_sub_f32_e32 v111, v111, v245\n"
"s_cmp_eq_i32 s78, 7\n"
"s_cbranch_scc1 .Lmk_x25s\n"
".Lmk_x25r:\n"
"v_mfma_f32_16x16x32_bf16 v[60:63], v[32:35], v[92:95], v[116:119]\n"
"ds_read_b128 v[40:43], v15 offset:10240\n"
"buffer_load_dwordx4 v[28:31], v0, s[4:7], s98 offen offset:1024 sc1\n"
"ds_read_b128 v[64:67], v254 offset:10496\n"
"v_exp_f32_e32 v44, v44\n"
"v_exp_f32_e32 v45, v45\n"
"v_exp_f32_e32 v46, v46\n"
"v_exp_f32_e32 v47, v47\n"
"v_exp_f32_e32 v36, v36\n"
"v_mfma_f32_16x16x32_bf16 v[68:71], v[32:35], v[96:99], v[120:123]\n"
"ds_read_b64_tr_b16 v[72:73], v16 offset:14336\n"
"buffer_load_dwordx4 v[32:35], v0, s[4:7], s98 offen offset:2048 sc1\n"
"v_exp_f32_e32 v37, v37\n"
"v_exp_f32_e32 v38, v38\n"
"v_exp_f32_e32 v39, v39\n"
"v_cvt_pk_bf16_f32 v76, v44, v45\n"
"v_cvt_pk_bf16_f32 v77, v46, v47\n"
"v_cvt_pk_bf16_f32 v78, v36, v37\n"
"v_mfma_f32_16x16x32_bf16 v[80:83], v[56:59], v[96:99], v[120:123]\n"
"ds_read_b64_tr_b16 v[74:75], v255 offset:14592\n"
"ds_read_b64_tr_b16 v[84:85], v255 offset:14336\n"
"v_cvt_pk_bf16_f32 v79, v38, v39\n"
"v_exp_f32_e32 v48, v48\n"
"v_exp_f32_e32 v49, v49\n"
"v_exp_f32_e32 v50, v50\n"
"v_exp_f32_e32 v51, v51\n"
"v_mfma_f32_16x16x32_bf16 v[124:127], v[56:59], v[92:95], v[116:119]\n"
"buffer_load_dwordx4 v[36:39], v0, s[4:7], s98 offen offset:3072 sc1\n"
"ds_read_b64_tr_b16 v[86:87], v16 offset:14592\n"
"v_exp_f32_e32 v52, v52\n"
"v_exp_f32_e32 v53, v53\n"
"v_exp_f32_e32 v54, v54\n"
"v_exp_f32_e32 v55, v55\n"
"v_cvt_pk_bf16_f32 v56, v48, v49\n"
"v_cvt_pk_bf16_f32 v57, v50, v51\n"
"v_cvt_pk_bf16_f32 v58, v52, v53\n"
"v_cvt_pk_bf16_f32 v59, v54, v55\n"
"s_waitcnt lgkmcnt(4)\n"
"s_cmp_eq_i32 s78, 6\n"
"s_cbranch_scc1 .Lmk_x26s\n"
".Lmk_x26r:\n"
"v_mfma_f32_16x16x32_bf16 v[128:131], v[40:43], v[92:95], v[116:119]\n"
"ds_read_b128 v[52:55], v15 offset:8192\n"
"v_exp_f32_e32 v60, v60\n"
"v_exp_f32_e32 v61, v61\n"
"v_exp_f32_e32 v62, v62\n"
"v_mfma_f32_16x16x32_bf16 v[132:135], v[40:43], v[96:99], v[120:123]\n"
"buffer_load_dwordx4 v[40:43], v1, s[4:7], s98 offen sc1\n"
"v_exp_f32_e32 v63, v63\n"
"v_mfma_f32_16x16x32_bf16 v[136:139], v[64:67], v[96:99], v[120:123]\n"
"ds_read_b128 v[140:143], v254 offset:8448\n"
"v_exp_f32_e32 v124, v124\n"
"v_exp_f32_e32 v125, v125\n"
"v_mfma_f32_16x16x32_bf16 v[144:147], v[64:67], v[92:95], v[116:119]\n"
"ds_read_b64_tr_b16 v[64:65], v16 offset:12288\n"
"v_exp_f32_e32 v126, v126\n"
"v_exp_f32_e32 v127, v127\n"
"s_waitcnt lgkmcnt(3)\n"
"v_mfma_f32_16x16x32_bf16 v[148:151], v[88:91], v[76:79], 0\n"
"buffer_load_dwordx4 v[44:47], v1, s[4:7], s98 offen offset:1024 sc1\n"
"v_cvt_pk_bf16_f32 v152, v60, v61\n"
"v_cvt_pk_bf16_f32 v153, v62, v63\n"
"v_cvt_pk_bf16_f32 v154, v124, v125\n"
"v_mfma_f32_16x16x32_bf16 v[156:159], v[72:75], v[76:79], 0\n"
"ds_read_b64_tr_b16 v[66:67], v255 offset:12544\n"
"v_cvt_pk_bf16_f32 v155, v126, v127\n"
"v_exp_f32_e32 v68, v68\n"
"v_exp_f32_e32 v69, v69\n"
"v_mfma_f32_16x16x32_bf16 v[124:127], v[84:87], v[76:79], 0\n"
"ds_read_b64_tr_b16 v[60:61], v255 offset:12288\n"
"v_exp_f32_e32 v70, v70\n"
"v_exp_f32_e32 v71, v71\n"
"v_mfma_f32_16x16x32_bf16 v[160:163], v[84:87], v[56:59], 0\n"
"buffer_load_dwordx4 v[48:51], v1, s[4:7], s98 offen offset:2048 sc1\n"
"v_exp_f32_e32 v80, v80\n"
"v_exp_f32_e32 v81, v81\n"
"v_mfma_f32_16x16x32_bf16 v[164:167], v[72:75], v[56:59], 0\n"
"ds_read_b64_tr_b16 v[62:63], v16 offset:12544\n"
"v_exp_f32_e32 v82, v82\n"
"v_exp_f32_e32 v83, v83\n"
"v_mfma_f32_16x16x32_bf16 v[168:171], v[88:91], v[56:59], 0\n"
"s_waitcnt lgkmcnt(4)\n"
"v_cvt_pk_bf16_f32 v72, v68, v69\n"
"v_cvt_pk_bf16_f32 v73, v70, v71\n"
"v_cvt_pk_bf16_f32 v74, v80, v81\n"
"v_cvt_pk_bf16_f32 v75, v82, v83\n"
"s_cmp_eq_i32 s78, 5\n"
"s_cbranch_scc1 .Lmk_x27s\n"
".Lmk_x27r:\n"
"v_mfma_f32_16x16x32_bf16 v[76:79], v[52:55], v[92:95], v[116:119]\n"
"ds_read_b64_tr_b16 v[68:69], v16 offset:10240\n"
"v_exp_f32_e32 v128, v128\n"
"v_exp_f32_e32 v129, v129\n"
"v_exp_f32_e32 v130, v130\n"
"v_mfma_f32_16x16x32_bf16 v[80:83], v[52:55], v[96:99], v[120:123]\n"
"buffer_load_dwordx4 v[52:55], v1, s[4:7], s98 offen offset:3072 sc1\n"
"v_exp_f32_e32 v131, v131\n"
"v_mfma_f32_16x16x32_bf16 v[84:87], v[140:143], v[96:99], v[120:123]\n"
"ds_read_b64_tr_b16 v[70:71], v255 offset:10496\n"
"v_exp_f32_e32 v144, v144\n"
"v_exp_f32_e32 v145, v145\n"
"v_mfma_f32_16x16x32_bf16 v[172:175], v[140:143], v[92:95], v[116:119]\n"
"s_waitcnt lgkmcnt(2)\n"
"v_exp_f32_e32 v146, v146\n"
"v_exp_f32_e32 v147, v147\n"
"v_cvt_pk_bf16_f32 v140, v128, v129\n"
"v_mfma_f32_16x16x32_bf16 v[148:151], v[88:91], v[152:155], v[148:151]\n"
"s_add_u32 s97, s69, 0x60a000\n"
"buffer_load_dwordx4 v[56:59], v0, s[4:7], s97 offen sc1\n"
"v_cvt_pk_bf16_f32 v141, v130, v131\n"
"v_cvt_pk_bf16_f32 v142, v144, v145\n"
"v_mfma_f32_16x16x32_bf16 v[156:159], v[64:67], v[152:155], v[156:159]\n"
"ds_read_b64_tr_b16 v[128:129], v255 offset:10240\n"
"v_cvt_pk_bf16_f32 v143, v146, v147\n"
"v_exp_f32_e32 v132, v132\n"
"v_exp_f32_e32 v133, v133\n"
"v_mfma_f32_16x16x32_bf16 v[124:127], v[60:63], v[152:155], v[124:127]\n"
"v_exp_f32_e32 v134, v134\n"
"v_exp_f32_e32 v135, v135\n"
"v_mfma_f32_16x16x32_bf16 v[160:163], v[60:63], v[72:75], v[160:163]\n"
"buffer_load_dwordx4 v[60:63], v0, s[4:7], s97 offen offset:1024 sc1\n"
"ds_read_b64_tr_b16 v[130:131], v16 offset:10496\n"
"v_exp_f32_e32 v136, v136\n"
"v_mfma_f32_16x16x32_bf16 v[164:167], v[64:67], v[72:75], v[164:167]\n"
"v_exp_f32_e32 v137, v137\n"
"v_exp_f32_e32 v138, v138\n"
"v_exp_f32_e32 v139, v139\n"
"v_mfma_f32_16x16x32_bf16 v[168:171], v[88:91], v[72:75], v[168:171]\n"
"s_waitcnt vmcnt(12)\n"
"v_cvt_pk_bf16_f32 v72, v132, v133\n"
"v_cvt_pk_bf16_f32 v73, v134, v135\n"
"v_cvt_pk_bf16_f32 v74, v136, v137\n"
"v_cvt_pk_bf16_f32 v75, v138, v139\n"
"s_cmp_lg_u32 s2, 0\n"
"s_cbranch_scc1 .Lmk_s28\n"
"s_barrier\n"
".Lmk_s28:\n"
"ds_read_b128 v[132:135], v15 offset:6144\n"
"ds_read_b128 v[136:139], v254 offset:6400\n"
"s_waitcnt lgkmcnt(2)\n"
"v_mfma_f32_16x16x32_bf16 v[148:151], v[88:91], v[140:143], v[148:151]\n"
"ds_read_b128 v[144:147], v15 offset:4096\n"
"v_exp_f32_e32 v76, v76\n"
"v_exp_f32_e32 v77, v77\n"
"v_exp_f32_e32 v78, v78\n"
"v_mfma_f32_16x16x32_bf16 v[156:159], v[68:71], v[140:143], v[156:159]\n"
"buffer_load_dwordx4 v[64:67], v0, s[4:7], s97 offen offset:2048 sc1\n"
"ds_read_b128 v[152:155], v254 offset:4352\n"
"v_exp_f32_e32 v79, v79\n"
"v_mfma_f32_16x16x32_bf16 v[124:127], v[128:131], v[140:143], v[124:127]\n"
"ds_read_b64_tr_b16 v[140:141], v16 offset:8192\n"
"v_exp_f32_e32 v172, v172\n"
"v_exp_f32_e32 v173, v173\n"
"v_mfma_f32_16x16x32_bf16 v[160:163], v[128:131], v[72:75], v[160:163]\n"
"ds_read_b64_tr_b16 v[142:143], v255 offset:8448\n"
"v_exp_f32_e32 v174, v174\n"
"v_exp_f32_e32 v175, v175\n"
"v_mfma_f32_16x16x32_bf16 v[164:167], v[68:71], v[72:75], v[164:167]\n"
"buffer_load_dwordx4 v[68:71], v0, s[4:7], s97 offen offset:3072 sc1\n"
"v_cvt_pk_bf16_f32 v128, v76, v77\n"
"v_cvt_pk_bf16_f32 v129, v78, v79\n"
"v_cvt_pk_bf16_f32 v130, v172, v173\n"
"v_cvt_pk_bf16_f32 v131, v174, v175\n"
"v_mfma_f32_16x16x32_bf16 v[168:171], v[88:91], v[72:75], v[168:171]\n"
"ds_read_b64_tr_b16 v[172:173], v255 offset:8192\n"
"v_exp_f32_e32 v80, v80\n"
"v_exp_f32_e32 v81, v81\n"
"s_waitcnt lgkmcnt(5)\n"
"v_mfma_f32_16x16x32_bf16 v[176:179], v[132:135], v[92:95], v[116:119]\n"
"ds_read_b64_tr_b16 v[174:175], v16 offset:8448\n"
"v_exp_f32_e32 v82, v82\n"
"v_exp_f32_e32 v83, v83\n"
"v_exp_f32_e32 v84, v84\n"
"v_mfma_f32_16x16x32_bf16 v[180:183], v[132:135], v[96:99], v[120:123]\n"
"buffer_load_dwordx4 v[72:75], v1, s[4:7], s97 offen sc1\n"
"s_waitcnt vmcnt(12)\n"
"v_cmp_ne_u64_e32 vcc, v[246:247], v[18:19]\n"
"v_mfma_f32_16x16x32_bf16 v[132:135], v[136:139], v[96:99], v[120:123]\n"
"v_exp_f32_e32 v85, v85\n"
"v_exp_f32_e32 v86, v86\n"
"v_exp_f32_e32 v87, v87\n"
"v_mfma_f32_16x16x32_bf16 v[184:187], v[136:139], v[92:95], v[116:119]\n"
"s_cbranch_vccnz .Lmk_f5s\n"
".Lmk_f5r:\n"
"v_cvt_pk_bf16_f32 v136, v80, v81\n"
"v_cvt_pk_bf16_f32 v137, v82, v83\n"
"v_cvt_pk_bf16_f32 v138, v84, v85\n"
"v_cvt_pk_bf16_f32 v139, v86, v87\n"
"s_waitcnt lgkmcnt(4)\n"
"v_mfma_f32_16x16x32_bf16 v[188:191], v[144:147], v[92:95], v[116:119]\n"
"ds_read_b128 v[192:195], v15 offset:2048\n"
"v_exp_f32_e32 v176, v176\n"
"v_exp_f32_e32 v177, v177\n"
"v_exp_f32_e32 v178, v178\n"
"v_mfma_f32_16x16x32_bf16 v[196:199], v[144:147], v[96:99], v[120:123]\n"
"buffer_load_dwordx4 v[76:79], v1, s[4:7], s97 offen offset:1024 sc1\n"
"v_exp_f32_e32 v179, v179\n"
"v_mfma_f32_16x16x32_bf16 v[144:147], v[152:155], v[96:99], v[120:123]\n"
"ds_read_b128 v[200:203], v254 offset:2304\n"
"v_exp_f32_e32 v184, v184\n"
"v_exp_f32_e32 v185, v185\n"
"v_mfma_f32_16x16x32_bf16 v[204:207], v[152:155], v[92:95], v[116:119]\n"
"ds_read_b64_tr_b16 v[152:153], v16 offset:6144\n"
"v_exp_f32_e32 v186, v186\n"
"v_exp_f32_e32 v187, v187\n"
"s_waitcnt lgkmcnt(3)\n"
"v_mfma_f32_16x16x32_bf16 v[148:151], v[88:91], v[128:131], v[148:151]\n"
"buffer_load_dwordx4 v[80:83], v1, s[4:7], s97 offen offset:2048 sc1\n"
"v_cvt_pk_bf16_f32 v208, v176, v177\n"
"v_cvt_pk_bf16_f32 v209, v178, v179\n"
"v_cvt_pk_bf16_f32 v210, v184, v185\n"
"v_mfma_f32_16x16x32_bf16 v[156:159], v[140:143], v[128:131], v[156:159]\n"
"ds_read_b64_tr_b16 v[154:155], v255 offset:6400\n"
"v_cvt_pk_bf16_f32 v211, v186, v187\n"
"v_exp_f32_e32 v180, v180\n"
"v_exp_f32_e32 v181, v181\n"
"v_mfma_f32_16x16x32_bf16 v[124:127], v[172:175], v[128:131], v[124:127]\n"
"ds_read_b64_tr_b16 v[128:129], v255 offset:6144\n"
"v_exp_f32_e32 v182, v182\n"
"v_exp_f32_e32 v183, v183\n"
"v_mfma_f32_16x16x32_bf16 v[160:163], v[172:175], v[136:139], v[160:163]\n"
"buffer_load_dwordx4 v[84:87], v1, s[4:7], s97 offen offset:3072 sc1\n"
"v_exp_f32_e32 v132, v132\n"
"v_exp_f32_e32 v133, v133\n"
"v_mfma_f32_16x16x32_bf16 v[164:167], v[140:143], v[136:139], v[164:167]\n"
"ds_read_b64_tr_b16 v[130:131], v16 offset:6400\n"
"v_exp_f32_e32 v134, v134\n"
"v_exp_f32_e32 v135, v135\n"
"v_mfma_f32_16x16x32_bf16 v[168:171], v[88:91], v[136:139], v[168:171]\n"
"s_waitcnt lgkmcnt(4)\n"
"v_cvt_pk_bf16_f32 v136, v180, v181\n"
"v_cvt_pk_bf16_f32 v137, v182, v183\n"
"v_cvt_pk_bf16_f32 v138, v132, v133\n"
"v_cvt_pk_bf16_f32 v139, v134, v135\n"
"v_mfma_f32_16x16x32_bf16 v[132:135], v[192:195], v[92:95], v[116:119]\n"
"ds_read_b128 v[140:143], v15\n"
"v_exp_f32_e32 v188, v188\n"
"v_exp_f32_e32 v189, v189\n"
"v_exp_f32_e32 v190, v190\n"
"v_mfma_f32_16x16x32_bf16 v[172:175], v[192:195], v[96:99], v[120:123]\n"
"s_add_u32 s97, s56, 0x40000\n"
"buffer_load_dwordx4 v[176:179], v0, s[4:7], s97 offen sc1\n"
"v_exp_f32_e32 v191, v191\n"
"v_mfma_f32_16x16x32_bf16 v[180:183], v[200:203], v[96:99], v[120:123]\n"
"ds_read_b128 v[184:187], v254 offset:256\n"
"v_exp_f32_e32 v204, v204\n"
"v_exp_f32_e32 v205, v205\n"
"v_exp_f32_e32 v206, v206\n"
"v_mfma_f32_16x16x32_bf16 v[192:195], v[200:203], v[92:95], v[116:119]\n"
"buffer_load_dwordx4 v[200:203], v0, s[4:7], s97 offen offset:1024 sc1\n"
"ds_read_b64_tr_b16 v[212:213], v16 offset:4096\n"
"v_exp_f32_e32 v207, v207\n"
"s_waitcnt lgkmcnt(3)\n"
"v_mfma_f32_16x16x32_bf16 v[148:151], v[88:91], v[208:211], v[148:151]\n"
"v_cvt_pk_bf16_f32 v216, v188, v189\n"
"v_cvt_pk_bf16_f32 v217, v190, v191\n"
"v_cvt_pk_bf16_f32 v218, v204, v205\n"
"v_cvt_pk_bf16_f32 v219, v206, v207\n"
"v_mfma_f32_16x16x32_bf16 v[156:159], v[152:155], v[208:211], v[156:159]\n"
"buffer_load_dwordx4 v[188:191], v0, s[4:7], s97 offen offset:2048 sc1\n"
"ds_read_b64_tr_b16 v[214:215], v255 offset:4352\n"
"v_exp_f32_e32 v196, v196\n"
"v_exp_f32_e32 v197, v197\n"
"v_mfma_f32_16x16x32_bf16 v[124:127], v[128:131], v[208:211], v[124:127]\n"
"buffer_load_dwordx4 v[204:207], v0, s[4:7], s97 offen offset:3072 sc1\n"
"v_exp_f32_e32 v198, v198\n"
"v_exp_f32_e32 v199, v199\n"
"v_mfma_f32_16x16x32_bf16 v[160:163], v[128:131], v[136:139], v[160:163]\n"
"ds_read_b64_tr_b16 v[128:129], v255 offset:4096\n"
"v_exp_f32_e32 v144, v144\n"
"v_exp_f32_e32 v145, v145\n"
"v_mfma_f32_16x16x32_bf16 v[164:167], v[152:155], v[136:139], v[164:167]\n"
"buffer_load_dwordx4 v[152:155], v1, s[4:7], s97 offen sc1\n"
"ds_read_b64_tr_b16 v[130:131], v16 offset:4352\n"
"v_exp_f32_e32 v146, v146\n"
"v_exp_f32_e32 v147, v147\n"
"v_mfma_f32_16x16x32_bf16 v[168:171], v[88:91], v[136:139], v[168:171]\n"
"s_waitcnt lgkmcnt(4)\n"
"v_cvt_pk_bf16_f32 v136, v196, v197\n"
"v_cvt_pk_bf16_f32 v137, v198, v199\n"
"v_cvt_pk_bf16_f32 v138, v144, v145\n"
"v_cvt_pk_bf16_f32 v139, v146, v147\n"
"v_mfma_f32_16x16x32_bf16 v[144:147], v[140:143], v[92:95], v[116:119]\n"
"ds_read_b64_tr_b16 v[196:197], v16 offset:2048\n"
"ds_read_b64_tr_b16 v[198:199], v255 offset:2304\n"
"buffer_load_dwordx4 v[208:211], v1, s[4:7], s97 offen offset:1024 sc1\n"
"v_exp_f32_e32 v132, v132\n"
"v_exp_f32_e32 v133, v133\n"
"v_mfma_f32_16x16x32_bf16 v[220:223], v[140:143], v[96:99], v[120:123]\n"
"ds_read_b64_tr_b16 v[140:141], v255 offset:2048\n"
"v_exp_f32_e32 v134, v134\n"
"v_exp_f32_e32 v135, v135\n"
"v_mfma_f32_16x16x32_bf16 v[224:227], v[184:187], v[96:99], v[120:123]\n"
"buffer_load_dwordx4 v[120:123], v1, s[4:7], s97 offen offset:2048 sc1\n"
"v_exp_f32_e32 v192, v192\n"
"v_exp_f32_e32 v193, v193\n"
"v_mfma_f32_16x16x32_bf16 v[228:231], v[184:187], v[92:95], v[116:119]\n"
"ds_read_b64_tr_b16 v[142:143], v16 offset:2304\n"
"ds_read_b64_tr_b16 v[116:117], v16 offset:16384\n"
"v_exp_f32_e32 v194, v194\n"
"v_exp_f32_e32 v195, v195\n"
"s_waitcnt lgkmcnt(5)\n"
"v_mfma_f32_16x16x32_bf16 v[148:151], v[88:91], v[216:219], v[148:151]\n"
"v_cvt_pk_bf16_f32 v184, v132, v133\n"
"v_cvt_pk_bf16_f32 v185, v134, v135\n"
"v_cvt_pk_bf16_f32 v186, v192, v193\n"
"v_mfma_f32_16x16x32_bf16 v[156:159], v[212:215], v[216:219], v[156:159]\n"
"buffer_load_dwordx4 v[132:135], v1, s[4:7], s97 offen offset:3072 sc1\n"
"ds_read_b64_tr_b16 v[118:119], v255 offset:16640\n"
"ds_read_b64_tr_b16 v[192:193], v255 offset:16384\n"
"v_cvt_pk_bf16_f32 v187, v194, v195\n"
"v_exp_f32_e32 v172, v172\n"
"v_exp_f32_e32 v173, v173\n"
"v_exp_f32_e32 v174, v174\n"
"v_mfma_f32_16x16x32_bf16 v[124:127], v[128:131], v[216:219], v[124:127]\n"
"s_add_u32 s97, s57, 0x40000\n"
"buffer_load_dwordx4 v[216:219], v0, s[4:7], s97 offen sc1\n"
"ds_read_b64_tr_b16 v[194:195], v16 offset:16640\n"
"v_exp_f32_e32 v175, v175\n"
"v_mfma_f32_16x16x32_bf16 v[160:163], v[128:131], v[136:139], v[160:163]\n"
"v_exp_f32_e32 v180, v180\n"
"v_add_u32_e32 v248, 0x26400, v0\n"
"v_exp_f32_e32 v181, v181\n"
"v_mfma_f32_16x16x32_bf16 v[164:167], v[212:215], v[136:139], v[164:167]\n"
"buffer_load_dwordx4 v[128:131], v0, s[4:7], s97 offen offset:1024 sc1\n"
"ds_read_b128 v[212:215], v248\n"
"v_exp_f32_e32 v182, v182\n"
"v_exp_f32_e32 v183, v183\n"
"v_mfma_f32_16x16x32_bf16 v[168:171], v[88:91], v[136:139], v[168:171]\n"
"ds_read_b128 v[232:235], v248 offset:1024\n"
"v_cvt_pk_bf16_f32 v136, v172, v173\n"
"v_cvt_pk_bf16_f32 v137, v174, v175\n"
"v_cvt_pk_bf16_f32 v138, v180, v181\n"
"v_cvt_pk_bf16_f32 v139, v182, v183\n"
"s_waitcnt lgkmcnt(6)\n"
"v_mfma_f32_16x16x32_bf16 v[148:151], v[88:91], v[184:187], v[148:151]\n"
"ds_read_b64_tr_b16 v[172:173], v16\n"
"buffer_load_dwordx4 v[180:183], v0, s[4:7], s97 offen offset:2048 sc1\n"
"v_cndmask_b32_e64 v144, v100, v144, s[20:21]\n"
"v_cndmask_b32_e64 v145, v101, v145, s[22:23]\n"
"v_cndmask_b32_e64 v146, v102, v146, s[24:25]\n"
"v_cndmask_b32_e64 v147, v103, v147, s[26:27]\n"
"v_cndmask_b32_e64 v228, v112, v228, s[28:29]\n"
"v_cndmask_b32_e64 v229, v113, v229, s[30:31]\n"
"v_cndmask_b32_e64 v230, v114, v230, s[32:33]\n"
"v_cndmask_b32_e64 v231, v115, v231, s[34:35]\n"
"v_exp_f32_e32 v144, v144\n"
"v_mfma_f32_16x16x32_bf16 v[156:159], v[196:199], v[184:187], v[156:159]\n"
"ds_read_b64_tr_b16 v[174:175], v255 offset:256\n"
"buffer_load_dwordx4 v[100:103], v0, s[4:7], s97 offen offset:3072 sc1\n"
"v_exp_f32_e32 v145, v145\n"
"v_exp_f32_e32 v146, v146\n"
"v_exp_f32_e32 v147, v147\n"
"v_exp_f32_e32 v228, v228\n"
"v_mfma_f32_16x16x32_bf16 v[124:127], v[140:143], v[184:187], v[124:127]\n"
"buffer_load_dwordx4 v[184:187], v1, s[4:7], s97 offen sc1\n"
"v_exp_f32_e32 v229, v229\n"
"v_exp_f32_e32 v230, v230\n"
"v_exp_f32_e32 v231, v231\n"
"v_cvt_pk_bf16_f32 v112, v144, v145\n"
"v_cvt_pk_bf16_f32 v113, v146, v147\n"
"v_cvt_pk_bf16_f32 v114, v228, v229\n"
"v_mfma_f32_16x16x32_bf16 v[160:163], v[140:143], v[136:139], v[160:163]\n"
"ds_read_b64_tr_b16 v[140:141], v255\n"
"v_cvt_pk_bf16_f32 v115, v230, v231\n"
"v_cndmask_b32_e64 v220, v104, v220, s[36:37]\n"
"v_cndmask_b32_e64 v221, v105, v221, s[38:39]\n"
"v_cndmask_b32_e64 v222, v106, v222, s[40:41]\n"
"v_cndmask_b32_e64 v223, v107, v223, s[42:43]\n"
"v_cndmask_b32_e64 v224, v108, v224, s[44:45]\n"
"v_cndmask_b32_e64 v225, v109, v225, s[46:47]\n"
"v_cndmask_b32_e64 v226, v110, v226, s[48:49]\n"
"v_cndmask_b32_e64 v227, v111, v227, s[50:51]\n"
"v_mfma_f32_16x16x32_bf16 v[164:167], v[196:199], v[136:139], v[164:167]\n"
"buffer_load_dwordx4 v[104:107], v1, s[4:7], s97 offen offset:1024 sc1\n"
"ds_read_b64_tr_b16 v[142:143], v16 offset:256\n"
"buffer_load_dwordx4 v[108:111], v1, s[4:7], s97 offen offset:2048 sc1\n"
"v_exp_f32_e32 v220, v220\n"
"v_exp_f32_e32 v221, v221\n"
"v_exp_f32_e32 v222, v222\n"
"v_exp_f32_e32 v223, v223\n"
"v_exp_f32_e32 v224, v224\n"
"v_mfma_f32_16x16x32_bf16 v[168:171], v[88:91], v[136:139], v[168:171]\n"
"s_waitcnt lgkmcnt(0)\n"
"v_exp_f32_e32 v225, v225\n"
"v_exp_f32_e32 v226, v226\n"
"v_exp_f32_e32 v227, v227\n"
"v_cvt_pk_bf16_f32 v136, v220, v221\n"
"v_cvt_pk_bf16_f32 v137, v222, v223\n"
"v_cvt_pk_bf16_f32 v138, v224, v225\n"
"v_cvt_pk_bf16_f32 v139, v226, v227\n"
"v_mfma_f32_16x16x32_bf16 v[148:151], v[88:91], v[112:115], v[148:151]\n"
"v_and_b32_e32 v212, v112, v212\n"
"v_and_b32_e32 v213, v113, v213\n"
"v_and_b32_e32 v214, v114, v214\n"
"v_and_b32_e32 v215, v115, v215\n"
"v_mfma_f32_16x16x32_bf16 v[168:171], v[88:91], v[136:139], v[168:171]\n"
"v_xor_b32_e32 v144, v112, v212\n"
"v_xor_b32_e32 v145, v113, v213\n"
"v_xor_b32_e32 v146, v114, v214\n"
"v_xor_b32_e32 v147, v115, v215\n"
"v_mfma_f32_16x16x32_bf16 v[156:159], v[172:175], v[212:215], v[156:159]\n"
"v_and_b32_e32 v232, v136, v232\n"
"v_and_b32_e32 v233, v137, v233\n"
"v_mfma_f32_16x16x32_bf16 v[124:127], v[140:143], v[212:215], v[124:127]\n"
"v_and_b32_e32 v234, v138, v234\n"
"v_and_b32_e32 v235, v139, v235\n"
"v_mfma_f32_16x16x32_bf16 v[156:159], v[116:119], v[144:147], v[156:159]\n"
"v_xor_b32_e32 v112, v136, v232\n"
"v_xor_b32_e32 v113, v137, v233\n"
"v_mfma_f32_16x16x32_bf16 v[124:127], v[192:195], v[144:147], v[124:127]\n"
"v_xor_b32_e32 v114, v138, v234\n"
"v_xor_b32_e32 v115, v139, v235\n"
"v_mfma_f32_16x16x32_bf16 v[164:167], v[172:175], v[232:235], v[164:167]\n"
"buffer_load_dwordx4 v[136:139], v1, s[4:7], s97 offen offset:3072 sc1\n"
"s_add_u32 s97, s58, 0x40000\n"
"buffer_load_dwordx4 v[144:147], v0, s[4:7], s97 offen sc1\n"
"v_mfma_f32_16x16x32_bf16 v[160:163], v[140:143], v[232:235], v[160:163]\n"
"buffer_load_dwordx4 v[140:143], v0, s[4:7], s97 offen offset:1024 sc1\n"
"v_mfma_f32_16x16x32_bf16 v[164:167], v[116:119], v[112:115], v[164:167]\n"
"buffer_load_dwordx4 v[116:119], v0, s[4:7], s97 offen offset:2048 sc1\n"
"v_mfma_f32_16x16x32_bf16 v[160:163], v[192:195], v[112:115], v[160:163]\n"
"buffer_load_dwordx4 v[112:115], v0, s[4:7], s97 offen offset:3072 sc1\n"
"v_max_f32_e32 v248, v148, v168\n"
"v_rcp_f32_e32 v148, v148\n"
"v_rcp_f32_e32 v168, v168\n"
"s_nop 0\n"
"v_max3_f32 v248, v248, v148, v168\n"
"v_cmp_nlt_f32_e32 vcc, v248, v252\n"
"s_cbranch_vccnz .Lmk_b24\n"
".Lmk_k29:\n"
"v_mul_f32_e32 v156, v156, v148\n"
"v_mul_f32_e32 v157, v157, v148\n"
"v_mul_f32_e32 v158, v158, v148\n"
"v_mul_f32_e32 v159, v159, v148\n"
"v_mul_f32_e32 v124, v124, v148\n"
"v_mul_f32_e32 v125, v125, v148\n"
"v_mul_f32_e32 v126, v126, v148\n"
"v_mul_f32_e32 v127, v127, v148\n"
"v_cvt_pk_bf16_f32 v88, v156, v157\n"
"v_cvt_pk_bf16_f32 v89, v158, v159\n"
"v_cvt_pk_bf16_f32 v90, v124, v125\n"
"v_cvt_pk_bf16_f32 v91, v126, v127\n"
"buffer_store_dwordx4 v[88:91], v0, s[4:7], s60 offen sc1\n"
"s_add_u32 s98, s60, 0x8000\n"
"v_mul_f32_e32 v164, v164, v168\n"
"v_mul_f32_e32 v165, v165, v168\n"
"v_mul_f32_e32 v166, v166, v168\n"
"v_mul_f32_e32 v167, v167, v168\n"
"v_mul_f32_e32 v160, v160, v168\n"
"v_mul_f32_e32 v161, v161, v168\n"
"v_mul_f32_e32 v162, v162, v168\n"
"v_mul_f32_e32 v163, v163, v168\n"
"v_cvt_pk_bf16_f32 v88, v164, v165\n"
"v_cvt_pk_bf16_f32 v89, v166, v167\n"
"v_cvt_pk_bf16_f32 v90, v160, v161\n"
"v_cvt_pk_bf16_f32 v91, v162, v163\n"
"buffer_store_dwordx4 v[88:91], v0, s[4:7], s98 offen sc1\n"
";\n"
"s_waitcnt vmcnt(20)\n"
"; @A G2 b.0\n"
"s_nop 0\n"
"v_mfma_f32_16x16x32_bf16 v[88:91], a[128:131], v[176:179], 0\n"
"v_mfma_f32_16x16x32_bf16 v[88:91], a[132:135], v[200:203], v[88:91]\n"
"s_waitcnt vmcnt(18)\n"
"v_mfma_f32_16x16x32_bf16 v[88:91], a[136:139], v[188:191], v[88:91]\n"
"buffer_load_dwordx4 v[92:95], v1, s[4:7], s97 offen sc1\n"
"v_mfma_f32_16x16x32_bf16 v[88:91], a[140:143], v[204:207], v[88:91]\n"
"s_waitcnt vmcnt(17)\n"
"v_mfma_f32_16x16x32_bf16 v[88:91], a[144:147], v[152:155], v[88:91]\n"
"v_mfma_f32_16x16x32_bf16 v[88:91], a[148:151], v[208:211], v[88:91]\n"
"s_waitcnt vmcnt(15)\n"
"v_mfma_f32_16x16x32_bf16 v[88:91], a[152:155], v[120:123], v[88:91]\n"
"buffer_load_dwordx4 v[96:99], v1, s[4:7], s97 offen offset:1024 sc1\n"
"v_mfma_f32_16x16x32_bf16 v[88:91], a[156:159], v[132:135], v[88:91]\n"
"v_mfma_f32_16x16x32_bf16 v[124:127], a[188:191], v[132:135], 0\n"
"v_mfma_f32_16x16x32_bf16 v[124:127], a[184:187], v[120:123], v[124:127]\n"
"v_mfma_f32_16x16x32_bf16 v[124:127], a[180:183], v[208:211], v[124:127]\n"
"buffer_load_dwordx4 v[148:151], v1, s[4:7], s97 offen offset:2048 sc1\n"
"v_mfma_f32_16x16x32_bf16 v[124:127], a[176:179], v[152:155], v[124:127]\n"
"v_mfma_f32_16x16x32_bf16 v[124:127], a[172:175], v[204:207], v[124:127]\n"
"s_nop 1\n"
"v_cmp_u_f32_e32 vcc, v88, v88\n"
"v_mfma_f32_16x16x32_bf16 v[124:127], a[168:171], v[188:191], v[124:127]\n"
"s_cbranch_vccnz .Lmk_p30s\n"
".Lmk_p30r:\n"
"v_mfma_f32_16x16x32_bf16 v[124:127], a[164:167], v[200:203], v[124:127]\n"
"ds_write_b128 v3, v[88:91]\n"
"v_mfma_f32_16x16x32_bf16 v[124:127], a[160:163], v[176:179], v[124:127]\n"
"buffer_load_dwordx4 v[88:91], v1, s[4:7], s97 offen offset:3072 sc1\n"
"v_mfma_f32_16x16x32_bf16 v[156:159], a[192:195], v[176:179], 0\n"
"v_mfma_f32_16x16x32_bf16 v[156:159], a[196:199], v[200:203], v[156:159]\n"
"s_add_u32 s98, s59, 0x40000\n"
"v_mfma_f32_16x16x32_bf16 v[156:159], a[200:203], v[188:191], v[156:159]\n"
"v_mfma_f32_16x16x32_bf16 v[156:159], a[204:207], v[204:207], v[156:159]\n"
"buffer_load_dwordx4 v[160:163], v0, s[4:7], s98 offen sc1\n"
"v_mfma_f32_16x16x32_bf16 v[156:159], a[208:211], v[152:155], v[156:159]\n"
"v_mfma_f32_16x16x32_bf16 v[156:159], a[212:215], v[208:211], v[156:159]\n"
"ds_write_b128 v3, v[124:127] offset:1024\n"
"v_mfma_f32_16x16x32_bf16 v[156:159], a[216:219], v[120:123], v[156:159]\n"
"v_mfma_f32_16x16x32_bf16 v[156:159], a[220:223], v[132:135], v[156:159]\n"
"buffer_load_dwordx4 v[124:127], v0, s[4:7], s98 offen offset:1024 sc1\n"
"v_mfma_f32_16x16x32_bf16 v[164:167], a[252:255], v[132:135], 0\n"
"v_mfma_f32_16x16x32_bf16 v[164:167], a[248:251], v[120:123], v[164:167]\n"
"v_mfma_f32_16x16x32_bf16 v[164:167], a[244:247], v[208:211], v[164:167]\n"
"v_mfma_f32_16x16x32_bf16 v[164:167], a[240:243], v[152:155], v[164:167]\n"
"buffer_load_dwordx4 v[120:123], v0, s[4:7], s98 offen offset:2048 sc1\n"
"v_mfma_f32_16x16x32_bf16 v[164:167], a[236:239], v[204:207], v[164:167]\n"
"v_mfma_f32_16x16x32_bf16 v[164:167], a[232:235], v[188:191], v[164:167]\n"
"ds_write_b128 v3, v[156:159] offset:2048\n"
"v_mfma_f32_16x16x32_bf16 v[164:167], a[228:231], v[200:203], v[164:167]\n"
"v_mfma_f32_16x16x32_bf16 v[164:167], a[224:227], v[176:179], v[164:167]\n"
"s_waitcnt vmcnt(17)\n"
"buffer_load_dwordx4 v[132:135], v0, s[4:7], s98 offen offset:3072 sc1\n"
"; @A G2 b.1\n"
"v_mfma_f32_16x16x32_bf16 v[152:155], a[128:131], v[216:219], 0\n"
"v_mfma_f32_16x16x32_bf16 v[152:155], a[132:135], v[128:131], v[152:155]\n"
"v_mfma_f32_16x16x32_bf16 v[152:155], a[136:139], v[180:183], v[152:155]\n"
"v_mfma_f32_16x16x32_bf16 v[152:155], a[140:143], v[100:103], v[152:155]\n"
"s_waitcnt vmcnt(14)\n"
"buffer_load_dwordx4 v[156:159], v1, s[4:7], s98 offen sc1\n"
"v_mfma_f32_16x16x32_bf16 v[152:155], a[144:147], v[184:187], v[152:155]\n"
"v_mfma_f32_16x16x32_bf16 v[152:155], a[148:151], v[104:107], v[152:155]\n"
"ds_write_b128 v3, v[164:167] offset:3072\n"
"v_mfma_f32_16x16x32_bf16 v[152:155], a[152:155], v[108:111], v[152:155]\n"
"v_mfma_f32_16x16x32_bf16 v[152:155], a[156:159], v[136:139], v[152:155]\n"
"buffer_load_dwordx4 v[164:167], v1, s[4:7], s98 offen offset:1024 sc1\n"
"v_mfma_f32_16x16x32_bf16 v[168:171], a[188:191], v[136:139], 0\n"
"v_mfma_f32_16x16x32_bf16 v[168:171], a[184:187], v[108:111], v[168:171]\n"
"v_mfma_f32_16x16x32_bf16 v[168:171], a[180:183], v[104:107], v[168:171]\n"
"v_mfma_f32_16x16x32_bf16 v[168:171], a[176:179], v[184:187], v[168:171]\n"
"buffer_load_dwordx4 v[172:175], v1, s[4:7], s98 offen offset:2048 sc1\n"
"v_mfma_f32_16x16x32_bf16 v[168:171], a[172:175], v[100:103], v[168:171]\n"
"s_nop 0\n"
"v_cmp_u_f32_e32 vcc, v152, v152\n"
"v_mfma_f32_16x16x32_bf16 v[168:171], a[168:171], v[180:183], v[168:171]\n"
"s_cbranch_vccnz .Lmk_p31s\n"
".Lmk_p31r:\n"
"v_mfma_f32_16x16x32_bf16 v[168:171], a[164:167], v[128:131], v[168:171]\n"
"ds_write_b128 v3, v[152:155] offset:6144\n"
"v_mfma_f32_16x16x32_bf16 v[168:171], a[160:163], v[216:219], v[168:171]\n"
"buffer_load_dwordx4 v[152:155], v1, s[4:7], s98 offen offset:3072 sc1\n"
"v_mfma_f32_16x16x32_bf16 v[176:179], a[192:195], v[216:219], 0\n"
"v_mfma_f32_16x16x32_bf16 v[176:179], a[196:199], v[128:131], v[176:179]\n"
"v_mfma_f32_16x16x32_bf16 v[176:179], a[200:203], v[180:183], v[176:179]\n"
"v_mfma_f32_16x16x32_bf16 v[176:179], a[204:207], v[100:103], v[176:179]\n"
"v_mfma_f32_16x16x32_bf16 v[176:179], a[208:211], v[184:187], v[176:179]\n"
"ds_read_b128 v[188:191], v2 offset:8192\n"
"ds_read_b128 v[192:195], v2 offset:9216\n"
"v_mfma_f32_16x16x32_bf16 v[176:179], a[212:215], v[104:107], v[176:179]\n"
"ds_write_b128 v3, v[168:171] offset:7168\n"
"v_mfma_f32_16x16x32_bf16 v[176:179], a[216:219], v[108:111], v[176:179]\n"
"v_mfma_f32_16x16x32_bf16 v[176:179], a[220:223], v[136:139], v[176:179]\n"
"v_mfma_f32_16x16x32_bf16 v[168:171], a[252:255], v[136:139], 0\n"
"s_mov_b32 s98, .Lmk_b0-.Lmk_g34\n"
"s_getpc_b64 s[84:85]\n"
".Lmk_g34:\n"
"s_add_u32 s84, s84, s98\n"
"s_addc_u32 s85, s85, 0\n"
"s_and_b32 s84, s84, -16\n"
"v_mfma_f32_16x16x32_bf16 v[168:171], a[248:251], v[108:111], v[168:171]\n"
"s_and_b32 s85, s85, 0xffff\n"
"s_sub_u32 s98, .Lmk_end-.Lmk_g34, s98\n"
"s_min_u32 s86, s98, 0x3000\n"
"s_mov_b32 s87, 0x20000\n"
"v_mfma_f32_16x16x32_bf16 v[168:171], a[244:247], v[104:107], v[168:171]\n"
"s_mov_b32 m0, 0x27800\n"
"s_nop 0\n"
"buffer_load_dwordx4 v0, s[84:87], s99 offen lds\n"
"v_mfma_f32_16x16x32_bf16 v[168:171], a[240:243], v[184:187], v[168:171]\n"
"s_waitcnt lgkmcnt(1)\n"
"v_lshlrev_b32_e32 v255, 16, v188\n"
"v_and_b32_e32 v254, 0xffff0000, v188\n"
"v_mfma_f32_16x16x32_bf16 v[168:171], a[236:239], v[100:103], v[168:171]\n"
"v_lshlrev_b32_e32 v253, 16, v189\n"
"v_and_b32_e32 v252, 0xffff0000, v189\n"
"v_mfma_f32_16x16x32_bf16 v[168:171], a[232:235], v[180:183], v[168:171]\n"
"ds_write_b128 v3, v[176:179] offset:8192\n"
"v_mfma_f32_16x16x32_bf16 v[168:171], a[228:231], v[128:131], v[168:171]\n"
"v_lshlrev_b32_e32 v251, 16, v190\n"
"v_and_b32_e32 v250, 0xffff0000, v190\n"
"v_mfma_f32_16x16x32_bf16 v[168:171], a[224:227], v[216:219], v[168:171]\n"
"s_waitcnt vmcnt(15)\n"
"v_lshlrev_b32_e32 v249, 16, v191\n"
"v_and_b32_e32 v248, 0xffff0000, v191\n"
"; @A G2 b.2\n"
"v_mfma_f32_16x16x32_bf16 v[100:103], a[128:131], v[144:147], 0\n"
"v_lshlrev_b32_e32 v247, 16, v192\n"
"v_and_b32_e32 v246, 0xffff0000, v192\n"
"v_mfma_f32_16x16x32_bf16 v[100:103], a[132:135], v[140:143], v[100:103]\n"
"v_lshlrev_b32_e32 v245, 16, v193\n"
"v_and_b32_e32 v244, 0xffff0000, v193\n"
"v_mfma_f32_16x16x32_bf16 v[100:103], a[136:139], v[116:119], v[100:103]\n"
"v_lshlrev_b32_e32 v243, 16, v194\n"
"v_and_b32_e32 v242, 0xffff0000, v194\n"
"v_mfma_f32_16x16x32_bf16 v[100:103], a[140:143], v[112:115], v[100:103]\n"
"s_waitcnt vmcnt(9)\n"
"v_lshlrev_b32_e32 v241, 16, v195\n"
"v_and_b32_e32 v240, 0xffff0000, v195\n"
"v_mfma_f32_16x16x32_bf16 v[100:103], a[144:147], v[92:95], v[100:103]\n"
"v_mfma_f32_16x16x32_bf16 v[100:103], a[148:151], v[96:99], v[100:103]\n"
"ds_write_b128 v3, v[168:171] offset:9216\n"
"v_mfma_f32_16x16x32_bf16 v[100:103], a[152:155], v[148:151], v[100:103]\n"
"v_mfma_f32_16x16x32_bf16 v[100:103], a[156:159], v[88:91], v[100:103]\n"
"v_mfma_f32_16x16x32_bf16 v[104:107], a[188:191], v[88:91], 0\n"
"v_mfma_f32_16x16x32_bf16 v[104:107], a[184:187], v[148:151], v[104:107]\n"
"s_waitcnt lgkmcnt(0)\n"
"s_barrier\n"
"v_mfma_f32_16x16x32_bf16 v[104:107], a[180:183], v[96:99], v[104:107]\n"
"ds_read_b128 v[108:111], v4\n"
"ds_read_b128 v[128:131], v5\n"
"v_mfma_f32_16x16x32_bf16 v[104:107], a[176:179], v[92:95], v[104:107]\n"
"v_mfma_f32_16x16x32_bf16 v[104:107], a[172:175], v[112:115], v[104:107]\n"
"s_add_u32 s98, s56, 0\n"
"v_cmp_u_f32_e32 vcc, v100, v100\n"
"v_mfma_f32_16x16x32_bf16 v[104:107], a[168:171], v[116:119], v[104:107]\n"
"s_cbranch_vccnz .Lmk_p32s\n"
".Lmk_p32r:\n"
"v_mfma_f32_16x16x32_bf16 v[104:107], a[164:167], v[140:143], v[104:107]\n"
"ds_write_b128 v3, v[100:103] offset:12288\n"
"v_mfma_f32_16x16x32_bf16 v[104:107], a[160:163], v[144:147], v[104:107]\n"
"buffer_load_dwordx4 v[100:103], v0, s[4:7], s98 offen sc1\n"
"v_mfma_f32_16x16x32_bf16 v[136:139], a[192:195], v[144:147], 0\n"
"ds_read_b128 v[168:171], v4 offset:1024\n"
"ds_read_b128 v[176:179], v5 offset:1024\n"
"v_mfma_f32_16x16x32_bf16 v[136:139], a[196:199], v[140:143], v[136:139]\n"
"s_waitcnt lgkmcnt(3)\n"
"v_add_f32_e32 v108, v108, v255\n"
"v_add_f32_e32 v109, v109, v254\n"
"v_mfma_f32_16x16x32_bf16 v[136:139], a[200:203], v[116:119], v[136:139]\n"
"buffer_load_dwordx4 v[180:183], v0, s[4:7], s98 offen offset:1024 sc1\n"
"v_mfma_f32_16x16x32_bf16 v[136:139], a[204:207], v[112:115], v[136:139]\n"
"v_add_f32_e32 v110, v110, v253\n"
"v_add_f32_e32 v111, v111, v252\n"
"v_mfma_f32_16x16x32_bf16 v[136:139], a[208:211], v[92:95], v[136:139]\n"
"v_add_f32_e32 v108, v108, v128\n"
"v_add_f32_e32 v109, v109, v129\n"
"v_mfma_f32_16x16x32_bf16 v[136:139], a[212:215], v[96:99], v[136:139]\n"
"ds_write_b128 v3, v[104:107] offset:13312\n"
"v_mfma_f32_16x16x32_bf16 v[136:139], a[216:219], v[148:151], v[136:139]\n"
"buffer_load_dwordx4 v[104:107], v0, s[4:7], s98 offen offset:2048 sc1\n"
"v_mfma_f32_16x16x32_bf16 v[136:139], a[220:223], v[88:91], v[136:139]\n"
"ds_read_b128 v[184:187], v4 offset:2048\n"
"ds_read_b128 v[188:191], v5 offset:2048\n"
"v_mfma_f32_16x16x32_bf16 v[192:195], a[252:255], v[88:91], 0\n"
"s_waitcnt lgkmcnt(3)\n"
"v_add_f32_e32 v110, v110, v130\n"
"v_add_f32_e32 v111, v111, v131\n"
"v_mfma_f32_16x16x32_bf16 v[192:195], a[248:251], v[148:151], v[192:195]\n"
"buffer_load_dwordx4 v[88:91], v0, s[4:7], s98 offen offset:3072 sc1\n"
"v_mfma_f32_16x16x32_bf16 v[192:195], a[244:247], v[96:99], v[192:195]\n"
"v_add_f32_e32 v168, v168, v251\n"
"v_add_f32_e32 v169, v169, v250\n"
"v_mfma_f32_16x16x32_bf16 v[192:195], a[240:243], v[92:95], v[192:195]\n"
"v_add_f32_e32 v170, v170, v249\n"
"v_add_f32_e32 v171, v171, v248\n"
"v_mfma_f32_16x16x32_bf16 v[192:195], a[236:239], v[112:115], v[192:195]\n"
"buffer_load_dwordx4 v[92:95], v1, s[4:7], s98 offen sc1\n"
"v_mfma_f32_16x16x32_bf16 v[192:195], a[232:235], v[116:119], v[192:195]\n"
"ds_write_b128 v3, v[136:139] offset:14336\n"
"v_mfma_f32_16x16x32_bf16 v[192:195], a[228:231], v[140:143], v[192:195]\n"
"ds_read_b128 v[96:99], v4 offset:3072\n"
"ds_read_b128 v[112:115], v5 offset:3072\n"
"v_mfma_f32_16x16x32_bf16 v[192:195], a[224:227], v[144:147], v[192:195]\n"
"s_waitcnt vmcnt(10)\n"
"buffer_load_dwordx4 v[116:119], v1, s[4:7], s98 offen offset:1024 sc1\n"
"; @A G2 b.3\n"
"v_mfma_f32_16x16x32_bf16 v[128:131], a[128:131], v[160:163], v[108:111]\n"
"v_add_f32_e32 v168, v168, v176\n"
"v_add_f32_e32 v169, v169, v177\n"
"v_mfma_f32_16x16x32_bf16 v[128:131], a[132:135], v[124:127], v[128:131]\n"
"s_waitcnt lgkmcnt(3)\n"
"v_add_f32_e32 v170, v170, v178\n"
"v_add_f32_e32 v171, v171, v179\n"
"v_mfma_f32_16x16x32_bf16 v[128:131], a[136:139], v[120:123], v[128:131]\n"
"buffer_load_dwordx4 v[136:139], v1, s[4:7], s98 offen offset:2048 sc1\n"
"v_mfma_f32_16x16x32_bf16 v[128:131], a[140:143], v[132:135], v[128:131]\n"
"s_waitcnt vmcnt(8)\n"
"v_add_f32_e32 v184, v184, v247\n"
"v_add_f32_e32 v185, v185, v246\n"
"v_mfma_f32_16x16x32_bf16 v[128:131], a[144:147], v[156:159], v[128:131]\n"
"v_add_f32_e32 v186, v186, v245\n"
"v_add_f32_e32 v187, v187, v244\n"
"v_mfma_f32_16x16x32_bf16 v[128:131], a[148:151], v[164:167], v[128:131]\n"
"ds_write_b128 v3, v[192:195] offset:15360\n"
"v_mfma_f32_16x16x32_bf16 v[128:131], a[152:155], v[172:175], v[128:131]\n"
"buffer_load_dwordx4 v[140:143], v1, s[4:7], s98 offen offset:3072 sc1\n"
"v_mfma_f32_16x16x32_bf16 v[128:131], a[156:159], v[152:155], v[128:131]\n"
"s_add_u32 s98, s57, 0\n"
"v_add_f32_e32 v184, v184, v188\n"
"v_add_f32_e32 v185, v185, v189\n"
"v_mfma_f32_16x16x32_bf16 v[144:147], a[188:191], v[152:155], v[168:171]\n"
"s_waitcnt lgkmcnt(1)\n"
"v_add_f32_e32 v186, v186, v190\n"
"v_add_f32_e32 v187, v187, v191\n"
"v_mfma_f32_16x16x32_bf16 v[144:147], a[184:187], v[172:175], v[144:147]\n"
"s_waitcnt lgkmcnt(0)\n"
"s_barrier\n"
"v_mfma_f32_16x16x32_bf16 v[144:147], a[180:183], v[164:167], v[144:147]\n"
"buffer_load_dwordx4 v[148:151], v0, s[4:7], s98 offen sc1\n"
"v_mfma_f32_16x16x32_bf16 v[144:147], a[176:179], v[156:159], v[144:147]\n"
"v_add_f32_e32 v96, v96, v243\n"
"v_add_f32_e32 v97, v97, v242\n"
"v_mfma_f32_16x16x32_bf16 v[144:147], a[172:175], v[132:135], v[144:147]\n"
"v_cmp_u_f32_e32 vcc, v128, v128\n"
"v_add_f32_e32 v98, v98, v241\n"
"v_mfma_f32_16x16x32_bf16 v[144:147], a[168:171], v[120:123], v[144:147]\n"
"s_cbranch_vccnz .Lmk_p33s\n"
".Lmk_p33r:\n"
"v_add_f32_e32 v99, v99, v240\n"
"v_mfma_f32_16x16x32_bf16 v[144:147], a[164:167], v[124:127], v[144:147]\n"
"buffer_load_dwordx4 v[108:111], v0, s[4:7], s98 offen offset:1024 sc1\n"
"v_mfma_f32_16x16x32_bf16 v[144:147], a[160:163], v[160:163], v[144:147]\n"
"v_add_f32_e32 v96, v96, v112\n"
"v_add_f32_e32 v97, v97, v113\n"
"v_mfma_f32_16x16x32_bf16 v[184:187], a[192:195], v[160:163], v[184:187]\n"
"v_add_f32_e32 v98, v98, v114\n"
"v_add_f32_e32 v99, v99, v115\n"
"v_mfma_f32_16x16x32_bf16 v[184:187], a[196:199], v[124:127], v[184:187]\n"
"buffer_load_dwordx4 v[112:115], v0, s[4:7], s98 offen offset:2048 sc1\n"
"v_mfma_f32_16x16x32_bf16 v[184:187], a[200:203], v[120:123], v[184:187]\n"
"ds_read_b128 v[168:171], v6\n"
"ds_read_b128 v[176:179], v6 offset:1024\n"
"v_mfma_f32_16x16x32_bf16 v[184:187], a[204:207], v[132:135], v[184:187]\n"
"ds_read_b128 v[188:191], v6 offset:2048\n"
"ds_read_b128 v[192:195], v6 offset:3072\n"
"v_mfma_f32_16x16x32_bf16 v[184:187], a[208:211], v[156:159], v[184:187]\n"
"buffer_load_dwordx4 v[196:199], v0, s[4:7], s98 offen offset:3072 sc1\n"
"v_mfma_f32_16x16x32_bf16 v[184:187], a[212:215], v[164:167], v[184:187]\n"
"v_mfma_f32_16x16x32_bf16 v[184:187], a[216:219], v[172:175], v[184:187]\n"
"v_mfma_f32_16x16x32_bf16 v[184:187], a[220:223], v[152:155], v[184:187]\n"
"buffer_load_dwordx4 v[200:203], v1, s[4:7], s98 offen sc1\n"
"v_mfma_f32_16x16x32_bf16 v[96:99], a[252:255], v[152:155], v[96:99]\n"
"s_waitcnt lgkmcnt(0)\n"
"v_add_f32_e32 v168, v128, v168\n"
"v_add_f32_e32 v169, v129, v169\n"
"v_mfma_f32_16x16x32_bf16 v[96:99], a[248:251], v[172:175], v[96:99]\n"
"v_add_f32_e32 v170, v130, v170\n"
"v_add_f32_e32 v171, v131, v171\n"
"v_mfma_f32_16x16x32_bf16 v[96:99], a[244:247], v[164:167], v[96:99]\n"
"buffer_load_dwordx4 v[128:131], v1, s[4:7], s98 offen offset:1024 sc1\n"
"v_mfma_f32_16x16x32_bf16 v[96:99], a[240:243], v[156:159], v[96:99]\n"
"v_add_f32_e32 v176, v144, v176\n"
"v_add_f32_e32 v177, v145, v177\n"
"v_mfma_f32_16x16x32_bf16 v[96:99], a[236:239], v[132:135], v[96:99]\n"
"v_add_f32_e32 v178, v146, v178\n"
"v_add_f32_e32 v179, v147, v179\n"
"v_mfma_f32_16x16x32_bf16 v[96:99], a[232:235], v[120:123], v[96:99]\n"
"buffer_load_dwordx4 v[120:123], v1, s[4:7], s98 offen offset:2048 sc1\n"
"v_mfma_f32_16x16x32_bf16 v[96:99], a[228:231], v[124:127], v[96:99]\n"
"v_cvt_pk_bf16_f32 v124, v168, v169\n"
"v_cvt_pk_bf16_f32 v125, v170, v171\n"
"v_mfma_f32_16x16x32_bf16 v[96:99], a[224:227], v[160:163], v[96:99]\n"
"s_add_u32 s97, s61, 0x440000\n"
"v_cvt_pk_bf16_f32 v126, v176, v177\n"
"v_cvt_pk_bf16_f32 v127, v178, v179\n"
"buffer_store_dwordx4 v[124:127], v0, s[4:7], s97 offen sc1\n"
"ds_write_b128 v2, v[124:127] offset:8192\n"
"buffer_load_dwordx4 v[132:135], v1, s[4:7], s98 offen offset:3072 sc1\n"
"; @A fin b\n"
"s_add_u32 s98, s61, 0x440000\n"
"v_add_f32_e32 v188, v184, v188\n"
"v_add_f32_e32 v189, v185, v189\n"
"v_add_f32_e32 v190, v186, v190\n"
"v_add_f32_e32 v191, v187, v191\n"
"v_add_f32_e32 v192, v96, v192\n"
"v_add_f32_e32 v193, v97, v193\n"
"v_add_f32_e32 v194, v98, v194\n"
"v_add_f32_e32 v195, v99, v195\n"
"v_cvt_pk_bf16_f32 v96, v188, v189\n"
"v_cvt_pk_bf16_f32 v97, v190, v191\n"
"v_cvt_pk_bf16_f32 v98, v192, v193\n"
"v_cvt_pk_bf16_f32 v99, v194, v195\n"
"buffer_store_dwordx4 v[96:99], v0, s[4:7], s98 offen offset:1024 sc1\n"
"s_waitcnt vmcnt(16)\n"
"; @A G2 a.0\n"
"v_mfma_f32_16x16x32_bf16 v[144:147], a[128:131], v[100:103], 0\n"
"s_add_u32 s98, s58, 0\n"
"buffer_load_dwordx4 v[152:155], v0, s[4:7], s98 offen sc1\n"
"v_mfma_f32_16x16x32_bf16 v[144:147], a[132:135], v[180:183], v[144:147]\n"
"s_waitcnt vmcnt(15)\n"
"ds_write_b128 v2, v[96:99] offset:9216\n"
"v_mfma_f32_16x16x32_bf16 v[144:147], a[136:139], v[104:107], v[144:147]\n"
"v_mov_b32_e32 v156, 0x3f803f80\n"
"v_mov_b32_e32 v157, 0x3f803f80\n"
"v_mfma_f32_16x16x32_bf16 v[144:147], a[140:143], v[88:91], v[144:147]\n"
"s_waitcnt vmcnt(13)\n"
"v_mov_b32_e32 v158, 0x3f803f80\n"
"v_mov_b32_e32 v159, 0x3f803f80\n"
"v_mfma_f32_16x16x32_bf16 v[144:147], a[144:147], v[92:95], v[144:147]\n"
"buffer_load_dwordx4 v[160:163], v0, s[4:7], s98 offen offset:1024 sc1\n"
"v_mfma_f32_16x16x32_bf16 v[164:167], v[156:159], v[124:127], 0\n"
"v_mfma_f32_16x16x32_bf16 v[164:167], v[156:159], v[96:99], v[164:167]\n"
"v_mfma_f32_16x16x32_bf16 v[156:159], v[124:127], v[124:127], 0\n"
"v_mfma_f32_16x16x32_bf16 v[156:159], v[96:99], v[96:99], v[156:159]\n"
"v_mfma_f32_16x16x32_bf16 v[144:147], a[148:151], v[116:119], v[144:147]\n"
"s_waitcnt vmcnt(12)\n"
"v_mfma_f32_16x16x32_bf16 v[144:147], a[152:155], v[136:139], v[144:147]\n"
"v_mfma_f32_16x16x32_bf16 v[144:147], a[156:159], v[140:143], v[144:147]\n"
"v_mfma_f32_16x16x32_bf16 v[96:99], a[188:191], v[140:143], 0\n"
"buffer_load_dwordx4 v[124:127], v0, s[4:7], s98 offen offset:2048 sc1\n"
"v_mfma_f32_16x16x32_bf16 v[96:99], a[184:187], v[136:139], v[96:99]\n"
"s_mov_b32 s96, 0xaaaaaaaa\n"
"s_mov_b32 s97, 0xaaaaaaaa\n"
"v_mfma_f32_16x16x32_bf16 v[96:99], a[180:183], v[116:119], v[96:99]\n"
"v_mfma_f32_16x16x32_bf16 v[96:99], a[176:179], v[92:95], v[96:99]\n"
"v_mfma_f32_16x16x32_bf16 v[96:99], a[172:175], v[88:91], v[96:99]\n"
"v_cmp_u_f32_e32 vcc, v144, v144\n"
"v_cndmask_b32_e64 v156, v156, v157, s[96:97]\n"
"v_mfma_f32_16x16x32_bf16 v[96:99], a[168:171], v[104:107], v[96:99]\n"
"s_cbranch_vccnz .Lmk_p35s\n"
".Lmk_p35r:\n"
"v_cndmask_b32_e64 v158, v158, v159, s[96:97]\n"
"v_mfma_f32_16x16x32_bf16 v[96:99], a[164:167], v[180:183], v[96:99]\n"
"ds_write_b128 v3, v[144:147]\n"
"v_mfma_f32_16x16x32_bf16 v[96:99], a[160:163], v[100:103], v[96:99]\n"
"buffer_load_dwordx4 v[144:147], v0, s[4:7], s98 offen offset:3072 sc1\n"
"v_mfma_f32_16x16x32_bf16 v[168:171], a[192:195], v[100:103], 0\n"
"s_mov_b32 s96, 0xcccccccc\n"
"s_mov_b32 s97, 0xcccccccc\n"
"v_mfma_f32_16x16x32_bf16 v[168:171], a[196:199], v[180:183], v[168:171]\n"
"v_cndmask_b32_e64 v165, v156, v158, s[96:97]\n"
"v_mfma_f32_16x16x32_bf16 v[168:171], a[200:203], v[104:107], v[168:171]\n"
"s_mov_b32 s96, 0xf0000f\n"
"s_mov_b32 s97, 0xf0000f00\n"
"v_mfma_f32_16x16x32_bf16 v[168:171], a[204:207], v[88:91], v[168:171]\n"
"buffer_load_dwordx4 v[156:159], v1, s[4:7], s98 offen sc1\n"
"v_mfma_f32_16x16x32_bf16 v[168:171], a[208:211], v[92:95], v[168:171]\n"
"v_cndmask_b32_e64 v165, 0, v165, s[96:97]\n"
"v_mov_b32_e32 v255, v165\n"
"v_mfma_f32_16x16x32_bf16 v[168:171], a[212:215], v[116:119], v[168:171]\n"
"ds_write_b128 v3, v[96:99] offset:1024\n"
"v_mfma_f32_16x16x32_bf16 v[168:171], a[216:219], v[136:139], v[168:171]\n"
"v_permlane16_swap_b32_e32 v165, v255\n"
"v_add_f32_e32 v165, v165, v255\n"
"v_mfma_f32_16x16x32_bf16 v[168:171], a[220:223], v[140:143], v[168:171]\n"
"buffer_load_dwordx4 v[172:175], v1, s[4:7], s98 offen offset:1024 sc1\n"
"v_mfma_f32_16x16x32_bf16 v[96:99], a[252:255], v[140:143], 0\n"
"v_mov_b32_e32 v255, v165\n"
"v_mfma_f32_16x16x32_bf16 v[96:99], a[248:251], v[136:139], v[96:99]\n"
"s_mov_b32 s96, 0xffff0000\n"
"v_permlane32_swap_b32_e32 v165, v255\n"
"v_add_f32_e32 v165, v165, v255\n"
"v_mfma_f32_16x16x32_bf16 v[96:99], a[244:247], v[116:119], v[96:99]\n"
"v_cndmask_b32_e64 v164, v164, v165, s[96:97]\n"
"s_add_u32 s97, s63, 0x44000\n"
"v_mfma_f32_16x16x32_bf16 v[96:99], a[240:243], v[92:95], v[96:99]\n"
"buffer_load_dwordx4 v[116:119], v1, s[4:7], s98 offen offset:2048 sc1\n"
"v_mfma_f32_16x16x32_bf16 v[96:99], a[236:239], v[88:91], v[96:99]\n"
"buffer_store_dword v164, v14, s[4:7], s97 offen sc1\n"
"v_mfma_f32_16x16x32_bf16 v[96:99], a[232:235], v[104:107], v[96:99]\n"
"ds_write_b128 v3, v[168:171] offset:2048\n"
"v_mfma_f32_16x16x32_bf16 v[96:99], a[228:231], v[180:183], v[96:99]\n"
"v_mfma_f32_16x16x32_bf16 v[96:99], a[224:227], v[100:103], v[96:99]\n"
"s_waitcnt vmcnt(14)\n"
"buffer_load_dwordx4 v[136:139], v1, s[4:7], s98 offen offset:3072 sc1\n"
"; @A G2 a.1\n"
"v_mfma_f32_16x16x32_bf16 v[88:91], a[128:131], v[148:151], 0\n"
"v_mfma_f32_16x16x32_bf16 v[88:91], a[132:135], v[108:111], v[88:91]\n"
"s_add_u32 s98, s59, 0\n"
"v_mfma_f32_16x16x32_bf16 v[88:91], a[136:139], v[112:115], v[88:91]\n"
"v_mfma_f32_16x16x32_bf16 v[88:91], a[140:143], v[196:199], v[88:91]\n"
"s_waitcnt vmcnt(10)\n"
"buffer_load_dwordx4 v[176:179], v0, s[4:7], s98 offen sc1\n"
"v_mfma_f32_16x16x32_bf16 v[88:91], a[144:147], v[200:203], v[88:91]\n"
"v_mfma_f32_16x16x32_bf16 v[88:91], a[148:151], v[128:131], v[88:91]\n"
"ds_write_b128 v3, v[96:99] offset:3072\n"
"v_mfma_f32_16x16x32_bf16 v[88:91], a[152:155], v[120:123], v[88:91]\n"
"v_mfma_f32_16x16x32_bf16 v[88:91], a[156:159], v[132:135], v[88:91]\n"
"buffer_load_dwordx4 v[180:183], v0, s[4:7], s98 offen offset:1024 sc1\n"
"v_mfma_f32_16x16x32_bf16 v[92:95], a[188:191], v[132:135], 0\n"
"v_mfma_f32_16x16x32_bf16 v[92:95], a[184:187], v[120:123], v[92:95]\n"
"v_mfma_f32_16x16x32_bf16 v[92:95], a[180:183], v[128:131], v[92:95]\n"
"v_mfma_f32_16x16x32_bf16 v[92:95], a[176:179], v[200:203], v[92:95]\n"
"buffer_load_dwordx4 v[168:171], v0, s[4:7], s98 offen offset:2048 sc1\n"
"v_mfma_f32_16x16x32_bf16 v[92:95], a[172:175], v[196:199], v[92:95]\n"
"s_nop 0\n"
"v_cmp_u_f32_e32 vcc, v88, v88\n"
"v_mfma_f32_16x16x32_bf16 v[92:95], a[168:171], v[112:115], v[92:95]\n"
"s_cbranch_vccnz .Lmk_p36s\n"
".Lmk_p36r:\n"
"v_mfma_f32_16x16x32_bf16 v[92:95], a[164:167], v[108:111], v[92:95]\n"
"ds_write_b128 v3, v[88:91] offset:6144\n"
"v_mfma_f32_16x16x32_bf16 v[92:95], a[160:163], v[148:151], v[92:95]\n"
"buffer_load_dwordx4 v[184:187], v0, s[4:7], s98 offen offset:3072 sc1\n"
"v_mfma_f32_16x16x32_bf16 v[88:91], a[192:195], v[148:151], 0\n"
"v_mfma_f32_16x16x32_bf16 v[88:91], a[196:199], v[108:111], v[88:91]\n"
"v_mfma_f32_16x16x32_bf16 v[88:91], a[200:203], v[112:115], v[88:91]\n"
"v_mfma_f32_16x16x32_bf16 v[88:91], a[204:207], v[196:199], v[88:91]\n"
"buffer_load_dwordx4 v[188:191], v1, s[4:7], s98 offen sc1\n"
"v_mfma_f32_16x16x32_bf16 v[88:91], a[208:211], v[200:203], v[88:91]\n"
"ds_read_b128 v[96:99], v2\n"
"ds_read_b128 v[100:103], v2 offset:1024\n"
"v_mfma_f32_16x16x32_bf16 v[88:91], a[212:215], v[128:131], v[88:91]\n"
"ds_write_b128 v3, v[92:95] offset:7168\n"
"v_mfma_f32_16x16x32_bf16 v[88:91], a[216:219], v[120:123], v[88:91]\n"
"v_mfma_f32_16x16x32_bf16 v[88:91], a[220:223], v[132:135], v[88:91]\n"
"buffer_load_dwordx4 v[164:167], v1, s[4:7], s98 offen offset:1024 sc1\n"
"v_mfma_f32_16x16x32_bf16 v[92:95], a[252:255], v[132:135], 0\n"
"v_mfma_f32_16x16x32_bf16 v[92:95], a[248:251], v[120:123], v[92:95]\n"
"s_waitcnt lgkmcnt(1)\n"
"v_lshlrev_b32_e32 v255, 16, v96\n"
"v_and_b32_e32 v254, 0xffff0000, v96\n"
"v_mfma_f32_16x16x32_bf16 v[92:95], a[244:247], v[128:131], v[92:95]\n"
"v_lshlrev_b32_e32 v253, 16, v97\n"
"v_and_b32_e32 v252, 0xffff0000, v97\n"
"v_mfma_f32_16x16x32_bf16 v[92:95], a[240:243], v[200:203], v[92:95]\n"
"buffer_load_dwordx4 v[192:195], v1, s[4:7], s98 offen offset:2048 sc1\n"
"v_mfma_f32_16x16x32_bf16 v[92:95], a[236:239], v[196:199], v[92:95]\n"
"v_lshlrev_b32_e32 v251, 16, v98\n"
"v_and_b32_e32 v250, 0xffff0000, v98\n"
"v_mfma_f32_16x16x32_bf16 v[92:95], a[232:235], v[112:115], v[92:95]\n"
"ds_write_b128 v3, v[88:91] offset:8192\n"
"v_mfma_f32_16x16x32_bf16 v[92:95], a[228:231], v[108:111], v[92:95]\n"
"v_lshlrev_b32_e32 v249, 16, v99\n"
"v_and_b32_e32 v248, 0xffff0000, v99\n"
"v_mfma_f32_16x16x32_bf16 v[92:95], a[224:227], v[148:151], v[92:95]\n"
"s_waitcnt vmcnt(12)\n"
"buffer_load_dwordx4 v[196:199], v1, s[4:7], s98 offen offset:3072 sc1\n"
"; @A G2 a.2\n"
"v_mfma_f32_16x16x32_bf16 v[96:99], a[128:131], v[152:155], 0\n"
"v_lshlrev_b32_e32 v247, 16, v100\n"
"v_and_b32_e32 v246, 0xffff0000, v100\n"
"v_mfma_f32_16x16x32_bf16 v[96:99], a[132:135], v[160:163], v[96:99]\n"
"v_lshlrev_b32_e32 v245, 16, v101\n"
"v_and_b32_e32 v244, 0xffff0000, v101\n"
"v_mfma_f32_16x16x32_bf16 v[96:99], a[136:139], v[124:127], v[96:99]\n"
"v_lshlrev_b32_e32 v243, 16, v102\n"
"v_and_b32_e32 v242, 0xffff0000, v102\n"
"v_mfma_f32_16x16x32_bf16 v[96:99], a[140:143], v[144:147], v[96:99]\n"
"s_waitcnt vmcnt(8)\n"
"v_lshlrev_b32_e32 v241, 16, v103\n"
"v_and_b32_e32 v240, 0xffff0000, v103\n"
"v_mfma_f32_16x16x32_bf16 v[96:99], a[144:147], v[156:159], v[96:99]\n"
"v_mfma_f32_16x16x32_bf16 v[96:99], a[148:151], v[172:175], v[96:99]\n"
"ds_write_b128 v3, v[92:95] offset:9216\n"
"v_mfma_f32_16x16x32_bf16 v[96:99], a[152:155], v[116:119], v[96:99]\n"
"v_mfma_f32_16x16x32_bf16 v[96:99], a[156:159], v[136:139], v[96:99]\n"
"v_mfma_f32_16x16x32_bf16 v[104:107], a[188:191], v[136:139], 0\n"
"s_add_u32 s98, s52, 0x440000\n"
"buffer_load_dwordx4 v[88:91], v0, s[4:7], s98 offen sc1\n"
"v_mfma_f32_16x16x32_bf16 v[104:107], a[184:187], v[116:119], v[104:107]\n"
"s_waitcnt lgkmcnt(0)\n"
"s_barrier\n"
"v_mfma_f32_16x16x32_bf16 v[104:107], a[180:183], v[172:175], v[104:107]\n"
"ds_read_b128 v[140:143], v4\n"
"ds_read_b128 v[112:115], v5\n"
"v_mfma_f32_16x16x32_bf16 v[104:107], a[176:179], v[156:159], v[104:107]\n"
"buffer_load_dwordx4 v[92:95], v0, s[4:7], s98 offen offset:1024 sc1\n"
"v_mfma_f32_16x16x32_bf16 v[104:107], a[172:175], v[144:147], v[104:107]\n"
"v_cmp_u_f32_e32 vcc, v96, v96\n"
"v_mfma_f32_16x16x32_bf16 v[104:107], a[168:171], v[124:127], v[104:107]\n"
"s_cbranch_vccnz .Lmk_p37s\n"
".Lmk_p37r:\n"
"v_mfma_f32_16x16x32_bf16 v[104:107], a[164:167], v[160:163], v[104:107]\n"
"ds_write_b128 v3, v[96:99] offset:12288\n"
"v_mfma_f32_16x16x32_bf16 v[104:107], a[160:163], v[152:155], v[104:107]\n"
"buffer_load_dwordx4 v[96:99], v0, s[4:7], s98 offen offset:2048 sc1\n"
"v_mfma_f32_16x16x32_bf16 v[120:123], a[192:195], v[152:155], 0\n"
"ds_read_b128 v[148:151], v4 offset:1024\n"
"ds_read_b128 v[128:131], v5 offset:1024\n"
"v_mfma_f32_16x16x32_bf16 v[120:123], a[196:199], v[160:163], v[120:123]\n"
"s_waitcnt lgkmcnt(3)\n"
"v_add_f32_e32 v140, v140, v255\n"
"v_add_f32_e32 v141, v141, v254\n"
"v_mfma_f32_16x16x32_bf16 v[120:123], a[200:203], v[124:127], v[120:123]\n"
"buffer_load_dwordx4 v[100:103], v0, s[4:7], s98 offen offset:3072 sc1\n"
"v_mfma_f32_16x16x32_bf16 v[120:123], a[204:207], v[144:147], v[120:123]\n"
"v_add_f32_e32 v142, v142, v253\n"
"v_add_f32_e32 v143, v143, v252\n"
"v_mfma_f32_16x16x32_bf16 v[120:123], a[208:211], v[156:159], v[120:123]\n"
"v_add_f32_e32 v140, v140, v112\n"
"v_add_f32_e32 v141, v141, v113\n"
"v_mfma_f32_16x16x32_bf16 v[120:123], a[212:215], v[172:175], v[120:123]\n"
"ds_write_b128 v3, v[104:107] offset:13312\n"
"v_mfma_f32_16x16x32_bf16 v[120:123], a[216:219], v[116:119], v[120:123]\n"
"buffer_load_dwordx4 v[104:107], v1, s[4:7], s98 offen sc1\n"
"v_mfma_f32_16x16x32_bf16 v[120:123], a[220:223], v[136:139], v[120:123]\n"
"ds_read_b128 v[108:111], v4 offset:2048\n"
"ds_read_b128 v[200:203], v5 offset:2048\n"
"v_mfma_f32_16x16x32_bf16 v[132:135], a[252:255], v[136:139], 0\n"
"s_waitcnt lgkmcnt(3)\n"
"v_add_f32_e32 v142, v142, v114\n"
"v_add_f32_e32 v143, v143, v115\n"
"v_mfma_f32_16x16x32_bf16 v[132:135], a[248:251], v[116:119], v[132:135]\n"
"buffer_load_dwordx4 v[112:115], v1, s[4:7], s98 offen offset:1024 sc1\n"
"v_mfma_f32_16x16x32_bf16 v[132:135], a[244:247], v[172:175], v[132:135]\n"
"v_add_f32_e32 v148, v148, v251\n"
"v_add_f32_e32 v149, v149, v250\n"
"v_mfma_f32_16x16x32_bf16 v[132:135], a[240:243], v[156:159], v[132:135]\n"
"v_add_f32_e32 v150, v150, v249\n"
"v_add_f32_e32 v151, v151, v248\n"
"v_mfma_f32_16x16x32_bf16 v[132:135], a[236:239], v[144:147], v[132:135]\n"
"buffer_load_dwordx4 v[116:119], v1, s[4:7], s98 offen offset:2048 sc1\n"
"v_mfma_f32_16x16x32_bf16 v[132:135], a[232:235], v[124:127], v[132:135]\n"
"ds_write_b128 v3, v[120:123] offset:14336\n"
"v_mfma_f32_16x16x32_bf16 v[132:135], a[228:231], v[160:163], v[132:135]\n"
"ds_read_b128 v[120:123], v4 offset:3072\n"
"ds_read_b128 v[144:147], v5 offset:3072\n"
"v_mfma_f32_16x16x32_bf16 v[132:135], a[224:227], v[152:155], v[132:135]\n"
"s_waitcnt vmcnt(11)\n"
"buffer_load_dwordx4 v[124:127], v1, s[4:7], s98 offen offset:3072 sc1\n"
"; @A G2 a.3\n"
"v_mfma_f32_16x16x32_bf16 v[172:175], a[128:131], v[176:179], v[140:143]\n"
"s_add_u32 s98, s53, 0x440000\n"
"v_add_f32_e32 v148, v148, v128\n"
"v_add_f32_e32 v149, v149, v129\n"
"v_mfma_f32_16x16x32_bf16 v[172:175], a[132:135], v[180:183], v[172:175]\n"
"s_waitcnt lgkmcnt(3)\n"
"v_add_f32_e32 v150, v150, v130\n"
"v_add_f32_e32 v151, v151, v131\n"
"v_mfma_f32_16x16x32_bf16 v[172:175], a[136:139], v[168:171], v[172:175]\n"
"buffer_load_dwordx4 v[128:131], v0, s[4:7], s98 offen sc1\n"
"v_mfma_f32_16x16x32_bf16 v[172:175], a[140:143], v[184:187], v[172:175]\n"
"s_waitcnt vmcnt(9)\n"
"v_add_f32_e32 v108, v108, v247\n"
"v_add_f32_e32 v109, v109, v246\n"
"v_mfma_f32_16x16x32_bf16 v[172:175], a[144:147], v[188:191], v[172:175]\n"
"v_add_f32_e32 v110, v110, v245\n"
"v_add_f32_e32 v111, v111, v244\n"
"v_mfma_f32_16x16x32_bf16 v[172:175], a[148:151], v[164:167], v[172:175]\n"
"ds_write_b128 v3, v[132:135] offset:15360\n"
"v_mfma_f32_16x16x32_bf16 v[172:175], a[152:155], v[192:195], v[172:175]\n"
"buffer_load_dwordx4 v[132:135], v0, s[4:7], s98 offen offset:1024 sc1\n"
"v_mfma_f32_16x16x32_bf16 v[172:175], a[156:159], v[196:199], v[172:175]\n"
"v_add_f32_e32 v108, v108, v200\n"
"v_add_f32_e32 v109, v109, v201\n"
"v_mfma_f32_16x16x32_bf16 v[204:207], a[188:191], v[196:199], v[148:151]\n"
"s_waitcnt lgkmcnt(1)\n"
"v_add_f32_e32 v110, v110, v202\n"
"v_add_f32_e32 v111, v111, v203\n"
"v_mfma_f32_16x16x32_bf16 v[204:207], a[184:187], v[192:195], v[204:207]\n"
"s_waitcnt lgkmcnt(0)\n"
"s_barrier\n"
"v_mfma_f32_16x16x32_bf16 v[204:207], a[180:183], v[164:167], v[204:207]\n"
"buffer_load_dwordx4 v[136:139], v0, s[4:7], s98 offen offset:2048 sc1\n"
"v_mfma_f32_16x16x32_bf16 v[204:207], a[176:179], v[188:191], v[204:207]\n"
"v_add_f32_e32 v120, v120, v243\n"
"v_add_f32_e32 v121, v121, v242\n"
"v_mfma_f32_16x16x32_bf16 v[204:207], a[172:175], v[184:187], v[204:207]\n"
"v_cmp_u_f32_e32 vcc, v172, v172\n"
"v_add_f32_e32 v122, v122, v241\n"
"v_mfma_f32_16x16x32_bf16 v[204:207], a[168:171], v[168:171], v[204:207]\n"
"s_cbranch_vccnz .Lmk_p38s\n"
".Lmk_p38r:\n"
"v_add_f32_e32 v123, v123, v240\n"
"v_mfma_f32_16x16x32_bf16 v[204:207], a[164:167], v[180:183], v[204:207]\n"
"buffer_load_dwordx4 v[140:143], v0, s[4:7], s98 offen offset:3072 sc1\n"
"v_mfma_f32_16x16x32_bf16 v[204:207], a[160:163], v[176:179], v[204:207]\n"
"v_add_f32_e32 v120, v120, v144\n"
"v_add_f32_e32 v121, v121, v145\n"
"v_mfma_f32_16x16x32_bf16 v[108:111], a[192:195], v[176:179], v[108:111]\n"
"v_add_f32_e32 v122, v122, v146\n"
"v_add_f32_e32 v123, v123, v147\n"
"v_mfma_f32_16x16x32_bf16 v[108:111], a[196:199], v[180:183], v[108:111]\n"
"buffer_load_dwordx4 v[144:147], v1, s[4:7], s98 offen sc1\n"
"v_mfma_f32_16x16x32_bf16 v[108:111], a[200:203], v[168:171], v[108:111]\n"
"ds_read_b128 v[200:203], v6\n"
"ds_read_b128 v[208:211], v6 offset:1024\n"
"v_mfma_f32_16x16x32_bf16 v[108:111], a[204:207], v[184:187], v[108:111]\n"
"ds_read_b128 v[148:151], v6 offset:2048\n"
"ds_read_b128 v[152:155], v6 offset:3072\n"
"v_mfma_f32_16x16x32_bf16 v[108:111], a[208:211], v[188:191], v[108:111]\n"
"buffer_load_dwordx4 v[156:159], v1, s[4:7], s98 offen offset:1024 sc1\n"
"v_mfma_f32_16x16x32_bf16 v[108:111], a[212:215], v[164:167], v[108:111]\n"
"v_mfma_f32_16x16x32_bf16 v[108:111], a[216:219], v[192:195], v[108:111]\n"
"v_mfma_f32_16x16x32_bf16 v[108:111], a[220:223], v[196:199], v[108:111]\n"
"buffer_load_dwordx4 v[160:163], v1, s[4:7], s98 offen offset:2048 sc1\n"
"v_mfma_f32_16x16x32_bf16 v[120:123], a[252:255], v[196:199], v[120:123]\n"
"s_waitcnt lgkmcnt(0)\n"
"v_add_f32_e32 v200, v172, v200\n"
"v_add_f32_e32 v201, v173, v201\n"
"v_mfma_f32_16x16x32_bf16 v[120:123], a[248:251], v[192:195], v[120:123]\n"
"v_add_f32_e32 v202, v174, v202\n"
"v_add_f32_e32 v203, v175, v203\n"
"v_mfma_f32_16x16x32_bf16 v[120:123], a[244:247], v[164:167], v[120:123]\n"
"buffer_load_dwordx4 v[164:167], v1, s[4:7], s98 offen offset:3072 sc1\n"
"v_mfma_f32_16x16x32_bf16 v[120:123], a[240:243], v[188:191], v[120:123]\n"
"v_add_f32_e32 v208, v204, v208\n"
"v_add_f32_e32 v209, v205, v209\n"
"v_mfma_f32_16x16x32_bf16 v[120:123], a[236:239], v[184:187], v[120:123]\n"
"v_add_f32_e32 v210, v206, v210\n"
"v_add_f32_e32 v211, v207, v211\n"
"v_mfma_f32_16x16x32_bf16 v[120:123], a[232:235], v[168:171], v[120:123]\n"
"v_cvt_pk_bf16_f32 v168, v200, v201\n"
"v_cvt_pk_bf16_f32 v169, v202, v203\n"
"v_mfma_f32_16x16x32_bf16 v[120:123], a[228:231], v[180:183], v[120:123]\n"
"s_add_u32 s98, s61, 0x400000\n"
"v_cvt_pk_bf16_f32 v170, v208, v209\n"
"v_cvt_pk_bf16_f32 v171, v210, v211\n"
"v_mfma_f32_16x16x32_bf16 v[120:123], a[224:227], v[176:179], v[120:123]\n"
"buffer_store_dwordx4 v[168:171], v0, s[4:7], s98 offen sc1\n"
"ds_write_b128 v2, v[168:171]\n"
".Lmk_loop:\n"
".Lmk_b0:\n"
"s_waitcnt vmcnt(13)\n"
"; @B G1 0.0\n"
"v_mfma_f32_16x16x32_bf16 v[172:175], a[0:3], v[88:91], 0\n"
"v_mfma_f32_16x16x32_bf16 v[172:175], a[4:7], v[92:95], v[172:175]\n"
"s_add_u32 s98, s62, 0x44000\n"
"v_mfma_f32_16x16x32_bf16 v[172:175], a[8:11], v[96:99], v[172:175]\n"
"s_add_u32 s97, s54, 0x440000\n"
"buffer_load_dwordx4 v[176:179], v0, s[4:7], s97 offen sc1\n"
"v_mfma_f32_16x16x32_bf16 v[172:175], a[12:15], v[100:103], v[172:175]\n"
"s_waitcnt vmcnt(10)\n"
"v_mfma_f32_16x16x32_bf16 v[172:175], a[16:19], v[104:107], v[172:175]\n"
"buffer_load_dword v255, v10, s[4:7], s98 offen sc1\n"
"v_mfma_f32_16x16x32_bf16 v[172:175], a[20:23], v[112:115], v[172:175]\n"
"v_add_f32_e32 v148, v108, v148\n"
"v_add_f32_e32 v149, v109, v149\n"
"v_mfma_f32_16x16x32_bf16 v[172:175], a[24:27], v[116:119], v[172:175]\n"
"v_add_f32_e32 v150, v110, v150\n"
"v_add_f32_e32 v151, v111, v151\n"
"v_mfma_f32_16x16x32_bf16 v[172:175], a[28:31], v[124:127], v[172:175]\n"
"buffer_load_dwordx4 v[108:111], v0, s[4:7], s97 offen offset:1024 sc1\n"
"v_mfma_f32_16x16x32_bf16 v[180:183], a[60:63], v[124:127], 0\n"
"buffer_load_dword v254, v10, s[4:7], s98 offen offset:256 sc1\n"
"v_mfma_f32_16x16x32_bf16 v[180:183], a[56:59], v[116:119], v[180:183]\n"
"v_add_f32_e32 v152, v120, v152\n"
"v_add_f32_e32 v153, v121, v153\n"
"v_mfma_f32_16x16x32_bf16 v[180:183], a[52:55], v[112:115], v[180:183]\n"
"v_add_f32_e32 v154, v122, v154\n"
"v_add_f32_e32 v155, v123, v155\n"
"v_mfma_f32_16x16x32_bf16 v[180:183], a[48:51], v[104:107], v[180:183]\n"
"buffer_load_dword v253, v10, s[4:7], s98 offen offset:512 sc1\n"
"v_mfma_f32_16x16x32_bf16 v[180:183], a[44:47], v[100:103], v[180:183]\n"
"v_cmp_u_f32_e32 vcc, v172, v172\n"
"v_cvt_pk_bf16_f32 v120, v148, v149\n"
"v_mfma_f32_16x16x32_bf16 v[180:183], a[40:43], v[96:99], v[180:183]\n"
"s_cbranch_vccnz .Lmk_p43s\n"
".Lmk_p43r:\n"
"v_cvt_pk_bf16_f32 v121, v150, v151\n"
"v_mfma_f32_16x16x32_bf16 v[180:183], a[36:39], v[92:95], v[180:183]\n"
"buffer_load_dwordx4 v[148:151], v0, s[4:7], s97 offen offset:2048 sc1\n"
"v_mfma_f32_16x16x32_bf16 v[180:183], a[32:35], v[88:91], v[180:183]\n"
"ds_write_b128 v3, v[172:175]\n"
"v_mfma_f32_16x16x32_bf16 v[172:175], a[64:67], v[88:91], 0\n"
"buffer_load_dword v252, v10, s[4:7], s98 offen offset:768 sc1\n"
"v_mfma_f32_16x16x32_bf16 v[172:175], a[68:71], v[92:95], v[172:175]\n"
"buffer_load_dwordx4 v[184:187], v0, s[4:7], s97 offen offset:3072 sc1\n"
"v_mfma_f32_16x16x32_bf16 v[172:175], a[72:75], v[96:99], v[172:175]\n"
"s_add_u32 s96, s61, 0x400000\n"
"v_cvt_pk_bf16_f32 v122, v152, v153\n"
"v_cvt_pk_bf16_f32 v123, v154, v155\n"
"v_mfma_f32_16x16x32_bf16 v[172:175], a[76:79], v[100:103], v[172:175]\n"
"buffer_load_dword v251, v10, s[4:7], s98 offen offset:1024 sc1\n"
"v_mfma_f32_16x16x32_bf16 v[172:175], a[80:83], v[104:107], v[172:175]\n"
"buffer_store_dwordx4 v[120:123], v0, s[4:7], s96 offen offset:1024 sc1\n"
"v_mfma_f32_16x16x32_bf16 v[172:175], a[84:87], v[112:115], v[172:175]\n"
"ds_write_b128 v3, v[180:183] offset:1024\n"
"v_mfma_f32_16x16x32_bf16 v[172:175], a[88:91], v[116:119], v[172:175]\n"
"buffer_load_dwordx4 v[152:155], v1, s[4:7], s97 offen sc1\n"
"v_mfma_f32_16x16x32_bf16 v[172:175], a[92:95], v[124:127], v[172:175]\n"
"buffer_load_dword v250, v10, s[4:7], s98 offen offset:1280 sc1\n"
"v_mfma_f32_16x16x32_bf16 v[180:183], a[124:127], v[124:127], 0\n"
"ds_write_b128 v2, v[120:123] offset:1024\n"
"v_mfma_f32_16x16x32_bf16 v[180:183], a[120:123], v[116:119], v[180:183]\n"
"v_mov_b32_e32 v188, 0x3f803f80\n"
"v_mov_b32_e32 v189, 0x3f803f80\n"
"v_mfma_f32_16x16x32_bf16 v[180:183], a[116:119], v[112:115], v[180:183]\n"
"buffer_load_dword v249, v10, s[4:7], s98 offen offset:1536 sc1\n"
"v_mfma_f32_16x16x32_bf16 v[180:183], a[112:115], v[104:107], v[180:183]\n"
"buffer_load_dwordx4 v[192:195], v1, s[4:7], s97 offen offset:1024 sc1\n"
"v_mfma_f32_16x16x32_bf16 v[180:183], a[108:111], v[100:103], v[180:183]\n"
"v_mov_b32_e32 v190, 0x3f803f80\n"
"v_mov_b32_e32 v191, 0x3f803f80\n"
"v_mfma_f32_16x16x32_bf16 v[180:183], a[104:107], v[96:99], v[180:183]\n"
"ds_write_b128 v3, v[172:175] offset:2048\n"
"v_mfma_f32_16x16x32_bf16 v[172:175], v[188:191], v[168:171], 0\n"
"v_mfma_f32_16x16x32_bf16 v[172:175], v[188:191], v[120:123], v[172:175]\n"
"v_mfma_f32_16x16x32_bf16 v[188:191], v[168:171], v[168:171], 0\n"
"v_mfma_f32_16x16x32_bf16 v[188:191], v[120:123], v[120:123], v[188:191]\n"
"v_mfma_f32_16x16x32_bf16 v[180:183], a[100:103], v[92:95], v[180:183]\n"
"v_mov_b32_e32 v120, -1\n"
"v_mov_b32_e32 v121, -1\n"
"v_mfma_f32_16x16x32_bf16 v[180:183], a[96:99], v[88:91], v[180:183]\n"
"v_mov_b32_e32 v122, -1\n"
"v_mov_b32_e32 v123, -1\n"
"v_mfma_f32_16x16x32_bf16 v[168:171], v[24:27], v[88:91], 0\n"
"buffer_load_dwordx4 v[196:199], v1, s[4:7], s97 offen offset:2048 sc1\n"
"v_mfma_f32_16x16x32_bf16 v[168:171], v[28:31], v[92:95], v[168:171]\n"
"s_add_u32 s96, s60, 0x448000\n"
"buffer_store_dwordx4 v[120:123], v0, s[4:7], s96 offen sc1\n"
"v_mfma_f32_16x16x32_bf16 v[168:171], v[32:35], v[96:99], v[168:171]\n"
"buffer_load_dword v248, v10, s[4:7], s98 offen offset:1792 sc1\n"
"v_mfma_f32_16x16x32_bf16 v[168:171], v[36:39], v[100:103], v[168:171]\n"
"s_mov_b32 s94, 0xaaaaaaaa\n"
"s_mov_b32 s95, 0xaaaaaaaa\n"
"v_mfma_f32_16x16x32_bf16 v[168:171], v[40:43], v[104:107], v[168:171]\n"
"v_mfma_f32_16x16x32_bf16 v[168:171], v[44:47], v[112:115], v[168:171]\n"
"buffer_load_dwordx4 v[200:203], v1, s[4:7], s97 offen offset:3072 sc1\n"
"v_mfma_f32_16x16x32_bf16 v[168:171], v[48:51], v[116:119], v[168:171]\n"
"ds_write_b128 v3, v[180:183] offset:3072\n"
"v_mfma_f32_16x16x32_bf16 v[168:171], v[52:55], v[124:127], v[168:171]\n"
"v_cndmask_b32_e64 v188, v188, v189, s[94:95]\n"
"v_cndmask_b32_e64 v190, v190, v191, s[94:95]\n"
"v_mfma_f32_16x16x32_bf16 v[180:183], v[84:87], v[124:127], 0\n"
"s_mov_b32 s94, 0xcccccccc\n"
"s_mov_b32 s95, 0xcccccccc\n"
"v_mfma_f32_16x16x32_bf16 v[180:183], v[80:83], v[116:119], v[180:183]\n"
"v_cndmask_b32_e64 v173, v188, v190, s[94:95]\n"
"v_mfma_f32_16x16x32_bf16 v[180:183], v[76:79], v[112:115], v[180:183]\n"
"s_mov_b32 s94, 0xf0000f\n"
"s_mov_b32 s95, 0xf0000f00\n"
"v_mfma_f32_16x16x32_bf16 v[180:183], v[72:75], v[104:107], v[180:183]\n"
"v_cndmask_b32_e64 v173, 0, v173, s[94:95]\n"
"v_mov_b32_e32 v247, v173\n"
"v_mfma_f32_16x16x32_bf16 v[180:183], v[68:71], v[100:103], v[180:183]\n"
"s_add_u32 s98, s70, 0x200000\n"
"buffer_load_dwordx4 a[128:131], v0, s[4:7], s98 offen sc1\n"
"v_mfma_f32_16x16x32_bf16 v[180:183], v[64:67], v[96:99], v[180:183]\n"
"ds_write_b128 v3, v[168:171] offset:4096\n"
"v_mfma_f32_16x16x32_bf16 v[180:183], v[60:63], v[92:95], v[180:183]\n"
"v_permlane16_swap_b32_e32 v173, v247\n"
"v_add_f32_e32 v173, v173, v247\n"
"v_mfma_f32_16x16x32_bf16 v[180:183], v[56:59], v[88:91], v[180:183]\n"
"s_waitcnt vmcnt(24)\n"
"v_mov_b32_e32 v247, v173\n"
"; @B G1 0.1\n"
"v_mfma_f32_16x16x32_bf16 v[88:91], a[0:3], v[128:131], 0\n"
"s_mov_b32 s94, 0xffff0000\n"
"v_permlane32_swap_b32_e32 v173, v247\n"
"v_add_f32_e32 v173, v173, v247\n"
"v_mfma_f32_16x16x32_bf16 v[88:91], a[4:7], v[132:135], v[88:91]\n"
"s_add_u32 s97, s63, 0x40000\n"
"v_cndmask_b32_e64 v172, v172, v173, s[94:95]\n"
"v_mfma_f32_16x16x32_bf16 v[88:91], a[8:11], v[136:139], v[88:91]\n"
"s_add_u32 s96, s55, 0x440000\n"
"buffer_load_dwordx4 v[92:95], v0, s[4:7], s96 offen sc1\n"
"v_mfma_f32_16x16x32_bf16 v[88:91], a[12:15], v[140:143], v[88:91]\n"
"s_waitcnt vmcnt(21)\n"
"buffer_store_dword v172, v14, s[4:7], s97 offen sc1\n"
"v_mfma_f32_16x16x32_bf16 v[88:91], a[16:19], v[144:147], v[88:91]\n"
"v_mfma_f32_16x16x32_bf16 v[88:91], a[20:23], v[156:159], v[88:91]\n"
"ds_write_b128 v3, v[180:183] offset:5120\n"
"v_mfma_f32_16x16x32_bf16 v[88:91], a[24:27], v[160:163], v[88:91]\n"
"v_mfma_f32_16x16x32_bf16 v[88:91], a[28:31], v[164:167], v[88:91]\n"
"buffer_load_dwordx4 v[96:99], v0, s[4:7], s96 offen offset:1024 sc1\n"
"v_mfma_f32_16x16x32_bf16 v[100:103], a[60:63], v[164:167], 0\n"
"v_mfma_f32_16x16x32_bf16 v[100:103], a[56:59], v[160:163], v[100:103]\n"
"v_mfma_f32_16x16x32_bf16 v[100:103], a[52:55], v[156:159], v[100:103]\n"
"v_mfma_f32_16x16x32_bf16 v[100:103], a[48:51], v[144:147], v[100:103]\n"
"v_mfma_f32_16x16x32_bf16 v[100:103], a[44:47], v[140:143], v[100:103]\n"
"s_nop 1\n"
"v_cmp_u_f32_e32 vcc, v88, v88\n"
"v_mfma_f32_16x16x32_bf16 v[100:103], a[40:43], v[136:139], v[100:103]\n"
"s_cbranch_vccnz .Lmk_p44s\n"
".Lmk_p44r:\n"
"v_mfma_f32_16x16x32_bf16 v[100:103], a[36:39], v[132:135], v[100:103]\n"
"buffer_load_dwordx4 v[104:107], v0, s[4:7], s96 offen offset:2048 sc1\n"
"v_mfma_f32_16x16x32_bf16 v[100:103], a[32:35], v[128:131], v[100:103]\n"
"ds_write_b128 v3, v[88:91] offset:6144\n"
"v_mfma_f32_16x16x32_bf16 v[88:91], a[64:67], v[128:131], 0\n"
"buffer_load_dwordx4 a[132:135], v0, s[4:7], s98 offen offset:1024 sc1\n"
"v_mfma_f32_16x16x32_bf16 v[88:91], a[68:71], v[132:135], v[88:91]\n"
"buffer_load_dwordx4 v[112:115], v0, s[4:7], s96 offen offset:3072 sc1\n"
"v_mfma_f32_16x16x32_bf16 v[88:91], a[72:75], v[136:139], v[88:91]\n"
"v_mfma_f32_16x16x32_bf16 v[88:91], a[76:79], v[140:143], v[88:91]\n"
"v_mfma_f32_16x16x32_bf16 v[88:91], a[80:83], v[144:147], v[88:91]\n"
"v_mfma_f32_16x16x32_bf16 v[88:91], a[84:87], v[156:159], v[88:91]\n"
"ds_write_b128 v3, v[100:103] offset:7168\n"
"v_mfma_f32_16x16x32_bf16 v[88:91], a[88:91], v[160:163], v[88:91]\n"
"buffer_load_dwordx4 v[100:103], v1, s[4:7], s96 offen sc1\n"
"v_mfma_f32_16x16x32_bf16 v[88:91], a[92:95], v[164:167], v[88:91]\n"
"v_mfma_f32_16x16x32_bf16 v[116:119], a[124:127], v[164:167], 0\n"
"v_mfma_f32_16x16x32_bf16 v[116:119], a[120:123], v[160:163], v[116:119]\n"
"v_mfma_f32_16x16x32_bf16 v[116:119], a[116:119], v[156:159], v[116:119]\n"
"v_mfma_f32_16x16x32_bf16 v[116:119], a[112:115], v[144:147], v[116:119]\n"
"buffer_load_dwordx4 v[124:127], v1, s[4:7], s96 offen offset:1024 sc1\n"
"v_mfma_f32_16x16x32_bf16 v[116:119], a[108:111], v[140:143], v[116:119]\n"
"v_mfma_f32_16x16x32_bf16 v[116:119], a[104:107], v[136:139], v[116:119]\n"
"s_nop 0\n"
"ds_write_b128 v3, v[88:91] offset:8192\n"
"v_mfma_f32_16x16x32_bf16 v[116:119], a[100:103], v[132:135], v[116:119]\n"
"v_mfma_f32_16x16x32_bf16 v[116:119], a[96:99], v[128:131], v[116:119]\n"
"v_mfma_f32_16x16x32_bf16 v[88:91], v[24:27], v[128:131], 0\n"
"buffer_load_dwordx4 v[168:171], v1, s[4:7], s96 offen offset:2048 sc1\n"
"v_mfma_f32_16x16x32_bf16 v[88:91], v[28:31], v[132:135], v[88:91]\n"
"s_add_u32 s97, s60, 0x440000\n"
"v_mfma_f32_16x16x32_bf16 v[88:91], v[32:35], v[136:139], v[88:91]\n"
"v_mfma_f32_16x16x32_bf16 v[88:91], v[36:39], v[140:143], v[88:91]\n"
"buffer_store_dwordx4 v[120:123], v0, s[4:7], s97 offen sc1\n"
"v_mfma_f32_16x16x32_bf16 v[88:91], v[40:43], v[144:147], v[88:91]\n"
"buffer_load_dwordx4 a[136:139], v0, s[4:7], s98 offen offset:2048 sc1\n"
"v_mfma_f32_16x16x32_bf16 v[88:91], v[44:47], v[156:159], v[88:91]\n"
"buffer_load_dwordx4 v[172:175], v1, s[4:7], s96 offen offset:3072 sc1\n"
"v_mfma_f32_16x16x32_bf16 v[88:91], v[48:51], v[160:163], v[88:91]\n"
"ds_write_b128 v3, v[116:119] offset:9216\n"
"v_mfma_f32_16x16x32_bf16 v[88:91], v[52:55], v[164:167], v[88:91]\n"
"s_waitcnt vmcnt(14)\n"
"v_add_f32_e32 v255, v255, v251\n"
"v_add_f32_e32 v254, v254, v250\n"
"v_mfma_f32_16x16x32_bf16 v[116:119], v[84:87], v[164:167], 0\n"
"v_add_f32_e32 v253, v253, v249\n"
"v_add_f32_e32 v252, v252, v248\n"
"v_mfma_f32_16x16x32_bf16 v[116:119], v[80:83], v[160:163], v[116:119]\n"
"v_add_f32_e32 v255, v255, v253\n"
"v_add_f32_e32 v254, v254, v252\n"
"v_mfma_f32_16x16x32_bf16 v[116:119], v[76:79], v[156:159], v[116:119]\n"
"v_add_f32_e32 v255, v255, v254\n"
"v_cmp_u_f32_e32 vcc, v255, v255\n"
"v_mfma_f32_16x16x32_bf16 v[116:119], v[72:75], v[144:147], v[116:119]\n"
"s_cbranch_vccnz .Lmk_m47s\n"
".Lmk_m47r:\n"
"v_mov_b32_e32 v254, v255\n"
"v_mfma_f32_16x16x32_bf16 v[116:119], v[68:71], v[140:143], v[116:119]\n"
"s_nop 0\n"
"v_permlane32_swap_b32_e32 v255, v254\n"
"v_add_f32_e32 v255, v255, v254\n"
"v_mfma_f32_16x16x32_bf16 v[116:119], v[64:67], v[136:139], v[116:119]\n"
"ds_write_b128 v3, v[88:91] offset:10240\n"
"v_mfma_f32_16x16x32_bf16 v[116:119], v[60:63], v[132:135], v[116:119]\n"
"v_mul_f32_e32 v255, 0x3a800000, v255\n"
"v_mov_b32_e32 v254, v255\n"
"v_mfma_f32_16x16x32_bf16 v[116:119], v[56:59], v[128:131], v[116:119]\n"
"s_nop 0\n"
"v_permlane16_swap_b32_e32 v255, v254\n"
"v_cvt_pk_bf16_f32 v253, -v255, 0\n"
"; @B G1 0.2\n"
"v_mfma_f32_16x16x32_bf16 v[88:91], a[0:3], v[176:179], 0\n"
"v_fma_f32 v254, -v255, v255, v254\n"
"v_lshlrev_b32_e32 v253, 16, v253\n"
"v_mfma_f32_16x16x32_bf16 v[88:91], a[4:7], v[108:111], v[88:91]\n"
"v_max_f32_e32 v254, 0, v254\n"
"v_cvt_pk_bf16_f32 v254, v254, 0\n"
"v_mfma_f32_16x16x32_bf16 v[88:91], a[8:11], v[148:151], v[88:91]\n"
"v_lshlrev_b32_e32 v254, 16, v254\n"
"v_add_f32_e32 v254, 0x3727c5ac, v254\n"
"v_mfma_f32_16x16x32_bf16 v[88:91], a[12:15], v[184:187], v[88:91]\n"
"s_waitcnt vmcnt(13)\n"
"v_cvt_pk_bf16_f32 v254, v254, 0\n"
"v_lshlrev_b32_e32 v254, 16, v254\n"
"v_mfma_f32_16x16x32_bf16 v[88:91], a[16:19], v[152:155], v[88:91]\n"
"s_mul_i32 s97, s0, .Lmk_exit-.Lmk_b2\n"
"s_add_u32 s97, s97, .Lmk_b2-.Lmk_g42\n"
"s_getpc_b64 s[84:85]\n"
".Lmk_g42:\n"
"s_add_u32 s84, s84, s97\n"
"s_addc_u32 s85, s85, 0\n"
"v_mfma_f32_16x16x32_bf16 v[88:91], a[20:23], v[192:195], v[88:91]\n"
"ds_write_b128 v3, v[116:119] offset:11264\n"
"v_mfma_f32_16x16x32_bf16 v[88:91], a[24:27], v[196:199], v[88:91]\n"
"s_and_b32 s84, s84, -16\n"
"s_and_b32 s85, s85, 0xffff\n"
"s_sub_u32 s97, .Lmk_end-.Lmk_g42, s97\n"
"s_min_u32 s86, s97, 0x4000\n"
"s_mov_b32 s87, 0x20000\n"
"v_mfma_f32_16x16x32_bf16 v[88:91], a[28:31], v[200:203], v[88:91]\n"
"s_mov_b32 m0, 0x27800\n"
"s_nop 0\n"
"buffer_load_dwordx4 v0, s[84:87], s99 offen lds\n"
"v_mfma_f32_16x16x32_bf16 v[116:119], a[60:63], v[200:203], 0\n"
"buffer_load_dwordx4 a[140:143], v0, s[4:7], s98 offen offset:3072 sc1\n"
"v_mfma_f32_16x16x32_bf16 v[116:119], a[56:59], v[196:199], v[116:119]\n"
"v_sqrt_f32_e32 v254, v254\n"
"v_mfma_f32_16x16x32_bf16 v[116:119], a[52:55], v[192:195], v[116:119]\n"
"s_add_u32 s97, s52, 0x448000\n"
"buffer_load_dwordx4 v[128:131], v0, s[4:7], s97 offen sc1\n"
"v_mfma_f32_16x16x32_bf16 v[116:119], a[48:51], v[152:155], v[116:119]\n"
"s_waitcnt lgkmcnt(0)\n"
"s_barrier\n"
"v_mfma_f32_16x16x32_bf16 v[116:119], a[44:47], v[184:187], v[116:119]\n"
"v_cmp_u_f32_e32 vcc, v88, v88\n"
"v_cvt_pk_bf16_f32 v254, v254, 0\n"
"v_mfma_f32_16x16x32_bf16 v[116:119], a[40:43], v[148:151], v[116:119]\n"
"s_cbranch_vccnz .Lmk_p45s\n"
".Lmk_p45r:\n"
"v_lshlrev_b32_e32 v254, 16, v254\n"
"v_mfma_f32_16x16x32_bf16 v[116:119], a[36:39], v[108:111], v[116:119]\n"
"ds_write_b128 v3, v[88:91] offset:12288\n"
"v_mfma_f32_16x16x32_bf16 v[116:119], a[32:35], v[176:179], v[116:119]\n"
"buffer_load_dwordx4 v[88:91], v0, s[4:7], s97 offen offset:1024 sc1\n"
"v_mfma_f32_16x16x32_bf16 v[132:135], a[64:67], v[176:179], 0\n"
"v_rcp_f32_e32 v255, v254\n"
"v_mfma_f32_16x16x32_bf16 v[132:135], a[68:71], v[108:111], v[132:135]\n"
"ds_read_b128 v[136:139], v4\n"
"v_mul_f32_e32 v254, 0x3e8293ee, v255\n"
"v_mfma_f32_16x16x32_bf16 v[132:135], a[72:75], v[148:151], v[132:135]\n"
"ds_read_b128 v[140:143], v9 offset:640\n"
"ds_read_b128 v[144:147], v5\n"
"v_mfma_f32_16x16x32_bf16 v[132:135], a[76:79], v[184:187], v[132:135]\n"
"ds_read_b128 v[156:159], v4 offset:1024\n"
"ds_read_b128 v[160:163], v9 offset:704\n"
"v_mfma_f32_16x16x32_bf16 v[132:135], a[80:83], v[152:155], v[132:135]\n"
"buffer_load_dwordx4 v[164:167], v0, s[4:7], s97 offen offset:2048 sc1\n"
"v_mfma_f32_16x16x32_bf16 v[132:135], a[84:87], v[192:195], v[132:135]\n"
"ds_write_b128 v3, v[116:119] offset:13312\n"
"v_mfma_f32_16x16x32_bf16 v[132:135], a[88:91], v[196:199], v[132:135]\n"
"ds_read_b128 v[116:119], v5 offset:1024\n"
"v_mfma_f32_16x16x32_bf16 v[132:135], a[92:95], v[200:203], v[132:135]\n"
"s_waitcnt lgkmcnt(4)\n"
"v_fmac_f32_e32 v136, v253, v140\n"
"v_fmac_f32_e32 v137, v253, v141\n"
"v_mfma_f32_16x16x32_bf16 v[180:183], a[124:127], v[200:203], 0\n"
"v_fmac_f32_e32 v138, v253, v142\n"
"v_fmac_f32_e32 v139, v253, v143\n"
"v_mfma_f32_16x16x32_bf16 v[180:183], a[120:123], v[196:199], v[180:183]\n"
"buffer_load_dwordx4 v[140:143], v0, s[4:7], s97 offen offset:3072 sc1\n"
"v_mfma_f32_16x16x32_bf16 v[180:183], a[116:119], v[192:195], v[180:183]\n"
"ds_read_b128 v[188:191], v4 offset:2048\n"
"ds_read_b128 v[204:207], v9 offset:768\n"
"v_mfma_f32_16x16x32_bf16 v[180:183], a[112:115], v[152:155], v[180:183]\n"
"ds_read_b128 v[208:211], v5 offset:2048\n"
"v_add_f32_e32 v136, v136, v144\n"
"v_mfma_f32_16x16x32_bf16 v[180:183], a[108:111], v[184:187], v[180:183]\n"
"buffer_load_dwordx4 a[144:147], v1, s[4:7], s98 offen sc1\n"
"v_mfma_f32_16x16x32_bf16 v[180:183], a[104:107], v[148:151], v[180:183]\n"
"ds_write_b128 v3, v[132:135] offset:14336\n"
"v_mfma_f32_16x16x32_bf16 v[180:183], a[100:103], v[108:111], v[180:183]\n"
"buffer_load_dwordx4 v[132:135], v1, s[4:7], s97 offen sc1\n"
"v_mfma_f32_16x16x32_bf16 v[180:183], a[96:99], v[176:179], v[180:183]\n"
"v_add_f32_e32 v137, v137, v145\n"
"v_add_f32_e32 v138, v138, v146\n"
"v_mfma_f32_16x16x32_bf16 v[212:215], v[24:27], v[176:179], 0\n"
"s_waitcnt lgkmcnt(4)\n"
"v_add_f32_e32 v139, v139, v147\n"
"v_fmac_f32_e32 v156, v253, v160\n"
"v_mfma_f32_16x16x32_bf16 v[212:215], v[28:31], v[108:111], v[212:215]\n"
"ds_read_b128 v[144:147], v4 offset:3072\n"
"ds_read_b128 v[216:219], v9 offset:832\n"
"v_mfma_f32_16x16x32_bf16 v[212:215], v[32:35], v[148:151], v[212:215]\n"
"ds_read_b128 v[220:223], v5 offset:3072\n"
"v_fmac_f32_e32 v157, v253, v161\n"
"v_mfma_f32_16x16x32_bf16 v[212:215], v[36:39], v[184:187], v[212:215]\n"
"buffer_load_dwordx4 v[224:227], v1, s[4:7], s97 offen offset:1024 sc1\n"
"v_mfma_f32_16x16x32_bf16 v[212:215], v[40:43], v[152:155], v[212:215]\n"
"s_add_u32 s96, s60, 0x400000\n"
"v_fmac_f32_e32 v158, v253, v162\n"
"v_fmac_f32_e32 v159, v253, v163\n"
"v_mfma_f32_16x16x32_bf16 v[212:215], v[44:47], v[192:195], v[212:215]\n"
"ds_write_b128 v3, v[180:183] offset:15360\n"
"v_mfma_f32_16x16x32_bf16 v[212:215], v[48:51], v[196:199], v[212:215]\n"
"buffer_store_dwordx4 v[120:123], v0, s[4:7], s96 offen sc1\n"
"v_mfma_f32_16x16x32_bf16 v[212:215], v[52:55], v[200:203], v[212:215]\n"
"v_add_f32_e32 v156, v156, v116\n"
"v_add_f32_e32 v157, v157, v117\n"
"v_mfma_f32_16x16x32_bf16 v[160:163], v[84:87], v[200:203], 0\n"
"buffer_load_dwordx4 v[180:183], v1, s[4:7], s97 offen offset:2048 sc1\n"
"v_mfma_f32_16x16x32_bf16 v[160:163], v[80:83], v[196:199], v[160:163]\n"
"s_add_u32 s96, s68, s80\n"
"s_add_u32 s96, s96, 0x10000\n"
"s_waitcnt vmcnt(21)\n"
"v_mfma_f32_16x16x32_bf16 v[160:163], v[76:79], v[192:195], v[160:163]\n"
"buffer_store_dwordx2 v[18:19], v11, s[4:7], s96 offen sc1\n"
"v_mfma_f32_16x16x32_bf16 v[160:163], v[72:75], v[152:155], v[160:163]\n"
"ds_read_b128 v[152:155], v4 offset:4096\n"
"ds_read_b128 v[192:195], v9 offset:512\n"
"v_mfma_f32_16x16x32_bf16 v[160:163], v[68:71], v[184:187], v[160:163]\n"
"ds_read_b128 v[184:187], v5 offset:4096\n"
"v_add_f32_e32 v158, v158, v118\n"
"v_mfma_f32_16x16x32_bf16 v[160:163], v[64:67], v[148:151], v[160:163]\n"
"buffer_load_dwordx4 v[148:151], v1, s[4:7], s97 offen offset:3072 sc1\n"
"v_mfma_f32_16x16x32_bf16 v[160:163], v[60:63], v[108:111], v[160:163]\n"
"ds_write_b128 v3, v[212:215] offset:16384\n"
"v_mfma_f32_16x16x32_bf16 v[160:163], v[56:59], v[176:179], v[160:163]\n"
"s_waitcnt vmcnt(19)\n"
"ds_read_b128 v[108:111], v4 offset:5120\n"
"ds_read_b128 v[176:179], v9 offset:576\n"
"; @B G1 0.3\n"
"v_mfma_f32_16x16x32_bf16 v[196:199], a[0:3], v[92:95], v[136:139]\n"
"buffer_load_dwordx4 a[148:151], v1, s[4:7], s98 offen offset:1024 sc1\n"
"v_mfma_f32_16x16x32_bf16 v[196:199], a[4:7], v[96:99], v[196:199]\n"
"ds_read_b128 v[200:203], v5 offset:5120\n"
"v_add_f32_e32 v159, v159, v119\n"
"v_mfma_f32_16x16x32_bf16 v[196:199], a[8:11], v[104:107], v[196:199]\n"
"s_add_u32 s97, s53, 0x448000\n"
"buffer_load_dwordx4 v[116:119], v0, s[4:7], s97 offen sc1\n"
"v_mfma_f32_16x16x32_bf16 v[196:199], a[12:15], v[112:115], v[196:199]\n"
"s_waitcnt vmcnt(15)\n"
"s_waitcnt lgkmcnt(12)\n"
"v_fmac_f32_e32 v188, v253, v204\n"
"v_mfma_f32_16x16x32_bf16 v[196:199], a[16:19], v[100:103], v[196:199]\n"
"v_fmac_f32_e32 v189, v253, v205\n"
"v_fmac_f32_e32 v190, v253, v206\n"
"v_mfma_f32_16x16x32_bf16 v[196:199], a[20:23], v[124:127], v[196:199]\n"
"ds_write_b128 v3, v[160:163] offset:17408\n"
"v_mfma_f32_16x16x32_bf16 v[196:199], a[24:27], v[168:171], v[196:199]\n"
"v_fmac_f32_e32 v191, v253, v207\n"
"v_add_f32_e32 v188, v188, v208\n"
"v_mfma_f32_16x16x32_bf16 v[196:199], a[28:31], v[172:175], v[196:199]\n"
"buffer_load_dwordx4 v[160:163], v0, s[4:7], s97 offen offset:1024 sc1\n"
"v_mfma_f32_16x16x32_bf16 v[204:207], a[60:63], v[172:175], v[156:159]\n"
"v_add_f32_e32 v189, v189, v209\n"
"v_add_f32_e32 v190, v190, v210\n"
"v_mfma_f32_16x16x32_bf16 v[204:207], a[56:59], v[168:171], v[204:207]\n"
"s_waitcnt lgkmcnt(9)\n"
"v_add_f32_e32 v191, v191, v211\n"
"v_fmac_f32_e32 v144, v253, v216\n"
"v_mfma_f32_16x16x32_bf16 v[204:207], a[52:55], v[124:127], v[204:207]\n"
"s_waitcnt lgkmcnt(0)\n"
"s_barrier\n"
"v_mfma_f32_16x16x32_bf16 v[204:207], a[48:51], v[100:103], v[204:207]\n"
"ds_read_b128 v[208:211], v6\n"
"ds_read_b128 v[212:215], v6 offset:1024\n"
"v_mfma_f32_16x16x32_bf16 v[204:207], a[44:47], v[112:115], v[204:207]\n"
"v_cmp_u_f32_e32 vcc, v196, v196\n"
"v_fmac_f32_e32 v145, v253, v217\n"
"v_mfma_f32_16x16x32_bf16 v[204:207], a[40:43], v[104:107], v[204:207]\n"
"s_cbranch_vccnz .Lmk_p46s\n"
".Lmk_p46r:\n"
"v_fmac_f32_e32 v146, v253, v218\n"
"v_mfma_f32_16x16x32_bf16 v[204:207], a[36:39], v[96:99], v[204:207]\n"
"buffer_load_dwordx4 v[136:139], v0, s[4:7], s97 offen offset:2048 sc1\n"
"v_mfma_f32_16x16x32_bf16 v[204:207], a[32:35], v[92:95], v[204:207]\n"
"v_fmac_f32_e32 v147, v253, v219\n"
"v_add_f32_e32 v144, v144, v220\n"
"v_mfma_f32_16x16x32_bf16 v[188:191], a[64:67], v[92:95], v[188:191]\n"
"v_add_f32_e32 v145, v145, v221\n"
"v_add_f32_e32 v146, v146, v222\n"
"v_mfma_f32_16x16x32_bf16 v[188:191], a[68:71], v[96:99], v[188:191]\n"
"buffer_load_dwordx4 v[156:159], v0, s[4:7], s97 offen offset:3072 sc1\n"
"v_mfma_f32_16x16x32_bf16 v[188:191], a[72:75], v[104:107], v[188:191]\n"
"v_add_f32_e32 v147, v147, v223\n"
"v_fmac_f32_e32 v152, v253, v192\n"
"v_mfma_f32_16x16x32_bf16 v[188:191], a[76:79], v[112:115], v[188:191]\n"
"v_fmac_f32_e32 v153, v253, v193\n"
"v_fmac_f32_e32 v154, v253, v194\n"
"v_mfma_f32_16x16x32_bf16 v[188:191], a[80:83], v[100:103], v[188:191]\n"
"buffer_load_dwordx4 a[152:155], v1, s[4:7], s98 offen offset:2048 sc1\n"
"v_mfma_f32_16x16x32_bf16 v[188:191], a[84:87], v[124:127], v[188:191]\n"
"s_waitcnt lgkmcnt(0)\n"
"v_fmac_f32_e32 v155, v253, v195\n"
"v_add_f32_e32 v152, v152, v184\n"
"v_mfma_f32_16x16x32_bf16 v[188:191], a[88:91], v[168:171], v[188:191]\n"
"buffer_load_dwordx4 v[192:195], v1, s[4:7], s97 offen sc1\n"
"v_mfma_f32_16x16x32_bf16 v[188:191], a[92:95], v[172:175], v[188:191]\n"
"ds_read_b128 v[216:219], v6 offset:2048\n"
"ds_read_b128 v[220:223], v6 offset:3072\n"
"v_mfma_f32_16x16x32_bf16 v[144:147], a[124:127], v[172:175], v[144:147]\n"
"v_add_f32_e32 v153, v153, v185\n"
"v_add_f32_e32 v154, v154, v186\n"
"v_mfma_f32_16x16x32_bf16 v[144:147], a[120:123], v[168:171], v[144:147]\n"
"v_add_f32_e32 v155, v155, v187\n"
"v_fmac_f32_e32 v108, v253, v176\n"
"v_mfma_f32_16x16x32_bf16 v[144:147], a[116:119], v[124:127], v[144:147]\n"
"v_fmac_f32_e32 v109, v253, v177\n"
"v_fmac_f32_e32 v110, v253, v178\n"
"v_mfma_f32_16x16x32_bf16 v[144:147], a[112:115], v[100:103], v[144:147]\n"
"buffer_load_dwordx4 v[184:187], v1, s[4:7], s97 offen offset:1024 sc1\n"
"v_mfma_f32_16x16x32_bf16 v[144:147], a[108:111], v[112:115], v[144:147]\n"
"v_fmac_f32_e32 v111, v253, v179\n"
"v_add_f32_e32 v108, v108, v200\n"
"v_mfma_f32_16x16x32_bf16 v[144:147], a[104:107], v[104:107], v[144:147]\n"
"v_add_f32_e32 v109, v109, v201\n"
"v_add_f32_e32 v110, v110, v202\n"
"v_mfma_f32_16x16x32_bf16 v[144:147], a[100:103], v[96:99], v[144:147]\n"
"v_add_f32_e32 v111, v111, v203\n"
"v_add_f32_e32 v196, v196, v208\n"
"v_mfma_f32_16x16x32_bf16 v[144:147], a[96:99], v[92:95], v[144:147]\n"
"v_add_f32_e32 v197, v197, v209\n"
"v_add_f32_e32 v198, v198, v210\n"
"v_mfma_f32_16x16x32_bf16 v[152:155], v[24:27], v[92:95], v[152:155]\n"
"buffer_load_dwordx4 v[176:179], v1, s[4:7], s97 offen offset:2048 sc1\n"
"v_mfma_f32_16x16x32_bf16 v[152:155], v[28:31], v[96:99], v[152:155]\n"
"v_add_f32_e32 v199, v199, v211\n"
"v_add_f32_e32 v204, v204, v212\n"
"v_mfma_f32_16x16x32_bf16 v[152:155], v[32:35], v[104:107], v[152:155]\n"
"v_add_f32_e32 v205, v205, v213\n"
"v_add_f32_e32 v206, v206, v214\n"
"v_mfma_f32_16x16x32_bf16 v[152:155], v[36:39], v[112:115], v[152:155]\n"
"v_add_f32_e32 v207, v207, v215\n"
"v_mul_f32_e32 v196, v196, v255\n"
"v_mfma_f32_16x16x32_bf16 v[152:155], v[40:43], v[100:103], v[152:155]\n"
"s_waitcnt lgkmcnt(0)\n"
"v_mul_f32_e32 v197, v197, v255\n"
"v_mul_f32_e32 v198, v198, v255\n"
"v_mfma_f32_16x16x32_bf16 v[152:155], v[44:47], v[124:127], v[152:155]\n"
"buffer_load_dwordx4 v[200:203], v1, s[4:7], s97 offen offset:3072 sc1\n"
"v_mfma_f32_16x16x32_bf16 v[152:155], v[48:51], v[168:171], v[152:155]\n"
"s_add_u32 s97, s60, 0x408000\n"
"ds_read_b128 v[208:211], v6 offset:4096\n"
"ds_read_b128 v[212:215], v6 offset:5120\n"
"v_mfma_f32_16x16x32_bf16 v[152:155], v[52:55], v[172:175], v[152:155]\n"
"v_mul_f32_e32 v199, v199, v255\n"
"v_mul_f32_e32 v204, v204, v255\n"
"v_mfma_f32_16x16x32_bf16 v[108:111], v[84:87], v[172:175], v[108:111]\n"
"buffer_store_dwordx4 v[120:123], v0, s[4:7], s97 offen sc1\n"
"v_mfma_f32_16x16x32_bf16 v[108:111], v[80:83], v[168:171], v[108:111]\n"
"v_mul_f32_e32 v205, v205, v255\n"
"v_mul_f32_e32 v206, v206, v255\n"
"v_mfma_f32_16x16x32_bf16 v[108:111], v[76:79], v[124:127], v[108:111]\n"
"v_mul_f32_e32 v207, v207, v255\n"
"v_cvt_pk_bf16_f32 v120, v196, v197\n"
"v_mfma_f32_16x16x32_bf16 v[108:111], v[72:75], v[100:103], v[108:111]\n"
"v_cvt_pk_bf16_f32 v121, v198, v199\n"
"v_cvt_pk_bf16_f32 v122, v204, v205\n"
"v_mfma_f32_16x16x32_bf16 v[108:111], v[68:71], v[112:115], v[108:111]\n"
"v_cvt_pk_bf16_f32 v123, v206, v207\n"
"v_add_f32_e32 v188, v188, v216\n"
"v_mfma_f32_16x16x32_bf16 v[108:111], v[64:67], v[104:107], v[108:111]\n"
"ds_write_b128 v7, v[120:123] offset:24576\n"
"v_mfma_f32_16x16x32_bf16 v[108:111], v[60:63], v[96:99], v[108:111]\n"
"v_add_f32_e32 v189, v189, v217\n"
"v_add_f32_e32 v190, v190, v218\n"
"v_mfma_f32_16x16x32_bf16 v[108:111], v[56:59], v[92:95], v[108:111]\n"
"v_add_f32_e32 v191, v191, v219\n"
"v_add_f32_e32 v144, v144, v220\n"
"v_add_f32_e32 v145, v145, v221\n"
"v_add_f32_e32 v146, v146, v222\n"
"v_add_f32_e32 v147, v147, v223\n"
"v_mul_f32_e32 v188, v188, v255\n"
"v_mul_f32_e32 v189, v189, v255\n"
"v_mul_f32_e32 v190, v190, v255\n"
"v_mul_f32_e32 v191, v191, v255\n"
"v_mul_f32_e32 v144, v144, v255\n"
"v_mul_f32_e32 v145, v145, v255\n"
"v_mul_f32_e32 v146, v146, v255\n"
"v_mul_f32_e32 v147, v147, v255\n"
"v_cvt_pk_bf16_f32 v92, v188, v189\n"
"v_cvt_pk_bf16_f32 v93, v190, v191\n"
"v_cvt_pk_bf16_f32 v94, v144, v145\n"
"v_cvt_pk_bf16_f32 v95, v146, v147\n"
"ds_write_b128 v7, v[92:95] offset:57344\n"
".Lmk_b1:\n"
"s_waitcnt vmcnt(18)\n"
"; @B G1 1.0\n"
"v_mfma_f32_16x16x32_bf16 v[92:95], a[0:3], v[128:131], 0\n"
"v_mfma_f32_16x16x32_bf16 v[92:95], a[4:7], v[88:91], v[92:95]\n"
"s_add_u32 s97, s62, 0x44800\n"
"v_mfma_f32_16x16x32_bf16 v[92:95], a[8:11], v[164:167], v[92:95]\n"
"s_add_u32 s96, s54, 0x448000\n"
"buffer_load_dwordx4 v[96:99], v0, s[4:7], s96 offen sc1\n"
"v_mfma_f32_16x16x32_bf16 v[92:95], a[12:15], v[140:143], v[92:95]\n"
"s_waitcnt vmcnt(12)\n"
"v_mfma_f32_16x16x32_bf16 v[92:95], a[16:19], v[132:135], v[92:95]\n"
"buffer_load_dword v255, v10, s[4:7], s97 offen sc1\n"
"v_mfma_f32_16x16x32_bf16 v[92:95], a[20:23], v[224:227], v[92:95]\n"
"s_waitcnt lgkmcnt(2)\n"
"v_add_f32_e32 v152, v152, v208\n"
"v_add_f32_e32 v153, v153, v209\n"
"v_mfma_f32_16x16x32_bf16 v[92:95], a[24:27], v[180:183], v[92:95]\n"
"v_add_f32_e32 v154, v154, v210\n"
"v_add_f32_e32 v155, v155, v211\n"
"v_mfma_f32_16x16x32_bf16 v[92:95], a[28:31], v[148:151], v[92:95]\n"
"buffer_load_dwordx4 v[100:103], v0, s[4:7], s96 offen offset:1024 sc1\n"
"v_mfma_f32_16x16x32_bf16 v[104:107], a[60:63], v[148:151], 0\n"
"buffer_load_dwordx4 a[156:159], v1, s[4:7], s98 offen offset:3072 sc1\n"
"v_mfma_f32_16x16x32_bf16 v[104:107], a[56:59], v[180:183], v[104:107]\n"
"buffer_load_dword v253, v10, s[4:7], s97 offen offset:256 sc1\n"
"v_mfma_f32_16x16x32_bf16 v[104:107], a[52:55], v[224:227], v[104:107]\n"
"v_add_f32_e32 v108, v108, v212\n"
"v_add_f32_e32 v109, v109, v213\n"
"v_mfma_f32_16x16x32_bf16 v[104:107], a[48:51], v[132:135], v[104:107]\n"
"v_add_f32_e32 v110, v110, v214\n"
"v_add_f32_e32 v111, v111, v215\n"
"v_mfma_f32_16x16x32_bf16 v[104:107], a[44:47], v[140:143], v[104:107]\n"
"v_cmp_u_f32_e32 vcc, v92, v92\n"
"v_mul_f32_e32 v152, v152, v254\n"
"v_mfma_f32_16x16x32_bf16 v[104:107], a[40:43], v[164:167], v[104:107]\n"
"s_cbranch_vccnz .Lmk_p48s\n"
".Lmk_p48r:\n"
"v_mul_f32_e32 v153, v153, v254\n"
"v_mfma_f32_16x16x32_bf16 v[104:107], a[36:39], v[88:91], v[104:107]\n"
"buffer_load_dwordx4 v[112:115], v0, s[4:7], s96 offen offset:2048 sc1\n"
"v_mfma_f32_16x16x32_bf16 v[104:107], a[32:35], v[128:131], v[104:107]\n"
"ds_write_b128 v3, v[92:95]\n"
"v_mfma_f32_16x16x32_bf16 v[92:95], a[64:67], v[128:131], 0\n"
"buffer_load_dword v252, v10, s[4:7], s97 offen offset:512 sc1\n"
"v_mfma_f32_16x16x32_bf16 v[92:95], a[68:71], v[88:91], v[92:95]\n"
"buffer_load_dwordx4 v[120:123], v0, s[4:7], s96 offen offset:3072 sc1\n"
"v_mfma_f32_16x16x32_bf16 v[92:95], a[72:75], v[164:167], v[92:95]\n"
"v_mul_f32_e32 v154, v154, v254\n"
"v_mul_f32_e32 v155, v155, v254\n"
"v_mfma_f32_16x16x32_bf16 v[92:95], a[76:79], v[140:143], v[92:95]\n"
"buffer_load_dword v251, v10, s[4:7], s97 offen offset:768 sc1\n"
"v_mfma_f32_16x16x32_bf16 v[92:95], a[80:83], v[132:135], v[92:95]\n"
"v_mul_f32_e32 v108, v108, v254\n"
"v_mul_f32_e32 v109, v109, v254\n"
"v_mfma_f32_16x16x32_bf16 v[92:95], a[84:87], v[224:227], v[92:95]\n"
"ds_write_b128 v3, v[104:107] offset:1024\n"
"v_mfma_f32_16x16x32_bf16 v[92:95], a[88:91], v[180:183], v[92:95]\n"
"buffer_load_dwordx4 v[104:107], v1, s[4:7], s96 offen sc1\n"
"v_mfma_f32_16x16x32_bf16 v[92:95], a[92:95], v[148:151], v[92:95]\n"
"buffer_load_dword v250, v10, s[4:7], s97 offen offset:1024 sc1\n"
"v_mfma_f32_16x16x32_bf16 v[124:127], a[124:127], v[148:151], 0\n"
"v_mul_f32_e32 v110, v110, v254\n"
"v_mul_f32_e32 v111, v111, v254\n"
"v_mfma_f32_16x16x32_bf16 v[124:127], a[120:123], v[180:183], v[124:127]\n"
"v_cvt_pk_bf16_f32 v144, v152, v153\n"
"v_cvt_pk_bf16_f32 v145, v154, v155\n"
"v_mfma_f32_16x16x32_bf16 v[124:127], a[116:119], v[224:227], v[124:127]\n"
"s_add_u32 s98, s70, 0x202000\n"
"buffer_load_dword v254, v10, s[4:7], s97 offen offset:1280 sc1\n"
"v_mfma_f32_16x16x32_bf16 v[124:127], a[112:115], v[132:135], v[124:127]\n"
"buffer_load_dwordx4 v[152:155], v1, s[4:7], s96 offen offset:1024 sc1\n"
"v_mfma_f32_16x16x32_bf16 v[124:127], a[108:111], v[140:143], v[124:127]\n"
"buffer_load_dwordx4 a[160:163], v0, s[4:7], s98 offen sc1\n"
"v_mfma_f32_16x16x32_bf16 v[124:127], a[104:107], v[164:167], v[124:127]\n"
"ds_write_b128 v3, v[92:95] offset:2048\n"
"v_mfma_f32_16x16x32_bf16 v[124:127], a[100:103], v[88:91], v[124:127]\n"
"buffer_load_dword v249, v10, s[4:7], s97 offen offset:1536 sc1\n"
"v_mfma_f32_16x16x32_bf16 v[124:127], a[96:99], v[128:131], v[124:127]\n"
"v_cvt_pk_bf16_f32 v146, v108, v109\n"
"v_cvt_pk_bf16_f32 v147, v110, v111\n"
"v_mfma_f32_16x16x32_bf16 v[92:95], v[24:27], v[128:131], 0\n"
"buffer_load_dwordx4 v[108:111], v1, s[4:7], s96 offen offset:2048 sc1\n"
"v_mfma_f32_16x16x32_bf16 v[92:95], v[28:31], v[88:91], v[92:95]\n"
"buffer_load_dword v248, v10, s[4:7], s97 offen offset:1792 sc1\n"
"v_mfma_f32_16x16x32_bf16 v[92:95], v[32:35], v[164:167], v[92:95]\n"
"ds_write_b128 v2, v[144:147] offset:49152\n"
"v_mfma_f32_16x16x32_bf16 v[92:95], v[36:39], v[140:143], v[92:95]\n"
"v_mfma_f32_16x16x32_bf16 v[92:95], v[40:43], v[132:135], v[92:95]\n"
"v_mfma_f32_16x16x32_bf16 v[92:95], v[44:47], v[224:227], v[92:95]\n"
"buffer_load_dwordx4 v[144:147], v1, s[4:7], s96 offen offset:3072 sc1\n"
"v_mfma_f32_16x16x32_bf16 v[92:95], v[48:51], v[180:183], v[92:95]\n"
"ds_write_b128 v3, v[124:127] offset:3072\n"
"v_mfma_f32_16x16x32_bf16 v[92:95], v[52:55], v[148:151], v[92:95]\n"
"v_mfma_f32_16x16x32_bf16 v[124:127], v[84:87], v[148:151], 0\n"
"v_mfma_f32_16x16x32_bf16 v[124:127], v[80:83], v[180:183], v[124:127]\n"
"v_mfma_f32_16x16x32_bf16 v[124:127], v[76:79], v[224:227], v[124:127]\n"
"v_mfma_f32_16x16x32_bf16 v[124:127], v[72:75], v[132:135], v[124:127]\n"
"v_mfma_f32_16x16x32_bf16 v[124:127], v[68:71], v[140:143], v[124:127]\n"
"v_mfma_f32_16x16x32_bf16 v[124:127], v[64:67], v[164:167], v[124:127]\n"
"s_nop 1\n"
"ds_write_b128 v3, v[92:95] offset:4096\n"
"v_mfma_f32_16x16x32_bf16 v[124:127], v[60:63], v[88:91], v[124:127]\n"
"v_mfma_f32_16x16x32_bf16 v[124:127], v[56:59], v[128:131], v[124:127]\n"
"s_waitcnt vmcnt(24)\n"
"; @B G1 1.1\n"
"v_mfma_f32_16x16x32_bf16 v[88:91], a[0:3], v[116:119], 0\n"
"buffer_load_dwordx4 a[164:167], v0, s[4:7], s98 offen offset:1024 sc1\n"
"v_mfma_f32_16x16x32_bf16 v[88:91], a[4:7], v[160:163], v[88:91]\n"
"v_mfma_f32_16x16x32_bf16 v[88:91], a[8:11], v[136:139], v[88:91]\n"
"s_add_u32 s97, s55, 0x448000\n"
"buffer_load_dwordx4 v[92:95], v0, s[4:7], s97 offen sc1\n"
"v_mfma_f32_16x16x32_bf16 v[88:91], a[12:15], v[156:159], v[88:91]\n"
"s_waitcnt vmcnt(21)\n"
"v_mfma_f32_16x16x32_bf16 v[88:91], a[16:19], v[192:195], v[88:91]\n"
"v_mfma_f32_16x16x32_bf16 v[88:91], a[20:23], v[184:187], v[88:91]\n"
"ds_write_b128 v3, v[124:127] offset:5120\n"
"v_mfma_f32_16x16x32_bf16 v[88:91], a[24:27], v[176:179], v[88:91]\n"
"v_mfma_f32_16x16x32_bf16 v[88:91], a[28:31], v[200:203], v[88:91]\n"
"buffer_load_dwordx4 v[124:127], v0, s[4:7], s97 offen offset:1024 sc1\n"
"v_mfma_f32_16x16x32_bf16 v[128:131], a[60:63], v[200:203], 0\n"
"v_mfma_f32_16x16x32_bf16 v[128:131], a[56:59], v[176:179], v[128:131]\n"
"v_mfma_f32_16x16x32_bf16 v[128:131], a[52:55], v[184:187], v[128:131]\n"
"v_mfma_f32_16x16x32_bf16 v[128:131], a[48:51], v[192:195], v[128:131]\n"
"v_mfma_f32_16x16x32_bf16 v[128:131], a[44:47], v[156:159], v[128:131]\n"
"s_add_u32 s96, s67, 0x10000\n"
"s_nop 0\n"
"v_cmp_u_f32_e32 vcc, v88, v88\n"
"v_mfma_f32_16x16x32_bf16 v[128:131], a[40:43], v[136:139], v[128:131]\n"
"s_cbranch_vccnz .Lmk_p49s\n"
".Lmk_p49r:\n"
"v_mfma_f32_16x16x32_bf16 v[128:131], a[36:39], v[160:163], v[128:131]\n"
"buffer_load_dwordx4 v[132:135], v0, s[4:7], s97 offen offset:2048 sc1\n"
"v_mfma_f32_16x16x32_bf16 v[128:131], a[32:35], v[116:119], v[128:131]\n"
"ds_write_b128 v3, v[88:91] offset:6144\n"
"v_mfma_f32_16x16x32_bf16 v[88:91], a[64:67], v[116:119], 0\n"
"buffer_load_dwordx2 v[246:247], v12, s[4:7], s96 offen sc1\n"
"v_mfma_f32_16x16x32_bf16 v[88:91], a[68:71], v[160:163], v[88:91]\n"
"buffer_load_dwordx4 v[140:143], v0, s[4:7], s97 offen offset:3072 sc1\n"
"v_mfma_f32_16x16x32_bf16 v[88:91], a[72:75], v[136:139], v[88:91]\n"
"v_mfma_f32_16x16x32_bf16 v[88:91], a[76:79], v[156:159], v[88:91]\n"
"v_mfma_f32_16x16x32_bf16 v[88:91], a[80:83], v[192:195], v[88:91]\n"
"v_mov_b32_e32 v148, -1\n"
"v_mov_b32_e32 v149, -1\n"
"v_mfma_f32_16x16x32_bf16 v[88:91], a[84:87], v[184:187], v[88:91]\n"
"ds_write_b128 v3, v[128:131] offset:7168\n"
"v_mfma_f32_16x16x32_bf16 v[88:91], a[88:91], v[176:179], v[88:91]\n"
"buffer_load_dwordx4 v[128:131], v1, s[4:7], s97 offen sc1\n"
"v_mfma_f32_16x16x32_bf16 v[88:91], a[92:95], v[200:203], v[88:91]\n"
"v_mov_b32_e32 v150, -1\n"
"v_mov_b32_e32 v151, -1\n"
"v_mfma_f32_16x16x32_bf16 v[164:167], a[124:127], v[200:203], 0\n"
"s_add_u32 s95, s61, 0x840000\n"
"buffer_store_dwordx4 v[148:151], v0, s[8:11], s95 offen sc1\n"
"v_mfma_f32_16x16x32_bf16 v[164:167], a[120:123], v[176:179], v[164:167]\n"
"buffer_load_dwordx4 a[168:171], v0, s[4:7], s98 offen offset:2048 sc1\n"
"v_mfma_f32_16x16x32_bf16 v[164:167], a[116:119], v[184:187], v[164:167]\n"
"v_mfma_f32_16x16x32_bf16 v[164:167], a[112:115], v[192:195], v[164:167]\n"
"buffer_load_dwordx4 v[168:171], v1, s[4:7], s97 offen offset:1024 sc1\n"
"v_mfma_f32_16x16x32_bf16 v[164:167], a[108:111], v[156:159], v[164:167]\n"
"v_mfma_f32_16x16x32_bf16 v[164:167], a[104:107], v[136:139], v[164:167]\n"
"ds_write_b128 v3, v[88:91] offset:8192\n"
"v_mfma_f32_16x16x32_bf16 v[164:167], a[100:103], v[160:163], v[164:167]\n"
"v_mfma_f32_16x16x32_bf16 v[164:167], a[96:99], v[116:119], v[164:167]\n"
"v_mfma_f32_16x16x32_bf16 v[88:91], v[24:27], v[116:119], 0\n"
"buffer_load_dwordx4 v[172:175], v1, s[4:7], s97 offen offset:2048 sc1\n"
"v_mfma_f32_16x16x32_bf16 v[88:91], v[28:31], v[160:163], v[88:91]\n"
"v_mfma_f32_16x16x32_bf16 v[88:91], v[32:35], v[136:139], v[88:91]\n"
"v_mfma_f32_16x16x32_bf16 v[88:91], v[36:39], v[156:159], v[88:91]\n"
"v_mfma_f32_16x16x32_bf16 v[88:91], v[40:43], v[192:195], v[88:91]\n"
"s_waitcnt vmcnt(12)\n"
"v_add_f32_e32 v255, v255, v250\n"
"v_add_f32_e32 v253, v253, v254\n"
"v_mfma_f32_16x16x32_bf16 v[88:91], v[44:47], v[184:187], v[88:91]\n"
"buffer_load_dwordx4 v[180:183], v1, s[4:7], s97 offen offset:3072 sc1\n"
"v_mfma_f32_16x16x32_bf16 v[88:91], v[48:51], v[176:179], v[88:91]\n"
"ds_write_b128 v3, v[164:167] offset:9216\n"
"v_mfma_f32_16x16x32_bf16 v[88:91], v[52:55], v[200:203], v[88:91]\n"
"v_add_f32_e32 v252, v252, v249\n"
"v_add_f32_e32 v251, v251, v248\n"
"v_mfma_f32_16x16x32_bf16 v[164:167], v[84:87], v[200:203], 0\n"
"v_add_f32_e32 v255, v255, v252\n"
"v_add_f32_e32 v253, v253, v251\n"
"v_mfma_f32_16x16x32_bf16 v[164:167], v[80:83], v[176:179], v[164:167]\n"
"v_add_f32_e32 v255, v255, v253\n"
"v_cmp_u_f32_e32 vcc, v255, v255\n"
"v_mfma_f32_16x16x32_bf16 v[164:167], v[76:79], v[184:187], v[164:167]\n"
"s_cbranch_vccnz .Lmk_m52s\n"
".Lmk_m52r:\n"
"v_mov_b32_e32 v253, v255\n"
"v_mfma_f32_16x16x32_bf16 v[164:167], v[72:75], v[192:195], v[164:167]\n"
"s_nop 0\n"
"v_permlane32_swap_b32_e32 v255, v253\n"
"v_add_f32_e32 v255, v255, v253\n"
"v_mfma_f32_16x16x32_bf16 v[164:167], v[68:71], v[156:159], v[164:167]\n"
"v_mul_f32_e32 v255, 0x3a800000, v255\n"
"v_mov_b32_e32 v253, v255\n"
"v_mfma_f32_16x16x32_bf16 v[164:167], v[64:67], v[136:139], v[164:167]\n"
"ds_write_b128 v3, v[88:91] offset:10240\n"
"v_mfma_f32_16x16x32_bf16 v[164:167], v[60:63], v[160:163], v[164:167]\n"
"buffer_load_dwordx4 a[172:175], v0, s[4:7], s98 offen offset:3072 sc1\n"
"v_mfma_f32_16x16x32_bf16 v[164:167], v[56:59], v[116:119], v[164:167]\n"
"v_permlane16_swap_b32_e32 v255, v253\n"
"v_cvt_pk_bf16_f32 v254, -v255, 0\n"
"; @B G1 1.2\n"
"v_mfma_f32_16x16x32_bf16 v[88:91], a[0:3], v[96:99], 0\n"
"v_fma_f32 v253, -v255, v255, v253\n"
"v_lshlrev_b32_e32 v254, 16, v254\n"
"v_mfma_f32_16x16x32_bf16 v[88:91], a[4:7], v[100:103], v[88:91]\n"
"s_waitcnt vmcnt(8)\n"
"v_cmp_ne_u64_e32 vcc, v[246:247], v[18:19]\n"
"v_mfma_f32_16x16x32_bf16 v[88:91], a[8:11], v[112:115], v[88:91]\n"
"s_cbranch_vccnz .Lmk_f39s\n"
".Lmk_f39r:\n"
"v_max_f32_e32 v253, 0, v253\n"
"v_mfma_f32_16x16x32_bf16 v[88:91], a[12:15], v[120:123], v[88:91]\n"
"v_cvt_pk_bf16_f32 v253, v253, 0\n"
"v_lshlrev_b32_e32 v253, 16, v253\n"
"v_mfma_f32_16x16x32_bf16 v[88:91], a[16:19], v[104:107], v[88:91]\n"
"v_add_f32_e32 v253, 0x3727c5ac, v253\n"
"v_cvt_pk_bf16_f32 v253, v253, 0\n"
"v_mfma_f32_16x16x32_bf16 v[88:91], a[20:23], v[152:155], v[88:91]\n"
"ds_write_b128 v3, v[164:167] offset:11264\n"
"v_mfma_f32_16x16x32_bf16 v[88:91], a[24:27], v[108:111], v[88:91]\n"
"v_lshlrev_b32_e32 v253, 16, v253\n"
"v_mfma_f32_16x16x32_bf16 v[88:91], a[28:31], v[144:147], v[88:91]\n"
"v_sqrt_f32_e32 v253, v253\n"
"v_mfma_f32_16x16x32_bf16 v[116:119], a[60:63], v[144:147], 0\n"
"v_cvt_pk_bf16_f32 v253, v253, 0\n"
"v_lshlrev_b32_e32 v253, 16, v253\n"
"v_mfma_f32_16x16x32_bf16 v[116:119], a[56:59], v[108:111], v[116:119]\n"
"v_rcp_f32_e32 v255, v253\n"
"v_mfma_f32_16x16x32_bf16 v[116:119], a[52:55], v[152:155], v[116:119]\n"
"s_add_u32 s97, s52, 0x408000\n"
"buffer_load_dwordx4 v[136:139], v0, s[4:7], s97 offen sc1\n"
"v_mfma_f32_16x16x32_bf16 v[116:119], a[48:51], v[104:107], v[116:119]\n"
"s_waitcnt lgkmcnt(0)\n"
"s_barrier\n"
"v_mfma_f32_16x16x32_bf16 v[116:119], a[44:47], v[120:123], v[116:119]\n"
"v_cmp_u_f32_e32 vcc, v88, v88\n"
"v_mul_f32_e32 v253, 0x3e8293ee, v255\n"
"v_mfma_f32_16x16x32_bf16 v[116:119], a[40:43], v[112:115], v[116:119]\n"
"s_cbranch_vccnz .Lmk_p50s\n"
".Lmk_p50r:\n"
"ds_read_b128 v[156:159], v4\n"
"v_mfma_f32_16x16x32_bf16 v[116:119], a[36:39], v[100:103], v[116:119]\n"
"ds_write_b128 v3, v[88:91] offset:12288\n"
"v_mfma_f32_16x16x32_bf16 v[116:119], a[32:35], v[96:99], v[116:119]\n"
"buffer_load_dwordx4 v[88:91], v0, s[4:7], s97 offen offset:1024 sc1\n"
"v_mfma_f32_16x16x32_bf16 v[160:163], a[64:67], v[96:99], 0\n"
"ds_read_b128 v[164:167], v9 offset:640\n"
"ds_read_b128 v[176:179], v5\n"
"v_mfma_f32_16x16x32_bf16 v[160:163], a[68:71], v[100:103], v[160:163]\n"
"v_mfma_f32_16x16x32_bf16 v[160:163], a[72:75], v[112:115], v[160:163]\n"
"buffer_load_dwordx4 a[176:179], v1, s[4:7], s98 offen sc1\n"
"v_mfma_f32_16x16x32_bf16 v[160:163], a[76:79], v[120:123], v[160:163]\n"
"ds_read_b128 v[184:187], v4 offset:1024\n"
"ds_read_b128 v[188:191], v9 offset:704\n"
"v_mfma_f32_16x16x32_bf16 v[160:163], a[80:83], v[104:107], v[160:163]\n"
"buffer_load_dwordx4 v[192:195], v0, s[4:7], s97 offen offset:2048 sc1\n"
"v_mfma_f32_16x16x32_bf16 v[160:163], a[84:87], v[152:155], v[160:163]\n"
"ds_write_b128 v3, v[116:119] offset:13312\n"
"v_mfma_f32_16x16x32_bf16 v[160:163], a[88:91], v[108:111], v[160:163]\n"
"ds_read_b128 v[116:119], v5 offset:1024\n"
"s_waitcnt lgkmcnt(4)\n"
"v_mfma_f32_16x16x32_bf16 v[160:163], a[92:95], v[144:147], v[160:163]\n"
"v_fmac_f32_e32 v156, v254, v164\n"
"v_fmac_f32_e32 v157, v254, v165\n"
"v_mfma_f32_16x16x32_bf16 v[196:199], a[124:127], v[144:147], 0\n"
"v_fmac_f32_e32 v158, v254, v166\n"
"v_fmac_f32_e32 v159, v254, v167\n"
"v_mfma_f32_16x16x32_bf16 v[196:199], a[120:123], v[108:111], v[196:199]\n"
"buffer_load_dwordx4 v[164:167], v0, s[4:7], s97 offen offset:3072 sc1\n"
"v_mfma_f32_16x16x32_bf16 v[196:199], a[116:119], v[152:155], v[196:199]\n"
"ds_read_b128 v[200:203], v4 offset:2048\n"
"ds_read_b128 v[204:207], v9 offset:768\n"
"v_mfma_f32_16x16x32_bf16 v[196:199], a[112:115], v[104:107], v[196:199]\n"
"ds_read_b128 v[208:211], v5 offset:2048\n"
"v_add_f32_e32 v156, v156, v176\n"
"v_mfma_f32_16x16x32_bf16 v[196:199], a[108:111], v[120:123], v[196:199]\n"
"v_add_f32_e32 v157, v157, v177\n"
"v_add_f32_e32 v158, v158, v178\n"
"v_mfma_f32_16x16x32_bf16 v[196:199], a[104:107], v[112:115], v[196:199]\n"
"ds_write_b128 v3, v[160:163] offset:14336\n"
"v_mfma_f32_16x16x32_bf16 v[196:199], a[100:103], v[100:103], v[196:199]\n"
"buffer_load_dwordx4 v[160:163], v1, s[4:7], s97 offen sc1\n"
"v_mfma_f32_16x16x32_bf16 v[196:199], a[96:99], v[96:99], v[196:199]\n"
"s_add_u32 s96, s61, 0x840000\n"
"v_add_f32_e32 v159, v159, v179\n"
"s_waitcnt lgkmcnt(4)\n"
"v_mfma_f32_16x16x32_bf16 v[176:179], v[24:27], v[96:99], 0\n"
"buffer_store_dwordx4 v[148:151], v0, s[8:11], s96 offen offset:1024 sc1\n"
"v_mfma_f32_16x16x32_bf16 v[176:179], v[28:31], v[100:103], v[176:179]\n"
"ds_read_b128 v[212:215], v4 offset:3072\n"
"ds_read_b128 v[216:219], v9 offset:832\n"
"v_mfma_f32_16x16x32_bf16 v[176:179], v[32:35], v[112:115], v[176:179]\n"
"ds_read_b128 v[220:223], v5 offset:3072\n"
"v_fmac_f32_e32 v184, v254, v188\n"
"v_mfma_f32_16x16x32_bf16 v[176:179], v[36:39], v[120:123], v[176:179]\n"
"buffer_load_dwordx4 v[224:227], v1, s[4:7], s97 offen offset:1024 sc1\n"
"v_mfma_f32_16x16x32_bf16 v[176:179], v[40:43], v[104:107], v[176:179]\n"
"v_fmac_f32_e32 v185, v254, v189\n"
"v_fmac_f32_e32 v186, v254, v190\n"
"v_mfma_f32_16x16x32_bf16 v[176:179], v[44:47], v[152:155], v[176:179]\n"
"ds_write_b128 v3, v[196:199] offset:15360\n"
"v_mfma_f32_16x16x32_bf16 v[176:179], v[48:51], v[108:111], v[176:179]\n"
"buffer_load_dwordx4 a[180:183], v1, s[4:7], s98 offen offset:1024 sc1\n"
"v_mfma_f32_16x16x32_bf16 v[176:179], v[52:55], v[144:147], v[176:179]\n"
"v_fmac_f32_e32 v187, v254, v191\n"
"v_add_f32_e32 v184, v184, v116\n"
"v_mfma_f32_16x16x32_bf16 v[188:191], v[84:87], v[144:147], 0\n"
"buffer_load_dwordx4 v[144:147], v1, s[4:7], s97 offen offset:2048 sc1\n"
"v_mfma_f32_16x16x32_bf16 v[188:191], v[80:83], v[108:111], v[188:191]\n"
"ds_read_b128 v[108:111], v4 offset:4096\n"
"ds_read_b128 v[196:199], v9 offset:512\n"
"v_mfma_f32_16x16x32_bf16 v[188:191], v[76:79], v[152:155], v[188:191]\n"
"ds_read_b128 v[152:155], v5 offset:4096\n"
"v_add_f32_e32 v185, v185, v117\n"
"v_mfma_f32_16x16x32_bf16 v[188:191], v[72:75], v[104:107], v[188:191]\n"
"s_waitcnt lgkmcnt(8)\n"
"v_add_f32_e32 v186, v186, v118\n"
"v_add_f32_e32 v187, v187, v119\n"
"v_mfma_f32_16x16x32_bf16 v[188:191], v[68:71], v[120:123], v[188:191]\n"
"v_fmac_f32_e32 v200, v254, v204\n"
"v_fmac_f32_e32 v201, v254, v205\n"
"v_mfma_f32_16x16x32_bf16 v[188:191], v[64:67], v[112:115], v[188:191]\n"
"buffer_load_dwordx4 v[104:107], v1, s[4:7], s97 offen offset:3072 sc1\n"
"v_mfma_f32_16x16x32_bf16 v[188:191], v[60:63], v[100:103], v[188:191]\n"
"ds_write_b128 v3, v[176:179] offset:16384\n"
"v_mfma_f32_16x16x32_bf16 v[188:191], v[56:59], v[96:99], v[188:191]\n"
"s_waitcnt vmcnt(18)\n"
"ds_read_b128 v[96:99], v4 offset:5120\n"
"ds_read_b128 v[100:103], v9 offset:576\n"
"; @B G1 1.3\n"
"v_mfma_f32_16x16x32_bf16 v[112:115], a[0:3], v[92:95], v[156:159]\n"
"ds_read_b128 v[116:119], v5 offset:5120\n"
"v_fmac_f32_e32 v202, v254, v206\n"
"v_mfma_f32_16x16x32_bf16 v[112:115], a[4:7], v[124:127], v[112:115]\n"
"v_fmac_f32_e32 v203, v254, v207\n"
"v_add_f32_e32 v200, v200, v208\n"
"v_mfma_f32_16x16x32_bf16 v[112:115], a[8:11], v[132:135], v[112:115]\n"
"s_add_u32 s97, s53, 0x408000\n"
"buffer_load_dwordx4 v[120:123], v0, s[4:7], s97 offen sc1\n"
"v_mfma_f32_16x16x32_bf16 v[112:115], a[12:15], v[140:143], v[112:115]\n"
"s_waitcnt vmcnt(13)\n"
"v_add_f32_e32 v201, v201, v209\n"
"v_add_f32_e32 v202, v202, v210\n"
"v_mfma_f32_16x16x32_bf16 v[112:115], a[16:19], v[128:131], v[112:115]\n"
"s_waitcnt lgkmcnt(8)\n"
"v_add_f32_e32 v203, v203, v211\n"
"v_fmac_f32_e32 v212, v254, v216\n"
"v_mfma_f32_16x16x32_bf16 v[112:115], a[20:23], v[168:171], v[112:115]\n"
"ds_write_b128 v3, v[188:191] offset:17408\n"
"v_mfma_f32_16x16x32_bf16 v[112:115], a[24:27], v[172:175], v[112:115]\n"
"v_fmac_f32_e32 v213, v254, v217\n"
"v_fmac_f32_e32 v214, v254, v218\n"
"v_mfma_f32_16x16x32_bf16 v[112:115], a[28:31], v[180:183], v[112:115]\n"
"buffer_load_dwordx4 v[176:179], v0, s[4:7], s97 offen offset:1024 sc1\n"
"v_mfma_f32_16x16x32_bf16 v[188:191], a[60:63], v[180:183], v[184:187]\n"
"v_fmac_f32_e32 v215, v254, v219\n"
"v_add_f32_e32 v212, v212, v220\n"
"v_mfma_f32_16x16x32_bf16 v[188:191], a[56:59], v[172:175], v[188:191]\n"
"v_add_f32_e32 v213, v213, v221\n"
"v_add_f32_e32 v214, v214, v222\n"
"v_mfma_f32_16x16x32_bf16 v[188:191], a[52:55], v[168:171], v[188:191]\n"
"s_waitcnt lgkmcnt(0)\n"
"s_barrier\n"
"v_mfma_f32_16x16x32_bf16 v[188:191], a[48:51], v[128:131], v[188:191]\n"
"buffer_load_dwordx4 a[184:187], v1, s[4:7], s98 offen offset:2048 sc1\n"
"v_mfma_f32_16x16x32_bf16 v[188:191], a[44:47], v[140:143], v[188:191]\n"
"ds_read_b128 v[204:207], v6\n"
"v_cmp_u_f32_e32 vcc, v112, v112\n"
"v_mfma_f32_16x16x32_bf16 v[188:191], a[40:43], v[132:135], v[188:191]\n"
"s_cbranch_vccnz .Lmk_p51s\n"
".Lmk_p51r:\n"
"ds_read_b128 v[156:159], v6 offset:1024\n"
"v_mfma_f32_16x16x32_bf16 v[188:191], a[36:39], v[124:127], v[188:191]\n"
"buffer_load_dwordx4 v[184:187], v0, s[4:7], s97 offen offset:2048 sc1\n"
"v_mfma_f32_16x16x32_bf16 v[188:191], a[32:35], v[92:95], v[188:191]\n"
"v_add_f32_e32 v215, v215, v223\n"
"v_fmac_f32_e32 v108, v254, v196\n"
"v_mfma_f32_16x16x32_bf16 v[200:203], a[64:67], v[92:95], v[200:203]\n"
"v_fmac_f32_e32 v109, v254, v197\n"
"v_fmac_f32_e32 v110, v254, v198\n"
"v_mfma_f32_16x16x32_bf16 v[200:203], a[68:71], v[124:127], v[200:203]\n"
"buffer_load_dwordx4 v[208:211], v0, s[4:7], s97 offen offset:3072 sc1\n"
"v_mfma_f32_16x16x32_bf16 v[200:203], a[72:75], v[132:135], v[200:203]\n"
"v_fmac_f32_e32 v111, v254, v199\n"
"v_add_f32_e32 v108, v108, v152\n"
"v_mfma_f32_16x16x32_bf16 v[200:203], a[76:79], v[140:143], v[200:203]\n"
"v_add_f32_e32 v109, v109, v153\n"
"v_add_f32_e32 v110, v110, v154\n"
"v_mfma_f32_16x16x32_bf16 v[200:203], a[80:83], v[128:131], v[200:203]\n"
"v_add_f32_e32 v111, v111, v155\n"
"v_fmac_f32_e32 v96, v254, v100\n"
"v_mfma_f32_16x16x32_bf16 v[200:203], a[84:87], v[168:171], v[200:203]\n"
"s_waitcnt lgkmcnt(0)\n"
"v_fmac_f32_e32 v97, v254, v101\n"
"v_fmac_f32_e32 v98, v254, v102\n"
"v_mfma_f32_16x16x32_bf16 v[200:203], a[88:91], v[172:175], v[200:203]\n"
"buffer_load_dwordx4 v[152:155], v1, s[4:7], s97 offen sc1\n"
"v_mfma_f32_16x16x32_bf16 v[200:203], a[92:95], v[180:183], v[200:203]\n"
"ds_read_b128 v[196:199], v6 offset:2048\n"
"ds_read_b128 v[216:219], v6 offset:3072\n"
"v_mfma_f32_16x16x32_bf16 v[212:215], a[124:127], v[180:183], v[212:215]\n"
"v_fmac_f32_e32 v99, v254, v103\n"
"v_add_f32_e32 v96, v96, v116\n"
"v_mfma_f32_16x16x32_bf16 v[212:215], a[120:123], v[172:175], v[212:215]\n"
"v_add_f32_e32 v97, v97, v117\n"
"v_add_f32_e32 v98, v98, v118\n"
"v_mfma_f32_16x16x32_bf16 v[212:215], a[116:119], v[168:171], v[212:215]\n"
"v_add_f32_e32 v99, v99, v119\n"
"v_add_f32_e32 v112, v112, v204\n"
"v_mfma_f32_16x16x32_bf16 v[212:215], a[112:115], v[128:131], v[212:215]\n"
"buffer_load_dwordx4 v[100:103], v1, s[4:7], s97 offen offset:1024 sc1\n"
"v_mfma_f32_16x16x32_bf16 v[212:215], a[108:111], v[140:143], v[212:215]\n"
"v_add_f32_e32 v113, v113, v205\n"
"v_add_f32_e32 v114, v114, v206\n"
"v_mfma_f32_16x16x32_bf16 v[212:215], a[104:107], v[132:135], v[212:215]\n"
"v_add_f32_e32 v115, v115, v207\n"
"v_add_f32_e32 v188, v188, v156\n"
"v_mfma_f32_16x16x32_bf16 v[212:215], a[100:103], v[124:127], v[212:215]\n"
"v_add_f32_e32 v189, v189, v157\n"
"v_add_f32_e32 v190, v190, v158\n"
"v_mfma_f32_16x16x32_bf16 v[212:215], a[96:99], v[92:95], v[212:215]\n"
"buffer_load_dwordx4 a[188:191], v1, s[4:7], s98 offen offset:3072 sc1\n"
"v_mfma_f32_16x16x32_bf16 v[108:111], v[24:27], v[92:95], v[108:111]\n"
"buffer_load_dwordx4 v[116:119], v1, s[4:7], s97 offen offset:2048 sc1\n"
"v_mfma_f32_16x16x32_bf16 v[108:111], v[28:31], v[124:127], v[108:111]\n"
"v_add_f32_e32 v191, v191, v159\n"
"v_mul_f32_e32 v112, v112, v255\n"
"v_mfma_f32_16x16x32_bf16 v[108:111], v[32:35], v[132:135], v[108:111]\n"
"v_mul_f32_e32 v113, v113, v255\n"
"v_mul_f32_e32 v114, v114, v255\n"
"v_mfma_f32_16x16x32_bf16 v[108:111], v[36:39], v[140:143], v[108:111]\n"
"v_mul_f32_e32 v115, v115, v255\n"
"v_mul_f32_e32 v188, v188, v255\n"
"v_mfma_f32_16x16x32_bf16 v[108:111], v[40:43], v[128:131], v[108:111]\n"
"s_waitcnt lgkmcnt(0)\n"
"v_mul_f32_e32 v189, v189, v255\n"
"v_mul_f32_e32 v190, v190, v255\n"
"v_mfma_f32_16x16x32_bf16 v[108:111], v[44:47], v[168:171], v[108:111]\n"
"buffer_load_dwordx4 v[156:159], v1, s[4:7], s97 offen offset:3072 sc1\n"
"v_mfma_f32_16x16x32_bf16 v[108:111], v[48:51], v[172:175], v[108:111]\n"
"s_add_u32 s98, s63, 0x84000\n"
"ds_read_b128 v[204:207], v6 offset:4096\n"
"ds_read_b128 v[220:223], v6 offset:5120\n"
"v_mfma_f32_16x16x32_bf16 v[108:111], v[52:55], v[180:183], v[108:111]\n"
"v_mul_f32_e32 v191, v191, v255\n"
"v_cvt_pk_bf16_f32 v228, v112, v113\n"
"v_mfma_f32_16x16x32_bf16 v[96:99], v[84:87], v[180:183], v[96:99]\n"
"buffer_store_dword v148, v14, s[8:11], s98 offen sc1\n"
"v_mfma_f32_16x16x32_bf16 v[96:99], v[80:83], v[172:175], v[96:99]\n"
"v_cvt_pk_bf16_f32 v229, v114, v115\n"
"v_cvt_pk_bf16_f32 v230, v188, v189\n"
"v_mfma_f32_16x16x32_bf16 v[96:99], v[76:79], v[168:171], v[96:99]\n"
"v_cvt_pk_bf16_f32 v231, v190, v191\n"
"v_add_f32_e32 v200, v200, v196\n"
"v_mfma_f32_16x16x32_bf16 v[96:99], v[72:75], v[128:131], v[96:99]\n"
"ds_write_b128 v8, v[228:231] offset:25600\n"
"v_mfma_f32_16x16x32_bf16 v[96:99], v[68:71], v[140:143], v[96:99]\n"
"v_add_f32_e32 v201, v201, v197\n"
"v_add_f32_e32 v202, v202, v198\n"
"v_mfma_f32_16x16x32_bf16 v[96:99], v[64:67], v[132:135], v[96:99]\n"
"v_add_f32_e32 v203, v203, v199\n"
"v_add_f32_e32 v212, v212, v216\n"
"v_mfma_f32_16x16x32_bf16 v[96:99], v[60:63], v[124:127], v[96:99]\n"
"v_add_f32_e32 v213, v213, v217\n"
"v_add_f32_e32 v214, v214, v218\n"
"v_mfma_f32_16x16x32_bf16 v[96:99], v[56:59], v[92:95], v[96:99]\n"
"v_add_f32_e32 v215, v215, v219\n"
"v_mul_f32_e32 v200, v200, v255\n"
"v_mul_f32_e32 v201, v201, v255\n"
"v_mul_f32_e32 v202, v202, v255\n"
"v_mul_f32_e32 v203, v203, v255\n"
"v_mul_f32_e32 v212, v212, v255\n"
"v_mul_f32_e32 v213, v213, v255\n"
"v_mul_f32_e32 v214, v214, v255\n"
"v_mul_f32_e32 v215, v215, v255\n"
"v_cvt_pk_bf16_f32 v92, v200, v201\n"
"v_cvt_pk_bf16_f32 v93, v202, v203\n"
"v_cvt_pk_bf16_f32 v94, v212, v213\n"
"v_cvt_pk_bf16_f32 v95, v214, v215\n"
"ds_write_b128 v7, v[92:95] offset:58368\n"
".Lmk_b2:\n"
"s_waitcnt vmcnt(17)\n"
"; @B G1 2.0\n"
"v_mfma_f32_16x16x32_bf16 v[112:115], a[0:3], v[136:139], 0\n"
"v_mfma_f32_16x16x32_bf16 v[112:115], a[4:7], v[88:91], v[112:115]\n"
"s_add_u32 s98, s62, 0x40800\n"
"v_mfma_f32_16x16x32_bf16 v[112:115], a[8:11], v[192:195], v[112:115]\n"
"s_add_u32 s97, s54, 0x408000\n"
"buffer_load_dwordx4 v[124:127], v0, s[4:7], s97 offen sc1\n"
"v_mfma_f32_16x16x32_bf16 v[112:115], a[12:15], v[164:167], v[112:115]\n"
"s_waitcnt vmcnt(12)\n"
"v_mfma_f32_16x16x32_bf16 v[112:115], a[16:19], v[160:163], v[112:115]\n"
"buffer_load_dword v255, v10, s[4:7], s98 offen sc1\n"
"v_mfma_f32_16x16x32_bf16 v[112:115], a[20:23], v[224:227], v[112:115]\n"
"s_waitcnt lgkmcnt(2)\n"
"v_add_f32_e32 v108, v108, v204\n"
"v_add_f32_e32 v109, v109, v205\n"
"v_mfma_f32_16x16x32_bf16 v[112:115], a[24:27], v[144:147], v[112:115]\n"
"v_add_f32_e32 v110, v110, v206\n"
"v_add_f32_e32 v111, v111, v207\n"
"v_mfma_f32_16x16x32_bf16 v[112:115], a[28:31], v[104:107], v[112:115]\n"
"buffer_load_dwordx4 v[128:131], v0, s[4:7], s97 offen offset:1024 sc1\n"
"v_mfma_f32_16x16x32_bf16 v[132:135], a[60:63], v[104:107], 0\n"
"s_add_u32 s96, s70, 0x204000\n"
"buffer_load_dwordx4 a[192:195], v0, s[4:7], s96 offen sc1\n"
"v_mfma_f32_16x16x32_bf16 v[132:135], a[56:59], v[144:147], v[132:135]\n"
"buffer_load_dword v254, v10, s[4:7], s98 offen offset:256 sc1\n"
"v_mfma_f32_16x16x32_bf16 v[132:135], a[52:55], v[224:227], v[132:135]\n"
"v_add_f32_e32 v96, v96, v220\n"
"v_add_f32_e32 v97, v97, v221\n"
"v_mfma_f32_16x16x32_bf16 v[132:135], a[48:51], v[160:163], v[132:135]\n"
"v_add_f32_e32 v98, v98, v222\n"
"v_add_f32_e32 v99, v99, v223\n"
"v_mfma_f32_16x16x32_bf16 v[132:135], a[44:47], v[164:167], v[132:135]\n"
"buffer_load_dwordx4 v[140:143], v0, s[4:7], s97 offen offset:2048 sc1\n"
"v_mfma_f32_16x16x32_bf16 v[132:135], a[40:43], v[192:195], v[132:135]\n"
"ds_write_b128 v3, v[112:115]\n"
"v_mfma_f32_16x16x32_bf16 v[132:135], a[36:39], v[88:91], v[132:135]\n"
"buffer_load_dword v252, v10, s[4:7], s98 offen offset:512 sc1\n"
"v_mfma_f32_16x16x32_bf16 v[132:135], a[32:35], v[136:139], v[132:135]\n"
"v_mul_f32_e32 v108, v108, v253\n"
"v_mul_f32_e32 v109, v109, v253\n"
"v_mfma_f32_16x16x32_bf16 v[112:115], a[64:67], v[136:139], 0\n"
"v_mul_f32_e32 v110, v110, v253\n"
"v_mul_f32_e32 v111, v111, v253\n"
"v_mfma_f32_16x16x32_bf16 v[112:115], a[68:71], v[88:91], v[112:115]\n"
"buffer_load_dwordx4 v[148:151], v0, s[4:7], s97 offen offset:3072 sc1\n"
"v_mfma_f32_16x16x32_bf16 v[112:115], a[72:75], v[192:195], v[112:115]\n"
"buffer_load_dword v251, v10, s[4:7], s98 offen offset:768 sc1\n"
"v_mfma_f32_16x16x32_bf16 v[112:115], a[76:79], v[164:167], v[112:115]\n"
"v_mul_f32_e32 v96, v96, v253\n"
"v_mul_f32_e32 v97, v97, v253\n"
"v_mfma_f32_16x16x32_bf16 v[112:115], a[80:83], v[160:163], v[112:115]\n"
"v_mul_f32_e32 v98, v98, v253\n"
"v_mul_f32_e32 v99, v99, v253\n"
"v_mfma_f32_16x16x32_bf16 v[112:115], a[84:87], v[224:227], v[112:115]\n"
"ds_write_b128 v3, v[132:135] offset:1024\n"
"v_mfma_f32_16x16x32_bf16 v[112:115], a[88:91], v[144:147], v[112:115]\n"
"buffer_load_dwordx4 v[132:135], v1, s[4:7], s97 offen sc1\n"
"v_mfma_f32_16x16x32_bf16 v[112:115], a[92:95], v[104:107], v[112:115]\n"
"buffer_load_dword v253, v10, s[4:7], s98 offen offset:1024 sc1\n"
"v_mfma_f32_16x16x32_bf16 v[168:171], a[124:127], v[104:107], 0\n"
"v_cvt_pk_bf16_f32 v172, v108, v109\n"
"v_cvt_pk_bf16_f32 v173, v110, v111\n"
"v_mfma_f32_16x16x32_bf16 v[168:171], a[120:123], v[144:147], v[168:171]\n"
"v_cvt_pk_bf16_f32 v174, v96, v97\n"
"v_cvt_pk_bf16_f32 v175, v98, v99\n"
"v_mfma_f32_16x16x32_bf16 v[168:171], a[116:119], v[224:227], v[168:171]\n"
"buffer_load_dword v250, v10, s[4:7], s98 offen offset:1280 sc1\n"
"v_mfma_f32_16x16x32_bf16 v[168:171], a[112:115], v[160:163], v[168:171]\n"
"buffer_load_dwordx4 v[96:99], v1, s[4:7], s97 offen offset:1024 sc1\n"
"v_mfma_f32_16x16x32_bf16 v[168:171], a[108:111], v[164:167], v[168:171]\n"
"buffer_load_dwordx4 a[196:199], v0, s[4:7], s96 offen offset:1024 sc1\n"
"v_mfma_f32_16x16x32_bf16 v[168:171], a[104:107], v[192:195], v[168:171]\n"
"ds_write_b128 v3, v[112:115] offset:2048\n"
"v_mfma_f32_16x16x32_bf16 v[168:171], a[100:103], v[88:91], v[168:171]\n"
"buffer_load_dword v249, v10, s[4:7], s98 offen offset:1536 sc1\n"
"v_mfma_f32_16x16x32_bf16 v[168:171], a[96:99], v[136:139], v[168:171]\n"
"ds_write_b128 v2, v[172:175] offset:50176\n"
"v_mfma_f32_16x16x32_bf16 v[108:111], v[24:27], v[136:139], 0\n"
"buffer_load_dwordx4 v[112:115], v1, s[4:7], s97 offen offset:2048 sc1\n"
"s_add_u32 s95, s64, 0x802000\n"
"v_mfma_f32_16x16x32_bf16 v[108:111], v[28:31], v[88:91], v[108:111]\n"
"buffer_load_dword v248, v10, s[4:7], s98 offen offset:1792 sc1\n"
"v_add_u32_e32 v247, 0x26c00, v0\n"
"v_mfma_f32_16x16x32_bf16 v[108:111], v[32:35], v[192:195], v[108:111]\n"
"buffer_store_dwordx4 v[228:231], v8, s[12:15], s95 offen offset:1024 sc1\n"
"v_mfma_f32_16x16x32_bf16 v[108:111], v[36:39], v[164:167], v[108:111]\n"
"ds_read_b128 v[172:175], v247\n"
"ds_read_b128 v[180:183], v247 offset:1024\n"
"v_xor_b32_e32 v247, 32, v15\n"
"v_mfma_f32_16x16x32_bf16 v[108:111], v[40:43], v[160:163], v[108:111]\n"
"ds_read_b128 v[188:191], v2 offset:50176\n"
"ds_read_b128 v[196:199], v15 offset:24576\n"
"ds_read_b128 v[200:203], v247 offset:24832\n"
"v_mfma_f32_16x16x32_bf16 v[108:111], v[44:47], v[224:227], v[108:111]\n"
"buffer_load_dwordx4 v[204:207], v1, s[4:7], s97 offen offset:3072 sc1\n"
"v_mfma_f32_16x16x32_bf16 v[108:111], v[48:51], v[144:147], v[108:111]\n"
"ds_write_b128 v3, v[168:171] offset:3072\n"
"v_mfma_f32_16x16x32_bf16 v[108:111], v[52:55], v[104:107], v[108:111]\n"
"v_mfma_f32_16x16x32_bf16 v[168:171], v[84:87], v[104:107], 0\n"
"v_mfma_f32_16x16x32_bf16 v[168:171], v[80:83], v[144:147], v[168:171]\n"
"s_waitcnt lgkmcnt(1)\n"
"v_mfma_f32_16x16x32_bf16 v[172:175], v[196:199], v[188:191], v[172:175]\n"
"v_mfma_f32_16x16x32_bf16 v[180:183], v[200:203], v[188:191], v[180:183]\n"
"ds_read_b64_tr_b16 v[104:105], v16 offset:24576\n"
"v_xor_b32_e32 v247, 32, v16\n"
"v_mfma_f32_16x16x32_bf16 v[168:171], v[76:79], v[224:227], v[168:171]\n"
"ds_read_b64_tr_b16 v[106:107], v247 offset:24832\n"
"ds_read_b64_tr_b16 v[144:145], v247 offset:24576\n"
"ds_read_b64_tr_b16 v[146:147], v16 offset:24832\n"
"v_mov_b32_e32 v188, 0x3f803f80\n"
"v_mfma_f32_16x16x32_bf16 v[168:171], v[72:75], v[160:163], v[168:171]\n"
"v_mov_b32_e32 v189, 0x3f803f80\n"
"v_mov_b32_e32 v190, 0x3f803f80\n"
"v_mov_b32_e32 v191, 0x3f803f80\n"
"v_mfma_f32_16x16x32_bf16 v[168:171], v[68:71], v[164:167], v[168:171]\n"
"v_max_f32_e32 v247, v172, v173\n"
"v_max3_f32 v247, v247, v174, v175\n"
"v_max3_f32 v247, v247, v180, v181\n"
"v_mfma_f32_16x16x32_bf16 v[168:171], v[64:67], v[192:195], v[168:171]\n"
"ds_write_b128 v3, v[108:111] offset:4096\n"
"v_mfma_f32_16x16x32_bf16 v[168:171], v[60:63], v[88:91], v[168:171]\n"
"v_max3_f32 v247, v247, v182, v183\n"
"v_mov_b32_e32 v246, v247\n"
"v_mfma_f32_16x16x32_bf16 v[168:171], v[56:59], v[136:139], v[168:171]\n"
"s_waitcnt vmcnt(25)\n"
"v_permlane16_swap_b32_e32 v247, v246\n"
"v_max_f32_e32 v247, v247, v246\n"
"v_mov_b32_e32 v246, v247\n"
"; @B G1 2.1\n"
"v_mfma_f32_16x16x32_bf16 v[88:91], a[0:3], v[120:123], 0\n"
"buffer_load_dwordx4 a[200:203], v0, s[4:7], s96 offen offset:2048 sc1\n"
"v_permlane32_swap_b32_e32 v247, v246\n"
"v_mfma_f32_16x16x32_bf16 v[88:91], a[4:7], v[176:179], v[88:91]\n"
"v_max_f32_e32 v247, v247, v246\n"
"v_sub_f32_e32 v172, v172, v247\n"
"v_sub_f32_e32 v173, v173, v247\n"
"v_mfma_f32_16x16x32_bf16 v[88:91], a[8:11], v[184:187], v[88:91]\n"
"s_add_u32 s98, s55, 0x408000\n"
"buffer_load_dwordx4 v[108:111], v0, s[4:7], s98 offen sc1\n"
"v_mfma_f32_16x16x32_bf16 v[88:91], a[12:15], v[208:211], v[88:91]\n"
"s_waitcnt vmcnt(22)\n"
"v_sub_f32_e32 v174, v174, v247\n"
"v_sub_f32_e32 v175, v175, v247\n"
"v_sub_f32_e32 v180, v180, v247\n"
"v_mfma_f32_16x16x32_bf16 v[88:91], a[16:19], v[152:155], v[88:91]\n"
"v_sub_f32_e32 v181, v181, v247\n"
"v_sub_f32_e32 v182, v182, v247\n"
"v_sub_f32_e32 v183, v183, v247\n"
"v_mfma_f32_16x16x32_bf16 v[88:91], a[20:23], v[100:103], v[88:91]\n"
"ds_write_b128 v3, v[168:171] offset:5120\n"
"v_mfma_f32_16x16x32_bf16 v[88:91], a[24:27], v[116:119], v[88:91]\n"
"v_exp_f32_e32 v172, v172\n"
"v_mfma_f32_16x16x32_bf16 v[88:91], a[28:31], v[156:159], v[88:91]\n"
"buffer_load_dwordx4 v[136:139], v0, s[4:7], s98 offen offset:1024 sc1\n"
"v_mfma_f32_16x16x32_bf16 v[160:163], a[60:63], v[156:159], 0\n"
"s_add_u32 s97, s64, 0x806000\n"
"v_exp_f32_e32 v173, v173\n"
"v_mfma_f32_16x16x32_bf16 v[160:163], a[56:59], v[116:119], v[160:163]\n"
"v_exp_f32_e32 v174, v174\n"
"v_mfma_f32_16x16x32_bf16 v[160:163], a[52:55], v[100:103], v[160:163]\n"
"buffer_store_dwordx4 v[92:95], v7, s[12:15], s97 offen offset:1024 sc1\n"
"v_mfma_f32_16x16x32_bf16 v[160:163], a[48:51], v[152:155], v[160:163]\n"
"v_exp_f32_e32 v175, v175\n"
"v_mfma_f32_16x16x32_bf16 v[160:163], a[44:47], v[208:211], v[160:163]\n"
"buffer_load_dwordx4 v[92:95], v0, s[4:7], s98 offen offset:2048 sc1\n"
"v_mfma_f32_16x16x32_bf16 v[160:163], a[40:43], v[184:187], v[160:163]\n"
"ds_write_b128 v3, v[88:91] offset:6144\n"
"v_mfma_f32_16x16x32_bf16 v[160:163], a[36:39], v[176:179], v[160:163]\n"
"v_exp_f32_e32 v180, v180\n"
"v_mfma_f32_16x16x32_bf16 v[160:163], a[32:35], v[120:123], v[160:163]\n"
"v_exp_f32_e32 v181, v181\n"
"v_mfma_f32_16x16x32_bf16 v[88:91], a[64:67], v[120:123], 0\n"
"v_exp_f32_e32 v182, v182\n"
"v_mfma_f32_16x16x32_bf16 v[88:91], a[68:71], v[176:179], v[88:91]\n"
"buffer_load_dwordx4 v[164:167], v0, s[4:7], s98 offen offset:3072 sc1\n"
"v_mfma_f32_16x16x32_bf16 v[88:91], a[72:75], v[184:187], v[88:91]\n"
"v_exp_f32_e32 v183, v183\n"
"v_cvt_pk_bf16_f32 v168, v172, v173\n"
"v_mfma_f32_16x16x32_bf16 v[88:91], a[76:79], v[208:211], v[88:91]\n"
"v_cvt_pk_bf16_f32 v169, v174, v175\n"
"v_cvt_pk_bf16_f32 v170, v180, v181\n"
"v_cvt_pk_bf16_f32 v171, v182, v183\n"
"v_mfma_f32_16x16x32_bf16 v[88:91], a[80:83], v[152:155], v[88:91]\n"
"s_waitcnt lgkmcnt(3)\n"
"v_mov_b32_e32 v172, -1\n"
"v_mov_b32_e32 v173, -1\n"
"v_mfma_f32_16x16x32_bf16 v[180:183], v[188:191], v[168:171], 0\n"
"v_mfma_f32_16x16x32_bf16 v[188:191], v[104:107], v[168:171], 0\n"
"v_mfma_f32_16x16x32_bf16 v[104:107], v[144:147], v[168:171], 0\n"
"v_mfma_f32_16x16x32_bf16 v[88:91], a[84:87], v[100:103], v[88:91]\n"
"ds_write_b128 v3, v[160:163] offset:7168\n"
"v_mfma_f32_16x16x32_bf16 v[88:91], a[88:91], v[116:119], v[88:91]\n"
"buffer_load_dwordx4 v[144:147], v1, s[4:7], s98 offen sc1\n"
"v_mfma_f32_16x16x32_bf16 v[88:91], a[92:95], v[156:159], v[88:91]\n"
"v_mov_b32_e32 v174, -1\n"
"v_mov_b32_e32 v175, -1\n"
"v_mfma_f32_16x16x32_bf16 v[160:163], a[124:127], v[156:159], 0\n"
"s_add_u32 s97, s60, 0x848000\n"
"buffer_store_dwordx4 v[172:175], v0, s[8:11], s97 offen sc1\n"
"v_mfma_f32_16x16x32_bf16 v[160:163], a[120:123], v[116:119], v[160:163]\n"
"buffer_load_dwordx4 a[204:207], v0, s[4:7], s96 offen offset:3072 sc1\n"
"v_mfma_f32_16x16x32_bf16 v[160:163], a[116:119], v[100:103], v[160:163]\n"
"v_rcp_f32_e32 v180, v180\n"
"v_mfma_f32_16x16x32_bf16 v[160:163], a[112:115], v[152:155], v[160:163]\n"
"buffer_load_dwordx4 v[168:171], v1, s[4:7], s98 offen offset:1024 sc1\n"
"v_mul_f32_e32 v188, v188, v180\n"
"v_mfma_f32_16x16x32_bf16 v[160:163], a[108:111], v[208:211], v[160:163]\n"
"v_mul_f32_e32 v189, v189, v180\n"
"v_mul_f32_e32 v190, v190, v180\n"
"v_mul_f32_e32 v191, v191, v180\n"
"v_mfma_f32_16x16x32_bf16 v[160:163], a[104:107], v[184:187], v[160:163]\n"
"ds_write_b128 v3, v[88:91] offset:8192\n"
"v_mfma_f32_16x16x32_bf16 v[160:163], a[100:103], v[176:179], v[160:163]\n"
"v_mul_f32_e32 v104, v104, v180\n"
"v_mul_f32_e32 v105, v105, v180\n"
"v_mul_f32_e32 v106, v106, v180\n"
"v_mfma_f32_16x16x32_bf16 v[160:163], a[96:99], v[120:123], v[160:163]\n"
"v_mul_f32_e32 v107, v107, v180\n"
"v_cvt_pk_bf16_f32 v88, v188, v189\n"
"v_cvt_pk_bf16_f32 v89, v190, v191\n"
"v_mfma_f32_16x16x32_bf16 v[180:183], v[24:27], v[120:123], 0\n"
"buffer_load_dwordx4 v[188:191], v1, s[4:7], s98 offen offset:2048 sc1\n"
"v_cvt_pk_bf16_f32 v90, v104, v105\n"
"v_mfma_f32_16x16x32_bf16 v[180:183], v[28:31], v[176:179], v[180:183]\n"
"s_add_u32 s97, s60, 0x448000\n"
"v_cvt_pk_bf16_f32 v91, v106, v107\n"
"v_mfma_f32_16x16x32_bf16 v[180:183], v[32:35], v[184:187], v[180:183]\n"
"buffer_store_dwordx4 v[88:91], v0, s[4:7], s97 offen sc1\n"
"v_mfma_f32_16x16x32_bf16 v[180:183], v[36:39], v[208:211], v[180:183]\n"
"v_mfma_f32_16x16x32_bf16 v[180:183], v[40:43], v[152:155], v[180:183]\n"
"s_waitcnt vmcnt(14)\n"
"v_add_f32_e32 v255, v255, v253\n"
"v_add_f32_e32 v254, v254, v250\n"
"v_mfma_f32_16x16x32_bf16 v[180:183], v[44:47], v[100:103], v[180:183]\n"
"buffer_load_dwordx4 v[88:91], v1, s[4:7], s98 offen offset:3072 sc1\n"
"v_mfma_f32_16x16x32_bf16 v[180:183], v[48:51], v[116:119], v[180:183]\n"
"ds_write_b128 v3, v[160:163] offset:9216\n"
"v_mfma_f32_16x16x32_bf16 v[180:183], v[52:55], v[156:159], v[180:183]\n"
"v_add_f32_e32 v252, v252, v249\n"
"v_add_f32_e32 v251, v251, v248\n"
"v_mfma_f32_16x16x32_bf16 v[104:107], v[84:87], v[156:159], 0\n"
"v_add_f32_e32 v255, v255, v252\n"
"v_add_f32_e32 v254, v254, v251\n"
"v_mfma_f32_16x16x32_bf16 v[104:107], v[80:83], v[116:119], v[104:107]\n"
"v_add_f32_e32 v255, v255, v254\n"
"v_mov_b32_e32 v254, v255\n"
"v_mfma_f32_16x16x32_bf16 v[104:107], v[76:79], v[100:103], v[104:107]\n"
"s_nop 0\n"
"v_permlane32_swap_b32_e32 v255, v254\n"
"v_add_f32_e32 v255, v255, v254\n"
"v_mfma_f32_16x16x32_bf16 v[104:107], v[72:75], v[152:155], v[104:107]\n"
"v_mul_f32_e32 v255, 0x3a800000, v255\n"
"v_mov_b32_e32 v254, v255\n"
"v_mfma_f32_16x16x32_bf16 v[104:107], v[68:71], v[208:211], v[104:107]\n"
"s_nop 0\n"
"v_permlane16_swap_b32_e32 v255, v254\n"
"v_cvt_pk_bf16_f32 v253, -v255, 0\n"
"v_mfma_f32_16x16x32_bf16 v[104:107], v[64:67], v[184:187], v[104:107]\n"
"ds_write_b128 v3, v[180:183] offset:10240\n"
"v_mfma_f32_16x16x32_bf16 v[104:107], v[60:63], v[176:179], v[104:107]\n"
"buffer_load_dwordx4 a[208:211], v1, s[4:7], s96 offen sc1\n"
"v_mfma_f32_16x16x32_bf16 v[104:107], v[56:59], v[120:123], v[104:107]\n"
"v_fma_f32 v254, -v255, v255, v254\n"
"v_lshlrev_b32_e32 v253, 16, v253\n"
"; @B G1 2.2\n"
"v_mfma_f32_16x16x32_bf16 v[100:103], a[0:3], v[124:127], 0\n"
"v_max_f32_e32 v254, 0, v254\n"
"v_cvt_pk_bf16_f32 v254, v254, 0\n"
"v_mfma_f32_16x16x32_bf16 v[100:103], a[4:7], v[128:131], v[100:103]\n"
"v_lshlrev_b32_e32 v254, 16, v254\n"
"v_add_f32_e32 v254, 0x3727c5ac, v254\n"
"v_mfma_f32_16x16x32_bf16 v[100:103], a[8:11], v[140:143], v[100:103]\n"
"v_cvt_pk_bf16_f32 v254, v254, 0\n"
"v_lshlrev_b32_e32 v254, 16, v254\n"
"v_mfma_f32_16x16x32_bf16 v[100:103], a[12:15], v[148:151], v[100:103]\n"
"s_waitcnt vmcnt(14)\n"
"v_sqrt_f32_e32 v254, v254\n"
"v_mfma_f32_16x16x32_bf16 v[100:103], a[16:19], v[132:135], v[100:103]\n"
"s_add_u32 s98, s68, 0x10000\n"
"v_cvt_pk_bf16_f32 v254, v254, 0\n"
"v_mfma_f32_16x16x32_bf16 v[100:103], a[20:23], v[96:99], v[100:103]\n"
"ds_write_b128 v3, v[104:107] offset:11264\n"
"v_mfma_f32_16x16x32_bf16 v[100:103], a[24:27], v[112:115], v[100:103]\n"
"buffer_store_dwordx2 v[18:19], v11, s[4:7], s98 offen offset:32 sc1\n"
"v_mfma_f32_16x16x32_bf16 v[100:103], a[28:31], v[204:207], v[100:103]\n"
"v_lshlrev_b32_e32 v254, 16, v254\n"
"v_mfma_f32_16x16x32_bf16 v[104:107], a[60:63], v[204:207], 0\n"
"v_rcp_f32_e32 v255, v254\n"
"v_mfma_f32_16x16x32_bf16 v[104:107], a[56:59], v[112:115], v[104:107]\n"
"v_mul_f32_e32 v254, 0x3e8293ee, v255\n"
"v_mfma_f32_16x16x32_bf16 v[104:107], a[52:55], v[96:99], v[104:107]\n"
"s_add_u32 s98, s52, 0x400000\n"
"buffer_load_dwordx4 v[116:119], v0, s[4:7], s98 offen sc1\n"
"v_mfma_f32_16x16x32_bf16 v[104:107], a[48:51], v[132:135], v[104:107]\n"
"s_waitcnt lgkmcnt(0)\n"
"s_barrier\n"
"v_mfma_f32_16x16x32_bf16 v[104:107], a[44:47], v[148:151], v[104:107]\n"
"ds_read_b128 v[120:123], v4\n"
"ds_read_b128 v[152:155], v9 offset:640\n"
"v_mfma_f32_16x16x32_bf16 v[104:107], a[40:43], v[140:143], v[104:107]\n"
"ds_write_b128 v3, v[100:103] offset:12288\n"
"v_mfma_f32_16x16x32_bf16 v[104:107], a[36:39], v[128:131], v[104:107]\n"
"ds_read_b128 v[100:103], v5\n"
"v_mfma_f32_16x16x32_bf16 v[104:107], a[32:35], v[124:127], v[104:107]\n"
"buffer_load_dwordx4 v[156:159], v0, s[4:7], s98 offen offset:1024 sc1\n"
"v_mfma_f32_16x16x32_bf16 v[160:163], a[64:67], v[124:127], 0\n"
"v_mfma_f32_16x16x32_bf16 v[160:163], a[68:71], v[128:131], v[160:163]\n"
"v_mfma_f32_16x16x32_bf16 v[160:163], a[72:75], v[140:143], v[160:163]\n"
"buffer_load_dwordx4 a[212:215], v1, s[4:7], s96 offen offset:1024 sc1\n"
"v_mfma_f32_16x16x32_bf16 v[160:163], a[76:79], v[148:151], v[160:163]\n"
"ds_read_b128 v[176:179], v4 offset:1024\n"
"ds_read_b128 v[180:183], v9 offset:704\n"
"v_mfma_f32_16x16x32_bf16 v[160:163], a[80:83], v[132:135], v[160:163]\n"
"buffer_load_dwordx4 v[184:187], v0, s[4:7], s98 offen offset:2048 sc1\n"
"v_mfma_f32_16x16x32_bf16 v[160:163], a[84:87], v[96:99], v[160:163]\n"
"ds_write_b128 v3, v[104:107] offset:13312\n"
"v_mfma_f32_16x16x32_bf16 v[160:163], a[88:91], v[112:115], v[160:163]\n"
"ds_read_b128 v[104:107], v5 offset:1024\n"
"s_waitcnt lgkmcnt(4)\n"
"v_mfma_f32_16x16x32_bf16 v[160:163], a[92:95], v[204:207], v[160:163]\n"
"v_fmac_f32_e32 v120, v253, v152\n"
"v_fmac_f32_e32 v121, v253, v153\n"
"v_mfma_f32_16x16x32_bf16 v[192:195], a[124:127], v[204:207], 0\n"
"v_fmac_f32_e32 v122, v253, v154\n"
"v_fmac_f32_e32 v123, v253, v155\n"
"v_mfma_f32_16x16x32_bf16 v[192:195], a[120:123], v[112:115], v[192:195]\n"
"buffer_load_dwordx4 v[152:155], v0, s[4:7], s98 offen offset:3072 sc1\n"
"v_mfma_f32_16x16x32_bf16 v[192:195], a[116:119], v[96:99], v[192:195]\n"
"ds_read_b128 v[196:199], v4 offset:2048\n"
"ds_read_b128 v[200:203], v9 offset:768\n"
"v_mfma_f32_16x16x32_bf16 v[192:195], a[112:115], v[132:135], v[192:195]\n"
"ds_read_b128 v[208:211], v5 offset:2048\n"
"v_add_f32_e32 v120, v120, v100\n"
"v_mfma_f32_16x16x32_bf16 v[192:195], a[108:111], v[148:151], v[192:195]\n"
"v_add_f32_e32 v121, v121, v101\n"
"v_add_f32_e32 v122, v122, v102\n"
"v_mfma_f32_16x16x32_bf16 v[192:195], a[104:107], v[140:143], v[192:195]\n"
"ds_write_b128 v3, v[160:163] offset:14336\n"
"v_mfma_f32_16x16x32_bf16 v[192:195], a[100:103], v[128:131], v[192:195]\n"
"buffer_load_dwordx4 v[160:163], v1, s[4:7], s98 offen sc1\n"
"v_mfma_f32_16x16x32_bf16 v[192:195], a[96:99], v[124:127], v[192:195]\n"
"s_add_u32 s97, s60, 0x840000\n"
"v_add_f32_e32 v123, v123, v103\n"
"s_waitcnt lgkmcnt(4)\n"
"v_mfma_f32_16x16x32_bf16 v[100:103], v[24:27], v[124:127], 0\n"
"buffer_store_dwordx4 v[172:175], v0, s[8:11], s97 offen sc1\n"
"v_mfma_f32_16x16x32_bf16 v[100:103], v[28:31], v[128:131], v[100:103]\n"
"ds_read_b128 v[212:215], v4 offset:3072\n"
"ds_read_b128 v[216:219], v9 offset:832\n"
"v_mfma_f32_16x16x32_bf16 v[100:103], v[32:35], v[140:143], v[100:103]\n"
"s_waitcnt vmcnt(18)\n"
"s_add_u32 s97, s68, 0x11000\n"
"ds_read_b128 v[220:223], v5 offset:3072\n"
"v_mfma_f32_16x16x32_bf16 v[100:103], v[36:39], v[148:151], v[100:103]\n"
"buffer_load_dwordx4 v[224:227], v1, s[4:7], s98 offen offset:1024 sc1\n"
"v_mfma_f32_16x16x32_bf16 v[100:103], v[40:43], v[132:135], v[100:103]\n"
"buffer_store_dwordx2 v[18:19], v11, s[12:15], s97 offen offset:64 sc1\n"
"v_mfma_f32_16x16x32_bf16 v[100:103], v[44:47], v[96:99], v[100:103]\n"
"ds_write_b128 v3, v[192:195] offset:15360\n"
"v_mfma_f32_16x16x32_bf16 v[100:103], v[48:51], v[112:115], v[100:103]\n"
"buffer_load_dwordx4 a[216:219], v1, s[4:7], s96 offen offset:2048 sc1\n"
"v_mfma_f32_16x16x32_bf16 v[100:103], v[52:55], v[204:207], v[100:103]\n"
"v_fmac_f32_e32 v176, v253, v180\n"
"v_fmac_f32_e32 v177, v253, v181\n"
"v_mfma_f32_16x16x32_bf16 v[192:195], v[84:87], v[204:207], 0\n"
"buffer_load_dwordx4 v[204:207], v1, s[4:7], s98 offen offset:2048 sc1\n"
"v_mfma_f32_16x16x32_bf16 v[192:195], v[80:83], v[112:115], v[192:195]\n"
"ds_read_b128 v[112:115], v4 offset:4096\n"
"ds_read_b128 v[228:231], v9 offset:512\n"
"v_mfma_f32_16x16x32_bf16 v[192:195], v[76:79], v[96:99], v[192:195]\n"
"ds_read_b128 v[96:99], v5 offset:4096\n"
"v_fmac_f32_e32 v178, v253, v182\n"
"v_mfma_f32_16x16x32_bf16 v[192:195], v[72:75], v[132:135], v[192:195]\n"
"v_fmac_f32_e32 v179, v253, v183\n"
"v_add_f32_e32 v176, v176, v104\n"
"v_mfma_f32_16x16x32_bf16 v[192:195], v[68:71], v[148:151], v[192:195]\n"
"v_add_f32_e32 v177, v177, v105\n"
"v_add_f32_e32 v178, v178, v106\n"
"v_mfma_f32_16x16x32_bf16 v[192:195], v[64:67], v[140:143], v[192:195]\n"
"buffer_load_dwordx4 v[132:135], v1, s[4:7], s98 offen offset:3072 sc1\n"
"v_mfma_f32_16x16x32_bf16 v[192:195], v[60:63], v[128:131], v[192:195]\n"
"ds_write_b128 v3, v[100:103] offset:16384\n"
"v_mfma_f32_16x16x32_bf16 v[192:195], v[56:59], v[124:127], v[192:195]\n"
"s_waitcnt vmcnt(21)\n"
"ds_read_b128 v[100:103], v4 offset:5120\n"
"ds_read_b128 v[124:127], v9 offset:576\n"
"; @B G1 2.3\n"
"v_mfma_f32_16x16x32_bf16 v[120:123], a[0:3], v[108:111], v[120:123]\n"
"ds_read_b128 v[128:131], v5 offset:5120\n"
"v_add_f32_e32 v179, v179, v107\n"
"v_mfma_f32_16x16x32_bf16 v[120:123], a[4:7], v[136:139], v[120:123]\n"
"s_waitcnt lgkmcnt(12)\n"
"v_fmac_f32_e32 v196, v253, v200\n"
"v_fmac_f32_e32 v197, v253, v201\n"
"v_mfma_f32_16x16x32_bf16 v[120:123], a[8:11], v[92:95], v[120:123]\n"
"s_add_u32 s98, s53, 0x400000\n"
"buffer_load_dwordx4 v[104:107], v0, s[4:7], s98 offen sc1\n"
"v_mfma_f32_16x16x32_bf16 v[120:123], a[12:15], v[164:167], v[120:123]\n"
"s_waitcnt vmcnt(15)\n"
"v_fmac_f32_e32 v198, v253, v202\n"
"v_fmac_f32_e32 v199, v253, v203\n"
"v_mfma_f32_16x16x32_bf16 v[120:123], a[16:19], v[144:147], v[120:123]\n"
"v_add_f32_e32 v196, v196, v208\n"
"v_add_f32_e32 v197, v197, v209\n"
"v_mfma_f32_16x16x32_bf16 v[120:123], a[20:23], v[168:171], v[120:123]\n"
"ds_write_b128 v3, v[192:195] offset:17408\n"
"v_mfma_f32_16x16x32_bf16 v[120:123], a[24:27], v[188:191], v[120:123]\n"
"s_waitcnt lgkmcnt(9)\n"
"v_add_f32_e32 v198, v198, v210\n"
"v_add_f32_e32 v199, v199, v211\n"
"v_mfma_f32_16x16x32_bf16 v[120:123], a[28:31], v[88:91], v[120:123]\n"
"buffer_load_dwordx4 v[140:143], v0, s[4:7], s98 offen offset:1024 sc1\n"
"v_mfma_f32_16x16x32_bf16 v[176:179], a[60:63], v[88:91], v[176:179]\n"
"v_fmac_f32_e32 v212, v253, v216\n"
"v_fmac_f32_e32 v213, v253, v217\n"
"v_mfma_f32_16x16x32_bf16 v[176:179], a[56:59], v[188:191], v[176:179]\n"
"v_fmac_f32_e32 v214, v253, v218\n"
"v_fmac_f32_e32 v215, v253, v219\n"
"v_mfma_f32_16x16x32_bf16 v[176:179], a[52:55], v[168:171], v[176:179]\n"
"s_waitcnt lgkmcnt(0)\n"
"s_barrier\n"
"v_mfma_f32_16x16x32_bf16 v[176:179], a[48:51], v[144:147], v[176:179]\n"
"buffer_load_dwordx4 a[220:223], v1, s[4:7], s96 offen offset:3072 sc1\n"
"v_mfma_f32_16x16x32_bf16 v[176:179], a[44:47], v[164:167], v[176:179]\n"
"buffer_load_dwordx4 v[148:151], v0, s[4:7], s98 offen offset:2048 sc1\n"
"v_mfma_f32_16x16x32_bf16 v[176:179], a[40:43], v[92:95], v[176:179]\n"
"ds_read_b128 v[180:183], v6\n"
"ds_read_b128 v[192:195], v6 offset:1024\n"
"v_mfma_f32_16x16x32_bf16 v[176:179], a[36:39], v[136:139], v[176:179]\n"
"v_add_f32_e32 v212, v212, v220\n"
"v_add_f32_e32 v213, v213, v221\n"
"v_mfma_f32_16x16x32_bf16 v[176:179], a[32:35], v[108:111], v[176:179]\n"
"v_add_f32_e32 v214, v214, v222\n"
"v_add_f32_e32 v215, v215, v223\n"
"v_mfma_f32_16x16x32_bf16 v[196:199], a[64:67], v[108:111], v[196:199]\n"
"v_fmac_f32_e32 v112, v253, v228\n"
"v_fmac_f32_e32 v113, v253, v229\n"
"v_mfma_f32_16x16x32_bf16 v[196:199], a[68:71], v[136:139], v[196:199]\n"
"buffer_load_dwordx4 v[200:203], v0, s[4:7], s98 offen offset:3072 sc1\n"
"v_mfma_f32_16x16x32_bf16 v[196:199], a[72:75], v[92:95], v[196:199]\n"
"v_fmac_f32_e32 v114, v253, v230\n"
"v_fmac_f32_e32 v115, v253, v231\n"
"v_mfma_f32_16x16x32_bf16 v[196:199], a[76:79], v[164:167], v[196:199]\n"
"v_add_f32_e32 v112, v112, v96\n"
"v_add_f32_e32 v113, v113, v97\n"
"v_mfma_f32_16x16x32_bf16 v[196:199], a[80:83], v[144:147], v[196:199]\n"
"v_add_f32_e32 v114, v114, v98\n"
"v_add_f32_e32 v115, v115, v99\n"
"v_mfma_f32_16x16x32_bf16 v[196:199], a[84:87], v[168:171], v[196:199]\n"
"s_waitcnt lgkmcnt(0)\n"
"v_fmac_f32_e32 v100, v253, v124\n"
"v_fmac_f32_e32 v101, v253, v125\n"
"v_mfma_f32_16x16x32_bf16 v[196:199], a[88:91], v[188:191], v[196:199]\n"
"buffer_load_dwordx4 v[96:99], v1, s[4:7], s98 offen sc1\n"
"v_mfma_f32_16x16x32_bf16 v[196:199], a[92:95], v[88:91], v[196:199]\n"
"ds_read_b128 v[208:211], v6 offset:2048\n"
"ds_read_b128 v[216:219], v6 offset:3072\n"
"v_mfma_f32_16x16x32_bf16 v[212:215], a[124:127], v[88:91], v[212:215]\n"
"v_fmac_f32_e32 v102, v253, v126\n"
"v_fmac_f32_e32 v103, v253, v127\n"
"v_mfma_f32_16x16x32_bf16 v[212:215], a[120:123], v[188:191], v[212:215]\n"
"v_add_f32_e32 v100, v100, v128\n"
"v_add_f32_e32 v101, v101, v129\n"
"v_mfma_f32_16x16x32_bf16 v[212:215], a[116:119], v[168:171], v[212:215]\n"
"v_add_f32_e32 v102, v102, v130\n"
"v_add_f32_e32 v103, v103, v131\n"
"v_mfma_f32_16x16x32_bf16 v[212:215], a[112:115], v[144:147], v[212:215]\n"
"buffer_load_dwordx4 v[124:127], v1, s[4:7], s98 offen offset:1024 sc1\n"
"v_mfma_f32_16x16x32_bf16 v[212:215], a[108:111], v[164:167], v[212:215]\n"
"v_add_f32_e32 v120, v120, v180\n"
"v_add_f32_e32 v121, v121, v181\n"
"v_mfma_f32_16x16x32_bf16 v[212:215], a[104:107], v[92:95], v[212:215]\n"
"s_add_u32 s97, s70, 0x206000\n"
"v_add_f32_e32 v122, v122, v182\n"
"v_add_f32_e32 v123, v123, v183\n"
"v_mfma_f32_16x16x32_bf16 v[212:215], a[100:103], v[136:139], v[212:215]\n"
"v_add_f32_e32 v176, v176, v192\n"
"v_add_f32_e32 v177, v177, v193\n"
"v_mfma_f32_16x16x32_bf16 v[212:215], a[96:99], v[108:111], v[212:215]\n"
"buffer_load_dwordx4 a[224:227], v0, s[4:7], s97 offen sc1\n"
"v_mfma_f32_16x16x32_bf16 v[112:115], v[24:27], v[108:111], v[112:115]\n"
"buffer_load_dwordx4 v[128:131], v1, s[4:7], s98 offen offset:2048 sc1\n"
"v_mfma_f32_16x16x32_bf16 v[112:115], v[28:31], v[136:139], v[112:115]\n"
"v_add_f32_e32 v178, v178, v194\n"
"v_add_f32_e32 v179, v179, v195\n"
"v_mfma_f32_16x16x32_bf16 v[112:115], v[32:35], v[92:95], v[112:115]\n"
"v_mul_f32_e32 v120, v120, v255\n"
"v_mul_f32_e32 v121, v121, v255\n"
"v_mfma_f32_16x16x32_bf16 v[112:115], v[36:39], v[164:167], v[112:115]\n"
"v_mul_f32_e32 v122, v122, v255\n"
"v_mul_f32_e32 v123, v123, v255\n"
"v_mfma_f32_16x16x32_bf16 v[112:115], v[40:43], v[144:147], v[112:115]\n"
"s_waitcnt lgkmcnt(0)\n"
"v_mul_f32_e32 v176, v176, v255\n"
"v_mul_f32_e32 v177, v177, v255\n"
"v_mfma_f32_16x16x32_bf16 v[112:115], v[44:47], v[168:171], v[112:115]\n"
"buffer_load_dwordx4 v[180:183], v1, s[4:7], s98 offen offset:3072 sc1\n"
"v_mfma_f32_16x16x32_bf16 v[112:115], v[48:51], v[188:191], v[112:115]\n"
"s_add_u32 s98, s60, 0x800000\n"
"ds_read_b128 v[192:195], v6 offset:4096\n"
"ds_read_b128 v[220:223], v6 offset:5120\n"
"v_mfma_f32_16x16x32_bf16 v[112:115], v[52:55], v[88:91], v[112:115]\n"
"v_mul_f32_e32 v178, v178, v255\n"
"v_mul_f32_e32 v179, v179, v255\n"
"v_mfma_f32_16x16x32_bf16 v[100:103], v[84:87], v[88:91], v[100:103]\n"
"buffer_store_dwordx4 v[172:175], v0, s[8:11], s98 offen sc1\n"
"v_mfma_f32_16x16x32_bf16 v[100:103], v[80:83], v[188:191], v[100:103]\n"
"v_cvt_pk_bf16_f32 v88, v120, v121\n"
"v_cvt_pk_bf16_f32 v89, v122, v123\n"
"v_mfma_f32_16x16x32_bf16 v[100:103], v[76:79], v[168:171], v[100:103]\n"
"v_cvt_pk_bf16_f32 v90, v176, v177\n"
"v_cvt_pk_bf16_f32 v91, v178, v179\n"
"v_mfma_f32_16x16x32_bf16 v[100:103], v[72:75], v[144:147], v[100:103]\n"
"ds_write_b128 v8, v[88:91] offset:17408\n"
"v_mfma_f32_16x16x32_bf16 v[100:103], v[68:71], v[164:167], v[100:103]\n"
"v_add_f32_e32 v196, v196, v208\n"
"v_add_f32_e32 v197, v197, v209\n"
"v_mfma_f32_16x16x32_bf16 v[100:103], v[64:67], v[92:95], v[100:103]\n"
"v_add_f32_e32 v198, v198, v210\n"
"v_add_f32_e32 v199, v199, v211\n"
"v_mfma_f32_16x16x32_bf16 v[100:103], v[60:63], v[136:139], v[100:103]\n"
"v_add_f32_e32 v212, v212, v216\n"
"v_add_f32_e32 v213, v213, v217\n"
"v_mfma_f32_16x16x32_bf16 v[100:103], v[56:59], v[108:111], v[100:103]\n"
"v_add_f32_e32 v214, v214, v218\n"
"v_add_f32_e32 v215, v215, v219\n"
"v_mul_f32_e32 v196, v196, v255\n"
"v_mul_f32_e32 v197, v197, v255\n"
"v_mul_f32_e32 v198, v198, v255\n"
"v_mul_f32_e32 v199, v199, v255\n"
"v_mul_f32_e32 v212, v212, v255\n"
"v_mul_f32_e32 v213, v213, v255\n"
"v_mul_f32_e32 v214, v214, v255\n"
"v_mul_f32_e32 v215, v215, v255\n"
"v_cvt_pk_bf16_f32 v88, v196, v197\n"
"v_cvt_pk_bf16_f32 v89, v198, v199\n"
"v_cvt_pk_bf16_f32 v90, v212, v213\n"
"v_cvt_pk_bf16_f32 v91, v214, v215\n"
"ds_write_b128 v7, v[88:91] offset:50176\n"
".Lmk_b3:\n"
"s_waitcnt vmcnt(18)\n"
"; @B G1 3.0\n"
"v_mfma_f32_16x16x32_bf16 v[88:91], a[0:3], v[116:119], 0\n"
"v_mfma_f32_16x16x32_bf16 v[88:91], a[4:7], v[156:159], v[88:91]\n"
"s_add_u32 s98, s62, 0x40000\n"
"v_mfma_f32_16x16x32_bf16 v[88:91], a[8:11], v[184:187], v[88:91]\n"
"s_add_u32 s96, s54, 0x400000\n"
"buffer_load_dwordx4 v[92:95], v0, s[4:7], s96 offen sc1\n"
"v_mfma_f32_16x16x32_bf16 v[88:91], a[12:15], v[152:155], v[88:91]\n"
"s_waitcnt vmcnt(12)\n"
"v_mfma_f32_16x16x32_bf16 v[88:91], a[16:19], v[160:163], v[88:91]\n"
"buffer_load_dword v255, v10, s[4:7], s98 offen sc1\n"
"v_mfma_f32_16x16x32_bf16 v[88:91], a[20:23], v[224:227], v[88:91]\n"
"s_waitcnt lgkmcnt(2)\n"
"v_add_f32_e32 v112, v112, v192\n"
"v_add_f32_e32 v113, v113, v193\n"
"v_mfma_f32_16x16x32_bf16 v[88:91], a[24:27], v[204:207], v[88:91]\n"
"v_add_f32_e32 v114, v114, v194\n"
"v_add_f32_e32 v115, v115, v195\n"
"v_mfma_f32_16x16x32_bf16 v[88:91], a[28:31], v[132:135], v[88:91]\n"
"buffer_load_dwordx4 v[108:111], v0, s[4:7], s96 offen offset:1024 sc1\n"
"v_mfma_f32_16x16x32_bf16 v[120:123], a[60:63], v[132:135], 0\n"
"s_waitcnt lgkmcnt(0)\n"
"s_barrier\n"
"v_mfma_f32_16x16x32_bf16 v[120:123], a[56:59], v[204:207], v[120:123]\n"
"buffer_load_dwordx4 a[228:231], v0, s[4:7], s97 offen offset:1024 sc1\n"
"v_mfma_f32_16x16x32_bf16 v[120:123], a[52:55], v[224:227], v[120:123]\n"
"buffer_load_dword v253, v10, s[4:7], s98 offen offset:256 sc1\n"
"v_mfma_f32_16x16x32_bf16 v[120:123], a[48:51], v[160:163], v[120:123]\n"
"v_add_f32_e32 v100, v100, v220\n"
"v_add_f32_e32 v101, v101, v221\n"
"v_mfma_f32_16x16x32_bf16 v[120:123], a[44:47], v[152:155], v[120:123]\n"
"buffer_load_dwordx4 v[136:139], v0, s[4:7], s96 offen offset:2048 sc1\n"
"v_mfma_f32_16x16x32_bf16 v[120:123], a[40:43], v[184:187], v[120:123]\n"
"ds_write_b128 v3, v[88:91]\n"
"v_mfma_f32_16x16x32_bf16 v[120:123], a[36:39], v[156:159], v[120:123]\n"
"buffer_load_dword v252, v10, s[4:7], s98 offen offset:512 sc1\n"
"v_mfma_f32_16x16x32_bf16 v[120:123], a[32:35], v[116:119], v[120:123]\n"
"v_add_f32_e32 v102, v102, v222\n"
"v_add_f32_e32 v103, v103, v223\n"
"v_mfma_f32_16x16x32_bf16 v[88:91], a[64:67], v[116:119], 0\n"
"v_mul_f32_e32 v112, v112, v254\n"
"v_mul_f32_e32 v113, v113, v254\n"
"v_mfma_f32_16x16x32_bf16 v[88:91], a[68:71], v[156:159], v[88:91]\n"
"buffer_load_dwordx4 v[144:147], v0, s[4:7], s96 offen offset:3072 sc1\n"
"v_mfma_f32_16x16x32_bf16 v[88:91], a[72:75], v[184:187], v[88:91]\n"
"buffer_load_dword v251, v10, s[4:7], s98 offen offset:768 sc1\n"
"v_mfma_f32_16x16x32_bf16 v[88:91], a[76:79], v[152:155], v[88:91]\n"
"v_mul_f32_e32 v114, v114, v254\n"
"v_mul_f32_e32 v115, v115, v254\n"
"v_mfma_f32_16x16x32_bf16 v[88:91], a[80:83], v[160:163], v[88:91]\n"
"v_mul_f32_e32 v100, v100, v254\n"
"v_mul_f32_e32 v101, v101, v254\n"
"v_add_u32_e32 v250, 0x26c00, v0\n"
"v_mfma_f32_16x16x32_bf16 v[88:91], a[84:87], v[224:227], v[88:91]\n"
"ds_write_b128 v3, v[120:123] offset:1024\n"
"v_mfma_f32_16x16x32_bf16 v[88:91], a[88:91], v[204:207], v[88:91]\n"
"buffer_load_dwordx4 v[120:123], v1, s[4:7], s96 offen sc1\n"
"ds_read_b128 v[164:167], v250\n"
"v_mfma_f32_16x16x32_bf16 v[88:91], a[92:95], v[132:135], v[88:91]\n"
"buffer_load_dwordx4 a[232:235], v0, s[4:7], s97 offen offset:2048 sc1\n"
"ds_read_b128 v[168:171], v250 offset:1024\n"
"v_mfma_f32_16x16x32_bf16 v[172:175], a[124:127], v[132:135], 0\n"
"buffer_load_dword v250, v10, s[4:7], s98 offen offset:1024 sc1\n"
"v_xor_b32_e32 v249, 32, v17\n"
"v_mfma_f32_16x16x32_bf16 v[172:175], a[120:123], v[204:207], v[172:175]\n"
"ds_read_b128 v[176:179], v2 offset:49152\n"
"v_mul_f32_e32 v102, v102, v254\n"
"v_mul_f32_e32 v103, v103, v254\n"
"v_mfma_f32_16x16x32_bf16 v[172:175], a[116:119], v[224:227], v[172:175]\n"
"v_cvt_pk_bf16_f32 v188, v112, v113\n"
"v_cvt_pk_bf16_f32 v189, v114, v115\n"
"v_mfma_f32_16x16x32_bf16 v[172:175], a[112:115], v[160:163], v[172:175]\n"
"ds_read_b128 v[112:115], v17 offset:23552\n"
"buffer_load_dwordx4 v[192:195], v1, s[4:7], s96 offen offset:1024 sc1\n"
"ds_read_b128 v[196:199], v249 offset:23808\n"
"v_mfma_f32_16x16x32_bf16 v[172:175], a[108:111], v[152:155], v[172:175]\n"
"buffer_load_dword v254, v10, s[4:7], s98 offen offset:1280 sc1\n"
"v_mfma_f32_16x16x32_bf16 v[172:175], a[104:107], v[184:187], v[172:175]\n"
"ds_write_b128 v3, v[88:91] offset:2048\n"
"v_mfma_f32_16x16x32_bf16 v[172:175], a[100:103], v[156:159], v[172:175]\n"
"v_cvt_pk_bf16_f32 v190, v100, v101\n"
"v_cvt_pk_bf16_f32 v191, v102, v103\n"
"v_mfma_f32_16x16x32_bf16 v[172:175], a[96:99], v[116:119], v[172:175]\n"
"buffer_load_dword v249, v10, s[4:7], s98 offen offset:1536 sc1\n"
"v_mfma_f32_16x16x32_bf16 v[88:91], v[24:27], v[116:119], 0\n"
"buffer_load_dwordx4 v[100:103], v1, s[4:7], s96 offen offset:2048 sc1\n"
"s_waitcnt lgkmcnt(1)\n"
"v_mfma_f32_16x16x32_bf16 v[164:167], v[112:115], v[176:179], v[164:167]\n"
"v_mfma_f32_16x16x32_bf16 v[168:171], v[196:199], v[176:179], v[168:171]\n"
"v_mfma_f32_16x16x32_bf16 v[88:91], v[28:31], v[156:159], v[88:91]\n"
"ds_write_b128 v2, v[188:191] offset:17408\n"
"v_mfma_f32_16x16x32_bf16 v[88:91], v[32:35], v[184:187], v[88:91]\n"
"buffer_load_dword v248, v10, s[4:7], s98 offen offset:1792 sc1\n"
"v_xor_b32_e32 v247, 32, v16\n"
"v_mfma_f32_16x16x32_bf16 v[88:91], v[36:39], v[152:155], v[88:91]\n"
"ds_read_b64_tr_b16 v[112:113], v16 offset:23552\n"
"ds_read_b64_tr_b16 v[114:115], v247 offset:23808\n"
"ds_read_b64_tr_b16 v[176:177], v247 offset:23552\n"
"v_mfma_f32_16x16x32_bf16 v[88:91], v[40:43], v[160:163], v[88:91]\n"
"buffer_load_dwordx4 a[236:239], v0, s[4:7], s97 offen offset:3072 sc1\n"
"ds_read_b64_tr_b16 v[178:179], v16 offset:23808\n"
"v_mfma_f32_16x16x32_bf16 v[88:91], v[44:47], v[224:227], v[88:91]\n"
"buffer_load_dwordx4 v[188:191], v1, s[4:7], s96 offen offset:3072 sc1\n"
"v_mov_b32_e32 v196, 0x3f803f80\n"
"v_mfma_f32_16x16x32_bf16 v[88:91], v[48:51], v[204:207], v[88:91]\n"
"ds_write_b128 v3, v[172:175] offset:3072\n"
"v_mfma_f32_16x16x32_bf16 v[88:91], v[52:55], v[132:135], v[88:91]\n"
"v_mov_b32_e32 v197, 0x3f803f80\n"
"v_mov_b32_e32 v198, 0x3f803f80\n"
"v_mov_b32_e32 v199, 0x3f803f80\n"
"v_mfma_f32_16x16x32_bf16 v[172:175], v[84:87], v[132:135], 0\n"
"v_max_f32_e32 v247, v164, v165\n"
"v_max3_f32 v247, v247, v166, v167\n"
"v_max3_f32 v247, v247, v168, v169\n"
"v_mfma_f32_16x16x32_bf16 v[172:175], v[80:83], v[204:207], v[172:175]\n"
"v_max3_f32 v247, v247, v170, v171\n"
"v_mov_b32_e32 v246, v247\n"
"v_mfma_f32_16x16x32_bf16 v[172:175], v[76:79], v[224:227], v[172:175]\n"
"s_nop 0\n"
"v_permlane16_swap_b32_e32 v247, v246\n"
"v_max_f32_e32 v247, v247, v246\n"
"v_mov_b32_e32 v246, v247\n"
"v_mfma_f32_16x16x32_bf16 v[172:175], v[72:75], v[160:163], v[172:175]\n"
"s_nop 0\n"
"v_permlane32_swap_b32_e32 v247, v246\n"
"v_max_f32_e32 v247, v247, v246\n"
"v_sub_f32_e32 v164, v164, v247\n"
"v_mfma_f32_16x16x32_bf16 v[172:175], v[68:71], v[152:155], v[172:175]\n"
"v_sub_f32_e32 v165, v165, v247\n"
"v_sub_f32_e32 v166, v166, v247\n"
"v_sub_f32_e32 v167, v167, v247\n"
"v_mfma_f32_16x16x32_bf16 v[172:175], v[64:67], v[184:187], v[172:175]\n"
"ds_write_b128 v3, v[88:91] offset:4096\n"
"v_mfma_f32_16x16x32_bf16 v[172:175], v[60:63], v[156:159], v[172:175]\n"
"v_sub_f32_e32 v168, v168, v247\n"
"v_sub_f32_e32 v169, v169, v247\n"
"v_sub_f32_e32 v170, v170, v247\n"
"v_mfma_f32_16x16x32_bf16 v[172:175], v[56:59], v[116:119], v[172:175]\n"
"s_waitcnt vmcnt(25)\n"
"v_sub_f32_e32 v171, v171, v247\n"
"v_exp_f32_e32 v164, v164\n"
"; @B G1 3.1\n"
"v_mfma_f32_16x16x32_bf16 v[88:91], a[0:3], v[104:107], 0\n"
"v_exp_f32_e32 v165, v165\n"
"v_mfma_f32_16x16x32_bf16 v[88:91], a[4:7], v[140:143], v[88:91]\n"
"buffer_load_dwordx4 a[240:243], v1, s[4:7], s97 offen sc1\n"
"v_mfma_f32_16x16x32_bf16 v[88:91], a[8:11], v[148:151], v[88:91]\n"
"s_add_u32 s98, s55, 0x400000\n"
"buffer_load_dwordx4 v[116:119], v0, s[4:7], s98 offen sc1\n"
"v_mfma_f32_16x16x32_bf16 v[88:91], a[12:15], v[200:203], v[88:91]\n"
"s_waitcnt vmcnt(22)\n"
"v_exp_f32_e32 v166, v166\n"
"v_mfma_f32_16x16x32_bf16 v[88:91], a[16:19], v[96:99], v[88:91]\n"
"v_exp_f32_e32 v167, v167\n"
"v_mfma_f32_16x16x32_bf16 v[88:91], a[20:23], v[124:127], v[88:91]\n"
"ds_write_b128 v3, v[172:175] offset:5120\n"
"v_mfma_f32_16x16x32_bf16 v[88:91], a[24:27], v[128:131], v[88:91]\n"
"v_exp_f32_e32 v168, v168\n"
"v_mfma_f32_16x16x32_bf16 v[88:91], a[28:31], v[180:183], v[88:91]\n"
"buffer_load_dwordx4 v[132:135], v0, s[4:7], s98 offen offset:1024 sc1\n"
"v_mfma_f32_16x16x32_bf16 v[152:155], a[60:63], v[180:183], 0\n"
"v_exp_f32_e32 v169, v169\n"
"v_mfma_f32_16x16x32_bf16 v[152:155], a[56:59], v[128:131], v[152:155]\n"
"v_exp_f32_e32 v170, v170\n"
"v_mfma_f32_16x16x32_bf16 v[152:155], a[52:55], v[124:127], v[152:155]\n"
"v_exp_f32_e32 v171, v171\n"
"v_cvt_pk_bf16_f32 v156, v164, v165\n"
"v_mfma_f32_16x16x32_bf16 v[152:155], a[48:51], v[96:99], v[152:155]\n"
"v_cvt_pk_bf16_f32 v157, v166, v167\n"
"v_cvt_pk_bf16_f32 v158, v168, v169\n"
"v_cvt_pk_bf16_f32 v159, v170, v171\n"
"v_mfma_f32_16x16x32_bf16 v[152:155], a[44:47], v[200:203], v[152:155]\n"
"buffer_load_dwordx4 v[160:163], v0, s[4:7], s98 offen offset:2048 sc1\n"
"s_waitcnt lgkmcnt(3)\n"
"v_mfma_f32_16x16x32_bf16 v[164:167], v[196:199], v[156:159], 0\n"
"v_mfma_f32_16x16x32_bf16 v[168:171], v[112:115], v[156:159], 0\n"
"v_mfma_f32_16x16x32_bf16 v[112:115], v[176:179], v[156:159], 0\n"
"v_mfma_f32_16x16x32_bf16 v[152:155], a[40:43], v[148:151], v[152:155]\n"
"ds_write_b128 v3, v[88:91] offset:6144\n"
"v_mfma_f32_16x16x32_bf16 v[152:155], a[36:39], v[140:143], v[152:155]\n"
"s_add_u32 s96, s73, 0\n"
"buffer_load_dwordx2 v[246:247], off, s[4:7], s96 sc1\n"
"v_mfma_f32_16x16x32_bf16 v[152:155], a[32:35], v[104:107], v[152:155]\n"
"buffer_load_dwordx4 a[244:247], v1, s[4:7], s97 offen offset:1024 sc1\n"
"v_mfma_f32_16x16x32_bf16 v[88:91], a[64:67], v[104:107], 0\n"
"v_mfma_f32_16x16x32_bf16 v[88:91], a[68:71], v[140:143], v[88:91]\n"
"buffer_load_dwordx4 v[156:159], v0, s[4:7], s98 offen offset:3072 sc1\n"
"v_mfma_f32_16x16x32_bf16 v[88:91], a[72:75], v[148:151], v[88:91]\n"
"v_rcp_f32_e32 v164, v164\n"
"v_mfma_f32_16x16x32_bf16 v[88:91], a[76:79], v[200:203], v[88:91]\n"
"v_mul_f32_e32 v168, v168, v164\n"
"v_mul_f32_e32 v169, v169, v164\n"
"v_mul_f32_e32 v170, v170, v164\n"
"v_mfma_f32_16x16x32_bf16 v[88:91], a[80:83], v[96:99], v[88:91]\n"
"v_mov_b32_e32 v172, -1\n"
"v_mov_b32_e32 v173, -1\n"
"v_mul_f32_e32 v171, v171, v164\n"
"v_mfma_f32_16x16x32_bf16 v[88:91], a[84:87], v[124:127], v[88:91]\n"
"ds_write_b128 v3, v[152:155] offset:7168\n"
"v_mfma_f32_16x16x32_bf16 v[88:91], a[88:91], v[128:131], v[88:91]\n"
"buffer_load_dwordx4 v[152:155], v1, s[4:7], s98 offen sc1\n"
"v_mul_f32_e32 v112, v112, v164\n"
"v_mfma_f32_16x16x32_bf16 v[88:91], a[92:95], v[180:183], v[88:91]\n"
"v_mov_b32_e32 v174, -1\n"
"v_mov_b32_e32 v175, -1\n"
"v_mul_f32_e32 v113, v113, v164\n"
"v_mfma_f32_16x16x32_bf16 v[176:179], a[124:127], v[180:183], 0\n"
"s_add_u32 s95, s60, 0x808000\n"
"buffer_store_dwordx4 v[172:175], v0, s[8:11], s95 offen sc1\n"
"v_mfma_f32_16x16x32_bf16 v[176:179], a[120:123], v[128:131], v[176:179]\n"
"v_mul_f32_e32 v114, v114, v164\n"
"v_mul_f32_e32 v115, v115, v164\n"
"v_cvt_pk_bf16_f32 v164, v168, v169\n"
"v_mfma_f32_16x16x32_bf16 v[176:179], a[116:119], v[124:127], v[176:179]\n"
"s_add_u32 s95, s60, 0x440000\n"
"v_cvt_pk_bf16_f32 v165, v170, v171\n"
"v_cvt_pk_bf16_f32 v166, v112, v113\n"
"v_cvt_pk_bf16_f32 v167, v114, v115\n"
"v_mfma_f32_16x16x32_bf16 v[176:179], a[112:115], v[96:99], v[176:179]\n"
"buffer_load_dwordx4 v[112:115], v1, s[4:7], s98 offen offset:1024 sc1\n"
"v_mfma_f32_16x16x32_bf16 v[176:179], a[108:111], v[200:203], v[176:179]\n"
"buffer_load_dwordx4 a[248:251], v1, s[4:7], s97 offen offset:2048 sc1\n"
"v_mfma_f32_16x16x32_bf16 v[176:179], a[104:107], v[148:151], v[176:179]\n"
"ds_write_b128 v3, v[88:91] offset:8192\n"
"v_mfma_f32_16x16x32_bf16 v[176:179], a[100:103], v[140:143], v[176:179]\n"
"buffer_store_dwordx4 v[164:167], v0, s[4:7], s95 offen sc1\n"
"v_mfma_f32_16x16x32_bf16 v[176:179], a[96:99], v[104:107], v[176:179]\n"
";\n"
"v_mfma_f32_16x16x32_bf16 v[88:91], v[24:27], v[104:107], 0\n"
"buffer_load_dwordx4 v[164:167], v1, s[4:7], s98 offen offset:2048 sc1\n"
"v_mfma_f32_16x16x32_bf16 v[88:91], v[28:31], v[140:143], v[88:91]\n"
"v_mfma_f32_16x16x32_bf16 v[88:91], v[32:35], v[148:151], v[88:91]\n"
"v_mfma_f32_16x16x32_bf16 v[88:91], v[36:39], v[200:203], v[88:91]\n"
"v_mfma_f32_16x16x32_bf16 v[88:91], v[40:43], v[96:99], v[88:91]\n"
"s_waitcnt vmcnt(15)\n"
"v_add_f32_e32 v255, v255, v250\n"
"v_add_f32_e32 v253, v253, v254\n"
"v_mfma_f32_16x16x32_bf16 v[88:91], v[44:47], v[124:127], v[88:91]\n"
"buffer_load_dwordx4 v[168:171], v1, s[4:7], s98 offen offset:3072 sc1\n"
"v_mfma_f32_16x16x32_bf16 v[88:91], v[48:51], v[128:131], v[88:91]\n"
"ds_write_b128 v3, v[176:179] offset:9216\n"
"v_mfma_f32_16x16x32_bf16 v[88:91], v[52:55], v[180:183], v[88:91]\n"
"v_add_f32_e32 v252, v252, v249\n"
"v_add_f32_e32 v251, v251, v248\n"
"v_mfma_f32_16x16x32_bf16 v[176:179], v[84:87], v[180:183], 0\n"
"v_add_f32_e32 v255, v255, v252\n"
"v_add_f32_e32 v253, v253, v251\n"
"v_mfma_f32_16x16x32_bf16 v[176:179], v[80:83], v[128:131], v[176:179]\n"
"buffer_load_dwordx4 a[252:255], v1, s[4:7], s97 offen offset:3072 sc1\n"
"v_mfma_f32_16x16x32_bf16 v[176:179], v[76:79], v[124:127], v[176:179]\n"
"v_add_f32_e32 v255, v255, v253\n"
"v_mov_b32_e32 v253, v255\n"
"v_mfma_f32_16x16x32_bf16 v[176:179], v[72:75], v[96:99], v[176:179]\n"
"s_nop 0\n"
"v_permlane32_swap_b32_e32 v255, v253\n"
"v_add_f32_e32 v255, v255, v253\n"
"v_mfma_f32_16x16x32_bf16 v[176:179], v[68:71], v[200:203], v[176:179]\n"
"v_mul_f32_e32 v255, 0x3a800000, v255\n"
"v_mov_b32_e32 v253, v255\n"
"v_mfma_f32_16x16x32_bf16 v[176:179], v[64:67], v[148:151], v[176:179]\n"
"ds_write_b128 v3, v[88:91] offset:10240\n"
"v_mfma_f32_16x16x32_bf16 v[176:179], v[60:63], v[140:143], v[176:179]\n"
"v_permlane16_swap_b32_e32 v255, v253\n"
"v_cvt_pk_bf16_f32 v254, -v255, 0\n"
"v_mfma_f32_16x16x32_bf16 v[176:179], v[56:59], v[104:107], v[176:179]\n"
"v_fma_f32 v253, -v255, v255, v253\n"
"v_lshlrev_b32_e32 v254, 16, v254\n"
"; @B G1 3.2\n"
"v_mfma_f32_16x16x32_bf16 v[88:91], a[0:3], v[92:95], 0\n"
"s_add_u32 s98, s67, 0x10020\n"
"buffer_load_dwordx2 v[250:251], v12, s[4:7], s98 offen sc1\n"
"v_mfma_f32_16x16x32_bf16 v[88:91], a[4:7], v[108:111], v[88:91]\n"
"v_max_f32_e32 v253, 0, v253\n"
"v_cvt_pk_bf16_f32 v253, v253, 0\n"
"v_mfma_f32_16x16x32_bf16 v[88:91], a[8:11], v[136:139], v[88:91]\n"
"v_lshlrev_b32_e32 v253, 16, v253\n"
"v_add_f32_e32 v253, 0x3727c5ac, v253\n"
"v_mfma_f32_16x16x32_bf16 v[88:91], a[12:15], v[144:147], v[88:91]\n"
"s_waitcnt vmcnt(16)\n"
"v_cvt_pk_bf16_f32 v253, v253, 0\n"
"v_lshlrev_b32_e32 v253, 16, v253\n"
"v_mfma_f32_16x16x32_bf16 v[88:91], a[16:19], v[120:123], v[88:91]\n"
"v_sqrt_f32_e32 v253, v253\n"
"v_mfma_f32_16x16x32_bf16 v[88:91], a[20:23], v[192:195], v[88:91]\n"
"ds_write_b128 v3, v[176:179] offset:11264\n"
"v_mfma_f32_16x16x32_bf16 v[88:91], a[24:27], v[100:103], v[88:91]\n"
"v_cvt_pk_bf16_f32 v253, v253, 0\n"
"v_lshlrev_b32_e32 v253, 16, v253\n"
"v_mfma_f32_16x16x32_bf16 v[88:91], a[28:31], v[188:191], v[88:91]\n"
"v_rcp_f32_e32 v255, v253\n"
"v_mfma_f32_16x16x32_bf16 v[96:99], a[60:63], v[188:191], 0\n"
"v_mul_f32_e32 v253, 0x3e8293ee, v255\n"
"v_add_u32_e32 v252, 0x26c00, v0\n"
"v_mfma_f32_16x16x32_bf16 v[96:99], a[56:59], v[100:103], v[96:99]\n"
"ds_read_b128 v[104:107], v252\n"
"ds_read_b128 v[124:127], v252 offset:1024\n"
"v_mfma_f32_16x16x32_bf16 v[96:99], a[52:55], v[192:195], v[96:99]\n"
"s_waitcnt lgkmcnt(0)\n"
"s_barrier\n"
"v_mfma_f32_16x16x32_bf16 v[96:99], a[48:51], v[120:123], v[96:99]\n"
"s_add_u32 s97, s61, 0xc40000\n"
"ds_read_b128 v[128:131], v4\n"
"ds_read_b128 v[140:143], v9 offset:640\n"
"v_mfma_f32_16x16x32_bf16 v[96:99], a[44:47], v[144:147], v[96:99]\n"
"ds_read_b128 v[148:151], v5\n"
"v_add_u32_e32 v252, 0x26c00, v0\n"
"v_mfma_f32_16x16x32_bf16 v[96:99], a[40:43], v[136:139], v[96:99]\n"
"ds_write_b128 v3, v[88:91] offset:12288\n"
"v_mfma_f32_16x16x32_bf16 v[96:99], a[36:39], v[108:111], v[96:99]\n"
"buffer_store_dwordx4 v[172:175], v0, s[8:11], s97 offen sc1\n"
"v_mfma_f32_16x16x32_bf16 v[96:99], a[32:35], v[92:95], v[96:99]\n"
"ds_read_b128 v[88:91], v252\n"
"ds_read_b128 v[176:179], v252 offset:1024\n"
"v_mfma_f32_16x16x32_bf16 v[180:183], a[64:67], v[92:95], 0\n"
"v_mov_b32_e32 v252, 0xff800000\n"
"v_mfma_f32_16x16x32_bf16 v[180:183], a[68:71], v[108:111], v[180:183]\n"
"s_waitcnt lgkmcnt(3)\n"
"v_fmac_f32_e32 v128, v254, v140\n"
"v_fmac_f32_e32 v129, v254, v141\n"
"v_mfma_f32_16x16x32_bf16 v[180:183], a[72:75], v[136:139], v[180:183]\n"
"ds_read_b128 v[184:187], v4 offset:1024\n"
"ds_read_b128 v[196:199], v9 offset:704\n"
"v_mfma_f32_16x16x32_bf16 v[180:183], a[76:79], v[144:147], v[180:183]\n"
"ds_read_b128 v[200:203], v5 offset:1024\n"
"v_fmac_f32_e32 v130, v254, v142\n"
"v_mfma_f32_16x16x32_bf16 v[180:183], a[80:83], v[120:123], v[180:183]\n"
"v_fmac_f32_e32 v131, v254, v143\n"
"v_add_f32_e32 v128, v128, v148\n"
"v_mfma_f32_16x16x32_bf16 v[180:183], a[84:87], v[192:195], v[180:183]\n"
"ds_write_b128 v3, v[96:99] offset:13312\n"
"v_mfma_f32_16x16x32_bf16 v[180:183], a[88:91], v[100:103], v[180:183]\n"
"s_waitcnt lgkmcnt(4)\n"
"v_add_f32_e32 v129, v129, v149\n"
"v_add_f32_e32 v130, v130, v150\n"
"v_mfma_f32_16x16x32_bf16 v[180:183], a[92:95], v[188:191], v[180:183]\n"
"v_add_f32_e32 v131, v131, v151\n"
"v_cndmask_b32_e64 v88, v88, v252, s[82:83]\n"
"v_mfma_f32_16x16x32_bf16 v[96:99], a[124:127], v[188:191], 0\n"
"s_waitcnt vmcnt(12)\n"
"v_cmp_ne_u64_e32 vcc, v[246:247], v[18:19]\n"
"v_mfma_f32_16x16x32_bf16 v[96:99], a[120:123], v[100:103], v[96:99]\n"
"s_cbranch_vccnz .Lmk_f40s\n"
".Lmk_f40r:\n"
"s_mov_b32 m0, 0x7c00\n"
"v_mfma_f32_16x16x32_bf16 v[96:99], a[116:119], v[192:195], v[96:99]\n"
"s_add_u32 s97, s66, 0x3c00\n"
"buffer_load_dwordx4 v0, s[4:7], s97 offen sc1 lds\n"
"v_mfma_f32_16x16x32_bf16 v[96:99], a[112:115], v[120:123], v[96:99]\n"
"s_mov_b32 m0, 0xfc00\n"
"s_add_u32 s97, s66, 0x7c00\n"
"ds_read_b128 v[140:143], v4 offset:2048\n"
"v_mfma_f32_16x16x32_bf16 v[96:99], a[108:111], v[144:147], v[96:99]\n"
"buffer_load_dwordx4 v0, s[4:7], s97 offen sc1 lds\n"
"v_mfma_f32_16x16x32_bf16 v[96:99], a[104:107], v[136:139], v[96:99]\n"
"ds_write_b128 v3, v[180:183] offset:14336\n"
"v_mfma_f32_16x16x32_bf16 v[96:99], a[100:103], v[108:111], v[96:99]\n"
"ds_read_b128 v[148:151], v9 offset:768\n"
"ds_read_b128 v[180:183], v5 offset:2048\n"
"v_mfma_f32_16x16x32_bf16 v[96:99], a[96:99], v[92:95], v[96:99]\n"
"s_waitcnt lgkmcnt(5)\n"
"v_fmac_f32_e32 v184, v254, v196\n"
"v_fmac_f32_e32 v185, v254, v197\n"
"v_mfma_f32_16x16x32_bf16 v[204:207], v[24:27], v[92:95], 0\n"
"ds_read_b128 v[208:211], v4 offset:3072\n"
"ds_read_b128 v[212:215], v9 offset:832\n"
"v_mfma_f32_16x16x32_bf16 v[204:207], v[28:31], v[108:111], v[204:207]\n"
"ds_read_b128 v[216:219], v5 offset:3072\n"
"v_fmac_f32_e32 v186, v254, v198\n"
"v_mfma_f32_16x16x32_bf16 v[204:207], v[32:35], v[136:139], v[204:207]\n"
"v_fmac_f32_e32 v187, v254, v199\n"
"v_add_f32_e32 v184, v184, v200\n"
"v_mfma_f32_16x16x32_bf16 v[204:207], v[36:39], v[144:147], v[204:207]\n"
"v_add_f32_e32 v185, v185, v201\n"
"v_add_f32_e32 v186, v186, v202\n"
"v_mfma_f32_16x16x32_bf16 v[204:207], v[40:43], v[120:123], v[204:207]\n"
"s_waitcnt lgkmcnt(3)\n"
"v_add_f32_e32 v187, v187, v203\n"
"v_fmac_f32_e32 v140, v254, v148\n"
"v_mfma_f32_16x16x32_bf16 v[204:207], v[44:47], v[192:195], v[204:207]\n"
"ds_write_b128 v3, v[96:99] offset:15360\n"
"v_mfma_f32_16x16x32_bf16 v[204:207], v[48:51], v[100:103], v[204:207]\n"
"v_fmac_f32_e32 v141, v254, v149\n"
"v_fmac_f32_e32 v142, v254, v150\n"
"v_mfma_f32_16x16x32_bf16 v[204:207], v[52:55], v[188:191], v[204:207]\n"
"ds_read_b128 v[96:99], v4 offset:4096\n"
"ds_read_b128 v[196:199], v9 offset:512\n"
"v_mfma_f32_16x16x32_bf16 v[200:203], v[84:87], v[188:191], 0\n"
"ds_read_b128 v[188:191], v5 offset:4096\n"
"v_fmac_f32_e32 v143, v254, v151\n"
"v_mfma_f32_16x16x32_bf16 v[200:203], v[80:83], v[100:103], v[200:203]\n"
"v_add_f32_e32 v140, v140, v180\n"
"v_add_f32_e32 v141, v141, v181\n"
"v_mfma_f32_16x16x32_bf16 v[200:203], v[76:79], v[192:195], v[200:203]\n"
"s_waitcnt lgkmcnt(4)\n"
"v_add_f32_e32 v142, v142, v182\n"
"v_add_f32_e32 v143, v143, v183\n"
"v_mfma_f32_16x16x32_bf16 v[200:203], v[72:75], v[120:123], v[200:203]\n"
"v_fmac_f32_e32 v208, v254, v212\n"
"v_fmac_f32_e32 v209, v254, v213\n"
"v_mfma_f32_16x16x32_bf16 v[200:203], v[68:71], v[144:147], v[200:203]\n"
"v_fmac_f32_e32 v210, v254, v214\n"
"v_fmac_f32_e32 v211, v254, v215\n"
"v_mfma_f32_16x16x32_bf16 v[200:203], v[64:67], v[136:139], v[200:203]\n"
"ds_write_b128 v3, v[204:207] offset:16384\n"
"v_mfma_f32_16x16x32_bf16 v[200:203], v[60:63], v[108:111], v[200:203]\n"
"ds_read_b128 v[100:103], v4 offset:5120\n"
"ds_read_b128 v[108:111], v9 offset:576\n"
"v_mfma_f32_16x16x32_bf16 v[200:203], v[56:59], v[92:95], v[200:203]\n"
"s_waitcnt vmcnt(12)\n"
"s_waitcnt vmcnt(3)\n"
"v_cmp_ne_u64_e32 vcc, v[250:251], v[18:19]\n"
"; @B G1 3.3\n"
"v_mfma_f32_16x16x32_bf16 v[128:131], a[0:3], v[116:119], v[128:131]\n"
"s_cbranch_vccnz .Lmk_f41s\n"
".Lmk_f41r:\n"
"ds_read_b128 v[92:95], v5 offset:5120\n"
"v_mfma_f32_16x16x32_bf16 v[128:131], a[4:7], v[132:135], v[128:131]\n"
"s_add_u32 s98, s61, 0xc40000\n"
"s_add_u32 s97, s56, 0x440000\n"
"v_add_f32_e32 v208, v208, v216\n"
"v_mfma_f32_16x16x32_bf16 v[128:131], a[8:11], v[160:163], v[128:131]\n"
"buffer_load_dwordx4 v[120:123], v0, s[4:7], s97 offen sc1\n"
"v_mfma_f32_16x16x32_bf16 v[128:131], a[12:15], v[156:159], v[128:131]\n"
"buffer_store_dwordx4 v[172:175], v0, s[8:11], s98 offen offset:1024 sc1\n"
"v_mfma_f32_16x16x32_bf16 v[128:131], a[16:19], v[152:155], v[128:131]\n"
"s_add_u32 s98, s69, 0\n"
"v_add_f32_e32 v209, v209, v217\n"
"v_add_f32_e32 v210, v210, v218\n"
"v_mfma_f32_16x16x32_bf16 v[128:131], a[20:23], v[112:115], v[128:131]\n"
"ds_write_b128 v3, v[200:203] offset:17408\n"
"v_mfma_f32_16x16x32_bf16 v[128:131], a[24:27], v[164:167], v[128:131]\n"
"buffer_load_dwordx4 v[136:139], v0, s[4:7], s97 offen offset:1024 sc1\n"
"v_mfma_f32_16x16x32_bf16 v[128:131], a[28:31], v[168:171], v[128:131]\n"
"s_waitcnt lgkmcnt(5)\n"
"v_add_f32_e32 v211, v211, v219\n"
"v_fmac_f32_e32 v96, v254, v196\n"
"v_mfma_f32_16x16x32_bf16 v[184:187], a[60:63], v[168:171], v[184:187]\n"
"v_fmac_f32_e32 v97, v254, v197\n"
"v_fmac_f32_e32 v98, v254, v198\n"
"v_mfma_f32_16x16x32_bf16 v[184:187], a[56:59], v[164:167], v[184:187]\n"
"buffer_load_dwordx4 v[144:147], v0, s[4:7], s97 offen offset:2048 sc1\n"
"v_mfma_f32_16x16x32_bf16 v[184:187], a[52:55], v[112:115], v[184:187]\n"
"s_waitcnt lgkmcnt(0)\n"
"s_barrier\n"
"v_mfma_f32_16x16x32_bf16 v[184:187], a[48:51], v[152:155], v[184:187]\n"
"ds_read_b128 v[148:151], v6\n"
"ds_read_b128 v[180:183], v6 offset:1024\n"
"v_mfma_f32_16x16x32_bf16 v[184:187], a[44:47], v[156:159], v[184:187]\n"
"buffer_load_dwordx4 v[192:195], v0, s[4:7], s97 offen offset:3072 sc1\n"
"v_mfma_f32_16x16x32_bf16 v[184:187], a[40:43], v[160:163], v[184:187]\n"
"v_fmac_f32_e32 v99, v254, v199\n"
"v_add_f32_e32 v96, v96, v188\n"
"v_mfma_f32_16x16x32_bf16 v[184:187], a[36:39], v[132:135], v[184:187]\n"
"v_add_f32_e32 v97, v97, v189\n"
"v_add_f32_e32 v98, v98, v190\n"
"v_mfma_f32_16x16x32_bf16 v[184:187], a[32:35], v[116:119], v[184:187]\n"
"buffer_load_dwordx4 v[196:199], v1, s[4:7], s97 offen sc1\n"
"v_mfma_f32_16x16x32_bf16 v[140:143], a[64:67], v[116:119], v[140:143]\n"
"v_add_f32_e32 v99, v99, v191\n"
"v_fmac_f32_e32 v100, v254, v108\n"
"v_mfma_f32_16x16x32_bf16 v[140:143], a[68:71], v[132:135], v[140:143]\n"
"v_fmac_f32_e32 v101, v254, v109\n"
"v_fmac_f32_e32 v102, v254, v110\n"
"v_mfma_f32_16x16x32_bf16 v[140:143], a[72:75], v[160:163], v[140:143]\n"
"buffer_load_dwordx4 v[188:191], v1, s[4:7], s97 offen offset:1024 sc1\n"
"v_mfma_f32_16x16x32_bf16 v[140:143], a[76:79], v[156:159], v[140:143]\n"
"v_fmac_f32_e32 v103, v254, v111\n"
"v_add_f32_e32 v100, v100, v92\n"
"v_mfma_f32_16x16x32_bf16 v[140:143], a[80:83], v[152:155], v[140:143]\n"
"s_waitcnt lgkmcnt(0)\n"
"v_add_f32_e32 v101, v101, v93\n"
"v_add_f32_e32 v102, v102, v94\n"
"v_mfma_f32_16x16x32_bf16 v[140:143], a[84:87], v[112:115], v[140:143]\n"
"buffer_load_dwordx4 v[108:111], v1, s[4:7], s97 offen offset:2048 sc1\n"
"v_mfma_f32_16x16x32_bf16 v[140:143], a[88:91], v[164:167], v[140:143]\n"
"ds_read_b128 v[200:203], v6 offset:2048\n"
"ds_read_b128 v[204:207], v6 offset:3072\n"
"v_mfma_f32_16x16x32_bf16 v[140:143], a[92:95], v[168:171], v[140:143]\n"
"v_add_f32_e32 v103, v103, v95\n"
"v_add_f32_e32 v128, v128, v148\n"
"v_mfma_f32_16x16x32_bf16 v[208:211], a[124:127], v[168:171], v[208:211]\n"
"buffer_load_dwordx4 v[92:95], v1, s[4:7], s97 offen offset:3072 sc1\n"
"v_mfma_f32_16x16x32_bf16 v[208:211], a[120:123], v[164:167], v[208:211]\n"
"s_add_u32 s97, s57, 0x440000\n"
"v_add_f32_e32 v129, v129, v149\n"
"v_add_f32_e32 v130, v130, v150\n"
"v_mfma_f32_16x16x32_bf16 v[208:211], a[116:119], v[112:115], v[208:211]\n"
"v_add_f32_e32 v131, v131, v151\n"
"v_add_f32_e32 v184, v184, v180\n"
"v_mfma_f32_16x16x32_bf16 v[208:211], a[112:115], v[152:155], v[208:211]\n"
"buffer_load_dwordx4 v[148:151], v0, s[4:7], s97 offen sc1\n"
"v_mfma_f32_16x16x32_bf16 v[208:211], a[108:111], v[156:159], v[208:211]\n"
"v_add_f32_e32 v185, v185, v181\n"
"v_add_f32_e32 v186, v186, v182\n"
"v_mfma_f32_16x16x32_bf16 v[208:211], a[104:107], v[160:163], v[208:211]\n"
"v_add_f32_e32 v187, v187, v183\n"
"v_mul_f32_e32 v128, v128, v255\n"
"v_mfma_f32_16x16x32_bf16 v[208:211], a[100:103], v[132:135], v[208:211]\n"
"buffer_load_dwordx4 v[180:183], v0, s[4:7], s97 offen offset:1024 sc1\n"
"v_mfma_f32_16x16x32_bf16 v[208:211], a[96:99], v[116:119], v[208:211]\n"
"v_mul_f32_e32 v129, v129, v255\n"
"v_mul_f32_e32 v130, v130, v255\n"
"v_mfma_f32_16x16x32_bf16 v[96:99], v[24:27], v[116:119], v[96:99]\n"
"v_mul_f32_e32 v131, v131, v255\n"
"v_mul_f32_e32 v184, v184, v255\n"
"v_mfma_f32_16x16x32_bf16 v[96:99], v[28:31], v[132:135], v[96:99]\n"
"buffer_load_dwordx4 v[24:27], v0, s[4:7], s97 offen offset:2048 sc1\n"
"v_mfma_f32_16x16x32_bf16 v[96:99], v[32:35], v[160:163], v[96:99]\n"
"v_mul_f32_e32 v185, v185, v255\n"
"v_mul_f32_e32 v186, v186, v255\n"
"v_mfma_f32_16x16x32_bf16 v[96:99], v[36:39], v[156:159], v[96:99]\n"
"v_mul_f32_e32 v187, v187, v255\n"
"v_cvt_pk_bf16_f32 v28, v128, v129\n"
"v_mfma_f32_16x16x32_bf16 v[96:99], v[40:43], v[152:155], v[96:99]\n"
"buffer_load_dwordx4 v[32:35], v0, s[4:7], s97 offen offset:3072 sc1\n"
"v_mfma_f32_16x16x32_bf16 v[96:99], v[44:47], v[112:115], v[96:99]\n"
"s_waitcnt lgkmcnt(0)\n"
"v_cvt_pk_bf16_f32 v29, v130, v131\n"
"v_cvt_pk_bf16_f32 v30, v184, v185\n"
"v_mfma_f32_16x16x32_bf16 v[96:99], v[48:51], v[164:167], v[96:99]\n"
"s_add_u32 s96, s63, 0xc4000\n"
"ds_read_b128 v[36:39], v6 offset:4096\n"
"ds_read_b128 v[40:43], v6 offset:5120\n"
"v_mfma_f32_16x16x32_bf16 v[96:99], v[52:55], v[168:171], v[96:99]\n"
"buffer_load_dwordx4 v[44:47], v1, s[4:7], s97 offen sc1\n"
"v_mfma_f32_16x16x32_bf16 v[100:103], v[84:87], v[168:171], v[100:103]\n"
"buffer_store_dword v172, v14, s[8:11], s96 offen sc1\n"
"v_mfma_f32_16x16x32_bf16 v[100:103], v[80:83], v[164:167], v[100:103]\n"
"v_cvt_pk_bf16_f32 v31, v186, v187\n"
"v_add_f32_e32 v140, v140, v200\n"
"v_mfma_f32_16x16x32_bf16 v[100:103], v[76:79], v[112:115], v[100:103]\n"
"buffer_load_dwordx4 v[48:51], v1, s[4:7], s97 offen offset:1024 sc1\n"
"ds_write_b128 v7, v[28:31] offset:16384\n"
"v_mfma_f32_16x16x32_bf16 v[100:103], v[72:75], v[152:155], v[100:103]\n"
"v_add_f32_e32 v141, v141, v201\n"
"v_add_f32_e32 v142, v142, v202\n"
"v_mfma_f32_16x16x32_bf16 v[100:103], v[68:71], v[156:159], v[100:103]\n"
"v_add_f32_e32 v143, v143, v203\n"
"v_add_f32_e32 v208, v208, v204\n"
"v_mfma_f32_16x16x32_bf16 v[100:103], v[64:67], v[160:163], v[100:103]\n"
"buffer_load_dwordx4 v[28:31], v1, s[4:7], s97 offen offset:2048 sc1\n"
"v_mfma_f32_16x16x32_bf16 v[100:103], v[60:63], v[132:135], v[100:103]\n"
"v_add_f32_e32 v209, v209, v205\n"
"v_add_f32_e32 v210, v210, v206\n"
"v_mfma_f32_16x16x32_bf16 v[100:103], v[56:59], v[116:119], v[100:103]\n"
"buffer_load_dwordx4 a[0:3], v0, s[8:11], s98 offen sc1\n"
"buffer_load_dwordx4 v[56:59], v1, s[4:7], s97 offen offset:3072 sc1\n"
"v_add_f32_e32 v211, v211, v207\n"
"v_mul_f32_e32 v140, v140, v255\n"
"v_mul_f32_e32 v141, v141, v255\n"
"v_mul_f32_e32 v142, v142, v255\n"
"v_mul_f32_e32 v143, v143, v255\n"
"v_mul_f32_e32 v208, v208, v255\n"
"v_mul_f32_e32 v209, v209, v255\n"
"v_mul_f32_e32 v210, v210, v255\n"
"v_mul_f32_e32 v211, v211, v255\n"
"v_cvt_pk_bf16_f32 v52, v140, v141\n"
"v_cvt_pk_bf16_f32 v53, v142, v143\n"
"v_cvt_pk_bf16_f32 v54, v208, v209\n"
"v_cvt_pk_bf16_f32 v55, v210, v211\n"
"v_cndmask_b32_e64 v89, v89, v252, s[82:83]\n"
"v_cndmask_b32_e64 v90, v90, v252, s[82:83]\n"
"v_cndmask_b32_e64 v91, v91, v252, s[82:83]\n"
"v_cndmask_b32_e64 v176, v176, v252, s[82:83]\n"
"v_cndmask_b32_e64 v177, v177, v252, s[82:83]\n"
"v_cndmask_b32_e64 v178, v178, v252, s[82:83]\n"
"v_cndmask_b32_e64 v179, v179, v252, s[82:83]\n"
"ds_write_b128 v7, v[52:55] offset:49152\n"
"buffer_load_dwordx4 a[4:7], v0, s[8:11], s98 offen offset:1024 sc1\n"
"buffer_load_dwordx4 a[8:11], v0, s[8:11], s98 offen offset:2048 sc1\n"
"buffer_load_dwordx4 a[12:15], v0, s[8:11], s98 offen offset:3072 sc1\n"
"buffer_load_dwordx4 a[16:19], v1, s[8:11], s98 offen sc1\n"
"buffer_load_dwordx4 a[20:23], v1, s[8:11], s98 offen offset:1024 sc1\n"
".Lmk_b4:\n"
"; @B tail\n"
";\n"
"v_xor_b32_e32 v255, 32, v15\n"
"v_xor_b32_e32 v254, 32, v17\n"
"v_xor_b32_e32 v252, 32, v16\n"
"v_xor_b32_e32 v251, 32, v16\n"
"s_waitcnt lgkmcnt(2)\n"
"s_waitcnt vmcnt(24)\n"
"ds_read_b128 v[52:55], v2 offset:17408\n"
"ds_read_b128 v[60:63], v15 offset:16384\n"
"ds_read_b128 v[64:67], v255 offset:16640\n"
"ds_read_b128 v[68:71], v17 offset:15360\n"
"ds_read_b128 v[72:75], v254 offset:15616\n"
"ds_read_b64_tr_b16 v[76:77], v16 offset:16384\n"
"ds_read_b64_tr_b16 v[78:79], v252 offset:16640\n"
"ds_read_b64_tr_b16 v[80:81], v252 offset:16384\n"
"ds_read_b64_tr_b16 v[82:83], v16 offset:16640\n"
"ds_read_b64_tr_b16 v[84:85], v16 offset:15360\n"
"ds_read_b64_tr_b16 v[86:87], v251 offset:15616\n"
"ds_read_b64_tr_b16 v[112:113], v251 offset:15360\n"
"ds_read_b64_tr_b16 v[114:115], v16 offset:15616\n"
"v_add_f32_e32 v96, v96, v36\n"
"v_add_f32_e32 v97, v97, v37\n"
"v_add_f32_e32 v98, v98, v38\n"
"v_add_f32_e32 v99, v99, v39\n"
"v_add_f32_e32 v100, v100, v40\n"
"v_add_f32_e32 v101, v101, v41\n"
"v_add_f32_e32 v102, v102, v42\n"
"v_add_f32_e32 v103, v103, v43\n"
"v_mul_f32_e32 v96, v96, v253\n"
"v_mul_f32_e32 v97, v97, v253\n"
"v_mul_f32_e32 v98, v98, v253\n"
"v_mul_f32_e32 v99, v99, v253\n"
"v_mul_f32_e32 v100, v100, v253\n"
"v_mul_f32_e32 v101, v101, v253\n"
"v_mul_f32_e32 v102, v102, v253\n"
"v_mul_f32_e32 v103, v103, v253\n"
"v_cvt_pk_bf16_f32 v36, v96, v97\n"
"v_cvt_pk_bf16_f32 v37, v98, v99\n"
"v_cvt_pk_bf16_f32 v38, v100, v101\n"
"v_cvt_pk_bf16_f32 v39, v102, v103\n"
";\n"
"; @B AT a\n"
";\n"
"v_mov_b32_e32 v40, 0x3f803f80\n"
"v_mov_b32_e32 v41, 0x3f803f80\n"
"v_mov_b32_e32 v42, 0x3f803f80\n"
"v_mov_b32_e32 v43, 0x3f803f80\n"
"v_mov_b32_e32 v96, 0x3f803f80\n"
"v_mov_b32_e32 v97, 0x3f803f80\n"
"v_mov_b32_e32 v98, 0x3f803f80\n"
"v_mov_b32_e32 v99, 0x3f803f80\n"
"s_waitcnt lgkmcnt(10)\n"
"v_mfma_f32_16x16x32_bf16 v[104:107], v[60:63], v[52:55], v[104:107]\n"
"v_mfma_f32_16x16x32_bf16 v[124:127], v[64:67], v[52:55], v[124:127]\n"
"s_waitcnt lgkmcnt(8)\n"
"v_mfma_f32_16x16x32_bf16 v[88:91], v[68:71], v[36:39], v[88:91]\n"
"v_mfma_f32_16x16x32_bf16 v[176:179], v[72:75], v[36:39], v[176:179]\n"
"s_waitcnt lgkmcnt(4)\n"
"s_nop 2\n"
"v_max_f32_e32 v255, v104, v105\n"
"s_nop 1\n"
"v_max_f32_e32 v254, v88, v89\n"
"v_max3_f32 v255, v255, v106, v107\n"
"v_max3_f32 v254, v254, v90, v91\n"
"v_max3_f32 v255, v255, v124, v125\n"
"v_max3_f32 v254, v254, v176, v177\n"
"v_max3_f32 v255, v255, v126, v127\n"
"v_max3_f32 v254, v254, v178, v179\n"
"v_mov_b32_e32 v253, v255\n"
"v_mov_b32_e32 v252, v254\n"
"s_nop 0\n"
"v_permlane16_swap_b32_e32 v255, v253\n"
"v_permlane16_swap_b32_e32 v254, v252\n"
"v_max_f32_e32 v255, v255, v253\n"
"v_max_f32_e32 v254, v254, v252\n"
"v_mov_b32_e32 v253, v255\n"
"v_mov_b32_e32 v252, v254\n"
"s_nop 0\n"
"v_permlane32_swap_b32_e32 v255, v253\n"
"v_permlane32_swap_b32_e32 v254, v252\n"
"v_max_f32_e32 v255, v255, v253\n"
"v_max_f32_e32 v254, v254, v252\n"
"v_sub_f32_e32 v104, v104, v255\n"
"v_sub_f32_e32 v105, v105, v255\n"
"v_sub_f32_e32 v106, v106, v255\n"
"v_sub_f32_e32 v107, v107, v255\n"
"v_sub_f32_e32 v124, v124, v255\n"
"v_sub_f32_e32 v125, v125, v255\n"
"v_sub_f32_e32 v126, v126, v255\n"
"v_sub_f32_e32 v127, v127, v255\n"
"v_exp_f32_e32 v104, v104\n"
"v_exp_f32_e32 v105, v105\n"
"v_exp_f32_e32 v106, v106\n"
"v_exp_f32_e32 v107, v107\n"
"v_exp_f32_e32 v124, v124\n"
"v_exp_f32_e32 v125, v125\n"
"v_exp_f32_e32 v126, v126\n"
"v_exp_f32_e32 v127, v127\n"
"v_cvt_pk_bf16_f32 v36, v104, v105\n"
"v_cvt_pk_bf16_f32 v37, v106, v107\n"
"v_cvt_pk_bf16_f32 v38, v124, v125\n"
"v_cvt_pk_bf16_f32 v39, v126, v127\n"
"v_sub_f32_e32 v88, v88, v254\n"
"v_sub_f32_e32 v89, v89, v254\n"
"v_sub_f32_e32 v90, v90, v254\n"
"v_sub_f32_e32 v91, v91, v254\n"
"v_sub_f32_e32 v176, v176, v254\n"
"v_sub_f32_e32 v177, v177, v254\n"
"v_sub_f32_e32 v178, v178, v254\n"
"v_mfma_f32_16x16x32_bf16 v[52:55], v[40:43], v[36:39], 0\n"
"v_sub_f32_e32 v179, v179, v254\n"
"v_exp_f32_e32 v88, v88\n"
"v_exp_f32_e32 v89, v89\n"
"v_exp_f32_e32 v90, v90\n"
"v_mfma_f32_16x16x32_bf16 v[40:43], v[76:79], v[36:39], 0\n"
"v_exp_f32_e32 v91, v91\n"
"v_exp_f32_e32 v176, v176\n"
"v_exp_f32_e32 v177, v177\n"
"v_exp_f32_e32 v178, v178\n"
"v_mfma_f32_16x16x32_bf16 v[60:63], v[80:83], v[36:39], 0\n"
"s_waitcnt lgkmcnt(0)\n"
"v_exp_f32_e32 v179, v179\n"
"v_cvt_pk_bf16_f32 v36, v88, v89\n"
"v_cvt_pk_bf16_f32 v37, v90, v91\n"
"v_cvt_pk_bf16_f32 v38, v176, v177\n"
"v_cvt_pk_bf16_f32 v39, v178, v179\n"
"v_rcp_f32_e32 v52, v52\n"
"s_nop 0\n"
"v_mul_f32_e32 v40, v40, v52\n"
"v_mul_f32_e32 v41, v41, v52\n"
"v_mfma_f32_16x16x32_bf16 v[64:67], v[96:99], v[36:39], 0\n"
"v_mul_f32_e32 v42, v42, v52\n"
"v_mul_f32_e32 v43, v43, v52\n"
"v_mul_f32_e32 v60, v60, v52\n"
"v_mfma_f32_16x16x32_bf16 v[68:71], v[84:87], v[36:39], 0\n"
"v_mul_f32_e32 v61, v61, v52\n"
"v_mul_f32_e32 v62, v62, v52\n"
"v_mul_f32_e32 v63, v63, v52\n"
"v_cvt_pk_bf16_f32 v52, v40, v41\n"
"v_mfma_f32_16x16x32_bf16 v[72:75], v[112:115], v[36:39], 0\n"
"s_add_u32 s97, s60, 0x408000\n"
"v_cvt_pk_bf16_f32 v53, v42, v43\n"
"v_cvt_pk_bf16_f32 v54, v60, v61\n"
"v_cvt_pk_bf16_f32 v55, v62, v63\n"
"buffer_store_dwordx4 v[52:55], v0, s[4:7], s97 offen sc1\n"
"s_add_u32 s97, s60, 0x400000\n"
"v_rcp_f32_e32 v64, v64\n"
"s_nop 0\n"
"v_mul_f32_e32 v68, v68, v64\n"
"v_mul_f32_e32 v69, v69, v64\n"
"v_mul_f32_e32 v70, v70, v64\n"
"v_mul_f32_e32 v71, v71, v64\n"
"v_mul_f32_e32 v72, v72, v64\n"
"v_mul_f32_e32 v73, v73, v64\n"
"v_mul_f32_e32 v74, v74, v64\n"
"v_mul_f32_e32 v75, v75, v64\n"
"v_cvt_pk_bf16_f32 v36, v68, v69\n"
"v_cvt_pk_bf16_f32 v37, v70, v71\n"
"v_cvt_pk_bf16_f32 v38, v72, v73\n"
"v_cvt_pk_bf16_f32 v39, v74, v75\n"
"buffer_store_dwordx4 v[36:39], v0, s[4:7], s97 offen sc1\n"
";\n"
"s_cmp_lg_u32 s0, 0\n"
"s_cbranch_scc1 .Lmk_exit\n"
"s_add_u32 s97, s58, 0x440000\n"
"buffer_load_dwordx4 v[36:39], v0, s[4:7], s97 offen sc1\n"
"buffer_load_dwordx4 v[40:43], v0, s[4:7], s97 offen offset:1024 sc1\n"
"buffer_load_dwordx4 v[52:55], v0, s[4:7], s97 offen offset:2048 sc1\n"
"buffer_load_dwordx4 v[60:63], v0, s[4:7], s97 offen offset:3072 sc1\n"
"buffer_load_dwordx4 v[64:67], v1, s[4:7], s97 offen sc1\n"
"buffer_load_dwordx4 v[68:71], v1, s[4:7], s97 offen offset:1024 sc1\n"
"buffer_load_dwordx4 v[72:75], v1, s[4:7], s97 offen offset:2048 sc1\n"
"buffer_load_dwordx4 v[76:79], v1, s[4:7], s97 offen offset:3072 sc1\n"
"buffer_load_dwordx4 a[24:27], v1, s[8:11], s98 offen offset:2048 sc1\n"
"buffer_load_dwordx4 a[28:31], v1, s[8:11], s98 offen offset:3072 sc1\n"
"s_add_u32 s98, s69, 0x2000\n"
"buffer_load_dwordx4 a[32:35], v0, s[8:11], s98 offen sc1\n"
"buffer_load_dwordx4 a[36:39], v0, s[8:11], s98 offen offset:1024 sc1\n"
"buffer_load_dwordx4 a[40:43], v0, s[8:11], s98 offen offset:2048 sc1\n"
"buffer_load_dwordx4 a[44:47], v0, s[8:11], s98 offen offset:3072 sc1\n"
"buffer_load_dwordx4 a[48:51], v1, s[8:11], s98 offen sc1\n"
"buffer_load_dwordx4 a[52:55], v1, s[8:11], s98 offen offset:1024 sc1\n"
"s_waitcnt vmcnt(39)\n"
"; @B G2 b.0\n"
"v_mfma_f32_16x16x32_bf16 v[80:83], a[128:131], v[120:123], 0\n"
"v_mfma_f32_16x16x32_bf16 v[80:83], a[132:135], v[136:139], v[80:83]\n"
"s_waitcnt vmcnt(37)\n"
"v_mfma_f32_16x16x32_bf16 v[80:83], a[136:139], v[144:147], v[80:83]\n"
"s_add_u32 s97, s59, 0x440000\n"
"buffer_load_dwordx4 v[84:87], v0, s[4:7], s97 offen sc1\n"
"v_mfma_f32_16x16x32_bf16 v[80:83], a[140:143], v[192:195], v[80:83]\n"
"s_waitcnt vmcnt(36)\n"
"v_mfma_f32_16x16x32_bf16 v[80:83], a[144:147], v[196:199], v[80:83]\n"
"v_mfma_f32_16x16x32_bf16 v[80:83], a[148:151], v[188:191], v[80:83]\n"
"s_waitcnt vmcnt(34)\n"
"buffer_load_dwordx4 v[88:91], v0, s[4:7], s97 offen offset:1024 sc1\n"
"v_mfma_f32_16x16x32_bf16 v[80:83], a[152:155], v[108:111], v[80:83]\n"
"v_mfma_f32_16x16x32_bf16 v[80:83], a[156:159], v[92:95], v[80:83]\n"
"v_mfma_f32_16x16x32_bf16 v[96:99], a[188:191], v[92:95], 0\n"
"buffer_load_dwordx4 v[100:103], v0, s[4:7], s97 offen offset:2048 sc1\n"
"v_mfma_f32_16x16x32_bf16 v[96:99], a[184:187], v[108:111], v[96:99]\n"
"v_mfma_f32_16x16x32_bf16 v[96:99], a[180:183], v[188:191], v[96:99]\n"
"v_mfma_f32_16x16x32_bf16 v[96:99], a[176:179], v[196:199], v[96:99]\n"
"buffer_load_dwordx4 v[104:107], v0, s[4:7], s97 offen offset:3072 sc1\n"
"v_mfma_f32_16x16x32_bf16 v[96:99], a[172:175], v[192:195], v[96:99]\n"
"s_nop 0\n"
"v_cmp_u_f32_e32 vcc, v80, v80\n"
"v_mfma_f32_16x16x32_bf16 v[96:99], a[168:171], v[144:147], v[96:99]\n"
"s_cbranch_vccnz .Lmk_p63s\n"
".Lmk_p63r:\n"
"v_mfma_f32_16x16x32_bf16 v[96:99], a[164:167], v[136:139], v[96:99]\n"
"ds_write_b128 v3, v[80:83]\n"
"v_mfma_f32_16x16x32_bf16 v[96:99], a[160:163], v[120:123], v[96:99]\n"
"buffer_load_dwordx4 v[80:83], v1, s[4:7], s97 offen sc1\n"
"v_mfma_f32_16x16x32_bf16 v[112:115], a[192:195], v[120:123], 0\n"
"v_mfma_f32_16x16x32_bf16 v[112:115], a[196:199], v[136:139], v[112:115]\n"
"v_mfma_f32_16x16x32_bf16 v[112:115], a[200:203], v[144:147], v[112:115]\n"
"buffer_load_dwordx4 v[116:119], v1, s[4:7], s97 offen offset:1024 sc1\n"
"v_mfma_f32_16x16x32_bf16 v[112:115], a[204:207], v[192:195], v[112:115]\n"
"v_mfma_f32_16x16x32_bf16 v[112:115], a[208:211], v[196:199], v[112:115]\n"
"v_mfma_f32_16x16x32_bf16 v[112:115], a[212:215], v[188:191], v[112:115]\n"
"ds_write_b128 v3, v[96:99] offset:1024\n"
"v_mfma_f32_16x16x32_bf16 v[112:115], a[216:219], v[108:111], v[112:115]\n"
"buffer_load_dwordx4 v[96:99], v1, s[4:7], s97 offen offset:2048 sc1\n"
"v_mfma_f32_16x16x32_bf16 v[112:115], a[220:223], v[92:95], v[112:115]\n"
"v_mfma_f32_16x16x32_bf16 v[124:127], a[252:255], v[92:95], 0\n"
"v_mfma_f32_16x16x32_bf16 v[124:127], a[248:251], v[108:111], v[124:127]\n"
"buffer_load_dwordx4 v[92:95], v1, s[4:7], s97 offen offset:3072 sc1\n"
"v_mfma_f32_16x16x32_bf16 v[124:127], a[244:247], v[188:191], v[124:127]\n"
"v_mfma_f32_16x16x32_bf16 v[124:127], a[240:243], v[196:199], v[124:127]\n"
"v_mfma_f32_16x16x32_bf16 v[124:127], a[236:239], v[192:195], v[124:127]\n"
"s_mov_b32 s97, .Lmk_a0-.Lmk_g67\n"
"s_getpc_b64 s[84:85]\n"
".Lmk_g67:\n"
"s_add_u32 s84, s84, s97\n"
"s_addc_u32 s85, s85, 0\n"
"s_and_b32 s84, s84, -16\n"
"v_mfma_f32_16x16x32_bf16 v[124:127], a[232:235], v[144:147], v[124:127]\n"
"ds_write_b128 v3, v[112:115] offset:2048\n"
"v_mfma_f32_16x16x32_bf16 v[124:127], a[228:231], v[136:139], v[124:127]\n"
"s_and_b32 s85, s85, 0xffff\n"
"s_sub_u32 s97, .Lmk_end-.Lmk_g67, s97\n"
"s_min_u32 s86, s97, 0x3000\n"
"s_mov_b32 s87, 0x20000\n"
"v_mfma_f32_16x16x32_bf16 v[124:127], a[224:227], v[120:123], v[124:127]\n"
"s_waitcnt vmcnt(37)\n"
"s_mov_b32 m0, 0x27800\n"
"s_nop 0\n"
"buffer_load_dwordx4 v0, s[84:87], s99 offen lds\n"
"; @B G2 b.1\n"
"v_mfma_f32_16x16x32_bf16 v[108:111], a[128:131], v[148:151], 0\n"
"v_mfma_f32_16x16x32_bf16 v[108:111], a[132:135], v[180:183], v[108:111]\n"
"v_mfma_f32_16x16x32_bf16 v[108:111], a[136:139], v[24:27], v[108:111]\n"
"v_mfma_f32_16x16x32_bf16 v[108:111], a[140:143], v[32:35], v[108:111]\n"
"s_waitcnt vmcnt(32)\n"
"v_mfma_f32_16x16x32_bf16 v[108:111], a[144:147], v[44:47], v[108:111]\n"
"v_mfma_f32_16x16x32_bf16 v[108:111], a[148:151], v[48:51], v[108:111]\n"
"ds_write_b128 v3, v[124:127] offset:3072\n"
"v_mfma_f32_16x16x32_bf16 v[108:111], a[152:155], v[28:31], v[108:111]\n"
"v_mfma_f32_16x16x32_bf16 v[108:111], a[156:159], v[56:59], v[108:111]\n"
"v_mfma_f32_16x16x32_bf16 v[112:115], a[188:191], v[56:59], 0\n"
"s_add_u32 s97, s56, 0x400000\n"
"buffer_load_dwordx4 v[120:123], v0, s[4:7], s97 offen sc1\n"
"v_mfma_f32_16x16x32_bf16 v[112:115], a[184:187], v[28:31], v[112:115]\n"
"v_mfma_f32_16x16x32_bf16 v[112:115], a[180:183], v[48:51], v[112:115]\n"
"v_mfma_f32_16x16x32_bf16 v[112:115], a[176:179], v[44:47], v[112:115]\n"
"buffer_load_dwordx4 v[124:127], v0, s[4:7], s97 offen offset:1024 sc1\n"
"v_mfma_f32_16x16x32_bf16 v[112:115], a[172:175], v[32:35], v[112:115]\n"
"v_cmp_u_f32_e32 vcc, v108, v108\n"
"v_mfma_f32_16x16x32_bf16 v[112:115], a[168:171], v[24:27], v[112:115]\n"
"s_cbranch_vccnz .Lmk_p64s\n"
".Lmk_p64r:\n"
"v_mfma_f32_16x16x32_bf16 v[112:115], a[164:167], v[180:183], v[112:115]\n"
"ds_write_b128 v3, v[108:111] offset:6144\n"
"v_mfma_f32_16x16x32_bf16 v[112:115], a[160:163], v[148:151], v[112:115]\n"
"buffer_load_dwordx4 v[108:111], v0, s[4:7], s97 offen offset:2048 sc1\n"
"v_mfma_f32_16x16x32_bf16 v[128:131], a[192:195], v[148:151], 0\n"
"v_mfma_f32_16x16x32_bf16 v[128:131], a[196:199], v[180:183], v[128:131]\n"
"v_mfma_f32_16x16x32_bf16 v[128:131], a[200:203], v[24:27], v[128:131]\n"
"buffer_load_dwordx4 v[132:135], v0, s[4:7], s97 offen offset:3072 sc1\n"
"v_mfma_f32_16x16x32_bf16 v[128:131], a[204:207], v[32:35], v[128:131]\n"
"v_mfma_f32_16x16x32_bf16 v[128:131], a[208:211], v[44:47], v[128:131]\n"
"ds_read_b128 v[136:139], v2 offset:8192\n"
"ds_read_b128 v[140:143], v2 offset:9216\n"
"v_mfma_f32_16x16x32_bf16 v[128:131], a[212:215], v[48:51], v[128:131]\n"
"ds_write_b128 v3, v[112:115] offset:7168\n"
"v_mfma_f32_16x16x32_bf16 v[128:131], a[216:219], v[28:31], v[128:131]\n"
"buffer_load_dwordx4 v[112:115], v1, s[4:7], s97 offen sc1\n"
"v_mfma_f32_16x16x32_bf16 v[128:131], a[220:223], v[56:59], v[128:131]\n"
"v_mfma_f32_16x16x32_bf16 v[144:147], a[252:255], v[56:59], 0\n"
"v_mfma_f32_16x16x32_bf16 v[144:147], a[248:251], v[28:31], v[144:147]\n"
"buffer_load_dwordx4 v[28:31], v1, s[4:7], s97 offen offset:1024 sc1\n"
"s_waitcnt lgkmcnt(1)\n"
"v_mfma_f32_16x16x32_bf16 v[144:147], a[244:247], v[48:51], v[144:147]\n"
"v_lshlrev_b32_e32 v255, 16, v136\n"
"v_and_b32_e32 v254, 0xffff0000, v136\n"
"v_lshlrev_b32_e32 v253, 16, v137\n"
"v_mfma_f32_16x16x32_bf16 v[144:147], a[240:243], v[44:47], v[144:147]\n"
"v_and_b32_e32 v252, 0xffff0000, v137\n"
"v_lshlrev_b32_e32 v251, 16, v138\n"
"v_and_b32_e32 v250, 0xffff0000, v138\n"
"v_mfma_f32_16x16x32_bf16 v[144:147], a[236:239], v[32:35], v[144:147]\n"
"buffer_load_dwordx4 v[32:35], v1, s[4:7], s97 offen offset:2048 sc1\n"
"v_lshlrev_b32_e32 v249, 16, v139\n"
"v_mfma_f32_16x16x32_bf16 v[144:147], a[232:235], v[24:27], v[144:147]\n"
"ds_write_b128 v3, v[128:131] offset:8192\n"
"v_mfma_f32_16x16x32_bf16 v[144:147], a[228:231], v[180:183], v[144:147]\n"
"v_and_b32_e32 v248, 0xffff0000, v139\n"
"v_lshlrev_b32_e32 v247, 16, v140\n"
"v_and_b32_e32 v246, 0xffff0000, v140\n"
"v_mfma_f32_16x16x32_bf16 v[144:147], a[224:227], v[148:151], v[144:147]\n"
"s_waitcnt vmcnt(28)\n"
"buffer_load_dwordx4 v[24:27], v1, s[4:7], s97 offen offset:3072 sc1\n"
"; @B G2 b.2\n"
"v_mfma_f32_16x16x32_bf16 v[44:47], a[128:131], v[36:39], 0\n"
"s_add_u32 s97, s57, 0x400000\n"
"v_lshlrev_b32_e32 v245, 16, v141\n"
"v_and_b32_e32 v244, 0xffff0000, v141\n"
"v_lshlrev_b32_e32 v243, 16, v142\n"
"v_mfma_f32_16x16x32_bf16 v[44:47], a[132:135], v[40:43], v[44:47]\n"
"v_and_b32_e32 v242, 0xffff0000, v142\n"
"v_lshlrev_b32_e32 v241, 16, v143\n"
"v_and_b32_e32 v240, 0xffff0000, v143\n"
"v_mfma_f32_16x16x32_bf16 v[44:47], a[136:139], v[52:55], v[44:47]\n"
"buffer_load_dwordx4 v[48:51], v0, s[4:7], s97 offen sc1\n"
"v_mfma_f32_16x16x32_bf16 v[44:47], a[140:143], v[60:63], v[44:47]\n"
"s_waitcnt vmcnt(26)\n"
"v_mfma_f32_16x16x32_bf16 v[44:47], a[144:147], v[64:67], v[44:47]\n"
"v_mfma_f32_16x16x32_bf16 v[44:47], a[148:151], v[68:71], v[44:47]\n"
"ds_write_b128 v3, v[144:147] offset:9216\n"
"v_mfma_f32_16x16x32_bf16 v[44:47], a[152:155], v[72:75], v[44:47]\n"
"buffer_load_dwordx4 v[56:59], v0, s[4:7], s97 offen offset:1024 sc1\n"
"v_mfma_f32_16x16x32_bf16 v[44:47], a[156:159], v[76:79], v[44:47]\n"
"v_mfma_f32_16x16x32_bf16 v[128:131], a[188:191], v[76:79], 0\n"
"v_mfma_f32_16x16x32_bf16 v[128:131], a[184:187], v[72:75], v[128:131]\n"
"s_waitcnt lgkmcnt(0)\n"
"s_barrier\n"
"v_mfma_f32_16x16x32_bf16 v[128:131], a[180:183], v[68:71], v[128:131]\n"
"buffer_load_dwordx4 v[136:139], v0, s[4:7], s97 offen offset:2048 sc1\n"
"v_mfma_f32_16x16x32_bf16 v[128:131], a[176:179], v[64:67], v[128:131]\n"
"ds_read_b128 v[140:143], v4\n"
"ds_read_b128 v[144:147], v5\n"
"v_mfma_f32_16x16x32_bf16 v[128:131], a[172:175], v[60:63], v[128:131]\n"
"v_cmp_u_f32_e32 vcc, v44, v44\n"
"v_mfma_f32_16x16x32_bf16 v[128:131], a[168:171], v[52:55], v[128:131]\n"
"s_cbranch_vccnz .Lmk_p65s\n"
".Lmk_p65r:\n"
"v_mfma_f32_16x16x32_bf16 v[128:131], a[164:167], v[40:43], v[128:131]\n"
"ds_write_b128 v3, v[44:47] offset:12288\n"
"v_mfma_f32_16x16x32_bf16 v[128:131], a[160:163], v[36:39], v[128:131]\n"
"buffer_load_dwordx4 v[44:47], v0, s[4:7], s97 offen offset:3072 sc1\n"
"v_mfma_f32_16x16x32_bf16 v[148:151], a[192:195], v[36:39], 0\n"
"ds_read_b128 v[152:155], v4 offset:1024\n"
"ds_read_b128 v[156:159], v5 offset:1024\n"
"s_waitcnt lgkmcnt(3)\n"
"v_mfma_f32_16x16x32_bf16 v[148:151], a[196:199], v[40:43], v[148:151]\n"
"v_add_f32_e32 v140, v140, v255\n"
"v_add_f32_e32 v141, v141, v254\n"
"v_add_f32_e32 v142, v142, v253\n"
"v_mfma_f32_16x16x32_bf16 v[148:151], a[200:203], v[52:55], v[148:151]\n"
"buffer_load_dwordx4 v[160:163], v1, s[4:7], s97 offen sc1\n"
"v_add_f32_e32 v143, v143, v252\n"
"v_mfma_f32_16x16x32_bf16 v[148:151], a[204:207], v[60:63], v[148:151]\n"
"v_add_f32_e32 v140, v140, v144\n"
"v_add_f32_e32 v141, v141, v145\n"
"v_add_f32_e32 v142, v142, v146\n"
"v_mfma_f32_16x16x32_bf16 v[148:151], a[208:211], v[64:67], v[148:151]\n"
"v_add_f32_e32 v143, v143, v147\n"
"v_mfma_f32_16x16x32_bf16 v[148:151], a[212:215], v[68:71], v[148:151]\n"
"ds_write_b128 v3, v[128:131] offset:13312\n"
"v_mfma_f32_16x16x32_bf16 v[148:151], a[216:219], v[72:75], v[148:151]\n"
"buffer_load_dwordx4 v[128:131], v1, s[4:7], s97 offen offset:1024 sc1\n"
"s_waitcnt lgkmcnt(1)\n"
"v_mfma_f32_16x16x32_bf16 v[148:151], a[220:223], v[76:79], v[148:151]\n"
"ds_read_b128 v[144:147], v4 offset:2048\n"
"ds_read_b128 v[164:167], v5 offset:2048\n"
"v_add_f32_e32 v152, v152, v251\n"
"v_mfma_f32_16x16x32_bf16 v[168:171], a[252:255], v[76:79], 0\n"
"v_add_f32_e32 v153, v153, v250\n"
"v_add_f32_e32 v154, v154, v249\n"
"v_add_f32_e32 v155, v155, v248\n"
"v_mfma_f32_16x16x32_bf16 v[168:171], a[248:251], v[72:75], v[168:171]\n"
"buffer_load_dwordx4 v[72:75], v1, s[4:7], s97 offen offset:2048 sc1\n"
"v_add_f32_e32 v152, v152, v156\n"
"v_mfma_f32_16x16x32_bf16 v[168:171], a[244:247], v[68:71], v[168:171]\n"
"v_add_f32_e32 v153, v153, v157\n"
"v_add_f32_e32 v154, v154, v158\n"
"v_add_f32_e32 v155, v155, v159\n"
"v_mfma_f32_16x16x32_bf16 v[168:171], a[240:243], v[64:67], v[168:171]\n"
"v_mfma_f32_16x16x32_bf16 v[168:171], a[236:239], v[60:63], v[168:171]\n"
"buffer_load_dwordx4 v[60:63], v1, s[4:7], s97 offen offset:3072 sc1\n"
"s_waitcnt lgkmcnt(0)\n"
"v_mfma_f32_16x16x32_bf16 v[168:171], a[232:235], v[52:55], v[168:171]\n"
"ds_write_b128 v3, v[148:151] offset:14336\n"
"v_mfma_f32_16x16x32_bf16 v[168:171], a[228:231], v[40:43], v[168:171]\n"
"s_add_u32 s97, s58, 0x400000\n"
"ds_read_b128 v[40:43], v4 offset:3072\n"
"ds_read_b128 v[52:55], v5 offset:3072\n"
"v_add_f32_e32 v144, v144, v247\n"
"v_mfma_f32_16x16x32_bf16 v[168:171], a[224:227], v[36:39], v[168:171]\n"
"s_waitcnt vmcnt(21)\n"
"buffer_load_dwordx4 v[36:39], v0, s[4:7], s97 offen sc1\n"
"; @B G2 b.3\n"
"v_mfma_f32_16x16x32_bf16 v[64:67], a[128:131], v[84:87], v[140:143]\n"
"v_add_f32_e32 v145, v145, v246\n"
"v_add_f32_e32 v146, v146, v245\n"
"v_add_f32_e32 v147, v147, v244\n"
"v_mfma_f32_16x16x32_bf16 v[64:67], a[132:135], v[88:91], v[64:67]\n"
"v_add_f32_e32 v144, v144, v164\n"
"v_add_f32_e32 v145, v145, v165\n"
"v_add_f32_e32 v146, v146, v166\n"
"v_mfma_f32_16x16x32_bf16 v[64:67], a[136:139], v[100:103], v[64:67]\n"
"buffer_load_dwordx4 v[68:71], v0, s[4:7], s97 offen offset:1024 sc1\n"
"v_add_f32_e32 v147, v147, v167\n"
"v_mfma_f32_16x16x32_bf16 v[64:67], a[140:143], v[104:107], v[64:67]\n"
"s_waitcnt vmcnt(19)\n"
"s_waitcnt lgkmcnt(0)\n"
"v_add_f32_e32 v40, v40, v243\n"
"v_add_f32_e32 v41, v41, v242\n"
"v_mfma_f32_16x16x32_bf16 v[64:67], a[144:147], v[80:83], v[64:67]\n"
"v_add_f32_e32 v42, v42, v241\n"
"v_add_f32_e32 v43, v43, v240\n"
"v_add_f32_e32 v40, v40, v52\n"
"v_mfma_f32_16x16x32_bf16 v[64:67], a[148:151], v[116:119], v[64:67]\n"
"ds_write_b128 v3, v[168:171] offset:15360\n"
"v_mfma_f32_16x16x32_bf16 v[64:67], a[152:155], v[96:99], v[64:67]\n"
"buffer_load_dwordx4 v[76:79], v0, s[4:7], s97 offen offset:2048 sc1\n"
"v_add_f32_e32 v41, v41, v53\n"
"v_mfma_f32_16x16x32_bf16 v[64:67], a[156:159], v[92:95], v[64:67]\n"
"v_add_f32_e32 v42, v42, v54\n"
"v_add_f32_e32 v43, v43, v55\n"
"v_mfma_f32_16x16x32_bf16 v[52:55], a[188:191], v[92:95], v[152:155]\n"
"v_mfma_f32_16x16x32_bf16 v[52:55], a[184:187], v[96:99], v[52:55]\n"
"s_waitcnt lgkmcnt(0)\n"
"s_barrier\n"
"v_mfma_f32_16x16x32_bf16 v[52:55], a[180:183], v[116:119], v[52:55]\n"
"buffer_load_dwordx4 v[148:151], v0, s[4:7], s97 offen offset:3072 sc1\n"
"v_mfma_f32_16x16x32_bf16 v[52:55], a[176:179], v[80:83], v[52:55]\n"
"ds_read_b128 v[156:159], v6\n"
"ds_read_b128 v[164:167], v6 offset:1024\n"
"v_mfma_f32_16x16x32_bf16 v[52:55], a[172:175], v[104:107], v[52:55]\n"
"ds_read_b128 v[168:171], v6 offset:2048\n"
"v_cmp_u_f32_e32 vcc, v64, v64\n"
"v_mfma_f32_16x16x32_bf16 v[52:55], a[168:171], v[100:103], v[52:55]\n"
"s_cbranch_vccnz .Lmk_p66s\n"
".Lmk_p66r:\n"
"ds_read_b128 v[140:143], v6 offset:3072\n"
"v_mfma_f32_16x16x32_bf16 v[52:55], a[164:167], v[88:91], v[52:55]\n"
"buffer_load_dwordx4 v[152:155], v1, s[4:7], s97 offen sc1\n"
"v_mfma_f32_16x16x32_bf16 v[52:55], a[160:163], v[84:87], v[52:55]\n"
"v_mfma_f32_16x16x32_bf16 v[144:147], a[192:195], v[84:87], v[144:147]\n"
"v_mfma_f32_16x16x32_bf16 v[144:147], a[196:199], v[88:91], v[144:147]\n"
"buffer_load_dwordx4 v[172:175], v1, s[4:7], s97 offen offset:1024 sc1\n"
"v_mfma_f32_16x16x32_bf16 v[144:147], a[200:203], v[100:103], v[144:147]\n"
"v_mfma_f32_16x16x32_bf16 v[144:147], a[204:207], v[104:107], v[144:147]\n"
"v_mfma_f32_16x16x32_bf16 v[144:147], a[208:211], v[80:83], v[144:147]\n"
"buffer_load_dwordx4 v[176:179], v1, s[4:7], s97 offen offset:2048 sc1\n"
"s_waitcnt lgkmcnt(0)\n"
"v_mfma_f32_16x16x32_bf16 v[144:147], a[212:215], v[116:119], v[144:147]\n"
"v_add_f32_e32 v156, v64, v156\n"
"v_add_f32_e32 v157, v65, v157\n"
"v_add_f32_e32 v158, v66, v158\n"
"v_mfma_f32_16x16x32_bf16 v[144:147], a[216:219], v[96:99], v[144:147]\n"
"v_add_f32_e32 v159, v67, v159\n"
"v_add_f32_e32 v164, v52, v164\n"
"v_add_f32_e32 v165, v53, v165\n"
"v_mfma_f32_16x16x32_bf16 v[144:147], a[220:223], v[92:95], v[144:147]\n"
"buffer_load_dwordx4 v[64:67], v1, s[4:7], s97 offen offset:3072 sc1\n"
"v_add_f32_e32 v166, v54, v166\n"
"v_mfma_f32_16x16x32_bf16 v[40:43], a[252:255], v[92:95], v[40:43]\n"
"v_add_f32_e32 v167, v55, v167\n"
"v_cvt_pk_bf16_f32 v52, v156, v157\n"
"v_cvt_pk_bf16_f32 v53, v158, v159\n"
"v_mfma_f32_16x16x32_bf16 v[40:43], a[248:251], v[96:99], v[40:43]\n"
"s_add_u32 s97, s61, 0x840000\n"
"v_cvt_pk_bf16_f32 v54, v164, v165\n"
"v_cvt_pk_bf16_f32 v55, v166, v167\n"
"v_mfma_f32_16x16x32_bf16 v[40:43], a[244:247], v[116:119], v[40:43]\n"
"buffer_store_dwordx4 v[52:55], v0, s[4:7], s97 offen sc1\n"
"v_mfma_f32_16x16x32_bf16 v[40:43], a[240:243], v[80:83], v[40:43]\n"
"ds_write_b128 v2, v[52:55] offset:8192\n"
"v_mfma_f32_16x16x32_bf16 v[40:43], a[236:239], v[104:107], v[40:43]\n"
"v_mfma_f32_16x16x32_bf16 v[40:43], a[232:235], v[100:103], v[40:43]\n"
"v_mfma_f32_16x16x32_bf16 v[40:43], a[228:231], v[88:91], v[40:43]\n"
"v_mfma_f32_16x16x32_bf16 v[40:43], a[224:227], v[84:87], v[40:43]\n"
"; @B fin b\n"
"s_add_u32 s97, s61, 0x840000\n"
"v_add_f32_e32 v168, v144, v168\n"
"v_add_f32_e32 v169, v145, v169\n"
"v_add_f32_e32 v170, v146, v170\n"
"v_add_f32_e32 v171, v147, v171\n"
"s_nop 2\n"
"v_add_f32_e32 v140, v40, v140\n"
"v_add_f32_e32 v141, v41, v141\n"
"v_add_f32_e32 v142, v42, v142\n"
"v_add_f32_e32 v143, v43, v143\n"
"v_cvt_pk_bf16_f32 v40, v168, v169\n"
"v_cvt_pk_bf16_f32 v41, v170, v171\n"
"v_cvt_pk_bf16_f32 v42, v140, v141\n"
"v_cvt_pk_bf16_f32 v43, v142, v143\n"
"buffer_store_dwordx4 v[40:43], v0, s[4:7], s97 offen offset:1024 sc1\n"
"s_add_u32 s97, s59, 0x400000\n"
"buffer_load_dwordx4 v[80:83], v0, s[4:7], s97 offen sc1\n"
"buffer_load_dwordx4 v[84:87], v0, s[4:7], s97 offen offset:1024 sc1\n"
"buffer_load_dwordx4 v[88:91], v0, s[4:7], s97 offen offset:2048 sc1\n"
"buffer_load_dwordx4 v[92:95], v0, s[4:7], s97 offen offset:3072 sc1\n"
"buffer_load_dwordx4 v[96:99], v1, s[4:7], s97 offen sc1\n"
"buffer_load_dwordx4 v[100:103], v1, s[4:7], s97 offen offset:1024 sc1\n"
"buffer_load_dwordx4 v[104:107], v1, s[4:7], s97 offen offset:2048 sc1\n"
"buffer_load_dwordx4 v[116:119], v1, s[4:7], s97 offen offset:3072 sc1\n"
"s_waitcnt vmcnt(32)\n"
"; @B G2 a.0\n"
"v_mfma_f32_16x16x32_bf16 v[140:143], a[128:131], v[120:123], 0\n"
"v_mfma_f32_16x16x32_bf16 v[140:143], a[132:135], v[124:127], v[140:143]\n"
"s_waitcnt vmcnt(30)\n"
"ds_write_b128 v2, v[40:43] offset:9216\n"
"v_mfma_f32_16x16x32_bf16 v[140:143], a[136:139], v[108:111], v[140:143]\n"
"v_mov_b32_e32 v144, 0x3f803f80\n"
"v_mov_b32_e32 v145, 0x3f803f80\n"
"v_mfma_f32_16x16x32_bf16 v[140:143], a[140:143], v[132:135], v[140:143]\n"
"s_waitcnt vmcnt(28)\n"
"v_mov_b32_e32 v146, 0x3f803f80\n"
"v_mov_b32_e32 v147, 0x3f803f80\n"
"v_mfma_f32_16x16x32_bf16 v[140:143], a[144:147], v[112:115], v[140:143]\n"
"s_nop 0\n"
"v_mfma_f32_16x16x32_bf16 v[156:159], v[144:147], v[52:55], 0\n"
"v_mfma_f32_16x16x32_bf16 v[156:159], v[144:147], v[40:43], v[156:159]\n"
"v_mfma_f32_16x16x32_bf16 v[144:147], v[52:55], v[52:55], 0\n"
"v_mfma_f32_16x16x32_bf16 v[144:147], v[40:43], v[40:43], v[144:147]\n"
"buffer_load_dwordx4 a[56:59], v1, s[8:11], s98 offen offset:2048 sc1\n"
"v_mfma_f32_16x16x32_bf16 v[140:143], a[148:151], v[28:31], v[140:143]\n"
"s_waitcnt vmcnt(27)\n"
"v_mfma_f32_16x16x32_bf16 v[140:143], a[152:155], v[32:35], v[140:143]\n"
"v_mfma_f32_16x16x32_bf16 v[140:143], a[156:159], v[24:27], v[140:143]\n"
"buffer_load_dwordx4 a[60:63], v1, s[8:11], s98 offen offset:3072 sc1\n"
"v_mfma_f32_16x16x32_bf16 v[40:43], a[188:191], v[24:27], 0\n"
"s_mov_b32 s96, 0xaaaaaaaa\n"
"s_mov_b32 s97, 0xaaaaaaaa\n"
"s_add_u32 s98, s69, 0x4000\n"
"v_mfma_f32_16x16x32_bf16 v[40:43], a[184:187], v[32:35], v[40:43]\n"
"v_mfma_f32_16x16x32_bf16 v[40:43], a[180:183], v[28:31], v[40:43]\n"
"buffer_load_dwordx4 a[64:67], v0, s[8:11], s98 offen sc1\n"
"v_mfma_f32_16x16x32_bf16 v[40:43], a[176:179], v[112:115], v[40:43]\n"
"v_cndmask_b32_e64 v144, v144, v145, s[96:97]\n"
"v_cndmask_b32_e64 v146, v146, v147, s[96:97]\n"
"v_mfma_f32_16x16x32_bf16 v[40:43], a[172:175], v[132:135], v[40:43]\n"
"v_cmp_u_f32_e32 vcc, v140, v140\n"
"v_mfma_f32_16x16x32_bf16 v[40:43], a[168:171], v[108:111], v[40:43]\n"
"s_cbranch_vccnz .Lmk_p68s\n"
".Lmk_p68r:\n"
"v_mfma_f32_16x16x32_bf16 v[40:43], a[164:167], v[124:127], v[40:43]\n"
"ds_write_b128 v3, v[140:143]\n"
"v_mfma_f32_16x16x32_bf16 v[40:43], a[160:163], v[120:123], v[40:43]\n"
"s_mov_b32 s96, 0xcccccccc\n"
"s_mov_b32 s97, 0xcccccccc\n"
"v_mfma_f32_16x16x32_bf16 v[52:55], a[192:195], v[120:123], 0\n"
"v_cndmask_b32_e64 v157, v144, v146, s[96:97]\n"
"v_mfma_f32_16x16x32_bf16 v[52:55], a[196:199], v[124:127], v[52:55]\n"
"s_mov_b32 s96, 0xf0000f\n"
"s_mov_b32 s97, 0xf0000f00\n"
"v_mfma_f32_16x16x32_bf16 v[52:55], a[200:203], v[108:111], v[52:55]\n"
"v_cndmask_b32_e64 v157, 0, v157, s[96:97]\n"
"v_mov_b32_e32 v255, v157\n"
"v_mfma_f32_16x16x32_bf16 v[52:55], a[204:207], v[132:135], v[52:55]\n"
"s_nop 0\n"
"v_permlane16_swap_b32_e32 v157, v255\n"
"v_add_f32_e32 v157, v157, v255\n"
"v_mfma_f32_16x16x32_bf16 v[52:55], a[208:211], v[112:115], v[52:55]\n"
"v_mov_b32_e32 v255, v157\n"
"v_mfma_f32_16x16x32_bf16 v[52:55], a[212:215], v[28:31], v[52:55]\n"
"ds_write_b128 v3, v[40:43] offset:1024\n"
"v_mfma_f32_16x16x32_bf16 v[52:55], a[216:219], v[32:35], v[52:55]\n"
"s_mov_b32 s96, 0xffff0000\n"
"v_permlane32_swap_b32_e32 v157, v255\n"
"v_add_f32_e32 v157, v157, v255\n"
"v_mfma_f32_16x16x32_bf16 v[52:55], a[220:223], v[24:27], v[52:55]\n"
"v_cndmask_b32_e64 v156, v156, v157, s[96:97]\n"
"s_add_u32 s97, s63, 0x84000\n"
"v_mfma_f32_16x16x32_bf16 v[40:43], a[252:255], v[24:27], 0\n"
"buffer_store_dword v156, v14, s[4:7], s97 offen sc1\n"
"v_mfma_f32_16x16x32_bf16 v[40:43], a[248:251], v[32:35], v[40:43]\n"
"buffer_load_dwordx4 a[68:71], v0, s[8:11], s98 offen offset:1024 sc1\n"
"v_mfma_f32_16x16x32_bf16 v[40:43], a[244:247], v[28:31], v[40:43]\n"
"v_mfma_f32_16x16x32_bf16 v[40:43], a[240:243], v[112:115], v[40:43]\n"
"v_mfma_f32_16x16x32_bf16 v[40:43], a[236:239], v[132:135], v[40:43]\n"
"buffer_load_dwordx4 a[72:75], v0, s[8:11], s98 offen offset:2048 sc1\n"
"v_mfma_f32_16x16x32_bf16 v[40:43], a[232:235], v[108:111], v[40:43]\n"
"ds_write_b128 v3, v[52:55] offset:2048\n"
"v_mfma_f32_16x16x32_bf16 v[40:43], a[228:231], v[124:127], v[40:43]\n"
"v_mfma_f32_16x16x32_bf16 v[40:43], a[224:227], v[120:123], v[40:43]\n"
"s_waitcnt vmcnt(28)\n"
"buffer_load_dwordx4 a[76:79], v0, s[8:11], s98 offen offset:3072 sc1\n"
"; @B G2 a.1\n"
"v_mfma_f32_16x16x32_bf16 v[24:27], a[128:131], v[48:51], 0\n"
"v_mfma_f32_16x16x32_bf16 v[24:27], a[132:135], v[56:59], v[24:27]\n"
"v_mfma_f32_16x16x32_bf16 v[24:27], a[136:139], v[136:139], v[24:27]\n"
"buffer_load_dwordx4 a[80:83], v1, s[8:11], s98 offen sc1\n"
"v_mfma_f32_16x16x32_bf16 v[24:27], a[140:143], v[44:47], v[24:27]\n"
"s_waitcnt vmcnt(26)\n"
"v_mfma_f32_16x16x32_bf16 v[24:27], a[144:147], v[160:163], v[24:27]\n"
"v_mfma_f32_16x16x32_bf16 v[24:27], a[148:151], v[128:131], v[24:27]\n"
"ds_write_b128 v3, v[40:43] offset:3072\n"
"v_mfma_f32_16x16x32_bf16 v[24:27], a[152:155], v[72:75], v[24:27]\n"
"buffer_load_dwordx4 a[84:87], v1, s[8:11], s98 offen offset:1024 sc1\n"
"v_mfma_f32_16x16x32_bf16 v[24:27], a[156:159], v[60:63], v[24:27]\n"
"v_mfma_f32_16x16x32_bf16 v[28:31], a[188:191], v[60:63], 0\n"
"v_mfma_f32_16x16x32_bf16 v[28:31], a[184:187], v[72:75], v[28:31]\n"
"buffer_load_dwordx4 a[88:91], v1, s[8:11], s98 offen offset:2048 sc1\n"
"v_mfma_f32_16x16x32_bf16 v[28:31], a[180:183], v[128:131], v[28:31]\n"
"v_mfma_f32_16x16x32_bf16 v[28:31], a[176:179], v[160:163], v[28:31]\n"
"v_mfma_f32_16x16x32_bf16 v[28:31], a[172:175], v[44:47], v[28:31]\n"
"s_nop 1\n"
"v_cmp_u_f32_e32 vcc, v24, v24\n"
"v_mfma_f32_16x16x32_bf16 v[28:31], a[168:171], v[136:139], v[28:31]\n"
"s_cbranch_vccnz .Lmk_p69s\n"
".Lmk_p69r:\n"
"v_mfma_f32_16x16x32_bf16 v[28:31], a[164:167], v[56:59], v[28:31]\n"
"ds_write_b128 v3, v[24:27] offset:6144\n"
"v_mfma_f32_16x16x32_bf16 v[28:31], a[160:163], v[48:51], v[28:31]\n"
"buffer_load_dwordx4 a[92:95], v1, s[8:11], s98 offen offset:3072 sc1\n"
"v_mfma_f32_16x16x32_bf16 v[24:27], a[192:195], v[48:51], 0\n"
"s_add_u32 s98, s69, 0x6000\n"
"v_mfma_f32_16x16x32_bf16 v[24:27], a[196:199], v[56:59], v[24:27]\n"
"v_mfma_f32_16x16x32_bf16 v[24:27], a[200:203], v[136:139], v[24:27]\n"
"buffer_load_dwordx4 a[96:99], v0, s[8:11], s98 offen sc1\n"
"v_mfma_f32_16x16x32_bf16 v[24:27], a[204:207], v[44:47], v[24:27]\n"
"v_mfma_f32_16x16x32_bf16 v[24:27], a[208:211], v[160:163], v[24:27]\n"
"ds_read_b128 v[32:35], v2\n"
"ds_read_b128 v[40:43], v2 offset:1024\n"
"v_mfma_f32_16x16x32_bf16 v[24:27], a[212:215], v[128:131], v[24:27]\n"
"ds_write_b128 v3, v[28:31] offset:7168\n"
"v_mfma_f32_16x16x32_bf16 v[24:27], a[216:219], v[72:75], v[24:27]\n"
"buffer_load_dwordx4 a[100:103], v0, s[8:11], s98 offen offset:1024 sc1\n"
"v_mfma_f32_16x16x32_bf16 v[24:27], a[220:223], v[60:63], v[24:27]\n"
"v_mfma_f32_16x16x32_bf16 v[28:31], a[252:255], v[60:63], 0\n"
"v_mfma_f32_16x16x32_bf16 v[28:31], a[248:251], v[72:75], v[28:31]\n"
"buffer_load_dwordx4 a[104:107], v0, s[8:11], s98 offen offset:2048 sc1\n"
"s_waitcnt lgkmcnt(1)\n"
"v_mfma_f32_16x16x32_bf16 v[28:31], a[244:247], v[128:131], v[28:31]\n"
"v_lshlrev_b32_e32 v255, 16, v32\n"
"v_and_b32_e32 v254, 0xffff0000, v32\n"
"v_lshlrev_b32_e32 v253, 16, v33\n"
"v_mfma_f32_16x16x32_bf16 v[28:31], a[240:243], v[160:163], v[28:31]\n"
"v_and_b32_e32 v252, 0xffff0000, v33\n"
"v_lshlrev_b32_e32 v251, 16, v34\n"
"v_and_b32_e32 v250, 0xffff0000, v34\n"
"v_mfma_f32_16x16x32_bf16 v[28:31], a[236:239], v[44:47], v[28:31]\n"
"buffer_load_dwordx4 a[108:111], v0, s[8:11], s98 offen offset:3072 sc1\n"
"v_lshlrev_b32_e32 v249, 16, v35\n"
"v_mfma_f32_16x16x32_bf16 v[28:31], a[232:235], v[136:139], v[28:31]\n"
"ds_write_b128 v3, v[24:27] offset:8192\n"
"v_mfma_f32_16x16x32_bf16 v[28:31], a[228:231], v[56:59], v[28:31]\n"
"v_and_b32_e32 v248, 0xffff0000, v35\n"
"v_lshlrev_b32_e32 v247, 16, v40\n"
"v_and_b32_e32 v246, 0xffff0000, v40\n"
"v_mfma_f32_16x16x32_bf16 v[28:31], a[224:227], v[48:51], v[28:31]\n"
"s_waitcnt vmcnt(29)\n"
"buffer_load_dwordx4 a[112:115], v1, s[8:11], s98 offen sc1\n"
"; @B G2 a.2\n"
"v_mfma_f32_16x16x32_bf16 v[24:27], a[128:131], v[36:39], 0\n"
"v_lshlrev_b32_e32 v245, 16, v41\n"
"v_and_b32_e32 v244, 0xffff0000, v41\n"
"v_lshlrev_b32_e32 v243, 16, v42\n"
"v_mfma_f32_16x16x32_bf16 v[24:27], a[132:135], v[68:71], v[24:27]\n"
"v_and_b32_e32 v242, 0xffff0000, v42\n"
"v_lshlrev_b32_e32 v241, 16, v43\n"
"v_and_b32_e32 v240, 0xffff0000, v43\n"
"v_mfma_f32_16x16x32_bf16 v[24:27], a[136:139], v[76:79], v[24:27]\n"
"buffer_load_dwordx4 a[116:119], v1, s[8:11], s98 offen offset:1024 sc1\n"
"v_mfma_f32_16x16x32_bf16 v[24:27], a[140:143], v[148:151], v[24:27]\n"
"s_waitcnt vmcnt(27)\n"
"v_mfma_f32_16x16x32_bf16 v[24:27], a[144:147], v[152:155], v[24:27]\n"
"v_mfma_f32_16x16x32_bf16 v[24:27], a[148:151], v[172:175], v[24:27]\n"
"ds_write_b128 v3, v[28:31] offset:9216\n"
"v_mfma_f32_16x16x32_bf16 v[24:27], a[152:155], v[176:179], v[24:27]\n"
"buffer_load_dwordx4 a[120:123], v1, s[8:11], s98 offen offset:2048 sc1\n"
"v_mfma_f32_16x16x32_bf16 v[24:27], a[156:159], v[64:67], v[24:27]\n"
"v_mfma_f32_16x16x32_bf16 v[28:31], a[188:191], v[64:67], 0\n"
"v_mfma_f32_16x16x32_bf16 v[28:31], a[184:187], v[176:179], v[28:31]\n"
"s_waitcnt lgkmcnt(0)\n"
"s_barrier\n"
"v_mfma_f32_16x16x32_bf16 v[28:31], a[180:183], v[172:175], v[28:31]\n"
"buffer_load_dwordx4 a[124:127], v1, s[8:11], s98 offen offset:3072 sc1\n"
"v_mfma_f32_16x16x32_bf16 v[28:31], a[176:179], v[152:155], v[28:31]\n"
"s_add_u32 s98, s69, 0x8000\n"
"ds_read_b128 v[32:35], v4\n"
"ds_read_b128 v[40:43], v5\n"
"v_mfma_f32_16x16x32_bf16 v[28:31], a[172:175], v[148:151], v[28:31]\n"
"v_cmp_u_f32_e32 vcc, v24, v24\n"
"v_mfma_f32_16x16x32_bf16 v[28:31], a[168:171], v[76:79], v[28:31]\n"
"s_cbranch_vccnz .Lmk_p70s\n"
".Lmk_p70r:\n"
"v_mfma_f32_16x16x32_bf16 v[28:31], a[164:167], v[68:71], v[28:31]\n"
"ds_write_b128 v3, v[24:27] offset:12288\n"
"v_mfma_f32_16x16x32_bf16 v[28:31], a[160:163], v[36:39], v[28:31]\n"
"buffer_load_dwordx4 v[24:27], v0, s[8:11], s98 offen sc1\n"
"v_mfma_f32_16x16x32_bf16 v[44:47], a[192:195], v[36:39], 0\n"
"ds_read_b128 v[48:51], v4 offset:1024\n"
"ds_read_b128 v[52:55], v5 offset:1024\n"
"s_waitcnt lgkmcnt(3)\n"
"v_mfma_f32_16x16x32_bf16 v[44:47], a[196:199], v[68:71], v[44:47]\n"
"v_add_f32_e32 v32, v32, v255\n"
"v_add_f32_e32 v33, v33, v254\n"
"v_add_f32_e32 v34, v34, v253\n"
"v_mfma_f32_16x16x32_bf16 v[44:47], a[200:203], v[76:79], v[44:47]\n"
"buffer_load_dwordx4 v[56:59], v0, s[8:11], s98 offen offset:1024 sc1\n"
"v_add_f32_e32 v35, v35, v252\n"
"v_mfma_f32_16x16x32_bf16 v[44:47], a[204:207], v[148:151], v[44:47]\n"
"v_add_f32_e32 v32, v32, v40\n"
"v_add_f32_e32 v33, v33, v41\n"
"v_add_f32_e32 v34, v34, v42\n"
"v_mfma_f32_16x16x32_bf16 v[44:47], a[208:211], v[152:155], v[44:47]\n"
"v_add_f32_e32 v35, v35, v43\n"
"v_mfma_f32_16x16x32_bf16 v[44:47], a[212:215], v[172:175], v[44:47]\n"
"ds_write_b128 v3, v[28:31] offset:13312\n"
"v_mfma_f32_16x16x32_bf16 v[44:47], a[216:219], v[176:179], v[44:47]\n"
"buffer_load_dwordx4 v[28:31], v0, s[8:11], s98 offen offset:2048 sc1\n"
"s_waitcnt lgkmcnt(1)\n"
"v_mfma_f32_16x16x32_bf16 v[44:47], a[220:223], v[64:67], v[44:47]\n"
"ds_read_b128 v[40:43], v4 offset:2048\n"
"ds_read_b128 v[60:63], v5 offset:2048\n"
"v_add_f32_e32 v48, v48, v251\n"
"v_mfma_f32_16x16x32_bf16 v[72:75], a[252:255], v[64:67], 0\n"
"v_add_f32_e32 v49, v49, v250\n"
"v_add_f32_e32 v50, v50, v249\n"
"v_add_f32_e32 v51, v51, v248\n"
"v_mfma_f32_16x16x32_bf16 v[72:75], a[248:251], v[176:179], v[72:75]\n"
"buffer_load_dwordx4 v[64:67], v0, s[8:11], s98 offen offset:3072 sc1\n"
"v_add_f32_e32 v48, v48, v52\n"
"v_mfma_f32_16x16x32_bf16 v[72:75], a[244:247], v[172:175], v[72:75]\n"
"v_add_f32_e32 v49, v49, v53\n"
"v_add_f32_e32 v50, v50, v54\n"
"v_add_f32_e32 v51, v51, v55\n"
"v_mfma_f32_16x16x32_bf16 v[72:75], a[240:243], v[152:155], v[72:75]\n"
"v_mfma_f32_16x16x32_bf16 v[72:75], a[236:239], v[148:151], v[72:75]\n"
"buffer_load_dwordx4 v[52:55], v1, s[8:11], s98 offen sc1\n"
"s_waitcnt lgkmcnt(0)\n"
"v_mfma_f32_16x16x32_bf16 v[72:75], a[232:235], v[76:79], v[72:75]\n"
"ds_write_b128 v3, v[44:47] offset:14336\n"
"v_mfma_f32_16x16x32_bf16 v[72:75], a[228:231], v[68:71], v[72:75]\n"
"ds_read_b128 v[44:47], v4 offset:3072\n"
"ds_read_b128 v[68:71], v5 offset:3072\n"
"v_add_f32_e32 v40, v40, v247\n"
"v_mfma_f32_16x16x32_bf16 v[72:75], a[224:227], v[36:39], v[72:75]\n"
"s_waitcnt vmcnt(28)\n"
"buffer_load_dwordx4 v[36:39], v1, s[8:11], s98 offen offset:1024 sc1\n"
"; @B G2 a.3\n"
"v_mfma_f32_16x16x32_bf16 v[76:79], a[128:131], v[80:83], v[32:35]\n"
"v_add_f32_e32 v41, v41, v246\n"
"v_add_f32_e32 v42, v42, v245\n"
"v_add_f32_e32 v43, v43, v244\n"
"v_mfma_f32_16x16x32_bf16 v[76:79], a[132:135], v[84:87], v[76:79]\n"
"v_add_f32_e32 v40, v40, v60\n"
"v_add_f32_e32 v41, v41, v61\n"
"v_add_f32_e32 v42, v42, v62\n"
"v_mfma_f32_16x16x32_bf16 v[76:79], a[136:139], v[88:91], v[76:79]\n"
"buffer_load_dwordx4 v[108:111], v1, s[8:11], s98 offen offset:2048 sc1\n"
"v_add_f32_e32 v43, v43, v63\n"
"v_mfma_f32_16x16x32_bf16 v[76:79], a[140:143], v[92:95], v[76:79]\n"
"s_waitcnt vmcnt(26)\n"
"s_waitcnt lgkmcnt(0)\n"
"v_add_f32_e32 v44, v44, v243\n"
"v_add_f32_e32 v45, v45, v242\n"
"v_mfma_f32_16x16x32_bf16 v[76:79], a[144:147], v[96:99], v[76:79]\n"
"v_add_f32_e32 v46, v46, v241\n"
"v_add_f32_e32 v47, v47, v240\n"
"v_add_f32_e32 v44, v44, v68\n"
"v_mfma_f32_16x16x32_bf16 v[76:79], a[148:151], v[100:103], v[76:79]\n"
"ds_write_b128 v3, v[72:75] offset:15360\n"
"v_mfma_f32_16x16x32_bf16 v[76:79], a[152:155], v[104:107], v[76:79]\n"
"buffer_load_dwordx4 v[60:63], v1, s[8:11], s98 offen offset:3072 sc1\n"
"v_add_f32_e32 v45, v45, v69\n"
"v_mfma_f32_16x16x32_bf16 v[76:79], a[156:159], v[116:119], v[76:79]\n"
"s_add_u32 s98, s69, 0xa000\n"
"v_add_f32_e32 v46, v46, v70\n"
"v_add_f32_e32 v47, v47, v71\n"
"v_mfma_f32_16x16x32_bf16 v[68:71], a[188:191], v[116:119], v[48:51]\n"
"v_mfma_f32_16x16x32_bf16 v[68:71], a[184:187], v[104:107], v[68:71]\n"
"s_waitcnt lgkmcnt(0)\n"
"s_barrier\n"
"v_mfma_f32_16x16x32_bf16 v[68:71], a[180:183], v[100:103], v[68:71]\n"
"buffer_load_dwordx4 v[72:75], v0, s[8:11], s98 offen sc1\n"
"v_mfma_f32_16x16x32_bf16 v[68:71], a[176:179], v[96:99], v[68:71]\n"
"ds_read_b128 v[112:115], v6\n"
"ds_read_b128 v[120:123], v6 offset:1024\n"
"v_mfma_f32_16x16x32_bf16 v[68:71], a[172:175], v[92:95], v[68:71]\n"
"ds_read_b128 v[124:127], v6 offset:2048\n"
"v_cmp_u_f32_e32 vcc, v76, v76\n"
"v_mfma_f32_16x16x32_bf16 v[68:71], a[168:171], v[88:91], v[68:71]\n"
"s_cbranch_vccnz .Lmk_p71s\n"
".Lmk_p71r:\n"
"ds_read_b128 v[32:35], v6 offset:3072\n"
"v_mfma_f32_16x16x32_bf16 v[68:71], a[164:167], v[84:87], v[68:71]\n"
"buffer_load_dwordx4 v[48:51], v0, s[8:11], s98 offen offset:1024 sc1\n"
"v_mfma_f32_16x16x32_bf16 v[68:71], a[160:163], v[80:83], v[68:71]\n"
"v_mfma_f32_16x16x32_bf16 v[40:43], a[192:195], v[80:83], v[40:43]\n"
"v_mfma_f32_16x16x32_bf16 v[40:43], a[196:199], v[84:87], v[40:43]\n"
"buffer_load_dwordx4 v[128:131], v0, s[8:11], s98 offen offset:2048 sc1\n"
"v_mfma_f32_16x16x32_bf16 v[40:43], a[200:203], v[88:91], v[40:43]\n"
"v_mfma_f32_16x16x32_bf16 v[40:43], a[204:207], v[92:95], v[40:43]\n"
"v_mfma_f32_16x16x32_bf16 v[40:43], a[208:211], v[96:99], v[40:43]\n"
"buffer_load_dwordx4 v[132:135], v0, s[8:11], s98 offen offset:3072 sc1\n"
"s_waitcnt lgkmcnt(0)\n"
"v_mfma_f32_16x16x32_bf16 v[40:43], a[212:215], v[100:103], v[40:43]\n"
"v_add_f32_e32 v112, v76, v112\n"
"v_add_f32_e32 v113, v77, v113\n"
"v_add_f32_e32 v114, v78, v114\n"
"v_mfma_f32_16x16x32_bf16 v[40:43], a[216:219], v[104:107], v[40:43]\n"
"v_add_f32_e32 v115, v79, v115\n"
"v_add_f32_e32 v120, v68, v120\n"
"v_add_f32_e32 v121, v69, v121\n"
"v_mfma_f32_16x16x32_bf16 v[40:43], a[220:223], v[116:119], v[40:43]\n"
"buffer_load_dwordx4 v[76:79], v1, s[8:11], s98 offen sc1\n"
"v_add_f32_e32 v122, v70, v122\n"
"v_mfma_f32_16x16x32_bf16 v[44:47], a[252:255], v[116:119], v[44:47]\n"
"v_add_f32_e32 v123, v71, v123\n"
"v_cvt_pk_bf16_f32 v68, v112, v113\n"
"v_cvt_pk_bf16_f32 v69, v114, v115\n"
"v_mfma_f32_16x16x32_bf16 v[44:47], a[248:251], v[104:107], v[44:47]\n"
"s_add_u32 s97, s61, 0x800000\n"
"v_cvt_pk_bf16_f32 v70, v120, v121\n"
"v_cvt_pk_bf16_f32 v71, v122, v123\n"
"v_mfma_f32_16x16x32_bf16 v[44:47], a[244:247], v[100:103], v[44:47]\n"
"buffer_load_dwordx4 v[100:103], v1, s[8:11], s98 offen offset:1024 sc1\n"
"v_mfma_f32_16x16x32_bf16 v[44:47], a[240:243], v[96:99], v[44:47]\n"
"buffer_store_dwordx4 v[68:71], v0, s[4:7], s97 offen sc1\n"
"v_mfma_f32_16x16x32_bf16 v[44:47], a[236:239], v[92:95], v[44:47]\n"
"ds_write_b128 v2, v[68:71]\n"
"v_mfma_f32_16x16x32_bf16 v[44:47], a[232:235], v[88:91], v[44:47]\n"
"buffer_load_dwordx4 v[88:91], v1, s[8:11], s98 offen offset:2048 sc1\n"
"v_mfma_f32_16x16x32_bf16 v[44:47], a[228:231], v[84:87], v[44:47]\n"
"v_mfma_f32_16x16x32_bf16 v[44:47], a[224:227], v[80:83], v[44:47]\n"
"buffer_load_dwordx4 v[80:83], v1, s[8:11], s98 offen offset:3072 sc1\n"
"s_add_u32 s98, s52, 0x840000\n"
"buffer_load_dwordx4 v[84:87], v0, s[8:11], s98 offen sc1\n"
"buffer_load_dwordx4 v[92:95], v0, s[8:11], s98 offen offset:1024 sc1\n"
"buffer_load_dwordx4 v[96:99], v0, s[8:11], s98 offen offset:2048 sc1\n"
"buffer_load_dwordx4 v[104:107], v0, s[8:11], s98 offen offset:3072 sc1\n"
"buffer_load_dwordx4 v[112:115], v1, s[8:11], s98 offen sc1\n"
"buffer_load_dwordx4 v[116:119], v1, s[8:11], s98 offen offset:1024 sc1\n"
"buffer_load_dwordx4 v[120:123], v1, s[8:11], s98 offen offset:2048 sc1\n"
"buffer_load_dwordx4 v[136:139], v1, s[8:11], s98 offen offset:3072 sc1\n"
"s_add_u32 s98, s53, 0x840000\n"
"buffer_load_dwordx4 v[140:143], v0, s[8:11], s98 offen sc1\n"
"buffer_load_dwordx4 v[144:147], v0, s[8:11], s98 offen offset:1024 sc1\n"
"buffer_load_dwordx4 v[148:151], v0, s[8:11], s98 offen offset:2048 sc1\n"
"buffer_load_dwordx4 v[152:155], v0, s[8:11], s98 offen offset:3072 sc1\n"
"buffer_load_dwordx4 v[156:159], v1, s[8:11], s98 offen sc1\n"
"buffer_load_dwordx4 v[160:163], v1, s[8:11], s98 offen offset:1024 sc1\n"
"buffer_load_dwordx4 v[164:167], v1, s[8:11], s98 offen offset:2048 sc1\n"
"buffer_load_dwordx4 v[168:171], v1, s[8:11], s98 offen offset:3072 sc1\n"
"s_lshr_b32 s98, s79, 7\n"
"s_add_u32 s52, s52, 0x800000\n"
"s_add_u32 s53, s53, 0x800000\n"
"s_add_u32 s54, s54, 0x800000\n"
"s_add_u32 s55, s55, 0x800000\n"
"s_add_u32 s56, s56, 0x800000\n"
"s_add_u32 s57, s57, 0x800000\n"
"s_add_u32 s58, s58, 0x800000\n"
"s_add_u32 s59, s59, 0x800000\n"
"s_add_u32 s60, s60, 0x800000\n"
"s_add_u32 s61, s61, 0x800000\n"
"s_add_u32 s62, s62, 0x80000\n"
"s_add_u32 s63, s63, 0x80000\n"
"s_add_u32 s64, s64, 0x1000000\n"
"s_add_u32 s65, s65, s79\n"
"s_add_u32 s66, s66, s79\n"
"s_add_u32 s67, s67, 0x20000\n"
"s_add_u32 s68, s68, 0x20000\n"
"s_add_u32 s71, s71, s98\n"
"s_add_u32 s73, s73, s98\n"
"s_mov_b32 s0, 1\n"
"s_mov_b32 s10, 0\n"
".Lmk_a0:\n"
"s_waitcnt vmcnt(12)\n"
"; @A G1 0.0\n"
"v_mfma_f32_16x16x32_bf16 v[172:175], a[0:3], v[84:87], 0\n"
"v_mfma_f32_16x16x32_bf16 v[172:175], a[4:7], v[92:95], v[172:175]\n"
"s_add_u32 s98, s62, 0x4000\n"
"v_mfma_f32_16x16x32_bf16 v[172:175], a[8:11], v[96:99], v[172:175]\n"
"s_add_u32 s97, s54, 0x40000\n"
"buffer_load_dwordx4 v[176:179], v0, s[4:7], s97 offen sc1\n"
"v_mfma_f32_16x16x32_bf16 v[172:175], a[12:15], v[104:107], v[172:175]\n"
"s_waitcnt vmcnt(9)\n"
"v_mfma_f32_16x16x32_bf16 v[172:175], a[16:19], v[112:115], v[172:175]\n"
"buffer_load_dword v255, v10, s[4:7], s98 offen sc1\n"
"v_mfma_f32_16x16x32_bf16 v[172:175], a[20:23], v[116:119], v[172:175]\n"
"v_add_f32_e32 v124, v40, v124\n"
"v_add_f32_e32 v125, v41, v125\n"
"v_mfma_f32_16x16x32_bf16 v[172:175], a[24:27], v[120:123], v[172:175]\n"
"v_add_f32_e32 v126, v42, v126\n"
"v_add_f32_e32 v127, v43, v127\n"
"v_mfma_f32_16x16x32_bf16 v[172:175], a[28:31], v[136:139], v[172:175]\n"
"buffer_load_dwordx4 v[40:43], v0, s[4:7], s97 offen offset:1024 sc1\n"
"v_mfma_f32_16x16x32_bf16 v[180:183], a[60:63], v[136:139], 0\n"
"buffer_load_dword v254, v10, s[4:7], s98 offen offset:256 sc1\n"
"v_mfma_f32_16x16x32_bf16 v[180:183], a[56:59], v[120:123], v[180:183]\n"
"v_add_f32_e32 v32, v44, v32\n"
"v_add_f32_e32 v33, v45, v33\n"
"v_mfma_f32_16x16x32_bf16 v[180:183], a[52:55], v[116:119], v[180:183]\n"
"v_add_f32_e32 v34, v46, v34\n"
"v_add_f32_e32 v35, v47, v35\n"
"v_mfma_f32_16x16x32_bf16 v[180:183], a[48:51], v[112:115], v[180:183]\n"
"buffer_load_dword v253, v10, s[4:7], s98 offen offset:512 sc1\n"
"v_mfma_f32_16x16x32_bf16 v[180:183], a[44:47], v[104:107], v[180:183]\n"
"v_cmp_u_f32_e32 vcc, v172, v172\n"
"v_cvt_pk_bf16_f32 v44, v124, v125\n"
"v_mfma_f32_16x16x32_bf16 v[180:183], a[40:43], v[96:99], v[180:183]\n"
"s_cbranch_vccnz .Lmk_p76s\n"
".Lmk_p76r:\n"
"v_cvt_pk_bf16_f32 v45, v126, v127\n"
"v_mfma_f32_16x16x32_bf16 v[180:183], a[36:39], v[92:95], v[180:183]\n"
"buffer_load_dwordx4 v[124:127], v0, s[4:7], s97 offen offset:2048 sc1\n"
"v_mfma_f32_16x16x32_bf16 v[180:183], a[32:35], v[84:87], v[180:183]\n"
"ds_write_b128 v3, v[172:175]\n"
"v_mfma_f32_16x16x32_bf16 v[172:175], a[64:67], v[84:87], 0\n"
"buffer_load_dword v252, v10, s[4:7], s98 offen offset:768 sc1\n"
"v_mfma_f32_16x16x32_bf16 v[172:175], a[68:71], v[92:95], v[172:175]\n"
"buffer_load_dwordx4 v[184:187], v0, s[4:7], s97 offen offset:3072 sc1\n"
"v_mfma_f32_16x16x32_bf16 v[172:175], a[72:75], v[96:99], v[172:175]\n"
"v_cvt_pk_bf16_f32 v46, v32, v33\n"
"v_cvt_pk_bf16_f32 v47, v34, v35\n"
"v_mfma_f32_16x16x32_bf16 v[172:175], a[76:79], v[104:107], v[172:175]\n"
"buffer_load_dword v251, v10, s[4:7], s98 offen offset:1024 sc1\n"
"v_mfma_f32_16x16x32_bf16 v[172:175], a[80:83], v[112:115], v[172:175]\n"
"buffer_store_dwordx4 v[44:47], v0, s[4:7], s61 offen offset:1024 sc1\n"
"v_mfma_f32_16x16x32_bf16 v[172:175], a[84:87], v[116:119], v[172:175]\n"
"ds_write_b128 v3, v[180:183] offset:1024\n"
"v_mfma_f32_16x16x32_bf16 v[172:175], a[88:91], v[120:123], v[172:175]\n"
"buffer_load_dwordx4 v[32:35], v1, s[4:7], s97 offen sc1\n"
"v_mfma_f32_16x16x32_bf16 v[172:175], a[92:95], v[136:139], v[172:175]\n"
"buffer_load_dword v250, v10, s[4:7], s98 offen offset:1280 sc1\n"
"v_mfma_f32_16x16x32_bf16 v[180:183], a[124:127], v[136:139], 0\n"
"ds_write_b128 v2, v[44:47] offset:1024\n"
"v_mfma_f32_16x16x32_bf16 v[180:183], a[120:123], v[120:123], v[180:183]\n"
"v_mov_b32_e32 v188, 0x3f803f80\n"
"v_mov_b32_e32 v189, 0x3f803f80\n"
"v_mfma_f32_16x16x32_bf16 v[180:183], a[116:119], v[116:119], v[180:183]\n"
"buffer_load_dword v249, v10, s[4:7], s98 offen offset:1536 sc1\n"
"v_mfma_f32_16x16x32_bf16 v[180:183], a[112:115], v[112:115], v[180:183]\n"
"buffer_load_dwordx4 v[192:195], v1, s[4:7], s97 offen offset:1024 sc1\n"
"v_mfma_f32_16x16x32_bf16 v[180:183], a[108:111], v[104:107], v[180:183]\n"
"v_mov_b32_e32 v190, 0x3f803f80\n"
"v_mov_b32_e32 v191, 0x3f803f80\n"
"v_mfma_f32_16x16x32_bf16 v[180:183], a[104:107], v[96:99], v[180:183]\n"
"ds_write_b128 v3, v[172:175] offset:2048\n"
"v_mfma_f32_16x16x32_bf16 v[172:175], v[188:191], v[68:71], 0\n"
"v_mfma_f32_16x16x32_bf16 v[172:175], v[188:191], v[44:47], v[172:175]\n"
"v_mfma_f32_16x16x32_bf16 v[188:191], v[68:71], v[68:71], 0\n"
"v_mfma_f32_16x16x32_bf16 v[188:191], v[44:47], v[44:47], v[188:191]\n"
"v_mfma_f32_16x16x32_bf16 v[180:183], a[100:103], v[92:95], v[180:183]\n"
"v_mov_b32_e32 v44, -1\n"
"v_mov_b32_e32 v45, -1\n"
"v_mfma_f32_16x16x32_bf16 v[180:183], a[96:99], v[84:87], v[180:183]\n"
"v_mov_b32_e32 v46, -1\n"
"v_mov_b32_e32 v47, -1\n"
"v_mfma_f32_16x16x32_bf16 v[68:71], v[24:27], v[84:87], 0\n"
"buffer_load_dwordx4 v[196:199], v1, s[4:7], s97 offen offset:2048 sc1\n"
"v_mfma_f32_16x16x32_bf16 v[68:71], v[56:59], v[92:95], v[68:71]\n"
"s_add_u32 s96, s64, 0x4000\n"
"buffer_store_dwordx4 v[44:47], v2, s[4:7], s96 offen sc1\n"
"v_mfma_f32_16x16x32_bf16 v[68:71], v[28:31], v[96:99], v[68:71]\n"
"buffer_load_dword v248, v10, s[4:7], s98 offen offset:1792 sc1\n"
"v_mfma_f32_16x16x32_bf16 v[68:71], v[64:67], v[104:107], v[68:71]\n"
"s_mov_b32 s94, 0xaaaaaaaa\n"
"s_mov_b32 s95, 0xaaaaaaaa\n"
"v_mfma_f32_16x16x32_bf16 v[68:71], v[52:55], v[112:115], v[68:71]\n"
"v_mfma_f32_16x16x32_bf16 v[68:71], v[36:39], v[116:119], v[68:71]\n"
"buffer_load_dwordx4 v[200:203], v1, s[4:7], s97 offen offset:3072 sc1\n"
"v_mfma_f32_16x16x32_bf16 v[68:71], v[108:111], v[120:123], v[68:71]\n"
"ds_write_b128 v3, v[180:183] offset:3072\n"
"v_mfma_f32_16x16x32_bf16 v[68:71], v[60:63], v[136:139], v[68:71]\n"
"v_cndmask_b32_e64 v188, v188, v189, s[94:95]\n"
"v_cndmask_b32_e64 v190, v190, v191, s[94:95]\n"
"v_mfma_f32_16x16x32_bf16 v[180:183], v[80:83], v[136:139], 0\n"
"s_mov_b32 s94, 0xcccccccc\n"
"s_mov_b32 s95, 0xcccccccc\n"
"v_mfma_f32_16x16x32_bf16 v[180:183], v[88:91], v[120:123], v[180:183]\n"
"v_cndmask_b32_e64 v173, v188, v190, s[94:95]\n"
"v_mfma_f32_16x16x32_bf16 v[180:183], v[100:103], v[116:119], v[180:183]\n"
"s_mov_b32 s94, 0xf0000f\n"
"s_mov_b32 s95, 0xf0000f00\n"
"v_mfma_f32_16x16x32_bf16 v[180:183], v[76:79], v[112:115], v[180:183]\n"
"v_cndmask_b32_e64 v173, 0, v173, s[94:95]\n"
"v_mov_b32_e32 v247, v173\n"
"v_mfma_f32_16x16x32_bf16 v[180:183], v[132:135], v[104:107], v[180:183]\n"
"s_nop 0\n"
"v_permlane16_swap_b32_e32 v173, v247\n"
"v_add_f32_e32 v173, v173, v247\n"
"v_mfma_f32_16x16x32_bf16 v[180:183], v[128:131], v[96:99], v[180:183]\n"
"ds_write_b128 v3, v[68:71] offset:4096\n"
"v_mfma_f32_16x16x32_bf16 v[180:183], v[48:51], v[92:95], v[180:183]\n"
"v_mov_b32_e32 v247, v173\n"
"v_mfma_f32_16x16x32_bf16 v[180:183], v[72:75], v[84:87], v[180:183]\n"
"s_waitcnt vmcnt(22)\n"
"v_permlane32_swap_b32_e32 v173, v247\n"
"v_add_f32_e32 v173, v173, v247\n"
"; @A G1 0.1\n"
"v_mfma_f32_16x16x32_bf16 v[68:71], a[0:3], v[140:143], 0\n"
"s_mov_b32 s94, 0xffff0000\n"
"v_cndmask_b32_e64 v172, v172, v173, s[94:95]\n"
"v_mfma_f32_16x16x32_bf16 v[68:71], a[4:7], v[144:147], v[68:71]\n"
"buffer_store_dword v172, v14, s[4:7], s63 offen sc1\n"
"v_mfma_f32_16x16x32_bf16 v[68:71], a[8:11], v[148:151], v[68:71]\n"
"s_add_u32 s98, s55, 0x40000\n"
"buffer_load_dwordx4 v[84:87], v0, s[4:7], s98 offen sc1\n"
"v_mfma_f32_16x16x32_bf16 v[68:71], a[12:15], v[152:155], v[68:71]\n"
"s_waitcnt vmcnt(20)\n"
"v_mfma_f32_16x16x32_bf16 v[68:71], a[16:19], v[156:159], v[68:71]\n"
"v_mfma_f32_16x16x32_bf16 v[68:71], a[20:23], v[160:163], v[68:71]\n"
"ds_write_b128 v3, v[180:183] offset:5120\n"
"v_mfma_f32_16x16x32_bf16 v[68:71], a[24:27], v[164:167], v[68:71]\n"
"v_mfma_f32_16x16x32_bf16 v[68:71], a[28:31], v[168:171], v[68:71]\n"
"buffer_load_dwordx4 v[92:95], v0, s[4:7], s98 offen offset:1024 sc1\n"
"v_mfma_f32_16x16x32_bf16 v[96:99], a[60:63], v[168:171], 0\n"
"v_mfma_f32_16x16x32_bf16 v[96:99], a[56:59], v[164:167], v[96:99]\n"
"v_mfma_f32_16x16x32_bf16 v[96:99], a[52:55], v[160:163], v[96:99]\n"
"v_mfma_f32_16x16x32_bf16 v[96:99], a[48:51], v[156:159], v[96:99]\n"
"v_mfma_f32_16x16x32_bf16 v[96:99], a[44:47], v[152:155], v[96:99]\n"
"s_nop 1\n"
"v_cmp_u_f32_e32 vcc, v68, v68\n"
"v_mfma_f32_16x16x32_bf16 v[96:99], a[40:43], v[148:151], v[96:99]\n"
"s_cbranch_vccnz .Lmk_p77s\n"
".Lmk_p77r:\n"
"v_mfma_f32_16x16x32_bf16 v[96:99], a[36:39], v[144:147], v[96:99]\n"
"buffer_load_dwordx4 v[104:107], v0, s[4:7], s98 offen offset:2048 sc1\n"
"v_mfma_f32_16x16x32_bf16 v[96:99], a[32:35], v[140:143], v[96:99]\n"
"ds_write_b128 v3, v[68:71] offset:6144\n"
"v_mfma_f32_16x16x32_bf16 v[68:71], a[64:67], v[140:143], 0\n"
"v_mfma_f32_16x16x32_bf16 v[68:71], a[68:71], v[144:147], v[68:71]\n"
"buffer_load_dwordx4 v[112:115], v0, s[4:7], s98 offen offset:3072 sc1\n"
"v_mfma_f32_16x16x32_bf16 v[68:71], a[72:75], v[148:151], v[68:71]\n"
"v_mfma_f32_16x16x32_bf16 v[68:71], a[76:79], v[152:155], v[68:71]\n"
"v_mfma_f32_16x16x32_bf16 v[68:71], a[80:83], v[156:159], v[68:71]\n"
"v_mfma_f32_16x16x32_bf16 v[68:71], a[84:87], v[160:163], v[68:71]\n"
"ds_write_b128 v3, v[96:99] offset:7168\n"
"v_mfma_f32_16x16x32_bf16 v[68:71], a[88:91], v[164:167], v[68:71]\n"
"buffer_load_dwordx4 v[96:99], v1, s[4:7], s98 offen sc1\n"
"v_mfma_f32_16x16x32_bf16 v[68:71], a[92:95], v[168:171], v[68:71]\n"
"v_mfma_f32_16x16x32_bf16 v[116:119], a[124:127], v[168:171], 0\n"
"v_mfma_f32_16x16x32_bf16 v[116:119], a[120:123], v[164:167], v[116:119]\n"
"v_mfma_f32_16x16x32_bf16 v[116:119], a[116:119], v[160:163], v[116:119]\n"
"v_mfma_f32_16x16x32_bf16 v[116:119], a[112:115], v[156:159], v[116:119]\n"
"buffer_load_dwordx4 v[120:123], v1, s[4:7], s98 offen offset:1024 sc1\n"
"v_mfma_f32_16x16x32_bf16 v[116:119], a[108:111], v[152:155], v[116:119]\n"
"v_mfma_f32_16x16x32_bf16 v[116:119], a[104:107], v[148:151], v[116:119]\n"
"s_nop 0\n"
"ds_write_b128 v3, v[68:71] offset:8192\n"
"v_mfma_f32_16x16x32_bf16 v[116:119], a[100:103], v[144:147], v[116:119]\n"
"v_mfma_f32_16x16x32_bf16 v[116:119], a[96:99], v[140:143], v[116:119]\n"
"v_mfma_f32_16x16x32_bf16 v[68:71], v[24:27], v[140:143], 0\n"
"buffer_load_dwordx4 v[136:139], v1, s[4:7], s98 offen offset:2048 sc1\n"
"v_mfma_f32_16x16x32_bf16 v[68:71], v[56:59], v[144:147], v[68:71]\n"
"s_add_u32 s97, s64, 0x4000\n"
"v_mfma_f32_16x16x32_bf16 v[68:71], v[28:31], v[148:151], v[68:71]\n"
"v_mfma_f32_16x16x32_bf16 v[68:71], v[64:67], v[152:155], v[68:71]\n"
"buffer_store_dwordx4 v[44:47], v2, s[4:7], s97 offen offset:1024 sc1\n"
"v_mfma_f32_16x16x32_bf16 v[68:71], v[52:55], v[156:159], v[68:71]\n"
"s_waitcnt vmcnt(10)\n"
"v_add_f32_e32 v255, v255, v251\n"
"v_add_f32_e32 v254, v254, v250\n"
"v_mfma_f32_16x16x32_bf16 v[68:71], v[36:39], v[160:163], v[68:71]\n"
"buffer_load_dwordx4 v[172:175], v1, s[4:7], s98 offen offset:3072 sc1\n"
"v_mfma_f32_16x16x32_bf16 v[68:71], v[108:111], v[164:167], v[68:71]\n"
"ds_write_b128 v3, v[116:119] offset:9216\n"
"v_mfma_f32_16x16x32_bf16 v[68:71], v[60:63], v[168:171], v[68:71]\n"
"v_add_f32_e32 v253, v253, v249\n"
"v_add_f32_e32 v252, v252, v248\n"
"v_mfma_f32_16x16x32_bf16 v[116:119], v[80:83], v[168:171], 0\n"
"v_add_f32_e32 v255, v255, v253\n"
"v_add_f32_e32 v254, v254, v252\n"
"v_mfma_f32_16x16x32_bf16 v[116:119], v[88:91], v[164:167], v[116:119]\n"
"v_add_f32_e32 v255, v255, v254\n"
"v_cmp_u_f32_e32 vcc, v255, v255\n"
"v_mfma_f32_16x16x32_bf16 v[116:119], v[100:103], v[160:163], v[116:119]\n"
"s_cbranch_vccnz .Lmk_m80s\n"
".Lmk_m80r:\n"
"v_mov_b32_e32 v254, v255\n"
"v_mfma_f32_16x16x32_bf16 v[116:119], v[76:79], v[156:159], v[116:119]\n"
"s_nop 0\n"
"v_permlane32_swap_b32_e32 v255, v254\n"
"v_add_f32_e32 v255, v255, v254\n"
"v_mfma_f32_16x16x32_bf16 v[116:119], v[132:135], v[152:155], v[116:119]\n"
"v_mul_f32_e32 v255, 0x3a800000, v255\n"
"v_mov_b32_e32 v254, v255\n"
"v_mfma_f32_16x16x32_bf16 v[116:119], v[128:131], v[148:151], v[116:119]\n"
"ds_write_b128 v3, v[68:71] offset:10240\n"
"v_mfma_f32_16x16x32_bf16 v[116:119], v[48:51], v[144:147], v[116:119]\n"
"v_permlane16_swap_b32_e32 v255, v254\n"
"v_cvt_pk_bf16_f32 v253, -v255, 0\n"
"v_mfma_f32_16x16x32_bf16 v[116:119], v[72:75], v[140:143], v[116:119]\n"
"v_fma_f32 v254, -v255, v255, v254\n"
"v_lshlrev_b32_e32 v253, 16, v253\n"
"; @A G1 0.2\n"
"v_mfma_f32_16x16x32_bf16 v[68:71], a[0:3], v[176:179], 0\n"
"v_max_f32_e32 v254, 0, v254\n"
"v_cvt_pk_bf16_f32 v254, v254, 0\n"
"v_mfma_f32_16x16x32_bf16 v[68:71], a[4:7], v[40:43], v[68:71]\n"
"v_lshlrev_b32_e32 v254, 16, v254\n"
"v_add_f32_e32 v254, 0x3727c5ac, v254\n"
"v_mfma_f32_16x16x32_bf16 v[68:71], a[8:11], v[124:127], v[68:71]\n"
"v_cvt_pk_bf16_f32 v254, v254, 0\n"
"v_lshlrev_b32_e32 v254, 16, v254\n"
"v_mfma_f32_16x16x32_bf16 v[68:71], a[12:15], v[184:187], v[68:71]\n"
"s_waitcnt vmcnt(10)\n"
"v_sqrt_f32_e32 v254, v254\n"
"v_mfma_f32_16x16x32_bf16 v[68:71], a[16:19], v[32:35], v[68:71]\n"
"s_mov_b32 s98, .Lmk_a2-.Lmk_g75\n"
"s_getpc_b64 s[84:85]\n"
".Lmk_g75:\n"
"s_add_u32 s84, s84, s98\n"
"s_addc_u32 s85, s85, 0\n"
"s_and_b32 s84, s84, -16\n"
"v_mfma_f32_16x16x32_bf16 v[68:71], a[20:23], v[192:195], v[68:71]\n"
"ds_write_b128 v3, v[116:119] offset:11264\n"
"v_mfma_f32_16x16x32_bf16 v[68:71], a[24:27], v[196:199], v[68:71]\n"
"s_and_b32 s85, s85, 0xffff\n"
"s_sub_u32 s98, .Lmk_end-.Lmk_g75, s98\n"
"s_min_u32 s86, s98, 0x3000\n"
"s_mov_b32 s87, 0x20000\n"
"v_mfma_f32_16x16x32_bf16 v[68:71], a[28:31], v[200:203], v[68:71]\n"
"s_mov_b32 m0, 0x27800\n"
"s_nop 0\n"
"buffer_load_dwordx4 v0, s[84:87], s99 offen lds\n"
"v_mfma_f32_16x16x32_bf16 v[116:119], a[60:63], v[200:203], 0\n"
"v_cvt_pk_bf16_f32 v254, v254, 0\n"
"v_lshlrev_b32_e32 v254, 16, v254\n"
"v_mfma_f32_16x16x32_bf16 v[116:119], a[56:59], v[196:199], v[116:119]\n"
"v_rcp_f32_e32 v255, v254\n"
"v_mfma_f32_16x16x32_bf16 v[116:119], a[52:55], v[192:195], v[116:119]\n"
"s_add_u32 s98, s52, 0x48000\n"
"buffer_load_dwordx4 v[140:143], v0, s[4:7], s98 offen sc1\n"
"v_mfma_f32_16x16x32_bf16 v[116:119], a[48:51], v[32:35], v[116:119]\n"
"s_waitcnt lgkmcnt(0)\n"
"s_barrier\n"
"v_mfma_f32_16x16x32_bf16 v[116:119], a[44:47], v[184:187], v[116:119]\n"
"v_cmp_u_f32_e32 vcc, v68, v68\n"
"v_mul_f32_e32 v254, 0x3e8293ee, v255\n"
"v_mfma_f32_16x16x32_bf16 v[116:119], a[40:43], v[124:127], v[116:119]\n"
"s_cbranch_vccnz .Lmk_p78s\n"
".Lmk_p78r:\n"
"ds_read_b128 v[144:147], v4\n"
"v_mfma_f32_16x16x32_bf16 v[116:119], a[36:39], v[40:43], v[116:119]\n"
"ds_write_b128 v3, v[68:71] offset:12288\n"
"v_mfma_f32_16x16x32_bf16 v[116:119], a[32:35], v[176:179], v[116:119]\n"
"buffer_load_dwordx4 v[68:71], v0, s[4:7], s98 offen offset:1024 sc1\n"
"v_mfma_f32_16x16x32_bf16 v[148:151], a[64:67], v[176:179], 0\n"
"ds_read_b128 v[152:155], v9 offset:128\n"
"ds_read_b128 v[156:159], v5\n"
"v_mfma_f32_16x16x32_bf16 v[148:151], a[68:71], v[40:43], v[148:151]\n"
"v_mfma_f32_16x16x32_bf16 v[148:151], a[72:75], v[124:127], v[148:151]\n"
"v_mfma_f32_16x16x32_bf16 v[148:151], a[76:79], v[184:187], v[148:151]\n"
"ds_read_b128 v[160:163], v4 offset:1024\n"
"ds_read_b128 v[164:167], v9 offset:192\n"
"v_mfma_f32_16x16x32_bf16 v[148:151], a[80:83], v[32:35], v[148:151]\n"
"buffer_load_dwordx4 v[168:171], v0, s[4:7], s98 offen offset:2048 sc1\n"
"v_mfma_f32_16x16x32_bf16 v[148:151], a[84:87], v[192:195], v[148:151]\n"
"ds_write_b128 v3, v[116:119] offset:13312\n"
"v_mfma_f32_16x16x32_bf16 v[148:151], a[88:91], v[196:199], v[148:151]\n"
"ds_read_b128 v[116:119], v5 offset:1024\n"
"s_waitcnt lgkmcnt(4)\n"
"v_mfma_f32_16x16x32_bf16 v[148:151], a[92:95], v[200:203], v[148:151]\n"
"v_fmac_f32_e32 v144, v253, v152\n"
"v_fmac_f32_e32 v145, v253, v153\n"
"v_mfma_f32_16x16x32_bf16 v[180:183], a[124:127], v[200:203], 0\n"
"v_fmac_f32_e32 v146, v253, v154\n"
"v_fmac_f32_e32 v147, v253, v155\n"
"v_mfma_f32_16x16x32_bf16 v[180:183], a[120:123], v[196:199], v[180:183]\n"
"buffer_load_dwordx4 v[152:155], v0, s[4:7], s98 offen offset:3072 sc1\n"
"v_mfma_f32_16x16x32_bf16 v[180:183], a[116:119], v[192:195], v[180:183]\n"
"ds_read_b128 v[188:191], v4 offset:2048\n"
"ds_read_b128 v[204:207], v9 offset:256\n"
"v_mfma_f32_16x16x32_bf16 v[180:183], a[112:115], v[32:35], v[180:183]\n"
"ds_read_b128 v[208:211], v5 offset:2048\n"
"v_add_f32_e32 v144, v144, v156\n"
"v_mfma_f32_16x16x32_bf16 v[180:183], a[108:111], v[184:187], v[180:183]\n"
"v_add_f32_e32 v145, v145, v157\n"
"v_add_f32_e32 v146, v146, v158\n"
"v_mfma_f32_16x16x32_bf16 v[180:183], a[104:107], v[124:127], v[180:183]\n"
"ds_write_b128 v3, v[148:151] offset:14336\n"
"v_mfma_f32_16x16x32_bf16 v[180:183], a[100:103], v[40:43], v[180:183]\n"
"buffer_load_dwordx4 v[148:151], v1, s[4:7], s98 offen sc1\n"
"v_mfma_f32_16x16x32_bf16 v[180:183], a[96:99], v[176:179], v[180:183]\n"
"s_waitcnt lgkmcnt(4)\n"
"v_add_f32_e32 v147, v147, v159\n"
"v_fmac_f32_e32 v160, v253, v164\n"
"v_mfma_f32_16x16x32_bf16 v[156:159], v[24:27], v[176:179], 0\n"
"v_fmac_f32_e32 v161, v253, v165\n"
"v_fmac_f32_e32 v162, v253, v166\n"
"v_mfma_f32_16x16x32_bf16 v[156:159], v[56:59], v[40:43], v[156:159]\n"
"ds_read_b128 v[212:215], v4 offset:3072\n"
"ds_read_b128 v[216:219], v9 offset:320\n"
"v_mfma_f32_16x16x32_bf16 v[156:159], v[28:31], v[124:127], v[156:159]\n"
"ds_read_b128 v[220:223], v5 offset:3072\n"
"v_fmac_f32_e32 v163, v253, v167\n"
"v_mfma_f32_16x16x32_bf16 v[156:159], v[64:67], v[184:187], v[156:159]\n"
"buffer_load_dwordx4 v[164:167], v1, s[4:7], s98 offen offset:1024 sc1\n"
"v_mfma_f32_16x16x32_bf16 v[156:159], v[52:55], v[32:35], v[156:159]\n"
"s_add_u32 s97, s64, 0x8000\n"
"v_add_f32_e32 v160, v160, v116\n"
"v_add_f32_e32 v161, v161, v117\n"
"v_mfma_f32_16x16x32_bf16 v[156:159], v[36:39], v[192:195], v[156:159]\n"
"ds_write_b128 v3, v[180:183] offset:15360\n"
"v_mfma_f32_16x16x32_bf16 v[156:159], v[108:111], v[196:199], v[156:159]\n"
"buffer_store_dwordx4 v[44:47], v2, s[4:7], s97 offen sc1\n"
"v_mfma_f32_16x16x32_bf16 v[156:159], v[60:63], v[200:203], v[156:159]\n"
"s_waitcnt lgkmcnt(5)\n"
"v_add_f32_e32 v162, v162, v118\n"
"v_add_f32_e32 v163, v163, v119\n"
"v_mfma_f32_16x16x32_bf16 v[116:119], v[80:83], v[200:203], 0\n"
"buffer_load_dwordx4 v[180:183], v1, s[4:7], s98 offen offset:2048 sc1\n"
"v_mfma_f32_16x16x32_bf16 v[116:119], v[88:91], v[196:199], v[116:119]\n"
"s_add_u32 s97, s68, s80\n"
"s_waitcnt vmcnt(18)\n"
"ds_read_b128 v[196:199], v4 offset:4096\n"
"v_mfma_f32_16x16x32_bf16 v[116:119], v[100:103], v[192:195], v[116:119]\n"
"buffer_store_dwordx2 v[18:19], v11, s[4:7], s97 offen sc1\n"
"v_mfma_f32_16x16x32_bf16 v[116:119], v[76:79], v[32:35], v[116:119]\n"
"ds_read_b128 v[32:35], v9\n"
"ds_read_b128 v[192:195], v5 offset:4096\n"
"v_mfma_f32_16x16x32_bf16 v[116:119], v[132:135], v[184:187], v[116:119]\n"
"v_fmac_f32_e32 v188, v253, v204\n"
"v_fmac_f32_e32 v189, v253, v205\n"
"v_mfma_f32_16x16x32_bf16 v[116:119], v[128:131], v[124:127], v[116:119]\n"
"buffer_load_dwordx4 v[124:127], v1, s[4:7], s98 offen offset:3072 sc1\n"
"v_mfma_f32_16x16x32_bf16 v[116:119], v[48:51], v[40:43], v[116:119]\n"
"ds_write_b128 v3, v[156:159] offset:16384\n"
"v_mfma_f32_16x16x32_bf16 v[116:119], v[72:75], v[176:179], v[116:119]\n"
"s_waitcnt vmcnt(16)\n"
"ds_read_b128 v[40:43], v4 offset:5120\n"
"ds_read_b128 v[156:159], v9 offset:64\n"
"; @A G1 0.3\n"
"v_mfma_f32_16x16x32_bf16 v[176:179], a[0:3], v[84:87], v[144:147]\n"
"ds_read_b128 v[184:187], v5 offset:5120\n"
"v_fmac_f32_e32 v190, v253, v206\n"
"v_mfma_f32_16x16x32_bf16 v[176:179], a[4:7], v[92:95], v[176:179]\n"
"v_fmac_f32_e32 v191, v253, v207\n"
"v_add_f32_e32 v188, v188, v208\n"
"v_mfma_f32_16x16x32_bf16 v[176:179], a[8:11], v[104:107], v[176:179]\n"
"s_add_u32 s98, s53, 0x48000\n"
"buffer_load_dwordx4 v[200:203], v0, s[4:7], s98 offen sc1\n"
"v_mfma_f32_16x16x32_bf16 v[176:179], a[12:15], v[112:115], v[176:179]\n"
"s_waitcnt vmcnt(12)\n"
"v_add_f32_e32 v189, v189, v209\n"
"v_add_f32_e32 v190, v190, v210\n"
"v_mfma_f32_16x16x32_bf16 v[176:179], a[16:19], v[96:99], v[176:179]\n"
"s_waitcnt lgkmcnt(8)\n"
"v_add_f32_e32 v191, v191, v211\n"
"v_fmac_f32_e32 v212, v253, v216\n"
"v_mfma_f32_16x16x32_bf16 v[176:179], a[20:23], v[120:123], v[176:179]\n"
"ds_write_b128 v3, v[116:119] offset:17408\n"
"v_mfma_f32_16x16x32_bf16 v[176:179], a[24:27], v[136:139], v[176:179]\n"
"v_fmac_f32_e32 v213, v253, v217\n"
"v_fmac_f32_e32 v214, v253, v218\n"
"v_mfma_f32_16x16x32_bf16 v[176:179], a[28:31], v[172:175], v[176:179]\n"
"buffer_load_dwordx4 v[116:119], v0, s[4:7], s98 offen offset:1024 sc1\n"
"v_mfma_f32_16x16x32_bf16 v[204:207], a[60:63], v[172:175], v[160:163]\n"
"v_fmac_f32_e32 v215, v253, v219\n"
"v_add_f32_e32 v212, v212, v220\n"
"v_mfma_f32_16x16x32_bf16 v[204:207], a[56:59], v[136:139], v[204:207]\n"
"v_add_f32_e32 v213, v213, v221\n"
"v_add_f32_e32 v214, v214, v222\n"
"v_mfma_f32_16x16x32_bf16 v[204:207], a[52:55], v[120:123], v[204:207]\n"
"s_waitcnt lgkmcnt(0)\n"
"s_barrier\n"
"v_mfma_f32_16x16x32_bf16 v[204:207], a[48:51], v[96:99], v[204:207]\n"
"ds_read_b128 v[208:211], v6\n"
"ds_read_b128 v[216:219], v6 offset:1024\n"
"v_mfma_f32_16x16x32_bf16 v[204:207], a[44:47], v[112:115], v[204:207]\n"
"v_cmp_u_f32_e32 vcc, v176, v176\n"
"v_add_f32_e32 v215, v215, v223\n"
"v_mfma_f32_16x16x32_bf16 v[204:207], a[40:43], v[104:107], v[204:207]\n"
"s_cbranch_vccnz .Lmk_p79s\n"
".Lmk_p79r:\n"
"v_fmac_f32_e32 v196, v253, v32\n"
"v_mfma_f32_16x16x32_bf16 v[204:207], a[36:39], v[92:95], v[204:207]\n"
"buffer_load_dwordx4 v[144:147], v0, s[4:7], s98 offen offset:2048 sc1\n"
"v_mfma_f32_16x16x32_bf16 v[204:207], a[32:35], v[84:87], v[204:207]\n"
"v_fmac_f32_e32 v197, v253, v33\n"
"v_fmac_f32_e32 v198, v253, v34\n"
"v_mfma_f32_16x16x32_bf16 v[188:191], a[64:67], v[84:87], v[188:191]\n"
"v_fmac_f32_e32 v199, v253, v35\n"
"v_add_f32_e32 v196, v196, v192\n"
"v_mfma_f32_16x16x32_bf16 v[188:191], a[68:71], v[92:95], v[188:191]\n"
"buffer_load_dwordx4 v[32:35], v0, s[4:7], s98 offen offset:3072 sc1\n"
"v_mfma_f32_16x16x32_bf16 v[188:191], a[72:75], v[104:107], v[188:191]\n"
"v_add_f32_e32 v197, v197, v193\n"
"v_add_f32_e32 v198, v198, v194\n"
"v_mfma_f32_16x16x32_bf16 v[188:191], a[76:79], v[112:115], v[188:191]\n"
"v_add_f32_e32 v199, v199, v195\n"
"v_fmac_f32_e32 v40, v253, v156\n"
"v_mfma_f32_16x16x32_bf16 v[188:191], a[80:83], v[96:99], v[188:191]\n"
"s_waitcnt lgkmcnt(0)\n"
"v_fmac_f32_e32 v41, v253, v157\n"
"v_fmac_f32_e32 v42, v253, v158\n"
"v_mfma_f32_16x16x32_bf16 v[188:191], a[84:87], v[120:123], v[188:191]\n"
"v_fmac_f32_e32 v43, v253, v159\n"
"v_add_f32_e32 v40, v40, v184\n"
"v_mfma_f32_16x16x32_bf16 v[188:191], a[88:91], v[136:139], v[188:191]\n"
"buffer_load_dwordx4 v[156:159], v1, s[4:7], s98 offen sc1\n"
"v_mfma_f32_16x16x32_bf16 v[188:191], a[92:95], v[172:175], v[188:191]\n"
"ds_read_b128 v[160:163], v6 offset:2048\n"
"ds_read_b128 v[192:195], v6 offset:3072\n"
"v_mfma_f32_16x16x32_bf16 v[212:215], a[124:127], v[172:175], v[212:215]\n"
"v_add_f32_e32 v41, v41, v185\n"
"v_add_f32_e32 v42, v42, v186\n"
"v_mfma_f32_16x16x32_bf16 v[212:215], a[120:123], v[136:139], v[212:215]\n"
"v_add_f32_e32 v43, v43, v187\n"
"v_add_f32_e32 v176, v176, v208\n"
"v_mfma_f32_16x16x32_bf16 v[212:215], a[116:119], v[120:123], v[212:215]\n"
"v_add_f32_e32 v177, v177, v209\n"
"v_add_f32_e32 v178, v178, v210\n"
"v_mfma_f32_16x16x32_bf16 v[212:215], a[112:115], v[96:99], v[212:215]\n"
"buffer_load_dwordx4 v[184:187], v1, s[4:7], s98 offen offset:1024 sc1\n"
"v_mfma_f32_16x16x32_bf16 v[212:215], a[108:111], v[112:115], v[212:215]\n"
"v_add_f32_e32 v179, v179, v211\n"
"v_add_f32_e32 v204, v204, v216\n"
"v_mfma_f32_16x16x32_bf16 v[212:215], a[104:107], v[104:107], v[212:215]\n"
"v_add_f32_e32 v205, v205, v217\n"
"v_add_f32_e32 v206, v206, v218\n"
"v_mfma_f32_16x16x32_bf16 v[212:215], a[100:103], v[92:95], v[212:215]\n"
"v_add_f32_e32 v207, v207, v219\n"
"v_mul_f32_e32 v176, v176, v255\n"
"v_mfma_f32_16x16x32_bf16 v[212:215], a[96:99], v[84:87], v[212:215]\n"
"v_mul_f32_e32 v177, v177, v255\n"
"v_mul_f32_e32 v178, v178, v255\n"
"v_mfma_f32_16x16x32_bf16 v[196:199], v[24:27], v[84:87], v[196:199]\n"
"buffer_load_dwordx4 v[208:211], v1, s[4:7], s98 offen offset:2048 sc1\n"
"v_mfma_f32_16x16x32_bf16 v[196:199], v[56:59], v[92:95], v[196:199]\n"
"v_mul_f32_e32 v179, v179, v255\n"
"v_mul_f32_e32 v204, v204, v255\n"
"v_mfma_f32_16x16x32_bf16 v[196:199], v[28:31], v[104:107], v[196:199]\n"
"v_mul_f32_e32 v205, v205, v255\n"
"v_mul_f32_e32 v206, v206, v255\n"
"v_mfma_f32_16x16x32_bf16 v[196:199], v[64:67], v[112:115], v[196:199]\n"
"v_mul_f32_e32 v207, v207, v255\n"
"v_cvt_pk_bf16_f32 v216, v176, v177\n"
"v_mfma_f32_16x16x32_bf16 v[196:199], v[52:55], v[96:99], v[196:199]\n"
"s_waitcnt lgkmcnt(0)\n"
"v_cvt_pk_bf16_f32 v217, v178, v179\n"
"v_cvt_pk_bf16_f32 v218, v204, v205\n"
"v_mfma_f32_16x16x32_bf16 v[196:199], v[36:39], v[120:123], v[196:199]\n"
"buffer_load_dwordx4 v[176:179], v1, s[4:7], s98 offen offset:3072 sc1\n"
"v_mfma_f32_16x16x32_bf16 v[196:199], v[108:111], v[136:139], v[196:199]\n"
"s_add_u32 s98, s64, 0x8000\n"
"ds_read_b128 v[220:223], v6 offset:4096\n"
"ds_read_b128 v[224:227], v6 offset:5120\n"
"v_mfma_f32_16x16x32_bf16 v[196:199], v[60:63], v[172:175], v[196:199]\n"
"v_cvt_pk_bf16_f32 v219, v206, v207\n"
"v_add_f32_e32 v188, v188, v160\n"
"v_mfma_f32_16x16x32_bf16 v[40:43], v[80:83], v[172:175], v[40:43]\n"
"buffer_store_dwordx4 v[44:47], v2, s[4:7], s98 offen offset:1024 sc1\n"
"v_mfma_f32_16x16x32_bf16 v[40:43], v[88:91], v[136:139], v[40:43]\n"
"ds_write_b128 v7, v[216:219] offset:24576\n"
"v_mfma_f32_16x16x32_bf16 v[40:43], v[100:103], v[120:123], v[40:43]\n"
"v_add_f32_e32 v189, v189, v161\n"
"v_add_f32_e32 v190, v190, v162\n"
"v_mfma_f32_16x16x32_bf16 v[40:43], v[76:79], v[96:99], v[40:43]\n"
"v_add_f32_e32 v191, v191, v163\n"
"v_add_f32_e32 v212, v212, v192\n"
"v_mfma_f32_16x16x32_bf16 v[40:43], v[132:135], v[112:115], v[40:43]\n"
"v_add_f32_e32 v213, v213, v193\n"
"v_add_f32_e32 v214, v214, v194\n"
"v_mfma_f32_16x16x32_bf16 v[40:43], v[128:131], v[104:107], v[40:43]\n"
"v_add_f32_e32 v215, v215, v195\n"
"v_mul_f32_e32 v188, v188, v255\n"
"v_mfma_f32_16x16x32_bf16 v[40:43], v[48:51], v[92:95], v[40:43]\n"
"v_mul_f32_e32 v189, v189, v255\n"
"v_mul_f32_e32 v190, v190, v255\n"
"v_mfma_f32_16x16x32_bf16 v[40:43], v[72:75], v[84:87], v[40:43]\n"
"v_mul_f32_e32 v191, v191, v255\n"
"v_mul_f32_e32 v212, v212, v255\n"
"v_mul_f32_e32 v213, v213, v255\n"
"v_mul_f32_e32 v214, v214, v255\n"
"v_mul_f32_e32 v215, v215, v255\n"
"v_cvt_pk_bf16_f32 v44, v188, v189\n"
"v_cvt_pk_bf16_f32 v45, v190, v191\n"
"v_cvt_pk_bf16_f32 v46, v212, v213\n"
"v_cvt_pk_bf16_f32 v47, v214, v215\n"
"ds_write_b128 v7, v[44:47] offset:57344\n"
".Lmk_a1:\n"
"s_waitcnt vmcnt(15)\n"
"; @A G1 1.0\n"
"v_mfma_f32_16x16x32_bf16 v[84:87], a[0:3], v[140:143], 0\n"
"v_mfma_f32_16x16x32_bf16 v[84:87], a[4:7], v[68:71], v[84:87]\n"
"s_add_u32 s98, s62, 0x4800\n"
"v_mfma_f32_16x16x32_bf16 v[84:87], a[8:11], v[168:171], v[84:87]\n"
"s_add_u32 s97, s54, 0x48000\n"
"buffer_load_dwordx4 v[92:95], v0, s[4:7], s97 offen sc1\n"
"v_mfma_f32_16x16x32_bf16 v[84:87], a[12:15], v[152:155], v[84:87]\n"
"s_waitcnt vmcnt(10)\n"
"v_mfma_f32_16x16x32_bf16 v[84:87], a[16:19], v[148:151], v[84:87]\n"
"buffer_load_dword v255, v10, s[4:7], s98 offen sc1\n"
"v_mfma_f32_16x16x32_bf16 v[84:87], a[20:23], v[164:167], v[84:87]\n"
"s_waitcnt lgkmcnt(2)\n"
"v_add_f32_e32 v196, v196, v220\n"
"v_add_f32_e32 v197, v197, v221\n"
"v_mfma_f32_16x16x32_bf16 v[84:87], a[24:27], v[180:183], v[84:87]\n"
"v_add_f32_e32 v198, v198, v222\n"
"v_add_f32_e32 v199, v199, v223\n"
"v_mfma_f32_16x16x32_bf16 v[84:87], a[28:31], v[124:127], v[84:87]\n"
"buffer_load_dwordx4 v[96:99], v0, s[4:7], s97 offen offset:1024 sc1\n"
"v_mfma_f32_16x16x32_bf16 v[104:107], a[60:63], v[124:127], 0\n"
"s_add_u32 s96, s70, 0\n"
"buffer_load_dwordx4 a[128:131], v0, s[4:7], s96 offen sc1\n"
"v_mfma_f32_16x16x32_bf16 v[104:107], a[56:59], v[180:183], v[104:107]\n"
"buffer_load_dword v253, v10, s[4:7], s98 offen offset:256 sc1\n"
"v_mfma_f32_16x16x32_bf16 v[104:107], a[52:55], v[164:167], v[104:107]\n"
"v_add_f32_e32 v40, v40, v224\n"
"v_add_f32_e32 v41, v41, v225\n"
"v_mfma_f32_16x16x32_bf16 v[104:107], a[48:51], v[148:151], v[104:107]\n"
"v_add_f32_e32 v42, v42, v226\n"
"v_add_f32_e32 v43, v43, v227\n"
"v_mfma_f32_16x16x32_bf16 v[104:107], a[44:47], v[152:155], v[104:107]\n"
"v_cmp_u_f32_e32 vcc, v84, v84\n"
"v_mul_f32_e32 v196, v196, v254\n"
"v_mfma_f32_16x16x32_bf16 v[104:107], a[40:43], v[168:171], v[104:107]\n"
"s_cbranch_vccnz .Lmk_p81s\n"
".Lmk_p81r:\n"
"v_mul_f32_e32 v197, v197, v254\n"
"v_mfma_f32_16x16x32_bf16 v[104:107], a[36:39], v[68:71], v[104:107]\n"
"buffer_load_dwordx4 v[112:115], v0, s[4:7], s97 offen offset:2048 sc1\n"
"v_mfma_f32_16x16x32_bf16 v[104:107], a[32:35], v[140:143], v[104:107]\n"
"ds_write_b128 v3, v[84:87]\n"
"v_mfma_f32_16x16x32_bf16 v[84:87], a[64:67], v[140:143], 0\n"
"buffer_load_dword v252, v10, s[4:7], s98 offen offset:512 sc1\n"
"v_mfma_f32_16x16x32_bf16 v[84:87], a[68:71], v[68:71], v[84:87]\n"
"buffer_load_dwordx4 v[120:123], v0, s[4:7], s97 offen offset:3072 sc1\n"
"v_mfma_f32_16x16x32_bf16 v[84:87], a[72:75], v[168:171], v[84:87]\n"
"v_mul_f32_e32 v198, v198, v254\n"
"v_mul_f32_e32 v199, v199, v254\n"
"v_mfma_f32_16x16x32_bf16 v[84:87], a[76:79], v[152:155], v[84:87]\n"
"buffer_load_dword v251, v10, s[4:7], s98 offen offset:768 sc1\n"
"v_mfma_f32_16x16x32_bf16 v[84:87], a[80:83], v[148:151], v[84:87]\n"
"v_mul_f32_e32 v40, v40, v254\n"
"v_mul_f32_e32 v41, v41, v254\n"
"v_mfma_f32_16x16x32_bf16 v[84:87], a[84:87], v[164:167], v[84:87]\n"
"ds_write_b128 v3, v[104:107] offset:1024\n"
"v_mfma_f32_16x16x32_bf16 v[84:87], a[88:91], v[180:183], v[84:87]\n"
"buffer_load_dwordx4 v[104:107], v1, s[4:7], s97 offen sc1\n"
"v_mfma_f32_16x16x32_bf16 v[84:87], a[92:95], v[124:127], v[84:87]\n"
"buffer_load_dword v250, v10, s[4:7], s98 offen offset:1024 sc1\n"
"v_mfma_f32_16x16x32_bf16 v[136:139], a[124:127], v[124:127], 0\n"
"v_mul_f32_e32 v42, v42, v254\n"
"v_mul_f32_e32 v43, v43, v254\n"
"v_mfma_f32_16x16x32_bf16 v[136:139], a[120:123], v[180:183], v[136:139]\n"
"v_cvt_pk_bf16_f32 v160, v196, v197\n"
"v_cvt_pk_bf16_f32 v161, v198, v199\n"
"v_mfma_f32_16x16x32_bf16 v[136:139], a[116:119], v[164:167], v[136:139]\n"
"buffer_load_dword v254, v10, s[4:7], s98 offen offset:1280 sc1\n"
"v_mfma_f32_16x16x32_bf16 v[136:139], a[112:115], v[148:151], v[136:139]\n"
"buffer_load_dwordx4 v[172:175], v1, s[4:7], s97 offen offset:1024 sc1\n"
"v_mfma_f32_16x16x32_bf16 v[136:139], a[108:111], v[152:155], v[136:139]\n"
"buffer_load_dwordx4 a[132:135], v0, s[4:7], s96 offen offset:1024 sc1\n"
"v_mfma_f32_16x16x32_bf16 v[136:139], a[104:107], v[168:171], v[136:139]\n"
"ds_write_b128 v3, v[84:87] offset:2048\n"
"v_mfma_f32_16x16x32_bf16 v[136:139], a[100:103], v[68:71], v[136:139]\n"
"buffer_load_dword v249, v10, s[4:7], s98 offen offset:1536 sc1\n"
"v_mfma_f32_16x16x32_bf16 v[136:139], a[96:99], v[140:143], v[136:139]\n"
"v_cvt_pk_bf16_f32 v162, v40, v41\n"
"v_cvt_pk_bf16_f32 v163, v42, v43\n"
"v_mfma_f32_16x16x32_bf16 v[40:43], v[24:27], v[140:143], 0\n"
"buffer_load_dwordx4 v[84:87], v1, s[4:7], s97 offen offset:2048 sc1\n"
"v_mfma_f32_16x16x32_bf16 v[40:43], v[56:59], v[68:71], v[40:43]\n"
"buffer_load_dword v248, v10, s[4:7], s98 offen offset:1792 sc1\n"
"v_mfma_f32_16x16x32_bf16 v[40:43], v[28:31], v[168:171], v[40:43]\n"
"ds_write_b128 v2, v[160:163] offset:49152\n"
"v_mfma_f32_16x16x32_bf16 v[40:43], v[64:67], v[152:155], v[40:43]\n"
"v_mfma_f32_16x16x32_bf16 v[40:43], v[52:55], v[148:151], v[40:43]\n"
"v_mfma_f32_16x16x32_bf16 v[40:43], v[36:39], v[164:167], v[40:43]\n"
"buffer_load_dwordx4 v[160:163], v1, s[4:7], s97 offen offset:3072 sc1\n"
"v_mfma_f32_16x16x32_bf16 v[40:43], v[108:111], v[180:183], v[40:43]\n"
"ds_write_b128 v3, v[136:139] offset:3072\n"
"v_mfma_f32_16x16x32_bf16 v[40:43], v[60:63], v[124:127], v[40:43]\n"
"v_mfma_f32_16x16x32_bf16 v[136:139], v[80:83], v[124:127], 0\n"
"v_mfma_f32_16x16x32_bf16 v[136:139], v[88:91], v[180:183], v[136:139]\n"
"v_mfma_f32_16x16x32_bf16 v[136:139], v[100:103], v[164:167], v[136:139]\n"
"v_mfma_f32_16x16x32_bf16 v[136:139], v[76:79], v[148:151], v[136:139]\n"
"v_mfma_f32_16x16x32_bf16 v[136:139], v[132:135], v[152:155], v[136:139]\n"
"v_mfma_f32_16x16x32_bf16 v[136:139], v[128:131], v[168:171], v[136:139]\n"
"s_nop 1\n"
"ds_write_b128 v3, v[40:43] offset:4096\n"
"v_mfma_f32_16x16x32_bf16 v[136:139], v[48:51], v[68:71], v[136:139]\n"
"v_mfma_f32_16x16x32_bf16 v[136:139], v[72:75], v[140:143], v[136:139]\n"
"s_waitcnt vmcnt(23)\n"
"; @A G1 1.1\n"
"v_mfma_f32_16x16x32_bf16 v[40:43], a[0:3], v[200:203], 0\n"
"buffer_load_dwordx4 a[136:139], v0, s[4:7], s96 offen offset:2048 sc1\n"
"v_mfma_f32_16x16x32_bf16 v[40:43], a[4:7], v[116:119], v[40:43]\n"
"v_mfma_f32_16x16x32_bf16 v[40:43], a[8:11], v[144:147], v[40:43]\n"
"s_add_u32 s98, s55, 0x48000\n"
"buffer_load_dwordx4 v[68:71], v0, s[4:7], s98 offen sc1\n"
"v_mfma_f32_16x16x32_bf16 v[40:43], a[12:15], v[32:35], v[40:43]\n"
"s_waitcnt vmcnt(21)\n"
"v_mfma_f32_16x16x32_bf16 v[40:43], a[16:19], v[156:159], v[40:43]\n"
"v_mfma_f32_16x16x32_bf16 v[40:43], a[20:23], v[184:187], v[40:43]\n"
"ds_write_b128 v3, v[136:139] offset:5120\n"
"v_mfma_f32_16x16x32_bf16 v[40:43], a[24:27], v[208:211], v[40:43]\n"
"v_mfma_f32_16x16x32_bf16 v[40:43], a[28:31], v[176:179], v[40:43]\n"
"buffer_load_dwordx4 v[124:127], v0, s[4:7], s98 offen offset:1024 sc1\n"
"v_mfma_f32_16x16x32_bf16 v[136:139], a[60:63], v[176:179], 0\n"
"v_mfma_f32_16x16x32_bf16 v[136:139], a[56:59], v[208:211], v[136:139]\n"
"v_mfma_f32_16x16x32_bf16 v[136:139], a[52:55], v[184:187], v[136:139]\n"
"v_mfma_f32_16x16x32_bf16 v[136:139], a[48:51], v[156:159], v[136:139]\n"
"v_mfma_f32_16x16x32_bf16 v[136:139], a[44:47], v[32:35], v[136:139]\n"
"s_add_u32 s97, s67, 0\n"
"s_nop 0\n"
"v_cmp_u_f32_e32 vcc, v40, v40\n"
"v_mfma_f32_16x16x32_bf16 v[136:139], a[40:43], v[144:147], v[136:139]\n"
"s_cbranch_vccnz .Lmk_p82s\n"
".Lmk_p82r:\n"
"v_mfma_f32_16x16x32_bf16 v[136:139], a[36:39], v[116:119], v[136:139]\n"
"buffer_load_dwordx4 v[140:143], v0, s[4:7], s98 offen offset:2048 sc1\n"
"s_add_u32 s95, s64, 0x2000\n"
"v_mfma_f32_16x16x32_bf16 v[136:139], a[32:35], v[200:203], v[136:139]\n"
"ds_write_b128 v3, v[40:43] offset:6144\n"
"v_mfma_f32_16x16x32_bf16 v[40:43], a[64:67], v[200:203], 0\n"
"buffer_load_dwordx2 v[246:247], v12, s[4:7], s97 offen sc1\n"
"v_mfma_f32_16x16x32_bf16 v[40:43], a[68:71], v[116:119], v[40:43]\n"
"buffer_load_dwordx4 v[148:151], v0, s[4:7], s98 offen offset:3072 sc1\n"
"v_mfma_f32_16x16x32_bf16 v[40:43], a[72:75], v[144:147], v[40:43]\n"
"buffer_store_dwordx4 v[216:219], v7, s[4:7], s95 offen sc1\n"
"v_mfma_f32_16x16x32_bf16 v[40:43], a[76:79], v[32:35], v[40:43]\n"
"v_mfma_f32_16x16x32_bf16 v[40:43], a[80:83], v[156:159], v[40:43]\n"
"buffer_load_dwordx4 a[140:143], v0, s[4:7], s96 offen offset:3072 sc1\n"
"v_mfma_f32_16x16x32_bf16 v[40:43], a[84:87], v[184:187], v[40:43]\n"
"ds_write_b128 v3, v[136:139] offset:7168\n"
"v_mfma_f32_16x16x32_bf16 v[40:43], a[88:91], v[208:211], v[40:43]\n"
"buffer_load_dwordx4 v[136:139], v1, s[4:7], s98 offen sc1\n"
"v_mfma_f32_16x16x32_bf16 v[40:43], a[92:95], v[176:179], v[40:43]\n"
"v_mfma_f32_16x16x32_bf16 v[152:155], a[124:127], v[176:179], 0\n"
"v_mfma_f32_16x16x32_bf16 v[152:155], a[120:123], v[208:211], v[152:155]\n"
"v_mfma_f32_16x16x32_bf16 v[152:155], a[116:119], v[184:187], v[152:155]\n"
"v_mfma_f32_16x16x32_bf16 v[152:155], a[112:115], v[156:159], v[152:155]\n"
"buffer_load_dwordx4 v[164:167], v1, s[4:7], s98 offen offset:1024 sc1\n"
"v_mfma_f32_16x16x32_bf16 v[152:155], a[108:111], v[32:35], v[152:155]\n"
"v_mfma_f32_16x16x32_bf16 v[152:155], a[104:107], v[144:147], v[152:155]\n"
"s_nop 0\n"
"ds_write_b128 v3, v[40:43] offset:8192\n"
"v_mfma_f32_16x16x32_bf16 v[152:155], a[100:103], v[116:119], v[152:155]\n"
"v_mfma_f32_16x16x32_bf16 v[152:155], a[96:99], v[200:203], v[152:155]\n"
"v_mfma_f32_16x16x32_bf16 v[40:43], v[24:27], v[200:203], 0\n"
"buffer_load_dwordx4 v[168:171], v1, s[4:7], s98 offen offset:2048 sc1\n"
"v_mfma_f32_16x16x32_bf16 v[40:43], v[56:59], v[116:119], v[40:43]\n"
"v_mfma_f32_16x16x32_bf16 v[40:43], v[28:31], v[144:147], v[40:43]\n"
"v_mfma_f32_16x16x32_bf16 v[40:43], v[64:67], v[32:35], v[40:43]\n"
"v_mfma_f32_16x16x32_bf16 v[40:43], v[52:55], v[156:159], v[40:43]\n"
"s_waitcnt vmcnt(12)\n"
"v_add_f32_e32 v255, v255, v250\n"
"v_add_f32_e32 v253, v253, v254\n"
"v_mfma_f32_16x16x32_bf16 v[40:43], v[36:39], v[184:187], v[40:43]\n"
"buffer_load_dwordx4 v[180:183], v1, s[4:7], s98 offen offset:3072 sc1\n"
"v_mfma_f32_16x16x32_bf16 v[40:43], v[108:111], v[208:211], v[40:43]\n"
"ds_write_b128 v3, v[152:155] offset:9216\n"
"v_mfma_f32_16x16x32_bf16 v[40:43], v[60:63], v[176:179], v[40:43]\n"
"v_add_f32_e32 v252, v252, v249\n"
"v_add_f32_e32 v251, v251, v248\n"
"v_mfma_f32_16x16x32_bf16 v[152:155], v[80:83], v[176:179], 0\n"
"buffer_load_dwordx4 a[144:147], v1, s[4:7], s96 offen sc1\n"
"s_add_u32 s98, s64, 0x6000\n"
"v_mfma_f32_16x16x32_bf16 v[152:155], v[88:91], v[208:211], v[152:155]\n"
"v_add_f32_e32 v255, v255, v252\n"
"v_add_f32_e32 v253, v253, v251\n"
"v_mfma_f32_16x16x32_bf16 v[152:155], v[100:103], v[184:187], v[152:155]\n"
"buffer_store_dwordx4 v[44:47], v7, s[4:7], s98 offen sc1\n"
"v_mfma_f32_16x16x32_bf16 v[152:155], v[76:79], v[156:159], v[152:155]\n"
"v_add_f32_e32 v255, v255, v253\n"
"v_cmp_u_f32_e32 vcc, v255, v255\n"
"v_mfma_f32_16x16x32_bf16 v[152:155], v[132:135], v[32:35], v[152:155]\n"
"s_cbranch_vccnz .Lmk_m85s\n"
".Lmk_m85r:\n"
"v_mov_b32_e32 v253, v255\n"
"v_mfma_f32_16x16x32_bf16 v[152:155], v[128:131], v[144:147], v[152:155]\n"
"ds_write_b128 v3, v[40:43] offset:10240\n"
"v_mfma_f32_16x16x32_bf16 v[152:155], v[48:51], v[116:119], v[152:155]\n"
"v_permlane32_swap_b32_e32 v255, v253\n"
"v_add_f32_e32 v255, v255, v253\n"
"v_mfma_f32_16x16x32_bf16 v[152:155], v[72:75], v[200:203], v[152:155]\n"
"v_mul_f32_e32 v255, 0x3a800000, v255\n"
"v_mov_b32_e32 v253, v255\n"
"; @A G1 1.2\n"
"v_mfma_f32_16x16x32_bf16 v[32:35], a[0:3], v[92:95], 0\n"
"s_nop 0\n"
"v_permlane16_swap_b32_e32 v255, v253\n"
"v_cvt_pk_bf16_f32 v254, -v255, 0\n"
"v_mfma_f32_16x16x32_bf16 v[32:35], a[4:7], v[96:99], v[32:35]\n"
"s_waitcnt vmcnt(9)\n"
"v_cmp_ne_u64_e32 vcc, v[246:247], v[18:19]\n"
"v_mfma_f32_16x16x32_bf16 v[32:35], a[8:11], v[112:115], v[32:35]\n"
"s_cbranch_vccnz .Lmk_f72s\n"
".Lmk_f72r:\n"
"v_fma_f32 v253, -v255, v255, v253\n"
"v_mfma_f32_16x16x32_bf16 v[32:35], a[12:15], v[120:123], v[32:35]\n"
"v_lshlrev_b32_e32 v254, 16, v254\n"
"v_max_f32_e32 v253, 0, v253\n"
"v_mfma_f32_16x16x32_bf16 v[32:35], a[16:19], v[104:107], v[32:35]\n"
"v_cvt_pk_bf16_f32 v253, v253, 0\n"
"v_lshlrev_b32_e32 v253, 16, v253\n"
"v_mfma_f32_16x16x32_bf16 v[32:35], a[20:23], v[172:175], v[32:35]\n"
"ds_write_b128 v3, v[152:155] offset:11264\n"
"v_mfma_f32_16x16x32_bf16 v[32:35], a[24:27], v[84:87], v[32:35]\n"
"v_add_f32_e32 v253, 0x3727c5ac, v253\n"
"v_cvt_pk_bf16_f32 v253, v253, 0\n"
"v_mfma_f32_16x16x32_bf16 v[32:35], a[28:31], v[160:163], v[32:35]\n"
"v_lshlrev_b32_e32 v253, 16, v253\n"
"v_mfma_f32_16x16x32_bf16 v[40:43], a[60:63], v[160:163], 0\n"
"v_sqrt_f32_e32 v253, v253\n"
"v_mfma_f32_16x16x32_bf16 v[40:43], a[56:59], v[84:87], v[40:43]\n"
"v_cvt_pk_bf16_f32 v253, v253, 0\n"
"v_lshlrev_b32_e32 v253, 16, v253\n"
"v_mfma_f32_16x16x32_bf16 v[40:43], a[52:55], v[172:175], v[40:43]\n"
"s_add_u32 s98, s52, 0x8000\n"
"buffer_load_dwordx4 v[44:47], v0, s[4:7], s98 offen sc1\n"
"v_mfma_f32_16x16x32_bf16 v[40:43], a[48:51], v[104:107], v[40:43]\n"
"s_waitcnt lgkmcnt(0)\n"
"s_barrier\n"
"v_mfma_f32_16x16x32_bf16 v[40:43], a[44:47], v[120:123], v[40:43]\n"
"ds_read_b128 v[116:119], v4\n"
"v_cmp_u_f32_e32 vcc, v32, v32\n"
"v_mfma_f32_16x16x32_bf16 v[40:43], a[40:43], v[112:115], v[40:43]\n"
"s_cbranch_vccnz .Lmk_p83s\n"
".Lmk_p83r:\n"
"ds_read_b128 v[144:147], v9 offset:128\n"
"v_mfma_f32_16x16x32_bf16 v[40:43], a[36:39], v[96:99], v[40:43]\n"
"ds_write_b128 v3, v[32:35] offset:12288\n"
"v_mfma_f32_16x16x32_bf16 v[40:43], a[32:35], v[92:95], v[40:43]\n"
"buffer_load_dwordx4 v[32:35], v0, s[4:7], s98 offen offset:1024 sc1\n"
"v_mfma_f32_16x16x32_bf16 v[152:155], a[64:67], v[92:95], 0\n"
"buffer_load_dwordx4 a[148:151], v1, s[4:7], s96 offen offset:1024 sc1\n"
"v_mfma_f32_16x16x32_bf16 v[152:155], a[68:71], v[96:99], v[152:155]\n"
"v_rcp_f32_e32 v255, v253\n"
"v_mfma_f32_16x16x32_bf16 v[152:155], a[72:75], v[112:115], v[152:155]\n"
"ds_read_b128 v[156:159], v5\n"
"v_mul_f32_e32 v253, 0x3e8293ee, v255\n"
"v_mfma_f32_16x16x32_bf16 v[152:155], a[76:79], v[120:123], v[152:155]\n"
"ds_read_b128 v[176:179], v4 offset:1024\n"
"ds_read_b128 v[184:187], v9 offset:192\n"
"v_mfma_f32_16x16x32_bf16 v[152:155], a[80:83], v[104:107], v[152:155]\n"
"buffer_load_dwordx4 v[188:191], v0, s[4:7], s98 offen offset:2048 sc1\n"
"v_mfma_f32_16x16x32_bf16 v[152:155], a[84:87], v[172:175], v[152:155]\n"
"ds_write_b128 v3, v[40:43] offset:13312\n"
"v_mfma_f32_16x16x32_bf16 v[152:155], a[88:91], v[84:87], v[152:155]\n"
"ds_read_b128 v[40:43], v5 offset:1024\n"
"v_mfma_f32_16x16x32_bf16 v[152:155], a[92:95], v[160:163], v[152:155]\n"
"s_waitcnt lgkmcnt(4)\n"
"v_fmac_f32_e32 v116, v254, v144\n"
"v_fmac_f32_e32 v117, v254, v145\n"
"v_mfma_f32_16x16x32_bf16 v[192:195], a[124:127], v[160:163], 0\n"
"v_fmac_f32_e32 v118, v254, v146\n"
"v_fmac_f32_e32 v119, v254, v147\n"
"v_mfma_f32_16x16x32_bf16 v[192:195], a[120:123], v[84:87], v[192:195]\n"
"buffer_load_dwordx4 v[144:147], v0, s[4:7], s98 offen offset:3072 sc1\n"
"v_mfma_f32_16x16x32_bf16 v[192:195], a[116:119], v[172:175], v[192:195]\n"
"ds_read_b128 v[196:199], v4 offset:2048\n"
"ds_read_b128 v[200:203], v9 offset:256\n"
"v_mfma_f32_16x16x32_bf16 v[192:195], a[112:115], v[104:107], v[192:195]\n"
"ds_read_b128 v[204:207], v5 offset:2048\n"
"v_add_f32_e32 v116, v116, v156\n"
"v_mfma_f32_16x16x32_bf16 v[192:195], a[108:111], v[120:123], v[192:195]\n"
"v_add_f32_e32 v117, v117, v157\n"
"v_add_f32_e32 v118, v118, v158\n"
"v_mfma_f32_16x16x32_bf16 v[192:195], a[104:107], v[112:115], v[192:195]\n"
"ds_write_b128 v3, v[152:155] offset:14336\n"
"v_mfma_f32_16x16x32_bf16 v[192:195], a[100:103], v[96:99], v[192:195]\n"
"buffer_load_dwordx4 v[152:155], v1, s[4:7], s98 offen sc1\n"
"v_mfma_f32_16x16x32_bf16 v[192:195], a[96:99], v[92:95], v[192:195]\n"
"s_waitcnt lgkmcnt(4)\n"
"v_add_f32_e32 v119, v119, v159\n"
"v_fmac_f32_e32 v176, v254, v184\n"
"v_mfma_f32_16x16x32_bf16 v[156:159], v[24:27], v[92:95], 0\n"
"v_fmac_f32_e32 v177, v254, v185\n"
"v_fmac_f32_e32 v178, v254, v186\n"
"v_mfma_f32_16x16x32_bf16 v[156:159], v[56:59], v[96:99], v[156:159]\n"
"ds_read_b128 v[208:211], v4 offset:3072\n"
"ds_read_b128 v[212:215], v9 offset:320\n"
"v_mfma_f32_16x16x32_bf16 v[156:159], v[28:31], v[112:115], v[156:159]\n"
"ds_read_b128 v[216:219], v5 offset:3072\n"
"v_fmac_f32_e32 v179, v254, v187\n"
"v_mfma_f32_16x16x32_bf16 v[156:159], v[64:67], v[120:123], v[156:159]\n"
"buffer_load_dwordx4 v[184:187], v1, s[4:7], s98 offen offset:1024 sc1\n"
"v_mfma_f32_16x16x32_bf16 v[156:159], v[52:55], v[104:107], v[156:159]\n"
"buffer_load_dwordx4 a[152:155], v1, s[4:7], s96 offen offset:2048 sc1\n"
"v_mfma_f32_16x16x32_bf16 v[156:159], v[36:39], v[172:175], v[156:159]\n"
"ds_write_b128 v3, v[192:195] offset:15360\n"
"v_mfma_f32_16x16x32_bf16 v[156:159], v[108:111], v[84:87], v[156:159]\n"
"v_add_f32_e32 v176, v176, v40\n"
"v_add_f32_e32 v177, v177, v41\n"
"v_mfma_f32_16x16x32_bf16 v[156:159], v[60:63], v[160:163], v[156:159]\n"
"s_waitcnt lgkmcnt(5)\n"
"v_add_f32_e32 v178, v178, v42\n"
"v_add_f32_e32 v179, v179, v43\n"
"v_mfma_f32_16x16x32_bf16 v[40:43], v[80:83], v[160:163], 0\n"
"buffer_load_dwordx4 v[160:163], v1, s[4:7], s98 offen offset:2048 sc1\n"
"v_mfma_f32_16x16x32_bf16 v[40:43], v[88:91], v[84:87], v[40:43]\n"
"ds_read_b128 v[84:87], v4 offset:4096\n"
"ds_read_b128 v[192:195], v9\n"
"v_mfma_f32_16x16x32_bf16 v[40:43], v[100:103], v[172:175], v[40:43]\n"
"ds_read_b128 v[172:175], v5 offset:4096\n"
"v_fmac_f32_e32 v196, v254, v200\n"
"v_mfma_f32_16x16x32_bf16 v[40:43], v[76:79], v[104:107], v[40:43]\n"
"v_fmac_f32_e32 v197, v254, v201\n"
"v_fmac_f32_e32 v198, v254, v202\n"
"v_mfma_f32_16x16x32_bf16 v[40:43], v[132:135], v[120:123], v[40:43]\n"
"v_fmac_f32_e32 v199, v254, v203\n"
"v_add_f32_e32 v196, v196, v204\n"
"v_mfma_f32_16x16x32_bf16 v[40:43], v[128:131], v[112:115], v[40:43]\n"
"buffer_load_dwordx4 v[104:107], v1, s[4:7], s98 offen offset:3072 sc1\n"
"v_mfma_f32_16x16x32_bf16 v[40:43], v[48:51], v[96:99], v[40:43]\n"
"ds_write_b128 v3, v[156:159] offset:16384\n"
"v_mfma_f32_16x16x32_bf16 v[40:43], v[72:75], v[92:95], v[40:43]\n"
"s_waitcnt vmcnt(18)\n"
"ds_read_b128 v[92:95], v4 offset:5120\n"
"ds_read_b128 v[96:99], v9 offset:64\n"
"; @A G1 1.3\n"
"v_mfma_f32_16x16x32_bf16 v[112:115], a[0:3], v[68:71], v[116:119]\n"
"ds_read_b128 v[120:123], v5 offset:5120\n"
"v_add_f32_e32 v197, v197, v205\n"
"v_mfma_f32_16x16x32_bf16 v[112:115], a[4:7], v[124:127], v[112:115]\n"
"s_waitcnt lgkmcnt(8)\n"
"v_add_f32_e32 v198, v198, v206\n"
"v_add_f32_e32 v199, v199, v207\n"
"v_mfma_f32_16x16x32_bf16 v[112:115], a[8:11], v[140:143], v[112:115]\n"
"s_add_u32 s98, s53, 0x8000\n"
"buffer_load_dwordx4 v[156:159], v0, s[4:7], s98 offen sc1\n"
"v_mfma_f32_16x16x32_bf16 v[112:115], a[12:15], v[148:151], v[112:115]\n"
"s_waitcnt vmcnt(13)\n"
"v_fmac_f32_e32 v208, v254, v212\n"
"v_fmac_f32_e32 v209, v254, v213\n"
"v_mfma_f32_16x16x32_bf16 v[112:115], a[16:19], v[136:139], v[112:115]\n"
"v_fmac_f32_e32 v210, v254, v214\n"
"v_fmac_f32_e32 v211, v254, v215\n"
"v_mfma_f32_16x16x32_bf16 v[112:115], a[20:23], v[164:167], v[112:115]\n"
"ds_write_b128 v3, v[40:43] offset:17408\n"
"v_mfma_f32_16x16x32_bf16 v[112:115], a[24:27], v[168:171], v[112:115]\n"
"v_add_f32_e32 v208, v208, v216\n"
"v_add_f32_e32 v209, v209, v217\n"
"v_mfma_f32_16x16x32_bf16 v[112:115], a[28:31], v[180:183], v[112:115]\n"
"buffer_load_dwordx4 v[40:43], v0, s[4:7], s98 offen offset:1024 sc1\n"
"v_mfma_f32_16x16x32_bf16 v[200:203], a[60:63], v[180:183], v[176:179]\n"
"buffer_load_dwordx4 a[156:159], v1, s[4:7], s96 offen offset:3072 sc1\n"
"v_mfma_f32_16x16x32_bf16 v[200:203], a[56:59], v[168:171], v[200:203]\n"
"s_waitcnt lgkmcnt(5)\n"
"v_add_f32_e32 v210, v210, v218\n"
"v_add_f32_e32 v211, v211, v219\n"
"v_mfma_f32_16x16x32_bf16 v[200:203], a[52:55], v[164:167], v[200:203]\n"
"s_waitcnt lgkmcnt(0)\n"
"s_barrier\n"
"v_mfma_f32_16x16x32_bf16 v[200:203], a[48:51], v[136:139], v[200:203]\n"
"ds_read_b128 v[204:207], v6\n"
"ds_read_b128 v[212:215], v6 offset:1024\n"
"v_mfma_f32_16x16x32_bf16 v[200:203], a[44:47], v[148:151], v[200:203]\n"
"v_cmp_u_f32_e32 vcc, v112, v112\n"
"v_fmac_f32_e32 v84, v254, v192\n"
"v_mfma_f32_16x16x32_bf16 v[200:203], a[40:43], v[140:143], v[200:203]\n"
"s_cbranch_vccnz .Lmk_p84s\n"
".Lmk_p84r:\n"
"v_fmac_f32_e32 v85, v254, v193\n"
"v_mfma_f32_16x16x32_bf16 v[200:203], a[36:39], v[124:127], v[200:203]\n"
"buffer_load_dwordx4 v[116:119], v0, s[4:7], s98 offen offset:2048 sc1\n"
"v_mfma_f32_16x16x32_bf16 v[200:203], a[32:35], v[68:71], v[200:203]\n"
"v_fmac_f32_e32 v86, v254, v194\n"
"v_fmac_f32_e32 v87, v254, v195\n"
"v_mfma_f32_16x16x32_bf16 v[196:199], a[64:67], v[68:71], v[196:199]\n"
"v_add_f32_e32 v84, v84, v172\n"
"v_add_f32_e32 v85, v85, v173\n"
"v_mfma_f32_16x16x32_bf16 v[196:199], a[68:71], v[124:127], v[196:199]\n"
"buffer_load_dwordx4 v[176:179], v0, s[4:7], s98 offen offset:3072 sc1\n"
"v_mfma_f32_16x16x32_bf16 v[196:199], a[72:75], v[140:143], v[196:199]\n"
"v_add_f32_e32 v86, v86, v174\n"
"v_add_f32_e32 v87, v87, v175\n"
"v_mfma_f32_16x16x32_bf16 v[196:199], a[76:79], v[148:151], v[196:199]\n"
"v_fmac_f32_e32 v92, v254, v96\n"
"v_fmac_f32_e32 v93, v254, v97\n"
"v_mfma_f32_16x16x32_bf16 v[196:199], a[80:83], v[136:139], v[196:199]\n"
"s_waitcnt lgkmcnt(0)\n"
"v_fmac_f32_e32 v94, v254, v98\n"
"v_fmac_f32_e32 v95, v254, v99\n"
"v_mfma_f32_16x16x32_bf16 v[196:199], a[84:87], v[164:167], v[196:199]\n"
"v_add_f32_e32 v92, v92, v120\n"
"v_add_f32_e32 v93, v93, v121\n"
"v_mfma_f32_16x16x32_bf16 v[196:199], a[88:91], v[168:171], v[196:199]\n"
"buffer_load_dwordx4 v[96:99], v1, s[4:7], s98 offen sc1\n"
"v_mfma_f32_16x16x32_bf16 v[196:199], a[92:95], v[180:183], v[196:199]\n"
"ds_read_b128 v[172:175], v6 offset:2048\n"
"ds_read_b128 v[192:195], v6 offset:3072\n"
"v_mfma_f32_16x16x32_bf16 v[208:211], a[124:127], v[180:183], v[208:211]\n"
"v_add_f32_e32 v94, v94, v122\n"
"v_add_f32_e32 v95, v95, v123\n"
"v_mfma_f32_16x16x32_bf16 v[208:211], a[120:123], v[168:171], v[208:211]\n"
"v_add_f32_e32 v112, v112, v204\n"
"v_add_f32_e32 v113, v113, v205\n"
"v_mfma_f32_16x16x32_bf16 v[208:211], a[116:119], v[164:167], v[208:211]\n"
"s_add_u32 s97, s70, 0x2000\n"
"v_add_f32_e32 v114, v114, v206\n"
"v_add_f32_e32 v115, v115, v207\n"
"v_mfma_f32_16x16x32_bf16 v[208:211], a[112:115], v[136:139], v[208:211]\n"
"buffer_load_dwordx4 v[120:123], v1, s[4:7], s98 offen offset:1024 sc1\n"
"v_mfma_f32_16x16x32_bf16 v[208:211], a[108:111], v[148:151], v[208:211]\n"
"buffer_load_dwordx4 a[160:163], v0, s[4:7], s97 offen sc1\n"
"v_mfma_f32_16x16x32_bf16 v[208:211], a[104:107], v[140:143], v[208:211]\n"
"v_add_f32_e32 v200, v200, v212\n"
"v_add_f32_e32 v201, v201, v213\n"
"v_mfma_f32_16x16x32_bf16 v[208:211], a[100:103], v[124:127], v[208:211]\n"
"v_add_f32_e32 v202, v202, v214\n"
"v_add_f32_e32 v203, v203, v215\n"
"v_mfma_f32_16x16x32_bf16 v[208:211], a[96:99], v[68:71], v[208:211]\n"
"v_mul_f32_e32 v112, v112, v255\n"
"v_mul_f32_e32 v113, v113, v255\n"
"v_mfma_f32_16x16x32_bf16 v[84:87], v[24:27], v[68:71], v[84:87]\n"
"buffer_load_dwordx4 v[204:207], v1, s[4:7], s98 offen offset:2048 sc1\n"
"v_mfma_f32_16x16x32_bf16 v[84:87], v[56:59], v[124:127], v[84:87]\n"
"v_mul_f32_e32 v114, v114, v255\n"
"v_mul_f32_e32 v115, v115, v255\n"
"v_mfma_f32_16x16x32_bf16 v[84:87], v[28:31], v[140:143], v[84:87]\n"
"v_mul_f32_e32 v200, v200, v255\n"
"v_mul_f32_e32 v201, v201, v255\n"
"v_mfma_f32_16x16x32_bf16 v[84:87], v[64:67], v[148:151], v[84:87]\n"
"v_mul_f32_e32 v202, v202, v255\n"
"v_mul_f32_e32 v203, v203, v255\n"
"v_mfma_f32_16x16x32_bf16 v[84:87], v[52:55], v[136:139], v[84:87]\n"
"s_waitcnt lgkmcnt(0)\n"
"v_cvt_pk_bf16_f32 v212, v112, v113\n"
"v_cvt_pk_bf16_f32 v213, v114, v115\n"
"v_mfma_f32_16x16x32_bf16 v[84:87], v[36:39], v[164:167], v[84:87]\n"
"buffer_load_dwordx4 v[112:115], v1, s[4:7], s98 offen offset:3072 sc1\n"
"v_mfma_f32_16x16x32_bf16 v[84:87], v[108:111], v[168:171], v[84:87]\n"
"ds_read_b128 v[216:219], v6 offset:4096\n"
"ds_read_b128 v[220:223], v6 offset:5120\n"
"v_mfma_f32_16x16x32_bf16 v[84:87], v[60:63], v[180:183], v[84:87]\n"
"v_cvt_pk_bf16_f32 v214, v200, v201\n"
"v_cvt_pk_bf16_f32 v215, v202, v203\n"
"v_mfma_f32_16x16x32_bf16 v[92:95], v[80:83], v[180:183], v[92:95]\n"
"ds_write_b128 v8, v[212:215] offset:25600\n"
"v_mfma_f32_16x16x32_bf16 v[92:95], v[88:91], v[168:171], v[92:95]\n"
"v_add_f32_e32 v196, v196, v172\n"
"v_add_f32_e32 v197, v197, v173\n"
"v_mfma_f32_16x16x32_bf16 v[92:95], v[100:103], v[164:167], v[92:95]\n"
"v_add_f32_e32 v198, v198, v174\n"
"v_add_f32_e32 v199, v199, v175\n"
"v_mfma_f32_16x16x32_bf16 v[92:95], v[76:79], v[136:139], v[92:95]\n"
"v_add_f32_e32 v208, v208, v192\n"
"v_add_f32_e32 v209, v209, v193\n"
"v_mfma_f32_16x16x32_bf16 v[92:95], v[132:135], v[148:151], v[92:95]\n"
"v_add_f32_e32 v210, v210, v194\n"
"v_add_f32_e32 v211, v211, v195\n"
"v_mfma_f32_16x16x32_bf16 v[92:95], v[128:131], v[140:143], v[92:95]\n"
"v_mul_f32_e32 v196, v196, v255\n"
"v_mul_f32_e32 v197, v197, v255\n"
"v_mfma_f32_16x16x32_bf16 v[92:95], v[48:51], v[124:127], v[92:95]\n"
"v_mul_f32_e32 v198, v198, v255\n"
"v_mul_f32_e32 v199, v199, v255\n"
"v_mfma_f32_16x16x32_bf16 v[92:95], v[72:75], v[68:71], v[92:95]\n"
"v_mul_f32_e32 v208, v208, v255\n"
"v_mul_f32_e32 v209, v209, v255\n"
"v_mul_f32_e32 v210, v210, v255\n"
"v_mul_f32_e32 v211, v211, v255\n"
"v_cvt_pk_bf16_f32 v68, v196, v197\n"
"v_cvt_pk_bf16_f32 v69, v198, v199\n"
"v_cvt_pk_bf16_f32 v70, v208, v209\n"
"v_cvt_pk_bf16_f32 v71, v210, v211\n"
"ds_write_b128 v7, v[68:71] offset:58368\n"
".Lmk_a2:\n"
"s_waitcnt vmcnt(15)\n"
"; @A G1 2.0\n"
"v_mfma_f32_16x16x32_bf16 v[124:127], a[0:3], v[44:47], 0\n"
"v_mfma_f32_16x16x32_bf16 v[124:127], a[4:7], v[32:35], v[124:127]\n"
"s_add_u32 s98, s62, 0x800\n"
"v_mfma_f32_16x16x32_bf16 v[124:127], a[8:11], v[188:191], v[124:127]\n"
"s_add_u32 s96, s54, 0x8000\n"
"buffer_load_dwordx4 v[136:139], v0, s[4:7], s96 offen sc1\n"
"v_mfma_f32_16x16x32_bf16 v[124:127], a[12:15], v[144:147], v[124:127]\n"
"s_waitcnt vmcnt(11)\n"
"v_mfma_f32_16x16x32_bf16 v[124:127], a[16:19], v[152:155], v[124:127]\n"
"buffer_load_dword v255, v10, s[4:7], s98 offen sc1\n"
"v_mfma_f32_16x16x32_bf16 v[124:127], a[20:23], v[184:187], v[124:127]\n"
"s_waitcnt lgkmcnt(2)\n"
"v_add_f32_e32 v84, v84, v216\n"
"v_add_f32_e32 v85, v85, v217\n"
"v_mfma_f32_16x16x32_bf16 v[124:127], a[24:27], v[160:163], v[124:127]\n"
"v_add_f32_e32 v86, v86, v218\n"
"v_add_f32_e32 v87, v87, v219\n"
"v_mfma_f32_16x16x32_bf16 v[124:127], a[28:31], v[104:107], v[124:127]\n"
"buffer_load_dwordx4 v[140:143], v0, s[4:7], s96 offen offset:1024 sc1\n"
"v_mfma_f32_16x16x32_bf16 v[148:151], a[60:63], v[104:107], 0\n"
"buffer_load_dwordx4 a[164:167], v0, s[4:7], s97 offen offset:1024 sc1\n"
"v_mfma_f32_16x16x32_bf16 v[148:151], a[56:59], v[160:163], v[148:151]\n"
"buffer_load_dword v254, v10, s[4:7], s98 offen offset:256 sc1\n"
"v_mfma_f32_16x16x32_bf16 v[148:151], a[52:55], v[184:187], v[148:151]\n"
"v_add_f32_e32 v92, v92, v220\n"
"v_add_f32_e32 v93, v93, v221\n"
"v_mfma_f32_16x16x32_bf16 v[148:151], a[48:51], v[152:155], v[148:151]\n"
"v_add_f32_e32 v94, v94, v222\n"
"v_add_f32_e32 v95, v95, v223\n"
"v_mfma_f32_16x16x32_bf16 v[148:151], a[44:47], v[144:147], v[148:151]\n"
"buffer_load_dwordx4 v[164:167], v0, s[4:7], s96 offen offset:2048 sc1\n"
"v_mfma_f32_16x16x32_bf16 v[148:151], a[40:43], v[188:191], v[148:151]\n"
"ds_write_b128 v3, v[124:127]\n"
"v_mfma_f32_16x16x32_bf16 v[148:151], a[36:39], v[32:35], v[148:151]\n"
"buffer_load_dword v252, v10, s[4:7], s98 offen offset:512 sc1\n"
"v_mfma_f32_16x16x32_bf16 v[148:151], a[32:35], v[44:47], v[148:151]\n"
"v_mul_f32_e32 v84, v84, v253\n"
"v_mul_f32_e32 v85, v85, v253\n"
"v_mfma_f32_16x16x32_bf16 v[124:127], a[64:67], v[44:47], 0\n"
"v_mul_f32_e32 v86, v86, v253\n"
"v_mul_f32_e32 v87, v87, v253\n"
"v_mfma_f32_16x16x32_bf16 v[124:127], a[68:71], v[32:35], v[124:127]\n"
"buffer_load_dwordx4 v[168:171], v0, s[4:7], s96 offen offset:3072 sc1\n"
"v_mfma_f32_16x16x32_bf16 v[124:127], a[72:75], v[188:191], v[124:127]\n"
"buffer_load_dword v251, v10, s[4:7], s98 offen offset:768 sc1\n"
"v_mfma_f32_16x16x32_bf16 v[124:127], a[76:79], v[144:147], v[124:127]\n"
"v_mul_f32_e32 v92, v92, v253\n"
"v_mul_f32_e32 v93, v93, v253\n"
"v_mfma_f32_16x16x32_bf16 v[124:127], a[80:83], v[152:155], v[124:127]\n"
"v_mul_f32_e32 v94, v94, v253\n"
"v_mul_f32_e32 v95, v95, v253\n"
"v_mfma_f32_16x16x32_bf16 v[124:127], a[84:87], v[184:187], v[124:127]\n"
"ds_write_b128 v3, v[148:151] offset:1024\n"
"v_mfma_f32_16x16x32_bf16 v[124:127], a[88:91], v[160:163], v[124:127]\n"
"buffer_load_dwordx4 v[148:151], v1, s[4:7], s96 offen sc1\n"
"v_mfma_f32_16x16x32_bf16 v[124:127], a[92:95], v[104:107], v[124:127]\n"
"buffer_load_dword v253, v10, s[4:7], s98 offen offset:1024 sc1\n"
"v_mfma_f32_16x16x32_bf16 v[172:175], a[124:127], v[104:107], 0\n"
"v_cvt_pk_bf16_f32 v180, v84, v85\n"
"v_cvt_pk_bf16_f32 v181, v86, v87\n"
"v_mfma_f32_16x16x32_bf16 v[172:175], a[120:123], v[160:163], v[172:175]\n"
"v_cvt_pk_bf16_f32 v182, v92, v93\n"
"v_cvt_pk_bf16_f32 v183, v94, v95\n"
"v_mfma_f32_16x16x32_bf16 v[172:175], a[116:119], v[184:187], v[172:175]\n"
"buffer_load_dword v250, v10, s[4:7], s98 offen offset:1280 sc1\n"
"v_mfma_f32_16x16x32_bf16 v[172:175], a[112:115], v[152:155], v[172:175]\n"
"buffer_load_dwordx4 v[84:87], v1, s[4:7], s96 offen offset:1024 sc1\n"
"v_mfma_f32_16x16x32_bf16 v[172:175], a[108:111], v[144:147], v[172:175]\n"
"buffer_load_dwordx4 a[168:171], v0, s[4:7], s97 offen offset:2048 sc1\n"
"v_mfma_f32_16x16x32_bf16 v[172:175], a[104:107], v[188:191], v[172:175]\n"
"ds_write_b128 v3, v[124:127] offset:2048\n"
"v_mfma_f32_16x16x32_bf16 v[172:175], a[100:103], v[32:35], v[172:175]\n"
"buffer_load_dword v249, v10, s[4:7], s98 offen offset:1536 sc1\n"
"v_mfma_f32_16x16x32_bf16 v[172:175], a[96:99], v[44:47], v[172:175]\n"
"ds_write_b128 v2, v[180:183] offset:50176\n"
"v_mfma_f32_16x16x32_bf16 v[92:95], v[24:27], v[44:47], 0\n"
"buffer_load_dwordx4 v[124:127], v1, s[4:7], s96 offen offset:2048 sc1\n"
"s_add_u32 s95, s64, 0x2000\n"
"v_mfma_f32_16x16x32_bf16 v[92:95], v[56:59], v[32:35], v[92:95]\n"
"buffer_load_dword v248, v10, s[4:7], s98 offen offset:1792 sc1\n"
"v_mfma_f32_16x16x32_bf16 v[92:95], v[28:31], v[188:191], v[92:95]\n"
"buffer_store_dwordx4 v[212:215], v8, s[4:7], s95 offen offset:1024 sc1\n"
"v_mfma_f32_16x16x32_bf16 v[92:95], v[64:67], v[144:147], v[92:95]\n"
"v_mfma_f32_16x16x32_bf16 v[92:95], v[52:55], v[152:155], v[92:95]\n"
"v_mfma_f32_16x16x32_bf16 v[92:95], v[36:39], v[184:187], v[92:95]\n"
"buffer_load_dwordx4 v[180:183], v1, s[4:7], s96 offen offset:3072 sc1\n"
"v_mfma_f32_16x16x32_bf16 v[92:95], v[108:111], v[160:163], v[92:95]\n"
"ds_write_b128 v3, v[172:175] offset:3072\n"
"v_mfma_f32_16x16x32_bf16 v[92:95], v[60:63], v[104:107], v[92:95]\n"
"v_mfma_f32_16x16x32_bf16 v[172:175], v[80:83], v[104:107], 0\n"
"v_mfma_f32_16x16x32_bf16 v[172:175], v[88:91], v[160:163], v[172:175]\n"
"v_mfma_f32_16x16x32_bf16 v[172:175], v[100:103], v[184:187], v[172:175]\n"
"v_mfma_f32_16x16x32_bf16 v[172:175], v[76:79], v[152:155], v[172:175]\n"
"v_mfma_f32_16x16x32_bf16 v[172:175], v[132:135], v[144:147], v[172:175]\n"
"v_mfma_f32_16x16x32_bf16 v[172:175], v[128:131], v[188:191], v[172:175]\n"
"s_nop 1\n"
"ds_write_b128 v3, v[92:95] offset:4096\n"
"v_mfma_f32_16x16x32_bf16 v[172:175], v[48:51], v[32:35], v[172:175]\n"
"v_mfma_f32_16x16x32_bf16 v[172:175], v[72:75], v[44:47], v[172:175]\n"
"s_waitcnt vmcnt(24)\n"
"; @A G1 2.1\n"
"v_mfma_f32_16x16x32_bf16 v[32:35], a[0:3], v[156:159], 0\n"
"buffer_load_dwordx4 a[172:175], v0, s[4:7], s97 offen offset:3072 sc1\n"
"v_mfma_f32_16x16x32_bf16 v[32:35], a[4:7], v[40:43], v[32:35]\n"
"v_mfma_f32_16x16x32_bf16 v[32:35], a[8:11], v[116:119], v[32:35]\n"
"s_add_u32 s98, s55, 0x8000\n"
"buffer_load_dwordx4 v[44:47], v0, s[4:7], s98 offen sc1\n"
"v_mfma_f32_16x16x32_bf16 v[32:35], a[12:15], v[176:179], v[32:35]\n"
"s_waitcnt vmcnt(21)\n"
"v_mfma_f32_16x16x32_bf16 v[32:35], a[16:19], v[96:99], v[32:35]\n"
"v_mfma_f32_16x16x32_bf16 v[32:35], a[20:23], v[120:123], v[32:35]\n"
"ds_write_b128 v3, v[172:175] offset:5120\n"
"v_mfma_f32_16x16x32_bf16 v[32:35], a[24:27], v[204:207], v[32:35]\n"
"v_mfma_f32_16x16x32_bf16 v[32:35], a[28:31], v[112:115], v[32:35]\n"
"buffer_load_dwordx4 v[92:95], v0, s[4:7], s98 offen offset:1024 sc1\n"
"v_mfma_f32_16x16x32_bf16 v[104:107], a[60:63], v[112:115], 0\n"
"s_add_u32 s96, s64, 0x6000\n"
"v_mfma_f32_16x16x32_bf16 v[104:107], a[56:59], v[204:207], v[104:107]\n"
"v_mfma_f32_16x16x32_bf16 v[104:107], a[52:55], v[120:123], v[104:107]\n"
"buffer_store_dwordx4 v[68:71], v7, s[4:7], s96 offen offset:1024 sc1\n"
"v_mfma_f32_16x16x32_bf16 v[104:107], a[48:51], v[96:99], v[104:107]\n"
"v_mfma_f32_16x16x32_bf16 v[104:107], a[44:47], v[176:179], v[104:107]\n"
"buffer_load_dwordx4 v[68:71], v0, s[4:7], s98 offen offset:2048 sc1\n"
"v_mfma_f32_16x16x32_bf16 v[104:107], a[40:43], v[116:119], v[104:107]\n"
"ds_write_b128 v3, v[32:35] offset:6144\n"
"v_mfma_f32_16x16x32_bf16 v[104:107], a[36:39], v[40:43], v[104:107]\n"
"v_mfma_f32_16x16x32_bf16 v[104:107], a[32:35], v[156:159], v[104:107]\n"
"v_mfma_f32_16x16x32_bf16 v[32:35], a[64:67], v[156:159], 0\n"
"v_mfma_f32_16x16x32_bf16 v[32:35], a[68:71], v[40:43], v[32:35]\n"
"buffer_load_dwordx4 v[144:147], v0, s[4:7], s98 offen offset:3072 sc1\n"
"v_mfma_f32_16x16x32_bf16 v[32:35], a[72:75], v[116:119], v[32:35]\n"
"v_mfma_f32_16x16x32_bf16 v[32:35], a[76:79], v[176:179], v[32:35]\n"
"v_mfma_f32_16x16x32_bf16 v[32:35], a[80:83], v[96:99], v[32:35]\n"
"buffer_load_dwordx4 a[176:179], v1, s[4:7], s97 offen sc1\n"
"v_mfma_f32_16x16x32_bf16 v[32:35], a[84:87], v[120:123], v[32:35]\n"
"ds_write_b128 v3, v[104:107] offset:7168\n"
"v_mfma_f32_16x16x32_bf16 v[32:35], a[88:91], v[204:207], v[32:35]\n"
"buffer_load_dwordx4 v[104:107], v1, s[4:7], s98 offen sc1\n"
"v_mfma_f32_16x16x32_bf16 v[32:35], a[92:95], v[112:115], v[32:35]\n"
"v_mfma_f32_16x16x32_bf16 v[152:155], a[124:127], v[112:115], 0\n"
"v_mfma_f32_16x16x32_bf16 v[152:155], a[120:123], v[204:207], v[152:155]\n"
"v_mfma_f32_16x16x32_bf16 v[152:155], a[116:119], v[120:123], v[152:155]\n"
"v_mfma_f32_16x16x32_bf16 v[152:155], a[112:115], v[96:99], v[152:155]\n"
"buffer_load_dwordx4 v[160:163], v1, s[4:7], s98 offen offset:1024 sc1\n"
"v_mfma_f32_16x16x32_bf16 v[152:155], a[108:111], v[176:179], v[152:155]\n"
"v_mfma_f32_16x16x32_bf16 v[152:155], a[104:107], v[116:119], v[152:155]\n"
"s_nop 0\n"
"ds_write_b128 v3, v[32:35] offset:8192\n"
"v_mfma_f32_16x16x32_bf16 v[152:155], a[100:103], v[40:43], v[152:155]\n"
"v_mfma_f32_16x16x32_bf16 v[152:155], a[96:99], v[156:159], v[152:155]\n"
"v_mfma_f32_16x16x32_bf16 v[32:35], v[24:27], v[156:159], 0\n"
"buffer_load_dwordx4 v[172:175], v1, s[4:7], s98 offen offset:2048 sc1\n"
"v_mfma_f32_16x16x32_bf16 v[32:35], v[56:59], v[40:43], v[32:35]\n"
"v_mfma_f32_16x16x32_bf16 v[32:35], v[28:31], v[116:119], v[32:35]\n"
"v_mfma_f32_16x16x32_bf16 v[32:35], v[64:67], v[176:179], v[32:35]\n"
"v_mfma_f32_16x16x32_bf16 v[32:35], v[52:55], v[96:99], v[32:35]\n"
"s_waitcnt vmcnt(12)\n"
"v_add_f32_e32 v255, v255, v253\n"
"v_add_f32_e32 v254, v254, v250\n"
"v_mfma_f32_16x16x32_bf16 v[32:35], v[36:39], v[120:123], v[32:35]\n"
"buffer_load_dwordx4 v[184:187], v1, s[4:7], s98 offen offset:3072 sc1\n"
"v_mfma_f32_16x16x32_bf16 v[32:35], v[108:111], v[204:207], v[32:35]\n"
"ds_write_b128 v3, v[152:155] offset:9216\n"
"v_mfma_f32_16x16x32_bf16 v[32:35], v[60:63], v[112:115], v[32:35]\n"
"v_add_f32_e32 v252, v252, v249\n"
"v_add_f32_e32 v251, v251, v248\n"
"v_mfma_f32_16x16x32_bf16 v[152:155], v[80:83], v[112:115], 0\n"
"buffer_load_dwordx4 a[180:183], v1, s[4:7], s97 offen offset:1024 sc1\n"
"v_mfma_f32_16x16x32_bf16 v[152:155], v[88:91], v[204:207], v[152:155]\n"
"v_add_f32_e32 v255, v255, v252\n"
"v_add_f32_e32 v254, v254, v251\n"
"v_mfma_f32_16x16x32_bf16 v[152:155], v[100:103], v[120:123], v[152:155]\n"
"v_add_f32_e32 v255, v255, v254\n"
"v_mov_b32_e32 v254, v255\n"
"v_mfma_f32_16x16x32_bf16 v[152:155], v[76:79], v[96:99], v[152:155]\n"
"s_nop 0\n"
"v_permlane32_swap_b32_e32 v255, v254\n"
"v_add_f32_e32 v255, v255, v254\n"
"v_mfma_f32_16x16x32_bf16 v[152:155], v[132:135], v[176:179], v[152:155]\n"
"v_mul_f32_e32 v255, 0x3a800000, v255\n"
"v_mov_b32_e32 v254, v255\n"
"v_mfma_f32_16x16x32_bf16 v[152:155], v[128:131], v[116:119], v[152:155]\n"
"ds_write_b128 v3, v[32:35] offset:10240\n"
"v_mfma_f32_16x16x32_bf16 v[152:155], v[48:51], v[40:43], v[152:155]\n"
"v_permlane16_swap_b32_e32 v255, v254\n"
"v_cvt_pk_bf16_f32 v253, -v255, 0\n"
"v_mfma_f32_16x16x32_bf16 v[152:155], v[72:75], v[156:159], v[152:155]\n"
"v_fma_f32 v254, -v255, v255, v254\n"
"v_lshlrev_b32_e32 v253, 16, v253\n"
"; @A G1 2.2\n"
"v_mfma_f32_16x16x32_bf16 v[32:35], a[0:3], v[136:139], 0\n"
"v_max_f32_e32 v254, 0, v254\n"
"v_cvt_pk_bf16_f32 v254, v254, 0\n"
"v_mfma_f32_16x16x32_bf16 v[32:35], a[4:7], v[140:143], v[32:35]\n"
"v_lshlrev_b32_e32 v254, 16, v254\n"
"v_add_f32_e32 v254, 0x3727c5ac, v254\n"
"v_mfma_f32_16x16x32_bf16 v[32:35], a[8:11], v[164:167], v[32:35]\n"
"v_cvt_pk_bf16_f32 v254, v254, 0\n"
"v_lshlrev_b32_e32 v254, 16, v254\n"
"v_mfma_f32_16x16x32_bf16 v[32:35], a[12:15], v[168:171], v[32:35]\n"
"s_waitcnt vmcnt(12)\n"
"v_sqrt_f32_e32 v254, v254\n"
"v_mfma_f32_16x16x32_bf16 v[32:35], a[16:19], v[148:151], v[32:35]\n"
"s_mov_b32 s98, .Lmk_a4-.Lmk_g86\n"
"s_getpc_b64 s[84:85]\n"
".Lmk_g86:\n"
"s_add_u32 s84, s84, s98\n"
"s_addc_u32 s85, s85, 0\n"
"s_and_b32 s84, s84, -16\n"
"v_mfma_f32_16x16x32_bf16 v[32:35], a[20:23], v[84:87], v[32:35]\n"
"ds_write_b128 v3, v[152:155] offset:11264\n"
"v_mfma_f32_16x16x32_bf16 v[32:35], a[24:27], v[124:127], v[32:35]\n"
"s_and_b32 s85, s85, 0xffff\n"
"s_sub_u32 s98, .Lmk_end-.Lmk_g86, s98\n"
"s_min_u32 s86, s98, 0x4000\n"
"s_mov_b32 s87, 0x20000\n"
"v_mfma_f32_16x16x32_bf16 v[32:35], a[28:31], v[180:183], v[32:35]\n"
"s_mov_b32 m0, 0x27800\n"
"s_nop 0\n"
"buffer_load_dwordx4 v0, s[84:87], s99 offen lds\n"
"v_mfma_f32_16x16x32_bf16 v[40:43], a[60:63], v[180:183], 0\n"
"v_cvt_pk_bf16_f32 v254, v254, 0\n"
"v_lshlrev_b32_e32 v254, 16, v254\n"
"v_mfma_f32_16x16x32_bf16 v[40:43], a[56:59], v[124:127], v[40:43]\n"
"v_rcp_f32_e32 v255, v254\n"
"v_mfma_f32_16x16x32_bf16 v[40:43], a[52:55], v[84:87], v[40:43]\n"
"s_add_u32 s98, s52, 0\n"
"buffer_load_dwordx4 v[96:99], v0, s[4:7], s98 offen sc1\n"
"v_mfma_f32_16x16x32_bf16 v[40:43], a[48:51], v[148:151], v[40:43]\n"
"s_waitcnt lgkmcnt(0)\n"
"s_barrier\n"
"v_mfma_f32_16x16x32_bf16 v[40:43], a[44:47], v[168:171], v[40:43]\n"
"buffer_load_dwordx4 a[184:187], v1, s[4:7], s97 offen offset:2048 sc1\n"
"v_mfma_f32_16x16x32_bf16 v[40:43], a[40:43], v[164:167], v[40:43]\n"
"ds_write_b128 v3, v[32:35] offset:12288\n"
"v_mfma_f32_16x16x32_bf16 v[40:43], a[36:39], v[140:143], v[40:43]\n"
"ds_read_b128 v[32:35], v4\n"
"v_mul_f32_e32 v254, 0x3e8293ee, v255\n"
"v_mfma_f32_16x16x32_bf16 v[40:43], a[32:35], v[136:139], v[40:43]\n"
"buffer_load_dwordx4 v[112:115], v0, s[4:7], s98 offen offset:1024 sc1\n"
"v_mfma_f32_16x16x32_bf16 v[116:119], a[64:67], v[136:139], 0\n"
"ds_read_b128 v[120:123], v9 offset:128\n"
"ds_read_b128 v[152:155], v5\n"
"v_mfma_f32_16x16x32_bf16 v[116:119], a[68:71], v[140:143], v[116:119]\n"
"v_mfma_f32_16x16x32_bf16 v[116:119], a[72:75], v[164:167], v[116:119]\n"
"v_mfma_f32_16x16x32_bf16 v[116:119], a[76:79], v[168:171], v[116:119]\n"
"ds_read_b128 v[156:159], v4 offset:1024\n"
"ds_read_b128 v[176:179], v9 offset:192\n"
"v_mfma_f32_16x16x32_bf16 v[116:119], a[80:83], v[148:151], v[116:119]\n"
"buffer_load_dwordx4 v[188:191], v0, s[4:7], s98 offen offset:2048 sc1\n"
"v_mfma_f32_16x16x32_bf16 v[116:119], a[84:87], v[84:87], v[116:119]\n"
"ds_write_b128 v3, v[40:43] offset:13312\n"
"v_mfma_f32_16x16x32_bf16 v[116:119], a[88:91], v[124:127], v[116:119]\n"
"ds_read_b128 v[40:43], v5 offset:1024\n"
"s_waitcnt lgkmcnt(4)\n"
"v_mfma_f32_16x16x32_bf16 v[116:119], a[92:95], v[180:183], v[116:119]\n"
"v_fmac_f32_e32 v32, v253, v120\n"
"v_fmac_f32_e32 v33, v253, v121\n"
"v_mfma_f32_16x16x32_bf16 v[192:195], a[124:127], v[180:183], 0\n"
"v_fmac_f32_e32 v34, v253, v122\n"
"v_fmac_f32_e32 v35, v253, v123\n"
"v_mfma_f32_16x16x32_bf16 v[192:195], a[120:123], v[124:127], v[192:195]\n"
"buffer_load_dwordx4 v[120:123], v0, s[4:7], s98 offen offset:3072 sc1\n"
"v_mfma_f32_16x16x32_bf16 v[192:195], a[116:119], v[84:87], v[192:195]\n"
"ds_read_b128 v[196:199], v4 offset:2048\n"
"ds_read_b128 v[200:203], v9 offset:256\n"
"v_mfma_f32_16x16x32_bf16 v[192:195], a[112:115], v[148:151], v[192:195]\n"
"ds_read_b128 v[204:207], v5 offset:2048\n"
"v_add_f32_e32 v32, v32, v152\n"
"v_mfma_f32_16x16x32_bf16 v[192:195], a[108:111], v[168:171], v[192:195]\n"
"v_add_f32_e32 v33, v33, v153\n"
"v_add_f32_e32 v34, v34, v154\n"
"v_mfma_f32_16x16x32_bf16 v[192:195], a[104:107], v[164:167], v[192:195]\n"
"ds_write_b128 v3, v[116:119] offset:14336\n"
"v_mfma_f32_16x16x32_bf16 v[192:195], a[100:103], v[140:143], v[192:195]\n"
"buffer_load_dwordx4 v[116:119], v1, s[4:7], s98 offen sc1\n"
"v_mfma_f32_16x16x32_bf16 v[192:195], a[96:99], v[136:139], v[192:195]\n"
"s_waitcnt lgkmcnt(4)\n"
"v_add_f32_e32 v35, v35, v155\n"
"v_fmac_f32_e32 v156, v253, v176\n"
"v_mfma_f32_16x16x32_bf16 v[152:155], v[24:27], v[136:139], 0\n"
"buffer_load_dwordx4 a[188:191], v1, s[4:7], s97 offen offset:3072 sc1\n"
"v_mfma_f32_16x16x32_bf16 v[152:155], v[56:59], v[140:143], v[152:155]\n"
"ds_read_b128 v[208:211], v4 offset:3072\n"
"ds_read_b128 v[212:215], v9 offset:320\n"
"v_mfma_f32_16x16x32_bf16 v[152:155], v[28:31], v[164:167], v[152:155]\n"
"s_waitcnt vmcnt(16)\n"
"s_add_u32 s97, s68, 0x1000\n"
"ds_read_b128 v[216:219], v5 offset:3072\n"
"v_mfma_f32_16x16x32_bf16 v[152:155], v[64:67], v[168:171], v[152:155]\n"
"buffer_load_dwordx4 v[220:223], v1, s[4:7], s98 offen offset:1024 sc1\n"
"v_mfma_f32_16x16x32_bf16 v[152:155], v[52:55], v[148:151], v[152:155]\n"
"buffer_store_dwordx2 v[18:19], v11, s[4:7], s97 offen offset:64 sc1\n"
"v_mfma_f32_16x16x32_bf16 v[152:155], v[36:39], v[84:87], v[152:155]\n"
"ds_write_b128 v3, v[192:195] offset:15360\n"
"v_mfma_f32_16x16x32_bf16 v[152:155], v[108:111], v[124:127], v[152:155]\n"
"v_fmac_f32_e32 v157, v253, v177\n"
"v_fmac_f32_e32 v158, v253, v178\n"
"v_mfma_f32_16x16x32_bf16 v[152:155], v[60:63], v[180:183], v[152:155]\n"
"v_fmac_f32_e32 v159, v253, v179\n"
"v_add_f32_e32 v156, v156, v40\n"
"v_mfma_f32_16x16x32_bf16 v[176:179], v[80:83], v[180:183], 0\n"
"buffer_load_dwordx4 v[180:183], v1, s[4:7], s98 offen offset:2048 sc1\n"
"v_mfma_f32_16x16x32_bf16 v[176:179], v[88:91], v[124:127], v[176:179]\n"
"ds_read_b128 v[124:127], v4 offset:4096\n"
"ds_read_b128 v[192:195], v9\n"
"v_mfma_f32_16x16x32_bf16 v[176:179], v[100:103], v[84:87], v[176:179]\n"
"ds_read_b128 v[84:87], v5 offset:4096\n"
"v_add_f32_e32 v157, v157, v41\n"
"v_mfma_f32_16x16x32_bf16 v[176:179], v[76:79], v[148:151], v[176:179]\n"
"s_waitcnt lgkmcnt(8)\n"
"v_add_f32_e32 v158, v158, v42\n"
"v_add_f32_e32 v159, v159, v43\n"
"v_mfma_f32_16x16x32_bf16 v[176:179], v[132:135], v[168:171], v[176:179]\n"
"v_fmac_f32_e32 v196, v253, v200\n"
"v_fmac_f32_e32 v197, v253, v201\n"
"v_mfma_f32_16x16x32_bf16 v[176:179], v[128:131], v[164:167], v[176:179]\n"
"buffer_load_dwordx4 v[40:43], v1, s[4:7], s98 offen offset:3072 sc1\n"
"v_mfma_f32_16x16x32_bf16 v[176:179], v[48:51], v[140:143], v[176:179]\n"
"ds_write_b128 v3, v[152:155] offset:16384\n"
"v_mfma_f32_16x16x32_bf16 v[176:179], v[72:75], v[136:139], v[176:179]\n"
"s_waitcnt vmcnt(18)\n"
"ds_read_b128 v[136:139], v4 offset:5120\n"
"ds_read_b128 v[140:143], v9 offset:64\n"
"; @A G1 2.3\n"
"v_mfma_f32_16x16x32_bf16 v[32:35], a[0:3], v[44:47], v[32:35]\n"
"ds_read_b128 v[148:151], v5 offset:5120\n"
"v_fmac_f32_e32 v198, v253, v202\n"
"v_mfma_f32_16x16x32_bf16 v[32:35], a[4:7], v[92:95], v[32:35]\n"
"v_fmac_f32_e32 v199, v253, v203\n"
"v_add_f32_e32 v196, v196, v204\n"
"v_mfma_f32_16x16x32_bf16 v[32:35], a[8:11], v[68:71], v[32:35]\n"
"s_add_u32 s98, s53, 0\n"
"buffer_load_dwordx4 v[152:155], v0, s[4:7], s98 offen sc1\n"
"v_mfma_f32_16x16x32_bf16 v[32:35], a[12:15], v[144:147], v[32:35]\n"
"s_waitcnt vmcnt(14)\n"
"s_add_u32 s97, s70, 0x4000\n"
"v_add_f32_e32 v197, v197, v205\n"
"v_mfma_f32_16x16x32_bf16 v[32:35], a[16:19], v[104:107], v[32:35]\n"
"buffer_load_dwordx4 a[192:195], v0, s[4:7], s97 offen sc1\n"
"v_mfma_f32_16x16x32_bf16 v[32:35], a[20:23], v[160:163], v[32:35]\n"
"ds_write_b128 v3, v[176:179] offset:17408\n"
"v_mfma_f32_16x16x32_bf16 v[32:35], a[24:27], v[172:175], v[32:35]\n"
"s_waitcnt lgkmcnt(9)\n"
"v_add_f32_e32 v198, v198, v206\n"
"v_add_f32_e32 v199, v199, v207\n"
"v_mfma_f32_16x16x32_bf16 v[32:35], a[28:31], v[184:187], v[32:35]\n"
"buffer_load_dwordx4 v[164:167], v0, s[4:7], s98 offen offset:1024 sc1\n"
"v_mfma_f32_16x16x32_bf16 v[156:159], a[60:63], v[184:187], v[156:159]\n"
"v_fmac_f32_e32 v208, v253, v212\n"
"v_fmac_f32_e32 v209, v253, v213\n"
"v_mfma_f32_16x16x32_bf16 v[156:159], a[56:59], v[172:175], v[156:159]\n"
"v_fmac_f32_e32 v210, v253, v214\n"
"v_fmac_f32_e32 v211, v253, v215\n"
"v_mfma_f32_16x16x32_bf16 v[156:159], a[52:55], v[160:163], v[156:159]\n"
"s_waitcnt lgkmcnt(0)\n"
"s_barrier\n"
"v_mfma_f32_16x16x32_bf16 v[156:159], a[48:51], v[104:107], v[156:159]\n"
"ds_read_b128 v[168:171], v6\n"
"ds_read_b128 v[176:179], v6 offset:1024\n"
"v_mfma_f32_16x16x32_bf16 v[156:159], a[44:47], v[144:147], v[156:159]\n"
"buffer_load_dwordx4 v[200:203], v0, s[4:7], s98 offen offset:2048 sc1\n"
"v_mfma_f32_16x16x32_bf16 v[156:159], a[40:43], v[68:71], v[156:159]\n"
"v_add_f32_e32 v208, v208, v216\n"
"v_add_f32_e32 v209, v209, v217\n"
"v_mfma_f32_16x16x32_bf16 v[156:159], a[36:39], v[92:95], v[156:159]\n"
"v_add_f32_e32 v210, v210, v218\n"
"v_add_f32_e32 v211, v211, v219\n"
"v_mfma_f32_16x16x32_bf16 v[156:159], a[32:35], v[44:47], v[156:159]\n"
"v_fmac_f32_e32 v124, v253, v192\n"
"v_fmac_f32_e32 v125, v253, v193\n"
"v_mfma_f32_16x16x32_bf16 v[196:199], a[64:67], v[44:47], v[196:199]\n"
"v_fmac_f32_e32 v126, v253, v194\n"
"v_fmac_f32_e32 v127, v253, v195\n"
"v_mfma_f32_16x16x32_bf16 v[196:199], a[68:71], v[92:95], v[196:199]\n"
"buffer_load_dwordx4 v[192:195], v0, s[4:7], s98 offen offset:3072 sc1\n"
"v_mfma_f32_16x16x32_bf16 v[196:199], a[72:75], v[68:71], v[196:199]\n"
"v_add_f32_e32 v124, v124, v84\n"
"v_add_f32_e32 v125, v125, v85\n"
"v_mfma_f32_16x16x32_bf16 v[196:199], a[76:79], v[144:147], v[196:199]\n"
"v_add_f32_e32 v126, v126, v86\n"
"v_add_f32_e32 v127, v127, v87\n"
"v_mfma_f32_16x16x32_bf16 v[196:199], a[80:83], v[104:107], v[196:199]\n"
"s_waitcnt lgkmcnt(0)\n"
"v_fmac_f32_e32 v136, v253, v140\n"
"v_fmac_f32_e32 v137, v253, v141\n"
"v_mfma_f32_16x16x32_bf16 v[196:199], a[84:87], v[160:163], v[196:199]\n"
"v_fmac_f32_e32 v138, v253, v142\n"
"v_fmac_f32_e32 v139, v253, v143\n"
"v_mfma_f32_16x16x32_bf16 v[196:199], a[88:91], v[172:175], v[196:199]\n"
"buffer_load_dwordx4 v[84:87], v1, s[4:7], s98 offen sc1\n"
"v_mfma_f32_16x16x32_bf16 v[196:199], a[92:95], v[184:187], v[196:199]\n"
"ds_read_b128 v[140:143], v6 offset:2048\n"
"ds_read_b128 v[204:207], v6 offset:3072\n"
"v_mfma_f32_16x16x32_bf16 v[208:211], a[124:127], v[184:187], v[208:211]\n"
"buffer_load_dwordx4 a[196:199], v0, s[4:7], s97 offen offset:1024 sc1\n"
"v_mfma_f32_16x16x32_bf16 v[208:211], a[120:123], v[172:175], v[208:211]\n"
"v_add_f32_e32 v136, v136, v148\n"
"v_add_f32_e32 v137, v137, v149\n"
"v_mfma_f32_16x16x32_bf16 v[208:211], a[116:119], v[160:163], v[208:211]\n"
"v_add_f32_e32 v138, v138, v150\n"
"v_add_f32_e32 v139, v139, v151\n"
"v_mfma_f32_16x16x32_bf16 v[208:211], a[112:115], v[104:107], v[208:211]\n"
"buffer_load_dwordx4 v[148:151], v1, s[4:7], s98 offen offset:1024 sc1\n"
"v_mfma_f32_16x16x32_bf16 v[208:211], a[108:111], v[144:147], v[208:211]\n"
"v_add_f32_e32 v32, v32, v168\n"
"v_add_f32_e32 v33, v33, v169\n"
"v_mfma_f32_16x16x32_bf16 v[208:211], a[104:107], v[68:71], v[208:211]\n"
"v_add_f32_e32 v34, v34, v170\n"
"v_add_f32_e32 v35, v35, v171\n"
"v_mfma_f32_16x16x32_bf16 v[208:211], a[100:103], v[92:95], v[208:211]\n"
"v_add_f32_e32 v156, v156, v176\n"
"v_add_f32_e32 v157, v157, v177\n"
"v_mfma_f32_16x16x32_bf16 v[208:211], a[96:99], v[44:47], v[208:211]\n"
"v_add_f32_e32 v158, v158, v178\n"
"v_add_f32_e32 v159, v159, v179\n"
"v_mfma_f32_16x16x32_bf16 v[124:127], v[24:27], v[44:47], v[124:127]\n"
"buffer_load_dwordx4 v[168:171], v1, s[4:7], s98 offen offset:2048 sc1\n"
"v_mfma_f32_16x16x32_bf16 v[124:127], v[56:59], v[92:95], v[124:127]\n"
"v_mul_f32_e32 v32, v32, v255\n"
"v_mul_f32_e32 v33, v33, v255\n"
"v_mfma_f32_16x16x32_bf16 v[124:127], v[28:31], v[68:71], v[124:127]\n"
"v_mul_f32_e32 v34, v34, v255\n"
"v_mul_f32_e32 v35, v35, v255\n"
"v_mfma_f32_16x16x32_bf16 v[124:127], v[64:67], v[144:147], v[124:127]\n"
"v_mul_f32_e32 v156, v156, v255\n"
"v_mul_f32_e32 v157, v157, v255\n"
"v_mfma_f32_16x16x32_bf16 v[124:127], v[52:55], v[104:107], v[124:127]\n"
"s_waitcnt lgkmcnt(0)\n"
"v_mul_f32_e32 v158, v158, v255\n"
"v_mul_f32_e32 v159, v159, v255\n"
"v_mfma_f32_16x16x32_bf16 v[124:127], v[36:39], v[160:163], v[124:127]\n"
"buffer_load_dwordx4 v[176:179], v1, s[4:7], s98 offen offset:3072 sc1\n"
"v_mfma_f32_16x16x32_bf16 v[124:127], v[108:111], v[172:175], v[124:127]\n"
"ds_read_b128 v[212:215], v6 offset:4096\n"
"ds_read_b128 v[216:219], v6 offset:5120\n"
"v_mfma_f32_16x16x32_bf16 v[124:127], v[60:63], v[184:187], v[124:127]\n"
"v_cvt_pk_bf16_f32 v224, v32, v33\n"
"v_cvt_pk_bf16_f32 v225, v34, v35\n"
"v_mfma_f32_16x16x32_bf16 v[136:139], v[80:83], v[184:187], v[136:139]\n"
"v_cvt_pk_bf16_f32 v226, v156, v157\n"
"v_cvt_pk_bf16_f32 v227, v158, v159\n"
"v_mfma_f32_16x16x32_bf16 v[136:139], v[88:91], v[172:175], v[136:139]\n"
"ds_write_b128 v8, v[224:227] offset:17408\n"
"v_mfma_f32_16x16x32_bf16 v[136:139], v[100:103], v[160:163], v[136:139]\n"
"v_add_f32_e32 v196, v196, v140\n"
"v_add_f32_e32 v197, v197, v141\n"
"v_mfma_f32_16x16x32_bf16 v[136:139], v[76:79], v[104:107], v[136:139]\n"
"v_add_f32_e32 v198, v198, v142\n"
"v_add_f32_e32 v199, v199, v143\n"
"v_mfma_f32_16x16x32_bf16 v[136:139], v[132:135], v[144:147], v[136:139]\n"
"v_add_f32_e32 v208, v208, v204\n"
"v_add_f32_e32 v209, v209, v205\n"
"v_mfma_f32_16x16x32_bf16 v[136:139], v[128:131], v[68:71], v[136:139]\n"
"v_add_f32_e32 v210, v210, v206\n"
"v_add_f32_e32 v211, v211, v207\n"
"v_mfma_f32_16x16x32_bf16 v[136:139], v[48:51], v[92:95], v[136:139]\n"
"v_mul_f32_e32 v196, v196, v255\n"
"v_mul_f32_e32 v197, v197, v255\n"
"v_mfma_f32_16x16x32_bf16 v[136:139], v[72:75], v[44:47], v[136:139]\n"
"v_mul_f32_e32 v198, v198, v255\n"
"v_mul_f32_e32 v199, v199, v255\n"
"v_mul_f32_e32 v208, v208, v255\n"
"v_mul_f32_e32 v209, v209, v255\n"
"v_mul_f32_e32 v210, v210, v255\n"
"v_mul_f32_e32 v211, v211, v255\n"
"v_cvt_pk_bf16_f32 v32, v196, v197\n"
"v_cvt_pk_bf16_f32 v33, v198, v199\n"
"v_cvt_pk_bf16_f32 v34, v208, v209\n"
"v_cvt_pk_bf16_f32 v35, v210, v211\n"
"ds_write_b128 v7, v[32:35] offset:50176\n"
".Lmk_a3:\n"
"s_waitcnt vmcnt(16)\n"
"; @A G1 3.0\n"
"v_mfma_f32_16x16x32_bf16 v[44:47], a[0:3], v[96:99], 0\n"
"v_mfma_f32_16x16x32_bf16 v[44:47], a[4:7], v[112:115], v[44:47]\n"
"s_add_u32 s98, s62, 0\n"
"v_mfma_f32_16x16x32_bf16 v[44:47], a[8:11], v[188:191], v[44:47]\n"
"s_add_u32 s96, s54, 0\n"
"buffer_load_dwordx4 v[68:71], v0, s[4:7], s96 offen sc1\n"
"v_mfma_f32_16x16x32_bf16 v[44:47], a[12:15], v[120:123], v[44:47]\n"
"s_waitcnt vmcnt(11)\n"
"v_mfma_f32_16x16x32_bf16 v[44:47], a[16:19], v[116:119], v[44:47]\n"
"buffer_load_dword v255, v10, s[4:7], s98 offen sc1\n"
"v_mfma_f32_16x16x32_bf16 v[44:47], a[20:23], v[220:223], v[44:47]\n"
"s_waitcnt lgkmcnt(2)\n"
"v_add_f32_e32 v124, v124, v212\n"
"v_add_f32_e32 v125, v125, v213\n"
"v_mfma_f32_16x16x32_bf16 v[44:47], a[24:27], v[180:183], v[44:47]\n"
"v_add_f32_e32 v126, v126, v214\n"
"v_add_f32_e32 v127, v127, v215\n"
"v_mfma_f32_16x16x32_bf16 v[44:47], a[28:31], v[40:43], v[44:47]\n"
"buffer_load_dwordx4 v[92:95], v0, s[4:7], s96 offen offset:1024 sc1\n"
"v_mfma_f32_16x16x32_bf16 v[104:107], a[60:63], v[40:43], 0\n"
"buffer_load_dwordx4 a[200:203], v0, s[4:7], s97 offen offset:2048 sc1\n"
"v_mfma_f32_16x16x32_bf16 v[104:107], a[56:59], v[180:183], v[104:107]\n"
"buffer_load_dword v253, v10, s[4:7], s98 offen offset:256 sc1\n"
"v_mfma_f32_16x16x32_bf16 v[104:107], a[52:55], v[220:223], v[104:107]\n"
"v_add_f32_e32 v136, v136, v216\n"
"v_add_f32_e32 v137, v137, v217\n"
"v_mfma_f32_16x16x32_bf16 v[104:107], a[48:51], v[116:119], v[104:107]\n"
"v_add_f32_e32 v138, v138, v218\n"
"v_add_f32_e32 v139, v139, v219\n"
"v_mfma_f32_16x16x32_bf16 v[104:107], a[44:47], v[120:123], v[104:107]\n"
"buffer_load_dwordx4 v[140:143], v0, s[4:7], s96 offen offset:2048 sc1\n"
"v_mfma_f32_16x16x32_bf16 v[104:107], a[40:43], v[188:191], v[104:107]\n"
"ds_write_b128 v3, v[44:47]\n"
"v_mfma_f32_16x16x32_bf16 v[104:107], a[36:39], v[112:115], v[104:107]\n"
"buffer_load_dword v252, v10, s[4:7], s98 offen offset:512 sc1\n"
"v_mfma_f32_16x16x32_bf16 v[104:107], a[32:35], v[96:99], v[104:107]\n"
"v_mul_f32_e32 v124, v124, v254\n"
"v_mul_f32_e32 v125, v125, v254\n"
"v_mfma_f32_16x16x32_bf16 v[44:47], a[64:67], v[96:99], 0\n"
"v_mul_f32_e32 v126, v126, v254\n"
"v_mul_f32_e32 v127, v127, v254\n"
"v_mfma_f32_16x16x32_bf16 v[44:47], a[68:71], v[112:115], v[44:47]\n"
"buffer_load_dwordx4 v[144:147], v0, s[4:7], s96 offen offset:3072 sc1\n"
"v_mfma_f32_16x16x32_bf16 v[44:47], a[72:75], v[188:191], v[44:47]\n"
"buffer_load_dword v251, v10, s[4:7], s98 offen offset:768 sc1\n"
"v_mfma_f32_16x16x32_bf16 v[44:47], a[76:79], v[120:123], v[44:47]\n"
"v_mul_f32_e32 v136, v136, v254\n"
"v_mul_f32_e32 v137, v137, v254\n"
"v_mfma_f32_16x16x32_bf16 v[44:47], a[80:83], v[116:119], v[44:47]\n"
"v_mul_f32_e32 v138, v138, v254\n"
"v_mul_f32_e32 v139, v139, v254\n"
"v_mfma_f32_16x16x32_bf16 v[44:47], a[84:87], v[220:223], v[44:47]\n"
"ds_write_b128 v3, v[104:107] offset:1024\n"
"v_mfma_f32_16x16x32_bf16 v[44:47], a[88:91], v[180:183], v[44:47]\n"
"buffer_load_dwordx4 v[104:107], v1, s[4:7], s96 offen sc1\n"
"v_mfma_f32_16x16x32_bf16 v[44:47], a[92:95], v[40:43], v[44:47]\n"
"buffer_load_dword v254, v10, s[4:7], s98 offen offset:1024 sc1\n"
"v_mfma_f32_16x16x32_bf16 v[156:159], a[124:127], v[40:43], 0\n"
"v_cvt_pk_bf16_f32 v160, v124, v125\n"
"v_cvt_pk_bf16_f32 v161, v126, v127\n"
"v_mfma_f32_16x16x32_bf16 v[156:159], a[120:123], v[180:183], v[156:159]\n"
"v_cvt_pk_bf16_f32 v162, v136, v137\n"
"v_cvt_pk_bf16_f32 v163, v138, v139\n"
"v_mfma_f32_16x16x32_bf16 v[156:159], a[116:119], v[220:223], v[156:159]\n"
"buffer_load_dwordx4 a[204:207], v0, s[4:7], s97 offen offset:3072 sc1\n"
"v_mfma_f32_16x16x32_bf16 v[156:159], a[112:115], v[116:119], v[156:159]\n"
"buffer_load_dwordx4 v[124:127], v1, s[4:7], s96 offen offset:1024 sc1\n"
"v_mfma_f32_16x16x32_bf16 v[156:159], a[108:111], v[120:123], v[156:159]\n"
"buffer_load_dword v250, v10, s[4:7], s98 offen offset:1280 sc1\n"
"v_mfma_f32_16x16x32_bf16 v[156:159], a[104:107], v[188:191], v[156:159]\n"
"ds_write_b128 v3, v[44:47] offset:2048\n"
"v_mfma_f32_16x16x32_bf16 v[156:159], a[100:103], v[112:115], v[156:159]\n"
"ds_write_b128 v2, v[160:163] offset:17408\n"
"v_mfma_f32_16x16x32_bf16 v[156:159], a[96:99], v[96:99], v[156:159]\n"
"buffer_load_dword v249, v10, s[4:7], s98 offen offset:1536 sc1\n"
"v_mfma_f32_16x16x32_bf16 v[44:47], v[24:27], v[96:99], 0\n"
"buffer_load_dwordx4 v[136:139], v1, s[4:7], s96 offen offset:2048 sc1\n"
"v_mfma_f32_16x16x32_bf16 v[44:47], v[56:59], v[112:115], v[44:47]\n"
"buffer_store_dwordx4 v[224:227], v8, s[4:7], s64 offen offset:1024 sc1\n"
"v_mfma_f32_16x16x32_bf16 v[44:47], v[28:31], v[188:191], v[44:47]\n"
"buffer_load_dword v248, v10, s[4:7], s98 offen offset:1792 sc1\n"
"v_mfma_f32_16x16x32_bf16 v[44:47], v[64:67], v[120:123], v[44:47]\n"
"v_mfma_f32_16x16x32_bf16 v[44:47], v[52:55], v[116:119], v[44:47]\n"
"v_mfma_f32_16x16x32_bf16 v[44:47], v[36:39], v[220:223], v[44:47]\n"
"buffer_load_dwordx4 v[160:163], v1, s[4:7], s96 offen offset:3072 sc1\n"
"v_mfma_f32_16x16x32_bf16 v[44:47], v[108:111], v[180:183], v[44:47]\n"
"ds_write_b128 v3, v[156:159] offset:3072\n"
"v_mfma_f32_16x16x32_bf16 v[44:47], v[60:63], v[40:43], v[44:47]\n"
"v_mfma_f32_16x16x32_bf16 v[156:159], v[80:83], v[40:43], 0\n"
"v_mfma_f32_16x16x32_bf16 v[156:159], v[88:91], v[180:183], v[156:159]\n"
"v_mfma_f32_16x16x32_bf16 v[156:159], v[100:103], v[220:223], v[156:159]\n"
"v_mfma_f32_16x16x32_bf16 v[156:159], v[76:79], v[116:119], v[156:159]\n"
"v_mfma_f32_16x16x32_bf16 v[156:159], v[132:135], v[120:123], v[156:159]\n"
"buffer_load_dwordx4 a[208:211], v1, s[4:7], s97 offen sc1\n"
"v_mfma_f32_16x16x32_bf16 v[156:159], v[128:131], v[188:191], v[156:159]\n"
"s_nop 0\n"
"ds_write_b128 v3, v[44:47] offset:4096\n"
"v_mfma_f32_16x16x32_bf16 v[156:159], v[48:51], v[112:115], v[156:159]\n"
"v_mfma_f32_16x16x32_bf16 v[156:159], v[72:75], v[96:99], v[156:159]\n"
"s_waitcnt vmcnt(25)\n"
"; @A G1 3.1\n"
"v_mfma_f32_16x16x32_bf16 v[40:43], a[0:3], v[152:155], 0\n"
"v_mfma_f32_16x16x32_bf16 v[40:43], a[4:7], v[164:167], v[40:43]\n"
"v_mfma_f32_16x16x32_bf16 v[40:43], a[8:11], v[200:203], v[40:43]\n"
"s_add_u32 s98, s55, 0\n"
"buffer_load_dwordx4 v[44:47], v0, s[4:7], s98 offen sc1\n"
"v_mfma_f32_16x16x32_bf16 v[40:43], a[12:15], v[192:195], v[40:43]\n"
"s_waitcnt vmcnt(21)\n"
"v_mfma_f32_16x16x32_bf16 v[40:43], a[16:19], v[84:87], v[40:43]\n"
"v_mfma_f32_16x16x32_bf16 v[40:43], a[20:23], v[148:151], v[40:43]\n"
"ds_write_b128 v3, v[156:159] offset:5120\n"
"v_mfma_f32_16x16x32_bf16 v[40:43], a[24:27], v[168:171], v[40:43]\n"
"v_mfma_f32_16x16x32_bf16 v[40:43], a[28:31], v[176:179], v[40:43]\n"
"buffer_load_dwordx4 v[96:99], v0, s[4:7], s98 offen offset:1024 sc1\n"
"s_add_u32 s96, s64, 0x4000\n"
"v_mfma_f32_16x16x32_bf16 v[112:115], a[60:63], v[176:179], 0\n"
"v_mfma_f32_16x16x32_bf16 v[112:115], a[56:59], v[168:171], v[112:115]\n"
"buffer_store_dwordx4 v[32:35], v7, s[4:7], s96 offen offset:1024 sc1\n"
"v_mfma_f32_16x16x32_bf16 v[112:115], a[52:55], v[148:151], v[112:115]\n"
"v_mfma_f32_16x16x32_bf16 v[112:115], a[48:51], v[84:87], v[112:115]\n"
"v_mfma_f32_16x16x32_bf16 v[112:115], a[44:47], v[192:195], v[112:115]\n"
"buffer_load_dwordx4 v[32:35], v0, s[4:7], s98 offen offset:2048 sc1\n"
"v_mfma_f32_16x16x32_bf16 v[112:115], a[40:43], v[200:203], v[112:115]\n"
"ds_write_b128 v3, v[40:43] offset:6144\n"
"v_mfma_f32_16x16x32_bf16 v[112:115], a[36:39], v[164:167], v[112:115]\n"
"buffer_load_dwordx4 a[212:215], v1, s[4:7], s97 offen offset:1024 sc1\n"
"v_mfma_f32_16x16x32_bf16 v[112:115], a[32:35], v[152:155], v[112:115]\n"
"v_mfma_f32_16x16x32_bf16 v[40:43], a[64:67], v[152:155], 0\n"
"v_mfma_f32_16x16x32_bf16 v[40:43], a[68:71], v[164:167], v[40:43]\n"
"buffer_load_dwordx4 v[116:119], v0, s[4:7], s98 offen offset:3072 sc1\n"
"v_mfma_f32_16x16x32_bf16 v[40:43], a[72:75], v[200:203], v[40:43]\n"
"v_mfma_f32_16x16x32_bf16 v[40:43], a[76:79], v[192:195], v[40:43]\n"
"v_mfma_f32_16x16x32_bf16 v[40:43], a[80:83], v[84:87], v[40:43]\n"
"v_mfma_f32_16x16x32_bf16 v[40:43], a[84:87], v[148:151], v[40:43]\n"
"s_nop 0\n"
"ds_write_b128 v3, v[112:115] offset:7168\n"
"v_mfma_f32_16x16x32_bf16 v[40:43], a[88:91], v[168:171], v[40:43]\n"
"buffer_load_dwordx4 v[112:115], v1, s[4:7], s98 offen sc1\n"
"v_mfma_f32_16x16x32_bf16 v[40:43], a[92:95], v[176:179], v[40:43]\n"
"v_mfma_f32_16x16x32_bf16 v[120:123], a[124:127], v[176:179], 0\n"
"v_mfma_f32_16x16x32_bf16 v[120:123], a[120:123], v[168:171], v[120:123]\n"
"v_mfma_f32_16x16x32_bf16 v[120:123], a[116:119], v[148:151], v[120:123]\n"
"v_mfma_f32_16x16x32_bf16 v[120:123], a[112:115], v[84:87], v[120:123]\n"
"buffer_load_dwordx4 v[156:159], v1, s[4:7], s98 offen offset:1024 sc1\n"
"v_mfma_f32_16x16x32_bf16 v[120:123], a[108:111], v[192:195], v[120:123]\n"
"v_mfma_f32_16x16x32_bf16 v[120:123], a[104:107], v[200:203], v[120:123]\n"
"s_nop 0\n"
"ds_write_b128 v3, v[40:43] offset:8192\n"
"v_mfma_f32_16x16x32_bf16 v[120:123], a[100:103], v[164:167], v[120:123]\n"
"v_mfma_f32_16x16x32_bf16 v[120:123], a[96:99], v[152:155], v[120:123]\n"
"v_mfma_f32_16x16x32_bf16 v[40:43], v[24:27], v[152:155], 0\n"
"buffer_load_dwordx4 v[172:175], v1, s[4:7], s98 offen offset:2048 sc1\n"
"v_mfma_f32_16x16x32_bf16 v[40:43], v[56:59], v[164:167], v[40:43]\n"
"buffer_load_dwordx4 a[216:219], v1, s[4:7], s97 offen offset:2048 sc1\n"
"v_mfma_f32_16x16x32_bf16 v[40:43], v[28:31], v[200:203], v[40:43]\n"
"v_mfma_f32_16x16x32_bf16 v[40:43], v[64:67], v[192:195], v[40:43]\n"
"v_mfma_f32_16x16x32_bf16 v[40:43], v[52:55], v[84:87], v[40:43]\n"
"s_waitcnt vmcnt(12)\n"
"v_add_f32_e32 v255, v255, v254\n"
"v_add_f32_e32 v253, v253, v250\n"
"v_mfma_f32_16x16x32_bf16 v[40:43], v[36:39], v[148:151], v[40:43]\n"
"buffer_load_dwordx4 v[180:183], v1, s[4:7], s98 offen offset:3072 sc1\n"
"v_mfma_f32_16x16x32_bf16 v[40:43], v[108:111], v[168:171], v[40:43]\n"
"ds_write_b128 v3, v[120:123] offset:9216\n"
"v_mfma_f32_16x16x32_bf16 v[40:43], v[60:63], v[176:179], v[40:43]\n"
"v_add_f32_e32 v252, v252, v249\n"
"v_add_f32_e32 v251, v251, v248\n"
"v_mfma_f32_16x16x32_bf16 v[120:123], v[80:83], v[176:179], 0\n"
"v_add_f32_e32 v255, v255, v252\n"
"v_add_f32_e32 v253, v253, v251\n"
"v_mfma_f32_16x16x32_bf16 v[120:123], v[88:91], v[168:171], v[120:123]\n"
"v_add_f32_e32 v255, v255, v253\n"
"v_mov_b32_e32 v253, v255\n"
"v_mfma_f32_16x16x32_bf16 v[120:123], v[100:103], v[148:151], v[120:123]\n"
"s_add_u32 s98, s71, 0\n"
"buffer_load_dwordx2 v[250:251], off, s[4:7], s98 sc1\n"
"v_mfma_f32_16x16x32_bf16 v[120:123], v[76:79], v[84:87], v[120:123]\n"
"v_permlane32_swap_b32_e32 v255, v253\n"
"v_add_f32_e32 v255, v255, v253\n"
"v_mfma_f32_16x16x32_bf16 v[120:123], v[132:135], v[192:195], v[120:123]\n"
"v_mul_f32_e32 v255, 0x3a800000, v255\n"
"v_mov_b32_e32 v253, v255\n"
"v_mfma_f32_16x16x32_bf16 v[120:123], v[128:131], v[200:203], v[120:123]\n"
"ds_write_b128 v3, v[40:43] offset:10240\n"
"v_mfma_f32_16x16x32_bf16 v[120:123], v[48:51], v[164:167], v[120:123]\n"
"buffer_store_dwordx2 v[18:19], v11, s[4:7], s68 offen offset:32 sc1\n"
"v_mfma_f32_16x16x32_bf16 v[120:123], v[72:75], v[152:155], v[120:123]\n"
"v_permlane16_swap_b32_e32 v255, v253\n"
"v_cvt_pk_bf16_f32 v254, -v255, 0\n"
"; @A G1 3.2\n"
"v_mfma_f32_16x16x32_bf16 v[40:43], a[0:3], v[68:71], 0\n"
"v_fma_f32 v253, -v255, v255, v253\n"
"v_lshlrev_b32_e32 v254, 16, v254\n"
"v_mfma_f32_16x16x32_bf16 v[40:43], a[4:7], v[92:95], v[40:43]\n"
"v_max_f32_e32 v253, 0, v253\n"
"v_cvt_pk_bf16_f32 v253, v253, 0\n"
"v_mfma_f32_16x16x32_bf16 v[40:43], a[8:11], v[140:143], v[40:43]\n"
"buffer_load_dwordx4 a[220:223], v1, s[4:7], s97 offen offset:3072 sc1\n"
"v_mfma_f32_16x16x32_bf16 v[40:43], a[12:15], v[144:147], v[40:43]\n"
"s_waitcnt vmcnt(15)\n"
"v_lshlrev_b32_e32 v253, 16, v253\n"
"v_add_f32_e32 v253, 0x3727c5ac, v253\n"
"v_mfma_f32_16x16x32_bf16 v[40:43], a[16:19], v[104:107], v[40:43]\n"
"s_add_u32 s97, s70, 0x6000\n"
"v_cvt_pk_bf16_f32 v253, v253, 0\n"
"v_lshlrev_b32_e32 v253, 16, v253\n"
"v_mfma_f32_16x16x32_bf16 v[40:43], a[20:23], v[124:127], v[40:43]\n"
"ds_write_b128 v3, v[120:123] offset:11264\n"
"v_mfma_f32_16x16x32_bf16 v[40:43], a[24:27], v[136:139], v[40:43]\n"
"buffer_load_dwordx4 a[224:227], v0, s[4:7], s97 offen sc1\n"
"v_mfma_f32_16x16x32_bf16 v[40:43], a[28:31], v[160:163], v[40:43]\n"
"v_sqrt_f32_e32 v253, v253\n"
"v_mfma_f32_16x16x32_bf16 v[84:87], a[60:63], v[160:163], 0\n"
"v_cvt_pk_bf16_f32 v253, v253, 0\n"
"v_lshlrev_b32_e32 v253, 16, v253\n"
"v_mfma_f32_16x16x32_bf16 v[84:87], a[56:59], v[136:139], v[84:87]\n"
"v_rcp_f32_e32 v255, v253\n"
"v_mfma_f32_16x16x32_bf16 v[84:87], a[52:55], v[124:127], v[84:87]\n"
"s_waitcnt lgkmcnt(0)\n"
"s_barrier\n"
"v_mfma_f32_16x16x32_bf16 v[84:87], a[48:51], v[104:107], v[84:87]\n"
"buffer_load_dwordx4 a[228:231], v0, s[4:7], s97 offen offset:1024 sc1\n"
"v_mfma_f32_16x16x32_bf16 v[84:87], a[44:47], v[144:147], v[84:87]\n"
"ds_read_b128 v[120:123], v4\n"
"v_mul_f32_e32 v253, 0x3e8293ee, v255\n"
"v_mfma_f32_16x16x32_bf16 v[84:87], a[40:43], v[140:143], v[84:87]\n"
"ds_write_b128 v3, v[40:43] offset:12288\n"
"v_mfma_f32_16x16x32_bf16 v[84:87], a[36:39], v[92:95], v[84:87]\n"
"ds_read_b128 v[40:43], v9 offset:128\n"
"ds_read_b128 v[148:151], v5\n"
"v_mfma_f32_16x16x32_bf16 v[84:87], a[32:35], v[68:71], v[84:87]\n"
"buffer_load_dwordx4 a[232:235], v0, s[4:7], s97 offen offset:2048 sc1\n"
"v_mfma_f32_16x16x32_bf16 v[152:155], a[64:67], v[68:71], 0\n"
"v_mfma_f32_16x16x32_bf16 v[152:155], a[68:71], v[92:95], v[152:155]\n"
"v_mfma_f32_16x16x32_bf16 v[152:155], a[72:75], v[140:143], v[152:155]\n"
"ds_read_b128 v[164:167], v4 offset:1024\n"
"ds_read_b128 v[168:171], v9 offset:192\n"
"v_mfma_f32_16x16x32_bf16 v[152:155], a[76:79], v[144:147], v[152:155]\n"
"buffer_load_dwordx4 a[236:239], v0, s[4:7], s97 offen offset:3072 sc1\n"
"v_mfma_f32_16x16x32_bf16 v[152:155], a[80:83], v[104:107], v[152:155]\n"
"ds_read_b128 v[176:179], v5 offset:1024\n"
"s_waitcnt lgkmcnt(3)\n"
"v_mfma_f32_16x16x32_bf16 v[152:155], a[84:87], v[124:127], v[152:155]\n"
"ds_write_b128 v3, v[84:87] offset:13312\n"
"v_mfma_f32_16x16x32_bf16 v[152:155], a[88:91], v[136:139], v[152:155]\n"
"v_fmac_f32_e32 v120, v254, v40\n"
"v_fmac_f32_e32 v121, v254, v41\n"
"v_mfma_f32_16x16x32_bf16 v[152:155], a[92:95], v[160:163], v[152:155]\n"
"buffer_load_dwordx4 a[240:243], v1, s[4:7], s97 offen sc1\n"
"v_mfma_f32_16x16x32_bf16 v[84:87], a[124:127], v[160:163], 0\n"
"v_fmac_f32_e32 v122, v254, v42\n"
"v_fmac_f32_e32 v123, v254, v43\n"
"v_mfma_f32_16x16x32_bf16 v[84:87], a[120:123], v[136:139], v[84:87]\n"
"ds_read_b128 v[40:43], v4 offset:2048\n"
"ds_read_b128 v[184:187], v9 offset:256\n"
"v_mfma_f32_16x16x32_bf16 v[84:87], a[116:119], v[124:127], v[84:87]\n"
"ds_read_b128 v[188:191], v5 offset:2048\n"
"v_add_f32_e32 v120, v120, v148\n"
"v_mfma_f32_16x16x32_bf16 v[84:87], a[112:115], v[104:107], v[84:87]\n"
"buffer_load_dwordx4 a[244:247], v1, s[4:7], s97 offen offset:1024 sc1\n"
"v_mfma_f32_16x16x32_bf16 v[84:87], a[108:111], v[144:147], v[84:87]\n"
"v_add_f32_e32 v121, v121, v149\n"
"v_add_f32_e32 v122, v122, v150\n"
"v_mfma_f32_16x16x32_bf16 v[84:87], a[104:107], v[140:143], v[84:87]\n"
"ds_write_b128 v3, v[152:155] offset:14336\n"
"v_mfma_f32_16x16x32_bf16 v[84:87], a[100:103], v[92:95], v[84:87]\n"
"s_waitcnt lgkmcnt(5)\n"
"v_add_f32_e32 v123, v123, v151\n"
"v_fmac_f32_e32 v164, v254, v168\n"
"v_mfma_f32_16x16x32_bf16 v[84:87], a[96:99], v[68:71], v[84:87]\n"
"buffer_load_dwordx4 a[248:251], v1, s[4:7], s97 offen offset:2048 sc1\n"
"v_mfma_f32_16x16x32_bf16 v[148:151], v[24:27], v[68:71], 0\n"
"ds_read_b128 v[152:155], v4 offset:3072\n"
"ds_read_b128 v[192:195], v9 offset:320\n"
"v_mfma_f32_16x16x32_bf16 v[148:151], v[56:59], v[92:95], v[148:151]\n"
"ds_read_b128 v[196:199], v5 offset:3072\n"
"v_fmac_f32_e32 v165, v254, v169\n"
"v_mfma_f32_16x16x32_bf16 v[148:151], v[28:31], v[140:143], v[148:151]\n"
"v_fmac_f32_e32 v166, v254, v170\n"
"v_fmac_f32_e32 v167, v254, v171\n"
"v_mfma_f32_16x16x32_bf16 v[148:151], v[64:67], v[144:147], v[148:151]\n"
"buffer_load_dwordx4 a[252:255], v1, s[4:7], s97 offen offset:3072 sc1\n"
"v_mfma_f32_16x16x32_bf16 v[148:151], v[52:55], v[104:107], v[148:151]\n"
"v_add_f32_e32 v164, v164, v176\n"
"v_add_f32_e32 v165, v165, v177\n"
"v_mfma_f32_16x16x32_bf16 v[148:151], v[36:39], v[124:127], v[148:151]\n"
"ds_write_b128 v3, v[84:87] offset:15360\n"
"v_mfma_f32_16x16x32_bf16 v[148:151], v[108:111], v[136:139], v[148:151]\n"
"s_waitcnt lgkmcnt(5)\n"
"v_add_f32_e32 v166, v166, v178\n"
"v_add_f32_e32 v167, v167, v179\n"
"v_mfma_f32_16x16x32_bf16 v[148:151], v[60:63], v[160:163], v[148:151]\n"
"ds_read_b128 v[84:87], v4 offset:4096\n"
"ds_read_b128 v[168:171], v9\n"
"v_mfma_f32_16x16x32_bf16 v[176:179], v[80:83], v[160:163], 0\n"
"ds_read_b128 v[160:163], v5 offset:4096\n"
"v_fmac_f32_e32 v40, v254, v184\n"
"v_mfma_f32_16x16x32_bf16 v[176:179], v[88:91], v[136:139], v[176:179]\n"
"v_fmac_f32_e32 v41, v254, v185\n"
"v_fmac_f32_e32 v42, v254, v186\n"
"v_mfma_f32_16x16x32_bf16 v[176:179], v[100:103], v[124:127], v[176:179]\n"
"v_fmac_f32_e32 v43, v254, v187\n"
"v_add_f32_e32 v40, v40, v188\n"
"v_mfma_f32_16x16x32_bf16 v[176:179], v[76:79], v[104:107], v[176:179]\n"
"v_add_f32_e32 v41, v41, v189\n"
"v_add_f32_e32 v42, v42, v190\n"
"v_mfma_f32_16x16x32_bf16 v[176:179], v[132:135], v[144:147], v[176:179]\n"
"s_waitcnt vmcnt(10)\n"
"v_cmp_ne_u64_e32 vcc, v[250:251], v[18:19]\n"
"v_mfma_f32_16x16x32_bf16 v[176:179], v[128:131], v[140:143], v[176:179]\n"
"ds_write_b128 v3, v[148:151] offset:16384\n"
"v_mfma_f32_16x16x32_bf16 v[176:179], v[48:51], v[92:95], v[176:179]\n"
"s_cbranch_vccnz .Lmk_f73s\n"
".Lmk_f73r:\n"
"s_add_u32 m0, s77, 0x6000\n"
"v_mfma_f32_16x16x32_bf16 v[176:179], v[72:75], v[68:71], v[176:179]\n"
"s_add_u32 s98, s65, 0x2000\n"
"ds_read_b128 v[68:71], v4 offset:5120\n"
"; @A G1 3.3\n"
"v_mfma_f32_16x16x32_bf16 v[120:123], a[0:3], v[44:47], v[120:123]\n"
"buffer_load_dwordx4 v2, s[4:7], s98 offen sc1 lds\n"
"v_mfma_f32_16x16x32_bf16 v[120:123], a[4:7], v[96:99], v[120:123]\n"
"buffer_load_dwordx4 v2, s[4:7], s98 offen offset:1024 sc1 lds\n"
"v_mfma_f32_16x16x32_bf16 v[120:123], a[8:11], v[32:35], v[120:123]\n"
"ds_read_b128 v[92:95], v9 offset:64\n"
"ds_read_b128 v[104:107], v5 offset:5120\n"
"v_mfma_f32_16x16x32_bf16 v[120:123], a[12:15], v[116:119], v[120:123]\n"
"s_waitcnt lgkmcnt(8)\n"
"v_add_f32_e32 v43, v43, v191\n"
"v_mfma_f32_16x16x32_bf16 v[120:123], a[16:19], v[112:115], v[120:123]\n"
"v_fmac_f32_e32 v152, v254, v192\n"
"v_fmac_f32_e32 v153, v254, v193\n"
"v_mfma_f32_16x16x32_bf16 v[120:123], a[20:23], v[156:159], v[120:123]\n"
"ds_write_b128 v3, v[176:179] offset:17408\n"
"v_mfma_f32_16x16x32_bf16 v[120:123], a[24:27], v[172:175], v[120:123]\n"
"v_fmac_f32_e32 v154, v254, v194\n"
"v_fmac_f32_e32 v155, v254, v195\n"
"v_mfma_f32_16x16x32_bf16 v[120:123], a[28:31], v[180:183], v[120:123]\n"
"s_add_u32 m0, s77, 0xe000\n"
"s_add_u32 s98, s65, 0x6000\n"
"v_add_f32_e32 v152, v152, v196\n"
"v_mfma_f32_16x16x32_bf16 v[164:167], a[60:63], v[180:183], v[164:167]\n"
"buffer_load_dwordx4 v2, s[4:7], s98 offen sc1 lds\n"
"v_mfma_f32_16x16x32_bf16 v[164:167], a[56:59], v[172:175], v[164:167]\n"
"buffer_load_dwordx4 v2, s[4:7], s98 offen offset:1024 sc1 lds\n"
"v_mfma_f32_16x16x32_bf16 v[164:167], a[52:55], v[156:159], v[164:167]\n"
"s_waitcnt lgkmcnt(0)\n"
"s_barrier\n"
"v_mfma_f32_16x16x32_bf16 v[164:167], a[48:51], v[112:115], v[164:167]\n"
"ds_read_b128 v[124:127], v6\n"
"ds_read_b128 v[136:139], v6 offset:1024\n"
"v_mfma_f32_16x16x32_bf16 v[164:167], a[44:47], v[116:119], v[164:167]\n"
"s_add_u32 s98, s69, 0x600000\n"
"buffer_load_dwordx4 a[0:3], v0, s[4:7], s98 offen sc1\n"
"v_mfma_f32_16x16x32_bf16 v[164:167], a[40:43], v[32:35], v[164:167]\n"
"v_add_f32_e32 v153, v153, v197\n"
"v_add_f32_e32 v154, v154, v198\n"
"v_mfma_f32_16x16x32_bf16 v[164:167], a[36:39], v[96:99], v[164:167]\n"
"v_add_f32_e32 v155, v155, v199\n"
"v_fmac_f32_e32 v84, v254, v168\n"
"v_mfma_f32_16x16x32_bf16 v[164:167], a[32:35], v[44:47], v[164:167]\n"
"buffer_load_dwordx4 a[4:7], v0, s[4:7], s98 offen offset:1024 sc1\n"
"v_mfma_f32_16x16x32_bf16 v[40:43], a[64:67], v[44:47], v[40:43]\n"
"v_fmac_f32_e32 v85, v254, v169\n"
"v_fmac_f32_e32 v86, v254, v170\n"
"v_mfma_f32_16x16x32_bf16 v[40:43], a[68:71], v[96:99], v[40:43]\n"
"v_fmac_f32_e32 v87, v254, v171\n"
"v_add_f32_e32 v84, v84, v160\n"
"v_mfma_f32_16x16x32_bf16 v[40:43], a[72:75], v[32:35], v[40:43]\n"
"buffer_load_dwordx4 a[8:11], v0, s[4:7], s98 offen offset:2048 sc1\n"
"v_mfma_f32_16x16x32_bf16 v[40:43], a[76:79], v[116:119], v[40:43]\n"
"v_add_f32_e32 v85, v85, v161\n"
"v_add_f32_e32 v86, v86, v162\n"
"v_mfma_f32_16x16x32_bf16 v[40:43], a[80:83], v[112:115], v[40:43]\n"
"v_add_f32_e32 v87, v87, v163\n"
"v_fmac_f32_e32 v68, v254, v92\n"
"v_mfma_f32_16x16x32_bf16 v[40:43], a[84:87], v[156:159], v[40:43]\n"
"buffer_load_dwordx4 a[12:15], v0, s[4:7], s98 offen offset:3072 sc1\n"
"v_mfma_f32_16x16x32_bf16 v[40:43], a[88:91], v[172:175], v[40:43]\n"
"ds_read_b128 v[140:143], v6 offset:2048\n"
"ds_read_b128 v[144:147], v6 offset:3072\n"
"v_mfma_f32_16x16x32_bf16 v[40:43], a[92:95], v[180:183], v[40:43]\n"
"s_waitcnt lgkmcnt(2)\n"
"v_fmac_f32_e32 v69, v254, v93\n"
"v_fmac_f32_e32 v70, v254, v94\n"
"v_mfma_f32_16x16x32_bf16 v[152:155], a[124:127], v[180:183], v[152:155]\n"
"buffer_load_dwordx4 a[16:19], v1, s[4:7], s98 offen sc1\n"
"v_mfma_f32_16x16x32_bf16 v[152:155], a[120:123], v[172:175], v[152:155]\n"
"v_fmac_f32_e32 v71, v254, v95\n"
"v_add_f32_e32 v68, v68, v104\n"
"v_mfma_f32_16x16x32_bf16 v[152:155], a[116:119], v[156:159], v[152:155]\n"
"v_add_f32_e32 v69, v69, v105\n"
"v_add_f32_e32 v70, v70, v106\n"
"v_mfma_f32_16x16x32_bf16 v[152:155], a[112:115], v[112:115], v[152:155]\n"
"buffer_load_dwordx4 a[20:23], v1, s[4:7], s98 offen offset:1024 sc1\n"
"v_mfma_f32_16x16x32_bf16 v[152:155], a[108:111], v[116:119], v[152:155]\n"
"v_add_f32_e32 v71, v71, v107\n"
"v_add_f32_e32 v120, v120, v124\n"
"v_mfma_f32_16x16x32_bf16 v[152:155], a[104:107], v[32:35], v[152:155]\n"
"v_add_f32_e32 v121, v121, v125\n"
"v_add_f32_e32 v122, v122, v126\n"
"v_mfma_f32_16x16x32_bf16 v[152:155], a[100:103], v[96:99], v[152:155]\n"
"buffer_load_dwordx4 a[24:27], v1, s[4:7], s98 offen offset:2048 sc1\n"
"v_mfma_f32_16x16x32_bf16 v[152:155], a[96:99], v[44:47], v[152:155]\n"
"v_add_f32_e32 v123, v123, v127\n"
"v_add_f32_e32 v164, v164, v136\n"
"v_mfma_f32_16x16x32_bf16 v[84:87], v[24:27], v[44:47], v[84:87]\n"
"v_add_f32_e32 v165, v165, v137\n"
"v_add_f32_e32 v166, v166, v138\n"
"v_mfma_f32_16x16x32_bf16 v[84:87], v[56:59], v[96:99], v[84:87]\n"
"buffer_load_dwordx4 a[28:31], v1, s[4:7], s98 offen offset:3072 sc1\n"
"v_mfma_f32_16x16x32_bf16 v[84:87], v[28:31], v[32:35], v[84:87]\n"
"v_add_f32_e32 v167, v167, v139\n"
"v_mul_f32_e32 v120, v120, v255\n"
"v_mfma_f32_16x16x32_bf16 v[84:87], v[64:67], v[116:119], v[84:87]\n"
"v_mul_f32_e32 v121, v121, v255\n"
"v_mul_f32_e32 v122, v122, v255\n"
"v_mfma_f32_16x16x32_bf16 v[84:87], v[52:55], v[112:115], v[84:87]\n"
"s_waitcnt lgkmcnt(0)\n"
"v_mul_f32_e32 v123, v123, v255\n"
"v_mul_f32_e32 v164, v164, v255\n"
"v_mfma_f32_16x16x32_bf16 v[84:87], v[36:39], v[156:159], v[84:87]\n"
"v_mul_f32_e32 v165, v165, v255\n"
"v_mul_f32_e32 v166, v166, v255\n"
"v_mfma_f32_16x16x32_bf16 v[84:87], v[108:111], v[172:175], v[84:87]\n"
"ds_read_b128 v[24:27], v6 offset:4096\n"
"ds_read_b128 v[28:31], v6 offset:5120\n"
"v_mfma_f32_16x16x32_bf16 v[84:87], v[60:63], v[180:183], v[84:87]\n"
"v_mul_f32_e32 v167, v167, v255\n"
"v_cvt_pk_bf16_f32 v36, v120, v121\n"
"v_mfma_f32_16x16x32_bf16 v[68:71], v[80:83], v[180:183], v[68:71]\n"
"v_cvt_pk_bf16_f32 v37, v122, v123\n"
"v_cvt_pk_bf16_f32 v38, v164, v165\n"
"v_mfma_f32_16x16x32_bf16 v[68:71], v[88:91], v[172:175], v[68:71]\n"
"v_cvt_pk_bf16_f32 v39, v166, v167\n"
"v_add_f32_e32 v40, v40, v140\n"
"v_mfma_f32_16x16x32_bf16 v[68:71], v[100:103], v[156:159], v[68:71]\n"
"ds_write_b128 v7, v[36:39] offset:16384\n"
"v_mfma_f32_16x16x32_bf16 v[68:71], v[76:79], v[112:115], v[68:71]\n"
"buffer_store_dwordx4 v[36:39], v7, s[4:7], s64 offen sc1\n"
"v_mfma_f32_16x16x32_bf16 v[68:71], v[132:135], v[116:119], v[68:71]\n"
"v_add_f32_e32 v41, v41, v141\n"
"v_add_f32_e32 v42, v42, v142\n"
"v_mfma_f32_16x16x32_bf16 v[68:71], v[128:131], v[32:35], v[68:71]\n"
"v_add_f32_e32 v43, v43, v143\n"
"v_add_f32_e32 v152, v152, v144\n"
"v_mfma_f32_16x16x32_bf16 v[68:71], v[48:51], v[96:99], v[68:71]\n"
"v_add_f32_e32 v153, v153, v145\n"
"v_add_f32_e32 v154, v154, v146\n"
"v_mfma_f32_16x16x32_bf16 v[68:71], v[72:75], v[44:47], v[68:71]\n"
"v_add_f32_e32 v155, v155, v147\n"
"v_mul_f32_e32 v40, v40, v255\n"
"v_mul_f32_e32 v41, v41, v255\n"
"v_mul_f32_e32 v42, v42, v255\n"
"v_mul_f32_e32 v43, v43, v255\n"
"v_mul_f32_e32 v152, v152, v255\n"
"v_mul_f32_e32 v153, v153, v255\n"
"v_mul_f32_e32 v154, v154, v255\n"
"v_mul_f32_e32 v155, v155, v255\n"
"v_cvt_pk_bf16_f32 v32, v40, v41\n"
"v_cvt_pk_bf16_f32 v33, v42, v43\n"
"v_cvt_pk_bf16_f32 v34, v152, v153\n"
"v_cvt_pk_bf16_f32 v35, v154, v155\n"
"v_xor_b32_e32 v255, 32, v16\n"
"v_xor_b32_e32 v254, 32, v15\n"
"v_mov_b32_e32 v88, 0x3f803f80\n"
"v_mov_b32_e32 v89, 0x3f803f80\n"
"v_mov_b32_e32 v90, 0x3f803f80\n"
"v_mov_b32_e32 v91, 0x3f803f80\n"
"v_mov_b32_e32 v252, 0x53800000\n"
"ds_write_b128 v7, v[32:35] offset:49152\n"
".Lmk_a4:\n"
"; @A tail\n"
"s_add_u32 s98, s64, 0x4000\n"
"buffer_store_dwordx4 v[32:35], v7, s[4:7], s98 offen sc1\n"
"s_waitcnt lgkmcnt(2)\n"
"v_add_f32_e32 v84, v84, v24\n"
"v_add_f32_e32 v85, v85, v25\n"
"v_add_f32_e32 v86, v86, v26\n"
"v_add_f32_e32 v87, v87, v27\n"
"v_add_f32_e32 v68, v68, v28\n"
"v_add_f32_e32 v69, v69, v29\n"
"v_add_f32_e32 v70, v70, v30\n"
"v_add_f32_e32 v71, v71, v31\n"
"v_mul_f32_e32 v84, v84, v253\n"
"v_mul_f32_e32 v85, v85, v253\n"
"v_mul_f32_e32 v86, v86, v253\n"
"v_mul_f32_e32 v87, v87, v253\n"
"v_mul_f32_e32 v68, v68, v253\n"
"v_mul_f32_e32 v69, v69, v253\n"
"v_mul_f32_e32 v70, v70, v253\n"
"v_mul_f32_e32 v71, v71, v253\n"
"v_cvt_pk_bf16_f32 v24, v84, v85\n"
"v_cvt_pk_bf16_f32 v25, v86, v87\n"
"v_cvt_pk_bf16_f32 v26, v68, v69\n"
"v_cvt_pk_bf16_f32 v27, v70, v71\n"
"ds_write_b128 v2, v[24:27] offset:16384\n"
"s_waitcnt lgkmcnt(0)\n"
"s_barrier\n"
"ds_read_b128 v[28:31], v2 offset:49152\n"
"ds_read_b128 v[32:35], v2 offset:50176\n"
"ds_read_b128 v[92:95], v2 offset:16384\n"
"ds_read_b128 v[96:99], v2 offset:17408\n"
"; @A AT b\n"
"s_mov_b32 s2, 0\n"
"v_mov_b32_e32 v253, 0xff800000\n"
"v_mov_b32_e32 v251, 0xff800000\n"
"v_mov_b32_e32 v250, 0x7f800000\n"
"v_mov_b32_e32 v249, 0x7f800000\n"
".Lmk_a91:\n"
"ds_read_b128 v[24:27], v15 offset:24576\n"
"ds_read_b128 v[36:39], v254 offset:24832\n"
"ds_read_b128 v[40:43], v15 offset:22528\n"
"ds_read_b128 v[44:47], v254 offset:22784\n"
"s_cmp_gt_i32 s78, 11\n"
"s_cselect_b32 s98, 0x80000000, 0\n"
"s_or_b32 s98, s98, 0x7f800000\n"
"s_waitcnt lgkmcnt(2)\n"
"v_mfma_f32_16x16x32_bf16 v[48:51], v[24:27], v[28:31], 0\n"
"v_mfma_f32_16x16x32_bf16 v[52:55], v[24:27], v[32:35], 0\n"
"v_mfma_f32_16x16x32_bf16 v[56:59], v[36:39], v[32:35], 0\n"
"v_mfma_f32_16x16x32_bf16 v[24:27], v[36:39], v[28:31], 0\n"
"s_waitcnt lgkmcnt(0)\n"
"s_nop 3\n"
"v_max_f32_e32 v248, v48, v49\n"
"v_max_f32_e32 v247, v52, v53\n"
"v_max3_f32 v248, v248, v50, v51\n"
"v_max3_f32 v247, v247, v54, v55\n"
"v_max3_f32 v248, v248, v24, v25\n"
"v_max3_f32 v247, v247, v56, v57\n"
"v_max3_f32 v248, v248, v26, v27\n"
"v_max3_f32 v247, v247, v58, v59\n"
"v_mov_b32_e32 v246, v248\n"
"v_mov_b32_e32 v245, v247\n"
"s_nop 0\n"
"v_permlane16_swap_b32_e32 v248, v246\n"
"v_permlane16_swap_b32_e32 v247, v245\n"
"v_max_f32_e32 v248, v248, v246\n"
"v_max_f32_e32 v247, v247, v245\n"
"v_mov_b32_e32 v246, v248\n"
"v_mov_b32_e32 v245, v247\n"
"s_nop 0\n"
"v_permlane32_swap_b32_e32 v248, v246\n"
"v_permlane32_swap_b32_e32 v247, v245\n"
"v_max_f32_e32 v248, v248, v246\n"
"v_max_f32_e32 v247, v247, v245\n"
"v_med3_f32 v248, v248, v253, v250\n"
"v_med3_f32 v247, v247, v251, v249\n"
"v_min_f32_e64 v36, s98, -v248\n"
"v_min_f32_e64 v37, s98, -v248\n"
"v_min_f32_e64 v38, s98, -v248\n"
"v_min_f32_e64 v39, s98, -v248\n"
"v_min_f32_e64 v60, s98, -v247\n"
"v_min_f32_e64 v61, s98, -v247\n"
"v_min_f32_e64 v62, s98, -v247\n"
"v_min_f32_e64 v63, s98, -v247\n"
"v_mfma_f32_16x16x32_bf16 v[64:67], v[40:43], v[28:31], v[36:39]\n"
"ds_read_b128 v[68:71], v15 offset:20480\n"
"s_add_u32 s98, s69, 0x602000\n"
"buffer_load_dwordx4 a[32:35], v0, s[4:7], s98 offen sc1\n"
"v_sub_f32_e32 v48, v48, v248\n"
"v_sub_f32_e32 v49, v49, v248\n"
"v_sub_f32_e32 v50, v50, v248\n"
"v_mfma_f32_16x16x32_bf16 v[72:75], v[40:43], v[32:35], v[60:63]\n"
"buffer_load_dwordx4 a[36:39], v0, s[4:7], s98 offen offset:1024 sc1\n"
"v_sub_f32_e32 v51, v51, v248\n"
"v_sub_f32_e32 v24, v24, v248\n"
"v_sub_f32_e32 v25, v25, v248\n"
"v_sub_f32_e32 v26, v26, v248\n"
"v_sub_f32_e32 v27, v27, v248\n"
"v_mfma_f32_16x16x32_bf16 v[40:43], v[44:47], v[32:35], v[60:63]\n"
"ds_read_b128 v[76:79], v254 offset:20736\n"
"v_sub_f32_e32 v52, v52, v247\n"
"v_sub_f32_e32 v53, v53, v247\n"
"v_sub_f32_e32 v54, v54, v247\n"
"v_sub_f32_e32 v55, v55, v247\n"
"v_mfma_f32_16x16x32_bf16 v[80:83], v[44:47], v[28:31], v[36:39]\n"
"buffer_load_dwordx4 a[40:43], v0, s[4:7], s98 offen offset:2048 sc1\n"
"v_sub_f32_e32 v56, v56, v247\n"
"v_sub_f32_e32 v57, v57, v247\n"
"v_sub_f32_e32 v58, v58, v247\n"
"v_sub_f32_e32 v59, v59, v247\n"
"s_waitcnt lgkmcnt(0)\n"
"v_mfma_f32_16x16x32_bf16 v[44:47], v[68:71], v[28:31], v[36:39]\n"
"ds_read_b128 v[84:87], v15 offset:18432\n"
"buffer_load_dwordx4 a[44:47], v0, s[4:7], s98 offen offset:3072 sc1\n"
"ds_read_b128 v[100:103], v254 offset:18688\n"
"v_exp_f32_e32 v64, v64\n"
"v_exp_f32_e32 v65, v65\n"
"v_exp_f32_e32 v66, v66\n"
"v_exp_f32_e32 v67, v67\n"
"v_exp_f32_e32 v80, v80\n"
"v_mfma_f32_16x16x32_bf16 v[104:107], v[68:71], v[32:35], v[60:63]\n"
"ds_read_b64_tr_b16 v[68:69], v16 offset:22528\n"
"buffer_load_dwordx4 a[48:51], v1, s[4:7], s98 offen sc1\n"
"v_exp_f32_e32 v81, v81\n"
"v_exp_f32_e32 v82, v82\n"
"v_exp_f32_e32 v83, v83\n"
"v_cvt_pk_bf16_f32 v108, v64, v65\n"
"v_cvt_pk_bf16_f32 v109, v66, v67\n"
"v_cvt_pk_bf16_f32 v110, v80, v81\n"
"v_mfma_f32_16x16x32_bf16 v[64:67], v[76:79], v[32:35], v[60:63]\n"
"ds_read_b64_tr_b16 v[70:71], v255 offset:22784\n"
"ds_read_b64_tr_b16 v[80:81], v255 offset:22528\n"
"buffer_load_dwordx4 a[52:55], v1, s[4:7], s98 offen offset:1024 sc1\n"
"v_cvt_pk_bf16_f32 v111, v82, v83\n"
"v_exp_f32_e32 v72, v72\n"
"v_exp_f32_e32 v73, v73\n"
"v_exp_f32_e32 v74, v74\n"
"v_exp_f32_e32 v75, v75\n"
"v_mfma_f32_16x16x32_bf16 v[112:115], v[76:79], v[28:31], v[36:39]\n"
"ds_read_b64_tr_b16 v[82:83], v16 offset:22784\n"
"v_exp_f32_e32 v40, v40\n"
"v_exp_f32_e32 v41, v41\n"
"v_exp_f32_e32 v42, v42\n"
"v_exp_f32_e32 v43, v43\n"
"v_cvt_pk_bf16_f32 v76, v72, v73\n"
"v_cvt_pk_bf16_f32 v77, v74, v75\n"
"v_cvt_pk_bf16_f32 v78, v40, v41\n"
"v_cvt_pk_bf16_f32 v79, v42, v43\n"
"s_waitcnt lgkmcnt(4)\n"
"v_mfma_f32_16x16x32_bf16 v[40:43], v[84:87], v[28:31], v[36:39]\n"
"ds_read_b128 v[72:75], v15 offset:16384\n"
"v_exp_f32_e32 v44, v44\n"
"v_exp_f32_e32 v45, v45\n"
"v_exp_f32_e32 v46, v46\n"
"v_mfma_f32_16x16x32_bf16 v[116:119], v[84:87], v[32:35], v[60:63]\n"
"buffer_load_dwordx4 a[56:59], v1, s[4:7], s98 offen offset:2048 sc1\n"
"v_exp_f32_e32 v47, v47\n"
"v_mfma_f32_16x16x32_bf16 v[84:87], v[100:103], v[32:35], v[60:63]\n"
"ds_read_b128 v[120:123], v254 offset:16640\n"
"v_exp_f32_e32 v112, v112\n"
"v_exp_f32_e32 v113, v113\n"
"v_mfma_f32_16x16x32_bf16 v[124:127], v[100:103], v[28:31], v[36:39]\n"
"ds_read_b64_tr_b16 v[100:101], v16 offset:20480\n"
"v_exp_f32_e32 v114, v114\n"
"v_exp_f32_e32 v115, v115\n"
"v_cvt_pk_bf16_f32 v128, v44, v45\n"
"s_waitcnt lgkmcnt(3)\n"
"v_mfma_f32_16x16x32_bf16 v[132:135], v[88:91], v[108:111], 0\n"
"buffer_load_dwordx4 a[60:63], v1, s[4:7], s98 offen offset:3072 sc1\n"
"v_cvt_pk_bf16_f32 v129, v46, v47\n"
"v_cvt_pk_bf16_f32 v130, v112, v113\n"
"v_cvt_pk_bf16_f32 v131, v114, v115\n"
"v_mfma_f32_16x16x32_bf16 v[44:47], v[68:71], v[108:111], 0\n"
"ds_read_b64_tr_b16 v[102:103], v255 offset:20736\n"
"v_exp_f32_e32 v104, v104\n"
"v_exp_f32_e32 v105, v105\n"
"v_mfma_f32_16x16x32_bf16 v[112:115], v[80:83], v[108:111], 0\n"
"ds_read_b64_tr_b16 v[108:109], v255 offset:20480\n"
"s_add_u32 s98, s69, 0x604000\n"
"buffer_load_dwordx4 a[64:67], v0, s[4:7], s98 offen sc1\n"
"v_exp_f32_e32 v106, v106\n"
"v_exp_f32_e32 v107, v107\n"
"v_mfma_f32_16x16x32_bf16 v[136:139], v[80:83], v[76:79], 0\n"
"v_exp_f32_e32 v64, v64\n"
"v_exp_f32_e32 v65, v65\n"
"v_mfma_f32_16x16x32_bf16 v[80:83], v[68:71], v[76:79], 0\n"
"ds_read_b64_tr_b16 v[110:111], v16 offset:20736\n"
"v_exp_f32_e32 v66, v66\n"
"v_exp_f32_e32 v67, v67\n"
"v_mfma_f32_16x16x32_bf16 v[68:71], v[88:91], v[76:79], 0\n"
"s_waitcnt vmcnt(19)\n"
"v_cvt_pk_bf16_f32 v76, v104, v105\n"
"v_cvt_pk_bf16_f32 v77, v106, v107\n"
"v_cvt_pk_bf16_f32 v78, v64, v65\n"
"v_cvt_pk_bf16_f32 v79, v66, v67\n"
"s_cmp_lg_u32 s2, 0\n"
"s_cbranch_scc1 .Lmk_s93\n"
"s_barrier\n"
".Lmk_s93:\n"
"s_waitcnt lgkmcnt(4)\n"
"v_mfma_f32_16x16x32_bf16 v[64:67], v[72:75], v[28:31], v[36:39]\n"
"ds_read_b128 v[104:107], v15 offset:14336\n"
"v_exp_f32_e32 v40, v40\n"
"v_exp_f32_e32 v41, v41\n"
"v_exp_f32_e32 v42, v42\n"
"v_mfma_f32_16x16x32_bf16 v[140:143], v[72:75], v[32:35], v[60:63]\n"
"buffer_load_dwordx4 a[68:71], v0, s[4:7], s98 offen offset:1024 sc1\n"
"v_exp_f32_e32 v43, v43\n"
"v_mfma_f32_16x16x32_bf16 v[72:75], v[120:123], v[32:35], v[60:63]\n"
"ds_read_b128 v[144:147], v254 offset:14592\n"
"v_exp_f32_e32 v124, v124\n"
"v_exp_f32_e32 v125, v125\n"
"v_mfma_f32_16x16x32_bf16 v[148:151], v[120:123], v[28:31], v[36:39]\n"
"ds_read_b64_tr_b16 v[120:121], v16 offset:18432\n"
"v_exp_f32_e32 v126, v126\n"
"v_exp_f32_e32 v127, v127\n"
"s_waitcnt lgkmcnt(3)\n"
"v_mfma_f32_16x16x32_bf16 v[132:135], v[88:91], v[128:131], v[132:135]\n"
"buffer_load_dwordx4 a[72:75], v0, s[4:7], s98 offen offset:2048 sc1\n"
"v_cvt_pk_bf16_f32 v152, v40, v41\n"
"v_cvt_pk_bf16_f32 v153, v42, v43\n"
"v_cvt_pk_bf16_f32 v154, v124, v125\n"
"v_mfma_f32_16x16x32_bf16 v[44:47], v[100:103], v[128:131], v[44:47]\n"
"ds_read_b64_tr_b16 v[122:123], v255 offset:18688\n"
"v_cvt_pk_bf16_f32 v155, v126, v127\n"
"v_exp_f32_e32 v116, v116\n"
"v_exp_f32_e32 v117, v117\n"
"v_mfma_f32_16x16x32_bf16 v[112:115], v[108:111], v[128:131], v[112:115]\n"
"ds_read_b64_tr_b16 v[40:41], v255 offset:18432\n"
"v_exp_f32_e32 v118, v118\n"
"v_exp_f32_e32 v119, v119\n"
"v_mfma_f32_16x16x32_bf16 v[136:139], v[108:111], v[76:79], v[136:139]\n"
"buffer_load_dwordx4 a[76:79], v0, s[4:7], s98 offen offset:3072 sc1\n"
"v_exp_f32_e32 v84, v84\n"
"v_exp_f32_e32 v85, v85\n"
"v_mfma_f32_16x16x32_bf16 v[80:83], v[100:103], v[76:79], v[80:83]\n"
"ds_read_b64_tr_b16 v[42:43], v16 offset:18688\n"
"v_exp_f32_e32 v86, v86\n"
"v_exp_f32_e32 v87, v87\n"
"v_mfma_f32_16x16x32_bf16 v[68:71], v[88:91], v[76:79], v[68:71]\n"
"s_waitcnt lgkmcnt(4)\n"
"v_cvt_pk_bf16_f32 v76, v116, v117\n"
"v_cvt_pk_bf16_f32 v77, v118, v119\n"
"v_cvt_pk_bf16_f32 v78, v84, v85\n"
"v_cvt_pk_bf16_f32 v79, v86, v87\n"
"s_cmp_eq_i32 s78, 8\n"
"s_cbranch_scc1 .Lmk_x94s\n"
".Lmk_x94r:\n"
"v_mfma_f32_16x16x32_bf16 v[84:87], v[104:107], v[28:31], v[36:39]\n"
"ds_read_b128 v[100:103], v15 offset:12288\n"
"v_exp_f32_e32 v64, v64\n"
"v_exp_f32_e32 v65, v65\n"
"v_exp_f32_e32 v66, v66\n"
"v_mfma_f32_16x16x32_bf16 v[108:111], v[104:107], v[32:35], v[60:63]\n"
"buffer_load_dwordx4 a[80:83], v1, s[4:7], s98 offen sc1\n"
"v_exp_f32_e32 v67, v67\n"
"v_mfma_f32_16x16x32_bf16 v[104:107], v[144:147], v[32:35], v[60:63]\n"
"ds_read_b128 v[116:119], v254 offset:12544\n"
"v_exp_f32_e32 v148, v148\n"
"v_exp_f32_e32 v149, v149\n"
"v_mfma_f32_16x16x32_bf16 v[124:127], v[144:147], v[28:31], v[36:39]\n"
"ds_read_b64_tr_b16 v[128:129], v16 offset:16384\n"
"v_exp_f32_e32 v150, v150\n"
"v_exp_f32_e32 v151, v151\n"
"s_waitcnt lgkmcnt(3)\n"
"v_mfma_f32_16x16x32_bf16 v[132:135], v[88:91], v[152:155], v[132:135]\n"
"buffer_load_dwordx4 a[84:87], v1, s[4:7], s98 offen offset:1024 sc1\n"
"v_cvt_pk_bf16_f32 v144, v64, v65\n"
"v_cvt_pk_bf16_f32 v145, v66, v67\n"
"v_cvt_pk_bf16_f32 v146, v148, v149\n"
"v_mfma_f32_16x16x32_bf16 v[44:47], v[120:123], v[152:155], v[44:47]\n"
"ds_read_b64_tr_b16 v[130:131], v255 offset:16640\n"
"v_cvt_pk_bf16_f32 v147, v150, v151\n"
"v_exp_f32_e32 v140, v140\n"
"v_exp_f32_e32 v141, v141\n"
"v_mfma_f32_16x16x32_bf16 v[112:115], v[40:43], v[152:155], v[112:115]\n"
"ds_read_b64_tr_b16 v[64:65], v255 offset:16384\n"
"v_exp_f32_e32 v142, v142\n"
"v_exp_f32_e32 v143, v143\n"
"v_mfma_f32_16x16x32_bf16 v[136:139], v[40:43], v[76:79], v[136:139]\n"
"buffer_load_dwordx4 a[88:91], v1, s[4:7], s98 offen offset:2048 sc1\n"
"v_exp_f32_e32 v72, v72\n"
"v_exp_f32_e32 v73, v73\n"
"v_mfma_f32_16x16x32_bf16 v[80:83], v[120:123], v[76:79], v[80:83]\n"
"ds_read_b64_tr_b16 v[66:67], v16 offset:16640\n"
"v_exp_f32_e32 v74, v74\n"
"v_exp_f32_e32 v75, v75\n"
"v_mfma_f32_16x16x32_bf16 v[68:71], v[88:91], v[76:79], v[68:71]\n"
"s_waitcnt lgkmcnt(4)\n"
"v_cvt_pk_bf16_f32 v40, v140, v141\n"
"v_cvt_pk_bf16_f32 v41, v142, v143\n"
"v_cvt_pk_bf16_f32 v42, v72, v73\n"
"v_cvt_pk_bf16_f32 v43, v74, v75\n"
"s_cmp_eq_i32 s78, 7\n"
"s_cbranch_scc1 .Lmk_x95s\n"
".Lmk_x95r:\n"
"v_mfma_f32_16x16x32_bf16 v[72:75], v[100:103], v[28:31], v[36:39]\n"
"ds_read_b128 v[76:79], v15 offset:10240\n"
"v_exp_f32_e32 v84, v84\n"
"v_exp_f32_e32 v85, v85\n"
"v_exp_f32_e32 v86, v86\n"
"v_mfma_f32_16x16x32_bf16 v[120:123], v[100:103], v[32:35], v[60:63]\n"
"buffer_load_dwordx4 a[92:95], v1, s[4:7], s98 offen offset:3072 sc1\n"
"v_exp_f32_e32 v87, v87\n"
"v_mfma_f32_16x16x32_bf16 v[100:103], v[116:119], v[32:35], v[60:63]\n"
"ds_read_b128 v[140:143], v254 offset:10496\n"
"v_exp_f32_e32 v124, v124\n"
"v_exp_f32_e32 v125, v125\n"
"v_mfma_f32_16x16x32_bf16 v[148:151], v[116:119], v[28:31], v[36:39]\n"
"ds_read_b64_tr_b16 v[116:117], v16 offset:14336\n"
"v_exp_f32_e32 v126, v126\n"
"v_exp_f32_e32 v127, v127\n"
"v_cvt_pk_bf16_f32 v152, v84, v85\n"
"s_waitcnt lgkmcnt(3)\n"
"v_mfma_f32_16x16x32_bf16 v[132:135], v[88:91], v[144:147], v[132:135]\n"
"s_add_u32 s98, s69, 0x606000\n"
"buffer_load_dwordx4 a[96:99], v0, s[4:7], s98 offen sc1\n"
"v_cvt_pk_bf16_f32 v153, v86, v87\n"
"v_cvt_pk_bf16_f32 v154, v124, v125\n"
"v_mfma_f32_16x16x32_bf16 v[44:47], v[128:131], v[144:147], v[44:47]\n"
"ds_read_b64_tr_b16 v[118:119], v255 offset:14592\n"
"v_cvt_pk_bf16_f32 v155, v126, v127\n"
"v_exp_f32_e32 v108, v108\n"
"v_exp_f32_e32 v109, v109\n"
"v_mfma_f32_16x16x32_bf16 v[112:115], v[64:67], v[144:147], v[112:115]\n"
"ds_read_b64_tr_b16 v[84:85], v255 offset:14336\n"
"v_exp_f32_e32 v110, v110\n"
"v_exp_f32_e32 v111, v111\n"
"v_mfma_f32_16x16x32_bf16 v[136:139], v[64:67], v[40:43], v[136:139]\n"
"buffer_load_dwordx4 a[100:103], v0, s[4:7], s98 offen offset:1024 sc1\n"
"v_exp_f32_e32 v104, v104\n"
"v_exp_f32_e32 v105, v105\n"
"v_mfma_f32_16x16x32_bf16 v[80:83], v[128:131], v[40:43], v[80:83]\n"
"ds_read_b64_tr_b16 v[86:87], v16 offset:14592\n"
"v_exp_f32_e32 v106, v106\n"
"v_exp_f32_e32 v107, v107\n"
"v_mfma_f32_16x16x32_bf16 v[68:71], v[88:91], v[40:43], v[68:71]\n"
"s_waitcnt lgkmcnt(4)\n"
"v_cvt_pk_bf16_f32 v40, v108, v109\n"
"v_cvt_pk_bf16_f32 v41, v110, v111\n"
"v_cvt_pk_bf16_f32 v42, v104, v105\n"
"v_cvt_pk_bf16_f32 v43, v106, v107\n"
"s_cmp_eq_i32 s78, 6\n"
"s_cbranch_scc1 .Lmk_x96s\n"
".Lmk_x96r:\n"
"v_mfma_f32_16x16x32_bf16 v[64:67], v[76:79], v[28:31], v[36:39]\n"
"ds_read_b128 v[104:107], v15 offset:8192\n"
"v_exp_f32_e32 v72, v72\n"
"v_exp_f32_e32 v73, v73\n"
"v_exp_f32_e32 v74, v74\n"
"v_mfma_f32_16x16x32_bf16 v[108:111], v[76:79], v[32:35], v[60:63]\n"
"buffer_load_dwordx4 a[104:107], v0, s[4:7], s98 offen offset:2048 sc1\n"
"v_exp_f32_e32 v75, v75\n"
"v_mfma_f32_16x16x32_bf16 v[76:79], v[140:143], v[32:35], v[60:63]\n"
"ds_read_b128 v[124:127], v254 offset:8448\n"
"v_exp_f32_e32 v148, v148\n"
"v_exp_f32_e32 v149, v149\n"
"v_mfma_f32_16x16x32_bf16 v[128:131], v[140:143], v[28:31], v[36:39]\n"
"ds_read_b64_tr_b16 v[140:141], v16 offset:12288\n"
"v_exp_f32_e32 v150, v150\n"
"v_exp_f32_e32 v151, v151\n"
"s_waitcnt lgkmcnt(3)\n"
"v_mfma_f32_16x16x32_bf16 v[132:135], v[88:91], v[152:155], v[132:135]\n"
"buffer_load_dwordx4 a[108:111], v0, s[4:7], s98 offen offset:3072 sc1\n"
"v_cvt_pk_bf16_f32 v144, v72, v73\n"
"v_cvt_pk_bf16_f32 v145, v74, v75\n"
"v_cvt_pk_bf16_f32 v146, v148, v149\n"
"v_mfma_f32_16x16x32_bf16 v[44:47], v[116:119], v[152:155], v[44:47]\n"
"ds_read_b64_tr_b16 v[142:143], v255 offset:12544\n"
"v_cvt_pk_bf16_f32 v147, v150, v151\n"
"v_exp_f32_e32 v120, v120\n"
"v_exp_f32_e32 v121, v121\n"
"v_mfma_f32_16x16x32_bf16 v[112:115], v[84:87], v[152:155], v[112:115]\n"
"ds_read_b64_tr_b16 v[72:73], v255 offset:12288\n"
"v_exp_f32_e32 v122, v122\n"
"v_exp_f32_e32 v123, v123\n"
"v_mfma_f32_16x16x32_bf16 v[136:139], v[84:87], v[40:43], v[136:139]\n"
"buffer_load_dwordx4 a[112:115], v1, s[4:7], s98 offen sc1\n"
"v_exp_f32_e32 v100, v100\n"
"v_exp_f32_e32 v101, v101\n"
"v_mfma_f32_16x16x32_bf16 v[80:83], v[116:119], v[40:43], v[80:83]\n"
"ds_read_b64_tr_b16 v[74:75], v16 offset:12544\n"
"v_exp_f32_e32 v102, v102\n"
"v_exp_f32_e32 v103, v103\n"
"v_mfma_f32_16x16x32_bf16 v[68:71], v[88:91], v[40:43], v[68:71]\n"
"s_waitcnt lgkmcnt(4)\n"
"v_cvt_pk_bf16_f32 v40, v120, v121\n"
"v_cvt_pk_bf16_f32 v41, v122, v123\n"
"v_cvt_pk_bf16_f32 v42, v100, v101\n"
"v_cvt_pk_bf16_f32 v43, v102, v103\n"
"s_cmp_eq_i32 s78, 5\n"
"s_cbranch_scc1 .Lmk_x97s\n"
".Lmk_x97r:\n"
"v_mfma_f32_16x16x32_bf16 v[84:87], v[104:107], v[28:31], v[36:39]\n"
"ds_read_b64_tr_b16 v[100:101], v16 offset:10240\n"
"ds_read_b64_tr_b16 v[102:103], v255 offset:10496\n"
"v_exp_f32_e32 v64, v64\n"
"v_exp_f32_e32 v65, v65\n"
"v_mfma_f32_16x16x32_bf16 v[116:119], v[104:107], v[32:35], v[60:63]\n"
"buffer_load_dwordx4 a[116:119], v1, s[4:7], s98 offen offset:1024 sc1\n"
"v_exp_f32_e32 v66, v66\n"
"v_exp_f32_e32 v67, v67\n"
"v_mfma_f32_16x16x32_bf16 v[104:107], v[124:127], v[32:35], v[60:63]\n"
"ds_read_b64_tr_b16 v[60:61], v255 offset:10240\n"
"ds_read_b64_tr_b16 v[62:63], v16 offset:10496\n"
"v_exp_f32_e32 v128, v128\n"
"v_exp_f32_e32 v129, v129\n"
"v_mfma_f32_16x16x32_bf16 v[120:123], v[124:127], v[28:31], v[36:39]\n"
"ds_read_b64_tr_b16 v[36:37], v16 offset:24576\n"
"v_exp_f32_e32 v130, v130\n"
"v_exp_f32_e32 v131, v131\n"
"s_waitcnt lgkmcnt(5)\n"
"v_mfma_f32_16x16x32_bf16 v[132:135], v[88:91], v[144:147], v[132:135]\n"
"buffer_load_dwordx4 a[120:123], v1, s[4:7], s98 offen offset:2048 sc1\n"
"ds_read_b64_tr_b16 v[38:39], v255 offset:24832\n"
"v_cvt_pk_bf16_f32 v124, v64, v65\n"
"v_cvt_pk_bf16_f32 v125, v66, v67\n"
"v_cvt_pk_bf16_f32 v126, v128, v129\n"
"v_mfma_f32_16x16x32_bf16 v[44:47], v[140:143], v[144:147], v[44:47]\n"
"ds_read_b64_tr_b16 v[64:65], v255 offset:24576\n"
"v_cvt_pk_bf16_f32 v127, v130, v131\n"
"v_exp_f32_e32 v108, v108\n"
"v_exp_f32_e32 v109, v109\n"
"v_mfma_f32_16x16x32_bf16 v[112:115], v[72:75], v[144:147], v[112:115]\n"
"ds_read_b64_tr_b16 v[66:67], v16 offset:24832\n"
"v_exp_f32_e32 v110, v110\n"
"v_exp_f32_e32 v111, v111\n"
"v_mfma_f32_16x16x32_bf16 v[136:139], v[72:75], v[40:43], v[136:139]\n"
"buffer_load_dwordx4 a[124:127], v1, s[4:7], s98 offen offset:3072 sc1\n"
"v_exp_f32_e32 v76, v76\n"
"v_add_u32_e32 v248, 0x26400, v0\n"
"v_exp_f32_e32 v77, v77\n"
"v_mfma_f32_16x16x32_bf16 v[80:83], v[140:143], v[40:43], v[80:83]\n"
"ds_read_b128 v[72:75], v248\n"
"v_exp_f32_e32 v78, v78\n"
"v_exp_f32_e32 v79, v79\n"
"v_mfma_f32_16x16x32_bf16 v[68:71], v[88:91], v[40:43], v[68:71]\n"
"ds_read_b128 v[128:131], v248 offset:1024\n"
"v_cvt_pk_bf16_f32 v40, v108, v109\n"
"v_cvt_pk_bf16_f32 v41, v110, v111\n"
"v_cvt_pk_bf16_f32 v42, v76, v77\n"
"v_cvt_pk_bf16_f32 v43, v78, v79\n"
"s_waitcnt lgkmcnt(6)\n"
"v_mfma_f32_16x16x32_bf16 v[132:135], v[88:91], v[124:127], v[132:135]\n"
"ds_read_b64_tr_b16 v[76:77], v16 offset:8192\n"
"v_cndmask_b32_e64 v84, v48, v84, s[20:21]\n"
"v_cndmask_b32_e64 v85, v49, v85, s[22:23]\n"
"v_cndmask_b32_e64 v86, v50, v86, s[24:25]\n"
"s_add_u32 m0, s77, 0x4000\n"
"s_add_u32 s98, s65, 0\n"
"buffer_load_dwordx4 v2, s[4:7], s98 offen sc1 lds\n"
"buffer_load_dwordx4 v2, s[4:7], s98 offen offset:1024 sc1 lds\n"
"v_cndmask_b32_e64 v87, v51, v87, s[26:27]\n"
"v_cndmask_b32_e64 v120, v24, v120, s[28:29]\n"
"v_mfma_f32_16x16x32_bf16 v[44:47], v[100:103], v[124:127], v[44:47]\n"
"ds_read_b64_tr_b16 v[78:79], v255 offset:8448\n"
"v_cndmask_b32_e64 v121, v25, v121, s[30:31]\n"
"v_cndmask_b32_e64 v122, v26, v122, s[32:33]\n"
"v_cndmask_b32_e64 v123, v27, v123, s[34:35]\n"
"v_exp_f32_e32 v84, v84\n"
"v_exp_f32_e32 v85, v85\n"
"v_exp_f32_e32 v86, v86\n"
"v_exp_f32_e32 v87, v87\n"
"v_mfma_f32_16x16x32_bf16 v[112:115], v[60:63], v[124:127], v[112:115]\n"
"v_exp_f32_e32 v120, v120\n"
"v_exp_f32_e32 v121, v121\n"
"v_exp_f32_e32 v122, v122\n"
"v_exp_f32_e32 v123, v123\n"
"v_cvt_pk_bf16_f32 v48, v84, v85\n"
"v_cvt_pk_bf16_f32 v49, v86, v87\n"
"v_cvt_pk_bf16_f32 v50, v120, v121\n"
"v_mfma_f32_16x16x32_bf16 v[136:139], v[60:63], v[40:43], v[136:139]\n"
"ds_read_b64_tr_b16 v[60:61], v255 offset:8192\n"
"s_add_u32 s98, s69, 0x608000\n"
"buffer_load_dwordx4 v[24:27], v0, s[4:7], s98 offen sc1\n"
"v_cvt_pk_bf16_f32 v51, v122, v123\n"
"v_cndmask_b32_e64 v116, v52, v116, s[36:37]\n"
"v_cndmask_b32_e64 v117, v53, v117, s[38:39]\n"
"v_cndmask_b32_e64 v118, v54, v118, s[40:41]\n"
"v_cndmask_b32_e64 v119, v55, v119, s[42:43]\n"
"v_cndmask_b32_e64 v104, v56, v104, s[44:45]\n"
"v_cndmask_b32_e64 v105, v57, v105, s[46:47]\n"
"v_mfma_f32_16x16x32_bf16 v[80:83], v[100:103], v[40:43], v[80:83]\n"
"ds_read_b64_tr_b16 v[62:63], v16 offset:8448\n"
"v_cndmask_b32_e64 v106, v58, v106, s[48:49]\n"
"v_cndmask_b32_e64 v107, v59, v107, s[50:51]\n"
"v_exp_f32_e32 v116, v116\n"
"v_exp_f32_e32 v117, v117\n"
"v_exp_f32_e32 v118, v118\n"
"v_exp_f32_e32 v119, v119\n"
"v_exp_f32_e32 v104, v104\n"
"v_mfma_f32_16x16x32_bf16 v[68:71], v[88:91], v[40:43], v[68:71]\n"
"s_waitcnt lgkmcnt(0)\n"
"v_exp_f32_e32 v105, v105\n"
"v_exp_f32_e32 v106, v106\n"
"v_exp_f32_e32 v107, v107\n"
"v_cvt_pk_bf16_f32 v40, v116, v117\n"
"v_cvt_pk_bf16_f32 v41, v118, v119\n"
"v_cvt_pk_bf16_f32 v42, v104, v105\n"
"v_cvt_pk_bf16_f32 v43, v106, v107\n"
"v_mfma_f32_16x16x32_bf16 v[132:135], v[88:91], v[48:51], v[132:135]\n"
"v_and_b32_e32 v72, v48, v72\n"
"v_and_b32_e32 v73, v49, v73\n"
"v_and_b32_e32 v74, v50, v74\n"
"v_and_b32_e32 v75, v51, v75\n"
"v_mfma_f32_16x16x32_bf16 v[68:71], v[88:91], v[40:43], v[68:71]\n"
"v_xor_b32_e32 v52, v48, v72\n"
"v_xor_b32_e32 v53, v49, v73\n"
"v_xor_b32_e32 v54, v50, v74\n"
"v_xor_b32_e32 v55, v51, v75\n"
"v_mfma_f32_16x16x32_bf16 v[44:47], v[76:79], v[72:75], v[44:47]\n"
"v_and_b32_e32 v128, v40, v128\n"
"v_and_b32_e32 v129, v41, v129\n"
"v_mfma_f32_16x16x32_bf16 v[112:115], v[60:63], v[72:75], v[112:115]\n"
"v_and_b32_e32 v130, v42, v130\n"
"v_and_b32_e32 v131, v43, v131\n"
"v_mfma_f32_16x16x32_bf16 v[44:47], v[36:39], v[52:55], v[44:47]\n"
"v_xor_b32_e32 v48, v40, v128\n"
"v_xor_b32_e32 v49, v41, v129\n"
"v_mfma_f32_16x16x32_bf16 v[112:115], v[64:67], v[52:55], v[112:115]\n"
"v_xor_b32_e32 v50, v42, v130\n"
"v_xor_b32_e32 v51, v43, v131\n"
"v_mfma_f32_16x16x32_bf16 v[80:83], v[76:79], v[128:131], v[80:83]\n"
"s_add_u32 m0, s77, 0xc000\n"
"s_add_u32 s97, s65, 0x4000\n"
"buffer_load_dwordx4 v2, s[4:7], s97 offen sc1 lds\n"
"buffer_load_dwordx4 v2, s[4:7], s97 offen offset:1024 sc1 lds\n"
"v_mfma_f32_16x16x32_bf16 v[136:139], v[60:63], v[128:131], v[136:139]\n"
"v_mfma_f32_16x16x32_bf16 v[80:83], v[36:39], v[48:51], v[80:83]\n"
"v_mfma_f32_16x16x32_bf16 v[136:139], v[64:67], v[48:51], v[136:139]\n"
"v_max_f32_e32 v248, v132, v68\n"
"v_rcp_f32_e32 v132, v132\n"
"v_rcp_f32_e32 v68, v68\n"
"s_nop 0\n"
"v_max3_f32 v248, v248, v132, v68\n"
"v_cmp_nlt_f32_e32 vcc, v248, v252\n"
"s_cbranch_vccnz .Lmk_b92\n"
".Lmk_k98:\n"
"s_add_u32 s97, s60, 0x40000\n"
"v_mul_f32_e32 v44, v44, v132\n"
"v_mul_f32_e32 v45, v45, v132\n"
"v_mul_f32_e32 v46, v46, v132\n"
"v_mul_f32_e32 v47, v47, v132\n"
"v_mul_f32_e32 v112, v112, v132\n"
"v_mul_f32_e32 v113, v113, v132\n"
"v_mul_f32_e32 v114, v114, v132\n"
"v_mul_f32_e32 v115, v115, v132\n"
"v_cvt_pk_bf16_f32 v28, v44, v45\n"
"v_cvt_pk_bf16_f32 v29, v46, v47\n"
"v_cvt_pk_bf16_f32 v30, v112, v113\n"
"v_cvt_pk_bf16_f32 v31, v114, v115\n"
"buffer_store_dwordx4 v[28:31], v0, s[4:7], s97 offen sc1\n"
"s_add_u32 s97, s60, 0x48000\n"
"v_mul_f32_e32 v80, v80, v68\n"
"v_mul_f32_e32 v81, v81, v68\n"
"v_mul_f32_e32 v82, v82, v68\n"
"v_mul_f32_e32 v83, v83, v68\n"
"v_mul_f32_e32 v136, v136, v68\n"
"v_mul_f32_e32 v137, v137, v68\n"
"v_mul_f32_e32 v138, v138, v68\n"
"v_mul_f32_e32 v139, v139, v68\n"
"v_cvt_pk_bf16_f32 v28, v80, v81\n"
"v_cvt_pk_bf16_f32 v29, v82, v83\n"
"v_cvt_pk_bf16_f32 v30, v136, v137\n"
"v_cvt_pk_bf16_f32 v31, v138, v139\n"
"buffer_store_dwordx4 v[28:31], v0, s[4:7], s97 offen sc1\n"
";\n"
"; @A AT a\n"
"s_mov_b32 s2, 0\n"
"v_mov_b32_e32 v253, 0xff800000\n"
"v_mov_b32_e32 v251, 0xff800000\n"
"v_mov_b32_e32 v250, 0x7f800000\n"
"v_mov_b32_e32 v249, 0x7f800000\n"
".Lmk_a99:\n"
"ds_read_b128 v[28:31], v15 offset:16384\n"
"ds_read_b128 v[32:35], v254 offset:16640\n"
"ds_read_b128 v[36:39], v15 offset:14336\n"
"ds_read_b128 v[40:43], v254 offset:14592\n"
"s_cmp_gt_i32 s78, 7\n"
"s_cselect_b32 s97, 0x80000000, 0\n"
"s_or_b32 s97, s97, 0x7f800000\n"
"s_add_u32 s96, s67, 32\n"
"buffer_load_dwordx2 v[246:247], v12, s[4:7], s96 offen sc1\n"
"s_waitcnt lgkmcnt(2)\n"
"v_mfma_f32_16x16x32_bf16 v[100:103], v[28:31], v[92:95], 0\n"
"v_mfma_f32_16x16x32_bf16 v[104:107], v[28:31], v[96:99], 0\n"
"v_mfma_f32_16x16x32_bf16 v[108:111], v[32:35], v[96:99], 0\n"
"v_mfma_f32_16x16x32_bf16 v[112:115], v[32:35], v[92:95], 0\n"
"s_waitcnt lgkmcnt(0)\n"
"s_nop 3\n"
"v_max_f32_e32 v248, v100, v101\n"
"v_max_f32_e32 v245, v104, v105\n"
"v_max3_f32 v248, v248, v102, v103\n"
"v_max3_f32 v245, v245, v106, v107\n"
"v_max3_f32 v248, v248, v112, v113\n"
"v_max3_f32 v245, v245, v108, v109\n"
"v_max3_f32 v248, v248, v114, v115\n"
"v_max3_f32 v245, v245, v110, v111\n"
"v_mov_b32_e32 v244, v248\n"
"v_mov_b32_e32 v243, v245\n"
"s_nop 0\n"
"v_permlane16_swap_b32_e32 v248, v244\n"
"v_permlane16_swap_b32_e32 v245, v243\n"
"v_max_f32_e32 v248, v248, v244\n"
"v_max_f32_e32 v245, v245, v243\n"
"v_mov_b32_e32 v244, v248\n"
"v_mov_b32_e32 v243, v245\n"
"s_nop 0\n"
"v_permlane32_swap_b32_e32 v248, v244\n"
"v_permlane32_swap_b32_e32 v245, v243\n"
"v_max_f32_e32 v248, v248, v244\n"
"v_max_f32_e32 v245, v245, v243\n"
"v_med3_f32 v248, v248, v253, v250\n"
"v_med3_f32 v245, v245, v251, v249\n"
"v_min_f32_e64 v116, s97, -v248\n"
"v_min_f32_e64 v117, s97, -v248\n"
"v_min_f32_e64 v118, s97, -v248\n"
"v_min_f32_e64 v119, s97, -v248\n"
"v_min_f32_e64 v120, s97, -v245\n"
"v_min_f32_e64 v121, s97, -v245\n"
"v_min_f32_e64 v122, s97, -v245\n"
"v_min_f32_e64 v123, s97, -v245\n"
"v_mfma_f32_16x16x32_bf16 v[44:47], v[36:39], v[92:95], v[116:119]\n"
"ds_read_b128 v[32:35], v15 offset:12288\n"
"v_sub_f32_e32 v100, v100, v248\n"
"v_sub_f32_e32 v101, v101, v248\n"
"v_sub_f32_e32 v102, v102, v248\n"
"v_sub_f32_e32 v103, v103, v248\n"
"v_mfma_f32_16x16x32_bf16 v[48:51], v[36:39], v[96:99], v[120:123]\n"
"v_sub_f32_e32 v112, v112, v248\n"
"v_sub_f32_e32 v113, v113, v248\n"
"v_sub_f32_e32 v114, v114, v248\n"
"v_sub_f32_e32 v115, v115, v248\n"
"v_mfma_f32_16x16x32_bf16 v[52:55], v[40:43], v[96:99], v[120:123]\n"
"ds_read_b128 v[56:59], v254 offset:12544\n"
"v_sub_f32_e32 v104, v104, v245\n"
"v_sub_f32_e32 v105, v105, v245\n"
"v_sub_f32_e32 v106, v106, v245\n"
"v_sub_f32_e32 v107, v107, v245\n"
"v_mfma_f32_16x16x32_bf16 v[36:39], v[40:43], v[92:95], v[116:119]\n"
"s_waitcnt lgkmcnt(0)\n"
"v_sub_f32_e32 v108, v108, v245\n"
"v_sub_f32_e32 v109, v109, v245\n"
"v_sub_f32_e32 v110, v110, v245\n"
"v_sub_f32_e32 v111, v111, v245\n"
"s_cmp_eq_i32 s78, 7\n"
"s_cbranch_scc1 .Lmk_x101s\n"
".Lmk_x101r:\n"
"v_mfma_f32_16x16x32_bf16 v[60:63], v[32:35], v[92:95], v[116:119]\n"
"ds_read_b128 v[40:43], v15 offset:10240\n"
"buffer_load_dwordx4 v[28:31], v0, s[4:7], s98 offen offset:1024 sc1\n"
"ds_read_b128 v[64:67], v254 offset:10496\n"
"v_exp_f32_e32 v44, v44\n"
"v_exp_f32_e32 v45, v45\n"
"v_exp_f32_e32 v46, v46\n"
"v_exp_f32_e32 v47, v47\n"
"v_exp_f32_e32 v36, v36\n"
"v_mfma_f32_16x16x32_bf16 v[68:71], v[32:35], v[96:99], v[120:123]\n"
"ds_read_b64_tr_b16 v[72:73], v16 offset:14336\n"
"buffer_load_dwordx4 v[32:35], v0, s[4:7], s98 offen offset:2048 sc1\n"
"v_exp_f32_e32 v37, v37\n"
"v_exp_f32_e32 v38, v38\n"
"v_exp_f32_e32 v39, v39\n"
"v_cvt_pk_bf16_f32 v76, v44, v45\n"
"v_cvt_pk_bf16_f32 v77, v46, v47\n"
"v_cvt_pk_bf16_f32 v78, v36, v37\n"
"v_mfma_f32_16x16x32_bf16 v[80:83], v[56:59], v[96:99], v[120:123]\n"
"ds_read_b64_tr_b16 v[74:75], v255 offset:14592\n"
"ds_read_b64_tr_b16 v[84:85], v255 offset:14336\n"
"v_cvt_pk_bf16_f32 v79, v38, v39\n"
"v_exp_f32_e32 v48, v48\n"
"v_exp_f32_e32 v49, v49\n"
"v_exp_f32_e32 v50, v50\n"
"v_exp_f32_e32 v51, v51\n"
"v_mfma_f32_16x16x32_bf16 v[124:127], v[56:59], v[92:95], v[116:119]\n"
"buffer_load_dwordx4 v[36:39], v0, s[4:7], s98 offen offset:3072 sc1\n"
"ds_read_b64_tr_b16 v[86:87], v16 offset:14592\n"
"v_exp_f32_e32 v52, v52\n"
"v_exp_f32_e32 v53, v53\n"
"v_exp_f32_e32 v54, v54\n"
"v_exp_f32_e32 v55, v55\n"
"v_cvt_pk_bf16_f32 v56, v48, v49\n"
"v_cvt_pk_bf16_f32 v57, v50, v51\n"
"v_cvt_pk_bf16_f32 v58, v52, v53\n"
"v_cvt_pk_bf16_f32 v59, v54, v55\n"
"s_waitcnt lgkmcnt(4)\n"
"s_cmp_eq_i32 s78, 6\n"
"s_cbranch_scc1 .Lmk_x102s\n"
".Lmk_x102r:\n"
"v_mfma_f32_16x16x32_bf16 v[128:131], v[40:43], v[92:95], v[116:119]\n"
"ds_read_b128 v[132:135], v15 offset:8192\n"
"ds_read_b128 v[136:139], v254 offset:8448\n"
"v_exp_f32_e32 v60, v60\n"
"v_exp_f32_e32 v61, v61\n"
"v_mfma_f32_16x16x32_bf16 v[140:143], v[40:43], v[96:99], v[120:123]\n"
"buffer_load_dwordx4 v[40:43], v1, s[4:7], s98 offen sc1\n"
"v_exp_f32_e32 v62, v62\n"
"v_exp_f32_e32 v63, v63\n"
"v_mfma_f32_16x16x32_bf16 v[144:147], v[64:67], v[96:99], v[120:123]\n"
"ds_read_b64_tr_b16 v[148:149], v16 offset:12288\n"
"ds_read_b64_tr_b16 v[150:151], v255 offset:12544\n"
"v_exp_f32_e32 v124, v124\n"
"v_exp_f32_e32 v125, v125\n"
"v_mfma_f32_16x16x32_bf16 v[152:155], v[64:67], v[92:95], v[116:119]\n"
"ds_read_b64_tr_b16 v[64:65], v255 offset:12288\n"
"v_exp_f32_e32 v126, v126\n"
"v_exp_f32_e32 v127, v127\n"
"s_waitcnt lgkmcnt(5)\n"
"v_mfma_f32_16x16x32_bf16 v[156:159], v[88:91], v[76:79], 0\n"
"buffer_load_dwordx4 v[44:47], v1, s[4:7], s98 offen offset:1024 sc1\n"
"v_cvt_pk_bf16_f32 v160, v60, v61\n"
"v_cvt_pk_bf16_f32 v161, v62, v63\n"
"v_cvt_pk_bf16_f32 v162, v124, v125\n"
"v_cvt_pk_bf16_f32 v163, v126, v127\n"
"v_mfma_f32_16x16x32_bf16 v[124:127], v[72:75], v[76:79], 0\n"
"ds_read_b64_tr_b16 v[66:67], v16 offset:12544\n"
"s_waitcnt vmcnt(8)\n"
"ds_read_b128 v[164:167], v2 offset:16384\n"
"v_exp_f32_e32 v68, v68\n"
"v_exp_f32_e32 v69, v69\n"
"v_mfma_f32_16x16x32_bf16 v[168:171], v[84:87], v[76:79], 0\n"
"v_exp_f32_e32 v70, v70\n"
"v_exp_f32_e32 v71, v71\n"
"v_mfma_f32_16x16x32_bf16 v[172:175], v[84:87], v[56:59], 0\n"
"buffer_load_dwordx4 v[48:51], v1, s[4:7], s98 offen offset:2048 sc1\n"
"ds_read_b128 v[76:79], v2 offset:17408\n"
"v_exp_f32_e32 v80, v80\n"
"v_exp_f32_e32 v81, v81\n"
"v_mfma_f32_16x16x32_bf16 v[176:179], v[72:75], v[56:59], 0\n"
"ds_read_b128 v[72:75], v2 offset:49152\n"
"v_exp_f32_e32 v82, v82\n"
"v_exp_f32_e32 v83, v83\n"
"v_mfma_f32_16x16x32_bf16 v[180:183], v[88:91], v[56:59], 0\n"
"ds_read_b128 v[84:87], v2 offset:50176\n"
"v_cvt_pk_bf16_f32 v184, v68, v69\n"
"v_cvt_pk_bf16_f32 v185, v70, v71\n"
"v_cvt_pk_bf16_f32 v186, v80, v81\n"
"v_cvt_pk_bf16_f32 v187, v82, v83\n"
"s_waitcnt lgkmcnt(8)\n"
"s_cmp_eq_i32 s78, 5\n"
"s_cbranch_scc1 .Lmk_x103s\n"
".Lmk_x103r:\n"
"v_mfma_f32_16x16x32_bf16 v[80:83], v[132:135], v[92:95], v[116:119]\n"
"ds_read_b64_tr_b16 v[68:69], v16 offset:10240\n"
"ds_read_b64_tr_b16 v[70:71], v255 offset:10496\n"
"buffer_load_dwordx4 v[52:55], v1, s[4:7], s98 offen offset:3072 sc1\n"
"v_exp_f32_e32 v128, v128\n"
"v_exp_f32_e32 v129, v129\n"
"v_mfma_f32_16x16x32_bf16 v[188:191], v[132:135], v[96:99], v[120:123]\n"
"ds_read_b64_tr_b16 v[132:133], v255 offset:10240\n"
"v_exp_f32_e32 v130, v130\n"
"v_exp_f32_e32 v131, v131\n"
"v_mfma_f32_16x16x32_bf16 v[192:195], v[136:139], v[96:99], v[120:123]\n"
"ds_read_b64_tr_b16 v[134:135], v16 offset:10496\n"
"v_exp_f32_e32 v152, v152\n"
"v_exp_f32_e32 v153, v153\n"
"v_exp_f32_e32 v154, v154\n"
"v_mfma_f32_16x16x32_bf16 v[196:199], v[136:139], v[92:95], v[116:119]\n"
"s_waitcnt lgkmcnt(4)\n"
"s_add_u32 s97, s69, 0x60a000\n"
"buffer_load_dwordx4 v[56:59], v0, s[4:7], s97 offen sc1\n"
"v_max_u32_e32 v248, v164, v165\n"
"v_exp_f32_e32 v155, v155\n"
"v_mfma_f32_16x16x32_bf16 v[156:159], v[88:91], v[160:163], v[156:159]\n"
"v_max3_u32 v248, v248, v166, v167\n"
"v_cvt_pk_bf16_f32 v136, v128, v129\n"
"v_cvt_pk_bf16_f32 v137, v130, v131\n"
"v_cvt_pk_bf16_f32 v138, v152, v153\n"
"v_mfma_f32_16x16x32_bf16 v[124:127], v[148:151], v[160:163], v[124:127]\n"
"v_max3_u32 v248, v248, v76, v77\n"
"v_cvt_pk_bf16_f32 v139, v154, v155\n"
"v_exp_f32_e32 v140, v140\n"
"v_max3_u32 v248, v248, v78, v79\n"
"v_exp_f32_e32 v141, v141\n"
"v_mfma_f32_16x16x32_bf16 v[168:171], v[64:67], v[160:163], v[168:171]\n"
"v_exp_f32_e32 v142, v142\n"
"v_max3_u32 v248, v248, v72, v73\n"
"v_exp_f32_e32 v143, v143\n"
"v_mfma_f32_16x16x32_bf16 v[172:175], v[64:67], v[184:187], v[172:175]\n"
"buffer_load_dwordx4 v[60:63], v0, s[4:7], s97 offen offset:1024 sc1\n"
"v_max3_u32 v248, v248, v74, v75\n"
"v_exp_f32_e32 v144, v144\n"
"v_exp_f32_e32 v145, v145\n"
"v_mfma_f32_16x16x32_bf16 v[176:179], v[148:151], v[184:187], v[176:179]\n"
"v_max3_u32 v248, v248, v84, v85\n"
"v_exp_f32_e32 v146, v146\n"
"v_max3_u32 v248, v248, v86, v87\n"
"v_exp_f32_e32 v147, v147\n"
"v_mfma_f32_16x16x32_bf16 v[180:183], v[88:91], v[184:187], v[180:183]\n"
"v_cvt_pk_bf16_f32 v128, v140, v141\n"
"v_cmp_eq_u32_e32 vcc, -1, v248\n"
"v_cvt_pk_bf16_f32 v129, v142, v143\n"
"v_cvt_pk_bf16_f32 v130, v144, v145\n"
"v_cvt_pk_bf16_f32 v131, v146, v147\n"
"s_cbranch_vccnz .Lmk_h90s\n"
".Lmk_h90r:\n"
"s_cmp_lg_u32 s2, 0\n"
"s_cbranch_scc1 .Lmk_s104\n"
"s_barrier\n"
".Lmk_s104:\n"
"ds_read_b128 v[72:75], v15 offset:6144\n"
"ds_read_b128 v[76:79], v254 offset:6400\n"
"s_waitcnt lgkmcnt(2)\n"
"v_mfma_f32_16x16x32_bf16 v[156:159], v[88:91], v[136:139], v[156:159]\n"
"ds_read_b128 v[84:87], v15 offset:4096\n"
"v_exp_f32_e32 v80, v80\n"
"v_exp_f32_e32 v81, v81\n"
"v_exp_f32_e32 v82, v82\n"
"v_mfma_f32_16x16x32_bf16 v[124:127], v[68:71], v[136:139], v[124:127]\n"
"buffer_load_dwordx4 v[64:67], v0, s[4:7], s97 offen offset:2048 sc1\n"
"ds_read_b128 v[140:143], v254 offset:4352\n"
"v_exp_f32_e32 v83, v83\n"
"v_mfma_f32_16x16x32_bf16 v[168:171], v[132:135], v[136:139], v[168:171]\n"
"ds_read_b64_tr_b16 v[136:137], v16 offset:8192\n"
"v_exp_f32_e32 v196, v196\n"
"v_exp_f32_e32 v197, v197\n"
"v_mfma_f32_16x16x32_bf16 v[172:175], v[132:135], v[128:131], v[172:175]\n"
"ds_read_b64_tr_b16 v[138:139], v255 offset:8448\n"
"v_exp_f32_e32 v198, v198\n"
"v_exp_f32_e32 v199, v199\n"
"v_mfma_f32_16x16x32_bf16 v[176:179], v[68:71], v[128:131], v[176:179]\n"
"buffer_load_dwordx4 v[68:71], v0, s[4:7], s97 offen offset:3072 sc1\n"
"v_cvt_pk_bf16_f32 v132, v80, v81\n"
"v_cvt_pk_bf16_f32 v133, v82, v83\n"
"v_cvt_pk_bf16_f32 v134, v196, v197\n"
"v_cvt_pk_bf16_f32 v135, v198, v199\n"
"v_mfma_f32_16x16x32_bf16 v[180:183], v[88:91], v[128:131], v[180:183]\n"
"ds_read_b64_tr_b16 v[128:129], v255 offset:8192\n"
"v_exp_f32_e32 v188, v188\n"
"v_exp_f32_e32 v189, v189\n"
"s_waitcnt lgkmcnt(5)\n"
"v_mfma_f32_16x16x32_bf16 v[144:147], v[72:75], v[92:95], v[116:119]\n"
"ds_read_b64_tr_b16 v[130:131], v16 offset:8448\n"
"v_exp_f32_e32 v190, v190\n"
"v_exp_f32_e32 v191, v191\n"
"v_exp_f32_e32 v192, v192\n"
"v_mfma_f32_16x16x32_bf16 v[148:151], v[72:75], v[96:99], v[120:123]\n"
"buffer_load_dwordx4 v[72:75], v1, s[4:7], s97 offen sc1\n"
"s_waitcnt vmcnt(12)\n"
"v_cmp_ne_u64_e32 vcc, v[246:247], v[18:19]\n"
"v_mfma_f32_16x16x32_bf16 v[152:155], v[76:79], v[96:99], v[120:123]\n"
"v_exp_f32_e32 v193, v193\n"
"v_exp_f32_e32 v194, v194\n"
"v_exp_f32_e32 v195, v195\n"
"v_mfma_f32_16x16x32_bf16 v[160:163], v[76:79], v[92:95], v[116:119]\n"
"s_cbranch_vccnz .Lmk_f74s\n"
".Lmk_f74r:\n"
"v_cvt_pk_bf16_f32 v164, v188, v189\n"
"v_cvt_pk_bf16_f32 v165, v190, v191\n"
"v_cvt_pk_bf16_f32 v166, v192, v193\n"
"v_cvt_pk_bf16_f32 v167, v194, v195\n"
"s_waitcnt lgkmcnt(4)\n"
"v_mfma_f32_16x16x32_bf16 v[184:187], v[84:87], v[92:95], v[116:119]\n"
"ds_read_b128 v[188:191], v15 offset:2048\n"
"v_exp_f32_e32 v144, v144\n"
"v_exp_f32_e32 v145, v145\n"
"v_exp_f32_e32 v146, v146\n"
"v_mfma_f32_16x16x32_bf16 v[192:195], v[84:87], v[96:99], v[120:123]\n"
"buffer_load_dwordx4 v[76:79], v1, s[4:7], s97 offen offset:1024 sc1\n"
"v_exp_f32_e32 v147, v147\n"
"v_mfma_f32_16x16x32_bf16 v[196:199], v[140:143], v[96:99], v[120:123]\n"
"ds_read_b128 v[200:203], v254 offset:2304\n"
"v_exp_f32_e32 v160, v160\n"
"v_exp_f32_e32 v161, v161\n"
"v_mfma_f32_16x16x32_bf16 v[204:207], v[140:143], v[92:95], v[116:119]\n"
"ds_read_b64_tr_b16 v[140:141], v16 offset:6144\n"
"v_exp_f32_e32 v162, v162\n"
"v_exp_f32_e32 v163, v163\n"
"s_waitcnt lgkmcnt(3)\n"
"v_mfma_f32_16x16x32_bf16 v[156:159], v[88:91], v[132:135], v[156:159]\n"
"buffer_load_dwordx4 v[80:83], v1, s[4:7], s97 offen offset:2048 sc1\n"
"v_cvt_pk_bf16_f32 v208, v144, v145\n"
"v_cvt_pk_bf16_f32 v209, v146, v147\n"
"v_cvt_pk_bf16_f32 v210, v160, v161\n"
"v_mfma_f32_16x16x32_bf16 v[124:127], v[136:139], v[132:135], v[124:127]\n"
"ds_read_b64_tr_b16 v[142:143], v255 offset:6400\n"
"v_cvt_pk_bf16_f32 v211, v162, v163\n"
"v_exp_f32_e32 v148, v148\n"
"v_exp_f32_e32 v149, v149\n"
"v_mfma_f32_16x16x32_bf16 v[168:171], v[128:131], v[132:135], v[168:171]\n"
"ds_read_b64_tr_b16 v[132:133], v255 offset:6144\n"
"v_exp_f32_e32 v150, v150\n"
"v_exp_f32_e32 v151, v151\n"
"v_mfma_f32_16x16x32_bf16 v[172:175], v[128:131], v[164:167], v[172:175]\n"
"buffer_load_dwordx4 v[84:87], v1, s[4:7], s97 offen offset:3072 sc1\n"
"v_exp_f32_e32 v152, v152\n"
"v_exp_f32_e32 v153, v153\n"
"v_mfma_f32_16x16x32_bf16 v[176:179], v[136:139], v[164:167], v[176:179]\n"
"ds_read_b64_tr_b16 v[134:135], v16 offset:6400\n"
"v_exp_f32_e32 v154, v154\n"
"v_exp_f32_e32 v155, v155\n"
"v_mfma_f32_16x16x32_bf16 v[180:183], v[88:91], v[164:167], v[180:183]\n"
"s_waitcnt lgkmcnt(4)\n"
"v_cvt_pk_bf16_f32 v128, v148, v149\n"
"v_cvt_pk_bf16_f32 v129, v150, v151\n"
"v_cvt_pk_bf16_f32 v130, v152, v153\n"
"v_cvt_pk_bf16_f32 v131, v154, v155\n"
"v_mfma_f32_16x16x32_bf16 v[136:139], v[188:191], v[92:95], v[116:119]\n"
"ds_read_b128 v[144:147], v15\n"
"v_exp_f32_e32 v184, v184\n"
"v_exp_f32_e32 v185, v185\n"
"v_exp_f32_e32 v186, v186\n"
"v_mfma_f32_16x16x32_bf16 v[148:151], v[188:191], v[96:99], v[120:123]\n"
"s_add_u32 s97, s56, 0x40000\n"
"buffer_load_dwordx4 v[152:155], v0, s[4:7], s97 offen sc1\n"
"v_exp_f32_e32 v187, v187\n"
"v_mfma_f32_16x16x32_bf16 v[160:163], v[200:203], v[96:99], v[120:123]\n"
"ds_read_b128 v[164:167], v254 offset:256\n"
"v_exp_f32_e32 v204, v204\n"
"v_exp_f32_e32 v205, v205\n"
"v_exp_f32_e32 v206, v206\n"
"v_mfma_f32_16x16x32_bf16 v[188:191], v[200:203], v[92:95], v[116:119]\n"
"buffer_load_dwordx4 v[200:203], v0, s[4:7], s97 offen offset:1024 sc1\n"
"ds_read_b64_tr_b16 v[212:213], v16 offset:4096\n"
"v_exp_f32_e32 v207, v207\n"
"s_waitcnt lgkmcnt(3)\n"
"v_mfma_f32_16x16x32_bf16 v[156:159], v[88:91], v[208:211], v[156:159]\n"
"v_cvt_pk_bf16_f32 v216, v184, v185\n"
"v_cvt_pk_bf16_f32 v217, v186, v187\n"
"v_cvt_pk_bf16_f32 v218, v204, v205\n"
"v_cvt_pk_bf16_f32 v219, v206, v207\n"
"v_mfma_f32_16x16x32_bf16 v[124:127], v[140:143], v[208:211], v[124:127]\n"
"buffer_load_dwordx4 v[184:187], v0, s[4:7], s97 offen offset:2048 sc1\n"
"ds_read_b64_tr_b16 v[214:215], v255 offset:4352\n"
"v_exp_f32_e32 v192, v192\n"
"v_exp_f32_e32 v193, v193\n"
"v_mfma_f32_16x16x32_bf16 v[168:171], v[132:135], v[208:211], v[168:171]\n"
"buffer_load_dwordx4 v[204:207], v0, s[4:7], s97 offen offset:3072 sc1\n"
"v_exp_f32_e32 v194, v194\n"
"v_exp_f32_e32 v195, v195\n"
"v_mfma_f32_16x16x32_bf16 v[172:175], v[132:135], v[128:131], v[172:175]\n"
"ds_read_b64_tr_b16 v[132:133], v255 offset:4096\n"
"v_exp_f32_e32 v196, v196\n"
"v_exp_f32_e32 v197, v197\n"
"v_mfma_f32_16x16x32_bf16 v[176:179], v[140:143], v[128:131], v[176:179]\n"
"buffer_load_dwordx4 v[140:143], v1, s[4:7], s97 offen sc1\n"
"ds_read_b64_tr_b16 v[134:135], v16 offset:4352\n"
"v_exp_f32_e32 v198, v198\n"
"v_exp_f32_e32 v199, v199\n"
"v_mfma_f32_16x16x32_bf16 v[180:183], v[88:91], v[128:131], v[180:183]\n"
"s_waitcnt lgkmcnt(4)\n"
"v_cvt_pk_bf16_f32 v128, v192, v193\n"
"v_cvt_pk_bf16_f32 v129, v194, v195\n"
"v_cvt_pk_bf16_f32 v130, v196, v197\n"
"v_cvt_pk_bf16_f32 v131, v198, v199\n"
"v_mfma_f32_16x16x32_bf16 v[192:195], v[144:147], v[92:95], v[116:119]\n"
"ds_read_b64_tr_b16 v[196:197], v16 offset:2048\n"
"ds_read_b64_tr_b16 v[198:199], v255 offset:2304\n"
"buffer_load_dwordx4 v[208:211], v1, s[4:7], s97 offen offset:1024 sc1\n"
"v_exp_f32_e32 v136, v136\n"
"v_exp_f32_e32 v137, v137\n"
"v_mfma_f32_16x16x32_bf16 v[220:223], v[144:147], v[96:99], v[120:123]\n"
"ds_read_b64_tr_b16 v[144:145], v255 offset:2048\n"
"v_exp_f32_e32 v138, v138\n"
"v_exp_f32_e32 v139, v139\n"
"v_mfma_f32_16x16x32_bf16 v[224:227], v[164:167], v[96:99], v[120:123]\n"
"buffer_load_dwordx4 v[120:123], v1, s[4:7], s97 offen offset:2048 sc1\n"
"v_exp_f32_e32 v188, v188\n"
"v_exp_f32_e32 v189, v189\n"
"v_mfma_f32_16x16x32_bf16 v[228:231], v[164:167], v[92:95], v[116:119]\n"
"ds_read_b64_tr_b16 v[146:147], v16 offset:2304\n"
"ds_read_b64_tr_b16 v[116:117], v16 offset:16384\n"
"v_exp_f32_e32 v190, v190\n"
"v_exp_f32_e32 v191, v191\n"
"s_waitcnt lgkmcnt(5)\n"
"v_mfma_f32_16x16x32_bf16 v[156:159], v[88:91], v[216:219], v[156:159]\n"
"v_cvt_pk_bf16_f32 v164, v136, v137\n"
"v_cvt_pk_bf16_f32 v165, v138, v139\n"
"v_cvt_pk_bf16_f32 v166, v188, v189\n"
"v_mfma_f32_16x16x32_bf16 v[124:127], v[212:215], v[216:219], v[124:127]\n"
"buffer_load_dwordx4 v[136:139], v1, s[4:7], s97 offen offset:3072 sc1\n"
"ds_read_b64_tr_b16 v[118:119], v255 offset:16640\n"
"ds_read_b64_tr_b16 v[188:189], v255 offset:16384\n"
"v_cvt_pk_bf16_f32 v167, v190, v191\n"
"v_exp_f32_e32 v148, v148\n"
"v_exp_f32_e32 v149, v149\n"
"v_exp_f32_e32 v150, v150\n"
"v_mfma_f32_16x16x32_bf16 v[168:171], v[132:135], v[216:219], v[168:171]\n"
"s_add_u32 s97, s57, 0x40000\n"
"buffer_load_dwordx4 v[216:219], v0, s[4:7], s97 offen sc1\n"
"ds_read_b64_tr_b16 v[190:191], v16 offset:16640\n"
"v_exp_f32_e32 v151, v151\n"
"v_mfma_f32_16x16x32_bf16 v[172:175], v[132:135], v[128:131], v[172:175]\n"
"v_exp_f32_e32 v160, v160\n"
"v_add_u32_e32 v248, 0x26400, v0\n"
"v_exp_f32_e32 v161, v161\n"
"v_mfma_f32_16x16x32_bf16 v[176:179], v[212:215], v[128:131], v[176:179]\n"
"buffer_load_dwordx4 v[132:135], v0, s[4:7], s97 offen offset:1024 sc1\n"
"ds_read_b128 v[212:215], v248\n"
"v_exp_f32_e32 v162, v162\n"
"v_exp_f32_e32 v163, v163\n"
"v_mfma_f32_16x16x32_bf16 v[180:183], v[88:91], v[128:131], v[180:183]\n"
"ds_read_b128 v[232:235], v248 offset:1024\n"
"v_cvt_pk_bf16_f32 v128, v148, v149\n"
"v_cvt_pk_bf16_f32 v129, v150, v151\n"
"v_cvt_pk_bf16_f32 v130, v160, v161\n"
"v_cvt_pk_bf16_f32 v131, v162, v163\n"
"s_waitcnt lgkmcnt(6)\n"
"v_mfma_f32_16x16x32_bf16 v[156:159], v[88:91], v[164:167], v[156:159]\n"
"ds_read_b64_tr_b16 v[148:149], v16\n"
"buffer_load_dwordx4 v[160:163], v0, s[4:7], s97 offen offset:2048 sc1\n"
"v_cndmask_b32_e64 v192, v100, v192, s[20:21]\n"
"v_cndmask_b32_e64 v193, v101, v193, s[22:23]\n"
"v_cndmask_b32_e64 v194, v102, v194, s[24:25]\n"
"v_cndmask_b32_e64 v195, v103, v195, s[26:27]\n"
"v_cndmask_b32_e64 v228, v112, v228, s[28:29]\n"
"v_cndmask_b32_e64 v229, v113, v229, s[30:31]\n"
"v_cndmask_b32_e64 v230, v114, v230, s[32:33]\n"
"v_cndmask_b32_e64 v231, v115, v231, s[34:35]\n"
"v_exp_f32_e32 v192, v192\n"
"v_mfma_f32_16x16x32_bf16 v[124:127], v[196:199], v[164:167], v[124:127]\n"
"ds_read_b64_tr_b16 v[150:151], v255 offset:256\n"
"buffer_load_dwordx4 v[100:103], v0, s[4:7], s97 offen offset:3072 sc1\n"
"v_exp_f32_e32 v193, v193\n"
"v_exp_f32_e32 v194, v194\n"
"v_exp_f32_e32 v195, v195\n"
"v_exp_f32_e32 v228, v228\n"
"v_mfma_f32_16x16x32_bf16 v[168:171], v[144:147], v[164:167], v[168:171]\n"
"buffer_load_dwordx4 v[164:167], v1, s[4:7], s97 offen sc1\n"
"v_exp_f32_e32 v229, v229\n"
"v_exp_f32_e32 v230, v230\n"
"v_exp_f32_e32 v231, v231\n"
"v_cvt_pk_bf16_f32 v112, v192, v193\n"
"v_cvt_pk_bf16_f32 v113, v194, v195\n"
"v_cvt_pk_bf16_f32 v114, v228, v229\n"
"v_mfma_f32_16x16x32_bf16 v[172:175], v[144:147], v[128:131], v[172:175]\n"
"ds_read_b64_tr_b16 v[144:145], v255\n"
"v_cvt_pk_bf16_f32 v115, v230, v231\n"
"v_cndmask_b32_e64 v220, v104, v220, s[36:37]\n"
"v_cndmask_b32_e64 v221, v105, v221, s[38:39]\n"
"v_cndmask_b32_e64 v222, v106, v222, s[40:41]\n"
"v_cndmask_b32_e64 v223, v107, v223, s[42:43]\n"
"v_cndmask_b32_e64 v224, v108, v224, s[44:45]\n"
"v_cndmask_b32_e64 v225, v109, v225, s[46:47]\n"
"v_cndmask_b32_e64 v226, v110, v226, s[48:49]\n"
"v_cndmask_b32_e64 v227, v111, v227, s[50:51]\n"
"v_mfma_f32_16x16x32_bf16 v[176:179], v[196:199], v[128:131], v[176:179]\n"
"buffer_load_dwordx4 v[104:107], v1, s[4:7], s97 offen offset:1024 sc1\n"
"ds_read_b64_tr_b16 v[146:147], v16 offset:256\n"
"buffer_load_dwordx4 v[108:111], v1, s[4:7], s97 offen offset:2048 sc1\n"
"v_exp_f32_e32 v220, v220\n"
"v_exp_f32_e32 v221, v221\n"
"v_exp_f32_e32 v222, v222\n"
"v_exp_f32_e32 v223, v223\n"
"v_exp_f32_e32 v224, v224\n"
"v_mfma_f32_16x16x32_bf16 v[180:183], v[88:91], v[128:131], v[180:183]\n"
"s_waitcnt lgkmcnt(0)\n"
"v_exp_f32_e32 v225, v225\n"
"v_exp_f32_e32 v226, v226\n"
"v_exp_f32_e32 v227, v227\n"
"v_cvt_pk_bf16_f32 v128, v220, v221\n"
"v_cvt_pk_bf16_f32 v129, v222, v223\n"
"v_cvt_pk_bf16_f32 v130, v224, v225\n"
"v_cvt_pk_bf16_f32 v131, v226, v227\n"
"v_mfma_f32_16x16x32_bf16 v[156:159], v[88:91], v[112:115], v[156:159]\n"
"v_and_b32_e32 v212, v112, v212\n"
"v_and_b32_e32 v213, v113, v213\n"
"v_and_b32_e32 v214, v114, v214\n"
"v_and_b32_e32 v215, v115, v215\n"
"v_mfma_f32_16x16x32_bf16 v[180:183], v[88:91], v[128:131], v[180:183]\n"
"v_xor_b32_e32 v192, v112, v212\n"
"v_xor_b32_e32 v193, v113, v213\n"
"v_xor_b32_e32 v194, v114, v214\n"
"v_xor_b32_e32 v195, v115, v215\n"
"v_mfma_f32_16x16x32_bf16 v[124:127], v[148:151], v[212:215], v[124:127]\n"
"v_and_b32_e32 v232, v128, v232\n"
"v_and_b32_e32 v233, v129, v233\n"
"v_mfma_f32_16x16x32_bf16 v[168:171], v[144:147], v[212:215], v[168:171]\n"
"v_and_b32_e32 v234, v130, v234\n"
"v_and_b32_e32 v235, v131, v235\n"
"v_mfma_f32_16x16x32_bf16 v[124:127], v[116:119], v[192:195], v[124:127]\n"
"v_xor_b32_e32 v112, v128, v232\n"
"v_xor_b32_e32 v113, v129, v233\n"
"v_mfma_f32_16x16x32_bf16 v[168:171], v[188:191], v[192:195], v[168:171]\n"
"v_xor_b32_e32 v114, v130, v234\n"
"v_xor_b32_e32 v115, v131, v235\n"
"v_mfma_f32_16x16x32_bf16 v[176:179], v[148:151], v[232:235], v[176:179]\n"
"buffer_load_dwordx4 v[128:131], v1, s[4:7], s97 offen offset:3072 sc1\n"
"s_add_u32 s97, s58, 0x40000\n"
"buffer_load_dwordx4 v[148:151], v0, s[4:7], s97 offen sc1\n"
"v_mfma_f32_16x16x32_bf16 v[172:175], v[144:147], v[232:235], v[172:175]\n"
"buffer_load_dwordx4 v[144:147], v0, s[4:7], s97 offen offset:1024 sc1\n"
"v_mfma_f32_16x16x32_bf16 v[176:179], v[116:119], v[112:115], v[176:179]\n"
"buffer_load_dwordx4 v[116:119], v0, s[4:7], s97 offen offset:2048 sc1\n"
"v_mfma_f32_16x16x32_bf16 v[172:175], v[188:191], v[112:115], v[172:175]\n"
"buffer_load_dwordx4 v[112:115], v0, s[4:7], s97 offen offset:3072 sc1\n"
"v_max_f32_e32 v248, v156, v180\n"
"v_rcp_f32_e32 v156, v156\n"
"v_rcp_f32_e32 v180, v180\n"
"s_nop 0\n"
"v_max3_f32 v248, v248, v156, v180\n"
"v_cmp_nlt_f32_e32 vcc, v248, v252\n"
"s_cbranch_vccnz .Lmk_b100\n"
".Lmk_k105:\n"
"v_mul_f32_e32 v124, v124, v156\n"
"v_mul_f32_e32 v125, v125, v156\n"
"v_mul_f32_e32 v126, v126, v156\n"
"v_mul_f32_e32 v127, v127, v156\n"
"v_mul_f32_e32 v168, v168, v156\n"
"v_mul_f32_e32 v169, v169, v156\n"
"v_mul_f32_e32 v170, v170, v156\n"
"v_mul_f32_e32 v171, v171, v156\n"
"v_cvt_pk_bf16_f32 v88, v124, v125\n"
"v_cvt_pk_bf16_f32 v89, v126, v127\n"
"v_cvt_pk_bf16_f32 v90, v168, v169\n"
"v_cvt_pk_bf16_f32 v91, v170, v171\n"
"buffer_store_dwordx4 v[88:91], v0, s[4:7], s60 offen sc1\n"
"s_add_u32 s98, s60, 0x8000\n"
"v_mul_f32_e32 v176, v176, v180\n"
"v_mul_f32_e32 v177, v177, v180\n"
"v_mul_f32_e32 v178, v178, v180\n"
"v_mul_f32_e32 v179, v179, v180\n"
"v_mul_f32_e32 v172, v172, v180\n"
"v_mul_f32_e32 v173, v173, v180\n"
"v_mul_f32_e32 v174, v174, v180\n"
"v_mul_f32_e32 v175, v175, v180\n"
"v_cvt_pk_bf16_f32 v88, v176, v177\n"
"v_cvt_pk_bf16_f32 v89, v178, v179\n"
"v_cvt_pk_bf16_f32 v90, v172, v173\n"
"v_cvt_pk_bf16_f32 v91, v174, v175\n"
"buffer_store_dwordx4 v[88:91], v0, s[4:7], s98 offen sc1\n"
";\n"
"s_waitcnt vmcnt(20)\n"
"; @A G2 b.0\n"
"s_nop 0\n"
"v_mfma_f32_16x16x32_bf16 v[88:91], a[128:131], v[152:155], 0\n"
"v_mfma_f32_16x16x32_bf16 v[88:91], a[132:135], v[200:203], v[88:91]\n"
"s_waitcnt vmcnt(18)\n"
"v_mfma_f32_16x16x32_bf16 v[88:91], a[136:139], v[184:187], v[88:91]\n"
"buffer_load_dwordx4 v[92:95], v1, s[4:7], s97 offen sc1\n"
"v_mfma_f32_16x16x32_bf16 v[88:91], a[140:143], v[204:207], v[88:91]\n"
"s_waitcnt vmcnt(17)\n"
"v_mfma_f32_16x16x32_bf16 v[88:91], a[144:147], v[140:143], v[88:91]\n"
"v_mfma_f32_16x16x32_bf16 v[88:91], a[148:151], v[208:211], v[88:91]\n"
"s_waitcnt vmcnt(15)\n"
"v_mfma_f32_16x16x32_bf16 v[88:91], a[152:155], v[120:123], v[88:91]\n"
"buffer_load_dwordx4 v[96:99], v1, s[4:7], s97 offen offset:1024 sc1\n"
"v_mfma_f32_16x16x32_bf16 v[88:91], a[156:159], v[136:139], v[88:91]\n"
"v_mfma_f32_16x16x32_bf16 v[124:127], a[188:191], v[136:139], 0\n"
"v_mfma_f32_16x16x32_bf16 v[124:127], a[184:187], v[120:123], v[124:127]\n"
"v_mfma_f32_16x16x32_bf16 v[124:127], a[180:183], v[208:211], v[124:127]\n"
"buffer_load_dwordx4 v[156:159], v1, s[4:7], s97 offen offset:2048 sc1\n"
"v_mfma_f32_16x16x32_bf16 v[124:127], a[176:179], v[140:143], v[124:127]\n"
"v_mfma_f32_16x16x32_bf16 v[124:127], a[172:175], v[204:207], v[124:127]\n"
"s_nop 1\n"
"v_cmp_u_f32_e32 vcc, v88, v88\n"
"v_mfma_f32_16x16x32_bf16 v[124:127], a[168:171], v[184:187], v[124:127]\n"
"s_cbranch_vccnz .Lmk_p106s\n"
".Lmk_p106r:\n"
"v_mfma_f32_16x16x32_bf16 v[124:127], a[164:167], v[200:203], v[124:127]\n"
"ds_write_b128 v3, v[88:91]\n"
"v_mfma_f32_16x16x32_bf16 v[124:127], a[160:163], v[152:155], v[124:127]\n"
"buffer_load_dwordx4 v[88:91], v1, s[4:7], s97 offen offset:3072 sc1\n"
"v_mfma_f32_16x16x32_bf16 v[168:171], a[192:195], v[152:155], 0\n"
"v_mfma_f32_16x16x32_bf16 v[168:171], a[196:199], v[200:203], v[168:171]\n"
"s_add_u32 s98, s59, 0x40000\n"
"v_mfma_f32_16x16x32_bf16 v[168:171], a[200:203], v[184:187], v[168:171]\n"
"v_mfma_f32_16x16x32_bf16 v[168:171], a[204:207], v[204:207], v[168:171]\n"
"buffer_load_dwordx4 v[172:175], v0, s[4:7], s98 offen sc1\n"
"v_mfma_f32_16x16x32_bf16 v[168:171], a[208:211], v[140:143], v[168:171]\n"
"v_mfma_f32_16x16x32_bf16 v[168:171], a[212:215], v[208:211], v[168:171]\n"
"ds_write_b128 v3, v[124:127] offset:1024\n"
"v_mfma_f32_16x16x32_bf16 v[168:171], a[216:219], v[120:123], v[168:171]\n"
"v_mfma_f32_16x16x32_bf16 v[168:171], a[220:223], v[136:139], v[168:171]\n"
"buffer_load_dwordx4 v[124:127], v0, s[4:7], s98 offen offset:1024 sc1\n"
"v_mfma_f32_16x16x32_bf16 v[176:179], a[252:255], v[136:139], 0\n"
"v_mfma_f32_16x16x32_bf16 v[176:179], a[248:251], v[120:123], v[176:179]\n"
"v_mfma_f32_16x16x32_bf16 v[176:179], a[244:247], v[208:211], v[176:179]\n"
"v_mfma_f32_16x16x32_bf16 v[176:179], a[240:243], v[140:143], v[176:179]\n"
"buffer_load_dwordx4 v[120:123], v0, s[4:7], s98 offen offset:2048 sc1\n"
"v_mfma_f32_16x16x32_bf16 v[176:179], a[236:239], v[204:207], v[176:179]\n"
"v_mfma_f32_16x16x32_bf16 v[176:179], a[232:235], v[184:187], v[176:179]\n"
"ds_write_b128 v3, v[168:171] offset:2048\n"
"v_mfma_f32_16x16x32_bf16 v[176:179], a[228:231], v[200:203], v[176:179]\n"
"v_mfma_f32_16x16x32_bf16 v[176:179], a[224:227], v[152:155], v[176:179]\n"
"s_waitcnt vmcnt(17)\n"
"buffer_load_dwordx4 v[136:139], v0, s[4:7], s98 offen offset:3072 sc1\n"
"; @A G2 b.1\n"
"v_mfma_f32_16x16x32_bf16 v[140:143], a[128:131], v[216:219], 0\n"
"v_mfma_f32_16x16x32_bf16 v[140:143], a[132:135], v[132:135], v[140:143]\n"
"v_mfma_f32_16x16x32_bf16 v[140:143], a[136:139], v[160:163], v[140:143]\n"
"v_mfma_f32_16x16x32_bf16 v[140:143], a[140:143], v[100:103], v[140:143]\n"
"s_waitcnt vmcnt(14)\n"
"buffer_load_dwordx4 v[152:155], v1, s[4:7], s98 offen sc1\n"
"v_mfma_f32_16x16x32_bf16 v[140:143], a[144:147], v[164:167], v[140:143]\n"
"v_mfma_f32_16x16x32_bf16 v[140:143], a[148:151], v[104:107], v[140:143]\n"
"ds_write_b128 v3, v[176:179] offset:3072\n"
"v_mfma_f32_16x16x32_bf16 v[140:143], a[152:155], v[108:111], v[140:143]\n"
"v_mfma_f32_16x16x32_bf16 v[140:143], a[156:159], v[128:131], v[140:143]\n"
"buffer_load_dwordx4 v[168:171], v1, s[4:7], s98 offen offset:1024 sc1\n"
"v_mfma_f32_16x16x32_bf16 v[176:179], a[188:191], v[128:131], 0\n"
"v_mfma_f32_16x16x32_bf16 v[176:179], a[184:187], v[108:111], v[176:179]\n"
"v_mfma_f32_16x16x32_bf16 v[176:179], a[180:183], v[104:107], v[176:179]\n"
"v_mfma_f32_16x16x32_bf16 v[176:179], a[176:179], v[164:167], v[176:179]\n"
"buffer_load_dwordx4 v[180:183], v1, s[4:7], s98 offen offset:2048 sc1\n"
"v_mfma_f32_16x16x32_bf16 v[176:179], a[172:175], v[100:103], v[176:179]\n"
"s_nop 0\n"
"v_cmp_u_f32_e32 vcc, v140, v140\n"
"v_mfma_f32_16x16x32_bf16 v[176:179], a[168:171], v[160:163], v[176:179]\n"
"s_cbranch_vccnz .Lmk_p107s\n"
".Lmk_p107r:\n"
"v_mfma_f32_16x16x32_bf16 v[176:179], a[164:167], v[132:135], v[176:179]\n"
"ds_write_b128 v3, v[140:143] offset:6144\n"
"v_mfma_f32_16x16x32_bf16 v[176:179], a[160:163], v[216:219], v[176:179]\n"
"buffer_load_dwordx4 v[140:143], v1, s[4:7], s98 offen offset:3072 sc1\n"
"v_mfma_f32_16x16x32_bf16 v[184:187], a[192:195], v[216:219], 0\n"
"v_mfma_f32_16x16x32_bf16 v[184:187], a[196:199], v[132:135], v[184:187]\n"
"v_mfma_f32_16x16x32_bf16 v[184:187], a[200:203], v[160:163], v[184:187]\n"
"v_mfma_f32_16x16x32_bf16 v[184:187], a[204:207], v[100:103], v[184:187]\n"
"v_mfma_f32_16x16x32_bf16 v[184:187], a[208:211], v[164:167], v[184:187]\n"
"ds_read_b128 v[188:191], v2 offset:8192\n"
"ds_read_b128 v[192:195], v2 offset:9216\n"
"v_mfma_f32_16x16x32_bf16 v[184:187], a[212:215], v[104:107], v[184:187]\n"
"ds_write_b128 v3, v[176:179] offset:7168\n"
"v_mfma_f32_16x16x32_bf16 v[184:187], a[216:219], v[108:111], v[184:187]\n"
"v_mfma_f32_16x16x32_bf16 v[184:187], a[220:223], v[128:131], v[184:187]\n"
"v_mfma_f32_16x16x32_bf16 v[176:179], a[252:255], v[128:131], 0\n"
"v_mfma_f32_16x16x32_bf16 v[176:179], a[248:251], v[108:111], v[176:179]\n"
"s_waitcnt lgkmcnt(1)\n"
"v_lshlrev_b32_e32 v255, 16, v188\n"
"v_and_b32_e32 v254, 0xffff0000, v188\n"
"v_mfma_f32_16x16x32_bf16 v[176:179], a[244:247], v[104:107], v[176:179]\n"
"v_lshlrev_b32_e32 v253, 16, v189\n"
"v_and_b32_e32 v252, 0xffff0000, v189\n"
"v_mfma_f32_16x16x32_bf16 v[176:179], a[240:243], v[164:167], v[176:179]\n"
"v_lshlrev_b32_e32 v251, 16, v190\n"
"v_and_b32_e32 v250, 0xffff0000, v190\n"
"v_mfma_f32_16x16x32_bf16 v[176:179], a[236:239], v[100:103], v[176:179]\n"
"v_lshlrev_b32_e32 v249, 16, v191\n"
"v_and_b32_e32 v248, 0xffff0000, v191\n"
"v_mfma_f32_16x16x32_bf16 v[176:179], a[232:235], v[160:163], v[176:179]\n"
"ds_write_b128 v3, v[184:187] offset:8192\n"
"v_mfma_f32_16x16x32_bf16 v[176:179], a[228:231], v[132:135], v[176:179]\n"
"v_lshlrev_b32_e32 v247, 16, v192\n"
"v_and_b32_e32 v246, 0xffff0000, v192\n"
"v_mfma_f32_16x16x32_bf16 v[176:179], a[224:227], v[216:219], v[176:179]\n"
"s_waitcnt vmcnt(14)\n"
"v_lshlrev_b32_e32 v245, 16, v193\n"
"v_and_b32_e32 v244, 0xffff0000, v193\n"
"; @A G2 b.2\n"
"v_mfma_f32_16x16x32_bf16 v[100:103], a[128:131], v[148:151], 0\n"
"v_lshlrev_b32_e32 v243, 16, v194\n"
"v_and_b32_e32 v242, 0xffff0000, v194\n"
"v_mfma_f32_16x16x32_bf16 v[100:103], a[132:135], v[144:147], v[100:103]\n"
"v_lshlrev_b32_e32 v241, 16, v195\n"
"v_and_b32_e32 v240, 0xffff0000, v195\n"
"v_mfma_f32_16x16x32_bf16 v[100:103], a[136:139], v[116:119], v[100:103]\n"
"v_mfma_f32_16x16x32_bf16 v[100:103], a[140:143], v[112:115], v[100:103]\n"
"s_waitcnt vmcnt(8)\n"
"v_mfma_f32_16x16x32_bf16 v[100:103], a[144:147], v[92:95], v[100:103]\n"
"v_mfma_f32_16x16x32_bf16 v[100:103], a[148:151], v[96:99], v[100:103]\n"
"ds_write_b128 v3, v[176:179] offset:9216\n"
"v_mfma_f32_16x16x32_bf16 v[100:103], a[152:155], v[156:159], v[100:103]\n"
"v_mfma_f32_16x16x32_bf16 v[100:103], a[156:159], v[88:91], v[100:103]\n"
"v_mfma_f32_16x16x32_bf16 v[104:107], a[188:191], v[88:91], 0\n"
"v_mfma_f32_16x16x32_bf16 v[104:107], a[184:187], v[156:159], v[104:107]\n"
"s_waitcnt lgkmcnt(0)\n"
"s_barrier\n"
"v_mfma_f32_16x16x32_bf16 v[104:107], a[180:183], v[96:99], v[104:107]\n"
"ds_read_b128 v[108:111], v4\n"
"ds_read_b128 v[128:131], v5\n"
"v_mfma_f32_16x16x32_bf16 v[104:107], a[176:179], v[92:95], v[104:107]\n"
"v_mfma_f32_16x16x32_bf16 v[104:107], a[172:175], v[112:115], v[104:107]\n"
"s_add_u32 s98, s56, 0\n"
"v_cmp_u_f32_e32 vcc, v100, v100\n"
"v_mfma_f32_16x16x32_bf16 v[104:107], a[168:171], v[116:119], v[104:107]\n"
"s_cbranch_vccnz .Lmk_p108s\n"
".Lmk_p108r:\n"
"v_mfma_f32_16x16x32_bf16 v[104:107], a[164:167], v[144:147], v[104:107]\n"
"ds_write_b128 v3, v[100:103] offset:12288\n"
"v_mfma_f32_16x16x32_bf16 v[104:107], a[160:163], v[148:151], v[104:107]\n"
"buffer_load_dwordx4 v[100:103], v0, s[4:7], s98 offen sc1\n"
"v_mfma_f32_16x16x32_bf16 v[132:135], a[192:195], v[148:151], 0\n"
"ds_read_b128 v[160:163], v4 offset:1024\n"
"ds_read_b128 v[164:167], v5 offset:1024\n"
"v_mfma_f32_16x16x32_bf16 v[132:135], a[196:199], v[144:147], v[132:135]\n"
"s_waitcnt lgkmcnt(3)\n"
"v_add_f32_e32 v108, v108, v255\n"
"v_add_f32_e32 v109, v109, v254\n"
"v_mfma_f32_16x16x32_bf16 v[132:135], a[200:203], v[116:119], v[132:135]\n"
"buffer_load_dwordx4 v[176:179], v0, s[4:7], s98 offen offset:1024 sc1\n"
"v_mfma_f32_16x16x32_bf16 v[132:135], a[204:207], v[112:115], v[132:135]\n"
"v_add_f32_e32 v110, v110, v253\n"
"v_add_f32_e32 v111, v111, v252\n"
"v_mfma_f32_16x16x32_bf16 v[132:135], a[208:211], v[92:95], v[132:135]\n"
"v_add_f32_e32 v108, v108, v128\n"
"v_add_f32_e32 v109, v109, v129\n"
"v_mfma_f32_16x16x32_bf16 v[132:135], a[212:215], v[96:99], v[132:135]\n"
"ds_write_b128 v3, v[104:107] offset:13312\n"
"v_mfma_f32_16x16x32_bf16 v[132:135], a[216:219], v[156:159], v[132:135]\n"
"buffer_load_dwordx4 v[104:107], v0, s[4:7], s98 offen offset:2048 sc1\n"
"v_mfma_f32_16x16x32_bf16 v[132:135], a[220:223], v[88:91], v[132:135]\n"
"ds_read_b128 v[184:187], v4 offset:2048\n"
"ds_read_b128 v[188:191], v5 offset:2048\n"
"v_mfma_f32_16x16x32_bf16 v[192:195], a[252:255], v[88:91], 0\n"
"s_waitcnt lgkmcnt(3)\n"
"v_add_f32_e32 v110, v110, v130\n"
"v_add_f32_e32 v111, v111, v131\n"
"v_mfma_f32_16x16x32_bf16 v[192:195], a[248:251], v[156:159], v[192:195]\n"
"buffer_load_dwordx4 v[88:91], v0, s[4:7], s98 offen offset:3072 sc1\n"
"v_mfma_f32_16x16x32_bf16 v[192:195], a[244:247], v[96:99], v[192:195]\n"
"v_add_f32_e32 v160, v160, v251\n"
"v_add_f32_e32 v161, v161, v250\n"
"v_mfma_f32_16x16x32_bf16 v[192:195], a[240:243], v[92:95], v[192:195]\n"
"v_add_f32_e32 v162, v162, v249\n"
"v_add_f32_e32 v163, v163, v248\n"
"v_mfma_f32_16x16x32_bf16 v[192:195], a[236:239], v[112:115], v[192:195]\n"
"buffer_load_dwordx4 v[92:95], v1, s[4:7], s98 offen sc1\n"
"v_mfma_f32_16x16x32_bf16 v[192:195], a[232:235], v[116:119], v[192:195]\n"
"ds_write_b128 v3, v[132:135] offset:14336\n"
"v_mfma_f32_16x16x32_bf16 v[192:195], a[228:231], v[144:147], v[192:195]\n"
"ds_read_b128 v[96:99], v4 offset:3072\n"
"ds_read_b128 v[112:115], v5 offset:3072\n"
"v_mfma_f32_16x16x32_bf16 v[192:195], a[224:227], v[148:151], v[192:195]\n"
"s_waitcnt vmcnt(9)\n"
"buffer_load_dwordx4 v[116:119], v1, s[4:7], s98 offen offset:1024 sc1\n"
"; @A G2 b.3\n"
"v_mfma_f32_16x16x32_bf16 v[128:131], a[128:131], v[172:175], v[108:111]\n"
"v_add_f32_e32 v160, v160, v164\n"
"v_add_f32_e32 v161, v161, v165\n"
"v_mfma_f32_16x16x32_bf16 v[128:131], a[132:135], v[124:127], v[128:131]\n"
"s_waitcnt lgkmcnt(3)\n"
"v_add_f32_e32 v162, v162, v166\n"
"v_add_f32_e32 v163, v163, v167\n"
"v_mfma_f32_16x16x32_bf16 v[128:131], a[136:139], v[120:123], v[128:131]\n"
"buffer_load_dwordx4 v[132:135], v1, s[4:7], s98 offen offset:2048 sc1\n"
"v_mfma_f32_16x16x32_bf16 v[128:131], a[140:143], v[136:139], v[128:131]\n"
"s_waitcnt vmcnt(7)\n"
"v_add_f32_e32 v184, v184, v247\n"
"v_add_f32_e32 v185, v185, v246\n"
"v_mfma_f32_16x16x32_bf16 v[128:131], a[144:147], v[152:155], v[128:131]\n"
"v_add_f32_e32 v186, v186, v245\n"
"v_add_f32_e32 v187, v187, v244\n"
"v_mfma_f32_16x16x32_bf16 v[128:131], a[148:151], v[168:171], v[128:131]\n"
"ds_write_b128 v3, v[192:195] offset:15360\n"
"v_mfma_f32_16x16x32_bf16 v[128:131], a[152:155], v[180:183], v[128:131]\n"
"buffer_load_dwordx4 v[144:147], v1, s[4:7], s98 offen offset:3072 sc1\n"
"v_mfma_f32_16x16x32_bf16 v[128:131], a[156:159], v[140:143], v[128:131]\n"
"s_add_u32 s98, s57, 0\n"
"v_add_f32_e32 v184, v184, v188\n"
"v_add_f32_e32 v185, v185, v189\n"
"v_mfma_f32_16x16x32_bf16 v[148:151], a[188:191], v[140:143], v[160:163]\n"
"s_waitcnt lgkmcnt(1)\n"
"v_add_f32_e32 v186, v186, v190\n"
"v_add_f32_e32 v187, v187, v191\n"
"v_mfma_f32_16x16x32_bf16 v[148:151], a[184:187], v[180:183], v[148:151]\n"
"s_waitcnt lgkmcnt(0)\n"
"s_barrier\n"
"v_mfma_f32_16x16x32_bf16 v[148:151], a[180:183], v[168:171], v[148:151]\n"
"buffer_load_dwordx4 v[156:159], v0, s[4:7], s98 offen sc1\n"
"v_mfma_f32_16x16x32_bf16 v[148:151], a[176:179], v[152:155], v[148:151]\n"
"v_add_f32_e32 v96, v96, v243\n"
"v_add_f32_e32 v97, v97, v242\n"
"v_mfma_f32_16x16x32_bf16 v[148:151], a[172:175], v[136:139], v[148:151]\n"
"v_cmp_u_f32_e32 vcc, v128, v128\n"
"v_add_f32_e32 v98, v98, v241\n"
"v_mfma_f32_16x16x32_bf16 v[148:151], a[168:171], v[120:123], v[148:151]\n"
"s_cbranch_vccnz .Lmk_p109s\n"
".Lmk_p109r:\n"
"v_add_f32_e32 v99, v99, v240\n"
"v_mfma_f32_16x16x32_bf16 v[148:151], a[164:167], v[124:127], v[148:151]\n"
"buffer_load_dwordx4 v[108:111], v0, s[4:7], s98 offen offset:1024 sc1\n"
"v_mfma_f32_16x16x32_bf16 v[148:151], a[160:163], v[172:175], v[148:151]\n"
"v_add_f32_e32 v96, v96, v112\n"
"v_add_f32_e32 v97, v97, v113\n"
"v_mfma_f32_16x16x32_bf16 v[184:187], a[192:195], v[172:175], v[184:187]\n"
"v_add_f32_e32 v98, v98, v114\n"
"v_add_f32_e32 v99, v99, v115\n"
"v_mfma_f32_16x16x32_bf16 v[184:187], a[196:199], v[124:127], v[184:187]\n"
"buffer_load_dwordx4 v[112:115], v0, s[4:7], s98 offen offset:2048 sc1\n"
"v_mfma_f32_16x16x32_bf16 v[184:187], a[200:203], v[120:123], v[184:187]\n"
"ds_read_b128 v[160:163], v6\n"
"ds_read_b128 v[164:167], v6 offset:1024\n"
"v_mfma_f32_16x16x32_bf16 v[184:187], a[204:207], v[136:139], v[184:187]\n"
"ds_read_b128 v[188:191], v6 offset:2048\n"
"ds_read_b128 v[192:195], v6 offset:3072\n"
"v_mfma_f32_16x16x32_bf16 v[184:187], a[208:211], v[152:155], v[184:187]\n"
"buffer_load_dwordx4 v[196:199], v0, s[4:7], s98 offen offset:3072 sc1\n"
"v_mfma_f32_16x16x32_bf16 v[184:187], a[212:215], v[168:171], v[184:187]\n"
"v_mfma_f32_16x16x32_bf16 v[184:187], a[216:219], v[180:183], v[184:187]\n"
"v_mfma_f32_16x16x32_bf16 v[184:187], a[220:223], v[140:143], v[184:187]\n"
"buffer_load_dwordx4 v[200:203], v1, s[4:7], s98 offen sc1\n"
"v_mfma_f32_16x16x32_bf16 v[96:99], a[252:255], v[140:143], v[96:99]\n"
"s_waitcnt lgkmcnt(0)\n"
"v_add_f32_e32 v160, v128, v160\n"
"v_add_f32_e32 v161, v129, v161\n"
"v_mfma_f32_16x16x32_bf16 v[96:99], a[248:251], v[180:183], v[96:99]\n"
"v_add_f32_e32 v162, v130, v162\n"
"v_add_f32_e32 v163, v131, v163\n"
"v_mfma_f32_16x16x32_bf16 v[96:99], a[244:247], v[168:171], v[96:99]\n"
"buffer_load_dwordx4 v[128:131], v1, s[4:7], s98 offen offset:1024 sc1\n"
"v_mfma_f32_16x16x32_bf16 v[96:99], a[240:243], v[152:155], v[96:99]\n"
"v_add_f32_e32 v164, v148, v164\n"
"v_add_f32_e32 v165, v149, v165\n"
"v_mfma_f32_16x16x32_bf16 v[96:99], a[236:239], v[136:139], v[96:99]\n"
"v_add_f32_e32 v166, v150, v166\n"
"v_add_f32_e32 v167, v151, v167\n"
"v_mfma_f32_16x16x32_bf16 v[96:99], a[232:235], v[120:123], v[96:99]\n"
"buffer_load_dwordx4 v[120:123], v1, s[4:7], s98 offen offset:2048 sc1\n"
"v_mfma_f32_16x16x32_bf16 v[96:99], a[228:231], v[124:127], v[96:99]\n"
"v_cvt_pk_bf16_f32 v124, v160, v161\n"
"v_cvt_pk_bf16_f32 v125, v162, v163\n"
"v_mfma_f32_16x16x32_bf16 v[96:99], a[224:227], v[172:175], v[96:99]\n"
"s_add_u32 s97, s61, 0x440000\n"
"v_cvt_pk_bf16_f32 v126, v164, v165\n"
"v_cvt_pk_bf16_f32 v127, v166, v167\n"
"buffer_store_dwordx4 v[124:127], v0, s[4:7], s97 offen sc1\n"
"ds_write_b128 v2, v[124:127] offset:8192\n"
"buffer_load_dwordx4 v[136:139], v1, s[4:7], s98 offen offset:3072 sc1\n"
"; @A fin b\n"
"s_add_u32 s98, s61, 0x440000\n"
"v_add_f32_e32 v188, v184, v188\n"
"v_add_f32_e32 v189, v185, v189\n"
"v_add_f32_e32 v190, v186, v190\n"
"v_add_f32_e32 v191, v187, v191\n"
"v_add_f32_e32 v192, v96, v192\n"
"v_add_f32_e32 v193, v97, v193\n"
"v_add_f32_e32 v194, v98, v194\n"
"v_add_f32_e32 v195, v99, v195\n"
"v_cvt_pk_bf16_f32 v96, v188, v189\n"
"v_cvt_pk_bf16_f32 v97, v190, v191\n"
"v_cvt_pk_bf16_f32 v98, v192, v193\n"
"v_cvt_pk_bf16_f32 v99, v194, v195\n"
"buffer_store_dwordx4 v[96:99], v0, s[4:7], s98 offen offset:1024 sc1\n"
"s_waitcnt vmcnt(16)\n"
"; @A G2 a.0\n"
"v_mfma_f32_16x16x32_bf16 v[140:143], a[128:131], v[100:103], 0\n"
"s_add_u32 s98, s58, 0\n"
"buffer_load_dwordx4 v[148:151], v0, s[4:7], s98 offen sc1\n"
"v_mfma_f32_16x16x32_bf16 v[140:143], a[132:135], v[176:179], v[140:143]\n"
"s_waitcnt vmcnt(15)\n"
"ds_write_b128 v2, v[96:99] offset:9216\n"
"v_mfma_f32_16x16x32_bf16 v[140:143], a[136:139], v[104:107], v[140:143]\n"
"v_mov_b32_e32 v152, 0x3f803f80\n"
"v_mov_b32_e32 v153, 0x3f803f80\n"
"v_mfma_f32_16x16x32_bf16 v[140:143], a[140:143], v[88:91], v[140:143]\n"
"s_waitcnt vmcnt(13)\n"
"v_mov_b32_e32 v154, 0x3f803f80\n"
"v_mov_b32_e32 v155, 0x3f803f80\n"
"v_mfma_f32_16x16x32_bf16 v[140:143], a[144:147], v[92:95], v[140:143]\n"
"buffer_load_dwordx4 v[160:163], v0, s[4:7], s98 offen offset:1024 sc1\n"
"v_mfma_f32_16x16x32_bf16 v[164:167], v[152:155], v[124:127], 0\n"
"v_mfma_f32_16x16x32_bf16 v[164:167], v[152:155], v[96:99], v[164:167]\n"
"v_mfma_f32_16x16x32_bf16 v[152:155], v[124:127], v[124:127], 0\n"
"v_mfma_f32_16x16x32_bf16 v[152:155], v[96:99], v[96:99], v[152:155]\n"
"v_mfma_f32_16x16x32_bf16 v[140:143], a[148:151], v[116:119], v[140:143]\n"
"s_waitcnt vmcnt(12)\n"
"v_mfma_f32_16x16x32_bf16 v[140:143], a[152:155], v[132:135], v[140:143]\n"
"v_mfma_f32_16x16x32_bf16 v[140:143], a[156:159], v[144:147], v[140:143]\n"
"v_mfma_f32_16x16x32_bf16 v[96:99], a[188:191], v[144:147], 0\n"
"buffer_load_dwordx4 v[124:127], v0, s[4:7], s98 offen offset:2048 sc1\n"
"v_mfma_f32_16x16x32_bf16 v[96:99], a[184:187], v[132:135], v[96:99]\n"
"s_mov_b32 s96, 0xaaaaaaaa\n"
"s_mov_b32 s97, 0xaaaaaaaa\n"
"v_mfma_f32_16x16x32_bf16 v[96:99], a[180:183], v[116:119], v[96:99]\n"
"v_mfma_f32_16x16x32_bf16 v[96:99], a[176:179], v[92:95], v[96:99]\n"
"v_mfma_f32_16x16x32_bf16 v[96:99], a[172:175], v[88:91], v[96:99]\n"
"v_cmp_u_f32_e32 vcc, v140, v140\n"
"v_cndmask_b32_e64 v152, v152, v153, s[96:97]\n"
"v_mfma_f32_16x16x32_bf16 v[96:99], a[168:171], v[104:107], v[96:99]\n"
"s_cbranch_vccnz .Lmk_p110s\n"
".Lmk_p110r:\n"
"v_cndmask_b32_e64 v154, v154, v155, s[96:97]\n"
"v_mfma_f32_16x16x32_bf16 v[96:99], a[164:167], v[176:179], v[96:99]\n"
"ds_write_b128 v3, v[140:143]\n"
"v_mfma_f32_16x16x32_bf16 v[96:99], a[160:163], v[100:103], v[96:99]\n"
"buffer_load_dwordx4 v[140:143], v0, s[4:7], s98 offen offset:3072 sc1\n"
"v_mfma_f32_16x16x32_bf16 v[168:171], a[192:195], v[100:103], 0\n"
"s_mov_b32 s96, 0xcccccccc\n"
"s_mov_b32 s97, 0xcccccccc\n"
"v_mfma_f32_16x16x32_bf16 v[168:171], a[196:199], v[176:179], v[168:171]\n"
"v_cndmask_b32_e64 v165, v152, v154, s[96:97]\n"
"v_mfma_f32_16x16x32_bf16 v[168:171], a[200:203], v[104:107], v[168:171]\n"
"s_mov_b32 s96, 0xf0000f\n"
"s_mov_b32 s97, 0xf0000f00\n"
"v_mfma_f32_16x16x32_bf16 v[168:171], a[204:207], v[88:91], v[168:171]\n"
"buffer_load_dwordx4 v[152:155], v1, s[4:7], s98 offen sc1\n"
"v_mfma_f32_16x16x32_bf16 v[168:171], a[208:211], v[92:95], v[168:171]\n"
"v_cndmask_b32_e64 v165, 0, v165, s[96:97]\n"
"v_mov_b32_e32 v255, v165\n"
"v_mfma_f32_16x16x32_bf16 v[168:171], a[212:215], v[116:119], v[168:171]\n"
"ds_write_b128 v3, v[96:99] offset:1024\n"
"v_mfma_f32_16x16x32_bf16 v[168:171], a[216:219], v[132:135], v[168:171]\n"
"v_permlane16_swap_b32_e32 v165, v255\n"
"v_add_f32_e32 v165, v165, v255\n"
"v_mfma_f32_16x16x32_bf16 v[168:171], a[220:223], v[144:147], v[168:171]\n"
"buffer_load_dwordx4 v[172:175], v1, s[4:7], s98 offen offset:1024 sc1\n"
"v_mfma_f32_16x16x32_bf16 v[96:99], a[252:255], v[144:147], 0\n"
"v_mov_b32_e32 v255, v165\n"
"v_mfma_f32_16x16x32_bf16 v[96:99], a[248:251], v[132:135], v[96:99]\n"
"s_mov_b32 s96, 0xffff0000\n"
"v_permlane32_swap_b32_e32 v165, v255\n"
"v_add_f32_e32 v165, v165, v255\n"
"v_mfma_f32_16x16x32_bf16 v[96:99], a[244:247], v[116:119], v[96:99]\n"
"v_cndmask_b32_e64 v164, v164, v165, s[96:97]\n"
"s_add_u32 s97, s63, 0x44000\n"
"v_mfma_f32_16x16x32_bf16 v[96:99], a[240:243], v[92:95], v[96:99]\n"
"buffer_load_dwordx4 v[116:119], v1, s[4:7], s98 offen offset:2048 sc1\n"
"v_mfma_f32_16x16x32_bf16 v[96:99], a[236:239], v[88:91], v[96:99]\n"
"buffer_store_dword v164, v14, s[4:7], s97 offen sc1\n"
"v_mfma_f32_16x16x32_bf16 v[96:99], a[232:235], v[104:107], v[96:99]\n"
"ds_write_b128 v3, v[168:171] offset:2048\n"
"v_mfma_f32_16x16x32_bf16 v[96:99], a[228:231], v[176:179], v[96:99]\n"
"v_mfma_f32_16x16x32_bf16 v[96:99], a[224:227], v[100:103], v[96:99]\n"
"s_waitcnt vmcnt(14)\n"
"buffer_load_dwordx4 v[132:135], v1, s[4:7], s98 offen offset:3072 sc1\n"
"; @A G2 a.1\n"
"v_mfma_f32_16x16x32_bf16 v[88:91], a[128:131], v[156:159], 0\n"
"v_mfma_f32_16x16x32_bf16 v[88:91], a[132:135], v[108:111], v[88:91]\n"
"s_add_u32 s98, s59, 0\n"
"v_mfma_f32_16x16x32_bf16 v[88:91], a[136:139], v[112:115], v[88:91]\n"
"v_mfma_f32_16x16x32_bf16 v[88:91], a[140:143], v[196:199], v[88:91]\n"
"s_waitcnt vmcnt(10)\n"
"buffer_load_dwordx4 v[176:179], v0, s[4:7], s98 offen sc1\n"
"v_mfma_f32_16x16x32_bf16 v[88:91], a[144:147], v[200:203], v[88:91]\n"
"v_mfma_f32_16x16x32_bf16 v[88:91], a[148:151], v[128:131], v[88:91]\n"
"ds_write_b128 v3, v[96:99] offset:3072\n"
"v_mfma_f32_16x16x32_bf16 v[88:91], a[152:155], v[120:123], v[88:91]\n"
"v_mfma_f32_16x16x32_bf16 v[88:91], a[156:159], v[136:139], v[88:91]\n"
"buffer_load_dwordx4 v[180:183], v0, s[4:7], s98 offen offset:1024 sc1\n"
"v_mfma_f32_16x16x32_bf16 v[92:95], a[188:191], v[136:139], 0\n"
"v_mfma_f32_16x16x32_bf16 v[92:95], a[184:187], v[120:123], v[92:95]\n"
"v_mfma_f32_16x16x32_bf16 v[92:95], a[180:183], v[128:131], v[92:95]\n"
"v_mfma_f32_16x16x32_bf16 v[92:95], a[176:179], v[200:203], v[92:95]\n"
"buffer_load_dwordx4 v[168:171], v0, s[4:7], s98 offen offset:2048 sc1\n"
"v_mfma_f32_16x16x32_bf16 v[92:95], a[172:175], v[196:199], v[92:95]\n"
"s_nop 0\n"
"v_cmp_u_f32_e32 vcc, v88, v88\n"
"v_mfma_f32_16x16x32_bf16 v[92:95], a[168:171], v[112:115], v[92:95]\n"
"s_cbranch_vccnz .Lmk_p111s\n"
".Lmk_p111r:\n"
"v_mfma_f32_16x16x32_bf16 v[92:95], a[164:167], v[108:111], v[92:95]\n"
"ds_write_b128 v3, v[88:91] offset:6144\n"
"v_mfma_f32_16x16x32_bf16 v[92:95], a[160:163], v[156:159], v[92:95]\n"
"buffer_load_dwordx4 v[184:187], v0, s[4:7], s98 offen offset:3072 sc1\n"
"v_mfma_f32_16x16x32_bf16 v[88:91], a[192:195], v[156:159], 0\n"
"v_mfma_f32_16x16x32_bf16 v[88:91], a[196:199], v[108:111], v[88:91]\n"
"v_mfma_f32_16x16x32_bf16 v[88:91], a[200:203], v[112:115], v[88:91]\n"
"v_mfma_f32_16x16x32_bf16 v[88:91], a[204:207], v[196:199], v[88:91]\n"
"buffer_load_dwordx4 v[188:191], v1, s[4:7], s98 offen sc1\n"
"v_mfma_f32_16x16x32_bf16 v[88:91], a[208:211], v[200:203], v[88:91]\n"
"ds_read_b128 v[96:99], v2\n"
"ds_read_b128 v[100:103], v2 offset:1024\n"
"v_mfma_f32_16x16x32_bf16 v[88:91], a[212:215], v[128:131], v[88:91]\n"
"ds_write_b128 v3, v[92:95] offset:7168\n"
"v_mfma_f32_16x16x32_bf16 v[88:91], a[216:219], v[120:123], v[88:91]\n"
"v_mfma_f32_16x16x32_bf16 v[88:91], a[220:223], v[136:139], v[88:91]\n"
"buffer_load_dwordx4 v[164:167], v1, s[4:7], s98 offen offset:1024 sc1\n"
"v_mfma_f32_16x16x32_bf16 v[92:95], a[252:255], v[136:139], 0\n"
"v_mfma_f32_16x16x32_bf16 v[92:95], a[248:251], v[120:123], v[92:95]\n"
"s_waitcnt lgkmcnt(1)\n"
"v_lshlrev_b32_e32 v255, 16, v96\n"
"v_and_b32_e32 v254, 0xffff0000, v96\n"
"v_mfma_f32_16x16x32_bf16 v[92:95], a[244:247], v[128:131], v[92:95]\n"
"v_lshlrev_b32_e32 v253, 16, v97\n"
"v_and_b32_e32 v252, 0xffff0000, v97\n"
"v_mfma_f32_16x16x32_bf16 v[92:95], a[240:243], v[200:203], v[92:95]\n"
"buffer_load_dwordx4 v[192:195], v1, s[4:7], s98 offen offset:2048 sc1\n"
"v_mfma_f32_16x16x32_bf16 v[92:95], a[236:239], v[196:199], v[92:95]\n"
"v_lshlrev_b32_e32 v251, 16, v98\n"
"v_and_b32_e32 v250, 0xffff0000, v98\n"
"v_mfma_f32_16x16x32_bf16 v[92:95], a[232:235], v[112:115], v[92:95]\n"
"ds_write_b128 v3, v[88:91] offset:8192\n"
"v_mfma_f32_16x16x32_bf16 v[92:95], a[228:231], v[108:111], v[92:95]\n"
"v_lshlrev_b32_e32 v249, 16, v99\n"
"v_and_b32_e32 v248, 0xffff0000, v99\n"
"v_mfma_f32_16x16x32_bf16 v[92:95], a[224:227], v[156:159], v[92:95]\n"
"s_waitcnt vmcnt(12)\n"
"buffer_load_dwordx4 v[196:199], v1, s[4:7], s98 offen offset:3072 sc1\n"
"; @A G2 a.2\n"
"v_mfma_f32_16x16x32_bf16 v[96:99], a[128:131], v[148:151], 0\n"
"v_lshlrev_b32_e32 v247, 16, v100\n"
"v_and_b32_e32 v246, 0xffff0000, v100\n"
"v_mfma_f32_16x16x32_bf16 v[96:99], a[132:135], v[160:163], v[96:99]\n"
"v_lshlrev_b32_e32 v245, 16, v101\n"
"v_and_b32_e32 v244, 0xffff0000, v101\n"
"v_mfma_f32_16x16x32_bf16 v[96:99], a[136:139], v[124:127], v[96:99]\n"
"v_lshlrev_b32_e32 v243, 16, v102\n"
"v_and_b32_e32 v242, 0xffff0000, v102\n"
"v_mfma_f32_16x16x32_bf16 v[96:99], a[140:143], v[140:143], v[96:99]\n"
"s_waitcnt vmcnt(8)\n"
"v_lshlrev_b32_e32 v241, 16, v103\n"
"v_and_b32_e32 v240, 0xffff0000, v103\n"
"v_mfma_f32_16x16x32_bf16 v[96:99], a[144:147], v[152:155], v[96:99]\n"
"v_mfma_f32_16x16x32_bf16 v[96:99], a[148:151], v[172:175], v[96:99]\n"
"ds_write_b128 v3, v[92:95] offset:9216\n"
"v_mfma_f32_16x16x32_bf16 v[96:99], a[152:155], v[116:119], v[96:99]\n"
"v_mfma_f32_16x16x32_bf16 v[96:99], a[156:159], v[132:135], v[96:99]\n"
"v_mfma_f32_16x16x32_bf16 v[104:107], a[188:191], v[132:135], 0\n"
"s_add_u32 s98, s52, 0x440000\n"
"buffer_load_dwordx4 v[88:91], v0, s[4:7], s98 offen sc1\n"
"v_mfma_f32_16x16x32_bf16 v[104:107], a[184:187], v[116:119], v[104:107]\n"
"s_waitcnt lgkmcnt(0)\n"
"s_barrier\n"
"v_mfma_f32_16x16x32_bf16 v[104:107], a[180:183], v[172:175], v[104:107]\n"
"ds_read_b128 v[144:147], v4\n"
"ds_read_b128 v[112:115], v5\n"
"v_mfma_f32_16x16x32_bf16 v[104:107], a[176:179], v[152:155], v[104:107]\n"
"buffer_load_dwordx4 v[92:95], v0, s[4:7], s98 offen offset:1024 sc1\n"
"v_mfma_f32_16x16x32_bf16 v[104:107], a[172:175], v[140:143], v[104:107]\n"
"v_cmp_u_f32_e32 vcc, v96, v96\n"
"v_mfma_f32_16x16x32_bf16 v[104:107], a[168:171], v[124:127], v[104:107]\n"
"s_cbranch_vccnz .Lmk_p112s\n"
".Lmk_p112r:\n"
"v_mfma_f32_16x16x32_bf16 v[104:107], a[164:167], v[160:163], v[104:107]\n"
"ds_write_b128 v3, v[96:99] offset:12288\n"
"v_mfma_f32_16x16x32_bf16 v[104:107], a[160:163], v[148:151], v[104:107]\n"
"buffer_load_dwordx4 v[96:99], v0, s[4:7], s98 offen offset:2048 sc1\n"
"v_mfma_f32_16x16x32_bf16 v[120:123], a[192:195], v[148:151], 0\n"
"ds_read_b128 v[156:159], v4 offset:1024\n"
"ds_read_b128 v[128:131], v5 offset:1024\n"
"v_mfma_f32_16x16x32_bf16 v[120:123], a[196:199], v[160:163], v[120:123]\n"
"s_waitcnt lgkmcnt(3)\n"
"v_add_f32_e32 v144, v144, v255\n"
"v_add_f32_e32 v145, v145, v254\n"
"v_mfma_f32_16x16x32_bf16 v[120:123], a[200:203], v[124:127], v[120:123]\n"
"buffer_load_dwordx4 v[100:103], v0, s[4:7], s98 offen offset:3072 sc1\n"
"v_mfma_f32_16x16x32_bf16 v[120:123], a[204:207], v[140:143], v[120:123]\n"
"v_add_f32_e32 v146, v146, v253\n"
"v_add_f32_e32 v147, v147, v252\n"
"v_mfma_f32_16x16x32_bf16 v[120:123], a[208:211], v[152:155], v[120:123]\n"
"v_add_f32_e32 v144, v144, v112\n"
"v_add_f32_e32 v145, v145, v113\n"
"v_mfma_f32_16x16x32_bf16 v[120:123], a[212:215], v[172:175], v[120:123]\n"
"ds_write_b128 v3, v[104:107] offset:13312\n"
"v_mfma_f32_16x16x32_bf16 v[120:123], a[216:219], v[116:119], v[120:123]\n"
"buffer_load_dwordx4 v[104:107], v1, s[4:7], s98 offen sc1\n"
"v_mfma_f32_16x16x32_bf16 v[120:123], a[220:223], v[132:135], v[120:123]\n"
"ds_read_b128 v[108:111], v4 offset:2048\n"
"ds_read_b128 v[136:139], v5 offset:2048\n"
"v_mfma_f32_16x16x32_bf16 v[200:203], a[252:255], v[132:135], 0\n"
"s_waitcnt lgkmcnt(3)\n"
"v_add_f32_e32 v146, v146, v114\n"
"v_add_f32_e32 v147, v147, v115\n"
"v_mfma_f32_16x16x32_bf16 v[200:203], a[248:251], v[116:119], v[200:203]\n"
"buffer_load_dwordx4 v[112:115], v1, s[4:7], s98 offen offset:1024 sc1\n"
"v_mfma_f32_16x16x32_bf16 v[200:203], a[244:247], v[172:175], v[200:203]\n"
"v_add_f32_e32 v156, v156, v251\n"
"v_add_f32_e32 v157, v157, v250\n"
"v_mfma_f32_16x16x32_bf16 v[200:203], a[240:243], v[152:155], v[200:203]\n"
"v_add_f32_e32 v158, v158, v249\n"
"v_add_f32_e32 v159, v159, v248\n"
"v_mfma_f32_16x16x32_bf16 v[200:203], a[236:239], v[140:143], v[200:203]\n"
"buffer_load_dwordx4 v[116:119], v1, s[4:7], s98 offen offset:2048 sc1\n"
"v_mfma_f32_16x16x32_bf16 v[200:203], a[232:235], v[124:127], v[200:203]\n"
"ds_write_b128 v3, v[120:123] offset:14336\n"
"v_mfma_f32_16x16x32_bf16 v[200:203], a[228:231], v[160:163], v[200:203]\n"
"ds_read_b128 v[120:123], v4 offset:3072\n"
"ds_read_b128 v[152:155], v5 offset:3072\n"
"v_mfma_f32_16x16x32_bf16 v[200:203], a[224:227], v[148:151], v[200:203]\n"
"s_waitcnt vmcnt(11)\n"
"buffer_load_dwordx4 v[124:127], v1, s[4:7], s98 offen offset:3072 sc1\n"
"; @A G2 a.3\n"
"v_mfma_f32_16x16x32_bf16 v[172:175], a[128:131], v[176:179], v[144:147]\n"
"s_add_u32 s98, s53, 0x440000\n"
"v_add_f32_e32 v156, v156, v128\n"
"v_add_f32_e32 v157, v157, v129\n"
"v_mfma_f32_16x16x32_bf16 v[172:175], a[132:135], v[180:183], v[172:175]\n"
"s_waitcnt lgkmcnt(3)\n"
"v_add_f32_e32 v158, v158, v130\n"
"v_add_f32_e32 v159, v159, v131\n"
"v_mfma_f32_16x16x32_bf16 v[172:175], a[136:139], v[168:171], v[172:175]\n"
"buffer_load_dwordx4 v[128:131], v0, s[4:7], s98 offen sc1\n"
"v_mfma_f32_16x16x32_bf16 v[172:175], a[140:143], v[184:187], v[172:175]\n"
"s_waitcnt vmcnt(9)\n"
"v_add_f32_e32 v108, v108, v247\n"
"v_add_f32_e32 v109, v109, v246\n"
"v_mfma_f32_16x16x32_bf16 v[172:175], a[144:147], v[188:191], v[172:175]\n"
"v_add_f32_e32 v110, v110, v245\n"
"v_add_f32_e32 v111, v111, v244\n"
"v_mfma_f32_16x16x32_bf16 v[172:175], a[148:151], v[164:167], v[172:175]\n"
"ds_write_b128 v3, v[200:203] offset:15360\n"
"v_mfma_f32_16x16x32_bf16 v[172:175], a[152:155], v[192:195], v[172:175]\n"
"buffer_load_dwordx4 v[132:135], v0, s[4:7], s98 offen offset:1024 sc1\n"
"v_mfma_f32_16x16x32_bf16 v[172:175], a[156:159], v[196:199], v[172:175]\n"
"v_add_f32_e32 v108, v108, v136\n"
"v_add_f32_e32 v109, v109, v137\n"
"v_mfma_f32_16x16x32_bf16 v[200:203], a[188:191], v[196:199], v[156:159]\n"
"s_waitcnt lgkmcnt(1)\n"
"v_add_f32_e32 v110, v110, v138\n"
"v_add_f32_e32 v111, v111, v139\n"
"v_mfma_f32_16x16x32_bf16 v[200:203], a[184:187], v[192:195], v[200:203]\n"
"s_waitcnt lgkmcnt(0)\n"
"s_barrier\n"
"v_mfma_f32_16x16x32_bf16 v[200:203], a[180:183], v[164:167], v[200:203]\n"
"buffer_load_dwordx4 v[136:139], v0, s[4:7], s98 offen offset:2048 sc1\n"
"v_mfma_f32_16x16x32_bf16 v[200:203], a[176:179], v[188:191], v[200:203]\n"
"v_add_f32_e32 v120, v120, v243\n"
"v_add_f32_e32 v121, v121, v242\n"
"v_mfma_f32_16x16x32_bf16 v[200:203], a[172:175], v[184:187], v[200:203]\n"
"v_cmp_u_f32_e32 vcc, v172, v172\n"
"v_add_f32_e32 v122, v122, v241\n"
"v_mfma_f32_16x16x32_bf16 v[200:203], a[168:171], v[168:171], v[200:203]\n"
"s_cbranch_vccnz .Lmk_p113s\n"
".Lmk_p113r:\n"
"v_add_f32_e32 v123, v123, v240\n"
"v_mfma_f32_16x16x32_bf16 v[200:203], a[164:167], v[180:183], v[200:203]\n"
"buffer_load_dwordx4 v[140:143], v0, s[4:7], s98 offen offset:3072 sc1\n"
"v_mfma_f32_16x16x32_bf16 v[200:203], a[160:163], v[176:179], v[200:203]\n"
"v_add_f32_e32 v120, v120, v152\n"
"v_add_f32_e32 v121, v121, v153\n"
"v_mfma_f32_16x16x32_bf16 v[108:111], a[192:195], v[176:179], v[108:111]\n"
"v_add_f32_e32 v122, v122, v154\n"
"v_add_f32_e32 v123, v123, v155\n"
"v_mfma_f32_16x16x32_bf16 v[108:111], a[196:199], v[180:183], v[108:111]\n"
"buffer_load_dwordx4 v[144:147], v1, s[4:7], s98 offen sc1\n"
"v_mfma_f32_16x16x32_bf16 v[108:111], a[200:203], v[168:171], v[108:111]\n"
"ds_read_b128 v[204:207], v6\n"
"ds_read_b128 v[208:211], v6 offset:1024\n"
"v_mfma_f32_16x16x32_bf16 v[108:111], a[204:207], v[184:187], v[108:111]\n"
"ds_read_b128 v[148:151], v6 offset:2048\n"
"ds_read_b128 v[152:155], v6 offset:3072\n"
"v_mfma_f32_16x16x32_bf16 v[108:111], a[208:211], v[188:191], v[108:111]\n"
"buffer_load_dwordx4 v[156:159], v1, s[4:7], s98 offen offset:1024 sc1\n"
"v_mfma_f32_16x16x32_bf16 v[108:111], a[212:215], v[164:167], v[108:111]\n"
"v_mfma_f32_16x16x32_bf16 v[108:111], a[216:219], v[192:195], v[108:111]\n"
"v_mfma_f32_16x16x32_bf16 v[108:111], a[220:223], v[196:199], v[108:111]\n"
"buffer_load_dwordx4 v[160:163], v1, s[4:7], s98 offen offset:2048 sc1\n"
"v_mfma_f32_16x16x32_bf16 v[120:123], a[252:255], v[196:199], v[120:123]\n"
"s_waitcnt lgkmcnt(0)\n"
"v_add_f32_e32 v204, v172, v204\n"
"v_add_f32_e32 v205, v173, v205\n"
"v_mfma_f32_16x16x32_bf16 v[120:123], a[248:251], v[192:195], v[120:123]\n"
"v_add_f32_e32 v206, v174, v206\n"
"v_add_f32_e32 v207, v175, v207\n"
"v_mfma_f32_16x16x32_bf16 v[120:123], a[244:247], v[164:167], v[120:123]\n"
"buffer_load_dwordx4 v[164:167], v1, s[4:7], s98 offen offset:3072 sc1\n"
"v_mfma_f32_16x16x32_bf16 v[120:123], a[240:243], v[188:191], v[120:123]\n"
"v_add_f32_e32 v208, v200, v208\n"
"v_add_f32_e32 v209, v201, v209\n"
"v_mfma_f32_16x16x32_bf16 v[120:123], a[236:239], v[184:187], v[120:123]\n"
"v_add_f32_e32 v210, v202, v210\n"
"v_add_f32_e32 v211, v203, v211\n"
"v_mfma_f32_16x16x32_bf16 v[120:123], a[232:235], v[168:171], v[120:123]\n"
"v_cvt_pk_bf16_f32 v168, v204, v205\n"
"v_cvt_pk_bf16_f32 v169, v206, v207\n"
"v_mfma_f32_16x16x32_bf16 v[120:123], a[228:231], v[180:183], v[120:123]\n"
"s_add_u32 s98, s61, 0x400000\n"
"v_cvt_pk_bf16_f32 v170, v208, v209\n"
"v_cvt_pk_bf16_f32 v171, v210, v211\n"
"v_mfma_f32_16x16x32_bf16 v[120:123], a[224:227], v[176:179], v[120:123]\n"
"buffer_store_dwordx4 v[168:171], v0, s[4:7], s98 offen sc1\n"
"ds_write_b128 v2, v[168:171]\n"
"s_branch .Lmk_loop\n"
".Lmk_f2s:\n"
";\n"
"s_mov_b32 s1, 0x10000\n"
".Lmk_f2l:\n"
"buffer_load_dwordx2 v[254:255], v13, s[4:7], s98 offen sc1\n"
"s_waitcnt vmcnt(0)\n"
"v_cmp_ne_u64_e32 vcc, v[254:255], v[18:19]\n"
"s_cbranch_vccz .Lmk_f2r\n"
"s_sub_u32 s1, s1, 1\n"
"s_cbranch_scc0 .Lmk_f2l\n"
"s_branch .Lmk_f2r\n"
".Lmk_f1s:\n"
";\n"
"s_mov_b32 s1, 0x10000\n"
".Lmk_f1l:\n"
"buffer_load_dwordx2 v[254:255], v12, s[4:7], s98 offen sc1\n"
"s_waitcnt vmcnt(0)\n"
"v_cmp_ne_u64_e32 vcc, v[254:255], v[18:19]\n"
"s_cbranch_vccz .Lmk_f1r\n"
"s_sub_u32 s1, s1, 1\n"
"s_cbranch_scc0 .Lmk_f1l\n"
"s_branch .Lmk_f1r\n"
".Lmk_f6s:\n"
";\n"
"s_mov_b32 s1, 0x10000\n"
".Lmk_f6l:\n"
"buffer_load_dwordx2 v[250:251], v41, s[4:7], s97 offen sc1\n"
"s_waitcnt vmcnt(0)\n"
"v_cmp_ne_u64_e32 vcc, v[250:251], v[18:19]\n"
"s_cbranch_vccz .Lmk_f6r\n"
"s_sub_u32 s1, s1, 1\n"
"s_cbranch_scc0 .Lmk_f6l\n"
"s_branch .Lmk_f6r\n"
".Lmk_f3s:\n"
";\n"
"s_mov_b32 s1, 0x10000\n"
".Lmk_f3l:\n"
"buffer_load_dwordx2 v[246:247], v12, s[4:7], s97 offen sc1\n"
"s_waitcnt vmcnt(0)\n"
"v_cmp_ne_u64_e32 vcc, v[246:247], v[18:19]\n"
"s_cbranch_vccz .Lmk_f3r\n"
"s_sub_u32 s1, s1, 1\n"
"s_cbranch_scc0 .Lmk_f3l\n"
"s_branch .Lmk_f3r\n"
".Lmk_f7s:\n"
";\n"
"s_mov_b32 s1, 0x10000\n"
".Lmk_f7l:\n"
"buffer_load_dwordx2 v[246:247], v13, s[4:7], s98 offen sc1\n"
"s_waitcnt vmcnt(0)\n"
"v_cmp_ne_u64_e32 vcc, v[246:247], v[18:19]\n"
"s_cbranch_vccz .Lmk_f7r\n"
"s_sub_u32 s1, s1, 1\n"
"s_cbranch_scc0 .Lmk_f7l\n"
"s_branch .Lmk_f7r\n"
".Lmk_f4s:\n"
";\n"
"s_mov_b32 s1, 0x10000\n"
".Lmk_f4l:\n"
"buffer_load_dwordx2 v[250:251], off, s[4:7], s96 sc1\n"
"s_waitcnt vmcnt(0)\n"
"v_cmp_ne_u64_e32 vcc, v[250:251], v[18:19]\n"
"s_cbranch_vccz .Lmk_f4r\n"
"s_sub_u32 s1, s1, 1\n"
"s_cbranch_scc0 .Lmk_f4l\n"
"s_branch .Lmk_f4r\n"
".Lmk_x18s:\n"
"s_nop 3\n"
"v_mov_b32_e32 v24, 0xff800000\n"
"v_mov_b32_e32 v25, 0xff800000\n"
"v_mov_b32_e32 v26, 0xff800000\n"
"v_mov_b32_e32 v27, 0xff800000\n"
"v_mov_b32_e32 v36, 0xff800000\n"
"v_mov_b32_e32 v37, 0xff800000\n"
"v_mov_b32_e32 v38, 0xff800000\n"
"v_mov_b32_e32 v39, 0xff800000\n"
"s_nop 1\n"
"s_branch .Lmk_x18r\n"
".Lmk_x19s:\n"
"s_nop 3\n"
"v_mov_b32_e32 v24, 0xff800000\n"
"v_mov_b32_e32 v25, 0xff800000\n"
"v_mov_b32_e32 v26, 0xff800000\n"
"v_mov_b32_e32 v27, 0xff800000\n"
"v_mov_b32_e32 v36, 0xff800000\n"
"v_mov_b32_e32 v37, 0xff800000\n"
"v_mov_b32_e32 v38, 0xff800000\n"
"v_mov_b32_e32 v39, 0xff800000\n"
"s_nop 1\n"
"s_branch .Lmk_x19r\n"
".Lmk_x20s:\n"
"s_nop 3\n"
"v_mov_b32_e32 v24, 0xff800000\n"
"v_mov_b32_e32 v25, 0xff800000\n"
"v_mov_b32_e32 v26, 0xff800000\n"
"v_mov_b32_e32 v27, 0xff800000\n"
"v_mov_b32_e32 v36, 0xff800000\n"
"v_mov_b32_e32 v37, 0xff800000\n"
"v_mov_b32_e32 v38, 0xff800000\n"
"v_mov_b32_e32 v39, 0xff800000\n"
"s_nop 1\n"
"s_branch .Lmk_x20r\n"
".Lmk_x21s:\n"
"s_nop 3\n"
"v_mov_b32_e32 v24, 0xff800000\n"
"v_mov_b32_e32 v25, 0xff800000\n"
"v_mov_b32_e32 v26, 0xff800000\n"
"v_mov_b32_e32 v27, 0xff800000\n"
"v_mov_b32_e32 v36, 0xff800000\n"
"v_mov_b32_e32 v37, 0xff800000\n"
"v_mov_b32_e32 v38, 0xff800000\n"
"v_mov_b32_e32 v39, 0xff800000\n"
"s_nop 1\n"
"s_branch .Lmk_x21r\n"
".Lmk_f14s:\n"
";\n"
"s_mov_b32 s1, 0x10000\n"
".Lmk_f14l:\n"
"buffer_load_dwordx2 v[238:239], off, s[4:7], s97 sc1\n"
"s_waitcnt vmcnt(0)\n"
"v_cmp_ne_u64_e32 vcc, v[238:239], v[18:19]\n"
"s_cbranch_vccz .Lmk_f14r\n"
"s_sub_u32 s1, s1, 1\n"
"s_cbranch_scc0 .Lmk_f14l\n"
"s_branch .Lmk_f14r\n"
".Lmk_b16:\n"
"s_cmp_lg_u32 s2, 0\n"
"s_cbranch_scc1 .Lmk_k22\n"
"s_mov_b32 s2, 1\n"
"s_waitcnt vmcnt(0) lgkmcnt(0)\n"
"s_nop 7\n"
"s_mov_b32 s97, 11\n"
"v_add_u32_e32 v248, 0x3800, v15\n"
"v_add_u32_e32 v247, 0x3800, v254\n"
"ds_read_b128 v[36:39], v15 offset:24576\n"
"ds_read_b128 v[40:43], v254 offset:24832\n"
"s_waitcnt lgkmcnt(0)\n"
"v_mfma_f32_16x16x32_bf16 v[48:51], v[36:39], v[28:31], 0\n"
"v_mfma_f32_16x16x32_bf16 v[52:55], v[36:39], v[32:35], 0\n"
"v_mfma_f32_16x16x32_bf16 v[36:39], v[40:43], v[32:35], 0\n"
"v_mfma_f32_16x16x32_bf16 v[56:59], v[40:43], v[28:31], 0\n"
".Lmk_b16l:\n"
"ds_read_b128 v[40:43], v248 offset:8192\n"
"ds_read_b128 v[60:63], v247 offset:8448\n"
"s_cmp_gt_i32 s78, s97\n"
"s_cselect_b32 s96, 0xff800000, 0\n"
"v_mov_b32_e32 v64, s96\n"
"v_mov_b32_e32 v65, s96\n"
"v_mov_b32_e32 v66, s96\n"
"v_mov_b32_e32 v67, s96\n"
"v_subrev_u32_e32 v248, 0x800, v248\n"
"v_subrev_u32_e32 v247, 0x800, v247\n"
"s_waitcnt lgkmcnt(0)\n"
"v_mfma_f32_16x16x32_bf16 v[72:75], v[40:43], v[28:31], v[64:67]\n"
"v_mfma_f32_16x16x32_bf16 v[76:79], v[40:43], v[32:35], v[64:67]\n"
"v_mfma_f32_16x16x32_bf16 v[40:43], v[60:63], v[32:35], v[64:67]\n"
"v_mfma_f32_16x16x32_bf16 v[84:87], v[60:63], v[28:31], v[64:67]\n"
"s_nop 7\n"
"s_cmp_lg_u32 s97, 4\n"
"s_cbranch_scc1 .Lmk_b16m\n"
"v_cndmask_b32_e64 v72, v48, v72, s[20:21]\n"
"v_cndmask_b32_e64 v73, v49, v73, s[22:23]\n"
"v_cndmask_b32_e64 v74, v50, v74, s[24:25]\n"
"v_cndmask_b32_e64 v75, v51, v75, s[26:27]\n"
"v_cndmask_b32_e64 v84, v56, v84, s[28:29]\n"
"v_cndmask_b32_e64 v85, v57, v85, s[30:31]\n"
"v_cndmask_b32_e64 v86, v58, v86, s[32:33]\n"
"v_cndmask_b32_e64 v87, v59, v87, s[34:35]\n"
"v_cndmask_b32_e64 v76, v52, v76, s[36:37]\n"
"v_cndmask_b32_e64 v77, v53, v77, s[38:39]\n"
"v_cndmask_b32_e64 v78, v54, v78, s[40:41]\n"
"v_cndmask_b32_e64 v79, v55, v79, s[42:43]\n"
"v_cndmask_b32_e64 v40, v36, v40, s[44:45]\n"
"v_cndmask_b32_e64 v41, v37, v41, s[46:47]\n"
"v_cndmask_b32_e64 v42, v38, v42, s[48:49]\n"
"v_cndmask_b32_e64 v43, v39, v43, s[50:51]\n"
".Lmk_b16m:\n"
"v_max3_f32 v253, v253, v72, v73\n"
"v_max3_f32 v251, v251, v76, v77\n"
"v_max3_f32 v253, v253, v74, v75\n"
"v_max3_f32 v251, v251, v78, v79\n"
"v_max3_f32 v253, v253, v84, v85\n"
"v_max3_f32 v251, v251, v40, v41\n"
"v_max3_f32 v253, v253, v86, v87\n"
"v_max3_f32 v251, v251, v42, v43\n"
"s_sub_u32 s97, s97, 1\n"
"s_cmp_ge_i32 s97, 4\n"
"s_cbranch_scc1 .Lmk_b16l\n"
"v_mov_b32_e32 v248, v253\n"
"v_mov_b32_e32 v247, v251\n"
"s_nop 0\n"
"v_permlane16_swap_b32_e32 v253, v248\n"
"v_permlane16_swap_b32_e32 v251, v247\n"
"v_max_f32_e32 v253, v253, v248\n"
"v_max_f32_e32 v251, v251, v247\n"
"v_mov_b32_e32 v248, v253\n"
"v_mov_b32_e32 v247, v251\n"
"s_nop 0\n"
"v_permlane32_swap_b32_e32 v253, v248\n"
"v_permlane32_swap_b32_e32 v251, v247\n"
"v_max_f32_e32 v253, v253, v248\n"
"v_max_f32_e32 v251, v251, v247\n"
"v_mov_b32_e32 v250, v253\n"
"v_mov_b32_e32 v249, v251\n"
"s_branch .Lmk_a15\n"
".Lmk_x25s:\n"
"s_nop 3\n"
"v_mov_b32_e32 v116, 0xff800000\n"
"v_mov_b32_e32 v117, 0xff800000\n"
"v_mov_b32_e32 v118, 0xff800000\n"
"v_mov_b32_e32 v119, 0xff800000\n"
"v_mov_b32_e32 v120, 0xff800000\n"
"v_mov_b32_e32 v121, 0xff800000\n"
"v_mov_b32_e32 v122, 0xff800000\n"
"v_mov_b32_e32 v123, 0xff800000\n"
"s_nop 1\n"
"s_branch .Lmk_x25r\n"
".Lmk_x26s:\n"
"s_nop 3\n"
"v_mov_b32_e32 v116, 0xff800000\n"
"v_mov_b32_e32 v117, 0xff800000\n"
"v_mov_b32_e32 v118, 0xff800000\n"
"v_mov_b32_e32 v119, 0xff800000\n"
"v_mov_b32_e32 v120, 0xff800000\n"
"v_mov_b32_e32 v121, 0xff800000\n"
"v_mov_b32_e32 v122, 0xff800000\n"
"v_mov_b32_e32 v123, 0xff800000\n"
"s_nop 1\n"
"s_branch .Lmk_x26r\n"
".Lmk_x27s:\n"
"s_nop 3\n"
"v_mov_b32_e32 v116, 0xff800000\n"
"v_mov_b32_e32 v117, 0xff800000\n"
"v_mov_b32_e32 v118, 0xff800000\n"
"v_mov_b32_e32 v119, 0xff800000\n"
"v_mov_b32_e32 v120, 0xff800000\n"
"v_mov_b32_e32 v121, 0xff800000\n"
"v_mov_b32_e32 v122, 0xff800000\n"
"v_mov_b32_e32 v123, 0xff800000\n"
"s_nop 1\n"
"s_branch .Lmk_x27r\n"
".Lmk_f5s:\n"
";\n"
"s_mov_b32 s1, 0x10000\n"
".Lmk_f5l:\n"
"buffer_load_dwordx2 v[246:247], v12, s[4:7], s96 offen sc1\n"
"s_waitcnt vmcnt(0)\n"
"v_cmp_ne_u64_e32 vcc, v[246:247], v[18:19]\n"
"s_cbranch_vccz .Lmk_f5r\n"
"s_sub_u32 s1, s1, 1\n"
"s_cbranch_scc0 .Lmk_f5l\n"
"s_branch .Lmk_f5r\n"
".Lmk_b24:\n"
"s_cmp_lg_u32 s2, 0\n"
"s_cbranch_scc1 .Lmk_k29\n"
"s_mov_b32 s2, 1\n"
"s_waitcnt vmcnt(0) lgkmcnt(0)\n"
"s_nop 7\n"
"s_mov_b32 s96, 7\n"
"v_add_u32_e32 v248, 0x3800, v15\n"
"v_add_u32_e32 v247, 0x3800, v254\n"
"ds_read_b128 v[172:175], v15 offset:16384\n"
"ds_read_b128 v[192:195], v254 offset:16640\n"
"s_waitcnt lgkmcnt(0)\n"
"v_mfma_f32_16x16x32_bf16 v[196:199], v[172:175], v[92:95], 0\n"
"v_mfma_f32_16x16x32_bf16 v[212:215], v[172:175], v[96:99], 0\n"
"v_mfma_f32_16x16x32_bf16 v[172:175], v[192:195], v[96:99], 0\n"
"v_mfma_f32_16x16x32_bf16 v[220:223], v[192:195], v[92:95], 0\n"
".Lmk_b24l:\n"
"ds_read_b128 v[192:195], v248\n"
"ds_read_b128 v[224:227], v247 offset:256\n"
"s_cmp_gt_i32 s78, s96\n"
"s_cselect_b32 s95, 0xff800000, 0\n"
"v_mov_b32_e32 v228, s95\n"
"v_mov_b32_e32 v229, s95\n"
"v_mov_b32_e32 v230, s95\n"
"v_mov_b32_e32 v231, s95\n"
"v_subrev_u32_e32 v248, 0x800, v248\n"
"v_subrev_u32_e32 v247, 0x800, v247\n"
"s_waitcnt lgkmcnt(0)\n"
"v_mfma_f32_16x16x32_bf16 v[232:235], v[192:195], v[92:95], v[228:231]\n"
"v_mfma_f32_16x16x32_bf16 v[236:239], v[192:195], v[96:99], v[228:231]\n"
"v_mfma_f32_16x16x32_bf16 v[192:195], v[224:227], v[96:99], v[228:231]\n"
"v_mfma_f32_16x16x32_bf16 v[240:243], v[224:227], v[92:95], v[228:231]\n"
"s_nop 7\n"
"s_cmp_lg_u32 s96, 0\n"
"s_cbranch_scc1 .Lmk_b24m\n"
"v_cndmask_b32_e64 v232, v196, v232, s[20:21]\n"
"v_cndmask_b32_e64 v233, v197, v233, s[22:23]\n"
"v_cndmask_b32_e64 v234, v198, v234, s[24:25]\n"
"v_cndmask_b32_e64 v235, v199, v235, s[26:27]\n"
"v_cndmask_b32_e64 v240, v220, v240, s[28:29]\n"
"v_cndmask_b32_e64 v241, v221, v241, s[30:31]\n"
"v_cndmask_b32_e64 v242, v222, v242, s[32:33]\n"
"v_cndmask_b32_e64 v243, v223, v243, s[34:35]\n"
"v_cndmask_b32_e64 v236, v212, v236, s[36:37]\n"
"v_cndmask_b32_e64 v237, v213, v237, s[38:39]\n"
"v_cndmask_b32_e64 v238, v214, v238, s[40:41]\n"
"v_cndmask_b32_e64 v239, v215, v239, s[42:43]\n"
"v_cndmask_b32_e64 v192, v172, v192, s[44:45]\n"
"v_cndmask_b32_e64 v193, v173, v193, s[46:47]\n"
"v_cndmask_b32_e64 v194, v174, v194, s[48:49]\n"
"v_cndmask_b32_e64 v195, v175, v195, s[50:51]\n"
".Lmk_b24m:\n"
"v_max3_f32 v253, v253, v232, v233\n"
"v_max3_f32 v251, v251, v236, v237\n"
"v_max3_f32 v253, v253, v234, v235\n"
"v_max3_f32 v251, v251, v238, v239\n"
"v_max3_f32 v253, v253, v240, v241\n"
"v_max3_f32 v251, v251, v192, v193\n"
"v_max3_f32 v253, v253, v242, v243\n"
"v_max3_f32 v251, v251, v194, v195\n"
"s_sub_u32 s96, s96, 1\n"
"s_cmp_ge_i32 s96, 0\n"
"s_cbranch_scc1 .Lmk_b24l\n"
"v_mov_b32_e32 v248, v253\n"
"v_mov_b32_e32 v247, v251\n"
"s_nop 0\n"
"v_permlane16_swap_b32_e32 v253, v248\n"
"v_permlane16_swap_b32_e32 v251, v247\n"
"v_max_f32_e32 v253, v253, v248\n"
"v_max_f32_e32 v251, v251, v247\n"
"v_mov_b32_e32 v248, v253\n"
"v_mov_b32_e32 v247, v251\n"
"s_nop 0\n"
"v_permlane32_swap_b32_e32 v253, v248\n"
"v_permlane32_swap_b32_e32 v251, v247\n"
"v_max_f32_e32 v253, v253, v248\n"
"v_max_f32_e32 v251, v251, v247\n"
"v_mov_b32_e32 v250, v253\n"
"v_mov_b32_e32 v249, v251\n"
"s_branch .Lmk_a23\n"
".Lmk_p30s:\n"
";\n"
"s_waitcnt vmcnt(16)\n"
"s_mov_b32 s1, 0x10000\n"
".Lmk_p30l:\n"
"s_add_u32 s98, s56, 0x40000\n"
"buffer_load_dwordx4 v[176:179], v0, s[4:7], s98 offen sc1\n"
"buffer_load_dwordx4 v[200:203], v0, s[4:7], s98 offen offset:1024 sc1\n"
"buffer_load_dwordx4 v[188:191], v0, s[4:7], s98 offen offset:2048 sc1\n"
"buffer_load_dwordx4 v[204:207], v0, s[4:7], s98 offen offset:3072 sc1\n"
"buffer_load_dwordx4 v[152:155], v1, s[4:7], s98 offen sc1\n"
"buffer_load_dwordx4 v[208:211], v1, s[4:7], s98 offen offset:1024 sc1\n"
"buffer_load_dwordx4 v[120:123], v1, s[4:7], s98 offen offset:2048 sc1\n"
"buffer_load_dwordx4 v[132:135], v1, s[4:7], s98 offen offset:3072 sc1\n"
"s_add_u32 s98, s57, 0x40000\n"
"buffer_load_dwordx4 v[216:219], v0, s[4:7], s98 offen sc1\n"
"buffer_load_dwordx4 v[128:131], v0, s[4:7], s98 offen offset:1024 sc1\n"
"buffer_load_dwordx4 v[180:183], v0, s[4:7], s98 offen offset:2048 sc1\n"
"buffer_load_dwordx4 v[100:103], v0, s[4:7], s98 offen offset:3072 sc1\n"
"buffer_load_dwordx4 v[184:187], v1, s[4:7], s98 offen sc1\n"
"buffer_load_dwordx4 v[104:107], v1, s[4:7], s98 offen offset:1024 sc1\n"
"buffer_load_dwordx4 v[108:111], v1, s[4:7], s98 offen offset:2048 sc1\n"
"buffer_load_dwordx4 v[136:139], v1, s[4:7], s98 offen offset:3072 sc1\n"
"s_add_u32 s98, s58, 0x40000\n"
"buffer_load_dwordx4 v[144:147], v0, s[4:7], s98 offen sc1\n"
"buffer_load_dwordx4 v[140:143], v0, s[4:7], s98 offen offset:1024 sc1\n"
"buffer_load_dwordx4 v[116:119], v0, s[4:7], s98 offen offset:2048 sc1\n"
"buffer_load_dwordx4 v[112:115], v0, s[4:7], s98 offen offset:3072 sc1\n"
"buffer_load_dwordx4 v[92:95], v1, s[4:7], s98 offen sc1\n"
"buffer_load_dwordx4 v[96:99], v1, s[4:7], s98 offen offset:1024 sc1\n"
"buffer_load_dwordx4 v[148:151], v1, s[4:7], s98 offen offset:2048 sc1\n"
"s_waitcnt vmcnt(0)\n"
"v_max_u32_e32 v255, v176, v177\n"
"v_max3_u32 v255, v255, v178, v179\n"
"v_max3_u32 v255, v255, v200, v201\n"
"v_max3_u32 v255, v255, v202, v203\n"
"v_max3_u32 v255, v255, v188, v189\n"
"v_max3_u32 v255, v255, v190, v191\n"
"v_max3_u32 v255, v255, v204, v205\n"
"v_max3_u32 v255, v255, v206, v207\n"
"v_max3_u32 v255, v255, v152, v153\n"
"v_max3_u32 v255, v255, v154, v155\n"
"v_max3_u32 v255, v255, v208, v209\n"
"v_max3_u32 v255, v255, v210, v211\n"
"v_max3_u32 v255, v255, v120, v121\n"
"v_max3_u32 v255, v255, v122, v123\n"
"v_max3_u32 v255, v255, v132, v133\n"
"v_max3_u32 v255, v255, v134, v135\n"
"v_cmp_eq_u32_e32 vcc, -1, v255\n"
"s_cbranch_vccz .Lmk_p30k\n"
"s_sub_u32 s1, s1, 1\n"
"s_cbranch_scc0 .Lmk_p30l\n"
".Lmk_p30k:\n"
"v_mfma_f32_16x16x32_bf16 v[88:91], a[128:131], v[176:179], 0\n"
"v_mfma_f32_16x16x32_bf16 v[88:91], a[132:135], v[200:203], v[88:91]\n"
"v_mfma_f32_16x16x32_bf16 v[88:91], a[136:139], v[188:191], v[88:91]\n"
"v_mfma_f32_16x16x32_bf16 v[88:91], a[140:143], v[204:207], v[88:91]\n"
"v_mfma_f32_16x16x32_bf16 v[88:91], a[144:147], v[152:155], v[88:91]\n"
"v_mfma_f32_16x16x32_bf16 v[88:91], a[148:151], v[208:211], v[88:91]\n"
"v_mfma_f32_16x16x32_bf16 v[88:91], a[152:155], v[120:123], v[88:91]\n"
"v_mfma_f32_16x16x32_bf16 v[88:91], a[156:159], v[132:135], v[88:91]\n"
"v_mfma_f32_16x16x32_bf16 v[124:127], a[188:191], v[132:135], 0\n"
"v_mfma_f32_16x16x32_bf16 v[124:127], a[184:187], v[120:123], v[124:127]\n"
"v_mfma_f32_16x16x32_bf16 v[124:127], a[180:183], v[208:211], v[124:127]\n"
"v_mfma_f32_16x16x32_bf16 v[124:127], a[176:179], v[152:155], v[124:127]\n"
"v_mfma_f32_16x16x32_bf16 v[124:127], a[172:175], v[204:207], v[124:127]\n"
"v_mfma_f32_16x16x32_bf16 v[124:127], a[168:171], v[188:191], v[124:127]\n"
"s_nop 7\n"
"s_branch .Lmk_p30r\n"
".Lmk_p31s:\n"
";\n"
"s_waitcnt vmcnt(16)\n"
"s_mov_b32 s1, 0x10000\n"
".Lmk_p31l:\n"
"s_add_u32 s97, s57, 0x40000\n"
"buffer_load_dwordx4 v[216:219], v0, s[4:7], s97 offen sc1\n"
"buffer_load_dwordx4 v[128:131], v0, s[4:7], s97 offen offset:1024 sc1\n"
"buffer_load_dwordx4 v[180:183], v0, s[4:7], s97 offen offset:2048 sc1\n"
"buffer_load_dwordx4 v[100:103], v0, s[4:7], s97 offen offset:3072 sc1\n"
"buffer_load_dwordx4 v[184:187], v1, s[4:7], s97 offen sc1\n"
"buffer_load_dwordx4 v[104:107], v1, s[4:7], s97 offen offset:1024 sc1\n"
"buffer_load_dwordx4 v[108:111], v1, s[4:7], s97 offen offset:2048 sc1\n"
"buffer_load_dwordx4 v[136:139], v1, s[4:7], s97 offen offset:3072 sc1\n"
"s_add_u32 s97, s58, 0x40000\n"
"buffer_load_dwordx4 v[144:147], v0, s[4:7], s97 offen sc1\n"
"buffer_load_dwordx4 v[140:143], v0, s[4:7], s97 offen offset:1024 sc1\n"
"buffer_load_dwordx4 v[116:119], v0, s[4:7], s97 offen offset:2048 sc1\n"
"buffer_load_dwordx4 v[112:115], v0, s[4:7], s97 offen offset:3072 sc1\n"
"buffer_load_dwordx4 v[92:95], v1, s[4:7], s97 offen sc1\n"
"buffer_load_dwordx4 v[96:99], v1, s[4:7], s97 offen offset:1024 sc1\n"
"buffer_load_dwordx4 v[148:151], v1, s[4:7], s97 offen offset:2048 sc1\n"
"buffer_load_dwordx4 v[88:91], v1, s[4:7], s97 offen offset:3072 sc1\n"
"s_add_u32 s97, s59, 0x40000\n"
"buffer_load_dwordx4 v[160:163], v0, s[4:7], s97 offen sc1\n"
"buffer_load_dwordx4 v[124:127], v0, s[4:7], s97 offen offset:1024 sc1\n"
"buffer_load_dwordx4 v[120:123], v0, s[4:7], s97 offen offset:2048 sc1\n"
"buffer_load_dwordx4 v[132:135], v0, s[4:7], s97 offen offset:3072 sc1\n"
"buffer_load_dwordx4 v[156:159], v1, s[4:7], s97 offen sc1\n"
"buffer_load_dwordx4 v[164:167], v1, s[4:7], s97 offen offset:1024 sc1\n"
"buffer_load_dwordx4 v[172:175], v1, s[4:7], s97 offen offset:2048 sc1\n"
"s_waitcnt vmcnt(0)\n"
"v_max_u32_e32 v255, v216, v217\n"
"v_max3_u32 v255, v255, v218, v219\n"
"v_max3_u32 v255, v255, v128, v129\n"
"v_max3_u32 v255, v255, v130, v131\n"
"v_max3_u32 v255, v255, v180, v181\n"
"v_max3_u32 v255, v255, v182, v183\n"
"v_max3_u32 v255, v255, v100, v101\n"
"v_max3_u32 v255, v255, v102, v103\n"
"v_max3_u32 v255, v255, v184, v185\n"
"v_max3_u32 v255, v255, v186, v187\n"
"v_max3_u32 v255, v255, v104, v105\n"
"v_max3_u32 v255, v255, v106, v107\n"
"v_max3_u32 v255, v255, v108, v109\n"
"v_max3_u32 v255, v255, v110, v111\n"
"v_max3_u32 v255, v255, v136, v137\n"
"v_max3_u32 v255, v255, v138, v139\n"
"v_cmp_eq_u32_e32 vcc, -1, v255\n"
"s_cbranch_vccz .Lmk_p31k\n"
"s_sub_u32 s1, s1, 1\n"
"s_cbranch_scc0 .Lmk_p31l\n"
".Lmk_p31k:\n"
"v_mfma_f32_16x16x32_bf16 v[152:155], a[128:131], v[216:219], 0\n"
"v_mfma_f32_16x16x32_bf16 v[152:155], a[132:135], v[128:131], v[152:155]\n"
"v_mfma_f32_16x16x32_bf16 v[152:155], a[136:139], v[180:183], v[152:155]\n"
"v_mfma_f32_16x16x32_bf16 v[152:155], a[140:143], v[100:103], v[152:155]\n"
"v_mfma_f32_16x16x32_bf16 v[152:155], a[144:147], v[184:187], v[152:155]\n"
"v_mfma_f32_16x16x32_bf16 v[152:155], a[148:151], v[104:107], v[152:155]\n"
"v_mfma_f32_16x16x32_bf16 v[152:155], a[152:155], v[108:111], v[152:155]\n"
"v_mfma_f32_16x16x32_bf16 v[152:155], a[156:159], v[136:139], v[152:155]\n"
"v_mfma_f32_16x16x32_bf16 v[168:171], a[188:191], v[136:139], 0\n"
"v_mfma_f32_16x16x32_bf16 v[168:171], a[184:187], v[108:111], v[168:171]\n"
"v_mfma_f32_16x16x32_bf16 v[168:171], a[180:183], v[104:107], v[168:171]\n"
"v_mfma_f32_16x16x32_bf16 v[168:171], a[176:179], v[184:187], v[168:171]\n"
"v_mfma_f32_16x16x32_bf16 v[168:171], a[172:175], v[100:103], v[168:171]\n"
"v_mfma_f32_16x16x32_bf16 v[168:171], a[168:171], v[180:183], v[168:171]\n"
"s_nop 7\n"
"s_branch .Lmk_p31r\n"
".Lmk_p32s:\n"
";\n"
"s_waitcnt vmcnt(16)\n"
"s_mov_b32 s1, 0x10000\n"
".Lmk_p32l:\n"
"s_add_u32 s97, s58, 0x40000\n"
"buffer_load_dwordx4 v[144:147], v0, s[4:7], s97 offen sc1\n"
"buffer_load_dwordx4 v[140:143], v0, s[4:7], s97 offen offset:1024 sc1\n"
"buffer_load_dwordx4 v[116:119], v0, s[4:7], s97 offen offset:2048 sc1\n"
"buffer_load_dwordx4 v[112:115], v0, s[4:7], s97 offen offset:3072 sc1\n"
"buffer_load_dwordx4 v[92:95], v1, s[4:7], s97 offen sc1\n"
"buffer_load_dwordx4 v[96:99], v1, s[4:7], s97 offen offset:1024 sc1\n"
"buffer_load_dwordx4 v[148:151], v1, s[4:7], s97 offen offset:2048 sc1\n"
"buffer_load_dwordx4 v[88:91], v1, s[4:7], s97 offen offset:3072 sc1\n"
"s_add_u32 s97, s59, 0x40000\n"
"buffer_load_dwordx4 v[160:163], v0, s[4:7], s97 offen sc1\n"
"buffer_load_dwordx4 v[124:127], v0, s[4:7], s97 offen offset:1024 sc1\n"
"buffer_load_dwordx4 v[120:123], v0, s[4:7], s97 offen offset:2048 sc1\n"
"buffer_load_dwordx4 v[132:135], v0, s[4:7], s97 offen offset:3072 sc1\n"
"buffer_load_dwordx4 v[156:159], v1, s[4:7], s97 offen sc1\n"
"buffer_load_dwordx4 v[164:167], v1, s[4:7], s97 offen offset:1024 sc1\n"
"buffer_load_dwordx4 v[172:175], v1, s[4:7], s97 offen offset:2048 sc1\n"
"buffer_load_dwordx4 v[152:155], v1, s[4:7], s97 offen offset:3072 sc1\n"
"s_waitcnt vmcnt(0)\n"
"v_max_u32_e32 v239, v144, v145\n"
"v_max3_u32 v239, v239, v146, v147\n"
"v_max3_u32 v239, v239, v140, v141\n"
"v_max3_u32 v239, v239, v142, v143\n"
"v_max3_u32 v239, v239, v116, v117\n"
"v_max3_u32 v239, v239, v118, v119\n"
"v_max3_u32 v239, v239, v112, v113\n"
"v_max3_u32 v239, v239, v114, v115\n"
"v_max3_u32 v239, v239, v92, v93\n"
"v_max3_u32 v239, v239, v94, v95\n"
"v_max3_u32 v239, v239, v96, v97\n"
"v_max3_u32 v239, v239, v98, v99\n"
"v_max3_u32 v239, v239, v148, v149\n"
"v_max3_u32 v239, v239, v150, v151\n"
"v_max3_u32 v239, v239, v88, v89\n"
"v_max3_u32 v239, v239, v90, v91\n"
"v_cmp_eq_u32_e32 vcc, -1, v239\n"
"s_cbranch_vccz .Lmk_p32k\n"
"s_sub_u32 s1, s1, 1\n"
"s_cbranch_scc0 .Lmk_p32l\n"
".Lmk_p32k:\n"
"v_mfma_f32_16x16x32_bf16 v[100:103], a[128:131], v[144:147], 0\n"
"v_mfma_f32_16x16x32_bf16 v[100:103], a[132:135], v[140:143], v[100:103]\n"
"v_mfma_f32_16x16x32_bf16 v[100:103], a[136:139], v[116:119], v[100:103]\n"
"v_mfma_f32_16x16x32_bf16 v[100:103], a[140:143], v[112:115], v[100:103]\n"
"v_mfma_f32_16x16x32_bf16 v[100:103], a[144:147], v[92:95], v[100:103]\n"
"v_mfma_f32_16x16x32_bf16 v[100:103], a[148:151], v[96:99], v[100:103]\n"
"v_mfma_f32_16x16x32_bf16 v[100:103], a[152:155], v[148:151], v[100:103]\n"
"v_mfma_f32_16x16x32_bf16 v[100:103], a[156:159], v[88:91], v[100:103]\n"
"v_mfma_f32_16x16x32_bf16 v[104:107], a[188:191], v[88:91], 0\n"
"v_mfma_f32_16x16x32_bf16 v[104:107], a[184:187], v[148:151], v[104:107]\n"
"v_mfma_f32_16x16x32_bf16 v[104:107], a[180:183], v[96:99], v[104:107]\n"
"v_mfma_f32_16x16x32_bf16 v[104:107], a[176:179], v[92:95], v[104:107]\n"
"v_mfma_f32_16x16x32_bf16 v[104:107], a[172:175], v[112:115], v[104:107]\n"
"v_mfma_f32_16x16x32_bf16 v[104:107], a[168:171], v[116:119], v[104:107]\n"
"s_nop 7\n"
"s_branch .Lmk_p32r\n"
".Lmk_p33s:\n"
";\n"
"s_waitcnt vmcnt(16)\n"
"s_mov_b32 s1, 0x10000\n"
".Lmk_p33l:\n"
"s_add_u32 s97, s59, 0x40000\n"
"buffer_load_dwordx4 v[160:163], v0, s[4:7], s97 offen sc1\n"
"buffer_load_dwordx4 v[124:127], v0, s[4:7], s97 offen offset:1024 sc1\n"
"buffer_load_dwordx4 v[120:123], v0, s[4:7], s97 offen offset:2048 sc1\n"
"buffer_load_dwordx4 v[132:135], v0, s[4:7], s97 offen offset:3072 sc1\n"
"buffer_load_dwordx4 v[156:159], v1, s[4:7], s97 offen sc1\n"
"buffer_load_dwordx4 v[164:167], v1, s[4:7], s97 offen offset:1024 sc1\n"
"buffer_load_dwordx4 v[172:175], v1, s[4:7], s97 offen offset:2048 sc1\n"
"buffer_load_dwordx4 v[152:155], v1, s[4:7], s97 offen offset:3072 sc1\n"
"s_add_u32 s97, s56, 0\n"
"buffer_load_dwordx4 v[100:103], v0, s[4:7], s97 offen sc1\n"
"buffer_load_dwordx4 v[180:183], v0, s[4:7], s97 offen offset:1024 sc1\n"
"buffer_load_dwordx4 v[104:107], v0, s[4:7], s97 offen offset:2048 sc1\n"
"buffer_load_dwordx4 v[88:91], v0, s[4:7], s97 offen offset:3072 sc1\n"
"buffer_load_dwordx4 v[92:95], v1, s[4:7], s97 offen sc1\n"
"buffer_load_dwordx4 v[116:119], v1, s[4:7], s97 offen offset:1024 sc1\n"
"buffer_load_dwordx4 v[136:139], v1, s[4:7], s97 offen offset:2048 sc1\n"
"buffer_load_dwordx4 v[140:143], v1, s[4:7], s97 offen offset:3072 sc1\n"
"s_add_u32 s97, s57, 0\n"
"buffer_load_dwordx4 v[148:151], v0, s[4:7], s97 offen sc1\n"
"s_waitcnt vmcnt(0)\n"
"v_max_u32_e32 v255, v160, v161\n"
"v_max3_u32 v255, v255, v162, v163\n"
"v_max3_u32 v255, v255, v124, v125\n"
"v_max3_u32 v255, v255, v126, v127\n"
"v_max3_u32 v255, v255, v120, v121\n"
"v_max3_u32 v255, v255, v122, v123\n"
"v_max3_u32 v255, v255, v132, v133\n"
"v_max3_u32 v255, v255, v134, v135\n"
"v_max3_u32 v255, v255, v156, v157\n"
"v_max3_u32 v255, v255, v158, v159\n"
"v_max3_u32 v255, v255, v164, v165\n"
"v_max3_u32 v255, v255, v166, v167\n"
"v_max3_u32 v255, v255, v172, v173\n"
"v_max3_u32 v255, v255, v174, v175\n"
"v_max3_u32 v255, v255, v152, v153\n"
"v_max3_u32 v255, v255, v154, v155\n"
"v_cmp_eq_u32_e32 vcc, -1, v255\n"
"s_cbranch_vccz .Lmk_p33k\n"
"s_sub_u32 s1, s1, 1\n"
"s_cbranch_scc0 .Lmk_p33l\n"
".Lmk_p33k:\n"
"v_mfma_f32_16x16x32_bf16 v[128:131], a[128:131], v[160:163], v[108:111]\n"
"v_mfma_f32_16x16x32_bf16 v[128:131], a[132:135], v[124:127], v[128:131]\n"
"v_mfma_f32_16x16x32_bf16 v[128:131], a[136:139], v[120:123], v[128:131]\n"
"v_mfma_f32_16x16x32_bf16 v[128:131], a[140:143], v[132:135], v[128:131]\n"
"v_mfma_f32_16x16x32_bf16 v[128:131], a[144:147], v[156:159], v[128:131]\n"
"v_mfma_f32_16x16x32_bf16 v[128:131], a[148:151], v[164:167], v[128:131]\n"
"v_mfma_f32_16x16x32_bf16 v[128:131], a[152:155], v[172:175], v[128:131]\n"
"v_mfma_f32_16x16x32_bf16 v[128:131], a[156:159], v[152:155], v[128:131]\n"
"v_mfma_f32_16x16x32_bf16 v[144:147], a[188:191], v[152:155], v[168:171]\n"
"v_mfma_f32_16x16x32_bf16 v[144:147], a[184:187], v[172:175], v[144:147]\n"
"v_mfma_f32_16x16x32_bf16 v[144:147], a[180:183], v[164:167], v[144:147]\n"
"v_mfma_f32_16x16x32_bf16 v[144:147], a[176:179], v[156:159], v[144:147]\n"
"v_mfma_f32_16x16x32_bf16 v[144:147], a[172:175], v[132:135], v[144:147]\n"
"v_mfma_f32_16x16x32_bf16 v[144:147], a[168:171], v[120:123], v[144:147]\n"
"s_nop 7\n"
"s_branch .Lmk_p33r\n"
".Lmk_p35s:\n"
";\n"
"s_waitcnt vmcnt(16)\n"
"s_mov_b32 s1, 0x10000\n"
".Lmk_p35l:\n"
"s_add_u32 s95, s56, 0\n"
"buffer_load_dwordx4 v[100:103], v0, s[4:7], s95 offen sc1\n"
"buffer_load_dwordx4 v[180:183], v0, s[4:7], s95 offen offset:1024 sc1\n"
"buffer_load_dwordx4 v[104:107], v0, s[4:7], s95 offen offset:2048 sc1\n"
"buffer_load_dwordx4 v[88:91], v0, s[4:7], s95 offen offset:3072 sc1\n"
"buffer_load_dwordx4 v[92:95], v1, s[4:7], s95 offen sc1\n"
"buffer_load_dwordx4 v[116:119], v1, s[4:7], s95 offen offset:1024 sc1\n"
"buffer_load_dwordx4 v[136:139], v1, s[4:7], s95 offen offset:2048 sc1\n"
"buffer_load_dwordx4 v[140:143], v1, s[4:7], s95 offen offset:3072 sc1\n"
"s_add_u32 s95, s57, 0\n"
"buffer_load_dwordx4 v[148:151], v0, s[4:7], s95 offen sc1\n"
"buffer_load_dwordx4 v[108:111], v0, s[4:7], s95 offen offset:1024 sc1\n"
"buffer_load_dwordx4 v[112:115], v0, s[4:7], s95 offen offset:2048 sc1\n"
"buffer_load_dwordx4 v[196:199], v0, s[4:7], s95 offen offset:3072 sc1\n"
"buffer_load_dwordx4 v[200:203], v1, s[4:7], s95 offen sc1\n"
"buffer_load_dwordx4 v[128:131], v1, s[4:7], s95 offen offset:1024 sc1\n"
"buffer_load_dwordx4 v[120:123], v1, s[4:7], s95 offen offset:2048 sc1\n"
"buffer_load_dwordx4 v[132:135], v1, s[4:7], s95 offen offset:3072 sc1\n"
"s_add_u32 s95, s58, 0\n"
"buffer_load_dwordx4 v[152:155], v0, s[4:7], s95 offen sc1\n"
"buffer_load_dwordx4 v[160:163], v0, s[4:7], s95 offen offset:1024 sc1\n"
"buffer_load_dwordx4 v[124:127], v0, s[4:7], s95 offen offset:2048 sc1\n"
"s_waitcnt vmcnt(0)\n"
"v_max_u32_e32 v255, v100, v101\n"
"v_max3_u32 v255, v255, v102, v103\n"
"v_max3_u32 v255, v255, v180, v181\n"
"v_max3_u32 v255, v255, v182, v183\n"
"v_max3_u32 v255, v255, v104, v105\n"
"v_max3_u32 v255, v255, v106, v107\n"
"v_max3_u32 v255, v255, v88, v89\n"
"v_max3_u32 v255, v255, v90, v91\n"
"v_max3_u32 v255, v255, v92, v93\n"
"v_max3_u32 v255, v255, v94, v95\n"
"v_max3_u32 v255, v255, v116, v117\n"
"v_max3_u32 v255, v255, v118, v119\n"
"v_max3_u32 v255, v255, v136, v137\n"
"v_max3_u32 v255, v255, v138, v139\n"
"v_max3_u32 v255, v255, v140, v141\n"
"v_max3_u32 v255, v255, v142, v143\n"
"v_cmp_eq_u32_e32 vcc, -1, v255\n"
"s_cbranch_vccz .Lmk_p35k\n"
"s_sub_u32 s1, s1, 1\n"
"s_cbranch_scc0 .Lmk_p35l\n"
".Lmk_p35k:\n"
"v_mfma_f32_16x16x32_bf16 v[144:147], a[128:131], v[100:103], 0\n"
"v_mfma_f32_16x16x32_bf16 v[144:147], a[132:135], v[180:183], v[144:147]\n"
"v_mfma_f32_16x16x32_bf16 v[144:147], a[136:139], v[104:107], v[144:147]\n"
"v_mfma_f32_16x16x32_bf16 v[144:147], a[140:143], v[88:91], v[144:147]\n"
"v_mfma_f32_16x16x32_bf16 v[144:147], a[144:147], v[92:95], v[144:147]\n"
"v_mfma_f32_16x16x32_bf16 v[144:147], a[148:151], v[116:119], v[144:147]\n"
"v_mfma_f32_16x16x32_bf16 v[144:147], a[152:155], v[136:139], v[144:147]\n"
"v_mfma_f32_16x16x32_bf16 v[144:147], a[156:159], v[140:143], v[144:147]\n"
"v_mfma_f32_16x16x32_bf16 v[96:99], a[188:191], v[140:143], 0\n"
"v_mfma_f32_16x16x32_bf16 v[96:99], a[184:187], v[136:139], v[96:99]\n"
"v_mfma_f32_16x16x32_bf16 v[96:99], a[180:183], v[116:119], v[96:99]\n"
"v_mfma_f32_16x16x32_bf16 v[96:99], a[176:179], v[92:95], v[96:99]\n"
"v_mfma_f32_16x16x32_bf16 v[96:99], a[172:175], v[88:91], v[96:99]\n"
"v_mfma_f32_16x16x32_bf16 v[96:99], a[168:171], v[104:107], v[96:99]\n"
"s_nop 7\n"
"s_branch .Lmk_p35r\n"
".Lmk_p36s:\n"
";\n"
"s_waitcnt vmcnt(16)\n"
"s_mov_b32 s1, 0x10000\n"
".Lmk_p36l:\n"
"s_add_u32 s97, s57, 0\n"
"buffer_load_dwordx4 v[148:151], v0, s[4:7], s97 offen sc1\n"
"buffer_load_dwordx4 v[108:111], v0, s[4:7], s97 offen offset:1024 sc1\n"
"buffer_load_dwordx4 v[112:115], v0, s[4:7], s97 offen offset:2048 sc1\n"
"buffer_load_dwordx4 v[196:199], v0, s[4:7], s97 offen offset:3072 sc1\n"
"buffer_load_dwordx4 v[200:203], v1, s[4:7], s97 offen sc1\n"
"buffer_load_dwordx4 v[128:131], v1, s[4:7], s97 offen offset:1024 sc1\n"
"buffer_load_dwordx4 v[120:123], v1, s[4:7], s97 offen offset:2048 sc1\n"
"buffer_load_dwordx4 v[132:135], v1, s[4:7], s97 offen offset:3072 sc1\n"
"s_add_u32 s97, s58, 0\n"
"buffer_load_dwordx4 v[152:155], v0, s[4:7], s97 offen sc1\n"
"buffer_load_dwordx4 v[160:163], v0, s[4:7], s97 offen offset:1024 sc1\n"
"buffer_load_dwordx4 v[124:127], v0, s[4:7], s97 offen offset:2048 sc1\n"
"buffer_load_dwordx4 v[144:147], v0, s[4:7], s97 offen offset:3072 sc1\n"
"buffer_load_dwordx4 v[156:159], v1, s[4:7], s97 offen sc1\n"
"buffer_load_dwordx4 v[172:175], v1, s[4:7], s97 offen offset:1024 sc1\n"
"buffer_load_dwordx4 v[116:119], v1, s[4:7], s97 offen offset:2048 sc1\n"
"buffer_load_dwordx4 v[136:139], v1, s[4:7], s97 offen offset:3072 sc1\n"
"s_add_u32 s97, s59, 0\n"
"buffer_load_dwordx4 v[176:179], v0, s[4:7], s97 offen sc1\n"
"buffer_load_dwordx4 v[180:183], v0, s[4:7], s97 offen offset:1024 sc1\n"
"buffer_load_dwordx4 v[168:171], v0, s[4:7], s97 offen offset:2048 sc1\n"
"s_waitcnt vmcnt(0)\n"
"v_max_u32_e32 v255, v148, v149\n"
"v_max3_u32 v255, v255, v150, v151\n"
"v_max3_u32 v255, v255, v108, v109\n"
"v_max3_u32 v255, v255, v110, v111\n"
"v_max3_u32 v255, v255, v112, v113\n"
"v_max3_u32 v255, v255, v114, v115\n"
"v_max3_u32 v255, v255, v196, v197\n"
"v_max3_u32 v255, v255, v198, v199\n"
"v_max3_u32 v255, v255, v200, v201\n"
"v_max3_u32 v255, v255, v202, v203\n"
"v_max3_u32 v255, v255, v128, v129\n"
"v_max3_u32 v255, v255, v130, v131\n"
"v_max3_u32 v255, v255, v120, v121\n"
"v_max3_u32 v255, v255, v122, v123\n"
"v_max3_u32 v255, v255, v132, v133\n"
"v_max3_u32 v255, v255, v134, v135\n"
"v_cmp_eq_u32_e32 vcc, -1, v255\n"
"s_cbranch_vccz .Lmk_p36k\n"
"s_sub_u32 s1, s1, 1\n"
"s_cbranch_scc0 .Lmk_p36l\n"
".Lmk_p36k:\n"
"v_mfma_f32_16x16x32_bf16 v[88:91], a[128:131], v[148:151], 0\n"
"v_mfma_f32_16x16x32_bf16 v[88:91], a[132:135], v[108:111], v[88:91]\n"
"v_mfma_f32_16x16x32_bf16 v[88:91], a[136:139], v[112:115], v[88:91]\n"
"v_mfma_f32_16x16x32_bf16 v[88:91], a[140:143], v[196:199], v[88:91]\n"
"v_mfma_f32_16x16x32_bf16 v[88:91], a[144:147], v[200:203], v[88:91]\n"
"v_mfma_f32_16x16x32_bf16 v[88:91], a[148:151], v[128:131], v[88:91]\n"
"v_mfma_f32_16x16x32_bf16 v[88:91], a[152:155], v[120:123], v[88:91]\n"
"v_mfma_f32_16x16x32_bf16 v[88:91], a[156:159], v[132:135], v[88:91]\n"
"v_mfma_f32_16x16x32_bf16 v[92:95], a[188:191], v[132:135], 0\n"
"v_mfma_f32_16x16x32_bf16 v[92:95], a[184:187], v[120:123], v[92:95]\n"
"v_mfma_f32_16x16x32_bf16 v[92:95], a[180:183], v[128:131], v[92:95]\n"
"v_mfma_f32_16x16x32_bf16 v[92:95], a[176:179], v[200:203], v[92:95]\n"
"v_mfma_f32_16x16x32_bf16 v[92:95], a[172:175], v[196:199], v[92:95]\n"
"v_mfma_f32_16x16x32_bf16 v[92:95], a[168:171], v[112:115], v[92:95]\n"
"s_nop 7\n"
"s_branch .Lmk_p36r\n"
".Lmk_p37s:\n"
";\n"
"s_waitcnt vmcnt(16)\n"
"s_mov_b32 s1, 0x10000\n"
".Lmk_p37l:\n"
"s_add_u32 s97, s58, 0\n"
"buffer_load_dwordx4 v[152:155], v0, s[4:7], s97 offen sc1\n"
"buffer_load_dwordx4 v[160:163], v0, s[4:7], s97 offen offset:1024 sc1\n"
"buffer_load_dwordx4 v[124:127], v0, s[4:7], s97 offen offset:2048 sc1\n"
"buffer_load_dwordx4 v[144:147], v0, s[4:7], s97 offen offset:3072 sc1\n"
"buffer_load_dwordx4 v[156:159], v1, s[4:7], s97 offen sc1\n"
"buffer_load_dwordx4 v[172:175], v1, s[4:7], s97 offen offset:1024 sc1\n"
"buffer_load_dwordx4 v[116:119], v1, s[4:7], s97 offen offset:2048 sc1\n"
"buffer_load_dwordx4 v[136:139], v1, s[4:7], s97 offen offset:3072 sc1\n"
"s_add_u32 s97, s59, 0\n"
"buffer_load_dwordx4 v[176:179], v0, s[4:7], s97 offen sc1\n"
"buffer_load_dwordx4 v[180:183], v0, s[4:7], s97 offen offset:1024 sc1\n"
"buffer_load_dwordx4 v[168:171], v0, s[4:7], s97 offen offset:2048 sc1\n"
"buffer_load_dwordx4 v[184:187], v0, s[4:7], s97 offen offset:3072 sc1\n"
"buffer_load_dwordx4 v[188:191], v1, s[4:7], s97 offen sc1\n"
"buffer_load_dwordx4 v[164:167], v1, s[4:7], s97 offen offset:1024 sc1\n"
"buffer_load_dwordx4 v[192:195], v1, s[4:7], s97 offen offset:2048 sc1\n"
"buffer_load_dwordx4 v[196:199], v1, s[4:7], s97 offen offset:3072 sc1\n"
"s_add_u32 s97, s52, 0x440000\n"
"buffer_load_dwordx4 v[88:91], v0, s[4:7], s97 offen sc1\n"
"buffer_load_dwordx4 v[92:95], v0, s[4:7], s97 offen offset:1024 sc1\n"
"s_waitcnt vmcnt(0)\n"
"v_max_u32_e32 v239, v152, v153\n"
"v_max3_u32 v239, v239, v154, v155\n"
"v_max3_u32 v239, v239, v160, v161\n"
"v_max3_u32 v239, v239, v162, v163\n"
"v_max3_u32 v239, v239, v124, v125\n"
"v_max3_u32 v239, v239, v126, v127\n"
"v_max3_u32 v239, v239, v144, v145\n"
"v_max3_u32 v239, v239, v146, v147\n"
"v_max3_u32 v239, v239, v156, v157\n"
"v_max3_u32 v239, v239, v158, v159\n"
"v_max3_u32 v239, v239, v172, v173\n"
"v_max3_u32 v239, v239, v174, v175\n"
"v_max3_u32 v239, v239, v116, v117\n"
"v_max3_u32 v239, v239, v118, v119\n"
"v_max3_u32 v239, v239, v136, v137\n"
"v_max3_u32 v239, v239, v138, v139\n"
"v_cmp_eq_u32_e32 vcc, -1, v239\n"
"s_cbranch_vccz .Lmk_p37k\n"
"s_sub_u32 s1, s1, 1\n"
"s_cbranch_scc0 .Lmk_p37l\n"
".Lmk_p37k:\n"
"v_mfma_f32_16x16x32_bf16 v[96:99], a[128:131], v[152:155], 0\n"
"v_mfma_f32_16x16x32_bf16 v[96:99], a[132:135], v[160:163], v[96:99]\n"
"v_mfma_f32_16x16x32_bf16 v[96:99], a[136:139], v[124:127], v[96:99]\n"
"v_mfma_f32_16x16x32_bf16 v[96:99], a[140:143], v[144:147], v[96:99]\n"
"v_mfma_f32_16x16x32_bf16 v[96:99], a[144:147], v[156:159], v[96:99]\n"
"v_mfma_f32_16x16x32_bf16 v[96:99], a[148:151], v[172:175], v[96:99]\n"
"v_mfma_f32_16x16x32_bf16 v[96:99], a[152:155], v[116:119], v[96:99]\n"
"v_mfma_f32_16x16x32_bf16 v[96:99], a[156:159], v[136:139], v[96:99]\n"
"v_mfma_f32_16x16x32_bf16 v[104:107], a[188:191], v[136:139], 0\n"
"v_mfma_f32_16x16x32_bf16 v[104:107], a[184:187], v[116:119], v[104:107]\n"
"v_mfma_f32_16x16x32_bf16 v[104:107], a[180:183], v[172:175], v[104:107]\n"
"v_mfma_f32_16x16x32_bf16 v[104:107], a[176:179], v[156:159], v[104:107]\n"
"v_mfma_f32_16x16x32_bf16 v[104:107], a[172:175], v[144:147], v[104:107]\n"
"v_mfma_f32_16x16x32_bf16 v[104:107], a[168:171], v[124:127], v[104:107]\n"
"s_nop 7\n"
"s_branch .Lmk_p37r\n"
".Lmk_p38s:\n"
";\n"
"s_waitcnt vmcnt(16)\n"
"s_mov_b32 s1, 0x10000\n"
".Lmk_p38l:\n"
"s_add_u32 s97, s59, 0\n"
"buffer_load_dwordx4 v[176:179], v0, s[4:7], s97 offen sc1\n"
"buffer_load_dwordx4 v[180:183], v0, s[4:7], s97 offen offset:1024 sc1\n"
"buffer_load_dwordx4 v[168:171], v0, s[4:7], s97 offen offset:2048 sc1\n"
"buffer_load_dwordx4 v[184:187], v0, s[4:7], s97 offen offset:3072 sc1\n"
"buffer_load_dwordx4 v[188:191], v1, s[4:7], s97 offen sc1\n"
"buffer_load_dwordx4 v[164:167], v1, s[4:7], s97 offen offset:1024 sc1\n"
"buffer_load_dwordx4 v[192:195], v1, s[4:7], s97 offen offset:2048 sc1\n"
"buffer_load_dwordx4 v[196:199], v1, s[4:7], s97 offen offset:3072 sc1\n"
"s_add_u32 s97, s52, 0x440000\n"
"buffer_load_dwordx4 v[88:91], v0, s[4:7], s97 offen sc1\n"
"buffer_load_dwordx4 v[92:95], v0, s[4:7], s97 offen offset:1024 sc1\n"
"buffer_load_dwordx4 v[96:99], v0, s[4:7], s97 offen offset:2048 sc1\n"
"buffer_load_dwordx4 v[100:103], v0, s[4:7], s97 offen offset:3072 sc1\n"
"buffer_load_dwordx4 v[104:107], v1, s[4:7], s97 offen sc1\n"
"buffer_load_dwordx4 v[112:115], v1, s[4:7], s97 offen offset:1024 sc1\n"
"buffer_load_dwordx4 v[116:119], v1, s[4:7], s97 offen offset:2048 sc1\n"
"buffer_load_dwordx4 v[124:127], v1, s[4:7], s97 offen offset:3072 sc1\n"
"s_add_u32 s97, s53, 0x440000\n"
"buffer_load_dwordx4 v[128:131], v0, s[4:7], s97 offen sc1\n"
"buffer_load_dwordx4 v[132:135], v0, s[4:7], s97 offen offset:1024 sc1\n"
"buffer_load_dwordx4 v[136:139], v0, s[4:7], s97 offen offset:2048 sc1\n"
"s_waitcnt vmcnt(0)\n"
"v_max_u32_e32 v255, v176, v177\n"
"v_max3_u32 v255, v255, v178, v179\n"
"v_max3_u32 v255, v255, v180, v181\n"
"v_max3_u32 v255, v255, v182, v183\n"
"v_max3_u32 v255, v255, v168, v169\n"
"v_max3_u32 v255, v255, v170, v171\n"
"v_max3_u32 v255, v255, v184, v185\n"
"v_max3_u32 v255, v255, v186, v187\n"
"v_max3_u32 v255, v255, v188, v189\n"
"v_max3_u32 v255, v255, v190, v191\n"
"v_max3_u32 v255, v255, v164, v165\n"
"v_max3_u32 v255, v255, v166, v167\n"
"v_max3_u32 v255, v255, v192, v193\n"
"v_max3_u32 v255, v255, v194, v195\n"
"v_max3_u32 v255, v255, v196, v197\n"
"v_max3_u32 v255, v255, v198, v199\n"
"v_cmp_eq_u32_e32 vcc, -1, v255\n"
"s_cbranch_vccz .Lmk_p38k\n"
"s_sub_u32 s1, s1, 1\n"
"s_cbranch_scc0 .Lmk_p38l\n"
".Lmk_p38k:\n"
"v_mfma_f32_16x16x32_bf16 v[172:175], a[128:131], v[176:179], v[140:143]\n"
"v_mfma_f32_16x16x32_bf16 v[172:175], a[132:135], v[180:183], v[172:175]\n"
"v_mfma_f32_16x16x32_bf16 v[172:175], a[136:139], v[168:171], v[172:175]\n"
"v_mfma_f32_16x16x32_bf16 v[172:175], a[140:143], v[184:187], v[172:175]\n"
"v_mfma_f32_16x16x32_bf16 v[172:175], a[144:147], v[188:191], v[172:175]\n"
"v_mfma_f32_16x16x32_bf16 v[172:175], a[148:151], v[164:167], v[172:175]\n"
"v_mfma_f32_16x16x32_bf16 v[172:175], a[152:155], v[192:195], v[172:175]\n"
"v_mfma_f32_16x16x32_bf16 v[172:175], a[156:159], v[196:199], v[172:175]\n"
"v_mfma_f32_16x16x32_bf16 v[204:207], a[188:191], v[196:199], v[148:151]\n"
"v_mfma_f32_16x16x32_bf16 v[204:207], a[184:187], v[192:195], v[204:207]\n"
"v_mfma_f32_16x16x32_bf16 v[204:207], a[180:183], v[164:167], v[204:207]\n"
"v_mfma_f32_16x16x32_bf16 v[204:207], a[176:179], v[188:191], v[204:207]\n"
"v_mfma_f32_16x16x32_bf16 v[204:207], a[172:175], v[184:187], v[204:207]\n"
"v_mfma_f32_16x16x32_bf16 v[204:207], a[168:171], v[168:171], v[204:207]\n"
"s_nop 7\n"
"s_branch .Lmk_p38r\n"
".Lmk_p43s:\n"
";\n"
"s_waitcnt vmcnt(16)\n"
"s_mov_b32 s1, 0x10000\n"
".Lmk_p43l:\n"
"s_add_u32 s96, s52, 0x440000\n"
"buffer_load_dwordx4 v[88:91], v0, s[4:7], s96 offen sc1\n"
"buffer_load_dwordx4 v[92:95], v0, s[4:7], s96 offen offset:1024 sc1\n"
"buffer_load_dwordx4 v[96:99], v0, s[4:7], s96 offen offset:2048 sc1\n"
"buffer_load_dwordx4 v[100:103], v0, s[4:7], s96 offen offset:3072 sc1\n"
"buffer_load_dwordx4 v[104:107], v1, s[4:7], s96 offen sc1\n"
"buffer_load_dwordx4 v[112:115], v1, s[4:7], s96 offen offset:1024 sc1\n"
"buffer_load_dwordx4 v[116:119], v1, s[4:7], s96 offen offset:2048 sc1\n"
"buffer_load_dwordx4 v[124:127], v1, s[4:7], s96 offen offset:3072 sc1\n"
"s_add_u32 s96, s53, 0x440000\n"
"buffer_load_dwordx4 v[128:131], v0, s[4:7], s96 offen sc1\n"
"buffer_load_dwordx4 v[132:135], v0, s[4:7], s96 offen offset:1024 sc1\n"
"buffer_load_dwordx4 v[136:139], v0, s[4:7], s96 offen offset:2048 sc1\n"
"buffer_load_dwordx4 v[140:143], v0, s[4:7], s96 offen offset:3072 sc1\n"
"buffer_load_dwordx4 v[144:147], v1, s[4:7], s96 offen sc1\n"
"buffer_load_dwordx4 v[156:159], v1, s[4:7], s96 offen offset:1024 sc1\n"
"buffer_load_dwordx4 v[160:163], v1, s[4:7], s96 offen offset:2048 sc1\n"
"buffer_load_dwordx4 v[164:167], v1, s[4:7], s96 offen offset:3072 sc1\n"
"s_add_u32 s96, s54, 0x440000\n"
"buffer_load_dwordx4 v[176:179], v0, s[4:7], s96 offen sc1\n"
"buffer_load_dwordx4 v[108:111], v0, s[4:7], s96 offen offset:1024 sc1\n"
"s_waitcnt vmcnt(0)\n"
"v_max_u32_e32 v252, v88, v89\n"
"v_max3_u32 v252, v252, v90, v91\n"
"v_max3_u32 v252, v252, v92, v93\n"
"v_max3_u32 v252, v252, v94, v95\n"
"v_max3_u32 v252, v252, v96, v97\n"
"v_max3_u32 v252, v252, v98, v99\n"
"v_max3_u32 v252, v252, v100, v101\n"
"v_max3_u32 v252, v252, v102, v103\n"
"v_max3_u32 v252, v252, v104, v105\n"
"v_max3_u32 v252, v252, v106, v107\n"
"v_max3_u32 v252, v252, v112, v113\n"
"v_max3_u32 v252, v252, v114, v115\n"
"v_max3_u32 v252, v252, v116, v117\n"
"v_max3_u32 v252, v252, v118, v119\n"
"v_max3_u32 v252, v252, v124, v125\n"
"v_max3_u32 v252, v252, v126, v127\n"
"v_cmp_eq_u32_e32 vcc, -1, v252\n"
"s_cbranch_vccz .Lmk_p43k\n"
"s_sub_u32 s1, s1, 1\n"
"s_cbranch_scc0 .Lmk_p43l\n"
".Lmk_p43k:\n"
"v_mfma_f32_16x16x32_bf16 v[172:175], a[0:3], v[88:91], 0\n"
"v_mfma_f32_16x16x32_bf16 v[172:175], a[4:7], v[92:95], v[172:175]\n"
"v_mfma_f32_16x16x32_bf16 v[172:175], a[8:11], v[96:99], v[172:175]\n"
"v_mfma_f32_16x16x32_bf16 v[172:175], a[12:15], v[100:103], v[172:175]\n"
"v_mfma_f32_16x16x32_bf16 v[172:175], a[16:19], v[104:107], v[172:175]\n"
"v_mfma_f32_16x16x32_bf16 v[172:175], a[20:23], v[112:115], v[172:175]\n"
"v_mfma_f32_16x16x32_bf16 v[172:175], a[24:27], v[116:119], v[172:175]\n"
"v_mfma_f32_16x16x32_bf16 v[172:175], a[28:31], v[124:127], v[172:175]\n"
"v_mfma_f32_16x16x32_bf16 v[180:183], a[60:63], v[124:127], 0\n"
"v_mfma_f32_16x16x32_bf16 v[180:183], a[56:59], v[116:119], v[180:183]\n"
"v_mfma_f32_16x16x32_bf16 v[180:183], a[52:55], v[112:115], v[180:183]\n"
"v_mfma_f32_16x16x32_bf16 v[180:183], a[48:51], v[104:107], v[180:183]\n"
"v_mfma_f32_16x16x32_bf16 v[180:183], a[44:47], v[100:103], v[180:183]\n"
"v_mfma_f32_16x16x32_bf16 v[180:183], a[40:43], v[96:99], v[180:183]\n"
"s_nop 7\n"
"s_branch .Lmk_p43r\n"
".Lmk_p44s:\n"
";\n"
"s_waitcnt vmcnt(16)\n"
"s_mov_b32 s1, 0x10000\n"
".Lmk_p44l:\n"
"s_add_u32 s97, s53, 0x440000\n"
"buffer_load_dwordx4 v[128:131], v0, s[4:7], s97 offen sc1\n"
"buffer_load_dwordx4 v[132:135], v0, s[4:7], s97 offen offset:1024 sc1\n"
"buffer_load_dwordx4 v[136:139], v0, s[4:7], s97 offen offset:2048 sc1\n"
"buffer_load_dwordx4 v[140:143], v0, s[4:7], s97 offen offset:3072 sc1\n"
"buffer_load_dwordx4 v[144:147], v1, s[4:7], s97 offen sc1\n"
"buffer_load_dwordx4 v[156:159], v1, s[4:7], s97 offen offset:1024 sc1\n"
"buffer_load_dwordx4 v[160:163], v1, s[4:7], s97 offen offset:2048 sc1\n"
"buffer_load_dwordx4 v[164:167], v1, s[4:7], s97 offen offset:3072 sc1\n"
"s_add_u32 s97, s54, 0x440000\n"
"buffer_load_dwordx4 v[176:179], v0, s[4:7], s97 offen sc1\n"
"buffer_load_dwordx4 v[108:111], v0, s[4:7], s97 offen offset:1024 sc1\n"
"buffer_load_dwordx4 v[148:151], v0, s[4:7], s97 offen offset:2048 sc1\n"
"buffer_load_dwordx4 v[184:187], v0, s[4:7], s97 offen offset:3072 sc1\n"
"buffer_load_dwordx4 v[152:155], v1, s[4:7], s97 offen sc1\n"
"buffer_load_dwordx4 v[192:195], v1, s[4:7], s97 offen offset:1024 sc1\n"
"buffer_load_dwordx4 v[196:199], v1, s[4:7], s97 offen offset:2048 sc1\n"
"buffer_load_dwordx4 v[200:203], v1, s[4:7], s97 offen offset:3072 sc1\n"
"s_add_u32 s97, s55, 0x440000\n"
"buffer_load_dwordx4 v[92:95], v0, s[4:7], s97 offen sc1\n"
"buffer_load_dwordx4 v[96:99], v0, s[4:7], s97 offen offset:1024 sc1\n"
"s_waitcnt vmcnt(0)\n"
"v_max_u32_e32 v247, v128, v129\n"
"v_max3_u32 v247, v247, v130, v131\n"
"v_max3_u32 v247, v247, v132, v133\n"
"v_max3_u32 v247, v247, v134, v135\n"
"v_max3_u32 v247, v247, v136, v137\n"
"v_max3_u32 v247, v247, v138, v139\n"
"v_max3_u32 v247, v247, v140, v141\n"
"v_max3_u32 v247, v247, v142, v143\n"
"v_max3_u32 v247, v247, v144, v145\n"
"v_max3_u32 v247, v247, v146, v147\n"
"v_max3_u32 v247, v247, v156, v157\n"
"v_max3_u32 v247, v247, v158, v159\n"
"v_max3_u32 v247, v247, v160, v161\n"
"v_max3_u32 v247, v247, v162, v163\n"
"v_max3_u32 v247, v247, v164, v165\n"
"v_max3_u32 v247, v247, v166, v167\n"
"v_cmp_eq_u32_e32 vcc, -1, v247\n"
"s_cbranch_vccz .Lmk_p44k\n"
"s_sub_u32 s1, s1, 1\n"
"s_cbranch_scc0 .Lmk_p44l\n"
".Lmk_p44k:\n"
"v_mfma_f32_16x16x32_bf16 v[88:91], a[0:3], v[128:131], 0\n"
"v_mfma_f32_16x16x32_bf16 v[88:91], a[4:7], v[132:135], v[88:91]\n"
"v_mfma_f32_16x16x32_bf16 v[88:91], a[8:11], v[136:139], v[88:91]\n"
"v_mfma_f32_16x16x32_bf16 v[88:91], a[12:15], v[140:143], v[88:91]\n"
"v_mfma_f32_16x16x32_bf16 v[88:91], a[16:19], v[144:147], v[88:91]\n"
"v_mfma_f32_16x16x32_bf16 v[88:91], a[20:23], v[156:159], v[88:91]\n"
"v_mfma_f32_16x16x32_bf16 v[88:91], a[24:27], v[160:163], v[88:91]\n"
"v_mfma_f32_16x16x32_bf16 v[88:91], a[28:31], v[164:167], v[88:91]\n"
"v_mfma_f32_16x16x32_bf16 v[100:103], a[60:63], v[164:167], 0\n"
"v_mfma_f32_16x16x32_bf16 v[100:103], a[56:59], v[160:163], v[100:103]\n"
"v_mfma_f32_16x16x32_bf16 v[100:103], a[52:55], v[156:159], v[100:103]\n"
"v_mfma_f32_16x16x32_bf16 v[100:103], a[48:51], v[144:147], v[100:103]\n"
"v_mfma_f32_16x16x32_bf16 v[100:103], a[44:47], v[140:143], v[100:103]\n"
"v_mfma_f32_16x16x32_bf16 v[100:103], a[40:43], v[136:139], v[100:103]\n"
"s_nop 7\n"
"s_branch .Lmk_p44r\n"
".Lmk_m47s:\n"
";\n"
"s_waitcnt vmcnt(16)\n"
"s_mov_b32 s1, 0x10000\n"
".Lmk_m47l:\n"
"s_add_u32 s97, s62, 0x44000\n"
"buffer_load_dword v255, v10, s[4:7], s97 offen sc1\n"
"buffer_load_dword v254, v10, s[4:7], s97 offen offset:256 sc1\n"
"buffer_load_dword v253, v10, s[4:7], s97 offen offset:512 sc1\n"
"buffer_load_dword v252, v10, s[4:7], s97 offen offset:768 sc1\n"
"buffer_load_dword v251, v10, s[4:7], s97 offen offset:1024 sc1\n"
"buffer_load_dword v250, v10, s[4:7], s97 offen offset:1280 sc1\n"
"buffer_load_dword v249, v10, s[4:7], s97 offen offset:1536 sc1\n"
"buffer_load_dword v248, v10, s[4:7], s97 offen offset:1792 sc1\n"
"s_waitcnt vmcnt(0)\n"
"v_max_u32_e32 v247, v255, v254\n"
"v_max3_u32 v247, v247, v253, v252\n"
"v_max3_u32 v247, v247, v251, v250\n"
"v_max3_u32 v247, v247, v249, v248\n"
"v_cmp_eq_u32_e32 vcc, -1, v247\n"
"s_cbranch_vccz .Lmk_m47k\n"
"s_sub_u32 s1, s1, 1\n"
"s_cbranch_scc0 .Lmk_m47l\n"
".Lmk_m47k:\n"
"v_add_f32_e32 v255, v255, v251\n"
"v_add_f32_e32 v254, v254, v250\n"
"v_add_f32_e32 v253, v253, v249\n"
"v_add_f32_e32 v252, v252, v248\n"
"v_add_f32_e32 v255, v255, v253\n"
"v_add_f32_e32 v254, v254, v252\n"
"v_add_f32_e32 v255, v255, v254\n"
"s_nop 1\n"
"s_branch .Lmk_m47r\n"
".Lmk_p45s:\n"
";\n"
"s_waitcnt vmcnt(16)\n"
"s_mov_b32 s1, 0x10000\n"
".Lmk_p45l:\n"
"s_add_u32 s96, s54, 0x440000\n"
"buffer_load_dwordx4 v[176:179], v0, s[4:7], s96 offen sc1\n"
"buffer_load_dwordx4 v[108:111], v0, s[4:7], s96 offen offset:1024 sc1\n"
"buffer_load_dwordx4 v[148:151], v0, s[4:7], s96 offen offset:2048 sc1\n"
"buffer_load_dwordx4 v[184:187], v0, s[4:7], s96 offen offset:3072 sc1\n"
"buffer_load_dwordx4 v[152:155], v1, s[4:7], s96 offen sc1\n"
"buffer_load_dwordx4 v[192:195], v1, s[4:7], s96 offen offset:1024 sc1\n"
"buffer_load_dwordx4 v[196:199], v1, s[4:7], s96 offen offset:2048 sc1\n"
"buffer_load_dwordx4 v[200:203], v1, s[4:7], s96 offen offset:3072 sc1\n"
"s_add_u32 s96, s55, 0x440000\n"
"buffer_load_dwordx4 v[92:95], v0, s[4:7], s96 offen sc1\n"
"buffer_load_dwordx4 v[96:99], v0, s[4:7], s96 offen offset:1024 sc1\n"
"buffer_load_dwordx4 v[104:107], v0, s[4:7], s96 offen offset:2048 sc1\n"
"buffer_load_dwordx4 v[112:115], v0, s[4:7], s96 offen offset:3072 sc1\n"
"buffer_load_dwordx4 v[100:103], v1, s[4:7], s96 offen sc1\n"
"buffer_load_dwordx4 v[124:127], v1, s[4:7], s96 offen offset:1024 sc1\n"
"buffer_load_dwordx4 v[168:171], v1, s[4:7], s96 offen offset:2048 sc1\n"
"buffer_load_dwordx4 v[172:175], v1, s[4:7], s96 offen offset:3072 sc1\n"
"s_add_u32 s96, s52, 0x448000\n"
"buffer_load_dwordx4 v[128:131], v0, s[4:7], s96 offen sc1\n"
"s_waitcnt vmcnt(0)\n"
"v_max_u32_e32 v255, v176, v177\n"
"v_max3_u32 v255, v255, v178, v179\n"
"v_max3_u32 v255, v255, v108, v109\n"
"v_max3_u32 v255, v255, v110, v111\n"
"v_max3_u32 v255, v255, v148, v149\n"
"v_max3_u32 v255, v255, v150, v151\n"
"v_max3_u32 v255, v255, v184, v185\n"
"v_max3_u32 v255, v255, v186, v187\n"
"v_max3_u32 v255, v255, v152, v153\n"
"v_max3_u32 v255, v255, v154, v155\n"
"v_max3_u32 v255, v255, v192, v193\n"
"v_max3_u32 v255, v255, v194, v195\n"
"v_max3_u32 v255, v255, v196, v197\n"
"v_max3_u32 v255, v255, v198, v199\n"
"v_max3_u32 v255, v255, v200, v201\n"
"v_max3_u32 v255, v255, v202, v203\n"
"v_cmp_eq_u32_e32 vcc, -1, v255\n"
"s_cbranch_vccz .Lmk_p45k\n"
"s_sub_u32 s1, s1, 1\n"
"s_cbranch_scc0 .Lmk_p45l\n"
".Lmk_p45k:\n"
"v_mfma_f32_16x16x32_bf16 v[88:91], a[0:3], v[176:179], 0\n"
"v_mfma_f32_16x16x32_bf16 v[88:91], a[4:7], v[108:111], v[88:91]\n"
"v_mfma_f32_16x16x32_bf16 v[88:91], a[8:11], v[148:151], v[88:91]\n"
"v_mfma_f32_16x16x32_bf16 v[88:91], a[12:15], v[184:187], v[88:91]\n"
"v_mfma_f32_16x16x32_bf16 v[88:91], a[16:19], v[152:155], v[88:91]\n"
"v_mfma_f32_16x16x32_bf16 v[88:91], a[20:23], v[192:195], v[88:91]\n"
"v_mfma_f32_16x16x32_bf16 v[88:91], a[24:27], v[196:199], v[88:91]\n"
"v_mfma_f32_16x16x32_bf16 v[88:91], a[28:31], v[200:203], v[88:91]\n"
"v_mfma_f32_16x16x32_bf16 v[116:119], a[60:63], v[200:203], 0\n"
"v_mfma_f32_16x16x32_bf16 v[116:119], a[56:59], v[196:199], v[116:119]\n"
"v_mfma_f32_16x16x32_bf16 v[116:119], a[52:55], v[192:195], v[116:119]\n"
"v_mfma_f32_16x16x32_bf16 v[116:119], a[48:51], v[152:155], v[116:119]\n"
"v_mfma_f32_16x16x32_bf16 v[116:119], a[44:47], v[184:187], v[116:119]\n"
"v_mfma_f32_16x16x32_bf16 v[116:119], a[40:43], v[148:151], v[116:119]\n"
"s_nop 7\n"
"s_branch .Lmk_p45r\n"
".Lmk_p46s:\n"
";\n"
"s_waitcnt vmcnt(16)\n"
"s_mov_b32 s1, 0x10000\n"
".Lmk_p46l:\n"
"s_add_u32 s96, s55, 0x440000\n"
"buffer_load_dwordx4 v[92:95], v0, s[4:7], s96 offen sc1\n"
"buffer_load_dwordx4 v[96:99], v0, s[4:7], s96 offen offset:1024 sc1\n"
"buffer_load_dwordx4 v[104:107], v0, s[4:7], s96 offen offset:2048 sc1\n"
"buffer_load_dwordx4 v[112:115], v0, s[4:7], s96 offen offset:3072 sc1\n"
"buffer_load_dwordx4 v[100:103], v1, s[4:7], s96 offen sc1\n"
"buffer_load_dwordx4 v[124:127], v1, s[4:7], s96 offen offset:1024 sc1\n"
"buffer_load_dwordx4 v[168:171], v1, s[4:7], s96 offen offset:2048 sc1\n"
"buffer_load_dwordx4 v[172:175], v1, s[4:7], s96 offen offset:3072 sc1\n"
"s_add_u32 s96, s52, 0x448000\n"
"buffer_load_dwordx4 v[128:131], v0, s[4:7], s96 offen sc1\n"
"buffer_load_dwordx4 v[88:91], v0, s[4:7], s96 offen offset:1024 sc1\n"
"buffer_load_dwordx4 v[164:167], v0, s[4:7], s96 offen offset:2048 sc1\n"
"buffer_load_dwordx4 v[140:143], v0, s[4:7], s96 offen offset:3072 sc1\n"
"buffer_load_dwordx4 v[132:135], v1, s[4:7], s96 offen sc1\n"
"buffer_load_dwordx4 v[224:227], v1, s[4:7], s96 offen offset:1024 sc1\n"
"buffer_load_dwordx4 v[180:183], v1, s[4:7], s96 offen offset:2048 sc1\n"
"buffer_load_dwordx4 v[148:151], v1, s[4:7], s96 offen offset:3072 sc1\n"
"s_add_u32 s96, s53, 0x448000\n"
"buffer_load_dwordx4 v[116:119], v0, s[4:7], s96 offen sc1\n"
"buffer_load_dwordx4 v[160:163], v0, s[4:7], s96 offen offset:1024 sc1\n"
"s_waitcnt vmcnt(0)\n"
"v_max_u32_e32 v252, v92, v93\n"
"v_max3_u32 v252, v252, v94, v95\n"
"v_max3_u32 v252, v252, v96, v97\n"
"v_max3_u32 v252, v252, v98, v99\n"
"v_max3_u32 v252, v252, v104, v105\n"
"v_max3_u32 v252, v252, v106, v107\n"
"v_max3_u32 v252, v252, v112, v113\n"
"v_max3_u32 v252, v252, v114, v115\n"
"v_max3_u32 v252, v252, v100, v101\n"
"v_max3_u32 v252, v252, v102, v103\n"
"v_max3_u32 v252, v252, v124, v125\n"
"v_max3_u32 v252, v252, v126, v127\n"
"v_max3_u32 v252, v252, v168, v169\n"
"v_max3_u32 v252, v252, v170, v171\n"
"v_max3_u32 v252, v252, v172, v173\n"
"v_max3_u32 v252, v252, v174, v175\n"
"v_cmp_eq_u32_e32 vcc, -1, v252\n"
"s_cbranch_vccz .Lmk_p46k\n"
"s_sub_u32 s1, s1, 1\n"
"s_cbranch_scc0 .Lmk_p46l\n"
".Lmk_p46k:\n"
"v_mfma_f32_16x16x32_bf16 v[196:199], a[0:3], v[92:95], v[136:139]\n"
"v_mfma_f32_16x16x32_bf16 v[196:199], a[4:7], v[96:99], v[196:199]\n"
"v_mfma_f32_16x16x32_bf16 v[196:199], a[8:11], v[104:107], v[196:199]\n"
"v_mfma_f32_16x16x32_bf16 v[196:199], a[12:15], v[112:115], v[196:199]\n"
"v_mfma_f32_16x16x32_bf16 v[196:199], a[16:19], v[100:103], v[196:199]\n"
"v_mfma_f32_16x16x32_bf16 v[196:199], a[20:23], v[124:127], v[196:199]\n"
"v_mfma_f32_16x16x32_bf16 v[196:199], a[24:27], v[168:171], v[196:199]\n"
"v_mfma_f32_16x16x32_bf16 v[196:199], a[28:31], v[172:175], v[196:199]\n"
"v_mfma_f32_16x16x32_bf16 v[204:207], a[60:63], v[172:175], v[156:159]\n"
"v_mfma_f32_16x16x32_bf16 v[204:207], a[56:59], v[168:171], v[204:207]\n"
"v_mfma_f32_16x16x32_bf16 v[204:207], a[52:55], v[124:127], v[204:207]\n"
"v_mfma_f32_16x16x32_bf16 v[204:207], a[48:51], v[100:103], v[204:207]\n"
"v_mfma_f32_16x16x32_bf16 v[204:207], a[44:47], v[112:115], v[204:207]\n"
"v_mfma_f32_16x16x32_bf16 v[204:207], a[40:43], v[104:107], v[204:207]\n"
"s_nop 7\n"
"s_branch .Lmk_p46r\n"
".Lmk_p48s:\n"
";\n"
"s_waitcnt vmcnt(16)\n"
"s_mov_b32 s1, 0x10000\n"
".Lmk_p48l:\n"
"s_add_u32 s98, s52, 0x448000\n"
"buffer_load_dwordx4 v[128:131], v0, s[4:7], s98 offen sc1\n"
"buffer_load_dwordx4 v[88:91], v0, s[4:7], s98 offen offset:1024 sc1\n"
"buffer_load_dwordx4 v[164:167], v0, s[4:7], s98 offen offset:2048 sc1\n"
"buffer_load_dwordx4 v[140:143], v0, s[4:7], s98 offen offset:3072 sc1\n"
"buffer_load_dwordx4 v[132:135], v1, s[4:7], s98 offen sc1\n"
"buffer_load_dwordx4 v[224:227], v1, s[4:7], s98 offen offset:1024 sc1\n"
"buffer_load_dwordx4 v[180:183], v1, s[4:7], s98 offen offset:2048 sc1\n"
"buffer_load_dwordx4 v[148:151], v1, s[4:7], s98 offen offset:3072 sc1\n"
"s_add_u32 s98, s53, 0x448000\n"
"buffer_load_dwordx4 v[116:119], v0, s[4:7], s98 offen sc1\n"
"buffer_load_dwordx4 v[160:163], v0, s[4:7], s98 offen offset:1024 sc1\n"
"buffer_load_dwordx4 v[136:139], v0, s[4:7], s98 offen offset:2048 sc1\n"
"buffer_load_dwordx4 v[156:159], v0, s[4:7], s98 offen offset:3072 sc1\n"
"buffer_load_dwordx4 v[192:195], v1, s[4:7], s98 offen sc1\n"
"buffer_load_dwordx4 v[184:187], v1, s[4:7], s98 offen offset:1024 sc1\n"
"buffer_load_dwordx4 v[176:179], v1, s[4:7], s98 offen offset:2048 sc1\n"
"buffer_load_dwordx4 v[200:203], v1, s[4:7], s98 offen offset:3072 sc1\n"
"s_add_u32 s98, s54, 0x448000\n"
"buffer_load_dwordx4 v[96:99], v0, s[4:7], s98 offen sc1\n"
"buffer_load_dwordx4 v[100:103], v0, s[4:7], s98 offen offset:1024 sc1\n"
"s_waitcnt vmcnt(0)\n"
"v_max_u32_e32 v252, v128, v129\n"
"v_max3_u32 v252, v252, v130, v131\n"
"v_max3_u32 v252, v252, v88, v89\n"
"v_max3_u32 v252, v252, v90, v91\n"
"v_max3_u32 v252, v252, v164, v165\n"
"v_max3_u32 v252, v252, v166, v167\n"
"v_max3_u32 v252, v252, v140, v141\n"
"v_max3_u32 v252, v252, v142, v143\n"
"v_max3_u32 v252, v252, v132, v133\n"
"v_max3_u32 v252, v252, v134, v135\n"
"v_max3_u32 v252, v252, v224, v225\n"
"v_max3_u32 v252, v252, v226, v227\n"
"v_max3_u32 v252, v252, v180, v181\n"
"v_max3_u32 v252, v252, v182, v183\n"
"v_max3_u32 v252, v252, v148, v149\n"
"v_max3_u32 v252, v252, v150, v151\n"
"v_cmp_eq_u32_e32 vcc, -1, v252\n"
"s_cbranch_vccz .Lmk_p48k\n"
"s_sub_u32 s1, s1, 1\n"
"s_cbranch_scc0 .Lmk_p48l\n"
".Lmk_p48k:\n"
"v_mfma_f32_16x16x32_bf16 v[92:95], a[0:3], v[128:131], 0\n"
"v_mfma_f32_16x16x32_bf16 v[92:95], a[4:7], v[88:91], v[92:95]\n"
"v_mfma_f32_16x16x32_bf16 v[92:95], a[8:11], v[164:167], v[92:95]\n"
"v_mfma_f32_16x16x32_bf16 v[92:95], a[12:15], v[140:143], v[92:95]\n"
"v_mfma_f32_16x16x32_bf16 v[92:95], a[16:19], v[132:135], v[92:95]\n"
"v_mfma_f32_16x16x32_bf16 v[92:95], a[20:23], v[224:227], v[92:95]\n"
"v_mfma_f32_16x16x32_bf16 v[92:95], a[24:27], v[180:183], v[92:95]\n"
"v_mfma_f32_16x16x32_bf16 v[92:95], a[28:31], v[148:151], v[92:95]\n"
"v_mfma_f32_16x16x32_bf16 v[104:107], a[60:63], v[148:151], 0\n"
"v_mfma_f32_16x16x32_bf16 v[104:107], a[56:59], v[180:183], v[104:107]\n"
"v_mfma_f32_16x16x32_bf16 v[104:107], a[52:55], v[224:227], v[104:107]\n"
"v_mfma_f32_16x16x32_bf16 v[104:107], a[48:51], v[132:135], v[104:107]\n"
"v_mfma_f32_16x16x32_bf16 v[104:107], a[44:47], v[140:143], v[104:107]\n"
"v_mfma_f32_16x16x32_bf16 v[104:107], a[40:43], v[164:167], v[104:107]\n"
"s_nop 7\n"
"s_branch .Lmk_p48r\n"
".Lmk_p49s:\n"
";\n"
"s_waitcnt vmcnt(16)\n"
"s_mov_b32 s1, 0x10000\n"
".Lmk_p49l:\n"
"s_add_u32 s95, s53, 0x448000\n"
"buffer_load_dwordx4 v[116:119], v0, s[4:7], s95 offen sc1\n"
"buffer_load_dwordx4 v[160:163], v0, s[4:7], s95 offen offset:1024 sc1\n"
"buffer_load_dwordx4 v[136:139], v0, s[4:7], s95 offen offset:2048 sc1\n"
"buffer_load_dwordx4 v[156:159], v0, s[4:7], s95 offen offset:3072 sc1\n"
"buffer_load_dwordx4 v[192:195], v1, s[4:7], s95 offen sc1\n"
"buffer_load_dwordx4 v[184:187], v1, s[4:7], s95 offen offset:1024 sc1\n"
"buffer_load_dwordx4 v[176:179], v1, s[4:7], s95 offen offset:2048 sc1\n"
"buffer_load_dwordx4 v[200:203], v1, s[4:7], s95 offen offset:3072 sc1\n"
"s_add_u32 s95, s54, 0x448000\n"
"buffer_load_dwordx4 v[96:99], v0, s[4:7], s95 offen sc1\n"
"buffer_load_dwordx4 v[100:103], v0, s[4:7], s95 offen offset:1024 sc1\n"
"buffer_load_dwordx4 v[112:115], v0, s[4:7], s95 offen offset:2048 sc1\n"
"buffer_load_dwordx4 v[120:123], v0, s[4:7], s95 offen offset:3072 sc1\n"
"buffer_load_dwordx4 v[104:107], v1, s[4:7], s95 offen sc1\n"
"buffer_load_dwordx4 v[152:155], v1, s[4:7], s95 offen offset:1024 sc1\n"
"buffer_load_dwordx4 v[108:111], v1, s[4:7], s95 offen offset:2048 sc1\n"
"buffer_load_dwordx4 v[144:147], v1, s[4:7], s95 offen offset:3072 sc1\n"
"s_add_u32 s95, s55, 0x448000\n"
"buffer_load_dwordx4 v[92:95], v0, s[4:7], s95 offen sc1\n"
"buffer_load_dwordx4 v[124:127], v0, s[4:7], s95 offen offset:1024 sc1\n"
"s_waitcnt vmcnt(0)\n"
"v_max_u32_e32 v247, v116, v117\n"
"v_max3_u32 v247, v247, v118, v119\n"
"v_max3_u32 v247, v247, v160, v161\n"
"v_max3_u32 v247, v247, v162, v163\n"
"v_max3_u32 v247, v247, v136, v137\n"
"v_max3_u32 v247, v247, v138, v139\n"
"v_max3_u32 v247, v247, v156, v157\n"
"v_max3_u32 v247, v247, v158, v159\n"
"v_max3_u32 v247, v247, v192, v193\n"
"v_max3_u32 v247, v247, v194, v195\n"
"v_max3_u32 v247, v247, v184, v185\n"
"v_max3_u32 v247, v247, v186, v187\n"
"v_max3_u32 v247, v247, v176, v177\n"
"v_max3_u32 v247, v247, v178, v179\n"
"v_max3_u32 v247, v247, v200, v201\n"
"v_max3_u32 v247, v247, v202, v203\n"
"v_cmp_eq_u32_e32 vcc, -1, v247\n"
"s_cbranch_vccz .Lmk_p49k\n"
"s_sub_u32 s1, s1, 1\n"
"s_cbranch_scc0 .Lmk_p49l\n"
".Lmk_p49k:\n"
"v_mfma_f32_16x16x32_bf16 v[88:91], a[0:3], v[116:119], 0\n"
"v_mfma_f32_16x16x32_bf16 v[88:91], a[4:7], v[160:163], v[88:91]\n"
"v_mfma_f32_16x16x32_bf16 v[88:91], a[8:11], v[136:139], v[88:91]\n"
"v_mfma_f32_16x16x32_bf16 v[88:91], a[12:15], v[156:159], v[88:91]\n"
"v_mfma_f32_16x16x32_bf16 v[88:91], a[16:19], v[192:195], v[88:91]\n"
"v_mfma_f32_16x16x32_bf16 v[88:91], a[20:23], v[184:187], v[88:91]\n"
"v_mfma_f32_16x16x32_bf16 v[88:91], a[24:27], v[176:179], v[88:91]\n"
"v_mfma_f32_16x16x32_bf16 v[88:91], a[28:31], v[200:203], v[88:91]\n"
"v_mfma_f32_16x16x32_bf16 v[128:131], a[60:63], v[200:203], 0\n"
"v_mfma_f32_16x16x32_bf16 v[128:131], a[56:59], v[176:179], v[128:131]\n"
"v_mfma_f32_16x16x32_bf16 v[128:131], a[52:55], v[184:187], v[128:131]\n"
"v_mfma_f32_16x16x32_bf16 v[128:131], a[48:51], v[192:195], v[128:131]\n"
"v_mfma_f32_16x16x32_bf16 v[128:131], a[44:47], v[156:159], v[128:131]\n"
"v_mfma_f32_16x16x32_bf16 v[128:131], a[40:43], v[136:139], v[128:131]\n"
"s_nop 7\n"
"s_branch .Lmk_p49r\n"
".Lmk_m52s:\n"
";\n"
"s_waitcnt vmcnt(16)\n"
"s_mov_b32 s1, 0x10000\n"
".Lmk_m52l:\n"
"s_add_u32 s97, s62, 0x44800\n"
"buffer_load_dword v255, v10, s[4:7], s97 offen sc1\n"
"buffer_load_dword v253, v10, s[4:7], s97 offen offset:256 sc1\n"
"buffer_load_dword v252, v10, s[4:7], s97 offen offset:512 sc1\n"
"buffer_load_dword v251, v10, s[4:7], s97 offen offset:768 sc1\n"
"buffer_load_dword v250, v10, s[4:7], s97 offen offset:1024 sc1\n"
"buffer_load_dword v254, v10, s[4:7], s97 offen offset:1280 sc1\n"
"buffer_load_dword v249, v10, s[4:7], s97 offen offset:1536 sc1\n"
"buffer_load_dword v248, v10, s[4:7], s97 offen offset:1792 sc1\n"
"s_waitcnt vmcnt(0)\n"
"v_max_u32_e32 v245, v255, v253\n"
"v_max3_u32 v245, v245, v252, v251\n"
"v_max3_u32 v245, v245, v250, v254\n"
"v_max3_u32 v245, v245, v249, v248\n"
"v_cmp_eq_u32_e32 vcc, -1, v245\n"
"s_cbranch_vccz .Lmk_m52k\n"
"s_sub_u32 s1, s1, 1\n"
"s_cbranch_scc0 .Lmk_m52l\n"
".Lmk_m52k:\n"
"v_add_f32_e32 v255, v255, v250\n"
"v_add_f32_e32 v253, v253, v254\n"
"v_add_f32_e32 v252, v252, v249\n"
"v_add_f32_e32 v251, v251, v248\n"
"v_add_f32_e32 v255, v255, v252\n"
"v_add_f32_e32 v253, v253, v251\n"
"v_add_f32_e32 v255, v255, v253\n"
"s_nop 1\n"
"s_branch .Lmk_m52r\n"
".Lmk_f39s:\n"
";\n"
"s_mov_b32 s1, 0x10000\n"
".Lmk_f39l:\n"
"buffer_load_dwordx2 v[246:247], v12, s[4:7], s96 offen sc1\n"
"s_waitcnt vmcnt(0)\n"
"v_cmp_ne_u64_e32 vcc, v[246:247], v[18:19]\n"
"s_cbranch_vccz .Lmk_f39r\n"
"s_sub_u32 s1, s1, 1\n"
"s_cbranch_scc0 .Lmk_f39l\n"
"s_branch .Lmk_f39r\n"
".Lmk_p50s:\n"
";\n"
"s_waitcnt vmcnt(16)\n"
"s_mov_b32 s1, 0x10000\n"
".Lmk_p50l:\n"
"s_add_u32 s96, s54, 0x448000\n"
"buffer_load_dwordx4 v[96:99], v0, s[4:7], s96 offen sc1\n"
"buffer_load_dwordx4 v[100:103], v0, s[4:7], s96 offen offset:1024 sc1\n"
"buffer_load_dwordx4 v[112:115], v0, s[4:7], s96 offen offset:2048 sc1\n"
"buffer_load_dwordx4 v[120:123], v0, s[4:7], s96 offen offset:3072 sc1\n"
"buffer_load_dwordx4 v[104:107], v1, s[4:7], s96 offen sc1\n"
"buffer_load_dwordx4 v[152:155], v1, s[4:7], s96 offen offset:1024 sc1\n"
"buffer_load_dwordx4 v[108:111], v1, s[4:7], s96 offen offset:2048 sc1\n"
"buffer_load_dwordx4 v[144:147], v1, s[4:7], s96 offen offset:3072 sc1\n"
"s_add_u32 s96, s55, 0x448000\n"
"buffer_load_dwordx4 v[92:95], v0, s[4:7], s96 offen sc1\n"
"buffer_load_dwordx4 v[124:127], v0, s[4:7], s96 offen offset:1024 sc1\n"
"buffer_load_dwordx4 v[132:135], v0, s[4:7], s96 offen offset:2048 sc1\n"
"buffer_load_dwordx4 v[140:143], v0, s[4:7], s96 offen offset:3072 sc1\n"
"buffer_load_dwordx4 v[128:131], v1, s[4:7], s96 offen sc1\n"
"buffer_load_dwordx4 v[168:171], v1, s[4:7], s96 offen offset:1024 sc1\n"
"buffer_load_dwordx4 v[172:175], v1, s[4:7], s96 offen offset:2048 sc1\n"
"buffer_load_dwordx4 v[180:183], v1, s[4:7], s96 offen offset:3072 sc1\n"
"s_waitcnt vmcnt(0)\n"
"v_max_u32_e32 v252, v96, v97\n"
"v_max3_u32 v252, v252, v98, v99\n"
"v_max3_u32 v252, v252, v100, v101\n"
"v_max3_u32 v252, v252, v102, v103\n"
"v_max3_u32 v252, v252, v112, v113\n"
"v_max3_u32 v252, v252, v114, v115\n"
"v_max3_u32 v252, v252, v120, v121\n"
"v_max3_u32 v252, v252, v122, v123\n"
"v_max3_u32 v252, v252, v104, v105\n"
"v_max3_u32 v252, v252, v106, v107\n"
"v_max3_u32 v252, v252, v152, v153\n"
"v_max3_u32 v252, v252, v154, v155\n"
"v_max3_u32 v252, v252, v108, v109\n"
"v_max3_u32 v252, v252, v110, v111\n"
"v_max3_u32 v252, v252, v144, v145\n"
"v_max3_u32 v252, v252, v146, v147\n"
"v_cmp_eq_u32_e32 vcc, -1, v252\n"
"s_cbranch_vccz .Lmk_p50k\n"
"s_sub_u32 s1, s1, 1\n"
"s_cbranch_scc0 .Lmk_p50l\n"
".Lmk_p50k:\n"
"v_mfma_f32_16x16x32_bf16 v[88:91], a[0:3], v[96:99], 0\n"
"v_mfma_f32_16x16x32_bf16 v[88:91], a[4:7], v[100:103], v[88:91]\n"
"v_mfma_f32_16x16x32_bf16 v[88:91], a[8:11], v[112:115], v[88:91]\n"
"v_mfma_f32_16x16x32_bf16 v[88:91], a[12:15], v[120:123], v[88:91]\n"
"v_mfma_f32_16x16x32_bf16 v[88:91], a[16:19], v[104:107], v[88:91]\n"
"v_mfma_f32_16x16x32_bf16 v[88:91], a[20:23], v[152:155], v[88:91]\n"
"v_mfma_f32_16x16x32_bf16 v[88:91], a[24:27], v[108:111], v[88:91]\n"
"v_mfma_f32_16x16x32_bf16 v[88:91], a[28:31], v[144:147], v[88:91]\n"
"v_mfma_f32_16x16x32_bf16 v[116:119], a[60:63], v[144:147], 0\n"
"v_mfma_f32_16x16x32_bf16 v[116:119], a[56:59], v[108:111], v[116:119]\n"
"v_mfma_f32_16x16x32_bf16 v[116:119], a[52:55], v[152:155], v[116:119]\n"
"v_mfma_f32_16x16x32_bf16 v[116:119], a[48:51], v[104:107], v[116:119]\n"
"v_mfma_f32_16x16x32_bf16 v[116:119], a[44:47], v[120:123], v[116:119]\n"
"v_mfma_f32_16x16x32_bf16 v[116:119], a[40:43], v[112:115], v[116:119]\n"
"s_nop 7\n"
"s_branch .Lmk_p50r\n"
".Lmk_p51s:\n"
";\n"
"s_waitcnt vmcnt(16)\n"
"s_mov_b32 s1, 0x10000\n"
".Lmk_p51l:\n"
"s_add_u32 s96, s55, 0x448000\n"
"buffer_load_dwordx4 v[92:95], v0, s[4:7], s96 offen sc1\n"
"buffer_load_dwordx4 v[124:127], v0, s[4:7], s96 offen offset:1024 sc1\n"
"buffer_load_dwordx4 v[132:135], v0, s[4:7], s96 offen offset:2048 sc1\n"
"buffer_load_dwordx4 v[140:143], v0, s[4:7], s96 offen offset:3072 sc1\n"
"buffer_load_dwordx4 v[128:131], v1, s[4:7], s96 offen sc1\n"
"buffer_load_dwordx4 v[168:171], v1, s[4:7], s96 offen offset:1024 sc1\n"
"buffer_load_dwordx4 v[172:175], v1, s[4:7], s96 offen offset:2048 sc1\n"
"buffer_load_dwordx4 v[180:183], v1, s[4:7], s96 offen offset:3072 sc1\n"
"s_waitcnt vmcnt(0)\n"
"v_max_u32_e32 v252, v92, v93\n"
"v_max3_u32 v252, v252, v94, v95\n"
"v_max3_u32 v252, v252, v124, v125\n"
"v_max3_u32 v252, v252, v126, v127\n"
"v_max3_u32 v252, v252, v132, v133\n"
"v_max3_u32 v252, v252, v134, v135\n"
"v_max3_u32 v252, v252, v140, v141\n"
"v_max3_u32 v252, v252, v142, v143\n"
"v_max3_u32 v252, v252, v128, v129\n"
"v_max3_u32 v252, v252, v130, v131\n"
"v_max3_u32 v252, v252, v168, v169\n"
"v_max3_u32 v252, v252, v170, v171\n"
"v_max3_u32 v252, v252, v172, v173\n"
"v_max3_u32 v252, v252, v174, v175\n"
"v_max3_u32 v252, v252, v180, v181\n"
"v_max3_u32 v252, v252, v182, v183\n"
"v_cmp_eq_u32_e32 vcc, -1, v252\n"
"s_cbranch_vccz .Lmk_p51k\n"
"s_sub_u32 s1, s1, 1\n"
"s_cbranch_scc0 .Lmk_p51l\n"
".Lmk_p51k:\n"
"v_mfma_f32_16x16x32_bf16 v[112:115], a[0:3], v[92:95], v[156:159]\n"
"v_mfma_f32_16x16x32_bf16 v[112:115], a[4:7], v[124:127], v[112:115]\n"
"v_mfma_f32_16x16x32_bf16 v[112:115], a[8:11], v[132:135], v[112:115]\n"
"v_mfma_f32_16x16x32_bf16 v[112:115], a[12:15], v[140:143], v[112:115]\n"
"v_mfma_f32_16x16x32_bf16 v[112:115], a[16:19], v[128:131], v[112:115]\n"
"v_mfma_f32_16x16x32_bf16 v[112:115], a[20:23], v[168:171], v[112:115]\n"
"v_mfma_f32_16x16x32_bf16 v[112:115], a[24:27], v[172:175], v[112:115]\n"
"v_mfma_f32_16x16x32_bf16 v[112:115], a[28:31], v[180:183], v[112:115]\n"
"v_mfma_f32_16x16x32_bf16 v[188:191], a[60:63], v[180:183], v[184:187]\n"
"v_mfma_f32_16x16x32_bf16 v[188:191], a[56:59], v[172:175], v[188:191]\n"
"v_mfma_f32_16x16x32_bf16 v[188:191], a[52:55], v[168:171], v[188:191]\n"
"v_mfma_f32_16x16x32_bf16 v[188:191], a[48:51], v[128:131], v[188:191]\n"
"v_mfma_f32_16x16x32_bf16 v[188:191], a[44:47], v[140:143], v[188:191]\n"
"v_mfma_f32_16x16x32_bf16 v[188:191], a[40:43], v[132:135], v[188:191]\n"
"s_nop 7\n"
"s_branch .Lmk_p51r\n"
".Lmk_f40s:\n"
";\n"
"s_mov_b32 s1, 0x10000\n"
".Lmk_f40l:\n"
"buffer_load_dwordx2 v[246:247], off, s[4:7], s96 sc1\n"
"s_waitcnt vmcnt(0)\n"
"v_cmp_ne_u64_e32 vcc, v[246:247], v[18:19]\n"
"s_cbranch_vccz .Lmk_f40r\n"
"s_sub_u32 s1, s1, 1\n"
"s_cbranch_scc0 .Lmk_f40l\n"
"s_branch .Lmk_f40r\n"
".Lmk_f41s:\n"
";\n"
"s_mov_b32 s1, 0x10000\n"
".Lmk_f41l:\n"
"buffer_load_dwordx2 v[250:251], v12, s[4:7], s98 offen sc1\n"
"s_waitcnt vmcnt(0)\n"
"v_cmp_ne_u64_e32 vcc, v[250:251], v[18:19]\n"
"s_cbranch_vccz .Lmk_f41r\n"
"s_sub_u32 s1, s1, 1\n"
"s_cbranch_scc0 .Lmk_f41l\n"
"s_branch .Lmk_f41r\n"
".Lmk_p55s:\n"
";\n"
"s_waitcnt vmcnt(16)\n"
"s_mov_b32 s1, 0x10000\n"
".Lmk_p55l:\n"
"s_add_u32 s96, s56, 0x440000\n"
"buffer_load_dwordx4 v[120:123], v0, s[4:7], s96 offen sc1\n"
"buffer_load_dwordx4 v[136:139], v0, s[4:7], s96 offen offset:1024 sc1\n"
"buffer_load_dwordx4 v[144:147], v0, s[4:7], s96 offen offset:2048 sc1\n"
"buffer_load_dwordx4 v[192:195], v0, s[4:7], s96 offen offset:3072 sc1\n"
"buffer_load_dwordx4 v[196:199], v1, s[4:7], s96 offen sc1\n"
"buffer_load_dwordx4 v[188:191], v1, s[4:7], s96 offen offset:1024 sc1\n"
"buffer_load_dwordx4 v[108:111], v1, s[4:7], s96 offen offset:2048 sc1\n"
"buffer_load_dwordx4 v[92:95], v1, s[4:7], s96 offen offset:3072 sc1\n"
"s_add_u32 s96, s57, 0x440000\n"
"buffer_load_dwordx4 v[148:151], v0, s[4:7], s96 offen sc1\n"
"buffer_load_dwordx4 v[180:183], v0, s[4:7], s96 offen offset:1024 sc1\n"
"buffer_load_dwordx4 v[24:27], v0, s[4:7], s96 offen offset:2048 sc1\n"
"buffer_load_dwordx4 v[32:35], v0, s[4:7], s96 offen offset:3072 sc1\n"
"buffer_load_dwordx4 v[44:47], v1, s[4:7], s96 offen sc1\n"
"buffer_load_dwordx4 v[48:51], v1, s[4:7], s96 offen offset:1024 sc1\n"
"buffer_load_dwordx4 v[28:31], v1, s[4:7], s96 offen offset:2048 sc1\n"
"buffer_load_dwordx4 v[56:59], v1, s[4:7], s96 offen offset:3072 sc1\n"
"s_add_u32 s96, s58, 0x440000\n"
"buffer_load_dwordx4 v[40:43], v0, s[4:7], s96 offen sc1\n"
"buffer_load_dwordx4 v[52:55], v0, s[4:7], s96 offen offset:1024 sc1\n"
"buffer_load_dwordx4 v[64:67], v0, s[4:7], s96 offen offset:2048 sc1\n"
"s_waitcnt vmcnt(0)\n"
"v_max_u32_e32 v255, v120, v121\n"
"v_max3_u32 v255, v255, v122, v123\n"
"v_max3_u32 v255, v255, v136, v137\n"
"v_max3_u32 v255, v255, v138, v139\n"
"v_max3_u32 v255, v255, v144, v145\n"
"v_max3_u32 v255, v255, v146, v147\n"
"v_max3_u32 v255, v255, v192, v193\n"
"v_max3_u32 v255, v255, v194, v195\n"
"v_max3_u32 v255, v255, v196, v197\n"
"v_max3_u32 v255, v255, v198, v199\n"
"v_max3_u32 v255, v255, v188, v189\n"
"v_max3_u32 v255, v255, v190, v191\n"
"v_max3_u32 v255, v255, v108, v109\n"
"v_max3_u32 v255, v255, v110, v111\n"
"v_max3_u32 v255, v255, v92, v93\n"
"v_max3_u32 v255, v255, v94, v95\n"
"v_cmp_eq_u32_e32 vcc, -1, v255\n"
"s_cbranch_vccz .Lmk_p55k\n"
"s_sub_u32 s1, s1, 1\n"
"s_cbranch_scc0 .Lmk_p55l\n"
".Lmk_p55k:\n"
"v_mfma_f32_16x16x32_bf16 v[36:39], a[128:131], v[120:123], 0\n"
"v_mfma_f32_16x16x32_bf16 v[36:39], a[132:135], v[136:139], v[36:39]\n"
"v_mfma_f32_16x16x32_bf16 v[36:39], a[136:139], v[144:147], v[36:39]\n"
"v_mfma_f32_16x16x32_bf16 v[36:39], a[140:143], v[192:195], v[36:39]\n"
"v_mfma_f32_16x16x32_bf16 v[36:39], a[144:147], v[196:199], v[36:39]\n"
"v_mfma_f32_16x16x32_bf16 v[36:39], a[148:151], v[188:191], v[36:39]\n"
"v_mfma_f32_16x16x32_bf16 v[36:39], a[152:155], v[108:111], v[36:39]\n"
"v_mfma_f32_16x16x32_bf16 v[36:39], a[156:159], v[92:95], v[36:39]\n"
"v_mfma_f32_16x16x32_bf16 v[60:63], a[188:191], v[92:95], 0\n"
"v_mfma_f32_16x16x32_bf16 v[60:63], a[184:187], v[108:111], v[60:63]\n"
"v_mfma_f32_16x16x32_bf16 v[60:63], a[180:183], v[188:191], v[60:63]\n"
"v_mfma_f32_16x16x32_bf16 v[60:63], a[176:179], v[196:199], v[60:63]\n"
"v_mfma_f32_16x16x32_bf16 v[60:63], a[172:175], v[192:195], v[60:63]\n"
"v_mfma_f32_16x16x32_bf16 v[60:63], a[168:171], v[144:147], v[60:63]\n"
"s_nop 7\n"
"s_branch .Lmk_p55r\n"
".Lmk_p56s:\n"
";\n"
"s_waitcnt vmcnt(16)\n"
"s_mov_b32 s1, 0x10000\n"
".Lmk_p56l:\n"
"s_add_u32 s96, s57, 0x440000\n"
"buffer_load_dwordx4 v[148:151], v0, s[4:7], s96 offen sc1\n"
"buffer_load_dwordx4 v[180:183], v0, s[4:7], s96 offen offset:1024 sc1\n"
"buffer_load_dwordx4 v[24:27], v0, s[4:7], s96 offen offset:2048 sc1\n"
"buffer_load_dwordx4 v[32:35], v0, s[4:7], s96 offen offset:3072 sc1\n"
"buffer_load_dwordx4 v[44:47], v1, s[4:7], s96 offen sc1\n"
"buffer_load_dwordx4 v[48:51], v1, s[4:7], s96 offen offset:1024 sc1\n"
"buffer_load_dwordx4 v[28:31], v1, s[4:7], s96 offen offset:2048 sc1\n"
"buffer_load_dwordx4 v[56:59], v1, s[4:7], s96 offen offset:3072 sc1\n"
"s_add_u32 s96, s58, 0x440000\n"
"buffer_load_dwordx4 v[40:43], v0, s[4:7], s96 offen sc1\n"
"buffer_load_dwordx4 v[52:55], v0, s[4:7], s96 offen offset:1024 sc1\n"
"buffer_load_dwordx4 v[64:67], v0, s[4:7], s96 offen offset:2048 sc1\n"
"buffer_load_dwordx4 v[36:39], v0, s[4:7], s96 offen offset:3072 sc1\n"
"buffer_load_dwordx4 v[72:75], v1, s[4:7], s96 offen sc1\n"
"buffer_load_dwordx4 v[60:63], v1, s[4:7], s96 offen offset:1024 sc1\n"
"buffer_load_dwordx4 v[80:83], v1, s[4:7], s96 offen offset:2048 sc1\n"
"buffer_load_dwordx4 v[68:71], v1, s[4:7], s96 offen offset:3072 sc1\n"
"s_add_u32 s96, s59, 0x440000\n"
"buffer_load_dwordx4 v[88:91], v0, s[4:7], s96 offen sc1\n"
"buffer_load_dwordx4 v[76:79], v0, s[4:7], s96 offen offset:1024 sc1\n"
"buffer_load_dwordx4 v[100:103], v0, s[4:7], s96 offen offset:2048 sc1\n"
"s_waitcnt vmcnt(0)\n"
"v_max_u32_e32 v254, v148, v149\n"
"v_max3_u32 v254, v254, v150, v151\n"
"v_max3_u32 v254, v254, v180, v181\n"
"v_max3_u32 v254, v254, v182, v183\n"
"v_max3_u32 v254, v254, v24, v25\n"
"v_max3_u32 v254, v254, v26, v27\n"
"v_max3_u32 v254, v254, v32, v33\n"
"v_max3_u32 v254, v254, v34, v35\n"
"v_max3_u32 v254, v254, v44, v45\n"
"v_max3_u32 v254, v254, v46, v47\n"
"v_max3_u32 v254, v254, v48, v49\n"
"v_max3_u32 v254, v254, v50, v51\n"
"v_max3_u32 v254, v254, v28, v29\n"
"v_max3_u32 v254, v254, v30, v31\n"
"v_max3_u32 v254, v254, v56, v57\n"
"v_max3_u32 v254, v254, v58, v59\n"
"v_cmp_eq_u32_e32 vcc, -1, v254\n"
"s_cbranch_vccz .Lmk_p56k\n"
"s_sub_u32 s1, s1, 1\n"
"s_cbranch_scc0 .Lmk_p56l\n"
".Lmk_p56k:\n"
"v_mfma_f32_16x16x32_bf16 v[84:87], a[128:131], v[148:151], 0\n"
"v_mfma_f32_16x16x32_bf16 v[84:87], a[132:135], v[180:183], v[84:87]\n"
"v_mfma_f32_16x16x32_bf16 v[84:87], a[136:139], v[24:27], v[84:87]\n"
"v_mfma_f32_16x16x32_bf16 v[84:87], a[140:143], v[32:35], v[84:87]\n"
"v_mfma_f32_16x16x32_bf16 v[84:87], a[144:147], v[44:47], v[84:87]\n"
"v_mfma_f32_16x16x32_bf16 v[84:87], a[148:151], v[48:51], v[84:87]\n"
"v_mfma_f32_16x16x32_bf16 v[84:87], a[152:155], v[28:31], v[84:87]\n"
"v_mfma_f32_16x16x32_bf16 v[84:87], a[156:159], v[56:59], v[84:87]\n"
"v_mfma_f32_16x16x32_bf16 v[96:99], a[188:191], v[56:59], 0\n"
"v_mfma_f32_16x16x32_bf16 v[96:99], a[184:187], v[28:31], v[96:99]\n"
"v_mfma_f32_16x16x32_bf16 v[96:99], a[180:183], v[48:51], v[96:99]\n"
"v_mfma_f32_16x16x32_bf16 v[96:99], a[176:179], v[44:47], v[96:99]\n"
"v_mfma_f32_16x16x32_bf16 v[96:99], a[172:175], v[32:35], v[96:99]\n"
"v_mfma_f32_16x16x32_bf16 v[96:99], a[168:171], v[24:27], v[96:99]\n"
"s_nop 7\n"
"s_branch .Lmk_p56r\n"
".Lmk_p57s:\n"
";\n"
"s_waitcnt vmcnt(16)\n"
"s_mov_b32 s1, 0x10000\n"
".Lmk_p57l:\n"
"s_add_u32 s96, s58, 0x440000\n"
"buffer_load_dwordx4 v[40:43], v0, s[4:7], s96 offen sc1\n"
"buffer_load_dwordx4 v[52:55], v0, s[4:7], s96 offen offset:1024 sc1\n"
"buffer_load_dwordx4 v[64:67], v0, s[4:7], s96 offen offset:2048 sc1\n"
"buffer_load_dwordx4 v[36:39], v0, s[4:7], s96 offen offset:3072 sc1\n"
"buffer_load_dwordx4 v[72:75], v1, s[4:7], s96 offen sc1\n"
"buffer_load_dwordx4 v[60:63], v1, s[4:7], s96 offen offset:1024 sc1\n"
"buffer_load_dwordx4 v[80:83], v1, s[4:7], s96 offen offset:2048 sc1\n"
"buffer_load_dwordx4 v[68:71], v1, s[4:7], s96 offen offset:3072 sc1\n"
"s_add_u32 s96, s59, 0x440000\n"
"buffer_load_dwordx4 v[88:91], v0, s[4:7], s96 offen sc1\n"
"buffer_load_dwordx4 v[76:79], v0, s[4:7], s96 offen offset:1024 sc1\n"
"buffer_load_dwordx4 v[100:103], v0, s[4:7], s96 offen offset:2048 sc1\n"
"buffer_load_dwordx4 v[84:87], v0, s[4:7], s96 offen offset:3072 sc1\n"
"buffer_load_dwordx4 v[112:115], v1, s[4:7], s96 offen sc1\n"
"buffer_load_dwordx4 v[96:99], v1, s[4:7], s96 offen offset:1024 sc1\n"
"buffer_load_dwordx4 v[132:135], v1, s[4:7], s96 offen offset:2048 sc1\n"
"buffer_load_dwordx4 v[104:107], v1, s[4:7], s96 offen offset:3072 sc1\n"
"s_waitcnt vmcnt(0)\n"
"v_max_u32_e32 v237, v40, v41\n"
"v_max3_u32 v237, v237, v42, v43\n"
"v_max3_u32 v237, v237, v52, v53\n"
"v_max3_u32 v237, v237, v54, v55\n"
"v_max3_u32 v237, v237, v64, v65\n"
"v_max3_u32 v237, v237, v66, v67\n"
"v_max3_u32 v237, v237, v36, v37\n"
"v_max3_u32 v237, v237, v38, v39\n"
"v_max3_u32 v237, v237, v72, v73\n"
"v_max3_u32 v237, v237, v74, v75\n"
"v_max3_u32 v237, v237, v60, v61\n"
"v_max3_u32 v237, v237, v62, v63\n"
"v_max3_u32 v237, v237, v80, v81\n"
"v_max3_u32 v237, v237, v82, v83\n"
"v_max3_u32 v237, v237, v68, v69\n"
"v_max3_u32 v237, v237, v70, v71\n"
"v_cmp_eq_u32_e32 vcc, -1, v237\n"
"s_cbranch_vccz .Lmk_p57k\n"
"s_sub_u32 s1, s1, 1\n"
"s_cbranch_scc0 .Lmk_p57l\n"
".Lmk_p57k:\n"
"v_mfma_f32_16x16x32_bf16 v[116:119], a[128:131], v[40:43], 0\n"
"v_mfma_f32_16x16x32_bf16 v[116:119], a[132:135], v[52:55], v[116:119]\n"
"v_mfma_f32_16x16x32_bf16 v[116:119], a[136:139], v[64:67], v[116:119]\n"
"v_mfma_f32_16x16x32_bf16 v[116:119], a[140:143], v[36:39], v[116:119]\n"
"v_mfma_f32_16x16x32_bf16 v[116:119], a[144:147], v[72:75], v[116:119]\n"
"v_mfma_f32_16x16x32_bf16 v[116:119], a[148:151], v[60:63], v[116:119]\n"
"v_mfma_f32_16x16x32_bf16 v[116:119], a[152:155], v[80:83], v[116:119]\n"
"v_mfma_f32_16x16x32_bf16 v[116:119], a[156:159], v[68:71], v[116:119]\n"
"v_mfma_f32_16x16x32_bf16 v[124:127], a[188:191], v[68:71], 0\n"
"v_mfma_f32_16x16x32_bf16 v[124:127], a[184:187], v[80:83], v[124:127]\n"
"v_mfma_f32_16x16x32_bf16 v[124:127], a[180:183], v[60:63], v[124:127]\n"
"v_mfma_f32_16x16x32_bf16 v[124:127], a[176:179], v[72:75], v[124:127]\n"
"v_mfma_f32_16x16x32_bf16 v[124:127], a[172:175], v[36:39], v[124:127]\n"
"v_mfma_f32_16x16x32_bf16 v[124:127], a[168:171], v[64:67], v[124:127]\n"
"s_nop 7\n"
"s_branch .Lmk_p57r\n"
".Lmk_p58s:\n"
";\n"
"s_waitcnt vmcnt(16)\n"
"s_mov_b32 s1, 0x10000\n"
".Lmk_p58l:\n"
"s_add_u32 s96, s59, 0x440000\n"
"buffer_load_dwordx4 v[88:91], v0, s[4:7], s96 offen sc1\n"
"buffer_load_dwordx4 v[76:79], v0, s[4:7], s96 offen offset:1024 sc1\n"
"buffer_load_dwordx4 v[100:103], v0, s[4:7], s96 offen offset:2048 sc1\n"
"buffer_load_dwordx4 v[84:87], v0, s[4:7], s96 offen offset:3072 sc1\n"
"buffer_load_dwordx4 v[112:115], v1, s[4:7], s96 offen sc1\n"
"buffer_load_dwordx4 v[96:99], v1, s[4:7], s96 offen offset:1024 sc1\n"
"buffer_load_dwordx4 v[132:135], v1, s[4:7], s96 offen offset:2048 sc1\n"
"buffer_load_dwordx4 v[104:107], v1, s[4:7], s96 offen offset:3072 sc1\n"
"s_add_u32 s96, s56, 0x400000\n"
"buffer_load_dwordx4 v[116:119], v0, s[4:7], s96 offen sc1\n"
"buffer_load_dwordx4 v[164:167], v0, s[4:7], s96 offen offset:1024 sc1\n"
"buffer_load_dwordx4 v[124:127], v0, s[4:7], s96 offen offset:2048 sc1\n"
"buffer_load_dwordx4 v[68:71], v0, s[4:7], s96 offen offset:3072 sc1\n"
"buffer_load_dwordx4 v[36:39], v1, s[4:7], s96 offen sc1\n"
"buffer_load_dwordx4 v[40:43], v1, s[4:7], s96 offen offset:1024 sc1\n"
"buffer_load_dwordx4 v[72:75], v1, s[4:7], s96 offen offset:2048 sc1\n"
"buffer_load_dwordx4 v[80:83], v1, s[4:7], s96 offen offset:3072 sc1\n"
"s_add_u32 s96, s57, 0x400000\n"
"buffer_load_dwordx4 v[152:155], v0, s[4:7], s96 offen sc1\n"
"s_waitcnt vmcnt(0)\n"
"v_max_u32_e32 v254, v88, v89\n"
"v_max3_u32 v254, v254, v90, v91\n"
"v_max3_u32 v254, v254, v76, v77\n"
"v_max3_u32 v254, v254, v78, v79\n"
"v_max3_u32 v254, v254, v100, v101\n"
"v_max3_u32 v254, v254, v102, v103\n"
"v_max3_u32 v254, v254, v84, v85\n"
"v_max3_u32 v254, v254, v86, v87\n"
"v_max3_u32 v254, v254, v112, v113\n"
"v_max3_u32 v254, v254, v114, v115\n"
"v_max3_u32 v254, v254, v96, v97\n"
"v_max3_u32 v254, v254, v98, v99\n"
"v_max3_u32 v254, v254, v132, v133\n"
"v_max3_u32 v254, v254, v134, v135\n"
"v_max3_u32 v254, v254, v104, v105\n"
"v_max3_u32 v254, v254, v106, v107\n"
"v_cmp_eq_u32_e32 vcc, -1, v254\n"
"s_cbranch_vccz .Lmk_p58k\n"
"s_sub_u32 s1, s1, 1\n"
"s_cbranch_scc0 .Lmk_p58l\n"
".Lmk_p58k:\n"
"v_mfma_f32_16x16x32_bf16 v[64:67], a[128:131], v[88:91], v[128:131]\n"
"v_mfma_f32_16x16x32_bf16 v[64:67], a[132:135], v[76:79], v[64:67]\n"
"v_mfma_f32_16x16x32_bf16 v[64:67], a[136:139], v[100:103], v[64:67]\n"
"v_mfma_f32_16x16x32_bf16 v[64:67], a[140:143], v[84:87], v[64:67]\n"
"v_mfma_f32_16x16x32_bf16 v[64:67], a[144:147], v[112:115], v[64:67]\n"
"v_mfma_f32_16x16x32_bf16 v[64:67], a[148:151], v[96:99], v[64:67]\n"
"v_mfma_f32_16x16x32_bf16 v[64:67], a[152:155], v[132:135], v[64:67]\n"
"v_mfma_f32_16x16x32_bf16 v[64:67], a[156:159], v[104:107], v[64:67]\n"
"v_mfma_f32_16x16x32_bf16 v[60:63], a[188:191], v[104:107], v[156:159]\n"
"v_mfma_f32_16x16x32_bf16 v[60:63], a[184:187], v[132:135], v[60:63]\n"
"v_mfma_f32_16x16x32_bf16 v[60:63], a[180:183], v[96:99], v[60:63]\n"
"v_mfma_f32_16x16x32_bf16 v[60:63], a[176:179], v[112:115], v[60:63]\n"
"v_mfma_f32_16x16x32_bf16 v[60:63], a[172:175], v[84:87], v[60:63]\n"
"v_mfma_f32_16x16x32_bf16 v[60:63], a[168:171], v[100:103], v[60:63]\n"
"s_nop 7\n"
"s_branch .Lmk_p58r\n"
".Lmk_p59s:\n"
";\n"
"s_waitcnt vmcnt(16)\n"
"s_mov_b32 s1, 0x10000\n"
".Lmk_p59l:\n"
"s_add_u32 s96, s56, 0x400000\n"
"buffer_load_dwordx4 v[116:119], v0, s[4:7], s96 offen sc1\n"
"buffer_load_dwordx4 v[164:167], v0, s[4:7], s96 offen offset:1024 sc1\n"
"buffer_load_dwordx4 v[124:127], v0, s[4:7], s96 offen offset:2048 sc1\n"
"buffer_load_dwordx4 v[68:71], v0, s[4:7], s96 offen offset:3072 sc1\n"
"buffer_load_dwordx4 v[36:39], v1, s[4:7], s96 offen sc1\n"
"buffer_load_dwordx4 v[40:43], v1, s[4:7], s96 offen offset:1024 sc1\n"
"buffer_load_dwordx4 v[72:75], v1, s[4:7], s96 offen offset:2048 sc1\n"
"buffer_load_dwordx4 v[80:83], v1, s[4:7], s96 offen offset:3072 sc1\n"
"s_add_u32 s96, s57, 0x400000\n"
"buffer_load_dwordx4 v[152:155], v0, s[4:7], s96 offen sc1\n"
"buffer_load_dwordx4 v[156:159], v0, s[4:7], s96 offen offset:1024 sc1\n"
"buffer_load_dwordx4 v[176:179], v0, s[4:7], s96 offen offset:2048 sc1\n"
"buffer_load_dwordx4 v[184:187], v0, s[4:7], s96 offen offset:3072 sc1\n"
"buffer_load_dwordx4 v[64:67], v1, s[4:7], s96 offen sc1\n"
"buffer_load_dwordx4 v[96:99], v1, s[4:7], s96 offen offset:1024 sc1\n"
"buffer_load_dwordx4 v[60:63], v1, s[4:7], s96 offen offset:2048 sc1\n"
"buffer_load_dwordx4 v[76:79], v1, s[4:7], s96 offen offset:3072 sc1\n"
"s_add_u32 s96, s58, 0x400000\n"
"buffer_load_dwordx4 v[88:91], v0, s[4:7], s96 offen sc1\n"
"buffer_load_dwordx4 v[100:103], v0, s[4:7], s96 offen offset:1024 sc1\n"
"buffer_load_dwordx4 v[52:55], v0, s[4:7], s96 offen offset:2048 sc1\n"
"buffer_load_dwordx4 v[128:131], v0, s[4:7], s96 offen offset:3072 sc1\n"
"s_waitcnt vmcnt(0)\n"
"v_max_u32_e32 v255, v116, v117\n"
"v_max3_u32 v255, v255, v118, v119\n"
"v_max3_u32 v255, v255, v164, v165\n"
"v_max3_u32 v255, v255, v166, v167\n"
"v_max3_u32 v255, v255, v124, v125\n"
"v_max3_u32 v255, v255, v126, v127\n"
"v_max3_u32 v255, v255, v68, v69\n"
"v_max3_u32 v255, v255, v70, v71\n"
"v_max3_u32 v255, v255, v36, v37\n"
"v_max3_u32 v255, v255, v38, v39\n"
"v_max3_u32 v255, v255, v40, v41\n"
"v_max3_u32 v255, v255, v42, v43\n"
"v_max3_u32 v255, v255, v72, v73\n"
"v_max3_u32 v255, v255, v74, v75\n"
"v_max3_u32 v255, v255, v80, v81\n"
"v_max3_u32 v255, v255, v82, v83\n"
"v_cmp_eq_u32_e32 vcc, -1, v255\n"
"s_cbranch_vccz .Lmk_p59k\n"
"s_sub_u32 s1, s1, 1\n"
"s_cbranch_scc0 .Lmk_p59l\n"
".Lmk_p59k:\n"
"v_mfma_f32_16x16x32_bf16 v[84:87], a[128:131], v[116:119], 0\n"
"v_mfma_f32_16x16x32_bf16 v[84:87], a[132:135], v[164:167], v[84:87]\n"
"v_mfma_f32_16x16x32_bf16 v[84:87], a[136:139], v[124:127], v[84:87]\n"
"v_mfma_f32_16x16x32_bf16 v[84:87], a[140:143], v[68:71], v[84:87]\n"
"v_mfma_f32_16x16x32_bf16 v[84:87], a[144:147], v[36:39], v[84:87]\n"
"v_mfma_f32_16x16x32_bf16 v[84:87], a[148:151], v[40:43], v[84:87]\n"
"v_mfma_f32_16x16x32_bf16 v[84:87], a[152:155], v[72:75], v[84:87]\n"
"v_mfma_f32_16x16x32_bf16 v[84:87], a[156:159], v[80:83], v[84:87]\n"
"v_mfma_f32_16x16x32_bf16 v[112:115], a[188:191], v[80:83], 0\n"
"v_mfma_f32_16x16x32_bf16 v[112:115], a[184:187], v[72:75], v[112:115]\n"
"v_mfma_f32_16x16x32_bf16 v[112:115], a[180:183], v[40:43], v[112:115]\n"
"v_mfma_f32_16x16x32_bf16 v[112:115], a[176:179], v[36:39], v[112:115]\n"
"v_mfma_f32_16x16x32_bf16 v[112:115], a[172:175], v[68:71], v[112:115]\n"
"v_mfma_f32_16x16x32_bf16 v[112:115], a[168:171], v[124:127], v[112:115]\n"
"s_nop 7\n"
"s_branch .Lmk_p59r\n"
".Lmk_p60s:\n"
";\n"
"s_waitcnt vmcnt(16)\n"
"s_mov_b32 s1, 0x10000\n"
".Lmk_p60l:\n"
"s_add_u32 s96, s57, 0x400000\n"
"buffer_load_dwordx4 v[152:155], v0, s[4:7], s96 offen sc1\n"
"buffer_load_dwordx4 v[156:159], v0, s[4:7], s96 offen offset:1024 sc1\n"
"buffer_load_dwordx4 v[176:179], v0, s[4:7], s96 offen offset:2048 sc1\n"
"buffer_load_dwordx4 v[184:187], v0, s[4:7], s96 offen offset:3072 sc1\n"
"buffer_load_dwordx4 v[64:67], v1, s[4:7], s96 offen sc1\n"
"buffer_load_dwordx4 v[96:99], v1, s[4:7], s96 offen offset:1024 sc1\n"
"buffer_load_dwordx4 v[60:63], v1, s[4:7], s96 offen offset:2048 sc1\n"
"buffer_load_dwordx4 v[76:79], v1, s[4:7], s96 offen offset:3072 sc1\n"
"s_add_u32 s96, s58, 0x400000\n"
"buffer_load_dwordx4 v[88:91], v0, s[4:7], s96 offen sc1\n"
"buffer_load_dwordx4 v[100:103], v0, s[4:7], s96 offen offset:1024 sc1\n"
"buffer_load_dwordx4 v[52:55], v0, s[4:7], s96 offen offset:2048 sc1\n"
"buffer_load_dwordx4 v[128:131], v0, s[4:7], s96 offen offset:3072 sc1\n"
"buffer_load_dwordx4 v[84:87], v1, s[4:7], s96 offen sc1\n"
"buffer_load_dwordx4 v[160:163], v1, s[4:7], s96 offen offset:1024 sc1\n"
"buffer_load_dwordx4 v[112:115], v1, s[4:7], s96 offen offset:2048 sc1\n"
"buffer_load_dwordx4 v[72:75], v1, s[4:7], s96 offen offset:3072 sc1\n"
"s_add_u32 s96, s59, 0x400000\n"
"buffer_load_dwordx4 v[36:39], v0, s[4:7], s96 offen sc1\n"
"buffer_load_dwordx4 v[40:43], v0, s[4:7], s96 offen offset:1024 sc1\n"
"buffer_load_dwordx4 v[80:83], v0, s[4:7], s96 offen offset:2048 sc1\n"
"buffer_load_dwordx4 v[116:119], v0, s[4:7], s96 offen offset:3072 sc1\n"
"buffer_load_dwordx4 v[132:135], v1, s[4:7], s96 offen sc1\n"
"s_waitcnt vmcnt(0)\n"
"v_max_u32_e32 v254, v152, v153\n"
"v_max3_u32 v254, v254, v154, v155\n"
"v_max3_u32 v254, v254, v156, v157\n"
"v_max3_u32 v254, v254, v158, v159\n"
"v_max3_u32 v254, v254, v176, v177\n"
"v_max3_u32 v254, v254, v178, v179\n"
"v_max3_u32 v254, v254, v184, v185\n"
"v_max3_u32 v254, v254, v186, v187\n"
"v_max3_u32 v254, v254, v64, v65\n"
"v_max3_u32 v254, v254, v66, v67\n"
"v_max3_u32 v254, v254, v96, v97\n"
"v_max3_u32 v254, v254, v98, v99\n"
"v_max3_u32 v254, v254, v60, v61\n"
"v_max3_u32 v254, v254, v62, v63\n"
"v_max3_u32 v254, v254, v76, v77\n"
"v_max3_u32 v254, v254, v78, v79\n"
"v_cmp_eq_u32_e32 vcc, -1, v254\n"
"s_cbranch_vccz .Lmk_p60k\n"
"s_sub_u32 s1, s1, 1\n"
"s_cbranch_scc0 .Lmk_p60l\n"
".Lmk_p60k:\n"
"v_mfma_f32_16x16x32_bf16 v[68:71], a[128:131], v[152:155], 0\n"
"v_mfma_f32_16x16x32_bf16 v[68:71], a[132:135], v[156:159], v[68:71]\n"
"v_mfma_f32_16x16x32_bf16 v[68:71], a[136:139], v[176:179], v[68:71]\n"
"v_mfma_f32_16x16x32_bf16 v[68:71], a[140:143], v[184:187], v[68:71]\n"
"v_mfma_f32_16x16x32_bf16 v[68:71], a[144:147], v[64:67], v[68:71]\n"
"v_mfma_f32_16x16x32_bf16 v[68:71], a[148:151], v[96:99], v[68:71]\n"
"v_mfma_f32_16x16x32_bf16 v[68:71], a[152:155], v[60:63], v[68:71]\n"
"v_mfma_f32_16x16x32_bf16 v[68:71], a[156:159], v[76:79], v[68:71]\n"
"v_mfma_f32_16x16x32_bf16 v[124:127], a[188:191], v[76:79], 0\n"
"v_mfma_f32_16x16x32_bf16 v[124:127], a[184:187], v[60:63], v[124:127]\n"
"v_mfma_f32_16x16x32_bf16 v[124:127], a[180:183], v[96:99], v[124:127]\n"
"v_mfma_f32_16x16x32_bf16 v[124:127], a[176:179], v[64:67], v[124:127]\n"
"v_mfma_f32_16x16x32_bf16 v[124:127], a[172:175], v[184:187], v[124:127]\n"
"v_mfma_f32_16x16x32_bf16 v[124:127], a[168:171], v[176:179], v[124:127]\n"
"s_nop 7\n"
"s_branch .Lmk_p60r\n"
".Lmk_p61s:\n"
";\n"
"s_waitcnt vmcnt(16)\n"
"s_mov_b32 s1, 0x10000\n"
".Lmk_p61l:\n"
"s_add_u32 s97, s58, 0x400000\n"
"buffer_load_dwordx4 v[88:91], v0, s[4:7], s97 offen sc1\n"
"buffer_load_dwordx4 v[100:103], v0, s[4:7], s97 offen offset:1024 sc1\n"
"buffer_load_dwordx4 v[52:55], v0, s[4:7], s97 offen offset:2048 sc1\n"
"buffer_load_dwordx4 v[128:131], v0, s[4:7], s97 offen offset:3072 sc1\n"
"buffer_load_dwordx4 v[84:87], v1, s[4:7], s97 offen sc1\n"
"buffer_load_dwordx4 v[160:163], v1, s[4:7], s97 offen offset:1024 sc1\n"
"buffer_load_dwordx4 v[112:115], v1, s[4:7], s97 offen offset:2048 sc1\n"
"buffer_load_dwordx4 v[72:75], v1, s[4:7], s97 offen offset:3072 sc1\n"
"s_add_u32 s97, s59, 0x400000\n"
"buffer_load_dwordx4 v[36:39], v0, s[4:7], s97 offen sc1\n"
"buffer_load_dwordx4 v[40:43], v0, s[4:7], s97 offen offset:1024 sc1\n"
"buffer_load_dwordx4 v[80:83], v0, s[4:7], s97 offen offset:2048 sc1\n"
"buffer_load_dwordx4 v[116:119], v0, s[4:7], s97 offen offset:3072 sc1\n"
"buffer_load_dwordx4 v[132:135], v1, s[4:7], s97 offen sc1\n"
"buffer_load_dwordx4 v[68:71], v1, s[4:7], s97 offen offset:1024 sc1\n"
"buffer_load_dwordx4 v[168:171], v1, s[4:7], s97 offen offset:2048 sc1\n"
"buffer_load_dwordx4 v[124:127], v1, s[4:7], s97 offen offset:3072 sc1\n"
"s_waitcnt vmcnt(0)\n"
"v_max_u32_e32 v253, v88, v89\n"
"v_max3_u32 v253, v253, v90, v91\n"
"v_max3_u32 v253, v253, v100, v101\n"
"v_max3_u32 v253, v253, v102, v103\n"
"v_max3_u32 v253, v253, v52, v53\n"
"v_max3_u32 v253, v253, v54, v55\n"
"v_max3_u32 v253, v253, v128, v129\n"
"v_max3_u32 v253, v253, v130, v131\n"
"v_max3_u32 v253, v253, v84, v85\n"
"v_max3_u32 v253, v253, v86, v87\n"
"v_max3_u32 v253, v253, v160, v161\n"
"v_max3_u32 v253, v253, v162, v163\n"
"v_max3_u32 v253, v253, v112, v113\n"
"v_max3_u32 v253, v253, v114, v115\n"
"v_max3_u32 v253, v253, v72, v73\n"
"v_max3_u32 v253, v253, v74, v75\n"
"v_cmp_eq_u32_e32 vcc, -1, v253\n"
"s_cbranch_vccz .Lmk_p61k\n"
"s_sub_u32 s1, s1, 1\n"
"s_cbranch_scc0 .Lmk_p61l\n"
".Lmk_p61k:\n"
"v_mfma_f32_16x16x32_bf16 v[60:63], a[128:131], v[88:91], 0\n"
"v_mfma_f32_16x16x32_bf16 v[60:63], a[132:135], v[100:103], v[60:63]\n"
"v_mfma_f32_16x16x32_bf16 v[60:63], a[136:139], v[52:55], v[60:63]\n"
"v_mfma_f32_16x16x32_bf16 v[60:63], a[140:143], v[128:131], v[60:63]\n"
"v_mfma_f32_16x16x32_bf16 v[60:63], a[144:147], v[84:87], v[60:63]\n"
"v_mfma_f32_16x16x32_bf16 v[60:63], a[148:151], v[160:163], v[60:63]\n"
"v_mfma_f32_16x16x32_bf16 v[60:63], a[152:155], v[112:115], v[60:63]\n"
"v_mfma_f32_16x16x32_bf16 v[60:63], a[156:159], v[72:75], v[60:63]\n"
"v_mfma_f32_16x16x32_bf16 v[64:67], a[188:191], v[72:75], 0\n"
"v_mfma_f32_16x16x32_bf16 v[64:67], a[184:187], v[112:115], v[64:67]\n"
"v_mfma_f32_16x16x32_bf16 v[64:67], a[180:183], v[160:163], v[64:67]\n"
"v_mfma_f32_16x16x32_bf16 v[64:67], a[176:179], v[84:87], v[64:67]\n"
"v_mfma_f32_16x16x32_bf16 v[64:67], a[172:175], v[128:131], v[64:67]\n"
"v_mfma_f32_16x16x32_bf16 v[64:67], a[168:171], v[52:55], v[64:67]\n"
"s_nop 7\n"
"s_branch .Lmk_p61r\n"
".Lmk_p62s:\n"
";\n"
"s_waitcnt vmcnt(16)\n"
"s_mov_b32 s1, 0x10000\n"
".Lmk_p62l:\n"
"s_add_u32 s97, s59, 0x400000\n"
"buffer_load_dwordx4 v[36:39], v0, s[4:7], s97 offen sc1\n"
"buffer_load_dwordx4 v[40:43], v0, s[4:7], s97 offen offset:1024 sc1\n"
"buffer_load_dwordx4 v[80:83], v0, s[4:7], s97 offen offset:2048 sc1\n"
"buffer_load_dwordx4 v[116:119], v0, s[4:7], s97 offen offset:3072 sc1\n"
"buffer_load_dwordx4 v[132:135], v1, s[4:7], s97 offen sc1\n"
"buffer_load_dwordx4 v[68:71], v1, s[4:7], s97 offen offset:1024 sc1\n"
"buffer_load_dwordx4 v[168:171], v1, s[4:7], s97 offen offset:2048 sc1\n"
"buffer_load_dwordx4 v[124:127], v1, s[4:7], s97 offen offset:3072 sc1\n"
"s_waitcnt vmcnt(0)\n"
"v_max_u32_e32 v254, v36, v37\n"
"v_max3_u32 v254, v254, v38, v39\n"
"v_max3_u32 v254, v254, v40, v41\n"
"v_max3_u32 v254, v254, v42, v43\n"
"v_max3_u32 v254, v254, v80, v81\n"
"v_max3_u32 v254, v254, v82, v83\n"
"v_max3_u32 v254, v254, v116, v117\n"
"v_max3_u32 v254, v254, v118, v119\n"
"v_max3_u32 v254, v254, v132, v133\n"
"v_max3_u32 v254, v254, v134, v135\n"
"v_max3_u32 v254, v254, v68, v69\n"
"v_max3_u32 v254, v254, v70, v71\n"
"v_max3_u32 v254, v254, v168, v169\n"
"v_max3_u32 v254, v254, v170, v171\n"
"v_max3_u32 v254, v254, v124, v125\n"
"v_max3_u32 v254, v254, v126, v127\n"
"v_cmp_eq_u32_e32 vcc, -1, v254\n"
"s_cbranch_vccz .Lmk_p62k\n"
"s_sub_u32 s1, s1, 1\n"
"s_cbranch_scc0 .Lmk_p62l\n"
".Lmk_p62k:\n"
"v_mfma_f32_16x16x32_bf16 v[52:55], a[128:131], v[36:39], v[76:79]\n"
"v_mfma_f32_16x16x32_bf16 v[52:55], a[132:135], v[40:43], v[52:55]\n"
"v_mfma_f32_16x16x32_bf16 v[52:55], a[136:139], v[80:83], v[52:55]\n"
"v_mfma_f32_16x16x32_bf16 v[52:55], a[140:143], v[116:119], v[52:55]\n"
"v_mfma_f32_16x16x32_bf16 v[52:55], a[144:147], v[132:135], v[52:55]\n"
"v_mfma_f32_16x16x32_bf16 v[52:55], a[148:151], v[68:71], v[52:55]\n"
"v_mfma_f32_16x16x32_bf16 v[52:55], a[152:155], v[168:171], v[52:55]\n"
"v_mfma_f32_16x16x32_bf16 v[52:55], a[156:159], v[124:127], v[52:55]\n"
"v_mfma_f32_16x16x32_bf16 v[60:63], a[188:191], v[124:127], v[104:107]\n"
"v_mfma_f32_16x16x32_bf16 v[60:63], a[184:187], v[168:171], v[60:63]\n"
"v_mfma_f32_16x16x32_bf16 v[60:63], a[180:183], v[68:71], v[60:63]\n"
"v_mfma_f32_16x16x32_bf16 v[60:63], a[176:179], v[132:135], v[60:63]\n"
"v_mfma_f32_16x16x32_bf16 v[60:63], a[172:175], v[116:119], v[60:63]\n"
"v_mfma_f32_16x16x32_bf16 v[60:63], a[168:171], v[80:83], v[60:63]\n"
"s_nop 7\n"
"s_branch .Lmk_p62r\n"
".Lmk_p63s:\n"
";\n"
"s_waitcnt vmcnt(16)\n"
"s_mov_b32 s1, 0x10000\n"
".Lmk_p63l:\n"
"s_add_u32 s96, s56, 0x440000\n"
"buffer_load_dwordx4 v[120:123], v0, s[4:7], s96 offen sc1\n"
"buffer_load_dwordx4 v[136:139], v0, s[4:7], s96 offen offset:1024 sc1\n"
"buffer_load_dwordx4 v[144:147], v0, s[4:7], s96 offen offset:2048 sc1\n"
"buffer_load_dwordx4 v[192:195], v0, s[4:7], s96 offen offset:3072 sc1\n"
"buffer_load_dwordx4 v[196:199], v1, s[4:7], s96 offen sc1\n"
"buffer_load_dwordx4 v[188:191], v1, s[4:7], s96 offen offset:1024 sc1\n"
"buffer_load_dwordx4 v[108:111], v1, s[4:7], s96 offen offset:2048 sc1\n"
"buffer_load_dwordx4 v[92:95], v1, s[4:7], s96 offen offset:3072 sc1\n"
"s_add_u32 s96, s57, 0x440000\n"
"buffer_load_dwordx4 v[148:151], v0, s[4:7], s96 offen sc1\n"
"buffer_load_dwordx4 v[180:183], v0, s[4:7], s96 offen offset:1024 sc1\n"
"buffer_load_dwordx4 v[24:27], v0, s[4:7], s96 offen offset:2048 sc1\n"
"buffer_load_dwordx4 v[32:35], v0, s[4:7], s96 offen offset:3072 sc1\n"
"buffer_load_dwordx4 v[44:47], v1, s[4:7], s96 offen sc1\n"
"buffer_load_dwordx4 v[48:51], v1, s[4:7], s96 offen offset:1024 sc1\n"
"buffer_load_dwordx4 v[28:31], v1, s[4:7], s96 offen offset:2048 sc1\n"
"buffer_load_dwordx4 v[56:59], v1, s[4:7], s96 offen offset:3072 sc1\n"
"s_add_u32 s96, s58, 0x440000\n"
"buffer_load_dwordx4 v[36:39], v0, s[4:7], s96 offen sc1\n"
"buffer_load_dwordx4 v[40:43], v0, s[4:7], s96 offen offset:1024 sc1\n"
"buffer_load_dwordx4 v[52:55], v0, s[4:7], s96 offen offset:2048 sc1\n"
"buffer_load_dwordx4 v[60:63], v0, s[4:7], s96 offen offset:3072 sc1\n"
"buffer_load_dwordx4 v[64:67], v1, s[4:7], s96 offen sc1\n"
"buffer_load_dwordx4 v[68:71], v1, s[4:7], s96 offen offset:1024 sc1\n"
"buffer_load_dwordx4 v[72:75], v1, s[4:7], s96 offen offset:2048 sc1\n"
"buffer_load_dwordx4 v[76:79], v1, s[4:7], s96 offen offset:3072 sc1\n"
"s_add_u32 s96, s59, 0x440000\n"
"buffer_load_dwordx4 v[84:87], v0, s[4:7], s96 offen sc1\n"
"buffer_load_dwordx4 v[88:91], v0, s[4:7], s96 offen offset:1024 sc1\n"
"buffer_load_dwordx4 v[100:103], v0, s[4:7], s96 offen offset:2048 sc1\n"
"buffer_load_dwordx4 v[104:107], v0, s[4:7], s96 offen offset:3072 sc1\n"
"s_waitcnt vmcnt(0)\n"
"v_max_u32_e32 v255, v120, v121\n"
"v_max3_u32 v255, v255, v122, v123\n"
"v_max3_u32 v255, v255, v136, v137\n"
"v_max3_u32 v255, v255, v138, v139\n"
"v_max3_u32 v255, v255, v144, v145\n"
"v_max3_u32 v255, v255, v146, v147\n"
"v_max3_u32 v255, v255, v192, v193\n"
"v_max3_u32 v255, v255, v194, v195\n"
"v_max3_u32 v255, v255, v196, v197\n"
"v_max3_u32 v255, v255, v198, v199\n"
"v_max3_u32 v255, v255, v188, v189\n"
"v_max3_u32 v255, v255, v190, v191\n"
"v_max3_u32 v255, v255, v108, v109\n"
"v_max3_u32 v255, v255, v110, v111\n"
"v_max3_u32 v255, v255, v92, v93\n"
"v_max3_u32 v255, v255, v94, v95\n"
"v_cmp_eq_u32_e32 vcc, -1, v255\n"
"s_cbranch_vccz .Lmk_p63k\n"
"s_sub_u32 s1, s1, 1\n"
"s_cbranch_scc0 .Lmk_p63l\n"
".Lmk_p63k:\n"
"v_mfma_f32_16x16x32_bf16 v[80:83], a[128:131], v[120:123], 0\n"
"v_mfma_f32_16x16x32_bf16 v[80:83], a[132:135], v[136:139], v[80:83]\n"
"v_mfma_f32_16x16x32_bf16 v[80:83], a[136:139], v[144:147], v[80:83]\n"
"v_mfma_f32_16x16x32_bf16 v[80:83], a[140:143], v[192:195], v[80:83]\n"
"v_mfma_f32_16x16x32_bf16 v[80:83], a[144:147], v[196:199], v[80:83]\n"
"v_mfma_f32_16x16x32_bf16 v[80:83], a[148:151], v[188:191], v[80:83]\n"
"v_mfma_f32_16x16x32_bf16 v[80:83], a[152:155], v[108:111], v[80:83]\n"
"v_mfma_f32_16x16x32_bf16 v[80:83], a[156:159], v[92:95], v[80:83]\n"
"v_mfma_f32_16x16x32_bf16 v[96:99], a[188:191], v[92:95], 0\n"
"v_mfma_f32_16x16x32_bf16 v[96:99], a[184:187], v[108:111], v[96:99]\n"
"v_mfma_f32_16x16x32_bf16 v[96:99], a[180:183], v[188:191], v[96:99]\n"
"v_mfma_f32_16x16x32_bf16 v[96:99], a[176:179], v[196:199], v[96:99]\n"
"v_mfma_f32_16x16x32_bf16 v[96:99], a[172:175], v[192:195], v[96:99]\n"
"v_mfma_f32_16x16x32_bf16 v[96:99], a[168:171], v[144:147], v[96:99]\n"
"s_nop 7\n"
"s_branch .Lmk_p63r\n"
".Lmk_p64s:\n"
";\n"
"s_waitcnt vmcnt(16)\n"
"s_mov_b32 s1, 0x10000\n"
".Lmk_p64l:\n"
"s_add_u32 s96, s57, 0x440000\n"
"buffer_load_dwordx4 v[148:151], v0, s[4:7], s96 offen sc1\n"
"buffer_load_dwordx4 v[180:183], v0, s[4:7], s96 offen offset:1024 sc1\n"
"buffer_load_dwordx4 v[24:27], v0, s[4:7], s96 offen offset:2048 sc1\n"
"buffer_load_dwordx4 v[32:35], v0, s[4:7], s96 offen offset:3072 sc1\n"
"buffer_load_dwordx4 v[44:47], v1, s[4:7], s96 offen sc1\n"
"buffer_load_dwordx4 v[48:51], v1, s[4:7], s96 offen offset:1024 sc1\n"
"buffer_load_dwordx4 v[28:31], v1, s[4:7], s96 offen offset:2048 sc1\n"
"buffer_load_dwordx4 v[56:59], v1, s[4:7], s96 offen offset:3072 sc1\n"
"s_add_u32 s96, s58, 0x440000\n"
"buffer_load_dwordx4 v[36:39], v0, s[4:7], s96 offen sc1\n"
"buffer_load_dwordx4 v[40:43], v0, s[4:7], s96 offen offset:1024 sc1\n"
"buffer_load_dwordx4 v[52:55], v0, s[4:7], s96 offen offset:2048 sc1\n"
"buffer_load_dwordx4 v[60:63], v0, s[4:7], s96 offen offset:3072 sc1\n"
"buffer_load_dwordx4 v[64:67], v1, s[4:7], s96 offen sc1\n"
"buffer_load_dwordx4 v[68:71], v1, s[4:7], s96 offen offset:1024 sc1\n"
"buffer_load_dwordx4 v[72:75], v1, s[4:7], s96 offen offset:2048 sc1\n"
"buffer_load_dwordx4 v[76:79], v1, s[4:7], s96 offen offset:3072 sc1\n"
"s_add_u32 s96, s59, 0x440000\n"
"buffer_load_dwordx4 v[84:87], v0, s[4:7], s96 offen sc1\n"
"buffer_load_dwordx4 v[88:91], v0, s[4:7], s96 offen offset:1024 sc1\n"
"buffer_load_dwordx4 v[100:103], v0, s[4:7], s96 offen offset:2048 sc1\n"
"buffer_load_dwordx4 v[104:107], v0, s[4:7], s96 offen offset:3072 sc1\n"
"buffer_load_dwordx4 v[80:83], v1, s[4:7], s96 offen sc1\n"
"buffer_load_dwordx4 v[116:119], v1, s[4:7], s96 offen offset:1024 sc1\n"
"buffer_load_dwordx4 v[96:99], v1, s[4:7], s96 offen offset:2048 sc1\n"
"buffer_load_dwordx4 v[92:95], v1, s[4:7], s96 offen offset:3072 sc1\n"
"s_add_u32 s96, s56, 0x400000\n"
"buffer_load_dwordx4 v[120:123], v0, s[4:7], s96 offen sc1\n"
"buffer_load_dwordx4 v[124:127], v0, s[4:7], s96 offen offset:1024 sc1\n"
"s_waitcnt vmcnt(0)\n"
"v_max_u32_e32 v255, v148, v149\n"
"v_max3_u32 v255, v255, v150, v151\n"
"v_max3_u32 v255, v255, v180, v181\n"
"v_max3_u32 v255, v255, v182, v183\n"
"v_max3_u32 v255, v255, v24, v25\n"
"v_max3_u32 v255, v255, v26, v27\n"
"v_max3_u32 v255, v255, v32, v33\n"
"v_max3_u32 v255, v255, v34, v35\n"
"v_max3_u32 v255, v255, v44, v45\n"
"v_max3_u32 v255, v255, v46, v47\n"
"v_max3_u32 v255, v255, v48, v49\n"
"v_max3_u32 v255, v255, v50, v51\n"
"v_max3_u32 v255, v255, v28, v29\n"
"v_max3_u32 v255, v255, v30, v31\n"
"v_max3_u32 v255, v255, v56, v57\n"
"v_max3_u32 v255, v255, v58, v59\n"
"v_cmp_eq_u32_e32 vcc, -1, v255\n"
"s_cbranch_vccz .Lmk_p64k\n"
"s_sub_u32 s1, s1, 1\n"
"s_cbranch_scc0 .Lmk_p64l\n"
".Lmk_p64k:\n"
"v_mfma_f32_16x16x32_bf16 v[108:111], a[128:131], v[148:151], 0\n"
"v_mfma_f32_16x16x32_bf16 v[108:111], a[132:135], v[180:183], v[108:111]\n"
"v_mfma_f32_16x16x32_bf16 v[108:111], a[136:139], v[24:27], v[108:111]\n"
"v_mfma_f32_16x16x32_bf16 v[108:111], a[140:143], v[32:35], v[108:111]\n"
"v_mfma_f32_16x16x32_bf16 v[108:111], a[144:147], v[44:47], v[108:111]\n"
"v_mfma_f32_16x16x32_bf16 v[108:111], a[148:151], v[48:51], v[108:111]\n"
"v_mfma_f32_16x16x32_bf16 v[108:111], a[152:155], v[28:31], v[108:111]\n"
"v_mfma_f32_16x16x32_bf16 v[108:111], a[156:159], v[56:59], v[108:111]\n"
"v_mfma_f32_16x16x32_bf16 v[112:115], a[188:191], v[56:59], 0\n"
"v_mfma_f32_16x16x32_bf16 v[112:115], a[184:187], v[28:31], v[112:115]\n"
"v_mfma_f32_16x16x32_bf16 v[112:115], a[180:183], v[48:51], v[112:115]\n"
"v_mfma_f32_16x16x32_bf16 v[112:115], a[176:179], v[44:47], v[112:115]\n"
"v_mfma_f32_16x16x32_bf16 v[112:115], a[172:175], v[32:35], v[112:115]\n"
"v_mfma_f32_16x16x32_bf16 v[112:115], a[168:171], v[24:27], v[112:115]\n"
"s_nop 7\n"
"s_branch .Lmk_p64r\n"
".Lmk_p65s:\n"
";\n"
"s_waitcnt vmcnt(16)\n"
"s_mov_b32 s1, 0x10000\n"
".Lmk_p65l:\n"
"s_add_u32 s96, s58, 0x440000\n"
"buffer_load_dwordx4 v[36:39], v0, s[4:7], s96 offen sc1\n"
"buffer_load_dwordx4 v[40:43], v0, s[4:7], s96 offen offset:1024 sc1\n"
"buffer_load_dwordx4 v[52:55], v0, s[4:7], s96 offen offset:2048 sc1\n"
"buffer_load_dwordx4 v[60:63], v0, s[4:7], s96 offen offset:3072 sc1\n"
"buffer_load_dwordx4 v[64:67], v1, s[4:7], s96 offen sc1\n"
"buffer_load_dwordx4 v[68:71], v1, s[4:7], s96 offen offset:1024 sc1\n"
"buffer_load_dwordx4 v[72:75], v1, s[4:7], s96 offen offset:2048 sc1\n"
"buffer_load_dwordx4 v[76:79], v1, s[4:7], s96 offen offset:3072 sc1\n"
"s_add_u32 s96, s59, 0x440000\n"
"buffer_load_dwordx4 v[84:87], v0, s[4:7], s96 offen sc1\n"
"buffer_load_dwordx4 v[88:91], v0, s[4:7], s96 offen offset:1024 sc1\n"
"buffer_load_dwordx4 v[100:103], v0, s[4:7], s96 offen offset:2048 sc1\n"
"buffer_load_dwordx4 v[104:107], v0, s[4:7], s96 offen offset:3072 sc1\n"
"buffer_load_dwordx4 v[80:83], v1, s[4:7], s96 offen sc1\n"
"buffer_load_dwordx4 v[116:119], v1, s[4:7], s96 offen offset:1024 sc1\n"
"buffer_load_dwordx4 v[96:99], v1, s[4:7], s96 offen offset:2048 sc1\n"
"buffer_load_dwordx4 v[92:95], v1, s[4:7], s96 offen offset:3072 sc1\n"
"s_add_u32 s96, s56, 0x400000\n"
"buffer_load_dwordx4 v[120:123], v0, s[4:7], s96 offen sc1\n"
"buffer_load_dwordx4 v[124:127], v0, s[4:7], s96 offen offset:1024 sc1\n"
"buffer_load_dwordx4 v[108:111], v0, s[4:7], s96 offen offset:2048 sc1\n"
"buffer_load_dwordx4 v[132:135], v0, s[4:7], s96 offen offset:3072 sc1\n"
"buffer_load_dwordx4 v[112:115], v1, s[4:7], s96 offen sc1\n"
"buffer_load_dwordx4 v[28:31], v1, s[4:7], s96 offen offset:1024 sc1\n"
"buffer_load_dwordx4 v[32:35], v1, s[4:7], s96 offen offset:2048 sc1\n"
"buffer_load_dwordx4 v[24:27], v1, s[4:7], s96 offen offset:3072 sc1\n"
"s_add_u32 s96, s57, 0x400000\n"
"buffer_load_dwordx4 v[48:51], v0, s[4:7], s96 offen sc1\n"
"buffer_load_dwordx4 v[56:59], v0, s[4:7], s96 offen offset:1024 sc1\n"
"buffer_load_dwordx4 v[136:139], v0, s[4:7], s96 offen offset:2048 sc1\n"
"s_waitcnt vmcnt(0)\n"
"v_max_u32_e32 v239, v36, v37\n"
"v_max3_u32 v239, v239, v38, v39\n"
"v_max3_u32 v239, v239, v40, v41\n"
"v_max3_u32 v239, v239, v42, v43\n"
"v_max3_u32 v239, v239, v52, v53\n"
"v_max3_u32 v239, v239, v54, v55\n"
"v_max3_u32 v239, v239, v60, v61\n"
"v_max3_u32 v239, v239, v62, v63\n"
"v_max3_u32 v239, v239, v64, v65\n"
"v_max3_u32 v239, v239, v66, v67\n"
"v_max3_u32 v239, v239, v68, v69\n"
"v_max3_u32 v239, v239, v70, v71\n"
"v_max3_u32 v239, v239, v72, v73\n"
"v_max3_u32 v239, v239, v74, v75\n"
"v_max3_u32 v239, v239, v76, v77\n"
"v_max3_u32 v239, v239, v78, v79\n"
"v_cmp_eq_u32_e32 vcc, -1, v239\n"
"s_cbranch_vccz .Lmk_p65k\n"
"s_sub_u32 s1, s1, 1\n"
"s_cbranch_scc0 .Lmk_p65l\n"
".Lmk_p65k:\n"
"v_mfma_f32_16x16x32_bf16 v[44:47], a[128:131], v[36:39], 0\n"
"v_mfma_f32_16x16x32_bf16 v[44:47], a[132:135], v[40:43], v[44:47]\n"
"v_mfma_f32_16x16x32_bf16 v[44:47], a[136:139], v[52:55], v[44:47]\n"
"v_mfma_f32_16x16x32_bf16 v[44:47], a[140:143], v[60:63], v[44:47]\n"
"v_mfma_f32_16x16x32_bf16 v[44:47], a[144:147], v[64:67], v[44:47]\n"
"v_mfma_f32_16x16x32_bf16 v[44:47], a[148:151], v[68:71], v[44:47]\n"
"v_mfma_f32_16x16x32_bf16 v[44:47], a[152:155], v[72:75], v[44:47]\n"
"v_mfma_f32_16x16x32_bf16 v[44:47], a[156:159], v[76:79], v[44:47]\n"
"v_mfma_f32_16x16x32_bf16 v[128:131], a[188:191], v[76:79], 0\n"
"v_mfma_f32_16x16x32_bf16 v[128:131], a[184:187], v[72:75], v[128:131]\n"
"v_mfma_f32_16x16x32_bf16 v[128:131], a[180:183], v[68:71], v[128:131]\n"
"v_mfma_f32_16x16x32_bf16 v[128:131], a[176:179], v[64:67], v[128:131]\n"
"v_mfma_f32_16x16x32_bf16 v[128:131], a[172:175], v[60:63], v[128:131]\n"
"v_mfma_f32_16x16x32_bf16 v[128:131], a[168:171], v[52:55], v[128:131]\n"
"s_nop 7\n"
"s_branch .Lmk_p65r\n"
".Lmk_p66s:\n"
";\n"
"s_waitcnt vmcnt(16)\n"
"s_mov_b32 s1, 0x10000\n"
".Lmk_p66l:\n"
"s_add_u32 s96, s59, 0x440000\n"
"buffer_load_dwordx4 v[84:87], v0, s[4:7], s96 offen sc1\n"
"buffer_load_dwordx4 v[88:91], v0, s[4:7], s96 offen offset:1024 sc1\n"
"buffer_load_dwordx4 v[100:103], v0, s[4:7], s96 offen offset:2048 sc1\n"
"buffer_load_dwordx4 v[104:107], v0, s[4:7], s96 offen offset:3072 sc1\n"
"buffer_load_dwordx4 v[80:83], v1, s[4:7], s96 offen sc1\n"
"buffer_load_dwordx4 v[116:119], v1, s[4:7], s96 offen offset:1024 sc1\n"
"buffer_load_dwordx4 v[96:99], v1, s[4:7], s96 offen offset:2048 sc1\n"
"buffer_load_dwordx4 v[92:95], v1, s[4:7], s96 offen offset:3072 sc1\n"
"s_add_u32 s96, s56, 0x400000\n"
"buffer_load_dwordx4 v[120:123], v0, s[4:7], s96 offen sc1\n"
"buffer_load_dwordx4 v[124:127], v0, s[4:7], s96 offen offset:1024 sc1\n"
"buffer_load_dwordx4 v[108:111], v0, s[4:7], s96 offen offset:2048 sc1\n"
"buffer_load_dwordx4 v[132:135], v0, s[4:7], s96 offen offset:3072 sc1\n"
"buffer_load_dwordx4 v[112:115], v1, s[4:7], s96 offen sc1\n"
"buffer_load_dwordx4 v[28:31], v1, s[4:7], s96 offen offset:1024 sc1\n"
"buffer_load_dwordx4 v[32:35], v1, s[4:7], s96 offen offset:2048 sc1\n"
"buffer_load_dwordx4 v[24:27], v1, s[4:7], s96 offen offset:3072 sc1\n"
"s_add_u32 s96, s57, 0x400000\n"
"buffer_load_dwordx4 v[48:51], v0, s[4:7], s96 offen sc1\n"
"buffer_load_dwordx4 v[56:59], v0, s[4:7], s96 offen offset:1024 sc1\n"
"buffer_load_dwordx4 v[136:139], v0, s[4:7], s96 offen offset:2048 sc1\n"
"buffer_load_dwordx4 v[44:47], v0, s[4:7], s96 offen offset:3072 sc1\n"
"buffer_load_dwordx4 v[160:163], v1, s[4:7], s96 offen sc1\n"
"buffer_load_dwordx4 v[128:131], v1, s[4:7], s96 offen offset:1024 sc1\n"
"buffer_load_dwordx4 v[72:75], v1, s[4:7], s96 offen offset:2048 sc1\n"
"buffer_load_dwordx4 v[60:63], v1, s[4:7], s96 offen offset:3072 sc1\n"
"s_add_u32 s96, s58, 0x400000\n"
"buffer_load_dwordx4 v[36:39], v0, s[4:7], s96 offen sc1\n"
"buffer_load_dwordx4 v[68:71], v0, s[4:7], s96 offen offset:1024 sc1\n"
"buffer_load_dwordx4 v[76:79], v0, s[4:7], s96 offen offset:2048 sc1\n"
"buffer_load_dwordx4 v[148:151], v0, s[4:7], s96 offen offset:3072 sc1\n"
"s_waitcnt vmcnt(0)\n"
"v_max_u32_e32 v255, v84, v85\n"
"v_max3_u32 v255, v255, v86, v87\n"
"v_max3_u32 v255, v255, v88, v89\n"
"v_max3_u32 v255, v255, v90, v91\n"
"v_max3_u32 v255, v255, v100, v101\n"
"v_max3_u32 v255, v255, v102, v103\n"
"v_max3_u32 v255, v255, v104, v105\n"
"v_max3_u32 v255, v255, v106, v107\n"
"v_max3_u32 v255, v255, v80, v81\n"
"v_max3_u32 v255, v255, v82, v83\n"
"v_max3_u32 v255, v255, v116, v117\n"
"v_max3_u32 v255, v255, v118, v119\n"
"v_max3_u32 v255, v255, v96, v97\n"
"v_max3_u32 v255, v255, v98, v99\n"
"v_max3_u32 v255, v255, v92, v93\n"
"v_max3_u32 v255, v255, v94, v95\n"
"v_cmp_eq_u32_e32 vcc, -1, v255\n"
"s_cbranch_vccz .Lmk_p66k\n"
"s_sub_u32 s1, s1, 1\n"
"s_cbranch_scc0 .Lmk_p66l\n"
".Lmk_p66k:\n"
"v_mfma_f32_16x16x32_bf16 v[64:67], a[128:131], v[84:87], v[140:143]\n"
"v_mfma_f32_16x16x32_bf16 v[64:67], a[132:135], v[88:91], v[64:67]\n"
"v_mfma_f32_16x16x32_bf16 v[64:67], a[136:139], v[100:103], v[64:67]\n"
"v_mfma_f32_16x16x32_bf16 v[64:67], a[140:143], v[104:107], v[64:67]\n"
"v_mfma_f32_16x16x32_bf16 v[64:67], a[144:147], v[80:83], v[64:67]\n"
"v_mfma_f32_16x16x32_bf16 v[64:67], a[148:151], v[116:119], v[64:67]\n"
"v_mfma_f32_16x16x32_bf16 v[64:67], a[152:155], v[96:99], v[64:67]\n"
"v_mfma_f32_16x16x32_bf16 v[64:67], a[156:159], v[92:95], v[64:67]\n"
"v_mfma_f32_16x16x32_bf16 v[52:55], a[188:191], v[92:95], v[152:155]\n"
"v_mfma_f32_16x16x32_bf16 v[52:55], a[184:187], v[96:99], v[52:55]\n"
"v_mfma_f32_16x16x32_bf16 v[52:55], a[180:183], v[116:119], v[52:55]\n"
"v_mfma_f32_16x16x32_bf16 v[52:55], a[176:179], v[80:83], v[52:55]\n"
"v_mfma_f32_16x16x32_bf16 v[52:55], a[172:175], v[104:107], v[52:55]\n"
"v_mfma_f32_16x16x32_bf16 v[52:55], a[168:171], v[100:103], v[52:55]\n"
"s_nop 7\n"
"s_branch .Lmk_p66r\n"
".Lmk_p68s:\n"
";\n"
"s_waitcnt vmcnt(16)\n"
"s_mov_b32 s1, 0x10000\n"
".Lmk_p68l:\n"
"s_add_u32 s95, s56, 0x400000\n"
"buffer_load_dwordx4 v[120:123], v0, s[4:7], s95 offen sc1\n"
"buffer_load_dwordx4 v[124:127], v0, s[4:7], s95 offen offset:1024 sc1\n"
"buffer_load_dwordx4 v[108:111], v0, s[4:7], s95 offen offset:2048 sc1\n"
"buffer_load_dwordx4 v[132:135], v0, s[4:7], s95 offen offset:3072 sc1\n"
"buffer_load_dwordx4 v[112:115], v1, s[4:7], s95 offen sc1\n"
"buffer_load_dwordx4 v[28:31], v1, s[4:7], s95 offen offset:1024 sc1\n"
"buffer_load_dwordx4 v[32:35], v1, s[4:7], s95 offen offset:2048 sc1\n"
"buffer_load_dwordx4 v[24:27], v1, s[4:7], s95 offen offset:3072 sc1\n"
"s_add_u32 s95, s57, 0x400000\n"
"buffer_load_dwordx4 v[48:51], v0, s[4:7], s95 offen sc1\n"
"buffer_load_dwordx4 v[56:59], v0, s[4:7], s95 offen offset:1024 sc1\n"
"buffer_load_dwordx4 v[136:139], v0, s[4:7], s95 offen offset:2048 sc1\n"
"buffer_load_dwordx4 v[44:47], v0, s[4:7], s95 offen offset:3072 sc1\n"
"buffer_load_dwordx4 v[160:163], v1, s[4:7], s95 offen sc1\n"
"buffer_load_dwordx4 v[128:131], v1, s[4:7], s95 offen offset:1024 sc1\n"
"buffer_load_dwordx4 v[72:75], v1, s[4:7], s95 offen offset:2048 sc1\n"
"buffer_load_dwordx4 v[60:63], v1, s[4:7], s95 offen offset:3072 sc1\n"
"s_add_u32 s95, s58, 0x400000\n"
"buffer_load_dwordx4 v[36:39], v0, s[4:7], s95 offen sc1\n"
"buffer_load_dwordx4 v[68:71], v0, s[4:7], s95 offen offset:1024 sc1\n"
"buffer_load_dwordx4 v[76:79], v0, s[4:7], s95 offen offset:2048 sc1\n"
"buffer_load_dwordx4 v[148:151], v0, s[4:7], s95 offen offset:3072 sc1\n"
"buffer_load_dwordx4 v[152:155], v1, s[4:7], s95 offen sc1\n"
"buffer_load_dwordx4 v[172:175], v1, s[4:7], s95 offen offset:1024 sc1\n"
"buffer_load_dwordx4 v[176:179], v1, s[4:7], s95 offen offset:2048 sc1\n"
"buffer_load_dwordx4 v[64:67], v1, s[4:7], s95 offen offset:3072 sc1\n"
"s_add_u32 s95, s59, 0x400000\n"
"buffer_load_dwordx4 v[80:83], v0, s[4:7], s95 offen sc1\n"
"buffer_load_dwordx4 v[84:87], v0, s[4:7], s95 offen offset:1024 sc1\n"
"buffer_load_dwordx4 v[88:91], v0, s[4:7], s95 offen offset:2048 sc1\n"
"buffer_load_dwordx4 v[92:95], v0, s[4:7], s95 offen offset:3072 sc1\n"
"buffer_load_dwordx4 v[96:99], v1, s[4:7], s95 offen sc1\n"
"buffer_load_dwordx4 v[100:103], v1, s[4:7], s95 offen offset:1024 sc1\n"
"buffer_load_dwordx4 v[104:107], v1, s[4:7], s95 offen offset:2048 sc1\n"
"buffer_load_dwordx4 v[116:119], v1, s[4:7], s95 offen offset:3072 sc1\n"
"s_waitcnt vmcnt(0)\n"
"v_max_u32_e32 v255, v120, v121\n"
"v_max3_u32 v255, v255, v122, v123\n"
"v_max3_u32 v255, v255, v124, v125\n"
"v_max3_u32 v255, v255, v126, v127\n"
"v_max3_u32 v255, v255, v108, v109\n"
"v_max3_u32 v255, v255, v110, v111\n"
"v_max3_u32 v255, v255, v132, v133\n"
"v_max3_u32 v255, v255, v134, v135\n"
"v_max3_u32 v255, v255, v112, v113\n"
"v_max3_u32 v255, v255, v114, v115\n"
"v_max3_u32 v255, v255, v28, v29\n"
"v_max3_u32 v255, v255, v30, v31\n"
"v_max3_u32 v255, v255, v32, v33\n"
"v_max3_u32 v255, v255, v34, v35\n"
"v_max3_u32 v255, v255, v24, v25\n"
"v_max3_u32 v255, v255, v26, v27\n"
"v_cmp_eq_u32_e32 vcc, -1, v255\n"
"s_cbranch_vccz .Lmk_p68k\n"
"s_sub_u32 s1, s1, 1\n"
"s_cbranch_scc0 .Lmk_p68l\n"
".Lmk_p68k:\n"
"v_mfma_f32_16x16x32_bf16 v[140:143], a[128:131], v[120:123], 0\n"
"v_mfma_f32_16x16x32_bf16 v[140:143], a[132:135], v[124:127], v[140:143]\n"
"v_mfma_f32_16x16x32_bf16 v[140:143], a[136:139], v[108:111], v[140:143]\n"
"v_mfma_f32_16x16x32_bf16 v[140:143], a[140:143], v[132:135], v[140:143]\n"
"v_mfma_f32_16x16x32_bf16 v[140:143], a[144:147], v[112:115], v[140:143]\n"
"v_mfma_f32_16x16x32_bf16 v[140:143], a[148:151], v[28:31], v[140:143]\n"
"v_mfma_f32_16x16x32_bf16 v[140:143], a[152:155], v[32:35], v[140:143]\n"
"v_mfma_f32_16x16x32_bf16 v[140:143], a[156:159], v[24:27], v[140:143]\n"
"v_mfma_f32_16x16x32_bf16 v[40:43], a[188:191], v[24:27], 0\n"
"v_mfma_f32_16x16x32_bf16 v[40:43], a[184:187], v[32:35], v[40:43]\n"
"v_mfma_f32_16x16x32_bf16 v[40:43], a[180:183], v[28:31], v[40:43]\n"
"v_mfma_f32_16x16x32_bf16 v[40:43], a[176:179], v[112:115], v[40:43]\n"
"v_mfma_f32_16x16x32_bf16 v[40:43], a[172:175], v[132:135], v[40:43]\n"
"v_mfma_f32_16x16x32_bf16 v[40:43], a[168:171], v[108:111], v[40:43]\n"
"s_nop 7\n"
"s_branch .Lmk_p68r\n"
".Lmk_p69s:\n"
";\n"
"s_waitcnt vmcnt(16)\n"
"s_mov_b32 s1, 0x10000\n"
".Lmk_p69l:\n"
"s_add_u32 s97, s57, 0x400000\n"
"buffer_load_dwordx4 v[48:51], v0, s[4:7], s97 offen sc1\n"
"buffer_load_dwordx4 v[56:59], v0, s[4:7], s97 offen offset:1024 sc1\n"
"buffer_load_dwordx4 v[136:139], v0, s[4:7], s97 offen offset:2048 sc1\n"
"buffer_load_dwordx4 v[44:47], v0, s[4:7], s97 offen offset:3072 sc1\n"
"buffer_load_dwordx4 v[160:163], v1, s[4:7], s97 offen sc1\n"
"buffer_load_dwordx4 v[128:131], v1, s[4:7], s97 offen offset:1024 sc1\n"
"buffer_load_dwordx4 v[72:75], v1, s[4:7], s97 offen offset:2048 sc1\n"
"buffer_load_dwordx4 v[60:63], v1, s[4:7], s97 offen offset:3072 sc1\n"
"s_add_u32 s97, s58, 0x400000\n"
"buffer_load_dwordx4 v[36:39], v0, s[4:7], s97 offen sc1\n"
"buffer_load_dwordx4 v[68:71], v0, s[4:7], s97 offen offset:1024 sc1\n"
"buffer_load_dwordx4 v[76:79], v0, s[4:7], s97 offen offset:2048 sc1\n"
"buffer_load_dwordx4 v[148:151], v0, s[4:7], s97 offen offset:3072 sc1\n"
"buffer_load_dwordx4 v[152:155], v1, s[4:7], s97 offen sc1\n"
"buffer_load_dwordx4 v[172:175], v1, s[4:7], s97 offen offset:1024 sc1\n"
"buffer_load_dwordx4 v[176:179], v1, s[4:7], s97 offen offset:2048 sc1\n"
"buffer_load_dwordx4 v[64:67], v1, s[4:7], s97 offen offset:3072 sc1\n"
"s_add_u32 s97, s59, 0x400000\n"
"buffer_load_dwordx4 v[80:83], v0, s[4:7], s97 offen sc1\n"
"buffer_load_dwordx4 v[84:87], v0, s[4:7], s97 offen offset:1024 sc1\n"
"buffer_load_dwordx4 v[88:91], v0, s[4:7], s97 offen offset:2048 sc1\n"
"buffer_load_dwordx4 v[92:95], v0, s[4:7], s97 offen offset:3072 sc1\n"
"buffer_load_dwordx4 v[96:99], v1, s[4:7], s97 offen sc1\n"
"buffer_load_dwordx4 v[100:103], v1, s[4:7], s97 offen offset:1024 sc1\n"
"buffer_load_dwordx4 v[104:107], v1, s[4:7], s97 offen offset:2048 sc1\n"
"buffer_load_dwordx4 v[116:119], v1, s[4:7], s97 offen offset:3072 sc1\n"
"s_waitcnt vmcnt(0)\n"
"v_max_u32_e32 v255, v48, v49\n"
"v_max3_u32 v255, v255, v50, v51\n"
"v_max3_u32 v255, v255, v56, v57\n"
"v_max3_u32 v255, v255, v58, v59\n"
"v_max3_u32 v255, v255, v136, v137\n"
"v_max3_u32 v255, v255, v138, v139\n"
"v_max3_u32 v255, v255, v44, v45\n"
"v_max3_u32 v255, v255, v46, v47\n"
"v_max3_u32 v255, v255, v160, v161\n"
"v_max3_u32 v255, v255, v162, v163\n"
"v_max3_u32 v255, v255, v128, v129\n"
"v_max3_u32 v255, v255, v130, v131\n"
"v_max3_u32 v255, v255, v72, v73\n"
"v_max3_u32 v255, v255, v74, v75\n"
"v_max3_u32 v255, v255, v60, v61\n"
"v_max3_u32 v255, v255, v62, v63\n"
"v_cmp_eq_u32_e32 vcc, -1, v255\n"
"s_cbranch_vccz .Lmk_p69k\n"
"s_sub_u32 s1, s1, 1\n"
"s_cbranch_scc0 .Lmk_p69l\n"
".Lmk_p69k:\n"
"v_mfma_f32_16x16x32_bf16 v[24:27], a[128:131], v[48:51], 0\n"
"v_mfma_f32_16x16x32_bf16 v[24:27], a[132:135], v[56:59], v[24:27]\n"
"v_mfma_f32_16x16x32_bf16 v[24:27], a[136:139], v[136:139], v[24:27]\n"
"v_mfma_f32_16x16x32_bf16 v[24:27], a[140:143], v[44:47], v[24:27]\n"
"v_mfma_f32_16x16x32_bf16 v[24:27], a[144:147], v[160:163], v[24:27]\n"
"v_mfma_f32_16x16x32_bf16 v[24:27], a[148:151], v[128:131], v[24:27]\n"
"v_mfma_f32_16x16x32_bf16 v[24:27], a[152:155], v[72:75], v[24:27]\n"
"v_mfma_f32_16x16x32_bf16 v[24:27], a[156:159], v[60:63], v[24:27]\n"
"v_mfma_f32_16x16x32_bf16 v[28:31], a[188:191], v[60:63], 0\n"
"v_mfma_f32_16x16x32_bf16 v[28:31], a[184:187], v[72:75], v[28:31]\n"
"v_mfma_f32_16x16x32_bf16 v[28:31], a[180:183], v[128:131], v[28:31]\n"
"v_mfma_f32_16x16x32_bf16 v[28:31], a[176:179], v[160:163], v[28:31]\n"
"v_mfma_f32_16x16x32_bf16 v[28:31], a[172:175], v[44:47], v[28:31]\n"
"v_mfma_f32_16x16x32_bf16 v[28:31], a[168:171], v[136:139], v[28:31]\n"
"s_nop 7\n"
"s_branch .Lmk_p69r\n"
".Lmk_p70s:\n"
";\n"
"s_waitcnt vmcnt(16)\n"
"s_mov_b32 s1, 0x10000\n"
".Lmk_p70l:\n"
"s_add_u32 s97, s58, 0x400000\n"
"buffer_load_dwordx4 v[36:39], v0, s[4:7], s97 offen sc1\n"
"buffer_load_dwordx4 v[68:71], v0, s[4:7], s97 offen offset:1024 sc1\n"
"buffer_load_dwordx4 v[76:79], v0, s[4:7], s97 offen offset:2048 sc1\n"
"buffer_load_dwordx4 v[148:151], v0, s[4:7], s97 offen offset:3072 sc1\n"
"buffer_load_dwordx4 v[152:155], v1, s[4:7], s97 offen sc1\n"
"buffer_load_dwordx4 v[172:175], v1, s[4:7], s97 offen offset:1024 sc1\n"
"buffer_load_dwordx4 v[176:179], v1, s[4:7], s97 offen offset:2048 sc1\n"
"buffer_load_dwordx4 v[64:67], v1, s[4:7], s97 offen offset:3072 sc1\n"
"s_add_u32 s97, s59, 0x400000\n"
"buffer_load_dwordx4 v[80:83], v0, s[4:7], s97 offen sc1\n"
"buffer_load_dwordx4 v[84:87], v0, s[4:7], s97 offen offset:1024 sc1\n"
"buffer_load_dwordx4 v[88:91], v0, s[4:7], s97 offen offset:2048 sc1\n"
"buffer_load_dwordx4 v[92:95], v0, s[4:7], s97 offen offset:3072 sc1\n"
"buffer_load_dwordx4 v[96:99], v1, s[4:7], s97 offen sc1\n"
"buffer_load_dwordx4 v[100:103], v1, s[4:7], s97 offen offset:1024 sc1\n"
"buffer_load_dwordx4 v[104:107], v1, s[4:7], s97 offen offset:2048 sc1\n"
"buffer_load_dwordx4 v[116:119], v1, s[4:7], s97 offen offset:3072 sc1\n"
"s_waitcnt vmcnt(0)\n"
"v_max_u32_e32 v239, v36, v37\n"
"v_max3_u32 v239, v239, v38, v39\n"
"v_max3_u32 v239, v239, v68, v69\n"
"v_max3_u32 v239, v239, v70, v71\n"
"v_max3_u32 v239, v239, v76, v77\n"
"v_max3_u32 v239, v239, v78, v79\n"
"v_max3_u32 v239, v239, v148, v149\n"
"v_max3_u32 v239, v239, v150, v151\n"
"v_max3_u32 v239, v239, v152, v153\n"
"v_max3_u32 v239, v239, v154, v155\n"
"v_max3_u32 v239, v239, v172, v173\n"
"v_max3_u32 v239, v239, v174, v175\n"
"v_max3_u32 v239, v239, v176, v177\n"
"v_max3_u32 v239, v239, v178, v179\n"
"v_max3_u32 v239, v239, v64, v65\n"
"v_max3_u32 v239, v239, v66, v67\n"
"v_cmp_eq_u32_e32 vcc, -1, v239\n"
"s_cbranch_vccz .Lmk_p70k\n"
"s_sub_u32 s1, s1, 1\n"
"s_cbranch_scc0 .Lmk_p70l\n"
".Lmk_p70k:\n"
"v_mfma_f32_16x16x32_bf16 v[24:27], a[128:131], v[36:39], 0\n"
"v_mfma_f32_16x16x32_bf16 v[24:27], a[132:135], v[68:71], v[24:27]\n"
"v_mfma_f32_16x16x32_bf16 v[24:27], a[136:139], v[76:79], v[24:27]\n"
"v_mfma_f32_16x16x32_bf16 v[24:27], a[140:143], v[148:151], v[24:27]\n"
"v_mfma_f32_16x16x32_bf16 v[24:27], a[144:147], v[152:155], v[24:27]\n"
"v_mfma_f32_16x16x32_bf16 v[24:27], a[148:151], v[172:175], v[24:27]\n"
"v_mfma_f32_16x16x32_bf16 v[24:27], a[152:155], v[176:179], v[24:27]\n"
"v_mfma_f32_16x16x32_bf16 v[24:27], a[156:159], v[64:67], v[24:27]\n"
"v_mfma_f32_16x16x32_bf16 v[28:31], a[188:191], v[64:67], 0\n"
"v_mfma_f32_16x16x32_bf16 v[28:31], a[184:187], v[176:179], v[28:31]\n"
"v_mfma_f32_16x16x32_bf16 v[28:31], a[180:183], v[172:175], v[28:31]\n"
"v_mfma_f32_16x16x32_bf16 v[28:31], a[176:179], v[152:155], v[28:31]\n"
"v_mfma_f32_16x16x32_bf16 v[28:31], a[172:175], v[148:151], v[28:31]\n"
"v_mfma_f32_16x16x32_bf16 v[28:31], a[168:171], v[76:79], v[28:31]\n"
"s_nop 7\n"
"s_branch .Lmk_p70r\n"
".Lmk_p71s:\n"
";\n"
"s_waitcnt vmcnt(16)\n"
"s_mov_b32 s1, 0x10000\n"
".Lmk_p71l:\n"
"s_add_u32 s97, s59, 0x400000\n"
"buffer_load_dwordx4 v[80:83], v0, s[4:7], s97 offen sc1\n"
"buffer_load_dwordx4 v[84:87], v0, s[4:7], s97 offen offset:1024 sc1\n"
"buffer_load_dwordx4 v[88:91], v0, s[4:7], s97 offen offset:2048 sc1\n"
"buffer_load_dwordx4 v[92:95], v0, s[4:7], s97 offen offset:3072 sc1\n"
"buffer_load_dwordx4 v[96:99], v1, s[4:7], s97 offen sc1\n"
"buffer_load_dwordx4 v[100:103], v1, s[4:7], s97 offen offset:1024 sc1\n"
"buffer_load_dwordx4 v[104:107], v1, s[4:7], s97 offen offset:2048 sc1\n"
"buffer_load_dwordx4 v[116:119], v1, s[4:7], s97 offen offset:3072 sc1\n"
"s_waitcnt vmcnt(0)\n"
"v_max_u32_e32 v255, v80, v81\n"
"v_max3_u32 v255, v255, v82, v83\n"
"v_max3_u32 v255, v255, v84, v85\n"
"v_max3_u32 v255, v255, v86, v87\n"
"v_max3_u32 v255, v255, v88, v89\n"
"v_max3_u32 v255, v255, v90, v91\n"
"v_max3_u32 v255, v255, v92, v93\n"
"v_max3_u32 v255, v255, v94, v95\n"
"v_max3_u32 v255, v255, v96, v97\n"
"v_max3_u32 v255, v255, v98, v99\n"
"v_max3_u32 v255, v255, v100, v101\n"
"v_max3_u32 v255, v255, v102, v103\n"
"v_max3_u32 v255, v255, v104, v105\n"
"v_max3_u32 v255, v255, v106, v107\n"
"v_max3_u32 v255, v255, v116, v117\n"
"v_max3_u32 v255, v255, v118, v119\n"
"v_cmp_eq_u32_e32 vcc, -1, v255\n"
"s_cbranch_vccz .Lmk_p71k\n"
"s_sub_u32 s1, s1, 1\n"
"s_cbranch_scc0 .Lmk_p71l\n"
".Lmk_p71k:\n"
"v_mfma_f32_16x16x32_bf16 v[76:79], a[128:131], v[80:83], v[32:35]\n"
"v_mfma_f32_16x16x32_bf16 v[76:79], a[132:135], v[84:87], v[76:79]\n"
"v_mfma_f32_16x16x32_bf16 v[76:79], a[136:139], v[88:91], v[76:79]\n"
"v_mfma_f32_16x16x32_bf16 v[76:79], a[140:143], v[92:95], v[76:79]\n"
"v_mfma_f32_16x16x32_bf16 v[76:79], a[144:147], v[96:99], v[76:79]\n"
"v_mfma_f32_16x16x32_bf16 v[76:79], a[148:151], v[100:103], v[76:79]\n"
"v_mfma_f32_16x16x32_bf16 v[76:79], a[152:155], v[104:107], v[76:79]\n"
"v_mfma_f32_16x16x32_bf16 v[76:79], a[156:159], v[116:119], v[76:79]\n"
"v_mfma_f32_16x16x32_bf16 v[68:71], a[188:191], v[116:119], v[48:51]\n"
"v_mfma_f32_16x16x32_bf16 v[68:71], a[184:187], v[104:107], v[68:71]\n"
"v_mfma_f32_16x16x32_bf16 v[68:71], a[180:183], v[100:103], v[68:71]\n"
"v_mfma_f32_16x16x32_bf16 v[68:71], a[176:179], v[96:99], v[68:71]\n"
"v_mfma_f32_16x16x32_bf16 v[68:71], a[172:175], v[92:95], v[68:71]\n"
"v_mfma_f32_16x16x32_bf16 v[68:71], a[168:171], v[88:91], v[68:71]\n"
"s_nop 7\n"
"s_branch .Lmk_p71r\n"
".Lmk_p76s:\n"
";\n"
"s_waitcnt vmcnt(16)\n"
"s_mov_b32 s1, 0x10000\n"
".Lmk_p76l:\n"
"s_add_u32 s96, s52, 0x40000\n"
"buffer_load_dwordx4 v[84:87], v0, s[4:7], s96 offen sc1\n"
"buffer_load_dwordx4 v[92:95], v0, s[4:7], s96 offen offset:1024 sc1\n"
"buffer_load_dwordx4 v[96:99], v0, s[4:7], s96 offen offset:2048 sc1\n"
"buffer_load_dwordx4 v[104:107], v0, s[4:7], s96 offen offset:3072 sc1\n"
"buffer_load_dwordx4 v[112:115], v1, s[4:7], s96 offen sc1\n"
"buffer_load_dwordx4 v[116:119], v1, s[4:7], s96 offen offset:1024 sc1\n"
"buffer_load_dwordx4 v[120:123], v1, s[4:7], s96 offen offset:2048 sc1\n"
"buffer_load_dwordx4 v[136:139], v1, s[4:7], s96 offen offset:3072 sc1\n"
"s_add_u32 s96, s53, 0x40000\n"
"buffer_load_dwordx4 v[140:143], v0, s[4:7], s96 offen sc1\n"
"buffer_load_dwordx4 v[144:147], v0, s[4:7], s96 offen offset:1024 sc1\n"
"buffer_load_dwordx4 v[148:151], v0, s[4:7], s96 offen offset:2048 sc1\n"
"buffer_load_dwordx4 v[152:155], v0, s[4:7], s96 offen offset:3072 sc1\n"
"buffer_load_dwordx4 v[156:159], v1, s[4:7], s96 offen sc1\n"
"buffer_load_dwordx4 v[160:163], v1, s[4:7], s96 offen offset:1024 sc1\n"
"buffer_load_dwordx4 v[164:167], v1, s[4:7], s96 offen offset:2048 sc1\n"
"buffer_load_dwordx4 v[168:171], v1, s[4:7], s96 offen offset:3072 sc1\n"
"s_add_u32 s96, s54, 0x40000\n"
"buffer_load_dwordx4 v[176:179], v0, s[4:7], s96 offen sc1\n"
"buffer_load_dwordx4 v[40:43], v0, s[4:7], s96 offen offset:1024 sc1\n"
"s_waitcnt vmcnt(0)\n"
"v_max_u32_e32 v252, v84, v85\n"
"v_max3_u32 v252, v252, v86, v87\n"
"v_max3_u32 v252, v252, v92, v93\n"
"v_max3_u32 v252, v252, v94, v95\n"
"v_max3_u32 v252, v252, v96, v97\n"
"v_max3_u32 v252, v252, v98, v99\n"
"v_max3_u32 v252, v252, v104, v105\n"
"v_max3_u32 v252, v252, v106, v107\n"
"v_max3_u32 v252, v252, v112, v113\n"
"v_max3_u32 v252, v252, v114, v115\n"
"v_max3_u32 v252, v252, v116, v117\n"
"v_max3_u32 v252, v252, v118, v119\n"
"v_max3_u32 v252, v252, v120, v121\n"
"v_max3_u32 v252, v252, v122, v123\n"
"v_max3_u32 v252, v252, v136, v137\n"
"v_max3_u32 v252, v252, v138, v139\n"
"v_cmp_eq_u32_e32 vcc, -1, v252\n"
"s_cbranch_vccz .Lmk_p76k\n"
"s_sub_u32 s1, s1, 1\n"
"s_cbranch_scc0 .Lmk_p76l\n"
".Lmk_p76k:\n"
"v_mfma_f32_16x16x32_bf16 v[172:175], a[0:3], v[84:87], 0\n"
"v_mfma_f32_16x16x32_bf16 v[172:175], a[4:7], v[92:95], v[172:175]\n"
"v_mfma_f32_16x16x32_bf16 v[172:175], a[8:11], v[96:99], v[172:175]\n"
"v_mfma_f32_16x16x32_bf16 v[172:175], a[12:15], v[104:107], v[172:175]\n"
"v_mfma_f32_16x16x32_bf16 v[172:175], a[16:19], v[112:115], v[172:175]\n"
"v_mfma_f32_16x16x32_bf16 v[172:175], a[20:23], v[116:119], v[172:175]\n"
"v_mfma_f32_16x16x32_bf16 v[172:175], a[24:27], v[120:123], v[172:175]\n"
"v_mfma_f32_16x16x32_bf16 v[172:175], a[28:31], v[136:139], v[172:175]\n"
"v_mfma_f32_16x16x32_bf16 v[180:183], a[60:63], v[136:139], 0\n"
"v_mfma_f32_16x16x32_bf16 v[180:183], a[56:59], v[120:123], v[180:183]\n"
"v_mfma_f32_16x16x32_bf16 v[180:183], a[52:55], v[116:119], v[180:183]\n"
"v_mfma_f32_16x16x32_bf16 v[180:183], a[48:51], v[112:115], v[180:183]\n"
"v_mfma_f32_16x16x32_bf16 v[180:183], a[44:47], v[104:107], v[180:183]\n"
"v_mfma_f32_16x16x32_bf16 v[180:183], a[40:43], v[96:99], v[180:183]\n"
"s_nop 7\n"
"s_branch .Lmk_p76r\n"
".Lmk_p77s:\n"
";\n"
"s_waitcnt vmcnt(16)\n"
"s_mov_b32 s1, 0x10000\n"
".Lmk_p77l:\n"
"s_add_u32 s97, s53, 0x40000\n"
"buffer_load_dwordx4 v[140:143], v0, s[4:7], s97 offen sc1\n"
"buffer_load_dwordx4 v[144:147], v0, s[4:7], s97 offen offset:1024 sc1\n"
"buffer_load_dwordx4 v[148:151], v0, s[4:7], s97 offen offset:2048 sc1\n"
"buffer_load_dwordx4 v[152:155], v0, s[4:7], s97 offen offset:3072 sc1\n"
"buffer_load_dwordx4 v[156:159], v1, s[4:7], s97 offen sc1\n"
"buffer_load_dwordx4 v[160:163], v1, s[4:7], s97 offen offset:1024 sc1\n"
"buffer_load_dwordx4 v[164:167], v1, s[4:7], s97 offen offset:2048 sc1\n"
"buffer_load_dwordx4 v[168:171], v1, s[4:7], s97 offen offset:3072 sc1\n"
"s_add_u32 s97, s54, 0x40000\n"
"buffer_load_dwordx4 v[176:179], v0, s[4:7], s97 offen sc1\n"
"buffer_load_dwordx4 v[40:43], v0, s[4:7], s97 offen offset:1024 sc1\n"
"buffer_load_dwordx4 v[124:127], v0, s[4:7], s97 offen offset:2048 sc1\n"
"buffer_load_dwordx4 v[184:187], v0, s[4:7], s97 offen offset:3072 sc1\n"
"buffer_load_dwordx4 v[32:35], v1, s[4:7], s97 offen sc1\n"
"buffer_load_dwordx4 v[192:195], v1, s[4:7], s97 offen offset:1024 sc1\n"
"buffer_load_dwordx4 v[196:199], v1, s[4:7], s97 offen offset:2048 sc1\n"
"buffer_load_dwordx4 v[200:203], v1, s[4:7], s97 offen offset:3072 sc1\n"
"s_add_u32 s97, s55, 0x40000\n"
"buffer_load_dwordx4 v[84:87], v0, s[4:7], s97 offen sc1\n"
"buffer_load_dwordx4 v[92:95], v0, s[4:7], s97 offen offset:1024 sc1\n"
"s_waitcnt vmcnt(0)\n"
"v_max_u32_e32 v247, v140, v141\n"
"v_max3_u32 v247, v247, v142, v143\n"
"v_max3_u32 v247, v247, v144, v145\n"
"v_max3_u32 v247, v247, v146, v147\n"
"v_max3_u32 v247, v247, v148, v149\n"
"v_max3_u32 v247, v247, v150, v151\n"
"v_max3_u32 v247, v247, v152, v153\n"
"v_max3_u32 v247, v247, v154, v155\n"
"v_max3_u32 v247, v247, v156, v157\n"
"v_max3_u32 v247, v247, v158, v159\n"
"v_max3_u32 v247, v247, v160, v161\n"
"v_max3_u32 v247, v247, v162, v163\n"
"v_max3_u32 v247, v247, v164, v165\n"
"v_max3_u32 v247, v247, v166, v167\n"
"v_max3_u32 v247, v247, v168, v169\n"
"v_max3_u32 v247, v247, v170, v171\n"
"v_cmp_eq_u32_e32 vcc, -1, v247\n"
"s_cbranch_vccz .Lmk_p77k\n"
"s_sub_u32 s1, s1, 1\n"
"s_cbranch_scc0 .Lmk_p77l\n"
".Lmk_p77k:\n"
"v_mfma_f32_16x16x32_bf16 v[68:71], a[0:3], v[140:143], 0\n"
"v_mfma_f32_16x16x32_bf16 v[68:71], a[4:7], v[144:147], v[68:71]\n"
"v_mfma_f32_16x16x32_bf16 v[68:71], a[8:11], v[148:151], v[68:71]\n"
"v_mfma_f32_16x16x32_bf16 v[68:71], a[12:15], v[152:155], v[68:71]\n"
"v_mfma_f32_16x16x32_bf16 v[68:71], a[16:19], v[156:159], v[68:71]\n"
"v_mfma_f32_16x16x32_bf16 v[68:71], a[20:23], v[160:163], v[68:71]\n"
"v_mfma_f32_16x16x32_bf16 v[68:71], a[24:27], v[164:167], v[68:71]\n"
"v_mfma_f32_16x16x32_bf16 v[68:71], a[28:31], v[168:171], v[68:71]\n"
"v_mfma_f32_16x16x32_bf16 v[96:99], a[60:63], v[168:171], 0\n"
"v_mfma_f32_16x16x32_bf16 v[96:99], a[56:59], v[164:167], v[96:99]\n"
"v_mfma_f32_16x16x32_bf16 v[96:99], a[52:55], v[160:163], v[96:99]\n"
"v_mfma_f32_16x16x32_bf16 v[96:99], a[48:51], v[156:159], v[96:99]\n"
"v_mfma_f32_16x16x32_bf16 v[96:99], a[44:47], v[152:155], v[96:99]\n"
"v_mfma_f32_16x16x32_bf16 v[96:99], a[40:43], v[148:151], v[96:99]\n"
"s_nop 7\n"
"s_branch .Lmk_p77r\n"
".Lmk_m80s:\n"
";\n"
"s_waitcnt vmcnt(16)\n"
"s_mov_b32 s1, 0x10000\n"
".Lmk_m80l:\n"
"s_add_u32 s98, s62, 0x4000\n"
"buffer_load_dword v255, v10, s[4:7], s98 offen sc1\n"
"buffer_load_dword v254, v10, s[4:7], s98 offen offset:256 sc1\n"
"buffer_load_dword v253, v10, s[4:7], s98 offen offset:512 sc1\n"
"buffer_load_dword v252, v10, s[4:7], s98 offen offset:768 sc1\n"
"buffer_load_dword v251, v10, s[4:7], s98 offen offset:1024 sc1\n"
"buffer_load_dword v250, v10, s[4:7], s98 offen offset:1280 sc1\n"
"buffer_load_dword v249, v10, s[4:7], s98 offen offset:1536 sc1\n"
"buffer_load_dword v248, v10, s[4:7], s98 offen offset:1792 sc1\n"
"s_waitcnt vmcnt(0)\n"
"v_max_u32_e32 v247, v255, v254\n"
"v_max3_u32 v247, v247, v253, v252\n"
"v_max3_u32 v247, v247, v251, v250\n"
"v_max3_u32 v247, v247, v249, v248\n"
"v_cmp_eq_u32_e32 vcc, -1, v247\n"
"s_cbranch_vccz .Lmk_m80k\n"
"s_sub_u32 s1, s1, 1\n"
"s_cbranch_scc0 .Lmk_m80l\n"
".Lmk_m80k:\n"
"v_add_f32_e32 v255, v255, v251\n"
"v_add_f32_e32 v254, v254, v250\n"
"v_add_f32_e32 v253, v253, v249\n"
"v_add_f32_e32 v252, v252, v248\n"
"v_add_f32_e32 v255, v255, v253\n"
"v_add_f32_e32 v254, v254, v252\n"
"v_add_f32_e32 v255, v255, v254\n"
"s_nop 1\n"
"s_branch .Lmk_m80r\n"
".Lmk_p78s:\n"
";\n"
"s_waitcnt vmcnt(16)\n"
"s_mov_b32 s1, 0x10000\n"
".Lmk_p78l:\n"
"s_add_u32 s97, s54, 0x40000\n"
"buffer_load_dwordx4 v[176:179], v0, s[4:7], s97 offen sc1\n"
"buffer_load_dwordx4 v[40:43], v0, s[4:7], s97 offen offset:1024 sc1\n"
"buffer_load_dwordx4 v[124:127], v0, s[4:7], s97 offen offset:2048 sc1\n"
"buffer_load_dwordx4 v[184:187], v0, s[4:7], s97 offen offset:3072 sc1\n"
"buffer_load_dwordx4 v[32:35], v1, s[4:7], s97 offen sc1\n"
"buffer_load_dwordx4 v[192:195], v1, s[4:7], s97 offen offset:1024 sc1\n"
"buffer_load_dwordx4 v[196:199], v1, s[4:7], s97 offen offset:2048 sc1\n"
"buffer_load_dwordx4 v[200:203], v1, s[4:7], s97 offen offset:3072 sc1\n"
"s_add_u32 s97, s55, 0x40000\n"
"buffer_load_dwordx4 v[84:87], v0, s[4:7], s97 offen sc1\n"
"buffer_load_dwordx4 v[92:95], v0, s[4:7], s97 offen offset:1024 sc1\n"
"buffer_load_dwordx4 v[104:107], v0, s[4:7], s97 offen offset:2048 sc1\n"
"buffer_load_dwordx4 v[112:115], v0, s[4:7], s97 offen offset:3072 sc1\n"
"buffer_load_dwordx4 v[96:99], v1, s[4:7], s97 offen sc1\n"
"buffer_load_dwordx4 v[120:123], v1, s[4:7], s97 offen offset:1024 sc1\n"
"buffer_load_dwordx4 v[136:139], v1, s[4:7], s97 offen offset:2048 sc1\n"
"buffer_load_dwordx4 v[172:175], v1, s[4:7], s97 offen offset:3072 sc1\n"
"s_add_u32 s97, s52, 0x48000\n"
"buffer_load_dwordx4 v[140:143], v0, s[4:7], s97 offen sc1\n"
"s_waitcnt vmcnt(0)\n"
"v_max_u32_e32 v252, v176, v177\n"
"v_max3_u32 v252, v252, v178, v179\n"
"v_max3_u32 v252, v252, v40, v41\n"
"v_max3_u32 v252, v252, v42, v43\n"
"v_max3_u32 v252, v252, v124, v125\n"
"v_max3_u32 v252, v252, v126, v127\n"
"v_max3_u32 v252, v252, v184, v185\n"
"v_max3_u32 v252, v252, v186, v187\n"
"v_max3_u32 v252, v252, v32, v33\n"
"v_max3_u32 v252, v252, v34, v35\n"
"v_max3_u32 v252, v252, v192, v193\n"
"v_max3_u32 v252, v252, v194, v195\n"
"v_max3_u32 v252, v252, v196, v197\n"
"v_max3_u32 v252, v252, v198, v199\n"
"v_max3_u32 v252, v252, v200, v201\n"
"v_max3_u32 v252, v252, v202, v203\n"
"v_cmp_eq_u32_e32 vcc, -1, v252\n"
"s_cbranch_vccz .Lmk_p78k\n"
"s_sub_u32 s1, s1, 1\n"
"s_cbranch_scc0 .Lmk_p78l\n"
".Lmk_p78k:\n"
"v_mfma_f32_16x16x32_bf16 v[68:71], a[0:3], v[176:179], 0\n"
"v_mfma_f32_16x16x32_bf16 v[68:71], a[4:7], v[40:43], v[68:71]\n"
"v_mfma_f32_16x16x32_bf16 v[68:71], a[8:11], v[124:127], v[68:71]\n"
"v_mfma_f32_16x16x32_bf16 v[68:71], a[12:15], v[184:187], v[68:71]\n"
"v_mfma_f32_16x16x32_bf16 v[68:71], a[16:19], v[32:35], v[68:71]\n"
"v_mfma_f32_16x16x32_bf16 v[68:71], a[20:23], v[192:195], v[68:71]\n"
"v_mfma_f32_16x16x32_bf16 v[68:71], a[24:27], v[196:199], v[68:71]\n"
"v_mfma_f32_16x16x32_bf16 v[68:71], a[28:31], v[200:203], v[68:71]\n"
"v_mfma_f32_16x16x32_bf16 v[116:119], a[60:63], v[200:203], 0\n"
"v_mfma_f32_16x16x32_bf16 v[116:119], a[56:59], v[196:199], v[116:119]\n"
"v_mfma_f32_16x16x32_bf16 v[116:119], a[52:55], v[192:195], v[116:119]\n"
"v_mfma_f32_16x16x32_bf16 v[116:119], a[48:51], v[32:35], v[116:119]\n"
"v_mfma_f32_16x16x32_bf16 v[116:119], a[44:47], v[184:187], v[116:119]\n"
"v_mfma_f32_16x16x32_bf16 v[116:119], a[40:43], v[124:127], v[116:119]\n"
"s_nop 7\n"
"s_branch .Lmk_p78r\n"
".Lmk_p79s:\n"
";\n"
"s_waitcnt vmcnt(16)\n"
"s_mov_b32 s1, 0x10000\n"
".Lmk_p79l:\n"
"s_add_u32 s97, s55, 0x40000\n"
"buffer_load_dwordx4 v[84:87], v0, s[4:7], s97 offen sc1\n"
"buffer_load_dwordx4 v[92:95], v0, s[4:7], s97 offen offset:1024 sc1\n"
"buffer_load_dwordx4 v[104:107], v0, s[4:7], s97 offen offset:2048 sc1\n"
"buffer_load_dwordx4 v[112:115], v0, s[4:7], s97 offen offset:3072 sc1\n"
"buffer_load_dwordx4 v[96:99], v1, s[4:7], s97 offen sc1\n"
"buffer_load_dwordx4 v[120:123], v1, s[4:7], s97 offen offset:1024 sc1\n"
"buffer_load_dwordx4 v[136:139], v1, s[4:7], s97 offen offset:2048 sc1\n"
"buffer_load_dwordx4 v[172:175], v1, s[4:7], s97 offen offset:3072 sc1\n"
"s_add_u32 s97, s52, 0x48000\n"
"buffer_load_dwordx4 v[140:143], v0, s[4:7], s97 offen sc1\n"
"buffer_load_dwordx4 v[68:71], v0, s[4:7], s97 offen offset:1024 sc1\n"
"buffer_load_dwordx4 v[168:171], v0, s[4:7], s97 offen offset:2048 sc1\n"
"buffer_load_dwordx4 v[152:155], v0, s[4:7], s97 offen offset:3072 sc1\n"
"buffer_load_dwordx4 v[148:151], v1, s[4:7], s97 offen sc1\n"
"buffer_load_dwordx4 v[164:167], v1, s[4:7], s97 offen offset:1024 sc1\n"
"buffer_load_dwordx4 v[180:183], v1, s[4:7], s97 offen offset:2048 sc1\n"
"buffer_load_dwordx4 v[124:127], v1, s[4:7], s97 offen offset:3072 sc1\n"
"s_add_u32 s97, s53, 0x48000\n"
"buffer_load_dwordx4 v[200:203], v0, s[4:7], s97 offen sc1\n"
"buffer_load_dwordx4 v[116:119], v0, s[4:7], s97 offen offset:1024 sc1\n"
"s_waitcnt vmcnt(0)\n"
"v_max_u32_e32 v252, v84, v85\n"
"v_max3_u32 v252, v252, v86, v87\n"
"v_max3_u32 v252, v252, v92, v93\n"
"v_max3_u32 v252, v252, v94, v95\n"
"v_max3_u32 v252, v252, v104, v105\n"
"v_max3_u32 v252, v252, v106, v107\n"
"v_max3_u32 v252, v252, v112, v113\n"
"v_max3_u32 v252, v252, v114, v115\n"
"v_max3_u32 v252, v252, v96, v97\n"
"v_max3_u32 v252, v252, v98, v99\n"
"v_max3_u32 v252, v252, v120, v121\n"
"v_max3_u32 v252, v252, v122, v123\n"
"v_max3_u32 v252, v252, v136, v137\n"
"v_max3_u32 v252, v252, v138, v139\n"
"v_max3_u32 v252, v252, v172, v173\n"
"v_max3_u32 v252, v252, v174, v175\n"
"v_cmp_eq_u32_e32 vcc, -1, v252\n"
"s_cbranch_vccz .Lmk_p79k\n"
"s_sub_u32 s1, s1, 1\n"
"s_cbranch_scc0 .Lmk_p79l\n"
".Lmk_p79k:\n"
"v_mfma_f32_16x16x32_bf16 v[176:179], a[0:3], v[84:87], v[144:147]\n"
"v_mfma_f32_16x16x32_bf16 v[176:179], a[4:7], v[92:95], v[176:179]\n"
"v_mfma_f32_16x16x32_bf16 v[176:179], a[8:11], v[104:107], v[176:179]\n"
"v_mfma_f32_16x16x32_bf16 v[176:179], a[12:15], v[112:115], v[176:179]\n"
"v_mfma_f32_16x16x32_bf16 v[176:179], a[16:19], v[96:99], v[176:179]\n"
"v_mfma_f32_16x16x32_bf16 v[176:179], a[20:23], v[120:123], v[176:179]\n"
"v_mfma_f32_16x16x32_bf16 v[176:179], a[24:27], v[136:139], v[176:179]\n"
"v_mfma_f32_16x16x32_bf16 v[176:179], a[28:31], v[172:175], v[176:179]\n"
"v_mfma_f32_16x16x32_bf16 v[204:207], a[60:63], v[172:175], v[160:163]\n"
"v_mfma_f32_16x16x32_bf16 v[204:207], a[56:59], v[136:139], v[204:207]\n"
"v_mfma_f32_16x16x32_bf16 v[204:207], a[52:55], v[120:123], v[204:207]\n"
"v_mfma_f32_16x16x32_bf16 v[204:207], a[48:51], v[96:99], v[204:207]\n"
"v_mfma_f32_16x16x32_bf16 v[204:207], a[44:47], v[112:115], v[204:207]\n"
"v_mfma_f32_16x16x32_bf16 v[204:207], a[40:43], v[104:107], v[204:207]\n"
"s_nop 7\n"
"s_branch .Lmk_p79r\n"
".Lmk_p81s:\n"
";\n"
"s_waitcnt vmcnt(16)\n"
"s_mov_b32 s1, 0x10000\n"
".Lmk_p81l:\n"
"s_add_u32 s95, s52, 0x48000\n"
"buffer_load_dwordx4 v[140:143], v0, s[4:7], s95 offen sc1\n"
"buffer_load_dwordx4 v[68:71], v0, s[4:7], s95 offen offset:1024 sc1\n"
"buffer_load_dwordx4 v[168:171], v0, s[4:7], s95 offen offset:2048 sc1\n"
"buffer_load_dwordx4 v[152:155], v0, s[4:7], s95 offen offset:3072 sc1\n"
"buffer_load_dwordx4 v[148:151], v1, s[4:7], s95 offen sc1\n"
"buffer_load_dwordx4 v[164:167], v1, s[4:7], s95 offen offset:1024 sc1\n"
"buffer_load_dwordx4 v[180:183], v1, s[4:7], s95 offen offset:2048 sc1\n"
"buffer_load_dwordx4 v[124:127], v1, s[4:7], s95 offen offset:3072 sc1\n"
"s_add_u32 s95, s53, 0x48000\n"
"buffer_load_dwordx4 v[200:203], v0, s[4:7], s95 offen sc1\n"
"buffer_load_dwordx4 v[116:119], v0, s[4:7], s95 offen offset:1024 sc1\n"
"buffer_load_dwordx4 v[144:147], v0, s[4:7], s95 offen offset:2048 sc1\n"
"buffer_load_dwordx4 v[32:35], v0, s[4:7], s95 offen offset:3072 sc1\n"
"buffer_load_dwordx4 v[156:159], v1, s[4:7], s95 offen sc1\n"
"buffer_load_dwordx4 v[184:187], v1, s[4:7], s95 offen offset:1024 sc1\n"
"buffer_load_dwordx4 v[208:211], v1, s[4:7], s95 offen offset:2048 sc1\n"
"buffer_load_dwordx4 v[176:179], v1, s[4:7], s95 offen offset:3072 sc1\n"
"s_add_u32 s95, s54, 0x48000\n"
"buffer_load_dwordx4 v[92:95], v0, s[4:7], s95 offen sc1\n"
"buffer_load_dwordx4 v[96:99], v0, s[4:7], s95 offen offset:1024 sc1\n"
"s_waitcnt vmcnt(0)\n"
"v_max_u32_e32 v252, v140, v141\n"
"v_max3_u32 v252, v252, v142, v143\n"
"v_max3_u32 v252, v252, v68, v69\n"
"v_max3_u32 v252, v252, v70, v71\n"
"v_max3_u32 v252, v252, v168, v169\n"
"v_max3_u32 v252, v252, v170, v171\n"
"v_max3_u32 v252, v252, v152, v153\n"
"v_max3_u32 v252, v252, v154, v155\n"
"v_max3_u32 v252, v252, v148, v149\n"
"v_max3_u32 v252, v252, v150, v151\n"
"v_max3_u32 v252, v252, v164, v165\n"
"v_max3_u32 v252, v252, v166, v167\n"
"v_max3_u32 v252, v252, v180, v181\n"
"v_max3_u32 v252, v252, v182, v183\n"
"v_max3_u32 v252, v252, v124, v125\n"
"v_max3_u32 v252, v252, v126, v127\n"
"v_cmp_eq_u32_e32 vcc, -1, v252\n"
"s_cbranch_vccz .Lmk_p81k\n"
"s_sub_u32 s1, s1, 1\n"
"s_cbranch_scc0 .Lmk_p81l\n"
".Lmk_p81k:\n"
"v_mfma_f32_16x16x32_bf16 v[84:87], a[0:3], v[140:143], 0\n"
"v_mfma_f32_16x16x32_bf16 v[84:87], a[4:7], v[68:71], v[84:87]\n"
"v_mfma_f32_16x16x32_bf16 v[84:87], a[8:11], v[168:171], v[84:87]\n"
"v_mfma_f32_16x16x32_bf16 v[84:87], a[12:15], v[152:155], v[84:87]\n"
"v_mfma_f32_16x16x32_bf16 v[84:87], a[16:19], v[148:151], v[84:87]\n"
"v_mfma_f32_16x16x32_bf16 v[84:87], a[20:23], v[164:167], v[84:87]\n"
"v_mfma_f32_16x16x32_bf16 v[84:87], a[24:27], v[180:183], v[84:87]\n"
"v_mfma_f32_16x16x32_bf16 v[84:87], a[28:31], v[124:127], v[84:87]\n"
"v_mfma_f32_16x16x32_bf16 v[104:107], a[60:63], v[124:127], 0\n"
"v_mfma_f32_16x16x32_bf16 v[104:107], a[56:59], v[180:183], v[104:107]\n"
"v_mfma_f32_16x16x32_bf16 v[104:107], a[52:55], v[164:167], v[104:107]\n"
"v_mfma_f32_16x16x32_bf16 v[104:107], a[48:51], v[148:151], v[104:107]\n"
"v_mfma_f32_16x16x32_bf16 v[104:107], a[44:47], v[152:155], v[104:107]\n"
"v_mfma_f32_16x16x32_bf16 v[104:107], a[40:43], v[168:171], v[104:107]\n"
"s_nop 7\n"
"s_branch .Lmk_p81r\n"
".Lmk_p82s:\n"
";\n"
"s_waitcnt vmcnt(16)\n"
"s_mov_b32 s1, 0x10000\n"
".Lmk_p82l:\n"
"s_add_u32 s95, s53, 0x48000\n"
"buffer_load_dwordx4 v[200:203], v0, s[4:7], s95 offen sc1\n"
"buffer_load_dwordx4 v[116:119], v0, s[4:7], s95 offen offset:1024 sc1\n"
"buffer_load_dwordx4 v[144:147], v0, s[4:7], s95 offen offset:2048 sc1\n"
"buffer_load_dwordx4 v[32:35], v0, s[4:7], s95 offen offset:3072 sc1\n"
"buffer_load_dwordx4 v[156:159], v1, s[4:7], s95 offen sc1\n"
"buffer_load_dwordx4 v[184:187], v1, s[4:7], s95 offen offset:1024 sc1\n"
"buffer_load_dwordx4 v[208:211], v1, s[4:7], s95 offen offset:2048 sc1\n"
"buffer_load_dwordx4 v[176:179], v1, s[4:7], s95 offen offset:3072 sc1\n"
"s_add_u32 s95, s54, 0x48000\n"
"buffer_load_dwordx4 v[92:95], v0, s[4:7], s95 offen sc1\n"
"buffer_load_dwordx4 v[96:99], v0, s[4:7], s95 offen offset:1024 sc1\n"
"buffer_load_dwordx4 v[112:115], v0, s[4:7], s95 offen offset:2048 sc1\n"
"buffer_load_dwordx4 v[120:123], v0, s[4:7], s95 offen offset:3072 sc1\n"
"buffer_load_dwordx4 v[104:107], v1, s[4:7], s95 offen sc1\n"
"buffer_load_dwordx4 v[172:175], v1, s[4:7], s95 offen offset:1024 sc1\n"
"buffer_load_dwordx4 v[84:87], v1, s[4:7], s95 offen offset:2048 sc1\n"
"buffer_load_dwordx4 v[160:163], v1, s[4:7], s95 offen offset:3072 sc1\n"
"s_add_u32 s95, s55, 0x48000\n"
"buffer_load_dwordx4 v[68:71], v0, s[4:7], s95 offen sc1\n"
"buffer_load_dwordx4 v[124:127], v0, s[4:7], s95 offen offset:1024 sc1\n"
"s_waitcnt vmcnt(0)\n"
"v_max_u32_e32 v247, v200, v201\n"
"v_max3_u32 v247, v247, v202, v203\n"
"v_max3_u32 v247, v247, v116, v117\n"
"v_max3_u32 v247, v247, v118, v119\n"
"v_max3_u32 v247, v247, v144, v145\n"
"v_max3_u32 v247, v247, v146, v147\n"
"v_max3_u32 v247, v247, v32, v33\n"
"v_max3_u32 v247, v247, v34, v35\n"
"v_max3_u32 v247, v247, v156, v157\n"
"v_max3_u32 v247, v247, v158, v159\n"
"v_max3_u32 v247, v247, v184, v185\n"
"v_max3_u32 v247, v247, v186, v187\n"
"v_max3_u32 v247, v247, v208, v209\n"
"v_max3_u32 v247, v247, v210, v211\n"
"v_max3_u32 v247, v247, v176, v177\n"
"v_max3_u32 v247, v247, v178, v179\n"
"v_cmp_eq_u32_e32 vcc, -1, v247\n"
"s_cbranch_vccz .Lmk_p82k\n"
"s_sub_u32 s1, s1, 1\n"
"s_cbranch_scc0 .Lmk_p82l\n"
".Lmk_p82k:\n"
"v_mfma_f32_16x16x32_bf16 v[40:43], a[0:3], v[200:203], 0\n"
"v_mfma_f32_16x16x32_bf16 v[40:43], a[4:7], v[116:119], v[40:43]\n"
"v_mfma_f32_16x16x32_bf16 v[40:43], a[8:11], v[144:147], v[40:43]\n"
"v_mfma_f32_16x16x32_bf16 v[40:43], a[12:15], v[32:35], v[40:43]\n"
"v_mfma_f32_16x16x32_bf16 v[40:43], a[16:19], v[156:159], v[40:43]\n"
"v_mfma_f32_16x16x32_bf16 v[40:43], a[20:23], v[184:187], v[40:43]\n"
"v_mfma_f32_16x16x32_bf16 v[40:43], a[24:27], v[208:211], v[40:43]\n"
"v_mfma_f32_16x16x32_bf16 v[40:43], a[28:31], v[176:179], v[40:43]\n"
"v_mfma_f32_16x16x32_bf16 v[136:139], a[60:63], v[176:179], 0\n"
"v_mfma_f32_16x16x32_bf16 v[136:139], a[56:59], v[208:211], v[136:139]\n"
"v_mfma_f32_16x16x32_bf16 v[136:139], a[52:55], v[184:187], v[136:139]\n"
"v_mfma_f32_16x16x32_bf16 v[136:139], a[48:51], v[156:159], v[136:139]\n"
"v_mfma_f32_16x16x32_bf16 v[136:139], a[44:47], v[32:35], v[136:139]\n"
"v_mfma_f32_16x16x32_bf16 v[136:139], a[40:43], v[144:147], v[136:139]\n"
"s_nop 7\n"
"s_branch .Lmk_p82r\n"
".Lmk_m85s:\n"
";\n"
"s_waitcnt vmcnt(16)\n"
"s_mov_b32 s1, 0x10000\n"
".Lmk_m85l:\n"
"s_add_u32 s98, s62, 0x4800\n"
"buffer_load_dword v255, v10, s[4:7], s98 offen sc1\n"
"buffer_load_dword v253, v10, s[4:7], s98 offen offset:256 sc1\n"
"buffer_load_dword v252, v10, s[4:7], s98 offen offset:512 sc1\n"
"buffer_load_dword v251, v10, s[4:7], s98 offen offset:768 sc1\n"
"buffer_load_dword v250, v10, s[4:7], s98 offen offset:1024 sc1\n"
"buffer_load_dword v254, v10, s[4:7], s98 offen offset:1280 sc1\n"
"buffer_load_dword v249, v10, s[4:7], s98 offen offset:1536 sc1\n"
"buffer_load_dword v248, v10, s[4:7], s98 offen offset:1792 sc1\n"
"s_waitcnt vmcnt(0)\n"
"v_max_u32_e32 v245, v255, v253\n"
"v_max3_u32 v245, v245, v252, v251\n"
"v_max3_u32 v245, v245, v250, v254\n"
"v_max3_u32 v245, v245, v249, v248\n"
"v_cmp_eq_u32_e32 vcc, -1, v245\n"
"s_cbranch_vccz .Lmk_m85k\n"
"s_sub_u32 s1, s1, 1\n"
"s_cbranch_scc0 .Lmk_m85l\n"
".Lmk_m85k:\n"
"v_add_f32_e32 v255, v255, v250\n"
"v_add_f32_e32 v253, v253, v254\n"
"v_add_f32_e32 v252, v252, v249\n"
"v_add_f32_e32 v251, v251, v248\n"
"v_add_f32_e32 v255, v255, v252\n"
"v_add_f32_e32 v253, v253, v251\n"
"v_add_f32_e32 v255, v255, v253\n"
"s_nop 1\n"
"s_branch .Lmk_m85r\n"
".Lmk_f72s:\n"
";\n"
"s_mov_b32 s1, 0x10000\n"
".Lmk_f72l:\n"
"buffer_load_dwordx2 v[246:247], v12, s[4:7], s97 offen sc1\n"
"s_waitcnt vmcnt(0)\n"
"v_cmp_ne_u64_e32 vcc, v[246:247], v[18:19]\n"
"s_cbranch_vccz .Lmk_f72r\n"
"s_sub_u32 s1, s1, 1\n"
"s_cbranch_scc0 .Lmk_f72l\n"
"s_branch .Lmk_f72r\n"
".Lmk_p83s:\n"
";\n"
"s_waitcnt vmcnt(16)\n"
"s_mov_b32 s1, 0x10000\n"
".Lmk_p83l:\n"
"s_add_u32 s97, s54, 0x48000\n"
"buffer_load_dwordx4 v[92:95], v0, s[4:7], s97 offen sc1\n"
"buffer_load_dwordx4 v[96:99], v0, s[4:7], s97 offen offset:1024 sc1\n"
"buffer_load_dwordx4 v[112:115], v0, s[4:7], s97 offen offset:2048 sc1\n"
"buffer_load_dwordx4 v[120:123], v0, s[4:7], s97 offen offset:3072 sc1\n"
"buffer_load_dwordx4 v[104:107], v1, s[4:7], s97 offen sc1\n"
"buffer_load_dwordx4 v[172:175], v1, s[4:7], s97 offen offset:1024 sc1\n"
"buffer_load_dwordx4 v[84:87], v1, s[4:7], s97 offen offset:2048 sc1\n"
"buffer_load_dwordx4 v[160:163], v1, s[4:7], s97 offen offset:3072 sc1\n"
"s_add_u32 s97, s55, 0x48000\n"
"buffer_load_dwordx4 v[68:71], v0, s[4:7], s97 offen sc1\n"
"buffer_load_dwordx4 v[124:127], v0, s[4:7], s97 offen offset:1024 sc1\n"
"buffer_load_dwordx4 v[140:143], v0, s[4:7], s97 offen offset:2048 sc1\n"
"buffer_load_dwordx4 v[148:151], v0, s[4:7], s97 offen offset:3072 sc1\n"
"buffer_load_dwordx4 v[136:139], v1, s[4:7], s97 offen sc1\n"
"buffer_load_dwordx4 v[164:167], v1, s[4:7], s97 offen offset:1024 sc1\n"
"buffer_load_dwordx4 v[168:171], v1, s[4:7], s97 offen offset:2048 sc1\n"
"buffer_load_dwordx4 v[180:183], v1, s[4:7], s97 offen offset:3072 sc1\n"
"s_waitcnt vmcnt(0)\n"
"v_max_u32_e32 v255, v92, v93\n"
"v_max3_u32 v255, v255, v94, v95\n"
"v_max3_u32 v255, v255, v96, v97\n"
"v_max3_u32 v255, v255, v98, v99\n"
"v_max3_u32 v255, v255, v112, v113\n"
"v_max3_u32 v255, v255, v114, v115\n"
"v_max3_u32 v255, v255, v120, v121\n"
"v_max3_u32 v255, v255, v122, v123\n"
"v_max3_u32 v255, v255, v104, v105\n"
"v_max3_u32 v255, v255, v106, v107\n"
"v_max3_u32 v255, v255, v172, v173\n"
"v_max3_u32 v255, v255, v174, v175\n"
"v_max3_u32 v255, v255, v84, v85\n"
"v_max3_u32 v255, v255, v86, v87\n"
"v_max3_u32 v255, v255, v160, v161\n"
"v_max3_u32 v255, v255, v162, v163\n"
"v_cmp_eq_u32_e32 vcc, -1, v255\n"
"s_cbranch_vccz .Lmk_p83k\n"
"s_sub_u32 s1, s1, 1\n"
"s_cbranch_scc0 .Lmk_p83l\n"
".Lmk_p83k:\n"
"v_mfma_f32_16x16x32_bf16 v[32:35], a[0:3], v[92:95], 0\n"
"v_mfma_f32_16x16x32_bf16 v[32:35], a[4:7], v[96:99], v[32:35]\n"
"v_mfma_f32_16x16x32_bf16 v[32:35], a[8:11], v[112:115], v[32:35]\n"
"v_mfma_f32_16x16x32_bf16 v[32:35], a[12:15], v[120:123], v[32:35]\n"
"v_mfma_f32_16x16x32_bf16 v[32:35], a[16:19], v[104:107], v[32:35]\n"
"v_mfma_f32_16x16x32_bf16 v[32:35], a[20:23], v[172:175], v[32:35]\n"
"v_mfma_f32_16x16x32_bf16 v[32:35], a[24:27], v[84:87], v[32:35]\n"
"v_mfma_f32_16x16x32_bf16 v[32:35], a[28:31], v[160:163], v[32:35]\n"
"v_mfma_f32_16x16x32_bf16 v[40:43], a[60:63], v[160:163], 0\n"
"v_mfma_f32_16x16x32_bf16 v[40:43], a[56:59], v[84:87], v[40:43]\n"
"v_mfma_f32_16x16x32_bf16 v[40:43], a[52:55], v[172:175], v[40:43]\n"
"v_mfma_f32_16x16x32_bf16 v[40:43], a[48:51], v[104:107], v[40:43]\n"
"v_mfma_f32_16x16x32_bf16 v[40:43], a[44:47], v[120:123], v[40:43]\n"
"v_mfma_f32_16x16x32_bf16 v[40:43], a[40:43], v[112:115], v[40:43]\n"
"s_nop 7\n"
"s_branch .Lmk_p83r\n"
".Lmk_p84s:\n"
";\n"
"s_waitcnt vmcnt(16)\n"
"s_mov_b32 s1, 0x10000\n"
".Lmk_p84l:\n"
"s_add_u32 s97, s55, 0x48000\n"
"buffer_load_dwordx4 v[68:71], v0, s[4:7], s97 offen sc1\n"
"buffer_load_dwordx4 v[124:127], v0, s[4:7], s97 offen offset:1024 sc1\n"
"buffer_load_dwordx4 v[140:143], v0, s[4:7], s97 offen offset:2048 sc1\n"
"buffer_load_dwordx4 v[148:151], v0, s[4:7], s97 offen offset:3072 sc1\n"
"buffer_load_dwordx4 v[136:139], v1, s[4:7], s97 offen sc1\n"
"buffer_load_dwordx4 v[164:167], v1, s[4:7], s97 offen offset:1024 sc1\n"
"buffer_load_dwordx4 v[168:171], v1, s[4:7], s97 offen offset:2048 sc1\n"
"buffer_load_dwordx4 v[180:183], v1, s[4:7], s97 offen offset:3072 sc1\n"
"s_waitcnt vmcnt(0)\n"
"v_max_u32_e32 v252, v68, v69\n"
"v_max3_u32 v252, v252, v70, v71\n"
"v_max3_u32 v252, v252, v124, v125\n"
"v_max3_u32 v252, v252, v126, v127\n"
"v_max3_u32 v252, v252, v140, v141\n"
"v_max3_u32 v252, v252, v142, v143\n"
"v_max3_u32 v252, v252, v148, v149\n"
"v_max3_u32 v252, v252, v150, v151\n"
"v_max3_u32 v252, v252, v136, v137\n"
"v_max3_u32 v252, v252, v138, v139\n"
"v_max3_u32 v252, v252, v164, v165\n"
"v_max3_u32 v252, v252, v166, v167\n"
"v_max3_u32 v252, v252, v168, v169\n"
"v_max3_u32 v252, v252, v170, v171\n"
"v_max3_u32 v252, v252, v180, v181\n"
"v_max3_u32 v252, v252, v182, v183\n"
"v_cmp_eq_u32_e32 vcc, -1, v252\n"
"s_cbranch_vccz .Lmk_p84k\n"
"s_sub_u32 s1, s1, 1\n"
"s_cbranch_scc0 .Lmk_p84l\n"
".Lmk_p84k:\n"
"v_mfma_f32_16x16x32_bf16 v[112:115], a[0:3], v[68:71], v[116:119]\n"
"v_mfma_f32_16x16x32_bf16 v[112:115], a[4:7], v[124:127], v[112:115]\n"
"v_mfma_f32_16x16x32_bf16 v[112:115], a[8:11], v[140:143], v[112:115]\n"
"v_mfma_f32_16x16x32_bf16 v[112:115], a[12:15], v[148:151], v[112:115]\n"
"v_mfma_f32_16x16x32_bf16 v[112:115], a[16:19], v[136:139], v[112:115]\n"
"v_mfma_f32_16x16x32_bf16 v[112:115], a[20:23], v[164:167], v[112:115]\n"
"v_mfma_f32_16x16x32_bf16 v[112:115], a[24:27], v[168:171], v[112:115]\n"
"v_mfma_f32_16x16x32_bf16 v[112:115], a[28:31], v[180:183], v[112:115]\n"
"v_mfma_f32_16x16x32_bf16 v[200:203], a[60:63], v[180:183], v[176:179]\n"
"v_mfma_f32_16x16x32_bf16 v[200:203], a[56:59], v[168:171], v[200:203]\n"
"v_mfma_f32_16x16x32_bf16 v[200:203], a[52:55], v[164:167], v[200:203]\n"
"v_mfma_f32_16x16x32_bf16 v[200:203], a[48:51], v[136:139], v[200:203]\n"
"v_mfma_f32_16x16x32_bf16 v[200:203], a[44:47], v[148:151], v[200:203]\n"
"v_mfma_f32_16x16x32_bf16 v[200:203], a[40:43], v[140:143], v[200:203]\n"
"s_nop 7\n"
"s_branch .Lmk_p84r\n"
".Lmk_f73s:\n"
";\n"
"s_mov_b32 s1, 0x10000\n"
".Lmk_f73l:\n"
"buffer_load_dwordx2 v[250:251], off, s[4:7], s98 sc1\n"
"s_waitcnt vmcnt(0)\n"
"v_cmp_ne_u64_e32 vcc, v[250:251], v[18:19]\n"
"s_cbranch_vccz .Lmk_f73r\n"
"s_sub_u32 s1, s1, 1\n"
"s_cbranch_scc0 .Lmk_f73l\n"
"s_branch .Lmk_f73r\n"
".Lmk_x94s:\n"
"s_nop 3\n"
"v_mov_b32_e32 v36, 0xff800000\n"
"v_mov_b32_e32 v37, 0xff800000\n"
"v_mov_b32_e32 v38, 0xff800000\n"
"v_mov_b32_e32 v39, 0xff800000\n"
"v_mov_b32_e32 v60, 0xff800000\n"
"v_mov_b32_e32 v61, 0xff800000\n"
"v_mov_b32_e32 v62, 0xff800000\n"
"v_mov_b32_e32 v63, 0xff800000\n"
"s_nop 1\n"
"s_branch .Lmk_x94r\n"
".Lmk_x95s:\n"
"s_nop 3\n"
"v_mov_b32_e32 v36, 0xff800000\n"
"v_mov_b32_e32 v37, 0xff800000\n"
"v_mov_b32_e32 v38, 0xff800000\n"
"v_mov_b32_e32 v39, 0xff800000\n"
"v_mov_b32_e32 v60, 0xff800000\n"
"v_mov_b32_e32 v61, 0xff800000\n"
"v_mov_b32_e32 v62, 0xff800000\n"
"v_mov_b32_e32 v63, 0xff800000\n"
"s_nop 1\n"
"s_branch .Lmk_x95r\n"
".Lmk_x96s:\n"
"s_nop 3\n"
"v_mov_b32_e32 v36, 0xff800000\n"
"v_mov_b32_e32 v37, 0xff800000\n"
"v_mov_b32_e32 v38, 0xff800000\n"
"v_mov_b32_e32 v39, 0xff800000\n"
"v_mov_b32_e32 v60, 0xff800000\n"
"v_mov_b32_e32 v61, 0xff800000\n"
"v_mov_b32_e32 v62, 0xff800000\n"
"v_mov_b32_e32 v63, 0xff800000\n"
"s_nop 1\n"
"s_branch .Lmk_x96r\n"
".Lmk_x97s:\n"
"s_nop 3\n"
"v_mov_b32_e32 v36, 0xff800000\n"
"v_mov_b32_e32 v37, 0xff800000\n"
"v_mov_b32_e32 v38, 0xff800000\n"
"v_mov_b32_e32 v39, 0xff800000\n"
"v_mov_b32_e32 v60, 0xff800000\n"
"v_mov_b32_e32 v61, 0xff800000\n"
"v_mov_b32_e32 v62, 0xff800000\n"
"v_mov_b32_e32 v63, 0xff800000\n"
"s_nop 1\n"
"s_branch .Lmk_x97r\n"
".Lmk_b92:\n"
"s_cmp_lg_u32 s2, 0\n"
"s_cbranch_scc1 .Lmk_k98\n"
"s_mov_b32 s2, 1\n"
"s_waitcnt vmcnt(0) lgkmcnt(0)\n"
"s_nop 7\n"
"s_mov_b32 s97, 11\n"
"v_add_u32_e32 v248, 0x3800, v15\n"
"v_add_u32_e32 v247, 0x3800, v254\n"
"ds_read_b128 v[36:39], v15 offset:24576\n"
"ds_read_b128 v[40:43], v254 offset:24832\n"
"s_waitcnt lgkmcnt(0)\n"
"v_mfma_f32_16x16x32_bf16 v[48:51], v[36:39], v[28:31], 0\n"
"v_mfma_f32_16x16x32_bf16 v[52:55], v[36:39], v[32:35], 0\n"
"v_mfma_f32_16x16x32_bf16 v[36:39], v[40:43], v[32:35], 0\n"
"v_mfma_f32_16x16x32_bf16 v[56:59], v[40:43], v[28:31], 0\n"
".Lmk_b92l:\n"
"ds_read_b128 v[40:43], v248 offset:8192\n"
"ds_read_b128 v[60:63], v247 offset:8448\n"
"s_cmp_gt_i32 s78, s97\n"
"s_cselect_b32 s96, 0xff800000, 0\n"
"v_mov_b32_e32 v64, s96\n"
"v_mov_b32_e32 v65, s96\n"
"v_mov_b32_e32 v66, s96\n"
"v_mov_b32_e32 v67, s96\n"
"v_subrev_u32_e32 v248, 0x800, v248\n"
"v_subrev_u32_e32 v247, 0x800, v247\n"
"s_waitcnt lgkmcnt(0)\n"
"v_mfma_f32_16x16x32_bf16 v[72:75], v[40:43], v[28:31], v[64:67]\n"
"v_mfma_f32_16x16x32_bf16 v[76:79], v[40:43], v[32:35], v[64:67]\n"
"v_mfma_f32_16x16x32_bf16 v[40:43], v[60:63], v[32:35], v[64:67]\n"
"v_mfma_f32_16x16x32_bf16 v[84:87], v[60:63], v[28:31], v[64:67]\n"
"s_nop 7\n"
"s_cmp_lg_u32 s97, 4\n"
"s_cbranch_scc1 .Lmk_b92m\n"
"v_cndmask_b32_e64 v72, v48, v72, s[20:21]\n"
"v_cndmask_b32_e64 v73, v49, v73, s[22:23]\n"
"v_cndmask_b32_e64 v74, v50, v74, s[24:25]\n"
"v_cndmask_b32_e64 v75, v51, v75, s[26:27]\n"
"v_cndmask_b32_e64 v84, v56, v84, s[28:29]\n"
"v_cndmask_b32_e64 v85, v57, v85, s[30:31]\n"
"v_cndmask_b32_e64 v86, v58, v86, s[32:33]\n"
"v_cndmask_b32_e64 v87, v59, v87, s[34:35]\n"
"v_cndmask_b32_e64 v76, v52, v76, s[36:37]\n"
"v_cndmask_b32_e64 v77, v53, v77, s[38:39]\n"
"v_cndmask_b32_e64 v78, v54, v78, s[40:41]\n"
"v_cndmask_b32_e64 v79, v55, v79, s[42:43]\n"
"v_cndmask_b32_e64 v40, v36, v40, s[44:45]\n"
"v_cndmask_b32_e64 v41, v37, v41, s[46:47]\n"
"v_cndmask_b32_e64 v42, v38, v42, s[48:49]\n"
"v_cndmask_b32_e64 v43, v39, v43, s[50:51]\n"
".Lmk_b92m:\n"
"v_max3_f32 v253, v253, v72, v73\n"
"v_max3_f32 v251, v251, v76, v77\n"
"v_max3_f32 v253, v253, v74, v75\n"
"v_max3_f32 v251, v251, v78, v79\n"
"v_max3_f32 v253, v253, v84, v85\n"
"v_max3_f32 v251, v251, v40, v41\n"
"v_max3_f32 v253, v253, v86, v87\n"
"v_max3_f32 v251, v251, v42, v43\n"
"s_sub_u32 s97, s97, 1\n"
"s_cmp_ge_i32 s97, 4\n"
"s_cbranch_scc1 .Lmk_b92l\n"
"v_mov_b32_e32 v248, v253\n"
"v_mov_b32_e32 v247, v251\n"
"s_nop 0\n"
"v_permlane16_swap_b32_e32 v253, v248\n"
"v_permlane16_swap_b32_e32 v251, v247\n"
"v_max_f32_e32 v253, v253, v248\n"
"v_max_f32_e32 v251, v251, v247\n"
"v_mov_b32_e32 v248, v253\n"
"v_mov_b32_e32 v247, v251\n"
"s_nop 0\n"
"v_permlane32_swap_b32_e32 v253, v248\n"
"v_permlane32_swap_b32_e32 v251, v247\n"
"v_max_f32_e32 v253, v253, v248\n"
"v_max_f32_e32 v251, v251, v247\n"
"v_mov_b32_e32 v250, v253\n"
"v_mov_b32_e32 v249, v251\n"
"s_branch .Lmk_a91\n"
".Lmk_x101s:\n"
"s_nop 3\n"
"v_mov_b32_e32 v116, 0xff800000\n"
"v_mov_b32_e32 v117, 0xff800000\n"
"v_mov_b32_e32 v118, 0xff800000\n"
"v_mov_b32_e32 v119, 0xff800000\n"
"v_mov_b32_e32 v120, 0xff800000\n"
"v_mov_b32_e32 v121, 0xff800000\n"
"v_mov_b32_e32 v122, 0xff800000\n"
"v_mov_b32_e32 v123, 0xff800000\n"
"s_nop 1\n"
"s_branch .Lmk_x101r\n"
".Lmk_x102s:\n"
"s_nop 3\n"
"v_mov_b32_e32 v116, 0xff800000\n"
"v_mov_b32_e32 v117, 0xff800000\n"
"v_mov_b32_e32 v118, 0xff800000\n"
"v_mov_b32_e32 v119, 0xff800000\n"
"v_mov_b32_e32 v120, 0xff800000\n"
"v_mov_b32_e32 v121, 0xff800000\n"
"v_mov_b32_e32 v122, 0xff800000\n"
"v_mov_b32_e32 v123, 0xff800000\n"
"s_nop 1\n"
"s_branch .Lmk_x102r\n"
".Lmk_x103s:\n"
"s_nop 3\n"
"v_mov_b32_e32 v116, 0xff800000\n"
"v_mov_b32_e32 v117, 0xff800000\n"
"v_mov_b32_e32 v118, 0xff800000\n"
"v_mov_b32_e32 v119, 0xff800000\n"
"v_mov_b32_e32 v120, 0xff800000\n"
"v_mov_b32_e32 v121, 0xff800000\n"
"v_mov_b32_e32 v122, 0xff800000\n"
"v_mov_b32_e32 v123, 0xff800000\n"
"s_nop 1\n"
"s_branch .Lmk_x103r\n"
".Lmk_h90s:\n"
";\n"
"s_waitcnt vmcnt(16)\n"
"s_mov_b32 s1, 0x10000\n"
".Lmk_h90l:\n"
"s_add_u32 m0, s77, 0x4000\n"
"s_add_u32 s95, s65, 0\n"
"buffer_load_dwordx4 v2, s[4:7], s95 offen sc1 lds\n"
"buffer_load_dwordx4 v2, s[4:7], s95 offen offset:1024 sc1 lds\n"
"s_add_u32 m0, s77, 0xc000\n"
"s_add_u32 s95, s65, 0x4000\n"
"buffer_load_dwordx4 v2, s[4:7], s95 offen sc1 lds\n"
"buffer_load_dwordx4 v2, s[4:7], s95 offen offset:1024 sc1 lds\n"
"s_waitcnt vmcnt(0)\n"
"ds_read_b128 v[164:167], v2 offset:16384\n"
"ds_read_b128 v[76:79], v2 offset:17408\n"
"ds_read_b128 v[72:75], v2 offset:49152\n"
"ds_read_b128 v[84:87], v2 offset:50176\n"
"s_waitcnt lgkmcnt(0)\n"
"s_waitcnt vmcnt(0)\n"
"v_max_u32_e32 v248, v164, v165\n"
"v_max3_u32 v248, v248, v166, v167\n"
"v_max3_u32 v248, v248, v76, v77\n"
"v_max3_u32 v248, v248, v78, v79\n"
"v_max3_u32 v248, v248, v72, v73\n"
"v_max3_u32 v248, v248, v74, v75\n"
"v_max3_u32 v248, v248, v84, v85\n"
"v_max3_u32 v248, v248, v86, v87\n"
"v_cmp_eq_u32_e32 vcc, -1, v248\n"
"s_cbranch_vccz .Lmk_h90k\n"
"s_sub_u32 s1, s1, 1\n"
"s_cbranch_scc0 .Lmk_h90l\n"
".Lmk_h90k:\n"
"s_branch .Lmk_h90r\n"
".Lmk_f74s:\n"
";\n"
"s_mov_b32 s1, 0x10000\n"
".Lmk_f74l:\n"
"buffer_load_dwordx2 v[246:247], v12, s[4:7], s96 offen sc1\n"
"s_waitcnt vmcnt(0)\n"
"v_cmp_ne_u64_e32 vcc, v[246:247], v[18:19]\n"
"s_cbranch_vccz .Lmk_f74r\n"
"s_sub_u32 s1, s1, 1\n"
"s_cbranch_scc0 .Lmk_f74l\n"
"s_branch .Lmk_f74r\n"
".Lmk_b100:\n"
"s_cmp_lg_u32 s2, 0\n"
"s_cbranch_scc1 .Lmk_k105\n"
"s_mov_b32 s2, 1\n"
"s_waitcnt vmcnt(0) lgkmcnt(0)\n"
"s_nop 7\n"
"s_mov_b32 s96, 7\n"
"v_add_u32_e32 v248, 0x3800, v15\n"
"v_add_u32_e32 v247, 0x3800, v254\n"
"ds_read_b128 v[188:191], v15 offset:16384\n"
"ds_read_b128 v[192:195], v254 offset:16640\n"
"s_waitcnt lgkmcnt(0)\n"
"v_mfma_f32_16x16x32_bf16 v[196:199], v[188:191], v[92:95], 0\n"
"v_mfma_f32_16x16x32_bf16 v[212:215], v[188:191], v[96:99], 0\n"
"v_mfma_f32_16x16x32_bf16 v[188:191], v[192:195], v[96:99], 0\n"
"v_mfma_f32_16x16x32_bf16 v[220:223], v[192:195], v[92:95], 0\n"
".Lmk_b100l:\n"
"ds_read_b128 v[192:195], v248\n"
"ds_read_b128 v[224:227], v247 offset:256\n"
"s_cmp_gt_i32 s78, s96\n"
"s_cselect_b32 s95, 0xff800000, 0\n"
"v_mov_b32_e32 v228, s95\n"
"v_mov_b32_e32 v229, s95\n"
"v_mov_b32_e32 v230, s95\n"
"v_mov_b32_e32 v231, s95\n"
"v_subrev_u32_e32 v248, 0x800, v248\n"
"v_subrev_u32_e32 v247, 0x800, v247\n"
"s_waitcnt lgkmcnt(0)\n"
"v_mfma_f32_16x16x32_bf16 v[232:235], v[192:195], v[92:95], v[228:231]\n"
"v_mfma_f32_16x16x32_bf16 v[236:239], v[192:195], v[96:99], v[228:231]\n"
"v_mfma_f32_16x16x32_bf16 v[192:195], v[224:227], v[96:99], v[228:231]\n"
"v_mfma_f32_16x16x32_bf16 v[240:243], v[224:227], v[92:95], v[228:231]\n"
"s_nop 7\n"
"s_cmp_lg_u32 s96, 0\n"
"s_cbranch_scc1 .Lmk_b100m\n"
"v_cndmask_b32_e64 v232, v196, v232, s[20:21]\n"
"v_cndmask_b32_e64 v233, v197, v233, s[22:23]\n"
"v_cndmask_b32_e64 v234, v198, v234, s[24:25]\n"
"v_cndmask_b32_e64 v235, v199, v235, s[26:27]\n"
"v_cndmask_b32_e64 v240, v220, v240, s[28:29]\n"
"v_cndmask_b32_e64 v241, v221, v241, s[30:31]\n"
"v_cndmask_b32_e64 v242, v222, v242, s[32:33]\n"
"v_cndmask_b32_e64 v243, v223, v243, s[34:35]\n"
"v_cndmask_b32_e64 v236, v212, v236, s[36:37]\n"
"v_cndmask_b32_e64 v237, v213, v237, s[38:39]\n"
"v_cndmask_b32_e64 v238, v214, v238, s[40:41]\n"
"v_cndmask_b32_e64 v239, v215, v239, s[42:43]\n"
"v_cndmask_b32_e64 v192, v188, v192, s[44:45]\n"
"v_cndmask_b32_e64 v193, v189, v193, s[46:47]\n"
"v_cndmask_b32_e64 v194, v190, v194, s[48:49]\n"
"v_cndmask_b32_e64 v195, v191, v195, s[50:51]\n"
".Lmk_b100m:\n"
"v_max3_f32 v253, v253, v232, v233\n"
"v_max3_f32 v251, v251, v236, v237\n"
"v_max3_f32 v253, v253, v234, v235\n"
"v_max3_f32 v251, v251, v238, v239\n"
"v_max3_f32 v253, v253, v240, v241\n"
"v_max3_f32 v251, v251, v192, v193\n"
"v_max3_f32 v253, v253, v242, v243\n"
"v_max3_f32 v251, v251, v194, v195\n"
"s_sub_u32 s96, s96, 1\n"
"s_cmp_ge_i32 s96, 0\n"
"s_cbranch_scc1 .Lmk_b100l\n"
"v_mov_b32_e32 v248, v253\n"
"v_mov_b32_e32 v247, v251\n"
"s_nop 0\n"
"v_permlane16_swap_b32_e32 v253, v248\n"
"v_permlane16_swap_b32_e32 v251, v247\n"
"v_max_f32_e32 v253, v253, v248\n"
"v_max_f32_e32 v251, v251, v247\n"
"v_mov_b32_e32 v248, v253\n"
"v_mov_b32_e32 v247, v251\n"
"s_nop 0\n"
"v_permlane32_swap_b32_e32 v253, v248\n"
"v_permlane32_swap_b32_e32 v251, v247\n"
"v_max_f32_e32 v253, v253, v248\n"
"v_max_f32_e32 v251, v251, v247\n"
"v_mov_b32_e32 v250, v253\n"
"v_mov_b32_e32 v249, v251\n"
"s_branch .Lmk_a99\n"
".Lmk_p106s:\n"
";\n"
"s_waitcnt vmcnt(16)\n"
"s_mov_b32 s1, 0x10000\n"
".Lmk_p106l:\n"
"s_add_u32 s98, s56, 0x40000\n"
"buffer_load_dwordx4 v[152:155], v0, s[4:7], s98 offen sc1\n"
"buffer_load_dwordx4 v[200:203], v0, s[4:7], s98 offen offset:1024 sc1\n"
"buffer_load_dwordx4 v[184:187], v0, s[4:7], s98 offen offset:2048 sc1\n"
"buffer_load_dwordx4 v[204:207], v0, s[4:7], s98 offen offset:3072 sc1\n"
"buffer_load_dwordx4 v[140:143], v1, s[4:7], s98 offen sc1\n"
"buffer_load_dwordx4 v[208:211], v1, s[4:7], s98 offen offset:1024 sc1\n"
"buffer_load_dwordx4 v[120:123], v1, s[4:7], s98 offen offset:2048 sc1\n"
"buffer_load_dwordx4 v[136:139], v1, s[4:7], s98 offen offset:3072 sc1\n"
"s_add_u32 s98, s57, 0x40000\n"
"buffer_load_dwordx4 v[216:219], v0, s[4:7], s98 offen sc1\n"
"buffer_load_dwordx4 v[132:135], v0, s[4:7], s98 offen offset:1024 sc1\n"
"buffer_load_dwordx4 v[160:163], v0, s[4:7], s98 offen offset:2048 sc1\n"
"buffer_load_dwordx4 v[100:103], v0, s[4:7], s98 offen offset:3072 sc1\n"
"buffer_load_dwordx4 v[164:167], v1, s[4:7], s98 offen sc1\n"
"buffer_load_dwordx4 v[104:107], v1, s[4:7], s98 offen offset:1024 sc1\n"
"buffer_load_dwordx4 v[108:111], v1, s[4:7], s98 offen offset:2048 sc1\n"
"buffer_load_dwordx4 v[128:131], v1, s[4:7], s98 offen offset:3072 sc1\n"
"s_add_u32 s98, s58, 0x40000\n"
"buffer_load_dwordx4 v[148:151], v0, s[4:7], s98 offen sc1\n"
"buffer_load_dwordx4 v[144:147], v0, s[4:7], s98 offen offset:1024 sc1\n"
"buffer_load_dwordx4 v[116:119], v0, s[4:7], s98 offen offset:2048 sc1\n"
"buffer_load_dwordx4 v[112:115], v0, s[4:7], s98 offen offset:3072 sc1\n"
"buffer_load_dwordx4 v[92:95], v1, s[4:7], s98 offen sc1\n"
"buffer_load_dwordx4 v[96:99], v1, s[4:7], s98 offen offset:1024 sc1\n"
"buffer_load_dwordx4 v[156:159], v1, s[4:7], s98 offen offset:2048 sc1\n"
"s_waitcnt vmcnt(0)\n"
"v_max_u32_e32 v255, v152, v153\n"
"v_max3_u32 v255, v255, v154, v155\n"
"v_max3_u32 v255, v255, v200, v201\n"
"v_max3_u32 v255, v255, v202, v203\n"
"v_max3_u32 v255, v255, v184, v185\n"
"v_max3_u32 v255, v255, v186, v187\n"
"v_max3_u32 v255, v255, v204, v205\n"
"v_max3_u32 v255, v255, v206, v207\n"
"v_max3_u32 v255, v255, v140, v141\n"
"v_max3_u32 v255, v255, v142, v143\n"
"v_max3_u32 v255, v255, v208, v209\n"
"v_max3_u32 v255, v255, v210, v211\n"
"v_max3_u32 v255, v255, v120, v121\n"
"v_max3_u32 v255, v255, v122, v123\n"
"v_max3_u32 v255, v255, v136, v137\n"
"v_max3_u32 v255, v255, v138, v139\n"
"v_cmp_eq_u32_e32 vcc, -1, v255\n"
"s_cbranch_vccz .Lmk_p106k\n"
"s_sub_u32 s1, s1, 1\n"
"s_cbranch_scc0 .Lmk_p106l\n"
".Lmk_p106k:\n"
"v_mfma_f32_16x16x32_bf16 v[88:91], a[128:131], v[152:155], 0\n"
"v_mfma_f32_16x16x32_bf16 v[88:91], a[132:135], v[200:203], v[88:91]\n"
"v_mfma_f32_16x16x32_bf16 v[88:91], a[136:139], v[184:187], v[88:91]\n"
"v_mfma_f32_16x16x32_bf16 v[88:91], a[140:143], v[204:207], v[88:91]\n"
"v_mfma_f32_16x16x32_bf16 v[88:91], a[144:147], v[140:143], v[88:91]\n"
"v_mfma_f32_16x16x32_bf16 v[88:91], a[148:151], v[208:211], v[88:91]\n"
"v_mfma_f32_16x16x32_bf16 v[88:91], a[152:155], v[120:123], v[88:91]\n"
"v_mfma_f32_16x16x32_bf16 v[88:91], a[156:159], v[136:139], v[88:91]\n"
"v_mfma_f32_16x16x32_bf16 v[124:127], a[188:191], v[136:139], 0\n"
"v_mfma_f32_16x16x32_bf16 v[124:127], a[184:187], v[120:123], v[124:127]\n"
"v_mfma_f32_16x16x32_bf16 v[124:127], a[180:183], v[208:211], v[124:127]\n"
"v_mfma_f32_16x16x32_bf16 v[124:127], a[176:179], v[140:143], v[124:127]\n"
"v_mfma_f32_16x16x32_bf16 v[124:127], a[172:175], v[204:207], v[124:127]\n"
"v_mfma_f32_16x16x32_bf16 v[124:127], a[168:171], v[184:187], v[124:127]\n"
"s_nop 7\n"
"s_branch .Lmk_p106r\n"
".Lmk_p107s:\n"
";\n"
"s_waitcnt vmcnt(16)\n"
"s_mov_b32 s1, 0x10000\n"
".Lmk_p107l:\n"
"s_add_u32 s97, s57, 0x40000\n"
"buffer_load_dwordx4 v[216:219], v0, s[4:7], s97 offen sc1\n"
"buffer_load_dwordx4 v[132:135], v0, s[4:7], s97 offen offset:1024 sc1\n"
"buffer_load_dwordx4 v[160:163], v0, s[4:7], s97 offen offset:2048 sc1\n"
"buffer_load_dwordx4 v[100:103], v0, s[4:7], s97 offen offset:3072 sc1\n"
"buffer_load_dwordx4 v[164:167], v1, s[4:7], s97 offen sc1\n"
"buffer_load_dwordx4 v[104:107], v1, s[4:7], s97 offen offset:1024 sc1\n"
"buffer_load_dwordx4 v[108:111], v1, s[4:7], s97 offen offset:2048 sc1\n"
"buffer_load_dwordx4 v[128:131], v1, s[4:7], s97 offen offset:3072 sc1\n"
"s_add_u32 s97, s58, 0x40000\n"
"buffer_load_dwordx4 v[148:151], v0, s[4:7], s97 offen sc1\n"
"buffer_load_dwordx4 v[144:147], v0, s[4:7], s97 offen offset:1024 sc1\n"
"buffer_load_dwordx4 v[116:119], v0, s[4:7], s97 offen offset:2048 sc1\n"
"buffer_load_dwordx4 v[112:115], v0, s[4:7], s97 offen offset:3072 sc1\n"
"buffer_load_dwordx4 v[92:95], v1, s[4:7], s97 offen sc1\n"
"buffer_load_dwordx4 v[96:99], v1, s[4:7], s97 offen offset:1024 sc1\n"
"buffer_load_dwordx4 v[156:159], v1, s[4:7], s97 offen offset:2048 sc1\n"
"buffer_load_dwordx4 v[88:91], v1, s[4:7], s97 offen offset:3072 sc1\n"
"s_add_u32 s97, s59, 0x40000\n"
"buffer_load_dwordx4 v[172:175], v0, s[4:7], s97 offen sc1\n"
"buffer_load_dwordx4 v[124:127], v0, s[4:7], s97 offen offset:1024 sc1\n"
"buffer_load_dwordx4 v[120:123], v0, s[4:7], s97 offen offset:2048 sc1\n"
"buffer_load_dwordx4 v[136:139], v0, s[4:7], s97 offen offset:3072 sc1\n"
"buffer_load_dwordx4 v[152:155], v1, s[4:7], s97 offen sc1\n"
"buffer_load_dwordx4 v[168:171], v1, s[4:7], s97 offen offset:1024 sc1\n"
"buffer_load_dwordx4 v[180:183], v1, s[4:7], s97 offen offset:2048 sc1\n"
"s_waitcnt vmcnt(0)\n"
"v_max_u32_e32 v255, v216, v217\n"
"v_max3_u32 v255, v255, v218, v219\n"
"v_max3_u32 v255, v255, v132, v133\n"
"v_max3_u32 v255, v255, v134, v135\n"
"v_max3_u32 v255, v255, v160, v161\n"
"v_max3_u32 v255, v255, v162, v163\n"
"v_max3_u32 v255, v255, v100, v101\n"
"v_max3_u32 v255, v255, v102, v103\n"
"v_max3_u32 v255, v255, v164, v165\n"
"v_max3_u32 v255, v255, v166, v167\n"
"v_max3_u32 v255, v255, v104, v105\n"
"v_max3_u32 v255, v255, v106, v107\n"
"v_max3_u32 v255, v255, v108, v109\n"
"v_max3_u32 v255, v255, v110, v111\n"
"v_max3_u32 v255, v255, v128, v129\n"
"v_max3_u32 v255, v255, v130, v131\n"
"v_cmp_eq_u32_e32 vcc, -1, v255\n"
"s_cbranch_vccz .Lmk_p107k\n"
"s_sub_u32 s1, s1, 1\n"
"s_cbranch_scc0 .Lmk_p107l\n"
".Lmk_p107k:\n"
"v_mfma_f32_16x16x32_bf16 v[140:143], a[128:131], v[216:219], 0\n"
"v_mfma_f32_16x16x32_bf16 v[140:143], a[132:135], v[132:135], v[140:143]\n"
"v_mfma_f32_16x16x32_bf16 v[140:143], a[136:139], v[160:163], v[140:143]\n"
"v_mfma_f32_16x16x32_bf16 v[140:143], a[140:143], v[100:103], v[140:143]\n"
"v_mfma_f32_16x16x32_bf16 v[140:143], a[144:147], v[164:167], v[140:143]\n"
"v_mfma_f32_16x16x32_bf16 v[140:143], a[148:151], v[104:107], v[140:143]\n"
"v_mfma_f32_16x16x32_bf16 v[140:143], a[152:155], v[108:111], v[140:143]\n"
"v_mfma_f32_16x16x32_bf16 v[140:143], a[156:159], v[128:131], v[140:143]\n"
"v_mfma_f32_16x16x32_bf16 v[176:179], a[188:191], v[128:131], 0\n"
"v_mfma_f32_16x16x32_bf16 v[176:179], a[184:187], v[108:111], v[176:179]\n"
"v_mfma_f32_16x16x32_bf16 v[176:179], a[180:183], v[104:107], v[176:179]\n"
"v_mfma_f32_16x16x32_bf16 v[176:179], a[176:179], v[164:167], v[176:179]\n"
"v_mfma_f32_16x16x32_bf16 v[176:179], a[172:175], v[100:103], v[176:179]\n"
"v_mfma_f32_16x16x32_bf16 v[176:179], a[168:171], v[160:163], v[176:179]\n"
"s_nop 7\n"
"s_branch .Lmk_p107r\n"
".Lmk_p108s:\n"
";\n"
"s_waitcnt vmcnt(16)\n"
"s_mov_b32 s1, 0x10000\n"
".Lmk_p108l:\n"
"s_add_u32 s97, s58, 0x40000\n"
"buffer_load_dwordx4 v[148:151], v0, s[4:7], s97 offen sc1\n"
"buffer_load_dwordx4 v[144:147], v0, s[4:7], s97 offen offset:1024 sc1\n"
"buffer_load_dwordx4 v[116:119], v0, s[4:7], s97 offen offset:2048 sc1\n"
"buffer_load_dwordx4 v[112:115], v0, s[4:7], s97 offen offset:3072 sc1\n"
"buffer_load_dwordx4 v[92:95], v1, s[4:7], s97 offen sc1\n"
"buffer_load_dwordx4 v[96:99], v1, s[4:7], s97 offen offset:1024 sc1\n"
"buffer_load_dwordx4 v[156:159], v1, s[4:7], s97 offen offset:2048 sc1\n"
"buffer_load_dwordx4 v[88:91], v1, s[4:7], s97 offen offset:3072 sc1\n"
"s_add_u32 s97, s59, 0x40000\n"
"buffer_load_dwordx4 v[172:175], v0, s[4:7], s97 offen sc1\n"
"buffer_load_dwordx4 v[124:127], v0, s[4:7], s97 offen offset:1024 sc1\n"
"buffer_load_dwordx4 v[120:123], v0, s[4:7], s97 offen offset:2048 sc1\n"
"buffer_load_dwordx4 v[136:139], v0, s[4:7], s97 offen offset:3072 sc1\n"
"buffer_load_dwordx4 v[152:155], v1, s[4:7], s97 offen sc1\n"
"buffer_load_dwordx4 v[168:171], v1, s[4:7], s97 offen offset:1024 sc1\n"
"buffer_load_dwordx4 v[180:183], v1, s[4:7], s97 offen offset:2048 sc1\n"
"buffer_load_dwordx4 v[140:143], v1, s[4:7], s97 offen offset:3072 sc1\n"
"s_waitcnt vmcnt(0)\n"
"v_max_u32_e32 v239, v148, v149\n"
"v_max3_u32 v239, v239, v150, v151\n"
"v_max3_u32 v239, v239, v144, v145\n"
"v_max3_u32 v239, v239, v146, v147\n"
"v_max3_u32 v239, v239, v116, v117\n"
"v_max3_u32 v239, v239, v118, v119\n"
"v_max3_u32 v239, v239, v112, v113\n"
"v_max3_u32 v239, v239, v114, v115\n"
"v_max3_u32 v239, v239, v92, v93\n"
"v_max3_u32 v239, v239, v94, v95\n"
"v_max3_u32 v239, v239, v96, v97\n"
"v_max3_u32 v239, v239, v98, v99\n"
"v_max3_u32 v239, v239, v156, v157\n"
"v_max3_u32 v239, v239, v158, v159\n"
"v_max3_u32 v239, v239, v88, v89\n"
"v_max3_u32 v239, v239, v90, v91\n"
"v_cmp_eq_u32_e32 vcc, -1, v239\n"
"s_cbranch_vccz .Lmk_p108k\n"
"s_sub_u32 s1, s1, 1\n"
"s_cbranch_scc0 .Lmk_p108l\n"
".Lmk_p108k:\n"
"v_mfma_f32_16x16x32_bf16 v[100:103], a[128:131], v[148:151], 0\n"
"v_mfma_f32_16x16x32_bf16 v[100:103], a[132:135], v[144:147], v[100:103]\n"
"v_mfma_f32_16x16x32_bf16 v[100:103], a[136:139], v[116:119], v[100:103]\n"
"v_mfma_f32_16x16x32_bf16 v[100:103], a[140:143], v[112:115], v[100:103]\n"
"v_mfma_f32_16x16x32_bf16 v[100:103], a[144:147], v[92:95], v[100:103]\n"
"v_mfma_f32_16x16x32_bf16 v[100:103], a[148:151], v[96:99], v[100:103]\n"
"v_mfma_f32_16x16x32_bf16 v[100:103], a[152:155], v[156:159], v[100:103]\n"
"v_mfma_f32_16x16x32_bf16 v[100:103], a[156:159], v[88:91], v[100:103]\n"
"v_mfma_f32_16x16x32_bf16 v[104:107], a[188:191], v[88:91], 0\n"
"v_mfma_f32_16x16x32_bf16 v[104:107], a[184:187], v[156:159], v[104:107]\n"
"v_mfma_f32_16x16x32_bf16 v[104:107], a[180:183], v[96:99], v[104:107]\n"
"v_mfma_f32_16x16x32_bf16 v[104:107], a[176:179], v[92:95], v[104:107]\n"
"v_mfma_f32_16x16x32_bf16 v[104:107], a[172:175], v[112:115], v[104:107]\n"
"v_mfma_f32_16x16x32_bf16 v[104:107], a[168:171], v[116:119], v[104:107]\n"
"s_nop 7\n"
"s_branch .Lmk_p108r\n"
".Lmk_p109s:\n"
";\n"
"s_waitcnt vmcnt(16)\n"
"s_mov_b32 s1, 0x10000\n"
".Lmk_p109l:\n"
"s_add_u32 s97, s59, 0x40000\n"
"buffer_load_dwordx4 v[172:175], v0, s[4:7], s97 offen sc1\n"
"buffer_load_dwordx4 v[124:127], v0, s[4:7], s97 offen offset:1024 sc1\n"
"buffer_load_dwordx4 v[120:123], v0, s[4:7], s97 offen offset:2048 sc1\n"
"buffer_load_dwordx4 v[136:139], v0, s[4:7], s97 offen offset:3072 sc1\n"
"buffer_load_dwordx4 v[152:155], v1, s[4:7], s97 offen sc1\n"
"buffer_load_dwordx4 v[168:171], v1, s[4:7], s97 offen offset:1024 sc1\n"
"buffer_load_dwordx4 v[180:183], v1, s[4:7], s97 offen offset:2048 sc1\n"
"buffer_load_dwordx4 v[140:143], v1, s[4:7], s97 offen offset:3072 sc1\n"
"s_add_u32 s97, s56, 0\n"
"buffer_load_dwordx4 v[100:103], v0, s[4:7], s97 offen sc1\n"
"buffer_load_dwordx4 v[176:179], v0, s[4:7], s97 offen offset:1024 sc1\n"
"buffer_load_dwordx4 v[104:107], v0, s[4:7], s97 offen offset:2048 sc1\n"
"buffer_load_dwordx4 v[88:91], v0, s[4:7], s97 offen offset:3072 sc1\n"
"buffer_load_dwordx4 v[92:95], v1, s[4:7], s97 offen sc1\n"
"buffer_load_dwordx4 v[116:119], v1, s[4:7], s97 offen offset:1024 sc1\n"
"buffer_load_dwordx4 v[132:135], v1, s[4:7], s97 offen offset:2048 sc1\n"
"buffer_load_dwordx4 v[144:147], v1, s[4:7], s97 offen offset:3072 sc1\n"
"s_add_u32 s97, s57, 0\n"
"buffer_load_dwordx4 v[156:159], v0, s[4:7], s97 offen sc1\n"
"s_waitcnt vmcnt(0)\n"
"v_max_u32_e32 v255, v172, v173\n"
"v_max3_u32 v255, v255, v174, v175\n"
"v_max3_u32 v255, v255, v124, v125\n"
"v_max3_u32 v255, v255, v126, v127\n"
"v_max3_u32 v255, v255, v120, v121\n"
"v_max3_u32 v255, v255, v122, v123\n"
"v_max3_u32 v255, v255, v136, v137\n"
"v_max3_u32 v255, v255, v138, v139\n"
"v_max3_u32 v255, v255, v152, v153\n"
"v_max3_u32 v255, v255, v154, v155\n"
"v_max3_u32 v255, v255, v168, v169\n"
"v_max3_u32 v255, v255, v170, v171\n"
"v_max3_u32 v255, v255, v180, v181\n"
"v_max3_u32 v255, v255, v182, v183\n"
"v_max3_u32 v255, v255, v140, v141\n"
"v_max3_u32 v255, v255, v142, v143\n"
"v_cmp_eq_u32_e32 vcc, -1, v255\n"
"s_cbranch_vccz .Lmk_p109k\n"
"s_sub_u32 s1, s1, 1\n"
"s_cbranch_scc0 .Lmk_p109l\n"
".Lmk_p109k:\n"
"v_mfma_f32_16x16x32_bf16 v[128:131], a[128:131], v[172:175], v[108:111]\n"
"v_mfma_f32_16x16x32_bf16 v[128:131], a[132:135], v[124:127], v[128:131]\n"
"v_mfma_f32_16x16x32_bf16 v[128:131], a[136:139], v[120:123], v[128:131]\n"
"v_mfma_f32_16x16x32_bf16 v[128:131], a[140:143], v[136:139], v[128:131]\n"
"v_mfma_f32_16x16x32_bf16 v[128:131], a[144:147], v[152:155], v[128:131]\n"
"v_mfma_f32_16x16x32_bf16 v[128:131], a[148:151], v[168:171], v[128:131]\n"
"v_mfma_f32_16x16x32_bf16 v[128:131], a[152:155], v[180:183], v[128:131]\n"
"v_mfma_f32_16x16x32_bf16 v[128:131], a[156:159], v[140:143], v[128:131]\n"
"v_mfma_f32_16x16x32_bf16 v[148:151], a[188:191], v[140:143], v[160:163]\n"
"v_mfma_f32_16x16x32_bf16 v[148:151], a[184:187], v[180:183], v[148:151]\n"
"v_mfma_f32_16x16x32_bf16 v[148:151], a[180:183], v[168:171], v[148:151]\n"
"v_mfma_f32_16x16x32_bf16 v[148:151], a[176:179], v[152:155], v[148:151]\n"
"v_mfma_f32_16x16x32_bf16 v[148:151], a[172:175], v[136:139], v[148:151]\n"
"v_mfma_f32_16x16x32_bf16 v[148:151], a[168:171], v[120:123], v[148:151]\n"
"s_nop 7\n"
"s_branch .Lmk_p109r\n"
".Lmk_p110s:\n"
";\n"
"s_waitcnt vmcnt(16)\n"
"s_mov_b32 s1, 0x10000\n"
".Lmk_p110l:\n"
"s_add_u32 s95, s56, 0\n"
"buffer_load_dwordx4 v[100:103], v0, s[4:7], s95 offen sc1\n"
"buffer_load_dwordx4 v[176:179], v0, s[4:7], s95 offen offset:1024 sc1\n"
"buffer_load_dwordx4 v[104:107], v0, s[4:7], s95 offen offset:2048 sc1\n"
"buffer_load_dwordx4 v[88:91], v0, s[4:7], s95 offen offset:3072 sc1\n"
"buffer_load_dwordx4 v[92:95], v1, s[4:7], s95 offen sc1\n"
"buffer_load_dwordx4 v[116:119], v1, s[4:7], s95 offen offset:1024 sc1\n"
"buffer_load_dwordx4 v[132:135], v1, s[4:7], s95 offen offset:2048 sc1\n"
"buffer_load_dwordx4 v[144:147], v1, s[4:7], s95 offen offset:3072 sc1\n"
"s_add_u32 s95, s57, 0\n"
"buffer_load_dwordx4 v[156:159], v0, s[4:7], s95 offen sc1\n"
"buffer_load_dwordx4 v[108:111], v0, s[4:7], s95 offen offset:1024 sc1\n"
"buffer_load_dwordx4 v[112:115], v0, s[4:7], s95 offen offset:2048 sc1\n"
"buffer_load_dwordx4 v[196:199], v0, s[4:7], s95 offen offset:3072 sc1\n"
"buffer_load_dwordx4 v[200:203], v1, s[4:7], s95 offen sc1\n"
"buffer_load_dwordx4 v[128:131], v1, s[4:7], s95 offen offset:1024 sc1\n"
"buffer_load_dwordx4 v[120:123], v1, s[4:7], s95 offen offset:2048 sc1\n"
"buffer_load_dwordx4 v[136:139], v1, s[4:7], s95 offen offset:3072 sc1\n"
"s_add_u32 s95, s58, 0\n"
"buffer_load_dwordx4 v[148:151], v0, s[4:7], s95 offen sc1\n"
"buffer_load_dwordx4 v[160:163], v0, s[4:7], s95 offen offset:1024 sc1\n"
"buffer_load_dwordx4 v[124:127], v0, s[4:7], s95 offen offset:2048 sc1\n"
"s_waitcnt vmcnt(0)\n"
"v_max_u32_e32 v255, v100, v101\n"
"v_max3_u32 v255, v255, v102, v103\n"
"v_max3_u32 v255, v255, v176, v177\n"
"v_max3_u32 v255, v255, v178, v179\n"
"v_max3_u32 v255, v255, v104, v105\n"
"v_max3_u32 v255, v255, v106, v107\n"
"v_max3_u32 v255, v255, v88, v89\n"
"v_max3_u32 v255, v255, v90, v91\n"
"v_max3_u32 v255, v255, v92, v93\n"
"v_max3_u32 v255, v255, v94, v95\n"
"v_max3_u32 v255, v255, v116, v117\n"
"v_max3_u32 v255, v255, v118, v119\n"
"v_max3_u32 v255, v255, v132, v133\n"
"v_max3_u32 v255, v255, v134, v135\n"
"v_max3_u32 v255, v255, v144, v145\n"
"v_max3_u32 v255, v255, v146, v147\n"
"v_cmp_eq_u32_e32 vcc, -1, v255\n"
"s_cbranch_vccz .Lmk_p110k\n"
"s_sub_u32 s1, s1, 1\n"
"s_cbranch_scc0 .Lmk_p110l\n"
".Lmk_p110k:\n"
"v_mfma_f32_16x16x32_bf16 v[140:143], a[128:131], v[100:103], 0\n"
"v_mfma_f32_16x16x32_bf16 v[140:143], a[132:135], v[176:179], v[140:143]\n"
"v_mfma_f32_16x16x32_bf16 v[140:143], a[136:139], v[104:107], v[140:143]\n"
"v_mfma_f32_16x16x32_bf16 v[140:143], a[140:143], v[88:91], v[140:143]\n"
"v_mfma_f32_16x16x32_bf16 v[140:143], a[144:147], v[92:95], v[140:143]\n"
"v_mfma_f32_16x16x32_bf16 v[140:143], a[148:151], v[116:119], v[140:143]\n"
"v_mfma_f32_16x16x32_bf16 v[140:143], a[152:155], v[132:135], v[140:143]\n"
"v_mfma_f32_16x16x32_bf16 v[140:143], a[156:159], v[144:147], v[140:143]\n"
"v_mfma_f32_16x16x32_bf16 v[96:99], a[188:191], v[144:147], 0\n"
"v_mfma_f32_16x16x32_bf16 v[96:99], a[184:187], v[132:135], v[96:99]\n"
"v_mfma_f32_16x16x32_bf16 v[96:99], a[180:183], v[116:119], v[96:99]\n"
"v_mfma_f32_16x16x32_bf16 v[96:99], a[176:179], v[92:95], v[96:99]\n"
"v_mfma_f32_16x16x32_bf16 v[96:99], a[172:175], v[88:91], v[96:99]\n"
"v_mfma_f32_16x16x32_bf16 v[96:99], a[168:171], v[104:107], v[96:99]\n"
"s_nop 7\n"
"s_branch .Lmk_p110r\n"
".Lmk_p111s:\n"
";\n"
"s_waitcnt vmcnt(16)\n"
"s_mov_b32 s1, 0x10000\n"
".Lmk_p111l:\n"
"s_add_u32 s97, s57, 0\n"
"buffer_load_dwordx4 v[156:159], v0, s[4:7], s97 offen sc1\n"
"buffer_load_dwordx4 v[108:111], v0, s[4:7], s97 offen offset:1024 sc1\n"
"buffer_load_dwordx4 v[112:115], v0, s[4:7], s97 offen offset:2048 sc1\n"
"buffer_load_dwordx4 v[196:199], v0, s[4:7], s97 offen offset:3072 sc1\n"
"buffer_load_dwordx4 v[200:203], v1, s[4:7], s97 offen sc1\n"
"buffer_load_dwordx4 v[128:131], v1, s[4:7], s97 offen offset:1024 sc1\n"
"buffer_load_dwordx4 v[120:123], v1, s[4:7], s97 offen offset:2048 sc1\n"
"buffer_load_dwordx4 v[136:139], v1, s[4:7], s97 offen offset:3072 sc1\n"
"s_add_u32 s97, s58, 0\n"
"buffer_load_dwordx4 v[148:151], v0, s[4:7], s97 offen sc1\n"
"buffer_load_dwordx4 v[160:163], v0, s[4:7], s97 offen offset:1024 sc1\n"
"buffer_load_dwordx4 v[124:127], v0, s[4:7], s97 offen offset:2048 sc1\n"
"buffer_load_dwordx4 v[140:143], v0, s[4:7], s97 offen offset:3072 sc1\n"
"buffer_load_dwordx4 v[152:155], v1, s[4:7], s97 offen sc1\n"
"buffer_load_dwordx4 v[172:175], v1, s[4:7], s97 offen offset:1024 sc1\n"
"buffer_load_dwordx4 v[116:119], v1, s[4:7], s97 offen offset:2048 sc1\n"
"buffer_load_dwordx4 v[132:135], v1, s[4:7], s97 offen offset:3072 sc1\n"
"s_add_u32 s97, s59, 0\n"
"buffer_load_dwordx4 v[176:179], v0, s[4:7], s97 offen sc1\n"
"buffer_load_dwordx4 v[180:183], v0, s[4:7], s97 offen offset:1024 sc1\n"
"buffer_load_dwordx4 v[168:171], v0, s[4:7], s97 offen offset:2048 sc1\n"
"s_waitcnt vmcnt(0)\n"
"v_max_u32_e32 v255, v156, v157\n"
"v_max3_u32 v255, v255, v158, v159\n"
"v_max3_u32 v255, v255, v108, v109\n"
"v_max3_u32 v255, v255, v110, v111\n"
"v_max3_u32 v255, v255, v112, v113\n"
"v_max3_u32 v255, v255, v114, v115\n"
"v_max3_u32 v255, v255, v196, v197\n"
"v_max3_u32 v255, v255, v198, v199\n"
"v_max3_u32 v255, v255, v200, v201\n"
"v_max3_u32 v255, v255, v202, v203\n"
"v_max3_u32 v255, v255, v128, v129\n"
"v_max3_u32 v255, v255, v130, v131\n"
"v_max3_u32 v255, v255, v120, v121\n"
"v_max3_u32 v255, v255, v122, v123\n"
"v_max3_u32 v255, v255, v136, v137\n"
"v_max3_u32 v255, v255, v138, v139\n"
"v_cmp_eq_u32_e32 vcc, -1, v255\n"
"s_cbranch_vccz .Lmk_p111k\n"
"s_sub_u32 s1, s1, 1\n"
"s_cbranch_scc0 .Lmk_p111l\n"
".Lmk_p111k:\n"
"v_mfma_f32_16x16x32_bf16 v[88:91], a[128:131], v[156:159], 0\n"
"v_mfma_f32_16x16x32_bf16 v[88:91], a[132:135], v[108:111], v[88:91]\n"
"v_mfma_f32_16x16x32_bf16 v[88:91], a[136:139], v[112:115], v[88:91]\n"
"v_mfma_f32_16x16x32_bf16 v[88:91], a[140:143], v[196:199], v[88:91]\n"
"v_mfma_f32_16x16x32_bf16 v[88:91], a[144:147], v[200:203], v[88:91]\n"
"v_mfma_f32_16x16x32_bf16 v[88:91], a[148:151], v[128:131], v[88:91]\n"
"v_mfma_f32_16x16x32_bf16 v[88:91], a[152:155], v[120:123], v[88:91]\n"
"v_mfma_f32_16x16x32_bf16 v[88:91], a[156:159], v[136:139], v[88:91]\n"
"v_mfma_f32_16x16x32_bf16 v[92:95], a[188:191], v[136:139], 0\n"
"v_mfma_f32_16x16x32_bf16 v[92:95], a[184:187], v[120:123], v[92:95]\n"
"v_mfma_f32_16x16x32_bf16 v[92:95], a[180:183], v[128:131], v[92:95]\n"
"v_mfma_f32_16x16x32_bf16 v[92:95], a[176:179], v[200:203], v[92:95]\n"
"v_mfma_f32_16x16x32_bf16 v[92:95], a[172:175], v[196:199], v[92:95]\n"
"v_mfma_f32_16x16x32_bf16 v[92:95], a[168:171], v[112:115], v[92:95]\n"
"s_nop 7\n"
"s_branch .Lmk_p111r\n"
".Lmk_p112s:\n"
";\n"
"s_waitcnt vmcnt(16)\n"
"s_mov_b32 s1, 0x10000\n"
".Lmk_p112l:\n"
"s_add_u32 s97, s58, 0\n"
"buffer_load_dwordx4 v[148:151], v0, s[4:7], s97 offen sc1\n"
"buffer_load_dwordx4 v[160:163], v0, s[4:7], s97 offen offset:1024 sc1\n"
"buffer_load_dwordx4 v[124:127], v0, s[4:7], s97 offen offset:2048 sc1\n"
"buffer_load_dwordx4 v[140:143], v0, s[4:7], s97 offen offset:3072 sc1\n"
"buffer_load_dwordx4 v[152:155], v1, s[4:7], s97 offen sc1\n"
"buffer_load_dwordx4 v[172:175], v1, s[4:7], s97 offen offset:1024 sc1\n"
"buffer_load_dwordx4 v[116:119], v1, s[4:7], s97 offen offset:2048 sc1\n"
"buffer_load_dwordx4 v[132:135], v1, s[4:7], s97 offen offset:3072 sc1\n"
"s_add_u32 s97, s59, 0\n"
"buffer_load_dwordx4 v[176:179], v0, s[4:7], s97 offen sc1\n"
"buffer_load_dwordx4 v[180:183], v0, s[4:7], s97 offen offset:1024 sc1\n"
"buffer_load_dwordx4 v[168:171], v0, s[4:7], s97 offen offset:2048 sc1\n"
"buffer_load_dwordx4 v[184:187], v0, s[4:7], s97 offen offset:3072 sc1\n"
"buffer_load_dwordx4 v[188:191], v1, s[4:7], s97 offen sc1\n"
"buffer_load_dwordx4 v[164:167], v1, s[4:7], s97 offen offset:1024 sc1\n"
"buffer_load_dwordx4 v[192:195], v1, s[4:7], s97 offen offset:2048 sc1\n"
"buffer_load_dwordx4 v[196:199], v1, s[4:7], s97 offen offset:3072 sc1\n"
"s_add_u32 s97, s52, 0x440000\n"
"buffer_load_dwordx4 v[88:91], v0, s[4:7], s97 offen sc1\n"
"buffer_load_dwordx4 v[92:95], v0, s[4:7], s97 offen offset:1024 sc1\n"
"s_waitcnt vmcnt(0)\n"
"v_max_u32_e32 v239, v148, v149\n"
"v_max3_u32 v239, v239, v150, v151\n"
"v_max3_u32 v239, v239, v160, v161\n"
"v_max3_u32 v239, v239, v162, v163\n"
"v_max3_u32 v239, v239, v124, v125\n"
"v_max3_u32 v239, v239, v126, v127\n"
"v_max3_u32 v239, v239, v140, v141\n"
"v_max3_u32 v239, v239, v142, v143\n"
"v_max3_u32 v239, v239, v152, v153\n"
"v_max3_u32 v239, v239, v154, v155\n"
"v_max3_u32 v239, v239, v172, v173\n"
"v_max3_u32 v239, v239, v174, v175\n"
"v_max3_u32 v239, v239, v116, v117\n"
"v_max3_u32 v239, v239, v118, v119\n"
"v_max3_u32 v239, v239, v132, v133\n"
"v_max3_u32 v239, v239, v134, v135\n"
"v_cmp_eq_u32_e32 vcc, -1, v239\n"
"s_cbranch_vccz .Lmk_p112k\n"
"s_sub_u32 s1, s1, 1\n"
"s_cbranch_scc0 .Lmk_p112l\n"
".Lmk_p112k:\n"
"v_mfma_f32_16x16x32_bf16 v[96:99], a[128:131], v[148:151], 0\n"
"v_mfma_f32_16x16x32_bf16 v[96:99], a[132:135], v[160:163], v[96:99]\n"
"v_mfma_f32_16x16x32_bf16 v[96:99], a[136:139], v[124:127], v[96:99]\n"
"v_mfma_f32_16x16x32_bf16 v[96:99], a[140:143], v[140:143], v[96:99]\n"
"v_mfma_f32_16x16x32_bf16 v[96:99], a[144:147], v[152:155], v[96:99]\n"
"v_mfma_f32_16x16x32_bf16 v[96:99], a[148:151], v[172:175], v[96:99]\n"
"v_mfma_f32_16x16x32_bf16 v[96:99], a[152:155], v[116:119], v[96:99]\n"
"v_mfma_f32_16x16x32_bf16 v[96:99], a[156:159], v[132:135], v[96:99]\n"
"v_mfma_f32_16x16x32_bf16 v[104:107], a[188:191], v[132:135], 0\n"
"v_mfma_f32_16x16x32_bf16 v[104:107], a[184:187], v[116:119], v[104:107]\n"
"v_mfma_f32_16x16x32_bf16 v[104:107], a[180:183], v[172:175], v[104:107]\n"
"v_mfma_f32_16x16x32_bf16 v[104:107], a[176:179], v[152:155], v[104:107]\n"
"v_mfma_f32_16x16x32_bf16 v[104:107], a[172:175], v[140:143], v[104:107]\n"
"v_mfma_f32_16x16x32_bf16 v[104:107], a[168:171], v[124:127], v[104:107]\n"
"s_nop 7\n"
"s_branch .Lmk_p112r\n"
".Lmk_p113s:\n"
";\n"
"s_waitcnt vmcnt(16)\n"
"s_mov_b32 s1, 0x10000\n"
".Lmk_p113l:\n"
"s_add_u32 s97, s59, 0\n"
"buffer_load_dwordx4 v[176:179], v0, s[4:7], s97 offen sc1\n"
"buffer_load_dwordx4 v[180:183], v0, s[4:7], s97 offen offset:1024 sc1\n"
"buffer_load_dwordx4 v[168:171], v0, s[4:7], s97 offen offset:2048 sc1\n"
"buffer_load_dwordx4 v[184:187], v0, s[4:7], s97 offen offset:3072 sc1\n"
"buffer_load_dwordx4 v[188:191], v1, s[4:7], s97 offen sc1\n"
"buffer_load_dwordx4 v[164:167], v1, s[4:7], s97 offen offset:1024 sc1\n"
"buffer_load_dwordx4 v[192:195], v1, s[4:7], s97 offen offset:2048 sc1\n"
"buffer_load_dwordx4 v[196:199], v1, s[4:7], s97 offen offset:3072 sc1\n"
"s_add_u32 s97, s52, 0x440000\n"
"buffer_load_dwordx4 v[88:91], v0, s[4:7], s97 offen sc1\n"
"buffer_load_dwordx4 v[92:95], v0, s[4:7], s97 offen offset:1024 sc1\n"
"buffer_load_dwordx4 v[96:99], v0, s[4:7], s97 offen offset:2048 sc1\n"
"buffer_load_dwordx4 v[100:103], v0, s[4:7], s97 offen offset:3072 sc1\n"
"buffer_load_dwordx4 v[104:107], v1, s[4:7], s97 offen sc1\n"
"buffer_load_dwordx4 v[112:115], v1, s[4:7], s97 offen offset:1024 sc1\n"
"buffer_load_dwordx4 v[116:119], v1, s[4:7], s97 offen offset:2048 sc1\n"
"buffer_load_dwordx4 v[124:127], v1, s[4:7], s97 offen offset:3072 sc1\n"
"s_add_u32 s97, s53, 0x440000\n"
"buffer_load_dwordx4 v[128:131], v0, s[4:7], s97 offen sc1\n"
"buffer_load_dwordx4 v[132:135], v0, s[4:7], s97 offen offset:1024 sc1\n"
"buffer_load_dwordx4 v[136:139], v0, s[4:7], s97 offen offset:2048 sc1\n"
"s_waitcnt vmcnt(0)\n"
"v_max_u32_e32 v255, v176, v177\n"
"v_max3_u32 v255, v255, v178, v179\n"
"v_max3_u32 v255, v255, v180, v181\n"
"v_max3_u32 v255, v255, v182, v183\n"
"v_max3_u32 v255, v255, v168, v169\n"
"v_max3_u32 v255, v255, v170, v171\n"
"v_max3_u32 v255, v255, v184, v185\n"
"v_max3_u32 v255, v255, v186, v187\n"
"v_max3_u32 v255, v255, v188, v189\n"
"v_max3_u32 v255, v255, v190, v191\n"
"v_max3_u32 v255, v255, v164, v165\n"
"v_max3_u32 v255, v255, v166, v167\n"
"v_max3_u32 v255, v255, v192, v193\n"
"v_max3_u32 v255, v255, v194, v195\n"
"v_max3_u32 v255, v255, v196, v197\n"
"v_max3_u32 v255, v255, v198, v199\n"
"v_cmp_eq_u32_e32 vcc, -1, v255\n"
"s_cbranch_vccz .Lmk_p113k\n"
"s_sub_u32 s1, s1, 1\n"
"s_cbranch_scc0 .Lmk_p113l\n"
".Lmk_p113k:\n"
"v_mfma_f32_16x16x32_bf16 v[172:175], a[128:131], v[176:179], v[144:147]\n"
"v_mfma_f32_16x16x32_bf16 v[172:175], a[132:135], v[180:183], v[172:175]\n"
"v_mfma_f32_16x16x32_bf16 v[172:175], a[136:139], v[168:171], v[172:175]\n"
"v_mfma_f32_16x16x32_bf16 v[172:175], a[140:143], v[184:187], v[172:175]\n"
"v_mfma_f32_16x16x32_bf16 v[172:175], a[144:147], v[188:191], v[172:175]\n"
"v_mfma_f32_16x16x32_bf16 v[172:175], a[148:151], v[164:167], v[172:175]\n"
"v_mfma_f32_16x16x32_bf16 v[172:175], a[152:155], v[192:195], v[172:175]\n"
"v_mfma_f32_16x16x32_bf16 v[172:175], a[156:159], v[196:199], v[172:175]\n"
"v_mfma_f32_16x16x32_bf16 v[200:203], a[188:191], v[196:199], v[156:159]\n"
"v_mfma_f32_16x16x32_bf16 v[200:203], a[184:187], v[192:195], v[200:203]\n"
"v_mfma_f32_16x16x32_bf16 v[200:203], a[180:183], v[164:167], v[200:203]\n"
"v_mfma_f32_16x16x32_bf16 v[200:203], a[176:179], v[188:191], v[200:203]\n"
"v_mfma_f32_16x16x32_bf16 v[200:203], a[172:175], v[184:187], v[200:203]\n"
"v_mfma_f32_16x16x32_bf16 v[200:203], a[168:171], v[168:171], v[200:203]\n"
"s_nop 7\n"
"s_branch .Lmk_p113r\n"
".Lmk_exit:\n"
"s_waitcnt vmcnt(23)\n"
"; @B G2 b.0\n"
"v_mfma_f32_16x16x32_bf16 v[36:39], a[128:131], v[120:123], 0\n"
"s_add_u32 s97, s58, 0x440000\n"
"buffer_load_dwordx4 v[40:43], v0, s[4:7], s97 offen sc1\n"
"v_mfma_f32_16x16x32_bf16 v[36:39], a[132:135], v[136:139], v[36:39]\n"
"s_waitcnt vmcnt(22)\n"
"v_mfma_f32_16x16x32_bf16 v[36:39], a[136:139], v[144:147], v[36:39]\n"
"v_mfma_f32_16x16x32_bf16 v[36:39], a[140:143], v[192:195], v[36:39]\n"
"s_waitcnt vmcnt(20)\n"
"v_mfma_f32_16x16x32_bf16 v[36:39], a[144:147], v[196:199], v[36:39]\n"
"buffer_load_dwordx4 v[52:55], v0, s[4:7], s97 offen offset:1024 sc1\n"
"v_mfma_f32_16x16x32_bf16 v[36:39], a[148:151], v[188:191], v[36:39]\n"
"s_waitcnt vmcnt(19)\n"
"v_mfma_f32_16x16x32_bf16 v[36:39], a[152:155], v[108:111], v[36:39]\n"
"v_mfma_f32_16x16x32_bf16 v[36:39], a[156:159], v[92:95], v[36:39]\n"
"v_mfma_f32_16x16x32_bf16 v[60:63], a[188:191], v[92:95], 0\n"
"buffer_load_dwordx4 v[64:67], v0, s[4:7], s97 offen offset:2048 sc1\n"
"v_mfma_f32_16x16x32_bf16 v[60:63], a[184:187], v[108:111], v[60:63]\n"
"v_mfma_f32_16x16x32_bf16 v[60:63], a[180:183], v[188:191], v[60:63]\n"
"v_mfma_f32_16x16x32_bf16 v[60:63], a[176:179], v[196:199], v[60:63]\n"
"v_mfma_f32_16x16x32_bf16 v[60:63], a[172:175], v[192:195], v[60:63]\n"
"s_nop 1\n"
"v_cmp_u_f32_e32 vcc, v36, v36\n"
"v_mfma_f32_16x16x32_bf16 v[60:63], a[168:171], v[144:147], v[60:63]\n"
"s_cbranch_vccnz .Lmk_p55s\n"
".Lmk_p55r:\n"
"v_mfma_f32_16x16x32_bf16 v[60:63], a[164:167], v[136:139], v[60:63]\n"
"ds_write_b128 v3, v[36:39]\n"
"v_mfma_f32_16x16x32_bf16 v[60:63], a[160:163], v[120:123], v[60:63]\n"
"buffer_load_dwordx4 v[36:39], v0, s[4:7], s97 offen offset:3072 sc1\n"
"v_mfma_f32_16x16x32_bf16 v[68:71], a[192:195], v[120:123], 0\n"
"v_bfe_u32 v255, v0, 4, 4\n"
"v_bfe_u32 v254, v0, 9, 1\n"
"v_mfma_f32_16x16x32_bf16 v[68:71], a[196:199], v[136:139], v[68:71]\n"
"v_and_b32_e32 v253, 7, v255\n"
"v_xor_b32_e32 v254, v254, v253\n"
"v_mfma_f32_16x16x32_bf16 v[68:71], a[200:203], v[144:147], v[68:71]\n"
"v_lshlrev_b32_e32 v255, 7, v255\n"
"v_lshl_add_u32 v255, v254, 4, v255\n"
"v_mfma_f32_16x16x32_bf16 v[68:71], a[204:207], v[192:195], v[68:71]\n"
"buffer_load_dwordx4 v[72:75], v1, s[4:7], s97 offen sc1\n"
"v_mfma_f32_16x16x32_bf16 v[68:71], a[208:211], v[196:199], v[68:71]\n"
"v_bfe_u32 v254, v0, 8, 1\n"
"v_lshl_add_u32 v255, v254, 3, v255\n"
"v_mfma_f32_16x16x32_bf16 v[68:71], a[212:215], v[188:191], v[68:71]\n"
"ds_write_b128 v3, v[60:63] offset:1024\n"
"v_mfma_f32_16x16x32_bf16 v[68:71], a[216:219], v[108:111], v[68:71]\n"
"v_add_u32_e32 v255, s77, v255\n"
"v_bfe_u32 v253, v0, 7, 3\n"
"v_mfma_f32_16x16x32_bf16 v[68:71], a[220:223], v[92:95], v[68:71]\n"
"buffer_load_dwordx4 v[60:63], v1, s[4:7], s97 offen offset:1024 sc1\n"
"v_mfma_f32_16x16x32_bf16 v[76:79], a[252:255], v[92:95], 0\n"
"v_bfe_u32 v254, v0, 4, 3\n"
"v_xor_b32_e32 v254, v254, v253\n"
"v_mfma_f32_16x16x32_bf16 v[76:79], a[248:251], v[108:111], v[76:79]\n"
"v_lshlrev_b32_e32 v253, 11, v253\n"
"v_lshl_add_u32 v253, v254, 4, v253\n"
"v_mfma_f32_16x16x32_bf16 v[76:79], a[244:247], v[188:191], v[76:79]\n"
"v_mfma_f32_16x16x32_bf16 v[76:79], a[240:243], v[196:199], v[76:79]\n"
"buffer_load_dwordx4 v[80:83], v1, s[4:7], s97 offen offset:2048 sc1\n"
"v_mfma_f32_16x16x32_bf16 v[76:79], a[236:239], v[192:195], v[76:79]\n"
"v_mfma_f32_16x16x32_bf16 v[76:79], a[232:235], v[144:147], v[76:79]\n"
"ds_write_b128 v3, v[68:71] offset:2048\n"
"v_mfma_f32_16x16x32_bf16 v[76:79], a[228:231], v[136:139], v[76:79]\n"
"v_mfma_f32_16x16x32_bf16 v[76:79], a[224:227], v[120:123], v[76:79]\n"
"s_waitcnt vmcnt(20)\n"
"buffer_load_dwordx4 v[68:71], v1, s[4:7], s97 offen offset:3072 sc1\n"
"; @B G2 b.1\n"
"v_mfma_f32_16x16x32_bf16 v[84:87], a[128:131], v[148:151], 0\n"
"v_mfma_f32_16x16x32_bf16 v[84:87], a[132:135], v[180:183], v[84:87]\n"
"s_add_u32 s97, s59, 0x440000\n"
"v_mfma_f32_16x16x32_bf16 v[84:87], a[136:139], v[24:27], v[84:87]\n"
"v_mfma_f32_16x16x32_bf16 v[84:87], a[140:143], v[32:35], v[84:87]\n"
"s_waitcnt vmcnt(15)\n"
"buffer_load_dwordx4 v[88:91], v0, s[4:7], s97 offen sc1\n"
"v_mfma_f32_16x16x32_bf16 v[84:87], a[144:147], v[44:47], v[84:87]\n"
"v_mfma_f32_16x16x32_bf16 v[84:87], a[148:151], v[48:51], v[84:87]\n"
"ds_write_b128 v3, v[76:79] offset:3072\n"
"v_mfma_f32_16x16x32_bf16 v[84:87], a[152:155], v[28:31], v[84:87]\n"
"v_mfma_f32_16x16x32_bf16 v[84:87], a[156:159], v[56:59], v[84:87]\n"
"buffer_load_dwordx4 v[76:79], v0, s[4:7], s97 offen offset:1024 sc1\n"
"v_mfma_f32_16x16x32_bf16 v[96:99], a[188:191], v[56:59], 0\n"
"v_mfma_f32_16x16x32_bf16 v[96:99], a[184:187], v[28:31], v[96:99]\n"
"v_mfma_f32_16x16x32_bf16 v[96:99], a[180:183], v[48:51], v[96:99]\n"
"v_mfma_f32_16x16x32_bf16 v[96:99], a[176:179], v[44:47], v[96:99]\n"
"buffer_load_dwordx4 v[100:103], v0, s[4:7], s97 offen offset:2048 sc1\n"
"v_mfma_f32_16x16x32_bf16 v[96:99], a[172:175], v[32:35], v[96:99]\n"
"s_nop 0\n"
"v_cmp_u_f32_e32 vcc, v84, v84\n"
"v_mfma_f32_16x16x32_bf16 v[96:99], a[168:171], v[24:27], v[96:99]\n"
"s_cbranch_vccnz .Lmk_p56s\n"
".Lmk_p56r:\n"
"v_mfma_f32_16x16x32_bf16 v[96:99], a[164:167], v[180:183], v[96:99]\n"
"ds_write_b128 v3, v[84:87] offset:6144\n"
"v_mfma_f32_16x16x32_bf16 v[96:99], a[160:163], v[148:151], v[96:99]\n"
"buffer_load_dwordx4 v[84:87], v0, s[4:7], s97 offen offset:3072 sc1\n"
"v_mfma_f32_16x16x32_bf16 v[104:107], a[192:195], v[148:151], 0\n"
"v_mfma_f32_16x16x32_bf16 v[104:107], a[196:199], v[180:183], v[104:107]\n"
"v_mfma_f32_16x16x32_bf16 v[104:107], a[200:203], v[24:27], v[104:107]\n"
"v_mfma_f32_16x16x32_bf16 v[104:107], a[204:207], v[32:35], v[104:107]\n"
"buffer_load_dwordx4 v[112:115], v1, s[4:7], s97 offen sc1\n"
"v_mfma_f32_16x16x32_bf16 v[104:107], a[208:211], v[44:47], v[104:107]\n"
"ds_read_b128 v[116:119], v2 offset:8192\n"
"ds_read_b128 v[124:127], v2 offset:9216\n"
"v_mfma_f32_16x16x32_bf16 v[104:107], a[212:215], v[48:51], v[104:107]\n"
"ds_write_b128 v3, v[96:99] offset:7168\n"
"v_mfma_f32_16x16x32_bf16 v[104:107], a[216:219], v[28:31], v[104:107]\n"
"v_mfma_f32_16x16x32_bf16 v[104:107], a[220:223], v[56:59], v[104:107]\n"
"buffer_load_dwordx4 v[96:99], v1, s[4:7], s97 offen offset:1024 sc1\n"
"v_mfma_f32_16x16x32_bf16 v[128:131], a[252:255], v[56:59], 0\n"
"v_mfma_f32_16x16x32_bf16 v[128:131], a[248:251], v[28:31], v[128:131]\n"
"s_waitcnt lgkmcnt(1)\n"
"v_lshlrev_b32_e32 v254, 16, v116\n"
"v_and_b32_e32 v252, 0xffff0000, v116\n"
"v_lshlrev_b32_e32 v251, 16, v117\n"
"v_mfma_f32_16x16x32_bf16 v[128:131], a[244:247], v[48:51], v[128:131]\n"
"v_and_b32_e32 v250, 0xffff0000, v117\n"
"v_lshlrev_b32_e32 v249, 16, v118\n"
"v_and_b32_e32 v248, 0xffff0000, v118\n"
"v_mfma_f32_16x16x32_bf16 v[128:131], a[240:243], v[44:47], v[128:131]\n"
"buffer_load_dwordx4 v[132:135], v1, s[4:7], s97 offen offset:2048 sc1\n"
"v_lshlrev_b32_e32 v247, 16, v119\n"
"v_mfma_f32_16x16x32_bf16 v[128:131], a[236:239], v[32:35], v[128:131]\n"
"v_and_b32_e32 v246, 0xffff0000, v119\n"
"v_lshlrev_b32_e32 v245, 16, v124\n"
"v_and_b32_e32 v244, 0xffff0000, v124\n"
"v_mfma_f32_16x16x32_bf16 v[128:131], a[232:235], v[24:27], v[128:131]\n"
"ds_write_b128 v3, v[104:107] offset:8192\n"
"v_mfma_f32_16x16x32_bf16 v[128:131], a[228:231], v[180:183], v[128:131]\n"
"v_lshlrev_b32_e32 v243, 16, v125\n"
"v_and_b32_e32 v242, 0xffff0000, v125\n"
"v_lshlrev_b32_e32 v241, 16, v126\n"
"v_mfma_f32_16x16x32_bf16 v[128:131], a[224:227], v[148:151], v[128:131]\n"
"s_waitcnt vmcnt(11)\n"
"buffer_load_dwordx4 v[104:107], v1, s[4:7], s97 offen offset:3072 sc1\n"
"; @B G2 b.2\n"
"v_mfma_f32_16x16x32_bf16 v[116:119], a[128:131], v[40:43], 0\n"
"v_and_b32_e32 v240, 0xffff0000, v126\n"
"v_lshlrev_b32_e32 v239, 16, v127\n"
"v_and_b32_e32 v238, 0xffff0000, v127\n"
"v_mfma_f32_16x16x32_bf16 v[116:119], a[132:135], v[52:55], v[116:119]\n"
"v_mfma_f32_16x16x32_bf16 v[116:119], a[136:139], v[64:67], v[116:119]\n"
"v_mfma_f32_16x16x32_bf16 v[116:119], a[140:143], v[36:39], v[116:119]\n"
"s_waitcnt vmcnt(8)\n"
"v_mfma_f32_16x16x32_bf16 v[116:119], a[144:147], v[72:75], v[116:119]\n"
"v_mfma_f32_16x16x32_bf16 v[116:119], a[148:151], v[60:63], v[116:119]\n"
"ds_write_b128 v3, v[128:131] offset:9216\n"
"v_mfma_f32_16x16x32_bf16 v[116:119], a[152:155], v[80:83], v[116:119]\n"
"v_mfma_f32_16x16x32_bf16 v[116:119], a[156:159], v[68:71], v[116:119]\n"
"v_mfma_f32_16x16x32_bf16 v[124:127], a[188:191], v[68:71], 0\n"
"v_mfma_f32_16x16x32_bf16 v[124:127], a[184:187], v[80:83], v[124:127]\n"
"s_waitcnt lgkmcnt(0)\n"
"s_barrier\n"
"v_mfma_f32_16x16x32_bf16 v[124:127], a[180:183], v[60:63], v[124:127]\n"
"ds_read_b128 v[128:131], v4\n"
"ds_read_b128 v[140:143], v5\n"
"v_mfma_f32_16x16x32_bf16 v[124:127], a[176:179], v[72:75], v[124:127]\n"
"v_mfma_f32_16x16x32_bf16 v[124:127], a[172:175], v[36:39], v[124:127]\n"
"s_add_u32 s97, s56, 0x400000\n"
"v_cmp_u_f32_e32 vcc, v116, v116\n"
"v_mfma_f32_16x16x32_bf16 v[124:127], a[168:171], v[64:67], v[124:127]\n"
"s_cbranch_vccnz .Lmk_p57s\n"
".Lmk_p57r:\n"
"v_mfma_f32_16x16x32_bf16 v[124:127], a[164:167], v[52:55], v[124:127]\n"
"ds_write_b128 v3, v[116:119] offset:12288\n"
"v_mfma_f32_16x16x32_bf16 v[124:127], a[160:163], v[40:43], v[124:127]\n"
"buffer_load_dwordx4 v[116:119], v0, s[4:7], s97 offen sc1\n"
"s_waitcnt lgkmcnt(1)\n"
"v_mfma_f32_16x16x32_bf16 v[152:155], a[192:195], v[40:43], 0\n"
"ds_read_b128 v[156:159], v4 offset:1024\n"
"ds_read_b128 v[160:163], v5 offset:1024\n"
"v_add_f32_e32 v128, v128, v254\n"
"v_mfma_f32_16x16x32_bf16 v[152:155], a[196:199], v[52:55], v[152:155]\n"
"v_add_f32_e32 v129, v129, v252\n"
"v_add_f32_e32 v130, v130, v251\n"
"v_add_f32_e32 v131, v131, v250\n"
"v_mfma_f32_16x16x32_bf16 v[152:155], a[200:203], v[64:67], v[152:155]\n"
"buffer_load_dwordx4 v[164:167], v0, s[4:7], s97 offen offset:1024 sc1\n"
"v_add_f32_e32 v128, v128, v140\n"
"v_mfma_f32_16x16x32_bf16 v[152:155], a[204:207], v[36:39], v[152:155]\n"
"v_add_f32_e32 v129, v129, v141\n"
"v_add_f32_e32 v130, v130, v142\n"
"v_add_f32_e32 v131, v131, v143\n"
"v_mfma_f32_16x16x32_bf16 v[152:155], a[208:211], v[72:75], v[152:155]\n"
"v_mfma_f32_16x16x32_bf16 v[152:155], a[212:215], v[60:63], v[152:155]\n"
"ds_write_b128 v3, v[124:127] offset:13312\n"
"v_mfma_f32_16x16x32_bf16 v[152:155], a[216:219], v[80:83], v[152:155]\n"
"buffer_load_dwordx4 v[124:127], v0, s[4:7], s97 offen offset:2048 sc1\n"
"s_waitcnt lgkmcnt(1)\n"
"v_mfma_f32_16x16x32_bf16 v[152:155], a[220:223], v[68:71], v[152:155]\n"
"ds_read_b128 v[140:143], v4 offset:2048\n"
"ds_read_b128 v[168:171], v5 offset:2048\n"
"v_add_f32_e32 v156, v156, v249\n"
"v_mfma_f32_16x16x32_bf16 v[172:175], a[252:255], v[68:71], 0\n"
"v_add_f32_e32 v157, v157, v248\n"
"v_add_f32_e32 v158, v158, v247\n"
"v_add_f32_e32 v159, v159, v246\n"
"v_mfma_f32_16x16x32_bf16 v[172:175], a[248:251], v[80:83], v[172:175]\n"
"buffer_load_dwordx4 v[68:71], v0, s[4:7], s97 offen offset:3072 sc1\n"
"v_add_f32_e32 v156, v156, v160\n"
"v_mfma_f32_16x16x32_bf16 v[172:175], a[244:247], v[60:63], v[172:175]\n"
"v_add_f32_e32 v157, v157, v161\n"
"v_add_f32_e32 v158, v158, v162\n"
"v_add_f32_e32 v159, v159, v163\n"
"v_mfma_f32_16x16x32_bf16 v[172:175], a[240:243], v[72:75], v[172:175]\n"
"v_mfma_f32_16x16x32_bf16 v[172:175], a[236:239], v[36:39], v[172:175]\n"
"buffer_load_dwordx4 v[36:39], v1, s[4:7], s97 offen sc1\n"
"s_waitcnt lgkmcnt(0)\n"
"v_mfma_f32_16x16x32_bf16 v[172:175], a[232:235], v[64:67], v[172:175]\n"
"ds_write_b128 v3, v[152:155] offset:14336\n"
"v_mfma_f32_16x16x32_bf16 v[172:175], a[228:231], v[52:55], v[172:175]\n"
"ds_read_b128 v[52:55], v4 offset:3072\n"
"ds_read_b128 v[60:63], v5 offset:3072\n"
"v_add_f32_e32 v140, v140, v245\n"
"v_mfma_f32_16x16x32_bf16 v[172:175], a[224:227], v[40:43], v[172:175]\n"
"s_waitcnt vmcnt(9)\n"
"buffer_load_dwordx4 v[40:43], v1, s[4:7], s97 offen offset:1024 sc1\n"
"; @B G2 b.3\n"
"v_mfma_f32_16x16x32_bf16 v[64:67], a[128:131], v[88:91], v[128:131]\n"
"v_add_f32_e32 v141, v141, v244\n"
"v_add_f32_e32 v142, v142, v243\n"
"v_add_f32_e32 v143, v143, v242\n"
"v_mfma_f32_16x16x32_bf16 v[64:67], a[132:135], v[76:79], v[64:67]\n"
"v_add_f32_e32 v140, v140, v168\n"
"v_add_f32_e32 v141, v141, v169\n"
"v_add_f32_e32 v142, v142, v170\n"
"v_mfma_f32_16x16x32_bf16 v[64:67], a[136:139], v[100:103], v[64:67]\n"
"buffer_load_dwordx4 v[72:75], v1, s[4:7], s97 offen offset:2048 sc1\n"
"v_add_f32_e32 v143, v143, v171\n"
"v_mfma_f32_16x16x32_bf16 v[64:67], a[140:143], v[84:87], v[64:67]\n"
"s_waitcnt vmcnt(7)\n"
"s_waitcnt lgkmcnt(0)\n"
"v_add_f32_e32 v52, v52, v241\n"
"v_add_f32_e32 v53, v53, v240\n"
"v_mfma_f32_16x16x32_bf16 v[64:67], a[144:147], v[112:115], v[64:67]\n"
"v_add_f32_e32 v54, v54, v239\n"
"v_add_f32_e32 v55, v55, v238\n"
"v_add_f32_e32 v52, v52, v60\n"
"v_mfma_f32_16x16x32_bf16 v[64:67], a[148:151], v[96:99], v[64:67]\n"
"ds_write_b128 v3, v[172:175] offset:15360\n"
"v_mfma_f32_16x16x32_bf16 v[64:67], a[152:155], v[132:135], v[64:67]\n"
"buffer_load_dwordx4 v[80:83], v1, s[4:7], s97 offen offset:3072 sc1\n"
"v_add_f32_e32 v53, v53, v61\n"
"v_mfma_f32_16x16x32_bf16 v[64:67], a[156:159], v[104:107], v[64:67]\n"
"s_add_u32 s97, s57, 0x400000\n"
"v_add_f32_e32 v54, v54, v62\n"
"v_add_f32_e32 v55, v55, v63\n"
"v_mfma_f32_16x16x32_bf16 v[60:63], a[188:191], v[104:107], v[156:159]\n"
"v_mfma_f32_16x16x32_bf16 v[60:63], a[184:187], v[132:135], v[60:63]\n"
"s_waitcnt lgkmcnt(0)\n"
"s_barrier\n"
"v_mfma_f32_16x16x32_bf16 v[60:63], a[180:183], v[96:99], v[60:63]\n"
"buffer_load_dwordx4 v[152:155], v0, s[4:7], s97 offen sc1\n"
"v_mfma_f32_16x16x32_bf16 v[60:63], a[176:179], v[112:115], v[60:63]\n"
"ds_read_b128 v[160:163], v6\n"
"ds_read_b128 v[168:171], v6 offset:1024\n"
"v_mfma_f32_16x16x32_bf16 v[60:63], a[172:175], v[84:87], v[60:63]\n"
"ds_read_b128 v[172:175], v6 offset:2048\n"
"v_cmp_u_f32_e32 vcc, v64, v64\n"
"v_mfma_f32_16x16x32_bf16 v[60:63], a[168:171], v[100:103], v[60:63]\n"
"s_cbranch_vccnz .Lmk_p58s\n"
".Lmk_p58r:\n"
"ds_read_b128 v[128:131], v6 offset:3072\n"
"v_mfma_f32_16x16x32_bf16 v[60:63], a[164:167], v[76:79], v[60:63]\n"
"buffer_load_dwordx4 v[156:159], v0, s[4:7], s97 offen offset:1024 sc1\n"
"v_mfma_f32_16x16x32_bf16 v[60:63], a[160:163], v[88:91], v[60:63]\n"
"v_mfma_f32_16x16x32_bf16 v[140:143], a[192:195], v[88:91], v[140:143]\n"
"v_mfma_f32_16x16x32_bf16 v[140:143], a[196:199], v[76:79], v[140:143]\n"
"buffer_load_dwordx4 v[176:179], v0, s[4:7], s97 offen offset:2048 sc1\n"
"v_mfma_f32_16x16x32_bf16 v[140:143], a[200:203], v[100:103], v[140:143]\n"
"v_mfma_f32_16x16x32_bf16 v[140:143], a[204:207], v[84:87], v[140:143]\n"
"v_mfma_f32_16x16x32_bf16 v[140:143], a[208:211], v[112:115], v[140:143]\n"
"buffer_load_dwordx4 v[184:187], v0, s[4:7], s97 offen offset:3072 sc1\n"
"s_waitcnt lgkmcnt(0)\n"
"v_mfma_f32_16x16x32_bf16 v[140:143], a[212:215], v[96:99], v[140:143]\n"
"v_add_f32_e32 v160, v64, v160\n"
"v_add_f32_e32 v161, v65, v161\n"
"v_add_f32_e32 v162, v66, v162\n"
"v_mfma_f32_16x16x32_bf16 v[140:143], a[216:219], v[132:135], v[140:143]\n"
"v_add_f32_e32 v163, v67, v163\n"
"v_add_f32_e32 v168, v60, v168\n"
"v_add_f32_e32 v169, v61, v169\n"
"v_mfma_f32_16x16x32_bf16 v[140:143], a[220:223], v[104:107], v[140:143]\n"
"buffer_load_dwordx4 v[64:67], v1, s[4:7], s97 offen sc1\n"
"v_add_f32_e32 v170, v62, v170\n"
"v_mfma_f32_16x16x32_bf16 v[52:55], a[252:255], v[104:107], v[52:55]\n"
"v_add_f32_e32 v171, v63, v171\n"
"v_cvt_pk_bf16_f32 v60, v160, v161\n"
"v_cvt_pk_bf16_f32 v61, v162, v163\n"
"v_mfma_f32_16x16x32_bf16 v[52:55], a[248:251], v[132:135], v[52:55]\n"
"v_cvt_pk_bf16_f32 v62, v168, v169\n"
"v_cvt_pk_bf16_f32 v63, v170, v171\n"
"v_xor_b32_e32 v254, 32, v255\n"
"v_mfma_f32_16x16x32_bf16 v[52:55], a[244:247], v[96:99], v[52:55]\n"
"buffer_load_dwordx4 v[96:99], v1, s[4:7], s97 offen offset:1024 sc1\n"
"v_mfma_f32_16x16x32_bf16 v[52:55], a[240:243], v[112:115], v[52:55]\n"
"ds_write_b64 v255, v[60:61] offset:8192\n"
"v_mfma_f32_16x16x32_bf16 v[52:55], a[236:239], v[84:87], v[52:55]\n"
"ds_write_b64 v254, v[62:63] offset:8192\n"
"v_mfma_f32_16x16x32_bf16 v[52:55], a[232:235], v[100:103], v[52:55]\n"
"buffer_load_dwordx4 v[60:63], v1, s[4:7], s97 offen offset:2048 sc1\n"
"v_mfma_f32_16x16x32_bf16 v[52:55], a[228:231], v[76:79], v[52:55]\n"
"v_mfma_f32_16x16x32_bf16 v[52:55], a[224:227], v[88:91], v[52:55]\n"
"buffer_load_dwordx4 v[76:79], v1, s[4:7], s97 offen offset:3072 sc1\n"
"s_waitcnt vmcnt(14)\n"
"; @B G2 a.0\n"
"v_mfma_f32_16x16x32_bf16 v[84:87], a[128:131], v[116:119], 0\n"
"s_add_u32 s97, s58, 0x400000\n"
"buffer_load_dwordx4 v[88:91], v0, s[4:7], s97 offen sc1\n"
"v_mfma_f32_16x16x32_bf16 v[84:87], a[132:135], v[164:167], v[84:87]\n"
"s_waitcnt vmcnt(13)\n"
"v_add_f32_e32 v172, v140, v172\n"
"v_add_f32_e32 v173, v141, v173\n"
"v_mfma_f32_16x16x32_bf16 v[84:87], a[136:139], v[124:127], v[84:87]\n"
"v_add_f32_e32 v174, v142, v174\n"
"v_add_f32_e32 v175, v143, v175\n"
"v_mfma_f32_16x16x32_bf16 v[84:87], a[140:143], v[68:71], v[84:87]\n"
"s_waitcnt vmcnt(11)\n"
"buffer_load_dwordx4 v[100:103], v0, s[4:7], s97 offen offset:1024 sc1\n"
"v_mfma_f32_16x16x32_bf16 v[84:87], a[144:147], v[36:39], v[84:87]\n"
"v_add_f32_e32 v128, v52, v128\n"
"v_add_f32_e32 v129, v53, v129\n"
"v_mfma_f32_16x16x32_bf16 v[84:87], a[148:151], v[40:43], v[84:87]\n"
"s_waitcnt vmcnt(10)\n"
"v_add_f32_e32 v130, v54, v130\n"
"v_add_f32_e32 v131, v55, v131\n"
"v_mfma_f32_16x16x32_bf16 v[84:87], a[152:155], v[72:75], v[84:87]\n"
"buffer_load_dwordx4 v[52:55], v0, s[4:7], s97 offen offset:2048 sc1\n"
"v_mfma_f32_16x16x32_bf16 v[84:87], a[156:159], v[80:83], v[84:87]\n"
"v_cvt_pk_bf16_f32 v104, v172, v173\n"
"v_cvt_pk_bf16_f32 v105, v174, v175\n"
"v_mfma_f32_16x16x32_bf16 v[112:115], a[188:191], v[80:83], 0\n"
"v_cvt_pk_bf16_f32 v106, v128, v129\n"
"v_cvt_pk_bf16_f32 v107, v130, v131\n"
"v_mfma_f32_16x16x32_bf16 v[112:115], a[184:187], v[72:75], v[112:115]\n"
"buffer_load_dwordx4 v[128:131], v0, s[4:7], s97 offen offset:3072 sc1\n"
"v_mfma_f32_16x16x32_bf16 v[112:115], a[180:183], v[40:43], v[112:115]\n"
"v_xor_b32_e32 v254, 64, v255\n"
"v_xor_b32_e32 v252, 0x60, v255\n"
"v_mfma_f32_16x16x32_bf16 v[112:115], a[176:179], v[36:39], v[112:115]\n"
"ds_write_b64 v254, v[104:105] offset:8192\n"
"v_mfma_f32_16x16x32_bf16 v[112:115], a[172:175], v[68:71], v[112:115]\n"
"v_cmp_u_f32_e32 vcc, v84, v84\n"
"v_mfma_f32_16x16x32_bf16 v[112:115], a[168:171], v[124:127], v[112:115]\n"
"s_cbranch_vccnz .Lmk_p59s\n"
".Lmk_p59r:\n"
"v_mfma_f32_16x16x32_bf16 v[112:115], a[164:167], v[164:167], v[112:115]\n"
"ds_write_b128 v3, v[84:87]\n"
"v_mfma_f32_16x16x32_bf16 v[112:115], a[160:163], v[116:119], v[112:115]\n"
"buffer_load_dwordx4 v[84:87], v1, s[4:7], s97 offen sc1\n"
"v_mfma_f32_16x16x32_bf16 v[132:135], a[192:195], v[116:119], 0\n"
"ds_write_b64 v252, v[106:107] offset:8192\n"
"v_mfma_f32_16x16x32_bf16 v[132:135], a[196:199], v[164:167], v[132:135]\n"
"ds_read_b128 v[104:107], v2 offset:8192\n"
"ds_read_b128 v[140:143], v2 offset:9216\n"
"v_mfma_f32_16x16x32_bf16 v[132:135], a[200:203], v[124:127], v[132:135]\n"
"buffer_load_dwordx4 v[160:163], v1, s[4:7], s97 offen offset:1024 sc1\n"
"v_mfma_f32_16x16x32_bf16 v[132:135], a[204:207], v[68:71], v[132:135]\n"
"v_bfe_u32 v255, v0, 4, 4\n"
"v_bfe_u32 v254, v0, 9, 1\n"
"v_mfma_f32_16x16x32_bf16 v[132:135], a[208:211], v[36:39], v[132:135]\n"
"v_and_b32_e32 v252, 7, v255\n"
"v_xor_b32_e32 v254, v254, v252\n"
"v_mfma_f32_16x16x32_bf16 v[132:135], a[212:215], v[40:43], v[132:135]\n"
"ds_write_b128 v3, v[112:115] offset:1024\n"
"v_mfma_f32_16x16x32_bf16 v[132:135], a[216:219], v[72:75], v[132:135]\n"
"buffer_load_dwordx4 v[112:115], v1, s[4:7], s97 offen offset:2048 sc1\n"
"v_mfma_f32_16x16x32_bf16 v[132:135], a[220:223], v[80:83], v[132:135]\n"
"s_waitcnt lgkmcnt(1)\n"
"v_lshlrev_b32_e32 v255, 7, v255\n"
"v_lshl_add_u32 v255, v254, 4, v255\n"
"v_mfma_f32_16x16x32_bf16 v[168:171], a[252:255], v[80:83], 0\n"
"v_bfe_u32 v254, v0, 8, 1\n"
"v_lshl_add_u32 v255, v254, 3, v255\n"
"v_mfma_f32_16x16x32_bf16 v[168:171], a[248:251], v[72:75], v[168:171]\n"
"buffer_load_dwordx4 v[72:75], v1, s[4:7], s97 offen offset:3072 sc1\n"
"v_mfma_f32_16x16x32_bf16 v[168:171], a[244:247], v[40:43], v[168:171]\n"
"s_add_u32 s97, s59, 0x400000\n"
"v_add_u32_e32 v255, s77, v255\n"
"v_bfe_u32 v252, v0, 7, 3\n"
"v_mfma_f32_16x16x32_bf16 v[168:171], a[240:243], v[36:39], v[168:171]\n"
"v_bfe_u32 v254, v0, 4, 3\n"
"v_xor_b32_e32 v254, v254, v252\n"
"v_mfma_f32_16x16x32_bf16 v[168:171], a[236:239], v[68:71], v[168:171]\n"
"buffer_load_dwordx4 v[36:39], v0, s[4:7], s97 offen sc1\n"
"v_mfma_f32_16x16x32_bf16 v[168:171], a[232:235], v[124:127], v[168:171]\n"
"ds_write_b128 v3, v[132:135] offset:2048\n"
"v_mfma_f32_16x16x32_bf16 v[168:171], a[228:231], v[164:167], v[168:171]\n"
"v_lshlrev_b32_e32 v252, 11, v252\n"
"v_lshl_add_u32 v252, v254, 4, v252\n"
"v_mfma_f32_16x16x32_bf16 v[168:171], a[224:227], v[116:119], v[168:171]\n"
"s_waitcnt vmcnt(13)\n"
"buffer_load_dwordx4 v[40:43], v0, s[4:7], s97 offen offset:1024 sc1\n"
"; @B G2 a.1\n"
"v_mfma_f32_16x16x32_bf16 v[68:71], a[128:131], v[152:155], 0\n"
"v_mfma_f32_16x16x32_bf16 v[68:71], a[132:135], v[156:159], v[68:71]\n"
"v_mfma_f32_16x16x32_bf16 v[68:71], a[136:139], v[176:179], v[68:71]\n"
"buffer_load_dwordx4 v[80:83], v0, s[4:7], s97 offen offset:2048 sc1\n"
"v_mfma_f32_16x16x32_bf16 v[68:71], a[140:143], v[184:187], v[68:71]\n"
"s_waitcnt vmcnt(11)\n"
"v_mfma_f32_16x16x32_bf16 v[68:71], a[144:147], v[64:67], v[68:71]\n"
"v_mfma_f32_16x16x32_bf16 v[68:71], a[148:151], v[96:99], v[68:71]\n"
"ds_write_b128 v3, v[168:171] offset:3072\n"
"v_mfma_f32_16x16x32_bf16 v[68:71], a[152:155], v[60:63], v[68:71]\n"
"buffer_load_dwordx4 v[116:119], v0, s[4:7], s97 offen offset:3072 sc1\n"
"v_mfma_f32_16x16x32_bf16 v[68:71], a[156:159], v[76:79], v[68:71]\n"
"v_mfma_f32_16x16x32_bf16 v[124:127], a[188:191], v[76:79], 0\n"
"v_mfma_f32_16x16x32_bf16 v[124:127], a[184:187], v[60:63], v[124:127]\n"
"buffer_load_dwordx4 v[132:135], v1, s[4:7], s97 offen sc1\n"
"v_mfma_f32_16x16x32_bf16 v[124:127], a[180:183], v[96:99], v[124:127]\n"
"v_mfma_f32_16x16x32_bf16 v[124:127], a[176:179], v[64:67], v[124:127]\n"
"v_mfma_f32_16x16x32_bf16 v[124:127], a[172:175], v[184:187], v[124:127]\n"
"s_nop 1\n"
"v_cmp_u_f32_e32 vcc, v68, v68\n"
"v_mfma_f32_16x16x32_bf16 v[124:127], a[168:171], v[176:179], v[124:127]\n"
"s_cbranch_vccnz .Lmk_p60s\n"
".Lmk_p60r:\n"
"v_mfma_f32_16x16x32_bf16 v[124:127], a[164:167], v[156:159], v[124:127]\n"
"ds_write_b128 v3, v[68:71] offset:6144\n"
"v_mfma_f32_16x16x32_bf16 v[124:127], a[160:163], v[152:155], v[124:127]\n"
"buffer_load_dwordx4 v[68:71], v1, s[4:7], s97 offen offset:1024 sc1\n"
"v_mfma_f32_16x16x32_bf16 v[164:167], a[192:195], v[152:155], 0\n"
"v_mfma_f32_16x16x32_bf16 v[164:167], a[196:199], v[156:159], v[164:167]\n"
"v_mfma_f32_16x16x32_bf16 v[164:167], a[200:203], v[176:179], v[164:167]\n"
"buffer_load_dwordx4 v[168:171], v1, s[4:7], s97 offen offset:2048 sc1\n"
"v_mfma_f32_16x16x32_bf16 v[164:167], a[204:207], v[184:187], v[164:167]\n"
"v_mfma_f32_16x16x32_bf16 v[164:167], a[208:211], v[64:67], v[164:167]\n"
"ds_read_b128 v[172:175], v2\n"
"ds_read_b128 v[200:203], v2 offset:1024\n"
"v_mfma_f32_16x16x32_bf16 v[164:167], a[212:215], v[96:99], v[164:167]\n"
"ds_write_b128 v3, v[124:127] offset:7168\n"
"v_mfma_f32_16x16x32_bf16 v[164:167], a[216:219], v[60:63], v[164:167]\n"
"buffer_load_dwordx4 v[124:127], v1, s[4:7], s97 offen offset:3072 sc1\n"
"v_mfma_f32_16x16x32_bf16 v[164:167], a[220:223], v[76:79], v[164:167]\n"
"v_mfma_f32_16x16x32_bf16 v[204:207], a[252:255], v[76:79], 0\n"
"v_mfma_f32_16x16x32_bf16 v[204:207], a[248:251], v[60:63], v[204:207]\n"
"s_waitcnt lgkmcnt(1)\n"
"v_lshlrev_b32_e32 v254, 16, v172\n"
"v_and_b32_e32 v251, 0xffff0000, v172\n"
"v_lshlrev_b32_e32 v250, 16, v173\n"
"v_mfma_f32_16x16x32_bf16 v[204:207], a[244:247], v[96:99], v[204:207]\n"
"v_and_b32_e32 v249, 0xffff0000, v173\n"
"v_lshlrev_b32_e32 v248, 16, v174\n"
"v_and_b32_e32 v247, 0xffff0000, v174\n"
"v_mfma_f32_16x16x32_bf16 v[204:207], a[240:243], v[64:67], v[204:207]\n"
"v_lshlrev_b32_e32 v246, 16, v175\n"
"v_and_b32_e32 v245, 0xffff0000, v175\n"
"v_lshlrev_b32_e32 v244, 16, v200\n"
"v_mfma_f32_16x16x32_bf16 v[204:207], a[236:239], v[184:187], v[204:207]\n"
"s_add_u32 s97, s74, 0x40000\n"
"buffer_store_dwordx4 v[104:107], v253, s[16:19], s97 offen sc1\n"
"v_mfma_f32_16x16x32_bf16 v[204:207], a[232:235], v[176:179], v[204:207]\n"
"ds_write_b128 v3, v[164:167] offset:8192\n"
"v_mfma_f32_16x16x32_bf16 v[204:207], a[228:231], v[156:159], v[204:207]\n"
"v_and_b32_e32 v243, 0xffff0000, v200\n"
"v_lshlrev_b32_e32 v242, 16, v201\n"
"v_and_b32_e32 v241, 0xffff0000, v201\n"
"v_mfma_f32_16x16x32_bf16 v[204:207], a[224:227], v[152:155], v[204:207]\n"
"s_waitcnt vmcnt(13)\n"
"v_lshlrev_b32_e32 v240, 16, v202\n"
"v_and_b32_e32 v239, 0xffff0000, v202\n"
"v_lshlrev_b32_e32 v238, 16, v203\n"
"; @B G2 a.2\n"
"v_mfma_f32_16x16x32_bf16 v[60:63], a[128:131], v[88:91], 0\n"
"v_and_b32_e32 v237, 0xffff0000, v203\n"
"v_mfma_f32_16x16x32_bf16 v[60:63], a[132:135], v[100:103], v[60:63]\n"
"v_mfma_f32_16x16x32_bf16 v[60:63], a[136:139], v[52:55], v[60:63]\n"
"v_mfma_f32_16x16x32_bf16 v[60:63], a[140:143], v[128:131], v[60:63]\n"
"s_waitcnt vmcnt(9)\n"
"v_mfma_f32_16x16x32_bf16 v[60:63], a[144:147], v[84:87], v[60:63]\n"
"v_mfma_f32_16x16x32_bf16 v[60:63], a[148:151], v[160:163], v[60:63]\n"
"ds_write_b128 v3, v[204:207] offset:9216\n"
"v_mfma_f32_16x16x32_bf16 v[60:63], a[152:155], v[112:115], v[60:63]\n"
"s_add_u32 s97, s74, 0x44000\n"
"v_mfma_f32_16x16x32_bf16 v[60:63], a[156:159], v[72:75], v[60:63]\n"
"v_mfma_f32_16x16x32_bf16 v[64:67], a[188:191], v[72:75], 0\n"
"buffer_store_dwordx4 v[140:143], v253, s[16:19], s97 offen sc1\n"
"v_mfma_f32_16x16x32_bf16 v[64:67], a[184:187], v[112:115], v[64:67]\n"
"s_waitcnt lgkmcnt(0)\n"
"s_barrier\n"
"v_mfma_f32_16x16x32_bf16 v[64:67], a[180:183], v[160:163], v[64:67]\n"
"ds_read_b128 v[76:79], v4\n"
"ds_read_b128 v[96:99], v5\n"
"v_mfma_f32_16x16x32_bf16 v[64:67], a[176:179], v[84:87], v[64:67]\n"
"v_mfma_f32_16x16x32_bf16 v[64:67], a[172:175], v[128:131], v[64:67]\n"
"v_cmp_u_f32_e32 vcc, v60, v60\n"
"v_mfma_f32_16x16x32_bf16 v[64:67], a[168:171], v[52:55], v[64:67]\n"
"s_cbranch_vccnz .Lmk_p61s\n"
".Lmk_p61r:\n"
"v_mfma_f32_16x16x32_bf16 v[64:67], a[164:167], v[100:103], v[64:67]\n"
"ds_write_b128 v3, v[60:63] offset:12288\n"
"v_mfma_f32_16x16x32_bf16 v[64:67], a[160:163], v[88:91], v[64:67]\n"
"s_waitcnt lgkmcnt(1)\n"
"v_add_f32_e32 v76, v76, v254\n"
"v_add_f32_e32 v77, v77, v251\n"
"v_add_f32_e32 v78, v78, v250\n"
"v_mfma_f32_16x16x32_bf16 v[60:63], a[192:195], v[88:91], 0\n"
"ds_read_b128 v[104:107], v4 offset:1024\n"
"ds_read_b128 v[140:143], v5 offset:1024\n"
"v_add_f32_e32 v79, v79, v249\n"
"v_mfma_f32_16x16x32_bf16 v[60:63], a[196:199], v[100:103], v[60:63]\n"
"v_add_f32_e32 v76, v76, v96\n"
"v_add_f32_e32 v77, v77, v97\n"
"v_add_f32_e32 v78, v78, v98\n"
"v_mfma_f32_16x16x32_bf16 v[60:63], a[200:203], v[52:55], v[60:63]\n"
"v_add_f32_e32 v79, v79, v99\n"
"v_mfma_f32_16x16x32_bf16 v[60:63], a[204:207], v[128:131], v[60:63]\n"
"v_mfma_f32_16x16x32_bf16 v[60:63], a[208:211], v[84:87], v[60:63]\n"
"v_mfma_f32_16x16x32_bf16 v[60:63], a[212:215], v[160:163], v[60:63]\n"
"ds_write_b128 v3, v[64:67] offset:13312\n"
"v_mfma_f32_16x16x32_bf16 v[60:63], a[216:219], v[112:115], v[60:63]\n"
"ds_read_b128 v[64:67], v4 offset:2048\n"
"ds_read_b128 v[96:99], v5 offset:2048\n"
"s_waitcnt lgkmcnt(3)\n"
"v_mfma_f32_16x16x32_bf16 v[60:63], a[220:223], v[72:75], v[60:63]\n"
"v_add_f32_e32 v104, v104, v248\n"
"v_add_f32_e32 v105, v105, v247\n"
"v_add_f32_e32 v106, v106, v246\n"
"v_mfma_f32_16x16x32_bf16 v[152:155], a[252:255], v[72:75], 0\n"
"v_add_f32_e32 v107, v107, v245\n"
"v_add_f32_e32 v104, v104, v140\n"
"v_add_f32_e32 v105, v105, v141\n"
"v_mfma_f32_16x16x32_bf16 v[152:155], a[248:251], v[112:115], v[152:155]\n"
"v_add_f32_e32 v106, v106, v142\n"
"v_add_f32_e32 v107, v107, v143\n"
"v_mfma_f32_16x16x32_bf16 v[152:155], a[244:247], v[160:163], v[152:155]\n"
"v_mfma_f32_16x16x32_bf16 v[152:155], a[240:243], v[84:87], v[152:155]\n"
"s_waitcnt lgkmcnt(0)\n"
"v_add_f32_e32 v64, v64, v244\n"
"v_add_f32_e32 v65, v65, v243\n"
"v_add_f32_e32 v66, v66, v242\n"
"v_mfma_f32_16x16x32_bf16 v[152:155], a[236:239], v[128:131], v[152:155]\n"
"ds_read_b128 v[72:75], v4 offset:3072\n"
"ds_read_b128 v[84:87], v5 offset:3072\n"
"v_add_f32_e32 v67, v67, v241\n"
"v_mfma_f32_16x16x32_bf16 v[152:155], a[232:235], v[52:55], v[152:155]\n"
"ds_write_b128 v3, v[60:63] offset:14336\n"
"v_mfma_f32_16x16x32_bf16 v[152:155], a[228:231], v[100:103], v[152:155]\n"
"v_add_f32_e32 v64, v64, v96\n"
"v_add_f32_e32 v65, v65, v97\n"
"v_add_f32_e32 v66, v66, v98\n"
"v_mfma_f32_16x16x32_bf16 v[152:155], a[224:227], v[88:91], v[152:155]\n"
"s_waitcnt vmcnt(6)\n"
"v_add_f32_e32 v67, v67, v99\n"
"; @B G2 a.3\n"
"v_mfma_f32_16x16x32_bf16 v[52:55], a[128:131], v[36:39], v[76:79]\n"
"v_mfma_f32_16x16x32_bf16 v[52:55], a[132:135], v[40:43], v[52:55]\n"
"s_waitcnt lgkmcnt(1)\n"
"v_add_f32_e32 v72, v72, v240\n"
"v_add_f32_e32 v73, v73, v239\n"
"v_add_f32_e32 v74, v74, v238\n"
"v_mfma_f32_16x16x32_bf16 v[52:55], a[136:139], v[80:83], v[52:55]\n"
"v_add_f32_e32 v75, v75, v237\n"
"v_add_f32_e32 v72, v72, v84\n"
"v_add_f32_e32 v73, v73, v85\n"
"v_mfma_f32_16x16x32_bf16 v[52:55], a[140:143], v[116:119], v[52:55]\n"
"s_waitcnt vmcnt(2)\n"
"v_add_f32_e32 v74, v74, v86\n"
"v_add_f32_e32 v75, v75, v87\n"
"v_mfma_f32_16x16x32_bf16 v[52:55], a[144:147], v[132:135], v[52:55]\n"
"v_mfma_f32_16x16x32_bf16 v[52:55], a[148:151], v[68:71], v[52:55]\n"
"ds_write_b128 v3, v[152:155] offset:15360\n"
"v_mfma_f32_16x16x32_bf16 v[52:55], a[152:155], v[168:171], v[52:55]\n"
"v_mfma_f32_16x16x32_bf16 v[52:55], a[156:159], v[124:127], v[52:55]\n"
"v_mfma_f32_16x16x32_bf16 v[60:63], a[188:191], v[124:127], v[104:107]\n"
"v_mfma_f32_16x16x32_bf16 v[60:63], a[184:187], v[168:171], v[60:63]\n"
"s_waitcnt lgkmcnt(0)\n"
"s_barrier\n"
"v_mfma_f32_16x16x32_bf16 v[60:63], a[180:183], v[68:71], v[60:63]\n"
"ds_read_b128 v[84:87], v6\n"
"ds_read_b128 v[88:91], v6 offset:1024\n"
"v_mfma_f32_16x16x32_bf16 v[60:63], a[176:179], v[132:135], v[60:63]\n"
"ds_read_b128 v[96:99], v6 offset:2048\n"
"ds_read_b128 v[100:103], v6 offset:3072\n"
"v_mfma_f32_16x16x32_bf16 v[60:63], a[172:175], v[116:119], v[60:63]\n"
"v_cmp_u_f32_e32 vcc, v52, v52\n"
"v_mfma_f32_16x16x32_bf16 v[60:63], a[168:171], v[80:83], v[60:63]\n"
"s_cbranch_vccnz .Lmk_p62s\n"
".Lmk_p62r:\n"
"v_mfma_f32_16x16x32_bf16 v[60:63], a[164:167], v[40:43], v[60:63]\n"
"v_mfma_f32_16x16x32_bf16 v[60:63], a[160:163], v[36:39], v[60:63]\n"
"v_mfma_f32_16x16x32_bf16 v[64:67], a[192:195], v[36:39], v[64:67]\n"
"v_mfma_f32_16x16x32_bf16 v[64:67], a[196:199], v[40:43], v[64:67]\n"
"v_mfma_f32_16x16x32_bf16 v[64:67], a[200:203], v[80:83], v[64:67]\n"
"v_mfma_f32_16x16x32_bf16 v[64:67], a[204:207], v[116:119], v[64:67]\n"
"v_mfma_f32_16x16x32_bf16 v[64:67], a[208:211], v[132:135], v[64:67]\n"
"s_waitcnt lgkmcnt(0)\n"
"v_add_f32_e32 v84, v52, v84\n"
"v_add_f32_e32 v85, v53, v85\n"
"v_add_f32_e32 v86, v54, v86\n"
"v_mfma_f32_16x16x32_bf16 v[64:67], a[212:215], v[68:71], v[64:67]\n"
"v_add_f32_e32 v87, v55, v87\n"
"v_add_f32_e32 v88, v60, v88\n"
"v_add_f32_e32 v89, v61, v89\n"
"v_mfma_f32_16x16x32_bf16 v[64:67], a[216:219], v[168:171], v[64:67]\n"
"v_add_f32_e32 v90, v62, v90\n"
"v_add_f32_e32 v91, v63, v91\n"
"v_cvt_pk_bf16_f32 v52, v84, v85\n"
"v_mfma_f32_16x16x32_bf16 v[64:67], a[220:223], v[124:127], v[64:67]\n"
"v_cvt_pk_bf16_f32 v53, v86, v87\n"
"v_cvt_pk_bf16_f32 v54, v88, v89\n"
"v_cvt_pk_bf16_f32 v55, v90, v91\n"
"v_mfma_f32_16x16x32_bf16 v[72:75], a[252:255], v[124:127], v[72:75]\n"
"v_xor_b32_e32 v254, 32, v255\n"
"v_mfma_f32_16x16x32_bf16 v[72:75], a[248:251], v[168:171], v[72:75]\n"
"ds_write_b64 v255, v[52:53] offset:0\n"
"v_mfma_f32_16x16x32_bf16 v[72:75], a[244:247], v[68:71], v[72:75]\n"
"ds_write_b64 v254, v[54:55] offset:0\n"
"v_mfma_f32_16x16x32_bf16 v[72:75], a[240:243], v[132:135], v[72:75]\n"
"v_mfma_f32_16x16x32_bf16 v[72:75], a[236:239], v[116:119], v[72:75]\n"
"v_mfma_f32_16x16x32_bf16 v[72:75], a[232:235], v[80:83], v[72:75]\n"
"v_mfma_f32_16x16x32_bf16 v[72:75], a[228:231], v[40:43], v[72:75]\n"
"v_mfma_f32_16x16x32_bf16 v[72:75], a[224:227], v[36:39], v[72:75]\n"
"v_add_f32_e32 v96, v64, v96\n"
"v_add_f32_e32 v97, v65, v97\n"
"v_add_f32_e32 v98, v66, v98\n"
"v_add_f32_e32 v99, v67, v99\n"
"s_nop 3\n"
"v_add_f32_e32 v100, v72, v100\n"
"v_add_f32_e32 v101, v73, v101\n"
"v_add_f32_e32 v102, v74, v102\n"
"v_add_f32_e32 v103, v75, v103\n"
"v_cvt_pk_bf16_f32 v36, v96, v97\n"
"v_cvt_pk_bf16_f32 v37, v98, v99\n"
"v_cvt_pk_bf16_f32 v38, v100, v101\n"
"v_cvt_pk_bf16_f32 v39, v102, v103\n"
"v_xor_b32_e32 v254, 64, v255\n"
"v_xor_b32_e32 v253, 0x60, v255\n"
"ds_write_b64 v254, v[36:37] offset:0\n"
"ds_write_b64 v253, v[38:39] offset:0\n"
"ds_read_b128 v[36:39], v2\n"
"ds_read_b128 v[40:43], v2 offset:1024\n"
"s_waitcnt lgkmcnt(0)\n"
"buffer_store_dwordx4 v[36:39], v252, s[16:19], s74 offen sc1\n"
"s_add_u32 s97, s74, 0x4000\n"
"buffer_store_dwordx4 v[40:43], v252, s[16:19], s97 offen sc1\n"
"s_cmp_eq_u32 s81, 0\n"
"s_cbranch_scc1 .Lmk_fin\n"
"buffer_store_dwordx2 v[18:19], v11, s[4:7], s81 offen sc1\n"
".Lmk_fin:\n"
"s_endpgm\n"
".Lmk_end:\n"
    :: "{s[4:7]}"(wsr), "{s[8:11]}"(wsr), "{s[12:15]}"(wsk), "{s[16:19]}"(outr), "{s[52:67]}"(sa), "{s[68:83]}"(sb),
       "{s[20:27]}"(pa), "{s[28:31]}"(wa), "{s[32:35]}"(wb), "{s[36:39]}"(oa), "{s[40:43]}"(ob), "{s[44:51]}"(pc), "{s99}"(uni((bix * 4 + w) * 1024)),
       "{v[0:15]}"(va), "{v[16:23]}"(vb), "{v[32:47]}"(pz), "{v48}"(sta), "{v50}"(pf)
    : "memory", "v255", "a255", "s98", "vcc");
  __builtin_unreachable();
}

}

extern "C" void kernel_launch(void* const* d_in, const int* in_sizes, int n_in, void* d_out, int out_size,
                              void* d_ws, size_t ws_size, hipStream_t stream) {
  (void)hipFuncSetAttribute((const void*)mega_kernel, hipFuncAttributeMaxDynamicSharedMemorySize, LDS_BYTES);
  mega_kernel<<<256, 256, LDS_BYTES, stream>>>((const u16*)d_in[0], (const u16*)d_in[1], (const u16*)d_in[2],
                                               (const u16*)d_in[4], (const u16*)d_in[5], (u16*)d_out, (char*)d_ws);
}
```
